```python
import torch

def _softmax(x, axis=-1):
    return torch.softmax(x, dim=axis)
def _gelu(x):
    return torch.nn.functional.gelu(x, approximate="tanh")
def _silu(x):
    return torch.nn.functional.silu(x)
def _sigmoid(x):
    return torch.sigmoid(x)
def _layernorm(x, axis=-1, eps=1e-5):
    m = x.mean(dim=axis, keepdim=True)
    v = x.var(dim=axis, keepdim=True, unbiased=False)
    return (x - m) / torch.sqrt(v + eps)
def _scatter_add(tgt, idx, src, axis=0):
    return tgt.clone().index_add_(axis, idx, src)
def _segment_sum(src, seg, n):
    out = torch.zeros((n,) + src.shape[1:], dtype=src.dtype)
    return out.index_add_(0, seg, src)
def _pad(x, pw):
    flat = tuple(v for pair in reversed(pw) for v in pair)
    return torch.nn.functional.pad(x, flat)

kernel_name = 'gated_mix__compressed_kv_cla__moe_arch_mod__prompt'

def setup_inputs():

    _w = (lambda a: (lambda t: t.to(torch.bfloat16) if t.is_floating_point() else t)(torch.as_tensor(a)))
    def _A_setup():
        B, H, Sq, Sk, D, M = 1, 16, 2048, 2048, 256, 448
        Dc = 4 * D
        return {
            "x": _w((lambda *s: torch.randn(*s, dtype=torch.bfloat16) * 0.05)(B, Sq, M)),
            "c_kv": _w((lambda *s: torch.randn(*s, dtype=torch.bfloat16) * 0.8)(B, Sk, Dc)),
            "w_q": _w((lambda *s: torch.randn(*s, dtype=torch.bfloat16) * 6.4)(M, H, D) * (M ** -0.5)),
            "w_uk": _w((lambda *s: torch.randn(*s, dtype=torch.bfloat16) * 6.4)(Dc, H, D) * (Dc ** -0.5)),
            "w_uv": _w((lambda *s: torch.randn(*s, dtype=torch.bfloat16) * 0.8)(Dc, H, D) * (Dc ** -0.5)),
            "w_o": _w((lambda *s: torch.randn(*s, dtype=torch.bfloat16) * 0.8)(H, D, M) * ((H * D) ** -0.5)),
        }
    _A_in = _A_setup()

    def _B_setup():
        B, S, D, F, C = 1, 2048, 448, 1792, 2
        return {
            "x": _w((lambda *s: torch.randn(*s, dtype=torch.bfloat16) * 0.05)(B, S, D)),
            "w_r": _w((lambda *s: torch.randn(*s, dtype=torch.bfloat16) * 3.2)(D)),
            "w_gate": _w((lambda *s: torch.randn(*s, dtype=torch.bfloat16) * 0.4)(D, F)),
            "w_up": _w((lambda *s: torch.randn(*s, dtype=torch.bfloat16) * 0.4)(D, F)),
            "w_down": _w((lambda *s: torch.randn(*s, dtype=torch.bfloat16) * 0.4)(F, D)),
        }
    _B_in = _B_setup()

    return {
        "A_x": _A_in["x"],
        "A_c_kv": _A_in["c_kv"],
        "A_w_q": _A_in["w_q"],
        "A_w_uk": _A_in["w_uk"],
        "A_w_uv": _A_in["w_uv"],
        "A_w_o": _A_in["w_o"],
        "B_x": _B_in["x"],
        "B_w_r": _B_in["w_r"],
        "B_w_gate": _B_in["w_gate"],
        "B_w_up": _B_in["w_up"],
        "B_w_down": _B_in["w_down"],
        "gate_w": _w((lambda *s: torch.randn(*s, dtype=torch.bfloat16) * 0.05)(448) * 16.0),
    }

def reference(A_x, A_c_kv, A_w_q, A_w_uk, A_w_uv, A_w_o, B_x, B_w_r, B_w_gate, B_w_up, B_w_down, gate_w):
    def _A_ref(x, c_kv, w_q, w_uk, w_uv, w_o):
        B, Sq, M = x.shape[0], x.shape[1], x.shape[2]
        Sk = c_kv.shape[1]
        H, D = w_q.shape[1], w_q.shape[2]
        q = torch.einsum("bqm,mhd->bhqd", x, w_q)
        k = torch.einsum("bkc,chd->bhkd", c_kv, w_uk)
        v = torch.einsum("bkc,chd->bhkd", c_kv, w_uv)
        scores = torch.einsum("bhqd,bhkd->bhqk", q, k) * (D ** -0.5)
        off = Sk - Sq
        qi = torch.arange(Sq)[:, None]
        kj = torch.arange(Sk)[None, :]
        cmask = kj <= (qi + off)
        scores = torch.where(cmask[None, None], scores, -1e9)
        p = _softmax(scores, axis=-1)
        ctx = torch.einsum("bhqk,bhkd->bhqd", p, v)
        return torch.einsum("bhqd,hdm->bqm", ctx, w_o)

    def _B_ref(x, w_r, w_gate, w_up, w_down):
        B, S, D = x.shape
        C = 2
        r = x @ w_r
        idx = torch.argsort(-r, axis=-1)[:, :C]
        idx_e = (lambda x, n, axis: torch.repeat_interleave(x, n, dim=axis))(idx.reshape(B, C, 1), D, axis=2)
        x_sel = (lambda x, i, axis: torch.take_along_dim(x, i, dim=axis))(x, idx_e, axis=1)
        g = _silu(x_sel @ w_gate)
        u = x_sel @ w_up
        y = (g * u) @ w_down
        r_sel = _sigmoid((lambda x, i, axis: torch.take_along_dim(x, i, dim=axis))(r, idx, axis=1))
        y = y * r_sel.reshape(B, C, 1)
        b_off = (torch.arange(B) * S).reshape(B, 1)
        flat_idx = (idx + b_off).reshape(B * C)
        zero = (x * 0.0).reshape(B * S, D)
        upd = _scatter_add(zero, flat_idx, y.reshape(B * C, D), axis=0)
        return x + upd.reshape(B, S, D)

    x = A_x
    g = _sigmoid((x * gate_w).sum(-1, keepdims=True))
    a = _A_ref(x, A_c_kv, A_w_q, A_w_uk, A_w_uv, A_w_o)
    b = _B_ref(x, B_w_r, B_w_gate, B_w_up, B_w_down)
    return g * a + (1 - g) * b
```

```cpp
#include <hip/hip_runtime.h>
#include <hip/hip_bf16.h>
#include <stdint.h>
#include <utility>

typedef __attribute__((ext_vector_type(4))) float f32x4;
typedef __attribute__((ext_vector_type(16))) float f32x16;
typedef __attribute__((ext_vector_type(4))) unsigned u32x4;
typedef __attribute__((ext_vector_type(2))) unsigned u32x2;
typedef unsigned short u16;

constexpr int S_ = 2048;
constexpr int M_ = 448;
constexpr int H_ = 16;
constexpr int D_ = 256;
constexpr int DC = 1024;
constexpr int HD = H_ * D_;
constexpr int F_ = 1792;
constexpr int FFN_N = F_ / 16;

constexpr size_t WS_Q = 0;
constexpr size_t WS_K = WS_Q + (size_t)S_ * HD * 2;
constexpr size_t WS_V = WS_K + (size_t)S_ * HD * 2;
constexpr size_t WS_Y = WS_V + (size_t)S_ * HD * 2;
constexpr size_t WS_X = WS_Y + (size_t)2 * H_ * 8 * 65536;
constexpr size_t WS_ML = WS_X + (size_t)2 * H_ * 8 * 65536;
constexpr size_t WS_WO = WS_ML + (size_t)H_ * 8 * 128 * 8;
constexpr size_t WS_GATE = WS_WO + (size_t)H_ * 262144;
constexpr size_t WS_R = WS_GATE + S_ * 4;
constexpr size_t WS_KEY = WS_R + S_ * 4;
constexpr size_t WS_IDX = WS_KEY + S_ * 8;
constexpr size_t WS_EP = WS_IDX + 768;
constexpr size_t WS_KVF = WS_EP + 256;
constexpr size_t WS_WF = WS_KVF + H_ * 256;
constexpr size_t WS_DF = WS_WF + (size_t)2 * H_ * 8 * 4 * 16;
constexpr size_t WS_BB = WS_DF + 4096;
constexpr size_t WS_YF = WS_BB + 2048;
constexpr size_t WS_END = WS_YF + (size_t)FFN_N * 2 * M_ * 4;
constexpr unsigned ML_SENT = 0xBF800000u;

typedef __attribute__((ext_vector_type(2))) float f32x2;
typedef __attribute__((ext_vector_type(2))) __bf16 bfx2;
__device__ __forceinline__ unsigned cvtpk(float lo, float hi) {
  bfx2 v = __builtin_convertvector((f32x2){lo, hi}, bfx2);
  return __builtin_bit_cast(unsigned, v);
}
__device__ __forceinline__ float rbf(float f) { return __uint_as_float(cvtpk(f, f) << 16); }
__device__ __forceinline__ unsigned lds_addr(const void* p) { return (unsigned)(uintptr_t)(const __attribute__((address_space(3))) char*)p; }
#define DI __device__ __forceinline__
typedef unsigned u32;
typedef __amdgpu_buffer_rsrc_t srd_t;
DI srd_t mk_srd(const void* p, unsigned bytes) { return __builtin_amdgcn_make_buffer_rsrc((void*)p, (short)0, (int)bytes, 0x00020000); }
DI u32 rfl(u32 x) { return __builtin_amdgcn_readfirstlane(x); }
DI int lane_id() { int l; asm volatile("v_mbcnt_lo_u32_b32 %0, -1, 0\n\tv_mbcnt_hi_u32_b32 %0, -1, %0" : "=v"(l)); return l; }
template <int... Is> using iseq = std::integer_sequence<int, Is...>;
template <int N> using make_iseq = std::make_integer_sequence<int, N>;
#define WT " sc0 sc1"
#define DV " sc1"
constexpr u32 OOB = 0x40000000u;
#define GAP() __builtin_amdgcn_sched_barrier(0)

template <int MO, int SO> DI void dma16(u32 m0base, u32 sbase, u32 voff, srd_t srd) {
  u32 t;
  asm volatile("s_add_u32 m0, %1, %2\n\ts_add_u32 %0, %3, %4\n\tbuffer_load_dwordx4 %5, %6, %0 offen lds"
               : "=&s"(t) : "s"(m0base), "i"(MO), "s"(sbase), "i"(SO), "v"(voff), "s"(srd) : "memory", "scc");
}
template <int MO, int SO, int J, bool FIRST = (J & 3) == 0> DI void dma16q(u32 m0base, u32 sbase, u32 voff, srd_t srd, u32& st) {
  if constexpr (FIRST)
    asm volatile("s_add_u32 m0, %1, %2\n\ts_add_u32 %0, %3, %4\n\tbuffer_load_dwordx4 %5, %6, %0 offen offset:%7" DV " lds"
                 : "=&s"(st) : "s"(m0base), "i"(MO + (J & 4) * 1024), "s"(sbase), "i"(SO + (J & 4) * 1024), "v"(voff), "s"(srd), "i"((J & 3) * 1024) : "memory", "scc");
  else
    asm volatile("buffer_load_dwordx4 %0, %1, %2 offen offset:%3" DV " lds" :: "v"(voff), "s"(srd), "s"(st), "i"((J & 3) * 1024) : "memory");
}
template <int OFF> DI void st16(u32x4 v, u32 voff, srd_t srd, u32 soff) {
  asm volatile("buffer_store_dwordx4 %0, %1, %2, %3 offen offset:%4" WT "\n\ts_nop 1" :: "v"(v), "v"(voff), "s"(srd), "s"(soff), "i"(OFF) : "memory");
}
#define CODE_WARM(SELF, END, m0, soff, voff) CODE_WARM_(SELF, END, m0, soff, voff)
#define CODE_WARM_(SELF, END, m0, soff, voff) do { \
    u32 lo_; int len_; unsigned long long pc_; \
    asm volatile("s_getpc_b64 %0\n.Lpc%=:\n\ts_mov_b32 %1, .Lpc%=-" #SELF "\n\ts_mov_b32 %2, " #END "-" #SELF : "=&s"(pc_), "=&s"(lo_), "=&s"(len_)); \
    dma16<0, 0>(m0, soff, voff, mk_srd((const void*)(pc_ - lo_), (len_ > 0 && len_ <= 393216) ? (u32)len_ & ~15u : 0u)); } while (0)
#define PSTAMP(i) do { } while (0)
#define ACL "a0","a1","a2","a3","a4","a5","a6","a7","a8","a9","a10","a11","a12","a13","a14","a15","a16","a17","a18","a19","a20","a21","a22","a23","a24","a25","a26","a27","a28","a29","a30","a31","a32","a33","a34","a35","a36","a37","a38","a39","a40","a41","a42","a43","a44","a45","a46","a47","a48","a49","a50","a51","a52","a53","a54","a55","a56","a57","a58","a59","a60","a61","a62","a63","a64","a65","a66","a67","a68","a69","a70","a71","a72","a73","a74","a75","a76","a77","a78","a79","a80","a81","a82","a83","a84","a85","a86","a87","a88","a89","a90","a91","a92","a93","a94","a95","a96","a97","a98","a99","a100","a101","a102","a103","a104","a105","a106","a107","a108","a109","a110","a111","a112","a113","a114","a115","a116","a117","a118","a119","a120","a121","a122","a123","a124","a125","a126","a127","a128","a129","a130","a131","a132","a133","a134","a135","a136","a137","a138","a139","a140","a141","a142","a143","a144","a145","a146","a147","a148","a149","a150","a151","a152","a153","a154","a155","a156","a157","a158","a159","a160","a161","a162","a163","a164","a165","a166","a167","a168","a169","a170","a171","a172","a173","a174","a175","a176","a177","a178","a179","a180","a181","a182","a183","a184","a185","a186","a187","a188","a189","a190","a191","a192","a193","a194","a195","a196","a197","a198","a199","a200","a201","a202","a203","a204","a205","a206","a207","a208","a209","a210","a211","a212","a213","a214","a215","a216","a217","a218","a219","a220","a221","a222","a223","a224","a225","a226","a227","a228","a229","a230","a231","a232","a233","a234","a235","a236","a237","a238","a239","a240","a241","a242","a243","a244","a245","a246","a247","a248","a249","a250","a251","a252","a253","a254","a255"
template <int R> DI float acc_rd() { float r; asm volatile("v_accvgpr_read_b32 %0, a[%c1]" : "=v"(r) : "i"(R)); return r; }
template <int R> DI void acc_wr(u32 v) { asm volatile("v_accvgpr_write_b32 a[%c1], %0" :: "v"(v), "i"(R) : ACL); }
template <int R> DI void acc_mul(float f) { float t; asm volatile("v_accvgpr_read_b32 %0, a[%c2]\n\tv_mul_f32 %0, %1, %0\n\tv_accvgpr_write_b32 a[%c2], %0" : "=&v"(t) : "v"(f), "i"(R) : ACL); }
template <int... Rs> DI void acc_mul_all(float f, iseq<Rs...>) { (acc_mul<Rs>(f), ...); }
template <int R> DI u32 acc_pk() {
  u32 w; float t;
  asm volatile("v_accvgpr_read_b32 %0, a[%c2]\n\tv_accvgpr_read_b32 %1, a[%c3]\n\tv_cvt_pk_bf16_f32 %0, %0, %1" : "=&v"(w), "=&v"(t) : "i"(R), "i"(R + 1));
  return w;
}
#define BF_LO(u) __uint_as_float((u) << 16)
#define BF_HI(u) __uint_as_float((u) & 0xffff0000u)
#define PINF(x) asm volatile("" : "+v"(x))
template <int CTRL> DI float dpp_add(float v) { return v + __uint_as_float(__builtin_amdgcn_update_dpp(0, __float_as_uint(v), CTRL, 0xf, 0xf, false)); }
template <bool W32> DI float sw_add(float a, float b) {
  if constexpr (W32) { const auto r = __builtin_amdgcn_permlane32_swap(__float_as_uint(a), __float_as_uint(b), false, false); return __uint_as_float(r[0]) + __uint_as_float(r[1]); }
  else { const auto r = __builtin_amdgcn_permlane16_swap(__float_as_uint(a), __float_as_uint(b), false, false); return __uint_as_float(r[0]) + __uint_as_float(r[1]); }
}
#define SPINB 18
constexpr u32 SPIN_MAX = 1u << SPINB;
constexpr int AT_X = 131072, AT_LDS = 163840;
constexpr int PJ_W0 = 65536;
struct PJ {
  u32x4 af[2][8];
  u32x2 wl[2][8], wh[2][8];
  u32 aKV[2], wb[8];
  u32 vAkv[4], vAq[4], vW[4], vWq[4], st;
  u32 m0A, m0W, m0Aq;
  u32 sAkv, sAq, sWkv, sWq;
  srd_t rA, rW, rX, rWq, rO;
  u32x4 pv[2], pl; float pt[2];
  u32 vO, sO, dO[5];
  u32x4 px[4]; float sg[2], sr[2], gv; u32 sRow, sM;
  const u16 *pwo, *pg, *pu, *pd; u32 sP, sPw, sPq, sPf, sPd, m0P, vP, vT;
  u32x2 wlo[4], whi[4]; u32 wra, sWo;
  u32 tag, sFl, vFl, poll;
  u32 sK0, sV0, vL, m0K;
  u32 wid;
  f32x16 qa[8];
  u32x2 ql[2][16], qh[2][16];
  u32x4 xf[2][4];
  u32 qb[4], xb[4];
  u32 nQf, sQf; u32x4 ones;
  u32 sQo; u32x2 iv;
};
template <bool VT> constexpr int pjT(int mt, int nt) { return VT ? 16 * (mt >> 1) + 2 * nt + (mt & 1) : 8 * mt + nt; }
template <bool VT> constexpr int pjMt(int T) { return VT ? 2 * (T >> 4) + (T & 1) : T >> 3; }
template <bool VT> constexpr int pjNt(int T) { return VT ? (T >> 1) & 7 : T & 7; }
template <bool SWAP, bool FIRST, int SET, int mt, int nt> DI void pj_mfma(PJ& s) {
  constexpr int ac = 4 * pjT<SWAP>(mt, nt);
  const u32x4 w = {s.wl[SET][nt][0], s.wl[SET][nt][1], s.wh[SET][nt][0], s.wh[SET][nt][1]};
  if constexpr (FIRST) {
    if constexpr (!SWAP) asm volatile("v_mfma_f32_16x16x32_bf16 a[%c2:%c3], %0, %1, 0" :: "v"(w), "v"(s.af[SET][mt]), "i"(ac), "i"(ac + 3) : ACL);
    else                 asm volatile("v_mfma_f32_16x16x32_bf16 a[%c2:%c3], %0, %1, 0" :: "v"(s.af[SET][mt]), "v"(w), "i"(ac), "i"(ac + 3) : ACL);
  } else {
    if constexpr (!SWAP) asm volatile("v_mfma_f32_16x16x32_bf16 a[%c2:%c3], %0, %1, a[%c2:%c3]" :: "v"(w), "v"(s.af[SET][mt]), "i"(ac), "i"(ac + 3) : ACL);
    else                 asm volatile("v_mfma_f32_16x16x32_bf16 a[%c2:%c3], %0, %1, a[%c2:%c3]" :: "v"(s.af[SET][mt]), "v"(w), "i"(ac), "i"(ac + 3) : ACL);
  }
}
template <int SET, int STG, int KS, int R> DI void pj_read(PJ& s) {
  if constexpr (R < 16) {
    constexpr int NT = R >> 1, OFF = STG * 32768 + KS * 16384;
    if constexpr ((R & 1) == 0) asm volatile("ds_read_b64_tr_b16 %0, %1 offset:%2" : "=v"(s.wl[SET][NT]) : "v"(s.wb[NT]), "i"(OFF) : "memory");
    else                        asm volatile("ds_read_b64_tr_b16 %0, %1 offset:%2" : "=v"(s.wh[SET][NT]) : "v"(s.wb[NT]), "i"(OFF + 2048) : "memory");
  } else if constexpr (R < 24) {
    constexpr int mt = R - 16;
    asm volatile("ds_read_b128 %0, %1 offset:%2" : "=v"(s.af[SET][mt]) : "v"(s.aKV[KS]), "i"(STG * 32768 + (mt & 1) * 2048 + (mt >> 1) * 8192) : "memory");
  }
}
template <int MO, int SO, int J, bool FIRST = (J == 0)> DI void dma16g(u32 m0base, u32 sbase, u32 voff, srd_t srd, u32& st) {
  if constexpr (FIRST)
    asm volatile("s_add_u32 m0, %1, %2\n\ts_add_u32 %0, %3, %4\n\tbuffer_load_dwordx4 %5, %6, %0 offen offset:%7 lds"
                 : "=&s"(st) : "s"(m0base), "i"(MO), "s"(sbase), "i"(SO), "v"(voff), "s"(srd), "i"(J * 1024) : "memory", "scc");
  else
    asm volatile("buffer_load_dwordx4 %0, %1, %2 offen offset:%3 lds" :: "v"(voff), "s"(srd), "s"(st), "i"(J * 1024) : "memory");
}
template <int STG, int KS, int J> DI void pj_dmaW(PJ& s, u32 sW) { dma16g<STG * 32768 + KS * 16384, KS * 262144, J>(s.m0W, sW, s.vW[J], s.rW, s.st); }
template <int STG, int J> DI void pj_dmaA(PJ& s, u32 sA) { dma16g<STG * 32768 + (J >> 2) * 4096, (J >> 2) * 65536, (J & 3)>(s.m0A, sA, s.vAkv[J & 3], s.rA, s.st); }
#define PJ_PIN(S) "+v"(s.wl[S][0]), "+v"(s.wl[S][1]), "+v"(s.wl[S][2]), "+v"(s.wl[S][3]), "+v"(s.wl[S][4]), "+v"(s.wl[S][5]), "+v"(s.wl[S][6]), "+v"(s.wl[S][7]), \
                  "+v"(s.wh[S][0]), "+v"(s.wh[S][1]), "+v"(s.wh[S][2]), "+v"(s.wh[S][3]), "+v"(s.wh[S][4]), "+v"(s.wh[S][5]), "+v"(s.wh[S][6]), "+v"(s.wh[S][7])
#define PJ_PIN4(S) PJ_PIN(S), "+v"(s.af[S][0]), "+v"(s.af[S][1]), "+v"(s.af[S][2]), "+v"(s.af[S][3])
#define PJ_PIN8(S) PJ_PIN4(S), "+v"(s.af[S][4]), "+v"(s.af[S][5]), "+v"(s.af[S][6]), "+v"(s.af[S][7])

DI void pj_wo_read(PJ& s) {
  asm volatile("ds_read_b64_tr_b16 %0, %8\n\tds_read_b64_tr_b16 %1, %8 offset:7168\n\tds_read_b64_tr_b16 %2, %8 offset:256\n\tds_read_b64_tr_b16 %3, %8 offset:7424\n\t"
               "ds_read_b64_tr_b16 %4, %8 offset:512\n\tds_read_b64_tr_b16 %5, %8 offset:7680\n\tds_read_b64_tr_b16 %6, %8 offset:768\n\tds_read_b64_tr_b16 %7, %8 offset:7936"
               : "=&v"(s.wlo[0]), "=&v"(s.whi[0]), "=&v"(s.wlo[1]), "=&v"(s.whi[1]), "=&v"(s.wlo[2]), "=&v"(s.whi[2]), "=&v"(s.wlo[3]), "=&v"(s.whi[3]) : "v"(s.wra) : "memory");
}
template <int Q> DI void pj_wo_store(PJ& s) {
  if constexpr (Q == 0) asm volatile("" : "+v"(s.wlo[0]), "+v"(s.whi[0]), "+v"(s.wlo[1]), "+v"(s.whi[1]), "+v"(s.wlo[2]), "+v"(s.whi[2]), "+v"(s.wlo[3]), "+v"(s.whi[3]));
  const u32 nt = s.wid + 4 * Q;
  const u32 so = nt < 14 ? s.sWo + nt * 1024 + (nt >= 7 ? 131072 - 7168 : 0) : OOB;
  st16<0>((u32x4){s.wlo[Q][0], s.wlo[Q][1], s.whi[Q][0], s.whi[Q][1]}, s.vP, s.rO, rfl(so));
}
template <int U> DI void gr_prod(PJ& s) {
  constexpr int e = U >> 1, rI = U & 1;
  const float gw = (e & 1) ? BF_HI(s.px[2][e >> 1]) : BF_LO(s.px[2][e >> 1]), wr = (e & 1) ? BF_HI(s.px[3][e >> 1]) : BF_LO(s.px[3][e >> 1]);
  const float xv = (e & 1) ? BF_HI(s.px[rI][e >> 1]) : BF_LO(s.px[rI][e >> 1]);
  if constexpr (e == 0) { s.sg[rI] = rbf(xv * gw); s.sr[rI] = xv * wr; } else { s.sg[rI] += rbf(xv * gw); s.sr[rI] = __builtin_fmaf(xv, wr, s.sr[rI]); }
  PINF(s.sg[rI]); PINF(s.sr[rI]);
}
template <int U> DI void gr_red(PJ& s) {
  if constexpr (U == 0) { s.sg[0] = sw_add<true>(s.sg[0], s.sg[1]); s.sr[0] = sw_add<true>(s.sr[0], s.sr[1]); PINF(s.sg[0]); PINF(s.sr[0]); }
  if constexpr (U == 1) { s.gv = sw_add<false>(s.sg[0], s.sr[0]); PINF(s.gv); }
  if constexpr (U >= 2 && U < 6) { s.gv = dpp_add<(U == 2 ? 0x128 : U == 3 ? 0x124 : U == 4 ? 0x122 : 0x121)>(s.gv); PINF(s.gv); }
  if constexpr (U == 6) { s.gv = rbf(s.gv); s.sg[0] = 1.f + __expf(-s.gv); PINF(s.gv); PINF(s.sg[0]); }
  if constexpr (U == 7) { s.sg[0] = rbf(__builtin_amdgcn_rcpf(s.sg[0])); PINF(s.sg[0]); }
}
DI void gr_store(PJ& s) {
  const int lane = lane_id(), rt = (lane >> 4) & 1;
  const u32 row = s.sRow + (lane >> 5);
  const bool ld = (lane & 15) == 0;
  const float v = rt ? s.gv : s.sg[0];
  const unsigned u = __float_as_uint(s.gv + 0.f) & 0xffff0000u;
  const u32x2 key = {((u & 0x80000000u) ? (~u & 0xffff0000u) : (u | 0x80000000u)) | (65535u - row), s.tag};
  const u32x2 ml = {0u, ML_SENT};
  asm volatile("buffer_store_dword %0, %1, %6, %7 offen" WT "\n\tbuffer_store_dwordx2 %2, %3, %6, %8 offen" WT "\n\tbuffer_store_dwordx2 %4, %5, %6, %9 offen" WT "\n\ts_nop 0"
               :: "v"(v), "v"(ld ? row * 4 + rt * 8192 : OOB), "v"(key), "v"((ld && rt) ? row * 8 : OOB), "v"(ml), "v"(lane < 32 ? (u32)lane * 8 : OOB),
                  "s"(s.rO), "s"((u32)WS_GATE), "s"((u32)WS_KEY), "s"(s.sM) : "memory");
}
template <bool SWAP, bool FIRST, int STG, bool EX, int M> DI void pj_gap0(PJ& s, u32 sWn, u32 sWd) {
  pj_mfma<SWAP, FIRST, 0, (M & 7), (M >> 3)>(s);
  if constexpr ((M & 1) == 0) pj_read<1, STG, 1, M / 2>(s);
  if constexpr (M % 8 == 5) {
    constexpr int q = M / 8;
    if constexpr (q < 4) pj_dmaW<STG ^ 1, 1, q>(s, sWn); else pj_dmaW<STG, 0, q - 4>(s, sWd);
  }
  if constexpr (EX && (M & 3) == 3) gr_prod<(M >> 2)>(s);
  if constexpr (EX) GAP();
}
template <bool SWAP, int STG, bool EX, int M> DI void pj_gap1(PJ& s, u32 sA) {
  pj_mfma<SWAP, false, 1, (M & 7), (M >> 3)>(s);
  if constexpr ((M & 1) == 0) pj_read<0, STG ^ 1, 0, M / 2>(s);
  if constexpr (M % 8 == 5) pj_dmaA<STG, M / 8>(s, sA);
  if constexpr (EX && (M & 3) == 3 && (M >> 2) < 8) gr_red<(M >> 2)>(s);
  if constexpr (EX) GAP();
}
template <bool SWAP, bool FIRST, int STG, bool EX, int VM, int... Ms> DI void pj_sub0(PJ& s, u32 sWn, u32 sWd, iseq<Ms...>) {
  asm volatile("s_waitcnt vmcnt(%24) lgkmcnt(0)\n\ts_barrier" : PJ_PIN8(0) : "i"(VM) : "memory");
  (pj_gap0<SWAP, FIRST, STG, EX, Ms>(s, sWn, sWd), ...);
}
template <bool SWAP, int STG, bool EX, int VM, int... Ms> DI void pj_sub1(PJ& s, u32 sA, iseq<Ms...>) {
  asm volatile("s_waitcnt vmcnt(%24) lgkmcnt(0)\n\ts_barrier" : PJ_PIN8(1) : "i"(VM) : "memory");
  (pj_gap1<SWAP, STG, EX, Ms>(s, sA), ...);
}
template <bool SWAP, bool FIRST, int STG, int P0, int P1, bool EX = false>
DI void pj_step(PJ& s, u32 sWn, u32 sWd, u32 sAd, srd_t rT, u32 sT0, u32 sT1, u32 vT) {
  pj_sub0<SWAP, FIRST, STG, EX, 4 + 8 + P0 + P1>(s, sWn, sWd, make_iseq<64>{});
  dma16<0, 0>(s.m0P, sT0, vT, rT);
  pj_sub1<SWAP, STG, EX, P1 + 4 + 4 + 1>(s, sAd, make_iseq<64>{});
  dma16<0, 0>(s.m0P, sT1, vT, rT);
}
template <int STG, int KS, int... Js> DI void pj_dmaW_all(PJ& s, u32 sW, iseq<Js...>) { (pj_dmaW<STG, KS, Js>(s, sW), ...); }
template <int STG, int... Js> DI void pj_dmaA_all(PJ& s, u32 sA, iseq<Js...>) { (pj_dmaA<STG, Js>(s, sA), ...); }
template <int... Rs> DI void pj_read_all(PJ& s, iseq<Rs...>) { (pj_read<0, 0, 0, Rs>(s), ...); }

constexpr int Z0 = 24, ZQ = 32, ZA = 46;
constexpr int zGaps(int z) { return z < ZQ ? 64 : z < ZA ? 16 : 32; }
constexpr int zBar(int z) { return z < ZQ ? 0 : z == ZQ + 12 ? -1 : z < ZA ? 8 : 24; }
#define ZF0 34
#define ZC0 40
constexpr int ZF3 = ZA, ZB = 47, ZE = 48;
#define QPAT "W.S.x.x.S.W.W.W."
constexpr char qPat(int M) { return QPAT[M]; }
constexpr int qCnt(char c, int M) { int n = 0; for (int i = 8; (i & 15) != M; ++i) n += qPat(i & 15) == c; return n; }
constexpr int zD(int z, int M) {
  if (z < ZQ) {
    if (z == 31) return (M & 1) && M < 8 ? 8 * 35 + M / 2 : -1;
    if (M % 8 != 5) return -1;
    const int q = M / 8;
    if (!(z & 1)) return 8 * (z + 3 + (q >> 2)) + (q & 3);
    return z < 29 ? 512 + 8 * (z / 2 + 2) + q : 512 + 8 * (16 + (q >> 2)) + (q & 3);
  }
  if (z == ZA) return M == 0 ? 1024 + 23 : -1;
  if (z == ZB) return M & 1 ? -1 : 1024 + 24 + M / 2;
  if (z == ZE) return M % 4 == 2 ? 1024 + 40 + M / 4 : -1;
  const int iv = z - ZQ - (M < 8);
  const char c = qPat(M);
  if (c == 'W' && iv >= 0 && iv <= 9) return 8 * (ZQ + iv + 4) + qCnt('W', M);
  if (c == 'x' && iv <= 8) return 512 + 8 * (18 + (iv + 1) / 2) + 2 * ((iv + 1) & 1) + qCnt('x', M);
  if (iv < 9 || (c != 'W' && c != 'x')) return -1;
  return 1024 + 6 * (iv - 10) + 2 + qCnt('W', M) + qCnt('x', M);
}
constexpr bool zDfirst(int z, int M) {
  const int d = zD(z, M);
  if ((d & 3) == 0) return true;
  for (;;) { if (--M < 0) { --z; M = zGaps(z) - 1; } if (zD(z, M) >= 0) return zD(z, M) != d - 1; }
}
constexpr int zS(int z, int M) {
  if (z == 31) return M % 8 == 7 ? M / 8 : -1;
  if (z < ZQ || z >= ZA || qPat(M) != 'S') return -1;
  const int iv = z - ZQ - (M < 8);
  return iv >= 0 && iv < 12 ? 8 + 2 * iv + qCnt('S', M) : -1;
}
constexpr int zPk(int z, int M) {
  if (z < ZQ || !(M & 1)) return -1;
  const int d = 32 + 8 * (z - ZQ) + M / 2;
  return d < 128 ? d : -1;
}
constexpr int zWo(int z, int M) { return (z == Z0 + 1 && M >= 41 && M % 6 == 5) ? (M - 41) / 6 : -1; }
constexpr int zPoll(int z, int M) { return 2 * (z == ZC0 - 2 && M == 9) + (z == ZE && (M == 15 || M == 17)); }
constexpr int qcvN(int J) { const int n = J <= 5 ? 0 : ((J - 5) * 64 + 55) / 56; return n > 64 ? 64 : n; }
constexpr bool qcv_ok() {
  for (int k = 0; k < 64; ++k) { int J = 0; while (qcvN(J + 1) <= k) ++J; if (J < 4 * (k >> 3) + 5 || J > 30 + 2 * (k >> 2)) return false; }
  return true;
}
static_assert(qcv_ok(), "q conversion: two products behind the tile's last, two gaps before the fragment's use");
constexpr int zJ(int z, int M) { return z >= ZA ? 32 * (z - ZA + 1) + M : z >= ZQ + 12 ? 16 * (z - ZQ - 12) + M : -1; }
#define QS0 19
#define QSD 3
constexpr int zQs(int z, int M) { const int J = zJ(z, M) - QS0; return J >= 0 && J % QSD == 0 && J / QSD < 16 ? J / QSD : -1; }
constexpr bool qs_ok() { for (int f = 0; f < 16; ++f) if (qcvN(QS0 + QSD * f) < 4 * f + 4) return false; return QS0 + QSD * 7 < 56 && QS0 + QSD * 15 < 80; }
static_assert(qs_ok(), "q rows are stored behind their conversion");
constexpr int zFlag(int z, int M) { return (z == ZF0 && M == 8) + (z == ZA && M == 24); }
constexpr int zOps(int z, int M) { return zFlag(z, M) + (zD(z, M) >= 0) + (zS(z, M) >= 0) + (zWo(z, M) >= 0) + (zQs(z, M) >= 0) + zPoll(z, M); }
constexpr int zYoung(int z0, int M0, int KIND, int z1, int M1) {
  int n = (KIND < 1 ? (zS(z0, M0) >= 0) : 0) + (KIND < 2 ? (zWo(z0, M0) >= 0) + (zQs(z0, M0) >= 0) : 0) + (KIND < 3 ? zPoll(z0, M0) : 0);
  for (int z = z0; z <= z1; ++z)
    for (int M = z == z0 ? M0 + 1 : 0; M < (z == z1 ? M1 : zGaps(z)); ++M) n += zOps(z, M);
  return n > 63 ? 63 : n;
}
constexpr int zYoungD(int d, int z1, int M1) { for (int z = Z0; z <= ZE; ++z) for (int M = 0; M < zGaps(z); ++M) if (zD(z, M) == d) return (z < z1 || (z == z1 && M < M1)) ? zYoung(z, M, 0, z1, M1) : -1000; return -1000; }
constexpr int zYoungS(int I, int z1, int M1) { for (int z = Z0; z <= ZA; ++z) for (int M = 0; M < zGaps(z); ++M) if (zS(z, M) == I) return (z < z1 || (z == z1 && M < M1)) ? zYoung(z, M, 1, z1, M1) : -1000; return -1000; }
constexpr int zYoungQ(int F, int z1, int M1) { for (int z = Z0; z <= ZE; ++z) for (int M = 0; M < zGaps(z); ++M) if (zQs(z, M) == F) return zYoung(z, M, 2, z1, M1); return -1000; }
constexpr int imin(int a, int b) { return a < b ? a : b; }
constexpr int imax(int a, int b) { return a > b ? a : b; }
constexpr int zWait(int z) {
  if (z == Z0) return 4 + 1 + 8 + 2;
  if (z == Z0 + 1) return 2 + 8;
  const int B = zBar(z); int n = 63;
  if (z < ZQ + 12) n = imin(n, zYoungD(8 * (z + 1) + 3, z, B));
  if (z == ZQ + 11) n = imin(n, zYoungD(8 * (z + 2) + 3, z, B));
  if ((z & 1) && z < ZQ + 12) n = imin(n, zYoungD(512 + 8 * (z + 1) / 2 + (z + 1 < ZQ ? 7 : 3), z, B));
  if (z == ZF0) n = imin(n, zYoungS(7, z, B));
  if (z == ZF3) n = imin(n, zYoungS(31, z, B));
  if (z == ZC0) n = imin(n, zYoung(ZC0 - 2, 9, 3, z, B));
  if (z == ZQ + 13) n = imin(n, zYoungD(1024 + 7, z, B));
  if (z == ZA) n = imin(n, zYoungD(1024 + 15, z, B));
  if (z == ZB) n = imin(n, zYoungD(1024 + 23, z, B));
  if (z == ZE) n = imin(n, zYoungD(1024 + 39, z, B));
  return n;
}
constexpr bool z_ok() {
  for (int z = Z0; z <= ZE; ++z) if (zBar(z) >= 0 && zWait(z) < 0) return false;
  int pk = 32, st = 8;
  for (int z = ZQ; z < ZA; ++z) for (int M = 0; M < 16; ++M) {
    if (zS(z, M) >= 0) { if (zS(z, M) != st || pk < 4 * st + 4) return false; ++st; }
    if (zPk(z, M) >= 0) { if (zPk(z, M) != pk || pk / 4 >= st + 2) return false; ++pk; }
  }
  return pk == 128 && st == 32 && ZC0 <= ZQ + 9 && zD(ZQ + 13, 14) == 1024 + 22 && zD(ZQ + 11, 7) < 1024 + 16;
}
static_assert(z_ok(), "what a barrier waits for was requested before it");
DI void kv_check(u32& poll, u32 tag, unsigned long long need, u32 vFl, srd_t r, u32 sFl) {
  unsigned long long t; u32 n = SPIN_MAX;
  asm volatile(".Lk%=:\n\tv_cmp_eq_u32_e32 vcc, %3, %0\n\ts_nop 3\n\ts_and_b64 %1, vcc, %4\n\ts_cmp_eq_u64 %1, %4\n\ts_cbranch_scc1 .Lg%=\n\t"
               "s_sub_u32 %2, %2, 1\n\ts_cbranch_scc1 .Lg%=\n\t"
               "s_sleep 1\n\tbuffer_load_dword %0, %5, %6, %7 offen sc0 sc1\n\ts_waitcnt vmcnt(0)\n\ts_branch .Lk%=\n.Lg%=:"
               : "+v"(poll), "=&s"(t), "+s"(n) : "s"(tag), "s"(need), "v"(vFl), "s"(r), "s"(sFl) : "memory", "scc", "vcc");
}
DI void kv_check_all(u32& poll, u32 tag, u32 vFl, srd_t r, u32 sFl) {
  u32 n = SPIN_MAX;
  asm volatile(".Lk%=:\n\tv_cmp_eq_u32_e32 vcc, %2, %0\n\ts_nop 3\n\ts_cmp_eq_u64 vcc, -1\n\ts_cbranch_scc1 .Lg%=\n\t"
               "s_sub_u32 %1, %1, 1\n\ts_cbranch_scc1 .Lg%=\n\t"
               "s_sleep 1\n\tbuffer_load_dword %0, %3, %4, %5 offen sc0 sc1\n\ts_waitcnt vmcnt(0)\n\ts_branch .Lk%=\n.Lg%=:"
               : "+v"(poll), "+s"(n) : "s"(tag), "v"(vFl), "s"(r), "s"(sFl) : "memory", "scc", "vcc");
}
DI void kv_poll(u32& poll, u32 vFl, srd_t r, u32 sFl) { asm volatile("buffer_load_dword %0, %1, %2, %3 offen sc0 sc1" : "=v"(poll) : "v"(vFl), "s"(r), "s"(sFl) : "memory"); }
template <int Z, int M> DI void z_dma(PJ& s) {
  constexpr int d = zD(Z, M);
  constexpr bool F = zDfirst(Z, M);
  if constexpr (d >= 1024) {
    constexpr int p = d - 1024, J = p & 7;
    dma16q<(p < 8 ? AT_X : p < 16 ? 32768 : 98304), (p >= 8 && p < 16 ? 32768 : 0), J, F>(s.m0K, p < 16 ? s.sK0 : s.sV0, s.vL, s.rO, s.st);
  } else if constexpr (d >= 512) {
    constexpr int g = (d - 512) >> 3, J = d & 7;
    if constexpr (g < 16) dma16g<(g & 1) * 32768 + (J >> 2) * 4096, g * 128 + (J >> 2) * 65536, (J & 3)>(s.m0A, s.sAkv, s.vAkv[J & 3], s.rA, s.st);
    else dma16g<(g & 1) * 16384, (g - 16) * 128, J, F>(s.m0Aq, s.sAq, s.vAq[J], s.rX, s.st);
  } else if constexpr (d >= 0) {
    constexpr int v = d >> 3, J = d & 7;
    if constexpr (v < ZQ) dma16g<(v & 3) * 16384, v * 262144, J>(s.m0W, s.sWkv, s.vW[J], s.rW, s.st);
    else dma16g<(v & 3) * 16384, (v - ZQ) * 262144, J>(s.m0W, s.sWq, s.vWq[J], s.rWq, s.st);
  }
}
#define STMASK 0xffffffffu
template <int I> DI void z_store(PJ& s, u32x4 v) {
  u32 vO = s.vO; if constexpr (!((STMASK >> I) & 1)) vO |= OOB;
  if constexpr (I == 0) asm volatile("buffer_store_dwordx4 %0, %1, %2, %3 offen" WT :: "v"(v), "v"(vO), "s"(s.rO), "s"(s.sO) : "memory");
  else asm volatile("s_add_u32 %0, %0, %4\n\tbuffer_store_dwordx4 %1, %2, %3, %0 offen" WT
                    : "+s"(s.sO) : "v"(v), "v"(vO), "s"(s.rO), "s"(s.dO[(I & 3) ? 0 : (I & 7) ? 1 : 1 + I / 8]) : "memory", "scc");
}
template <int LV> DI void z_flag(PJ& s) {
  asm volatile("buffer_store_dword %0, %1, %2, %3 offen offset:%4" WT :: "v"(s.tag), "v"(s.vP ? OOB : 0u), "s"(s.rO), "s"(s.wid ? OOB : s.sFl), "i"(4 * LV) : "memory");
}
template <int U, int F, int H> DI void q_read(PJ& s) {
  constexpr bool JT = U >= 12;
  constexpr int dt = JT ? F >> 2 : F & 7, v = JT ? 12 + ((F >> 1) & 1) : U, kk = JT ? F & 1 : F >> 3, set = JT ? F >> 4 : U & 1, i = F & 15;
  constexpr int OFF = (v & 3) * 16384 + kk * 8192 + (dt >> 2) * 256 + H * 2048;
  if constexpr (H == 0) asm volatile("ds_read_b64_tr_b16 %0, %1 offset:%2" : "=v"(s.ql[set][i]) : "v"(s.qb[dt & 3]), "i"(OFF) : "memory");
  else                  asm volatile("ds_read_b64_tr_b16 %0, %1 offset:%2" : "=v"(s.qh[set][i]) : "v"(s.qb[dt & 3]), "i"(OFF) : "memory");
}
template <int G, int KK> DI void x_read(PJ& s) { asm volatile("ds_read_b128 %0, %1 offset:%2" : "=v"(s.xf[G & 1][KK]) : "v"(s.xb[KK]), "i"((G & 1) * 16384) : "memory"); }
template <bool VT, int Z, int M> DI void z_gap(PJ& s) {
  constexpr int SUB = Z & 1, STG = (Z >> 1) & 1;
  if constexpr (M == 0) {
    asm volatile("s_waitcnt vmcnt(%24) lgkmcnt(0)\n\ts_barrier" : PJ_PIN8(SUB) : "i"(zWait(Z)) : "memory");
  }
  if constexpr (Z == 30 && M == 48) asm volatile("s_waitcnt lgkmcnt(6)" : PJ_PIN(1), "+v"(s.af[1][0]), "+v"(s.af[1][1]) :: "memory");
  if constexpr (Z == 30) {
    if constexpr (M < 48) pj_mfma<VT, false, 0, M % 6, M / 6>(s); else pj_mfma<VT, false, 1, pjMt<VT>(M - 48), pjNt<VT>(M - 48)>(s);
  } else if constexpr (Z == 31) {
    constexpr int T = M < 16 ? 16 + M : M < 32 ? 32 + M : M;
    pj_mfma<VT, false, (M >= 16 && M < 32) ? 0 : 1, pjMt<VT>(T), pjNt<VT>(T)>(s);
  } else pj_mfma<VT, false, SUB, (M & 7), (M >> 3)>(s);
  if constexpr (Z < 31) { if constexpr ((M & 1) == 0) pj_read<SUB ^ 1, STG ^ SUB, SUB ^ 1, M / 2>(s); }
  else {
    if constexpr ((M & 1) && M >= 9 && M < 49) { constexpr int r = (M - 9) / 2; if constexpr (r < 4) x_read<16, r>(s); else q_read<0, (r - 4) / 2, (r - 4) & 1>(s); }
    if constexpr ((M & 1) == 0) asm volatile("v_accvgpr_read_b32 %0, a[%c2]\n\tv_accvgpr_read_b32 %1, a[%c3]" : "=v"(s.pt[0]), "=v"(s.pt[1]) : "i"(M), "i"(M + 1));
    else { u32 w; asm volatile("v_cvt_pk_bf16_f32 %0, %1, %2" : "=v"(w) : "v"(s.pt[0]), "v"(s.pt[1])); s.pv[0][(M >> 1) & 3] = w; }
  }
  z_dma<Z, M>(s);
  if constexpr (Z == 31 && M > 0 && zS(Z, M - 1) >= 0) asm volatile("" :: "v"(s.pl));
  if constexpr (zS(Z, M) >= 0) { z_store<zS(Z, M)>(s, s.pv[0]); s.pl = s.pv[0]; }
  if constexpr (Z == Z0 && M == 51) pj_wo_read(s);
  if constexpr (zWo(Z, M) >= 0) pj_wo_store<zWo(Z, M)>(s);
  if constexpr (Z == 31) GAP();
}
template <bool VT, int Z, int... Ms> DI void z_sub(PJ& s, iseq<Ms...>) { (z_gap<VT, Z, Ms>(s), ...); }
template <bool VT, int... Zs> DI void z_all(PJ& s, iseq<Zs...>) { (z_sub<VT, Z0 + Zs>(s, make_iseq<64>{}), ...); }

template <bool VT> DI void pj_body1(PJ& s) {
  constexpr u32 WS = 524288;
  asm volatile("s_waitcnt vmcnt(16) lgkmcnt(0)\n\ts_barrier" : "+v"(s.px[0]), "+v"(s.px[1]), "+v"(s.px[2]), "+v"(s.px[3]), "+s"(s.tag) :: "memory");
  s.tag = rfl(s.tag * 1664525u + 1013904223u);
  asm volatile("" : "+s"(s.tag));
  pj_read_all(s, make_iseq<24>{});
  PSTAMP(1);
  pj_step<VT, true, 0, 0, 0, true>(s, s.sWkv + 1 * WS, s.sWkv + 2 * WS, s.sAkv + 2 * 128, s.rA, s.sP, s.sP + 1048576u, s.vP);
  gr_store(s);
  pj_step<VT, false, 1, 1, 4>(s, s.sWkv + 2 * WS, s.sWkv + 3 * WS, s.sAkv + 3 * 128, s.rA, s.sP + 2097152u, s.sP + 3145728u, s.vP);
  CODE_WARM(k_main, .Lk_code_end, s.m0P, ((blockIdx.x >> 3) * 4 + s.wid) * 1024 + 131072, s.vP);
  for (int t = 2; t < Z0 / 2; t += 2) {
    PSTAMP(1 + t / 2);
    const bool ff = t == 10;
    const srd_t rT0 = t < 8 ? s.rW : t == 8 ? s.rWq : mk_srd(s.pg, M_ * F_ * 2), rT1 = ff ? mk_srd(s.pu, M_ * F_ * 2) : rT0;
    const u32 dT = t < 8 ? WS : ff ? 917504u : 1048576u, sT0 = t < 8 ? s.sPw + (2 * t + 1) * WS : t == 8 ? s.sPq : s.sPf, sT1 = ff ? sT0 : sT0 + 2 * dT, vT = ff ? s.vT : s.vP;
    const srd_t rP = t == 6 ? s.rX : ff ? mk_srd(s.pd, M_ * F_ * 2) : mk_srd(s.pwo, t < 6 ? 16 * M_ * 2 : 0);
    const u32 m3 = s.m0P + (t < 6 ? (u32)(t - 1) * 4096u : 0u), s3 = ff ? s.sPd : t >= 6 ? s.sP : (s.sP & 3072u) + (u32)(t - 2) * 4096u, d3 = t < 6 ? 4096u : 0u, e3 = ff ? 131072u : t >= 6 ? 1048576u : 4096u;
    pj_step<VT, false, 0, 1, 2>(s, s.sWkv + (t + 1) * WS, s.sWkv + (t + 2) * WS, s.sAkv + (t + 2) * 128, rT0, sT0, sT0 + dT, vT);
    dma16<0, 0>(m3, s3, s.vP, rP);
    pj_step<VT, false, 1, 1, 2>(s, s.sWkv + (t + 1) * WS + WS, s.sWkv + (t + 2) * WS + WS, s.sAkv + (t + 3) * 128, rT1, sT1, sT1 + dT, vT);
    dma16<0, 0>(m3 + d3, s3 + e3, s.vP, rP);
    asm volatile("s_cmp_eq_u32 %0, 0\n\ts_cbranch_scc1 .Lq%=\n\tbuffer_store_dwordx4 %2, %3, %4, %1 offen" WT "\n\tbuffer_store_dwordx4 %2, %3, %4, %1 offen offset:1024" WT "\n\t"
                 "buffer_store_dwordx4 %2, %3, %4, %1 offen offset:2048" WT "\n\tbuffer_store_dwordx4 %2, %3, %4, %1 offen offset:3072" WT "\n\ts_add_u32 %1, %1, 4096\n\ts_sub_u32 %0, %0, 1\n.Lq%=:"
                 : "+s"(s.nQf), "+s"(s.sQf) : "v"(s.ones), "v"(s.vP), "s"(s.rO) : "memory", "scc");
  }
}
#define Q_PIN4(S, F) "+v"(s.ql[S][F]), "+v"(s.qh[S][F]), "+v"(s.ql[S][F + 1]), "+v"(s.qh[S][F + 1]), "+v"(s.ql[S][F + 2]), "+v"(s.qh[S][F + 2]), "+v"(s.ql[S][F + 3]), "+v"(s.qh[S][F + 3])
#define X_PIN(S) "+v"(s.xf[S][0]), "+v"(s.xf[S][1]), "+v"(s.xf[S][2]), "+v"(s.xf[S][3])
template <bool VT> DI void pj_body2(PJ& s) {
  PSTAMP(8);
  z_all<VT>(s, make_iseq<8>{});
  asm volatile("s_waitcnt lgkmcnt(0)\n\ts_nop 0" : Q_PIN4(0, 0), Q_PIN4(0, 4), X_PIN(0) : "v"(s.pl) : "memory");
}

constexpr int qrN(int u, int M) {
  if (u >= 12) { const int J = 16 * (u - 12) + M, n = 16 + (5 * J + 1) / 2; return 384 + (n > 64 ? 64 : n); }
  return 32 * u + (M <= 6 ? 16 + (5 * M + 1) / 2 : M <= 8 ? 32 : 32 + 2 * (M - 8));
}
static_assert(qrN(0, 0) == 16 && qrN(0, 16) == qrN(1, 0) && qrN(11, 16) == qrN(12, 0) && qrN(12, 16) == qrN(13, 0) && qrN(13, 5) == 448, "read sequence");
constexpr int qrX(int u, int M) { return ((u & 1) && u < 12 && M >= 8 && M < 12) ? M - 8 : -1; }
constexpr int qrYoung(int u, int M, int h) {
  int n = qrN(u, M) - h - 1;
  for (int g = 0; g < 16 * u + M; ++g) if (qrX(g / 16, g % 16) >= 0 && qrN(g / 16, g % 16) > h) ++n;
  return n;
}
template <int H0, int... Hs> DI void q_reads(PJ& s, iseq<Hs...>) {
  (q_read<((H0 + Hs) >= 384 ? 12 : (H0 + Hs) / 32), ((H0 + Hs) >= 384 ? (H0 + Hs - 384) / 2 : ((H0 + Hs) / 2) & 15), (H0 + Hs) & 1>(s), ...);
}
template <int K, int B = 192> DI void q_cv(PJ& s) {
  float t0, t1;
  asm volatile("v_mul_f32_e32 %0, %4, %2\n\tv_mul_f32_e32 %1, %4, %3\n\tv_cvt_pk_bf16_f32 %0, %0, %1\n\tv_accvgpr_write_b32 a[%c5], %0"
               : "=&v"(t0), "=&v"(t1) : "v"(s.qa[K >> 3][2 * (K & 7)]), "v"(s.qa[K >> 3][2 * (K & 7) + 1]), "s"(0.0625f * 1.4426950408889634f), "i"(B + K) : ACL);
}
template <int J> DI void q_cvs(PJ& s) {
  if constexpr (qcvN(J + 1) > qcvN(J)) q_cv<qcvN(J)>(s);
  if constexpr (qcvN(J + 1) > qcvN(J) + 1) q_cv<qcvN(J) + 1>(s);
}
template <int F> DI void q_store(u32 vl16, srd_t r, u32 so) {
  u32 t;
  asm volatile("s_add_u32 %0, %3, %4\n\tbuffer_store_dwordx4 a[%c5:%c6], %1, %2, %0 offen offset:%7" WT
               : "=&s"(t) : "v"(vl16), "s"(r), "s"(so), "i"((F >> 2) * 4096), "i"(192 + 4 * F), "i"(195 + 4 * F), "i"((F & 3) * 1024) : "memory", "scc");
}
template <int F> DI void at_kread0(u32x4 (&kf)[32], u32 kb0) {
  asm volatile("ds_read_b128 %0, %1 offset:%2" : "=v"(kf[F]) : "v"(kb0), "i"((F >> 1) * 2048 + (F & 1) * 512) : "memory");
}
template <int U, int M> DI void q_gap(PJ& s, u32x4 (&kf)[32], u32 kb0) {
  constexpr int Z = ZQ + U, J = zJ(Z, M);
  constexpr bool JT = U >= 12;
  constexpr int F = JT ? J : M, dt = JT ? F >> 2 : F & 7, kk = JT ? F & 3 : 2 * (U & 1) + (F >> 3), set = JT ? F >> 4 : U & 1, i = F & 15;
  if constexpr (M == zBar(Z)) {
    if constexpr (Z == ZC0) asm volatile("s_waitcnt vmcnt(%18) lgkmcnt(0)\n\ts_barrier" : Q_PIN4(set, 8), Q_PIN4(set, 12), "+v"(s.poll), "+v"(s.iv) : "i"(zWait(Z)) : "memory");
    else asm volatile("s_waitcnt vmcnt(%16) lgkmcnt(0)\n\ts_barrier" : Q_PIN4(set, 8), Q_PIN4(set, 12) : "i"(zWait(Z)) : "memory");
    if constexpr (Z == ZF0) z_flag<0>(s);
    if constexpr (Z == ZC0) kv_check_all(s.poll, s.tag, s.vFl, s.rO, s.sFl & ~255u);
  } else if constexpr (M % 4 == 0 && (JT ? J < 24 : M < 8) && !(U == 0 && M < 8)) {
    constexpr int n = imin(15, qrYoung(U, M, (JT ? 384 : 32 * U) + 2 * F + 7));
    static_assert(n >= 0, "lgkmcnt");
    if constexpr (M == 0 && !(U & 1)) asm volatile("s_waitcnt lgkmcnt(%12)" : Q_PIN4(set, i), X_PIN((U >> 1) & 1) : "i"(n) : "memory");
    else asm volatile("s_waitcnt lgkmcnt(%8)" : Q_PIN4(set, i) : "i"(n) : "memory");
  }
  {
    const u32x4 w = {s.ql[set][i][0], s.ql[set][i][1], s.qh[set][i][0], s.qh[set][i][1]};
    if constexpr (U == 0 && kk == 0) asm volatile("v_mfma_f32_32x32x16_bf16 %0, %1, %2, 0" : "=&v"(s.qa[dt]) : "v"(w), "v"(s.xf[0][0]));
    else asm volatile("v_mfma_f32_32x32x16_bf16 %0, %1, %2, %0" : "+v"(s.qa[dt]) : "v"(w), "v"(s.xf[(U >> 1) & 1][kk]));
  }
  if constexpr (qrX(U, M) >= 0) x_read<16 + (U + 1) / 2, qrX(U, M)>(s);
  q_reads<qrN(U, M)>(s, make_iseq<qrN(U, M + 1) - qrN(U, M)>{});
  if constexpr (U == 13 && M >= 8 && M < 12) { at_kread0<2 * (M - 8)>(kf, kb0); at_kread0<2 * (M - 8) + 1>(kf, kb0); }
  if constexpr (U == 13 && M >= 12) at_kread0<M - 4>(kf, kb0);
  z_dma<Z, M>(s);
  if constexpr (zPk(Z, M) >= 0) s.pv[(zPk(Z, M) >> 2) & 1][zPk(Z, M) & 3] = acc_pk<2 * zPk(Z, M)>();
  if constexpr (M > 0 ? zS(Z, M - 1) >= 0 : U > 0 && zS(Z - 1, 15) >= 0) asm volatile("" :: "v"(s.pl));
  if constexpr (zS(Z, M) >= 0) { z_store<zS(Z, M)>(s, s.pv[zS(Z, M) & 1]); s.pl = s.pv[zS(Z, M) & 1]; }
  if constexpr (JT) q_cvs<J>(s);
  if constexpr (zQs(Z, M) >= 0) q_store<zQs(Z, M)>(s.vP, s.rO, s.sQo);
  if constexpr (zPoll(Z, M)) {
    u32 z = 0u;
    asm volatile("buffer_load_dwordx2 %0, %1, %2, %3 offen sc0 sc1" : "=v"(s.iv) : "v"(z), "s"(s.rO), "s"((u32)WS_IDX) : "memory");
    kv_poll(s.poll, s.vFl, s.rO, s.sFl & ~255u);
  }
  GAP();
}
template <int U, int... Ms> DI void q_sub(PJ& s, u32x4 (&kf)[32], u32 kb0, iseq<Ms...>) { (q_gap<U, Ms>(s, kf, kb0), ...); }
template <int... Us> DI void q_all(PJ& s, u32x4 (&kf)[32], u32 kb0, iseq<Us...>) { (q_sub<Us>(s, kf, kb0, make_iseq<16>{}), ...); }
constexpr int FA = 0;
struct FF {
  float v[32], xs[8], t[3], hq; u32 rd[8]; f32x4 h0, h1;
  u32 vw, vx, vd, vy, lr, lw, lh, lg;
  u32 i0, i1;
  const u16 *x, *wg, *wu, *wd; srd_t rA;
};
template <int L> DI void ff_load(FF& f) {
  if constexpr (L < 8) {
    constexpr int q = L >> 1, mat = L & 1, R = FA + 16 * mat + 4 * q;
    asm volatile("buffer_load_dwordx4 a[%c3:%c4], %0, %1, %2 offen offset:%5" :: "v"(f.vw), "s"(mk_srd(mat ? f.wu : f.wg, M_ * F_ * 2)), "s"((q >> 1) * 7168), "i"(R), "i"(R + 3), "i"((q & 1) * 3584) : "memory", ACL);
  } else if constexpr (L < 10) {
    constexpr int R = FA + 48 + 2 * (L - 8);
    asm volatile("buffer_load_dwordx2 a[%c3:%c4], %0, %1, %2 offen" :: "v"(f.vx), "s"(mk_srd(f.x, S_ * M_ * 2)), "s"((L == 8 ? f.i0 : f.i1) * (M_ * 2)), "i"(R), "i"(R + 1) : "memory", ACL);
  } else {
    constexpr int j = L - 10, R = FA + 32 + 4 * j;
    asm volatile("buffer_load_dwordx4 a[%c2:%c3], %0, %1, 0 offen offset:%4" :: "v"(f.vd), "s"(mk_srd(f.wd, M_ * F_ * 2)), "i"(R), "i"(R + 3), "i"(j * 896) : "memory", ACL);
  }
}
template <int R, bool FIRST, int HALF> DI void ff_unit(FF& f, int b, float a0, float a1) {
  if constexpr (HALF == 0)
    asm volatile("v_accvgpr_read_b32 %1, a[%c2]\n\tv_lshlrev_b32_e32 %0, 16, %1\n\tv_and_b32_e32 %1, 0xffff0000, %1" : "=&v"(f.t[0]), "=&v"(f.t[1]) : "i"(R));
  else if constexpr (FIRST)
    asm volatile("v_mul_f32_e32 %0, %4, %6\n\tv_mul_f32_e32 %1, %4, %7\n\tv_mul_f32_e32 %2, %5, %6\n\tv_mul_f32_e32 %3, %5, %7"
                 : "=&v"(f.v[b]), "=&v"(f.v[b + 1]), "=&v"(f.v[b + 8]), "=&v"(f.v[b + 9]) : "v"(a0), "v"(a1), "v"(f.t[0]), "v"(f.t[1]));
  else
    asm volatile("v_fmac_f32_e32 %0, %4, %6\n\tv_fmac_f32_e32 %1, %4, %7\n\tv_fmac_f32_e32 %2, %5, %6\n\tv_fmac_f32_e32 %3, %5, %7"
                 : "+v"(f.v[b]), "+v"(f.v[b + 1]), "+v"(f.v[b + 8]), "+v"(f.v[b + 9]) : "v"(a0), "v"(a1), "v"(f.t[0]), "v"(f.t[1]));
}
template <int Q> DI void ff_step(FF& f) {
  if constexpr (Q == 56 || Q == 57) {
    constexpr int c = Q - 56;
    const u32 a = __float_as_uint(acc_rd<FA + 48 + 2 * c>()), b = __float_as_uint(acc_rd<FA + 49 + 2 * c>());
    f.xs[4 * c] = BF_LO(a); f.xs[4 * c + 1] = BF_HI(a); f.xs[4 * c + 2] = BF_LO(b); f.xs[4 * c + 3] = BF_HI(b);
    PINF(f.xs[4 * c]); PINF(f.xs[4 * c + 1]); PINF(f.xs[4 * c + 2]); PINF(f.xs[4 * c + 3]);
  }
  if constexpr (Q >= 58 && Q < 122) {
    constexpr int u = (Q - 58) >> 1, q = u >> 3, mat = (u >> 2) & 1, dw = u & 3;
    ff_unit<FA + 16 * mat + 4 * q + dw, q == 0, (Q & 1)>(f, 16 * mat + 2 * dw, f.xs[q], f.xs[4 + q]);
  }
  if constexpr (Q >= 122 && Q < 128) {
#pragma unroll
    for (int i = 3 * (Q - 122); i < 3 * (Q - 122) + 3 && i < 16; ++i) { f.v[i] = sw_add<true>(f.v[i], f.v[16 + i]); PINF(f.v[i]); }
  }
  if constexpr (Q >= 128 && Q < 131) {
#pragma unroll
    for (int i = 3 * (Q - 128); i < 3 * (Q - 128) + 3 && i < 8; ++i) { f.v[i] = sw_add<false>(f.v[i], f.v[8 + i]); PINF(f.v[i]); }
  }
  if constexpr (Q >= 131 && Q < 134) {
#pragma unroll
    for (int i = 0; i < 8; ++i) { f.v[i] = dpp_add<(Q == 131 ? 0x128 : Q == 132 ? 0x124 : 0x122)>(f.v[i]); PINF(f.v[i]); }
  }
  if constexpr (Q == 134) {
    const f32x4 a = {f.v[0], f.v[1], f.v[2], f.v[3]}, b = {f.v[4], f.v[5], f.v[6], f.v[7]};
    asm volatile("ds_write_b128 %0, %1\n\tds_write_b128 %0, %2 offset:16\n\tds_write_b128 %0, %1 offset:8192\n\tds_write_b128 %0, %2 offset:8208\n\t"
                 "ds_write_b128 %0, %1 offset:16384\n\tds_write_b128 %0, %2 offset:16400\n\tds_write_b128 %0, %1 offset:24576\n\tds_write_b128 %0, %2 offset:24592" :: "v"(f.lw), "v"(a), "v"(b) : "memory");
  }
  if constexpr (Q == 137)
    asm volatile("ds_read_b32 %0, %8\n\tds_read_b32 %1, %8 offset:256\n\tds_read_b32 %2, %8 offset:512\n\tds_read_b32 %3, %8 offset:768\n\t"
                 "ds_read_b32 %4, %8 offset:128\n\tds_read_b32 %5, %8 offset:384\n\tds_read_b32 %6, %8 offset:640\n\tds_read_b32 %7, %8 offset:896"
                 : "=&v"(f.rd[0]), "=&v"(f.rd[1]), "=&v"(f.rd[2]), "=&v"(f.rd[3]), "=&v"(f.rd[4]), "=&v"(f.rd[5]), "=&v"(f.rd[6]), "=&v"(f.rd[7]) : "v"(f.lr) : "memory");
  if constexpr (Q == 144) {
    asm volatile("s_waitcnt lgkmcnt(5)" : "+v"(f.rd[0]), "+v"(f.rd[1]), "+v"(f.rd[2]), "+v"(f.rd[3]), "+v"(f.rd[4]), "+v"(f.rd[5]), "+v"(f.rd[6]), "+v"(f.rd[7]));
    f.t[0] = rbf((__uint_as_float(f.rd[0]) + __uint_as_float(f.rd[1])) + (__uint_as_float(f.rd[2]) + __uint_as_float(f.rd[3])));
    f.t[1] = rbf((__uint_as_float(f.rd[4]) + __uint_as_float(f.rd[5])) + (__uint_as_float(f.rd[6]) + __uint_as_float(f.rd[7])));
    PINF(f.t[0]); PINF(f.t[1]);
  }
  if constexpr (Q == 145) { f.t[2] = f.t[0] * __builtin_amdgcn_rcpf(1.f + __expf(-f.t[0])); PINF(f.t[2]); }
  if constexpr (Q == 146) {
    f.hq = rbf(rbf(f.t[2]) * f.t[1]);
    asm volatile("ds_write_b32 %2, %3\n\tds_read_b128 %0, %4\n\tds_read_b128 %1, %4 offset:64" : "=&v"(f.h0), "=&v"(f.h1) : "v"(f.lh), "v"(f.hq), "v"(f.lg) : "memory");
  }
  if constexpr (Q == 156) asm volatile("s_waitcnt lgkmcnt(4)" : "+v"(f.h0), "+v"(f.h1));
  if constexpr (Q >= 156 && Q < 188) {
    constexpr int u = (Q - 156) >> 1, j = u >> 2, dw = u & 3;
    ff_unit<FA + 32 + 4 * j + dw, j == 0, (Q & 1)>(f, 2 * dw, f.h0[j], f.h1[j]);
  }
  if constexpr (Q >= 188 && Q < 191) {
#pragma unroll
    for (int i = 3 * (Q - 188); i < 3 * (Q - 188) + 3 && i < 8; ++i) { f.v[i] = sw_add<true>(f.v[i], f.v[8 + i]); PINF(f.v[i]); }
  }
  if constexpr (Q == 191) {
#pragma unroll
    for (int i = 0; i < 4; ++i) { f.v[i] = sw_add<false>(f.v[i], f.v[4 + i]); PINF(f.v[i]); }
  }
  if constexpr (Q == 192) {
    const f32x4 a = {f.v[0], f.v[1], f.v[2], f.v[3]};
    asm volatile("buffer_store_dwordx4 %0, %1, %2, %3 offen" WT "\n\ts_nop 1" :: "v"(a), "v"(f.vy), "s"(f.rA), "s"((u32)WS_YF) : "memory");
  }
}
template <int Q0, int... Qs> DI void ff_steps(FF& f, iseq<Qs...>) { (ff_step<Q0 + Qs>(f), ...); }

constexpr float AT_THR = 20.f;
constexpr int VS(int sl) { return (sl ^ 1) * 32768; }
DI float max2f(float a, float b) { float r; asm("v_max_f32_e32 %0, %1, %2" : "=v"(r) : "v"(a), "v"(b)); return r; }
DI float fadd_s(float a, float b) { float r; asm("v_add_f32_e32 %0, %1, %2" : "=v"(r) : "v"(a), "v"(b)); return r; }
DI float fsub_s(float a, float b) { float r; asm("v_sub_f32_e32 %0, %1, %2" : "=v"(r) : "v"(a), "v"(b)); return r; }

struct AT {
  f32x16 S[2][2];
  f32x16 negm;
  u32x4 P[4], Pz;
  u32x4 kf[32], vf[32];
  u32 kb_, vb_, kb0;
  u32 vL;
  u32 sW0;
  u32 m0K, m0V, m0X;
  srd_t rA;
  u32 sK, sV;
  u32 itn, v0, vtag;
  u32 sE0;
  int rem, mk;
  float l0; u32 pad0; float l1; u32 pad1; float mhat; u32 pad2; float mx[4], fct;
  u32 st;
  FF f; u32x4 ones; u32 sYf, sDf;
  u32x4 pb, pl; float mo, lo; u32 sM, v8;
  u32 sPB, vl16;
  int dq;
  bool pend; u32 nz;
  u32 tag, vFl, sFl;
};
template <int SL, int F> DI void at_kread(AT& s) {
  asm volatile("ds_read_b128 %0, %1 offset:%2" : "=v"(s.kf[F]) : "v"(SL == 2 ? s.kb0 : s.kb_), "i"((SL & 1) * 32768 + (F >> 1) * 2048 + (F & 1) * 512) : "memory");
}
template <int SL, int M, int F = M> DI void at_vread(AT& s) {
  asm volatile("ds_read_b128 %0, %1 offset:%2" : "=v"(s.vf[M]) : "v"(s.vb_), "i"(VS(SL) + (F >> 3) * 8192 + (F & 7) * 512) : "memory");
}
template <int X, int M, int QB, bool ZC> DI void at_qk(AT& s) {
  constexpr int kb = M & 1, ds = M >> 1, aq = QB + 4 * ds;
  if constexpr (ds == 0 && ZC) asm volatile("v_mfma_f32_32x32x16_bf16 %0, %1, a[%c2:%c3], 0" : "=&v"(s.S[X][kb]) : "v"(s.kf[M]), "i"(aq), "i"(aq + 3) : ACL);
  else if constexpr (ds == 0) asm volatile("v_mfma_f32_32x32x16_bf16 %0, %1, a[%c3:%c4], %2" : "=&v"(s.S[X][kb]) : "v"(s.kf[M]), "v"(s.negm), "i"(aq), "i"(aq + 3) : ACL);
  else                   asm volatile("v_mfma_f32_32x32x16_bf16 %0, %1, a[%c2:%c3], %0" : "+v"(s.S[X][kb]) : "v"(s.kf[M]), "i"(aq), "i"(aq + 3) : ACL);
}
template <int M, int F, bool ZC, bool PZ = false> DI void at_pv(AT& s) {
  constexpr int ks = F >> 3, ao = 16 * (F & 7);
  if constexpr (ZC && ks == 0) asm volatile("v_mfma_f32_32x32x16_bf16 a[%c2:%c3], %0, %1, 0" :: "v"(s.vf[M]), "v"(PZ ? s.Pz : s.P[ks]), "i"(ao), "i"(ao + 15) : ACL);
  else asm volatile("v_mfma_f32_32x32x16_bf16 a[%c2:%c3], %0, %1, a[%c2:%c3]" :: "v"(s.vf[M]), "v"(PZ && ks == 0 ? s.Pz : s.P[ks]), "i"(ao), "i"(ao + 15) : ACL);
}
constexpr int dbF(int M) { return 8 * (M & 3) + (M >> 2); }
template <int X, int KB, int E> DI void at_exp(AT& s) { float e = __builtin_amdgcn_exp2f(s.S[X][KB][E]); asm volatile("" : "+v"(e)); s.S[X][KB][E] = e; }
template <int X, int KB, int E> DI void at_sum(AT& s) {
  if constexpr (E & 1) asm volatile("v_add_f32_e32 %0, %0, %1" : "+v"(s.l1) : "v"(s.S[X][KB][E]));
  else                 asm volatile("v_add_f32_e32 %0, %0, %1" : "+v"(s.l0) : "v"(s.S[X][KB][E]));
}
template <int X, int KB, int C> DI void at_pack(AT& s) {
  u32 w = cvtpk(s.S[X][KB][2 * C], s.S[X][KB][2 * C + 1]); asm volatile("" : "+v"(w));
  s.P[2 * KB + (C >> 2)][C & 3] = w;
}
#define AT_PIN4(A, M) "+v"(s.A[M]), "+v"(s.A[M + 1]), "+v"(s.A[M + 2]), "+v"(s.A[M + 3])
#define AT_PIN8(A, M) AT_PIN4(A, M), AT_PIN4(A, M + 4)

constexpr int expB(int M) {
  if (M < 9 || M % 4 == 2 || (M >= 24 && M < 28)) return -1;
  int n = 0; for (int g = 9; g < M; ++g) if (!(g % 4 == 2 || (g >= 24 && g < 28))) ++n;
  return n;
}
constexpr int expA(int M) {
  if (M >= 24 || M % 4 == 2) return -1;
  int n = 14; for (int g = 0; g < M; ++g) if (g % 4 != 2) ++n;
  return n;
}
static_assert(expB(31) == 13 && expA(0) == 14 && expA(23) == 31, "exp schedule");
template <int X, int E> DI void at_e(AT& s) { if constexpr (E >= 0) at_exp<X, (E >> 4), (E & 15)>(s); }
template <int X, int E> DI void at_a(AT& s) { if constexpr (E >= 0) at_sum<X, (E >> 4), (E & 15)>(s); }
constexpr int dmaB(int M) { return M == 0 ? 0 : M == 6 ? 1 : (M >= 10 && M % 4 == 2) ? (M - 2) / 4 : -1; }

#define AT_NT0 18
constexpr int NT0 = AT_NT0;
template <int R> DI void acc_mov(void) { asm volatile("v_accvgpr_mov_b32 a[%c0], a[%c1]" :: "i"(192 + R), "i"(128 + R) : ACL); }
DI float at_lsum(float l0, float l1) {
  const float ls = fadd_s(l0, l1);
  const auto pr = __builtin_amdgcn_permlane32_swap(__float_as_uint(ls), __float_as_uint(ls), false, false);
  return fadd_s(__uint_as_float(pr[0]), __uint_as_float(pr[1]));
}
DI void at_mask16(f32x16& v, int dq, float ninf) {
  float e[16];
#pragma unroll
  for (int r = 0; r < 16; ++r) e[r] = v[r];
  asm volatile("v_cmpx_gt_i32_e32 vcc, 27, %16\n\tv_mov_b32_e32 %15, %17\n\t" "v_cmpx_gt_i32_e32 vcc, 26, %16\n\tv_mov_b32_e32 %14, %17\n\t"
               "v_cmpx_gt_i32_e32 vcc, 25, %16\n\tv_mov_b32_e32 %13, %17\n\t" "v_cmpx_gt_i32_e32 vcc, 24, %16\n\tv_mov_b32_e32 %12, %17\n\t"
               "v_cmpx_gt_i32_e32 vcc, 19, %16\n\tv_mov_b32_e32 %7, %17\n\t" "v_cmpx_gt_i32_e32 vcc, 18, %16\n\tv_mov_b32_e32 %6, %17\n\t"
               "v_cmpx_gt_i32_e32 vcc, 17, %16\n\tv_mov_b32_e32 %5, %17\n\t" "v_cmpx_gt_i32_e32 vcc, 16, %16\n\tv_mov_b32_e32 %4, %17\n\t"
               "v_cmpx_gt_i32_e32 vcc, 11, %16\n\tv_mov_b32_e32 %11, %17\n\t" "v_cmpx_gt_i32_e32 vcc, 10, %16\n\tv_mov_b32_e32 %10, %17\n\t"
               "v_cmpx_gt_i32_e32 vcc, 9, %16\n\tv_mov_b32_e32 %9, %17\n\t" "v_cmpx_gt_i32_e32 vcc, 8, %16\n\tv_mov_b32_e32 %8, %17\n\t"
               "v_cmpx_gt_i32_e32 vcc, 3, %16\n\tv_mov_b32_e32 %3, %17\n\t" "v_cmpx_gt_i32_e32 vcc, 2, %16\n\tv_mov_b32_e32 %2, %17\n\t"
               "v_cmpx_gt_i32_e32 vcc, 1, %16\n\tv_mov_b32_e32 %1, %17\n\t" "v_cmpx_gt_i32_e32 vcc, 0, %16\n\tv_mov_b32_e32 %0, %17\n\t"
               "s_mov_b64 exec, -1\n\ts_nop 2"
               : "+v"(e[0]), "+v"(e[1]), "+v"(e[2]), "+v"(e[3]), "+v"(e[4]), "+v"(e[5]), "+v"(e[6]), "+v"(e[7]), "+v"(e[8]), "+v"(e[9]), "+v"(e[10]), "+v"(e[11]), "+v"(e[12]), "+v"(e[13]), "+v"(e[14]), "+v"(e[15]) : "v"(dq), "v"(ninf) : "vcc");
#pragma unroll
  for (int r = 0; r < 16; ++r) v[r] = e[r];
}
template <int X, bool NEAR> DI void at_mask(AT& s, int rem) {
  if (__builtin_expect(rem <= s.mk, NEAR)) {
    const int dq = s.dq + 64 * rem - 128;
    at_mask16(s.S[X][1], dq - 32, -__builtin_inff()); at_mask16(s.S[X][0], dq, -__builtin_inff());
  }
}
template <int X, int M> DI void at_max(AT& s) {
#define SV(c, i) s.S[X][(c) >> 1][8 * ((c) & 1) + (i)]
  if constexpr (M == 2)
    asm volatile("v_max3_f32 %0, %4, %5, %6\n\tv_max3_f32 %1, %7, %8, %9\n\tv_max3_f32 %2, %10, %11, %12\n\tv_max3_f32 %3, %13, %14, %15"
                 : "=&v"(s.mx[0]), "=&v"(s.mx[1]), "=&v"(s.mx[2]), "=&v"(s.mx[3])
                 : "v"(SV(0, 0)), "v"(SV(0, 1)), "v"(SV(0, 2)), "v"(SV(1, 0)), "v"(SV(1, 1)), "v"(SV(1, 2)), "v"(SV(2, 0)), "v"(SV(2, 1)), "v"(SV(2, 2)), "v"(SV(3, 0)), "v"(SV(3, 1)), "v"(SV(3, 2)));
  if constexpr (M == 3 || M == 4)
    asm volatile("v_max3_f32 %0, %0, %4, %5\n\tv_max3_f32 %1, %1, %6, %7\n\tv_max3_f32 %2, %2, %8, %9\n\tv_max3_f32 %3, %3, %10, %11"
                 : "+v"(s.mx[0]), "+v"(s.mx[1]), "+v"(s.mx[2]), "+v"(s.mx[3])
                 : "v"(SV(0, 2 * M - 3)), "v"(SV(0, 2 * M - 2)), "v"(SV(1, 2 * M - 3)), "v"(SV(1, 2 * M - 2)), "v"(SV(2, 2 * M - 3)), "v"(SV(2, 2 * M - 2)), "v"(SV(3, 2 * M - 3)), "v"(SV(3, 2 * M - 2)));
  if constexpr (M == 5)
    asm volatile("v_max3_f32 %0, %0, %4, %5\n\tv_max3_f32 %2, %2, %6, %7\n\tv_max_f32_e32 %1, %1, %3"
                 : "+v"(s.mx[0]), "+v"(s.mx[1]), "+v"(s.mx[2]) : "v"(s.mx[3]), "v"(SV(0, 7)), "v"(SV(1, 7)), "v"(SV(2, 7)), "v"(SV(3, 7)));
  if constexpr (M == 6) asm volatile("v_max3_f32 %0, %0, %1, %2\n\tv_mov_b32_e32 %1, %0" : "+v"(s.mx[0]), "+v"(s.mx[1]) : "v"(s.mx[2]));
  if constexpr (M == 7) asm volatile("v_permlane32_swap_b32_e32 %0, %1\n\tv_max_f32_e32 %0, %0, %1" : "+v"(s.mx[0]), "+v"(s.mx[1]));
  if constexpr (M == 8) GAP();
#undef SV
}
DI void at_nextK(AT& s) {
  asm volatile("s_add_u32 %0, %0, 0x8000\n\ts_cmp_eq_u32 %1, 2\n\ts_cselect_b32 %0, %2, %0" : "+s"(s.sK) : "s"(s.rem), "s"(s.sE0) : "scc");
}
template <int X> DI void at_first(AT& s) {
  const float am = __builtin_fabsf(s.mx[0]);
  const bool out = am > AT_THR && am < 3.0e38f;
  if (__builtin_expect(__any(out || s.nz != 0u), 0)) {
    const float m = out ? s.mx[0] : 0.f;
    asm volatile("v_mov_b32_e32 %0, %1" : "+v"(s.mhat) : "v"(m));
#pragma unroll
    for (int kb = 0; kb < 2; ++kb)
#pragma unroll
      for (int r = 0; r < 16; ++r) { float e = s.S[X][kb][r]; asm volatile("v_sub_f32_e32 %0, %0, %1" : "+v"(e) : "v"(m)); s.S[X][kb][r] = e; }
#pragma unroll
    for (int r = 0; r < 16; ++r) { float e = s.negm[r]; asm volatile("v_sub_f32_e32 %0, 0, %1" : "+v"(e) : "v"(m)); s.negm[r] = e; }
    asm volatile("s_mov_b32 %0, 1" : "+s"(s.nz));
  }
}
constexpr int swN(int g) { const int n = g <= 5 ? 0 : ((g - 5) * 8 + 6) / 7; return n > 64 ? 64 : n; }
constexpr int swS(int g) { return g >= 1 && swN(g) / 4 > swN(g - 1) / 4 ? swN(g - 1) / 4 : -1; }
constexpr bool sw_ok() {
  for (int g = 0; g < 64; ++g) if (swN(g + 1) > swN(g) && (g < 4 * ((swN(g + 1) - 1) >> 3) + 5 || swN(g) / 4 != (swN(g + 1) - 1) / 4)) return false;
  return swS(61) == 15;
}
static_assert(sw_ok(), "the first job's O: behind the block's last product; a gap packs dwords of one fragment");
constexpr int swSt(int g0, int g1) { int n = 0; for (int g = g0; g < g1; ++g) n += swS(g) >= 0; return n; }
template <int G> DI void at_sw(AT& s) {
  if constexpr (swS(G) >= 0) {
    constexpr int F = swS(G);
    u32 t;
    asm volatile("s_add_u32 %0, %4, %5\n\tbuffer_store_dwordx4 %1, %2, %3, %0 offen offset:%6" WT
                 : "=&s"(t) : "v"(s.pb), "v"(s.vl16), "s"(s.rA), "s"(s.sPB), "i"((F >> 2) * 4096), "i"((F & 3) * 1024) : "memory", "scc");
    s.pl = s.pb;
  }
  if constexpr (swN(G + 1) > swN(G)) s.pb[swN(G) & 3] = acc_pk<2 * swN(G)>();
  if constexpr (swN(G + 1) > swN(G) + 1) s.pb[(swN(G) + 1) & 3] = acc_pk<2 * swN(G) + 2>();
  if constexpr (swS(G) >= 0) asm volatile("" :: "v"(s.pl));
}
template <int B, int F> DI void pb_ld(u32 vl16, srd_t r, u32 so) {
  u32 t;
  asm volatile("s_add_u32 %0, %3, %4\n\tbuffer_load_dwordx4 a[%c5:%c6], %1, %2, %0 offen offset:%7 sc0 sc1"
               : "=&s"(t) : "v"(vl16), "s"(r), "s"(so), "i"((F >> 2) * 4096), "i"(B + 4 * F), "i"(B + 3 + 4 * F), "i"((F & 3) * 1024) : "memory", "scc", ACL);
}
template <int OFF> DI void at_flag(AT& s, u32 so) { asm volatile("buffer_store_dword %0, %1, %2, %3 offen offset:%4" WT :: "v"(s.vtag), "v"(s.v0), "s"(s.rA), "s"(so), "i"(OFF) : "memory"); }
template <bool OWN, int M> DI void at_gap1(AT& s, PJ& j) {
  if constexpr (M % 4 == 0 && M < 24) asm volatile("s_waitcnt lgkmcnt(8)" : AT_PIN4(kf, M) :: "memory");
  if constexpr (M == 24) {
    asm volatile("s_waitcnt vmcnt(%8) lgkmcnt(0)\n\ts_barrier" : AT_PIN8(kf, 24) : "i"(OWN ? zWait(ZA) : 63) : "memory");
    if constexpr (OWN) z_flag<3>(j);
  }
  at_qk<0, M, 192, true>(s);
  if constexpr (M < 20) at_kread<2, M + 12>(s);
  if constexpr (M >= 24 && M < 28) { at_kread<1, 2 * (M - 24)>(s); at_kread<1, 2 * (M - 24) + 1>(s); }
  if constexpr (M >= 28) at_kread<1, M - 20>(s);
  if constexpr (OWN) {
    z_dma<ZA, M>(j);
    q_cvs<32 + M>(j);
    if constexpr (zQs(ZA, M) >= 0) q_store<zQs(ZA, M)>(s.vl16, s.rA, j.sQo);
  } else {
    if constexpr (M < 8) { ff_step<106 + 2 * M>(s.f); ff_step<107 + 2 * M>(s.f); }
    if constexpr (M >= 8 && M <= 20) ff_step<114 + M>(s.f);
    if constexpr (M == 24) ff_step<137>(s.f);
    if constexpr (M >= 29) ff_step<115 + M>(s.f);
  }
  if constexpr (M >= 28) {
#pragma unroll
    for (int r = 4 * (M - 28); r < 4 * (M - 27); ++r) { float e; asm volatile("v_mov_b32_e32 %0, 0" : "=v"(e)); s.negm[r] = e; }
  }
  if constexpr (M == 27) at_nextK(s);
  if constexpr (M == 29) asm volatile("s_sub_u32 %0, %0, 1" : "+s"(s.rem) :: "scc");
  if constexpr (M == 31) asm volatile("s_add_u32 %0, %0, 0x8000" : "+s"(s.sV) :: "scc");
  GAP();
}
template <bool OWN, int... Ms> DI void at_phase1(AT& s, PJ& j, iseq<Ms...>) { (at_gap1<OWN, Ms>(s, j), ...); }
template <int X, int MODE, int M> DI void at_gapA(AT& s) {
  if constexpr (M == 0) {
    if constexpr (MODE != 2 && MODE < 4) asm volatile("s_waitcnt lgkmcnt(8)\n\ts_nop 0" : AT_PIN4(kf, M), "+v"(s.negm) :: "memory");
    else asm volatile("s_waitcnt lgkmcnt(8)" : AT_PIN4(kf, M) :: "memory");
  }
  if constexpr (M % 4 == 0 && M > 0 && M < 24) asm volatile("s_waitcnt lgkmcnt(8)" : AT_PIN4(kf, M) :: "memory");
  if constexpr (M == 24) {
    constexpr int VM = MODE == 4 ? zWait(ZB) : MODE == 5 ? 63 : MODE == 3 ? 14 + swSt(0, 56) : 14;
    asm volatile("s_waitcnt vmcnt(%8) lgkmcnt(0)\n\ts_barrier" : AT_PIN8(kf, 24) : "i"(VM) : "memory");
  }
  at_qk<X, M, MODE == 2 ? 128 : 192, MODE == 2 || MODE >= 4>(s);
  if constexpr (M < 20) at_kread<X, M + 12>(s);
  if constexpr (MODE >= 4) {
    if constexpr ((M & 1) == 0 && M < 16) dma16q<VS(X), 0, M / 2>(s.m0V, s.sV, s.vL, s.rA, s.st);
    if constexpr ((M & 1) == 0 && M >= 16) dma16q<(X ^ 1) * 32768, 0, M / 2 - 8>(s.m0K, s.sK, s.vL, s.rA, s.st);
    if constexpr (MODE == 4 && zQs(ZB, M) >= 0) q_store<zQs(ZB, M)>(s.vl16, s.rA, s.sPB);
  } else if constexpr (M % 4 == 2) dma16q<VS(X), 0, M / 4>(s.m0V, s.sV, s.vL, s.rA, s.st);
  if constexpr (M == (MODE >= 4 ? 31 : 27)) at_nextK(s);
  if constexpr (M == 31) asm volatile("s_add_u32 %0, %0, 0x8000" : "+s"(s.sV) :: "scc");
  if constexpr (MODE == 3) at_sw<32 + M>(s);
  if constexpr (MODE == 5 && M >= 8 && M < 24) ff_step<148 + M>(s.f);
  if constexpr (MODE == 5 && M >= 24) { ff_step<124 + 2 * M>(s.f); ff_step<125 + 2 * M>(s.f); }
  if constexpr (MODE == 2) { acc_mov<2 * M>(); acc_mov<2 * M + 1>(); }
  constexpr bool DB = MODE == 2;
  if constexpr (M >= 24 && M < 28) { at_vread<X ^ 1, 2 * (M - 24), DB ? dbF(2 * (M - 24)) : 2 * (M - 24)>(s); at_vread<X ^ 1, 2 * (M - 24) + 1, DB ? dbF(2 * (M - 24) + 1) : 2 * (M - 24) + 1>(s); }
  if constexpr (M >= 28) at_vread<X ^ 1, M - 20, DB ? dbF(M - 20) : M - 20>(s);
  if constexpr (MODE >= 4) {
    if constexpr (M == 1 && MODE == 5) at_mask<X ^ 1, true>(s, s.rem + 1);
    at_max<X ^ 1, M>(s);
    if constexpr (M == 8) at_first<X ^ 1>(s);
    if constexpr (M >= 10) at_a<X ^ 1, M - 10>(s);
    if constexpr (M >= 9) at_e<X ^ 1, M - 9>(s);
    if constexpr (M >= 24) at_pack<X ^ 1, 0, M - 24>(s);
    if constexpr (M == 31) asm volatile("" : "+v"(s.P[0]), "+v"(s.P[1]), "+v"(s.l0), "+v"(s.l1));
  } else {
    if constexpr (M == 0) at_a<X ^ 1, 13>(s); else at_a<X ^ 1, expA(M - 1)>(s);
    at_e<X ^ 1, expA(M)>(s);
    if constexpr (M == 2 || M == 6) { at_pack<X ^ 1, 0, 4 + (M - 2) / 2>(s); at_pack<X ^ 1, 0, 5 + (M - 2) / 2>(s); }
    if constexpr (M == 14 || M == 18) { at_pack<X ^ 1, 1, (M - 14) / 2>(s); at_pack<X ^ 1, 1, 1 + (M - 14) / 2>(s); }
    if constexpr (M >= 28) at_pack<X ^ 1, 1, M - 24>(s);
    if constexpr (M == 31) asm volatile("" : "+v"(s.P[1]), "+v"(s.P[2]), "+v"(s.P[3]), "+v"(s.l0), "+v"(s.l1));
  }
  GAP();
}
template <int X, int MODE, int... Ms> DI void at_phaseA(AT& s, iseq<Ms...>) { (at_gapA<X, MODE, Ms>(s), ...); }

#define PPL(i, o) "buffer_load_dwordx4 a[" #i ":" #i "+3], %[vl], %[ra], %0 offen offset:" #o " sc0 sc1\n\t"
#define PPL16(B) "s_mov_b32 %0, " B "\n\t" PPL(128, 0) PPL(132, 1024) PPL(136, 2048) PPL(140, 3072) "s_add_u32 %0, " B ", 4096\n\t" PPL(144, 0) PPL(148, 1024) PPL(152, 2048) PPL(156, 3072) \
                 "s_add_u32 %0, " B ", 8192\n\t" PPL(160, 0) PPL(164, 1024) PPL(168, 2048) PPL(172, 3072) "s_add_u32 %0, " B ", 12288\n\t" PPL(176, 0) PPL(180, 1024) PPL(184, 2048) PPL(188, 3072)
template <int X, int MODE, int M> DI void at_gapB(AT& s) {
  constexpr bool DB = MODE == 2;
  if constexpr (DB && M == 0) s.Pz = s.P[0];
  if constexpr (M % 4 == 0 && M < 24) asm volatile("s_waitcnt lgkmcnt(8)" : AT_PIN4(vf, M) :: "memory");
  if constexpr (M == 24) {
    constexpr int VM = MODE == 4 ? zWait(ZE) : MODE == 5 ? 7 : MODE == 2 ? 14 + swSt(0, 24) : MODE == 3 ? 7 : 14;
    asm volatile("s_waitcnt vmcnt(%8) lgkmcnt(0)\n\ts_barrier" : AT_PIN8(vf, 24) : "i"(VM) : "memory");
    if constexpr (MODE == 3) { const f32x2 ml = {s.mo, s.lo}; asm volatile("buffer_store_dwordx2 %0, %1, %2, %3 offen" WT :: "v"(ml), "v"(s.v8), "s"(s.rA), "s"(s.sM) : "memory"); }
  }
  at_pv<M, DB ? dbF(M) : M, MODE >= 3, DB>(s);
  if constexpr (M < 20) at_vread<X ^ 1, M + 12, DB ? dbF(M + 12) : M + 12>(s);
  if constexpr (MODE >= 4) { if constexpr (M % 4 == 2) dma16q<X * 32768, 0, M / 4>(s.m0K, s.sK, s.vL, s.rA, s.st); }
  else if constexpr (dmaB(M) >= 0) dma16q<X * 32768, 0, dmaB(M)>(s.m0K, s.sK, s.vL, s.rA, s.st);
  if constexpr (MODE == 4 && M == 15) asm volatile("buffer_load_dwordx2 a[190:191], %0, %1, %2 offen sc0 sc1" :: "v"(s.v8), "s"(s.rA), "s"(s.sM) : "memory", ACL);
  if constexpr (MODE == 4 && M == 17) asm volatile("buffer_load_dword a189, %0, %1, %2 offen sc0 sc1" :: "v"(s.vFl), "s"(s.rA), "s"(s.sFl) : "memory", ACL);
  if constexpr (M >= 24 && M < 28) { at_kread<X ^ 1, 2 * (M - 24)>(s); at_kread<X ^ 1, 2 * (M - 24) + 1>(s); }
  if constexpr (M >= 28) at_kread<X ^ 1, M - 20>(s);
  if constexpr (MODE == 2 && M == 0) {
    s.lo = at_lsum(s.l0, s.l1); s.mo = s.mhat; s.l0 = 0.f; s.l1 = 0.f;
    asm volatile("" : "+v"(s.lo), "+v"(s.mo), "+v"(s.l0), "+v"(s.l1));
  }
  if constexpr (MODE == 2) at_sw<M>(s);
  if constexpr (MODE == 5 && M >= 16 && M <= 20) ff_step<172 + M>(s.f);
  if constexpr (MODE >= 4) {
    if constexpr (M == 0) { at_a<X ^ 1, 22>(s); at_e<X ^ 1, 23>(s); at_e<X ^ 1, 24>(s); }
    if constexpr (M == 1 || M == 2) { at_a<X ^ 1, 21 + 2 * M>(s); at_a<X ^ 1, 22 + 2 * M>(s); at_e<X ^ 1, 23 + 2 * M>(s); at_e<X ^ 1, 24 + 2 * M>(s); }
    if constexpr (M == 3) at_a<X ^ 1, 27>(s);
    if constexpr (M >= 3 && M <= 6) at_a<X ^ 1, 25 + M>(s);
    if constexpr (M >= 3 && M <= 5) at_e<X ^ 1, 26 + M>(s);
    if constexpr (M >= 7 && M < 15) at_pack<X ^ 1, 1, M - 7>(s);
    if constexpr (M == 14) asm volatile("" : "+v"(s.P[2]), "+v"(s.P[3]));
    if constexpr (M == 1) {
      if (__builtin_expect(s.nz != 0u, 0)) {
#pragma unroll
        for (int kb = 0; kb < 2; ++kb)
#pragma unroll
          for (int r = 0; r < 16; ++r) { float e = s.S[X][kb][r]; asm volatile("v_sub_f32_e32 %0, %0, %1" : "+v"(e) : "v"(s.mhat)); s.S[X][kb][r] = e; }
      }
    }
  }
  if constexpr (M == 1 && MODE != 4) at_mask<X, MODE != 0>(s, s.rem);
  at_max<X, M>(s);
  if constexpr (M == 8) {
    if constexpr (MODE == 2) at_first<X>(s);
    else if (__builtin_expect(__any(s.mx[0] > AT_THR), 0)) {
      const float dl = max2f(s.mx[0], 0.f);
      s.fct = __builtin_amdgcn_exp2f(-dl);
      asm volatile("v_add_f32_e32 %0, %0, %1" : "+v"(s.mhat) : "v"(dl));
#pragma unroll
      for (int kb = 0; kb < 2; ++kb)
#pragma unroll
        for (int r = 0; r < 16; ++r) { float e = s.S[X][kb][r]; asm volatile("v_sub_f32_e32 %0, %0, %1" : "+v"(e) : "v"(dl)); s.S[X][kb][r] = e; }
#pragma unroll
      for (int r = 0; r < 16; ++r) { float e = s.negm[r]; asm volatile("v_sub_f32_e32 %0, 0, %1" : "+v"(e) : "v"(s.mhat)); s.negm[r] = e; }
      asm volatile("s_nop 0\n\tv_mul_f32_e32 %0, %0, %2\n\tv_mul_f32_e32 %1, %1, %2" : "+v"(s.l0), "+v"(s.l1) : "v"(s.fct));
      s.pend = true; asm volatile("s_mov_b32 %0, 1" : "+s"(s.nz));
    }
  }
  if constexpr (M >= 1) at_a<X, expB(M - 1)>(s);
  at_e<X, expB(M)>(s);
  if constexpr (M == 22 || M == 26) { at_pack<X, 0, (M - 22) / 2>(s); at_pack<X, 0, 1 + (M - 22) / 2>(s); }
  if constexpr (M == 25) asm volatile("s_sub_u32 %0, %0, 1" : "+s"(s.rem) :: "scc");
  if constexpr (M == 31) asm volatile("" : "+v"(s.P[0]), "+v"(s.S[X][0]), "+v"(s.l0), "+v"(s.l1), "+v"(s.negm));
  GAP();
}
template <int X, int MODE, int... Ms> DI void at_phaseB(AT& s, iseq<Ms...>) {
  (at_gapB<X, MODE, Ms>(s), ...);
  if constexpr (MODE != 2) {
    if (__builtin_expect(s.pend, 0)) {
      asm volatile("s_nop 7\n\ts_nop 7" ::: "memory");
      acc_mul_all(s.fct, make_iseq<128>{});
      asm volatile("s_nop 1" ::: "memory");
      s.pend = false;
    }
    GAP();
  }
}
template <int X, int MODE> DI void at_period(AT& s) { at_phaseA<X, MODE>(s, make_iseq<32>{}); at_phaseB<X, MODE>(s, make_iseq<32>{}); }
template <bool BLOCK> DI void at_check(AT& s, u32& want) {
  u32 st, t, n = SPIN_MAX;
  asm volatile("" : "=v"(t));
  asm volatile("s_cmp_eq_u32 %1, 0\n\ts_cbranch_scc1 .Lce%=\n\ts_bitcmp0_b32 %1, 1\n\ts_cbranch_scc1 .Lcl%=\n"
               ".Lca%=:\n\tv_accvgpr_read_b32 %3, a189\n\t"
               "v_cmp_eq_u32_e32 vcc, %9, %3\n\ts_nop 3\n\ts_cmp_eq_u64 vcc, -1\n\ts_cbranch_scc1 .Lcb%=\n\ts_sub_u32 %2, %2, 1\n\ts_cbranch_scc1 .Lcb%=\n\t"
               "buffer_load_dword a189, %4, %6, %11 offen sc0 sc1\n\ts_waitcnt vmcnt(0)\n\ts_branch .Lca%=\n"
               ".Lcb%=:\n\tv_accvgpr_write_b32 a189, 0\n\ts_mov_b32 %2, %12\n\ts_and_b32 %1, %1, 1\n\ts_cbranch_scc0 .Lce%=\n"
               ".Lcl%=:\n\tv_accvgpr_read_b32 %3, a191\n\t"
               "v_cmp_ne_u32_e32 vcc, 0xbf800000, %3\n\ts_nop 3\n\ts_cmp_eq_u64 vcc, -1\n\ts_cbranch_scc1 .Lcg%=\n\t"
               ".if %10\n\ts_sub_u32 %2, %2, 1\n\ts_cbranch_scc1 .Lcg%=\n\t.endif\n\t"
               "buffer_load_dwordx2 a[190:191], %13, %6, %8 offen sc0 sc1\n\t"
               ".if %10\n\ts_waitcnt vmcnt(0)\n\ts_branch .Lcl%=\n\t.else\n\ts_branch .Lce%=\n\t.endif\n"
               ".Lcg%=:\n\ts_mov_b32 %1, 0\n\t" PPL16("%7")
               ".Lce%=:"
               : "=&s"(st), "+s"(want), "+s"(n) : "v"(t), "v"(s.vFl), [vl] "v"(s.vl16), [ra] "s"(s.rA), "s"(s.sPB), "s"(s.sM), "s"(s.tag), "i"(BLOCK ? 1 : 0), "s"(s.sFl), "i"(SPIN_MAX), "v"(s.v8)
               : "memory", "scc", "vcc", ACL);
}
#define QPL(i, o) "buffer_load_dwordx4 a[" #i ":" #i "+3], %5, %6, %1 offen offset:" #o " sc0 sc1\n\t"
#define QPM(i, k) "v_accvgpr_read_b32 %2, a[" #i "]\n\tv_accvgpr_read_b32 %3, a[" #k "]\n\tv_max3_u32 %4, %4, %2, %3\n\t"
DI void at_getq(AT& s) {
  u32 st, n = SPIN_MAX, t, u, m;
  asm volatile(".Lqg%=:\n\tv_mov_b32 %4, 0\n\t"
               QPM(195, 199) QPM(203, 207) QPM(211, 215) QPM(219, 223) QPM(227, 231) QPM(235, 239) QPM(243, 247) QPM(251, 255)
               "v_cmp_eq_u32_e32 vcc, -1, %4\n\ts_nop 3\n\ts_cmp_eq_u64 vcc, 0\n\ts_cbranch_scc1 .Lqe%=\n\t"
               "s_sub_u32 %0, %0, 1\n\ts_cbranch_scc1 .Lqe%=\n\ts_mov_b32 %1, %7\n\t"
               QPL(192, 0) QPL(196, 1024) QPL(200, 2048) QPL(204, 3072) "s_add_u32 %1, %7, 4096\n\t"
               QPL(208, 0) QPL(212, 1024) QPL(216, 2048) QPL(220, 3072) "s_add_u32 %1, %7, 8192\n\t"
               QPL(224, 0) QPL(228, 1024) QPL(232, 2048) QPL(236, 3072) "s_add_u32 %1, %7, 12288\n\t"
               QPL(240, 0) QPL(244, 1024) QPL(248, 2048) QPL(252, 3072)
               "s_waitcnt vmcnt(0)\n\ts_branch .Lqg%=\n"
               ".Lqe%=:"
               : "+s"(n), "=&s"(st), "=&v"(t), "=&v"(u), "=&v"(m) : "v"(s.vl16), "s"(s.rA), "s"(s.sPB) : "memory", "scc", "vcc", ACL);
}

struct RD { u32x2 xv[4], bv[4], fl; float gt[2]; u32 row, vx, vc, vf; srd_t rX; };
DI void rd_look(RD& r, srd_t rA) {
  asm volatile("buffer_load_dwordx2 %0, %1, %2, %3 offen sc0 sc1" : "=v"(r.fl) : "v"(r.vf), "s"(rA), "s"((u32)WS_IDX) : "memory");
}
DI void rd_check(RD& r, u32 tag, srd_t rA) {
  u32 n = SPIN_MAX;
  while (!__all(r.fl[1] == tag) && --n)
    asm volatile("s_sleep 1\n\tbuffer_load_dwordx2 %0, %1, %2, %3 offen sc0 sc1\n\ts_waitcnt vmcnt(0)" : "+v"(r.fl) : "v"(r.vf), "s"(rA), "s"((u32)WS_IDX) : "memory");
}
template <int U> DI void rd_x(RD& r) {
  asm volatile("buffer_load_dwordx2 %0, %2, %3, %4 offen\n\tbuffer_load_dwordx2 %1, %2, %3, %4 offen offset:32" : "=&v"(r.xv[2 * U]), "=&v"(r.xv[2 * U + 1]) : "v"(r.vx), "s"(r.rX), "s"(U * 16 * M_ * 2) : "memory");
}
template <int I> DI void rd_g(RD& r, srd_t rA) {
  if constexpr (I == 0)
    asm volatile("buffer_load_dword %0, %2, %3, %4 offen sc0 sc1\n\tbuffer_load_dword %1, %2, %3, %4 offen offset:64 sc0 sc1" : "=&v"(r.gt[0]), "=&v"(r.gt[1]) : "v"(r.row * 4), "s"(rA), "s"((u32)WS_GATE) : "memory");
  else {
    const u32 idx = __builtin_amdgcn_readlane(r.fl[0], 63), rw = r.row + 16 * (I - 1), c = r.vc;
    r.vf = rw == (idx & 0xffffu) ? c : rw == idx >> 16 ? c + M_ * 2 : OOB;
    asm volatile("buffer_load_dwordx2 %0, %2, %3, %4 offen sc0 sc1\n\tbuffer_load_dwordx2 %1, %2, %3, %4 offen offset:32 sc0 sc1" : "=&v"(r.bv[2 * I - 2]), "=&v"(r.bv[2 * I - 1]) : "v"(r.vf), "s"(rA), "s"((u32)WS_BB) : "memory");
  }
}

struct OP {
  u32x4 cf[16];
  u32x4 wf[28], st, id[2];
  f32x16 y[3];
  float inv;
  u32 sb[5];
  u32 vL, vl16, wofs, sW1, sq, sY, sX, sXp, tag, xp, sWf, sWfp;
  srd_t rA;
  RD r;
};
template <int W, int F> DI void op_read(OP& o, u32 rb) {
  asm volatile("ds_read_b128 %0, %1 offset:%2" : "=v"(o.wf[W]) : "v"(rb), "i"((F / 7) * 8192 + (F % 7) * 1024) : "memory");
}
template <int F> DI void op_xst(OP& o) {
  u32 t;
  asm volatile("s_add_u32 %0, %4, %5\n\tbuffer_store_dwordx4 %1, %2, %3, %0 offen offset:%6" WT
               : "=&s"(t) : "v"(o.cf[F]), "v"(o.vl16), "s"(o.rA), "s"(o.sX), "i"((F >> 2) * 4096), "i"((F & 3) * 1024) : "memory", "scc");
}
template <int D> DI void op_cv(OP& o) {
  u32 w; float t0;
  asm volatile("v_accvgpr_read_b32 %0, a[%c3]\n\tv_accvgpr_read_b32 %1, a[%c4]\n\tv_mul_f32_e32 %0, %0, %2\n\tv_mul_f32_e32 %1, %1, %2\n\tv_cvt_pk_bf16_f32 %0, %0, %1"
               : "=&v"(w), "=&v"(t0) : "v"(o.inv), "i"(2 * D), "i"(2 * D + 1));
  o.cf[D >> 2][D & 3] = w;
}
template <int D> DI void op_st(OP& o) {
  if constexpr (D >= 0 && D < 56) {
    constexpr int I = D >> 2, W = D & 3, nt = I >> 1, e = 8 * (I & 1) + 2 * W;
    u32 w;
    if constexpr (nt < 4) w = acc_pk<192 + 16 * nt + e>();
    else asm volatile("v_cvt_pk_bf16_f32 %0, %1, %2" : "=v"(w) : "v"(o.y[nt < 4 ? 0 : nt - 4][e]), "v"(o.y[nt < 4 ? 0 : nt - 4][e + 1]));
    o.st[W] = w;
    if constexpr (W == 3) asm volatile("buffer_store_dwordx4 %0, %1, %2, %3 offen offset:%4" WT "\n\ts_nop 1" :: "v"(o.st), "v"(o.vl16), "s"(o.rA), "s"(o.sY + (I >> 2) * 4096), "i"((I & 3) * 1024) : "memory");
  }
}
template <int PASS, int NT, int KS, int W> DI void op_mfma(OP& o) {
  constexpr int ay = 192 + 16 * NT;
  if constexpr (PASS == 0) {
    if constexpr (NT < 4) {
      if constexpr (KS == 0) asm volatile("v_mfma_f32_32x32x16_bf16 a[%c2:%c3], %0, %1, 0" :: "v"(o.wf[W]), "v"(o.cf[KS]), "i"(ay), "i"(ay + 15) : ACL);
      else                   asm volatile("v_mfma_f32_32x32x16_bf16 a[%c2:%c3], %0, %1, a[%c2:%c3]" :: "v"(o.wf[W]), "v"(o.cf[KS]), "i"(ay), "i"(ay + 15) : ACL);
    } else {
      if constexpr (KS == 0) asm volatile("v_mfma_f32_32x32x16_bf16 %0, %1, %2, 0" : "=&v"(o.y[NT - 4]) : "v"(o.wf[W]), "v"(o.cf[KS]));
      else                   asm volatile("v_mfma_f32_32x32x16_bf16 %0, %1, %2, %0" : "+v"(o.y[NT - 4]) : "v"(o.wf[W]), "v"(o.cf[KS]));
    }
  } else {
    if constexpr (NT < 4) asm volatile("v_mfma_f32_32x32x16_bf16 a[%c1:%c2], %0, a[%c3:%c4], a[%c1:%c2]" :: "v"(o.wf[W]), "i"(ay), "i"(ay + 15), "i"(4 * KS), "i"(4 * KS + 3) : ACL);
    else                  asm volatile("v_mfma_f32_32x32x16_bf16 %0, %1, a[%c2:%c3], %0" : "+v"(o.y[NT - 4]) : "v"(o.wf[W]), "i"(4 * KS), "i"(4 * KS + 3));
  }
}
constexpr int DR(bool mrg) { return mrg ? 48 : 32; }
constexpr int cvD(bool mrg, int q) { const int n = q + (mrg ? 27 : 13); return n < 0 ? 0 : n > 64 ? 64 : n; }
constexpr int stepI(int q, int q0, int d, int n) { return q >= q0 && (q - q0) % d == 0 && (q - q0) / d < n ? (q - q0) / d : -1; }
#define XS0 -9
#define XSD 4
#define XFA 40
#define XFB 72
#define XPA 60
#define XCA 76
#define XPB 92
constexpr int opXs(int q) { return stepI(q, XS0, XSD, 16); }
constexpr int opX(int q) { return stepI(q, XCA + 1, 4, 8) >= 0 ? stepI(q, XCA + 1, 4, 8) : stepI(q, 109, 4, 8) >= 0 ? 8 + stepI(q, 109, 4, 8) : -1; }
constexpr int opD(int q) { const int i = stepI(q, 1, 4, 14) >= 0 ? stepI(q, 1, 4, 14) : stepI(q, 55, 4, 14) >= 0 ? 14 + stepI(q, 55, 4, 14) : -1; return i >= 0 ? 32 + 8 * (i / 7) + i % 7 : -1; }
constexpr int opDq(int piece) { for (int q = 0; q < 168; ++q) if (opD(q) == piece) return q; return -1; }
constexpr int opN(bool mrg, int q) {
  return (q < 0 && (q + DR(mrg)) % 4 == 2 && q + DR(mrg) < 28) + (q == XFA) + (q == XFB) + (q == XPA) + (q == XPB) + (q == 141) + 2 * (q == 143 || q == 147) + (opD(q) >= 0) + (opX(q) >= 0) + (opXs(q) >= 0);
}
constexpr int opYoung(bool mrg, int q0, int q1) { int n = 0; for (int q = q0 + 1; q < q1; ++q) n += opN(mrg, q); return n > 63 ? 63 : n; }
constexpr bool op_ok() {
  for (int f = 0; f < 16; ++f) if (cvD(false, 7 * f - 1) < 4 * f + 4 || cvD(false, XS0 + XSD * f) < 4 * f + 4) return false;
  return opDq(40) > 24 && opDq(48) > 52 && opDq(56) > 80 && XS0 + 7 * XSD < XFA && XS0 + 15 * XSD < XFB && XPA < XCA && XCA + 13 < 108 && XPB < 108 && XPB > XCA && cvD(false, XCA) >= 32;
}
static_assert(op_ok(), "schedule of the out projection");
#define OP_PIN4(W) "+v"(o.wf[(W) % 28]), "+v"(o.wf[((W) + 1) % 28]), "+v"(o.wf[((W) + 2) % 28]), "+v"(o.wf[((W) + 3) % 28])
template <bool MRG, int Q> DI void op_ctx(OP& o) {
  if constexpr (opXs(Q) >= 0) op_xst<opXs(Q)>(o);
  if constexpr (cvD(MRG, Q + 1) > cvD(MRG, Q)) op_cv<cvD(MRG, Q)>(o);
  if constexpr (opXs(Q - 1) >= 0) asm volatile("" :: "v"(o.cf[opXs(Q - 1)]));
}
template <bool MRG, int C, int M> DI void op_gap(AT& s, OP& o, u32 rb, u32 rbn, u32 rbn2) {
  constexpr int ks = M / 7, nt = M % 7, q = 28 * C + M;
  if constexpr (M % 4 == 0 && M < 24) asm volatile("s_waitcnt lgkmcnt(4)" : OP_PIN4(M) :: "memory");
  if constexpr (q == XFA || q == XFB) { asm volatile("s_waitcnt vmcnt(%0)" :: "i"(opYoung(MRG, XS0 + XSD * (q == XFA ? 7 : 15), q)) : "memory"); at_flag<(q == XFA ? 8 : 12)>(s, o.sWf); }
  if constexpr (q == XCA) {
    asm volatile("s_waitcnt vmcnt(%1)" : "+v"(o.xp) : "i"(opYoung(MRG, XPA, q)) : "memory");
    u32 z = 0u; kv_check_all(o.xp, o.tag, z, o.rA, o.sWfp + 8u);
  }
  if constexpr (M == 24) {
    constexpr int q1 = C < 2 ? -99 : C == 2 ? 26 - DR(MRG) : C == 3 ? imax(imax(XCA + 13, opDq(38)), XPB) : C == 4 ? imax(XCA + 29, opDq(46)) : imax(141, opDq(62));
    constexpr int VM = q1 == -99 ? 63 : opYoung(MRG, q1, q);
    if constexpr (C == 3) {
      asm volatile("s_waitcnt vmcnt(%5) lgkmcnt(0)\n\ts_barrier" : OP_PIN4(24), "+v"(o.xp) : "i"(VM) : "memory");
      u32 z = 0u; kv_check_all(o.xp, o.tag, z, o.rA, o.sWfp + 12u);
    } else if constexpr (C == 5) {
      asm volatile("s_waitcnt vmcnt(%5) lgkmcnt(0)\n\ts_barrier" : OP_PIN4(24), "+v"(o.r.fl) : "i"(VM) : "memory");
      rd_check(o.r, o.tag, o.rA);
    } else asm volatile("s_waitcnt vmcnt(%4) lgkmcnt(0)\n\ts_barrier" : OP_PIN4(24) : "i"(VM) : "memory");
  }
  op_mfma<C / 4, nt, 4 * (C % 4) + ks, M>(o);
  if constexpr (M < 20) op_read<M + 8, M + 8>(o, rb);
  if constexpr (M >= 24) {
    constexpr int i = 2 * (M - 24);
    if constexpr (C < 5) { op_read<i, i>(o, rbn); op_read<i + 1, i + 1>(o, rbn); }
    else { op_read<i, 7 * (i & 3)>(o, i < 4 ? rbn : rbn2); op_read<i + 1, 7 * ((i + 1) & 3)>(o, i < 4 ? rbn : rbn2); }
  }
  if constexpr (opD(q) >= 0) dma16q<0, (opD(q) / 8 - 4) * 32768, opD(q) % 8>(o.sb[(opD(q) / 8) % 5] + o.wofs, o.sW1, o.vL, o.rA, o.sq);
  if constexpr (opX(q) >= 0) pb_ld<0, opX(q)>(o.vl16, o.rA, o.sXp);
  if constexpr (q == XPA || q == XPB) { u32 z = 0u; kv_poll(o.xp, z, o.rA, o.sWfp + (q == XPA ? 8u : 12u)); }
  if constexpr (q == 141) rd_look(o.r, o.rA);
  if constexpr (q >= 143 && q <= 147 && q % 4 == 3) rd_x<(q - 143) / 4>(o.r);
  if constexpr (q >= 165) rd_g<q - 165>(o.r, o.rA);
  op_ctx<MRG, q>(o);
  GAP();
}
template <bool MRG, int C, int... Ms> DI void op_chunk(AT& s, OP& o, iseq<Ms...>) {
  const u32 rb = o.vl16 + o.sb[C % 5], rbn = o.vl16 + o.sb[(C + 1) % 5], rbn2 = o.vl16 + o.sb[(C + 2) % 5];
  (op_gap<MRG, C, Ms>(s, o, rb, rbn, rbn2), ...);
}
template <int I> DI void op_gapL(OP& o, u32 rb6, u32 rb7) {
  constexpr int nt = I / 8, ks = I % 8, J = I + 8;
  if constexpr (I % 4 == 0) asm volatile("s_waitcnt lgkmcnt(%4)" : OP_PIN4(I) : "i"(I < 52 ? 4 : 0) : "memory");
  op_mfma<1, nt, 8 + ks, I % 28>(o);
  if constexpr (J < 56) op_read<J % 28, 7 * (J & 3) + J / 8>(o, (J & 4) ? rb7 : rb6);
  op_st<I - 10>(o);
  GAP();
}
template <int... Is> DI void op_last(OP& o, iseq<Is...>) { const u32 rb6 = o.vl16 + o.sb[1], rb7 = o.vl16 + o.sb[2]; (op_gapL<Is>(o, rb6, rb7), ...); }
template <int... Ds> DI void op_tail(OP& o, iseq<Ds...>) { (op_st<46 + Ds>(o), ...); }

DI u32 bf_scale(u32 w, float f) { return cvtpk(BF_LO(w) * f, BF_HI(w) * f); }
template <int... Rs> DI void pb_scale(float f, iseq<Rs...>) { (acc_wr<128 + Rs>(bf_scale(__float_as_uint(acc_rd<128 + Rs>()), f)), ...); }
constexpr int XL = 1;
constexpr int drF(int i) { return i < 16 ? i : 8 * (2 + (i & 1)) + ((i - 16) >> 1); }
constexpr int drI(bool mrg, int M) { return M < 16 || !mrg ? M : (M & 3) < 2 ? 16 + ((M - 16) >> 2) * 2 + (M & 1) : -1; }
template <bool MRG, int M> DI void at_gapD(AT& s, OP& o, u32 rb0) {
  constexpr int i = drI(MRG, M), q = M - DR(MRG), MB = DR(MRG) - 8;
  if constexpr (M % 4 == 0 && M < (MRG ? 16 : 24)) asm volatile("s_waitcnt lgkmcnt(8)" : AT_PIN4(vf, M) :: "memory");
  if constexpr (MRG && M % 4 == 0 && M >= 16 && M < MB) asm volatile("s_waitcnt lgkmcnt(%2)" : "+v"(s.vf[i]), "+v"(s.vf[i + 1]) : "i"(imin(i + 11, 31) - i - 1) : "memory");
  if constexpr (M == 16) {
    if constexpr (MRG) asm volatile("s_waitcnt vmcnt(4)" : "+v"(s.mo), "+v"(s.lo) :: "memory"); else asm volatile("s_waitcnt vmcnt(4)" ::: "memory");
  }
  if constexpr (M == MB) {
    if constexpr (MRG) asm volatile("s_waitcnt lgkmcnt(0)\n\ts_barrier" : AT_PIN4(vf, 28) :: "memory"); else asm volatile("s_waitcnt lgkmcnt(0)\n\ts_barrier" : AT_PIN8(vf, 24) :: "memory");
  }
  if constexpr (i >= 0) at_pv<i, drF(i), false>(s);
  else {
    constexpr int db = (M - 16) >> 2, F = 2 * db + (M & 1);
    asm volatile("v_mfma_f32_32x32x16_bf16 a[%c1:%c2], %0, a[%c3:%c4], a[%c1:%c2]" :: "v"(o.id[M & 1]), "i"(16 * db), "i"(16 * db + 15), "i"(128 + 4 * F), "i"(131 + 4 * F) : ACL);
  }
  if constexpr (i >= 0 && i < 20) at_vread<XL, i + 12, drF(i + 12)>(s);
  if constexpr (M % 4 == 2 && M < 28) dma16q<VS(XL ^ 1), 3 * 32768, M / 4>(s.m0V, s.sW0, s.vL, s.rA, s.st);
  if constexpr (M >= MB && M < MB + 4) { op_read<2 * (M - MB), 2 * (M - MB)>(o, rb0); op_read<2 * (M - MB) + 1, 2 * (M - MB) + 1>(o, rb0); }
  if constexpr (M <= 14) at_a<XL, 17 + M>(s);
  if constexpr (M <= 13) at_e<XL, 18 + M>(s);
  if constexpr (M >= 6 && M <= 9) at_pack<XL, 1, M - 6>(s);
  if constexpr (M == 8 || M == 10 || M == 12 || M == 14) at_pack<XL, 1, 4 + (M - 8) / 2>(s);
  if constexpr (M == 15) { s.l0 = at_lsum(s.l0, s.l1); asm volatile("" : "+v"(s.l0), "+v"(s.P[2]), "+v"(s.P[3])); }
  if constexpr (MRG && M == 17) {
    if (__builtin_expect(__any(s.lo > 0.f && s.mo != s.mhat), 0)) {
      const float r = max2f(s.lo > 0.f ? s.mo : s.mhat, s.mhat), f0 = __builtin_amdgcn_exp2f(s.mhat - r), f1 = __builtin_amdgcn_exp2f(s.lo > 0.f ? s.mo - r : 0.f);
      asm volatile("s_nop 7\n\ts_nop 7" ::: "memory");
      acc_mul_all(f0, make_iseq<128>{});
      pb_scale(f1, make_iseq<64>{});
      asm volatile("v_mul_f32_e32 %0, %0, %2\n\tv_mul_f32_e32 %1, %1, %3" : "+v"(s.l0), "+v"(s.lo) : "v"(f0), "v"(f1));
#pragma unroll
      for (int c = 0; c < 8; ++c) { u32 w = s.P[2 + (c >> 2)][c & 3]; const u32 v = bf_scale(w, f0); asm volatile("v_mov_b32_e32 %0, %1" : "+v"(w) : "v"(v)); s.P[2 + (c >> 2)][c & 3] = w; }
      asm volatile("s_nop 1" ::: "memory");
    }
  }
  if constexpr (MRG && M == 18) { s.l0 = fadd_s(s.l0, s.lo); asm volatile("" : "+v"(s.l0)); }
  if constexpr (M == (MRG ? 19 : 17)) { o.inv = __builtin_amdgcn_rcpf(s.l0); asm volatile("" : "+v"(o.inv)); }
  op_ctx<MRG, q>(o);
  GAP();
}
template <bool MRG, int... Ms> DI void at_drain(AT& s, OP& o, u32 rb0, iseq<Ms...>) { (at_gapD<MRG, Ms>(s, o, rb0), ...); }
template <int... Fs> DI void at_vread_first(AT& s, iseq<Fs...>) { (at_vread<XL, Fs>(s), ...); }
template <bool MRG> DI void at_finish(AT& s, OP& o, u32& want) {
  asm volatile("s_waitcnt vmcnt(8) lgkmcnt(0)\n\ts_barrier" ::: "memory");
  GAP();
  at_vread_first(s, make_iseq<12>{});
  if constexpr (MRG) {
    at_check<true>(s, want);
    asm volatile("buffer_load_dword %0, %2, %3, %4 offen sc0 sc1\n\tbuffer_load_dword %1, %2, %3, %4 offen offset:4 sc0 sc1" : "=&v"(s.mo), "=&v"(s.lo) : "v"(s.v8), "s"(s.rA), "s"(s.sM) : "memory");
  }
  at_a<XL, 13>(s); at_e<XL, 14>(s); at_e<XL, 15>(s); at_e<XL, 16>(s); at_e<XL, 17>(s); at_a<XL, 14>(s); at_a<XL, 15>(s); at_a<XL, 16>(s);
  at_pack<XL, 0, 4>(s); at_pack<XL, 0, 5>(s); at_pack<XL, 0, 6>(s); at_pack<XL, 0, 7>(s);
  asm volatile("" : "+v"(s.P[1]));
  GAP();
  PSTAMP(4);
  at_drain<MRG>(s, o, o.vl16 + o.sb[0], make_iseq<DR(MRG)>{});
  PSTAMP(5);
  op_chunk<MRG, 0>(s, o, make_iseq<28>{});
  PSTAMP(22);
  op_chunk<MRG, 1>(s, o, make_iseq<28>{});
  PSTAMP(6);
  op_chunk<MRG, 2>(s, o, make_iseq<28>{});
  PSTAMP(23);
  op_chunk<MRG, 3>(s, o, make_iseq<28>{});
  PSTAMP(7);
  op_chunk<MRG, 4>(s, o, make_iseq<28>{});
  PSTAMP(27);
  op_chunk<MRG, 5>(s, o, make_iseq<28>{});
  PSTAMP(8);
  op_last(o, make_iseq<56>{});
  PSTAMP(9);
  asm volatile("s_nop 7\n\ts_nop 3" ::: "memory"); GAP();
  op_tail(o, make_iseq<10>{});
  PSTAMP(10);
}

#define IDS(bid, lane, wid) \
  const int xcd = bid & 7, j = bid >> 3, h = 2 * xcd + (j & 1); \
  const int p = (j >> 1) & 7, role = (j >> 4) ^ 1;                  \
  const bool two = role && p < 7;                                   \
  const int qbH = 15 - p, qbO = role ? p : qbH;                     \
  const u32 lb = lds_addr(smem); \
  const u32 sM = (u32)WS_ML + (u32)((h * 8 + p) * 128 + wid * 32) * 8;     \
  const u32 sKb = (u32)WS_K + h * S_ * 512, sVb = (u32)WS_V + h * S_ * 512
#define SWF(rr, hh) ((u32)WS_WF + (((((rr) * 16 + (hh)) * 8 + p) * 4 + wid) << 4))
#define SPL(hh) ((u32)(((role * 16 + (hh)) * 8 + p) * 4 + wid) << 14)
DI int kv_tile(int rb, int l) { const int i = 3 * rb + l - 1; return l == 0 ? (rb < 5 ? rb : rb + 13) : i < 13 ? 5 + i : 8 + i; }
static_assert(WS_KVF % 512 == 0, "alignment of the words");
template <bool VT> DI void phase1(PJ& s, char* smem, const u16* x, const u16* ckv, const u16* wq, const u16* wkv, char* ws, const u16* gate_w, const u16* w_r, const u16* wo, int wid) {
  const int bid = blockIdx.x, lane = threadIdx.x & 63;
  IDS(bid, lane, wid);
  const srd_t rO = mk_srd(ws, (u32)WS_END);
  const int wm = wid >> 1, wn = wid & 1, fr = lane & 15, g = lane >> 4;
  constexpr int which = VT;
  const int rb = j >> 2;
  asm volatile("s_load_dword %0, %1, %2" : "=s"(s.tag) : "s"(ws), "s"((u32)WS_EP) : "memory");
  s.wid = wid;
  s.rA = mk_srd(ckv, S_ * DC * 2); s.rW = mk_srd(wkv, DC * HD * 2);
  s.rX = mk_srd(x, S_ * M_ * 2); s.rWq = mk_srd(wq, M_ * HD * 2); s.rO = rO;
  s.sRow = bid * 8 + wid * 2;
  {
    const u32 vo = lane < 56 ? (u32)lane * 16 : OOB;
    asm volatile("buffer_load_dwordx4 %0, %4, %5, %6 offen\n\tbuffer_load_dwordx4 %1, %4, %5, %6 offen offset:896\n\t"
                 "buffer_load_dwordx4 %2, %4, %7, 0 offen\n\tbuffer_load_dwordx4 %3, %4, %8, 0 offen"
                 : "=&v"(s.px[0]), "=&v"(s.px[1]), "=&v"(s.px[2]), "=&v"(s.px[3])
                 : "v"(vo), "s"(s.rX), "s"(rfl(s.sRow * 896)), "s"(mk_srd(gate_w, M_ * 2)), "s"(mk_srd(w_r, M_ * 2)) : "memory");
  }
  s.vP = lane * 16; s.m0P = lb + AT_X + wid * 1024;
  CODE_WARM(k_main, .Lk_code_end, s.m0P, (j * 4 + wid) * 1024, s.vP);
#pragma unroll
  for (int q = 0; q < 4; ++q) {
    const u32 c = ((lane & 7) ^ (((q & 1) << 2) | (lane >> 4))) << 4;
    s.vAkv[q] = (lane >> 3) * 2048 + c + q * 15360;
  }
#pragma unroll
  for (int v = 0; v < 4; ++v) { const u32 f = ((v & 1) << 1) | (lane >> 5) | ((wid & 1) << 2); s.vW[v] = (lane >> 5) * 8192 + (((lane & 31) ^ (f << 1)) << 4) + v * 15360; }
  s.m0A = lb + wid * 8192; s.m0W = lb + PJ_W0 + wid * 4096;
  s.sAkv = kv_tile(rb, wid) * 131072; s.sWkv = 8 * wid * 8192 + h * 512;
  pj_dmaW_all<0, 0>(s, s.sWkv, make_iseq<4>{});
  pj_dmaA_all<0>(s, s.sAkv, make_iseq<8>{});
  pj_dmaW_all<0, 1>(s, s.sWkv, make_iseq<4>{});
  pj_dmaA_all<1>(s, s.sAkv + 128, make_iseq<8>{});
  pj_dmaW_all<1, 0>(s, s.sWkv + 524288, make_iseq<4>{});
  GAP();
  s.pwo = wo + (size_t)(h * 256 + (8 * which + rb) * 16) * M_;
  s.sM = rfl(role ? OOB : sM);
  s.sPw = ((((j >> 2) * 2 + (j & 1)) * 8 + xcd) * 4 + wid) * 1024;
  s.sP = (bid * 4 + wid) * 1024; s.sPq = ((j * 4 + wid) * 8 + xcd) * 1024;
  s.sPf = (j * 4 + wid) * 7168 + xcd * 448; s.sPd = (j * 4 + wid) * 1024 + xcd * 200704; s.vT = lane < 28 ? lane * 16 : lane < 56 ? 3584 + (lane - 28) * 16 : OOB;
#pragma unroll
  for (int ks = 0; ks < 2; ++ks) s.aKV[ks] = lb + (32 * wm + fr) * 128 + (((4 * ks + g) ^ (fr >> 1)) << 4);
  { const int a4 = fr >> 2, f0 = a4 | ((g & 1) << 2);
#pragma unroll
    for (int nt = 0; nt < 8; ++nt) s.wb[nt] = lb + PJ_W0 + (8 * g + a4) * 512 + (8 * wn + (nt ^ f0)) * 32 + (lane & 3) * 8; }
  s.nQf = rfl(two ? 4 : 0); s.sQf = rfl((u32)WS_Q + ((h * 64 + qbH * 4 + wid) << 14));
  s.ones = (u32x4){0xffffffffu, 0xffffffffu, 0xffffffffu, 0xffffffffu};
  asm volatile("" : "+v"(s.ones));
  asm volatile("" : "+s"(s.pwo), "+s"(s.pg), "+s"(s.pu), "+s"(s.pd), "+s"(s.nQf), "+s"(s.sQf));
  pj_body1<VT>(s);
  asm volatile("s_waitcnt lgkmcnt(0)" : PJ_PIN8(0) :: "memory");
  {
    int bid = blockIdx.x, wid = s.wid;
    asm volatile("" : "+s"(bid), "+s"(wid));
    bid = rfl(bid); wid = rfl(wid);
    const int lane = lane_id();
    IDS(bid, lane, wid);
    const int wm = wid >> 1, wn = wid & 1, fr = lane & 15, g = lane >> 4, rb = j >> 2;
    s.wra = lb + AT_X + 4096u + (4 * (lane >> 5) + ((lane & 15) >> 2)) * 896 + wid * 64 + ((lane >> 4) & 1) * 32 + (lane & 3) * 8;
    s.sWo = (u32)WS_WO + h * 262144 + (8 * which + rb) * 8192;
    s.m0Aq = lb + wid * 4096;
    s.sAq = rfl((qbO * 128 + 32 * wid) * 896); s.sWq = 8 * wid * 8192 + h * 512;
#pragma unroll
    for (int q = 0; q < 4; ++q) {
      s.vAq[q] = (lane >> 3) * 896 + (((lane & 7) ^ (((q & 1) << 2) | (lane >> 4))) << 4) + q * 6144;
      s.vWq[q] = (lane >> 5) * 8192 + (((lane & 31) ^ ((2 * (q & 1) + (lane >> 5)) << 2)) << 4) + q * 15360;
      s.qb[q] = lb + PJ_W0 + (8 * (lane >> 5) + (fr >> 2)) * 512 + (q ^ (fr >> 2)) * 64 + (lane & 2) * 16 + (g & 1) * 8 + (lane & 1) * 16;
      s.xb[q] = lb + (32 * wid + (lane & 31)) * 128 + (((2 * q + (lane >> 5)) ^ ((lane >> 1) & 7)) << 4);
    }
    const u32 perm = (g & 1) * 2 + (g >> 1), t0 = h * 32 + kv_tile(rb, 0);
    if (which) { s.vO = g * 4096 + fr * 16; s.sO = (u32)WS_V + t0 * 32768 + wm * 16384 + wn * 2048; }
    else       { s.vO = perm * 1024 + fr * 16; s.sO = (u32)WS_K + t0 * 32768 + wn * 16384 + wm * 512; }
    s.sO = rfl(s.sO);
    if (bid == 112 && wid == 0) {
      u32x4 kv[16]; bool ok; u32 n = 1u << 16;
      do {
#pragma unroll
        for (int q = 0; q < 16; ++q)
          asm volatile("buffer_load_dwordx4 %0, %1, %2, %3 offen offset:%4 sc0 sc1" : "=v"(kv[q]) : "v"(s.vP), "s"(s.rO), "s"((u32)WS_KEY + (q >> 2) * 4096), "i"((q & 3) * 1024) : "memory");
        asm volatile("s_waitcnt vmcnt(0)" : "+v"(kv[0]), "+v"(kv[1]), "+v"(kv[2]), "+v"(kv[3]), "+v"(kv[4]), "+v"(kv[5]), "+v"(kv[6]), "+v"(kv[7]),
                     "+v"(kv[8]), "+v"(kv[9]), "+v"(kv[10]), "+v"(kv[11]), "+v"(kv[12]), "+v"(kv[13]), "+v"(kv[14]), "+v"(kv[15]) :: "memory");
        u32 d = 0u;
#pragma unroll
        for (int q = 0; q < 16; ++q) d |= (kv[q][1] ^ s.tag) | (kv[q][3] ^ s.tag);
        ok = __all(d == 0u);
      } while (!ok && --n);
      unsigned m1 = 0u, m2 = 0u;
#pragma unroll
      for (int q = 0; q < 16; ++q)
#pragma unroll
        for (int e = 0; e < 4; e += 2) { const unsigned k = kv[q][e]; m2 = max(m2, min(m1, k)); m1 = max(m1, k); }
#pragma unroll
      for (int o = 32; o > 0; o >>= 1) {
        const unsigned b1 = __shfl_xor(m1, o), b2 = __shfl_xor(m2, o);
        m2 = max(min(m1, b1), max(m2, b2)); m1 = max(m1, b1);
      }
      const u32x2 iv = {(65535u - (m1 & 0xffffu)) | ((65535u - (m2 & 0xffffu)) << 16), s.tag};
      asm volatile("buffer_store_dwordx2 %0, %1, %2, %3 offen" WT "\n\tbuffer_store_dword %4, %1, %2, %5 offen" WT "\n\ts_nop 0"
                   :: "v"(iv), "v"(lane ? OOB : 0u), "s"(s.rO), "s"((u32)WS_IDX), "v"(s.tag), "s"((u32)WS_EP) : "memory");
    }
    constexpr u32 a = which ? 256u : 4096u, b = which ? 1024u : 256u;
    s.dO[0] = a; s.dO[1] = b - 3u * a;
    GAP();
  }
  pj_body2<VT>(s);
}
DI void at_side(AT& s, u32 sT) {
  u32 st;
  asm volatile("s_cmp_gt_u32 %1, 3\n\ts_cbranch_scc1 .Lv%=\n\ts_lshl_b32 %0, %1, 11\n\ts_add_u32 m0, %3, %0\n\ts_add_u32 %0, %4, %0\n\t"
               "buffer_load_dwordx4 %5, %6, %0 offen" DV " lds\n\tbuffer_load_dwordx4 %5, %6, %0 offen offset:1024" DV " lds\n\t"
               "buffer_store_dwordx4 %9, %8, %6, %2 offen" WT "\n\tbuffer_store_dwordx4 %9, %8, %6, %2 offen offset:1024" WT "\n\tbuffer_store_dwordx4 %9, %8, %6, %2 offen offset:2048" WT "\n\t"
               "buffer_store_dwordx4 %9, %8, %6, %2 offen offset:3072" WT "\n\ts_add_u32 %2, %2, 4096\n\ts_branch .Lw%=\n"
               ".Lv%=:\n\ts_cmp_lg_u32 %1, 4\n\ts_cbranch_scc1 .Lw%=\n\tbuffer_store_dword %10, %11, %6, %12 offen" WT "\n\t"
               "s_add_u32 m0, %3, 7168\n\ts_add_u32 %0, %7, 1024\n\tbuffer_load_dwordx4 %8, %6, %7 offen lds\n\tbuffer_load_dwordx4 %8, %6, %0 offen lds\n"
               ".Lw%=:\n\ts_add_u32 %1, %1, 1"
               : "=&s"(st), "+s"(s.itn), "+s"(s.sYf) : "s"(s.m0X), "s"(s.sW0), "v"(s.vL), "s"(s.rA), "s"(sT), "v"(s.vl16), "v"(s.ones), "v"(s.vtag), "v"(s.v0), "s"(s.sDf) : "memory", "scc");
}
template <int... Rs> DI void acc_zero_mid(iseq<Rs...>) { (acc_wr<128 + Rs>(0u), ...); }
template <int K> DI void q_keep1(PJ& j, FF& f) {
  constexpr int L = K < 32 ? (K % 8 ? -1 : K / 8) : (K - 32) % 3 || K > 59 ? -1 : 4 + (K - 32) / 3;
  if constexpr (L >= 0) ff_load<(L < 2 ? 8 + L : L < 10 ? L - 2 : L)>(f);
  if constexpr (K < qcvN(32)) asm volatile("v_accvgpr_mov_b32 a[%c0], a[%c1]" :: "i"(128 + K), "i"(192 + K) : ACL); else q_cv<K, 128>(j);
}
template <int... Ks> DI void q_keep(PJ& j, FF& f, iseq<Ks...>) { (q_keep1<Ks>(j, f), ...); }
template <int F0, int... Fs> DI void q_get(AT& s, iseq<Fs...>) { (pb_ld<192, F0 + Fs>(s.vl16, s.rA, s.sPB), ...); }

DI void ff_row(AT& s, int task, int lane, srd_t rX) {
  u32 n = SPIN_MAX; u32x2 iv; bool ok;
  do {
    u32x4 a, b, c, d;
    asm volatile("buffer_load_dwordx4 %0, %5, %6, %7 offen sc0 sc1\n\tbuffer_load_dwordx4 %1, %5, %6, %7 offen offset:1024 sc0 sc1\n\tbuffer_load_dwordx4 %2, %5, %6, %7 offen offset:2048 sc0 sc1\n\t"
                 "buffer_load_dwordx4 %3, %5, %6, %7 offen offset:3072 sc0 sc1\n\tbuffer_load_dwordx2 %4, %8, %6, %9 offen sc0 sc1\n\ts_waitcnt vmcnt(0)"
                 : "=&v"(a), "=&v"(b), "=&v"(c), "=&v"(d), "=&v"(iv) : "v"(s.vl16), "s"(s.rA), "s"((u32)WS_DF), "v"(0u), "s"((u32)WS_IDX) : "memory");
    u32 x = iv[1] ^ s.tag;
#pragma unroll
    for (int e = 0; e < 4; ++e) x |= ((a[e] ^ s.tag) | (b[e] ^ s.tag)) | ((c[e] ^ s.tag) | (d[e] ^ s.tag));
    ok = __all(x == 0u);
    if (!ok) __builtin_amdgcn_s_sleep(4);
  } while (!ok && --n);
  const u32 idx = rfl(iv[0]), k = task >= 28, ir = (k ? idx >> 16 : idx) & 2047u, col = (task - 28 * k) * 16 + (lane & 3) * 4;
  f32x4 v[7]; u32x2 xv; float rv; const u32 sf = rfl((u32)WS_YF + task * (FFN_N * 64));
#pragma unroll
  for (int q = 0; q < 7; ++q) asm volatile("buffer_load_dwordx4 %0, %1, %2, %3 offen offset:%4 sc0 sc1" : "=v"(v[q]) : "v"(s.vl16), "s"(s.rA), "s"(sf + (q >> 2) * 4096), "i"((q & 3) * 1024) : "memory");
  asm volatile("buffer_load_dwordx2 %0, %9, %10, %11 offen\n\tbuffer_load_dword %1, %12, %13, %14 offen sc0 sc1\n\ts_waitcnt vmcnt(0)"
               : "=&v"(xv), "=&v"(rv), "+v"(v[0]), "+v"(v[1]), "+v"(v[2]), "+v"(v[3]), "+v"(v[4]), "+v"(v[5]), "+v"(v[6])
               : "v"(col * 2), "s"(rX), "s"(ir * (M_ * 2)), "v"(0u), "s"(s.rA), "s"((u32)WS_R + ir * 4) : "memory");
  const f32x4 fs = ((v[0] + v[1]) + (v[2] + v[3])) + ((v[4] + v[5]) + v[6]);
  const float sg = rbf(1.f / (1.f + __expf(-rv))), xs[4] = {BF_LO(xv[0]), BF_HI(xv[0]), BF_LO(xv[1]), BF_HI(xv[1])};
  float r[4];
#pragma unroll
  for (int e = 0; e < 4; ++e) {
    unsigned w = __float_as_uint(fs[e]);
    const auto s32 = __builtin_amdgcn_permlane32_swap(w, w, false, false);
    w = __float_as_uint(__uint_as_float(s32[0]) + __uint_as_float(s32[1]));
    const auto s16 = __builtin_amdgcn_permlane16_swap(w, w, false, false);
    float a = __uint_as_float(s16[0]) + __uint_as_float(s16[1]);
    a += __uint_as_float(__builtin_amdgcn_update_dpp(0u, __float_as_uint(a), 0x128, 0xf, 0xf, false));
    a += __uint_as_float(__builtin_amdgcn_update_dpp(0u, __float_as_uint(a), 0x124, 0xf, 0xf, false));
    r[e] = xs[e] + rbf(rbf(a) * sg);
  }
  const u32x2 o = {cvtpk(r[0], r[1]), cvtpk(r[2], r[3])};
  asm volatile("buffer_store_dwordx2 %0, %1, %2, %3 offen" WT "\n\ts_waitcnt vmcnt(0)\n\tbuffer_store_dword %4, %5, %2, %6 offen offset:12" WT
               :: "v"(o), "v"(lane < 4 ? col * 2 : OOB), "s"(s.rA), "s"((u32)WS_BB + k * (M_ * 2)), "v"(s.vtag), "v"(s.v0), "s"((u32)WS_IDX + task * 8) : "memory");
}
typedef __attribute__((ext_vector_type(8))) __bf16 bfx8;
#define RD_PIN "+v"(y[0]), "+v"(y[1]), "+v"(y[2]), "+v"(y[3]), "+v"(y[4]), "+v"(y[5]), "+v"(y[6]), "+v"(y[7]), "+v"(y[8]), "+v"(y[9]), "+v"(y[10]), "+v"(y[11]), "+v"(y[12]), "+v"(y[13]), "+v"(y[14]), "+v"(y[15])
#define RDL(i, b, o) "s_bitcmp0_b32 %16, " #i "\n\ts_cbranch_scc1 .Lr" #i "_%=\n\tbuffer_load_dwordx4 %" #i ", %17, %18, %" #b " offen offset:" #o " sc0 sc1\n.Lr" #i "_%=:\n\t"
template <int I> DI void rd_ld(u32x4 (&y)[16], u32 vy, srd_t rA, u32 sy) {
  asm volatile("buffer_load_dwordx4 %0, %1, %2, %3 offen offset:%4 sc0 sc1" : "=v"(y[I]) : "v"(vy), "s"(rA), "s"(sy + (I & 1) * (4u << 20)), "i"(((I >> 1) & 1) * 512 + ((I >> 2) & 1) * 1024 + (I >> 3) * 256) : "memory");
}
template <int... Is> DI void rd_ld_all(u32x4 (&y)[16], u32 vy, srd_t rA, u32 sy, iseq<Is...>) { (rd_ld<Is>(y, vy, rA, sy), ...); }
DI void rd_task(AT& s, RD& d, u32 sy, srd_t rOut) {
  const int lane = lane_id(), r = lane & 15, g = lane >> 4;
  const u32 vy = g * (1u << 20) + r * 16;
  u32x4 y[16];
  rd_ld_all(y, vy, s.rA, sy, make_iseq<16>{});
  asm volatile("s_waitcnt vmcnt(16)" : "+v"(d.gt[0]), "+v"(d.gt[1]), "+v"(d.xv[0]), "+v"(d.xv[1]), "+v"(d.xv[2]), "+v"(d.xv[3]), "+v"(d.bv[0]), "+v"(d.bv[1]), "+v"(d.bv[2]), "+v"(d.bv[3]) :: "memory");
  PSTAMP(28);
  u32x4 sel[2];
#pragma unroll
  for (int hi = 0; hi < 2; ++hi) {
    const int rr = r - 4 * hi, e = (rr & ~3) == 0 ? rr : (rr & ~3) == 8 ? rr - 4 : -1;
#pragma unroll
    for (int w = 0; w < 4; ++w) sel[hi][w] = e == 2 * w ? 0x3f80u : e == 2 * w + 1 ? 0x3f800000u : 0u;
  }
  const u32 idx = __builtin_amdgcn_readlane(d.fl[0], 63);
  float bb[4][4];
#pragma unroll
  for (int k = 0; k < 4; ++k) {
    const u32 rw = d.row + 16 * (k >> 1);
    const bool rt = rw == (idx & 0xffffu) || rw == idx >> 16;
    const u32 a = rt ? d.bv[k][0] : d.xv[k][0], b = rt ? d.bv[k][1] : d.xv[k][1];
    const float omg = rbf(1.f - d.gt[k >> 1]);
    bb[k][0] = rbf(omg * BF_LO(a)); bb[k][1] = rbf(omg * BF_HI(a)); bb[k][2] = rbf(omg * BF_LO(b)); bb[k][3] = rbf(omg * BF_HI(b));
    PINF(bb[k][0]); PINF(bb[k][1]); PINF(bb[k][2]); PINF(bb[k][3]);
  }
  asm volatile("" : "+v"(sel[0]), "+v"(sel[1]));
  asm volatile("s_waitcnt vmcnt(0)" : RD_PIN :: "memory");
  for (u32 n = SPIN_MAX;;) {
    u32 miss = 0u;
#pragma unroll
    for (int i = 0; i < 16; ++i) miss |= __any(y[i][3] == 0xffffffffu) ? 1u << i : 0u;
    if (!miss || !--n) break;
    asm volatile(RDL(0, 19, 0) RDL(1, 20, 0) RDL(2, 19, 512) RDL(3, 20, 512) RDL(4, 19, 1024) RDL(5, 20, 1024) RDL(6, 19, 1536) RDL(7, 20, 1536)
                 RDL(8, 19, 256) RDL(9, 20, 256) RDL(10, 19, 768) RDL(11, 20, 768) RDL(12, 19, 1280) RDL(13, 20, 1280) RDL(14, 19, 1792) RDL(15, 20, 1792) "s_waitcnt vmcnt(0)"
                 : RD_PIN : "s"(rfl(miss)), "v"(vy), "s"(s.rA), "s"(sy), "s"(sy + (4u << 20)) : "memory", "scc");
  }
  PSTAMP(29);
  u32 pk[4][2];
#pragma unroll
  for (int k = 0; k < 4; ++k) {
    f32x4 a = {0.f, 0.f, 0.f, 0.f};
#pragma unroll
    for (int i = 0; i < 4; ++i) a = __builtin_amdgcn_mfma_f32_16x16x32_bf16(__builtin_bit_cast(bfx8, sel[i >> 1]), __builtin_bit_cast(bfx8, y[8 * (k >> 1) + 4 * (k & 1) + i]), a, 0, 0, 0);
    const float gg = d.gt[k >> 1];
    pk[k][0] = cvtpk(rbf(gg * rbf(a[0])) + bb[k][0], rbf(gg * rbf(a[1])) + bb[k][1]); pk[k][1] = cvtpk(rbf(gg * rbf(a[2])) + bb[k][2], rbf(gg * rbf(a[3])) + bb[k][3]);
  }
#pragma unroll
  for (int u = 0; u < 2; ++u) {
    const auto r0 = __builtin_amdgcn_permlane16_swap(pk[2 * u][0], pk[2 * u + 1][0], false, false);
    const auto r1 = __builtin_amdgcn_permlane16_swap(pk[2 * u][1], pk[2 * u + 1][1], false, false);
    st16<0>((u32x4){r0[0], r1[0], r0[1], r1[1]}, d.vx - g * 8 + (g & 1) * 32 + (g >> 1) * 16, rOut, u * 16 * M_ * 2);
  }
  PSTAMP(30);
}

#define RSTAMP() do { } while (0)
#define FINISH(MRG) \
  o.sb[0] = lb + AT_X; o.sb[1] = lb; o.sb[2] = lb + 32768; o.sb[3] = lb + 98304; o.sb[4] = lb + 65536; \
  o.wofs = wid * 8192; o.rA = s.rA; o.vL = s.vL; o.vl16 = s.vl16; o.tag = s.tag; \
  o.sY = (u32)WS_Y + SPL(h); o.sX = (u32)WS_X + SPL(h); o.sXp = (u32)WS_X + SPL(h ^ 1); \
  o.sW1 = sW1; o.sWf = SWF(role, h); o.sWfp = SWF(role, h ^ 1); \
  const int task = (xcd * 14 + (j & 15)) * 4 + wid, qb = task / 14 + (role ? 0 : 32), t = task % 14, ch = t >= 7; \
  o.r.row = qb * 32 + (ln & 15); o.r.vc = (32 * t + 4 * (ln >> 4)) * 2; o.r.vx = o.r.row * (M_ * 2) + o.r.vc; o.r.vf = ln < 56 ? 8 + 8 * ln : 0; o.r.rX = mk_srd(x, S_ * M_ * 2); \
  at_finish<MRG>(s, o, want); \
    \
  if (p < 7) rd_task(s, o.r, rfl((u32)WS_Y + (((role * 16 + ch) * 8 + (role ? qb >> 2 : 15 - (qb >> 2))) * 4 + (qb & 3)) * 16384u + (t - 7 * ch) * 2048), mk_srd(out, S_ * M_ * 2))
#define LOOP_END \
  const u16* x; u16* out; \
  asm volatile("s_load_dwordx2 %0, %2, 0x0\n\ts_load_dwordx2 %1, %2, 0x60\n\ts_waitcnt lgkmcnt(0)" : "=&s"(x), "=&s"(out) : "s"(ka) : "memory"); \
  GAP(); PSTAMP(3);     \
  int ln = lane; asm volatile("" : "+v"(ln))
extern "C" __global__ __launch_bounds__(256, 1)
void k_main(const u16* x_, const u16* ckv, const u16* wq, const u16* wuk, const u16* wuv, char* ws,
            const u16* gate_w, const u16* w_r, const u16* wo, const u16* wg_, const u16* wu_, const u16* wd_, u16* out_) {
  extern __shared__ __attribute__((aligned(16))) char smem[];
  int wid = rfl(threadIdx.x >> 6);
  PJ pj;
  pj.pg = wg_; pj.pu = wu_; pj.pd = wd_;
  if (blockIdx.x & 16) phase1<true>(pj, smem, x_, ckv, wq, wuv, ws, gate_w, w_r, wo, wid);
  else                 phase1<false>(pj, smem, x_, ckv, wq, wuk, ws, gate_w, w_r, wo, wid);
  GAP();
  int bid = blockIdx.x;
  asm volatile("" : "+s"(bid), "+s"(wid));
  bid = rfl(bid); wid = rfl(wid);
  const int lane = lane_id();
  IDS(bid, lane, wid);
  const int l31 = lane & 31, hi = lane >> 5;
  AT s;
  typedef const __attribute__((address_space(4))) char* karg_t;
  karg_t ka = (karg_t)__builtin_amdgcn_kernarg_segment_ptr();
  const u32 sq = rfl(p < 7 ? (u32)WS_Q + ((h * 64 + qbH * 4 + wid) << 14) : OOB);
  { const int which = (j >> 1) & 1, rb = j >> 2;
    const u32 a = which ? 256u : 4096u, b = which ? 1024u : 256u;
    pj.dO[0] = rfl(a); pj.dO[1] = rfl(b - 3u * a);
#pragma unroll
    for (int l = 0; l < 3; ++l) pj.dO[2 + l] = rfl((u32)(kv_tile(rb, l + 1) - kv_tile(rb, l)) * 32768u - b - 3u * a);
    pj.sO = rfl(pj.sO); pj.tag = rfl(pj.tag); pj.wid = wid;
    pj.rX = mk_srd(x_, S_ * M_ * 2); pj.rWq = mk_srd(wq, M_ * HD * 2); pj.rO = mk_srd(ws, (u32)WS_END);
    pj.m0W = lb + PJ_W0 + wid * 4096; pj.m0Aq = lb + wid * 4096; pj.sAq = rfl((qbO * 128 + 32 * wid) * 896); pj.sWq = 8 * wid * 8192 + h * 512;
    pj.sFl = rfl((u32)WS_KVF + h * 256 + (2 * rb + which) * 16); pj.vFl = (lane & 15) * 16; pj.poll = 0u;
    pj.sK0 = rfl(sKb + (two ? NT0 * 32768u : 0u)); pj.sV0 = pj.sK0 + (u32)(WS_V - WS_K);
    pj.vL = lane * 16 + wid * 8192; pj.m0K = lb + wid * 8192;
    pj.sQo = rfl(role ? OOB : sq);
    const int sg = (l31 & 7) | ((l31 & 8) << 1) | ((l31 & 16) >> 1);
    s.kb_ = lb + hi * 1024 + sg * 16; s.kb0 = s.kb_ + AT_X;
    asm volatile("s_load_dwordx2 %0, %4, 0x0\n\ts_load_dwordx2 %1, %4, 0x48\n\ts_load_dwordx2 %2, %4, 0x50\n\ts_load_dwordx2 %3, %4, 0x58"
                 : "=&s"(s.f.x), "=&s"(s.f.wg), "=&s"(s.f.wu), "=&s"(s.f.wd) : "s"(ka) : "memory");
    PSTAMP(9);
    GAP();
    q_all(pj, s.kf, s.kb0, make_iseq<14>{});
    asm volatile("" : "+s"(s.f.x), "+s"(s.f.wg), "+s"(s.f.wu), "+s"(s.f.wd));
    PSTAMP(10);
  }
  s.rA = pj.rO; s.sW0 = (u32)WS_WO + h * 262144 + (h & 1) * 131072; s.tag = pj.tag;
  s.vL = pj.vL; s.vl16 = pj.vP;
  s.rem = rfl(two ? 14 - 2 * p : role ? 16 : NT0); s.mk = rfl((role || p == 7) ? 2 : 0);
  s.sE0 = rfl(two ? sKb : s.sW0 + 32768u);
  static_assert(NT0 == 18, "tiles");
  s.sK = pj.sK0 + 32768u; s.sV = pj.sV0;
  s.m0K = pj.m0K; s.m0V = lb + 65536 + wid * 8192; s.m0X = lb + AT_X + wid * 8192;
  s.vb_ = lb + 65536 + hi * 4096 + l31 * 16;
  s.dq = wid * 32 + l31 - 4 * hi;
  s.mhat = 0.f; s.l0 = 0.f; s.l1 = 0.f; s.fct = 1.f; s.pend = false; s.nz = 0u; s.itn = 0u;
  s.sFl = (pj.sFl & ~511u) + 12u; s.vFl = l31 * 16;
  s.v0 = lane ? OOB : 0u; s.vtag = s.tag; s.sPB = role && !two ? OOB : sq; s.sM = rfl(p < 7 ? sM : OOB); s.v8 = l31 * 8;
  const u32 sW1 = (u32)WS_WO + (h ^ 1) * 262144 + (h & 1) * 131072, sT = rfl(sW1 + ((j >> 1) * 4 + wid) * 2048);
  s.sYf = rfl((u32)WS_Y + SPL(h)); s.sDf = rfl((u32)WS_DF + (bid * 4 + wid) * 4);
  asm volatile("" : "+s"(s.rem), "+s"(s.mk), "+s"(s.sE0), "+s"(s.sPB), "+s"(s.sM), "+s"(s.sYf), "+s"(s.sDf));
  asm volatile("" : "+v"(s.v0), "+v"(s.vtag), "+v"(s.v8));
  OP o;
  GAP();
  if (two) {
    FF& f = s.f;
    z_dma<ZA, 0>(pj);
    const int sl = xcd * 14 + j, rp = lane >> 1, fq = lane >> 4, n8 = lane & 15;
    const u32 o1 = rp < 28 ? 0u : OOB, o2 = n8 < 14 ? 0u : OOB;
    f.rA = s.rA;
    f.vw = (u32)((112 * wid + 4 * rp) * F_ + sl * 16 + (lane & 1) * 8) * 2 | o1;
    f.vx = (u32)(112 * wid + 4 * rp) * 2 | o1;
    f.vd = (u32)((sl * 16 + 4 * fq) * M_ + 112 * wid + 8 * n8) * 2 | o2;
    const int n = 112 * wid + 8 * n8 + 4 * (fq & 1);
    f.vy = (u32)((((fq >> 1) * 28 + (n >> 4)) * FFN_N + sl) * 16 + (n & 15)) * 4 | o2;
    f.lw = lb + wid * 256 + fq * 64 + (lane & 1) * 32; f.lr = lb + wid * 8192 + (lane & 31) * 4;
    f.lh = f.lr + 1024; f.lg = lb + wid * 8192 + 1024 + fq * 16;
    { u32 c = SPIN_MAX, z = 0u;
      while (rfl(pj.iv[1]) != s.tag && --c)
        asm volatile("buffer_load_dwordx2 %0, %1, %2, %3 offen sc0 sc1\n\ts_waitcnt vmcnt(0)" : "+v"(pj.iv) : "v"(z), "s"(s.rA), "s"((u32)WS_IDX) : "memory");
      f.i0 = rfl(pj.iv[0]); f.i1 = (f.i0 >> 16) & 2047u; f.i0 &= 2047u; }
    asm volatile("" : "+s"(f.i0), "+s"(f.i1));
    asm volatile("s_waitcnt lgkmcnt(0)" : AT_PIN8(kf, 0), AT_PIN4(kf, 8) :: "memory");
    GAP();
    u32 kp;
    q_keep(pj, f, make_iseq<64>{});
    asm volatile("s_waitcnt vmcnt(14)\n\ts_barrier" ::: "memory");
    z_flag<3>(pj);
    PSTAMP(20);
    q_get<0>(s, make_iseq<16>{});
    kv_poll(kp, s.vFl, s.rA, s.sFl);
    PSTAMP(21);
    asm volatile("s_waitcnt vmcnt(17)" ::: "memory");
    ff_steps<56>(f, make_iseq<50>{});
    PSTAMP(19);
    asm volatile("s_waitcnt vmcnt(0)" : "+v"(kp) :: "memory");
    at_getq(s);
    kv_check_all(kp, s.tag, s.vFl, s.rA, s.sFl);
    GAP();
    PSTAMP(1);
    at_phase1<false>(s, pj, make_iseq<32>{});
    at_period<1, 5>(s);
    asm volatile("s_waitcnt lgkmcnt(0)" ::: "memory");
    GAP();
    PSTAMP(12);
    s.ones = (u32x4){0xffffffffu, 0xffffffffu, 0xffffffffu, 0xffffffffu};
    asm volatile("" : "+v"(s.ones));
    for (int seg = 0;; ++seg) {
      while (s.rem > 0) { RSTAMP(); at_period<0, 1>(s); at_side(s, sT); at_period<1, 1>(s); }
      if (seg) break;
      PSTAMP(2);
      s.rem = 2 * p + 2; s.sE0 = s.sW0 + 32768u; s.sV = sVb;
      PSTAMP(24);
      at_period<0, 2>(s);
      PSTAMP(25);
      at_period<1, 3>(s);
      asm volatile("s_waitcnt lgkmcnt(0)" ::: "memory");
      PSTAMP(26);
      GAP();
    }
    u32 want = 0u;
    LOOP_END;
    FINISH(false);
    __builtin_amdgcn_endpgm();
  }
  {
    PSTAMP(1);
    at_phase1<true>(s, pj, make_iseq<32>{});
    PSTAMP(24);
    if (p == 7) acc_zero_mid(make_iseq<64>{});
    at_phaseA<1, 4>(s, make_iseq<32>{});
    PSTAMP(25);
    at_phaseB<1, 4>(s, make_iseq<32>{});
    asm volatile("s_waitcnt vmcnt(4) lgkmcnt(0)" ::: "memory");
    GAP();
    PSTAMP(12);
    u32 want = 2u + (u32)((14 - p) >> 3);
    s.ones = (u32x4){0xffffffffu, 0xffffffffu, 0xffffffffu, 0xffffffffu};
    asm volatile("" : "+s"(want));
    asm volatile("" : "+v"(s.ones));
    while (s.rem > 0) { RSTAMP(); at_period<0, 0>(s); at_check<false>(s, want); at_side(s, sT); at_period<1, 0>(s); }
    LOOP_END;
    if (role && (xcd * 2 + (j & 1)) * 4 + wid < 56) ff_row(s, (xcd * 2 + (j & 1)) * 4 + wid, ln, mk_srd(x, S_ * M_ * 2));
#pragma unroll
    for (int a = 0; a < 2; ++a) {
      const int rr = (ln & 31) - 16 * a - 4 * (ln >> 5), e = (rr & ~3) == 0 ? rr : (rr & ~3) == 8 ? rr - 4 : -1;
#pragma unroll
      for (int w = 0; w < 4; ++w) o.id[a][w] = e == 2 * w ? 0x3f80u : e == 2 * w + 1 ? 0x3f800000u : 0u;
    }
    FINISH(true);
  }
  asm volatile(".Lk_code_end:");
}

extern "C" void kernel_launch(void* const* d_in, const int* in_sizes, int n_in,
                              void* d_out, int out_size, void* d_ws, size_t ws_size,
                              hipStream_t stream) {
  (void)hipFuncSetAttribute((const void*)k_main, hipFuncAttributeMaxDynamicSharedMemorySize, AT_LDS);
  k_main<<<256, 256, AT_LDS, stream>>>((const u16*)d_in[0], (const u16*)d_in[1], (const u16*)d_in[2], (const u16*)d_in[3], (const u16*)d_in[4],
      (char*)d_ws, (const u16*)d_in[11], (const u16*)d_in[7], (const u16*)d_in[5], (const u16*)d_in[8], (const u16*)d_in[9], (const u16*)d_in[10], (u16*)d_out);
}
```

```cpp
#include <hip/hip_runtime.h>
#include <stdint.h>

extern "C" __global__ __launch_bounds__(256, 1)
void k_main(const void* x, const void* ckv, const void* wq, const void* wuk, const void* wuv, void* ws,
            const void* gate_w, const void* w_r, const void* wo, const void* wg, const void* wu, const void* wd, void* out) {
#if defined(__HIP_DEVICE_COMPILE__)
  asm volatile(
"s_mov_b64 s[8:9], %0\ns_mov_b32 s10, %1\nv_mov_b32 v0, %2\n.macro WL n\ns_waitcnt lgkmcnt(\\n)\n.endm\n.macro D0 x,r,s,o\n"
"buffer_load_dwordx4 v\\x, s[\\r:\\r+3], s\\s offen offset:\\o lds\n.endm\n.macro WV n\ns_waitcnt vmcnt(\\n)\n.endm\n.macro Ta d,x,o\n"
"ds_read_b64_tr_b16 a[\\d:\\d+1], v\\x offset:\\o\n.endm\n.macro Ra d,x,o\nds_read_b128 a[\\d:\\d+3], v\\x offset:\\o\n.endm\n.macro Zvaa d,x,y\n"
"v_mfma_f32_16x16x32_bf16 v[\\d:\\d+3], a[\\x:\\x+3], a[\\y:\\y+3], 0\n.endm\n.macro Zaaa d,x,y\nv_mfma_f32_16x16x32_bf16 a[\\d:\\d+3], a[\\x:\\x+3], a[\\y:\\y+3], 0\n.endm\n"
".macro Mvaa d,x,y\nv_mfma_f32_16x16x32_bf16 v[\\d:\\d+3], a[\\x:\\x+3], a[\\y:\\y+3], v[\\d:\\d+3]\n.endm\n.macro Maaa d,x,y\n"
"v_mfma_f32_16x16x32_bf16 a[\\d:\\d+3], a[\\x:\\x+3], a[\\y:\\y+3], a[\\d:\\d+3]\n.endm\n.macro MU d,x,y\nv_mul_f32 v\\d, v\\x, v\\y\n.endm\n.macro CV d,x,y\n"
"v_cvt_pk_bf16_f32 v\\d, v\\x, v\\y\n.endm\n.macro FM d,x,y\nv_fmac_f32 v\\d, v\\x, v\\y\n.endm\n.macro AD d,x,y\nv_add_f32 v\\d, v\\x, v\\y\n.endm\n.macro EX d,x\n"
"v_exp_f32 v\\d, v\\x\n.endm\n.macro Sv d,x,r,s,o\nbuffer_store_dwordx4 v[\\d:\\d+3], v\\x, s[\\r:\\r+3], s\\s offen offset:\\o sc0 sc1\n.endm\n.macro Rv d,x,o\n"
"ds_read_b128 v[\\d:\\d+3], v\\x offset:\\o\n.endm\n.macro D1 x,r,s,o\nbuffer_load_dwordx4 v\\x, s[\\r:\\r+3], s\\s offen offset:\\o sc1 lds\n.endm\n.macro AR d,x\n"
"v_accvgpr_read_b32 v\\d, a\\x\n.endm\n.macro Tv d,x,o\nds_read_b64_tr_b16 v[\\d:\\d+1], v\\x offset:\\o\n.endm\n.macro Cvav d,x,y,c\n"
"v_mfma_f32_16x16x32_bf16 v[\\d:\\d+3], a[\\x:\\x+3], v[\\y:\\y+3], v[\\c:\\c+3]\n.endm\n.macro Mvav d,x,y\n"
"v_mfma_f32_16x16x32_bf16 v[\\d:\\d+3], a[\\x:\\x+3], v[\\y:\\y+3], v[\\d:\\d+3]\n.endm\n.macro AW d,x\nv_accvgpr_write_b32 a\\d, v\\x\n.endm\n.macro Zaav d,x,y\n"
"v_mfma_f32_16x16x32_bf16 a[\\d:\\d+3], a[\\x:\\x+3], v[\\y:\\y+3], 0\n.endm\n.macro Maav d,x,y\nv_mfma_f32_16x16x32_bf16 a[\\d:\\d+3], a[\\x:\\x+3], v[\\y:\\y+3], a[\\d:\\d+3]\n"
".endm\n.macro SU d,x,y\nv_sub_f32 v\\d, v\\x, v\\y\n.endm\n.macro Mava d,x,y\nv_mfma_f32_16x16x32_bf16 a[\\d:\\d+3], v[\\x:\\x+3], a[\\y:\\y+3], a[\\d:\\d+3]\n.endm\n"
".macro Zvav d,x,y\nv_mfma_f32_16x16x32_bf16 v[\\d:\\d+3], a[\\x:\\x+3], v[\\y:\\y+3], 0\n.endm\n.macro Zvvv d,x,y\n"
"v_mfma_f32_16x16x32_bf16 v[\\d:\\d+3], v[\\x:\\x+3], v[\\y:\\y+3], 0\n.endm\n.macro Mvvv d,x,y\nv_mfma_f32_16x16x32_bf16 v[\\d:\\d+3], v[\\x:\\x+3], v[\\y:\\y+3], v[\\d:\\d+3]\n"
".endm\n.macro Cvaa d,x,y,c\nv_mfma_f32_16x16x32_bf16 v[\\d:\\d+3], a[\\x:\\x+3], a[\\y:\\y+3], v[\\c:\\c+3]\n.endm\n.Lcode0:\ns_nop 0\ns_load_dwordx16 s[44:59], s[8:9], 0x0\n"
"s_load_dwordx8 s[60:67], s[8:9], 0x40\ns_load_dwordx2 s[68:69], s[8:9], 0x60\nv_lshrrev_b32 v2, 6, v0\nv_and_b32 v0, 63, v0\nv_readfirstlane_b32 s11, v2\nv_lshlrev_b32 v1, 4, v0\n"
"s_and_b32 s73, s10, 7\ns_lshr_b32 s74, s10, 3\ns_and_b32 s2, s74, 1\ns_lshl1_add_u32 s12, s73, s2\ns_bfe_u32 s13, s74, 0x30001\ns_lshr_b32 s14, s74, 4\ns_xor_b32 s14, s14, 1\n"
"s_mov_b32 s70, 0\nv_lshrrev_b32 v32, 5, v0\ns_and_b32 s3, s11, 1\ns_lshl_b32 s2, s3, 3\nv_lshl_or_b32 v33, v32, 1, s2\nv_and_b32 v34, 31, v0\nv_xor_b32 v33, v33, v34\n"
"v_lshlrev_b32 v33, 4, v33\nv_lshl_or_b32 v6, v32, 13, v33\nv_xor_b32 v33, 64, v6\nv_add_u32 v7, 15360, v33\nv_add_u32 v8, 30720, v6\nv_add_u32 v9, 46080, v33\n"
"s_lshl_b32 s2, s11, 16\ns_lshl_b32 s4, s12, 9\ns_add_u32 s5, s2, s4\nWL 0\ns_mov_b32 s16, s54\ns_and_b32 s17, s55, 0xffff\ns_mov_b32 s18, 0x5cae800\ns_mov_b32 s19, 0x20000\n"
"s_mov_b32 s28, s44\ns_and_b32 s29, s45, 0xffff\ns_mov_b32 s30, 0x1c0000\ns_mov_b32 s31, 0x20000\ns_mov_b32 s32, s48\ns_and_b32 s33, s49, 0xffff\ns_mov_b32 s34, 0x380000\n"
"s_mov_b32 s35, 0x20000\ns_mov_b32 s20, s46\ns_and_b32 s21, s47, 0xffff\ns_mov_b32 s22, 0x400000\ns_mov_b32 s23, 0x20000\ns_bitcmp1_b32 s13, 0\n"
"s_cselect_b64 s[50:51], s[52:53], s[50:51]\ns_mov_b32 s24, s50\ns_and_b32 s25, s51, 0xffff\ns_mov_b32 s26, 0x800000\ns_mov_b32 s27, 0x20000\ns_mov_b32 s2, 0x5c48000\n"
"s_load_dword s15, s[54:55], s2\ns_sub_u32 s44, s5, 0x40000\ns_mov_b32 s51, s5\ns_lshl_b32 s47, s11, 12\ns_add_u32 s47, s47, 0x10000\ns_lshl_b32 s48, s11, 13\ns_cmp_eq_u32 s14, 0\n"
"s_cbranch_scc1 .R01\ns_add_u32 m0, s47, 0\ns_add_u32 s44, s44, 0x40000\nD0 6,24,44,0\nD0 7,24,44,1024\nD0 8,24,44,2048\nD0 9,24,44,3072\nv_lshrrev_b32 v32, 4, v0\n"
"v_and_b32 v33, 7, v0\nv_xor_b32 v33, v33, v32\nv_lshlrev_b32 v33, 4, v33\nv_lshrrev_b32 v32, 3, v0\nv_lshl_or_b32 v2, v32, 11, v33\nv_xor_b32 v33, 64, v2\n"
"v_add_u32 v3, 15360, v33\nv_add_u32 v4, 30720, v2\nv_add_u32 v5, 46080, v33\ns_lshr_b32 s2, s13, 2\ns_bfe_u32 s3, s13, 0x10001\ns_mul_i32 s3, s3, 17\ns_add_u32 s2, s2, s3\n"
"s_xor_b32 s3, s14, 1\ns_lshl3_add_u32 s2, s3, s2\ns_lshl1_add_u32 s2, s11, s2\ns_cmp_eq_u32 s2, 32\ns_cselect_b32 s4, 16, s2\ns_lshl_b32 s45, s4, 17\ns_sub_u32 s45, s45, 128\n"
"s_add_u32 m0, s48, 0\ns_add_u32 s45, s45, 128\nD0 2,20,45,0\nD0 3,20,45,1024\nD0 4,20,45,2048\nD0 5,20,45,3072\ns_add_u32 m0, s48, 4096\ns_add_u32 s46, s45, 0x10000\nD0 2,20,46,0\n"
"D0 3,20,46,1024\nD0 4,20,46,2048\nD0 5,20,46,3072\ns_add_u32 m0, s47, 16384\ns_add_u32 s44, s44, 0x40000\nD0 6,24,44,0\nD0 7,24,44,1024\nD0 8,24,44,2048\nD0 9,24,44,3072\n"
"v_mov_b32 v52, 0x40000000\nv_cmp_gt_u32 vcc, 56, v0\ns_lshl2_add_u32 s2, s10, s11\ns_mul_i32 s2, s2, 1792\nv_cndmask_b32 v53, v52, v1, vcc\ns_mov_b32 s36, s56\n"
"s_and_b32 s37, s57, 0xffff\ns_mov_b32 s38, 0x380\ns_mov_b32 s39, 0x20000\ns_mov_b32 s40, s58\ns_and_b32 s41, s59, 0xffff\ns_mov_b32 s42, 0x380\ns_mov_b32 s43, 0x20000\n"
"buffer_load_dwordx4 v[32:35], v53, s[28:31], s2 offen offset:0\nbuffer_load_dwordx4 v[36:39], v53, s[28:31], s2 offen offset:896\n"
"buffer_load_dwordx4 v[40:43], v53, s[36:39], 0 offen offset:0\nbuffer_load_dwordx4 v[44:47], v53, s[40:43], 0 offen offset:0\ns_add_u32 m0, s48, 32768\ns_add_u32 s45, s45, 128\n"
"D0 2,20,45,0\nD0 3,20,45,1024\nD0 4,20,45,2048\nD0 5,20,45,3072\ns_add_u32 m0, s48, 36864\ns_add_u32 s46, s45, 0x10000\nD0 2,20,46,0\nD0 3,20,46,1024\nD0 4,20,46,2048\n"
"D0 5,20,46,3072\ns_add_u32 m0, s47, 32768\ns_add_u32 s44, s44, 0x40000\nD0 6,24,44,0\nD0 7,24,44,1024\nD0 8,24,44,2048\nD0 9,24,44,3072\ns_getpc_b64 s[0:1]\n.Lpc:\n"
"s_sub_u32 s40, s0, .Lpc-.Lcode0\ns_subb_u32 s41, s1, 0\ns_mov_b32 s42, .Lcode1-.Lcode0\ns_mov_b32 s43, 0x20000\ns_lshl2_add_u32 s2, s74, s11\ns_lshl_b32 s2, s2, 7\n"
"v_lshlrev_b32 v54, 14, v0\nbuffer_load_dword v31, v54, s[40:43], s2 offen\nv_and_b32 v48, 15, v0\nv_lshrrev_b32 v49, 4, v0\nv_lshrrev_b32 v50, 1, v48\nv_xor_b32 v50, v50, v49\n"
"v_lshlrev_b32 v50, 4, v50\nv_lshl_or_b32 v50, v48, 7, v50\ns_lshr_b32 s2, s11, 1\ns_lshl_b32 s2, s2, 12\nv_add_u32 v10, s2, v50\nv_xor_b32 v11, 64, v10\nv_lshrrev_b32 v50, 2, v48\n"
"v_lshl_add_u32 v51, v49, 3, v50\nv_and_b32 v52, 1, v49\nv_lshl_or_b32 v50, v52, 2, v50\nv_lshlrev_b32 v50, 5, v50\nv_lshl_or_b32 v50, v51, 9, v50\nv_and_b32 v52, 3, v0\n"
"v_lshl_or_b32 v50, v52, 3, v50\ns_and_b32 s2, s11, 1\ns_lshl_b32 s2, s2, 8\ns_add_u32 s2, s2, 0x10000\nv_add_u32 v12, s2, v50\nv_xor_b32 v13, 32, v12\nv_xor_b32 v14, 64, v12\n"
"v_xor_b32 v15, 96, v12\nv_xor_b32 v16, 128, v12\nv_xor_b32 v17, 160, v12\nv_xor_b32 v18, 192, v12\nv_xor_b32 v19, 224, v12\nv_mov_b32 v20, -1\nv_mov_b32 v21, -1\n"
"v_mov_b32 v22, -1\nv_mov_b32 v23, -1\nWL 0\ns_mul_i32 s15, s15, 1664525\ns_add_u32 s15, s15, 1013904223\nv_mov_b32 v25, s15\ns_lshl4_add_u32 s2, s14, s12\n"
"s_lshl3_add_u32 s2, s2, s13\ns_lshl2_add_u32 s72, s2, s11\ns_mul_i32 s2, s72, 33792\ns_add_u32 s49, s2, 0x3500000\ns_and_b32 s2, s72, 511\ns_mul_i32 s56, s2, 17408\n"
"s_add_u32 s56, s56, 0x2300000\ns_xor_b32 s2, s2, 1\ns_mul_i32 s53, s2, 17408\ns_add_u32 s53, s53, 0x2302000\ns_mov_b32 s57, 0x3db8aa3b\ns_mov_b32 s90, 0x40000000\n"
"s_and_b32 s59, s11, 1\ns_lshl_b32 s59, s59, 13\nv_lshrrev_b32 v24, 2, v0\nv_lshlrev_b32 v24, 13, v24\nv_and_b32 v26, 3, v0\nv_lshl_or_b32 v24, v26, 7, v24\n"
"v_lshlrev_b32 v26, 11, v0\ns_lshr_b32 s99, s13, 1\ns_xor_b32 s2, s14, 1\ns_lshl2_add_u32 s99, s2, s99\ns_and_b32 s91, s74, 3\ns_mov_b32 s98, 0\ns_lshl_b32 s96, s11, 17\n"
"s_lshl_b32 s2, s12, 9\ns_add_u32 s96, s96, s2\ns_add_u32 s96, s96, 0\nWV 21\ns_barrier\nTa 128,12,0\nTa 130,12,2048\nTa 132,13,0\nTa 134,13,2048\nTa 136,14,0\nTa 138,14,2048\n"
"Ta 140,15,0\nTa 142,15,2048\nTa 144,16,0\nTa 146,16,2048\nTa 148,17,0\nTa 150,17,2048\nTa 152,18,0\nTa 154,18,2048\nTa 156,19,0\nTa 158,19,2048\nRa 160,10,0\nRa 164,10,2048\n"
"Ra 168,10,8192\nRa 172,10,10240\nRa 176,10,16384\nRa 180,10,18432\nRa 184,10,24576\nRa 188,10,26624\ns_waitcnt vmcnt(17) lgkmcnt(0)\ns_barrier\ns_bitcmp1_b32 s13, 0\n"
"s_cbranch_scc1 .V14\nZvaa 64,128,160\nTa 192,12,16384\nZvaa 96,128,164\nZvaa 128,128,168\nTa 194,12,18432\nZvaa 160,128,172\ns_add_u32 m0, s47, 49152\ns_add_u32 s44, s44, 0x40000\n"
"Zaaa 0,128,176\nTa 196,13,16384\nZaaa 32,128,180\nD0 6,24,44,0\nZaaa 64,128,184\nTa 198,13,18432\nZaaa 96,128,188\nZaaa 100,132,188\nTa 200,14,16384\nZaaa 68,132,184\n"
"Zaaa 36,132,180\nTa 202,14,18432\nZaaa 4,132,176\nZvaa 164,132,172\nTa 204,15,16384\nZvaa 132,132,168\nD0 7,24,44,1024\nZvaa 100,132,164\nTa 206,15,18432\nZvaa 68,132,160\n"
"Zvaa 72,136,160\nTa 208,16,16384\nZvaa 104,136,164\nZvaa 136,136,168\nTa 210,16,18432\nZvaa 168,136,172\nZaaa 8,136,176\nTa 212,17,16384\nZaaa 40,136,180\nD0 8,24,44,2048\n"
"Zaaa 72,136,184\nTa 214,17,18432\nZaaa 104,136,188\nZaaa 108,140,188\nTa 216,18,16384\nZaaa 76,140,184\nZaaa 44,140,180\nTa 218,18,18432\nZaaa 12,140,176\nZvaa 172,140,172\n"
"Ta 220,19,16384\nZvaa 140,140,168\nD0 9,24,44,3072\nZvaa 108,140,164\nTa 222,19,18432\nZvaa 76,140,160\nZvaa 80,144,160\nRa 224,11,0\nZvaa 112,144,164\nZvaa 144,144,168\n"
"Ra 228,11,2048\nZvaa 176,144,172\ns_add_u32 m0, s47, 0\ns_add_u32 s44, s44, 0x40000\nZaaa 16,144,176\nRa 232,11,8192\nZaaa 48,144,180\nD0 6,24,44,0\nZaaa 80,144,184\n"
"Ra 236,11,10240\nZaaa 112,144,188\nZaaa 116,148,188\nRa 240,11,16384\nZaaa 84,148,184\nZaaa 52,148,180\nRa 244,11,18432\nZaaa 20,148,176\nZvaa 180,148,172\nRa 248,11,24576\n"
"Zvaa 148,148,168\nD0 7,24,44,1024\nZvaa 116,148,164\nRa 252,11,26624\nZvaa 84,148,160\nZvaa 88,152,160\nZvaa 120,152,164\nZvaa 152,152,168\nZvaa 184,152,172\nZaaa 24,152,176\n"
"Zaaa 56,152,180\nD0 8,24,44,2048\nZaaa 88,152,184\nZaaa 120,152,188\nZaaa 124,156,188\nZaaa 92,156,184\nZaaa 60,156,180\nZaaa 28,156,176\nZvaa 188,156,172\nZvaa 156,156,168\n"
"D0 9,24,44,3072\nZvaa 124,156,164\nZvaa 92,156,160\ns_waitcnt vmcnt(9) lgkmcnt(0)\ns_barrier\nMvaa 64,192,224\nTa 128,12,32768\nMvaa 96,192,228\nv_lshlrev_b32 v48, 16, v32\n"
"v_lshlrev_b32 v49, 16, v36\nMvaa 128,192,232\nTa 130,12,34816\nv_lshlrev_b32 v50, 16, v40\nMvaa 160,192,236\ns_add_u32 m0, s48, 0\ns_add_u32 s45, s45, 128\nMaaa 0,192,240\n"
"Ta 132,13,32768\nv_lshlrev_b32 v51, 16, v44\nMaaa 32,192,244\nD0 2,20,45,0\nMU 52,48,50\nMaaa 64,192,248\nTa 134,13,34816\nMU 53,49,50\nMaaa 96,192,252\nCV 52,52,53\n"
"v_lshlrev_b32 v54, 16, v52\nMaaa 100,196,252\nTa 136,14,32768\nv_and_b32 v55, 0xffff0000, v52\nMaaa 68,196,248\nMU 56,48,51\nMU 57,49,51\nMaaa 36,196,244\nTa 138,14,34816\n"
"v_and_b32 v48, 0xffff0000, v32\nMaaa 4,196,240\nv_and_b32 v49, 0xffff0000, v36\nv_and_b32 v50, 0xffff0000, v40\nMvaa 164,196,236\nTa 140,15,32768\nv_and_b32 v51, 0xffff0000, v44\n"
"Mvaa 132,196,232\nD0 3,20,45,1024\nMU 52,48,50\nMvaa 100,196,228\nTa 142,15,34816\nMU 53,49,50\nMvaa 68,196,224\nCV 52,52,53\nv_and_b32 v53, 0xffff0000, v52\nMvaa 72,200,224\n"
"Ta 144,16,32768\nv_lshlrev_b32 v52, 16, v52\nMvaa 104,200,228\nFM 56,48,51\nFM 57,49,51\nMvaa 136,200,232\nTa 146,16,34816\nAD 54,54,52\nMvaa 168,200,236\nAD 55,55,53\n"
"v_lshlrev_b32 v48, 16, v33\nMaaa 8,200,240\nTa 148,17,32768\nv_lshlrev_b32 v49, 16, v37\nMaaa 40,200,244\nD0 4,20,45,2048\nv_lshlrev_b32 v50, 16, v41\nMaaa 72,200,248\n"
"Ta 150,17,34816\nv_lshlrev_b32 v51, 16, v45\nMaaa 104,200,252\nMU 52,48,50\nMU 53,49,50\nMaaa 108,204,252\nTa 152,18,32768\nCV 52,52,53\nMaaa 76,204,248\n"
"v_and_b32 v53, 0xffff0000, v52\nv_lshlrev_b32 v52, 16, v52\nMaaa 44,204,244\nTa 154,18,34816\nFM 56,48,51\nMaaa 12,204,240\nFM 57,49,51\nAD 54,54,52\nMvaa 172,204,236\n"
"Ta 156,19,32768\nAD 55,55,53\nMvaa 140,204,232\nD0 5,20,45,3072\nv_and_b32 v48, 0xffff0000, v33\nMvaa 108,204,228\nTa 158,19,34816\nv_and_b32 v49, 0xffff0000, v37\n"
"Mvaa 76,204,224\nv_and_b32 v50, 0xffff0000, v41\nv_and_b32 v51, 0xffff0000, v45\nMvaa 80,208,224\nRa 160,10,32768\nMU 52,48,50\nMvaa 112,208,228\nMU 53,49,50\nCV 52,52,53\n"
"Mvaa 144,208,232\nRa 164,10,34816\nv_and_b32 v53, 0xffff0000, v52\nMvaa 176,208,236\ns_add_u32 m0, s48, 4096\ns_add_u32 s46, s45, 0x10000\nMaaa 16,208,240\nRa 168,10,40960\n"
"v_lshlrev_b32 v52, 16, v52\nMaaa 48,208,244\nD0 2,20,46,0\nFM 56,48,51\nMaaa 80,208,248\nRa 172,10,43008\nFM 57,49,51\nMaaa 112,208,252\nAD 54,54,52\nAD 55,55,53\n"
"Maaa 116,212,252\nRa 176,10,49152\nv_lshlrev_b32 v48, 16, v34\nMaaa 84,212,248\nv_lshlrev_b32 v49, 16, v38\nv_lshlrev_b32 v50, 16, v42\nMaaa 52,212,244\nRa 180,10,51200\n"
"v_lshlrev_b32 v51, 16, v46\nMaaa 20,212,240\nMU 52,48,50\nMU 53,49,50\nMvaa 180,212,236\nRa 184,10,57344\nCV 52,52,53\nMvaa 148,212,232\nD0 3,20,46,1024\n"
"v_and_b32 v53, 0xffff0000, v52\nMvaa 116,212,228\nRa 188,10,59392\nv_lshlrev_b32 v52, 16, v52\nMvaa 84,212,224\nFM 56,48,51\nFM 57,49,51\nMvaa 88,216,224\nAD 54,54,52\n"
"AD 55,55,53\nMvaa 120,216,228\nv_and_b32 v48, 0xffff0000, v34\nv_and_b32 v49, 0xffff0000, v38\nMvaa 152,216,232\nv_and_b32 v50, 0xffff0000, v42\nv_and_b32 v51, 0xffff0000, v46\n"
"Mvaa 184,216,236\nMU 52,48,50\nMU 53,49,50\nMaaa 24,216,240\nCV 52,52,53\nv_and_b32 v53, 0xffff0000, v52\nMaaa 56,216,244\nD0 4,20,46,2048\nv_lshlrev_b32 v52, 16, v52\n"
"Maaa 88,216,248\nFM 56,48,51\nFM 57,49,51\nMaaa 120,216,252\nAD 54,54,52\nAD 55,55,53\nMaaa 124,220,252\nv_lshlrev_b32 v48, 16, v35\nv_lshlrev_b32 v49, 16, v39\nMaaa 92,220,248\n"
"v_lshlrev_b32 v50, 16, v43\nv_lshlrev_b32 v51, 16, v47\nMaaa 60,220,244\nMU 52,48,50\nMU 53,49,50\nMaaa 28,220,240\nCV 52,52,53\nv_and_b32 v53, 0xffff0000, v52\nMvaa 188,220,236\n"
"v_lshlrev_b32 v52, 16, v52\nFM 56,48,51\nMvaa 156,220,232\nD0 5,20,46,3072\nFM 57,49,51\nMvaa 124,220,228\nAD 54,54,52\nAD 55,55,53\nMvaa 92,220,224\n"
"s_waitcnt vmcnt(12) lgkmcnt(0)\ns_barrier\nMvaa 64,128,160\nTa 192,12,49152\nv_and_b32 v48, 0xffff0000, v35\nMvaa 96,128,164\nv_and_b32 v49, 0xffff0000, v39\n"
"v_and_b32 v50, 0xffff0000, v43\nMvaa 128,128,168\nTa 194,12,51200\nv_and_b32 v51, 0xffff0000, v47\nMvaa 160,128,172\ns_add_u32 m0, s47, 16384\ns_add_u32 s44, s44, 0x40000\n"
"Maaa 0,128,176\nTa 196,13,49152\nMU 52,48,50\nMaaa 32,128,180\nD0 6,24,44,0\nMU 53,49,50\nMaaa 64,128,184\nTa 198,13,51200\nCV 52,52,53\nMaaa 96,128,188\n"
"v_and_b32 v53, 0xffff0000, v52\nv_lshlrev_b32 v52, 16, v52\nMaaa 100,132,188\nTa 200,14,49152\nFM 56,48,51\nMaaa 68,132,184\nFM 57,49,51\nAD 54,54,52\nMaaa 36,132,180\n"
"Ta 202,14,51200\nAD 55,55,53\nMaaa 4,132,176\ns_nop 0\nv_permlane32_swap_b32 v54, v55\nv_permlane32_swap_b32 v56, v57\nMvaa 164,132,172\nTa 204,15,49152\nAD 54,54,55\n"
"Mvaa 132,132,168\nD0 7,24,44,1024\nAD 56,56,57\nMvaa 100,132,164\nTa 206,15,51200\nMvaa 68,132,160\nv_permlane16_swap_b32 v54, v56\nAD 54,54,56\nMvaa 72,136,160\nTa 208,16,49152\n"
"Mvaa 104,136,164\nv_add_f32_dpp v54, v54, v54 row_ror:8 row_mask:0xf bank_mask:0xf\nMvaa 136,136,168\nTa 210,16,51200\nMvaa 168,136,172\n"
"v_add_f32_dpp v54, v54, v54 row_ror:4 row_mask:0xf bank_mask:0xf\nMaaa 8,136,176\nTa 212,17,49152\nMaaa 40,136,180\nD0 8,24,44,2048\n"
"v_add_f32_dpp v54, v54, v54 row_ror:2 row_mask:0xf bank_mask:0xf\nMaaa 72,136,184\nTa 214,17,51200\nMaaa 104,136,188\n"
"v_add_f32_dpp v54, v54, v54 row_ror:1 row_mask:0xf bank_mask:0xf\nCV 54,54,54\nMaaa 108,140,188\nTa 216,18,49152\nv_lshlrev_b32 v54, 16, v54\nMaaa 76,140,184\n"
"v_mul_f32 v55, 0xbfb8aa3b, v54\nMaaa 44,140,180\nTa 218,18,51200\nMaaa 12,140,176\nEX 55,55\nMvaa 172,140,172\nTa 220,19,49152\nv_add_f32 v56, 0, v54\nMvaa 140,140,168\n"
"D0 9,24,44,3072\nv_add_f32 v55, 1.0, v55\nMvaa 108,140,164\nTa 222,19,51200\nMvaa 76,140,160\nv_rcp_f32 v55, v55\nMvaa 80,144,160\nRa 224,11,32768\nv_ashrrev_i32 v57, 31, v56\n"
"Mvaa 112,144,164\nCV 55,55,55\nv_lshlrev_b32 v55, 16, v55\nMvaa 144,144,168\nRa 228,11,34816\nv_or_b32 v57, 0x80000000, v57\nMvaa 176,144,172\ns_add_u32 m0, s47, 32768\n"
"s_add_u32 s44, s44, 0x40000\nMaaa 16,144,176\nRa 232,11,40960\nv_xor_b32 v56, v56, v57\nMaaa 48,144,180\nD0 6,24,44,0\nv_and_b32 v56, 0xffff0000, v56\nMaaa 80,144,184\n"
"Ra 236,11,43008\ns_lshl2_add_u32 s2, s10, s11\nMaaa 112,144,188\nv_lshrrev_b32 v48, 5, v0\nv_lshl_add_u32 v48, s2, 1, v48\nMaaa 116,148,188\nRa 240,11,49152\n"
"v_sub_u32 v49, 0xffff, v48\nMaaa 84,148,184\nv_or_b32 v56, v56, v49\nv_mov_b32 v57, s15\nMaaa 52,148,180\nRa 244,11,51200\nv_and_b32 v49, 31, v0\nMaaa 20,148,176\n"
"v_mov_b32 v52, 0x40000000\nv_cmp_eq_u32 vcc, 16, v49\nv_lshlrev_b32 v50, 3, v48\nv_lshlrev_b32 v51, 9, v49\nv_cndmask_b32 v50, v52, v50, vcc\nv_cndmask_b32 v55, v55, v54, vcc\n"
"Mvaa 180,148,172\nRa 248,11,57344\nv_lshl_add_u32 v51, v48, 2, v51\nMvaa 148,148,168\nD0 7,24,44,1024\nv_and_b32 v49, 15, v0\nMvaa 116,148,164\nRa 252,11,59392\n"
"v_cmp_eq_u32 vcc, 0, v49\ns_mov_b32 s2, 0x5c40000\ns_mov_b32 s3, 0x5c44000\nv_cndmask_b32 v51, v52, v51, vcc\nbuffer_store_dwordx2 v[56:57], v50, s[16:19], s3 offen sc0 sc1\n"
"buffer_store_dword v55, v51, s[16:19], s2 offen sc0 sc1\nMvaa 84,148,160\nMvaa 88,152,160\nMvaa 120,152,164\nMvaa 152,152,168\nMvaa 184,152,172\nMaaa 24,152,176\nMaaa 56,152,180\n"
"D0 8,24,44,2048\nMaaa 88,152,184\nMaaa 120,152,188\nMaaa 124,156,188\nMaaa 92,156,184\nMaaa 60,156,180\nMaaa 28,156,176\nMvaa 188,156,172\nMvaa 156,156,168\nD0 9,24,44,3072\n"
"Mvaa 124,156,164\nMvaa 92,156,160\ns_waitcnt vmcnt(10) lgkmcnt(0)\ns_barrier\nMvaa 64,192,224\nTa 128,12,0\nMvaa 96,192,228\nMvaa 128,192,232\nTa 130,12,2048\nMvaa 160,192,236\n"
"s_add_u32 m0, s48, 32768\ns_add_u32 s45, s45, 128\nMaaa 0,192,240\nTa 132,13,0\nMaaa 32,192,244\nD0 2,20,45,0\nMaaa 64,192,248\nTa 134,13,2048\nMaaa 96,192,252\nMaaa 100,196,252\n"
"Ta 136,14,0\nMaaa 68,196,248\nMaaa 36,196,244\nTa 138,14,2048\nMaaa 4,196,240\nMvaa 164,196,236\nTa 140,15,0\nMvaa 132,196,232\nD0 3,20,45,1024\nMvaa 100,196,228\nTa 142,15,2048\n"
"Mvaa 68,196,224\nMvaa 72,200,224\nTa 144,16,0\nMvaa 104,200,228\nMvaa 136,200,232\nTa 146,16,2048\nMvaa 168,200,236\nMaaa 8,200,240\nTa 148,17,0\nMaaa 40,200,244\nD0 4,20,45,2048\n"
"Maaa 72,200,248\nTa 150,17,2048\nMaaa 104,200,252\nMaaa 108,204,252\nTa 152,18,0\nMaaa 76,204,248\nMaaa 44,204,244\nTa 154,18,2048\nMaaa 12,204,240\nMvaa 172,204,236\nTa 156,19,0\n"
"Mvaa 140,204,232\nD0 5,20,45,3072\nMvaa 108,204,228\nTa 158,19,2048\nMvaa 76,204,224\nMvaa 80,208,224\nRa 160,10,0\nMvaa 112,208,228\nMvaa 144,208,232\nRa 164,10,2048\n"
"Mvaa 176,208,236\ns_add_u32 m0, s48, 36864\ns_add_u32 s46, s45, 0x10000\nMaaa 16,208,240\nRa 168,10,8192\nMaaa 48,208,244\nD0 2,20,46,0\nMaaa 80,208,248\nRa 172,10,10240\n"
"Maaa 112,208,252\nMaaa 116,212,252\nRa 176,10,16384\nMaaa 84,212,248\nMaaa 52,212,244\nRa 180,10,18432\nMaaa 20,212,240\nMvaa 180,212,236\nRa 184,10,24576\nMvaa 148,212,232\n"
"D0 3,20,46,1024\nMvaa 116,212,228\nRa 188,10,26624\nMvaa 84,212,224\nMvaa 88,216,224\nMvaa 120,216,228\nMvaa 152,216,232\nMvaa 184,216,236\nMaaa 24,216,240\nMaaa 56,216,244\n"
"D0 4,20,46,2048\nMaaa 88,216,248\nMaaa 120,216,252\nMaaa 124,220,252\nMaaa 92,220,248\nMaaa 60,220,244\nMaaa 28,220,240\nMvaa 188,220,236\nMvaa 156,220,232\nD0 5,20,46,3072\n"
"Mvaa 124,220,228\nMvaa 92,220,224\ns_waitcnt vmcnt(14) lgkmcnt(0)\ns_barrier\ns_branch .MK2\n.V14:\nZvaa 64,160,128\nTa 192,12,16384\nZvaa 68,164,128\nZvaa 128,168,128\n"
"Ta 194,12,18432\nZvaa 132,172,128\ns_add_u32 m0, s47, 49152\ns_add_u32 s44, s44, 0x40000\nZaaa 0,176,128\nTa 196,13,16384\nZaaa 4,180,128\nD0 6,24,44,0\nZaaa 64,184,128\n"
"Ta 198,13,18432\nZaaa 68,188,128\nZaaa 76,188,132\nTa 200,14,16384\nZaaa 72,184,132\nZaaa 12,180,132\nTa 202,14,18432\nZaaa 8,176,132\nZvaa 140,172,132\nTa 204,15,16384\n"
"Zvaa 136,168,132\nD0 7,24,44,1024\nZvaa 76,164,132\nTa 206,15,18432\nZvaa 72,160,132\nZvaa 80,160,136\nTa 208,16,16384\nZvaa 84,164,136\nZvaa 144,168,136\nTa 210,16,18432\n"
"Zvaa 148,172,136\nZaaa 16,176,136\nTa 212,17,16384\nZaaa 20,180,136\nD0 8,24,44,2048\nZaaa 80,184,136\nTa 214,17,18432\nZaaa 84,188,136\nZaaa 92,188,140\nTa 216,18,16384\n"
"Zaaa 88,184,140\nZaaa 28,180,140\nTa 218,18,18432\nZaaa 24,176,140\nZvaa 156,172,140\nTa 220,19,16384\nZvaa 152,168,140\nD0 9,24,44,3072\nZvaa 92,164,140\nTa 222,19,18432\n"
"Zvaa 88,160,140\nZvaa 96,160,144\nRa 224,11,0\nZvaa 100,164,144\nZvaa 160,168,144\nRa 228,11,2048\nZvaa 164,172,144\ns_add_u32 m0, s47, 0\ns_add_u32 s44, s44, 0x40000\n"
"Zaaa 32,176,144\nRa 232,11,8192\nZaaa 36,180,144\nD0 6,24,44,0\nZaaa 96,184,144\nRa 236,11,10240\nZaaa 100,188,144\nZaaa 108,188,148\nRa 240,11,16384\nZaaa 104,184,148\n"
"Zaaa 44,180,148\nRa 244,11,18432\nZaaa 40,176,148\nZvaa 172,172,148\nRa 248,11,24576\nZvaa 168,168,148\nD0 7,24,44,1024\nZvaa 108,164,148\nRa 252,11,26624\nZvaa 104,160,148\n"
"Zvaa 112,160,152\nZvaa 116,164,152\nZvaa 176,168,152\nZvaa 180,172,152\nZaaa 48,176,152\nZaaa 52,180,152\nD0 8,24,44,2048\nZaaa 112,184,152\nZaaa 116,188,152\nZaaa 124,188,156\n"
"Zaaa 120,184,156\nZaaa 60,180,156\nZaaa 56,176,156\nZvaa 188,172,156\nZvaa 184,168,156\nD0 9,24,44,3072\nZvaa 124,164,156\nZvaa 120,160,156\ns_waitcnt vmcnt(9) lgkmcnt(0)\n"
"s_barrier\nMvaa 64,224,192\nTa 128,12,32768\nMvaa 68,228,192\nv_lshlrev_b32 v48, 16, v32\nv_lshlrev_b32 v49, 16, v36\nMvaa 128,232,192\nTa 130,12,34816\n"
"v_lshlrev_b32 v50, 16, v40\nMvaa 132,236,192\ns_add_u32 m0, s48, 0\ns_add_u32 s45, s45, 128\nMaaa 0,240,192\nTa 132,13,32768\nv_lshlrev_b32 v51, 16, v44\nMaaa 4,244,192\n"
"D0 2,20,45,0\nMU 52,48,50\nMaaa 64,248,192\nTa 134,13,34816\nMU 53,49,50\nMaaa 68,252,192\nCV 52,52,53\nv_lshlrev_b32 v54, 16, v52\nMaaa 76,252,196\nTa 136,14,32768\n"
"v_and_b32 v55, 0xffff0000, v52\nMaaa 72,248,196\nMU 56,48,51\nMU 57,49,51\nMaaa 12,244,196\nTa 138,14,34816\nv_and_b32 v48, 0xffff0000, v32\nMaaa 8,240,196\n"
"v_and_b32 v49, 0xffff0000, v36\nv_and_b32 v50, 0xffff0000, v40\nMvaa 140,236,196\nTa 140,15,32768\nv_and_b32 v51, 0xffff0000, v44\nMvaa 136,232,196\nD0 3,20,45,1024\nMU 52,48,50\n"
"Mvaa 76,228,196\nTa 142,15,34816\nMU 53,49,50\nMvaa 72,224,196\nCV 52,52,53\nv_and_b32 v53, 0xffff0000, v52\nMvaa 80,224,200\nTa 144,16,32768\nv_lshlrev_b32 v52, 16, v52\n"
"Mvaa 84,228,200\nFM 56,48,51\nFM 57,49,51\nMvaa 144,232,200\nTa 146,16,34816\nAD 54,54,52\nMvaa 148,236,200\nAD 55,55,53\nv_lshlrev_b32 v48, 16, v33\nMaaa 16,240,200\n"
"Ta 148,17,32768\nv_lshlrev_b32 v49, 16, v37\nMaaa 20,244,200\nD0 4,20,45,2048\nv_lshlrev_b32 v50, 16, v41\nMaaa 80,248,200\nTa 150,17,34816\nv_lshlrev_b32 v51, 16, v45\n"
"Maaa 84,252,200\nMU 52,48,50\nMU 53,49,50\nMaaa 92,252,204\nTa 152,18,32768\nCV 52,52,53\nMaaa 88,248,204\nv_and_b32 v53, 0xffff0000, v52\nv_lshlrev_b32 v52, 16, v52\n"
"Maaa 28,244,204\nTa 154,18,34816\nFM 56,48,51\nMaaa 24,240,204\nFM 57,49,51\nAD 54,54,52\nMvaa 156,236,204\nTa 156,19,32768\nAD 55,55,53\nMvaa 152,232,204\nD0 5,20,45,3072\n"
"v_and_b32 v48, 0xffff0000, v33\nMvaa 92,228,204\nTa 158,19,34816\nv_and_b32 v49, 0xffff0000, v37\nMvaa 88,224,204\nv_and_b32 v50, 0xffff0000, v41\nv_and_b32 v51, 0xffff0000, v45\n"
"Mvaa 96,224,208\nRa 160,10,32768\nMU 52,48,50\nMvaa 100,228,208\nMU 53,49,50\nCV 52,52,53\nMvaa 160,232,208\nRa 164,10,34816\nv_and_b32 v53, 0xffff0000, v52\nMvaa 164,236,208\n"
"s_add_u32 m0, s48, 4096\ns_add_u32 s46, s45, 0x10000\nMaaa 32,240,208\nRa 168,10,40960\nv_lshlrev_b32 v52, 16, v52\nMaaa 36,244,208\nD0 2,20,46,0\nFM 56,48,51\nMaaa 96,248,208\n"
"Ra 172,10,43008\nFM 57,49,51\nMaaa 100,252,208\nAD 54,54,52\nAD 55,55,53\nMaaa 108,252,212\nRa 176,10,49152\nv_lshlrev_b32 v48, 16, v34\nMaaa 104,248,212\n"
"v_lshlrev_b32 v49, 16, v38\nv_lshlrev_b32 v50, 16, v42\nMaaa 44,244,212\nRa 180,10,51200\nv_lshlrev_b32 v51, 16, v46\nMaaa 40,240,212\nMU 52,48,50\nMU 53,49,50\nMvaa 172,236,212\n"
"Ra 184,10,57344\nCV 52,52,53\nMvaa 168,232,212\nD0 3,20,46,1024\nv_and_b32 v53, 0xffff0000, v52\nMvaa 108,228,212\nRa 188,10,59392\nv_lshlrev_b32 v52, 16, v52\nMvaa 104,224,212\n"
"FM 56,48,51\nFM 57,49,51\nMvaa 112,224,216\nAD 54,54,52\nAD 55,55,53\nMvaa 116,228,216\nv_and_b32 v48, 0xffff0000, v34\nv_and_b32 v49, 0xffff0000, v38\nMvaa 176,232,216\n"
"v_and_b32 v50, 0xffff0000, v42\nv_and_b32 v51, 0xffff0000, v46\nMvaa 180,236,216\nMU 52,48,50\nMU 53,49,50\nMaaa 48,240,216\nCV 52,52,53\nv_and_b32 v53, 0xffff0000, v52\n"
"Maaa 52,244,216\nD0 4,20,46,2048\nv_lshlrev_b32 v52, 16, v52\nMaaa 112,248,216\nFM 56,48,51\nFM 57,49,51\nMaaa 116,252,216\nAD 54,54,52\nAD 55,55,53\nMaaa 124,252,220\n"
"v_lshlrev_b32 v48, 16, v35\nv_lshlrev_b32 v49, 16, v39\nMaaa 120,248,220\nv_lshlrev_b32 v50, 16, v43\nv_lshlrev_b32 v51, 16, v47\nMaaa 60,244,220\nMU 52,48,50\nMU 53,49,50\n"
"Maaa 56,240,220\nCV 52,52,53\nv_and_b32 v53, 0xffff0000, v52\nMvaa 188,236,220\nv_lshlrev_b32 v52, 16, v52\nFM 56,48,51\nMvaa 184,232,220\nD0 5,20,46,3072\nFM 57,49,51\n"
"Mvaa 124,228,220\nAD 54,54,52\nAD 55,55,53\nMvaa 120,224,220\ns_waitcnt vmcnt(12) lgkmcnt(0)\ns_barrier\nMvaa 64,160,128\nTa 192,12,49152\nv_and_b32 v48, 0xffff0000, v35\n"
"Mvaa 68,164,128\nv_and_b32 v49, 0xffff0000, v39\nv_and_b32 v50, 0xffff0000, v43\nMvaa 128,168,128\nTa 194,12,51200\nv_and_b32 v51, 0xffff0000, v47\nMvaa 132,172,128\n"
"s_add_u32 m0, s47, 16384\ns_add_u32 s44, s44, 0x40000\nMaaa 0,176,128\nTa 196,13,49152\nMU 52,48,50\nMaaa 4,180,128\nD0 6,24,44,0\nMU 53,49,50\nMaaa 64,184,128\nTa 198,13,51200\n"
"CV 52,52,53\nMaaa 68,188,128\nv_and_b32 v53, 0xffff0000, v52\nv_lshlrev_b32 v52, 16, v52\nMaaa 76,188,132\nTa 200,14,49152\nFM 56,48,51\nMaaa 72,184,132\nFM 57,49,51\nAD 54,54,52\n"
"Maaa 12,180,132\nTa 202,14,51200\nAD 55,55,53\nMaaa 8,176,132\ns_nop 0\nv_permlane32_swap_b32 v54, v55\nv_permlane32_swap_b32 v56, v57\nMvaa 140,172,132\nTa 204,15,49152\n"
"AD 54,54,55\nMvaa 136,168,132\nD0 7,24,44,1024\nAD 56,56,57\nMvaa 76,164,132\nTa 206,15,51200\nMvaa 72,160,132\nv_permlane16_swap_b32 v54, v56\nAD 54,54,56\nMvaa 80,160,136\n"
"Ta 208,16,49152\nMvaa 84,164,136\nv_add_f32_dpp v54, v54, v54 row_ror:8 row_mask:0xf bank_mask:0xf\nMvaa 144,168,136\nTa 210,16,51200\nMvaa 148,172,136\n"
"v_add_f32_dpp v54, v54, v54 row_ror:4 row_mask:0xf bank_mask:0xf\nMaaa 16,176,136\nTa 212,17,49152\nMaaa 20,180,136\nD0 8,24,44,2048\n"
"v_add_f32_dpp v54, v54, v54 row_ror:2 row_mask:0xf bank_mask:0xf\nMaaa 80,184,136\nTa 214,17,51200\nMaaa 84,188,136\n"
"v_add_f32_dpp v54, v54, v54 row_ror:1 row_mask:0xf bank_mask:0xf\nCV 54,54,54\nMaaa 92,188,140\nTa 216,18,49152\nv_lshlrev_b32 v54, 16, v54\nMaaa 88,184,140\n"
"v_mul_f32 v55, 0xbfb8aa3b, v54\nMaaa 28,180,140\nTa 218,18,51200\nMaaa 24,176,140\nEX 55,55\nMvaa 156,172,140\nTa 220,19,49152\nv_add_f32 v56, 0, v54\nMvaa 152,168,140\n"
"D0 9,24,44,3072\nv_add_f32 v55, 1.0, v55\nMvaa 92,164,140\nTa 222,19,51200\nMvaa 88,160,140\nv_rcp_f32 v55, v55\nMvaa 96,160,144\nRa 224,11,32768\nv_ashrrev_i32 v57, 31, v56\n"
"Mvaa 100,164,144\nCV 55,55,55\nv_lshlrev_b32 v55, 16, v55\nMvaa 160,168,144\nRa 228,11,34816\nv_or_b32 v57, 0x80000000, v57\nMvaa 164,172,144\ns_add_u32 m0, s47, 32768\n"
"s_add_u32 s44, s44, 0x40000\nMaaa 32,176,144\nRa 232,11,40960\nv_xor_b32 v56, v56, v57\nMaaa 36,180,144\nD0 6,24,44,0\nv_and_b32 v56, 0xffff0000, v56\nMaaa 96,184,144\n"
"Ra 236,11,43008\ns_lshl2_add_u32 s2, s10, s11\nMaaa 100,188,144\nv_lshrrev_b32 v48, 5, v0\nv_lshl_add_u32 v48, s2, 1, v48\nMaaa 108,188,148\nRa 240,11,49152\n"
"v_sub_u32 v49, 0xffff, v48\nMaaa 104,184,148\nv_or_b32 v56, v56, v49\nv_mov_b32 v57, s15\nMaaa 44,180,148\nRa 244,11,51200\nv_and_b32 v49, 31, v0\nMaaa 40,176,148\n"
"v_mov_b32 v52, 0x40000000\nv_cmp_eq_u32 vcc, 16, v49\nv_lshlrev_b32 v50, 3, v48\nv_lshlrev_b32 v51, 9, v49\nv_cndmask_b32 v50, v52, v50, vcc\nv_cndmask_b32 v55, v55, v54, vcc\n"
"Mvaa 172,172,148\nRa 248,11,57344\nv_lshl_add_u32 v51, v48, 2, v51\nMvaa 168,168,148\nD0 7,24,44,1024\nv_and_b32 v49, 15, v0\nMvaa 108,164,148\nRa 252,11,59392\n"
"v_cmp_eq_u32 vcc, 0, v49\ns_mov_b32 s2, 0x5c40000\ns_mov_b32 s3, 0x5c44000\nv_cndmask_b32 v51, v52, v51, vcc\nbuffer_store_dwordx2 v[56:57], v50, s[16:19], s3 offen sc0 sc1\n"
"buffer_store_dword v55, v51, s[16:19], s2 offen sc0 sc1\nMvaa 104,160,148\nMvaa 112,160,152\nMvaa 116,164,152\nMvaa 176,168,152\nMvaa 180,172,152\nMaaa 48,176,152\n"
"Maaa 52,180,152\nD0 8,24,44,2048\nMaaa 112,184,152\nMaaa 116,188,152\nMaaa 124,188,156\nMaaa 120,184,156\nMaaa 60,180,156\nMaaa 56,176,156\nMvaa 188,172,156\nMvaa 184,168,156\n"
"D0 9,24,44,3072\nMvaa 124,164,156\nMvaa 120,160,156\ns_waitcnt vmcnt(10) lgkmcnt(0)\ns_barrier\nMvaa 64,224,192\nTa 128,12,0\nMvaa 68,228,192\nMvaa 128,232,192\nTa 130,12,2048\n"
"Mvaa 132,236,192\ns_add_u32 m0, s48, 32768\ns_add_u32 s45, s45, 128\nMaaa 0,240,192\nTa 132,13,0\nMaaa 4,244,192\nD0 2,20,45,0\nMaaa 64,248,192\nTa 134,13,2048\nMaaa 68,252,192\n"
"Maaa 76,252,196\nTa 136,14,0\nMaaa 72,248,196\nMaaa 12,244,196\nTa 138,14,2048\nMaaa 8,240,196\nMvaa 140,236,196\nTa 140,15,0\nMvaa 136,232,196\nD0 3,20,45,1024\nMvaa 76,228,196\n"
"Ta 142,15,2048\nMvaa 72,224,196\nMvaa 80,224,200\nTa 144,16,0\nMvaa 84,228,200\nMvaa 144,232,200\nTa 146,16,2048\nMvaa 148,236,200\nMaaa 16,240,200\nTa 148,17,0\nMaaa 20,244,200\n"
"D0 4,20,45,2048\nMaaa 80,248,200\nTa 150,17,2048\nMaaa 84,252,200\nMaaa 92,252,204\nTa 152,18,0\nMaaa 88,248,204\nMaaa 28,244,204\nTa 154,18,2048\nMaaa 24,240,204\n"
"Mvaa 156,236,204\nTa 156,19,0\nMvaa 152,232,204\nD0 5,20,45,3072\nMvaa 92,228,204\nTa 158,19,2048\nMvaa 88,224,204\nMvaa 96,224,208\nRa 160,10,0\nMvaa 100,228,208\n"
"Mvaa 160,232,208\nRa 164,10,2048\nMvaa 164,236,208\ns_add_u32 m0, s48, 36864\ns_add_u32 s46, s45, 0x10000\nMaaa 32,240,208\nRa 168,10,8192\nMaaa 36,244,208\nD0 2,20,46,0\n"
"Maaa 96,248,208\nRa 172,10,10240\nMaaa 100,252,208\nMaaa 108,252,212\nRa 176,10,16384\nMaaa 104,248,212\nMaaa 44,244,212\nRa 180,10,18432\nMaaa 40,240,212\nMvaa 172,236,212\n"
"Ra 184,10,24576\nMvaa 168,232,212\nD0 3,20,46,1024\nMvaa 108,228,212\nRa 188,10,26624\nMvaa 104,224,212\nMvaa 112,224,216\nMvaa 116,228,216\nMvaa 176,232,216\nMvaa 180,236,216\n"
"Maaa 48,240,216\nMaaa 52,244,216\nD0 4,20,46,2048\nMaaa 112,248,216\nMaaa 116,252,216\nMaaa 124,252,220\nMaaa 120,248,220\nMaaa 60,244,220\nMaaa 56,240,220\nMvaa 188,236,220\n"
"Mvaa 184,232,220\nD0 5,20,46,3072\nMvaa 124,228,220\nMvaa 120,224,220\ns_waitcnt vmcnt(14) lgkmcnt(0)\ns_barrier\ns_branch .MV3\n.R01:\ns_add_u32 m0, s47, 65536\n"
"s_add_u32 s46, s51, 0\nD0 6,32,46,0\nD0 7,32,46,1024\nD0 8,32,46,2048\nD0 9,32,46,3072\nv_lshrrev_b32 v144, 4, v0\nv_and_b32 v145, 7, v0\nv_xor_b32 v145, v145, v144\n"
"v_lshlrev_b32 v145, 4, v145\nv_lshrrev_b32 v144, 3, v0\nv_mul_u32_u24 v144, 896, v144\nv_add_u32 v136, v144, v145\nv_xor_b32 v145, 64, v136\nv_add_u32 v137, 6144, v145\n"
"v_add_u32 v138, 12288, v136\nv_add_u32 v139, 18432, v145\ns_sub_u32 s2, 15, s13\ns_lshl2_add_u32 s2, s2, s11\ns_mul_i32 s52, s2, 28672\ns_add_u32 m0, s47, -65536\n"
"s_add_u32 s46, s52, 0\nD0 136,28,46,0\nD0 137,28,46,1024\nD0 138,28,46,2048\nD0 139,28,46,3072\ns_add_u32 m0, s47, 81920\ns_add_u32 s46, s51, 262144\nD0 6,32,46,0\n"
"D0 7,32,46,1024\nD0 8,32,46,2048\nD0 9,32,46,3072\ns_add_u32 m0, s47, 0\ns_add_u32 s46, s51, 524288\nD0 6,32,46,0\nD0 7,32,46,1024\nD0 8,32,46,2048\nD0 9,32,46,3072\n"
"s_add_u32 m0, s47, -49152\ns_add_u32 s46, s52, 128\nD0 136,28,46,0\nD0 137,28,46,1024\nD0 138,28,46,2048\nD0 139,28,46,3072\ns_add_u32 m0, s47, 16384\ns_add_u32 s46, s51, 786432\n"
"D0 6,32,46,0\nD0 7,32,46,1024\nD0 8,32,46,2048\nD0 9,32,46,3072\nv_mov_b32 v116, 0x40000000\nv_cmp_gt_u32 vcc, 56, v0\ns_lshl2_add_u32 s2, s10, s11\ns_mul_i32 s2, s2, 1792\n"
"v_cndmask_b32 v117, v116, v1, vcc\ns_mov_b32 s36, s56\ns_and_b32 s37, s57, 0xffff\ns_mov_b32 s38, 0x380\ns_mov_b32 s39, 0x20000\ns_mov_b32 s40, s58\ns_and_b32 s41, s59, 0xffff\n"
"s_mov_b32 s42, 0x380\ns_mov_b32 s43, 0x20000\nbuffer_load_dwordx4 v[96:99], v117, s[28:31], s2 offen offset:0\nbuffer_load_dwordx4 v[100:103], v117, s[28:31], s2 offen offset:896\n"
"buffer_load_dwordx4 v[104:107], v117, s[36:39], 0 offen offset:0\nbuffer_load_dwordx4 v[108:111], v117, s[40:43], 0 offen offset:0\ns_getpc_b64 s[0:1]\n.Lpd:\n"
"s_sub_u32 s40, s0, .Lpd-.Lcode0\ns_subb_u32 s41, s1, 0\ns_mov_b32 s42, .Lcode1-.Lcode0\ns_mov_b32 s43, 0x20000\ns_lshl2_add_u32 s2, s74, s11\ns_lshl_b32 s2, s2, 7\n"
"v_lshlrev_b32 v54, 14, v0\nbuffer_load_dword v31, v54, s[40:43], s2 offen\nv_and_b32 v48, 15, v0\nv_lshrrev_b32 v49, 4, v0\nv_lshrrev_b32 v50, 1, v48\nv_xor_b32 v50, v50, v49\n"
"v_lshlrev_b32 v50, 4, v50\nv_lshl_or_b32 v50, v48, 7, v50\ns_lshr_b32 s2, s11, 1\ns_lshl_b32 s2, s2, 12\nv_add_u32 v10, s2, v50\nv_xor_b32 v11, 64, v10\nv_lshrrev_b32 v50, 2, v48\n"
"v_lshl_add_u32 v51, v49, 3, v50\nv_and_b32 v52, 1, v49\nv_lshl_or_b32 v50, v52, 2, v50\nv_lshlrev_b32 v50, 5, v50\nv_lshl_or_b32 v50, v51, 9, v50\nv_and_b32 v52, 3, v0\n"
"v_lshl_or_b32 v50, v52, 3, v50\ns_and_b32 s2, s11, 1\ns_lshl_b32 s2, s2, 8\ns_add_u32 s2, s2, 0x10000\nv_add_u32 v12, s2, v50\nv_xor_b32 v13, 32, v12\nv_xor_b32 v14, 64, v12\n"
"v_xor_b32 v15, 96, v12\nv_xor_b32 v16, 128, v12\nv_xor_b32 v17, 160, v12\nv_xor_b32 v18, 192, v12\nv_xor_b32 v19, 224, v12\nv_mov_b32 v20, -1\nv_mov_b32 v21, -1\n"
"v_mov_b32 v22, -1\nv_mov_b32 v23, -1\nv_add_u32 v128, 0x10000, v12\nv_add_u32 v129, 0x10000, v13\nv_add_u32 v130, 0x10000, v14\nv_add_u32 v131, 0x10000, v15\n"
"v_add_u32 v132, 0x10000, v16\nv_add_u32 v133, 0x10000, v17\nv_add_u32 v134, 0x10000, v18\nv_add_u32 v135, 0x10000, v19\ns_add_u32 s2, s11, 1\ns_lshr_b32 s2, s2, 1\n"
"s_lshl_b32 s2, s2, 12\nv_add_u32 v140, s2, v10\nv_add_u32 v141, s2, v11\nv_xor_b32 v142, 0x1000, v140\nv_xor_b32 v143, 0x1000, v141\ns_add_u32 m0, s47, 32768\n"
"s_add_u32 s46, s51, 1048576\nD0 6,32,46,0\nD0 7,32,46,1024\nD0 8,32,46,2048\nD0 9,32,46,3072\ns_add_u32 m0, s47, -32768\ns_add_u32 s46, s52, 256\nD0 136,28,46,0\n"
"D0 137,28,46,1024\nD0 138,28,46,2048\nD0 139,28,46,3072\ns_add_u32 m0, s47, 49152\ns_add_u32 s46, s51, 1310720\nD0 6,32,46,0\nD0 7,32,46,1024\nD0 8,32,46,2048\nD0 9,32,46,3072\n"
"v_lshrrev_b32 v32, 4, v0\nv_and_b32 v33, 7, v0\nv_xor_b32 v33, v33, v32\nv_lshlrev_b32 v33, 4, v33\nv_lshrrev_b32 v32, 3, v0\nv_lshl_or_b32 v2, v32, 11, v33\n"
"v_xor_b32 v33, 64, v2\nv_add_u32 v3, 15360, v33\nv_add_u32 v4, 30720, v2\nv_add_u32 v5, 46080, v33\ns_lshr_b32 s2, s13, 2\ns_bfe_u32 s3, s13, 0x10001\ns_mul_i32 s3, s3, 17\n"
"s_add_u32 s2, s2, s3\ns_xor_b32 s3, s14, 1\ns_lshl3_add_u32 s2, s3, s2\ns_lshl1_add_u32 s2, s11, s2\ns_cmp_eq_u32 s2, 32\ns_cselect_b32 s4, 16, s2\ns_lshl_b32 s45, s4, 17\n"
"s_sub_u32 s45, s45, 128\nWL 0\ns_mul_i32 s15, s15, 1664525\ns_add_u32 s15, s15, 1013904223\nv_mov_b32 v25, s15\ns_lshl4_add_u32 s2, s14, s12\ns_lshl3_add_u32 s2, s2, s13\n"
"s_lshl2_add_u32 s72, s2, s11\ns_mul_i32 s2, s72, 33792\ns_add_u32 s49, s2, 0x3500000\ns_and_b32 s2, s72, 511\ns_mul_i32 s56, s2, 17408\ns_add_u32 s56, s56, 0x2300000\n"
"s_xor_b32 s2, s2, 1\ns_mul_i32 s53, s2, 17408\ns_add_u32 s53, s53, 0x2302000\ns_mov_b32 s57, 0x3db8aa3b\ns_mov_b32 s90, 0x40000000\ns_and_b32 s59, s11, 1\ns_lshl_b32 s59, s59, 13\n"
"v_lshrrev_b32 v24, 2, v0\nv_lshlrev_b32 v24, 13, v24\nv_and_b32 v26, 3, v0\nv_lshl_or_b32 v24, v26, 7, v24\nv_lshlrev_b32 v26, 11, v0\ns_lshr_b32 s99, s13, 1\n"
"s_xor_b32 s2, s14, 1\ns_lshl2_add_u32 s99, s2, s99\ns_and_b32 s91, s74, 3\ns_mov_b32 s98, 0\ns_lshl_b32 s96, s11, 17\ns_lshl_b32 s2, s12, 9\ns_add_u32 s96, s96, s2\n"
"s_add_u32 s96, s96, 0\nWV 29\ns_barrier\nRa 160,140,0\nRa 164,140,2048\nRa 168,142,0\nRa 172,142,2048\nTa 128,128,0\nTa 130,128,2048\nTa 132,129,0\nTa 134,129,2048\nTa 136,130,0\n"
"Ta 138,130,2048\nTa 140,131,0\nTa 142,131,2048\nTa 144,132,0\nTa 146,132,2048\nTa 148,133,0\nTa 150,133,2048\nTa 152,134,0\nTa 154,134,2048\nTa 156,135,0\nTa 158,135,2048\nWL 0\n"
"Zvaa 64,128,160\nRa 224,141,0\ns_add_u32 m0, s47, -16384\ns_add_u32 s46, s52, 384\nZvaa 32,128,164\nRa 228,141,2048\nZvaa 192,128,168\nRa 232,143,0\nD0 136,28,46,0\n"
"Zvaa 224,128,172\nRa 236,143,2048\nZvaa 228,132,172\nTa 192,128,16384\nZvaa 196,132,168\nZvaa 36,132,164\nTa 194,128,18432\nZvaa 68,132,160\nTa 196,129,16384\nD0 137,28,46,1024\n"
"Zvaa 72,136,160\nTa 198,129,18432\nZvaa 40,136,164\nTa 200,130,16384\nZvaa 200,136,168\nTa 202,130,18432\nZvaa 232,136,172\nZvaa 236,140,172\nTa 204,131,16384\nD0 138,28,46,2048\n"
"Zvaa 204,140,168\nTa 206,131,18432\nZvaa 44,140,164\nTa 208,132,16384\nZvaa 76,140,160\nTa 210,132,18432\nZvaa 80,144,160\nTa 212,133,16384\nZvaa 48,144,164\nD0 139,28,46,3072\n"
"Zvaa 208,144,168\nTa 214,133,18432\nZvaa 240,144,172\nTa 216,134,16384\nZvaa 244,148,172\nTa 218,134,18432\nZvaa 212,148,168\nTa 220,135,16384\nZvaa 52,148,164\nTa 222,135,18432\n"
"Zvaa 84,148,160\nZvaa 88,152,160\nZvaa 56,152,164\nZvaa 216,152,168\nZvaa 248,152,172\nZvaa 252,156,172\nZvaa 220,156,168\nZvaa 60,156,164\nZvaa 92,156,160\n"
"s_waitcnt vmcnt(21) lgkmcnt(0)\ns_barrier\nMvaa 64,192,224\nRa 160,140,16384\ns_add_u32 m0, s47, 65536\ns_add_u32 s46, s51, 1572864\nMvaa 32,192,228\nRa 164,140,18432\n"
"Mvaa 192,192,232\nRa 168,142,16384\nD0 6,32,46,0\nMvaa 224,192,236\nRa 172,142,18432\nMvaa 228,196,236\nTa 128,12,0\nMvaa 196,196,232\nMvaa 36,196,228\nTa 130,12,2048\n"
"Mvaa 68,196,224\nTa 132,13,0\nD0 7,32,46,1024\nMvaa 72,200,224\nTa 134,13,2048\nMvaa 40,200,228\nTa 136,14,0\nMvaa 200,200,232\nTa 138,14,2048\nMvaa 232,200,236\nMvaa 236,204,236\n"
"Ta 140,15,0\nD0 8,32,46,2048\nMvaa 204,204,232\nTa 142,15,2048\nMvaa 44,204,228\nTa 144,16,0\nMvaa 76,204,224\nTa 146,16,2048\nMvaa 80,208,224\nTa 148,17,0\nMvaa 48,208,228\n"
"D0 9,32,46,3072\nMvaa 208,208,232\nTa 150,17,2048\nMvaa 240,208,236\nTa 152,18,0\nMvaa 244,212,236\nTa 154,18,2048\ns_add_u32 m0, s47, 81920\ns_add_u32 s46, s51, 1835008\n"
"Mvaa 212,212,232\nTa 156,19,0\nMvaa 52,212,228\nTa 158,19,2048\nD0 6,32,46,0\nMvaa 84,212,224\nMvaa 88,216,224\nMvaa 56,216,228\nMvaa 216,216,232\nMvaa 248,216,236\n"
"D0 7,32,46,1024\nMvaa 252,220,236\nMvaa 220,220,232\nMvaa 60,220,228\nMvaa 92,220,224\nWL 0\nMvaa 64,128,160\nRa 224,141,16384\nD0 8,32,46,2048\nMvaa 32,128,164\nRa 228,141,18432\n"
"WV 24\nMvaa 192,128,168\nRa 232,143,16384\nv_lshlrev_b32 v112, 16, v96\nMvaa 224,128,172\nRa 236,143,18432\nv_lshlrev_b32 v113, 16, v100\nMvaa 228,132,172\nTa 192,12,16384\n"
"v_lshlrev_b32 v114, 16, v104\nMvaa 196,132,168\nD0 9,32,46,3072\nv_lshlrev_b32 v115, 16, v108\nMvaa 36,132,164\nTa 194,12,18432\nMU 116,112,114\nMvaa 68,132,160\nTa 196,13,16384\n"
"MU 117,113,114\nMvaa 72,136,160\nTa 198,13,18432\ns_add_u32 m0, s47, -65536\ns_add_u32 s46, s52, 512\nMvaa 40,136,164\nTa 200,14,16384\nCV 116,116,117\nMvaa 200,136,168\n"
"Ta 202,14,18432\nD0 136,28,46,0\nMvaa 232,136,172\nv_lshlrev_b32 v118, 16, v116\nv_and_b32 v119, 0xffff0000, v116\nMvaa 236,140,172\nTa 204,15,16384\nMU 120,112,115\n"
"Mvaa 204,140,168\nTa 206,15,18432\nMU 121,113,115\nMvaa 44,140,164\nTa 208,16,16384\nv_and_b32 v112, 0xffff0000, v96\nMvaa 76,140,160\nTa 210,16,18432\nD0 137,28,46,1024\n"
"Mvaa 80,144,160\nTa 212,17,16384\nv_and_b32 v113, 0xffff0000, v100\nMvaa 48,144,164\nv_and_b32 v114, 0xffff0000, v104\nv_and_b32 v115, 0xffff0000, v108\nMvaa 208,144,168\n"
"Ta 214,17,18432\nMU 116,112,114\nMvaa 240,144,172\nTa 216,18,16384\nMU 117,113,114\nMvaa 244,148,172\nTa 218,18,18432\nD0 138,28,46,2048\nMvaa 212,148,168\nTa 220,19,16384\n"
"CV 116,116,117\nMvaa 52,148,164\nTa 222,19,18432\nv_and_b32 v117, 0xffff0000, v116\nMvaa 84,148,160\nv_lshlrev_b32 v116, 16, v116\nFM 120,112,115\nMvaa 88,152,160\nFM 121,113,115\n"
"AD 118,118,116\nMvaa 56,152,164\nD0 139,28,46,3072\nAD 119,119,117\nMvaa 216,152,168\nv_lshlrev_b32 v112, 16, v97\nv_lshlrev_b32 v113, 16, v101\nMvaa 248,152,172\n"
"v_lshlrev_b32 v114, 16, v105\nv_lshlrev_b32 v115, 16, v109\nMvaa 252,156,172\nMU 116,112,114\nMU 117,113,114\nMvaa 220,156,168\nCV 116,116,117\nv_and_b32 v117, 0xffff0000, v116\n"
"Mvaa 60,156,164\nv_lshlrev_b32 v116, 16, v116\nFM 120,112,115\nMvaa 92,156,160\ns_waitcnt vmcnt(16) lgkmcnt(0)\ns_barrier\nMvaa 64,192,224\nRa 160,140,32768\ns_add_u32 m0, s47, 0\n"
"s_add_u32 s46, s51, 2097152\nMvaa 32,192,228\nRa 164,140,34816\nFM 121,113,115\nMvaa 192,192,232\nRa 168,142,32768\nD0 6,32,46,0\nMvaa 224,192,236\nRa 172,142,34816\n"
"AD 118,118,116\nMvaa 228,196,236\nTa 128,12,32768\nAD 119,119,117\nMvaa 196,196,232\nv_and_b32 v112, 0xffff0000, v97\nv_and_b32 v113, 0xffff0000, v101\nMvaa 36,196,228\n"
"Ta 130,12,34816\nv_and_b32 v114, 0xffff0000, v105\nMvaa 68,196,224\nTa 132,13,32768\nD0 7,32,46,1024\nMvaa 72,200,224\nTa 134,13,34816\nv_and_b32 v115, 0xffff0000, v109\n"
"Mvaa 40,200,228\nTa 136,14,32768\nMU 116,112,114\nMvaa 200,200,232\nTa 138,14,34816\nMU 117,113,114\nMvaa 232,200,236\nCV 116,116,117\nv_and_b32 v117, 0xffff0000, v116\n"
"Mvaa 236,204,236\nTa 140,15,32768\nD0 8,32,46,2048\nMvaa 204,204,232\nTa 142,15,34816\nv_lshlrev_b32 v116, 16, v116\nMvaa 44,204,228\nTa 144,16,32768\nFM 120,112,115\n"
"Mvaa 76,204,224\nTa 146,16,34816\nFM 121,113,115\nMvaa 80,208,224\nTa 148,17,32768\nAD 118,118,116\nMvaa 48,208,228\nD0 9,32,46,3072\nAD 119,119,117\nMvaa 208,208,232\n"
"Ta 150,17,34816\nv_lshlrev_b32 v112, 16, v98\nMvaa 240,208,236\nTa 152,18,32768\nv_lshlrev_b32 v113, 16, v102\nMvaa 244,212,236\nTa 154,18,34816\ns_add_u32 m0, s47, 16384\n"
"s_add_u32 s46, s51, 2359296\nMvaa 212,212,232\nTa 156,19,32768\nv_lshlrev_b32 v114, 16, v106\nMvaa 52,212,228\nTa 158,19,34816\nD0 6,32,46,0\nMvaa 84,212,224\n"
"v_lshlrev_b32 v115, 16, v110\nMU 116,112,114\nMvaa 88,216,224\nMU 117,113,114\nCV 116,116,117\nMvaa 56,216,228\nv_and_b32 v117, 0xffff0000, v116\nv_lshlrev_b32 v116, 16, v116\n"
"Mvaa 216,216,232\nFM 120,112,115\nFM 121,113,115\nMvaa 248,216,236\nD0 7,32,46,1024\nAD 118,118,116\nMvaa 252,220,236\nAD 119,119,117\nv_and_b32 v112, 0xffff0000, v98\n"
"Mvaa 220,220,232\nv_and_b32 v113, 0xffff0000, v102\nv_and_b32 v114, 0xffff0000, v106\nMvaa 60,220,228\nv_and_b32 v115, 0xffff0000, v110\nMU 116,112,114\nMvaa 92,220,224\nWL 0\n"
"Mvaa 64,128,160\nRa 224,141,32768\nD0 8,32,46,2048\nMvaa 32,128,164\nRa 228,141,34816\nMU 117,113,114\nMvaa 192,128,168\nRa 232,143,32768\nCV 116,116,117\nMvaa 224,128,172\n"
"Ra 236,143,34816\nv_and_b32 v117, 0xffff0000, v116\nMvaa 228,132,172\nTa 192,12,49152\nv_lshlrev_b32 v116, 16, v116\nMvaa 196,132,168\nD0 9,32,46,3072\nFM 120,112,115\n"
"Mvaa 36,132,164\nTa 194,12,51200\nFM 121,113,115\nMvaa 68,132,160\nTa 196,13,49152\nAD 118,118,116\nMvaa 72,136,160\nTa 198,13,51200\nAD 119,119,117\nMvaa 40,136,164\n"
"Ta 200,14,49152\nv_lshlrev_b32 v112, 16, v99\nMvaa 200,136,168\nTa 202,14,51200\nv_lshlrev_b32 v113, 16, v103\nMvaa 232,136,172\nv_lshlrev_b32 v114, 16, v107\n"
"v_lshlrev_b32 v115, 16, v111\nMvaa 236,140,172\nTa 204,15,49152\nMU 116,112,114\nMvaa 204,140,168\nTa 206,15,51200\nMU 117,113,114\nMvaa 44,140,164\nTa 208,16,49152\n"
"CV 116,116,117\nMvaa 76,140,160\nTa 210,16,51200\nv_and_b32 v117, 0xffff0000, v116\nMvaa 80,144,160\nTa 212,17,49152\nv_lshlrev_b32 v116, 16, v116\nMvaa 48,144,164\n"
"FM 120,112,115\nFM 121,113,115\nMvaa 208,144,168\nTa 214,17,51200\nAD 118,118,116\nMvaa 240,144,172\nTa 216,18,49152\nAD 119,119,117\nMvaa 244,148,172\nTa 218,18,51200\n"
"v_and_b32 v112, 0xffff0000, v99\nMvaa 212,148,168\nTa 220,19,49152\nv_and_b32 v113, 0xffff0000, v103\nMvaa 52,148,164\nTa 222,19,51200\nv_and_b32 v114, 0xffff0000, v107\n"
"Mvaa 84,148,160\nv_and_b32 v115, 0xffff0000, v111\nMU 116,112,114\nMvaa 88,152,160\nMU 117,113,114\nCV 116,116,117\nMvaa 56,152,164\nv_and_b32 v117, 0xffff0000, v116\n"
"v_lshlrev_b32 v116, 16, v116\nMvaa 216,152,168\nFM 120,112,115\nFM 121,113,115\nMvaa 248,152,172\nAD 118,118,116\nAD 119,119,117\nMvaa 252,156,172\ns_nop 0\n"
"v_permlane32_swap_b32 v118, v119\nv_permlane32_swap_b32 v120, v121\nMvaa 220,156,168\nAD 118,118,119\nAD 120,120,121\nMvaa 60,156,164\nMvaa 92,156,160\n"
"s_waitcnt vmcnt(12) lgkmcnt(0)\ns_barrier\nMvaa 64,192,224\nRa 160,140,49152\ns_add_u32 m0, s47, 32768\ns_add_u32 s46, s51, 2621440\nMvaa 32,192,228\nRa 164,140,51200\n"
"v_permlane16_swap_b32 v118, v120\nMvaa 192,192,232\nRa 168,142,49152\nD0 6,32,46,0\nMvaa 224,192,236\nRa 172,142,51200\nAD 118,118,120\nMvaa 228,196,236\nTa 128,128,0\n"
"Mvaa 196,196,232\nv_add_f32_dpp v118, v118, v118 row_ror:8 row_mask:0xf bank_mask:0xf\nMvaa 36,196,228\nTa 130,128,2048\nMvaa 68,196,224\nTa 132,129,0\nD0 7,32,46,1024\n"
"Mvaa 72,200,224\nTa 134,129,2048\nv_add_f32_dpp v118, v118, v118 row_ror:4 row_mask:0xf bank_mask:0xf\nMvaa 40,200,228\nTa 136,130,0\nMvaa 200,200,232\nTa 138,130,2048\n"
"v_add_f32_dpp v118, v118, v118 row_ror:2 row_mask:0xf bank_mask:0xf\nMvaa 232,200,236\nMvaa 236,204,236\nTa 140,131,0\nD0 8,32,46,2048\nMvaa 204,204,232\nTa 142,131,2048\n"
"v_add_f32_dpp v118, v118, v118 row_ror:1 row_mask:0xf bank_mask:0xf\nMvaa 44,204,228\nTa 144,132,0\nCV 118,118,118\nMvaa 76,204,224\nTa 146,132,2048\nv_lshlrev_b32 v118, 16, v118\n"
"Mvaa 80,208,224\nTa 148,133,0\nv_mul_f32 v119, 0xbfb8aa3b, v118\nMvaa 48,208,228\nD0 9,32,46,3072\nMvaa 208,208,232\nTa 150,133,2048\nMvaa 240,208,236\nTa 152,134,0\n"
"Mvaa 244,212,236\nTa 154,134,2048\ns_add_u32 m0, s47, -32768\ns_add_u32 s46, s52, 640\nMvaa 212,212,232\nTa 156,135,0\nMvaa 52,212,228\nTa 158,135,2048\nD0 136,28,46,0\n"
"Mvaa 84,212,224\nEX 119,119\nMvaa 88,216,224\nv_add_f32 v120, 0, v118\nv_add_f32 v119, 1.0, v119\nMvaa 56,216,228\nv_rcp_f32 v119, v119\nMvaa 216,216,232\n"
"v_ashrrev_i32 v121, 31, v120\nCV 119,119,119\nMvaa 248,216,236\nD0 137,28,46,1024\nv_lshlrev_b32 v119, 16, v119\nMvaa 252,220,236\nv_or_b32 v121, 0x80000000, v121\n"
"v_xor_b32 v120, v120, v121\nMvaa 220,220,232\nv_and_b32 v120, 0xffff0000, v120\ns_lshl2_add_u32 s2, s10, s11\nMvaa 60,220,228\nv_lshrrev_b32 v112, 5, v0\n"
"v_lshl_add_u32 v112, s2, 1, v112\nMvaa 92,220,224\nWL 0\nMvaa 64,128,160\nRa 224,141,49152\nD0 138,28,46,2048\nMvaa 32,128,164\nRa 228,141,51200\nv_sub_u32 v113, 0xffff, v112\n"
"Mvaa 192,128,168\nRa 232,143,49152\nv_or_b32 v120, v120, v113\nMvaa 224,128,172\nRa 236,143,51200\nv_mov_b32 v121, s15\nMvaa 228,132,172\nTa 192,128,16384\nv_and_b32 v113, 31, v0\n"
"Mvaa 196,132,168\nD0 139,28,46,3072\nv_mov_b32 v116, 0x40000000\nMvaa 36,132,164\nTa 194,128,18432\nv_cmp_eq_u32 vcc, 16, v113\nv_lshlrev_b32 v114, 3, v112\n"
"v_lshlrev_b32 v115, 9, v113\nv_cndmask_b32 v114, v116, v114, vcc\nv_cndmask_b32 v119, v119, v118, vcc\nMvaa 68,132,160\nTa 196,129,16384\nv_lshl_add_u32 v115, v112, 2, v115\n"
"Mvaa 72,136,160\nTa 198,129,18432\ns_add_u32 m0, s47, 49152\ns_add_u32 s46, s51, 2883584\nMvaa 40,136,164\nTa 200,130,16384\nv_and_b32 v113, 15, v0\nMvaa 200,136,168\n"
"Ta 202,130,18432\nD0 6,32,46,0\nMvaa 232,136,172\nv_cmp_eq_u32 vcc, 0, v113\ns_mov_b32 s2, 0x5c40000\ns_mov_b32 s3, 0x5c44000\nv_cndmask_b32 v115, v116, v115, vcc\n"
"buffer_store_dwordx2 v[120:121], v114, s[16:19], s3 offen sc0 sc1\nbuffer_store_dword v119, v115, s[16:19], s2 offen sc0 sc1\nMvaa 236,140,172\nTa 204,131,16384\nMvaa 204,140,168\n"
"Ta 206,131,18432\nMvaa 44,140,164\nTa 208,132,16384\nMvaa 76,140,160\nTa 210,132,18432\nD0 7,32,46,1024\nMvaa 80,144,160\nTa 212,133,16384\nMvaa 48,144,164\nMvaa 208,144,168\n"
"Ta 214,133,18432\nMvaa 240,144,172\nTa 216,134,16384\nMvaa 244,148,172\nTa 218,134,18432\nD0 8,32,46,2048\nMvaa 212,148,168\nTa 220,135,16384\nMvaa 52,148,164\nTa 222,135,18432\n"
"Mvaa 84,148,160\nMvaa 88,152,160\nMvaa 56,152,164\nD0 9,32,46,3072\nMvaa 216,152,168\nMvaa 248,152,172\nMvaa 252,156,172\nMvaa 220,156,168\nMvaa 60,156,164\nMvaa 92,156,160\n"
"s_waitcnt vmcnt(14) lgkmcnt(0)\ns_barrier\nMvaa 64,192,224\nRa 160,140,0\ns_add_u32 m0, s47, 65536\ns_add_u32 s46, s51, 3145728\nMvaa 32,192,228\nRa 164,140,2048\n"
"Mvaa 192,192,232\nRa 168,142,0\nD0 6,32,46,0\nMvaa 224,192,236\nRa 172,142,2048\nMvaa 228,196,236\nTa 128,12,0\nMvaa 196,196,232\nMvaa 36,196,228\nTa 130,12,2048\nMvaa 68,196,224\n"
"Ta 132,13,0\nD0 7,32,46,1024\nMvaa 72,200,224\nTa 134,13,2048\nMvaa 40,200,228\nTa 136,14,0\nMvaa 200,200,232\nTa 138,14,2048\nMvaa 232,200,236\nMvaa 236,204,236\nTa 140,15,0\n"
"D0 8,32,46,2048\nMvaa 204,204,232\nTa 142,15,2048\nMvaa 44,204,228\nTa 144,16,0\nMvaa 76,204,224\nTa 146,16,2048\nMvaa 80,208,224\nTa 148,17,0\nMvaa 48,208,228\nD0 9,32,46,3072\n"
"Mvaa 208,208,232\nTa 150,17,2048\nMvaa 240,208,236\nTa 152,18,0\nMvaa 244,212,236\nTa 154,18,2048\ns_add_u32 m0, s47, -16384\ns_add_u32 s46, s52, 768\nMvaa 212,212,232\n"
"Ta 156,19,0\nMvaa 52,212,228\nTa 158,19,2048\nD0 136,28,46,0\nMvaa 84,212,224\nMvaa 88,216,224\nMvaa 56,216,228\nMvaa 216,216,232\nMvaa 248,216,236\nD0 137,28,46,1024\n"
"Mvaa 252,220,236\nMvaa 220,220,232\nMvaa 60,220,228\nMvaa 92,220,224\nWL 0\nMvaa 64,128,160\nRa 224,141,0\nD0 138,28,46,2048\nMvaa 32,128,164\nRa 228,141,2048\nMvaa 192,128,168\n"
"Ra 232,143,0\nMvaa 224,128,172\nRa 236,143,2048\nMvaa 228,132,172\nTa 192,12,16384\nMvaa 196,132,168\nD0 139,28,46,3072\nMvaa 36,132,164\nTa 194,12,18432\nMvaa 68,132,160\n"
"Ta 196,13,16384\nMvaa 72,136,160\nTa 198,13,18432\ns_add_u32 m0, s47, 81920\ns_add_u32 s46, s51, 3407872\nMvaa 40,136,164\nTa 200,14,16384\nMvaa 200,136,168\nTa 202,14,18432\n"
"D0 6,32,46,0\nMvaa 232,136,172\nMvaa 236,140,172\nTa 204,15,16384\nMvaa 204,140,168\nTa 206,15,18432\nMvaa 44,140,164\nTa 208,16,16384\nMvaa 76,140,160\nTa 210,16,18432\n"
"D0 7,32,46,1024\nMvaa 80,144,160\nTa 212,17,16384\nMvaa 48,144,164\nMvaa 208,144,168\nTa 214,17,18432\nMvaa 240,144,172\nTa 216,18,16384\nMvaa 244,148,172\nTa 218,18,18432\n"
"D0 8,32,46,2048\nMvaa 212,148,168\nTa 220,19,16384\nMvaa 52,148,164\nTa 222,19,18432\nMvaa 84,148,160\nMvaa 88,152,160\nMvaa 56,152,164\nD0 9,32,46,3072\nMvaa 216,152,168\n"
"Mvaa 248,152,172\nMvaa 252,156,172\nMvaa 220,156,168\nMvaa 60,156,164\nMvaa 92,156,160\ns_waitcnt vmcnt(12) lgkmcnt(0)\ns_barrier\nMvaa 64,192,224\nRa 160,140,32768\n"
"s_add_u32 m0, s47, 0\ns_add_u32 s44, s44, 0x40000\nMvaa 32,192,228\nRa 164,140,34816\nMvaa 192,192,232\nRa 168,142,32768\nD0 6,24,44,0\nMvaa 224,192,236\nRa 172,142,34816\n"
"Mvaa 228,196,236\nTa 128,12,32768\nMvaa 196,196,232\nMvaa 36,196,228\nTa 130,12,34816\nMvaa 68,196,224\nTa 132,13,32768\nD0 7,24,44,1024\nMvaa 72,200,224\nTa 134,13,34816\n"
"Mvaa 40,200,228\nTa 136,14,32768\nMvaa 200,200,232\nTa 138,14,34816\nMvaa 232,200,236\nMvaa 236,204,236\nTa 140,15,32768\nD0 8,24,44,2048\nMvaa 204,204,232\nTa 142,15,34816\n"
"Mvaa 44,204,228\nTa 144,16,32768\nMvaa 76,204,224\nTa 146,16,34816\nMvaa 80,208,224\nTa 148,17,32768\nMvaa 48,208,228\nD0 9,24,44,3072\nMvaa 208,208,232\nTa 150,17,34816\n"
"Mvaa 240,208,236\nTa 152,18,32768\nMvaa 244,212,236\nTa 154,18,34816\ns_add_u32 m0, s48, 0\ns_add_u32 s45, s45, 128\nMvaa 212,212,232\nTa 156,19,32768\nMvaa 52,212,228\n"
"Ta 158,19,34816\nD0 2,20,45,0\nMvaa 84,212,224\nMvaa 88,216,224\nMvaa 56,216,228\nMvaa 216,216,232\nMvaa 248,216,236\nD0 3,20,45,1024\nMvaa 252,220,236\nMvaa 220,220,232\n"
"Mvaa 60,220,228\nMvaa 92,220,224\nWL 0\nMvaa 64,128,160\nRa 224,141,32768\nD0 4,20,45,2048\nMvaa 32,128,164\nRa 228,141,34816\nMvaa 192,128,168\nRa 232,143,32768\n"
"Mvaa 224,128,172\nRa 236,143,34816\nMvaa 228,132,172\nTa 192,12,49152\nMvaa 196,132,168\nD0 5,20,45,3072\nMvaa 36,132,164\nTa 194,12,51200\nMvaa 68,132,160\nTa 196,13,49152\n"
"Mvaa 72,136,160\nTa 198,13,51200\ns_add_u32 m0, s48, 4096\ns_add_u32 s46, s45, 0x10000\nMvaa 40,136,164\nTa 200,14,49152\nMvaa 200,136,168\nTa 202,14,51200\nD0 2,20,46,0\n"
"Mvaa 232,136,172\nMvaa 236,140,172\nTa 204,15,49152\nMvaa 204,140,168\nTa 206,15,51200\nMvaa 44,140,164\nTa 208,16,49152\nMvaa 76,140,160\nTa 210,16,51200\nD0 3,20,46,1024\n"
"Mvaa 80,144,160\nTa 212,17,49152\nMvaa 48,144,164\nMvaa 208,144,168\nTa 214,17,51200\nMvaa 240,144,172\nTa 216,18,49152\nMvaa 244,148,172\nTa 218,18,51200\nD0 4,20,46,2048\n"
"Mvaa 212,148,168\nTa 220,19,49152\nMvaa 52,148,164\nTa 222,19,51200\nMvaa 84,148,160\nMvaa 88,152,160\nMvaa 56,152,164\nD0 5,20,46,3072\nMvaa 216,152,168\nMvaa 248,152,172\n"
"Mvaa 252,156,172\ns_add_u32 m0, s47, 16384\ns_add_u32 s44, s44, 0x40000\nMvaa 220,156,168\nMvaa 60,156,164\nD0 6,24,44,0\nMvaa 92,156,160\ns_waitcnt vmcnt(13) lgkmcnt(0)\n"
"s_barrier\nMvaa 64,192,224\nRa 160,140,49152\nMvaa 32,192,228\nRa 164,140,51200\nMvaa 192,192,232\nRa 168,142,49152\nMvaa 224,192,236\nRa 172,142,51200\nD0 7,24,44,1024\n"
"Mvaa 228,196,236\nTa 128,128,0\nMvaa 196,196,232\nMvaa 36,196,228\nTa 130,128,2048\nMvaa 68,196,224\nTa 132,129,0\nMvaa 72,200,224\nTa 134,129,2048\nD0 8,24,44,2048\n"
"Mvaa 40,200,228\nTa 136,130,0\nMvaa 200,200,232\nTa 138,130,2048\nMvaa 232,200,236\nMvaa 236,204,236\nTa 140,131,0\nMvaa 204,204,232\nTa 142,131,2048\nD0 9,24,44,3072\n"
"Mvaa 44,204,228\nTa 144,132,0\nMvaa 76,204,224\nTa 146,132,2048\nMvaa 80,208,224\nTa 148,133,0\ns_add_u32 m0, s47, 32768\ns_add_u32 s44, s44, 0x40000\nMvaa 48,208,228\n"
"Mvaa 208,208,232\nTa 150,133,2048\nD0 6,24,44,0\nMvaa 240,208,236\nTa 152,134,0\nMvaa 244,212,236\nTa 154,134,2048\nMvaa 212,212,232\nTa 156,135,0\nMvaa 52,212,228\n"
"Ta 158,135,2048\nMvaa 84,212,224\nD0 7,24,44,1024\nMvaa 88,216,224\nMvaa 56,216,228\nMvaa 216,216,232\nMvaa 248,216,236\nMvaa 252,220,236\nD0 8,24,44,2048\nMvaa 220,220,232\n"
"Mvaa 60,220,228\nMvaa 92,220,224\nWL 0\nMvaa 64,128,160\nRa 224,141,49152\nMvaa 32,128,164\nRa 228,141,51200\nD0 9,24,44,3072\nMvaa 192,128,168\nRa 232,143,49152\n"
"Mvaa 224,128,172\nRa 236,143,51200\nMvaa 228,132,172\nTa 192,128,16384\nMvaa 196,132,168\nMvaa 36,132,164\nTa 194,128,18432\nMvaa 68,132,160\nTa 196,129,16384\nMvaa 72,136,160\n"
"Ta 198,129,18432\nMvaa 40,136,164\nTa 200,130,16384\nMvaa 200,136,168\nTa 202,130,18432\nMvaa 232,136,172\nMvaa 236,140,172\nTa 204,131,16384\nMvaa 204,140,168\nTa 206,131,18432\n"
"Mvaa 44,140,164\nTa 208,132,16384\nMvaa 76,140,160\nTa 210,132,18432\nMvaa 80,144,160\nTa 212,133,16384\nMvaa 48,144,164\nMvaa 208,144,168\nTa 214,133,18432\nMvaa 240,144,172\n"
"Ta 216,134,16384\nMvaa 244,148,172\nTa 218,134,18432\nMvaa 212,148,168\nTa 220,135,16384\nMvaa 52,148,164\nTa 222,135,18432\nMvaa 84,148,160\nMvaa 88,152,160\nMvaa 56,152,164\n"
"Mvaa 216,152,168\nMvaa 248,152,172\nMvaa 252,156,172\nMvaa 220,156,168\nMvaa 60,156,164\nMvaa 92,156,160\ns_waitcnt vmcnt(8) lgkmcnt(0)\ns_barrier\nMvaa 64,192,224\n"
"s_add_u32 m0, s48, 32768\ns_add_u32 s45, s45, 128\nTa 128,12,0\nMvaa 68,196,224\nTa 130,12,2048\nMvaa 72,200,224\nD0 2,20,45,0\nTa 132,13,0\nMvaa 76,204,224\nTa 134,13,2048\n"
"Mvaa 80,208,224\nTa 136,14,0\nMvaa 84,212,224\nTa 138,14,2048\nMvaa 88,216,224\nTa 140,15,0\nv_mul_f32 v64, s57, v64\nMvaa 92,220,224\nD0 3,20,45,1024\nTa 142,15,2048\n"
"Mvaa 32,192,228\nTa 144,16,0\nv_mul_f32 v65, s57, v65\nMvaa 36,196,228\nTa 146,16,2048\nCV 64,64,65\nMvaa 40,200,228\nTa 148,17,0\nv_mul_f32 v66, s57, v66\nMvaa 44,204,228\n"
"Ta 150,17,2048\nv_mul_f32 v67, s57, v67\nMvaa 48,208,228\nD0 4,20,45,2048\nTa 152,18,0\nMvaa 52,212,228\nTa 154,18,2048\nCV 65,66,67\nMvaa 56,216,228\nTa 156,19,0\n"
"v_mul_f32 v68, s57, v68\nMvaa 60,220,228\nTa 158,19,2048\nv_mul_f32 v69, s57, v69\nMvaa 192,192,232\nRa 160,10,0\nCV 66,68,69\nMvaa 196,196,232\nD0 5,20,45,3072\nRa 164,10,2048\n"
"Mvaa 200,200,232\nRa 168,10,8192\nv_mul_f32 v70, s57, v70\nMvaa 204,204,232\nRa 172,10,10240\nv_mul_f32 v71, s57, v71\nMvaa 208,208,232\nRa 176,10,16384\nCV 67,70,71\n"
"Mvaa 212,212,232\nRa 180,10,18432\nv_mul_f32 v72, s57, v72\nMvaa 216,216,232\nRa 184,10,24576\nv_mul_f32 v73, s57, v73\nMvaa 220,220,232\nRa 188,10,26624\nCV 72,72,73\n"
"Mvaa 224,192,236\nv_mul_f32 v74, s57, v74\nv_mul_f32 v75, s57, v75\nMvaa 228,196,236\nCV 73,74,75\nv_mul_f32 v76, s57, v76\nMvaa 232,200,236\nv_mul_f32 v77, s57, v77\nCV 74,76,77\n"
"Mvaa 236,204,236\nv_mul_f32 v78, s57, v78\nv_mul_f32 v79, s57, v79\nMvaa 240,208,236\nCV 75,78,79\nMvaa 244,212,236\nMvaa 248,216,236\nMvaa 252,220,236\n"
"s_waitcnt vmcnt(8) lgkmcnt(0)\ns_barrier\ns_bitcmp1_b32 s13, 0\ns_cbranch_scc1 .V15\nZaaa 0,128,176\nTa 192,12,16384\nv_mul_f32 v80, s57, v80\nZaaa 32,128,180\n"
"s_add_u32 m0, s48, 36864\ns_add_u32 s46, s45, 0x10000\ns_add_u32 s6, s56, 0\nSv 64,1,16,6,0\nZaaa 64,128,184\nTa 194,12,18432\nv_mul_f32 v81, s57, v81\nZaaa 96,128,188\n"
"D0 2,20,46,0\nCV 80,80,81\nZaaa 100,132,188\nTa 196,13,16384\nv_mul_f32 v82, s57, v82\nZaaa 68,132,184\nv_mul_f32 v83, s57, v83\nCV 81,82,83\nZaaa 36,132,180\nTa 198,13,18432\n"
"v_mul_f32 v84, s57, v84\nZaaa 4,132,176\nD0 3,20,46,1024\nv_mul_f32 v85, s57, v85\nZaaa 8,136,176\nTa 200,14,16384\nCV 82,84,85\nZaaa 40,136,180\nv_mul_f32 v86, s57, v86\n"
"v_mul_f32 v87, s57, v87\ns_add_u32 s6, s56, 0\nSv 72,1,16,6,2048\nZaaa 72,136,184\nTa 202,14,18432\nCV 83,86,87\nZaaa 104,136,188\nD0 4,20,46,2048\nv_mul_f32 v88, s57, v88\n"
"Zaaa 108,140,188\nTa 204,15,16384\nv_mul_f32 v89, s57, v89\nZaaa 76,140,184\nCV 88,88,89\nv_mul_f32 v90, s57, v90\nZaaa 44,140,180\nTa 206,15,18432\nv_mul_f32 v91, s57, v91\n"
"Zaaa 12,140,176\nD0 5,20,46,3072\nCV 89,90,91\nZaaa 16,144,176\nTa 208,16,16384\nv_mul_f32 v92, s57, v92\nZaaa 48,144,180\nv_mul_f32 v93, s57, v93\nCV 90,92,93\nZaaa 80,144,184\n"
"Ta 210,16,18432\nv_mul_f32 v94, s57, v94\nZaaa 112,144,188\nv_mul_f32 v95, s57, v95\nCV 91,94,95\nZaaa 116,148,188\nTa 212,17,16384\nZaaa 84,148,184\nZaaa 52,148,180\n"
"Ta 214,17,18432\nZaaa 20,148,176\nZaaa 24,152,176\nTa 216,18,16384\nZaaa 56,152,180\ns_add_u32 s6, s56, 4096\nSv 80,1,16,6,0\nZaaa 88,152,184\nTa 218,18,18432\nZaaa 120,152,188\n"
"Zaaa 124,156,188\nTa 220,19,16384\nZaaa 92,156,184\ns_add_u32 s6, s56, 4096\nSv 88,1,16,6,2048\nZaaa 60,156,180\nTa 222,19,18432\nZaaa 28,156,176\ns_add_u32 m0, s47, 49152\n"
"s_add_u32 s44, s44, 0x40000\nZvaa 64,128,160\nRa 224,11,0\nv_mul_f32 v32, s57, v32\nZvaa 96,128,164\nD0 6,24,44,0\nv_mul_f32 v33, s57, v33\nZvaa 128,128,168\nRa 228,11,2048\n"
"CV 32,32,33\nZvaa 160,128,172\nv_mul_f32 v34, s57, v34\nv_mul_f32 v35, s57, v35\nZvaa 164,132,172\nRa 232,11,8192\nCV 33,34,35\nZvaa 132,132,168\nD0 7,24,44,1024\n"
"v_mul_f32 v36, s57, v36\nZvaa 100,132,164\nRa 236,11,10240\nv_mul_f32 v37, s57, v37\nZvaa 68,132,160\nCV 34,36,37\nv_mul_f32 v38, s57, v38\nZvaa 72,136,160\nRa 240,11,16384\n"
"v_mul_f32 v39, s57, v39\nZvaa 104,136,164\nD0 8,24,44,2048\nCV 35,38,39\nZvaa 136,136,168\nRa 244,11,18432\nZvaa 168,136,172\ns_add_u32 s6, s56, 0\nSv 32,1,16,6,1024\n"
"Zvaa 172,140,172\nRa 248,11,24576\nZvaa 140,140,168\nD0 9,24,44,3072\nZvaa 108,140,164\nRa 252,11,26624\nZvaa 76,140,160\ns_add_u32 m0, s47, 0\ns_add_u32 s44, s44, 0x40000\n"
"Zvaa 80,144,160\nZvaa 112,144,164\nD0 6,24,44,0\nv_mul_f32 v40, s57, v40\nZvaa 144,144,168\nv_mul_f32 v41, s57, v41\nCV 40,40,41\nZvaa 176,144,172\nv_mul_f32 v42, s57, v42\n"
"v_mul_f32 v43, s57, v43\nZvaa 180,148,172\nCV 41,42,43\nv_mul_f32 v44, s57, v44\nZvaa 148,148,168\nD0 7,24,44,1024\nv_mul_f32 v45, s57, v45\nZvaa 116,148,164\nCV 42,44,45\n"
"v_mul_f32 v46, s57, v46\nZvaa 84,148,160\nv_mul_f32 v47, s57, v47\nCV 43,46,47\nZvaa 88,152,160\ns_add_u32 s6, s56, 0\nSv 40,1,16,6,3072\nZvaa 120,152,164\nD0 8,24,44,2048\n"
"Zvaa 152,152,168\nZvaa 184,152,172\nZvaa 188,156,172\nZvaa 156,156,168\nD0 9,24,44,3072\nZvaa 124,156,164\nZvaa 92,156,160\ns_waitcnt vmcnt(12) lgkmcnt(0)\ns_barrier\n"
"Mvaa 64,192,224\nTa 128,12,32768\nMvaa 96,192,228\nMvaa 128,192,232\nTa 130,12,34816\nv_mul_f32 v48, s57, v48\nMvaa 160,192,236\ns_add_u32 m0, s48, 0\ns_add_u32 s45, s45, 128\n"
"Maaa 0,192,240\nTa 132,13,32768\nv_mul_f32 v49, s57, v49\nMaaa 32,192,244\nD0 2,20,45,0\nCV 48,48,49\nMaaa 64,192,248\nTa 134,13,34816\nv_mul_f32 v50, s57, v50\nMaaa 96,192,252\n"
"v_mul_f32 v51, s57, v51\nCV 49,50,51\nMaaa 100,196,252\nTa 136,14,32768\nv_mul_f32 v52, s57, v52\nMaaa 68,196,248\nv_mul_f32 v53, s57, v53\nCV 50,52,53\nMaaa 36,196,244\n"
"Ta 138,14,34816\nv_mul_f32 v54, s57, v54\nMaaa 4,196,240\nv_mul_f32 v55, s57, v55\nCV 51,54,55\nMvaa 164,196,236\nTa 140,15,32768\nMvaa 132,196,232\nD0 3,20,45,1024\n"
"Mvaa 100,196,228\nTa 142,15,34816\nMvaa 68,196,224\ns_add_u32 s6, s56, 4096\nSv 48,1,16,6,1024\nMvaa 72,200,224\nTa 144,16,32768\nMvaa 104,200,228\nMvaa 136,200,232\n"
"Ta 146,16,34816\nMvaa 168,200,236\nv_mul_f32 v56, s57, v56\nv_mul_f32 v57, s57, v57\nMaaa 8,200,240\nTa 148,17,32768\nCV 56,56,57\nMaaa 40,200,244\nD0 4,20,45,2048\n"
"v_mul_f32 v58, s57, v58\nMaaa 72,200,248\nTa 150,17,34816\nv_mul_f32 v59, s57, v59\nMaaa 104,200,252\nCV 57,58,59\nv_mul_f32 v60, s57, v60\nMaaa 108,204,252\nTa 152,18,32768\n"
"v_mul_f32 v61, s57, v61\nMaaa 76,204,248\nCV 58,60,61\nv_mul_f32 v62, s57, v62\nMaaa 44,204,244\nTa 154,18,34816\nv_mul_f32 v63, s57, v63\nMaaa 12,204,240\nCV 59,62,63\n"
"Mvaa 172,204,236\nTa 156,19,32768\nMvaa 140,204,232\nD0 5,20,45,3072\nMvaa 108,204,228\nTa 158,19,34816\nMvaa 76,204,224\ns_add_u32 s6, s56, 4096\nSv 56,1,16,6,3072\n"
"Mvaa 80,208,224\nRa 160,10,32768\nMvaa 112,208,228\nMvaa 144,208,232\nRa 164,10,34816\nMvaa 176,208,236\ns_add_u32 m0, s48, 4096\ns_add_u32 s46, s45, 0x10000\nMaaa 16,208,240\n"
"Ra 168,10,40960\nv_mul_f32 v192, s57, v192\nMaaa 48,208,244\nD0 2,20,46,0\nv_mul_f32 v193, s57, v193\nMaaa 80,208,248\nRa 172,10,43008\nCV 192,192,193\nMaaa 112,208,252\n"
"v_mul_f32 v194, s57, v194\nv_mul_f32 v195, s57, v195\nMaaa 116,212,252\nRa 176,10,49152\nCV 193,194,195\nMaaa 84,212,248\nv_mul_f32 v196, s57, v196\nv_mul_f32 v197, s57, v197\n"
"Maaa 52,212,244\nRa 180,10,51200\nCV 194,196,197\nMaaa 20,212,240\nv_mul_f32 v198, s57, v198\nv_mul_f32 v199, s57, v199\nMvaa 180,212,236\nRa 184,10,57344\nCV 195,198,199\n"
"Mvaa 148,212,232\nD0 3,20,46,1024\nMvaa 116,212,228\nRa 188,10,59392\nMvaa 84,212,224\ns_add_u32 s6, s53, 0\nSv 192,1,16,6,0\nMvaa 88,216,224\nMvaa 120,216,228\nMvaa 152,216,232\n"
"Mvaa 184,216,236\nMaaa 24,216,240\nMaaa 56,216,244\nD0 4,20,46,2048\nv_mul_f32 v200, s57, v200\nMaaa 88,216,248\nv_mul_f32 v201, s57, v201\nCV 200,200,201\nMaaa 120,216,252\n"
"v_mul_f32 v202, s57, v202\nv_mul_f32 v203, s57, v203\nMaaa 124,220,252\nCV 201,202,203\nv_mul_f32 v204, s57, v204\nMaaa 92,220,248\nv_mul_f32 v205, s57, v205\nCV 202,204,205\n"
"Maaa 60,220,244\nv_mul_f32 v206, s57, v206\nv_mul_f32 v207, s57, v207\nMaaa 28,220,240\nCV 203,206,207\nMvaa 188,220,236\ns_add_u32 s6, s53, 0\nSv 200,1,16,6,2048\n"
"Mvaa 156,220,232\nD0 5,20,46,3072\nMvaa 124,220,228\nMvaa 92,220,224\ns_waitcnt vmcnt(17) lgkmcnt(0)\ns_barrier\nMvaa 64,128,160\nTa 192,12,49152\nMvaa 96,128,164\n"
"Mvaa 128,128,168\nTa 194,12,51200\nMvaa 160,128,172\ns_add_u32 m0, s47, 16384\ns_add_u32 s44, s44, 0x40000\nMaaa 0,128,176\nTa 196,13,49152\nMaaa 32,128,180\nD0 6,24,44,0\n"
"Maaa 64,128,184\nTa 198,13,51200\nv_mul_f32 v208, s57, v208\nMaaa 96,128,188\nv_mul_f32 v209, s57, v209\nCV 208,208,209\nMaaa 100,132,188\nTa 200,14,49152\n"
"v_mul_f32 v210, s57, v210\nMaaa 68,132,184\nv_mul_f32 v211, s57, v211\nCV 209,210,211\nMaaa 36,132,180\nTa 202,14,51200\nv_mul_f32 v212, s57, v212\nMaaa 4,132,176\n"
"v_mul_f32 v213, s57, v213\nCV 210,212,213\nMvaa 164,132,172\nTa 204,15,49152\nv_mul_f32 v214, s57, v214\nMvaa 132,132,168\nD0 7,24,44,1024\nv_mul_f32 v215, s57, v215\n"
"Mvaa 100,132,164\nTa 206,15,51200\nCV 211,214,215\nMvaa 68,132,160\ns_add_u32 s6, s53, 4096\nSv 208,1,16,6,0\nMvaa 72,136,160\nTa 208,16,49152\nMvaa 104,136,164\nMvaa 136,136,168\n"
"Ta 210,16,51200\nMvaa 168,136,172\nMaaa 8,136,176\nTa 212,17,49152\nMaaa 40,136,180\nD0 8,24,44,2048\nMaaa 72,136,184\nTa 214,17,51200\nMaaa 104,136,188\n"
"v_mul_f32 v216, s57, v216\nv_mul_f32 v217, s57, v217\nMaaa 108,140,188\nTa 216,18,49152\nCV 216,216,217\nMaaa 76,140,184\nv_mul_f32 v218, s57, v218\nv_mul_f32 v219, s57, v219\n"
"Maaa 44,140,180\nTa 218,18,51200\nCV 217,218,219\nMaaa 12,140,176\nv_mul_f32 v220, s57, v220\nv_mul_f32 v221, s57, v221\nMvaa 172,140,172\nTa 220,19,49152\nCV 218,220,221\n"
"Mvaa 140,140,168\nD0 9,24,44,3072\nv_mul_f32 v222, s57, v222\nMvaa 108,140,164\nTa 222,19,51200\nv_mul_f32 v223, s57, v223\nMvaa 76,140,160\nCV 219,222,223\nMvaa 80,144,160\n"
"Ra 224,11,32768\nMvaa 112,144,164\ns_add_u32 s6, s53, 4096\nSv 216,1,16,6,2048\nMvaa 144,144,168\nRa 228,11,34816\nMvaa 176,144,172\ns_add_u32 m0, s47, 32768\n"
"s_add_u32 s44, s44, 0x40000\nMaaa 16,144,176\nRa 232,11,40960\nMaaa 48,144,180\nD0 6,24,44,0\nMaaa 80,144,184\nRa 236,11,43008\nMaaa 112,144,188\nMaaa 116,148,188\n"
"Ra 240,11,49152\nv_mul_f32 v224, s57, v224\nMaaa 84,148,184\nv_mul_f32 v225, s57, v225\nCV 224,224,225\nMaaa 52,148,180\nRa 244,11,51200\nv_mul_f32 v226, s57, v226\n"
"Maaa 20,148,176\nv_mul_f32 v227, s57, v227\nCV 225,226,227\nMvaa 180,148,172\nRa 248,11,57344\nv_mul_f32 v228, s57, v228\nMvaa 148,148,168\nD0 7,24,44,1024\n"
"v_mul_f32 v229, s57, v229\nMvaa 116,148,164\nRa 252,11,59392\nCV 226,228,229\nMvaa 84,148,160\nv_mul_f32 v230, s57, v230\nv_mul_f32 v231, s57, v231\nMvaa 88,152,160\n"
"CV 227,230,231\nMvaa 120,152,164\ns_add_u32 s6, s53, 0\nSv 224,1,16,6,1024\nMvaa 152,152,168\nMvaa 184,152,172\nMaaa 24,152,176\nMaaa 56,152,180\nD0 8,24,44,2048\nMaaa 88,152,184\n"
"Maaa 120,152,188\nMaaa 124,156,188\nMaaa 92,156,184\nv_mul_f32 v232, s57, v232\nv_mul_f32 v233, s57, v233\nMaaa 60,156,180\nCV 232,232,233\nv_mul_f32 v234, s57, v234\n"
"Maaa 28,156,176\nv_mul_f32 v235, s57, v235\nCV 233,234,235\nMvaa 188,156,172\nv_mul_f32 v236, s57, v236\nv_mul_f32 v237, s57, v237\nMvaa 156,156,168\nD0 9,24,44,3072\n"
"CV 234,236,237\nMvaa 124,156,164\nv_mul_f32 v238, s57, v238\nv_mul_f32 v239, s57, v239\nMvaa 92,156,160\ns_waitcnt vmcnt(11) lgkmcnt(0)\ns_barrier\nMvaa 64,192,224\nTa 128,12,0\n"
"CV 235,238,239\nMvaa 96,192,228\ns_add_u32 s6, s53, 0\nSv 232,1,16,6,3072\nMvaa 128,192,232\nTa 130,12,2048\nMvaa 160,192,236\ns_add_u32 m0, s48, 32768\ns_add_u32 s45, s45, 128\n"
"Maaa 0,192,240\nTa 132,13,0\nMaaa 32,192,244\nD0 2,20,45,0\nMaaa 64,192,248\nTa 134,13,2048\nMaaa 96,192,252\nMaaa 100,196,252\nTa 136,14,0\nMaaa 68,196,248\nMaaa 36,196,244\n"
"Ta 138,14,2048\nv_mul_f32 v240, s57, v240\nMaaa 4,196,240\nv_mul_f32 v241, s57, v241\nCV 240,240,241\nMvaa 164,196,236\nTa 140,15,0\nv_mul_f32 v242, s57, v242\nMvaa 132,196,232\n"
"D0 3,20,45,1024\nv_mul_f32 v243, s57, v243\nMvaa 100,196,228\nTa 142,15,2048\nCV 241,242,243\nMvaa 68,196,224\nv_mul_f32 v244, s57, v244\nv_mul_f32 v245, s57, v245\n"
"Mvaa 72,200,224\nTa 144,16,0\nCV 242,244,245\nMvaa 104,200,228\nv_mul_f32 v246, s57, v246\nv_mul_f32 v247, s57, v247\nMvaa 136,200,232\nTa 146,16,2048\nCV 243,246,247\n"
"Mvaa 168,200,236\ns_add_u32 s6, s53, 4096\nSv 240,1,16,6,1024\nMaaa 8,200,240\nTa 148,17,0\nMaaa 40,200,244\nD0 4,20,45,2048\nMaaa 72,200,248\nTa 150,17,2048\nMaaa 104,200,252\n"
"Maaa 108,204,252\nTa 152,18,0\nMaaa 76,204,248\nMaaa 44,204,244\nTa 154,18,2048\nMaaa 12,204,240\nv_mul_f32 v248, s57, v248\nv_mul_f32 v249, s57, v249\nMvaa 172,204,236\n"
"Ta 156,19,0\nCV 248,248,249\nMvaa 140,204,232\nD0 5,20,45,3072\nv_mul_f32 v250, s57, v250\nMvaa 108,204,228\nTa 158,19,2048\nv_mul_f32 v251, s57, v251\nMvaa 76,204,224\n"
"CV 249,250,251\nv_mul_f32 v252, s57, v252\nMvaa 80,208,224\nRa 160,10,0\nv_mul_f32 v253, s57, v253\nMvaa 112,208,228\nCV 250,252,253\nv_mul_f32 v254, s57, v254\nMvaa 144,208,232\n"
"Ra 164,10,2048\nv_mul_f32 v255, s57, v255\nMvaa 176,208,236\ns_add_u32 m0, s48, 36864\ns_add_u32 s46, s45, 0x10000\nMaaa 16,208,240\nRa 168,10,8192\nCV 251,254,255\n"
"Maaa 48,208,244\nD0 2,20,46,0\nMaaa 80,208,248\nRa 172,10,10240\nMaaa 112,208,252\ns_add_u32 s6, s53, 4096\nSv 248,1,16,6,3072\nMaaa 116,212,252\nRa 176,10,16384\nMaaa 84,212,248\n"
"Maaa 52,212,244\nRa 180,10,18432\nMaaa 20,212,240\nMvaa 180,212,236\nRa 184,10,24576\nMvaa 148,212,232\nD0 3,20,46,1024\nMvaa 116,212,228\nRa 188,10,26624\nMvaa 84,212,224\n"
"Mvaa 88,216,224\nMvaa 120,216,228\nMvaa 152,216,232\nMvaa 184,216,236\nMaaa 24,216,240\nMaaa 56,216,244\nD0 4,20,46,2048\nMaaa 88,216,248\nMaaa 120,216,252\nMaaa 124,220,252\n"
"Maaa 92,220,248\nMaaa 60,220,244\nMaaa 28,220,240\ns_and_b32 s90, s72, 511\ns_lshl2_add_u32 s90, s90, 0x5c49400\nMvaa 188,220,236\nMvaa 156,220,232\nD0 5,20,46,3072\n"
"Mvaa 124,220,228\nMvaa 92,220,224\ns_waitcnt vmcnt(17) lgkmcnt(0)\ns_barrier\ns_branch .MK2\n.V15:\nZaaa 0,176,128\nTa 192,12,16384\nv_mul_f32 v80, s57, v80\nZaaa 4,180,128\n"
"s_add_u32 m0, s48, 36864\ns_add_u32 s46, s45, 0x10000\ns_add_u32 s6, s56, 0\nSv 64,1,16,6,0\nZaaa 64,184,128\nTa 194,12,18432\nv_mul_f32 v81, s57, v81\nZaaa 68,188,128\n"
"D0 2,20,46,0\nCV 80,80,81\nZaaa 76,188,132\nTa 196,13,16384\nv_mul_f32 v82, s57, v82\nZaaa 72,184,132\nv_mul_f32 v83, s57, v83\nCV 81,82,83\nZaaa 12,180,132\nTa 198,13,18432\n"
"v_mul_f32 v84, s57, v84\nZaaa 8,176,132\nD0 3,20,46,1024\nv_mul_f32 v85, s57, v85\nZaaa 16,176,136\nTa 200,14,16384\nCV 82,84,85\nZaaa 20,180,136\nv_mul_f32 v86, s57, v86\n"
"v_mul_f32 v87, s57, v87\ns_add_u32 s6, s56, 0\nSv 72,1,16,6,2048\nZaaa 80,184,136\nTa 202,14,18432\nCV 83,86,87\nZaaa 84,188,136\nD0 4,20,46,2048\nv_mul_f32 v88, s57, v88\n"
"Zaaa 92,188,140\nTa 204,15,16384\nv_mul_f32 v89, s57, v89\nZaaa 88,184,140\nCV 88,88,89\nv_mul_f32 v90, s57, v90\nZaaa 28,180,140\nTa 206,15,18432\nv_mul_f32 v91, s57, v91\n"
"Zaaa 24,176,140\nD0 5,20,46,3072\nCV 89,90,91\nZaaa 32,176,144\nTa 208,16,16384\nv_mul_f32 v92, s57, v92\nZaaa 36,180,144\nv_mul_f32 v93, s57, v93\nCV 90,92,93\nZaaa 96,184,144\n"
"Ta 210,16,18432\nv_mul_f32 v94, s57, v94\nZaaa 100,188,144\nv_mul_f32 v95, s57, v95\nCV 91,94,95\nZaaa 108,188,148\nTa 212,17,16384\nZaaa 104,184,148\nZaaa 44,180,148\n"
"Ta 214,17,18432\nZaaa 40,176,148\nZaaa 48,176,152\nTa 216,18,16384\nZaaa 52,180,152\ns_add_u32 s6, s56, 4096\nSv 80,1,16,6,0\nZaaa 112,184,152\nTa 218,18,18432\nZaaa 116,188,152\n"
"Zaaa 124,188,156\nTa 220,19,16384\nZaaa 120,184,156\ns_add_u32 s6, s56, 4096\nSv 88,1,16,6,2048\nZaaa 60,180,156\nTa 222,19,18432\nZaaa 56,176,156\ns_add_u32 m0, s47, 49152\n"
"s_add_u32 s44, s44, 0x40000\nZvaa 64,160,128\nRa 224,11,0\nv_mul_f32 v32, s57, v32\nZvaa 68,164,128\nD0 6,24,44,0\nv_mul_f32 v33, s57, v33\nZvaa 128,168,128\nRa 228,11,2048\n"
"CV 32,32,33\nZvaa 132,172,128\nv_mul_f32 v34, s57, v34\nv_mul_f32 v35, s57, v35\nZvaa 140,172,132\nRa 232,11,8192\nCV 33,34,35\nZvaa 136,168,132\nD0 7,24,44,1024\n"
"v_mul_f32 v36, s57, v36\nZvaa 76,164,132\nRa 236,11,10240\nv_mul_f32 v37, s57, v37\nZvaa 72,160,132\nCV 34,36,37\nv_mul_f32 v38, s57, v38\nZvaa 80,160,136\nRa 240,11,16384\n"
"v_mul_f32 v39, s57, v39\nZvaa 84,164,136\nD0 8,24,44,2048\nCV 35,38,39\nZvaa 144,168,136\nRa 244,11,18432\nZvaa 148,172,136\ns_add_u32 s6, s56, 0\nSv 32,1,16,6,1024\n"
"Zvaa 156,172,140\nRa 248,11,24576\nZvaa 152,168,140\nD0 9,24,44,3072\nZvaa 92,164,140\nRa 252,11,26624\nZvaa 88,160,140\ns_add_u32 m0, s47, 0\ns_add_u32 s44, s44, 0x40000\n"
"Zvaa 96,160,144\nZvaa 100,164,144\nD0 6,24,44,0\nv_mul_f32 v40, s57, v40\nZvaa 160,168,144\nv_mul_f32 v41, s57, v41\nCV 40,40,41\nZvaa 164,172,144\nv_mul_f32 v42, s57, v42\n"
"v_mul_f32 v43, s57, v43\nZvaa 172,172,148\nCV 41,42,43\nv_mul_f32 v44, s57, v44\nZvaa 168,168,148\nD0 7,24,44,1024\nv_mul_f32 v45, s57, v45\nZvaa 108,164,148\nCV 42,44,45\n"
"v_mul_f32 v46, s57, v46\nZvaa 104,160,148\nv_mul_f32 v47, s57, v47\nCV 43,46,47\nZvaa 112,160,152\ns_add_u32 s6, s56, 0\nSv 40,1,16,6,3072\nZvaa 116,164,152\nD0 8,24,44,2048\n"
"Zvaa 176,168,152\nZvaa 180,172,152\nZvaa 188,172,156\nZvaa 184,168,156\nD0 9,24,44,3072\nZvaa 124,164,156\nZvaa 120,160,156\ns_waitcnt vmcnt(12) lgkmcnt(0)\ns_barrier\n"
"Mvaa 64,224,192\nTa 128,12,32768\nMvaa 68,228,192\nMvaa 128,232,192\nTa 130,12,34816\nv_mul_f32 v48, s57, v48\nMvaa 132,236,192\ns_add_u32 m0, s48, 0\ns_add_u32 s45, s45, 128\n"
"Maaa 0,240,192\nTa 132,13,32768\nv_mul_f32 v49, s57, v49\nMaaa 4,244,192\nD0 2,20,45,0\nCV 48,48,49\nMaaa 64,248,192\nTa 134,13,34816\nv_mul_f32 v50, s57, v50\nMaaa 68,252,192\n"
"v_mul_f32 v51, s57, v51\nCV 49,50,51\nMaaa 76,252,196\nTa 136,14,32768\nv_mul_f32 v52, s57, v52\nMaaa 72,248,196\nv_mul_f32 v53, s57, v53\nCV 50,52,53\nMaaa 12,244,196\n"
"Ta 138,14,34816\nv_mul_f32 v54, s57, v54\nMaaa 8,240,196\nv_mul_f32 v55, s57, v55\nCV 51,54,55\nMvaa 140,236,196\nTa 140,15,32768\nMvaa 136,232,196\nD0 3,20,45,1024\n"
"Mvaa 76,228,196\nTa 142,15,34816\nMvaa 72,224,196\ns_add_u32 s6, s56, 4096\nSv 48,1,16,6,1024\nMvaa 80,224,200\nTa 144,16,32768\nMvaa 84,228,200\nMvaa 144,232,200\n"
"Ta 146,16,34816\nMvaa 148,236,200\nv_mul_f32 v56, s57, v56\nv_mul_f32 v57, s57, v57\nMaaa 16,240,200\nTa 148,17,32768\nCV 56,56,57\nMaaa 20,244,200\nD0 4,20,45,2048\n"
"v_mul_f32 v58, s57, v58\nMaaa 80,248,200\nTa 150,17,34816\nv_mul_f32 v59, s57, v59\nMaaa 84,252,200\nCV 57,58,59\nv_mul_f32 v60, s57, v60\nMaaa 92,252,204\nTa 152,18,32768\n"
"v_mul_f32 v61, s57, v61\nMaaa 88,248,204\nCV 58,60,61\nv_mul_f32 v62, s57, v62\nMaaa 28,244,204\nTa 154,18,34816\nv_mul_f32 v63, s57, v63\nMaaa 24,240,204\nCV 59,62,63\n"
"Mvaa 156,236,204\nTa 156,19,32768\nMvaa 152,232,204\nD0 5,20,45,3072\nMvaa 92,228,204\nTa 158,19,34816\nMvaa 88,224,204\ns_add_u32 s6, s56, 4096\nSv 56,1,16,6,3072\n"
"Mvaa 96,224,208\nRa 160,10,32768\nMvaa 100,228,208\nMvaa 160,232,208\nRa 164,10,34816\nMvaa 164,236,208\ns_add_u32 m0, s48, 4096\ns_add_u32 s46, s45, 0x10000\nMaaa 32,240,208\n"
"Ra 168,10,40960\nv_mul_f32 v192, s57, v192\nMaaa 36,244,208\nD0 2,20,46,0\nv_mul_f32 v193, s57, v193\nMaaa 96,248,208\nRa 172,10,43008\nCV 192,192,193\nMaaa 100,252,208\n"
"v_mul_f32 v194, s57, v194\nv_mul_f32 v195, s57, v195\nMaaa 108,252,212\nRa 176,10,49152\nCV 193,194,195\nMaaa 104,248,212\nv_mul_f32 v196, s57, v196\nv_mul_f32 v197, s57, v197\n"
"Maaa 44,244,212\nRa 180,10,51200\nCV 194,196,197\nMaaa 40,240,212\nv_mul_f32 v198, s57, v198\nv_mul_f32 v199, s57, v199\nMvaa 172,236,212\nRa 184,10,57344\nCV 195,198,199\n"
"Mvaa 168,232,212\nD0 3,20,46,1024\nMvaa 108,228,212\nRa 188,10,59392\nMvaa 104,224,212\ns_add_u32 s6, s53, 0\nSv 192,1,16,6,0\nMvaa 112,224,216\nMvaa 116,228,216\n"
"Mvaa 176,232,216\nMvaa 180,236,216\nMaaa 48,240,216\nMaaa 52,244,216\nD0 4,20,46,2048\nv_mul_f32 v200, s57, v200\nMaaa 112,248,216\nv_mul_f32 v201, s57, v201\nCV 200,200,201\n"
"Maaa 116,252,216\nv_mul_f32 v202, s57, v202\nv_mul_f32 v203, s57, v203\nMaaa 124,252,220\nCV 201,202,203\nv_mul_f32 v204, s57, v204\nMaaa 120,248,220\nv_mul_f32 v205, s57, v205\n"
"CV 202,204,205\nMaaa 60,244,220\nv_mul_f32 v206, s57, v206\nv_mul_f32 v207, s57, v207\nMaaa 56,240,220\nCV 203,206,207\nMvaa 188,236,220\ns_add_u32 s6, s53, 0\nSv 200,1,16,6,2048\n"
"Mvaa 184,232,220\nD0 5,20,46,3072\nMvaa 124,228,220\nMvaa 120,224,220\ns_waitcnt vmcnt(17) lgkmcnt(0)\ns_barrier\nMvaa 64,160,128\nTa 192,12,49152\nMvaa 68,164,128\n"
"Mvaa 128,168,128\nTa 194,12,51200\nMvaa 132,172,128\ns_add_u32 m0, s47, 16384\ns_add_u32 s44, s44, 0x40000\nMaaa 0,176,128\nTa 196,13,49152\nMaaa 4,180,128\nD0 6,24,44,0\n"
"Maaa 64,184,128\nTa 198,13,51200\nv_mul_f32 v208, s57, v208\nMaaa 68,188,128\nv_mul_f32 v209, s57, v209\nCV 208,208,209\nMaaa 76,188,132\nTa 200,14,49152\n"
"v_mul_f32 v210, s57, v210\nMaaa 72,184,132\nv_mul_f32 v211, s57, v211\nCV 209,210,211\nMaaa 12,180,132\nTa 202,14,51200\nv_mul_f32 v212, s57, v212\nMaaa 8,176,132\n"
"v_mul_f32 v213, s57, v213\nCV 210,212,213\nMvaa 140,172,132\nTa 204,15,49152\nv_mul_f32 v214, s57, v214\nMvaa 136,168,132\nD0 7,24,44,1024\nv_mul_f32 v215, s57, v215\n"
"Mvaa 76,164,132\nTa 206,15,51200\nCV 211,214,215\nMvaa 72,160,132\ns_add_u32 s6, s53, 4096\nSv 208,1,16,6,0\nMvaa 80,160,136\nTa 208,16,49152\nMvaa 84,164,136\nMvaa 144,168,136\n"
"Ta 210,16,51200\nMvaa 148,172,136\nMaaa 16,176,136\nTa 212,17,49152\nMaaa 20,180,136\nD0 8,24,44,2048\nMaaa 80,184,136\nTa 214,17,51200\nMaaa 84,188,136\n"
"v_mul_f32 v216, s57, v216\nv_mul_f32 v217, s57, v217\nMaaa 92,188,140\nTa 216,18,49152\nCV 216,216,217\nMaaa 88,184,140\nv_mul_f32 v218, s57, v218\nv_mul_f32 v219, s57, v219\n"
"Maaa 28,180,140\nTa 218,18,51200\nCV 217,218,219\nMaaa 24,176,140\nv_mul_f32 v220, s57, v220\nv_mul_f32 v221, s57, v221\nMvaa 156,172,140\nTa 220,19,49152\nCV 218,220,221\n"
"Mvaa 152,168,140\nD0 9,24,44,3072\nv_mul_f32 v222, s57, v222\nMvaa 92,164,140\nTa 222,19,51200\nv_mul_f32 v223, s57, v223\nMvaa 88,160,140\nCV 219,222,223\nMvaa 96,160,144\n"
"Ra 224,11,32768\nMvaa 100,164,144\ns_add_u32 s6, s53, 4096\nSv 216,1,16,6,2048\nMvaa 160,168,144\nRa 228,11,34816\nMvaa 164,172,144\ns_add_u32 m0, s47, 32768\n"
"s_add_u32 s44, s44, 0x40000\nMaaa 32,176,144\nRa 232,11,40960\nMaaa 36,180,144\nD0 6,24,44,0\nMaaa 96,184,144\nRa 236,11,43008\nMaaa 100,188,144\nMaaa 108,188,148\n"
"Ra 240,11,49152\nv_mul_f32 v224, s57, v224\nMaaa 104,184,148\nv_mul_f32 v225, s57, v225\nCV 224,224,225\nMaaa 44,180,148\nRa 244,11,51200\nv_mul_f32 v226, s57, v226\n"
"Maaa 40,176,148\nv_mul_f32 v227, s57, v227\nCV 225,226,227\nMvaa 172,172,148\nRa 248,11,57344\nv_mul_f32 v228, s57, v228\nMvaa 168,168,148\nD0 7,24,44,1024\n"
"v_mul_f32 v229, s57, v229\nMvaa 108,164,148\nRa 252,11,59392\nCV 226,228,229\nMvaa 104,160,148\nv_mul_f32 v230, s57, v230\nv_mul_f32 v231, s57, v231\nMvaa 112,160,152\n"
"CV 227,230,231\nMvaa 116,164,152\ns_add_u32 s6, s53, 0\nSv 224,1,16,6,1024\nMvaa 176,168,152\nMvaa 180,172,152\nMaaa 48,176,152\nMaaa 52,180,152\nD0 8,24,44,2048\n"
"Maaa 112,184,152\nMaaa 116,188,152\nMaaa 124,188,156\nMaaa 120,184,156\nv_mul_f32 v232, s57, v232\nv_mul_f32 v233, s57, v233\nMaaa 60,180,156\nCV 232,232,233\n"
"v_mul_f32 v234, s57, v234\nMaaa 56,176,156\nv_mul_f32 v235, s57, v235\nCV 233,234,235\nMvaa 188,172,156\nv_mul_f32 v236, s57, v236\nv_mul_f32 v237, s57, v237\nMvaa 184,168,156\n"
"D0 9,24,44,3072\nCV 234,236,237\nMvaa 124,164,156\nv_mul_f32 v238, s57, v238\nv_mul_f32 v239, s57, v239\nMvaa 120,160,156\ns_waitcnt vmcnt(11) lgkmcnt(0)\ns_barrier\n"
"Mvaa 64,224,192\nTa 128,12,0\nCV 235,238,239\nMvaa 68,228,192\ns_add_u32 s6, s53, 0\nSv 232,1,16,6,3072\nMvaa 128,232,192\nTa 130,12,2048\nMvaa 132,236,192\n"
"s_add_u32 m0, s48, 32768\ns_add_u32 s45, s45, 128\nMaaa 0,240,192\nTa 132,13,0\nMaaa 4,244,192\nD0 2,20,45,0\nMaaa 64,248,192\nTa 134,13,2048\nMaaa 68,252,192\nMaaa 76,252,196\n"
"Ta 136,14,0\nMaaa 72,248,196\nMaaa 12,244,196\nTa 138,14,2048\nv_mul_f32 v240, s57, v240\nMaaa 8,240,196\nv_mul_f32 v241, s57, v241\nCV 240,240,241\nMvaa 140,236,196\nTa 140,15,0\n"
"v_mul_f32 v242, s57, v242\nMvaa 136,232,196\nD0 3,20,45,1024\nv_mul_f32 v243, s57, v243\nMvaa 76,228,196\nTa 142,15,2048\nCV 241,242,243\nMvaa 72,224,196\n"
"v_mul_f32 v244, s57, v244\nv_mul_f32 v245, s57, v245\nMvaa 80,224,200\nTa 144,16,0\nCV 242,244,245\nMvaa 84,228,200\nv_mul_f32 v246, s57, v246\nv_mul_f32 v247, s57, v247\n"
"Mvaa 144,232,200\nTa 146,16,2048\nCV 243,246,247\nMvaa 148,236,200\ns_add_u32 s6, s53, 4096\nSv 240,1,16,6,1024\nMaaa 16,240,200\nTa 148,17,0\nMaaa 20,244,200\nD0 4,20,45,2048\n"
"Maaa 80,248,200\nTa 150,17,2048\nMaaa 84,252,200\nMaaa 92,252,204\nTa 152,18,0\nMaaa 88,248,204\nMaaa 28,244,204\nTa 154,18,2048\nMaaa 24,240,204\nv_mul_f32 v248, s57, v248\n"
"v_mul_f32 v249, s57, v249\nMvaa 156,236,204\nTa 156,19,0\nCV 248,248,249\nMvaa 152,232,204\nD0 5,20,45,3072\nv_mul_f32 v250, s57, v250\nMvaa 92,228,204\nTa 158,19,2048\n"
"v_mul_f32 v251, s57, v251\nMvaa 88,224,204\nCV 249,250,251\nv_mul_f32 v252, s57, v252\nMvaa 96,224,208\nRa 160,10,0\nv_mul_f32 v253, s57, v253\nMvaa 100,228,208\nCV 250,252,253\n"
"v_mul_f32 v254, s57, v254\nMvaa 160,232,208\nRa 164,10,2048\nv_mul_f32 v255, s57, v255\nMvaa 164,236,208\ns_add_u32 m0, s48, 36864\ns_add_u32 s46, s45, 0x10000\nMaaa 32,240,208\n"
"Ra 168,10,8192\nCV 251,254,255\nMaaa 36,244,208\nD0 2,20,46,0\nMaaa 96,248,208\nRa 172,10,10240\nMaaa 100,252,208\ns_add_u32 s6, s53, 4096\nSv 248,1,16,6,3072\nMaaa 108,252,212\n"
"Ra 176,10,16384\nMaaa 104,248,212\nMaaa 44,244,212\nRa 180,10,18432\nMaaa 40,240,212\nMvaa 172,236,212\nRa 184,10,24576\nMvaa 168,232,212\nD0 3,20,46,1024\nMvaa 108,228,212\n"
"Ra 188,10,26624\nMvaa 104,224,212\nMvaa 112,224,216\nMvaa 116,228,216\nMvaa 176,232,216\nMvaa 180,236,216\nMaaa 48,240,216\nMaaa 52,244,216\nD0 4,20,46,2048\nMaaa 112,248,216\n"
"Maaa 116,252,216\nMaaa 124,252,220\nMaaa 120,248,220\nMaaa 60,244,220\nMaaa 56,240,220\ns_and_b32 s90, s72, 511\ns_lshl2_add_u32 s90, s90, 0x5c49400\nMvaa 188,236,220\n"
"Mvaa 184,232,220\nD0 5,20,46,3072\nMvaa 124,228,220\nMvaa 120,224,220\ns_waitcnt vmcnt(17) lgkmcnt(0)\ns_barrier\ns_branch .MV3\n.MK2:\nMvaa 64,128,160\nTa 192,12,16384\n"
"s_and_b32 s2, s12, 1\nMvaa 96,128,164\ns_and_b32 s3, s13, 6\ns_add_u32 s2, s2, s3\nMvaa 128,128,168\nTa 194,12,18432\ns_lshl3_add_u32 s2, s14, s2\nMvaa 160,128,172\n"
"s_add_u32 m0, s47, 49152\ns_add_u32 s44, s44, 0x40000\nMaaa 0,128,176\nTa 196,13,16384\nMaaa 32,128,180\nD0 6,24,44,0\nMaaa 64,128,184\nTa 198,13,18432\nMaaa 96,128,188\n"
"s_cmp_lt_u32 s2, 14\ns_cselect_b32 s5, 0, 0x40000000\nMaaa 100,132,188\nTa 200,14,16384\ns_mul_i32 s4, s73, 14\nMaaa 68,132,184\ns_add_u32 s4, s4, s2\ns_mov_b32 s36, s62\n"
"Maaa 36,132,180\nTa 202,14,18432\ns_and_b32 s37, s63, 0xffff\nMaaa 4,132,176\ns_mov_b32 s38, 0x188000\ns_mov_b32 s39, 0x20000\nMvaa 164,132,172\nTa 204,15,16384\n"
"s_mov_b32 s40, s64\nMvaa 132,132,168\nD0 7,24,44,1024\ns_and_b32 s41, s65, 0xffff\nMvaa 100,132,164\nTa 206,15,18432\ns_mov_b32 s42, 0x188000\nMvaa 68,132,160\n"
"s_mov_b32 s43, 0x20000\ns_mov_b32 s92, s66\nMvaa 72,136,160\nTa 208,16,16384\ns_and_b32 s93, s67, 0xffff\nMvaa 104,136,164\ns_mov_b32 s94, 0x188000\ns_mov_b32 s95, 0x20000\n"
"Mvaa 136,136,168\nTa 210,16,18432\nv_lshrrev_b32 v252, 1, v0\nMvaa 168,136,172\nv_mul_u32_u24 v253, 14336, v252\nv_and_b32 v254, 1, v0\nMaaa 8,136,176\nTa 212,17,16384\n"
"v_lshl_add_u32 v253, v254, 4, v253\nMaaa 40,136,180\nD0 8,24,44,2048\nv_mov_b32 v254, 0x40000000\nMaaa 72,136,184\nTa 214,17,18432\nv_cmp_gt_u32 vcc, 56, v0\n"
"v_lshlrev_b32 v252, 3, v252\ns_mul_i32 s2, s11, 401408\nv_cndmask_b32 v253, v254, v253, vcc\nv_cndmask_b32 v255, v254, v252, vcc\nMaaa 104,136,188\ns_lshl_b32 s3, s4, 5\n"
"s_add_u32 s2, s2, s3\nMaaa 108,140,188\nTa 216,18,16384\ns_or_b32 s86, s2, s5\nMaaa 76,140,184\ns_add_u32 s87, s86, 7168\nv_lshrrev_b32 v251, 4, v0\nMaaa 44,140,180\n"
"Ta 218,18,18432\nv_mul_u32_u24 v252, 3584, v251\nMaaa 12,140,176\nv_and_b32 v251, 15, v0\nv_lshl_add_u32 v252, v251, 4, v252\nMvaa 172,140,172\nTa 220,19,16384\n"
"v_cmp_gt_u32 vcc, 14, v251\ns_mul_i32 s2, s4, 14336\ns_mul_i32 s3, s11, 224\nv_cndmask_b32 v252, v254, v252, vcc\nMvaa 140,140,168\nD0 9,24,44,3072\ns_add_u32 s2, s2, s3\n"
"Mvaa 108,140,164\nTa 222,19,18432\ns_or_b32 s88, s2, s5\nMvaa 76,140,160\nMvaa 80,144,160\nRa 224,11,0\nMvaa 112,144,164\nMvaa 144,144,168\nRa 228,11,2048\nMvaa 176,144,172\n"
"s_add_u32 m0, s47, 0\ns_add_u32 s44, s44, 0x40000\nMaaa 16,144,176\nRa 232,11,8192\nMaaa 48,144,180\nD0 6,24,44,0\nMaaa 80,144,184\nRa 236,11,10240\nMaaa 112,144,188\n"
"Maaa 116,148,188\nRa 240,11,16384\nMaaa 84,148,184\nMaaa 52,148,180\nRa 244,11,18432\nMaaa 20,148,176\nSv 20,1,16,49,0\ns_add_u32 s49, s49, 1024\nMvaa 180,148,172\n"
"Ra 248,11,24576\nMvaa 148,148,168\nD0 7,24,44,1024\nMvaa 116,148,164\nRa 252,11,26624\nMvaa 84,148,160\nMvaa 88,152,160\nMvaa 120,152,164\nSv 20,1,16,49,0\n"
"s_add_u32 s49, s49, 1024\nMvaa 152,152,168\nMvaa 184,152,172\nMaaa 24,152,176\nMaaa 56,152,180\nD0 8,24,44,2048\nMaaa 88,152,184\nMaaa 120,152,188\nMaaa 124,156,188\n"
"Maaa 92,156,184\nSv 20,1,16,49,0\ns_add_u32 s49, s49, 1024\nMaaa 60,156,180\nMaaa 28,156,176\nMvaa 188,156,172\nMvaa 156,156,168\nD0 9,24,44,3072\nMvaa 124,156,164\n"
"Mvaa 92,156,160\ns_waitcnt vmcnt(11) lgkmcnt(0)\ns_barrier\nMvaa 64,192,224\nTa 128,12,32768\nMvaa 96,192,228\nbuffer_load_dwordx4 v[192:195], v253, s[36:39], s86 offen offset:0\n"
"Mvaa 128,192,232\nTa 130,12,34816\nMvaa 160,192,236\ns_add_u32 m0, s48, 0\ns_add_u32 s45, s45, 128\nMaaa 0,192,240\nTa 132,13,32768\nMaaa 32,192,244\nD0 2,20,45,0\n"
"Maaa 64,192,248\nTa 134,13,34816\nMaaa 96,192,252\nMaaa 100,196,252\nTa 136,14,32768\nMaaa 68,196,248\nMaaa 36,196,244\nTa 138,14,34816\nMaaa 4,196,240\nMvaa 164,196,236\n"
"Ta 140,15,32768\nMvaa 132,196,232\nD0 3,20,45,1024\nMvaa 100,196,228\nTa 142,15,34816\nMvaa 68,196,224\nMvaa 72,200,224\nTa 144,16,32768\nMvaa 104,200,228\nMvaa 136,200,232\n"
"Ta 146,16,34816\nMvaa 168,200,236\nMaaa 8,200,240\nTa 148,17,32768\nMaaa 40,200,244\nD0 4,20,45,2048\nMaaa 72,200,248\nTa 150,17,34816\nMaaa 104,200,252\nMaaa 108,204,252\n"
"Ta 152,18,32768\nMaaa 76,204,248\nMaaa 44,204,244\nTa 154,18,34816\nMaaa 12,204,240\nMvaa 172,204,236\nTa 156,19,32768\nMvaa 140,204,232\nD0 5,20,45,3072\nMvaa 108,204,228\n"
"Ta 158,19,34816\nMvaa 76,204,224\nMvaa 80,208,224\nRa 160,10,32768\nMvaa 112,208,228\nMvaa 144,208,232\nRa 164,10,34816\nMvaa 176,208,236\ns_add_u32 m0, s48, 4096\n"
"s_add_u32 s46, s45, 0x10000\nMaaa 16,208,240\nRa 168,10,40960\nMaaa 48,208,244\nD0 2,20,46,0\nMaaa 80,208,248\nRa 172,10,43008\nMaaa 112,208,252\nMaaa 116,212,252\n"
"Ra 176,10,49152\nMaaa 84,212,248\nMaaa 52,212,244\nRa 180,10,51200\nMaaa 20,212,240\nMvaa 180,212,236\nRa 184,10,57344\nMvaa 148,212,232\nD0 3,20,46,1024\nMvaa 116,212,228\n"
"Ra 188,10,59392\nMvaa 84,212,224\nMvaa 88,216,224\nMvaa 120,216,228\nMvaa 152,216,232\nMvaa 184,216,236\nMaaa 24,216,240\nMaaa 56,216,244\nD0 4,20,46,2048\nMaaa 88,216,248\n"
"Maaa 120,216,252\nMaaa 124,220,252\nMaaa 92,220,248\nMaaa 60,220,244\nMaaa 28,220,240\nMvaa 188,220,236\nMvaa 156,220,232\nD0 5,20,46,3072\nMvaa 124,220,228\nMvaa 92,220,224\n"
"s_waitcnt vmcnt(16) lgkmcnt(0)\ns_barrier\nMvaa 64,128,160\nTa 192,12,49152\nMvaa 96,128,164\nbuffer_load_dwordx4 v[208:211], v253, s[40:43], s86 offen offset:0\nMvaa 128,128,168\n"
"Ta 194,12,51200\nMvaa 160,128,172\ns_add_u32 m0, s47, 16384\ns_add_u32 s44, s44, 0x40000\nMaaa 0,128,176\nTa 196,13,49152\nMaaa 32,128,180\nD0 6,24,44,0\nMaaa 64,128,184\n"
"Ta 198,13,51200\nMaaa 96,128,188\nMaaa 100,132,188\nTa 200,14,49152\nMaaa 68,132,184\nbuffer_store_dword v25, off, s[16:19], s90 offset:0 sc0 sc1\nMaaa 36,132,180\n"
"Ta 202,14,51200\nMaaa 4,132,176\nMvaa 164,132,172\nTa 204,15,49152\nMvaa 132,132,168\nD0 7,24,44,1024\nMvaa 100,132,164\nTa 206,15,51200\nMvaa 68,132,160\nMvaa 72,136,160\n"
"Ta 208,16,49152\nMvaa 104,136,164\nMvaa 136,136,168\nTa 210,16,51200\nMvaa 168,136,172\nMaaa 8,136,176\nTa 212,17,49152\nMaaa 40,136,180\nD0 8,24,44,2048\nMaaa 72,136,184\n"
"Ta 214,17,51200\nMaaa 104,136,188\nMaaa 108,140,188\nTa 216,18,49152\nMaaa 76,140,184\nMaaa 44,140,180\nTa 218,18,51200\nMaaa 12,140,176\nMvaa 172,140,172\nTa 220,19,49152\n"
"Mvaa 140,140,168\nD0 9,24,44,3072\nMvaa 108,140,164\nTa 222,19,51200\nMvaa 76,140,160\nMvaa 80,144,160\nRa 224,11,32768\nMvaa 112,144,164\nMvaa 144,144,168\nRa 228,11,34816\n"
"Mvaa 176,144,172\ns_add_u32 m0, s47, 32768\ns_add_u32 s44, s44, 0x40000\nMaaa 16,144,176\nRa 232,11,40960\nMaaa 48,144,180\nD0 6,24,44,0\nMaaa 80,144,184\nRa 236,11,43008\n"
"Maaa 112,144,188\nMaaa 116,148,188\nRa 240,11,49152\nMaaa 84,148,184\nMaaa 52,148,180\nRa 244,11,51200\nMaaa 20,148,176\nSv 20,1,16,49,0\ns_add_u32 s49, s49, 1024\n"
"Mvaa 180,148,172\nRa 248,11,57344\nMvaa 148,148,168\nD0 7,24,44,1024\nMvaa 116,148,164\nRa 252,11,59392\nMvaa 84,148,160\nMvaa 88,152,160\nMvaa 120,152,164\nSv 20,1,16,49,0\n"
"s_add_u32 s49, s49, 1024\nMvaa 152,152,168\nMvaa 184,152,172\nMaaa 24,152,176\nMaaa 56,152,180\nD0 8,24,44,2048\nMaaa 88,152,184\nMaaa 120,152,188\nMaaa 124,156,188\n"
"Maaa 92,156,184\nSv 20,1,16,49,0\ns_add_u32 s49, s49, 1024\nMaaa 60,156,180\nMaaa 28,156,176\nMvaa 188,156,172\nMvaa 156,156,168\nD0 9,24,44,3072\nMvaa 124,156,164\n"
"Mvaa 92,156,160\ns_waitcnt vmcnt(13) lgkmcnt(0)\ns_barrier\nMvaa 64,192,224\nTa 128,12,0\nMvaa 96,192,228\nbuffer_load_dwordx4 v[196:199], v253, s[36:39], s86 offen offset:3584\n"
"Mvaa 128,192,232\nTa 130,12,2048\nMvaa 160,192,236\ns_add_u32 m0, s48, 32768\ns_add_u32 s45, s45, 128\nMaaa 0,192,240\nTa 132,13,0\nMaaa 32,192,244\nD0 2,20,45,0\nMaaa 64,192,248\n"
"Ta 134,13,2048\nMaaa 96,192,252\nMaaa 100,196,252\nTa 136,14,0\nMaaa 68,196,248\nMaaa 36,196,244\nTa 138,14,2048\nMaaa 4,196,240\nMvaa 164,196,236\nTa 140,15,0\nMvaa 132,196,232\n"
"D0 3,20,45,1024\nMvaa 100,196,228\nTa 142,15,2048\nMvaa 68,196,224\nMvaa 72,200,224\nTa 144,16,0\nMvaa 104,200,228\nMvaa 136,200,232\nTa 146,16,2048\nMvaa 168,200,236\n"
"Maaa 8,200,240\nTa 148,17,0\nMaaa 40,200,244\nD0 4,20,45,2048\nMaaa 72,200,248\nTa 150,17,2048\nMaaa 104,200,252\nMaaa 108,204,252\nTa 152,18,0\nMaaa 76,204,248\nMaaa 44,204,244\n"
"Ta 154,18,2048\nMaaa 12,204,240\nMvaa 172,204,236\nTa 156,19,0\nMvaa 140,204,232\nD0 5,20,45,3072\nMvaa 108,204,228\nTa 158,19,2048\nMvaa 76,204,224\nMvaa 80,208,224\nRa 160,10,0\n"
"Mvaa 112,208,228\nMvaa 144,208,232\nRa 164,10,2048\nMvaa 176,208,236\ns_add_u32 m0, s48, 36864\ns_add_u32 s46, s45, 0x10000\nMaaa 16,208,240\nRa 168,10,8192\nMaaa 48,208,244\n"
"D0 2,20,46,0\nMaaa 80,208,248\nRa 172,10,10240\nMaaa 112,208,252\nMaaa 116,212,252\nRa 176,10,16384\nMaaa 84,212,248\nMaaa 52,212,244\nRa 180,10,18432\nMaaa 20,212,240\n"
"Mvaa 180,212,236\nRa 184,10,24576\nMvaa 148,212,232\nD0 3,20,46,1024\nMvaa 116,212,228\nRa 188,10,26624\nMvaa 84,212,224\nMvaa 88,216,224\nMvaa 120,216,228\nMvaa 152,216,232\n"
"Mvaa 184,216,236\nMaaa 24,216,240\nMaaa 56,216,244\nD0 4,20,46,2048\nMaaa 88,216,248\nMaaa 120,216,252\nMaaa 124,220,252\nMaaa 92,220,248\nMaaa 60,220,244\nMaaa 28,220,240\n"
"Mvaa 188,220,236\nMvaa 156,220,232\nD0 5,20,46,3072\nMvaa 124,220,228\nMvaa 92,220,224\ns_waitcnt vmcnt(16) lgkmcnt(0)\ns_barrier\nMvaa 64,128,160\nTa 192,12,16384\n"
"Mvaa 96,128,164\nbuffer_load_dwordx4 v[212:215], v253, s[40:43], s86 offen offset:3584\nMvaa 128,128,168\nTa 194,12,18432\nMvaa 160,128,172\ns_add_u32 m0, s47, 49152\n"
"s_add_u32 s44, s44, 0x40000\nMaaa 0,128,176\nTa 196,13,16384\nMaaa 32,128,180\nD0 6,24,44,0\nMaaa 64,128,184\nTa 198,13,18432\nMaaa 96,128,188\nMaaa 100,132,188\nTa 200,14,16384\n"
"Maaa 68,132,184\nMaaa 36,132,180\nTa 202,14,18432\nMaaa 4,132,176\nMvaa 164,132,172\nTa 204,15,16384\nMvaa 132,132,168\nD0 7,24,44,1024\nMvaa 100,132,164\nTa 206,15,18432\n"
"Mvaa 68,132,160\nMvaa 72,136,160\nTa 208,16,16384\nMvaa 104,136,164\nMvaa 136,136,168\nTa 210,16,18432\nMvaa 168,136,172\nMaaa 8,136,176\nTa 212,17,16384\nMaaa 40,136,180\n"
"D0 8,24,44,2048\nMaaa 72,136,184\nTa 214,17,18432\nMaaa 104,136,188\nMaaa 108,140,188\nTa 216,18,16384\nMaaa 76,140,184\nMaaa 44,140,180\nTa 218,18,18432\nMaaa 12,140,176\n"
"Mvaa 172,140,172\nTa 220,19,16384\nMvaa 140,140,168\nD0 9,24,44,3072\nMvaa 108,140,164\nTa 222,19,18432\nMvaa 76,140,160\nMvaa 80,144,160\nRa 224,11,0\nMvaa 112,144,164\n"
"Mvaa 144,144,168\nRa 228,11,2048\nMvaa 176,144,172\ns_add_u32 m0, s47, 0\ns_add_u32 s44, s44, 0x40000\nMaaa 16,144,176\nRa 232,11,8192\nMaaa 48,144,180\nD0 6,24,44,0\n"
"Maaa 80,144,184\nRa 236,11,10240\nMaaa 112,144,188\nMaaa 116,148,188\nRa 240,11,16384\nMaaa 84,148,184\nMaaa 52,148,180\nRa 244,11,18432\nMaaa 20,148,176\nSv 20,1,16,49,0\n"
"s_add_u32 s49, s49, 1024\nMvaa 180,148,172\nRa 248,11,24576\nMvaa 148,148,168\nD0 7,24,44,1024\nMvaa 116,148,164\nRa 252,11,26624\nMvaa 84,148,160\nMvaa 88,152,160\n"
"Mvaa 120,152,164\nSv 20,1,16,49,0\ns_add_u32 s49, s49, 1024\nMvaa 152,152,168\nMvaa 184,152,172\nMaaa 24,152,176\nMaaa 56,152,180\nD0 8,24,44,2048\nMaaa 88,152,184\n"
"Maaa 120,152,188\nMaaa 124,156,188\nMaaa 92,156,184\nSv 20,1,16,49,0\ns_add_u32 s49, s49, 1024\nMaaa 60,156,180\nMaaa 28,156,176\nMvaa 188,156,172\nMvaa 156,156,168\n"
"D0 9,24,44,3072\nMvaa 124,156,164\nMvaa 92,156,160\ns_waitcnt vmcnt(12) lgkmcnt(0)\ns_barrier\nMvaa 64,192,224\nTa 128,12,32768\nMvaa 96,192,228\n"
"buffer_load_dwordx4 v[200:203], v253, s[36:39], s87 offen offset:0\nMvaa 128,192,232\nTa 130,12,34816\nMvaa 160,192,236\ns_add_u32 m0, s48, 0\ns_add_u32 s45, s45, 128\n"
"Maaa 0,192,240\nTa 132,13,32768\nMaaa 32,192,244\nD0 2,20,45,0\nMaaa 64,192,248\nTa 134,13,34816\nMaaa 96,192,252\ns_lshl_b32 s6, s11, 12\ns_add_u32 s6, s6, 0x5c44000\n"
"Maaa 100,196,252\nTa 136,14,32768\nbuffer_load_dwordx4 v[32:35], v1, s[16:19], s6 offen offset:0 sc0 sc1\nMaaa 68,196,248\nMaaa 36,196,244\nTa 138,14,34816\nMaaa 4,196,240\n"
"Mvaa 164,196,236\nTa 140,15,32768\nbuffer_load_dwordx4 v[36:39], v1, s[16:19], s6 offen offset:1024 sc0 sc1\nMvaa 132,196,232\nD0 3,20,45,1024\nMvaa 100,196,228\nTa 142,15,34816\n"
"Mvaa 68,196,224\nMvaa 72,200,224\nTa 144,16,32768\nbuffer_load_dwordx4 v[40:43], v1, s[16:19], s6 offen offset:2048 sc0 sc1\nMvaa 104,200,228\nMvaa 136,200,232\nTa 146,16,34816\n"
"Mvaa 168,200,236\nMaaa 8,200,240\nTa 148,17,32768\nbuffer_load_dwordx4 v[44:47], v1, s[16:19], s6 offen offset:3072 sc0 sc1\nMaaa 40,200,244\nD0 4,20,45,2048\nMaaa 72,200,248\n"
"Ta 150,17,34816\nMaaa 104,200,252\nMaaa 108,204,252\nTa 152,18,32768\nMaaa 76,204,248\nMaaa 44,204,244\nTa 154,18,34816\nMaaa 12,204,240\nMvaa 172,204,236\nTa 156,19,32768\n"
"Mvaa 140,204,232\nD0 5,20,45,3072\nMvaa 108,204,228\nTa 158,19,34816\nMvaa 76,204,224\nMvaa 80,208,224\nRa 160,10,32768\nMvaa 112,208,228\nMvaa 144,208,232\nRa 164,10,34816\n"
"Mvaa 176,208,236\ns_add_u32 m0, s48, 4096\ns_add_u32 s46, s45, 0x10000\nMaaa 16,208,240\nRa 168,10,40960\nMaaa 48,208,244\nD0 2,20,46,0\nMaaa 80,208,248\nRa 172,10,43008\n"
"Maaa 112,208,252\nMaaa 116,212,252\nRa 176,10,49152\nMaaa 84,212,248\nMaaa 52,212,244\nRa 180,10,51200\nMaaa 20,212,240\nMvaa 180,212,236\nRa 184,10,57344\nMvaa 148,212,232\n"
"D0 3,20,46,1024\nMvaa 116,212,228\nRa 188,10,59392\nMvaa 84,212,224\nMvaa 88,216,224\nMvaa 120,216,228\nMvaa 152,216,232\nMvaa 184,216,236\nMaaa 24,216,240\nMaaa 56,216,244\n"
"D0 4,20,46,2048\nMaaa 88,216,248\nMaaa 120,216,252\nMaaa 124,220,252\nMaaa 92,220,248\nMaaa 60,220,244\nMaaa 28,220,240\nMvaa 188,220,236\nMvaa 156,220,232\nD0 5,20,46,3072\n"
"Mvaa 124,220,228\nMvaa 92,220,224\ns_waitcnt vmcnt(20) lgkmcnt(0)\ns_barrier\nMvaa 64,128,160\nTa 192,12,49152\nMvaa 96,128,164\n"
"buffer_load_dwordx4 v[216:219], v253, s[40:43], s87 offen offset:0\nMvaa 128,128,168\nTa 194,12,51200\nMvaa 160,128,172\ns_add_u32 m0, s47, 16384\ns_add_u32 s44, s44, 0x40000\n"
"Maaa 0,128,176\nTa 196,13,49152\nMaaa 32,128,180\nD0 6,24,44,0\nMaaa 64,128,184\nTa 198,13,51200\nMaaa 96,128,188\nMaaa 100,132,188\nTa 200,14,49152\nMaaa 68,132,184\n"
"Maaa 36,132,180\nTa 202,14,51200\nMaaa 4,132,176\nMvaa 164,132,172\nTa 204,15,49152\nMvaa 132,132,168\nD0 7,24,44,1024\nMvaa 100,132,164\nTa 206,15,51200\nMvaa 68,132,160\n"
"Mvaa 72,136,160\nTa 208,16,49152\nMvaa 104,136,164\nMvaa 136,136,168\nTa 210,16,51200\nMvaa 168,136,172\nMaaa 8,136,176\nTa 212,17,49152\nMaaa 40,136,180\nD0 8,24,44,2048\n"
"Maaa 72,136,184\nTa 214,17,51200\nMaaa 104,136,188\nMaaa 108,140,188\nTa 216,18,49152\nMaaa 76,140,184\nMaaa 44,140,180\nTa 218,18,51200\nMaaa 12,140,176\nMvaa 172,140,172\n"
"Ta 220,19,49152\nMvaa 140,140,168\nD0 9,24,44,3072\nMvaa 108,140,164\nTa 222,19,51200\nMvaa 76,140,160\nMvaa 80,144,160\nRa 224,11,32768\nMvaa 112,144,164\nMvaa 144,144,168\n"
"Ra 228,11,34816\nMvaa 176,144,172\ns_add_u32 m0, s47, 32768\ns_add_u32 s44, s44, 0x40000\nMaaa 16,144,176\nRa 232,11,40960\nMaaa 48,144,180\nD0 6,24,44,0\nMaaa 80,144,184\n"
"Ra 236,11,43008\nMaaa 112,144,188\nMaaa 116,148,188\nRa 240,11,49152\nMaaa 84,148,184\nMaaa 52,148,180\nRa 244,11,51200\nMaaa 20,148,176\nSv 20,1,16,49,0\n"
"s_add_u32 s49, s49, 1024\nMvaa 180,148,172\nRa 248,11,57344\nMvaa 148,148,168\nD0 7,24,44,1024\nMvaa 116,148,164\nRa 252,11,59392\nMvaa 84,148,160\nMvaa 88,152,160\n"
"Mvaa 120,152,164\nSv 20,1,16,49,0\ns_add_u32 s49, s49, 1024\nMvaa 152,152,168\nMvaa 184,152,172\nMaaa 24,152,176\nMaaa 56,152,180\nD0 8,24,44,2048\nMaaa 88,152,184\n"
"Maaa 120,152,188\nMaaa 124,156,188\nMaaa 92,156,184\nSv 20,1,16,49,0\ns_add_u32 s49, s49, 1024\nMaaa 60,156,180\nMaaa 28,156,176\nMvaa 188,156,172\nMvaa 156,156,168\n"
"D0 9,24,44,3072\nMvaa 124,156,164\nMvaa 92,156,160\ns_waitcnt vmcnt(12) lgkmcnt(0)\ns_barrier\nMvaa 64,192,224\nTa 128,12,0\nMvaa 96,192,228\n"
"buffer_load_dwordx4 v[204:207], v253, s[36:39], s87 offen offset:3584\nMvaa 128,192,232\nTa 130,12,2048\nMvaa 160,192,236\ns_add_u32 m0, s48, 32768\ns_add_u32 s45, s45, 128\n"
"Maaa 0,192,240\nTa 132,13,0\nMaaa 32,192,244\nD0 2,20,45,0\nMaaa 64,192,248\nTa 134,13,2048\nMaaa 96,192,252\nMaaa 100,196,252\nTa 136,14,0\nMaaa 68,196,248\nMaaa 36,196,244\n"
"Ta 138,14,2048\nMaaa 4,196,240\nMvaa 164,196,236\nTa 140,15,0\nMvaa 132,196,232\nD0 3,20,45,1024\nMvaa 100,196,228\nTa 142,15,2048\nMvaa 68,196,224\nMvaa 72,200,224\nTa 144,16,0\n"
"Mvaa 104,200,228\nMvaa 136,200,232\nTa 146,16,2048\nMvaa 168,200,236\nMaaa 8,200,240\nTa 148,17,0\nMaaa 40,200,244\nD0 4,20,45,2048\nMaaa 72,200,248\nTa 150,17,2048\n"
"Maaa 104,200,252\nMaaa 108,204,252\nTa 152,18,0\nMaaa 76,204,248\nMaaa 44,204,244\nTa 154,18,2048\nMaaa 12,204,240\nMvaa 172,204,236\nTa 156,19,0\nMvaa 140,204,232\n"
"D0 5,20,45,3072\nMvaa 108,204,228\nTa 158,19,2048\nMvaa 76,204,224\nMvaa 80,208,224\nRa 160,10,0\nMvaa 112,208,228\nMvaa 144,208,232\nRa 164,10,2048\nMvaa 176,208,236\n"
"s_add_u32 m0, s48, 36864\ns_add_u32 s46, s45, 0x10000\nMaaa 16,208,240\nRa 168,10,8192\nMaaa 48,208,244\nD0 2,20,46,0\nMaaa 80,208,248\nRa 172,10,10240\nMaaa 112,208,252\n"
"Maaa 116,212,252\nRa 176,10,16384\nMaaa 84,212,248\nMaaa 52,212,244\nRa 180,10,18432\nMaaa 20,212,240\nMvaa 180,212,236\nRa 184,10,24576\nMvaa 148,212,232\nD0 3,20,46,1024\n"
"Mvaa 116,212,228\nRa 188,10,26624\nMvaa 84,212,224\nMvaa 88,216,224\nMvaa 120,216,228\nMvaa 152,216,232\nMvaa 184,216,236\nMaaa 24,216,240\nMaaa 56,216,244\nD0 4,20,46,2048\n"
"Maaa 88,216,248\nMaaa 120,216,252\nMaaa 124,220,252\nMaaa 92,220,248\nMaaa 60,220,244\nMaaa 28,220,240\nMvaa 188,220,236\nMvaa 156,220,232\nD0 5,20,46,3072\nMvaa 124,220,228\n"
"Mvaa 92,220,224\ns_waitcnt vmcnt(16) lgkmcnt(0)\ns_barrier\nMvaa 64,128,160\nTa 192,12,16384\nv_xor_b32 v48, s15, v33\nMvaa 96,128,164\n"
"buffer_load_dwordx4 v[220:223], v253, s[40:43], s87 offen offset:3584\nv_xor_b32 v49, s15, v35\nMvaa 128,128,168\nTa 194,12,18432\nv_xor_b32 v50, s15, v37\nMvaa 160,128,172\n"
"s_add_u32 m0, s47, 49152\ns_add_u32 s44, s44, 0x40000\nMaaa 0,128,176\nTa 196,13,16384\nv_xor_b32 v51, s15, v39\nMaaa 32,128,180\nD0 6,24,44,0\nv_xor_b32 v52, s15, v41\n"
"Maaa 64,128,184\nTa 198,13,18432\nv_xor_b32 v53, s15, v43\nMaaa 96,128,188\nv_xor_b32 v54, s15, v45\nv_xor_b32 v55, s15, v47\nMaaa 100,132,188\nTa 200,14,16384\n"
"v_or3_b32 v48, v48, v49, v50\nMaaa 68,132,184\nv_or3_b32 v51, v51, v52, v53\nv_or3_b32 v48, v48, v54, v55\nMaaa 36,132,180\nTa 202,14,18432\nv_or_b32 v48, v48, v51\n"
"Maaa 4,132,176\nv_cmp_ne_u32 vcc, 0, v48\nMvaa 164,132,172\nTa 204,15,16384\ns_cbranch_vccnz .S27\n.S28:\nMvaa 132,132,168\nD0 7,24,44,1024\nv_max_u32 v48, v32, v34\n"
"Mvaa 100,132,164\nTa 206,15,18432\nv_min_u32 v49, v32, v34\nMvaa 68,132,160\nv_med3_u32 v49, v48, v49, v36\nv_max_u32 v48, v48, v36\nMvaa 72,136,160\nTa 208,16,16384\n"
"v_med3_u32 v49, v48, v49, v38\nMvaa 104,136,164\nv_max_u32 v48, v48, v38\nv_med3_u32 v49, v48, v49, v40\nMvaa 136,136,168\nTa 210,16,18432\nv_max_u32 v48, v48, v40\n"
"Mvaa 168,136,172\nv_med3_u32 v49, v48, v49, v42\nv_max_u32 v48, v48, v42\nMaaa 8,136,176\nTa 212,17,16384\nv_med3_u32 v49, v48, v49, v44\nMaaa 40,136,180\nD0 8,24,44,2048\n"
"v_max_u32 v48, v48, v44\nMaaa 72,136,184\nTa 214,17,18432\nv_med3_u32 v49, v48, v49, v46\nMaaa 104,136,188\nv_max_u32 v48, v48, v46\nv_mov_b32 v50, v48\nMaaa 108,140,188\n"
"Ta 216,18,16384\nMaaa 76,140,184\nv_max_u32_dpp v50, v50, v50 row_ror:8 row_mask:0xf bank_mask:0xf\nMaaa 44,140,180\nTa 218,18,18432\nMaaa 12,140,176\n"
"v_max_u32_dpp v50, v50, v50 row_ror:4 row_mask:0xf bank_mask:0xf\nMvaa 172,140,172\nTa 220,19,16384\nMvaa 140,140,168\nD0 9,24,44,3072\n"
"v_max_u32_dpp v50, v50, v50 row_ror:2 row_mask:0xf bank_mask:0xf\nMvaa 108,140,164\nTa 222,19,18432\nMvaa 76,140,160\n"
"v_max_u32_dpp v50, v50, v50 row_ror:1 row_mask:0xf bank_mask:0xf\nv_mov_b32 v52, v50\nMvaa 80,144,160\nRa 224,11,0\nMvaa 112,144,164\nv_permlane16_swap_b32 v50, v52\n"
"v_max_u32 v50, v50, v52\nMvaa 144,144,168\nRa 228,11,2048\nv_mov_b32 v52, v50\nMvaa 176,144,172\ns_add_u32 m0, s47, 0\ns_add_u32 s44, s44, 0x40000\nMaaa 16,144,176\n"
"Ra 232,11,8192\nv_permlane32_swap_b32 v50, v52\nMaaa 48,144,180\nD0 6,24,44,0\nv_max_u32 v50, v50, v52\nMaaa 80,144,184\nRa 236,11,10240\nv_cmp_eq_u32 vcc, v48, v50\n"
"s_lshl_b32 s2, s11, 3\ns_add_u32 s2, s2, 131072\nv_cndmask_b32 v51, v48, v49, vcc\nMaaa 112,144,188\nMaaa 116,148,188\nRa 240,11,16384\n"
"v_max_u32_dpp v51, v51, v51 row_ror:8 row_mask:0xf bank_mask:0xf\nMaaa 84,148,184\nMaaa 52,148,180\nRa 244,11,18432\n"
"v_max_u32_dpp v51, v51, v51 row_ror:4 row_mask:0xf bank_mask:0xf\nMaaa 20,148,176\nSv 20,1,16,49,0\ns_add_u32 s49, s49, 1024\nMvaa 180,148,172\nRa 248,11,24576\n"
"v_max_u32_dpp v51, v51, v51 row_ror:2 row_mask:0xf bank_mask:0xf\nMvaa 148,148,168\nD0 7,24,44,1024\nMvaa 116,148,164\nRa 252,11,26624\n"
"v_max_u32_dpp v51, v51, v51 row_ror:1 row_mask:0xf bank_mask:0xf\nMvaa 84,148,160\nv_mov_b32 v52, v51\nMvaa 88,152,160\nMvaa 120,152,164\nSv 20,1,16,49,0\n"
"s_add_u32 s49, s49, 1024\nMvaa 152,152,168\nv_permlane16_swap_b32 v51, v52\nv_max_u32 v51, v51, v52\nMvaa 184,152,172\nv_mov_b32 v52, v51\nMaaa 24,152,176\nMaaa 56,152,180\n"
"D0 8,24,44,2048\nv_permlane32_swap_b32 v51, v52\nMaaa 88,152,184\nv_max_u32 v51, v51, v52\nv_mov_b32 v52, s2\nMaaa 120,152,188\nds_write_b64 v52, v[50:51]\nv_mov_b32 v52, 131072\n"
"Maaa 124,156,188\nMaaa 92,156,184\nSv 20,1,16,49,0\ns_add_u32 s49, s49, 1024\nMaaa 60,156,180\nMaaa 28,156,176\nMvaa 188,156,172\nMvaa 156,156,168\nD0 9,24,44,3072\n"
"Mvaa 124,156,164\nMvaa 92,156,160\ns_waitcnt vmcnt(12) lgkmcnt(1)\ns_barrier\nMvaa 64,192,224\nTa 128,12,32768\nMvaa 96,192,228\n"
"buffer_load_dwordx4 v[224:227], v252, s[92:95], s88 offen offset:0\nMvaa 128,192,232\nTa 130,12,34816\nMvaa 160,192,236\ns_add_u32 m0, s48, 0\ns_add_u32 s45, s45, 128\n"
"Maaa 0,192,240\nTa 132,13,32768\nMaaa 32,192,244\nD0 2,20,45,0\nMaaa 64,192,248\nTa 134,13,34816\nMaaa 96,192,252\nMaaa 100,196,252\nTa 136,14,32768\nMaaa 68,196,248\n"
"Maaa 36,196,244\nTa 138,14,34816\nMaaa 4,196,240\nMvaa 164,196,236\nTa 140,15,32768\nMvaa 132,196,232\nD0 3,20,45,1024\nMvaa 100,196,228\nTa 142,15,34816\nMvaa 68,196,224\n"
"Mvaa 72,200,224\nTa 144,16,32768\nMvaa 104,200,228\nMvaa 136,200,232\nTa 146,16,34816\nMvaa 168,200,236\nMaaa 8,200,240\nTa 148,17,32768\nMaaa 40,200,244\nD0 4,20,45,2048\n"
"Maaa 72,200,248\nTa 150,17,34816\nMaaa 104,200,252\nMaaa 108,204,252\nTa 152,18,32768\nMaaa 76,204,248\nMaaa 44,204,244\nTa 154,18,34816\nMaaa 12,204,240\nMvaa 172,204,236\n"
"Ta 156,19,32768\nMvaa 140,204,232\nD0 5,20,45,3072\nMvaa 108,204,228\nTa 158,19,34816\nMvaa 76,204,224\nMvaa 80,208,224\nRa 160,10,32768\nMvaa 112,208,228\n"
"buffer_load_dwordx4 v[228:231], v252, s[92:95], s88 offen offset:896\nMvaa 144,208,232\nRa 164,10,34816\nMvaa 176,208,236\ns_add_u32 m0, s48, 4096\ns_add_u32 s46, s45, 0x10000\n"
"Maaa 16,208,240\nRa 168,10,40960\nMaaa 48,208,244\nD0 2,20,46,0\nMaaa 80,208,248\nRa 172,10,43008\nMaaa 112,208,252\nMaaa 116,212,252\nRa 176,10,49152\nMaaa 84,212,248\n"
"Maaa 52,212,244\nRa 180,10,51200\nMaaa 20,212,240\nMvaa 180,212,236\nRa 184,10,57344\nMvaa 148,212,232\nD0 3,20,46,1024\nMvaa 116,212,228\nRa 188,10,59392\nMvaa 84,212,224\n"
"Mvaa 88,216,224\nMvaa 120,216,228\nMvaa 152,216,232\nMvaa 184,216,236\nMaaa 24,216,240\nMaaa 56,216,244\nD0 4,20,46,2048\nMaaa 88,216,248\nMaaa 120,216,252\nMaaa 124,220,252\n"
"Maaa 92,220,248\nMaaa 60,220,244\nMaaa 28,220,240\nMvaa 188,220,236\nMvaa 156,220,232\nD0 5,20,46,3072\nMvaa 124,220,228\nMvaa 92,220,224\ns_waitcnt vmcnt(17) lgkmcnt(0)\n"
"s_barrier\nMvaa 64,128,160\nTa 192,12,49152\nRv 32,52,0\nMvaa 96,128,164\nbuffer_load_dwordx4 v[232:235], v252, s[92:95], s88 offen offset:1792\nRv 36,52,16\nMvaa 128,128,168\n"
"Ta 194,12,51200\nMvaa 160,128,172\ns_add_u32 m0, s47, 16384\ns_add_u32 s44, s44, 0x40000\nMaaa 0,128,176\nTa 196,13,49152\nMaaa 32,128,180\nD0 6,24,44,0\nMaaa 64,128,184\n"
"Ta 198,13,51200\nMaaa 96,128,188\nMaaa 100,132,188\nTa 200,14,49152\nMaaa 68,132,184\nMaaa 36,132,180\nTa 202,14,51200\nMaaa 4,132,176\nMvaa 164,132,172\nTa 204,15,49152\n"
"Mvaa 132,132,168\nD0 7,24,44,1024\nWL 6\nMvaa 100,132,164\nTa 206,15,51200\nv_max_u32 v48, v32, v33\nMvaa 68,132,160\nv_min_u32 v49, v32, v33\nv_med3_u32 v49, v48, v49, v34\n"
"Mvaa 72,136,160\nTa 208,16,49152\nv_max_u32 v48, v48, v34\nMvaa 104,136,164\nv_med3_u32 v49, v48, v49, v35\nv_max_u32 v48, v48, v35\nMvaa 136,136,168\nTa 210,16,51200\n"
"v_med3_u32 v49, v48, v49, v36\nMvaa 168,136,172\nv_max_u32 v48, v48, v36\nv_med3_u32 v49, v48, v49, v37\nMaaa 8,136,176\nTa 212,17,49152\nv_max_u32 v48, v48, v37\nMaaa 40,136,180\n"
"D0 8,24,44,2048\nv_med3_u32 v49, v48, v49, v38\nMaaa 72,136,184\nTa 214,17,51200\nv_max_u32 v48, v48, v38\nMaaa 104,136,188\nv_med3_u32 v49, v48, v49, v39\n"
"v_max_u32 v48, v48, v39\nMaaa 108,140,188\nTa 216,18,49152\nv_readfirstlane_b32 s2, v48\nMaaa 76,140,184\nv_readfirstlane_b32 s3, v49\nMaaa 44,140,180\nTa 218,18,51200\n"
"Maaa 12,140,176\nMvaa 172,140,172\nTa 220,19,49152\ns_andn2_b32 s2, 0x7ff, s2\nMvaa 140,140,168\nD0 9,24,44,3072\ns_andn2_b32 s3, 0x7ff, s3\nMvaa 108,140,164\nTa 222,19,51200\n"
"s_lshl_b32 s71, s3, 16\nMvaa 76,140,160\ns_or_b32 s71, s71, s2\ns_mul_i32 s2, s2, 896\nMvaa 80,144,160\nRa 224,11,32768\ns_mul_i32 s3, s3, 896\nMvaa 112,144,164\n"
"buffer_load_dwordx4 v[236:239], v252, s[92:95], s88 offen offset:2688\ns_mul_i32 s6, s11, 224\nMvaa 144,144,168\nRa 228,11,34816\ns_add_u32 s2, s2, s6\nMvaa 176,144,172\n"
"s_add_u32 m0, s47, 32768\ns_add_u32 s44, s44, 0x40000\nMaaa 16,144,176\nRa 232,11,40960\ns_add_u32 s3, s3, s6\nMaaa 48,144,180\nD0 6,24,44,0\n"
"buffer_load_dwordx2 v[240:241], v255, s[28:31], s2 offen\nMaaa 80,144,184\nRa 236,11,43008\nMaaa 112,144,188\nMaaa 116,148,188\nRa 240,11,49152\nMaaa 84,148,184\n"
"buffer_load_dwordx2 v[242:243], v255, s[28:31], s3 offen\ns_lshl2_add_u32 s2, s10, s11\ns_cmp_eq_u32 s2, 0\ns_cselect_b32 s2, 0, 0x40000000\ns_add_u32 s2, s2, 0x5c48000\n"
"Maaa 52,148,180\nRa 244,11,51200\nMaaa 20,148,176\nSv 20,1,16,49,0\ns_add_u32 s49, s49, 1024\nMvaa 180,148,172\nRa 248,11,57344\nMvaa 148,148,168\nD0 7,24,44,1024\n"
"buffer_store_dword v25, off, s[16:19], s2 offset:0 sc0 sc1\nMvaa 116,148,164\nRa 252,11,59392\nMvaa 84,148,160\nMvaa 88,152,160\nMvaa 120,152,164\nSv 20,1,16,49,0\n"
"s_add_u32 s49, s49, 1024\nMvaa 152,152,168\nMvaa 184,152,172\nMaaa 24,152,176\nMaaa 56,152,180\nD0 8,24,44,2048\nMaaa 88,152,184\nMaaa 120,152,188\nMaaa 124,156,188\n"
"Maaa 92,156,184\nSv 20,1,16,49,0\ns_add_u32 s49, s49, 1024\nMaaa 60,156,180\nMaaa 28,156,176\nMvaa 188,156,172\nMvaa 156,156,168\nD0 9,24,44,3072\nMvaa 124,156,164\n"
"Mvaa 92,156,160\ns_waitcnt vmcnt(16) lgkmcnt(0)\ns_barrier\nMvaa 64,192,224\nTa 128,12,0\nMvaa 96,192,228\nMvaa 128,192,232\nTa 130,12,2048\nMvaa 160,192,236\n"
"s_add_u32 m0, s48, 32768\ns_add_u32 s45, s45, 128\nMaaa 0,192,240\nTa 132,13,0\nMaaa 32,192,244\nD0 2,20,45,0\nMaaa 64,192,248\nTa 134,13,2048\nMaaa 96,192,252\nMaaa 100,196,252\n"
"Ta 136,14,0\nMaaa 68,196,248\nMaaa 36,196,244\nTa 138,14,2048\nMaaa 4,196,240\nMvaa 164,196,236\nTa 140,15,0\nMvaa 132,196,232\nD0 3,20,45,1024\nMvaa 100,196,228\nTa 142,15,2048\n"
"Mvaa 68,196,224\nMvaa 72,200,224\nTa 144,16,0\nMvaa 104,200,228\nMvaa 136,200,232\nTa 146,16,2048\nMvaa 168,200,236\nMaaa 8,200,240\nTa 148,17,0\nMaaa 40,200,244\nD0 4,20,45,2048\n"
"Maaa 72,200,248\nTa 150,17,2048\nMaaa 104,200,252\nMaaa 108,204,252\nTa 152,18,0\nMaaa 76,204,248\nMaaa 44,204,244\nTa 154,18,2048\nMaaa 12,204,240\nMvaa 172,204,236\nTa 156,19,0\n"
"Mvaa 140,204,232\nD0 5,20,45,3072\nMvaa 108,204,228\nTa 158,19,2048\nMvaa 76,204,224\nMvaa 80,208,224\nRa 160,10,0\nMvaa 112,208,228\nMvaa 144,208,232\nRa 164,10,2048\n"
"Mvaa 176,208,236\ns_add_u32 m0, s48, 36864\ns_add_u32 s46, s45, 0x10000\nMaaa 16,208,240\nRa 168,10,8192\nMaaa 48,208,244\nD0 2,20,46,0\nMaaa 80,208,248\nRa 172,10,10240\n"
"Maaa 112,208,252\nMaaa 116,212,252\nRa 176,10,16384\nMaaa 84,212,248\nMaaa 52,212,244\nRa 180,10,18432\nMaaa 20,212,240\nMvaa 180,212,236\nRa 184,10,24576\nMvaa 148,212,232\n"
"D0 3,20,46,1024\nMvaa 116,212,228\nRa 188,10,26624\nMvaa 84,212,224\nMvaa 88,216,224\nMvaa 120,216,228\nMvaa 152,216,232\nMvaa 184,216,236\nMaaa 24,216,240\nMaaa 56,216,244\n"
"D0 4,20,46,2048\nMaaa 88,216,248\nMaaa 120,216,252\nMaaa 124,220,252\nMaaa 92,220,248\nMaaa 60,220,244\nMaaa 28,220,240\nMvaa 188,220,236\nMvaa 156,220,232\nD0 5,20,46,3072\n"
"Mvaa 124,220,228\nMvaa 92,220,224\ns_waitcnt vmcnt(19) lgkmcnt(0)\ns_barrier\nMvaa 64,128,160\nTa 192,12,16384\nMvaa 96,128,164\nMvaa 128,128,168\nTa 194,12,18432\n"
"Mvaa 160,128,172\ns_add_u32 m0, s47, 49152\ns_add_u32 s44, s44, 0x40000\nMaaa 0,128,176\nTa 196,13,16384\nMaaa 32,128,180\nD0 6,24,44,0\nMaaa 64,128,184\nTa 198,13,18432\n"
"Maaa 96,128,188\nMaaa 100,132,188\nTa 200,14,16384\nMaaa 68,132,184\nMaaa 36,132,180\nTa 202,14,18432\nMaaa 4,132,176\nMvaa 164,132,172\nTa 204,15,16384\nMvaa 132,132,168\n"
"D0 7,24,44,1024\nMvaa 100,132,164\nTa 206,15,18432\nMvaa 68,132,160\nMvaa 72,136,160\nTa 208,16,16384\nMvaa 104,136,164\nMvaa 136,136,168\nTa 210,16,18432\nMvaa 168,136,172\n"
"Maaa 8,136,176\nTa 212,17,16384\nMaaa 40,136,180\nD0 8,24,44,2048\nMaaa 72,136,184\nTa 214,17,18432\nMaaa 104,136,188\nMaaa 108,140,188\nTa 216,18,16384\nMaaa 76,140,184\n"
"Maaa 44,140,180\nTa 218,18,18432\nMaaa 12,140,176\nMvaa 172,140,172\nTa 220,19,16384\nMvaa 140,140,168\nD0 9,24,44,3072\nMvaa 108,140,164\nTa 222,19,18432\nMvaa 76,140,160\n"
"Mvaa 80,144,160\nRa 224,11,0\nMvaa 112,144,164\nMvaa 144,144,168\nRa 228,11,2048\nMvaa 176,144,172\ns_add_u32 m0, s47, 0\ns_add_u32 s44, s44, 0x40000\nMaaa 16,144,176\n"
"Ra 232,11,8192\nMaaa 48,144,180\nD0 6,24,44,0\nMaaa 80,144,184\nRa 236,11,10240\nMaaa 112,144,188\nMaaa 116,148,188\nRa 240,11,16384\nMaaa 84,148,184\nMaaa 52,148,180\n"
"Ra 244,11,18432\nMaaa 20,148,176\nSv 20,1,16,49,0\ns_add_u32 s49, s49, 1024\nMvaa 180,148,172\nRa 248,11,24576\nMvaa 148,148,168\nD0 7,24,44,1024\nMvaa 116,148,164\n"
"Ra 252,11,26624\nMvaa 84,148,160\nMvaa 88,152,160\nMvaa 120,152,164\nSv 20,1,16,49,0\ns_add_u32 s49, s49, 1024\nMvaa 152,152,168\nMvaa 184,152,172\nMaaa 24,152,176\n"
"Maaa 56,152,180\nD0 8,24,44,2048\nMaaa 88,152,184\nMaaa 120,152,188\nMaaa 124,156,188\nMaaa 92,156,184\nSv 20,1,16,49,0\ns_add_u32 s49, s49, 1024\nMaaa 60,156,180\n"
"Maaa 28,156,176\nMvaa 188,156,172\nMvaa 156,156,168\nD0 9,24,44,3072\nMvaa 124,156,164\nMvaa 92,156,160\ns_waitcnt vmcnt(11) lgkmcnt(0)\ns_barrier\nMvaa 64,192,224\n"
"Ta 128,12,32768\nMvaa 96,192,228\nv_lshlrev_b32 v244, 16, v240\nv_and_b32 v245, 0xffff0000, v240\nMvaa 128,192,232\nTa 130,12,34816\nv_lshlrev_b32 v246, 16, v241\n"
"Mvaa 160,192,236\ns_add_u32 m0, s48, 0\ns_add_u32 s45, s45, 128\nMaaa 0,192,240\nTa 132,13,32768\nv_and_b32 v247, 0xffff0000, v241\nMaaa 32,192,244\nD0 2,20,45,0\n"
"v_lshlrev_b32 v248, 16, v242\nMaaa 64,192,248\nTa 134,13,34816\nv_and_b32 v249, 0xffff0000, v242\nMaaa 96,192,252\nv_lshlrev_b32 v250, 16, v243\nv_and_b32 v251, 0xffff0000, v243\n"
"Maaa 100,196,252\nTa 136,14,32768\nv_lshlrev_b32 v252, 16, v192\nMaaa 68,196,248\nv_and_b32 v253, 0xffff0000, v192\nMU 32,244,252\nMaaa 36,196,244\nTa 138,14,34816\nMU 33,244,253\n"
"Maaa 4,196,240\nMU 40,248,252\nMU 41,248,253\nMvaa 164,196,236\nTa 140,15,32768\nv_lshlrev_b32 v252, 16, v193\nMvaa 132,196,232\nD0 3,20,45,1024\nv_and_b32 v253, 0xffff0000, v193\n"
"Mvaa 100,196,228\nTa 142,15,34816\nMU 34,244,252\nMvaa 68,196,224\nMU 35,244,253\nMU 42,248,252\nMvaa 72,200,224\nTa 144,16,32768\nMU 43,248,253\nMvaa 104,200,228\n"
"v_lshlrev_b32 v252, 16, v194\nv_and_b32 v253, 0xffff0000, v194\nMvaa 136,200,232\nTa 146,16,34816\nMU 36,244,252\nMvaa 168,200,236\nMU 37,244,253\nMU 44,248,252\nMaaa 8,200,240\n"
"Ta 148,17,32768\nMU 45,248,253\nMaaa 40,200,244\nD0 4,20,45,2048\nv_lshlrev_b32 v252, 16, v195\nMaaa 72,200,248\nTa 150,17,34816\nv_and_b32 v253, 0xffff0000, v195\n"
"Maaa 104,200,252\nMU 38,244,252\nMU 39,244,253\nMaaa 108,204,252\nTa 152,18,32768\nMU 46,248,252\nMaaa 76,204,248\nMU 47,248,253\nv_lshlrev_b32 v252, 16, v208\nMaaa 44,204,244\n"
"Ta 154,18,34816\nv_and_b32 v253, 0xffff0000, v208\nMaaa 12,204,240\nMU 48,244,252\nMU 49,244,253\nMvaa 172,204,236\nTa 156,19,32768\nMU 56,248,252\nMvaa 140,204,232\n"
"D0 5,20,45,3072\nMU 57,248,253\nMvaa 108,204,228\nTa 158,19,34816\nv_lshlrev_b32 v252, 16, v209\nMvaa 76,204,224\nv_and_b32 v253, 0xffff0000, v209\nMU 50,244,252\nMvaa 80,208,224\n"
"Ra 160,10,32768\nMU 51,244,253\nMvaa 112,208,228\nMU 58,248,252\nMU 59,248,253\nMvaa 144,208,232\nRa 164,10,34816\nv_lshlrev_b32 v252, 16, v210\nMvaa 176,208,236\n"
"s_add_u32 m0, s48, 4096\ns_add_u32 s46, s45, 0x10000\nMaaa 16,208,240\nRa 168,10,40960\nv_and_b32 v253, 0xffff0000, v210\nMaaa 48,208,244\nD0 2,20,46,0\nMU 52,244,252\n"
"Maaa 80,208,248\nRa 172,10,43008\nMU 53,244,253\nMaaa 112,208,252\nMU 60,248,252\nMU 61,248,253\nMaaa 116,212,252\nRa 176,10,49152\nv_lshlrev_b32 v252, 16, v211\nMaaa 84,212,248\n"
"v_and_b32 v253, 0xffff0000, v211\nMU 54,244,252\nMaaa 52,212,244\nRa 180,10,51200\nMU 55,244,253\nMaaa 20,212,240\nMU 62,248,252\nMU 63,248,253\nMvaa 180,212,236\nRa 184,10,57344\n"
"v_lshlrev_b32 v252, 16, v196\nMvaa 148,212,232\nD0 3,20,46,1024\nv_and_b32 v253, 0xffff0000, v196\nMvaa 116,212,228\nRa 188,10,59392\nFM 32,245,252\nMvaa 84,212,224\n"
"FM 33,245,253\nFM 40,249,252\nMvaa 88,216,224\nFM 41,249,253\nv_lshlrev_b32 v252, 16, v197\nMvaa 120,216,228\nv_and_b32 v253, 0xffff0000, v197\nFM 34,245,252\nMvaa 152,216,232\n"
"FM 35,245,253\nFM 42,249,252\nMvaa 184,216,236\nFM 43,249,253\nv_lshlrev_b32 v252, 16, v198\nMaaa 24,216,240\nv_and_b32 v253, 0xffff0000, v198\nFM 36,245,252\nMaaa 56,216,244\n"
"D0 4,20,46,2048\nFM 37,245,253\nMaaa 88,216,248\nFM 44,249,252\nFM 45,249,253\nMaaa 120,216,252\nv_lshlrev_b32 v252, 16, v199\nv_and_b32 v253, 0xffff0000, v199\nMaaa 124,220,252\n"
"FM 38,245,252\nFM 39,245,253\nMaaa 92,220,248\nFM 46,249,252\nFM 47,249,253\nMaaa 60,220,244\nv_lshlrev_b32 v252, 16, v212\nv_and_b32 v253, 0xffff0000, v212\nMaaa 28,220,240\n"
"FM 48,245,252\nFM 49,245,253\nMvaa 188,220,236\nFM 56,249,252\nFM 57,249,253\nMvaa 156,220,232\nD0 5,20,46,3072\nv_lshlrev_b32 v252, 16, v213\nMvaa 124,220,228\n"
"v_and_b32 v253, 0xffff0000, v213\nFM 50,245,252\nMvaa 92,220,224\ns_waitcnt vmcnt(15) lgkmcnt(0)\ns_barrier\nMvaa 64,128,160\nTa 192,12,49152\nFM 51,245,253\nMvaa 96,128,164\n"
"FM 58,249,252\nFM 59,249,253\nMvaa 128,128,168\nTa 194,12,51200\nv_lshlrev_b32 v252, 16, v214\nMvaa 160,128,172\ns_add_u32 m0, s47, 16384\ns_add_u32 s44, s44, 0x40000\n"
"Maaa 0,128,176\nTa 196,13,49152\nv_and_b32 v253, 0xffff0000, v214\nMaaa 32,128,180\nD0 6,24,44,0\nFM 52,245,252\nMaaa 64,128,184\nTa 198,13,51200\nFM 53,245,253\nMaaa 96,128,188\n"
"FM 60,249,252\nFM 61,249,253\nMaaa 100,132,188\nTa 200,14,49152\nv_lshlrev_b32 v252, 16, v215\nMaaa 68,132,184\nv_and_b32 v253, 0xffff0000, v215\nFM 54,245,252\nMaaa 36,132,180\n"
"Ta 202,14,51200\nFM 55,245,253\nMaaa 4,132,176\nFM 62,249,252\nFM 63,249,253\nMvaa 164,132,172\nTa 204,15,49152\nv_lshlrev_b32 v252, 16, v200\nMvaa 132,132,168\nD0 7,24,44,1024\n"
"v_and_b32 v253, 0xffff0000, v200\nMvaa 100,132,164\nTa 206,15,51200\nFM 32,246,252\nMvaa 68,132,160\nFM 33,246,253\nFM 40,250,252\nMvaa 72,136,160\nTa 208,16,49152\nFM 41,250,253\n"
"Mvaa 104,136,164\nv_lshlrev_b32 v252, 16, v201\nv_and_b32 v253, 0xffff0000, v201\nMvaa 136,136,168\nTa 210,16,51200\nFM 34,246,252\nMvaa 168,136,172\nFM 35,246,253\nFM 42,250,252\n"
"Maaa 8,136,176\nTa 212,17,49152\nFM 43,250,253\nMaaa 40,136,180\nD0 8,24,44,2048\nv_lshlrev_b32 v252, 16, v202\nMaaa 72,136,184\nTa 214,17,51200\nv_and_b32 v253, 0xffff0000, v202\n"
"Maaa 104,136,188\nFM 36,246,252\nFM 37,246,253\nMaaa 108,140,188\nTa 216,18,49152\nFM 44,250,252\nMaaa 76,140,184\nFM 45,250,253\nv_lshlrev_b32 v252, 16, v203\nMaaa 44,140,180\n"
"Ta 218,18,51200\nv_and_b32 v253, 0xffff0000, v203\nMaaa 12,140,176\nFM 38,246,252\nFM 39,246,253\nMvaa 172,140,172\nTa 220,19,49152\nFM 46,250,252\nMvaa 140,140,168\n"
"D0 9,24,44,3072\nFM 47,250,253\nMvaa 108,140,164\nTa 222,19,51200\nv_lshlrev_b32 v252, 16, v216\nMvaa 76,140,160\nv_and_b32 v253, 0xffff0000, v216\nFM 48,246,252\nMvaa 80,144,160\n"
"Ra 224,11,32768\nFM 49,246,253\nMvaa 112,144,164\nFM 56,250,252\nFM 57,250,253\nMvaa 144,144,168\nRa 228,11,34816\nv_lshlrev_b32 v252, 16, v217\nMvaa 176,144,172\n"
"s_add_u32 m0, s47, 32768\ns_add_u32 s44, s44, 0x40000\nMaaa 16,144,176\nRa 232,11,40960\nv_and_b32 v253, 0xffff0000, v217\nMaaa 48,144,180\nD0 6,24,44,0\nFM 50,246,252\n"
"Maaa 80,144,184\nRa 236,11,43008\nFM 51,246,253\nMaaa 112,144,188\nFM 58,250,252\nFM 59,250,253\nMaaa 116,148,188\nRa 240,11,49152\nv_lshlrev_b32 v252, 16, v218\nMaaa 84,148,184\n"
"v_and_b32 v253, 0xffff0000, v218\nFM 52,246,252\nMaaa 52,148,180\nRa 244,11,51200\nFM 53,246,253\nMaaa 20,148,176\nSv 20,1,16,49,0\ns_add_u32 s49, s49, 1024\nMvaa 180,148,172\n"
"Ra 248,11,57344\nFM 60,250,252\nMvaa 148,148,168\nD0 7,24,44,1024\nFM 61,250,253\nMvaa 116,148,164\nRa 252,11,59392\nv_lshlrev_b32 v252, 16, v219\nMvaa 84,148,160\n"
"v_and_b32 v253, 0xffff0000, v219\nFM 54,246,252\nMvaa 88,152,160\nFM 55,246,253\nFM 62,250,252\nMvaa 120,152,164\nSv 20,1,16,49,0\ns_add_u32 s49, s49, 1024\nMvaa 152,152,168\n"
"FM 63,250,253\nv_lshlrev_b32 v252, 16, v204\nMvaa 184,152,172\nv_and_b32 v253, 0xffff0000, v204\nFM 32,247,252\nMaaa 24,152,176\nFM 33,247,253\nFM 40,251,252\nMaaa 56,152,180\n"
"D0 8,24,44,2048\nFM 41,251,253\nMaaa 88,152,184\nv_lshlrev_b32 v252, 16, v205\nv_and_b32 v253, 0xffff0000, v205\nMaaa 120,152,188\nFM 34,247,252\nFM 35,247,253\nMaaa 124,156,188\n"
"FM 42,251,252\nFM 43,251,253\nMaaa 92,156,184\nSv 20,1,16,49,0\ns_add_u32 s49, s49, 1024\nMaaa 60,156,180\nv_lshlrev_b32 v252, 16, v206\nv_and_b32 v253, 0xffff0000, v206\n"
"Maaa 28,156,176\nFM 36,247,252\nFM 37,247,253\nMvaa 188,156,172\nFM 44,251,252\nFM 45,251,253\nMvaa 156,156,168\nD0 9,24,44,3072\nv_lshlrev_b32 v252, 16, v207\nMvaa 124,156,164\n"
"v_and_b32 v253, 0xffff0000, v207\nFM 38,247,252\nMvaa 92,156,160\ns_waitcnt vmcnt(11) lgkmcnt(0)\ns_barrier\nMvaa 64,192,224\nTa 128,12,0\nFM 39,247,253\nMvaa 96,192,228\n"
"FM 46,251,252\nFM 47,251,253\nMvaa 128,192,232\nTa 130,12,2048\nv_lshlrev_b32 v252, 16, v220\nMvaa 160,192,236\ns_add_u32 m0, s48, 32768\ns_add_u32 s45, s45, 128\nMaaa 0,192,240\n"
"Ta 132,13,0\nv_and_b32 v253, 0xffff0000, v220\nMaaa 32,192,244\nD0 2,20,45,0\nFM 48,247,252\nMaaa 64,192,248\nTa 134,13,2048\nFM 49,247,253\nMaaa 96,192,252\nFM 56,251,252\n"
"FM 57,251,253\nMaaa 100,196,252\nTa 136,14,0\nv_lshlrev_b32 v252, 16, v221\nMaaa 68,196,248\nv_and_b32 v253, 0xffff0000, v221\nFM 50,247,252\nMaaa 36,196,244\nTa 138,14,2048\n"
"FM 51,247,253\nMaaa 4,196,240\nFM 58,251,252\nFM 59,251,253\nMvaa 164,196,236\nTa 140,15,0\nv_lshlrev_b32 v252, 16, v222\nMvaa 132,196,232\nD0 3,20,45,1024\n"
"v_and_b32 v253, 0xffff0000, v222\nMvaa 100,196,228\nTa 142,15,2048\nFM 52,247,252\nMvaa 68,196,224\nFM 53,247,253\nFM 60,251,252\nMvaa 72,200,224\nTa 144,16,0\nFM 61,251,253\n"
"Mvaa 104,200,228\nv_lshlrev_b32 v252, 16, v223\nv_and_b32 v253, 0xffff0000, v223\nMvaa 136,200,232\nTa 146,16,2048\nFM 54,247,252\nMvaa 168,200,236\nFM 55,247,253\nFM 62,251,252\n"
"Maaa 8,200,240\nTa 148,17,0\nFM 63,251,253\nMaaa 40,200,244\nD0 4,20,45,2048\nv_permlane32_swap_b32 v32, v48\nMaaa 72,200,248\nTa 150,17,2048\nv_permlane32_swap_b32 v33, v49\n"
"Maaa 104,200,252\nv_permlane32_swap_b32 v34, v50\nv_permlane32_swap_b32 v35, v51\nMaaa 108,204,252\nTa 152,18,0\nv_permlane32_swap_b32 v36, v52\nMaaa 76,204,248\n"
"v_permlane32_swap_b32 v37, v53\nv_permlane32_swap_b32 v38, v54\nMaaa 44,204,244\nTa 154,18,2048\nv_permlane32_swap_b32 v39, v55\nMaaa 12,204,240\nv_permlane32_swap_b32 v40, v56\n"
"v_permlane32_swap_b32 v41, v57\nMvaa 172,204,236\nTa 156,19,0\nv_permlane32_swap_b32 v42, v58\nMvaa 140,204,232\nD0 5,20,45,3072\nv_permlane32_swap_b32 v43, v59\nMvaa 108,204,228\n"
"Ta 158,19,2048\nv_permlane32_swap_b32 v44, v60\nMvaa 76,204,224\nv_permlane32_swap_b32 v45, v61\nv_permlane32_swap_b32 v46, v62\nMvaa 80,208,224\nRa 160,10,0\n"
"v_permlane32_swap_b32 v47, v63\nMvaa 112,208,228\nAD 32,32,48\nAD 33,33,49\nMvaa 144,208,232\nRa 164,10,2048\nAD 34,34,50\nMvaa 176,208,236\ns_add_u32 m0, s48, 36864\n"
"s_add_u32 s46, s45, 0x10000\nMaaa 16,208,240\nRa 168,10,8192\nAD 35,35,51\nMaaa 48,208,244\nD0 2,20,46,0\nAD 36,36,52\nMaaa 80,208,248\nRa 172,10,10240\nAD 37,37,53\n"
"Maaa 112,208,252\nAD 38,38,54\nAD 39,39,55\nMaaa 116,212,252\nRa 176,10,16384\nAD 40,40,56\nMaaa 84,212,248\nAD 41,41,57\nAD 42,42,58\nMaaa 52,212,244\nRa 180,10,18432\n"
"AD 43,43,59\nMaaa 20,212,240\nAD 44,44,60\nAD 45,45,61\nMvaa 180,212,236\nRa 184,10,24576\nAD 46,46,62\nMvaa 148,212,232\nD0 3,20,46,1024\nAD 47,47,63\nMvaa 116,212,228\n"
"Ra 188,10,26624\nv_permlane16_swap_b32 v32, v40\nMvaa 84,212,224\nv_permlane16_swap_b32 v33, v41\nv_permlane16_swap_b32 v34, v42\nMvaa 88,216,224\nv_permlane16_swap_b32 v35, v43\n"
"v_permlane16_swap_b32 v36, v44\nMvaa 120,216,228\nv_permlane16_swap_b32 v37, v45\nv_permlane16_swap_b32 v38, v46\nMvaa 152,216,232\nv_permlane16_swap_b32 v39, v47\nAD 32,32,40\n"
"Mvaa 184,216,236\nAD 33,33,41\nAD 34,34,42\nMaaa 24,216,240\nAD 35,35,43\nAD 36,36,44\nMaaa 56,216,244\nD0 4,20,46,2048\nAD 37,37,45\nMaaa 88,216,248\nAD 38,38,46\nAD 39,39,47\n"
"Maaa 120,216,252\nv_add_f32_dpp v32, v32, v32 row_ror:8 row_mask:0xf bank_mask:0xf\nv_add_f32_dpp v33, v33, v33 row_ror:8 row_mask:0xf bank_mask:0xf\nMaaa 124,220,252\n"
"v_add_f32_dpp v34, v34, v34 row_ror:8 row_mask:0xf bank_mask:0xf\nv_add_f32_dpp v35, v35, v35 row_ror:8 row_mask:0xf bank_mask:0xf\nMaaa 92,220,248\n"
"v_add_f32_dpp v36, v36, v36 row_ror:8 row_mask:0xf bank_mask:0xf\nv_add_f32_dpp v37, v37, v37 row_ror:8 row_mask:0xf bank_mask:0xf\nMaaa 60,220,244\n"
"v_add_f32_dpp v38, v38, v38 row_ror:8 row_mask:0xf bank_mask:0xf\nv_add_f32_dpp v39, v39, v39 row_ror:8 row_mask:0xf bank_mask:0xf\nMaaa 28,220,240\n"
"v_add_f32_dpp v32, v32, v32 row_ror:4 row_mask:0xf bank_mask:0xf\nv_add_f32_dpp v33, v33, v33 row_ror:4 row_mask:0xf bank_mask:0xf\nMvaa 188,220,236\n"
"v_add_f32_dpp v34, v34, v34 row_ror:4 row_mask:0xf bank_mask:0xf\nv_add_f32_dpp v35, v35, v35 row_ror:4 row_mask:0xf bank_mask:0xf\nMvaa 156,220,232\nD0 5,20,46,3072\n"
"v_add_f32_dpp v36, v36, v36 row_ror:4 row_mask:0xf bank_mask:0xf\nMvaa 124,220,228\nv_add_f32_dpp v37, v37, v37 row_ror:4 row_mask:0xf bank_mask:0xf\n"
"v_add_f32_dpp v38, v38, v38 row_ror:4 row_mask:0xf bank_mask:0xf\nMvaa 92,220,224\ns_waitcnt vmcnt(15) lgkmcnt(0)\ns_barrier\nMvaa 64,128,160\nTa 192,12,16384\n"
"v_add_f32_dpp v39, v39, v39 row_ror:4 row_mask:0xf bank_mask:0xf\nMvaa 96,128,164\nv_add_f32_dpp v32, v32, v32 row_ror:2 row_mask:0xf bank_mask:0xf\n"
"v_add_f32_dpp v33, v33, v33 row_ror:2 row_mask:0xf bank_mask:0xf\nMvaa 128,128,168\nTa 194,12,18432\nv_add_f32_dpp v34, v34, v34 row_ror:2 row_mask:0xf bank_mask:0xf\n"
"Mvaa 160,128,172\ns_add_u32 m0, s47, 49152\ns_add_u32 s44, s44, 0x40000\nMaaa 0,128,176\nTa 196,13,16384\nv_add_f32_dpp v35, v35, v35 row_ror:2 row_mask:0xf bank_mask:0xf\n"
"Maaa 32,128,180\nD0 6,24,44,0\nv_add_f32_dpp v36, v36, v36 row_ror:2 row_mask:0xf bank_mask:0xf\nMaaa 64,128,184\nTa 198,13,18432\n"
"v_add_f32_dpp v37, v37, v37 row_ror:2 row_mask:0xf bank_mask:0xf\nMaaa 96,128,188\nv_add_f32_dpp v38, v38, v38 row_ror:2 row_mask:0xf bank_mask:0xf\n"
"v_add_f32_dpp v39, v39, v39 row_ror:2 row_mask:0xf bank_mask:0xf\nMaaa 100,132,188\nTa 200,14,16384\nv_lshrrev_b32 v48, 4, v0\nMaaa 68,132,184\nv_and_b32 v49, 1, v0\n"
"v_lshlrev_b32 v49, 5, v49\nMaaa 36,132,180\nTa 202,14,18432\nv_lshl_add_u32 v49, v48, 6, v49\nMaaa 4,132,176\ns_lshl_b32 s2, s11, 8\ns_add_u32 s2, s2, 132096\nMvaa 164,132,172\n"
"Ta 204,15,16384\nv_add_u32 v49, s2, v49\nMvaa 132,132,168\nD0 7,24,44,1024\nds_write_b128 v49, v[32:35]\nMvaa 100,132,164\nTa 206,15,18432\nds_write_b128 v49, v[36:39] offset:16\n"
"Mvaa 68,132,160\nv_and_b32 v50, 31, v0\nv_lshlrev_b32 v50, 2, v50\nMvaa 72,136,160\nTa 208,16,16384\ns_lshl_b32 s2, s11, 10\nMvaa 104,136,164\ns_add_u32 s2, s2, 133120\n"
"v_add_u32 v51, s2, v50\nMvaa 136,136,168\nTa 210,16,18432\nv_add_u32 v50, 132096, v50\nMvaa 168,136,172\nv_lshl_add_u32 v52, v48, 4, s2\nMaaa 8,136,176\nTa 212,17,16384\n"
"Maaa 40,136,180\nD0 8,24,44,2048\nMaaa 72,136,184\nTa 214,17,18432\nMaaa 104,136,188\nMaaa 108,140,188\nTa 216,18,16384\nMaaa 76,140,184\nMaaa 44,140,180\nTa 218,18,18432\n"
"Maaa 12,140,176\nMvaa 172,140,172\nTa 220,19,16384\nMvaa 140,140,168\nD0 9,24,44,3072\nMvaa 108,140,164\nTa 222,19,18432\nMvaa 76,140,160\nMvaa 80,144,160\nRa 224,11,0\n"
"Mvaa 112,144,164\nMvaa 144,144,168\nRa 228,11,2048\nMvaa 176,144,172\ns_add_u32 m0, s47, 0\ns_add_u32 s44, s44, 0x40000\nMaaa 16,144,176\nRa 232,11,8192\nMaaa 48,144,180\n"
"D0 6,24,44,0\nMaaa 80,144,184\nRa 236,11,10240\nMaaa 112,144,188\nMaaa 116,148,188\nRa 240,11,16384\nMaaa 84,148,184\nMaaa 52,148,180\nRa 244,11,18432\nMaaa 20,148,176\n"
"Sv 20,1,16,49,0\ns_add_u32 s49, s49, 1024\nMvaa 180,148,172\nRa 248,11,24576\nMvaa 148,148,168\nD0 7,24,44,1024\nMvaa 116,148,164\nRa 252,11,26624\nMvaa 84,148,160\n"
"Mvaa 88,152,160\nMvaa 120,152,164\nSv 20,1,16,49,0\ns_add_u32 s49, s49, 1024\nMvaa 152,152,168\nMvaa 184,152,172\nMaaa 24,152,176\nMaaa 56,152,180\nD0 8,24,44,2048\n"
"Maaa 88,152,184\nMaaa 120,152,188\nMaaa 124,156,188\nMaaa 92,156,184\nSv 20,1,16,49,0\ns_add_u32 s49, s49, 1024\nMaaa 60,156,180\nMaaa 28,156,176\nMvaa 188,156,172\n"
"Mvaa 156,156,168\nD0 9,24,44,3072\nMvaa 124,156,164\nMvaa 92,156,160\ns_waitcnt vmcnt(11) lgkmcnt(0)\ns_barrier\nMvaa 64,192,224\nTa 128,12,32768\nds_read_b32 v32, v50 offset:0\n"
"Mvaa 96,192,228\nds_read_b32 v33, v50 offset:256\nds_read_b32 v34, v50 offset:512\nMvaa 128,192,232\nTa 130,12,34816\nds_read_b32 v35, v50 offset:768\nMvaa 160,192,236\n"
"s_add_u32 m0, s48, 0\ns_add_u32 s45, s45, 128\nMaaa 0,192,240\nTa 132,13,32768\nds_read_b32 v36, v50 offset:128\nMaaa 32,192,244\nD0 2,20,45,0\nds_read_b32 v37, v50 offset:384\n"
"Maaa 64,192,248\nTa 134,13,34816\nds_read_b32 v38, v50 offset:640\nMaaa 96,192,252\nds_read_b32 v39, v50 offset:896\nMaaa 100,196,252\nTa 136,14,32768\nMaaa 68,196,248\n"
"Maaa 36,196,244\nTa 138,14,34816\nMaaa 4,196,240\nMvaa 164,196,236\nTa 140,15,32768\nMvaa 132,196,232\nD0 3,20,45,1024\nMvaa 100,196,228\nTa 142,15,34816\nMvaa 68,196,224\n"
"Mvaa 72,200,224\nTa 144,16,32768\nMvaa 104,200,228\nWL 5\nAD 32,32,33\nMvaa 136,200,232\nTa 146,16,34816\nAD 34,34,35\nMvaa 168,200,236\nAD 36,36,37\nAD 38,38,39\nMaaa 8,200,240\n"
"Ta 148,17,32768\nAD 32,32,34\nMaaa 40,200,244\nD0 4,20,45,2048\nAD 36,36,38\nMaaa 72,200,248\nTa 150,17,34816\nCV 32,32,36\nMaaa 104,200,252\nv_and_b32 v36, 0xffff0000, v32\n"
"v_lshlrev_b32 v32, 16, v32\nMaaa 108,204,252\nTa 152,18,32768\nv_mul_f32 v33, 0xbfb8aa3b, v32\nMaaa 76,204,248\nEX 33,33\nMaaa 44,204,244\nTa 154,18,34816\n"
"v_add_f32 v33, 1.0, v33\nMaaa 12,204,240\nv_rcp_f32 v33, v33\nMvaa 172,204,236\nTa 156,19,32768\nMU 33,32,33\nMvaa 140,204,232\nD0 5,20,45,3072\nCV 33,33,33\nMvaa 108,204,228\n"
"Ta 158,19,34816\nv_lshlrev_b32 v33, 16, v33\nMvaa 76,204,224\nMU 33,33,36\nCV 33,33,33\nMvaa 80,208,224\nRa 160,10,32768\nv_lshlrev_b32 v33, 16, v33\nMvaa 112,208,228\n"
"ds_write_b32 v51, v33\nRv 244,52,0\nMvaa 144,208,232\nRa 164,10,34816\nRv 248,52,64\nMvaa 176,208,236\ns_add_u32 m0, s48, 4096\ns_add_u32 s46, s45, 0x10000\nMaaa 16,208,240\n"
"Ra 168,10,40960\ns_and_b32 s2, s12, 1\nMaaa 48,208,244\nD0 2,20,46,0\ns_and_b32 s3, s13, 6\nMaaa 80,208,248\nRa 172,10,43008\ns_add_u32 s2, s2, s3\nMaaa 112,208,252\n"
"s_lshl3_add_u32 s2, s14, s2\nMaaa 116,212,252\nRa 176,10,49152\nMaaa 84,212,248\ns_cmp_lt_u32 s2, 14\ns_cselect_b32 s5, 0, 0x40000000\nMaaa 52,212,244\nRa 180,10,51200\n"
"s_mul_i32 s4, s73, 14\nMaaa 20,212,240\ns_add_u32 s4, s4, s2\nv_and_b32 v49, 15, v0\nMvaa 180,212,236\nRa 184,10,57344\nv_lshlrev_b32 v50, 3, v49\nMvaa 148,212,232\n"
"D0 3,20,46,1024\nv_and_b32 v51, 1, v48\nMvaa 116,212,228\nRa 188,10,59392\nv_lshl_add_u32 v50, v51, 2, v50\nMvaa 84,212,224\ns_mul_i32 s2, s11, 112\nv_add_u32 v50, s2, v50\n"
"Mvaa 88,216,224\nv_lshrrev_b32 v51, 4, v50\nv_lshrrev_b32 v48, 1, v48\nMvaa 120,216,228\nv_mad_u32_u24 v51, v48, 28, v51\nv_mul_u32_u24 v51, 7168, v51\nMvaa 152,216,232\n"
"v_and_b32 v50, 15, v50\nv_lshl_add_u32 v51, v50, 2, v51\nMvaa 184,216,236\nv_cmp_gt_u32 vcc, 14, v49\ns_lshl_b32 s4, s4, 6\ns_add_u32 s4, s4, 0x5c4c800\n"
"v_cndmask_b32 v51, v254, v51, vcc\nMaaa 24,216,240\ns_or_b32 s4, s4, s5\nWL 6\nMaaa 56,216,244\nD0 4,20,46,2048\nv_lshlrev_b32 v252, 16, v224\nMaaa 88,216,248\n"
"v_and_b32 v253, 0xffff0000, v224\nMU 32,244,252\nMaaa 120,216,252\nMU 33,244,253\nMU 40,248,252\nMaaa 124,220,252\nMU 41,248,253\nv_lshlrev_b32 v252, 16, v225\nMaaa 92,220,248\n"
"v_and_b32 v253, 0xffff0000, v225\nMU 34,244,252\nMaaa 60,220,244\nMU 35,244,253\nMU 42,248,252\nMaaa 28,220,240\nMU 43,248,253\nv_lshlrev_b32 v252, 16, v226\nMvaa 188,220,236\n"
"v_and_b32 v253, 0xffff0000, v226\nMU 36,244,252\nMvaa 156,220,232\nD0 5,20,46,3072\nMU 37,244,253\nMvaa 124,220,228\nMU 44,248,252\nMU 45,248,253\nMvaa 92,220,224\n"
"s_waitcnt vmcnt(15) lgkmcnt(0)\ns_barrier\nMvaa 64,128,160\nTa 192,12,49152\nv_lshlrev_b32 v252, 16, v227\nMvaa 96,128,164\nv_and_b32 v253, 0xffff0000, v227\nMU 38,244,252\n"
"Mvaa 128,128,168\nTa 194,12,51200\nMU 39,244,253\nMvaa 160,128,172\ns_add_u32 m0, s47, 16384\ns_add_u32 s44, s44, 0x40000\nMaaa 0,128,176\nTa 196,13,49152\nMU 46,248,252\n"
"Maaa 32,128,180\nD0 6,24,44,0\nMU 47,248,253\nMaaa 64,128,184\nTa 198,13,51200\nv_lshlrev_b32 v252, 16, v228\nMaaa 96,128,188\nv_and_b32 v253, 0xffff0000, v228\nFM 32,245,252\n"
"Maaa 100,132,188\nTa 200,14,49152\nFM 33,245,253\nMaaa 68,132,184\nFM 40,249,252\nFM 41,249,253\nMaaa 36,132,180\nTa 202,14,51200\nv_lshlrev_b32 v252, 16, v229\nMaaa 4,132,176\n"
"v_and_b32 v253, 0xffff0000, v229\nFM 34,245,252\nMvaa 164,132,172\nTa 204,15,49152\nFM 35,245,253\nMvaa 132,132,168\nD0 7,24,44,1024\nFM 42,249,252\nMvaa 100,132,164\n"
"Ta 206,15,51200\nFM 43,249,253\nMvaa 68,132,160\nv_lshlrev_b32 v252, 16, v230\nv_and_b32 v253, 0xffff0000, v230\nMvaa 72,136,160\nTa 208,16,49152\nFM 36,245,252\nMvaa 104,136,164\n"
"FM 37,245,253\nFM 44,249,252\nMvaa 136,136,168\nTa 210,16,51200\nFM 45,249,253\nMvaa 168,136,172\nv_lshlrev_b32 v252, 16, v231\nv_and_b32 v253, 0xffff0000, v231\nMaaa 8,136,176\n"
"Ta 212,17,49152\nFM 38,245,252\nMaaa 40,136,180\nD0 8,24,44,2048\nFM 39,245,253\nMaaa 72,136,184\nTa 214,17,51200\nFM 46,249,252\nMaaa 104,136,188\nFM 47,249,253\n"
"v_lshlrev_b32 v252, 16, v232\nMaaa 108,140,188\nTa 216,18,49152\nv_and_b32 v253, 0xffff0000, v232\nMaaa 76,140,184\nFM 32,246,252\nFM 33,246,253\nMaaa 44,140,180\nTa 218,18,51200\n"
"FM 40,250,252\nMaaa 12,140,176\nFM 41,250,253\nv_lshlrev_b32 v252, 16, v233\nMvaa 172,140,172\nTa 220,19,49152\nv_and_b32 v253, 0xffff0000, v233\nMvaa 140,140,168\n"
"D0 9,24,44,3072\nFM 34,246,252\nMvaa 108,140,164\nTa 222,19,51200\nFM 35,246,253\nMvaa 76,140,160\nFM 42,250,252\nFM 43,250,253\nMvaa 80,144,160\nRa 224,11,32768\n"
"v_lshlrev_b32 v252, 16, v234\nMvaa 112,144,164\nv_and_b32 v253, 0xffff0000, v234\nFM 36,246,252\nMvaa 144,144,168\nRa 228,11,34816\nFM 37,246,253\nMvaa 176,144,172\n"
"s_add_u32 m0, s47, 32768\ns_add_u32 s44, s44, 0x40000\nMaaa 16,144,176\nRa 232,11,40960\nFM 44,250,252\nMaaa 48,144,180\nD0 6,24,44,0\nFM 45,250,253\nMaaa 80,144,184\n"
"Ra 236,11,43008\nv_lshlrev_b32 v252, 16, v235\nMaaa 112,144,188\nv_and_b32 v253, 0xffff0000, v235\nFM 38,246,252\nMaaa 116,148,188\nRa 240,11,49152\nFM 39,246,253\n"
"Maaa 84,148,184\nFM 46,250,252\nFM 47,250,253\nMaaa 52,148,180\nRa 244,11,51200\nv_lshlrev_b32 v252, 16, v236\nMaaa 20,148,176\nSv 20,1,16,49,0\ns_add_u32 s49, s49, 1024\n"
"Mvaa 180,148,172\nRa 248,11,57344\nv_and_b32 v253, 0xffff0000, v236\nMvaa 148,148,168\nD0 7,24,44,1024\nFM 32,247,252\nMvaa 116,148,164\nRa 252,11,59392\nFM 33,247,253\n"
"Mvaa 84,148,160\nFM 40,251,252\nFM 41,251,253\nMvaa 88,152,160\nv_lshlrev_b32 v252, 16, v237\nv_and_b32 v253, 0xffff0000, v237\nMvaa 120,152,164\nSv 20,1,16,49,0\n"
"s_add_u32 s49, s49, 1024\nMvaa 152,152,168\nFM 34,247,252\nFM 35,247,253\nMvaa 184,152,172\nFM 42,251,252\nFM 43,251,253\nMaaa 24,152,176\nv_lshlrev_b32 v252, 16, v238\n"
"v_and_b32 v253, 0xffff0000, v238\nMaaa 56,152,180\nD0 8,24,44,2048\nFM 36,247,252\nMaaa 88,152,184\nFM 37,247,253\nFM 44,251,252\nMaaa 120,152,188\nFM 45,251,253\n"
"v_lshlrev_b32 v252, 16, v239\nMaaa 124,156,188\nv_and_b32 v253, 0xffff0000, v239\nFM 38,247,252\nMaaa 92,156,184\nSv 20,1,16,49,0\ns_add_u32 s49, s49, 1024\nMaaa 60,156,180\n"
"FM 39,247,253\nFM 46,251,252\nMaaa 28,156,176\nFM 47,251,253\nMvaa 188,156,172\nv_permlane32_swap_b32 v32, v40\nv_permlane32_swap_b32 v33, v41\nMvaa 156,156,168\nD0 9,24,44,3072\n"
"v_permlane32_swap_b32 v34, v42\nMvaa 124,156,164\nv_permlane32_swap_b32 v35, v43\nv_permlane32_swap_b32 v36, v44\nMvaa 92,156,160\ns_waitcnt vmcnt(11) lgkmcnt(0)\ns_barrier\n"
"Mvaa 64,192,224\nTa 128,12,0\nv_permlane32_swap_b32 v37, v45\nMvaa 96,192,228\nv_permlane32_swap_b32 v38, v46\nv_permlane32_swap_b32 v39, v47\nMvaa 128,192,232\nTa 130,12,2048\n"
"AD 32,32,40\nMvaa 160,192,236\ns_add_u32 m0, s48, 32768\ns_add_u32 s45, s45, 128\nMaaa 0,192,240\nTa 132,13,0\nAD 33,33,41\nMaaa 32,192,244\nD0 2,20,45,0\nAD 34,34,42\n"
"Maaa 64,192,248\nTa 134,13,2048\nAD 35,35,43\nMaaa 96,192,252\nAD 36,36,44\nAD 37,37,45\nMaaa 100,196,252\nTa 136,14,0\nAD 38,38,46\nMaaa 68,196,248\nAD 39,39,47\nMaaa 36,196,244\n"
"Ta 138,14,2048\nv_permlane16_swap_b32 v32, v36\nMaaa 4,196,240\nv_permlane16_swap_b32 v33, v37\nv_permlane16_swap_b32 v34, v38\nMvaa 164,196,236\nTa 140,15,0\n"
"v_permlane16_swap_b32 v35, v39\nMvaa 132,196,232\nD0 3,20,45,1024\nAD 32,32,36\nMvaa 100,196,228\nTa 142,15,2048\nAD 33,33,37\nMvaa 68,196,224\nAD 34,34,38\nAD 35,35,39\n"
"Mvaa 72,200,224\nTa 144,16,0\nSv 32,51,16,4,0\nMvaa 104,200,228\nMvaa 136,200,232\nTa 146,16,2048\nMvaa 168,200,236\nMaaa 8,200,240\nTa 148,17,0\nMaaa 40,200,244\nD0 4,20,45,2048\n"
"Maaa 72,200,248\nTa 150,17,2048\nMaaa 104,200,252\nMaaa 108,204,252\nTa 152,18,0\nMaaa 76,204,248\nMaaa 44,204,244\nTa 154,18,2048\nMaaa 12,204,240\nMvaa 172,204,236\nTa 156,19,0\n"
"Mvaa 140,204,232\nD0 5,20,45,3072\nMvaa 108,204,228\nTa 158,19,2048\nMvaa 76,204,224\nMvaa 80,208,224\nRa 160,10,0\nMvaa 112,208,228\nMvaa 144,208,232\nRa 164,10,2048\n"
"Mvaa 176,208,236\ns_add_u32 m0, s48, 36864\ns_add_u32 s46, s45, 0x10000\nMaaa 16,208,240\nRa 168,10,8192\nMaaa 48,208,244\nD0 2,20,46,0\nMaaa 80,208,248\nRa 172,10,10240\n"
"Maaa 112,208,252\nMaaa 116,212,252\nRa 176,10,16384\nMaaa 84,212,248\nMaaa 52,212,244\nRa 180,10,18432\nMaaa 20,212,240\nMvaa 180,212,236\nRa 184,10,24576\nMvaa 148,212,232\n"
"D0 3,20,46,1024\nMvaa 116,212,228\nRa 188,10,26624\nMvaa 84,212,224\nMvaa 88,216,224\nMvaa 120,216,228\nMvaa 152,216,232\nMvaa 184,216,236\nMaaa 24,216,240\nMaaa 56,216,244\n"
"D0 4,20,46,2048\nMaaa 88,216,248\nMaaa 120,216,252\nMaaa 124,220,252\nMaaa 92,220,248\nMaaa 60,220,244\nMaaa 28,220,240\nMvaa 188,220,236\nMvaa 156,220,232\nD0 5,20,46,3072\n"
"Mvaa 124,220,228\nMvaa 92,220,224\ns_waitcnt vmcnt(16) lgkmcnt(0)\ns_barrier\nMvaa 64,128,160\nTa 192,12,16384\nMvaa 96,128,164\nMvaa 128,128,168\nTa 194,12,18432\n"
"Mvaa 160,128,172\ns_add_u32 m0, s47, 49152\ns_add_u32 s44, s44, 0x40000\nMaaa 0,128,176\nTa 196,13,16384\nMaaa 32,128,180\nD0 6,24,44,0\nMaaa 64,128,184\nTa 198,13,18432\n"
"Maaa 96,128,188\nMaaa 100,132,188\nTa 200,14,16384\nMaaa 68,132,184\nMaaa 36,132,180\nTa 202,14,18432\nMaaa 4,132,176\nMvaa 164,132,172\nTa 204,15,16384\nMvaa 132,132,168\n"
"D0 7,24,44,1024\nMvaa 100,132,164\nTa 206,15,18432\nMvaa 68,132,160\nMvaa 72,136,160\nTa 208,16,16384\nMvaa 104,136,164\nMvaa 136,136,168\nTa 210,16,18432\nMvaa 168,136,172\n"
"Maaa 8,136,176\nTa 212,17,16384\nMaaa 40,136,180\nD0 8,24,44,2048\nMaaa 72,136,184\nTa 214,17,18432\nMaaa 104,136,188\nMaaa 108,140,188\nTa 216,18,16384\nMaaa 76,140,184\n"
"Maaa 44,140,180\nTa 218,18,18432\nMaaa 12,140,176\nMvaa 172,140,172\nTa 220,19,16384\nMvaa 140,140,168\nD0 9,24,44,3072\nMvaa 108,140,164\nTa 222,19,18432\nMvaa 76,140,160\n"
"Mvaa 80,144,160\nRa 224,11,0\nMvaa 112,144,164\nMvaa 144,144,168\nRa 228,11,2048\nMvaa 176,144,172\ns_add_u32 m0, s47, 0\ns_add_u32 s44, s44, 0x40000\nMaaa 16,144,176\n"
"Ra 232,11,8192\nMaaa 48,144,180\nD0 6,24,44,0\nMaaa 80,144,184\nRa 236,11,10240\nMaaa 112,144,188\nMaaa 116,148,188\nRa 240,11,16384\nMaaa 84,148,184\nMaaa 52,148,180\n"
"Ra 244,11,18432\nMaaa 20,148,176\nMvaa 180,148,172\nRa 248,11,24576\nMvaa 148,148,168\nD0 7,24,44,1024\nMvaa 116,148,164\nRa 252,11,26624\nMvaa 84,148,160\nMvaa 88,152,160\n"
"Mvaa 120,152,164\nMvaa 152,152,168\nMvaa 184,152,172\nMaaa 24,152,176\nMaaa 56,152,180\nD0 8,24,44,2048\nMaaa 88,152,184\nMaaa 120,152,188\nMaaa 124,156,188\nMaaa 92,156,184\n"
"Maaa 60,156,180\nMaaa 28,156,176\nMvaa 188,156,172\nMvaa 156,156,168\nD0 9,24,44,3072\nMvaa 124,156,164\nMvaa 92,156,160\ns_waitcnt vmcnt(8) lgkmcnt(0)\ns_barrier\n"
"Mvaa 64,192,224\nTa 128,12,32768\nMvaa 96,192,228\nMvaa 128,192,232\nTa 130,12,34816\nMvaa 160,192,236\ns_add_u32 m0, s48, 0\ns_add_u32 s45, s45, 128\nMaaa 0,192,240\n"
"Ta 132,13,32768\nMaaa 32,192,244\nD0 2,20,45,0\nMaaa 64,192,248\nTa 134,13,34816\nMaaa 96,192,252\nMaaa 100,196,252\nTa 136,14,32768\nMaaa 68,196,248\nMaaa 36,196,244\n"
"Ta 138,14,34816\nMaaa 4,196,240\nMvaa 164,196,236\nTa 140,15,32768\nMvaa 132,196,232\nD0 3,20,45,1024\nMvaa 100,196,228\nTa 142,15,34816\nMvaa 68,196,224\nMvaa 72,200,224\n"
"Ta 144,16,32768\nMvaa 104,200,228\nMvaa 136,200,232\nTa 146,16,34816\nMvaa 168,200,236\nMaaa 8,200,240\nTa 148,17,32768\nMaaa 40,200,244\nD0 4,20,45,2048\nMaaa 72,200,248\n"
"Ta 150,17,34816\nMaaa 104,200,252\nMaaa 108,204,252\nTa 152,18,32768\nMaaa 76,204,248\nMaaa 44,204,244\nTa 154,18,34816\nMaaa 12,204,240\nMvaa 172,204,236\nTa 156,19,32768\n"
"Mvaa 140,204,232\nD0 5,20,45,3072\nMvaa 108,204,228\nTa 158,19,34816\nMvaa 76,204,224\nMvaa 80,208,224\nRa 160,10,32768\nMvaa 112,208,228\nMvaa 144,208,232\nRa 164,10,34816\n"
"Mvaa 176,208,236\ns_add_u32 m0, s48, 4096\ns_add_u32 s46, s45, 0x10000\nMaaa 16,208,240\nRa 168,10,40960\nMaaa 48,208,244\nD0 2,20,46,0\nMaaa 80,208,248\nRa 172,10,43008\n"
"Maaa 112,208,252\nMaaa 116,212,252\nRa 176,10,49152\nMaaa 84,212,248\nMaaa 52,212,244\nRa 180,10,51200\nMaaa 20,212,240\nMvaa 180,212,236\nRa 184,10,57344\nMvaa 148,212,232\n"
"D0 3,20,46,1024\nMvaa 116,212,228\nRa 188,10,59392\nMvaa 84,212,224\nMvaa 88,216,224\nMvaa 120,216,228\nMvaa 152,216,232\nMvaa 184,216,236\nMaaa 24,216,240\nMaaa 56,216,244\n"
"D0 4,20,46,2048\nMaaa 88,216,248\nMaaa 120,216,252\nMaaa 124,220,252\nMaaa 92,220,248\nMaaa 60,220,244\nMaaa 28,220,240\nMvaa 188,220,236\nMvaa 156,220,232\nD0 5,20,46,3072\n"
"Mvaa 124,220,228\nMvaa 92,220,224\ns_waitcnt vmcnt(12) lgkmcnt(0)\ns_barrier\nMvaa 64,128,160\nTa 192,12,49152\nMvaa 96,128,164\nMvaa 128,128,168\nTa 194,12,51200\n"
"Mvaa 160,128,172\ns_add_u32 m0, s47, 16384\ns_add_u32 s44, s44, 0x40000\nMaaa 0,128,176\nTa 196,13,49152\nMaaa 32,128,180\nD0 6,24,44,0\nMaaa 64,128,184\nTa 198,13,51200\n"
"Maaa 96,128,188\nMaaa 100,132,188\nTa 200,14,49152\nMaaa 68,132,184\nMaaa 36,132,180\nTa 202,14,51200\nMaaa 4,132,176\nMvaa 164,132,172\nTa 204,15,49152\nMvaa 132,132,168\n"
"D0 7,24,44,1024\nMvaa 100,132,164\nTa 206,15,51200\nMvaa 68,132,160\nMvaa 72,136,160\nTa 208,16,49152\nMvaa 104,136,164\nMvaa 136,136,168\nTa 210,16,51200\nMvaa 168,136,172\n"
"Maaa 8,136,176\nTa 212,17,49152\nMaaa 40,136,180\nD0 8,24,44,2048\nMaaa 72,136,184\nTa 214,17,51200\nMaaa 104,136,188\nMaaa 108,140,188\nTa 216,18,49152\nMaaa 76,140,184\n"
"Maaa 44,140,180\nTa 218,18,51200\nMaaa 12,140,176\nMvaa 172,140,172\nTa 220,19,49152\nMvaa 140,140,168\nD0 9,24,44,3072\nMvaa 108,140,164\nTa 222,19,51200\nMvaa 76,140,160\n"
"Mvaa 80,144,160\nRa 224,11,32768\nMvaa 112,144,164\nMvaa 144,144,168\nRa 228,11,34816\nMvaa 176,144,172\ns_add_u32 m0, s47, 32768\ns_add_u32 s44, s44, 0x40000\nMaaa 16,144,176\n"
"Ra 232,11,40960\nMaaa 48,144,180\nD0 6,24,44,0\nMaaa 80,144,184\nRa 236,11,43008\nMaaa 112,144,188\nMaaa 116,148,188\nRa 240,11,49152\nMaaa 84,148,184\nMaaa 52,148,180\n"
"Ra 244,11,51200\nMaaa 20,148,176\nMvaa 180,148,172\nRa 248,11,57344\nMvaa 148,148,168\nD0 7,24,44,1024\nMvaa 116,148,164\nRa 252,11,59392\nMvaa 84,148,160\nMvaa 88,152,160\n"
"Mvaa 120,152,164\nMvaa 152,152,168\nMvaa 184,152,172\nMaaa 24,152,176\nMaaa 56,152,180\nD0 8,24,44,2048\nMaaa 88,152,184\nMaaa 120,152,188\nMaaa 124,156,188\nMaaa 92,156,184\n"
"Maaa 60,156,180\nMaaa 28,156,176\nMvaa 188,156,172\nMvaa 156,156,168\nD0 9,24,44,3072\nMvaa 124,156,164\nMvaa 92,156,160\ns_waitcnt vmcnt(8) lgkmcnt(0)\ns_barrier\n"
"Mvaa 64,192,224\nTa 128,12,0\nMvaa 96,192,228\nMvaa 128,192,232\nTa 130,12,2048\nMvaa 160,192,236\ns_add_u32 m0, s48, 32768\ns_add_u32 s45, s45, 128\nMaaa 0,192,240\nTa 132,13,0\n"
"Maaa 32,192,244\nD0 2,20,45,0\nMaaa 64,192,248\nTa 134,13,2048\nMaaa 96,192,252\nMaaa 100,196,252\nTa 136,14,0\nMaaa 68,196,248\nMaaa 36,196,244\nTa 138,14,2048\nMaaa 4,196,240\n"
"Mvaa 164,196,236\nTa 140,15,0\nMvaa 132,196,232\nD0 3,20,45,1024\nMvaa 100,196,228\nTa 142,15,2048\nMvaa 68,196,224\nMvaa 72,200,224\nTa 144,16,0\nMvaa 104,200,228\n"
"Mvaa 136,200,232\nTa 146,16,2048\nMvaa 168,200,236\nMaaa 8,200,240\nTa 148,17,0\nMaaa 40,200,244\nD0 4,20,45,2048\nMaaa 72,200,248\nTa 150,17,2048\nMaaa 104,200,252\n"
"Maaa 108,204,252\nTa 152,18,0\nMaaa 76,204,248\nMaaa 44,204,244\nTa 154,18,2048\nMaaa 12,204,240\nMvaa 172,204,236\nTa 156,19,0\nMvaa 140,204,232\nD0 5,20,45,3072\n"
"Mvaa 108,204,228\nTa 158,19,2048\nMvaa 76,204,224\nMvaa 80,208,224\nRa 160,10,0\nMvaa 112,208,228\nMvaa 144,208,232\nRa 164,10,2048\nMvaa 176,208,236\ns_add_u32 m0, s48, 36864\n"
"s_add_u32 s46, s45, 0x10000\nMaaa 16,208,240\nRa 168,10,8192\nMaaa 48,208,244\nD0 2,20,46,0\nMaaa 80,208,248\nRa 172,10,10240\nMaaa 112,208,252\nMaaa 116,212,252\nRa 176,10,16384\n"
"Maaa 84,212,248\nMaaa 52,212,244\nRa 180,10,18432\nMaaa 20,212,240\nMvaa 180,212,236\nRa 184,10,24576\nMvaa 148,212,232\nD0 3,20,46,1024\nMvaa 116,212,228\nRa 188,10,26624\n"
"Mvaa 84,212,224\nMvaa 88,216,224\nMvaa 120,216,228\nMvaa 152,216,232\nMvaa 184,216,236\nMaaa 24,216,240\nMaaa 56,216,244\nD0 4,20,46,2048\nMaaa 88,216,248\nMaaa 120,216,252\n"
"Maaa 124,220,252\nMaaa 92,220,248\nMaaa 60,220,244\nMaaa 28,220,240\nMvaa 188,220,236\nMvaa 156,220,232\nD0 5,20,46,3072\nMvaa 124,220,228\nMvaa 92,220,224\n"
"s_waitcnt vmcnt(12) lgkmcnt(0)\ns_barrier\ns_cmp_eq_u32 s14, 0\ns_cbranch_scc1 .T0K4\nMvaa 64,128,160\nTa 192,12,16384\nv_lshrrev_b32 v48, 4, v0\nMvaa 96,128,164\n"
"v_and_b32 v49, 7, v0\nv_xor_b32 v49, v49, v48\ns_add_u32 m0, s47, 49152\ns_add_u32 s44, s44, 0x40000\nMvaa 128,128,168\nTa 194,12,18432\nv_lshlrev_b32 v49, 4, v49\n"
"Mvaa 160,128,172\nv_lshrrev_b32 v48, 3, v0\nv_mul_u32_u24 v48, 896, v48\nD0 6,24,44,0\nMaaa 0,128,176\nTa 196,13,16384\nv_add_u32 v40, v48, v49\nMaaa 32,128,180\n"
"v_xor_b32 v49, 64, v40\nv_add_u32 v41, 6144, v49\nMaaa 64,128,184\nTa 198,13,18432\nv_add_u32 v42, 12288, v40\nMaaa 96,128,188\nv_add_u32 v43, 18432, v49\ns_mov_b32 s2, s13\n"
"Maaa 100,132,188\nTa 200,14,16384\ns_lshl2_add_u32 s2, s2, s11\nD0 7,24,44,1024\nMaaa 68,132,184\ns_mul_i32 s52, s2, 28672\ns_lshr_b32 s2, s13, 2\nMaaa 36,132,180\n"
"Ta 202,14,18432\ns_bfe_u32 s3, s13, 0x10001\nMaaa 4,132,176\ns_mul_i32 s3, s3, 17\ns_add_u32 s2, s2, s3\nMvaa 164,132,172\nTa 204,15,16384\ns_xor_b32 s3, s14, 1\nMvaa 132,132,168\n"
"s_lshl3_add_u32 s2, s3, s2\ns_lshl1_add_u32 s2, 0, s2\nD0 8,24,44,2048\nMvaa 100,132,164\nTa 206,15,18432\nMvaa 68,132,160\ns_cmp_eq_u32 s2, 32\ns_cselect_b32 s4, 16, s2\n"
"Mvaa 72,136,160\nTa 208,16,16384\ns_mul_i32 s75, s4, 33792\nMvaa 104,136,164\ns_lshr_b32 s2, s13, 2\ns_bfe_u32 s3, s13, 0x10001\nMvaa 136,136,168\nTa 210,16,18432\n"
"s_mul_i32 s3, s3, 17\nD0 9,24,44,3072\nMvaa 168,136,172\ns_add_u32 s2, s2, s3\ns_xor_b32 s3, s14, 1\nMaaa 8,136,176\nTa 212,17,16384\ns_lshl3_add_u32 s2, s3, s2\nMaaa 40,136,180\n"
"s_lshl1_add_u32 s2, 1, s2\ns_add_u32 m0, s47, 65536\ns_add_u32 s46, s51, 0\nMaaa 72,136,184\nTa 214,17,18432\nMaaa 104,136,188\ns_cmp_eq_u32 s2, 32\ns_cselect_b32 s4, 16, s2\n"
"D0 6,32,46,0\nMaaa 108,140,188\nTa 216,18,16384\ns_mul_i32 s76, s4, 33792\nMaaa 76,140,184\ns_lshr_b32 s2, s13, 2\ns_bfe_u32 s3, s13, 0x10001\nMaaa 44,140,180\nTa 218,18,18432\n"
"s_mul_i32 s3, s3, 17\nMaaa 12,140,176\ns_add_u32 s2, s2, s3\ns_xor_b32 s3, s14, 1\nMvaa 172,140,172\nTa 220,19,16384\ns_lshl3_add_u32 s2, s3, s2\nD0 7,32,46,1024\n"
"Mvaa 140,140,168\ns_lshl1_add_u32 s2, 2, s2\nMvaa 108,140,164\nTa 222,19,18432\nMvaa 76,140,160\ns_cmp_eq_u32 s2, 32\ns_cselect_b32 s4, 16, s2\nMvaa 80,144,160\nRa 224,11,0\n"
"s_mul_i32 s77, s4, 33792\nMvaa 112,144,164\ns_lshr_b32 s2, s13, 2\ns_bfe_u32 s3, s13, 0x10001\nD0 8,32,46,2048\nMvaa 144,144,168\nRa 228,11,2048\ns_mul_i32 s3, s3, 17\n"
"Mvaa 176,144,172\ns_add_u32 s2, s2, s3\ns_xor_b32 s3, s14, 1\nMaaa 16,144,176\nRa 232,11,8192\ns_lshl3_add_u32 s2, s3, s2\nMaaa 48,144,180\ns_lshl1_add_u32 s2, 3, s2\n"
"Maaa 80,144,184\nRa 236,11,10240\nD0 9,32,46,3072\nMaaa 112,144,188\ns_cmp_eq_u32 s2, 32\ns_cselect_b32 s4, 16, s2\nMaaa 116,148,188\nRa 240,11,16384\ns_mul_i32 s78, s4, 33792\n"
"Maaa 84,148,184\ns_mul_i32 s2, s12, 1083392\ns_lshr_b32 s3, s11, 1\ns_add_u32 m0, s47, 81920\ns_add_u32 s46, s51, 262144\nMaaa 52,148,180\nRa 244,11,18432\ns_lshl_b32 s3, s3, 14\n"
"Maaa 20,148,176\ns_add_u32 s2, s2, s3\ns_and_b32 s3, s11, 1\nD0 6,32,46,0\nMvaa 180,148,172\nRa 248,11,24576\ns_lshl_b32 s3, s3, 12\nMvaa 148,148,168\ns_add_u32 s2, s2, s3\n"
"s_add_u32 s2, s2, 0x0\nMvaa 116,148,164\nRa 252,11,26624\ns_mov_b32 s55, 8192\nMvaa 84,148,160\ns_add_u32 s75, s75, s2\ns_add_u32 s76, s76, s2\nMvaa 88,152,160\n"
"s_add_u32 s77, s77, s2\ns_add_u32 s78, s78, s2\nD0 7,32,46,1024\nMvaa 120,152,164\nv_add_u32 v32, 0x10000, v12\nv_add_u32 v33, 0x10000, v13\nMvaa 152,152,168\n"
"v_add_u32 v34, 0x10000, v14\nv_add_u32 v35, 0x10000, v15\nMvaa 184,152,172\nv_add_u32 v36, 0x10000, v16\nv_add_u32 v37, 0x10000, v17\nMaaa 24,152,176\nv_add_u32 v38, 0x10000, v18\n"
"v_add_u32 v39, 0x10000, v19\nMaaa 56,152,180\ns_add_u32 s2, s11, 1\ns_lshr_b32 s2, s2, 1\nD0 8,32,46,2048\nMaaa 88,152,184\ns_lshl_b32 s2, s2, 12\nv_add_u32 v44, s2, v10\n"
"Maaa 120,152,188\nv_add_u32 v45, s2, v11\nv_xor_b32 v46, 0x1000, v44\nMaaa 124,156,188\nv_xor_b32 v47, 0x1000, v45\ns_mul_i32 s2, s12, 1083392\nMaaa 92,156,184\n"
"s_lshl_b32 s3, s11, 12\ns_add_u32 s2, s2, s3\nMaaa 60,156,180\ns_add_u32 s89, s3, 0x10000\ns_mov_b32 s88, 0xffffbc00\nD0 9,32,46,3072\nMaaa 28,156,176\ns_lshl_b32 s4, s13, 1\n"
"s_sub_u32 s79, 15, s4\nMvaa 188,156,172\ns_and_b32 s5, s12, 1\ns_lshl1_add_u32 s5, s12, s5\nMvaa 156,156,168\ns_mul_i32 s5, s5, 119808\ns_add_u32 s5, s5, s3\nMvaa 124,156,164\n"
"s_add_u32 s96, s5, 0x5684400\ns_mov_b32 s63, 0x53800000\nMvaa 92,156,160\ns_waitcnt vmcnt(12) lgkmcnt(0)\ns_barrier\nMvaa 64,192,224\nTa 128,12,32768\ns_mov_b32 s64, 0x2b800000\n"
"Mvaa 96,192,228\ns_mov_b32 s98, s4\ns_lshl3_add_u32 s5, s12, s13\ns_add_u32 m0, s47, -65536\ns_add_u32 s46, s52, 0\nMvaa 128,192,232\nTa 130,12,34816\ns_lshl2_add_u32 s5, s5, s11\n"
"Mvaa 160,192,236\ns_lshl_b32 s65, s5, 9\ns_add_u32 s65, s65, 0x5c00000\nD0 40,28,46,0\nMaaa 0,192,240\nTa 132,13,32768\ns_mul_i32 s66, s5, 17408\nMaaa 32,192,244\n"
"s_add_u32 s66, s66, 0x2c00000\ns_lshr_b32 s58, s12, 2\nMaaa 64,192,248\nTa 134,13,34816\ns_lshl_b32 s58, s58, 10\nMaaa 96,192,252\ns_add_u32 s58, s58, 0x5c48400\n"
"s_lshl1_add_u32 s5, s12, 1\nMaaa 100,196,252\nTa 136,14,32768\ns_lshl3_add_u32 s5, s5, s13\nD0 41,28,46,1024\nMaaa 68,196,248\ns_lshl4_add_u32 s54, s5, 0x5c48400\nMaaa 36,196,244\n"
"Ta 138,14,34816\nMaaa 4,196,240\ns_cmp_eq_u32 s11, 0\ns_cselect_b32 s54, s54, 0x40000000\nMvaa 164,196,236\nTa 140,15,32768\ns_getpc_b64 s[8:9]\n.PC30:\n"
"s_add_u32 s8, s8, .HD11-.PC30\ns_addc_u32 s9, s9, 0\nMvaa 132,196,232\ns_mov_b32 s97, 0xff800000\ns_mov_b32 s90, 1\nD0 42,28,46,2048\nMvaa 100,196,228\nTa 142,15,34816\n"
"s_lshl_b32 s5, s11, 2\nMvaa 68,196,224\ns_lshr_b32 s92, 0xf112, s5\ns_and_b32 s92, s92, 15\nMvaa 72,200,224\nTa 144,16,32768\ns_lshr_b32 s93, s11, 1\nMvaa 104,200,228\n"
"s_xor_b32 s93, s93, 1\ns_mul_i32 s4, s79, 33792\nMvaa 136,200,232\nTa 146,16,34816\ns_sub_u32 s4, s2, s4\nD0 43,28,46,3072\nMvaa 168,200,236\ns_add_u32 s94, s4, 0x10800\n"
"s_add_u32 s95, s4, 0x110a400\nMaaa 8,200,240\nTa 148,17,32768\ns_add_u32 s86, s2, 0x9cc00\nMaaa 40,200,244\ns_add_u32 s87, s2, 0x1196800\ns_sub_u32 s99, s79, 1\n"
"s_add_u32 m0, s47, 0\ns_add_u32 s46, s51, 524288\nMaaa 72,200,248\nTa 150,17,34816\ns_mov_b32 s91, 100\nMaaa 104,200,252\ns_cmp_eq_u32 s11, 3\ns_cselect_b32 s92, 100, s92\n"
"D0 6,32,46,0\nMaaa 108,204,252\nTa 152,18,32768\nMaaa 76,204,248\nMaaa 44,204,244\nTa 154,18,34816\nMaaa 12,204,240\nMvaa 172,204,236\nTa 156,19,32768\nD0 7,32,46,1024\n"
"Mvaa 140,204,232\nMvaa 108,204,228\nTa 158,19,34816\nMvaa 76,204,224\nMvaa 80,208,224\nRa 160,10,32768\nMvaa 112,208,228\nD0 8,32,46,2048\nMvaa 144,208,232\nRa 164,10,34816\n"
"Mvaa 176,208,236\nMaaa 16,208,240\nRa 168,10,40960\nMaaa 48,208,244\nMaaa 80,208,248\nRa 172,10,43008\nD0 9,32,46,3072\nMaaa 112,208,252\nMaaa 116,212,252\nRa 176,10,49152\n"
"Maaa 84,212,248\ns_add_u32 m0, s47, 16384\ns_add_u32 s46, s51, 786432\nMaaa 52,212,244\nRa 180,10,51200\nMaaa 20,212,240\nD0 6,32,46,0\nMvaa 180,212,236\nRa 184,10,57344\n"
"Mvaa 148,212,232\nMvaa 116,212,228\nRa 188,10,59392\nMvaa 84,212,224\nMvaa 88,216,224\nD0 7,32,46,1024\nMvaa 120,216,228\nMvaa 152,216,232\nMvaa 184,216,236\nMaaa 24,216,240\n"
"Maaa 56,216,244\nD0 8,32,46,2048\nMaaa 88,216,248\nMaaa 120,216,252\nMaaa 124,220,252\nMaaa 92,220,248\nMaaa 60,220,244\nD0 9,32,46,3072\nMaaa 28,220,240\nMvaa 188,220,236\n"
"Mvaa 156,220,232\nMvaa 124,220,228\nMvaa 92,220,224\ns_waitcnt vmcnt(20) lgkmcnt(0)\ns_barrier\nMvaa 64,128,160\nTa 192,12,49152\nMvaa 96,128,164\ns_add_u32 m0, s47, -49152\n"
"s_add_u32 s46, s52, 256\nMvaa 128,128,168\nTa 194,12,51200\nMvaa 160,128,172\nD0 40,28,46,0\nMaaa 0,128,176\nTa 196,13,49152\nMaaa 32,128,180\nMaaa 64,128,184\nTa 198,13,51200\n"
"Maaa 96,128,188\nMaaa 100,132,188\nTa 200,14,49152\nD0 41,28,46,1024\nMaaa 68,132,184\nMaaa 36,132,180\nTa 202,14,51200\nMaaa 4,132,176\nMvaa 164,132,172\nTa 204,15,49152\n"
"Mvaa 132,132,168\nD0 42,28,46,2048\nMvaa 100,132,164\nTa 206,15,51200\nMvaa 68,132,160\nMvaa 72,136,160\nTa 208,16,49152\nMvaa 104,136,164\nMvaa 136,136,168\nTa 210,16,51200\n"
"D0 43,28,46,3072\nMvaa 168,136,172\nMaaa 8,136,176\nTa 212,17,49152\nMaaa 40,136,180\ns_add_u32 m0, s47, 32768\ns_add_u32 s46, s51, 1048576\nMaaa 72,136,184\nTa 214,17,51200\n"
"Maaa 104,136,188\nD0 6,32,46,0\nMaaa 108,140,188\nTa 216,18,49152\nMaaa 76,140,184\nMaaa 44,140,180\nTa 218,18,51200\nMaaa 12,140,176\nMvaa 172,140,172\nTa 220,19,49152\n"
"D0 7,32,46,1024\nMvaa 140,140,168\nMvaa 108,140,164\nTa 222,19,51200\nMvaa 76,140,160\nMvaa 80,144,160\nRa 224,11,32768\nMvaa 112,144,164\nD0 8,32,46,2048\nMvaa 144,144,168\n"
"Ra 228,11,34816\nMvaa 176,144,172\nMaaa 16,144,176\nRa 232,11,40960\nMaaa 48,144,180\nMaaa 80,144,184\nRa 236,11,43008\nD0 9,32,46,3072\nMaaa 112,144,188\nMaaa 116,148,188\n"
"Ra 240,11,49152\nMaaa 84,148,184\nMaaa 52,148,180\nRa 244,11,51200\nMaaa 20,148,176\nMvaa 180,148,172\nRa 248,11,57344\nMvaa 148,148,168\nMvaa 116,148,164\nRa 252,11,59392\n"
"Mvaa 84,148,160\nMvaa 88,152,160\nMvaa 120,152,164\nMvaa 152,152,168\nMvaa 184,152,172\nMaaa 24,152,176\nMaaa 56,152,180\nMaaa 88,152,184\nMaaa 120,152,188\nMaaa 124,156,188\n"
"Maaa 92,156,184\nMaaa 60,156,180\nMaaa 28,156,176\nMvaa 188,156,172\nMvaa 156,156,168\nMvaa 124,156,164\nMvaa 92,156,160\ns_waitcnt vmcnt(16) lgkmcnt(0)\ns_barrier\n"
"Mvaa 64,192,224\ns_add_u32 m0, s47, -16384\ns_add_u32 s46, s52, 128\nMvaa 68,196,224\nMvaa 72,200,224\nD0 40,28,46,0\nMvaa 76,204,224\nMvaa 80,208,224\nMvaa 84,212,224\n"
"Mvaa 88,216,224\nD0 41,28,46,1024\nCV 192,64,65\nMvaa 92,220,224\nCV 193,66,67\nCV 194,68,69\nMvaa 96,192,228\nCV 195,70,71\nCV 196,72,73\nMvaa 100,196,228\nCV 197,74,75\n"
"CV 198,76,77\nMvaa 104,200,228\nD0 42,28,46,2048\nCV 199,78,79\nMvaa 108,204,228\nCV 200,80,81\nCV 201,82,83\nMvaa 112,208,228\nCV 202,84,85\nCV 203,86,87\nMvaa 116,212,228\n"
"CV 204,88,89\nCV 205,90,91\nMvaa 120,216,228\nD0 43,28,46,3072\nCV 206,92,93\nMvaa 124,220,228\nCV 207,94,95\nCV 208,96,97\nMvaa 128,192,232\ns_add_u32 m0, s47, 49152\n"
"s_add_u32 s46, s51, 1310720\nMvaa 132,196,232\nCV 209,98,99\nCV 210,100,101\nMvaa 136,200,232\nD0 6,32,46,0\nCV 211,102,103\nMvaa 140,204,232\nCV 212,104,105\nCV 213,106,107\n"
"Mvaa 144,208,232\nCV 214,108,109\nCV 215,110,111\nMvaa 148,212,232\nCV 216,112,113\nCV 217,114,115\nMvaa 152,216,232\nD0 7,32,46,1024\nCV 218,116,117\nMvaa 156,220,232\n"
"CV 219,118,119\nCV 220,120,121\nMvaa 160,192,236\nCV 221,122,123\nCV 222,124,125\nMvaa 164,196,236\nCV 223,126,127\nCV 224,128,129\nMvaa 168,200,236\nD0 8,32,46,2048\n"
"CV 225,130,131\nMvaa 172,204,236\nCV 226,132,133\nCV 227,134,135\nMvaa 176,208,236\nCV 228,136,137\nCV 229,138,139\nMvaa 180,212,236\nCV 230,140,141\nCV 231,142,143\n"
"Mvaa 184,216,236\nD0 9,32,46,3072\nCV 232,144,145\nMvaa 188,220,236\nCV 233,146,147\nCV 234,148,149\nMaaa 0,192,240\ns_add_u32 m0, s47, -32768\ns_add_u32 s46, s52, 384\n"
"Maaa 4,196,240\nCV 235,150,151\nCV 236,152,153\nMaaa 8,200,240\nD0 40,28,46,0\nCV 237,154,155\nMaaa 12,204,240\nCV 238,156,157\nCV 239,158,159\nMaaa 16,208,240\nRa 160,44,0\n"
"CV 240,160,161\nMaaa 20,212,240\nRa 164,44,2048\nCV 241,162,163\nMaaa 24,216,240\nD0 41,28,46,1024\nRa 168,46,0\nMaaa 28,220,240\nRa 172,46,2048\nCV 242,164,165\nMaaa 32,192,244\n"
"Ta 128,32,0\nCV 243,166,167\nMaaa 36,196,244\nTa 130,32,2048\nCV 244,168,169\nMaaa 40,200,244\nD0 42,28,46,2048\nTa 132,33,0\nMaaa 44,204,244\nTa 134,33,2048\nCV 245,170,171\n"
"Maaa 48,208,244\nTa 136,34,0\nCV 246,172,173\nMaaa 52,212,244\nTa 138,34,2048\nCV 247,174,175\nMaaa 56,216,244\nD0 43,28,46,3072\nTa 140,35,0\nMaaa 60,220,244\nTa 142,35,2048\n"
"CV 248,176,177\nMaaa 64,192,248\nTa 144,36,0\nCV 249,178,179\nMaaa 68,196,248\nTa 146,36,2048\nCV 250,180,181\nMaaa 72,200,248\nTa 148,37,0\nCV 251,182,183\nSv 192,1,16,75,0\n"
"Maaa 76,204,248\nTa 150,37,2048\nCV 252,184,185\nMaaa 80,208,248\nTa 152,38,0\nCV 253,186,187\nMaaa 84,212,248\nTa 154,38,2048\nCV 254,188,189\nMaaa 88,216,248\nTa 156,39,0\n"
"CV 255,190,191\nMaaa 92,220,248\nTa 158,39,2048\nMaaa 96,192,252\nSv 196,1,16,75,1024\nMaaa 100,196,252\nMaaa 104,200,252\nMaaa 108,204,252\nMaaa 112,208,252\nMaaa 116,212,252\n"
"Maaa 120,216,252\nSv 200,1,16,75,2048\nMaaa 124,220,252\nWL 0\ns_branch .Q16\n.T0K4:\nMvaa 64,128,160\nTa 192,12,16384\ns_mul_i32 s2, s12, 1083392\nMvaa 96,128,164\n"
"s_lshl_b32 s3, s11, 12\ns_add_u32 s2, s2, s3\ns_add_u32 m0, s47, 49152\ns_add_u32 s44, s44, 0x40000\nMvaa 128,128,168\nTa 194,12,18432\ns_add_u32 s89, s3, 0x0\nMvaa 160,128,172\n"
"s_mov_b32 s88, 0xffffbc00\ns_lshl_b32 s4, s13, 1\nD0 6,24,44,0\nMaaa 0,128,176\nTa 196,13,16384\ns_sub_u32 s79, 15, s4\nMaaa 32,128,180\ns_and_b32 s5, s12, 1\n"
"s_lshl1_add_u32 s5, s12, s5\nMaaa 64,128,184\nTa 198,13,18432\ns_mul_i32 s5, s5, 119808\nMaaa 96,128,188\ns_add_u32 s5, s5, s3\ns_add_u32 s96, s5, 0x5684400\nD0 7,24,44,1024\n"
"Maaa 100,132,188\nTa 200,14,16384\ns_mov_b32 s63, 0x53800000\nMaaa 68,132,184\ns_mov_b32 s64, 0x2b800000\ns_mov_b32 s98, s4\nMaaa 36,132,180\nTa 202,14,18432\n"
"s_lshl3_add_u32 s5, s12, s13\nMaaa 4,132,176\ns_lshl2_add_u32 s5, s5, s11\ns_lshl_b32 s65, s5, 9\nD0 8,24,44,2048\nMvaa 164,132,172\nTa 204,15,16384\n"
"s_add_u32 s65, s65, 0x5c00000\nMvaa 132,132,168\ns_mul_i32 s66, s5, 17408\ns_add_u32 s66, s66, 0x2c00000\nMvaa 100,132,164\nTa 206,15,18432\ns_lshr_b32 s58, s12, 2\n"
"Mvaa 68,132,160\ns_lshl_b32 s58, s58, 10\ns_add_u32 s58, s58, 0x5c48400\nD0 9,24,44,3072\nMvaa 72,136,160\nTa 208,16,16384\ns_lshl1_add_u32 s5, s12, 0\nMvaa 104,136,164\n"
"s_lshl3_add_u32 s5, s5, s13\ns_lshl4_add_u32 s54, s5, 0x5c48400\ns_add_u32 s6, s56, 0\nMvaa 136,136,168\nTa 210,16,18432\nMvaa 168,136,172\ns_cmp_eq_u32 s11, 0\n"
"s_cselect_b32 s54, s54, 0x40000000\nbuffer_load_dwordx4 v[192:195], v1, s[16:19], s6 offen offset:0 sc1\nMaaa 8,136,176\nTa 212,17,16384\ns_getpc_b64 s[8:9]\n.PC31:\n"
"s_add_u32 s8, s8, .HD10-.PC31\ns_addc_u32 s9, s9, 0\nMaaa 40,136,180\ns_mov_b32 s97, 0xff800000\ns_mov_b32 s90, 3\nMaaa 72,136,184\nTa 214,17,18432\ns_lshl_b32 s5, s11, 2\n"
"Maaa 104,136,188\ns_lshr_b32 s92, 0xf112, s5\ns_and_b32 s92, s92, 15\nbuffer_load_dwordx4 v[196:199], v1, s[16:19], s6 offen offset:1024 sc1\nMaaa 108,140,188\nTa 216,18,16384\n"
"s_lshr_b32 s93, s11, 1\nMaaa 76,140,184\ns_xor_b32 s93, s93, 1\ns_add_u32 s86, s2, 0x10800\nMaaa 44,140,180\nTa 218,18,18432\ns_add_u32 s87, s2, 0x110a400\nMaaa 12,140,176\n"
"s_mov_b32 s94, s86\ns_mov_b32 s95, s87\nbuffer_load_dwordx4 v[200:203], v1, s[16:19], s6 offen offset:2048 sc1\nMvaa 172,140,172\nTa 220,19,16384\nMvaa 140,140,168\n"
"s_cmp_eq_u32 s13, 7\ns_cselect_b32 s5, 0xfffffffd, 64\nMvaa 108,140,164\nTa 222,19,18432\ns_add_u32 s92, s92, s5\nMvaa 76,140,160\ns_add_u32 s93, s93, s5\n"
"s_or_b32 s92, s92, 0x100\nbuffer_load_dwordx4 v[204:207], v1, s[16:19], s6 offen offset:3072 sc1\nMvaa 80,144,160\nRa 224,11,0\nMvaa 112,144,164\ns_cmp_lt_u32 s12, 14\n"
"s_cselect_b32 s5, s11, 1\ns_add_u32 s6, s56, 4096\nMvaa 144,144,168\nRa 228,11,2048\nMvaa 176,144,172\ns_cmp_eq_u32 s5, 0\ns_cselect_b32 s91, 3, 100\n"
"buffer_load_dwordx4 v[208:211], v1, s[16:19], s6 offen offset:0 sc1\nMaaa 16,144,176\nRa 232,11,8192\nbuffer_load_dword v27, v1, s[16:19], s58 offen offset:0 sc0 sc1\n"
"Maaa 48,144,180\ns_lshr_b32 s2, s13, 2\ns_bfe_u32 s3, s13, 0x10001\nMaaa 80,144,184\nRa 236,11,10240\ns_mul_i32 s3, s3, 17\nMaaa 112,144,188\ns_add_u32 s2, s2, s3\n"
"s_xor_b32 s3, s14, 1\nbuffer_load_dwordx4 v[212:215], v1, s[16:19], s6 offen offset:1024 sc1\nMaaa 116,148,188\nRa 240,11,16384\ns_lshl3_add_u32 s2, s3, s2\nMaaa 84,148,184\n"
"s_lshl1_add_u32 s2, 0, s2\nMaaa 52,148,180\nRa 244,11,18432\nMaaa 20,148,176\ns_cmp_eq_u32 s2, 32\ns_cselect_b32 s4, 16, s2\n"
"buffer_load_dwordx4 v[216:219], v1, s[16:19], s6 offen offset:2048 sc1\nMvaa 180,148,172\nRa 248,11,24576\ns_mul_i32 s75, s4, 33792\nMvaa 148,148,168\ns_lshr_b32 s2, s13, 2\n"
"s_bfe_u32 s3, s13, 0x10001\nMvaa 116,148,164\nRa 252,11,26624\ns_mul_i32 s3, s3, 17\nMvaa 84,148,160\ns_add_u32 s2, s2, s3\ns_xor_b32 s3, s14, 1\n"
"buffer_load_dwordx4 v[220:223], v1, s[16:19], s6 offen offset:3072 sc1\nMvaa 88,152,160\ns_lshl3_add_u32 s2, s3, s2\ns_lshl1_add_u32 s2, 1, s2\nMvaa 120,152,164\n"
"s_cmp_eq_u32 s2, 32\ns_cselect_b32 s4, 16, s2\ns_add_u32 s6, s56, 8192\nMvaa 152,152,168\ns_mul_i32 s76, s4, 33792\ns_lshr_b32 s2, s13, 2\nMvaa 184,152,172\n"
"s_bfe_u32 s3, s13, 0x10001\ns_mul_i32 s3, s3, 17\nbuffer_load_dwordx4 v[224:227], v1, s[16:19], s6 offen offset:0 sc1\nMaaa 24,152,176\ns_add_u32 s2, s2, s3\ns_xor_b32 s3, s14, 1\n"
"Maaa 56,152,180\ns_lshl3_add_u32 s2, s3, s2\ns_lshl1_add_u32 s2, 2, s2\nMaaa 88,152,184\ns_cmp_eq_u32 s2, 32\ns_cselect_b32 s4, 16, s2\nMaaa 120,152,188\ns_mul_i32 s77, s4, 33792\n"
"s_lshr_b32 s2, s13, 2\nbuffer_load_dwordx4 v[228:231], v1, s[16:19], s6 offen offset:1024 sc1\nMaaa 124,156,188\ns_bfe_u32 s3, s13, 0x10001\ns_mul_i32 s3, s3, 17\nMaaa 92,156,184\n"
"s_add_u32 s2, s2, s3\ns_xor_b32 s3, s14, 1\nMaaa 60,156,180\ns_lshl3_add_u32 s2, s3, s2\ns_lshl1_add_u32 s2, 3, s2\nMaaa 28,156,176\ns_cmp_eq_u32 s2, 32\ns_cselect_b32 s4, 16, s2\n"
"buffer_load_dwordx4 v[232:235], v1, s[16:19], s6 offen offset:2048 sc1\nMvaa 188,156,172\ns_mul_i32 s78, s4, 33792\ns_mul_i32 s2, s12, 1083392\nMvaa 156,156,168\n"
"s_lshr_b32 s3, s11, 1\ns_lshl_b32 s3, s3, 14\nMvaa 124,156,164\ns_add_u32 s2, s2, s3\ns_and_b32 s3, s11, 1\nMvaa 92,156,160\ns_waitcnt vmcnt(16) lgkmcnt(0)\ns_barrier\n"
"Mvaa 64,192,224\nTa 128,12,32768\ns_lshl_b32 s3, s3, 12\nbuffer_load_dwordx4 v[236:239], v1, s[16:19], s6 offen offset:3072 sc1\nMvaa 96,192,228\ns_add_u32 s2, s2, s3\n"
"s_add_u32 s2, s2, 0x0\nMvaa 128,192,232\nTa 130,12,34816\ns_mov_b32 s55, 8192\ns_add_u32 s6, s56, 12288\nMvaa 160,192,236\ns_add_u32 s75, s75, s2\ns_add_u32 s76, s76, s2\n"
"Maaa 0,192,240\nTa 132,13,32768\ns_add_u32 s77, s77, s2\nbuffer_load_dwordx4 v[240:243], v1, s[16:19], s6 offen offset:0 sc1\nMaaa 32,192,244\ns_add_u32 s78, s78, s2\n"
"Maaa 64,192,248\nTa 134,13,34816\nMaaa 96,192,252\nMaaa 100,196,252\nTa 136,14,32768\nbuffer_load_dwordx4 v[244:247], v1, s[16:19], s6 offen offset:1024 sc1\nMaaa 68,196,248\n"
"Maaa 36,196,244\nTa 138,14,34816\nMaaa 4,196,240\nMvaa 164,196,236\nTa 140,15,32768\nbuffer_load_dwordx4 v[248:251], v1, s[16:19], s6 offen offset:2048 sc1\nMvaa 132,196,232\n"
"Mvaa 100,196,228\nTa 142,15,34816\nMvaa 68,196,224\nMvaa 72,200,224\nTa 144,16,32768\nbuffer_load_dwordx4 v[252:255], v1, s[16:19], s6 offen offset:3072 sc1\nMvaa 104,200,228\n"
"Mvaa 136,200,232\nTa 146,16,34816\nMvaa 168,200,236\nMaaa 8,200,240\nTa 148,17,32768\nMaaa 40,200,244\nMaaa 72,200,248\nTa 150,17,34816\nMaaa 104,200,252\nMaaa 108,204,252\n"
"Ta 152,18,32768\nMaaa 76,204,248\nMaaa 44,204,244\nTa 154,18,34816\nMaaa 12,204,240\nMvaa 172,204,236\nTa 156,19,32768\nMvaa 140,204,232\nMvaa 108,204,228\nTa 158,19,34816\n"
"Mvaa 76,204,224\nMvaa 80,208,224\nRa 160,10,32768\nMvaa 112,208,228\nMvaa 144,208,232\nRa 164,10,34816\nMvaa 176,208,236\nMaaa 16,208,240\nRa 168,10,40960\nMaaa 48,208,244\n"
"Maaa 80,208,248\nRa 172,10,43008\nMaaa 112,208,252\nMaaa 116,212,252\nRa 176,10,49152\nWV 11\nv_cmp_ne_u32 vcc, s15, v27\ns_and_b32 s2, s12, 3\ns_lshl_b32 s2, s2, 4\ns_nop 1\n"
"s_lshr_b64 vcc, vcc, s2\ns_and_b64 vcc, vcc, 0xff00\ns_cbranch_vccnz .S32\n.S33:\ns_sub_u32 s45, s86, 67584\nMaaa 84,212,248\ns_add_u32 m0, s47, 0\ns_add_u32 s46, s45, 0x0\n"
"Maaa 52,212,244\nRa 180,10,51200\nMaaa 20,212,240\nD1 1,16,46,0\nMvaa 180,212,236\nRa 184,10,57344\nMvaa 148,212,232\nMvaa 116,212,228\nRa 188,10,59392\nMvaa 84,212,224\n"
"D1 1,16,46,1024\nMvaa 88,216,224\nMvaa 120,216,228\nMvaa 152,216,232\nMvaa 184,216,236\nD1 1,16,46,2048\nMaaa 24,216,240\nMaaa 56,216,244\nMaaa 88,216,248\nMaaa 120,216,252\n"
"D1 1,16,46,3072\nMaaa 124,220,252\nMaaa 92,220,248\nMaaa 60,220,244\nMaaa 28,220,240\nMvaa 188,220,236\nMvaa 156,220,232\nMvaa 124,220,228\nMvaa 92,220,224\nWL 0\ns_barrier\n"
"Mvaa 64,128,160\nTa 192,12,49152\nMvaa 96,128,164\ns_add_u32 m0, s47, 32768\ns_add_u32 s46, s45, 0x4000\nMvaa 128,128,168\nTa 194,12,51200\nMvaa 160,128,172\nD1 1,16,46,0\n"
"Maaa 0,128,176\nTa 196,13,49152\nMaaa 32,128,180\nMaaa 64,128,184\nTa 198,13,51200\nMaaa 96,128,188\nD1 1,16,46,1024\nMaaa 100,132,188\nTa 200,14,49152\nMaaa 68,132,184\n"
"Maaa 36,132,180\nTa 202,14,51200\nMaaa 4,132,176\nD1 1,16,46,2048\nMvaa 164,132,172\nTa 204,15,49152\nMvaa 132,132,168\nMvaa 100,132,164\nTa 206,15,51200\nMvaa 68,132,160\n"
"D1 1,16,46,3072\nMvaa 72,136,160\nTa 208,16,49152\nMvaa 104,136,164\ns_add_u32 m0, s47, -65536\ns_add_u32 s46, s45, 0x8400\nMvaa 136,136,168\nTa 210,16,51200\nMvaa 168,136,172\n"
"D1 1,16,46,0\nMaaa 8,136,176\nTa 212,17,49152\nMaaa 40,136,180\nMaaa 72,136,184\nTa 214,17,51200\nMaaa 104,136,188\nD1 1,16,46,1024\nMaaa 108,140,188\nTa 216,18,49152\n"
"Maaa 76,140,184\nMaaa 44,140,180\nTa 218,18,51200\nMaaa 12,140,176\nD1 1,16,46,2048\nMvaa 172,140,172\nTa 220,19,49152\nMvaa 140,140,168\nMvaa 108,140,164\nTa 222,19,51200\n"
"Mvaa 76,140,160\nD1 1,16,46,3072\nMvaa 80,144,160\nRa 224,11,32768\nMvaa 112,144,164\ns_add_u32 m0, s47, -49152\ns_add_u32 s46, s45, 0x1106000\nMvaa 144,144,168\nRa 228,11,34816\n"
"Mvaa 176,144,172\nD1 1,16,46,0\nMaaa 16,144,176\nRa 232,11,40960\nMaaa 48,144,180\nMaaa 80,144,184\nRa 236,11,43008\nMaaa 112,144,188\nD1 1,16,46,1024\nMaaa 116,148,188\n"
"Ra 240,11,49152\nMaaa 84,148,184\nMaaa 52,148,180\nRa 244,11,51200\nMaaa 20,148,176\nD1 1,16,46,2048\nMvaa 180,148,172\nRa 248,11,57344\nMvaa 148,148,168\nMvaa 116,148,164\n"
"Ra 252,11,59392\nMvaa 84,148,160\nD1 1,16,46,3072\nMvaa 88,152,160\nMvaa 120,152,164\nv_add_u32 v2, 0x10000, v1\ns_lshr_b32 s2, s59, 1\nMvaa 152,152,168\nv_add_u32 v24, s2, v2\n"
"v_xor_b32 v26, 0x1000, v24\nMvaa 184,152,172\nv_lshrrev_b32 v20, 4, v0\nv_lshlrev_b32 v20, 2, v20\nMaaa 24,152,176\nv_and_b32 v0, 15, v0\nv_sub_u32 v0, v0, v20\nMaaa 56,152,180\n"
"s_and_b32 s2, s11, 1\ns_lshl_b32 s2, s2, 6\nMaaa 88,152,184\nv_subrev_u32 v3, s2, v0\ns_xor_b32 s2, s2, 64\nMaaa 120,152,188\nv_subrev_u32 v0, s2, v0\nv_mov_b32 v4, 0\n"
"Maaa 124,156,188\nv_mov_b32 v5, 0\nv_mov_b32 v6, 0\nMaaa 92,156,184\nv_mov_b32 v7, 0\nv_mov_b32 v8, 0\nMaaa 60,156,180\nv_mov_b32 v9, 0\nv_mov_b32 v10, 0\nMaaa 28,156,176\n"
"v_mov_b32 v11, 0\nv_mov_b32 v12, 0\nMvaa 188,156,172\nv_mov_b32 v13, 0\nv_mov_b32 v14, 0\nMvaa 156,156,168\nv_mov_b32 v15, 0\nv_mov_b32 v16, 0\nMvaa 124,156,164\nv_mov_b32 v17, 0\n"
"v_mov_b32 v18, 0\nMvaa 92,156,160\ns_waitcnt vmcnt(12) lgkmcnt(0)\ns_barrier\nMvaa 64,192,224\nv_mov_b32 v19, 0\nMvaa 68,196,224\ns_add_u32 m0, s47, 49152\n"
"s_add_u32 s46, s45, 0x1102000\nMvaa 72,200,224\nMvaa 76,204,224\nD1 1,16,46,0\nMvaa 80,208,224\nMvaa 84,212,224\nMvaa 88,216,224\nCV 32,64,65\nCV 33,66,67\nMvaa 92,220,224\n"
"D1 1,16,46,1024\nCV 34,68,69\nMvaa 96,192,228\nCV 35,70,71\nCV 36,72,73\nMvaa 100,196,228\nCV 37,74,75\nCV 38,76,77\nMvaa 104,200,228\nCV 39,78,79\nCV 40,80,81\nMvaa 108,204,228\n"
"D1 1,16,46,2048\nCV 41,82,83\nMvaa 112,208,228\nCV 42,84,85\nCV 43,86,87\nMvaa 116,212,228\nCV 44,88,89\nCV 45,90,91\nMvaa 120,216,228\nCV 46,92,93\nCV 47,94,95\nMvaa 124,220,228\n"
"D1 1,16,46,3072\nCV 48,96,97\nMvaa 128,192,232\nCV 49,98,99\nCV 50,100,101\nMvaa 132,196,232\nCV 51,102,103\nCV 52,104,105\nMvaa 136,200,232\nCV 53,106,107\nCV 54,108,109\n"
"Mvaa 140,204,232\nCV 55,110,111\nCV 56,112,113\nMvaa 144,208,232\nCV 57,114,115\nCV 58,116,117\nMvaa 148,212,232\nCV 59,118,119\nCV 60,120,121\nMvaa 152,216,232\nCV 61,122,123\n"
"CV 62,124,125\nMvaa 156,220,232\nCV 63,126,127\nCV 96,128,129\nMvaa 160,192,236\nCV 97,130,131\nCV 98,132,133\nMvaa 164,196,236\nCV 99,134,135\nCV 100,136,137\nSv 32,1,16,75,0\n"
"Mvaa 168,200,236\nCV 101,138,139\nCV 102,140,141\nMvaa 172,204,236\nCV 103,142,143\nCV 104,144,145\nMvaa 176,208,236\nCV 105,146,147\nCV 106,148,149\nMvaa 180,212,236\n"
"CV 107,150,151\nCV 108,152,153\nSv 36,1,16,75,1024\nMvaa 184,216,236\nCV 109,154,155\nCV 110,156,157\nMvaa 188,220,236\nCV 111,158,159\nCV 112,160,161\nMaaa 0,192,240\n"
"CV 113,162,163\nCV 114,164,165\nMaaa 4,196,240\nCV 115,166,167\nCV 116,168,169\nSv 40,1,16,75,2048\nMaaa 8,200,240\nCV 117,170,171\nCV 118,172,173\nMaaa 12,204,240\n"
"CV 119,174,175\nCV 120,176,177\nMaaa 16,208,240\nCV 121,178,179\nCV 122,180,181\nMaaa 20,212,240\nCV 123,182,183\nCV 124,184,185\nSv 44,1,16,75,3072\nMaaa 24,216,240\n"
"CV 125,186,187\nCV 126,188,189\nMaaa 28,220,240\nCV 127,190,191\nv_mov_b32 v86, 0\ns_add_u32 s75, s75, s55\nMaaa 32,192,244\nv_mov_b32 v87, 0\nv_mov_b32 v88, 0\nMaaa 36,196,244\n"
"v_mov_b32 v89, 0\nSv 48,1,16,75,0\nMaaa 40,200,244\nAR 92,0\nAR 93,1\nMaaa 44,204,244\nCV 128,92,93\nAR 92,2\nMaaa 48,208,244\nAR 93,3\nCV 129,92,93\nMaaa 52,212,244\n"
"Sv 52,1,16,75,1024\nAR 92,4\nMaaa 56,216,244\nAR 93,5\nCV 130,92,93\nMaaa 60,220,244\nAR 92,6\nAR 93,7\nMaaa 64,192,248\nRa 128,24,0\nCV 131,92,93\nMaaa 68,196,248\n"
"Sv 56,1,16,75,2048\nAR 92,8\nMaaa 72,200,248\nRa 132,24,1024\nAR 93,9\nMaaa 76,204,248\nCV 132,92,93\nAR 92,10\nMaaa 80,208,248\nRa 136,24,2048\nAR 93,11\nMaaa 84,212,248\n"
"Sv 60,1,16,75,3072\nCV 133,92,93\nMaaa 88,216,248\nRa 140,24,3072\nAR 92,12\nMaaa 92,220,248\nAR 93,13\nCV 134,92,93\nMaaa 96,192,252\nRa 144,26,0\nAR 92,14\nMaaa 100,196,252\n"
"AR 93,15\nCV 135,92,93\nMaaa 104,200,252\nRa 148,26,1024\nMaaa 108,204,252\nMaaa 112,208,252\nRa 152,26,2048\nMaaa 116,212,252\nMaaa 120,216,252\nRa 156,26,3072\nMaaa 124,220,252\n"
"WL 4\ns_branch .F07\n.S27:\ns_mov_b32 s7, 262144\n.S29:\ns_sleep 1\nbuffer_load_dwordx4 v[32:35], v1, s[16:19], s6 offen offset:0 sc0 sc1\n"
"buffer_load_dwordx4 v[36:39], v1, s[16:19], s6 offen offset:1024 sc0 sc1\nbuffer_load_dwordx4 v[40:43], v1, s[16:19], s6 offen offset:2048 sc0 sc1\n"
"buffer_load_dwordx4 v[44:47], v1, s[16:19], s6 offen offset:3072 sc0 sc1\nWV 0\nv_xor_b32 v48, s15, v33\nv_xor_b32 v49, s15, v35\nv_xor_b32 v50, s15, v37\nv_xor_b32 v51, s15, v39\n"
"v_xor_b32 v52, s15, v41\nv_xor_b32 v53, s15, v43\nv_xor_b32 v54, s15, v45\nv_xor_b32 v55, s15, v47\nv_or3_b32 v48, v48, v49, v50\nv_or3_b32 v51, v51, v52, v53\n"
"v_or3_b32 v48, v48, v54, v55\nv_or_b32 v48, v48, v51\nv_cmp_ne_u32 vcc, 0, v48\ns_nop 0\ns_cbranch_vccz .S28\ns_sub_u32 s7, s7, 1\ns_cbranch_scc0 .S29\ns_branch .S28\n.S32:\n"
"s_mov_b32 s7, 262144\n.S34:\ns_sleep 1\nbuffer_load_dword v27, v1, s[16:19], s58 offen offset:0 sc0 sc1\nWV 0\nv_cmp_ne_u32 vcc, s15, v27\ns_and_b32 s2, s12, 3\n"
"s_lshl_b32 s2, s2, 4\ns_nop 1\ns_lshr_b64 vcc, vcc, s2\ns_and_b64 vcc, vcc, 0xff00\ns_cbranch_vccz .S33\ns_sub_u32 s7, s7, 1\ns_cbranch_scc0 .S34\ns_branch .S33\n.MV3:\n"
"Mvaa 64,160,128\nTa 192,12,16384\ns_mov_b32 s36, s60\nMvaa 68,164,128\ns_and_b32 s37, s61, 0xffff\ns_mov_b32 s38, 0x380000\nMvaa 128,168,128\nTa 194,12,18432\n"
"s_mov_b32 s39, 0x20000\nMvaa 132,172,128\ns_add_u32 m0, s47, 49152\ns_add_u32 s44, s44, 0x40000\nMaaa 0,176,128\nTa 196,13,16384\ns_lshr_b32 s4, s13, 1\nMaaa 4,180,128\n"
"D0 6,24,44,0\ns_lshl2_add_u32 s4, s14, s4\nMaaa 64,184,128\nTa 198,13,18432\ns_lshl3_add_u32 s2, s12, s4\nMaaa 68,188,128\ns_lshl2_add_u32 s2, s2, s11\ns_mul_i32 s2, s2, 7168\n"
"Maaa 76,188,132\nTa 200,14,16384\ns_mul_i32 s3, s11, 7168\nMaaa 72,184,132\ns_add_u32 s3, s3, 131072\nMaaa 12,180,132\nTa 202,14,18432\nMaaa 8,176,132\nMvaa 140,172,132\n"
"Ta 204,15,16384\nMvaa 136,168,132\nD0 7,24,44,1024\nMvaa 76,164,132\nTa 206,15,18432\nMvaa 72,160,132\nMvaa 80,160,136\nTa 208,16,16384\nMvaa 84,164,136\nMvaa 144,168,136\n"
"Ta 210,16,18432\nMvaa 148,172,136\nMaaa 16,176,136\nTa 212,17,16384\nMaaa 20,180,136\nD0 8,24,44,2048\nMaaa 80,184,136\nTa 214,17,18432\nMaaa 84,188,136\nMaaa 92,188,140\n"
"Ta 216,18,16384\nMaaa 88,184,140\nMaaa 28,180,140\nTa 218,18,18432\nMaaa 24,176,140\nMvaa 156,172,140\nTa 220,19,16384\nMvaa 152,168,140\nD0 9,24,44,3072\nMvaa 92,164,140\n"
"Ta 222,19,18432\ns_add_u32 m0, s3, 0\ns_add_u32 s5, s2, 0\nD0 1,36,5,0\nD0 1,36,5,1024\nMvaa 88,160,140\nMvaa 96,160,144\nRa 224,11,0\nMvaa 100,164,144\nMvaa 160,168,144\n"
"Ra 228,11,2048\nMvaa 164,172,144\ns_add_u32 m0, s47, 0\ns_add_u32 s44, s44, 0x40000\nMaaa 32,176,144\nRa 232,11,8192\nMaaa 36,180,144\nD0 6,24,44,0\nMaaa 96,184,144\n"
"Ra 236,11,10240\nMaaa 100,188,144\nMaaa 108,188,148\nRa 240,11,16384\nMaaa 104,184,148\nMaaa 44,180,148\nRa 244,11,18432\nMaaa 40,176,148\nSv 20,1,16,49,0\n"
"s_add_u32 s49, s49, 1024\nMvaa 172,172,148\nRa 248,11,24576\nMvaa 168,168,148\nD0 7,24,44,1024\nMvaa 108,164,148\nRa 252,11,26624\nMvaa 104,160,148\nMvaa 112,160,152\n"
"Mvaa 116,164,152\nSv 20,1,16,49,0\ns_add_u32 s49, s49, 1024\nMvaa 176,168,152\nMvaa 180,172,152\nMaaa 48,176,152\nMaaa 52,180,152\nD0 8,24,44,2048\nMaaa 112,184,152\n"
"Maaa 116,188,152\nMaaa 124,188,156\nMaaa 120,184,156\nSv 20,1,16,49,0\ns_add_u32 s49, s49, 1024\nMaaa 60,180,156\nMaaa 56,176,156\nMvaa 188,172,156\nMvaa 184,168,156\n"
"D0 9,24,44,3072\nMvaa 124,164,156\ns_add_u32 m0, s3, 2048\ns_add_u32 s5, s2, 2048\nD0 1,36,5,0\nD0 1,36,5,1024\nMvaa 120,160,156\ns_waitcnt vmcnt(15) lgkmcnt(0)\ns_barrier\n"
"Mvaa 64,224,192\nTa 128,12,32768\nMvaa 68,228,192\nMvaa 128,232,192\nTa 130,12,34816\nMvaa 132,236,192\ns_add_u32 m0, s48, 0\ns_add_u32 s45, s45, 128\nMaaa 0,240,192\n"
"Ta 132,13,32768\nMaaa 4,244,192\nD0 2,20,45,0\nMaaa 64,248,192\nTa 134,13,34816\nMaaa 68,252,192\nMaaa 76,252,196\nTa 136,14,32768\nMaaa 72,248,196\nMaaa 12,244,196\n"
"Ta 138,14,34816\nMaaa 8,240,196\nMvaa 140,236,196\nTa 140,15,32768\nMvaa 136,232,196\nD0 3,20,45,1024\nMvaa 76,228,196\nTa 142,15,34816\nMvaa 72,224,196\nMvaa 80,224,200\n"
"Ta 144,16,32768\nMvaa 84,228,200\nMvaa 144,232,200\nTa 146,16,34816\nMvaa 148,236,200\nMaaa 16,240,200\nTa 148,17,32768\nMaaa 20,244,200\nD0 4,20,45,2048\nMaaa 80,248,200\n"
"Ta 150,17,34816\nMaaa 84,252,200\nMaaa 92,252,204\nTa 152,18,32768\nMaaa 88,248,204\nMaaa 28,244,204\nTa 154,18,34816\nMaaa 24,240,204\nMvaa 156,236,204\nTa 156,19,32768\n"
"Mvaa 152,232,204\nD0 5,20,45,3072\nMvaa 92,228,204\nTa 158,19,34816\ns_add_u32 m0, s3, 4096\ns_add_u32 s5, s2, 4096\nD0 1,36,5,0\nD0 1,36,5,1024\nMvaa 88,224,204\nMvaa 96,224,208\n"
"Ra 160,10,32768\nMvaa 100,228,208\nMvaa 160,232,208\nRa 164,10,34816\nMvaa 164,236,208\ns_add_u32 m0, s48, 4096\ns_add_u32 s46, s45, 0x10000\nMaaa 32,240,208\nRa 168,10,40960\n"
"Maaa 36,244,208\nD0 2,20,46,0\nMaaa 96,248,208\nRa 172,10,43008\nMaaa 100,252,208\nMaaa 108,252,212\nRa 176,10,49152\nMaaa 104,248,212\nMaaa 44,244,212\nRa 180,10,51200\n"
"Maaa 40,240,212\nMvaa 172,236,212\nRa 184,10,57344\nMvaa 168,232,212\nD0 3,20,46,1024\nMvaa 108,228,212\nRa 188,10,59392\nMvaa 104,224,212\nMvaa 112,224,216\nMvaa 116,228,216\n"
"Mvaa 176,232,216\nMvaa 180,236,216\nMaaa 48,240,216\nMaaa 52,244,216\nD0 4,20,46,2048\nMaaa 112,248,216\nMaaa 116,252,216\nMaaa 124,252,220\nMaaa 120,248,220\nMaaa 60,244,220\n"
"Maaa 56,240,220\nMvaa 188,236,220\nMvaa 184,232,220\nD0 5,20,46,3072\nMvaa 124,228,220\ns_add_u32 m0, s3, 6144\ns_add_u32 s5, s2, 6144\nD0 1,36,5,0\nMvaa 120,224,220\n"
"s_waitcnt vmcnt(22) lgkmcnt(0)\ns_barrier\nMvaa 64,160,128\nTa 192,12,49152\nMvaa 68,164,128\nMvaa 128,168,128\nTa 194,12,51200\nMvaa 132,172,128\ns_add_u32 m0, s47, 16384\n"
"s_add_u32 s44, s44, 0x40000\nMaaa 0,176,128\nTa 196,13,49152\nMaaa 4,180,128\nD0 6,24,44,0\nMaaa 64,184,128\nTa 198,13,51200\nMaaa 68,188,128\nMaaa 76,188,132\nTa 200,14,49152\n"
"Maaa 72,184,132\nbuffer_store_dword v25, off, s[16:19], s90 offset:0 sc0 sc1\nMaaa 12,180,132\nTa 202,14,51200\nMaaa 8,176,132\nMvaa 140,172,132\nTa 204,15,49152\n"
"Mvaa 136,168,132\nD0 7,24,44,1024\nMvaa 76,164,132\nTa 206,15,51200\nMvaa 72,160,132\nMvaa 80,160,136\nTa 208,16,49152\nMvaa 84,164,136\nMvaa 144,168,136\nTa 210,16,51200\n"
"Mvaa 148,172,136\nMaaa 16,176,136\nTa 212,17,49152\nMaaa 20,180,136\nD0 8,24,44,2048\nMaaa 80,184,136\nTa 214,17,51200\nMaaa 84,188,136\nMaaa 92,188,140\nTa 216,18,49152\n"
"Maaa 88,184,140\nMaaa 28,180,140\nTa 218,18,51200\nMaaa 24,176,140\nMvaa 156,172,140\nTa 220,19,49152\nMvaa 152,168,140\nD0 9,24,44,3072\nMvaa 92,164,140\nTa 222,19,51200\n"
"Mvaa 88,160,140\nMvaa 96,160,144\nRa 224,11,32768\nMvaa 100,164,144\nMvaa 160,168,144\nRa 228,11,34816\nMvaa 164,172,144\ns_add_u32 m0, s47, 32768\ns_add_u32 s44, s44, 0x40000\n"
"Maaa 32,176,144\nRa 232,11,40960\nMaaa 36,180,144\nD0 6,24,44,0\nMaaa 96,184,144\nRa 236,11,43008\nMaaa 100,188,144\nMaaa 108,188,148\nRa 240,11,49152\nMaaa 104,184,148\n"
"Maaa 44,180,148\nRa 244,11,51200\nMaaa 40,176,148\nSv 20,1,16,49,0\ns_add_u32 s49, s49, 1024\nMvaa 172,172,148\nRa 248,11,57344\nMvaa 168,168,148\nD0 7,24,44,1024\n"
"Mvaa 108,164,148\nRa 252,11,59392\nMvaa 104,160,148\nMvaa 112,160,152\nMvaa 116,164,152\nSv 20,1,16,49,0\ns_add_u32 s49, s49, 1024\nMvaa 176,168,152\nMvaa 180,172,152\n"
"Maaa 48,176,152\nMaaa 52,180,152\nD0 8,24,44,2048\nMaaa 112,184,152\nMaaa 116,188,152\nMaaa 124,188,156\nMaaa 120,184,156\nSv 20,1,16,49,0\ns_add_u32 s49, s49, 1024\n"
"Maaa 60,180,156\nMaaa 56,176,156\nMvaa 188,172,156\nMvaa 184,168,156\nD0 9,24,44,3072\nMvaa 124,164,156\nMvaa 120,160,156\ns_waitcnt vmcnt(13) lgkmcnt(0)\ns_barrier\n"
"Mvaa 64,224,192\nTa 128,12,0\nMvaa 68,228,192\nMvaa 128,232,192\nTa 130,12,2048\nMvaa 132,236,192\ns_add_u32 m0, s48, 32768\ns_add_u32 s45, s45, 128\nMaaa 0,240,192\nTa 132,13,0\n"
"Maaa 4,244,192\nD0 2,20,45,0\nMaaa 64,248,192\nTa 134,13,2048\nMaaa 68,252,192\nMaaa 76,252,196\nTa 136,14,0\nMaaa 72,248,196\nMaaa 12,244,196\nTa 138,14,2048\nMaaa 8,240,196\n"
"Mvaa 140,236,196\nTa 140,15,0\nMvaa 136,232,196\nD0 3,20,45,1024\nMvaa 76,228,196\nTa 142,15,2048\nMvaa 72,224,196\nMvaa 80,224,200\nTa 144,16,0\nMvaa 84,228,200\n"
"Mvaa 144,232,200\nTa 146,16,2048\nMvaa 148,236,200\nMaaa 16,240,200\nTa 148,17,0\nMaaa 20,244,200\nD0 4,20,45,2048\nMaaa 80,248,200\nTa 150,17,2048\nMaaa 84,252,200\n"
"Maaa 92,252,204\nTa 152,18,0\nMaaa 88,248,204\nMaaa 28,244,204\nTa 154,18,2048\nMaaa 24,240,204\nMvaa 156,236,204\nTa 156,19,0\nMvaa 152,232,204\nD0 5,20,45,3072\nMvaa 92,228,204\n"
"Ta 158,19,2048\nMvaa 88,224,204\nMvaa 96,224,208\nRa 160,10,0\nMvaa 100,228,208\nMvaa 160,232,208\nRa 164,10,2048\nMvaa 164,236,208\ns_add_u32 m0, s48, 36864\n"
"s_add_u32 s46, s45, 0x10000\nMaaa 32,240,208\nRa 168,10,8192\nMaaa 36,244,208\nD0 2,20,46,0\nMaaa 96,248,208\nRa 172,10,10240\nMaaa 100,252,208\nMaaa 108,252,212\nRa 176,10,16384\n"
"Maaa 104,248,212\nMaaa 44,244,212\nRa 180,10,18432\nMaaa 40,240,212\nMvaa 172,236,212\nRa 184,10,24576\nMvaa 168,232,212\nD0 3,20,46,1024\nMvaa 108,228,212\nRa 188,10,26624\n"
"Mvaa 104,224,212\nMvaa 112,224,216\nMvaa 116,228,216\nMvaa 176,232,216\nMvaa 180,236,216\nMaaa 48,240,216\nMaaa 52,244,216\nD0 4,20,46,2048\nMaaa 112,248,216\nMaaa 116,252,216\n"
"Maaa 124,252,220\nMaaa 120,248,220\nMaaa 60,244,220\nMaaa 56,240,220\nMvaa 188,236,220\nMvaa 184,232,220\nD0 5,20,46,3072\nMvaa 124,228,220\nMvaa 120,224,220\n"
"s_waitcnt vmcnt(15) lgkmcnt(0)\ns_barrier\nMvaa 64,160,128\nTa 192,12,16384\nMvaa 68,164,128\nMvaa 128,168,128\nTa 194,12,18432\nMvaa 132,172,128\ns_add_u32 m0, s47, 49152\n"
"s_add_u32 s44, s44, 0x40000\nMaaa 0,176,128\nTa 196,13,16384\nMaaa 4,180,128\nD0 6,24,44,0\nMaaa 64,184,128\nTa 198,13,18432\nMaaa 68,188,128\nMaaa 76,188,132\nTa 200,14,16384\n"
"Maaa 72,184,132\nMaaa 12,180,132\nTa 202,14,18432\nMaaa 8,176,132\nMvaa 140,172,132\nTa 204,15,16384\nMvaa 136,168,132\nD0 7,24,44,1024\nMvaa 76,164,132\nTa 206,15,18432\n"
"Mvaa 72,160,132\nMvaa 80,160,136\nTa 208,16,16384\nMvaa 84,164,136\nMvaa 144,168,136\nTa 210,16,18432\nMvaa 148,172,136\nMaaa 16,176,136\nTa 212,17,16384\nMaaa 20,180,136\n"
"D0 8,24,44,2048\nMaaa 80,184,136\nTa 214,17,18432\nMaaa 84,188,136\nMaaa 92,188,140\nTa 216,18,16384\nMaaa 88,184,140\nMaaa 28,180,140\nTa 218,18,18432\nMaaa 24,176,140\n"
"Mvaa 156,172,140\nTa 220,19,16384\nMvaa 152,168,140\nD0 9,24,44,3072\nMvaa 92,164,140\nTa 222,19,18432\nMvaa 88,160,140\nMvaa 96,160,144\nRa 224,11,0\nMvaa 100,164,144\n"
"Mvaa 160,168,144\nRa 228,11,2048\nMvaa 164,172,144\ns_add_u32 m0, s47, 0\ns_add_u32 s44, s44, 0x40000\nMaaa 32,176,144\nRa 232,11,8192\nMaaa 36,180,144\nD0 6,24,44,0\n"
"Maaa 96,184,144\nRa 236,11,10240\nMaaa 100,188,144\nMaaa 108,188,148\nRa 240,11,16384\nMaaa 104,184,148\nMaaa 44,180,148\nRa 244,11,18432\nMaaa 40,176,148\nSv 20,1,16,49,0\n"
"s_add_u32 s49, s49, 1024\nMvaa 172,172,148\nRa 248,11,24576\nMvaa 168,168,148\nD0 7,24,44,1024\nMvaa 108,164,148\nRa 252,11,26624\nMvaa 104,160,148\nMvaa 112,160,152\n"
"Mvaa 116,164,152\nSv 20,1,16,49,0\ns_add_u32 s49, s49, 1024\nMvaa 176,168,152\nMvaa 180,172,152\nMaaa 48,176,152\nMaaa 52,180,152\nD0 8,24,44,2048\nMaaa 112,184,152\n"
"Maaa 116,188,152\nMaaa 124,188,156\nMaaa 120,184,156\nSv 20,1,16,49,0\ns_add_u32 s49, s49, 1024\nMaaa 60,180,156\nMaaa 56,176,156\nMvaa 188,172,156\nMvaa 184,168,156\n"
"D0 9,24,44,3072\nMvaa 124,164,156\nMvaa 120,160,156\ns_waitcnt vmcnt(11) lgkmcnt(0)\ns_barrier\nMvaa 64,224,192\nTa 128,12,32768\nv_lshrrev_b32 v32, 2, v0\nMvaa 68,228,192\n"
"v_mul_u32_u24 v32, 896, v32\nv_and_b32 v33, 3, v0\nMvaa 128,232,192\nTa 130,12,34816\nv_lshl_add_u32 v32, v33, 4, v32\nMvaa 132,236,192\ns_add_u32 m0, s48, 0\n"
"s_add_u32 s45, s45, 128\nMaaa 0,240,192\nTa 132,13,32768\ns_lshr_b32 s2, s11, 1\nMaaa 4,244,192\nD0 2,20,45,0\ns_mul_i32 s3, s2, 448\nMaaa 64,248,192\nTa 134,13,34816\n"
"s_and_b32 s5, s11, 1\nMaaa 68,252,192\ns_lshl3_add_u32 s3, s5, s3\ns_add_u32 s3, s3, 131072\nMaaa 76,252,196\nTa 136,14,32768\nv_add_u32 v32, s3, v32\nMaaa 72,248,196\nTv 36,32,0\n"
"Tv 38,32,14336\nMaaa 12,244,196\nTa 138,14,34816\nTv 40,32,64\nMaaa 8,240,196\nTv 42,32,14400\nTv 44,32,128\nMvaa 140,236,196\nTa 140,15,32768\nTv 46,32,14464\nMvaa 136,232,196\n"
"D0 3,20,45,1024\nTv 48,32,192\nMvaa 76,228,196\nTa 142,15,34816\nTv 50,32,14528\nMvaa 72,224,196\nTv 52,32,256\nTv 54,32,14592\nMvaa 80,224,200\nTa 144,16,32768\nTv 56,32,320\n"
"Mvaa 84,228,200\nTv 58,32,14656\nTv 60,32,384\nMvaa 144,232,200\nTa 146,16,34816\nTv 62,32,14720\nMvaa 148,236,200\ns_lshl1_add_u32 s3, s12, s2\ns_mul_i32 s3, s3, 119808\n"
"Maaa 16,240,200\nTa 148,17,32768\ns_add_u32 s3, s3, 0x5700000\nMaaa 20,244,200\nD0 4,20,45,2048\ns_lshr_b32 s0, s4, 1\nMaaa 80,248,200\nTa 150,17,34816\ns_mul_i32 s0, s0, 28\n"
"Maaa 84,252,200\ns_lshl1_add_u32 s0, s5, s0\ns_and_b32 s6, s4, 1\nMaaa 92,252,204\nTa 152,18,32768\ns_add_u32 s0, s0, s6\nMaaa 88,248,204\ns_lshr_b32 s1, s4, 2\n"
"s_mul_i32 s1, s1, 56\nMaaa 28,244,204\nTa 154,18,34816\ns_lshl2_add_u32 s1, s5, s1\nMaaa 24,240,204\ns_and_b32 s6, s4, 3\ns_add_u32 s1, s1, s6\nMvaa 156,236,204\nTa 156,19,32768\n"
"s_and_b32 s4, s12, 1\nMvaa 152,232,204\nD0 5,20,45,3072\ns_mov_b32 s7, 0x6543210\nMvaa 92,228,204\nTa 158,19,34816\ns_cmp_eq_u32 s4, s2\ns_cselect_b32 s6, s0, s1\n"
"s_cselect_b32 s5, 4, 8\ns_cselect_b32 s7, 0x6352410, s7\nMvaa 88,224,204\nWL 14\ns_lshr_b32 s0, s6, 4\nMvaa 96,224,208\nRa 160,10,32768\ns_lshl_b32 s0, s0, 2\nMvaa 100,228,208\n"
"s_lshr_b32 s0, s7, s0\ns_and_b32 s0, s0, 7\nMvaa 160,232,208\nRa 164,10,34816\ns_lshr_b32 s1, s0, 1\nMvaa 164,236,208\ns_add_u32 m0, s48, 4096\ns_add_u32 s46, s45, 0x10000\n"
"Maaa 32,240,208\nRa 168,10,40960\ns_lshl4_add_u32 s0, s0, s1\nMaaa 36,244,208\nD0 2,20,46,0\ns_and_b32 s1, s6, 15\nMaaa 96,248,208\nRa 172,10,43008\ns_add_u32 s0, s0, s1\n"
"Maaa 100,252,208\ns_lshl_b32 s0, s0, 10\ns_add_u32 s0, s0, s3\nMaaa 108,252,212\nRa 176,10,49152\nSv 36,1,16,0,0\nMaaa 104,248,212\ns_add_u32 s6, s6, s5\nMaaa 44,244,212\n"
"Ra 180,10,51200\nMaaa 40,240,212\nMvaa 172,236,212\nRa 184,10,57344\nMvaa 168,232,212\nD0 3,20,46,1024\nMvaa 108,228,212\nRa 188,10,59392\nMvaa 104,224,212\nMvaa 112,224,216\n"
"Mvaa 116,228,216\ns_lshr_b32 s0, s6, 4\ns_lshl_b32 s0, s0, 2\nMvaa 176,232,216\ns_lshr_b32 s0, s7, s0\ns_and_b32 s0, s0, 7\nMvaa 180,236,216\ns_lshr_b32 s1, s0, 1\n"
"s_lshl4_add_u32 s0, s0, s1\nMaaa 48,240,216\ns_and_b32 s1, s6, 15\ns_add_u32 s0, s0, s1\nMaaa 52,244,216\nD0 4,20,46,2048\ns_lshl_b32 s0, s0, 10\nMaaa 112,248,216\n"
"s_add_u32 s0, s0, s3\nSv 40,1,16,0,0\nMaaa 116,252,216\ns_add_u32 s6, s6, s5\nMaaa 124,252,220\nMaaa 120,248,220\nMaaa 60,244,220\nMaaa 56,240,220\nMvaa 188,236,220\n"
"Mvaa 184,232,220\nD0 5,20,46,3072\nMvaa 124,228,220\nMvaa 120,224,220\ns_waitcnt vmcnt(17) lgkmcnt(0)\ns_barrier\nMvaa 64,160,128\nTa 192,12,49152\ns_lshr_b32 s0, s6, 4\n"
"Mvaa 68,164,128\ns_lshl_b32 s0, s0, 2\ns_lshr_b32 s0, s7, s0\nMvaa 128,168,128\nTa 194,12,51200\ns_and_b32 s0, s0, 7\nMvaa 132,172,128\ns_add_u32 m0, s47, 16384\n"
"s_add_u32 s44, s44, 0x40000\nMaaa 0,176,128\nTa 196,13,49152\ns_lshr_b32 s1, s0, 1\nMaaa 4,180,128\nD0 6,24,44,0\ns_lshl4_add_u32 s0, s0, s1\nMaaa 64,184,128\nTa 198,13,51200\n"
"s_and_b32 s1, s6, 15\nMaaa 68,188,128\ns_add_u32 s0, s0, s1\ns_lshl_b32 s0, s0, 10\nMaaa 76,188,132\nTa 200,14,49152\ns_add_u32 s0, s0, s3\nMaaa 72,184,132\nSv 44,1,16,0,0\n"
"s_add_u32 s6, s6, s5\nMaaa 12,180,132\nTa 202,14,51200\nMaaa 8,176,132\nMvaa 140,172,132\nTa 204,15,49152\nMvaa 136,168,132\nD0 7,24,44,1024\nMvaa 76,164,132\nTa 206,15,51200\n"
"Mvaa 72,160,132\nMvaa 80,160,136\nTa 208,16,49152\nMvaa 84,164,136\ns_lshr_b32 s0, s6, 4\ns_lshl_b32 s0, s0, 2\nMvaa 144,168,136\nTa 210,16,51200\ns_lshr_b32 s0, s7, s0\n"
"Mvaa 148,172,136\ns_and_b32 s0, s0, 7\ns_lshr_b32 s1, s0, 1\nMaaa 16,176,136\nTa 212,17,49152\ns_lshl4_add_u32 s0, s0, s1\nMaaa 20,180,136\nD0 8,24,44,2048\ns_and_b32 s1, s6, 15\n"
"Maaa 80,184,136\nTa 214,17,51200\ns_add_u32 s0, s0, s1\nMaaa 84,188,136\ns_lshl_b32 s0, s0, 10\ns_add_u32 s0, s0, s3\nMaaa 92,188,140\nTa 216,18,49152\nSv 48,1,16,0,0\n"
"Maaa 88,184,140\ns_add_u32 s6, s6, s5\nMaaa 28,180,140\nTa 218,18,51200\nMaaa 24,176,140\nMvaa 156,172,140\nTa 220,19,49152\nMvaa 152,168,140\nD0 9,24,44,3072\nMvaa 92,164,140\n"
"Ta 222,19,51200\nMvaa 88,160,140\nMvaa 96,160,144\nRa 224,11,32768\nMvaa 100,164,144\ns_lshr_b32 s0, s6, 4\ns_lshl_b32 s0, s0, 2\nMvaa 160,168,144\nRa 228,11,34816\n"
"s_lshr_b32 s0, s7, s0\nMvaa 164,172,144\ns_add_u32 m0, s47, 32768\ns_add_u32 s44, s44, 0x40000\nMaaa 32,176,144\nRa 232,11,40960\ns_and_b32 s0, s0, 7\nMaaa 36,180,144\n"
"D0 6,24,44,0\ns_lshr_b32 s1, s0, 1\nMaaa 96,184,144\nRa 236,11,43008\ns_lshl4_add_u32 s0, s0, s1\nMaaa 100,188,144\ns_and_b32 s1, s6, 15\ns_add_u32 s0, s0, s1\nMaaa 108,188,148\n"
"Ra 240,11,49152\ns_lshl_b32 s0, s0, 10\nMaaa 104,184,148\ns_add_u32 s0, s0, s3\nSv 52,1,16,0,0\nMaaa 44,180,148\nRa 244,11,51200\ns_add_u32 s6, s6, s5\nMaaa 40,176,148\n"
"Sv 20,1,16,49,0\ns_add_u32 s49, s49, 1024\nMvaa 172,172,148\nRa 248,11,57344\nMvaa 168,168,148\nD0 7,24,44,1024\nMvaa 108,164,148\nRa 252,11,59392\nMvaa 104,160,148\n"
"Mvaa 112,160,152\nMvaa 116,164,152\nSv 20,1,16,49,0\ns_add_u32 s49, s49, 1024\nMvaa 176,168,152\ns_lshr_b32 s0, s6, 4\ns_lshl_b32 s0, s0, 2\nMvaa 180,172,152\n"
"s_lshr_b32 s0, s7, s0\ns_and_b32 s0, s0, 7\nMaaa 48,176,152\ns_lshr_b32 s1, s0, 1\ns_lshl4_add_u32 s0, s0, s1\nMaaa 52,180,152\nD0 8,24,44,2048\ns_and_b32 s1, s6, 15\n"
"Maaa 112,184,152\ns_add_u32 s0, s0, s1\ns_lshl_b32 s0, s0, 10\nMaaa 116,188,152\ns_add_u32 s0, s0, s3\nSv 56,1,16,0,0\nMaaa 124,188,156\ns_add_u32 s6, s6, s5\nMaaa 120,184,156\n"
"Sv 20,1,16,49,0\ns_add_u32 s49, s49, 1024\nMaaa 60,180,156\nMaaa 56,176,156\nMvaa 188,172,156\nMvaa 184,168,156\nD0 9,24,44,3072\nMvaa 124,164,156\nMvaa 120,160,156\n"
"s_waitcnt vmcnt(15) lgkmcnt(0)\ns_barrier\nMvaa 64,224,192\nTa 128,12,0\ns_lshr_b32 s0, s6, 4\nMvaa 68,228,192\ns_lshl_b32 s0, s0, 2\ns_lshr_b32 s0, s7, s0\nMvaa 128,232,192\n"
"Ta 130,12,2048\ns_and_b32 s0, s0, 7\nMvaa 132,236,192\ns_add_u32 m0, s48, 32768\ns_add_u32 s45, s45, 128\nMaaa 0,240,192\nTa 132,13,0\ns_lshr_b32 s1, s0, 1\nMaaa 4,244,192\n"
"D0 2,20,45,0\ns_lshl4_add_u32 s0, s0, s1\nMaaa 64,248,192\nTa 134,13,2048\ns_and_b32 s1, s6, 15\nMaaa 68,252,192\ns_add_u32 s0, s0, s1\ns_lshl_b32 s0, s0, 10\nMaaa 76,252,196\n"
"Ta 136,14,0\ns_add_u32 s0, s0, s3\nMaaa 72,248,196\nSv 60,1,16,0,0\ns_add_u32 s6, s6, s5\nMaaa 12,244,196\nTa 138,14,2048\nMaaa 8,240,196\nMvaa 140,236,196\nTa 140,15,0\n"
"Mvaa 136,232,196\nD0 3,20,45,1024\nMvaa 76,228,196\nTa 142,15,2048\nMvaa 72,224,196\nMvaa 80,224,200\nTa 144,16,0\nMvaa 84,228,200\nMvaa 144,232,200\nTa 146,16,2048\n"
"Mvaa 148,236,200\nMaaa 16,240,200\nTa 148,17,0\nMaaa 20,244,200\nD0 4,20,45,2048\nMaaa 80,248,200\nTa 150,17,2048\nMaaa 84,252,200\nMaaa 92,252,204\nTa 152,18,0\nMaaa 88,248,204\n"
"Maaa 28,244,204\nTa 154,18,2048\nMaaa 24,240,204\nMvaa 156,236,204\nTa 156,19,0\nMvaa 152,232,204\nD0 5,20,45,3072\nMvaa 92,228,204\nTa 158,19,2048\nMvaa 88,224,204\n"
"Mvaa 96,224,208\nRa 160,10,0\nMvaa 100,228,208\nMvaa 160,232,208\nRa 164,10,2048\nMvaa 164,236,208\ns_add_u32 m0, s48, 36864\ns_add_u32 s46, s45, 0x10000\nMaaa 32,240,208\n"
"Ra 168,10,8192\nMaaa 36,244,208\nD0 2,20,46,0\nMaaa 96,248,208\nRa 172,10,10240\nMaaa 100,252,208\nMaaa 108,252,212\nRa 176,10,16384\nMaaa 104,248,212\nMaaa 44,244,212\n"
"Ra 180,10,18432\nMaaa 40,240,212\nMvaa 172,236,212\nRa 184,10,24576\nMvaa 168,232,212\nD0 3,20,46,1024\nMvaa 108,228,212\nRa 188,10,26624\nMvaa 104,224,212\nMvaa 112,224,216\n"
"Mvaa 116,228,216\nMvaa 176,232,216\nMvaa 180,236,216\nMaaa 48,240,216\nMaaa 52,244,216\nD0 4,20,46,2048\nMaaa 112,248,216\nMaaa 116,252,216\nMaaa 124,252,220\nMaaa 120,248,220\n"
"Maaa 60,244,220\nMaaa 56,240,220\nMvaa 188,236,220\nMvaa 184,232,220\nD0 5,20,46,3072\nMvaa 124,228,220\nMvaa 120,224,220\ns_waitcnt vmcnt(18) lgkmcnt(0)\ns_barrier\n"
"s_mov_b32 s50, 2\n.GV35:\nMvaa 64,160,128\nTa 192,12,16384\nMvaa 68,164,128\nMvaa 128,168,128\nTa 194,12,18432\nMvaa 132,172,128\ns_add_u32 m0, s47, 49152\n"
"s_add_u32 s44, s44, 0x40000\nMaaa 0,176,128\nTa 196,13,16384\nMaaa 4,180,128\nD0 6,24,44,0\nMaaa 64,184,128\nTa 198,13,18432\nMaaa 68,188,128\nMaaa 76,188,132\nTa 200,14,16384\n"
"Maaa 72,184,132\nMaaa 12,180,132\nTa 202,14,18432\nMaaa 8,176,132\nMvaa 140,172,132\nTa 204,15,16384\nMvaa 136,168,132\nD0 7,24,44,1024\nMvaa 76,164,132\nTa 206,15,18432\n"
"Mvaa 72,160,132\nMvaa 80,160,136\nTa 208,16,16384\nMvaa 84,164,136\nMvaa 144,168,136\nTa 210,16,18432\nMvaa 148,172,136\nMaaa 16,176,136\nTa 212,17,16384\nMaaa 20,180,136\n"
"D0 8,24,44,2048\nMaaa 80,184,136\nTa 214,17,18432\nMaaa 84,188,136\nMaaa 92,188,140\nTa 216,18,16384\nMaaa 88,184,140\nMaaa 28,180,140\nTa 218,18,18432\nMaaa 24,176,140\n"
"Mvaa 156,172,140\nTa 220,19,16384\nMvaa 152,168,140\nD0 9,24,44,3072\nMvaa 92,164,140\nTa 222,19,18432\nMvaa 88,160,140\nMvaa 96,160,144\nRa 224,11,0\nMvaa 100,164,144\n"
"Mvaa 160,168,144\nRa 228,11,2048\nMvaa 164,172,144\ns_add_u32 m0, s47, 0\ns_add_u32 s44, s44, 0x40000\nMaaa 32,176,144\nRa 232,11,8192\nMaaa 36,180,144\nD0 6,24,44,0\n"
"Maaa 96,184,144\nRa 236,11,10240\nMaaa 100,188,144\nMaaa 108,188,148\nRa 240,11,16384\nMaaa 104,184,148\nMaaa 44,180,148\nRa 244,11,18432\nMaaa 40,176,148\nSv 20,1,16,49,0\n"
"s_add_u32 s49, s49, 1024\nMvaa 172,172,148\nRa 248,11,24576\nMvaa 168,168,148\nD0 7,24,44,1024\nMvaa 108,164,148\nRa 252,11,26624\nMvaa 104,160,148\nMvaa 112,160,152\n"
"Mvaa 116,164,152\nSv 20,1,16,49,0\ns_add_u32 s49, s49, 1024\nMvaa 176,168,152\nMvaa 180,172,152\nMaaa 48,176,152\nMaaa 52,180,152\nD0 8,24,44,2048\nMaaa 112,184,152\n"
"Maaa 116,188,152\nMaaa 124,188,156\nMaaa 120,184,156\nSv 20,1,16,49,0\ns_add_u32 s49, s49, 1024\nMaaa 60,180,156\nMaaa 56,176,156\nMvaa 188,172,156\nMvaa 184,168,156\n"
"D0 9,24,44,3072\nMvaa 124,164,156\nMvaa 120,160,156\ns_waitcnt vmcnt(11) lgkmcnt(0)\ns_barrier\nMvaa 64,224,192\nTa 128,12,32768\nMvaa 68,228,192\nMvaa 128,232,192\n"
"Ta 130,12,34816\nMvaa 132,236,192\ns_add_u32 m0, s48, 0\ns_add_u32 s45, s45, 128\nMaaa 0,240,192\nTa 132,13,32768\nMaaa 4,244,192\nD0 2,20,45,0\nMaaa 64,248,192\nTa 134,13,34816\n"
"Maaa 68,252,192\nMaaa 76,252,196\nTa 136,14,32768\nMaaa 72,248,196\nMaaa 12,244,196\nTa 138,14,34816\nMaaa 8,240,196\nMvaa 140,236,196\nTa 140,15,32768\nMvaa 136,232,196\n"
"D0 3,20,45,1024\nMvaa 76,228,196\nTa 142,15,34816\nMvaa 72,224,196\nMvaa 80,224,200\nTa 144,16,32768\nMvaa 84,228,200\nMvaa 144,232,200\nTa 146,16,34816\nMvaa 148,236,200\n"
"Maaa 16,240,200\nTa 148,17,32768\nMaaa 20,244,200\nD0 4,20,45,2048\nMaaa 80,248,200\nTa 150,17,34816\nMaaa 84,252,200\nMaaa 92,252,204\nTa 152,18,32768\nMaaa 88,248,204\n"
"Maaa 28,244,204\nTa 154,18,34816\nMaaa 24,240,204\nMvaa 156,236,204\nTa 156,19,32768\nMvaa 152,232,204\nD0 5,20,45,3072\nMvaa 92,228,204\nTa 158,19,34816\nMvaa 88,224,204\n"
"Mvaa 96,224,208\nRa 160,10,32768\nMvaa 100,228,208\nMvaa 160,232,208\nRa 164,10,34816\nMvaa 164,236,208\ns_add_u32 m0, s48, 4096\ns_add_u32 s46, s45, 0x10000\nMaaa 32,240,208\n"
"Ra 168,10,40960\nMaaa 36,244,208\nD0 2,20,46,0\nMaaa 96,248,208\nRa 172,10,43008\nMaaa 100,252,208\nMaaa 108,252,212\nRa 176,10,49152\nMaaa 104,248,212\nMaaa 44,244,212\n"
"Ra 180,10,51200\nMaaa 40,240,212\nMvaa 172,236,212\nRa 184,10,57344\nMvaa 168,232,212\nD0 3,20,46,1024\nMvaa 108,228,212\nRa 188,10,59392\nMvaa 104,224,212\nMvaa 112,224,216\n"
"Mvaa 116,228,216\nMvaa 176,232,216\nMvaa 180,236,216\nMaaa 48,240,216\nMaaa 52,244,216\nD0 4,20,46,2048\nMaaa 112,248,216\nMaaa 116,252,216\nMaaa 124,252,220\nMaaa 120,248,220\n"
"Maaa 60,244,220\nMaaa 56,240,220\nMvaa 188,236,220\nMvaa 184,232,220\nD0 5,20,46,3072\nMvaa 124,228,220\nMvaa 120,224,220\ns_waitcnt vmcnt(15) lgkmcnt(0)\ns_barrier\n"
"Mvaa 64,160,128\nTa 192,12,49152\nMvaa 68,164,128\nMvaa 128,168,128\nTa 194,12,51200\nMvaa 132,172,128\ns_add_u32 m0, s47, 16384\ns_add_u32 s44, s44, 0x40000\nMaaa 0,176,128\n"
"Ta 196,13,49152\nMaaa 4,180,128\nD0 6,24,44,0\nMaaa 64,184,128\nTa 198,13,51200\nMaaa 68,188,128\nMaaa 76,188,132\nTa 200,14,49152\nMaaa 72,184,132\nMaaa 12,180,132\n"
"Ta 202,14,51200\nMaaa 8,176,132\nMvaa 140,172,132\nTa 204,15,49152\nMvaa 136,168,132\nD0 7,24,44,1024\nMvaa 76,164,132\nTa 206,15,51200\nMvaa 72,160,132\nMvaa 80,160,136\n"
"Ta 208,16,49152\nMvaa 84,164,136\nMvaa 144,168,136\nTa 210,16,51200\nMvaa 148,172,136\nMaaa 16,176,136\nTa 212,17,49152\nMaaa 20,180,136\nD0 8,24,44,2048\nMaaa 80,184,136\n"
"Ta 214,17,51200\nMaaa 84,188,136\nMaaa 92,188,140\nTa 216,18,49152\nMaaa 88,184,140\nMaaa 28,180,140\nTa 218,18,51200\nMaaa 24,176,140\nMvaa 156,172,140\nTa 220,19,49152\n"
"Mvaa 152,168,140\nD0 9,24,44,3072\nMvaa 92,164,140\nTa 222,19,51200\nMvaa 88,160,140\nMvaa 96,160,144\nRa 224,11,32768\nMvaa 100,164,144\nMvaa 160,168,144\nRa 228,11,34816\n"
"Mvaa 164,172,144\ns_add_u32 m0, s47, 32768\ns_add_u32 s44, s44, 0x40000\nMaaa 32,176,144\nRa 232,11,40960\nMaaa 36,180,144\nD0 6,24,44,0\nMaaa 96,184,144\nRa 236,11,43008\n"
"Maaa 100,188,144\nMaaa 108,188,148\nRa 240,11,49152\nMaaa 104,184,148\nMaaa 44,180,148\nRa 244,11,51200\nMaaa 40,176,148\nSv 20,1,16,49,0\ns_add_u32 s49, s49, 1024\n"
"Mvaa 172,172,148\nRa 248,11,57344\nMvaa 168,168,148\nD0 7,24,44,1024\nMvaa 108,164,148\nRa 252,11,59392\nMvaa 104,160,148\nMvaa 112,160,152\nMvaa 116,164,152\nSv 20,1,16,49,0\n"
"s_add_u32 s49, s49, 1024\nMvaa 176,168,152\nMvaa 180,172,152\nMaaa 48,176,152\nMaaa 52,180,152\nD0 8,24,44,2048\nMaaa 112,184,152\nMaaa 116,188,152\nMaaa 124,188,156\n"
"Maaa 120,184,156\nSv 20,1,16,49,0\ns_add_u32 s49, s49, 1024\nMaaa 60,180,156\nMaaa 56,176,156\nMvaa 188,172,156\nMvaa 184,168,156\nD0 9,24,44,3072\nMvaa 124,164,156\n"
"Mvaa 120,160,156\ns_waitcnt vmcnt(11) lgkmcnt(0)\ns_barrier\nMvaa 64,224,192\nTa 128,12,0\nMvaa 68,228,192\nMvaa 128,232,192\nTa 130,12,2048\nMvaa 132,236,192\n"
"s_add_u32 m0, s48, 32768\ns_add_u32 s45, s45, 128\nMaaa 0,240,192\nTa 132,13,0\nMaaa 4,244,192\nD0 2,20,45,0\nMaaa 64,248,192\nTa 134,13,2048\nMaaa 68,252,192\nMaaa 76,252,196\n"
"Ta 136,14,0\nMaaa 72,248,196\nMaaa 12,244,196\nTa 138,14,2048\nMaaa 8,240,196\nMvaa 140,236,196\nTa 140,15,0\nMvaa 136,232,196\nD0 3,20,45,1024\nMvaa 76,228,196\nTa 142,15,2048\n"
"Mvaa 72,224,196\nMvaa 80,224,200\nTa 144,16,0\nMvaa 84,228,200\nMvaa 144,232,200\nTa 146,16,2048\nMvaa 148,236,200\nMaaa 16,240,200\nTa 148,17,0\nMaaa 20,244,200\nD0 4,20,45,2048\n"
"Maaa 80,248,200\nTa 150,17,2048\nMaaa 84,252,200\nMaaa 92,252,204\nTa 152,18,0\nMaaa 88,248,204\nMaaa 28,244,204\nTa 154,18,2048\nMaaa 24,240,204\nMvaa 156,236,204\nTa 156,19,0\n"
"Mvaa 152,232,204\nD0 5,20,45,3072\nMvaa 92,228,204\nTa 158,19,2048\nMvaa 88,224,204\nMvaa 96,224,208\nRa 160,10,0\nMvaa 100,228,208\nMvaa 160,232,208\nRa 164,10,2048\n"
"Mvaa 164,236,208\ns_add_u32 m0, s48, 36864\ns_add_u32 s46, s45, 0x10000\nMaaa 32,240,208\nRa 168,10,8192\nMaaa 36,244,208\nD0 2,20,46,0\nMaaa 96,248,208\nRa 172,10,10240\n"
"Maaa 100,252,208\nMaaa 108,252,212\nRa 176,10,16384\nMaaa 104,248,212\nMaaa 44,244,212\nRa 180,10,18432\nMaaa 40,240,212\nMvaa 172,236,212\nRa 184,10,24576\nMvaa 168,232,212\n"
"D0 3,20,46,1024\nMvaa 108,228,212\nRa 188,10,26624\nMvaa 104,224,212\nMvaa 112,224,216\nMvaa 116,228,216\nMvaa 176,232,216\nMvaa 180,236,216\nMaaa 48,240,216\nMaaa 52,244,216\n"
"D0 4,20,46,2048\nMaaa 112,248,216\nMaaa 116,252,216\nMaaa 124,252,220\nMaaa 120,248,220\nMaaa 60,244,220\nMaaa 56,240,220\nMvaa 188,236,220\nMvaa 184,232,220\nD0 5,20,46,3072\n"
"Mvaa 124,228,220\nMvaa 120,224,220\ns_waitcnt vmcnt(15) lgkmcnt(0)\ns_barrier\ns_sub_u32 s50, s50, 1\ns_cbranch_scc0 .GV35\nMvaa 64,160,128\nTa 192,12,16384\nMvaa 68,164,128\n"
"Mvaa 128,168,128\nTa 194,12,18432\nMvaa 132,172,128\ns_add_u32 m0, s47, 49152\ns_add_u32 s44, s44, 0x40000\nMaaa 0,176,128\nTa 196,13,16384\nMaaa 4,180,128\nD0 6,24,44,0\n"
"Maaa 64,184,128\nTa 198,13,18432\nMaaa 68,188,128\nMaaa 76,188,132\nTa 200,14,16384\nMaaa 72,184,132\nMaaa 12,180,132\nTa 202,14,18432\nMaaa 8,176,132\nMvaa 140,172,132\n"
"Ta 204,15,16384\nMvaa 136,168,132\nD0 7,24,44,1024\nMvaa 76,164,132\nTa 206,15,18432\nMvaa 72,160,132\nMvaa 80,160,136\nTa 208,16,16384\nMvaa 84,164,136\nMvaa 144,168,136\n"
"Ta 210,16,18432\nMvaa 148,172,136\nMaaa 16,176,136\nTa 212,17,16384\nMaaa 20,180,136\nD0 8,24,44,2048\nMaaa 80,184,136\nTa 214,17,18432\nMaaa 84,188,136\nMaaa 92,188,140\n"
"Ta 216,18,16384\nMaaa 88,184,140\nMaaa 28,180,140\nTa 218,18,18432\nMaaa 24,176,140\nMvaa 156,172,140\nTa 220,19,16384\nMvaa 152,168,140\nD0 9,24,44,3072\nMvaa 92,164,140\n"
"Ta 222,19,18432\nMvaa 88,160,140\nMvaa 96,160,144\nRa 224,11,0\nMvaa 100,164,144\nMvaa 160,168,144\nRa 228,11,2048\nMvaa 164,172,144\ns_add_u32 m0, s47, 0\n"
"s_add_u32 s44, s44, 0x40000\nMaaa 32,176,144\nRa 232,11,8192\nMaaa 36,180,144\nD0 6,24,44,0\nMaaa 96,184,144\nRa 236,11,10240\nMaaa 100,188,144\nMaaa 108,188,148\nRa 240,11,16384\n"
"Maaa 104,184,148\nMaaa 44,180,148\nRa 244,11,18432\nMaaa 40,176,148\nMvaa 172,172,148\nRa 248,11,24576\nMvaa 168,168,148\nD0 7,24,44,1024\nMvaa 108,164,148\nRa 252,11,26624\n"
"Mvaa 104,160,148\nMvaa 112,160,152\nMvaa 116,164,152\nMvaa 176,168,152\nMvaa 180,172,152\nMaaa 48,176,152\nMaaa 52,180,152\nD0 8,24,44,2048\nMaaa 112,184,152\nMaaa 116,188,152\n"
"Maaa 124,188,156\nMaaa 120,184,156\nMaaa 60,180,156\nMaaa 56,176,156\nMvaa 188,172,156\nMvaa 184,168,156\nD0 9,24,44,3072\nMvaa 124,164,156\nMvaa 120,160,156\n"
"s_waitcnt vmcnt(8) lgkmcnt(0)\ns_barrier\nMvaa 64,224,192\nTa 128,12,32768\nMvaa 68,228,192\nMvaa 128,232,192\nTa 130,12,34816\nMvaa 132,236,192\ns_add_u32 m0, s48, 0\n"
"s_add_u32 s45, s45, 128\nMaaa 0,240,192\nTa 132,13,32768\nMaaa 4,244,192\nD0 2,20,45,0\nMaaa 64,248,192\nTa 134,13,34816\nMaaa 68,252,192\nMaaa 76,252,196\nTa 136,14,32768\n"
"Maaa 72,248,196\nMaaa 12,244,196\nTa 138,14,34816\nMaaa 8,240,196\nMvaa 140,236,196\nTa 140,15,32768\nMvaa 136,232,196\nD0 3,20,45,1024\nMvaa 76,228,196\nTa 142,15,34816\n"
"Mvaa 72,224,196\nMvaa 80,224,200\nTa 144,16,32768\nMvaa 84,228,200\nMvaa 144,232,200\nTa 146,16,34816\nMvaa 148,236,200\nMaaa 16,240,200\nTa 148,17,32768\nMaaa 20,244,200\n"
"D0 4,20,45,2048\nMaaa 80,248,200\nTa 150,17,34816\nMaaa 84,252,200\nMaaa 92,252,204\nTa 152,18,32768\nMaaa 88,248,204\nMaaa 28,244,204\nTa 154,18,34816\nMaaa 24,240,204\n"
"Mvaa 156,236,204\nTa 156,19,32768\nMvaa 152,232,204\nD0 5,20,45,3072\nMvaa 92,228,204\nTa 158,19,34816\nMvaa 88,224,204\nMvaa 96,224,208\nRa 160,10,32768\nMvaa 100,228,208\n"
"Mvaa 160,232,208\nRa 164,10,34816\nMvaa 164,236,208\ns_add_u32 m0, s48, 4096\ns_add_u32 s46, s45, 0x10000\nMaaa 32,240,208\nRa 168,10,40960\nMaaa 36,244,208\nD0 2,20,46,0\n"
"Maaa 96,248,208\nRa 172,10,43008\nMaaa 100,252,208\nMaaa 108,252,212\nRa 176,10,49152\nMaaa 104,248,212\nMaaa 44,244,212\nRa 180,10,51200\nMaaa 40,240,212\nMvaa 172,236,212\n"
"Ra 184,10,57344\nMvaa 168,232,212\nD0 3,20,46,1024\nMvaa 108,228,212\nRa 188,10,59392\nMvaa 104,224,212\nMvaa 112,224,216\nMvaa 116,228,216\nMvaa 176,232,216\nMvaa 180,236,216\n"
"Maaa 48,240,216\nMaaa 52,244,216\nD0 4,20,46,2048\nMaaa 112,248,216\nMaaa 116,252,216\nMaaa 124,252,220\nMaaa 120,248,220\nMaaa 60,244,220\nMaaa 56,240,220\nMvaa 188,236,220\n"
"Mvaa 184,232,220\nD0 5,20,46,3072\nMvaa 124,228,220\nMvaa 120,224,220\ns_waitcnt vmcnt(12) lgkmcnt(0)\ns_barrier\nMvaa 64,160,128\nTa 192,12,49152\nMvaa 68,164,128\n"
"Mvaa 128,168,128\nTa 194,12,51200\nMvaa 132,172,128\ns_add_u32 m0, s47, 16384\ns_add_u32 s44, s44, 0x40000\nMaaa 0,176,128\nTa 196,13,49152\nMaaa 4,180,128\nD0 6,24,44,0\n"
"Maaa 64,184,128\nTa 198,13,51200\nMaaa 68,188,128\nMaaa 76,188,132\nTa 200,14,49152\nMaaa 72,184,132\nMaaa 12,180,132\nTa 202,14,51200\nMaaa 8,176,132\nMvaa 140,172,132\n"
"Ta 204,15,49152\nMvaa 136,168,132\nD0 7,24,44,1024\nMvaa 76,164,132\nTa 206,15,51200\nMvaa 72,160,132\nMvaa 80,160,136\nTa 208,16,49152\nMvaa 84,164,136\nMvaa 144,168,136\n"
"Ta 210,16,51200\nMvaa 148,172,136\nMaaa 16,176,136\nTa 212,17,49152\nMaaa 20,180,136\nD0 8,24,44,2048\nMaaa 80,184,136\nTa 214,17,51200\nMaaa 84,188,136\nMaaa 92,188,140\n"
"Ta 216,18,49152\nMaaa 88,184,140\nMaaa 28,180,140\nTa 218,18,51200\nMaaa 24,176,140\nMvaa 156,172,140\nTa 220,19,49152\nMvaa 152,168,140\nD0 9,24,44,3072\nMvaa 92,164,140\n"
"Ta 222,19,51200\nMvaa 88,160,140\nMvaa 96,160,144\nRa 224,11,32768\nMvaa 100,164,144\nMvaa 160,168,144\nRa 228,11,34816\nMvaa 164,172,144\ns_add_u32 m0, s47, 32768\n"
"s_add_u32 s44, s44, 0x40000\nMaaa 32,176,144\nRa 232,11,40960\nMaaa 36,180,144\nD0 6,24,44,0\nMaaa 96,184,144\nRa 236,11,43008\nMaaa 100,188,144\nMaaa 108,188,148\n"
"Ra 240,11,49152\nMaaa 104,184,148\nMaaa 44,180,148\nRa 244,11,51200\nMaaa 40,176,148\nMvaa 172,172,148\nRa 248,11,57344\nMvaa 168,168,148\nD0 7,24,44,1024\nMvaa 108,164,148\n"
"Ra 252,11,59392\nMvaa 104,160,148\nMvaa 112,160,152\nMvaa 116,164,152\nMvaa 176,168,152\nMvaa 180,172,152\nMaaa 48,176,152\nMaaa 52,180,152\nD0 8,24,44,2048\nMaaa 112,184,152\n"
"Maaa 116,188,152\nMaaa 124,188,156\nMaaa 120,184,156\nMaaa 60,180,156\nMaaa 56,176,156\nMvaa 188,172,156\nMvaa 184,168,156\nD0 9,24,44,3072\nMvaa 124,164,156\nMvaa 120,160,156\n"
"s_waitcnt vmcnt(8) lgkmcnt(0)\ns_barrier\nMvaa 64,224,192\nTa 128,12,0\nMvaa 68,228,192\nMvaa 128,232,192\nTa 130,12,2048\nMvaa 132,236,192\ns_add_u32 m0, s48, 32768\n"
"s_add_u32 s45, s45, 128\nMaaa 0,240,192\nTa 132,13,0\nMaaa 4,244,192\nD0 2,20,45,0\nMaaa 64,248,192\nTa 134,13,2048\nMaaa 68,252,192\nMaaa 76,252,196\nTa 136,14,0\n"
"Maaa 72,248,196\nMaaa 12,244,196\nTa 138,14,2048\nMaaa 8,240,196\nMvaa 140,236,196\nTa 140,15,0\nMvaa 136,232,196\nD0 3,20,45,1024\nMvaa 76,228,196\nTa 142,15,2048\n"
"Mvaa 72,224,196\nMvaa 80,224,200\nTa 144,16,0\nMvaa 84,228,200\nMvaa 144,232,200\nTa 146,16,2048\nMvaa 148,236,200\nMaaa 16,240,200\nTa 148,17,0\nMaaa 20,244,200\nD0 4,20,45,2048\n"
"Maaa 80,248,200\nTa 150,17,2048\nMaaa 84,252,200\nMaaa 92,252,204\nTa 152,18,0\nMaaa 88,248,204\nMaaa 28,244,204\nTa 154,18,2048\nMaaa 24,240,204\nMvaa 156,236,204\nTa 156,19,0\n"
"Mvaa 152,232,204\nD0 5,20,45,3072\nMvaa 92,228,204\nTa 158,19,2048\nMvaa 88,224,204\nMvaa 96,224,208\nRa 160,10,0\nMvaa 100,228,208\nMvaa 160,232,208\nRa 164,10,2048\n"
"Mvaa 164,236,208\ns_add_u32 m0, s48, 36864\ns_add_u32 s46, s45, 0x10000\nMaaa 32,240,208\nRa 168,10,8192\nMaaa 36,244,208\nD0 2,20,46,0\nMaaa 96,248,208\nRa 172,10,10240\n"
"Maaa 100,252,208\nMaaa 108,252,212\nRa 176,10,16384\nMaaa 104,248,212\nMaaa 44,244,212\nRa 180,10,18432\nMaaa 40,240,212\nMvaa 172,236,212\nRa 184,10,24576\nMvaa 168,232,212\n"
"D0 3,20,46,1024\nMvaa 108,228,212\nRa 188,10,26624\nMvaa 104,224,212\nMvaa 112,224,216\nMvaa 116,228,216\nMvaa 176,232,216\nMvaa 180,236,216\nMaaa 48,240,216\nMaaa 52,244,216\n"
"D0 4,20,46,2048\nMaaa 112,248,216\nMaaa 116,252,216\nMaaa 124,252,220\nMaaa 120,248,220\nMaaa 60,244,220\nMaaa 56,240,220\nMvaa 188,236,220\nMvaa 184,232,220\nD0 5,20,46,3072\n"
"Mvaa 124,228,220\nMvaa 120,224,220\ns_waitcnt vmcnt(12) lgkmcnt(0)\ns_barrier\ns_cmp_eq_u32 s14, 0\ns_cbranch_scc1 .T0V5\nMvaa 64,160,128\nTa 192,12,16384\n"
"v_lshrrev_b32 v48, 4, v0\nMvaa 68,164,128\nv_and_b32 v49, 7, v0\nv_xor_b32 v49, v49, v48\ns_add_u32 m0, s47, 49152\ns_add_u32 s44, s44, 0x40000\nMvaa 128,168,128\nTa 194,12,18432\n"
"v_lshlrev_b32 v49, 4, v49\nMvaa 132,172,128\nv_lshrrev_b32 v48, 3, v0\nv_mul_u32_u24 v48, 896, v48\nD0 6,24,44,0\nMaaa 0,176,128\nTa 196,13,16384\nv_add_u32 v40, v48, v49\n"
"Maaa 4,180,128\nv_xor_b32 v49, 64, v40\nv_add_u32 v41, 6144, v49\nMaaa 64,184,128\nTa 198,13,18432\nv_add_u32 v42, 12288, v40\nMaaa 68,188,128\nv_add_u32 v43, 18432, v49\n"
"s_mov_b32 s2, s13\nMaaa 76,188,132\nTa 200,14,16384\ns_lshl2_add_u32 s2, s2, s11\nD0 7,24,44,1024\nMaaa 72,184,132\ns_mul_i32 s52, s2, 28672\ns_lshr_b32 s2, s13, 2\n"
"Maaa 12,180,132\nTa 202,14,18432\ns_bfe_u32 s3, s13, 0x10001\nMaaa 8,176,132\ns_mul_i32 s3, s3, 17\ns_add_u32 s2, s2, s3\nMvaa 140,172,132\nTa 204,15,16384\ns_xor_b32 s3, s14, 1\n"
"Mvaa 136,168,132\ns_lshl3_add_u32 s2, s3, s2\ns_lshl1_add_u32 s2, 0, s2\nD0 8,24,44,2048\nMvaa 76,164,132\nTa 206,15,18432\nMvaa 72,160,132\ns_cmp_eq_u32 s2, 32\n"
"s_cselect_b32 s4, 16, s2\nMvaa 80,160,136\nTa 208,16,16384\ns_mul_i32 s75, s4, 33792\nMvaa 84,164,136\ns_lshr_b32 s2, s13, 2\ns_bfe_u32 s3, s13, 0x10001\nMvaa 144,168,136\n"
"Ta 210,16,18432\ns_mul_i32 s3, s3, 17\nD0 9,24,44,3072\nMvaa 148,172,136\ns_add_u32 s2, s2, s3\ns_xor_b32 s3, s14, 1\nMaaa 16,176,136\nTa 212,17,16384\ns_lshl3_add_u32 s2, s3, s2\n"
"Maaa 20,180,136\ns_lshl1_add_u32 s2, 1, s2\ns_add_u32 m0, s47, 65536\ns_add_u32 s46, s51, 0\nMaaa 80,184,136\nTa 214,17,18432\nMaaa 84,188,136\ns_cmp_eq_u32 s2, 32\n"
"s_cselect_b32 s4, 16, s2\nD0 6,32,46,0\nMaaa 92,188,140\nTa 216,18,16384\ns_mul_i32 s76, s4, 33792\nMaaa 88,184,140\ns_lshr_b32 s2, s13, 2\ns_bfe_u32 s3, s13, 0x10001\n"
"Maaa 28,180,140\nTa 218,18,18432\ns_mul_i32 s3, s3, 17\nMaaa 24,176,140\ns_add_u32 s2, s2, s3\ns_xor_b32 s3, s14, 1\nMvaa 156,172,140\nTa 220,19,16384\ns_lshl3_add_u32 s2, s3, s2\n"
"D0 7,32,46,1024\nMvaa 152,168,140\ns_lshl1_add_u32 s2, 2, s2\nMvaa 92,164,140\nTa 222,19,18432\nMvaa 88,160,140\ns_cmp_eq_u32 s2, 32\ns_cselect_b32 s4, 16, s2\nMvaa 96,160,144\n"
"Ra 224,11,0\ns_mul_i32 s77, s4, 33792\nMvaa 100,164,144\ns_lshr_b32 s2, s13, 2\ns_bfe_u32 s3, s13, 0x10001\nD0 8,32,46,2048\nMvaa 160,168,144\nRa 228,11,2048\n"
"s_mul_i32 s3, s3, 17\nMvaa 164,172,144\ns_add_u32 s2, s2, s3\ns_xor_b32 s3, s14, 1\nMaaa 32,176,144\nRa 232,11,8192\ns_lshl3_add_u32 s2, s3, s2\nMaaa 36,180,144\n"
"s_lshl1_add_u32 s2, 3, s2\nMaaa 96,184,144\nRa 236,11,10240\nD0 9,32,46,3072\nMaaa 100,188,144\ns_cmp_eq_u32 s2, 32\ns_cselect_b32 s4, 16, s2\nMaaa 108,188,148\nRa 240,11,16384\n"
"s_mul_i32 s78, s4, 33792\nMaaa 104,184,148\ns_mul_i32 s2, s12, 1083392\ns_lshr_b32 s3, s11, 1\ns_add_u32 m0, s47, 81920\ns_add_u32 s46, s51, 262144\nMaaa 44,180,148\n"
"Ra 244,11,18432\ns_lshl_b32 s3, s3, 14\nMaaa 40,176,148\ns_add_u32 s2, s2, s3\ns_and_b32 s3, s11, 1\nD0 6,32,46,0\nMvaa 172,172,148\nRa 248,11,24576\ns_lshl_b32 s3, s3, 13\n"
"Mvaa 168,168,148\ns_add_u32 s2, s2, s3\ns_add_u32 s2, s2, 0x1102000\nMvaa 108,164,148\nRa 252,11,26624\ns_mov_b32 s55, 4096\nMvaa 104,160,148\ns_add_u32 s75, s75, s2\n"
"s_add_u32 s76, s76, s2\nMvaa 112,160,152\ns_add_u32 s77, s77, s2\ns_add_u32 s78, s78, s2\nD0 7,32,46,1024\nMvaa 116,164,152\nv_add_u32 v32, 0x10000, v12\n"
"v_add_u32 v33, 0x10000, v13\nMvaa 176,168,152\nv_add_u32 v34, 0x10000, v14\nv_add_u32 v35, 0x10000, v15\nMvaa 180,172,152\nv_add_u32 v36, 0x10000, v16\n"
"v_add_u32 v37, 0x10000, v17\nMaaa 48,176,152\nv_add_u32 v38, 0x10000, v18\nv_add_u32 v39, 0x10000, v19\nMaaa 52,180,152\ns_add_u32 s2, s11, 1\ns_lshr_b32 s2, s2, 1\n"
"D0 8,32,46,2048\nMaaa 112,184,152\ns_lshl_b32 s2, s2, 12\nv_add_u32 v44, s2, v10\nMaaa 116,188,152\nv_add_u32 v45, s2, v11\nv_xor_b32 v46, 0x1000, v44\nMaaa 124,188,156\n"
"v_xor_b32 v47, 0x1000, v45\ns_mul_i32 s2, s12, 1083392\nMaaa 120,184,156\ns_lshl_b32 s3, s11, 12\ns_add_u32 s2, s2, s3\nMaaa 60,180,156\ns_add_u32 s89, s3, 0x10000\n"
"s_mov_b32 s88, 0xffffbc00\nD0 9,32,46,3072\nMaaa 56,176,156\ns_lshl_b32 s4, s13, 1\ns_sub_u32 s79, 15, s4\nMvaa 188,172,156\ns_and_b32 s5, s12, 1\ns_lshl1_add_u32 s5, s12, s5\n"
"Mvaa 184,168,156\ns_mul_i32 s5, s5, 119808\ns_add_u32 s5, s5, s3\nMvaa 124,164,156\ns_add_u32 s96, s5, 0x5684400\ns_mov_b32 s63, 0x53800000\nMvaa 120,160,156\n"
"s_waitcnt vmcnt(12) lgkmcnt(0)\ns_barrier\nMvaa 64,224,192\nTa 128,12,32768\ns_mov_b32 s64, 0x2b800000\nMvaa 68,228,192\ns_mov_b32 s98, s4\ns_lshl3_add_u32 s5, s12, s13\n"
"s_add_u32 m0, s47, -65536\ns_add_u32 s46, s52, 0\nMvaa 128,232,192\nTa 130,12,34816\ns_lshl2_add_u32 s5, s5, s11\nMvaa 132,236,192\ns_lshl_b32 s65, s5, 9\n"
"s_add_u32 s65, s65, 0x5c00000\nD0 40,28,46,0\nMaaa 0,240,192\nTa 132,13,32768\ns_mul_i32 s66, s5, 17408\nMaaa 4,244,192\ns_add_u32 s66, s66, 0x2c00000\ns_lshr_b32 s58, s12, 2\n"
"Maaa 64,248,192\nTa 134,13,34816\ns_lshl_b32 s58, s58, 10\nMaaa 68,252,192\ns_add_u32 s58, s58, 0x5c48400\ns_lshl1_add_u32 s5, s12, 1\nMaaa 76,252,196\nTa 136,14,32768\n"
"s_lshl3_add_u32 s5, s5, s13\nD0 41,28,46,1024\nMaaa 72,248,196\ns_lshl4_add_u32 s54, s5, 0x5c48400\nMaaa 12,244,196\nTa 138,14,34816\nMaaa 8,240,196\ns_cmp_eq_u32 s11, 0\n"
"s_cselect_b32 s54, s54, 0x40000000\nMvaa 140,236,196\nTa 140,15,32768\ns_getpc_b64 s[8:9]\n.PC36:\ns_add_u32 s8, s8, .HD11-.PC36\ns_addc_u32 s9, s9, 0\nMvaa 136,232,196\n"
"s_mov_b32 s97, 0xff800000\ns_mov_b32 s90, 1\nD0 42,28,46,2048\nMvaa 76,228,196\nTa 142,15,34816\ns_lshl_b32 s5, s11, 2\nMvaa 72,224,196\ns_lshr_b32 s92, 0xf112, s5\n"
"s_and_b32 s92, s92, 15\nMvaa 80,224,200\nTa 144,16,32768\ns_lshr_b32 s93, s11, 1\nMvaa 84,228,200\ns_xor_b32 s93, s93, 1\ns_mul_i32 s4, s79, 33792\nMvaa 144,232,200\n"
"Ta 146,16,34816\ns_sub_u32 s4, s2, s4\nD0 43,28,46,3072\nMvaa 148,236,200\ns_add_u32 s94, s4, 0x10800\ns_add_u32 s95, s4, 0x110a400\nMaaa 16,240,200\nTa 148,17,32768\n"
"s_add_u32 s86, s2, 0x9cc00\nMaaa 20,244,200\ns_add_u32 s87, s2, 0x1196800\ns_sub_u32 s99, s79, 1\ns_add_u32 m0, s47, 0\ns_add_u32 s46, s51, 524288\nMaaa 80,248,200\n"
"Ta 150,17,34816\ns_mov_b32 s91, 100\nMaaa 84,252,200\ns_cmp_eq_u32 s11, 3\ns_cselect_b32 s92, 100, s92\nD0 6,32,46,0\nMaaa 92,252,204\nTa 152,18,32768\nMaaa 88,248,204\n"
"Maaa 28,244,204\nTa 154,18,34816\nMaaa 24,240,204\nMvaa 156,236,204\nTa 156,19,32768\nD0 7,32,46,1024\nMvaa 152,232,204\nMvaa 92,228,204\nTa 158,19,34816\nMvaa 88,224,204\n"
"Mvaa 96,224,208\nRa 160,10,32768\nMvaa 100,228,208\nD0 8,32,46,2048\nMvaa 160,232,208\nRa 164,10,34816\nMvaa 164,236,208\nMaaa 32,240,208\nRa 168,10,40960\nMaaa 36,244,208\n"
"Maaa 96,248,208\nRa 172,10,43008\nD0 9,32,46,3072\nMaaa 100,252,208\nMaaa 108,252,212\nRa 176,10,49152\nMaaa 104,248,212\ns_add_u32 m0, s47, 16384\ns_add_u32 s46, s51, 786432\n"
"Maaa 44,244,212\nRa 180,10,51200\nMaaa 40,240,212\nD0 6,32,46,0\nMvaa 172,236,212\nRa 184,10,57344\nMvaa 168,232,212\nMvaa 108,228,212\nRa 188,10,59392\nMvaa 104,224,212\n"
"Mvaa 112,224,216\nD0 7,32,46,1024\nMvaa 116,228,216\nMvaa 176,232,216\nMvaa 180,236,216\nMaaa 48,240,216\nMaaa 52,244,216\nD0 8,32,46,2048\nMaaa 112,248,216\nMaaa 116,252,216\n"
"Maaa 124,252,220\nMaaa 120,248,220\nMaaa 60,244,220\nD0 9,32,46,3072\nMaaa 56,240,220\nMvaa 188,236,220\nMvaa 184,232,220\nMvaa 124,228,220\nMvaa 120,224,220\n"
"s_waitcnt vmcnt(20) lgkmcnt(0)\ns_barrier\nMvaa 64,160,128\nTa 192,12,49152\nMvaa 68,164,128\ns_add_u32 m0, s47, -49152\ns_add_u32 s46, s52, 256\nMvaa 128,168,128\n"
"Ta 194,12,51200\nMvaa 132,172,128\nD0 40,28,46,0\nMaaa 0,176,128\nTa 196,13,49152\nMaaa 4,180,128\nMaaa 64,184,128\nTa 198,13,51200\nMaaa 68,188,128\nMaaa 76,188,132\n"
"Ta 200,14,49152\nD0 41,28,46,1024\nMaaa 72,184,132\nMaaa 12,180,132\nTa 202,14,51200\nMaaa 8,176,132\nMvaa 140,172,132\nTa 204,15,49152\nMvaa 136,168,132\nD0 42,28,46,2048\n"
"Mvaa 76,164,132\nTa 206,15,51200\nMvaa 72,160,132\nMvaa 80,160,136\nTa 208,16,49152\nMvaa 84,164,136\nMvaa 144,168,136\nTa 210,16,51200\nD0 43,28,46,3072\nMvaa 148,172,136\n"
"Maaa 16,176,136\nTa 212,17,49152\nMaaa 20,180,136\ns_add_u32 m0, s47, 32768\ns_add_u32 s46, s51, 1048576\nMaaa 80,184,136\nTa 214,17,51200\nMaaa 84,188,136\nD0 6,32,46,0\n"
"Maaa 92,188,140\nTa 216,18,49152\nMaaa 88,184,140\nMaaa 28,180,140\nTa 218,18,51200\nMaaa 24,176,140\nMvaa 156,172,140\nTa 220,19,49152\nD0 7,32,46,1024\nMvaa 152,168,140\n"
"Mvaa 92,164,140\nTa 222,19,51200\nMvaa 88,160,140\nMvaa 96,160,144\nRa 224,11,32768\nMvaa 100,164,144\nD0 8,32,46,2048\nMvaa 160,168,144\nRa 228,11,34816\nMvaa 164,172,144\n"
"Maaa 32,176,144\nRa 232,11,40960\nMaaa 36,180,144\nMaaa 96,184,144\nRa 236,11,43008\nD0 9,32,46,3072\nMaaa 100,188,144\nMaaa 108,188,148\nRa 240,11,49152\nMaaa 104,184,148\n"
"Maaa 44,180,148\nRa 244,11,51200\nMaaa 40,176,148\nMvaa 172,172,148\nRa 248,11,57344\nMvaa 168,168,148\nMvaa 108,164,148\nRa 252,11,59392\nMvaa 104,160,148\nMvaa 112,160,152\n"
"Mvaa 116,164,152\nMvaa 176,168,152\nMvaa 180,172,152\nMaaa 48,176,152\nMaaa 52,180,152\nMaaa 112,184,152\nMaaa 116,188,152\nMaaa 124,188,156\nMaaa 120,184,156\nMaaa 60,180,156\n"
"Maaa 56,176,156\nMvaa 188,172,156\nMvaa 184,168,156\nMvaa 124,164,156\nMvaa 120,160,156\ns_waitcnt vmcnt(16) lgkmcnt(0)\ns_barrier\nMvaa 64,224,192\ns_add_u32 m0, s47, -16384\n"
"s_add_u32 s46, s52, 128\nMvaa 68,228,192\nMvaa 72,224,196\nD0 40,28,46,0\nMvaa 76,228,196\nMvaa 80,224,200\nMvaa 84,228,200\nMvaa 88,224,204\nD0 41,28,46,1024\nCV 192,64,65\n"
"Mvaa 92,228,204\nCV 193,66,67\nCV 194,68,69\nMvaa 96,224,208\nCV 195,70,71\nCV 196,72,73\nMvaa 100,228,208\nCV 197,74,75\nCV 198,76,77\nMvaa 104,224,212\nD0 42,28,46,2048\n"
"CV 199,78,79\nMvaa 108,228,212\nCV 200,80,81\nCV 201,82,83\nMvaa 112,224,216\nCV 202,84,85\nCV 203,86,87\nMvaa 116,228,216\nCV 204,88,89\nCV 205,90,91\nMvaa 120,224,220\n"
"D0 43,28,46,3072\nCV 206,92,93\nMvaa 124,228,220\nCV 207,94,95\nCV 208,96,97\nMvaa 128,232,192\ns_add_u32 m0, s47, 49152\ns_add_u32 s46, s51, 1310720\nMvaa 132,236,192\n"
"CV 209,98,99\nCV 210,100,101\nMvaa 136,232,196\nD0 6,32,46,0\nCV 211,102,103\nMvaa 140,236,196\nCV 212,104,105\nCV 213,106,107\nMvaa 144,232,200\nCV 214,108,109\nCV 215,110,111\n"
"Mvaa 148,236,200\nCV 216,112,113\nCV 217,114,115\nMvaa 152,232,204\nD0 7,32,46,1024\nCV 218,116,117\nMvaa 156,236,204\nCV 219,118,119\nCV 220,120,121\nMvaa 160,232,208\n"
"CV 221,122,123\nCV 222,124,125\nMvaa 164,236,208\nCV 223,126,127\nCV 224,128,129\nMvaa 168,232,212\nD0 8,32,46,2048\nCV 225,130,131\nMvaa 172,236,212\nCV 226,132,133\n"
"CV 227,134,135\nMvaa 176,232,216\nCV 228,136,137\nCV 229,138,139\nMvaa 180,236,216\nCV 230,140,141\nCV 231,142,143\nMvaa 184,232,220\nD0 9,32,46,3072\nCV 232,144,145\n"
"Mvaa 188,236,220\nCV 233,146,147\nCV 234,148,149\nMaaa 0,240,192\ns_add_u32 m0, s47, -32768\ns_add_u32 s46, s52, 384\nMaaa 4,244,192\nCV 235,150,151\nCV 236,152,153\n"
"Maaa 8,240,196\nD0 40,28,46,0\nCV 237,154,155\nMaaa 12,244,196\nCV 238,156,157\nCV 239,158,159\nMaaa 16,240,200\nRa 160,44,0\nCV 240,160,161\nMaaa 20,244,200\nRa 164,44,2048\n"
"CV 241,162,163\nMaaa 24,240,204\nD0 41,28,46,1024\nRa 168,46,0\nMaaa 28,244,204\nRa 172,46,2048\nCV 242,164,165\nMaaa 32,240,208\nTa 128,32,0\nCV 243,166,167\nMaaa 36,244,208\n"
"Ta 130,32,2048\nCV 244,168,169\nMaaa 40,240,212\nD0 42,28,46,2048\nTa 132,33,0\nMaaa 44,244,212\nTa 134,33,2048\nCV 245,170,171\nMaaa 48,240,216\nTa 136,34,0\nCV 246,172,173\n"
"Maaa 52,244,216\nTa 138,34,2048\nCV 247,174,175\nMaaa 56,240,220\nD0 43,28,46,3072\nTa 140,35,0\nMaaa 60,244,220\nTa 142,35,2048\nCV 248,176,177\nMaaa 64,248,192\nTa 144,36,0\n"
"CV 249,178,179\nMaaa 68,252,192\nTa 146,36,2048\nCV 250,180,181\nMaaa 72,248,196\nTa 148,37,0\nCV 251,182,183\nSv 192,1,16,75,0\nMaaa 76,252,196\nTa 150,37,2048\nCV 252,184,185\n"
"Maaa 80,248,200\nTa 152,38,0\nCV 253,186,187\nMaaa 84,252,200\nTa 154,38,2048\nCV 254,188,189\nMaaa 88,248,204\nTa 156,39,0\nCV 255,190,191\nMaaa 92,252,204\nTa 158,39,2048\n"
"Maaa 96,248,208\nSv 196,1,16,75,1024\nMaaa 100,252,208\nMaaa 104,248,212\nMaaa 108,252,212\nMaaa 112,248,216\nMaaa 116,252,216\nMaaa 120,248,220\nSv 200,1,16,75,2048\n"
"Maaa 124,252,220\nWL 0\ns_branch .Q16\n.T0V5:\nMvaa 64,160,128\nTa 192,12,16384\ns_mul_i32 s2, s12, 1083392\nMvaa 68,164,128\ns_lshl_b32 s3, s11, 12\ns_add_u32 s2, s2, s3\n"
"s_add_u32 m0, s47, 49152\ns_add_u32 s44, s44, 0x40000\nMvaa 128,168,128\nTa 194,12,18432\ns_add_u32 s89, s3, 0x0\nMvaa 132,172,128\ns_mov_b32 s88, 0xffffbc00\n"
"s_lshl_b32 s4, s13, 1\nD0 6,24,44,0\nMaaa 0,176,128\nTa 196,13,16384\ns_sub_u32 s79, 15, s4\nMaaa 4,180,128\ns_and_b32 s5, s12, 1\ns_lshl1_add_u32 s5, s12, s5\nMaaa 64,184,128\n"
"Ta 198,13,18432\ns_mul_i32 s5, s5, 119808\nMaaa 68,188,128\ns_add_u32 s5, s5, s3\ns_add_u32 s96, s5, 0x5684400\nD0 7,24,44,1024\nMaaa 76,188,132\nTa 200,14,16384\n"
"s_mov_b32 s63, 0x53800000\nMaaa 72,184,132\ns_mov_b32 s64, 0x2b800000\ns_mov_b32 s98, s4\nMaaa 12,180,132\nTa 202,14,18432\ns_lshl3_add_u32 s5, s12, s13\nMaaa 8,176,132\n"
"s_lshl2_add_u32 s5, s5, s11\ns_lshl_b32 s65, s5, 9\nD0 8,24,44,2048\nMvaa 140,172,132\nTa 204,15,16384\ns_add_u32 s65, s65, 0x5c00000\nMvaa 136,168,132\ns_mul_i32 s66, s5, 17408\n"
"s_add_u32 s66, s66, 0x2c00000\nMvaa 76,164,132\nTa 206,15,18432\ns_lshr_b32 s58, s12, 2\nMvaa 72,160,132\ns_lshl_b32 s58, s58, 10\ns_add_u32 s58, s58, 0x5c48400\nD0 9,24,44,3072\n"
"Mvaa 80,160,136\nTa 208,16,16384\ns_lshl1_add_u32 s5, s12, 0\nMvaa 84,164,136\ns_lshl3_add_u32 s5, s5, s13\ns_lshl4_add_u32 s54, s5, 0x5c48400\ns_add_u32 s6, s56, 0\n"
"Mvaa 144,168,136\nTa 210,16,18432\nMvaa 148,172,136\ns_cmp_eq_u32 s11, 0\ns_cselect_b32 s54, s54, 0x40000000\nbuffer_load_dwordx4 v[192:195], v1, s[16:19], s6 offen offset:0 sc1\n"
"Maaa 16,176,136\nTa 212,17,16384\ns_getpc_b64 s[8:9]\n.PC37:\ns_add_u32 s8, s8, .HD10-.PC37\ns_addc_u32 s9, s9, 0\nMaaa 20,180,136\ns_mov_b32 s97, 0xff800000\ns_mov_b32 s90, 3\n"
"Maaa 80,184,136\nTa 214,17,18432\ns_lshl_b32 s5, s11, 2\nMaaa 84,188,136\ns_lshr_b32 s92, 0xf112, s5\ns_and_b32 s92, s92, 15\n"
"buffer_load_dwordx4 v[196:199], v1, s[16:19], s6 offen offset:1024 sc1\nMaaa 92,188,140\nTa 216,18,16384\ns_lshr_b32 s93, s11, 1\nMaaa 88,184,140\ns_xor_b32 s93, s93, 1\n"
"s_add_u32 s86, s2, 0x10800\nMaaa 28,180,140\nTa 218,18,18432\ns_add_u32 s87, s2, 0x110a400\nMaaa 24,176,140\ns_mov_b32 s94, s86\ns_mov_b32 s95, s87\n"
"buffer_load_dwordx4 v[200:203], v1, s[16:19], s6 offen offset:2048 sc1\nMvaa 156,172,140\nTa 220,19,16384\nMvaa 152,168,140\ns_cmp_eq_u32 s13, 7\ns_cselect_b32 s5, 0xfffffffd, 64\n"
"Mvaa 92,164,140\nTa 222,19,18432\ns_add_u32 s92, s92, s5\nMvaa 88,160,140\ns_add_u32 s93, s93, s5\ns_or_b32 s92, s92, 0x100\n"
"buffer_load_dwordx4 v[204:207], v1, s[16:19], s6 offen offset:3072 sc1\nMvaa 96,160,144\nRa 224,11,0\ns_mov_b32 s91, 100\nMvaa 100,164,144\n"
"buffer_load_dword v27, v1, s[16:19], s58 offen offset:0 sc0 sc1\ns_lshr_b32 s2, s13, 2\ns_add_u32 s6, s56, 4096\nMvaa 160,168,144\nRa 228,11,2048\ns_bfe_u32 s3, s13, 0x10001\n"
"Mvaa 164,172,144\ns_mul_i32 s3, s3, 17\ns_add_u32 s2, s2, s3\nbuffer_load_dwordx4 v[208:211], v1, s[16:19], s6 offen offset:0 sc1\nMaaa 32,176,144\nRa 232,11,8192\n"
"s_xor_b32 s3, s14, 1\nMaaa 36,180,144\ns_lshl3_add_u32 s2, s3, s2\ns_lshl1_add_u32 s2, 0, s2\nMaaa 96,184,144\nRa 236,11,10240\nMaaa 100,188,144\ns_cmp_eq_u32 s2, 32\n"
"s_cselect_b32 s4, 16, s2\nbuffer_load_dwordx4 v[212:215], v1, s[16:19], s6 offen offset:1024 sc1\nMaaa 108,188,148\nRa 240,11,16384\ns_mul_i32 s75, s4, 33792\nMaaa 104,184,148\n"
"s_lshr_b32 s2, s13, 2\ns_bfe_u32 s3, s13, 0x10001\nMaaa 44,180,148\nRa 244,11,18432\ns_mul_i32 s3, s3, 17\nMaaa 40,176,148\ns_add_u32 s2, s2, s3\ns_xor_b32 s3, s14, 1\n"
"buffer_load_dwordx4 v[216:219], v1, s[16:19], s6 offen offset:2048 sc1\nMvaa 172,172,148\nRa 248,11,24576\ns_lshl3_add_u32 s2, s3, s2\nMvaa 168,168,148\ns_lshl1_add_u32 s2, 1, s2\n"
"Mvaa 108,164,148\nRa 252,11,26624\nMvaa 104,160,148\ns_cmp_eq_u32 s2, 32\ns_cselect_b32 s4, 16, s2\nbuffer_load_dwordx4 v[220:223], v1, s[16:19], s6 offen offset:3072 sc1\n"
"Mvaa 112,160,152\ns_mul_i32 s76, s4, 33792\ns_lshr_b32 s2, s13, 2\nMvaa 116,164,152\ns_bfe_u32 s3, s13, 0x10001\ns_mul_i32 s3, s3, 17\ns_add_u32 s6, s56, 8192\nMvaa 176,168,152\n"
"s_add_u32 s2, s2, s3\ns_xor_b32 s3, s14, 1\nMvaa 180,172,152\ns_lshl3_add_u32 s2, s3, s2\ns_lshl1_add_u32 s2, 2, s2\n"
"buffer_load_dwordx4 v[224:227], v1, s[16:19], s6 offen offset:0 sc1\nMaaa 48,176,152\ns_cmp_eq_u32 s2, 32\ns_cselect_b32 s4, 16, s2\nMaaa 52,180,152\ns_mul_i32 s77, s4, 33792\n"
"s_lshr_b32 s2, s13, 2\nMaaa 112,184,152\ns_bfe_u32 s3, s13, 0x10001\ns_mul_i32 s3, s3, 17\nMaaa 116,188,152\ns_add_u32 s2, s2, s3\ns_xor_b32 s3, s14, 1\n"
"buffer_load_dwordx4 v[228:231], v1, s[16:19], s6 offen offset:1024 sc1\nMaaa 124,188,156\ns_lshl3_add_u32 s2, s3, s2\ns_lshl1_add_u32 s2, 3, s2\nMaaa 120,184,156\n"
"s_cmp_eq_u32 s2, 32\ns_cselect_b32 s4, 16, s2\nMaaa 60,180,156\ns_mul_i32 s78, s4, 33792\ns_mul_i32 s2, s12, 1083392\nMaaa 56,176,156\ns_lshr_b32 s3, s11, 1\n"
"s_lshl_b32 s3, s3, 14\nbuffer_load_dwordx4 v[232:235], v1, s[16:19], s6 offen offset:2048 sc1\nMvaa 188,172,156\ns_add_u32 s2, s2, s3\ns_and_b32 s3, s11, 1\nMvaa 184,168,156\n"
"s_lshl_b32 s3, s3, 13\ns_add_u32 s2, s2, s3\nMvaa 124,164,156\ns_add_u32 s2, s2, 0x1102000\ns_mov_b32 s55, 4096\nMvaa 120,160,156\ns_waitcnt vmcnt(16) lgkmcnt(0)\ns_barrier\n"
"Mvaa 64,224,192\nTa 128,12,32768\ns_add_u32 s75, s75, s2\nbuffer_load_dwordx4 v[236:239], v1, s[16:19], s6 offen offset:3072 sc1\nMvaa 68,228,192\ns_add_u32 s76, s76, s2\n"
"s_add_u32 s77, s77, s2\nMvaa 128,232,192\nTa 130,12,34816\ns_add_u32 s78, s78, s2\ns_add_u32 s6, s56, 12288\nMvaa 132,236,192\nMaaa 0,240,192\nTa 132,13,32768\n"
"buffer_load_dwordx4 v[240:243], v1, s[16:19], s6 offen offset:0 sc1\nMaaa 4,244,192\nMaaa 64,248,192\nTa 134,13,34816\nMaaa 68,252,192\nMaaa 76,252,196\nTa 136,14,32768\n"
"buffer_load_dwordx4 v[244:247], v1, s[16:19], s6 offen offset:1024 sc1\nMaaa 72,248,196\nMaaa 12,244,196\nTa 138,14,34816\nMaaa 8,240,196\nMvaa 140,236,196\nTa 140,15,32768\n"
"buffer_load_dwordx4 v[248:251], v1, s[16:19], s6 offen offset:2048 sc1\nMvaa 136,232,196\nMvaa 76,228,196\nTa 142,15,34816\nMvaa 72,224,196\nMvaa 80,224,200\nTa 144,16,32768\n"
"buffer_load_dwordx4 v[252:255], v1, s[16:19], s6 offen offset:3072 sc1\nMvaa 84,228,200\nMvaa 144,232,200\nTa 146,16,34816\nMvaa 148,236,200\nMaaa 16,240,200\nTa 148,17,32768\n"
"Maaa 20,244,200\nMaaa 80,248,200\nTa 150,17,34816\nMaaa 84,252,200\nMaaa 92,252,204\nTa 152,18,32768\nMaaa 88,248,204\nMaaa 28,244,204\nTa 154,18,34816\nMaaa 24,240,204\n"
"Mvaa 156,236,204\nTa 156,19,32768\nMvaa 152,232,204\nMvaa 92,228,204\nTa 158,19,34816\nMvaa 88,224,204\nMvaa 96,224,208\nRa 160,10,32768\nMvaa 100,228,208\nMvaa 160,232,208\n"
"Ra 164,10,34816\nMvaa 164,236,208\nMaaa 32,240,208\nRa 168,10,40960\nMaaa 36,244,208\nMaaa 96,248,208\nRa 172,10,43008\nMaaa 100,252,208\nMaaa 108,252,212\nRa 176,10,49152\nWV 12\n"
"v_cmp_ne_u32 vcc, s15, v27\ns_and_b32 s2, s12, 3\ns_lshl_b32 s2, s2, 4\ns_nop 1\ns_lshr_b64 vcc, vcc, s2\ns_and_b64 vcc, vcc, 0xff00\ns_cbranch_vccnz .S38\n.S39:\n"
"s_sub_u32 s45, s86, 67584\nMaaa 104,248,212\ns_add_u32 m0, s47, 0\ns_add_u32 s46, s45, 0x0\nMaaa 44,244,212\nRa 180,10,51200\nMaaa 40,240,212\nD1 1,16,46,0\nMvaa 172,236,212\n"
"Ra 184,10,57344\nMvaa 168,232,212\nMvaa 108,228,212\nRa 188,10,59392\nMvaa 104,224,212\nD1 1,16,46,1024\nMvaa 112,224,216\nMvaa 116,228,216\nMvaa 176,232,216\nMvaa 180,236,216\n"
"D1 1,16,46,2048\nMaaa 48,240,216\nMaaa 52,244,216\nMaaa 112,248,216\nMaaa 116,252,216\nD1 1,16,46,3072\nMaaa 124,252,220\nMaaa 120,248,220\nMaaa 60,244,220\nMaaa 56,240,220\n"
"Mvaa 188,236,220\nMvaa 184,232,220\nMvaa 124,228,220\nMvaa 120,224,220\nWL 0\ns_barrier\nMvaa 64,160,128\nTa 192,12,49152\nMvaa 68,164,128\ns_add_u32 m0, s47, 32768\n"
"s_add_u32 s46, s45, 0x4000\nMvaa 128,168,128\nTa 194,12,51200\nMvaa 132,172,128\nD1 1,16,46,0\nMaaa 0,176,128\nTa 196,13,49152\nMaaa 4,180,128\nMaaa 64,184,128\nTa 198,13,51200\n"
"Maaa 68,188,128\nD1 1,16,46,1024\nMaaa 76,188,132\nTa 200,14,49152\nMaaa 72,184,132\nMaaa 12,180,132\nTa 202,14,51200\nMaaa 8,176,132\nD1 1,16,46,2048\nMvaa 140,172,132\n"
"Ta 204,15,49152\nMvaa 136,168,132\nMvaa 76,164,132\nTa 206,15,51200\nMvaa 72,160,132\nD1 1,16,46,3072\nMvaa 80,160,136\nTa 208,16,49152\nMvaa 84,164,136\n"
"s_add_u32 m0, s47, -65536\ns_add_u32 s46, s45, 0x8400\nMvaa 144,168,136\nTa 210,16,51200\nMvaa 148,172,136\nD1 1,16,46,0\nMaaa 16,176,136\nTa 212,17,49152\nMaaa 20,180,136\n"
"Maaa 80,184,136\nTa 214,17,51200\nMaaa 84,188,136\nD1 1,16,46,1024\nMaaa 92,188,140\nTa 216,18,49152\nMaaa 88,184,140\nMaaa 28,180,140\nTa 218,18,51200\nMaaa 24,176,140\n"
"D1 1,16,46,2048\nMvaa 156,172,140\nTa 220,19,49152\nMvaa 152,168,140\nMvaa 92,164,140\nTa 222,19,51200\nMvaa 88,160,140\nD1 1,16,46,3072\nMvaa 96,160,144\nRa 224,11,32768\n"
"Mvaa 100,164,144\ns_add_u32 m0, s47, -49152\ns_add_u32 s46, s45, 0x1106000\nMvaa 160,168,144\nRa 228,11,34816\nMvaa 164,172,144\nD1 1,16,46,0\nMaaa 32,176,144\nRa 232,11,40960\n"
"Maaa 36,180,144\nMaaa 96,184,144\nRa 236,11,43008\nMaaa 100,188,144\nD1 1,16,46,1024\nMaaa 108,188,148\nRa 240,11,49152\nMaaa 104,184,148\nMaaa 44,180,148\nRa 244,11,51200\n"
"Maaa 40,176,148\nD1 1,16,46,2048\nMvaa 172,172,148\nRa 248,11,57344\nMvaa 168,168,148\nMvaa 108,164,148\nRa 252,11,59392\nMvaa 104,160,148\nD1 1,16,46,3072\nMvaa 112,160,152\n"
"Mvaa 116,164,152\nv_add_u32 v2, 0x10000, v1\ns_lshr_b32 s2, s59, 1\nMvaa 176,168,152\nv_add_u32 v24, s2, v2\nv_xor_b32 v26, 0x1000, v24\nMvaa 180,172,152\n"
"v_lshrrev_b32 v20, 4, v0\nv_lshlrev_b32 v20, 2, v20\nMaaa 48,176,152\nv_and_b32 v0, 15, v0\nv_sub_u32 v0, v0, v20\nMaaa 52,180,152\ns_and_b32 s2, s11, 1\ns_lshl_b32 s2, s2, 6\n"
"Maaa 112,184,152\nv_subrev_u32 v3, s2, v0\ns_xor_b32 s2, s2, 64\nMaaa 116,188,152\nv_subrev_u32 v0, s2, v0\nv_mov_b32 v4, 0\nMaaa 124,188,156\nv_mov_b32 v5, 0\nv_mov_b32 v6, 0\n"
"Maaa 120,184,156\nv_mov_b32 v7, 0\nv_mov_b32 v8, 0\nMaaa 60,180,156\nv_mov_b32 v9, 0\nv_mov_b32 v10, 0\nMaaa 56,176,156\nv_mov_b32 v11, 0\nv_mov_b32 v12, 0\nMvaa 188,172,156\n"
"v_mov_b32 v13, 0\nv_mov_b32 v14, 0\nMvaa 184,168,156\nv_mov_b32 v15, 0\nv_mov_b32 v16, 0\nMvaa 124,164,156\nv_mov_b32 v17, 0\nv_mov_b32 v18, 0\nMvaa 120,160,156\n"
"s_waitcnt vmcnt(12) lgkmcnt(0)\ns_barrier\nMvaa 64,224,192\nv_mov_b32 v19, 0\nMvaa 68,228,192\ns_add_u32 m0, s47, 49152\ns_add_u32 s46, s45, 0x1102000\nMvaa 72,224,196\n"
"Mvaa 76,228,196\nD1 1,16,46,0\nMvaa 80,224,200\nMvaa 84,228,200\nMvaa 88,224,204\nCV 32,64,65\nCV 33,66,67\nMvaa 92,228,204\nD1 1,16,46,1024\nCV 34,68,69\nMvaa 96,224,208\n"
"CV 35,70,71\nCV 36,72,73\nMvaa 100,228,208\nCV 37,74,75\nCV 38,76,77\nMvaa 104,224,212\nCV 39,78,79\nCV 40,80,81\nMvaa 108,228,212\nD1 1,16,46,2048\nCV 41,82,83\nMvaa 112,224,216\n"
"CV 42,84,85\nCV 43,86,87\nMvaa 116,228,216\nCV 44,88,89\nCV 45,90,91\nMvaa 120,224,220\nCV 46,92,93\nCV 47,94,95\nMvaa 124,228,220\nD1 1,16,46,3072\nCV 48,96,97\nMvaa 128,232,192\n"
"CV 49,98,99\nCV 50,100,101\nMvaa 132,236,192\nCV 51,102,103\nCV 52,104,105\nMvaa 136,232,196\nCV 53,106,107\nCV 54,108,109\nMvaa 140,236,196\nCV 55,110,111\nCV 56,112,113\n"
"Mvaa 144,232,200\nCV 57,114,115\nCV 58,116,117\nMvaa 148,236,200\nCV 59,118,119\nCV 60,120,121\nMvaa 152,232,204\nCV 61,122,123\nCV 62,124,125\nMvaa 156,236,204\nCV 63,126,127\n"
"CV 96,128,129\nMvaa 160,232,208\nCV 97,130,131\nCV 98,132,133\nMvaa 164,236,208\nCV 99,134,135\nCV 100,136,137\nSv 32,1,16,75,0\nMvaa 168,232,212\nCV 101,138,139\nCV 102,140,141\n"
"Mvaa 172,236,212\nCV 103,142,143\nCV 104,144,145\nMvaa 176,232,216\nCV 105,146,147\nCV 106,148,149\nMvaa 180,236,216\nCV 107,150,151\nCV 108,152,153\nSv 36,1,16,75,1024\n"
"Mvaa 184,232,220\nCV 109,154,155\nCV 110,156,157\nMvaa 188,236,220\nCV 111,158,159\nCV 112,160,161\nMaaa 0,240,192\nCV 113,162,163\nCV 114,164,165\nMaaa 4,244,192\nCV 115,166,167\n"
"CV 116,168,169\nSv 40,1,16,75,2048\nMaaa 8,240,196\nCV 117,170,171\nCV 118,172,173\nMaaa 12,244,196\nCV 119,174,175\nCV 120,176,177\nMaaa 16,240,200\nCV 121,178,179\n"
"CV 122,180,181\nMaaa 20,244,200\nCV 123,182,183\nCV 124,184,185\nSv 44,1,16,75,3072\nMaaa 24,240,204\nCV 125,186,187\nCV 126,188,189\nMaaa 28,244,204\nCV 127,190,191\n"
"v_mov_b32 v86, 0\ns_add_u32 s75, s75, s55\nMaaa 32,240,208\nv_mov_b32 v87, 0\nv_mov_b32 v88, 0\nMaaa 36,244,208\nv_mov_b32 v89, 0\nSv 48,1,16,75,0\nMaaa 40,240,212\nAR 92,0\n"
"AR 93,1\nMaaa 44,244,212\nCV 128,92,93\nAR 92,2\nMaaa 48,240,216\nAR 93,3\nCV 129,92,93\nMaaa 52,244,216\nSv 52,1,16,75,1024\nAR 92,4\nMaaa 56,240,220\nAR 93,5\nCV 130,92,93\n"
"Maaa 60,244,220\nAR 92,6\nAR 93,7\nMaaa 64,248,192\nRa 128,24,0\nCV 131,92,93\nMaaa 68,252,192\nSv 56,1,16,75,2048\nAR 92,8\nMaaa 72,248,196\nRa 132,24,1024\nAR 93,9\n"
"Maaa 76,252,196\nCV 132,92,93\nAR 92,10\nMaaa 80,248,200\nRa 136,24,2048\nAR 93,11\nMaaa 84,252,200\nSv 60,1,16,75,3072\nCV 133,92,93\nMaaa 88,248,204\nRa 140,24,3072\nAR 92,12\n"
"Maaa 92,252,204\nAR 93,13\nCV 134,92,93\nMaaa 96,248,208\nRa 144,26,0\nAR 92,14\nMaaa 100,252,208\nAR 93,15\nCV 135,92,93\nMaaa 104,248,212\nRa 148,26,1024\nMaaa 108,252,212\n"
"Maaa 112,248,216\nRa 152,26,2048\nMaaa 116,252,216\nMaaa 120,248,220\nRa 156,26,3072\nMaaa 124,252,220\nWL 4\ns_branch .F07\n.S38:\ns_mov_b32 s7, 262144\n.S40:\ns_sleep 1\n"
"buffer_load_dword v27, v1, s[16:19], s58 offen offset:0 sc0 sc1\nWV 0\nv_cmp_ne_u32 vcc, s15, v27\ns_and_b32 s2, s12, 3\ns_lshl_b32 s2, s2, 4\ns_nop 1\ns_lshr_b64 vcc, vcc, s2\n"
"s_and_b64 vcc, vcc, 0xff00\ns_cbranch_vccz .S39\ns_sub_u32 s7, s7, 1\ns_cbranch_scc0 .S40\ns_branch .S39\n.Q16:\nZvaa 64,128,160\nRa 224,45,0\nZvaa 96,128,164\nRa 228,45,2048\n"
"Zvaa 128,128,168\nRa 232,47,0\nZvaa 160,128,172\nRa 236,47,2048\nSv 204,1,16,75,3072\ns_and_b32 s5, s72, 511\ns_lshl2_add_u32 s5, s5, 0x5c49400\n"
"buffer_load_dword v27, off, s[16:19], s5 sc0 sc1\nZvaa 164,132,172\nTa 192,32,16384\nZvaa 132,132,168\nZvaa 100,132,164\nTa 194,32,18432\nZvaa 68,132,160\nTa 196,33,16384\n"
"Zvaa 72,136,160\nTa 198,33,18432\nZvaa 104,136,164\nTa 200,34,16384\ns_add_u32 s75, s75, s55\nSv 208,1,16,75,0\nZvaa 136,136,168\nTa 202,34,18432\nZvaa 168,136,172\n"
"Zvaa 172,140,172\nTa 204,35,16384\nZvaa 140,140,168\nTa 206,35,18432\nZvaa 108,140,164\nTa 208,36,16384\nZvaa 76,140,160\nTa 210,36,18432\nSv 212,1,16,75,1024\nZvaa 80,144,160\n"
"Ta 212,37,16384\nZvaa 112,144,164\nZvaa 144,144,168\nTa 214,37,18432\nZvaa 176,144,172\nTa 216,38,16384\nZvaa 180,148,172\nTa 218,38,18432\nZvaa 148,148,168\nTa 220,39,16384\n"
"Sv 216,1,16,75,2048\nZvaa 116,148,164\nTa 222,39,18432\nZvaa 84,148,160\nZvaa 88,152,160\nZvaa 120,152,164\nZvaa 152,152,168\nZvaa 184,152,172\nSv 220,1,16,75,3072\n"
"Zvaa 188,156,172\nZvaa 156,156,168\nZvaa 124,156,164\nZvaa 92,156,160\ns_waitcnt vmcnt(17) lgkmcnt(0)\ns_barrier\nMvaa 64,192,224\nRa 160,44,49152\nMvaa 96,192,228\n"
"Ra 164,44,51200\ns_add_u32 m0, s47, 65536\ns_add_u32 s46, s51, 1572864\nD0 6,32,46,0\nMvaa 128,192,232\nRa 168,46,49152\nMvaa 160,192,236\nRa 172,46,51200\nMvaa 164,196,236\n"
"Ta 128,12,0\nMvaa 132,196,232\nD0 7,32,46,1024\nMvaa 100,196,228\nTa 130,12,2048\nMvaa 68,196,224\nTa 132,13,0\nMvaa 72,200,224\nTa 134,13,2048\nMvaa 104,200,228\nTa 136,14,0\n"
"D0 8,32,46,2048\nMvaa 136,200,232\nTa 138,14,2048\nMvaa 168,200,236\nMvaa 172,204,236\nTa 140,15,0\nMvaa 140,204,232\nTa 142,15,2048\nD0 9,32,46,3072\nMvaa 108,204,228\n"
"Ta 144,16,0\nMvaa 76,204,224\nTa 146,16,2048\ns_add_u32 m0, s47, 81920\ns_add_u32 s46, s51, 1835008\nMvaa 80,208,224\nTa 148,17,0\nMvaa 112,208,228\nD0 6,32,46,0\n"
"Mvaa 144,208,232\nTa 150,17,2048\nMvaa 176,208,236\nTa 152,18,0\nMvaa 180,212,236\nTa 154,18,2048\nMvaa 148,212,232\nTa 156,19,0\nD0 7,32,46,1024\nMvaa 116,212,228\n"
"Ta 158,19,2048\nMvaa 84,212,224\nMvaa 88,216,224\nMvaa 120,216,228\nD0 8,32,46,2048\nMvaa 152,216,232\nMvaa 184,216,236\nMvaa 188,220,236\nMvaa 156,220,232\nD0 9,32,46,3072\n"
"Mvaa 124,220,228\nMvaa 92,220,224\nWL 0\nMvaa 64,128,160\nRa 224,45,49152\nMvaa 96,128,164\nRa 228,45,51200\ns_add_u32 m0, s47, -65536\ns_add_u32 s46, s52, 512\nD0 40,28,46,0\n"
"Mvaa 128,128,168\nRa 232,47,49152\nMvaa 160,128,172\nRa 236,47,51200\nMvaa 164,132,172\nTa 192,12,16384\nMvaa 132,132,168\nD0 41,28,46,1024\nMvaa 100,132,164\nTa 194,12,18432\n"
"Mvaa 68,132,160\nTa 196,13,16384\nMvaa 72,136,160\nTa 198,13,18432\nMvaa 104,136,164\nTa 200,14,16384\nD0 42,28,46,2048\nMvaa 136,136,168\nTa 202,14,18432\nMvaa 168,136,172\n"
"Mvaa 172,140,172\nTa 204,15,16384\nMvaa 140,140,168\nTa 206,15,18432\nD0 43,28,46,3072\nMvaa 108,140,164\nTa 208,16,16384\nSv 224,1,16,76,0\nMvaa 76,140,160\nTa 210,16,18432\n"
"Mvaa 80,144,160\nTa 212,17,16384\nMvaa 112,144,164\nMvaa 144,144,168\nTa 214,17,18432\nMvaa 176,144,172\nTa 216,18,16384\nSv 228,1,16,76,1024\nMvaa 180,148,172\nTa 218,18,18432\n"
"Mvaa 148,148,168\nTa 220,19,16384\nMvaa 116,148,164\nTa 222,19,18432\nMvaa 84,148,160\nMvaa 88,152,160\nSv 232,1,16,76,2048\nMvaa 120,152,164\nMvaa 152,152,168\nMvaa 184,152,172\n"
"Mvaa 188,156,172\nMvaa 156,156,168\nSv 236,1,16,76,3072\nMvaa 124,156,164\nMvaa 92,156,160\ns_waitcnt vmcnt(16) lgkmcnt(0)\ns_barrier\nMvaa 64,192,224\nRa 160,44,16384\n"
"v_cmp_ne_u32 vcc, s15, v27\ns_nop 0\ns_cbranch_vccnz .S51\n.S52:\nMvaa 96,192,228\nRa 164,44,18432\nbuffer_store_dword v25, off, s[16:19], s54 offset:0 sc0 sc1\n"
"s_add_u32 m0, s47, 0\ns_add_u32 s46, s51, 2097152\nD0 6,32,46,0\nMvaa 128,192,232\nRa 168,46,16384\nMvaa 160,192,236\nRa 172,46,18432\nMvaa 164,196,236\nTa 128,12,32768\n"
"Mvaa 132,196,232\nD0 7,32,46,1024\nMvaa 100,196,228\nTa 130,12,34816\nMvaa 68,196,224\nTa 132,13,32768\nMvaa 72,200,224\nTa 134,13,34816\nMvaa 104,200,228\nTa 136,14,32768\n"
"D0 8,32,46,2048\nMvaa 136,200,232\nTa 138,14,34816\nMvaa 168,200,236\nMvaa 172,204,236\nTa 140,15,32768\nMvaa 140,204,232\nTa 142,15,34816\nD0 9,32,46,3072\nMvaa 108,204,228\n"
"Ta 144,16,32768\nMvaa 76,204,224\nTa 146,16,34816\ns_add_u32 m0, s47, 16384\ns_add_u32 s46, s51, 2359296\nMvaa 80,208,224\nTa 148,17,32768\nMvaa 112,208,228\nD0 6,32,46,0\n"
"Mvaa 144,208,232\nTa 150,17,34816\nMvaa 176,208,236\nTa 152,18,32768\nMvaa 180,212,236\nTa 154,18,34816\nMvaa 148,212,232\nTa 156,19,32768\nD0 7,32,46,1024\nMvaa 116,212,228\n"
"Ta 158,19,34816\nMvaa 84,212,224\nMvaa 88,216,224\nMvaa 120,216,228\nD0 8,32,46,2048\nMvaa 152,216,232\nMvaa 184,216,236\nMvaa 188,220,236\nMvaa 156,220,232\nD0 9,32,46,3072\n"
"Mvaa 124,220,228\nMvaa 92,220,224\nWL 0\nMvaa 64,128,160\nRa 224,45,16384\nMvaa 96,128,164\nRa 228,45,18432\ns_add_u32 m0, s47, -16384\ns_add_u32 s46, s52, 640\nD0 40,28,46,0\n"
"Mvaa 128,128,168\nRa 232,47,16384\nMvaa 160,128,172\nRa 236,47,18432\nMvaa 164,132,172\nTa 192,12,49152\nMvaa 132,132,168\nD0 41,28,46,1024\nMvaa 100,132,164\nTa 194,12,51200\n"
"Mvaa 68,132,160\nTa 196,13,49152\nMvaa 72,136,160\nTa 198,13,51200\nMvaa 104,136,164\nTa 200,14,49152\nD0 42,28,46,2048\nMvaa 136,136,168\nTa 202,14,51200\nMvaa 168,136,172\n"
"Mvaa 172,140,172\nTa 204,15,49152\nMvaa 140,140,168\nTa 206,15,51200\nD0 43,28,46,3072\nMvaa 108,140,164\nTa 208,16,49152\ns_add_u32 s76, s76, s55\nSv 240,1,16,76,0\n"
"Mvaa 76,140,160\nTa 210,16,51200\nMvaa 80,144,160\nTa 212,17,49152\nMvaa 112,144,164\nMvaa 144,144,168\nTa 214,17,51200\nMvaa 176,144,172\nTa 216,18,49152\nSv 244,1,16,76,1024\n"
"Mvaa 180,148,172\nTa 218,18,51200\nMvaa 148,148,168\nTa 220,19,49152\nMvaa 116,148,164\nTa 222,19,51200\nMvaa 84,148,160\nMvaa 88,152,160\nSv 248,1,16,76,2048\nMvaa 120,152,164\n"
"Mvaa 152,152,168\nMvaa 184,152,172\nMvaa 188,156,172\nMvaa 156,156,168\nSv 252,1,16,76,3072\nMvaa 124,156,164\nMvaa 92,156,160\ns_waitcnt vmcnt(25) lgkmcnt(0)\ns_barrier\n"
"Mvaa 64,192,224\nRa 160,44,32768\nAR 60,0\nAR 61,1\nMvaa 96,192,228\nRa 164,44,34816\ns_add_u32 m0, s47, 32768\ns_add_u32 s46, s51, 2621440\nD0 6,32,46,0\nMvaa 128,192,232\n"
"Ra 168,46,32768\nCV 52,60,61\nMvaa 160,192,236\nRa 172,46,34816\nAR 60,2\nMvaa 164,196,236\nTa 128,32,0\nAR 61,3\nMvaa 132,196,232\nD0 7,32,46,1024\nCV 53,60,61\nMvaa 100,196,228\n"
"Ta 130,32,2048\nAR 60,4\nMvaa 68,196,224\nTa 132,33,0\nAR 61,5\nMvaa 72,200,224\nTa 134,33,2048\nCV 54,60,61\nMvaa 104,200,228\nTa 136,34,0\nD0 8,32,46,2048\nMvaa 136,200,232\n"
"Ta 138,34,2048\nAR 60,6\nMvaa 168,200,236\nAR 61,7\nCV 55,60,61\nMvaa 172,204,236\nTa 140,35,0\nAR 60,8\nMvaa 140,204,232\nTa 142,35,2048\nD0 9,32,46,3072\nMvaa 108,204,228\n"
"Ta 144,36,0\nAR 61,9\nMvaa 76,204,224\nTa 146,36,2048\ns_add_u32 m0, s47, 49152\ns_add_u32 s46, s51, 2883584\nMvaa 80,208,224\nTa 148,37,0\nCV 56,60,61\nAR 60,10\n"
"Mvaa 112,208,228\nD0 6,32,46,0\nAR 61,11\nMvaa 144,208,232\nTa 150,37,2048\nCV 57,60,61\nMvaa 176,208,236\nTa 152,38,0\nAR 60,12\nMvaa 180,212,236\nTa 154,38,2048\nAR 61,13\n"
"Mvaa 148,212,232\nTa 156,39,0\nD0 7,32,46,1024\nMvaa 116,212,228\nTa 158,39,2048\nCV 58,60,61\nMvaa 84,212,224\nAR 60,14\nAR 61,15\nMvaa 88,216,224\nCV 59,60,61\nAR 60,16\n"
"Mvaa 120,216,228\nD0 8,32,46,2048\nAR 61,17\nMvaa 152,216,232\nCV 20,60,61\nAR 60,18\nMvaa 184,216,236\nAR 61,19\nCV 21,60,61\nMvaa 188,220,236\nAR 60,20\nAR 61,21\n"
"Mvaa 156,220,232\nD0 9,32,46,3072\nCV 22,60,61\nMvaa 124,220,228\nAR 60,22\nAR 61,23\nMvaa 92,220,224\nWL 0\nMvaa 64,128,160\nRa 224,45,32768\nCV 23,60,61\nAR 60,24\n"
"Mvaa 96,128,164\nRa 228,45,34816\ns_add_u32 m0, s47, -49152\ns_add_u32 s46, s52, 768\nD0 40,28,46,0\nMvaa 128,128,168\nRa 232,47,32768\nAR 61,25\nMvaa 160,128,172\n"
"Ra 236,47,34816\nCV 2,60,61\nMvaa 164,132,172\nTa 192,32,16384\nAR 60,26\nMvaa 132,132,168\nD0 41,28,46,1024\nAR 61,27\nMvaa 100,132,164\nTa 194,32,18432\nCV 3,60,61\n"
"Mvaa 68,132,160\nTa 196,33,16384\nAR 60,28\nMvaa 72,136,160\nTa 198,33,18432\nAR 61,29\nMvaa 104,136,164\nTa 200,34,16384\nD0 42,28,46,2048\nMvaa 136,136,168\nTa 202,34,18432\n"
"CV 4,60,61\nMvaa 168,136,172\nAR 60,30\nAR 61,31\nMvaa 172,140,172\nTa 204,35,16384\nCV 5,60,61\nMvaa 140,140,168\nTa 206,35,18432\nD0 43,28,46,3072\nMvaa 108,140,164\n"
"Ta 208,36,16384\nSv 52,1,16,77,0\nMvaa 76,140,160\nTa 210,36,18432\nMvaa 80,144,160\nTa 212,37,16384\nMvaa 112,144,164\nMvaa 144,144,168\nTa 214,37,18432\nMvaa 176,144,172\n"
"Ta 216,38,16384\nSv 56,1,16,77,1024\nMvaa 180,148,172\nTa 218,38,18432\nMvaa 148,148,168\nTa 220,39,16384\nMvaa 116,148,164\nTa 222,39,18432\nMvaa 84,148,160\nMvaa 88,152,160\n"
"Sv 20,1,16,77,2048\nMvaa 120,152,164\nMvaa 152,152,168\nMvaa 184,152,172\nMvaa 188,156,172\nMvaa 156,156,168\nSv 2,1,16,77,3072\nMvaa 124,156,164\nMvaa 92,156,160\n"
"s_waitcnt vmcnt(24) lgkmcnt(0)\ns_barrier\nMvaa 64,192,224\nRa 160,44,0\nbuffer_load_dword v27, v1, s[16:19], s58 offen offset:0 sc0 sc1\nAR 60,32\nMvaa 96,192,228\n"
"Ra 164,44,2048\ns_add_u32 m0, s47, 65536\ns_add_u32 s46, s51, 3145728\nD0 6,32,46,0\nMvaa 128,192,232\nRa 168,46,0\nAR 61,33\nMvaa 160,192,236\nRa 172,46,2048\nCV 52,60,61\n"
"Mvaa 164,196,236\nTa 128,12,0\nAR 60,34\nMvaa 132,196,232\nD0 7,32,46,1024\nAR 61,35\nMvaa 100,196,228\nTa 130,12,2048\nCV 53,60,61\nMvaa 68,196,224\nTa 132,13,0\nAR 60,36\n"
"Mvaa 72,200,224\nTa 134,13,2048\nAR 61,37\nCV 54,60,61\nMvaa 104,200,228\nTa 136,14,0\nD0 8,32,46,2048\nMvaa 136,200,232\nTa 138,14,2048\nAR 60,38\nMvaa 168,200,236\nAR 61,39\n"
"CV 55,60,61\nMvaa 172,204,236\nTa 140,15,0\nAR 60,40\nMvaa 140,204,232\nTa 142,15,2048\nD0 9,32,46,3072\nMvaa 108,204,228\nTa 144,16,0\nAR 61,41\nMvaa 76,204,224\nTa 146,16,2048\n"
"s_add_u32 m0, s47, 81920\ns_add_u32 s46, s51, 3407872\nMvaa 80,208,224\nTa 148,17,0\nCV 56,60,61\nAR 60,42\nMvaa 112,208,228\nD0 6,32,46,0\nAR 61,43\nMvaa 144,208,232\n"
"Ta 150,17,2048\nCV 57,60,61\nMvaa 176,208,236\nTa 152,18,0\nAR 60,44\nMvaa 180,212,236\nTa 154,18,2048\nAR 61,45\nMvaa 148,212,232\nTa 156,19,0\nD0 7,32,46,1024\nMvaa 116,212,228\n"
"Ta 158,19,2048\nCV 58,60,61\nMvaa 84,212,224\nAR 60,46\nAR 61,47\nMvaa 88,216,224\nCV 59,60,61\nAR 60,48\nAR 61,49\nMvaa 120,216,228\nD0 8,32,46,2048\nCV 20,60,61\n"
"Mvaa 152,216,232\nAR 60,50\nAR 61,51\nMvaa 184,216,236\nCV 21,60,61\nAR 60,52\nMvaa 188,220,236\nAR 61,53\nCV 22,60,61\nMvaa 156,220,232\nD0 9,32,46,3072\nAR 60,54\n"
"Mvaa 124,220,228\nAR 61,55\nCV 23,60,61\nMvaa 92,220,224\nWL 0\nMvaa 64,128,160\nRa 224,45,0\nAR 60,56\nAR 61,57\nMvaa 96,128,164\nRa 228,45,2048\ns_add_u32 s6, s56, 0\n"
"buffer_load_dwordx4 v[192:195], v1, s[16:19], s6 offen offset:0 sc1\nMvaa 128,128,168\nRa 232,47,0\nCV 2,60,61\nMvaa 160,128,172\nRa 236,47,2048\nAR 60,58\nMvaa 164,132,172\n"
"Ta 192,12,16384\nAR 61,59\nMvaa 132,132,168\nbuffer_load_dwordx4 v[196:199], v1, s[16:19], s6 offen offset:1024 sc1\nCV 3,60,61\nMvaa 100,132,164\nTa 194,12,18432\nAR 60,60\n"
"Mvaa 68,132,160\nTa 196,13,16384\nAR 61,61\nMvaa 72,136,160\nTa 198,13,18432\nCV 4,60,61\nAR 60,62\nMvaa 104,136,164\nTa 200,14,16384\n"
"buffer_load_dwordx4 v[200:203], v1, s[16:19], s6 offen offset:2048 sc1\nMvaa 136,136,168\nTa 202,14,18432\nAR 61,63\nMvaa 168,136,172\nCV 5,60,61\nMvaa 172,140,172\n"
"Ta 204,15,16384\nMvaa 140,140,168\nTa 206,15,18432\nbuffer_load_dwordx4 v[204:207], v1, s[16:19], s6 offen offset:3072 sc1\nMvaa 108,140,164\nTa 208,16,16384\n"
"s_add_u32 s77, s77, s55\nSv 52,1,16,77,0\nMvaa 76,140,160\nTa 210,16,18432\nMvaa 80,144,160\nTa 212,17,16384\nMvaa 112,144,164\nMvaa 144,144,168\nTa 214,17,18432\n"
"Mvaa 176,144,172\nTa 216,18,16384\nSv 56,1,16,77,1024\nMvaa 180,148,172\nTa 218,18,18432\nMvaa 148,148,168\nTa 220,19,16384\nMvaa 116,148,164\nTa 222,19,18432\nMvaa 84,148,160\n"
"Mvaa 88,152,160\nSv 20,1,16,77,2048\nMvaa 120,152,164\nMvaa 152,152,168\nMvaa 184,152,172\nMvaa 188,156,172\nMvaa 156,156,168\nSv 2,1,16,77,3072\nMvaa 124,156,164\n"
"Mvaa 92,156,160\ns_waitcnt vmcnt(16) lgkmcnt(0)\ns_barrier\nMvaa 64,192,224\nRa 160,44,49152\nv_cmp_ne_u32 vcc, s15, v27\ns_and_b32 s2, s12, 3\ns_lshl_b32 s2, s2, 4\ns_nop 1\n"
"s_lshr_b64 vcc, vcc, s2\ns_and_b64 vcc, vcc, 0xff00\ns_cbranch_vccnz .S54\n.S55:\ns_sub_u32 s45, s86, 67584\ns_cmp_eq_u32 s79, 1\ns_cselect_b32 s86, s94, s86\n"
"s_add_u32 m0, s47, -65536\ns_add_u32 s46, s45, 0x0\nMvaa 96,192,228\nRa 164,44,51200\nAR 60,64\nbuffer_store_dword v25, off, s[16:19], s54 offset:8 sc0 sc1\nMvaa 128,192,232\n"
"Ra 168,46,49152\nD1 1,16,46,0\nMvaa 160,192,236\nRa 172,46,51200\nAR 61,65\nMvaa 164,196,236\nTa 128,12,32768\nCV 52,60,61\nMvaa 132,196,232\nAR 60,66\nAR 61,67\nMvaa 100,196,228\n"
"Ta 130,12,34816\nD1 1,16,46,1024\nMvaa 68,196,224\nTa 132,13,32768\nCV 53,60,61\nMvaa 72,200,224\nTa 134,13,34816\nAR 60,68\nAR 61,69\nMvaa 104,200,228\nTa 136,14,32768\n"
"CV 54,60,61\nMvaa 136,200,232\nTa 138,14,34816\nD1 1,16,46,2048\nMvaa 168,200,236\nAR 60,70\nAR 61,71\nMvaa 172,204,236\nTa 140,15,32768\nCV 55,60,61\nMvaa 140,204,232\n"
"Ta 142,15,34816\nAR 60,72\nMvaa 108,204,228\nTa 144,16,32768\nD1 1,16,46,3072\nMvaa 76,204,224\nTa 146,16,34816\nAR 61,73\nMvaa 80,208,224\nTa 148,17,32768\n"
"s_add_u32 s6, s56, 4096\nCV 56,60,61\nMvaa 112,208,228\nAR 60,74\nAR 61,75\nMvaa 144,208,232\nTa 150,17,34816\nbuffer_load_dwordx4 v[208:211], v1, s[16:19], s6 offen offset:0 sc1\n"
"Mvaa 176,208,236\nTa 152,18,32768\nCV 57,60,61\nMvaa 180,212,236\nTa 154,18,34816\nAR 60,76\nMvaa 148,212,232\nTa 156,19,32768\nAR 61,77\nMvaa 116,212,228\nTa 158,19,34816\n"
"buffer_load_dwordx4 v[212:215], v1, s[16:19], s6 offen offset:1024 sc1\nMvaa 84,212,224\nCV 58,60,61\nAR 60,78\nMvaa 88,216,224\nAR 61,79\nCV 59,60,61\nAR 60,80\nMvaa 120,216,228\n"
"AR 61,81\nCV 20,60,61\nMvaa 152,216,232\nbuffer_load_dwordx4 v[216:219], v1, s[16:19], s6 offen offset:2048 sc1\nAR 60,82\nMvaa 184,216,236\nAR 61,83\nCV 21,60,61\n"
"Mvaa 188,220,236\nAR 60,84\nAR 61,85\nMvaa 156,220,232\nCV 22,60,61\nAR 60,86\nMvaa 124,220,228\nbuffer_load_dwordx4 v[220:223], v1, s[16:19], s6 offen offset:3072 sc1\nAR 61,87\n"
"Mvaa 92,220,224\nWL 0\nMvaa 64,128,160\nRa 224,45,49152\ns_add_u32 s6, s56, 8192\nCV 23,60,61\nMvaa 96,128,164\nRa 228,45,51200\nAR 60,88\nMvaa 128,128,168\nRa 232,47,49152\n"
"buffer_load_dwordx4 v[224:227], v1, s[16:19], s6 offen offset:0 sc1\nMvaa 160,128,172\nRa 236,47,51200\nAR 61,89\nMvaa 164,132,172\nTa 192,12,49152\nCV 2,60,61\nMvaa 132,132,168\n"
"AR 60,90\nAR 61,91\nMvaa 100,132,164\nTa 194,12,51200\nbuffer_load_dwordx4 v[228:231], v1, s[16:19], s6 offen offset:1024 sc1\nMvaa 68,132,160\nTa 196,13,49152\nCV 3,60,61\n"
"Mvaa 72,136,160\nTa 198,13,51200\nAR 60,92\nAR 61,93\nMvaa 104,136,164\nTa 200,14,49152\nCV 4,60,61\nMvaa 136,136,168\nTa 202,14,51200\n"
"buffer_load_dwordx4 v[232:235], v1, s[16:19], s6 offen offset:2048 sc1\nMvaa 168,136,172\nAR 60,94\nAR 61,95\nMvaa 172,140,172\nTa 204,15,49152\nCV 5,60,61\nMvaa 140,140,168\n"
"Ta 206,15,51200\nMvaa 108,140,164\nTa 208,16,49152\nbuffer_load_dwordx4 v[236:239], v1, s[16:19], s6 offen offset:3072 sc1\nSv 52,1,16,78,0\nMvaa 76,140,160\nTa 210,16,51200\n"
"Mvaa 80,144,160\nTa 212,17,49152\nMvaa 112,144,164\nMvaa 144,144,168\nTa 214,17,51200\nMvaa 176,144,172\nTa 216,18,49152\nSv 56,1,16,78,1024\nMvaa 180,148,172\nTa 218,18,51200\n"
"Mvaa 148,148,168\nTa 220,19,49152\nMvaa 116,148,164\nTa 222,19,51200\nMvaa 84,148,160\nMvaa 88,152,160\nSv 20,1,16,78,2048\nMvaa 120,152,164\nMvaa 152,152,168\nMvaa 184,152,172\n"
"Mvaa 188,156,172\nMvaa 156,156,168\nSv 2,1,16,78,3072\nMvaa 124,156,164\nMvaa 92,156,160\ns_waitcnt vmcnt(25) lgkmcnt(0)\ns_barrier\nMvaa 64,192,224\nRa 160,44,16384\nAR 60,96\n"
"AR 61,97\nMvaa 96,192,228\nRa 164,44,18432\ns_add_u32 s6, s56, 12288\nbuffer_load_dwordx4 v[240:243], v1, s[16:19], s6 offen offset:0 sc1\nMvaa 128,192,232\nRa 168,46,16384\n"
"CV 52,60,61\nMvaa 160,192,236\nRa 172,46,18432\nAR 60,98\nMvaa 164,196,236\nTa 128,32,0\nAR 61,99\nMvaa 132,196,232\n"
"buffer_load_dwordx4 v[244:247], v1, s[16:19], s6 offen offset:1024 sc1\nCV 53,60,61\nMvaa 100,196,228\nTa 130,32,2048\nAR 60,100\nMvaa 68,196,224\nTa 132,33,0\nAR 61,101\n"
"Mvaa 72,200,224\nTa 134,33,2048\nCV 54,60,61\nMvaa 104,200,228\nTa 136,34,0\nbuffer_load_dwordx4 v[248:251], v1, s[16:19], s6 offen offset:2048 sc1\nMvaa 136,200,232\n"
"Ta 138,34,2048\nAR 60,102\nMvaa 168,200,236\nAR 61,103\nCV 55,60,61\nMvaa 172,204,236\nTa 140,35,0\nAR 60,104\nMvaa 140,204,232\nTa 142,35,2048\n"
"buffer_load_dwordx4 v[252:255], v1, s[16:19], s6 offen offset:3072 sc1\nMvaa 108,204,228\nTa 144,36,0\nAR 61,105\nMvaa 76,204,224\nTa 146,36,2048\ns_add_u32 m0, s47, -32768\n"
"s_add_u32 s46, s45, 0x4000\nMvaa 80,208,224\nTa 148,37,0\nCV 56,60,61\nAR 60,106\nMvaa 112,208,228\nD1 1,16,46,0\nAR 61,107\nMvaa 144,208,232\nTa 150,37,2048\nCV 57,60,61\n"
"Mvaa 176,208,236\nTa 152,38,0\nAR 60,108\nMvaa 180,212,236\nTa 154,38,2048\nAR 61,109\nMvaa 148,212,232\nTa 156,39,0\nD1 1,16,46,1024\nMvaa 116,212,228\nTa 158,39,2048\n"
"CV 58,60,61\nMvaa 84,212,224\nAR 60,110\nAR 61,111\nMvaa 88,216,224\nCV 59,60,61\nAR 60,112\nMvaa 120,216,228\nD1 1,16,46,2048\nAR 61,113\nMvaa 152,216,232\nCV 20,60,61\n"
"AR 60,114\nMvaa 184,216,236\nAR 61,115\nCV 21,60,61\nMvaa 188,220,236\nAR 60,116\nAR 61,117\nMvaa 156,220,232\nD1 1,16,46,3072\nCV 22,60,61\nMvaa 124,220,228\nAR 60,118\n"
"AR 61,119\nMvaa 92,220,224\nWL 0\nMvaa 64,128,160\nRa 224,45,16384\nCV 23,60,61\nAR 60,120\nMvaa 96,128,164\nRa 228,45,18432\ns_add_u32 m0, s47, -16384\n"
"s_add_u32 s46, s45, 0x1102000\nD1 1,16,46,0\nMvaa 128,128,168\nRa 232,47,16384\nAR 61,121\nMvaa 160,128,172\nRa 236,47,18432\nCV 2,60,61\nMvaa 164,132,172\nTa 192,32,16384\n"
"AR 60,122\nMvaa 132,132,168\nD1 1,16,46,1024\nAR 61,123\nMvaa 100,132,164\nTa 194,32,18432\nCV 3,60,61\nMvaa 68,132,160\nTa 196,33,16384\nAR 60,124\nMvaa 72,136,160\n"
"Ta 198,33,18432\nAR 61,125\nMvaa 104,136,164\nTa 200,34,16384\nD1 1,16,46,2048\nMvaa 136,136,168\nTa 202,34,18432\nCV 4,60,61\nMvaa 168,136,172\nAR 60,126\nAR 61,127\n"
"Mvaa 172,140,172\nTa 204,35,16384\nCV 5,60,61\nMvaa 140,140,168\nTa 206,35,18432\nD1 1,16,46,3072\nMvaa 108,140,164\nTa 208,36,16384\ns_add_u32 s78, s78, s55\nSv 52,1,16,78,0\n"
"Mvaa 76,140,160\nTa 210,36,18432\nMvaa 80,144,160\nTa 212,37,16384\nMvaa 112,144,164\nMvaa 144,144,168\nTa 214,37,18432\nMvaa 176,144,172\nTa 216,38,16384\nSv 56,1,16,78,1024\n"
"Mvaa 180,148,172\nTa 218,38,18432\nMvaa 148,148,168\nTa 220,39,16384\nMvaa 116,148,164\nTa 222,39,18432\nMvaa 84,148,160\nMvaa 88,152,160\nSv 20,1,16,78,2048\nMvaa 120,152,164\n"
"Mvaa 152,152,168\nMvaa 184,152,172\nMvaa 188,156,172\nMvaa 156,156,168\nSv 2,1,16,78,3072\nMvaa 124,156,164\nMvaa 92,156,160\ns_waitcnt vmcnt(28) lgkmcnt(0)\ns_barrier\n"
"Mvaa 64,192,224\nv_add_u32 v2, 0x0, v1\ns_lshr_b32 s2, s59, 1\nMvaa 68,196,224\ns_add_u32 m0, s47, 0\ns_add_u32 s46, s86, 0xffff7c00\nD1 1,16,46,0\nMvaa 72,200,224\n"
"v_add_u32 v24, s2, v2\nv_xor_b32 v26, 0x1000, v24\nMvaa 76,204,224\nv_lshrrev_b32 v20, 4, v0\nv_lshlrev_b32 v20, 2, v20\nMvaa 80,208,224\nv_and_b32 v0, 15, v0\n"
"v_sub_u32 v0, v0, v20\nMvaa 84,212,224\nD1 1,16,46,1024\ns_and_b32 s2, s11, 1\nMvaa 88,216,224\ns_lshl_b32 s2, s2, 6\nv_subrev_u32 v3, s2, v0\nMvaa 92,220,224\n"
"s_xor_b32 s2, s2, 64\nv_subrev_u32 v0, s2, v0\nv_mul_f32 v64, s57, v64\nMvaa 96,192,228\nv_mul_f32 v65, s57, v65\nCV 64,64,65\nv_mul_f32 v66, s57, v66\nMvaa 100,196,228\n"
"D1 1,16,46,2048\nv_mul_f32 v67, s57, v67\nCV 65,66,67\nMvaa 104,200,228\nv_mul_f32 v68, s57, v68\nv_mul_f32 v69, s57, v69\nCV 66,68,69\nMvaa 108,204,228\nv_mov_b32 v4, 0\n"
"v_mov_b32 v5, 0\nMvaa 112,208,228\nRa 128,24,0\nv_mov_b32 v6, 0\nv_mul_f32 v70, s57, v70\nMvaa 116,212,228\nD1 1,16,46,3072\nv_mov_b32 v7, 0\nv_mul_f32 v71, s57, v71\n"
"Mvaa 120,216,228\nRa 132,24,1024\nv_mov_b32 v8, 0\nCV 67,70,71\nMvaa 124,220,228\ns_add_u32 m0, s47, 16384\ns_add_u32 s46, s45, 0x1106000\nv_mul_f32 v72, s57, v72\n"
"Mvaa 128,192,232\nRa 136,24,2048\nv_mov_b32 v9, 0\nv_mul_f32 v73, s57, v73\nMvaa 132,196,232\nD1 1,16,46,0\nv_mov_b32 v10, 0\nMvaa 136,200,232\nRa 140,24,3072\nv_mov_b32 v11, 0\n"
"CV 72,72,73\nMvaa 140,204,232\nv_mov_b32 v12, 0\nv_mov_b32 v13, 0\nv_mul_f32 v74, s57, v74\nMvaa 144,208,232\nRa 144,26,0\nv_mov_b32 v14, 0\nv_mul_f32 v75, s57, v75\n"
"Mvaa 148,212,232\nD1 1,16,46,1024\nv_mov_b32 v15, 0\nCV 73,74,75\nMvaa 152,216,232\nRa 148,26,1024\nv_mov_b32 v16, 0\nMvaa 156,220,232\nv_mov_b32 v17, 0\nv_mov_b32 v18, 0\n"
"v_mul_f32 v76, s57, v76\nMvaa 160,192,236\nRa 152,26,2048\nv_mov_b32 v19, 0\nv_mul_f32 v77, s57, v77\nMvaa 164,196,236\nD1 1,16,46,2048\ns_lshl_b32 s2, s11, 13\nCV 74,76,77\n"
"Mvaa 168,200,236\nRa 156,26,3072\ns_add_u32 s2, s2, 0x20000\nv_mul_f32 v78, s57, v78\nMvaa 172,204,236\nv_add_u32 v22, s2, v1\nv_xor_b32 v23, 0x2000, v22\nMvaa 176,208,236\n"
"v_mul_f32 v79, s57, v79\nCV 75,78,79\nv_mul_f32 v80, s57, v80\nMvaa 180,212,236\nD1 1,16,46,3072\nv_mul_f32 v81, s57, v81\nCV 80,80,81\nMvaa 184,216,236\nv_mul_f32 v82, s57, v82\n"
"v_mul_f32 v83, s57, v83\nCV 81,82,83\nMvaa 188,220,236\ns_waitcnt vmcnt(32) lgkmcnt(4)\ns_cmp_eq_u32 s79, 1\ns_cbranch_scc1 .E58\n.E59:\ns_nop 2\nCvav 32,128,192,4\n"
"Ra 160,24,8192\nv_mul_f32 v84, s57, v84\nv_mul_f32 v85, s57, v85\nMvav 32,132,200\nCV 82,84,85\nv_mul_f32 v86, s57, v86\nMvav 32,136,208\nRa 164,24,9216\nv_mul_f32 v87, s57, v87\n"
"CV 83,86,87\nMvav 32,140,216\nv_mul_f32 v88, s57, v88\nv_mul_f32 v89, s57, v89\nCV 88,88,89\nCvav 36,140,220,8\nRa 168,24,10240\nv_mul_f32 v90, s57, v90\nv_mul_f32 v91, s57, v91\n"
"Mvav 36,136,212\nCV 89,90,91\nv_mul_f32 v92, s57, v92\nv_mul_f32 v93, s57, v93\nMvav 36,132,204\nRa 172,24,11264\nMvav 36,128,196\ns_waitcnt vmcnt(20) lgkmcnt(4)\ns_barrier\n"
"s_add_u32 m0, s89, 32768\ns_add_u32 s46, s86, s88\nMvav 36,144,228\nRa 176,26,8192\nCV 90,92,93\nv_mul_f32 v94, s57, v94\nMvav 36,148,236\nv_mul_f32 v95, s57, v95\nCV 91,94,95\n"
"AW 192,64\nMvav 36,152,244\nRa 180,26,9216\nAW 193,65\nAW 194,66\nMvav 36,156,252\nAW 195,67\nAW 200,72\nMvav 32,156,248\nRa 184,26,10240\nAW 201,73\nAW 202,74\nMvav 32,152,240\n"
"D1 1,16,46,0\nAW 203,75\nAW 208,80\nMvav 32,148,232\nRa 188,26,11264\nWL 4\nMvav 32,144,224\ns_cmp_eq_u32 s99, 0\ns_cselect_b32 s87, s95, s87\nAW 209,81\nCvav 40,160,192,12\n"
"AW 210,82\nAW 211,83\nAW 216,88\nMvav 40,164,200\nAW 217,89\nAW 218,90\nMvav 40,168,208\nbuffer_load_dword v27, v1, s[16:19], s58 offen offset:8 sc0 sc1\nAW 219,91\n"
"v_mov_b32 v86, 0\nMvav 40,172,216\nv_mov_b32 v87, 0\nv_mov_b32 v88, 0\nv_mov_b32 v89, 0\nv_mul_f32 v128, s57, v128\nv_mul_f32 v129, s57, v129\nCV 128,128,129\n"
"v_mul_f32 v130, s57, v130\nCvav 44,172,220,16\nv_mul_f32 v131, s57, v131\nCV 129,130,131\nv_mul_f32 v132, s57, v132\nv_mul_f32 v133, s57, v133\nCV 130,132,133\n"
"v_mul_f32 v134, s57, v134\nv_mul_f32 v135, s57, v135\nCV 131,134,135\nMvav 44,168,212\nD1 1,16,46,1024\nds_write_b128 v23, v[128:131] offset:0\nv_mul_f32 v136, s57, v136\n"
"v_mul_f32 v137, s57, v137\nCV 136,136,137\nv_mul_f32 v138, s57, v138\nv_mul_f32 v139, s57, v139\nCV 137,138,139\nMvav 44,164,204\nWL 1\nMvav 44,160,196\ns_add_u32 s88, s88, 17408\n"
"v_mul_f32 v140, s57, v140\nv_mul_f32 v141, s57, v141\nCV 138,140,141\nv_mul_f32 v142, s57, v142\nv_mul_f32 v143, s57, v143\nCV 139,142,143\n"
"ds_write_b128 v23, v[136:139] offset:2048\nMvav 44,176,228\nv_mul_f32 v144, s57, v144\nv_mul_f32 v145, s57, v145\nCV 144,144,145\nv_mul_f32 v146, s57, v146\n"
"v_mul_f32 v147, s57, v147\nCV 145,146,147\nv_mul_f32 v148, s57, v148\nv_mul_f32 v149, s57, v149\nMvav 44,180,236\nEX 80,32\nCV 146,148,149\nv_mul_f32 v150, s57, v150\n"
"v_mul_f32 v151, s57, v151\nCV 147,150,151\nds_write_b128 v23, v[144:147] offset:4096\nMvav 44,184,244\nv_mul_f32 v152, s57, v152\nv_mul_f32 v153, s57, v153\nCV 152,152,153\n"
"v_mul_f32 v154, s57, v154\nv_mul_f32 v155, s57, v155\nCV 153,154,155\nv_mul_f32 v156, s57, v156\nv_mul_f32 v157, s57, v157\nMvav 44,188,252\nEX 81,33\nCV 154,156,157\n"
"v_mul_f32 v158, s57, v158\nv_mul_f32 v159, s57, v159\nCV 155,158,159\nds_write_b128 v23, v[152:155] offset:6144\nMvav 40,188,248\nv_mul_f32 v160, s57, v160\n"
"v_mul_f32 v161, s57, v161\nCV 160,160,161\nv_mul_f32 v162, s57, v162\nv_mul_f32 v163, s57, v163\nCV 161,162,163\nv_mul_f32 v164, s57, v164\nv_mul_f32 v165, s57, v165\n"
"Mvav 40,184,240\nD1 1,16,46,2048\nMvav 40,180,232\nMvav 40,176,224\nCV 162,164,165\nv_mul_f32 v166, s57, v166\nv_mul_f32 v167, s57, v167\nCV 163,166,167\n"
"ds_write_b128 v23, v[160:163] offset:1024\nv_mul_f32 v168, s57, v168\nv_mul_f32 v169, s57, v169\nCV 168,168,169\nAD 84,80,81\nv_mul_f32 v170, s57, v170\nv_mul_f32 v171, s57, v171\n"
"CV 169,170,171\nv_mul_f32 v172, s57, v172\nv_mul_f32 v173, s57, v173\nCV 170,172,173\nv_mul_f32 v174, s57, v174\nEX 82,34\nv_mul_f32 v175, s57, v175\nCV 171,174,175\n"
"ds_write_b128 v23, v[168:171] offset:3072\nv_mul_f32 v176, s57, v176\nv_mul_f32 v177, s57, v177\nCV 64,80,81\nCV 176,176,177\nv_mul_f32 v178, s57, v178\nv_mul_f32 v179, s57, v179\n"
"CV 177,178,179\nv_mul_f32 v180, s57, v180\nv_mul_f32 v181, s57, v181\nCV 178,180,181\nEX 83,35\nv_mul_f32 v182, s57, v182\nv_mul_f32 v183, s57, v183\nCV 179,182,183\n"
"ds_write_b128 v23, v[176:179] offset:5120\nv_mul_f32 v184, s57, v184\nAD 84,84,82\nv_mul_f32 v185, s57, v185\nCV 184,184,185\nv_mul_f32 v186, s57, v186\nv_mul_f32 v187, s57, v187\n"
"CV 185,186,187\nv_mul_f32 v188, s57, v188\nv_mul_f32 v189, s57, v189\nD1 1,16,46,3072\ns_add_u32 m0, s89, 49152\nCV 186,188,189\nv_mul_f32 v190, s57, v190\n"
"v_mul_f32 v191, s57, v191\nCV 187,190,191\nds_write_b128 v23, v[184:187] offset:7168\ns_waitcnt vmcnt(13) lgkmcnt(0)\ns_barrier\nRa 128,24,32768\nAD 84,84,83\nRa 224,22,0\n"
"Ra 228,22,1024\nEX 80,36\nbuffer_store_dword v25, off, s[16:19], s54 offset:4 sc0 sc1\nRa 232,22,2048\nRa 132,24,33792\nCV 65,82,83\nRa 236,22,3072\nRa 240,22,4096\nEX 81,37\n"
"Ra 244,22,5120\nRa 248,22,6144\nRa 136,24,34816\nRa 252,22,7168\nv_mul_f32 v96, s57, v96\nv_mul_f32 v97, s57, v97\ns_add_u32 s46, s87, s88\nD1 1,16,46,0\nCV 96,96,97\n"
"v_mul_f32 v98, s57, v98\nRa 140,24,35840\ns_cmp_eq_u32 s99, 1\ns_cselect_b32 s86, s94, s86\nv_mul_f32 v99, s57, v99\nCV 97,98,99\nRa 144,26,32768\nAD 85,80,81\n"
"v_mul_f32 v100, s57, v100\nv_mul_f32 v101, s57, v101\nEX 82,38\nCV 98,100,101\nRa 148,26,33792\nCV 68,80,81\nv_mul_f32 v102, s57, v102\nv_mul_f32 v103, s57, v103\nEX 83,39\n"
"CV 99,102,103\nAW 196,96\nRa 152,26,34816\nAD 85,85,82\nAW 197,97\nAW 198,98\nD1 1,16,46,1024\nAW 199,99\nv_mul_f32 v104, s57, v104\nv_mul_f32 v105, s57, v105\nRa 156,26,35840\n"
"s_cmp_eq_u32 s99, s93\ns_cbranch_scc1 .E41\n.E42:\nRa 160,24,40960\nAD 85,85,83\nCV 104,104,105\nv_mul_f32 v106, s57, v106\nEX 80,40\nv_mul_f32 v107, s57, v107\nCV 105,106,107\n"
"Ra 164,24,41984\nCV 69,82,83\nv_mul_f32 v108, s57, v108\nv_mul_f32 v109, s57, v109\nEX 81,41\nCV 106,108,109\nv_mul_f32 v110, s57, v110\nRa 168,24,43008\nAD 84,84,80\n"
"v_mul_f32 v111, s57, v111\nCV 107,110,111\nD1 1,16,46,2048\nAW 204,104\nAW 205,105\nAW 206,106\nRa 172,24,44032\nWL 8\ns_xor_b32 s89, s89, 0x10000\nAW 207,107\n"
"v_mul_f32 v112, s57, v112\nv_mul_f32 v113, s57, v113\nCvav 48,128,192,4\nRa 176,26,40960\nAD 84,84,81\nCV 112,112,113\nv_mul_f32 v114, s57, v114\nMvav 48,132,200\nEX 82,42\n"
"v_mul_f32 v115, s57, v115\nMvav 48,136,208\nRa 180,26,41984\nCV 66,80,81\nCV 113,114,115\nv_mul_f32 v116, s57, v116\nMvav 48,140,216\nEX 83,43\nv_mul_f32 v117, s57, v117\n"
"CV 114,116,117\nWV 7\nv_cmp_ne_u32 vcc, s15, v27\ns_and_b32 s2, s12, 3\ns_lshl_b32 s2, s2, 4\ns_nop 1\ns_lshr_b64 vcc, vcc, s2\ns_and_b64 vcc, vcc, 0xff00\ns_cbranch_vccnz .S60\n"
".S61:\nCvav 52,140,220,8\nRa 184,26,43008\nAD 84,84,82\nv_mul_f32 v118, s57, v118\nv_mul_f32 v119, s57, v119\nMvav 52,136,212\nD1 1,16,46,3072\nCV 115,118,119\nAW 212,112\n"
"AW 213,113\nMvav 52,132,204\nRa 188,26,44032\nWL 8\nMvav 52,128,196\ns_add_u32 m0, s89, 0\ns_add_u32 s46, s86, s88\nAW 214,114\nAW 215,115\nMvav 52,144,228\nRa 128,2,49152\n"
"AD 84,84,83\nv_mul_f32 v120, s57, v120\nv_mul_f32 v121, s57, v121\nMvav 52,148,236\nEX 80,44\nCV 120,120,121\nv_mul_f32 v122, s57, v122\nMvav 52,152,244\nRa 132,2,50176\n"
"CV 67,82,83\nv_mul_f32 v123, s57, v123\nCV 121,122,123\nMvav 52,156,252\nEX 81,45\nv_mul_f32 v124, s57, v124\nv_mul_f32 v125, s57, v125\nMvav 48,156,248\nRa 136,2,51200\n"
"AD 85,85,80\nCV 122,124,125\nv_mul_f32 v126, s57, v126\nMvav 48,152,240\nD1 1,16,46,0\nv_xor_b32 v24, 0x10000, v24\nv_mul_f32 v127, s57, v127\nCV 123,126,127\nMvav 48,148,232\n"
"Ra 140,2,52224\nWL 8\nMvav 48,144,224\nAW 220,120\nAW 221,121\nAW 222,122\nAW 223,123\nCvav 56,160,192,12\nRa 144,2,53248\nAD 85,85,81\nMvav 56,164,200\nEX 82,46\nMvav 56,168,208\n"
"Ra 148,2,54272\nCV 70,80,81\nMvav 56,172,216\nEX 83,47\nCvav 60,172,220,16\nRa 152,2,55296\nAD 85,85,82\nMvav 60,168,212\nD1 1,16,46,1024\nv_xor_b32 v26, 0x10000, v26\n"
"Mvav 60,164,204\nRa 156,2,56320\nWL 8\nMvav 60,160,196\ns_add_u32 s88, s88, 16384\nMvav 60,176,228\nRa 160,2,57344\nAD 85,85,83\nMvav 60,180,236\nEX 80,48\nMvav 60,184,244\n"
"Ra 164,2,58368\nCV 71,82,83\nMvav 60,188,252\nEX 81,49\nMvav 56,188,248\nRa 168,2,59392\nv_max_f32 v90, v84, v85\nMvav 56,184,240\nD1 1,16,46,2048\nv_cmp_lt_f32 vcc, s63, v90\n"
"Mvav 56,180,232\nRa 172,2,60416\nWL 8\nMvav 56,176,224\ns_cbranch_vccnz .T43\n.T44:\nv_mov_b32 v20, v84\nv_mov_b32 v21, v85\nv_mov_b32 v22, v20\nv_mov_b32 v23, v21\ns_nop 0\n"
"v_permlane32_swap_b32 v20, v22\nv_permlane32_swap_b32 v21, v23\nAD 20,20,22\nAD 21,21,23\nv_mov_b32 v22, v20\nv_mov_b32 v23, v21\ns_nop 0\nv_permlane16_swap_b32 v20, v22\n"
"v_permlane16_swap_b32 v21, v23\nAD 20,20,22\nAD 21,21,23\nv_min_f32 v90, v20, v21\nv_cmp_gt_f32 vcc, s64, v90\ns_nop 0\ns_cbranch_vccnz .T45\n.T46:\nAD 86,86,84\nZaav 0,128,64\n"
"Ra 176,2,61440\nAD 84,80,81\nZaav 4,128,68\nEX 82,50\nZaav 12,132,68\nRa 180,2,62464\nCV 72,80,81\nZaav 8,132,64\nEX 83,51\nZaav 16,136,64\nRa 184,2,63488\nAD 84,84,82\n"
"Zaav 20,136,68\nD1 1,16,46,3072\ns_add_u32 m0, s89, 16384\nZaav 28,140,68\nRa 188,2,64512\nZaav 24,140,64\nWL 8\ns_barrier\nZaav 32,144,64\nRa 128,24,0\nAD 84,84,83\n"
"Zaav 36,144,68\nEX 80,52\nZaav 44,148,68\nRa 132,24,1024\nCV 73,82,83\nZaav 40,148,64\nEX 81,53\nZaav 48,152,64\nRa 136,24,2048\nAD 87,87,85\nZaav 52,152,68\n"
"s_add_u32 s46, s87, s88\nD1 1,16,46,0\nZaav 60,156,68\nRa 140,24,3072\nWL 8\nZaav 56,156,64\ns_sub_u32 s90, s90, 1\ns_cbranch_scc1 .E47\n.E48:\nZaav 64,160,64\nRa 144,26,0\n"
"AD 85,80,81\nZaav 68,160,68\nEX 82,54\nZaav 76,164,68\nRa 148,26,1024\nCV 76,80,81\nZaav 72,164,64\nEX 83,55\nZaav 80,168,64\nRa 152,26,2048\nAD 85,85,82\nZaav 84,168,68\n"
"D1 1,16,46,1024\nv_xor_b32 v2, 0x10000, v2\nZaav 92,172,68\nRa 156,26,3072\nWL 8\nZaav 88,172,64\ns_cmp_eq_u32 s99, s92\ns_cbranch_scc1 .E49\n.E50:\ns_nop 2\nZaav 96,176,64\n"
"Ra 160,24,8192\nAD 85,85,83\nZaav 100,176,68\nEX 80,56\nZaav 108,180,68\nRa 164,24,9216\nCV 77,82,83\nbuffer_load_dword v27, v1, s[16:19], s58 offen offset:4 sc0 sc1\n"
"Zaav 104,180,64\nEX 81,57\nZaav 112,184,64\nRa 168,24,10240\nAD 84,84,80\nZaav 116,184,68\nD1 1,16,46,2048\nZaav 124,188,68\nRa 172,24,11264\nWL 8\nZaav 120,188,64\n"
"s_sub_u32 s99, s99, 1\ns_cbranch_scc1 .SW8\ns_branch .A16\n.E41:\nv_mov_b32 v12, s97\nv_mov_b32 v13, s97\nv_mov_b32 v14, s97\nv_mov_b32 v15, s97\nv_cmpx_gt_i32 vcc, 3, v0\n"
"v_mov_b32 v7, s97\nv_mov_b32 v19, s97\nv_cmpx_gt_i32 vcc, 2, v0\nv_mov_b32 v6, s97\nv_mov_b32 v18, s97\nv_cmpx_gt_i32 vcc, 1, v0\nv_mov_b32 v5, s97\nv_mov_b32 v17, s97\n"
"v_cmpx_gt_i32 vcc, 0, v0\nv_mov_b32 v4, s97\nv_mov_b32 v16, s97\nv_cmpx_gt_i32 vcc, -16, v0\nv_mov_b32 v8, s97\nv_mov_b32 v9, s97\nv_mov_b32 v10, s97\nv_mov_b32 v11, s97\n"
"s_mov_b64 exec, -1\ns_branch .E42\n.T43:\ns_mov_b32 s62, 1\ns_call_b64 s[80:81], .RA12\ns_branch .T44\n.T45:\ns_mov_b32 s62, 1\ns_call_b64 s[80:81], .RA12\ns_branch .T46\n.E47:\n"
"s_swappc_b64 s[80:81], s[8:9]\ns_branch .E48\n.E49:\nv_mov_b32 v12, s97\nv_mov_b32 v13, s97\nv_mov_b32 v14, s97\nv_mov_b32 v15, s97\nv_cmpx_gt_i32 vcc, 3, v3\nv_mov_b32 v7, s97\n"
"v_mov_b32 v19, s97\nv_cmpx_gt_i32 vcc, 2, v3\nv_mov_b32 v6, s97\nv_mov_b32 v18, s97\nv_cmpx_gt_i32 vcc, 1, v3\nv_mov_b32 v5, s97\nv_mov_b32 v17, s97\nv_cmpx_gt_i32 vcc, 0, v3\n"
"v_mov_b32 v4, s97\nv_mov_b32 v16, s97\nv_cmpx_gt_i32 vcc, -16, v3\nv_mov_b32 v8, s97\nv_mov_b32 v9, s97\nv_mov_b32 v10, s97\nv_mov_b32 v11, s97\ns_mov_b64 exec, -1\ns_branch .E50\n"
".S51:\ns_mov_b32 s7, 262144\n.S53:\ns_sleep 1\nbuffer_load_dword v27, off, s[16:19], s5 sc0 sc1\nWV 0\nv_cmp_ne_u32 vcc, s15, v27\ns_nop 0\ns_cbranch_vccz .S52\n"
"s_sub_u32 s7, s7, 1\ns_cbranch_scc0 .S53\ns_branch .S52\n.S54:\ns_mov_b32 s7, 262144\n.S56:\ns_sleep 1\nbuffer_load_dword v27, v1, s[16:19], s58 offen offset:0 sc0 sc1\nWV 0\n"
"v_cmp_ne_u32 vcc, s15, v27\ns_and_b32 s2, s12, 3\ns_lshl_b32 s2, s2, 4\ns_nop 1\ns_lshr_b64 vcc, vcc, s2\ns_and_b64 vcc, vcc, 0xff00\ns_cbranch_vccz .S55\ns_sub_u32 s7, s7, 1\n"
"s_cbranch_scc0 .S56\ns_branch .S55\n.E58:\ns_cmp_eq_u32 s11, 3\ns_cbranch_scc1 .P57\ns_cmp_lt_u32 s11, 2\ns_cselect_b32 s64, -1.0, s64\ns_cselect_b32 s2, 64, 0\n"
"v_subrev_u32 v20, s2, v3\nv_mov_b32 v12, s97\nv_mov_b32 v13, s97\nv_mov_b32 v14, s97\nv_mov_b32 v15, s97\nv_cmpx_gt_i32 vcc, 3, v20\nv_mov_b32 v7, s97\nv_mov_b32 v19, s97\n"
"v_cmpx_gt_i32 vcc, 2, v20\nv_mov_b32 v6, s97\nv_mov_b32 v18, s97\nv_cmpx_gt_i32 vcc, 1, v20\nv_mov_b32 v5, s97\nv_mov_b32 v17, s97\nv_cmpx_gt_i32 vcc, 0, v20\nv_mov_b32 v4, s97\n"
"v_mov_b32 v16, s97\nv_cmpx_gt_i32 vcc, -16, v20\nv_mov_b32 v8, s97\nv_mov_b32 v9, s97\nv_mov_b32 v10, s97\nv_mov_b32 v11, s97\ns_mov_b64 exec, -1\n.P57:\ns_branch .E59\n.S60:\n"
"s_mov_b32 s7, 262144\n.S62:\ns_sleep 1\nbuffer_load_dword v27, v1, s[16:19], s58 offen offset:8 sc0 sc1\nWV 0\nv_cmp_ne_u32 vcc, s15, v27\ns_and_b32 s2, s12, 3\n"
"s_lshl_b32 s2, s2, 4\ns_nop 1\ns_lshr_b64 vcc, vcc, s2\ns_and_b64 vcc, vcc, 0xff00\ns_cbranch_vccz .S61\ns_sub_u32 s7, s7, 1\ns_cbranch_scc0 .S62\ns_branch .S61\n.F07:\n"
"Cvav 32,128,192,4\nRa 160,24,8192\nMvav 32,132,200\nMvav 32,136,208\nRa 164,24,9216\nMvav 32,140,216\nCvav 36,140,220,8\nRa 168,24,10240\nAR 92,16\nAR 93,17\nCV 136,92,93\n"
"Mvav 36,136,212\nAR 92,18\nAR 93,19\nCV 137,92,93\nAR 92,20\nMvav 36,132,204\nRa 172,24,11264\nMvav 36,128,196\nWL 4\ns_barrier\ns_add_u32 m0, s89, 32768\ns_add_u32 s46, s86, s88\n"
"Mvav 36,144,228\nRa 176,26,8192\nAR 93,21\nCV 138,92,93\nAR 92,22\nMvav 36,148,236\nAR 93,23\nCV 139,92,93\nAR 92,24\nMvav 36,152,244\nRa 180,26,9216\nAR 93,25\nCV 140,92,93\n"
"AR 92,26\nMvav 36,156,252\nAR 93,27\nCV 141,92,93\nAR 92,28\nMvav 32,156,248\nRa 184,26,10240\nAR 93,29\nCV 142,92,93\nAR 92,30\nMvav 32,152,240\nD1 1,16,46,0\nAR 93,31\n"
"CV 143,92,93\nAR 92,32\nMvav 32,148,232\nRa 188,26,11264\nWL 4\nMvav 32,144,224\nSv 96,1,16,76,0\nAR 93,33\nCV 144,92,93\nAR 92,34\nAR 93,35\nCvav 40,160,192,12\nCV 145,92,93\n"
"AR 92,36\nAR 93,37\nCV 146,92,93\nMvav 40,164,200\nAR 92,38\nAR 93,39\nCV 147,92,93\nMvav 40,168,208\nbuffer_load_dword v27, v1, s[16:19], s58 offen offset:8 sc0 sc1\nAR 92,40\n"
"AR 93,41\nCV 148,92,93\nMvav 40,172,216\nAR 92,42\nAR 93,43\nCV 149,92,93\nCvav 44,172,220,16\nAR 92,44\nAR 93,45\nCV 150,92,93\nAR 92,46\nMvav 44,168,212\nD1 1,16,46,1024\n"
"AR 93,47\nCV 151,92,93\nAR 92,48\nMvav 44,164,204\nWL 0\nMvav 44,160,196\ns_add_u32 s88, s88, 17408\nAR 93,49\nCV 152,92,93\nAR 92,50\nMvav 44,176,228\nAR 93,51\nCV 153,92,93\n"
"AR 92,52\nAR 93,53\nMvav 44,180,236\nEX 80,32\nCV 154,92,93\nMvav 44,184,244\nSv 100,1,16,76,1024\nAR 92,54\nAR 93,55\nCV 155,92,93\nAR 92,56\nMvav 44,188,252\nEX 81,33\nAR 93,57\n"
"Mvav 40,188,248\nCV 156,92,93\nAR 92,58\nAR 93,59\nCV 157,92,93\nMvav 40,184,240\nD1 1,16,46,2048\nMvav 40,180,232\nMvav 40,176,224\nAR 92,60\nAR 93,61\nCV 158,92,93\nAR 92,62\n"
"AD 84,80,81\nAR 93,63\nCV 159,92,93\nAR 92,64\nEX 82,34\nAR 93,65\nCV 64,80,81\nCV 160,92,93\nAR 92,66\nAR 93,67\nEX 83,35\nCV 161,92,93\nAD 84,84,82\nAR 92,68\nAR 93,69\n"
"CV 162,92,93\nD1 1,16,46,3072\ns_add_u32 m0, s89, 49152\nAR 92,70\nAR 93,71\nWV 15\ns_barrier\nRa 128,24,32768\nAD 84,84,83\nCV 163,92,93\nAR 92,72\nEX 80,36\nAR 93,73\n"
"Ra 132,24,33792\nCV 65,82,83\nCV 164,92,93\nAR 92,74\nEX 81,37\nAR 93,75\nRa 136,24,34816\nCV 165,92,93\nAR 92,76\nAR 93,77\ns_add_u32 s46, s87, s88\nD1 1,16,46,0\nCV 166,92,93\n"
"AR 92,78\nRa 140,24,35840\nSv 104,1,16,76,2048\nAR 93,79\nCV 167,92,93\nAR 92,80\nAR 93,81\nRa 144,26,32768\nAD 85,80,81\nCV 168,92,93\nAR 92,82\nEX 82,38\nAR 93,83\n"
"Ra 148,26,33792\nCV 68,80,81\nCV 169,92,93\nAR 92,84\nEX 83,39\nAR 93,85\nRa 152,26,34816\nAD 85,85,82\nCV 170,92,93\nAR 92,86\nD1 1,16,46,1024\nAR 93,87\nCV 171,92,93\nAR 92,88\n"
"Ra 156,26,35840\nAR 93,89\nCV 172,92,93\nAR 92,90\nAR 93,91\nRa 160,24,40960\nAD 85,85,83\nCV 173,92,93\nAR 92,92\nEX 80,40\nAR 93,93\nRa 164,24,41984\nCV 69,82,83\nCV 174,92,93\n"
"AR 92,94\nEX 81,41\nAR 93,95\nRa 168,24,43008\nAD 84,84,80\nCV 175,92,93\nAR 92,96\nD1 1,16,46,2048\nAR 93,97\nCV 176,92,93\nAR 92,98\nRa 172,24,44032\nWL 8\nSv 108,1,16,76,3072\n"
"s_xor_b32 s89, s89, 0x10000\nAR 93,99\nCV 177,92,93\nAR 92,100\nCvav 48,128,192,4\nRa 176,26,40960\nAD 84,84,81\nAR 93,101\nCV 178,92,93\nMvav 48,132,200\nEX 82,42\nAR 92,102\n"
"Mvav 48,136,208\nRa 180,26,41984\nCV 66,80,81\nAR 93,103\nCV 179,92,93\nMvav 48,140,216\nEX 83,43\nAR 92,104\nWV 9\nv_cmp_ne_u32 vcc, s15, v27\ns_and_b32 s2, s12, 3\n"
"s_lshl_b32 s2, s2, 4\ns_nop 1\ns_lshr_b64 vcc, vcc, s2\ns_and_b64 vcc, vcc, 0xff00\ns_cbranch_vccnz .S78\n.S79:\nCvav 52,140,220,8\nRa 184,26,43008\nAD 84,84,82\nAR 93,105\n"
"CV 180,92,93\nMvav 52,136,212\nD1 1,16,46,3072\nAR 92,106\nAR 93,107\nCV 181,92,93\nMvav 52,132,204\nRa 188,26,44032\nWL 8\nMvav 52,128,196\ns_add_u32 m0, s89, 0\n"
"s_add_u32 s46, s86, s88\nAR 92,108\nAR 93,109\nMvav 52,144,228\nRa 128,2,49152\nAD 84,84,83\nCV 182,92,93\nAR 92,110\nMvav 52,148,236\nEX 80,44\nAR 93,111\nMvav 52,152,244\n"
"Ra 132,2,50176\nCV 67,82,83\nCV 183,92,93\nAR 92,112\nMvav 52,156,252\nEX 81,45\nAR 93,113\nMvav 48,156,248\nRa 136,2,51200\nAD 85,85,80\nCV 184,92,93\nAR 92,114\nMvav 48,152,240\n"
"D1 1,16,46,0\nv_xor_b32 v24, 0x10000, v24\nAR 93,115\nCV 185,92,93\nMvav 48,148,232\nRa 140,2,52224\nWL 8\nMvav 48,144,224\ns_add_u32 s76, s76, s55\nSv 112,1,16,76,0\nAR 92,116\n"
"AR 93,117\nCV 186,92,93\nAR 92,118\nCvav 56,160,192,12\nRa 144,2,53248\nAD 85,85,81\nAR 93,119\nCV 187,92,93\nMvav 56,164,200\nEX 82,46\nAR 92,120\nMvav 56,168,208\n"
"Ra 148,2,54272\nCV 70,80,81\nAR 93,121\nCV 188,92,93\nMvav 56,172,216\nEX 83,47\nAR 92,122\nCvav 60,172,220,16\nRa 152,2,55296\nAD 85,85,82\nAR 93,123\nCV 189,92,93\n"
"Mvav 60,168,212\nD1 1,16,46,1024\nv_xor_b32 v26, 0x10000, v26\nAR 92,124\nAR 93,125\nMvav 60,164,204\nRa 156,2,56320\nWL 8\nMvav 60,160,196\ns_add_u32 s88, s88, 16384\n"
"CV 190,92,93\nAR 92,126\nAR 93,127\nMvav 60,176,228\nRa 160,2,57344\nAD 85,85,83\nCV 191,92,93\nMvav 60,180,236\nEX 80,48\nMvav 60,184,244\nSv 116,1,16,76,1024\nRa 164,2,58368\n"
"CV 71,82,83\nMvav 60,188,252\nEX 81,49\nMvav 56,188,248\nRa 168,2,59392\nv_max_f32 v90, v84, v85\nMvav 56,184,240\nD1 1,16,46,2048\nv_cmp_lt_f32 vcc, s63, v90\nMvav 56,180,232\n"
"Ra 172,2,60416\nWL 8\nMvav 56,176,224\ns_cbranch_vccnz .T63\n.T64:\nv_mov_b32 v20, v84\nv_mov_b32 v21, v85\nv_mov_b32 v22, v20\nv_mov_b32 v23, v21\ns_nop 0\n"
"v_permlane32_swap_b32 v20, v22\nv_permlane32_swap_b32 v21, v23\nAD 20,20,22\nAD 21,21,23\nv_mov_b32 v22, v20\nv_mov_b32 v23, v21\ns_nop 0\nv_permlane16_swap_b32 v20, v22\n"
"v_permlane16_swap_b32 v21, v23\nAD 20,20,22\nAD 21,21,23\nv_min_f32 v90, v20, v21\nv_cmp_gt_f32 vcc, s64, v90\ns_nop 0\ns_cbranch_vccnz .T65\n.T66:\nAD 86,86,84\nZaav 0,128,64\n"
"Ra 176,2,61440\nAD 84,80,81\nZaav 4,128,68\nEX 82,50\nZaav 12,132,68\nRa 180,2,62464\nCV 72,80,81\nZaav 8,132,64\nEX 83,51\nZaav 16,136,64\nRa 184,2,63488\nAD 84,84,82\n"
"Zaav 20,136,68\nD1 1,16,46,3072\ns_add_u32 m0, s89, 16384\nZaav 28,140,68\nRa 188,2,64512\nZaav 24,140,64\nWL 8\ns_barrier\nZaav 32,144,64\nRa 128,24,0\nAD 84,84,83\n"
"Zaav 36,144,68\nEX 80,52\nZaav 44,148,68\nRa 132,24,1024\nCV 73,82,83\nZaav 40,148,64\nEX 81,53\nZaav 48,152,64\nRa 136,24,2048\nAD 87,87,85\nZaav 52,152,68\n"
"s_add_u32 s46, s87, s88\nD1 1,16,46,0\nZaav 60,156,68\nRa 140,24,3072\nWL 8\nZaav 56,156,64\nSv 120,1,16,76,2048\nZaav 64,160,64\nRa 144,26,0\nAD 85,80,81\nZaav 68,160,68\n"
"EX 82,54\nZaav 76,164,68\nRa 148,26,1024\nCV 76,80,81\nZaav 72,164,64\nEX 83,55\nZaav 80,168,64\nRa 152,26,2048\nAD 85,85,82\nZaav 84,168,68\nD1 1,16,46,1024\n"
"v_xor_b32 v2, 0x10000, v2\nZaav 92,172,68\nRa 156,26,3072\nWL 8\nZaav 88,172,64\nZaav 96,176,64\nRa 160,24,8192\nAD 85,85,83\nZaav 100,176,68\nEX 80,56\nZaav 108,180,68\n"
"Ra 164,24,9216\nCV 77,82,83\nZaav 104,180,64\nEX 81,57\nZaav 112,184,64\nRa 168,24,10240\nAD 84,84,80\nZaav 116,184,68\nD1 1,16,46,2048\nZaav 124,188,68\nRa 172,24,11264\nWL 8\n"
"Zaav 120,188,64\nSv 124,1,16,76,3072\nCvav 32,128,192,4\nRa 176,26,8192\nAD 84,84,81\nMvav 32,132,200\nEX 82,58\nMvav 32,136,208\nRa 180,26,9216\nCV 74,80,81\nMvav 32,140,216\n"
"EX 83,59\nCvav 36,140,220,8\nRa 184,26,10240\nAD 84,84,82\nMvav 36,136,212\nD1 1,16,46,3072\nMvav 36,132,204\nRa 188,26,11264\nWL 8\nMvav 36,128,196\ns_add_u32 m0, s89, 32768\n"
"s_add_u32 s46, s86, s88\nMvav 36,144,228\nRa 128,2,16384\nAD 84,84,83\nMvav 36,148,236\nEX 80,60\nMvav 36,152,244\nRa 132,2,17408\nCV 75,82,83\nMvav 36,156,252\nEX 81,61\n"
"Mvav 32,156,248\nRa 136,2,18432\nAD 85,85,80\nMvav 32,152,240\nD1 1,16,46,0\nMvav 32,148,232\nRa 140,2,19456\nWL 8\nMvav 32,144,224\nSv 128,1,16,77,0\nCvav 40,160,192,12\n"
"Ra 144,2,20480\nAD 85,85,81\nMvav 40,164,200\nEX 82,62\nMvav 40,168,208\nRa 148,2,21504\nCV 78,80,81\nMvav 40,172,216\nEX 83,63\nCvav 44,172,220,16\nRa 152,2,22528\nAD 85,85,82\n"
"Mvav 44,168,212\nD1 1,16,46,1024\nMvav 44,164,204\nRa 156,2,23552\nWL 8\nMvav 44,160,196\ns_add_u32 s88, s88, 17408\nMvav 44,176,228\nRa 160,2,24576\nAD 85,85,83\nMvav 44,180,236\n"
"EX 80,32\nMvav 44,184,244\nSv 132,1,16,77,1024\nRa 164,2,25600\nCV 79,82,83\nMvav 44,188,252\nEX 81,33\nMvav 40,188,248\nRa 168,2,26624\nv_max_f32 v90, v84, v85\nMvav 40,184,240\n"
"D1 1,16,46,2048\nv_cmp_lt_f32 vcc, s63, v90\nMvav 40,180,232\nRa 172,2,27648\nWL 8\nMvav 40,176,224\ns_cbranch_vccnz .T67\n.T68:\nAD 86,86,84\nMaav 0,128,72\nRa 176,2,28672\n"
"AD 84,80,81\nMaav 4,128,76\nEX 82,34\nMaav 12,132,76\nRa 180,2,29696\nCV 64,80,81\nMaav 8,132,72\nEX 83,35\nMaav 16,136,72\nRa 184,2,30720\nAD 84,84,82\nMaav 20,136,76\n"
"D1 1,16,46,3072\ns_add_u32 m0, s89, 49152\nMaav 28,140,76\nRa 188,2,31744\nMaav 24,140,72\ns_waitcnt vmcnt(18) lgkmcnt(8)\ns_barrier\nMaav 32,144,72\nRa 128,24,32768\nAD 84,84,83\n"
"Maav 36,144,76\nEX 80,36\nMaav 44,148,76\nRa 132,24,33792\nCV 65,82,83\nMaav 40,148,72\nEX 81,37\nMaav 48,152,72\nRa 136,24,34816\nAD 87,87,85\nMaav 52,152,76\n"
"s_add_u32 s46, s87, s88\nD1 1,16,46,0\nMaav 60,156,76\nRa 140,24,35840\nWL 8\nMaav 56,156,72\nSv 136,1,16,77,2048\nMaav 64,160,72\nRa 144,26,32768\nAD 85,80,81\nMaav 68,160,76\n"
"EX 82,38\nMaav 76,164,76\nRa 148,26,33792\nCV 68,80,81\nMaav 72,164,72\nEX 83,39\nMaav 80,168,72\nRa 152,26,34816\nAD 85,85,82\nMaav 84,168,76\nD1 1,16,46,1024\nMaav 92,172,76\n"
"Ra 156,26,35840\nWL 8\nMaav 88,172,72\nMaav 96,176,72\nRa 160,24,40960\nAD 85,85,83\nMaav 100,176,76\nEX 80,40\nMaav 108,180,76\nRa 164,24,41984\nCV 69,82,83\nMaav 104,180,72\n"
"EX 81,41\nMaav 112,184,72\nRa 168,24,43008\nAD 84,84,80\nMaav 116,184,76\nD1 1,16,46,2048\nMaav 124,188,76\nRa 172,24,44032\nWL 8\nMaav 120,188,72\nSv 140,1,16,77,3072\n"
"s_xor_b32 s89, s89, 0x10000\nCvav 48,128,192,4\nRa 176,26,40960\nAD 84,84,81\nMvav 48,132,200\nEX 82,42\nMvav 48,136,208\nRa 180,26,41984\nCV 66,80,81\nMvav 48,140,216\nEX 83,43\n"
"Cvav 52,140,220,8\nRa 184,26,43008\nAD 84,84,82\nMvav 52,136,212\nD1 1,16,46,3072\nMvav 52,132,204\nRa 188,26,44032\nWL 8\nMvav 52,128,196\ns_add_u32 m0, s89, 0\n"
"s_add_u32 s46, s86, s88\nMvav 52,144,228\nRa 128,2,49152\nAD 84,84,83\nMvav 52,148,236\nEX 80,44\nMvav 52,152,244\nRa 132,2,50176\nCV 67,82,83\nMvav 52,156,252\nEX 81,45\n"
"Mvav 48,156,248\nRa 136,2,51200\nAD 85,85,80\nMvav 48,152,240\nD1 1,16,46,0\nv_xor_b32 v24, 0x10000, v24\nMvav 48,148,232\nRa 140,2,52224\nWL 8\nMvav 48,144,224\n"
"s_add_u32 s77, s77, s55\nSv 144,1,16,77,0\nCvav 56,160,192,12\nRa 144,2,53248\nAD 85,85,81\nMvav 56,164,200\nEX 82,46\nMvav 56,168,208\nRa 148,2,54272\nCV 70,80,81\n"
"Mvav 56,172,216\nEX 83,47\nCvav 60,172,220,16\nRa 152,2,55296\nAD 85,85,82\nMvav 60,168,212\nD1 1,16,46,1024\nv_xor_b32 v26, 0x10000, v26\nMvav 60,164,204\nRa 156,2,56320\nWL 8\n"
"Mvav 60,160,196\ns_add_u32 s88, s88, 16384\nMvav 60,176,228\nRa 160,2,57344\nAD 85,85,83\nMvav 60,180,236\nEX 80,48\nMvav 60,184,244\nSv 148,1,16,77,1024\nRa 164,2,58368\n"
"CV 71,82,83\nMvav 60,188,252\nEX 81,49\nMvav 56,188,248\nRa 168,2,59392\nv_max_f32 v90, v84, v85\nMvav 56,184,240\nD1 1,16,46,2048\nv_cmp_lt_f32 vcc, s63, v90\nMvav 56,180,232\n"
"Ra 172,2,60416\nWL 8\nMvav 56,176,224\ns_cbranch_vccnz .T69\n.T70:\nAD 86,86,84\nMaav 0,128,64\nRa 176,2,61440\nAD 84,80,81\nMaav 4,128,68\nEX 82,50\nMaav 12,132,68\n"
"Ra 180,2,62464\nCV 72,80,81\nMaav 8,132,64\nEX 83,51\nMaav 16,136,64\nRa 184,2,63488\nAD 84,84,82\nMaav 20,136,68\nD1 1,16,46,3072\ns_add_u32 m0, s89, 16384\nMaav 28,140,68\n"
"Ra 188,2,64512\nMaav 24,140,64\ns_waitcnt vmcnt(18) lgkmcnt(8)\ns_barrier\nMaav 32,144,64\nRa 128,24,0\nAD 84,84,83\nMaav 36,144,68\nEX 80,52\nMaav 44,148,68\nRa 132,24,1024\n"
"CV 73,82,83\nMaav 40,148,64\nEX 81,53\nMaav 48,152,64\nRa 136,24,2048\nAD 87,87,85\nMaav 52,152,68\ns_add_u32 s46, s87, s88\nD1 1,16,46,0\nMaav 60,156,68\nRa 140,24,3072\nWL 8\n"
"Maav 56,156,64\nSv 152,1,16,77,2048\nMaav 64,160,64\nRa 144,26,0\nAD 85,80,81\nMaav 68,160,68\nEX 82,54\nMaav 76,164,68\nRa 148,26,1024\nCV 76,80,81\nMaav 72,164,64\nEX 83,55\n"
"Maav 80,168,64\nRa 152,26,2048\nAD 85,85,82\nMaav 84,168,68\nD1 1,16,46,1024\nv_xor_b32 v2, 0x10000, v2\nMaav 92,172,68\nRa 156,26,3072\nWL 8\nMaav 88,172,64\nMaav 96,176,64\n"
"Ra 160,24,8192\nAD 85,85,83\nMaav 100,176,68\nEX 80,56\nMaav 108,180,68\nRa 164,24,9216\nCV 77,82,83\nMaav 104,180,64\nEX 81,57\nMaav 112,184,64\nRa 168,24,10240\nAD 84,84,80\n"
"Maav 116,184,68\nD1 1,16,46,2048\nMaav 124,188,68\nRa 172,24,11264\nWL 8\nMaav 120,188,64\nSv 156,1,16,77,3072\nCvav 32,128,192,4\nRa 176,26,8192\nAD 84,84,81\nMvav 32,132,200\n"
"EX 82,58\nMvav 32,136,208\nRa 180,26,9216\nCV 74,80,81\nMvav 32,140,216\nEX 83,59\nCvav 36,140,220,8\nRa 184,26,10240\nAD 84,84,82\nMvav 36,136,212\nD1 1,16,46,3072\n"
"Mvav 36,132,204\nRa 188,26,11264\nWL 8\nMvav 36,128,196\ns_add_u32 m0, s89, 32768\ns_add_u32 s46, s86, s88\nMvav 36,144,228\nRa 128,2,16384\nAD 84,84,83\nMvav 36,148,236\n"
"EX 80,60\nMvav 36,152,244\nRa 132,2,17408\nCV 75,82,83\nbuffer_load_dword v27, v1, s[16:19], s58 offen offset:4 sc0 sc1\nMvav 36,156,252\nEX 81,61\nMvav 32,156,248\n"
"Ra 136,2,18432\nAD 85,85,80\nMvav 32,152,240\nD1 1,16,46,0\nMvav 32,148,232\nRa 140,2,19456\nWL 8\nMvav 32,144,224\nSv 160,1,16,78,0\nCvav 40,160,192,12\nRa 144,2,20480\n"
"AD 85,85,81\nMvav 40,164,200\nEX 82,62\nMvav 40,168,208\nRa 148,2,21504\nCV 78,80,81\nMvav 40,172,216\nEX 83,63\nCvav 44,172,220,16\nRa 152,2,22528\nAD 85,85,82\nMvav 44,168,212\n"
"D1 1,16,46,1024\nMvav 44,164,204\nRa 156,2,23552\nWL 8\nMvav 44,160,196\ns_add_u32 s88, s88, 17408\nMvav 44,176,228\nRa 160,2,24576\nAD 85,85,83\nMvav 44,180,236\nEX 80,32\n"
"Mvav 44,184,244\nSv 164,1,16,78,1024\nRa 164,2,25600\nCV 79,82,83\nMvav 44,188,252\nEX 81,33\nMvav 40,188,248\nRa 168,2,26624\nv_max_f32 v90, v84, v85\nMvav 40,184,240\n"
"D1 1,16,46,2048\nv_cmp_lt_f32 vcc, s63, v90\nMvav 40,180,232\nRa 172,2,27648\nWL 8\nMvav 40,176,224\ns_cbranch_vccnz .T71\n.T72:\nAD 86,86,84\nMaav 0,128,72\nRa 176,2,28672\n"
"AD 84,80,81\nMaav 4,128,76\nEX 82,34\nMaav 12,132,76\nRa 180,2,29696\nCV 64,80,81\nMaav 8,132,72\nEX 83,35\nMaav 16,136,72\nRa 184,2,30720\nAD 84,84,82\nMaav 20,136,76\n"
"D1 1,16,46,3072\ns_add_u32 m0, s89, 49152\nMaav 28,140,76\nRa 188,2,31744\nMaav 24,140,72\ns_waitcnt vmcnt(19) lgkmcnt(8)\ns_barrier\nMaav 32,144,72\nRa 128,24,32768\nAD 84,84,83\n"
"Maav 36,144,76\nEX 80,36\nMaav 44,148,76\nRa 132,24,33792\nCV 65,82,83\nMaav 40,148,72\nEX 81,37\nMaav 48,152,72\nRa 136,24,34816\nAD 87,87,85\nMaav 52,152,76\n"
"s_add_u32 s46, s87, s88\nD1 1,16,46,0\nMaav 60,156,76\nRa 140,24,35840\nWL 8\nMaav 56,156,72\nSv 168,1,16,78,2048\nMaav 64,160,72\nRa 144,26,32768\nAD 85,80,81\nMaav 68,160,76\n"
"EX 82,38\nMaav 76,164,76\nRa 148,26,33792\nCV 68,80,81\nMaav 72,164,72\nEX 83,39\nMaav 80,168,72\nRa 152,26,34816\nAD 85,85,82\nMaav 84,168,76\nD1 1,16,46,1024\nMaav 92,172,76\n"
"Ra 156,26,35840\nWL 8\nMaav 88,172,72\nMaav 96,176,72\nRa 160,24,40960\nAD 85,85,83\nMaav 100,176,76\nEX 80,40\nMaav 108,180,76\nRa 164,24,41984\nCV 69,82,83\nMaav 104,180,72\n"
"EX 81,41\nMaav 112,184,72\nRa 168,24,43008\nAD 84,84,80\nMaav 116,184,76\nD1 1,16,46,2048\nMaav 124,188,76\nRa 172,24,44032\nWL 8\nMaav 120,188,72\nSv 172,1,16,78,3072\n"
"s_xor_b32 s89, s89, 0x10000\nCvav 48,128,192,4\nRa 176,26,40960\nAD 84,84,81\nMvav 48,132,200\nEX 82,42\nMvav 48,136,208\nRa 180,26,41984\nCV 66,80,81\nMvav 48,140,216\nEX 83,43\n"
"WV 11\nv_cmp_ne_u32 vcc, s15, v27\ns_and_b32 s2, s12, 3\ns_lshl_b32 s2, s2, 4\ns_nop 1\ns_lshr_b64 vcc, vcc, s2\ns_and_b64 vcc, vcc, 0xff00\ns_cbranch_vccnz .S75\n.S76:\n"
"Cvav 52,140,220,8\nRa 184,26,43008\nAD 84,84,82\nMvav 52,136,212\nD1 1,16,46,3072\nMvav 52,132,204\nRa 188,26,44032\nWL 8\nMvav 52,128,196\ns_add_u32 m0, s89, 0\n"
"s_add_u32 s46, s86, s88\nMvav 52,144,228\nRa 128,2,49152\nAD 84,84,83\nv_lshrrev_b32 v92, 8, v1\nMvav 52,148,236\nEX 80,44\nMvav 52,152,244\nRa 132,2,50176\nCV 67,82,83\n"
"Mvav 52,156,252\nEX 81,45\nMvav 48,156,248\nRa 136,2,51200\nAD 85,85,80\nMvav 48,152,240\nD1 1,16,46,0\nv_xor_b32 v24, 0x10000, v24\nMvav 48,148,232\nRa 140,2,52224\nWL 8\n"
"Mvav 48,144,224\ns_add_u32 s78, s78, s55\nSv 176,1,16,78,0\nCvav 56,160,192,12\nRa 144,2,53248\nAD 85,85,81\nv_bfe_u32 v93, v1, 6, 2\nMvav 56,164,200\nEX 82,46\nMvav 56,168,208\n"
"Ra 148,2,54272\nCV 70,80,81\nMvav 56,172,216\nEX 83,47\nCvav 60,172,220,16\nRa 152,2,55296\nAD 85,85,82\nMvav 60,168,212\nD1 1,16,46,1024\nv_xor_b32 v26, 0x10000, v26\n"
"Mvav 60,164,204\nRa 156,2,56320\nWL 8\nMvav 60,160,196\ns_add_u32 s88, s88, 16384\nv_cmp_eq_u32 vcc, v92, v93\nv_and_b32 v92, 16, v1\nv_mov_b32 v93, 0x3f80\n"
"v_cndmask_b32 v93, 0, v93, vcc\nMvav 60,176,228\nRa 160,2,57344\nAD 85,85,83\nv_lshlrev_b32 v93, v92, v93\nMvav 60,180,236\nEX 80,48\nMvav 60,184,244\nSv 180,1,16,78,1024\n"
"Ra 164,2,58368\nCV 71,82,83\nMvav 60,188,252\nEX 81,49\nMvav 56,188,248\nRa 168,2,59392\nv_max_f32 v90, v84, v85\nMvav 56,184,240\nD1 1,16,46,2048\nv_cmp_lt_f32 vcc, s63, v90\n"
"Mvav 56,180,232\nRa 172,2,60416\nWL 8\nMvav 56,176,224\ns_cbranch_vccnz .T73\n.T74:\nAD 86,86,84\nMaav 0,128,64\nRa 176,2,61440\nAD 84,80,81\nv_and_b32 v92, 32, v1\nMaav 4,128,68\n"
"EX 82,50\nMaav 12,132,68\nRa 180,2,62464\nCV 72,80,81\nMaav 8,132,64\nEX 83,51\nMaav 16,136,64\nRa 184,2,63488\nAD 84,84,82\nMaav 20,136,68\nD1 1,16,46,3072\n"
"s_add_u32 m0, s89, 16384\nMaav 28,140,68\nRa 188,2,64512\nMaav 24,140,64\nWL 8\ns_barrier\nMaav 32,144,64\nRa 128,24,0\nAD 84,84,83\nMaav 36,144,68\nEX 80,52\nMaav 44,148,68\n"
"Ra 132,24,1024\nCV 73,82,83\nMaav 40,148,64\nEX 81,53\nMaav 48,152,64\nRa 136,24,2048\nAD 87,87,85\nMaav 52,152,68\ns_add_u32 s46, s87, s88\nD1 1,16,46,0\nMaav 60,156,68\n"
"Ra 140,24,3072\nWL 8\nMaav 56,156,64\nSv 184,1,16,78,2048\nv_cmp_eq_u32 vcc, 0, v92\nv_mov_b32 v128, 0\nv_mov_b32 v129, 0\nv_cndmask_b32 v130, 0, v93, vcc\nMaav 64,160,64\n"
"Ra 144,26,0\nAD 85,80,81\nv_sub_u32 v131, v93, v130\nMaav 68,160,68\nEX 82,54\nMaav 76,164,68\nRa 148,26,1024\nCV 76,80,81\nMaav 72,164,64\nEX 83,55\nMaav 80,168,64\n"
"Ra 152,26,2048\nAD 85,85,82\nMaav 84,168,68\nD1 1,16,46,1024\nv_xor_b32 v2, 0x10000, v2\nMaav 92,172,68\nRa 156,26,3072\nWL 8\nMaav 88,172,64\nv_mov_b32 v132, 0\n"
"v_mov_b32 v133, 0\nMaav 96,176,64\nRa 160,24,8192\nAD 85,85,83\nMaav 100,176,68\nEX 80,56\nMaav 108,180,68\nRa 164,24,9216\nCV 77,82,83\nMaav 104,180,64\nEX 81,57\n"
"Maav 112,184,64\nRa 168,24,10240\nAD 84,84,80\nMaav 116,184,68\nD1 1,16,46,2048\nMaav 124,188,68\nRa 172,24,11264\nWL 8\nMaav 120,188,64\nSv 188,1,16,78,3072\ns_mov_b32 s99, 11\n"
"s_sub_u32 s90, s90, 3\n.p2align 6\n.A16:\nCvav 32,128,192,4\nRa 176,26,8192\nAD 84,84,81\nMvav 32,132,200\nEX 82,58\nMvav 32,136,208\nRa 180,26,9216\nCV 74,80,81\nMvav 32,140,216\n"
"EX 83,59\nCvav 36,140,220,8\nRa 184,26,10240\nAD 84,84,82\nMvav 36,136,212\nD1 1,16,46,3072\nMvav 36,132,204\nRa 188,26,11264\nWL 8\nMvav 36,128,196\ns_add_u32 m0, s89, 32768\n"
"s_add_u32 s46, s86, s88\nMvav 36,144,228\nRa 128,2,16384\nAD 84,84,83\nMvav 36,148,236\nEX 80,60\nMvav 36,152,244\nRa 132,2,17408\nCV 75,82,83\nMvav 36,156,252\nEX 81,61\n"
"Mvav 32,156,248\nRa 136,2,18432\nAD 85,85,80\nMvav 32,152,240\nD1 1,16,46,0\nMvav 32,148,232\nRa 140,2,19456\nWL 8\nMvav 32,144,224\ns_cmp_eq_u32 s99, 0\n"
"s_cselect_b32 s87, s95, s87\nCvav 40,160,192,12\nRa 144,2,20480\nAD 85,85,81\nMvav 40,164,200\nEX 82,62\nMvav 40,168,208\nRa 148,2,21504\nCV 78,80,81\nMvav 40,172,216\nEX 83,63\n"
"Cvav 44,172,220,16\nRa 152,2,22528\nAD 85,85,82\nMvav 44,168,212\nD1 1,16,46,1024\nMvav 44,164,204\nRa 156,2,23552\nWL 8\nMvav 44,160,196\ns_add_u32 s88, s88, 17408\n"
"Mvav 44,176,228\nRa 160,2,24576\nAD 85,85,83\nMvav 44,180,236\nEX 80,32\nMvav 44,184,244\nRa 164,2,25600\nCV 79,82,83\nMvav 44,188,252\nEX 81,33\nMvav 40,188,248\nRa 168,2,26624\n"
"v_max_f32 v90, v84, v85\nMvav 40,184,240\nD1 1,16,46,2048\nv_cmp_lt_f32 vcc, s63, v90\nMvav 40,180,232\nRa 172,2,27648\nWL 8\nMvav 40,176,224\ns_cbranch_vccnz .T17\n.T18:\n"
"AD 86,86,84\nMaav 0,128,72\nRa 176,2,28672\nAD 84,80,81\nMaav 4,128,76\nEX 82,34\nMaav 12,132,76\nRa 180,2,29696\nCV 64,80,81\nMaav 8,132,72\nEX 83,35\nMaav 16,136,72\n"
"Ra 184,2,30720\nAD 84,84,82\nMaav 20,136,76\nD1 1,16,46,3072\ns_add_u32 m0, s89, 49152\nMaav 28,140,76\nRa 188,2,31744\nMaav 24,140,72\ns_waitcnt vmcnt(12) lgkmcnt(8)\ns_barrier\n"
"Maav 32,144,72\nRa 128,24,32768\nAD 84,84,83\nMaav 36,144,76\nEX 80,36\nMaav 44,148,76\nRa 132,24,33792\nCV 65,82,83\nMaav 40,148,72\nEX 81,37\nMaav 48,152,72\nRa 136,24,34816\n"
"AD 87,87,85\nMaav 52,152,76\ns_add_u32 s46, s87, s88\nD1 1,16,46,0\nMaav 60,156,76\nRa 140,24,35840\nWL 8\nMaav 56,156,72\ns_cmp_eq_u32 s99, 1\ns_cselect_b32 s86, s94, s86\n"
"Maav 64,160,72\nRa 144,26,32768\nAD 85,80,81\nMaav 68,160,76\nEX 82,38\nMaav 76,164,76\nRa 148,26,33792\nCV 68,80,81\nMaav 72,164,72\nEX 83,39\nMaav 80,168,72\nRa 152,26,34816\n"
"AD 85,85,82\nMaav 84,168,76\nD1 1,16,46,1024\nMaav 92,172,76\nRa 156,26,35840\nWL 8\nMaav 88,172,72\ns_cmp_eq_u32 s99, s93\ns_cbranch_scc1 .E19\n.E20:\ns_nop 2\nMaav 96,176,72\n"
"Ra 160,24,40960\nAD 85,85,83\nMaav 100,176,76\nEX 80,40\nMaav 108,180,76\nRa 164,24,41984\nCV 69,82,83\nMaav 104,180,72\nEX 81,41\nMaav 112,184,72\nRa 168,24,43008\nAD 84,84,80\n"
"Maav 116,184,76\nD1 1,16,46,2048\nMaav 124,188,76\nRa 172,24,44032\nWL 8\nMaav 120,188,72\ns_xor_b32 s89, s89, 0x10000\nCvav 48,128,192,4\nRa 176,26,40960\nAD 84,84,81\n"
"Mvav 48,132,200\nEX 82,42\nMvav 48,136,208\nRa 180,26,41984\nCV 66,80,81\nMvav 48,140,216\nEX 83,43\nCvav 52,140,220,8\nRa 184,26,43008\nAD 84,84,82\nMvav 52,136,212\n"
"D1 1,16,46,3072\nMvav 52,132,204\nRa 188,26,44032\nWL 8\nMvav 52,128,196\ns_add_u32 m0, s89, 0\ns_add_u32 s46, s86, s88\nMvav 52,144,228\nRa 128,2,49152\nAD 84,84,83\n"
"Mvav 52,148,236\nEX 80,44\nMvav 52,152,244\nRa 132,2,50176\nCV 67,82,83\nMvav 52,156,252\nEX 81,45\nMvav 48,156,248\nRa 136,2,51200\nAD 85,85,80\nMvav 48,152,240\nD1 1,16,46,0\n"
"v_xor_b32 v24, 0x10000, v24\nMvav 48,148,232\nRa 140,2,52224\nWL 8\nMvav 48,144,224\nCvav 56,160,192,12\nRa 144,2,53248\nAD 85,85,81\nMvav 56,164,200\nEX 82,46\nMvav 56,168,208\n"
"Ra 148,2,54272\nCV 70,80,81\nMvav 56,172,216\nEX 83,47\nCvav 60,172,220,16\nRa 152,2,55296\nAD 85,85,82\nMvav 60,168,212\nD1 1,16,46,1024\nv_xor_b32 v26, 0x10000, v26\n"
"Mvav 60,164,204\nRa 156,2,56320\nWL 8\nMvav 60,160,196\ns_add_u32 s88, s88, 16384\nMvav 60,176,228\nRa 160,2,57344\nAD 85,85,83\nMvav 60,180,236\nEX 80,48\nMvav 60,184,244\n"
"Ra 164,2,58368\nCV 71,82,83\nMvav 60,188,252\nEX 81,49\nMvav 56,188,248\nRa 168,2,59392\nv_max_f32 v90, v84, v85\nMvav 56,184,240\nD1 1,16,46,2048\nv_cmp_lt_f32 vcc, s63, v90\n"
"Mvav 56,180,232\nRa 172,2,60416\nWL 8\nMvav 56,176,224\ns_cbranch_vccnz .T21\n.T22:\nAD 86,86,84\nMaav 0,128,64\nRa 176,2,61440\nAD 84,80,81\nMaav 4,128,68\nEX 82,50\n"
"Maav 12,132,68\nRa 180,2,62464\nCV 72,80,81\nMaav 8,132,64\nEX 83,51\nMaav 16,136,64\nRa 184,2,63488\nAD 84,84,82\nMaav 20,136,68\nD1 1,16,46,3072\ns_add_u32 m0, s89, 16384\n"
"Maav 28,140,68\nRa 188,2,64512\nMaav 24,140,64\ns_waitcnt vmcnt(12) lgkmcnt(8)\ns_barrier\nMaav 32,144,64\nRa 128,24,0\nAD 84,84,83\nMaav 36,144,68\nEX 80,52\nMaav 44,148,68\n"
"Ra 132,24,1024\nCV 73,82,83\nMaav 40,148,64\nEX 81,53\nMaav 48,152,64\nRa 136,24,2048\nAD 87,87,85\nMaav 52,152,68\ns_add_u32 s46, s87, s88\nD1 1,16,46,0\nMaav 60,156,68\n"
"Ra 140,24,3072\nWL 8\nMaav 56,156,64\ns_sub_u32 s90, s90, 1\ns_cbranch_scc1 .E23\n.E24:\nMaav 64,160,64\nRa 144,26,0\nAD 85,80,81\nMaav 68,160,68\nEX 82,54\nMaav 76,164,68\n"
"Ra 148,26,1024\nCV 76,80,81\nMaav 72,164,64\nEX 83,55\nMaav 80,168,64\nRa 152,26,2048\nAD 85,85,82\nMaav 84,168,68\nD1 1,16,46,1024\nv_xor_b32 v2, 0x10000, v2\nMaav 92,172,68\n"
"Ra 156,26,3072\nWL 8\nMaav 88,172,64\ns_cmp_eq_u32 s99, s92\ns_cbranch_scc1 .E25\n.E26:\ns_nop 2\nMaav 96,176,64\nRa 160,24,8192\nAD 85,85,83\nMaav 100,176,68\nEX 80,56\n"
"Maav 108,180,68\nRa 164,24,9216\nCV 77,82,83\nMaav 104,180,64\nEX 81,57\nMaav 112,184,64\nRa 168,24,10240\nAD 84,84,80\nMaav 116,184,68\nD1 1,16,46,2048\nMaav 124,188,68\n"
"Ra 172,24,11264\nWL 8\nMaav 120,188,64\ns_sub_u32 s99, s99, 1\ns_cbranch_scc0 .A16\ns_cmp_lg_u32 s14, 0\ns_cbranch_scc1 .SW8\ns_branch .X081\n.T17:\ns_mov_b32 s62, 0\n"
"s_call_b64 s[80:81], .RB13\ns_branch .T18\n.E19:\nv_mov_b32 v12, s97\nv_mov_b32 v13, s97\nv_mov_b32 v14, s97\nv_mov_b32 v15, s97\nv_cmpx_gt_i32 vcc, 3, v0\nv_mov_b32 v7, s97\n"
"v_mov_b32 v19, s97\nv_cmpx_gt_i32 vcc, 2, v0\nv_mov_b32 v6, s97\nv_mov_b32 v18, s97\nv_cmpx_gt_i32 vcc, 1, v0\nv_mov_b32 v5, s97\nv_mov_b32 v17, s97\nv_cmpx_gt_i32 vcc, 0, v0\n"
"v_mov_b32 v4, s97\nv_mov_b32 v16, s97\nv_cmpx_gt_i32 vcc, -16, v0\nv_mov_b32 v8, s97\nv_mov_b32 v9, s97\nv_mov_b32 v10, s97\nv_mov_b32 v11, s97\ns_mov_b64 exec, -1\ns_branch .E20\n"
".T21:\ns_mov_b32 s62, 0\ns_call_b64 s[80:81], .RA12\ns_branch .T22\n.E23:\ns_swappc_b64 s[80:81], s[8:9]\ns_branch .E24\n.E25:\nv_mov_b32 v12, s97\nv_mov_b32 v13, s97\n"
"v_mov_b32 v14, s97\nv_mov_b32 v15, s97\nv_cmpx_gt_i32 vcc, 3, v3\nv_mov_b32 v7, s97\nv_mov_b32 v19, s97\nv_cmpx_gt_i32 vcc, 2, v3\nv_mov_b32 v6, s97\nv_mov_b32 v18, s97\n"
"v_cmpx_gt_i32 vcc, 1, v3\nv_mov_b32 v5, s97\nv_mov_b32 v17, s97\nv_cmpx_gt_i32 vcc, 0, v3\nv_mov_b32 v4, s97\nv_mov_b32 v16, s97\nv_cmpx_gt_i32 vcc, -16, v3\nv_mov_b32 v8, s97\n"
"v_mov_b32 v9, s97\nv_mov_b32 v10, s97\nv_mov_b32 v11, s97\ns_mov_b64 exec, -1\ns_branch .E26\n.HD11:\nv_cmp_ne_u32 vcc, s15, v27\ns_and_b32 s2, s12, 3\ns_lshl_b32 s2, s2, 4\n"
"s_nop 1\ns_lshr_b64 vcc, vcc, s2\ns_and_b64 vcc, vcc, 0xff00\ns_cbranch_vccnz .S83\n.S84:\ns_mov_b32 s90, 2\ns_swappc_b64 s[8:9], s[80:81]\ns_branch .HJ82\n.HD10:\n"
"buffer_store_dword v25, off, s[16:19], s54 offset:4 sc0 sc1\ns_mov_b32 s90, 0\ns_swappc_b64 s[8:9], s[80:81]\n.HJ82:\n"
"buffer_load_dword v27, v1, s[16:19], s58 offen offset:4 sc0 sc1\ns_mov_b32 s90, 0\ns_swappc_b64 s[8:9], s[80:81]\nv_cmp_ne_u32 vcc, s15, v27\ns_and_b32 s2, s12, 2\n"
"s_lshl_b32 s2, s2, 4\ns_nop 1\ns_lshr_b64 vcc, vcc, s2\ns_mov_b32 vcc_hi, 0\ns_cbranch_vccnz .S86\n.S87:\ns_mov_b32 s90, s91\ns_swappc_b64 s[8:9], s[80:81]\n"
"s_mov_b32 s6, 0x5c48400\nbuffer_load_dword v96, v1, s[16:19], s6 offen offset:4 sc0 sc1\nbuffer_load_dword v97, v1, s[16:19], s6 offen offset:1028 sc0 sc1\n"
"buffer_load_dword v98, v1, s[16:19], s6 offen offset:2052 sc0 sc1\nbuffer_load_dword v99, v1, s[16:19], s6 offen offset:3076 sc0 sc1\ns_mov_b32 s90, 0\n"
"s_swappc_b64 s[8:9], s[80:81]\ns_mov_b32 s6, 0x5c48400\nv_xor_b32 v100, s15, v96\nv_xor_b32 v101, s15, v97\nv_xor_b32 v102, s15, v98\nv_xor_b32 v103, s15, v99\n"
"v_or3_b32 v100, v100, v101, v102\nv_or_b32 v100, v100, v103\nv_cmp_ne_u32 vcc, 0, v100\ns_nop 0\ns_cbranch_vccnz .S89\n.S90:\ns_lshr_b32 s4, s13, 1\ns_lshl2_add_u32 s4, s12, s4\n"
"s_lshr_b32 s5, s71, 16\ns_cmp_lt_u32 s4, 28\ns_cselect_b32 s5, s71, s5\ns_cselect_b32 s3, 0, 28\ns_and_b32 s5, s5, 0x7ff\ns_sub_u32 s3, s4, s3\ns_mul_i32 s2, s4, 7168\n"
"s_add_u32 s2, s2, 0x5c4c800\ns_add_u32 s6, s2, 4096\nbuffer_load_dwordx4 v[96:99], v1, s[16:19], s2 offen offset:0 sc0 sc1\n"
"buffer_load_dwordx4 v[100:103], v1, s[16:19], s2 offen offset:1024 sc0 sc1\nbuffer_load_dwordx4 v[104:107], v1, s[16:19], s2 offen offset:2048 sc0 sc1\n"
"buffer_load_dwordx4 v[108:111], v1, s[16:19], s2 offen offset:3072 sc0 sc1\nbuffer_load_dwordx4 v[112:115], v1, s[16:19], s6 offen offset:0 sc0 sc1\n"
"buffer_load_dwordx4 v[116:119], v1, s[16:19], s6 offen offset:1024 sc0 sc1\nbuffer_load_dwordx4 v[120:123], v1, s[16:19], s6 offen offset:2048 sc0 sc1\nv_lshrrev_b32 v127, 1, v1\n"
"v_and_b32 v127, 24, v127\ns_mul_i32 s2, s5, 896\ns_lshl_b32 s3, s3, 5\ns_add_u32 s2, s2, s3\ns_lshl2_add_u32 s5, s5, 0x5c42000\n"
"buffer_load_dwordx2 v[124:125], v127, s[28:31], s2 offen\nbuffer_load_dword v126, off, s[16:19], s5 sc0 sc1\ns_mov_b32 s90, 0\ns_swappc_b64 s[8:9], s[80:81]\nAD 96,96,100\n"
"AD 97,97,101\nAD 98,98,102\nAD 99,99,103\nAD 104,104,108\nAD 105,105,109\nAD 106,106,110\nAD 107,107,111\nAD 112,112,116\nAD 113,113,117\nAD 114,114,118\nAD 115,115,119\n"
"AD 96,96,104\nAD 97,97,105\nAD 98,98,106\nAD 99,99,107\nAD 112,112,120\nAD 113,113,121\nAD 114,114,122\nAD 115,115,123\nAD 96,96,112\nAD 97,97,113\nAD 98,98,114\nAD 99,99,115\n"
"v_mov_b32 v100, v96\nv_mov_b32 v101, v97\nv_mov_b32 v102, v98\nv_mov_b32 v103, v99\nv_permlane32_swap_b32 v96, v100\nv_permlane32_swap_b32 v97, v101\n"
"v_permlane32_swap_b32 v98, v102\nv_permlane32_swap_b32 v99, v103\nAD 96,96,100\nAD 97,97,101\nAD 98,98,102\nAD 99,99,103\nv_mov_b32 v100, v96\nv_mov_b32 v101, v97\n"
"v_mov_b32 v102, v98\nv_mov_b32 v103, v99\nv_permlane16_swap_b32 v96, v100\nv_permlane16_swap_b32 v97, v101\nv_permlane16_swap_b32 v98, v102\nv_permlane16_swap_b32 v99, v103\n"
"AD 96,96,100\nAD 97,97,101\nAD 98,98,102\nAD 99,99,103\nv_add_f32_dpp v96, v96, v96 row_ror:8 row_mask:0xf bank_mask:0xf\n"
"v_add_f32_dpp v97, v97, v97 row_ror:8 row_mask:0xf bank_mask:0xf\nv_add_f32_dpp v98, v98, v98 row_ror:8 row_mask:0xf bank_mask:0xf\n"
"v_add_f32_dpp v99, v99, v99 row_ror:8 row_mask:0xf bank_mask:0xf\nv_add_f32_dpp v96, v96, v96 row_ror:4 row_mask:0xf bank_mask:0xf\n"
"v_add_f32_dpp v97, v97, v97 row_ror:4 row_mask:0xf bank_mask:0xf\nv_add_f32_dpp v98, v98, v98 row_ror:4 row_mask:0xf bank_mask:0xf\n"
"v_add_f32_dpp v99, v99, v99 row_ror:4 row_mask:0xf bank_mask:0xf\nv_mul_f32 v126, 0xbfb8aa3b, v126\nEX 126,126\nCV 96,96,97\nv_add_f32 v126, 1.0, v126\nv_rcp_f32 v126, v126\n"
"CV 98,98,99\nCV 126,126,126\nv_lshlrev_b32 v126, 16, v126\nv_and_b32 v97, 0xffff0000, v96\nv_lshlrev_b32 v96, 16, v96\nv_and_b32 v99, 0xffff0000, v98\nv_lshlrev_b32 v98, 16, v98\n"
"MU 96,96,126\nMU 97,97,126\nMU 98,98,126\nMU 99,99,126\nCV 96,96,97\nCV 98,98,99\nv_and_b32 v97, 0xffff0000, v96\nv_lshlrev_b32 v96, 16, v96\nv_and_b32 v99, 0xffff0000, v98\n"
"v_lshlrev_b32 v98, 16, v98\nv_lshlrev_b32 v100, 16, v124\nv_and_b32 v101, 0xffff0000, v124\nv_lshlrev_b32 v102, 16, v125\nv_and_b32 v103, 0xffff0000, v125\nAD 96,96,100\n"
"AD 97,97,101\nAD 98,98,102\nAD 99,99,103\nCV 96,96,97\nCV 97,98,99\nv_mov_b32 v98, s15\nv_mov_b32 v99, s71\ns_lshr_b32 s4, s13, 1\ns_lshl2_add_u32 s4, s12, s4\n"
"s_lshr_b32 s5, s71, 16\ns_cmp_lt_u32 s4, 28\ns_cselect_b32 s5, s71, s5\ns_cselect_b32 s3, 0, 28\ns_and_b32 s5, s5, 0x7ff\ns_sub_u32 s3, s4, s3\nv_mov_b32 v100, 0x40000000\n"
"v_cmp_gt_u32 vcc, 64, v1\ns_lshl_b32 s4, s4, 6\ns_add_u32 s4, s4, 0x5c4b800\nv_cndmask_b32 v100, v100, v1, vcc\nSv 96,100,16,4,0\ns_mov_b32 s90, 100\n"
"s_swappc_b64 s[8:9], s[80:81]\n.S83:\ns_mov_b32 s7, 262144\n.S85:\ns_sleep 1\nbuffer_load_dword v27, v1, s[16:19], s58 offen offset:4 sc0 sc1\nWV 0\nv_cmp_ne_u32 vcc, s15, v27\n"
"s_and_b32 s2, s12, 3\ns_lshl_b32 s2, s2, 4\ns_nop 1\ns_lshr_b64 vcc, vcc, s2\ns_and_b64 vcc, vcc, 0xff00\ns_cbranch_vccz .S84\ns_sub_u32 s7, s7, 1\ns_cbranch_scc0 .S85\n"
"s_branch .S84\n.S86:\ns_mov_b32 s7, 262144\n.S88:\ns_sleep 1\nbuffer_load_dword v27, v1, s[16:19], s58 offen offset:4 sc0 sc1\nWV 0\nv_cmp_ne_u32 vcc, s15, v27\n"
"s_and_b32 s2, s12, 2\ns_lshl_b32 s2, s2, 4\ns_nop 1\ns_lshr_b64 vcc, vcc, s2\ns_mov_b32 vcc_hi, 0\ns_cbranch_vccz .S87\ns_sub_u32 s7, s7, 1\ns_cbranch_scc0 .S88\ns_branch .S87\n"
".S89:\ns_mov_b32 s7, 262144\n.S91:\ns_sleep 1\nbuffer_load_dword v96, v1, s[16:19], s6 offen offset:4 sc0 sc1\nbuffer_load_dword v97, v1, s[16:19], s6 offen offset:1028 sc0 sc1\n"
"buffer_load_dword v98, v1, s[16:19], s6 offen offset:2052 sc0 sc1\nbuffer_load_dword v99, v1, s[16:19], s6 offen offset:3076 sc0 sc1\nWV 0\nv_xor_b32 v100, s15, v96\n"
"v_xor_b32 v101, s15, v97\nv_xor_b32 v102, s15, v98\nv_xor_b32 v103, s15, v99\nv_or3_b32 v100, v100, v101, v102\nv_or_b32 v100, v100, v103\nv_cmp_ne_u32 vcc, 0, v100\ns_nop 0\n"
"s_cbranch_vccz .S90\ns_sub_u32 s7, s7, 1\ns_cbranch_scc0 .S91\ns_branch .S90\n.X081:\nCvav 32,128,192,4\nRa 176,26,8192\nAD 84,84,81\nMvav 32,132,200\nEX 82,58\nMvav 32,136,208\n"
"Ra 180,26,9216\nCV 74,80,81\nMvav 32,140,216\nEX 83,59\nCvav 36,140,220,8\nRa 184,26,10240\nAD 84,84,82\nMvav 36,136,212\nD1 1,16,46,3072\nMvav 36,132,204\nRa 188,26,11264\nWL 8\n"
"Mvav 36,128,196\ns_add_u32 m0, s89, 32768\ns_add_u32 s46, s86, s88\ns_mov_b32 s94, s96\ns_add_u32 s95, s96, 33792\nv_and_b32 v108, 0xf0, v1\nMvav 36,144,228\nRa 128,2,16384\n"
"AD 84,84,83\nMvav 36,148,236\nEX 80,60\nMvav 36,152,244\nRa 132,2,17408\nCV 75,82,83\nMvav 36,156,252\nEX 81,61\nMvav 32,156,248\nRa 136,2,18432\nAD 85,85,80\nMvav 32,152,240\n"
"D1 1,16,46,0\nMvav 32,148,232\nRa 140,2,19456\nWL 8\nMvav 32,144,224\nbuffer_load_dwordx4 v[100:103], v108, s[16:19], s65 offen offset:0 sc0 sc1\n"
"buffer_load_dwordx4 v[104:107], v108, s[16:19], s65 offen offset:256 sc0 sc1\nCvav 40,160,192,12\nRa 144,2,20480\nAD 85,85,81\nMvav 40,164,200\nEX 82,62\nMvav 40,168,208\n"
"Ra 148,2,21504\nCV 78,80,81\nMvav 40,172,216\nEX 83,63\nCvav 44,172,220,16\nRa 152,2,22528\nAD 85,85,82\nMvav 44,168,212\nD1 1,16,46,1024\nMvav 44,164,204\nRa 156,2,23552\nWL 8\n"
"Mvav 44,160,196\ns_add_u32 s88, s88, 17408\nMvav 44,176,228\nRa 160,2,24576\nAD 85,85,83\nMvav 44,180,236\nEX 80,32\nMvav 44,184,244\nRa 164,2,25600\nCV 79,82,83\nMvav 44,188,252\n"
"EX 81,33\nMvav 40,188,248\nRa 168,2,26624\nv_max_f32 v90, v84, v85\nMvav 40,184,240\nD1 1,16,46,2048\nv_cmp_lt_f32 vcc, s63, v90\nMvav 40,180,232\nRa 172,2,27648\nWL 8\n"
"Mvav 40,176,224\ns_cbranch_vccnz .T92\n.T93:\nAD 86,86,84\nMaav 0,128,72\nRa 176,2,28672\nAD 84,80,81\nMaav 4,128,76\nEX 82,34\nMaav 12,132,76\nRa 180,2,29696\nCV 64,80,81\n"
"Maav 8,132,72\nEX 83,35\nMaav 16,136,72\nRa 184,2,30720\nAD 84,84,82\nMaav 20,136,76\nD1 1,16,46,3072\ns_add_u32 m0, s89, 49152\nMaav 28,140,76\nRa 188,2,31744\nMaav 24,140,72\n"
"s_waitcnt vmcnt(14) lgkmcnt(8)\ns_barrier\nMaav 32,144,72\nRa 128,24,32768\nAD 84,84,83\nMaav 36,144,76\nEX 80,36\nMaav 44,148,76\nRa 132,24,33792\nCV 65,82,83\nMaav 40,148,72\n"
"EX 81,37\nMaav 48,152,72\nRa 136,24,34816\nAD 87,87,85\nMaav 52,152,76\ns_add_u32 s46, s87, s88\nD1 1,16,46,0\nMaav 60,156,76\nRa 140,24,35840\nWL 8\nMaav 56,156,72\n"
"s_mov_b32 s86, s94\nMaav 64,160,72\nRa 144,26,32768\nAD 85,80,81\nMaav 68,160,76\nEX 82,38\nMaav 76,164,76\nRa 148,26,33792\nCV 68,80,81\nMaav 72,164,72\nEX 83,39\nMaav 80,168,72\n"
"Ra 152,26,34816\nAD 85,85,82\nMaav 84,168,76\nD1 1,16,46,1024\nMaav 92,172,76\nRa 156,26,35840\nWL 8\nMaav 88,172,72\nMaav 96,176,72\nRa 160,24,40960\nAD 85,85,83\n"
"Maav 100,176,76\nEX 80,40\nMaav 108,180,76\nRa 164,24,41984\nCV 69,82,83\nMaav 104,180,72\nEX 81,41\nMaav 112,184,72\nRa 168,24,43008\nAD 84,84,80\nMaav 116,184,76\n"
"D1 1,16,46,2048\nMaav 124,188,76\nRa 172,24,44032\nWL 8\nMaav 120,188,72\ns_xor_b32 s89, s89, 0x10000\nCvav 48,128,192,4\nRa 176,26,40960\nAD 84,84,81\nMvav 48,132,200\nEX 82,42\n"
"Mvav 48,136,208\nRa 180,26,41984\nCV 66,80,81\nMvav 48,140,216\nEX 83,43\nCvav 52,140,220,8\nRa 184,26,43008\nAD 84,84,82\nMvav 52,136,212\nD1 1,16,46,3072\nMvav 52,132,204\n"
"Ra 188,26,44032\nWL 8\nMvav 52,128,196\ns_add_u32 m0, s89, 0\ns_add_u32 s46, s86, s88\nMvav 52,144,228\nRa 128,2,49152\nAD 84,84,83\nMvav 52,148,236\nEX 80,44\nMvav 52,152,244\n"
"Ra 132,2,50176\nCV 67,82,83\nMvav 52,156,252\nEX 81,45\nMvav 48,156,248\nRa 136,2,51200\nAD 85,85,80\nMvav 48,152,240\nD1 1,16,46,0\nv_xor_b32 v24, 0x10000, v24\nMvav 48,148,232\n"
"Ra 140,2,52224\nWL 8\nMvav 48,144,224\nWV 8\nv_xor_b32 v109, s15, v102\nv_xor_b32 v110, s15, v106\nv_or_b32 v109, v109, v110\nv_cmp_ne_u32 vcc, 0, v109\ns_nop 3\n"
"s_cmp_lg_u64 vcc, 0\ns_cselect_b32 s67, 1, 0\ns_cselect_b32 s6, 0x40000000, s66\nCvav 56,160,192,12\nRa 144,2,53248\nAD 85,85,81\ns_add_u32 s5, s6, 0\nMvav 56,164,200\nEX 82,46\n"
"Mvav 56,168,208\nRa 148,2,54272\nCV 70,80,81\nbuffer_load_dwordx4 a[192:195], v1, s[16:19], s5 offen offset:0 sc1\nMvav 56,172,216\nEX 83,47\nCvav 60,172,220,16\nRa 152,2,55296\n"
"AD 85,85,82\nMvav 60,168,212\nD1 1,16,46,1024\nv_xor_b32 v26, 0x10000, v26\nMvav 60,164,204\nRa 156,2,56320\nWL 8\nMvav 60,160,196\ns_add_u32 s88, s88, 16384\nMvav 60,176,228\n"
"Ra 160,2,57344\nAD 85,85,83\nMvav 60,180,236\nEX 80,48\nMvav 60,184,244\nRa 164,2,58368\nCV 71,82,83\nbuffer_load_dwordx4 a[196:199], v1, s[16:19], s5 offen offset:1024 sc1\n"
"Mvav 60,188,252\nEX 81,49\nMvav 56,188,248\nRa 168,2,59392\nv_max_f32 v90, v84, v85\nMvav 56,184,240\nD1 1,16,46,2048\nv_cmp_lt_f32 vcc, s63, v90\nMvav 56,180,232\nRa 172,2,60416\n"
"WL 8\nMvav 56,176,224\ns_cbranch_vccnz .T94\n.T95:\nAD 86,86,84\nMaav 0,128,64\nRa 176,2,61440\nAD 84,80,81\nMaav 4,128,68\nEX 82,50\nMaav 12,132,68\nRa 180,2,62464\nCV 72,80,81\n"
"buffer_load_dwordx4 a[200:203], v1, s[16:19], s5 offen offset:2048 sc1\nMaav 8,132,64\nEX 83,51\nMaav 16,136,64\nRa 184,2,63488\nAD 84,84,82\nMaav 20,136,68\nD1 1,16,46,3072\n"
"s_add_u32 m0, s89, 16384\nMaav 28,140,68\nRa 188,2,64512\nMaav 24,140,64\nWL 8\ns_barrier\nMaav 32,144,64\nRa 128,24,0\nAD 84,84,83\nMaav 36,144,68\nEX 80,52\nMaav 44,148,68\n"
"Ra 132,24,1024\nCV 73,82,83\nbuffer_load_dwordx4 a[204:207], v1, s[16:19], s5 offen offset:3072 sc1\nMaav 40,148,64\nEX 81,53\nMaav 48,152,64\nRa 136,24,2048\nAD 87,87,85\n"
"Maav 52,152,68\ns_add_u32 s46, s87, s88\nD1 1,16,46,0\nMaav 60,156,68\nRa 140,24,3072\nWL 8\nMaav 56,156,64\nMaav 64,160,64\nRa 144,26,0\nAD 85,80,81\ns_add_u32 s5, s6, 4096\n"
"Maav 68,160,68\nEX 82,54\nMaav 76,164,68\nRa 148,26,1024\nCV 76,80,81\nbuffer_load_dwordx4 a[208:211], v1, s[16:19], s5 offen offset:0 sc1\nMaav 72,164,64\nEX 83,55\n"
"Maav 80,168,64\nRa 152,26,2048\nAD 85,85,82\nMaav 84,168,68\nD1 1,16,46,1024\nv_xor_b32 v2, 0x10000, v2\nMaav 92,172,68\nRa 156,26,3072\nWL 8\nMaav 88,172,64\n"
"s_cmp_eq_u32 s99, s92\ns_cbranch_scc1 .E96\n.E97:\ns_nop 2\nMaav 96,176,64\nRa 160,24,8192\nAD 85,85,83\nMaav 100,176,68\nEX 80,56\nMaav 108,180,68\nRa 164,24,9216\nCV 77,82,83\n"
"buffer_load_dwordx4 a[212:215], v1, s[16:19], s5 offen offset:1024 sc1\nMaav 104,180,64\nEX 81,57\nMaav 112,184,64\nRa 168,24,10240\nAD 84,84,80\nMaav 116,184,68\nD1 1,16,46,2048\n"
"Maav 124,188,68\nRa 172,24,11264\nWL 8\nMaav 120,188,64\ns_sub_u32 s99, s99, 1\nCvav 32,128,192,4\nRa 176,26,8192\nAD 84,84,81\nMvav 32,132,200\nEX 82,58\nMvav 32,136,208\n"
"Ra 180,26,9216\nCV 74,80,81\nbuffer_load_dwordx4 a[216:219], v1, s[16:19], s5 offen offset:2048 sc1\nMvav 32,140,216\nEX 83,59\nCvav 36,140,220,8\nRa 184,26,10240\nAD 84,84,82\n"
"Mvav 36,136,212\nD1 1,16,46,3072\nMvav 36,132,204\nRa 188,26,11264\nWL 8\nMvav 36,128,196\ns_add_u32 m0, s89, 32768\ns_add_u32 s46, s86, s88\nMvav 36,144,228\nRa 128,2,16384\n"
"AD 84,84,83\nMvav 36,148,236\nEX 80,60\nMvav 36,152,244\nRa 132,2,17408\nCV 75,82,83\nbuffer_load_dwordx4 a[220:223], v1, s[16:19], s5 offen offset:3072 sc1\nMvav 36,156,252\n"
"EX 81,61\nMvav 32,156,248\nRa 136,2,18432\nAD 85,85,80\nMvav 32,152,240\nD1 1,16,46,0\nMvav 32,148,232\nRa 140,2,19456\nWL 8\nMvav 32,144,224\ns_mov_b32 s87, s95\n"
"Cvav 40,160,192,12\nRa 144,2,20480\nAD 85,85,81\ns_add_u32 s5, s6, 8192\nMvav 40,164,200\nEX 82,62\nMvav 40,168,208\nRa 148,2,21504\nCV 78,80,81\n"
"buffer_load_dwordx4 a[224:227], v1, s[16:19], s5 offen offset:0 sc1\nMvav 40,172,216\nEX 83,63\nCvav 44,172,220,16\nRa 152,2,22528\nAD 85,85,82\nMvav 44,168,212\nD1 1,16,46,1024\n"
"Mvav 44,164,204\nRa 156,2,23552\nWL 8\nMvav 44,160,196\ns_add_u32 s88, s88, 17408\nMvav 44,176,228\nRa 160,2,24576\nAD 85,85,83\nMvav 44,180,236\nEX 80,32\nMvav 44,184,244\n"
"Ra 164,2,25600\nCV 79,82,83\nbuffer_load_dwordx4 a[228:231], v1, s[16:19], s5 offen offset:1024 sc1\nMvav 44,188,252\nEX 81,33\nMvav 40,188,248\nRa 168,2,26624\n"
"v_max_f32 v90, v84, v85\nMvav 40,184,240\nD1 1,16,46,2048\nv_cmp_lt_f32 vcc, s63, v90\nMvav 40,180,232\nRa 172,2,27648\nWL 8\nMvav 40,176,224\ns_cbranch_vccnz .T98\n.T99:\n"
"AD 86,86,84\nMaav 0,128,72\nRa 176,2,28672\nAD 84,80,81\nMaav 4,128,76\nEX 82,34\nMaav 12,132,76\nRa 180,2,29696\nCV 64,80,81\n"
"buffer_load_dwordx4 a[232:235], v1, s[16:19], s5 offen offset:2048 sc1\nMaav 8,132,72\nEX 83,35\nMaav 16,136,72\nRa 184,2,30720\nAD 84,84,82\nMaav 20,136,76\nD1 1,16,46,3072\n"
"s_add_u32 m0, s89, 49152\nMaav 28,140,76\nRa 188,2,31744\nMaav 24,140,72\ns_waitcnt vmcnt(23) lgkmcnt(8)\ns_barrier\nMaav 32,144,72\nRa 128,24,32768\nAD 84,84,83\nMaav 36,144,76\n"
"EX 80,36\nMaav 44,148,76\nRa 132,24,33792\nCV 65,82,83\nbuffer_load_dwordx4 a[236:239], v1, s[16:19], s5 offen offset:3072 sc1\nMaav 40,148,72\nEX 81,37\nMaav 48,152,72\n"
"Ra 136,24,34816\nAD 87,87,85\nMaav 52,152,76\ns_add_u32 s46, s87, s88\nD1 1,16,46,0\nMaav 60,156,76\nRa 140,24,35840\nWL 8\nMaav 56,156,72\nMaav 64,160,72\nRa 144,26,32768\n"
"AD 85,80,81\ns_add_u32 s5, s6, 12288\nMaav 68,160,76\nEX 82,38\nMaav 76,164,76\nRa 148,26,33792\nCV 68,80,81\nbuffer_load_dwordx4 a[240:243], v1, s[16:19], s5 offen offset:0 sc1\n"
"Maav 72,164,72\nEX 83,39\nMaav 80,168,72\nRa 152,26,34816\nAD 85,85,82\nMaav 84,168,76\nD1 1,16,46,1024\nMaav 92,172,76\nRa 156,26,35840\nWL 8\nMaav 88,172,72\n"
"s_cmp_eq_u32 s99, s93\ns_cbranch_scc1 .E100\n.E101:\ns_nop 2\nMaav 96,176,72\nRa 160,24,40960\nAD 85,85,83\nMaav 100,176,76\nEX 80,40\nMaav 108,180,76\nRa 164,24,41984\n"
"CV 69,82,83\nbuffer_load_dwordx4 a[244:247], v1, s[16:19], s5 offen offset:1024 sc1\nMaav 104,180,72\nEX 81,41\nMaav 112,184,72\nRa 168,24,43008\nAD 84,84,80\nMaav 116,184,76\n"
"D1 1,16,46,2048\nMaav 124,188,76\nRa 172,24,44032\nWL 8\nMaav 120,188,72\ns_xor_b32 s89, s89, 0x10000\nCvav 48,128,192,4\nRa 176,26,40960\nAD 84,84,81\nMvav 48,132,200\nEX 82,42\n"
"Mvav 48,136,208\nRa 180,26,41984\nCV 66,80,81\nbuffer_load_dwordx4 a[248:251], v1, s[16:19], s5 offen offset:2048 sc1\nMvav 48,140,216\nEX 83,43\nCvav 52,140,220,8\n"
"Ra 184,26,43008\nAD 84,84,82\nMvav 52,136,212\nD1 1,16,46,3072\nMvav 52,132,204\nRa 188,26,44032\nWL 8\nMvav 52,128,196\ns_add_u32 m0, s89, 0\ns_add_u32 s46, s86, s88\n"
"Mvav 52,144,228\nRa 128,2,49152\nAD 84,84,83\nMvav 52,148,236\nEX 80,44\nMvav 52,152,244\nRa 132,2,50176\nCV 67,82,83\n"
"buffer_load_dwordx4 a[252:255], v1, s[16:19], s5 offen offset:3072 sc1\nMvav 52,156,252\nEX 81,45\nMvav 48,156,248\nRa 136,2,51200\nAD 85,85,80\nMvav 48,152,240\nD1 1,16,46,0\n"
"v_xor_b32 v24, 0x10000, v24\nMvav 48,148,232\nRa 140,2,52224\nWL 8\nMvav 48,144,224\nCvav 56,160,192,12\nRa 144,2,53248\nAD 85,85,81\nMvav 56,164,200\nEX 82,46\nMvav 56,168,208\n"
"Ra 148,2,54272\nCV 70,80,81\nMvav 56,172,216\nEX 83,47\nCvav 60,172,220,16\nRa 152,2,55296\nAD 85,85,82\nMvav 60,168,212\nD1 1,16,46,1024\nv_xor_b32 v26, 0x10000, v26\n"
"Mvav 60,164,204\nRa 156,2,56320\nWL 8\nMvav 60,160,196\ns_add_u32 s88, s88, 16384\nMvav 60,176,228\nRa 160,2,57344\nAD 85,85,83\nMvav 60,180,236\nEX 80,48\nMvav 60,184,244\n"
"Ra 164,2,58368\nCV 71,82,83\nMvav 60,188,252\nEX 81,49\nMvav 56,188,248\nRa 168,2,59392\nv_max_f32 v90, v84, v85\nMvav 56,184,240\nD1 1,16,46,2048\nv_cmp_lt_f32 vcc, s63, v90\n"
"Mvav 56,180,232\nRa 172,2,60416\nWL 8\nMvav 56,176,224\ns_cbranch_vccnz .T102\n.T103:\nAD 86,86,84\nMaav 0,128,64\nRa 176,2,61440\nAD 84,80,81\nMaav 4,128,68\nEX 82,50\n"
"Maav 12,132,68\nRa 180,2,62464\nCV 72,80,81\nMaav 8,132,64\nEX 83,51\nMaav 16,136,64\nRa 184,2,63488\nAD 84,84,82\nMaav 20,136,68\nD1 1,16,46,3072\ns_add_u32 m0, s89, 16384\n"
"Maav 28,140,68\nRa 188,2,64512\nMaav 24,140,64\ns_waitcnt vmcnt(21) lgkmcnt(8)\ns_barrier\nMaav 32,144,64\nRa 128,24,0\nAD 84,84,83\nMaav 36,144,68\nEX 80,52\nMaav 44,148,68\n"
"Ra 132,24,1024\nCV 73,82,83\nMaav 40,148,64\nEX 81,53\nMaav 48,152,64\nRa 136,24,2048\nAD 87,87,85\nMaav 52,152,68\ns_add_u32 s46, s87, s88\nD1 1,16,46,0\nMaav 60,156,68\n"
"Ra 140,24,3072\nWL 8\nMaav 56,156,64\ns_cmp_lg_u32 s67, 0\ns_cbranch_scc1 .E105\n.E106:\nMaav 64,160,64\nRa 144,26,0\nAD 85,80,81\nMaav 68,160,68\nEX 82,54\nMaav 76,164,68\n"
"Ra 148,26,1024\nCV 76,80,81\nMaav 72,164,64\nEX 83,55\nMaav 80,168,64\nRa 152,26,2048\nAD 85,85,82\nMaav 84,168,68\nD1 1,16,46,1024\nv_xor_b32 v2, 0x10000, v2\nMaav 92,172,68\n"
"Ra 156,26,3072\nWL 8\nMaav 88,172,64\nMaav 96,176,64\nRa 160,24,8192\nAD 85,85,83\nMaav 100,176,68\nEX 80,56\nMaav 108,180,68\nRa 164,24,9216\nCV 77,82,83\nMaav 104,180,64\n"
"EX 81,57\nMaav 112,184,64\nRa 168,24,10240\nAD 84,84,80\nMaav 116,184,68\nD1 1,16,46,2048\nMaav 124,188,68\nRa 172,24,11264\nMaav 120,188,64\ns_cmp_lg_u32 s67, 0\n"
"s_cbranch_scc1 .D107\n.D108:\nWV 7\nv_cmp_lt_f32 vcc, 0, v101\nSU 20,100,88\nv_mul_f32 v101, 0x3e800000, v101\nv_cndmask_b32 v20, 0, v20, vcc\nv_cmp_lt_f32 vcc, 0, v105\n"
"SU 21,104,89\nv_mul_f32 v105, 0x3e800000, v105\nv_cndmask_b32 v21, 0, v21, vcc\nv_or_b32 v91, v20, v21\nv_cmp_ne_u32 vcc, 0, v91\ns_nop 0\ns_cbranch_vccnz .D109\nAD 86,86,101\n"
"AD 87,87,105\n.D110:\nMava 0,130,192\nAD 84,84,81\nMava 8,128,192\nEX 82,58\nMava 12,128,196\nCV 74,80,81\nMava 4,130,196\nEX 83,59\nMava 16,130,200\nAD 84,84,82\nMava 24,128,200\n"
"D1 1,16,46,3072\nMava 28,128,204\nMava 20,130,204\ns_add_u32 m0, s89, 32768\ns_add_u32 s46, s86, s88\nMava 32,130,208\nRa 128,2,16384\nAD 84,84,83\nv_xor_b32 v3, 0x10000, v2\n"
"v_add_u32 v0, 0x20000, v1\nMava 40,128,208\nEX 80,60\ns_mul_i32 s2, s72, 33792\ns_add_u32 s94, s2, 0x3500000\nMava 44,128,212\nRa 132,2,17408\nCV 75,82,83\n"
"s_add_u32 s95, s2, 0x3504000\nMava 36,130,212\nEX 81,61\ns_xor_b32 s2, s72, 32\ns_mul_i32 s2, s2, 33792\nMava 48,130,216\nRa 136,2,18432\nAD 85,85,80\n"
"s_add_u32 s92, s2, 0x3500000\nMava 56,128,216\nD1 1,16,46,0\ns_and_b32 s2, s12, 1\ns_xor_b32 s3, s12, 1\ns_lshl1_add_u32 s3, s3, s2\nMava 60,128,220\nRa 140,2,19456\n"
"Mava 52,130,220\ns_mul_i32 s3, s3, 119808\ns_lshl_b32 s89, s11, 12\ns_add_u32 s3, s3, s89\ns_add_u32 s93, s3, 0x5700000\nMava 64,130,224\nRa 144,2,20480\nAD 85,85,81\n"
"s_mov_b32 s36, s68\ns_and_b32 s37, s69, 0xffff\nMava 72,128,224\nEX 82,62\ns_mov_b32 s38, 0x1c0000\nMava 76,128,228\nRa 148,2,21504\nCV 78,80,81\ns_mov_b32 s39, 0x20000\n"
"s_and_b32 s2, s74, 15\nMava 68,130,228\nEX 83,63\ns_mul_i32 s3, s73, 14\ns_add_u32 s2, s2, s3\nMava 80,130,232\nRa 152,2,22528\nAD 85,85,82\ns_lshl2_add_u32 s2, s2, s11\n"
"s_mul_i32 s3, s2, 4682\nMava 88,128,232\nD1 1,16,46,1024\ns_lshr_b32 s3, s3, 16\ns_mul_i32 s91, s3, 14\nMava 92,128,236\nRa 156,2,23552\nMava 84,130,236\ns_sub_u32 s91, s2, s91\n"
"s_lshl_b32 s90, s3, 5\ns_add_u32 s90, s90, 1024\ns_xor_b32 s3, s3, 28\nMava 96,130,240\nRa 160,2,24576\nAD 85,85,83\ns_cmp_gt_u32 s91, 6\ns_cselect_b32 s2, 1067008, 0\n"
"Mava 104,128,240\ns_mul_i32 s3, s3, 33792\ns_add_u32 s2, s2, s3\ns_lshl_b32 s3, s91, 11\nMava 108,128,244\nRa 164,2,25600\nCV 79,82,83\ns_add_u32 s2, s2, s3\n"
"s_add_u32 s98, s2, 0x3504000\nMava 100,130,244\ns_lshl_b32 s2, s91, 7\ns_add_u32 s0, s2, 0x5c4b800\ns_mul_i32 s2, s90, 896\ns_lshl_b32 s3, s91, 6\nMava 112,130,248\n"
"Ra 168,2,26624\nv_max_f32 v90, v84, v85\ns_add_u32 s91, s2, s3\ns_add_u32 s1, s91, 14336\nMava 120,128,248\nD1 1,16,46,2048\nv_cmp_lt_f32 vcc, s63, v90\nMava 124,128,252\n"
"Ra 172,2,27648\nWL 8\nMava 116,130,252\ns_cbranch_vccnz .T114\n.T115:\nAD 86,86,84\nAD 87,87,85\nMaav 0,128,72\nRa 176,2,28672\nv_mov_b32 v84, v86\nv_mov_b32 v85, v87\n"
"Maav 4,128,76\nv_permlane32_swap_b32 v86, v84\nv_permlane32_swap_b32 v87, v85\nMaav 12,132,76\nRa 180,2,29696\nAD 86,86,84\nMaav 8,132,72\nAD 87,87,85\nv_mov_b32 v84, v86\n"
"Maav 16,136,72\nRa 184,2,30720\nv_mov_b32 v85, v87\nMaav 20,136,76\nD1 1,16,46,3072\nv_permlane16_swap_b32 v86, v84\nMaav 28,140,76\nRa 188,2,31744\nWL 8\nMaav 24,140,72\n"
"v_permlane16_swap_b32 v87, v85\nAD 86,86,84\nAD 87,87,85\nMaav 32,144,72\nRa 128,2,0\nMaav 36,144,76\nv_rcp_f32 v86, v86\nMaav 44,148,76\nRa 132,2,1024\nv_rcp_f32 v87, v87\n"
"Maav 40,148,72\ns_add_u32 m0, s89, 131072\ns_add_u32 s46, s93, 0\nAR 82,0\nAR 83,1\nMU 82,82,86\nMaav 48,152,72\nRa 136,2,2048\nMU 83,83,86\nCV 96,82,83\nAR 82,2\nAR 83,3\n"
"MU 82,82,86\nMaav 52,152,76\nD1 1,16,46,0\nMU 83,83,86\nCV 97,82,83\nAR 82,4\nAR 83,5\nMU 82,82,87\nMaav 60,156,76\nRa 140,2,3072\nWL 8\nMaav 56,156,72\nMU 83,83,87\nCV 100,82,83\n"
"AR 82,6\nAR 83,7\nMU 82,82,87\nMU 83,83,87\nMaav 64,160,72\nRa 144,2,4096\nCV 101,82,83\nAR 82,8\nAR 83,9\nMU 82,82,86\nMU 83,83,86\nMaav 68,160,76\nCV 98,82,83\nAR 82,10\n"
"AR 83,11\nMU 82,82,86\nMU 83,83,86\nMaav 76,164,76\nRa 148,2,5120\nCV 99,82,83\ns_add_u32 s5, s94, 0\nSv 96,1,16,5,0\nAR 82,12\nAR 83,13\nMaav 72,164,72\nMU 82,82,87\nMU 83,83,87\n"
"CV 102,82,83\nAR 82,14\nAR 83,15\nMaav 80,168,72\nRa 152,2,6144\nMU 82,82,87\nMU 83,83,87\nCV 103,82,83\nSv 100,1,16,5,1024\nAR 82,16\nMaav 84,168,76\nD1 1,16,46,1024\nAR 83,17\n"
"MU 82,82,86\nMU 83,83,86\nCV 104,82,83\nAR 82,18\nMaav 92,172,76\nRa 156,2,7168\nWL 8\nMaav 88,172,72\nAR 83,19\nMU 82,82,86\nMU 83,83,86\nCV 105,82,83\nAR 82,20\nAR 83,21\n"
"Maav 96,176,72\nRa 160,2,8192\nMU 82,82,87\nMU 83,83,87\nCV 108,82,83\nAR 82,22\nAR 83,23\nMaav 100,176,76\nMU 82,82,87\nMU 83,83,87\nCV 109,82,83\nAR 82,24\nAR 83,25\n"
"Maav 108,180,76\nRa 164,2,9216\nMU 82,82,86\nMU 83,83,86\nCV 106,82,83\nAR 82,26\nAR 83,27\nMaav 104,180,72\nMU 82,82,86\nMU 83,83,86\nCV 107,82,83\nSv 104,1,16,5,2048\nAR 82,28\n"
"Maav 112,184,72\nRa 168,2,10240\nAR 83,29\nMU 82,82,87\nMU 83,83,87\nCV 110,82,83\nAR 82,30\nMaav 116,184,76\nD1 1,16,46,2048\nAR 83,31\nMU 82,82,87\nMU 83,83,87\nCV 111,82,83\n"
"Sv 108,1,16,5,3072\nMaav 124,188,76\nRa 172,2,11264\nWL 8\nMaav 120,188,72\nAR 82,32\nAR 83,33\nMU 82,82,86\nZvav 136,128,96\nRa 176,2,12288\nMU 83,83,86\nCV 112,82,83\nAR 82,34\n"
"Mvav 136,132,104\nAR 83,35\nMU 82,82,86\nMU 83,83,86\nZvav 140,132,108\nRa 180,2,13312\nCV 113,82,83\nAR 82,36\nMvav 140,128,100\nAR 83,37\nMU 82,82,87\nMU 83,83,87\n"
"Zvav 148,136,100\nRa 184,2,14336\nCV 116,82,83\nAR 82,38\nMvav 148,140,108\nD1 1,16,46,3072\nAR 83,39\nMU 82,82,87\nZvav 144,140,104\nRa 188,2,15360\nMvav 144,136,96\nWL 8\n"
"s_barrier\nZvav 152,144,96\nRa 128,2,32768\nMU 83,83,87\nCV 117,82,83\nMvav 152,148,104\nAR 82,40\nAR 83,41\nMU 82,82,86\nZvav 156,148,108\nRa 132,2,33792\nMU 83,83,86\n"
"CV 114,82,83\nv_lshrrev_b32 v88, 8, v1\nMvav 156,144,100\ns_add_u32 m0, s89, 114688\ns_add_u32 s46, s96, 608256\nAR 82,42\nZvav 164,152,100\nRa 136,2,34816\nAR 83,43\nMU 82,82,86\n"
"v_lshlrev_b32 v89, 5, v88\nMvav 164,156,108\nD1 1,16,46,0\nMU 83,83,86\nCV 115,82,83\nZvav 160,156,104\nRa 140,2,35840\nWL 8\nMvav 160,152,96\ns_add_u32 s5, s94, 4096\n"
"Sv 112,1,16,5,0\nAR 82,44\nZvav 168,160,96\nRa 144,2,36864\nAR 83,45\nMU 82,82,87\nMU 83,83,87\nMvav 168,164,104\nCV 118,82,83\nAR 82,46\nAR 83,47\nZvav 172,164,108\n"
"Ra 148,2,37888\nMU 82,82,87\nMU 83,83,87\nMvav 172,160,100\nCV 119,82,83\nSv 116,1,16,5,1024\nAR 82,48\nbuffer_load_dwordx4 v[64:67], v89, s[16:19], s0 offen offset:0 sc0 sc1\n"
"Zvav 180,168,100\nRa 152,2,38912\nAR 83,49\nMU 82,82,86\nMvav 180,172,108\nD1 1,16,46,1024\nMU 83,83,86\nCV 120,82,83\nZvav 176,172,104\nRa 156,2,39936\nWL 8\nMvav 176,168,96\n"
"AR 82,50\nAR 83,51\nMU 82,82,86\nbuffer_load_dwordx4 v[68:71], v89, s[16:19], s0 offen offset:16 sc0 sc1\nZvav 184,176,96\nRa 160,2,40960\nMU 83,83,86\nCV 121,82,83\n"
"Mvav 184,180,104\nAR 82,52\nAR 83,53\nMU 82,82,87\nZvav 188,180,108\nRa 164,2,41984\nMU 83,83,87\nCV 124,82,83\nMvav 188,176,100\nAR 82,54\nAR 83,55\nMU 82,82,87\n"
"Zvav 196,184,100\nRa 168,2,43008\nMU 83,83,87\nCV 125,82,83\nbuffer_load_dwordx4 v[72:75], v89, s[16:19], s0 offen offset:1792 sc0 sc1\nMvav 196,188,108\nD1 1,16,46,2048\n"
"AR 82,56\nAR 83,57\nZvav 192,188,104\nRa 172,2,44032\nWL 8\nMvav 192,184,96\nMU 82,82,86\nMU 83,83,86\nCV 122,82,83\nZvav 200,128,96\nRa 176,2,45056\nAR 82,58\nAR 83,59\n"
"MU 82,82,86\nMvav 200,132,104\nMU 83,83,86\nCV 123,82,83\nSv 120,1,16,5,2048\nZvav 204,132,108\nRa 180,2,46080\nAR 82,60\nAR 83,61\nMvav 204,128,100\nMU 82,82,87\nMU 83,83,87\n"
"CV 126,82,83\nbuffer_load_dwordx4 v[76:79], v89, s[16:19], s0 offen offset:1808 sc0 sc1\nZvav 212,136,100\nRa 184,2,47104\nAR 82,62\nAR 83,63\nMvav 212,140,108\nD1 1,16,46,3072\n"
"MU 82,82,87\nMU 83,83,87\nZvav 208,140,104\nRa 188,2,48128\nWL 8\nMvav 208,136,96\nCV 127,82,83\nSv 124,1,16,5,3072\nv_bfe_u32 v90, v1, 4, 4\nv_mul_u32_u24 v91, 896, v90\n"
"Zvav 216,144,96\nRa 128,2,49152\nAR 82,64\nAR 83,65\nMU 82,82,86\nMvav 216,148,104\nMU 83,83,86\nCV 32,82,83\nAR 82,66\nZvav 220,148,108\nRa 132,2,50176\nAR 83,67\nMU 82,82,86\n"
"v_lshl_add_u32 v91, v88, 4, v91\nMvav 220,144,100\ns_add_u32 m0, s89, 147456\ns_add_u32 s46, s93, 16384\nMU 83,83,86\nZvav 228,152,100\nRa 136,2,51200\nCV 33,82,83\nAR 82,68\n"
"buffer_load_dwordx4 v[12:15], v91, s[28:31], s91 offen offset:0\nMvav 228,156,108\nD1 1,16,46,0\nAR 83,69\nMU 82,82,87\nZvav 224,156,104\nRa 140,2,52224\nWL 8\nMvav 224,152,96\n"
"MU 83,83,87\nCV 36,82,83\nAR 82,70\nZvav 232,160,96\nRa 144,2,53248\nAR 83,71\nMU 82,82,87\nMU 83,83,87\nMvav 232,164,104\nCV 37,82,83\nAR 82,72\nAR 83,73\nZvav 236,164,108\n"
"Ra 148,2,54272\nMU 82,82,86\nMU 83,83,86\nMvav 236,160,100\nCV 34,82,83\nAR 82,74\nAR 83,75\nbuffer_load_dwordx4 v[16:19], v91, s[28:31], s1 offen offset:0\nZvav 244,168,100\n"
"Ra 152,2,55296\nMU 82,82,86\nMU 83,83,86\nMvav 244,172,108\nD1 1,16,46,1024\nCV 35,82,83\ns_add_u32 s5, s94, 8192\nv_add_u32 v90, s90, v90\nZvav 240,172,104\nRa 156,2,56320\nWL 8\n"
"Mvav 240,168,96\nSv 32,1,16,5,0\nAR 82,76\nAR 83,77\nv_lshlrev_b32 v88, 2, v90\nMvav 136,176,112\nRa 160,2,57344\nMU 82,82,87\nMU 83,83,87\nCV 38,82,83\nMvav 136,180,120\n"
"AR 82,78\nAR 83,79\nMU 82,82,87\nMvav 140,180,124\nRa 164,2,58368\nMU 83,83,87\nCV 39,82,83\nMvav 140,176,116\nSv 36,1,16,5,1024\nAR 82,80\nAR 83,81\nMvav 148,184,116\n"
"Ra 168,2,59392\nMU 82,82,86\nMU 83,83,86\nMvav 148,188,124\nD1 1,16,46,2048\nCV 40,82,83\nAR 82,82\nMvav 144,188,120\nRa 172,2,60416\nWL 8\nMvav 144,184,112\nAR 83,83\n"
"MU 82,82,86\nMU 83,83,86\nMvav 152,128,112\nRa 176,2,61440\nCV 41,82,83\nAR 82,84\nAR 83,85\nMvav 152,132,120\nMU 82,82,87\nMU 83,83,87\nCV 44,82,83\nMvav 156,132,124\n"
"Ra 180,2,62464\nAR 82,86\nAR 83,87\nMvav 156,128,116\nMU 82,82,87\nMU 83,83,87\nCV 45,82,83\nMvav 164,136,116\nRa 184,2,63488\nAR 82,88\nAR 83,89\nMvav 164,140,124\n"
"D1 1,16,46,3072\nMU 82,82,86\nMU 83,83,86\nMvav 160,140,120\nRa 188,2,64512\nMvav 160,136,112\ns_waitcnt vmcnt(32) lgkmcnt(8)\ns_barrier\nMvav 168,144,112\nRa 128,3,0\n"
"CV 42,82,83\nAR 82,90\nAR 83,91\nMvav 168,148,120\nMU 82,82,86\nMU 83,83,86\nCV 43,82,83\nMvav 172,148,124\nRa 132,3,1024\nSv 40,1,16,5,2048\nAR 82,92\nMvav 172,144,116\nAR 83,93\n"
"MU 82,82,87\nMU 83,83,87\nMvav 180,152,116\nRa 136,3,2048\nCV 46,82,83\nAR 82,94\nMvav 180,156,124\nAR 83,95\nMU 82,82,87\nMU 83,83,87\nMvav 176,156,120\nRa 140,3,3072\nWL 8\n"
"Mvav 176,152,112\nCV 47,82,83\nSv 44,1,16,5,3072\nMvav 184,160,112\nRa 144,3,4096\nAR 82,96\nAR 83,97\nMvav 184,164,120\nMU 82,82,86\nMU 83,83,86\nMvav 188,164,124\nRa 148,3,5120\n"
"CV 48,82,83\nAR 82,98\nMvav 188,160,116\nAR 83,99\nMU 82,82,86\nMU 83,83,86\nMvav 196,168,116\nRa 152,3,6144\nCV 49,82,83\nAR 82,100\nMvav 196,172,124\nAR 83,101\nMU 82,82,87\n"
"MU 83,83,87\nMvav 192,172,120\nRa 156,3,7168\nWL 8\nMvav 192,168,112\nCV 52,82,83\nAR 82,102\nAR 83,103\nMvav 200,176,112\nRa 160,3,8192\nMU 82,82,87\nMU 83,83,87\n"
"Mvav 200,180,120\nCV 53,82,83\nAR 82,104\nAR 83,105\nMvav 204,180,124\nRa 164,3,9216\nMU 82,82,86\nMU 83,83,86\nMvav 204,176,116\nCV 50,82,83\nAR 82,106\nMvav 212,184,116\n"
"Ra 168,3,10240\nAR 83,107\nMU 82,82,86\nMvav 212,188,124\nMU 83,83,86\nCV 51,82,83\ns_add_u32 s5, s94, 12288\nMvav 208,188,120\nRa 172,3,11264\nWL 8\nMvav 208,184,112\n"
"Sv 48,1,16,5,0\nAR 82,108\nAR 83,109\nMvav 216,128,112\nRa 176,3,12288\nMU 82,82,87\nMU 83,83,87\nMvav 216,132,120\nCV 54,82,83\nAR 82,110\nMvav 220,132,124\nRa 180,3,13312\n"
"AR 83,111\nMU 82,82,87\nMvav 220,128,116\nMU 83,83,87\nCV 55,82,83\nSv 52,1,16,5,1024\nMvav 228,136,116\nRa 184,3,14336\nAR 82,112\nAR 83,113\nMvav 228,140,124\nMU 82,82,86\n"
"MU 83,83,86\nCV 56,82,83\nMvav 224,140,120\nRa 188,3,15360\nWL 8\nMvav 224,136,112\nAR 82,114\nAR 83,115\nMU 82,82,86\nMvav 232,144,112\nRa 128,3,16384\nMU 83,83,86\nCV 57,82,83\n"
"Mvav 232,148,120\nAR 82,116\nAR 83,117\nMU 82,82,87\nMvav 236,148,124\nRa 132,3,17408\nMU 83,83,87\nCV 60,82,83\nMvav 236,144,116\nAR 82,118\nAR 83,119\nMvav 244,152,116\n"
"Ra 136,3,18432\nMU 82,82,87\nMU 83,83,87\nMvav 244,156,124\nCV 61,82,83\nAR 82,120\nAR 83,121\nMvav 240,156,120\nRa 140,3,19456\nWL 8\nMvav 240,152,112\nMU 82,82,86\nMU 83,83,86\n"
"CV 58,82,83\nMvav 136,160,32\nRa 144,3,20480\nAR 82,122\nAR 83,123\nMvav 136,164,40\nMU 82,82,86\nMU 83,83,86\nMvav 140,164,44\nRa 148,3,21504\nCV 59,82,83\nSv 56,1,16,5,2048\n"
"Mvav 140,160,36\nAR 82,124\nAR 83,125\nMU 82,82,87\nMvav 148,168,36\nRa 152,3,22528\nMU 83,83,87\nCV 62,82,83\nMvav 148,172,44\nAR 82,126\nAR 83,127\nMU 82,82,87\nMvav 144,172,40\n"
"Ra 156,3,23552\nWL 8\nMvav 144,168,32\nMU 83,83,87\nCV 63,82,83\nSv 60,1,16,5,3072\nMvav 152,176,32\nRa 160,3,24576\nWV 13\nMvav 152,180,40\nv_xor_b32 v92, s15, v66\n"
"v_xor_b32 v93, s15, v70\nMvav 156,180,44\nRa 164,3,25600\nv_xor_b32 v94, s15, v74\nMvav 156,176,36\nv_xor_b32 v95, s15, v78\nv_or3_b32 v92, v92, v93, v94\nMvav 164,184,36\n"
"Ra 168,3,26624\nv_or_b32 v92, v92, v95\nMvav 164,188,44\nv_cmp_ne_u32 vcc, 0, v92\ns_nop 0\ns_cbranch_vccnz .S123\n.S124:\nMvav 160,188,40\nRa 172,3,27648\nWL 8\nMvav 160,184,32\n"
"s_mov_b32 s2, 0x5c40000\nbuffer_load_dword v80, v88, s[16:19], s2 offen sc0 sc1\nMvav 168,128,32\nRa 176,3,28672\nMvav 168,132,40\nMvav 172,132,44\nRa 180,3,29696\n"
"Mvav 172,128,36\nbuffer_load_dword v81, v88, s[16:19], s2 offen offset:64 sc0 sc1\nv_and_b32 v92, 0x7ff, v67\nMvav 180,136,36\nRa 184,3,30720\nv_lshrrev_b32 v93, 16, v67\n"
"Mvav 180,140,44\nv_add_u32 v94, 16, v90\nv_cmp_eq_u32 vcc, v92, v90\ns_nop 1\nv_cndmask_b32 v12, v12, v64, vcc\nv_cndmask_b32 v13, v13, v65, vcc\nv_cndmask_b32 v14, v14, v68, vcc\n"
"v_cndmask_b32 v15, v15, v69, vcc\nMvav 176,140,40\nRa 188,3,31744\nMvav 176,136,32\ns_waitcnt vmcnt(13) lgkmcnt(8)\ns_barrier\nMvav 184,144,32\nRa 128,3,32768\n"
"v_cmp_eq_u32 vcc, v93, v90\ns_nop 1\nv_cndmask_b32 v12, v12, v72, vcc\nv_cndmask_b32 v13, v13, v73, vcc\nv_cndmask_b32 v14, v14, v76, vcc\nv_cndmask_b32 v15, v15, v77, vcc\n"
"Mvav 184,148,40\nv_cmp_eq_u32 vcc, v92, v94\ns_nop 1\nv_cndmask_b32 v16, v16, v64, vcc\nv_cndmask_b32 v17, v17, v65, vcc\nv_cndmask_b32 v18, v18, v68, vcc\n"
"v_cndmask_b32 v19, v19, v69, vcc\nMvav 188,148,44\nRa 132,3,33792\nv_cmp_eq_u32 vcc, v93, v94\ns_nop 1\nv_cndmask_b32 v16, v16, v72, vcc\nv_cndmask_b32 v17, v17, v73, vcc\n"
"v_cndmask_b32 v18, v18, v76, vcc\nv_cndmask_b32 v19, v19, v77, vcc\nMvav 188,144,36\nMvav 196,152,36\nRa 136,3,34816\nMvav 196,156,44\nMvav 192,156,40\nRa 140,3,35840\nWL 8\n"
"Mvav 192,152,32\nMvav 200,160,32\nRa 144,3,36864\nMvav 200,164,40\ns_add_u32 s4, s92, 0\nMvav 204,164,44\nRa 148,3,37888\nMvav 204,160,36\ns_add_u32 m0, s89, 16384\n"
"s_add_u32 s46, s93, 33792\nbuffer_load_dwordx4 a[192:195], v1, s[16:19], s4 offen offset:0 sc0 sc1\nMvav 212,168,36\nRa 152,3,38912\nMvav 212,172,44\nD1 1,16,46,0\n"
"Mvav 208,172,40\nRa 156,3,39936\nWL 8\nMvav 208,168,32\nbuffer_load_dwordx4 a[196:199], v1, s[16:19], s4 offen offset:1024 sc0 sc1\nMvav 216,176,32\nRa 160,3,40960\n"
"Mvav 216,180,40\nMvav 220,180,44\nRa 164,3,41984\nMvav 220,176,36\nbuffer_load_dwordx4 a[200:203], v1, s[16:19], s4 offen offset:2048 sc0 sc1\nMvav 228,184,36\nRa 168,3,43008\n"
"Mvav 228,188,44\nD1 1,16,46,1024\nMvav 224,188,40\nRa 172,3,44032\nWL 8\nMvav 224,184,32\nbuffer_load_dwordx4 a[204:207], v1, s[16:19], s4 offen offset:3072 sc0 sc1\n"
"Mvav 232,128,32\nRa 176,3,45056\nMvav 232,132,40\ns_add_u32 s4, s92, 4096\nMvav 236,132,44\nRa 180,3,46080\nMvav 236,128,36\n"
"buffer_load_dwordx4 a[208:211], v1, s[16:19], s4 offen offset:0 sc0 sc1\nMvav 244,136,36\nRa 184,3,47104\nMvav 244,140,44\nD1 1,16,46,2048\nMvav 240,140,40\nRa 188,3,48128\nWL 8\n"
"Mvav 240,136,32\nbuffer_load_dwordx4 a[212:215], v1, s[16:19], s4 offen offset:1024 sc0 sc1\nMvav 136,144,48\nRa 128,3,49152\nMvav 136,148,56\nMvav 140,148,60\nRa 132,3,50176\n"
"Mvav 140,144,52\nbuffer_load_dwordx4 a[216:219], v1, s[16:19], s4 offen offset:2048 sc0 sc1\nMvav 148,152,52\nRa 136,3,51200\nMvav 148,156,60\nD1 1,16,46,3072\nMvav 144,156,56\n"
"Ra 140,3,52224\nWL 8\nMvav 144,152,48\nbuffer_load_dwordx4 a[220:223], v1, s[16:19], s4 offen offset:3072 sc0 sc1\nMvav 152,160,48\nRa 144,3,53248\nMvav 152,164,56\n"
"Mvav 156,164,60\nRa 148,3,54272\nMvav 156,160,52\ns_add_u32 m0, s89, 0\ns_add_u32 s46, s93, 50176\nMvav 164,168,52\nRa 152,3,55296\nMvav 164,172,60\nD1 1,16,46,0\nMvav 160,172,56\n"
"Ra 156,3,56320\nWL 8\nMvav 160,168,48\nMvav 168,176,48\nRa 160,3,57344\nMvav 168,180,56\nMvav 172,180,60\nRa 164,3,58368\nMvav 172,176,52\nMvav 180,184,52\nRa 168,3,59392\n"
"Mvav 180,188,60\nD1 1,16,46,1024\nMvav 176,188,56\nRa 172,3,60416\nWL 8\nMvav 176,184,48\nMvav 184,128,48\nRa 176,3,61440\nMvav 184,132,56\nMvav 188,132,60\nRa 180,3,62464\n"
"Mvav 188,128,52\nMvav 196,136,52\nRa 184,3,63488\nMvav 196,140,60\nD1 1,16,46,2048\nMvav 192,140,56\nRa 188,3,64512\nMvav 192,136,48\ns_waitcnt vmcnt(23) lgkmcnt(8)\ns_barrier\n"
"Mvav 200,144,48\nRa 128,0,0\nMvav 200,148,56\nMvav 204,148,60\nRa 132,0,1024\nMvav 204,144,52\nMvav 212,152,52\nRa 136,0,2048\nMvav 212,156,60\nD1 1,16,46,3072\nMvav 208,156,56\n"
"Ra 140,0,3072\nWL 8\nMvav 208,152,48\nMvav 216,160,48\nRa 144,0,4096\nMvav 216,164,56\nMvav 220,164,60\nRa 148,0,5120\nMvav 220,160,52\ns_add_u32 m0, s89, 32768\n"
"s_add_u32 s46, s93, 67584\nMvav 228,168,52\nRa 152,0,6144\nMvav 228,172,60\nD1 1,16,46,0\nMvav 224,172,56\nRa 156,0,7168\nWL 8\nMvav 224,168,48\nWV 5\nAR 22,195\nAR 23,199\n"
"Mvav 232,176,48\nRa 160,0,8192\nv_max_u32 v21, v22, v23\nMvav 232,180,56\nAR 22,203\nAR 23,207\nMvav 236,180,60\nRa 164,0,9216\nv_max3_u32 v21, v21, v22, v23\nAR 22,211\n"
"Mvav 236,176,52\nAR 23,215\nv_max3_u32 v21, v21, v22, v23\nMvav 244,184,52\nRa 168,0,10240\nAR 22,219\nMvav 244,188,60\nD1 1,16,46,1024\nAR 23,223\nv_max3_u32 v21, v21, v22, v23\n"
"Mvav 240,188,56\nRa 172,0,11264\nWL 8\nMvav 240,184,48\nv_cmp_eq_u32 vcc, -1, v21\ns_nop 0\ns_cbranch_vccnz .S117\n.S118:\nMvaa 136,128,192\nRa 176,0,12288\nMvaa 136,132,200\n"
"v_sub_f32 v92, 1.0, v80\nv_sub_f32 v93, 1.0, v81\nMvaa 136,136,208\nRa 180,0,13312\nCV 92,92,93\nMvaa 136,140,216\nv_and_b32 v93, 0xffff0000, v92\nv_lshlrev_b32 v92, 16, v92\n"
"Mvaa 140,140,220\nRa 184,0,14336\nv_lshlrev_b32 v64, 16, v12\nMvaa 140,136,212\nD1 1,16,46,2048\nv_and_b32 v65, 0xffff0000, v12\nMvaa 140,132,204\nRa 188,0,15360\nWL 8\n"
"Mvaa 140,128,196\nMU 64,64,92\nMU 65,65,92\nMvaa 144,144,192\nRa 128,0,16384\nv_lshlrev_b32 v66, 16, v13\nMvaa 144,148,200\nv_and_b32 v67, 0xffff0000, v13\nMU 66,66,92\n"
"Mvaa 144,152,208\nRa 132,0,17408\nMU 67,67,92\nMvaa 144,156,216\nv_lshlrev_b32 v68, 16, v14\nv_and_b32 v69, 0xffff0000, v14\nMvaa 148,156,220\nRa 136,0,18432\nMU 68,68,92\n"
"Mvaa 148,152,212\nD1 1,16,46,3072\nMU 69,69,92\nMvaa 148,148,204\nRa 140,0,19456\nWL 8\nMvaa 148,144,196\nv_lshlrev_b32 v70, 16, v15\nv_and_b32 v71, 0xffff0000, v15\n"
"Mvaa 152,160,192\nRa 144,0,20480\nMU 70,70,92\nMvaa 152,164,200\nMU 71,71,92\nv_lshlrev_b32 v72, 16, v16\nMvaa 152,168,208\nRa 148,0,21504\nv_and_b32 v73, 0xffff0000, v16\n"
"Mvaa 152,172,216\ns_add_u32 m0, s89, 49152\ns_add_u32 s46, s93, 83968\nMvaa 156,172,220\nRa 152,0,22528\nMU 72,72,93\nMvaa 156,168,212\nD1 1,16,46,0\nMU 73,73,93\n"
"Mvaa 156,164,204\nRa 156,0,23552\nWL 8\nMvaa 156,160,196\nv_lshlrev_b32 v74, 16, v17\nv_and_b32 v75, 0xffff0000, v17\nMvaa 160,176,192\nRa 160,0,24576\nMU 74,74,93\n"
"Mvaa 160,180,200\ns_add_u32 s4, s92, 8192\nMU 75,75,93\nMvaa 160,184,208\nRa 164,0,25600\nv_lshlrev_b32 v76, 16, v18\nMvaa 160,188,216\n"
"buffer_load_dwordx4 a[224:227], v1, s[16:19], s4 offen offset:0 sc0 sc1\nv_and_b32 v77, 0xffff0000, v18\nMvaa 164,188,220\nRa 168,0,26624\nMU 76,76,93\nMvaa 164,184,212\n"
"D1 1,16,46,1024\nMU 77,77,93\nMvaa 164,180,204\nRa 172,0,27648\nWL 8\nMvaa 164,176,196\nbuffer_load_dwordx4 a[228:231], v1, s[16:19], s4 offen offset:1024 sc0 sc1\n"
"v_lshlrev_b32 v78, 16, v19\nMvaa 168,128,192\nRa 176,0,28672\nv_and_b32 v79, 0xffff0000, v19\nMvaa 168,132,200\nMU 78,78,93\nMU 79,79,93\nMvaa 168,136,208\nRa 180,0,29696\n"
"Mvaa 168,140,216\nbuffer_load_dwordx4 a[232:235], v1, s[16:19], s4 offen offset:2048 sc0 sc1\nMvaa 172,140,220\nRa 184,0,30720\nMvaa 172,136,212\nD1 1,16,46,2048\n"
"Mvaa 172,132,204\nRa 188,0,31744\nMvaa 172,128,196\ns_waitcnt vmcnt(10) lgkmcnt(8)\ns_barrier\nMvaa 176,144,192\nRa 128,2,16384\n"
"buffer_load_dwordx4 a[236:239], v1, s[16:19], s4 offen offset:3072 sc0 sc1\nMvaa 176,148,200\nMvaa 176,152,208\nRa 132,2,17408\ns_add_u32 s4, s92, 12288\nMvaa 176,156,216\n"
"Mvaa 180,156,220\nRa 136,2,18432\nbuffer_load_dwordx4 a[240:243], v1, s[16:19], s4 offen offset:0 sc0 sc1\nMvaa 180,152,212\nD1 1,16,46,3072\nMvaa 180,148,204\nRa 140,2,19456\n"
"WL 8\nMvaa 180,144,196\nMvaa 184,160,192\nRa 144,2,20480\nbuffer_load_dwordx4 a[244:247], v1, s[16:19], s4 offen offset:1024 sc0 sc1\nMvaa 184,164,200\nMvaa 184,168,208\n"
"Ra 148,2,21504\nMvaa 184,172,216\ns_add_u32 m0, s89, 65536\ns_add_u32 s46, s93, 101376\nMvaa 188,172,220\nRa 152,2,22528\n"
"buffer_load_dwordx4 a[248:251], v1, s[16:19], s4 offen offset:2048 sc0 sc1\nMvaa 188,168,212\nD1 1,16,46,0\nMvaa 188,164,204\nRa 156,2,23552\nWL 8\nMvaa 188,160,196\n"
"Mvaa 192,176,192\nRa 160,2,24576\nbuffer_load_dwordx4 a[252:255], v1, s[16:19], s4 offen offset:3072 sc0 sc1\nMvaa 192,180,200\nMvaa 192,184,208\nRa 164,2,25600\nMvaa 192,188,216\n"
"Mvaa 196,188,220\nRa 168,2,26624\nMvaa 196,184,212\nD1 1,16,46,1024\nMvaa 196,180,204\nRa 172,2,27648\nWL 8\nMvaa 196,176,196\nMvaa 200,128,192\nRa 176,2,28672\nMvaa 200,132,200\n"
"Mvaa 200,136,208\nRa 180,2,29696\nMvaa 200,140,216\nMvaa 204,140,220\nRa 184,2,30720\nMvaa 204,136,212\nD1 1,16,46,2048\nMvaa 204,132,204\nRa 188,2,31744\nWL 8\nMvaa 204,128,196\n"
"Mvaa 208,144,192\nRa 128,2,0\nMvaa 208,148,200\nMvaa 208,152,208\nRa 132,2,1024\nMvaa 208,156,216\nMvaa 212,156,220\nRa 136,2,2048\nMvaa 212,152,212\nD1 1,16,46,3072\n"
"Mvaa 212,148,204\nRa 140,2,3072\nWL 8\nMvaa 212,144,196\nMvaa 216,160,192\nRa 144,2,4096\nMvaa 216,164,200\nMvaa 216,168,208\nRa 148,2,5120\nMvaa 216,172,216\nMvaa 220,172,220\n"
"Ra 152,2,6144\nMvaa 220,168,212\nMvaa 220,164,204\nRa 156,2,7168\nWL 8\nMvaa 220,160,196\nMvaa 224,176,192\nRa 160,2,8192\nMvaa 224,180,200\nMvaa 224,184,208\nRa 164,2,9216\n"
"Mvaa 224,188,216\nMvaa 228,188,220\nRa 168,2,10240\nMvaa 228,184,212\nMvaa 228,180,204\nRa 172,2,11264\nWL 8\nMvaa 228,176,196\nMvaa 232,128,192\nRa 176,2,12288\nMvaa 232,132,200\n"
"Mvaa 232,136,208\nRa 180,2,13312\nMvaa 232,140,216\nMvaa 236,140,220\nRa 184,2,14336\nMvaa 236,136,212\nMvaa 236,132,204\nRa 188,2,15360\nMvaa 236,128,196\n"
"s_waitcnt vmcnt(7) lgkmcnt(8)\ns_barrier\nMvaa 240,144,192\nRa 128,2,32768\nWV 3\nAR 22,227\nMvaa 240,148,200\nAR 23,231\nv_max_u32 v21, v22, v23\nAR 22,235\nMvaa 240,152,208\n"
"Ra 132,2,33792\nAR 23,239\nMvaa 240,156,216\nv_max3_u32 v21, v21, v22, v23\nAR 22,243\nAR 23,247\nMvaa 244,156,220\nRa 136,2,34816\nv_max3_u32 v21, v21, v22, v23\n"
"Mvaa 244,152,212\nAR 22,251\nAR 23,255\nv_max3_u32 v21, v21, v22, v23\nMvaa 244,148,204\nRa 140,2,35840\nWL 8\nMvaa 244,144,196\nv_cmp_eq_u32 vcc, -1, v21\ns_nop 0\n"
"s_cbranch_vccnz .S120\n.S121:\nMvaa 136,160,224\nRa 144,2,36864\nMvaa 136,164,232\nMvaa 136,168,240\nRa 148,2,37888\nMvaa 136,172,248\nMvaa 140,172,252\nRa 152,2,38912\n"
"Mvaa 140,168,244\nMvaa 140,164,236\nRa 156,2,39936\nWL 8\nMvaa 140,160,228\nMvaa 144,176,224\nRa 160,2,40960\nMvaa 144,180,232\nMvaa 144,184,240\nRa 164,2,41984\nMvaa 144,188,248\n"
"Mvaa 148,188,252\nRa 168,2,43008\nMvaa 148,184,244\nMvaa 148,180,236\nRa 172,2,44032\nWL 8\nMvaa 148,176,228\nCV 136,136,137\nCV 137,138,139\nMvaa 152,128,224\nRa 176,2,45056\n"
"CV 138,144,145\nMvaa 152,132,232\nCV 139,146,147\ns_add_u32 s5, s95, 0\nMvaa 152,136,240\nRa 180,2,46080\nSv 136,1,16,5,0\nMvaa 152,140,248\nCV 140,140,141\nCV 141,142,143\n"
"Mvaa 156,140,252\nRa 184,2,47104\nCV 142,148,149\nMvaa 156,136,244\nCV 143,150,151\nSv 140,1,16,5,1024\nMvaa 156,132,236\nRa 188,2,48128\nWL 8\nMvaa 156,128,228\nMvaa 160,144,224\n"
"Ra 128,2,49152\nMvaa 160,148,232\nMvaa 160,152,240\nRa 132,2,50176\nMvaa 160,156,248\nMvaa 164,156,252\nRa 136,2,51200\nMvaa 164,152,244\nMvaa 164,148,236\nRa 140,2,52224\nWL 8\n"
"Mvaa 164,144,228\nCV 152,152,153\nCV 153,154,155\nMvaa 168,160,224\nRa 144,2,53248\nCV 154,160,161\nMvaa 168,164,232\nCV 155,162,163\nSv 152,1,16,5,2048\nMvaa 168,168,240\n"
"Ra 148,2,54272\nMvaa 168,172,248\nCV 156,156,157\nCV 157,158,159\nMvaa 172,172,252\nRa 152,2,55296\nCV 158,164,165\nMvaa 172,168,244\nCV 159,166,167\nSv 156,1,16,5,3072\n"
"Mvaa 172,164,236\nRa 156,2,56320\nWL 8\nMvaa 172,160,228\nMvaa 176,176,224\nRa 160,2,57344\nMvaa 176,180,232\nMvaa 176,184,240\nRa 164,2,58368\nMvaa 176,188,248\nMvaa 180,188,252\n"
"Ra 168,2,59392\nMvaa 180,184,244\nMvaa 180,180,236\nRa 172,2,60416\nWL 8\nMvaa 180,176,228\nCV 168,168,169\nCV 169,170,171\nMvaa 184,128,224\nRa 176,2,61440\nCV 170,176,177\n"
"Mvaa 184,132,232\nCV 171,178,179\ns_add_u32 s5, s95, 4096\nMvaa 184,136,240\nRa 180,2,62464\nSv 168,1,16,5,0\nMvaa 184,140,248\nCV 172,172,173\nCV 173,174,175\nMvaa 188,140,252\n"
"Ra 184,2,63488\nCV 174,180,181\nMvaa 188,136,244\nCV 175,182,183\nSv 172,1,16,5,1024\nMvaa 188,132,236\nRa 188,2,64512\nMvaa 188,128,228\ns_waitcnt vmcnt(6) lgkmcnt(8)\ns_barrier\n"
"Mvaa 192,144,224\nRa 128,3,0\nMvaa 192,148,232\nMvaa 192,152,240\nRa 132,3,1024\nMvaa 192,156,248\nMvaa 196,156,252\nRa 136,3,2048\nMvaa 196,152,244\nMvaa 196,148,236\n"
"Ra 140,3,3072\nWL 8\nMvaa 196,144,228\nCV 184,184,185\nCV 185,186,187\nMvaa 200,160,224\nRa 144,3,4096\nCV 186,192,193\nMvaa 200,164,232\nCV 187,194,195\nSv 184,1,16,5,2048\n"
"Mvaa 200,168,240\nRa 148,3,5120\nMvaa 200,172,248\nCV 188,188,189\nCV 189,190,191\nMvaa 204,172,252\nRa 152,3,6144\nCV 190,196,197\nMvaa 204,168,244\nCV 191,198,199\n"
"Sv 188,1,16,5,3072\nMvaa 204,164,236\nRa 156,3,7168\nWL 8\nMvaa 204,160,228\nMvaa 208,176,224\nRa 160,3,8192\nMvaa 208,180,232\nMvaa 208,184,240\nRa 164,3,9216\nMvaa 208,188,248\n"
"Mvaa 212,188,252\nRa 168,3,10240\nMvaa 212,184,244\nMvaa 212,180,236\nRa 172,3,11264\nWL 8\nMvaa 212,176,228\nCV 200,200,201\nCV 201,202,203\nMvaa 216,128,224\nRa 176,3,12288\n"
"CV 202,208,209\nMvaa 216,132,232\nCV 203,210,211\ns_add_u32 s5, s95, 8192\nMvaa 216,136,240\nRa 180,3,13312\nSv 200,1,16,5,0\nMvaa 216,140,248\nCV 204,204,205\nCV 205,206,207\n"
"Mvaa 220,140,252\nRa 184,3,14336\nCV 206,212,213\nMvaa 220,136,244\nCV 207,214,215\nSv 204,1,16,5,1024\nMvaa 220,132,236\nRa 188,3,15360\nWL 8\nMvaa 220,128,228\nMvaa 224,144,224\n"
"Mvaa 224,148,232\nMvaa 224,152,240\nMvaa 224,156,248\nMvaa 228,156,252\nMvaa 228,152,244\nMvaa 228,148,236\nWL 4\nMvaa 228,144,228\nCV 216,216,217\nCV 217,218,219\n"
"Mvaa 232,160,224\nCV 218,224,225\nCV 219,226,227\nMvaa 232,164,232\nSv 216,1,16,5,2048\nMvaa 232,168,240\nMvaa 232,172,248\nCV 220,220,221\nCV 221,222,223\nMvaa 236,172,252\n"
"CV 222,228,229\nCV 223,230,231\nMvaa 236,168,244\nSv 220,1,16,5,3072\nMvaa 236,164,236\nWL 0\nMvaa 236,160,228\nMvaa 240,176,224\nMvaa 240,180,232\nMvaa 240,184,240\n"
"Mvaa 240,188,248\nMvaa 244,188,252\nMvaa 244,184,244\nMvaa 244,180,236\nMvaa 244,176,228\nCV 232,232,233\nCV 233,234,235\ns_nop 1\nCV 234,240,241\nCV 235,242,243\n"
"s_add_u32 s5, s95, 12288\nSv 232,1,16,5,0\nCV 236,236,237\nCV 237,238,239\nCV 238,244,245\nCV 239,246,247\nSv 236,1,16,5,1024\ns_cmp_eq_u32 s13, 7\ns_cbranch_scc1 .Z126\n"
"s_mov_b32 s7, 262144\ns_mov_b32 s3, 0xff\n.Z127:\ns_bitcmp1_b32 s3, 0\ns_cbranch_scc0 .Z129\ns_add_u32 s2, s98, 0\n"
"buffer_load_dwordx4 v[96:99], v1, s[16:19], s2 offen offset:0 sc0 sc1\nbuffer_load_dwordx4 v[100:103], v1, s[16:19], s2 offen offset:1024 sc0 sc1\n.Z129:\ns_bitcmp1_b32 s3, 1\n"
"s_cbranch_scc0 .Z130\ns_add_u32 s2, s98, 2162688\nbuffer_load_dwordx4 v[104:107], v1, s[16:19], s2 offen offset:0 sc0 sc1\n"
"buffer_load_dwordx4 v[108:111], v1, s[16:19], s2 offen offset:1024 sc0 sc1\n.Z130:\ns_bitcmp1_b32 s3, 2\ns_cbranch_scc0 .Z131\ns_add_u32 s2, s98, 4325376\n"
"buffer_load_dwordx4 v[112:115], v1, s[16:19], s2 offen offset:0 sc0 sc1\nbuffer_load_dwordx4 v[116:119], v1, s[16:19], s2 offen offset:1024 sc0 sc1\n.Z131:\ns_bitcmp1_b32 s3, 3\n"
"s_cbranch_scc0 .Z132\ns_add_u32 s2, s98, 6488064\nbuffer_load_dwordx4 v[120:123], v1, s[16:19], s2 offen offset:0 sc0 sc1\n"
"buffer_load_dwordx4 v[124:127], v1, s[16:19], s2 offen offset:1024 sc0 sc1\n.Z132:\ns_bitcmp1_b32 s3, 4\ns_cbranch_scc0 .Z133\ns_add_u32 s2, s98, 8650752\n"
"buffer_load_dwordx4 v[32:35], v1, s[16:19], s2 offen offset:0 sc0 sc1\nbuffer_load_dwordx4 v[36:39], v1, s[16:19], s2 offen offset:1024 sc0 sc1\n.Z133:\ns_bitcmp1_b32 s3, 5\n"
"s_cbranch_scc0 .Z134\ns_add_u32 s2, s98, 10813440\nbuffer_load_dwordx4 v[40:43], v1, s[16:19], s2 offen offset:0 sc0 sc1\n"
"buffer_load_dwordx4 v[44:47], v1, s[16:19], s2 offen offset:1024 sc0 sc1\n.Z134:\ns_bitcmp1_b32 s3, 6\ns_cbranch_scc0 .Z135\ns_add_u32 s2, s98, 12976128\n"
"buffer_load_dwordx4 v[48:51], v1, s[16:19], s2 offen offset:0 sc0 sc1\nbuffer_load_dwordx4 v[52:55], v1, s[16:19], s2 offen offset:1024 sc0 sc1\n.Z135:\ns_bitcmp1_b32 s3, 7\n"
"s_cbranch_scc0 .Z136\ns_add_u32 s2, s98, 15138816\nbuffer_load_dwordx4 v[56:59], v1, s[16:19], s2 offen offset:0 sc0 sc1\n"
"buffer_load_dwordx4 v[60:63], v1, s[16:19], s2 offen offset:1024 sc0 sc1\n.Z136:\ns_mov_b32 s3, 0\nWV 0\nv_max_u32 v82, v99, v103\nv_cmp_eq_u32 s[44:45], -1, v82\n"
"v_max_u32 v82, v107, v111\nv_cmp_eq_u32 s[46:47], -1, v82\nv_max_u32 v82, v115, v119\nv_cmp_eq_u32 s[48:49], -1, v82\nv_max_u32 v82, v123, v127\nv_cmp_eq_u32 s[50:51], -1, v82\n"
"v_max_u32 v82, v35, v39\nv_cmp_eq_u32 s[52:53], -1, v82\nv_max_u32 v82, v43, v47\nv_cmp_eq_u32 s[54:55], -1, v82\nv_max_u32 v82, v51, v55\nv_cmp_eq_u32 s[56:57], -1, v82\n"
"v_max_u32 v82, v59, v63\nv_cmp_eq_u32 s[58:59], -1, v82\ns_cmp_lg_u64 s[44:45], 0\ns_cselect_b32 s2, 1, 0\ns_or_b32 s3, s3, s2\ns_cmp_lg_u64 s[46:47], 0\ns_cselect_b32 s2, 2, 0\n"
"s_or_b32 s3, s3, s2\ns_cmp_lg_u64 s[48:49], 0\ns_cselect_b32 s2, 4, 0\ns_or_b32 s3, s3, s2\ns_cmp_lg_u64 s[50:51], 0\ns_cselect_b32 s2, 8, 0\ns_or_b32 s3, s3, s2\n"
"s_cmp_lg_u64 s[52:53], 0\ns_cselect_b32 s2, 16, 0\ns_or_b32 s3, s3, s2\ns_cmp_lg_u64 s[54:55], 0\ns_cselect_b32 s2, 32, 0\ns_or_b32 s3, s3, s2\ns_cmp_lg_u64 s[56:57], 0\n"
"s_cselect_b32 s2, 64, 0\ns_or_b32 s3, s3, s2\ns_cmp_lg_u64 s[58:59], 0\ns_cselect_b32 s2, 128, 0\ns_or_b32 s3, s3, s2\ns_cbranch_scc0 .Z128\ns_sub_u32 s7, s7, 1\n"
"s_cbranch_scc0 .Z127\n.Z128:\nZvvv 136,130,96\nMvvv 136,130,104\nMvvv 136,130,112\nMvvv 136,130,120\nMvvv 136,130,32\nMvvv 136,130,40\nMvvv 136,130,48\nMvvv 136,130,56\n"
"Zvvv 140,128,96\nMvvv 140,128,104\nMvvv 140,128,112\nMvvv 140,128,120\nMvvv 140,128,32\nMvvv 140,128,40\nMvvv 140,128,48\nMvvv 140,128,56\nZvvv 144,130,100\nMvvv 144,130,108\n"
"Mvvv 144,130,116\nMvvv 144,130,124\nMvvv 144,130,36\nMvvv 144,130,44\nMvvv 144,130,52\nMvvv 144,130,60\nZvvv 148,128,100\nMvvv 148,128,108\nMvvv 148,128,116\nMvvv 148,128,124\n"
"Mvvv 148,128,36\nMvvv 148,128,44\nMvvv 148,128,52\nMvvv 148,128,60\nFM 64,80,136\nFM 65,80,137\nFM 66,80,138\nFM 67,80,139\nFM 68,80,140\nFM 69,80,141\nFM 70,80,142\nFM 71,80,143\n"
"CV 12,64,65\nCV 13,66,67\nCV 14,68,69\nCV 15,70,71\nSv 12,91,36,91,0\nFM 72,81,144\nFM 73,81,145\nFM 74,81,146\nFM 75,81,147\nFM 76,81,148\nFM 77,81,149\nFM 78,81,150\n"
"FM 79,81,151\nCV 16,72,73\nCV 17,74,75\nCV 18,76,77\nCV 19,78,79\nSv 16,91,36,1,0\n.Z126:\ns_endpgm\n.T63:\ns_mov_b32 s62, 1\ns_call_b64 s[80:81], .RA12\ns_branch .T64\n.T65:\n"
"s_mov_b32 s62, 1\ns_call_b64 s[80:81], .RA12\ns_branch .T66\n.T67:\ns_mov_b32 s62, 0\ns_call_b64 s[80:81], .RB13\ns_branch .T68\n.T69:\ns_mov_b32 s62, 0\n"
"s_call_b64 s[80:81], .RA12\ns_branch .T70\n.T71:\ns_mov_b32 s62, 0\ns_call_b64 s[80:81], .RB13\ns_branch .T72\n.T73:\ns_mov_b32 s62, 0\ns_call_b64 s[80:81], .RA12\ns_branch .T74\n"
".S75:\ns_mov_b32 s7, 262144\n.S77:\ns_sleep 1\nbuffer_load_dword v27, v1, s[16:19], s58 offen offset:4 sc0 sc1\nWV 0\nv_cmp_ne_u32 vcc, s15, v27\ns_and_b32 s2, s12, 3\n"
"s_lshl_b32 s2, s2, 4\ns_nop 1\ns_lshr_b64 vcc, vcc, s2\ns_and_b64 vcc, vcc, 0xff00\ns_cbranch_vccz .S76\ns_sub_u32 s7, s7, 1\ns_cbranch_scc0 .S77\ns_branch .S76\n.S78:\n"
"s_mov_b32 s7, 262144\n.S80:\ns_sleep 1\nbuffer_load_dword v27, v1, s[16:19], s58 offen offset:8 sc0 sc1\nWV 0\nv_cmp_ne_u32 vcc, s15, v27\ns_and_b32 s2, s12, 3\n"
"s_lshl_b32 s2, s2, 4\ns_nop 1\ns_lshr_b64 vcc, vcc, s2\ns_and_b64 vcc, vcc, 0xff00\ns_cbranch_vccz .S79\ns_sub_u32 s7, s7, 1\ns_cbranch_scc0 .S80\ns_branch .S79\n.T92:\n"
"s_mov_b32 s62, 0\ns_call_b64 s[80:81], .RB13\ns_branch .T93\n.T94:\ns_mov_b32 s62, 0\ns_call_b64 s[80:81], .RA12\ns_branch .T95\n.E96:\nv_mov_b32 v12, s97\nv_mov_b32 v13, s97\n"
"v_mov_b32 v14, s97\nv_mov_b32 v15, s97\nv_cmpx_gt_i32 vcc, 3, v3\nv_mov_b32 v7, s97\nv_mov_b32 v19, s97\nv_cmpx_gt_i32 vcc, 2, v3\nv_mov_b32 v6, s97\nv_mov_b32 v18, s97\n"
"v_cmpx_gt_i32 vcc, 1, v3\nv_mov_b32 v5, s97\nv_mov_b32 v17, s97\nv_cmpx_gt_i32 vcc, 0, v3\nv_mov_b32 v4, s97\nv_mov_b32 v16, s97\nv_cmpx_gt_i32 vcc, -16, v3\nv_mov_b32 v8, s97\n"
"v_mov_b32 v9, s97\nv_mov_b32 v10, s97\nv_mov_b32 v11, s97\ns_mov_b64 exec, -1\ns_branch .E97\n.T98:\ns_mov_b32 s62, 0\ns_call_b64 s[80:81], .RB13\ns_branch .T99\n.E100:\n"
"v_mov_b32 v12, s97\nv_mov_b32 v13, s97\nv_mov_b32 v14, s97\nv_mov_b32 v15, s97\nv_cmpx_gt_i32 vcc, 3, v0\nv_mov_b32 v7, s97\nv_mov_b32 v19, s97\nv_cmpx_gt_i32 vcc, 2, v0\n"
"v_mov_b32 v6, s97\nv_mov_b32 v18, s97\nv_cmpx_gt_i32 vcc, 1, v0\nv_mov_b32 v5, s97\nv_mov_b32 v17, s97\nv_cmpx_gt_i32 vcc, 0, v0\nv_mov_b32 v4, s97\nv_mov_b32 v16, s97\n"
"v_cmpx_gt_i32 vcc, -16, v0\nv_mov_b32 v8, s97\nv_mov_b32 v9, s97\nv_mov_b32 v10, s97\nv_mov_b32 v11, s97\ns_mov_b64 exec, -1\ns_branch .E101\n.T102:\ns_mov_b32 s62, 0\n"
"s_call_b64 s[80:81], .RA12\ns_branch .T103\n.E105:\nbuffer_load_dwordx4 v[100:103], v108, s[16:19], s65 offen offset:0 sc0 sc1\n"
"buffer_load_dwordx4 v[104:107], v108, s[16:19], s65 offen offset:256 sc0 sc1\ns_branch .E106\n.S111:\ns_mov_b32 s7, 262144\n.S113:\ns_sleep 1\n"
"buffer_load_dwordx4 v[100:103], v108, s[16:19], s65 offen offset:0 sc0 sc1\nbuffer_load_dwordx4 v[104:107], v108, s[16:19], s65 offen offset:256 sc0 sc1\nWV 0\n"
"v_xor_b32 v109, s15, v102\nv_xor_b32 v110, s15, v106\nv_or_b32 v109, v109, v110\nv_cmp_ne_u32 vcc, 0, v109\ns_nop 0\ns_cbranch_vccz .S112\ns_sub_u32 s7, s7, 1\n"
"s_cbranch_scc0 .S113\ns_branch .S112\n.D107:\nWV 0\nv_xor_b32 v109, s15, v102\nv_xor_b32 v110, s15, v106\nv_or_b32 v109, v109, v110\nv_cmp_ne_u32 vcc, 0, v109\ns_nop 0\n"
"s_cbranch_vccnz .S111\n.S112:\ns_add_u32 s5, s66, 0\nbuffer_load_dwordx4 a[192:195], v1, s[16:19], s5 offen offset:0 sc1\n"
"buffer_load_dwordx4 a[196:199], v1, s[16:19], s5 offen offset:1024 sc1\nbuffer_load_dwordx4 a[200:203], v1, s[16:19], s5 offen offset:2048 sc1\n"
"buffer_load_dwordx4 a[204:207], v1, s[16:19], s5 offen offset:3072 sc1\ns_add_u32 s5, s66, 4096\nbuffer_load_dwordx4 a[208:211], v1, s[16:19], s5 offen offset:0 sc1\n"
"buffer_load_dwordx4 a[212:215], v1, s[16:19], s5 offen offset:1024 sc1\nbuffer_load_dwordx4 a[216:219], v1, s[16:19], s5 offen offset:2048 sc1\n"
"buffer_load_dwordx4 a[220:223], v1, s[16:19], s5 offen offset:3072 sc1\ns_add_u32 s5, s66, 8192\nbuffer_load_dwordx4 a[224:227], v1, s[16:19], s5 offen offset:0 sc1\n"
"buffer_load_dwordx4 a[228:231], v1, s[16:19], s5 offen offset:1024 sc1\nbuffer_load_dwordx4 a[232:235], v1, s[16:19], s5 offen offset:2048 sc1\n"
"buffer_load_dwordx4 a[236:239], v1, s[16:19], s5 offen offset:3072 sc1\ns_add_u32 s5, s66, 12288\nbuffer_load_dwordx4 a[240:243], v1, s[16:19], s5 offen offset:0 sc1\n"
"buffer_load_dwordx4 a[244:247], v1, s[16:19], s5 offen offset:1024 sc1\nbuffer_load_dwordx4 a[248:251], v1, s[16:19], s5 offen offset:2048 sc1\n"
"buffer_load_dwordx4 a[252:255], v1, s[16:19], s5 offen offset:3072 sc1\nWV 0\ns_mov_b32 s67, 0\ns_branch .D108\n.D109:\nv_mov_b32 v130, 0\nv_mov_b32 v131, 0\n"
"s_mov_b32 s63, 0xff800000\ns_mov_b32 s67, 1\ns_branch .D110\n.T114:\ns_lshl3_add_u32 s62, s67, 2\ns_call_b64 s[80:81], .RB13\ns_cmp_eq_u32 s67, 0\ns_cbranch_scc1 .T115\n"
"SU 20,100,88\nv_min_f32 v20, 0, v20\nEX 20,20\ns_nop 0\nFM 86,101,20\nSU 21,104,89\nv_min_f32 v21, 0, v21\nEX 21,21\ns_nop 0\nFM 87,105,21\nAR 22,192\n"
"v_and_b32 v23, 0xffff0000, v22\nv_lshlrev_b32 v22, 16, v22\nMU 22,22,20\nMU 23,23,20\nCV 22,22,23\nAW 192,22\nAR 22,193\nv_and_b32 v23, 0xffff0000, v22\n"
"v_lshlrev_b32 v22, 16, v22\nMU 22,22,20\nMU 23,23,20\nCV 22,22,23\nAW 193,22\nAR 22,194\nv_and_b32 v23, 0xffff0000, v22\nv_lshlrev_b32 v22, 16, v22\nMU 22,22,20\nMU 23,23,20\n"
"CV 22,22,23\nAW 194,22\nAR 22,195\nv_and_b32 v23, 0xffff0000, v22\nv_lshlrev_b32 v22, 16, v22\nMU 22,22,20\nMU 23,23,20\nCV 22,22,23\nAW 195,22\nAR 22,196\n"
"v_and_b32 v23, 0xffff0000, v22\nv_lshlrev_b32 v22, 16, v22\nMU 22,22,21\nMU 23,23,21\nCV 22,22,23\nAW 196,22\nAR 22,197\nv_and_b32 v23, 0xffff0000, v22\n"
"v_lshlrev_b32 v22, 16, v22\nMU 22,22,21\nMU 23,23,21\nCV 22,22,23\nAW 197,22\nAR 22,198\nv_and_b32 v23, 0xffff0000, v22\nv_lshlrev_b32 v22, 16, v22\nMU 22,22,21\nMU 23,23,21\n"
"CV 22,22,23\nAW 198,22\nAR 22,199\nv_and_b32 v23, 0xffff0000, v22\nv_lshlrev_b32 v22, 16, v22\nMU 22,22,21\nMU 23,23,21\nCV 22,22,23\nAW 199,22\nAR 22,200\n"
"v_and_b32 v23, 0xffff0000, v22\nv_lshlrev_b32 v22, 16, v22\nMU 22,22,20\nMU 23,23,20\nCV 22,22,23\nAW 200,22\nAR 22,201\nv_and_b32 v23, 0xffff0000, v22\n"
"v_lshlrev_b32 v22, 16, v22\nMU 22,22,20\nMU 23,23,20\nCV 22,22,23\nAW 201,22\nAR 22,202\nv_and_b32 v23, 0xffff0000, v22\nv_lshlrev_b32 v22, 16, v22\nMU 22,22,20\nMU 23,23,20\n"
"CV 22,22,23\nAW 202,22\nAR 22,203\nv_and_b32 v23, 0xffff0000, v22\nv_lshlrev_b32 v22, 16, v22\nMU 22,22,20\nMU 23,23,20\nCV 22,22,23\nAW 203,22\nAR 22,204\n"
"v_and_b32 v23, 0xffff0000, v22\nv_lshlrev_b32 v22, 16, v22\nMU 22,22,21\nMU 23,23,21\nCV 22,22,23\nAW 204,22\nAR 22,205\nv_and_b32 v23, 0xffff0000, v22\n"
"v_lshlrev_b32 v22, 16, v22\nMU 22,22,21\nMU 23,23,21\nCV 22,22,23\nAW 205,22\nAR 22,206\nv_and_b32 v23, 0xffff0000, v22\nv_lshlrev_b32 v22, 16, v22\nMU 22,22,21\nMU 23,23,21\n"
"CV 22,22,23\nAW 206,22\nAR 22,207\nv_and_b32 v23, 0xffff0000, v22\nv_lshlrev_b32 v22, 16, v22\nMU 22,22,21\nMU 23,23,21\nCV 22,22,23\nAW 207,22\nAR 22,208\n"
"v_and_b32 v23, 0xffff0000, v22\nv_lshlrev_b32 v22, 16, v22\nMU 22,22,20\nMU 23,23,20\nCV 22,22,23\nAW 208,22\nAR 22,209\nv_and_b32 v23, 0xffff0000, v22\n"
"v_lshlrev_b32 v22, 16, v22\nMU 22,22,20\nMU 23,23,20\nCV 22,22,23\nAW 209,22\nAR 22,210\nv_and_b32 v23, 0xffff0000, v22\nv_lshlrev_b32 v22, 16, v22\nMU 22,22,20\nMU 23,23,20\n"
"CV 22,22,23\nAW 210,22\nAR 22,211\nv_and_b32 v23, 0xffff0000, v22\nv_lshlrev_b32 v22, 16, v22\nMU 22,22,20\nMU 23,23,20\nCV 22,22,23\nAW 211,22\nAR 22,212\n"
"v_and_b32 v23, 0xffff0000, v22\nv_lshlrev_b32 v22, 16, v22\nMU 22,22,21\nMU 23,23,21\nCV 22,22,23\nAW 212,22\nAR 22,213\nv_and_b32 v23, 0xffff0000, v22\n"
"v_lshlrev_b32 v22, 16, v22\nMU 22,22,21\nMU 23,23,21\nCV 22,22,23\nAW 213,22\nAR 22,214\nv_and_b32 v23, 0xffff0000, v22\nv_lshlrev_b32 v22, 16, v22\nMU 22,22,21\nMU 23,23,21\n"
"CV 22,22,23\nAW 214,22\nAR 22,215\nv_and_b32 v23, 0xffff0000, v22\nv_lshlrev_b32 v22, 16, v22\nMU 22,22,21\nMU 23,23,21\nCV 22,22,23\nAW 215,22\nAR 22,216\n"
"v_and_b32 v23, 0xffff0000, v22\nv_lshlrev_b32 v22, 16, v22\nMU 22,22,20\nMU 23,23,20\nCV 22,22,23\nAW 216,22\nAR 22,217\nv_and_b32 v23, 0xffff0000, v22\n"
"v_lshlrev_b32 v22, 16, v22\nMU 22,22,20\nMU 23,23,20\nCV 22,22,23\nAW 217,22\nAR 22,218\nv_and_b32 v23, 0xffff0000, v22\nv_lshlrev_b32 v22, 16, v22\nMU 22,22,20\nMU 23,23,20\n"
"CV 22,22,23\nAW 218,22\nAR 22,219\nv_and_b32 v23, 0xffff0000, v22\nv_lshlrev_b32 v22, 16, v22\nMU 22,22,20\nMU 23,23,20\nCV 22,22,23\nAW 219,22\nAR 22,220\n"
"v_and_b32 v23, 0xffff0000, v22\nv_lshlrev_b32 v22, 16, v22\nMU 22,22,21\nMU 23,23,21\nCV 22,22,23\nAW 220,22\nAR 22,221\nv_and_b32 v23, 0xffff0000, v22\n"
"v_lshlrev_b32 v22, 16, v22\nMU 22,22,21\nMU 23,23,21\nCV 22,22,23\nAW 221,22\nAR 22,222\nv_and_b32 v23, 0xffff0000, v22\nv_lshlrev_b32 v22, 16, v22\nMU 22,22,21\nMU 23,23,21\n"
"CV 22,22,23\nAW 222,22\nAR 22,223\nv_and_b32 v23, 0xffff0000, v22\nv_lshlrev_b32 v22, 16, v22\nMU 22,22,21\nMU 23,23,21\nCV 22,22,23\nAW 223,22\nAR 22,224\n"
"v_and_b32 v23, 0xffff0000, v22\nv_lshlrev_b32 v22, 16, v22\nMU 22,22,20\nMU 23,23,20\nCV 22,22,23\nAW 224,22\nAR 22,225\nv_and_b32 v23, 0xffff0000, v22\n"
"v_lshlrev_b32 v22, 16, v22\nMU 22,22,20\nMU 23,23,20\nCV 22,22,23\nAW 225,22\nAR 22,226\nv_and_b32 v23, 0xffff0000, v22\nv_lshlrev_b32 v22, 16, v22\nMU 22,22,20\nMU 23,23,20\n"
"CV 22,22,23\nAW 226,22\nAR 22,227\nv_and_b32 v23, 0xffff0000, v22\nv_lshlrev_b32 v22, 16, v22\nMU 22,22,20\nMU 23,23,20\nCV 22,22,23\nAW 227,22\nAR 22,228\n"
"v_and_b32 v23, 0xffff0000, v22\nv_lshlrev_b32 v22, 16, v22\nMU 22,22,21\nMU 23,23,21\nCV 22,22,23\nAW 228,22\nAR 22,229\nv_and_b32 v23, 0xffff0000, v22\n"
"v_lshlrev_b32 v22, 16, v22\nMU 22,22,21\nMU 23,23,21\nCV 22,22,23\nAW 229,22\nAR 22,230\nv_and_b32 v23, 0xffff0000, v22\nv_lshlrev_b32 v22, 16, v22\nMU 22,22,21\nMU 23,23,21\n"
"CV 22,22,23\nAW 230,22\nAR 22,231\nv_and_b32 v23, 0xffff0000, v22\nv_lshlrev_b32 v22, 16, v22\nMU 22,22,21\nMU 23,23,21\nCV 22,22,23\nAW 231,22\nAR 22,232\n"
"v_and_b32 v23, 0xffff0000, v22\nv_lshlrev_b32 v22, 16, v22\nMU 22,22,20\nMU 23,23,20\nCV 22,22,23\nAW 232,22\nAR 22,233\nv_and_b32 v23, 0xffff0000, v22\n"
"v_lshlrev_b32 v22, 16, v22\nMU 22,22,20\nMU 23,23,20\nCV 22,22,23\nAW 233,22\nAR 22,234\nv_and_b32 v23, 0xffff0000, v22\nv_lshlrev_b32 v22, 16, v22\nMU 22,22,20\nMU 23,23,20\n"
"CV 22,22,23\nAW 234,22\nAR 22,235\nv_and_b32 v23, 0xffff0000, v22\nv_lshlrev_b32 v22, 16, v22\nMU 22,22,20\nMU 23,23,20\nCV 22,22,23\nAW 235,22\nAR 22,236\n"
"v_and_b32 v23, 0xffff0000, v22\nv_lshlrev_b32 v22, 16, v22\nMU 22,22,21\nMU 23,23,21\nCV 22,22,23\nAW 236,22\nAR 22,237\nv_and_b32 v23, 0xffff0000, v22\n"
"v_lshlrev_b32 v22, 16, v22\nMU 22,22,21\nMU 23,23,21\nCV 22,22,23\nAW 237,22\nAR 22,238\nv_and_b32 v23, 0xffff0000, v22\nv_lshlrev_b32 v22, 16, v22\nMU 22,22,21\nMU 23,23,21\n"
"CV 22,22,23\nAW 238,22\nAR 22,239\nv_and_b32 v23, 0xffff0000, v22\nv_lshlrev_b32 v22, 16, v22\nMU 22,22,21\nMU 23,23,21\nCV 22,22,23\nAW 239,22\nAR 22,240\n"
"v_and_b32 v23, 0xffff0000, v22\nv_lshlrev_b32 v22, 16, v22\nMU 22,22,20\nMU 23,23,20\nCV 22,22,23\nAW 240,22\nAR 22,241\nv_and_b32 v23, 0xffff0000, v22\n"
"v_lshlrev_b32 v22, 16, v22\nMU 22,22,20\nMU 23,23,20\nCV 22,22,23\nAW 241,22\nAR 22,242\nv_and_b32 v23, 0xffff0000, v22\nv_lshlrev_b32 v22, 16, v22\nMU 22,22,20\nMU 23,23,20\n"
"CV 22,22,23\nAW 242,22\nAR 22,243\nv_and_b32 v23, 0xffff0000, v22\nv_lshlrev_b32 v22, 16, v22\nMU 22,22,20\nMU 23,23,20\nCV 22,22,23\nAW 243,22\nAR 22,244\n"
"v_and_b32 v23, 0xffff0000, v22\nv_lshlrev_b32 v22, 16, v22\nMU 22,22,21\nMU 23,23,21\nCV 22,22,23\nAW 244,22\nAR 22,245\nv_and_b32 v23, 0xffff0000, v22\n"
"v_lshlrev_b32 v22, 16, v22\nMU 22,22,21\nMU 23,23,21\nCV 22,22,23\nAW 245,22\nAR 22,246\nv_and_b32 v23, 0xffff0000, v22\nv_lshlrev_b32 v22, 16, v22\nMU 22,22,21\nMU 23,23,21\n"
"CV 22,22,23\nAW 246,22\nAR 22,247\nv_and_b32 v23, 0xffff0000, v22\nv_lshlrev_b32 v22, 16, v22\nMU 22,22,21\nMU 23,23,21\nCV 22,22,23\nAW 247,22\nAR 22,248\n"
"v_and_b32 v23, 0xffff0000, v22\nv_lshlrev_b32 v22, 16, v22\nMU 22,22,20\nMU 23,23,20\nCV 22,22,23\nAW 248,22\nAR 22,249\nv_and_b32 v23, 0xffff0000, v22\n"
"v_lshlrev_b32 v22, 16, v22\nMU 22,22,20\nMU 23,23,20\nCV 22,22,23\nAW 249,22\nAR 22,250\nv_and_b32 v23, 0xffff0000, v22\nv_lshlrev_b32 v22, 16, v22\nMU 22,22,20\nMU 23,23,20\n"
"CV 22,22,23\nAW 250,22\nAR 22,251\nv_and_b32 v23, 0xffff0000, v22\nv_lshlrev_b32 v22, 16, v22\nMU 22,22,20\nMU 23,23,20\nCV 22,22,23\nAW 251,22\nAR 22,252\n"
"v_and_b32 v23, 0xffff0000, v22\nv_lshlrev_b32 v22, 16, v22\nMU 22,22,21\nMU 23,23,21\nCV 22,22,23\nAW 252,22\nAR 22,253\nv_and_b32 v23, 0xffff0000, v22\n"
"v_lshlrev_b32 v22, 16, v22\nMU 22,22,21\nMU 23,23,21\nCV 22,22,23\nAW 253,22\nAR 22,254\nv_and_b32 v23, 0xffff0000, v22\nv_lshlrev_b32 v22, 16, v22\nMU 22,22,21\nMU 23,23,21\n"
"CV 22,22,23\nAW 254,22\nAR 22,255\nv_and_b32 v23, 0xffff0000, v22\nv_lshlrev_b32 v22, 16, v22\nMU 22,22,21\nMU 23,23,21\nCV 22,22,23\nAW 255,22\nv_lshrrev_b32 v22, 8, v1\n"
"v_bfe_u32 v23, v1, 6, 2\nv_cmp_eq_u32 vcc, v22, v23\nv_and_b32 v22, 16, v1\nv_mov_b32 v23, 0x3f80\nv_cndmask_b32 v23, 0, v23, vcc\nv_lshlrev_b32 v23, v22, v23\n"
"v_and_b32 v22, 32, v1\nv_cmp_eq_u32 vcc, 0, v22\nv_mov_b32 v128, 0\nv_mov_b32 v129, 0\nv_cndmask_b32 v130, 0, v23, vcc\nv_sub_u32 v131, v23, v130\nv_mov_b32 v132, 0\n"
"v_mov_b32 v133, 0\ns_nop 1\nMava 0,130,192\nMava 8,128,192\nMava 12,128,196\nMava 4,130,196\nMava 16,130,200\nMava 24,128,200\nMava 28,128,204\nMava 20,130,204\nMava 32,130,208\n"
"Mava 40,128,208\nMava 44,128,212\nMava 36,130,212\nMava 48,130,216\nMava 56,128,216\nMava 60,128,220\nMava 52,130,220\nMava 64,130,224\nMava 72,128,224\nMava 76,128,228\n"
"Mava 68,130,228\nMava 80,130,232\nMava 88,128,232\nMava 92,128,236\nMava 84,130,236\nMava 96,130,240\nMava 104,128,240\nMava 108,128,244\nMava 100,130,244\nMava 112,130,248\n"
"Mava 120,128,248\nMava 124,128,252\nMava 116,130,252\ns_nop 7\ns_branch .T115\n.S117:\ns_mov_b32 s7, 262144\n.S119:\ns_sleep 1\ns_add_u32 s4, s92, 0\n"
"buffer_load_dwordx4 a[192:195], v1, s[16:19], s4 offen offset:0 sc0 sc1\nbuffer_load_dwordx4 a[196:199], v1, s[16:19], s4 offen offset:1024 sc0 sc1\n"
"buffer_load_dwordx4 a[200:203], v1, s[16:19], s4 offen offset:2048 sc0 sc1\nbuffer_load_dwordx4 a[204:207], v1, s[16:19], s4 offen offset:3072 sc0 sc1\ns_add_u32 s4, s92, 4096\n"
"buffer_load_dwordx4 a[208:211], v1, s[16:19], s4 offen offset:0 sc0 sc1\nbuffer_load_dwordx4 a[212:215], v1, s[16:19], s4 offen offset:1024 sc0 sc1\n"
"buffer_load_dwordx4 a[216:219], v1, s[16:19], s4 offen offset:2048 sc0 sc1\nbuffer_load_dwordx4 a[220:223], v1, s[16:19], s4 offen offset:3072 sc0 sc1\nWV 0\nAR 22,195\nAR 23,199\n"
"v_max_u32 v21, v22, v23\nAR 22,203\nAR 23,207\nv_max3_u32 v21, v21, v22, v23\nAR 22,211\nAR 23,215\nv_max3_u32 v21, v21, v22, v23\nAR 22,219\nAR 23,223\n"
"v_max3_u32 v21, v21, v22, v23\nv_cmp_eq_u32 vcc, -1, v21\ns_nop 0\ns_cbranch_vccz .S118\ns_sub_u32 s7, s7, 1\ns_cbranch_scc0 .S119\ns_branch .S118\n.S120:\ns_mov_b32 s7, 262144\n"
".S122:\ns_sleep 1\ns_add_u32 s4, s92, 8192\nbuffer_load_dwordx4 a[224:227], v1, s[16:19], s4 offen offset:0 sc0 sc1\n"
"buffer_load_dwordx4 a[228:231], v1, s[16:19], s4 offen offset:1024 sc0 sc1\nbuffer_load_dwordx4 a[232:235], v1, s[16:19], s4 offen offset:2048 sc0 sc1\n"
"buffer_load_dwordx4 a[236:239], v1, s[16:19], s4 offen offset:3072 sc0 sc1\ns_add_u32 s4, s92, 12288\nbuffer_load_dwordx4 a[240:243], v1, s[16:19], s4 offen offset:0 sc0 sc1\n"
"buffer_load_dwordx4 a[244:247], v1, s[16:19], s4 offen offset:1024 sc0 sc1\nbuffer_load_dwordx4 a[248:251], v1, s[16:19], s4 offen offset:2048 sc0 sc1\n"
"buffer_load_dwordx4 a[252:255], v1, s[16:19], s4 offen offset:3072 sc0 sc1\nWV 0\nAR 22,227\nAR 23,231\nv_max_u32 v21, v22, v23\nAR 22,235\nAR 23,239\n"
"v_max3_u32 v21, v21, v22, v23\nAR 22,243\nAR 23,247\nv_max3_u32 v21, v21, v22, v23\nAR 22,251\nAR 23,255\nv_max3_u32 v21, v21, v22, v23\nv_cmp_eq_u32 vcc, -1, v21\ns_nop 0\n"
"s_cbranch_vccz .S121\ns_sub_u32 s7, s7, 1\ns_cbranch_scc0 .S122\ns_branch .S121\n.S123:\ns_mov_b32 s7, 262144\n.S125:\ns_sleep 1\n"
"buffer_load_dwordx4 v[64:67], v89, s[16:19], s0 offen offset:0 sc0 sc1\nbuffer_load_dwordx4 v[68:71], v89, s[16:19], s0 offen offset:16 sc0 sc1\n"
"buffer_load_dwordx4 v[72:75], v89, s[16:19], s0 offen offset:1792 sc0 sc1\nbuffer_load_dwordx4 v[76:79], v89, s[16:19], s0 offen offset:1808 sc0 sc1\nWV 0\n"
"v_xor_b32 v92, s15, v66\nv_xor_b32 v93, s15, v70\nv_xor_b32 v94, s15, v74\nv_xor_b32 v95, s15, v78\nv_or3_b32 v92, v92, v93, v94\nv_or_b32 v92, v92, v95\nv_cmp_ne_u32 vcc, 0, v92\n"
"s_nop 0\ns_cbranch_vccz .S124\ns_sub_u32 s7, s7, 1\ns_cbranch_scc0 .S125\ns_branch .S124\n.RA12:\ns_nop 7\ns_nop 3\nv_max3_f32 v22, v32, v33, v34\nv_max3_f32 v22, v22, v35, v40\n"
"v_max3_f32 v22, v22, v41, v42\nv_max_f32 v22, v22, v43\nv_mov_b32 v91, v22\ns_nop 1\nv_permlane32_swap_b32 v22, v91\nv_max_f32 v22, v22, v91\nv_mov_b32 v91, v22\ns_nop 1\n"
"v_permlane16_swap_b32 v22, v91\nv_max_f32 v22, v22, v91\nv_ceil_f32 v22, v22\nv_max3_f32 v23, v36, v37, v38\nv_max3_f32 v23, v23, v39, v44\nv_max3_f32 v23, v23, v45, v46\n"
"v_max_f32 v23, v23, v47\nv_mov_b32 v91, v23\ns_nop 1\nv_permlane32_swap_b32 v23, v91\nv_max_f32 v23, v23, v91\nv_mov_b32 v91, v23\ns_nop 1\nv_permlane16_swap_b32 v23, v91\n"
"v_max_f32 v23, v23, v91\nv_ceil_f32 v23, v23\ns_bitcmp1_b32 s62, 0\ns_cbranch_scc0 .R137\nv_cmp_lt_f32 vcc, 0xff000000, v22\ns_nop 1\nv_cndmask_b32 v22, 0, v22, vcc\n"
"v_cmp_lt_f32 vcc, 0xff000000, v23\ns_nop 1\nv_cndmask_b32 v23, 0, v23, vcc\ns_branch .R138\n.R137:\nv_max_f32 v22, 0, v22\nv_max_f32 v23, 0, v23\ns_bitcmp1_b32 s62, 3\n"
"s_cbranch_scc0 .R138\nv_max_f32 v22, v20, v22\nv_max_f32 v23, v21, v23\n.R138:\nAD 88,88,22\nAD 89,89,23\nSU 32,32,22\nSU 33,33,22\nSU 34,34,22\nSU 35,35,22\nSU 36,36,23\n"
"SU 37,37,23\nSU 38,38,23\nSU 39,39,23\nSU 40,40,22\nSU 41,41,22\nSU 42,42,22\nSU 43,43,22\nSU 44,44,23\nSU 45,45,23\nSU 46,46,23\nSU 47,47,23\nv_mov_b32 v90, v80\n"
"v_mov_b32 v91, v81\nEX 80,32\nEX 81,33\ns_nop 0\nAD 84,80,81\nCV 64,80,81\nEX 82,34\nEX 83,35\nAD 84,84,82\nAD 84,84,83\nCV 65,82,83\nEX 80,36\nEX 81,37\ns_nop 0\nAD 85,80,81\n"
"CV 68,80,81\nEX 82,38\nEX 83,39\nAD 85,85,82\nAD 85,85,83\nCV 69,82,83\nEX 80,40\nEX 81,41\nAD 84,84,80\nAD 84,84,81\nCV 66,80,81\nEX 82,42\nEX 83,43\nAD 84,84,82\nAD 84,84,83\n"
"CV 67,82,83\nEX 80,44\nEX 81,45\nAD 85,85,80\nAD 85,85,81\nCV 70,80,81\nEX 82,46\nEX 83,47\nAD 85,85,82\nAD 85,85,83\nCV 71,82,83\nv_mov_b32 v80, v90\nv_mov_b32 v81, v91\n"
"s_bitcmp1_b32 s62, 1\ns_cbranch_scc1 .R139\nSU 48,48,22\nSU 49,49,22\nSU 50,50,22\nSU 51,51,22\nSU 52,52,23\nSU 53,53,23\nSU 54,54,23\nSU 55,55,23\nSU 56,56,22\nSU 57,57,22\n"
"SU 58,58,22\nSU 59,59,22\nSU 60,60,23\nSU 61,61,23\nSU 62,62,23\nSU 63,63,23\nSU 4,4,22\nSU 5,5,22\nSU 6,6,22\nSU 7,7,22\nSU 8,8,23\nSU 9,9,23\nSU 10,10,23\nSU 11,11,23\n"
"SU 12,12,22\nSU 13,13,22\nSU 14,14,22\nSU 15,15,22\nSU 16,16,23\nSU 17,17,23\nSU 18,18,23\nSU 19,19,23\nEX 80,48\nEX 81,49\n.R139:\ns_bitcmp1_b32 s62, 0\ns_cbranch_scc0 .RS9\n"
"s_nop 1\ns_setpc_b64 s[80:81]\n.RB13:\ns_nop 7\ns_nop 3\nv_max3_f32 v22, v48, v49, v50\nv_max3_f32 v22, v22, v51, v56\nv_max3_f32 v22, v22, v57, v58\nv_max_f32 v22, v22, v59\n"
"v_mov_b32 v91, v22\ns_nop 1\nv_permlane32_swap_b32 v22, v91\nv_max_f32 v22, v22, v91\nv_mov_b32 v91, v22\ns_nop 1\nv_permlane16_swap_b32 v22, v91\nv_max_f32 v22, v22, v91\n"
"v_ceil_f32 v22, v22\nv_max3_f32 v23, v52, v53, v54\nv_max3_f32 v23, v23, v55, v60\nv_max3_f32 v23, v23, v61, v62\nv_max_f32 v23, v23, v63\nv_mov_b32 v91, v23\ns_nop 1\n"
"v_permlane32_swap_b32 v23, v91\nv_max_f32 v23, v23, v91\nv_mov_b32 v91, v23\ns_nop 1\nv_permlane16_swap_b32 v23, v91\nv_max_f32 v23, v23, v91\nv_ceil_f32 v23, v23\n"
"s_bitcmp1_b32 s62, 0\ns_cbranch_scc0 .R142\nv_cmp_lt_f32 vcc, 0xff000000, v22\nv_cndmask_b32 v22, 0, v22, vcc\nv_cmp_lt_f32 vcc, 0xff000000, v23\nv_cndmask_b32 v23, 0, v23, vcc\n"
"s_branch .R143\n.R142:\nv_max_f32 v22, 0, v22\nv_max_f32 v23, 0, v23\ns_bitcmp1_b32 s62, 3\ns_cbranch_scc0 .R143\nv_max_f32 v22, v20, v22\nv_max_f32 v23, v21, v23\n.R143:\n"
"AD 88,88,22\nAD 89,89,23\nSU 48,48,22\nSU 49,49,22\nSU 50,50,22\nSU 51,51,22\nSU 52,52,23\nSU 53,53,23\nSU 54,54,23\nSU 55,55,23\nSU 56,56,22\nSU 57,57,22\nSU 58,58,22\n"
"SU 59,59,22\nSU 60,60,23\nSU 61,61,23\nSU 62,62,23\nSU 63,63,23\nv_mov_b32 v90, v80\nv_mov_b32 v91, v81\nEX 80,48\nEX 81,49\ns_nop 0\nAD 84,80,81\nCV 72,80,81\nEX 82,50\nEX 83,51\n"
"AD 84,84,82\nAD 84,84,83\nCV 73,82,83\nEX 80,52\nEX 81,53\ns_nop 0\nAD 85,80,81\nCV 76,80,81\nEX 82,54\nEX 83,55\nAD 85,85,82\nAD 85,85,83\nCV 77,82,83\nEX 80,56\nEX 81,57\n"
"AD 84,84,80\nAD 84,84,81\nCV 74,80,81\nEX 82,58\nEX 83,59\nAD 84,84,82\nAD 84,84,83\nCV 75,82,83\nEX 80,60\nEX 81,61\nAD 85,85,80\nAD 85,85,81\nCV 78,80,81\nEX 82,62\nEX 83,63\n"
"AD 85,85,82\nAD 85,85,83\nCV 79,82,83\nv_mov_b32 v80, v90\nv_mov_b32 v81, v91\ns_bitcmp1_b32 s62, 1\ns_cbranch_scc1 .R144\nSU 32,32,22\nSU 33,33,22\nSU 34,34,22\nSU 35,35,22\n"
"SU 36,36,23\nSU 37,37,23\nSU 38,38,23\nSU 39,39,23\nSU 40,40,22\nSU 41,41,22\nSU 42,42,22\nSU 43,43,22\nSU 44,44,23\nSU 45,45,23\nSU 46,46,23\nSU 47,47,23\nSU 4,4,22\nSU 5,5,22\n"
"SU 6,6,22\nSU 7,7,22\nSU 8,8,23\nSU 9,9,23\nSU 10,10,23\nSU 11,11,23\nSU 12,12,22\nSU 13,13,22\nSU 14,14,22\nSU 15,15,22\nSU 16,16,23\nSU 17,17,23\nSU 18,18,23\nSU 19,19,23\n"
"EX 80,32\nEX 81,33\n.R144:\ns_bitcmp1_b32 s62, 0\ns_cbranch_scc0 .RS9\ns_nop 1\ns_setpc_b64 s[80:81]\n.RS9:\nv_exp_f32 v22, -v22\nv_exp_f32 v23, -v23\nMU 86,86,22\nMU 87,87,23\n"
"s_bitcmp1_b32 s62, 2\ns_cbranch_scc1 .R147\nAR 91,0\nMU 91,91,22\nAW 0,91\nAR 91,1\nMU 91,91,22\nAW 1,91\nAR 91,2\nMU 91,91,22\nAW 2,91\nAR 91,3\nMU 91,91,22\nAW 3,91\nAR 91,4\n"
"MU 91,91,23\nAW 4,91\nAR 91,5\nMU 91,91,23\nAW 5,91\nAR 91,6\nMU 91,91,23\nAW 6,91\nAR 91,7\nMU 91,91,23\nAW 7,91\nAR 91,8\nMU 91,91,22\nAW 8,91\nAR 91,9\nMU 91,91,22\nAW 9,91\n"
"AR 91,10\nMU 91,91,22\nAW 10,91\nAR 91,11\nMU 91,91,22\nAW 11,91\nAR 91,12\nMU 91,91,23\nAW 12,91\nAR 91,13\nMU 91,91,23\nAW 13,91\nAR 91,14\nMU 91,91,23\nAW 14,91\nAR 91,15\n"
"MU 91,91,23\nAW 15,91\nAR 91,16\nMU 91,91,22\nAW 16,91\nAR 91,17\nMU 91,91,22\nAW 17,91\nAR 91,18\nMU 91,91,22\nAW 18,91\nAR 91,19\nMU 91,91,22\nAW 19,91\nAR 91,20\nMU 91,91,23\n"
"AW 20,91\nAR 91,21\nMU 91,91,23\nAW 21,91\nAR 91,22\nMU 91,91,23\nAW 22,91\nAR 91,23\nMU 91,91,23\nAW 23,91\nAR 91,24\nMU 91,91,22\nAW 24,91\nAR 91,25\nMU 91,91,22\nAW 25,91\n"
"AR 91,26\nMU 91,91,22\nAW 26,91\nAR 91,27\nMU 91,91,22\nAW 27,91\nAR 91,28\nMU 91,91,23\nAW 28,91\nAR 91,29\nMU 91,91,23\nAW 29,91\nAR 91,30\nMU 91,91,23\nAW 30,91\nAR 91,31\n"
"MU 91,91,23\nAW 31,91\nAR 91,32\nMU 91,91,22\nAW 32,91\nAR 91,33\nMU 91,91,22\nAW 33,91\nAR 91,34\nMU 91,91,22\nAW 34,91\nAR 91,35\nMU 91,91,22\nAW 35,91\nAR 91,36\nMU 91,91,23\n"
"AW 36,91\nAR 91,37\nMU 91,91,23\nAW 37,91\nAR 91,38\nMU 91,91,23\nAW 38,91\nAR 91,39\nMU 91,91,23\nAW 39,91\nAR 91,40\nMU 91,91,22\nAW 40,91\nAR 91,41\nMU 91,91,22\nAW 41,91\n"
"AR 91,42\nMU 91,91,22\nAW 42,91\nAR 91,43\nMU 91,91,22\nAW 43,91\nAR 91,44\nMU 91,91,23\nAW 44,91\nAR 91,45\nMU 91,91,23\nAW 45,91\nAR 91,46\nMU 91,91,23\nAW 46,91\nAR 91,47\n"
"MU 91,91,23\nAW 47,91\nAR 91,48\nMU 91,91,22\nAW 48,91\nAR 91,49\nMU 91,91,22\nAW 49,91\nAR 91,50\nMU 91,91,22\nAW 50,91\nAR 91,51\nMU 91,91,22\nAW 51,91\nAR 91,52\nMU 91,91,23\n"
"AW 52,91\nAR 91,53\nMU 91,91,23\nAW 53,91\nAR 91,54\nMU 91,91,23\nAW 54,91\nAR 91,55\nMU 91,91,23\nAW 55,91\nAR 91,56\nMU 91,91,22\nAW 56,91\nAR 91,57\nMU 91,91,22\nAW 57,91\n"
"AR 91,58\nMU 91,91,22\nAW 58,91\nAR 91,59\nMU 91,91,22\nAW 59,91\nAR 91,60\nMU 91,91,23\nAW 60,91\nAR 91,61\nMU 91,91,23\nAW 61,91\nAR 91,62\nMU 91,91,23\nAW 62,91\nAR 91,63\n"
"MU 91,91,23\nAW 63,91\nAR 91,64\nMU 91,91,22\nAW 64,91\nAR 91,65\nMU 91,91,22\nAW 65,91\nAR 91,66\nMU 91,91,22\nAW 66,91\nAR 91,67\nMU 91,91,22\nAW 67,91\nAR 91,68\nMU 91,91,23\n"
"AW 68,91\nAR 91,69\nMU 91,91,23\nAW 69,91\nAR 91,70\nMU 91,91,23\nAW 70,91\nAR 91,71\nMU 91,91,23\nAW 71,91\nAR 91,72\nMU 91,91,22\nAW 72,91\nAR 91,73\nMU 91,91,22\nAW 73,91\n"
"AR 91,74\nMU 91,91,22\nAW 74,91\nAR 91,75\nMU 91,91,22\nAW 75,91\nAR 91,76\nMU 91,91,23\nAW 76,91\nAR 91,77\nMU 91,91,23\nAW 77,91\nAR 91,78\nMU 91,91,23\nAW 78,91\nAR 91,79\n"
"MU 91,91,23\nAW 79,91\nAR 91,80\nMU 91,91,22\nAW 80,91\nAR 91,81\nMU 91,91,22\nAW 81,91\nAR 91,82\nMU 91,91,22\nAW 82,91\nAR 91,83\nMU 91,91,22\nAW 83,91\nAR 91,84\nMU 91,91,23\n"
"AW 84,91\nAR 91,85\nMU 91,91,23\nAW 85,91\nAR 91,86\nMU 91,91,23\nAW 86,91\nAR 91,87\nMU 91,91,23\nAW 87,91\nAR 91,88\nMU 91,91,22\nAW 88,91\nAR 91,89\nMU 91,91,22\nAW 89,91\n"
"AR 91,90\nMU 91,91,22\nAW 90,91\nAR 91,91\nMU 91,91,22\nAW 91,91\nAR 91,92\nMU 91,91,23\nAW 92,91\nAR 91,93\nMU 91,91,23\nAW 93,91\nAR 91,94\nMU 91,91,23\nAW 94,91\nAR 91,95\n"
"MU 91,91,23\nAW 95,91\nAR 91,96\nMU 91,91,22\nAW 96,91\nAR 91,97\nMU 91,91,22\nAW 97,91\nAR 91,98\nMU 91,91,22\nAW 98,91\nAR 91,99\nMU 91,91,22\nAW 99,91\nAR 91,100\nMU 91,91,23\n"
"AW 100,91\nAR 91,101\nMU 91,91,23\nAW 101,91\nAR 91,102\nMU 91,91,23\nAW 102,91\nAR 91,103\nMU 91,91,23\nAW 103,91\nAR 91,104\nMU 91,91,22\nAW 104,91\nAR 91,105\nMU 91,91,22\n"
"AW 105,91\nAR 91,106\nMU 91,91,22\nAW 106,91\nAR 91,107\nMU 91,91,22\nAW 107,91\nAR 91,108\nMU 91,91,23\nAW 108,91\nAR 91,109\nMU 91,91,23\nAW 109,91\nAR 91,110\nMU 91,91,23\n"
"AW 110,91\nAR 91,111\nMU 91,91,23\nAW 111,91\nAR 91,112\nMU 91,91,22\nAW 112,91\nAR 91,113\nMU 91,91,22\nAW 113,91\nAR 91,114\nMU 91,91,22\nAW 114,91\nAR 91,115\nMU 91,91,22\n"
"AW 115,91\nAR 91,116\nMU 91,91,23\nAW 116,91\nAR 91,117\nMU 91,91,23\nAW 117,91\nAR 91,118\nMU 91,91,23\nAW 118,91\nAR 91,119\nMU 91,91,23\nAW 119,91\nAR 91,120\nMU 91,91,22\n"
"AW 120,91\nAR 91,121\nMU 91,91,22\nAW 121,91\nAR 91,122\nMU 91,91,22\nAW 122,91\nAR 91,123\nMU 91,91,22\nAW 123,91\nAR 91,124\nMU 91,91,23\nAW 124,91\nAR 91,125\nMU 91,91,23\n"
"AW 125,91\nAR 91,126\nMU 91,91,23\nAW 126,91\nAR 91,127\nMU 91,91,23\nAW 127,91\ns_nop 1\ns_setpc_b64 s[80:81]\n.R147:\nMU 128,128,22\nMU 129,129,22\nMU 130,130,22\nMU 131,131,22\n"
"MU 132,132,23\nMU 133,133,23\nMU 134,134,23\nMU 135,135,23\nMU 136,136,22\nMU 137,137,22\nMU 138,138,22\nMU 139,139,22\nMU 140,140,23\nMU 141,141,23\nMU 142,142,23\nMU 143,143,23\n"
"MU 144,144,22\nMU 145,145,22\nMU 146,146,22\nMU 147,147,22\nMU 148,148,23\nMU 149,149,23\nMU 150,150,23\nMU 151,151,23\nMU 152,152,22\nMU 153,153,22\nMU 154,154,22\nMU 155,155,22\n"
"MU 156,156,23\nMU 157,157,23\nMU 158,158,23\nMU 159,159,23\nMU 160,160,22\nMU 161,161,22\nMU 162,162,22\nMU 163,163,22\nMU 164,164,23\nMU 165,165,23\nMU 166,166,23\nMU 167,167,23\n"
"MU 168,168,22\nMU 169,169,22\nMU 170,170,22\nMU 171,171,22\nMU 172,172,23\nMU 173,173,23\nMU 174,174,23\nMU 175,175,23\nMU 176,176,22\nMU 177,177,22\nMU 178,178,22\nMU 179,179,22\n"
"MU 180,180,23\nMU 181,181,23\nMU 182,182,23\nMU 183,183,23\nMU 184,184,22\nMU 185,185,22\nMU 186,186,22\nMU 187,187,22\nMU 188,188,23\nMU 189,189,23\nMU 190,190,23\nMU 191,191,23\n"
"MU 192,192,22\nMU 193,193,22\nMU 194,194,22\nMU 195,195,22\nMU 196,196,23\nMU 197,197,23\nMU 198,198,23\nMU 199,199,23\nMU 200,200,22\nMU 201,201,22\nMU 202,202,22\nMU 203,203,22\n"
"MU 204,204,23\nMU 205,205,23\nMU 206,206,23\nMU 207,207,23\nMU 208,208,22\nMU 209,209,22\nMU 210,210,22\nMU 211,211,22\nMU 212,212,23\nMU 213,213,23\nMU 214,214,23\nMU 215,215,23\n"
"MU 216,216,22\nMU 217,217,22\nMU 218,218,22\nMU 219,219,22\nMU 220,220,23\nMU 221,221,23\nMU 222,222,23\nMU 223,223,23\nMU 224,224,22\nMU 225,225,22\nMU 226,226,22\nMU 227,227,22\n"
"MU 228,228,23\nMU 229,229,23\nMU 230,230,23\nMU 231,231,23\nMU 232,232,22\nMU 233,233,22\nMU 234,234,22\nMU 235,235,22\nMU 236,236,23\nMU 237,237,23\nMU 238,238,23\nMU 239,239,23\n"
"MU 240,240,22\nMU 241,241,22\nMU 242,242,22\nMU 243,243,22\nMU 244,244,23\nMU 245,245,23\nMU 246,246,23\nMU 247,247,23\nMU 248,248,22\nMU 249,249,22\nMU 250,250,22\nMU 251,251,22\n"
"MU 252,252,23\nMU 253,253,23\nMU 254,254,23\nMU 255,255,23\ns_nop 1\ns_setpc_b64 s[80:81]\n.SW8:\nv_mov_b32 v4, 0\nv_mov_b32 v5, 0\nv_mov_b32 v6, 0\nv_mov_b32 v7, 0\n"
"v_mov_b32 v8, 0\nv_mov_b32 v9, 0\nv_mov_b32 v10, 0\nv_mov_b32 v11, 0\nv_mov_b32 v12, 0\nv_mov_b32 v13, 0\nv_mov_b32 v14, 0\nv_mov_b32 v15, 0\nv_mov_b32 v16, 0\nv_mov_b32 v17, 0\n"
"v_mov_b32 v18, 0\nv_mov_b32 v19, 0\ns_add_u32 s99, s98, 1\ns_mov_b32 s94, s96\ns_add_u32 s95, s96, 33792\ns_or_b32 s2, s13, s11\ns_cmp_eq_u32 s2, 0\ns_cbranch_scc1 .E148\n.E149:\n"
"s_nop 2\nCvaa 32,128,192,4\nRa 176,26,8192\nAD 84,84,81\nMvaa 32,132,200\nEX 82,58\nMvaa 32,136,208\nRa 180,26,9216\nCV 74,80,81\nMvaa 32,140,216\nEX 83,59\nCvaa 36,140,220,8\n"
"Ra 184,26,10240\nAD 84,84,82\nMvaa 36,136,212\nD1 1,16,46,3072\nMvaa 36,132,204\nRa 188,26,11264\nWL 8\nMvaa 36,128,196\ns_add_u32 m0, s89, 32768\ns_add_u32 s46, s86, s88\n"
"Mvaa 36,144,228\nRa 128,2,16384\nAD 84,84,83\nMvaa 36,148,236\nEX 80,60\nMvaa 36,152,244\nRa 132,2,17408\nCV 75,82,83\nMvaa 36,156,252\nEX 81,61\nMvaa 32,156,248\nRa 136,2,18432\n"
"AD 85,85,80\nMvaa 32,152,240\nD1 1,16,46,0\nMvaa 32,148,232\nRa 140,2,19456\nWL 8\nMvaa 32,144,224\nCvaa 40,160,192,12\nRa 144,2,20480\nAD 85,85,81\nMvaa 40,164,200\nEX 82,62\n"
"Mvaa 40,168,208\nRa 148,2,21504\nCV 78,80,81\nMvaa 40,172,216\nEX 83,63\nCvaa 44,172,220,16\nRa 152,2,22528\nAD 85,85,82\nMvaa 44,168,212\nD1 1,16,46,1024\nMvaa 44,164,204\n"
"Ra 156,2,23552\nWL 8\nMvaa 44,160,196\ns_add_u32 s88, s88, 17408\nMvaa 44,176,228\nRa 160,2,24576\nAD 85,85,83\nMvaa 44,180,236\nEX 80,32\nMvaa 44,184,244\nRa 164,2,25600\n"
"CV 79,82,83\nMvaa 44,188,252\nEX 81,33\nMvaa 40,188,248\nRa 168,2,26624\nv_max_f32 v90, v84, v85\nMvaa 40,184,240\nD1 1,16,46,2048\nv_cmp_lt_f32 vcc, s63, v90\nMvaa 40,180,232\n"
"Ra 172,2,27648\nWL 8\nMvaa 40,176,224\ns_cbranch_vccnz .T150\n.T151:\nAD 86,86,84\nMaav 0,128,72\nRa 176,2,28672\nAD 84,80,81\nMaav 4,128,76\nEX 82,34\nMaav 12,132,76\n"
"Ra 180,2,29696\nCV 64,80,81\nMaav 8,132,72\nEX 83,35\nMaav 16,136,72\nRa 184,2,30720\nAD 84,84,82\nMaav 20,136,76\nD1 1,16,46,3072\ns_add_u32 m0, s89, 49152\nMaav 28,140,76\n"
"Ra 188,2,31744\nMaav 24,140,72\ns_waitcnt vmcnt(12) lgkmcnt(8)\ns_barrier\nMaav 32,144,72\nRa 128,24,32768\nAD 84,84,83\nAR 98,0\nAR 99,1\nCV 100,98,99\nMaav 36,144,76\nEX 80,36\n"
"AR 98,2\nAR 99,3\nCV 101,98,99\nMaav 44,148,76\nRa 132,24,33792\nCV 65,82,83\nAR 98,8\nAR 99,9\nCV 102,98,99\nAR 98,10\nMaav 40,148,72\nEX 81,37\nAR 99,11\nCV 103,98,99\n"
"Maav 48,152,72\nRa 136,24,34816\nAD 87,87,85\ns_add_u32 s5, s66, 0\nSv 100,1,16,5,0\nAR 98,4\nAR 99,5\nMaav 52,152,76\ns_add_u32 s46, s87, s88\nD1 1,16,46,0\nv_mov_b32 v96, v86\n"
"CV 104,98,99\nAR 98,6\nMaav 60,156,76\nRa 140,24,35840\nWL 8\nMaav 56,156,72\ns_cmp_eq_u32 s99, 1\ns_cselect_b32 s86, s94, s86\nv_mov_b32 v97, v87\nAR 99,7\nCV 105,98,99\n"
"Maav 64,160,72\nRa 144,26,32768\nAD 85,80,81\nv_permlane32_swap_b32 v86, v96\nAR 98,12\nAR 99,13\nCV 106,98,99\nMaav 68,160,76\nEX 82,38\nAR 98,14\nAR 99,15\nCV 107,98,99\n"
"Maav 76,164,76\nRa 148,26,33792\nCV 68,80,81\nv_permlane32_swap_b32 v87, v97\ns_add_u32 s5, s66, 0\nSv 104,1,16,5,1024\nMaav 72,164,72\nEX 83,39\nAR 98,16\nAR 99,17\nCV 108,98,99\n"
"AR 98,18\nMaav 80,168,72\nRa 152,26,34816\nAD 85,85,82\nAD 86,86,96\nAR 99,19\nCV 109,98,99\nMaav 84,168,76\nD1 1,16,46,1024\nAD 87,87,97\nv_mov_b32 v96, v86\nAR 98,24\nAR 99,25\n"
"CV 110,98,99\nMaav 92,172,76\nRa 156,26,35840\nWL 8\nMaav 88,172,72\ns_cmp_eq_u32 s99, s93\ns_cbranch_scc1 .E152\n.E153:\ns_nop 2\nMaav 96,176,72\nRa 160,24,40960\nAD 85,85,83\n"
"v_mov_b32 v97, v87\nAR 98,26\nAR 99,27\nMaav 100,176,76\nEX 80,40\nCV 111,98,99\ns_add_u32 s5, s66, 0\nSv 108,1,16,5,2048\nMaav 108,180,76\nRa 164,24,41984\nCV 69,82,83\n"
"v_permlane16_swap_b32 v86, v96\nAR 98,20\nAR 99,21\nCV 100,98,99\nMaav 104,180,72\nEX 81,41\nAR 98,22\nAR 99,23\nCV 101,98,99\nMaav 112,184,72\nRa 168,24,43008\nAD 84,84,80\n"
"v_permlane16_swap_b32 v87, v97\nAR 98,28\nAR 99,29\nCV 102,98,99\nMaav 116,184,76\nD1 1,16,46,2048\nAD 86,86,96\nAD 87,87,97\nAR 98,30\nAR 99,31\nMaav 124,188,76\nRa 172,24,44032\n"
"WL 8\nMaav 120,188,72\ns_xor_b32 s89, s89, 0x10000\nv_cmp_gt_u32 vcc, 0x100, v1\nv_mov_b32 v94, s15\nv_mov_b32 v95, s15\nv_cndmask_b32 v92, v89, v88, vcc\n"
"v_cndmask_b32 v93, v87, v86, vcc\nCvaa 48,128,192,4\nRa 176,26,40960\nAD 84,84,81\nv_mov_b32 v86, 0\nCV 103,98,99\ns_add_u32 s5, s66, 0\nSv 100,1,16,5,3072\nMvaa 48,132,200\n"
"EX 82,42\nAR 98,32\nAR 99,33\nCV 104,98,99\nMvaa 48,136,208\nRa 180,26,41984\nCV 66,80,81\nv_mov_b32 v87, 0\nAR 98,34\nAR 99,35\nMvaa 48,140,216\nEX 83,43\nCV 105,98,99\nAR 98,40\n"
"AR 99,41\nCV 106,98,99\nCvaa 52,140,220,8\nRa 184,26,43008\nAD 84,84,82\nv_mov_b32 v88, 0\nAR 98,42\nAR 99,43\nMvaa 52,136,212\nD1 1,16,46,3072\nv_mov_b32 v89, 0\n"
"s_mov_b32 s64, 0x2b800000\nCV 107,98,99\ns_add_u32 s5, s66, 4096\nSv 104,1,16,5,0\nMvaa 52,132,204\nRa 188,26,44032\nWL 8\nMvaa 52,128,196\ns_add_u32 m0, s89, 0\n"
"s_add_u32 s46, s86, s88\nAR 98,36\nAR 99,37\nCV 108,98,99\nAR 98,38\nMvaa 52,144,228\nRa 128,2,49152\nAD 84,84,83\nAR 99,39\nCV 109,98,99\nAR 98,44\nMvaa 52,148,236\nEX 80,44\n"
"AR 99,45\nCV 110,98,99\nAR 98,46\nMvaa 52,152,244\nRa 132,2,50176\nCV 67,82,83\nAR 99,47\nCV 111,98,99\ns_add_u32 s5, s66, 4096\nSv 108,1,16,5,1024\nMvaa 52,156,252\nEX 81,45\n"
"AR 98,48\nAR 99,49\nCV 100,98,99\nMvaa 48,156,248\nRa 136,2,51200\nAD 85,85,80\nAR 98,50\nAR 99,51\nCV 101,98,99\nAR 98,56\nMvaa 48,152,240\nD1 1,16,46,0\n"
"v_xor_b32 v24, 0x10000, v24\nAR 99,57\nCV 102,98,99\nAR 98,58\nMvaa 48,148,232\nRa 140,2,52224\nWL 8\nMvaa 48,144,224\nAR 99,59\nCV 103,98,99\ns_add_u32 s5, s66, 4096\n"
"Sv 100,1,16,5,2048\nAR 98,52\nCvaa 56,160,192,12\nRa 144,2,53248\nAD 85,85,81\nAR 99,53\nCV 104,98,99\nAR 98,54\nAR 99,55\nMvaa 56,164,200\nEX 82,46\nCV 105,98,99\nAR 98,60\n"
"AR 99,61\nMvaa 56,168,208\nRa 148,2,54272\nCV 70,80,81\nCV 106,98,99\nAR 98,62\nAR 99,63\nMvaa 56,172,216\nEX 83,47\nCV 107,98,99\ns_add_u32 s5, s66, 4096\nSv 104,1,16,5,3072\n"
"AR 98,64\nCvaa 60,172,220,16\nRa 152,2,55296\nAD 85,85,82\nAR 99,65\nCV 108,98,99\nAR 98,66\nMvaa 60,168,212\nD1 1,16,46,1024\nv_xor_b32 v26, 0x10000, v26\nAR 99,67\nCV 109,98,99\n"
"AR 98,72\nAR 99,73\nMvaa 60,164,204\nRa 156,2,56320\nWL 8\nMvaa 60,160,196\ns_add_u32 s88, s88, 16384\nCV 110,98,99\nAR 98,74\nAR 99,75\nCV 111,98,99\nMvaa 60,176,228\n"
"Ra 160,2,57344\nAD 85,85,83\ns_add_u32 s5, s66, 8192\nSv 108,1,16,5,0\nAR 98,68\nMvaa 60,180,236\nEX 80,48\nAR 99,69\nCV 100,98,99\nAR 98,70\nMvaa 60,184,244\nRa 164,2,58368\n"
"CV 71,82,83\nAR 99,71\nCV 101,98,99\nAR 98,76\nAR 99,77\nMvaa 60,188,252\nEX 81,49\nCV 102,98,99\nAR 98,78\nAR 99,79\nMvaa 56,188,248\nRa 168,2,59392\nv_max_f32 v90, v84, v85\n"
"CV 103,98,99\ns_add_u32 s5, s66, 8192\nSv 100,1,16,5,1024\nAR 98,80\nMvaa 56,184,240\nD1 1,16,46,2048\nv_cmp_lt_f32 vcc, s63, v90\nMvaa 56,180,232\nRa 172,2,60416\nWL 8\n"
"Mvaa 56,176,224\ns_cbranch_vccnz .T154\n.T155:\nv_mov_b32 v20, v84\nv_mov_b32 v21, v85\nv_mov_b32 v22, v20\nv_mov_b32 v23, v21\ns_nop 0\nv_permlane32_swap_b32 v20, v22\n"
"v_permlane32_swap_b32 v21, v23\nAD 20,20,22\nAD 21,21,23\nv_mov_b32 v22, v20\nv_mov_b32 v23, v21\ns_nop 0\nv_permlane16_swap_b32 v20, v22\nv_permlane16_swap_b32 v21, v23\n"
"AD 20,20,22\nAD 21,21,23\nv_min_f32 v90, v20, v21\nv_cmp_gt_f32 vcc, s64, v90\ns_nop 0\ns_cbranch_vccnz .T156\n.T157:\nAD 86,86,84\nZvav 128,128,64\nRa 176,2,61440\nAD 84,80,81\n"
"AR 99,81\nCV 104,98,99\nAR 98,82\nAR 99,83\nZvav 132,128,68\nEX 82,50\nCV 105,98,99\nAR 98,88\nAR 99,89\nZvav 140,132,68\nRa 180,2,62464\nCV 72,80,81\nCV 106,98,99\nAR 98,90\n"
"AR 99,91\nZvav 136,132,64\nEX 83,51\nCV 107,98,99\ns_add_u32 s5, s66, 8192\nSv 104,1,16,5,2048\nAR 98,84\nZvav 144,136,64\nRa 184,2,63488\nAD 84,84,82\nAR 99,85\nCV 108,98,99\n"
"AR 98,86\nZvav 148,136,68\nD1 1,16,46,3072\ns_add_u32 m0, s89, 16384\nAR 99,87\nCV 109,98,99\nAR 98,92\nAR 99,93\nZvav 156,140,68\nRa 188,2,64512\nZvav 152,140,64\n"
"s_waitcnt vmcnt(23) lgkmcnt(8)\ns_barrier\nZvav 160,144,64\nRa 128,24,0\nAD 84,84,83\nCV 110,98,99\nAR 98,94\nAR 99,95\nZvav 164,144,68\nEX 80,52\nCV 111,98,99\n"
"s_add_u32 s5, s66, 8192\nSv 108,1,16,5,3072\nZvav 172,148,68\nRa 132,24,1024\nCV 73,82,83\nAR 98,96\nAR 99,97\nCV 100,98,99\nAR 98,98\nZvav 168,148,64\nEX 81,53\nAR 99,99\n"
"CV 101,98,99\nAR 98,104\nZvav 176,152,64\nRa 136,24,2048\nAD 87,87,85\nAR 99,105\nCV 102,98,99\nAR 98,106\nAR 99,107\nZvav 180,152,68\ns_add_u32 s46, s87, s88\nD1 1,16,46,0\n"
"CV 103,98,99\ns_add_u32 s5, s66, 12288\nSv 100,1,16,5,0\nZvav 188,156,68\nRa 140,24,3072\nWL 8\nZvav 184,156,64\ns_sub_u32 s90, s90, 1\ns_cbranch_scc1 .E158\n.E159:\n"
"Zvav 192,160,64\nRa 144,26,0\nAD 85,80,81\nAR 98,100\nAR 99,101\nCV 104,98,99\nAR 98,102\nZvav 196,160,68\nEX 82,54\nAR 99,103\nCV 105,98,99\nAR 98,108\nZvav 204,164,68\n"
"Ra 148,26,1024\nCV 76,80,81\nAR 99,109\nCV 106,98,99\nAR 98,110\nZvav 200,164,64\nEX 83,55\nAR 99,111\nCV 107,98,99\ns_add_u32 s5, s66, 12288\nSv 104,1,16,5,1024\nZvav 208,168,64\n"
"Ra 152,26,2048\nAD 85,85,82\nAR 98,112\nAR 99,113\nCV 108,98,99\nZvav 212,168,68\nD1 1,16,46,1024\nv_xor_b32 v2, 0x10000, v2\nAR 98,114\nAR 99,115\nCV 109,98,99\nAR 98,120\n"
"Zvav 220,172,68\nRa 156,26,3072\nWL 8\nZvav 216,172,64\ns_cmp_eq_u32 s99, s92\ns_cbranch_scc1 .E160\n.E161:\ns_nop 2\nZvav 224,176,64\nRa 160,24,8192\nAD 85,85,83\nAR 99,121\n"
"CV 110,98,99\nAR 98,122\nZvav 228,176,68\nEX 80,56\nAR 99,123\nCV 111,98,99\nZvav 236,180,68\nRa 164,24,9216\nCV 77,82,83\ns_add_u32 s5, s66, 12288\nSv 108,1,16,5,2048\nAR 98,116\n"
"AR 99,117\nZvav 232,180,64\nEX 81,57\nCV 100,98,99\nAR 98,118\nAR 99,119\nZvav 240,184,64\nRa 168,24,10240\nAD 84,84,80\nCV 101,98,99\nAR 98,124\nAR 99,125\nCV 102,98,99\n"
"Zvav 244,184,68\nD1 1,16,46,2048\nAR 98,126\nAR 99,127\nCV 103,98,99\nZvav 252,188,68\nRa 172,24,11264\nWL 8\nZvav 248,188,64\ns_add_u32 s5, s66, 12288\nSv 100,1,16,5,3072\n"
"s_sub_u32 s99, s99, 1\n.p2align 6\n.A162:\nCvaa 32,128,192,4\nRa 176,26,8192\nAD 84,84,81\nMvaa 32,132,200\nEX 82,58\nMvaa 32,136,208\nRa 180,26,9216\nCV 74,80,81\n"
"Mvaa 32,140,216\nEX 83,59\nCvaa 36,140,220,8\nRa 184,26,10240\nAD 84,84,82\nMvaa 36,136,212\nD1 1,16,46,3072\nMvaa 36,132,204\nRa 188,26,11264\nWL 8\nMvaa 36,128,196\n"
"s_add_u32 m0, s89, 32768\ns_add_u32 s46, s86, s88\nMvaa 36,144,228\nRa 128,2,16384\nAD 84,84,83\nMvaa 36,148,236\nEX 80,60\nMvaa 36,152,244\nRa 132,2,17408\nCV 75,82,83\n"
"Mvaa 36,156,252\nEX 81,61\nMvaa 32,156,248\nRa 136,2,18432\nAD 85,85,80\nMvaa 32,152,240\nD1 1,16,46,0\nMvaa 32,148,232\nRa 140,2,19456\nWL 8\nMvaa 32,144,224\n"
"s_cmp_eq_u32 s99, 0\ns_cselect_b32 s87, s95, s87\nCvaa 40,160,192,12\nRa 144,2,20480\nAD 85,85,81\nMvaa 40,164,200\nEX 82,62\nMvaa 40,168,208\nRa 148,2,21504\nCV 78,80,81\n"
"Mvaa 40,172,216\nEX 83,63\nCvaa 44,172,220,16\nRa 152,2,22528\nAD 85,85,82\nMvaa 44,168,212\nD1 1,16,46,1024\nMvaa 44,164,204\nRa 156,2,23552\nWL 8\nMvaa 44,160,196\n"
"s_add_u32 s88, s88, 17408\nMvaa 44,176,228\nRa 160,2,24576\nAD 85,85,83\nMvaa 44,180,236\nEX 80,32\nMvaa 44,184,244\nRa 164,2,25600\nCV 79,82,83\nMvaa 44,188,252\nEX 81,33\n"
"Mvaa 40,188,248\nRa 168,2,26624\nv_max_f32 v90, v84, v85\nMvaa 40,184,240\nD1 1,16,46,2048\nv_cmp_lt_f32 vcc, s63, v90\nMvaa 40,180,232\nRa 172,2,27648\nWL 8\nMvaa 40,176,224\n"
"s_cbranch_vccnz .T163\n.T164:\nAD 86,86,84\nMvav 128,128,72\nRa 176,2,28672\nAD 84,80,81\nMvav 132,128,76\nEX 82,34\nMvav 140,132,76\nRa 180,2,29696\nCV 64,80,81\nMvav 136,132,72\n"
"EX 83,35\nMvav 144,136,72\nRa 184,2,30720\nAD 84,84,82\nMvav 148,136,76\nD1 1,16,46,3072\ns_add_u32 m0, s89, 49152\nMvav 156,140,76\nRa 188,2,31744\nMvav 152,140,72\n"
"s_waitcnt vmcnt(12) lgkmcnt(8)\ns_barrier\nMvav 160,144,72\nRa 128,24,32768\nAD 84,84,83\nMvav 164,144,76\nEX 80,36\nMvav 172,148,76\nRa 132,24,33792\nCV 65,82,83\n"
"Mvav 168,148,72\nEX 81,37\nMvav 176,152,72\nRa 136,24,34816\nAD 87,87,85\nMvav 180,152,76\ns_add_u32 s46, s87, s88\nD1 1,16,46,0\nMvav 188,156,76\nRa 140,24,35840\nWL 8\n"
"Mvav 184,156,72\ns_cmp_eq_u32 s99, 1\ns_cselect_b32 s86, s94, s86\nMvav 192,160,72\nRa 144,26,32768\nAD 85,80,81\nMvav 196,160,76\nEX 82,38\nMvav 204,164,76\nRa 148,26,33792\n"
"CV 68,80,81\nMvav 200,164,72\nEX 83,39\nMvav 208,168,72\nRa 152,26,34816\nAD 85,85,82\nMvav 212,168,76\nD1 1,16,46,1024\nMvav 220,172,76\nRa 156,26,35840\nWL 8\nMvav 216,172,72\n"
"s_cmp_eq_u32 s99, s93\ns_cbranch_scc1 .E165\n.E166:\ns_nop 2\nMvav 224,176,72\nRa 160,24,40960\nAD 85,85,83\nMvav 228,176,76\nEX 80,40\nMvav 236,180,76\nRa 164,24,41984\n"
"CV 69,82,83\nMvav 232,180,72\nEX 81,41\nMvav 240,184,72\nRa 168,24,43008\nAD 84,84,80\nMvav 244,184,76\nD1 1,16,46,2048\nMvav 252,188,76\nRa 172,24,44032\nWL 8\nMvav 248,188,72\n"
"s_xor_b32 s89, s89, 0x10000\nCvaa 48,128,192,4\nRa 176,26,40960\nAD 84,84,81\nMvaa 48,132,200\nEX 82,42\nMvaa 48,136,208\nRa 180,26,41984\nCV 66,80,81\nMvaa 48,140,216\nEX 83,43\n"
"Cvaa 52,140,220,8\nRa 184,26,43008\nAD 84,84,82\nMvaa 52,136,212\nD1 1,16,46,3072\nMvaa 52,132,204\nRa 188,26,44032\nWL 8\nMvaa 52,128,196\ns_add_u32 m0, s89, 0\n"
"s_add_u32 s46, s86, s88\nMvaa 52,144,228\nRa 128,2,49152\nAD 84,84,83\nMvaa 52,148,236\nEX 80,44\nMvaa 52,152,244\nRa 132,2,50176\nCV 67,82,83\nMvaa 52,156,252\nEX 81,45\n"
"Mvaa 48,156,248\nRa 136,2,51200\nAD 85,85,80\nMvaa 48,152,240\nD1 1,16,46,0\nv_xor_b32 v24, 0x10000, v24\nMvaa 48,148,232\nRa 140,2,52224\nWL 8\nMvaa 48,144,224\n"
"s_cmp_eq_u32 s99, s98\ns_cbranch_scc1 .E167\n.E168:\nCvaa 56,160,192,12\nRa 144,2,53248\nAD 85,85,81\nMvaa 56,164,200\nEX 82,46\nMvaa 56,168,208\nRa 148,2,54272\nCV 70,80,81\n"
"Mvaa 56,172,216\nEX 83,47\nCvaa 60,172,220,16\nRa 152,2,55296\nAD 85,85,82\nMvaa 60,168,212\nD1 1,16,46,1024\nv_xor_b32 v26, 0x10000, v26\nMvaa 60,164,204\nRa 156,2,56320\nWL 8\n"
"Mvaa 60,160,196\ns_add_u32 s88, s88, 16384\nMvaa 60,176,228\nRa 160,2,57344\nAD 85,85,83\nMvaa 60,180,236\nEX 80,48\nMvaa 60,184,244\nRa 164,2,58368\nCV 71,82,83\nMvaa 60,188,252\n"
"EX 81,49\nMvaa 56,188,248\nRa 168,2,59392\nv_max_f32 v90, v84, v85\nMvaa 56,184,240\nD1 1,16,46,2048\nv_cmp_lt_f32 vcc, s63, v90\nMvaa 56,180,232\nRa 172,2,60416\nWL 8\n"
"Mvaa 56,176,224\ns_cbranch_vccnz .T169\n.T170:\nAD 86,86,84\nMvav 128,128,64\nRa 176,2,61440\nAD 84,80,81\nMvav 132,128,68\nEX 82,50\nMvav 140,132,68\nRa 180,2,62464\nCV 72,80,81\n"
"Mvav 136,132,64\nEX 83,51\nMvav 144,136,64\nRa 184,2,63488\nAD 84,84,82\nMvav 148,136,68\nD1 1,16,46,3072\ns_add_u32 m0, s89, 16384\nMvav 156,140,68\nRa 188,2,64512\n"
"Mvav 152,140,64\ns_waitcnt vmcnt(12) lgkmcnt(8)\ns_barrier\nMvav 160,144,64\nRa 128,24,0\nAD 84,84,83\nMvav 164,144,68\nEX 80,52\nMvav 172,148,68\nRa 132,24,1024\nCV 73,82,83\n"
"Mvav 168,148,64\nEX 81,53\nMvav 176,152,64\nRa 136,24,2048\nAD 87,87,85\nMvav 180,152,68\ns_add_u32 s46, s87, s88\nD1 1,16,46,0\nMvav 188,156,68\nRa 140,24,3072\nWL 8\n"
"Mvav 184,156,64\ns_sub_u32 s90, s90, 1\ns_cbranch_scc1 .E171\n.E172:\nMvav 192,160,64\nRa 144,26,0\nAD 85,80,81\nMvav 196,160,68\nEX 82,54\nMvav 204,164,68\nRa 148,26,1024\n"
"CV 76,80,81\nMvav 200,164,64\nEX 83,55\nMvav 208,168,64\nRa 152,26,2048\nAD 85,85,82\nMvav 212,168,68\nD1 1,16,46,1024\nv_xor_b32 v2, 0x10000, v2\nMvav 220,172,68\nRa 156,26,3072\n"
"WL 8\nMvav 216,172,64\ns_cmp_eq_u32 s99, s92\ns_cbranch_scc1 .E173\n.E174:\ns_nop 2\nMvav 224,176,64\nRa 160,24,8192\nAD 85,85,83\nMvav 228,176,68\nEX 80,56\nMvav 236,180,68\n"
"Ra 164,24,9216\nCV 77,82,83\nMvav 232,180,64\nEX 81,57\nMvav 240,184,64\nRa 168,24,10240\nAD 84,84,80\nMvav 244,184,68\nD1 1,16,46,2048\nMvav 252,188,68\nRa 172,24,11264\nWL 8\n"
"Mvav 248,188,64\ns_sub_u32 s99, s99, 1\ns_cbranch_scc0 .A162\nAD 84,84,81\nEX 82,58\nCV 74,80,81\nEX 83,59\nAD 84,84,82\nD1 1,16,46,3072\ns_add_u32 m0, s89, 32768\n"
"s_add_u32 s46, s86, s88\nRa 128,2,16384\nAD 84,84,83\nv_xor_b32 v3, 0x10000, v2\nv_add_u32 v0, 0x20000, v1\nEX 80,60\ns_mul_i32 s2, s72, 33792\ns_add_u32 s94, s2, 0x3500000\n"
"Ra 132,2,17408\nCV 75,82,83\ns_add_u32 s95, s2, 0x3504000\nEX 81,61\ns_xor_b32 s2, s72, 32\ns_mul_i32 s2, s2, 33792\nRa 136,2,18432\nAD 85,85,80\ns_add_u32 s92, s2, 0x3500000\n"
"D1 1,16,46,0\ns_and_b32 s2, s12, 1\ns_xor_b32 s3, s12, 1\ns_lshl1_add_u32 s3, s3, s2\nRa 140,2,19456\ns_mul_i32 s3, s3, 119808\ns_lshl_b32 s89, s11, 12\ns_add_u32 s3, s3, s89\n"
"s_add_u32 s93, s3, 0x5700000\nRa 144,2,20480\nAD 85,85,81\ns_mov_b32 s36, s68\ns_and_b32 s37, s69, 0xffff\nEX 82,62\ns_mov_b32 s38, 0x1c0000\nRa 148,2,21504\nCV 78,80,81\n"
"s_mov_b32 s39, 0x20000\ns_and_b32 s2, s74, 15\nEX 83,63\ns_mul_i32 s3, s73, 14\nRa 152,2,22528\nAD 85,85,82\ns_add_u32 s2, s2, s3\ns_lshl2_add_u32 s2, s2, s11\nD1 1,16,46,1024\n"
"s_mul_i32 s3, s2, 4682\ns_lshr_b32 s3, s3, 16\ns_mul_i32 s91, s3, 14\nRa 156,2,23552\ns_sub_u32 s91, s2, s91\ns_lshl_b32 s90, s3, 5\ns_cmp_gt_u32 s91, 6\n"
"s_cselect_b32 s2, 1067008, 0\nRa 160,2,24576\nAD 85,85,83\ns_mul_i32 s3, s3, 33792\ns_add_u32 s2, s2, s3\ns_lshl_b32 s3, s91, 11\ns_add_u32 s2, s2, s3\n"
"s_add_u32 s98, s2, 0x4584000\nRa 164,2,25600\nCV 79,82,83\ns_lshl_b32 s2, s91, 7\ns_add_u32 s0, s2, 0x5c4b800\ns_mul_i32 s2, s90, 896\ns_lshl_b32 s3, s91, 6\n"
"s_add_u32 s91, s2, s3\nRa 168,2,26624\nv_max_f32 v90, v84, v85\ns_add_u32 s1, s91, 14336\nD1 1,16,46,2048\nv_cmp_lt_f32 vcc, s63, v90\nRa 172,2,27648\nWL 8\ns_cbranch_vccnz .T175\n"
".T176:\nAD 86,86,84\nAD 87,87,85\nMvav 128,128,72\nRa 176,2,28672\nv_mov_b32 v84, v86\nv_mov_b32 v85, v87\nMvav 132,128,76\nv_permlane32_swap_b32 v86, v84\n"
"v_permlane32_swap_b32 v87, v85\nMvav 140,132,76\nRa 180,2,29696\nAD 86,86,84\nMvav 136,132,72\nAD 87,87,85\nv_mov_b32 v84, v86\nMvav 144,136,72\nRa 184,2,30720\n"
"v_mov_b32 v85, v87\nMvav 148,136,76\nD1 1,16,46,3072\nv_permlane16_swap_b32 v86, v84\nMvav 156,140,76\nRa 188,2,31744\nWL 8\nMvav 152,140,72\nv_permlane16_swap_b32 v87, v85\n"
"AD 86,86,84\nAD 87,87,85\nMvav 160,144,72\nRa 128,2,0\nMvav 164,144,76\nv_rcp_f32 v86, v86\nMvav 172,148,76\nRa 132,2,1024\nv_rcp_f32 v87, v87\nMvav 168,148,72\n"
"s_add_u32 m0, s89, 131072\ns_add_u32 s46, s93, 0\nMU 128,128,86\nMvav 176,152,72\nRa 136,2,2048\nMU 129,129,86\nCV 96,128,129\nMU 130,130,86\nMvav 180,152,76\nD1 1,16,46,0\n"
"MU 131,131,86\nCV 97,130,131\nMU 132,132,87\nMvav 188,156,76\nRa 140,2,3072\nWL 8\nMvav 184,156,72\nMU 133,133,87\nCV 100,132,133\nMU 134,134,87\nMU 135,135,87\nMvav 192,160,72\n"
"Ra 144,2,4096\nCV 101,134,135\nMU 136,136,86\nMU 137,137,86\nMvav 196,160,76\nCV 98,136,137\nMU 138,138,86\nMU 139,139,86\nMvav 204,164,76\nRa 148,2,5120\nCV 99,138,139\n"
"s_add_u32 s5, s94, 0\nSv 96,1,16,5,0\nMvav 200,164,72\nMU 140,140,87\nMU 141,141,87\nCV 102,140,141\nMU 142,142,87\nMvav 208,168,72\nRa 152,2,6144\nMU 143,143,87\nCV 103,142,143\n"
"Sv 100,1,16,5,1024\nMvav 212,168,76\nD1 1,16,46,1024\nMU 144,144,86\nMU 145,145,86\nCV 104,144,145\nMvav 220,172,76\nRa 156,2,7168\nWL 8\nMvav 216,172,72\nMU 146,146,86\n"
"MU 147,147,86\nCV 105,146,147\nMU 148,148,87\nMvav 224,176,72\nRa 160,2,8192\nMU 149,149,87\nCV 108,148,149\nMU 150,150,87\nMvav 228,176,76\nMU 151,151,87\nCV 109,150,151\n"
"MU 152,152,86\nMU 153,153,86\nMvav 236,180,76\nRa 164,2,9216\nCV 106,152,153\nMU 154,154,86\nMU 155,155,86\nMvav 232,180,72\nCV 107,154,155\nSv 104,1,16,5,2048\nMU 156,156,87\n"
"Mvav 240,184,72\nRa 168,2,10240\nMU 157,157,87\nCV 110,156,157\nMU 158,158,87\nMvav 244,184,76\nD1 1,16,46,2048\nMU 159,159,87\nCV 111,158,159\nSv 108,1,16,5,3072\n"
"Mvav 252,188,76\nRa 172,2,11264\nWL 8\nMvav 248,188,72\nMU 160,160,86\nMU 161,161,86\nZaav 0,128,96\nRa 176,2,12288\nCV 112,160,161\nMU 162,162,86\nMaav 0,132,104\nMU 163,163,86\n"
"CV 113,162,163\nZaav 4,132,108\nRa 180,2,13312\nMU 164,164,87\nMaav 4,128,100\nMU 165,165,87\nCV 116,164,165\nZaav 12,136,100\nRa 184,2,14336\nMU 166,166,87\nMaav 12,140,108\n"
"D1 1,16,46,3072\nMU 167,167,87\nZaav 8,140,104\nRa 188,2,15360\nMaav 8,136,96\ns_waitcnt vmcnt(20) lgkmcnt(8)\ns_barrier\nZaav 16,144,96\nRa 128,2,32768\nCV 117,166,167\n"
"MU 168,168,86\nv_lshrrev_b32 v88, 8, v1\nv_lshlrev_b32 v89, 5, v88\nMaav 16,148,104\nMU 169,169,86\nCV 114,168,169\n"
"buffer_load_dwordx4 v[64:67], v89, s[16:19], s0 offen offset:0 sc0 sc1\nZaav 20,148,108\nRa 132,2,33792\nMU 170,170,86\nMaav 20,144,100\ns_add_u32 m0, s89, 49152\n"
"s_add_u32 s46, s96, 608256\nZaav 28,152,100\nRa 136,2,34816\nMU 171,171,86\nMaav 28,156,108\nD1 1,16,46,0\nCV 115,170,171\n"
"buffer_load_dwordx4 v[68:71], v89, s[16:19], s0 offen offset:16 sc0 sc1\nZaav 24,156,104\nRa 140,2,35840\nWL 8\nMaav 24,152,96\ns_add_u32 s5, s94, 4096\nSv 112,1,16,5,0\n"
"Zaav 32,160,96\nRa 144,2,36864\nMU 172,172,87\nMU 173,173,87\nMaav 32,164,104\nCV 118,172,173\nMU 174,174,87\n"
"buffer_load_dwordx4 v[72:75], v89, s[16:19], s0 offen offset:1792 sc0 sc1\nZaav 36,164,108\nRa 148,2,37888\nMU 175,175,87\nMaav 36,160,100\nCV 119,174,175\nSv 116,1,16,5,1024\n"
"Zaav 44,168,100\nRa 152,2,38912\nMU 176,176,86\nMaav 44,172,108\nD1 1,16,46,1024\nMU 177,177,86\nbuffer_load_dwordx4 v[76:79], v89, s[16:19], s0 offen offset:1808 sc0 sc1\n"
"Zaav 40,172,104\nRa 156,2,39936\nWL 8\nMaav 40,168,96\nCV 120,176,177\nMU 178,178,86\nZaav 48,176,96\nRa 160,2,40960\nMU 179,179,86\nCV 121,178,179\nMaav 48,180,104\n"
"MU 180,180,87\nMU 181,181,87\nv_bfe_u32 v90, v1, 4, 4\nv_mul_u32_u24 v91, 896, v90\nv_lshl_add_u32 v91, v88, 4, v91\nZaav 52,180,108\nRa 164,2,41984\nCV 124,180,181\n"
"buffer_load_dwordx4 v[12:15], v91, s[28:31], s91 offen offset:0\nMaav 52,176,100\nMU 182,182,87\nMU 183,183,87\nZaav 60,184,100\nRa 168,2,43008\nCV 125,182,183\nMaav 60,188,108\n"
"D1 1,16,46,2048\nMU 184,184,86\nZaav 56,188,104\nRa 172,2,44032\nWL 8\nMaav 56,184,96\nMU 185,185,86\nCV 122,184,185\n"
"buffer_load_dwordx4 v[16:19], v91, s[28:31], s1 offen offset:0\nv_add_u32 v90, s90, v90\nv_lshlrev_b32 v88, 2, v90\nZaav 64,128,96\nRa 176,2,45056\nMU 186,186,86\nMU 187,187,86\n"
"v_lshrrev_b32 v92, 8, v1\nv_bfe_u32 v93, v1, 6, 2\nMaav 64,132,104\nCV 123,186,187\nSv 120,1,16,5,2048\nZaav 68,132,108\nRa 180,2,46080\nMU 188,188,87\nMaav 68,128,100\n"
"MU 189,189,87\nCV 126,188,189\nZaav 76,136,100\nRa 184,2,47104\nMU 190,190,87\nMaav 76,140,108\nD1 1,16,46,3072\nMU 191,191,87\nZaav 72,140,104\nRa 188,2,48128\nWL 8\n"
"Maav 72,136,96\nCV 127,190,191\nSv 124,1,16,5,3072\nZaav 80,144,96\nRa 128,2,49152\nMU 192,192,86\nMU 193,193,86\nMaav 80,148,104\nCV 32,192,193\nMU 194,194,86\nZaav 84,148,108\n"
"Ra 132,2,50176\nMU 195,195,86\nMaav 84,144,100\ns_add_u32 m0, s89, 147456\ns_add_u32 s46, s93, 16384\nv_cmp_eq_u32 vcc, v92, v93\nv_and_b32 v92, 16, v1\nv_mov_b32 v93, 0x3f80\n"
"v_cndmask_b32 v93, 0, v93, vcc\nZaav 92,152,100\nRa 136,2,51200\nCV 33,194,195\nMU 196,196,87\nv_lshlrev_b32 v93, v92, v93\nv_and_b32 v92, 32, v1\nMaav 92,156,108\nD1 1,16,46,0\n"
"MU 197,197,87\nZaav 88,156,104\nRa 140,2,52224\nWL 8\nMaav 88,152,96\nCV 36,196,197\nMU 198,198,87\nZaav 96,160,96\nRa 144,2,53248\nMU 199,199,87\nCV 37,198,199\nMaav 96,164,104\n"
"MU 200,200,86\nMU 201,201,86\nZaav 100,164,108\nRa 148,2,54272\nCV 34,200,201\nMaav 100,160,100\nMU 202,202,86\nMU 203,203,86\nZaav 108,168,100\nRa 152,2,55296\nCV 35,202,203\n"
"s_add_u32 s5, s94, 8192\nMaav 108,172,108\nD1 1,16,46,1024\nSv 32,1,16,5,0\nZaav 104,172,104\nRa 156,2,56320\nWL 8\nMaav 104,168,96\nMU 204,204,87\nMU 205,205,87\nMaav 0,176,112\n"
"Ra 160,2,57344\nCV 38,204,205\nMU 206,206,87\nMaav 0,180,120\nMU 207,207,87\nCV 39,206,207\nMaav 4,180,124\nRa 164,2,58368\nSv 36,1,16,5,1024\nMaav 4,176,116\nMU 208,208,86\n"
"MU 209,209,86\nv_cmp_eq_u32 vcc, 0, v92\nv_mov_b32 v4, 0\nv_mov_b32 v5, 0\nv_cndmask_b32 v6, 0, v93, vcc\nMaav 12,184,116\nRa 168,2,59392\nCV 40,208,209\nMU 210,210,86\n"
"v_sub_u32 v7, v93, v6\nv_mov_b32 v8, 0\nMaav 12,188,124\nD1 1,16,46,2048\nMU 211,211,86\nv_mov_b32 v9, 0\nMaav 8,188,120\nRa 172,2,60416\nWL 8\nMaav 8,184,112\nCV 41,210,211\n"
"MU 212,212,87\nMaav 16,128,112\nRa 176,2,61440\nMU 213,213,87\nCV 44,212,213\nMaav 16,132,120\nMU 214,214,87\nMU 215,215,87\nMaav 20,132,124\nRa 180,2,62464\nCV 45,214,215\n"
"Maav 20,128,116\nMU 216,216,86\nMU 217,217,86\nMaav 28,136,116\nRa 184,2,63488\nCV 42,216,217\nMU 218,218,86\nMaav 28,140,124\nD1 1,16,46,3072\nMU 219,219,86\nMaav 24,140,120\n"
"Ra 188,2,64512\nMaav 24,136,112\ns_waitcnt vmcnt(32) lgkmcnt(8)\ns_barrier\nMaav 32,144,112\nRa 128,3,0\nCV 43,218,219\nSv 40,1,16,5,2048\nMaav 32,148,120\nMU 220,220,87\n"
"MU 221,221,87\nMaav 36,148,124\nRa 132,3,1024\nCV 46,220,221\nMaav 36,144,116\nMU 222,222,87\nMU 223,223,87\nMaav 44,152,116\nRa 136,3,2048\nCV 47,222,223\nSv 44,1,16,5,3072\n"
"Maav 44,156,124\nMU 224,224,86\nMU 225,225,86\nMaav 40,156,120\nRa 140,3,3072\nWL 8\nMaav 40,152,112\nCV 48,224,225\nMU 226,226,86\nMaav 48,160,112\nRa 144,3,4096\nMU 227,227,86\n"
"Maav 48,164,120\nCV 49,226,227\nMU 228,228,87\nMaav 52,164,124\nRa 148,3,5120\nMU 229,229,87\nMaav 52,160,116\nCV 52,228,229\nMU 230,230,87\nMaav 60,168,116\nRa 152,3,6144\n"
"MU 231,231,87\nMaav 60,172,124\nCV 53,230,231\nMU 232,232,86\nMaav 56,172,120\nRa 156,3,7168\nWL 8\nMaav 56,168,112\nMU 233,233,86\nCV 50,232,233\nMaav 64,176,112\nRa 160,3,8192\n"
"MU 234,234,86\nMaav 64,180,120\nMU 235,235,86\nCV 51,234,235\nMaav 68,180,124\nRa 164,3,9216\ns_add_u32 s5, s94, 12288\nMaav 68,176,116\nSv 48,1,16,5,0\nMU 236,236,87\n"
"Maav 76,184,116\nRa 168,3,10240\nMU 237,237,87\nMaav 76,188,124\nCV 54,236,237\nMU 238,238,87\nMaav 72,188,120\nRa 172,3,11264\nWL 8\nMaav 72,184,112\nMU 239,239,87\n"
"CV 55,238,239\nMaav 80,128,112\nRa 176,3,12288\nSv 52,1,16,5,1024\nMaav 80,132,120\nMU 240,240,86\nMU 241,241,86\nMaav 84,132,124\nRa 180,3,13312\nCV 56,240,241\nMaav 84,128,116\n"
"MU 242,242,86\nMU 243,243,86\nMaav 92,136,116\nRa 184,3,14336\nCV 57,242,243\nMaav 92,140,124\nMU 244,244,87\nMU 245,245,87\nMaav 88,140,120\nRa 188,3,15360\nWL 8\n"
"Maav 88,136,112\nCV 60,244,245\nMU 246,246,87\nMaav 96,144,112\nRa 128,3,16384\nMU 247,247,87\nMaav 96,148,120\nCV 61,246,247\nMU 248,248,86\nMaav 100,148,124\nRa 132,3,17408\n"
"MU 249,249,86\nMaav 100,144,116\nCV 58,248,249\nMU 250,250,86\nMaav 108,152,116\nRa 136,3,18432\nMU 251,251,86\nMaav 108,156,124\nCV 59,250,251\nSv 56,1,16,5,2048\n"
"Maav 104,156,120\nRa 140,3,19456\nWL 8\nMaav 104,152,112\nMU 252,252,87\nMU 253,253,87\nMaav 0,160,32\nRa 144,3,20480\nCV 62,252,253\nMaav 0,164,40\nMU 254,254,87\nMU 255,255,87\n"
"Maav 4,164,44\nRa 148,3,21504\nCV 63,254,255\nMaav 4,160,36\nSv 60,1,16,5,3072\nWV 15\nMaav 12,168,36\nRa 152,3,22528\nv_xor_b32 v92, s15, v66\nMaav 12,172,44\n"
"v_xor_b32 v93, s15, v70\nv_xor_b32 v94, s15, v74\nMaav 8,172,40\nRa 156,3,23552\nWL 8\nMaav 8,168,32\nv_xor_b32 v95, s15, v78\nv_or3_b32 v92, v92, v93, v94\nMaav 16,176,32\n"
"Ra 160,3,24576\nv_or_b32 v92, v92, v95\nMaav 16,180,40\nv_cmp_ne_u32 vcc, 0, v92\ns_nop 0\ns_cbranch_vccnz .S183\n.S184:\nMaav 20,180,44\nRa 164,3,25600\ns_mov_b32 s2, 0x5c40000\n"
"Maav 20,176,36\nbuffer_load_dword v80, v88, s[16:19], s2 offen sc0 sc1\nMaav 28,184,36\nRa 168,3,26624\nMaav 28,188,44\nMaav 24,188,40\nRa 172,3,27648\nWL 8\nMaav 24,184,32\n"
"buffer_load_dword v81, v88, s[16:19], s2 offen offset:64 sc0 sc1\nv_and_b32 v92, 0x7ff, v67\nMaav 32,128,32\nRa 176,3,28672\nv_lshrrev_b32 v93, 16, v67\nMaav 32,132,40\n"
"v_add_u32 v94, 16, v90\nv_cmp_eq_u32 vcc, v92, v90\ns_nop 1\nv_cndmask_b32 v12, v12, v64, vcc\nv_cndmask_b32 v13, v13, v65, vcc\nv_cndmask_b32 v14, v14, v68, vcc\n"
"v_cndmask_b32 v15, v15, v69, vcc\nMaav 36,132,44\nRa 180,3,29696\nv_cmp_eq_u32 vcc, v93, v90\ns_nop 1\nv_cndmask_b32 v12, v12, v72, vcc\nv_cndmask_b32 v13, v13, v73, vcc\n"
"v_cndmask_b32 v14, v14, v76, vcc\nv_cndmask_b32 v15, v15, v77, vcc\nMaav 36,128,36\nv_cmp_eq_u32 vcc, v92, v94\ns_nop 1\nv_cndmask_b32 v16, v16, v64, vcc\n"
"v_cndmask_b32 v17, v17, v65, vcc\nv_cndmask_b32 v18, v18, v68, vcc\nv_cndmask_b32 v19, v19, v69, vcc\nMaav 44,136,36\nRa 184,3,30720\nv_cmp_eq_u32 vcc, v93, v94\ns_nop 1\n"
"v_cndmask_b32 v16, v16, v72, vcc\nv_cndmask_b32 v17, v17, v73, vcc\nv_cndmask_b32 v18, v18, v76, vcc\nv_cndmask_b32 v19, v19, v77, vcc\nMaav 44,140,44\nMaav 40,140,40\n"
"Ra 188,3,31744\nMaav 40,136,32\ns_waitcnt vmcnt(15) lgkmcnt(8)\ns_barrier\nMaav 48,144,32\nRa 128,3,32768\nMaav 48,148,40\nMaav 52,148,44\nRa 132,3,33792\nMaav 52,144,36\n"
"Maav 60,152,36\nRa 136,3,34816\nMaav 60,156,44\nMaav 56,156,40\nRa 140,3,35840\nWL 8\nMaav 56,152,32\nMaav 64,160,32\nRa 144,3,36864\nMaav 64,164,40\ns_add_u32 s4, s92, 0\n"
"Maav 68,164,44\nRa 148,3,37888\nMaav 68,160,36\ns_add_u32 m0, s89, 81920\ns_add_u32 s46, s93, 33792\nbuffer_load_dwordx4 a[192:195], v1, s[16:19], s4 offen offset:0 sc0 sc1\n"
"Maav 76,168,36\nRa 152,3,38912\nMaav 76,172,44\nD1 1,16,46,0\nMaav 72,172,40\nRa 156,3,39936\nWL 8\nMaav 72,168,32\n"
"buffer_load_dwordx4 a[196:199], v1, s[16:19], s4 offen offset:1024 sc0 sc1\nMaav 80,176,32\nRa 160,3,40960\nMaav 80,180,40\nMaav 84,180,44\nRa 164,3,41984\nMaav 84,176,36\n"
"buffer_load_dwordx4 a[200:203], v1, s[16:19], s4 offen offset:2048 sc0 sc1\nMaav 92,184,36\nRa 168,3,43008\nMaav 92,188,44\nD1 1,16,46,1024\nMaav 88,188,40\nRa 172,3,44032\nWL 8\n"
"Maav 88,184,32\nbuffer_load_dwordx4 a[204:207], v1, s[16:19], s4 offen offset:3072 sc0 sc1\nMaav 96,128,32\nRa 176,3,45056\nMaav 96,132,40\ns_add_u32 s4, s92, 4096\n"
"Maav 100,132,44\nRa 180,3,46080\nMaav 100,128,36\nbuffer_load_dwordx4 a[208:211], v1, s[16:19], s4 offen offset:0 sc0 sc1\nMaav 108,136,36\nRa 184,3,47104\nMaav 108,140,44\n"
"D1 1,16,46,2048\nMaav 104,140,40\nRa 188,3,48128\nWL 8\nMaav 104,136,32\nbuffer_load_dwordx4 a[212:215], v1, s[16:19], s4 offen offset:1024 sc0 sc1\nMaav 0,144,48\nRa 128,3,49152\n"
"Maav 0,148,56\nMaav 4,148,60\nRa 132,3,50176\nMaav 4,144,52\nbuffer_load_dwordx4 a[216:219], v1, s[16:19], s4 offen offset:2048 sc0 sc1\nMaav 12,152,52\nRa 136,3,51200\n"
"Maav 12,156,60\nD1 1,16,46,3072\nMaav 8,156,56\nRa 140,3,52224\nWL 8\nMaav 8,152,48\nbuffer_load_dwordx4 a[220:223], v1, s[16:19], s4 offen offset:3072 sc0 sc1\nMaav 16,160,48\n"
"Ra 144,3,53248\nMaav 16,164,56\nMaav 20,164,60\nRa 148,3,54272\nMaav 20,160,52\ns_add_u32 m0, s89, 65536\ns_add_u32 s46, s93, 50176\nMaav 28,168,52\nRa 152,3,55296\n"
"Maav 28,172,60\nD1 1,16,46,0\nMaav 24,172,56\nRa 156,3,56320\nWL 8\nMaav 24,168,48\nMaav 32,176,48\nRa 160,3,57344\nMaav 32,180,56\nMaav 36,180,60\nRa 164,3,58368\nMaav 36,176,52\n"
"Maav 44,184,52\nRa 168,3,59392\nMaav 44,188,60\nD1 1,16,46,1024\nMaav 40,188,56\nRa 172,3,60416\nWL 8\nMaav 40,184,48\nMaav 48,128,48\nRa 176,3,61440\nMaav 48,132,56\n"
"Maav 52,132,60\nRa 180,3,62464\nMaav 52,128,52\nMaav 60,136,52\nRa 184,3,63488\nMaav 60,140,60\nD1 1,16,46,2048\nMaav 56,140,56\nRa 188,3,64512\nMaav 56,136,48\n"
"s_waitcnt vmcnt(23) lgkmcnt(8)\ns_barrier\nMaav 64,144,48\nRa 128,0,0\nMaav 64,148,56\nMaav 68,148,60\nRa 132,0,1024\nMaav 68,144,52\nMaav 76,152,52\nRa 136,0,2048\n"
"Maav 76,156,60\nD1 1,16,46,3072\nMaav 72,156,56\nRa 140,0,3072\nWL 8\nMaav 72,152,48\nMaav 80,160,48\nRa 144,0,4096\nMaav 80,164,56\nMaav 84,164,60\nRa 148,0,5120\nMaav 84,160,52\n"
"s_add_u32 m0, s89, 98304\ns_add_u32 s46, s93, 67584\nMaav 92,168,52\nRa 152,0,6144\nMaav 92,172,60\nD1 1,16,46,0\nMaav 88,172,56\nRa 156,0,7168\nWL 8\nMaav 88,168,48\nWV 5\n"
"AR 22,195\nAR 23,199\nMaav 96,176,48\nRa 160,0,8192\nv_max_u32 v21, v22, v23\nMaav 96,180,56\nAR 22,203\nAR 23,207\nMaav 100,180,60\nRa 164,0,9216\nv_max3_u32 v21, v21, v22, v23\n"
"AR 22,211\nMaav 100,176,52\nAR 23,215\nv_max3_u32 v21, v21, v22, v23\nMaav 108,184,52\nRa 168,0,10240\nAR 22,219\nMaav 108,188,60\nD1 1,16,46,1024\nAR 23,223\n"
"v_max3_u32 v21, v21, v22, v23\nMaav 104,188,56\nRa 172,0,11264\nWL 8\nMaav 104,184,48\nv_cmp_eq_u32 vcc, -1, v21\ns_nop 0\ns_cbranch_vccnz .S177\n.S178:\nMaaa 0,128,192\n"
"Ra 176,0,12288\nMaaa 0,132,200\nv_sub_f32 v92, 1.0, v80\nv_sub_f32 v93, 1.0, v81\nMaaa 0,136,208\nRa 180,0,13312\nCV 92,92,93\nMaaa 0,140,216\nv_and_b32 v93, 0xffff0000, v92\n"
"v_lshlrev_b32 v92, 16, v92\nMaaa 4,140,220\nRa 184,0,14336\nv_lshlrev_b32 v64, 16, v12\nMaaa 4,136,212\nD1 1,16,46,2048\nv_and_b32 v65, 0xffff0000, v12\nMaaa 4,132,204\n"
"Ra 188,0,15360\nWL 8\nMaaa 4,128,196\nMU 64,64,92\nMU 65,65,92\nMaaa 8,144,192\nRa 128,0,16384\nv_lshlrev_b32 v66, 16, v13\nMaaa 8,148,200\nv_and_b32 v67, 0xffff0000, v13\n"
"MU 66,66,92\nMaaa 8,152,208\nRa 132,0,17408\nMU 67,67,92\nMaaa 8,156,216\nv_lshlrev_b32 v68, 16, v14\nv_and_b32 v69, 0xffff0000, v14\nMaaa 12,156,220\nRa 136,0,18432\nMU 68,68,92\n"
"Maaa 12,152,212\nD1 1,16,46,3072\nMU 69,69,92\nMaaa 12,148,204\nRa 140,0,19456\nWL 8\nMaaa 12,144,196\nv_lshlrev_b32 v70, 16, v15\nv_and_b32 v71, 0xffff0000, v15\nMaaa 16,160,192\n"
"Ra 144,0,20480\nMU 70,70,92\nMaaa 16,164,200\nMU 71,71,92\nv_lshlrev_b32 v72, 16, v16\nMaaa 16,168,208\nRa 148,0,21504\nv_and_b32 v73, 0xffff0000, v16\nMaaa 16,172,216\n"
"s_add_u32 m0, s89, 114688\ns_add_u32 s46, s93, 83968\nMaaa 20,172,220\nRa 152,0,22528\nMU 72,72,93\nMaaa 20,168,212\nD1 1,16,46,0\nMU 73,73,93\nMaaa 20,164,204\nRa 156,0,23552\n"
"WL 8\nMaaa 20,160,196\nv_lshlrev_b32 v74, 16, v17\nv_and_b32 v75, 0xffff0000, v17\nMaaa 24,176,192\nRa 160,0,24576\nMU 74,74,93\nMaaa 24,180,200\ns_add_u32 s4, s92, 8192\n"
"MU 75,75,93\nMaaa 24,184,208\nRa 164,0,25600\nv_lshlrev_b32 v76, 16, v18\nMaaa 24,188,216\nbuffer_load_dwordx4 a[224:227], v1, s[16:19], s4 offen offset:0 sc0 sc1\n"
"v_and_b32 v77, 0xffff0000, v18\nMaaa 28,188,220\nRa 168,0,26624\nMU 76,76,93\nMaaa 28,184,212\nD1 1,16,46,1024\nMU 77,77,93\nMaaa 28,180,204\nRa 172,0,27648\nWL 8\n"
"Maaa 28,176,196\nbuffer_load_dwordx4 a[228:231], v1, s[16:19], s4 offen offset:1024 sc0 sc1\nv_lshlrev_b32 v78, 16, v19\nMaaa 32,128,192\nRa 176,0,28672\n"
"v_and_b32 v79, 0xffff0000, v19\nMaaa 32,132,200\nMU 78,78,93\nMU 79,79,93\nMaaa 32,136,208\nRa 180,0,29696\nMaaa 32,140,216\n"
"buffer_load_dwordx4 a[232:235], v1, s[16:19], s4 offen offset:2048 sc0 sc1\nMaaa 36,140,220\nRa 184,0,30720\nMaaa 36,136,212\nD1 1,16,46,2048\nMaaa 36,132,204\nRa 188,0,31744\n"
"Maaa 36,128,196\ns_waitcnt vmcnt(10) lgkmcnt(8)\ns_barrier\nMaaa 40,144,192\nRa 128,2,16384\nbuffer_load_dwordx4 a[236:239], v1, s[16:19], s4 offen offset:3072 sc0 sc1\n"
"Maaa 40,148,200\nMaaa 40,152,208\nRa 132,2,17408\ns_add_u32 s4, s92, 12288\nMaaa 40,156,216\nMaaa 44,156,220\nRa 136,2,18432\n"
"buffer_load_dwordx4 a[240:243], v1, s[16:19], s4 offen offset:0 sc0 sc1\nMaaa 44,152,212\nD1 1,16,46,3072\nMaaa 44,148,204\nRa 140,2,19456\nWL 8\nMaaa 44,144,196\nMaaa 48,160,192\n"
"Ra 144,2,20480\nbuffer_load_dwordx4 a[244:247], v1, s[16:19], s4 offen offset:1024 sc0 sc1\nMaaa 48,164,200\nMaaa 48,168,208\nRa 148,2,21504\nMaaa 48,172,216\n"
"s_add_u32 m0, s89, 0\ns_add_u32 s46, s93, 101376\nMaaa 52,172,220\nRa 152,2,22528\nbuffer_load_dwordx4 a[248:251], v1, s[16:19], s4 offen offset:2048 sc0 sc1\nMaaa 52,168,212\n"
"D1 1,16,46,0\nMaaa 52,164,204\nRa 156,2,23552\nWL 8\nMaaa 52,160,196\nMaaa 56,176,192\nRa 160,2,24576\nbuffer_load_dwordx4 a[252:255], v1, s[16:19], s4 offen offset:3072 sc0 sc1\n"
"Maaa 56,180,200\nMaaa 56,184,208\nRa 164,2,25600\nMaaa 56,188,216\nMaaa 60,188,220\nRa 168,2,26624\nMaaa 60,184,212\nD1 1,16,46,1024\nMaaa 60,180,204\nRa 172,2,27648\nWL 8\n"
"Maaa 60,176,196\nMaaa 64,128,192\nRa 176,2,28672\nMaaa 64,132,200\nMaaa 64,136,208\nRa 180,2,29696\nMaaa 64,140,216\nMaaa 68,140,220\nRa 184,2,30720\nMaaa 68,136,212\n"
"D1 1,16,46,2048\nMaaa 68,132,204\nRa 188,2,31744\nWL 8\nMaaa 68,128,196\nMaaa 72,144,192\nRa 128,2,0\nMaaa 72,148,200\nMaaa 72,152,208\nRa 132,2,1024\nMaaa 72,156,216\n"
"Maaa 76,156,220\nRa 136,2,2048\nMaaa 76,152,212\nD1 1,16,46,3072\nMaaa 76,148,204\nRa 140,2,3072\nWL 8\nMaaa 76,144,196\nMaaa 80,160,192\nRa 144,2,4096\nMaaa 80,164,200\n"
"Maaa 80,168,208\nRa 148,2,5120\nMaaa 80,172,216\nMaaa 84,172,220\nRa 152,2,6144\nMaaa 84,168,212\nMaaa 84,164,204\nRa 156,2,7168\nWL 8\nMaaa 84,160,196\nMaaa 88,176,192\n"
"Ra 160,2,8192\nMaaa 88,180,200\nMaaa 88,184,208\nRa 164,2,9216\nMaaa 88,188,216\nMaaa 92,188,220\nRa 168,2,10240\nMaaa 92,184,212\nMaaa 92,180,204\nRa 172,2,11264\nWL 8\n"
"Maaa 92,176,196\nMaaa 96,128,192\nRa 176,2,12288\nMaaa 96,132,200\nMaaa 96,136,208\nRa 180,2,13312\nMaaa 96,140,216\nMaaa 100,140,220\nRa 184,2,14336\nMaaa 100,136,212\n"
"Maaa 100,132,204\nRa 188,2,15360\nMaaa 100,128,196\ns_waitcnt vmcnt(7) lgkmcnt(8)\ns_barrier\nMaaa 104,144,192\nRa 128,2,32768\nWV 3\nAR 22,227\nMaaa 104,148,200\nAR 23,231\n"
"v_max_u32 v21, v22, v23\nAR 22,235\nMaaa 104,152,208\nRa 132,2,33792\nAR 23,239\nMaaa 104,156,216\nv_max3_u32 v21, v21, v22, v23\nAR 22,243\nAR 23,247\nMaaa 108,156,220\n"
"Ra 136,2,34816\nv_max3_u32 v21, v21, v22, v23\nMaaa 108,152,212\nAR 22,251\nAR 23,255\nv_max3_u32 v21, v21, v22, v23\nMaaa 108,148,204\nRa 140,2,35840\nWL 8\nMaaa 108,144,196\n"
"v_cmp_eq_u32 vcc, -1, v21\ns_nop 0\ns_cbranch_vccnz .S180\n.S181:\nMaaa 0,160,224\nRa 144,2,36864\nMaaa 0,164,232\nMaaa 0,168,240\nRa 148,2,37888\nMaaa 0,172,248\nMaaa 4,172,252\n"
"Ra 152,2,38912\nMaaa 4,168,244\nMaaa 4,164,236\nRa 156,2,39936\nWL 8\nMaaa 4,160,228\nMaaa 8,176,224\nRa 160,2,40960\nMaaa 8,180,232\nMaaa 8,184,240\nRa 164,2,41984\n"
"Maaa 8,188,248\nMaaa 12,188,252\nRa 168,2,43008\nMaaa 12,184,244\nMaaa 12,180,236\nRa 172,2,44032\nWL 8\nMaaa 12,176,228\nAR 82,0\nAR 83,1\nMaaa 16,128,224\nRa 176,2,45056\n"
"CV 20,82,83\nAR 82,2\nMaaa 16,132,232\nAR 83,3\nCV 21,82,83\nMaaa 16,136,240\nRa 180,2,46080\nAR 82,8\nMaaa 16,140,248\nAR 83,9\nCV 22,82,83\nMaaa 20,140,252\nRa 184,2,47104\n"
"AR 82,10\nAR 83,11\nMaaa 20,136,244\nCV 23,82,83\ns_add_u32 s5, s95, 0\nMaaa 20,132,236\nRa 188,2,48128\nWL 8\nMaaa 20,128,228\nSv 20,1,16,5,0\nAR 82,4\nMaaa 24,144,224\n"
"Ra 128,2,49152\nAR 83,5\nCV 84,82,83\nMaaa 24,148,232\nAR 82,6\nAR 83,7\nMaaa 24,152,240\nRa 132,2,50176\nCV 85,82,83\nMaaa 24,156,248\nAR 82,12\nAR 83,13\nMaaa 28,156,252\n"
"Ra 136,2,51200\nCV 86,82,83\nAR 82,14\nMaaa 28,152,244\nAR 83,15\nCV 87,82,83\nMaaa 28,148,236\nRa 140,2,52224\nWL 8\nMaaa 28,144,228\nSv 84,1,16,5,1024\nAR 82,16\n"
"Maaa 32,160,224\nRa 144,2,53248\nAR 83,17\nCV 20,82,83\nMaaa 32,164,232\nAR 82,18\nAR 83,19\nMaaa 32,168,240\nRa 148,2,54272\nCV 21,82,83\nMaaa 32,172,248\nAR 82,24\nAR 83,25\n"
"Maaa 36,172,252\nRa 152,2,55296\nCV 22,82,83\nAR 82,26\nMaaa 36,168,244\nAR 83,27\nCV 23,82,83\nMaaa 36,164,236\nRa 156,2,56320\nWL 8\nMaaa 36,160,228\nSv 20,1,16,5,2048\n"
"AR 82,20\nMaaa 40,176,224\nRa 160,2,57344\nAR 83,21\nCV 84,82,83\nMaaa 40,180,232\nAR 82,22\nAR 83,23\nMaaa 40,184,240\nRa 164,2,58368\nCV 85,82,83\nMaaa 40,188,248\nAR 82,28\n"
"AR 83,29\nMaaa 44,188,252\nRa 168,2,59392\nCV 86,82,83\nAR 82,30\nMaaa 44,184,244\nAR 83,31\nCV 87,82,83\nMaaa 44,180,236\nRa 172,2,60416\nWL 8\nMaaa 44,176,228\n"
"Sv 84,1,16,5,3072\nAR 82,32\nMaaa 48,128,224\nRa 176,2,61440\nAR 83,33\nCV 20,82,83\nMaaa 48,132,232\nAR 82,34\nAR 83,35\nMaaa 48,136,240\nRa 180,2,62464\nCV 21,82,83\n"
"Maaa 48,140,248\nAR 82,40\nAR 83,41\nMaaa 52,140,252\nRa 184,2,63488\nCV 22,82,83\nAR 82,42\nMaaa 52,136,244\nAR 83,43\nCV 23,82,83\nMaaa 52,132,236\nRa 188,2,64512\n"
"Maaa 52,128,228\ns_waitcnt vmcnt(4) lgkmcnt(8)\ns_barrier\nMaaa 56,144,224\nRa 128,3,0\ns_add_u32 s5, s95, 4096\nSv 20,1,16,5,0\nMaaa 56,148,232\nAR 82,36\nAR 83,37\n"
"Maaa 56,152,240\nRa 132,3,1024\nCV 84,82,83\nMaaa 56,156,248\nAR 82,38\nAR 83,39\nMaaa 60,156,252\nRa 136,3,2048\nCV 85,82,83\nAR 82,44\nMaaa 60,152,244\nAR 83,45\nCV 86,82,83\n"
"Maaa 60,148,236\nRa 140,3,3072\nWL 8\nMaaa 60,144,228\nAR 82,46\nAR 83,47\nMaaa 64,160,224\nRa 144,3,4096\nCV 87,82,83\nSv 84,1,16,5,1024\nMaaa 64,164,232\nAR 82,48\nAR 83,49\n"
"Maaa 64,168,240\nRa 148,3,5120\nCV 20,82,83\nMaaa 64,172,248\nAR 82,50\nAR 83,51\nMaaa 68,172,252\nRa 152,3,6144\nCV 21,82,83\nAR 82,56\nMaaa 68,168,244\nAR 83,57\nCV 22,82,83\n"
"Maaa 68,164,236\nRa 156,3,7168\nWL 8\nMaaa 68,160,228\nAR 82,58\nAR 83,59\nMaaa 72,176,224\nRa 160,3,8192\nCV 23,82,83\nSv 20,1,16,5,2048\nMaaa 72,180,232\nAR 82,52\nAR 83,53\n"
"Maaa 72,184,240\nRa 164,3,9216\nCV 84,82,83\nMaaa 72,188,248\nAR 82,54\nAR 83,55\nMaaa 76,188,252\nRa 168,3,10240\nCV 85,82,83\nAR 82,60\nMaaa 76,184,244\nAR 83,61\nCV 86,82,83\n"
"Maaa 76,180,236\nRa 172,3,11264\nWL 8\nMaaa 76,176,228\nAR 82,62\nAR 83,63\nMaaa 80,128,224\nRa 176,3,12288\nCV 87,82,83\nSv 84,1,16,5,3072\nMaaa 80,132,232\nAR 82,64\nAR 83,65\n"
"Maaa 80,136,240\nRa 180,3,13312\nCV 20,82,83\nMaaa 80,140,248\nAR 82,66\nAR 83,67\nMaaa 84,140,252\nRa 184,3,14336\nCV 21,82,83\nAR 82,72\nMaaa 84,136,244\nAR 83,73\nCV 22,82,83\n"
"Maaa 84,132,236\nRa 188,3,15360\nWL 8\nMaaa 84,128,228\nAR 82,74\nAR 83,75\nMaaa 88,144,224\nCV 23,82,83\ns_add_u32 s5, s95, 8192\nSv 20,1,16,5,0\nMaaa 88,148,232\nAR 82,68\n"
"AR 83,69\nMaaa 88,152,240\nCV 84,82,83\nAR 82,70\nMaaa 88,156,248\nAR 83,71\nCV 85,82,83\nMaaa 92,156,252\nAR 82,76\nAR 83,77\nCV 86,82,83\nMaaa 92,152,244\nAR 82,78\nAR 83,79\n"
"Maaa 92,148,236\nWL 4\nMaaa 92,144,228\nCV 87,82,83\nSv 84,1,16,5,1024\nMaaa 96,160,224\nAR 82,80\nAR 83,81\nCV 20,82,83\nMaaa 96,164,232\nAR 82,82\nAR 83,83\nMaaa 96,168,240\n"
"CV 21,82,83\nAR 82,88\nMaaa 96,172,248\nAR 83,89\nCV 22,82,83\nMaaa 100,172,252\nAR 82,90\nAR 83,91\nCV 23,82,83\nMaaa 100,168,244\nSv 20,1,16,5,2048\nAR 82,84\nMaaa 100,164,236\n"
"WL 0\nMaaa 100,160,228\nAR 83,85\nCV 84,82,83\nMaaa 104,176,224\nAR 82,86\nAR 83,87\nCV 85,82,83\nMaaa 104,180,232\nAR 82,92\nAR 83,93\nMaaa 104,184,240\nCV 86,82,83\nAR 82,94\n"
"Maaa 104,188,248\nAR 83,95\nCV 87,82,83\nMaaa 108,188,252\nSv 84,1,16,5,3072\nMaaa 108,184,244\nMaaa 108,180,236\nMaaa 108,176,228\nAR 82,96\nAR 83,97\nCV 20,82,83\nAR 82,98\n"
"AR 83,99\nCV 21,82,83\nAR 82,104\nAR 83,105\nCV 22,82,83\nAR 82,106\nAR 83,107\nCV 23,82,83\ns_add_u32 s5, s95, 12288\nSv 20,1,16,5,0\nAR 82,100\nAR 83,101\nCV 84,82,83\n"
"AR 82,102\nAR 83,103\nCV 85,82,83\nAR 82,108\nAR 83,109\nCV 86,82,83\nAR 82,110\nAR 83,111\nCV 87,82,83\nSv 84,1,16,5,1024\ns_cmp_eq_u32 s13, 7\ns_cbranch_scc1 .Z186\n"
"s_mov_b32 s7, 262144\ns_mov_b32 s3, 0xff\n.Z187:\ns_bitcmp1_b32 s3, 0\ns_cbranch_scc0 .Z189\ns_add_u32 s2, s98, 0\n"
"buffer_load_dwordx4 v[96:99], v1, s[16:19], s2 offen offset:0 sc0 sc1\nbuffer_load_dwordx4 v[100:103], v1, s[16:19], s2 offen offset:1024 sc0 sc1\n.Z189:\ns_bitcmp1_b32 s3, 1\n"
"s_cbranch_scc0 .Z190\ns_add_u32 s2, s98, 2162688\nbuffer_load_dwordx4 v[104:107], v1, s[16:19], s2 offen offset:0 sc0 sc1\n"
"buffer_load_dwordx4 v[108:111], v1, s[16:19], s2 offen offset:1024 sc0 sc1\n.Z190:\ns_bitcmp1_b32 s3, 2\ns_cbranch_scc0 .Z191\ns_add_u32 s2, s98, 4325376\n"
"buffer_load_dwordx4 v[112:115], v1, s[16:19], s2 offen offset:0 sc0 sc1\nbuffer_load_dwordx4 v[116:119], v1, s[16:19], s2 offen offset:1024 sc0 sc1\n.Z191:\ns_bitcmp1_b32 s3, 3\n"
"s_cbranch_scc0 .Z192\ns_add_u32 s2, s98, 6488064\nbuffer_load_dwordx4 v[120:123], v1, s[16:19], s2 offen offset:0 sc0 sc1\n"
"buffer_load_dwordx4 v[124:127], v1, s[16:19], s2 offen offset:1024 sc0 sc1\n.Z192:\ns_bitcmp1_b32 s3, 4\ns_cbranch_scc0 .Z193\ns_add_u32 s2, s98, 8650752\n"
"buffer_load_dwordx4 v[32:35], v1, s[16:19], s2 offen offset:0 sc0 sc1\nbuffer_load_dwordx4 v[36:39], v1, s[16:19], s2 offen offset:1024 sc0 sc1\n.Z193:\ns_bitcmp1_b32 s3, 5\n"
"s_cbranch_scc0 .Z194\ns_add_u32 s2, s98, 10813440\nbuffer_load_dwordx4 v[40:43], v1, s[16:19], s2 offen offset:0 sc0 sc1\n"
"buffer_load_dwordx4 v[44:47], v1, s[16:19], s2 offen offset:1024 sc0 sc1\n.Z194:\ns_bitcmp1_b32 s3, 6\ns_cbranch_scc0 .Z195\ns_add_u32 s2, s98, 12976128\n"
"buffer_load_dwordx4 v[48:51], v1, s[16:19], s2 offen offset:0 sc0 sc1\nbuffer_load_dwordx4 v[52:55], v1, s[16:19], s2 offen offset:1024 sc0 sc1\n.Z195:\ns_bitcmp1_b32 s3, 7\n"
"s_cbranch_scc0 .Z196\ns_add_u32 s2, s98, 15138816\nbuffer_load_dwordx4 v[56:59], v1, s[16:19], s2 offen offset:0 sc0 sc1\n"
"buffer_load_dwordx4 v[60:63], v1, s[16:19], s2 offen offset:1024 sc0 sc1\n.Z196:\ns_mov_b32 s3, 0\nWV 0\nv_max_u32 v82, v99, v103\nv_cmp_eq_u32 s[44:45], -1, v82\n"
"v_max_u32 v82, v107, v111\nv_cmp_eq_u32 s[46:47], -1, v82\nv_max_u32 v82, v115, v119\nv_cmp_eq_u32 s[48:49], -1, v82\nv_max_u32 v82, v123, v127\nv_cmp_eq_u32 s[50:51], -1, v82\n"
"v_max_u32 v82, v35, v39\nv_cmp_eq_u32 s[52:53], -1, v82\nv_max_u32 v82, v43, v47\nv_cmp_eq_u32 s[54:55], -1, v82\nv_max_u32 v82, v51, v55\nv_cmp_eq_u32 s[56:57], -1, v82\n"
"v_max_u32 v82, v59, v63\nv_cmp_eq_u32 s[58:59], -1, v82\ns_cmp_lg_u64 s[44:45], 0\ns_cselect_b32 s2, 1, 0\ns_or_b32 s3, s3, s2\ns_cmp_lg_u64 s[46:47], 0\ns_cselect_b32 s2, 2, 0\n"
"s_or_b32 s3, s3, s2\ns_cmp_lg_u64 s[48:49], 0\ns_cselect_b32 s2, 4, 0\ns_or_b32 s3, s3, s2\ns_cmp_lg_u64 s[50:51], 0\ns_cselect_b32 s2, 8, 0\ns_or_b32 s3, s3, s2\n"
"s_cmp_lg_u64 s[52:53], 0\ns_cselect_b32 s2, 16, 0\ns_or_b32 s3, s3, s2\ns_cmp_lg_u64 s[54:55], 0\ns_cselect_b32 s2, 32, 0\ns_or_b32 s3, s3, s2\ns_cmp_lg_u64 s[56:57], 0\n"
"s_cselect_b32 s2, 64, 0\ns_or_b32 s3, s3, s2\ns_cmp_lg_u64 s[58:59], 0\ns_cselect_b32 s2, 128, 0\ns_or_b32 s3, s3, s2\ns_cbranch_scc0 .Z188\ns_sub_u32 s7, s7, 1\n"
"s_cbranch_scc0 .Z187\n.Z188:\nZvvv 136,6,96\nMvvv 136,6,104\nMvvv 136,6,112\nMvvv 136,6,120\nMvvv 136,6,32\nMvvv 136,6,40\nMvvv 136,6,48\nMvvv 136,6,56\nZvvv 140,4,96\n"
"Mvvv 140,4,104\nMvvv 140,4,112\nMvvv 140,4,120\nMvvv 140,4,32\nMvvv 140,4,40\nMvvv 140,4,48\nMvvv 140,4,56\nZvvv 144,6,100\nMvvv 144,6,108\nMvvv 144,6,116\nMvvv 144,6,124\n"
"Mvvv 144,6,36\nMvvv 144,6,44\nMvvv 144,6,52\nMvvv 144,6,60\nZvvv 148,4,100\nMvvv 148,4,108\nMvvv 148,4,116\nMvvv 148,4,124\nMvvv 148,4,36\nMvvv 148,4,44\nMvvv 148,4,52\n"
"Mvvv 148,4,60\nFM 64,80,136\nFM 65,80,137\nFM 66,80,138\nFM 67,80,139\nFM 68,80,140\nFM 69,80,141\nFM 70,80,142\nFM 71,80,143\nCV 12,64,65\nCV 13,66,67\nCV 14,68,69\nCV 15,70,71\n"
"Sv 12,91,36,91,0\nFM 72,81,144\nFM 73,81,145\nFM 74,81,146\nFM 75,81,147\nFM 76,81,148\nFM 77,81,149\nFM 78,81,150\nFM 79,81,151\nCV 16,72,73\nCV 17,74,75\nCV 18,76,77\n"
"CV 19,78,79\nSv 16,91,36,1,0\n.Z186:\ns_endpgm\n.E148:\nv_mov_b32 v12, s97\nv_mov_b32 v13, s97\nv_mov_b32 v14, s97\nv_mov_b32 v15, s97\nv_cmpx_gt_i32 vcc, 3, v3\n"
"v_mov_b32 v7, s97\nv_mov_b32 v19, s97\nv_cmpx_gt_i32 vcc, 2, v3\nv_mov_b32 v6, s97\nv_mov_b32 v18, s97\nv_cmpx_gt_i32 vcc, 1, v3\nv_mov_b32 v5, s97\nv_mov_b32 v17, s97\n"
"v_cmpx_gt_i32 vcc, 0, v3\nv_mov_b32 v4, s97\nv_mov_b32 v16, s97\nv_cmpx_gt_i32 vcc, -16, v3\nv_mov_b32 v8, s97\nv_mov_b32 v9, s97\nv_mov_b32 v10, s97\nv_mov_b32 v11, s97\n"
"s_mov_b64 exec, -1\ns_branch .E149\n.T150:\ns_mov_b32 s62, 2\ns_call_b64 s[80:81], .RB13\ns_branch .T151\n.E152:\nv_mov_b32 v12, s97\nv_mov_b32 v13, s97\nv_mov_b32 v14, s97\n"
"v_mov_b32 v15, s97\nv_cmpx_gt_i32 vcc, 3, v0\nv_mov_b32 v7, s97\nv_mov_b32 v19, s97\nv_cmpx_gt_i32 vcc, 2, v0\nv_mov_b32 v6, s97\nv_mov_b32 v18, s97\nv_cmpx_gt_i32 vcc, 1, v0\n"
"v_mov_b32 v5, s97\nv_mov_b32 v17, s97\nv_cmpx_gt_i32 vcc, 0, v0\nv_mov_b32 v4, s97\nv_mov_b32 v16, s97\nv_cmpx_gt_i32 vcc, -16, v0\nv_mov_b32 v8, s97\nv_mov_b32 v9, s97\n"
"v_mov_b32 v10, s97\nv_mov_b32 v11, s97\ns_mov_b64 exec, -1\ns_branch .E153\n.T154:\ns_mov_b32 s62, 5\ns_call_b64 s[80:81], .RA12\ns_branch .T155\n.T156:\ns_mov_b32 s62, 5\n"
"s_call_b64 s[80:81], .RA12\ns_branch .T157\n.E158:\ns_swappc_b64 s[80:81], s[8:9]\ns_branch .E159\n.E160:\nv_mov_b32 v12, s97\nv_mov_b32 v13, s97\nv_mov_b32 v14, s97\n"
"v_mov_b32 v15, s97\nv_cmpx_gt_i32 vcc, 3, v3\nv_mov_b32 v7, s97\nv_mov_b32 v19, s97\nv_cmpx_gt_i32 vcc, 2, v3\nv_mov_b32 v6, s97\nv_mov_b32 v18, s97\nv_cmpx_gt_i32 vcc, 1, v3\n"
"v_mov_b32 v5, s97\nv_mov_b32 v17, s97\nv_cmpx_gt_i32 vcc, 0, v3\nv_mov_b32 v4, s97\nv_mov_b32 v16, s97\nv_cmpx_gt_i32 vcc, -16, v3\nv_mov_b32 v8, s97\nv_mov_b32 v9, s97\n"
"v_mov_b32 v10, s97\nv_mov_b32 v11, s97\ns_mov_b64 exec, -1\ns_branch .E161\n.T163:\ns_mov_b32 s62, 4\ns_call_b64 s[80:81], .RB13\ns_branch .T164\n.E165:\nv_mov_b32 v12, s97\n"
"v_mov_b32 v13, s97\nv_mov_b32 v14, s97\nv_mov_b32 v15, s97\nv_cmpx_gt_i32 vcc, 3, v0\nv_mov_b32 v7, s97\nv_mov_b32 v19, s97\nv_cmpx_gt_i32 vcc, 2, v0\nv_mov_b32 v6, s97\n"
"v_mov_b32 v18, s97\nv_cmpx_gt_i32 vcc, 1, v0\nv_mov_b32 v5, s97\nv_mov_b32 v17, s97\nv_cmpx_gt_i32 vcc, 0, v0\nv_mov_b32 v4, s97\nv_mov_b32 v16, s97\nv_cmpx_gt_i32 vcc, -16, v0\n"
"v_mov_b32 v8, s97\nv_mov_b32 v9, s97\nv_mov_b32 v10, s97\nv_mov_b32 v11, s97\ns_mov_b64 exec, -1\ns_branch .E166\n.E167:\nv_mov_b32 v91, 0x40000000\nv_cmp_gt_u32 vcc, 0x200, v1\n"
"WV 10\ns_nop 0\nv_cndmask_b32 v91, v91, v1, vcc\nSv 92,91,16,65,0\ns_branch .E168\n.T169:\ns_mov_b32 s62, 4\ns_call_b64 s[80:81], .RA12\ns_branch .T170\n.E171:\n"
"s_swappc_b64 s[80:81], s[8:9]\ns_branch .E172\n.E173:\nv_mov_b32 v12, s97\nv_mov_b32 v13, s97\nv_mov_b32 v14, s97\nv_mov_b32 v15, s97\nv_cmpx_gt_i32 vcc, 3, v3\nv_mov_b32 v7, s97\n"
"v_mov_b32 v19, s97\nv_cmpx_gt_i32 vcc, 2, v3\nv_mov_b32 v6, s97\nv_mov_b32 v18, s97\nv_cmpx_gt_i32 vcc, 1, v3\nv_mov_b32 v5, s97\nv_mov_b32 v17, s97\nv_cmpx_gt_i32 vcc, 0, v3\n"
"v_mov_b32 v4, s97\nv_mov_b32 v16, s97\nv_cmpx_gt_i32 vcc, -16, v3\nv_mov_b32 v8, s97\nv_mov_b32 v9, s97\nv_mov_b32 v10, s97\nv_mov_b32 v11, s97\ns_mov_b64 exec, -1\n"
"s_branch .E174\n.T175:\ns_mov_b32 s62, 6\ns_call_b64 s[80:81], .RB13\ns_branch .T176\n.S177:\ns_mov_b32 s7, 262144\n.S179:\ns_sleep 1\ns_add_u32 s4, s92, 0\n"
"buffer_load_dwordx4 a[192:195], v1, s[16:19], s4 offen offset:0 sc0 sc1\nbuffer_load_dwordx4 a[196:199], v1, s[16:19], s4 offen offset:1024 sc0 sc1\n"
"buffer_load_dwordx4 a[200:203], v1, s[16:19], s4 offen offset:2048 sc0 sc1\nbuffer_load_dwordx4 a[204:207], v1, s[16:19], s4 offen offset:3072 sc0 sc1\ns_add_u32 s4, s92, 4096\n"
"buffer_load_dwordx4 a[208:211], v1, s[16:19], s4 offen offset:0 sc0 sc1\nbuffer_load_dwordx4 a[212:215], v1, s[16:19], s4 offen offset:1024 sc0 sc1\n"
"buffer_load_dwordx4 a[216:219], v1, s[16:19], s4 offen offset:2048 sc0 sc1\nbuffer_load_dwordx4 a[220:223], v1, s[16:19], s4 offen offset:3072 sc0 sc1\nWV 0\nAR 22,195\nAR 23,199\n"
"v_max_u32 v21, v22, v23\nAR 22,203\nAR 23,207\nv_max3_u32 v21, v21, v22, v23\nAR 22,211\nAR 23,215\nv_max3_u32 v21, v21, v22, v23\nAR 22,219\nAR 23,223\n"
"v_max3_u32 v21, v21, v22, v23\nv_cmp_eq_u32 vcc, -1, v21\ns_nop 0\ns_cbranch_vccz .S178\ns_sub_u32 s7, s7, 1\ns_cbranch_scc0 .S179\ns_branch .S178\n.S180:\ns_mov_b32 s7, 262144\n"
".S182:\ns_sleep 1\ns_add_u32 s4, s92, 8192\nbuffer_load_dwordx4 a[224:227], v1, s[16:19], s4 offen offset:0 sc0 sc1\n"
"buffer_load_dwordx4 a[228:231], v1, s[16:19], s4 offen offset:1024 sc0 sc1\nbuffer_load_dwordx4 a[232:235], v1, s[16:19], s4 offen offset:2048 sc0 sc1\n"
"buffer_load_dwordx4 a[236:239], v1, s[16:19], s4 offen offset:3072 sc0 sc1\ns_add_u32 s4, s92, 12288\nbuffer_load_dwordx4 a[240:243], v1, s[16:19], s4 offen offset:0 sc0 sc1\n"
"buffer_load_dwordx4 a[244:247], v1, s[16:19], s4 offen offset:1024 sc0 sc1\nbuffer_load_dwordx4 a[248:251], v1, s[16:19], s4 offen offset:2048 sc0 sc1\n"
"buffer_load_dwordx4 a[252:255], v1, s[16:19], s4 offen offset:3072 sc0 sc1\nWV 0\nAR 22,227\nAR 23,231\nv_max_u32 v21, v22, v23\nAR 22,235\nAR 23,239\n"
"v_max3_u32 v21, v21, v22, v23\nAR 22,243\nAR 23,247\nv_max3_u32 v21, v21, v22, v23\nAR 22,251\nAR 23,255\nv_max3_u32 v21, v21, v22, v23\nv_cmp_eq_u32 vcc, -1, v21\ns_nop 0\n"
"s_cbranch_vccz .S181\ns_sub_u32 s7, s7, 1\ns_cbranch_scc0 .S182\ns_branch .S181\n.S183:\ns_mov_b32 s7, 262144\n.S185:\ns_sleep 1\n"
"buffer_load_dwordx4 v[64:67], v89, s[16:19], s0 offen offset:0 sc0 sc1\nbuffer_load_dwordx4 v[68:71], v89, s[16:19], s0 offen offset:16 sc0 sc1\n"
"buffer_load_dwordx4 v[72:75], v89, s[16:19], s0 offen offset:1792 sc0 sc1\nbuffer_load_dwordx4 v[76:79], v89, s[16:19], s0 offen offset:1808 sc0 sc1\nWV 0\n"
"v_xor_b32 v92, s15, v66\nv_xor_b32 v93, s15, v70\nv_xor_b32 v94, s15, v74\nv_xor_b32 v95, s15, v78\nv_or3_b32 v92, v92, v93, v94\nv_or_b32 v92, v92, v95\nv_cmp_ne_u32 vcc, 0, v92\n"
"s_nop 0\ns_cbranch_vccz .S184\ns_sub_u32 s7, s7, 1\ns_cbranch_scc0 .S185\ns_branch .S184\n.Lcode1:\n"
  :: "s"(__builtin_amdgcn_kernarg_segment_ptr()), "s"(blockIdx.x), "v"(threadIdx.x)
  : "memory", "scc", "vcc", "v1", "v2", "v3", "v4", "v5", "v6", "v7", "v8", "v9", "v10", "v11", "v12", "v13", "v14", "v15", "v16", "v17", "v18", "v19", "v20", "v21", "v22", "v23", "v24", "v25", "v26", "v27", "v28", "v29", "v30", "v31", "v32", "v33", "v34", "v35", "v36", "v37", "v38", "v39", "v40", "v41", "v42", "v43", "v44", "v45", "v46", "v47", "v48", "v49", "v50", "v51", "v52", "v53", "v54", "v55", "v56", "v57", "v58", "v59", "v60", "v61", "v62", "v63", "v64", "v65", "v66", "v67", "v68", "v69", "v70", "v71", "v72", "v73", "v74", "v75", "v76", "v77", "v78", "v79", "v80", "v81", "v82", "v83", "v84", "v85", "v86", "v87", "v88", "v89", "v90", "v91", "v92", "v93", "v94", "v95", "v96", "v97", "v98", "v99", "v100", "v101", "v102", "v103", "v104", "v105", "v106", "v107", "v108", "v109", "v110", "v111", "v112", "v113", "v114", "v115", "v116", "v117", "v118", "v119", "v120", "v121", "v122", "v123", "v124", "v125", "v126", "v127", "v128", "v129", "v130", "v131", "v132", "v133", "v134", "v135", "v136", "v137", "v138", "v139", "v140", "v141", "v142", "v143", "v144", "v145", "v146", "v147", "v148", "v149", "v150", "v151", "v152", "v153", "v154", "v155", "v156", "v157", "v158", "v159", "v160", "v161", "v162", "v163", "v164", "v165", "v166", "v167", "v168", "v169", "v170", "v171", "v172", "v173", "v174", "v175", "v176", "v177", "v178", "v179", "v180", "v181", "v182", "v183", "v184", "v185", "v186", "v187", "v188", "v189", "v190", "v191", "v192", "v193", "v194", "v195", "v196", "v197", "v198", "v199", "v200", "v201", "v202", "v203", "v204", "v205", "v206", "v207", "v208", "v209", "v210", "v211", "v212", "v213", "v214", "v215", "v216", "v217", "v218", "v219", "v220", "v221", "v222", "v223", "v224", "v225", "v226", "v227", "v228", "v229", "v230", "v231", "v232", "v233", "v234", "v235", "v236", "v237", "v238", "v239", "v240", "v241", "v242", "v243", "v244", "v245", "v246", "v247", "v248", "v249", "v250", "v251", "v252", "v253", "v254", "v255", "a0", "a1", "a2", "a3", "a4", "a5", "a6", "a7", "a8", "a9", "a10", "a11", "a12", "a13", "a14", "a15", "a16", "a17", "a18", "a19", "a20", "a21", "a22", "a23", "a24", "a25", "a26", "a27", "a28", "a29", "a30", "a31", "a32", "a33", "a34", "a35", "a36", "a37", "a38", "a39", "a40", "a41", "a42", "a43", "a44", "a45", "a46", "a47", "a48", "a49", "a50", "a51", "a52", "a53", "a54", "a55", "a56", "a57", "a58", "a59", "a60", "a61", "a62", "a63", "a64", "a65", "a66", "a67", "a68", "a69", "a70", "a71", "a72", "a73", "a74", "a75", "a76", "a77", "a78", "a79", "a80", "a81", "a82", "a83", "a84", "a85", "a86", "a87", "a88", "a89", "a90", "a91", "a92", "a93", "a94", "a95", "a96", "a97", "a98", "a99", "a100", "a101", "a102", "a103", "a104", "a105", "a106", "a107", "a108", "a109", "a110", "a111", "a112", "a113", "a114", "a115", "a116", "a117", "a118", "a119", "a120", "a121", "a122", "a123", "a124", "a125", "a126", "a127", "a128", "a129", "a130", "a131", "a132", "a133", "a134", "a135", "a136", "a137", "a138", "a139", "a140", "a141", "a142", "a143", "a144", "a145", "a146", "a147", "a148", "a149", "a150", "a151", "a152", "a153", "a154", "a155", "a156", "a157", "a158", "a159", "a160", "a161", "a162", "a163", "a164", "a165", "a166", "a167", "a168", "a169", "a170", "a171", "a172", "a173", "a174", "a175", "a176", "a177", "a178", "a179", "a180", "a181", "a182", "a183", "a184", "a185", "a186", "a187", "a188", "a189", "a190", "a191", "a192", "a193", "a194", "a195", "a196", "a197", "a198", "a199", "a200", "a201", "a202", "a203", "a204", "a205", "a206", "a207", "a208", "a209", "a210", "a211", "a212", "a213", "a214", "a215", "a216", "a217", "a218", "a219", "a220", "a221", "a222", "a223", "a224", "a225", "a226", "a227", "a228", "a229", "a230", "a231", "a232", "a233", "a234", "a235", "a236", "a237", "a238", "a239", "a240", "a241", "a242", "a243", "a244", "a245", "a246", "a247", "a248", "a249", "a250", "a251", "a252", "a253", "a254", "a255", "s8", "s9", "s10", "s11", "s12", "s13", "s14", "s15", "s16", "s17", "s18", "s19", "s20", "s21", "s22", "s23", "s24", "s25", "s26", "s27", "s28", "s29", "s30", "s31", "s33", "s34", "s35", "s36", "s37", "s38", "s39", "s40", "s41", "s42", "s43", "s44", "s45", "s46", "s47", "s48", "s49", "s50", "s51", "s52", "s53", "s54", "s55", "s56", "s57", "s58", "s59", "s60", "s61", "s62", "s63", "s64", "s65", "s66", "s67", "s68", "s69", "s70", "s71", "s72", "s73", "s74", "s75", "s76", "s77", "s78", "s79", "s80", "s81", "s82", "s83", "s84", "s85", "s86", "s87", "s88", "s89", "s90", "s91", "s92", "s93", "s94", "s95", "s96", "s97", "s98", "s99");
#endif
}

extern "C" void kernel_launch(void* const* d_in, const int* in_sizes, int n_in,
                              void* d_out, int out_size, void* d_ws, size_t ws_size,
                              hipStream_t stream) {
  (void)hipFuncSetAttribute((const void*)k_main, hipFuncAttributeMaxDynamicSharedMemorySize, 163840);
  k_main<<<256, 256, 163840, stream>>>(d_in[0], d_in[1], d_in[2], d_in[3], d_in[4], d_ws, d_in[11], d_in[7], d_in[5], d_in[8], d_in[9], d_in[10], d_out);
}
```

```cpp
#include <hip/hip_runtime.h>
#include <stdint.h>

extern "C" __global__ __launch_bounds__(256, 1)
void k_main(const void* x, const void* ckv, const void* wq, const void* wuk, const void* wuv, void* ws,
            const void* gate_w, const void* w_r, const void* wo, const void* wg, const void* wu, const void* wd, void* out) {
#if defined(__HIP_DEVICE_COMPILE__)
  asm volatile(
"s_mov_b64 s[8:9], %0\ns_mov_b32 s10, %1\nv_mov_b32 v0, %2\n.macro WL n\ns_waitcnt lgkmcnt(\\n)\n.endm\n.macro D0 x,r,s,o\n"
"buffer_load_dwordx4 v\\x, s[\\r:\\r+3], s\\s offen offset:\\o lds\n.endm\n.macro WV n\ns_waitcnt vmcnt(\\n)\n.endm\n.macro Ta d,x,o\n"
"ds_read_b64_tr_b16 a[\\d:\\d+1], v\\x offset:\\o\n.endm\n.macro Ra d,x,o\nds_read_b128 a[\\d:\\d+3], v\\x offset:\\o\n.endm\n.macro Zvaa d,x,y\n"
"v_mfma_f32_16x16x32_bf16 v[\\d:\\d+3], a[\\x:\\x+3], a[\\y:\\y+3], 0\n.endm\n.macro Zaaa d,x,y\nv_mfma_f32_16x16x32_bf16 a[\\d:\\d+3], a[\\x:\\x+3], a[\\y:\\y+3], 0\n.endm\n"
".macro Mvaa d,x,y\nv_mfma_f32_16x16x32_bf16 v[\\d:\\d+3], a[\\x:\\x+3], a[\\y:\\y+3], v[\\d:\\d+3]\n.endm\n.macro Maaa d,x,y\n"
"v_mfma_f32_16x16x32_bf16 a[\\d:\\d+3], a[\\x:\\x+3], a[\\y:\\y+3], a[\\d:\\d+3]\n.endm\n.macro MU d,x,y\nv_mul_f32 v\\d, v\\x, v\\y\n.endm\n.macro CV d,x,y\n"
"v_cvt_pk_bf16_f32 v\\d, v\\x, v\\y\n.endm\n.macro FM d,x,y\nv_fmac_f32 v\\d, v\\x, v\\y\n.endm\n.macro AD d,x,y\nv_add_f32 v\\d, v\\x, v\\y\n.endm\n.macro EX d,x\n"
"v_exp_f32 v\\d, v\\x\n.endm\n.macro Sv d,x,r,s,o\nbuffer_store_dwordx4 v[\\d:\\d+3], v\\x, s[\\r:\\r+3], s\\s offen offset:\\o sc0 sc1\n.endm\n.macro Rv d,x,o\n"
"ds_read_b128 v[\\d:\\d+3], v\\x offset:\\o\n.endm\n.macro D1 x,r,s,o\nbuffer_load_dwordx4 v\\x, s[\\r:\\r+3], s\\s offen offset:\\o sc1 lds\n.endm\n.macro AR d,x\n"
"v_accvgpr_read_b32 v\\d, a\\x\n.endm\n.macro Tv d,x,o\nds_read_b64_tr_b16 v[\\d:\\d+1], v\\x offset:\\o\n.endm\n.macro AW d,x\nv_accvgpr_write_b32 a\\d, v\\x\n.endm\n"
".macro Cvav d,x,y,c\nv_mfma_f32_16x16x32_bf16 v[\\d:\\d+3], a[\\x:\\x+3], v[\\y:\\y+3], v[\\c:\\c+3]\n.endm\n.macro Mvav d,x,y\n"
"v_mfma_f32_16x16x32_bf16 v[\\d:\\d+3], a[\\x:\\x+3], v[\\y:\\y+3], v[\\d:\\d+3]\n.endm\n.macro Sa d,x,r,s,o\n"
"buffer_store_dwordx4 a[\\d:\\d+3], v\\x, s[\\r:\\r+3], s\\s offen offset:\\o sc0 sc1\n.endm\n.macro Zaav d,x,y\n"
"v_mfma_f32_16x16x32_bf16 a[\\d:\\d+3], a[\\x:\\x+3], v[\\y:\\y+3], 0\n.endm\n.macro Maav d,x,y\nv_mfma_f32_16x16x32_bf16 a[\\d:\\d+3], a[\\x:\\x+3], v[\\y:\\y+3], a[\\d:\\d+3]\n"
".endm\n.macro SU d,x,y\nv_sub_f32 v\\d, v\\x, v\\y\n.endm\n.macro Mava d,x,y\nv_mfma_f32_16x16x32_bf16 a[\\d:\\d+3], v[\\x:\\x+3], a[\\y:\\y+3], a[\\d:\\d+3]\n.endm\n"
".macro Zvav d,x,y\nv_mfma_f32_16x16x32_bf16 v[\\d:\\d+3], a[\\x:\\x+3], v[\\y:\\y+3], 0\n.endm\n.macro Zvvv d,x,y\n"
"v_mfma_f32_16x16x32_bf16 v[\\d:\\d+3], v[\\x:\\x+3], v[\\y:\\y+3], 0\n.endm\n.macro Mvvv d,x,y\nv_mfma_f32_16x16x32_bf16 v[\\d:\\d+3], v[\\x:\\x+3], v[\\y:\\y+3], v[\\d:\\d+3]\n"
".endm\n.macro Cvaa d,x,y,c\nv_mfma_f32_16x16x32_bf16 v[\\d:\\d+3], a[\\x:\\x+3], a[\\y:\\y+3], v[\\c:\\c+3]\n.endm\n.Lcode0:\ns_nop 0\ns_load_dwordx16 s[44:59], s[8:9], 0x0\n"
"s_load_dwordx8 s[60:67], s[8:9], 0x40\ns_load_dwordx2 s[68:69], s[8:9], 0x60\nv_lshrrev_b32 v2, 6, v0\nv_and_b32 v0, 63, v0\nv_readfirstlane_b32 s11, v2\nv_lshlrev_b32 v1, 4, v0\n"
"s_and_b32 s73, s10, 7\ns_lshr_b32 s74, s10, 3\ns_and_b32 s2, s74, 1\ns_lshl1_add_u32 s12, s73, s2\ns_bfe_u32 s13, s74, 0x30001\ns_lshr_b32 s14, s74, 4\ns_xor_b32 s14, s14, 1\n"
"s_mov_b32 s70, 0\nv_lshrrev_b32 v32, 5, v0\ns_and_b32 s3, s11, 1\ns_lshl_b32 s2, s3, 3\nv_lshl_or_b32 v33, v32, 1, s2\nv_and_b32 v34, 31, v0\nv_xor_b32 v33, v33, v34\n"
"v_lshlrev_b32 v33, 4, v33\nv_lshl_or_b32 v6, v32, 13, v33\nv_xor_b32 v33, 64, v6\nv_add_u32 v7, 15360, v33\nv_add_u32 v8, 30720, v6\nv_add_u32 v9, 46080, v33\n"
"s_lshl_b32 s2, s11, 16\ns_lshl_b32 s4, s12, 9\ns_add_u32 s5, s2, s4\nWL 0\ns_add_u32 s54, s54, 0x1fffff\ns_addc_u32 s55, s55, 0\ns_and_b32 s54, s54, 0xffe00000\n"
"s_mov_b32 s16, s54\ns_and_b32 s17, s55, 0xffff\ns_mov_b32 s18, 0xc0ae800\ns_mov_b32 s19, 0x20000\ns_mov_b32 s28, s44\ns_and_b32 s29, s45, 0xffff\ns_mov_b32 s30, 0x1c0000\n"
"s_mov_b32 s31, 0x20000\ns_mov_b32 s32, s48\ns_and_b32 s33, s49, 0xffff\ns_mov_b32 s34, 0x380000\ns_mov_b32 s35, 0x20000\ns_mov_b32 s20, s46\ns_and_b32 s21, s47, 0xffff\n"
"s_mov_b32 s22, 0x400000\ns_mov_b32 s23, 0x20000\ns_bitcmp1_b32 s13, 0\ns_cselect_b64 s[50:51], s[52:53], s[50:51]\ns_mov_b32 s24, s50\ns_and_b32 s25, s51, 0xffff\n"
"s_mov_b32 s26, 0x800000\ns_mov_b32 s27, 0x20000\ns_mov_b32 s2, 0xc048000\ns_load_dword s15, s[54:55], s2\ns_sub_u32 s44, s5, 0x40000\ns_mov_b32 s51, s5\ns_lshl_b32 s47, s11, 12\n"
"s_add_u32 s47, s47, 0x10000\ns_lshl_b32 s48, s11, 13\ns_cmp_eq_u32 s14, 0\ns_cbranch_scc1 .R01\ns_add_u32 m0, s47, 0\ns_add_u32 s44, s44, 0x40000\nD0 6,24,44,0\nD0 7,24,44,1024\n"
"D0 8,24,44,2048\nD0 9,24,44,3072\nv_lshrrev_b32 v32, 4, v0\nv_and_b32 v33, 7, v0\nv_xor_b32 v33, v33, v32\nv_lshlrev_b32 v33, 4, v33\nv_lshrrev_b32 v32, 3, v0\n"
"v_lshl_or_b32 v2, v32, 11, v33\nv_xor_b32 v33, 64, v2\nv_add_u32 v3, 15360, v33\nv_add_u32 v4, 30720, v2\nv_add_u32 v5, 46080, v33\ns_lshr_b32 s2, s13, 2\n"
"s_bfe_u32 s3, s13, 0x10001\ns_mul_i32 s3, s3, 17\ns_add_u32 s2, s2, s3\ns_lshl2_add_u32 s3, s14, s11\ns_lshl_b32 s3, s3, 2\ns_lshr_b32 s3, 0xe420ca86, s3\ns_and_b32 s3, s3, 15\n"
"s_add_u32 s2, s2, s3\ns_cmp_eq_u32 s2, 32\ns_cselect_b32 s4, 16, s2\ns_lshl_b32 s45, s4, 17\ns_sub_u32 s45, s45, 128\ns_add_u32 m0, s48, 0\ns_add_u32 s45, s45, 128\nD0 2,20,45,0\n"
"D0 3,20,45,1024\nD0 4,20,45,2048\nD0 5,20,45,3072\ns_add_u32 m0, s48, 4096\ns_add_u32 s46, s45, 0x10000\nD0 2,20,46,0\nD0 3,20,46,1024\nD0 4,20,46,2048\nD0 5,20,46,3072\n"
"s_add_u32 m0, s47, 16384\ns_add_u32 s44, s44, 0x40000\nD0 6,24,44,0\nD0 7,24,44,1024\nD0 8,24,44,2048\nD0 9,24,44,3072\nv_mov_b32 v52, 0x40000000\nv_cmp_gt_u32 vcc, 56, v0\n"
"s_lshl2_add_u32 s2, s10, s11\ns_mul_i32 s2, s2, 1792\nv_cndmask_b32 v53, v52, v1, vcc\ns_mov_b32 s36, s56\ns_and_b32 s37, s57, 0xffff\ns_mov_b32 s38, 0x380\n"
"s_mov_b32 s39, 0x20000\ns_mov_b32 s40, s58\ns_and_b32 s41, s59, 0xffff\ns_mov_b32 s42, 0x380\ns_mov_b32 s43, 0x20000\n"
"buffer_load_dwordx4 v[32:35], v53, s[28:31], s2 offen offset:0\nbuffer_load_dwordx4 v[36:39], v53, s[28:31], s2 offen offset:896\n"
"buffer_load_dwordx4 v[40:43], v53, s[36:39], 0 offen offset:0\nbuffer_load_dwordx4 v[44:47], v53, s[40:43], 0 offen offset:0\ns_add_u32 m0, s48, 32768\ns_add_u32 s45, s45, 128\n"
"D0 2,20,45,0\nD0 3,20,45,1024\nD0 4,20,45,2048\nD0 5,20,45,3072\ns_add_u32 m0, s48, 36864\ns_add_u32 s46, s45, 0x10000\nD0 2,20,46,0\nD0 3,20,46,1024\nD0 4,20,46,2048\n"
"D0 5,20,46,3072\ns_add_u32 m0, s47, 32768\ns_add_u32 s44, s44, 0x40000\nD0 6,24,44,0\nD0 7,24,44,1024\nD0 8,24,44,2048\nD0 9,24,44,3072\ns_getpc_b64 s[40:41]\n.PC14:\n"
"s_sub_u32 s40, s40, .PC14-.Lcode0\ns_subb_u32 s41, s41, 0\ns_mov_b32 s42, .Lcode1-.Lcode0\ns_mov_b32 s43, 0x20000\ns_lshl2_add_u32 s2, s74, s11\ns_lshl_b32 s2, s2, 7\n"
"v_lshlrev_b32 v54, 10, v1\nbuffer_load_dword v31, v54, s[40:43], s2 offen\nv_and_b32 v48, 15, v0\nv_lshrrev_b32 v49, 4, v0\nv_lshrrev_b32 v50, 1, v48\nv_xor_b32 v50, v50, v49\n"
"v_lshlrev_b32 v50, 4, v50\nv_lshl_or_b32 v50, v48, 7, v50\ns_lshr_b32 s2, s11, 1\ns_lshl_b32 s2, s2, 12\nv_add_u32 v10, s2, v50\nv_xor_b32 v11, 64, v10\nv_lshrrev_b32 v50, 2, v48\n"
"v_lshl_add_u32 v51, v49, 3, v50\nv_and_b32 v52, 1, v49\nv_lshl_or_b32 v50, v52, 2, v50\nv_lshlrev_b32 v50, 5, v50\nv_lshl_or_b32 v50, v51, 9, v50\nv_and_b32 v52, 3, v0\n"
"v_lshl_or_b32 v50, v52, 3, v50\ns_and_b32 s2, s11, 1\ns_lshl_b32 s2, s2, 8\ns_add_u32 s2, s2, 0x10000\nv_add_u32 v12, s2, v50\nv_xor_b32 v13, 32, v12\nv_xor_b32 v14, 64, v12\n"
"v_xor_b32 v15, 96, v12\nv_xor_b32 v16, 128, v12\nv_xor_b32 v17, 160, v12\nv_xor_b32 v18, 192, v12\nv_xor_b32 v19, 224, v12\nWL 0\ns_mul_i32 s15, s15, 1664525\n"
"s_add_u32 s15, s15, 1013904223\nv_mov_b32 v25, s15\ns_mov_b32 s57, 0x3db8aa3b\ns_mov_b32 s90, 0x40000000\ns_and_b32 s59, s11, 1\ns_lshl_b32 s59, s59, 13\nv_lshrrev_b32 v24, 3, v0\n"
"v_lshlrev_b32 v24, 13, v24\nv_and_b32 v26, 7, v0\nv_lshl_or_b32 v24, v26, 7, v24\nv_lshrrev_b32 v26, 4, v0\nv_lshl_or_b32 v24, v26, 28, v24\nv_lshrrev_b32 v26, 5, v0\n"
"v_lshlrev_b32 v26, 30, v26\nv_lshl_or_b32 v26, v0, 7, v26\ns_and_b32 s2, s13, 6\ns_and_b32 s3, s12, 1\ns_add_u32 s2, s2, s3\ns_lshl2_add_u32 s2, s2, s11\ns_lshl_b32 s2, s2, 14\n"
"s_lshl_b32 s3, s73, 10\ns_add_u32 s2, s2, s3\ns_xor_b32 s3, s14, 1\ns_lshl_b32 s3, s3, 30\ns_add_u32 s2, s2, s3\ns_add_u32 s96, s2, 1048576\ns_and_b32 s2, s10, 127\n"
"s_lshl2_add_u32 s2, s2, s11\ns_lshl_b32 s97, s2, 12\ns_mul_i32 s2, s73, 25165824\ns_and_b32 s3, s12, 1\ns_lshl_b32 s3, s3, 21\ns_add_u32 s2, s2, s3\ns_lshl2_add_u32 s3, s13, s11\n"
"s_lshl_b32 s3, s3, 15\ns_add_u32 s2, s2, s3\ns_add_u32 s56, s2, 0x800000\ns_xor_b32 s53, s56, 0xc000\ns_and_b32 s2, s74, 3\ns_lshl2_add_u32 s2, s73, s2\ns_lshl_b32 s2, s2, 12\n"
"s_add_u32 s3, s45, 512\ns_and_b32 s3, s3, 0xfffe0000\ns_add_u32 s2, s2, s3\nbuffer_load_dword v31, v26, s[20:23], s2 offen\nbuffer_load_dword v31, v24, s[24:27], s96 offen\n"
"s_add_u32 s96, s96, 0x80000\nbuffer_load_dword v31, v24, s[24:27], s96 offen\ns_add_u32 s96, s96, 0x80000\nWV 24\ns_barrier\nTa 128,12,0\nTa 130,12,2048\nTa 132,13,0\n"
"Ta 134,13,2048\nTa 136,14,0\nTa 138,14,2048\nTa 140,15,0\nTa 142,15,2048\nTa 144,16,0\nTa 146,16,2048\nTa 148,17,0\nTa 150,17,2048\nTa 152,18,0\nTa 154,18,2048\nTa 156,19,0\n"
"Ta 158,19,2048\nRa 160,10,0\nRa 164,10,2048\nRa 168,10,8192\nRa 172,10,10240\nRa 176,10,16384\nRa 180,10,18432\nRa 184,10,24576\nRa 188,10,26624\ns_waitcnt vmcnt(20) lgkmcnt(0)\n"
"s_barrier\ns_bitcmp1_b32 s13, 0\ns_cbranch_scc1 .V15\nZvaa 64,128,160\nTa 192,12,16384\nZvaa 96,128,164\nZvaa 128,128,168\nTa 194,12,18432\nZvaa 160,128,172\n"
"s_add_u32 m0, s47, 49152\ns_add_u32 s44, s44, 0x40000\nZaaa 0,128,176\nTa 196,13,16384\nZaaa 32,128,180\nD0 6,24,44,0\nZaaa 64,128,184\nTa 198,13,18432\nZaaa 96,128,188\n"
"Zaaa 100,132,188\nTa 200,14,16384\nZaaa 68,132,184\nZaaa 36,132,180\nTa 202,14,18432\nZaaa 4,132,176\nZvaa 164,132,172\nTa 204,15,16384\nZvaa 132,132,168\nD0 7,24,44,1024\n"
"Zvaa 100,132,164\nTa 206,15,18432\nZvaa 68,132,160\nZvaa 72,136,160\nTa 208,16,16384\nZvaa 104,136,164\nZvaa 136,136,168\nTa 210,16,18432\nZvaa 168,136,172\nZaaa 8,136,176\n"
"Ta 212,17,16384\nZaaa 40,136,180\nD0 8,24,44,2048\nZaaa 72,136,184\nTa 214,17,18432\nZaaa 104,136,188\nZaaa 108,140,188\nTa 216,18,16384\nZaaa 76,140,184\nZaaa 44,140,180\n"
"Ta 218,18,18432\nZaaa 12,140,176\nZvaa 172,140,172\nTa 220,19,16384\nZvaa 140,140,168\nD0 9,24,44,3072\nZvaa 108,140,164\nTa 222,19,18432\nZvaa 76,140,160\n"
"buffer_load_dword v31, v24, s[24:27], s96 offen\ns_add_u32 s96, s96, 0x80000\nZvaa 80,144,160\nRa 224,11,0\nZvaa 112,144,164\nZvaa 144,144,168\nRa 228,11,2048\nZvaa 176,144,172\n"
"s_add_u32 m0, s47, 0\ns_add_u32 s44, s44, 0x40000\nZaaa 16,144,176\nRa 232,11,8192\nZaaa 48,144,180\nD0 6,24,44,0\nZaaa 80,144,184\nRa 236,11,10240\nZaaa 112,144,188\n"
"Zaaa 116,148,188\nRa 240,11,16384\nZaaa 84,148,184\nZaaa 52,148,180\nRa 244,11,18432\nZaaa 20,148,176\nZvaa 180,148,172\nRa 248,11,24576\nZvaa 148,148,168\nD0 7,24,44,1024\n"
"Zvaa 116,148,164\nRa 252,11,26624\nZvaa 84,148,160\nZvaa 88,152,160\nZvaa 120,152,164\nZvaa 152,152,168\nZvaa 184,152,172\nZaaa 24,152,176\nZaaa 56,152,180\nD0 8,24,44,2048\n"
"Zaaa 88,152,184\nZaaa 120,152,188\nZaaa 124,156,188\nZaaa 92,156,184\nZaaa 60,156,180\nZaaa 28,156,176\nZvaa 188,156,172\nZvaa 156,156,168\nD0 9,24,44,3072\nZvaa 124,156,164\n"
"Zvaa 92,156,160\ns_waitcnt vmcnt(13) lgkmcnt(0)\ns_barrier\nMvaa 64,192,224\nTa 128,12,32768\nMvaa 96,192,228\nv_lshlrev_b32 v48, 16, v32\nv_lshlrev_b32 v49, 16, v36\n"
"Mvaa 128,192,232\nTa 130,12,34816\nv_lshlrev_b32 v50, 16, v40\nMvaa 160,192,236\ns_add_u32 m0, s48, 0\ns_add_u32 s45, s45, 128\nMaaa 0,192,240\nTa 132,13,32768\n"
"v_lshlrev_b32 v51, 16, v44\nMaaa 32,192,244\nD0 2,20,45,0\nMU 52,48,50\nMaaa 64,192,248\nTa 134,13,34816\nMU 53,49,50\nMaaa 96,192,252\nCV 52,52,53\nv_lshlrev_b32 v54, 16, v52\n"
"Maaa 100,196,252\nTa 136,14,32768\nv_and_b32 v55, 0xffff0000, v52\nMaaa 68,196,248\nMU 56,48,51\nMU 57,49,51\nMaaa 36,196,244\nTa 138,14,34816\nv_and_b32 v48, 0xffff0000, v32\n"
"Maaa 4,196,240\nv_and_b32 v49, 0xffff0000, v36\nv_and_b32 v50, 0xffff0000, v40\nMvaa 164,196,236\nTa 140,15,32768\nv_and_b32 v51, 0xffff0000, v44\nMvaa 132,196,232\n"
"D0 3,20,45,1024\nMU 52,48,50\nMvaa 100,196,228\nTa 142,15,34816\nMU 53,49,50\nMvaa 68,196,224\nCV 52,52,53\nv_and_b32 v53, 0xffff0000, v52\nMvaa 72,200,224\nTa 144,16,32768\n"
"v_lshlrev_b32 v52, 16, v52\nMvaa 104,200,228\nFM 56,48,51\nFM 57,49,51\nMvaa 136,200,232\nTa 146,16,34816\nAD 54,54,52\nMvaa 168,200,236\nAD 55,55,53\nv_lshlrev_b32 v48, 16, v33\n"
"Maaa 8,200,240\nTa 148,17,32768\nv_lshlrev_b32 v49, 16, v37\nMaaa 40,200,244\nD0 4,20,45,2048\nv_lshlrev_b32 v50, 16, v41\nMaaa 72,200,248\nTa 150,17,34816\n"
"v_lshlrev_b32 v51, 16, v45\nMaaa 104,200,252\nMU 52,48,50\nMU 53,49,50\nMaaa 108,204,252\nTa 152,18,32768\nCV 52,52,53\nMaaa 76,204,248\nv_and_b32 v53, 0xffff0000, v52\n"
"v_lshlrev_b32 v52, 16, v52\nMaaa 44,204,244\nTa 154,18,34816\nFM 56,48,51\nMaaa 12,204,240\nFM 57,49,51\nAD 54,54,52\nMvaa 172,204,236\nTa 156,19,32768\nAD 55,55,53\n"
"Mvaa 140,204,232\nD0 5,20,45,3072\nv_and_b32 v48, 0xffff0000, v33\nMvaa 108,204,228\nTa 158,19,34816\nv_and_b32 v49, 0xffff0000, v37\nMvaa 76,204,224\n"
"v_and_b32 v50, 0xffff0000, v41\nv_and_b32 v51, 0xffff0000, v45\nMvaa 80,208,224\nRa 160,10,32768\nMU 52,48,50\nMvaa 112,208,228\nMU 53,49,50\nCV 52,52,53\nMvaa 144,208,232\n"
"Ra 164,10,34816\nv_and_b32 v53, 0xffff0000, v52\nMvaa 176,208,236\ns_add_u32 m0, s48, 4096\ns_add_u32 s46, s45, 0x10000\nMaaa 16,208,240\nRa 168,10,40960\n"
"v_lshlrev_b32 v52, 16, v52\nMaaa 48,208,244\nD0 2,20,46,0\nFM 56,48,51\nMaaa 80,208,248\nRa 172,10,43008\nFM 57,49,51\nMaaa 112,208,252\nAD 54,54,52\nAD 55,55,53\n"
"Maaa 116,212,252\nRa 176,10,49152\nv_lshlrev_b32 v48, 16, v34\nMaaa 84,212,248\nv_lshlrev_b32 v49, 16, v38\nv_lshlrev_b32 v50, 16, v42\nMaaa 52,212,244\nRa 180,10,51200\n"
"v_lshlrev_b32 v51, 16, v46\nMaaa 20,212,240\nMU 52,48,50\nMU 53,49,50\nMvaa 180,212,236\nRa 184,10,57344\nCV 52,52,53\nMvaa 148,212,232\nD0 3,20,46,1024\n"
"v_and_b32 v53, 0xffff0000, v52\nMvaa 116,212,228\nRa 188,10,59392\nv_lshlrev_b32 v52, 16, v52\nMvaa 84,212,224\nFM 56,48,51\nFM 57,49,51\nMvaa 88,216,224\nAD 54,54,52\n"
"AD 55,55,53\nMvaa 120,216,228\nv_and_b32 v48, 0xffff0000, v34\nv_and_b32 v49, 0xffff0000, v38\nMvaa 152,216,232\nv_and_b32 v50, 0xffff0000, v42\nv_and_b32 v51, 0xffff0000, v46\n"
"Mvaa 184,216,236\nMU 52,48,50\nMU 53,49,50\nMaaa 24,216,240\nCV 52,52,53\nv_and_b32 v53, 0xffff0000, v52\nMaaa 56,216,244\nD0 4,20,46,2048\nv_lshlrev_b32 v52, 16, v52\n"
"Maaa 88,216,248\nFM 56,48,51\nFM 57,49,51\nMaaa 120,216,252\nAD 54,54,52\nAD 55,55,53\nMaaa 124,220,252\nv_lshlrev_b32 v48, 16, v35\nv_lshlrev_b32 v49, 16, v39\nMaaa 92,220,248\n"
"v_lshlrev_b32 v50, 16, v43\nv_lshlrev_b32 v51, 16, v47\nMaaa 60,220,244\nMU 52,48,50\nMU 53,49,50\nMaaa 28,220,240\nCV 52,52,53\nv_and_b32 v53, 0xffff0000, v52\nMvaa 188,220,236\n"
"v_lshlrev_b32 v52, 16, v52\nFM 56,48,51\nMvaa 156,220,232\nD0 5,20,46,3072\nFM 57,49,51\nMvaa 124,220,228\nAD 54,54,52\nAD 55,55,53\nMvaa 92,220,224\n"
"s_waitcnt vmcnt(13) lgkmcnt(0)\ns_barrier\nMvaa 64,128,160\nTa 192,12,49152\nv_and_b32 v48, 0xffff0000, v35\nMvaa 96,128,164\nv_and_b32 v49, 0xffff0000, v39\n"
"v_and_b32 v50, 0xffff0000, v43\nMvaa 128,128,168\nTa 194,12,51200\nv_and_b32 v51, 0xffff0000, v47\nMvaa 160,128,172\ns_add_u32 m0, s47, 16384\ns_add_u32 s44, s44, 0x40000\n"
"Maaa 0,128,176\nTa 196,13,49152\nMU 52,48,50\nMaaa 32,128,180\nD0 6,24,44,0\nMU 53,49,50\nMaaa 64,128,184\nTa 198,13,51200\nCV 52,52,53\nMaaa 96,128,188\n"
"v_and_b32 v53, 0xffff0000, v52\nv_lshlrev_b32 v52, 16, v52\nMaaa 100,132,188\nTa 200,14,49152\nFM 56,48,51\nMaaa 68,132,184\nFM 57,49,51\nAD 54,54,52\nMaaa 36,132,180\n"
"Ta 202,14,51200\nAD 55,55,53\nMaaa 4,132,176\ns_nop 0\nv_permlane32_swap_b32 v54, v55\nv_permlane32_swap_b32 v56, v57\nMvaa 164,132,172\nTa 204,15,49152\nAD 54,54,55\n"
"Mvaa 132,132,168\nD0 7,24,44,1024\nAD 56,56,57\nMvaa 100,132,164\nTa 206,15,51200\nMvaa 68,132,160\nv_permlane16_swap_b32 v54, v56\nAD 54,54,56\nMvaa 72,136,160\nTa 208,16,49152\n"
"Mvaa 104,136,164\nv_add_f32_dpp v54, v54, v54 row_ror:8 row_mask:0xf bank_mask:0xf\nMvaa 136,136,168\nTa 210,16,51200\nMvaa 168,136,172\n"
"v_add_f32_dpp v54, v54, v54 row_ror:4 row_mask:0xf bank_mask:0xf\nMaaa 8,136,176\nTa 212,17,49152\nMaaa 40,136,180\nD0 8,24,44,2048\n"
"v_add_f32_dpp v54, v54, v54 row_ror:2 row_mask:0xf bank_mask:0xf\nMaaa 72,136,184\nTa 214,17,51200\nMaaa 104,136,188\n"
"v_add_f32_dpp v54, v54, v54 row_ror:1 row_mask:0xf bank_mask:0xf\nCV 54,54,54\nMaaa 108,140,188\nTa 216,18,49152\nv_lshlrev_b32 v54, 16, v54\nMaaa 76,140,184\n"
"v_mul_f32 v55, 0xbfb8aa3b, v54\nMaaa 44,140,180\nTa 218,18,51200\nMaaa 12,140,176\nEX 55,55\nMvaa 172,140,172\nTa 220,19,49152\nv_add_f32 v56, 0, v54\nMvaa 140,140,168\n"
"D0 9,24,44,3072\nv_add_f32 v55, 1.0, v55\nMvaa 108,140,164\nTa 222,19,51200\nMvaa 76,140,160\nbuffer_load_dword v31, v24, s[24:27], s96 offen\ns_add_u32 s96, s96, 0x80000\n"
"Mvaa 80,144,160\nRa 224,11,32768\nMvaa 112,144,164\nv_rcp_f32 v55, v55\nMvaa 144,144,168\nRa 228,11,34816\nv_ashrrev_i32 v57, 31, v56\nMvaa 176,144,172\ns_add_u32 m0, s47, 32768\n"
"s_add_u32 s44, s44, 0x40000\nMaaa 16,144,176\nRa 232,11,40960\nCV 55,55,55\nMaaa 48,144,180\nD0 6,24,44,0\nv_lshlrev_b32 v55, 16, v55\nMaaa 80,144,184\nRa 236,11,43008\n"
"v_or_b32 v57, 0x80000000, v57\nMaaa 112,144,188\nv_xor_b32 v56, v56, v57\nv_and_b32 v56, 0xffff0000, v56\nMaaa 116,148,188\nRa 240,11,49152\ns_lshl2_add_u32 s2, s10, s11\n"
"Maaa 84,148,184\nv_lshrrev_b32 v48, 5, v0\nv_lshl_add_u32 v48, s2, 1, v48\nMaaa 52,148,180\nRa 244,11,51200\nv_sub_u32 v49, 0xffff, v48\nMaaa 20,148,176\nv_or_b32 v56, v56, v49\n"
"v_mov_b32 v57, s15\nMvaa 180,148,172\nRa 248,11,57344\nv_and_b32 v49, 31, v0\nMvaa 148,148,168\nD0 7,24,44,1024\nv_mov_b32 v52, 0x40000000\nMvaa 116,148,164\nRa 252,11,59392\n"
"v_cmp_eq_u32 vcc, 16, v49\nv_lshlrev_b32 v50, 3, v48\nv_lshlrev_b32 v51, 9, v49\nv_cndmask_b32 v50, v52, v50, vcc\nv_cndmask_b32 v55, v55, v54, vcc\nMvaa 84,148,160\n"
"v_lshl_add_u32 v51, v48, 2, v51\nv_and_b32 v49, 15, v0\nMvaa 88,152,160\nv_cmp_eq_u32 vcc, 0, v49\ns_mov_b32 s2, 0xc040000\ns_mov_b32 s3, 0xc044000\n"
"v_cndmask_b32 v51, v52, v51, vcc\nbuffer_store_dwordx2 v[56:57], v50, s[16:19], s3 offen sc0 sc1\nbuffer_store_dword v55, v51, s[16:19], s2 offen sc0 sc1\nMvaa 120,152,164\n"
"Mvaa 152,152,168\nMvaa 184,152,172\nMaaa 24,152,176\nMaaa 56,152,180\nD0 8,24,44,2048\nMaaa 88,152,184\nMaaa 120,152,188\nMaaa 124,156,188\nMaaa 92,156,184\nMaaa 60,156,180\n"
"Maaa 28,156,176\nMvaa 188,156,172\nMvaa 156,156,168\nD0 9,24,44,3072\nMvaa 124,156,164\nMvaa 92,156,160\ns_waitcnt vmcnt(11) lgkmcnt(0)\ns_barrier\nMvaa 64,192,224\nTa 128,12,0\n"
"Mvaa 96,192,228\nMvaa 128,192,232\nTa 130,12,2048\nMvaa 160,192,236\ns_add_u32 m0, s48, 32768\ns_add_u32 s45, s45, 128\nMaaa 0,192,240\nTa 132,13,0\nMaaa 32,192,244\nD0 2,20,45,0\n"
"Maaa 64,192,248\nTa 134,13,2048\nMaaa 96,192,252\nMaaa 100,196,252\nTa 136,14,0\nMaaa 68,196,248\nMaaa 36,196,244\nTa 138,14,2048\nMaaa 4,196,240\nMvaa 164,196,236\nTa 140,15,0\n"
"Mvaa 132,196,232\nD0 3,20,45,1024\nMvaa 100,196,228\nTa 142,15,2048\nMvaa 68,196,224\nMvaa 72,200,224\nTa 144,16,0\nMvaa 104,200,228\nMvaa 136,200,232\nTa 146,16,2048\n"
"Mvaa 168,200,236\nMaaa 8,200,240\nTa 148,17,0\nMaaa 40,200,244\nD0 4,20,45,2048\nMaaa 72,200,248\nTa 150,17,2048\nMaaa 104,200,252\nMaaa 108,204,252\nTa 152,18,0\nMaaa 76,204,248\n"
"Maaa 44,204,244\nTa 154,18,2048\nMaaa 12,204,240\nMvaa 172,204,236\nTa 156,19,0\nMvaa 140,204,232\nD0 5,20,45,3072\nMvaa 108,204,228\nTa 158,19,2048\nMvaa 76,204,224\n"
"Mvaa 80,208,224\nRa 160,10,0\nMvaa 112,208,228\nMvaa 144,208,232\nRa 164,10,2048\nMvaa 176,208,236\ns_add_u32 m0, s48, 36864\ns_add_u32 s46, s45, 0x10000\nMaaa 16,208,240\n"
"Ra 168,10,8192\nMaaa 48,208,244\nD0 2,20,46,0\nMaaa 80,208,248\nRa 172,10,10240\nMaaa 112,208,252\nMaaa 116,212,252\nRa 176,10,16384\nMaaa 84,212,248\nMaaa 52,212,244\n"
"Ra 180,10,18432\nMaaa 20,212,240\nMvaa 180,212,236\nRa 184,10,24576\nMvaa 148,212,232\nD0 3,20,46,1024\nMvaa 116,212,228\nRa 188,10,26624\nMvaa 84,212,224\nMvaa 88,216,224\n"
"Mvaa 120,216,228\nMvaa 152,216,232\nMvaa 184,216,236\nMaaa 24,216,240\nMaaa 56,216,244\nD0 4,20,46,2048\nMaaa 88,216,248\nMaaa 120,216,252\nMaaa 124,220,252\nMaaa 92,220,248\n"
"Maaa 60,220,244\nMaaa 28,220,240\nMvaa 188,220,236\nMvaa 156,220,232\nD0 5,20,46,3072\nMvaa 124,220,228\nMvaa 92,220,224\ns_waitcnt vmcnt(15) lgkmcnt(0)\ns_barrier\ns_branch .MK2\n"
".V15:\nZvaa 64,160,128\nTa 192,12,16384\nZvaa 68,164,128\ns_and_b32 s86, s13, 6\ns_and_b32 s87, s12, 1\nZvaa 128,168,128\nTa 194,12,18432\ns_add_u32 s86, s86, s87\n"
"Zvaa 132,172,128\ns_add_u32 m0, s47, 49152\ns_add_u32 s44, s44, 0x40000\nZaaa 0,176,128\nTa 196,13,16384\ns_lshl2_add_u32 s86, s86, s11\nZaaa 4,180,128\nD0 6,24,44,0\n"
"s_lshl_b32 s93, s73, 7\nZaaa 64,184,128\nTa 198,13,18432\ns_lshl2_add_u32 s93, s86, s93\nZaaa 68,188,128\ns_add_u32 s93, s93, 0xc04ac00\ns_lshl_b32 s86, s86, 14\nZaaa 76,188,132\n"
"Ta 200,14,16384\ns_mul_i32 s87, s73, 25165824\nZaaa 72,184,132\ns_add_u32 s86, s86, s87\ns_add_u32 s94, s86, 0x1500000\nZaaa 12,180,132\nTa 202,14,18432\n"
"s_add_u32 s95, s94, 0x2000\nZaaa 8,176,132\ns_mov_b32 s92, 1024\nv_lshrrev_b32 v60, 3, v0\nZvaa 140,172,132\nTa 204,15,16384\nv_and_b32 v61, 0x70, v1\nZvaa 136,168,132\n"
"D0 7,24,44,1024\nv_lshl_or_b32 v60, v60, 10, v61\nZvaa 76,164,132\nTa 206,15,18432\nZvaa 72,160,132\nZvaa 80,160,136\nTa 208,16,16384\nZvaa 84,164,136\nZvaa 144,168,136\n"
"Ta 210,16,18432\nZvaa 148,172,136\nZaaa 16,176,136\nTa 212,17,16384\nZaaa 20,180,136\nD0 8,24,44,2048\nZaaa 80,184,136\nTa 214,17,18432\nZaaa 84,188,136\nZaaa 92,188,140\n"
"Ta 216,18,16384\nZaaa 88,184,140\nZaaa 28,180,140\nTa 218,18,18432\nZaaa 24,176,140\nZvaa 156,172,140\nTa 220,19,16384\nZvaa 152,168,140\nD0 9,24,44,3072\nZvaa 92,164,140\n"
"Ta 222,19,18432\nZvaa 88,160,140\nbuffer_load_dword v31, v24, s[24:27], s96 offen\ns_add_u32 s96, s96, 0x80000\nZvaa 96,160,144\nRa 224,11,0\nZvaa 100,164,144\nSv 2,60,16,94,0\n"
"Zvaa 160,168,144\nRa 228,11,2048\nZvaa 164,172,144\ns_add_u32 m0, s47, 0\ns_add_u32 s44, s44, 0x40000\nZaaa 32,176,144\nRa 232,11,8192\nZaaa 36,180,144\nD0 6,24,44,0\n"
"Zaaa 96,184,144\nRa 236,11,10240\nZaaa 100,188,144\nSv 2,60,16,95,0\ns_add_u32 s86, s94, 512\nZaaa 108,188,148\nRa 240,11,16384\ns_load_dword s87, s[54:55], s86\nZaaa 104,184,148\n"
"Zaaa 44,180,148\nRa 244,11,18432\nZaaa 40,176,148\nZvaa 172,172,148\nRa 248,11,24576\nZvaa 168,168,148\nD0 7,24,44,1024\nZvaa 108,164,148\nRa 252,11,26624\nZvaa 104,160,148\n"
"Zvaa 112,160,152\nZvaa 116,164,152\nZvaa 176,168,152\nZvaa 180,172,152\nZaaa 48,176,152\nZaaa 52,180,152\nD0 8,24,44,2048\nZaaa 112,184,152\nZaaa 116,188,152\nZaaa 124,188,156\n"
"Zaaa 120,184,156\nZaaa 60,180,156\nZaaa 56,176,156\nZvaa 188,172,156\nZvaa 184,168,156\nD0 9,24,44,3072\nZvaa 124,164,156\nZvaa 120,160,156\ns_waitcnt vmcnt(15) lgkmcnt(0)\n"
"s_barrier\nMvaa 64,224,192\nTa 128,12,32768\nMvaa 68,228,192\nv_lshlrev_b32 v48, 16, v32\nv_lshlrev_b32 v49, 16, v36\nMvaa 128,232,192\nTa 130,12,34816\n"
"v_lshlrev_b32 v50, 16, v40\nMvaa 132,236,192\ns_add_u32 m0, s48, 0\ns_add_u32 s45, s45, 128\nMaaa 0,240,192\nTa 132,13,32768\nv_lshlrev_b32 v51, 16, v44\nMaaa 4,244,192\n"
"D0 2,20,45,0\nMU 52,48,50\nMaaa 64,248,192\nTa 134,13,34816\nMU 53,49,50\nMaaa 68,252,192\nCV 52,52,53\nv_lshlrev_b32 v54, 16, v52\nMaaa 76,252,196\nTa 136,14,32768\n"
"v_and_b32 v55, 0xffff0000, v52\nMaaa 72,248,196\nMU 56,48,51\nMU 57,49,51\nMaaa 12,244,196\nTa 138,14,34816\nv_and_b32 v48, 0xffff0000, v32\nMaaa 8,240,196\n"
"v_and_b32 v49, 0xffff0000, v36\nv_and_b32 v50, 0xffff0000, v40\nMvaa 140,236,196\nTa 140,15,32768\nv_and_b32 v51, 0xffff0000, v44\nMvaa 136,232,196\nD0 3,20,45,1024\nMU 52,48,50\n"
"Mvaa 76,228,196\nTa 142,15,34816\nMU 53,49,50\nMvaa 72,224,196\nCV 52,52,53\nv_and_b32 v53, 0xffff0000, v52\nMvaa 80,224,200\nTa 144,16,32768\nv_lshlrev_b32 v52, 16, v52\n"
"Mvaa 84,228,200\nFM 56,48,51\nFM 57,49,51\nMvaa 144,232,200\nTa 146,16,34816\nAD 54,54,52\nMvaa 148,236,200\nAD 55,55,53\nv_lshlrev_b32 v48, 16, v33\nMaaa 16,240,200\n"
"Ta 148,17,32768\nv_lshlrev_b32 v49, 16, v37\nMaaa 20,244,200\nD0 4,20,45,2048\nv_lshlrev_b32 v50, 16, v41\nMaaa 80,248,200\nTa 150,17,34816\nv_lshlrev_b32 v51, 16, v45\n"
"Maaa 84,252,200\nMU 52,48,50\nMU 53,49,50\nMaaa 92,252,204\nTa 152,18,32768\nCV 52,52,53\nMaaa 88,248,204\nv_and_b32 v53, 0xffff0000, v52\nv_lshlrev_b32 v52, 16, v52\n"
"Maaa 28,244,204\nTa 154,18,34816\nFM 56,48,51\nMaaa 24,240,204\nFM 57,49,51\nAD 54,54,52\nMvaa 156,236,204\nTa 156,19,32768\nAD 55,55,53\nMvaa 152,232,204\nD0 5,20,45,3072\n"
"v_and_b32 v48, 0xffff0000, v33\nMvaa 92,228,204\nTa 158,19,34816\nv_and_b32 v49, 0xffff0000, v37\nMvaa 88,224,204\nv_and_b32 v50, 0xffff0000, v41\nv_and_b32 v51, 0xffff0000, v45\n"
"Mvaa 96,224,208\nRa 160,10,32768\nMU 52,48,50\nMvaa 100,228,208\nMU 53,49,50\nCV 52,52,53\nMvaa 160,232,208\nRa 164,10,34816\nv_and_b32 v53, 0xffff0000, v52\nMvaa 164,236,208\n"
"s_add_u32 m0, s48, 4096\ns_add_u32 s46, s45, 0x10000\nMaaa 32,240,208\nRa 168,10,40960\nv_lshlrev_b32 v52, 16, v52\nMaaa 36,244,208\nD0 2,20,46,0\nFM 56,48,51\nMaaa 96,248,208\n"
"Ra 172,10,43008\nFM 57,49,51\nMaaa 100,252,208\nAD 54,54,52\nAD 55,55,53\nMaaa 108,252,212\nRa 176,10,49152\nv_lshlrev_b32 v48, 16, v34\nMaaa 104,248,212\n"
"v_lshlrev_b32 v49, 16, v38\nv_lshlrev_b32 v50, 16, v42\nMaaa 44,244,212\nRa 180,10,51200\nv_lshlrev_b32 v51, 16, v46\nMaaa 40,240,212\nMU 52,48,50\nMU 53,49,50\nMvaa 172,236,212\n"
"Ra 184,10,57344\nCV 52,52,53\nMvaa 168,232,212\nD0 3,20,46,1024\nv_and_b32 v53, 0xffff0000, v52\nMvaa 108,228,212\nRa 188,10,59392\nv_lshlrev_b32 v52, 16, v52\nMvaa 104,224,212\n"
"FM 56,48,51\nFM 57,49,51\nMvaa 112,224,216\nAD 54,54,52\nAD 55,55,53\nMvaa 116,228,216\nv_and_b32 v48, 0xffff0000, v34\nv_and_b32 v49, 0xffff0000, v38\nMvaa 176,232,216\n"
"v_and_b32 v50, 0xffff0000, v42\nv_and_b32 v51, 0xffff0000, v46\nMvaa 180,236,216\nMU 52,48,50\nMU 53,49,50\nMaaa 48,240,216\nCV 52,52,53\nv_and_b32 v53, 0xffff0000, v52\n"
"Maaa 52,244,216\nD0 4,20,46,2048\nv_lshlrev_b32 v52, 16, v52\nMaaa 112,248,216\nFM 56,48,51\nFM 57,49,51\nMaaa 116,252,216\nAD 54,54,52\nAD 55,55,53\nMaaa 124,252,220\n"
"v_lshlrev_b32 v48, 16, v35\nv_lshlrev_b32 v49, 16, v39\nMaaa 120,248,220\nv_lshlrev_b32 v50, 16, v43\nv_lshlrev_b32 v51, 16, v47\nMaaa 60,244,220\nMU 52,48,50\nMU 53,49,50\n"
"Maaa 56,240,220\nCV 52,52,53\nv_and_b32 v53, 0xffff0000, v52\nMvaa 188,236,220\nv_lshlrev_b32 v52, 16, v52\nFM 56,48,51\nMvaa 184,232,220\nD0 5,20,46,3072\nFM 57,49,51\n"
"Mvaa 124,228,220\nAD 54,54,52\nAD 55,55,53\nMvaa 120,224,220\ns_waitcnt vmcnt(15) lgkmcnt(0)\ns_barrier\nMvaa 64,160,128\nTa 192,12,49152\nv_and_b32 v48, 0xffff0000, v35\n"
"Mvaa 68,164,128\nv_and_b32 v49, 0xffff0000, v39\nv_and_b32 v50, 0xffff0000, v43\nMvaa 128,168,128\nTa 194,12,51200\nv_and_b32 v51, 0xffff0000, v47\nMvaa 132,172,128\n"
"s_add_u32 m0, s47, 16384\ns_add_u32 s44, s44, 0x40000\nMaaa 0,176,128\nTa 196,13,49152\nMU 52,48,50\nMaaa 4,180,128\nD0 6,24,44,0\nMU 53,49,50\nMaaa 64,184,128\nTa 198,13,51200\n"
"CV 52,52,53\nMaaa 68,188,128\nv_and_b32 v53, 0xffff0000, v52\nv_lshlrev_b32 v52, 16, v52\nMaaa 76,188,132\nTa 200,14,49152\nFM 56,48,51\nMaaa 72,184,132\nFM 57,49,51\nAD 54,54,52\n"
"Maaa 12,180,132\nTa 202,14,51200\nAD 55,55,53\nMaaa 8,176,132\ns_nop 0\nv_permlane32_swap_b32 v54, v55\nv_permlane32_swap_b32 v56, v57\nMvaa 140,172,132\nTa 204,15,49152\n"
"AD 54,54,55\nMvaa 136,168,132\nD0 7,24,44,1024\nAD 56,56,57\nMvaa 76,164,132\nTa 206,15,51200\nMvaa 72,160,132\nv_permlane16_swap_b32 v54, v56\nAD 54,54,56\nMvaa 80,160,136\n"
"Ta 208,16,49152\nMvaa 84,164,136\nv_add_f32_dpp v54, v54, v54 row_ror:8 row_mask:0xf bank_mask:0xf\nMvaa 144,168,136\nTa 210,16,51200\nMvaa 148,172,136\n"
"v_add_f32_dpp v54, v54, v54 row_ror:4 row_mask:0xf bank_mask:0xf\nMaaa 16,176,136\nTa 212,17,49152\nMaaa 20,180,136\nD0 8,24,44,2048\n"
"v_add_f32_dpp v54, v54, v54 row_ror:2 row_mask:0xf bank_mask:0xf\nMaaa 80,184,136\nTa 214,17,51200\nMaaa 84,188,136\n"
"v_add_f32_dpp v54, v54, v54 row_ror:1 row_mask:0xf bank_mask:0xf\nCV 54,54,54\nMaaa 92,188,140\nTa 216,18,49152\nv_lshlrev_b32 v54, 16, v54\nMaaa 88,184,140\n"
"v_mul_f32 v55, 0xbfb8aa3b, v54\nMaaa 28,180,140\nTa 218,18,51200\nMaaa 24,176,140\nEX 55,55\nMvaa 156,172,140\nTa 220,19,49152\nv_add_f32 v56, 0, v54\nMvaa 152,168,140\n"
"D0 9,24,44,3072\nv_add_f32 v55, 1.0, v55\nMvaa 92,164,140\nTa 222,19,51200\nMvaa 88,160,140\nbuffer_load_dword v31, v24, s[24:27], s96 offen\ns_add_u32 s96, s96, 0x80000\n"
"Mvaa 96,160,144\nRa 224,11,32768\nMvaa 100,164,144\nv_rcp_f32 v55, v55\nMvaa 160,168,144\nRa 228,11,34816\nv_ashrrev_i32 v57, 31, v56\nMvaa 164,172,144\ns_add_u32 m0, s47, 32768\n"
"s_add_u32 s44, s44, 0x40000\nMaaa 32,176,144\nRa 232,11,40960\nCV 55,55,55\nMaaa 36,180,144\nD0 6,24,44,0\nv_lshlrev_b32 v55, 16, v55\nMaaa 96,184,144\nRa 236,11,43008\n"
"v_or_b32 v57, 0x80000000, v57\nMaaa 100,188,144\nv_xor_b32 v56, v56, v57\nv_and_b32 v56, 0xffff0000, v56\nMaaa 108,188,148\nRa 240,11,49152\ns_lshl2_add_u32 s2, s10, s11\n"
"Maaa 104,184,148\nv_lshrrev_b32 v48, 5, v0\nv_lshl_add_u32 v48, s2, 1, v48\nMaaa 44,180,148\nRa 244,11,51200\nv_sub_u32 v49, 0xffff, v48\nMaaa 40,176,148\nv_or_b32 v56, v56, v49\n"
"v_mov_b32 v57, s15\nMvaa 172,172,148\nRa 248,11,57344\nv_and_b32 v49, 31, v0\nMvaa 168,168,148\nD0 7,24,44,1024\nv_mov_b32 v52, 0x40000000\nMvaa 108,164,148\nRa 252,11,59392\n"
"v_cmp_eq_u32 vcc, 16, v49\nv_lshlrev_b32 v50, 3, v48\nv_lshlrev_b32 v51, 9, v49\nv_cndmask_b32 v50, v52, v50, vcc\nv_cndmask_b32 v55, v55, v54, vcc\nMvaa 104,160,148\n"
"v_lshl_add_u32 v51, v48, 2, v51\nv_and_b32 v49, 15, v0\nMvaa 112,160,152\nv_cmp_eq_u32 vcc, 0, v49\ns_mov_b32 s2, 0xc040000\ns_mov_b32 s3, 0xc044000\n"
"v_cndmask_b32 v51, v52, v51, vcc\nbuffer_store_dwordx2 v[56:57], v50, s[16:19], s3 offen sc0 sc1\nbuffer_store_dword v55, v51, s[16:19], s2 offen sc0 sc1\nMvaa 116,164,152\n"
"Mvaa 176,168,152\nMvaa 180,172,152\nMaaa 48,176,152\nMaaa 52,180,152\nD0 8,24,44,2048\nMaaa 112,184,152\nMaaa 116,188,152\nMaaa 124,188,156\nMaaa 120,184,156\nMaaa 60,180,156\n"
"Maaa 56,176,156\nMvaa 188,172,156\nMvaa 184,168,156\nD0 9,24,44,3072\nMvaa 124,164,156\nMvaa 120,160,156\ns_waitcnt vmcnt(11) lgkmcnt(0)\ns_barrier\nMvaa 64,224,192\nTa 128,12,0\n"
"Mvaa 68,228,192\nMvaa 128,232,192\nTa 130,12,2048\nMvaa 132,236,192\ns_add_u32 m0, s48, 32768\ns_add_u32 s45, s45, 128\nMaaa 0,240,192\nTa 132,13,0\nMaaa 4,244,192\nD0 2,20,45,0\n"
"Maaa 64,248,192\nTa 134,13,2048\nMaaa 68,252,192\nMaaa 76,252,196\nTa 136,14,0\nMaaa 72,248,196\nMaaa 12,244,196\nTa 138,14,2048\nMaaa 8,240,196\nMvaa 140,236,196\nTa 140,15,0\n"
"Mvaa 136,232,196\nD0 3,20,45,1024\nMvaa 76,228,196\nTa 142,15,2048\nMvaa 72,224,196\nMvaa 80,224,200\nTa 144,16,0\nMvaa 84,228,200\nMvaa 144,232,200\nTa 146,16,2048\n"
"Mvaa 148,236,200\nMaaa 16,240,200\nTa 148,17,0\nMaaa 20,244,200\nD0 4,20,45,2048\nMaaa 80,248,200\nTa 150,17,2048\nMaaa 84,252,200\nMaaa 92,252,204\nTa 152,18,0\nMaaa 88,248,204\n"
"Maaa 28,244,204\nTa 154,18,2048\nMaaa 24,240,204\nMvaa 156,236,204\nTa 156,19,0\nMvaa 152,232,204\nD0 5,20,45,3072\nMvaa 92,228,204\nTa 158,19,2048\nMvaa 88,224,204\n"
"Mvaa 96,224,208\nRa 160,10,0\nMvaa 100,228,208\nMvaa 160,232,208\nRa 164,10,2048\nMvaa 164,236,208\ns_add_u32 m0, s48, 36864\ns_add_u32 s46, s45, 0x10000\nMaaa 32,240,208\n"
"Ra 168,10,8192\nMaaa 36,244,208\nD0 2,20,46,0\nMaaa 96,248,208\nRa 172,10,10240\nMaaa 100,252,208\nMaaa 108,252,212\nRa 176,10,16384\nMaaa 104,248,212\nMaaa 44,244,212\n"
"Ra 180,10,18432\nMaaa 40,240,212\nMvaa 172,236,212\nRa 184,10,24576\nMvaa 168,232,212\nD0 3,20,46,1024\nMvaa 108,228,212\nRa 188,10,26624\nMvaa 104,224,212\nMvaa 112,224,216\n"
"Mvaa 116,228,216\nMvaa 176,232,216\nMvaa 180,236,216\nMaaa 48,240,216\nMaaa 52,244,216\nD0 4,20,46,2048\nMaaa 112,248,216\nMaaa 116,252,216\nMaaa 124,252,220\nMaaa 120,248,220\n"
"Maaa 60,244,220\nMaaa 56,240,220\nMvaa 188,236,220\nMvaa 184,232,220\nD0 5,20,46,3072\nMvaa 124,228,220\nMvaa 120,224,220\ns_waitcnt vmcnt(15) lgkmcnt(0)\ns_barrier\n"
"s_branch .MV3\n.R01:\ns_add_u32 m0, s47, 65536\ns_add_u32 s46, s51, 0\nD0 6,32,46,0\nD0 7,32,46,1024\nD0 8,32,46,2048\nD0 9,32,46,3072\nv_lshrrev_b32 v144, 4, v0\n"
"v_and_b32 v145, 7, v0\nv_xor_b32 v145, v145, v144\nv_lshlrev_b32 v145, 4, v145\nv_lshrrev_b32 v144, 3, v0\nv_mul_u32_u24 v144, 896, v144\nv_add_u32 v136, v144, v145\n"
"v_xor_b32 v145, 64, v136\nv_add_u32 v137, 6144, v145\nv_add_u32 v138, 12288, v136\nv_add_u32 v139, 18432, v145\ns_sub_u32 s2, 15, s13\ns_lshl2_add_u32 s2, s2, s11\n"
"s_mul_i32 s52, s2, 28672\ns_add_u32 m0, s47, -65536\ns_add_u32 s46, s52, 0\nD0 136,28,46,0\nD0 137,28,46,1024\nD0 138,28,46,2048\nD0 139,28,46,3072\ns_add_u32 m0, s47, 81920\n"
"s_add_u32 s46, s51, 262144\nD0 6,32,46,0\nD0 7,32,46,1024\nD0 8,32,46,2048\nD0 9,32,46,3072\ns_add_u32 m0, s47, 0\ns_add_u32 s46, s51, 524288\nD0 6,32,46,0\nD0 7,32,46,1024\n"
"D0 8,32,46,2048\nD0 9,32,46,3072\ns_add_u32 m0, s47, -49152\ns_add_u32 s46, s52, 128\nD0 136,28,46,0\nD0 137,28,46,1024\nD0 138,28,46,2048\nD0 139,28,46,3072\n"
"s_add_u32 m0, s47, 16384\ns_add_u32 s46, s51, 786432\nD0 6,32,46,0\nD0 7,32,46,1024\nD0 8,32,46,2048\nD0 9,32,46,3072\nv_mov_b32 v116, 0x40000000\nv_cmp_gt_u32 vcc, 56, v0\n"
"s_lshl2_add_u32 s2, s10, s11\ns_mul_i32 s2, s2, 1792\nv_cndmask_b32 v117, v116, v1, vcc\ns_mov_b32 s36, s56\ns_and_b32 s37, s57, 0xffff\ns_mov_b32 s38, 0x380\n"
"s_mov_b32 s39, 0x20000\ns_mov_b32 s40, s58\ns_and_b32 s41, s59, 0xffff\ns_mov_b32 s42, 0x380\ns_mov_b32 s43, 0x20000\n"
"buffer_load_dwordx4 v[96:99], v117, s[28:31], s2 offen offset:0\nbuffer_load_dwordx4 v[100:103], v117, s[28:31], s2 offen offset:896\n"
"buffer_load_dwordx4 v[104:107], v117, s[36:39], 0 offen offset:0\nbuffer_load_dwordx4 v[108:111], v117, s[40:43], 0 offen offset:0\ns_getpc_b64 s[40:41]\n.PC16:\n"
"s_sub_u32 s40, s40, .PC16-.Lcode0\ns_subb_u32 s41, s41, 0\ns_mov_b32 s42, .Lcode1-.Lcode0\ns_mov_b32 s43, 0x20000\ns_lshl2_add_u32 s2, s74, s11\ns_lshl_b32 s2, s2, 7\n"
"v_lshlrev_b32 v54, 10, v1\nbuffer_load_dword v31, v54, s[40:43], s2 offen\nv_and_b32 v48, 15, v0\nv_lshrrev_b32 v49, 4, v0\nv_lshrrev_b32 v50, 1, v48\nv_xor_b32 v50, v50, v49\n"
"v_lshlrev_b32 v50, 4, v50\nv_lshl_or_b32 v50, v48, 7, v50\ns_lshr_b32 s2, s11, 1\ns_lshl_b32 s2, s2, 12\nv_add_u32 v10, s2, v50\nv_xor_b32 v11, 64, v10\nv_lshrrev_b32 v50, 2, v48\n"
"v_lshl_add_u32 v51, v49, 3, v50\nv_and_b32 v52, 1, v49\nv_lshl_or_b32 v50, v52, 2, v50\nv_lshlrev_b32 v50, 5, v50\nv_lshl_or_b32 v50, v51, 9, v50\nv_and_b32 v52, 3, v0\n"
"v_lshl_or_b32 v50, v52, 3, v50\ns_and_b32 s2, s11, 1\ns_lshl_b32 s2, s2, 8\ns_add_u32 s2, s2, 0x10000\nv_add_u32 v12, s2, v50\nv_xor_b32 v13, 32, v12\nv_xor_b32 v14, 64, v12\n"
"v_xor_b32 v15, 96, v12\nv_xor_b32 v16, 128, v12\nv_xor_b32 v17, 160, v12\nv_xor_b32 v18, 192, v12\nv_xor_b32 v19, 224, v12\nv_add_u32 v128, 0x10000, v12\n"
"v_add_u32 v129, 0x10000, v13\nv_add_u32 v130, 0x10000, v14\nv_add_u32 v131, 0x10000, v15\nv_add_u32 v132, 0x10000, v16\nv_add_u32 v133, 0x10000, v17\nv_add_u32 v134, 0x10000, v18\n"
"v_add_u32 v135, 0x10000, v19\ns_add_u32 s2, s11, 1\ns_lshr_b32 s2, s2, 1\ns_lshl_b32 s2, s2, 12\nv_add_u32 v140, s2, v10\nv_add_u32 v141, s2, v11\nv_xor_b32 v142, 0x1000, v140\n"
"v_xor_b32 v143, 0x1000, v141\ns_add_u32 m0, s47, 32768\ns_add_u32 s46, s51, 1048576\nD0 6,32,46,0\nD0 7,32,46,1024\nD0 8,32,46,2048\nD0 9,32,46,3072\ns_add_u32 m0, s47, -32768\n"
"s_add_u32 s46, s52, 256\nD0 136,28,46,0\nD0 137,28,46,1024\nD0 138,28,46,2048\nD0 139,28,46,3072\ns_add_u32 m0, s47, 49152\ns_add_u32 s46, s51, 1310720\nD0 6,32,46,0\n"
"D0 7,32,46,1024\nD0 8,32,46,2048\nD0 9,32,46,3072\nv_lshrrev_b32 v32, 4, v0\nv_and_b32 v33, 7, v0\nv_xor_b32 v33, v33, v32\nv_lshlrev_b32 v33, 4, v33\nv_lshrrev_b32 v32, 3, v0\n"
"v_lshl_or_b32 v2, v32, 11, v33\nv_xor_b32 v33, 64, v2\nv_add_u32 v3, 15360, v33\nv_add_u32 v4, 30720, v2\nv_add_u32 v5, 46080, v33\ns_lshr_b32 s2, s13, 2\n"
"s_bfe_u32 s3, s13, 0x10001\ns_mul_i32 s3, s3, 17\ns_add_u32 s2, s2, s3\ns_lshl2_add_u32 s3, s14, s11\ns_lshl_b32 s3, s3, 2\ns_lshr_b32 s3, 0xe420ca86, s3\ns_and_b32 s3, s3, 15\n"
"s_add_u32 s2, s2, s3\ns_cmp_eq_u32 s2, 32\ns_cselect_b32 s4, 16, s2\ns_lshl_b32 s45, s4, 17\ns_sub_u32 s45, s45, 128\nWL 0\ns_mul_i32 s15, s15, 1664525\n"
"s_add_u32 s15, s15, 1013904223\nv_mov_b32 v25, s15\ns_mov_b32 s57, 0x3db8aa3b\ns_mov_b32 s90, 0x40000000\ns_and_b32 s59, s11, 1\ns_lshl_b32 s59, s59, 13\nv_lshrrev_b32 v24, 3, v0\n"
"v_lshlrev_b32 v24, 13, v24\nv_and_b32 v26, 7, v0\nv_lshl_or_b32 v24, v26, 7, v24\nv_lshrrev_b32 v26, 4, v0\nv_lshl_or_b32 v24, v26, 28, v24\nv_lshrrev_b32 v26, 5, v0\n"
"v_lshlrev_b32 v26, 30, v26\nv_lshl_or_b32 v26, v0, 7, v26\ns_and_b32 s2, s13, 6\ns_and_b32 s3, s12, 1\ns_add_u32 s2, s2, s3\ns_lshl2_add_u32 s2, s2, s11\ns_lshl_b32 s2, s2, 14\n"
"s_lshl_b32 s3, s73, 10\ns_add_u32 s2, s2, s3\ns_xor_b32 s3, s14, 1\ns_lshl_b32 s3, s3, 30\ns_add_u32 s2, s2, s3\ns_add_u32 s96, s2, 1048576\ns_and_b32 s2, s10, 127\n"
"s_lshl2_add_u32 s2, s2, s11\ns_lshl_b32 s97, s2, 12\ns_mul_i32 s2, s73, 25165824\ns_and_b32 s3, s12, 1\ns_lshl_b32 s3, s3, 21\ns_add_u32 s2, s2, s3\ns_lshl2_add_u32 s3, s13, s11\n"
"s_lshl_b32 s3, s3, 15\ns_add_u32 s2, s2, s3\ns_add_u32 s56, s2, 0x800000\ns_xor_b32 s53, s56, 0xc000\nWV 29\ns_barrier\nRa 160,140,0\nRa 164,140,2048\nRa 168,142,0\n"
"Ra 172,142,2048\nTa 128,128,0\nTa 130,128,2048\nTa 132,129,0\nTa 134,129,2048\nTa 136,130,0\nTa 138,130,2048\nTa 140,131,0\nTa 142,131,2048\nTa 144,132,0\nTa 146,132,2048\n"
"Ta 148,133,0\nTa 150,133,2048\nTa 152,134,0\nTa 154,134,2048\nTa 156,135,0\nTa 158,135,2048\nWL 0\nZvaa 64,128,160\nRa 224,141,0\ns_add_u32 m0, s47, -16384\n"
"s_add_u32 s46, s52, 384\nZvaa 32,128,164\nRa 228,141,2048\nZvaa 192,128,168\nRa 232,143,0\nD0 136,28,46,0\nZvaa 224,128,172\nRa 236,143,2048\nZvaa 228,132,172\nTa 192,128,16384\n"
"Zvaa 196,132,168\nZvaa 36,132,164\nTa 194,128,18432\nZvaa 68,132,160\nTa 196,129,16384\nD0 137,28,46,1024\nZvaa 72,136,160\nTa 198,129,18432\nZvaa 40,136,164\nTa 200,130,16384\n"
"Zvaa 200,136,168\nTa 202,130,18432\nZvaa 232,136,172\nZvaa 236,140,172\nTa 204,131,16384\nD0 138,28,46,2048\nZvaa 204,140,168\nTa 206,131,18432\nZvaa 44,140,164\nTa 208,132,16384\n"
"Zvaa 76,140,160\nTa 210,132,18432\nZvaa 80,144,160\nTa 212,133,16384\nZvaa 48,144,164\nD0 139,28,46,3072\nZvaa 208,144,168\nTa 214,133,18432\nZvaa 240,144,172\nTa 216,134,16384\n"
"Zvaa 244,148,172\nTa 218,134,18432\nZvaa 212,148,168\nTa 220,135,16384\nZvaa 52,148,164\nTa 222,135,18432\nZvaa 84,148,160\nZvaa 88,152,160\nZvaa 56,152,164\nZvaa 216,152,168\n"
"Zvaa 248,152,172\nZvaa 252,156,172\ns_and_b32 s2, s74, 3\ns_lshl2_add_u32 s2, s73, s2\ns_lshl_b32 s2, s2, 12\ns_add_u32 s3, s45, 512\ns_and_b32 s3, s3, 0xfffe0000\n"
"s_add_u32 s2, s2, s3\nbuffer_load_dword v31, v26, s[20:23], s2 offen\nZvaa 220,156,168\nZvaa 60,156,164\nZvaa 92,156,160\ns_waitcnt vmcnt(22) lgkmcnt(0)\ns_barrier\n"
"Mvaa 64,192,224\nRa 160,140,16384\ns_add_u32 m0, s47, 65536\ns_add_u32 s46, s51, 1572864\nMvaa 32,192,228\nRa 164,140,18432\nWV 18\nMvaa 192,192,232\nRa 168,142,16384\n"
"D0 6,32,46,0\nMvaa 224,192,236\nRa 172,142,18432\nv_lshlrev_b32 v112, 16, v96\nMvaa 228,196,236\nTa 128,12,0\nv_lshlrev_b32 v113, 16, v100\nMvaa 196,196,232\n"
"v_lshlrev_b32 v114, 16, v104\nv_lshlrev_b32 v115, 16, v108\nMvaa 36,196,228\nTa 130,12,2048\nMU 116,112,114\nMvaa 68,196,224\nTa 132,13,0\nD0 7,32,46,1024\nMvaa 72,200,224\n"
"Ta 134,13,2048\nMU 117,113,114\nMvaa 40,200,228\nTa 136,14,0\nCV 116,116,117\nMvaa 200,200,232\nTa 138,14,2048\nv_lshlrev_b32 v118, 16, v116\nMvaa 232,200,236\n"
"v_and_b32 v119, 0xffff0000, v116\nMU 120,112,115\nMvaa 236,204,236\nTa 140,15,0\nD0 8,32,46,2048\nMvaa 204,204,232\nTa 142,15,2048\nMU 121,113,115\nMvaa 44,204,228\nTa 144,16,0\n"
"v_and_b32 v112, 0xffff0000, v96\nMvaa 76,204,224\nTa 146,16,2048\nv_and_b32 v113, 0xffff0000, v100\nMvaa 80,208,224\nTa 148,17,0\nv_and_b32 v114, 0xffff0000, v104\n"
"Mvaa 48,208,228\nD0 9,32,46,3072\nv_and_b32 v115, 0xffff0000, v108\nMvaa 208,208,232\nTa 150,17,2048\nMU 116,112,114\nMvaa 240,208,236\nTa 152,18,0\nMU 117,113,114\n"
"Mvaa 244,212,236\nTa 154,18,2048\ns_add_u32 m0, s47, 81920\ns_add_u32 s46, s51, 1835008\nMvaa 212,212,232\nTa 156,19,0\nCV 116,116,117\nMvaa 52,212,228\nTa 158,19,2048\n"
"D0 6,32,46,0\nMvaa 84,212,224\nv_and_b32 v117, 0xffff0000, v116\nv_lshlrev_b32 v116, 16, v116\nMvaa 88,216,224\nFM 120,112,115\nFM 121,113,115\nMvaa 56,216,228\nAD 118,118,116\n"
"AD 119,119,117\nMvaa 216,216,232\nv_lshlrev_b32 v112, 16, v97\nv_lshlrev_b32 v113, 16, v101\nMvaa 248,216,236\nD0 7,32,46,1024\nv_lshlrev_b32 v114, 16, v105\nMvaa 252,220,236\n"
"v_lshlrev_b32 v115, 16, v109\nMU 116,112,114\nMvaa 220,220,232\nMU 117,113,114\nCV 116,116,117\nMvaa 60,220,228\nv_and_b32 v117, 0xffff0000, v116\nv_lshlrev_b32 v116, 16, v116\n"
"Mvaa 92,220,224\nWL 0\nMvaa 64,128,160\nRa 224,141,16384\nD0 8,32,46,2048\nMvaa 32,128,164\nRa 228,141,18432\nFM 120,112,115\nMvaa 192,128,168\nRa 232,143,16384\nFM 121,113,115\n"
"Mvaa 224,128,172\nRa 236,143,18432\nAD 118,118,116\nMvaa 228,132,172\nTa 192,12,16384\nAD 119,119,117\nMvaa 196,132,168\nD0 9,32,46,3072\nv_and_b32 v112, 0xffff0000, v97\n"
"Mvaa 36,132,164\nTa 194,12,18432\nv_and_b32 v113, 0xffff0000, v101\nMvaa 68,132,160\nTa 196,13,16384\nv_and_b32 v114, 0xffff0000, v105\nMvaa 72,136,160\nTa 198,13,18432\n"
"s_add_u32 m0, s47, -65536\ns_add_u32 s46, s52, 512\nMvaa 40,136,164\nTa 200,14,16384\nv_and_b32 v115, 0xffff0000, v109\nMvaa 200,136,168\nTa 202,14,18432\nD0 136,28,46,0\n"
"Mvaa 232,136,172\nMU 116,112,114\nMU 117,113,114\nMvaa 236,140,172\nTa 204,15,16384\nCV 116,116,117\nMvaa 204,140,168\nTa 206,15,18432\nv_and_b32 v117, 0xffff0000, v116\n"
"Mvaa 44,140,164\nTa 208,16,16384\nv_lshlrev_b32 v116, 16, v116\nMvaa 76,140,160\nTa 210,16,18432\nD0 137,28,46,1024\nMvaa 80,144,160\nTa 212,17,16384\nFM 120,112,115\n"
"Mvaa 48,144,164\nFM 121,113,115\nAD 118,118,116\nMvaa 208,144,168\nTa 214,17,18432\nAD 119,119,117\nMvaa 240,144,172\nTa 216,18,16384\nv_lshlrev_b32 v112, 16, v98\n"
"Mvaa 244,148,172\nTa 218,18,18432\nD0 138,28,46,2048\nMvaa 212,148,168\nTa 220,19,16384\nv_lshlrev_b32 v113, 16, v102\nMvaa 52,148,164\nTa 222,19,18432\n"
"v_lshlrev_b32 v114, 16, v106\nMvaa 84,148,160\nv_lshlrev_b32 v115, 16, v110\nMU 116,112,114\nMvaa 88,152,160\nMU 117,113,114\nCV 116,116,117\nMvaa 56,152,164\nD0 139,28,46,3072\n"
"v_and_b32 v117, 0xffff0000, v116\nMvaa 216,152,168\nv_lshlrev_b32 v116, 16, v116\nFM 120,112,115\nMvaa 248,152,172\nFM 121,113,115\nAD 118,118,116\nMvaa 252,156,172\n"
"AD 119,119,117\nv_and_b32 v112, 0xffff0000, v98\nMvaa 220,156,168\nv_and_b32 v113, 0xffff0000, v102\nv_and_b32 v114, 0xffff0000, v106\nMvaa 60,156,164\n"
"v_and_b32 v115, 0xffff0000, v110\nMU 116,112,114\nMvaa 92,156,160\ns_waitcnt vmcnt(17) lgkmcnt(0)\ns_barrier\nMvaa 64,192,224\nRa 160,140,32768\ns_add_u32 m0, s47, 0\n"
"s_add_u32 s46, s51, 2097152\nMvaa 32,192,228\nRa 164,140,34816\nMU 117,113,114\nMvaa 192,192,232\nRa 168,142,32768\nD0 6,32,46,0\nMvaa 224,192,236\nRa 172,142,34816\n"
"CV 116,116,117\nMvaa 228,196,236\nTa 128,12,32768\nv_and_b32 v117, 0xffff0000, v116\nMvaa 196,196,232\nv_lshlrev_b32 v116, 16, v116\nFM 120,112,115\nMvaa 36,196,228\n"
"Ta 130,12,34816\nFM 121,113,115\nMvaa 68,196,224\nTa 132,13,32768\nD0 7,32,46,1024\nMvaa 72,200,224\nTa 134,13,34816\nAD 118,118,116\nMvaa 40,200,228\nTa 136,14,32768\n"
"AD 119,119,117\nMvaa 200,200,232\nTa 138,14,34816\nv_lshlrev_b32 v112, 16, v99\nMvaa 232,200,236\nv_lshlrev_b32 v113, 16, v103\nv_lshlrev_b32 v114, 16, v107\nMvaa 236,204,236\n"
"Ta 140,15,32768\nD0 8,32,46,2048\nMvaa 204,204,232\nTa 142,15,34816\nv_lshlrev_b32 v115, 16, v111\nMvaa 44,204,228\nTa 144,16,32768\nMU 116,112,114\nMvaa 76,204,224\n"
"Ta 146,16,34816\nMU 117,113,114\nMvaa 80,208,224\nTa 148,17,32768\nCV 116,116,117\nMvaa 48,208,228\nD0 9,32,46,3072\nv_and_b32 v117, 0xffff0000, v116\nMvaa 208,208,232\n"
"Ta 150,17,34816\nv_lshlrev_b32 v116, 16, v116\nMvaa 240,208,236\nTa 152,18,32768\nFM 120,112,115\nMvaa 244,212,236\nTa 154,18,34816\ns_add_u32 m0, s47, 16384\n"
"s_add_u32 s46, s51, 2359296\nMvaa 212,212,232\nTa 156,19,32768\nFM 121,113,115\nMvaa 52,212,228\nTa 158,19,34816\nD0 6,32,46,0\nMvaa 84,212,224\nAD 118,118,116\nAD 119,119,117\n"
"Mvaa 88,216,224\nv_and_b32 v112, 0xffff0000, v99\nv_and_b32 v113, 0xffff0000, v103\nMvaa 56,216,228\nv_and_b32 v114, 0xffff0000, v107\nv_and_b32 v115, 0xffff0000, v111\n"
"Mvaa 216,216,232\nMU 116,112,114\nMU 117,113,114\nMvaa 248,216,236\nD0 7,32,46,1024\nCV 116,116,117\nMvaa 252,220,236\nv_and_b32 v117, 0xffff0000, v116\n"
"v_lshlrev_b32 v116, 16, v116\nMvaa 220,220,232\nFM 120,112,115\nFM 121,113,115\nMvaa 60,220,228\nAD 118,118,116\nAD 119,119,117\nMvaa 92,220,224\nWL 0\nMvaa 64,128,160\n"
"Ra 224,141,32768\nD0 8,32,46,2048\nMvaa 32,128,164\nRa 228,141,34816\nv_permlane32_swap_b32 v118, v119\nMvaa 192,128,168\nRa 232,143,32768\nv_permlane32_swap_b32 v120, v121\n"
"Mvaa 224,128,172\nRa 236,143,34816\nAD 118,118,119\nMvaa 228,132,172\nTa 192,12,49152\nAD 120,120,121\nMvaa 196,132,168\nD0 9,32,46,3072\nMvaa 36,132,164\nTa 194,12,51200\n"
"v_permlane16_swap_b32 v118, v120\nMvaa 68,132,160\nTa 196,13,49152\nAD 118,118,120\nMvaa 72,136,160\nTa 198,13,51200\nMvaa 40,136,164\nTa 200,14,49152\n"
"v_add_f32_dpp v118, v118, v118 row_ror:8 row_mask:0xf bank_mask:0xf\nMvaa 200,136,168\nTa 202,14,51200\nMvaa 232,136,172\n"
"v_add_f32_dpp v118, v118, v118 row_ror:4 row_mask:0xf bank_mask:0xf\nMvaa 236,140,172\nTa 204,15,49152\nMvaa 204,140,168\nTa 206,15,51200\n"
"v_add_f32_dpp v118, v118, v118 row_ror:2 row_mask:0xf bank_mask:0xf\nMvaa 44,140,164\nTa 208,16,49152\nMvaa 76,140,160\nTa 210,16,51200\n"
"v_add_f32_dpp v118, v118, v118 row_ror:1 row_mask:0xf bank_mask:0xf\nMvaa 80,144,160\nTa 212,17,49152\nCV 118,118,118\nMvaa 48,144,164\nv_lshlrev_b32 v118, 16, v118\n"
"v_mul_f32 v119, 0xbfb8aa3b, v118\nMvaa 208,144,168\nTa 214,17,51200\nMvaa 240,144,172\nTa 216,18,49152\nMvaa 244,148,172\nTa 218,18,51200\nMvaa 212,148,168\nTa 220,19,49152\n"
"Mvaa 52,148,164\nTa 222,19,51200\nMvaa 84,148,160\nEX 119,119\nMvaa 88,152,160\nv_add_f32 v120, 0, v118\nv_add_f32 v119, 1.0, v119\nMvaa 56,152,164\nv_rcp_f32 v119, v119\n"
"Mvaa 216,152,168\nv_ashrrev_i32 v121, 31, v120\nCV 119,119,119\nMvaa 248,152,172\nv_lshlrev_b32 v119, 16, v119\nv_or_b32 v121, 0x80000000, v121\nMvaa 252,156,172\n"
"v_xor_b32 v120, v120, v121\nv_and_b32 v120, 0xffff0000, v120\ns_mov_b32 s92, s62\ns_and_b32 s93, s63, 0xffff\ns_mov_b32 s94, 0x188000\ns_mov_b32 s95, 0x20000\n"
"buffer_load_dword v31, v26, s[92:95], s97 offen\nMvaa 220,156,168\ns_lshl2_add_u32 s2, s10, s11\nv_lshrrev_b32 v112, 5, v0\nMvaa 60,156,164\nv_lshl_add_u32 v112, s2, 1, v112\n"
"v_sub_u32 v113, 0xffff, v112\nMvaa 92,156,160\ns_waitcnt vmcnt(13) lgkmcnt(0)\ns_barrier\nMvaa 64,192,224\nRa 160,140,49152\ns_add_u32 m0, s47, 32768\ns_add_u32 s46, s51, 2621440\n"
"Mvaa 32,192,228\nRa 164,140,51200\nv_or_b32 v120, v120, v113\nMvaa 192,192,232\nRa 168,142,49152\nD0 6,32,46,0\nMvaa 224,192,236\nRa 172,142,51200\nv_mov_b32 v121, s15\n"
"Mvaa 228,196,236\nTa 128,128,0\nv_and_b32 v113, 31, v0\nMvaa 196,196,232\nv_mov_b32 v116, 0x40000000\nv_cmp_eq_u32 vcc, 16, v113\nv_lshlrev_b32 v114, 3, v112\n"
"v_lshlrev_b32 v115, 9, v113\nv_cndmask_b32 v114, v116, v114, vcc\nv_cndmask_b32 v119, v119, v118, vcc\nMvaa 36,196,228\nTa 130,128,2048\nv_lshl_add_u32 v115, v112, 2, v115\n"
"Mvaa 68,196,224\nTa 132,129,0\nD0 7,32,46,1024\nMvaa 72,200,224\nTa 134,129,2048\nv_and_b32 v113, 15, v0\nMvaa 40,200,228\nTa 136,130,0\nv_cmp_eq_u32 vcc, 0, v113\n"
"s_mov_b32 s2, 0xc040000\ns_mov_b32 s3, 0xc044000\nv_cndmask_b32 v115, v116, v115, vcc\nbuffer_store_dwordx2 v[120:121], v114, s[16:19], s3 offen sc0 sc1\n"
"buffer_store_dword v119, v115, s[16:19], s2 offen sc0 sc1\nMvaa 200,200,232\nTa 138,130,2048\nMvaa 232,200,236\nMvaa 236,204,236\nTa 140,131,0\nD0 8,32,46,2048\nMvaa 204,204,232\n"
"Ta 142,131,2048\nMvaa 44,204,228\nTa 144,132,0\nMvaa 76,204,224\nTa 146,132,2048\nMvaa 80,208,224\nTa 148,133,0\nMvaa 48,208,228\nD0 9,32,46,3072\nMvaa 208,208,232\n"
"Ta 150,133,2048\nMvaa 240,208,236\nTa 152,134,0\nMvaa 244,212,236\nTa 154,134,2048\ns_add_u32 m0, s47, -32768\ns_add_u32 s46, s52, 640\nMvaa 212,212,232\nTa 156,135,0\n"
"Mvaa 52,212,228\nTa 158,135,2048\nD0 136,28,46,0\nMvaa 84,212,224\nMvaa 88,216,224\nMvaa 56,216,228\nMvaa 216,216,232\nMvaa 248,216,236\nD0 137,28,46,1024\nMvaa 252,220,236\n"
"Mvaa 220,220,232\nMvaa 60,220,228\nMvaa 92,220,224\nWL 0\nMvaa 64,128,160\nRa 224,141,49152\nD0 138,28,46,2048\nMvaa 32,128,164\nRa 228,141,51200\nMvaa 192,128,168\n"
"Ra 232,143,49152\nMvaa 224,128,172\nRa 236,143,51200\nMvaa 228,132,172\nTa 192,128,16384\nMvaa 196,132,168\nD0 139,28,46,3072\nMvaa 36,132,164\nTa 194,128,18432\nMvaa 68,132,160\n"
"Ta 196,129,16384\nMvaa 72,136,160\nTa 198,129,18432\ns_add_u32 m0, s47, 49152\ns_add_u32 s46, s51, 2883584\nMvaa 40,136,164\nTa 200,130,16384\nMvaa 200,136,168\nTa 202,130,18432\n"
"D0 6,32,46,0\nMvaa 232,136,172\nMvaa 236,140,172\nTa 204,131,16384\nMvaa 204,140,168\nTa 206,131,18432\nMvaa 44,140,164\nTa 208,132,16384\nMvaa 76,140,160\nTa 210,132,18432\n"
"D0 7,32,46,1024\nMvaa 80,144,160\nTa 212,133,16384\nMvaa 48,144,164\nMvaa 208,144,168\nTa 214,133,18432\nMvaa 240,144,172\nTa 216,134,16384\nMvaa 244,148,172\nTa 218,134,18432\n"
"D0 8,32,46,2048\nMvaa 212,148,168\nTa 220,135,16384\nMvaa 52,148,164\nTa 222,135,18432\nMvaa 84,148,160\nMvaa 88,152,160\nMvaa 56,152,164\nD0 9,32,46,3072\nMvaa 216,152,168\n"
"Mvaa 248,152,172\nMvaa 252,156,172\ns_mov_b32 s92, s64\ns_and_b32 s93, s65, 0xffff\ns_mov_b32 s94, 0x188000\ns_mov_b32 s95, 0x20000\n"
"buffer_load_dword v31, v26, s[92:95], s97 offen\nMvaa 220,156,168\nMvaa 60,156,164\nMvaa 92,156,160\ns_waitcnt vmcnt(16) lgkmcnt(0)\ns_barrier\nMvaa 64,192,224\nRa 160,140,0\n"
"s_add_u32 m0, s47, 65536\ns_add_u32 s46, s51, 3145728\nMvaa 32,192,228\nRa 164,140,2048\nMvaa 192,192,232\nRa 168,142,0\nD0 6,32,46,0\nMvaa 224,192,236\nRa 172,142,2048\n"
"Mvaa 228,196,236\nTa 128,12,0\nMvaa 196,196,232\nMvaa 36,196,228\nTa 130,12,2048\nMvaa 68,196,224\nTa 132,13,0\nD0 7,32,46,1024\nMvaa 72,200,224\nTa 134,13,2048\nMvaa 40,200,228\n"
"Ta 136,14,0\nMvaa 200,200,232\nTa 138,14,2048\nMvaa 232,200,236\nMvaa 236,204,236\nTa 140,15,0\nD0 8,32,46,2048\nMvaa 204,204,232\nTa 142,15,2048\nMvaa 44,204,228\nTa 144,16,0\n"
"Mvaa 76,204,224\nTa 146,16,2048\nMvaa 80,208,224\nTa 148,17,0\nMvaa 48,208,228\nD0 9,32,46,3072\nMvaa 208,208,232\nTa 150,17,2048\nMvaa 240,208,236\nTa 152,18,0\nMvaa 244,212,236\n"
"Ta 154,18,2048\ns_add_u32 m0, s47, -16384\ns_add_u32 s46, s52, 768\nMvaa 212,212,232\nTa 156,19,0\nMvaa 52,212,228\nTa 158,19,2048\nD0 136,28,46,0\nMvaa 84,212,224\n"
"Mvaa 88,216,224\nMvaa 56,216,228\nMvaa 216,216,232\nMvaa 248,216,236\nD0 137,28,46,1024\nMvaa 252,220,236\nMvaa 220,220,232\nMvaa 60,220,228\nMvaa 92,220,224\nWL 0\n"
"Mvaa 64,128,160\nRa 224,141,0\nD0 138,28,46,2048\nMvaa 32,128,164\nRa 228,141,2048\nMvaa 192,128,168\nRa 232,143,0\nMvaa 224,128,172\nRa 236,143,2048\nMvaa 228,132,172\n"
"Ta 192,12,16384\nMvaa 196,132,168\nD0 139,28,46,3072\nMvaa 36,132,164\nTa 194,12,18432\nMvaa 68,132,160\nTa 196,13,16384\nMvaa 72,136,160\nTa 198,13,18432\n"
"s_add_u32 m0, s47, 81920\ns_add_u32 s46, s51, 3407872\nMvaa 40,136,164\nTa 200,14,16384\nMvaa 200,136,168\nTa 202,14,18432\nD0 6,32,46,0\nMvaa 232,136,172\nMvaa 236,140,172\n"
"Ta 204,15,16384\nMvaa 204,140,168\nTa 206,15,18432\nMvaa 44,140,164\nTa 208,16,16384\nMvaa 76,140,160\nTa 210,16,18432\nD0 7,32,46,1024\nMvaa 80,144,160\nTa 212,17,16384\n"
"Mvaa 48,144,164\nMvaa 208,144,168\nTa 214,17,18432\nMvaa 240,144,172\nTa 216,18,16384\nMvaa 244,148,172\nTa 218,18,18432\nD0 8,32,46,2048\nMvaa 212,148,168\nTa 220,19,16384\n"
"Mvaa 52,148,164\nTa 222,19,18432\nMvaa 84,148,160\nMvaa 88,152,160\nMvaa 56,152,164\nD0 9,32,46,3072\nMvaa 216,152,168\nMvaa 248,152,172\nMvaa 252,156,172\ns_mov_b32 s92, s66\n"
"s_and_b32 s93, s67, 0xffff\ns_mov_b32 s94, 0x188000\ns_mov_b32 s95, 0x20000\nbuffer_load_dword v31, v26, s[92:95], s97 offen\nMvaa 220,156,168\nMvaa 60,156,164\nMvaa 92,156,160\n"
"s_waitcnt vmcnt(14) lgkmcnt(0)\ns_barrier\nMvaa 64,192,224\nRa 160,140,32768\ns_add_u32 m0, s47, 0\ns_add_u32 s44, s44, 0x40000\nMvaa 32,192,228\nRa 164,140,34816\n"
"Mvaa 192,192,232\nRa 168,142,32768\nD0 6,24,44,0\nMvaa 224,192,236\nRa 172,142,34816\nMvaa 228,196,236\nTa 128,12,32768\nMvaa 196,196,232\nMvaa 36,196,228\nTa 130,12,34816\n"
"Mvaa 68,196,224\nTa 132,13,32768\nD0 7,24,44,1024\nMvaa 72,200,224\nTa 134,13,34816\nMvaa 40,200,228\nTa 136,14,32768\nMvaa 200,200,232\nTa 138,14,34816\nMvaa 232,200,236\n"
"Mvaa 236,204,236\nTa 140,15,32768\nD0 8,24,44,2048\nMvaa 204,204,232\nTa 142,15,34816\nMvaa 44,204,228\nTa 144,16,32768\nMvaa 76,204,224\nTa 146,16,34816\nMvaa 80,208,224\n"
"Ta 148,17,32768\nMvaa 48,208,228\nD0 9,24,44,3072\nMvaa 208,208,232\nTa 150,17,34816\nMvaa 240,208,236\nTa 152,18,32768\nMvaa 244,212,236\nTa 154,18,34816\ns_add_u32 m0, s48, 0\n"
"s_add_u32 s45, s45, 128\nMvaa 212,212,232\nTa 156,19,32768\nMvaa 52,212,228\nTa 158,19,34816\nD0 2,20,45,0\nMvaa 84,212,224\nMvaa 88,216,224\nMvaa 56,216,228\nMvaa 216,216,232\n"
"Mvaa 248,216,236\nD0 3,20,45,1024\nMvaa 252,220,236\nMvaa 220,220,232\nMvaa 60,220,228\nMvaa 92,220,224\nWL 0\nMvaa 64,128,160\nRa 224,141,32768\nD0 4,20,45,2048\nMvaa 32,128,164\n"
"Ra 228,141,34816\nMvaa 192,128,168\nRa 232,143,32768\nMvaa 224,128,172\nRa 236,143,34816\nMvaa 228,132,172\nTa 192,12,49152\nMvaa 196,132,168\nD0 5,20,45,3072\nMvaa 36,132,164\n"
"Ta 194,12,51200\nMvaa 68,132,160\nTa 196,13,49152\nMvaa 72,136,160\nTa 198,13,51200\ns_add_u32 m0, s48, 4096\ns_add_u32 s46, s45, 0x10000\nMvaa 40,136,164\nTa 200,14,49152\n"
"Mvaa 200,136,168\nTa 202,14,51200\nD0 2,20,46,0\nMvaa 232,136,172\nMvaa 236,140,172\nTa 204,15,49152\nMvaa 204,140,168\nTa 206,15,51200\nMvaa 44,140,164\nTa 208,16,49152\n"
"Mvaa 76,140,160\nTa 210,16,51200\nD0 3,20,46,1024\nMvaa 80,144,160\nTa 212,17,49152\nMvaa 48,144,164\nMvaa 208,144,168\nTa 214,17,51200\nMvaa 240,144,172\nTa 216,18,49152\n"
"Mvaa 244,148,172\nTa 218,18,51200\nD0 4,20,46,2048\nMvaa 212,148,168\nTa 220,19,49152\nMvaa 52,148,164\nTa 222,19,51200\nMvaa 84,148,160\nMvaa 88,152,160\nMvaa 56,152,164\n"
"D0 5,20,46,3072\nMvaa 216,152,168\nMvaa 248,152,172\nMvaa 252,156,172\ns_add_u32 m0, s47, 16384\ns_add_u32 s44, s44, 0x40000\nMvaa 220,156,168\nMvaa 60,156,164\nD0 6,24,44,0\n"
"Mvaa 92,156,160\ns_waitcnt vmcnt(14) lgkmcnt(0)\ns_barrier\nMvaa 64,192,224\nRa 160,140,49152\nMvaa 32,192,228\nRa 164,140,51200\nMvaa 192,192,232\nRa 168,142,49152\n"
"Mvaa 224,192,236\nRa 172,142,51200\nD0 7,24,44,1024\nMvaa 228,196,236\nTa 128,128,0\nMvaa 196,196,232\nMvaa 36,196,228\nTa 130,128,2048\nMvaa 68,196,224\nTa 132,129,0\n"
"Mvaa 72,200,224\nTa 134,129,2048\nD0 8,24,44,2048\nMvaa 40,200,228\nTa 136,130,0\nMvaa 200,200,232\nTa 138,130,2048\nMvaa 232,200,236\nMvaa 236,204,236\nTa 140,131,0\n"
"Mvaa 204,204,232\nTa 142,131,2048\nD0 9,24,44,3072\nMvaa 44,204,228\nTa 144,132,0\nMvaa 76,204,224\nTa 146,132,2048\nMvaa 80,208,224\nTa 148,133,0\ns_add_u32 m0, s47, 32768\n"
"s_add_u32 s44, s44, 0x40000\nMvaa 48,208,228\nMvaa 208,208,232\nTa 150,133,2048\nD0 6,24,44,0\nMvaa 240,208,236\nTa 152,134,0\nMvaa 244,212,236\nTa 154,134,2048\nMvaa 212,212,232\n"
"Ta 156,135,0\nMvaa 52,212,228\nTa 158,135,2048\nMvaa 84,212,224\nD0 7,24,44,1024\nMvaa 88,216,224\nMvaa 56,216,228\nMvaa 216,216,232\nMvaa 248,216,236\nMvaa 252,220,236\n"
"D0 8,24,44,2048\nMvaa 220,220,232\nMvaa 60,220,228\nMvaa 92,220,224\nWL 0\nMvaa 64,128,160\nRa 224,141,49152\nMvaa 32,128,164\nRa 228,141,51200\nD0 9,24,44,3072\nMvaa 192,128,168\n"
"Ra 232,143,49152\nMvaa 224,128,172\nRa 236,143,51200\nMvaa 228,132,172\nTa 192,128,16384\nMvaa 196,132,168\nMvaa 36,132,164\nTa 194,128,18432\nMvaa 68,132,160\nTa 196,129,16384\n"
"Mvaa 72,136,160\nTa 198,129,18432\nMvaa 40,136,164\nTa 200,130,16384\nMvaa 200,136,168\nTa 202,130,18432\nMvaa 232,136,172\nMvaa 236,140,172\nTa 204,131,16384\nMvaa 204,140,168\n"
"Ta 206,131,18432\nMvaa 44,140,164\nTa 208,132,16384\nMvaa 76,140,160\nTa 210,132,18432\nMvaa 80,144,160\nTa 212,133,16384\nMvaa 48,144,164\nMvaa 208,144,168\nTa 214,133,18432\n"
"Mvaa 240,144,172\nTa 216,134,16384\nMvaa 244,148,172\nTa 218,134,18432\nMvaa 212,148,168\nTa 220,135,16384\nMvaa 52,148,164\nTa 222,135,18432\nMvaa 84,148,160\nMvaa 88,152,160\n"
"Mvaa 56,152,164\nMvaa 216,152,168\nMvaa 248,152,172\nMvaa 252,156,172\nMvaa 220,156,168\nMvaa 60,156,164\nMvaa 92,156,160\ns_waitcnt vmcnt(8) lgkmcnt(0)\ns_barrier\n"
"Mvaa 64,192,224\ns_add_u32 m0, s48, 32768\ns_add_u32 s45, s45, 128\nTa 128,12,0\nMvaa 68,196,224\nTa 130,12,2048\nMvaa 72,200,224\nD0 2,20,45,0\nTa 132,13,0\nMvaa 76,204,224\n"
"Ta 134,13,2048\nMvaa 80,208,224\nTa 136,14,0\nMvaa 84,212,224\nTa 138,14,2048\nMvaa 88,216,224\nTa 140,15,0\nv_mul_f32 v64, s57, v64\nMvaa 92,220,224\nD0 3,20,45,1024\n"
"Ta 142,15,2048\nMvaa 32,192,228\nTa 144,16,0\nv_mul_f32 v65, s57, v65\nMvaa 36,196,228\nTa 146,16,2048\nCV 64,64,65\nMvaa 40,200,228\nTa 148,17,0\nv_mul_f32 v66, s57, v66\n"
"Mvaa 44,204,228\nTa 150,17,2048\nv_mul_f32 v67, s57, v67\nMvaa 48,208,228\nD0 4,20,45,2048\nTa 152,18,0\nMvaa 52,212,228\nTa 154,18,2048\nCV 65,66,67\nMvaa 56,216,228\n"
"Ta 156,19,0\nv_mul_f32 v68, s57, v68\nMvaa 60,220,228\nTa 158,19,2048\nv_mul_f32 v69, s57, v69\nMvaa 192,192,232\nRa 160,10,0\nCV 66,68,69\nMvaa 196,196,232\nD0 5,20,45,3072\n"
"Ra 164,10,2048\nMvaa 200,200,232\nRa 168,10,8192\nv_mul_f32 v70, s57, v70\nMvaa 204,204,232\nRa 172,10,10240\nv_mul_f32 v71, s57, v71\nMvaa 208,208,232\nRa 176,10,16384\n"
"CV 67,70,71\nMvaa 212,212,232\nRa 180,10,18432\nv_mul_f32 v72, s57, v72\nMvaa 216,216,232\nRa 184,10,24576\nv_mul_f32 v73, s57, v73\nMvaa 220,220,232\nRa 188,10,26624\n"
"CV 72,72,73\nMvaa 224,192,236\nv_mul_f32 v74, s57, v74\nv_mul_f32 v75, s57, v75\nMvaa 228,196,236\nCV 73,74,75\nv_mul_f32 v76, s57, v76\nMvaa 232,200,236\nv_mul_f32 v77, s57, v77\n"
"CV 74,76,77\nMvaa 236,204,236\nv_mul_f32 v78, s57, v78\nv_mul_f32 v79, s57, v79\nMvaa 240,208,236\nCV 75,78,79\nMvaa 244,212,236\nMvaa 248,216,236\nMvaa 252,220,236\n"
"s_waitcnt vmcnt(8) lgkmcnt(0)\ns_barrier\ns_bitcmp1_b32 s13, 0\ns_cbranch_scc1 .V17\nZaaa 0,128,176\nTa 192,12,16384\nv_mul_f32 v80, s57, v80\nZaaa 32,128,180\n"
"s_add_u32 m0, s48, 36864\ns_add_u32 s46, s45, 0x10000\ns_add_u32 s6, s56, 0\nSv 64,1,16,6,0\nZaaa 64,128,184\nTa 194,12,18432\nv_mul_f32 v81, s57, v81\nZaaa 96,128,188\n"
"D0 2,20,46,0\nCV 80,80,81\nZaaa 100,132,188\nTa 196,13,16384\nv_mul_f32 v82, s57, v82\nZaaa 68,132,184\nv_mul_f32 v83, s57, v83\nCV 81,82,83\nZaaa 36,132,180\nTa 198,13,18432\n"
"v_mul_f32 v84, s57, v84\nZaaa 4,132,176\nD0 3,20,46,1024\nv_mul_f32 v85, s57, v85\nZaaa 8,136,176\nTa 200,14,16384\nCV 82,84,85\nZaaa 40,136,180\nv_mul_f32 v86, s57, v86\n"
"v_mul_f32 v87, s57, v87\ns_add_u32 s6, s56, 0\nSv 72,1,16,6,2048\nZaaa 72,136,184\nTa 202,14,18432\nCV 83,86,87\nZaaa 104,136,188\nD0 4,20,46,2048\nv_mul_f32 v88, s57, v88\n"
"Zaaa 108,140,188\nTa 204,15,16384\nv_mul_f32 v89, s57, v89\nZaaa 76,140,184\nCV 88,88,89\nv_mul_f32 v90, s57, v90\nZaaa 44,140,180\nTa 206,15,18432\nv_mul_f32 v91, s57, v91\n"
"Zaaa 12,140,176\nD0 5,20,46,3072\nCV 89,90,91\nZaaa 16,144,176\nTa 208,16,16384\nv_mul_f32 v92, s57, v92\nZaaa 48,144,180\nv_mul_f32 v93, s57, v93\nCV 90,92,93\nZaaa 80,144,184\n"
"Ta 210,16,18432\nv_mul_f32 v94, s57, v94\nZaaa 112,144,188\nv_mul_f32 v95, s57, v95\nCV 91,94,95\nZaaa 116,148,188\nTa 212,17,16384\nZaaa 84,148,184\nZaaa 52,148,180\n"
"Ta 214,17,18432\nZaaa 20,148,176\nZaaa 24,152,176\nTa 216,18,16384\nZaaa 56,152,180\ns_add_u32 s6, s56, 4096\nSv 80,1,16,6,0\nZaaa 88,152,184\nTa 218,18,18432\nZaaa 120,152,188\n"
"Zaaa 124,156,188\nTa 220,19,16384\nZaaa 92,156,184\ns_add_u32 s6, s56, 4096\nSv 88,1,16,6,2048\nZaaa 60,156,180\nTa 222,19,18432\nZaaa 28,156,176\ns_add_u32 m0, s47, 49152\n"
"s_add_u32 s44, s44, 0x40000\nZvaa 64,128,160\nRa 224,11,0\nv_mul_f32 v32, s57, v32\nZvaa 96,128,164\nD0 6,24,44,0\nv_mul_f32 v33, s57, v33\nZvaa 128,128,168\nRa 228,11,2048\n"
"CV 32,32,33\nZvaa 160,128,172\nv_mul_f32 v34, s57, v34\nv_mul_f32 v35, s57, v35\nZvaa 164,132,172\nRa 232,11,8192\nCV 33,34,35\nZvaa 132,132,168\nD0 7,24,44,1024\n"
"v_mul_f32 v36, s57, v36\nZvaa 100,132,164\nRa 236,11,10240\nv_mul_f32 v37, s57, v37\nZvaa 68,132,160\nCV 34,36,37\nv_mul_f32 v38, s57, v38\nZvaa 72,136,160\nRa 240,11,16384\n"
"v_mul_f32 v39, s57, v39\nZvaa 104,136,164\nD0 8,24,44,2048\nCV 35,38,39\nZvaa 136,136,168\nRa 244,11,18432\nZvaa 168,136,172\ns_add_u32 s6, s56, 0\nSv 32,1,16,6,1024\n"
"Zvaa 172,140,172\nRa 248,11,24576\nZvaa 140,140,168\nD0 9,24,44,3072\nZvaa 108,140,164\nRa 252,11,26624\nZvaa 76,140,160\ns_add_u32 m0, s47, 0\ns_add_u32 s44, s44, 0x40000\n"
"Zvaa 80,144,160\nZvaa 112,144,164\nD0 6,24,44,0\nv_mul_f32 v40, s57, v40\nZvaa 144,144,168\nv_mul_f32 v41, s57, v41\nCV 40,40,41\nZvaa 176,144,172\nv_mul_f32 v42, s57, v42\n"
"v_mul_f32 v43, s57, v43\nZvaa 180,148,172\nCV 41,42,43\nv_mul_f32 v44, s57, v44\nZvaa 148,148,168\nD0 7,24,44,1024\nv_mul_f32 v45, s57, v45\nZvaa 116,148,164\nCV 42,44,45\n"
"v_mul_f32 v46, s57, v46\nZvaa 84,148,160\nv_mul_f32 v47, s57, v47\nCV 43,46,47\nZvaa 88,152,160\ns_add_u32 s6, s56, 0\nSv 40,1,16,6,3072\nZvaa 120,152,164\nD0 8,24,44,2048\n"
"Zvaa 152,152,168\nZvaa 184,152,172\nZvaa 188,156,172\nZvaa 156,156,168\nD0 9,24,44,3072\nZvaa 124,156,164\nZvaa 92,156,160\ns_waitcnt vmcnt(12) lgkmcnt(0)\ns_barrier\n"
"Mvaa 64,192,224\nTa 128,12,32768\nMvaa 96,192,228\nMvaa 128,192,232\nTa 130,12,34816\nv_mul_f32 v48, s57, v48\nMvaa 160,192,236\ns_add_u32 m0, s48, 0\ns_add_u32 s45, s45, 128\n"
"Maaa 0,192,240\nTa 132,13,32768\nv_mul_f32 v49, s57, v49\nMaaa 32,192,244\nD0 2,20,45,0\nCV 48,48,49\nMaaa 64,192,248\nTa 134,13,34816\nv_mul_f32 v50, s57, v50\nMaaa 96,192,252\n"
"v_mul_f32 v51, s57, v51\nCV 49,50,51\nMaaa 100,196,252\nTa 136,14,32768\nv_mul_f32 v52, s57, v52\nMaaa 68,196,248\nv_mul_f32 v53, s57, v53\nCV 50,52,53\nMaaa 36,196,244\n"
"Ta 138,14,34816\nv_mul_f32 v54, s57, v54\nMaaa 4,196,240\nv_mul_f32 v55, s57, v55\nCV 51,54,55\nMvaa 164,196,236\nTa 140,15,32768\nMvaa 132,196,232\nD0 3,20,45,1024\n"
"Mvaa 100,196,228\nTa 142,15,34816\nMvaa 68,196,224\ns_add_u32 s6, s56, 4096\nSv 48,1,16,6,1024\nMvaa 72,200,224\nTa 144,16,32768\nMvaa 104,200,228\nMvaa 136,200,232\n"
"Ta 146,16,34816\nMvaa 168,200,236\nv_mul_f32 v56, s57, v56\nv_mul_f32 v57, s57, v57\nMaaa 8,200,240\nTa 148,17,32768\nCV 56,56,57\nMaaa 40,200,244\nD0 4,20,45,2048\n"
"v_mul_f32 v58, s57, v58\nMaaa 72,200,248\nTa 150,17,34816\nv_mul_f32 v59, s57, v59\nMaaa 104,200,252\nCV 57,58,59\nv_mul_f32 v60, s57, v60\nMaaa 108,204,252\nTa 152,18,32768\n"
"v_mul_f32 v61, s57, v61\nMaaa 76,204,248\nCV 58,60,61\nv_mul_f32 v62, s57, v62\nMaaa 44,204,244\nTa 154,18,34816\nv_mul_f32 v63, s57, v63\nMaaa 12,204,240\nCV 59,62,63\n"
"Mvaa 172,204,236\nTa 156,19,32768\nMvaa 140,204,232\nD0 5,20,45,3072\nMvaa 108,204,228\nTa 158,19,34816\nMvaa 76,204,224\ns_add_u32 s6, s56, 4096\nSv 56,1,16,6,3072\n"
"Mvaa 80,208,224\nRa 160,10,32768\nMvaa 112,208,228\nMvaa 144,208,232\nRa 164,10,34816\nMvaa 176,208,236\ns_add_u32 m0, s48, 4096\ns_add_u32 s46, s45, 0x10000\nMaaa 16,208,240\n"
"Ra 168,10,40960\nv_mul_f32 v192, s57, v192\nMaaa 48,208,244\nD0 2,20,46,0\nv_mul_f32 v193, s57, v193\nMaaa 80,208,248\nRa 172,10,43008\nCV 192,192,193\nMaaa 112,208,252\n"
"v_mul_f32 v194, s57, v194\nv_mul_f32 v195, s57, v195\nMaaa 116,212,252\nRa 176,10,49152\nCV 193,194,195\nMaaa 84,212,248\nv_mul_f32 v196, s57, v196\nv_mul_f32 v197, s57, v197\n"
"Maaa 52,212,244\nRa 180,10,51200\nCV 194,196,197\nMaaa 20,212,240\nv_mul_f32 v198, s57, v198\nv_mul_f32 v199, s57, v199\nMvaa 180,212,236\nRa 184,10,57344\nCV 195,198,199\n"
"Mvaa 148,212,232\nD0 3,20,46,1024\nMvaa 116,212,228\nRa 188,10,59392\nMvaa 84,212,224\ns_add_u32 s6, s53, 0\nSv 192,1,16,6,0\nMvaa 88,216,224\nMvaa 120,216,228\nMvaa 152,216,232\n"
"Mvaa 184,216,236\nMaaa 24,216,240\nMaaa 56,216,244\nD0 4,20,46,2048\nv_mul_f32 v200, s57, v200\nMaaa 88,216,248\nv_mul_f32 v201, s57, v201\nCV 200,200,201\nMaaa 120,216,252\n"
"v_mul_f32 v202, s57, v202\nv_mul_f32 v203, s57, v203\nMaaa 124,220,252\nCV 201,202,203\nv_mul_f32 v204, s57, v204\nMaaa 92,220,248\nv_mul_f32 v205, s57, v205\nCV 202,204,205\n"
"Maaa 60,220,244\nv_mul_f32 v206, s57, v206\nv_mul_f32 v207, s57, v207\nMaaa 28,220,240\nCV 203,206,207\nMvaa 188,220,236\ns_add_u32 s6, s53, 0\nSv 200,1,16,6,2048\n"
"Mvaa 156,220,232\nD0 5,20,46,3072\nMvaa 124,220,228\nMvaa 92,220,224\ns_waitcnt vmcnt(17) lgkmcnt(0)\ns_barrier\nMvaa 64,128,160\nTa 192,12,49152\nMvaa 96,128,164\n"
"Mvaa 128,128,168\nTa 194,12,51200\nMvaa 160,128,172\ns_add_u32 m0, s47, 16384\ns_add_u32 s44, s44, 0x40000\nMaaa 0,128,176\nTa 196,13,49152\nMaaa 32,128,180\nD0 6,24,44,0\n"
"Maaa 64,128,184\nTa 198,13,51200\nv_mul_f32 v208, s57, v208\nMaaa 96,128,188\nv_mul_f32 v209, s57, v209\nCV 208,208,209\nMaaa 100,132,188\nTa 200,14,49152\n"
"v_mul_f32 v210, s57, v210\nMaaa 68,132,184\nv_mul_f32 v211, s57, v211\nCV 209,210,211\nMaaa 36,132,180\nTa 202,14,51200\nv_mul_f32 v212, s57, v212\nMaaa 4,132,176\n"
"v_mul_f32 v213, s57, v213\nCV 210,212,213\nMvaa 164,132,172\nTa 204,15,49152\nv_mul_f32 v214, s57, v214\nMvaa 132,132,168\nD0 7,24,44,1024\nv_mul_f32 v215, s57, v215\n"
"Mvaa 100,132,164\nTa 206,15,51200\nCV 211,214,215\nMvaa 68,132,160\ns_add_u32 s6, s53, 4096\nSv 208,1,16,6,0\nMvaa 72,136,160\nTa 208,16,49152\nMvaa 104,136,164\nMvaa 136,136,168\n"
"Ta 210,16,51200\nMvaa 168,136,172\nMaaa 8,136,176\nTa 212,17,49152\nMaaa 40,136,180\nD0 8,24,44,2048\nMaaa 72,136,184\nTa 214,17,51200\nMaaa 104,136,188\n"
"v_mul_f32 v216, s57, v216\nv_mul_f32 v217, s57, v217\nMaaa 108,140,188\nTa 216,18,49152\nCV 216,216,217\nMaaa 76,140,184\nv_mul_f32 v218, s57, v218\nv_mul_f32 v219, s57, v219\n"
"Maaa 44,140,180\nTa 218,18,51200\nCV 217,218,219\nMaaa 12,140,176\nv_mul_f32 v220, s57, v220\nv_mul_f32 v221, s57, v221\nMvaa 172,140,172\nTa 220,19,49152\nCV 218,220,221\n"
"Mvaa 140,140,168\nD0 9,24,44,3072\nv_mul_f32 v222, s57, v222\nMvaa 108,140,164\nTa 222,19,51200\nv_mul_f32 v223, s57, v223\nMvaa 76,140,160\nCV 219,222,223\nMvaa 80,144,160\n"
"Ra 224,11,32768\nMvaa 112,144,164\ns_add_u32 s6, s53, 4096\nSv 216,1,16,6,2048\nMvaa 144,144,168\nRa 228,11,34816\nMvaa 176,144,172\ns_add_u32 m0, s47, 32768\n"
"s_add_u32 s44, s44, 0x40000\nMaaa 16,144,176\nRa 232,11,40960\nMaaa 48,144,180\nD0 6,24,44,0\nMaaa 80,144,184\nRa 236,11,43008\nMaaa 112,144,188\nMaaa 116,148,188\n"
"Ra 240,11,49152\nv_mul_f32 v224, s57, v224\nMaaa 84,148,184\nv_mul_f32 v225, s57, v225\nCV 224,224,225\nMaaa 52,148,180\nRa 244,11,51200\nv_mul_f32 v226, s57, v226\n"
"Maaa 20,148,176\nv_mul_f32 v227, s57, v227\nCV 225,226,227\nMvaa 180,148,172\nRa 248,11,57344\nv_mul_f32 v228, s57, v228\nMvaa 148,148,168\nD0 7,24,44,1024\n"
"v_mul_f32 v229, s57, v229\nMvaa 116,148,164\nRa 252,11,59392\nCV 226,228,229\nMvaa 84,148,160\nv_mul_f32 v230, s57, v230\nv_mul_f32 v231, s57, v231\nMvaa 88,152,160\n"
"CV 227,230,231\nMvaa 120,152,164\ns_add_u32 s6, s53, 0\nSv 224,1,16,6,1024\nMvaa 152,152,168\nMvaa 184,152,172\nMaaa 24,152,176\nMaaa 56,152,180\nD0 8,24,44,2048\nMaaa 88,152,184\n"
"Maaa 120,152,188\nMaaa 124,156,188\nMaaa 92,156,184\nv_mul_f32 v232, s57, v232\nv_mul_f32 v233, s57, v233\nMaaa 60,156,180\nCV 232,232,233\nv_mul_f32 v234, s57, v234\n"
"Maaa 28,156,176\nv_mul_f32 v235, s57, v235\nCV 233,234,235\nMvaa 188,156,172\nv_mul_f32 v236, s57, v236\nv_mul_f32 v237, s57, v237\nMvaa 156,156,168\nD0 9,24,44,3072\n"
"CV 234,236,237\nMvaa 124,156,164\nv_mul_f32 v238, s57, v238\nv_mul_f32 v239, s57, v239\nMvaa 92,156,160\ns_waitcnt vmcnt(11) lgkmcnt(0)\ns_barrier\nMvaa 64,192,224\nTa 128,12,0\n"
"CV 235,238,239\nMvaa 96,192,228\ns_add_u32 s6, s53, 0\nSv 232,1,16,6,3072\nMvaa 128,192,232\nTa 130,12,2048\nMvaa 160,192,236\ns_add_u32 m0, s48, 32768\ns_add_u32 s45, s45, 128\n"
"Maaa 0,192,240\nTa 132,13,0\nMaaa 32,192,244\nD0 2,20,45,0\nMaaa 64,192,248\nTa 134,13,2048\nMaaa 96,192,252\nMaaa 100,196,252\nTa 136,14,0\nMaaa 68,196,248\nMaaa 36,196,244\n"
"Ta 138,14,2048\nv_mul_f32 v240, s57, v240\nMaaa 4,196,240\nv_mul_f32 v241, s57, v241\nCV 240,240,241\nMvaa 164,196,236\nTa 140,15,0\nv_mul_f32 v242, s57, v242\nMvaa 132,196,232\n"
"D0 3,20,45,1024\nv_mul_f32 v243, s57, v243\nMvaa 100,196,228\nTa 142,15,2048\nCV 241,242,243\nMvaa 68,196,224\nv_mul_f32 v244, s57, v244\nv_mul_f32 v245, s57, v245\n"
"Mvaa 72,200,224\nTa 144,16,0\nCV 242,244,245\nMvaa 104,200,228\nv_mul_f32 v246, s57, v246\nv_mul_f32 v247, s57, v247\nMvaa 136,200,232\nTa 146,16,2048\nCV 243,246,247\n"
"Mvaa 168,200,236\ns_add_u32 s6, s53, 4096\nSv 240,1,16,6,1024\nMaaa 8,200,240\nTa 148,17,0\nMaaa 40,200,244\nD0 4,20,45,2048\nMaaa 72,200,248\nTa 150,17,2048\nMaaa 104,200,252\n"
"Maaa 108,204,252\nTa 152,18,0\nMaaa 76,204,248\nMaaa 44,204,244\nTa 154,18,2048\nMaaa 12,204,240\nv_mul_f32 v248, s57, v248\nv_mul_f32 v249, s57, v249\nMvaa 172,204,236\n"
"Ta 156,19,0\nCV 248,248,249\nMvaa 140,204,232\nD0 5,20,45,3072\nv_mul_f32 v250, s57, v250\nMvaa 108,204,228\nTa 158,19,2048\nv_mul_f32 v251, s57, v251\nMvaa 76,204,224\n"
"CV 249,250,251\nv_mul_f32 v252, s57, v252\nMvaa 80,208,224\nRa 160,10,0\nv_mul_f32 v253, s57, v253\nMvaa 112,208,228\nCV 250,252,253\nv_mul_f32 v254, s57, v254\nMvaa 144,208,232\n"
"Ra 164,10,2048\nv_mul_f32 v255, s57, v255\nMvaa 176,208,236\ns_add_u32 m0, s48, 36864\ns_add_u32 s46, s45, 0x10000\nMaaa 16,208,240\nRa 168,10,8192\nCV 251,254,255\n"
"Maaa 48,208,244\nD0 2,20,46,0\nMaaa 80,208,248\nRa 172,10,10240\nMaaa 112,208,252\ns_add_u32 s6, s53, 4096\nSv 248,1,16,6,3072\nMaaa 116,212,252\nRa 176,10,16384\nMaaa 84,212,248\n"
"Maaa 52,212,244\nRa 180,10,18432\nMaaa 20,212,240\nMvaa 180,212,236\nRa 184,10,24576\nMvaa 148,212,232\nD0 3,20,46,1024\nMvaa 116,212,228\nRa 188,10,26624\nMvaa 84,212,224\n"
"Mvaa 88,216,224\nMvaa 120,216,228\nMvaa 152,216,232\nMvaa 184,216,236\nMaaa 24,216,240\nMaaa 56,216,244\nD0 4,20,46,2048\nMaaa 88,216,248\nMaaa 120,216,252\nMaaa 124,220,252\n"
"Maaa 92,220,248\nMaaa 60,220,244\nMaaa 28,220,240\ns_lshl3_add_u32 s90, s12, s13\ns_lshl2_add_u32 s90, s90, s11\ns_lshl2_add_u32 s90, s90, 0xc04a400\nMvaa 188,220,236\n"
"Mvaa 156,220,232\nD0 5,20,46,3072\nMvaa 124,220,228\nMvaa 92,220,224\ns_waitcnt vmcnt(17) lgkmcnt(0)\ns_barrier\ns_branch .MK2\n.V17:\nZaaa 0,176,128\nTa 192,12,16384\n"
"v_mul_f32 v80, s57, v80\nZaaa 4,180,128\ns_add_u32 m0, s48, 36864\ns_add_u32 s46, s45, 0x10000\ns_add_u32 s6, s56, 0\nSv 64,1,16,6,0\nZaaa 64,184,128\nTa 194,12,18432\n"
"v_mul_f32 v81, s57, v81\nZaaa 68,188,128\nD0 2,20,46,0\nCV 80,80,81\nZaaa 76,188,132\nTa 196,13,16384\nv_mul_f32 v82, s57, v82\nZaaa 72,184,132\nv_mul_f32 v83, s57, v83\n"
"CV 81,82,83\nZaaa 12,180,132\nTa 198,13,18432\nv_mul_f32 v84, s57, v84\nZaaa 8,176,132\nD0 3,20,46,1024\nv_mul_f32 v85, s57, v85\nZaaa 16,176,136\nTa 200,14,16384\nCV 82,84,85\n"
"Zaaa 20,180,136\nv_mul_f32 v86, s57, v86\nv_mul_f32 v87, s57, v87\ns_add_u32 s6, s56, 0\nSv 72,1,16,6,2048\nZaaa 80,184,136\nTa 202,14,18432\nCV 83,86,87\nZaaa 84,188,136\n"
"D0 4,20,46,2048\nv_mul_f32 v88, s57, v88\nZaaa 92,188,140\nTa 204,15,16384\nv_mul_f32 v89, s57, v89\nZaaa 88,184,140\nCV 88,88,89\nv_mul_f32 v90, s57, v90\nZaaa 28,180,140\n"
"Ta 206,15,18432\nv_mul_f32 v91, s57, v91\nZaaa 24,176,140\nD0 5,20,46,3072\nCV 89,90,91\nZaaa 32,176,144\nTa 208,16,16384\nv_mul_f32 v92, s57, v92\nZaaa 36,180,144\n"
"v_mul_f32 v93, s57, v93\nCV 90,92,93\nZaaa 96,184,144\nTa 210,16,18432\nv_mul_f32 v94, s57, v94\nZaaa 100,188,144\nv_mul_f32 v95, s57, v95\nCV 91,94,95\nZaaa 108,188,148\n"
"Ta 212,17,16384\nZaaa 104,184,148\nZaaa 44,180,148\nTa 214,17,18432\nZaaa 40,176,148\nZaaa 48,176,152\nTa 216,18,16384\nZaaa 52,180,152\ns_add_u32 s6, s56, 4096\nSv 80,1,16,6,0\n"
"Zaaa 112,184,152\nTa 218,18,18432\nZaaa 116,188,152\nZaaa 124,188,156\nTa 220,19,16384\nZaaa 120,184,156\ns_add_u32 s6, s56, 4096\nSv 88,1,16,6,2048\nZaaa 60,180,156\n"
"Ta 222,19,18432\nZaaa 56,176,156\ns_add_u32 m0, s47, 49152\ns_add_u32 s44, s44, 0x40000\nZvaa 64,160,128\nRa 224,11,0\nv_mul_f32 v32, s57, v32\nZvaa 68,164,128\nD0 6,24,44,0\n"
"v_mul_f32 v33, s57, v33\nZvaa 128,168,128\nRa 228,11,2048\nCV 32,32,33\nZvaa 132,172,128\nv_mul_f32 v34, s57, v34\nv_mul_f32 v35, s57, v35\nZvaa 140,172,132\nRa 232,11,8192\n"
"CV 33,34,35\nZvaa 136,168,132\nD0 7,24,44,1024\nv_mul_f32 v36, s57, v36\nZvaa 76,164,132\nRa 236,11,10240\nv_mul_f32 v37, s57, v37\nZvaa 72,160,132\nCV 34,36,37\n"
"v_mul_f32 v38, s57, v38\nZvaa 80,160,136\nRa 240,11,16384\nv_mul_f32 v39, s57, v39\nZvaa 84,164,136\nD0 8,24,44,2048\nCV 35,38,39\nZvaa 144,168,136\nRa 244,11,18432\n"
"Zvaa 148,172,136\ns_add_u32 s6, s56, 0\nSv 32,1,16,6,1024\nZvaa 156,172,140\nRa 248,11,24576\nZvaa 152,168,140\nD0 9,24,44,3072\nZvaa 92,164,140\nRa 252,11,26624\nZvaa 88,160,140\n"
"s_add_u32 m0, s47, 0\ns_add_u32 s44, s44, 0x40000\nZvaa 96,160,144\nZvaa 100,164,144\nD0 6,24,44,0\nv_mul_f32 v40, s57, v40\nZvaa 160,168,144\nv_mul_f32 v41, s57, v41\n"
"CV 40,40,41\nZvaa 164,172,144\nv_mul_f32 v42, s57, v42\nv_mul_f32 v43, s57, v43\nZvaa 172,172,148\nCV 41,42,43\nv_mul_f32 v44, s57, v44\nZvaa 168,168,148\nD0 7,24,44,1024\n"
"v_mul_f32 v45, s57, v45\nZvaa 108,164,148\nCV 42,44,45\nv_mul_f32 v46, s57, v46\nZvaa 104,160,148\nv_mul_f32 v47, s57, v47\nCV 43,46,47\nZvaa 112,160,152\ns_add_u32 s6, s56, 0\n"
"Sv 40,1,16,6,3072\nZvaa 116,164,152\nD0 8,24,44,2048\nZvaa 176,168,152\nZvaa 180,172,152\nZvaa 188,172,156\nZvaa 184,168,156\nD0 9,24,44,3072\nZvaa 124,164,156\nZvaa 120,160,156\n"
"s_waitcnt vmcnt(12) lgkmcnt(0)\ns_barrier\nMvaa 64,224,192\nTa 128,12,32768\nMvaa 68,228,192\nMvaa 128,232,192\nTa 130,12,34816\nv_mul_f32 v48, s57, v48\nMvaa 132,236,192\n"
"s_add_u32 m0, s48, 0\ns_add_u32 s45, s45, 128\nMaaa 0,240,192\nTa 132,13,32768\nv_mul_f32 v49, s57, v49\nMaaa 4,244,192\nD0 2,20,45,0\nCV 48,48,49\nMaaa 64,248,192\n"
"Ta 134,13,34816\nv_mul_f32 v50, s57, v50\nMaaa 68,252,192\nv_mul_f32 v51, s57, v51\nCV 49,50,51\nMaaa 76,252,196\nTa 136,14,32768\nv_mul_f32 v52, s57, v52\nMaaa 72,248,196\n"
"v_mul_f32 v53, s57, v53\nCV 50,52,53\nMaaa 12,244,196\nTa 138,14,34816\nv_mul_f32 v54, s57, v54\nMaaa 8,240,196\nv_mul_f32 v55, s57, v55\nCV 51,54,55\nMvaa 140,236,196\n"
"Ta 140,15,32768\nMvaa 136,232,196\nD0 3,20,45,1024\nMvaa 76,228,196\nTa 142,15,34816\nMvaa 72,224,196\ns_add_u32 s6, s56, 4096\nSv 48,1,16,6,1024\nMvaa 80,224,200\n"
"Ta 144,16,32768\nMvaa 84,228,200\nMvaa 144,232,200\nTa 146,16,34816\nMvaa 148,236,200\nv_mul_f32 v56, s57, v56\nv_mul_f32 v57, s57, v57\nMaaa 16,240,200\nTa 148,17,32768\n"
"CV 56,56,57\nMaaa 20,244,200\nD0 4,20,45,2048\nv_mul_f32 v58, s57, v58\nMaaa 80,248,200\nTa 150,17,34816\nv_mul_f32 v59, s57, v59\nMaaa 84,252,200\nCV 57,58,59\n"
"v_mul_f32 v60, s57, v60\nMaaa 92,252,204\nTa 152,18,32768\nv_mul_f32 v61, s57, v61\nMaaa 88,248,204\nCV 58,60,61\nv_mul_f32 v62, s57, v62\nMaaa 28,244,204\nTa 154,18,34816\n"
"v_mul_f32 v63, s57, v63\nMaaa 24,240,204\nCV 59,62,63\nMvaa 156,236,204\nTa 156,19,32768\nMvaa 152,232,204\nD0 5,20,45,3072\nMvaa 92,228,204\nTa 158,19,34816\nMvaa 88,224,204\n"
"s_add_u32 s6, s56, 4096\nSv 56,1,16,6,3072\nMvaa 96,224,208\nRa 160,10,32768\nMvaa 100,228,208\nMvaa 160,232,208\nRa 164,10,34816\nMvaa 164,236,208\ns_add_u32 m0, s48, 4096\n"
"s_add_u32 s46, s45, 0x10000\nMaaa 32,240,208\nRa 168,10,40960\nv_mul_f32 v192, s57, v192\nMaaa 36,244,208\nD0 2,20,46,0\nv_mul_f32 v193, s57, v193\nMaaa 96,248,208\n"
"Ra 172,10,43008\nCV 192,192,193\nMaaa 100,252,208\nv_mul_f32 v194, s57, v194\nv_mul_f32 v195, s57, v195\nMaaa 108,252,212\nRa 176,10,49152\nCV 193,194,195\nMaaa 104,248,212\n"
"v_mul_f32 v196, s57, v196\nv_mul_f32 v197, s57, v197\nMaaa 44,244,212\nRa 180,10,51200\nCV 194,196,197\nMaaa 40,240,212\nv_mul_f32 v198, s57, v198\nv_mul_f32 v199, s57, v199\n"
"Mvaa 172,236,212\nRa 184,10,57344\nCV 195,198,199\nMvaa 168,232,212\nD0 3,20,46,1024\nMvaa 108,228,212\nRa 188,10,59392\nMvaa 104,224,212\ns_add_u32 s6, s53, 0\nSv 192,1,16,6,0\n"
"Mvaa 112,224,216\nMvaa 116,228,216\nMvaa 176,232,216\nMvaa 180,236,216\nMaaa 48,240,216\nMaaa 52,244,216\nD0 4,20,46,2048\nv_mul_f32 v200, s57, v200\nMaaa 112,248,216\n"
"v_mul_f32 v201, s57, v201\nCV 200,200,201\nMaaa 116,252,216\nv_mul_f32 v202, s57, v202\nv_mul_f32 v203, s57, v203\nMaaa 124,252,220\nCV 201,202,203\nv_mul_f32 v204, s57, v204\n"
"Maaa 120,248,220\nv_mul_f32 v205, s57, v205\nCV 202,204,205\nMaaa 60,244,220\nv_mul_f32 v206, s57, v206\nv_mul_f32 v207, s57, v207\nMaaa 56,240,220\nCV 203,206,207\n"
"Mvaa 188,236,220\ns_add_u32 s6, s53, 0\nSv 200,1,16,6,2048\nMvaa 184,232,220\nD0 5,20,46,3072\nMvaa 124,228,220\nMvaa 120,224,220\ns_waitcnt vmcnt(17) lgkmcnt(0)\ns_barrier\n"
"Mvaa 64,160,128\nTa 192,12,49152\nMvaa 68,164,128\nMvaa 128,168,128\nTa 194,12,51200\nMvaa 132,172,128\ns_add_u32 m0, s47, 16384\ns_add_u32 s44, s44, 0x40000\nMaaa 0,176,128\n"
"Ta 196,13,49152\nMaaa 4,180,128\nD0 6,24,44,0\nMaaa 64,184,128\nTa 198,13,51200\nv_mul_f32 v208, s57, v208\nMaaa 68,188,128\nv_mul_f32 v209, s57, v209\nCV 208,208,209\n"
"Maaa 76,188,132\nTa 200,14,49152\nv_mul_f32 v210, s57, v210\nMaaa 72,184,132\nv_mul_f32 v211, s57, v211\nCV 209,210,211\nMaaa 12,180,132\nTa 202,14,51200\n"
"v_mul_f32 v212, s57, v212\nMaaa 8,176,132\nv_mul_f32 v213, s57, v213\nCV 210,212,213\nMvaa 140,172,132\nTa 204,15,49152\nv_mul_f32 v214, s57, v214\nMvaa 136,168,132\n"
"D0 7,24,44,1024\nv_mul_f32 v215, s57, v215\nMvaa 76,164,132\nTa 206,15,51200\nCV 211,214,215\nMvaa 72,160,132\ns_add_u32 s6, s53, 4096\nSv 208,1,16,6,0\nMvaa 80,160,136\n"
"Ta 208,16,49152\nMvaa 84,164,136\nMvaa 144,168,136\nTa 210,16,51200\nMvaa 148,172,136\nMaaa 16,176,136\nTa 212,17,49152\nMaaa 20,180,136\nD0 8,24,44,2048\nMaaa 80,184,136\n"
"Ta 214,17,51200\nMaaa 84,188,136\nv_mul_f32 v216, s57, v216\nv_mul_f32 v217, s57, v217\nMaaa 92,188,140\nTa 216,18,49152\nCV 216,216,217\nMaaa 88,184,140\n"
"v_mul_f32 v218, s57, v218\nv_mul_f32 v219, s57, v219\nMaaa 28,180,140\nTa 218,18,51200\nCV 217,218,219\nMaaa 24,176,140\nv_mul_f32 v220, s57, v220\nv_mul_f32 v221, s57, v221\n"
"Mvaa 156,172,140\nTa 220,19,49152\nCV 218,220,221\nMvaa 152,168,140\nD0 9,24,44,3072\nv_mul_f32 v222, s57, v222\nMvaa 92,164,140\nTa 222,19,51200\nv_mul_f32 v223, s57, v223\n"
"Mvaa 88,160,140\nCV 219,222,223\nMvaa 96,160,144\nRa 224,11,32768\nMvaa 100,164,144\ns_add_u32 s6, s53, 4096\nSv 216,1,16,6,2048\nMvaa 160,168,144\nRa 228,11,34816\n"
"Mvaa 164,172,144\ns_add_u32 m0, s47, 32768\ns_add_u32 s44, s44, 0x40000\nMaaa 32,176,144\nRa 232,11,40960\nMaaa 36,180,144\nD0 6,24,44,0\nMaaa 96,184,144\nRa 236,11,43008\n"
"Maaa 100,188,144\nMaaa 108,188,148\nRa 240,11,49152\nv_mul_f32 v224, s57, v224\nMaaa 104,184,148\nv_mul_f32 v225, s57, v225\nCV 224,224,225\nMaaa 44,180,148\nRa 244,11,51200\n"
"v_mul_f32 v226, s57, v226\nMaaa 40,176,148\nv_mul_f32 v227, s57, v227\nCV 225,226,227\nMvaa 172,172,148\nRa 248,11,57344\nv_mul_f32 v228, s57, v228\nMvaa 168,168,148\n"
"D0 7,24,44,1024\nv_mul_f32 v229, s57, v229\nMvaa 108,164,148\nRa 252,11,59392\nCV 226,228,229\nMvaa 104,160,148\nv_mul_f32 v230, s57, v230\nv_mul_f32 v231, s57, v231\n"
"Mvaa 112,160,152\nCV 227,230,231\nMvaa 116,164,152\ns_add_u32 s6, s53, 0\nSv 224,1,16,6,1024\nMvaa 176,168,152\nMvaa 180,172,152\nMaaa 48,176,152\nMaaa 52,180,152\n"
"D0 8,24,44,2048\nMaaa 112,184,152\nMaaa 116,188,152\nMaaa 124,188,156\nMaaa 120,184,156\nv_mul_f32 v232, s57, v232\nv_mul_f32 v233, s57, v233\nMaaa 60,180,156\nCV 232,232,233\n"
"v_mul_f32 v234, s57, v234\nMaaa 56,176,156\nv_mul_f32 v235, s57, v235\nCV 233,234,235\nMvaa 188,172,156\nv_mul_f32 v236, s57, v236\nv_mul_f32 v237, s57, v237\nMvaa 184,168,156\n"
"D0 9,24,44,3072\nCV 234,236,237\nMvaa 124,164,156\nv_mul_f32 v238, s57, v238\nv_mul_f32 v239, s57, v239\nMvaa 120,160,156\ns_waitcnt vmcnt(11) lgkmcnt(0)\ns_barrier\n"
"Mvaa 64,224,192\nTa 128,12,0\nCV 235,238,239\nMvaa 68,228,192\ns_add_u32 s6, s53, 0\nSv 232,1,16,6,3072\nMvaa 128,232,192\nTa 130,12,2048\nMvaa 132,236,192\n"
"s_add_u32 m0, s48, 32768\ns_add_u32 s45, s45, 128\nMaaa 0,240,192\nTa 132,13,0\nMaaa 4,244,192\nD0 2,20,45,0\nMaaa 64,248,192\nTa 134,13,2048\nMaaa 68,252,192\nMaaa 76,252,196\n"
"Ta 136,14,0\nMaaa 72,248,196\nMaaa 12,244,196\nTa 138,14,2048\nv_mul_f32 v240, s57, v240\nMaaa 8,240,196\nv_mul_f32 v241, s57, v241\nCV 240,240,241\nMvaa 140,236,196\nTa 140,15,0\n"
"v_mul_f32 v242, s57, v242\nMvaa 136,232,196\nD0 3,20,45,1024\nv_mul_f32 v243, s57, v243\nMvaa 76,228,196\nTa 142,15,2048\nCV 241,242,243\nMvaa 72,224,196\n"
"v_mul_f32 v244, s57, v244\nv_mul_f32 v245, s57, v245\nMvaa 80,224,200\nTa 144,16,0\nCV 242,244,245\nMvaa 84,228,200\nv_mul_f32 v246, s57, v246\nv_mul_f32 v247, s57, v247\n"
"Mvaa 144,232,200\nTa 146,16,2048\nCV 243,246,247\nMvaa 148,236,200\ns_add_u32 s6, s53, 4096\nSv 240,1,16,6,1024\nMaaa 16,240,200\nTa 148,17,0\nMaaa 20,244,200\nD0 4,20,45,2048\n"
"Maaa 80,248,200\nTa 150,17,2048\nMaaa 84,252,200\nMaaa 92,252,204\nTa 152,18,0\nMaaa 88,248,204\nMaaa 28,244,204\nTa 154,18,2048\nMaaa 24,240,204\nv_mul_f32 v248, s57, v248\n"
"v_mul_f32 v249, s57, v249\nMvaa 156,236,204\nTa 156,19,0\nCV 248,248,249\nMvaa 152,232,204\nD0 5,20,45,3072\nv_mul_f32 v250, s57, v250\nMvaa 92,228,204\nTa 158,19,2048\n"
"v_mul_f32 v251, s57, v251\nMvaa 88,224,204\nCV 249,250,251\nv_mul_f32 v252, s57, v252\nMvaa 96,224,208\nRa 160,10,0\nv_mul_f32 v253, s57, v253\nMvaa 100,228,208\nCV 250,252,253\n"
"v_mul_f32 v254, s57, v254\nMvaa 160,232,208\nRa 164,10,2048\nv_mul_f32 v255, s57, v255\nMvaa 164,236,208\ns_add_u32 m0, s48, 36864\ns_add_u32 s46, s45, 0x10000\nMaaa 32,240,208\n"
"Ra 168,10,8192\nCV 251,254,255\nMaaa 36,244,208\nD0 2,20,46,0\nMaaa 96,248,208\nRa 172,10,10240\nMaaa 100,252,208\ns_add_u32 s6, s53, 4096\nSv 248,1,16,6,3072\nMaaa 108,252,212\n"
"Ra 176,10,16384\nMaaa 104,248,212\nMaaa 44,244,212\nRa 180,10,18432\nMaaa 40,240,212\nMvaa 172,236,212\nRa 184,10,24576\nMvaa 168,232,212\nD0 3,20,46,1024\nMvaa 108,228,212\n"
"Ra 188,10,26624\nMvaa 104,224,212\nMvaa 112,224,216\nMvaa 116,228,216\nMvaa 176,232,216\nMvaa 180,236,216\nMaaa 48,240,216\nMaaa 52,244,216\nD0 4,20,46,2048\nMaaa 112,248,216\n"
"Maaa 116,252,216\ns_mov_b32 s93, 0x40000000\ns_mov_b32 s92, 0\nMaaa 124,252,220\nMaaa 120,248,220\ns_mov_b32 s94, 0xc048000\ns_mov_b32 s95, 0xc048000\nMaaa 60,244,220\n"
"Maaa 56,240,220\ns_lshl3_add_u32 s90, s12, s13\ns_lshl2_add_u32 s90, s90, s11\ns_lshl2_add_u32 s90, s90, 0xc04a400\nMvaa 188,236,220\nMvaa 184,232,220\nD0 5,20,46,3072\n"
"Mvaa 124,228,220\nMvaa 120,224,220\ns_waitcnt vmcnt(17) lgkmcnt(0)\ns_barrier\ns_branch .MV3\n.MK2:\nMvaa 64,128,160\nTa 192,12,16384\nMvaa 96,128,164\nMvaa 128,128,168\n"
"Ta 194,12,18432\nMvaa 160,128,172\ns_add_u32 m0, s47, 49152\ns_add_u32 s44, s44, 0x40000\nMaaa 0,128,176\nTa 196,13,16384\nMaaa 32,128,180\nD0 6,24,44,0\nMaaa 64,128,184\n"
"Ta 198,13,18432\nMaaa 96,128,188\nMaaa 100,132,188\nTa 200,14,16384\nMaaa 68,132,184\nMaaa 36,132,180\nTa 202,14,18432\nMaaa 4,132,176\nMvaa 164,132,172\nTa 204,15,16384\n"
"Mvaa 132,132,168\nD0 7,24,44,1024\nMvaa 100,132,164\nTa 206,15,18432\nMvaa 68,132,160\nMvaa 72,136,160\nTa 208,16,16384\nMvaa 104,136,164\nMvaa 136,136,168\nTa 210,16,18432\n"
"Mvaa 168,136,172\nMaaa 8,136,176\nTa 212,17,16384\nMaaa 40,136,180\nD0 8,24,44,2048\nMaaa 72,136,184\nTa 214,17,18432\nMaaa 104,136,188\nMaaa 108,140,188\nTa 216,18,16384\n"
"Maaa 76,140,184\nMaaa 44,140,180\nTa 218,18,18432\nMaaa 12,140,176\nMvaa 172,140,172\nTa 220,19,16384\nMvaa 140,140,168\nD0 9,24,44,3072\nMvaa 108,140,164\nTa 222,19,18432\n"
"Mvaa 76,140,160\nbuffer_load_dword v31, v24, s[24:27], s96 offen\ns_add_u32 s96, s96, 0x80000\nMvaa 80,144,160\nRa 224,11,0\nMvaa 112,144,164\nMvaa 144,144,168\nRa 228,11,2048\n"
"Mvaa 176,144,172\ns_add_u32 m0, s47, 0\ns_add_u32 s44, s44, 0x40000\nMaaa 16,144,176\nRa 232,11,8192\nMaaa 48,144,180\nD0 6,24,44,0\nMaaa 80,144,184\nRa 236,11,10240\n"
"Maaa 112,144,188\nMaaa 116,148,188\nRa 240,11,16384\nMaaa 84,148,184\nMaaa 52,148,180\nRa 244,11,18432\nMaaa 20,148,176\nMvaa 180,148,172\nRa 248,11,24576\nMvaa 148,148,168\n"
"D0 7,24,44,1024\nMvaa 116,148,164\nRa 252,11,26624\nMvaa 84,148,160\nMvaa 88,152,160\nMvaa 120,152,164\nMvaa 152,152,168\nMvaa 184,152,172\nMaaa 24,152,176\nMaaa 56,152,180\n"
"D0 8,24,44,2048\nMaaa 88,152,184\nMaaa 120,152,188\nMaaa 124,156,188\nMaaa 92,156,184\nMaaa 60,156,180\nMaaa 28,156,176\nMvaa 188,156,172\nMvaa 156,156,168\nD0 9,24,44,3072\n"
"Mvaa 124,156,164\nMvaa 92,156,160\ns_waitcnt vmcnt(9) lgkmcnt(0)\ns_barrier\nMvaa 64,192,224\nTa 128,12,32768\nMvaa 96,192,228\nMvaa 128,192,232\nTa 130,12,34816\n"
"Mvaa 160,192,236\ns_add_u32 m0, s48, 0\ns_add_u32 s45, s45, 128\nMaaa 0,192,240\nTa 132,13,32768\nMaaa 32,192,244\nD0 2,20,45,0\nMaaa 64,192,248\nTa 134,13,34816\nMaaa 96,192,252\n"
"Maaa 100,196,252\nTa 136,14,32768\nMaaa 68,196,248\nMaaa 36,196,244\nTa 138,14,34816\nMaaa 4,196,240\nMvaa 164,196,236\nTa 140,15,32768\nMvaa 132,196,232\nD0 3,20,45,1024\n"
"Mvaa 100,196,228\nTa 142,15,34816\nMvaa 68,196,224\nMvaa 72,200,224\nTa 144,16,32768\nMvaa 104,200,228\nMvaa 136,200,232\nTa 146,16,34816\nMvaa 168,200,236\nMaaa 8,200,240\n"
"Ta 148,17,32768\nMaaa 40,200,244\nD0 4,20,45,2048\nMaaa 72,200,248\nTa 150,17,34816\nMaaa 104,200,252\nMaaa 108,204,252\nTa 152,18,32768\nMaaa 76,204,248\nMaaa 44,204,244\n"
"Ta 154,18,34816\nMaaa 12,204,240\nMvaa 172,204,236\nTa 156,19,32768\nMvaa 140,204,232\nD0 5,20,45,3072\nMvaa 108,204,228\nTa 158,19,34816\nMvaa 76,204,224\nMvaa 80,208,224\n"
"Ra 160,10,32768\nMvaa 112,208,228\nMvaa 144,208,232\nRa 164,10,34816\nMvaa 176,208,236\ns_add_u32 m0, s48, 4096\ns_add_u32 s46, s45, 0x10000\nMaaa 16,208,240\nRa 168,10,40960\n"
"Maaa 48,208,244\nD0 2,20,46,0\nMaaa 80,208,248\nRa 172,10,43008\nMaaa 112,208,252\nMaaa 116,212,252\nRa 176,10,49152\nMaaa 84,212,248\nMaaa 52,212,244\nRa 180,10,51200\n"
"Maaa 20,212,240\nMvaa 180,212,236\nRa 184,10,57344\nMvaa 148,212,232\nD0 3,20,46,1024\nMvaa 116,212,228\nRa 188,10,59392\nMvaa 84,212,224\nMvaa 88,216,224\nMvaa 120,216,228\n"
"Mvaa 152,216,232\nMvaa 184,216,236\nMaaa 24,216,240\nMaaa 56,216,244\nD0 4,20,46,2048\nMaaa 88,216,248\nMaaa 120,216,252\nMaaa 124,220,252\nMaaa 92,220,248\nMaaa 60,220,244\n"
"Maaa 28,220,240\nMvaa 188,220,236\nMvaa 156,220,232\nD0 5,20,46,3072\nMvaa 124,220,228\nMvaa 92,220,224\ns_waitcnt vmcnt(13) lgkmcnt(0)\ns_barrier\nMvaa 64,128,160\n"
"Ta 192,12,49152\nMvaa 96,128,164\nMvaa 128,128,168\nTa 194,12,51200\nMvaa 160,128,172\ns_add_u32 m0, s47, 16384\ns_add_u32 s44, s44, 0x40000\nMaaa 0,128,176\nTa 196,13,49152\n"
"Maaa 32,128,180\nD0 6,24,44,0\nMaaa 64,128,184\nTa 198,13,51200\nMaaa 96,128,188\nMaaa 100,132,188\nTa 200,14,49152\nMaaa 68,132,184\nMaaa 36,132,180\nTa 202,14,51200\n"
"Maaa 4,132,176\nMvaa 164,132,172\nTa 204,15,49152\nMvaa 132,132,168\nD0 7,24,44,1024\nMvaa 100,132,164\nTa 206,15,51200\nMvaa 68,132,160\nMvaa 72,136,160\nTa 208,16,49152\n"
"Mvaa 104,136,164\nMvaa 136,136,168\nTa 210,16,51200\nMvaa 168,136,172\nMaaa 8,136,176\nTa 212,17,49152\nMaaa 40,136,180\nD0 8,24,44,2048\nMaaa 72,136,184\nTa 214,17,51200\n"
"Maaa 104,136,188\nMaaa 108,140,188\nTa 216,18,49152\nMaaa 76,140,184\nMaaa 44,140,180\nTa 218,18,51200\nMaaa 12,140,176\nMvaa 172,140,172\nTa 220,19,49152\nMvaa 140,140,168\n"
"D0 9,24,44,3072\nMvaa 108,140,164\nTa 222,19,51200\nMvaa 76,140,160\nbuffer_load_dword v31, v24, s[24:27], s96 offen\ns_add_u32 s96, s96, 0x80000\nMvaa 80,144,160\n"
"Ra 224,11,32768\nMvaa 112,144,164\nMvaa 144,144,168\nRa 228,11,34816\nMvaa 176,144,172\ns_add_u32 m0, s47, 32768\ns_add_u32 s44, s44, 0x40000\nMaaa 16,144,176\nRa 232,11,40960\n"
"Maaa 48,144,180\nD0 6,24,44,0\nMaaa 80,144,184\nRa 236,11,43008\nMaaa 112,144,188\nMaaa 116,148,188\nRa 240,11,49152\nMaaa 84,148,184\nMaaa 52,148,180\nRa 244,11,51200\n"
"Maaa 20,148,176\nMvaa 180,148,172\nRa 248,11,57344\nMvaa 148,148,168\nD0 7,24,44,1024\nMvaa 116,148,164\nRa 252,11,59392\nMvaa 84,148,160\nMvaa 88,152,160\nMvaa 120,152,164\n"
"Mvaa 152,152,168\nMvaa 184,152,172\nMaaa 24,152,176\nMaaa 56,152,180\nD0 8,24,44,2048\nMaaa 88,152,184\nMaaa 120,152,188\nMaaa 124,156,188\nMaaa 92,156,184\nMaaa 60,156,180\n"
"Maaa 28,156,176\nMvaa 188,156,172\nMvaa 156,156,168\nD0 9,24,44,3072\nMvaa 124,156,164\nMvaa 92,156,160\ns_waitcnt vmcnt(9) lgkmcnt(0)\ns_barrier\nMvaa 64,192,224\nTa 128,12,0\n"
"Mvaa 96,192,228\nMvaa 128,192,232\nTa 130,12,2048\nMvaa 160,192,236\ns_add_u32 m0, s48, 32768\ns_add_u32 s45, s45, 128\nMaaa 0,192,240\nTa 132,13,0\nMaaa 32,192,244\nD0 2,20,45,0\n"
"Maaa 64,192,248\nTa 134,13,2048\nMaaa 96,192,252\nMaaa 100,196,252\nTa 136,14,0\nMaaa 68,196,248\nMaaa 36,196,244\nTa 138,14,2048\nMaaa 4,196,240\nMvaa 164,196,236\nTa 140,15,0\n"
"Mvaa 132,196,232\nD0 3,20,45,1024\nMvaa 100,196,228\nTa 142,15,2048\nMvaa 68,196,224\nMvaa 72,200,224\nTa 144,16,0\nMvaa 104,200,228\nMvaa 136,200,232\nTa 146,16,2048\n"
"Mvaa 168,200,236\nMaaa 8,200,240\nTa 148,17,0\nMaaa 40,200,244\nD0 4,20,45,2048\nMaaa 72,200,248\nTa 150,17,2048\nMaaa 104,200,252\nMaaa 108,204,252\nTa 152,18,0\nMaaa 76,204,248\n"
"Maaa 44,204,244\nTa 154,18,2048\nMaaa 12,204,240\nMvaa 172,204,236\nTa 156,19,0\nMvaa 140,204,232\nD0 5,20,45,3072\nMvaa 108,204,228\nTa 158,19,2048\nMvaa 76,204,224\n"
"Mvaa 80,208,224\nRa 160,10,0\nMvaa 112,208,228\nMvaa 144,208,232\nRa 164,10,2048\nMvaa 176,208,236\ns_add_u32 m0, s48, 36864\ns_add_u32 s46, s45, 0x10000\nMaaa 16,208,240\n"
"Ra 168,10,8192\nMaaa 48,208,244\nD0 2,20,46,0\nMaaa 80,208,248\nRa 172,10,10240\nMaaa 112,208,252\nMaaa 116,212,252\nRa 176,10,16384\nMaaa 84,212,248\nMaaa 52,212,244\n"
"Ra 180,10,18432\nMaaa 20,212,240\nMvaa 180,212,236\nRa 184,10,24576\nMvaa 148,212,232\nD0 3,20,46,1024\nMvaa 116,212,228\nRa 188,10,26624\nMvaa 84,212,224\nMvaa 88,216,224\n"
"Mvaa 120,216,228\nMvaa 152,216,232\nMvaa 184,216,236\nMaaa 24,216,240\nMaaa 56,216,244\nD0 4,20,46,2048\nMaaa 88,216,248\nMaaa 120,216,252\nMaaa 124,220,252\nMaaa 92,220,248\n"
"Maaa 60,220,244\nMaaa 28,220,240\nMvaa 188,220,236\nMvaa 156,220,232\nD0 5,20,46,3072\nMvaa 124,220,228\nMvaa 92,220,224\ns_waitcnt vmcnt(13) lgkmcnt(0)\ns_barrier\n"
"Mvaa 64,128,160\nTa 192,12,16384\ns_and_b32 s2, s12, 1\nMvaa 96,128,164\ns_and_b32 s3, s13, 6\ns_add_u32 s2, s2, s3\nMvaa 128,128,168\nTa 194,12,18432\n"
"s_lshl3_add_u32 s2, s14, s2\nMvaa 160,128,172\ns_add_u32 m0, s47, 49152\ns_add_u32 s44, s44, 0x40000\nMaaa 0,128,176\nTa 196,13,16384\nMaaa 32,128,180\nD0 6,24,44,0\n"
"Maaa 64,128,184\nTa 198,13,18432\nMaaa 96,128,188\ns_cmp_lt_u32 s2, 14\ns_cselect_b32 s5, 0, 0x40000000\nMaaa 100,132,188\nTa 200,14,16384\ns_mul_i32 s4, s73, 14\nMaaa 68,132,184\n"
"s_add_u32 s4, s4, s2\ns_mov_b32 s36, s62\nMaaa 36,132,180\nTa 202,14,18432\ns_and_b32 s37, s63, 0xffff\nMaaa 4,132,176\ns_mov_b32 s38, 0x188000\ns_mov_b32 s39, 0x20000\n"
"Mvaa 164,132,172\nTa 204,15,16384\ns_mov_b32 s40, s64\nMvaa 132,132,168\nD0 7,24,44,1024\ns_and_b32 s41, s65, 0xffff\nMvaa 100,132,164\nTa 206,15,18432\ns_mov_b32 s42, 0x188000\n"
"Mvaa 68,132,160\ns_mov_b32 s43, 0x20000\ns_mov_b32 s92, s66\nMvaa 72,136,160\nTa 208,16,16384\ns_and_b32 s93, s67, 0xffff\nMvaa 104,136,164\ns_mov_b32 s94, 0x188000\n"
"s_mov_b32 s95, 0x20000\nMvaa 136,136,168\nTa 210,16,18432\nv_lshrrev_b32 v252, 1, v0\nMvaa 168,136,172\nv_mul_u32_u24 v253, 14336, v252\nv_and_b32 v254, 1, v0\nMaaa 8,136,176\n"
"Ta 212,17,16384\nv_lshl_add_u32 v253, v254, 4, v253\nMaaa 40,136,180\nD0 8,24,44,2048\nv_mov_b32 v254, 0x40000000\nMaaa 72,136,184\nTa 214,17,18432\nv_cmp_gt_u32 vcc, 56, v0\n"
"v_lshlrev_b32 v252, 3, v252\ns_mul_i32 s2, s11, 401408\nv_cndmask_b32 v253, v254, v253, vcc\nv_cndmask_b32 v255, v254, v252, vcc\nMaaa 104,136,188\ns_lshl_b32 s3, s4, 5\n"
"s_add_u32 s2, s2, s3\nMaaa 108,140,188\nTa 216,18,16384\ns_or_b32 s86, s2, s5\nMaaa 76,140,184\ns_add_u32 s87, s86, 7168\nv_lshrrev_b32 v251, 4, v0\nMaaa 44,140,180\n"
"Ta 218,18,18432\nv_mul_u32_u24 v252, 3584, v251\nMaaa 12,140,176\nv_and_b32 v251, 15, v0\nv_lshl_add_u32 v252, v251, 4, v252\nMvaa 172,140,172\nTa 220,19,16384\n"
"v_cmp_gt_u32 vcc, 14, v251\ns_mul_i32 s2, s4, 14336\ns_mul_i32 s3, s11, 224\nv_cndmask_b32 v252, v254, v252, vcc\nMvaa 140,140,168\nD0 9,24,44,3072\ns_add_u32 s2, s2, s3\n"
"Mvaa 108,140,164\nTa 222,19,18432\ns_or_b32 s88, s2, s5\nMvaa 76,140,160\nbuffer_load_dword v31, v24, s[24:27], s96 offen\ns_add_u32 s96, s96, 0x80000\nMvaa 80,144,160\n"
"Ra 224,11,0\nMvaa 112,144,164\nMvaa 144,144,168\nRa 228,11,2048\nMvaa 176,144,172\ns_add_u32 m0, s47, 0\ns_add_u32 s44, s44, 0x40000\nMaaa 16,144,176\nRa 232,11,8192\n"
"Maaa 48,144,180\nD0 6,24,44,0\nMaaa 80,144,184\nRa 236,11,10240\nMaaa 112,144,188\nMaaa 116,148,188\nRa 240,11,16384\nMaaa 84,148,184\nMaaa 52,148,180\nRa 244,11,18432\n"
"Maaa 20,148,176\nMvaa 180,148,172\nRa 248,11,24576\nMvaa 148,148,168\nD0 7,24,44,1024\nMvaa 116,148,164\nRa 252,11,26624\nMvaa 84,148,160\nMvaa 88,152,160\nMvaa 120,152,164\n"
"Mvaa 152,152,168\nMvaa 184,152,172\nMaaa 24,152,176\nMaaa 56,152,180\nD0 8,24,44,2048\nMaaa 88,152,184\nMaaa 120,152,188\nMaaa 124,156,188\nMaaa 92,156,184\nMaaa 60,156,180\n"
"Maaa 28,156,176\nMvaa 188,156,172\nMvaa 156,156,168\nD0 9,24,44,3072\nMvaa 124,156,164\nMvaa 92,156,160\ns_waitcnt vmcnt(9) lgkmcnt(0)\ns_barrier\nMvaa 64,192,224\n"
"Ta 128,12,32768\nMvaa 96,192,228\nbuffer_load_dwordx4 v[192:195], v253, s[36:39], s86 offen offset:0\nMvaa 128,192,232\nTa 130,12,34816\nMvaa 160,192,236\ns_add_u32 m0, s48, 0\n"
"s_add_u32 s45, s45, 128\nMaaa 0,192,240\nTa 132,13,32768\nMaaa 32,192,244\nD0 2,20,45,0\nMaaa 64,192,248\nTa 134,13,34816\nMaaa 96,192,252\nMaaa 100,196,252\nTa 136,14,32768\n"
"Maaa 68,196,248\nMaaa 36,196,244\nTa 138,14,34816\nMaaa 4,196,240\nMvaa 164,196,236\nTa 140,15,32768\nMvaa 132,196,232\nD0 3,20,45,1024\nMvaa 100,196,228\nTa 142,15,34816\n"
"Mvaa 68,196,224\nMvaa 72,200,224\nTa 144,16,32768\nMvaa 104,200,228\nMvaa 136,200,232\nTa 146,16,34816\nMvaa 168,200,236\nMaaa 8,200,240\nTa 148,17,32768\nMaaa 40,200,244\n"
"D0 4,20,45,2048\nMaaa 72,200,248\nTa 150,17,34816\nMaaa 104,200,252\nMaaa 108,204,252\nTa 152,18,32768\nMaaa 76,204,248\nMaaa 44,204,244\nTa 154,18,34816\nMaaa 12,204,240\n"
"Mvaa 172,204,236\nTa 156,19,32768\nMvaa 140,204,232\nD0 5,20,45,3072\nMvaa 108,204,228\nTa 158,19,34816\nMvaa 76,204,224\nMvaa 80,208,224\nRa 160,10,32768\nMvaa 112,208,228\n"
"Mvaa 144,208,232\nRa 164,10,34816\nMvaa 176,208,236\ns_add_u32 m0, s48, 4096\ns_add_u32 s46, s45, 0x10000\nMaaa 16,208,240\nRa 168,10,40960\nMaaa 48,208,244\nD0 2,20,46,0\n"
"Maaa 80,208,248\nRa 172,10,43008\nMaaa 112,208,252\nMaaa 116,212,252\nRa 176,10,49152\nMaaa 84,212,248\nMaaa 52,212,244\nRa 180,10,51200\nMaaa 20,212,240\nMvaa 180,212,236\n"
"Ra 184,10,57344\nMvaa 148,212,232\nD0 3,20,46,1024\nMvaa 116,212,228\nRa 188,10,59392\nMvaa 84,212,224\nMvaa 88,216,224\nMvaa 120,216,228\nMvaa 152,216,232\nMvaa 184,216,236\n"
"Maaa 24,216,240\nMaaa 56,216,244\nD0 4,20,46,2048\nMaaa 88,216,248\nMaaa 120,216,252\nMaaa 124,220,252\nMaaa 92,220,248\nMaaa 60,220,244\nMaaa 28,220,240\nMvaa 188,220,236\n"
"Mvaa 156,220,232\nD0 5,20,46,3072\nMvaa 124,220,228\nMvaa 92,220,224\ns_waitcnt vmcnt(14) lgkmcnt(0)\ns_barrier\nMvaa 64,128,160\nTa 192,12,49152\nMvaa 96,128,164\n"
"buffer_load_dwordx4 v[208:211], v253, s[40:43], s86 offen offset:0\nMvaa 128,128,168\nTa 194,12,51200\nMvaa 160,128,172\ns_add_u32 m0, s47, 16384\ns_add_u32 s44, s44, 0x40000\n"
"Maaa 0,128,176\nTa 196,13,49152\nMaaa 32,128,180\nD0 6,24,44,0\nMaaa 64,128,184\nTa 198,13,51200\nMaaa 96,128,188\nMaaa 100,132,188\nTa 200,14,49152\nMaaa 68,132,184\n"
"buffer_store_dword v25, off, s[16:19], s90 offset:0 sc0 sc1\nMaaa 36,132,180\nTa 202,14,51200\nMaaa 4,132,176\nMvaa 164,132,172\nTa 204,15,49152\nMvaa 132,132,168\n"
"D0 7,24,44,1024\nMvaa 100,132,164\nTa 206,15,51200\nMvaa 68,132,160\nMvaa 72,136,160\nTa 208,16,49152\nMvaa 104,136,164\nMvaa 136,136,168\nTa 210,16,51200\nMvaa 168,136,172\n"
"Maaa 8,136,176\nTa 212,17,49152\nMaaa 40,136,180\nD0 8,24,44,2048\nMaaa 72,136,184\nTa 214,17,51200\nMaaa 104,136,188\nMaaa 108,140,188\nTa 216,18,49152\nMaaa 76,140,184\n"
"Maaa 44,140,180\nTa 218,18,51200\nMaaa 12,140,176\nMvaa 172,140,172\nTa 220,19,49152\nMvaa 140,140,168\nD0 9,24,44,3072\nMvaa 108,140,164\nTa 222,19,51200\nMvaa 76,140,160\n"
"buffer_load_dword v31, v24, s[24:27], s96 offen\ns_add_u32 s96, s96, 0x80000\nMvaa 80,144,160\nRa 224,11,32768\nMvaa 112,144,164\nMvaa 144,144,168\nRa 228,11,34816\n"
"Mvaa 176,144,172\ns_add_u32 m0, s47, 32768\ns_add_u32 s44, s44, 0x40000\nMaaa 16,144,176\nRa 232,11,40960\nMaaa 48,144,180\nD0 6,24,44,0\nMaaa 80,144,184\nRa 236,11,43008\n"
"Maaa 112,144,188\nMaaa 116,148,188\nRa 240,11,49152\nMaaa 84,148,184\nMaaa 52,148,180\nRa 244,11,51200\nMaaa 20,148,176\nMvaa 180,148,172\nRa 248,11,57344\nMvaa 148,148,168\n"
"D0 7,24,44,1024\nMvaa 116,148,164\nRa 252,11,59392\nMvaa 84,148,160\nMvaa 88,152,160\nMvaa 120,152,164\nMvaa 152,152,168\nMvaa 184,152,172\nMaaa 24,152,176\nMaaa 56,152,180\n"
"D0 8,24,44,2048\nMaaa 88,152,184\nMaaa 120,152,188\nMaaa 124,156,188\nMaaa 92,156,184\nMaaa 60,156,180\nMaaa 28,156,176\nMvaa 188,156,172\nMvaa 156,156,168\nD0 9,24,44,3072\n"
"Mvaa 124,156,164\nMvaa 92,156,160\ns_waitcnt vmcnt(11) lgkmcnt(0)\ns_barrier\nMvaa 64,192,224\nTa 128,12,0\nMvaa 96,192,228\n"
"buffer_load_dwordx4 v[196:199], v253, s[36:39], s86 offen offset:3584\nMvaa 128,192,232\nTa 130,12,2048\nMvaa 160,192,236\ns_add_u32 m0, s48, 32768\ns_add_u32 s45, s45, 128\n"
"Maaa 0,192,240\nTa 132,13,0\nMaaa 32,192,244\nD0 2,20,45,0\nMaaa 64,192,248\nTa 134,13,2048\nMaaa 96,192,252\nMaaa 100,196,252\nTa 136,14,0\nMaaa 68,196,248\nMaaa 36,196,244\n"
"Ta 138,14,2048\nMaaa 4,196,240\nMvaa 164,196,236\nTa 140,15,0\nMvaa 132,196,232\nD0 3,20,45,1024\nMvaa 100,196,228\nTa 142,15,2048\nMvaa 68,196,224\nMvaa 72,200,224\nTa 144,16,0\n"
"Mvaa 104,200,228\nMvaa 136,200,232\nTa 146,16,2048\nMvaa 168,200,236\nMaaa 8,200,240\nTa 148,17,0\nMaaa 40,200,244\nD0 4,20,45,2048\nMaaa 72,200,248\nTa 150,17,2048\n"
"Maaa 104,200,252\nMaaa 108,204,252\nTa 152,18,0\nMaaa 76,204,248\nMaaa 44,204,244\nTa 154,18,2048\nMaaa 12,204,240\nMvaa 172,204,236\nTa 156,19,0\nMvaa 140,204,232\n"
"D0 5,20,45,3072\nMvaa 108,204,228\nTa 158,19,2048\nMvaa 76,204,224\nMvaa 80,208,224\nRa 160,10,0\nMvaa 112,208,228\nMvaa 144,208,232\nRa 164,10,2048\nMvaa 176,208,236\n"
"s_add_u32 m0, s48, 36864\ns_add_u32 s46, s45, 0x10000\nMaaa 16,208,240\nRa 168,10,8192\nMaaa 48,208,244\nD0 2,20,46,0\nMaaa 80,208,248\nRa 172,10,10240\nMaaa 112,208,252\n"
"Maaa 116,212,252\nRa 176,10,16384\nMaaa 84,212,248\nMaaa 52,212,244\nRa 180,10,18432\nMaaa 20,212,240\nMvaa 180,212,236\nRa 184,10,24576\nMvaa 148,212,232\nD0 3,20,46,1024\n"
"Mvaa 116,212,228\nRa 188,10,26624\nMvaa 84,212,224\nMvaa 88,216,224\nMvaa 120,216,228\nMvaa 152,216,232\nMvaa 184,216,236\nMaaa 24,216,240\nMaaa 56,216,244\nD0 4,20,46,2048\n"
"Maaa 88,216,248\nMaaa 120,216,252\nMaaa 124,220,252\nMaaa 92,220,248\nMaaa 60,220,244\nMaaa 28,220,240\nMvaa 188,220,236\nMvaa 156,220,232\nD0 5,20,46,3072\nMvaa 124,220,228\n"
"Mvaa 92,220,224\ns_waitcnt vmcnt(14) lgkmcnt(0)\ns_barrier\nMvaa 64,128,160\nTa 192,12,16384\nMvaa 96,128,164\n"
"buffer_load_dwordx4 v[212:215], v253, s[40:43], s86 offen offset:3584\nMvaa 128,128,168\nTa 194,12,18432\nMvaa 160,128,172\ns_add_u32 m0, s47, 49152\ns_add_u32 s44, s44, 0x40000\n"
"Maaa 0,128,176\nTa 196,13,16384\nMaaa 32,128,180\nD0 6,24,44,0\nMaaa 64,128,184\nTa 198,13,18432\nMaaa 96,128,188\nMaaa 100,132,188\nTa 200,14,16384\nMaaa 68,132,184\n"
"Maaa 36,132,180\nTa 202,14,18432\nMaaa 4,132,176\nMvaa 164,132,172\nTa 204,15,16384\nMvaa 132,132,168\nD0 7,24,44,1024\nMvaa 100,132,164\nTa 206,15,18432\nMvaa 68,132,160\n"
"Mvaa 72,136,160\nTa 208,16,16384\nMvaa 104,136,164\nMvaa 136,136,168\nTa 210,16,18432\nMvaa 168,136,172\nMaaa 8,136,176\nTa 212,17,16384\nMaaa 40,136,180\nD0 8,24,44,2048\n"
"Maaa 72,136,184\nTa 214,17,18432\nMaaa 104,136,188\nMaaa 108,140,188\nTa 216,18,16384\nMaaa 76,140,184\nMaaa 44,140,180\nTa 218,18,18432\nMaaa 12,140,176\nMvaa 172,140,172\n"
"Ta 220,19,16384\nMvaa 140,140,168\nD0 9,24,44,3072\nMvaa 108,140,164\nTa 222,19,18432\nMvaa 76,140,160\nbuffer_load_dword v31, v24, s[24:27], s96 offen\n"
"s_add_u32 s96, s96, 0x80000\nMvaa 80,144,160\nRa 224,11,0\nMvaa 112,144,164\nMvaa 144,144,168\nRa 228,11,2048\nMvaa 176,144,172\ns_add_u32 m0, s47, 0\ns_add_u32 s44, s44, 0x40000\n"
"Maaa 16,144,176\nRa 232,11,8192\nMaaa 48,144,180\nD0 6,24,44,0\nMaaa 80,144,184\nRa 236,11,10240\nMaaa 112,144,188\nMaaa 116,148,188\nRa 240,11,16384\nMaaa 84,148,184\n"
"Maaa 52,148,180\nRa 244,11,18432\nMaaa 20,148,176\nMvaa 180,148,172\nRa 248,11,24576\nMvaa 148,148,168\nD0 7,24,44,1024\nMvaa 116,148,164\nRa 252,11,26624\nMvaa 84,148,160\n"
"Mvaa 88,152,160\nMvaa 120,152,164\nMvaa 152,152,168\nMvaa 184,152,172\nMaaa 24,152,176\nMaaa 56,152,180\nD0 8,24,44,2048\nMaaa 88,152,184\nMaaa 120,152,188\nMaaa 124,156,188\n"
"Maaa 92,156,184\nMaaa 60,156,180\nMaaa 28,156,176\nMvaa 188,156,172\nMvaa 156,156,168\nD0 9,24,44,3072\nMvaa 124,156,164\nMvaa 92,156,160\ns_waitcnt vmcnt(10) lgkmcnt(0)\n"
"s_barrier\nMvaa 64,192,224\nTa 128,12,32768\nMvaa 96,192,228\nbuffer_load_dwordx4 v[200:203], v253, s[36:39], s87 offen offset:0\nMvaa 128,192,232\nTa 130,12,34816\n"
"Mvaa 160,192,236\ns_add_u32 m0, s48, 0\ns_add_u32 s45, s45, 128\nMaaa 0,192,240\nTa 132,13,32768\nMaaa 32,192,244\nD0 2,20,45,0\nMaaa 64,192,248\nTa 134,13,34816\nMaaa 96,192,252\n"
"s_lshl_b32 s6, s11, 12\ns_add_u32 s6, s6, 0xc044000\nMaaa 100,196,252\nTa 136,14,32768\nbuffer_load_dwordx4 v[32:35], v1, s[16:19], s6 offen offset:0 sc0 sc1\nMaaa 68,196,248\n"
"Maaa 36,196,244\nTa 138,14,34816\nMaaa 4,196,240\nMvaa 164,196,236\nTa 140,15,32768\nbuffer_load_dwordx4 v[36:39], v1, s[16:19], s6 offen offset:1024 sc0 sc1\nMvaa 132,196,232\n"
"D0 3,20,45,1024\nMvaa 100,196,228\nTa 142,15,34816\nMvaa 68,196,224\nMvaa 72,200,224\nTa 144,16,32768\nbuffer_load_dwordx4 v[40:43], v1, s[16:19], s6 offen offset:2048 sc0 sc1\n"
"Mvaa 104,200,228\nMvaa 136,200,232\nTa 146,16,34816\nMvaa 168,200,236\nMaaa 8,200,240\nTa 148,17,32768\nbuffer_load_dwordx4 v[44:47], v1, s[16:19], s6 offen offset:3072 sc0 sc1\n"
"Maaa 40,200,244\nD0 4,20,45,2048\nMaaa 72,200,248\nTa 150,17,34816\nMaaa 104,200,252\nMaaa 108,204,252\nTa 152,18,32768\nMaaa 76,204,248\nMaaa 44,204,244\nTa 154,18,34816\n"
"Maaa 12,204,240\nMvaa 172,204,236\nTa 156,19,32768\nMvaa 140,204,232\nD0 5,20,45,3072\nMvaa 108,204,228\nTa 158,19,34816\nMvaa 76,204,224\nMvaa 80,208,224\nRa 160,10,32768\n"
"Mvaa 112,208,228\nMvaa 144,208,232\nRa 164,10,34816\nMvaa 176,208,236\ns_add_u32 m0, s48, 4096\ns_add_u32 s46, s45, 0x10000\nMaaa 16,208,240\nRa 168,10,40960\nMaaa 48,208,244\n"
"D0 2,20,46,0\nMaaa 80,208,248\nRa 172,10,43008\nMaaa 112,208,252\nMaaa 116,212,252\nRa 176,10,49152\nMaaa 84,212,248\nMaaa 52,212,244\nRa 180,10,51200\nMaaa 20,212,240\n"
"Mvaa 180,212,236\nRa 184,10,57344\nMvaa 148,212,232\nD0 3,20,46,1024\nMvaa 116,212,228\nRa 188,10,59392\nMvaa 84,212,224\nMvaa 88,216,224\nMvaa 120,216,228\nMvaa 152,216,232\n"
"Mvaa 184,216,236\nMaaa 24,216,240\nMaaa 56,216,244\nD0 4,20,46,2048\nMaaa 88,216,248\nMaaa 120,216,252\nMaaa 124,220,252\nMaaa 92,220,248\nMaaa 60,220,244\nMaaa 28,220,240\n"
"Mvaa 188,220,236\nMvaa 156,220,232\nD0 5,20,46,3072\nMvaa 124,220,228\nMvaa 92,220,224\ns_waitcnt vmcnt(18) lgkmcnt(0)\ns_barrier\nMvaa 64,128,160\nTa 192,12,49152\n"
"Mvaa 96,128,164\nbuffer_load_dwordx4 v[216:219], v253, s[40:43], s87 offen offset:0\nMvaa 128,128,168\nTa 194,12,51200\nMvaa 160,128,172\ns_add_u32 m0, s47, 16384\n"
"s_add_u32 s44, s44, 0x40000\nMaaa 0,128,176\nTa 196,13,49152\nMaaa 32,128,180\nD0 6,24,44,0\nMaaa 64,128,184\nTa 198,13,51200\nMaaa 96,128,188\nMaaa 100,132,188\nTa 200,14,49152\n"
"Maaa 68,132,184\nMaaa 36,132,180\nTa 202,14,51200\nMaaa 4,132,176\nMvaa 164,132,172\nTa 204,15,49152\nMvaa 132,132,168\nD0 7,24,44,1024\nMvaa 100,132,164\nTa 206,15,51200\n"
"Mvaa 68,132,160\nMvaa 72,136,160\nTa 208,16,49152\nMvaa 104,136,164\nMvaa 136,136,168\nTa 210,16,51200\nMvaa 168,136,172\nMaaa 8,136,176\nTa 212,17,49152\nMaaa 40,136,180\n"
"D0 8,24,44,2048\nMaaa 72,136,184\nTa 214,17,51200\nMaaa 104,136,188\nMaaa 108,140,188\nTa 216,18,49152\nMaaa 76,140,184\nMaaa 44,140,180\nTa 218,18,51200\nMaaa 12,140,176\n"
"Mvaa 172,140,172\nTa 220,19,49152\nMvaa 140,140,168\nD0 9,24,44,3072\nMvaa 108,140,164\nTa 222,19,51200\nMvaa 76,140,160\nbuffer_load_dword v31, v24, s[24:27], s96 offen\n"
"s_add_u32 s96, s96, 0x80000\nMvaa 80,144,160\nRa 224,11,32768\nMvaa 112,144,164\nMvaa 144,144,168\nRa 228,11,34816\nMvaa 176,144,172\ns_add_u32 m0, s47, 32768\n"
"s_add_u32 s44, s44, 0x40000\nMaaa 16,144,176\nRa 232,11,40960\nMaaa 48,144,180\nD0 6,24,44,0\nMaaa 80,144,184\nRa 236,11,43008\nMaaa 112,144,188\nMaaa 116,148,188\n"
"Ra 240,11,49152\nMaaa 84,148,184\nMaaa 52,148,180\nRa 244,11,51200\nMaaa 20,148,176\nMvaa 180,148,172\nRa 248,11,57344\nMvaa 148,148,168\nD0 7,24,44,1024\nMvaa 116,148,164\n"
"Ra 252,11,59392\nMvaa 84,148,160\nMvaa 88,152,160\nMvaa 120,152,164\nMvaa 152,152,168\nMvaa 184,152,172\nMaaa 24,152,176\nMaaa 56,152,180\nD0 8,24,44,2048\nMaaa 88,152,184\n"
"Maaa 120,152,188\nMaaa 124,156,188\nMaaa 92,156,184\nMaaa 60,156,180\nMaaa 28,156,176\nMvaa 188,156,172\nMvaa 156,156,168\nD0 9,24,44,3072\nMvaa 124,156,164\nMvaa 92,156,160\n"
"s_waitcnt vmcnt(10) lgkmcnt(0)\ns_barrier\nMvaa 64,192,224\nTa 128,12,0\nMvaa 96,192,228\nbuffer_load_dwordx4 v[204:207], v253, s[36:39], s87 offen offset:3584\nMvaa 128,192,232\n"
"Ta 130,12,2048\nMvaa 160,192,236\ns_add_u32 m0, s48, 32768\ns_add_u32 s45, s45, 128\nMaaa 0,192,240\nTa 132,13,0\nMaaa 32,192,244\nD0 2,20,45,0\nMaaa 64,192,248\nTa 134,13,2048\n"
"Maaa 96,192,252\nMaaa 100,196,252\nTa 136,14,0\nMaaa 68,196,248\nMaaa 36,196,244\nTa 138,14,2048\nMaaa 4,196,240\nMvaa 164,196,236\nTa 140,15,0\nMvaa 132,196,232\nD0 3,20,45,1024\n"
"Mvaa 100,196,228\nTa 142,15,2048\nMvaa 68,196,224\nMvaa 72,200,224\nTa 144,16,0\nMvaa 104,200,228\nMvaa 136,200,232\nTa 146,16,2048\nMvaa 168,200,236\nMaaa 8,200,240\nTa 148,17,0\n"
"Maaa 40,200,244\nD0 4,20,45,2048\nMaaa 72,200,248\nTa 150,17,2048\nMaaa 104,200,252\nMaaa 108,204,252\nTa 152,18,0\nMaaa 76,204,248\nMaaa 44,204,244\nTa 154,18,2048\n"
"Maaa 12,204,240\nMvaa 172,204,236\nTa 156,19,0\nMvaa 140,204,232\nD0 5,20,45,3072\nMvaa 108,204,228\nTa 158,19,2048\nMvaa 76,204,224\nMvaa 80,208,224\nRa 160,10,0\n"
"Mvaa 112,208,228\nMvaa 144,208,232\nRa 164,10,2048\nMvaa 176,208,236\ns_add_u32 m0, s48, 36864\ns_add_u32 s46, s45, 0x10000\nMaaa 16,208,240\nRa 168,10,8192\nMaaa 48,208,244\n"
"D0 2,20,46,0\nMaaa 80,208,248\nRa 172,10,10240\nMaaa 112,208,252\nMaaa 116,212,252\nRa 176,10,16384\nMaaa 84,212,248\nMaaa 52,212,244\nRa 180,10,18432\nMaaa 20,212,240\n"
"Mvaa 180,212,236\nRa 184,10,24576\nMvaa 148,212,232\nD0 3,20,46,1024\nMvaa 116,212,228\nRa 188,10,26624\nMvaa 84,212,224\nMvaa 88,216,224\nMvaa 120,216,228\nMvaa 152,216,232\n"
"Mvaa 184,216,236\nMaaa 24,216,240\nMaaa 56,216,244\nD0 4,20,46,2048\nMaaa 88,216,248\nMaaa 120,216,252\nMaaa 124,220,252\nMaaa 92,220,248\nMaaa 60,220,244\nMaaa 28,220,240\n"
"Mvaa 188,220,236\nMvaa 156,220,232\nD0 5,20,46,3072\nMvaa 124,220,228\nMvaa 92,220,224\ns_waitcnt vmcnt(14) lgkmcnt(0)\ns_barrier\nMvaa 64,128,160\nTa 192,12,16384\n"
"v_xor_b32 v48, s15, v33\nMvaa 96,128,164\nbuffer_load_dwordx4 v[220:223], v253, s[40:43], s87 offen offset:3584\nv_xor_b32 v49, s15, v35\nMvaa 128,128,168\nTa 194,12,18432\n"
"v_xor_b32 v50, s15, v37\nMvaa 160,128,172\ns_add_u32 m0, s47, 49152\ns_add_u32 s44, s44, 0x40000\nMaaa 0,128,176\nTa 196,13,16384\nv_xor_b32 v51, s15, v39\nMaaa 32,128,180\n"
"D0 6,24,44,0\nv_xor_b32 v52, s15, v41\nMaaa 64,128,184\nTa 198,13,18432\nv_xor_b32 v53, s15, v43\nMaaa 96,128,188\nv_xor_b32 v54, s15, v45\nv_xor_b32 v55, s15, v47\n"
"Maaa 100,132,188\nTa 200,14,16384\nv_or3_b32 v48, v48, v49, v50\nMaaa 68,132,184\nv_or3_b32 v51, v51, v52, v53\nv_or3_b32 v48, v48, v54, v55\nMaaa 36,132,180\nTa 202,14,18432\n"
"v_or_b32 v48, v48, v51\nMaaa 4,132,176\nv_cmp_ne_u32 vcc, 0, v48\nMvaa 164,132,172\nTa 204,15,16384\ns_cbranch_vccnz .S29\n.S30:\nMvaa 132,132,168\nD0 7,24,44,1024\n"
"v_max_u32 v48, v32, v34\nMvaa 100,132,164\nTa 206,15,18432\nv_min_u32 v49, v32, v34\nMvaa 68,132,160\nv_med3_u32 v49, v48, v49, v36\nv_max_u32 v48, v48, v36\nMvaa 72,136,160\n"
"Ta 208,16,16384\nv_med3_u32 v49, v48, v49, v38\nMvaa 104,136,164\nv_max_u32 v48, v48, v38\nv_med3_u32 v49, v48, v49, v40\nMvaa 136,136,168\nTa 210,16,18432\n"
"v_max_u32 v48, v48, v40\nMvaa 168,136,172\nv_med3_u32 v49, v48, v49, v42\nv_max_u32 v48, v48, v42\nMaaa 8,136,176\nTa 212,17,16384\nv_med3_u32 v49, v48, v49, v44\nMaaa 40,136,180\n"
"D0 8,24,44,2048\nv_max_u32 v48, v48, v44\nMaaa 72,136,184\nTa 214,17,18432\nv_med3_u32 v49, v48, v49, v46\nMaaa 104,136,188\nv_max_u32 v48, v48, v46\nv_mov_b32 v50, v48\n"
"Maaa 108,140,188\nTa 216,18,16384\nMaaa 76,140,184\nv_max_u32_dpp v50, v50, v50 row_ror:8 row_mask:0xf bank_mask:0xf\nMaaa 44,140,180\nTa 218,18,18432\nMaaa 12,140,176\n"
"v_max_u32_dpp v50, v50, v50 row_ror:4 row_mask:0xf bank_mask:0xf\nMvaa 172,140,172\nTa 220,19,16384\nMvaa 140,140,168\nD0 9,24,44,3072\n"
"v_max_u32_dpp v50, v50, v50 row_ror:2 row_mask:0xf bank_mask:0xf\nMvaa 108,140,164\nTa 222,19,18432\nMvaa 76,140,160\nbuffer_load_dword v31, v24, s[24:27], s96 offen\n"
"s_add_u32 s96, s96, 0x80000\nMvaa 80,144,160\nRa 224,11,0\nv_max_u32_dpp v50, v50, v50 row_ror:1 row_mask:0xf bank_mask:0xf\nMvaa 112,144,164\nv_mov_b32 v52, v50\n"
"Mvaa 144,144,168\nRa 228,11,2048\nMvaa 176,144,172\ns_add_u32 m0, s47, 0\ns_add_u32 s44, s44, 0x40000\nMaaa 16,144,176\nRa 232,11,8192\nv_permlane16_swap_b32 v50, v52\n"
"Maaa 48,144,180\nD0 6,24,44,0\nv_max_u32 v50, v50, v52\nMaaa 80,144,184\nRa 236,11,10240\nv_mov_b32 v52, v50\nMaaa 112,144,188\nMaaa 116,148,188\nRa 240,11,16384\n"
"v_permlane32_swap_b32 v50, v52\nMaaa 84,148,184\nv_max_u32 v50, v50, v52\nv_cmp_eq_u32 vcc, v48, v50\ns_lshl_b32 s2, s11, 3\ns_add_u32 s2, s2, 131072\n"
"v_cndmask_b32 v51, v48, v49, vcc\nMaaa 52,148,180\nRa 244,11,18432\nMaaa 20,148,176\nv_max_u32_dpp v51, v51, v51 row_ror:8 row_mask:0xf bank_mask:0xf\nMvaa 180,148,172\n"
"Ra 248,11,24576\nMvaa 148,148,168\nD0 7,24,44,1024\nv_max_u32_dpp v51, v51, v51 row_ror:4 row_mask:0xf bank_mask:0xf\nMvaa 116,148,164\nRa 252,11,26624\nMvaa 84,148,160\n"
"v_max_u32_dpp v51, v51, v51 row_ror:2 row_mask:0xf bank_mask:0xf\nMvaa 88,152,160\nMvaa 120,152,164\nv_max_u32_dpp v51, v51, v51 row_ror:1 row_mask:0xf bank_mask:0xf\n"
"v_mov_b32 v52, v51\nMvaa 152,152,168\nMvaa 184,152,172\nv_permlane16_swap_b32 v51, v52\nv_max_u32 v51, v51, v52\nMaaa 24,152,176\nv_mov_b32 v52, v51\nMaaa 56,152,180\n"
"D0 8,24,44,2048\nMaaa 88,152,184\nv_permlane32_swap_b32 v51, v52\nv_max_u32 v51, v51, v52\nMaaa 120,152,188\nv_mov_b32 v52, s2\nds_write_b64 v52, v[50:51]\nMaaa 124,156,188\n"
"v_mov_b32 v52, 131072\nMaaa 92,156,184\nMaaa 60,156,180\nMaaa 28,156,176\nMvaa 188,156,172\nMvaa 156,156,168\nD0 9,24,44,3072\nMvaa 124,156,164\nMvaa 92,156,160\n"
"s_waitcnt vmcnt(10) lgkmcnt(1)\ns_barrier\nMvaa 64,192,224\nTa 128,12,32768\nMvaa 96,192,228\nbuffer_load_dwordx4 v[224:227], v252, s[92:95], s88 offen offset:0\nMvaa 128,192,232\n"
"Ta 130,12,34816\nMvaa 160,192,236\ns_add_u32 m0, s48, 0\ns_add_u32 s45, s45, 128\nMaaa 0,192,240\nTa 132,13,32768\nMaaa 32,192,244\nD0 2,20,45,0\nMaaa 64,192,248\nTa 134,13,34816\n"
"Maaa 96,192,252\nMaaa 100,196,252\nTa 136,14,32768\nMaaa 68,196,248\nMaaa 36,196,244\nTa 138,14,34816\nMaaa 4,196,240\nMvaa 164,196,236\nTa 140,15,32768\nMvaa 132,196,232\n"
"D0 3,20,45,1024\nMvaa 100,196,228\nTa 142,15,34816\nMvaa 68,196,224\nMvaa 72,200,224\nTa 144,16,32768\nMvaa 104,200,228\nMvaa 136,200,232\nTa 146,16,34816\nMvaa 168,200,236\n"
"Maaa 8,200,240\nTa 148,17,32768\nMaaa 40,200,244\nD0 4,20,45,2048\nMaaa 72,200,248\nTa 150,17,34816\nMaaa 104,200,252\nMaaa 108,204,252\nTa 152,18,32768\nMaaa 76,204,248\n"
"Maaa 44,204,244\nTa 154,18,34816\nMaaa 12,204,240\nMvaa 172,204,236\nTa 156,19,32768\nMvaa 140,204,232\nD0 5,20,45,3072\nMvaa 108,204,228\nTa 158,19,34816\nMvaa 76,204,224\n"
"Mvaa 80,208,224\nRa 160,10,32768\nMvaa 112,208,228\nbuffer_load_dwordx4 v[228:231], v252, s[92:95], s88 offen offset:896\nMvaa 144,208,232\nRa 164,10,34816\nMvaa 176,208,236\n"
"s_add_u32 m0, s48, 4096\ns_add_u32 s46, s45, 0x10000\nMaaa 16,208,240\nRa 168,10,40960\nMaaa 48,208,244\nD0 2,20,46,0\nMaaa 80,208,248\nRa 172,10,43008\nMaaa 112,208,252\n"
"Maaa 116,212,252\nRa 176,10,49152\nMaaa 84,212,248\nMaaa 52,212,244\nRa 180,10,51200\nMaaa 20,212,240\nMvaa 180,212,236\nRa 184,10,57344\nMvaa 148,212,232\nD0 3,20,46,1024\n"
"Mvaa 116,212,228\nRa 188,10,59392\nMvaa 84,212,224\nMvaa 88,216,224\nMvaa 120,216,228\nMvaa 152,216,232\nMvaa 184,216,236\nMaaa 24,216,240\nMaaa 56,216,244\nD0 4,20,46,2048\n"
"Maaa 88,216,248\nMaaa 120,216,252\nMaaa 124,220,252\nMaaa 92,220,248\nMaaa 60,220,244\nMaaa 28,220,240\nMvaa 188,220,236\nMvaa 156,220,232\nD0 5,20,46,3072\nMvaa 124,220,228\n"
"Mvaa 92,220,224\ns_waitcnt vmcnt(15) lgkmcnt(0)\ns_barrier\nMvaa 64,128,160\nTa 192,12,49152\nRv 32,52,0\nMvaa 96,128,164\n"
"buffer_load_dwordx4 v[232:235], v252, s[92:95], s88 offen offset:1792\nRv 36,52,16\nMvaa 128,128,168\nTa 194,12,51200\nMvaa 160,128,172\ns_add_u32 m0, s47, 16384\n"
"s_add_u32 s44, s44, 0x40000\nMaaa 0,128,176\nTa 196,13,49152\nMaaa 32,128,180\nD0 6,24,44,0\nMaaa 64,128,184\nTa 198,13,51200\nMaaa 96,128,188\nMaaa 100,132,188\nTa 200,14,49152\n"
"Maaa 68,132,184\nMaaa 36,132,180\nTa 202,14,51200\nMaaa 4,132,176\nMvaa 164,132,172\nTa 204,15,49152\nMvaa 132,132,168\nD0 7,24,44,1024\nWL 6\nMvaa 100,132,164\nTa 206,15,51200\n"
"v_max_u32 v48, v32, v33\nMvaa 68,132,160\nv_min_u32 v49, v32, v33\nv_med3_u32 v49, v48, v49, v34\nMvaa 72,136,160\nTa 208,16,49152\nv_max_u32 v48, v48, v34\nMvaa 104,136,164\n"
"v_med3_u32 v49, v48, v49, v35\nv_max_u32 v48, v48, v35\nMvaa 136,136,168\nTa 210,16,51200\nv_med3_u32 v49, v48, v49, v36\nMvaa 168,136,172\nv_max_u32 v48, v48, v36\n"
"v_med3_u32 v49, v48, v49, v37\nMaaa 8,136,176\nTa 212,17,49152\nv_max_u32 v48, v48, v37\nMaaa 40,136,180\nD0 8,24,44,2048\nv_med3_u32 v49, v48, v49, v38\nMaaa 72,136,184\n"
"Ta 214,17,51200\nv_max_u32 v48, v48, v38\nMaaa 104,136,188\nv_med3_u32 v49, v48, v49, v39\nv_max_u32 v48, v48, v39\nMaaa 108,140,188\nTa 216,18,49152\nv_readfirstlane_b32 s2, v48\n"
"Maaa 76,140,184\nv_readfirstlane_b32 s3, v49\nMaaa 44,140,180\nTa 218,18,51200\nMaaa 12,140,176\nMvaa 172,140,172\nTa 220,19,49152\ns_andn2_b32 s2, 0x7ff, s2\nMvaa 140,140,168\n"
"D0 9,24,44,3072\ns_andn2_b32 s3, 0x7ff, s3\nMvaa 108,140,164\nTa 222,19,51200\ns_lshl_b32 s71, s3, 16\nMvaa 76,140,160\nbuffer_load_dword v31, v24, s[24:27], s96 offen\n"
"s_add_u32 s96, s96, 0x80000\nMvaa 80,144,160\nRa 224,11,32768\ns_or_b32 s71, s71, s2\nMvaa 112,144,164\nbuffer_load_dwordx4 v[236:239], v252, s[92:95], s88 offen offset:2688\n"
"s_mul_i32 s2, s2, 896\nMvaa 144,144,168\nRa 228,11,34816\ns_mul_i32 s3, s3, 896\nMvaa 176,144,172\ns_add_u32 m0, s47, 32768\ns_add_u32 s44, s44, 0x40000\nMaaa 16,144,176\n"
"Ra 232,11,40960\ns_mul_i32 s6, s11, 224\nMaaa 48,144,180\nD0 6,24,44,0\ns_add_u32 s2, s2, s6\nMaaa 80,144,184\nRa 236,11,43008\ns_add_u32 s3, s3, s6\nMaaa 112,144,188\n"
"buffer_load_dwordx2 v[240:241], v255, s[28:31], s2 offen\nMaaa 116,148,188\nRa 240,11,49152\nMaaa 84,148,184\nMaaa 52,148,180\nRa 244,11,51200\nMaaa 20,148,176\n"
"buffer_load_dwordx2 v[242:243], v255, s[28:31], s3 offen\ns_lshl2_add_u32 s2, s10, s11\ns_cmp_eq_u32 s2, 0\ns_cselect_b32 s2, 0, 0x40000000\ns_add_u32 s2, s2, 0xc048000\n"
"Mvaa 180,148,172\nRa 248,11,57344\nMvaa 148,148,168\nD0 7,24,44,1024\nMvaa 116,148,164\nRa 252,11,59392\nMvaa 84,148,160\n"
"buffer_store_dword v25, off, s[16:19], s2 offset:0 sc0 sc1\nMvaa 88,152,160\nMvaa 120,152,164\nMvaa 152,152,168\nMvaa 184,152,172\nMaaa 24,152,176\nMaaa 56,152,180\n"
"D0 8,24,44,2048\nMaaa 88,152,184\nMaaa 120,152,188\nMaaa 124,156,188\nMaaa 92,156,184\nMaaa 60,156,180\nMaaa 28,156,176\nMvaa 188,156,172\nMvaa 156,156,168\nD0 9,24,44,3072\n"
"Mvaa 124,156,164\nMvaa 92,156,160\ns_waitcnt vmcnt(14) lgkmcnt(0)\ns_barrier\nMvaa 64,192,224\nTa 128,12,0\nMvaa 96,192,228\nMvaa 128,192,232\nTa 130,12,2048\nMvaa 160,192,236\n"
"s_add_u32 m0, s48, 32768\ns_add_u32 s45, s45, 128\nMaaa 0,192,240\nTa 132,13,0\nMaaa 32,192,244\nD0 2,20,45,0\nMaaa 64,192,248\nTa 134,13,2048\nMaaa 96,192,252\nMaaa 100,196,252\n"
"Ta 136,14,0\nMaaa 68,196,248\nMaaa 36,196,244\nTa 138,14,2048\nMaaa 4,196,240\nMvaa 164,196,236\nTa 140,15,0\nMvaa 132,196,232\nD0 3,20,45,1024\nMvaa 100,196,228\nTa 142,15,2048\n"
"Mvaa 68,196,224\nMvaa 72,200,224\nTa 144,16,0\nMvaa 104,200,228\nMvaa 136,200,232\nTa 146,16,2048\nMvaa 168,200,236\nMaaa 8,200,240\nTa 148,17,0\nMaaa 40,200,244\nD0 4,20,45,2048\n"
"Maaa 72,200,248\nTa 150,17,2048\nMaaa 104,200,252\nMaaa 108,204,252\nTa 152,18,0\nMaaa 76,204,248\nMaaa 44,204,244\nTa 154,18,2048\nMaaa 12,204,240\nMvaa 172,204,236\nTa 156,19,0\n"
"Mvaa 140,204,232\nD0 5,20,45,3072\nMvaa 108,204,228\nTa 158,19,2048\nMvaa 76,204,224\nMvaa 80,208,224\nRa 160,10,0\nMvaa 112,208,228\nMvaa 144,208,232\nRa 164,10,2048\n"
"Mvaa 176,208,236\ns_add_u32 m0, s48, 36864\ns_add_u32 s46, s45, 0x10000\nMaaa 16,208,240\nRa 168,10,8192\nMaaa 48,208,244\nD0 2,20,46,0\nMaaa 80,208,248\nRa 172,10,10240\n"
"Maaa 112,208,252\nMaaa 116,212,252\nRa 176,10,16384\nMaaa 84,212,248\nMaaa 52,212,244\nRa 180,10,18432\nMaaa 20,212,240\nMvaa 180,212,236\nRa 184,10,24576\nMvaa 148,212,232\n"
"D0 3,20,46,1024\nMvaa 116,212,228\nRa 188,10,26624\nMvaa 84,212,224\nMvaa 88,216,224\nMvaa 120,216,228\nMvaa 152,216,232\nMvaa 184,216,236\nMaaa 24,216,240\nMaaa 56,216,244\n"
"D0 4,20,46,2048\nMaaa 88,216,248\nMaaa 120,216,252\nMaaa 124,220,252\nMaaa 92,220,248\nMaaa 60,220,244\nMaaa 28,220,240\nMvaa 188,220,236\nMvaa 156,220,232\nD0 5,20,46,3072\n"
"Mvaa 124,220,228\nMvaa 92,220,224\ns_waitcnt vmcnt(17) lgkmcnt(0)\ns_barrier\nMvaa 64,128,160\nTa 192,12,16384\nMvaa 96,128,164\nMvaa 128,128,168\nTa 194,12,18432\n"
"Mvaa 160,128,172\ns_add_u32 m0, s47, 49152\ns_add_u32 s44, s44, 0x40000\nMaaa 0,128,176\nTa 196,13,16384\nMaaa 32,128,180\nD0 6,24,44,0\nMaaa 64,128,184\nTa 198,13,18432\n"
"Maaa 96,128,188\nMaaa 100,132,188\nTa 200,14,16384\nMaaa 68,132,184\nMaaa 36,132,180\nTa 202,14,18432\nMaaa 4,132,176\nMvaa 164,132,172\nTa 204,15,16384\nMvaa 132,132,168\n"
"D0 7,24,44,1024\nMvaa 100,132,164\nTa 206,15,18432\nMvaa 68,132,160\nMvaa 72,136,160\nTa 208,16,16384\nMvaa 104,136,164\nMvaa 136,136,168\nTa 210,16,18432\nMvaa 168,136,172\n"
"Maaa 8,136,176\nTa 212,17,16384\nMaaa 40,136,180\nD0 8,24,44,2048\nMaaa 72,136,184\nTa 214,17,18432\nMaaa 104,136,188\nMaaa 108,140,188\nTa 216,18,16384\nMaaa 76,140,184\n"
"Maaa 44,140,180\nTa 218,18,18432\nMaaa 12,140,176\nMvaa 172,140,172\nTa 220,19,16384\nMvaa 140,140,168\nD0 9,24,44,3072\nMvaa 108,140,164\nTa 222,19,18432\nMvaa 76,140,160\n"
"buffer_load_dword v31, v24, s[24:27], s96 offen\ns_add_u32 s96, s96, 0x80000\nMvaa 80,144,160\nRa 224,11,0\nMvaa 112,144,164\ns_lshl_b32 s5, s73, 7\ns_add_u32 s5, s5, 0xc04ac00\n"
"v_bfe_u32 v27, v1, 2, 7\nbuffer_load_dword v27, v27, s[16:19], s5 offen sc0 sc1\nMvaa 144,144,168\nRa 228,11,2048\nMvaa 176,144,172\ns_add_u32 m0, s47, 0\n"
"s_add_u32 s44, s44, 0x40000\nMaaa 16,144,176\nRa 232,11,8192\nMaaa 48,144,180\nD0 6,24,44,0\nMaaa 80,144,184\nRa 236,11,10240\nMaaa 112,144,188\nMaaa 116,148,188\nRa 240,11,16384\n"
"Maaa 84,148,184\nMaaa 52,148,180\nRa 244,11,18432\nMaaa 20,148,176\nMvaa 180,148,172\nRa 248,11,24576\nMvaa 148,148,168\nD0 7,24,44,1024\nMvaa 116,148,164\nRa 252,11,26624\n"
"Mvaa 84,148,160\nMvaa 88,152,160\nMvaa 120,152,164\nMvaa 152,152,168\nMvaa 184,152,172\nMaaa 24,152,176\nMaaa 56,152,180\nD0 8,24,44,2048\nMaaa 88,152,184\nMaaa 120,152,188\n"
"Maaa 124,156,188\nMaaa 92,156,184\nMaaa 60,156,180\nMaaa 28,156,176\nMvaa 188,156,172\nMvaa 156,156,168\nD0 9,24,44,3072\nMvaa 124,156,164\nMvaa 92,156,160\n"
"s_waitcnt vmcnt(10) lgkmcnt(0)\ns_barrier\nMvaa 64,192,224\nTa 128,12,32768\nMvaa 96,192,228\nv_lshlrev_b32 v244, 16, v240\nv_and_b32 v245, 0xffff0000, v240\nMvaa 128,192,232\n"
"Ta 130,12,34816\nv_lshlrev_b32 v246, 16, v241\nMvaa 160,192,236\ns_add_u32 m0, s48, 0\ns_add_u32 s45, s45, 128\nMaaa 0,192,240\nTa 132,13,32768\nv_and_b32 v247, 0xffff0000, v241\n"
"Maaa 32,192,244\nD0 2,20,45,0\nv_lshlrev_b32 v248, 16, v242\nMaaa 64,192,248\nTa 134,13,34816\nv_and_b32 v249, 0xffff0000, v242\nMaaa 96,192,252\nv_lshlrev_b32 v250, 16, v243\n"
"v_and_b32 v251, 0xffff0000, v243\nMaaa 100,196,252\nTa 136,14,32768\nv_lshlrev_b32 v252, 16, v192\nMaaa 68,196,248\nv_and_b32 v253, 0xffff0000, v192\nMU 32,244,252\n"
"Maaa 36,196,244\nTa 138,14,34816\nMU 33,244,253\nMaaa 4,196,240\nMU 40,248,252\nMU 41,248,253\nMvaa 164,196,236\nTa 140,15,32768\nv_lshlrev_b32 v252, 16, v193\nMvaa 132,196,232\n"
"D0 3,20,45,1024\nv_and_b32 v253, 0xffff0000, v193\nMvaa 100,196,228\nTa 142,15,34816\nMU 34,244,252\nMvaa 68,196,224\nMU 35,244,253\nMU 42,248,252\nMvaa 72,200,224\n"
"Ta 144,16,32768\nMU 43,248,253\nMvaa 104,200,228\nv_lshlrev_b32 v252, 16, v194\nv_and_b32 v253, 0xffff0000, v194\nMvaa 136,200,232\nTa 146,16,34816\nMU 36,244,252\n"
"Mvaa 168,200,236\nMU 37,244,253\nMU 44,248,252\nMaaa 8,200,240\nTa 148,17,32768\nMU 45,248,253\nMaaa 40,200,244\nD0 4,20,45,2048\nv_lshlrev_b32 v252, 16, v195\nMaaa 72,200,248\n"
"Ta 150,17,34816\nv_and_b32 v253, 0xffff0000, v195\nMaaa 104,200,252\nMU 38,244,252\nMU 39,244,253\nMaaa 108,204,252\nTa 152,18,32768\nMU 46,248,252\nMaaa 76,204,248\n"
"MU 47,248,253\nv_lshlrev_b32 v252, 16, v208\nMaaa 44,204,244\nTa 154,18,34816\nv_and_b32 v253, 0xffff0000, v208\nMaaa 12,204,240\nMU 48,244,252\nMU 49,244,253\nMvaa 172,204,236\n"
"Ta 156,19,32768\nMU 56,248,252\nMvaa 140,204,232\nD0 5,20,45,3072\nMU 57,248,253\nMvaa 108,204,228\nTa 158,19,34816\nv_lshlrev_b32 v252, 16, v209\nMvaa 76,204,224\n"
"v_and_b32 v253, 0xffff0000, v209\nMU 50,244,252\nMvaa 80,208,224\nRa 160,10,32768\nMU 51,244,253\nMvaa 112,208,228\nMU 58,248,252\nMU 59,248,253\nMvaa 144,208,232\n"
"Ra 164,10,34816\nv_lshlrev_b32 v252, 16, v210\nMvaa 176,208,236\ns_add_u32 m0, s48, 4096\ns_add_u32 s46, s45, 0x10000\nMaaa 16,208,240\nRa 168,10,40960\n"
"v_and_b32 v253, 0xffff0000, v210\nMaaa 48,208,244\nD0 2,20,46,0\nMU 52,244,252\nMaaa 80,208,248\nRa 172,10,43008\nMU 53,244,253\nMaaa 112,208,252\nMU 60,248,252\nMU 61,248,253\n"
"Maaa 116,212,252\nRa 176,10,49152\nv_lshlrev_b32 v252, 16, v211\nMaaa 84,212,248\nv_and_b32 v253, 0xffff0000, v211\nMU 54,244,252\nMaaa 52,212,244\nRa 180,10,51200\nMU 55,244,253\n"
"Maaa 20,212,240\nMU 62,248,252\nMU 63,248,253\nMvaa 180,212,236\nRa 184,10,57344\nv_lshlrev_b32 v252, 16, v196\nMvaa 148,212,232\nD0 3,20,46,1024\n"
"v_and_b32 v253, 0xffff0000, v196\nMvaa 116,212,228\nRa 188,10,59392\nFM 32,245,252\nMvaa 84,212,224\nFM 33,245,253\nFM 40,249,252\nMvaa 88,216,224\nFM 41,249,253\n"
"v_lshlrev_b32 v252, 16, v197\nMvaa 120,216,228\nv_and_b32 v253, 0xffff0000, v197\nFM 34,245,252\nMvaa 152,216,232\nFM 35,245,253\nFM 42,249,252\nMvaa 184,216,236\nFM 43,249,253\n"
"v_lshlrev_b32 v252, 16, v198\nMaaa 24,216,240\nv_and_b32 v253, 0xffff0000, v198\nFM 36,245,252\nMaaa 56,216,244\nD0 4,20,46,2048\nFM 37,245,253\nMaaa 88,216,248\nFM 44,249,252\n"
"FM 45,249,253\nMaaa 120,216,252\nv_lshlrev_b32 v252, 16, v199\nv_and_b32 v253, 0xffff0000, v199\nMaaa 124,220,252\nFM 38,245,252\nFM 39,245,253\nMaaa 92,220,248\nFM 46,249,252\n"
"FM 47,249,253\nMaaa 60,220,244\nv_lshlrev_b32 v252, 16, v212\nv_and_b32 v253, 0xffff0000, v212\nMaaa 28,220,240\nFM 48,245,252\nFM 49,245,253\nMvaa 188,220,236\nFM 56,249,252\n"
"FM 57,249,253\nMvaa 156,220,232\nD0 5,20,46,3072\nv_lshlrev_b32 v252, 16, v213\nMvaa 124,220,228\nv_and_b32 v253, 0xffff0000, v213\nFM 50,245,252\nMvaa 92,220,224\n"
"s_waitcnt vmcnt(14) lgkmcnt(0)\ns_barrier\nMvaa 64,128,160\nTa 192,12,49152\nFM 51,245,253\nMvaa 96,128,164\nFM 58,249,252\nFM 59,249,253\nMvaa 128,128,168\nTa 194,12,51200\n"
"v_lshlrev_b32 v252, 16, v214\nMvaa 160,128,172\ns_add_u32 m0, s47, 16384\ns_add_u32 s44, s44, 0x40000\nMaaa 0,128,176\nTa 196,13,49152\nv_and_b32 v253, 0xffff0000, v214\n"
"Maaa 32,128,180\nD0 6,24,44,0\nFM 52,245,252\nMaaa 64,128,184\nTa 198,13,51200\nFM 53,245,253\nMaaa 96,128,188\nFM 60,249,252\nFM 61,249,253\nMaaa 100,132,188\nTa 200,14,49152\n"
"v_lshlrev_b32 v252, 16, v215\nMaaa 68,132,184\nv_and_b32 v253, 0xffff0000, v215\nFM 54,245,252\nMaaa 36,132,180\nTa 202,14,51200\nFM 55,245,253\nMaaa 4,132,176\nFM 62,249,252\n"
"FM 63,249,253\nMvaa 164,132,172\nTa 204,15,49152\nv_lshlrev_b32 v252, 16, v200\nMvaa 132,132,168\nD0 7,24,44,1024\nv_and_b32 v253, 0xffff0000, v200\nMvaa 100,132,164\n"
"Ta 206,15,51200\nFM 32,246,252\nMvaa 68,132,160\nFM 33,246,253\nFM 40,250,252\nMvaa 72,136,160\nTa 208,16,49152\nFM 41,250,253\nMvaa 104,136,164\nv_lshlrev_b32 v252, 16, v201\n"
"v_and_b32 v253, 0xffff0000, v201\nMvaa 136,136,168\nTa 210,16,51200\nFM 34,246,252\nMvaa 168,136,172\nFM 35,246,253\nFM 42,250,252\nMaaa 8,136,176\nTa 212,17,49152\nFM 43,250,253\n"
"Maaa 40,136,180\nD0 8,24,44,2048\nv_lshlrev_b32 v252, 16, v202\nMaaa 72,136,184\nTa 214,17,51200\nv_and_b32 v253, 0xffff0000, v202\nMaaa 104,136,188\nFM 36,246,252\nFM 37,246,253\n"
"Maaa 108,140,188\nTa 216,18,49152\nFM 44,250,252\nMaaa 76,140,184\nFM 45,250,253\nv_lshlrev_b32 v252, 16, v203\nMaaa 44,140,180\nTa 218,18,51200\nv_and_b32 v253, 0xffff0000, v203\n"
"Maaa 12,140,176\nFM 38,246,252\nFM 39,246,253\nMvaa 172,140,172\nTa 220,19,49152\nFM 46,250,252\nMvaa 140,140,168\nD0 9,24,44,3072\nFM 47,250,253\nMvaa 108,140,164\n"
"Ta 222,19,51200\nv_lshlrev_b32 v252, 16, v216\nMvaa 76,140,160\nbuffer_load_dword v31, v24, s[24:27], s96 offen\ns_add_u32 s96, s96, 0x80000\nMvaa 80,144,160\nRa 224,11,32768\n"
"v_and_b32 v253, 0xffff0000, v216\nMvaa 112,144,164\nFM 48,246,252\nFM 49,246,253\nMvaa 144,144,168\nRa 228,11,34816\nFM 56,250,252\nMvaa 176,144,172\ns_add_u32 m0, s47, 32768\n"
"s_add_u32 s44, s44, 0x40000\nMaaa 16,144,176\nRa 232,11,40960\nFM 57,250,253\nMaaa 48,144,180\nD0 6,24,44,0\nv_lshlrev_b32 v252, 16, v217\nMaaa 80,144,184\nRa 236,11,43008\n"
"v_and_b32 v253, 0xffff0000, v217\nMaaa 112,144,188\nFM 50,246,252\nFM 51,246,253\nMaaa 116,148,188\nRa 240,11,49152\nFM 58,250,252\nMaaa 84,148,184\nFM 59,250,253\n"
"v_lshlrev_b32 v252, 16, v218\nMaaa 52,148,180\nRa 244,11,51200\nv_and_b32 v253, 0xffff0000, v218\nMaaa 20,148,176\nFM 52,246,252\nFM 53,246,253\nMvaa 180,148,172\nRa 248,11,57344\n"
"FM 60,250,252\nMvaa 148,148,168\nD0 7,24,44,1024\nFM 61,250,253\nMvaa 116,148,164\nRa 252,11,59392\nv_lshlrev_b32 v252, 16, v219\nMvaa 84,148,160\n"
"v_and_b32 v253, 0xffff0000, v219\nFM 54,246,252\nMvaa 88,152,160\nWV 19\nv_xor_b32 v27, s15, v27\nv_and_b32 v255, 0xffffdfff, v27\nv_cmp_ne_u32 vcc, 0, v255\ns_nop 0\n"
"s_cbranch_vccnz .S32\n.S33:\nv_cmp_ne_u32 vcc, 0, v27\ns_nop 3\ns_bcnt1_i32_b64 s4, vcc\ns_cmp_gt_u32 s4, 32\ns_cselect_b32 s4, 1, 0\nMvaa 120,152,164\ns_xor_b32 s4, s4, s12\n"
"s_and_b32 s4, s4, 1\nMvaa 152,152,168\ns_lshl_b32 s4, s4, 13\ns_mul_i32 s72, s73, 25165824\nMvaa 184,152,172\ns_add_u32 s72, s72, s4\ns_and_b32 s4, s12, 1\nMaaa 24,152,176\n"
"s_lshl_b32 s4, s4, 21\ns_add_u32 s72, s72, s4\nMaaa 56,152,180\nD0 8,24,44,2048\ns_lshl3_add_u32 s2, s13, s11\nMaaa 88,152,184\ns_lshl_b32 s2, s2, 11\ns_lshl_b32 s3, s14, 22\n"
"Maaa 120,152,188\ns_add_u32 s2, s2, s3\ns_add_u32 s2, s2, s72\nMaaa 124,156,188\ns_add_u32 s49, s2, 0xc00000\nFM 55,246,253\nMaaa 92,156,184\nFM 62,250,252\nFM 63,250,253\n"
"Maaa 60,156,180\nv_lshlrev_b32 v252, 16, v204\nv_and_b32 v253, 0xffff0000, v204\nMaaa 28,156,176\nFM 32,247,252\nFM 33,247,253\nMvaa 188,156,172\nFM 40,251,252\nFM 41,251,253\n"
"Mvaa 156,156,168\nD0 9,24,44,3072\nv_lshlrev_b32 v252, 16, v205\nMvaa 124,156,164\nv_and_b32 v253, 0xffff0000, v205\nFM 34,247,252\nMvaa 92,156,160\ns_waitcnt vmcnt(9) lgkmcnt(0)\n"
"s_barrier\nMvaa 64,192,224\nTa 128,12,0\nFM 35,247,253\nMvaa 96,192,228\nFM 42,251,252\nFM 43,251,253\nMvaa 128,192,232\nTa 130,12,2048\nv_lshlrev_b32 v252, 16, v206\n"
"Mvaa 160,192,236\ns_add_u32 m0, s48, 32768\ns_add_u32 s45, s45, 128\nMaaa 0,192,240\nTa 132,13,0\nv_and_b32 v253, 0xffff0000, v206\nMaaa 32,192,244\nD0 2,20,45,0\nFM 36,247,252\n"
"Maaa 64,192,248\nTa 134,13,2048\nFM 37,247,253\nMaaa 96,192,252\nFM 44,251,252\nFM 45,251,253\nMaaa 100,196,252\nTa 136,14,0\nv_lshlrev_b32 v252, 16, v207\nMaaa 68,196,248\n"
"v_and_b32 v253, 0xffff0000, v207\nFM 38,247,252\nMaaa 36,196,244\nTa 138,14,2048\nFM 39,247,253\nMaaa 4,196,240\nFM 46,251,252\nFM 47,251,253\nMvaa 164,196,236\nTa 140,15,0\n"
"v_lshlrev_b32 v252, 16, v220\nMvaa 132,196,232\nD0 3,20,45,1024\nv_and_b32 v253, 0xffff0000, v220\nMvaa 100,196,228\nTa 142,15,2048\nFM 48,247,252\nMvaa 68,196,224\nFM 49,247,253\n"
"FM 56,251,252\nMvaa 72,200,224\nTa 144,16,0\nFM 57,251,253\nMvaa 104,200,228\nv_lshlrev_b32 v252, 16, v221\nv_and_b32 v253, 0xffff0000, v221\nMvaa 136,200,232\nTa 146,16,2048\n"
"FM 50,247,252\nMvaa 168,200,236\nFM 51,247,253\nFM 58,251,252\nMaaa 8,200,240\nTa 148,17,0\nFM 59,251,253\nMaaa 40,200,244\nD0 4,20,45,2048\nv_lshlrev_b32 v252, 16, v222\n"
"Maaa 72,200,248\nTa 150,17,2048\nv_and_b32 v253, 0xffff0000, v222\nMaaa 104,200,252\nFM 52,247,252\nFM 53,247,253\nMaaa 108,204,252\nTa 152,18,0\nFM 60,251,252\nMaaa 76,204,248\n"
"FM 61,251,253\nv_lshlrev_b32 v252, 16, v223\nMaaa 44,204,244\nTa 154,18,2048\nv_and_b32 v253, 0xffff0000, v223\nMaaa 12,204,240\nFM 54,247,252\nFM 55,247,253\nMvaa 172,204,236\n"
"Ta 156,19,0\nFM 62,251,252\nMvaa 140,204,232\nD0 5,20,45,3072\nFM 63,251,253\nMvaa 108,204,228\nTa 158,19,2048\nv_permlane32_swap_b32 v32, v48\nMvaa 76,204,224\n"
"v_permlane32_swap_b32 v33, v49\nv_permlane32_swap_b32 v34, v50\nMvaa 80,208,224\nRa 160,10,0\nv_permlane32_swap_b32 v35, v51\nMvaa 112,208,228\nv_permlane32_swap_b32 v36, v52\n"
"v_permlane32_swap_b32 v37, v53\nMvaa 144,208,232\nRa 164,10,2048\nv_permlane32_swap_b32 v38, v54\nMvaa 176,208,236\ns_add_u32 m0, s48, 36864\ns_add_u32 s46, s45, 0x10000\n"
"Maaa 16,208,240\nRa 168,10,8192\nv_permlane32_swap_b32 v39, v55\nMaaa 48,208,244\nD0 2,20,46,0\nv_permlane32_swap_b32 v40, v56\nMaaa 80,208,248\nRa 172,10,10240\n"
"v_permlane32_swap_b32 v41, v57\nMaaa 112,208,252\nv_permlane32_swap_b32 v42, v58\nv_permlane32_swap_b32 v43, v59\nMaaa 116,212,252\nRa 176,10,16384\n"
"v_permlane32_swap_b32 v44, v60\nMaaa 84,212,248\nv_permlane32_swap_b32 v45, v61\nv_permlane32_swap_b32 v46, v62\nMaaa 52,212,244\nRa 180,10,18432\nv_permlane32_swap_b32 v47, v63\n"
"Maaa 20,212,240\nAD 32,32,48\nAD 33,33,49\nMvaa 180,212,236\nRa 184,10,24576\nAD 34,34,50\nMvaa 148,212,232\nD0 3,20,46,1024\nAD 35,35,51\nMvaa 116,212,228\nRa 188,10,26624\n"
"AD 36,36,52\nMvaa 84,212,224\nAD 37,37,53\nAD 38,38,54\nMvaa 88,216,224\nAD 39,39,55\nAD 40,40,56\nMvaa 120,216,228\nAD 41,41,57\nAD 42,42,58\nMvaa 152,216,232\nAD 43,43,59\n"
"AD 44,44,60\nMvaa 184,216,236\nAD 45,45,61\nAD 46,46,62\nMaaa 24,216,240\nAD 47,47,63\nMaaa 56,216,244\nD0 4,20,46,2048\nv_permlane16_swap_b32 v32, v40\nMaaa 88,216,248\n"
"v_permlane16_swap_b32 v33, v41\nv_permlane16_swap_b32 v34, v42\nMaaa 120,216,252\nv_permlane16_swap_b32 v35, v43\nv_permlane16_swap_b32 v36, v44\nMaaa 124,220,252\n"
"v_permlane16_swap_b32 v37, v45\nv_permlane16_swap_b32 v38, v46\nMaaa 92,220,248\nv_permlane16_swap_b32 v39, v47\nAD 32,32,40\nMaaa 60,220,244\nAD 33,33,41\nAD 34,34,42\n"
"Maaa 28,220,240\nAD 35,35,43\nAD 36,36,44\nMvaa 188,220,236\nAD 37,37,45\nAD 38,38,46\nMvaa 156,220,232\nD0 5,20,46,3072\nAD 39,39,47\nMvaa 124,220,228\n"
"v_add_f32_dpp v32, v32, v32 row_ror:8 row_mask:0xf bank_mask:0xf\nv_add_f32_dpp v33, v33, v33 row_ror:8 row_mask:0xf bank_mask:0xf\nMvaa 92,220,224\n"
"s_waitcnt vmcnt(13) lgkmcnt(0)\ns_barrier\nMvaa 64,128,160\nTa 192,12,16384\nv_add_f32_dpp v34, v34, v34 row_ror:8 row_mask:0xf bank_mask:0xf\nMvaa 96,128,164\n"
"v_add_f32_dpp v35, v35, v35 row_ror:8 row_mask:0xf bank_mask:0xf\nv_add_f32_dpp v36, v36, v36 row_ror:8 row_mask:0xf bank_mask:0xf\nMvaa 128,128,168\nTa 194,12,18432\n"
"v_add_f32_dpp v37, v37, v37 row_ror:8 row_mask:0xf bank_mask:0xf\nMvaa 160,128,172\ns_add_u32 m0, s47, 49152\ns_add_u32 s44, s44, 0x40000\nMaaa 0,128,176\nTa 196,13,16384\n"
"v_add_f32_dpp v38, v38, v38 row_ror:8 row_mask:0xf bank_mask:0xf\nMaaa 32,128,180\nD0 6,24,44,0\nv_add_f32_dpp v39, v39, v39 row_ror:8 row_mask:0xf bank_mask:0xf\nMaaa 64,128,184\n"
"Ta 198,13,18432\nv_add_f32_dpp v32, v32, v32 row_ror:4 row_mask:0xf bank_mask:0xf\nMaaa 96,128,188\nv_add_f32_dpp v33, v33, v33 row_ror:4 row_mask:0xf bank_mask:0xf\n"
"v_add_f32_dpp v34, v34, v34 row_ror:4 row_mask:0xf bank_mask:0xf\nMaaa 100,132,188\nTa 200,14,16384\nv_add_f32_dpp v35, v35, v35 row_ror:4 row_mask:0xf bank_mask:0xf\n"
"Maaa 68,132,184\nv_add_f32_dpp v36, v36, v36 row_ror:4 row_mask:0xf bank_mask:0xf\nv_add_f32_dpp v37, v37, v37 row_ror:4 row_mask:0xf bank_mask:0xf\nMaaa 36,132,180\n"
"Ta 202,14,18432\nv_add_f32_dpp v38, v38, v38 row_ror:4 row_mask:0xf bank_mask:0xf\nMaaa 4,132,176\nv_add_f32_dpp v39, v39, v39 row_ror:4 row_mask:0xf bank_mask:0xf\n"
"v_add_f32_dpp v32, v32, v32 row_ror:2 row_mask:0xf bank_mask:0xf\nMvaa 164,132,172\nTa 204,15,16384\nv_add_f32_dpp v33, v33, v33 row_ror:2 row_mask:0xf bank_mask:0xf\n"
"Mvaa 132,132,168\nD0 7,24,44,1024\nv_add_f32_dpp v34, v34, v34 row_ror:2 row_mask:0xf bank_mask:0xf\nMvaa 100,132,164\nTa 206,15,18432\n"
"v_add_f32_dpp v35, v35, v35 row_ror:2 row_mask:0xf bank_mask:0xf\nMvaa 68,132,160\nv_add_f32_dpp v36, v36, v36 row_ror:2 row_mask:0xf bank_mask:0xf\n"
"v_add_f32_dpp v37, v37, v37 row_ror:2 row_mask:0xf bank_mask:0xf\nMvaa 72,136,160\nTa 208,16,16384\nv_add_f32_dpp v38, v38, v38 row_ror:2 row_mask:0xf bank_mask:0xf\n"
"Mvaa 104,136,164\nv_add_f32_dpp v39, v39, v39 row_ror:2 row_mask:0xf bank_mask:0xf\nv_lshrrev_b32 v48, 4, v0\nMvaa 136,136,168\nTa 210,16,18432\nv_and_b32 v49, 1, v0\n"
"Mvaa 168,136,172\nv_lshlrev_b32 v49, 5, v49\nv_lshl_add_u32 v49, v48, 6, v49\nMaaa 8,136,176\nTa 212,17,16384\ns_lshl_b32 s2, s11, 8\nMaaa 40,136,180\nD0 8,24,44,2048\n"
"s_add_u32 s2, s2, 132096\nMaaa 72,136,184\nTa 214,17,18432\nv_add_u32 v49, s2, v49\nMaaa 104,136,188\nds_write_b128 v49, v[32:35]\nds_write_b128 v49, v[36:39] offset:16\n"
"Maaa 108,140,188\nTa 216,18,16384\nv_and_b32 v50, 31, v0\nMaaa 76,140,184\nv_lshlrev_b32 v50, 2, v50\ns_lshl_b32 s2, s11, 10\nMaaa 44,140,180\nTa 218,18,18432\n"
"s_add_u32 s2, s2, 133120\nMaaa 12,140,176\nv_add_u32 v51, s2, v50\nv_add_u32 v50, 132096, v50\nMvaa 172,140,172\nTa 220,19,16384\nv_lshl_add_u32 v52, v48, 4, s2\nMvaa 140,140,168\n"
"D0 9,24,44,3072\nMvaa 108,140,164\nTa 222,19,18432\nMvaa 76,140,160\nMvaa 80,144,160\nRa 224,11,0\nMvaa 112,144,164\nMvaa 144,144,168\nRa 228,11,2048\nMvaa 176,144,172\n"
"s_add_u32 m0, s47, 0\ns_add_u32 s44, s44, 0x40000\nMaaa 16,144,176\nRa 232,11,8192\nMaaa 48,144,180\nD0 6,24,44,0\nMaaa 80,144,184\nRa 236,11,10240\nMaaa 112,144,188\n"
"Maaa 116,148,188\nRa 240,11,16384\nMaaa 84,148,184\nMaaa 52,148,180\nRa 244,11,18432\nMaaa 20,148,176\nMvaa 180,148,172\nRa 248,11,24576\nMvaa 148,148,168\nD0 7,24,44,1024\n"
"Mvaa 116,148,164\nRa 252,11,26624\nMvaa 84,148,160\nMvaa 88,152,160\nMvaa 120,152,164\nMvaa 152,152,168\nMvaa 184,152,172\nMaaa 24,152,176\nMaaa 56,152,180\nD0 8,24,44,2048\n"
"Maaa 88,152,184\nMaaa 120,152,188\nMaaa 124,156,188\nMaaa 92,156,184\nMaaa 60,156,180\nMaaa 28,156,176\nMvaa 188,156,172\nMvaa 156,156,168\nD0 9,24,44,3072\nMvaa 124,156,164\n"
"Mvaa 92,156,160\ns_waitcnt vmcnt(8) lgkmcnt(0)\ns_barrier\nMvaa 64,192,224\nTa 128,12,32768\nds_read_b32 v32, v50 offset:0\nMvaa 96,192,228\nds_read_b32 v33, v50 offset:256\n"
"ds_read_b32 v34, v50 offset:512\nMvaa 128,192,232\nTa 130,12,34816\nds_read_b32 v35, v50 offset:768\nMvaa 160,192,236\ns_add_u32 m0, s48, 0\ns_add_u32 s45, s45, 128\n"
"Maaa 0,192,240\nTa 132,13,32768\nds_read_b32 v36, v50 offset:128\nMaaa 32,192,244\nD0 2,20,45,0\nds_read_b32 v37, v50 offset:384\nMaaa 64,192,248\nTa 134,13,34816\n"
"ds_read_b32 v38, v50 offset:640\nMaaa 96,192,252\nds_read_b32 v39, v50 offset:896\nMaaa 100,196,252\nTa 136,14,32768\nMaaa 68,196,248\nMaaa 36,196,244\nTa 138,14,34816\n"
"Maaa 4,196,240\nMvaa 164,196,236\nTa 140,15,32768\nMvaa 132,196,232\nD0 3,20,45,1024\nMvaa 100,196,228\nTa 142,15,34816\nMvaa 68,196,224\nMvaa 72,200,224\nTa 144,16,32768\n"
"Mvaa 104,200,228\nWL 5\nAD 32,32,33\nMvaa 136,200,232\nTa 146,16,34816\nAD 34,34,35\nMvaa 168,200,236\nAD 36,36,37\nAD 38,38,39\nMaaa 8,200,240\nTa 148,17,32768\nAD 32,32,34\n"
"Maaa 40,200,244\nD0 4,20,45,2048\nAD 36,36,38\nMaaa 72,200,248\nTa 150,17,34816\nCV 32,32,36\nMaaa 104,200,252\nv_and_b32 v36, 0xffff0000, v32\nv_lshlrev_b32 v32, 16, v32\n"
"Maaa 108,204,252\nTa 152,18,32768\nv_mul_f32 v33, 0xbfb8aa3b, v32\nMaaa 76,204,248\nEX 33,33\nMaaa 44,204,244\nTa 154,18,34816\nv_add_f32 v33, 1.0, v33\nMaaa 12,204,240\n"
"v_rcp_f32 v33, v33\nMvaa 172,204,236\nTa 156,19,32768\nMU 33,32,33\nMvaa 140,204,232\nD0 5,20,45,3072\nCV 33,33,33\nMvaa 108,204,228\nTa 158,19,34816\nv_lshlrev_b32 v33, 16, v33\n"
"Mvaa 76,204,224\nMU 33,33,36\nCV 33,33,33\nMvaa 80,208,224\nRa 160,10,32768\nv_lshlrev_b32 v33, 16, v33\nMvaa 112,208,228\nds_write_b32 v51, v33\nRv 244,52,0\nMvaa 144,208,232\n"
"Ra 164,10,34816\nRv 248,52,64\nMvaa 176,208,236\ns_add_u32 m0, s48, 4096\ns_add_u32 s46, s45, 0x10000\nMaaa 16,208,240\nRa 168,10,40960\ns_and_b32 s2, s12, 1\nMaaa 48,208,244\n"
"D0 2,20,46,0\ns_and_b32 s3, s13, 6\nMaaa 80,208,248\nRa 172,10,43008\ns_add_u32 s2, s2, s3\nMaaa 112,208,252\ns_lshl3_add_u32 s2, s14, s2\nMaaa 116,212,252\nRa 176,10,49152\n"
"Maaa 84,212,248\ns_cmp_lt_u32 s2, 14\ns_cselect_b32 s5, 0, 0x40000000\nMaaa 52,212,244\nRa 180,10,51200\ns_mul_i32 s4, s73, 14\nMaaa 20,212,240\ns_add_u32 s4, s4, s2\n"
"v_and_b32 v49, 15, v0\nMvaa 180,212,236\nRa 184,10,57344\nv_lshlrev_b32 v50, 3, v49\nMvaa 148,212,232\nD0 3,20,46,1024\nv_and_b32 v51, 1, v48\nMvaa 116,212,228\nRa 188,10,59392\n"
"v_lshl_add_u32 v50, v51, 2, v50\nMvaa 84,212,224\ns_mul_i32 s2, s11, 112\nv_add_u32 v50, s2, v50\nMvaa 88,216,224\nv_lshrrev_b32 v51, 4, v50\nv_lshrrev_b32 v48, 1, v48\n"
"Mvaa 120,216,228\nv_mad_u32_u24 v51, v48, 28, v51\nv_mul_u32_u24 v51, 7168, v51\nMvaa 152,216,232\nv_and_b32 v50, 15, v50\nv_lshl_add_u32 v51, v50, 2, v51\nMvaa 184,216,236\n"
"v_cmp_gt_u32 vcc, 14, v49\ns_lshl_b32 s4, s4, 6\ns_add_u32 s4, s4, 0xc04c800\nv_cndmask_b32 v51, v254, v51, vcc\nMaaa 24,216,240\ns_or_b32 s4, s4, s5\nWL 6\nMaaa 56,216,244\n"
"D0 4,20,46,2048\nv_lshlrev_b32 v252, 16, v224\nMaaa 88,216,248\nv_and_b32 v253, 0xffff0000, v224\nMU 32,244,252\nMaaa 120,216,252\nMU 33,244,253\nMU 40,248,252\nMaaa 124,220,252\n"
"MU 41,248,253\nv_lshlrev_b32 v252, 16, v225\nMaaa 92,220,248\nv_and_b32 v253, 0xffff0000, v225\nMU 34,244,252\nMaaa 60,220,244\nMU 35,244,253\nMU 42,248,252\nMaaa 28,220,240\n"
"MU 43,248,253\nv_lshlrev_b32 v252, 16, v226\nMvaa 188,220,236\nv_and_b32 v253, 0xffff0000, v226\nMU 36,244,252\nMvaa 156,220,232\nD0 5,20,46,3072\nMU 37,244,253\nMvaa 124,220,228\n"
"MU 44,248,252\nMU 45,248,253\nMvaa 92,220,224\ns_waitcnt vmcnt(12) lgkmcnt(0)\ns_barrier\nMvaa 64,128,160\nTa 192,12,49152\nv_lshlrev_b32 v252, 16, v227\nMvaa 96,128,164\n"
"v_and_b32 v253, 0xffff0000, v227\nMU 38,244,252\nMvaa 128,128,168\nTa 194,12,51200\nMU 39,244,253\nMvaa 160,128,172\ns_add_u32 m0, s47, 16384\ns_add_u32 s44, s44, 0x40000\n"
"Maaa 0,128,176\nTa 196,13,49152\nMU 46,248,252\nMaaa 32,128,180\nD0 6,24,44,0\nMU 47,248,253\nMaaa 64,128,184\nTa 198,13,51200\nv_lshlrev_b32 v252, 16, v228\nMaaa 96,128,188\n"
"v_and_b32 v253, 0xffff0000, v228\nFM 32,245,252\nMaaa 100,132,188\nTa 200,14,49152\nFM 33,245,253\nMaaa 68,132,184\nFM 40,249,252\nFM 41,249,253\nMaaa 36,132,180\nTa 202,14,51200\n"
"v_lshlrev_b32 v252, 16, v229\nMaaa 4,132,176\nv_and_b32 v253, 0xffff0000, v229\nFM 34,245,252\nMvaa 164,132,172\nTa 204,15,49152\nFM 35,245,253\nMvaa 132,132,168\nD0 7,24,44,1024\n"
"FM 42,249,252\nMvaa 100,132,164\nTa 206,15,51200\nFM 43,249,253\nMvaa 68,132,160\nv_lshlrev_b32 v252, 16, v230\nv_and_b32 v253, 0xffff0000, v230\nMvaa 72,136,160\nTa 208,16,49152\n"
"FM 36,245,252\nMvaa 104,136,164\nFM 37,245,253\nFM 44,249,252\nMvaa 136,136,168\nTa 210,16,51200\nFM 45,249,253\nMvaa 168,136,172\nv_lshlrev_b32 v252, 16, v231\n"
"v_and_b32 v253, 0xffff0000, v231\nMaaa 8,136,176\nTa 212,17,49152\nFM 38,245,252\nMaaa 40,136,180\nD0 8,24,44,2048\nFM 39,245,253\nMaaa 72,136,184\nTa 214,17,51200\nFM 46,249,252\n"
"Maaa 104,136,188\nFM 47,249,253\nv_lshlrev_b32 v252, 16, v232\nMaaa 108,140,188\nTa 216,18,49152\nv_and_b32 v253, 0xffff0000, v232\nMaaa 76,140,184\nFM 32,246,252\nFM 33,246,253\n"
"Maaa 44,140,180\nTa 218,18,51200\nFM 40,250,252\nMaaa 12,140,176\nFM 41,250,253\nv_lshlrev_b32 v252, 16, v233\nMvaa 172,140,172\nTa 220,19,49152\nv_and_b32 v253, 0xffff0000, v233\n"
"Mvaa 140,140,168\nD0 9,24,44,3072\nFM 34,246,252\nMvaa 108,140,164\nTa 222,19,51200\nFM 35,246,253\nMvaa 76,140,160\nFM 42,250,252\nFM 43,250,253\nMvaa 80,144,160\n"
"Ra 224,11,32768\nv_lshlrev_b32 v252, 16, v234\nMvaa 112,144,164\nv_and_b32 v253, 0xffff0000, v234\nFM 36,246,252\nMvaa 144,144,168\nRa 228,11,34816\nFM 37,246,253\n"
"Mvaa 176,144,172\ns_add_u32 m0, s47, 32768\ns_add_u32 s44, s44, 0x40000\nMaaa 16,144,176\nRa 232,11,40960\nFM 44,250,252\nMaaa 48,144,180\nD0 6,24,44,0\nFM 45,250,253\n"
"Maaa 80,144,184\nRa 236,11,43008\nv_lshlrev_b32 v252, 16, v235\nMaaa 112,144,188\nv_and_b32 v253, 0xffff0000, v235\nFM 38,246,252\nMaaa 116,148,188\nRa 240,11,49152\n"
"FM 39,246,253\nMaaa 84,148,184\nFM 46,250,252\nFM 47,250,253\nMaaa 52,148,180\nRa 244,11,51200\nv_lshlrev_b32 v252, 16, v236\nMaaa 20,148,176\nv_and_b32 v253, 0xffff0000, v236\n"
"FM 32,247,252\nMvaa 180,148,172\nRa 248,11,57344\nFM 33,247,253\nMvaa 148,148,168\nD0 7,24,44,1024\nFM 40,251,252\nMvaa 116,148,164\nRa 252,11,59392\nFM 41,251,253\n"
"Mvaa 84,148,160\nv_lshlrev_b32 v252, 16, v237\nv_and_b32 v253, 0xffff0000, v237\nMvaa 88,152,160\nFM 34,247,252\nFM 35,247,253\nMvaa 120,152,164\nFM 42,251,252\nFM 43,251,253\n"
"Mvaa 152,152,168\nv_lshlrev_b32 v252, 16, v238\nv_and_b32 v253, 0xffff0000, v238\nMvaa 184,152,172\nFM 36,247,252\nFM 37,247,253\nMaaa 24,152,176\nFM 44,251,252\nFM 45,251,253\n"
"Maaa 56,152,180\nD0 8,24,44,2048\nv_lshlrev_b32 v252, 16, v239\nMaaa 88,152,184\nv_and_b32 v253, 0xffff0000, v239\nFM 38,247,252\nMaaa 120,152,188\nFM 39,247,253\nFM 46,251,252\n"
"Maaa 124,156,188\nFM 47,251,253\nMaaa 92,156,184\nv_permlane32_swap_b32 v32, v40\nv_permlane32_swap_b32 v33, v41\nMaaa 60,156,180\nv_permlane32_swap_b32 v34, v42\n"
"v_permlane32_swap_b32 v35, v43\nMaaa 28,156,176\nv_permlane32_swap_b32 v36, v44\nv_permlane32_swap_b32 v37, v45\nMvaa 188,156,172\nv_permlane32_swap_b32 v38, v46\n"
"v_permlane32_swap_b32 v39, v47\nMvaa 156,156,168\nD0 9,24,44,3072\nAD 32,32,40\nMvaa 124,156,164\nAD 33,33,41\nAD 34,34,42\nMvaa 92,156,160\ns_waitcnt vmcnt(8) lgkmcnt(0)\n"
"s_barrier\nMvaa 64,192,224\nTa 128,12,0\nAD 35,35,43\nMvaa 96,192,228\nAD 36,36,44\nAD 37,37,45\nMvaa 128,192,232\nTa 130,12,2048\nAD 38,38,46\nMvaa 160,192,236\n"
"s_add_u32 m0, s48, 32768\ns_add_u32 s45, s45, 128\nMaaa 0,192,240\nTa 132,13,0\nAD 39,39,47\nMaaa 32,192,244\nD0 2,20,45,0\nv_permlane16_swap_b32 v32, v36\nMaaa 64,192,248\n"
"Ta 134,13,2048\nv_permlane16_swap_b32 v33, v37\nMaaa 96,192,252\nv_permlane16_swap_b32 v34, v38\nv_permlane16_swap_b32 v35, v39\nMaaa 100,196,252\nTa 136,14,0\nAD 32,32,36\n"
"Maaa 68,196,248\nAD 33,33,37\nAD 34,34,38\nMaaa 36,196,244\nTa 138,14,2048\nAD 35,35,39\nMaaa 4,196,240\nSv 32,51,16,4,0\nMvaa 164,196,236\nTa 140,15,0\nMvaa 132,196,232\n"
"D0 3,20,45,1024\nMvaa 100,196,228\nTa 142,15,2048\nMvaa 68,196,224\nMvaa 72,200,224\nTa 144,16,0\nMvaa 104,200,228\nMvaa 136,200,232\nTa 146,16,2048\nMvaa 168,200,236\n"
"Maaa 8,200,240\nTa 148,17,0\nMaaa 40,200,244\nD0 4,20,45,2048\nMaaa 72,200,248\nTa 150,17,2048\nMaaa 104,200,252\nMaaa 108,204,252\nTa 152,18,0\nMaaa 76,204,248\nMaaa 44,204,244\n"
"Ta 154,18,2048\nMaaa 12,204,240\nMvaa 172,204,236\nTa 156,19,0\nMvaa 140,204,232\nD0 5,20,45,3072\nMvaa 108,204,228\nTa 158,19,2048\nMvaa 76,204,224\nMvaa 80,208,224\nRa 160,10,0\n"
"Mvaa 112,208,228\nMvaa 144,208,232\nRa 164,10,2048\nMvaa 176,208,236\ns_add_u32 m0, s48, 36864\ns_add_u32 s46, s45, 0x10000\nMaaa 16,208,240\nRa 168,10,8192\nMaaa 48,208,244\n"
"D0 2,20,46,0\nMaaa 80,208,248\nRa 172,10,10240\nMaaa 112,208,252\nMaaa 116,212,252\nRa 176,10,16384\nMaaa 84,212,248\nMaaa 52,212,244\nRa 180,10,18432\nMaaa 20,212,240\n"
"Mvaa 180,212,236\nRa 184,10,24576\nMvaa 148,212,232\nD0 3,20,46,1024\nMvaa 116,212,228\nRa 188,10,26624\nMvaa 84,212,224\nMvaa 88,216,224\nMvaa 120,216,228\nMvaa 152,216,232\n"
"Mvaa 184,216,236\nMaaa 24,216,240\nMaaa 56,216,244\nD0 4,20,46,2048\nMaaa 88,216,248\nMaaa 120,216,252\nMaaa 124,220,252\nMaaa 92,220,248\nMaaa 60,220,244\nMaaa 28,220,240\n"
"Mvaa 188,220,236\nMvaa 156,220,232\nD0 5,20,46,3072\nMvaa 124,220,228\nMvaa 92,220,224\ns_waitcnt vmcnt(13) lgkmcnt(0)\ns_barrier\ns_cmp_eq_u32 s14, 0\ns_cbranch_scc1 .T0K4\n"
"Mvaa 64,128,160\nTa 192,12,16384\nv_lshrrev_b32 v48, 4, v0\nMvaa 96,128,164\nv_and_b32 v49, 7, v0\nv_xor_b32 v49, v49, v48\ns_add_u32 m0, s47, 49152\ns_add_u32 s44, s44, 0x40000\n"
"Mvaa 128,128,168\nTa 194,12,18432\nv_lshlrev_b32 v49, 4, v49\nMvaa 160,128,172\nv_lshrrev_b32 v48, 3, v0\nv_mul_u32_u24 v48, 896, v48\nD0 6,24,44,0\nMaaa 0,128,176\n"
"Ta 196,13,16384\nv_add_u32 v40, v48, v49\nMaaa 32,128,180\nv_xor_b32 v49, 64, v40\nv_add_u32 v41, 6144, v49\nMaaa 64,128,184\nTa 198,13,18432\nv_add_u32 v42, 12288, v40\n"
"Maaa 96,128,188\nv_add_u32 v43, 18432, v49\ns_mov_b32 s2, s13\nMaaa 100,132,188\nTa 200,14,16384\ns_lshl2_add_u32 s2, s2, s11\nD0 7,24,44,1024\nMaaa 68,132,184\n"
"s_mul_i32 s52, s2, 28672\ns_lshr_b32 s2, s13, 2\nMaaa 36,132,180\nTa 202,14,18432\ns_bfe_u32 s3, s13, 0x10001\nMaaa 4,132,176\ns_mul_i32 s3, s3, 17\ns_add_u32 s2, s2, s3\n"
"Mvaa 164,132,172\nTa 204,15,16384\ns_lshl2_add_u32 s3, s14, 0\nMvaa 132,132,168\ns_lshl_b32 s3, s3, 2\ns_lshr_b32 s3, 0xe420ca86, s3\nD0 8,24,44,2048\nMvaa 100,132,164\n"
"Ta 206,15,18432\ns_and_b32 s3, s3, 15\nMvaa 68,132,160\ns_add_u32 s2, s2, s3\nMvaa 72,136,160\nTa 208,16,16384\nMvaa 104,136,164\ns_cmp_eq_u32 s2, 32\ns_cselect_b32 s4, 16, s2\n"
"Mvaa 136,136,168\nTa 210,16,18432\ns_mul_i32 s75, s4, 65536\nD0 9,24,44,3072\nMvaa 168,136,172\ns_lshr_b32 s2, s13, 2\ns_bfe_u32 s3, s13, 0x10001\nMaaa 8,136,176\nTa 212,17,16384\n"
"s_mul_i32 s3, s3, 17\nMaaa 40,136,180\ns_add_u32 s2, s2, s3\ns_lshl2_add_u32 s3, s14, 1\ns_add_u32 m0, s47, 65536\ns_add_u32 s46, s51, 0\nMaaa 72,136,184\nTa 214,17,18432\n"
"s_lshl_b32 s3, s3, 2\nMaaa 104,136,188\ns_lshr_b32 s3, 0xe420ca86, s3\ns_and_b32 s3, s3, 15\nD0 6,32,46,0\nMaaa 108,140,188\nTa 216,18,16384\ns_add_u32 s2, s2, s3\n"
"Maaa 76,140,184\ns_cmp_eq_u32 s2, 32\ns_cselect_b32 s4, 16, s2\nMaaa 44,140,180\nTa 218,18,18432\ns_mul_i32 s76, s4, 65536\nMaaa 12,140,176\ns_lshr_b32 s2, s13, 2\n"
"s_bfe_u32 s3, s13, 0x10001\nMvaa 172,140,172\nTa 220,19,16384\ns_mul_i32 s3, s3, 17\nD0 7,32,46,1024\nMvaa 140,140,168\ns_add_u32 s2, s2, s3\ns_lshl2_add_u32 s3, s14, 2\n"
"Mvaa 108,140,164\nTa 222,19,18432\ns_lshl_b32 s3, s3, 2\nMvaa 76,140,160\ns_lshr_b32 s3, 0xe420ca86, s3\ns_and_b32 s3, s3, 15\nMvaa 80,144,160\nRa 224,11,0\ns_add_u32 s2, s2, s3\n"
"Mvaa 112,144,164\ns_cmp_eq_u32 s2, 32\ns_cselect_b32 s4, 16, s2\nD0 8,32,46,2048\nMvaa 144,144,168\nRa 228,11,2048\ns_mul_i32 s77, s4, 65536\nMvaa 176,144,172\n"
"s_lshr_b32 s2, s13, 2\ns_bfe_u32 s3, s13, 0x10001\nMaaa 16,144,176\nRa 232,11,8192\ns_mul_i32 s3, s3, 17\nMaaa 48,144,180\ns_add_u32 s2, s2, s3\ns_lshl2_add_u32 s3, s14, 3\n"
"Maaa 80,144,184\nRa 236,11,10240\ns_lshl_b32 s3, s3, 2\nD0 9,32,46,3072\nMaaa 112,144,188\ns_lshr_b32 s3, 0xe420ca86, s3\ns_and_b32 s3, s3, 15\nMaaa 116,148,188\nRa 240,11,16384\n"
"s_add_u32 s2, s2, s3\nMaaa 84,148,184\ns_cmp_eq_u32 s2, 32\ns_cselect_b32 s4, 16, s2\ns_add_u32 m0, s47, 81920\ns_add_u32 s46, s51, 262144\nMaaa 52,148,180\nRa 244,11,18432\n"
"s_mul_i32 s78, s4, 65536\nMaaa 20,148,176\ns_lshr_b32 s3, s11, 1\ns_lshl_b32 s3, s3, 15\nD0 6,32,46,0\nMvaa 180,148,172\nRa 248,11,24576\ns_add_u32 s2, s72, s3\nMvaa 148,148,168\n"
"s_and_b32 s3, s11, 1\ns_lshl_b32 s3, s3, 12\nMvaa 116,148,164\nRa 252,11,26624\ns_add_u32 s2, s2, s3\nMvaa 84,148,160\ns_add_u32 s2, s2, 0x0\ns_mov_b32 s55, 16384\n"
"Mvaa 88,152,160\ns_add_u32 s75, s75, s2\ns_add_u32 s76, s76, s2\nD0 7,32,46,1024\nMvaa 120,152,164\ns_add_u32 s77, s77, s2\ns_add_u32 s78, s78, s2\nMvaa 152,152,168\n"
"v_add_u32 v32, 0x10000, v12\nv_add_u32 v33, 0x10000, v13\nMvaa 184,152,172\nv_add_u32 v34, 0x10000, v14\nv_add_u32 v35, 0x10000, v15\nMaaa 24,152,176\nv_add_u32 v36, 0x10000, v16\n"
"v_add_u32 v37, 0x10000, v17\nMaaa 56,152,180\nv_add_u32 v38, 0x10000, v18\nv_add_u32 v39, 0x10000, v19\nD0 8,32,46,2048\nMaaa 88,152,184\ns_add_u32 s2, s11, 1\n"
"s_lshr_b32 s2, s2, 1\nMaaa 120,152,188\ns_lshl_b32 s2, s2, 12\nv_add_u32 v44, s2, v10\nMaaa 124,156,188\nv_add_u32 v45, s2, v11\nv_xor_b32 v46, 0x1000, v44\nMaaa 92,156,184\n"
"v_xor_b32 v47, 0x1000, v45\ns_lshr_b32 s3, s11, 1\nMaaa 60,156,180\ns_lshl_b32 s3, s3, 13\ns_lshl_b32 s2, s11, 12\nD0 9,32,46,3072\nMaaa 28,156,176\ns_add_u32 s2, s2, s3\n"
"s_add_u32 s2, s2, s72\nMvaa 188,156,172\ns_lshl_b32 s3, s11, 12\ns_add_u32 s89, s3, 0x10000\nMvaa 156,156,168\ns_mov_b32 s88, 0xffff8000\ns_lshl_b32 s4, s13, 1\nMvaa 124,156,164\n"
"s_sub_u32 s79, 15, s4\ns_and_b32 s5, s12, 1\nMvaa 92,156,160\ns_waitcnt vmcnt(12) lgkmcnt(0)\ns_barrier\nMvaa 64,192,224\nTa 128,12,32768\ns_mul_i32 s5, s5, 229376\n"
"Mvaa 96,192,228\ns_add_u32 s5, s5, s2\ns_add_u32 s96, s5, 0x1310000\ns_add_u32 m0, s47, -65536\ns_add_u32 s46, s52, 0\nMvaa 128,192,232\nTa 130,12,34816\n"
"s_mov_b32 s63, 0x53800000\nMvaa 160,192,236\ns_mov_b32 s64, 0x2b800000\ns_mov_b32 s98, s4\nD0 40,28,46,0\nMaaa 0,192,240\nTa 132,13,32768\ns_lshl3_add_u32 s5, s12, s13\n"
"Maaa 32,192,244\ns_lshl2_add_u32 s5, s5, s11\ns_lshl_b32 s65, s5, 9\nMaaa 64,192,248\nTa 134,13,34816\ns_add_u32 s65, s65, 0xc000000\nMaaa 96,192,252\ns_and_b32 s66, s72, 0x2000\n"
"s_add_u32 s66, s66, s56\nMaaa 100,196,252\nTa 136,14,32768\ns_add_u32 s66, s66, 0x100000\nD0 41,28,46,1024\nMaaa 68,196,248\ns_lshl_b32 s61, s12, 8\ns_add_u32 s61, s61, 0xc048400\n"
"Maaa 36,196,244\nTa 138,14,34816\ns_lshr_b32 s58, s12, 1\nMaaa 4,196,240\ns_lshl_b32 s58, s58, 9\ns_add_u32 s58, s58, 0xc048400\nMvaa 164,196,236\nTa 140,15,32768\n"
"s_lshl1_add_u32 s5, s12, 1\nMvaa 132,196,232\ns_lshl3_add_u32 s5, s5, s13\ns_lshl4_add_u32 s54, s5, 0xc048400\nD0 42,28,46,2048\nMvaa 100,196,228\nTa 142,15,34816\n"
"Mvaa 68,196,224\ns_cmp_eq_u32 s11, 0\ns_cselect_b32 s54, s54, 0x40000000\nMvaa 72,200,224\nTa 144,16,32768\ns_getpc_b64 s[8:9]\n.PC35:\ns_add_u32 s8, s8, .HD11-.PC35\n"
"s_addc_u32 s9, s9, 0\nMvaa 104,200,228\ns_mov_b32 s97, 0xff800000\ns_mov_b32 s90, 1\nMvaa 136,200,232\nTa 146,16,34816\ns_lshl_b32 s5, s11, 2\nD0 43,28,46,3072\nMvaa 168,200,236\n"
"s_lshr_b32 s92, 0xf112, s5\ns_and_b32 s92, s92, 15\nMaaa 8,200,240\nTa 148,17,32768\ns_lshr_b32 s93, s11, 1\nMaaa 40,200,244\ns_xor_b32 s93, s93, 1\ns_mul_i32 s4, s79, 65536\n"
"s_add_u32 m0, s47, 0\ns_add_u32 s46, s51, 524288\nMaaa 72,200,248\nTa 150,17,34816\ns_sub_u32 s4, s2, s4\nMaaa 104,200,252\ns_add_u32 s94, s4, 0x20000\n"
"s_add_u32 s95, s4, 0x410000\nD0 6,32,46,0\nMaaa 108,204,252\nTa 152,18,32768\ns_add_u32 s86, s2, 0x130000\nMaaa 76,204,248\ns_add_u32 s87, s2, 0x520000\ns_sub_u32 s99, s79, 1\n"
"Maaa 44,204,244\nTa 154,18,34816\ns_mov_b32 s91, 100\nMaaa 12,204,240\ns_cmp_eq_u32 s11, 3\ns_cselect_b32 s92, 100, s92\nMvaa 172,204,236\nTa 156,19,32768\nD0 7,32,46,1024\n"
"Mvaa 140,204,232\nMvaa 108,204,228\nTa 158,19,34816\nMvaa 76,204,224\nMvaa 80,208,224\nRa 160,10,32768\nMvaa 112,208,228\nD0 8,32,46,2048\nMvaa 144,208,232\nRa 164,10,34816\n"
"Mvaa 176,208,236\nMaaa 16,208,240\nRa 168,10,40960\nMaaa 48,208,244\nMaaa 80,208,248\nRa 172,10,43008\nD0 9,32,46,3072\nMaaa 112,208,252\nMaaa 116,212,252\nRa 176,10,49152\n"
"Maaa 84,212,248\ns_add_u32 m0, s47, 16384\ns_add_u32 s46, s51, 786432\nMaaa 52,212,244\nRa 180,10,51200\nMaaa 20,212,240\nD0 6,32,46,0\nMvaa 180,212,236\nRa 184,10,57344\n"
"Mvaa 148,212,232\nMvaa 116,212,228\nRa 188,10,59392\nMvaa 84,212,224\nMvaa 88,216,224\nD0 7,32,46,1024\nMvaa 120,216,228\nMvaa 152,216,232\nMvaa 184,216,236\nMaaa 24,216,240\n"
"Maaa 56,216,244\nD0 8,32,46,2048\nMaaa 88,216,248\nMaaa 120,216,252\nMaaa 124,220,252\nMaaa 92,220,248\nMaaa 60,220,244\nD0 9,32,46,3072\nMaaa 28,220,240\nMvaa 188,220,236\n"
"Mvaa 156,220,232\nMvaa 124,220,228\nMvaa 92,220,224\ns_waitcnt vmcnt(20) lgkmcnt(0)\ns_barrier\nMvaa 64,128,160\nTa 192,12,49152\nMvaa 96,128,164\ns_add_u32 m0, s47, -49152\n"
"s_add_u32 s46, s52, 256\nMvaa 128,128,168\nTa 194,12,51200\nMvaa 160,128,172\nD0 40,28,46,0\nMaaa 0,128,176\nTa 196,13,49152\nMaaa 32,128,180\nMaaa 64,128,184\nTa 198,13,51200\n"
"Maaa 96,128,188\nMaaa 100,132,188\nTa 200,14,49152\nD0 41,28,46,1024\nMaaa 68,132,184\nMaaa 36,132,180\nTa 202,14,51200\nMaaa 4,132,176\nMvaa 164,132,172\nTa 204,15,49152\n"
"Mvaa 132,132,168\nD0 42,28,46,2048\nMvaa 100,132,164\nTa 206,15,51200\nMvaa 68,132,160\nMvaa 72,136,160\nTa 208,16,49152\nMvaa 104,136,164\nMvaa 136,136,168\nTa 210,16,51200\n"
"D0 43,28,46,3072\nMvaa 168,136,172\nMaaa 8,136,176\nTa 212,17,49152\nMaaa 40,136,180\ns_add_u32 m0, s47, 32768\ns_add_u32 s46, s51, 1048576\nMaaa 72,136,184\nTa 214,17,51200\n"
"Maaa 104,136,188\nD0 6,32,46,0\nMaaa 108,140,188\nTa 216,18,49152\nMaaa 76,140,184\nMaaa 44,140,180\nTa 218,18,51200\nMaaa 12,140,176\nMvaa 172,140,172\nTa 220,19,49152\n"
"D0 7,32,46,1024\nMvaa 140,140,168\nMvaa 108,140,164\nTa 222,19,51200\nMvaa 76,140,160\nMvaa 80,144,160\nRa 224,11,32768\nMvaa 112,144,164\nD0 8,32,46,2048\nMvaa 144,144,168\n"
"Ra 228,11,34816\nMvaa 176,144,172\nMaaa 16,144,176\nRa 232,11,40960\nMaaa 48,144,180\nMaaa 80,144,184\nRa 236,11,43008\nD0 9,32,46,3072\nMaaa 112,144,188\nMaaa 116,148,188\n"
"Ra 240,11,49152\nMaaa 84,148,184\nMaaa 52,148,180\nRa 244,11,51200\nMaaa 20,148,176\nMvaa 180,148,172\nRa 248,11,57344\nMvaa 148,148,168\nMvaa 116,148,164\nRa 252,11,59392\n"
"Mvaa 84,148,160\nMvaa 88,152,160\nMvaa 120,152,164\nMvaa 152,152,168\nMvaa 184,152,172\nMaaa 24,152,176\nMaaa 56,152,180\nMaaa 88,152,184\nMaaa 120,152,188\nMaaa 124,156,188\n"
"Maaa 92,156,184\nMaaa 60,156,180\nMaaa 28,156,176\nMvaa 188,156,172\nMvaa 156,156,168\nMvaa 124,156,164\nMvaa 92,156,160\ns_waitcnt vmcnt(16) lgkmcnt(0)\ns_barrier\n"
"Mvaa 64,192,224\ns_add_u32 m0, s47, -16384\ns_add_u32 s46, s52, 128\nMvaa 68,196,224\nMvaa 72,200,224\nD0 40,28,46,0\nMvaa 76,204,224\nMvaa 80,208,224\nMvaa 84,212,224\n"
"Mvaa 88,216,224\nD0 41,28,46,1024\nCV 192,64,65\nMvaa 92,220,224\nCV 193,66,67\nCV 194,68,69\nMvaa 96,192,228\nCV 195,70,71\nCV 196,72,73\nMvaa 100,196,228\nCV 197,74,75\n"
"CV 198,76,77\nMvaa 104,200,228\nD0 42,28,46,2048\nCV 199,78,79\nMvaa 108,204,228\nCV 200,80,81\nCV 201,82,83\nMvaa 112,208,228\nCV 202,84,85\nCV 203,86,87\nMvaa 116,212,228\n"
"CV 204,88,89\nCV 205,90,91\nMvaa 120,216,228\nD0 43,28,46,3072\nCV 206,92,93\nMvaa 124,220,228\nCV 207,94,95\nCV 208,96,97\nMvaa 128,192,232\ns_add_u32 m0, s47, 49152\n"
"s_add_u32 s46, s51, 1310720\nMvaa 132,196,232\nCV 209,98,99\nCV 210,100,101\nMvaa 136,200,232\nD0 6,32,46,0\nCV 211,102,103\nMvaa 140,204,232\nCV 212,104,105\nCV 213,106,107\n"
"Mvaa 144,208,232\nCV 214,108,109\nCV 215,110,111\nMvaa 148,212,232\nCV 216,112,113\nCV 217,114,115\nMvaa 152,216,232\nD0 7,32,46,1024\nCV 218,116,117\nMvaa 156,220,232\n"
"CV 219,118,119\nCV 220,120,121\nMvaa 160,192,236\nCV 221,122,123\nCV 222,124,125\nMvaa 164,196,236\nCV 223,126,127\nCV 224,128,129\nMvaa 168,200,236\nD0 8,32,46,2048\n"
"CV 225,130,131\nMvaa 172,204,236\nCV 226,132,133\nCV 227,134,135\nMvaa 176,208,236\nCV 228,136,137\nCV 229,138,139\nMvaa 180,212,236\nCV 230,140,141\nCV 231,142,143\n"
"Mvaa 184,216,236\nD0 9,32,46,3072\nCV 232,144,145\nMvaa 188,220,236\nCV 233,146,147\nCV 234,148,149\nMaaa 0,192,240\ns_add_u32 m0, s47, -32768\ns_add_u32 s46, s52, 384\n"
"Maaa 4,196,240\nCV 235,150,151\nCV 236,152,153\nMaaa 8,200,240\nD0 40,28,46,0\nCV 237,154,155\nMaaa 12,204,240\nCV 238,156,157\nCV 239,158,159\nMaaa 16,208,240\nRa 160,44,0\n"
"CV 240,160,161\nMaaa 20,212,240\nRa 164,44,2048\nCV 241,162,163\nMaaa 24,216,240\nD0 41,28,46,1024\nRa 168,46,0\nMaaa 28,220,240\nRa 172,46,2048\nCV 242,164,165\nMaaa 32,192,244\n"
"Ta 128,32,0\nCV 243,166,167\nMaaa 36,196,244\nTa 130,32,2048\nCV 244,168,169\nMaaa 40,200,244\nD0 42,28,46,2048\nTa 132,33,0\nMaaa 44,204,244\nTa 134,33,2048\nCV 245,170,171\n"
"Maaa 48,208,244\nTa 136,34,0\nCV 246,172,173\nMaaa 52,212,244\nTa 138,34,2048\nCV 247,174,175\nMaaa 56,216,244\nD0 43,28,46,3072\nTa 140,35,0\nMaaa 60,220,244\nTa 142,35,2048\n"
"CV 248,176,177\nMaaa 64,192,248\nTa 144,36,0\nCV 249,178,179\nMaaa 68,196,248\nTa 146,36,2048\nCV 250,180,181\nMaaa 72,200,248\nTa 148,37,0\nCV 251,182,183\nSv 192,1,16,75,0\n"
"Maaa 76,204,248\nTa 150,37,2048\nCV 252,184,185\nMaaa 80,208,248\nTa 152,38,0\nCV 253,186,187\nMaaa 84,212,248\nTa 154,38,2048\nCV 254,188,189\nMaaa 88,216,248\nTa 156,39,0\n"
"CV 255,190,191\nMaaa 92,220,248\nTa 158,39,2048\nMaaa 96,192,252\nSv 196,1,16,75,1024\nMaaa 100,196,252\nMaaa 104,200,252\nMaaa 108,204,252\nMaaa 112,208,252\nMaaa 116,212,252\n"
"Maaa 120,216,252\nSv 200,1,16,75,2048\nMaaa 124,220,252\nWL 0\ns_branch .Q16\n.T0K4:\nMvaa 64,128,160\nTa 192,12,16384\ns_lshr_b32 s3, s11, 1\nMvaa 96,128,164\n"
"s_lshl_b32 s3, s3, 13\ns_lshl_b32 s2, s11, 12\ns_add_u32 m0, s47, 49152\ns_add_u32 s44, s44, 0x40000\nMvaa 128,128,168\nTa 194,12,18432\ns_add_u32 s2, s2, s3\nMvaa 160,128,172\n"
"s_add_u32 s2, s2, s72\ns_lshl_b32 s3, s11, 12\nD0 6,24,44,0\nMaaa 0,128,176\nTa 196,13,16384\ns_add_u32 s89, s3, 0x0\nMaaa 32,128,180\ns_mov_b32 s88, 0xffff8000\n"
"s_lshl_b32 s4, s13, 1\nMaaa 64,128,184\nTa 198,13,18432\ns_sub_u32 s79, 15, s4\nMaaa 96,128,188\ns_and_b32 s5, s12, 1\ns_mul_i32 s5, s5, 229376\nD0 7,24,44,1024\nMaaa 100,132,188\n"
"Ta 200,14,16384\ns_add_u32 s5, s5, s2\nMaaa 68,132,184\ns_add_u32 s96, s5, 0x1310000\ns_mov_b32 s63, 0x53800000\nMaaa 36,132,180\nTa 202,14,18432\ns_mov_b32 s64, 0x2b800000\n"
"Maaa 4,132,176\ns_mov_b32 s98, s4\ns_lshl3_add_u32 s5, s12, s13\nD0 8,24,44,2048\nMvaa 164,132,172\nTa 204,15,16384\ns_lshl2_add_u32 s5, s5, s11\nMvaa 132,132,168\n"
"s_lshl_b32 s65, s5, 9\ns_add_u32 s65, s65, 0xc000000\nMvaa 100,132,164\nTa 206,15,18432\ns_and_b32 s66, s72, 0x2000\nMvaa 68,132,160\ns_add_u32 s66, s66, s56\n"
"s_add_u32 s66, s66, 0x100000\nD0 9,24,44,3072\nMvaa 72,136,160\nTa 208,16,16384\ns_lshl_b32 s61, s12, 8\nMvaa 104,136,164\ns_add_u32 s61, s61, 0xc048400\ns_lshr_b32 s58, s12, 1\n"
"s_add_u32 s6, s56, 0\nMvaa 136,136,168\nTa 210,16,18432\ns_lshl_b32 s58, s58, 9\nMvaa 168,136,172\ns_add_u32 s58, s58, 0xc048400\ns_lshl1_add_u32 s5, s12, 0\n"
"buffer_load_dwordx4 v[192:195], v1, s[16:19], s6 offen offset:0 sc1\nMaaa 8,136,176\nTa 212,17,16384\ns_lshl3_add_u32 s5, s5, s13\nMaaa 40,136,180\n"
"s_lshl4_add_u32 s54, s5, 0xc048400\nMaaa 72,136,184\nTa 214,17,18432\nMaaa 104,136,188\ns_cmp_eq_u32 s11, 0\ns_cselect_b32 s54, s54, 0x40000000\n"
"buffer_load_dwordx4 v[196:199], v1, s[16:19], s6 offen offset:1024 sc1\nMaaa 108,140,188\nTa 216,18,16384\ns_getpc_b64 s[8:9]\n.PC36:\ns_add_u32 s8, s8, .HD10-.PC36\n"
"s_addc_u32 s9, s9, 0\nMaaa 76,140,184\ns_mov_b32 s97, 0xff800000\ns_mov_b32 s90, 3\nMaaa 44,140,180\nTa 218,18,18432\ns_lshl_b32 s5, s11, 2\nMaaa 12,140,176\n"
"s_lshr_b32 s92, 0xf112, s5\ns_and_b32 s92, s92, 15\nbuffer_load_dwordx4 v[200:203], v1, s[16:19], s6 offen offset:2048 sc1\nMvaa 172,140,172\nTa 220,19,16384\n"
"s_lshr_b32 s93, s11, 1\nMvaa 140,140,168\ns_xor_b32 s93, s93, 1\ns_add_u32 s86, s2, 0x20000\nMvaa 108,140,164\nTa 222,19,18432\ns_add_u32 s87, s2, 0x410000\nMvaa 76,140,160\n"
"s_mov_b32 s94, s86\ns_mov_b32 s95, s87\nbuffer_load_dwordx4 v[204:207], v1, s[16:19], s6 offen offset:3072 sc1\nMvaa 80,144,160\nRa 224,11,0\nMvaa 112,144,164\n"
"s_cmp_eq_u32 s13, 7\ns_cselect_b32 s5, 0xfffffffd, 64\ns_add_u32 s6, s56, 4096\nMvaa 144,144,168\nRa 228,11,2048\ns_add_u32 s92, s92, s5\nMvaa 176,144,172\ns_add_u32 s93, s93, s5\n"
"s_or_b32 s92, s92, 0x100\nbuffer_load_dwordx4 v[208:211], v1, s[16:19], s6 offen offset:0 sc1\nMaaa 16,144,176\nRa 232,11,8192\nMaaa 48,144,180\ns_cmp_lt_u32 s12, 14\n"
"s_cselect_b32 s5, s11, 1\nMaaa 80,144,184\nRa 236,11,10240\nMaaa 112,144,188\ns_cmp_eq_u32 s5, 0\ns_cselect_b32 s91, 3, 100\n"
"buffer_load_dwordx4 v[212:215], v1, s[16:19], s6 offen offset:1024 sc1\nMaaa 116,148,188\nRa 240,11,16384\nMaaa 84,148,184\nv_and_b32 v27, 0x70, v1\n"
"buffer_load_dword v27, v27, s[16:19], s61 offen offset:128 sc0 sc1\nMaaa 52,148,180\nRa 244,11,18432\ns_lshr_b32 s2, s13, 2\nMaaa 20,148,176\ns_bfe_u32 s3, s13, 0x10001\n"
"s_mul_i32 s3, s3, 17\nbuffer_load_dwordx4 v[216:219], v1, s[16:19], s6 offen offset:2048 sc1\nMvaa 180,148,172\nRa 248,11,24576\ns_add_u32 s2, s2, s3\nMvaa 148,148,168\n"
"s_lshl2_add_u32 s3, s14, 0\ns_lshl_b32 s3, s3, 2\nMvaa 116,148,164\nRa 252,11,26624\ns_lshr_b32 s3, 0xe420ca86, s3\nMvaa 84,148,160\ns_and_b32 s3, s3, 15\ns_add_u32 s2, s2, s3\n"
"buffer_load_dwordx4 v[220:223], v1, s[16:19], s6 offen offset:3072 sc1\nMvaa 88,152,160\ns_cmp_eq_u32 s2, 32\ns_cselect_b32 s4, 16, s2\nMvaa 120,152,164\ns_mul_i32 s75, s4, 65536\n"
"s_lshr_b32 s2, s13, 2\ns_add_u32 s6, s56, 16384\nMvaa 152,152,168\ns_bfe_u32 s3, s13, 0x10001\ns_mul_i32 s3, s3, 17\nMvaa 184,152,172\ns_add_u32 s2, s2, s3\n"
"s_lshl2_add_u32 s3, s14, 1\nbuffer_load_dwordx4 v[224:227], v1, s[16:19], s6 offen offset:0 sc1\nMaaa 24,152,176\ns_lshl_b32 s3, s3, 2\ns_lshr_b32 s3, 0xe420ca86, s3\n"
"Maaa 56,152,180\ns_and_b32 s3, s3, 15\ns_add_u32 s2, s2, s3\nMaaa 88,152,184\ns_cmp_eq_u32 s2, 32\ns_cselect_b32 s4, 16, s2\nMaaa 120,152,188\ns_mul_i32 s76, s4, 65536\n"
"s_lshr_b32 s2, s13, 2\nbuffer_load_dwordx4 v[228:231], v1, s[16:19], s6 offen offset:1024 sc1\nMaaa 124,156,188\ns_bfe_u32 s3, s13, 0x10001\ns_mul_i32 s3, s3, 17\nMaaa 92,156,184\n"
"s_add_u32 s2, s2, s3\ns_lshl2_add_u32 s3, s14, 2\nMaaa 60,156,180\ns_lshl_b32 s3, s3, 2\ns_lshr_b32 s3, 0xe420ca86, s3\nMaaa 28,156,176\ns_and_b32 s3, s3, 15\n"
"s_add_u32 s2, s2, s3\nbuffer_load_dwordx4 v[232:235], v1, s[16:19], s6 offen offset:2048 sc1\nMvaa 188,156,172\ns_cmp_eq_u32 s2, 32\ns_cselect_b32 s4, 16, s2\nMvaa 156,156,168\n"
"s_mul_i32 s77, s4, 65536\ns_lshr_b32 s2, s13, 2\nMvaa 124,156,164\ns_bfe_u32 s3, s13, 0x10001\ns_mul_i32 s3, s3, 17\nMvaa 92,156,160\ns_waitcnt vmcnt(16) lgkmcnt(0)\ns_barrier\n"
"Mvaa 64,192,224\nTa 128,12,32768\ns_add_u32 s2, s2, s3\nbuffer_load_dwordx4 v[236:239], v1, s[16:19], s6 offen offset:3072 sc1\nMvaa 96,192,228\ns_lshl2_add_u32 s3, s14, 3\n"
"s_lshl_b32 s3, s3, 2\nMvaa 128,192,232\nTa 130,12,34816\ns_lshr_b32 s3, 0xe420ca86, s3\ns_add_u32 s6, s56, 20480\nMvaa 160,192,236\ns_and_b32 s3, s3, 15\ns_add_u32 s2, s2, s3\n"
"Maaa 0,192,240\nTa 132,13,32768\nbuffer_load_dwordx4 v[240:243], v1, s[16:19], s6 offen offset:0 sc1\nMaaa 32,192,244\ns_cmp_eq_u32 s2, 32\ns_cselect_b32 s4, 16, s2\n"
"Maaa 64,192,248\nTa 134,13,34816\ns_mul_i32 s78, s4, 65536\nMaaa 96,192,252\ns_lshr_b32 s3, s11, 1\ns_lshl_b32 s3, s3, 15\nMaaa 100,196,252\nTa 136,14,32768\n"
"s_add_u32 s2, s72, s3\nbuffer_load_dwordx4 v[244:247], v1, s[16:19], s6 offen offset:1024 sc1\nMaaa 68,196,248\ns_and_b32 s3, s11, 1\ns_lshl_b32 s3, s3, 12\nMaaa 36,196,244\n"
"Ta 138,14,34816\ns_add_u32 s2, s2, s3\nMaaa 4,196,240\ns_add_u32 s2, s2, 0x0\ns_mov_b32 s55, 16384\nMvaa 164,196,236\nTa 140,15,32768\ns_add_u32 s75, s75, s2\n"
"buffer_load_dwordx4 v[248:251], v1, s[16:19], s6 offen offset:2048 sc1\nMvaa 132,196,232\ns_add_u32 s76, s76, s2\ns_add_u32 s77, s77, s2\nMvaa 100,196,228\nTa 142,15,34816\n"
"s_add_u32 s78, s78, s2\nMvaa 68,196,224\nMvaa 72,200,224\nTa 144,16,32768\nbuffer_load_dwordx4 v[252:255], v1, s[16:19], s6 offen offset:3072 sc1\nMvaa 104,200,228\n"
"Mvaa 136,200,232\nTa 146,16,34816\nMvaa 168,200,236\nMaaa 8,200,240\nTa 148,17,32768\nMaaa 40,200,244\nMaaa 72,200,248\nTa 150,17,34816\nMaaa 104,200,252\nMaaa 108,204,252\n"
"Ta 152,18,32768\nMaaa 76,204,248\nMaaa 44,204,244\nTa 154,18,34816\nMaaa 12,204,240\nMvaa 172,204,236\nTa 156,19,32768\nMvaa 140,204,232\nMvaa 108,204,228\nTa 158,19,34816\n"
"Mvaa 76,204,224\nMvaa 80,208,224\nRa 160,10,32768\nMvaa 112,208,228\nMvaa 144,208,232\nRa 164,10,34816\nMvaa 176,208,236\nMaaa 16,208,240\nRa 168,10,40960\nMaaa 48,208,244\n"
"Maaa 80,208,248\nRa 172,10,43008\nMaaa 112,208,252\nMaaa 116,212,252\nRa 176,10,49152\nWV 10\nv_cmp_ne_u32 vcc, s15, v27\ns_nop 0\ns_cbranch_vccnz .S37\n.S38:\n"
"s_sub_u32 s45, s86, 131072\nMaaa 84,212,248\ns_add_u32 m0, s47, 0\ns_add_u32 s46, s45, 0x0\nMaaa 52,212,244\nRa 180,10,51200\nMaaa 20,212,240\nD1 1,16,46,0\nMvaa 180,212,236\n"
"Ra 184,10,57344\nMvaa 148,212,232\nMvaa 116,212,228\nRa 188,10,59392\nMvaa 84,212,224\nD1 1,16,46,1024\nMvaa 88,216,224\nMvaa 120,216,228\nMvaa 152,216,232\nMvaa 184,216,236\n"
"D1 1,16,46,2048\nMaaa 24,216,240\nMaaa 56,216,244\nMaaa 88,216,248\nMaaa 120,216,252\nD1 1,16,46,3072\nMaaa 124,220,252\nMaaa 92,220,248\nMaaa 60,220,244\nMaaa 28,220,240\n"
"Mvaa 188,220,236\nMvaa 156,220,232\nMvaa 124,220,228\nMvaa 92,220,224\nWL 0\ns_barrier\nMvaa 64,128,160\nTa 192,12,49152\nMvaa 96,128,164\ns_add_u32 m0, s47, 32768\n"
"s_add_u32 s46, s45, 0x8000\nMvaa 128,128,168\nTa 194,12,51200\nMvaa 160,128,172\nD1 1,16,46,0\nMaaa 0,128,176\nTa 196,13,49152\nMaaa 32,128,180\nMaaa 64,128,184\nTa 198,13,51200\n"
"Maaa 96,128,188\nD1 1,16,46,1024\nMaaa 100,132,188\nTa 200,14,49152\nMaaa 68,132,184\nMaaa 36,132,180\nTa 202,14,51200\nMaaa 4,132,176\nD1 1,16,46,2048\nMvaa 164,132,172\n"
"Ta 204,15,49152\nMvaa 132,132,168\nMvaa 100,132,164\nTa 206,15,51200\nMvaa 68,132,160\nD1 1,16,46,3072\nMvaa 72,136,160\nTa 208,16,49152\nMvaa 104,136,164\n"
"s_add_u32 m0, s47, -65536\ns_add_u32 s46, s45, 0x10000\nMvaa 136,136,168\nTa 210,16,51200\nMvaa 168,136,172\nD1 1,16,46,0\nMaaa 8,136,176\nTa 212,17,49152\nMaaa 40,136,180\n"
"Maaa 72,136,184\nTa 214,17,51200\nMaaa 104,136,188\nD1 1,16,46,1024\nMaaa 108,140,188\nTa 216,18,49152\nMaaa 76,140,184\nMaaa 44,140,180\nTa 218,18,51200\nMaaa 12,140,176\n"
"D1 1,16,46,2048\nMvaa 172,140,172\nTa 220,19,49152\nMvaa 140,140,168\nMvaa 108,140,164\nTa 222,19,51200\nMvaa 76,140,160\nD1 1,16,46,3072\nMvaa 80,144,160\nRa 224,11,32768\n"
"Mvaa 112,144,164\ns_add_u32 m0, s47, -49152\ns_add_u32 s46, s45, 0x408000\nMvaa 144,144,168\nRa 228,11,34816\nMvaa 176,144,172\nD1 1,16,46,0\nMaaa 16,144,176\nRa 232,11,40960\n"
"Maaa 48,144,180\nMaaa 80,144,184\nRa 236,11,43008\nMaaa 112,144,188\nD1 1,16,46,1024\nMaaa 116,148,188\nRa 240,11,49152\nMaaa 84,148,184\nMaaa 52,148,180\nRa 244,11,51200\n"
"Maaa 20,148,176\nD1 1,16,46,2048\nMvaa 180,148,172\nRa 248,11,57344\nMvaa 148,148,168\nMvaa 116,148,164\nRa 252,11,59392\nMvaa 84,148,160\nD1 1,16,46,3072\nMvaa 88,152,160\n"
"Mvaa 120,152,164\nv_add_u32 v2, 0x10000, v1\ns_lshr_b32 s2, s59, 1\nMvaa 152,152,168\nv_add_u32 v24, s2, v2\nv_xor_b32 v26, 0x1000, v24\nMvaa 184,152,172\n"
"v_lshrrev_b32 v20, 4, v0\nv_lshlrev_b32 v20, 2, v20\nMaaa 24,152,176\nv_and_b32 v0, 15, v0\nv_sub_u32 v0, v0, v20\nMaaa 56,152,180\ns_and_b32 s2, s11, 1\ns_lshl_b32 s2, s2, 6\n"
"Maaa 88,152,184\nv_subrev_u32 v3, s2, v0\ns_xor_b32 s2, s2, 64\nMaaa 120,152,188\nv_subrev_u32 v0, s2, v0\nv_mov_b32 v4, 0\nMaaa 124,156,188\nv_mov_b32 v5, 0\nv_mov_b32 v6, 0\n"
"Maaa 92,156,184\nv_mov_b32 v7, 0\nv_mov_b32 v8, 0\nMaaa 60,156,180\nv_mov_b32 v9, 0\nv_mov_b32 v10, 0\nMaaa 28,156,176\nv_mov_b32 v11, 0\nv_mov_b32 v12, 0\nMvaa 188,156,172\n"
"v_mov_b32 v13, 0\nv_mov_b32 v14, 0\nMvaa 156,156,168\nv_mov_b32 v15, 0\nv_mov_b32 v16, 0\nMvaa 124,156,164\nv_mov_b32 v17, 0\nv_mov_b32 v18, 0\nMvaa 92,156,160\n"
"s_waitcnt vmcnt(12) lgkmcnt(0)\ns_barrier\nMvaa 64,192,224\nv_mov_b32 v19, 0\nMvaa 68,196,224\ns_add_u32 m0, s47, 49152\ns_add_u32 s46, s45, 0x400000\nMvaa 72,200,224\n"
"Mvaa 76,204,224\nD1 1,16,46,0\nMvaa 80,208,224\nMvaa 84,212,224\nMvaa 88,216,224\nCV 32,64,65\nCV 33,66,67\nMvaa 92,220,224\nD1 1,16,46,1024\nCV 34,68,69\nMvaa 96,192,228\n"
"CV 35,70,71\nCV 36,72,73\nMvaa 100,196,228\nCV 37,74,75\nCV 38,76,77\nMvaa 104,200,228\nCV 39,78,79\nCV 40,80,81\nMvaa 108,204,228\nD1 1,16,46,2048\nCV 41,82,83\nMvaa 112,208,228\n"
"CV 42,84,85\nCV 43,86,87\nMvaa 116,212,228\nCV 44,88,89\nCV 45,90,91\nMvaa 120,216,228\nCV 46,92,93\nCV 47,94,95\nMvaa 124,220,228\nD1 1,16,46,3072\nCV 48,96,97\nMvaa 128,192,232\n"
"CV 49,98,99\nCV 50,100,101\nMvaa 132,196,232\nCV 51,102,103\nCV 52,104,105\nMvaa 136,200,232\nCV 53,106,107\nCV 54,108,109\nMvaa 140,204,232\nCV 55,110,111\nCV 56,112,113\n"
"Mvaa 144,208,232\nCV 57,114,115\nCV 58,116,117\nMvaa 148,212,232\nCV 59,118,119\nCV 60,120,121\nMvaa 152,216,232\nCV 61,122,123\nCV 62,124,125\nMvaa 156,220,232\nCV 63,126,127\n"
"CV 96,128,129\nMvaa 160,192,236\nCV 97,130,131\nCV 98,132,133\nMvaa 164,196,236\nCV 99,134,135\nCV 100,136,137\nSv 32,1,16,75,0\nMvaa 168,200,236\nCV 101,138,139\nCV 102,140,141\n"
"Mvaa 172,204,236\nCV 103,142,143\nCV 104,144,145\nMvaa 176,208,236\nCV 105,146,147\nCV 106,148,149\nMvaa 180,212,236\nCV 107,150,151\nCV 108,152,153\nSv 36,1,16,75,1024\n"
"Mvaa 184,216,236\nCV 109,154,155\nCV 110,156,157\nMvaa 188,220,236\nCV 111,158,159\nCV 112,160,161\nMaaa 0,192,240\nCV 113,162,163\nCV 114,164,165\nMaaa 4,196,240\nCV 115,166,167\n"
"CV 116,168,169\nSv 40,1,16,75,2048\nMaaa 8,200,240\nCV 117,170,171\nCV 118,172,173\nMaaa 12,204,240\nCV 119,174,175\nCV 120,176,177\nMaaa 16,208,240\nCV 121,178,179\n"
"CV 122,180,181\nMaaa 20,212,240\nCV 123,182,183\nCV 124,184,185\nSv 44,1,16,75,3072\nMaaa 24,216,240\nCV 125,186,187\nCV 126,188,189\nMaaa 28,220,240\nCV 127,190,191\n"
"v_mov_b32 v86, 0\ns_add_u32 s75, s75, s55\nMaaa 32,192,244\nv_mov_b32 v87, 0\nv_mov_b32 v88, 0\nMaaa 36,196,244\nv_mov_b32 v89, 0\nSv 48,1,16,75,0\nMaaa 40,200,244\nAR 92,0\n"
"AR 93,1\nMaaa 44,204,244\nCV 128,92,93\nAR 92,2\nMaaa 48,208,244\nAR 93,3\nCV 129,92,93\nMaaa 52,212,244\nSv 52,1,16,75,1024\nAR 92,4\nMaaa 56,216,244\nAR 93,5\nCV 130,92,93\n"
"Maaa 60,220,244\nAR 92,6\nAR 93,7\nMaaa 64,192,248\nRa 128,24,0\nCV 131,92,93\nMaaa 68,196,248\nSv 56,1,16,75,2048\nAR 92,8\nMaaa 72,200,248\nRa 132,24,1024\nAR 93,9\n"
"Maaa 76,204,248\nCV 132,92,93\nAR 92,10\nMaaa 80,208,248\nRa 136,24,2048\nAR 93,11\nMaaa 84,212,248\nSv 60,1,16,75,3072\nCV 133,92,93\nMaaa 88,216,248\nRa 140,24,3072\nAR 92,12\n"
"Maaa 92,220,248\nAR 93,13\nCV 134,92,93\nMaaa 96,192,252\nRa 144,26,0\nAR 92,14\nMaaa 100,196,252\nAR 93,15\nCV 135,92,93\nMaaa 104,200,252\nRa 148,26,1024\nMaaa 108,204,252\n"
"Maaa 112,208,252\nRa 152,26,2048\nMaaa 116,212,252\nMaaa 120,216,252\nRa 156,26,3072\nMaaa 124,220,252\nWL 4\ns_branch .F07\n.S29:\ns_mov_b32 s7, 262144\n.S31:\ns_sleep 8\n"
"buffer_load_dwordx4 v[32:35], v1, s[16:19], s6 offen offset:0 sc0 sc1\nbuffer_load_dwordx4 v[36:39], v1, s[16:19], s6 offen offset:1024 sc0 sc1\n"
"buffer_load_dwordx4 v[40:43], v1, s[16:19], s6 offen offset:2048 sc0 sc1\nbuffer_load_dwordx4 v[44:47], v1, s[16:19], s6 offen offset:3072 sc0 sc1\nWV 0\nv_xor_b32 v48, s15, v33\n"
"v_xor_b32 v49, s15, v35\nv_xor_b32 v50, s15, v37\nv_xor_b32 v51, s15, v39\nv_xor_b32 v52, s15, v41\nv_xor_b32 v53, s15, v43\nv_xor_b32 v54, s15, v45\nv_xor_b32 v55, s15, v47\n"
"v_or3_b32 v48, v48, v49, v50\nv_or3_b32 v51, v51, v52, v53\nv_or3_b32 v48, v48, v54, v55\nv_or_b32 v48, v48, v51\nv_cmp_ne_u32 vcc, 0, v48\ns_nop 0\ns_cbranch_vccz .S30\n"
"s_sub_u32 s7, s7, 1\ns_cbranch_scc0 .S31\ns_branch .S30\n.S32:\ns_mov_b32 s7, 262144\n.S34:\ns_sleep 8\ns_lshl_b32 s5, s73, 7\ns_add_u32 s5, s5, 0xc04ac00\n"
"v_bfe_u32 v27, v1, 2, 7\nbuffer_load_dword v27, v27, s[16:19], s5 offen sc0 sc1\nWV 0\nv_xor_b32 v27, s15, v27\nv_and_b32 v255, 0xffffdfff, v27\nv_cmp_ne_u32 vcc, 0, v255\n"
"s_nop 0\ns_cbranch_vccz .S33\ns_sub_u32 s7, s7, 1\ns_cbranch_scc0 .S34\ns_branch .S33\n.S37:\ns_mov_b32 s7, 262144\n.S39:\ns_sleep 8\nv_and_b32 v27, 0x70, v1\n"
"buffer_load_dword v27, v27, s[16:19], s61 offen offset:128 sc0 sc1\nWV 0\nv_cmp_ne_u32 vcc, s15, v27\ns_nop 0\ns_cbranch_vccz .S38\ns_sub_u32 s7, s7, 1\ns_cbranch_scc0 .S39\n"
"s_branch .S38\n.MV3:\ns_mov_b32 s50, 2\ns_mov_b32 s88, 0\ns_mov_b32 s89, 0\n.GV43:\nMvaa 64,160,128\nTa 192,12,16384\nMvaa 68,164,128\ns_mov_b32 s86, 0x1d2c3b4a\n"
"s_load_dword s86, s[54:55], s94\nMvaa 128,168,128\nTa 194,12,18432\ns_add_u32 s94, s94, s92\nMvaa 132,172,128\ns_add_u32 m0, s47, 49152\ns_add_u32 s44, s44, 0x40000\n"
"Maaa 0,176,128\nTa 196,13,16384\nMaaa 4,180,128\nD0 6,24,44,0\nMaaa 64,184,128\nTa 198,13,18432\nMaaa 68,188,128\ns_cmp_eq_u32 s86, 0x1d2c3b4a\ns_addc_u32 s88, s88, 0\n"
"Maaa 76,188,132\nTa 200,14,16384\nMaaa 72,184,132\nMaaa 12,180,132\nTa 202,14,18432\nMaaa 8,176,132\ns_cmp_eq_u32 s86, 0x1d2c3b4a\ns_addc_u32 s88, s88, 0\nMvaa 140,172,132\n"
"Ta 204,15,16384\nMvaa 136,168,132\nD0 7,24,44,1024\nMvaa 76,164,132\nTa 206,15,18432\nMvaa 72,160,132\ns_cmp_eq_u32 s86, 0x1d2c3b4a\ns_addc_u32 s88, s88, 0\nMvaa 80,160,136\n"
"Ta 208,16,16384\nMvaa 84,164,136\nMvaa 144,168,136\nTa 210,16,18432\nMvaa 148,172,136\ns_cmp_eq_u32 s86, 0x1d2c3b4a\ns_addc_u32 s88, s88, 0\nMaaa 16,176,136\nTa 212,17,16384\n"
"Maaa 20,180,136\nD0 8,24,44,2048\nMaaa 80,184,136\nTa 214,17,18432\nMaaa 84,188,136\ns_cmp_eq_u32 s86, 0x1d2c3b4a\ns_addc_u32 s88, s88, 0\nMaaa 92,188,140\nTa 216,18,16384\n"
"Maaa 88,184,140\nMaaa 28,180,140\nTa 218,18,18432\nMaaa 24,176,140\ns_cmp_eq_u32 s86, 0x1d2c3b4a\ns_addc_u32 s88, s88, 0\nMvaa 156,172,140\nTa 220,19,16384\nMvaa 152,168,140\n"
"D0 9,24,44,3072\nMvaa 92,164,140\nTa 222,19,18432\nMvaa 88,160,140\nbuffer_load_dword v31, v24, s[24:27], s96 offen\ns_add_u32 s96, s96, 0x80000\ns_cmp_eq_u32 s86, 0x1d2c3b4a\n"
"s_addc_u32 s88, s88, 0\nMvaa 96,160,144\nRa 224,11,0\nMvaa 100,164,144\nMvaa 160,168,144\nRa 228,11,2048\nMvaa 164,172,144\ns_add_u32 m0, s47, 0\ns_add_u32 s44, s44, 0x40000\n"
"s_cmp_eq_u32 s86, 0x1d2c3b4a\ns_addc_u32 s88, s88, 0\nMaaa 32,176,144\nRa 232,11,8192\nMaaa 36,180,144\nD0 6,24,44,0\nMaaa 96,184,144\nRa 236,11,10240\nMaaa 100,188,144\n"
"s_cmp_eq_u32 s86, 0x1d2c3b4a\ns_addc_u32 s88, s88, 0\nMaaa 108,188,148\nRa 240,11,16384\nMaaa 104,184,148\nMaaa 44,180,148\nRa 244,11,18432\nMaaa 40,176,148\n"
"s_cmp_eq_u32 s86, 0x1d2c3b4a\ns_addc_u32 s88, s88, 0\nMvaa 172,172,148\nRa 248,11,24576\nMvaa 168,168,148\nD0 7,24,44,1024\nMvaa 108,164,148\nRa 252,11,26624\nMvaa 104,160,148\n"
"s_cmp_eq_u32 s86, 0x1d2c3b4a\ns_addc_u32 s88, s88, 0\nMvaa 112,160,152\nMvaa 116,164,152\nMvaa 176,168,152\nMvaa 180,172,152\ns_cmp_eq_u32 s86, 0x1d2c3b4a\ns_addc_u32 s88, s88, 0\n"
"Maaa 48,176,152\nMaaa 52,180,152\nD0 8,24,44,2048\nMaaa 112,184,152\nMaaa 116,188,152\ns_cmp_eq_u32 s86, 0x1d2c3b4a\ns_addc_u32 s88, s88, 0\nMaaa 124,188,156\nMaaa 120,184,156\n"
"Maaa 60,180,156\nMaaa 56,176,156\ns_cmp_eq_u32 s86, 0x1d2c3b4a\ns_addc_u32 s88, s88, 0\nMvaa 188,172,156\nMvaa 184,168,156\nD0 9,24,44,3072\nMvaa 124,164,156\nMvaa 120,160,156\n"
"s_waitcnt vmcnt(9) lgkmcnt(0)\ns_barrier\nMvaa 64,224,192\nTa 128,12,32768\nMvaa 68,228,192\ns_mov_b32 s86, 0x1d2c3b4a\ns_load_dword s86, s[54:55], s95\nMvaa 128,232,192\n"
"Ta 130,12,34816\ns_add_u32 s95, s95, s92\nMvaa 132,236,192\ns_add_u32 m0, s48, 0\ns_add_u32 s45, s45, 128\nMaaa 0,240,192\nTa 132,13,32768\nMaaa 4,244,192\nD0 2,20,45,0\n"
"Maaa 64,248,192\nTa 134,13,34816\nMaaa 68,252,192\ns_cmp_eq_u32 s86, 0x1d2c3b4a\ns_addc_u32 s89, s89, 0\nMaaa 76,252,196\nTa 136,14,32768\nMaaa 72,248,196\nMaaa 12,244,196\n"
"Ta 138,14,34816\nMaaa 8,240,196\ns_cmp_eq_u32 s86, 0x1d2c3b4a\ns_addc_u32 s89, s89, 0\nMvaa 140,236,196\nTa 140,15,32768\nMvaa 136,232,196\nD0 3,20,45,1024\nMvaa 76,228,196\n"
"Ta 142,15,34816\nMvaa 72,224,196\ns_cmp_eq_u32 s86, 0x1d2c3b4a\ns_addc_u32 s89, s89, 0\nMvaa 80,224,200\nTa 144,16,32768\nMvaa 84,228,200\nMvaa 144,232,200\nTa 146,16,34816\n"
"Mvaa 148,236,200\ns_cmp_eq_u32 s86, 0x1d2c3b4a\ns_addc_u32 s89, s89, 0\nMaaa 16,240,200\nTa 148,17,32768\nMaaa 20,244,200\nD0 4,20,45,2048\nMaaa 80,248,200\nTa 150,17,34816\n"
"Maaa 84,252,200\ns_cmp_eq_u32 s86, 0x1d2c3b4a\ns_addc_u32 s89, s89, 0\nMaaa 92,252,204\nTa 152,18,32768\nMaaa 88,248,204\nMaaa 28,244,204\nTa 154,18,34816\nMaaa 24,240,204\n"
"s_cmp_eq_u32 s86, 0x1d2c3b4a\ns_addc_u32 s89, s89, 0\nMvaa 156,236,204\nTa 156,19,32768\nMvaa 152,232,204\nD0 5,20,45,3072\nMvaa 92,228,204\nTa 158,19,34816\nMvaa 88,224,204\n"
"s_cmp_eq_u32 s86, 0x1d2c3b4a\ns_addc_u32 s89, s89, 0\nMvaa 96,224,208\nRa 160,10,32768\nMvaa 100,228,208\nMvaa 160,232,208\nRa 164,10,34816\nMvaa 164,236,208\n"
"s_add_u32 m0, s48, 4096\ns_add_u32 s46, s45, 0x10000\ns_cmp_eq_u32 s86, 0x1d2c3b4a\ns_addc_u32 s89, s89, 0\nMaaa 32,240,208\nRa 168,10,40960\nMaaa 36,244,208\nD0 2,20,46,0\n"
"Maaa 96,248,208\nRa 172,10,43008\nMaaa 100,252,208\ns_cmp_eq_u32 s86, 0x1d2c3b4a\ns_addc_u32 s89, s89, 0\nMaaa 108,252,212\nRa 176,10,49152\nMaaa 104,248,212\nMaaa 44,244,212\n"
"Ra 180,10,51200\nMaaa 40,240,212\ns_cmp_eq_u32 s86, 0x1d2c3b4a\ns_addc_u32 s89, s89, 0\nMvaa 172,236,212\nRa 184,10,57344\nMvaa 168,232,212\nD0 3,20,46,1024\nMvaa 108,228,212\n"
"Ra 188,10,59392\nMvaa 104,224,212\ns_cmp_eq_u32 s86, 0x1d2c3b4a\ns_addc_u32 s89, s89, 0\nMvaa 112,224,216\nMvaa 116,228,216\nMvaa 176,232,216\nMvaa 180,236,216\n"
"s_cmp_eq_u32 s86, 0x1d2c3b4a\ns_addc_u32 s89, s89, 0\nMaaa 48,240,216\nMaaa 52,244,216\nD0 4,20,46,2048\nMaaa 112,248,216\nMaaa 116,252,216\ns_cmp_eq_u32 s86, 0x1d2c3b4a\n"
"s_addc_u32 s89, s89, 0\nMaaa 124,252,220\nMaaa 120,248,220\nMaaa 60,244,220\nMaaa 56,240,220\ns_cmp_eq_u32 s86, 0x1d2c3b4a\ns_addc_u32 s89, s89, 0\nMvaa 188,236,220\n"
"Mvaa 184,232,220\nD0 5,20,46,3072\nMvaa 124,228,220\nMvaa 120,224,220\ns_waitcnt vmcnt(13) lgkmcnt(0)\ns_barrier\nMvaa 64,160,128\nTa 192,12,49152\nMvaa 68,164,128\n"
"s_mov_b32 s86, 0x1d2c3b4a\ns_load_dword s86, s[54:55], s95\nMvaa 128,168,128\nTa 194,12,51200\ns_add_u32 s95, s95, s92\nMvaa 132,172,128\ns_add_u32 m0, s47, 16384\n"
"s_add_u32 s44, s44, 0x40000\nMaaa 0,176,128\nTa 196,13,49152\nMaaa 4,180,128\nD0 6,24,44,0\nMaaa 64,184,128\nTa 198,13,51200\nMaaa 68,188,128\ns_cmp_eq_u32 s86, 0x1d2c3b4a\n"
"s_addc_u32 s89, s89, 0\nMaaa 76,188,132\nTa 200,14,49152\nMaaa 72,184,132\nMaaa 12,180,132\nTa 202,14,51200\nMaaa 8,176,132\ns_cmp_eq_u32 s86, 0x1d2c3b4a\ns_addc_u32 s89, s89, 0\n"
"Mvaa 140,172,132\nTa 204,15,49152\nMvaa 136,168,132\nD0 7,24,44,1024\nMvaa 76,164,132\nTa 206,15,51200\nMvaa 72,160,132\ns_cmp_eq_u32 s86, 0x1d2c3b4a\ns_addc_u32 s89, s89, 0\n"
"Mvaa 80,160,136\nTa 208,16,49152\nMvaa 84,164,136\nMvaa 144,168,136\nTa 210,16,51200\nMvaa 148,172,136\ns_cmp_eq_u32 s86, 0x1d2c3b4a\ns_addc_u32 s89, s89, 0\nMaaa 16,176,136\n"
"Ta 212,17,49152\nMaaa 20,180,136\nD0 8,24,44,2048\nMaaa 80,184,136\nTa 214,17,51200\nMaaa 84,188,136\ns_cmp_eq_u32 s86, 0x1d2c3b4a\ns_addc_u32 s89, s89, 0\nMaaa 92,188,140\n"
"Ta 216,18,49152\nMaaa 88,184,140\nMaaa 28,180,140\nTa 218,18,51200\nMaaa 24,176,140\ns_cmp_eq_u32 s86, 0x1d2c3b4a\ns_addc_u32 s89, s89, 0\nMvaa 156,172,140\nTa 220,19,49152\n"
"Mvaa 152,168,140\nD0 9,24,44,3072\nMvaa 92,164,140\nTa 222,19,51200\nMvaa 88,160,140\nbuffer_load_dword v31, v24, s[24:27], s96 offen\ns_add_u32 s96, s96, 0x80000\n"
"s_cmp_eq_u32 s86, 0x1d2c3b4a\ns_addc_u32 s89, s89, 0\nMvaa 96,160,144\nRa 224,11,32768\nMvaa 100,164,144\nMvaa 160,168,144\nRa 228,11,34816\nMvaa 164,172,144\n"
"s_add_u32 m0, s47, 32768\ns_add_u32 s44, s44, 0x40000\ns_cmp_eq_u32 s86, 0x1d2c3b4a\ns_addc_u32 s89, s89, 0\nMaaa 32,176,144\nRa 232,11,40960\nMaaa 36,180,144\nD0 6,24,44,0\n"
"Maaa 96,184,144\nRa 236,11,43008\nMaaa 100,188,144\ns_cmp_eq_u32 s86, 0x1d2c3b4a\ns_addc_u32 s89, s89, 0\nMaaa 108,188,148\nRa 240,11,49152\nMaaa 104,184,148\nMaaa 44,180,148\n"
"Ra 244,11,51200\nMaaa 40,176,148\ns_cmp_eq_u32 s86, 0x1d2c3b4a\ns_addc_u32 s89, s89, 0\nMvaa 172,172,148\nRa 248,11,57344\nMvaa 168,168,148\nD0 7,24,44,1024\nMvaa 108,164,148\n"
"Ra 252,11,59392\nMvaa 104,160,148\ns_cmp_eq_u32 s86, 0x1d2c3b4a\ns_addc_u32 s89, s89, 0\nMvaa 112,160,152\nMvaa 116,164,152\nMvaa 176,168,152\nMvaa 180,172,152\n"
"s_cmp_eq_u32 s86, 0x1d2c3b4a\ns_addc_u32 s89, s89, 0\nMaaa 48,176,152\nMaaa 52,180,152\nD0 8,24,44,2048\nMaaa 112,184,152\nMaaa 116,188,152\ns_cmp_eq_u32 s86, 0x1d2c3b4a\n"
"s_addc_u32 s89, s89, 0\nMaaa 124,188,156\nMaaa 120,184,156\nMaaa 60,180,156\nMaaa 56,176,156\ns_cmp_eq_u32 s86, 0x1d2c3b4a\ns_addc_u32 s89, s89, 0\nMvaa 188,172,156\n"
"Mvaa 184,168,156\nD0 9,24,44,3072\nMvaa 124,164,156\nMvaa 120,160,156\ns_waitcnt vmcnt(9) lgkmcnt(0)\ns_barrier\nMvaa 64,224,192\nTa 128,12,0\nMvaa 68,228,192\n"
"s_mov_b32 s86, 0x1d2c3b4a\ns_load_dword s86, s[54:55], s94\nMvaa 128,232,192\nTa 130,12,2048\ns_add_u32 s94, s94, s92\nMvaa 132,236,192\ns_add_u32 m0, s48, 32768\n"
"s_add_u32 s45, s45, 128\nMaaa 0,240,192\nTa 132,13,0\nMaaa 4,244,192\nD0 2,20,45,0\nMaaa 64,248,192\nTa 134,13,2048\nMaaa 68,252,192\ns_cmp_eq_u32 s86, 0x1d2c3b4a\n"
"s_addc_u32 s88, s88, 0\nMaaa 76,252,196\nTa 136,14,0\nMaaa 72,248,196\nMaaa 12,244,196\nTa 138,14,2048\nMaaa 8,240,196\ns_cmp_eq_u32 s86, 0x1d2c3b4a\ns_addc_u32 s88, s88, 0\n"
"Mvaa 140,236,196\nTa 140,15,0\nMvaa 136,232,196\nD0 3,20,45,1024\nMvaa 76,228,196\nTa 142,15,2048\nMvaa 72,224,196\ns_cmp_eq_u32 s86, 0x1d2c3b4a\ns_addc_u32 s88, s88, 0\n"
"Mvaa 80,224,200\nTa 144,16,0\nMvaa 84,228,200\nMvaa 144,232,200\nTa 146,16,2048\nMvaa 148,236,200\ns_cmp_eq_u32 s86, 0x1d2c3b4a\ns_addc_u32 s88, s88, 0\nMaaa 16,240,200\n"
"Ta 148,17,0\nMaaa 20,244,200\nD0 4,20,45,2048\nMaaa 80,248,200\nTa 150,17,2048\nMaaa 84,252,200\ns_cmp_eq_u32 s86, 0x1d2c3b4a\ns_addc_u32 s88, s88, 0\nMaaa 92,252,204\n"
"Ta 152,18,0\nMaaa 88,248,204\nMaaa 28,244,204\nTa 154,18,2048\nMaaa 24,240,204\ns_cmp_eq_u32 s86, 0x1d2c3b4a\ns_addc_u32 s88, s88, 0\nMvaa 156,236,204\nTa 156,19,0\n"
"Mvaa 152,232,204\nD0 5,20,45,3072\nMvaa 92,228,204\nTa 158,19,2048\nMvaa 88,224,204\ns_cmp_eq_u32 s86, 0x1d2c3b4a\ns_addc_u32 s88, s88, 0\nMvaa 96,224,208\nRa 160,10,0\n"
"Mvaa 100,228,208\nMvaa 160,232,208\nRa 164,10,2048\nMvaa 164,236,208\ns_add_u32 m0, s48, 36864\ns_add_u32 s46, s45, 0x10000\ns_cmp_eq_u32 s86, 0x1d2c3b4a\ns_addc_u32 s88, s88, 0\n"
"Maaa 32,240,208\nRa 168,10,8192\nMaaa 36,244,208\nD0 2,20,46,0\nMaaa 96,248,208\nRa 172,10,10240\nMaaa 100,252,208\ns_cmp_eq_u32 s86, 0x1d2c3b4a\ns_addc_u32 s88, s88, 0\n"
"Maaa 108,252,212\nRa 176,10,16384\nMaaa 104,248,212\nMaaa 44,244,212\nRa 180,10,18432\nMaaa 40,240,212\ns_cmp_eq_u32 s86, 0x1d2c3b4a\ns_addc_u32 s88, s88, 0\nMvaa 172,236,212\n"
"Ra 184,10,24576\nMvaa 168,232,212\nD0 3,20,46,1024\nMvaa 108,228,212\nRa 188,10,26624\nMvaa 104,224,212\ns_cmp_eq_u32 s86, 0x1d2c3b4a\ns_addc_u32 s88, s88, 0\nMvaa 112,224,216\n"
"Mvaa 116,228,216\nMvaa 176,232,216\nMvaa 180,236,216\ns_cmp_eq_u32 s86, 0x1d2c3b4a\ns_addc_u32 s88, s88, 0\nMaaa 48,240,216\nMaaa 52,244,216\nD0 4,20,46,2048\nMaaa 112,248,216\n"
"Maaa 116,252,216\ns_cmp_eq_u32 s86, 0x1d2c3b4a\ns_addc_u32 s88, s88, 0\nMaaa 124,252,220\nMaaa 120,248,220\nMaaa 60,244,220\nMaaa 56,240,220\ns_cmp_eq_u32 s86, 0x1d2c3b4a\n"
"s_addc_u32 s88, s88, 0\nMvaa 188,236,220\nMvaa 184,232,220\nD0 5,20,46,3072\nMvaa 124,228,220\nMvaa 120,224,220\ns_waitcnt vmcnt(13) lgkmcnt(0)\ns_barrier\ns_sub_u32 s50, s50, 1\n"
"s_cbranch_scc0 .GV43\nMvaa 64,160,128\nTa 192,12,16384\nMvaa 68,164,128\ns_cmp_lt_u32 s89, s88\ns_cselect_b32 s88, 0x2000, 0\nMvaa 128,168,128\nTa 194,12,18432\n"
"v_xor_b32 v27, s88, v25\nMvaa 132,172,128\ns_add_u32 m0, s47, 49152\ns_add_u32 s44, s44, 0x40000\nMaaa 0,176,128\nTa 196,13,16384\n"
"buffer_store_dword v27, off, s[16:19], s93 sc0 sc1\nMaaa 4,180,128\nD0 6,24,44,0\nMaaa 64,184,128\nTa 198,13,18432\nMaaa 68,188,128\nMaaa 76,188,132\nTa 200,14,16384\n"
"Maaa 72,184,132\nbuffer_store_dword v25, off, s[16:19], s90 offset:0 sc0 sc1\nMaaa 12,180,132\nTa 202,14,18432\nMaaa 8,176,132\nMvaa 140,172,132\nTa 204,15,16384\n"
"Mvaa 136,168,132\nD0 7,24,44,1024\nMvaa 76,164,132\nTa 206,15,18432\nMvaa 72,160,132\nMvaa 80,160,136\nTa 208,16,16384\nMvaa 84,164,136\nMvaa 144,168,136\nTa 210,16,18432\n"
"Mvaa 148,172,136\nMaaa 16,176,136\nTa 212,17,16384\nMaaa 20,180,136\nD0 8,24,44,2048\nMaaa 80,184,136\nTa 214,17,18432\nMaaa 84,188,136\nMaaa 92,188,140\nTa 216,18,16384\n"
"Maaa 88,184,140\nMaaa 28,180,140\nTa 218,18,18432\nMaaa 24,176,140\nMvaa 156,172,140\nTa 220,19,16384\nMvaa 152,168,140\nD0 9,24,44,3072\nMvaa 92,164,140\nTa 222,19,18432\n"
"Mvaa 88,160,140\nbuffer_load_dword v31, v24, s[24:27], s96 offen\ns_add_u32 s96, s96, 0x80000\nMvaa 96,160,144\nRa 224,11,0\nMvaa 100,164,144\nMvaa 160,168,144\nRa 228,11,2048\n"
"Mvaa 164,172,144\ns_add_u32 m0, s47, 0\ns_add_u32 s44, s44, 0x40000\nMaaa 32,176,144\nRa 232,11,8192\nMaaa 36,180,144\nD0 6,24,44,0\nMaaa 96,184,144\nRa 236,11,10240\n"
"Maaa 100,188,144\nMaaa 108,188,148\nRa 240,11,16384\nMaaa 104,184,148\nMaaa 44,180,148\nRa 244,11,18432\nMaaa 40,176,148\nMvaa 172,172,148\nRa 248,11,24576\nMvaa 168,168,148\n"
"D0 7,24,44,1024\nMvaa 108,164,148\nRa 252,11,26624\nMvaa 104,160,148\nMvaa 112,160,152\nMvaa 116,164,152\nMvaa 176,168,152\nMvaa 180,172,152\nMaaa 48,176,152\nMaaa 52,180,152\n"
"D0 8,24,44,2048\nMaaa 112,184,152\nMaaa 116,188,152\nMaaa 124,188,156\nMaaa 120,184,156\nMaaa 60,180,156\nMaaa 56,176,156\nMvaa 188,172,156\nMvaa 184,168,156\nD0 9,24,44,3072\n"
"Mvaa 124,164,156\nMvaa 120,160,156\ns_waitcnt vmcnt(11) lgkmcnt(0)\ns_barrier\nMvaa 64,224,192\nTa 128,12,32768\nMvaa 68,228,192\nMvaa 128,232,192\nTa 130,12,34816\n"
"Mvaa 132,236,192\ns_add_u32 m0, s48, 0\ns_add_u32 s45, s45, 128\nMaaa 0,240,192\nTa 132,13,32768\nMaaa 4,244,192\nD0 2,20,45,0\nMaaa 64,248,192\nTa 134,13,34816\nMaaa 68,252,192\n"
"Maaa 76,252,196\nTa 136,14,32768\nMaaa 72,248,196\nMaaa 12,244,196\nTa 138,14,34816\nMaaa 8,240,196\nMvaa 140,236,196\nTa 140,15,32768\nMvaa 136,232,196\nD0 3,20,45,1024\n"
"Mvaa 76,228,196\nTa 142,15,34816\nMvaa 72,224,196\nMvaa 80,224,200\nTa 144,16,32768\nMvaa 84,228,200\nMvaa 144,232,200\nTa 146,16,34816\nMvaa 148,236,200\nMaaa 16,240,200\n"
"Ta 148,17,32768\nMaaa 20,244,200\nD0 4,20,45,2048\nMaaa 80,248,200\nTa 150,17,34816\nMaaa 84,252,200\nMaaa 92,252,204\nTa 152,18,32768\nMaaa 88,248,204\nMaaa 28,244,204\n"
"Ta 154,18,34816\nMaaa 24,240,204\nMvaa 156,236,204\nTa 156,19,32768\nMvaa 152,232,204\nD0 5,20,45,3072\nMvaa 92,228,204\nTa 158,19,34816\nMvaa 88,224,204\nMvaa 96,224,208\n"
"Ra 160,10,32768\nMvaa 100,228,208\nMvaa 160,232,208\nRa 164,10,34816\nMvaa 164,236,208\ns_add_u32 m0, s48, 4096\ns_add_u32 s46, s45, 0x10000\nMaaa 32,240,208\nRa 168,10,40960\n"
"Maaa 36,244,208\nD0 2,20,46,0\nMaaa 96,248,208\nRa 172,10,43008\nMaaa 100,252,208\nMaaa 108,252,212\nRa 176,10,49152\nMaaa 104,248,212\nMaaa 44,244,212\nRa 180,10,51200\n"
"Maaa 40,240,212\nMvaa 172,236,212\nRa 184,10,57344\nMvaa 168,232,212\nD0 3,20,46,1024\nMvaa 108,228,212\nRa 188,10,59392\nMvaa 104,224,212\nMvaa 112,224,216\nMvaa 116,228,216\n"
"Mvaa 176,232,216\nMvaa 180,236,216\nMaaa 48,240,216\nMaaa 52,244,216\nD0 4,20,46,2048\nMaaa 112,248,216\nMaaa 116,252,216\nMaaa 124,252,220\nMaaa 120,248,220\nMaaa 60,244,220\n"
"Maaa 56,240,220\nMvaa 188,236,220\nMvaa 184,232,220\nD0 5,20,46,3072\nMvaa 124,228,220\nMvaa 120,224,220\ns_waitcnt vmcnt(13) lgkmcnt(0)\ns_barrier\nMvaa 64,160,128\n"
"Ta 192,12,49152\nMvaa 68,164,128\nMvaa 128,168,128\nTa 194,12,51200\nMvaa 132,172,128\ns_add_u32 m0, s47, 16384\ns_add_u32 s44, s44, 0x40000\nMaaa 0,176,128\nTa 196,13,49152\n"
"Maaa 4,180,128\nD0 6,24,44,0\nMaaa 64,184,128\nTa 198,13,51200\nMaaa 68,188,128\nMaaa 76,188,132\nTa 200,14,49152\nMaaa 72,184,132\nMaaa 12,180,132\nTa 202,14,51200\n"
"Maaa 8,176,132\nMvaa 140,172,132\nTa 204,15,49152\nMvaa 136,168,132\nD0 7,24,44,1024\nMvaa 76,164,132\nTa 206,15,51200\nMvaa 72,160,132\nMvaa 80,160,136\nTa 208,16,49152\n"
"Mvaa 84,164,136\nMvaa 144,168,136\nTa 210,16,51200\nMvaa 148,172,136\nMaaa 16,176,136\nTa 212,17,49152\nMaaa 20,180,136\nD0 8,24,44,2048\nMaaa 80,184,136\nTa 214,17,51200\n"
"Maaa 84,188,136\nMaaa 92,188,140\nTa 216,18,49152\nMaaa 88,184,140\nMaaa 28,180,140\nTa 218,18,51200\nMaaa 24,176,140\nMvaa 156,172,140\nTa 220,19,49152\nMvaa 152,168,140\n"
"D0 9,24,44,3072\nMvaa 92,164,140\nTa 222,19,51200\nMvaa 88,160,140\nbuffer_load_dword v31, v24, s[24:27], s96 offen\ns_add_u32 s96, s96, 0x80000\nMvaa 96,160,144\nRa 224,11,32768\n"
"Mvaa 100,164,144\ns_lshl_b32 s5, s73, 7\ns_add_u32 s5, s5, 0xc04ac00\nv_bfe_u32 v27, v1, 2, 7\nbuffer_load_dword v27, v27, s[16:19], s5 offen sc0 sc1\nMvaa 160,168,144\n"
"Ra 228,11,34816\nMvaa 164,172,144\ns_add_u32 m0, s47, 32768\ns_add_u32 s44, s44, 0x40000\nMaaa 32,176,144\nRa 232,11,40960\nMaaa 36,180,144\nD0 6,24,44,0\nMaaa 96,184,144\n"
"Ra 236,11,43008\nMaaa 100,188,144\nMaaa 108,188,148\nRa 240,11,49152\nMaaa 104,184,148\nMaaa 44,180,148\nRa 244,11,51200\nMaaa 40,176,148\nMvaa 172,172,148\nRa 248,11,57344\n"
"Mvaa 168,168,148\nD0 7,24,44,1024\nMvaa 108,164,148\nRa 252,11,59392\nMvaa 104,160,148\nMvaa 112,160,152\nMvaa 116,164,152\nMvaa 176,168,152\nMvaa 180,172,152\nMaaa 48,176,152\n"
"Maaa 52,180,152\nD0 8,24,44,2048\nMaaa 112,184,152\nMaaa 116,188,152\nMaaa 124,188,156\nMaaa 120,184,156\nMaaa 60,180,156\nMaaa 56,176,156\nMvaa 188,172,156\nMvaa 184,168,156\n"
"D0 9,24,44,3072\nMvaa 124,164,156\nMvaa 120,160,156\ns_waitcnt vmcnt(10) lgkmcnt(0)\ns_barrier\nMvaa 64,224,192\nTa 128,12,0\nMvaa 68,228,192\nMvaa 128,232,192\nTa 130,12,2048\n"
"Mvaa 132,236,192\ns_add_u32 m0, s48, 32768\ns_add_u32 s45, s45, 128\nMaaa 0,240,192\nTa 132,13,0\nMaaa 4,244,192\nD0 2,20,45,0\nMaaa 64,248,192\nTa 134,13,2048\nMaaa 68,252,192\n"
"Maaa 76,252,196\nTa 136,14,0\nMaaa 72,248,196\nMaaa 12,244,196\nTa 138,14,2048\nMaaa 8,240,196\nMvaa 140,236,196\nTa 140,15,0\nMvaa 136,232,196\nD0 3,20,45,1024\nMvaa 76,228,196\n"
"Ta 142,15,2048\nMvaa 72,224,196\nMvaa 80,224,200\nTa 144,16,0\nMvaa 84,228,200\nMvaa 144,232,200\nTa 146,16,2048\nMvaa 148,236,200\nMaaa 16,240,200\nTa 148,17,0\nMaaa 20,244,200\n"
"D0 4,20,45,2048\nMaaa 80,248,200\nTa 150,17,2048\nMaaa 84,252,200\nMaaa 92,252,204\nTa 152,18,0\nMaaa 88,248,204\nMaaa 28,244,204\nTa 154,18,2048\nMaaa 24,240,204\n"
"Mvaa 156,236,204\nTa 156,19,0\nMvaa 152,232,204\nD0 5,20,45,3072\nMvaa 92,228,204\nTa 158,19,2048\nMvaa 88,224,204\nMvaa 96,224,208\nRa 160,10,0\nMvaa 100,228,208\n"
"Mvaa 160,232,208\nRa 164,10,2048\nMvaa 164,236,208\ns_add_u32 m0, s48, 36864\ns_add_u32 s46, s45, 0x10000\nMaaa 32,240,208\nRa 168,10,8192\nWV 8\nv_xor_b32 v27, s15, v27\n"
"v_and_b32 v32, 0xffffdfff, v27\nv_cmp_ne_u32 vcc, 0, v32\ns_nop 0\ns_cbranch_vccnz .S40\n.S41:\nv_cmp_ne_u32 vcc, 0, v27\ns_nop 3\ns_bcnt1_i32_b64 s4, vcc\ns_cmp_gt_u32 s4, 32\n"
"s_cselect_b32 s4, 1, 0\nMaaa 36,244,208\nD0 2,20,46,0\ns_xor_b32 s4, s4, s12\nMaaa 96,248,208\nRa 172,10,10240\ns_and_b32 s4, s4, 1\nMaaa 100,252,208\ns_lshl_b32 s4, s4, 13\n"
"s_mul_i32 s72, s73, 25165824\nMaaa 108,252,212\nRa 176,10,16384\ns_add_u32 s72, s72, s4\nMaaa 104,248,212\ns_and_b32 s4, s12, 1\ns_lshl_b32 s4, s4, 21\nMaaa 44,244,212\n"
"Ra 180,10,18432\ns_add_u32 s72, s72, s4\nMaaa 40,240,212\ns_lshl3_add_u32 s2, s13, s11\ns_lshl_b32 s2, s2, 11\nMvaa 172,236,212\nRa 184,10,24576\ns_lshl_b32 s3, s14, 22\n"
"Mvaa 168,232,212\nD0 3,20,46,1024\ns_add_u32 s2, s2, s3\nMvaa 108,228,212\nRa 188,10,26624\ns_add_u32 s2, s2, s72\nMvaa 104,224,212\ns_add_u32 s49, s2, 0xc00000\nMvaa 112,224,216\n"
"Mvaa 116,228,216\nMvaa 176,232,216\nMvaa 180,236,216\nMaaa 48,240,216\nMaaa 52,244,216\nD0 4,20,46,2048\nMaaa 112,248,216\nMaaa 116,252,216\nMaaa 124,252,220\nMaaa 120,248,220\n"
"Maaa 60,244,220\nMaaa 56,240,220\nMvaa 188,236,220\nMvaa 184,232,220\nD0 5,20,46,3072\nMvaa 124,228,220\nMvaa 120,224,220\nWL 0\ns_barrier\nMvaa 64,160,128\nTa 192,12,16384\n"
"s_mov_b32 s36, s60\nMvaa 68,164,128\ns_and_b32 s37, s61, 0xffff\ns_mov_b32 s38, 0x380000\nMvaa 128,168,128\nTa 194,12,18432\ns_mov_b32 s39, 0x20000\nMvaa 132,172,128\n"
"s_add_u32 m0, s47, 49152\ns_add_u32 s44, s44, 0x40000\nMaaa 0,176,128\nTa 196,13,16384\ns_lshr_b32 s4, s13, 1\nMaaa 4,180,128\nD0 6,24,44,0\ns_lshl2_add_u32 s4, s14, s4\n"
"Maaa 64,184,128\nTa 198,13,18432\ns_lshl3_add_u32 s2, s12, s4\nMaaa 68,188,128\ns_lshl2_add_u32 s2, s2, s11\ns_mul_i32 s2, s2, 7168\nMaaa 76,188,132\nTa 200,14,16384\n"
"s_mul_i32 s3, s11, 7168\nMaaa 72,184,132\ns_add_u32 s3, s3, 131072\nMaaa 12,180,132\nTa 202,14,18432\nMaaa 8,176,132\nMvaa 140,172,132\nTa 204,15,16384\nMvaa 136,168,132\n"
"D0 7,24,44,1024\nMvaa 76,164,132\nTa 206,15,18432\nMvaa 72,160,132\nMvaa 80,160,136\nTa 208,16,16384\nMvaa 84,164,136\nMvaa 144,168,136\nTa 210,16,18432\nMvaa 148,172,136\n"
"Maaa 16,176,136\nTa 212,17,16384\nMaaa 20,180,136\nD0 8,24,44,2048\nMaaa 80,184,136\nTa 214,17,18432\nMaaa 84,188,136\nMaaa 92,188,140\nTa 216,18,16384\nMaaa 88,184,140\n"
"Maaa 28,180,140\nTa 218,18,18432\nMaaa 24,176,140\nMvaa 156,172,140\nTa 220,19,16384\nMvaa 152,168,140\nD0 9,24,44,3072\nMvaa 92,164,140\nTa 222,19,18432\nMvaa 88,160,140\n"
"buffer_load_dword v31, v24, s[24:27], s96 offen\ns_add_u32 s96, s96, 0x80000\nMvaa 96,160,144\nRa 224,11,0\ns_add_u32 m0, s3, 0\ns_add_u32 s5, s2, 0\nD0 1,36,5,0\nD0 1,36,5,1024\n"
"Mvaa 100,164,144\nMvaa 160,168,144\nRa 228,11,2048\nMvaa 164,172,144\ns_add_u32 m0, s47, 0\ns_add_u32 s44, s44, 0x40000\nMaaa 32,176,144\nRa 232,11,8192\nMaaa 36,180,144\n"
"D0 6,24,44,0\nMaaa 96,184,144\nRa 236,11,10240\nMaaa 100,188,144\nMaaa 108,188,148\nRa 240,11,16384\nMaaa 104,184,148\nMaaa 44,180,148\nRa 244,11,18432\nMaaa 40,176,148\n"
"Mvaa 172,172,148\nRa 248,11,24576\nMvaa 168,168,148\nD0 7,24,44,1024\nMvaa 108,164,148\nRa 252,11,26624\nMvaa 104,160,148\nMvaa 112,160,152\nMvaa 116,164,152\nMvaa 176,168,152\n"
"Mvaa 180,172,152\nMaaa 48,176,152\nMaaa 52,180,152\nD0 8,24,44,2048\nMaaa 112,184,152\nMaaa 116,188,152\nMaaa 124,188,156\nMaaa 120,184,156\nMaaa 60,180,156\nMaaa 56,176,156\n"
"Mvaa 188,172,156\nMvaa 184,168,156\nD0 9,24,44,3072\nMvaa 124,164,156\ns_add_u32 m0, s3, 2048\ns_add_u32 s5, s2, 2048\nD0 1,36,5,0\nD0 1,36,5,1024\nMvaa 120,160,156\n"
"s_waitcnt vmcnt(13) lgkmcnt(0)\ns_barrier\nMvaa 64,224,192\nTa 128,12,32768\nMvaa 68,228,192\nMvaa 128,232,192\nTa 130,12,34816\nMvaa 132,236,192\ns_add_u32 m0, s48, 0\n"
"s_add_u32 s45, s45, 128\nMaaa 0,240,192\nTa 132,13,32768\nMaaa 4,244,192\nD0 2,20,45,0\nMaaa 64,248,192\nTa 134,13,34816\nMaaa 68,252,192\nMaaa 76,252,196\nTa 136,14,32768\n"
"Maaa 72,248,196\nMaaa 12,244,196\nTa 138,14,34816\nMaaa 8,240,196\nMvaa 140,236,196\nTa 140,15,32768\nMvaa 136,232,196\nD0 3,20,45,1024\nMvaa 76,228,196\nTa 142,15,34816\n"
"Mvaa 72,224,196\nMvaa 80,224,200\nTa 144,16,32768\nMvaa 84,228,200\nMvaa 144,232,200\nTa 146,16,34816\nMvaa 148,236,200\nMaaa 16,240,200\nTa 148,17,32768\nMaaa 20,244,200\n"
"D0 4,20,45,2048\nMaaa 80,248,200\nTa 150,17,34816\nMaaa 84,252,200\nMaaa 92,252,204\nTa 152,18,32768\nMaaa 88,248,204\nMaaa 28,244,204\nTa 154,18,34816\nMaaa 24,240,204\n"
"Mvaa 156,236,204\nTa 156,19,32768\nMvaa 152,232,204\nD0 5,20,45,3072\nMvaa 92,228,204\nTa 158,19,34816\nMvaa 88,224,204\nMvaa 96,224,208\nRa 160,10,32768\nMvaa 100,228,208\n"
"Mvaa 160,232,208\nRa 164,10,34816\nMvaa 164,236,208\ns_add_u32 m0, s48, 4096\ns_add_u32 s46, s45, 0x10000\nMaaa 32,240,208\nRa 168,10,40960\nMaaa 36,244,208\nD0 2,20,46,0\n"
"Maaa 96,248,208\nRa 172,10,43008\nMaaa 100,252,208\nMaaa 108,252,212\nRa 176,10,49152\nMaaa 104,248,212\nMaaa 44,244,212\nRa 180,10,51200\nMaaa 40,240,212\nMvaa 172,236,212\n"
"Ra 184,10,57344\nMvaa 168,232,212\nD0 3,20,46,1024\nMvaa 108,228,212\nRa 188,10,59392\nMvaa 104,224,212\nMvaa 112,224,216\nMvaa 116,228,216\nMvaa 176,232,216\nMvaa 180,236,216\n"
"Maaa 48,240,216\nMaaa 52,244,216\nD0 4,20,46,2048\nMaaa 112,248,216\nMaaa 116,252,216\nMaaa 124,252,220\nMaaa 120,248,220\nMaaa 60,244,220\nMaaa 56,240,220\nMvaa 188,236,220\n"
"Mvaa 184,232,220\nD0 5,20,46,3072\nMvaa 124,228,220\nMvaa 120,224,220\ns_waitcnt vmcnt(17) lgkmcnt(0)\ns_barrier\nMvaa 64,160,128\nTa 192,12,49152\nMvaa 68,164,128\n"
"Mvaa 128,168,128\nTa 194,12,51200\nMvaa 132,172,128\ns_add_u32 m0, s47, 16384\ns_add_u32 s44, s44, 0x40000\nMaaa 0,176,128\nTa 196,13,49152\nMaaa 4,180,128\nD0 6,24,44,0\n"
"Maaa 64,184,128\nTa 198,13,51200\nMaaa 68,188,128\nMaaa 76,188,132\nTa 200,14,49152\nMaaa 72,184,132\nMaaa 12,180,132\nTa 202,14,51200\nMaaa 8,176,132\nMvaa 140,172,132\n"
"Ta 204,15,49152\nMvaa 136,168,132\nD0 7,24,44,1024\nMvaa 76,164,132\nTa 206,15,51200\nMvaa 72,160,132\nMvaa 80,160,136\nTa 208,16,49152\nMvaa 84,164,136\nMvaa 144,168,136\n"
"Ta 210,16,51200\nMvaa 148,172,136\nMaaa 16,176,136\nTa 212,17,49152\nMaaa 20,180,136\nD0 8,24,44,2048\nMaaa 80,184,136\nTa 214,17,51200\nMaaa 84,188,136\nMaaa 92,188,140\n"
"Ta 216,18,49152\nMaaa 88,184,140\nMaaa 28,180,140\nTa 218,18,51200\nMaaa 24,176,140\nMvaa 156,172,140\nTa 220,19,49152\nMvaa 152,168,140\nD0 9,24,44,3072\nMvaa 92,164,140\n"
"Ta 222,19,51200\nMvaa 88,160,140\nbuffer_load_dword v31, v24, s[24:27], s96 offen\ns_add_u32 s96, s96, 0x80000\nMvaa 96,160,144\nRa 224,11,32768\ns_add_u32 m0, s3, 4096\n"
"s_add_u32 s5, s2, 4096\nD0 1,36,5,0\nD0 1,36,5,1024\nMvaa 100,164,144\nMvaa 160,168,144\nRa 228,11,34816\nMvaa 164,172,144\ns_add_u32 m0, s47, 32768\ns_add_u32 s44, s44, 0x40000\n"
"Maaa 32,176,144\nRa 232,11,40960\nMaaa 36,180,144\nD0 6,24,44,0\nMaaa 96,184,144\nRa 236,11,43008\nMaaa 100,188,144\nMaaa 108,188,148\nRa 240,11,49152\nMaaa 104,184,148\n"
"Maaa 44,180,148\nRa 244,11,51200\nMaaa 40,176,148\nMvaa 172,172,148\nRa 248,11,57344\nMvaa 168,168,148\nD0 7,24,44,1024\nMvaa 108,164,148\nRa 252,11,59392\nMvaa 104,160,148\n"
"Mvaa 112,160,152\nMvaa 116,164,152\nMvaa 176,168,152\nMvaa 180,172,152\nMaaa 48,176,152\nMaaa 52,180,152\nD0 8,24,44,2048\nMaaa 112,184,152\nMaaa 116,188,152\nMaaa 124,188,156\n"
"Maaa 120,184,156\nMaaa 60,180,156\nMaaa 56,176,156\nMvaa 188,172,156\nMvaa 184,168,156\nD0 9,24,44,3072\nMvaa 124,164,156\ns_add_u32 m0, s3, 6144\ns_add_u32 s5, s2, 6144\n"
"D0 1,36,5,0\nMvaa 120,160,156\ns_waitcnt vmcnt(12) lgkmcnt(0)\ns_barrier\nMvaa 64,224,192\nTa 128,12,0\nMvaa 68,228,192\nMvaa 128,232,192\nTa 130,12,2048\nMvaa 132,236,192\n"
"s_add_u32 m0, s48, 32768\ns_add_u32 s45, s45, 128\nMaaa 0,240,192\nTa 132,13,0\nMaaa 4,244,192\nD0 2,20,45,0\nMaaa 64,248,192\nTa 134,13,2048\nMaaa 68,252,192\nMaaa 76,252,196\n"
"Ta 136,14,0\nMaaa 72,248,196\nMaaa 12,244,196\nTa 138,14,2048\nMaaa 8,240,196\nMvaa 140,236,196\nTa 140,15,0\nMvaa 136,232,196\nD0 3,20,45,1024\nMvaa 76,228,196\nTa 142,15,2048\n"
"Mvaa 72,224,196\nMvaa 80,224,200\nTa 144,16,0\nMvaa 84,228,200\nMvaa 144,232,200\nTa 146,16,2048\nMvaa 148,236,200\nMaaa 16,240,200\nTa 148,17,0\nMaaa 20,244,200\nD0 4,20,45,2048\n"
"Maaa 80,248,200\nTa 150,17,2048\nMaaa 84,252,200\nMaaa 92,252,204\nTa 152,18,0\nMaaa 88,248,204\nMaaa 28,244,204\nTa 154,18,2048\nMaaa 24,240,204\nMvaa 156,236,204\nTa 156,19,0\n"
"Mvaa 152,232,204\nD0 5,20,45,3072\nMvaa 92,228,204\nTa 158,19,2048\nMvaa 88,224,204\nMvaa 96,224,208\nRa 160,10,0\nMvaa 100,228,208\nMvaa 160,232,208\nRa 164,10,2048\n"
"Mvaa 164,236,208\ns_add_u32 m0, s48, 36864\ns_add_u32 s46, s45, 0x10000\nMaaa 32,240,208\nRa 168,10,8192\nMaaa 36,244,208\nD0 2,20,46,0\nMaaa 96,248,208\nRa 172,10,10240\n"
"Maaa 100,252,208\nMaaa 108,252,212\nRa 176,10,16384\nMaaa 104,248,212\nMaaa 44,244,212\nRa 180,10,18432\nMaaa 40,240,212\nMvaa 172,236,212\nRa 184,10,24576\nMvaa 168,232,212\n"
"D0 3,20,46,1024\nMvaa 108,228,212\nRa 188,10,26624\nMvaa 104,224,212\nMvaa 112,224,216\nMvaa 116,228,216\nMvaa 176,232,216\nMvaa 180,236,216\nMaaa 48,240,216\nMaaa 52,244,216\n"
"D0 4,20,46,2048\nMaaa 112,248,216\nMaaa 116,252,216\nMaaa 124,252,220\nMaaa 120,248,220\nMaaa 60,244,220\nMaaa 56,240,220\nMvaa 188,236,220\nMvaa 184,232,220\nD0 5,20,46,3072\n"
"Mvaa 124,228,220\nMvaa 120,224,220\ns_waitcnt vmcnt(16) lgkmcnt(0)\ns_barrier\nMvaa 64,160,128\nTa 192,12,16384\nMvaa 68,164,128\nMvaa 128,168,128\nTa 194,12,18432\n"
"Mvaa 132,172,128\ns_add_u32 m0, s47, 49152\ns_add_u32 s44, s44, 0x40000\nMaaa 0,176,128\nTa 196,13,16384\nMaaa 4,180,128\nD0 6,24,44,0\nMaaa 64,184,128\nTa 198,13,18432\n"
"Maaa 68,188,128\nMaaa 76,188,132\nTa 200,14,16384\nMaaa 72,184,132\nMaaa 12,180,132\nTa 202,14,18432\nMaaa 8,176,132\nMvaa 140,172,132\nTa 204,15,16384\nMvaa 136,168,132\n"
"D0 7,24,44,1024\nMvaa 76,164,132\nTa 206,15,18432\nMvaa 72,160,132\nMvaa 80,160,136\nTa 208,16,16384\nMvaa 84,164,136\nMvaa 144,168,136\nTa 210,16,18432\nMvaa 148,172,136\n"
"Maaa 16,176,136\nTa 212,17,16384\nMaaa 20,180,136\nD0 8,24,44,2048\nMaaa 80,184,136\nTa 214,17,18432\nMaaa 84,188,136\nMaaa 92,188,140\nTa 216,18,16384\nMaaa 88,184,140\n"
"Maaa 28,180,140\nTa 218,18,18432\nMaaa 24,176,140\nMvaa 156,172,140\nTa 220,19,16384\nMvaa 152,168,140\nD0 9,24,44,3072\nMvaa 92,164,140\nTa 222,19,18432\nMvaa 88,160,140\n"
"Mvaa 96,160,144\nRa 224,11,0\nMvaa 100,164,144\nMvaa 160,168,144\nRa 228,11,2048\nMvaa 164,172,144\ns_add_u32 m0, s47, 0\ns_add_u32 s44, s44, 0x40000\nMaaa 32,176,144\n"
"Ra 232,11,8192\nMaaa 36,180,144\nD0 6,24,44,0\nMaaa 96,184,144\nRa 236,11,10240\nMaaa 100,188,144\nMaaa 108,188,148\nRa 240,11,16384\nMaaa 104,184,148\nMaaa 44,180,148\n"
"Ra 244,11,18432\nMaaa 40,176,148\nMvaa 172,172,148\nRa 248,11,24576\nMvaa 168,168,148\nD0 7,24,44,1024\nMvaa 108,164,148\nRa 252,11,26624\nMvaa 104,160,148\nMvaa 112,160,152\n"
"Mvaa 116,164,152\nMvaa 176,168,152\nMvaa 180,172,152\nMaaa 48,176,152\nMaaa 52,180,152\nD0 8,24,44,2048\nMaaa 112,184,152\nMaaa 116,188,152\nMaaa 124,188,156\nMaaa 120,184,156\n"
"Maaa 60,180,156\nMaaa 56,176,156\nMvaa 188,172,156\nMvaa 184,168,156\nD0 9,24,44,3072\nMvaa 124,164,156\nMvaa 120,160,156\ns_waitcnt vmcnt(8) lgkmcnt(0)\ns_barrier\n"
"Mvaa 64,224,192\nTa 128,12,32768\nv_lshrrev_b32 v32, 2, v0\nMvaa 68,228,192\nv_mul_u32_u24 v32, 896, v32\nv_and_b32 v33, 3, v0\nMvaa 128,232,192\nTa 130,12,34816\n"
"v_lshl_add_u32 v32, v33, 4, v32\nMvaa 132,236,192\ns_add_u32 m0, s48, 0\ns_add_u32 s45, s45, 128\nMaaa 0,240,192\nTa 132,13,32768\ns_lshr_b32 s2, s11, 1\nMaaa 4,244,192\n"
"D0 2,20,45,0\ns_mul_i32 s3, s2, 448\nMaaa 64,248,192\nTa 134,13,34816\ns_and_b32 s5, s11, 1\nMaaa 68,252,192\ns_lshl3_add_u32 s3, s5, s3\ns_add_u32 s3, s3, 131072\n"
"Maaa 76,252,196\nTa 136,14,32768\nv_add_u32 v32, s3, v32\nMaaa 72,248,196\nTv 36,32,0\nTv 38,32,14336\nMaaa 12,244,196\nTa 138,14,34816\nTv 40,32,64\nMaaa 8,240,196\n"
"Tv 42,32,14400\nTv 44,32,128\nMvaa 140,236,196\nTa 140,15,32768\nTv 46,32,14464\nMvaa 136,232,196\nD0 3,20,45,1024\nTv 48,32,192\nMvaa 76,228,196\nTa 142,15,34816\nTv 50,32,14528\n"
"Mvaa 72,224,196\nTv 52,32,256\nTv 54,32,14592\nMvaa 80,224,200\nTa 144,16,32768\nTv 56,32,320\nMvaa 84,228,200\nTv 58,32,14656\nTv 60,32,384\nMvaa 144,232,200\nTa 146,16,34816\n"
"Tv 62,32,14720\nMvaa 148,236,200\ns_mul_i32 s3, s2, 229376\ns_add_u32 s3, s3, s72\nMaaa 16,240,200\nTa 148,17,32768\ns_add_u32 s3, s3, 0x1400000\nMaaa 20,244,200\nD0 4,20,45,2048\n"
"s_lshr_b32 s0, s4, 1\nMaaa 80,248,200\nTa 150,17,34816\ns_mul_i32 s0, s0, 28\nMaaa 84,252,200\ns_lshl1_add_u32 s0, s5, s0\ns_and_b32 s6, s4, 1\nMaaa 92,252,204\nTa 152,18,32768\n"
"s_add_u32 s0, s0, s6\nMaaa 88,248,204\ns_lshr_b32 s1, s4, 2\ns_mul_i32 s1, s1, 56\nMaaa 28,244,204\nTa 154,18,34816\ns_lshl2_add_u32 s1, s5, s1\nMaaa 24,240,204\n"
"s_and_b32 s6, s4, 3\ns_add_u32 s1, s1, s6\nMvaa 156,236,204\nTa 156,19,32768\ns_and_b32 s4, s12, 1\nMvaa 152,232,204\nD0 5,20,45,3072\ns_mov_b32 s7, 0x6543210\nMvaa 92,228,204\n"
"Ta 158,19,34816\ns_cmp_eq_u32 s4, s2\ns_cselect_b32 s6, s0, s1\ns_cselect_b32 s5, 4, 8\ns_cselect_b32 s7, 0x6352410, s7\nMvaa 88,224,204\nWL 14\ns_lshr_b32 s0, s6, 4\n"
"Mvaa 96,224,208\nRa 160,10,32768\ns_lshl_b32 s0, s0, 2\nMvaa 100,228,208\ns_lshr_b32 s0, s7, s0\ns_and_b32 s0, s0, 7\nMvaa 160,232,208\nRa 164,10,34816\ns_lshl_b32 s0, s0, 5\n"
"Mvaa 164,236,208\ns_add_u32 m0, s48, 4096\ns_add_u32 s46, s45, 0x10000\nMaaa 32,240,208\nRa 168,10,40960\ns_and_b32 s1, s6, 15\nMaaa 36,244,208\nD0 2,20,46,0\n"
"s_add_u32 s0, s0, s1\nMaaa 96,248,208\nRa 172,10,43008\ns_and_b32 s1, s6, 8\nMaaa 100,252,208\ns_add_u32 s0, s0, s1\ns_lshl_b32 s0, s0, 10\nMaaa 108,252,212\nRa 176,10,49152\n"
"s_add_u32 s0, s0, s3\nMaaa 104,248,212\nSv 36,1,16,0,0\ns_add_u32 s6, s6, s5\nMaaa 44,244,212\nRa 180,10,51200\nMaaa 40,240,212\nMvaa 172,236,212\nRa 184,10,57344\n"
"Mvaa 168,232,212\nD0 3,20,46,1024\nMvaa 108,228,212\nRa 188,10,59392\nMvaa 104,224,212\nMvaa 112,224,216\nMvaa 116,228,216\ns_lshr_b32 s0, s6, 4\ns_lshl_b32 s0, s0, 2\n"
"Mvaa 176,232,216\ns_lshr_b32 s0, s7, s0\ns_and_b32 s0, s0, 7\nMvaa 180,236,216\ns_lshl_b32 s0, s0, 5\ns_and_b32 s1, s6, 15\nMaaa 48,240,216\ns_add_u32 s0, s0, s1\n"
"s_and_b32 s1, s6, 8\nMaaa 52,244,216\nD0 4,20,46,2048\ns_add_u32 s0, s0, s1\nMaaa 112,248,216\ns_lshl_b32 s0, s0, 10\ns_add_u32 s0, s0, s3\nMaaa 116,252,216\nSv 40,1,16,0,0\n"
"s_add_u32 s6, s6, s5\nMaaa 124,252,220\nMaaa 120,248,220\nMaaa 60,244,220\nMaaa 56,240,220\nMvaa 188,236,220\nMvaa 184,232,220\nD0 5,20,46,3072\nMvaa 124,228,220\n"
"Mvaa 120,224,220\ns_waitcnt vmcnt(14) lgkmcnt(0)\ns_barrier\nMvaa 64,160,128\nTa 192,12,49152\ns_lshr_b32 s0, s6, 4\nMvaa 68,164,128\ns_lshl_b32 s0, s0, 2\ns_lshr_b32 s0, s7, s0\n"
"Mvaa 128,168,128\nTa 194,12,51200\ns_and_b32 s0, s0, 7\nMvaa 132,172,128\ns_add_u32 m0, s47, 16384\ns_add_u32 s44, s44, 0x40000\nMaaa 0,176,128\nTa 196,13,49152\n"
"s_lshl_b32 s0, s0, 5\nMaaa 4,180,128\nD0 6,24,44,0\ns_and_b32 s1, s6, 15\nMaaa 64,184,128\nTa 198,13,51200\ns_add_u32 s0, s0, s1\nMaaa 68,188,128\ns_and_b32 s1, s6, 8\n"
"s_add_u32 s0, s0, s1\nMaaa 76,188,132\nTa 200,14,49152\ns_lshl_b32 s0, s0, 10\nMaaa 72,184,132\ns_add_u32 s0, s0, s3\nSv 44,1,16,0,0\nMaaa 12,180,132\nTa 202,14,51200\n"
"s_add_u32 s6, s6, s5\nMaaa 8,176,132\nMvaa 140,172,132\nTa 204,15,49152\nMvaa 136,168,132\nD0 7,24,44,1024\nMvaa 76,164,132\nTa 206,15,51200\nMvaa 72,160,132\nMvaa 80,160,136\n"
"Ta 208,16,49152\nMvaa 84,164,136\nMvaa 144,168,136\nTa 210,16,51200\ns_lshr_b32 s0, s6, 4\nMvaa 148,172,136\ns_lshl_b32 s0, s0, 2\ns_lshr_b32 s0, s7, s0\nMaaa 16,176,136\n"
"Ta 212,17,49152\ns_and_b32 s0, s0, 7\nMaaa 20,180,136\nD0 8,24,44,2048\ns_lshl_b32 s0, s0, 5\nMaaa 80,184,136\nTa 214,17,51200\ns_and_b32 s1, s6, 15\nMaaa 84,188,136\n"
"s_add_u32 s0, s0, s1\ns_and_b32 s1, s6, 8\nMaaa 92,188,140\nTa 216,18,49152\ns_add_u32 s0, s0, s1\nMaaa 88,184,140\ns_lshl_b32 s0, s0, 10\ns_add_u32 s0, s0, s3\nMaaa 28,180,140\n"
"Ta 218,18,51200\nSv 48,1,16,0,0\nMaaa 24,176,140\ns_add_u32 s6, s6, s5\nMvaa 156,172,140\nTa 220,19,49152\nMvaa 152,168,140\nD0 9,24,44,3072\nMvaa 92,164,140\nTa 222,19,51200\n"
"Mvaa 88,160,140\nMvaa 96,160,144\nRa 224,11,32768\nMvaa 100,164,144\nMvaa 160,168,144\nRa 228,11,34816\nMvaa 164,172,144\ns_add_u32 m0, s47, 32768\ns_add_u32 s44, s44, 0x40000\n"
"Maaa 32,176,144\nRa 232,11,40960\ns_lshr_b32 s0, s6, 4\nMaaa 36,180,144\nD0 6,24,44,0\ns_lshl_b32 s0, s0, 2\nMaaa 96,184,144\nRa 236,11,43008\ns_lshr_b32 s0, s7, s0\n"
"Maaa 100,188,144\ns_and_b32 s0, s0, 7\ns_lshl_b32 s0, s0, 5\nMaaa 108,188,148\nRa 240,11,49152\ns_and_b32 s1, s6, 15\nMaaa 104,184,148\ns_add_u32 s0, s0, s1\ns_and_b32 s1, s6, 8\n"
"Maaa 44,180,148\nRa 244,11,51200\ns_add_u32 s0, s0, s1\nMaaa 40,176,148\ns_lshl_b32 s0, s0, 10\ns_add_u32 s0, s0, s3\nMvaa 172,172,148\nRa 248,11,57344\nSv 52,1,16,0,0\n"
"Mvaa 168,168,148\nD0 7,24,44,1024\ns_add_u32 s6, s6, s5\nMvaa 108,164,148\nRa 252,11,59392\nMvaa 104,160,148\nMvaa 112,160,152\nMvaa 116,164,152\nMvaa 176,168,152\n"
"Mvaa 180,172,152\nMaaa 48,176,152\nMaaa 52,180,152\nD0 8,24,44,2048\ns_lshr_b32 s0, s6, 4\nMaaa 112,184,152\ns_lshl_b32 s0, s0, 2\ns_lshr_b32 s0, s7, s0\nMaaa 116,188,152\n"
"s_and_b32 s0, s0, 7\ns_lshl_b32 s0, s0, 5\nMaaa 124,188,156\ns_and_b32 s1, s6, 15\ns_add_u32 s0, s0, s1\nMaaa 120,184,156\ns_and_b32 s1, s6, 8\ns_add_u32 s0, s0, s1\n"
"Maaa 60,180,156\ns_lshl_b32 s0, s0, 10\ns_add_u32 s0, s0, s3\nMaaa 56,176,156\nSv 56,1,16,0,0\ns_add_u32 s6, s6, s5\nMvaa 188,172,156\nMvaa 184,168,156\nD0 9,24,44,3072\n"
"Mvaa 124,164,156\nMvaa 120,160,156\ns_waitcnt vmcnt(12) lgkmcnt(0)\ns_barrier\nMvaa 64,224,192\nTa 128,12,0\nMvaa 68,228,192\nMvaa 128,232,192\nTa 130,12,2048\nMvaa 132,236,192\n"
"s_add_u32 m0, s48, 32768\ns_add_u32 s45, s45, 128\nMaaa 0,240,192\nTa 132,13,0\ns_lshr_b32 s0, s6, 4\nMaaa 4,244,192\nD0 2,20,45,0\ns_lshl_b32 s0, s0, 2\nMaaa 64,248,192\n"
"Ta 134,13,2048\ns_lshr_b32 s0, s7, s0\nMaaa 68,252,192\ns_and_b32 s0, s0, 7\ns_lshl_b32 s0, s0, 5\nMaaa 76,252,196\nTa 136,14,0\ns_and_b32 s1, s6, 15\nMaaa 72,248,196\n"
"s_add_u32 s0, s0, s1\ns_and_b32 s1, s6, 8\nMaaa 12,244,196\nTa 138,14,2048\ns_add_u32 s0, s0, s1\nMaaa 8,240,196\ns_lshl_b32 s0, s0, 10\ns_add_u32 s0, s0, s3\nMvaa 140,236,196\n"
"Ta 140,15,0\nSv 60,1,16,0,0\nMvaa 136,232,196\nD0 3,20,45,1024\ns_add_u32 s6, s6, s5\nMvaa 76,228,196\nTa 142,15,2048\nMvaa 72,224,196\nMvaa 80,224,200\nTa 144,16,0\n"
"Mvaa 84,228,200\nMvaa 144,232,200\nTa 146,16,2048\nMvaa 148,236,200\nMaaa 16,240,200\nTa 148,17,0\nMaaa 20,244,200\nD0 4,20,45,2048\nMaaa 80,248,200\nTa 150,17,2048\n"
"Maaa 84,252,200\nMaaa 92,252,204\nTa 152,18,0\nMaaa 88,248,204\nMaaa 28,244,204\nTa 154,18,2048\nMaaa 24,240,204\nMvaa 156,236,204\nTa 156,19,0\nMvaa 152,232,204\nD0 5,20,45,3072\n"
"Mvaa 92,228,204\nTa 158,19,2048\nMvaa 88,224,204\nMvaa 96,224,208\nRa 160,10,0\nMvaa 100,228,208\nMvaa 160,232,208\nRa 164,10,2048\nMvaa 164,236,208\ns_add_u32 m0, s48, 36864\n"
"s_add_u32 s46, s45, 0x10000\nMaaa 32,240,208\nRa 168,10,8192\nMaaa 36,244,208\nD0 2,20,46,0\nMaaa 96,248,208\nRa 172,10,10240\nMaaa 100,252,208\nMaaa 108,252,212\nRa 176,10,16384\n"
"Maaa 104,248,212\nMaaa 44,244,212\nRa 180,10,18432\nMaaa 40,240,212\nMvaa 172,236,212\nRa 184,10,24576\nMvaa 168,232,212\nD0 3,20,46,1024\nMvaa 108,228,212\nRa 188,10,26624\n"
"Mvaa 104,224,212\nMvaa 112,224,216\nMvaa 116,228,216\nMvaa 176,232,216\nMvaa 180,236,216\nMaaa 48,240,216\nMaaa 52,244,216\nD0 4,20,46,2048\nMaaa 112,248,216\nMaaa 116,252,216\n"
"Maaa 124,252,220\nMaaa 120,248,220\nMaaa 60,244,220\nMaaa 56,240,220\nMvaa 188,236,220\nMvaa 184,232,220\nD0 5,20,46,3072\nMvaa 124,228,220\nMvaa 120,224,220\n"
"s_waitcnt vmcnt(15) lgkmcnt(0)\ns_barrier\ns_cmp_eq_u32 s14, 0\ns_cbranch_scc1 .T0V5\nMvaa 64,160,128\nTa 192,12,16384\nv_lshrrev_b32 v48, 4, v0\nMvaa 68,164,128\n"
"v_and_b32 v49, 7, v0\nv_xor_b32 v49, v49, v48\ns_add_u32 m0, s47, 49152\ns_add_u32 s44, s44, 0x40000\nMvaa 128,168,128\nTa 194,12,18432\nv_lshlrev_b32 v49, 4, v49\n"
"Mvaa 132,172,128\nv_lshrrev_b32 v48, 3, v0\nv_mul_u32_u24 v48, 896, v48\nD0 6,24,44,0\nMaaa 0,176,128\nTa 196,13,16384\nv_add_u32 v40, v48, v49\nMaaa 4,180,128\n"
"v_xor_b32 v49, 64, v40\nv_add_u32 v41, 6144, v49\nMaaa 64,184,128\nTa 198,13,18432\nv_add_u32 v42, 12288, v40\nMaaa 68,188,128\nv_add_u32 v43, 18432, v49\ns_mov_b32 s2, s13\n"
"Maaa 76,188,132\nTa 200,14,16384\ns_lshl2_add_u32 s2, s2, s11\nD0 7,24,44,1024\nMaaa 72,184,132\ns_mul_i32 s52, s2, 28672\ns_lshr_b32 s2, s13, 2\nMaaa 12,180,132\nTa 202,14,18432\n"
"s_bfe_u32 s3, s13, 0x10001\nMaaa 8,176,132\ns_mul_i32 s3, s3, 17\ns_add_u32 s2, s2, s3\nMvaa 140,172,132\nTa 204,15,16384\ns_lshl2_add_u32 s3, s14, 0\nMvaa 136,168,132\n"
"s_lshl_b32 s3, s3, 2\ns_lshr_b32 s3, 0xe420ca86, s3\nD0 8,24,44,2048\nMvaa 76,164,132\nTa 206,15,18432\ns_and_b32 s3, s3, 15\nMvaa 72,160,132\ns_add_u32 s2, s2, s3\n"
"Mvaa 80,160,136\nTa 208,16,16384\nMvaa 84,164,136\ns_cmp_eq_u32 s2, 32\ns_cselect_b32 s4, 16, s2\nMvaa 144,168,136\nTa 210,16,18432\ns_mul_i32 s75, s4, 65536\nD0 9,24,44,3072\n"
"Mvaa 148,172,136\ns_lshr_b32 s2, s13, 2\ns_bfe_u32 s3, s13, 0x10001\nMaaa 16,176,136\nTa 212,17,16384\ns_mul_i32 s3, s3, 17\nMaaa 20,180,136\ns_add_u32 s2, s2, s3\n"
"s_lshl2_add_u32 s3, s14, 1\ns_add_u32 m0, s47, 65536\ns_add_u32 s46, s51, 0\nMaaa 80,184,136\nTa 214,17,18432\ns_lshl_b32 s3, s3, 2\nMaaa 84,188,136\n"
"s_lshr_b32 s3, 0xe420ca86, s3\ns_and_b32 s3, s3, 15\nD0 6,32,46,0\nMaaa 92,188,140\nTa 216,18,16384\ns_add_u32 s2, s2, s3\nMaaa 88,184,140\ns_cmp_eq_u32 s2, 32\n"
"s_cselect_b32 s4, 16, s2\nMaaa 28,180,140\nTa 218,18,18432\ns_mul_i32 s76, s4, 65536\nMaaa 24,176,140\ns_lshr_b32 s2, s13, 2\ns_bfe_u32 s3, s13, 0x10001\nMvaa 156,172,140\n"
"Ta 220,19,16384\ns_mul_i32 s3, s3, 17\nD0 7,32,46,1024\nMvaa 152,168,140\ns_add_u32 s2, s2, s3\ns_lshl2_add_u32 s3, s14, 2\nMvaa 92,164,140\nTa 222,19,18432\ns_lshl_b32 s3, s3, 2\n"
"Mvaa 88,160,140\ns_lshr_b32 s3, 0xe420ca86, s3\ns_and_b32 s3, s3, 15\nMvaa 96,160,144\nRa 224,11,0\ns_add_u32 s2, s2, s3\nMvaa 100,164,144\ns_cmp_eq_u32 s2, 32\n"
"s_cselect_b32 s4, 16, s2\nD0 8,32,46,2048\nMvaa 160,168,144\nRa 228,11,2048\ns_mul_i32 s77, s4, 65536\nMvaa 164,172,144\ns_lshr_b32 s2, s13, 2\ns_bfe_u32 s3, s13, 0x10001\n"
"Maaa 32,176,144\nRa 232,11,8192\ns_mul_i32 s3, s3, 17\nMaaa 36,180,144\ns_add_u32 s2, s2, s3\ns_lshl2_add_u32 s3, s14, 3\nMaaa 96,184,144\nRa 236,11,10240\ns_lshl_b32 s3, s3, 2\n"
"D0 9,32,46,3072\nMaaa 100,188,144\ns_lshr_b32 s3, 0xe420ca86, s3\ns_and_b32 s3, s3, 15\nMaaa 108,188,148\nRa 240,11,16384\ns_add_u32 s2, s2, s3\nMaaa 104,184,148\n"
"s_cmp_eq_u32 s2, 32\ns_cselect_b32 s4, 16, s2\ns_add_u32 m0, s47, 81920\ns_add_u32 s46, s51, 262144\nMaaa 44,180,148\nRa 244,11,18432\ns_mul_i32 s78, s4, 65536\nMaaa 40,176,148\n"
"s_lshr_b32 s3, s11, 1\ns_lshl_b32 s3, s3, 15\nD0 6,32,46,0\nMvaa 172,172,148\nRa 248,11,24576\ns_add_u32 s2, s72, s3\nMvaa 168,168,148\ns_and_b32 s3, s11, 1\n"
"s_lshl_b32 s3, s3, 14\nMvaa 108,164,148\nRa 252,11,26624\ns_add_u32 s2, s2, s3\nMvaa 104,160,148\ns_add_u32 s2, s2, 0x400000\ns_mov_b32 s55, 4096\nMvaa 112,160,152\n"
"s_add_u32 s75, s75, s2\ns_add_u32 s76, s76, s2\nD0 7,32,46,1024\nMvaa 116,164,152\ns_add_u32 s77, s77, s2\ns_add_u32 s78, s78, s2\nMvaa 176,168,152\nv_add_u32 v32, 0x10000, v12\n"
"v_add_u32 v33, 0x10000, v13\nMvaa 180,172,152\nv_add_u32 v34, 0x10000, v14\nv_add_u32 v35, 0x10000, v15\nMaaa 48,176,152\nv_add_u32 v36, 0x10000, v16\nv_add_u32 v37, 0x10000, v17\n"
"Maaa 52,180,152\nv_add_u32 v38, 0x10000, v18\nv_add_u32 v39, 0x10000, v19\nD0 8,32,46,2048\nMaaa 112,184,152\ns_add_u32 s2, s11, 1\ns_lshr_b32 s2, s2, 1\nMaaa 116,188,152\n"
"s_lshl_b32 s2, s2, 12\nv_add_u32 v44, s2, v10\nMaaa 124,188,156\nv_add_u32 v45, s2, v11\nv_xor_b32 v46, 0x1000, v44\nMaaa 120,184,156\nv_xor_b32 v47, 0x1000, v45\n"
"s_lshr_b32 s3, s11, 1\nMaaa 60,180,156\ns_lshl_b32 s3, s3, 13\ns_lshl_b32 s2, s11, 12\nD0 9,32,46,3072\nMaaa 56,176,156\ns_add_u32 s2, s2, s3\ns_add_u32 s2, s2, s72\n"
"Mvaa 188,172,156\ns_lshl_b32 s3, s11, 12\ns_add_u32 s89, s3, 0x10000\nMvaa 184,168,156\ns_mov_b32 s88, 0xffff8000\ns_lshl_b32 s4, s13, 1\nMvaa 124,164,156\ns_sub_u32 s79, 15, s4\n"
"s_and_b32 s5, s12, 1\nMvaa 120,160,156\ns_waitcnt vmcnt(12) lgkmcnt(0)\ns_barrier\nMvaa 64,224,192\nTa 128,12,32768\ns_mul_i32 s5, s5, 229376\nMvaa 68,228,192\n"
"s_add_u32 s5, s5, s2\ns_add_u32 s96, s5, 0x1310000\ns_add_u32 m0, s47, -65536\ns_add_u32 s46, s52, 0\nMvaa 128,232,192\nTa 130,12,34816\ns_mov_b32 s63, 0x53800000\n"
"Mvaa 132,236,192\ns_mov_b32 s64, 0x2b800000\ns_mov_b32 s98, s4\nD0 40,28,46,0\nMaaa 0,240,192\nTa 132,13,32768\ns_lshl3_add_u32 s5, s12, s13\nMaaa 4,244,192\n"
"s_lshl2_add_u32 s5, s5, s11\ns_lshl_b32 s65, s5, 9\nMaaa 64,248,192\nTa 134,13,34816\ns_add_u32 s65, s65, 0xc000000\nMaaa 68,252,192\ns_and_b32 s66, s72, 0x2000\n"
"s_add_u32 s66, s66, s56\nMaaa 76,252,196\nTa 136,14,32768\ns_add_u32 s66, s66, 0x100000\nD0 41,28,46,1024\nMaaa 72,248,196\ns_lshl_b32 s61, s12, 8\ns_add_u32 s61, s61, 0xc048400\n"
"Maaa 12,244,196\nTa 138,14,34816\ns_lshr_b32 s58, s12, 1\nMaaa 8,240,196\ns_lshl_b32 s58, s58, 9\ns_add_u32 s58, s58, 0xc048400\nMvaa 140,236,196\nTa 140,15,32768\n"
"s_lshl1_add_u32 s5, s12, 1\nMvaa 136,232,196\ns_lshl3_add_u32 s5, s5, s13\ns_lshl4_add_u32 s54, s5, 0xc048400\nD0 42,28,46,2048\nMvaa 76,228,196\nTa 142,15,34816\nMvaa 72,224,196\n"
"s_cmp_eq_u32 s11, 0\ns_cselect_b32 s54, s54, 0x40000000\nMvaa 80,224,200\nTa 144,16,32768\ns_getpc_b64 s[8:9]\n.PC44:\ns_add_u32 s8, s8, .HD11-.PC44\ns_addc_u32 s9, s9, 0\n"
"Mvaa 84,228,200\ns_mov_b32 s97, 0xff800000\ns_mov_b32 s90, 1\nMvaa 144,232,200\nTa 146,16,34816\ns_lshl_b32 s5, s11, 2\nD0 43,28,46,3072\nMvaa 148,236,200\n"
"s_lshr_b32 s92, 0xf112, s5\ns_and_b32 s92, s92, 15\nMaaa 16,240,200\nTa 148,17,32768\ns_lshr_b32 s93, s11, 1\nMaaa 20,244,200\ns_xor_b32 s93, s93, 1\ns_mul_i32 s4, s79, 65536\n"
"s_add_u32 m0, s47, 0\ns_add_u32 s46, s51, 524288\nMaaa 80,248,200\nTa 150,17,34816\ns_sub_u32 s4, s2, s4\nMaaa 84,252,200\ns_add_u32 s94, s4, 0x20000\ns_add_u32 s95, s4, 0x410000\n"
"D0 6,32,46,0\nMaaa 92,252,204\nTa 152,18,32768\ns_add_u32 s86, s2, 0x130000\nMaaa 88,248,204\ns_add_u32 s87, s2, 0x520000\ns_sub_u32 s99, s79, 1\nMaaa 28,244,204\nTa 154,18,34816\n"
"s_mov_b32 s91, 100\nMaaa 24,240,204\ns_cmp_eq_u32 s11, 3\ns_cselect_b32 s92, 100, s92\nMvaa 156,236,204\nTa 156,19,32768\nD0 7,32,46,1024\nMvaa 152,232,204\nMvaa 92,228,204\n"
"Ta 158,19,34816\nMvaa 88,224,204\nMvaa 96,224,208\nRa 160,10,32768\nMvaa 100,228,208\nD0 8,32,46,2048\nMvaa 160,232,208\nRa 164,10,34816\nMvaa 164,236,208\nMaaa 32,240,208\n"
"Ra 168,10,40960\nMaaa 36,244,208\nMaaa 96,248,208\nRa 172,10,43008\nD0 9,32,46,3072\nMaaa 100,252,208\nMaaa 108,252,212\nRa 176,10,49152\nMaaa 104,248,212\n"
"s_add_u32 m0, s47, 16384\ns_add_u32 s46, s51, 786432\nMaaa 44,244,212\nRa 180,10,51200\nMaaa 40,240,212\nD0 6,32,46,0\nMvaa 172,236,212\nRa 184,10,57344\nMvaa 168,232,212\n"
"Mvaa 108,228,212\nRa 188,10,59392\nMvaa 104,224,212\nMvaa 112,224,216\nD0 7,32,46,1024\nMvaa 116,228,216\nMvaa 176,232,216\nMvaa 180,236,216\nMaaa 48,240,216\nMaaa 52,244,216\n"
"D0 8,32,46,2048\nMaaa 112,248,216\nMaaa 116,252,216\nMaaa 124,252,220\nMaaa 120,248,220\nMaaa 60,244,220\nD0 9,32,46,3072\nMaaa 56,240,220\nMvaa 188,236,220\nMvaa 184,232,220\n"
"Mvaa 124,228,220\nMvaa 120,224,220\ns_waitcnt vmcnt(20) lgkmcnt(0)\ns_barrier\nMvaa 64,160,128\nTa 192,12,49152\nMvaa 68,164,128\ns_add_u32 m0, s47, -49152\n"
"s_add_u32 s46, s52, 256\nMvaa 128,168,128\nTa 194,12,51200\nMvaa 132,172,128\nD0 40,28,46,0\nMaaa 0,176,128\nTa 196,13,49152\nMaaa 4,180,128\nMaaa 64,184,128\nTa 198,13,51200\n"
"Maaa 68,188,128\nMaaa 76,188,132\nTa 200,14,49152\nD0 41,28,46,1024\nMaaa 72,184,132\nMaaa 12,180,132\nTa 202,14,51200\nMaaa 8,176,132\nMvaa 140,172,132\nTa 204,15,49152\n"
"Mvaa 136,168,132\nD0 42,28,46,2048\nMvaa 76,164,132\nTa 206,15,51200\nMvaa 72,160,132\nMvaa 80,160,136\nTa 208,16,49152\nMvaa 84,164,136\nMvaa 144,168,136\nTa 210,16,51200\n"
"D0 43,28,46,3072\nMvaa 148,172,136\nMaaa 16,176,136\nTa 212,17,49152\nMaaa 20,180,136\ns_add_u32 m0, s47, 32768\ns_add_u32 s46, s51, 1048576\nMaaa 80,184,136\nTa 214,17,51200\n"
"Maaa 84,188,136\nD0 6,32,46,0\nMaaa 92,188,140\nTa 216,18,49152\nMaaa 88,184,140\nMaaa 28,180,140\nTa 218,18,51200\nMaaa 24,176,140\nMvaa 156,172,140\nTa 220,19,49152\n"
"D0 7,32,46,1024\nMvaa 152,168,140\nMvaa 92,164,140\nTa 222,19,51200\nMvaa 88,160,140\nMvaa 96,160,144\nRa 224,11,32768\nMvaa 100,164,144\nD0 8,32,46,2048\nMvaa 160,168,144\n"
"Ra 228,11,34816\nMvaa 164,172,144\nMaaa 32,176,144\nRa 232,11,40960\nMaaa 36,180,144\nMaaa 96,184,144\nRa 236,11,43008\nD0 9,32,46,3072\nMaaa 100,188,144\nMaaa 108,188,148\n"
"Ra 240,11,49152\nMaaa 104,184,148\nMaaa 44,180,148\nRa 244,11,51200\nMaaa 40,176,148\nMvaa 172,172,148\nRa 248,11,57344\nMvaa 168,168,148\nMvaa 108,164,148\nRa 252,11,59392\n"
"Mvaa 104,160,148\nMvaa 112,160,152\nMvaa 116,164,152\nMvaa 176,168,152\nMvaa 180,172,152\nMaaa 48,176,152\nMaaa 52,180,152\nMaaa 112,184,152\nMaaa 116,188,152\nMaaa 124,188,156\n"
"Maaa 120,184,156\nMaaa 60,180,156\nMaaa 56,176,156\nMvaa 188,172,156\nMvaa 184,168,156\nMvaa 124,164,156\nMvaa 120,160,156\ns_waitcnt vmcnt(16) lgkmcnt(0)\ns_barrier\n"
"Mvaa 64,224,192\ns_add_u32 m0, s47, -16384\ns_add_u32 s46, s52, 128\nMvaa 68,228,192\nMvaa 72,224,196\nD0 40,28,46,0\nMvaa 76,228,196\nMvaa 80,224,200\nMvaa 84,228,200\n"
"Mvaa 88,224,204\nD0 41,28,46,1024\nCV 192,64,65\nMvaa 92,228,204\nCV 193,66,67\nCV 194,68,69\nMvaa 96,224,208\nCV 195,70,71\nCV 196,72,73\nMvaa 100,228,208\nCV 197,74,75\n"
"CV 198,76,77\nMvaa 104,224,212\nD0 42,28,46,2048\nCV 199,78,79\nMvaa 108,228,212\nCV 200,80,81\nCV 201,82,83\nMvaa 112,224,216\nCV 202,84,85\nCV 203,86,87\nMvaa 116,228,216\n"
"CV 204,88,89\nCV 205,90,91\nMvaa 120,224,220\nD0 43,28,46,3072\nCV 206,92,93\nMvaa 124,228,220\nCV 207,94,95\nCV 208,96,97\nMvaa 128,232,192\ns_add_u32 m0, s47, 49152\n"
"s_add_u32 s46, s51, 1310720\nMvaa 132,236,192\nCV 209,98,99\nCV 210,100,101\nMvaa 136,232,196\nD0 6,32,46,0\nCV 211,102,103\nMvaa 140,236,196\nCV 212,104,105\nCV 213,106,107\n"
"Mvaa 144,232,200\nCV 214,108,109\nCV 215,110,111\nMvaa 148,236,200\nCV 216,112,113\nCV 217,114,115\nMvaa 152,232,204\nD0 7,32,46,1024\nCV 218,116,117\nMvaa 156,236,204\n"
"CV 219,118,119\nCV 220,120,121\nMvaa 160,232,208\nCV 221,122,123\nCV 222,124,125\nMvaa 164,236,208\nCV 223,126,127\nCV 224,128,129\nMvaa 168,232,212\nD0 8,32,46,2048\n"
"CV 225,130,131\nMvaa 172,236,212\nCV 226,132,133\nCV 227,134,135\nMvaa 176,232,216\nCV 228,136,137\nCV 229,138,139\nMvaa 180,236,216\nCV 230,140,141\nCV 231,142,143\n"
"Mvaa 184,232,220\nD0 9,32,46,3072\nCV 232,144,145\nMvaa 188,236,220\nCV 233,146,147\nCV 234,148,149\nMaaa 0,240,192\ns_add_u32 m0, s47, -32768\ns_add_u32 s46, s52, 384\n"
"Maaa 4,244,192\nCV 235,150,151\nCV 236,152,153\nMaaa 8,240,196\nD0 40,28,46,0\nCV 237,154,155\nMaaa 12,244,196\nCV 238,156,157\nCV 239,158,159\nMaaa 16,240,200\nRa 160,44,0\n"
"CV 240,160,161\nMaaa 20,244,200\nRa 164,44,2048\nCV 241,162,163\nMaaa 24,240,204\nD0 41,28,46,1024\nRa 168,46,0\nMaaa 28,244,204\nRa 172,46,2048\nCV 242,164,165\nMaaa 32,240,208\n"
"Ta 128,32,0\nCV 243,166,167\nMaaa 36,244,208\nTa 130,32,2048\nCV 244,168,169\nMaaa 40,240,212\nD0 42,28,46,2048\nTa 132,33,0\nMaaa 44,244,212\nTa 134,33,2048\nCV 245,170,171\n"
"Maaa 48,240,216\nTa 136,34,0\nCV 246,172,173\nMaaa 52,244,216\nTa 138,34,2048\nCV 247,174,175\nMaaa 56,240,220\nD0 43,28,46,3072\nTa 140,35,0\nMaaa 60,244,220\nTa 142,35,2048\n"
"CV 248,176,177\nMaaa 64,248,192\nTa 144,36,0\nCV 249,178,179\nMaaa 68,252,192\nTa 146,36,2048\nCV 250,180,181\nMaaa 72,248,196\nTa 148,37,0\nCV 251,182,183\nSv 192,1,16,75,0\n"
"Maaa 76,252,196\nTa 150,37,2048\nCV 252,184,185\nMaaa 80,248,200\nTa 152,38,0\nCV 253,186,187\nMaaa 84,252,200\nTa 154,38,2048\nCV 254,188,189\nMaaa 88,248,204\nTa 156,39,0\n"
"CV 255,190,191\nMaaa 92,252,204\nTa 158,39,2048\nMaaa 96,248,208\nSv 196,1,16,75,1024\nMaaa 100,252,208\nMaaa 104,248,212\nMaaa 108,252,212\nMaaa 112,248,216\nMaaa 116,252,216\n"
"Maaa 120,248,220\nSv 200,1,16,75,2048\nMaaa 124,252,220\nWL 0\ns_branch .Q16\n.T0V5:\nMvaa 64,160,128\nTa 192,12,16384\ns_lshr_b32 s3, s11, 1\nMvaa 68,164,128\n"
"s_lshl_b32 s3, s3, 13\ns_lshl_b32 s2, s11, 12\ns_add_u32 m0, s47, 49152\ns_add_u32 s44, s44, 0x40000\nMvaa 128,168,128\nTa 194,12,18432\ns_add_u32 s2, s2, s3\nMvaa 132,172,128\n"
"s_add_u32 s2, s2, s72\ns_lshl_b32 s3, s11, 12\nD0 6,24,44,0\nMaaa 0,176,128\nTa 196,13,16384\ns_add_u32 s89, s3, 0x0\nMaaa 4,180,128\ns_mov_b32 s88, 0xffff8000\n"
"s_lshl_b32 s4, s13, 1\nMaaa 64,184,128\nTa 198,13,18432\ns_sub_u32 s79, 15, s4\nMaaa 68,188,128\ns_and_b32 s5, s12, 1\ns_mul_i32 s5, s5, 229376\nD0 7,24,44,1024\nMaaa 76,188,132\n"
"Ta 200,14,16384\ns_add_u32 s5, s5, s2\nMaaa 72,184,132\ns_add_u32 s96, s5, 0x1310000\ns_mov_b32 s63, 0x53800000\nMaaa 12,180,132\nTa 202,14,18432\ns_mov_b32 s64, 0x2b800000\n"
"Maaa 8,176,132\ns_mov_b32 s98, s4\ns_lshl3_add_u32 s5, s12, s13\nD0 8,24,44,2048\nMvaa 140,172,132\nTa 204,15,16384\ns_lshl2_add_u32 s5, s5, s11\nMvaa 136,168,132\n"
"s_lshl_b32 s65, s5, 9\ns_add_u32 s65, s65, 0xc000000\nMvaa 76,164,132\nTa 206,15,18432\ns_and_b32 s66, s72, 0x2000\nMvaa 72,160,132\ns_add_u32 s66, s66, s56\n"
"s_add_u32 s66, s66, 0x100000\nD0 9,24,44,3072\nMvaa 80,160,136\nTa 208,16,16384\ns_lshl_b32 s61, s12, 8\nMvaa 84,164,136\ns_add_u32 s61, s61, 0xc048400\ns_lshr_b32 s58, s12, 1\n"
"s_add_u32 s6, s56, 0\nMvaa 144,168,136\nTa 210,16,18432\ns_lshl_b32 s58, s58, 9\nMvaa 148,172,136\ns_add_u32 s58, s58, 0xc048400\ns_lshl1_add_u32 s5, s12, 0\n"
"buffer_load_dwordx4 v[192:195], v1, s[16:19], s6 offen offset:0 sc1\nMaaa 16,176,136\nTa 212,17,16384\ns_lshl3_add_u32 s5, s5, s13\nMaaa 20,180,136\n"
"s_lshl4_add_u32 s54, s5, 0xc048400\nMaaa 80,184,136\nTa 214,17,18432\nMaaa 84,188,136\ns_cmp_eq_u32 s11, 0\ns_cselect_b32 s54, s54, 0x40000000\n"
"buffer_load_dwordx4 v[196:199], v1, s[16:19], s6 offen offset:1024 sc1\nMaaa 92,188,140\nTa 216,18,16384\ns_getpc_b64 s[8:9]\n.PC45:\ns_add_u32 s8, s8, .HD10-.PC45\n"
"s_addc_u32 s9, s9, 0\nMaaa 88,184,140\ns_mov_b32 s97, 0xff800000\ns_mov_b32 s90, 3\nMaaa 28,180,140\nTa 218,18,18432\ns_lshl_b32 s5, s11, 2\nMaaa 24,176,140\n"
"s_lshr_b32 s92, 0xf112, s5\ns_and_b32 s92, s92, 15\nbuffer_load_dwordx4 v[200:203], v1, s[16:19], s6 offen offset:2048 sc1\nMvaa 156,172,140\nTa 220,19,16384\n"
"s_lshr_b32 s93, s11, 1\nMvaa 152,168,140\ns_xor_b32 s93, s93, 1\ns_add_u32 s86, s2, 0x20000\nMvaa 92,164,140\nTa 222,19,18432\ns_add_u32 s87, s2, 0x410000\nMvaa 88,160,140\n"
"s_mov_b32 s94, s86\ns_mov_b32 s95, s87\nbuffer_load_dwordx4 v[204:207], v1, s[16:19], s6 offen offset:3072 sc1\nMvaa 96,160,144\nRa 224,11,0\nMvaa 100,164,144\n"
"s_cmp_eq_u32 s13, 7\ns_cselect_b32 s5, 0xfffffffd, 64\ns_add_u32 s6, s56, 4096\nMvaa 160,168,144\nRa 228,11,2048\ns_add_u32 s92, s92, s5\nMvaa 164,172,144\ns_add_u32 s93, s93, s5\n"
"s_or_b32 s92, s92, 0x100\nbuffer_load_dwordx4 v[208:211], v1, s[16:19], s6 offen offset:0 sc1\nMaaa 32,176,144\nRa 232,11,8192\ns_mov_b32 s91, 100\nMaaa 36,180,144\n"
"v_and_b32 v27, 0x70, v1\nbuffer_load_dword v27, v27, s[16:19], s61 offen offset:128 sc0 sc1\nMaaa 96,184,144\nRa 236,11,10240\ns_lshr_b32 s2, s13, 2\nMaaa 100,188,144\n"
"s_bfe_u32 s3, s13, 0x10001\ns_mul_i32 s3, s3, 17\nbuffer_load_dwordx4 v[212:215], v1, s[16:19], s6 offen offset:1024 sc1\nMaaa 108,188,148\nRa 240,11,16384\ns_add_u32 s2, s2, s3\n"
"Maaa 104,184,148\ns_lshl2_add_u32 s3, s14, 0\ns_lshl_b32 s3, s3, 2\nMaaa 44,180,148\nRa 244,11,18432\ns_lshr_b32 s3, 0xe420ca86, s3\nMaaa 40,176,148\ns_and_b32 s3, s3, 15\n"
"s_add_u32 s2, s2, s3\nbuffer_load_dwordx4 v[216:219], v1, s[16:19], s6 offen offset:2048 sc1\nMvaa 172,172,148\nRa 248,11,24576\nMvaa 168,168,148\ns_cmp_eq_u32 s2, 32\n"
"s_cselect_b32 s4, 16, s2\nMvaa 108,164,148\nRa 252,11,26624\ns_mul_i32 s75, s4, 65536\nMvaa 104,160,148\ns_lshr_b32 s2, s13, 2\ns_bfe_u32 s3, s13, 0x10001\n"
"buffer_load_dwordx4 v[220:223], v1, s[16:19], s6 offen offset:3072 sc1\nMvaa 112,160,152\ns_mul_i32 s3, s3, 17\ns_add_u32 s2, s2, s3\nMvaa 116,164,152\ns_lshl2_add_u32 s3, s14, 1\n"
"s_lshl_b32 s3, s3, 2\ns_add_u32 s6, s56, 16384\nMvaa 176,168,152\ns_lshr_b32 s3, 0xe420ca86, s3\ns_and_b32 s3, s3, 15\nMvaa 180,172,152\ns_add_u32 s2, s2, s3\n"
"buffer_load_dwordx4 v[224:227], v1, s[16:19], s6 offen offset:0 sc1\nMaaa 48,176,152\ns_cmp_eq_u32 s2, 32\ns_cselect_b32 s4, 16, s2\nMaaa 52,180,152\ns_mul_i32 s76, s4, 65536\n"
"s_lshr_b32 s2, s13, 2\nMaaa 112,184,152\ns_bfe_u32 s3, s13, 0x10001\ns_mul_i32 s3, s3, 17\nMaaa 116,188,152\ns_add_u32 s2, s2, s3\ns_lshl2_add_u32 s3, s14, 2\n"
"buffer_load_dwordx4 v[228:231], v1, s[16:19], s6 offen offset:1024 sc1\nMaaa 124,188,156\ns_lshl_b32 s3, s3, 2\ns_lshr_b32 s3, 0xe420ca86, s3\nMaaa 120,184,156\n"
"s_and_b32 s3, s3, 15\ns_add_u32 s2, s2, s3\nMaaa 60,180,156\ns_cmp_eq_u32 s2, 32\ns_cselect_b32 s4, 16, s2\nMaaa 56,176,156\ns_mul_i32 s77, s4, 65536\ns_lshr_b32 s2, s13, 2\n"
"buffer_load_dwordx4 v[232:235], v1, s[16:19], s6 offen offset:2048 sc1\nMvaa 188,172,156\ns_bfe_u32 s3, s13, 0x10001\ns_mul_i32 s3, s3, 17\nMvaa 184,168,156\ns_add_u32 s2, s2, s3\n"
"s_lshl2_add_u32 s3, s14, 3\nMvaa 124,164,156\ns_lshl_b32 s3, s3, 2\ns_lshr_b32 s3, 0xe420ca86, s3\nMvaa 120,160,156\ns_waitcnt vmcnt(16) lgkmcnt(0)\ns_barrier\nMvaa 64,224,192\n"
"Ta 128,12,32768\ns_and_b32 s3, s3, 15\nbuffer_load_dwordx4 v[236:239], v1, s[16:19], s6 offen offset:3072 sc1\nMvaa 68,228,192\ns_add_u32 s2, s2, s3\nMvaa 128,232,192\n"
"Ta 130,12,34816\ns_add_u32 s6, s56, 20480\nMvaa 132,236,192\ns_cmp_eq_u32 s2, 32\ns_cselect_b32 s4, 16, s2\nMaaa 0,240,192\nTa 132,13,32768\ns_mul_i32 s78, s4, 65536\n"
"buffer_load_dwordx4 v[240:243], v1, s[16:19], s6 offen offset:0 sc1\nMaaa 4,244,192\ns_lshr_b32 s3, s11, 1\ns_lshl_b32 s3, s3, 15\nMaaa 64,248,192\nTa 134,13,34816\n"
"s_add_u32 s2, s72, s3\nMaaa 68,252,192\ns_and_b32 s3, s11, 1\ns_lshl_b32 s3, s3, 14\nMaaa 76,252,196\nTa 136,14,32768\ns_add_u32 s2, s2, s3\n"
"buffer_load_dwordx4 v[244:247], v1, s[16:19], s6 offen offset:1024 sc1\nMaaa 72,248,196\ns_add_u32 s2, s2, 0x400000\ns_mov_b32 s55, 4096\nMaaa 12,244,196\nTa 138,14,34816\n"
"s_add_u32 s75, s75, s2\nMaaa 8,240,196\ns_add_u32 s76, s76, s2\ns_add_u32 s77, s77, s2\nMvaa 140,236,196\nTa 140,15,32768\ns_add_u32 s78, s78, s2\n"
"buffer_load_dwordx4 v[248:251], v1, s[16:19], s6 offen offset:2048 sc1\nMvaa 136,232,196\nMvaa 76,228,196\nTa 142,15,34816\nMvaa 72,224,196\nMvaa 80,224,200\nTa 144,16,32768\n"
"buffer_load_dwordx4 v[252:255], v1, s[16:19], s6 offen offset:3072 sc1\nMvaa 84,228,200\nMvaa 144,232,200\nTa 146,16,34816\nMvaa 148,236,200\nMaaa 16,240,200\nTa 148,17,32768\n"
"Maaa 20,244,200\nMaaa 80,248,200\nTa 150,17,34816\nMaaa 84,252,200\nMaaa 92,252,204\nTa 152,18,32768\nMaaa 88,248,204\nMaaa 28,244,204\nTa 154,18,34816\nMaaa 24,240,204\n"
"Mvaa 156,236,204\nTa 156,19,32768\nMvaa 152,232,204\nMvaa 92,228,204\nTa 158,19,34816\nMvaa 88,224,204\nMvaa 96,224,208\nRa 160,10,32768\nMvaa 100,228,208\nMvaa 160,232,208\n"
"Ra 164,10,34816\nMvaa 164,236,208\nMaaa 32,240,208\nRa 168,10,40960\nMaaa 36,244,208\nMaaa 96,248,208\nRa 172,10,43008\nMaaa 100,252,208\nMaaa 108,252,212\nRa 176,10,49152\nWV 11\n"
"v_cmp_ne_u32 vcc, s15, v27\ns_nop 0\ns_cbranch_vccnz .S46\n.S47:\ns_sub_u32 s45, s86, 131072\nMaaa 104,248,212\ns_add_u32 m0, s47, 0\ns_add_u32 s46, s45, 0x0\nMaaa 44,244,212\n"
"Ra 180,10,51200\nMaaa 40,240,212\nD1 1,16,46,0\nMvaa 172,236,212\nRa 184,10,57344\nMvaa 168,232,212\nMvaa 108,228,212\nRa 188,10,59392\nMvaa 104,224,212\nD1 1,16,46,1024\n"
"Mvaa 112,224,216\nMvaa 116,228,216\nMvaa 176,232,216\nMvaa 180,236,216\nD1 1,16,46,2048\nMaaa 48,240,216\nMaaa 52,244,216\nMaaa 112,248,216\nMaaa 116,252,216\nD1 1,16,46,3072\n"
"Maaa 124,252,220\nMaaa 120,248,220\nMaaa 60,244,220\nMaaa 56,240,220\nMvaa 188,236,220\nMvaa 184,232,220\nMvaa 124,228,220\nMvaa 120,224,220\nWL 0\ns_barrier\nMvaa 64,160,128\n"
"Ta 192,12,49152\nMvaa 68,164,128\ns_add_u32 m0, s47, 32768\ns_add_u32 s46, s45, 0x8000\nMvaa 128,168,128\nTa 194,12,51200\nMvaa 132,172,128\nD1 1,16,46,0\nMaaa 0,176,128\n"
"Ta 196,13,49152\nMaaa 4,180,128\nMaaa 64,184,128\nTa 198,13,51200\nMaaa 68,188,128\nD1 1,16,46,1024\nMaaa 76,188,132\nTa 200,14,49152\nMaaa 72,184,132\nMaaa 12,180,132\n"
"Ta 202,14,51200\nMaaa 8,176,132\nD1 1,16,46,2048\nMvaa 140,172,132\nTa 204,15,49152\nMvaa 136,168,132\nMvaa 76,164,132\nTa 206,15,51200\nMvaa 72,160,132\nD1 1,16,46,3072\n"
"Mvaa 80,160,136\nTa 208,16,49152\nMvaa 84,164,136\ns_add_u32 m0, s47, -65536\ns_add_u32 s46, s45, 0x10000\nMvaa 144,168,136\nTa 210,16,51200\nMvaa 148,172,136\nD1 1,16,46,0\n"
"Maaa 16,176,136\nTa 212,17,49152\nMaaa 20,180,136\nMaaa 80,184,136\nTa 214,17,51200\nMaaa 84,188,136\nD1 1,16,46,1024\nMaaa 92,188,140\nTa 216,18,49152\nMaaa 88,184,140\n"
"Maaa 28,180,140\nTa 218,18,51200\nMaaa 24,176,140\nD1 1,16,46,2048\nMvaa 156,172,140\nTa 220,19,49152\nMvaa 152,168,140\nMvaa 92,164,140\nTa 222,19,51200\nMvaa 88,160,140\n"
"D1 1,16,46,3072\nMvaa 96,160,144\nRa 224,11,32768\nMvaa 100,164,144\ns_add_u32 m0, s47, -49152\ns_add_u32 s46, s45, 0x408000\nMvaa 160,168,144\nRa 228,11,34816\nMvaa 164,172,144\n"
"D1 1,16,46,0\nMaaa 32,176,144\nRa 232,11,40960\nMaaa 36,180,144\nMaaa 96,184,144\nRa 236,11,43008\nMaaa 100,188,144\nD1 1,16,46,1024\nMaaa 108,188,148\nRa 240,11,49152\n"
"Maaa 104,184,148\nMaaa 44,180,148\nRa 244,11,51200\nMaaa 40,176,148\nD1 1,16,46,2048\nMvaa 172,172,148\nRa 248,11,57344\nMvaa 168,168,148\nMvaa 108,164,148\nRa 252,11,59392\n"
"Mvaa 104,160,148\nD1 1,16,46,3072\nMvaa 112,160,152\nMvaa 116,164,152\nv_add_u32 v2, 0x10000, v1\ns_lshr_b32 s2, s59, 1\nMvaa 176,168,152\nv_add_u32 v24, s2, v2\n"
"v_xor_b32 v26, 0x1000, v24\nMvaa 180,172,152\nv_lshrrev_b32 v20, 4, v0\nv_lshlrev_b32 v20, 2, v20\nMaaa 48,176,152\nv_and_b32 v0, 15, v0\nv_sub_u32 v0, v0, v20\nMaaa 52,180,152\n"
"s_and_b32 s2, s11, 1\ns_lshl_b32 s2, s2, 6\nMaaa 112,184,152\nv_subrev_u32 v3, s2, v0\ns_xor_b32 s2, s2, 64\nMaaa 116,188,152\nv_subrev_u32 v0, s2, v0\nv_mov_b32 v4, 0\n"
"Maaa 124,188,156\nv_mov_b32 v5, 0\nv_mov_b32 v6, 0\nMaaa 120,184,156\nv_mov_b32 v7, 0\nv_mov_b32 v8, 0\nMaaa 60,180,156\nv_mov_b32 v9, 0\nv_mov_b32 v10, 0\nMaaa 56,176,156\n"
"v_mov_b32 v11, 0\nv_mov_b32 v12, 0\nMvaa 188,172,156\nv_mov_b32 v13, 0\nv_mov_b32 v14, 0\nMvaa 184,168,156\nv_mov_b32 v15, 0\nv_mov_b32 v16, 0\nMvaa 124,164,156\nv_mov_b32 v17, 0\n"
"v_mov_b32 v18, 0\nMvaa 120,160,156\ns_waitcnt vmcnt(12) lgkmcnt(0)\ns_barrier\nMvaa 64,224,192\nv_mov_b32 v19, 0\nMvaa 68,228,192\ns_add_u32 m0, s47, 49152\n"
"s_add_u32 s46, s45, 0x400000\nMvaa 72,224,196\nMvaa 76,228,196\nD1 1,16,46,0\nMvaa 80,224,200\nMvaa 84,228,200\nMvaa 88,224,204\nCV 32,64,65\nCV 33,66,67\nMvaa 92,228,204\n"
"D1 1,16,46,1024\nCV 34,68,69\nMvaa 96,224,208\nCV 35,70,71\nCV 36,72,73\nMvaa 100,228,208\nCV 37,74,75\nCV 38,76,77\nMvaa 104,224,212\nCV 39,78,79\nCV 40,80,81\nMvaa 108,228,212\n"
"D1 1,16,46,2048\nCV 41,82,83\nMvaa 112,224,216\nCV 42,84,85\nCV 43,86,87\nMvaa 116,228,216\nCV 44,88,89\nCV 45,90,91\nMvaa 120,224,220\nCV 46,92,93\nCV 47,94,95\nMvaa 124,228,220\n"
"D1 1,16,46,3072\nCV 48,96,97\nMvaa 128,232,192\nCV 49,98,99\nCV 50,100,101\nMvaa 132,236,192\nCV 51,102,103\nCV 52,104,105\nMvaa 136,232,196\nCV 53,106,107\nCV 54,108,109\n"
"Mvaa 140,236,196\nCV 55,110,111\nCV 56,112,113\nMvaa 144,232,200\nCV 57,114,115\nCV 58,116,117\nMvaa 148,236,200\nCV 59,118,119\nCV 60,120,121\nMvaa 152,232,204\nCV 61,122,123\n"
"CV 62,124,125\nMvaa 156,236,204\nCV 63,126,127\nCV 96,128,129\nMvaa 160,232,208\nCV 97,130,131\nCV 98,132,133\nMvaa 164,236,208\nCV 99,134,135\nCV 100,136,137\nSv 32,1,16,75,0\n"
"Mvaa 168,232,212\nCV 101,138,139\nCV 102,140,141\nMvaa 172,236,212\nCV 103,142,143\nCV 104,144,145\nMvaa 176,232,216\nCV 105,146,147\nCV 106,148,149\nMvaa 180,236,216\n"
"CV 107,150,151\nCV 108,152,153\nSv 36,1,16,75,1024\nMvaa 184,232,220\nCV 109,154,155\nCV 110,156,157\nMvaa 188,236,220\nCV 111,158,159\nCV 112,160,161\nMaaa 0,240,192\n"
"CV 113,162,163\nCV 114,164,165\nMaaa 4,244,192\nCV 115,166,167\nCV 116,168,169\nSv 40,1,16,75,2048\nMaaa 8,240,196\nCV 117,170,171\nCV 118,172,173\nMaaa 12,244,196\n"
"CV 119,174,175\nCV 120,176,177\nMaaa 16,240,200\nCV 121,178,179\nCV 122,180,181\nMaaa 20,244,200\nCV 123,182,183\nCV 124,184,185\nSv 44,1,16,75,3072\nMaaa 24,240,204\n"
"CV 125,186,187\nCV 126,188,189\nMaaa 28,244,204\nCV 127,190,191\nv_mov_b32 v86, 0\ns_add_u32 s75, s75, s55\nMaaa 32,240,208\nv_mov_b32 v87, 0\nv_mov_b32 v88, 0\nMaaa 36,244,208\n"
"v_mov_b32 v89, 0\nSv 48,1,16,75,0\nMaaa 40,240,212\nAR 92,0\nAR 93,1\nMaaa 44,244,212\nCV 128,92,93\nAR 92,2\nMaaa 48,240,216\nAR 93,3\nCV 129,92,93\nMaaa 52,244,216\n"
"Sv 52,1,16,75,1024\nAR 92,4\nMaaa 56,240,220\nAR 93,5\nCV 130,92,93\nMaaa 60,244,220\nAR 92,6\nAR 93,7\nMaaa 64,248,192\nRa 128,24,0\nCV 131,92,93\nMaaa 68,252,192\n"
"Sv 56,1,16,75,2048\nAR 92,8\nMaaa 72,248,196\nRa 132,24,1024\nAR 93,9\nMaaa 76,252,196\nCV 132,92,93\nAR 92,10\nMaaa 80,248,200\nRa 136,24,2048\nAR 93,11\nMaaa 84,252,200\n"
"Sv 60,1,16,75,3072\nCV 133,92,93\nMaaa 88,248,204\nRa 140,24,3072\nAR 92,12\nMaaa 92,252,204\nAR 93,13\nCV 134,92,93\nMaaa 96,248,208\nRa 144,26,0\nAR 92,14\nMaaa 100,252,208\n"
"AR 93,15\nCV 135,92,93\nMaaa 104,248,212\nRa 148,26,1024\nMaaa 108,252,212\nMaaa 112,248,216\nRa 152,26,2048\nMaaa 116,252,216\nMaaa 120,248,220\nRa 156,26,3072\nMaaa 124,252,220\n"
"WL 4\ns_branch .F07\n.S40:\ns_mov_b32 s7, 262144\n.S42:\ns_sleep 8\ns_lshl_b32 s5, s73, 7\ns_add_u32 s5, s5, 0xc04ac00\nv_bfe_u32 v27, v1, 2, 7\n"
"buffer_load_dword v27, v27, s[16:19], s5 offen sc0 sc1\nWV 0\nv_xor_b32 v27, s15, v27\nv_and_b32 v32, 0xffffdfff, v27\nv_cmp_ne_u32 vcc, 0, v32\ns_nop 0\ns_cbranch_vccz .S41\n"
"s_sub_u32 s7, s7, 1\ns_cbranch_scc0 .S42\ns_branch .S41\n.S46:\ns_mov_b32 s7, 262144\n.S48:\ns_sleep 8\nv_and_b32 v27, 0x70, v1\n"
"buffer_load_dword v27, v27, s[16:19], s61 offen offset:128 sc0 sc1\nWV 0\nv_cmp_ne_u32 vcc, s15, v27\ns_nop 0\ns_cbranch_vccz .S47\ns_sub_u32 s7, s7, 1\ns_cbranch_scc0 .S48\n"
"s_branch .S47\n.Q16:\nZvaa 64,128,160\nRa 224,45,0\nZvaa 96,128,164\nRa 228,45,2048\nZvaa 128,128,168\nRa 232,47,0\nZvaa 160,128,172\nRa 236,47,2048\nSv 204,1,16,75,3072\n"
"s_lshl3_add_u32 s5, s12, s13\ns_lshl2_add_u32 s5, s5, s11\ns_lshl2_add_u32 s5, s5, 0xc04a400\nbuffer_load_dword v27, off, s[16:19], s5 sc0 sc1\nZvaa 164,132,172\nTa 192,32,16384\n"
"Zvaa 132,132,168\nZvaa 100,132,164\nTa 194,32,18432\nZvaa 68,132,160\nTa 196,33,16384\nZvaa 72,136,160\nTa 198,33,18432\nZvaa 104,136,164\nTa 200,34,16384\n"
"s_add_u32 s75, s75, s55\nSv 208,1,16,75,0\nZvaa 136,136,168\nTa 202,34,18432\nZvaa 168,136,172\nZvaa 172,140,172\nTa 204,35,16384\nZvaa 140,140,168\nTa 206,35,18432\n"
"Zvaa 108,140,164\nTa 208,36,16384\nZvaa 76,140,160\nTa 210,36,18432\nSv 212,1,16,75,1024\nZvaa 80,144,160\nTa 212,37,16384\nZvaa 112,144,164\nZvaa 144,144,168\nTa 214,37,18432\n"
"Zvaa 176,144,172\nTa 216,38,16384\nZvaa 180,148,172\nTa 218,38,18432\nZvaa 148,148,168\nTa 220,39,16384\nSv 216,1,16,75,2048\nZvaa 116,148,164\nTa 222,39,18432\nZvaa 84,148,160\n"
"Zvaa 88,152,160\nZvaa 120,152,164\nZvaa 152,152,168\nZvaa 184,152,172\nSv 220,1,16,75,3072\nZvaa 188,156,172\nZvaa 156,156,168\nZvaa 124,156,164\nZvaa 92,156,160\n"
"s_waitcnt vmcnt(17) lgkmcnt(0)\ns_barrier\nMvaa 64,192,224\nRa 160,44,49152\nMvaa 96,192,228\nRa 164,44,51200\ns_add_u32 m0, s47, 65536\ns_add_u32 s46, s51, 1572864\nD0 6,32,46,0\n"
"Mvaa 128,192,232\nRa 168,46,49152\nMvaa 160,192,236\nRa 172,46,51200\nMvaa 164,196,236\nTa 128,12,0\nMvaa 132,196,232\nD0 7,32,46,1024\nMvaa 100,196,228\nTa 130,12,2048\n"
"Mvaa 68,196,224\nTa 132,13,0\nMvaa 72,200,224\nTa 134,13,2048\nMvaa 104,200,228\nTa 136,14,0\nD0 8,32,46,2048\nMvaa 136,200,232\nTa 138,14,2048\nMvaa 168,200,236\n"
"Mvaa 172,204,236\nTa 140,15,0\nMvaa 140,204,232\nTa 142,15,2048\nD0 9,32,46,3072\nMvaa 108,204,228\nTa 144,16,0\nMvaa 76,204,224\nTa 146,16,2048\ns_add_u32 m0, s47, 81920\n"
"s_add_u32 s46, s51, 1835008\nMvaa 80,208,224\nTa 148,17,0\nMvaa 112,208,228\nD0 6,32,46,0\nMvaa 144,208,232\nTa 150,17,2048\nMvaa 176,208,236\nTa 152,18,0\nMvaa 180,212,236\n"
"Ta 154,18,2048\nMvaa 148,212,232\nTa 156,19,0\nD0 7,32,46,1024\nMvaa 116,212,228\nTa 158,19,2048\nMvaa 84,212,224\nMvaa 88,216,224\nMvaa 120,216,228\nD0 8,32,46,2048\n"
"Mvaa 152,216,232\nMvaa 184,216,236\nMvaa 188,220,236\nMvaa 156,220,232\nD0 9,32,46,3072\nMvaa 124,220,228\nMvaa 92,220,224\nWL 0\nMvaa 64,128,160\nRa 224,45,49152\n"
"Mvaa 96,128,164\nRa 228,45,51200\ns_add_u32 m0, s47, -65536\ns_add_u32 s46, s52, 512\nD0 40,28,46,0\nMvaa 128,128,168\nRa 232,47,49152\nMvaa 160,128,172\nRa 236,47,51200\n"
"Mvaa 164,132,172\nTa 192,12,16384\nMvaa 132,132,168\nD0 41,28,46,1024\nMvaa 100,132,164\nTa 194,12,18432\nMvaa 68,132,160\nTa 196,13,16384\nMvaa 72,136,160\nTa 198,13,18432\n"
"Mvaa 104,136,164\nTa 200,14,16384\nD0 42,28,46,2048\nMvaa 136,136,168\nTa 202,14,18432\nMvaa 168,136,172\nMvaa 172,140,172\nTa 204,15,16384\nMvaa 140,140,168\nTa 206,15,18432\n"
"D0 43,28,46,3072\nMvaa 108,140,164\nTa 208,16,16384\nSv 224,1,16,76,0\nMvaa 76,140,160\nTa 210,16,18432\nMvaa 80,144,160\nTa 212,17,16384\nMvaa 112,144,164\nMvaa 144,144,168\n"
"Ta 214,17,18432\nMvaa 176,144,172\nTa 216,18,16384\nSv 228,1,16,76,1024\nMvaa 180,148,172\nTa 218,18,18432\nMvaa 148,148,168\nTa 220,19,16384\nMvaa 116,148,164\nTa 222,19,18432\n"
"Mvaa 84,148,160\nMvaa 88,152,160\nSv 232,1,16,76,2048\nMvaa 120,152,164\nMvaa 152,152,168\nMvaa 184,152,172\nMvaa 188,156,172\nMvaa 156,156,168\nSv 236,1,16,76,3072\n"
"Mvaa 124,156,164\nMvaa 92,156,160\ns_waitcnt vmcnt(16) lgkmcnt(0)\ns_barrier\nMvaa 64,192,224\nRa 160,44,16384\nv_cmp_ne_u32 vcc, s15, v27\ns_nop 0\ns_cbranch_vccnz .S59\n.S60:\n"
"Mvaa 96,192,228\nRa 164,44,18432\nbuffer_store_dword v25, off, s[16:19], s54 offset:0 sc0 sc1\ns_add_u32 m0, s47, 0\ns_add_u32 s46, s51, 2097152\nD0 6,32,46,0\nMvaa 128,192,232\n"
"Ra 168,46,16384\nMvaa 160,192,236\nRa 172,46,18432\nMvaa 164,196,236\nTa 128,12,32768\nMvaa 132,196,232\nD0 7,32,46,1024\nMvaa 100,196,228\nTa 130,12,34816\nMvaa 68,196,224\n"
"Ta 132,13,32768\nMvaa 72,200,224\nTa 134,13,34816\nMvaa 104,200,228\nTa 136,14,32768\nD0 8,32,46,2048\nMvaa 136,200,232\nTa 138,14,34816\nMvaa 168,200,236\nMvaa 172,204,236\n"
"Ta 140,15,32768\nMvaa 140,204,232\nTa 142,15,34816\nD0 9,32,46,3072\nMvaa 108,204,228\nTa 144,16,32768\nMvaa 76,204,224\nTa 146,16,34816\ns_add_u32 m0, s47, 16384\n"
"s_add_u32 s46, s51, 2359296\nMvaa 80,208,224\nTa 148,17,32768\nMvaa 112,208,228\nD0 6,32,46,0\nMvaa 144,208,232\nTa 150,17,34816\nMvaa 176,208,236\nTa 152,18,32768\n"
"Mvaa 180,212,236\nTa 154,18,34816\nMvaa 148,212,232\nTa 156,19,32768\nD0 7,32,46,1024\nMvaa 116,212,228\nTa 158,19,34816\nMvaa 84,212,224\nMvaa 88,216,224\nMvaa 120,216,228\n"
"D0 8,32,46,2048\nMvaa 152,216,232\nMvaa 184,216,236\nMvaa 188,220,236\nMvaa 156,220,232\nD0 9,32,46,3072\nMvaa 124,220,228\nMvaa 92,220,224\nWL 0\nMvaa 64,128,160\n"
"Ra 224,45,16384\nMvaa 96,128,164\nRa 228,45,18432\ns_add_u32 m0, s47, -16384\ns_add_u32 s46, s52, 640\nD0 40,28,46,0\nMvaa 128,128,168\nRa 232,47,16384\nMvaa 160,128,172\n"
"Ra 236,47,18432\nMvaa 164,132,172\nTa 192,12,49152\nMvaa 132,132,168\nD0 41,28,46,1024\nMvaa 100,132,164\nTa 194,12,51200\nMvaa 68,132,160\nTa 196,13,49152\nMvaa 72,136,160\n"
"Ta 198,13,51200\nMvaa 104,136,164\nTa 200,14,49152\nD0 42,28,46,2048\nMvaa 136,136,168\nTa 202,14,51200\nMvaa 168,136,172\nMvaa 172,140,172\nTa 204,15,49152\nMvaa 140,140,168\n"
"Ta 206,15,51200\nD0 43,28,46,3072\nMvaa 108,140,164\nTa 208,16,49152\ns_add_u32 s76, s76, s55\nSv 240,1,16,76,0\nMvaa 76,140,160\nTa 210,16,51200\nMvaa 80,144,160\n"
"Ta 212,17,49152\nMvaa 112,144,164\nMvaa 144,144,168\nTa 214,17,51200\nMvaa 176,144,172\nTa 216,18,49152\nSv 244,1,16,76,1024\nMvaa 180,148,172\nTa 218,18,51200\nMvaa 148,148,168\n"
"Ta 220,19,49152\nMvaa 116,148,164\nTa 222,19,51200\nMvaa 84,148,160\nMvaa 88,152,160\nSv 248,1,16,76,2048\nMvaa 120,152,164\nMvaa 152,152,168\nMvaa 184,152,172\nMvaa 188,156,172\n"
"Mvaa 156,156,168\nSv 252,1,16,76,3072\nMvaa 124,156,164\nMvaa 92,156,160\ns_waitcnt vmcnt(25) lgkmcnt(0)\ns_barrier\nMvaa 64,192,224\nRa 160,44,32768\nAR 60,0\nAR 61,1\n"
"Mvaa 96,192,228\nRa 164,44,34816\ns_add_u32 m0, s47, 32768\ns_add_u32 s46, s51, 2621440\nD0 6,32,46,0\nMvaa 128,192,232\nRa 168,46,32768\nCV 52,60,61\nMvaa 160,192,236\n"
"Ra 172,46,34816\nAR 60,2\nMvaa 164,196,236\nTa 128,32,0\nAR 61,3\nMvaa 132,196,232\nD0 7,32,46,1024\nCV 53,60,61\nMvaa 100,196,228\nTa 130,32,2048\nAR 60,4\nMvaa 68,196,224\n"
"Ta 132,33,0\nAR 61,5\nMvaa 72,200,224\nTa 134,33,2048\nCV 54,60,61\nMvaa 104,200,228\nTa 136,34,0\nD0 8,32,46,2048\nMvaa 136,200,232\nTa 138,34,2048\nAR 60,6\nMvaa 168,200,236\n"
"AR 61,7\nCV 55,60,61\nMvaa 172,204,236\nTa 140,35,0\nAR 60,8\nMvaa 140,204,232\nTa 142,35,2048\nD0 9,32,46,3072\nMvaa 108,204,228\nTa 144,36,0\nAR 61,9\nMvaa 76,204,224\n"
"Ta 146,36,2048\ns_add_u32 m0, s47, 49152\ns_add_u32 s46, s51, 2883584\nMvaa 80,208,224\nTa 148,37,0\nCV 56,60,61\nAR 60,10\nMvaa 112,208,228\nD0 6,32,46,0\nAR 61,11\n"
"Mvaa 144,208,232\nTa 150,37,2048\nCV 57,60,61\nMvaa 176,208,236\nTa 152,38,0\nAR 60,12\nMvaa 180,212,236\nTa 154,38,2048\nAR 61,13\nMvaa 148,212,232\nTa 156,39,0\nD0 7,32,46,1024\n"
"Mvaa 116,212,228\nTa 158,39,2048\nCV 58,60,61\nMvaa 84,212,224\nAR 60,14\nAR 61,15\nMvaa 88,216,224\nCV 59,60,61\nAR 60,16\nMvaa 120,216,228\nD0 8,32,46,2048\nAR 61,17\n"
"Mvaa 152,216,232\nCV 20,60,61\nAR 60,18\nMvaa 184,216,236\nAR 61,19\nCV 21,60,61\nMvaa 188,220,236\nAR 60,20\nAR 61,21\nMvaa 156,220,232\nD0 9,32,46,3072\nCV 22,60,61\n"
"Mvaa 124,220,228\nAR 60,22\nAR 61,23\nMvaa 92,220,224\nWL 0\nMvaa 64,128,160\nRa 224,45,32768\nCV 23,60,61\nAR 60,24\nMvaa 96,128,164\nRa 228,45,34816\ns_add_u32 m0, s47, -49152\n"
"s_add_u32 s46, s52, 768\nD0 40,28,46,0\nMvaa 128,128,168\nRa 232,47,32768\nAR 61,25\nMvaa 160,128,172\nRa 236,47,34816\nCV 2,60,61\nMvaa 164,132,172\nTa 192,32,16384\nAR 60,26\n"
"Mvaa 132,132,168\nD0 41,28,46,1024\nAR 61,27\nMvaa 100,132,164\nTa 194,32,18432\nCV 3,60,61\nMvaa 68,132,160\nTa 196,33,16384\nAR 60,28\nMvaa 72,136,160\nTa 198,33,18432\n"
"AR 61,29\nMvaa 104,136,164\nTa 200,34,16384\nD0 42,28,46,2048\nMvaa 136,136,168\nTa 202,34,18432\nCV 4,60,61\nMvaa 168,136,172\nAR 60,30\nAR 61,31\nMvaa 172,140,172\n"
"Ta 204,35,16384\nCV 5,60,61\nMvaa 140,140,168\nTa 206,35,18432\nD0 43,28,46,3072\nMvaa 108,140,164\nTa 208,36,16384\nSv 52,1,16,77,0\nMvaa 76,140,160\nTa 210,36,18432\n"
"Mvaa 80,144,160\nTa 212,37,16384\nMvaa 112,144,164\nMvaa 144,144,168\nTa 214,37,18432\nMvaa 176,144,172\nTa 216,38,16384\nSv 56,1,16,77,1024\nMvaa 180,148,172\nTa 218,38,18432\n"
"Mvaa 148,148,168\nTa 220,39,16384\nMvaa 116,148,164\nTa 222,39,18432\nMvaa 84,148,160\nMvaa 88,152,160\nSv 20,1,16,77,2048\nMvaa 120,152,164\nMvaa 152,152,168\nMvaa 184,152,172\n"
"Mvaa 188,156,172\nMvaa 156,156,168\nSv 2,1,16,77,3072\nMvaa 124,156,164\nMvaa 92,156,160\ns_waitcnt vmcnt(24) lgkmcnt(0)\ns_barrier\nMvaa 64,192,224\nRa 160,44,0\n"
"v_and_b32 v27, 0x70, v1\nbuffer_load_dword v27, v27, s[16:19], s61 offen offset:128 sc0 sc1\nMvaa 96,192,228\nRa 164,44,2048\ns_add_u32 m0, s47, 65536\n"
"s_add_u32 s46, s51, 3145728\nD0 6,32,46,0\nMvaa 128,192,232\nRa 168,46,0\nAR 60,32\nMvaa 160,192,236\nRa 172,46,2048\nAR 61,33\nMvaa 164,196,236\nTa 128,12,0\nCV 52,60,61\n"
"Mvaa 132,196,232\nD0 7,32,46,1024\nAR 60,34\nMvaa 100,196,228\nTa 130,12,2048\nAR 61,35\nMvaa 68,196,224\nTa 132,13,0\nCV 53,60,61\nMvaa 72,200,224\nTa 134,13,2048\nAR 60,36\n"
"AR 61,37\nMvaa 104,200,228\nTa 136,14,0\nD0 8,32,46,2048\nMvaa 136,200,232\nTa 138,14,2048\nCV 54,60,61\nMvaa 168,200,236\nAR 60,38\nAR 61,39\nMvaa 172,204,236\nTa 140,15,0\n"
"CV 55,60,61\nMvaa 140,204,232\nTa 142,15,2048\nD0 9,32,46,3072\nMvaa 108,204,228\nTa 144,16,0\nAR 60,40\nMvaa 76,204,224\nTa 146,16,2048\ns_add_u32 m0, s47, 81920\n"
"s_add_u32 s46, s51, 3407872\nMvaa 80,208,224\nTa 148,17,0\nAR 61,41\nCV 56,60,61\nMvaa 112,208,228\nD0 6,32,46,0\nAR 60,42\nMvaa 144,208,232\nTa 150,17,2048\nAR 61,43\n"
"Mvaa 176,208,236\nTa 152,18,0\nCV 57,60,61\nMvaa 180,212,236\nTa 154,18,2048\nAR 60,44\nMvaa 148,212,232\nTa 156,19,0\nD0 7,32,46,1024\nMvaa 116,212,228\nTa 158,19,2048\nAR 61,45\n"
"Mvaa 84,212,224\nCV 58,60,61\nAR 60,46\nMvaa 88,216,224\nAR 61,47\nCV 59,60,61\nAR 60,48\nMvaa 120,216,228\nD0 8,32,46,2048\nAR 61,49\nMvaa 152,216,232\nCV 20,60,61\nAR 60,50\n"
"Mvaa 184,216,236\nAR 61,51\nCV 21,60,61\nMvaa 188,220,236\nAR 60,52\nAR 61,53\nMvaa 156,220,232\nD0 9,32,46,3072\nCV 22,60,61\nMvaa 124,220,228\nAR 60,54\nAR 61,55\n"
"Mvaa 92,220,224\nWL 0\nMvaa 64,128,160\nRa 224,45,0\nCV 23,60,61\nAR 60,56\nMvaa 96,128,164\nRa 228,45,2048\ns_add_u32 s6, s56, 0\n"
"buffer_load_dwordx4 v[192:195], v1, s[16:19], s6 offen offset:0 sc1\nMvaa 128,128,168\nRa 232,47,0\nAR 61,57\nMvaa 160,128,172\nRa 236,47,2048\nCV 2,60,61\nMvaa 164,132,172\n"
"Ta 192,12,16384\nAR 60,58\nMvaa 132,132,168\nbuffer_load_dwordx4 v[196:199], v1, s[16:19], s6 offen offset:1024 sc1\nAR 61,59\nMvaa 100,132,164\nTa 194,12,18432\nCV 3,60,61\n"
"Mvaa 68,132,160\nTa 196,13,16384\nAR 60,60\nMvaa 72,136,160\nTa 198,13,18432\nAR 61,61\nCV 4,60,61\nMvaa 104,136,164\nTa 200,14,16384\n"
"buffer_load_dwordx4 v[200:203], v1, s[16:19], s6 offen offset:2048 sc1\nMvaa 136,136,168\nTa 202,14,18432\nAR 60,62\nMvaa 168,136,172\nAR 61,63\nCV 5,60,61\nMvaa 172,140,172\n"
"Ta 204,15,16384\nMvaa 140,140,168\nTa 206,15,18432\nbuffer_load_dwordx4 v[204:207], v1, s[16:19], s6 offen offset:3072 sc1\nMvaa 108,140,164\nTa 208,16,16384\n"
"s_add_u32 s77, s77, s55\nSv 52,1,16,77,0\nMvaa 76,140,160\nTa 210,16,18432\nMvaa 80,144,160\nTa 212,17,16384\nMvaa 112,144,164\nMvaa 144,144,168\nTa 214,17,18432\n"
"Mvaa 176,144,172\nTa 216,18,16384\nSv 56,1,16,77,1024\nMvaa 180,148,172\nTa 218,18,18432\nMvaa 148,148,168\nTa 220,19,16384\nMvaa 116,148,164\nTa 222,19,18432\nMvaa 84,148,160\n"
"Mvaa 88,152,160\nSv 20,1,16,77,2048\nMvaa 120,152,164\nMvaa 152,152,168\nMvaa 184,152,172\nMvaa 188,156,172\nMvaa 156,156,168\nSv 2,1,16,77,3072\nMvaa 124,156,164\n"
"Mvaa 92,156,160\ns_waitcnt vmcnt(16) lgkmcnt(0)\ns_barrier\nMvaa 64,192,224\nRa 160,44,49152\nv_cmp_ne_u32 vcc, s15, v27\ns_nop 0\ns_cbranch_vccnz .S62\n.S63:\n"
"s_sub_u32 s45, s86, 131072\ns_cmp_eq_u32 s79, 1\ns_cselect_b32 s86, s94, s86\ns_add_u32 m0, s47, -65536\ns_add_u32 s46, s45, 0x0\nMvaa 96,192,228\nRa 164,44,51200\n"
"buffer_store_dword v25, off, s[16:19], s54 offset:8 sc0 sc1\nMvaa 128,192,232\nRa 168,46,49152\nD1 1,16,46,0\nMvaa 160,192,236\nRa 172,46,51200\nAR 60,126\nMvaa 164,196,236\n"
"Ta 128,12,32768\nAR 61,127\nMvaa 132,196,232\nCV 60,60,61\nAW 127,60\nMvaa 100,196,228\nTa 130,12,34816\nD1 1,16,46,1024\nMvaa 68,196,224\nTa 132,13,32768\nAR 60,124\n"
"Mvaa 72,200,224\nTa 134,13,34816\nAR 61,125\nMvaa 104,200,228\nTa 136,14,32768\nCV 60,60,61\nMvaa 136,200,232\nTa 138,14,34816\nD1 1,16,46,2048\nMvaa 168,200,236\nAW 126,60\n"
"AR 60,122\nMvaa 172,204,236\nTa 140,15,32768\nAR 61,123\nMvaa 140,204,232\nTa 142,15,34816\nCV 60,60,61\nMvaa 108,204,228\nTa 144,16,32768\nD1 1,16,46,3072\nMvaa 76,204,224\n"
"Ta 146,16,34816\nAW 125,60\nMvaa 80,208,224\nTa 148,17,32768\ns_add_u32 s6, s56, 4096\nMvaa 112,208,228\nAR 60,120\nAR 61,121\nMvaa 144,208,232\nTa 150,17,34816\n"
"buffer_load_dwordx4 v[208:211], v1, s[16:19], s6 offen offset:0 sc1\nMvaa 176,208,236\nTa 152,18,32768\nCV 60,60,61\nMvaa 180,212,236\nTa 154,18,34816\nAW 124,60\n"
"Mvaa 148,212,232\nTa 156,19,32768\nAR 60,118\nMvaa 116,212,228\nTa 158,19,34816\nbuffer_load_dwordx4 v[212:215], v1, s[16:19], s6 offen offset:1024 sc1\nMvaa 84,212,224\n"
"AR 61,119\nCV 60,60,61\nMvaa 88,216,224\nAW 123,60\nAR 60,116\nMvaa 120,216,228\nAR 61,117\nCV 60,60,61\nMvaa 152,216,232\n"
"buffer_load_dwordx4 v[216:219], v1, s[16:19], s6 offen offset:2048 sc1\nAW 122,60\nMvaa 184,216,236\nAR 60,114\nAR 61,115\nMvaa 188,220,236\nCV 60,60,61\nAW 121,60\n"
"Mvaa 156,220,232\nAR 60,112\nAR 61,113\nMvaa 124,220,228\nbuffer_load_dwordx4 v[220:223], v1, s[16:19], s6 offen offset:3072 sc1\nCV 60,60,61\nMvaa 92,220,224\nWL 0\n"
"Mvaa 64,128,160\nRa 224,45,49152\ns_add_u32 s6, s56, 16384\nMvaa 96,128,164\nRa 228,45,51200\nAW 120,60\nMvaa 128,128,168\nRa 232,47,49152\n"
"buffer_load_dwordx4 v[224:227], v1, s[16:19], s6 offen offset:0 sc1\nMvaa 160,128,172\nRa 236,47,51200\nAR 60,110\nMvaa 164,132,172\nTa 192,12,49152\nAR 61,111\nMvaa 132,132,168\n"
"CV 60,60,61\nAW 119,60\nMvaa 100,132,164\nTa 194,12,51200\nbuffer_load_dwordx4 v[228:231], v1, s[16:19], s6 offen offset:1024 sc1\nMvaa 68,132,160\nTa 196,13,49152\nAR 60,108\n"
"Mvaa 72,136,160\nTa 198,13,51200\nAR 61,109\nMvaa 104,136,164\nTa 200,14,49152\nCV 60,60,61\nMvaa 136,136,168\nTa 202,14,51200\n"
"buffer_load_dwordx4 v[232:235], v1, s[16:19], s6 offen offset:2048 sc1\nMvaa 168,136,172\nAW 118,60\nAR 60,106\nMvaa 172,140,172\nTa 204,15,49152\nAR 61,107\nMvaa 140,140,168\n"
"Ta 206,15,51200\nCV 60,60,61\nMvaa 108,140,164\nTa 208,16,49152\nbuffer_load_dwordx4 v[236:239], v1, s[16:19], s6 offen offset:3072 sc1\nMvaa 76,140,160\nTa 210,16,51200\n"
"AW 117,60\nMvaa 80,144,160\nTa 212,17,49152\nAR 60,104\nMvaa 112,144,164\nAR 61,105\nCV 60,60,61\nMvaa 144,144,168\nTa 214,17,51200\nAW 116,60\nMvaa 176,144,172\nTa 216,18,49152\n"
"AR 60,102\nMvaa 180,148,172\nTa 218,18,51200\nAR 61,103\nMvaa 148,148,168\nTa 220,19,49152\nCV 60,60,61\nMvaa 116,148,164\nTa 222,19,51200\nAW 115,60\nMvaa 84,148,160\nAR 60,100\n"
"AR 61,101\nMvaa 88,152,160\nCV 60,60,61\nAW 114,60\nMvaa 120,152,164\nAR 60,98\nAR 61,99\nMvaa 152,152,168\nCV 60,60,61\nAW 113,60\nMvaa 184,152,172\nAR 60,96\nAR 61,97\n"
"Mvaa 188,156,172\nCV 60,60,61\nAW 112,60\nMvaa 156,156,168\nAR 60,94\nAR 61,95\nMvaa 124,156,164\nCV 60,60,61\nAW 111,60\nMvaa 92,156,160\ns_waitcnt vmcnt(21) lgkmcnt(0)\n"
"s_barrier\nMvaa 64,192,224\nRa 160,44,16384\nAR 60,92\nMvaa 96,192,228\nRa 164,44,18432\ns_add_u32 s6, s56, 20480\n"
"buffer_load_dwordx4 v[240:243], v1, s[16:19], s6 offen offset:0 sc1\nMvaa 128,192,232\nRa 168,46,16384\nAR 61,93\nMvaa 160,192,236\nRa 172,46,18432\nCV 60,60,61\nMvaa 164,196,236\n"
"Ta 128,32,0\nAW 110,60\nMvaa 132,196,232\nbuffer_load_dwordx4 v[244:247], v1, s[16:19], s6 offen offset:1024 sc1\nAR 60,90\nMvaa 100,196,228\nTa 130,32,2048\nAR 61,91\n"
"Mvaa 68,196,224\nTa 132,33,0\nCV 60,60,61\nMvaa 72,200,224\nTa 134,33,2048\nAW 109,60\nMvaa 104,200,228\nTa 136,34,0\n"
"buffer_load_dwordx4 v[248:251], v1, s[16:19], s6 offen offset:2048 sc1\nMvaa 136,200,232\nTa 138,34,2048\nAR 60,88\nMvaa 168,200,236\nAR 61,89\nCV 60,60,61\nMvaa 172,204,236\n"
"Ta 140,35,0\nAW 108,60\nMvaa 140,204,232\nTa 142,35,2048\nbuffer_load_dwordx4 v[252:255], v1, s[16:19], s6 offen offset:3072 sc1\nMvaa 108,204,228\nTa 144,36,0\nAR 60,86\n"
"Mvaa 76,204,224\nTa 146,36,2048\ns_add_u32 m0, s47, -32768\ns_add_u32 s46, s45, 0x8000\nMvaa 80,208,224\nTa 148,37,0\nAR 61,87\nMvaa 112,208,228\nD1 1,16,46,0\nCV 60,60,61\n"
"Mvaa 144,208,232\nTa 150,37,2048\nAW 107,60\nMvaa 176,208,236\nTa 152,38,0\nAR 60,84\nMvaa 180,212,236\nTa 154,38,2048\nAR 61,85\nMvaa 148,212,232\nTa 156,39,0\nD1 1,16,46,1024\n"
"Mvaa 116,212,228\nTa 158,39,2048\nCV 60,60,61\nMvaa 84,212,224\nAW 106,60\nAR 60,82\nMvaa 88,216,224\nAR 61,83\nCV 60,60,61\nMvaa 120,216,228\nD1 1,16,46,2048\nAW 105,60\n"
"Mvaa 152,216,232\nAR 60,80\nAR 61,81\nMvaa 184,216,236\nCV 60,60,61\nAW 104,60\nMvaa 188,220,236\nAR 60,78\nAR 61,79\nMvaa 156,220,232\nD1 1,16,46,3072\nCV 60,60,61\n"
"Mvaa 124,220,228\nAW 103,60\nAR 60,76\nMvaa 92,220,224\nWL 0\nMvaa 64,128,160\nRa 224,45,16384\nAR 61,77\nMvaa 96,128,164\nRa 228,45,18432\ns_add_u32 m0, s47, -16384\n"
"s_add_u32 s46, s45, 0x400000\nD1 1,16,46,0\nMvaa 128,128,168\nRa 232,47,16384\nCV 60,60,61\nMvaa 160,128,172\nRa 236,47,18432\nAW 102,60\nMvaa 164,132,172\nTa 192,32,16384\n"
"AR 60,74\nMvaa 132,132,168\nD1 1,16,46,1024\nAR 61,75\nMvaa 100,132,164\nTa 194,32,18432\nCV 60,60,61\nMvaa 68,132,160\nTa 196,33,16384\nAW 101,60\nMvaa 72,136,160\n"
"Ta 198,33,18432\nAR 60,72\nMvaa 104,136,164\nTa 200,34,16384\nD1 1,16,46,2048\nMvaa 136,136,168\nTa 202,34,18432\nAR 61,73\nMvaa 168,136,172\nCV 60,60,61\nAW 100,60\n"
"Mvaa 172,140,172\nTa 204,35,16384\nAR 60,70\nMvaa 140,140,168\nTa 206,35,18432\nD1 1,16,46,3072\nMvaa 108,140,164\nTa 208,36,16384\nAR 61,71\nMvaa 76,140,160\nTa 210,36,18432\n"
"CV 60,60,61\nMvaa 80,144,160\nTa 212,37,16384\nAW 99,60\nMvaa 112,144,164\nAR 60,68\nAR 61,69\nMvaa 144,144,168\nTa 214,37,18432\nCV 60,60,61\nMvaa 176,144,172\nTa 216,38,16384\n"
"AW 98,60\nMvaa 180,148,172\nTa 218,38,18432\nAR 60,66\nMvaa 148,148,168\nTa 220,39,16384\nAR 61,67\nMvaa 116,148,164\nTa 222,39,18432\nCV 60,60,61\nMvaa 84,148,160\nAW 97,60\n"
"AR 60,64\nMvaa 88,152,160\nAR 61,65\nCV 60,60,61\nMvaa 120,152,164\nAW 96,60\nMvaa 152,152,168\nMvaa 184,152,172\nMvaa 188,156,172\nMvaa 156,156,168\nMvaa 124,156,164\n"
"Mvaa 92,156,160\ns_waitcnt vmcnt(20) lgkmcnt(0)\ns_barrier\nMvaa 64,192,224\nv_add_u32 v2, 0x0, v1\ns_lshr_b32 s2, s59, 1\nMvaa 68,196,224\ns_add_u32 m0, s47, 0\n"
"s_add_u32 s46, s86, 0xffff0000\nD1 1,16,46,0\nbuffer_store_dword v25, off, s[16:19], s54 offset:12 sc0 sc1\nMvaa 72,200,224\nv_add_u32 v24, s2, v2\nv_xor_b32 v26, 0x1000, v24\n"
"Mvaa 76,204,224\nv_lshrrev_b32 v20, 4, v0\nv_lshlrev_b32 v20, 2, v20\nMvaa 80,208,224\nv_and_b32 v0, 15, v0\nv_sub_u32 v0, v0, v20\nMvaa 84,212,224\nD1 1,16,46,1024\n"
"s_and_b32 s2, s11, 1\nMvaa 88,216,224\ns_lshl_b32 s2, s2, 6\nv_subrev_u32 v3, s2, v0\nv_mul_f32 v64, s57, v64\nMvaa 92,220,224\ns_xor_b32 s2, s2, 64\nv_subrev_u32 v0, s2, v0\n"
"v_mul_f32 v65, s57, v65\nMvaa 96,192,228\nCV 64,64,65\nv_mul_f32 v66, s57, v66\nv_mul_f32 v67, s57, v67\nMvaa 100,196,228\nD1 1,16,46,2048\nCV 65,66,67\nMvaa 104,200,228\n"
"v_mul_f32 v68, s57, v68\nv_mul_f32 v69, s57, v69\nCV 66,68,69\nMvaa 108,204,228\nv_mov_b32 v4, 0\nv_mov_b32 v5, 0\nv_mul_f32 v70, s57, v70\nMvaa 112,208,228\nRa 128,24,0\n"
"v_mov_b32 v6, 0\nv_mul_f32 v71, s57, v71\nMvaa 116,212,228\nD1 1,16,46,3072\nv_mov_b32 v7, 0\nCV 67,70,71\nMvaa 120,216,228\nRa 132,24,1024\nv_mov_b32 v8, 0\n"
"v_mul_f32 v72, s57, v72\nMvaa 124,220,228\ns_add_u32 m0, s47, 16384\ns_add_u32 s46, s45, 0x408000\nv_mul_f32 v73, s57, v73\nMvaa 128,192,232\nRa 136,24,2048\nv_mov_b32 v9, 0\n"
"CV 72,72,73\nMvaa 132,196,232\nD1 1,16,46,0\nv_mov_b32 v10, 0\nMvaa 136,200,232\nRa 140,24,3072\nv_mov_b32 v11, 0\nv_mul_f32 v74, s57, v74\nMvaa 140,204,232\nv_mov_b32 v12, 0\n"
"v_mov_b32 v13, 0\nv_mul_f32 v75, s57, v75\nMvaa 144,208,232\nRa 144,26,0\nv_mov_b32 v14, 0\nCV 73,74,75\nMvaa 148,212,232\nD1 1,16,46,1024\nv_mov_b32 v15, 0\n"
"v_mul_f32 v76, s57, v76\nMvaa 152,216,232\nRa 148,26,1024\nv_mov_b32 v16, 0\nv_mul_f32 v77, s57, v77\nMvaa 156,220,232\nv_mov_b32 v17, 0\nv_mov_b32 v18, 0\nCV 74,76,77\n"
"Mvaa 160,192,236\nRa 152,26,2048\nv_mov_b32 v19, 0\nv_mul_f32 v78, s57, v78\nMvaa 164,196,236\nD1 1,16,46,2048\ns_lshl_b32 s2, s11, 13\nMvaa 168,200,236\nRa 156,26,3072\n"
"s_add_u32 s2, s2, 0x20000\nv_mul_f32 v79, s57, v79\nMvaa 172,204,236\nv_add_u32 v22, s2, v1\nv_xor_b32 v23, 0x2000, v22\nCV 75,78,79\nMvaa 176,208,236\nv_mul_f32 v80, s57, v80\n"
"v_mul_f32 v81, s57, v81\nCV 80,80,81\nMvaa 180,212,236\nD1 1,16,46,3072\nv_mul_f32 v82, s57, v82\nv_mul_f32 v83, s57, v83\nMvaa 184,216,236\nCV 81,82,83\nv_mul_f32 v84, s57, v84\n"
"v_mul_f32 v85, s57, v85\nMvaa 188,220,236\ns_waitcnt vmcnt(25) lgkmcnt(4)\ns_cmp_eq_u32 s79, 1\ns_cbranch_scc1 .E66\n.E67:\ns_nop 2\nCvav 32,128,192,4\nRa 160,24,8192\n"
"CV 82,84,85\nv_mul_f32 v86, s57, v86\nMvav 32,132,200\nv_mul_f32 v87, s57, v87\nCV 83,86,87\nMvav 32,136,208\nRa 164,24,9216\nv_mul_f32 v88, s57, v88\nv_mul_f32 v89, s57, v89\n"
"Mvav 32,140,216\nCV 88,88,89\nv_mul_f32 v90, s57, v90\nv_mul_f32 v91, s57, v91\nCvav 36,140,220,8\nRa 168,24,10240\nCV 89,90,91\nv_mul_f32 v92, s57, v92\nMvav 36,136,212\n"
"v_mul_f32 v93, s57, v93\nCV 90,92,93\nv_mul_f32 v94, s57, v94\nMvav 36,132,204\nRa 172,24,11264\nMvav 36,128,196\ns_waitcnt vmcnt(17) lgkmcnt(4)\ns_barrier\n"
"s_add_u32 m0, s89, 32768\ns_add_u32 s46, s86, s88\nMvav 36,144,228\nRa 176,26,8192\nv_mul_f32 v95, s57, v95\nCV 91,94,95\nMvav 36,148,236\nAW 192,64\nAW 193,65\nMvav 36,152,244\n"
"Ra 180,26,9216\nAW 194,66\nAW 195,67\nSa 96,1,16,78,0\nMvav 36,156,252\nAW 200,72\nAW 201,73\nAW 202,74\nMvav 32,156,248\nRa 184,26,10240\nAW 203,75\nAW 208,80\nMvav 32,152,240\n"
"D1 1,16,46,0\nAW 209,81\nAW 210,82\nMvav 32,148,232\nRa 188,26,11264\nWL 4\nMvav 32,144,224\ns_cmp_eq_u32 s99, 0\ns_cselect_b32 s87, s95, s87\nAW 211,83\nCvav 40,160,192,12\n"
"AW 216,88\nAW 217,89\nAW 218,90\nMvav 40,164,200\nAW 219,91\nv_mov_b32 v86, 0\nMvav 40,168,208\nv_and_b32 v27, 0x70, v1\n"
"buffer_load_dword v27, v27, s[16:19], s61 offen offset:136 sc0 sc1\nv_mov_b32 v87, 0\nSa 100,1,16,78,1024\nMvav 40,172,216\nv_mov_b32 v88, 0\nv_mov_b32 v89, 0\n"
"v_mul_f32 v128, s57, v128\nv_mul_f32 v129, s57, v129\nCV 128,128,129\nv_mul_f32 v130, s57, v130\nv_mul_f32 v131, s57, v131\nCvav 44,172,220,16\nCV 129,130,131\n"
"v_mul_f32 v132, s57, v132\nv_mul_f32 v133, s57, v133\nCV 130,132,133\nv_mul_f32 v134, s57, v134\nv_mul_f32 v135, s57, v135\nCV 131,134,135\nds_write_b128 v23, v[128:131] offset:0\n"
"Mvav 44,168,212\nD1 1,16,46,1024\nv_mul_f32 v136, s57, v136\nv_mul_f32 v137, s57, v137\nCV 136,136,137\nv_mul_f32 v138, s57, v138\nv_mul_f32 v139, s57, v139\nCV 137,138,139\n"
"v_mul_f32 v140, s57, v140\nMvav 44,164,204\nWL 1\nMvav 44,160,196\ns_add_u32 s88, s88, 32768\nv_mul_f32 v141, s57, v141\nCV 138,140,141\nv_mul_f32 v142, s57, v142\n"
"v_mul_f32 v143, s57, v143\nCV 139,142,143\nds_write_b128 v23, v[136:139] offset:2048\nv_mul_f32 v144, s57, v144\nMvav 44,176,228\nv_mul_f32 v145, s57, v145\nCV 144,144,145\n"
"v_mul_f32 v146, s57, v146\nv_mul_f32 v147, s57, v147\nCV 145,146,147\nv_mul_f32 v148, s57, v148\nv_mul_f32 v149, s57, v149\nCV 146,148,149\nMvav 44,180,236\nEX 80,32\n"
"v_mul_f32 v150, s57, v150\nv_mul_f32 v151, s57, v151\nCV 147,150,151\nds_write_b128 v23, v[144:147] offset:4096\nv_mul_f32 v152, s57, v152\nMvav 44,184,244\n"
"v_mul_f32 v153, s57, v153\nCV 152,152,153\nv_mul_f32 v154, s57, v154\nv_mul_f32 v155, s57, v155\nCV 153,154,155\nv_mul_f32 v156, s57, v156\nv_mul_f32 v157, s57, v157\n"
"CV 154,156,157\nSa 104,1,16,78,2048\nMvav 44,188,252\nEX 81,33\nv_mul_f32 v158, s57, v158\nv_mul_f32 v159, s57, v159\nCV 155,158,159\nds_write_b128 v23, v[152:155] offset:6144\n"
"v_mul_f32 v160, s57, v160\nMvav 40,188,248\nv_mul_f32 v161, s57, v161\nCV 160,160,161\nv_mul_f32 v162, s57, v162\nv_mul_f32 v163, s57, v163\nCV 161,162,163\n"
"v_mul_f32 v164, s57, v164\nv_mul_f32 v165, s57, v165\nCV 162,164,165\nMvav 40,184,240\nD1 1,16,46,2048\nMvav 40,180,232\nMvav 40,176,224\nv_mul_f32 v166, s57, v166\n"
"v_mul_f32 v167, s57, v167\nCV 163,166,167\nds_write_b128 v23, v[160:163] offset:1024\nv_mul_f32 v168, s57, v168\nv_mul_f32 v169, s57, v169\nCV 168,168,169\n"
"v_mul_f32 v170, s57, v170\nAD 84,80,81\nv_mul_f32 v171, s57, v171\nCV 169,170,171\nv_mul_f32 v172, s57, v172\nv_mul_f32 v173, s57, v173\nCV 170,172,173\nv_mul_f32 v174, s57, v174\n"
"v_mul_f32 v175, s57, v175\nEX 82,34\nCV 171,174,175\nds_write_b128 v23, v[168:171] offset:3072\nv_mul_f32 v176, s57, v176\nv_mul_f32 v177, s57, v177\nCV 176,176,177\nCV 64,80,81\n"
"v_mul_f32 v178, s57, v178\nv_mul_f32 v179, s57, v179\nCV 177,178,179\nv_mul_f32 v180, s57, v180\nv_mul_f32 v181, s57, v181\nCV 178,180,181\nv_mul_f32 v182, s57, v182\n"
"Sa 108,1,16,78,3072\nEX 83,35\nv_mul_f32 v183, s57, v183\nCV 179,182,183\nds_write_b128 v23, v[176:179] offset:5120\nv_mul_f32 v184, s57, v184\nv_mul_f32 v185, s57, v185\n"
"AD 84,84,82\nCV 184,184,185\nv_mul_f32 v186, s57, v186\nv_mul_f32 v187, s57, v187\nCV 185,186,187\nv_mul_f32 v188, s57, v188\nv_mul_f32 v189, s57, v189\nCV 186,188,189\n"
"D1 1,16,46,3072\ns_add_u32 m0, s89, 49152\nv_mul_f32 v190, s57, v190\nv_mul_f32 v191, s57, v191\nCV 187,190,191\nds_write_b128 v23, v[184:187] offset:7168\n"
"s_waitcnt vmcnt(18) lgkmcnt(0)\ns_barrier\nRa 128,24,32768\nAD 84,84,83\nRa 224,22,0\nRa 228,22,1024\nEX 80,36\nRa 232,22,2048\nRa 236,22,3072\nRa 132,24,33792\nCV 65,82,83\n"
"Ra 240,22,4096\nRa 244,22,5120\nEX 81,37\nRa 248,22,6144\nRa 252,22,7168\nRa 136,24,34816\nv_mul_f32 v96, s57, v96\nv_mul_f32 v97, s57, v97\nCV 96,96,97\ns_add_u32 s46, s87, s88\n"
"D1 1,16,46,0\nv_mul_f32 v98, s57, v98\nv_mul_f32 v99, s57, v99\nRa 140,24,35840\ns_cmp_eq_u32 s99, 1\ns_cselect_b32 s86, s94, s86\nCV 97,98,99\nv_mul_f32 v100, s57, v100\n"
"Ra 144,26,32768\nAD 85,80,81\nv_mul_f32 v101, s57, v101\nCV 98,100,101\nv_mul_f32 v102, s57, v102\nEX 82,38\nv_mul_f32 v103, s57, v103\nCV 99,102,103\nRa 148,26,33792\n"
"CV 68,80,81\nAW 196,96\nAW 197,97\nEX 83,39\nAW 198,98\nAW 199,99\nRa 152,26,34816\nAD 85,85,82\nv_mul_f32 v104, s57, v104\nv_mul_f32 v105, s57, v105\nD1 1,16,46,1024\n"
"CV 104,104,105\nv_mul_f32 v106, s57, v106\nv_mul_f32 v107, s57, v107\nRa 156,26,35840\ns_cmp_eq_u32 s99, s93\ns_cbranch_scc1 .E49\n.E50:\nRa 160,24,40960\nAD 85,85,83\n"
"CV 105,106,107\nv_mul_f32 v108, s57, v108\nEX 80,40\nv_mul_f32 v109, s57, v109\nCV 106,108,109\nRa 164,24,41984\nCV 69,82,83\nv_mul_f32 v110, s57, v110\nv_mul_f32 v111, s57, v111\n"
"EX 81,41\nCV 107,110,111\nAW 204,104\nRa 168,24,43008\nAD 84,84,80\nAW 205,105\nAW 206,106\nD1 1,16,46,2048\nAW 207,107\nv_mul_f32 v112, s57, v112\nv_mul_f32 v113, s57, v113\n"
"Ra 172,24,44032\nWL 8\ns_xor_b32 s89, s89, 0x10000\nCV 112,112,113\nv_mul_f32 v114, s57, v114\nv_mul_f32 v115, s57, v115\nCvav 48,128,192,4\nRa 176,26,40960\nAD 84,84,81\n"
"CV 113,114,115\nv_mul_f32 v116, s57, v116\nv_mul_f32 v117, s57, v117\nMvav 48,132,200\nEX 82,42\nCV 114,116,117\nv_mul_f32 v118, s57, v118\nMvav 48,136,208\nRa 180,26,41984\n"
"CV 66,80,81\nv_mul_f32 v119, s57, v119\nCV 115,118,119\nMvav 48,140,216\nEX 83,43\nAW 212,112\nAW 213,113\nWV 9\nv_cmp_ne_u32 vcc, s15, v27\ns_nop 0\ns_cbranch_vccnz .S68\n.S69:\n"
"Cvav 52,140,220,8\nRa 184,26,43008\nAD 84,84,82\nAW 214,114\nAW 215,115\nMvav 52,136,212\nD1 1,16,46,3072\nv_mul_f32 v120, s57, v120\nv_mul_f32 v121, s57, v121\nCV 120,120,121\n"
"Mvav 52,132,204\nRa 188,26,44032\nWL 8\nMvav 52,128,196\ns_add_u32 m0, s89, 0\ns_add_u32 s46, s86, s88\nv_mul_f32 v122, s57, v122\nv_mul_f32 v123, s57, v123\nMvav 52,144,228\n"
"Ra 128,2,49152\nAD 84,84,83\nCV 121,122,123\nv_mul_f32 v124, s57, v124\nMvav 52,148,236\nEX 80,44\nv_mul_f32 v125, s57, v125\nCV 122,124,125\nMvav 52,152,244\nRa 132,2,50176\n"
"CV 67,82,83\nv_mul_f32 v126, s57, v126\nv_mul_f32 v127, s57, v127\ns_add_u32 s78, s78, s55\nSa 112,1,16,78,0\nMvav 52,156,252\nEX 81,45\nCV 123,126,127\nAW 220,120\n"
"Mvav 48,156,248\nRa 136,2,51200\nAD 85,85,80\nAW 221,121\nAW 222,122\nMvav 48,152,240\nD1 1,16,46,0\nv_xor_b32 v24, 0x10000, v24\nAW 223,123\nv_mov_b32 v112, -1\nMvav 48,148,232\n"
"Ra 140,2,52224\nWL 8\nMvav 48,144,224\nv_mov_b32 v113, -1\nv_mov_b32 v114, -1\nv_mov_b32 v115, -1\nCvav 56,160,192,12\nRa 144,2,53248\nAD 85,85,81\nMvav 56,164,200\nEX 82,46\n"
"Mvav 56,168,208\nRa 148,2,54272\nCV 70,80,81\nSa 116,1,16,78,1024\nMvav 56,172,216\nEX 83,47\nCvav 60,172,220,16\nRa 152,2,55296\nAD 85,85,82\nMvav 60,168,212\nD1 1,16,46,1024\n"
"v_xor_b32 v26, 0x10000, v26\nMvav 60,164,204\nRa 156,2,56320\nWL 8\nMvav 60,160,196\ns_add_u32 s88, s88, 32768\nMvav 60,176,228\nRa 160,2,57344\nAD 85,85,83\nMvav 60,180,236\n"
"EX 80,48\nMvav 60,184,244\nRa 164,2,58368\nCV 71,82,83\nSa 120,1,16,78,2048\nMvav 60,188,252\nEX 81,49\nMvav 56,188,248\nRa 168,2,59392\nv_max_f32 v90, v84, v85\nMvav 56,184,240\n"
"D1 1,16,46,2048\nv_cmp_lt_f32 vcc, s63, v90\nMvav 56,180,232\nRa 172,2,60416\nWL 8\nMvav 56,176,224\ns_cbranch_vccnz .T51\n.T52:\nv_mov_b32 v20, v84\nv_mov_b32 v21, v85\n"
"v_mov_b32 v22, v20\nv_mov_b32 v23, v21\ns_nop 0\nv_permlane32_swap_b32 v20, v22\nv_permlane32_swap_b32 v21, v23\nAD 20,20,22\nAD 21,21,23\nv_mov_b32 v22, v20\nv_mov_b32 v23, v21\n"
"s_nop 0\nv_permlane16_swap_b32 v20, v22\nv_permlane16_swap_b32 v21, v23\nAD 20,20,22\nAD 21,21,23\nv_min_f32 v90, v20, v21\nv_cmp_gt_f32 vcc, s64, v90\ns_nop 0\n"
"s_cbranch_vccnz .T53\n.T54:\nAD 86,86,84\nZaav 0,128,64\nRa 176,2,61440\nAD 84,80,81\nZaav 4,128,68\nEX 82,50\nZaav 12,132,68\nRa 180,2,62464\nCV 72,80,81\nSa 124,1,16,78,3072\n"
"Zaav 8,132,64\nEX 83,51\nZaav 16,136,64\nRa 184,2,63488\nAD 84,84,82\nZaav 20,136,68\nD1 1,16,46,3072\ns_add_u32 m0, s89, 16384\nZaav 28,140,68\nRa 188,2,64512\nZaav 24,140,64\n"
"WL 8\ns_barrier\nZaav 32,144,64\nRa 128,24,0\nAD 84,84,83\nZaav 36,144,68\nEX 80,52\nZaav 44,148,68\nRa 132,24,1024\nCV 73,82,83\nZaav 40,148,64\nEX 81,53\nZaav 48,152,64\n"
"Ra 136,24,2048\nAD 87,87,85\nZaav 52,152,68\ns_add_u32 s46, s87, s88\nD1 1,16,46,0\nZaav 60,156,68\nRa 140,24,3072\nWL 8\nZaav 56,156,64\ns_sub_u32 s90, s90, 1\n"
"s_cbranch_scc1 .E55\n.E56:\nZaav 64,160,64\nRa 144,26,0\nAD 85,80,81\nZaav 68,160,68\nEX 82,54\nZaav 76,164,68\nRa 148,26,1024\nCV 76,80,81\nZaav 72,164,64\nEX 83,55\n"
"Zaav 80,168,64\nRa 152,26,2048\nAD 85,85,82\nZaav 84,168,68\nD1 1,16,46,1024\nv_xor_b32 v2, 0x10000, v2\nZaav 92,172,68\nRa 156,26,3072\nWL 8\nZaav 88,172,64\n"
"s_cmp_eq_u32 s99, s92\ns_cbranch_scc1 .E57\n.E58:\ns_nop 2\nZaav 96,176,64\nRa 160,24,8192\nAD 85,85,83\nZaav 100,176,68\nEX 80,56\nZaav 108,180,68\nRa 164,24,9216\nCV 77,82,83\n"
"v_and_b32 v27, 0xf0, v1\nbuffer_load_dword v27, v27, s[16:19], s61 offen offset:12 sc0 sc1\nZaav 104,180,64\nEX 81,57\nZaav 112,184,64\nRa 168,24,10240\nAD 84,84,80\n"
"Zaav 116,184,68\nD1 1,16,46,2048\nZaav 124,188,68\nRa 172,24,11264\nWL 8\nZaav 120,188,64\ns_sub_u32 s99, s99, 1\ns_cbranch_scc1 .SW8\ns_branch .A18\n.E49:\nv_mov_b32 v12, s97\n"
"v_mov_b32 v13, s97\nv_mov_b32 v14, s97\nv_mov_b32 v15, s97\nv_cmpx_gt_i32 vcc, 3, v0\nv_mov_b32 v7, s97\nv_mov_b32 v19, s97\nv_cmpx_gt_i32 vcc, 2, v0\nv_mov_b32 v6, s97\n"
"v_mov_b32 v18, s97\nv_cmpx_gt_i32 vcc, 1, v0\nv_mov_b32 v5, s97\nv_mov_b32 v17, s97\nv_cmpx_gt_i32 vcc, 0, v0\nv_mov_b32 v4, s97\nv_mov_b32 v16, s97\nv_cmpx_gt_i32 vcc, -16, v0\n"
"v_mov_b32 v8, s97\nv_mov_b32 v9, s97\nv_mov_b32 v10, s97\nv_mov_b32 v11, s97\ns_mov_b64 exec, -1\ns_branch .E50\n.T51:\ns_mov_b32 s62, 1\ns_call_b64 s[80:81], .RA12\n"
"s_branch .T52\n.T53:\ns_mov_b32 s62, 1\ns_call_b64 s[80:81], .RA12\ns_branch .T54\n.E55:\ns_swappc_b64 s[80:81], s[8:9]\ns_branch .E56\n.E57:\nv_mov_b32 v12, s97\n"
"v_mov_b32 v13, s97\nv_mov_b32 v14, s97\nv_mov_b32 v15, s97\nv_cmpx_gt_i32 vcc, 3, v3\nv_mov_b32 v7, s97\nv_mov_b32 v19, s97\nv_cmpx_gt_i32 vcc, 2, v3\nv_mov_b32 v6, s97\n"
"v_mov_b32 v18, s97\nv_cmpx_gt_i32 vcc, 1, v3\nv_mov_b32 v5, s97\nv_mov_b32 v17, s97\nv_cmpx_gt_i32 vcc, 0, v3\nv_mov_b32 v4, s97\nv_mov_b32 v16, s97\nv_cmpx_gt_i32 vcc, -16, v3\n"
"v_mov_b32 v8, s97\nv_mov_b32 v9, s97\nv_mov_b32 v10, s97\nv_mov_b32 v11, s97\ns_mov_b64 exec, -1\ns_branch .E58\n.S59:\ns_mov_b32 s7, 262144\n.S61:\ns_sleep 8\n"
"buffer_load_dword v27, off, s[16:19], s5 sc0 sc1\nWV 0\nv_cmp_ne_u32 vcc, s15, v27\ns_nop 0\ns_cbranch_vccz .S60\ns_sub_u32 s7, s7, 1\ns_cbranch_scc0 .S61\ns_branch .S60\n.S62:\n"
"s_mov_b32 s7, 262144\n.S64:\ns_sleep 8\nv_and_b32 v27, 0x70, v1\nbuffer_load_dword v27, v27, s[16:19], s61 offen offset:128 sc0 sc1\nWV 0\nv_cmp_ne_u32 vcc, s15, v27\ns_nop 0\n"
"s_cbranch_vccz .S63\ns_sub_u32 s7, s7, 1\ns_cbranch_scc0 .S64\ns_branch .S63\n.E66:\ns_cmp_eq_u32 s11, 3\ns_cbranch_scc1 .P65\ns_cmp_lt_u32 s11, 2\ns_cselect_b32 s64, -1.0, s64\n"
"s_cselect_b32 s2, 64, 0\nv_subrev_u32 v20, s2, v3\nv_mov_b32 v12, s97\nv_mov_b32 v13, s97\nv_mov_b32 v14, s97\nv_mov_b32 v15, s97\nv_cmpx_gt_i32 vcc, 3, v20\nv_mov_b32 v7, s97\n"
"v_mov_b32 v19, s97\nv_cmpx_gt_i32 vcc, 2, v20\nv_mov_b32 v6, s97\nv_mov_b32 v18, s97\nv_cmpx_gt_i32 vcc, 1, v20\nv_mov_b32 v5, s97\nv_mov_b32 v17, s97\nv_cmpx_gt_i32 vcc, 0, v20\n"
"v_mov_b32 v4, s97\nv_mov_b32 v16, s97\nv_cmpx_gt_i32 vcc, -16, v20\nv_mov_b32 v8, s97\nv_mov_b32 v9, s97\nv_mov_b32 v10, s97\nv_mov_b32 v11, s97\ns_mov_b64 exec, -1\n.P65:\n"
"s_branch .E67\n.S68:\ns_mov_b32 s7, 262144\n.S70:\ns_sleep 8\nv_and_b32 v27, 0x70, v1\nbuffer_load_dword v27, v27, s[16:19], s61 offen offset:136 sc0 sc1\nWV 0\n"
"v_cmp_ne_u32 vcc, s15, v27\ns_nop 0\ns_cbranch_vccz .S69\ns_sub_u32 s7, s7, 1\ns_cbranch_scc0 .S70\ns_branch .S69\n.F07:\nCvav 32,128,192,4\nRa 160,24,8192\nMvav 32,132,200\n"
"Mvav 32,136,208\nRa 164,24,9216\nMvav 32,140,216\nCvav 36,140,220,8\nRa 168,24,10240\nAR 92,16\nAR 93,17\nCV 136,92,93\nMvav 36,136,212\nAR 92,18\nAR 93,19\nCV 137,92,93\n"
"AR 92,20\nMvav 36,132,204\nRa 172,24,11264\nMvav 36,128,196\nWL 4\ns_barrier\ns_add_u32 m0, s89, 32768\ns_add_u32 s46, s86, s88\nMvav 36,144,228\nRa 176,26,8192\nAR 93,21\n"
"CV 138,92,93\nAR 92,22\nMvav 36,148,236\nAR 93,23\nCV 139,92,93\nAR 92,24\nMvav 36,152,244\nRa 180,26,9216\nAR 93,25\nCV 140,92,93\nAR 92,26\nSv 96,1,16,76,0\nMvav 36,156,252\n"
"AR 93,27\nCV 141,92,93\nAR 92,28\nMvav 32,156,248\nRa 184,26,10240\nAR 93,29\nCV 142,92,93\nAR 92,30\nMvav 32,152,240\nD1 1,16,46,0\nAR 93,31\nCV 143,92,93\nAR 92,32\n"
"Mvav 32,148,232\nRa 188,26,11264\nWL 4\nMvav 32,144,224\nAR 93,33\nCV 144,92,93\nAR 92,34\nAR 93,35\nCvav 40,160,192,12\nCV 145,92,93\nAR 92,36\nAR 93,37\nCV 146,92,93\n"
"Mvav 40,164,200\nAR 92,38\nAR 93,39\nCV 147,92,93\nMvav 40,168,208\nv_and_b32 v27, 0x70, v1\nbuffer_load_dword v27, v27, s[16:19], s61 offen offset:136 sc0 sc1\nAR 92,40\n"
"AR 93,41\nSv 100,1,16,76,1024\nMvav 40,172,216\nCV 148,92,93\nAR 92,42\nAR 93,43\nCvav 44,172,220,16\nCV 149,92,93\nAR 92,44\nAR 93,45\nCV 150,92,93\nMvav 44,168,212\n"
"D1 1,16,46,1024\nAR 92,46\nAR 93,47\nCV 151,92,93\nMvav 44,164,204\nWL 0\nMvav 44,160,196\ns_add_u32 s88, s88, 32768\nAR 92,48\nAR 93,49\nCV 152,92,93\nMvav 44,176,228\nAR 92,50\n"
"AR 93,51\nCV 153,92,93\nAR 92,52\nMvav 44,180,236\nEX 80,32\nAR 93,53\nMvav 44,184,244\nCV 154,92,93\nAR 92,54\nAR 93,55\nCV 155,92,93\nSv 104,1,16,76,2048\nMvav 44,188,252\n"
"EX 81,33\nAR 92,56\nMvav 40,188,248\nAR 93,57\nCV 156,92,93\nAR 92,58\nAR 93,59\nMvav 40,184,240\nD1 1,16,46,2048\nMvav 40,180,232\nMvav 40,176,224\nCV 157,92,93\nAR 92,60\n"
"AR 93,61\nCV 158,92,93\nAD 84,80,81\nAR 92,62\nAR 93,63\nCV 159,92,93\nEX 82,34\nAR 92,64\nCV 64,80,81\nAR 93,65\nCV 160,92,93\nAR 92,66\nSv 108,1,16,76,3072\nEX 83,35\nAR 93,67\n"
"AD 84,84,82\nCV 161,92,93\nAR 92,68\nAR 93,69\nD1 1,16,46,3072\ns_add_u32 m0, s89, 49152\nCV 162,92,93\nAR 92,70\nWV 9\ns_barrier\nRa 128,24,32768\nAD 84,84,83\nAR 93,71\n"
"CV 163,92,93\nEX 80,36\nbuffer_store_dword v25, off, s[16:19], s54 offset:12 sc0 sc1\nRa 132,24,33792\nCV 65,82,83\nAR 92,72\nAR 93,73\nEX 81,37\nCV 164,92,93\nRa 136,24,34816\n"
"AR 92,74\nAR 93,75\nCV 165,92,93\ns_add_u32 s46, s87, s88\nD1 1,16,46,0\nAR 92,76\nAR 93,77\nRa 140,24,35840\nCV 166,92,93\nAR 92,78\nAR 93,79\nCV 167,92,93\nRa 144,26,32768\n"
"AD 85,80,81\nAR 92,80\nAR 93,81\nEX 82,38\nCV 168,92,93\nRa 148,26,33792\nCV 68,80,81\nAR 92,82\nAR 93,83\nEX 83,39\nCV 169,92,93\nRa 152,26,34816\nAD 85,85,82\nAR 92,84\n"
"AR 93,85\nD1 1,16,46,1024\nCV 170,92,93\nAR 92,86\nAR 93,87\nRa 156,26,35840\nCV 171,92,93\nAR 92,88\nAR 93,89\nCV 172,92,93\nRa 160,24,40960\nAD 85,85,83\nAR 92,90\nAR 93,91\n"
"EX 80,40\nCV 173,92,93\nRa 164,24,41984\nCV 69,82,83\nAR 92,92\nAR 93,93\nEX 81,41\nCV 174,92,93\nRa 168,24,43008\nAD 84,84,80\nAR 92,94\nAR 93,95\nD1 1,16,46,2048\nCV 175,92,93\n"
"AR 92,96\nAR 93,97\nRa 172,24,44032\nWL 8\ns_xor_b32 s89, s89, 0x10000\nCV 176,92,93\nAR 92,98\nAR 93,99\nCvav 48,128,192,4\nRa 176,26,40960\nAD 84,84,81\nCV 177,92,93\nAR 92,100\n"
"Mvav 48,132,200\nEX 82,42\nAR 93,101\nMvav 48,136,208\nRa 180,26,41984\nCV 66,80,81\nCV 178,92,93\nAR 92,102\nMvav 48,140,216\nEX 83,43\nAR 93,103\nWV 10\n"
"v_cmp_ne_u32 vcc, s15, v27\ns_nop 0\ns_cbranch_vccnz .S86\n.S87:\nCvav 52,140,220,8\nRa 184,26,43008\nAD 84,84,82\nCV 179,92,93\nAR 92,104\nMvav 52,136,212\nD1 1,16,46,3072\n"
"AR 93,105\nCV 180,92,93\nAR 92,106\nMvav 52,132,204\nRa 188,26,44032\nWL 8\nMvav 52,128,196\ns_add_u32 m0, s89, 0\ns_add_u32 s46, s86, s88\nAR 93,107\nCV 181,92,93\n"
"Mvav 52,144,228\nRa 128,2,49152\nAD 84,84,83\nAR 92,108\nAR 93,109\nMvav 52,148,236\nEX 80,44\nCV 182,92,93\nMvav 52,152,244\nRa 132,2,50176\nCV 67,82,83\nAR 92,110\nAR 93,111\n"
"s_add_u32 s76, s76, s55\nSv 112,1,16,76,0\nMvav 52,156,252\nEX 81,45\nCV 183,92,93\nMvav 48,156,248\nRa 136,2,51200\nAD 85,85,80\nAR 92,112\nAR 93,113\nMvav 48,152,240\n"
"D1 1,16,46,0\nv_xor_b32 v24, 0x10000, v24\nCV 184,92,93\nAR 92,114\nMvav 48,148,232\nRa 140,2,52224\nWL 8\nMvav 48,144,224\nAR 93,115\nCV 185,92,93\nAR 92,116\nAR 93,117\n"
"Cvav 56,160,192,12\nRa 144,2,53248\nAD 85,85,81\nCV 186,92,93\nAR 92,118\nMvav 56,164,200\nEX 82,46\nAR 93,119\nMvav 56,168,208\nRa 148,2,54272\nCV 70,80,81\nCV 187,92,93\n"
"AR 92,120\nSv 116,1,16,76,1024\nMvav 56,172,216\nEX 83,47\nAR 93,121\nCvav 60,172,220,16\nRa 152,2,55296\nAD 85,85,82\nCV 188,92,93\nAR 92,122\nMvav 60,168,212\nD1 1,16,46,1024\n"
"v_xor_b32 v26, 0x10000, v26\nAR 93,123\nCV 189,92,93\nMvav 60,164,204\nRa 156,2,56320\nWL 8\nMvav 60,160,196\ns_add_u32 s88, s88, 32768\nAR 92,124\nAR 93,125\nCV 190,92,93\n"
"Mvav 60,176,228\nRa 160,2,57344\nAD 85,85,83\nAR 92,126\nAR 93,127\nMvav 60,180,236\nEX 80,48\nCV 191,92,93\nMvav 60,184,244\nRa 164,2,58368\nCV 71,82,83\nSv 120,1,16,76,2048\n"
"Mvav 60,188,252\nEX 81,49\nMvav 56,188,248\nRa 168,2,59392\nv_max_f32 v90, v84, v85\nMvav 56,184,240\nD1 1,16,46,2048\nv_cmp_lt_f32 vcc, s63, v90\nMvav 56,180,232\nRa 172,2,60416\n"
"WL 8\nMvav 56,176,224\ns_cbranch_vccnz .T71\n.T72:\nv_mov_b32 v20, v84\nv_mov_b32 v21, v85\nv_mov_b32 v22, v20\nv_mov_b32 v23, v21\ns_nop 0\nv_permlane32_swap_b32 v20, v22\n"
"v_permlane32_swap_b32 v21, v23\nAD 20,20,22\nAD 21,21,23\nv_mov_b32 v22, v20\nv_mov_b32 v23, v21\ns_nop 0\nv_permlane16_swap_b32 v20, v22\nv_permlane16_swap_b32 v21, v23\n"
"AD 20,20,22\nAD 21,21,23\nv_min_f32 v90, v20, v21\nv_cmp_gt_f32 vcc, s64, v90\ns_nop 0\ns_cbranch_vccnz .T73\n.T74:\nAD 86,86,84\nZaav 0,128,64\nRa 176,2,61440\nAD 84,80,81\n"
"Zaav 4,128,68\nEX 82,50\nZaav 12,132,68\nRa 180,2,62464\nCV 72,80,81\nSv 124,1,16,76,3072\nZaav 8,132,64\nEX 83,51\nZaav 16,136,64\nRa 184,2,63488\nAD 84,84,82\nZaav 20,136,68\n"
"D1 1,16,46,3072\ns_add_u32 m0, s89, 16384\nZaav 28,140,68\nRa 188,2,64512\nZaav 24,140,64\nWL 8\ns_barrier\nZaav 32,144,64\nRa 128,24,0\nAD 84,84,83\nZaav 36,144,68\nEX 80,52\n"
"Zaav 44,148,68\nRa 132,24,1024\nCV 73,82,83\nZaav 40,148,64\nEX 81,53\nZaav 48,152,64\nRa 136,24,2048\nAD 87,87,85\nZaav 52,152,68\ns_add_u32 s46, s87, s88\nD1 1,16,46,0\n"
"Zaav 60,156,68\nRa 140,24,3072\nWL 8\nZaav 56,156,64\nZaav 64,160,64\nRa 144,26,0\nAD 85,80,81\nZaav 68,160,68\nEX 82,54\nZaav 76,164,68\nRa 148,26,1024\nCV 76,80,81\n"
"Zaav 72,164,64\nEX 83,55\nZaav 80,168,64\nRa 152,26,2048\nAD 85,85,82\nZaav 84,168,68\nD1 1,16,46,1024\nv_xor_b32 v2, 0x10000, v2\nZaav 92,172,68\nRa 156,26,3072\nWL 8\n"
"Zaav 88,172,64\nZaav 96,176,64\nRa 160,24,8192\nAD 85,85,83\nZaav 100,176,68\nEX 80,56\nZaav 108,180,68\nRa 164,24,9216\nCV 77,82,83\nZaav 104,180,64\nEX 81,57\nZaav 112,184,64\n"
"Ra 168,24,10240\nAD 84,84,80\nZaav 116,184,68\nD1 1,16,46,2048\nZaav 124,188,68\nRa 172,24,11264\nWL 8\nZaav 120,188,64\nCvav 32,128,192,4\nRa 176,26,8192\nAD 84,84,81\n"
"Mvav 32,132,200\nEX 82,58\nMvav 32,136,208\nRa 180,26,9216\nCV 74,80,81\nMvav 32,140,216\nEX 83,59\nCvav 36,140,220,8\nRa 184,26,10240\nAD 84,84,82\nMvav 36,136,212\n"
"D1 1,16,46,3072\nMvav 36,132,204\nRa 188,26,11264\nWL 8\nMvav 36,128,196\ns_add_u32 m0, s89, 32768\ns_add_u32 s46, s86, s88\nMvav 36,144,228\nRa 128,2,16384\nAD 84,84,83\n"
"Mvav 36,148,236\nEX 80,60\nMvav 36,152,244\nRa 132,2,17408\nCV 75,82,83\nSv 128,1,16,77,0\nMvav 36,156,252\nEX 81,61\nMvav 32,156,248\nRa 136,2,18432\nAD 85,85,80\n"
"Mvav 32,152,240\nD1 1,16,46,0\nMvav 32,148,232\nRa 140,2,19456\nWL 8\nMvav 32,144,224\nCvav 40,160,192,12\nRa 144,2,20480\nAD 85,85,81\nMvav 40,164,200\nEX 82,62\nMvav 40,168,208\n"
"Ra 148,2,21504\nCV 78,80,81\nSv 132,1,16,77,1024\nMvav 40,172,216\nEX 83,63\nCvav 44,172,220,16\nRa 152,2,22528\nAD 85,85,82\nMvav 44,168,212\nD1 1,16,46,1024\nMvav 44,164,204\n"
"Ra 156,2,23552\nWL 8\nMvav 44,160,196\ns_add_u32 s88, s88, 32768\nMvav 44,176,228\nRa 160,2,24576\nAD 85,85,83\nMvav 44,180,236\nEX 80,32\nMvav 44,184,244\nRa 164,2,25600\n"
"CV 79,82,83\nSv 136,1,16,77,2048\nMvav 44,188,252\nEX 81,33\nMvav 40,188,248\nRa 168,2,26624\nv_max_f32 v90, v84, v85\nMvav 40,184,240\nD1 1,16,46,2048\n"
"v_cmp_lt_f32 vcc, s63, v90\nMvav 40,180,232\nRa 172,2,27648\nWL 8\nMvav 40,176,224\ns_cbranch_vccnz .T75\n.T76:\nAD 86,86,84\nMaav 0,128,72\nRa 176,2,28672\nAD 84,80,81\n"
"Maav 4,128,76\nEX 82,34\nMaav 12,132,76\nRa 180,2,29696\nCV 64,80,81\nSv 140,1,16,77,3072\nMaav 8,132,72\nEX 83,35\nMaav 16,136,72\nRa 184,2,30720\nAD 84,84,82\nMaav 20,136,76\n"
"D1 1,16,46,3072\ns_add_u32 m0, s89, 49152\nMaav 28,140,76\nRa 188,2,31744\nMaav 24,140,72\ns_waitcnt vmcnt(20) lgkmcnt(8)\ns_barrier\nMaav 32,144,72\nRa 128,24,32768\nAD 84,84,83\n"
"Maav 36,144,76\nEX 80,36\nMaav 44,148,76\nRa 132,24,33792\nCV 65,82,83\nMaav 40,148,72\nEX 81,37\nMaav 48,152,72\nRa 136,24,34816\nAD 87,87,85\nMaav 52,152,76\n"
"s_add_u32 s46, s87, s88\nD1 1,16,46,0\nMaav 60,156,76\nRa 140,24,35840\nWL 8\nMaav 56,156,72\nv_mov_b32 v112, -1\nv_mov_b32 v113, -1\nMaav 64,160,72\nRa 144,26,32768\nAD 85,80,81\n"
"Maav 68,160,76\nEX 82,38\nMaav 76,164,76\nRa 148,26,33792\nCV 68,80,81\nMaav 72,164,72\nEX 83,39\nMaav 80,168,72\nRa 152,26,34816\nAD 85,85,82\nMaav 84,168,76\nD1 1,16,46,1024\n"
"v_mov_b32 v114, -1\nMaav 92,172,76\nRa 156,26,35840\nWL 8\nMaav 88,172,72\nv_mov_b32 v115, -1\nMaav 96,176,72\nRa 160,24,40960\nAD 85,85,83\nMaav 100,176,76\nEX 80,40\n"
"Maav 108,180,76\nRa 164,24,41984\nCV 69,82,83\nMaav 104,180,72\nEX 81,41\nMaav 112,184,72\nRa 168,24,43008\nAD 84,84,80\nMaav 116,184,76\nD1 1,16,46,2048\nMaav 124,188,76\n"
"Ra 172,24,44032\nWL 8\nMaav 120,188,72\ns_xor_b32 s89, s89, 0x10000\nCvav 48,128,192,4\nRa 176,26,40960\nAD 84,84,81\nMvav 48,132,200\nEX 82,42\nMvav 48,136,208\nRa 180,26,41984\n"
"CV 66,80,81\nMvav 48,140,216\nEX 83,43\nCvav 52,140,220,8\nRa 184,26,43008\nAD 84,84,82\nv_and_b32 v27, 0xf0, v1\n"
"buffer_load_dword v27, v27, s[16:19], s61 offen offset:12 sc0 sc1\nMvav 52,136,212\nD1 1,16,46,3072\nMvav 52,132,204\nRa 188,26,44032\nWL 8\nMvav 52,128,196\ns_add_u32 m0, s89, 0\n"
"s_add_u32 s46, s86, s88\nMvav 52,144,228\nRa 128,2,49152\nAD 84,84,83\nMvav 52,148,236\nEX 80,44\nMvav 52,152,244\nRa 132,2,50176\nCV 67,82,83\ns_add_u32 s77, s77, s55\n"
"Sv 144,1,16,77,0\nMvav 52,156,252\nEX 81,45\nMvav 48,156,248\nRa 136,2,51200\nAD 85,85,80\nMvav 48,152,240\nD1 1,16,46,0\nv_xor_b32 v24, 0x10000, v24\nMvav 48,148,232\n"
"Ra 140,2,52224\nWL 8\nMvav 48,144,224\nCvav 56,160,192,12\nRa 144,2,53248\nAD 85,85,81\nMvav 56,164,200\nEX 82,46\nMvav 56,168,208\nRa 148,2,54272\nCV 70,80,81\n"
"Sv 148,1,16,77,1024\nMvav 56,172,216\nEX 83,47\nCvav 60,172,220,16\nRa 152,2,55296\nAD 85,85,82\nMvav 60,168,212\nD1 1,16,46,1024\nv_xor_b32 v26, 0x10000, v26\nMvav 60,164,204\n"
"Ra 156,2,56320\nWL 8\nMvav 60,160,196\ns_add_u32 s88, s88, 32768\nMvav 60,176,228\nRa 160,2,57344\nAD 85,85,83\nMvav 60,180,236\nEX 80,48\nMvav 60,184,244\nRa 164,2,58368\n"
"CV 71,82,83\nSv 152,1,16,77,2048\nMvav 60,188,252\nEX 81,49\nMvav 56,188,248\nRa 168,2,59392\nv_max_f32 v90, v84, v85\nMvav 56,184,240\nD1 1,16,46,2048\n"
"v_cmp_lt_f32 vcc, s63, v90\nMvav 56,180,232\nRa 172,2,60416\nWL 8\nMvav 56,176,224\ns_cbranch_vccnz .T77\n.T78:\nAD 86,86,84\nMaav 0,128,64\nRa 176,2,61440\nAD 84,80,81\n"
"Maav 4,128,68\nEX 82,50\nMaav 12,132,68\nRa 180,2,62464\nCV 72,80,81\nSv 156,1,16,77,3072\nMaav 8,132,64\nEX 83,51\nMaav 16,136,64\nRa 184,2,63488\nAD 84,84,82\nMaav 20,136,68\n"
"D1 1,16,46,3072\ns_add_u32 m0, s89, 16384\nMaav 28,140,68\nRa 188,2,64512\nMaav 24,140,64\ns_waitcnt vmcnt(21) lgkmcnt(8)\ns_barrier\nMaav 32,144,64\nRa 128,24,0\nAD 84,84,83\n"
"Maav 36,144,68\nEX 80,52\nMaav 44,148,68\nRa 132,24,1024\nCV 73,82,83\nMaav 40,148,64\nEX 81,53\nMaav 48,152,64\nRa 136,24,2048\nAD 87,87,85\nMaav 52,152,68\n"
"s_add_u32 s46, s87, s88\nD1 1,16,46,0\nMaav 60,156,68\nRa 140,24,3072\nWL 8\nMaav 56,156,64\nMaav 64,160,64\nRa 144,26,0\nAD 85,80,81\nMaav 68,160,68\nEX 82,54\nMaav 76,164,68\n"
"Ra 148,26,1024\nCV 76,80,81\nMaav 72,164,64\nEX 83,55\nMaav 80,168,64\nRa 152,26,2048\nAD 85,85,82\nMaav 84,168,68\nD1 1,16,46,1024\nv_xor_b32 v2, 0x10000, v2\nMaav 92,172,68\n"
"Ra 156,26,3072\nWL 8\nMaav 88,172,64\nMaav 96,176,64\nRa 160,24,8192\nAD 85,85,83\nMaav 100,176,68\nEX 80,56\nMaav 108,180,68\nRa 164,24,9216\nCV 77,82,83\nMaav 104,180,64\n"
"EX 81,57\nMaav 112,184,64\nRa 168,24,10240\nAD 84,84,80\nMaav 116,184,68\nD1 1,16,46,2048\nMaav 124,188,68\nRa 172,24,11264\nWL 8\nMaav 120,188,64\nCvav 32,128,192,4\n"
"Ra 176,26,8192\nAD 84,84,81\nMvav 32,132,200\nEX 82,58\nMvav 32,136,208\nRa 180,26,9216\nCV 74,80,81\nMvav 32,140,216\nEX 83,59\nCvav 36,140,220,8\nRa 184,26,10240\nAD 84,84,82\n"
"Mvav 36,136,212\nD1 1,16,46,3072\nMvav 36,132,204\nRa 188,26,11264\nWL 8\nMvav 36,128,196\ns_add_u32 m0, s89, 32768\ns_add_u32 s46, s86, s88\nMvav 36,144,228\nRa 128,2,16384\n"
"AD 84,84,83\nMvav 36,148,236\nEX 80,60\nMvav 36,152,244\nRa 132,2,17408\nCV 75,82,83\nSv 112,1,16,49,0\nSv 160,1,16,78,0\nMvav 36,156,252\nEX 81,61\nMvav 32,156,248\n"
"Ra 136,2,18432\nAD 85,85,80\nMvav 32,152,240\nD1 1,16,46,0\nMvav 32,148,232\nRa 140,2,19456\nWL 8\nMvav 32,144,224\nCvav 40,160,192,12\nRa 144,2,20480\nAD 85,85,81\n"
"Mvav 40,164,200\nEX 82,62\nMvav 40,168,208\nRa 148,2,21504\nCV 78,80,81\nSv 164,1,16,78,1024\nMvav 40,172,216\nEX 83,63\nCvav 44,172,220,16\nRa 152,2,22528\nAD 85,85,82\n"
"Mvav 44,168,212\nD1 1,16,46,1024\nMvav 44,164,204\nRa 156,2,23552\nWL 8\nMvav 44,160,196\ns_add_u32 s88, s88, 32768\nMvav 44,176,228\nRa 160,2,24576\nAD 85,85,83\nMvav 44,180,236\n"
"EX 80,32\nMvav 44,184,244\nRa 164,2,25600\nCV 79,82,83\nSv 168,1,16,78,2048\nMvav 44,188,252\nEX 81,33\nMvav 40,188,248\nRa 168,2,26624\nv_max_f32 v90, v84, v85\nMvav 40,184,240\n"
"D1 1,16,46,2048\nv_cmp_lt_f32 vcc, s63, v90\nMvav 40,180,232\nRa 172,2,27648\nWL 8\nMvav 40,176,224\ns_cbranch_vccnz .T79\n.T80:\nAD 86,86,84\nMaav 0,128,72\nRa 176,2,28672\n"
"AD 84,80,81\nMaav 4,128,76\nEX 82,34\nMaav 12,132,76\nRa 180,2,29696\nCV 64,80,81\nSv 172,1,16,78,3072\nMaav 8,132,72\nEX 83,35\nMaav 16,136,72\nRa 184,2,30720\nAD 84,84,82\n"
"Maav 20,136,76\nD1 1,16,46,3072\ns_add_u32 m0, s89, 49152\nMaav 28,140,76\nRa 188,2,31744\nMaav 24,140,72\ns_waitcnt vmcnt(21) lgkmcnt(8)\ns_barrier\nMaav 32,144,72\n"
"Ra 128,24,32768\nAD 84,84,83\nMaav 36,144,76\nEX 80,36\nMaav 44,148,76\nRa 132,24,33792\nCV 65,82,83\nMaav 40,148,72\nEX 81,37\nMaav 48,152,72\nRa 136,24,34816\nAD 87,87,85\n"
"Maav 52,152,76\ns_add_u32 s46, s87, s88\nD1 1,16,46,0\nMaav 60,156,76\nRa 140,24,35840\nWL 8\nMaav 56,156,72\nMaav 64,160,72\nRa 144,26,32768\nAD 85,80,81\nMaav 68,160,76\n"
"EX 82,38\nMaav 76,164,76\nRa 148,26,33792\nCV 68,80,81\nMaav 72,164,72\nEX 83,39\nMaav 80,168,72\nRa 152,26,34816\nAD 85,85,82\nMaav 84,168,76\nD1 1,16,46,1024\nMaav 92,172,76\n"
"Ra 156,26,35840\nWL 8\nMaav 88,172,72\nMaav 96,176,72\nRa 160,24,40960\nAD 85,85,83\nMaav 100,176,76\nEX 80,40\nMaav 108,180,76\nRa 164,24,41984\nCV 69,82,83\nMaav 104,180,72\n"
"EX 81,41\nMaav 112,184,72\nRa 168,24,43008\nAD 84,84,80\nMaav 116,184,76\nD1 1,16,46,2048\nMaav 124,188,76\nRa 172,24,44032\nWL 8\nMaav 120,188,72\ns_xor_b32 s89, s89, 0x10000\n"
"Cvav 48,128,192,4\nRa 176,26,40960\nAD 84,84,81\nMvav 48,132,200\nEX 82,42\nMvav 48,136,208\nRa 180,26,41984\nCV 66,80,81\nMvav 48,140,216\nEX 83,43\nv_cmp_ne_u32 vcc, s15, v27\n"
"s_nop 0\ns_cbranch_vccnz .S83\n.S84:\nCvav 52,140,220,8\nRa 184,26,43008\nAD 84,84,82\nMvav 52,136,212\nD1 1,16,46,3072\nMvav 52,132,204\nRa 188,26,44032\nWL 8\nMvav 52,128,196\n"
"s_add_u32 m0, s89, 0\ns_add_u32 s46, s86, s88\nMvav 52,144,228\nRa 128,2,49152\nAD 84,84,83\nMvav 52,148,236\nEX 80,44\nMvav 52,152,244\nRa 132,2,50176\nCV 67,82,83\n"
"Sv 112,1,16,49,1024\ns_add_u32 s78, s78, s55\nSv 176,1,16,78,0\nMvav 52,156,252\nEX 81,45\nMvav 48,156,248\nRa 136,2,51200\nAD 85,85,80\nMvav 48,152,240\nD1 1,16,46,0\n"
"v_xor_b32 v24, 0x10000, v24\nMvav 48,148,232\nRa 140,2,52224\nWL 8\nMvav 48,144,224\nv_lshrrev_b32 v92, 8, v1\nv_bfe_u32 v93, v1, 6, 2\nCvav 56,160,192,12\nRa 144,2,53248\n"
"AD 85,85,81\nMvav 56,164,200\nEX 82,46\nMvav 56,168,208\nRa 148,2,54272\nCV 70,80,81\nSv 180,1,16,78,1024\nMvav 56,172,216\nEX 83,47\nCvav 60,172,220,16\nRa 152,2,55296\n"
"AD 85,85,82\nMvav 60,168,212\nD1 1,16,46,1024\nv_xor_b32 v26, 0x10000, v26\nMvav 60,164,204\nRa 156,2,56320\nWL 8\nMvav 60,160,196\ns_add_u32 s88, s88, 32768\n"
"v_cmp_eq_u32 vcc, v92, v93\nv_and_b32 v92, 16, v1\nv_mov_b32 v93, 0x3f80\nv_cndmask_b32 v93, 0, v93, vcc\nMvav 60,176,228\nRa 160,2,57344\nAD 85,85,83\nMvav 60,180,236\nEX 80,48\n"
"Mvav 60,184,244\nRa 164,2,58368\nCV 71,82,83\nSv 184,1,16,78,2048\nMvav 60,188,252\nEX 81,49\nMvav 56,188,248\nRa 168,2,59392\nv_max_f32 v90, v84, v85\nMvav 56,184,240\n"
"D1 1,16,46,2048\nv_cmp_lt_f32 vcc, s63, v90\nMvav 56,180,232\nRa 172,2,60416\nWL 8\nMvav 56,176,224\ns_cbranch_vccnz .T81\n.T82:\nAD 86,86,84\nMaav 0,128,64\nRa 176,2,61440\n"
"AD 84,80,81\nv_lshlrev_b32 v93, v92, v93\nMaav 4,128,68\nEX 82,50\nMaav 12,132,68\nRa 180,2,62464\nCV 72,80,81\nSv 188,1,16,78,3072\nMaav 8,132,64\nEX 83,51\nMaav 16,136,64\n"
"Ra 184,2,63488\nAD 84,84,82\nMaav 20,136,68\nD1 1,16,46,3072\ns_add_u32 m0, s89, 16384\nMaav 28,140,68\nRa 188,2,64512\nMaav 24,140,64\ns_waitcnt vmcnt(22) lgkmcnt(8)\ns_barrier\n"
"Maav 32,144,64\nRa 128,24,0\nAD 84,84,83\nMaav 36,144,68\nEX 80,52\nMaav 44,148,68\nRa 132,24,1024\nCV 73,82,83\nMaav 40,148,64\nEX 81,53\nMaav 48,152,64\nRa 136,24,2048\n"
"AD 87,87,85\nMaav 52,152,68\ns_add_u32 s46, s87, s88\nD1 1,16,46,0\nMaav 60,156,68\nRa 140,24,3072\nWL 8\nMaav 56,156,64\nv_and_b32 v92, 32, v1\nv_cmp_eq_u32 vcc, 0, v92\n"
"v_mov_b32 v128, 0\nv_mov_b32 v129, 0\nv_cndmask_b32 v130, 0, v93, vcc\nMaav 64,160,64\nRa 144,26,0\nAD 85,80,81\nv_sub_u32 v131, v93, v130\nMaav 68,160,68\nEX 82,54\n"
"Maav 76,164,68\nRa 148,26,1024\nCV 76,80,81\nMaav 72,164,64\nEX 83,55\nMaav 80,168,64\nRa 152,26,2048\nAD 85,85,82\nMaav 84,168,68\nD1 1,16,46,1024\nv_xor_b32 v2, 0x10000, v2\n"
"Maav 92,172,68\nRa 156,26,3072\nWL 8\nMaav 88,172,64\nv_mov_b32 v132, 0\nv_mov_b32 v133, 0\nMaav 96,176,64\nRa 160,24,8192\nAD 85,85,83\nMaav 100,176,68\nEX 80,56\n"
"Maav 108,180,68\nRa 164,24,9216\nCV 77,82,83\nMaav 104,180,64\nEX 81,57\nMaav 112,184,64\nRa 168,24,10240\nAD 84,84,80\nMaav 116,184,68\ns_add_u32 s49, s49, 131072\n"
"D1 1,16,46,2048\nMaav 124,188,68\nRa 172,24,11264\nWL 8\nMaav 120,188,64\ns_mov_b32 s99, 11\ns_sub_u32 s90, s90, 3\n.p2align 6\n.A18:\nCvav 32,128,192,4\nRa 176,26,8192\n"
"AD 84,84,81\nMvav 32,132,200\nEX 82,58\nMvav 32,136,208\nRa 180,26,9216\nCV 74,80,81\nMvav 32,140,216\nEX 83,59\nCvav 36,140,220,8\nRa 184,26,10240\nAD 84,84,82\nMvav 36,136,212\n"
"D1 1,16,46,3072\nMvav 36,132,204\nRa 188,26,11264\nWL 8\nMvav 36,128,196\ns_add_u32 m0, s89, 32768\ns_add_u32 s46, s86, s88\nMvav 36,144,228\nRa 128,2,16384\nAD 84,84,83\n"
"Mvav 36,148,236\nEX 80,60\nMvav 36,152,244\nRa 132,2,17408\nCV 75,82,83\nSv 112,1,16,49,0\nMvav 36,156,252\nEX 81,61\nMvav 32,156,248\nRa 136,2,18432\nAD 85,85,80\n"
"Mvav 32,152,240\nD1 1,16,46,0\nMvav 32,148,232\nRa 140,2,19456\nWL 8\nMvav 32,144,224\ns_cmp_eq_u32 s99, 0\ns_cselect_b32 s87, s95, s87\nCvav 40,160,192,12\nRa 144,2,20480\n"
"AD 85,85,81\nMvav 40,164,200\nEX 82,62\nMvav 40,168,208\nRa 148,2,21504\nCV 78,80,81\nMvav 40,172,216\nEX 83,63\nCvav 44,172,220,16\nRa 152,2,22528\nAD 85,85,82\nMvav 44,168,212\n"
"D1 1,16,46,1024\nMvav 44,164,204\nRa 156,2,23552\nWL 8\nMvav 44,160,196\ns_add_u32 s88, s88, 32768\nMvav 44,176,228\nRa 160,2,24576\nAD 85,85,83\nMvav 44,180,236\nEX 80,32\n"
"Mvav 44,184,244\nRa 164,2,25600\nCV 79,82,83\nMvav 44,188,252\nEX 81,33\nMvav 40,188,248\nRa 168,2,26624\nv_max_f32 v90, v84, v85\nMvav 40,184,240\nD1 1,16,46,2048\n"
"v_cmp_lt_f32 vcc, s63, v90\nMvav 40,180,232\nRa 172,2,27648\nWL 8\nMvav 40,176,224\ns_cbranch_vccnz .T19\n.T20:\nAD 86,86,84\nMaav 0,128,72\nRa 176,2,28672\nAD 84,80,81\n"
"Maav 4,128,76\nEX 82,34\nMaav 12,132,76\nRa 180,2,29696\nCV 64,80,81\nMaav 8,132,72\nEX 83,35\nMaav 16,136,72\nRa 184,2,30720\nAD 84,84,82\nMaav 20,136,76\nD1 1,16,46,3072\n"
"s_add_u32 m0, s89, 49152\nMaav 28,140,76\nRa 188,2,31744\nMaav 24,140,72\ns_waitcnt vmcnt(14) lgkmcnt(8)\ns_barrier\nMaav 32,144,72\nRa 128,24,32768\nAD 84,84,83\nMaav 36,144,76\n"
"EX 80,36\nMaav 44,148,76\nRa 132,24,33792\nCV 65,82,83\nMaav 40,148,72\nEX 81,37\nMaav 48,152,72\nRa 136,24,34816\nAD 87,87,85\nMaav 52,152,76\ns_add_u32 s46, s87, s88\n"
"D1 1,16,46,0\nMaav 60,156,76\nRa 140,24,35840\nWL 8\nMaav 56,156,72\ns_cmp_eq_u32 s99, 1\ns_cselect_b32 s86, s94, s86\nMaav 64,160,72\nRa 144,26,32768\nAD 85,80,81\n"
"Maav 68,160,76\nEX 82,38\nMaav 76,164,76\nRa 148,26,33792\nCV 68,80,81\nMaav 72,164,72\nEX 83,39\nMaav 80,168,72\nRa 152,26,34816\nAD 85,85,82\nMaav 84,168,76\nD1 1,16,46,1024\n"
"Maav 92,172,76\nRa 156,26,35840\nWL 8\nMaav 88,172,72\ns_cmp_eq_u32 s99, s93\ns_cbranch_scc1 .E21\n.E22:\ns_nop 2\nMaav 96,176,72\nRa 160,24,40960\nAD 85,85,83\nMaav 100,176,76\n"
"EX 80,40\nMaav 108,180,76\nRa 164,24,41984\nCV 69,82,83\nMaav 104,180,72\nEX 81,41\nMaav 112,184,72\nRa 168,24,43008\nAD 84,84,80\nMaav 116,184,76\nD1 1,16,46,2048\n"
"Maav 124,188,76\nRa 172,24,44032\nWL 8\nMaav 120,188,72\ns_xor_b32 s89, s89, 0x10000\nCvav 48,128,192,4\nRa 176,26,40960\nAD 84,84,81\nMvav 48,132,200\nEX 82,42\nMvav 48,136,208\n"
"Ra 180,26,41984\nCV 66,80,81\nMvav 48,140,216\nEX 83,43\nCvav 52,140,220,8\nRa 184,26,43008\nAD 84,84,82\nMvav 52,136,212\nD1 1,16,46,3072\nMvav 52,132,204\nRa 188,26,44032\nWL 8\n"
"Mvav 52,128,196\ns_add_u32 m0, s89, 0\ns_add_u32 s46, s86, s88\nMvav 52,144,228\nRa 128,2,49152\nAD 84,84,83\nMvav 52,148,236\nEX 80,44\nMvav 52,152,244\nRa 132,2,50176\n"
"CV 67,82,83\nSv 112,1,16,49,1024\nMvav 52,156,252\nEX 81,45\nMvav 48,156,248\nRa 136,2,51200\nAD 85,85,80\nMvav 48,152,240\nD1 1,16,46,0\nv_xor_b32 v24, 0x10000, v24\n"
"Mvav 48,148,232\nRa 140,2,52224\nWL 8\nMvav 48,144,224\nCvav 56,160,192,12\nRa 144,2,53248\nAD 85,85,81\nMvav 56,164,200\nEX 82,46\nMvav 56,168,208\nRa 148,2,54272\nCV 70,80,81\n"
"Mvav 56,172,216\nEX 83,47\nCvav 60,172,220,16\nRa 152,2,55296\nAD 85,85,82\nMvav 60,168,212\nD1 1,16,46,1024\nv_xor_b32 v26, 0x10000, v26\nMvav 60,164,204\nRa 156,2,56320\nWL 8\n"
"Mvav 60,160,196\ns_add_u32 s88, s88, 32768\nMvav 60,176,228\nRa 160,2,57344\nAD 85,85,83\nMvav 60,180,236\nEX 80,48\nMvav 60,184,244\nRa 164,2,58368\nCV 71,82,83\nMvav 60,188,252\n"
"EX 81,49\nMvav 56,188,248\nRa 168,2,59392\nv_max_f32 v90, v84, v85\nMvav 56,184,240\nD1 1,16,46,2048\nv_cmp_lt_f32 vcc, s63, v90\nMvav 56,180,232\nRa 172,2,60416\nWL 8\n"
"Mvav 56,176,224\ns_cbranch_vccnz .T23\n.T24:\nAD 86,86,84\nMaav 0,128,64\nRa 176,2,61440\nAD 84,80,81\nMaav 4,128,68\nEX 82,50\nMaav 12,132,68\nRa 180,2,62464\nCV 72,80,81\n"
"Maav 8,132,64\nEX 83,51\nMaav 16,136,64\nRa 184,2,63488\nAD 84,84,82\nMaav 20,136,68\nD1 1,16,46,3072\ns_add_u32 m0, s89, 16384\nMaav 28,140,68\nRa 188,2,64512\nMaav 24,140,64\n"
"s_waitcnt vmcnt(14) lgkmcnt(8)\ns_barrier\nMaav 32,144,64\nRa 128,24,0\nAD 84,84,83\nMaav 36,144,68\nEX 80,52\nMaav 44,148,68\nRa 132,24,1024\nCV 73,82,83\nMaav 40,148,64\n"
"EX 81,53\nMaav 48,152,64\nRa 136,24,2048\nAD 87,87,85\nMaav 52,152,68\ns_add_u32 s46, s87, s88\nD1 1,16,46,0\nMaav 60,156,68\nRa 140,24,3072\nWL 8\nMaav 56,156,64\n"
"s_sub_u32 s90, s90, 1\ns_cbranch_scc1 .E25\n.E26:\nMaav 64,160,64\nRa 144,26,0\nAD 85,80,81\nMaav 68,160,68\nEX 82,54\nMaav 76,164,68\nRa 148,26,1024\nCV 76,80,81\nMaav 72,164,64\n"
"EX 83,55\nMaav 80,168,64\nRa 152,26,2048\nAD 85,85,82\nMaav 84,168,68\nD1 1,16,46,1024\nv_xor_b32 v2, 0x10000, v2\nMaav 92,172,68\nRa 156,26,3072\nWL 8\nMaav 88,172,64\n"
"s_cmp_eq_u32 s99, s92\ns_cbranch_scc1 .E27\n.E28:\ns_nop 2\nMaav 96,176,64\nRa 160,24,8192\nAD 85,85,83\nMaav 100,176,68\nEX 80,56\nMaav 108,180,68\nRa 164,24,9216\nCV 77,82,83\n"
"Maav 104,180,64\nEX 81,57\nMaav 112,184,64\nRa 168,24,10240\nAD 84,84,80\nMaav 116,184,68\ns_add_u32 s49, s49, 131072\nD1 1,16,46,2048\nMaav 124,188,68\nRa 172,24,11264\nWL 8\n"
"Maav 120,188,64\ns_sub_u32 s99, s99, 1\ns_cbranch_scc0 .A18\ns_cmp_lg_u32 s14, 0\ns_cbranch_scc1 .SW8\ns_branch .X089\n.T19:\ns_mov_b32 s62, 0\ns_call_b64 s[80:81], .RB13\n"
"s_branch .T20\n.E21:\nv_mov_b32 v12, s97\nv_mov_b32 v13, s97\nv_mov_b32 v14, s97\nv_mov_b32 v15, s97\nv_cmpx_gt_i32 vcc, 3, v0\nv_mov_b32 v7, s97\nv_mov_b32 v19, s97\n"
"v_cmpx_gt_i32 vcc, 2, v0\nv_mov_b32 v6, s97\nv_mov_b32 v18, s97\nv_cmpx_gt_i32 vcc, 1, v0\nv_mov_b32 v5, s97\nv_mov_b32 v17, s97\nv_cmpx_gt_i32 vcc, 0, v0\nv_mov_b32 v4, s97\n"
"v_mov_b32 v16, s97\nv_cmpx_gt_i32 vcc, -16, v0\nv_mov_b32 v8, s97\nv_mov_b32 v9, s97\nv_mov_b32 v10, s97\nv_mov_b32 v11, s97\ns_mov_b64 exec, -1\ns_branch .E22\n.T23:\n"
"s_mov_b32 s62, 0\ns_call_b64 s[80:81], .RA12\ns_branch .T24\n.E25:\ns_swappc_b64 s[80:81], s[8:9]\ns_branch .E26\n.E27:\nv_mov_b32 v12, s97\nv_mov_b32 v13, s97\n"
"v_mov_b32 v14, s97\nv_mov_b32 v15, s97\nv_cmpx_gt_i32 vcc, 3, v3\nv_mov_b32 v7, s97\nv_mov_b32 v19, s97\nv_cmpx_gt_i32 vcc, 2, v3\nv_mov_b32 v6, s97\nv_mov_b32 v18, s97\n"
"v_cmpx_gt_i32 vcc, 1, v3\nv_mov_b32 v5, s97\nv_mov_b32 v17, s97\nv_cmpx_gt_i32 vcc, 0, v3\nv_mov_b32 v4, s97\nv_mov_b32 v16, s97\nv_cmpx_gt_i32 vcc, -16, v3\nv_mov_b32 v8, s97\n"
"v_mov_b32 v9, s97\nv_mov_b32 v10, s97\nv_mov_b32 v11, s97\ns_mov_b64 exec, -1\ns_branch .E28\n.HD11:\nv_cmp_ne_u32 vcc, s15, v27\ns_nop 0\ns_cbranch_vccnz .S91\n.S92:\n"
"buffer_store_dword v25, off, s[16:19], s54 offset:4 sc0 sc1\ns_mov_b32 s90, 2\ns_swappc_b64 s[8:9], s[80:81]\ns_branch .HJ90\n.HD10:\n"
"buffer_store_dword v25, off, s[16:19], s54 offset:4 sc0 sc1\ns_mov_b32 s90, 0\ns_swappc_b64 s[8:9], s[80:81]\n.HJ90:\nv_and_b32 v27, 0x1f0, v1\n"
"buffer_load_dword v27, v27, s[16:19], s58 offen offset:4 sc0 sc1\ns_mov_b32 s90, 0\ns_swappc_b64 s[8:9], s[80:81]\nv_cmp_ne_u32 vcc, s15, v27\ns_nop 0\ns_cbranch_vccnz .S94\n"
".S95:\ns_mov_b32 s90, s91\ns_swappc_b64 s[8:9], s[80:81]\ns_mov_b32 s6, 0xc048400\nbuffer_load_dword v136, v1, s[16:19], s6 offen offset:4 sc0 sc1\n"
"buffer_load_dword v137, v1, s[16:19], s6 offen offset:1028 sc0 sc1\nbuffer_load_dword v138, v1, s[16:19], s6 offen offset:2052 sc0 sc1\n"
"buffer_load_dword v139, v1, s[16:19], s6 offen offset:3076 sc0 sc1\ns_mov_b32 s90, 0\ns_swappc_b64 s[8:9], s[80:81]\ns_mov_b32 s6, 0xc048400\nv_xor_b32 v140, s15, v136\n"
"v_xor_b32 v141, s15, v137\nv_xor_b32 v142, s15, v138\nv_xor_b32 v143, s15, v139\nv_or3_b32 v140, v140, v141, v142\nv_or_b32 v140, v140, v143\nv_cmp_ne_u32 vcc, 0, v140\ns_nop 0\n"
"s_cbranch_vccnz .S97\n.S98:\ns_lshr_b32 s4, s13, 1\ns_lshl2_add_u32 s4, s12, s4\ns_lshr_b32 s5, s71, 16\ns_cmp_lt_u32 s4, 28\ns_cselect_b32 s5, s71, s5\ns_cselect_b32 s3, 0, 28\n"
"s_and_b32 s5, s5, 0x7ff\ns_sub_u32 s3, s4, s3\ns_mul_i32 s2, s4, 7168\ns_add_u32 s2, s2, 0xc04c800\ns_add_u32 s6, s2, 4096\n"
"buffer_load_dwordx4 v[136:139], v1, s[16:19], s2 offen offset:0 sc0 sc1\nbuffer_load_dwordx4 v[140:143], v1, s[16:19], s2 offen offset:1024 sc0 sc1\n"
"buffer_load_dwordx4 v[144:147], v1, s[16:19], s2 offen offset:2048 sc0 sc1\nbuffer_load_dwordx4 v[148:151], v1, s[16:19], s2 offen offset:3072 sc0 sc1\n"
"buffer_load_dwordx4 v[152:155], v1, s[16:19], s6 offen offset:0 sc0 sc1\nbuffer_load_dwordx4 v[156:159], v1, s[16:19], s6 offen offset:1024 sc0 sc1\n"
"buffer_load_dwordx4 v[160:163], v1, s[16:19], s6 offen offset:2048 sc0 sc1\nv_lshrrev_b32 v167, 1, v1\nv_and_b32 v167, 24, v167\ns_mul_i32 s2, s5, 896\ns_lshl_b32 s3, s3, 5\n"
"s_add_u32 s2, s2, s3\ns_lshl2_add_u32 s5, s5, 0xc042000\nbuffer_load_dwordx2 v[164:165], v167, s[28:31], s2 offen\nbuffer_load_dword v166, off, s[16:19], s5 sc0 sc1\n"
"s_mov_b32 s90, 0\ns_swappc_b64 s[8:9], s[80:81]\nAD 136,136,140\nAD 137,137,141\nAD 138,138,142\nAD 139,139,143\nAD 144,144,148\nAD 145,145,149\nAD 146,146,150\nAD 147,147,151\n"
"AD 152,152,156\nAD 153,153,157\nAD 154,154,158\nAD 155,155,159\nAD 136,136,144\nAD 137,137,145\nAD 138,138,146\nAD 139,139,147\nAD 152,152,160\nAD 153,153,161\nAD 154,154,162\n"
"AD 155,155,163\nAD 136,136,152\nAD 137,137,153\nAD 138,138,154\nAD 139,139,155\nv_mov_b32 v140, v136\nv_mov_b32 v141, v137\nv_mov_b32 v142, v138\nv_mov_b32 v143, v139\n"
"v_permlane32_swap_b32 v136, v140\nv_permlane32_swap_b32 v137, v141\nv_permlane32_swap_b32 v138, v142\nv_permlane32_swap_b32 v139, v143\nAD 136,136,140\nAD 137,137,141\n"
"AD 138,138,142\nAD 139,139,143\nv_mov_b32 v140, v136\nv_mov_b32 v141, v137\nv_mov_b32 v142, v138\nv_mov_b32 v143, v139\nv_permlane16_swap_b32 v136, v140\n"
"v_permlane16_swap_b32 v137, v141\nv_permlane16_swap_b32 v138, v142\nv_permlane16_swap_b32 v139, v143\nAD 136,136,140\nAD 137,137,141\nAD 138,138,142\nAD 139,139,143\n"
"v_add_f32_dpp v136, v136, v136 row_ror:8 row_mask:0xf bank_mask:0xf\nv_add_f32_dpp v137, v137, v137 row_ror:8 row_mask:0xf bank_mask:0xf\n"
"v_add_f32_dpp v138, v138, v138 row_ror:8 row_mask:0xf bank_mask:0xf\nv_add_f32_dpp v139, v139, v139 row_ror:8 row_mask:0xf bank_mask:0xf\n"
"v_add_f32_dpp v136, v136, v136 row_ror:4 row_mask:0xf bank_mask:0xf\nv_add_f32_dpp v137, v137, v137 row_ror:4 row_mask:0xf bank_mask:0xf\n"
"v_add_f32_dpp v138, v138, v138 row_ror:4 row_mask:0xf bank_mask:0xf\nv_add_f32_dpp v139, v139, v139 row_ror:4 row_mask:0xf bank_mask:0xf\nv_mul_f32 v166, 0xbfb8aa3b, v166\n"
"EX 166,166\nCV 136,136,137\nv_add_f32 v166, 1.0, v166\nv_rcp_f32 v166, v166\nCV 138,138,139\nCV 166,166,166\nv_lshlrev_b32 v166, 16, v166\nv_and_b32 v137, 0xffff0000, v136\n"
"v_lshlrev_b32 v136, 16, v136\nv_and_b32 v139, 0xffff0000, v138\nv_lshlrev_b32 v138, 16, v138\nMU 136,136,166\nMU 137,137,166\nMU 138,138,166\nMU 139,139,166\nCV 136,136,137\n"
"CV 138,138,139\nv_and_b32 v137, 0xffff0000, v136\nv_lshlrev_b32 v136, 16, v136\nv_and_b32 v139, 0xffff0000, v138\nv_lshlrev_b32 v138, 16, v138\nv_lshlrev_b32 v140, 16, v164\n"
"v_and_b32 v141, 0xffff0000, v164\nv_lshlrev_b32 v142, 16, v165\nv_and_b32 v143, 0xffff0000, v165\nAD 136,136,140\nAD 137,137,141\nAD 138,138,142\nAD 139,139,143\nCV 136,136,137\n"
"CV 137,138,139\nv_mov_b32 v138, s15\nv_mov_b32 v139, s71\ns_lshr_b32 s4, s13, 1\ns_lshl2_add_u32 s4, s12, s4\ns_lshr_b32 s5, s71, 16\ns_cmp_lt_u32 s4, 28\n"
"s_cselect_b32 s5, s71, s5\ns_cselect_b32 s3, 0, 28\ns_and_b32 s5, s5, 0x7ff\ns_sub_u32 s3, s4, s3\nv_mov_b32 v140, 0x40000000\nv_cmp_gt_u32 vcc, 64, v1\ns_lshl_b32 s4, s4, 6\n"
"s_add_u32 s4, s4, 0xc04b800\nv_cndmask_b32 v140, v140, v1, vcc\nSv 136,140,16,4,0\ns_mov_b32 s90, 100\ns_swappc_b64 s[8:9], s[80:81]\n.S91:\ns_mov_b32 s7, 262144\n.S93:\n"
"s_sleep 8\nv_and_b32 v27, 0xf0, v1\nbuffer_load_dword v27, v27, s[16:19], s61 offen offset:12 sc0 sc1\nWV 0\nv_cmp_ne_u32 vcc, s15, v27\ns_nop 0\ns_cbranch_vccz .S92\n"
"s_sub_u32 s7, s7, 1\ns_cbranch_scc0 .S93\ns_branch .S92\n.S94:\ns_mov_b32 s7, 262144\n.S96:\ns_sleep 8\nv_and_b32 v27, 0x1f0, v1\n"
"buffer_load_dword v27, v27, s[16:19], s58 offen offset:4 sc0 sc1\nWV 0\nv_cmp_ne_u32 vcc, s15, v27\ns_nop 0\ns_cbranch_vccz .S95\ns_sub_u32 s7, s7, 1\ns_cbranch_scc0 .S96\n"
"s_branch .S95\n.S97:\ns_mov_b32 s7, 262144\n.S99:\ns_sleep 8\nbuffer_load_dword v96, v1, s[16:19], s6 offen offset:4 sc0 sc1\n"
"buffer_load_dword v97, v1, s[16:19], s6 offen offset:1028 sc0 sc1\nbuffer_load_dword v98, v1, s[16:19], s6 offen offset:2052 sc0 sc1\n"
"buffer_load_dword v99, v1, s[16:19], s6 offen offset:3076 sc0 sc1\nWV 0\nv_xor_b32 v100, s15, v96\nv_xor_b32 v101, s15, v97\nv_xor_b32 v102, s15, v98\nv_xor_b32 v103, s15, v99\n"
"v_or3_b32 v100, v100, v101, v102\nv_or_b32 v100, v100, v103\nv_cmp_ne_u32 vcc, 0, v100\ns_nop 0\ns_cbranch_vccz .S98\ns_sub_u32 s7, s7, 1\ns_cbranch_scc0 .S99\ns_branch .S98\n"
".X089:\nCvav 32,128,192,4\nRa 176,26,8192\nAD 84,84,81\nMvav 32,132,200\nEX 82,58\nMvav 32,136,208\nRa 180,26,9216\nCV 74,80,81\nMvav 32,140,216\nEX 83,59\nCvav 36,140,220,8\n"
"Ra 184,26,10240\nAD 84,84,82\nMvav 36,136,212\nD1 1,16,46,3072\nMvav 36,132,204\nRa 188,26,11264\nWL 8\nMvav 36,128,196\ns_add_u32 m0, s89, 32768\ns_add_u32 s46, s86, s88\n"
"s_mov_b32 s94, s96\ns_add_u32 s95, s96, 65536\nv_and_b32 v108, 0xf0, v1\nMvav 36,144,228\nRa 128,2,16384\nAD 84,84,83\nMvav 36,148,236\nEX 80,60\nMvav 36,152,244\nRa 132,2,17408\n"
"CV 75,82,83\nSv 112,1,16,49,0\nMvav 36,156,252\nEX 81,61\nMvav 32,156,248\nRa 136,2,18432\nAD 85,85,80\nMvav 32,152,240\nD1 1,16,46,0\nMvav 32,148,232\nRa 140,2,19456\nWL 8\n"
"Mvav 32,144,224\nbuffer_load_dwordx4 v[100:103], v108, s[16:19], s65 offen offset:0 sc0 sc1\nbuffer_load_dwordx4 v[104:107], v108, s[16:19], s65 offen offset:256 sc0 sc1\n"
"Cvav 40,160,192,12\nRa 144,2,20480\nAD 85,85,81\nMvav 40,164,200\nEX 82,62\nMvav 40,168,208\nRa 148,2,21504\nCV 78,80,81\nMvav 40,172,216\nEX 83,63\nCvav 44,172,220,16\n"
"Ra 152,2,22528\nAD 85,85,82\nMvav 44,168,212\nD1 1,16,46,1024\nMvav 44,164,204\nRa 156,2,23552\nWL 8\nMvav 44,160,196\ns_add_u32 s88, s88, 32768\nMvav 44,176,228\nRa 160,2,24576\n"
"AD 85,85,83\nMvav 44,180,236\nEX 80,32\nMvav 44,184,244\nRa 164,2,25600\nCV 79,82,83\nMvav 44,188,252\nEX 81,33\nMvav 40,188,248\nRa 168,2,26624\nv_max_f32 v90, v84, v85\n"
"Mvav 40,184,240\nD1 1,16,46,2048\nv_cmp_lt_f32 vcc, s63, v90\nMvav 40,180,232\nRa 172,2,27648\nWL 8\nMvav 40,176,224\ns_cbranch_vccnz .T100\n.T101:\nAD 86,86,84\nMaav 0,128,72\n"
"Ra 176,2,28672\nAD 84,80,81\nMaav 4,128,76\nEX 82,34\nMaav 12,132,76\nRa 180,2,29696\nCV 64,80,81\nMaav 8,132,72\nEX 83,35\nMaav 16,136,72\nRa 184,2,30720\nAD 84,84,82\n"
"Maav 20,136,76\nD1 1,16,46,3072\ns_add_u32 m0, s89, 49152\nMaav 28,140,76\nRa 188,2,31744\nMaav 24,140,72\ns_waitcnt vmcnt(16) lgkmcnt(8)\ns_barrier\nMaav 32,144,72\n"
"Ra 128,24,32768\nAD 84,84,83\nMaav 36,144,76\nEX 80,36\nMaav 44,148,76\nRa 132,24,33792\nCV 65,82,83\nMaav 40,148,72\nEX 81,37\nMaav 48,152,72\nRa 136,24,34816\nAD 87,87,85\n"
"Maav 52,152,76\ns_add_u32 s46, s87, s88\nD1 1,16,46,0\nMaav 60,156,76\nRa 140,24,35840\nWL 8\nMaav 56,156,72\ns_mov_b32 s86, s94\nMaav 64,160,72\nRa 144,26,32768\nAD 85,80,81\n"
"Maav 68,160,76\nEX 82,38\nMaav 76,164,76\nRa 148,26,33792\nCV 68,80,81\nMaav 72,164,72\nEX 83,39\nMaav 80,168,72\nRa 152,26,34816\nAD 85,85,82\nMaav 84,168,76\nD1 1,16,46,1024\n"
"Maav 92,172,76\nRa 156,26,35840\nWL 8\nMaav 88,172,72\nMaav 96,176,72\nRa 160,24,40960\nAD 85,85,83\nMaav 100,176,76\nEX 80,40\nMaav 108,180,76\nRa 164,24,41984\nCV 69,82,83\n"
"Maav 104,180,72\nEX 81,41\nMaav 112,184,72\nRa 168,24,43008\nAD 84,84,80\nMaav 116,184,76\nD1 1,16,46,2048\nMaav 124,188,76\nRa 172,24,44032\nWL 8\nMaav 120,188,72\n"
"s_xor_b32 s89, s89, 0x10000\nCvav 48,128,192,4\nRa 176,26,40960\nAD 84,84,81\nMvav 48,132,200\nEX 82,42\nMvav 48,136,208\nRa 180,26,41984\nCV 66,80,81\nMvav 48,140,216\nEX 83,43\n"
"Cvav 52,140,220,8\nRa 184,26,43008\nAD 84,84,82\nMvav 52,136,212\nD1 1,16,46,3072\nMvav 52,132,204\nRa 188,26,44032\nWL 8\nMvav 52,128,196\ns_add_u32 m0, s89, 0\n"
"s_add_u32 s46, s86, s88\nMvav 52,144,228\nRa 128,2,49152\nAD 84,84,83\nMvav 52,148,236\nEX 80,44\nMvav 52,152,244\nRa 132,2,50176\nCV 67,82,83\nSv 112,1,16,49,1024\n"
"Mvav 52,156,252\nEX 81,45\nMvav 48,156,248\nRa 136,2,51200\nAD 85,85,80\nMvav 48,152,240\nD1 1,16,46,0\nv_xor_b32 v24, 0x10000, v24\nMvav 48,148,232\nRa 140,2,52224\nWL 8\n"
"Mvav 48,144,224\nWV 9\nv_xor_b32 v109, s15, v102\nv_xor_b32 v110, s15, v106\nv_or_b32 v109, v109, v110\nv_cmp_ne_u32 vcc, 0, v109\ns_nop 3\ns_cmp_lg_u64 vcc, 0\n"
"s_cselect_b32 s67, 1, 0\ns_cselect_b32 s6, 0x40000000, s66\nCvav 56,160,192,12\nRa 144,2,53248\nAD 85,85,81\ns_add_u32 s5, s6, 0\nMvav 56,164,200\nEX 82,46\nMvav 56,168,208\n"
"Ra 148,2,54272\nCV 70,80,81\nbuffer_load_dwordx4 a[192:195], v1, s[16:19], s5 offen offset:0 sc1\nMvav 56,172,216\nEX 83,47\nCvav 60,172,220,16\nRa 152,2,55296\nAD 85,85,82\n"
"Mvav 60,168,212\nD1 1,16,46,1024\nv_xor_b32 v26, 0x10000, v26\nMvav 60,164,204\nRa 156,2,56320\nWL 8\nMvav 60,160,196\ns_add_u32 s88, s88, 32768\nMvav 60,176,228\nRa 160,2,57344\n"
"AD 85,85,83\nMvav 60,180,236\nEX 80,48\nMvav 60,184,244\nRa 164,2,58368\nCV 71,82,83\nbuffer_load_dwordx4 a[196:199], v1, s[16:19], s5 offen offset:1024 sc1\nMvav 60,188,252\n"
"EX 81,49\nMvav 56,188,248\nRa 168,2,59392\nv_max_f32 v90, v84, v85\nMvav 56,184,240\nD1 1,16,46,2048\nv_cmp_lt_f32 vcc, s63, v90\nMvav 56,180,232\nRa 172,2,60416\nWL 8\n"
"Mvav 56,176,224\ns_cbranch_vccnz .T102\n.T103:\nAD 86,86,84\nMaav 0,128,64\nRa 176,2,61440\nAD 84,80,81\nMaav 4,128,68\nEX 82,50\nMaav 12,132,68\nRa 180,2,62464\nCV 72,80,81\n"
"buffer_load_dwordx4 a[200:203], v1, s[16:19], s5 offen offset:2048 sc1\nMaav 8,132,64\nEX 83,51\nMaav 16,136,64\nRa 184,2,63488\nAD 84,84,82\nMaav 20,136,68\nD1 1,16,46,3072\n"
"s_add_u32 m0, s89, 16384\nMaav 28,140,68\nRa 188,2,64512\nMaav 24,140,64\nWL 8\ns_barrier\nMaav 32,144,64\nRa 128,24,0\nAD 84,84,83\nMaav 36,144,68\nEX 80,52\nMaav 44,148,68\n"
"Ra 132,24,1024\nCV 73,82,83\nbuffer_load_dwordx4 a[204:207], v1, s[16:19], s5 offen offset:3072 sc1\nMaav 40,148,64\nEX 81,53\nMaav 48,152,64\nRa 136,24,2048\nAD 87,87,85\n"
"Maav 52,152,68\ns_add_u32 s46, s87, s88\nD1 1,16,46,0\nMaav 60,156,68\nRa 140,24,3072\nWL 8\nMaav 56,156,64\nMaav 64,160,64\nRa 144,26,0\nAD 85,80,81\ns_add_u32 s5, s6, 4096\n"
"Maav 68,160,68\nEX 82,54\nMaav 76,164,68\nRa 148,26,1024\nCV 76,80,81\nbuffer_load_dwordx4 a[208:211], v1, s[16:19], s5 offen offset:0 sc1\nMaav 72,164,64\nEX 83,55\n"
"Maav 80,168,64\nRa 152,26,2048\nAD 85,85,82\nMaav 84,168,68\nD1 1,16,46,1024\nv_xor_b32 v2, 0x10000, v2\nMaav 92,172,68\nRa 156,26,3072\nWL 8\nMaav 88,172,64\n"
"s_cmp_eq_u32 s99, s92\ns_cbranch_scc1 .E104\n.E105:\ns_nop 2\nMaav 96,176,64\nRa 160,24,8192\nAD 85,85,83\nMaav 100,176,68\nEX 80,56\nMaav 108,180,68\nRa 164,24,9216\nCV 77,82,83\n"
"buffer_load_dwordx4 a[212:215], v1, s[16:19], s5 offen offset:1024 sc1\nMaav 104,180,64\nEX 81,57\nMaav 112,184,64\nRa 168,24,10240\nAD 84,84,80\nMaav 116,184,68\n"
"s_add_u32 s49, s49, 131072\nD1 1,16,46,2048\nMaav 124,188,68\nRa 172,24,11264\nWL 8\nMaav 120,188,64\ns_sub_u32 s99, s99, 1\nCvav 32,128,192,4\nRa 176,26,8192\nAD 84,84,81\n"
"Mvav 32,132,200\nEX 82,58\nMvav 32,136,208\nRa 180,26,9216\nCV 74,80,81\nbuffer_load_dwordx4 a[216:219], v1, s[16:19], s5 offen offset:2048 sc1\nMvav 32,140,216\nEX 83,59\n"
"Cvav 36,140,220,8\nRa 184,26,10240\nAD 84,84,82\nMvav 36,136,212\nD1 1,16,46,3072\nMvav 36,132,204\nRa 188,26,11264\nWL 8\nMvav 36,128,196\ns_add_u32 m0, s89, 32768\n"
"s_add_u32 s46, s86, s88\nMvav 36,144,228\nRa 128,2,16384\nAD 84,84,83\nMvav 36,148,236\nEX 80,60\nMvav 36,152,244\nRa 132,2,17408\nCV 75,82,83\nSv 112,1,16,49,0\n"
"buffer_load_dwordx4 a[220:223], v1, s[16:19], s5 offen offset:3072 sc1\nMvav 36,156,252\nEX 81,61\nMvav 32,156,248\nRa 136,2,18432\nAD 85,85,80\nMvav 32,152,240\nD1 1,16,46,0\n"
"Mvav 32,148,232\nRa 140,2,19456\nWL 8\nMvav 32,144,224\ns_mov_b32 s87, s95\nCvav 40,160,192,12\nRa 144,2,20480\nAD 85,85,81\ns_add_u32 s5, s6, 16384\nMvav 40,164,200\nEX 82,62\n"
"Mvav 40,168,208\nRa 148,2,21504\nCV 78,80,81\nbuffer_load_dwordx4 a[224:227], v1, s[16:19], s5 offen offset:0 sc1\nMvav 40,172,216\nEX 83,63\nCvav 44,172,220,16\nRa 152,2,22528\n"
"AD 85,85,82\nMvav 44,168,212\nD1 1,16,46,1024\nMvav 44,164,204\nRa 156,2,23552\nWL 8\nMvav 44,160,196\ns_add_u32 s88, s88, 32768\nMvav 44,176,228\nRa 160,2,24576\nAD 85,85,83\n"
"Mvav 44,180,236\nEX 80,32\nMvav 44,184,244\nRa 164,2,25600\nCV 79,82,83\nbuffer_load_dwordx4 a[228:231], v1, s[16:19], s5 offen offset:1024 sc1\nMvav 44,188,252\nEX 81,33\n"
"Mvav 40,188,248\nRa 168,2,26624\nv_max_f32 v90, v84, v85\nMvav 40,184,240\nD1 1,16,46,2048\nv_cmp_lt_f32 vcc, s63, v90\nMvav 40,180,232\nRa 172,2,27648\nWL 8\nMvav 40,176,224\n"
"s_cbranch_vccnz .T106\n.T107:\nAD 86,86,84\nMaav 0,128,72\nRa 176,2,28672\nAD 84,80,81\nMaav 4,128,76\nEX 82,34\nMaav 12,132,76\nRa 180,2,29696\nCV 64,80,81\n"
"buffer_load_dwordx4 a[232:235], v1, s[16:19], s5 offen offset:2048 sc1\nMaav 8,132,72\nEX 83,35\nMaav 16,136,72\nRa 184,2,30720\nAD 84,84,82\nMaav 20,136,76\nD1 1,16,46,3072\n"
"s_add_u32 m0, s89, 49152\nMaav 28,140,76\nRa 188,2,31744\nMaav 24,140,72\ns_waitcnt vmcnt(25) lgkmcnt(8)\ns_barrier\nMaav 32,144,72\nRa 128,24,32768\nAD 84,84,83\nMaav 36,144,76\n"
"EX 80,36\nMaav 44,148,76\nRa 132,24,33792\nCV 65,82,83\nbuffer_load_dwordx4 a[236:239], v1, s[16:19], s5 offen offset:3072 sc1\nMaav 40,148,72\nEX 81,37\nMaav 48,152,72\n"
"Ra 136,24,34816\nAD 87,87,85\nMaav 52,152,76\ns_add_u32 s46, s87, s88\nD1 1,16,46,0\nMaav 60,156,76\nRa 140,24,35840\nWL 8\nMaav 56,156,72\nMaav 64,160,72\nRa 144,26,32768\n"
"AD 85,80,81\ns_add_u32 s5, s6, 20480\nMaav 68,160,76\nEX 82,38\nMaav 76,164,76\nRa 148,26,33792\nCV 68,80,81\nbuffer_load_dwordx4 a[240:243], v1, s[16:19], s5 offen offset:0 sc1\n"
"Maav 72,164,72\nEX 83,39\nMaav 80,168,72\nRa 152,26,34816\nAD 85,85,82\nMaav 84,168,76\nD1 1,16,46,1024\nMaav 92,172,76\nRa 156,26,35840\nWL 8\nMaav 88,172,72\n"
"s_cmp_eq_u32 s99, s93\ns_cbranch_scc1 .E108\n.E109:\ns_nop 2\nMaav 96,176,72\nRa 160,24,40960\nAD 85,85,83\nMaav 100,176,76\nEX 80,40\nMaav 108,180,76\nRa 164,24,41984\n"
"CV 69,82,83\nbuffer_load_dwordx4 a[244:247], v1, s[16:19], s5 offen offset:1024 sc1\nMaav 104,180,72\nEX 81,41\nMaav 112,184,72\nRa 168,24,43008\nAD 84,84,80\nMaav 116,184,76\n"
"D1 1,16,46,2048\nMaav 124,188,76\nRa 172,24,44032\nWL 8\nMaav 120,188,72\ns_xor_b32 s89, s89, 0x10000\nCvav 48,128,192,4\nRa 176,26,40960\nAD 84,84,81\nMvav 48,132,200\nEX 82,42\n"
"Mvav 48,136,208\nRa 180,26,41984\nCV 66,80,81\nbuffer_load_dwordx4 a[248:251], v1, s[16:19], s5 offen offset:2048 sc1\nMvav 48,140,216\nEX 83,43\nCvav 52,140,220,8\n"
"Ra 184,26,43008\nAD 84,84,82\nMvav 52,136,212\nD1 1,16,46,3072\nMvav 52,132,204\nRa 188,26,44032\nWL 8\nMvav 52,128,196\ns_add_u32 m0, s89, 0\ns_add_u32 s46, s86, s88\n"
"Mvav 52,144,228\nRa 128,2,49152\nAD 84,84,83\nMvav 52,148,236\nEX 80,44\nMvav 52,152,244\nRa 132,2,50176\nCV 67,82,83\nSv 112,1,16,49,1024\n"
"buffer_load_dwordx4 a[252:255], v1, s[16:19], s5 offen offset:3072 sc1\nMvav 52,156,252\nEX 81,45\nMvav 48,156,248\nRa 136,2,51200\nAD 85,85,80\nMvav 48,152,240\nD1 1,16,46,0\n"
"v_xor_b32 v24, 0x10000, v24\nMvav 48,148,232\nRa 140,2,52224\nWL 8\nMvav 48,144,224\nCvav 56,160,192,12\nRa 144,2,53248\nAD 85,85,81\nMvav 56,164,200\nEX 82,46\nMvav 56,168,208\n"
"Ra 148,2,54272\nCV 70,80,81\nMvav 56,172,216\nEX 83,47\nCvav 60,172,220,16\nRa 152,2,55296\nAD 85,85,82\nMvav 60,168,212\nD1 1,16,46,1024\nv_xor_b32 v26, 0x10000, v26\n"
"Mvav 60,164,204\nRa 156,2,56320\nWL 8\nMvav 60,160,196\ns_add_u32 s88, s88, 32768\nMvav 60,176,228\nRa 160,2,57344\nAD 85,85,83\nMvav 60,180,236\nEX 80,48\nMvav 60,184,244\n"
"Ra 164,2,58368\nCV 71,82,83\nMvav 60,188,252\nEX 81,49\nMvav 56,188,248\nRa 168,2,59392\nv_max_f32 v90, v84, v85\nMvav 56,184,240\nD1 1,16,46,2048\nv_cmp_lt_f32 vcc, s63, v90\n"
"Mvav 56,180,232\nRa 172,2,60416\nWL 8\nMvav 56,176,224\ns_cbranch_vccnz .T110\n.T111:\nAD 86,86,84\nMaav 0,128,64\nRa 176,2,61440\nAD 84,80,81\nMaav 4,128,68\nEX 82,50\n"
"Maav 12,132,68\nRa 180,2,62464\nCV 72,80,81\nMaav 8,132,64\nEX 83,51\nMaav 16,136,64\nRa 184,2,63488\nAD 84,84,82\nMaav 20,136,68\nD1 1,16,46,3072\ns_add_u32 m0, s89, 16384\n"
"Maav 28,140,68\nRa 188,2,64512\nMaav 24,140,64\ns_waitcnt vmcnt(23) lgkmcnt(8)\ns_barrier\nMaav 32,144,64\nRa 128,24,0\nAD 84,84,83\nMaav 36,144,68\nEX 80,52\nMaav 44,148,68\n"
"Ra 132,24,1024\nCV 73,82,83\nMaav 40,148,64\nEX 81,53\nMaav 48,152,64\nRa 136,24,2048\nAD 87,87,85\nMaav 52,152,68\ns_add_u32 s46, s87, s88\nD1 1,16,46,0\nMaav 60,156,68\n"
"Ra 140,24,3072\nWL 8\nMaav 56,156,64\ns_cmp_lg_u32 s67, 0\ns_cbranch_scc1 .E113\n.E114:\nMaav 64,160,64\nRa 144,26,0\nAD 85,80,81\nMaav 68,160,68\nEX 82,54\nMaav 76,164,68\n"
"Ra 148,26,1024\nCV 76,80,81\nMaav 72,164,64\nEX 83,55\nMaav 80,168,64\nRa 152,26,2048\nAD 85,85,82\nMaav 84,168,68\nD1 1,16,46,1024\nv_xor_b32 v2, 0x10000, v2\nMaav 92,172,68\n"
"Ra 156,26,3072\nWL 8\nMaav 88,172,64\nMaav 96,176,64\nRa 160,24,8192\nAD 85,85,83\nMaav 100,176,68\nEX 80,56\nMaav 108,180,68\nRa 164,24,9216\nCV 77,82,83\nMaav 104,180,64\n"
"EX 81,57\nMaav 112,184,64\nRa 168,24,10240\nAD 84,84,80\nMaav 116,184,68\ns_add_u32 s49, s49, 131072\nD1 1,16,46,2048\nMaav 124,188,68\nRa 172,24,11264\nMaav 120,188,64\n"
"s_cmp_lg_u32 s67, 0\ns_cbranch_scc1 .D115\n.D116:\nWV 7\nv_cmp_lt_f32 vcc, 0, v101\nSU 20,100,88\nv_mul_f32 v101, 0x3e800000, v101\nv_cndmask_b32 v20, 0, v20, vcc\n"
"v_cmp_lt_f32 vcc, 0, v105\nSU 21,104,89\nv_mul_f32 v105, 0x3e800000, v105\nv_cndmask_b32 v21, 0, v21, vcc\nv_or_b32 v91, v20, v21\nv_cmp_ne_u32 vcc, 0, v91\ns_nop 0\n"
"s_cbranch_vccnz .D117\nAD 86,86,101\nAD 87,87,105\n.D118:\nMava 0,130,192\nAD 84,84,81\nMava 8,128,192\nEX 82,58\nMava 12,128,196\nCV 74,80,81\nMava 4,130,196\nEX 83,59\n"
"Mava 16,130,200\nAD 84,84,82\nMava 24,128,200\nD1 1,16,46,3072\nMava 28,128,204\nMava 20,130,204\ns_add_u32 m0, s89, 32768\ns_add_u32 s46, s86, s88\nMava 32,130,208\n"
"Ra 128,2,16384\nAD 84,84,83\nv_xor_b32 v3, 0x10000, v2\nMava 40,128,208\nEX 80,60\nv_add_u32 v0, 0x20000, v1\nMava 44,128,212\nRa 132,2,17408\nCV 75,82,83\nMava 36,130,212\n"
"EX 81,61\ns_sub_u32 s94, s49, 1966080\nMava 48,130,216\nRa 136,2,18432\nAD 85,85,80\nMava 56,128,216\nD1 1,16,46,0\ns_add_u32 s95, s94, 1048576\ns_xor_b32 s92, s94, 0x202000\n"
"Mava 60,128,220\nRa 140,2,19456\nMava 52,130,220\ns_add_u32 s93, s96, 983040\ns_xor_b32 s93, s93, 0x202000\ns_lshl_b32 s89, s11, 12\nMava 64,130,224\nRa 144,2,20480\nAD 85,85,81\n"
"s_mov_b32 s36, s68\ns_and_b32 s37, s69, 0xffff\ns_mov_b32 s38, 0x1c0000\nMava 72,128,224\nEX 82,62\ns_mov_b32 s39, 0x20000\ns_and_b32 s2, s74, 15\nMava 76,128,228\nRa 148,2,21504\n"
"CV 78,80,81\ns_mul_i32 s3, s73, 14\ns_add_u32 s2, s2, s3\ns_lshl2_add_u32 s2, s2, s11\nMava 68,130,228\nEX 83,63\ns_mul_i32 s3, s2, 4682\ns_lshr_b32 s3, s3, 16\nMava 80,130,232\n"
"Ra 152,2,22528\nAD 85,85,82\ns_mul_i32 s91, s3, 14\ns_sub_u32 s91, s2, s91\ns_lshl_b32 s90, s3, 5\nMava 88,128,232\nD1 1,16,46,1024\ns_add_u32 s90, s90, 1024\n"
"s_xor_b32 s3, s3, 28\ns_bfe_u32 s6, s3, 0x30002\ns_xor_b32 s60, s12, 1\nMava 92,128,236\nRa 156,2,23552\nMava 84,130,236\ns_lshl4_add_u32 s60, s60, s13\n"
"s_lshl4_add_u32 s60, s60, 0xc049400\ns_cmp_gt_u32 s91, 6\ns_cselect_b32 s2, 1179648, 0\ns_cselect_b32 s1, 16, 0\nMava 96,130,240\nRa 160,2,24576\nAD 85,85,83\n"
"s_add_u32 s6, s6, s1\ns_lshl4_add_u32 s6, s6, 0xc049400\ns_and_b32 s1, s3, 28\nMava 104,128,240\ns_add_u32 s3, s3, s1\ns_lshl_b32 s3, s3, 11\ns_add_u32 s2, s2, s3\n"
"s_lshl_b32 s3, s91, 17\nMava 108,128,244\nRa 164,2,25600\nCV 79,82,83\ns_add_u32 s2, s2, s3\ns_add_u32 s98, s2, 0xd00000\ns_lshl_b32 s2, s91, 7\nMava 100,130,244\n"
"s_add_u32 s0, s2, 0xc04b800\ns_mul_i32 s2, s90, 896\ns_lshl_b32 s3, s91, 6\ns_add_u32 s91, s2, s3\nMava 112,130,248\nRa 168,2,26624\nv_max_f32 v90, v84, v85\n"
"s_add_u32 s1, s91, 14336\nMava 120,128,248\nD1 1,16,46,2048\nv_cmp_lt_f32 vcc, s63, v90\nMava 124,128,252\nRa 172,2,27648\nWL 8\nMava 116,130,252\ns_cbranch_vccnz .T122\n.T123:\n"
"AD 86,86,84\nAD 87,87,85\nMaav 0,128,72\nRa 176,2,28672\nv_mov_b32 v84, v86\nv_mov_b32 v85, v87\nMaav 4,128,76\nv_permlane32_swap_b32 v86, v84\nv_permlane32_swap_b32 v87, v85\n"
"Maav 12,132,76\nRa 180,2,29696\nAD 86,86,84\nMaav 8,132,72\nAD 87,87,85\nv_mov_b32 v84, v86\nMaav 16,136,72\nRa 184,2,30720\nv_mov_b32 v85, v87\nMaav 20,136,76\nD1 1,16,46,3072\n"
"v_permlane16_swap_b32 v86, v84\nMaav 28,140,76\nRa 188,2,31744\nWL 8\nMaav 24,140,72\nv_permlane16_swap_b32 v87, v85\nAD 86,86,84\nAD 87,87,85\nMaav 32,144,72\nRa 128,2,0\n"
"Maav 36,144,76\nv_rcp_f32 v86, v86\nMaav 44,148,76\nRa 132,2,1024\nv_rcp_f32 v87, v87\nMaav 40,148,72\ns_add_u32 m0, s89, 131072\ns_add_u32 s46, s93, 0\nAR 82,0\nAR 83,1\n"
"MU 82,82,86\nMaav 48,152,72\nRa 136,2,2048\nMU 83,83,86\nCV 96,82,83\nAR 82,2\nAR 83,3\nMU 82,82,86\nMaav 52,152,76\nD1 1,16,46,0\nMU 83,83,86\nCV 97,82,83\nAR 82,4\nAR 83,5\n"
"MU 82,82,87\nMaav 60,156,76\nRa 140,2,3072\nWL 8\nMaav 56,156,72\nMU 83,83,87\nCV 100,82,83\nAR 82,6\nAR 83,7\nMU 82,82,87\nMU 83,83,87\nMaav 64,160,72\nRa 144,2,4096\n"
"CV 101,82,83\nAR 82,8\nAR 83,9\nMU 82,82,86\nMU 83,83,86\nMaav 68,160,76\nCV 98,82,83\nAR 82,10\nAR 83,11\nMU 82,82,86\nMU 83,83,86\nMaav 76,164,76\nRa 148,2,5120\nCV 99,82,83\n"
"s_add_u32 s5, s94, 0\nSv 96,1,16,5,0\nAR 82,12\nAR 83,13\nMaav 72,164,72\nMU 82,82,87\nMU 83,83,87\nCV 102,82,83\nAR 82,14\nAR 83,15\nMU 82,82,87\nMaav 80,168,72\nRa 152,2,6144\n"
"MU 83,83,87\nCV 103,82,83\nSv 100,1,16,5,1024\nAR 82,16\nAR 83,17\nMaav 84,168,76\nD1 1,16,46,1024\nMU 82,82,86\nMU 83,83,86\nCV 104,82,83\nAR 82,18\nAR 83,19\nMaav 92,172,76\n"
"Ra 156,2,7168\nWL 8\nMaav 88,172,72\nMU 82,82,86\nMU 83,83,86\nCV 105,82,83\nAR 82,20\nAR 83,21\nMU 82,82,87\nMaav 96,176,72\nRa 160,2,8192\nMU 83,83,87\nCV 108,82,83\nAR 82,22\n"
"AR 83,23\nMU 82,82,87\nMaav 100,176,76\nMU 83,83,87\nCV 109,82,83\nAR 82,24\nAR 83,25\nMU 82,82,86\nMU 83,83,86\nMaav 108,180,76\nRa 164,2,9216\nCV 106,82,83\nAR 82,26\nAR 83,27\n"
"MU 82,82,86\nMU 83,83,86\nMaav 104,180,72\nCV 107,82,83\ns_add_u32 s5, s94, 131072\nSv 104,1,16,5,0\nAR 82,28\nAR 83,29\nMaav 112,184,72\nRa 168,2,10240\nMU 82,82,87\nMU 83,83,87\n"
"CV 110,82,83\nAR 82,30\nAR 83,31\nMaav 116,184,76\nD1 1,16,46,2048\nMU 82,82,87\nMU 83,83,87\nCV 111,82,83\nSv 108,1,16,5,1024\nMaav 124,188,76\nRa 172,2,11264\nWL 8\n"
"Maav 120,188,72\nAR 82,32\nAR 83,33\nMU 82,82,86\nZvav 136,128,96\nRa 176,2,12288\nMU 83,83,86\nCV 112,82,83\nAR 82,34\nMvav 136,132,104\nAR 83,35\nMU 82,82,86\nMU 83,83,86\n"
"s_and_b32 s63, s72, 0x2000\nv_xor_b32 v25, s63, v25\nZvav 140,132,108\nRa 180,2,13312\nCV 113,82,83\nAR 82,36\nMvav 140,128,100\nAR 83,37\nMU 82,82,87\nMU 83,83,87\n"
"Zvav 148,136,100\nRa 184,2,14336\nCV 116,82,83\nAR 82,38\nMvav 148,140,108\nD1 1,16,46,3072\nAR 83,39\nMU 82,82,87\nZvav 144,140,104\nRa 188,2,15360\nMvav 144,136,96\nWL 8\n"
"s_barrier\nZvav 152,144,96\nRa 128,2,32768\nMU 83,83,87\nCV 117,82,83\nMvav 152,148,104\nAR 82,40\nAR 83,41\nMU 82,82,86\ns_add_u32 s54, s54, 4096\n"
"buffer_store_dword v25, off, s[16:19], s54 offset:0 sc0 sc1\nZvav 156,148,108\nRa 132,2,33792\nMU 83,83,86\nCV 114,82,83\nv_lshrrev_b32 v88, 8, v1\nMvav 156,144,100\n"
"s_add_u32 m0, s89, 114688\ns_add_u32 s46, s96, 1179648\nAR 82,42\nZvav 164,152,100\nRa 136,2,34816\nAR 83,43\nMU 82,82,86\nv_lshlrev_b32 v89, 5, v88\nMvav 164,156,108\n"
"D1 1,16,46,0\nMU 83,83,86\nCV 115,82,83\nZvav 160,156,104\nRa 140,2,35840\nWL 8\nMvav 160,152,96\ns_add_u32 s5, s94, 262144\nSv 112,1,16,5,0\nAR 82,44\nZvav 168,160,96\n"
"Ra 144,2,36864\nAR 83,45\nMU 82,82,87\nMU 83,83,87\nMvav 168,164,104\nCV 118,82,83\nAR 82,46\nAR 83,47\nZvav 172,164,108\nRa 148,2,37888\nMU 82,82,87\nMU 83,83,87\n"
"Mvav 172,160,100\nCV 119,82,83\nSv 116,1,16,5,1024\nAR 82,48\nbuffer_load_dwordx4 v[64:67], v89, s[16:19], s0 offen offset:0 sc0 sc1\nZvav 180,168,100\nRa 152,2,38912\nAR 83,49\n"
"MU 82,82,86\nMvav 180,172,108\nD1 1,16,46,1024\nMU 83,83,86\nCV 120,82,83\nZvav 176,172,104\nRa 156,2,39936\nWL 8\nMvav 176,168,96\nAR 82,50\nAR 83,51\nMU 82,82,86\n"
"buffer_load_dwordx4 v[68:71], v89, s[16:19], s0 offen offset:16 sc0 sc1\nZvav 184,176,96\nRa 160,2,40960\nMU 83,83,86\nCV 121,82,83\nMvav 184,180,104\nAR 82,52\nAR 83,53\n"
"MU 82,82,87\nZvav 188,180,108\nRa 164,2,41984\nMU 83,83,87\nCV 124,82,83\nMvav 188,176,100\nAR 82,54\nAR 83,55\nMU 82,82,87\nZvav 196,184,100\nRa 168,2,43008\nMU 83,83,87\n"
"CV 125,82,83\nbuffer_load_dwordx4 v[72:75], v89, s[16:19], s0 offen offset:1792 sc0 sc1\nMvav 196,188,108\nD1 1,16,46,2048\nAR 82,56\nAR 83,57\nZvav 192,188,104\nRa 172,2,44032\n"
"WL 8\nMvav 192,184,96\nMU 82,82,86\nMU 83,83,86\nCV 122,82,83\nZvav 200,128,96\nRa 176,2,45056\nAR 82,58\nAR 83,59\nMU 82,82,86\nMvav 200,132,104\nMU 83,83,86\nCV 123,82,83\n"
"s_add_u32 s5, s94, 393216\nZvav 204,132,108\nRa 180,2,46080\nSv 120,1,16,5,0\nAR 82,60\nMvav 204,128,100\nAR 83,61\nMU 82,82,87\nMU 83,83,87\n"
"buffer_load_dwordx4 v[76:79], v89, s[16:19], s0 offen offset:1808 sc0 sc1\nZvav 212,136,100\nRa 184,2,47104\nCV 126,82,83\nAR 82,62\nMvav 212,140,108\nD1 1,16,46,3072\nAR 83,63\n"
"MU 82,82,87\nZvav 208,140,104\nRa 188,2,48128\nWL 8\nMvav 208,136,96\nMU 83,83,87\nCV 127,82,83\nSv 124,1,16,5,1024\nv_bfe_u32 v90, v1, 4, 4\nZvav 216,144,96\nRa 128,2,49152\n"
"AR 82,64\nAR 83,65\nMU 82,82,86\nMvav 216,148,104\nMU 83,83,86\nCV 32,82,83\nAR 82,66\nZvav 220,148,108\nRa 132,2,50176\nAR 83,67\nMU 82,82,86\nv_mul_u32_u24 v91, 896, v90\n"
"Mvav 220,144,100\ns_add_u32 m0, s89, 147456\ns_add_u32 s46, s93, 32768\nMU 83,83,86\nZvav 228,152,100\nRa 136,2,51200\nCV 33,82,83\nAR 82,68\nv_lshl_add_u32 v91, v88, 4, v91\n"
"Mvav 228,156,108\nD1 1,16,46,0\nAR 83,69\nMU 82,82,87\nZvav 224,156,104\nRa 140,2,52224\nWL 8\nMvav 224,152,96\nMU 83,83,87\nCV 36,82,83\nAR 82,70\nZvav 232,160,96\n"
"Ra 144,2,53248\nAR 83,71\nMU 82,82,87\nMU 83,83,87\nMvav 232,164,104\nCV 37,82,83\nAR 82,72\nAR 83,73\nZvav 236,164,108\nRa 148,2,54272\nMU 82,82,86\nMU 83,83,86\n"
"Mvav 236,160,100\nCV 34,82,83\nAR 82,74\nAR 83,75\nbuffer_load_dwordx4 v[12:15], v91, s[28:31], s91 offen offset:0\nZvav 244,168,100\nRa 152,2,55296\nMU 82,82,86\nMU 83,83,86\n"
"Mvav 244,172,108\nD1 1,16,46,1024\nCV 35,82,83\ns_add_u32 s5, s94, 524288\nZvav 240,172,104\nRa 156,2,56320\nWL 8\nMvav 240,168,96\nSv 32,1,16,5,0\nAR 82,76\nAR 83,77\n"
"buffer_load_dwordx4 v[16:19], v91, s[28:31], s1 offen offset:0\nMvav 136,176,112\nRa 160,2,57344\nMU 82,82,87\nMU 83,83,87\nCV 38,82,83\nMvav 136,180,120\nAR 82,78\nAR 83,79\n"
"MU 82,82,87\nMvav 140,180,124\nRa 164,2,58368\nMU 83,83,87\nCV 39,82,83\nv_add_u32 v90, s90, v90\nMvav 140,176,116\nSv 36,1,16,5,1024\nAR 82,80\nAR 83,81\nMvav 148,184,116\n"
"Ra 168,2,59392\nMU 82,82,86\nMU 83,83,86\nv_lshlrev_b32 v88, 2, v90\nMvav 148,188,124\nD1 1,16,46,2048\nCV 40,82,83\nAR 82,82\nMvav 144,188,120\nRa 172,2,60416\nWL 8\n"
"Mvav 144,184,112\nAR 83,83\nMU 82,82,86\nMU 83,83,86\nMvav 152,128,112\nRa 176,2,61440\nCV 41,82,83\nAR 82,84\nAR 83,85\nMvav 152,132,120\nMU 82,82,87\nMU 83,83,87\nCV 44,82,83\n"
"Mvav 156,132,124\nRa 180,2,62464\nAR 82,86\nAR 83,87\nMvav 156,128,116\nMU 82,82,87\nMU 83,83,87\nCV 45,82,83\nMvav 164,136,116\nRa 184,2,63488\nAR 82,88\nAR 83,89\n"
"Mvav 164,140,124\nD1 1,16,46,3072\nMU 82,82,86\nMU 83,83,86\nMvav 160,140,120\nRa 188,2,64512\nMvav 160,136,112\ns_waitcnt vmcnt(33) lgkmcnt(8)\ns_barrier\nMvav 168,144,112\n"
"Ra 128,3,0\nCV 42,82,83\nAR 82,90\nAR 83,91\nMvav 168,148,120\nMU 82,82,86\nMU 83,83,86\nCV 43,82,83\nMvav 172,148,124\nRa 132,3,1024\ns_add_u32 s5, s94, 655360\nSv 40,1,16,5,0\n"
"Mvav 172,144,116\nAR 82,92\nAR 83,93\nMU 82,82,87\nMvav 180,152,116\nRa 136,3,2048\nMU 83,83,87\nCV 46,82,83\nMvav 180,156,124\nAR 82,94\nAR 83,95\nMU 82,82,87\nMvav 176,156,120\n"
"Ra 140,3,3072\nWL 8\nMvav 176,152,112\nMU 83,83,87\nCV 47,82,83\nSv 44,1,16,5,1024\nMvav 184,160,112\nRa 144,3,4096\nAR 82,96\nAR 83,97\nMvav 184,164,120\nMU 82,82,86\n"
"MU 83,83,86\nbuffer_load_dword v27, off, s[16:19], s60 sc0 sc1\nMvav 188,164,124\nRa 148,3,5120\nCV 48,82,83\nAR 82,98\nMvav 188,160,116\nAR 83,99\nMU 82,82,86\nMU 83,83,86\n"
"Mvav 196,168,116\nRa 152,3,6144\nCV 49,82,83\nAR 82,100\nMvav 196,172,124\nAR 83,101\nMU 82,82,87\nMU 83,83,87\nMvav 192,172,120\nRa 156,3,7168\nWL 8\nMvav 192,168,112\n"
"CV 52,82,83\nAR 82,102\nAR 83,103\nMvav 200,176,112\nRa 160,3,8192\nMU 82,82,87\nMU 83,83,87\nMvav 200,180,120\nCV 53,82,83\nAR 82,104\nAR 83,105\nMvav 204,180,124\nRa 164,3,9216\n"
"MU 82,82,86\nMU 83,83,86\nMvav 204,176,116\nCV 50,82,83\nAR 82,106\nAR 83,107\nMvav 212,184,116\nRa 168,3,10240\nMU 82,82,86\nMU 83,83,86\nMvav 212,188,124\nCV 51,82,83\n"
"s_add_u32 s5, s94, 786432\nSv 48,1,16,5,0\nMvav 208,188,120\nRa 172,3,11264\nWL 8\nMvav 208,184,112\nAR 82,108\nAR 83,109\nMU 82,82,87\nMvav 216,128,112\nRa 176,3,12288\n"
"MU 83,83,87\nCV 54,82,83\nMvav 216,132,120\nAR 82,110\nAR 83,111\nMvav 220,132,124\nRa 180,3,13312\nMU 82,82,87\nMU 83,83,87\nMvav 220,128,116\nCV 55,82,83\nSv 52,1,16,5,1024\n"
"AR 82,112\nMvav 228,136,116\nRa 184,3,14336\nAR 83,113\nMU 82,82,86\nMvav 228,140,124\nMU 83,83,86\nCV 56,82,83\nAR 82,114\nMvav 224,140,120\nRa 188,3,15360\nWL 8\n"
"Mvav 224,136,112\nAR 83,115\nMU 82,82,86\nMU 83,83,86\nMvav 232,144,112\nRa 128,3,16384\nCV 57,82,83\nAR 82,116\nMvav 232,148,120\nAR 83,117\nMU 82,82,87\nMU 83,83,87\n"
"Mvav 236,148,124\nRa 132,3,17408\nCV 60,82,83\nAR 82,118\nMvav 236,144,116\nAR 83,119\nMU 82,82,87\nMU 83,83,87\nMvav 244,152,116\nRa 136,3,18432\nCV 61,82,83\nAR 82,120\n"
"Mvav 244,156,124\nAR 83,121\nMU 82,82,86\nMU 83,83,86\nMvav 240,156,120\nRa 140,3,19456\nWL 8\nMvav 240,152,112\nCV 58,82,83\nAR 82,122\nAR 83,123\nMvav 136,160,32\n"
"Ra 144,3,20480\nMU 82,82,86\nMU 83,83,86\nMvav 136,164,40\nCV 59,82,83\ns_add_u32 s5, s94, 917504\nMvav 140,164,44\nRa 148,3,21504\nSv 56,1,16,5,0\nAR 82,124\nMvav 140,160,36\n"
"AR 83,125\nMU 82,82,87\nMU 83,83,87\nMvav 148,168,36\nRa 152,3,22528\nCV 62,82,83\nAR 82,126\nMvav 148,172,44\nAR 83,127\nMU 82,82,87\nMU 83,83,87\nMvav 144,172,40\n"
"Ra 156,3,23552\nWL 8\nMvav 144,168,32\nCV 63,82,83\nSv 60,1,16,5,1024\nMvav 152,176,32\nRa 160,3,24576\nWV 13\nMvav 152,180,40\nv_xor_b32 v92, s15, v66\nv_xor_b32 v93, s15, v70\n"
"Mvav 156,180,44\nRa 164,3,25600\nv_xor_b32 v94, s15, v74\nMvav 156,176,36\nv_or3_b32 v92, v92, v93, v94\nv_xor_b32 v93, s15, v78\nMvav 164,184,36\nRa 168,3,26624\n"
"v_or_b32 v92, v92, v93\nMvav 164,188,44\nv_cmp_ne_u32 vcc, 0, v92\ns_nop 0\ns_cbranch_vccnz .S131\n.S132:\nMvav 160,188,40\nRa 172,3,27648\nWL 8\nMvav 160,184,32\n"
"s_mov_b32 s2, 0xc040000\nbuffer_load_dword v80, v88, s[16:19], s2 offen sc0 sc1\nMvav 168,128,32\nRa 176,3,28672\nMvav 168,132,40\nMvav 172,132,44\nRa 180,3,29696\n"
"Mvav 172,128,36\nbuffer_load_dword v81, v88, s[16:19], s2 offen offset:64 sc0 sc1\nv_and_b32 v92, 0x7ff, v67\nMvav 180,136,36\nRa 184,3,30720\nv_lshrrev_b32 v93, 16, v67\n"
"Mvav 180,140,44\nv_add_u32 v94, 16, v90\nv_cmp_eq_u32 vcc, v92, v90\ns_nop 1\nv_cndmask_b32 v12, v12, v64, vcc\nv_cndmask_b32 v13, v13, v65, vcc\nv_cndmask_b32 v14, v14, v68, vcc\n"
"v_cndmask_b32 v15, v15, v69, vcc\nMvav 176,140,40\nRa 188,3,31744\nMvav 176,136,32\ns_waitcnt vmcnt(12) lgkmcnt(8)\ns_barrier\nMvav 184,144,32\nRa 128,3,32768\n"
"v_cmp_eq_u32 vcc, v93, v90\ns_nop 1\nv_cndmask_b32 v12, v12, v72, vcc\nv_cndmask_b32 v13, v13, v73, vcc\nv_cndmask_b32 v14, v14, v76, vcc\nv_cndmask_b32 v15, v15, v77, vcc\n"
"Mvav 184,148,40\nv_cmp_eq_u32 vcc, v92, v94\ns_nop 1\nv_cndmask_b32 v16, v16, v64, vcc\nv_cndmask_b32 v17, v17, v65, vcc\nv_cndmask_b32 v18, v18, v68, vcc\n"
"v_cndmask_b32 v19, v19, v69, vcc\nMvav 188,148,44\nRa 132,3,33792\nv_cmp_eq_u32 vcc, v93, v94\ns_nop 1\nv_cndmask_b32 v16, v16, v72, vcc\nv_cndmask_b32 v17, v17, v73, vcc\n"
"v_cndmask_b32 v18, v18, v76, vcc\nv_cndmask_b32 v19, v19, v77, vcc\nMvav 188,144,36\nMvav 196,152,36\nRa 136,3,34816\nMvav 196,156,44\nMvav 192,156,40\nRa 140,3,35840\nWL 8\n"
"Mvav 192,152,32\nMvav 200,160,32\nRa 144,3,36864\nWV 6\nv_xor_b32 v27, s15, v27\nv_and_b32 v24, 0xffffdfff, v27\nv_cmp_ne_u32 vcc, 0, v24\ns_nop 0\ns_cbranch_vccnz .S134\n.S135:\n"
"Mvav 200,164,40\ns_add_u32 s4, s92, 0\nMvav 204,164,44\nRa 148,3,37888\nMvav 204,160,36\ns_add_u32 m0, s89, 16384\ns_add_u32 s46, s93, 65536\n"
"buffer_load_dwordx4 a[192:195], v1, s[16:19], s4 offen offset:0 sc0 sc1\nMvav 212,168,36\nRa 152,3,38912\nMvav 212,172,44\nD1 1,16,46,0\nMvav 208,172,40\nRa 156,3,39936\nWL 8\n"
"Mvav 208,168,32\nbuffer_load_dwordx4 a[196:199], v1, s[16:19], s4 offen offset:1024 sc0 sc1\nMvav 216,176,32\nRa 160,3,40960\nMvav 216,180,40\ns_add_u32 s4, s92, 131072\n"
"Mvav 220,180,44\nRa 164,3,41984\nMvav 220,176,36\nbuffer_load_dwordx4 a[200:203], v1, s[16:19], s4 offen offset:0 sc0 sc1\nMvav 228,184,36\nRa 168,3,43008\nMvav 228,188,44\n"
"D1 1,16,46,1024\nMvav 224,188,40\nRa 172,3,44032\nWL 8\nMvav 224,184,32\nbuffer_load_dwordx4 a[204:207], v1, s[16:19], s4 offen offset:1024 sc0 sc1\nMvav 232,128,32\n"
"Ra 176,3,45056\nMvav 232,132,40\ns_add_u32 s4, s92, 262144\nMvav 236,132,44\nRa 180,3,46080\nMvav 236,128,36\n"
"buffer_load_dwordx4 a[208:211], v1, s[16:19], s4 offen offset:0 sc0 sc1\nMvav 244,136,36\nRa 184,3,47104\nMvav 244,140,44\nD1 1,16,46,2048\nMvav 240,140,40\nRa 188,3,48128\nWL 8\n"
"Mvav 240,136,32\nbuffer_load_dwordx4 a[212:215], v1, s[16:19], s4 offen offset:1024 sc0 sc1\nMvav 136,144,48\nRa 128,3,49152\nMvav 136,148,56\ns_add_u32 s4, s92, 393216\n"
"Mvav 140,148,60\nRa 132,3,50176\nMvav 140,144,52\nbuffer_load_dwordx4 a[216:219], v1, s[16:19], s4 offen offset:0 sc0 sc1\nMvav 148,152,52\nRa 136,3,51200\nMvav 148,156,60\n"
"D1 1,16,46,3072\nMvav 144,156,56\nRa 140,3,52224\nWL 8\nMvav 144,152,48\nbuffer_load_dwordx4 a[220:223], v1, s[16:19], s4 offen offset:1024 sc0 sc1\nMvav 152,160,48\n"
"Ra 144,3,53248\nMvav 152,164,56\nMvav 156,164,60\nRa 148,3,54272\nMvav 156,160,52\ns_add_u32 m0, s89, 0\ns_add_u32 s46, s93, 98304\nMvav 164,168,52\nRa 152,3,55296\n"
"Mvav 164,172,60\nD1 1,16,46,0\nMvav 160,172,56\nRa 156,3,56320\nWL 8\nMvav 160,168,48\nMvav 168,176,48\nRa 160,3,57344\nMvav 168,180,56\nMvav 172,180,60\nRa 164,3,58368\n"
"Mvav 172,176,52\nMvav 180,184,52\nRa 168,3,59392\nMvav 180,188,60\nD1 1,16,46,1024\nMvav 176,188,56\nRa 172,3,60416\nWL 8\nMvav 176,184,48\nMvav 184,128,48\nRa 176,3,61440\n"
"Mvav 184,132,56\nMvav 188,132,60\nRa 180,3,62464\nMvav 188,128,52\nMvav 196,136,52\nRa 184,3,63488\nMvav 196,140,60\nD1 1,16,46,2048\nMvav 192,140,56\nRa 188,3,64512\n"
"Mvav 192,136,48\nWL 8\ns_barrier\nMvav 200,144,48\nRa 128,0,0\nMvav 200,148,56\nMvav 204,148,60\nRa 132,0,1024\nMvav 204,144,52\nMvav 212,152,52\nRa 136,0,2048\nMvav 212,156,60\n"
"D1 1,16,46,3072\nMvav 208,156,56\nRa 140,0,3072\nWL 8\nMvav 208,152,48\nMvav 216,160,48\nRa 144,0,4096\nMvav 216,164,56\nMvav 220,164,60\nRa 148,0,5120\nMvav 220,160,52\n"
"s_add_u32 m0, s89, 32768\ns_add_u32 s46, s93, 131072\nMvav 228,168,52\nRa 152,0,6144\nMvav 228,172,60\nD1 1,16,46,0\nMvav 224,172,56\nRa 156,0,7168\nWL 8\nMvav 224,168,48\nWV 5\n"
"AR 22,195\nAR 23,199\nMvav 232,176,48\nRa 160,0,8192\nv_max_u32 v21, v22, v23\nMvav 232,180,56\nAR 22,203\nAR 23,207\nMvav 236,180,60\nRa 164,0,9216\n"
"v_max3_u32 v21, v21, v22, v23\nAR 22,211\nMvav 236,176,52\nAR 23,215\nv_max3_u32 v21, v21, v22, v23\nMvav 244,184,52\nRa 168,0,10240\nAR 22,219\nMvav 244,188,60\nD1 1,16,46,1024\n"
"AR 23,223\nv_max3_u32 v21, v21, v22, v23\nMvav 240,188,56\nRa 172,0,11264\nWL 8\nMvav 240,184,48\nv_cmp_eq_u32 vcc, -1, v21\ns_nop 0\ns_cbranch_vccnz .S125\n.S126:\n"
"Mvaa 136,128,192\nRa 176,0,12288\nMvaa 136,132,200\nv_sub_f32 v92, 1.0, v80\nv_sub_f32 v93, 1.0, v81\nMvaa 136,136,208\nRa 180,0,13312\nCV 92,92,93\nMvaa 136,140,216\n"
"v_and_b32 v93, 0xffff0000, v92\nv_lshlrev_b32 v92, 16, v92\nMvaa 140,140,220\nRa 184,0,14336\nv_lshlrev_b32 v64, 16, v12\nMvaa 140,136,212\nD1 1,16,46,2048\n"
"v_and_b32 v65, 0xffff0000, v12\nMvaa 140,132,204\nRa 188,0,15360\nWL 8\nMvaa 140,128,196\nMU 64,64,92\nMU 65,65,92\nMvaa 144,144,192\nRa 128,0,16384\nv_lshlrev_b32 v66, 16, v13\n"
"Mvaa 144,148,200\nv_and_b32 v67, 0xffff0000, v13\nMU 66,66,92\nMvaa 144,152,208\nRa 132,0,17408\nMU 67,67,92\nMvaa 144,156,216\nv_lshlrev_b32 v68, 16, v14\n"
"v_and_b32 v69, 0xffff0000, v14\nMvaa 148,156,220\nRa 136,0,18432\nMU 68,68,92\nMvaa 148,152,212\nD1 1,16,46,3072\nMU 69,69,92\nMvaa 148,148,204\nRa 140,0,19456\nWL 8\n"
"Mvaa 148,144,196\nv_lshlrev_b32 v70, 16, v15\nv_and_b32 v71, 0xffff0000, v15\nMvaa 152,160,192\nRa 144,0,20480\nMU 70,70,92\nMvaa 152,164,200\nMU 71,71,92\n"
"v_lshlrev_b32 v72, 16, v16\nMvaa 152,168,208\nRa 148,0,21504\nv_and_b32 v73, 0xffff0000, v16\nMvaa 152,172,216\ns_add_u32 m0, s89, 49152\ns_add_u32 s46, s93, 163840\n"
"Mvaa 156,172,220\nRa 152,0,22528\nMU 72,72,93\nMvaa 156,168,212\nD1 1,16,46,0\nMU 73,73,93\nMvaa 156,164,204\nRa 156,0,23552\nWL 8\nMvaa 156,160,196\nv_lshlrev_b32 v74, 16, v17\n"
"v_and_b32 v75, 0xffff0000, v17\nMvaa 160,176,192\nRa 160,0,24576\nMU 74,74,93\nMvaa 160,180,200\ns_add_u32 s4, s92, 524288\nMU 75,75,93\nMvaa 160,184,208\nRa 164,0,25600\n"
"v_lshlrev_b32 v76, 16, v18\nMvaa 160,188,216\nbuffer_load_dwordx4 a[224:227], v1, s[16:19], s4 offen offset:0 sc0 sc1\nv_and_b32 v77, 0xffff0000, v18\nMvaa 164,188,220\n"
"Ra 168,0,26624\nMU 76,76,93\nMvaa 164,184,212\nD1 1,16,46,1024\nMU 77,77,93\nMvaa 164,180,204\nRa 172,0,27648\nWL 8\nMvaa 164,176,196\n"
"buffer_load_dwordx4 a[228:231], v1, s[16:19], s4 offen offset:1024 sc0 sc1\nv_lshlrev_b32 v78, 16, v19\nMvaa 168,128,192\nRa 176,0,28672\nv_and_b32 v79, 0xffff0000, v19\n"
"Mvaa 168,132,200\ns_add_u32 s4, s92, 655360\nMU 78,78,93\nMvaa 168,136,208\nRa 180,0,29696\nMU 79,79,93\nMvaa 168,140,216\n"
"buffer_load_dwordx4 a[232:235], v1, s[16:19], s4 offen offset:0 sc0 sc1\nMvaa 172,140,220\nRa 184,0,30720\nMvaa 172,136,212\nD1 1,16,46,2048\nMvaa 172,132,204\nRa 188,0,31744\n"
"Mvaa 172,128,196\ns_waitcnt vmcnt(10) lgkmcnt(8)\ns_barrier\nMvaa 176,144,192\nRa 128,2,16384\nbuffer_load_dwordx4 a[236:239], v1, s[16:19], s4 offen offset:1024 sc0 sc1\n"
"Mvaa 176,148,200\nMvaa 176,152,208\nRa 132,2,17408\ns_add_u32 s4, s92, 786432\nMvaa 176,156,216\nMvaa 180,156,220\nRa 136,2,18432\n"
"buffer_load_dwordx4 a[240:243], v1, s[16:19], s4 offen offset:0 sc0 sc1\nMvaa 180,152,212\nD1 1,16,46,3072\nMvaa 180,148,204\nRa 140,2,19456\nWL 8\nMvaa 180,144,196\n"
"Mvaa 184,160,192\nRa 144,2,20480\nbuffer_load_dwordx4 a[244:247], v1, s[16:19], s4 offen offset:1024 sc0 sc1\nMvaa 184,164,200\nMvaa 184,168,208\nRa 148,2,21504\n"
"s_add_u32 s4, s92, 917504\nMvaa 184,172,216\ns_add_u32 m0, s89, 65536\ns_add_u32 s46, s93, 196608\nMvaa 188,172,220\nRa 152,2,22528\n"
"buffer_load_dwordx4 a[248:251], v1, s[16:19], s4 offen offset:0 sc0 sc1\nMvaa 188,168,212\nD1 1,16,46,0\nMvaa 188,164,204\nRa 156,2,23552\nWL 8\nMvaa 188,160,196\n"
"Mvaa 192,176,192\nRa 160,2,24576\nbuffer_load_dwordx4 a[252:255], v1, s[16:19], s4 offen offset:1024 sc0 sc1\nMvaa 192,180,200\nMvaa 192,184,208\nRa 164,2,25600\nMvaa 192,188,216\n"
"Mvaa 196,188,220\nRa 168,2,26624\nMvaa 196,184,212\nD1 1,16,46,1024\nMvaa 196,180,204\nRa 172,2,27648\nWL 8\nMvaa 196,176,196\nMvaa 200,128,192\nRa 176,2,28672\nMvaa 200,132,200\n"
"Mvaa 200,136,208\nRa 180,2,29696\nMvaa 200,140,216\nMvaa 204,140,220\nRa 184,2,30720\nMvaa 204,136,212\nD1 1,16,46,2048\nMvaa 204,132,204\nRa 188,2,31744\nWL 8\nMvaa 204,128,196\n"
"Mvaa 208,144,192\nRa 128,2,0\nMvaa 208,148,200\nMvaa 208,152,208\nRa 132,2,1024\nMvaa 208,156,216\nMvaa 212,156,220\nRa 136,2,2048\nMvaa 212,152,212\nD1 1,16,46,3072\n"
"Mvaa 212,148,204\nRa 140,2,3072\nWL 8\nMvaa 212,144,196\nMvaa 216,160,192\nRa 144,2,4096\nMvaa 216,164,200\nMvaa 216,168,208\nRa 148,2,5120\nMvaa 216,172,216\nMvaa 220,172,220\n"
"Ra 152,2,6144\nMvaa 220,168,212\nMvaa 220,164,204\nRa 156,2,7168\nWL 8\nMvaa 220,160,196\nMvaa 224,176,192\nRa 160,2,8192\nMvaa 224,180,200\nMvaa 224,184,208\nRa 164,2,9216\n"
"Mvaa 224,188,216\nMvaa 228,188,220\nRa 168,2,10240\nMvaa 228,184,212\nMvaa 228,180,204\nRa 172,2,11264\nWL 8\nMvaa 228,176,196\nMvaa 232,128,192\nRa 176,2,12288\nMvaa 232,132,200\n"
"v_and_b32 v27, 0x70, v1\nv_lshlrev_b32 v27, 5, v27\nbuffer_load_dword v27, v27, s[16:19], s6 offen sc0 sc1\nMvaa 232,136,208\nRa 180,2,13312\nMvaa 232,140,216\nMvaa 236,140,220\n"
"Ra 184,2,14336\nMvaa 236,136,212\nMvaa 236,132,204\nRa 188,2,15360\nMvaa 236,128,196\ns_waitcnt vmcnt(8) lgkmcnt(8)\ns_barrier\nMvaa 240,144,192\nRa 128,2,32768\nWV 4\nAR 22,227\n"
"Mvaa 240,148,200\nAR 23,231\nv_max_u32 v21, v22, v23\nAR 22,235\nMvaa 240,152,208\nRa 132,2,33792\nAR 23,239\nMvaa 240,156,216\nv_max3_u32 v21, v21, v22, v23\nAR 22,243\n"
"AR 23,247\nMvaa 244,156,220\nRa 136,2,34816\nv_max3_u32 v21, v21, v22, v23\nMvaa 244,152,212\nAR 22,251\nAR 23,255\nv_max3_u32 v21, v21, v22, v23\nMvaa 244,148,204\n"
"Ra 140,2,35840\nWL 8\nMvaa 244,144,196\nv_cmp_eq_u32 vcc, -1, v21\ns_nop 0\ns_cbranch_vccnz .S128\n.S129:\nMvaa 136,160,224\nRa 144,2,36864\nMvaa 136,164,232\nMvaa 136,168,240\n"
"Ra 148,2,37888\nMvaa 136,172,248\nMvaa 140,172,252\nRa 152,2,38912\nMvaa 140,168,244\nMvaa 140,164,236\nRa 156,2,39936\nWL 8\nMvaa 140,160,228\nMvaa 144,176,224\nRa 160,2,40960\n"
"Mvaa 144,180,232\nMvaa 144,184,240\nRa 164,2,41984\nMvaa 144,188,248\nMvaa 148,188,252\nRa 168,2,43008\nMvaa 148,184,244\nMvaa 148,180,236\nRa 172,2,44032\nWL 8\nMvaa 148,176,228\n"
"CV 136,136,137\nCV 137,138,139\nMvaa 152,128,224\nRa 176,2,45056\nCV 138,144,145\nMvaa 152,132,232\nCV 139,146,147\ns_add_u32 s5, s95, 0\nMvaa 152,136,240\nRa 180,2,46080\n"
"Sv 136,1,16,5,0\nMvaa 152,140,248\nCV 140,140,141\nCV 141,142,143\nMvaa 156,140,252\nRa 184,2,47104\nCV 142,148,149\nMvaa 156,136,244\nCV 143,150,151\nSv 140,1,16,5,1024\n"
"Mvaa 156,132,236\nRa 188,2,48128\nWL 8\nMvaa 156,128,228\nMvaa 160,144,224\nRa 128,2,49152\nMvaa 160,148,232\nMvaa 160,152,240\nRa 132,2,50176\nMvaa 160,156,248\nMvaa 164,156,252\n"
"Ra 136,2,51200\nMvaa 164,152,244\nMvaa 164,148,236\nRa 140,2,52224\nWL 8\nMvaa 164,144,228\nCV 152,152,153\nCV 153,154,155\nMvaa 168,160,224\nRa 144,2,53248\nCV 154,160,161\n"
"Mvaa 168,164,232\nCV 155,162,163\ns_add_u32 s5, s95, 131072\nMvaa 168,168,240\nRa 148,2,54272\nSv 152,1,16,5,0\nMvaa 168,172,248\nCV 156,156,157\nCV 157,158,159\nMvaa 172,172,252\n"
"Ra 152,2,55296\nCV 158,164,165\nMvaa 172,168,244\nCV 159,166,167\nSv 156,1,16,5,1024\nMvaa 172,164,236\nRa 156,2,56320\nWL 8\nMvaa 172,160,228\nMvaa 176,176,224\nRa 160,2,57344\n"
"Mvaa 176,180,232\nMvaa 176,184,240\nRa 164,2,58368\nMvaa 176,188,248\nMvaa 180,188,252\nRa 168,2,59392\nMvaa 180,184,244\nMvaa 180,180,236\nRa 172,2,60416\nWL 8\nMvaa 180,176,228\n"
"CV 168,168,169\nCV 169,170,171\nMvaa 184,128,224\nRa 176,2,61440\nCV 170,176,177\nMvaa 184,132,232\nCV 171,178,179\ns_add_u32 s5, s95, 262144\nMvaa 184,136,240\nRa 180,2,62464\n"
"Sv 168,1,16,5,0\nMvaa 184,140,248\nCV 172,172,173\nCV 173,174,175\nMvaa 188,140,252\nRa 184,2,63488\nCV 174,180,181\nMvaa 188,136,244\nCV 175,182,183\nSv 172,1,16,5,1024\n"
"Mvaa 188,132,236\nRa 188,2,64512\nMvaa 188,128,228\ns_waitcnt vmcnt(7) lgkmcnt(8)\ns_barrier\nMvaa 192,144,224\nRa 128,3,0\nMvaa 192,148,232\nMvaa 192,152,240\nRa 132,3,1024\n"
"Mvaa 192,156,248\nMvaa 196,156,252\nRa 136,3,2048\nMvaa 196,152,244\nMvaa 196,148,236\nRa 140,3,3072\nWL 8\nMvaa 196,144,228\nCV 184,184,185\nCV 185,186,187\nMvaa 200,160,224\n"
"Ra 144,3,4096\nCV 186,192,193\nMvaa 200,164,232\nCV 187,194,195\ns_add_u32 s5, s95, 393216\nMvaa 200,168,240\nRa 148,3,5120\nSv 184,1,16,5,0\nMvaa 200,172,248\nCV 188,188,189\n"
"CV 189,190,191\nMvaa 204,172,252\nRa 152,3,6144\nCV 190,196,197\nMvaa 204,168,244\nCV 191,198,199\nSv 188,1,16,5,1024\nMvaa 204,164,236\nRa 156,3,7168\nWL 8\nMvaa 204,160,228\n"
"Mvaa 208,176,224\nRa 160,3,8192\nMvaa 208,180,232\nMvaa 208,184,240\nRa 164,3,9216\nMvaa 208,188,248\nMvaa 212,188,252\nRa 168,3,10240\nMvaa 212,184,244\nMvaa 212,180,236\n"
"Ra 172,3,11264\nWL 8\nMvaa 212,176,228\nCV 200,200,201\nCV 201,202,203\nMvaa 216,128,224\nRa 176,3,12288\nCV 202,208,209\nMvaa 216,132,232\nCV 203,210,211\n"
"s_add_u32 s5, s95, 524288\nMvaa 216,136,240\nRa 180,3,13312\nSv 200,1,16,5,0\nMvaa 216,140,248\nCV 204,204,205\nCV 205,206,207\nMvaa 220,140,252\nRa 184,3,14336\nCV 206,212,213\n"
"Mvaa 220,136,244\nCV 207,214,215\nSv 204,1,16,5,1024\nMvaa 220,132,236\nRa 188,3,15360\nWL 8\nMvaa 220,128,228\nMvaa 224,144,224\nMvaa 224,148,232\nMvaa 224,152,240\n"
"Mvaa 224,156,248\nMvaa 228,156,252\nMvaa 228,152,244\nMvaa 228,148,236\nWL 4\nMvaa 228,144,228\nCV 216,216,217\nCV 217,218,219\nMvaa 232,160,224\nCV 218,224,225\nCV 219,226,227\n"
"Mvaa 232,164,232\ns_add_u32 s5, s95, 655360\nSv 216,1,16,5,0\nMvaa 232,168,240\nMvaa 232,172,248\nCV 220,220,221\nCV 221,222,223\nMvaa 236,172,252\nCV 222,228,229\nCV 223,230,231\n"
"Mvaa 236,168,244\nSv 220,1,16,5,1024\nMvaa 236,164,236\nWL 0\nMvaa 236,160,228\nMvaa 240,176,224\nMvaa 240,180,232\nMvaa 240,184,240\nMvaa 240,188,248\nMvaa 244,188,252\n"
"Mvaa 244,184,244\nMvaa 244,180,236\nMvaa 244,176,228\nCV 232,232,233\nCV 233,234,235\ns_nop 1\nCV 234,240,241\nCV 235,242,243\ns_add_u32 s5, s95, 786432\nSv 232,1,16,5,0\n"
"CV 236,236,237\nCV 237,238,239\nCV 238,244,245\nCV 239,246,247\nSv 236,1,16,5,1024\ns_cmp_eq_u32 s13, 7\ns_cbranch_scc1 .Z140\nWV 14\nv_xor_b32 v27, s15, v27\n"
"v_and_b32 v24, 0xffffdfff, v27\nv_cmp_ne_u32 vcc, 0, v24\ns_nop 0\ns_cbranch_vccnz .S137\n.S138:\nv_cmp_ne_u32 s[62:63], 0, v27\ns_mov_b32 s7, 262144\ns_mov_b32 s3, 0xff\n.Z141:\n"
"s_bitcmp1_b32 s3, 0\ns_cbranch_scc0 .Z143\ns_bitcmp1_b32 s62, 0\ns_cselect_b32 s2, 0x2000, 0\ns_add_u32 s2, s2, 0x0\ns_add_u32 s2, s2, s98\n"
"buffer_load_dwordx4 v[96:99], v1, s[16:19], s2 offen offset:0 sc0 sc1\nbuffer_load_dwordx4 v[100:103], v1, s[16:19], s2 offen offset:1024 sc0 sc1\n.Z143:\ns_bitcmp1_b32 s3, 1\n"
"s_cbranch_scc0 .Z144\ns_bitcmp1_b32 s62, 1\ns_cselect_b32 s2, 0x2000, 0\ns_add_u32 s2, s2, 0x1800000\ns_add_u32 s2, s2, s98\n"
"buffer_load_dwordx4 v[104:107], v1, s[16:19], s2 offen offset:0 sc0 sc1\nbuffer_load_dwordx4 v[108:111], v1, s[16:19], s2 offen offset:1024 sc0 sc1\n.Z144:\ns_bitcmp1_b32 s3, 2\n"
"s_cbranch_scc0 .Z145\ns_bitcmp1_b32 s62, 2\ns_cselect_b32 s2, 0x2000, 0\ns_add_u32 s2, s2, 0x3000000\ns_add_u32 s2, s2, s98\n"
"buffer_load_dwordx4 v[112:115], v1, s[16:19], s2 offen offset:0 sc0 sc1\nbuffer_load_dwordx4 v[116:119], v1, s[16:19], s2 offen offset:1024 sc0 sc1\n.Z145:\ns_bitcmp1_b32 s3, 3\n"
"s_cbranch_scc0 .Z146\ns_bitcmp1_b32 s62, 3\ns_cselect_b32 s2, 0x2000, 0\ns_add_u32 s2, s2, 0x4800000\ns_add_u32 s2, s2, s98\n"
"buffer_load_dwordx4 v[120:123], v1, s[16:19], s2 offen offset:0 sc0 sc1\nbuffer_load_dwordx4 v[124:127], v1, s[16:19], s2 offen offset:1024 sc0 sc1\n.Z146:\ns_bitcmp1_b32 s3, 4\n"
"s_cbranch_scc0 .Z147\ns_bitcmp1_b32 s62, 4\ns_cselect_b32 s2, 0x2000, 0\ns_add_u32 s2, s2, 0x6000000\ns_add_u32 s2, s2, s98\n"
"buffer_load_dwordx4 v[32:35], v1, s[16:19], s2 offen offset:0 sc0 sc1\nbuffer_load_dwordx4 v[36:39], v1, s[16:19], s2 offen offset:1024 sc0 sc1\n.Z147:\ns_bitcmp1_b32 s3, 5\n"
"s_cbranch_scc0 .Z148\ns_bitcmp1_b32 s62, 5\ns_cselect_b32 s2, 0x2000, 0\ns_add_u32 s2, s2, 0x7800000\ns_add_u32 s2, s2, s98\n"
"buffer_load_dwordx4 v[40:43], v1, s[16:19], s2 offen offset:0 sc0 sc1\nbuffer_load_dwordx4 v[44:47], v1, s[16:19], s2 offen offset:1024 sc0 sc1\n.Z148:\ns_bitcmp1_b32 s3, 6\n"
"s_cbranch_scc0 .Z149\ns_bitcmp1_b32 s62, 6\ns_cselect_b32 s2, 0x2000, 0\ns_add_u32 s2, s2, 0x9000000\ns_add_u32 s2, s2, s98\n"
"buffer_load_dwordx4 v[48:51], v1, s[16:19], s2 offen offset:0 sc0 sc1\nbuffer_load_dwordx4 v[52:55], v1, s[16:19], s2 offen offset:1024 sc0 sc1\n.Z149:\ns_bitcmp1_b32 s3, 7\n"
"s_cbranch_scc0 .Z150\ns_bitcmp1_b32 s62, 7\ns_cselect_b32 s2, 0x2000, 0\ns_add_u32 s2, s2, 0xa800000\ns_add_u32 s2, s2, s98\n"
"buffer_load_dwordx4 v[56:59], v1, s[16:19], s2 offen offset:0 sc0 sc1\nbuffer_load_dwordx4 v[60:63], v1, s[16:19], s2 offen offset:1024 sc0 sc1\n.Z150:\ns_mov_b32 s3, 0\nWV 0\n"
"v_max_u32 v82, v99, v103\nv_cmp_eq_u32 s[44:45], -1, v82\nv_max_u32 v82, v107, v111\nv_cmp_eq_u32 s[46:47], -1, v82\nv_max_u32 v82, v115, v119\nv_cmp_eq_u32 s[48:49], -1, v82\n"
"v_max_u32 v82, v123, v127\nv_cmp_eq_u32 s[50:51], -1, v82\nv_max_u32 v82, v35, v39\nv_cmp_eq_u32 s[52:53], -1, v82\nv_max_u32 v82, v43, v47\nv_cmp_eq_u32 s[54:55], -1, v82\n"
"v_max_u32 v82, v51, v55\nv_cmp_eq_u32 s[56:57], -1, v82\nv_max_u32 v82, v59, v63\nv_cmp_eq_u32 s[58:59], -1, v82\ns_cmp_lg_u64 s[44:45], 0\ns_cselect_b32 s2, 1, 0\n"
"s_or_b32 s3, s3, s2\ns_cmp_lg_u64 s[46:47], 0\ns_cselect_b32 s2, 2, 0\ns_or_b32 s3, s3, s2\ns_cmp_lg_u64 s[48:49], 0\ns_cselect_b32 s2, 4, 0\ns_or_b32 s3, s3, s2\n"
"s_cmp_lg_u64 s[50:51], 0\ns_cselect_b32 s2, 8, 0\ns_or_b32 s3, s3, s2\ns_cmp_lg_u64 s[52:53], 0\ns_cselect_b32 s2, 16, 0\ns_or_b32 s3, s3, s2\ns_cmp_lg_u64 s[54:55], 0\n"
"s_cselect_b32 s2, 32, 0\ns_or_b32 s3, s3, s2\ns_cmp_lg_u64 s[56:57], 0\ns_cselect_b32 s2, 64, 0\ns_or_b32 s3, s3, s2\ns_cmp_lg_u64 s[58:59], 0\ns_cselect_b32 s2, 128, 0\n"
"s_or_b32 s3, s3, s2\ns_cbranch_scc0 .Z142\ns_sub_u32 s7, s7, 1\ns_cbranch_scc0 .Z141\n.Z142:\nZvvv 136,130,96\nMvvv 136,130,104\nMvvv 136,130,112\nMvvv 136,130,120\n"
"Mvvv 136,130,32\nMvvv 136,130,40\nMvvv 136,130,48\nMvvv 136,130,56\nZvvv 140,128,96\nMvvv 140,128,104\nMvvv 140,128,112\nMvvv 140,128,120\nMvvv 140,128,32\nMvvv 140,128,40\n"
"Mvvv 140,128,48\nMvvv 140,128,56\nZvvv 144,130,100\nMvvv 144,130,108\nMvvv 144,130,116\nMvvv 144,130,124\nMvvv 144,130,36\nMvvv 144,130,44\nMvvv 144,130,52\nMvvv 144,130,60\n"
"Zvvv 148,128,100\nMvvv 148,128,108\nMvvv 148,128,116\nMvvv 148,128,124\nMvvv 148,128,36\nMvvv 148,128,44\nMvvv 148,128,52\nMvvv 148,128,60\nFM 64,80,136\nFM 65,80,137\n"
"FM 66,80,138\nFM 67,80,139\nFM 68,80,140\nFM 69,80,141\nFM 70,80,142\nFM 71,80,143\nCV 12,64,65\nCV 13,66,67\nCV 14,68,69\nCV 15,70,71\nSv 12,91,36,91,0\nFM 72,81,144\n"
"FM 73,81,145\nFM 74,81,146\nFM 75,81,147\nFM 76,81,148\nFM 77,81,149\nFM 78,81,150\nFM 79,81,151\nCV 16,72,73\nCV 17,74,75\nCV 18,76,77\nCV 19,78,79\nSv 16,91,36,1,0\n.Z140:\n"
"s_endpgm\n.T71:\ns_mov_b32 s62, 1\ns_call_b64 s[80:81], .RA12\ns_branch .T72\n.T73:\ns_mov_b32 s62, 1\ns_call_b64 s[80:81], .RA12\ns_branch .T74\n.T75:\ns_mov_b32 s62, 0\n"
"s_call_b64 s[80:81], .RB13\ns_branch .T76\n.T77:\ns_mov_b32 s62, 0\ns_call_b64 s[80:81], .RA12\ns_branch .T78\n.T79:\ns_mov_b32 s62, 0\ns_call_b64 s[80:81], .RB13\ns_branch .T80\n"
".T81:\ns_mov_b32 s62, 0\ns_call_b64 s[80:81], .RA12\ns_branch .T82\n.S83:\ns_mov_b32 s7, 262144\n.S85:\ns_sleep 8\nv_and_b32 v27, 0xf0, v1\n"
"buffer_load_dword v27, v27, s[16:19], s61 offen offset:12 sc0 sc1\nWV 0\nv_cmp_ne_u32 vcc, s15, v27\ns_nop 0\ns_cbranch_vccz .S84\ns_sub_u32 s7, s7, 1\ns_cbranch_scc0 .S85\n"
"s_branch .S84\n.S86:\ns_mov_b32 s7, 262144\n.S88:\ns_sleep 8\nv_and_b32 v27, 0x70, v1\nbuffer_load_dword v27, v27, s[16:19], s61 offen offset:136 sc0 sc1\nWV 0\n"
"v_cmp_ne_u32 vcc, s15, v27\ns_nop 0\ns_cbranch_vccz .S87\ns_sub_u32 s7, s7, 1\ns_cbranch_scc0 .S88\ns_branch .S87\n.T100:\ns_mov_b32 s62, 0\ns_call_b64 s[80:81], .RB13\n"
"s_branch .T101\n.T102:\ns_mov_b32 s62, 0\ns_call_b64 s[80:81], .RA12\ns_branch .T103\n.E104:\nv_mov_b32 v12, s97\nv_mov_b32 v13, s97\nv_mov_b32 v14, s97\nv_mov_b32 v15, s97\n"
"v_cmpx_gt_i32 vcc, 3, v3\nv_mov_b32 v7, s97\nv_mov_b32 v19, s97\nv_cmpx_gt_i32 vcc, 2, v3\nv_mov_b32 v6, s97\nv_mov_b32 v18, s97\nv_cmpx_gt_i32 vcc, 1, v3\nv_mov_b32 v5, s97\n"
"v_mov_b32 v17, s97\nv_cmpx_gt_i32 vcc, 0, v3\nv_mov_b32 v4, s97\nv_mov_b32 v16, s97\nv_cmpx_gt_i32 vcc, -16, v3\nv_mov_b32 v8, s97\nv_mov_b32 v9, s97\nv_mov_b32 v10, s97\n"
"v_mov_b32 v11, s97\ns_mov_b64 exec, -1\ns_branch .E105\n.T106:\ns_mov_b32 s62, 0\ns_call_b64 s[80:81], .RB13\ns_branch .T107\n.E108:\nv_mov_b32 v12, s97\nv_mov_b32 v13, s97\n"
"v_mov_b32 v14, s97\nv_mov_b32 v15, s97\nv_cmpx_gt_i32 vcc, 3, v0\nv_mov_b32 v7, s97\nv_mov_b32 v19, s97\nv_cmpx_gt_i32 vcc, 2, v0\nv_mov_b32 v6, s97\nv_mov_b32 v18, s97\n"
"v_cmpx_gt_i32 vcc, 1, v0\nv_mov_b32 v5, s97\nv_mov_b32 v17, s97\nv_cmpx_gt_i32 vcc, 0, v0\nv_mov_b32 v4, s97\nv_mov_b32 v16, s97\nv_cmpx_gt_i32 vcc, -16, v0\nv_mov_b32 v8, s97\n"
"v_mov_b32 v9, s97\nv_mov_b32 v10, s97\nv_mov_b32 v11, s97\ns_mov_b64 exec, -1\ns_branch .E109\n.T110:\ns_mov_b32 s62, 0\ns_call_b64 s[80:81], .RA12\ns_branch .T111\n.E113:\n"
"buffer_load_dwordx4 v[100:103], v108, s[16:19], s65 offen offset:0 sc0 sc1\nbuffer_load_dwordx4 v[104:107], v108, s[16:19], s65 offen offset:256 sc0 sc1\ns_branch .E114\n.S119:\n"
"s_mov_b32 s7, 262144\n.S121:\ns_sleep 8\nbuffer_load_dwordx4 v[100:103], v108, s[16:19], s65 offen offset:0 sc0 sc1\n"
"buffer_load_dwordx4 v[104:107], v108, s[16:19], s65 offen offset:256 sc0 sc1\nWV 0\nv_xor_b32 v109, s15, v102\nv_xor_b32 v110, s15, v106\nv_or_b32 v109, v109, v110\n"
"v_cmp_ne_u32 vcc, 0, v109\ns_nop 0\ns_cbranch_vccz .S120\ns_sub_u32 s7, s7, 1\ns_cbranch_scc0 .S121\ns_branch .S120\n.D115:\nWV 0\nv_xor_b32 v109, s15, v102\n"
"v_xor_b32 v110, s15, v106\nv_or_b32 v109, v109, v110\nv_cmp_ne_u32 vcc, 0, v109\ns_nop 0\ns_cbranch_vccnz .S119\n.S120:\ns_add_u32 s5, s66, 0\n"
"buffer_load_dwordx4 a[192:195], v1, s[16:19], s5 offen offset:0 sc1\nbuffer_load_dwordx4 a[196:199], v1, s[16:19], s5 offen offset:1024 sc1\n"
"buffer_load_dwordx4 a[200:203], v1, s[16:19], s5 offen offset:2048 sc1\nbuffer_load_dwordx4 a[204:207], v1, s[16:19], s5 offen offset:3072 sc1\ns_add_u32 s5, s66, 4096\n"
"buffer_load_dwordx4 a[208:211], v1, s[16:19], s5 offen offset:0 sc1\nbuffer_load_dwordx4 a[212:215], v1, s[16:19], s5 offen offset:1024 sc1\n"
"buffer_load_dwordx4 a[216:219], v1, s[16:19], s5 offen offset:2048 sc1\nbuffer_load_dwordx4 a[220:223], v1, s[16:19], s5 offen offset:3072 sc1\ns_add_u32 s5, s66, 16384\n"
"buffer_load_dwordx4 a[224:227], v1, s[16:19], s5 offen offset:0 sc1\nbuffer_load_dwordx4 a[228:231], v1, s[16:19], s5 offen offset:1024 sc1\n"
"buffer_load_dwordx4 a[232:235], v1, s[16:19], s5 offen offset:2048 sc1\nbuffer_load_dwordx4 a[236:239], v1, s[16:19], s5 offen offset:3072 sc1\ns_add_u32 s5, s66, 20480\n"
"buffer_load_dwordx4 a[240:243], v1, s[16:19], s5 offen offset:0 sc1\nbuffer_load_dwordx4 a[244:247], v1, s[16:19], s5 offen offset:1024 sc1\n"
"buffer_load_dwordx4 a[248:251], v1, s[16:19], s5 offen offset:2048 sc1\nbuffer_load_dwordx4 a[252:255], v1, s[16:19], s5 offen offset:3072 sc1\nWV 0\ns_mov_b32 s67, 0\n"
"s_branch .D116\n.D117:\nv_mov_b32 v130, 0\nv_mov_b32 v131, 0\ns_mov_b32 s63, 0xff800000\ns_mov_b32 s67, 1\ns_branch .D118\n.T122:\ns_lshl3_add_u32 s62, s67, 2\n"
"s_call_b64 s[80:81], .RB13\ns_cmp_eq_u32 s67, 0\ns_cbranch_scc1 .T123\nSU 20,100,88\nv_min_f32 v20, 0, v20\nEX 20,20\ns_nop 0\nFM 86,101,20\nSU 21,104,89\nv_min_f32 v21, 0, v21\n"
"EX 21,21\ns_nop 0\nFM 87,105,21\nAR 22,192\nv_and_b32 v23, 0xffff0000, v22\nv_lshlrev_b32 v22, 16, v22\nMU 22,22,20\nMU 23,23,20\nCV 22,22,23\nAW 192,22\nAR 22,193\n"
"v_and_b32 v23, 0xffff0000, v22\nv_lshlrev_b32 v22, 16, v22\nMU 22,22,20\nMU 23,23,20\nCV 22,22,23\nAW 193,22\nAR 22,194\nv_and_b32 v23, 0xffff0000, v22\n"
"v_lshlrev_b32 v22, 16, v22\nMU 22,22,20\nMU 23,23,20\nCV 22,22,23\nAW 194,22\nAR 22,195\nv_and_b32 v23, 0xffff0000, v22\nv_lshlrev_b32 v22, 16, v22\nMU 22,22,20\nMU 23,23,20\n"
"CV 22,22,23\nAW 195,22\nAR 22,196\nv_and_b32 v23, 0xffff0000, v22\nv_lshlrev_b32 v22, 16, v22\nMU 22,22,21\nMU 23,23,21\nCV 22,22,23\nAW 196,22\nAR 22,197\n"
"v_and_b32 v23, 0xffff0000, v22\nv_lshlrev_b32 v22, 16, v22\nMU 22,22,21\nMU 23,23,21\nCV 22,22,23\nAW 197,22\nAR 22,198\nv_and_b32 v23, 0xffff0000, v22\n"
"v_lshlrev_b32 v22, 16, v22\nMU 22,22,21\nMU 23,23,21\nCV 22,22,23\nAW 198,22\nAR 22,199\nv_and_b32 v23, 0xffff0000, v22\nv_lshlrev_b32 v22, 16, v22\nMU 22,22,21\nMU 23,23,21\n"
"CV 22,22,23\nAW 199,22\nAR 22,200\nv_and_b32 v23, 0xffff0000, v22\nv_lshlrev_b32 v22, 16, v22\nMU 22,22,20\nMU 23,23,20\nCV 22,22,23\nAW 200,22\nAR 22,201\n"
"v_and_b32 v23, 0xffff0000, v22\nv_lshlrev_b32 v22, 16, v22\nMU 22,22,20\nMU 23,23,20\nCV 22,22,23\nAW 201,22\nAR 22,202\nv_and_b32 v23, 0xffff0000, v22\n"
"v_lshlrev_b32 v22, 16, v22\nMU 22,22,20\nMU 23,23,20\nCV 22,22,23\nAW 202,22\nAR 22,203\nv_and_b32 v23, 0xffff0000, v22\nv_lshlrev_b32 v22, 16, v22\nMU 22,22,20\nMU 23,23,20\n"
"CV 22,22,23\nAW 203,22\nAR 22,204\nv_and_b32 v23, 0xffff0000, v22\nv_lshlrev_b32 v22, 16, v22\nMU 22,22,21\nMU 23,23,21\nCV 22,22,23\nAW 204,22\nAR 22,205\n"
"v_and_b32 v23, 0xffff0000, v22\nv_lshlrev_b32 v22, 16, v22\nMU 22,22,21\nMU 23,23,21\nCV 22,22,23\nAW 205,22\nAR 22,206\nv_and_b32 v23, 0xffff0000, v22\n"
"v_lshlrev_b32 v22, 16, v22\nMU 22,22,21\nMU 23,23,21\nCV 22,22,23\nAW 206,22\nAR 22,207\nv_and_b32 v23, 0xffff0000, v22\nv_lshlrev_b32 v22, 16, v22\nMU 22,22,21\nMU 23,23,21\n"
"CV 22,22,23\nAW 207,22\nAR 22,208\nv_and_b32 v23, 0xffff0000, v22\nv_lshlrev_b32 v22, 16, v22\nMU 22,22,20\nMU 23,23,20\nCV 22,22,23\nAW 208,22\nAR 22,209\n"
"v_and_b32 v23, 0xffff0000, v22\nv_lshlrev_b32 v22, 16, v22\nMU 22,22,20\nMU 23,23,20\nCV 22,22,23\nAW 209,22\nAR 22,210\nv_and_b32 v23, 0xffff0000, v22\n"
"v_lshlrev_b32 v22, 16, v22\nMU 22,22,20\nMU 23,23,20\nCV 22,22,23\nAW 210,22\nAR 22,211\nv_and_b32 v23, 0xffff0000, v22\nv_lshlrev_b32 v22, 16, v22\nMU 22,22,20\nMU 23,23,20\n"
"CV 22,22,23\nAW 211,22\nAR 22,212\nv_and_b32 v23, 0xffff0000, v22\nv_lshlrev_b32 v22, 16, v22\nMU 22,22,21\nMU 23,23,21\nCV 22,22,23\nAW 212,22\nAR 22,213\n"
"v_and_b32 v23, 0xffff0000, v22\nv_lshlrev_b32 v22, 16, v22\nMU 22,22,21\nMU 23,23,21\nCV 22,22,23\nAW 213,22\nAR 22,214\nv_and_b32 v23, 0xffff0000, v22\n"
"v_lshlrev_b32 v22, 16, v22\nMU 22,22,21\nMU 23,23,21\nCV 22,22,23\nAW 214,22\nAR 22,215\nv_and_b32 v23, 0xffff0000, v22\nv_lshlrev_b32 v22, 16, v22\nMU 22,22,21\nMU 23,23,21\n"
"CV 22,22,23\nAW 215,22\nAR 22,216\nv_and_b32 v23, 0xffff0000, v22\nv_lshlrev_b32 v22, 16, v22\nMU 22,22,20\nMU 23,23,20\nCV 22,22,23\nAW 216,22\nAR 22,217\n"
"v_and_b32 v23, 0xffff0000, v22\nv_lshlrev_b32 v22, 16, v22\nMU 22,22,20\nMU 23,23,20\nCV 22,22,23\nAW 217,22\nAR 22,218\nv_and_b32 v23, 0xffff0000, v22\n"
"v_lshlrev_b32 v22, 16, v22\nMU 22,22,20\nMU 23,23,20\nCV 22,22,23\nAW 218,22\nAR 22,219\nv_and_b32 v23, 0xffff0000, v22\nv_lshlrev_b32 v22, 16, v22\nMU 22,22,20\nMU 23,23,20\n"
"CV 22,22,23\nAW 219,22\nAR 22,220\nv_and_b32 v23, 0xffff0000, v22\nv_lshlrev_b32 v22, 16, v22\nMU 22,22,21\nMU 23,23,21\nCV 22,22,23\nAW 220,22\nAR 22,221\n"
"v_and_b32 v23, 0xffff0000, v22\nv_lshlrev_b32 v22, 16, v22\nMU 22,22,21\nMU 23,23,21\nCV 22,22,23\nAW 221,22\nAR 22,222\nv_and_b32 v23, 0xffff0000, v22\n"
"v_lshlrev_b32 v22, 16, v22\nMU 22,22,21\nMU 23,23,21\nCV 22,22,23\nAW 222,22\nAR 22,223\nv_and_b32 v23, 0xffff0000, v22\nv_lshlrev_b32 v22, 16, v22\nMU 22,22,21\nMU 23,23,21\n"
"CV 22,22,23\nAW 223,22\nAR 22,224\nv_and_b32 v23, 0xffff0000, v22\nv_lshlrev_b32 v22, 16, v22\nMU 22,22,20\nMU 23,23,20\nCV 22,22,23\nAW 224,22\nAR 22,225\n"
"v_and_b32 v23, 0xffff0000, v22\nv_lshlrev_b32 v22, 16, v22\nMU 22,22,20\nMU 23,23,20\nCV 22,22,23\nAW 225,22\nAR 22,226\nv_and_b32 v23, 0xffff0000, v22\n"
"v_lshlrev_b32 v22, 16, v22\nMU 22,22,20\nMU 23,23,20\nCV 22,22,23\nAW 226,22\nAR 22,227\nv_and_b32 v23, 0xffff0000, v22\nv_lshlrev_b32 v22, 16, v22\nMU 22,22,20\nMU 23,23,20\n"
"CV 22,22,23\nAW 227,22\nAR 22,228\nv_and_b32 v23, 0xffff0000, v22\nv_lshlrev_b32 v22, 16, v22\nMU 22,22,21\nMU 23,23,21\nCV 22,22,23\nAW 228,22\nAR 22,229\n"
"v_and_b32 v23, 0xffff0000, v22\nv_lshlrev_b32 v22, 16, v22\nMU 22,22,21\nMU 23,23,21\nCV 22,22,23\nAW 229,22\nAR 22,230\nv_and_b32 v23, 0xffff0000, v22\n"
"v_lshlrev_b32 v22, 16, v22\nMU 22,22,21\nMU 23,23,21\nCV 22,22,23\nAW 230,22\nAR 22,231\nv_and_b32 v23, 0xffff0000, v22\nv_lshlrev_b32 v22, 16, v22\nMU 22,22,21\nMU 23,23,21\n"
"CV 22,22,23\nAW 231,22\nAR 22,232\nv_and_b32 v23, 0xffff0000, v22\nv_lshlrev_b32 v22, 16, v22\nMU 22,22,20\nMU 23,23,20\nCV 22,22,23\nAW 232,22\nAR 22,233\n"
"v_and_b32 v23, 0xffff0000, v22\nv_lshlrev_b32 v22, 16, v22\nMU 22,22,20\nMU 23,23,20\nCV 22,22,23\nAW 233,22\nAR 22,234\nv_and_b32 v23, 0xffff0000, v22\n"
"v_lshlrev_b32 v22, 16, v22\nMU 22,22,20\nMU 23,23,20\nCV 22,22,23\nAW 234,22\nAR 22,235\nv_and_b32 v23, 0xffff0000, v22\nv_lshlrev_b32 v22, 16, v22\nMU 22,22,20\nMU 23,23,20\n"
"CV 22,22,23\nAW 235,22\nAR 22,236\nv_and_b32 v23, 0xffff0000, v22\nv_lshlrev_b32 v22, 16, v22\nMU 22,22,21\nMU 23,23,21\nCV 22,22,23\nAW 236,22\nAR 22,237\n"
"v_and_b32 v23, 0xffff0000, v22\nv_lshlrev_b32 v22, 16, v22\nMU 22,22,21\nMU 23,23,21\nCV 22,22,23\nAW 237,22\nAR 22,238\nv_and_b32 v23, 0xffff0000, v22\n"
"v_lshlrev_b32 v22, 16, v22\nMU 22,22,21\nMU 23,23,21\nCV 22,22,23\nAW 238,22\nAR 22,239\nv_and_b32 v23, 0xffff0000, v22\nv_lshlrev_b32 v22, 16, v22\nMU 22,22,21\nMU 23,23,21\n"
"CV 22,22,23\nAW 239,22\nAR 22,240\nv_and_b32 v23, 0xffff0000, v22\nv_lshlrev_b32 v22, 16, v22\nMU 22,22,20\nMU 23,23,20\nCV 22,22,23\nAW 240,22\nAR 22,241\n"
"v_and_b32 v23, 0xffff0000, v22\nv_lshlrev_b32 v22, 16, v22\nMU 22,22,20\nMU 23,23,20\nCV 22,22,23\nAW 241,22\nAR 22,242\nv_and_b32 v23, 0xffff0000, v22\n"
"v_lshlrev_b32 v22, 16, v22\nMU 22,22,20\nMU 23,23,20\nCV 22,22,23\nAW 242,22\nAR 22,243\nv_and_b32 v23, 0xffff0000, v22\nv_lshlrev_b32 v22, 16, v22\nMU 22,22,20\nMU 23,23,20\n"
"CV 22,22,23\nAW 243,22\nAR 22,244\nv_and_b32 v23, 0xffff0000, v22\nv_lshlrev_b32 v22, 16, v22\nMU 22,22,21\nMU 23,23,21\nCV 22,22,23\nAW 244,22\nAR 22,245\n"
"v_and_b32 v23, 0xffff0000, v22\nv_lshlrev_b32 v22, 16, v22\nMU 22,22,21\nMU 23,23,21\nCV 22,22,23\nAW 245,22\nAR 22,246\nv_and_b32 v23, 0xffff0000, v22\n"
"v_lshlrev_b32 v22, 16, v22\nMU 22,22,21\nMU 23,23,21\nCV 22,22,23\nAW 246,22\nAR 22,247\nv_and_b32 v23, 0xffff0000, v22\nv_lshlrev_b32 v22, 16, v22\nMU 22,22,21\nMU 23,23,21\n"
"CV 22,22,23\nAW 247,22\nAR 22,248\nv_and_b32 v23, 0xffff0000, v22\nv_lshlrev_b32 v22, 16, v22\nMU 22,22,20\nMU 23,23,20\nCV 22,22,23\nAW 248,22\nAR 22,249\n"
"v_and_b32 v23, 0xffff0000, v22\nv_lshlrev_b32 v22, 16, v22\nMU 22,22,20\nMU 23,23,20\nCV 22,22,23\nAW 249,22\nAR 22,250\nv_and_b32 v23, 0xffff0000, v22\n"
"v_lshlrev_b32 v22, 16, v22\nMU 22,22,20\nMU 23,23,20\nCV 22,22,23\nAW 250,22\nAR 22,251\nv_and_b32 v23, 0xffff0000, v22\nv_lshlrev_b32 v22, 16, v22\nMU 22,22,20\nMU 23,23,20\n"
"CV 22,22,23\nAW 251,22\nAR 22,252\nv_and_b32 v23, 0xffff0000, v22\nv_lshlrev_b32 v22, 16, v22\nMU 22,22,21\nMU 23,23,21\nCV 22,22,23\nAW 252,22\nAR 22,253\n"
"v_and_b32 v23, 0xffff0000, v22\nv_lshlrev_b32 v22, 16, v22\nMU 22,22,21\nMU 23,23,21\nCV 22,22,23\nAW 253,22\nAR 22,254\nv_and_b32 v23, 0xffff0000, v22\n"
"v_lshlrev_b32 v22, 16, v22\nMU 22,22,21\nMU 23,23,21\nCV 22,22,23\nAW 254,22\nAR 22,255\nv_and_b32 v23, 0xffff0000, v22\nv_lshlrev_b32 v22, 16, v22\nMU 22,22,21\nMU 23,23,21\n"
"CV 22,22,23\nAW 255,22\nv_lshrrev_b32 v22, 8, v1\nv_bfe_u32 v23, v1, 6, 2\nv_cmp_eq_u32 vcc, v22, v23\nv_and_b32 v22, 16, v1\nv_mov_b32 v23, 0x3f80\n"
"v_cndmask_b32 v23, 0, v23, vcc\nv_lshlrev_b32 v23, v22, v23\nv_and_b32 v22, 32, v1\nv_cmp_eq_u32 vcc, 0, v22\nv_mov_b32 v128, 0\nv_mov_b32 v129, 0\n"
"v_cndmask_b32 v130, 0, v23, vcc\nv_sub_u32 v131, v23, v130\nv_mov_b32 v132, 0\nv_mov_b32 v133, 0\ns_nop 1\nMava 0,130,192\nMava 8,128,192\nMava 12,128,196\nMava 4,130,196\n"
"Mava 16,130,200\nMava 24,128,200\nMava 28,128,204\nMava 20,130,204\nMava 32,130,208\nMava 40,128,208\nMava 44,128,212\nMava 36,130,212\nMava 48,130,216\nMava 56,128,216\n"
"Mava 60,128,220\nMava 52,130,220\nMava 64,130,224\nMava 72,128,224\nMava 76,128,228\nMava 68,130,228\nMava 80,130,232\nMava 88,128,232\nMava 92,128,236\nMava 84,130,236\n"
"Mava 96,130,240\nMava 104,128,240\nMava 108,128,244\nMava 100,130,244\nMava 112,130,248\nMava 120,128,248\nMava 124,128,252\nMava 116,130,252\ns_nop 7\ns_branch .T123\n.S125:\n"
"s_mov_b32 s7, 262144\n.S127:\ns_sleep 8\ns_add_u32 s4, s92, 0\nbuffer_load_dwordx4 a[192:195], v1, s[16:19], s4 offen offset:0 sc0 sc1\n"
"buffer_load_dwordx4 a[196:199], v1, s[16:19], s4 offen offset:1024 sc0 sc1\ns_add_u32 s4, s92, 131072\nbuffer_load_dwordx4 a[200:203], v1, s[16:19], s4 offen offset:0 sc0 sc1\n"
"buffer_load_dwordx4 a[204:207], v1, s[16:19], s4 offen offset:1024 sc0 sc1\ns_add_u32 s4, s92, 262144\nbuffer_load_dwordx4 a[208:211], v1, s[16:19], s4 offen offset:0 sc0 sc1\n"
"buffer_load_dwordx4 a[212:215], v1, s[16:19], s4 offen offset:1024 sc0 sc1\ns_add_u32 s4, s92, 393216\nbuffer_load_dwordx4 a[216:219], v1, s[16:19], s4 offen offset:0 sc0 sc1\n"
"buffer_load_dwordx4 a[220:223], v1, s[16:19], s4 offen offset:1024 sc0 sc1\nWV 0\nAR 22,195\nAR 23,199\nv_max_u32 v21, v22, v23\nAR 22,203\nAR 23,207\n"
"v_max3_u32 v21, v21, v22, v23\nAR 22,211\nAR 23,215\nv_max3_u32 v21, v21, v22, v23\nAR 22,219\nAR 23,223\nv_max3_u32 v21, v21, v22, v23\nv_cmp_eq_u32 vcc, -1, v21\ns_nop 0\n"
"s_cbranch_vccz .S126\ns_sub_u32 s7, s7, 1\ns_cbranch_scc0 .S127\ns_branch .S126\n.S128:\ns_mov_b32 s7, 262144\n.S130:\ns_sleep 8\ns_add_u32 s4, s92, 524288\n"
"buffer_load_dwordx4 a[224:227], v1, s[16:19], s4 offen offset:0 sc0 sc1\nbuffer_load_dwordx4 a[228:231], v1, s[16:19], s4 offen offset:1024 sc0 sc1\ns_add_u32 s4, s92, 655360\n"
"buffer_load_dwordx4 a[232:235], v1, s[16:19], s4 offen offset:0 sc0 sc1\nbuffer_load_dwordx4 a[236:239], v1, s[16:19], s4 offen offset:1024 sc0 sc1\ns_add_u32 s4, s92, 786432\n"
"buffer_load_dwordx4 a[240:243], v1, s[16:19], s4 offen offset:0 sc0 sc1\nbuffer_load_dwordx4 a[244:247], v1, s[16:19], s4 offen offset:1024 sc0 sc1\ns_add_u32 s4, s92, 917504\n"
"buffer_load_dwordx4 a[248:251], v1, s[16:19], s4 offen offset:0 sc0 sc1\nbuffer_load_dwordx4 a[252:255], v1, s[16:19], s4 offen offset:1024 sc0 sc1\nWV 0\nAR 22,227\nAR 23,231\n"
"v_max_u32 v21, v22, v23\nAR 22,235\nAR 23,239\nv_max3_u32 v21, v21, v22, v23\nAR 22,243\nAR 23,247\nv_max3_u32 v21, v21, v22, v23\nAR 22,251\nAR 23,255\n"
"v_max3_u32 v21, v21, v22, v23\nv_cmp_eq_u32 vcc, -1, v21\ns_nop 0\ns_cbranch_vccz .S129\ns_sub_u32 s7, s7, 1\ns_cbranch_scc0 .S130\ns_branch .S129\n.S131:\ns_mov_b32 s7, 262144\n"
".S133:\ns_sleep 8\nbuffer_load_dwordx4 v[64:67], v89, s[16:19], s0 offen offset:0 sc0 sc1\nbuffer_load_dwordx4 v[68:71], v89, s[16:19], s0 offen offset:16 sc0 sc1\n"
"buffer_load_dwordx4 v[72:75], v89, s[16:19], s0 offen offset:1792 sc0 sc1\nbuffer_load_dwordx4 v[76:79], v89, s[16:19], s0 offen offset:1808 sc0 sc1\nWV 0\n"
"v_xor_b32 v92, s15, v66\nv_xor_b32 v93, s15, v70\nv_xor_b32 v94, s15, v74\nv_or3_b32 v92, v92, v93, v94\nv_xor_b32 v93, s15, v78\nv_or_b32 v92, v92, v93\nv_cmp_ne_u32 vcc, 0, v92\n"
"s_nop 0\ns_cbranch_vccz .S132\ns_sub_u32 s7, s7, 1\ns_cbranch_scc0 .S133\ns_branch .S132\n.S134:\ns_mov_b32 s7, 262144\n.S136:\ns_sleep 8\n"
"buffer_load_dword v27, off, s[16:19], s60 sc0 sc1\nWV 0\nv_xor_b32 v27, s15, v27\nv_and_b32 v24, 0xffffdfff, v27\nv_cmp_ne_u32 vcc, 0, v24\ns_nop 0\ns_cbranch_vccz .S135\n"
"s_sub_u32 s7, s7, 1\ns_cbranch_scc0 .S136\ns_branch .S135\n.S137:\ns_mov_b32 s7, 262144\n.S139:\ns_sleep 8\nv_and_b32 v27, 0x70, v1\nv_lshlrev_b32 v27, 5, v27\n"
"buffer_load_dword v27, v27, s[16:19], s6 offen sc0 sc1\nWV 0\nv_xor_b32 v27, s15, v27\nv_and_b32 v24, 0xffffdfff, v27\nv_cmp_ne_u32 vcc, 0, v24\ns_nop 0\ns_cbranch_vccz .S138\n"
"s_sub_u32 s7, s7, 1\ns_cbranch_scc0 .S139\ns_branch .S138\n.RA12:\ns_nop 7\ns_nop 3\nv_max3_f32 v22, v32, v33, v34\nv_max3_f32 v22, v22, v35, v40\nv_max3_f32 v22, v22, v41, v42\n"
"v_max_f32 v22, v22, v43\nv_mov_b32 v91, v22\ns_nop 1\nv_permlane32_swap_b32 v22, v91\nv_max_f32 v22, v22, v91\nv_mov_b32 v91, v22\ns_nop 1\nv_permlane16_swap_b32 v22, v91\n"
"v_max_f32 v22, v22, v91\nv_ceil_f32 v22, v22\nv_max3_f32 v23, v36, v37, v38\nv_max3_f32 v23, v23, v39, v44\nv_max3_f32 v23, v23, v45, v46\nv_max_f32 v23, v23, v47\n"
"v_mov_b32 v91, v23\ns_nop 1\nv_permlane32_swap_b32 v23, v91\nv_max_f32 v23, v23, v91\nv_mov_b32 v91, v23\ns_nop 1\nv_permlane16_swap_b32 v23, v91\nv_max_f32 v23, v23, v91\n"
"v_ceil_f32 v23, v23\ns_bitcmp1_b32 s62, 0\ns_cbranch_scc0 .R151\nv_cmp_lt_f32 vcc, 0xff000000, v22\ns_nop 1\nv_cndmask_b32 v22, 0, v22, vcc\nv_cmp_lt_f32 vcc, 0xff000000, v23\n"
"s_nop 1\nv_cndmask_b32 v23, 0, v23, vcc\ns_branch .R152\n.R151:\nv_max_f32 v22, 0, v22\nv_max_f32 v23, 0, v23\ns_bitcmp1_b32 s62, 3\ns_cbranch_scc0 .R152\nv_max_f32 v22, v20, v22\n"
"v_max_f32 v23, v21, v23\n.R152:\nAD 88,88,22\nAD 89,89,23\nSU 32,32,22\nSU 33,33,22\nSU 34,34,22\nSU 35,35,22\nSU 36,36,23\nSU 37,37,23\nSU 38,38,23\nSU 39,39,23\nSU 40,40,22\n"
"SU 41,41,22\nSU 42,42,22\nSU 43,43,22\nSU 44,44,23\nSU 45,45,23\nSU 46,46,23\nSU 47,47,23\nv_mov_b32 v90, v80\nv_mov_b32 v91, v81\nEX 80,32\nEX 81,33\ns_nop 0\nAD 84,80,81\n"
"CV 64,80,81\nEX 82,34\nEX 83,35\nAD 84,84,82\nAD 84,84,83\nCV 65,82,83\nEX 80,36\nEX 81,37\ns_nop 0\nAD 85,80,81\nCV 68,80,81\nEX 82,38\nEX 83,39\nAD 85,85,82\nAD 85,85,83\n"
"CV 69,82,83\nEX 80,40\nEX 81,41\nAD 84,84,80\nAD 84,84,81\nCV 66,80,81\nEX 82,42\nEX 83,43\nAD 84,84,82\nAD 84,84,83\nCV 67,82,83\nEX 80,44\nEX 81,45\nAD 85,85,80\nAD 85,85,81\n"
"CV 70,80,81\nEX 82,46\nEX 83,47\nAD 85,85,82\nAD 85,85,83\nCV 71,82,83\nv_mov_b32 v80, v90\nv_mov_b32 v81, v91\ns_bitcmp1_b32 s62, 1\ns_cbranch_scc1 .R153\nSU 48,48,22\n"
"SU 49,49,22\nSU 50,50,22\nSU 51,51,22\nSU 52,52,23\nSU 53,53,23\nSU 54,54,23\nSU 55,55,23\nSU 56,56,22\nSU 57,57,22\nSU 58,58,22\nSU 59,59,22\nSU 60,60,23\nSU 61,61,23\n"
"SU 62,62,23\nSU 63,63,23\nSU 4,4,22\nSU 5,5,22\nSU 6,6,22\nSU 7,7,22\nSU 8,8,23\nSU 9,9,23\nSU 10,10,23\nSU 11,11,23\nSU 12,12,22\nSU 13,13,22\nSU 14,14,22\nSU 15,15,22\n"
"SU 16,16,23\nSU 17,17,23\nSU 18,18,23\nSU 19,19,23\nEX 80,48\nEX 81,49\n.R153:\ns_bitcmp1_b32 s62, 0\ns_cbranch_scc0 .RS9\ns_nop 1\ns_setpc_b64 s[80:81]\n.RB13:\ns_nop 7\ns_nop 3\n"
"v_max3_f32 v22, v48, v49, v50\nv_max3_f32 v22, v22, v51, v56\nv_max3_f32 v22, v22, v57, v58\nv_max_f32 v22, v22, v59\nv_mov_b32 v91, v22\ns_nop 1\nv_permlane32_swap_b32 v22, v91\n"
"v_max_f32 v22, v22, v91\nv_mov_b32 v91, v22\ns_nop 1\nv_permlane16_swap_b32 v22, v91\nv_max_f32 v22, v22, v91\nv_ceil_f32 v22, v22\nv_max3_f32 v23, v52, v53, v54\n"
"v_max3_f32 v23, v23, v55, v60\nv_max3_f32 v23, v23, v61, v62\nv_max_f32 v23, v23, v63\nv_mov_b32 v91, v23\ns_nop 1\nv_permlane32_swap_b32 v23, v91\nv_max_f32 v23, v23, v91\n"
"v_mov_b32 v91, v23\ns_nop 1\nv_permlane16_swap_b32 v23, v91\nv_max_f32 v23, v23, v91\nv_ceil_f32 v23, v23\ns_bitcmp1_b32 s62, 0\ns_cbranch_scc0 .R156\n"
"v_cmp_lt_f32 vcc, 0xff000000, v22\nv_cndmask_b32 v22, 0, v22, vcc\nv_cmp_lt_f32 vcc, 0xff000000, v23\nv_cndmask_b32 v23, 0, v23, vcc\ns_branch .R157\n.R156:\n"
"v_max_f32 v22, 0, v22\nv_max_f32 v23, 0, v23\ns_bitcmp1_b32 s62, 3\ns_cbranch_scc0 .R157\nv_max_f32 v22, v20, v22\nv_max_f32 v23, v21, v23\n.R157:\nAD 88,88,22\nAD 89,89,23\n"
"SU 48,48,22\nSU 49,49,22\nSU 50,50,22\nSU 51,51,22\nSU 52,52,23\nSU 53,53,23\nSU 54,54,23\nSU 55,55,23\nSU 56,56,22\nSU 57,57,22\nSU 58,58,22\nSU 59,59,22\nSU 60,60,23\n"
"SU 61,61,23\nSU 62,62,23\nSU 63,63,23\nv_mov_b32 v90, v80\nv_mov_b32 v91, v81\nEX 80,48\nEX 81,49\ns_nop 0\nAD 84,80,81\nCV 72,80,81\nEX 82,50\nEX 83,51\nAD 84,84,82\nAD 84,84,83\n"
"CV 73,82,83\nEX 80,52\nEX 81,53\ns_nop 0\nAD 85,80,81\nCV 76,80,81\nEX 82,54\nEX 83,55\nAD 85,85,82\nAD 85,85,83\nCV 77,82,83\nEX 80,56\nEX 81,57\nAD 84,84,80\nAD 84,84,81\n"
"CV 74,80,81\nEX 82,58\nEX 83,59\nAD 84,84,82\nAD 84,84,83\nCV 75,82,83\nEX 80,60\nEX 81,61\nAD 85,85,80\nAD 85,85,81\nCV 78,80,81\nEX 82,62\nEX 83,63\nAD 85,85,82\nAD 85,85,83\n"
"CV 79,82,83\nv_mov_b32 v80, v90\nv_mov_b32 v81, v91\ns_bitcmp1_b32 s62, 1\ns_cbranch_scc1 .R158\nSU 32,32,22\nSU 33,33,22\nSU 34,34,22\nSU 35,35,22\nSU 36,36,23\nSU 37,37,23\n"
"SU 38,38,23\nSU 39,39,23\nSU 40,40,22\nSU 41,41,22\nSU 42,42,22\nSU 43,43,22\nSU 44,44,23\nSU 45,45,23\nSU 46,46,23\nSU 47,47,23\nSU 4,4,22\nSU 5,5,22\nSU 6,6,22\nSU 7,7,22\n"
"SU 8,8,23\nSU 9,9,23\nSU 10,10,23\nSU 11,11,23\nSU 12,12,22\nSU 13,13,22\nSU 14,14,22\nSU 15,15,22\nSU 16,16,23\nSU 17,17,23\nSU 18,18,23\nSU 19,19,23\nEX 80,32\nEX 81,33\n.R158:\n"
"s_bitcmp1_b32 s62, 0\ns_cbranch_scc0 .RS9\ns_nop 1\ns_setpc_b64 s[80:81]\n.RS9:\nv_exp_f32 v22, -v22\nv_exp_f32 v23, -v23\nMU 86,86,22\nMU 87,87,23\ns_bitcmp1_b32 s62, 2\n"
"s_cbranch_scc1 .R161\nAR 91,0\nMU 91,91,22\nAW 0,91\nAR 91,1\nMU 91,91,22\nAW 1,91\nAR 91,2\nMU 91,91,22\nAW 2,91\nAR 91,3\nMU 91,91,22\nAW 3,91\nAR 91,4\nMU 91,91,23\nAW 4,91\n"
"AR 91,5\nMU 91,91,23\nAW 5,91\nAR 91,6\nMU 91,91,23\nAW 6,91\nAR 91,7\nMU 91,91,23\nAW 7,91\nAR 91,8\nMU 91,91,22\nAW 8,91\nAR 91,9\nMU 91,91,22\nAW 9,91\nAR 91,10\nMU 91,91,22\n"
"AW 10,91\nAR 91,11\nMU 91,91,22\nAW 11,91\nAR 91,12\nMU 91,91,23\nAW 12,91\nAR 91,13\nMU 91,91,23\nAW 13,91\nAR 91,14\nMU 91,91,23\nAW 14,91\nAR 91,15\nMU 91,91,23\nAW 15,91\n"
"AR 91,16\nMU 91,91,22\nAW 16,91\nAR 91,17\nMU 91,91,22\nAW 17,91\nAR 91,18\nMU 91,91,22\nAW 18,91\nAR 91,19\nMU 91,91,22\nAW 19,91\nAR 91,20\nMU 91,91,23\nAW 20,91\nAR 91,21\n"
"MU 91,91,23\nAW 21,91\nAR 91,22\nMU 91,91,23\nAW 22,91\nAR 91,23\nMU 91,91,23\nAW 23,91\nAR 91,24\nMU 91,91,22\nAW 24,91\nAR 91,25\nMU 91,91,22\nAW 25,91\nAR 91,26\nMU 91,91,22\n"
"AW 26,91\nAR 91,27\nMU 91,91,22\nAW 27,91\nAR 91,28\nMU 91,91,23\nAW 28,91\nAR 91,29\nMU 91,91,23\nAW 29,91\nAR 91,30\nMU 91,91,23\nAW 30,91\nAR 91,31\nMU 91,91,23\nAW 31,91\n"
"AR 91,32\nMU 91,91,22\nAW 32,91\nAR 91,33\nMU 91,91,22\nAW 33,91\nAR 91,34\nMU 91,91,22\nAW 34,91\nAR 91,35\nMU 91,91,22\nAW 35,91\nAR 91,36\nMU 91,91,23\nAW 36,91\nAR 91,37\n"
"MU 91,91,23\nAW 37,91\nAR 91,38\nMU 91,91,23\nAW 38,91\nAR 91,39\nMU 91,91,23\nAW 39,91\nAR 91,40\nMU 91,91,22\nAW 40,91\nAR 91,41\nMU 91,91,22\nAW 41,91\nAR 91,42\nMU 91,91,22\n"
"AW 42,91\nAR 91,43\nMU 91,91,22\nAW 43,91\nAR 91,44\nMU 91,91,23\nAW 44,91\nAR 91,45\nMU 91,91,23\nAW 45,91\nAR 91,46\nMU 91,91,23\nAW 46,91\nAR 91,47\nMU 91,91,23\nAW 47,91\n"
"AR 91,48\nMU 91,91,22\nAW 48,91\nAR 91,49\nMU 91,91,22\nAW 49,91\nAR 91,50\nMU 91,91,22\nAW 50,91\nAR 91,51\nMU 91,91,22\nAW 51,91\nAR 91,52\nMU 91,91,23\nAW 52,91\nAR 91,53\n"
"MU 91,91,23\nAW 53,91\nAR 91,54\nMU 91,91,23\nAW 54,91\nAR 91,55\nMU 91,91,23\nAW 55,91\nAR 91,56\nMU 91,91,22\nAW 56,91\nAR 91,57\nMU 91,91,22\nAW 57,91\nAR 91,58\nMU 91,91,22\n"
"AW 58,91\nAR 91,59\nMU 91,91,22\nAW 59,91\nAR 91,60\nMU 91,91,23\nAW 60,91\nAR 91,61\nMU 91,91,23\nAW 61,91\nAR 91,62\nMU 91,91,23\nAW 62,91\nAR 91,63\nMU 91,91,23\nAW 63,91\n"
"AR 91,64\nMU 91,91,22\nAW 64,91\nAR 91,65\nMU 91,91,22\nAW 65,91\nAR 91,66\nMU 91,91,22\nAW 66,91\nAR 91,67\nMU 91,91,22\nAW 67,91\nAR 91,68\nMU 91,91,23\nAW 68,91\nAR 91,69\n"
"MU 91,91,23\nAW 69,91\nAR 91,70\nMU 91,91,23\nAW 70,91\nAR 91,71\nMU 91,91,23\nAW 71,91\nAR 91,72\nMU 91,91,22\nAW 72,91\nAR 91,73\nMU 91,91,22\nAW 73,91\nAR 91,74\nMU 91,91,22\n"
"AW 74,91\nAR 91,75\nMU 91,91,22\nAW 75,91\nAR 91,76\nMU 91,91,23\nAW 76,91\nAR 91,77\nMU 91,91,23\nAW 77,91\nAR 91,78\nMU 91,91,23\nAW 78,91\nAR 91,79\nMU 91,91,23\nAW 79,91\n"
"AR 91,80\nMU 91,91,22\nAW 80,91\nAR 91,81\nMU 91,91,22\nAW 81,91\nAR 91,82\nMU 91,91,22\nAW 82,91\nAR 91,83\nMU 91,91,22\nAW 83,91\nAR 91,84\nMU 91,91,23\nAW 84,91\nAR 91,85\n"
"MU 91,91,23\nAW 85,91\nAR 91,86\nMU 91,91,23\nAW 86,91\nAR 91,87\nMU 91,91,23\nAW 87,91\nAR 91,88\nMU 91,91,22\nAW 88,91\nAR 91,89\nMU 91,91,22\nAW 89,91\nAR 91,90\nMU 91,91,22\n"
"AW 90,91\nAR 91,91\nMU 91,91,22\nAW 91,91\nAR 91,92\nMU 91,91,23\nAW 92,91\nAR 91,93\nMU 91,91,23\nAW 93,91\nAR 91,94\nMU 91,91,23\nAW 94,91\nAR 91,95\nMU 91,91,23\nAW 95,91\n"
"AR 91,96\nMU 91,91,22\nAW 96,91\nAR 91,97\nMU 91,91,22\nAW 97,91\nAR 91,98\nMU 91,91,22\nAW 98,91\nAR 91,99\nMU 91,91,22\nAW 99,91\nAR 91,100\nMU 91,91,23\nAW 100,91\nAR 91,101\n"
"MU 91,91,23\nAW 101,91\nAR 91,102\nMU 91,91,23\nAW 102,91\nAR 91,103\nMU 91,91,23\nAW 103,91\nAR 91,104\nMU 91,91,22\nAW 104,91\nAR 91,105\nMU 91,91,22\nAW 105,91\nAR 91,106\n"
"MU 91,91,22\nAW 106,91\nAR 91,107\nMU 91,91,22\nAW 107,91\nAR 91,108\nMU 91,91,23\nAW 108,91\nAR 91,109\nMU 91,91,23\nAW 109,91\nAR 91,110\nMU 91,91,23\nAW 110,91\nAR 91,111\n"
"MU 91,91,23\nAW 111,91\nAR 91,112\nMU 91,91,22\nAW 112,91\nAR 91,113\nMU 91,91,22\nAW 113,91\nAR 91,114\nMU 91,91,22\nAW 114,91\nAR 91,115\nMU 91,91,22\nAW 115,91\nAR 91,116\n"
"MU 91,91,23\nAW 116,91\nAR 91,117\nMU 91,91,23\nAW 117,91\nAR 91,118\nMU 91,91,23\nAW 118,91\nAR 91,119\nMU 91,91,23\nAW 119,91\nAR 91,120\nMU 91,91,22\nAW 120,91\nAR 91,121\n"
"MU 91,91,22\nAW 121,91\nAR 91,122\nMU 91,91,22\nAW 122,91\nAR 91,123\nMU 91,91,22\nAW 123,91\nAR 91,124\nMU 91,91,23\nAW 124,91\nAR 91,125\nMU 91,91,23\nAW 125,91\nAR 91,126\n"
"MU 91,91,23\nAW 126,91\nAR 91,127\nMU 91,91,23\nAW 127,91\ns_nop 1\ns_setpc_b64 s[80:81]\n.R161:\nMU 128,128,22\nMU 129,129,22\nMU 130,130,22\nMU 131,131,22\nMU 132,132,23\n"
"MU 133,133,23\nMU 134,134,23\nMU 135,135,23\nMU 136,136,22\nMU 137,137,22\nMU 138,138,22\nMU 139,139,22\nMU 140,140,23\nMU 141,141,23\nMU 142,142,23\nMU 143,143,23\nMU 144,144,22\n"
"MU 145,145,22\nMU 146,146,22\nMU 147,147,22\nMU 148,148,23\nMU 149,149,23\nMU 150,150,23\nMU 151,151,23\nMU 152,152,22\nMU 153,153,22\nMU 154,154,22\nMU 155,155,22\nMU 156,156,23\n"
"MU 157,157,23\nMU 158,158,23\nMU 159,159,23\nMU 160,160,22\nMU 161,161,22\nMU 162,162,22\nMU 163,163,22\nMU 164,164,23\nMU 165,165,23\nMU 166,166,23\nMU 167,167,23\nMU 168,168,22\n"
"MU 169,169,22\nMU 170,170,22\nMU 171,171,22\nMU 172,172,23\nMU 173,173,23\nMU 174,174,23\nMU 175,175,23\nMU 176,176,22\nMU 177,177,22\nMU 178,178,22\nMU 179,179,22\nMU 180,180,23\n"
"MU 181,181,23\nMU 182,182,23\nMU 183,183,23\nMU 184,184,22\nMU 185,185,22\nMU 186,186,22\nMU 187,187,22\nMU 188,188,23\nMU 189,189,23\nMU 190,190,23\nMU 191,191,23\nMU 192,192,22\n"
"MU 193,193,22\nMU 194,194,22\nMU 195,195,22\nMU 196,196,23\nMU 197,197,23\nMU 198,198,23\nMU 199,199,23\nMU 200,200,22\nMU 201,201,22\nMU 202,202,22\nMU 203,203,22\nMU 204,204,23\n"
"MU 205,205,23\nMU 206,206,23\nMU 207,207,23\nMU 208,208,22\nMU 209,209,22\nMU 210,210,22\nMU 211,211,22\nMU 212,212,23\nMU 213,213,23\nMU 214,214,23\nMU 215,215,23\nMU 216,216,22\n"
"MU 217,217,22\nMU 218,218,22\nMU 219,219,22\nMU 220,220,23\nMU 221,221,23\nMU 222,222,23\nMU 223,223,23\nMU 224,224,22\nMU 225,225,22\nMU 226,226,22\nMU 227,227,22\nMU 228,228,23\n"
"MU 229,229,23\nMU 230,230,23\nMU 231,231,23\nMU 232,232,22\nMU 233,233,22\nMU 234,234,22\nMU 235,235,22\nMU 236,236,23\nMU 237,237,23\nMU 238,238,23\nMU 239,239,23\nMU 240,240,22\n"
"MU 241,241,22\nMU 242,242,22\nMU 243,243,22\nMU 244,244,23\nMU 245,245,23\nMU 246,246,23\nMU 247,247,23\nMU 248,248,22\nMU 249,249,22\nMU 250,250,22\nMU 251,251,22\nMU 252,252,23\n"
"MU 253,253,23\nMU 254,254,23\nMU 255,255,23\ns_nop 1\ns_setpc_b64 s[80:81]\n.SW8:\nv_mov_b32 v4, 0\nv_mov_b32 v5, 0\nv_mov_b32 v6, 0\nv_mov_b32 v7, 0\nv_mov_b32 v8, 0\n"
"v_mov_b32 v9, 0\nv_mov_b32 v10, 0\nv_mov_b32 v11, 0\nv_mov_b32 v12, 0\nv_mov_b32 v13, 0\nv_mov_b32 v14, 0\nv_mov_b32 v15, 0\nv_mov_b32 v16, 0\nv_mov_b32 v17, 0\nv_mov_b32 v18, 0\n"
"v_mov_b32 v19, 0\ns_add_u32 s99, s98, 1\ns_mov_b32 s94, s96\ns_add_u32 s95, s96, 65536\ns_or_b32 s2, s13, s11\ns_cmp_eq_u32 s2, 0\ns_cbranch_scc1 .E162\n.E163:\ns_nop 2\n"
"Cvaa 32,128,192,4\nRa 176,26,8192\nAD 84,84,81\nMvaa 32,132,200\nEX 82,58\nMvaa 32,136,208\nRa 180,26,9216\nCV 74,80,81\nMvaa 32,140,216\nEX 83,59\nCvaa 36,140,220,8\n"
"Ra 184,26,10240\nAD 84,84,82\nMvaa 36,136,212\nD1 1,16,46,3072\nMvaa 36,132,204\nRa 188,26,11264\nWL 8\nMvaa 36,128,196\ns_add_u32 m0, s89, 32768\ns_add_u32 s46, s86, s88\n"
"Mvaa 36,144,228\nRa 128,2,16384\nAD 84,84,83\nMvaa 36,148,236\nEX 80,60\nMvaa 36,152,244\nRa 132,2,17408\nCV 75,82,83\nMvaa 36,156,252\nEX 81,61\nMvaa 32,156,248\nRa 136,2,18432\n"
"AD 85,85,80\nMvaa 32,152,240\nD1 1,16,46,0\nMvaa 32,148,232\nRa 140,2,19456\nWL 8\nMvaa 32,144,224\nCvaa 40,160,192,12\nRa 144,2,20480\nAD 85,85,81\nMvaa 40,164,200\nEX 82,62\n"
"Mvaa 40,168,208\nRa 148,2,21504\nCV 78,80,81\nMvaa 40,172,216\nEX 83,63\nCvaa 44,172,220,16\nRa 152,2,22528\nAD 85,85,82\nMvaa 44,168,212\nD1 1,16,46,1024\nMvaa 44,164,204\n"
"Ra 156,2,23552\nWL 8\nMvaa 44,160,196\ns_add_u32 s88, s88, 32768\nMvaa 44,176,228\nRa 160,2,24576\nAD 85,85,83\nMvaa 44,180,236\nEX 80,32\nMvaa 44,184,244\nRa 164,2,25600\n"
"CV 79,82,83\nMvaa 44,188,252\nEX 81,33\nMvaa 40,188,248\nRa 168,2,26624\nv_max_f32 v90, v84, v85\nMvaa 40,184,240\nD1 1,16,46,2048\nv_cmp_lt_f32 vcc, s63, v90\nMvaa 40,180,232\n"
"Ra 172,2,27648\nWL 8\nMvaa 40,176,224\ns_cbranch_vccnz .T164\n.T165:\nAD 86,86,84\nMaav 0,128,72\nRa 176,2,28672\nAD 84,80,81\nMaav 4,128,76\nEX 82,34\nMaav 12,132,76\n"
"Ra 180,2,29696\nCV 64,80,81\nMaav 8,132,72\nEX 83,35\nMaav 16,136,72\nRa 184,2,30720\nAD 84,84,82\nMaav 20,136,76\nD1 1,16,46,3072\ns_add_u32 m0, s89, 49152\nMaav 28,140,76\n"
"Ra 188,2,31744\nMaav 24,140,72\ns_waitcnt vmcnt(13) lgkmcnt(8)\ns_barrier\nMaav 32,144,72\nRa 128,24,32768\nAD 84,84,83\nAR 98,0\nAR 99,1\nCV 100,98,99\nMaav 36,144,76\nEX 80,36\n"
"AR 98,2\nAR 99,3\nCV 101,98,99\nMaav 44,148,76\nRa 132,24,33792\nCV 65,82,83\nAR 98,8\nAR 99,9\nCV 102,98,99\nAR 98,10\nMaav 40,148,72\nEX 81,37\nAR 99,11\nCV 103,98,99\n"
"Maav 48,152,72\nRa 136,24,34816\nAD 87,87,85\ns_add_u32 s5, s66, 0\nSv 100,1,16,5,0\nAR 98,4\nAR 99,5\nMaav 52,152,76\ns_add_u32 s46, s87, s88\nD1 1,16,46,0\nv_mov_b32 v96, v86\n"
"CV 104,98,99\nAR 98,6\nMaav 60,156,76\nRa 140,24,35840\nWL 8\nMaav 56,156,72\ns_cmp_eq_u32 s99, 1\ns_cselect_b32 s86, s94, s86\nv_mov_b32 v97, v87\nAR 99,7\nCV 105,98,99\n"
"Maav 64,160,72\nRa 144,26,32768\nAD 85,80,81\nv_permlane32_swap_b32 v86, v96\nAR 98,12\nAR 99,13\nCV 106,98,99\nMaav 68,160,76\nEX 82,38\nAR 98,14\nAR 99,15\nCV 107,98,99\n"
"Maav 76,164,76\nRa 148,26,33792\nCV 68,80,81\nv_permlane32_swap_b32 v87, v97\ns_add_u32 s5, s66, 0\nSv 104,1,16,5,1024\nMaav 72,164,72\nEX 83,39\nAR 98,16\nAR 99,17\nCV 108,98,99\n"
"AR 98,18\nMaav 80,168,72\nRa 152,26,34816\nAD 85,85,82\nAD 86,86,96\nAR 99,19\nCV 109,98,99\nMaav 84,168,76\nD1 1,16,46,1024\nAD 87,87,97\nv_mov_b32 v96, v86\nAR 98,24\nAR 99,25\n"
"CV 110,98,99\nMaav 92,172,76\nRa 156,26,35840\nWL 8\nMaav 88,172,72\ns_cmp_eq_u32 s99, s93\ns_cbranch_scc1 .E166\n.E167:\ns_nop 2\nMaav 96,176,72\nRa 160,24,40960\nAD 85,85,83\n"
"v_mov_b32 v97, v87\nAR 98,26\nAR 99,27\nMaav 100,176,76\nEX 80,40\nCV 111,98,99\ns_add_u32 s5, s66, 0\nSv 108,1,16,5,2048\nMaav 108,180,76\nRa 164,24,41984\nCV 69,82,83\n"
"v_permlane16_swap_b32 v86, v96\nAR 98,20\nAR 99,21\nCV 100,98,99\nMaav 104,180,72\nEX 81,41\nAR 98,22\nAR 99,23\nCV 101,98,99\nMaav 112,184,72\nRa 168,24,43008\nAD 84,84,80\n"
"v_permlane16_swap_b32 v87, v97\nAR 98,28\nAR 99,29\nCV 102,98,99\nMaav 116,184,76\nD1 1,16,46,2048\nAD 86,86,96\nAD 87,87,97\nAR 98,30\nAR 99,31\nMaav 124,188,76\nRa 172,24,44032\n"
"WL 8\nMaav 120,188,72\ns_xor_b32 s89, s89, 0x10000\nv_cmp_gt_u32 vcc, 0x100, v1\nv_mov_b32 v94, s15\nv_mov_b32 v95, s15\nv_cndmask_b32 v92, v89, v88, vcc\n"
"v_cndmask_b32 v93, v87, v86, vcc\nCvaa 48,128,192,4\nRa 176,26,40960\nAD 84,84,81\nv_mov_b32 v86, 0\nCV 103,98,99\ns_add_u32 s5, s66, 0\nSv 100,1,16,5,3072\nMvaa 48,132,200\n"
"EX 82,42\nAR 98,32\nAR 99,33\nCV 104,98,99\nMvaa 48,136,208\nRa 180,26,41984\nCV 66,80,81\nv_mov_b32 v87, 0\nAR 98,34\nAR 99,35\nMvaa 48,140,216\nEX 83,43\nCV 105,98,99\nAR 98,40\n"
"AR 99,41\nCV 106,98,99\nCvaa 52,140,220,8\nRa 184,26,43008\nAD 84,84,82\nv_mov_b32 v88, 0\nAR 98,42\nAR 99,43\nMvaa 52,136,212\nD1 1,16,46,3072\nv_mov_b32 v89, 0\n"
"s_mov_b32 s64, 0x2b800000\nCV 107,98,99\ns_add_u32 s5, s66, 4096\nSv 104,1,16,5,0\nMvaa 52,132,204\nRa 188,26,44032\nWL 8\nMvaa 52,128,196\ns_add_u32 m0, s89, 0\n"
"s_add_u32 s46, s86, s88\nAR 98,36\nAR 99,37\nCV 108,98,99\nAR 98,38\nMvaa 52,144,228\nRa 128,2,49152\nAD 84,84,83\nAR 99,39\nCV 109,98,99\nAR 98,44\nMvaa 52,148,236\nEX 80,44\n"
"AR 99,45\nCV 110,98,99\nAR 98,46\nMvaa 52,152,244\nRa 132,2,50176\nCV 67,82,83\nAR 99,47\nCV 111,98,99\ns_add_u32 s5, s66, 4096\nSv 108,1,16,5,1024\nMvaa 52,156,252\nEX 81,45\n"
"AR 98,48\nAR 99,49\nCV 100,98,99\nMvaa 48,156,248\nRa 136,2,51200\nAD 85,85,80\nAR 98,50\nAR 99,51\nCV 101,98,99\nAR 98,56\nMvaa 48,152,240\nD1 1,16,46,0\n"
"v_xor_b32 v24, 0x10000, v24\nAR 99,57\nCV 102,98,99\nAR 98,58\nMvaa 48,148,232\nRa 140,2,52224\nWL 8\nMvaa 48,144,224\nAR 99,59\nCV 103,98,99\ns_add_u32 s5, s66, 4096\n"
"Sv 100,1,16,5,2048\nAR 98,52\nCvaa 56,160,192,12\nRa 144,2,53248\nAD 85,85,81\nAR 99,53\nCV 104,98,99\nAR 98,54\nAR 99,55\nMvaa 56,164,200\nEX 82,46\nCV 105,98,99\nAR 98,60\n"
"AR 99,61\nMvaa 56,168,208\nRa 148,2,54272\nCV 70,80,81\nCV 106,98,99\nAR 98,62\nAR 99,63\nMvaa 56,172,216\nEX 83,47\nCV 107,98,99\ns_add_u32 s5, s66, 4096\nSv 104,1,16,5,3072\n"
"AR 98,64\nCvaa 60,172,220,16\nRa 152,2,55296\nAD 85,85,82\nAR 99,65\nCV 108,98,99\nAR 98,66\nMvaa 60,168,212\nD1 1,16,46,1024\nv_xor_b32 v26, 0x10000, v26\nAR 99,67\nCV 109,98,99\n"
"AR 98,72\nAR 99,73\nMvaa 60,164,204\nRa 156,2,56320\nWL 8\nMvaa 60,160,196\ns_add_u32 s88, s88, 32768\nCV 110,98,99\nAR 98,74\nAR 99,75\nCV 111,98,99\nMvaa 60,176,228\n"
"Ra 160,2,57344\nAD 85,85,83\ns_add_u32 s5, s66, 16384\nSv 108,1,16,5,0\nAR 98,68\nMvaa 60,180,236\nEX 80,48\nAR 99,69\nCV 100,98,99\nAR 98,70\nMvaa 60,184,244\nRa 164,2,58368\n"
"CV 71,82,83\nAR 99,71\nCV 101,98,99\nAR 98,76\nAR 99,77\nMvaa 60,188,252\nEX 81,49\nCV 102,98,99\nAR 98,78\nAR 99,79\nMvaa 56,188,248\nRa 168,2,59392\nv_max_f32 v90, v84, v85\n"
"CV 103,98,99\ns_add_u32 s5, s66, 16384\nSv 100,1,16,5,1024\nAR 98,80\nMvaa 56,184,240\nD1 1,16,46,2048\nv_cmp_lt_f32 vcc, s63, v90\nMvaa 56,180,232\nRa 172,2,60416\nWL 8\n"
"Mvaa 56,176,224\ns_cbranch_vccnz .T168\n.T169:\nv_mov_b32 v20, v84\nv_mov_b32 v21, v85\nv_mov_b32 v22, v20\nv_mov_b32 v23, v21\ns_nop 0\nv_permlane32_swap_b32 v20, v22\n"
"v_permlane32_swap_b32 v21, v23\nAD 20,20,22\nAD 21,21,23\nv_mov_b32 v22, v20\nv_mov_b32 v23, v21\ns_nop 0\nv_permlane16_swap_b32 v20, v22\nv_permlane16_swap_b32 v21, v23\n"
"AD 20,20,22\nAD 21,21,23\nv_min_f32 v90, v20, v21\nv_cmp_gt_f32 vcc, s64, v90\ns_nop 0\ns_cbranch_vccnz .T170\n.T171:\nAD 86,86,84\nZvav 128,128,64\nRa 176,2,61440\nAD 84,80,81\n"
"AR 99,81\nCV 104,98,99\nAR 98,82\nAR 99,83\nZvav 132,128,68\nEX 82,50\nCV 105,98,99\nAR 98,88\nAR 99,89\nZvav 140,132,68\nRa 180,2,62464\nCV 72,80,81\nCV 106,98,99\nAR 98,90\n"
"AR 99,91\nZvav 136,132,64\nEX 83,51\nCV 107,98,99\ns_add_u32 s5, s66, 16384\nSv 104,1,16,5,2048\nAR 98,84\nZvav 144,136,64\nRa 184,2,63488\nAD 84,84,82\nAR 99,85\nCV 108,98,99\n"
"AR 98,86\nZvav 148,136,68\nD1 1,16,46,3072\ns_add_u32 m0, s89, 16384\nAR 99,87\nCV 109,98,99\nAR 98,92\nAR 99,93\nZvav 156,140,68\nRa 188,2,64512\nZvav 152,140,64\n"
"s_waitcnt vmcnt(23) lgkmcnt(8)\ns_barrier\nZvav 160,144,64\nRa 128,24,0\nAD 84,84,83\nCV 110,98,99\nAR 98,94\nAR 99,95\nZvav 164,144,68\nEX 80,52\nCV 111,98,99\n"
"s_add_u32 s5, s66, 16384\nSv 108,1,16,5,3072\nZvav 172,148,68\nRa 132,24,1024\nCV 73,82,83\nAR 98,96\nAR 99,97\nCV 100,98,99\nAR 98,98\nZvav 168,148,64\nEX 81,53\nAR 99,99\n"
"CV 101,98,99\nAR 98,104\nZvav 176,152,64\nRa 136,24,2048\nAD 87,87,85\nAR 99,105\nCV 102,98,99\nAR 98,106\nAR 99,107\nZvav 180,152,68\ns_add_u32 s46, s87, s88\nD1 1,16,46,0\n"
"CV 103,98,99\ns_add_u32 s5, s66, 20480\nSv 100,1,16,5,0\nZvav 188,156,68\nRa 140,24,3072\nWL 8\nZvav 184,156,64\ns_sub_u32 s90, s90, 1\ns_cbranch_scc1 .E172\n.E173:\n"
"Zvav 192,160,64\nRa 144,26,0\nAD 85,80,81\nAR 98,100\nAR 99,101\nCV 104,98,99\nAR 98,102\nZvav 196,160,68\nEX 82,54\nAR 99,103\nCV 105,98,99\nAR 98,108\nZvav 204,164,68\n"
"Ra 148,26,1024\nCV 76,80,81\nAR 99,109\nCV 106,98,99\nAR 98,110\nZvav 200,164,64\nEX 83,55\nAR 99,111\nCV 107,98,99\ns_add_u32 s5, s66, 20480\nSv 104,1,16,5,1024\nZvav 208,168,64\n"
"Ra 152,26,2048\nAD 85,85,82\nAR 98,112\nAR 99,113\nCV 108,98,99\nZvav 212,168,68\nD1 1,16,46,1024\nv_xor_b32 v2, 0x10000, v2\nAR 98,114\nAR 99,115\nCV 109,98,99\nAR 98,120\n"
"Zvav 220,172,68\nRa 156,26,3072\nWL 8\nZvav 216,172,64\ns_cmp_eq_u32 s99, s92\ns_cbranch_scc1 .E174\n.E175:\ns_nop 2\nZvav 224,176,64\nRa 160,24,8192\nAD 85,85,83\nAR 99,121\n"
"CV 110,98,99\nAR 98,122\nZvav 228,176,68\nEX 80,56\nAR 99,123\nCV 111,98,99\nZvav 236,180,68\nRa 164,24,9216\nCV 77,82,83\ns_add_u32 s5, s66, 20480\nSv 108,1,16,5,2048\nAR 98,116\n"
"AR 99,117\nZvav 232,180,64\nEX 81,57\nCV 100,98,99\nAR 98,118\nAR 99,119\nZvav 240,184,64\nRa 168,24,10240\nAD 84,84,80\nCV 101,98,99\nAR 98,124\nAR 99,125\nCV 102,98,99\n"
"Zvav 244,184,68\nD1 1,16,46,2048\nAR 98,126\nAR 99,127\nCV 103,98,99\nZvav 252,188,68\nRa 172,24,11264\nWL 8\nZvav 248,188,64\ns_add_u32 s5, s66, 20480\nSv 100,1,16,5,3072\n"
"s_sub_u32 s99, s99, 1\n.p2align 6\n.A176:\nCvaa 32,128,192,4\nRa 176,26,8192\nAD 84,84,81\nMvaa 32,132,200\nEX 82,58\nMvaa 32,136,208\nRa 180,26,9216\nCV 74,80,81\n"
"Mvaa 32,140,216\nEX 83,59\nCvaa 36,140,220,8\nRa 184,26,10240\nAD 84,84,82\nMvaa 36,136,212\nD1 1,16,46,3072\nMvaa 36,132,204\nRa 188,26,11264\nWL 8\nMvaa 36,128,196\n"
"s_add_u32 m0, s89, 32768\ns_add_u32 s46, s86, s88\nMvaa 36,144,228\nRa 128,2,16384\nAD 84,84,83\nMvaa 36,148,236\nEX 80,60\nMvaa 36,152,244\nRa 132,2,17408\nCV 75,82,83\n"
"Sv 112,1,16,49,0\nMvaa 36,156,252\nEX 81,61\nMvaa 32,156,248\nRa 136,2,18432\nAD 85,85,80\nMvaa 32,152,240\nD1 1,16,46,0\nMvaa 32,148,232\nRa 140,2,19456\nWL 8\nMvaa 32,144,224\n"
"s_cmp_eq_u32 s99, 0\ns_cselect_b32 s87, s95, s87\nCvaa 40,160,192,12\nRa 144,2,20480\nAD 85,85,81\nMvaa 40,164,200\nEX 82,62\nMvaa 40,168,208\nRa 148,2,21504\nCV 78,80,81\n"
"Mvaa 40,172,216\nEX 83,63\nCvaa 44,172,220,16\nRa 152,2,22528\nAD 85,85,82\nMvaa 44,168,212\nD1 1,16,46,1024\nMvaa 44,164,204\nRa 156,2,23552\nWL 8\nMvaa 44,160,196\n"
"s_add_u32 s88, s88, 32768\nMvaa 44,176,228\nRa 160,2,24576\nAD 85,85,83\nMvaa 44,180,236\nEX 80,32\nMvaa 44,184,244\nRa 164,2,25600\nCV 79,82,83\nMvaa 44,188,252\nEX 81,33\n"
"Mvaa 40,188,248\nRa 168,2,26624\nv_max_f32 v90, v84, v85\nMvaa 40,184,240\nD1 1,16,46,2048\nv_cmp_lt_f32 vcc, s63, v90\nMvaa 40,180,232\nRa 172,2,27648\nWL 8\nMvaa 40,176,224\n"
"s_cbranch_vccnz .T177\n.T178:\nAD 86,86,84\nMvav 128,128,72\nRa 176,2,28672\nAD 84,80,81\nMvav 132,128,76\nEX 82,34\nMvav 140,132,76\nRa 180,2,29696\nCV 64,80,81\nMvav 136,132,72\n"
"EX 83,35\nMvav 144,136,72\nRa 184,2,30720\nAD 84,84,82\nMvav 148,136,76\nD1 1,16,46,3072\ns_add_u32 m0, s89, 49152\nMvav 156,140,76\nRa 188,2,31744\nMvav 152,140,72\n"
"s_waitcnt vmcnt(14) lgkmcnt(8)\ns_barrier\nMvav 160,144,72\nRa 128,24,32768\nAD 84,84,83\nMvav 164,144,76\nEX 80,36\nMvav 172,148,76\nRa 132,24,33792\nCV 65,82,83\n"
"Mvav 168,148,72\nEX 81,37\nMvav 176,152,72\nRa 136,24,34816\nAD 87,87,85\nMvav 180,152,76\ns_add_u32 s46, s87, s88\nD1 1,16,46,0\nMvav 188,156,76\nRa 140,24,35840\nWL 8\n"
"Mvav 184,156,72\ns_cmp_eq_u32 s99, 1\ns_cselect_b32 s86, s94, s86\nMvav 192,160,72\nRa 144,26,32768\nAD 85,80,81\nMvav 196,160,76\nEX 82,38\nMvav 204,164,76\nRa 148,26,33792\n"
"CV 68,80,81\nMvav 200,164,72\nEX 83,39\nMvav 208,168,72\nRa 152,26,34816\nAD 85,85,82\nMvav 212,168,76\nD1 1,16,46,1024\nMvav 220,172,76\nRa 156,26,35840\nWL 8\nMvav 216,172,72\n"
"s_cmp_eq_u32 s99, s93\ns_cbranch_scc1 .E179\n.E180:\ns_nop 2\nMvav 224,176,72\nRa 160,24,40960\nAD 85,85,83\nMvav 228,176,76\nEX 80,40\nMvav 236,180,76\nRa 164,24,41984\n"
"CV 69,82,83\nMvav 232,180,72\nEX 81,41\nMvav 240,184,72\nRa 168,24,43008\nAD 84,84,80\nMvav 244,184,76\nD1 1,16,46,2048\nMvav 252,188,76\nRa 172,24,44032\nWL 8\nMvav 248,188,72\n"
"s_xor_b32 s89, s89, 0x10000\nCvaa 48,128,192,4\nRa 176,26,40960\nAD 84,84,81\nMvaa 48,132,200\nEX 82,42\nMvaa 48,136,208\nRa 180,26,41984\nCV 66,80,81\nMvaa 48,140,216\nEX 83,43\n"
"Cvaa 52,140,220,8\nRa 184,26,43008\nAD 84,84,82\nMvaa 52,136,212\nD1 1,16,46,3072\nMvaa 52,132,204\nRa 188,26,44032\nWL 8\nMvaa 52,128,196\ns_add_u32 m0, s89, 0\n"
"s_add_u32 s46, s86, s88\nMvaa 52,144,228\nRa 128,2,49152\nAD 84,84,83\nMvaa 52,148,236\nEX 80,44\nMvaa 52,152,244\nRa 132,2,50176\nCV 67,82,83\nSv 112,1,16,49,1024\n"
"Mvaa 52,156,252\nEX 81,45\nMvaa 48,156,248\nRa 136,2,51200\nAD 85,85,80\nMvaa 48,152,240\nD1 1,16,46,0\nv_xor_b32 v24, 0x10000, v24\nMvaa 48,148,232\nRa 140,2,52224\nWL 8\n"
"Mvaa 48,144,224\ns_cmp_eq_u32 s99, s98\ns_cbranch_scc1 .E181\n.E182:\nCvaa 56,160,192,12\nRa 144,2,53248\nAD 85,85,81\nMvaa 56,164,200\nEX 82,46\nMvaa 56,168,208\nRa 148,2,54272\n"
"CV 70,80,81\nMvaa 56,172,216\nEX 83,47\nCvaa 60,172,220,16\nRa 152,2,55296\nAD 85,85,82\nMvaa 60,168,212\nD1 1,16,46,1024\nv_xor_b32 v26, 0x10000, v26\nMvaa 60,164,204\n"
"Ra 156,2,56320\nWL 8\nMvaa 60,160,196\ns_add_u32 s88, s88, 32768\nMvaa 60,176,228\nRa 160,2,57344\nAD 85,85,83\nMvaa 60,180,236\nEX 80,48\nMvaa 60,184,244\nRa 164,2,58368\n"
"CV 71,82,83\nMvaa 60,188,252\nEX 81,49\nMvaa 56,188,248\nRa 168,2,59392\nv_max_f32 v90, v84, v85\nMvaa 56,184,240\nD1 1,16,46,2048\nv_cmp_lt_f32 vcc, s63, v90\nMvaa 56,180,232\n"
"Ra 172,2,60416\nWL 8\nMvaa 56,176,224\ns_cbranch_vccnz .T183\n.T184:\nAD 86,86,84\nMvav 128,128,64\nRa 176,2,61440\nAD 84,80,81\nMvav 132,128,68\nEX 82,50\nMvav 140,132,68\n"
"Ra 180,2,62464\nCV 72,80,81\nMvav 136,132,64\nEX 83,51\nMvav 144,136,64\nRa 184,2,63488\nAD 84,84,82\nMvav 148,136,68\nD1 1,16,46,3072\ns_add_u32 m0, s89, 16384\nMvav 156,140,68\n"
"Ra 188,2,64512\nMvav 152,140,64\ns_waitcnt vmcnt(14) lgkmcnt(8)\ns_barrier\nMvav 160,144,64\nRa 128,24,0\nAD 84,84,83\nMvav 164,144,68\nEX 80,52\nMvav 172,148,68\nRa 132,24,1024\n"
"CV 73,82,83\nMvav 168,148,64\nEX 81,53\nMvav 176,152,64\nRa 136,24,2048\nAD 87,87,85\nMvav 180,152,68\ns_add_u32 s46, s87, s88\nD1 1,16,46,0\nMvav 188,156,68\nRa 140,24,3072\n"
"WL 8\nMvav 184,156,64\ns_sub_u32 s90, s90, 1\ns_cbranch_scc1 .E185\n.E186:\nMvav 192,160,64\nRa 144,26,0\nAD 85,80,81\nMvav 196,160,68\nEX 82,54\nMvav 204,164,68\nRa 148,26,1024\n"
"CV 76,80,81\nMvav 200,164,64\nEX 83,55\nMvav 208,168,64\nRa 152,26,2048\nAD 85,85,82\nMvav 212,168,68\nD1 1,16,46,1024\nv_xor_b32 v2, 0x10000, v2\nMvav 220,172,68\nRa 156,26,3072\n"
"WL 8\nMvav 216,172,64\ns_cmp_eq_u32 s99, s92\ns_cbranch_scc1 .E187\n.E188:\ns_nop 2\nMvav 224,176,64\nRa 160,24,8192\nAD 85,85,83\nMvav 228,176,68\nEX 80,56\nMvav 236,180,68\n"
"Ra 164,24,9216\nCV 77,82,83\nMvav 232,180,64\nEX 81,57\nMvav 240,184,64\nRa 168,24,10240\nAD 84,84,80\nMvav 244,184,68\ns_add_u32 s49, s49, 131072\nD1 1,16,46,2048\n"
"Mvav 252,188,68\nRa 172,24,11264\nWL 8\nMvav 248,188,64\ns_sub_u32 s99, s99, 1\ns_cbranch_scc0 .A176\nAD 84,84,81\nEX 82,58\nCV 74,80,81\nEX 83,59\nAD 84,84,82\nD1 1,16,46,3072\n"
"s_add_u32 m0, s89, 32768\ns_add_u32 s46, s86, s88\nRa 128,2,16384\nAD 84,84,83\nv_xor_b32 v3, 0x10000, v2\nEX 80,60\nv_add_u32 v0, 0x20000, v1\nRa 132,2,17408\nCV 75,82,83\n"
"EX 81,61\ns_sub_u32 s94, s49, 1966080\nRa 136,2,18432\nAD 85,85,80\nD1 1,16,46,0\ns_add_u32 s95, s94, 1048576\ns_xor_b32 s92, s94, 0x202000\nRa 140,2,19456\n"
"s_add_u32 s93, s96, 983040\ns_xor_b32 s93, s93, 0x202000\ns_lshl_b32 s89, s11, 12\nRa 144,2,20480\nAD 85,85,81\ns_mov_b32 s36, s68\ns_and_b32 s37, s69, 0xffff\n"
"s_mov_b32 s38, 0x1c0000\nEX 82,62\ns_mov_b32 s39, 0x20000\ns_and_b32 s2, s74, 15\nRa 148,2,21504\nCV 78,80,81\ns_mul_i32 s3, s73, 14\ns_add_u32 s2, s2, s3\nEX 83,63\n"
"s_lshl2_add_u32 s2, s2, s11\ns_mul_i32 s3, s2, 4682\nRa 152,2,22528\nAD 85,85,82\ns_lshr_b32 s3, s3, 16\ns_mul_i32 s91, s3, 14\ns_sub_u32 s91, s2, s91\nD1 1,16,46,1024\n"
"s_lshl_b32 s90, s3, 5\ns_bfe_u32 s6, s3, 0x30002\ns_xor_b32 s60, s12, 1\nRa 156,2,23552\ns_lshl4_add_u32 s60, s60, s13\ns_lshl4_add_u32 s60, s60, 0xc049480\ns_cmp_gt_u32 s91, 6\n"
"s_cselect_b32 s2, 1179648, 0\ns_cselect_b32 s1, 16, 0\nRa 160,2,24576\nAD 85,85,83\ns_add_u32 s6, s6, s1\ns_lshl4_add_u32 s6, s6, 0xc049480\ns_and_b32 s1, s3, 28\n"
"s_add_u32 s3, s3, s1\ns_lshl_b32 s3, s3, 11\ns_add_u32 s2, s2, s3\nRa 164,2,25600\nCV 79,82,83\ns_lshl_b32 s3, s91, 17\ns_add_u32 s2, s2, s3\ns_add_u32 s98, s2, 0x1100000\n"
"s_lshl_b32 s2, s91, 7\ns_add_u32 s0, s2, 0xc04b800\ns_mul_i32 s2, s90, 896\ns_lshl_b32 s3, s91, 6\nRa 168,2,26624\nv_max_f32 v90, v84, v85\ns_add_u32 s91, s2, s3\n"
"s_add_u32 s1, s91, 14336\nD1 1,16,46,2048\nv_cmp_lt_f32 vcc, s63, v90\nRa 172,2,27648\nWL 8\ns_cbranch_vccnz .T189\n.T190:\nAD 86,86,84\nAD 87,87,85\nMvav 128,128,72\n"
"Ra 176,2,28672\nv_mov_b32 v84, v86\nv_mov_b32 v85, v87\nMvav 132,128,76\nv_permlane32_swap_b32 v86, v84\nv_permlane32_swap_b32 v87, v85\nMvav 140,132,76\nRa 180,2,29696\n"
"AD 86,86,84\nMvav 136,132,72\nAD 87,87,85\nv_mov_b32 v84, v86\nMvav 144,136,72\nRa 184,2,30720\nv_mov_b32 v85, v87\nMvav 148,136,76\nD1 1,16,46,3072\n"
"v_permlane16_swap_b32 v86, v84\nMvav 156,140,76\nRa 188,2,31744\nWL 8\nMvav 152,140,72\nv_permlane16_swap_b32 v87, v85\nAD 86,86,84\nAD 87,87,85\nMvav 160,144,72\nRa 128,2,0\n"
"Mvav 164,144,76\nv_rcp_f32 v86, v86\nMvav 172,148,76\nRa 132,2,1024\nv_rcp_f32 v87, v87\nMvav 168,148,72\ns_add_u32 m0, s89, 131072\ns_add_u32 s46, s93, 0\nMU 128,128,86\n"
"MU 129,129,86\nMvav 176,152,72\nRa 136,2,2048\nCV 96,128,129\nMU 130,130,86\nMU 131,131,86\nMvav 180,152,76\nD1 1,16,46,0\nCV 97,130,131\nMU 132,132,87\nMU 133,133,87\n"
"Mvav 188,156,76\nRa 140,2,3072\nWL 8\nMvav 184,156,72\nCV 100,132,133\nMU 134,134,87\nMU 135,135,87\nCV 101,134,135\nMvav 192,160,72\nRa 144,2,4096\nMU 136,136,86\nMU 137,137,86\n"
"CV 98,136,137\nMvav 196,160,76\nMU 138,138,86\nMU 139,139,86\nCV 99,138,139\nMvav 204,164,76\nRa 148,2,5120\ns_add_u32 s5, s94, 0\nSv 96,1,16,5,0\nMU 140,140,87\nMvav 200,164,72\n"
"MU 141,141,87\nCV 102,140,141\nMU 142,142,87\nMU 143,143,87\nMvav 208,168,72\nRa 152,2,6144\nCV 103,142,143\nSv 100,1,16,5,1024\nMU 144,144,86\nMvav 212,168,76\nD1 1,16,46,1024\n"
"MU 145,145,86\nCV 104,144,145\nMU 146,146,86\nMvav 220,172,76\nRa 156,2,7168\nWL 8\nMvav 216,172,72\nMU 147,147,86\nCV 105,146,147\nMU 148,148,87\nMU 149,149,87\nMvav 224,176,72\n"
"Ra 160,2,8192\nCV 108,148,149\nMU 150,150,87\nMU 151,151,87\nMvav 228,176,76\nCV 109,150,151\nMU 152,152,86\nMU 153,153,86\nMvav 236,180,76\nRa 164,2,9216\nCV 106,152,153\n"
"MU 154,154,86\nMU 155,155,86\nMvav 232,180,72\nCV 107,154,155\ns_add_u32 s5, s94, 131072\nSv 104,1,16,5,0\nMU 156,156,87\nMvav 240,184,72\nRa 168,2,10240\nMU 157,157,87\n"
"CV 110,156,157\nMU 158,158,87\nMvav 244,184,76\nD1 1,16,46,2048\nMU 159,159,87\nCV 111,158,159\nSv 108,1,16,5,1024\nMvav 252,188,76\nRa 172,2,11264\nWL 8\nMvav 248,188,72\n"
"MU 160,160,86\nMU 161,161,86\nZaav 0,128,96\nRa 176,2,12288\nCV 112,160,161\nMU 162,162,86\nMaav 0,132,104\nMU 163,163,86\nCV 113,162,163\ns_and_b32 s63, s72, 0x2000\n"
"v_xor_b32 v25, s63, v25\nZaav 4,132,108\nRa 180,2,13312\nMU 164,164,87\nMaav 4,128,100\nMU 165,165,87\nCV 116,164,165\nZaav 12,136,100\nRa 184,2,14336\nMU 166,166,87\n"
"MU 167,167,87\nMaav 12,140,108\nD1 1,16,46,3072\nCV 117,166,167\nZaav 8,140,104\nRa 188,2,15360\nMaav 8,136,96\ns_waitcnt vmcnt(20) lgkmcnt(8)\ns_barrier\nZaav 16,144,96\n"
"Ra 128,2,32768\nMU 168,168,86\nMU 169,169,86\nv_lshrrev_b32 v88, 8, v1\nv_lshlrev_b32 v89, 5, v88\nMaav 16,148,104\nCV 114,168,169\nMU 170,170,86\n"
"buffer_load_dwordx4 v[64:67], v89, s[16:19], s0 offen offset:0 sc0 sc1\ns_add_u32 s54, s54, 4096\nbuffer_store_dword v25, off, s[16:19], s54 offset:0 sc0 sc1\nZaav 20,148,108\n"
"Ra 132,2,33792\nMU 171,171,86\nMaav 20,144,100\ns_add_u32 m0, s89, 49152\ns_add_u32 s46, s96, 1179648\nZaav 28,152,100\nRa 136,2,34816\nCV 115,170,171\ns_add_u32 s5, s94, 262144\n"
"Maav 28,156,108\nD1 1,16,46,0\nSv 112,1,16,5,0\nbuffer_load_dwordx4 v[68:71], v89, s[16:19], s0 offen offset:16 sc0 sc1\nZaav 24,156,104\nRa 140,2,35840\nWL 8\nMaav 24,152,96\n"
"MU 172,172,87\nMU 173,173,87\nZaav 32,160,96\nRa 144,2,36864\nCV 118,172,173\nMU 174,174,87\nMaav 32,164,104\nMU 175,175,87\nCV 119,174,175\n"
"buffer_load_dwordx4 v[72:75], v89, s[16:19], s0 offen offset:1792 sc0 sc1\nZaav 36,164,108\nRa 148,2,37888\nSv 116,1,16,5,1024\nMaav 36,160,100\nMU 176,176,86\nMU 177,177,86\n"
"Zaav 44,168,100\nRa 152,2,38912\nCV 120,176,177\nMU 178,178,86\nMaav 44,172,108\nD1 1,16,46,1024\nMU 179,179,86\n"
"buffer_load_dwordx4 v[76:79], v89, s[16:19], s0 offen offset:1808 sc0 sc1\nZaav 40,172,104\nRa 156,2,39936\nWL 8\nMaav 40,168,96\nCV 121,178,179\nMU 180,180,87\nZaav 48,176,96\n"
"Ra 160,2,40960\nMU 181,181,87\nCV 124,180,181\nMaav 48,180,104\nMU 182,182,87\nMU 183,183,87\nv_bfe_u32 v90, v1, 4, 4\nv_mul_u32_u24 v91, 896, v90\n"
"v_lshl_add_u32 v91, v88, 4, v91\nZaav 52,180,108\nRa 164,2,41984\nCV 125,182,183\nbuffer_load_dwordx4 v[12:15], v91, s[28:31], s91 offen offset:0\nMaav 52,176,100\nMU 184,184,86\n"
"MU 185,185,86\nZaav 60,184,100\nRa 168,2,43008\nCV 122,184,185\nMU 186,186,86\nMaav 60,188,108\nD1 1,16,46,2048\nMU 187,187,86\nZaav 56,188,104\nRa 172,2,44032\nWL 8\n"
"Maav 56,184,96\nCV 123,186,187\ns_add_u32 s5, s94, 393216\nbuffer_load_dwordx4 v[16:19], v91, s[28:31], s1 offen offset:0\nv_add_u32 v90, s90, v90\nv_lshlrev_b32 v88, 2, v90\n"
"Zaav 64,128,96\nRa 176,2,45056\nSv 120,1,16,5,0\nMU 188,188,87\nv_lshrrev_b32 v92, 8, v1\nv_bfe_u32 v93, v1, 6, 2\nMaav 64,132,104\nMU 189,189,87\nCV 126,188,189\nZaav 68,132,108\n"
"Ra 180,2,46080\nMU 190,190,87\nMaav 68,128,100\nMU 191,191,87\nCV 127,190,191\nZaav 76,136,100\nRa 184,2,47104\nSv 124,1,16,5,1024\nMaav 76,140,108\nD1 1,16,46,3072\n"
"MU 192,192,86\nZaav 72,140,104\nRa 188,2,48128\nWL 8\nMaav 72,136,96\nMU 193,193,86\nCV 32,192,193\nZaav 80,144,96\nRa 128,2,49152\nMU 194,194,86\nMU 195,195,86\nMaav 80,148,104\n"
"CV 33,194,195\nMU 196,196,87\nZaav 84,148,108\nRa 132,2,50176\nMU 197,197,87\nMaav 84,144,100\ns_add_u32 m0, s89, 147456\ns_add_u32 s46, s93, 32768\nv_cmp_eq_u32 vcc, v92, v93\n"
"v_and_b32 v92, 16, v1\nv_mov_b32 v93, 0x3f80\nv_cndmask_b32 v93, 0, v93, vcc\nZaav 92,152,100\nRa 136,2,51200\nCV 36,196,197\nv_lshlrev_b32 v93, v92, v93\nv_and_b32 v92, 32, v1\n"
"Maav 92,156,108\nD1 1,16,46,0\nMU 198,198,87\nZaav 88,156,104\nRa 140,2,52224\nWL 8\nMaav 88,152,96\nMU 199,199,87\nCV 37,198,199\nZaav 96,160,96\nRa 144,2,53248\nMU 200,200,86\n"
"MU 201,201,86\nMaav 96,164,104\nCV 34,200,201\nMU 202,202,86\nZaav 100,164,108\nRa 148,2,54272\nMU 203,203,86\nMaav 100,160,100\nCV 35,202,203\ns_add_u32 s5, s94, 524288\n"
"Zaav 108,168,100\nRa 152,2,55296\nSv 32,1,16,5,0\nMaav 108,172,108\nD1 1,16,46,1024\nMU 204,204,87\nZaav 104,172,104\nRa 156,2,56320\nWL 8\nMaav 104,168,96\nMU 205,205,87\n"
"CV 38,204,205\nMaav 0,176,112\nRa 160,2,57344\nMU 206,206,87\nMU 207,207,87\nMaav 0,180,120\nCV 39,206,207\nSv 36,1,16,5,1024\nMaav 4,180,124\nRa 164,2,58368\nMU 208,208,86\n"
"Maav 4,176,116\nMU 209,209,86\nCV 40,208,209\nv_cmp_eq_u32 vcc, 0, v92\nv_mov_b32 v4, 0\nv_mov_b32 v5, 0\nv_cndmask_b32 v6, 0, v93, vcc\nMaav 12,184,116\nRa 168,2,59392\n"
"MU 210,210,86\nv_sub_u32 v7, v93, v6\nv_mov_b32 v8, 0\nv_mov_b32 v9, 0\nMaav 12,188,124\nD1 1,16,46,2048\nMU 211,211,86\nMaav 8,188,120\nRa 172,2,60416\nWL 8\nMaav 8,184,112\n"
"CV 41,210,211\nMU 212,212,87\nMaav 16,128,112\nRa 176,2,61440\nMU 213,213,87\nCV 44,212,213\nMaav 16,132,120\nMU 214,214,87\nMU 215,215,87\nMaav 20,132,124\nRa 180,2,62464\n"
"CV 45,214,215\nMaav 20,128,116\nMU 216,216,86\nMU 217,217,86\nMaav 28,136,116\nRa 184,2,63488\nCV 42,216,217\nMaav 28,140,124\nD1 1,16,46,3072\nMU 218,218,86\nMaav 24,140,120\n"
"Ra 188,2,64512\nMaav 24,136,112\ns_waitcnt vmcnt(33) lgkmcnt(8)\ns_barrier\nMaav 32,144,112\nRa 128,3,0\nMU 219,219,86\nCV 43,218,219\nMaav 32,148,120\ns_add_u32 s5, s94, 655360\n"
"Sv 40,1,16,5,0\nMaav 36,148,124\nRa 132,3,1024\nMU 220,220,87\nMaav 36,144,116\nMU 221,221,87\nCV 46,220,221\nMaav 44,152,116\nRa 136,3,2048\nMU 222,222,87\nMaav 44,156,124\n"
"MU 223,223,87\nCV 47,222,223\nMaav 40,156,120\nRa 140,3,3072\nWL 8\nMaav 40,152,112\nSv 44,1,16,5,1024\nMaav 48,160,112\nRa 144,3,4096\nMU 224,224,86\nMaav 48,164,120\n"
"MU 225,225,86\nCV 48,224,225\nbuffer_load_dword v27, off, s[16:19], s60 sc0 sc1\nMaav 52,164,124\nRa 148,3,5120\nMU 226,226,86\nMaav 52,160,116\nMU 227,227,86\nCV 49,226,227\n"
"Maav 60,168,116\nRa 152,3,6144\nMU 228,228,87\nMaav 60,172,124\nMU 229,229,87\nCV 52,228,229\nMaav 56,172,120\nRa 156,3,7168\nWL 8\nMaav 56,168,112\nMU 230,230,87\nMU 231,231,87\n"
"Maav 64,176,112\nRa 160,3,8192\nCV 53,230,231\nMaav 64,180,120\nMU 232,232,86\nMU 233,233,86\nMaav 68,180,124\nRa 164,3,9216\nCV 50,232,233\nMaav 68,176,116\nMU 234,234,86\n"
"MU 235,235,86\nMaav 76,184,116\nRa 168,3,10240\nCV 51,234,235\nMaav 76,188,124\ns_add_u32 s5, s94, 786432\nSv 48,1,16,5,0\nMaav 72,188,120\nRa 172,3,11264\nWL 8\nMaav 72,184,112\n"
"MU 236,236,87\nMU 237,237,87\nMaav 80,128,112\nRa 176,3,12288\nCV 54,236,237\nMaav 80,132,120\nMU 238,238,87\nMU 239,239,87\nMaav 84,132,124\nRa 180,3,13312\nCV 55,238,239\n"
"Maav 84,128,116\nSv 52,1,16,5,1024\nMU 240,240,86\nMaav 92,136,116\nRa 184,3,14336\nMU 241,241,86\nMaav 92,140,124\nCV 56,240,241\nMU 242,242,86\nMaav 88,140,120\nRa 188,3,15360\n"
"WL 8\nMaav 88,136,112\nMU 243,243,86\nCV 57,242,243\nMaav 96,144,112\nRa 128,3,16384\nMU 244,244,87\nMaav 96,148,120\nMU 245,245,87\nCV 60,244,245\nMaav 100,148,124\n"
"Ra 132,3,17408\nMU 246,246,87\nMaav 100,144,116\nMU 247,247,87\nCV 61,246,247\nMaav 108,152,116\nRa 136,3,18432\nMU 248,248,86\nMaav 108,156,124\nMU 249,249,86\nCV 58,248,249\n"
"Maav 104,156,120\nRa 140,3,19456\nWL 8\nMaav 104,152,112\nMU 250,250,86\nMU 251,251,86\nMaav 0,160,32\nRa 144,3,20480\nCV 59,250,251\nMaav 0,164,40\ns_add_u32 s5, s94, 917504\n"
"Sv 56,1,16,5,0\nMaav 4,164,44\nRa 148,3,21504\nMU 252,252,87\nMaav 4,160,36\nMU 253,253,87\nCV 62,252,253\nMaav 12,168,36\nRa 152,3,22528\nMU 254,254,87\nMaav 12,172,44\n"
"MU 255,255,87\nCV 63,254,255\nMaav 8,172,40\nRa 156,3,23552\nWL 8\nMaav 8,168,32\nSv 60,1,16,5,1024\nWV 18\nMaav 16,176,32\nRa 160,3,24576\nv_xor_b32 v92, s15, v66\n"
"Maav 16,180,40\nv_xor_b32 v93, s15, v70\nv_xor_b32 v94, s15, v74\nMaav 20,180,44\nRa 164,3,25600\nv_or3_b32 v92, v92, v93, v94\nMaav 20,176,36\nv_xor_b32 v93, s15, v78\n"
"v_or_b32 v92, v92, v93\nMaav 28,184,36\nRa 168,3,26624\nMaav 28,188,44\nv_cmp_ne_u32 vcc, 0, v92\ns_nop 0\ns_cbranch_vccnz .S197\n.S198:\nMaav 24,188,40\nRa 172,3,27648\nWL 8\n"
"Maav 24,184,32\ns_mov_b32 s2, 0xc040000\nbuffer_load_dword v80, v88, s[16:19], s2 offen sc0 sc1\nMaav 32,128,32\nRa 176,3,28672\nMaav 32,132,40\nMaav 36,132,44\nRa 180,3,29696\n"
"Maav 36,128,36\nbuffer_load_dword v81, v88, s[16:19], s2 offen offset:64 sc0 sc1\nv_and_b32 v92, 0x7ff, v67\nMaav 44,136,36\nRa 184,3,30720\nv_lshrrev_b32 v93, 16, v67\n"
"Maav 44,140,44\nv_add_u32 v94, 16, v90\nv_cmp_eq_u32 vcc, v92, v90\ns_nop 1\nv_cndmask_b32 v12, v12, v64, vcc\nv_cndmask_b32 v13, v13, v65, vcc\nv_cndmask_b32 v14, v14, v68, vcc\n"
"v_cndmask_b32 v15, v15, v69, vcc\nMaav 40,140,40\nRa 188,3,31744\nMaav 40,136,32\ns_waitcnt vmcnt(15) lgkmcnt(8)\ns_barrier\nMaav 48,144,32\nRa 128,3,32768\n"
"v_cmp_eq_u32 vcc, v93, v90\ns_nop 1\nv_cndmask_b32 v12, v12, v72, vcc\nv_cndmask_b32 v13, v13, v73, vcc\nv_cndmask_b32 v14, v14, v76, vcc\nv_cndmask_b32 v15, v15, v77, vcc\n"
"Maav 48,148,40\nv_cmp_eq_u32 vcc, v92, v94\ns_nop 1\nv_cndmask_b32 v16, v16, v64, vcc\nv_cndmask_b32 v17, v17, v65, vcc\nv_cndmask_b32 v18, v18, v68, vcc\n"
"v_cndmask_b32 v19, v19, v69, vcc\nMaav 52,148,44\nRa 132,3,33792\nv_cmp_eq_u32 vcc, v93, v94\ns_nop 1\nv_cndmask_b32 v16, v16, v72, vcc\nv_cndmask_b32 v17, v17, v73, vcc\n"
"v_cndmask_b32 v18, v18, v76, vcc\nv_cndmask_b32 v19, v19, v77, vcc\nMaav 52,144,36\nMaav 60,152,36\nRa 136,3,34816\nMaav 60,156,44\nMaav 56,156,40\nRa 140,3,35840\nWL 8\n"
"Maav 56,152,32\nMaav 64,160,32\nRa 144,3,36864\nWV 6\nv_xor_b32 v27, s15, v27\nv_and_b32 v24, 0xffffdfff, v27\nv_cmp_ne_u32 vcc, 0, v24\ns_nop 0\ns_cbranch_vccnz .S200\n.S201:\n"
"Maav 64,164,40\ns_add_u32 s4, s92, 0\nMaav 68,164,44\nRa 148,3,37888\nMaav 68,160,36\ns_add_u32 m0, s89, 81920\ns_add_u32 s46, s93, 65536\n"
"buffer_load_dwordx4 a[192:195], v1, s[16:19], s4 offen offset:0 sc0 sc1\nMaav 76,168,36\nRa 152,3,38912\nMaav 76,172,44\nD1 1,16,46,0\nMaav 72,172,40\nRa 156,3,39936\nWL 8\n"
"Maav 72,168,32\nbuffer_load_dwordx4 a[196:199], v1, s[16:19], s4 offen offset:1024 sc0 sc1\nMaav 80,176,32\nRa 160,3,40960\nMaav 80,180,40\ns_add_u32 s4, s92, 131072\n"
"Maav 84,180,44\nRa 164,3,41984\nMaav 84,176,36\nbuffer_load_dwordx4 a[200:203], v1, s[16:19], s4 offen offset:0 sc0 sc1\nMaav 92,184,36\nRa 168,3,43008\nMaav 92,188,44\n"
"D1 1,16,46,1024\nMaav 88,188,40\nRa 172,3,44032\nWL 8\nMaav 88,184,32\nbuffer_load_dwordx4 a[204:207], v1, s[16:19], s4 offen offset:1024 sc0 sc1\nMaav 96,128,32\nRa 176,3,45056\n"
"Maav 96,132,40\ns_add_u32 s4, s92, 262144\nMaav 100,132,44\nRa 180,3,46080\nMaav 100,128,36\nbuffer_load_dwordx4 a[208:211], v1, s[16:19], s4 offen offset:0 sc0 sc1\n"
"Maav 108,136,36\nRa 184,3,47104\nMaav 108,140,44\nD1 1,16,46,2048\nMaav 104,140,40\nRa 188,3,48128\nWL 8\nMaav 104,136,32\n"
"buffer_load_dwordx4 a[212:215], v1, s[16:19], s4 offen offset:1024 sc0 sc1\nMaav 0,144,48\nRa 128,3,49152\nMaav 0,148,56\ns_add_u32 s4, s92, 393216\nMaav 4,148,60\nRa 132,3,50176\n"
"Maav 4,144,52\nbuffer_load_dwordx4 a[216:219], v1, s[16:19], s4 offen offset:0 sc0 sc1\nMaav 12,152,52\nRa 136,3,51200\nMaav 12,156,60\nD1 1,16,46,3072\nMaav 8,156,56\n"
"Ra 140,3,52224\nWL 8\nMaav 8,152,48\nbuffer_load_dwordx4 a[220:223], v1, s[16:19], s4 offen offset:1024 sc0 sc1\nMaav 16,160,48\nRa 144,3,53248\nMaav 16,164,56\nMaav 20,164,60\n"
"Ra 148,3,54272\nMaav 20,160,52\ns_add_u32 m0, s89, 65536\ns_add_u32 s46, s93, 98304\nMaav 28,168,52\nRa 152,3,55296\nMaav 28,172,60\nD1 1,16,46,0\nMaav 24,172,56\nRa 156,3,56320\n"
"WL 8\nMaav 24,168,48\nMaav 32,176,48\nRa 160,3,57344\nMaav 32,180,56\nMaav 36,180,60\nRa 164,3,58368\nMaav 36,176,52\nMaav 44,184,52\nRa 168,3,59392\nMaav 44,188,60\n"
"D1 1,16,46,1024\nMaav 40,188,56\nRa 172,3,60416\nWL 8\nMaav 40,184,48\nMaav 48,128,48\nRa 176,3,61440\nMaav 48,132,56\nMaav 52,132,60\nRa 180,3,62464\nMaav 52,128,52\n"
"Maav 60,136,52\nRa 184,3,63488\nMaav 60,140,60\nD1 1,16,46,2048\nMaav 56,140,56\nRa 188,3,64512\nMaav 56,136,48\nWL 8\ns_barrier\nMaav 64,144,48\nRa 128,0,0\nMaav 64,148,56\n"
"Maav 68,148,60\nRa 132,0,1024\nMaav 68,144,52\nMaav 76,152,52\nRa 136,0,2048\nMaav 76,156,60\nD1 1,16,46,3072\nMaav 72,156,56\nRa 140,0,3072\nWL 8\nMaav 72,152,48\nMaav 80,160,48\n"
"Ra 144,0,4096\nMaav 80,164,56\nMaav 84,164,60\nRa 148,0,5120\nMaav 84,160,52\ns_add_u32 m0, s89, 98304\ns_add_u32 s46, s93, 131072\nMaav 92,168,52\nRa 152,0,6144\nMaav 92,172,60\n"
"D1 1,16,46,0\nMaav 88,172,56\nRa 156,0,7168\nWL 8\nMaav 88,168,48\nWV 5\nAR 22,195\nAR 23,199\nMaav 96,176,48\nRa 160,0,8192\nv_max_u32 v21, v22, v23\nMaav 96,180,56\nAR 22,203\n"
"AR 23,207\nMaav 100,180,60\nRa 164,0,9216\nv_max3_u32 v21, v21, v22, v23\nAR 22,211\nMaav 100,176,52\nAR 23,215\nv_max3_u32 v21, v21, v22, v23\nMaav 108,184,52\nRa 168,0,10240\n"
"AR 22,219\nMaav 108,188,60\nD1 1,16,46,1024\nAR 23,223\nv_max3_u32 v21, v21, v22, v23\nMaav 104,188,56\nRa 172,0,11264\nWL 8\nMaav 104,184,48\nv_cmp_eq_u32 vcc, -1, v21\ns_nop 0\n"
"s_cbranch_vccnz .S191\n.S192:\nMaaa 0,128,192\nRa 176,0,12288\nMaaa 0,132,200\nv_sub_f32 v92, 1.0, v80\nv_sub_f32 v93, 1.0, v81\nMaaa 0,136,208\nRa 180,0,13312\nCV 92,92,93\n"
"Maaa 0,140,216\nv_and_b32 v93, 0xffff0000, v92\nv_lshlrev_b32 v92, 16, v92\nMaaa 4,140,220\nRa 184,0,14336\nv_lshlrev_b32 v64, 16, v12\nMaaa 4,136,212\nD1 1,16,46,2048\n"
"v_and_b32 v65, 0xffff0000, v12\nMaaa 4,132,204\nRa 188,0,15360\nWL 8\nMaaa 4,128,196\nMU 64,64,92\nMU 65,65,92\nMaaa 8,144,192\nRa 128,0,16384\nv_lshlrev_b32 v66, 16, v13\n"
"Maaa 8,148,200\nv_and_b32 v67, 0xffff0000, v13\nMU 66,66,92\nMaaa 8,152,208\nRa 132,0,17408\nMU 67,67,92\nMaaa 8,156,216\nv_lshlrev_b32 v68, 16, v14\n"
"v_and_b32 v69, 0xffff0000, v14\nMaaa 12,156,220\nRa 136,0,18432\nMU 68,68,92\nMaaa 12,152,212\nD1 1,16,46,3072\nMU 69,69,92\nMaaa 12,148,204\nRa 140,0,19456\nWL 8\n"
"Maaa 12,144,196\nv_lshlrev_b32 v70, 16, v15\nv_and_b32 v71, 0xffff0000, v15\nMaaa 16,160,192\nRa 144,0,20480\nMU 70,70,92\nMaaa 16,164,200\nMU 71,71,92\n"
"v_lshlrev_b32 v72, 16, v16\nMaaa 16,168,208\nRa 148,0,21504\nv_and_b32 v73, 0xffff0000, v16\nMaaa 16,172,216\ns_add_u32 m0, s89, 114688\ns_add_u32 s46, s93, 163840\n"
"Maaa 20,172,220\nRa 152,0,22528\nMU 72,72,93\nMaaa 20,168,212\nD1 1,16,46,0\nMU 73,73,93\nMaaa 20,164,204\nRa 156,0,23552\nWL 8\nMaaa 20,160,196\nv_lshlrev_b32 v74, 16, v17\n"
"v_and_b32 v75, 0xffff0000, v17\nMaaa 24,176,192\nRa 160,0,24576\nMU 74,74,93\nMaaa 24,180,200\ns_add_u32 s4, s92, 524288\nMU 75,75,93\nMaaa 24,184,208\nRa 164,0,25600\n"
"v_lshlrev_b32 v76, 16, v18\nMaaa 24,188,216\nbuffer_load_dwordx4 a[224:227], v1, s[16:19], s4 offen offset:0 sc0 sc1\nv_and_b32 v77, 0xffff0000, v18\nMaaa 28,188,220\n"
"Ra 168,0,26624\nMU 76,76,93\nMaaa 28,184,212\nD1 1,16,46,1024\nMU 77,77,93\nMaaa 28,180,204\nRa 172,0,27648\nWL 8\nMaaa 28,176,196\n"
"buffer_load_dwordx4 a[228:231], v1, s[16:19], s4 offen offset:1024 sc0 sc1\nv_lshlrev_b32 v78, 16, v19\nMaaa 32,128,192\nRa 176,0,28672\nv_and_b32 v79, 0xffff0000, v19\n"
"Maaa 32,132,200\ns_add_u32 s4, s92, 655360\nMU 78,78,93\nMaaa 32,136,208\nRa 180,0,29696\nMU 79,79,93\nMaaa 32,140,216\n"
"buffer_load_dwordx4 a[232:235], v1, s[16:19], s4 offen offset:0 sc0 sc1\nMaaa 36,140,220\nRa 184,0,30720\nMaaa 36,136,212\nD1 1,16,46,2048\nMaaa 36,132,204\nRa 188,0,31744\n"
"Maaa 36,128,196\ns_waitcnt vmcnt(10) lgkmcnt(8)\ns_barrier\nMaaa 40,144,192\nRa 128,2,16384\nbuffer_load_dwordx4 a[236:239], v1, s[16:19], s4 offen offset:1024 sc0 sc1\n"
"Maaa 40,148,200\nMaaa 40,152,208\nRa 132,2,17408\ns_add_u32 s4, s92, 786432\nMaaa 40,156,216\nMaaa 44,156,220\nRa 136,2,18432\n"
"buffer_load_dwordx4 a[240:243], v1, s[16:19], s4 offen offset:0 sc0 sc1\nMaaa 44,152,212\nD1 1,16,46,3072\nMaaa 44,148,204\nRa 140,2,19456\nWL 8\nMaaa 44,144,196\nMaaa 48,160,192\n"
"Ra 144,2,20480\nbuffer_load_dwordx4 a[244:247], v1, s[16:19], s4 offen offset:1024 sc0 sc1\nMaaa 48,164,200\nMaaa 48,168,208\nRa 148,2,21504\ns_add_u32 s4, s92, 917504\n"
"Maaa 48,172,216\ns_add_u32 m0, s89, 0\ns_add_u32 s46, s93, 196608\nMaaa 52,172,220\nRa 152,2,22528\nbuffer_load_dwordx4 a[248:251], v1, s[16:19], s4 offen offset:0 sc0 sc1\n"
"Maaa 52,168,212\nD1 1,16,46,0\nMaaa 52,164,204\nRa 156,2,23552\nWL 8\nMaaa 52,160,196\nMaaa 56,176,192\nRa 160,2,24576\n"
"buffer_load_dwordx4 a[252:255], v1, s[16:19], s4 offen offset:1024 sc0 sc1\nMaaa 56,180,200\nMaaa 56,184,208\nRa 164,2,25600\nMaaa 56,188,216\nMaaa 60,188,220\nRa 168,2,26624\n"
"Maaa 60,184,212\nD1 1,16,46,1024\nMaaa 60,180,204\nRa 172,2,27648\nWL 8\nMaaa 60,176,196\nMaaa 64,128,192\nRa 176,2,28672\nMaaa 64,132,200\nMaaa 64,136,208\nRa 180,2,29696\n"
"Maaa 64,140,216\nMaaa 68,140,220\nRa 184,2,30720\nMaaa 68,136,212\nD1 1,16,46,2048\nMaaa 68,132,204\nRa 188,2,31744\nWL 8\nMaaa 68,128,196\nMaaa 72,144,192\nRa 128,2,0\n"
"Maaa 72,148,200\nMaaa 72,152,208\nRa 132,2,1024\nMaaa 72,156,216\nMaaa 76,156,220\nRa 136,2,2048\nMaaa 76,152,212\nD1 1,16,46,3072\nMaaa 76,148,204\nRa 140,2,3072\nWL 8\n"
"Maaa 76,144,196\nMaaa 80,160,192\nRa 144,2,4096\nMaaa 80,164,200\nMaaa 80,168,208\nRa 148,2,5120\nMaaa 80,172,216\nMaaa 84,172,220\nRa 152,2,6144\nMaaa 84,168,212\n"
"Maaa 84,164,204\nRa 156,2,7168\nWL 8\nMaaa 84,160,196\nMaaa 88,176,192\nRa 160,2,8192\nMaaa 88,180,200\nMaaa 88,184,208\nRa 164,2,9216\nMaaa 88,188,216\nMaaa 92,188,220\n"
"Ra 168,2,10240\nMaaa 92,184,212\nMaaa 92,180,204\nRa 172,2,11264\nWL 8\nMaaa 92,176,196\nMaaa 96,128,192\nRa 176,2,12288\nMaaa 96,132,200\nv_and_b32 v27, 0x70, v1\n"
"v_lshlrev_b32 v27, 5, v27\nbuffer_load_dword v27, v27, s[16:19], s6 offen sc0 sc1\nMaaa 96,136,208\nRa 180,2,13312\nMaaa 96,140,216\nMaaa 100,140,220\nRa 184,2,14336\n"
"Maaa 100,136,212\nMaaa 100,132,204\nRa 188,2,15360\nMaaa 100,128,196\ns_waitcnt vmcnt(8) lgkmcnt(8)\ns_barrier\nMaaa 104,144,192\nRa 128,2,32768\nWV 4\nAR 22,227\n"
"Maaa 104,148,200\nAR 23,231\nv_max_u32 v21, v22, v23\nAR 22,235\nMaaa 104,152,208\nRa 132,2,33792\nAR 23,239\nMaaa 104,156,216\nv_max3_u32 v21, v21, v22, v23\nAR 22,243\n"
"AR 23,247\nMaaa 108,156,220\nRa 136,2,34816\nv_max3_u32 v21, v21, v22, v23\nMaaa 108,152,212\nAR 22,251\nAR 23,255\nv_max3_u32 v21, v21, v22, v23\nMaaa 108,148,204\n"
"Ra 140,2,35840\nWL 8\nMaaa 108,144,196\nv_cmp_eq_u32 vcc, -1, v21\ns_nop 0\ns_cbranch_vccnz .S194\n.S195:\nMaaa 0,160,224\nRa 144,2,36864\nMaaa 0,164,232\nMaaa 0,168,240\n"
"Ra 148,2,37888\nMaaa 0,172,248\nMaaa 4,172,252\nRa 152,2,38912\nMaaa 4,168,244\nMaaa 4,164,236\nRa 156,2,39936\nWL 8\nMaaa 4,160,228\nMaaa 8,176,224\nRa 160,2,40960\n"
"Maaa 8,180,232\nMaaa 8,184,240\nRa 164,2,41984\nMaaa 8,188,248\nMaaa 12,188,252\nRa 168,2,43008\nMaaa 12,184,244\nMaaa 12,180,236\nRa 172,2,44032\nWL 8\nMaaa 12,176,228\nAR 82,0\n"
"AR 83,1\nMaaa 16,128,224\nRa 176,2,45056\nCV 20,82,83\nAR 82,2\nMaaa 16,132,232\nAR 83,3\nCV 21,82,83\nMaaa 16,136,240\nRa 180,2,46080\nAR 82,8\nMaaa 16,140,248\nAR 83,9\n"
"CV 22,82,83\nMaaa 20,140,252\nRa 184,2,47104\nAR 82,10\nAR 83,11\nMaaa 20,136,244\nCV 23,82,83\ns_add_u32 s5, s95, 0\nMaaa 20,132,236\nRa 188,2,48128\nWL 8\nMaaa 20,128,228\n"
"Sv 20,1,16,5,0\nAR 82,4\nMaaa 24,144,224\nRa 128,2,49152\nAR 83,5\nCV 84,82,83\nMaaa 24,148,232\nAR 82,6\nAR 83,7\nMaaa 24,152,240\nRa 132,2,50176\nCV 85,82,83\nMaaa 24,156,248\n"
"AR 82,12\nAR 83,13\nMaaa 28,156,252\nRa 136,2,51200\nCV 86,82,83\nAR 82,14\nMaaa 28,152,244\nAR 83,15\nCV 87,82,83\nMaaa 28,148,236\nRa 140,2,52224\nWL 8\nMaaa 28,144,228\n"
"Sv 84,1,16,5,1024\nAR 82,16\nMaaa 32,160,224\nRa 144,2,53248\nAR 83,17\nCV 20,82,83\nMaaa 32,164,232\nAR 82,18\nAR 83,19\nMaaa 32,168,240\nRa 148,2,54272\nCV 21,82,83\n"
"Maaa 32,172,248\nAR 82,24\nAR 83,25\nMaaa 36,172,252\nRa 152,2,55296\nCV 22,82,83\nAR 82,26\nMaaa 36,168,244\nAR 83,27\nCV 23,82,83\nMaaa 36,164,236\nRa 156,2,56320\nWL 8\n"
"Maaa 36,160,228\ns_add_u32 s5, s95, 131072\nSv 20,1,16,5,0\nMaaa 40,176,224\nRa 160,2,57344\nAR 82,20\nAR 83,21\nMaaa 40,180,232\nCV 84,82,83\nAR 82,22\nMaaa 40,184,240\n"
"Ra 164,2,58368\nAR 83,23\nMaaa 40,188,248\nCV 85,82,83\nAR 82,28\nMaaa 44,188,252\nRa 168,2,59392\nAR 83,29\nCV 86,82,83\nMaaa 44,184,244\nAR 82,30\nAR 83,31\nMaaa 44,180,236\n"
"Ra 172,2,60416\nWL 8\nMaaa 44,176,228\nCV 87,82,83\nSv 84,1,16,5,1024\nMaaa 48,128,224\nRa 176,2,61440\nAR 82,32\nAR 83,33\nMaaa 48,132,232\nCV 20,82,83\nAR 82,34\n"
"Maaa 48,136,240\nRa 180,2,62464\nAR 83,35\nMaaa 48,140,248\nCV 21,82,83\nAR 82,40\nMaaa 52,140,252\nRa 184,2,63488\nAR 83,41\nCV 22,82,83\nMaaa 52,136,244\nAR 82,42\nAR 83,43\n"
"Maaa 52,132,236\nRa 188,2,64512\nMaaa 52,128,228\ns_waitcnt vmcnt(5) lgkmcnt(8)\ns_barrier\nMaaa 56,144,224\nRa 128,3,0\nCV 23,82,83\ns_add_u32 s5, s95, 262144\nMaaa 56,148,232\n"
"Sv 20,1,16,5,0\nAR 82,36\nMaaa 56,152,240\nRa 132,3,1024\nAR 83,37\nMaaa 56,156,248\nCV 84,82,83\nAR 82,38\nMaaa 60,156,252\nRa 136,3,2048\nAR 83,39\nCV 85,82,83\nMaaa 60,152,244\n"
"AR 82,44\nAR 83,45\nMaaa 60,148,236\nRa 140,3,3072\nWL 8\nMaaa 60,144,228\nCV 86,82,83\nAR 82,46\nMaaa 64,160,224\nRa 144,3,4096\nAR 83,47\nCV 87,82,83\nMaaa 64,164,232\n"
"Sv 84,1,16,5,1024\nAR 82,48\nMaaa 64,168,240\nRa 148,3,5120\nAR 83,49\nMaaa 64,172,248\nCV 20,82,83\nAR 82,50\nMaaa 68,172,252\nRa 152,3,6144\nAR 83,51\nCV 21,82,83\n"
"Maaa 68,168,244\nAR 82,56\nAR 83,57\nMaaa 68,164,236\nRa 156,3,7168\nWL 8\nMaaa 68,160,228\nCV 22,82,83\nAR 82,58\nMaaa 72,176,224\nRa 160,3,8192\nAR 83,59\nCV 23,82,83\n"
"Maaa 72,180,232\ns_add_u32 s5, s95, 393216\nSv 20,1,16,5,0\nMaaa 72,184,240\nRa 164,3,9216\nAR 82,52\nMaaa 72,188,248\nAR 83,53\nCV 84,82,83\nMaaa 76,188,252\nRa 168,3,10240\n"
"AR 82,54\nAR 83,55\nMaaa 76,184,244\nCV 85,82,83\nAR 82,60\nMaaa 76,180,236\nRa 172,3,11264\nWL 8\nMaaa 76,176,228\nAR 83,61\nCV 86,82,83\nMaaa 80,128,224\nRa 176,3,12288\n"
"AR 82,62\nAR 83,63\nMaaa 80,132,232\nCV 87,82,83\nSv 84,1,16,5,1024\nMaaa 80,136,240\nRa 180,3,13312\nAR 82,64\nMaaa 80,140,248\nAR 83,65\nCV 20,82,83\nMaaa 84,140,252\n"
"Ra 184,3,14336\nAR 82,66\nAR 83,67\nMaaa 84,136,244\nCV 21,82,83\nAR 82,72\nMaaa 84,132,236\nRa 188,3,15360\nWL 8\nMaaa 84,128,228\nAR 83,73\nCV 22,82,83\nMaaa 88,144,224\n"
"AR 82,74\nAR 83,75\nCV 23,82,83\nMaaa 88,148,232\ns_add_u32 s5, s95, 524288\nSv 20,1,16,5,0\nMaaa 88,152,240\nAR 82,68\nAR 83,69\nMaaa 88,156,248\nCV 84,82,83\nAR 82,70\n"
"Maaa 92,156,252\nAR 83,71\nCV 85,82,83\nAR 82,76\nMaaa 92,152,244\nAR 83,77\nCV 86,82,83\nMaaa 92,148,236\nWL 4\nMaaa 92,144,228\nAR 82,78\nAR 83,79\nMaaa 96,160,224\nCV 87,82,83\n"
"Sv 84,1,16,5,1024\nAR 82,80\nMaaa 96,164,232\nAR 83,81\nCV 20,82,83\nMaaa 96,168,240\nAR 82,82\nAR 83,83\nMaaa 96,172,248\nCV 21,82,83\nAR 82,88\nMaaa 100,172,252\nAR 83,89\n"
"CV 22,82,83\nAR 82,90\nMaaa 100,168,244\nAR 83,91\nCV 23,82,83\nMaaa 100,164,236\nWL 0\nMaaa 100,160,228\ns_add_u32 s5, s95, 655360\nSv 20,1,16,5,0\nMaaa 104,176,224\nAR 82,84\n"
"AR 83,85\nCV 84,82,83\nMaaa 104,180,232\nAR 82,86\nAR 83,87\nMaaa 104,184,240\nCV 85,82,83\nAR 82,92\nMaaa 104,188,248\nAR 83,93\nCV 86,82,83\nMaaa 108,188,252\nAR 82,94\n"
"AR 83,95\nCV 87,82,83\nMaaa 108,184,244\nSv 84,1,16,5,1024\nMaaa 108,180,236\nMaaa 108,176,228\nAR 82,96\nAR 83,97\nCV 20,82,83\nAR 82,98\nAR 83,99\nCV 21,82,83\nAR 82,104\n"
"AR 83,105\nCV 22,82,83\nAR 82,106\nAR 83,107\nCV 23,82,83\ns_add_u32 s5, s95, 786432\nSv 20,1,16,5,0\nAR 82,100\nAR 83,101\nCV 84,82,83\nAR 82,102\nAR 83,103\nCV 85,82,83\n"
"AR 82,108\nAR 83,109\nCV 86,82,83\nAR 82,110\nAR 83,111\nCV 87,82,83\nSv 84,1,16,5,1024\ns_cmp_eq_u32 s13, 7\ns_cbranch_scc1 .Z206\nWV 14\nv_xor_b32 v27, s15, v27\n"
"v_and_b32 v24, 0xffffdfff, v27\nv_cmp_ne_u32 vcc, 0, v24\ns_nop 0\ns_cbranch_vccnz .S203\n.S204:\nv_cmp_ne_u32 s[62:63], 0, v27\ns_mov_b32 s7, 262144\ns_mov_b32 s3, 0xff\n.Z207:\n"
"s_bitcmp1_b32 s3, 0\ns_cbranch_scc0 .Z209\ns_bitcmp1_b32 s62, 0\ns_cselect_b32 s2, 0x2000, 0\ns_add_u32 s2, s2, 0x0\ns_add_u32 s2, s2, s98\n"
"buffer_load_dwordx4 v[96:99], v1, s[16:19], s2 offen offset:0 sc0 sc1\nbuffer_load_dwordx4 v[100:103], v1, s[16:19], s2 offen offset:1024 sc0 sc1\n.Z209:\ns_bitcmp1_b32 s3, 1\n"
"s_cbranch_scc0 .Z210\ns_bitcmp1_b32 s62, 1\ns_cselect_b32 s2, 0x2000, 0\ns_add_u32 s2, s2, 0x1800000\ns_add_u32 s2, s2, s98\n"
"buffer_load_dwordx4 v[104:107], v1, s[16:19], s2 offen offset:0 sc0 sc1\nbuffer_load_dwordx4 v[108:111], v1, s[16:19], s2 offen offset:1024 sc0 sc1\n.Z210:\ns_bitcmp1_b32 s3, 2\n"
"s_cbranch_scc0 .Z211\ns_bitcmp1_b32 s62, 2\ns_cselect_b32 s2, 0x2000, 0\ns_add_u32 s2, s2, 0x3000000\ns_add_u32 s2, s2, s98\n"
"buffer_load_dwordx4 v[112:115], v1, s[16:19], s2 offen offset:0 sc0 sc1\nbuffer_load_dwordx4 v[116:119], v1, s[16:19], s2 offen offset:1024 sc0 sc1\n.Z211:\ns_bitcmp1_b32 s3, 3\n"
"s_cbranch_scc0 .Z212\ns_bitcmp1_b32 s62, 3\ns_cselect_b32 s2, 0x2000, 0\ns_add_u32 s2, s2, 0x4800000\ns_add_u32 s2, s2, s98\n"
"buffer_load_dwordx4 v[120:123], v1, s[16:19], s2 offen offset:0 sc0 sc1\nbuffer_load_dwordx4 v[124:127], v1, s[16:19], s2 offen offset:1024 sc0 sc1\n.Z212:\ns_bitcmp1_b32 s3, 4\n"
"s_cbranch_scc0 .Z213\ns_bitcmp1_b32 s62, 4\ns_cselect_b32 s2, 0x2000, 0\ns_add_u32 s2, s2, 0x6000000\ns_add_u32 s2, s2, s98\n"
"buffer_load_dwordx4 v[32:35], v1, s[16:19], s2 offen offset:0 sc0 sc1\nbuffer_load_dwordx4 v[36:39], v1, s[16:19], s2 offen offset:1024 sc0 sc1\n.Z213:\ns_bitcmp1_b32 s3, 5\n"
"s_cbranch_scc0 .Z214\ns_bitcmp1_b32 s62, 5\ns_cselect_b32 s2, 0x2000, 0\ns_add_u32 s2, s2, 0x7800000\ns_add_u32 s2, s2, s98\n"
"buffer_load_dwordx4 v[40:43], v1, s[16:19], s2 offen offset:0 sc0 sc1\nbuffer_load_dwordx4 v[44:47], v1, s[16:19], s2 offen offset:1024 sc0 sc1\n.Z214:\ns_bitcmp1_b32 s3, 6\n"
"s_cbranch_scc0 .Z215\ns_bitcmp1_b32 s62, 6\ns_cselect_b32 s2, 0x2000, 0\ns_add_u32 s2, s2, 0x9000000\ns_add_u32 s2, s2, s98\n"
"buffer_load_dwordx4 v[48:51], v1, s[16:19], s2 offen offset:0 sc0 sc1\nbuffer_load_dwordx4 v[52:55], v1, s[16:19], s2 offen offset:1024 sc0 sc1\n.Z215:\ns_bitcmp1_b32 s3, 7\n"
"s_cbranch_scc0 .Z216\ns_bitcmp1_b32 s62, 7\ns_cselect_b32 s2, 0x2000, 0\ns_add_u32 s2, s2, 0xa800000\ns_add_u32 s2, s2, s98\n"
"buffer_load_dwordx4 v[56:59], v1, s[16:19], s2 offen offset:0 sc0 sc1\nbuffer_load_dwordx4 v[60:63], v1, s[16:19], s2 offen offset:1024 sc0 sc1\n.Z216:\ns_mov_b32 s3, 0\nWV 0\n"
"v_max_u32 v82, v99, v103\nv_cmp_eq_u32 s[44:45], -1, v82\nv_max_u32 v82, v107, v111\nv_cmp_eq_u32 s[46:47], -1, v82\nv_max_u32 v82, v115, v119\nv_cmp_eq_u32 s[48:49], -1, v82\n"
"v_max_u32 v82, v123, v127\nv_cmp_eq_u32 s[50:51], -1, v82\nv_max_u32 v82, v35, v39\nv_cmp_eq_u32 s[52:53], -1, v82\nv_max_u32 v82, v43, v47\nv_cmp_eq_u32 s[54:55], -1, v82\n"
"v_max_u32 v82, v51, v55\nv_cmp_eq_u32 s[56:57], -1, v82\nv_max_u32 v82, v59, v63\nv_cmp_eq_u32 s[58:59], -1, v82\ns_cmp_lg_u64 s[44:45], 0\ns_cselect_b32 s2, 1, 0\n"
"s_or_b32 s3, s3, s2\ns_cmp_lg_u64 s[46:47], 0\ns_cselect_b32 s2, 2, 0\ns_or_b32 s3, s3, s2\ns_cmp_lg_u64 s[48:49], 0\ns_cselect_b32 s2, 4, 0\ns_or_b32 s3, s3, s2\n"
"s_cmp_lg_u64 s[50:51], 0\ns_cselect_b32 s2, 8, 0\ns_or_b32 s3, s3, s2\ns_cmp_lg_u64 s[52:53], 0\ns_cselect_b32 s2, 16, 0\ns_or_b32 s3, s3, s2\ns_cmp_lg_u64 s[54:55], 0\n"
"s_cselect_b32 s2, 32, 0\ns_or_b32 s3, s3, s2\ns_cmp_lg_u64 s[56:57], 0\ns_cselect_b32 s2, 64, 0\ns_or_b32 s3, s3, s2\ns_cmp_lg_u64 s[58:59], 0\ns_cselect_b32 s2, 128, 0\n"
"s_or_b32 s3, s3, s2\ns_cbranch_scc0 .Z208\ns_sub_u32 s7, s7, 1\ns_cbranch_scc0 .Z207\n.Z208:\nZvvv 136,6,96\nMvvv 136,6,104\nMvvv 136,6,112\nMvvv 136,6,120\nMvvv 136,6,32\n"
"Mvvv 136,6,40\nMvvv 136,6,48\nMvvv 136,6,56\nZvvv 140,4,96\nMvvv 140,4,104\nMvvv 140,4,112\nMvvv 140,4,120\nMvvv 140,4,32\nMvvv 140,4,40\nMvvv 140,4,48\nMvvv 140,4,56\n"
"Zvvv 144,6,100\nMvvv 144,6,108\nMvvv 144,6,116\nMvvv 144,6,124\nMvvv 144,6,36\nMvvv 144,6,44\nMvvv 144,6,52\nMvvv 144,6,60\nZvvv 148,4,100\nMvvv 148,4,108\nMvvv 148,4,116\n"
"Mvvv 148,4,124\nMvvv 148,4,36\nMvvv 148,4,44\nMvvv 148,4,52\nMvvv 148,4,60\nFM 64,80,136\nFM 65,80,137\nFM 66,80,138\nFM 67,80,139\nFM 68,80,140\nFM 69,80,141\nFM 70,80,142\n"
"FM 71,80,143\nCV 12,64,65\nCV 13,66,67\nCV 14,68,69\nCV 15,70,71\nSv 12,91,36,91,0\nFM 72,81,144\nFM 73,81,145\nFM 74,81,146\nFM 75,81,147\nFM 76,81,148\nFM 77,81,149\n"
"FM 78,81,150\nFM 79,81,151\nCV 16,72,73\nCV 17,74,75\nCV 18,76,77\nCV 19,78,79\nSv 16,91,36,1,0\n.Z206:\ns_endpgm\n.E162:\nv_mov_b32 v12, s97\nv_mov_b32 v13, s97\n"
"v_mov_b32 v14, s97\nv_mov_b32 v15, s97\nv_cmpx_gt_i32 vcc, 3, v3\nv_mov_b32 v7, s97\nv_mov_b32 v19, s97\nv_cmpx_gt_i32 vcc, 2, v3\nv_mov_b32 v6, s97\nv_mov_b32 v18, s97\n"
"v_cmpx_gt_i32 vcc, 1, v3\nv_mov_b32 v5, s97\nv_mov_b32 v17, s97\nv_cmpx_gt_i32 vcc, 0, v3\nv_mov_b32 v4, s97\nv_mov_b32 v16, s97\nv_cmpx_gt_i32 vcc, -16, v3\nv_mov_b32 v8, s97\n"
"v_mov_b32 v9, s97\nv_mov_b32 v10, s97\nv_mov_b32 v11, s97\ns_mov_b64 exec, -1\ns_branch .E163\n.T164:\ns_mov_b32 s62, 2\ns_call_b64 s[80:81], .RB13\ns_branch .T165\n.E166:\n"
"v_mov_b32 v12, s97\nv_mov_b32 v13, s97\nv_mov_b32 v14, s97\nv_mov_b32 v15, s97\nv_cmpx_gt_i32 vcc, 3, v0\nv_mov_b32 v7, s97\nv_mov_b32 v19, s97\nv_cmpx_gt_i32 vcc, 2, v0\n"
"v_mov_b32 v6, s97\nv_mov_b32 v18, s97\nv_cmpx_gt_i32 vcc, 1, v0\nv_mov_b32 v5, s97\nv_mov_b32 v17, s97\nv_cmpx_gt_i32 vcc, 0, v0\nv_mov_b32 v4, s97\nv_mov_b32 v16, s97\n"
"v_cmpx_gt_i32 vcc, -16, v0\nv_mov_b32 v8, s97\nv_mov_b32 v9, s97\nv_mov_b32 v10, s97\nv_mov_b32 v11, s97\ns_mov_b64 exec, -1\ns_branch .E167\n.T168:\ns_mov_b32 s62, 5\n"
"s_call_b64 s[80:81], .RA12\ns_branch .T169\n.T170:\ns_mov_b32 s62, 5\ns_call_b64 s[80:81], .RA12\ns_branch .T171\n.E172:\ns_swappc_b64 s[80:81], s[8:9]\ns_branch .E173\n.E174:\n"
"v_mov_b32 v12, s97\nv_mov_b32 v13, s97\nv_mov_b32 v14, s97\nv_mov_b32 v15, s97\nv_cmpx_gt_i32 vcc, 3, v3\nv_mov_b32 v7, s97\nv_mov_b32 v19, s97\nv_cmpx_gt_i32 vcc, 2, v3\n"
"v_mov_b32 v6, s97\nv_mov_b32 v18, s97\nv_cmpx_gt_i32 vcc, 1, v3\nv_mov_b32 v5, s97\nv_mov_b32 v17, s97\nv_cmpx_gt_i32 vcc, 0, v3\nv_mov_b32 v4, s97\nv_mov_b32 v16, s97\n"
"v_cmpx_gt_i32 vcc, -16, v3\nv_mov_b32 v8, s97\nv_mov_b32 v9, s97\nv_mov_b32 v10, s97\nv_mov_b32 v11, s97\ns_mov_b64 exec, -1\ns_branch .E175\n.T177:\ns_mov_b32 s62, 4\n"
"s_call_b64 s[80:81], .RB13\ns_branch .T178\n.E179:\nv_mov_b32 v12, s97\nv_mov_b32 v13, s97\nv_mov_b32 v14, s97\nv_mov_b32 v15, s97\nv_cmpx_gt_i32 vcc, 3, v0\nv_mov_b32 v7, s97\n"
"v_mov_b32 v19, s97\nv_cmpx_gt_i32 vcc, 2, v0\nv_mov_b32 v6, s97\nv_mov_b32 v18, s97\nv_cmpx_gt_i32 vcc, 1, v0\nv_mov_b32 v5, s97\nv_mov_b32 v17, s97\nv_cmpx_gt_i32 vcc, 0, v0\n"
"v_mov_b32 v4, s97\nv_mov_b32 v16, s97\nv_cmpx_gt_i32 vcc, -16, v0\nv_mov_b32 v8, s97\nv_mov_b32 v9, s97\nv_mov_b32 v10, s97\nv_mov_b32 v11, s97\ns_mov_b64 exec, -1\n"
"s_branch .E180\n.E181:\nv_mov_b32 v91, 0x40000000\nv_cmp_gt_u32 vcc, 0x200, v1\nWV 12\ns_nop 0\nv_cndmask_b32 v91, v91, v1, vcc\nSv 92,91,16,65,0\ns_branch .E182\n.T183:\n"
"s_mov_b32 s62, 4\ns_call_b64 s[80:81], .RA12\ns_branch .T184\n.E185:\ns_swappc_b64 s[80:81], s[8:9]\ns_branch .E186\n.E187:\nv_mov_b32 v12, s97\nv_mov_b32 v13, s97\n"
"v_mov_b32 v14, s97\nv_mov_b32 v15, s97\nv_cmpx_gt_i32 vcc, 3, v3\nv_mov_b32 v7, s97\nv_mov_b32 v19, s97\nv_cmpx_gt_i32 vcc, 2, v3\nv_mov_b32 v6, s97\nv_mov_b32 v18, s97\n"
"v_cmpx_gt_i32 vcc, 1, v3\nv_mov_b32 v5, s97\nv_mov_b32 v17, s97\nv_cmpx_gt_i32 vcc, 0, v3\nv_mov_b32 v4, s97\nv_mov_b32 v16, s97\nv_cmpx_gt_i32 vcc, -16, v3\nv_mov_b32 v8, s97\n"
"v_mov_b32 v9, s97\nv_mov_b32 v10, s97\nv_mov_b32 v11, s97\ns_mov_b64 exec, -1\ns_branch .E188\n.T189:\ns_mov_b32 s62, 6\ns_call_b64 s[80:81], .RB13\ns_branch .T190\n.S191:\n"
"s_mov_b32 s7, 262144\n.S193:\ns_sleep 8\ns_add_u32 s4, s92, 0\nbuffer_load_dwordx4 a[192:195], v1, s[16:19], s4 offen offset:0 sc0 sc1\n"
"buffer_load_dwordx4 a[196:199], v1, s[16:19], s4 offen offset:1024 sc0 sc1\ns_add_u32 s4, s92, 131072\nbuffer_load_dwordx4 a[200:203], v1, s[16:19], s4 offen offset:0 sc0 sc1\n"
"buffer_load_dwordx4 a[204:207], v1, s[16:19], s4 offen offset:1024 sc0 sc1\ns_add_u32 s4, s92, 262144\nbuffer_load_dwordx4 a[208:211], v1, s[16:19], s4 offen offset:0 sc0 sc1\n"
"buffer_load_dwordx4 a[212:215], v1, s[16:19], s4 offen offset:1024 sc0 sc1\ns_add_u32 s4, s92, 393216\nbuffer_load_dwordx4 a[216:219], v1, s[16:19], s4 offen offset:0 sc0 sc1\n"
"buffer_load_dwordx4 a[220:223], v1, s[16:19], s4 offen offset:1024 sc0 sc1\nWV 0\nAR 22,195\nAR 23,199\nv_max_u32 v21, v22, v23\nAR 22,203\nAR 23,207\n"
"v_max3_u32 v21, v21, v22, v23\nAR 22,211\nAR 23,215\nv_max3_u32 v21, v21, v22, v23\nAR 22,219\nAR 23,223\nv_max3_u32 v21, v21, v22, v23\nv_cmp_eq_u32 vcc, -1, v21\ns_nop 0\n"
"s_cbranch_vccz .S192\ns_sub_u32 s7, s7, 1\ns_cbranch_scc0 .S193\ns_branch .S192\n.S194:\ns_mov_b32 s7, 262144\n.S196:\ns_sleep 8\ns_add_u32 s4, s92, 524288\n"
"buffer_load_dwordx4 a[224:227], v1, s[16:19], s4 offen offset:0 sc0 sc1\nbuffer_load_dwordx4 a[228:231], v1, s[16:19], s4 offen offset:1024 sc0 sc1\ns_add_u32 s4, s92, 655360\n"
"buffer_load_dwordx4 a[232:235], v1, s[16:19], s4 offen offset:0 sc0 sc1\nbuffer_load_dwordx4 a[236:239], v1, s[16:19], s4 offen offset:1024 sc0 sc1\ns_add_u32 s4, s92, 786432\n"
"buffer_load_dwordx4 a[240:243], v1, s[16:19], s4 offen offset:0 sc0 sc1\nbuffer_load_dwordx4 a[244:247], v1, s[16:19], s4 offen offset:1024 sc0 sc1\ns_add_u32 s4, s92, 917504\n"
"buffer_load_dwordx4 a[248:251], v1, s[16:19], s4 offen offset:0 sc0 sc1\nbuffer_load_dwordx4 a[252:255], v1, s[16:19], s4 offen offset:1024 sc0 sc1\nWV 0\nAR 22,227\nAR 23,231\n"
"v_max_u32 v21, v22, v23\nAR 22,235\nAR 23,239\nv_max3_u32 v21, v21, v22, v23\nAR 22,243\nAR 23,247\nv_max3_u32 v21, v21, v22, v23\nAR 22,251\nAR 23,255\n"
"v_max3_u32 v21, v21, v22, v23\nv_cmp_eq_u32 vcc, -1, v21\ns_nop 0\ns_cbranch_vccz .S195\ns_sub_u32 s7, s7, 1\ns_cbranch_scc0 .S196\ns_branch .S195\n.S197:\ns_mov_b32 s7, 262144\n"
".S199:\ns_sleep 8\nbuffer_load_dwordx4 v[64:67], v89, s[16:19], s0 offen offset:0 sc0 sc1\nbuffer_load_dwordx4 v[68:71], v89, s[16:19], s0 offen offset:16 sc0 sc1\n"
"buffer_load_dwordx4 v[72:75], v89, s[16:19], s0 offen offset:1792 sc0 sc1\nbuffer_load_dwordx4 v[76:79], v89, s[16:19], s0 offen offset:1808 sc0 sc1\nWV 0\n"
"v_xor_b32 v92, s15, v66\nv_xor_b32 v93, s15, v70\nv_xor_b32 v94, s15, v74\nv_or3_b32 v92, v92, v93, v94\nv_xor_b32 v93, s15, v78\nv_or_b32 v92, v92, v93\nv_cmp_ne_u32 vcc, 0, v92\n"
"s_nop 0\ns_cbranch_vccz .S198\ns_sub_u32 s7, s7, 1\ns_cbranch_scc0 .S199\ns_branch .S198\n.S200:\ns_mov_b32 s7, 262144\n.S202:\ns_sleep 8\n"
"buffer_load_dword v27, off, s[16:19], s60 sc0 sc1\nWV 0\nv_xor_b32 v27, s15, v27\nv_and_b32 v24, 0xffffdfff, v27\nv_cmp_ne_u32 vcc, 0, v24\ns_nop 0\ns_cbranch_vccz .S201\n"
"s_sub_u32 s7, s7, 1\ns_cbranch_scc0 .S202\ns_branch .S201\n.S203:\ns_mov_b32 s7, 262144\n.S205:\ns_sleep 8\nv_and_b32 v27, 0x70, v1\nv_lshlrev_b32 v27, 5, v27\n"
"buffer_load_dword v27, v27, s[16:19], s6 offen sc0 sc1\nWV 0\nv_xor_b32 v27, s15, v27\nv_and_b32 v24, 0xffffdfff, v27\nv_cmp_ne_u32 vcc, 0, v24\ns_nop 0\ns_cbranch_vccz .S204\n"
"s_sub_u32 s7, s7, 1\ns_cbranch_scc0 .S205\ns_branch .S204\n.Lcode1:\n"
  :: "s"(__builtin_amdgcn_kernarg_segment_ptr()), "s"(blockIdx.x), "v"(threadIdx.x)
  : "memory", "scc", "vcc", "v1", "v2", "v3", "v4", "v5", "v6", "v7", "v8", "v9", "v10", "v11", "v12", "v13", "v14", "v15", "v16", "v17", "v18", "v19", "v20", "v21", "v22", "v23", "v24", "v25", "v26", "v27", "v28", "v29", "v30", "v31", "v32", "v33", "v34", "v35", "v36", "v37", "v38", "v39", "v40", "v41", "v42", "v43", "v44", "v45", "v46", "v47", "v48", "v49", "v50", "v51", "v52", "v53", "v54", "v55", "v56", "v57", "v58", "v59", "v60", "v61", "v62", "v63", "v64", "v65", "v66", "v67", "v68", "v69", "v70", "v71", "v72", "v73", "v74", "v75", "v76", "v77", "v78", "v79", "v80", "v81", "v82", "v83", "v84", "v85", "v86", "v87", "v88", "v89", "v90", "v91", "v92", "v93", "v94", "v95", "v96", "v97", "v98", "v99", "v100", "v101", "v102", "v103", "v104", "v105", "v106", "v107", "v108", "v109", "v110", "v111", "v112", "v113", "v114", "v115", "v116", "v117", "v118", "v119", "v120", "v121", "v122", "v123", "v124", "v125", "v126", "v127", "v128", "v129", "v130", "v131", "v132", "v133", "v134", "v135", "v136", "v137", "v138", "v139", "v140", "v141", "v142", "v143", "v144", "v145", "v146", "v147", "v148", "v149", "v150", "v151", "v152", "v153", "v154", "v155", "v156", "v157", "v158", "v159", "v160", "v161", "v162", "v163", "v164", "v165", "v166", "v167", "v168", "v169", "v170", "v171", "v172", "v173", "v174", "v175", "v176", "v177", "v178", "v179", "v180", "v181", "v182", "v183", "v184", "v185", "v186", "v187", "v188", "v189", "v190", "v191", "v192", "v193", "v194", "v195", "v196", "v197", "v198", "v199", "v200", "v201", "v202", "v203", "v204", "v205", "v206", "v207", "v208", "v209", "v210", "v211", "v212", "v213", "v214", "v215", "v216", "v217", "v218", "v219", "v220", "v221", "v222", "v223", "v224", "v225", "v226", "v227", "v228", "v229", "v230", "v231", "v232", "v233", "v234", "v235", "v236", "v237", "v238", "v239", "v240", "v241", "v242", "v243", "v244", "v245", "v246", "v247", "v248", "v249", "v250", "v251", "v252", "v253", "v254", "v255", "a0", "a1", "a2", "a3", "a4", "a5", "a6", "a7", "a8", "a9", "a10", "a11", "a12", "a13", "a14", "a15", "a16", "a17", "a18", "a19", "a20", "a21", "a22", "a23", "a24", "a25", "a26", "a27", "a28", "a29", "a30", "a31", "a32", "a33", "a34", "a35", "a36", "a37", "a38", "a39", "a40", "a41", "a42", "a43", "a44", "a45", "a46", "a47", "a48", "a49", "a50", "a51", "a52", "a53", "a54", "a55", "a56", "a57", "a58", "a59", "a60", "a61", "a62", "a63", "a64", "a65", "a66", "a67", "a68", "a69", "a70", "a71", "a72", "a73", "a74", "a75", "a76", "a77", "a78", "a79", "a80", "a81", "a82", "a83", "a84", "a85", "a86", "a87", "a88", "a89", "a90", "a91", "a92", "a93", "a94", "a95", "a96", "a97", "a98", "a99", "a100", "a101", "a102", "a103", "a104", "a105", "a106", "a107", "a108", "a109", "a110", "a111", "a112", "a113", "a114", "a115", "a116", "a117", "a118", "a119", "a120", "a121", "a122", "a123", "a124", "a125", "a126", "a127", "a128", "a129", "a130", "a131", "a132", "a133", "a134", "a135", "a136", "a137", "a138", "a139", "a140", "a141", "a142", "a143", "a144", "a145", "a146", "a147", "a148", "a149", "a150", "a151", "a152", "a153", "a154", "a155", "a156", "a157", "a158", "a159", "a160", "a161", "a162", "a163", "a164", "a165", "a166", "a167", "a168", "a169", "a170", "a171", "a172", "a173", "a174", "a175", "a176", "a177", "a178", "a179", "a180", "a181", "a182", "a183", "a184", "a185", "a186", "a187", "a188", "a189", "a190", "a191", "a192", "a193", "a194", "a195", "a196", "a197", "a198", "a199", "a200", "a201", "a202", "a203", "a204", "a205", "a206", "a207", "a208", "a209", "a210", "a211", "a212", "a213", "a214", "a215", "a216", "a217", "a218", "a219", "a220", "a221", "a222", "a223", "a224", "a225", "a226", "a227", "a228", "a229", "a230", "a231", "a232", "a233", "a234", "a235", "a236", "a237", "a238", "a239", "a240", "a241", "a242", "a243", "a244", "a245", "a246", "a247", "a248", "a249", "a250", "a251", "a252", "a253", "a254", "a255", "s8", "s9", "s10", "s11", "s12", "s13", "s14", "s15", "s16", "s17", "s18", "s19", "s20", "s21", "s22", "s23", "s24", "s25", "s26", "s27", "s28", "s29", "s30", "s31", "s33", "s34", "s35", "s36", "s37", "s38", "s39", "s40", "s41", "s42", "s43", "s44", "s45", "s46", "s47", "s48", "s49", "s50", "s51", "s52", "s53", "s54", "s55", "s56", "s57", "s58", "s59", "s60", "s61", "s62", "s63", "s64", "s65", "s66", "s67", "s68", "s69", "s70", "s71", "s72", "s73", "s74", "s75", "s76", "s77", "s78", "s79", "s80", "s81", "s82", "s83", "s84", "s85", "s86", "s87", "s88", "s89", "s90", "s91", "s92", "s93", "s94", "s95", "s96", "s97", "s98", "s99");
#endif
}

extern "C" void kernel_launch(void* const* d_in, const int* in_sizes, int n_in,
                              void* d_out, int out_size, void* d_ws, size_t ws_size,
                              hipStream_t stream) {
  (void)hipFuncSetAttribute((const void*)k_main, hipFuncAttributeMaxDynamicSharedMemorySize, 163840);
  k_main<<<256, 256, 163840, stream>>>(d_in[0], d_in[1], d_in[2], d_in[3], d_in[4], d_ws, d_in[11], d_in[7], d_in[5], d_in[8], d_in[9], d_in[10], d_out);
}
```

```cpp
#include <hip/hip_runtime.h>
#include <stdint.h>

extern "C" __global__ __launch_bounds__(256, 1)
void k_main(const void* x, const void* ckv, const void* wq, const void* wuk, const void* wuv, void* ws,
            const void* gate_w, const void* w_r, const void* wo, const void* wg, const void* wu, const void* wd, void* out) {
#if defined(__HIP_DEVICE_COMPILE__)
  asm volatile(
"s_mov_b64 s[8:9], %0\ns_mov_b32 s10, %1\nv_mov_b32 v0, %2\n.macro WL n\ns_waitcnt lgkmcnt(\\n)\n.endm\n.macro D0 x,r,s,o\n"
"buffer_load_dwordx4 v\\x, s[\\r:\\r+3], s\\s offen offset:\\o lds\n.endm\n.macro WV n\ns_waitcnt vmcnt(\\n)\n.endm\n.macro Ra d,x,o\nds_read_b128 a[\\d:\\d+3], v\\x offset:\\o\n"
".endm\n.macro Ta d,x,o\nds_read_b64_tr_b16 a[\\d:\\d+1], v\\x offset:\\o\n.endm\n.macro Zvaa d,x,y\nv_mfma_f32_16x16x32_bf16 v[\\d:\\d+3], a[\\x:\\x+3], a[\\y:\\y+3], 0\n.endm\n"
".macro Mvaa d,x,y\nv_mfma_f32_16x16x32_bf16 v[\\d:\\d+3], a[\\x:\\x+3], a[\\y:\\y+3], v[\\d:\\d+3]\n.endm\n.macro MU d,x,y\nv_mul_f32 v\\d, v\\x, v\\y\n.endm\n.macro CV d,x,y\n"
"v_cvt_pk_bf16_f32 v\\d, v\\x, v\\y\n.endm\n.macro FM d,x,y\nv_fmac_f32 v\\d, v\\x, v\\y\n.endm\n.macro AD d,x,y\nv_add_f32 v\\d, v\\x, v\\y\n.endm\n.macro EX d,x\n"
"v_exp_f32 v\\d, v\\x\n.endm\n.macro Zaaa d,x,y\nv_mfma_f32_16x16x32_bf16 a[\\d:\\d+3], a[\\x:\\x+3], a[\\y:\\y+3], 0\n.endm\n.macro Sv d,x,r,s,o\n"
"buffer_store_dwordx4 v[\\d:\\d+3], v\\x, s[\\r:\\r+3], s\\s offen offset:\\o sc0 sc1\n.endm\n.macro Maaa d,x,y\n"
"v_mfma_f32_16x16x32_bf16 a[\\d:\\d+3], a[\\x:\\x+3], a[\\y:\\y+3], a[\\d:\\d+3]\n.endm\n.macro Rv d,x,o\nds_read_b128 v[\\d:\\d+3], v\\x offset:\\o\n.endm\n.macro D1 x,r,s,o\n"
"buffer_load_dwordx4 v\\x, s[\\r:\\r+3], s\\s offen offset:\\o sc1 lds\n.endm\n.macro AR d,x\nv_accvgpr_read_b32 v\\d, a\\x\n.endm\n.macro Cvav d,x,y,c\n"
"v_mfma_f32_16x16x32_bf16 v[\\d:\\d+3], a[\\x:\\x+3], v[\\y:\\y+3], v[\\c:\\c+3]\n.endm\n.macro Mvav d,x,y\n"
"v_mfma_f32_16x16x32_bf16 v[\\d:\\d+3], a[\\x:\\x+3], v[\\y:\\y+3], v[\\d:\\d+3]\n.endm\n.macro Zaav d,x,y\nv_mfma_f32_16x16x32_bf16 a[\\d:\\d+3], a[\\x:\\x+3], v[\\y:\\y+3], 0\n"
".endm\n.macro Maav d,x,y\nv_mfma_f32_16x16x32_bf16 a[\\d:\\d+3], a[\\x:\\x+3], v[\\y:\\y+3], a[\\d:\\d+3]\n.endm\n.macro SU d,x,y\nv_sub_f32 v\\d, v\\x, v\\y\n.endm\n"
".macro Mava d,x,y\nv_mfma_f32_16x16x32_bf16 a[\\d:\\d+3], v[\\x:\\x+3], a[\\y:\\y+3], a[\\d:\\d+3]\n.endm\n.macro Zvav d,x,y\n"
"v_mfma_f32_16x16x32_bf16 v[\\d:\\d+3], a[\\x:\\x+3], v[\\y:\\y+3], 0\n.endm\n.macro Zvvv d,x,y\nv_mfma_f32_16x16x32_bf16 v[\\d:\\d+3], v[\\x:\\x+3], v[\\y:\\y+3], 0\n.endm\n"
".macro Mvvv d,x,y\nv_mfma_f32_16x16x32_bf16 v[\\d:\\d+3], v[\\x:\\x+3], v[\\y:\\y+3], v[\\d:\\d+3]\n.endm\n.macro AW d,x\nv_accvgpr_write_b32 a\\d, v\\x\n.endm\n.macro Tv d,x,o\n"
"ds_read_b64_tr_b16 v[\\d:\\d+1], v\\x offset:\\o\n.endm\n.macro Sa d,x,r,s,o\nbuffer_store_dwordx4 a[\\d:\\d+3], v\\x, s[\\r:\\r+3], s\\s offen offset:\\o sc0 sc1\n.endm\n"
".macro Cvaa d,x,y,c\nv_mfma_f32_16x16x32_bf16 v[\\d:\\d+3], a[\\x:\\x+3], a[\\y:\\y+3], v[\\c:\\c+3]\n.endm\n.Lcode0:\ns_mov_b32 s70, 0\ns_load_dwordx16 s[44:59], s[8:9], 0x0\n"
"s_load_dwordx8 s[60:67], s[8:9], 0x40\ns_load_dwordx2 s[68:69], s[8:9], 0x60\nv_lshrrev_b32 v2, 6, v0\nv_and_b32 v0, 63, v0\nv_readfirstlane_b32 s11, v2\nv_lshlrev_b32 v1, 4, v0\n"
"s_and_b32 s73, s10, 7\ns_lshr_b32 s74, s10, 3\ns_and_b32 s2, s74, 1\ns_lshl1_add_u32 s12, s73, s2\ns_bfe_u32 s13, s74, 0x30001\ns_lshr_b32 s14, s74, 4\ns_xor_b32 s14, s14, 1\n"
"v_lshrrev_b32 v32, 5, v0\ns_and_b32 s3, s11, 1\ns_lshl_b32 s2, s3, 3\nv_lshl_or_b32 v33, v32, 1, s2\nv_and_b32 v34, 31, v0\nv_xor_b32 v33, v33, v34\nv_lshlrev_b32 v33, 4, v33\n"
"v_lshl_or_b32 v6, v32, 13, v33\nv_xor_b32 v33, 64, v6\nv_add_u32 v7, 15360, v33\nv_add_u32 v8, 30720, v6\nv_add_u32 v9, 46080, v33\ns_lshl_b32 s2, s11, 16\ns_lshl_b32 s4, s12, 9\n"
"s_add_u32 s5, s2, s4\nWL 0\ns_add_u32 s54, s54, 0x1fffff\ns_addc_u32 s55, s55, 0\ns_and_b32 s54, s54, 0xffe00000\ns_mov_b32 s16, s54\ns_and_b32 s17, s55, 0xffff\n"
"s_mov_b32 s18, 0xc0ae800\ns_mov_b32 s19, 0x20000\ns_mov_b32 s28, s44\ns_and_b32 s29, s45, 0xffff\ns_mov_b32 s30, 0x1c0000\ns_mov_b32 s31, 0x20000\ns_mov_b32 s32, s48\n"
"s_and_b32 s33, s49, 0xffff\ns_mov_b32 s34, 0x380000\ns_mov_b32 s35, 0x20000\ns_mov_b32 s20, s46\ns_and_b32 s21, s47, 0xffff\ns_mov_b32 s22, 0x400000\ns_mov_b32 s23, 0x20000\n"
"s_bitcmp1_b32 s13, 0\ns_cselect_b64 s[50:51], s[52:53], s[50:51]\ns_mov_b32 s24, s50\ns_and_b32 s25, s51, 0xffff\ns_mov_b32 s26, 0x800000\ns_mov_b32 s27, 0x20000\n"
"s_mov_b32 s2, 0xc048000\ns_load_dword s15, s[54:55], s2\ns_sub_u32 s44, s5, 0x40000\ns_mov_b32 s51, s5\ns_lshl_b32 s47, s11, 12\ns_add_u32 s47, s47, 0x10000\n"
"s_lshl_b32 s48, s11, 13\ns_cmp_eq_u32 s14, 1\ns_cbranch_scc1 .R11\ns_add_u32 m0, s47, 65536\ns_add_u32 s46, s51, 0\nD0 6,32,46,0\nD0 7,32,46,1024\nD0 8,32,46,2048\n"
"D0 9,32,46,3072\nv_lshrrev_b32 v144, 4, v0\nv_and_b32 v145, 7, v0\nv_xor_b32 v145, v145, v144\nv_lshlrev_b32 v145, 4, v145\nv_lshrrev_b32 v144, 3, v0\n"
"v_mul_u32_u24 v144, 896, v144\nv_add_u32 v136, v144, v145\nv_xor_b32 v145, 64, v136\nv_add_u32 v137, 6144, v145\nv_add_u32 v138, 12288, v136\nv_add_u32 v139, 18432, v145\n"
"s_sub_u32 s2, 15, s13\ns_lshl2_add_u32 s2, s2, s11\ns_mul_i32 s52, s2, 28672\ns_add_u32 m0, s47, -65536\ns_add_u32 s46, s52, 0\nD0 136,28,46,0\nD0 137,28,46,1024\n"
"D0 138,28,46,2048\nD0 139,28,46,3072\ns_add_u32 m0, s47, 81920\ns_add_u32 s46, s51, 262144\nD0 6,32,46,0\nD0 7,32,46,1024\nD0 8,32,46,2048\nD0 9,32,46,3072\ns_add_u32 m0, s47, 0\n"
"s_add_u32 s46, s51, 524288\nD0 6,32,46,0\nD0 7,32,46,1024\nD0 8,32,46,2048\nD0 9,32,46,3072\ns_add_u32 m0, s47, -49152\ns_add_u32 s46, s52, 128\nD0 136,28,46,0\nD0 137,28,46,1024\n"
"D0 138,28,46,2048\nD0 139,28,46,3072\ns_add_u32 m0, s47, 16384\ns_add_u32 s46, s51, 786432\nD0 6,32,46,0\nD0 7,32,46,1024\nD0 8,32,46,2048\nD0 9,32,46,3072\n"
"v_mov_b32 v116, 0x40000000\nv_cmp_gt_u32 vcc, 56, v0\ns_lshl2_add_u32 s2, s10, s11\ns_mul_i32 s2, s2, 1792\nv_cndmask_b32 v117, v116, v1, vcc\ns_mov_b32 s36, s56\n"
"s_and_b32 s37, s57, 0xffff\ns_mov_b32 s38, 0x380\ns_mov_b32 s39, 0x20000\ns_mov_b32 s40, s58\ns_and_b32 s41, s59, 0xffff\ns_mov_b32 s42, 0x380\ns_mov_b32 s43, 0x20000\n"
"buffer_load_dwordx4 v[96:99], v117, s[28:31], s2 offen offset:0\nbuffer_load_dwordx4 v[100:103], v117, s[28:31], s2 offen offset:896\n"
"buffer_load_dwordx4 v[104:107], v117, s[36:39], 0 offen offset:0\nbuffer_load_dwordx4 v[108:111], v117, s[40:43], 0 offen offset:0\ns_getpc_b64 s[40:41]\n.PC6:\n"
"s_sub_u32 s40, s40, .PC6-.Lcode0\ns_subb_u32 s41, s41, 0\ns_mov_b32 s42, .Lcode1-.Lcode0\ns_mov_b32 s43, 0x20000\ns_lshl2_add_u32 s2, s74, s11\ns_lshl_b32 s2, s2, 7\n"
"v_lshlrev_b32 v54, 10, v1\nbuffer_load_dword v31, v54, s[40:43], s2 offen\nv_and_b32 v48, 15, v0\nv_lshrrev_b32 v49, 4, v0\nv_lshrrev_b32 v50, 1, v48\nv_xor_b32 v50, v50, v49\n"
"v_lshlrev_b32 v50, 4, v50\nv_lshl_or_b32 v50, v48, 7, v50\ns_lshr_b32 s2, s11, 1\ns_lshl_b32 s2, s2, 12\nv_add_u32 v10, s2, v50\nv_xor_b32 v11, 64, v10\nv_lshrrev_b32 v50, 2, v48\n"
"v_lshl_add_u32 v51, v49, 3, v50\nv_and_b32 v52, 1, v49\nv_lshl_or_b32 v50, v52, 2, v50\nv_lshlrev_b32 v50, 5, v50\nv_lshl_or_b32 v50, v51, 9, v50\nv_and_b32 v52, 3, v0\n"
"v_lshl_or_b32 v50, v52, 3, v50\ns_and_b32 s2, s11, 1\ns_lshl_b32 s2, s2, 8\ns_add_u32 s2, s2, 0x10000\nv_add_u32 v12, s2, v50\nv_xor_b32 v13, 32, v12\nv_xor_b32 v14, 64, v12\n"
"v_xor_b32 v15, 96, v12\nv_xor_b32 v16, 128, v12\nv_xor_b32 v17, 160, v12\nv_xor_b32 v18, 192, v12\nv_xor_b32 v19, 224, v12\nv_add_u32 v128, 0x10000, v12\n"
"v_add_u32 v129, 0x10000, v13\nv_add_u32 v130, 0x10000, v14\nv_add_u32 v131, 0x10000, v15\nv_add_u32 v132, 0x10000, v16\nv_add_u32 v133, 0x10000, v17\nv_add_u32 v134, 0x10000, v18\n"
"v_add_u32 v135, 0x10000, v19\ns_add_u32 s2, s11, 1\ns_lshr_b32 s2, s2, 1\ns_lshl_b32 s2, s2, 12\nv_add_u32 v140, s2, v10\nv_add_u32 v141, s2, v11\nv_xor_b32 v142, 0x1000, v140\n"
"v_xor_b32 v143, 0x1000, v141\ns_add_u32 m0, s47, 32768\ns_add_u32 s46, s51, 1048576\nD0 6,32,46,0\nD0 7,32,46,1024\nD0 8,32,46,2048\nD0 9,32,46,3072\ns_add_u32 m0, s47, -32768\n"
"s_add_u32 s46, s52, 256\nD0 136,28,46,0\nD0 137,28,46,1024\nD0 138,28,46,2048\nD0 139,28,46,3072\ns_add_u32 m0, s47, 49152\ns_add_u32 s46, s51, 1310720\nD0 6,32,46,0\n"
"D0 7,32,46,1024\nD0 8,32,46,2048\nD0 9,32,46,3072\nv_lshrrev_b32 v32, 4, v0\nv_and_b32 v33, 7, v0\nv_xor_b32 v33, v33, v32\nv_lshlrev_b32 v33, 4, v33\nv_lshrrev_b32 v32, 3, v0\n"
"v_lshl_or_b32 v2, v32, 11, v33\nv_xor_b32 v33, 64, v2\nv_add_u32 v3, 15360, v33\nv_add_u32 v4, 30720, v2\nv_add_u32 v5, 46080, v33\ns_lshr_b32 s2, s13, 2\n"
"s_bfe_u32 s3, s13, 0x10001\ns_mul_i32 s3, s3, 17\ns_add_u32 s2, s2, s3\ns_lshl2_add_u32 s3, s14, s11\ns_lshl_b32 s3, s3, 2\ns_lshr_b32 s3, 0xe420ca86, s3\ns_and_b32 s3, s3, 15\n"
"s_add_u32 s2, s2, s3\ns_cmp_eq_u32 s2, 32\ns_cselect_b32 s4, 16, s2\ns_lshl_b32 s45, s4, 17\ns_sub_u32 s45, s45, 128\nWL 0\ns_mul_i32 s15, s15, 1664525\n"
"s_add_u32 s15, s15, 1013904223\nv_mov_b32 v25, s15\ns_mov_b32 s57, 0x3db8aa3b\ns_and_b32 s59, s11, 1\ns_lshl_b32 s59, s59, 13\nv_lshrrev_b32 v24, 3, v0\n"
"v_lshlrev_b32 v24, 13, v24\nv_and_b32 v26, 7, v0\nv_lshl_or_b32 v24, v26, 7, v24\nv_lshrrev_b32 v26, 4, v0\nv_lshl_or_b32 v24, v26, 28, v24\nv_lshrrev_b32 v26, 5, v0\n"
"v_lshlrev_b32 v26, 30, v26\nv_lshl_or_b32 v26, v0, 7, v26\ns_and_b32 s2, s13, 6\ns_and_b32 s3, s12, 1\ns_add_u32 s2, s2, s3\ns_lshl2_add_u32 s2, s2, s11\ns_lshl_b32 s2, s2, 14\n"
"s_lshl_b32 s3, s73, 10\ns_add_u32 s2, s2, s3\ns_xor_b32 s3, s14, 1\ns_lshl_b32 s3, s3, 30\ns_add_u32 s2, s2, s3\ns_add_u32 s96, s2, 1048576\ns_and_b32 s2, s10, 127\n"
"s_lshl2_add_u32 s2, s2, s11\ns_lshl_b32 s97, s2, 12\ns_mul_i32 s2, s73, 25165824\ns_and_b32 s3, s12, 1\ns_lshl_b32 s3, s3, 21\ns_add_u32 s2, s2, s3\ns_lshl2_add_u32 s3, s13, s11\n"
"s_lshl_b32 s3, s3, 15\ns_add_u32 s2, s2, s3\ns_add_u32 s56, s2, 0x800000\ns_xor_b32 s53, s56, 0xc000\ns_and_b32 s2, s74, 15\ns_lshl2_add_u32 s2, s2, s11\ns_lshl_b32 s2, s2, 14\n"
"s_lshl_b32 s3, s73, 10\ns_add_u32 s2, s2, s3\ns_add_u32 s2, s2, 1572864\nbuffer_load_dword v31, v24, s[32:35], s2 offen\ns_add_u32 s2, s2, 1048576\n"
"buffer_load_dword v31, v24, s[32:35], s2 offen\ns_getpc_b64 s[0:1]\n.PC7:\ns_bitcmp1_b32 s13, 0\ns_mov_b32 s2, .PA2-.PC7\ns_cselect_b32 s2, .PA3-.PC7, s2\ns_add_u32 s0, s0, s2\n"
"s_addc_u32 s1, s1, 0\ns_setpc_b64 s[0:1]\n.R11:\ns_add_u32 m0, s47, 0\ns_add_u32 s44, s44, 0x40000\nD0 6,24,44,0\nD0 7,24,44,1024\nD0 8,24,44,2048\nD0 9,24,44,3072\n"
"v_lshrrev_b32 v32, 4, v0\nv_and_b32 v33, 7, v0\nv_xor_b32 v33, v33, v32\nv_lshlrev_b32 v33, 4, v33\nv_lshrrev_b32 v32, 3, v0\nv_lshl_or_b32 v2, v32, 11, v33\n"
"v_xor_b32 v33, 64, v2\nv_add_u32 v3, 15360, v33\nv_add_u32 v4, 30720, v2\nv_add_u32 v5, 46080, v33\ns_lshr_b32 s2, s13, 2\ns_bfe_u32 s3, s13, 0x10001\ns_mul_i32 s3, s3, 17\n"
"s_add_u32 s2, s2, s3\ns_lshl2_add_u32 s3, s14, s11\ns_lshl_b32 s3, s3, 2\ns_lshr_b32 s3, 0xe420ca86, s3\ns_and_b32 s3, s3, 15\ns_add_u32 s2, s2, s3\ns_cmp_eq_u32 s2, 32\n"
"s_cselect_b32 s4, 16, s2\ns_lshl_b32 s45, s4, 17\ns_sub_u32 s45, s45, 128\ns_add_u32 m0, s48, 0\ns_add_u32 s45, s45, 128\nD0 2,20,45,0\nD0 3,20,45,1024\nD0 4,20,45,2048\n"
"D0 5,20,45,3072\ns_add_u32 m0, s48, 4096\ns_add_u32 s46, s45, 0x10000\nD0 2,20,46,0\nD0 3,20,46,1024\nD0 4,20,46,2048\nD0 5,20,46,3072\ns_add_u32 m0, s47, 16384\n"
"s_add_u32 s44, s44, 0x40000\nD0 6,24,44,0\nD0 7,24,44,1024\nD0 8,24,44,2048\nD0 9,24,44,3072\nv_mov_b32 v52, 0x40000000\nv_cmp_gt_u32 vcc, 56, v0\ns_lshl2_add_u32 s2, s10, s11\n"
"s_mul_i32 s2, s2, 1792\nv_cndmask_b32 v53, v52, v1, vcc\ns_mov_b32 s36, s56\ns_and_b32 s37, s57, 0xffff\ns_mov_b32 s38, 0x380\ns_mov_b32 s39, 0x20000\ns_mov_b32 s40, s58\n"
"s_and_b32 s41, s59, 0xffff\ns_mov_b32 s42, 0x380\ns_mov_b32 s43, 0x20000\nbuffer_load_dwordx4 v[32:35], v53, s[28:31], s2 offen offset:0\n"
"buffer_load_dwordx4 v[36:39], v53, s[28:31], s2 offen offset:896\nbuffer_load_dwordx4 v[40:43], v53, s[36:39], 0 offen offset:0\n"
"buffer_load_dwordx4 v[44:47], v53, s[40:43], 0 offen offset:0\ns_add_u32 m0, s48, 32768\ns_add_u32 s45, s45, 128\nD0 2,20,45,0\nD0 3,20,45,1024\nD0 4,20,45,2048\nD0 5,20,45,3072\n"
"s_add_u32 m0, s48, 36864\ns_add_u32 s46, s45, 0x10000\nD0 2,20,46,0\nD0 3,20,46,1024\nD0 4,20,46,2048\nD0 5,20,46,3072\ns_add_u32 m0, s47, 32768\ns_add_u32 s44, s44, 0x40000\n"
"D0 6,24,44,0\nD0 7,24,44,1024\nD0 8,24,44,2048\nD0 9,24,44,3072\ns_getpc_b64 s[40:41]\n.PC8:\ns_sub_u32 s40, s40, .PC8-.Lcode0\ns_subb_u32 s41, s41, 0\n"
"s_mov_b32 s42, .Lcode1-.Lcode0\ns_mov_b32 s43, 0x20000\ns_lshl2_add_u32 s2, s74, s11\ns_lshl_b32 s2, s2, 7\nv_lshlrev_b32 v54, 10, v1\n"
"buffer_load_dword v31, v54, s[40:43], s2 offen\nv_and_b32 v48, 15, v0\nv_lshrrev_b32 v49, 4, v0\nv_lshrrev_b32 v50, 1, v48\nv_xor_b32 v50, v50, v49\nv_lshlrev_b32 v50, 4, v50\n"
"v_lshl_or_b32 v50, v48, 7, v50\ns_lshr_b32 s2, s11, 1\ns_lshl_b32 s2, s2, 12\nv_add_u32 v10, s2, v50\nv_xor_b32 v11, 64, v10\nv_lshrrev_b32 v50, 2, v48\n"
"v_lshl_add_u32 v51, v49, 3, v50\nv_and_b32 v52, 1, v49\nv_lshl_or_b32 v50, v52, 2, v50\nv_lshlrev_b32 v50, 5, v50\nv_lshl_or_b32 v50, v51, 9, v50\nv_and_b32 v52, 3, v0\n"
"v_lshl_or_b32 v50, v52, 3, v50\ns_and_b32 s2, s11, 1\ns_lshl_b32 s2, s2, 8\ns_add_u32 s2, s2, 0x10000\nv_add_u32 v12, s2, v50\nv_xor_b32 v13, 32, v12\nv_xor_b32 v14, 64, v12\n"
"v_xor_b32 v15, 96, v12\nv_xor_b32 v16, 128, v12\nv_xor_b32 v17, 160, v12\nv_xor_b32 v18, 192, v12\nv_xor_b32 v19, 224, v12\nWL 0\ns_mul_i32 s15, s15, 1664525\n"
"s_add_u32 s15, s15, 1013904223\nv_mov_b32 v25, s15\ns_mov_b32 s57, 0x3db8aa3b\ns_and_b32 s59, s11, 1\ns_lshl_b32 s59, s59, 13\nv_lshrrev_b32 v24, 3, v0\n"
"v_lshlrev_b32 v24, 13, v24\nv_and_b32 v26, 7, v0\nv_lshl_or_b32 v24, v26, 7, v24\nv_lshrrev_b32 v26, 4, v0\nv_lshl_or_b32 v24, v26, 28, v24\nv_lshrrev_b32 v26, 5, v0\n"
"v_lshlrev_b32 v26, 30, v26\nv_lshl_or_b32 v26, v0, 7, v26\ns_and_b32 s2, s13, 6\ns_and_b32 s3, s12, 1\ns_add_u32 s2, s2, s3\ns_lshl2_add_u32 s2, s2, s11\ns_lshl_b32 s2, s2, 14\n"
"s_lshl_b32 s3, s73, 10\ns_add_u32 s2, s2, s3\ns_xor_b32 s3, s14, 1\ns_lshl_b32 s3, s3, 30\ns_add_u32 s2, s2, s3\ns_add_u32 s96, s2, 1048576\ns_and_b32 s2, s10, 127\n"
"s_lshl2_add_u32 s2, s2, s11\ns_lshl_b32 s97, s2, 12\ns_mul_i32 s2, s73, 25165824\ns_and_b32 s3, s12, 1\ns_lshl_b32 s3, s3, 21\ns_add_u32 s2, s2, s3\ns_lshl2_add_u32 s3, s13, s11\n"
"s_lshl_b32 s3, s3, 15\ns_add_u32 s2, s2, s3\ns_add_u32 s56, s2, 0x800000\ns_xor_b32 s53, s56, 0xc000\ns_and_b32 s2, s74, 3\ns_lshl2_add_u32 s2, s73, s2\ns_lshl_b32 s2, s2, 12\n"
"s_add_u32 s3, s45, 512\ns_and_b32 s3, s3, 0xfffe0000\ns_add_u32 s2, s2, s3\nbuffer_load_dword v31, v26, s[20:23], s2 offen\nbuffer_load_dword v31, v24, s[24:27], s96 offen\n"
"s_add_u32 s96, s96, 0x80000\nbuffer_load_dword v31, v24, s[24:27], s96 offen\ns_add_u32 s96, s96, 0x80000\ns_getpc_b64 s[0:1]\n.PC9:\ns_bitcmp1_b32 s13, 0\n"
"s_mov_b32 s2, .PA4-.PC9\ns_cselect_b32 s2, .PA5-.PC9, s2\ns_add_u32 s0, s0, s2\ns_addc_u32 s1, s1, 0\ns_setpc_b64 s[0:1]\n.p2align 6\n.PA2:\nWV 31\ns_barrier\nRa 160,140,0\n"
"Ra 164,140,2048\nRa 168,142,0\nRa 172,142,2048\nTa 128,128,0\nTa 130,128,2048\nTa 132,129,0\nTa 134,129,2048\nTa 136,130,0\nTa 138,130,2048\nTa 140,131,0\nTa 142,131,2048\n"
"Ta 144,132,0\nTa 146,132,2048\nTa 148,133,0\nTa 150,133,2048\nTa 152,134,0\nTa 154,134,2048\nTa 156,135,0\nTa 158,135,2048\nWL 0\nZvaa 64,128,160\nRa 224,141,0\n"
"s_add_u32 m0, s47, -16384\ns_add_u32 s46, s52, 384\nZvaa 32,128,164\nRa 228,141,2048\nZvaa 192,128,168\nRa 232,143,0\nD0 136,28,46,0\nZvaa 224,128,172\nRa 236,143,2048\n"
"Zvaa 228,132,172\nTa 192,128,16384\nZvaa 196,132,168\nZvaa 36,132,164\nTa 194,128,18432\nZvaa 68,132,160\nTa 196,129,16384\nD0 137,28,46,1024\nZvaa 72,136,160\nTa 198,129,18432\n"
"Zvaa 40,136,164\nTa 200,130,16384\nZvaa 200,136,168\nTa 202,130,18432\nZvaa 232,136,172\nZvaa 236,140,172\nTa 204,131,16384\nD0 138,28,46,2048\nZvaa 204,140,168\nTa 206,131,18432\n"
"Zvaa 44,140,164\nTa 208,132,16384\nZvaa 76,140,160\nTa 210,132,18432\nZvaa 80,144,160\nTa 212,133,16384\nZvaa 48,144,164\nD0 139,28,46,3072\nZvaa 208,144,168\nTa 214,133,18432\n"
"Zvaa 240,144,172\nTa 216,134,16384\nZvaa 244,148,172\nTa 218,134,18432\nZvaa 212,148,168\nTa 220,135,16384\nZvaa 52,148,164\nTa 222,135,18432\nZvaa 84,148,160\nZvaa 88,152,160\n"
"Zvaa 56,152,164\nZvaa 216,152,168\nZvaa 248,152,172\nZvaa 252,156,172\nZvaa 220,156,168\nZvaa 60,156,164\nZvaa 92,156,160\ns_waitcnt vmcnt(23) lgkmcnt(0)\ns_barrier\n"
"Mvaa 64,192,224\nRa 160,140,16384\ns_add_u32 m0, s47, 65536\ns_add_u32 s46, s51, 1572864\nMvaa 32,192,228\nRa 164,140,18432\nMvaa 192,192,232\nRa 168,142,16384\nD0 6,32,46,0\n"
"Mvaa 224,192,236\nRa 172,142,18432\nWV 20\nMvaa 228,196,236\nTa 128,12,0\nMvaa 196,196,232\nv_lshlrev_b32 v112, 16, v96\nMvaa 36,196,228\nTa 130,12,2048\n"
"v_lshlrev_b32 v113, 16, v100\nMvaa 68,196,224\nTa 132,13,0\nD0 7,32,46,1024\nMvaa 72,200,224\nTa 134,13,2048\nv_lshlrev_b32 v114, 16, v104\nMvaa 40,200,228\nTa 136,14,0\n"
"Mvaa 200,200,232\nTa 138,14,2048\nMvaa 232,200,236\nv_lshlrev_b32 v115, 16, v108\nMU 116,112,114\nMvaa 236,204,236\nTa 140,15,0\nD0 8,32,46,2048\nMvaa 204,204,232\nTa 142,15,2048\n"
"Mvaa 44,204,228\nTa 144,16,0\nMU 117,113,114\nMvaa 76,204,224\nTa 146,16,2048\nMvaa 80,208,224\nTa 148,17,0\nCV 116,116,117\nMvaa 48,208,228\nD0 9,32,46,3072\nMvaa 208,208,232\n"
"Ta 150,17,2048\nMvaa 240,208,236\nTa 152,18,0\nv_lshlrev_b32 v118, 16, v116\nMvaa 244,212,236\nTa 154,18,2048\ns_add_u32 m0, s47, 81920\ns_add_u32 s46, s51, 1835008\n"
"Mvaa 212,212,232\nTa 156,19,0\nMvaa 52,212,228\nTa 158,19,2048\nD0 6,32,46,0\nMvaa 84,212,224\nv_and_b32 v119, 0xffff0000, v116\nMvaa 88,216,224\nMU 120,112,115\nMU 121,113,115\n"
"Mvaa 56,216,228\nv_and_b32 v112, 0xffff0000, v96\nMvaa 216,216,232\nv_and_b32 v113, 0xffff0000, v100\nMvaa 248,216,236\nD0 7,32,46,1024\nv_and_b32 v114, 0xffff0000, v104\n"
"Mvaa 252,220,236\nv_and_b32 v115, 0xffff0000, v108\nMvaa 220,220,232\nMU 116,112,114\nMvaa 60,220,228\nMU 117,113,114\nCV 116,116,117\nMvaa 92,220,224\nWL 0\nMvaa 64,128,160\n"
"Ra 224,141,16384\nD0 8,32,46,2048\nMvaa 32,128,164\nRa 228,141,18432\nMvaa 192,128,168\nRa 232,143,16384\nMvaa 224,128,172\nRa 236,143,18432\nv_and_b32 v117, 0xffff0000, v116\n"
"Mvaa 228,132,172\nTa 192,12,16384\nMvaa 196,132,168\nD0 9,32,46,3072\nMvaa 36,132,164\nTa 194,12,18432\nv_lshlrev_b32 v116, 16, v116\nMvaa 68,132,160\nTa 196,13,16384\n"
"Mvaa 72,136,160\nTa 198,13,18432\ns_add_u32 m0, s47, -65536\ns_add_u32 s46, s52, 512\nMvaa 40,136,164\nTa 200,14,16384\nMvaa 200,136,168\nTa 202,14,18432\nD0 136,28,46,0\n"
"Mvaa 232,136,172\nFM 120,112,115\nFM 121,113,115\nMvaa 236,140,172\nTa 204,15,16384\nMvaa 204,140,168\nTa 206,15,18432\nMvaa 44,140,164\nTa 208,16,16384\nAD 118,118,116\n"
"Mvaa 76,140,160\nTa 210,16,18432\nD0 137,28,46,1024\nMvaa 80,144,160\nTa 212,17,16384\nAD 119,119,117\nMvaa 48,144,164\nv_lshlrev_b32 v112, 16, v97\nMvaa 208,144,168\n"
"Ta 214,17,18432\nMvaa 240,144,172\nTa 216,18,16384\nv_lshlrev_b32 v113, 16, v101\nMvaa 244,148,172\nTa 218,18,18432\nD0 138,28,46,2048\nMvaa 212,148,168\nTa 220,19,16384\n"
"Mvaa 52,148,164\nTa 222,19,18432\nv_lshlrev_b32 v114, 16, v105\nMvaa 84,148,160\nv_lshlrev_b32 v115, 16, v109\nMvaa 88,152,160\nMU 116,112,114\nMU 117,113,114\nMvaa 56,152,164\n"
"D0 139,28,46,3072\nMvaa 216,152,168\nCV 116,116,117\nMvaa 248,152,172\nv_and_b32 v117, 0xffff0000, v116\nv_lshlrev_b32 v116, 16, v116\nMvaa 252,156,172\nFM 120,112,115\n"
"Mvaa 220,156,168\nFM 121,113,115\nMvaa 60,156,164\nAD 118,118,116\nAD 119,119,117\nMvaa 92,156,160\ns_waitcnt vmcnt(18) lgkmcnt(0)\ns_barrier\nMvaa 64,192,224\nRa 160,140,32768\n"
"s_add_u32 m0, s47, 0\ns_add_u32 s46, s51, 2097152\nMvaa 32,192,228\nRa 164,140,34816\nMvaa 192,192,232\nRa 168,142,32768\nD0 6,32,46,0\nMvaa 224,192,236\nRa 172,142,34816\n"
"v_and_b32 v112, 0xffff0000, v97\nMvaa 228,196,236\nTa 128,12,32768\nMvaa 196,196,232\nv_and_b32 v113, 0xffff0000, v101\nMvaa 36,196,228\nTa 130,12,34816\n"
"v_and_b32 v114, 0xffff0000, v105\nMvaa 68,196,224\nTa 132,13,32768\nD0 7,32,46,1024\nMvaa 72,200,224\nTa 134,13,34816\nv_and_b32 v115, 0xffff0000, v109\nMvaa 40,200,228\n"
"Ta 136,14,32768\nMvaa 200,200,232\nTa 138,14,34816\nMvaa 232,200,236\nMU 116,112,114\nMU 117,113,114\nMvaa 236,204,236\nTa 140,15,32768\nD0 8,32,46,2048\nMvaa 204,204,232\n"
"Ta 142,15,34816\nMvaa 44,204,228\nTa 144,16,32768\nCV 116,116,117\nMvaa 76,204,224\nTa 146,16,34816\nMvaa 80,208,224\nTa 148,17,32768\nv_and_b32 v117, 0xffff0000, v116\n"
"Mvaa 48,208,228\nD0 9,32,46,3072\nMvaa 208,208,232\nTa 150,17,34816\nMvaa 240,208,236\nTa 152,18,32768\nv_lshlrev_b32 v116, 16, v116\nMvaa 244,212,236\nTa 154,18,34816\n"
"s_add_u32 m0, s47, 16384\ns_add_u32 s46, s51, 2359296\nMvaa 212,212,232\nTa 156,19,32768\nMvaa 52,212,228\nTa 158,19,34816\nD0 6,32,46,0\nMvaa 84,212,224\nFM 120,112,115\n"
"Mvaa 88,216,224\nFM 121,113,115\nAD 118,118,116\nMvaa 56,216,228\nAD 119,119,117\nMvaa 216,216,232\nv_lshlrev_b32 v112, 16, v98\nMvaa 248,216,236\nD0 7,32,46,1024\n"
"v_lshlrev_b32 v113, 16, v102\nMvaa 252,220,236\nv_lshlrev_b32 v114, 16, v106\nMvaa 220,220,232\nv_lshlrev_b32 v115, 16, v110\nMvaa 60,220,228\nMU 116,112,114\nMU 117,113,114\n"
"Mvaa 92,220,224\nWL 0\nMvaa 64,128,160\nRa 224,141,32768\nD0 8,32,46,2048\nMvaa 32,128,164\nRa 228,141,34816\nMvaa 192,128,168\nRa 232,143,32768\nMvaa 224,128,172\n"
"Ra 236,143,34816\nCV 116,116,117\nMvaa 228,132,172\nTa 192,12,49152\nMvaa 196,132,168\nD0 9,32,46,3072\nMvaa 36,132,164\nTa 194,12,51200\nv_and_b32 v117, 0xffff0000, v116\n"
"Mvaa 68,132,160\nTa 196,13,49152\nMvaa 72,136,160\nTa 198,13,51200\nv_lshlrev_b32 v116, 16, v116\nMvaa 40,136,164\nTa 200,14,49152\nMvaa 200,136,168\nTa 202,14,51200\n"
"Mvaa 232,136,172\nFM 120,112,115\nFM 121,113,115\nMvaa 236,140,172\nTa 204,15,49152\nMvaa 204,140,168\nTa 206,15,51200\nMvaa 44,140,164\nTa 208,16,49152\nAD 118,118,116\n"
"Mvaa 76,140,160\nTa 210,16,51200\nMvaa 80,144,160\nTa 212,17,49152\nAD 119,119,117\nMvaa 48,144,164\nv_and_b32 v112, 0xffff0000, v98\nMvaa 208,144,168\nTa 214,17,51200\n"
"Mvaa 240,144,172\nTa 216,18,49152\nv_and_b32 v113, 0xffff0000, v102\nMvaa 244,148,172\nTa 218,18,51200\nMvaa 212,148,168\nTa 220,19,49152\nMvaa 52,148,164\nTa 222,19,51200\n"
"v_and_b32 v114, 0xffff0000, v106\nMvaa 84,148,160\nv_and_b32 v115, 0xffff0000, v110\nMvaa 88,152,160\nMU 116,112,114\nMU 117,113,114\nMvaa 56,152,164\nCV 116,116,117\n"
"Mvaa 216,152,168\nv_and_b32 v117, 0xffff0000, v116\nMvaa 248,152,172\nv_lshlrev_b32 v116, 16, v116\nFM 120,112,115\nMvaa 252,156,172\nFM 121,113,115\nMvaa 220,156,168\n"
"AD 118,118,116\nMvaa 60,156,164\nAD 119,119,117\nv_lshlrev_b32 v112, 16, v99\nMvaa 92,156,160\ns_waitcnt vmcnt(12) lgkmcnt(0)\ns_barrier\nMvaa 64,192,224\nRa 160,140,49152\n"
"s_add_u32 m0, s47, 32768\ns_add_u32 s46, s51, 2621440\nMvaa 32,192,228\nRa 164,140,51200\nMvaa 192,192,232\nRa 168,142,49152\nD0 6,32,46,0\nMvaa 224,192,236\nRa 172,142,51200\n"
"v_lshlrev_b32 v113, 16, v103\nMvaa 228,196,236\nTa 128,128,0\nMvaa 196,196,232\nv_lshlrev_b32 v114, 16, v107\nMvaa 36,196,228\nTa 130,128,2048\nv_lshlrev_b32 v115, 16, v111\n"
"Mvaa 68,196,224\nTa 132,129,0\nD0 7,32,46,1024\nMvaa 72,200,224\nTa 134,129,2048\nMU 116,112,114\nMvaa 40,200,228\nTa 136,130,0\nMvaa 200,200,232\nTa 138,130,2048\n"
"Mvaa 232,200,236\nMU 117,113,114\nCV 116,116,117\nMvaa 236,204,236\nTa 140,131,0\nD0 8,32,46,2048\nMvaa 204,204,232\nTa 142,131,2048\nMvaa 44,204,228\nTa 144,132,0\n"
"v_and_b32 v117, 0xffff0000, v116\nMvaa 76,204,224\nTa 146,132,2048\nMvaa 80,208,224\nTa 148,133,0\nv_lshlrev_b32 v116, 16, v116\nMvaa 48,208,228\nD0 9,32,46,3072\n"
"Mvaa 208,208,232\nTa 150,133,2048\nMvaa 240,208,236\nTa 152,134,0\nFM 120,112,115\nMvaa 244,212,236\nTa 154,134,2048\ns_add_u32 m0, s47, -32768\ns_add_u32 s46, s52, 640\n"
"Mvaa 212,212,232\nTa 156,135,0\nMvaa 52,212,228\nTa 158,135,2048\nD0 136,28,46,0\nMvaa 84,212,224\nFM 121,113,115\nMvaa 88,216,224\nAD 118,118,116\nAD 119,119,117\n"
"Mvaa 56,216,228\nv_and_b32 v112, 0xffff0000, v99\nMvaa 216,216,232\nv_and_b32 v113, 0xffff0000, v103\nMvaa 248,216,236\nD0 137,28,46,1024\nv_and_b32 v114, 0xffff0000, v107\n"
"Mvaa 252,220,236\nv_and_b32 v115, 0xffff0000, v111\nMvaa 220,220,232\nMU 116,112,114\nMvaa 60,220,228\nMU 117,113,114\nCV 116,116,117\nMvaa 92,220,224\nWL 0\nMvaa 64,128,160\n"
"Ra 224,141,49152\nD0 138,28,46,2048\nMvaa 32,128,164\nRa 228,141,51200\nMvaa 192,128,168\nRa 232,143,49152\nMvaa 224,128,172\nRa 236,143,51200\nv_and_b32 v117, 0xffff0000, v116\n"
"Mvaa 228,132,172\nTa 192,128,16384\nMvaa 196,132,168\nD0 139,28,46,3072\nMvaa 36,132,164\nTa 194,128,18432\nv_lshlrev_b32 v116, 16, v116\nMvaa 68,132,160\nTa 196,129,16384\n"
"Mvaa 72,136,160\nTa 198,129,18432\ns_add_u32 m0, s47, 49152\ns_add_u32 s46, s51, 2883584\nMvaa 40,136,164\nTa 200,130,16384\nMvaa 200,136,168\nTa 202,130,18432\nD0 6,32,46,0\n"
"Mvaa 232,136,172\nFM 120,112,115\nFM 121,113,115\nMvaa 236,140,172\nTa 204,131,16384\nMvaa 204,140,168\nTa 206,131,18432\nMvaa 44,140,164\nTa 208,132,16384\nAD 118,118,116\n"
"Mvaa 76,140,160\nTa 210,132,18432\nD0 7,32,46,1024\nMvaa 80,144,160\nTa 212,133,16384\nAD 119,119,117\nMvaa 48,144,164\ns_nop 0\nv_permlane32_swap_b32 v118, v119\n"
"Mvaa 208,144,168\nTa 214,133,18432\nMvaa 240,144,172\nTa 216,134,16384\nv_permlane32_swap_b32 v120, v121\nMvaa 244,148,172\nTa 218,134,18432\nD0 8,32,46,2048\nMvaa 212,148,168\n"
"Ta 220,135,16384\nMvaa 52,148,164\nTa 222,135,18432\nAD 118,118,119\nMvaa 84,148,160\nAD 120,120,121\nMvaa 88,152,160\nMvaa 56,152,164\nD0 9,32,46,3072\nMvaa 216,152,168\n"
"v_permlane16_swap_b32 v118, v120\nMvaa 248,152,172\nAD 118,118,120\nMvaa 252,156,172\ns_and_b32 s2, s74, 3\ns_lshl2_add_u32 s2, s73, s2\ns_lshl_b32 s2, s2, 12\n"
"s_add_u32 s3, s45, 512\ns_and_b32 s3, s3, 0xfffe0000\ns_add_u32 s2, s2, s3\nbuffer_load_dword v31, v26, s[20:23], s2 offen\nMvaa 220,156,168\n"
"v_add_f32_dpp v118, v118, v118 row_ror:8 row_mask:0xf bank_mask:0xf\nMvaa 60,156,164\nMvaa 92,156,160\ns_waitcnt vmcnt(13) lgkmcnt(0)\ns_barrier\nMvaa 64,192,224\nRa 160,140,0\n"
"s_add_u32 m0, s47, 65536\ns_add_u32 s46, s51, 3145728\nMvaa 32,192,228\nRa 164,140,2048\nMvaa 192,192,232\nRa 168,142,0\nD0 6,32,46,0\nMvaa 224,192,236\nRa 172,142,2048\n"
"v_add_f32_dpp v118, v118, v118 row_ror:4 row_mask:0xf bank_mask:0xf\nMvaa 228,196,236\nTa 128,12,0\nMvaa 196,196,232\n"
"v_add_f32_dpp v118, v118, v118 row_ror:2 row_mask:0xf bank_mask:0xf\nMvaa 36,196,228\nTa 130,12,2048\nMvaa 68,196,224\nTa 132,13,0\nD0 7,32,46,1024\nMvaa 72,200,224\n"
"Ta 134,13,2048\nv_add_f32_dpp v118, v118, v118 row_ror:1 row_mask:0xf bank_mask:0xf\nMvaa 40,200,228\nTa 136,14,0\nMvaa 200,200,232\nTa 138,14,2048\nMvaa 232,200,236\n"
"CV 118,118,118\nv_lshlrev_b32 v118, 16, v118\nMvaa 236,204,236\nTa 140,15,0\nD0 8,32,46,2048\nMvaa 204,204,232\nTa 142,15,2048\nMvaa 44,204,228\nTa 144,16,0\n"
"v_mul_f32 v119, 0xbfb8aa3b, v118\nMvaa 76,204,224\nTa 146,16,2048\nEX 119,119\nMvaa 80,208,224\nTa 148,17,0\nv_add_f32 v120, 0, v118\nMvaa 48,208,228\nD0 9,32,46,3072\n"
"Mvaa 208,208,232\nTa 150,17,2048\nMvaa 240,208,236\nTa 152,18,0\nv_add_f32 v119, 1.0, v119\nMvaa 244,212,236\nTa 154,18,2048\ns_add_u32 m0, s47, -16384\ns_add_u32 s46, s52, 768\n"
"Mvaa 212,212,232\nTa 156,19,0\nv_rcp_f32 v119, v119\nMvaa 52,212,228\nTa 158,19,2048\nD0 136,28,46,0\nMvaa 84,212,224\nv_ashrrev_i32 v121, 31, v120\nMvaa 88,216,224\n"
"CV 119,119,119\nv_lshlrev_b32 v119, 16, v119\nMvaa 56,216,228\nv_or_b32 v121, 0x80000000, v121\nMvaa 216,216,232\nv_xor_b32 v120, v120, v121\nMvaa 248,216,236\nD0 137,28,46,1024\n"
"v_and_b32 v120, 0xffff0000, v120\nMvaa 252,220,236\ns_lshl2_add_u32 s2, s10, s11\nMvaa 220,220,232\nv_lshrrev_b32 v112, 5, v0\nMvaa 60,220,228\nv_lshl_add_u32 v112, s2, 1, v112\n"
"v_sub_u32 v113, 0xffff, v112\nMvaa 92,220,224\nWL 0\nMvaa 64,128,160\nRa 224,141,0\nD0 138,28,46,2048\nMvaa 32,128,164\nRa 228,141,2048\nMvaa 192,128,168\nRa 232,143,0\n"
"Mvaa 224,128,172\nRa 236,143,2048\nv_or_b32 v120, v120, v113\nMvaa 228,132,172\nTa 192,12,16384\nMvaa 196,132,168\nD0 139,28,46,3072\nMvaa 36,132,164\nTa 194,12,18432\n"
"v_mov_b32 v121, s15\nMvaa 68,132,160\nTa 196,13,16384\nMvaa 72,136,160\nTa 198,13,18432\ns_add_u32 m0, s47, 81920\ns_add_u32 s46, s51, 3407872\nMvaa 40,136,164\nTa 200,14,16384\n"
"Mvaa 200,136,168\nTa 202,14,18432\nD0 6,32,46,0\nMvaa 232,136,172\nv_and_b32 v113, 31, v0\nv_mov_b32 v116, 0x40000000\nMvaa 236,140,172\nTa 204,15,16384\n"
"v_cmp_eq_u32 vcc, 16, v113\nv_lshlrev_b32 v114, 3, v112\nv_lshlrev_b32 v115, 9, v113\nv_cndmask_b32 v114, v116, v114, vcc\nv_cndmask_b32 v119, v119, v118, vcc\nMvaa 204,140,168\n"
"Ta 206,15,18432\nMvaa 44,140,164\nTa 208,16,16384\nv_lshl_add_u32 v115, v112, 2, v115\nMvaa 76,140,160\nTa 210,16,18432\nD0 7,32,46,1024\nMvaa 80,144,160\nTa 212,17,16384\n"
"v_and_b32 v113, 15, v0\nMvaa 48,144,164\nv_cmp_eq_u32 vcc, 0, v113\ns_mov_b32 s2, 0xc040000\ns_mov_b32 s3, 0xc044000\nv_cndmask_b32 v115, v116, v115, vcc\n"
"buffer_store_dwordx2 v[120:121], v114, s[16:19], s3 offen sc0 sc1\nbuffer_store_dword v119, v115, s[16:19], s2 offen sc0 sc1\nMvaa 208,144,168\nTa 214,17,18432\nMvaa 240,144,172\n"
"Ta 216,18,16384\nMvaa 244,148,172\nTa 218,18,18432\nD0 8,32,46,2048\nMvaa 212,148,168\nTa 220,19,16384\nMvaa 52,148,164\nTa 222,19,18432\nMvaa 84,148,160\nMvaa 88,152,160\n"
"Mvaa 56,152,164\nD0 9,32,46,3072\nMvaa 216,152,168\nMvaa 248,152,172\nMvaa 252,156,172\nMvaa 220,156,168\nMvaa 60,156,164\nMvaa 92,156,160\ns_waitcnt vmcnt(15) lgkmcnt(0)\n"
"s_barrier\nMvaa 64,192,224\nRa 160,140,32768\ns_add_u32 m0, s47, 0\ns_add_u32 s44, s44, 0x40000\nMvaa 32,192,228\nRa 164,140,34816\nMvaa 192,192,232\nRa 168,142,32768\n"
"D0 6,24,44,0\nMvaa 224,192,236\nRa 172,142,34816\nMvaa 228,196,236\nTa 128,12,32768\nMvaa 196,196,232\nMvaa 36,196,228\nTa 130,12,34816\nMvaa 68,196,224\nTa 132,13,32768\n"
"D0 7,24,44,1024\nMvaa 72,200,224\nTa 134,13,34816\nMvaa 40,200,228\nTa 136,14,32768\nMvaa 200,200,232\nTa 138,14,34816\nMvaa 232,200,236\nMvaa 236,204,236\nTa 140,15,32768\n"
"D0 8,24,44,2048\nMvaa 204,204,232\nTa 142,15,34816\nMvaa 44,204,228\nTa 144,16,32768\nMvaa 76,204,224\nTa 146,16,34816\nMvaa 80,208,224\nTa 148,17,32768\nMvaa 48,208,228\n"
"D0 9,24,44,3072\nMvaa 208,208,232\nTa 150,17,34816\nMvaa 240,208,236\nTa 152,18,32768\nMvaa 244,212,236\nTa 154,18,34816\ns_add_u32 m0, s48, 0\ns_add_u32 s45, s45, 128\n"
"Mvaa 212,212,232\nTa 156,19,32768\nMvaa 52,212,228\nTa 158,19,34816\nD0 2,20,45,0\nMvaa 84,212,224\nMvaa 88,216,224\nMvaa 56,216,228\nMvaa 216,216,232\nMvaa 248,216,236\n"
"D0 3,20,45,1024\nMvaa 252,220,236\nMvaa 220,220,232\nMvaa 60,220,228\nMvaa 92,220,224\nWL 0\nMvaa 64,128,160\nRa 224,141,32768\nD0 4,20,45,2048\nMvaa 32,128,164\nRa 228,141,34816\n"
"Mvaa 192,128,168\nRa 232,143,32768\nMvaa 224,128,172\nRa 236,143,34816\nMvaa 228,132,172\nTa 192,12,49152\nMvaa 196,132,168\nD0 5,20,45,3072\nMvaa 36,132,164\nTa 194,12,51200\n"
"Mvaa 68,132,160\nTa 196,13,49152\nMvaa 72,136,160\nTa 198,13,51200\ns_add_u32 m0, s48, 4096\ns_add_u32 s46, s45, 0x10000\nMvaa 40,136,164\nTa 200,14,49152\nMvaa 200,136,168\n"
"Ta 202,14,51200\nD0 2,20,46,0\nMvaa 232,136,172\nMvaa 236,140,172\nTa 204,15,49152\nMvaa 204,140,168\nTa 206,15,51200\nMvaa 44,140,164\nTa 208,16,49152\nMvaa 76,140,160\n"
"Ta 210,16,51200\nD0 3,20,46,1024\nMvaa 80,144,160\nTa 212,17,49152\nMvaa 48,144,164\nMvaa 208,144,168\nTa 214,17,51200\nMvaa 240,144,172\nTa 216,18,49152\nMvaa 244,148,172\n"
"Ta 218,18,51200\nD0 4,20,46,2048\nMvaa 212,148,168\nTa 220,19,49152\nMvaa 52,148,164\nTa 222,19,51200\nMvaa 84,148,160\nMvaa 88,152,160\nMvaa 56,152,164\nD0 5,20,46,3072\n"
"Mvaa 216,152,168\nMvaa 248,152,172\nMvaa 252,156,172\ns_add_u32 m0, s47, 16384\ns_add_u32 s44, s44, 0x40000\ns_mov_b32 s92, s62\ns_and_b32 s93, s63, 0xffff\n"
"s_mov_b32 s94, 0x188000\ns_mov_b32 s95, 0x20000\nbuffer_load_dword v31, v26, s[92:95], s97 offen\nMvaa 220,156,168\nMvaa 60,156,164\nD0 6,24,44,0\nMvaa 92,156,160\n"
"s_waitcnt vmcnt(14) lgkmcnt(0)\ns_barrier\nMvaa 64,192,224\nRa 160,140,49152\nMvaa 32,192,228\nRa 164,140,51200\nMvaa 192,192,232\nRa 168,142,49152\nMvaa 224,192,236\n"
"Ra 172,142,51200\nD0 7,24,44,1024\nMvaa 228,196,236\nTa 128,128,0\nMvaa 196,196,232\nMvaa 36,196,228\nTa 130,128,2048\nMvaa 68,196,224\nTa 132,129,0\nMvaa 72,200,224\n"
"Ta 134,129,2048\nD0 8,24,44,2048\nMvaa 40,200,228\nTa 136,130,0\nMvaa 200,200,232\nTa 138,130,2048\nMvaa 232,200,236\nMvaa 236,204,236\nTa 140,131,0\nMvaa 204,204,232\n"
"Ta 142,131,2048\nD0 9,24,44,3072\nMvaa 44,204,228\nTa 144,132,0\nMvaa 76,204,224\nTa 146,132,2048\nMvaa 80,208,224\nTa 148,133,0\ns_add_u32 m0, s47, 32768\n"
"s_add_u32 s44, s44, 0x40000\nMvaa 48,208,228\nMvaa 208,208,232\nTa 150,133,2048\nD0 6,24,44,0\nMvaa 240,208,236\nTa 152,134,0\nMvaa 244,212,236\nTa 154,134,2048\nMvaa 212,212,232\n"
"Ta 156,135,0\nMvaa 52,212,228\nTa 158,135,2048\nMvaa 84,212,224\nD0 7,24,44,1024\nMvaa 88,216,224\nMvaa 56,216,228\nMvaa 216,216,232\nMvaa 248,216,236\nMvaa 252,220,236\n"
"D0 8,24,44,2048\ns_mov_b32 s92, s64\ns_and_b32 s93, s65, 0xffff\ns_mov_b32 s94, 0x188000\ns_mov_b32 s95, 0x20000\nbuffer_load_dword v31, v26, s[92:95], s97 offen\n"
"Mvaa 220,220,232\nMvaa 60,220,228\nMvaa 92,220,224\nWL 0\nMvaa 64,128,160\nRa 224,141,49152\nMvaa 32,128,164\nRa 228,141,51200\nD0 9,24,44,3072\nMvaa 192,128,168\n"
"Ra 232,143,49152\nMvaa 224,128,172\nRa 236,143,51200\nMvaa 228,132,172\nTa 192,128,16384\nMvaa 196,132,168\nMvaa 36,132,164\nTa 194,128,18432\nMvaa 68,132,160\nTa 196,129,16384\n"
"Mvaa 72,136,160\nTa 198,129,18432\nMvaa 40,136,164\nTa 200,130,16384\nMvaa 200,136,168\nTa 202,130,18432\nMvaa 232,136,172\nMvaa 236,140,172\nTa 204,131,16384\nMvaa 204,140,168\n"
"Ta 206,131,18432\nMvaa 44,140,164\nTa 208,132,16384\nMvaa 76,140,160\nTa 210,132,18432\nMvaa 80,144,160\nTa 212,133,16384\nMvaa 48,144,164\nMvaa 208,144,168\nTa 214,133,18432\n"
"Mvaa 240,144,172\nTa 216,134,16384\nMvaa 244,148,172\nTa 218,134,18432\nMvaa 212,148,168\nTa 220,135,16384\nMvaa 52,148,164\nTa 222,135,18432\nMvaa 84,148,160\nMvaa 88,152,160\n"
"Mvaa 56,152,164\nMvaa 216,152,168\nMvaa 248,152,172\nMvaa 252,156,172\ns_mov_b32 s92, s66\ns_and_b32 s93, s67, 0xffff\ns_mov_b32 s94, 0x188000\ns_mov_b32 s95, 0x20000\n"
"buffer_load_dword v31, v26, s[92:95], s97 offen\nMvaa 220,156,168\nMvaa 60,156,164\nMvaa 92,156,160\ns_waitcnt vmcnt(11) lgkmcnt(0)\ns_barrier\nMvaa 64,192,224\n"
"s_add_u32 m0, s48, 32768\ns_add_u32 s45, s45, 128\nTa 128,12,0\nMvaa 68,196,224\nTa 130,12,2048\nMvaa 72,200,224\nD0 2,20,45,0\nTa 132,13,0\nMvaa 76,204,224\nTa 134,13,2048\n"
"Mvaa 80,208,224\nTa 136,14,0\nMvaa 84,212,224\nTa 138,14,2048\nMvaa 88,216,224\nTa 140,15,0\nv_mul_f32 v64, s57, v64\nMvaa 92,220,224\nD0 3,20,45,1024\nTa 142,15,2048\n"
"Mvaa 32,192,228\nTa 144,16,0\nv_mul_f32 v65, s57, v65\nMvaa 36,196,228\nTa 146,16,2048\nMvaa 40,200,228\nTa 148,17,0\nCV 64,64,65\nMvaa 44,204,228\nTa 150,17,2048\n"
"v_mul_f32 v66, s57, v66\nMvaa 48,208,228\nD0 4,20,45,2048\nTa 152,18,0\nMvaa 52,212,228\nTa 154,18,2048\nMvaa 56,216,228\nTa 156,19,0\nv_mul_f32 v67, s57, v67\nMvaa 60,220,228\n"
"Ta 158,19,2048\nCV 65,66,67\nMvaa 192,192,232\nRa 160,10,0\nv_mul_f32 v68, s57, v68\nMvaa 196,196,232\nD0 5,20,45,3072\nRa 164,10,2048\nMvaa 200,200,232\nRa 168,10,8192\n"
"v_mul_f32 v69, s57, v69\nMvaa 204,204,232\nRa 172,10,10240\nCV 66,68,69\nMvaa 208,208,232\nRa 176,10,16384\nv_mul_f32 v70, s57, v70\nMvaa 212,212,232\nRa 180,10,18432\n"
"Mvaa 216,216,232\nRa 184,10,24576\nv_mul_f32 v71, s57, v71\nMvaa 220,220,232\nRa 188,10,26624\nCV 67,70,71\nMvaa 224,192,236\nv_mul_f32 v72, s57, v72\nv_mul_f32 v73, s57, v73\n"
"Mvaa 228,196,236\nCV 72,72,73\nMvaa 232,200,236\nv_mul_f32 v74, s57, v74\nv_mul_f32 v75, s57, v75\nMvaa 236,204,236\nCV 73,74,75\nv_mul_f32 v76, s57, v76\nMvaa 240,208,236\n"
"v_mul_f32 v77, s57, v77\nCV 74,76,77\nMvaa 244,212,236\nv_mul_f32 v78, s57, v78\nMvaa 248,216,236\nv_mul_f32 v79, s57, v79\nCV 75,78,79\nMvaa 252,220,236\n"
"s_waitcnt vmcnt(10) lgkmcnt(0)\ns_barrier\nZaaa 0,128,176\nTa 192,12,16384\nv_mul_f32 v80, s57, v80\nZaaa 32,128,180\ns_add_u32 m0, s48, 36864\ns_add_u32 s46, s45, 0x10000\n"
"s_add_u32 s6, s56, 0\nSv 64,1,16,6,0\nZaaa 64,128,184\nTa 194,12,18432\nv_mul_f32 v81, s57, v81\nZaaa 96,128,188\nD0 2,20,46,0\nCV 80,80,81\nZaaa 100,132,188\nTa 196,13,16384\n"
"v_mul_f32 v82, s57, v82\nZaaa 68,132,184\nv_mul_f32 v83, s57, v83\nZaaa 36,132,180\nTa 198,13,18432\nCV 81,82,83\nZaaa 4,132,176\nD0 3,20,46,1024\nv_mul_f32 v84, s57, v84\n"
"Zaaa 8,136,176\nTa 200,14,16384\nv_mul_f32 v85, s57, v85\nZaaa 40,136,180\nCV 82,84,85\ns_add_u32 s6, s56, 0\nSv 72,1,16,6,2048\nZaaa 72,136,184\nTa 202,14,18432\n"
"v_mul_f32 v86, s57, v86\nZaaa 104,136,188\nD0 4,20,46,2048\nv_mul_f32 v87, s57, v87\nZaaa 108,140,188\nTa 204,15,16384\nCV 83,86,87\nZaaa 76,140,184\nv_mul_f32 v88, s57, v88\n"
"Zaaa 44,140,180\nTa 206,15,18432\nv_mul_f32 v89, s57, v89\nZaaa 12,140,176\nD0 5,20,46,3072\nCV 88,88,89\nZaaa 16,144,176\nTa 208,16,16384\nv_mul_f32 v90, s57, v90\n"
"Zaaa 48,144,180\nv_mul_f32 v91, s57, v91\nZaaa 80,144,184\nTa 210,16,18432\nCV 89,90,91\nZaaa 112,144,188\nv_mul_f32 v92, s57, v92\nv_mul_f32 v93, s57, v93\nZaaa 116,148,188\n"
"Ta 212,17,16384\nCV 90,92,93\nZaaa 84,148,184\nv_mul_f32 v94, s57, v94\nZaaa 52,148,180\nTa 214,17,18432\nv_mul_f32 v95, s57, v95\nZaaa 20,148,176\nCV 91,94,95\nZaaa 24,152,176\n"
"Ta 216,18,16384\nZaaa 56,152,180\ns_add_u32 s6, s56, 4096\nSv 80,1,16,6,0\nZaaa 88,152,184\nTa 218,18,18432\nZaaa 120,152,188\nZaaa 124,156,188\nTa 220,19,16384\nZaaa 92,156,184\n"
"s_add_u32 s6, s56, 4096\nSv 88,1,16,6,2048\nZaaa 60,156,180\nTa 222,19,18432\nZaaa 28,156,176\ns_add_u32 m0, s47, 49152\ns_add_u32 s44, s44, 0x40000\nZvaa 64,128,160\nRa 224,11,0\n"
"v_mul_f32 v32, s57, v32\nZvaa 96,128,164\nD0 6,24,44,0\nZvaa 128,128,168\nRa 228,11,2048\nZvaa 160,128,172\nv_mul_f32 v33, s57, v33\nZvaa 164,132,172\nRa 232,11,8192\nCV 32,32,33\n"
"Zvaa 132,132,168\nD0 7,24,44,1024\nZvaa 100,132,164\nRa 236,11,10240\nZvaa 68,132,160\nv_mul_f32 v34, s57, v34\nZvaa 72,136,160\nRa 240,11,16384\nv_mul_f32 v35, s57, v35\n"
"Zvaa 104,136,164\nD0 8,24,44,2048\nZvaa 136,136,168\nRa 244,11,18432\nZvaa 168,136,172\nCV 33,34,35\nZvaa 172,140,172\nRa 248,11,24576\nv_mul_f32 v36, s57, v36\nZvaa 140,140,168\n"
"D0 9,24,44,3072\nZvaa 108,140,164\nRa 252,11,26624\nZvaa 76,140,160\ns_add_u32 m0, s47, 0\ns_add_u32 s44, s44, 0x40000\nZvaa 80,144,160\nv_mul_f32 v37, s57, v37\nCV 34,36,37\n"
"Zvaa 112,144,164\nD0 6,24,44,0\nZvaa 144,144,168\nv_mul_f32 v38, s57, v38\nZvaa 176,144,172\nv_mul_f32 v39, s57, v39\nZvaa 180,148,172\nCV 35,38,39\nZvaa 148,148,168\n"
"D0 7,24,44,1024\ns_add_u32 s6, s56, 0\nSv 32,1,16,6,1024\nZvaa 116,148,164\nv_mul_f32 v40, s57, v40\nZvaa 84,148,160\nv_mul_f32 v41, s57, v41\nZvaa 88,152,160\nCV 40,40,41\n"
"v_mul_f32 v42, s57, v42\nZvaa 120,152,164\nD0 8,24,44,2048\nZvaa 152,152,168\nv_mul_f32 v43, s57, v43\nZvaa 184,152,172\nCV 41,42,43\nZvaa 188,156,172\nv_mul_f32 v44, s57, v44\n"
"v_mul_f32 v45, s57, v45\nZvaa 156,156,168\nD0 9,24,44,3072\nZvaa 124,156,164\nCV 42,44,45\nZvaa 92,156,160\ns_waitcnt vmcnt(11) lgkmcnt(0)\ns_barrier\nMvaa 64,192,224\n"
"Ta 128,12,32768\nv_mul_f32 v46, s57, v46\nMvaa 96,192,228\nv_mul_f32 v47, s57, v47\nMvaa 128,192,232\nTa 130,12,34816\nMvaa 160,192,236\ns_add_u32 m0, s48, 0\n"
"s_add_u32 s45, s45, 128\nMaaa 0,192,240\nTa 132,13,32768\nCV 43,46,47\nMaaa 32,192,244\nD0 2,20,45,0\ns_add_u32 s6, s56, 0\nSv 40,1,16,6,3072\nMaaa 64,192,248\nTa 134,13,34816\n"
"Maaa 96,192,252\nv_mul_f32 v48, s57, v48\nMaaa 100,196,252\nTa 136,14,32768\nv_mul_f32 v49, s57, v49\nMaaa 68,196,248\nCV 48,48,49\nMaaa 36,196,244\nTa 138,14,34816\n"
"Maaa 4,196,240\nv_mul_f32 v50, s57, v50\nMvaa 164,196,236\nTa 140,15,32768\nv_mul_f32 v51, s57, v51\nMvaa 132,196,232\nD0 3,20,45,1024\nMvaa 100,196,228\nTa 142,15,34816\n"
"Mvaa 68,196,224\nCV 49,50,51\nMvaa 72,200,224\nTa 144,16,32768\nv_mul_f32 v52, s57, v52\nMvaa 104,200,228\nv_mul_f32 v53, s57, v53\nMvaa 136,200,232\nTa 146,16,34816\n"
"Mvaa 168,200,236\nCV 50,52,53\nMaaa 8,200,240\nTa 148,17,32768\nv_mul_f32 v54, s57, v54\nMaaa 40,200,244\nD0 4,20,45,2048\nMaaa 72,200,248\nTa 150,17,34816\nMaaa 104,200,252\n"
"v_mul_f32 v55, s57, v55\nMaaa 108,204,252\nTa 152,18,32768\nCV 51,54,55\nMaaa 76,204,248\ns_add_u32 s6, s56, 4096\nSv 48,1,16,6,1024\nMaaa 44,204,244\nTa 154,18,34816\n"
"Maaa 12,204,240\nv_mul_f32 v56, s57, v56\nMvaa 172,204,236\nTa 156,19,32768\nv_mul_f32 v57, s57, v57\nMvaa 140,204,232\nD0 5,20,45,3072\nMvaa 108,204,228\nTa 158,19,34816\n"
"Mvaa 76,204,224\nCV 56,56,57\nMvaa 80,208,224\nRa 160,10,32768\nv_mul_f32 v58, s57, v58\nMvaa 112,208,228\nv_mul_f32 v59, s57, v59\nMvaa 144,208,232\nRa 164,10,34816\n"
"Mvaa 176,208,236\ns_add_u32 m0, s48, 4096\ns_add_u32 s46, s45, 0x10000\nMaaa 16,208,240\nRa 168,10,40960\nCV 57,58,59\nMaaa 48,208,244\nD0 2,20,46,0\nMaaa 80,208,248\n"
"Ra 172,10,43008\nMaaa 112,208,252\nv_mul_f32 v60, s57, v60\nMaaa 116,212,252\nRa 176,10,49152\nv_mul_f32 v61, s57, v61\nMaaa 84,212,248\nCV 58,60,61\nMaaa 52,212,244\n"
"Ra 180,10,51200\nMaaa 20,212,240\nv_mul_f32 v62, s57, v62\nMvaa 180,212,236\nRa 184,10,57344\nv_mul_f32 v63, s57, v63\nMvaa 148,212,232\nD0 3,20,46,1024\nMvaa 116,212,228\n"
"Ra 188,10,59392\nMvaa 84,212,224\nCV 59,62,63\ns_add_u32 s6, s56, 4096\nSv 56,1,16,6,3072\nMvaa 88,216,224\nv_mul_f32 v192, s57, v192\nv_mul_f32 v193, s57, v193\nMvaa 120,216,228\n"
"CV 192,192,193\nMvaa 152,216,232\nv_mul_f32 v194, s57, v194\nMvaa 184,216,236\nv_mul_f32 v195, s57, v195\nMaaa 24,216,240\nCV 193,194,195\nv_mul_f32 v196, s57, v196\n"
"Maaa 56,216,244\nD0 4,20,46,2048\nMaaa 88,216,248\nv_mul_f32 v197, s57, v197\nMaaa 120,216,252\nCV 194,196,197\nMaaa 124,220,252\nv_mul_f32 v198, s57, v198\n"
"v_mul_f32 v199, s57, v199\nMaaa 92,220,248\nCV 195,198,199\ns_add_u32 s6, s53, 0\nSv 192,1,16,6,0\nMaaa 60,220,244\nv_mul_f32 v200, s57, v200\nMaaa 28,220,240\n"
"v_mul_f32 v201, s57, v201\nMvaa 188,220,236\nCV 200,200,201\nv_mul_f32 v202, s57, v202\nMvaa 156,220,232\nD0 5,20,46,3072\nMvaa 124,220,228\nv_mul_f32 v203, s57, v203\n"
"Mvaa 92,220,224\ns_waitcnt vmcnt(17) lgkmcnt(0)\ns_barrier\nMvaa 64,128,160\nTa 192,12,49152\nCV 201,202,203\nMvaa 96,128,164\nv_mul_f32 v204, s57, v204\nMvaa 128,128,168\n"
"Ta 194,12,51200\nMvaa 160,128,172\ns_add_u32 m0, s47, 16384\ns_add_u32 s44, s44, 0x40000\nMaaa 0,128,176\nTa 196,13,49152\nv_mul_f32 v205, s57, v205\nMaaa 32,128,180\n"
"D0 6,24,44,0\nMaaa 64,128,184\nTa 198,13,51200\nMaaa 96,128,188\nCV 202,204,205\nMaaa 100,132,188\nTa 200,14,49152\nv_mul_f32 v206, s57, v206\nMaaa 68,132,184\n"
"v_mul_f32 v207, s57, v207\nMaaa 36,132,180\nTa 202,14,51200\nMaaa 4,132,176\nCV 203,206,207\ns_add_u32 s6, s53, 0\nSv 200,1,16,6,2048\nMvaa 164,132,172\nTa 204,15,49152\n"
"v_mul_f32 v208, s57, v208\nMvaa 132,132,168\nD0 7,24,44,1024\nMvaa 100,132,164\nTa 206,15,51200\nMvaa 68,132,160\nv_mul_f32 v209, s57, v209\nMvaa 72,136,160\nTa 208,16,49152\n"
"CV 208,208,209\nMvaa 104,136,164\nv_mul_f32 v210, s57, v210\nMvaa 136,136,168\nTa 210,16,51200\nMvaa 168,136,172\nv_mul_f32 v211, s57, v211\nMaaa 8,136,176\nTa 212,17,49152\n"
"CV 209,210,211\nMaaa 40,136,180\nD0 8,24,44,2048\nMaaa 72,136,184\nTa 214,17,51200\nMaaa 104,136,188\nv_mul_f32 v212, s57, v212\nMaaa 108,140,188\nTa 216,18,49152\n"
"v_mul_f32 v213, s57, v213\nMaaa 76,140,184\nCV 210,212,213\nMaaa 44,140,180\nTa 218,18,51200\nMaaa 12,140,176\nv_mul_f32 v214, s57, v214\nMvaa 172,140,172\nTa 220,19,49152\n"
"v_mul_f32 v215, s57, v215\nMvaa 140,140,168\nD0 9,24,44,3072\nMvaa 108,140,164\nTa 222,19,51200\nMvaa 76,140,160\nCV 211,214,215\ns_add_u32 s6, s53, 4096\nSv 208,1,16,6,0\n"
"Mvaa 80,144,160\nRa 224,11,32768\nv_mul_f32 v216, s57, v216\nMvaa 112,144,164\nv_mul_f32 v217, s57, v217\nMvaa 144,144,168\nRa 228,11,34816\nMvaa 176,144,172\n"
"s_add_u32 m0, s47, 32768\ns_add_u32 s44, s44, 0x40000\nMaaa 16,144,176\nRa 232,11,40960\nCV 216,216,217\nMaaa 48,144,180\nD0 6,24,44,0\nMaaa 80,144,184\nRa 236,11,43008\n"
"Maaa 112,144,188\nv_mul_f32 v218, s57, v218\nMaaa 116,148,188\nRa 240,11,49152\nv_mul_f32 v219, s57, v219\nMaaa 84,148,184\nCV 217,218,219\nMaaa 52,148,180\nRa 244,11,51200\n"
"Maaa 20,148,176\nv_mul_f32 v220, s57, v220\nMvaa 180,148,172\nRa 248,11,57344\nv_mul_f32 v221, s57, v221\nMvaa 148,148,168\nD0 7,24,44,1024\nMvaa 116,148,164\nRa 252,11,59392\n"
"Mvaa 84,148,160\nCV 218,220,221\nMvaa 88,152,160\nv_mul_f32 v222, s57, v222\nv_mul_f32 v223, s57, v223\nMvaa 120,152,164\nCV 219,222,223\ns_add_u32 s6, s53, 4096\n"
"Sv 216,1,16,6,2048\nMvaa 152,152,168\nv_mul_f32 v224, s57, v224\nMvaa 184,152,172\nv_mul_f32 v225, s57, v225\nMaaa 24,152,176\nCV 224,224,225\nv_mul_f32 v226, s57, v226\n"
"Maaa 56,152,180\nD0 8,24,44,2048\nMaaa 88,152,184\nv_mul_f32 v227, s57, v227\nMaaa 120,152,188\nCV 225,226,227\nMaaa 124,156,188\nv_mul_f32 v228, s57, v228\n"
"v_mul_f32 v229, s57, v229\nMaaa 92,156,184\nCV 226,228,229\nMaaa 60,156,180\nv_mul_f32 v230, s57, v230\nMaaa 28,156,176\nv_mul_f32 v231, s57, v231\nMvaa 188,156,172\n"
"CV 227,230,231\nMvaa 156,156,168\nD0 9,24,44,3072\ns_add_u32 s6, s53, 0\nSv 224,1,16,6,1024\nMvaa 124,156,164\nv_mul_f32 v232, s57, v232\nMvaa 92,156,160\n"
"s_waitcnt vmcnt(12) lgkmcnt(0)\ns_barrier\nMvaa 64,192,224\nTa 128,12,0\nv_mul_f32 v233, s57, v233\nMvaa 96,192,228\nCV 232,232,233\nMvaa 128,192,232\nTa 130,12,2048\n"
"Mvaa 160,192,236\ns_add_u32 m0, s48, 32768\ns_add_u32 s45, s45, 128\nMaaa 0,192,240\nTa 132,13,0\nv_mul_f32 v234, s57, v234\nMaaa 32,192,244\nD0 2,20,45,0\nMaaa 64,192,248\n"
"Ta 134,13,2048\nMaaa 96,192,252\nv_mul_f32 v235, s57, v235\nMaaa 100,196,252\nTa 136,14,0\nCV 233,234,235\nMaaa 68,196,248\nv_mul_f32 v236, s57, v236\nMaaa 36,196,244\n"
"Ta 138,14,2048\nMaaa 4,196,240\nv_mul_f32 v237, s57, v237\nMvaa 164,196,236\nTa 140,15,0\nCV 234,236,237\nMvaa 132,196,232\nD0 3,20,45,1024\nMvaa 100,196,228\nTa 142,15,2048\n"
"Mvaa 68,196,224\nv_mul_f32 v238, s57, v238\nMvaa 72,200,224\nTa 144,16,0\nv_mul_f32 v239, s57, v239\nMvaa 104,200,228\nCV 235,238,239\ns_add_u32 s6, s53, 0\nSv 232,1,16,6,3072\n"
"Mvaa 136,200,232\nTa 146,16,2048\nMvaa 168,200,236\nv_mul_f32 v240, s57, v240\nMaaa 8,200,240\nTa 148,17,0\nv_mul_f32 v241, s57, v241\nMaaa 40,200,244\nD0 4,20,45,2048\n"
"Maaa 72,200,248\nTa 150,17,2048\nMaaa 104,200,252\nCV 240,240,241\nMaaa 108,204,252\nTa 152,18,0\nv_mul_f32 v242, s57, v242\nMaaa 76,204,248\nv_mul_f32 v243, s57, v243\n"
"Maaa 44,204,244\nTa 154,18,2048\nMaaa 12,204,240\nCV 241,242,243\nMvaa 172,204,236\nTa 156,19,0\nv_mul_f32 v244, s57, v244\nMvaa 140,204,232\nD0 5,20,45,3072\nMvaa 108,204,228\n"
"Ta 158,19,2048\nMvaa 76,204,224\nv_mul_f32 v245, s57, v245\nMvaa 80,208,224\nRa 160,10,0\nCV 242,244,245\nMvaa 112,208,228\nv_mul_f32 v246, s57, v246\nMvaa 144,208,232\n"
"Ra 164,10,2048\nMvaa 176,208,236\ns_add_u32 m0, s48, 36864\ns_add_u32 s46, s45, 0x10000\nMaaa 16,208,240\nRa 168,10,8192\nv_mul_f32 v247, s57, v247\nMaaa 48,208,244\nD0 2,20,46,0\n"
"Maaa 80,208,248\nRa 172,10,10240\nMaaa 112,208,252\nCV 243,246,247\ns_add_u32 s6, s53, 4096\nSv 240,1,16,6,1024\nMaaa 116,212,252\nRa 176,10,16384\nv_mul_f32 v248, s57, v248\n"
"Maaa 84,212,248\nv_mul_f32 v249, s57, v249\nMaaa 52,212,244\nRa 180,10,18432\nMaaa 20,212,240\nCV 248,248,249\nMvaa 180,212,236\nRa 184,10,24576\nv_mul_f32 v250, s57, v250\n"
"Mvaa 148,212,232\nD0 3,20,46,1024\nMvaa 116,212,228\nRa 188,10,26624\nMvaa 84,212,224\nv_mul_f32 v251, s57, v251\nMvaa 88,216,224\nCV 249,250,251\nv_mul_f32 v252, s57, v252\n"
"Mvaa 120,216,228\nv_mul_f32 v253, s57, v253\nMvaa 152,216,232\nCV 250,252,253\nMvaa 184,216,236\nv_mul_f32 v254, s57, v254\nMaaa 24,216,240\nv_mul_f32 v255, s57, v255\n"
"CV 251,254,255\nMaaa 56,216,244\nD0 4,20,46,2048\ns_add_u32 s6, s53, 4096\nSv 248,1,16,6,3072\nMaaa 88,216,248\nMaaa 120,216,252\nMaaa 124,220,252\nMaaa 92,220,248\n"
"Maaa 60,220,244\nMaaa 28,220,240\ns_lshl3_add_u32 s90, s12, s13\ns_lshl2_add_u32 s90, s90, s11\ns_lshl2_add_u32 s90, s90, 0xc04a400\nMvaa 188,220,236\nMvaa 156,220,232\n"
"D0 5,20,46,3072\nMvaa 124,220,228\nMvaa 92,220,224\ns_waitcnt vmcnt(18) lgkmcnt(0)\ns_barrier\nMvaa 64,128,160\nTa 192,12,16384\nMvaa 96,128,164\nMvaa 128,128,168\n"
"Ta 194,12,18432\nMvaa 160,128,172\ns_add_u32 m0, s47, 49152\ns_add_u32 s44, s44, 0x40000\nMaaa 0,128,176\nTa 196,13,16384\nMaaa 32,128,180\nD0 6,24,44,0\nMaaa 64,128,184\n"
"Ta 198,13,18432\nMaaa 96,128,188\nMaaa 100,132,188\nTa 200,14,16384\nMaaa 68,132,184\nMaaa 36,132,180\nTa 202,14,18432\nMaaa 4,132,176\nMvaa 164,132,172\nTa 204,15,16384\n"
"Mvaa 132,132,168\nD0 7,24,44,1024\nMvaa 100,132,164\nTa 206,15,18432\nMvaa 68,132,160\nMvaa 72,136,160\nTa 208,16,16384\nMvaa 104,136,164\nMvaa 136,136,168\nTa 210,16,18432\n"
"Mvaa 168,136,172\nMaaa 8,136,176\nTa 212,17,16384\nMaaa 40,136,180\nD0 8,24,44,2048\nMaaa 72,136,184\nTa 214,17,18432\nMaaa 104,136,188\nMaaa 108,140,188\nTa 216,18,16384\n"
"Maaa 76,140,184\nMaaa 44,140,180\nTa 218,18,18432\nMaaa 12,140,176\nMvaa 172,140,172\nTa 220,19,16384\nMvaa 140,140,168\nD0 9,24,44,3072\nMvaa 108,140,164\nTa 222,19,18432\n"
"Mvaa 76,140,160\nMvaa 80,144,160\nRa 224,11,0\nMvaa 112,144,164\nMvaa 144,144,168\nRa 228,11,2048\nMvaa 176,144,172\ns_add_u32 m0, s47, 0\ns_add_u32 s44, s44, 0x40000\n"
"Maaa 16,144,176\nRa 232,11,8192\nMaaa 48,144,180\nD0 6,24,44,0\nMaaa 80,144,184\nRa 236,11,10240\nMaaa 112,144,188\nMaaa 116,148,188\nRa 240,11,16384\nMaaa 84,148,184\n"
"Maaa 52,148,180\nRa 244,11,18432\nMaaa 20,148,176\nMvaa 180,148,172\nRa 248,11,24576\nMvaa 148,148,168\nD0 7,24,44,1024\nMvaa 116,148,164\nRa 252,11,26624\nMvaa 84,148,160\n"
"Mvaa 88,152,160\nMvaa 120,152,164\nMvaa 152,152,168\nMvaa 184,152,172\nMaaa 24,152,176\nMaaa 56,152,180\nD0 8,24,44,2048\nMaaa 88,152,184\nMaaa 120,152,188\nMaaa 124,156,188\n"
"Maaa 92,156,184\nMaaa 60,156,180\nMaaa 28,156,176\nMvaa 188,156,172\nMvaa 156,156,168\nD0 9,24,44,3072\nMvaa 124,156,164\nMvaa 92,156,160\ns_waitcnt vmcnt(8) lgkmcnt(0)\n"
"s_barrier\nMvaa 64,192,224\nTa 128,12,32768\nMvaa 96,192,228\nMvaa 128,192,232\nTa 130,12,34816\nMvaa 160,192,236\ns_add_u32 m0, s48, 0\ns_add_u32 s45, s45, 128\nMaaa 0,192,240\n"
"Ta 132,13,32768\nMaaa 32,192,244\nD0 2,20,45,0\nMaaa 64,192,248\nTa 134,13,34816\nMaaa 96,192,252\nMaaa 100,196,252\nTa 136,14,32768\nMaaa 68,196,248\nMaaa 36,196,244\n"
"Ta 138,14,34816\nMaaa 4,196,240\nMvaa 164,196,236\nTa 140,15,32768\nMvaa 132,196,232\nD0 3,20,45,1024\nMvaa 100,196,228\nTa 142,15,34816\nMvaa 68,196,224\nMvaa 72,200,224\n"
"Ta 144,16,32768\nMvaa 104,200,228\nMvaa 136,200,232\nTa 146,16,34816\nMvaa 168,200,236\nMaaa 8,200,240\nTa 148,17,32768\nMaaa 40,200,244\nD0 4,20,45,2048\nMaaa 72,200,248\n"
"Ta 150,17,34816\nMaaa 104,200,252\nMaaa 108,204,252\nTa 152,18,32768\nMaaa 76,204,248\nMaaa 44,204,244\nTa 154,18,34816\nMaaa 12,204,240\nMvaa 172,204,236\nTa 156,19,32768\n"
"Mvaa 140,204,232\nD0 5,20,45,3072\nMvaa 108,204,228\nTa 158,19,34816\nMvaa 76,204,224\nMvaa 80,208,224\nRa 160,10,32768\nMvaa 112,208,228\nMvaa 144,208,232\nRa 164,10,34816\n"
"Mvaa 176,208,236\ns_add_u32 m0, s48, 4096\ns_add_u32 s46, s45, 0x10000\nMaaa 16,208,240\nRa 168,10,40960\nMaaa 48,208,244\nD0 2,20,46,0\nMaaa 80,208,248\nRa 172,10,43008\n"
"Maaa 112,208,252\nMaaa 116,212,252\nRa 176,10,49152\nMaaa 84,212,248\nMaaa 52,212,244\nRa 180,10,51200\nMaaa 20,212,240\nMvaa 180,212,236\nRa 184,10,57344\nMvaa 148,212,232\n"
"D0 3,20,46,1024\nMvaa 116,212,228\nRa 188,10,59392\nMvaa 84,212,224\nMvaa 88,216,224\nMvaa 120,216,228\nMvaa 152,216,232\nMvaa 184,216,236\nMaaa 24,216,240\nMaaa 56,216,244\n"
"D0 4,20,46,2048\nMaaa 88,216,248\nMaaa 120,216,252\nMaaa 124,220,252\nMaaa 92,220,248\nMaaa 60,220,244\nMaaa 28,220,240\nMvaa 188,220,236\nMvaa 156,220,232\nD0 5,20,46,3072\n"
"Mvaa 124,220,228\nMvaa 92,220,224\ns_waitcnt vmcnt(12) lgkmcnt(0)\ns_barrier\nMvaa 64,128,160\nTa 192,12,49152\nMvaa 96,128,164\nMvaa 128,128,168\nTa 194,12,51200\n"
"Mvaa 160,128,172\ns_add_u32 m0, s47, 16384\ns_add_u32 s44, s44, 0x40000\nMaaa 0,128,176\nTa 196,13,49152\nMaaa 32,128,180\nD0 6,24,44,0\nMaaa 64,128,184\nTa 198,13,51200\n"
"Maaa 96,128,188\nMaaa 100,132,188\nTa 200,14,49152\nMaaa 68,132,184\nbuffer_store_dword v25, off, s[16:19], s90 offset:0 sc0 sc1\nMaaa 36,132,180\nTa 202,14,51200\nMaaa 4,132,176\n"
"Mvaa 164,132,172\nTa 204,15,49152\nMvaa 132,132,168\nD0 7,24,44,1024\nMvaa 100,132,164\nTa 206,15,51200\nMvaa 68,132,160\nMvaa 72,136,160\nTa 208,16,49152\nMvaa 104,136,164\n"
"Mvaa 136,136,168\nTa 210,16,51200\nMvaa 168,136,172\nMaaa 8,136,176\nTa 212,17,49152\nMaaa 40,136,180\nD0 8,24,44,2048\nMaaa 72,136,184\nTa 214,17,51200\nMaaa 104,136,188\n"
"Maaa 108,140,188\nTa 216,18,49152\nMaaa 76,140,184\nMaaa 44,140,180\nTa 218,18,51200\nMaaa 12,140,176\nMvaa 172,140,172\nTa 220,19,49152\nMvaa 140,140,168\nD0 9,24,44,3072\n"
"Mvaa 108,140,164\nTa 222,19,51200\nMvaa 76,140,160\nMvaa 80,144,160\nRa 224,11,32768\nMvaa 112,144,164\nMvaa 144,144,168\nRa 228,11,34816\nMvaa 176,144,172\n"
"s_add_u32 m0, s47, 32768\ns_add_u32 s44, s44, 0x40000\nMaaa 16,144,176\nRa 232,11,40960\nMaaa 48,144,180\nD0 6,24,44,0\nMaaa 80,144,184\nRa 236,11,43008\nMaaa 112,144,188\n"
"Maaa 116,148,188\nRa 240,11,49152\nMaaa 84,148,184\nMaaa 52,148,180\nRa 244,11,51200\nMaaa 20,148,176\nMvaa 180,148,172\nRa 248,11,57344\nMvaa 148,148,168\nD0 7,24,44,1024\n"
"Mvaa 116,148,164\nRa 252,11,59392\nMvaa 84,148,160\nMvaa 88,152,160\nMvaa 120,152,164\nMvaa 152,152,168\nMvaa 184,152,172\nMaaa 24,152,176\nMaaa 56,152,180\nD0 8,24,44,2048\n"
"Maaa 88,152,184\nMaaa 120,152,188\nMaaa 124,156,188\nMaaa 92,156,184\nMaaa 60,156,180\nMaaa 28,156,176\nMvaa 188,156,172\nMvaa 156,156,168\nD0 9,24,44,3072\nMvaa 124,156,164\n"
"Mvaa 92,156,160\ns_waitcnt vmcnt(9) lgkmcnt(0)\ns_barrier\nMvaa 64,192,224\nTa 128,12,0\nMvaa 96,192,228\nMvaa 128,192,232\nTa 130,12,2048\nMvaa 160,192,236\n"
"s_add_u32 m0, s48, 32768\ns_add_u32 s45, s45, 128\nMaaa 0,192,240\nTa 132,13,0\nMaaa 32,192,244\nD0 2,20,45,0\nMaaa 64,192,248\nTa 134,13,2048\nMaaa 96,192,252\nMaaa 100,196,252\n"
"Ta 136,14,0\nMaaa 68,196,248\nMaaa 36,196,244\nTa 138,14,2048\nMaaa 4,196,240\nMvaa 164,196,236\nTa 140,15,0\nMvaa 132,196,232\nD0 3,20,45,1024\nMvaa 100,196,228\nTa 142,15,2048\n"
"Mvaa 68,196,224\nMvaa 72,200,224\nTa 144,16,0\nMvaa 104,200,228\nMvaa 136,200,232\nTa 146,16,2048\nMvaa 168,200,236\nMaaa 8,200,240\nTa 148,17,0\nMaaa 40,200,244\nD0 4,20,45,2048\n"
"Maaa 72,200,248\nTa 150,17,2048\nMaaa 104,200,252\nMaaa 108,204,252\nTa 152,18,0\nMaaa 76,204,248\nMaaa 44,204,244\nTa 154,18,2048\nMaaa 12,204,240\nMvaa 172,204,236\nTa 156,19,0\n"
"Mvaa 140,204,232\nD0 5,20,45,3072\nMvaa 108,204,228\nTa 158,19,2048\nMvaa 76,204,224\nMvaa 80,208,224\nRa 160,10,0\nMvaa 112,208,228\nMvaa 144,208,232\nRa 164,10,2048\n"
"Mvaa 176,208,236\ns_add_u32 m0, s48, 36864\ns_add_u32 s46, s45, 0x10000\nMaaa 16,208,240\nRa 168,10,8192\nMaaa 48,208,244\nD0 2,20,46,0\nMaaa 80,208,248\nRa 172,10,10240\n"
"Maaa 112,208,252\nMaaa 116,212,252\nRa 176,10,16384\nMaaa 84,212,248\nMaaa 52,212,244\nRa 180,10,18432\nMaaa 20,212,240\nMvaa 180,212,236\nRa 184,10,24576\nMvaa 148,212,232\n"
"D0 3,20,46,1024\nMvaa 116,212,228\nRa 188,10,26624\nMvaa 84,212,224\nMvaa 88,216,224\nMvaa 120,216,228\nMvaa 152,216,232\nMvaa 184,216,236\nMaaa 24,216,240\nMaaa 56,216,244\n"
"D0 4,20,46,2048\nMaaa 88,216,248\nMaaa 120,216,252\nMaaa 124,220,252\nMaaa 92,220,248\nMaaa 60,220,244\nMaaa 28,220,240\nMvaa 188,220,236\nMvaa 156,220,232\nD0 5,20,46,3072\n"
"Mvaa 124,220,228\nMvaa 92,220,224\ns_waitcnt vmcnt(12) lgkmcnt(0)\ns_barrier\nMvaa 64,128,160\nTa 192,12,16384\ns_and_b32 s2, s12, 1\nMvaa 96,128,164\ns_and_b32 s3, s13, 6\n"
"Mvaa 128,128,168\nTa 194,12,18432\nMvaa 160,128,172\ns_add_u32 m0, s47, 49152\ns_add_u32 s44, s44, 0x40000\nMaaa 0,128,176\nTa 196,13,16384\nMaaa 32,128,180\nD0 6,24,44,0\n"
"Maaa 64,128,184\nTa 198,13,18432\nMaaa 96,128,188\ns_add_u32 s2, s2, s3\nMaaa 100,132,188\nTa 200,14,16384\ns_lshl3_add_u32 s2, s14, s2\nMaaa 68,132,184\ns_cmp_lt_u32 s2, 14\n"
"s_cselect_b32 s5, 0, 0x40000000\nMaaa 36,132,180\nTa 202,14,18432\nMaaa 4,132,176\ns_mul_i32 s4, s73, 14\nMvaa 164,132,172\nTa 204,15,16384\nMvaa 132,132,168\nD0 7,24,44,1024\n"
"Mvaa 100,132,164\nTa 206,15,18432\nMvaa 68,132,160\ns_add_u32 s4, s4, s2\nMvaa 72,136,160\nTa 208,16,16384\ns_mov_b32 s36, s62\nMvaa 104,136,164\ns_and_b32 s37, s63, 0xffff\n"
"Mvaa 136,136,168\nTa 210,16,18432\nMvaa 168,136,172\ns_mov_b32 s38, 0x188000\nMaaa 8,136,176\nTa 212,17,16384\nMaaa 40,136,180\nD0 8,24,44,2048\nMaaa 72,136,184\nTa 214,17,18432\n"
"Maaa 104,136,188\ns_mov_b32 s39, 0x20000\nMaaa 108,140,188\nTa 216,18,16384\ns_mov_b32 s40, s64\nMaaa 76,140,184\ns_and_b32 s41, s65, 0xffff\nMaaa 44,140,180\nTa 218,18,18432\n"
"Maaa 12,140,176\ns_mov_b32 s42, 0x188000\nMvaa 172,140,172\nTa 220,19,16384\nMvaa 140,140,168\nD0 9,24,44,3072\nMvaa 108,140,164\nTa 222,19,18432\nMvaa 76,140,160\n"
"s_mov_b32 s43, 0x20000\nMvaa 80,144,160\nRa 224,11,0\ns_mov_b32 s92, s66\nMvaa 112,144,164\ns_and_b32 s93, s67, 0xffff\nMvaa 144,144,168\nRa 228,11,2048\nMvaa 176,144,172\n"
"s_add_u32 m0, s47, 0\ns_add_u32 s44, s44, 0x40000\nMaaa 16,144,176\nRa 232,11,8192\nMaaa 48,144,180\nD0 6,24,44,0\nMaaa 80,144,184\nRa 236,11,10240\nMaaa 112,144,188\n"
"s_mov_b32 s94, 0x188000\nMaaa 116,148,188\nRa 240,11,16384\ns_mov_b32 s95, 0x20000\nMaaa 84,148,184\nv_lshrrev_b32 v252, 1, v0\nMaaa 52,148,180\nRa 244,11,18432\nMaaa 20,148,176\n"
"v_mul_u32_u24 v253, 14336, v252\nMvaa 180,148,172\nRa 248,11,24576\nMvaa 148,148,168\nD0 7,24,44,1024\nMvaa 116,148,164\nRa 252,11,26624\nMvaa 84,148,160\nv_and_b32 v254, 1, v0\n"
"Mvaa 88,152,160\nv_lshl_add_u32 v253, v254, 4, v253\nv_mov_b32 v254, 0x40000000\nMvaa 120,152,164\nv_cmp_gt_u32 vcc, 56, v0\nv_lshlrev_b32 v252, 3, v252\n"
"s_mul_i32 s2, s11, 401408\nv_cndmask_b32 v253, v254, v253, vcc\nv_cndmask_b32 v255, v254, v252, vcc\nMvaa 152,152,168\ns_lshl_b32 s3, s4, 5\nMvaa 184,152,172\n"
"s_add_u32 s2, s2, s3\nMaaa 24,152,176\ns_or_b32 s86, s2, s5\nMaaa 56,152,180\nD0 8,24,44,2048\nMaaa 88,152,184\ns_add_u32 s87, s86, 7168\nMaaa 120,152,188\n"
"v_lshrrev_b32 v251, 4, v0\nMaaa 124,156,188\nv_mul_u32_u24 v252, 3584, v251\nv_and_b32 v251, 15, v0\nMaaa 92,156,184\nv_lshl_add_u32 v252, v251, 4, v252\n"
"v_cmp_gt_u32 vcc, 14, v251\ns_mul_i32 s2, s4, 14336\ns_mul_i32 s3, s11, 224\nv_cndmask_b32 v20, v254, v252, vcc\nMaaa 60,156,180\ns_add_u32 s2, s2, s3\nMaaa 28,156,176\n"
"s_or_b32 s88, s2, s5\nMvaa 188,156,172\nMvaa 156,156,168\nD0 9,24,44,3072\nMvaa 124,156,164\nMvaa 92,156,160\ns_waitcnt vmcnt(8) lgkmcnt(0)\ns_barrier\nMvaa 64,192,224\n"
"Ta 128,12,32768\nMvaa 96,192,228\nMvaa 128,192,232\nTa 130,12,34816\nMvaa 160,192,236\ns_add_u32 m0, s48, 0\ns_add_u32 s45, s45, 128\nMaaa 0,192,240\nTa 132,13,32768\n"
"Maaa 32,192,244\nD0 2,20,45,0\nMaaa 64,192,248\nTa 134,13,34816\nMaaa 96,192,252\nMaaa 100,196,252\nTa 136,14,32768\nMaaa 68,196,248\nMaaa 36,196,244\nTa 138,14,34816\n"
"Maaa 4,196,240\nMvaa 164,196,236\nTa 140,15,32768\nMvaa 132,196,232\nD0 3,20,45,1024\nMvaa 100,196,228\nTa 142,15,34816\nMvaa 68,196,224\nMvaa 72,200,224\nTa 144,16,32768\n"
"Mvaa 104,200,228\nMvaa 136,200,232\nTa 146,16,34816\nMvaa 168,200,236\nMaaa 8,200,240\nTa 148,17,32768\nMaaa 40,200,244\nD0 4,20,45,2048\nMaaa 72,200,248\nTa 150,17,34816\n"
"Maaa 104,200,252\nMaaa 108,204,252\nTa 152,18,32768\nMaaa 76,204,248\nMaaa 44,204,244\nTa 154,18,34816\nMaaa 12,204,240\nMvaa 172,204,236\nTa 156,19,32768\nMvaa 140,204,232\n"
"D0 5,20,45,3072\nMvaa 108,204,228\nTa 158,19,34816\nMvaa 76,204,224\nMvaa 80,208,224\nRa 160,10,32768\nMvaa 112,208,228\nMvaa 144,208,232\nRa 164,10,34816\nMvaa 176,208,236\n"
"s_add_u32 m0, s48, 4096\ns_add_u32 s46, s45, 0x10000\nMaaa 16,208,240\nRa 168,10,40960\nMaaa 48,208,244\nD0 2,20,46,0\nMaaa 80,208,248\nRa 172,10,43008\nMaaa 112,208,252\n"
"Maaa 116,212,252\nRa 176,10,49152\nMaaa 84,212,248\nMaaa 52,212,244\nRa 180,10,51200\nMaaa 20,212,240\nMvaa 180,212,236\nRa 184,10,57344\nMvaa 148,212,232\nD0 3,20,46,1024\n"
"Mvaa 116,212,228\nRa 188,10,59392\nMvaa 84,212,224\nMvaa 88,216,224\nMvaa 120,216,228\nMvaa 152,216,232\nMvaa 184,216,236\nMaaa 24,216,240\nMaaa 56,216,244\nD0 4,20,46,2048\n"
"Maaa 88,216,248\nMaaa 120,216,252\nMaaa 124,220,252\nMaaa 92,220,248\nMaaa 60,220,244\nMaaa 28,220,240\nMvaa 188,220,236\nMvaa 156,220,232\nD0 5,20,46,3072\nMvaa 124,220,228\n"
"Mvaa 92,220,224\ns_waitcnt vmcnt(12) lgkmcnt(0)\ns_barrier\nMvaa 64,128,160\nTa 192,12,49152\nMvaa 96,128,164\nMvaa 128,128,168\nTa 194,12,51200\nMvaa 160,128,172\n"
"s_add_u32 m0, s47, 16384\ns_add_u32 s44, s44, 0x40000\nMaaa 0,128,176\nTa 196,13,49152\nMaaa 32,128,180\nD0 6,24,44,0\nMaaa 64,128,184\nTa 198,13,51200\nMaaa 96,128,188\n"
"Maaa 100,132,188\nTa 200,14,49152\nMaaa 68,132,184\nMaaa 36,132,180\nTa 202,14,51200\nMaaa 4,132,176\nMvaa 164,132,172\nTa 204,15,49152\nMvaa 132,132,168\nD0 7,24,44,1024\n"
"Mvaa 100,132,164\nTa 206,15,51200\nMvaa 68,132,160\nMvaa 72,136,160\nTa 208,16,49152\nMvaa 104,136,164\nMvaa 136,136,168\nTa 210,16,51200\nMvaa 168,136,172\nMaaa 8,136,176\n"
"Ta 212,17,49152\nMaaa 40,136,180\nD0 8,24,44,2048\nMaaa 72,136,184\nTa 214,17,51200\nMaaa 104,136,188\nMaaa 108,140,188\nTa 216,18,49152\nMaaa 76,140,184\nMaaa 44,140,180\n"
"Ta 218,18,51200\nMaaa 12,140,176\nMvaa 172,140,172\nTa 220,19,49152\nMvaa 140,140,168\nD0 9,24,44,3072\nMvaa 108,140,164\nTa 222,19,51200\nMvaa 76,140,160\nMvaa 80,144,160\n"
"Ra 224,11,32768\nMvaa 112,144,164\nMvaa 144,144,168\nRa 228,11,34816\nMvaa 176,144,172\ns_add_u32 m0, s47, 32768\ns_add_u32 s44, s44, 0x40000\nMaaa 16,144,176\nRa 232,11,40960\n"
"Maaa 48,144,180\nD0 6,24,44,0\nMaaa 80,144,184\nRa 236,11,43008\nMaaa 112,144,188\nMaaa 116,148,188\nRa 240,11,49152\nMaaa 84,148,184\nMaaa 52,148,180\nRa 244,11,51200\n"
"Maaa 20,148,176\nMvaa 180,148,172\nRa 248,11,57344\nMvaa 148,148,168\nD0 7,24,44,1024\nMvaa 116,148,164\nRa 252,11,59392\nMvaa 84,148,160\nMvaa 88,152,160\nMvaa 120,152,164\n"
"Mvaa 152,152,168\nMvaa 184,152,172\nMaaa 24,152,176\nMaaa 56,152,180\nD0 8,24,44,2048\nMaaa 88,152,184\nMaaa 120,152,188\nMaaa 124,156,188\nMaaa 92,156,184\nMaaa 60,156,180\n"
"Maaa 28,156,176\nMvaa 188,156,172\nMvaa 156,156,168\nD0 9,24,44,3072\nMvaa 124,156,164\nMvaa 92,156,160\ns_waitcnt vmcnt(8) lgkmcnt(0)\ns_barrier\nMvaa 64,192,224\nTa 128,12,0\n"
"Mvaa 96,192,228\nbuffer_load_dwordx4 v[192:195], v253, s[36:39], s86 offen offset:0\nMvaa 128,192,232\nTa 130,12,2048\nMvaa 160,192,236\ns_add_u32 m0, s48, 32768\n"
"s_add_u32 s45, s45, 128\nMaaa 0,192,240\nTa 132,13,0\nMaaa 32,192,244\nD0 2,20,45,0\nMaaa 64,192,248\nTa 134,13,2048\nMaaa 96,192,252\nMaaa 100,196,252\nTa 136,14,0\n"
"Maaa 68,196,248\nMaaa 36,196,244\nTa 138,14,2048\nMaaa 4,196,240\nMvaa 164,196,236\nTa 140,15,0\nMvaa 132,196,232\nD0 3,20,45,1024\nMvaa 100,196,228\nTa 142,15,2048\n"
"Mvaa 68,196,224\nMvaa 72,200,224\nTa 144,16,0\nMvaa 104,200,228\nMvaa 136,200,232\nTa 146,16,2048\nMvaa 168,200,236\nMaaa 8,200,240\nTa 148,17,0\nMaaa 40,200,244\nD0 4,20,45,2048\n"
"Maaa 72,200,248\nTa 150,17,2048\nMaaa 104,200,252\nMaaa 108,204,252\nTa 152,18,0\nMaaa 76,204,248\nMaaa 44,204,244\nTa 154,18,2048\nMaaa 12,204,240\nMvaa 172,204,236\nTa 156,19,0\n"
"Mvaa 140,204,232\nD0 5,20,45,3072\nMvaa 108,204,228\nTa 158,19,2048\nMvaa 76,204,224\nMvaa 80,208,224\nRa 160,10,0\nMvaa 112,208,228\nMvaa 144,208,232\nRa 164,10,2048\n"
"Mvaa 176,208,236\ns_add_u32 m0, s48, 36864\ns_add_u32 s46, s45, 0x10000\nMaaa 16,208,240\nRa 168,10,8192\nMaaa 48,208,244\nD0 2,20,46,0\nMaaa 80,208,248\nRa 172,10,10240\n"
"Maaa 112,208,252\nMaaa 116,212,252\nRa 176,10,16384\nMaaa 84,212,248\nMaaa 52,212,244\nRa 180,10,18432\nMaaa 20,212,240\nMvaa 180,212,236\nRa 184,10,24576\nMvaa 148,212,232\n"
"D0 3,20,46,1024\nMvaa 116,212,228\nRa 188,10,26624\nMvaa 84,212,224\nMvaa 88,216,224\nMvaa 120,216,228\nMvaa 152,216,232\nMvaa 184,216,236\nMaaa 24,216,240\nMaaa 56,216,244\n"
"D0 4,20,46,2048\nMaaa 88,216,248\nMaaa 120,216,252\nMaaa 124,220,252\nMaaa 92,220,248\nMaaa 60,220,244\nMaaa 28,220,240\nMvaa 188,220,236\nMvaa 156,220,232\nD0 5,20,46,3072\n"
"Mvaa 124,220,228\nMvaa 92,220,224\ns_waitcnt vmcnt(13) lgkmcnt(0)\ns_barrier\nMvaa 64,128,160\nTa 192,12,16384\nMvaa 96,128,164\n"
"buffer_load_dwordx4 v[208:211], v253, s[40:43], s86 offen offset:0\nMvaa 128,128,168\nTa 194,12,18432\nMvaa 160,128,172\ns_add_u32 m0, s47, 49152\ns_add_u32 s44, s44, 0x40000\n"
"Maaa 0,128,176\nTa 196,13,16384\nMaaa 32,128,180\nD0 6,24,44,0\nMaaa 64,128,184\nTa 198,13,18432\nMaaa 96,128,188\nMaaa 100,132,188\nTa 200,14,16384\nMaaa 68,132,184\n"
"Maaa 36,132,180\nTa 202,14,18432\nMaaa 4,132,176\nMvaa 164,132,172\nTa 204,15,16384\nMvaa 132,132,168\nD0 7,24,44,1024\nMvaa 100,132,164\nTa 206,15,18432\nMvaa 68,132,160\n"
"Mvaa 72,136,160\nTa 208,16,16384\nMvaa 104,136,164\nMvaa 136,136,168\nTa 210,16,18432\nMvaa 168,136,172\nMaaa 8,136,176\nTa 212,17,16384\nMaaa 40,136,180\nD0 8,24,44,2048\n"
"Maaa 72,136,184\nTa 214,17,18432\nMaaa 104,136,188\nMaaa 108,140,188\nTa 216,18,16384\nMaaa 76,140,184\nMaaa 44,140,180\nTa 218,18,18432\nMaaa 12,140,176\nMvaa 172,140,172\n"
"Ta 220,19,16384\nMvaa 140,140,168\nD0 9,24,44,3072\nMvaa 108,140,164\nTa 222,19,18432\nMvaa 76,140,160\nMvaa 80,144,160\nRa 224,11,0\nMvaa 112,144,164\nMvaa 144,144,168\n"
"Ra 228,11,2048\nMvaa 176,144,172\ns_add_u32 m0, s47, 0\ns_add_u32 s44, s44, 0x40000\nMaaa 16,144,176\nRa 232,11,8192\nMaaa 48,144,180\nD0 6,24,44,0\nMaaa 80,144,184\n"
"Ra 236,11,10240\nMaaa 112,144,188\nMaaa 116,148,188\nRa 240,11,16384\nMaaa 84,148,184\nMaaa 52,148,180\nRa 244,11,18432\nMaaa 20,148,176\nMvaa 180,148,172\nRa 248,11,24576\n"
"Mvaa 148,148,168\nD0 7,24,44,1024\nMvaa 116,148,164\nRa 252,11,26624\nMvaa 84,148,160\nMvaa 88,152,160\nMvaa 120,152,164\nMvaa 152,152,168\nMvaa 184,152,172\nMaaa 24,152,176\n"
"Maaa 56,152,180\nD0 8,24,44,2048\nMaaa 88,152,184\nMaaa 120,152,188\nMaaa 124,156,188\nMaaa 92,156,184\nMaaa 60,156,180\nMaaa 28,156,176\nMvaa 188,156,172\nMvaa 156,156,168\n"
"D0 9,24,44,3072\nMvaa 124,156,164\nMvaa 92,156,160\ns_waitcnt vmcnt(9) lgkmcnt(0)\ns_barrier\nMvaa 64,192,224\nTa 128,12,32768\ns_lshl_b32 s6, s11, 12\n"
"s_add_u32 s6, s6, 0xc044000\nMvaa 96,192,228\nbuffer_load_dwordx4 v[196:199], v253, s[36:39], s86 offen offset:3584\nMvaa 128,192,232\nTa 130,12,34816\nMvaa 160,192,236\n"
"s_add_u32 m0, s48, 0\ns_add_u32 s45, s45, 128\nMaaa 0,192,240\nTa 132,13,32768\nMaaa 32,192,244\nD0 2,20,45,0\nMaaa 64,192,248\nTa 134,13,34816\nMaaa 96,192,252\n"
"buffer_load_dwordx4 v[32:35], v1, s[16:19], s6 offen offset:0 sc0 sc1\nMaaa 100,196,252\nTa 136,14,32768\nMaaa 68,196,248\nMaaa 36,196,244\nTa 138,14,34816\nMaaa 4,196,240\n"
"buffer_load_dwordx4 v[36:39], v1, s[16:19], s6 offen offset:1024 sc0 sc1\nMvaa 164,196,236\nTa 140,15,32768\nMvaa 132,196,232\nD0 3,20,45,1024\nMvaa 100,196,228\nTa 142,15,34816\n"
"Mvaa 68,196,224\nbuffer_load_dwordx4 v[40:43], v1, s[16:19], s6 offen offset:2048 sc0 sc1\nMvaa 72,200,224\nTa 144,16,32768\nMvaa 104,200,228\nMvaa 136,200,232\nTa 146,16,34816\n"
"Mvaa 168,200,236\nbuffer_load_dwordx4 v[44:47], v1, s[16:19], s6 offen offset:3072 sc0 sc1\nMaaa 8,200,240\nTa 148,17,32768\nMaaa 40,200,244\nD0 4,20,45,2048\nMaaa 72,200,248\n"
"Ta 150,17,34816\nMaaa 104,200,252\nMaaa 108,204,252\nTa 152,18,32768\nMaaa 76,204,248\nMaaa 44,204,244\nTa 154,18,34816\nMaaa 12,204,240\nMvaa 172,204,236\nTa 156,19,32768\n"
"Mvaa 140,204,232\nD0 5,20,45,3072\nMvaa 108,204,228\nTa 158,19,34816\nMvaa 76,204,224\nMvaa 80,208,224\nRa 160,10,32768\nMvaa 112,208,228\nMvaa 144,208,232\nRa 164,10,34816\n"
"Mvaa 176,208,236\ns_add_u32 m0, s48, 4096\ns_add_u32 s46, s45, 0x10000\nMaaa 16,208,240\nRa 168,10,40960\nMaaa 48,208,244\nD0 2,20,46,0\nMaaa 80,208,248\nRa 172,10,43008\n"
"Maaa 112,208,252\nMaaa 116,212,252\nRa 176,10,49152\nMaaa 84,212,248\nMaaa 52,212,244\nRa 180,10,51200\nMaaa 20,212,240\nMvaa 180,212,236\nRa 184,10,57344\nMvaa 148,212,232\n"
"D0 3,20,46,1024\nMvaa 116,212,228\nRa 188,10,59392\nMvaa 84,212,224\nMvaa 88,216,224\nMvaa 120,216,228\nMvaa 152,216,232\nMvaa 184,216,236\nMaaa 24,216,240\nMaaa 56,216,244\n"
"D0 4,20,46,2048\nMaaa 88,216,248\nMaaa 120,216,252\nMaaa 124,220,252\nMaaa 92,220,248\nMaaa 60,220,244\nMaaa 28,220,240\nMvaa 188,220,236\nMvaa 156,220,232\nD0 5,20,46,3072\n"
"Mvaa 124,220,228\nMvaa 92,220,224\ns_waitcnt vmcnt(17) lgkmcnt(0)\ns_barrier\nMvaa 64,128,160\nTa 192,12,49152\nMvaa 96,128,164\n"
"buffer_load_dwordx4 v[212:215], v253, s[40:43], s86 offen offset:3584\nMvaa 128,128,168\nTa 194,12,51200\nMvaa 160,128,172\ns_add_u32 m0, s47, 16384\ns_add_u32 s44, s44, 0x40000\n"
"Maaa 0,128,176\nTa 196,13,49152\nMaaa 32,128,180\nD0 6,24,44,0\nMaaa 64,128,184\nTa 198,13,51200\nMaaa 96,128,188\nMaaa 100,132,188\nTa 200,14,49152\nMaaa 68,132,184\n"
"Maaa 36,132,180\nTa 202,14,51200\nMaaa 4,132,176\nMvaa 164,132,172\nTa 204,15,49152\nMvaa 132,132,168\nD0 7,24,44,1024\nMvaa 100,132,164\nTa 206,15,51200\nMvaa 68,132,160\n"
"Mvaa 72,136,160\nTa 208,16,49152\nMvaa 104,136,164\nMvaa 136,136,168\nTa 210,16,51200\nMvaa 168,136,172\nMaaa 8,136,176\nTa 212,17,49152\nMaaa 40,136,180\nD0 8,24,44,2048\n"
"Maaa 72,136,184\nTa 214,17,51200\nMaaa 104,136,188\nMaaa 108,140,188\nTa 216,18,49152\nMaaa 76,140,184\nMaaa 44,140,180\nTa 218,18,51200\nMaaa 12,140,176\nMvaa 172,140,172\n"
"Ta 220,19,49152\nMvaa 140,140,168\nD0 9,24,44,3072\nMvaa 108,140,164\nTa 222,19,51200\nMvaa 76,140,160\nMvaa 80,144,160\nRa 224,11,32768\nMvaa 112,144,164\nMvaa 144,144,168\n"
"Ra 228,11,34816\nMvaa 176,144,172\ns_add_u32 m0, s47, 32768\ns_add_u32 s44, s44, 0x40000\nMaaa 16,144,176\nRa 232,11,40960\nMaaa 48,144,180\nD0 6,24,44,0\nMaaa 80,144,184\n"
"Ra 236,11,43008\nMaaa 112,144,188\nMaaa 116,148,188\nRa 240,11,49152\nMaaa 84,148,184\nMaaa 52,148,180\nRa 244,11,51200\nMaaa 20,148,176\nMvaa 180,148,172\nRa 248,11,57344\n"
"Mvaa 148,148,168\nD0 7,24,44,1024\nMvaa 116,148,164\nRa 252,11,59392\nMvaa 84,148,160\nMvaa 88,152,160\nMvaa 120,152,164\nMvaa 152,152,168\nMvaa 184,152,172\nMaaa 24,152,176\n"
"Maaa 56,152,180\nD0 8,24,44,2048\nMaaa 88,152,184\nMaaa 120,152,188\nMaaa 124,156,188\nMaaa 92,156,184\nMaaa 60,156,180\nMaaa 28,156,176\nMvaa 188,156,172\nMvaa 156,156,168\n"
"D0 9,24,44,3072\nMvaa 124,156,164\nMvaa 92,156,160\ns_waitcnt vmcnt(9) lgkmcnt(0)\ns_barrier\nMvaa 64,192,224\nTa 128,12,0\nMvaa 96,192,228\n"
"buffer_load_dwordx4 v[200:203], v253, s[36:39], s87 offen offset:0\nMvaa 128,192,232\nTa 130,12,2048\nMvaa 160,192,236\ns_add_u32 m0, s48, 32768\ns_add_u32 s45, s45, 128\n"
"Maaa 0,192,240\nTa 132,13,0\nMaaa 32,192,244\nD0 2,20,45,0\nMaaa 64,192,248\nTa 134,13,2048\nMaaa 96,192,252\nMaaa 100,196,252\nTa 136,14,0\nMaaa 68,196,248\nMaaa 36,196,244\n"
"Ta 138,14,2048\nMaaa 4,196,240\nMvaa 164,196,236\nTa 140,15,0\nMvaa 132,196,232\nD0 3,20,45,1024\nMvaa 100,196,228\nTa 142,15,2048\nMvaa 68,196,224\nMvaa 72,200,224\nTa 144,16,0\n"
"Mvaa 104,200,228\nMvaa 136,200,232\nTa 146,16,2048\nMvaa 168,200,236\nMaaa 8,200,240\nTa 148,17,0\nMaaa 40,200,244\nD0 4,20,45,2048\nMaaa 72,200,248\nTa 150,17,2048\n"
"Maaa 104,200,252\nMaaa 108,204,252\nTa 152,18,0\nMaaa 76,204,248\nMaaa 44,204,244\nTa 154,18,2048\nMaaa 12,204,240\nMvaa 172,204,236\nTa 156,19,0\nMvaa 140,204,232\n"
"D0 5,20,45,3072\nMvaa 108,204,228\nTa 158,19,2048\nMvaa 76,204,224\nMvaa 80,208,224\nRa 160,10,0\nMvaa 112,208,228\nMvaa 144,208,232\nRa 164,10,2048\nMvaa 176,208,236\n"
"s_add_u32 m0, s48, 36864\ns_add_u32 s46, s45, 0x10000\nMaaa 16,208,240\nRa 168,10,8192\nMaaa 48,208,244\nD0 2,20,46,0\nMaaa 80,208,248\nRa 172,10,10240\nMaaa 112,208,252\n"
"Maaa 116,212,252\nRa 176,10,16384\nMaaa 84,212,248\nMaaa 52,212,244\nRa 180,10,18432\nMaaa 20,212,240\nMvaa 180,212,236\nRa 184,10,24576\nMvaa 148,212,232\nD0 3,20,46,1024\n"
"Mvaa 116,212,228\nRa 188,10,26624\nMvaa 84,212,224\nMvaa 88,216,224\nMvaa 120,216,228\nMvaa 152,216,232\nMvaa 184,216,236\nMaaa 24,216,240\nMaaa 56,216,244\nD0 4,20,46,2048\n"
"Maaa 88,216,248\nMaaa 120,216,252\nMaaa 124,220,252\nMaaa 92,220,248\nMaaa 60,220,244\nMaaa 28,220,240\nMvaa 188,220,236\nMvaa 156,220,232\nD0 5,20,46,3072\nMvaa 124,220,228\n"
"Mvaa 92,220,224\ns_waitcnt vmcnt(13) lgkmcnt(0)\ns_barrier\nMvaa 64,128,160\nTa 192,12,16384\nMvaa 96,128,164\nbuffer_load_dwordx4 v[216:219], v253, s[40:43], s87 offen offset:0\n"
"Mvaa 128,128,168\nTa 194,12,18432\nMvaa 160,128,172\ns_add_u32 m0, s47, 49152\ns_add_u32 s44, s44, 0x40000\nMaaa 0,128,176\nTa 196,13,16384\nMaaa 32,128,180\nD0 6,24,44,0\n"
"Maaa 64,128,184\nTa 198,13,18432\nMaaa 96,128,188\nv_xor_b32 v48, s15, v33\nMaaa 100,132,188\nTa 200,14,16384\nMaaa 68,132,184\nv_xor_b32 v49, s15, v35\nMaaa 36,132,180\n"
"Ta 202,14,18432\nMaaa 4,132,176\nv_xor_b32 v50, s15, v37\nMvaa 164,132,172\nTa 204,15,16384\nMvaa 132,132,168\nD0 7,24,44,1024\nMvaa 100,132,164\nTa 206,15,18432\nMvaa 68,132,160\n"
"v_xor_b32 v51, s15, v39\nMvaa 72,136,160\nTa 208,16,16384\nMvaa 104,136,164\nv_xor_b32 v52, s15, v41\nMvaa 136,136,168\nTa 210,16,18432\nMvaa 168,136,172\nv_xor_b32 v53, s15, v43\n"
"Maaa 8,136,176\nTa 212,17,16384\nMaaa 40,136,180\nD0 8,24,44,2048\nMaaa 72,136,184\nTa 214,17,18432\nMaaa 104,136,188\nv_xor_b32 v54, s15, v45\nMaaa 108,140,188\nTa 216,18,16384\n"
"Maaa 76,140,184\nv_xor_b32 v55, s15, v47\nMaaa 44,140,180\nTa 218,18,18432\nMaaa 12,140,176\nv_or3_b32 v48, v48, v49, v50\nMvaa 172,140,172\nTa 220,19,16384\nMvaa 140,140,168\n"
"D0 9,24,44,3072\nMvaa 108,140,164\nTa 222,19,18432\nMvaa 76,140,160\nv_or3_b32 v51, v51, v52, v53\nMvaa 80,144,160\nRa 224,11,0\nMvaa 112,144,164\nv_or3_b32 v48, v48, v54, v55\n"
"Mvaa 144,144,168\nRa 228,11,2048\nMvaa 176,144,172\ns_add_u32 m0, s47, 0\ns_add_u32 s44, s44, 0x40000\nMaaa 16,144,176\nRa 232,11,8192\nMaaa 48,144,180\nD0 6,24,44,0\n"
"Maaa 80,144,184\nRa 236,11,10240\nMaaa 112,144,188\nv_or_b32 v48, v48, v51\nMaaa 116,148,188\nRa 240,11,16384\nMaaa 84,148,184\nv_cmp_ne_u32 vcc, 0, v48\nMaaa 52,148,180\n"
"Ra 244,11,18432\nMaaa 20,148,176\ns_cbranch_vccnz .S14\n.S15:\nMvaa 180,148,172\nRa 248,11,24576\nMvaa 148,148,168\nD0 7,24,44,1024\nMvaa 116,148,164\nRa 252,11,26624\n"
"Mvaa 84,148,160\nv_max_u32 v48, v32, v34\nMvaa 88,152,160\nv_min_u32 v49, v32, v34\nMvaa 120,152,164\nv_med3_u32 v49, v48, v49, v36\nMvaa 152,152,168\nv_max_u32 v48, v48, v36\n"
"Mvaa 184,152,172\nv_med3_u32 v49, v48, v49, v38\nMaaa 24,152,176\nv_max_u32 v48, v48, v38\nMaaa 56,152,180\nD0 8,24,44,2048\nMaaa 88,152,184\nv_med3_u32 v49, v48, v49, v40\n"
"Maaa 120,152,188\nv_max_u32 v48, v48, v40\nMaaa 124,156,188\nv_med3_u32 v49, v48, v49, v42\nMaaa 92,156,184\nv_max_u32 v48, v48, v42\nMaaa 60,156,180\n"
"v_med3_u32 v49, v48, v49, v44\nMaaa 28,156,176\nv_max_u32 v48, v48, v44\nMvaa 188,156,172\nv_med3_u32 v49, v48, v49, v46\nMvaa 156,156,168\nD0 9,24,44,3072\nMvaa 124,156,164\n"
"v_max_u32 v48, v48, v46\nMvaa 92,156,160\ns_waitcnt vmcnt(9) lgkmcnt(0)\ns_barrier\nMvaa 64,192,224\nTa 128,12,32768\nMvaa 96,192,228\n"
"buffer_load_dwordx4 v[204:207], v253, s[36:39], s87 offen offset:3584\nMvaa 128,192,232\nTa 130,12,34816\nMvaa 160,192,236\ns_add_u32 m0, s48, 0\ns_add_u32 s45, s45, 128\n"
"Maaa 0,192,240\nTa 132,13,32768\nMaaa 32,192,244\nD0 2,20,45,0\nMaaa 64,192,248\nTa 134,13,34816\nMaaa 96,192,252\nv_mov_b32 v50, v48\nMaaa 100,196,252\nTa 136,14,32768\n"
"Maaa 68,196,248\nv_max_u32_dpp v50, v50, v50 row_ror:8 row_mask:0xf bank_mask:0xf\nMaaa 36,196,244\nTa 138,14,34816\nMaaa 4,196,240\n"
"v_max_u32_dpp v50, v50, v50 row_ror:4 row_mask:0xf bank_mask:0xf\nMvaa 164,196,236\nTa 140,15,32768\nMvaa 132,196,232\nD0 3,20,45,1024\nMvaa 100,196,228\nTa 142,15,34816\n"
"Mvaa 68,196,224\nv_max_u32_dpp v50, v50, v50 row_ror:2 row_mask:0xf bank_mask:0xf\nMvaa 72,200,224\nTa 144,16,32768\nMvaa 104,200,228\n"
"v_max_u32_dpp v50, v50, v50 row_ror:1 row_mask:0xf bank_mask:0xf\nMvaa 136,200,232\nTa 146,16,34816\nMvaa 168,200,236\nv_mov_b32 v52, v50\nMaaa 8,200,240\nTa 148,17,32768\n"
"Maaa 40,200,244\nD0 4,20,45,2048\nMaaa 72,200,248\nTa 150,17,34816\nMaaa 104,200,252\nv_permlane16_swap_b32 v50, v52\nMaaa 108,204,252\nTa 152,18,32768\nMaaa 76,204,248\n"
"v_max_u32 v50, v50, v52\nMaaa 44,204,244\nTa 154,18,34816\nMaaa 12,204,240\nv_mov_b32 v52, v50\nMvaa 172,204,236\nTa 156,19,32768\nMvaa 140,204,232\nD0 5,20,45,3072\n"
"Mvaa 108,204,228\nTa 158,19,34816\nMvaa 76,204,224\nv_permlane32_swap_b32 v50, v52\nMvaa 80,208,224\nRa 160,10,32768\nMvaa 112,208,228\nv_max_u32 v50, v50, v52\n"
"v_cmp_eq_u32 vcc, v48, v50\ns_lshl_b32 s2, s11, 3\ns_add_u32 s2, s2, 131072\nv_cndmask_b32 v51, v48, v49, vcc\nMvaa 144,208,232\nRa 164,10,34816\nMvaa 176,208,236\n"
"s_add_u32 m0, s48, 4096\ns_add_u32 s46, s45, 0x10000\nMaaa 16,208,240\nRa 168,10,40960\nMaaa 48,208,244\nD0 2,20,46,0\nMaaa 80,208,248\nRa 172,10,43008\nMaaa 112,208,252\n"
"v_max_u32_dpp v51, v51, v51 row_ror:8 row_mask:0xf bank_mask:0xf\nMaaa 116,212,252\nRa 176,10,49152\nMaaa 84,212,248\n"
"v_max_u32_dpp v51, v51, v51 row_ror:4 row_mask:0xf bank_mask:0xf\nMaaa 52,212,244\nRa 180,10,51200\nMaaa 20,212,240\n"
"v_max_u32_dpp v51, v51, v51 row_ror:2 row_mask:0xf bank_mask:0xf\nMvaa 180,212,236\nRa 184,10,57344\nMvaa 148,212,232\nD0 3,20,46,1024\nMvaa 116,212,228\nRa 188,10,59392\n"
"Mvaa 84,212,224\nv_max_u32_dpp v51, v51, v51 row_ror:1 row_mask:0xf bank_mask:0xf\nMvaa 88,216,224\nv_mov_b32 v52, v51\nMvaa 120,216,228\nMvaa 152,216,232\n"
"v_permlane16_swap_b32 v51, v52\nMvaa 184,216,236\nv_max_u32 v51, v51, v52\nMaaa 24,216,240\nv_mov_b32 v52, v51\nMaaa 56,216,244\nD0 4,20,46,2048\nMaaa 88,216,248\n"
"v_permlane32_swap_b32 v51, v52\nMaaa 120,216,252\nv_max_u32 v51, v51, v52\nMaaa 124,220,252\nv_mov_b32 v52, s2\nMaaa 92,220,248\nds_write_b64 v52, v[50:51]\nMaaa 60,220,244\n"
"v_mov_b32 v52, 131072\nMaaa 28,220,240\nMvaa 188,220,236\nMvaa 156,220,232\nD0 5,20,46,3072\nMvaa 124,220,228\nMvaa 92,220,224\ns_waitcnt vmcnt(13) lgkmcnt(0)\ns_barrier\n"
"Mvaa 64,128,160\nTa 192,12,49152\nMvaa 96,128,164\nbuffer_load_dwordx4 v[220:223], v253, s[40:43], s87 offen offset:3584\nMvaa 128,128,168\nTa 194,12,51200\nMvaa 160,128,172\n"
"s_add_u32 m0, s47, 16384\ns_add_u32 s44, s44, 0x40000\nMaaa 0,128,176\nTa 196,13,49152\nMaaa 32,128,180\nD0 6,24,44,0\nMaaa 64,128,184\nTa 198,13,51200\nMaaa 96,128,188\n"
"Rv 32,52,0\nMaaa 100,132,188\nTa 200,14,49152\nMaaa 68,132,184\nRv 36,52,16\nMaaa 36,132,180\nTa 202,14,51200\nMaaa 4,132,176\nMvaa 164,132,172\nTa 204,15,49152\nMvaa 132,132,168\n"
"D0 7,24,44,1024\nMvaa 100,132,164\nTa 206,15,51200\nMvaa 68,132,160\nMvaa 72,136,160\nTa 208,16,49152\nMvaa 104,136,164\nMvaa 136,136,168\nTa 210,16,51200\nMvaa 168,136,172\n"
"Maaa 8,136,176\nTa 212,17,49152\nMaaa 40,136,180\nD0 8,24,44,2048\nMaaa 72,136,184\nTa 214,17,51200\nMaaa 104,136,188\nWL 7\nMaaa 108,140,188\nTa 216,18,49152\nMaaa 76,140,184\n"
"v_max_u32 v48, v32, v33\nMaaa 44,140,180\nTa 218,18,51200\nMaaa 12,140,176\nv_min_u32 v49, v32, v33\nMvaa 172,140,172\nTa 220,19,49152\nMvaa 140,140,168\nD0 9,24,44,3072\n"
"Mvaa 108,140,164\nTa 222,19,51200\nMvaa 76,140,160\nv_med3_u32 v49, v48, v49, v34\nMvaa 80,144,160\nRa 224,11,32768\nMvaa 112,144,164\nv_max_u32 v48, v48, v34\nMvaa 144,144,168\n"
"Ra 228,11,34816\nMvaa 176,144,172\ns_add_u32 m0, s47, 32768\ns_add_u32 s44, s44, 0x40000\nMaaa 16,144,176\nRa 232,11,40960\nMaaa 48,144,180\nD0 6,24,44,0\nMaaa 80,144,184\n"
"Ra 236,11,43008\nMaaa 112,144,188\nv_med3_u32 v49, v48, v49, v35\nMaaa 116,148,188\nRa 240,11,49152\nMaaa 84,148,184\nv_max_u32 v48, v48, v35\nMaaa 52,148,180\nRa 244,11,51200\n"
"Maaa 20,148,176\nv_med3_u32 v49, v48, v49, v36\nMvaa 180,148,172\nRa 248,11,57344\nMvaa 148,148,168\nD0 7,24,44,1024\nMvaa 116,148,164\nRa 252,11,59392\nMvaa 84,148,160\n"
"v_max_u32 v48, v48, v36\nMvaa 88,152,160\nv_med3_u32 v49, v48, v49, v37\nMvaa 120,152,164\nv_max_u32 v48, v48, v37\nMvaa 152,152,168\nv_med3_u32 v49, v48, v49, v38\n"
"Mvaa 184,152,172\nv_max_u32 v48, v48, v38\nMaaa 24,152,176\nv_med3_u32 v49, v48, v49, v39\nMaaa 56,152,180\nD0 8,24,44,2048\nMaaa 88,152,184\nv_max_u32 v48, v48, v39\n"
"Maaa 120,152,188\nv_readfirstlane_b32 s2, v48\nMaaa 124,156,188\nv_readfirstlane_b32 s3, v49\nMaaa 92,156,184\nMaaa 60,156,180\nMaaa 28,156,176\ns_andn2_b32 s2, 0x7ff, s2\n"
"Mvaa 188,156,172\ns_andn2_b32 s3, 0x7ff, s3\nMvaa 156,156,168\nD0 9,24,44,3072\nMvaa 124,156,164\ns_lshl_b32 s71, s3, 16\nMvaa 92,156,160\ns_waitcnt vmcnt(9) lgkmcnt(0)\n"
"s_barrier\nMvaa 64,192,224\nTa 128,12,0\nMvaa 96,192,228\nbuffer_load_dwordx4 v[224:227], v20, s[92:95], s88 offen offset:0\nMvaa 128,192,232\nTa 130,12,2048\nMvaa 160,192,236\n"
"s_add_u32 m0, s48, 32768\ns_add_u32 s45, s45, 128\nMaaa 0,192,240\nTa 132,13,0\nMaaa 32,192,244\nD0 2,20,45,0\nMaaa 64,192,248\nTa 134,13,2048\nMaaa 96,192,252\n"
"s_or_b32 s71, s71, s2\nMaaa 100,196,252\nTa 136,14,0\nMaaa 68,196,248\ns_mul_i32 s2, s2, 896\nMaaa 36,196,244\nTa 138,14,2048\nMaaa 4,196,240\ns_mul_i32 s3, s3, 896\n"
"Mvaa 164,196,236\nTa 140,15,0\nMvaa 132,196,232\nD0 3,20,45,1024\nMvaa 100,196,228\nTa 142,15,2048\nMvaa 68,196,224\ns_mul_i32 s6, s11, 224\nMvaa 72,200,224\nTa 144,16,0\n"
"Mvaa 104,200,228\ns_add_u32 s2, s2, s6\nMvaa 136,200,232\nTa 146,16,2048\nMvaa 168,200,236\ns_add_u32 s3, s3, s6\nMaaa 8,200,240\nTa 148,17,0\nMaaa 40,200,244\nD0 4,20,45,2048\n"
"Maaa 72,200,248\nTa 150,17,2048\nMaaa 104,200,252\nbuffer_load_dwordx2 v[240:241], v255, s[28:31], s2 offen\nMaaa 108,204,252\nTa 152,18,0\nMaaa 76,204,248\nMaaa 44,204,244\n"
"Ta 154,18,2048\nMaaa 12,204,240\nbuffer_load_dwordx2 v[242:243], v255, s[28:31], s3 offen\ns_lshl2_add_u32 s2, s10, s11\ns_cmp_eq_u32 s2, 0\ns_cselect_b32 s2, 0, 0x40000000\n"
"s_add_u32 s2, s2, 0xc048000\nMvaa 172,204,236\nTa 156,19,0\nMvaa 140,204,232\nD0 5,20,45,3072\nMvaa 108,204,228\nTa 158,19,2048\nMvaa 76,204,224\n"
"buffer_store_dword v25, off, s[16:19], s2 offset:0 sc0 sc1\nMvaa 80,208,224\nRa 160,10,0\nMvaa 112,208,228\nbuffer_load_dwordx4 v[228:231], v20, s[92:95], s88 offen offset:896\n"
"Mvaa 144,208,232\nRa 164,10,2048\nMvaa 176,208,236\ns_add_u32 m0, s48, 36864\ns_add_u32 s46, s45, 0x10000\nMaaa 16,208,240\nRa 168,10,8192\nMaaa 48,208,244\nD0 2,20,46,0\n"
"Maaa 80,208,248\nRa 172,10,10240\nMaaa 112,208,252\nMaaa 116,212,252\nRa 176,10,16384\nMaaa 84,212,248\nMaaa 52,212,244\nRa 180,10,18432\nMaaa 20,212,240\nMvaa 180,212,236\n"
"Ra 184,10,24576\nMvaa 148,212,232\nD0 3,20,46,1024\nMvaa 116,212,228\nRa 188,10,26624\nMvaa 84,212,224\nMvaa 88,216,224\nMvaa 120,216,228\nMvaa 152,216,232\nMvaa 184,216,236\n"
"Maaa 24,216,240\nMaaa 56,216,244\nD0 4,20,46,2048\nMaaa 88,216,248\nMaaa 120,216,252\nMaaa 124,220,252\nMaaa 92,220,248\nMaaa 60,220,244\nMaaa 28,220,240\nMvaa 188,220,236\n"
"Mvaa 156,220,232\nD0 5,20,46,3072\nMvaa 124,220,228\nMvaa 92,220,224\ns_waitcnt vmcnt(17) lgkmcnt(0)\ns_barrier\nMvaa 64,128,160\nTa 192,12,16384\nMvaa 96,128,164\n"
"buffer_load_dwordx4 v[232:235], v20, s[92:95], s88 offen offset:1792\nMvaa 128,128,168\nTa 194,12,18432\nMvaa 160,128,172\ns_add_u32 m0, s47, 49152\ns_add_u32 s44, s44, 0x40000\n"
"Maaa 0,128,176\nTa 196,13,16384\nMaaa 32,128,180\nD0 6,24,44,0\nMaaa 64,128,184\nTa 198,13,18432\nMaaa 96,128,188\nMaaa 100,132,188\nTa 200,14,16384\nMaaa 68,132,184\n"
"Maaa 36,132,180\nTa 202,14,18432\nMaaa 4,132,176\nMvaa 164,132,172\nTa 204,15,16384\nMvaa 132,132,168\nD0 7,24,44,1024\nMvaa 100,132,164\nTa 206,15,18432\nMvaa 68,132,160\n"
"Mvaa 72,136,160\nTa 208,16,16384\nMvaa 104,136,164\nMvaa 136,136,168\nTa 210,16,18432\nMvaa 168,136,172\nMaaa 8,136,176\nTa 212,17,16384\nMaaa 40,136,180\nD0 8,24,44,2048\n"
"Maaa 72,136,184\nTa 214,17,18432\nMaaa 104,136,188\nMaaa 108,140,188\nTa 216,18,16384\nMaaa 76,140,184\nMaaa 44,140,180\nTa 218,18,18432\nMaaa 12,140,176\nMvaa 172,140,172\n"
"Ta 220,19,16384\nMvaa 140,140,168\nD0 9,24,44,3072\nMvaa 108,140,164\nTa 222,19,18432\nMvaa 76,140,160\nMvaa 80,144,160\nRa 224,11,0\nMvaa 112,144,164\n"
"buffer_load_dwordx4 v[236:239], v20, s[92:95], s88 offen offset:2688\ns_lshl_b32 s5, s73, 7\ns_add_u32 s5, s5, 0xc04ac00\nv_bfe_u32 v27, v1, 2, 7\n"
"buffer_load_dword v27, v27, s[16:19], s5 offen sc0 sc1\nMvaa 144,144,168\nRa 228,11,2048\nMvaa 176,144,172\ns_add_u32 m0, s47, 0\ns_add_u32 s44, s44, 0x40000\nMaaa 16,144,176\n"
"Ra 232,11,8192\nMaaa 48,144,180\nD0 6,24,44,0\nMaaa 80,144,184\nRa 236,11,10240\nMaaa 112,144,188\nMaaa 116,148,188\nRa 240,11,16384\nMaaa 84,148,184\nMaaa 52,148,180\n"
"Ra 244,11,18432\nMaaa 20,148,176\nMvaa 180,148,172\nRa 248,11,24576\nMvaa 148,148,168\nD0 7,24,44,1024\nMvaa 116,148,164\nRa 252,11,26624\nMvaa 84,148,160\nMvaa 88,152,160\n"
"Mvaa 120,152,164\nMvaa 152,152,168\nMvaa 184,152,172\nMaaa 24,152,176\nMaaa 56,152,180\nD0 8,24,44,2048\nMaaa 88,152,184\nMaaa 120,152,188\nMaaa 124,156,188\nMaaa 92,156,184\n"
"Maaa 60,156,180\nMaaa 28,156,176\nMvaa 188,156,172\nMvaa 156,156,168\nD0 9,24,44,3072\nMvaa 124,156,164\nMvaa 92,156,160\ns_waitcnt vmcnt(11) lgkmcnt(0)\ns_barrier\n"
"Mvaa 64,192,224\nTa 128,12,32768\nMvaa 96,192,228\nv_lshlrev_b32 v244, 16, v240\nMvaa 128,192,232\nTa 130,12,34816\nv_and_b32 v245, 0xffff0000, v240\nMvaa 160,192,236\n"
"s_add_u32 m0, s48, 0\ns_add_u32 s45, s45, 128\nMaaa 0,192,240\nTa 132,13,32768\nv_lshlrev_b32 v246, 16, v241\nMaaa 32,192,244\nD0 2,20,45,0\nv_and_b32 v247, 0xffff0000, v241\n"
"Maaa 64,192,248\nTa 134,13,34816\nMaaa 96,192,252\nv_lshlrev_b32 v248, 16, v242\nv_and_b32 v249, 0xffff0000, v242\nMaaa 100,196,252\nTa 136,14,32768\nv_lshlrev_b32 v250, 16, v243\n"
"Maaa 68,196,248\nv_and_b32 v251, 0xffff0000, v243\nv_lshlrev_b32 v252, 16, v192\nMaaa 36,196,244\nTa 138,14,34816\nv_and_b32 v253, 0xffff0000, v192\nMaaa 4,196,240\nMU 32,244,252\n"
"Mvaa 164,196,236\nTa 140,15,32768\nMU 33,244,253\nMvaa 132,196,232\nD0 3,20,45,1024\nMU 40,248,252\nMvaa 100,196,228\nTa 142,15,34816\nMU 41,248,253\nMvaa 68,196,224\n"
"v_lshlrev_b32 v252, 16, v193\nv_and_b32 v253, 0xffff0000, v193\nMvaa 72,200,224\nTa 144,16,32768\nMU 34,244,252\nMvaa 104,200,228\nMU 35,244,253\nMvaa 136,200,232\n"
"Ta 146,16,34816\nMU 42,248,252\nMvaa 168,200,236\nMU 43,248,253\nv_lshlrev_b32 v252, 16, v194\nMaaa 8,200,240\nTa 148,17,32768\nv_and_b32 v253, 0xffff0000, v194\nMaaa 40,200,244\n"
"D0 4,20,45,2048\nMU 36,244,252\nMaaa 72,200,248\nTa 150,17,34816\nMaaa 104,200,252\nMU 37,244,253\nMU 44,248,252\nMaaa 108,204,252\nTa 152,18,32768\nMU 45,248,253\n"
"Maaa 76,204,248\nv_lshlrev_b32 v252, 16, v195\nv_and_b32 v253, 0xffff0000, v195\nMaaa 44,204,244\nTa 154,18,34816\nMU 38,244,252\nMaaa 12,204,240\nMU 39,244,253\nMvaa 172,204,236\n"
"Ta 156,19,32768\nMU 46,248,252\nMvaa 140,204,232\nD0 5,20,45,3072\nMU 47,248,253\nMvaa 108,204,228\nTa 158,19,34816\nv_lshlrev_b32 v252, 16, v208\nMvaa 76,204,224\n"
"v_and_b32 v253, 0xffff0000, v208\nMU 48,244,252\nMvaa 80,208,224\nRa 160,10,32768\nMU 49,244,253\nMvaa 112,208,228\nMU 56,248,252\nMvaa 144,208,232\nRa 164,10,34816\n"
"MU 57,248,253\nMvaa 176,208,236\ns_add_u32 m0, s48, 4096\ns_add_u32 s46, s45, 0x10000\nMaaa 16,208,240\nRa 168,10,40960\nv_lshlrev_b32 v252, 16, v209\nMaaa 48,208,244\n"
"D0 2,20,46,0\nv_and_b32 v253, 0xffff0000, v209\nMaaa 80,208,248\nRa 172,10,43008\nMaaa 112,208,252\nMU 50,244,252\nMU 51,244,253\nMaaa 116,212,252\nRa 176,10,49152\nMU 58,248,252\n"
"Maaa 84,212,248\nMU 59,248,253\nv_lshlrev_b32 v252, 16, v210\nMaaa 52,212,244\nRa 180,10,51200\nv_and_b32 v253, 0xffff0000, v210\nMaaa 20,212,240\nMU 52,244,252\nMvaa 180,212,236\n"
"Ra 184,10,57344\nMU 53,244,253\nMvaa 148,212,232\nD0 3,20,46,1024\nMU 60,248,252\nMvaa 116,212,228\nRa 188,10,59392\nMU 61,248,253\nMvaa 84,212,224\nv_lshlrev_b32 v252, 16, v211\n"
"v_and_b32 v253, 0xffff0000, v211\nMvaa 88,216,224\nMU 54,244,252\nMU 55,244,253\nMvaa 120,216,228\nMU 62,248,252\nMvaa 152,216,232\nMU 63,248,253\nv_lshlrev_b32 v252, 16, v196\n"
"Mvaa 184,216,236\nv_and_b32 v253, 0xffff0000, v196\nFM 32,245,252\nMaaa 24,216,240\nFM 33,245,253\nFM 40,249,252\nMaaa 56,216,244\nD0 4,20,46,2048\nFM 41,249,253\nMaaa 88,216,248\n"
"v_lshlrev_b32 v252, 16, v197\nMaaa 120,216,252\nv_and_b32 v253, 0xffff0000, v197\nFM 34,245,252\nMaaa 124,220,252\nFM 35,245,253\nFM 42,249,252\nMaaa 92,220,248\nFM 43,249,253\n"
"v_lshlrev_b32 v252, 16, v198\nMaaa 60,220,244\nv_and_b32 v253, 0xffff0000, v198\nFM 36,245,252\nMaaa 28,220,240\nFM 37,245,253\nMvaa 188,220,236\nFM 44,249,252\nFM 45,249,253\n"
"Mvaa 156,220,232\nD0 5,20,46,3072\nv_lshlrev_b32 v252, 16, v199\nMvaa 124,220,228\nv_and_b32 v253, 0xffff0000, v199\nFM 38,245,252\nMvaa 92,220,224\n"
"s_waitcnt vmcnt(14) lgkmcnt(0)\ns_barrier\nMvaa 64,128,160\nTa 192,12,49152\nFM 39,245,253\nMvaa 96,128,164\nFM 46,249,252\nMvaa 128,128,168\nTa 194,12,51200\nFM 47,249,253\n"
"Mvaa 160,128,172\ns_add_u32 m0, s47, 16384\ns_add_u32 s44, s44, 0x40000\nMaaa 0,128,176\nTa 196,13,49152\nv_lshlrev_b32 v252, 16, v212\nMaaa 32,128,180\nD0 6,24,44,0\n"
"v_and_b32 v253, 0xffff0000, v212\nMaaa 64,128,184\nTa 198,13,51200\nMaaa 96,128,188\nFM 48,245,252\nFM 49,245,253\nMaaa 100,132,188\nTa 200,14,49152\nFM 56,249,252\n"
"Maaa 68,132,184\nFM 57,249,253\nv_lshlrev_b32 v252, 16, v213\nMaaa 36,132,180\nTa 202,14,51200\nv_and_b32 v253, 0xffff0000, v213\nMaaa 4,132,176\nFM 50,245,252\nMvaa 164,132,172\n"
"Ta 204,15,49152\nFM 51,245,253\nMvaa 132,132,168\nD0 7,24,44,1024\nFM 58,249,252\nMvaa 100,132,164\nTa 206,15,51200\nFM 59,249,253\nMvaa 68,132,160\nv_lshlrev_b32 v252, 16, v214\n"
"v_and_b32 v253, 0xffff0000, v214\nMvaa 72,136,160\nTa 208,16,49152\nFM 52,245,252\nMvaa 104,136,164\nFM 53,245,253\nMvaa 136,136,168\nTa 210,16,51200\nFM 60,249,252\n"
"Mvaa 168,136,172\nFM 61,249,253\nv_lshlrev_b32 v252, 16, v215\nMaaa 8,136,176\nTa 212,17,49152\nv_and_b32 v253, 0xffff0000, v215\nMaaa 40,136,180\nD0 8,24,44,2048\nFM 54,245,252\n"
"Maaa 72,136,184\nTa 214,17,51200\nMaaa 104,136,188\nFM 55,245,253\nFM 62,249,252\nMaaa 108,140,188\nTa 216,18,49152\nFM 63,249,253\nMaaa 76,140,184\nv_lshlrev_b32 v252, 16, v200\n"
"v_and_b32 v253, 0xffff0000, v200\nMaaa 44,140,180\nTa 218,18,51200\nFM 32,246,252\nMaaa 12,140,176\nFM 33,246,253\nMvaa 172,140,172\nTa 220,19,49152\nFM 40,250,252\n"
"Mvaa 140,140,168\nD0 9,24,44,3072\nFM 41,250,253\nMvaa 108,140,164\nTa 222,19,51200\nv_lshlrev_b32 v252, 16, v201\nMvaa 76,140,160\nv_and_b32 v253, 0xffff0000, v201\n"
"FM 34,246,252\nMvaa 80,144,160\nRa 224,11,32768\nFM 35,246,253\nMvaa 112,144,164\nFM 42,250,252\nMvaa 144,144,168\nRa 228,11,34816\nFM 43,250,253\nMvaa 176,144,172\n"
"s_add_u32 m0, s47, 32768\ns_add_u32 s44, s44, 0x40000\nMaaa 16,144,176\nRa 232,11,40960\nv_lshlrev_b32 v252, 16, v202\nMaaa 48,144,180\nD0 6,24,44,0\n"
"v_and_b32 v253, 0xffff0000, v202\nMaaa 80,144,184\nRa 236,11,43008\nMaaa 112,144,188\nFM 36,246,252\nFM 37,246,253\nMaaa 116,148,188\nRa 240,11,49152\nFM 44,250,252\n"
"Maaa 84,148,184\nFM 45,250,253\nv_lshlrev_b32 v252, 16, v203\nMaaa 52,148,180\nRa 244,11,51200\nv_and_b32 v253, 0xffff0000, v203\nMaaa 20,148,176\nFM 38,246,252\nMvaa 180,148,172\n"
"Ra 248,11,57344\nFM 39,246,253\nMvaa 148,148,168\nD0 7,24,44,1024\nFM 46,250,252\nMvaa 116,148,164\nRa 252,11,59392\nFM 47,250,253\nMvaa 84,148,160\nv_lshlrev_b32 v252, 16, v216\n"
"v_and_b32 v253, 0xffff0000, v216\nMvaa 88,152,160\nFM 48,246,252\nFM 49,246,253\nMvaa 120,152,164\nFM 56,250,252\nMvaa 152,152,168\nFM 57,250,253\nv_lshlrev_b32 v252, 16, v217\n"
"Mvaa 184,152,172\nv_and_b32 v253, 0xffff0000, v217\nFM 50,246,252\nMaaa 24,152,176\nFM 51,246,253\nFM 58,250,252\nMaaa 56,152,180\nD0 8,24,44,2048\nFM 59,250,253\nMaaa 88,152,184\n"
"v_lshlrev_b32 v252, 16, v218\nMaaa 120,152,188\nv_and_b32 v253, 0xffff0000, v218\nFM 52,246,252\nMaaa 124,156,188\nFM 53,246,253\nFM 60,250,252\nMaaa 92,156,184\nFM 61,250,253\n"
"v_lshlrev_b32 v252, 16, v219\nMaaa 60,156,180\nv_and_b32 v253, 0xffff0000, v219\nFM 54,246,252\nMaaa 28,156,176\nWV 19\nv_xor_b32 v27, s15, v27\nv_and_b32 v255, 0xffffdfff, v27\n"
"v_cmp_ne_u32 vcc, 0, v255\ns_nop 0\ns_cbranch_vccnz .S17\n.S18:\nv_cmp_ne_u32 vcc, 0, v27\ns_nop 3\ns_bcnt1_i32_b64 s4, vcc\ns_cmp_gt_u32 s4, 32\ns_cselect_b32 s4, 1, 0\n"
"Mvaa 188,156,172\ns_xor_b32 s4, s4, s12\ns_and_b32 s4, s4, 1\nMvaa 156,156,168\nD0 9,24,44,3072\ns_lshl_b32 s4, s4, 13\nMvaa 124,156,164\ns_mul_i32 s72, s73, 25165824\n"
"s_add_u32 s72, s72, s4\nMvaa 92,156,160\ns_waitcnt vmcnt(8) lgkmcnt(0)\ns_barrier\nMvaa 64,192,224\nTa 128,12,0\ns_and_b32 s4, s12, 1\nMvaa 96,192,228\ns_lshl_b32 s4, s4, 21\n"
"Mvaa 128,192,232\nTa 130,12,2048\ns_add_u32 s72, s72, s4\nMvaa 160,192,236\ns_add_u32 m0, s48, 32768\ns_add_u32 s45, s45, 128\nMaaa 0,192,240\nTa 132,13,0\n"
"s_lshl3_add_u32 s2, s13, s11\nMaaa 32,192,244\nD0 2,20,45,0\ns_lshl_b32 s2, s2, 11\nMaaa 64,192,248\nTa 134,13,2048\nMaaa 96,192,252\ns_lshl_b32 s3, s14, 22\ns_add_u32 s2, s2, s3\n"
"Maaa 100,196,252\nTa 136,14,0\ns_add_u32 s2, s2, s72\nMaaa 68,196,248\ns_add_u32 s49, s2, 0xc00000\nFM 55,246,253\nMaaa 36,196,244\nTa 138,14,2048\nFM 62,250,252\nMaaa 4,196,240\n"
"FM 63,250,253\nMvaa 164,196,236\nTa 140,15,0\nv_lshlrev_b32 v252, 16, v204\nMvaa 132,196,232\nD0 3,20,45,1024\nv_and_b32 v253, 0xffff0000, v204\nMvaa 100,196,228\nTa 142,15,2048\n"
"FM 32,247,252\nMvaa 68,196,224\nFM 33,247,253\nFM 40,251,252\nMvaa 72,200,224\nTa 144,16,0\nFM 41,251,253\nMvaa 104,200,228\nv_lshlrev_b32 v252, 16, v205\nMvaa 136,200,232\n"
"Ta 146,16,2048\nv_and_b32 v253, 0xffff0000, v205\nMvaa 168,200,236\nFM 34,247,252\nFM 35,247,253\nMaaa 8,200,240\nTa 148,17,0\nFM 42,251,252\nMaaa 40,200,244\nD0 4,20,45,2048\n"
"FM 43,251,253\nMaaa 72,200,248\nTa 150,17,2048\nMaaa 104,200,252\nv_lshlrev_b32 v252, 16, v206\nv_and_b32 v253, 0xffff0000, v206\nMaaa 108,204,252\nTa 152,18,0\nFM 36,247,252\n"
"Maaa 76,204,248\nFM 37,247,253\nFM 44,251,252\nMaaa 44,204,244\nTa 154,18,2048\nFM 45,251,253\nMaaa 12,204,240\nv_lshlrev_b32 v252, 16, v207\nMvaa 172,204,236\nTa 156,19,0\n"
"v_and_b32 v253, 0xffff0000, v207\nMvaa 140,204,232\nD0 5,20,45,3072\nFM 38,247,252\nMvaa 108,204,228\nTa 158,19,2048\nFM 39,247,253\nMvaa 76,204,224\nFM 46,251,252\nFM 47,251,253\n"
"Mvaa 80,208,224\nRa 160,10,0\nv_lshlrev_b32 v252, 16, v220\nMvaa 112,208,228\nv_and_b32 v253, 0xffff0000, v220\nMvaa 144,208,232\nRa 164,10,2048\nFM 48,247,252\nMvaa 176,208,236\n"
"s_add_u32 m0, s48, 36864\ns_add_u32 s46, s45, 0x10000\nMaaa 16,208,240\nRa 168,10,8192\nFM 49,247,253\nMaaa 48,208,244\nD0 2,20,46,0\nFM 56,251,252\nMaaa 80,208,248\n"
"Ra 172,10,10240\nMaaa 112,208,252\nFM 57,251,253\nv_lshlrev_b32 v252, 16, v221\nMaaa 116,212,252\nRa 176,10,16384\nv_and_b32 v253, 0xffff0000, v221\nMaaa 84,212,248\n"
"FM 50,247,252\nFM 51,247,253\nMaaa 52,212,244\nRa 180,10,18432\nFM 58,251,252\nMaaa 20,212,240\nFM 59,251,253\nMvaa 180,212,236\nRa 184,10,24576\nv_lshlrev_b32 v252, 16, v222\n"
"Mvaa 148,212,232\nD0 3,20,46,1024\nv_and_b32 v253, 0xffff0000, v222\nMvaa 116,212,228\nRa 188,10,26624\nFM 52,247,252\nMvaa 84,212,224\nFM 53,247,253\nFM 60,251,252\n"
"Mvaa 88,216,224\nFM 61,251,253\nv_lshlrev_b32 v252, 16, v223\nMvaa 120,216,228\nv_and_b32 v253, 0xffff0000, v223\nMvaa 152,216,232\nFM 54,247,252\nFM 55,247,253\nMvaa 184,216,236\n"
"FM 62,251,252\nFM 63,251,253\nMaaa 24,216,240\nv_permlane32_swap_b32 v32, v48\nv_permlane32_swap_b32 v33, v49\nMaaa 56,216,244\nD0 4,20,46,2048\nv_permlane32_swap_b32 v34, v50\n"
"Maaa 88,216,248\nv_permlane32_swap_b32 v35, v51\nMaaa 120,216,252\nv_permlane32_swap_b32 v36, v52\nv_permlane32_swap_b32 v37, v53\nMaaa 124,220,252\n"
"v_permlane32_swap_b32 v38, v54\nv_permlane32_swap_b32 v39, v55\nMaaa 92,220,248\nv_permlane32_swap_b32 v40, v56\nv_permlane32_swap_b32 v41, v57\nMaaa 60,220,244\n"
"v_permlane32_swap_b32 v42, v58\nv_permlane32_swap_b32 v43, v59\nMaaa 28,220,240\nv_permlane32_swap_b32 v44, v60\nMvaa 188,220,236\nv_permlane32_swap_b32 v45, v61\n"
"v_permlane32_swap_b32 v46, v62\nMvaa 156,220,232\nD0 5,20,46,3072\nv_permlane32_swap_b32 v47, v63\nMvaa 124,220,228\nAD 32,32,48\nAD 33,33,49\nMvaa 92,220,224\n"
"s_waitcnt vmcnt(12) lgkmcnt(0)\ns_barrier\nMvaa 64,128,160\nTa 192,12,16384\nAD 34,34,50\nMvaa 96,128,164\nAD 35,35,51\nMvaa 128,128,168\nTa 194,12,18432\nAD 36,36,52\n"
"Mvaa 160,128,172\ns_add_u32 m0, s47, 49152\ns_add_u32 s44, s44, 0x40000\nMaaa 0,128,176\nTa 196,13,16384\nAD 37,37,53\nMaaa 32,128,180\nD0 6,24,44,0\nAD 38,38,54\nMaaa 64,128,184\n"
"Ta 198,13,18432\nMaaa 96,128,188\nAD 39,39,55\nAD 40,40,56\nMaaa 100,132,188\nTa 200,14,16384\nAD 41,41,57\nMaaa 68,132,184\nAD 42,42,58\nAD 43,43,59\nMaaa 36,132,180\n"
"Ta 202,14,18432\nAD 44,44,60\nMaaa 4,132,176\nAD 45,45,61\nMvaa 164,132,172\nTa 204,15,16384\nAD 46,46,62\nMvaa 132,132,168\nD0 7,24,44,1024\nAD 47,47,63\nMvaa 100,132,164\n"
"Ta 206,15,18432\nv_permlane16_swap_b32 v32, v40\nMvaa 68,132,160\nv_permlane16_swap_b32 v33, v41\nv_permlane16_swap_b32 v34, v42\nMvaa 72,136,160\nTa 208,16,16384\n"
"v_permlane16_swap_b32 v35, v43\nMvaa 104,136,164\nv_permlane16_swap_b32 v36, v44\nMvaa 136,136,168\nTa 210,16,18432\nv_permlane16_swap_b32 v37, v45\nMvaa 168,136,172\n"
"v_permlane16_swap_b32 v38, v46\nv_permlane16_swap_b32 v39, v47\nMaaa 8,136,176\nTa 212,17,16384\nAD 32,32,40\nMaaa 40,136,180\nD0 8,24,44,2048\nAD 33,33,41\nMaaa 72,136,184\n"
"Ta 214,17,18432\nMaaa 104,136,188\nAD 34,34,42\nAD 35,35,43\nMaaa 108,140,188\nTa 216,18,16384\nAD 36,36,44\nMaaa 76,140,184\nAD 37,37,45\nAD 38,38,46\nMaaa 44,140,180\n"
"Ta 218,18,18432\nAD 39,39,47\nMaaa 12,140,176\nv_add_f32_dpp v32, v32, v32 row_ror:8 row_mask:0xf bank_mask:0xf\nMvaa 172,140,172\nTa 220,19,16384\n"
"v_add_f32_dpp v33, v33, v33 row_ror:8 row_mask:0xf bank_mask:0xf\nMvaa 140,140,168\nD0 9,24,44,3072\nv_add_f32_dpp v34, v34, v34 row_ror:8 row_mask:0xf bank_mask:0xf\n"
"Mvaa 108,140,164\nTa 222,19,18432\nv_add_f32_dpp v35, v35, v35 row_ror:8 row_mask:0xf bank_mask:0xf\nMvaa 76,140,160\n"
"v_add_f32_dpp v36, v36, v36 row_ror:8 row_mask:0xf bank_mask:0xf\nv_add_f32_dpp v37, v37, v37 row_ror:8 row_mask:0xf bank_mask:0xf\nMvaa 80,144,160\nRa 224,11,0\n"
"v_add_f32_dpp v38, v38, v38 row_ror:8 row_mask:0xf bank_mask:0xf\nMvaa 112,144,164\nv_add_f32_dpp v39, v39, v39 row_ror:8 row_mask:0xf bank_mask:0xf\nMvaa 144,144,168\n"
"Ra 228,11,2048\nv_add_f32_dpp v32, v32, v32 row_ror:4 row_mask:0xf bank_mask:0xf\nMvaa 176,144,172\ns_add_u32 m0, s47, 0\ns_add_u32 s44, s44, 0x40000\nMaaa 16,144,176\n"
"Ra 232,11,8192\nv_add_f32_dpp v33, v33, v33 row_ror:4 row_mask:0xf bank_mask:0xf\nMaaa 48,144,180\nD0 6,24,44,0\nv_add_f32_dpp v34, v34, v34 row_ror:4 row_mask:0xf bank_mask:0xf\n"
"Maaa 80,144,184\nRa 236,11,10240\nMaaa 112,144,188\nv_add_f32_dpp v35, v35, v35 row_ror:4 row_mask:0xf bank_mask:0xf\n"
"v_add_f32_dpp v36, v36, v36 row_ror:4 row_mask:0xf bank_mask:0xf\nMaaa 116,148,188\nRa 240,11,16384\nv_add_f32_dpp v37, v37, v37 row_ror:4 row_mask:0xf bank_mask:0xf\n"
"Maaa 84,148,184\nv_add_f32_dpp v38, v38, v38 row_ror:4 row_mask:0xf bank_mask:0xf\nv_add_f32_dpp v39, v39, v39 row_ror:4 row_mask:0xf bank_mask:0xf\nMaaa 52,148,180\n"
"Ra 244,11,18432\nv_add_f32_dpp v32, v32, v32 row_ror:2 row_mask:0xf bank_mask:0xf\nMaaa 20,148,176\nv_add_f32_dpp v33, v33, v33 row_ror:2 row_mask:0xf bank_mask:0xf\n"
"Mvaa 180,148,172\nRa 248,11,24576\nv_add_f32_dpp v34, v34, v34 row_ror:2 row_mask:0xf bank_mask:0xf\nMvaa 148,148,168\nD0 7,24,44,1024\n"
"v_add_f32_dpp v35, v35, v35 row_ror:2 row_mask:0xf bank_mask:0xf\nMvaa 116,148,164\nRa 252,11,26624\nv_add_f32_dpp v36, v36, v36 row_ror:2 row_mask:0xf bank_mask:0xf\n"
"Mvaa 84,148,160\nv_add_f32_dpp v37, v37, v37 row_ror:2 row_mask:0xf bank_mask:0xf\nv_add_f32_dpp v38, v38, v38 row_ror:2 row_mask:0xf bank_mask:0xf\nMvaa 88,152,160\n"
"v_add_f32_dpp v39, v39, v39 row_ror:2 row_mask:0xf bank_mask:0xf\nv_lshrrev_b32 v48, 4, v0\nMvaa 120,152,164\nv_and_b32 v49, 1, v0\nMvaa 152,152,168\nv_lshlrev_b32 v49, 5, v49\n"
"v_lshl_add_u32 v49, v48, 6, v49\nMvaa 184,152,172\ns_lshl_b32 s2, s11, 8\ns_add_u32 s2, s2, 132096\nMaaa 24,152,176\nv_add_u32 v49, s2, v49\nds_write_b128 v49, v[32:35]\n"
"Maaa 56,152,180\nD0 8,24,44,2048\nds_write_b128 v49, v[36:39] offset:16\nMaaa 88,152,184\nv_and_b32 v50, 31, v0\nMaaa 120,152,188\nv_lshlrev_b32 v50, 2, v50\n"
"s_lshl_b32 s2, s11, 10\nMaaa 124,156,188\ns_add_u32 s2, s2, 133120\nv_add_u32 v51, s2, v50\nMaaa 92,156,184\nv_add_u32 v50, 132096, v50\nv_lshl_add_u32 v52, v48, 4, s2\n"
"Maaa 60,156,180\nMaaa 28,156,176\nMvaa 188,156,172\nMvaa 156,156,168\nD0 9,24,44,3072\nMvaa 124,156,164\nMvaa 92,156,160\ns_waitcnt vmcnt(8) lgkmcnt(0)\ns_barrier\n"
"Mvaa 64,192,224\nTa 128,12,32768\nds_read_b32 v32, v50 offset:0\nMvaa 96,192,228\nds_read_b32 v33, v50 offset:256\nMvaa 128,192,232\nTa 130,12,34816\n"
"ds_read_b32 v34, v50 offset:512\nMvaa 160,192,236\ns_add_u32 m0, s48, 0\ns_add_u32 s45, s45, 128\nMaaa 0,192,240\nTa 132,13,32768\nds_read_b32 v35, v50 offset:768\n"
"Maaa 32,192,244\nD0 2,20,45,0\nMaaa 64,192,248\nTa 134,13,34816\nds_read_b32 v36, v50 offset:128\nMaaa 96,192,252\nds_read_b32 v37, v50 offset:384\nMaaa 100,196,252\n"
"Ta 136,14,32768\nds_read_b32 v38, v50 offset:640\nMaaa 68,196,248\nds_read_b32 v39, v50 offset:896\nMaaa 36,196,244\nTa 138,14,34816\nMaaa 4,196,240\nMvaa 164,196,236\n"
"Ta 140,15,32768\nMvaa 132,196,232\nD0 3,20,45,1024\nMvaa 100,196,228\nTa 142,15,34816\nMvaa 68,196,224\nMvaa 72,200,224\nTa 144,16,32768\nMvaa 104,200,228\nMvaa 136,200,232\n"
"Ta 146,16,34816\nMvaa 168,200,236\nWL 5\nMaaa 8,200,240\nTa 148,17,32768\nAD 32,32,33\nMaaa 40,200,244\nD0 4,20,45,2048\nMaaa 72,200,248\nTa 150,17,34816\nAD 34,34,35\n"
"Maaa 104,200,252\nAD 36,36,37\nMaaa 108,204,252\nTa 152,18,32768\nAD 38,38,39\nMaaa 76,204,248\nAD 32,32,34\nMaaa 44,204,244\nTa 154,18,34816\nAD 36,36,38\nMaaa 12,204,240\n"
"CV 32,32,36\nMvaa 172,204,236\nTa 156,19,32768\nv_and_b32 v36, 0xffff0000, v32\nMvaa 140,204,232\nD0 5,20,45,3072\nMvaa 108,204,228\nTa 158,19,34816\nv_lshlrev_b32 v32, 16, v32\n"
"Mvaa 76,204,224\nv_mul_f32 v33, 0xbfb8aa3b, v32\nEX 33,33\nMvaa 80,208,224\nRa 160,10,32768\nv_add_f32 v33, 1.0, v33\nMvaa 112,208,228\nv_rcp_f32 v33, v33\nMvaa 144,208,232\n"
"Ra 164,10,34816\nMU 33,32,33\nMvaa 176,208,236\ns_add_u32 m0, s48, 4096\ns_add_u32 s46, s45, 0x10000\nMaaa 16,208,240\nRa 168,10,40960\nCV 33,33,33\nMaaa 48,208,244\nD0 2,20,46,0\n"
"Maaa 80,208,248\nRa 172,10,43008\nv_lshlrev_b32 v33, 16, v33\nMaaa 112,208,252\nMU 33,33,36\nMaaa 116,212,252\nRa 176,10,49152\nCV 33,33,33\nMaaa 84,212,248\n"
"v_lshlrev_b32 v33, 16, v33\nMaaa 52,212,244\nRa 180,10,51200\nds_write_b32 v51, v33\nMaaa 20,212,240\nRv 244,52,0\nMvaa 180,212,236\nRa 184,10,57344\nRv 248,52,64\n"
"Mvaa 148,212,232\nD0 3,20,46,1024\nMvaa 116,212,228\nRa 188,10,59392\ns_and_b32 s2, s12, 1\nMvaa 84,212,224\ns_and_b32 s3, s13, 6\nMvaa 88,216,224\ns_add_u32 s2, s2, s3\n"
"s_lshl3_add_u32 s2, s14, s2\nMvaa 120,216,228\ns_cmp_lt_u32 s2, 14\ns_cselect_b32 s5, 0, 0x40000000\nMvaa 152,216,232\ns_mul_i32 s4, s73, 14\ns_add_u32 s4, s4, s2\n"
"Mvaa 184,216,236\nv_and_b32 v49, 15, v0\nMaaa 24,216,240\nv_lshlrev_b32 v50, 3, v49\nv_and_b32 v51, 1, v48\nMaaa 56,216,244\nD0 4,20,46,2048\nMaaa 88,216,248\n"
"v_lshl_add_u32 v50, v51, 2, v50\ns_mul_i32 s2, s11, 112\nMaaa 120,216,252\nv_add_u32 v50, s2, v50\nMaaa 124,220,252\nv_lshrrev_b32 v51, 4, v50\nv_lshrrev_b32 v48, 1, v48\n"
"Maaa 92,220,248\nv_mad_u32_u24 v51, v48, 28, v51\nMaaa 60,220,244\nv_mul_u32_u24 v51, 7168, v51\nv_and_b32 v50, 15, v50\nMaaa 28,220,240\nv_lshl_add_u32 v51, v50, 2, v51\n"
"v_cmp_gt_u32 vcc, 14, v49\ns_lshl_b32 s4, s4, 6\ns_add_u32 s4, s4, 0xc04c800\nv_cndmask_b32 v51, v254, v51, vcc\nMvaa 188,220,236\ns_or_b32 s4, s4, s5\nMvaa 156,220,232\n"
"D0 5,20,46,3072\nMvaa 124,220,228\nv_lshlrev_b32 v252, 16, v224\nv_and_b32 v253, 0xffff0000, v224\nMvaa 92,220,224\ns_waitcnt vmcnt(12) lgkmcnt(0)\ns_barrier\nMvaa 64,128,160\n"
"Ta 192,12,49152\nMU 32,244,252\nMvaa 96,128,164\nMU 33,244,253\nMvaa 128,128,168\nTa 194,12,51200\nMU 40,248,252\nMvaa 160,128,172\ns_add_u32 m0, s47, 16384\n"
"s_add_u32 s44, s44, 0x40000\nMaaa 0,128,176\nTa 196,13,49152\nMU 41,248,253\nMaaa 32,128,180\nD0 6,24,44,0\nMaaa 64,128,184\nTa 198,13,51200\nv_lshlrev_b32 v252, 16, v225\n"
"Maaa 96,128,188\nv_and_b32 v253, 0xffff0000, v225\nMaaa 100,132,188\nTa 200,14,49152\nMU 34,244,252\nMaaa 68,132,184\nMU 35,244,253\nMaaa 36,132,180\nTa 202,14,51200\n"
"MU 42,248,252\nMaaa 4,132,176\nMU 43,248,253\nMvaa 164,132,172\nTa 204,15,49152\nv_lshlrev_b32 v252, 16, v226\nMvaa 132,132,168\nD0 7,24,44,1024\nMvaa 100,132,164\n"
"Ta 206,15,51200\nv_and_b32 v253, 0xffff0000, v226\nMvaa 68,132,160\nMU 36,244,252\nMvaa 72,136,160\nTa 208,16,49152\nMU 37,244,253\nMvaa 104,136,164\nMU 44,248,252\n"
"Mvaa 136,136,168\nTa 210,16,51200\nMU 45,248,253\nMvaa 168,136,172\nv_lshlrev_b32 v252, 16, v227\nMaaa 8,136,176\nTa 212,17,49152\nv_and_b32 v253, 0xffff0000, v227\n"
"Maaa 40,136,180\nD0 8,24,44,2048\nMaaa 72,136,184\nTa 214,17,51200\nMU 38,244,252\nMaaa 104,136,188\nMU 39,244,253\nMaaa 108,140,188\nTa 216,18,49152\nMU 46,248,252\n"
"Maaa 76,140,184\nMU 47,248,253\nMaaa 44,140,180\nTa 218,18,51200\nv_lshlrev_b32 v252, 16, v228\nMaaa 12,140,176\nv_and_b32 v253, 0xffff0000, v228\nMvaa 172,140,172\n"
"Ta 220,19,49152\nFM 32,245,252\nMvaa 140,140,168\nD0 9,24,44,3072\nMvaa 108,140,164\nTa 222,19,51200\nFM 33,245,253\nMvaa 76,140,160\nFM 40,249,252\nMvaa 80,144,160\n"
"Ra 224,11,32768\nFM 41,249,253\nMvaa 112,144,164\nv_lshlrev_b32 v252, 16, v229\nMvaa 144,144,168\nRa 228,11,34816\nv_and_b32 v253, 0xffff0000, v229\nMvaa 176,144,172\n"
"s_add_u32 m0, s47, 32768\ns_add_u32 s44, s44, 0x40000\nMaaa 16,144,176\nRa 232,11,40960\nFM 34,245,252\nMaaa 48,144,180\nD0 6,24,44,0\nMaaa 80,144,184\nRa 236,11,43008\n"
"FM 35,245,253\nMaaa 112,144,188\nFM 42,249,252\nMaaa 116,148,188\nRa 240,11,49152\nFM 43,249,253\nMaaa 84,148,184\nv_lshlrev_b32 v252, 16, v230\nMaaa 52,148,180\nRa 244,11,51200\n"
"v_and_b32 v253, 0xffff0000, v230\nMaaa 20,148,176\nFM 36,245,252\nMvaa 180,148,172\nRa 248,11,57344\nFM 37,245,253\nMvaa 148,148,168\nD0 7,24,44,1024\nMvaa 116,148,164\n"
"Ra 252,11,59392\nFM 44,249,252\nMvaa 84,148,160\nFM 45,249,253\nMvaa 88,152,160\nv_lshlrev_b32 v252, 16, v231\nv_and_b32 v253, 0xffff0000, v231\nMvaa 120,152,164\nFM 38,245,252\n"
"Mvaa 152,152,168\nFM 39,245,253\nFM 46,249,252\nMvaa 184,152,172\nFM 47,249,253\nMaaa 24,152,176\nv_lshlrev_b32 v252, 16, v232\nv_and_b32 v253, 0xffff0000, v232\nMaaa 56,152,180\n"
"D0 8,24,44,2048\nMaaa 88,152,184\nFM 32,246,252\nFM 33,246,253\nMaaa 120,152,188\nFM 40,250,252\nMaaa 124,156,188\nFM 41,250,253\nv_lshlrev_b32 v252, 16, v233\nMaaa 92,156,184\n"
"v_and_b32 v253, 0xffff0000, v233\nMaaa 60,156,180\nFM 34,246,252\nFM 35,246,253\nMaaa 28,156,176\nFM 42,250,252\nMvaa 188,156,172\nFM 43,250,253\nv_lshlrev_b32 v252, 16, v234\n"
"Mvaa 156,156,168\nD0 9,24,44,3072\nMvaa 124,156,164\nv_and_b32 v253, 0xffff0000, v234\nFM 36,246,252\nMvaa 92,156,160\ns_waitcnt vmcnt(8) lgkmcnt(0)\ns_barrier\nMvaa 64,192,224\n"
"Ta 128,12,0\nFM 37,246,253\nMvaa 96,192,228\nFM 44,250,252\nMvaa 128,192,232\nTa 130,12,2048\nFM 45,250,253\nMvaa 160,192,236\ns_add_u32 m0, s48, 32768\ns_add_u32 s45, s45, 128\n"
"Maaa 0,192,240\nTa 132,13,0\nv_lshlrev_b32 v252, 16, v235\nMaaa 32,192,244\nD0 2,20,45,0\nMaaa 64,192,248\nTa 134,13,2048\nv_and_b32 v253, 0xffff0000, v235\nMaaa 96,192,252\n"
"FM 38,246,252\nMaaa 100,196,252\nTa 136,14,0\nFM 39,246,253\nMaaa 68,196,248\nFM 46,250,252\nMaaa 36,196,244\nTa 138,14,2048\nFM 47,250,253\nMaaa 4,196,240\n"
"v_lshlrev_b32 v252, 16, v236\nMvaa 164,196,236\nTa 140,15,0\nv_and_b32 v253, 0xffff0000, v236\nMvaa 132,196,232\nD0 3,20,45,1024\nMvaa 100,196,228\nTa 142,15,2048\nFM 32,247,252\n"
"Mvaa 68,196,224\nFM 33,247,253\nMvaa 72,200,224\nTa 144,16,0\nFM 40,251,252\nMvaa 104,200,228\nFM 41,251,253\nMvaa 136,200,232\nTa 146,16,2048\nv_lshlrev_b32 v252, 16, v237\n"
"Mvaa 168,200,236\nv_and_b32 v253, 0xffff0000, v237\nMaaa 8,200,240\nTa 148,17,0\nFM 34,247,252\nMaaa 40,200,244\nD0 4,20,45,2048\nMaaa 72,200,248\nTa 150,17,2048\nFM 35,247,253\n"
"Maaa 104,200,252\nFM 42,251,252\nMaaa 108,204,252\nTa 152,18,0\nFM 43,251,253\nMaaa 76,204,248\nv_lshlrev_b32 v252, 16, v238\nMaaa 44,204,244\nTa 154,18,2048\n"
"v_and_b32 v253, 0xffff0000, v238\nMaaa 12,204,240\nFM 36,247,252\nMvaa 172,204,236\nTa 156,19,0\nFM 37,247,253\nMvaa 140,204,232\nD0 5,20,45,3072\nMvaa 108,204,228\n"
"Ta 158,19,2048\nFM 44,251,252\nMvaa 76,204,224\nFM 45,251,253\nMvaa 80,208,224\nRa 160,10,0\nv_lshlrev_b32 v252, 16, v239\nMvaa 112,208,228\nv_and_b32 v253, 0xffff0000, v239\n"
"Mvaa 144,208,232\nRa 164,10,2048\nFM 38,247,252\nMvaa 176,208,236\ns_add_u32 m0, s48, 36864\ns_add_u32 s46, s45, 0x10000\nMaaa 16,208,240\nRa 168,10,8192\nFM 39,247,253\n"
"Maaa 48,208,244\nD0 2,20,46,0\nMaaa 80,208,248\nRa 172,10,10240\nFM 46,251,252\nMaaa 112,208,252\nFM 47,251,253\nMaaa 116,212,252\nRa 176,10,16384\nv_permlane32_swap_b32 v32, v40\n"
"Maaa 84,212,248\nv_permlane32_swap_b32 v33, v41\nMaaa 52,212,244\nRa 180,10,18432\nv_permlane32_swap_b32 v34, v42\nMaaa 20,212,240\nv_permlane32_swap_b32 v35, v43\n"
"Mvaa 180,212,236\nRa 184,10,24576\nv_permlane32_swap_b32 v36, v44\nMvaa 148,212,232\nD0 3,20,46,1024\nMvaa 116,212,228\nRa 188,10,26624\nv_permlane32_swap_b32 v37, v45\n"
"Mvaa 84,212,224\nv_permlane32_swap_b32 v38, v46\nMvaa 88,216,224\nv_permlane32_swap_b32 v39, v47\nAD 32,32,40\nMvaa 120,216,228\nAD 33,33,41\nMvaa 152,216,232\nAD 34,34,42\n"
"AD 35,35,43\nMvaa 184,216,236\nAD 36,36,44\nMaaa 24,216,240\nAD 37,37,45\nAD 38,38,46\nMaaa 56,216,244\nD0 4,20,46,2048\nMaaa 88,216,248\nAD 39,39,47\nMaaa 120,216,252\n"
"v_permlane16_swap_b32 v32, v36\nMaaa 124,220,252\nv_permlane16_swap_b32 v33, v37\nv_permlane16_swap_b32 v34, v38\nMaaa 92,220,248\nv_permlane16_swap_b32 v35, v39\nMaaa 60,220,244\n"
"AD 32,32,36\nAD 33,33,37\nMaaa 28,220,240\nAD 34,34,38\nMvaa 188,220,236\nAD 35,35,39\nSv 32,51,16,4,0\nMvaa 156,220,232\nD0 5,20,46,3072\nMvaa 124,220,228\nMvaa 92,220,224\n"
"s_waitcnt vmcnt(13) lgkmcnt(0)\ns_barrier\nMvaa 64,128,160\nTa 192,12,16384\ns_lshr_b32 s3, s11, 1\nMvaa 96,128,164\ns_lshl_b32 s3, s3, 13\ns_add_u32 m0, s47, 49152\n"
"s_add_u32 s44, s44, 0x40000\nMvaa 128,128,168\nTa 194,12,18432\ns_lshl_b32 s2, s11, 12\nMvaa 160,128,172\ns_add_u32 s2, s2, s3\ns_add_u32 s2, s2, s72\nD0 6,24,44,0\n"
"Maaa 0,128,176\nTa 196,13,16384\nMaaa 32,128,180\ns_lshl_b32 s3, s11, 12\ns_add_u32 s89, s3, 0x0\nMaaa 64,128,184\nTa 198,13,18432\ns_mov_b32 s88, 0xffff8000\nMaaa 96,128,188\n"
"s_lshl_b32 s4, s13, 1\nD0 7,24,44,1024\nMaaa 100,132,188\nTa 200,14,16384\ns_sub_u32 s79, 15, s4\nMaaa 68,132,184\ns_and_b32 s5, s12, 1\ns_mul_i32 s5, s5, 229376\nMaaa 36,132,180\n"
"Ta 202,14,18432\nMaaa 4,132,176\ns_add_u32 s5, s5, s2\ns_add_u32 s96, s5, 0x1310000\nD0 8,24,44,2048\nMvaa 164,132,172\nTa 204,15,16384\ns_mov_b32 s63, 0x53800000\n"
"Mvaa 132,132,168\ns_mov_b32 s64, 0x2b800000\nMvaa 100,132,164\nTa 206,15,18432\ns_mov_b32 s98, s4\nMvaa 68,132,160\ns_lshl_b32 s5, s13, 5\ns_add_u32 s5, s5, s11\nD0 9,24,44,3072\n"
"Mvaa 72,136,160\nTa 208,16,16384\ns_lshl_b32 s65, s5, 9\nMvaa 104,136,164\ns_add_u32 s65, s65, s72\ns_add_u32 s6, s56, 0\nMvaa 136,136,168\nTa 210,16,18432\n"
"s_add_u32 s65, s65, 0x1580000\nMvaa 168,136,172\ns_and_b32 s66, s72, 0x2000\ns_add_u32 s66, s66, s56\nbuffer_load_dwordx4 v[192:195], v1, s[16:19], s6 offen offset:0 sc1\n"
"Maaa 8,136,176\nTa 212,17,16384\nMaaa 40,136,180\ns_add_u32 s66, s66, 0x100000\ns_and_b32 s5, s12, 1\nMaaa 72,136,184\nTa 214,17,18432\ns_mul_i32 s58, s5, 0x202000\n"
"Maaa 104,136,188\ns_xor_b32 s58, s58, s72\nbuffer_load_dwordx4 v[196:199], v1, s[16:19], s6 offen offset:1024 sc1\nMaaa 108,140,188\nTa 216,18,16384\n"
"s_add_u32 s58, s58, 0x15c0000\nMaaa 76,140,184\ns_lshl_b32 s5, s5, 8\ns_add_u32 s61, s58, s5\nMaaa 44,140,180\nTa 218,18,18432\nMaaa 12,140,176\ns_lshl4_add_u32 s60, s13, s61\n"
"s_add_u32 s54, s60, 0\nbuffer_load_dwordx4 v[200:203], v1, s[16:19], s6 offen offset:2048 sc1\nMvaa 172,140,172\nTa 220,19,16384\ns_xor_b32 s60, s54, 0x100\nMvaa 140,140,168\n"
"s_cmp_eq_u32 s11, 0\ns_cselect_b32 s54, s54, 0x40000000\nMvaa 108,140,164\nTa 222,19,18432\ns_getpc_b64 s[8:9]\n.PC20:\ns_add_u32 s8, s8, .HD13-.PC20\ns_addc_u32 s9, s9, 0\n"
"Mvaa 76,140,160\ns_mov_b32 s97, 0xff800000\ns_mov_b32 s90, 3\nbuffer_load_dwordx4 v[204:207], v1, s[16:19], s6 offen offset:3072 sc1\nMvaa 80,144,160\nRa 224,11,0\n"
"s_lshl_b32 s5, s11, 2\nMvaa 112,144,164\ns_lshr_b32 s92, 0xf112, s5\ns_add_u32 s6, s56, 4096\nMvaa 144,144,168\nRa 228,11,2048\ns_and_b32 s92, s92, 15\nMvaa 176,144,172\n"
"s_lshr_b32 s93, s11, 1\ns_xor_b32 s93, s93, 1\nbuffer_load_dwordx4 v[208:211], v1, s[16:19], s6 offen offset:0 sc1\nMaaa 16,144,176\nRa 232,11,8192\nMaaa 48,144,180\n"
"s_add_u32 s86, s2, 0x20000\ns_add_u32 s87, s2, 0x410000\nMaaa 80,144,184\nRa 236,11,10240\nMaaa 112,144,188\ns_cmp_eq_u32 s13, 7\ns_cselect_b32 s5, 0xfffffffd, 64\n"
"buffer_load_dwordx4 v[212:215], v1, s[16:19], s6 offen offset:1024 sc1\nMaaa 116,148,188\nRa 240,11,16384\ns_add_u32 s92, s92, s5\nMaaa 84,148,184\ns_add_u32 s93, s93, s5\n"
"s_or_b32 s92, s92, 0x100\nMaaa 52,148,180\nRa 244,11,18432\ns_cmp_lt_u32 s12, 14\ns_cselect_b32 s5, s11, 1\nMaaa 20,148,176\ns_cmp_eq_u32 s5, 0\ns_cselect_b32 s91, 3, 100\n"
"buffer_load_dwordx4 v[216:219], v1, s[16:19], s6 offen offset:2048 sc1\nMvaa 180,148,172\nRa 248,11,24576\nMvaa 148,148,168\nv_and_b32 v27, 0x70, v1\n"
"buffer_load_dword v27, v27, s[16:19], s61 offen offset:128 sc0 sc1\nMvaa 116,148,164\nRa 252,11,26624\ns_lshr_b32 s2, s13, 2\nMvaa 84,148,160\ns_bfe_u32 s3, s13, 0x10001\n"
"s_mul_i32 s3, s3, 17\nbuffer_load_dwordx4 v[220:223], v1, s[16:19], s6 offen offset:3072 sc1\nMvaa 88,152,160\ns_add_u32 s2, s2, s3\ns_lshl2_add_u32 s3, s14, 0\nMvaa 120,152,164\n"
"s_lshl_b32 s3, s3, 2\ns_add_u32 s6, s56, 16384\nMvaa 152,152,168\ns_lshr_b32 s3, 0xe420ca86, s3\ns_and_b32 s3, s3, 15\nMvaa 184,152,172\ns_add_u32 s2, s2, s3\n"
"buffer_load_dwordx4 v[224:227], v1, s[16:19], s6 offen offset:0 sc1\nMaaa 24,152,176\ns_cmp_eq_u32 s2, 32\ns_cselect_b32 s4, 16, s2\nMaaa 56,152,180\ns_mul_i32 s75, s4, 65536\n"
"s_lshr_b32 s2, s13, 2\nMaaa 88,152,184\ns_bfe_u32 s3, s13, 0x10001\ns_mul_i32 s3, s3, 17\nMaaa 120,152,188\ns_add_u32 s2, s2, s3\n"
"buffer_load_dwordx4 v[228:231], v1, s[16:19], s6 offen offset:1024 sc1\nMaaa 124,156,188\ns_lshl2_add_u32 s3, s14, 1\ns_lshl_b32 s3, s3, 2\nMaaa 92,156,184\n"
"s_lshr_b32 s3, 0xe420ca86, s3\ns_and_b32 s3, s3, 15\nMaaa 60,156,180\ns_add_u32 s2, s2, s3\ns_cmp_eq_u32 s2, 32\ns_cselect_b32 s4, 16, s2\nMaaa 28,156,176\n"
"s_mul_i32 s76, s4, 65536\ns_lshr_b32 s2, s13, 2\nbuffer_load_dwordx4 v[232:235], v1, s[16:19], s6 offen offset:2048 sc1\nMvaa 188,156,172\ns_bfe_u32 s3, s13, 0x10001\n"
"s_mul_i32 s3, s3, 17\nMvaa 156,156,168\ns_add_u32 s2, s2, s3\nMvaa 124,156,164\ns_lshl2_add_u32 s3, s14, 2\ns_lshl_b32 s3, s3, 2\nMvaa 92,156,160\ns_waitcnt vmcnt(16) lgkmcnt(0)\n"
"s_barrier\nMvaa 64,192,224\nTa 128,12,32768\ns_lshr_b32 s3, 0xe420ca86, s3\nbuffer_load_dwordx4 v[236:239], v1, s[16:19], s6 offen offset:3072 sc1\nMvaa 96,192,228\n"
"s_and_b32 s3, s3, 15\nMvaa 128,192,232\nTa 130,12,34816\ns_add_u32 s2, s2, s3\ns_add_u32 s6, s56, 20480\nMvaa 160,192,236\ns_cmp_eq_u32 s2, 32\ns_cselect_b32 s4, 16, s2\n"
"Maaa 0,192,240\nTa 132,13,32768\nbuffer_load_dwordx4 v[240:243], v1, s[16:19], s6 offen offset:0 sc1\nMaaa 32,192,244\ns_mul_i32 s77, s4, 65536\ns_lshr_b32 s2, s13, 2\n"
"Maaa 64,192,248\nTa 134,13,34816\ns_bfe_u32 s3, s13, 0x10001\nMaaa 96,192,252\ns_mul_i32 s3, s3, 17\nMaaa 100,196,252\nTa 136,14,32768\ns_add_u32 s2, s2, s3\n"
"buffer_load_dwordx4 v[244:247], v1, s[16:19], s6 offen offset:1024 sc1\nMaaa 68,196,248\ns_lshl2_add_u32 s3, s14, 3\ns_lshl_b32 s3, s3, 2\nMaaa 36,196,244\nTa 138,14,34816\n"
"Maaa 4,196,240\ns_lshr_b32 s3, 0xe420ca86, s3\ns_and_b32 s3, s3, 15\nMvaa 164,196,236\nTa 140,15,32768\ns_add_u32 s2, s2, s3\n"
"buffer_load_dwordx4 v[248:251], v1, s[16:19], s6 offen offset:2048 sc1\nMvaa 132,196,232\ns_cmp_eq_u32 s2, 32\ns_cselect_b32 s4, 16, s2\nMvaa 100,196,228\nTa 142,15,34816\n"
"s_mul_i32 s78, s4, 65536\nMvaa 68,196,224\ns_lshr_b32 s3, s11, 1\ns_lshl_b32 s3, s3, 15\nMvaa 72,200,224\nTa 144,16,32768\ns_add_u32 s2, s72, s3\n"
"buffer_load_dwordx4 v[252:255], v1, s[16:19], s6 offen offset:3072 sc1\nMvaa 104,200,228\ns_and_b32 s3, s11, 1\nMvaa 136,200,232\nTa 146,16,34816\ns_lshl_b32 s3, s3, 12\n"
"Mvaa 168,200,236\ns_add_u32 s2, s2, s3\ns_add_u32 s2, s2, 0x0\nMaaa 8,200,240\nTa 148,17,32768\nMaaa 40,200,244\ns_mov_b32 s55, 16384\ns_add_u32 s75, s75, s2\nMaaa 72,200,248\n"
"Ta 150,17,34816\ns_add_u32 s76, s76, s2\nMaaa 104,200,252\ns_add_u32 s77, s77, s2\nMaaa 108,204,252\nTa 152,18,32768\ns_add_u32 s78, s78, s2\nMaaa 76,204,248\nMaaa 44,204,244\n"
"Ta 154,18,34816\nMaaa 12,204,240\nMvaa 172,204,236\nTa 156,19,32768\nMvaa 140,204,232\nMvaa 108,204,228\nTa 158,19,34816\nMvaa 76,204,224\nMvaa 80,208,224\nRa 160,10,32768\n"
"Mvaa 112,208,228\nMvaa 144,208,232\nRa 164,10,34816\nMvaa 176,208,236\nMaaa 16,208,240\nRa 168,10,40960\nMaaa 48,208,244\nMaaa 80,208,248\nRa 172,10,43008\nMaaa 112,208,252\n"
"Maaa 116,212,252\nRa 176,10,49152\nWV 9\nv_cmp_ne_u32 vcc, s15, v27\ns_nop 0\ns_cbranch_vccnz .S21\n.S22:\ns_sub_u32 s45, s86, 131072\nMaaa 84,212,248\ns_add_u32 m0, s47, 0\n"
"s_add_u32 s46, s45, 0x0\nMaaa 52,212,244\nRa 180,10,51200\nMaaa 20,212,240\nD1 1,16,46,0\nMvaa 180,212,236\nRa 184,10,57344\nMvaa 148,212,232\nMvaa 116,212,228\nRa 188,10,59392\n"
"Mvaa 84,212,224\nD1 1,16,46,1024\nMvaa 88,216,224\nMvaa 120,216,228\nMvaa 152,216,232\ns_mov_b32 s36, s68\nMvaa 184,216,236\nD1 1,16,46,2048\nMaaa 24,216,240\n"
"s_and_b32 s37, s69, 0xffff\nMaaa 56,216,244\ns_mov_b32 s38, 0x1c0000\nMaaa 88,216,248\ns_mov_b32 s39, 0x20000\ns_and_b32 s2, s74, 15\nMaaa 120,216,252\nD1 1,16,46,3072\n"
"Maaa 124,220,252\ns_mul_i32 s3, s73, 14\nMaaa 92,220,248\ns_add_u32 s2, s2, s3\nMaaa 60,220,244\ns_lshl2_add_u32 s2, s2, s11\nMaaa 28,220,240\ns_mul_i32 s3, s2, 4682\n"
"s_lshr_b32 s3, s3, 16\nMvaa 188,220,236\ns_mul_i32 s22, s3, 14\nMvaa 156,220,232\ns_sub_u32 s22, s2, s22\nMvaa 124,220,228\ns_lshl_b32 s21, s3, 5\nMvaa 92,220,224\nWL 0\n"
"s_barrier\nMvaa 64,128,160\nTa 192,12,49152\ns_add_u32 s21, s21, 1024\nMvaa 96,128,164\ns_add_u32 m0, s47, 32768\ns_add_u32 s46, s45, 0x8000\nMvaa 128,128,168\nTa 194,12,51200\n"
"Mvaa 160,128,172\nD1 1,16,46,0\nMaaa 0,128,176\nTa 196,13,49152\nMaaa 32,128,180\ns_xor_b32 s3, s3, 28\nMaaa 64,128,184\nTa 198,13,51200\ns_bfe_u32 s41, s3, 0x30002\n"
"Maaa 96,128,188\nD1 1,16,46,1024\ns_cmp_gt_u32 s22, 6\ns_cselect_b32 s2, 1187840, 0\ns_cselect_b32 s1, 16, 0\nMaaa 100,132,188\nTa 200,14,49152\nMaaa 68,132,184\n"
"s_add_u32 s41, s41, s1\nMaaa 36,132,180\nTa 202,14,51200\nMaaa 4,132,176\nD1 1,16,46,2048\ns_lshl4_add_u32 s41, s41, 0x15c0200\nMvaa 164,132,172\nTa 204,15,49152\n"
"Mvaa 132,132,168\ns_and_b32 s1, s3, 28\nMvaa 100,132,164\nTa 206,15,51200\nMvaa 68,132,160\nD1 1,16,46,3072\nMvaa 72,136,160\nTa 208,16,49152\ns_add_u32 s3, s3, s1\n"
"Mvaa 104,136,164\ns_add_u32 m0, s47, -65536\ns_add_u32 s46, s45, 0x10000\nMvaa 136,136,168\nTa 210,16,51200\nMvaa 168,136,172\nD1 1,16,46,0\nMaaa 8,136,176\nTa 212,17,49152\n"
"Maaa 40,136,180\ns_lshl_b32 s3, s3, 11\nMaaa 72,136,184\nTa 214,17,51200\ns_add_u32 s2, s2, s3\nMaaa 104,136,188\nD1 1,16,46,1024\nMaaa 108,140,188\nTa 216,18,49152\n"
"Maaa 76,140,184\ns_lshl_b32 s3, s22, 17\nMaaa 44,140,180\nTa 218,18,51200\nMaaa 12,140,176\nD1 1,16,46,2048\ns_add_u32 s2, s2, s3\nMvaa 172,140,172\nTa 220,19,49152\n"
"Mvaa 140,140,168\ns_add_u32 s20, s2, 0xd00000\nMvaa 108,140,164\nTa 222,19,51200\nMvaa 76,140,160\nD1 1,16,46,3072\nMvaa 80,144,160\nRa 224,11,32768\ns_lshl_b32 s2, s22, 7\n"
"Mvaa 112,144,164\ns_add_u32 m0, s47, -49152\ns_add_u32 s46, s45, 0x408000\nMvaa 144,144,168\nRa 228,11,34816\nMvaa 176,144,172\nD1 1,16,46,0\nMaaa 16,144,176\nRa 232,11,40960\n"
"Maaa 48,144,180\ns_add_u32 s40, s2, 0xc04b800\nMaaa 80,144,184\nRa 236,11,43008\ns_mul_i32 s2, s21, 896\nMaaa 112,144,188\nD1 1,16,46,1024\nMaaa 116,148,188\nRa 240,11,49152\n"
"Maaa 84,148,184\ns_lshl_b32 s3, s22, 6\nMaaa 52,148,180\nRa 244,11,51200\nMaaa 20,148,176\nD1 1,16,46,2048\ns_add_u32 s22, s2, s3\nMvaa 180,148,172\nRa 248,11,57344\n"
"Mvaa 148,148,168\ns_add_u32 s23, s22, 14336\nMvaa 116,148,164\nRa 252,11,59392\nMvaa 84,148,160\nD1 1,16,46,3072\nMvaa 88,152,160\nMvaa 120,152,164\nv_add_u32 v2, 0x10000, v1\n"
"Mvaa 152,152,168\ns_lshr_b32 s2, s59, 1\nMvaa 184,152,172\nv_add_u32 v24, s2, v2\nMaaa 24,152,176\nv_xor_b32 v26, 0x1000, v24\nMaaa 56,152,180\nv_lshrrev_b32 v20, 4, v0\n"
"Maaa 88,152,184\nv_lshlrev_b32 v20, 2, v20\nMaaa 120,152,188\nv_and_b32 v0, 15, v0\nMaaa 124,156,188\nv_sub_u32 v0, v0, v20\nMaaa 92,156,184\ns_and_b32 s2, s11, 1\n"
"Maaa 60,156,180\ns_lshl_b32 s2, s2, 6\nMaaa 28,156,176\nv_subrev_u32 v3, s2, v0\nMvaa 188,156,172\ns_xor_b32 s2, s2, 64\nMvaa 156,156,168\nv_subrev_u32 v0, s2, v0\n"
"Mvaa 124,156,164\nv_mov_b32 v4, 0\nMvaa 92,156,160\ns_waitcnt vmcnt(12) lgkmcnt(0)\ns_barrier\nMvaa 64,192,224\nv_mov_b32 v5, 0\nMvaa 68,196,224\ns_add_u32 m0, s47, 49152\n"
"s_add_u32 s46, s45, 0x400000\nMvaa 72,200,224\nv_mov_b32 v6, 0\nMvaa 76,204,224\nD1 1,16,46,0\nMvaa 80,208,224\nv_mov_b32 v7, 0\nMvaa 84,212,224\nv_mov_b32 v8, 0\nMvaa 88,216,224\n"
"v_mov_b32 v9, 0\nCV 32,64,65\nMvaa 92,220,224\nD1 1,16,46,1024\nCV 33,66,67\nMvaa 96,192,228\nv_mov_b32 v10, 0\nCV 34,68,69\nMvaa 100,196,228\nv_mov_b32 v11, 0\nCV 35,70,71\n"
"Mvaa 104,200,228\nv_mov_b32 v12, 0\nCV 36,72,73\nMvaa 108,204,228\nD1 1,16,46,2048\nCV 37,74,75\nMvaa 112,208,228\nv_mov_b32 v13, 0\nCV 38,76,77\nMvaa 116,212,228\n"
"v_mov_b32 v14, 0\nCV 39,78,79\nMvaa 120,216,228\nv_mov_b32 v15, 0\nCV 40,80,81\nMvaa 124,220,228\nD1 1,16,46,3072\nCV 41,82,83\nMvaa 128,192,232\nv_mov_b32 v16, 0\nCV 42,84,85\n"
"Mvaa 132,196,232\nv_mov_b32 v17, 0\nMvaa 136,200,232\nv_mov_b32 v18, 0\nCV 43,86,87\nMvaa 140,204,232\nv_mov_b32 v19, 0\nCV 44,88,89\nMvaa 144,208,232\nCV 45,90,91\nCV 46,92,93\n"
"Mvaa 148,212,232\nCV 47,94,95\nCV 48,96,97\nMvaa 152,216,232\nCV 49,98,99\nCV 50,100,101\nMvaa 156,220,232\nCV 51,102,103\nCV 52,104,105\nMvaa 160,192,236\nCV 53,106,107\n"
"CV 54,108,109\nMvaa 164,196,236\nCV 55,110,111\nCV 56,112,113\nSv 32,1,16,75,0\nMvaa 168,200,236\nCV 57,114,115\nCV 58,116,117\nMvaa 172,204,236\nCV 59,118,119\nCV 60,120,121\n"
"Mvaa 176,208,236\nCV 61,122,123\nCV 62,124,125\nMvaa 180,212,236\nCV 63,126,127\nCV 96,128,129\nSv 36,1,16,75,1024\nMvaa 184,216,236\nCV 97,130,131\nCV 98,132,133\n"
"Mvaa 188,220,236\nCV 99,134,135\nCV 100,136,137\nMaaa 0,192,240\nCV 101,138,139\nCV 102,140,141\nMaaa 4,196,240\nCV 103,142,143\nSv 40,1,16,75,2048\nMaaa 8,200,240\n"
"CV 104,144,145\nCV 105,146,147\nMaaa 12,204,240\nCV 106,148,149\nCV 107,150,151\nMaaa 16,208,240\nCV 108,152,153\nCV 109,154,155\nMaaa 20,212,240\nCV 110,156,157\nCV 111,158,159\n"
"Sv 44,1,16,75,3072\nMaaa 24,216,240\nCV 112,160,161\nCV 113,162,163\nMaaa 28,220,240\nCV 114,164,165\nCV 115,166,167\ns_add_u32 s75, s75, s55\nMaaa 32,192,244\nCV 116,168,169\n"
"CV 117,170,171\nMaaa 36,196,244\nCV 118,172,173\nCV 119,174,175\nSv 48,1,16,75,0\nMaaa 40,200,244\nCV 120,176,177\nCV 121,178,179\nMaaa 44,204,244\nCV 122,180,181\nCV 123,182,183\n"
"Maaa 48,208,244\nCV 124,184,185\nCV 125,186,187\nMaaa 52,212,244\nCV 126,188,189\nCV 127,190,191\nSv 52,1,16,75,1024\nMaaa 56,216,244\nv_mov_b32 v86, 0\nv_mov_b32 v87, 0\n"
"Maaa 60,220,244\nv_mov_b32 v88, 0\nv_mov_b32 v89, 0\nMaaa 64,192,248\nRa 128,24,0\nAR 92,0\nAR 93,1\nMaaa 68,196,248\nSv 56,1,16,75,2048\nCV 128,92,93\nMaaa 72,200,248\n"
"Ra 132,24,1024\nAR 92,2\nMaaa 76,204,248\nAR 93,3\nCV 129,92,93\nMaaa 80,208,248\nRa 136,24,2048\nAR 92,4\nAR 93,5\nMaaa 84,212,248\nSv 60,1,16,75,3072\nCV 130,92,93\n"
"Maaa 88,216,248\nRa 140,24,3072\nAR 92,6\nMaaa 92,220,248\nAR 93,7\nCV 131,92,93\nMaaa 96,192,252\nRa 144,26,0\nAR 92,8\nAR 93,9\nMaaa 100,196,252\nCV 132,92,93\nAR 92,10\n"
"Maaa 104,200,252\nRa 148,26,1024\nAR 93,11\nMaaa 108,204,252\nCV 133,92,93\nAR 92,12\nMaaa 112,208,252\nRa 152,26,2048\nAR 93,13\nCV 134,92,93\nMaaa 116,212,252\nAR 92,14\n"
"AR 93,15\nMaaa 120,216,252\nRa 156,26,3072\nCV 135,92,93\nMaaa 124,220,252\nWL 4\nCvav 32,128,192,4\nRa 160,24,8192\nMvav 32,132,200\nMvav 32,136,208\nRa 164,24,9216\n"
"Mvav 32,140,216\nCvav 36,140,220,8\nRa 168,24,10240\nAR 92,16\nAR 93,17\nMvav 36,136,212\ns_add_u32 m0, s89, 32768\nCV 136,92,93\nAR 92,18\nAR 93,19\nMvav 36,132,204\n"
"Ra 172,24,11264\nWL 4\ns_barrier\nMvav 36,128,196\nCV 137,92,93\nAR 92,20\nAR 93,21\nCV 138,92,93\nMvav 36,144,228\nRa 176,26,8192\nAR 92,22\nAR 93,23\nSv 96,1,16,76,0\n"
"Mvav 36,148,236\ns_add_u32 s46, s86, s88\nCV 139,92,93\nAR 92,24\nMvav 36,152,244\nRa 180,26,9216\nAR 93,25\nCV 140,92,93\nAR 92,26\nMvav 36,156,252\nAR 93,27\nCV 141,92,93\n"
"AR 92,28\nMvav 32,156,248\nRa 184,26,10240\nAR 93,29\nCV 142,92,93\nAR 92,30\nMvav 32,152,240\nD1 1,16,46,0\nAR 93,31\nCV 143,92,93\nMvav 32,148,232\nRa 188,26,11264\nWL 4\n"
"AR 92,32\nAR 93,33\nMvav 32,144,224\nCV 144,92,93\nAR 92,34\nAR 93,35\nCvav 40,160,192,12\nCV 145,92,93\nAR 92,36\nAR 93,37\nCV 146,92,93\nMvav 40,164,200\nAR 92,38\nAR 93,39\n"
"CV 147,92,93\nMvav 40,168,208\nv_and_b32 v27, 0x70, v1\nbuffer_load_dword v27, v27, s[16:19], s61 offen offset:136 sc0 sc1\nAR 92,40\nMvav 40,172,216\nAR 93,41\nCV 148,92,93\n"
"AR 92,42\nAR 93,43\nCvav 44,172,220,16\nCV 149,92,93\nAR 92,44\nAR 93,45\nMvav 44,168,212\nD1 1,16,46,1024\nCV 150,92,93\nAR 92,46\nAR 93,47\nMvav 44,164,204\nWL 0\nCV 151,92,93\n"
"AR 92,48\nMvav 44,160,196\nAR 93,49\nCV 152,92,93\nAR 92,50\nAR 93,51\nMvav 44,176,228\nCV 153,92,93\nAR 92,52\nAR 93,53\nSv 100,1,16,76,1024\nMvav 44,180,236\n"
"s_add_u32 s88, s88, 32768\nEX 80,32\nMvav 44,184,244\nCV 154,92,93\nAR 92,54\nAR 93,55\nCV 155,92,93\nMvav 44,188,252\nEX 81,33\nMvav 40,188,248\nAR 92,56\nAR 93,57\nCV 156,92,93\n"
"AR 92,58\nMvav 40,184,240\nD1 1,16,46,2048\nAR 93,59\nCV 157,92,93\nMvav 40,180,232\nAR 92,60\nAR 93,61\nCV 158,92,93\nAR 92,62\nMvav 40,176,224\nAD 84,80,81\nCV 64,80,81\n"
"AR 93,63\nCV 159,92,93\nAR 92,64\nAR 93,65\nCV 160,92,93\nEX 82,34\nAR 92,66\nAR 93,67\nCV 161,92,93\nAR 92,68\nWV 6\nEX 83,35\nAR 93,69\nAD 84,84,82\nCV 162,92,93\nAR 92,70\n"
"D1 1,16,46,3072\ns_add_u32 m0, s89, 49152\nAR 93,71\nCV 163,92,93\ns_barrier\nAR 92,72\nAR 93,73\nAD 84,84,83\nCV 65,82,83\nCV 164,92,93\nAR 92,74\nRa 128,24,32768\nAR 93,75\n"
"CV 165,92,93\nSv 104,1,16,76,2048\ns_add_u32 s46, s87, s88\nEX 80,36\nbuffer_store_dword v25, off, s[16:19], s54 offset:12 sc0 sc1\nRa 132,24,33792\nAR 92,76\nAR 93,77\n"
"CV 166,92,93\nEX 81,37\nRa 136,24,34816\nAR 92,78\nAR 93,79\nCV 167,92,93\nD1 1,16,46,0\nAR 92,80\nAR 93,81\nRa 140,24,35840\nCV 168,92,93\nAR 92,82\nAR 93,83\nAD 85,80,81\n"
"CV 68,80,81\nCV 169,92,93\nRa 144,26,32768\nAR 92,84\nAR 93,85\nCV 170,92,93\nEX 82,38\nAR 92,86\nRa 148,26,33792\nAR 93,87\nCV 171,92,93\nEX 83,39\nAR 92,88\nRa 152,26,34816\n"
"AD 85,85,82\nAR 93,89\nD1 1,16,46,1024\nCV 172,92,93\nAR 92,90\nAR 93,91\nRa 156,26,35840\nCV 173,92,93\nAR 92,92\nAD 85,85,83\nCV 69,82,83\nAR 93,93\nCV 174,92,93\n"
"Ra 160,24,40960\nAR 92,94\nAR 93,95\nSv 108,1,16,76,3072\ns_xor_b32 s89, s89, 0x10000\nEX 80,40\nRa 164,24,41984\nCV 175,92,93\nAR 92,96\nAR 93,97\nWL 6\nEX 81,41\n"
"Ra 168,24,43008\nAD 84,84,80\nCV 176,92,93\nAR 92,98\nD1 1,16,46,2048\nAR 93,99\nCV 177,92,93\nRa 172,24,44032\nAR 92,100\nAR 93,101\nCV 178,92,93\nAD 84,84,81\nCV 66,80,81\n"
"AR 92,102\nCvav 48,128,192,4\nRa 176,26,40960\nAR 93,103\nCV 179,92,93\nAR 92,104\nMvav 48,132,200\nEX 82,42\nAR 93,105\nMvav 48,136,208\nRa 180,26,41984\nCV 180,92,93\nAR 92,106\n"
"Mvav 48,140,216\nWL 6\nEX 83,43\nAR 93,107\nWV 10\nv_cmp_ne_u32 vcc, s15, v27\ns_nop 0\ns_cbranch_vccnz .S39\n.S40:\nCvav 52,140,220,8\nRa 184,26,43008\nAD 84,84,82\nCV 181,92,93\n"
"Mvav 52,136,212\nD1 1,16,46,3072\ns_add_u32 m0, s89, 0\nAR 92,108\nAR 93,109\nMvav 52,132,204\nRa 188,26,44032\nCV 182,92,93\nAR 92,110\nMvav 52,128,196\nAD 84,84,83\nCV 67,82,83\n"
"AR 93,111\nCV 183,92,93\nMvav 52,144,228\nRa 128,2,49152\nAR 92,112\nAR 93,113\ns_add_u32 s76, s76, s55\nSv 112,1,16,76,0\nMvav 52,148,236\ns_add_u32 s46, s86, s88\nEX 80,44\n"
"Mvav 52,152,244\nRa 132,2,50176\nv_xor_b32 v24, 0x10000, v24\nCV 184,92,93\nAR 92,114\nMvav 52,156,252\nWL 6\nEX 81,45\nMvav 48,156,248\nRa 136,2,51200\nAD 85,85,80\nAR 93,115\n"
"CV 185,92,93\nMvav 48,152,240\nD1 1,16,46,0\nAR 92,116\nAR 93,117\nMvav 48,148,232\nRa 140,2,52224\nCV 186,92,93\nAR 92,118\nAR 93,119\nMvav 48,144,224\nAD 85,85,81\nCV 70,80,81\n"
"CV 187,92,93\nCvav 56,160,192,12\nRa 144,2,53248\nAR 92,120\nAR 93,121\nCV 188,92,93\nMvav 56,164,200\nEX 82,46\nAR 92,122\nMvav 56,168,208\nRa 148,2,54272\n"
"v_xor_b32 v26, 0x10000, v26\nAR 93,123\nMvav 56,172,216\nWL 6\nEX 83,47\nCV 189,92,93\nCvav 60,172,220,16\nRa 152,2,55296\nAD 85,85,82\nAR 92,124\nMvav 60,168,212\n"
"D1 1,16,46,1024\nAR 93,125\nCV 190,92,93\nAR 92,126\nMvav 60,164,204\nRa 156,2,56320\nAR 93,127\nCV 191,92,93\nMvav 60,160,196\nAD 85,85,83\nCV 71,82,83\nMvav 60,176,228\n"
"Ra 160,2,57344\nv_max_f32 v90, v84, v85\nSv 116,1,16,76,1024\nMvav 60,180,236\ns_add_u32 s88, s88, 32768\nEX 80,48\nMvav 60,184,244\nRa 164,2,58368\nv_cmp_lt_f32 vcc, s63, v90\n"
"Mvav 60,188,252\nWL 8\nEX 81,49\nMvav 56,188,248\nRa 168,2,59392\ns_cbranch_vccnz .T24\n.T25:\nv_mov_b32 v20, v84\nv_mov_b32 v21, v85\nv_mov_b32 v22, v20\nv_mov_b32 v23, v21\n"
"Mvav 56,184,240\nD1 1,16,46,2048\nv_permlane32_swap_b32 v20, v22\nv_permlane32_swap_b32 v21, v23\nAD 20,20,22\nAD 21,21,23\nv_mov_b32 v22, v20\nv_mov_b32 v23, v21\n"
"Mvav 56,180,232\nRa 172,2,60416\nv_permlane16_swap_b32 v20, v22\nv_permlane16_swap_b32 v21, v23\nAD 20,20,22\nAD 21,21,23\nv_min_f32 v90, v20, v21\nv_cmp_gt_f32 vcc, s64, v90\n"
"s_nop 0\ns_cbranch_vccnz .T26\n.T27:\nAD 86,86,84\nMvav 56,176,224\nAD 84,80,81\nCV 72,80,81\nZaav 0,128,64\nRa 176,2,61440\nZaav 4,128,68\nEX 82,50\nZaav 12,132,68\n"
"Ra 180,2,62464\nZaav 8,132,64\nWL 8\nEX 83,51\nZaav 16,136,64\nRa 184,2,63488\nAD 84,84,82\nZaav 20,136,68\nD1 1,16,46,3072\ns_add_u32 m0, s89, 16384\nZaav 28,140,68\n"
"Ra 188,2,64512\ns_barrier\nZaav 24,140,64\nAD 84,84,83\nCV 73,82,83\nZaav 32,144,64\nRa 128,24,0\nSv 120,1,16,76,2048\nZaav 36,144,68\ns_add_u32 s46, s87, s88\nEX 80,52\n"
"Zaav 44,148,68\nRa 132,24,1024\nv_xor_b32 v2, 0x10000, v2\nZaav 40,148,64\nWL 8\nEX 81,53\nZaav 48,152,64\nRa 136,24,2048\nAD 87,87,85\nZaav 52,152,68\nD1 1,16,46,0\n"
"Zaav 60,156,68\nRa 140,24,3072\nZaav 56,156,64\nAD 85,80,81\nCV 76,80,81\nZaav 64,160,64\nRa 144,26,0\nZaav 68,160,68\nEX 82,54\nZaav 76,164,68\nRa 148,26,1024\nZaav 72,164,64\n"
"WL 8\nEX 83,55\nZaav 80,168,64\nRa 152,26,2048\nAD 85,85,82\nZaav 84,168,68\nD1 1,16,46,1024\nZaav 92,172,68\nRa 156,26,3072\nZaav 88,172,64\nAD 85,85,83\nCV 77,82,83\n"
"Zaav 96,176,64\nRa 160,24,8192\nSv 124,1,16,76,3072\nZaav 100,176,68\nEX 80,56\nZaav 108,180,68\nRa 164,24,9216\nZaav 104,180,64\nWL 6\nEX 81,57\nZaav 112,184,64\nRa 168,24,10240\n"
"AD 84,84,80\nZaav 116,184,68\nD1 1,16,46,2048\nZaav 124,188,68\nRa 172,24,11264\nZaav 120,188,64\nAD 84,84,81\nCV 74,80,81\nCvav 32,128,192,4\nRa 176,26,8192\nMvav 32,132,200\n"
"EX 82,58\nMvav 32,136,208\nRa 180,26,9216\nMvav 32,140,216\nWL 6\nEX 83,59\nCvav 36,140,220,8\nRa 184,26,10240\nAD 84,84,82\nMvav 36,136,212\nD1 1,16,46,3072\n"
"s_add_u32 m0, s89, 32768\nMvav 36,132,204\nRa 188,26,11264\nMvav 36,128,196\nAD 84,84,83\nCV 75,82,83\nMvav 36,144,228\nRa 128,2,16384\nSv 128,1,16,77,0\nMvav 36,148,236\n"
"s_add_u32 s46, s86, s88\nEX 80,60\nMvav 36,152,244\nRa 132,2,17408\nMvav 36,156,252\nWL 6\nEX 81,61\nMvav 32,156,248\nRa 136,2,18432\nAD 85,85,80\nMvav 32,152,240\nD1 1,16,46,0\n"
"Mvav 32,148,232\nRa 140,2,19456\nMvav 32,144,224\nAD 85,85,81\nCV 78,80,81\nCvav 40,160,192,12\nRa 144,2,20480\nMvav 40,164,200\nEX 82,62\nMvav 40,168,208\nRa 148,2,21504\n"
"Mvav 40,172,216\nWL 6\nEX 83,63\nCvav 44,172,220,16\nRa 152,2,22528\nAD 85,85,82\nMvav 44,168,212\nD1 1,16,46,1024\nMvav 44,164,204\nRa 156,2,23552\nMvav 44,160,196\nAD 85,85,83\n"
"CV 79,82,83\nMvav 44,176,228\nRa 160,2,24576\nv_max_f32 v90, v84, v85\nSv 132,1,16,77,1024\nMvav 44,180,236\ns_add_u32 s88, s88, 32768\nEX 80,32\nMvav 44,184,244\nRa 164,2,25600\n"
"v_cmp_lt_f32 vcc, s63, v90\nMvav 44,188,252\nWL 8\nEX 81,33\nMvav 40,188,248\nRa 168,2,26624\ns_cbranch_vccnz .T28\n.T29:\nMvav 40,184,240\nD1 1,16,46,2048\nMvav 40,180,232\n"
"Ra 172,2,27648\nAD 86,86,84\nMvav 40,176,224\nAD 84,80,81\nCV 64,80,81\nMaav 0,128,72\nRa 176,2,28672\nv_mov_b32 v112, -1\nMaav 4,128,76\nEX 82,34\nMaav 12,132,76\nRa 180,2,29696\n"
"Maav 8,132,72\ns_waitcnt vmcnt(17) lgkmcnt(8)\nEX 83,35\nMaav 16,136,72\nRa 184,2,30720\nAD 84,84,82\nMaav 20,136,76\nD1 1,16,46,3072\ns_add_u32 m0, s89, 49152\nMaav 28,140,76\n"
"Ra 188,2,31744\ns_barrier\nMaav 24,140,72\nAD 84,84,83\nCV 65,82,83\nMaav 32,144,72\nRa 128,24,32768\nSv 136,1,16,77,2048\nMaav 36,144,76\ns_add_u32 s46, s87, s88\nEX 80,36\n"
"Maav 44,148,76\nRa 132,24,33792\nMaav 40,148,72\nWL 8\nEX 81,37\nMaav 48,152,72\nRa 136,24,34816\nAD 87,87,85\nMaav 52,152,76\nD1 1,16,46,0\nMaav 60,156,76\nRa 140,24,35840\n"
"Maav 56,156,72\nAD 85,80,81\nCV 68,80,81\nMaav 64,160,72\nRa 144,26,32768\nv_mov_b32 v113, -1\nMaav 68,160,76\nEX 82,38\nMaav 76,164,76\nRa 148,26,33792\nMaav 72,164,72\nWL 8\n"
"EX 83,39\nMaav 80,168,72\nRa 152,26,34816\nAD 85,85,82\nMaav 84,168,76\nD1 1,16,46,1024\nMaav 92,172,76\nRa 156,26,35840\nMaav 88,172,72\nAD 85,85,83\nCV 69,82,83\nMaav 96,176,72\n"
"Ra 160,24,40960\nSv 140,1,16,77,3072\nMaav 100,176,76\ns_xor_b32 s89, s89, 0x10000\nEX 80,40\nMaav 108,180,76\nRa 164,24,41984\nMaav 104,180,72\nWL 6\nEX 81,41\nMaav 112,184,72\n"
"Ra 168,24,43008\nAD 84,84,80\nMaav 116,184,76\nD1 1,16,46,2048\nMaav 124,188,76\nRa 172,24,44032\nMaav 120,188,72\nAD 84,84,81\nCV 66,80,81\nCvav 48,128,192,4\nRa 176,26,40960\n"
"v_mov_b32 v114, -1\nMvav 48,132,200\nEX 82,42\nMvav 48,136,208\nRa 180,26,41984\nMvav 48,140,216\nWL 6\nEX 83,43\nCvav 52,140,220,8\nRa 184,26,43008\nAD 84,84,82\n"
"v_and_b32 v27, 0xf0, v1\nbuffer_load_dword v27, v27, s[16:19], s61 offen offset:12 sc0 sc1\nMvav 52,136,212\nD1 1,16,46,3072\ns_add_u32 m0, s89, 0\nMvav 52,132,204\n"
"Ra 188,26,44032\nMvav 52,128,196\nAD 84,84,83\nCV 67,82,83\nMvav 52,144,228\nRa 128,2,49152\ns_add_u32 s77, s77, s55\nSv 144,1,16,77,0\nMvav 52,148,236\ns_add_u32 s46, s86, s88\n"
"EX 80,44\nMvav 52,152,244\nRa 132,2,50176\nv_xor_b32 v24, 0x10000, v24\nMvav 52,156,252\nWL 6\nEX 81,45\nMvav 48,156,248\nRa 136,2,51200\nAD 85,85,80\nMvav 48,152,240\n"
"D1 1,16,46,0\nMvav 48,148,232\nRa 140,2,52224\nMvav 48,144,224\nAD 85,85,81\nCV 70,80,81\nCvav 56,160,192,12\nRa 144,2,53248\nv_mov_b32 v115, -1\nMvav 56,164,200\nEX 82,46\n"
"Mvav 56,168,208\nRa 148,2,54272\nv_xor_b32 v26, 0x10000, v26\nMvav 56,172,216\nWL 6\nEX 83,47\nCvav 60,172,220,16\nRa 152,2,55296\nAD 85,85,82\nMvav 60,168,212\nD1 1,16,46,1024\n"
"Mvav 60,164,204\nRa 156,2,56320\nMvav 60,160,196\nAD 85,85,83\nCV 71,82,83\nMvav 60,176,228\nRa 160,2,57344\nv_max_f32 v90, v84, v85\nSv 148,1,16,77,1024\nMvav 60,180,236\n"
"s_add_u32 s88, s88, 32768\nEX 80,48\nMvav 60,184,244\nRa 164,2,58368\nv_cmp_lt_f32 vcc, s63, v90\nMvav 60,188,252\nWL 8\nEX 81,49\nMvav 56,188,248\nRa 168,2,59392\n"
"s_cbranch_vccnz .T30\n.T31:\nMvav 56,184,240\nD1 1,16,46,2048\nMvav 56,180,232\nRa 172,2,60416\nAD 86,86,84\nMvav 56,176,224\nAD 84,80,81\nCV 72,80,81\nMaav 0,128,64\n"
"Ra 176,2,61440\nMaav 4,128,68\nEX 82,50\nMaav 12,132,68\nRa 180,2,62464\nMaav 8,132,64\ns_waitcnt vmcnt(18) lgkmcnt(8)\nEX 83,51\nMaav 16,136,64\nRa 184,2,63488\nAD 84,84,82\n"
"Maav 20,136,68\nD1 1,16,46,3072\ns_add_u32 m0, s89, 16384\nMaav 28,140,68\nRa 188,2,64512\ns_barrier\nMaav 24,140,64\nAD 84,84,83\nCV 73,82,83\nMaav 32,144,64\nRa 128,24,0\n"
"Sv 152,1,16,77,2048\nMaav 36,144,68\ns_add_u32 s46, s87, s88\nEX 80,52\nMaav 44,148,68\nRa 132,24,1024\nv_xor_b32 v2, 0x10000, v2\nMaav 40,148,64\nWL 8\nEX 81,53\nMaav 48,152,64\n"
"Ra 136,24,2048\nAD 87,87,85\nMaav 52,152,68\nD1 1,16,46,0\nMaav 60,156,68\nRa 140,24,3072\nMaav 56,156,64\nAD 85,80,81\nCV 76,80,81\nMaav 64,160,64\nRa 144,26,0\nMaav 68,160,68\n"
"EX 82,54\nMaav 76,164,68\nRa 148,26,1024\nMaav 72,164,64\nWL 8\nEX 83,55\nMaav 80,168,64\nRa 152,26,2048\nAD 85,85,82\nMaav 84,168,68\nD1 1,16,46,1024\nMaav 92,172,68\n"
"Ra 156,26,3072\nMaav 88,172,64\nAD 85,85,83\nCV 77,82,83\nMaav 96,176,64\nRa 160,24,8192\nSv 156,1,16,77,3072\nMaav 100,176,68\nEX 80,56\nMaav 108,180,68\nRa 164,24,9216\n"
"Maav 104,180,64\nWL 6\nEX 81,57\nMaav 112,184,64\nRa 168,24,10240\nAD 84,84,80\nMaav 116,184,68\nD1 1,16,46,2048\nMaav 124,188,68\nRa 172,24,11264\nMaav 120,188,64\nAD 84,84,81\n"
"CV 74,80,81\nCvav 32,128,192,4\nRa 176,26,8192\nMvav 32,132,200\nEX 82,58\nMvav 32,136,208\nRa 180,26,9216\nMvav 32,140,216\nWL 6\nEX 83,59\nCvav 36,140,220,8\nRa 184,26,10240\n"
"AD 84,84,82\nMvav 36,136,212\nD1 1,16,46,3072\ns_add_u32 m0, s89, 32768\nMvav 36,132,204\nRa 188,26,11264\nMvav 36,128,196\nAD 84,84,83\nCV 75,82,83\nMvav 36,144,228\n"
"Ra 128,2,16384\nSv 160,1,16,78,0\nMvav 36,148,236\ns_add_u32 s46, s86, s88\nEX 80,60\nMvav 36,152,244\nRa 132,2,17408\nMvav 36,156,252\nWL 6\nEX 81,61\nMvav 32,156,248\n"
"Ra 136,2,18432\nAD 85,85,80\nMvav 32,152,240\nD1 1,16,46,0\nMvav 32,148,232\nRa 140,2,19456\nSv 112,1,16,49,0\nMvav 32,144,224\nAD 85,85,81\nCV 78,80,81\nCvav 40,160,192,12\n"
"Ra 144,2,20480\nMvav 40,164,200\nEX 82,62\nMvav 40,168,208\nRa 148,2,21504\nMvav 40,172,216\nWL 6\nEX 83,63\nCvav 44,172,220,16\nRa 152,2,22528\nAD 85,85,82\nMvav 44,168,212\n"
"D1 1,16,46,1024\nMvav 44,164,204\nRa 156,2,23552\nMvav 44,160,196\nAD 85,85,83\nCV 79,82,83\nMvav 44,176,228\nRa 160,2,24576\nv_max_f32 v90, v84, v85\nSv 164,1,16,78,1024\n"
"Mvav 44,180,236\ns_add_u32 s88, s88, 32768\nEX 80,32\nMvav 44,184,244\nRa 164,2,25600\nv_cmp_lt_f32 vcc, s63, v90\nMvav 44,188,252\nWL 8\nEX 81,33\nMvav 40,188,248\n"
"Ra 168,2,26624\ns_cbranch_vccnz .T32\n.T33:\nMvav 40,184,240\nD1 1,16,46,2048\nMvav 40,180,232\nRa 172,2,27648\nAD 86,86,84\nMvav 40,176,224\nAD 84,80,81\nCV 64,80,81\n"
"Maav 0,128,72\nRa 176,2,28672\nMaav 4,128,76\nEX 82,34\nMaav 12,132,76\nRa 180,2,29696\nMaav 8,132,72\ns_waitcnt vmcnt(18) lgkmcnt(8)\nEX 83,35\nMaav 16,136,72\nRa 184,2,30720\n"
"AD 84,84,82\nMaav 20,136,76\nD1 1,16,46,3072\ns_add_u32 m0, s89, 49152\nMaav 28,140,76\nRa 188,2,31744\ns_barrier\nMaav 24,140,72\nAD 84,84,83\nCV 65,82,83\nMaav 32,144,72\n"
"Ra 128,24,32768\nSv 168,1,16,78,2048\nMaav 36,144,76\ns_add_u32 s46, s87, s88\nEX 80,36\nMaav 44,148,76\nRa 132,24,33792\nMaav 40,148,72\nWL 8\nEX 81,37\nMaav 48,152,72\n"
"Ra 136,24,34816\nAD 87,87,85\nMaav 52,152,76\nD1 1,16,46,0\nMaav 60,156,76\nRa 140,24,35840\nMaav 56,156,72\nAD 85,80,81\nCV 68,80,81\nMaav 64,160,72\nRa 144,26,32768\n"
"Maav 68,160,76\nEX 82,38\nMaav 76,164,76\nRa 148,26,33792\nMaav 72,164,72\nWL 8\nEX 83,39\nMaav 80,168,72\nRa 152,26,34816\nAD 85,85,82\nMaav 84,168,76\nD1 1,16,46,1024\n"
"Maav 92,172,76\nRa 156,26,35840\nMaav 88,172,72\nAD 85,85,83\nCV 69,82,83\nMaav 96,176,72\nRa 160,24,40960\nSv 172,1,16,78,3072\nMaav 100,176,76\ns_xor_b32 s89, s89, 0x10000\n"
"EX 80,40\nMaav 108,180,76\nRa 164,24,41984\nMaav 104,180,72\nWL 6\nEX 81,41\nMaav 112,184,72\nRa 168,24,43008\nAD 84,84,80\nMaav 116,184,76\nD1 1,16,46,2048\nMaav 124,188,76\n"
"Ra 172,24,44032\nMaav 120,188,72\nAD 84,84,81\nCV 66,80,81\nCvav 48,128,192,4\nRa 176,26,40960\nMvav 48,132,200\nEX 82,42\nMvav 48,136,208\nRa 180,26,41984\nMvav 48,140,216\nWL 6\n"
"EX 83,43\nv_cmp_ne_u32 vcc, s15, v27\ns_nop 0\ns_cbranch_vccnz .S36\n.S37:\nCvav 52,140,220,8\nRa 184,26,43008\nAD 84,84,82\nMvav 52,136,212\nD1 1,16,46,3072\n"
"s_add_u32 m0, s89, 0\nMvav 52,132,204\nRa 188,26,44032\nMvav 52,128,196\nAD 84,84,83\nCV 67,82,83\nMvav 52,144,228\nRa 128,2,49152\ns_add_u32 s78, s78, s55\nSv 176,1,16,78,0\n"
"Mvav 52,148,236\ns_add_u32 s46, s86, s88\nEX 80,44\nMvav 52,152,244\nRa 132,2,50176\nv_xor_b32 v24, 0x10000, v24\nMvav 52,156,252\nWL 6\nEX 81,45\nMvav 48,156,248\nRa 136,2,51200\n"
"AD 85,85,80\nMvav 48,152,240\nD1 1,16,46,0\nv_lshrrev_b32 v92, 8, v1\nMvav 48,148,232\nRa 140,2,52224\nSv 112,1,16,49,1024\nMvav 48,144,224\nAD 85,85,81\nCV 70,80,81\n"
"Cvav 56,160,192,12\nRa 144,2,53248\nv_bfe_u32 v93, v1, 6, 2\nMvav 56,164,200\ns_add_u32 s49, s49, 131072\nEX 82,46\nMvav 56,168,208\nRa 148,2,54272\nv_xor_b32 v26, 0x10000, v26\n"
"Mvav 56,172,216\nWL 6\nEX 83,47\nCvav 60,172,220,16\nRa 152,2,55296\nAD 85,85,82\nMvav 60,168,212\nD1 1,16,46,1024\nv_cmp_eq_u32 vcc, v92, v93\nv_and_b32 v92, 16, v1\n"
"v_mov_b32 v93, 0x3f80\nv_cndmask_b32 v93, 0, v93, vcc\nMvav 60,164,204\nRa 156,2,56320\nMvav 60,160,196\nAD 85,85,83\nCV 71,82,83\nMvav 60,176,228\nRa 160,2,57344\n"
"v_max_f32 v90, v84, v85\nSv 180,1,16,78,1024\nMvav 60,180,236\ns_add_u32 s88, s88, 32768\nEX 80,48\nMvav 60,184,244\nRa 164,2,58368\nv_cmp_lt_f32 vcc, s63, v90\nMvav 60,188,252\n"
"WL 8\nEX 81,49\nMvav 56,188,248\nRa 168,2,59392\ns_cbranch_vccnz .T34\n.T35:\nMvav 56,184,240\nD1 1,16,46,2048\nv_lshlrev_b32 v93, v92, v93\nMvav 56,180,232\nRa 172,2,60416\n"
"AD 86,86,84\nMvav 56,176,224\nAD 84,80,81\nCV 72,80,81\nMaav 0,128,64\nRa 176,2,61440\nv_and_b32 v92, 32, v1\nMaav 4,128,68\nEX 82,50\nMaav 12,132,68\nRa 180,2,62464\n"
"v_cmp_eq_u32 vcc, 0, v92\nv_mov_b32 v128, 0\nv_mov_b32 v129, 0\nv_cndmask_b32 v130, 0, v93, vcc\nMaav 8,132,64\ns_waitcnt vmcnt(19) lgkmcnt(8)\nEX 83,51\nMaav 16,136,64\n"
"Ra 184,2,63488\nAD 84,84,82\nMaav 20,136,68\nD1 1,16,46,3072\ns_add_u32 m0, s89, 16384\nMaav 28,140,68\nRa 188,2,64512\ns_barrier\nMaav 24,140,64\nAD 84,84,83\nCV 73,82,83\n"
"Maav 32,144,64\nRa 128,24,0\nSv 184,1,16,78,2048\nMaav 36,144,68\ns_add_u32 s46, s87, s88\nEX 80,52\nMaav 44,148,68\nRa 132,24,1024\nv_xor_b32 v2, 0x10000, v2\nMaav 40,148,64\n"
"WL 8\nEX 81,53\nMaav 48,152,64\nRa 136,24,2048\nAD 87,87,85\nMaav 52,152,68\nD1 1,16,46,0\nv_sub_u32 v131, v93, v130\nMaav 60,156,68\nRa 140,24,3072\nMaav 56,156,64\nAD 85,80,81\n"
"CV 76,80,81\nMaav 64,160,64\nRa 144,26,0\nv_mov_b32 v132, 0\nMaav 68,160,68\nEX 82,54\nMaav 76,164,68\nRa 148,26,1024\nMaav 72,164,64\nWL 8\nEX 83,55\nMaav 80,168,64\n"
"Ra 152,26,2048\nAD 85,85,82\nMaav 84,168,68\nD1 1,16,46,1024\nMaav 92,172,68\nRa 156,26,3072\nMaav 88,172,64\nAD 85,85,83\nCV 77,82,83\nMaav 96,176,64\nRa 160,24,8192\n"
"Sv 188,1,16,78,3072\nMaav 100,176,68\nEX 80,56\nMaav 108,180,68\nRa 164,24,9216\nv_mov_b32 v133, 0\nMaav 104,180,64\nWL 6\nEX 81,57\nMaav 112,184,64\nRa 168,24,10240\nAD 84,84,80\n"
"Maav 116,184,68\nD1 1,16,46,2048\nMaav 124,188,68\nRa 172,24,11264\nMaav 120,188,64\nAD 84,84,81\nCV 74,80,81\ns_mov_b32 s99, 11\ns_sub_u32 s90, s90, 3\n.p2align 6\n.A42:\n"
"Cvav 32,128,192,4\nRa 176,26,8192\nMvav 32,132,200\nEX 82,58\nMvav 32,136,208\nRa 180,26,9216\nMvav 32,140,216\nWL 6\nEX 83,59\nCvav 36,140,220,8\nRa 184,26,10240\nAD 84,84,82\n"
"Mvav 36,136,212\nD1 1,16,46,3072\ns_add_u32 m0, s89, 32768\nMvav 36,132,204\nRa 188,26,11264\nMvav 36,128,196\nAD 84,84,83\nCV 75,82,83\nMvav 36,144,228\nRa 128,2,16384\n"
"Mvav 36,148,236\ns_add_u32 s46, s86, s88\nEX 80,60\nMvav 36,152,244\nRa 132,2,17408\nMvav 36,156,252\nWL 6\nEX 81,61\nMvav 32,156,248\nRa 136,2,18432\nAD 85,85,80\n"
"Mvav 32,152,240\nD1 1,16,46,0\nMvav 32,148,232\nRa 140,2,19456\nSv 112,1,16,49,0\nMvav 32,144,224\nAD 85,85,81\nCV 78,80,81\nCvav 40,160,192,12\nRa 144,2,20480\nMvav 40,164,200\n"
"EX 82,62\nMvav 40,168,208\nRa 148,2,21504\nMvav 40,172,216\nWL 6\nEX 83,63\nCvav 44,172,220,16\nRa 152,2,22528\nAD 85,85,82\nMvav 44,168,212\nD1 1,16,46,1024\nMvav 44,164,204\n"
"Ra 156,2,23552\nMvav 44,160,196\nAD 85,85,83\nCV 79,82,83\nMvav 44,176,228\nRa 160,2,24576\nv_max_f32 v90, v84, v85\nMvav 44,180,236\ns_add_u32 s88, s88, 32768\nEX 80,32\n"
"Mvav 44,184,244\nRa 164,2,25600\nv_cmp_lt_f32 vcc, s63, v90\nMvav 44,188,252\nWL 8\nEX 81,33\nMvav 40,188,248\nRa 168,2,26624\ns_cbranch_vccnz .T45\n.T46:\nMvav 40,184,240\n"
"D1 1,16,46,2048\nMvav 40,180,232\nRa 172,2,27648\nAD 86,86,84\nMvav 40,176,224\nAD 84,80,81\nCV 64,80,81\nMaav 0,128,72\nRa 176,2,28672\nMaav 4,128,76\nEX 82,34\nMaav 12,132,76\n"
"Ra 180,2,29696\nMaav 8,132,72\ns_waitcnt vmcnt(13) lgkmcnt(8)\nEX 83,35\nMaav 16,136,72\nRa 184,2,30720\nAD 84,84,82\nMaav 20,136,76\nD1 1,16,46,3072\ns_add_u32 m0, s89, 49152\n"
"Maav 28,140,76\nRa 188,2,31744\ns_barrier\nMaav 24,140,72\nAD 84,84,83\nCV 65,82,83\nMaav 32,144,72\nRa 128,24,32768\nMaav 36,144,76\ns_add_u32 s46, s87, s88\nEX 80,36\n"
"Maav 44,148,76\nRa 132,24,33792\nMaav 40,148,72\nWL 8\nEX 81,37\nMaav 48,152,72\nRa 136,24,34816\nAD 87,87,85\nMaav 52,152,76\nD1 1,16,46,0\nMaav 60,156,76\nRa 140,24,35840\n"
"Maav 56,156,72\nAD 85,80,81\nCV 68,80,81\nMaav 64,160,72\nRa 144,26,32768\nMaav 68,160,76\nEX 82,38\nMaav 76,164,76\nRa 148,26,33792\nMaav 72,164,72\nWL 8\nEX 83,39\n"
"Maav 80,168,72\nRa 152,26,34816\nAD 85,85,82\nMaav 84,168,76\nD1 1,16,46,1024\nMaav 92,172,76\nRa 156,26,35840\nMaav 88,172,72\nAD 85,85,83\nCV 69,82,83\nMaav 96,176,72\n"
"Ra 160,24,40960\nMaav 100,176,76\ns_xor_b32 s89, s89, 0x10000\nEX 80,40\nMaav 108,180,76\nRa 164,24,41984\nMaav 104,180,72\nWL 6\nEX 81,41\nMaav 112,184,72\nRa 168,24,43008\n"
"AD 84,84,80\nMaav 116,184,76\nD1 1,16,46,2048\nMaav 124,188,76\nRa 172,24,44032\nMaav 120,188,72\nAD 84,84,81\nCV 66,80,81\nCvav 48,128,192,4\nRa 176,26,40960\nMvav 48,132,200\n"
"EX 82,42\nMvav 48,136,208\nRa 180,26,41984\nMvav 48,140,216\nWL 6\nEX 83,43\nCvav 52,140,220,8\nRa 184,26,43008\nAD 84,84,82\nMvav 52,136,212\nD1 1,16,46,3072\n"
"s_add_u32 m0, s89, 0\nMvav 52,132,204\nRa 188,26,44032\nMvav 52,128,196\nAD 84,84,83\nCV 67,82,83\nMvav 52,144,228\nRa 128,2,49152\nMvav 52,148,236\ns_add_u32 s46, s86, s88\n"
"EX 80,44\nMvav 52,152,244\nRa 132,2,50176\nv_xor_b32 v24, 0x10000, v24\nMvav 52,156,252\nWL 6\nEX 81,45\nMvav 48,156,248\nRa 136,2,51200\nAD 85,85,80\nMvav 48,152,240\n"
"D1 1,16,46,0\nMvav 48,148,232\nRa 140,2,52224\nSv 112,1,16,49,1024\nMvav 48,144,224\nAD 85,85,81\nCV 70,80,81\nCvav 56,160,192,12\nRa 144,2,53248\nMvav 56,164,200\n"
"s_add_u32 s49, s49, 131072\nEX 82,46\nMvav 56,168,208\nRa 148,2,54272\nv_xor_b32 v26, 0x10000, v26\nMvav 56,172,216\nWL 6\nEX 83,47\nCvav 60,172,220,16\nRa 152,2,55296\n"
"AD 85,85,82\nMvav 60,168,212\nD1 1,16,46,1024\nMvav 60,164,204\nRa 156,2,56320\nMvav 60,160,196\nAD 85,85,83\nCV 71,82,83\nMvav 60,176,228\nRa 160,2,57344\n"
"v_max_f32 v90, v84, v85\nMvav 60,180,236\ns_add_u32 s88, s88, 32768\nEX 80,48\nMvav 60,184,244\nRa 164,2,58368\nv_cmp_lt_f32 vcc, s63, v90\nMvav 60,188,252\nWL 8\nEX 81,49\n"
"Mvav 56,188,248\nRa 168,2,59392\ns_cbranch_vccnz .T47\n.T48:\nMvav 56,184,240\nD1 1,16,46,2048\nMvav 56,180,232\nRa 172,2,60416\nAD 86,86,84\nMvav 56,176,224\nAD 84,80,81\n"
"CV 72,80,81\nMaav 0,128,64\nRa 176,2,61440\nMaav 4,128,68\nEX 82,50\nMaav 12,132,68\nRa 180,2,62464\nMaav 8,132,64\ns_waitcnt vmcnt(13) lgkmcnt(8)\nEX 83,51\nMaav 16,136,64\n"
"Ra 184,2,63488\nAD 84,84,82\nMaav 20,136,68\nD1 1,16,46,3072\ns_add_u32 m0, s89, 16384\nMaav 28,140,68\nRa 188,2,64512\ns_barrier\nMaav 24,140,64\nAD 84,84,83\nCV 73,82,83\n"
"Maav 32,144,64\nRa 128,24,0\nMaav 36,144,68\ns_add_u32 s46, s87, s88\nEX 80,52\nMaav 44,148,68\nRa 132,24,1024\nv_xor_b32 v2, 0x10000, v2\nMaav 40,148,64\nWL 8\nEX 81,53\n"
"Maav 48,152,64\nRa 136,24,2048\nAD 87,87,85\nMaav 52,152,68\nD1 1,16,46,0\nMaav 60,156,68\nRa 140,24,3072\nMaav 56,156,64\nAD 85,80,81\nCV 76,80,81\nMaav 64,160,64\nRa 144,26,0\n"
"Maav 68,160,68\nEX 82,54\nMaav 76,164,68\nRa 148,26,1024\nMaav 72,164,64\nWL 8\nEX 83,55\nMaav 80,168,64\nRa 152,26,2048\nAD 85,85,82\nMaav 84,168,68\nD1 1,16,46,1024\n"
"Maav 92,172,68\nRa 156,26,3072\nMaav 88,172,64\nAD 85,85,83\nCV 77,82,83\nMaav 96,176,64\nRa 160,24,8192\nMaav 100,176,68\ns_sub_u32 s90, s90, 1\nEX 80,56\nMaav 108,180,68\n"
"Ra 164,24,9216\ns_cbranch_scc1 .E43\n.E44:\nMaav 104,180,64\nWL 6\nEX 81,57\nMaav 112,184,64\nRa 168,24,10240\nAD 84,84,80\nMaav 116,184,68\nD1 1,16,46,2048\n"
"s_sub_u32 s99, s99, 1\nMaav 124,188,68\nRa 172,24,11264\nMaav 120,188,64\nAD 84,84,81\nCV 74,80,81\ns_cbranch_scc0 .A42\nCvav 32,128,192,4\nRa 176,26,8192\nMvav 32,132,200\n"
"EX 82,58\nMvav 32,136,208\nRa 180,26,9216\nMvav 32,140,216\nWL 6\nEX 83,59\nCvav 36,140,220,8\nRa 184,26,10240\nAD 84,84,82\nMvav 36,136,212\nD1 1,16,46,3072\n"
"s_add_u32 m0, s89, 32768\nMvav 36,132,204\nRa 188,26,11264\nMvav 36,128,196\nAD 84,84,83\nCV 75,82,83\ns_mov_b32 s94, s96\ns_add_u32 s95, s96, 65536\nv_and_b32 v108, 0xf0, v1\n"
"Mvav 36,144,228\nRa 128,2,16384\nMvav 36,148,236\ns_add_u32 s46, s86, s88\nEX 80,60\nMvav 36,152,244\nRa 132,2,17408\nMvav 36,156,252\nWL 6\nEX 81,61\nMvav 32,156,248\n"
"Ra 136,2,18432\nAD 85,85,80\nMvav 32,152,240\nD1 1,16,46,0\nMvav 32,148,232\nRa 140,2,19456\nSv 112,1,16,49,0\nMvav 32,144,224\nAD 85,85,81\nCV 78,80,81\n"
"buffer_load_dwordx4 v[100:103], v108, s[16:19], s65 offen offset:0 sc0 sc1\nbuffer_load_dwordx4 v[104:107], v108, s[16:19], s65 offen offset:256 sc0 sc1\nCvav 40,160,192,12\n"
"Ra 144,2,20480\nMvav 40,164,200\nEX 82,62\nMvav 40,168,208\nRa 148,2,21504\nMvav 40,172,216\nWL 6\nEX 83,63\nCvav 44,172,220,16\nRa 152,2,22528\nAD 85,85,82\nMvav 44,168,212\n"
"D1 1,16,46,1024\nMvav 44,164,204\nRa 156,2,23552\nMvav 44,160,196\nAD 85,85,83\nCV 79,82,83\nMvav 44,176,228\nRa 160,2,24576\nv_max_f32 v90, v84, v85\nMvav 44,180,236\n"
"s_add_u32 s88, s88, 32768\nEX 80,32\nMvav 44,184,244\nRa 164,2,25600\nv_cmp_lt_f32 vcc, s63, v90\nMvav 44,188,252\nWL 8\nEX 81,33\nMvav 40,188,248\nRa 168,2,26624\n"
"s_cbranch_vccnz .T51\n.T52:\nMvav 40,184,240\nD1 1,16,46,2048\nMvav 40,180,232\nRa 172,2,27648\nAD 86,86,84\nMvav 40,176,224\nAD 84,80,81\nCV 64,80,81\nMaav 0,128,72\n"
"Ra 176,2,28672\nMaav 4,128,76\nEX 82,34\nMaav 12,132,76\nRa 180,2,29696\nMaav 8,132,72\ns_waitcnt vmcnt(15) lgkmcnt(8)\nEX 83,35\nMaav 16,136,72\nRa 184,2,30720\nAD 84,84,82\n"
"Maav 20,136,76\nD1 1,16,46,3072\ns_add_u32 m0, s89, 49152\nMaav 28,140,76\nRa 188,2,31744\ns_barrier\nMaav 24,140,72\nAD 84,84,83\nCV 65,82,83\nMaav 32,144,72\nRa 128,24,32768\n"
"Maav 36,144,76\ns_add_u32 s46, s87, s88\nEX 80,36\nMaav 44,148,76\nRa 132,24,33792\nMaav 40,148,72\nWL 8\nEX 81,37\nMaav 48,152,72\nRa 136,24,34816\nAD 87,87,85\nMaav 52,152,76\n"
"D1 1,16,46,0\nMaav 60,156,76\nRa 140,24,35840\nMaav 56,156,72\nAD 85,80,81\nCV 68,80,81\nMaav 64,160,72\nRa 144,26,32768\nMaav 68,160,76\nEX 82,38\nMaav 76,164,76\n"
"Ra 148,26,33792\nMaav 72,164,72\nWL 8\nEX 83,39\nMaav 80,168,72\nRa 152,26,34816\nAD 85,85,82\nMaav 84,168,76\nD1 1,16,46,1024\nMaav 92,172,76\nRa 156,26,35840\nMaav 88,172,72\n"
"AD 85,85,83\nCV 69,82,83\nMaav 96,176,72\nRa 160,24,40960\nMaav 100,176,76\ns_xor_b32 s89, s89, 0x10000\nEX 80,40\nMaav 108,180,76\nRa 164,24,41984\nMaav 104,180,72\nWL 6\n"
"EX 81,41\nMaav 112,184,72\nRa 168,24,43008\nAD 84,84,80\nMaav 116,184,76\nD1 1,16,46,2048\nMaav 124,188,76\nRa 172,24,44032\nMaav 120,188,72\nAD 84,84,81\nCV 66,80,81\n"
"Cvav 48,128,192,4\nRa 176,26,40960\nMvav 48,132,200\ns_mov_b32 s86, s94\nEX 82,42\nMvav 48,136,208\nRa 180,26,41984\nMvav 48,140,216\nWL 6\nEX 83,43\nCvav 52,140,220,8\n"
"Ra 184,26,43008\nAD 84,84,82\nMvav 52,136,212\nD1 1,16,46,3072\ns_add_u32 m0, s89, 0\nMvav 52,132,204\nRa 188,26,44032\nMvav 52,128,196\nAD 84,84,83\nCV 67,82,83\nMvav 52,144,228\n"
"Ra 128,2,49152\nMvav 52,148,236\ns_add_u32 s46, s86, s88\nEX 80,44\nMvav 52,152,244\nRa 132,2,50176\nv_xor_b32 v24, 0x10000, v24\nMvav 52,156,252\nWL 6\nEX 81,45\nMvav 48,156,248\n"
"Ra 136,2,51200\nAD 85,85,80\nMvav 48,152,240\nD1 1,16,46,0\nMvav 48,148,232\nRa 140,2,52224\nSv 112,1,16,49,1024\nMvav 48,144,224\nAD 85,85,81\nCV 70,80,81\nWV 9\n"
"v_xor_b32 v109, s15, v102\nv_xor_b32 v110, s15, v106\nv_or_b32 v109, v109, v110\nv_cmp_ne_u32 vcc, 0, v109\ns_nop 3\ns_cmp_lg_u64 vcc, 0\ns_cselect_b32 s67, 1, 0\n"
"s_cselect_b32 s6, 0x40000000, s66\nCvav 56,160,192,12\nRa 144,2,53248\ns_add_u32 s5, s6, 0\nMvav 56,164,200\ns_add_u32 s49, s49, 131072\nEX 82,46\nMvav 56,168,208\nRa 148,2,54272\n"
"v_xor_b32 v26, 0x10000, v26\nbuffer_load_dwordx4 a[192:195], v1, s[16:19], s5 offen offset:0 sc1\nMvav 56,172,216\nWL 6\nEX 83,47\nCvav 60,172,220,16\nRa 152,2,55296\nAD 85,85,82\n"
"Mvav 60,168,212\nD1 1,16,46,1024\nMvav 60,164,204\nRa 156,2,56320\nMvav 60,160,196\nAD 85,85,83\nCV 71,82,83\nMvav 60,176,228\nRa 160,2,57344\nv_max_f32 v90, v84, v85\n"
"Mvav 60,180,236\ns_add_u32 s88, s88, 32768\nEX 80,48\nMvav 60,184,244\nRa 164,2,58368\nv_cmp_lt_f32 vcc, s63, v90\nMvav 60,188,252\nWL 8\nEX 81,49\nMvav 56,188,248\n"
"Ra 168,2,59392\ns_cbranch_vccnz .T53\n.T54:\nbuffer_load_dwordx4 a[196:199], v1, s[16:19], s5 offen offset:1024 sc1\nMvav 56,184,240\nD1 1,16,46,2048\nMvav 56,180,232\n"
"Ra 172,2,60416\nAD 86,86,84\nMvav 56,176,224\nAD 84,80,81\nCV 72,80,81\nMaav 0,128,64\nRa 176,2,61440\nMaav 4,128,68\nEX 82,50\nMaav 12,132,68\nRa 180,2,62464\nMaav 8,132,64\n"
"WL 8\nEX 83,51\nMaav 16,136,64\nRa 184,2,63488\nAD 84,84,82\nbuffer_load_dwordx4 a[200:203], v1, s[16:19], s5 offen offset:2048 sc1\nMaav 20,136,68\nD1 1,16,46,3072\n"
"s_add_u32 m0, s89, 16384\nMaav 28,140,68\nRa 188,2,64512\ns_barrier\nMaav 24,140,64\nAD 84,84,83\nCV 73,82,83\nMaav 32,144,64\nRa 128,24,0\nMaav 36,144,68\n"
"s_add_u32 s46, s87, s88\nEX 80,52\nMaav 44,148,68\nRa 132,24,1024\nv_xor_b32 v2, 0x10000, v2\nMaav 40,148,64\nWL 8\nEX 81,53\nMaav 48,152,64\nRa 136,24,2048\nAD 87,87,85\n"
"buffer_load_dwordx4 a[204:207], v1, s[16:19], s5 offen offset:3072 sc1\nMaav 52,152,68\nD1 1,16,46,0\nMaav 60,156,68\nRa 140,24,3072\nMaav 56,156,64\nAD 85,80,81\nCV 76,80,81\n"
"Maav 64,160,64\nRa 144,26,0\nMaav 68,160,68\ns_cmp_eq_u32 s99, s92\nEX 82,54\nMaav 76,164,68\nRa 148,26,1024\ns_cbranch_scc1 .E49\n.E50:\ns_add_u32 s5, s6, 4096\nMaav 72,164,64\n"
"WL 8\nEX 83,55\nMaav 80,168,64\nRa 152,26,2048\nAD 85,85,82\nbuffer_load_dwordx4 a[208:211], v1, s[16:19], s5 offen offset:0 sc1\nMaav 84,168,68\nD1 1,16,46,1024\nMaav 92,172,68\n"
"Ra 156,26,3072\nMaav 88,172,64\nAD 85,85,83\nCV 77,82,83\nMaav 96,176,64\nRa 160,24,8192\nMaav 100,176,68\nEX 80,56\nMaav 108,180,68\nRa 164,24,9216\nMaav 104,180,64\nWL 6\n"
"EX 81,57\nMaav 112,184,64\nRa 168,24,10240\nAD 84,84,80\nbuffer_load_dwordx4 a[212:215], v1, s[16:19], s5 offen offset:1024 sc1\nMaav 116,184,68\nD1 1,16,46,2048\nMaav 124,188,68\n"
"Ra 172,24,11264\nMaav 120,188,64\nAD 84,84,81\nCV 74,80,81\ns_sub_u32 s99, s99, 1\nCvav 32,128,192,4\nRa 176,26,8192\nMvav 32,132,200\nEX 82,58\nMvav 32,136,208\nRa 180,26,9216\n"
"Mvav 32,140,216\nWL 6\nEX 83,59\nCvav 36,140,220,8\nRa 184,26,10240\nAD 84,84,82\nbuffer_load_dwordx4 a[216:219], v1, s[16:19], s5 offen offset:2048 sc1\nMvav 36,136,212\n"
"D1 1,16,46,3072\ns_add_u32 m0, s89, 32768\nMvav 36,132,204\nRa 188,26,11264\nMvav 36,128,196\nAD 84,84,83\nCV 75,82,83\nMvav 36,144,228\nRa 128,2,16384\nMvav 36,148,236\n"
"s_add_u32 s46, s86, s88\nEX 80,60\nMvav 36,152,244\nRa 132,2,17408\nMvav 36,156,252\nWL 6\nEX 81,61\nMvav 32,156,248\nRa 136,2,18432\nAD 85,85,80\n"
"buffer_load_dwordx4 a[220:223], v1, s[16:19], s5 offen offset:3072 sc1\nMvav 32,152,240\nD1 1,16,46,0\nMvav 32,148,232\nRa 140,2,19456\nSv 112,1,16,49,0\nMvav 32,144,224\n"
"AD 85,85,81\nCV 78,80,81\nCvav 40,160,192,12\nRa 144,2,20480\nMvav 40,164,200\ns_mov_b32 s87, s95\nEX 82,62\nMvav 40,168,208\nRa 148,2,21504\ns_add_u32 s5, s6, 16384\n"
"Mvav 40,172,216\nWL 6\nEX 83,63\nCvav 44,172,220,16\nRa 152,2,22528\nAD 85,85,82\nbuffer_load_dwordx4 a[224:227], v1, s[16:19], s5 offen offset:0 sc1\nMvav 44,168,212\n"
"D1 1,16,46,1024\nMvav 44,164,204\nRa 156,2,23552\nMvav 44,160,196\nAD 85,85,83\nCV 79,82,83\nMvav 44,176,228\nRa 160,2,24576\nv_max_f32 v90, v84, v85\nMvav 44,180,236\n"
"s_add_u32 s88, s88, 32768\nEX 80,32\nMvav 44,184,244\nRa 164,2,25600\nv_cmp_lt_f32 vcc, s63, v90\nMvav 44,188,252\nWL 8\nEX 81,33\nMvav 40,188,248\nRa 168,2,26624\n"
"s_cbranch_vccnz .T57\n.T58:\nbuffer_load_dwordx4 a[228:231], v1, s[16:19], s5 offen offset:1024 sc1\nMvav 40,184,240\nD1 1,16,46,2048\nMvav 40,180,232\nRa 172,2,27648\n"
"AD 86,86,84\nMvav 40,176,224\nAD 84,80,81\nCV 64,80,81\nMaav 0,128,72\nRa 176,2,28672\nMaav 4,128,76\nEX 82,34\nMaav 12,132,76\nRa 180,2,29696\nMaav 8,132,72\n"
"s_waitcnt vmcnt(23) lgkmcnt(8)\nEX 83,35\nMaav 16,136,72\nRa 184,2,30720\nAD 84,84,82\nbuffer_load_dwordx4 a[232:235], v1, s[16:19], s5 offen offset:2048 sc1\nMaav 20,136,76\n"
"D1 1,16,46,3072\ns_add_u32 m0, s89, 49152\nMaav 28,140,76\nRa 188,2,31744\ns_barrier\nMaav 24,140,72\nAD 84,84,83\nCV 65,82,83\nMaav 32,144,72\nRa 128,24,32768\nMaav 36,144,76\n"
"s_add_u32 s46, s87, s88\nEX 80,36\nMaav 44,148,76\nRa 132,24,33792\nMaav 40,148,72\nWL 8\nEX 81,37\nMaav 48,152,72\nRa 136,24,34816\nAD 87,87,85\n"
"buffer_load_dwordx4 a[236:239], v1, s[16:19], s5 offen offset:3072 sc1\nMaav 52,152,76\nD1 1,16,46,0\nMaav 60,156,76\nRa 140,24,35840\nMaav 56,156,72\nAD 85,80,81\nCV 68,80,81\n"
"Maav 64,160,72\nRa 144,26,32768\nMaav 68,160,76\ns_cmp_eq_u32 s99, s93\nEX 82,38\nMaav 76,164,76\nRa 148,26,33792\ns_cbranch_scc1 .E55\n.E56:\ns_add_u32 s5, s6, 20480\n"
"Maav 72,164,72\nWL 8\nEX 83,39\nMaav 80,168,72\nRa 152,26,34816\nAD 85,85,82\nbuffer_load_dwordx4 a[240:243], v1, s[16:19], s5 offen offset:0 sc1\nMaav 84,168,76\nD1 1,16,46,1024\n"
"Maav 92,172,76\nRa 156,26,35840\nMaav 88,172,72\nAD 85,85,83\nCV 69,82,83\nMaav 96,176,72\nRa 160,24,40960\nMaav 100,176,76\ns_xor_b32 s89, s89, 0x10000\nEX 80,40\n"
"Maav 108,180,76\nRa 164,24,41984\nMaav 104,180,72\nWL 6\nEX 81,41\nMaav 112,184,72\nRa 168,24,43008\nAD 84,84,80\n"
"buffer_load_dwordx4 a[244:247], v1, s[16:19], s5 offen offset:1024 sc1\nMaav 116,184,76\nD1 1,16,46,2048\nMaav 124,188,76\nRa 172,24,44032\nMaav 120,188,72\nAD 84,84,81\n"
"CV 66,80,81\nCvav 48,128,192,4\nRa 176,26,40960\nMvav 48,132,200\nEX 82,42\nMvav 48,136,208\nRa 180,26,41984\nMvav 48,140,216\nWL 6\nEX 83,43\nCvav 52,140,220,8\nRa 184,26,43008\n"
"AD 84,84,82\nbuffer_load_dwordx4 a[248:251], v1, s[16:19], s5 offen offset:2048 sc1\nMvav 52,136,212\nD1 1,16,46,3072\ns_add_u32 m0, s89, 0\nMvav 52,132,204\nRa 188,26,44032\n"
"Mvav 52,128,196\nAD 84,84,83\nCV 67,82,83\nMvav 52,144,228\nRa 128,2,49152\nMvav 52,148,236\ns_add_u32 s46, s86, s88\nEX 80,44\nMvav 52,152,244\nRa 132,2,50176\n"
"v_xor_b32 v24, 0x10000, v24\nMvav 52,156,252\nWL 6\nEX 81,45\nMvav 48,156,248\nRa 136,2,51200\nAD 85,85,80\nbuffer_load_dwordx4 a[252:255], v1, s[16:19], s5 offen offset:3072 sc1\n"
"Mvav 48,152,240\nD1 1,16,46,0\nMvav 48,148,232\nRa 140,2,52224\nSv 112,1,16,49,1024\nMvav 48,144,224\nAD 85,85,81\nCV 70,80,81\nCvav 56,160,192,12\nRa 144,2,53248\n"
"Mvav 56,164,200\ns_add_u32 s49, s49, 131072\nEX 82,46\nMvav 56,168,208\nRa 148,2,54272\nv_xor_b32 v26, 0x10000, v26\nMvav 56,172,216\nWL 6\nEX 83,47\nCvav 60,172,220,16\n"
"Ra 152,2,55296\nAD 85,85,82\nMvav 60,168,212\nD1 1,16,46,1024\nMvav 60,164,204\nRa 156,2,56320\nMvav 60,160,196\nAD 85,85,83\nCV 71,82,83\nMvav 60,176,228\nRa 160,2,57344\n"
"v_max_f32 v90, v84, v85\nMvav 60,180,236\ns_add_u32 s88, s88, 32768\nEX 80,48\nMvav 60,184,244\nRa 164,2,58368\nv_cmp_lt_f32 vcc, s63, v90\nMvav 60,188,252\nWL 8\nEX 81,49\n"
"Mvav 56,188,248\nRa 168,2,59392\ns_cbranch_vccnz .T59\n.T60:\nMvav 56,184,240\nD1 1,16,46,2048\nMvav 56,180,232\nRa 172,2,60416\nAD 86,86,84\nMvav 56,176,224\nAD 84,80,81\n"
"CV 72,80,81\nMaav 0,128,64\nRa 176,2,61440\nMaav 4,128,68\nEX 82,50\nMaav 12,132,68\nRa 180,2,62464\nMaav 8,132,64\ns_waitcnt vmcnt(22) lgkmcnt(8)\nEX 83,51\nMaav 16,136,64\n"
"Ra 184,2,63488\nAD 84,84,82\nMaav 20,136,68\nD1 1,16,46,3072\ns_add_u32 m0, s89, 16384\nMaav 28,140,68\nRa 188,2,64512\ns_barrier\nMaav 24,140,64\nAD 84,84,83\nCV 73,82,83\n"
"Maav 32,144,64\nRa 128,24,0\nMaav 36,144,68\ns_add_u32 s46, s87, s88\nEX 80,52\nMaav 44,148,68\nRa 132,24,1024\nv_xor_b32 v2, 0x10000, v2\nMaav 40,148,64\nWL 8\nEX 81,53\n"
"Maav 48,152,64\nRa 136,24,2048\nAD 87,87,85\nMaav 52,152,68\nD1 1,16,46,0\nMaav 60,156,68\nRa 140,24,3072\nMaav 56,156,64\nAD 85,80,81\nCV 76,80,81\ns_cmp_lg_u32 s67, 0\n"
"s_cbranch_scc1 .E62\n.E63:\nMaav 64,160,64\nRa 144,26,0\nMaav 68,160,68\nEX 82,54\nMaav 76,164,68\nRa 148,26,1024\nMaav 72,164,64\nWL 8\nEX 83,55\nMaav 80,168,64\nRa 152,26,2048\n"
"AD 85,85,82\nMaav 84,168,68\nD1 1,16,46,1024\nMaav 92,172,68\nRa 156,26,3072\nMaav 88,172,64\nAD 85,85,83\nCV 77,82,83\nMaav 96,176,64\nRa 160,24,8192\nMaav 100,176,68\nEX 80,56\n"
"Maav 108,180,68\nRa 164,24,9216\nMaav 104,180,64\nWL 10\nEX 81,57\nMaav 112,184,64\nRa 168,24,10240\nAD 84,84,80\nMaav 116,184,68\nD1 1,16,46,2048\nMaav 124,188,68\n"
"Ra 172,24,11264\nMaav 120,188,64\nAD 84,84,81\nCV 74,80,81\ns_cmp_lg_u32 s67, 0\ns_cbranch_scc1 .D64\n.D65:\nWV 8\nv_cmp_lt_f32 vcc, 0, v101\nSU 20,100,88\n"
"v_mul_f32 v101, 0x3e800000, v101\nv_cndmask_b32 v20, 0, v20, vcc\nv_cmp_lt_f32 vcc, 0, v105\nSU 21,104,89\nv_mul_f32 v105, 0x3e800000, v105\nv_cndmask_b32 v21, 0, v21, vcc\n"
"v_or_b32 v91, v20, v21\nv_cmp_ne_u32 vcc, 0, v91\ns_nop 0\ns_cbranch_vccnz .D66\nAD 86,86,101\nAD 87,87,105\n.D67:\nMava 0,130,192\nMava 8,128,192\nEX 82,58\nMava 12,128,196\n"
"Mava 4,130,196\nEX 83,59\nMava 16,130,200\nAD 84,84,82\nMava 24,128,200\nD1 1,16,46,3072\nMava 28,128,204\ns_add_u32 m0, s89, 32768\ns_add_u32 s46, s86, s88\nMava 20,130,204\n"
"AD 84,84,83\nCV 75,82,83\nMava 32,130,208\nRa 128,2,16384\nv_xor_b32 v3, 0x10000, v2\nMava 40,128,208\nEX 80,60\nMava 44,128,212\nRa 132,2,17408\nv_add_u32 v0, 0x20000, v1\n"
"Mava 36,130,212\nEX 81,61\nMava 48,130,216\nRa 136,2,18432\nAD 85,85,80\nMava 56,128,216\nD1 1,16,46,0\ns_sub_u32 s94, s49, 1966080\nMava 60,128,220\nRa 140,2,19456\n"
"s_add_u32 s95, s94, 1048576\nMava 52,130,220\nAD 85,85,81\nCV 78,80,81\nMava 64,130,224\nRa 144,2,20480\ns_xor_b32 s92, s94, 0x202000\nMava 72,128,224\nEX 82,62\nMava 76,128,228\n"
"Ra 148,2,21504\ns_add_u32 s93, s96, 983040\nMava 68,130,228\nEX 83,63\nMava 80,130,232\nRa 152,2,22528\nAD 85,85,82\nMava 88,128,232\nD1 1,16,46,1024\n"
"s_xor_b32 s93, s93, 0x202000\nMava 92,128,236\nRa 156,2,23552\ns_lshl_b32 s89, s11, 12\nMava 84,130,236\nAD 85,85,83\nCV 79,82,83\nMava 96,130,240\nRa 160,2,24576\n"
"v_max_f32 v90, v84, v85\nMava 104,128,240\nv_cmp_lt_f32 vcc, s63, v90\nMava 108,128,244\nRa 164,2,25600\nMava 100,130,244\nWL 8\nMava 112,130,248\nRa 168,2,26624\n"
"Mava 120,128,248\nD1 1,16,46,2048\nMava 124,128,252\nRa 172,2,27648\nMava 116,130,252\ns_cbranch_vccnz .T71\n.T72:\nAD 86,86,84\nAD 87,87,85\nMaav 0,128,72\nRa 176,2,28672\n"
"v_mov_b32 v84, v86\nv_mov_b32 v85, v87\nMaav 4,128,76\nv_permlane32_swap_b32 v86, v84\nv_permlane32_swap_b32 v87, v85\nMaav 12,132,76\nRa 180,2,29696\nAD 86,86,84\nMaav 8,132,72\n"
"WL 8\nAD 87,87,85\nMaav 16,136,72\nRa 184,2,30720\nv_mov_b32 v84, v86\nv_mov_b32 v85, v87\nMaav 20,136,76\nD1 1,16,46,3072\nv_permlane16_swap_b32 v86, v84\nMaav 28,140,76\n"
"Ra 188,2,31744\nv_permlane16_swap_b32 v87, v85\nMaav 24,140,72\nAD 86,86,84\nAD 87,87,85\nMaav 32,144,72\nRa 128,2,0\nv_rcp_f32 v86, v86\nMaav 36,144,76\nv_rcp_f32 v87, v87\n"
"Maav 44,148,76\nRa 132,2,1024\nAR 82,0\nAR 83,1\nMU 82,82,86\nMU 83,83,86\nMaav 40,148,72\nWL 8\ns_add_u32 m0, s89, 131072\ns_add_u32 s46, s93, 0\nCV 96,82,83\nAR 82,2\n"
"Maav 48,152,72\nRa 136,2,2048\nAR 83,3\nMU 82,82,86\nMU 83,83,86\nCV 97,82,83\nMaav 52,152,76\nD1 1,16,46,0\nAR 82,4\nAR 83,5\nMU 82,82,87\nMU 83,83,87\nMaav 60,156,76\n"
"Ra 140,2,3072\nCV 100,82,83\nAR 82,6\nAR 83,7\nMU 82,82,87\nMaav 56,156,72\nMU 83,83,87\nCV 101,82,83\nAR 82,8\nAR 83,9\nMU 82,82,86\nMaav 64,160,72\nRa 144,2,4096\nMU 83,83,86\n"
"CV 98,82,83\nAR 82,10\nAR 83,11\nMaav 68,160,76\nMU 82,82,86\nMU 83,83,86\nCV 99,82,83\ns_add_u32 s5, s94, 0\nSv 96,1,16,5,0\nMaav 76,164,76\nRa 148,2,5120\nAR 82,12\nAR 83,13\n"
"MU 82,82,87\nMU 83,83,87\nMaav 72,164,72\nWL 8\nCV 102,82,83\nAR 82,14\nAR 83,15\nMU 82,82,87\nMaav 80,168,72\nRa 152,2,6144\nMU 83,83,87\nCV 103,82,83\nSv 100,1,16,5,1024\n"
"AR 82,16\nMaav 84,168,76\nD1 1,16,46,1024\nAR 83,17\nMU 82,82,86\nMU 83,83,86\nCV 104,82,83\nMaav 92,172,76\nRa 156,2,7168\nAR 82,18\nAR 83,19\nMU 82,82,86\nMU 83,83,86\n"
"Maav 88,172,72\nCV 105,82,83\nAR 82,20\nAR 83,21\nMU 82,82,87\nMU 83,83,87\nMaav 96,176,72\nRa 160,2,8192\nCV 108,82,83\nAR 82,22\nAR 83,23\nMU 82,82,87\nMaav 100,176,76\n"
"MU 83,83,87\nCV 109,82,83\nAR 82,24\nAR 83,25\nMU 82,82,86\nMaav 108,180,76\nRa 164,2,9216\nMU 83,83,86\nCV 106,82,83\nAR 82,26\nAR 83,27\nMaav 104,180,72\nWL 8\nMU 82,82,86\n"
"MU 83,83,86\nCV 107,82,83\ns_add_u32 s5, s94, 131072\nMaav 112,184,72\nRa 168,2,10240\nSv 104,1,16,5,0\nAR 82,28\nAR 83,29\nMU 82,82,87\nMaav 116,184,76\nD1 1,16,46,2048\n"
"MU 83,83,87\nCV 110,82,83\nAR 82,30\nAR 83,31\nMaav 124,188,76\nRa 172,2,11264\nMU 82,82,87\nMU 83,83,87\nCV 111,82,83\nSv 108,1,16,5,1024\nMaav 120,188,72\nAR 82,32\nAR 83,33\n"
"MU 82,82,86\nZvav 136,128,96\nRa 176,2,12288\nMU 83,83,86\nCV 112,82,83\nMvav 136,132,104\nAR 82,34\nAR 83,35\nZvav 140,132,108\nRa 180,2,13312\nMU 82,82,86\nMU 83,83,86\n"
"Mvav 140,128,100\ns_waitcnt vmcnt(18) lgkmcnt(8)\nCV 113,82,83\nAR 82,36\nZvav 148,136,100\nRa 184,2,14336\nAR 83,37\nMU 82,82,87\nMvav 148,140,108\nD1 1,16,46,3072\nMU 83,83,87\n"
"CV 116,82,83\nZvav 144,140,104\nRa 188,2,15360\ns_barrier\nAR 82,38\nMvav 144,136,96\nAR 83,39\nMU 82,82,87\nMU 83,83,87\nZvav 152,144,96\nRa 128,2,32768\nCV 117,82,83\nAR 82,40\n"
"v_lshrrev_b32 v88, 8, v1\nMvav 152,148,104\nAR 83,41\nMU 82,82,86\nMU 83,83,86\nbuffer_store_dword v25, off, s[16:19], s54 offset:512 sc0 sc1\nZvav 156,148,108\nRa 132,2,33792\n"
"CV 114,82,83\nAR 82,42\nMvav 156,144,100\nWL 8\ns_add_u32 m0, s89, 114688\ns_add_u32 s46, s96, 1179648\nZvav 164,152,100\nRa 136,2,34816\nAR 83,43\nMU 82,82,86\n"
"v_lshlrev_b32 v89, 5, v88\nMvav 164,156,108\nD1 1,16,46,0\nMU 83,83,86\nCV 115,82,83\nZvav 160,156,104\nRa 140,2,35840\ns_add_u32 s5, s94, 262144\nSv 112,1,16,5,0\n"
"Mvav 160,152,96\nAR 82,44\nAR 83,45\nMU 82,82,87\nZvav 168,160,96\nRa 144,2,36864\nMU 83,83,87\nCV 118,82,83\n"
"buffer_load_dwordx4 v[64:67], v89, s[16:19], s40 offen offset:0 sc0 sc1\nMvav 168,164,104\nAR 82,46\nAR 83,47\nZvav 172,164,108\nRa 148,2,37888\nMU 82,82,87\nMU 83,83,87\n"
"Mvav 172,160,100\nWL 8\nCV 119,82,83\nSv 116,1,16,5,1024\nZvav 180,168,100\nRa 152,2,38912\nAR 82,48\nAR 83,49\n"
"buffer_load_dwordx4 v[68:71], v89, s[16:19], s40 offen offset:16 sc0 sc1\nMvav 180,172,108\nD1 1,16,46,1024\nMU 82,82,86\nMU 83,83,86\nZvav 176,172,104\nRa 156,2,39936\n"
"CV 120,82,83\nAR 82,50\nMvav 176,168,96\nAR 83,51\nMU 82,82,86\nMU 83,83,86\nZvav 184,176,96\nRa 160,2,40960\nCV 121,82,83\nAR 82,52\n"
"buffer_load_dwordx4 v[72:75], v89, s[16:19], s40 offen offset:1792 sc0 sc1\nMvav 184,180,104\nAR 83,53\nMU 82,82,87\nMU 83,83,87\nZvav 188,180,108\nRa 164,2,41984\nCV 124,82,83\n"
"AR 82,54\nMvav 188,176,100\nWL 8\nAR 83,55\nMU 82,82,87\nZvav 196,184,100\nRa 168,2,43008\nMU 83,83,87\nCV 125,82,83\n"
"buffer_load_dwordx4 v[76:79], v89, s[16:19], s40 offen offset:1808 sc0 sc1\nMvav 196,188,108\nD1 1,16,46,2048\nAR 82,56\nAR 83,57\nZvav 192,188,104\nRa 172,2,44032\nMU 82,82,86\n"
"MU 83,83,86\nMvav 192,184,96\nCV 122,82,83\nAR 82,58\nAR 83,59\nZvav 200,128,96\nRa 176,2,45056\nMU 82,82,86\nMU 83,83,86\nv_bfe_u32 v90, v1, 4, 4\nMvav 200,132,104\nCV 123,82,83\n"
"s_add_u32 s5, s94, 393216\nv_mul_u32_u24 v91, 896, v90\nZvav 204,132,108\nRa 180,2,46080\nSv 120,1,16,5,0\nAR 82,60\nMvav 204,128,100\nWL 8\nAR 83,61\nMU 82,82,87\n"
"Zvav 212,136,100\nRa 184,2,47104\nMU 83,83,87\nCV 126,82,83\nv_lshl_add_u32 v91, v88, 4, v91\nMvav 212,140,108\nD1 1,16,46,3072\nAR 82,62\nAR 83,63\nZvav 208,140,104\n"
"Ra 188,2,48128\nMU 82,82,87\nMU 83,83,87\nMvav 208,136,96\nCV 127,82,83\nSv 124,1,16,5,1024\nbuffer_load_dwordx4 v[12:15], v91, s[28:31], s22 offen offset:0\nZvav 216,144,96\n"
"Ra 128,2,49152\nAR 82,64\nAR 83,65\nMvav 216,148,104\nMU 82,82,86\nMU 83,83,86\nCV 32,82,83\nZvav 220,148,108\nRa 132,2,50176\nAR 82,66\nAR 83,67\nMvav 220,144,100\nWL 8\n"
"s_add_u32 m0, s89, 147456\ns_add_u32 s46, s93, 32768\nZvav 228,152,100\nRa 136,2,51200\nMU 82,82,86\nMU 83,83,86\nbuffer_load_dwordx4 v[16:19], v91, s[28:31], s23 offen offset:0\n"
"Mvav 228,156,108\nD1 1,16,46,0\nCV 33,82,83\nAR 82,68\nZvav 224,156,104\nRa 140,2,52224\nAR 83,69\nMU 82,82,87\nMvav 224,152,96\nMU 83,83,87\nCV 36,82,83\nAR 82,70\n"
"Zvav 232,160,96\nRa 144,2,53248\nAR 83,71\nMU 82,82,87\nv_add_u32 v90, s21, v90\nMvav 232,164,104\nMU 83,83,87\nCV 37,82,83\nv_lshlrev_b32 v88, 2, v90\nZvav 236,164,108\n"
"Ra 148,2,54272\nAR 82,72\nAR 83,73\nMvav 236,160,100\nWL 8\nMU 82,82,86\nMU 83,83,86\nZvav 244,168,100\nRa 152,2,55296\nCV 34,82,83\nAR 82,74\nMvav 244,172,108\nD1 1,16,46,1024\n"
"AR 83,75\nMU 82,82,86\nZvav 240,172,104\nRa 156,2,56320\nMU 83,83,86\nCV 35,82,83\nMvav 240,168,96\ns_add_u32 s5, s94, 524288\nSv 32,1,16,5,0\nAR 82,76\nMvav 136,176,112\n"
"Ra 160,2,57344\nAR 83,77\nMU 82,82,87\nMvav 136,180,120\nMU 83,83,87\nCV 38,82,83\nAR 82,78\nMvav 140,180,124\nRa 164,2,58368\nAR 83,79\nMU 82,82,87\nMvav 140,176,116\nWL 8\n"
"MU 83,83,87\nCV 39,82,83\nMvav 148,184,116\nRa 168,2,59392\nSv 36,1,16,5,1024\nAR 82,80\nMvav 148,188,124\nD1 1,16,46,2048\nAR 83,81\nMU 82,82,86\nMvav 144,188,120\n"
"Ra 172,2,60416\nMU 83,83,86\nCV 40,82,83\nMvav 144,184,112\nAR 82,82\nAR 83,83\nMU 82,82,86\nMvav 152,128,112\nRa 176,2,61440\nMU 83,83,86\nCV 41,82,83\nMvav 152,132,120\n"
"AR 82,84\nAR 83,85\nMvav 156,132,124\nRa 180,2,62464\nMU 82,82,87\nMU 83,83,87\nMvav 156,128,116\ns_waitcnt vmcnt(32) lgkmcnt(8)\nCV 44,82,83\nAR 82,86\nMvav 164,136,116\n"
"Ra 184,2,63488\nAR 83,87\nMU 82,82,87\nMvav 164,140,124\nD1 1,16,46,3072\nMU 83,83,87\nCV 45,82,83\nMvav 160,140,120\nRa 188,2,64512\ns_barrier\nAR 82,88\nMvav 160,136,112\n"
"AR 83,89\nMU 82,82,86\nMU 83,83,86\nMvav 168,144,112\nRa 128,3,0\nCV 42,82,83\nAR 82,90\nMvav 168,148,120\nAR 83,91\nMU 82,82,86\nMU 83,83,86\nMvav 172,148,124\nRa 132,3,1024\n"
"CV 43,82,83\ns_add_u32 s5, s94, 655360\nMvav 172,144,116\nWL 8\nSv 40,1,16,5,0\nAR 82,92\nMvav 180,152,116\nRa 136,3,2048\nAR 83,93\nMU 82,82,87\nMvav 180,156,124\nMU 83,83,87\n"
"CV 46,82,83\nAR 82,94\nMvav 176,156,120\nRa 140,3,3072\nAR 83,95\nMU 82,82,87\nMvav 176,152,112\nMU 83,83,87\nCV 47,82,83\nSv 44,1,16,5,1024\nMvav 184,160,112\nRa 144,3,4096\n"
"AR 82,96\nAR 83,97\nMvav 184,164,120\nMU 82,82,86\nMU 83,83,86\nbuffer_load_dword v27, off, s[16:19], s60 offset:512 sc0 sc1\nMvav 188,164,124\nRa 148,3,5120\nCV 48,82,83\n"
"AR 82,98\nMvav 188,160,116\nWL 8\nAR 83,99\nMU 82,82,86\nMvav 196,168,116\nRa 152,3,6144\nMU 83,83,86\nCV 49,82,83\nMvav 196,172,124\nAR 82,100\nAR 83,101\nMU 82,82,87\n"
"Mvav 192,172,120\nRa 156,3,7168\nMU 83,83,87\nMvav 192,168,112\nCV 52,82,83\nAR 82,102\nAR 83,103\nMvav 200,176,112\nRa 160,3,8192\nMU 82,82,87\nMU 83,83,87\nMvav 200,180,120\n"
"CV 53,82,83\nAR 82,104\nAR 83,105\nMvav 204,180,124\nRa 164,3,9216\nMU 82,82,86\nMU 83,83,86\nMvav 204,176,116\nWL 8\nCV 50,82,83\nMvav 212,184,116\nRa 168,3,10240\nAR 82,106\n"
"AR 83,107\nMvav 212,188,124\nMU 82,82,86\nMU 83,83,86\nCV 51,82,83\nMvav 208,188,120\nRa 172,3,11264\ns_add_u32 s5, s94, 786432\nSv 48,1,16,5,0\nMvav 208,184,112\nAR 82,108\n"
"AR 83,109\nMU 82,82,87\nMvav 216,128,112\nRa 176,3,12288\nMU 83,83,87\nCV 54,82,83\nMvav 216,132,120\nAR 82,110\nAR 83,111\nMvav 220,132,124\nRa 180,3,13312\nMU 82,82,87\n"
"MU 83,83,87\nMvav 220,128,116\nWL 8\nCV 55,82,83\nSv 52,1,16,5,1024\nMvav 228,136,116\nRa 184,3,14336\nAR 82,112\nAR 83,113\nMvav 228,140,124\nMU 82,82,86\nMU 83,83,86\n"
"CV 56,82,83\nMvav 224,140,120\nRa 188,3,15360\nAR 82,114\nMvav 224,136,112\nAR 83,115\nMU 82,82,86\nMU 83,83,86\nMvav 232,144,112\nRa 128,3,16384\nCV 57,82,83\nAR 82,116\n"
"Mvav 232,148,120\nAR 83,117\nMU 82,82,87\nMU 83,83,87\nMvav 236,148,124\nRa 132,3,17408\nCV 60,82,83\nAR 82,118\nMvav 236,144,116\nWL 8\nAR 83,119\nMvav 244,152,116\n"
"Ra 136,3,18432\nMU 82,82,87\nMU 83,83,87\nMvav 244,156,124\nCV 61,82,83\nAR 82,120\nAR 83,121\nMvav 240,156,120\nRa 140,3,19456\nMU 82,82,86\nMU 83,83,86\nMvav 240,152,112\n"
"CV 58,82,83\nAR 82,122\nAR 83,123\nMvav 136,160,32\nRa 144,3,20480\nMU 82,82,86\nMU 83,83,86\nMvav 136,164,40\nCV 59,82,83\ns_add_u32 s5, s94, 917504\nMvav 140,164,44\n"
"Ra 148,3,21504\nSv 56,1,16,5,0\nAR 82,124\nMvav 140,160,36\nWL 8\nAR 83,125\nMU 82,82,87\nMvav 148,168,36\nRa 152,3,22528\nMU 83,83,87\nCV 62,82,83\nMvav 148,172,44\nAR 82,126\n"
"AR 83,127\nMU 82,82,87\nMvav 144,172,40\nRa 156,3,23552\nMU 83,83,87\nMvav 144,168,32\nCV 63,82,83\nSv 60,1,16,5,1024\nMvav 152,176,32\nRa 160,3,24576\nWV 19\nMvav 152,180,40\n"
"v_xor_b32 v92, s15, v66\nMvav 156,180,44\nRa 164,3,25600\nMvav 156,176,36\nWL 8\nMvav 164,184,36\nRa 168,3,26624\nMvav 164,188,44\nv_xor_b32 v93, s15, v70\nMvav 160,188,40\n"
"Ra 172,3,27648\nMvav 160,184,32\nv_xor_b32 v94, s15, v74\nMvav 168,128,32\nRa 176,3,28672\nv_or3_b32 v92, v92, v93, v94\nMvav 168,132,40\nv_xor_b32 v93, s15, v78\nMvav 172,132,44\n"
"Ra 180,3,29696\nMvav 172,128,36\ns_waitcnt vmcnt(16) lgkmcnt(8)\nMvav 180,136,36\nRa 184,3,30720\nMvav 180,140,44\nv_or_b32 v92, v92, v93\nv_cmp_ne_u32 vcc, 0, v92\ns_nop 0\n"
"s_cbranch_vccnz .S80\n.S81:\nMvav 176,140,40\nRa 188,3,31744\ns_barrier\nMvav 176,136,32\ns_mov_b32 s2, 0xc040000\nMvav 184,144,32\nRa 128,3,32768\n"
"buffer_load_dword v80, v88, s[16:19], s2 offen sc0 sc1\nMvav 184,148,40\nMvav 188,148,44\nRa 132,3,33792\nMvav 188,144,36\nWL 8\nMvav 196,152,36\nRa 136,3,34816\nMvav 196,156,44\n"
"buffer_load_dword v81, v88, s[16:19], s2 offen offset:64 sc0 sc1\nMvav 192,156,40\nRa 140,3,35840\nMvav 192,152,32\nv_and_b32 v92, 0x7ff, v67\nMvav 200,160,32\nRa 144,3,36864\n"
"v_lshrrev_b32 v93, 16, v67\nWV 6\nv_cmp_ne_u32 vcc, s15, v27\ns_nop 0\ns_cbranch_vccnz .S83\n.S84:\nMvav 200,164,40\ns_add_u32 s4, s92, 0\nMvav 204,164,44\nRa 148,3,37888\n"
"Mvav 204,160,36\nWL 8\ns_add_u32 m0, s89, 16384\ns_add_u32 s46, s93, 65536\nbuffer_load_dwordx4 a[192:195], v1, s[16:19], s4 offen offset:0 sc0 sc1\nMvav 212,168,36\n"
"Ra 152,3,38912\nMvav 212,172,44\nD1 1,16,46,0\nMvav 208,172,40\nRa 156,3,39936\nMvav 208,168,32\nbuffer_load_dwordx4 a[196:199], v1, s[16:19], s4 offen offset:1024 sc0 sc1\n"
"Mvav 216,176,32\nRa 160,3,40960\nv_add_u32 v94, 16, v90\nMvav 216,180,40\ns_add_u32 s4, s92, 131072\nv_cmp_eq_u32 vcc, v92, v90\ns_nop 1\nv_cndmask_b32 v12, v12, v64, vcc\n"
"v_cndmask_b32 v13, v13, v65, vcc\nv_cndmask_b32 v14, v14, v68, vcc\nv_cndmask_b32 v15, v15, v69, vcc\nMvav 220,180,44\nRa 164,3,41984\nv_cmp_eq_u32 vcc, v93, v90\ns_nop 1\n"
"v_cndmask_b32 v12, v12, v72, vcc\nv_cndmask_b32 v13, v13, v73, vcc\nv_cndmask_b32 v14, v14, v76, vcc\nv_cndmask_b32 v15, v15, v77, vcc\nMvav 220,176,36\nWL 8\n"
"buffer_load_dwordx4 a[200:203], v1, s[16:19], s4 offen offset:0 sc0 sc1\nMvav 228,184,36\nRa 168,3,43008\nv_cmp_eq_u32 vcc, v92, v94\ns_nop 1\nv_cndmask_b32 v16, v16, v64, vcc\n"
"v_cndmask_b32 v17, v17, v65, vcc\nv_cndmask_b32 v18, v18, v68, vcc\nv_cndmask_b32 v19, v19, v69, vcc\nMvav 228,188,44\nD1 1,16,46,1024\nv_cmp_eq_u32 vcc, v93, v94\ns_nop 1\n"
"v_cndmask_b32 v16, v16, v72, vcc\nv_cndmask_b32 v17, v17, v73, vcc\nv_cndmask_b32 v18, v18, v76, vcc\nv_cndmask_b32 v19, v19, v77, vcc\nMvav 224,188,40\nRa 172,3,44032\n"
"Mvav 224,184,32\nbuffer_load_dwordx4 a[204:207], v1, s[16:19], s4 offen offset:1024 sc0 sc1\nMvav 232,128,32\nRa 176,3,45056\nMvav 232,132,40\ns_add_u32 s4, s92, 262144\n"
"Mvav 236,132,44\nRa 180,3,46080\nMvav 236,128,36\nWL 8\nbuffer_load_dwordx4 a[208:211], v1, s[16:19], s4 offen offset:0 sc0 sc1\nMvav 244,136,36\nRa 184,3,47104\nMvav 244,140,44\n"
"D1 1,16,46,2048\nMvav 240,140,40\nRa 188,3,48128\nMvav 240,136,32\nbuffer_load_dwordx4 a[212:215], v1, s[16:19], s4 offen offset:1024 sc0 sc1\nMvav 136,144,48\nRa 128,3,49152\n"
"Mvav 136,148,56\ns_add_u32 s4, s92, 393216\nMvav 140,148,60\nRa 132,3,50176\nMvav 140,144,52\nWL 8\nbuffer_load_dwordx4 a[216:219], v1, s[16:19], s4 offen offset:0 sc0 sc1\n"
"Mvav 148,152,52\nRa 136,3,51200\nMvav 148,156,60\nD1 1,16,46,3072\nMvav 144,156,56\nRa 140,3,52224\nMvav 144,152,48\n"
"buffer_load_dwordx4 a[220:223], v1, s[16:19], s4 offen offset:1024 sc0 sc1\nMvav 152,160,48\nRa 144,3,53248\nMvav 152,164,56\nMvav 156,164,60\nRa 148,3,54272\nMvav 156,160,52\n"
"WL 8\ns_add_u32 m0, s89, 0\ns_add_u32 s46, s93, 98304\nMvav 164,168,52\nRa 152,3,55296\nMvav 164,172,60\nD1 1,16,46,0\nMvav 160,172,56\nRa 156,3,56320\nMvav 160,168,48\n"
"Mvav 168,176,48\nRa 160,3,57344\nMvav 168,180,56\nMvav 172,180,60\nRa 164,3,58368\nMvav 172,176,52\nWL 8\nMvav 180,184,52\nRa 168,3,59392\nMvav 180,188,60\nD1 1,16,46,1024\n"
"Mvav 176,188,56\nRa 172,3,60416\nMvav 176,184,48\nMvav 184,128,48\nRa 176,3,61440\nMvav 184,132,56\nMvav 188,132,60\nRa 180,3,62464\nMvav 188,128,52\nWL 8\nMvav 196,136,52\n"
"Ra 184,3,63488\nMvav 196,140,60\nD1 1,16,46,2048\nMvav 192,140,56\nRa 188,3,64512\ns_barrier\nMvav 192,136,48\nMvav 200,144,48\nRa 128,0,0\nMvav 200,148,56\nMvav 204,148,60\n"
"Ra 132,0,1024\nMvav 204,144,52\nWL 8\nMvav 212,152,52\nRa 136,0,2048\nMvav 212,156,60\nD1 1,16,46,3072\nMvav 208,156,56\nRa 140,0,3072\nMvav 208,152,48\nMvav 216,160,48\n"
"Ra 144,0,4096\nMvav 216,164,56\nMvav 220,164,60\nRa 148,0,5120\nMvav 220,160,52\nWL 8\ns_add_u32 m0, s89, 32768\ns_add_u32 s46, s93, 131072\nMvav 228,168,52\nRa 152,0,6144\n"
"Mvav 228,172,60\nD1 1,16,46,0\nMvav 224,172,56\nRa 156,0,7168\nWV 5\nAR 22,195\nMvav 224,168,48\nAR 23,199\nv_max_u32 v21, v22, v23\nMvav 232,176,48\nRa 160,0,8192\nAR 22,203\n"
"Mvav 232,180,56\nAR 23,207\nv_max3_u32 v21, v21, v22, v23\nMvav 236,180,60\nRa 164,0,9216\nAR 22,211\nMvav 236,176,52\nWL 6\nAR 23,215\nv_max3_u32 v21, v21, v22, v23\n"
"Mvav 244,184,52\nRa 168,0,10240\nAR 22,219\nMvav 244,188,60\nD1 1,16,46,1024\nAR 23,223\nMvav 240,188,56\nRa 172,0,11264\nv_max3_u32 v21, v21, v22, v23\nMvav 240,184,48\n"
"v_cmp_eq_u32 vcc, -1, v21\ns_nop 0\ns_cbranch_vccnz .S74\n.S75:\nMvaa 136,128,192\nRa 176,0,12288\nMvaa 136,132,200\nv_sub_f32 v92, 1.0, v80\nMvaa 136,136,208\nRa 180,0,13312\n"
"Mvaa 136,140,216\nWL 6\nMvaa 140,140,220\nRa 184,0,14336\nv_sub_f32 v93, 1.0, v81\nMvaa 140,136,212\nD1 1,16,46,2048\nMvaa 140,132,204\nRa 188,0,15360\nMvaa 140,128,196\n"
"CV 92,92,93\nMvaa 144,144,192\nRa 128,0,16384\nv_and_b32 v93, 0xffff0000, v92\nMvaa 144,148,200\nv_lshlrev_b32 v92, 16, v92\nMvaa 144,152,208\nRa 132,0,17408\nMvaa 144,156,216\n"
"WL 6\nMvaa 148,156,220\nRa 136,0,18432\nv_lshlrev_b32 v64, 16, v12\nMvaa 148,152,212\nD1 1,16,46,3072\nMvaa 148,148,204\nRa 140,0,19456\nMvaa 148,144,196\n"
"v_and_b32 v65, 0xffff0000, v12\nMvaa 152,160,192\nRa 144,0,20480\nMU 64,64,92\nMvaa 152,164,200\nMU 65,65,92\nMvaa 152,168,208\nRa 148,0,21504\nMvaa 152,172,216\nWL 6\n"
"s_add_u32 m0, s89, 49152\ns_add_u32 s46, s93, 163840\nMvaa 156,172,220\nRa 152,0,22528\nv_lshlrev_b32 v66, 16, v13\nMvaa 156,168,212\nD1 1,16,46,0\nMvaa 156,164,204\n"
"Ra 156,0,23552\nMvaa 156,160,196\nv_and_b32 v67, 0xffff0000, v13\nMvaa 160,176,192\nRa 160,0,24576\nMU 66,66,92\nMvaa 160,180,200\ns_add_u32 s4, s92, 524288\nMvaa 160,184,208\n"
"Ra 164,0,25600\nMvaa 160,188,216\nWL 6\nbuffer_load_dwordx4 a[224:227], v1, s[16:19], s4 offen offset:0 sc0 sc1\nMvaa 164,188,220\nRa 168,0,26624\nMU 67,67,92\nMvaa 164,184,212\n"
"D1 1,16,46,1024\nMvaa 164,180,204\nRa 172,0,27648\nMvaa 164,176,196\nbuffer_load_dwordx4 a[228:231], v1, s[16:19], s4 offen offset:1024 sc0 sc1\nMvaa 168,128,192\nRa 176,0,28672\n"
"v_lshlrev_b32 v68, 16, v14\nMvaa 168,132,200\ns_add_u32 s4, s92, 655360\nMvaa 168,136,208\nRa 180,0,29696\nMvaa 168,140,216\ns_waitcnt vmcnt(8) lgkmcnt(6)\n"
"buffer_load_dwordx4 a[232:235], v1, s[16:19], s4 offen offset:0 sc0 sc1\nMvaa 172,140,220\nRa 184,0,30720\nv_and_b32 v69, 0xffff0000, v14\nMvaa 172,136,212\nD1 1,16,46,2048\n"
"Mvaa 172,132,204\nRa 188,0,31744\ns_barrier\nMvaa 172,128,196\nbuffer_load_dwordx4 a[236:239], v1, s[16:19], s4 offen offset:1024 sc0 sc1\nMvaa 176,144,192\nRa 128,2,16384\n"
"MU 68,68,92\nMvaa 176,148,200\ns_add_u32 s4, s92, 786432\nMvaa 176,152,208\nRa 132,2,17408\nMvaa 176,156,216\nWL 6\n"
"buffer_load_dwordx4 a[240:243], v1, s[16:19], s4 offen offset:0 sc0 sc1\nMvaa 180,156,220\nRa 136,2,18432\nMU 69,69,92\nMvaa 180,152,212\nD1 1,16,46,3072\nMvaa 180,148,204\n"
"Ra 140,2,19456\nMvaa 180,144,196\nbuffer_load_dwordx4 a[244:247], v1, s[16:19], s4 offen offset:1024 sc0 sc1\nMvaa 184,160,192\nRa 144,2,20480\nv_lshlrev_b32 v70, 16, v15\n"
"Mvaa 184,164,200\ns_add_u32 s4, s92, 917504\nMvaa 184,168,208\nRa 148,2,21504\nMvaa 184,172,216\nWL 6\ns_add_u32 m0, s89, 65536\ns_add_u32 s46, s93, 196608\n"
"buffer_load_dwordx4 a[248:251], v1, s[16:19], s4 offen offset:0 sc0 sc1\nMvaa 188,172,220\nRa 152,2,22528\nv_and_b32 v71, 0xffff0000, v15\nMvaa 188,168,212\nD1 1,16,46,0\n"
"Mvaa 188,164,204\nRa 156,2,23552\nMvaa 188,160,196\nbuffer_load_dwordx4 a[252:255], v1, s[16:19], s4 offen offset:1024 sc0 sc1\nMvaa 192,176,192\nRa 160,2,24576\nMU 70,70,92\n"
"Mvaa 192,180,200\nMU 71,71,92\nMvaa 192,184,208\nRa 164,2,25600\nMvaa 192,188,216\nWL 6\nMvaa 196,188,220\nRa 168,2,26624\nv_lshlrev_b32 v72, 16, v16\nMvaa 196,184,212\n"
"D1 1,16,46,1024\nMvaa 196,180,204\nRa 172,2,27648\nMvaa 196,176,196\nv_and_b32 v73, 0xffff0000, v16\nMvaa 200,128,192\nRa 176,2,28672\nMU 72,72,93\nMvaa 200,132,200\nMU 73,73,93\n"
"Mvaa 200,136,208\nRa 180,2,29696\nMvaa 200,140,216\nWL 6\nMvaa 204,140,220\nRa 184,2,30720\nv_lshlrev_b32 v74, 16, v17\nMvaa 204,136,212\nD1 1,16,46,2048\nMvaa 204,132,204\n"
"Ra 188,2,31744\nMvaa 204,128,196\nv_and_b32 v75, 0xffff0000, v17\nMvaa 208,144,192\nRa 128,2,0\nMU 74,74,93\nMvaa 208,148,200\nMU 75,75,93\nMvaa 208,152,208\nRa 132,2,1024\n"
"Mvaa 208,156,216\nWL 6\nMvaa 212,156,220\nRa 136,2,2048\nv_lshlrev_b32 v76, 16, v18\nMvaa 212,152,212\nD1 1,16,46,3072\nMvaa 212,148,204\nRa 140,2,3072\nMvaa 212,144,196\n"
"v_and_b32 v77, 0xffff0000, v18\nMvaa 216,160,192\nRa 144,2,4096\nMU 76,76,93\nMvaa 216,164,200\nv_bfe_u32 v24, v1, 4, 3\nMvaa 216,168,208\nRa 148,2,5120\nMvaa 216,172,216\nWL 6\n"
"Mvaa 220,172,220\nRa 152,2,6144\nMU 77,77,93\nMvaa 220,168,212\nv_mul_u32_u24 v24, 0xc00, v24\nMvaa 220,164,204\nRa 156,2,7168\nMvaa 220,160,196\nv_bfe_u32 v26, v1, 7, 1\n"
"Mvaa 224,176,192\nRa 160,2,8192\nv_lshlrev_b32 v78, 16, v19\nMvaa 224,180,200\nv_add_lshl_u32 v24, v24, v26, 13\nMvaa 224,184,208\nRa 164,2,9216\nMvaa 224,188,216\nWL 6\n"
"Mvaa 228,188,220\nRa 168,2,10240\nv_and_b32 v79, 0xffff0000, v19\nMvaa 228,184,212\nbuffer_load_dword v27, v24, s[16:19], s41 offen sc0 sc1\nMvaa 228,180,204\nRa 172,2,11264\n"
"Mvaa 228,176,196\nMU 78,78,93\nMvaa 232,128,192\nRa 176,2,12288\nMU 79,79,93\nMvaa 232,132,200\nMvaa 232,136,208\nRa 180,2,13312\nMvaa 232,140,216\ns_waitcnt vmcnt(8) lgkmcnt(6)\n"
"Mvaa 236,140,220\nRa 184,2,14336\nMvaa 236,136,212\nMvaa 236,132,204\nRa 188,2,15360\ns_barrier\nWV 4\nMvaa 236,128,196\nAR 22,227\nAR 23,231\nMvaa 240,144,192\nRa 128,2,32768\n"
"v_max_u32 v21, v22, v23\nMvaa 240,148,200\nAR 22,235\nAR 23,239\nMvaa 240,152,208\nRa 132,2,33792\nv_max3_u32 v21, v21, v22, v23\nMvaa 240,156,216\nWL 6\nAR 22,243\nAR 23,247\n"
"Mvaa 244,156,220\nRa 136,2,34816\nv_max3_u32 v21, v21, v22, v23\nMvaa 244,152,212\nAR 22,251\nAR 23,255\nMvaa 244,148,204\nRa 140,2,35840\nv_max3_u32 v21, v21, v22, v23\n"
"Mvaa 244,144,196\nv_cmp_eq_u32 vcc, -1, v21\ns_nop 0\ns_cbranch_vccnz .S77\n.S78:\nMvaa 136,160,224\nRa 144,2,36864\nMvaa 136,164,232\nMvaa 136,168,240\nRa 148,2,37888\n"
"Mvaa 136,172,248\nWL 6\nMvaa 140,172,252\nRa 152,2,38912\nMvaa 140,168,244\nMvaa 140,164,236\nRa 156,2,39936\nMvaa 140,160,228\nMvaa 144,176,224\nRa 160,2,40960\nMvaa 144,180,232\n"
"Mvaa 144,184,240\nRa 164,2,41984\nMvaa 144,188,248\nWL 6\nMvaa 148,188,252\nRa 168,2,43008\nMvaa 148,184,244\nMvaa 148,180,236\nRa 172,2,44032\nMvaa 148,176,228\nCV 136,136,137\n"
"Mvaa 152,128,224\nRa 176,2,45056\nCV 137,138,139\nMvaa 152,132,232\nCV 138,144,145\nMvaa 152,136,240\nRa 180,2,46080\nMvaa 152,140,248\nWL 6\nMvaa 156,140,252\nRa 184,2,47104\n"
"Mvaa 156,136,244\nCV 139,146,147\nMvaa 156,132,236\nRa 188,2,48128\ns_add_u32 s5, s95, 0\nMvaa 156,128,228\nSv 136,1,16,5,0\nMvaa 160,144,224\nRa 128,2,49152\nMvaa 160,148,232\n"
"CV 140,140,141\nMvaa 160,152,240\nRa 132,2,50176\nMvaa 160,156,248\nWL 6\nCV 141,142,143\nMvaa 164,156,252\nRa 136,2,51200\nMvaa 164,152,244\nCV 142,148,149\nMvaa 164,148,236\n"
"Ra 140,2,52224\nMvaa 164,144,228\nCV 143,150,151\nMvaa 168,160,224\nRa 144,2,53248\nSv 140,1,16,5,1024\nMvaa 168,164,232\nCV 152,152,153\nMvaa 168,168,240\nRa 148,2,54272\n"
"Mvaa 168,172,248\nWL 6\nMvaa 172,172,252\nRa 152,2,55296\nMvaa 172,168,244\nCV 153,154,155\nMvaa 172,164,236\nRa 156,2,56320\nCV 154,160,161\nMvaa 172,160,228\nCV 155,162,163\n"
"Mvaa 176,176,224\nRa 160,2,57344\nMvaa 176,180,232\ns_add_u32 s5, s95, 131072\nMvaa 176,184,240\nRa 164,2,58368\nMvaa 176,188,248\nWL 6\nSv 152,1,16,5,0\nMvaa 180,188,252\n"
"Ra 168,2,59392\nMvaa 180,184,244\nCV 156,156,157\nMvaa 180,180,236\nRa 172,2,60416\nMvaa 180,176,228\nCV 157,158,159\nMvaa 184,128,224\nRa 176,2,61440\nCV 158,164,165\n"
"Mvaa 184,132,232\nCV 159,166,167\nMvaa 184,136,240\nRa 180,2,62464\nMvaa 184,140,248\ns_waitcnt vmcnt(4) lgkmcnt(6)\nMvaa 188,140,252\nRa 184,2,63488\nMvaa 188,136,244\n"
"Sv 156,1,16,5,1024\nMvaa 188,132,236\nRa 188,2,64512\ns_barrier\nMvaa 188,128,228\nCV 168,168,169\nMvaa 192,144,224\nRa 128,3,0\nMvaa 192,148,232\nCV 169,170,171\n"
"Mvaa 192,152,240\nRa 132,3,1024\nMvaa 192,156,248\nWL 6\nCV 170,176,177\nMvaa 196,156,252\nRa 136,3,2048\nMvaa 196,152,244\nCV 171,178,179\nMvaa 196,148,236\nRa 140,3,3072\n"
"Mvaa 196,144,228\ns_add_u32 s5, s95, 262144\nMvaa 200,160,224\nRa 144,3,4096\nSv 168,1,16,5,0\nMvaa 200,164,232\nCV 172,172,173\nMvaa 200,168,240\nRa 148,3,5120\nMvaa 200,172,248\n"
"WL 6\nMvaa 204,172,252\nRa 152,3,6144\nMvaa 204,168,244\nCV 173,174,175\nMvaa 204,164,236\nRa 156,3,7168\nCV 174,180,181\nMvaa 204,160,228\nCV 175,182,183\nMvaa 208,176,224\n"
"Ra 160,3,8192\nMvaa 208,180,232\nSv 172,1,16,5,1024\nMvaa 208,184,240\nRa 164,3,9216\nMvaa 208,188,248\nWL 6\nCV 184,184,185\nMvaa 212,188,252\nRa 168,3,10240\nMvaa 212,184,244\n"
"CV 185,186,187\nMvaa 212,180,236\nRa 172,3,11264\nMvaa 212,176,228\nCV 186,192,193\nMvaa 216,128,224\nRa 176,3,12288\nCV 187,194,195\nMvaa 216,132,232\ns_add_u32 s5, s95, 393216\n"
"Mvaa 216,136,240\nRa 180,3,13312\nMvaa 216,140,248\nWL 6\nMvaa 220,140,252\nRa 184,3,14336\nMvaa 220,136,244\nSv 184,1,16,5,0\nMvaa 220,132,236\nRa 188,3,15360\nCV 188,188,189\n"
"Mvaa 220,128,228\nCV 189,190,191\nMvaa 224,144,224\nCV 190,196,197\nMvaa 224,148,232\nCV 191,198,199\nMvaa 224,152,240\nSv 188,1,16,5,1024\nMvaa 224,156,248\nWL 4\nCV 200,200,201\n"
"Mvaa 228,156,252\nCV 201,202,203\nMvaa 228,152,244\nCV 202,208,209\nMvaa 228,148,236\nCV 203,210,211\nMvaa 228,144,228\ns_add_u32 s5, s95, 524288\nMvaa 232,160,224\n"
"Sv 200,1,16,5,0\nCV 204,204,205\nMvaa 232,164,232\nCV 205,206,207\nMvaa 232,168,240\nCV 206,212,213\nMvaa 232,172,248\nWL 0\nMvaa 236,172,252\nCV 207,214,215\nMvaa 236,168,244\n"
"Sv 204,1,16,5,1024\nMvaa 236,164,236\nCV 216,216,217\nCV 217,218,219\nMvaa 236,160,228\nCV 218,224,225\nMvaa 240,176,224\nCV 219,226,227\nMvaa 240,180,232\n"
"s_add_u32 s5, s95, 655360\nMvaa 240,184,240\nSv 216,1,16,5,0\nMvaa 240,188,248\nCV 220,220,221\nCV 221,222,223\nMvaa 244,188,252\nCV 222,228,229\nMvaa 244,184,244\nCV 223,230,231\n"
"Mvaa 244,180,236\nSv 220,1,16,5,1024\nMvaa 244,176,228\nCV 232,232,233\nCV 233,234,235\nCV 234,240,241\nCV 235,242,243\ns_add_u32 s5, s95, 786432\nSv 232,1,16,5,0\nCV 236,236,237\n"
"CV 237,238,239\nCV 238,244,245\nCV 239,246,247\nSv 236,1,16,5,1024\ns_cmp_eq_u32 s13, 7\ns_cbranch_scc1 .Z89\nWV 14\nv_cmp_eq_u32 s[62:63], s15, v27\ns_mov_b32 vcc_hi, 0\ns_nop 2\n"
"s_lshr_b32 s63, s62, 8\ns_or_b32 s62, s62, s63\ns_andn2_b32 vcc_lo, 0xff, s62\ns_mov_b64 vcc, vcc\ns_cbranch_vccnz .S86\n.S87:\ns_mov_b32 s7, 262144\ns_mov_b32 s3, 0xff\n.Z90:\n"
"s_bitcmp1_b32 s3, 0\ns_cbranch_scc0 .Z92\ns_bitcmp1_b32 s63, 0\ns_cselect_b32 s2, 0x2000, 0\ns_xor_b32 s2, s2, s20\ns_add_u32 s2, s2, 0x0\n"
"buffer_load_dwordx4 v[96:99], v1, s[16:19], s2 offen offset:0 sc0 sc1\nbuffer_load_dwordx4 v[100:103], v1, s[16:19], s2 offen offset:1024 sc0 sc1\n.Z92:\ns_bitcmp1_b32 s3, 1\n"
"s_cbranch_scc0 .Z93\ns_bitcmp1_b32 s63, 1\ns_cselect_b32 s2, 0x2000, 0\ns_xor_b32 s2, s2, s20\ns_add_u32 s2, s2, 0x1800000\n"
"buffer_load_dwordx4 v[104:107], v1, s[16:19], s2 offen offset:0 sc0 sc1\nbuffer_load_dwordx4 v[108:111], v1, s[16:19], s2 offen offset:1024 sc0 sc1\n.Z93:\ns_bitcmp1_b32 s3, 2\n"
"s_cbranch_scc0 .Z94\ns_bitcmp1_b32 s63, 2\ns_cselect_b32 s2, 0x2000, 0\ns_xor_b32 s2, s2, s20\ns_add_u32 s2, s2, 0x3000000\n"
"buffer_load_dwordx4 v[112:115], v1, s[16:19], s2 offen offset:0 sc0 sc1\nbuffer_load_dwordx4 v[116:119], v1, s[16:19], s2 offen offset:1024 sc0 sc1\n.Z94:\ns_bitcmp1_b32 s3, 3\n"
"s_cbranch_scc0 .Z95\ns_bitcmp1_b32 s63, 3\ns_cselect_b32 s2, 0x2000, 0\ns_xor_b32 s2, s2, s20\ns_add_u32 s2, s2, 0x4800000\n"
"buffer_load_dwordx4 v[120:123], v1, s[16:19], s2 offen offset:0 sc0 sc1\nbuffer_load_dwordx4 v[124:127], v1, s[16:19], s2 offen offset:1024 sc0 sc1\n.Z95:\ns_bitcmp1_b32 s3, 4\n"
"s_cbranch_scc0 .Z96\ns_bitcmp1_b32 s63, 4\ns_cselect_b32 s2, 0x2000, 0\ns_xor_b32 s2, s2, s20\ns_add_u32 s2, s2, 0x6000000\n"
"buffer_load_dwordx4 v[32:35], v1, s[16:19], s2 offen offset:0 sc0 sc1\nbuffer_load_dwordx4 v[36:39], v1, s[16:19], s2 offen offset:1024 sc0 sc1\n.Z96:\ns_bitcmp1_b32 s3, 5\n"
"s_cbranch_scc0 .Z97\ns_bitcmp1_b32 s63, 5\ns_cselect_b32 s2, 0x2000, 0\ns_xor_b32 s2, s2, s20\ns_add_u32 s2, s2, 0x7800000\n"
"buffer_load_dwordx4 v[40:43], v1, s[16:19], s2 offen offset:0 sc0 sc1\nbuffer_load_dwordx4 v[44:47], v1, s[16:19], s2 offen offset:1024 sc0 sc1\n.Z97:\ns_bitcmp1_b32 s3, 6\n"
"s_cbranch_scc0 .Z98\ns_bitcmp1_b32 s63, 6\ns_cselect_b32 s2, 0x2000, 0\ns_xor_b32 s2, s2, s20\ns_add_u32 s2, s2, 0x9000000\n"
"buffer_load_dwordx4 v[48:51], v1, s[16:19], s2 offen offset:0 sc0 sc1\nbuffer_load_dwordx4 v[52:55], v1, s[16:19], s2 offen offset:1024 sc0 sc1\n.Z98:\ns_bitcmp1_b32 s3, 7\n"
"s_cbranch_scc0 .Z99\ns_bitcmp1_b32 s63, 7\ns_cselect_b32 s2, 0x2000, 0\ns_xor_b32 s2, s2, s20\ns_add_u32 s2, s2, 0xa800000\n"
"buffer_load_dwordx4 v[56:59], v1, s[16:19], s2 offen offset:0 sc0 sc1\nbuffer_load_dwordx4 v[60:63], v1, s[16:19], s2 offen offset:1024 sc0 sc1\n.Z99:\ns_mov_b32 s3, 0\nWV 0\n"
"v_max_u32 v82, v99, v103\nv_cmp_eq_u32 s[44:45], -1, v82\nv_max_u32 v82, v107, v111\nv_cmp_eq_u32 s[46:47], -1, v82\nv_max_u32 v82, v115, v119\nv_cmp_eq_u32 s[48:49], -1, v82\n"
"v_max_u32 v82, v123, v127\nv_cmp_eq_u32 s[50:51], -1, v82\nv_max_u32 v82, v35, v39\nv_cmp_eq_u32 s[52:53], -1, v82\nv_max_u32 v82, v43, v47\nv_cmp_eq_u32 s[54:55], -1, v82\n"
"v_max_u32 v82, v51, v55\nv_cmp_eq_u32 s[56:57], -1, v82\nv_max_u32 v82, v59, v63\nv_cmp_eq_u32 s[58:59], -1, v82\ns_cmp_lg_u64 s[44:45], 0\ns_cselect_b32 s2, 1, 0\n"
"s_or_b32 s3, s3, s2\ns_cmp_lg_u64 s[46:47], 0\ns_cselect_b32 s2, 2, 0\ns_or_b32 s3, s3, s2\ns_cmp_lg_u64 s[48:49], 0\ns_cselect_b32 s2, 4, 0\ns_or_b32 s3, s3, s2\n"
"s_cmp_lg_u64 s[50:51], 0\ns_cselect_b32 s2, 8, 0\ns_or_b32 s3, s3, s2\ns_cmp_lg_u64 s[52:53], 0\ns_cselect_b32 s2, 16, 0\ns_or_b32 s3, s3, s2\ns_cmp_lg_u64 s[54:55], 0\n"
"s_cselect_b32 s2, 32, 0\ns_or_b32 s3, s3, s2\ns_cmp_lg_u64 s[56:57], 0\ns_cselect_b32 s2, 64, 0\ns_or_b32 s3, s3, s2\ns_cmp_lg_u64 s[58:59], 0\ns_cselect_b32 s2, 128, 0\n"
"s_or_b32 s3, s3, s2\ns_cbranch_scc0 .Z91\ns_sub_u32 s7, s7, 1\ns_cbranch_scc0 .Z90\n.Z91:\nZvvv 136,130,96\nMvvv 136,130,104\nMvvv 136,130,112\nMvvv 136,130,120\nMvvv 136,130,32\n"
"Mvvv 136,130,40\nMvvv 136,130,48\nMvvv 136,130,56\nZvvv 140,128,96\nMvvv 140,128,104\nMvvv 140,128,112\nMvvv 140,128,120\nMvvv 140,128,32\nMvvv 140,128,40\nMvvv 140,128,48\n"
"Mvvv 140,128,56\nZvvv 144,130,100\nMvvv 144,130,108\nMvvv 144,130,116\nMvvv 144,130,124\nMvvv 144,130,36\nMvvv 144,130,44\nMvvv 144,130,52\nMvvv 144,130,60\nZvvv 148,128,100\n"
"Mvvv 148,128,108\nMvvv 148,128,116\nMvvv 148,128,124\nMvvv 148,128,36\nMvvv 148,128,44\nMvvv 148,128,52\nMvvv 148,128,60\nFM 64,80,136\nFM 65,80,137\nFM 66,80,138\nFM 67,80,139\n"
"FM 68,80,140\nFM 69,80,141\nFM 70,80,142\nFM 71,80,143\nCV 12,64,65\nCV 13,66,67\nCV 14,68,69\nCV 15,70,71\nSv 12,91,36,22,0\nFM 72,81,144\nFM 73,81,145\nFM 74,81,146\n"
"FM 75,81,147\nFM 76,81,148\nFM 77,81,149\nFM 78,81,150\nFM 79,81,151\nCV 16,72,73\nCV 17,74,75\nCV 18,76,77\nCV 19,78,79\nSv 16,91,36,23,0\n.Z89:\ns_endpgm\n.HD13:\n"
"buffer_store_dword v25, off, s[16:19], s54 offset:4 sc0 sc1\ns_mov_b32 s90, 0\ns_swappc_b64 s[8:9], s[80:81]\nv_and_b32 v27, 0x1f0, v1\n"
"buffer_load_dword v27, v27, s[16:19], s58 offen offset:4 sc0 sc1\ns_mov_b32 s90, 0\ns_swappc_b64 s[8:9], s[80:81]\nv_cmp_ne_u32 vcc, s15, v27\ns_nop 0\ns_cbranch_vccnz .S100\n"
".S101:\ns_mov_b32 s90, s91\ns_swappc_b64 s[8:9], s[80:81]\nv_lshrrev_b32 v144, 9, v1\nv_mul_u32_u24 v144, 0x18000, v144\nv_lshlrev_b32 v144, 8, v144\nv_and_b32 v145, 0x1f0, v1\n"
"v_add_u32 v144, v144, v145\ns_mov_b32 s6, 0x15c0000\nbuffer_load_dword v136, v144, s[16:19], s6 offen offset:4 sc0 sc1\ns_mov_b32 s6, 0x15c2000\n"
"buffer_load_dword v137, v144, s[16:19], s6 offen offset:4 sc0 sc1\ns_mov_b32 s6, 0x45c0000\nbuffer_load_dword v138, v144, s[16:19], s6 offen offset:4 sc0 sc1\n"
"s_mov_b32 s6, 0x45c2000\nbuffer_load_dword v139, v144, s[16:19], s6 offen offset:4 sc0 sc1\ns_mov_b32 s6, 0x75c0000\n"
"buffer_load_dword v140, v144, s[16:19], s6 offen offset:4 sc0 sc1\ns_mov_b32 s6, 0x75c2000\nbuffer_load_dword v141, v144, s[16:19], s6 offen offset:4 sc0 sc1\n"
"s_mov_b32 s6, 0xa5c0000\nbuffer_load_dword v142, v144, s[16:19], s6 offen offset:4 sc0 sc1\ns_mov_b32 s6, 0xa5c2000\n"
"buffer_load_dword v143, v144, s[16:19], s6 offen offset:4 sc0 sc1\ns_mov_b32 s90, 0\ns_swappc_b64 s[8:9], s[80:81]\nv_xor_b32 v136, s15, v136\nv_xor_b32 v137, s15, v137\n"
"v_xor_b32 v138, s15, v138\nv_xor_b32 v139, s15, v139\nv_xor_b32 v140, s15, v140\nv_xor_b32 v141, s15, v141\nv_xor_b32 v142, s15, v142\nv_xor_b32 v143, s15, v143\n"
"v_min_u32 v136, v136, v137\nv_min_u32 v138, v138, v139\nv_min_u32 v140, v140, v141\nv_min_u32 v142, v142, v143\nv_or3_b32 v136, v136, v138, v140\nv_or_b32 v136, v136, v142\n"
"v_cmp_ne_u32 vcc, 0, v136\ns_nop 0\ns_cbranch_vccnz .S103\n.S104:\ns_lshr_b32 s4, s13, 1\ns_lshl2_add_u32 s4, s12, s4\ns_lshr_b32 s5, s71, 16\ns_cmp_lt_u32 s4, 28\n"
"s_cselect_b32 s5, s71, s5\ns_cselect_b32 s3, 0, 28\ns_and_b32 s5, s5, 0x7ff\ns_sub_u32 s3, s4, s3\ns_mul_i32 s2, s4, 7168\ns_add_u32 s2, s2, 0xc04c800\ns_add_u32 s6, s2, 4096\n"
"buffer_load_dwordx4 v[136:139], v1, s[16:19], s2 offen offset:0 sc0 sc1\nbuffer_load_dwordx4 v[140:143], v1, s[16:19], s2 offen offset:1024 sc0 sc1\n"
"buffer_load_dwordx4 v[144:147], v1, s[16:19], s2 offen offset:2048 sc0 sc1\nbuffer_load_dwordx4 v[148:151], v1, s[16:19], s2 offen offset:3072 sc0 sc1\n"
"buffer_load_dwordx4 v[152:155], v1, s[16:19], s6 offen offset:0 sc0 sc1\nbuffer_load_dwordx4 v[156:159], v1, s[16:19], s6 offen offset:1024 sc0 sc1\n"
"buffer_load_dwordx4 v[160:163], v1, s[16:19], s6 offen offset:2048 sc0 sc1\nv_lshrrev_b32 v167, 1, v1\nv_and_b32 v167, 24, v167\ns_mul_i32 s2, s5, 896\ns_lshl_b32 s3, s3, 5\n"
"s_add_u32 s2, s2, s3\ns_lshl2_add_u32 s5, s5, 0xc042000\nbuffer_load_dwordx2 v[164:165], v167, s[28:31], s2 offen\nbuffer_load_dword v166, off, s[16:19], s5 sc0 sc1\n"
"s_mov_b32 s90, 0\ns_swappc_b64 s[8:9], s[80:81]\nAD 136,136,140\nAD 137,137,141\nAD 138,138,142\nAD 139,139,143\nAD 144,144,148\nAD 145,145,149\nAD 146,146,150\nAD 147,147,151\n"
"AD 152,152,156\nAD 153,153,157\nAD 154,154,158\nAD 155,155,159\nAD 136,136,144\nAD 137,137,145\nAD 138,138,146\nAD 139,139,147\nAD 152,152,160\nAD 153,153,161\nAD 154,154,162\n"
"AD 155,155,163\nAD 136,136,152\nAD 137,137,153\nAD 138,138,154\nAD 139,139,155\nv_mov_b32 v140, v136\nv_mov_b32 v141, v137\nv_mov_b32 v142, v138\nv_mov_b32 v143, v139\n"
"v_permlane32_swap_b32 v136, v140\nv_permlane32_swap_b32 v137, v141\nv_permlane32_swap_b32 v138, v142\nv_permlane32_swap_b32 v139, v143\nAD 136,136,140\nAD 137,137,141\n"
"AD 138,138,142\nAD 139,139,143\nv_mov_b32 v140, v136\nv_mov_b32 v141, v137\nv_mov_b32 v142, v138\nv_mov_b32 v143, v139\nv_permlane16_swap_b32 v136, v140\n"
"v_permlane16_swap_b32 v137, v141\nv_permlane16_swap_b32 v138, v142\nv_permlane16_swap_b32 v139, v143\nAD 136,136,140\nAD 137,137,141\nAD 138,138,142\nAD 139,139,143\n"
"v_add_f32_dpp v136, v136, v136 row_ror:8 row_mask:0xf bank_mask:0xf\nv_add_f32_dpp v137, v137, v137 row_ror:8 row_mask:0xf bank_mask:0xf\n"
"v_add_f32_dpp v138, v138, v138 row_ror:8 row_mask:0xf bank_mask:0xf\nv_add_f32_dpp v139, v139, v139 row_ror:8 row_mask:0xf bank_mask:0xf\n"
"v_add_f32_dpp v136, v136, v136 row_ror:4 row_mask:0xf bank_mask:0xf\nv_add_f32_dpp v137, v137, v137 row_ror:4 row_mask:0xf bank_mask:0xf\n"
"v_add_f32_dpp v138, v138, v138 row_ror:4 row_mask:0xf bank_mask:0xf\nv_add_f32_dpp v139, v139, v139 row_ror:4 row_mask:0xf bank_mask:0xf\nv_mul_f32 v166, 0xbfb8aa3b, v166\n"
"EX 166,166\nCV 136,136,137\nv_add_f32 v166, 1.0, v166\nv_rcp_f32 v166, v166\nCV 138,138,139\nCV 166,166,166\nv_lshlrev_b32 v166, 16, v166\nv_and_b32 v137, 0xffff0000, v136\n"
"v_lshlrev_b32 v136, 16, v136\nv_and_b32 v139, 0xffff0000, v138\nv_lshlrev_b32 v138, 16, v138\nMU 136,136,166\nMU 137,137,166\nMU 138,138,166\nMU 139,139,166\nCV 136,136,137\n"
"CV 138,138,139\nv_and_b32 v137, 0xffff0000, v136\nv_lshlrev_b32 v136, 16, v136\nv_and_b32 v139, 0xffff0000, v138\nv_lshlrev_b32 v138, 16, v138\nv_lshlrev_b32 v140, 16, v164\n"
"v_and_b32 v141, 0xffff0000, v164\nv_lshlrev_b32 v142, 16, v165\nv_and_b32 v143, 0xffff0000, v165\nAD 136,136,140\nAD 137,137,141\nAD 138,138,142\nAD 139,139,143\nCV 136,136,137\n"
"CV 137,138,139\nv_mov_b32 v138, s15\nv_mov_b32 v139, s71\ns_lshr_b32 s4, s13, 1\ns_lshl2_add_u32 s4, s12, s4\ns_lshr_b32 s5, s71, 16\ns_cmp_lt_u32 s4, 28\n"
"s_cselect_b32 s5, s71, s5\ns_cselect_b32 s3, 0, 28\ns_and_b32 s5, s5, 0x7ff\ns_sub_u32 s3, s4, s3\nv_mov_b32 v140, 0x40000000\nv_cmp_gt_u32 vcc, 64, v1\ns_lshl_b32 s4, s4, 6\n"
"s_add_u32 s4, s4, 0xc04b800\nv_cndmask_b32 v140, v140, v1, vcc\nSv 136,140,16,4,0\ns_mov_b32 s90, 100\ns_swappc_b64 s[8:9], s[80:81]\n.S14:\ns_mov_b32 s7, 262144\n.S16:\n"
"s_sleep 8\nbuffer_load_dwordx4 v[32:35], v1, s[16:19], s6 offen offset:0 sc0 sc1\nbuffer_load_dwordx4 v[36:39], v1, s[16:19], s6 offen offset:1024 sc0 sc1\n"
"buffer_load_dwordx4 v[40:43], v1, s[16:19], s6 offen offset:2048 sc0 sc1\nbuffer_load_dwordx4 v[44:47], v1, s[16:19], s6 offen offset:3072 sc0 sc1\nWV 0\nv_xor_b32 v48, s15, v33\n"
"v_xor_b32 v49, s15, v35\nv_xor_b32 v50, s15, v37\nv_xor_b32 v51, s15, v39\nv_xor_b32 v52, s15, v41\nv_xor_b32 v53, s15, v43\nv_xor_b32 v54, s15, v45\nv_xor_b32 v55, s15, v47\n"
"v_or3_b32 v48, v48, v49, v50\nv_or3_b32 v51, v51, v52, v53\nv_or3_b32 v48, v48, v54, v55\nv_or_b32 v48, v48, v51\nv_cmp_ne_u32 vcc, 0, v48\ns_nop 0\ns_cbranch_vccz .S15\n"
"s_sub_u32 s7, s7, 1\ns_cbranch_scc0 .S16\ns_branch .S15\n.S17:\ns_mov_b32 s7, 262144\n.S19:\ns_sleep 8\ns_lshl_b32 s5, s73, 7\ns_add_u32 s5, s5, 0xc04ac00\n"
"v_bfe_u32 v27, v1, 2, 7\nbuffer_load_dword v27, v27, s[16:19], s5 offen sc0 sc1\nWV 0\nv_xor_b32 v27, s15, v27\nv_and_b32 v255, 0xffffdfff, v27\nv_cmp_ne_u32 vcc, 0, v255\n"
"s_nop 0\ns_cbranch_vccz .S18\ns_sub_u32 s7, s7, 1\ns_cbranch_scc0 .S19\ns_branch .S18\n.S21:\ns_mov_b32 s7, 262144\n.S23:\ns_sleep 8\nv_and_b32 v27, 0x70, v1\n"
"buffer_load_dword v27, v27, s[16:19], s61 offen offset:128 sc0 sc1\nWV 0\nv_cmp_ne_u32 vcc, s15, v27\ns_nop 0\ns_cbranch_vccz .S22\ns_sub_u32 s7, s7, 1\ns_cbranch_scc0 .S23\n"
"s_branch .S22\n.T24:\ns_mov_b32 s62, 1\ns_call_b64 s[80:81], .RA10\ns_branch .T25\n.T26:\ns_mov_b32 s62, 1\ns_call_b64 s[80:81], .RA10\ns_branch .T27\n.T28:\ns_mov_b32 s62, 0\n"
"s_call_b64 s[80:81], .RB11\ns_branch .T29\n.T30:\ns_mov_b32 s62, 0\ns_call_b64 s[80:81], .RA10\ns_branch .T31\n.T32:\ns_mov_b32 s62, 0\ns_call_b64 s[80:81], .RB11\ns_branch .T33\n"
".T34:\ns_mov_b32 s62, 0\ns_call_b64 s[80:81], .RA10\ns_branch .T35\n.S36:\ns_mov_b32 s7, 262144\n.S38:\ns_sleep 8\nv_and_b32 v27, 0xf0, v1\n"
"buffer_load_dword v27, v27, s[16:19], s61 offen offset:12 sc0 sc1\nWV 0\nv_cmp_ne_u32 vcc, s15, v27\ns_nop 0\ns_cbranch_vccz .S37\ns_sub_u32 s7, s7, 1\ns_cbranch_scc0 .S38\n"
"s_branch .S37\n.S39:\ns_mov_b32 s7, 262144\n.S41:\ns_sleep 8\nv_and_b32 v27, 0x70, v1\nbuffer_load_dword v27, v27, s[16:19], s61 offen offset:136 sc0 sc1\nWV 0\n"
"v_cmp_ne_u32 vcc, s15, v27\ns_nop 0\ns_cbranch_vccz .S40\ns_sub_u32 s7, s7, 1\ns_cbranch_scc0 .S41\ns_branch .S40\n.E43:\ns_swappc_b64 s[80:81], s[8:9]\ns_branch .E44\n.T45:\n"
"s_mov_b32 s62, 0\ns_call_b64 s[80:81], .RB11\ns_branch .T46\n.T47:\ns_mov_b32 s62, 0\ns_call_b64 s[80:81], .RA10\ns_branch .T48\n.E49:\nv_mov_b32 v12, s97\nv_mov_b32 v13, s97\n"
"v_mov_b32 v14, s97\nv_mov_b32 v15, s97\nv_cmpx_gt_i32 vcc, 3, v3\nv_mov_b32 v7, s97\nv_mov_b32 v19, s97\nv_cmpx_gt_i32 vcc, 2, v3\nv_mov_b32 v6, s97\nv_mov_b32 v18, s97\n"
"v_cmpx_gt_i32 vcc, 1, v3\nv_mov_b32 v5, s97\nv_mov_b32 v17, s97\nv_cmpx_gt_i32 vcc, 0, v3\nv_mov_b32 v4, s97\nv_mov_b32 v16, s97\nv_cmpx_gt_i32 vcc, -16, v3\nv_mov_b32 v8, s97\n"
"v_mov_b32 v9, s97\nv_mov_b32 v10, s97\nv_mov_b32 v11, s97\ns_mov_b64 exec, -1\ns_nop 2\ns_branch .E50\n.T51:\ns_mov_b32 s62, 0\ns_call_b64 s[80:81], .RB11\ns_branch .T52\n.T53:\n"
"s_mov_b32 s62, 0\ns_call_b64 s[80:81], .RA10\ns_branch .T54\n.E55:\nv_mov_b32 v12, s97\nv_mov_b32 v13, s97\nv_mov_b32 v14, s97\nv_mov_b32 v15, s97\nv_cmpx_gt_i32 vcc, 3, v0\n"
"v_mov_b32 v7, s97\nv_mov_b32 v19, s97\nv_cmpx_gt_i32 vcc, 2, v0\nv_mov_b32 v6, s97\nv_mov_b32 v18, s97\nv_cmpx_gt_i32 vcc, 1, v0\nv_mov_b32 v5, s97\nv_mov_b32 v17, s97\n"
"v_cmpx_gt_i32 vcc, 0, v0\nv_mov_b32 v4, s97\nv_mov_b32 v16, s97\nv_cmpx_gt_i32 vcc, -16, v0\nv_mov_b32 v8, s97\nv_mov_b32 v9, s97\nv_mov_b32 v10, s97\nv_mov_b32 v11, s97\n"
"s_mov_b64 exec, -1\ns_nop 2\ns_branch .E56\n.T57:\ns_mov_b32 s62, 0\ns_call_b64 s[80:81], .RB11\ns_branch .T58\n.T59:\ns_mov_b32 s62, 0\ns_call_b64 s[80:81], .RA10\ns_branch .T60\n"
".E62:\nbuffer_load_dwordx4 v[100:103], v108, s[16:19], s65 offen offset:0 sc0 sc1\nbuffer_load_dwordx4 v[104:107], v108, s[16:19], s65 offen offset:256 sc0 sc1\ns_branch .E63\n"
".S68:\ns_mov_b32 s7, 262144\n.S70:\ns_sleep 8\nbuffer_load_dwordx4 v[100:103], v108, s[16:19], s65 offen offset:0 sc0 sc1\n"
"buffer_load_dwordx4 v[104:107], v108, s[16:19], s65 offen offset:256 sc0 sc1\nWV 0\nv_xor_b32 v109, s15, v102\nv_xor_b32 v110, s15, v106\nv_or_b32 v109, v109, v110\n"
"v_cmp_ne_u32 vcc, 0, v109\ns_nop 0\ns_cbranch_vccz .S69\ns_sub_u32 s7, s7, 1\ns_cbranch_scc0 .S70\ns_branch .S69\n.D64:\nWV 0\nv_xor_b32 v109, s15, v102\n"
"v_xor_b32 v110, s15, v106\nv_or_b32 v109, v109, v110\nv_cmp_ne_u32 vcc, 0, v109\ns_nop 0\ns_cbranch_vccnz .S68\n.S69:\ns_add_u32 s5, s66, 0\n"
"buffer_load_dwordx4 a[192:195], v1, s[16:19], s5 offen offset:0 sc1\nbuffer_load_dwordx4 a[196:199], v1, s[16:19], s5 offen offset:1024 sc1\n"
"buffer_load_dwordx4 a[200:203], v1, s[16:19], s5 offen offset:2048 sc1\nbuffer_load_dwordx4 a[204:207], v1, s[16:19], s5 offen offset:3072 sc1\ns_add_u32 s5, s66, 4096\n"
"buffer_load_dwordx4 a[208:211], v1, s[16:19], s5 offen offset:0 sc1\nbuffer_load_dwordx4 a[212:215], v1, s[16:19], s5 offen offset:1024 sc1\n"
"buffer_load_dwordx4 a[216:219], v1, s[16:19], s5 offen offset:2048 sc1\nbuffer_load_dwordx4 a[220:223], v1, s[16:19], s5 offen offset:3072 sc1\ns_add_u32 s5, s66, 16384\n"
"buffer_load_dwordx4 a[224:227], v1, s[16:19], s5 offen offset:0 sc1\nbuffer_load_dwordx4 a[228:231], v1, s[16:19], s5 offen offset:1024 sc1\n"
"buffer_load_dwordx4 a[232:235], v1, s[16:19], s5 offen offset:2048 sc1\nbuffer_load_dwordx4 a[236:239], v1, s[16:19], s5 offen offset:3072 sc1\ns_add_u32 s5, s66, 20480\n"
"buffer_load_dwordx4 a[240:243], v1, s[16:19], s5 offen offset:0 sc1\nbuffer_load_dwordx4 a[244:247], v1, s[16:19], s5 offen offset:1024 sc1\n"
"buffer_load_dwordx4 a[248:251], v1, s[16:19], s5 offen offset:2048 sc1\nbuffer_load_dwordx4 a[252:255], v1, s[16:19], s5 offen offset:3072 sc1\nWV 0\ns_mov_b32 s67, 0\n"
"s_branch .D65\n.D66:\nv_mov_b32 v130, 0\nv_mov_b32 v131, 0\ns_mov_b32 s63, 0xff800000\ns_mov_b32 s67, 1\ns_branch .D67\n.T71:\ns_lshl3_add_u32 s62, s67, 2\n"
"s_call_b64 s[80:81], .RB11\ns_cmp_eq_u32 s67, 0\ns_cbranch_scc1 .T72\nSU 20,100,88\nv_min_f32 v20, 0, v20\nEX 20,20\ns_nop 0\nFM 86,101,20\nSU 21,104,89\nv_min_f32 v21, 0, v21\n"
"EX 21,21\ns_nop 0\nFM 87,105,21\nAR 22,192\nv_and_b32 v23, 0xffff0000, v22\nv_lshlrev_b32 v22, 16, v22\nMU 22,22,20\nMU 23,23,20\nCV 22,22,23\nAW 192,22\nAR 22,193\n"
"v_and_b32 v23, 0xffff0000, v22\nv_lshlrev_b32 v22, 16, v22\nMU 22,22,20\nMU 23,23,20\nCV 22,22,23\nAW 193,22\nAR 22,194\nv_and_b32 v23, 0xffff0000, v22\n"
"v_lshlrev_b32 v22, 16, v22\nMU 22,22,20\nMU 23,23,20\nCV 22,22,23\nAW 194,22\nAR 22,195\nv_and_b32 v23, 0xffff0000, v22\nv_lshlrev_b32 v22, 16, v22\nMU 22,22,20\nMU 23,23,20\n"
"CV 22,22,23\nAW 195,22\nAR 22,196\nv_and_b32 v23, 0xffff0000, v22\nv_lshlrev_b32 v22, 16, v22\nMU 22,22,21\nMU 23,23,21\nCV 22,22,23\nAW 196,22\nAR 22,197\n"
"v_and_b32 v23, 0xffff0000, v22\nv_lshlrev_b32 v22, 16, v22\nMU 22,22,21\nMU 23,23,21\nCV 22,22,23\nAW 197,22\nAR 22,198\nv_and_b32 v23, 0xffff0000, v22\n"
"v_lshlrev_b32 v22, 16, v22\nMU 22,22,21\nMU 23,23,21\nCV 22,22,23\nAW 198,22\nAR 22,199\nv_and_b32 v23, 0xffff0000, v22\nv_lshlrev_b32 v22, 16, v22\nMU 22,22,21\nMU 23,23,21\n"
"CV 22,22,23\nAW 199,22\nAR 22,200\nv_and_b32 v23, 0xffff0000, v22\nv_lshlrev_b32 v22, 16, v22\nMU 22,22,20\nMU 23,23,20\nCV 22,22,23\nAW 200,22\nAR 22,201\n"
"v_and_b32 v23, 0xffff0000, v22\nv_lshlrev_b32 v22, 16, v22\nMU 22,22,20\nMU 23,23,20\nCV 22,22,23\nAW 201,22\nAR 22,202\nv_and_b32 v23, 0xffff0000, v22\n"
"v_lshlrev_b32 v22, 16, v22\nMU 22,22,20\nMU 23,23,20\nCV 22,22,23\nAW 202,22\nAR 22,203\nv_and_b32 v23, 0xffff0000, v22\nv_lshlrev_b32 v22, 16, v22\nMU 22,22,20\nMU 23,23,20\n"
"CV 22,22,23\nAW 203,22\nAR 22,204\nv_and_b32 v23, 0xffff0000, v22\nv_lshlrev_b32 v22, 16, v22\nMU 22,22,21\nMU 23,23,21\nCV 22,22,23\nAW 204,22\nAR 22,205\n"
"v_and_b32 v23, 0xffff0000, v22\nv_lshlrev_b32 v22, 16, v22\nMU 22,22,21\nMU 23,23,21\nCV 22,22,23\nAW 205,22\nAR 22,206\nv_and_b32 v23, 0xffff0000, v22\n"
"v_lshlrev_b32 v22, 16, v22\nMU 22,22,21\nMU 23,23,21\nCV 22,22,23\nAW 206,22\nAR 22,207\nv_and_b32 v23, 0xffff0000, v22\nv_lshlrev_b32 v22, 16, v22\nMU 22,22,21\nMU 23,23,21\n"
"CV 22,22,23\nAW 207,22\nAR 22,208\nv_and_b32 v23, 0xffff0000, v22\nv_lshlrev_b32 v22, 16, v22\nMU 22,22,20\nMU 23,23,20\nCV 22,22,23\nAW 208,22\nAR 22,209\n"
"v_and_b32 v23, 0xffff0000, v22\nv_lshlrev_b32 v22, 16, v22\nMU 22,22,20\nMU 23,23,20\nCV 22,22,23\nAW 209,22\nAR 22,210\nv_and_b32 v23, 0xffff0000, v22\n"
"v_lshlrev_b32 v22, 16, v22\nMU 22,22,20\nMU 23,23,20\nCV 22,22,23\nAW 210,22\nAR 22,211\nv_and_b32 v23, 0xffff0000, v22\nv_lshlrev_b32 v22, 16, v22\nMU 22,22,20\nMU 23,23,20\n"
"CV 22,22,23\nAW 211,22\nAR 22,212\nv_and_b32 v23, 0xffff0000, v22\nv_lshlrev_b32 v22, 16, v22\nMU 22,22,21\nMU 23,23,21\nCV 22,22,23\nAW 212,22\nAR 22,213\n"
"v_and_b32 v23, 0xffff0000, v22\nv_lshlrev_b32 v22, 16, v22\nMU 22,22,21\nMU 23,23,21\nCV 22,22,23\nAW 213,22\nAR 22,214\nv_and_b32 v23, 0xffff0000, v22\n"
"v_lshlrev_b32 v22, 16, v22\nMU 22,22,21\nMU 23,23,21\nCV 22,22,23\nAW 214,22\nAR 22,215\nv_and_b32 v23, 0xffff0000, v22\nv_lshlrev_b32 v22, 16, v22\nMU 22,22,21\nMU 23,23,21\n"
"CV 22,22,23\nAW 215,22\nAR 22,216\nv_and_b32 v23, 0xffff0000, v22\nv_lshlrev_b32 v22, 16, v22\nMU 22,22,20\nMU 23,23,20\nCV 22,22,23\nAW 216,22\nAR 22,217\n"
"v_and_b32 v23, 0xffff0000, v22\nv_lshlrev_b32 v22, 16, v22\nMU 22,22,20\nMU 23,23,20\nCV 22,22,23\nAW 217,22\nAR 22,218\nv_and_b32 v23, 0xffff0000, v22\n"
"v_lshlrev_b32 v22, 16, v22\nMU 22,22,20\nMU 23,23,20\nCV 22,22,23\nAW 218,22\nAR 22,219\nv_and_b32 v23, 0xffff0000, v22\nv_lshlrev_b32 v22, 16, v22\nMU 22,22,20\nMU 23,23,20\n"
"CV 22,22,23\nAW 219,22\nAR 22,220\nv_and_b32 v23, 0xffff0000, v22\nv_lshlrev_b32 v22, 16, v22\nMU 22,22,21\nMU 23,23,21\nCV 22,22,23\nAW 220,22\nAR 22,221\n"
"v_and_b32 v23, 0xffff0000, v22\nv_lshlrev_b32 v22, 16, v22\nMU 22,22,21\nMU 23,23,21\nCV 22,22,23\nAW 221,22\nAR 22,222\nv_and_b32 v23, 0xffff0000, v22\n"
"v_lshlrev_b32 v22, 16, v22\nMU 22,22,21\nMU 23,23,21\nCV 22,22,23\nAW 222,22\nAR 22,223\nv_and_b32 v23, 0xffff0000, v22\nv_lshlrev_b32 v22, 16, v22\nMU 22,22,21\nMU 23,23,21\n"
"CV 22,22,23\nAW 223,22\nAR 22,224\nv_and_b32 v23, 0xffff0000, v22\nv_lshlrev_b32 v22, 16, v22\nMU 22,22,20\nMU 23,23,20\nCV 22,22,23\nAW 224,22\nAR 22,225\n"
"v_and_b32 v23, 0xffff0000, v22\nv_lshlrev_b32 v22, 16, v22\nMU 22,22,20\nMU 23,23,20\nCV 22,22,23\nAW 225,22\nAR 22,226\nv_and_b32 v23, 0xffff0000, v22\n"
"v_lshlrev_b32 v22, 16, v22\nMU 22,22,20\nMU 23,23,20\nCV 22,22,23\nAW 226,22\nAR 22,227\nv_and_b32 v23, 0xffff0000, v22\nv_lshlrev_b32 v22, 16, v22\nMU 22,22,20\nMU 23,23,20\n"
"CV 22,22,23\nAW 227,22\nAR 22,228\nv_and_b32 v23, 0xffff0000, v22\nv_lshlrev_b32 v22, 16, v22\nMU 22,22,21\nMU 23,23,21\nCV 22,22,23\nAW 228,22\nAR 22,229\n"
"v_and_b32 v23, 0xffff0000, v22\nv_lshlrev_b32 v22, 16, v22\nMU 22,22,21\nMU 23,23,21\nCV 22,22,23\nAW 229,22\nAR 22,230\nv_and_b32 v23, 0xffff0000, v22\n"
"v_lshlrev_b32 v22, 16, v22\nMU 22,22,21\nMU 23,23,21\nCV 22,22,23\nAW 230,22\nAR 22,231\nv_and_b32 v23, 0xffff0000, v22\nv_lshlrev_b32 v22, 16, v22\nMU 22,22,21\nMU 23,23,21\n"
"CV 22,22,23\nAW 231,22\nAR 22,232\nv_and_b32 v23, 0xffff0000, v22\nv_lshlrev_b32 v22, 16, v22\nMU 22,22,20\nMU 23,23,20\nCV 22,22,23\nAW 232,22\nAR 22,233\n"
"v_and_b32 v23, 0xffff0000, v22\nv_lshlrev_b32 v22, 16, v22\nMU 22,22,20\nMU 23,23,20\nCV 22,22,23\nAW 233,22\nAR 22,234\nv_and_b32 v23, 0xffff0000, v22\n"
"v_lshlrev_b32 v22, 16, v22\nMU 22,22,20\nMU 23,23,20\nCV 22,22,23\nAW 234,22\nAR 22,235\nv_and_b32 v23, 0xffff0000, v22\nv_lshlrev_b32 v22, 16, v22\nMU 22,22,20\nMU 23,23,20\n"
"CV 22,22,23\nAW 235,22\nAR 22,236\nv_and_b32 v23, 0xffff0000, v22\nv_lshlrev_b32 v22, 16, v22\nMU 22,22,21\nMU 23,23,21\nCV 22,22,23\nAW 236,22\nAR 22,237\n"
"v_and_b32 v23, 0xffff0000, v22\nv_lshlrev_b32 v22, 16, v22\nMU 22,22,21\nMU 23,23,21\nCV 22,22,23\nAW 237,22\nAR 22,238\nv_and_b32 v23, 0xffff0000, v22\n"
"v_lshlrev_b32 v22, 16, v22\nMU 22,22,21\nMU 23,23,21\nCV 22,22,23\nAW 238,22\nAR 22,239\nv_and_b32 v23, 0xffff0000, v22\nv_lshlrev_b32 v22, 16, v22\nMU 22,22,21\nMU 23,23,21\n"
"CV 22,22,23\nAW 239,22\nAR 22,240\nv_and_b32 v23, 0xffff0000, v22\nv_lshlrev_b32 v22, 16, v22\nMU 22,22,20\nMU 23,23,20\nCV 22,22,23\nAW 240,22\nAR 22,241\n"
"v_and_b32 v23, 0xffff0000, v22\nv_lshlrev_b32 v22, 16, v22\nMU 22,22,20\nMU 23,23,20\nCV 22,22,23\nAW 241,22\nAR 22,242\nv_and_b32 v23, 0xffff0000, v22\n"
"v_lshlrev_b32 v22, 16, v22\nMU 22,22,20\nMU 23,23,20\nCV 22,22,23\nAW 242,22\nAR 22,243\nv_and_b32 v23, 0xffff0000, v22\nv_lshlrev_b32 v22, 16, v22\nMU 22,22,20\nMU 23,23,20\n"
"CV 22,22,23\nAW 243,22\nAR 22,244\nv_and_b32 v23, 0xffff0000, v22\nv_lshlrev_b32 v22, 16, v22\nMU 22,22,21\nMU 23,23,21\nCV 22,22,23\nAW 244,22\nAR 22,245\n"
"v_and_b32 v23, 0xffff0000, v22\nv_lshlrev_b32 v22, 16, v22\nMU 22,22,21\nMU 23,23,21\nCV 22,22,23\nAW 245,22\nAR 22,246\nv_and_b32 v23, 0xffff0000, v22\n"
"v_lshlrev_b32 v22, 16, v22\nMU 22,22,21\nMU 23,23,21\nCV 22,22,23\nAW 246,22\nAR 22,247\nv_and_b32 v23, 0xffff0000, v22\nv_lshlrev_b32 v22, 16, v22\nMU 22,22,21\nMU 23,23,21\n"
"CV 22,22,23\nAW 247,22\nAR 22,248\nv_and_b32 v23, 0xffff0000, v22\nv_lshlrev_b32 v22, 16, v22\nMU 22,22,20\nMU 23,23,20\nCV 22,22,23\nAW 248,22\nAR 22,249\n"
"v_and_b32 v23, 0xffff0000, v22\nv_lshlrev_b32 v22, 16, v22\nMU 22,22,20\nMU 23,23,20\nCV 22,22,23\nAW 249,22\nAR 22,250\nv_and_b32 v23, 0xffff0000, v22\n"
"v_lshlrev_b32 v22, 16, v22\nMU 22,22,20\nMU 23,23,20\nCV 22,22,23\nAW 250,22\nAR 22,251\nv_and_b32 v23, 0xffff0000, v22\nv_lshlrev_b32 v22, 16, v22\nMU 22,22,20\nMU 23,23,20\n"
"CV 22,22,23\nAW 251,22\nAR 22,252\nv_and_b32 v23, 0xffff0000, v22\nv_lshlrev_b32 v22, 16, v22\nMU 22,22,21\nMU 23,23,21\nCV 22,22,23\nAW 252,22\nAR 22,253\n"
"v_and_b32 v23, 0xffff0000, v22\nv_lshlrev_b32 v22, 16, v22\nMU 22,22,21\nMU 23,23,21\nCV 22,22,23\nAW 253,22\nAR 22,254\nv_and_b32 v23, 0xffff0000, v22\n"
"v_lshlrev_b32 v22, 16, v22\nMU 22,22,21\nMU 23,23,21\nCV 22,22,23\nAW 254,22\nAR 22,255\nv_and_b32 v23, 0xffff0000, v22\nv_lshlrev_b32 v22, 16, v22\nMU 22,22,21\nMU 23,23,21\n"
"CV 22,22,23\nAW 255,22\nv_lshrrev_b32 v22, 8, v1\nv_bfe_u32 v23, v1, 6, 2\nv_cmp_eq_u32 vcc, v22, v23\nv_and_b32 v22, 16, v1\nv_mov_b32 v23, 0x3f80\n"
"v_cndmask_b32 v23, 0, v23, vcc\nv_lshlrev_b32 v23, v22, v23\nv_and_b32 v22, 32, v1\nv_cmp_eq_u32 vcc, 0, v22\nv_mov_b32 v128, 0\nv_mov_b32 v129, 0\n"
"v_cndmask_b32 v130, 0, v23, vcc\nv_sub_u32 v131, v23, v130\nv_mov_b32 v132, 0\nv_mov_b32 v133, 0\ns_nop 1\nMava 0,130,192\nMava 8,128,192\nMava 12,128,196\nMava 4,130,196\n"
"Mava 16,130,200\nMava 24,128,200\nMava 28,128,204\nMava 20,130,204\nMava 32,130,208\nMava 40,128,208\nMava 44,128,212\nMava 36,130,212\nMava 48,130,216\nMava 56,128,216\n"
"Mava 60,128,220\nMava 52,130,220\nMava 64,130,224\nMava 72,128,224\nMava 76,128,228\nMava 68,130,228\nMava 80,130,232\nMava 88,128,232\nMava 92,128,236\nMava 84,130,236\n"
"Mava 96,130,240\nMava 104,128,240\nMava 108,128,244\nMava 100,130,244\nMava 112,130,248\nMava 120,128,248\nMava 124,128,252\nMava 116,130,252\ns_nop 7\ns_branch .T72\n.S74:\n"
"s_mov_b32 s7, 262144\n.S76:\ns_sleep 8\ns_add_u32 s4, s92, 0\nbuffer_load_dwordx4 a[192:195], v1, s[16:19], s4 offen offset:0 sc0 sc1\n"
"buffer_load_dwordx4 a[196:199], v1, s[16:19], s4 offen offset:1024 sc0 sc1\ns_add_u32 s4, s92, 131072\nbuffer_load_dwordx4 a[200:203], v1, s[16:19], s4 offen offset:0 sc0 sc1\n"
"buffer_load_dwordx4 a[204:207], v1, s[16:19], s4 offen offset:1024 sc0 sc1\ns_add_u32 s4, s92, 262144\nbuffer_load_dwordx4 a[208:211], v1, s[16:19], s4 offen offset:0 sc0 sc1\n"
"buffer_load_dwordx4 a[212:215], v1, s[16:19], s4 offen offset:1024 sc0 sc1\ns_add_u32 s4, s92, 393216\nbuffer_load_dwordx4 a[216:219], v1, s[16:19], s4 offen offset:0 sc0 sc1\n"
"buffer_load_dwordx4 a[220:223], v1, s[16:19], s4 offen offset:1024 sc0 sc1\nWV 0\nAR 22,195\nAR 23,199\nv_max_u32 v21, v22, v23\nAR 22,203\nAR 23,207\n"
"v_max3_u32 v21, v21, v22, v23\nAR 22,211\nAR 23,215\nv_max3_u32 v21, v21, v22, v23\nAR 22,219\nAR 23,223\nv_max3_u32 v21, v21, v22, v23\nv_cmp_eq_u32 vcc, -1, v21\ns_nop 0\n"
"s_cbranch_vccz .S75\ns_sub_u32 s7, s7, 1\ns_cbranch_scc0 .S76\ns_branch .S75\n.S77:\ns_mov_b32 s7, 262144\n.S79:\ns_sleep 8\ns_add_u32 s4, s92, 524288\n"
"buffer_load_dwordx4 a[224:227], v1, s[16:19], s4 offen offset:0 sc0 sc1\nbuffer_load_dwordx4 a[228:231], v1, s[16:19], s4 offen offset:1024 sc0 sc1\ns_add_u32 s4, s92, 655360\n"
"buffer_load_dwordx4 a[232:235], v1, s[16:19], s4 offen offset:0 sc0 sc1\nbuffer_load_dwordx4 a[236:239], v1, s[16:19], s4 offen offset:1024 sc0 sc1\ns_add_u32 s4, s92, 786432\n"
"buffer_load_dwordx4 a[240:243], v1, s[16:19], s4 offen offset:0 sc0 sc1\nbuffer_load_dwordx4 a[244:247], v1, s[16:19], s4 offen offset:1024 sc0 sc1\ns_add_u32 s4, s92, 917504\n"
"buffer_load_dwordx4 a[248:251], v1, s[16:19], s4 offen offset:0 sc0 sc1\nbuffer_load_dwordx4 a[252:255], v1, s[16:19], s4 offen offset:1024 sc0 sc1\nWV 0\nAR 22,227\nAR 23,231\n"
"v_max_u32 v21, v22, v23\nAR 22,235\nAR 23,239\nv_max3_u32 v21, v21, v22, v23\nAR 22,243\nAR 23,247\nv_max3_u32 v21, v21, v22, v23\nAR 22,251\nAR 23,255\n"
"v_max3_u32 v21, v21, v22, v23\nv_cmp_eq_u32 vcc, -1, v21\ns_nop 0\ns_cbranch_vccz .S78\ns_sub_u32 s7, s7, 1\ns_cbranch_scc0 .S79\ns_branch .S78\n.S80:\ns_mov_b32 s7, 262144\n"
".S82:\ns_sleep 8\nbuffer_load_dwordx4 v[64:67], v89, s[16:19], s40 offen offset:0 sc0 sc1\nbuffer_load_dwordx4 v[68:71], v89, s[16:19], s40 offen offset:16 sc0 sc1\n"
"buffer_load_dwordx4 v[72:75], v89, s[16:19], s40 offen offset:1792 sc0 sc1\nbuffer_load_dwordx4 v[76:79], v89, s[16:19], s40 offen offset:1808 sc0 sc1\nWV 0\n"
"v_xor_b32 v92, s15, v66\nv_xor_b32 v93, s15, v70\nv_xor_b32 v94, s15, v74\nv_or3_b32 v92, v92, v93, v94\nv_xor_b32 v93, s15, v78\nv_or_b32 v92, v92, v93\nv_cmp_ne_u32 vcc, 0, v92\n"
"s_nop 0\ns_cbranch_vccz .S81\ns_sub_u32 s7, s7, 1\ns_cbranch_scc0 .S82\ns_branch .S81\n.S83:\ns_mov_b32 s7, 262144\n.S85:\ns_sleep 8\n"
"buffer_load_dword v27, off, s[16:19], s60 offset:512 sc0 sc1\nWV 0\nv_cmp_ne_u32 vcc, s15, v27\ns_nop 0\ns_cbranch_vccz .S84\ns_sub_u32 s7, s7, 1\ns_cbranch_scc0 .S85\n"
"s_branch .S84\n.S86:\ns_mov_b32 s7, 262144\n.S88:\ns_sleep 8\nbuffer_load_dword v27, v24, s[16:19], s41 offen sc0 sc1\nWV 0\nv_cmp_eq_u32 s[62:63], s15, v27\ns_mov_b32 vcc_hi, 0\n"
"s_nop 2\ns_lshr_b32 s63, s62, 8\ns_or_b32 s62, s62, s63\ns_andn2_b32 vcc_lo, 0xff, s62\ns_mov_b64 vcc, vcc\ns_cbranch_vccz .S87\ns_sub_u32 s7, s7, 1\ns_cbranch_scc0 .S88\n"
"s_branch .S87\n.S100:\ns_mov_b32 s7, 262144\n.S102:\ns_sleep 8\nv_and_b32 v27, 0x1f0, v1\nbuffer_load_dword v27, v27, s[16:19], s58 offen offset:4 sc0 sc1\nWV 0\n"
"v_cmp_ne_u32 vcc, s15, v27\ns_nop 0\ns_cbranch_vccz .S101\ns_sub_u32 s7, s7, 1\ns_cbranch_scc0 .S102\ns_branch .S101\n.S103:\ns_mov_b32 s7, 262144\n.S105:\ns_sleep 8\n"
"v_lshrrev_b32 v144, 9, v1\nv_mul_u32_u24 v144, 0x18000, v144\nv_lshlrev_b32 v144, 8, v144\nv_and_b32 v145, 0x1f0, v1\nv_add_u32 v144, v144, v145\ns_mov_b32 s6, 0x15c0000\n"
"buffer_load_dword v136, v144, s[16:19], s6 offen offset:4 sc0 sc1\ns_mov_b32 s6, 0x15c2000\nbuffer_load_dword v137, v144, s[16:19], s6 offen offset:4 sc0 sc1\n"
"s_mov_b32 s6, 0x45c0000\nbuffer_load_dword v138, v144, s[16:19], s6 offen offset:4 sc0 sc1\ns_mov_b32 s6, 0x45c2000\n"
"buffer_load_dword v139, v144, s[16:19], s6 offen offset:4 sc0 sc1\ns_mov_b32 s6, 0x75c0000\nbuffer_load_dword v140, v144, s[16:19], s6 offen offset:4 sc0 sc1\n"
"s_mov_b32 s6, 0x75c2000\nbuffer_load_dword v141, v144, s[16:19], s6 offen offset:4 sc0 sc1\ns_mov_b32 s6, 0xa5c0000\n"
"buffer_load_dword v142, v144, s[16:19], s6 offen offset:4 sc0 sc1\ns_mov_b32 s6, 0xa5c2000\nbuffer_load_dword v143, v144, s[16:19], s6 offen offset:4 sc0 sc1\nWV 0\n"
"v_xor_b32 v136, s15, v136\nv_xor_b32 v137, s15, v137\nv_xor_b32 v138, s15, v138\nv_xor_b32 v139, s15, v139\nv_xor_b32 v140, s15, v140\nv_xor_b32 v141, s15, v141\n"
"v_xor_b32 v142, s15, v142\nv_xor_b32 v143, s15, v143\nv_min_u32 v136, v136, v137\nv_min_u32 v138, v138, v139\nv_min_u32 v140, v140, v141\nv_min_u32 v142, v142, v143\n"
"v_or3_b32 v136, v136, v138, v140\nv_or_b32 v136, v136, v142\nv_cmp_ne_u32 vcc, 0, v136\ns_nop 0\ns_cbranch_vccz .S104\ns_sub_u32 s7, s7, 1\ns_cbranch_scc0 .S105\ns_branch .S104\n"
".RA10:\ns_nop 7\ns_nop 3\nv_max3_f32 v22, v32, v33, v34\nv_max3_f32 v22, v22, v35, v40\nv_max3_f32 v22, v22, v41, v42\nv_max_f32 v22, v22, v43\nv_mov_b32 v91, v22\ns_nop 1\n"
"v_permlane32_swap_b32 v22, v91\nv_max_f32 v22, v22, v91\nv_mov_b32 v91, v22\ns_nop 1\nv_permlane16_swap_b32 v22, v91\nv_max_f32 v22, v22, v91\nv_ceil_f32 v22, v22\n"
"v_max3_f32 v23, v36, v37, v38\nv_max3_f32 v23, v23, v39, v44\nv_max3_f32 v23, v23, v45, v46\nv_max_f32 v23, v23, v47\nv_mov_b32 v91, v23\ns_nop 1\nv_permlane32_swap_b32 v23, v91\n"
"v_max_f32 v23, v23, v91\nv_mov_b32 v91, v23\ns_nop 1\nv_permlane16_swap_b32 v23, v91\nv_max_f32 v23, v23, v91\nv_ceil_f32 v23, v23\ns_bitcmp1_b32 s62, 0\ns_cbranch_scc0 .R106\n"
"v_cmp_lt_f32 vcc, 0xff000000, v22\ns_nop 1\nv_cndmask_b32 v22, 0, v22, vcc\nv_cmp_lt_f32 vcc, 0xff000000, v23\ns_nop 1\nv_cndmask_b32 v23, 0, v23, vcc\ns_branch .R107\n.R106:\n"
"v_max_f32 v22, 0, v22\nv_max_f32 v23, 0, v23\ns_bitcmp1_b32 s62, 3\ns_cbranch_scc0 .R107\nv_max_f32 v22, v20, v22\nv_max_f32 v23, v21, v23\n.R107:\nAD 88,88,22\nAD 89,89,23\n"
"SU 32,32,22\nSU 33,33,22\nSU 34,34,22\nSU 35,35,22\nSU 36,36,23\nSU 37,37,23\nSU 38,38,23\nSU 39,39,23\nSU 40,40,22\nSU 41,41,22\nSU 42,42,22\nSU 43,43,22\nSU 44,44,23\n"
"SU 45,45,23\nSU 46,46,23\nSU 47,47,23\nv_mov_b32 v90, v80\nv_mov_b32 v91, v81\nEX 80,32\nEX 81,33\ns_nop 0\nAD 84,80,81\nCV 64,80,81\nEX 82,34\nEX 83,35\nAD 84,84,82\nAD 84,84,83\n"
"CV 65,82,83\nEX 80,36\nEX 81,37\ns_nop 0\nAD 85,80,81\nCV 68,80,81\nEX 82,38\nEX 83,39\nAD 85,85,82\nAD 85,85,83\nCV 69,82,83\nEX 80,40\nEX 81,41\nAD 84,84,80\nAD 84,84,81\n"
"CV 66,80,81\nEX 82,42\nEX 83,43\nAD 84,84,82\nAD 84,84,83\nCV 67,82,83\nEX 80,44\nEX 81,45\nAD 85,85,80\nAD 85,85,81\nCV 70,80,81\nEX 82,46\nEX 83,47\nAD 85,85,82\nAD 85,85,83\n"
"CV 71,82,83\nv_mov_b32 v80, v90\nv_mov_b32 v81, v91\ns_bitcmp1_b32 s62, 1\ns_cbranch_scc1 .R108\nSU 48,48,22\nSU 49,49,22\nSU 50,50,22\nSU 51,51,22\nSU 52,52,23\nSU 53,53,23\n"
"SU 54,54,23\nSU 55,55,23\nSU 56,56,22\nSU 57,57,22\nSU 58,58,22\nSU 59,59,22\nSU 60,60,23\nSU 61,61,23\nSU 62,62,23\nSU 63,63,23\nSU 4,4,22\nSU 5,5,22\nSU 6,6,22\nSU 7,7,22\n"
"SU 8,8,23\nSU 9,9,23\nSU 10,10,23\nSU 11,11,23\nSU 12,12,22\nSU 13,13,22\nSU 14,14,22\nSU 15,15,22\nSU 16,16,23\nSU 17,17,23\nSU 18,18,23\nSU 19,19,23\nEX 80,48\nEX 81,49\n.R108:\n"
"s_bitcmp1_b32 s62, 0\ns_cbranch_scc0 .RS12\ns_nop 1\ns_setpc_b64 s[80:81]\n.RB11:\ns_nop 7\ns_nop 3\nv_max3_f32 v22, v48, v49, v50\nv_max3_f32 v22, v22, v51, v56\n"
"v_max3_f32 v22, v22, v57, v58\nv_max_f32 v22, v22, v59\nv_mov_b32 v91, v22\ns_nop 1\nv_permlane32_swap_b32 v22, v91\nv_max_f32 v22, v22, v91\nv_mov_b32 v91, v22\ns_nop 1\n"
"v_permlane16_swap_b32 v22, v91\nv_max_f32 v22, v22, v91\nv_ceil_f32 v22, v22\nv_max3_f32 v23, v52, v53, v54\nv_max3_f32 v23, v23, v55, v60\nv_max3_f32 v23, v23, v61, v62\n"
"v_max_f32 v23, v23, v63\nv_mov_b32 v91, v23\ns_nop 1\nv_permlane32_swap_b32 v23, v91\nv_max_f32 v23, v23, v91\nv_mov_b32 v91, v23\ns_nop 1\nv_permlane16_swap_b32 v23, v91\n"
"v_max_f32 v23, v23, v91\nv_ceil_f32 v23, v23\ns_bitcmp1_b32 s62, 0\ns_cbranch_scc0 .R111\nv_cmp_lt_f32 vcc, 0xff000000, v22\nv_cndmask_b32 v22, 0, v22, vcc\n"
"v_cmp_lt_f32 vcc, 0xff000000, v23\nv_cndmask_b32 v23, 0, v23, vcc\ns_branch .R112\n.R111:\nv_max_f32 v22, 0, v22\nv_max_f32 v23, 0, v23\ns_bitcmp1_b32 s62, 3\n"
"s_cbranch_scc0 .R112\nv_max_f32 v22, v20, v22\nv_max_f32 v23, v21, v23\n.R112:\nAD 88,88,22\nAD 89,89,23\nSU 48,48,22\nSU 49,49,22\nSU 50,50,22\nSU 51,51,22\nSU 52,52,23\n"
"SU 53,53,23\nSU 54,54,23\nSU 55,55,23\nSU 56,56,22\nSU 57,57,22\nSU 58,58,22\nSU 59,59,22\nSU 60,60,23\nSU 61,61,23\nSU 62,62,23\nSU 63,63,23\nv_mov_b32 v90, v80\n"
"v_mov_b32 v91, v81\nEX 80,48\nEX 81,49\ns_nop 0\nAD 84,80,81\nCV 72,80,81\nEX 82,50\nEX 83,51\nAD 84,84,82\nAD 84,84,83\nCV 73,82,83\nEX 80,52\nEX 81,53\ns_nop 0\nAD 85,80,81\n"
"CV 76,80,81\nEX 82,54\nEX 83,55\nAD 85,85,82\nAD 85,85,83\nCV 77,82,83\nEX 80,56\nEX 81,57\nAD 84,84,80\nAD 84,84,81\nCV 74,80,81\nEX 82,58\nEX 83,59\nAD 84,84,82\nAD 84,84,83\n"
"CV 75,82,83\nEX 80,60\nEX 81,61\nAD 85,85,80\nAD 85,85,81\nCV 78,80,81\nEX 82,62\nEX 83,63\nAD 85,85,82\nAD 85,85,83\nCV 79,82,83\nv_mov_b32 v80, v90\nv_mov_b32 v81, v91\n"
"s_bitcmp1_b32 s62, 1\ns_cbranch_scc1 .R113\nSU 32,32,22\nSU 33,33,22\nSU 34,34,22\nSU 35,35,22\nSU 36,36,23\nSU 37,37,23\nSU 38,38,23\nSU 39,39,23\nSU 40,40,22\nSU 41,41,22\n"
"SU 42,42,22\nSU 43,43,22\nSU 44,44,23\nSU 45,45,23\nSU 46,46,23\nSU 47,47,23\nSU 4,4,22\nSU 5,5,22\nSU 6,6,22\nSU 7,7,22\nSU 8,8,23\nSU 9,9,23\nSU 10,10,23\nSU 11,11,23\n"
"SU 12,12,22\nSU 13,13,22\nSU 14,14,22\nSU 15,15,22\nSU 16,16,23\nSU 17,17,23\nSU 18,18,23\nSU 19,19,23\nEX 80,32\nEX 81,33\n.R113:\ns_bitcmp1_b32 s62, 0\ns_cbranch_scc0 .RS12\n"
"s_nop 1\ns_setpc_b64 s[80:81]\n.RS12:\nv_exp_f32 v22, -v22\nv_exp_f32 v23, -v23\nMU 86,86,22\nMU 87,87,23\nAR 91,0\nMU 91,91,22\nAW 0,91\nAR 91,1\nMU 91,91,22\nAW 1,91\nAR 91,2\n"
"MU 91,91,22\nAW 2,91\nAR 91,3\nMU 91,91,22\nAW 3,91\nAR 91,4\nMU 91,91,23\nAW 4,91\nAR 91,5\nMU 91,91,23\nAW 5,91\nAR 91,6\nMU 91,91,23\nAW 6,91\nAR 91,7\nMU 91,91,23\nAW 7,91\n"
"AR 91,8\nMU 91,91,22\nAW 8,91\nAR 91,9\nMU 91,91,22\nAW 9,91\nAR 91,10\nMU 91,91,22\nAW 10,91\nAR 91,11\nMU 91,91,22\nAW 11,91\nAR 91,12\nMU 91,91,23\nAW 12,91\nAR 91,13\n"
"MU 91,91,23\nAW 13,91\nAR 91,14\nMU 91,91,23\nAW 14,91\nAR 91,15\nMU 91,91,23\nAW 15,91\nAR 91,16\nMU 91,91,22\nAW 16,91\nAR 91,17\nMU 91,91,22\nAW 17,91\nAR 91,18\nMU 91,91,22\n"
"AW 18,91\nAR 91,19\nMU 91,91,22\nAW 19,91\nAR 91,20\nMU 91,91,23\nAW 20,91\nAR 91,21\nMU 91,91,23\nAW 21,91\nAR 91,22\nMU 91,91,23\nAW 22,91\nAR 91,23\nMU 91,91,23\nAW 23,91\n"
"AR 91,24\nMU 91,91,22\nAW 24,91\nAR 91,25\nMU 91,91,22\nAW 25,91\nAR 91,26\nMU 91,91,22\nAW 26,91\nAR 91,27\nMU 91,91,22\nAW 27,91\nAR 91,28\nMU 91,91,23\nAW 28,91\nAR 91,29\n"
"MU 91,91,23\nAW 29,91\nAR 91,30\nMU 91,91,23\nAW 30,91\nAR 91,31\nMU 91,91,23\nAW 31,91\nAR 91,32\nMU 91,91,22\nAW 32,91\nAR 91,33\nMU 91,91,22\nAW 33,91\nAR 91,34\nMU 91,91,22\n"
"AW 34,91\nAR 91,35\nMU 91,91,22\nAW 35,91\nAR 91,36\nMU 91,91,23\nAW 36,91\nAR 91,37\nMU 91,91,23\nAW 37,91\nAR 91,38\nMU 91,91,23\nAW 38,91\nAR 91,39\nMU 91,91,23\nAW 39,91\n"
"AR 91,40\nMU 91,91,22\nAW 40,91\nAR 91,41\nMU 91,91,22\nAW 41,91\nAR 91,42\nMU 91,91,22\nAW 42,91\nAR 91,43\nMU 91,91,22\nAW 43,91\nAR 91,44\nMU 91,91,23\nAW 44,91\nAR 91,45\n"
"MU 91,91,23\nAW 45,91\nAR 91,46\nMU 91,91,23\nAW 46,91\nAR 91,47\nMU 91,91,23\nAW 47,91\nAR 91,48\nMU 91,91,22\nAW 48,91\nAR 91,49\nMU 91,91,22\nAW 49,91\nAR 91,50\nMU 91,91,22\n"
"AW 50,91\nAR 91,51\nMU 91,91,22\nAW 51,91\nAR 91,52\nMU 91,91,23\nAW 52,91\nAR 91,53\nMU 91,91,23\nAW 53,91\nAR 91,54\nMU 91,91,23\nAW 54,91\nAR 91,55\nMU 91,91,23\nAW 55,91\n"
"AR 91,56\nMU 91,91,22\nAW 56,91\nAR 91,57\nMU 91,91,22\nAW 57,91\nAR 91,58\nMU 91,91,22\nAW 58,91\nAR 91,59\nMU 91,91,22\nAW 59,91\nAR 91,60\nMU 91,91,23\nAW 60,91\nAR 91,61\n"
"MU 91,91,23\nAW 61,91\nAR 91,62\nMU 91,91,23\nAW 62,91\nAR 91,63\nMU 91,91,23\nAW 63,91\nAR 91,64\nMU 91,91,22\nAW 64,91\nAR 91,65\nMU 91,91,22\nAW 65,91\nAR 91,66\nMU 91,91,22\n"
"AW 66,91\nAR 91,67\nMU 91,91,22\nAW 67,91\nAR 91,68\nMU 91,91,23\nAW 68,91\nAR 91,69\nMU 91,91,23\nAW 69,91\nAR 91,70\nMU 91,91,23\nAW 70,91\nAR 91,71\nMU 91,91,23\nAW 71,91\n"
"AR 91,72\nMU 91,91,22\nAW 72,91\nAR 91,73\nMU 91,91,22\nAW 73,91\nAR 91,74\nMU 91,91,22\nAW 74,91\nAR 91,75\nMU 91,91,22\nAW 75,91\nAR 91,76\nMU 91,91,23\nAW 76,91\nAR 91,77\n"
"MU 91,91,23\nAW 77,91\nAR 91,78\nMU 91,91,23\nAW 78,91\nAR 91,79\nMU 91,91,23\nAW 79,91\nAR 91,80\nMU 91,91,22\nAW 80,91\nAR 91,81\nMU 91,91,22\nAW 81,91\nAR 91,82\nMU 91,91,22\n"
"AW 82,91\nAR 91,83\nMU 91,91,22\nAW 83,91\nAR 91,84\nMU 91,91,23\nAW 84,91\nAR 91,85\nMU 91,91,23\nAW 85,91\nAR 91,86\nMU 91,91,23\nAW 86,91\nAR 91,87\nMU 91,91,23\nAW 87,91\n"
"AR 91,88\nMU 91,91,22\nAW 88,91\nAR 91,89\nMU 91,91,22\nAW 89,91\nAR 91,90\nMU 91,91,22\nAW 90,91\nAR 91,91\nMU 91,91,22\nAW 91,91\nAR 91,92\nMU 91,91,23\nAW 92,91\nAR 91,93\n"
"MU 91,91,23\nAW 93,91\nAR 91,94\nMU 91,91,23\nAW 94,91\nAR 91,95\nMU 91,91,23\nAW 95,91\nAR 91,96\nMU 91,91,22\nAW 96,91\nAR 91,97\nMU 91,91,22\nAW 97,91\nAR 91,98\nMU 91,91,22\n"
"AW 98,91\nAR 91,99\nMU 91,91,22\nAW 99,91\nAR 91,100\nMU 91,91,23\nAW 100,91\nAR 91,101\nMU 91,91,23\nAW 101,91\nAR 91,102\nMU 91,91,23\nAW 102,91\nAR 91,103\nMU 91,91,23\n"
"AW 103,91\nAR 91,104\nMU 91,91,22\nAW 104,91\nAR 91,105\nMU 91,91,22\nAW 105,91\nAR 91,106\nMU 91,91,22\nAW 106,91\nAR 91,107\nMU 91,91,22\nAW 107,91\nAR 91,108\nMU 91,91,23\n"
"AW 108,91\nAR 91,109\nMU 91,91,23\nAW 109,91\nAR 91,110\nMU 91,91,23\nAW 110,91\nAR 91,111\nMU 91,91,23\nAW 111,91\nAR 91,112\nMU 91,91,22\nAW 112,91\nAR 91,113\nMU 91,91,22\n"
"AW 113,91\nAR 91,114\nMU 91,91,22\nAW 114,91\nAR 91,115\nMU 91,91,22\nAW 115,91\nAR 91,116\nMU 91,91,23\nAW 116,91\nAR 91,117\nMU 91,91,23\nAW 117,91\nAR 91,118\nMU 91,91,23\n"
"AW 118,91\nAR 91,119\nMU 91,91,23\nAW 119,91\nAR 91,120\nMU 91,91,22\nAW 120,91\nAR 91,121\nMU 91,91,22\nAW 121,91\nAR 91,122\nMU 91,91,22\nAW 122,91\nAR 91,123\nMU 91,91,22\n"
"AW 123,91\nAR 91,124\nMU 91,91,23\nAW 124,91\nAR 91,125\nMU 91,91,23\nAW 125,91\nAR 91,126\nMU 91,91,23\nAW 126,91\nAR 91,127\nMU 91,91,23\nAW 127,91\ns_nop 1\n"
"s_setpc_b64 s[80:81]\n.p2align 6\n.PA3:\nWV 31\ns_barrier\nRa 160,140,0\nRa 164,140,2048\nRa 168,142,0\nRa 172,142,2048\nTa 128,128,0\nTa 130,128,2048\nTa 132,129,0\n"
"Ta 134,129,2048\nTa 136,130,0\nTa 138,130,2048\nTa 140,131,0\nTa 142,131,2048\nTa 144,132,0\nTa 146,132,2048\nTa 148,133,0\nTa 150,133,2048\nTa 152,134,0\nTa 154,134,2048\n"
"Ta 156,135,0\nTa 158,135,2048\nWL 0\nZvaa 64,128,160\nRa 224,141,0\ns_add_u32 m0, s47, -16384\ns_add_u32 s46, s52, 384\nZvaa 32,128,164\nRa 228,141,2048\nZvaa 192,128,168\n"
"Ra 232,143,0\nD0 136,28,46,0\nZvaa 224,128,172\nRa 236,143,2048\nZvaa 228,132,172\nTa 192,128,16384\nZvaa 196,132,168\nZvaa 36,132,164\nTa 194,128,18432\nZvaa 68,132,160\n"
"Ta 196,129,16384\nD0 137,28,46,1024\nZvaa 72,136,160\nTa 198,129,18432\nZvaa 40,136,164\nTa 200,130,16384\nZvaa 200,136,168\nTa 202,130,18432\nZvaa 232,136,172\nZvaa 236,140,172\n"
"Ta 204,131,16384\nD0 138,28,46,2048\nZvaa 204,140,168\nTa 206,131,18432\nZvaa 44,140,164\nTa 208,132,16384\nZvaa 76,140,160\nTa 210,132,18432\nZvaa 80,144,160\nTa 212,133,16384\n"
"Zvaa 48,144,164\nD0 139,28,46,3072\nZvaa 208,144,168\nTa 214,133,18432\nZvaa 240,144,172\nTa 216,134,16384\nZvaa 244,148,172\nTa 218,134,18432\nZvaa 212,148,168\nTa 220,135,16384\n"
"Zvaa 52,148,164\nTa 222,135,18432\nZvaa 84,148,160\nZvaa 88,152,160\nZvaa 56,152,164\nZvaa 216,152,168\nZvaa 248,152,172\nZvaa 252,156,172\nZvaa 220,156,168\nZvaa 60,156,164\n"
"Zvaa 92,156,160\ns_waitcnt vmcnt(23) lgkmcnt(0)\ns_barrier\nMvaa 64,192,224\nRa 160,140,16384\ns_add_u32 m0, s47, 65536\ns_add_u32 s46, s51, 1572864\nMvaa 32,192,228\n"
"Ra 164,140,18432\nMvaa 192,192,232\nRa 168,142,16384\nD0 6,32,46,0\nMvaa 224,192,236\nRa 172,142,18432\nWV 20\nMvaa 228,196,236\nTa 128,12,0\nMvaa 196,196,232\n"
"v_lshlrev_b32 v112, 16, v96\nMvaa 36,196,228\nTa 130,12,2048\nv_lshlrev_b32 v113, 16, v100\nMvaa 68,196,224\nTa 132,13,0\nD0 7,32,46,1024\nMvaa 72,200,224\nTa 134,13,2048\n"
"v_lshlrev_b32 v114, 16, v104\nMvaa 40,200,228\nTa 136,14,0\nMvaa 200,200,232\nTa 138,14,2048\nMvaa 232,200,236\nv_lshlrev_b32 v115, 16, v108\nMU 116,112,114\nMvaa 236,204,236\n"
"Ta 140,15,0\nD0 8,32,46,2048\nMvaa 204,204,232\nTa 142,15,2048\nMvaa 44,204,228\nTa 144,16,0\nMU 117,113,114\nMvaa 76,204,224\nTa 146,16,2048\nMvaa 80,208,224\nTa 148,17,0\n"
"CV 116,116,117\nMvaa 48,208,228\nD0 9,32,46,3072\nMvaa 208,208,232\nTa 150,17,2048\nMvaa 240,208,236\nTa 152,18,0\nv_lshlrev_b32 v118, 16, v116\nMvaa 244,212,236\nTa 154,18,2048\n"
"s_add_u32 m0, s47, 81920\ns_add_u32 s46, s51, 1835008\nMvaa 212,212,232\nTa 156,19,0\nMvaa 52,212,228\nTa 158,19,2048\nD0 6,32,46,0\nMvaa 84,212,224\n"
"v_and_b32 v119, 0xffff0000, v116\nMvaa 88,216,224\nMU 120,112,115\nMU 121,113,115\nMvaa 56,216,228\nv_and_b32 v112, 0xffff0000, v96\nMvaa 216,216,232\n"
"v_and_b32 v113, 0xffff0000, v100\nMvaa 248,216,236\nD0 7,32,46,1024\nv_and_b32 v114, 0xffff0000, v104\nMvaa 252,220,236\nv_and_b32 v115, 0xffff0000, v108\nMvaa 220,220,232\n"
"MU 116,112,114\nMvaa 60,220,228\nMU 117,113,114\nCV 116,116,117\nMvaa 92,220,224\nWL 0\nMvaa 64,128,160\nRa 224,141,16384\nD0 8,32,46,2048\nMvaa 32,128,164\nRa 228,141,18432\n"
"Mvaa 192,128,168\nRa 232,143,16384\nMvaa 224,128,172\nRa 236,143,18432\nv_and_b32 v117, 0xffff0000, v116\nMvaa 228,132,172\nTa 192,12,16384\nMvaa 196,132,168\nD0 9,32,46,3072\n"
"Mvaa 36,132,164\nTa 194,12,18432\nv_lshlrev_b32 v116, 16, v116\nMvaa 68,132,160\nTa 196,13,16384\nMvaa 72,136,160\nTa 198,13,18432\ns_add_u32 m0, s47, -65536\n"
"s_add_u32 s46, s52, 512\nMvaa 40,136,164\nTa 200,14,16384\nMvaa 200,136,168\nTa 202,14,18432\nD0 136,28,46,0\nMvaa 232,136,172\nFM 120,112,115\nFM 121,113,115\nMvaa 236,140,172\n"
"Ta 204,15,16384\nMvaa 204,140,168\nTa 206,15,18432\nMvaa 44,140,164\nTa 208,16,16384\nAD 118,118,116\nMvaa 76,140,160\nTa 210,16,18432\nD0 137,28,46,1024\nMvaa 80,144,160\n"
"Ta 212,17,16384\nAD 119,119,117\nMvaa 48,144,164\nv_lshlrev_b32 v112, 16, v97\nMvaa 208,144,168\nTa 214,17,18432\nMvaa 240,144,172\nTa 216,18,16384\nv_lshlrev_b32 v113, 16, v101\n"
"Mvaa 244,148,172\nTa 218,18,18432\nD0 138,28,46,2048\nMvaa 212,148,168\nTa 220,19,16384\nMvaa 52,148,164\nTa 222,19,18432\nv_lshlrev_b32 v114, 16, v105\nMvaa 84,148,160\n"
"v_lshlrev_b32 v115, 16, v109\nMvaa 88,152,160\nMU 116,112,114\nMU 117,113,114\nMvaa 56,152,164\nD0 139,28,46,3072\nMvaa 216,152,168\nCV 116,116,117\nMvaa 248,152,172\n"
"v_and_b32 v117, 0xffff0000, v116\nv_lshlrev_b32 v116, 16, v116\nMvaa 252,156,172\nFM 120,112,115\nMvaa 220,156,168\nFM 121,113,115\nMvaa 60,156,164\nAD 118,118,116\n"
"AD 119,119,117\nMvaa 92,156,160\ns_waitcnt vmcnt(18) lgkmcnt(0)\ns_barrier\nMvaa 64,192,224\nRa 160,140,32768\ns_add_u32 m0, s47, 0\ns_add_u32 s46, s51, 2097152\nMvaa 32,192,228\n"
"Ra 164,140,34816\nMvaa 192,192,232\nRa 168,142,32768\nD0 6,32,46,0\nMvaa 224,192,236\nRa 172,142,34816\nv_and_b32 v112, 0xffff0000, v97\nMvaa 228,196,236\nTa 128,12,32768\n"
"Mvaa 196,196,232\nv_and_b32 v113, 0xffff0000, v101\nMvaa 36,196,228\nTa 130,12,34816\nv_and_b32 v114, 0xffff0000, v105\nMvaa 68,196,224\nTa 132,13,32768\nD0 7,32,46,1024\n"
"Mvaa 72,200,224\nTa 134,13,34816\nv_and_b32 v115, 0xffff0000, v109\nMvaa 40,200,228\nTa 136,14,32768\nMvaa 200,200,232\nTa 138,14,34816\nMvaa 232,200,236\nMU 116,112,114\n"
"MU 117,113,114\nMvaa 236,204,236\nTa 140,15,32768\nD0 8,32,46,2048\nMvaa 204,204,232\nTa 142,15,34816\nMvaa 44,204,228\nTa 144,16,32768\nCV 116,116,117\nMvaa 76,204,224\n"
"Ta 146,16,34816\nMvaa 80,208,224\nTa 148,17,32768\nv_and_b32 v117, 0xffff0000, v116\nMvaa 48,208,228\nD0 9,32,46,3072\nMvaa 208,208,232\nTa 150,17,34816\nMvaa 240,208,236\n"
"Ta 152,18,32768\nv_lshlrev_b32 v116, 16, v116\nMvaa 244,212,236\nTa 154,18,34816\ns_add_u32 m0, s47, 16384\ns_add_u32 s46, s51, 2359296\nMvaa 212,212,232\nTa 156,19,32768\n"
"Mvaa 52,212,228\nTa 158,19,34816\nD0 6,32,46,0\nMvaa 84,212,224\nFM 120,112,115\nMvaa 88,216,224\nFM 121,113,115\nAD 118,118,116\nMvaa 56,216,228\nAD 119,119,117\n"
"Mvaa 216,216,232\nv_lshlrev_b32 v112, 16, v98\nMvaa 248,216,236\nD0 7,32,46,1024\nv_lshlrev_b32 v113, 16, v102\nMvaa 252,220,236\nv_lshlrev_b32 v114, 16, v106\nMvaa 220,220,232\n"
"v_lshlrev_b32 v115, 16, v110\nMvaa 60,220,228\nMU 116,112,114\nMU 117,113,114\nMvaa 92,220,224\nWL 0\nMvaa 64,128,160\nRa 224,141,32768\nD0 8,32,46,2048\nMvaa 32,128,164\n"
"Ra 228,141,34816\nMvaa 192,128,168\nRa 232,143,32768\nMvaa 224,128,172\nRa 236,143,34816\nCV 116,116,117\nMvaa 228,132,172\nTa 192,12,49152\nMvaa 196,132,168\nD0 9,32,46,3072\n"
"Mvaa 36,132,164\nTa 194,12,51200\nv_and_b32 v117, 0xffff0000, v116\nMvaa 68,132,160\nTa 196,13,49152\nMvaa 72,136,160\nTa 198,13,51200\nv_lshlrev_b32 v116, 16, v116\n"
"Mvaa 40,136,164\nTa 200,14,49152\nMvaa 200,136,168\nTa 202,14,51200\nMvaa 232,136,172\nFM 120,112,115\nFM 121,113,115\nMvaa 236,140,172\nTa 204,15,49152\nMvaa 204,140,168\n"
"Ta 206,15,51200\nMvaa 44,140,164\nTa 208,16,49152\nAD 118,118,116\nMvaa 76,140,160\nTa 210,16,51200\nMvaa 80,144,160\nTa 212,17,49152\nAD 119,119,117\nMvaa 48,144,164\n"
"v_and_b32 v112, 0xffff0000, v98\nMvaa 208,144,168\nTa 214,17,51200\nMvaa 240,144,172\nTa 216,18,49152\nv_and_b32 v113, 0xffff0000, v102\nMvaa 244,148,172\nTa 218,18,51200\n"
"Mvaa 212,148,168\nTa 220,19,49152\nMvaa 52,148,164\nTa 222,19,51200\nv_and_b32 v114, 0xffff0000, v106\nMvaa 84,148,160\nv_and_b32 v115, 0xffff0000, v110\nMvaa 88,152,160\n"
"MU 116,112,114\nMU 117,113,114\nMvaa 56,152,164\nCV 116,116,117\nMvaa 216,152,168\nv_and_b32 v117, 0xffff0000, v116\nMvaa 248,152,172\nv_lshlrev_b32 v116, 16, v116\n"
"FM 120,112,115\nMvaa 252,156,172\nFM 121,113,115\nMvaa 220,156,168\nAD 118,118,116\nMvaa 60,156,164\nAD 119,119,117\nv_lshlrev_b32 v112, 16, v99\nMvaa 92,156,160\n"
"s_waitcnt vmcnt(12) lgkmcnt(0)\ns_barrier\nMvaa 64,192,224\nRa 160,140,49152\ns_add_u32 m0, s47, 32768\ns_add_u32 s46, s51, 2621440\nMvaa 32,192,228\nRa 164,140,51200\n"
"Mvaa 192,192,232\nRa 168,142,49152\nD0 6,32,46,0\nMvaa 224,192,236\nRa 172,142,51200\nv_lshlrev_b32 v113, 16, v103\nMvaa 228,196,236\nTa 128,128,0\nMvaa 196,196,232\n"
"v_lshlrev_b32 v114, 16, v107\nMvaa 36,196,228\nTa 130,128,2048\nv_lshlrev_b32 v115, 16, v111\nMvaa 68,196,224\nTa 132,129,0\nD0 7,32,46,1024\nMvaa 72,200,224\nTa 134,129,2048\n"
"MU 116,112,114\nMvaa 40,200,228\nTa 136,130,0\nMvaa 200,200,232\nTa 138,130,2048\nMvaa 232,200,236\nMU 117,113,114\nCV 116,116,117\nMvaa 236,204,236\nTa 140,131,0\n"
"D0 8,32,46,2048\nMvaa 204,204,232\nTa 142,131,2048\nMvaa 44,204,228\nTa 144,132,0\nv_and_b32 v117, 0xffff0000, v116\nMvaa 76,204,224\nTa 146,132,2048\nMvaa 80,208,224\n"
"Ta 148,133,0\nv_lshlrev_b32 v116, 16, v116\nMvaa 48,208,228\nD0 9,32,46,3072\nMvaa 208,208,232\nTa 150,133,2048\nMvaa 240,208,236\nTa 152,134,0\nFM 120,112,115\nMvaa 244,212,236\n"
"Ta 154,134,2048\ns_add_u32 m0, s47, -32768\ns_add_u32 s46, s52, 640\nMvaa 212,212,232\nTa 156,135,0\nMvaa 52,212,228\nTa 158,135,2048\nD0 136,28,46,0\nMvaa 84,212,224\n"
"FM 121,113,115\nMvaa 88,216,224\nAD 118,118,116\nAD 119,119,117\nMvaa 56,216,228\nv_and_b32 v112, 0xffff0000, v99\nMvaa 216,216,232\nv_and_b32 v113, 0xffff0000, v103\n"
"Mvaa 248,216,236\nD0 137,28,46,1024\nv_and_b32 v114, 0xffff0000, v107\nMvaa 252,220,236\nv_and_b32 v115, 0xffff0000, v111\nMvaa 220,220,232\nMU 116,112,114\nMvaa 60,220,228\n"
"MU 117,113,114\nCV 116,116,117\nMvaa 92,220,224\nWL 0\nMvaa 64,128,160\nRa 224,141,49152\nD0 138,28,46,2048\nMvaa 32,128,164\nRa 228,141,51200\nMvaa 192,128,168\nRa 232,143,49152\n"
"Mvaa 224,128,172\nRa 236,143,51200\nv_and_b32 v117, 0xffff0000, v116\nMvaa 228,132,172\nTa 192,128,16384\nMvaa 196,132,168\nD0 139,28,46,3072\nMvaa 36,132,164\nTa 194,128,18432\n"
"v_lshlrev_b32 v116, 16, v116\nMvaa 68,132,160\nTa 196,129,16384\nMvaa 72,136,160\nTa 198,129,18432\ns_add_u32 m0, s47, 49152\ns_add_u32 s46, s51, 2883584\nMvaa 40,136,164\n"
"Ta 200,130,16384\nMvaa 200,136,168\nTa 202,130,18432\nD0 6,32,46,0\nMvaa 232,136,172\nFM 120,112,115\nFM 121,113,115\nMvaa 236,140,172\nTa 204,131,16384\nMvaa 204,140,168\n"
"Ta 206,131,18432\nMvaa 44,140,164\nTa 208,132,16384\nAD 118,118,116\nMvaa 76,140,160\nTa 210,132,18432\nD0 7,32,46,1024\nMvaa 80,144,160\nTa 212,133,16384\nAD 119,119,117\n"
"Mvaa 48,144,164\ns_nop 0\nv_permlane32_swap_b32 v118, v119\nMvaa 208,144,168\nTa 214,133,18432\nMvaa 240,144,172\nTa 216,134,16384\nv_permlane32_swap_b32 v120, v121\n"
"Mvaa 244,148,172\nTa 218,134,18432\nD0 8,32,46,2048\nMvaa 212,148,168\nTa 220,135,16384\nMvaa 52,148,164\nTa 222,135,18432\nAD 118,118,119\nMvaa 84,148,160\nAD 120,120,121\n"
"Mvaa 88,152,160\nMvaa 56,152,164\nD0 9,32,46,3072\nMvaa 216,152,168\nv_permlane16_swap_b32 v118, v120\nMvaa 248,152,172\nAD 118,118,120\nMvaa 252,156,172\ns_and_b32 s2, s74, 3\n"
"s_lshl2_add_u32 s2, s73, s2\ns_lshl_b32 s2, s2, 12\ns_add_u32 s3, s45, 512\ns_and_b32 s3, s3, 0xfffe0000\ns_add_u32 s2, s2, s3\nbuffer_load_dword v31, v26, s[20:23], s2 offen\n"
"Mvaa 220,156,168\nv_add_f32_dpp v118, v118, v118 row_ror:8 row_mask:0xf bank_mask:0xf\nMvaa 60,156,164\nMvaa 92,156,160\ns_waitcnt vmcnt(13) lgkmcnt(0)\ns_barrier\n"
"Mvaa 64,192,224\nRa 160,140,0\ns_add_u32 m0, s47, 65536\ns_add_u32 s46, s51, 3145728\nMvaa 32,192,228\nRa 164,140,2048\nMvaa 192,192,232\nRa 168,142,0\nD0 6,32,46,0\n"
"Mvaa 224,192,236\nRa 172,142,2048\nv_add_f32_dpp v118, v118, v118 row_ror:4 row_mask:0xf bank_mask:0xf\nMvaa 228,196,236\nTa 128,12,0\nMvaa 196,196,232\n"
"v_add_f32_dpp v118, v118, v118 row_ror:2 row_mask:0xf bank_mask:0xf\nMvaa 36,196,228\nTa 130,12,2048\nMvaa 68,196,224\nTa 132,13,0\nD0 7,32,46,1024\nMvaa 72,200,224\n"
"Ta 134,13,2048\nv_add_f32_dpp v118, v118, v118 row_ror:1 row_mask:0xf bank_mask:0xf\nMvaa 40,200,228\nTa 136,14,0\nMvaa 200,200,232\nTa 138,14,2048\nMvaa 232,200,236\n"
"CV 118,118,118\nv_lshlrev_b32 v118, 16, v118\nMvaa 236,204,236\nTa 140,15,0\nD0 8,32,46,2048\nMvaa 204,204,232\nTa 142,15,2048\nMvaa 44,204,228\nTa 144,16,0\n"
"v_mul_f32 v119, 0xbfb8aa3b, v118\nMvaa 76,204,224\nTa 146,16,2048\nEX 119,119\nMvaa 80,208,224\nTa 148,17,0\nv_add_f32 v120, 0, v118\nMvaa 48,208,228\nD0 9,32,46,3072\n"
"Mvaa 208,208,232\nTa 150,17,2048\nMvaa 240,208,236\nTa 152,18,0\nv_add_f32 v119, 1.0, v119\nMvaa 244,212,236\nTa 154,18,2048\ns_add_u32 m0, s47, -16384\ns_add_u32 s46, s52, 768\n"
"Mvaa 212,212,232\nTa 156,19,0\nv_rcp_f32 v119, v119\nMvaa 52,212,228\nTa 158,19,2048\nD0 136,28,46,0\nMvaa 84,212,224\nv_ashrrev_i32 v121, 31, v120\nMvaa 88,216,224\n"
"CV 119,119,119\nv_lshlrev_b32 v119, 16, v119\nMvaa 56,216,228\nv_or_b32 v121, 0x80000000, v121\nMvaa 216,216,232\nv_xor_b32 v120, v120, v121\nMvaa 248,216,236\nD0 137,28,46,1024\n"
"v_and_b32 v120, 0xffff0000, v120\nMvaa 252,220,236\ns_lshl2_add_u32 s2, s10, s11\nMvaa 220,220,232\nv_lshrrev_b32 v112, 5, v0\nMvaa 60,220,228\nv_lshl_add_u32 v112, s2, 1, v112\n"
"v_sub_u32 v113, 0xffff, v112\nMvaa 92,220,224\nWL 0\nMvaa 64,128,160\nRa 224,141,0\nD0 138,28,46,2048\nMvaa 32,128,164\nRa 228,141,2048\nMvaa 192,128,168\nRa 232,143,0\n"
"Mvaa 224,128,172\nRa 236,143,2048\nv_or_b32 v120, v120, v113\nMvaa 228,132,172\nTa 192,12,16384\nMvaa 196,132,168\nD0 139,28,46,3072\nMvaa 36,132,164\nTa 194,12,18432\n"
"v_mov_b32 v121, s15\nMvaa 68,132,160\nTa 196,13,16384\nMvaa 72,136,160\nTa 198,13,18432\ns_add_u32 m0, s47, 81920\ns_add_u32 s46, s51, 3407872\nMvaa 40,136,164\nTa 200,14,16384\n"
"Mvaa 200,136,168\nTa 202,14,18432\nD0 6,32,46,0\nMvaa 232,136,172\nv_and_b32 v113, 31, v0\nv_mov_b32 v116, 0x40000000\nMvaa 236,140,172\nTa 204,15,16384\n"
"v_cmp_eq_u32 vcc, 16, v113\nv_lshlrev_b32 v114, 3, v112\nv_lshlrev_b32 v115, 9, v113\nv_cndmask_b32 v114, v116, v114, vcc\nv_cndmask_b32 v119, v119, v118, vcc\nMvaa 204,140,168\n"
"Ta 206,15,18432\nMvaa 44,140,164\nTa 208,16,16384\nv_lshl_add_u32 v115, v112, 2, v115\nMvaa 76,140,160\nTa 210,16,18432\nD0 7,32,46,1024\nMvaa 80,144,160\nTa 212,17,16384\n"
"v_and_b32 v113, 15, v0\nMvaa 48,144,164\nv_cmp_eq_u32 vcc, 0, v113\ns_mov_b32 s2, 0xc040000\ns_mov_b32 s3, 0xc044000\nv_cndmask_b32 v115, v116, v115, vcc\n"
"buffer_store_dwordx2 v[120:121], v114, s[16:19], s3 offen sc0 sc1\nbuffer_store_dword v119, v115, s[16:19], s2 offen sc0 sc1\nMvaa 208,144,168\nTa 214,17,18432\nMvaa 240,144,172\n"
"Ta 216,18,16384\nMvaa 244,148,172\nTa 218,18,18432\nD0 8,32,46,2048\nMvaa 212,148,168\nTa 220,19,16384\nMvaa 52,148,164\nTa 222,19,18432\nMvaa 84,148,160\nMvaa 88,152,160\n"
"Mvaa 56,152,164\nD0 9,32,46,3072\nMvaa 216,152,168\nMvaa 248,152,172\nMvaa 252,156,172\nMvaa 220,156,168\nMvaa 60,156,164\nMvaa 92,156,160\ns_waitcnt vmcnt(15) lgkmcnt(0)\n"
"s_barrier\nMvaa 64,192,224\nRa 160,140,32768\ns_add_u32 m0, s47, 0\ns_add_u32 s44, s44, 0x40000\nMvaa 32,192,228\nRa 164,140,34816\nMvaa 192,192,232\nRa 168,142,32768\n"
"D0 6,24,44,0\nMvaa 224,192,236\nRa 172,142,34816\nMvaa 228,196,236\nTa 128,12,32768\nMvaa 196,196,232\nMvaa 36,196,228\nTa 130,12,34816\nMvaa 68,196,224\nTa 132,13,32768\n"
"D0 7,24,44,1024\nMvaa 72,200,224\nTa 134,13,34816\nMvaa 40,200,228\nTa 136,14,32768\nMvaa 200,200,232\nTa 138,14,34816\nMvaa 232,200,236\nMvaa 236,204,236\nTa 140,15,32768\n"
"D0 8,24,44,2048\nMvaa 204,204,232\nTa 142,15,34816\nMvaa 44,204,228\nTa 144,16,32768\nMvaa 76,204,224\nTa 146,16,34816\nMvaa 80,208,224\nTa 148,17,32768\nMvaa 48,208,228\n"
"D0 9,24,44,3072\nMvaa 208,208,232\nTa 150,17,34816\nMvaa 240,208,236\nTa 152,18,32768\nMvaa 244,212,236\nTa 154,18,34816\ns_add_u32 m0, s48, 0\ns_add_u32 s45, s45, 128\n"
"Mvaa 212,212,232\nTa 156,19,32768\nMvaa 52,212,228\nTa 158,19,34816\nD0 2,20,45,0\nMvaa 84,212,224\nMvaa 88,216,224\nMvaa 56,216,228\nMvaa 216,216,232\nMvaa 248,216,236\n"
"D0 3,20,45,1024\nMvaa 252,220,236\nMvaa 220,220,232\nMvaa 60,220,228\nMvaa 92,220,224\nWL 0\nMvaa 64,128,160\nRa 224,141,32768\nD0 4,20,45,2048\nMvaa 32,128,164\nRa 228,141,34816\n"
"Mvaa 192,128,168\nRa 232,143,32768\nMvaa 224,128,172\nRa 236,143,34816\nMvaa 228,132,172\nTa 192,12,49152\nMvaa 196,132,168\nD0 5,20,45,3072\nMvaa 36,132,164\nTa 194,12,51200\n"
"Mvaa 68,132,160\nTa 196,13,49152\nMvaa 72,136,160\nTa 198,13,51200\ns_add_u32 m0, s48, 4096\ns_add_u32 s46, s45, 0x10000\nMvaa 40,136,164\nTa 200,14,49152\nMvaa 200,136,168\n"
"Ta 202,14,51200\nD0 2,20,46,0\nMvaa 232,136,172\nMvaa 236,140,172\nTa 204,15,49152\nMvaa 204,140,168\nTa 206,15,51200\nMvaa 44,140,164\nTa 208,16,49152\nMvaa 76,140,160\n"
"Ta 210,16,51200\nD0 3,20,46,1024\nMvaa 80,144,160\nTa 212,17,49152\nMvaa 48,144,164\nMvaa 208,144,168\nTa 214,17,51200\nMvaa 240,144,172\nTa 216,18,49152\nMvaa 244,148,172\n"
"Ta 218,18,51200\nD0 4,20,46,2048\nMvaa 212,148,168\nTa 220,19,49152\nMvaa 52,148,164\nTa 222,19,51200\nMvaa 84,148,160\nMvaa 88,152,160\nMvaa 56,152,164\nD0 5,20,46,3072\n"
"Mvaa 216,152,168\nMvaa 248,152,172\nMvaa 252,156,172\ns_add_u32 m0, s47, 16384\ns_add_u32 s44, s44, 0x40000\ns_mov_b32 s92, s62\ns_and_b32 s93, s63, 0xffff\n"
"s_mov_b32 s94, 0x188000\ns_mov_b32 s95, 0x20000\nbuffer_load_dword v31, v26, s[92:95], s97 offen\nMvaa 220,156,168\nMvaa 60,156,164\nD0 6,24,44,0\nMvaa 92,156,160\n"
"s_waitcnt vmcnt(14) lgkmcnt(0)\ns_barrier\nMvaa 64,192,224\nRa 160,140,49152\nMvaa 32,192,228\nRa 164,140,51200\nMvaa 192,192,232\nRa 168,142,49152\nMvaa 224,192,236\n"
"Ra 172,142,51200\nD0 7,24,44,1024\nMvaa 228,196,236\nTa 128,128,0\nMvaa 196,196,232\nMvaa 36,196,228\nTa 130,128,2048\nMvaa 68,196,224\nTa 132,129,0\nMvaa 72,200,224\n"
"Ta 134,129,2048\nD0 8,24,44,2048\nMvaa 40,200,228\nTa 136,130,0\nMvaa 200,200,232\nTa 138,130,2048\nMvaa 232,200,236\nMvaa 236,204,236\nTa 140,131,0\nMvaa 204,204,232\n"
"Ta 142,131,2048\nD0 9,24,44,3072\nMvaa 44,204,228\nTa 144,132,0\nMvaa 76,204,224\nTa 146,132,2048\nMvaa 80,208,224\nTa 148,133,0\ns_add_u32 m0, s47, 32768\n"
"s_add_u32 s44, s44, 0x40000\nMvaa 48,208,228\nMvaa 208,208,232\nTa 150,133,2048\nD0 6,24,44,0\nMvaa 240,208,236\nTa 152,134,0\nMvaa 244,212,236\nTa 154,134,2048\nMvaa 212,212,232\n"
"Ta 156,135,0\nMvaa 52,212,228\nTa 158,135,2048\nMvaa 84,212,224\nD0 7,24,44,1024\nMvaa 88,216,224\nMvaa 56,216,228\nMvaa 216,216,232\nMvaa 248,216,236\nMvaa 252,220,236\n"
"D0 8,24,44,2048\ns_mov_b32 s92, s64\ns_and_b32 s93, s65, 0xffff\ns_mov_b32 s94, 0x188000\ns_mov_b32 s95, 0x20000\nbuffer_load_dword v31, v26, s[92:95], s97 offen\n"
"Mvaa 220,220,232\nMvaa 60,220,228\nMvaa 92,220,224\nWL 0\nMvaa 64,128,160\nRa 224,141,49152\nMvaa 32,128,164\nRa 228,141,51200\nD0 9,24,44,3072\nMvaa 192,128,168\n"
"Ra 232,143,49152\nMvaa 224,128,172\nRa 236,143,51200\nMvaa 228,132,172\nTa 192,128,16384\nMvaa 196,132,168\nMvaa 36,132,164\nTa 194,128,18432\nMvaa 68,132,160\nTa 196,129,16384\n"
"Mvaa 72,136,160\nTa 198,129,18432\nMvaa 40,136,164\nTa 200,130,16384\nMvaa 200,136,168\nTa 202,130,18432\nMvaa 232,136,172\nMvaa 236,140,172\nTa 204,131,16384\nMvaa 204,140,168\n"
"Ta 206,131,18432\nMvaa 44,140,164\nTa 208,132,16384\nMvaa 76,140,160\nTa 210,132,18432\nMvaa 80,144,160\nTa 212,133,16384\nMvaa 48,144,164\nMvaa 208,144,168\nTa 214,133,18432\n"
"Mvaa 240,144,172\nTa 216,134,16384\nMvaa 244,148,172\nTa 218,134,18432\nMvaa 212,148,168\nTa 220,135,16384\nMvaa 52,148,164\nTa 222,135,18432\nMvaa 84,148,160\nMvaa 88,152,160\n"
"Mvaa 56,152,164\nMvaa 216,152,168\nMvaa 248,152,172\nMvaa 252,156,172\ns_mov_b32 s92, s66\ns_and_b32 s93, s67, 0xffff\ns_mov_b32 s94, 0x188000\ns_mov_b32 s95, 0x20000\n"
"buffer_load_dword v31, v26, s[92:95], s97 offen\nMvaa 220,156,168\nMvaa 60,156,164\nMvaa 92,156,160\ns_waitcnt vmcnt(11) lgkmcnt(0)\ns_barrier\nMvaa 64,192,224\n"
"s_add_u32 m0, s48, 32768\ns_add_u32 s45, s45, 128\nTa 128,12,0\nMvaa 68,196,224\nTa 130,12,2048\nMvaa 72,200,224\nD0 2,20,45,0\nTa 132,13,0\nMvaa 76,204,224\nTa 134,13,2048\n"
"Mvaa 80,208,224\nTa 136,14,0\nMvaa 84,212,224\nTa 138,14,2048\nMvaa 88,216,224\nTa 140,15,0\nv_mul_f32 v64, s57, v64\nMvaa 92,220,224\nD0 3,20,45,1024\nTa 142,15,2048\n"
"Mvaa 32,192,228\nTa 144,16,0\nv_mul_f32 v65, s57, v65\nMvaa 36,196,228\nTa 146,16,2048\nMvaa 40,200,228\nTa 148,17,0\nCV 64,64,65\nMvaa 44,204,228\nTa 150,17,2048\n"
"v_mul_f32 v66, s57, v66\nMvaa 48,208,228\nD0 4,20,45,2048\nTa 152,18,0\nMvaa 52,212,228\nTa 154,18,2048\nMvaa 56,216,228\nTa 156,19,0\nv_mul_f32 v67, s57, v67\nMvaa 60,220,228\n"
"Ta 158,19,2048\nCV 65,66,67\nMvaa 192,192,232\nRa 160,10,0\nv_mul_f32 v68, s57, v68\nMvaa 196,196,232\nD0 5,20,45,3072\nRa 164,10,2048\nMvaa 200,200,232\nRa 168,10,8192\n"
"v_mul_f32 v69, s57, v69\nMvaa 204,204,232\nRa 172,10,10240\nCV 66,68,69\nMvaa 208,208,232\nRa 176,10,16384\nv_mul_f32 v70, s57, v70\nMvaa 212,212,232\nRa 180,10,18432\n"
"Mvaa 216,216,232\nRa 184,10,24576\nv_mul_f32 v71, s57, v71\nMvaa 220,220,232\nRa 188,10,26624\nCV 67,70,71\nMvaa 224,192,236\nv_mul_f32 v72, s57, v72\nv_mul_f32 v73, s57, v73\n"
"Mvaa 228,196,236\nCV 72,72,73\nMvaa 232,200,236\nv_mul_f32 v74, s57, v74\nv_mul_f32 v75, s57, v75\nMvaa 236,204,236\nCV 73,74,75\nv_mul_f32 v76, s57, v76\nMvaa 240,208,236\n"
"v_mul_f32 v77, s57, v77\nCV 74,76,77\nMvaa 244,212,236\nv_mul_f32 v78, s57, v78\nMvaa 248,216,236\nv_mul_f32 v79, s57, v79\nCV 75,78,79\nMvaa 252,220,236\n"
"s_waitcnt vmcnt(10) lgkmcnt(0)\ns_barrier\nZaaa 0,176,128\nTa 192,12,16384\nv_mul_f32 v80, s57, v80\nZaaa 4,180,128\ns_add_u32 m0, s48, 36864\ns_add_u32 s46, s45, 0x10000\n"
"s_add_u32 s6, s56, 0\nSv 64,1,16,6,0\nZaaa 64,184,128\nTa 194,12,18432\nv_mul_f32 v81, s57, v81\nZaaa 68,188,128\nD0 2,20,46,0\nCV 80,80,81\nZaaa 76,188,132\nTa 196,13,16384\n"
"v_mul_f32 v82, s57, v82\nZaaa 72,184,132\nv_mul_f32 v83, s57, v83\nZaaa 12,180,132\nTa 198,13,18432\nCV 81,82,83\nZaaa 8,176,132\nD0 3,20,46,1024\nv_mul_f32 v84, s57, v84\n"
"Zaaa 16,176,136\nTa 200,14,16384\nv_mul_f32 v85, s57, v85\nZaaa 20,180,136\nCV 82,84,85\ns_add_u32 s6, s56, 0\nSv 72,1,16,6,2048\nZaaa 80,184,136\nTa 202,14,18432\n"
"v_mul_f32 v86, s57, v86\nZaaa 84,188,136\nD0 4,20,46,2048\nv_mul_f32 v87, s57, v87\nZaaa 92,188,140\nTa 204,15,16384\nCV 83,86,87\nZaaa 88,184,140\nv_mul_f32 v88, s57, v88\n"
"Zaaa 28,180,140\nTa 206,15,18432\nv_mul_f32 v89, s57, v89\nZaaa 24,176,140\nD0 5,20,46,3072\nCV 88,88,89\nZaaa 32,176,144\nTa 208,16,16384\nv_mul_f32 v90, s57, v90\n"
"Zaaa 36,180,144\nv_mul_f32 v91, s57, v91\nZaaa 96,184,144\nTa 210,16,18432\nCV 89,90,91\nZaaa 100,188,144\nv_mul_f32 v92, s57, v92\nv_mul_f32 v93, s57, v93\nZaaa 108,188,148\n"
"Ta 212,17,16384\nCV 90,92,93\nZaaa 104,184,148\nv_mul_f32 v94, s57, v94\nZaaa 44,180,148\nTa 214,17,18432\nv_mul_f32 v95, s57, v95\nZaaa 40,176,148\nCV 91,94,95\nZaaa 48,176,152\n"
"Ta 216,18,16384\nZaaa 52,180,152\ns_add_u32 s6, s56, 4096\nSv 80,1,16,6,0\nZaaa 112,184,152\nTa 218,18,18432\nZaaa 116,188,152\nZaaa 124,188,156\nTa 220,19,16384\n"
"Zaaa 120,184,156\ns_add_u32 s6, s56, 4096\nSv 88,1,16,6,2048\nZaaa 60,180,156\nTa 222,19,18432\nZaaa 56,176,156\ns_add_u32 m0, s47, 49152\ns_add_u32 s44, s44, 0x40000\n"
"Zvaa 64,160,128\nRa 224,11,0\nv_mul_f32 v32, s57, v32\nZvaa 68,164,128\nD0 6,24,44,0\nZvaa 128,168,128\nRa 228,11,2048\nZvaa 132,172,128\nv_mul_f32 v33, s57, v33\n"
"Zvaa 140,172,132\nRa 232,11,8192\nCV 32,32,33\nZvaa 136,168,132\nD0 7,24,44,1024\nZvaa 76,164,132\nRa 236,11,10240\nZvaa 72,160,132\nv_mul_f32 v34, s57, v34\nZvaa 80,160,136\n"
"Ra 240,11,16384\nv_mul_f32 v35, s57, v35\nZvaa 84,164,136\nD0 8,24,44,2048\nZvaa 144,168,136\nRa 244,11,18432\nZvaa 148,172,136\nCV 33,34,35\nZvaa 156,172,140\nRa 248,11,24576\n"
"v_mul_f32 v36, s57, v36\nZvaa 152,168,140\nD0 9,24,44,3072\nZvaa 92,164,140\nRa 252,11,26624\nZvaa 88,160,140\ns_add_u32 m0, s47, 0\ns_add_u32 s44, s44, 0x40000\nZvaa 96,160,144\n"
"v_mul_f32 v37, s57, v37\nCV 34,36,37\nZvaa 100,164,144\nD0 6,24,44,0\nZvaa 160,168,144\nv_mul_f32 v38, s57, v38\nZvaa 164,172,144\nv_mul_f32 v39, s57, v39\nZvaa 172,172,148\n"
"CV 35,38,39\nZvaa 168,168,148\nD0 7,24,44,1024\ns_add_u32 s6, s56, 0\nSv 32,1,16,6,1024\nZvaa 108,164,148\nv_mul_f32 v40, s57, v40\nZvaa 104,160,148\nv_mul_f32 v41, s57, v41\n"
"Zvaa 112,160,152\nCV 40,40,41\nv_mul_f32 v42, s57, v42\nZvaa 116,164,152\nD0 8,24,44,2048\nZvaa 176,168,152\nv_mul_f32 v43, s57, v43\nZvaa 180,172,152\nCV 41,42,43\n"
"Zvaa 188,172,156\nv_mul_f32 v44, s57, v44\nv_mul_f32 v45, s57, v45\nZvaa 184,168,156\nD0 9,24,44,3072\nZvaa 124,164,156\nCV 42,44,45\nZvaa 120,160,156\n"
"s_waitcnt vmcnt(11) lgkmcnt(0)\ns_barrier\nMvaa 64,224,192\nTa 128,12,32768\nv_mul_f32 v46, s57, v46\nMvaa 68,228,192\nv_mul_f32 v47, s57, v47\nMvaa 128,232,192\nTa 130,12,34816\n"
"Mvaa 132,236,192\ns_add_u32 m0, s48, 0\ns_add_u32 s45, s45, 128\nMaaa 0,240,192\nTa 132,13,32768\nCV 43,46,47\nMaaa 4,244,192\nD0 2,20,45,0\ns_add_u32 s6, s56, 0\n"
"Sv 40,1,16,6,3072\nMaaa 64,248,192\nTa 134,13,34816\nMaaa 68,252,192\nv_mul_f32 v48, s57, v48\nMaaa 76,252,196\nTa 136,14,32768\nv_mul_f32 v49, s57, v49\nMaaa 72,248,196\n"
"CV 48,48,49\nMaaa 12,244,196\nTa 138,14,34816\nMaaa 8,240,196\nv_mul_f32 v50, s57, v50\nMvaa 140,236,196\nTa 140,15,32768\nv_mul_f32 v51, s57, v51\nMvaa 136,232,196\n"
"D0 3,20,45,1024\nMvaa 76,228,196\nTa 142,15,34816\nMvaa 72,224,196\nCV 49,50,51\nMvaa 80,224,200\nTa 144,16,32768\nv_mul_f32 v52, s57, v52\nMvaa 84,228,200\n"
"v_mul_f32 v53, s57, v53\nMvaa 144,232,200\nTa 146,16,34816\nMvaa 148,236,200\nCV 50,52,53\nMaaa 16,240,200\nTa 148,17,32768\nv_mul_f32 v54, s57, v54\nMaaa 20,244,200\n"
"D0 4,20,45,2048\nMaaa 80,248,200\nTa 150,17,34816\nMaaa 84,252,200\nv_mul_f32 v55, s57, v55\nMaaa 92,252,204\nTa 152,18,32768\nCV 51,54,55\nMaaa 88,248,204\n"
"s_add_u32 s6, s56, 4096\nSv 48,1,16,6,1024\nMaaa 28,244,204\nTa 154,18,34816\nMaaa 24,240,204\nv_mul_f32 v56, s57, v56\nMvaa 156,236,204\nTa 156,19,32768\nv_mul_f32 v57, s57, v57\n"
"Mvaa 152,232,204\nD0 5,20,45,3072\nMvaa 92,228,204\nTa 158,19,34816\nMvaa 88,224,204\nCV 56,56,57\nMvaa 96,224,208\nRa 160,10,32768\nv_mul_f32 v58, s57, v58\nMvaa 100,228,208\n"
"v_mul_f32 v59, s57, v59\nMvaa 160,232,208\nRa 164,10,34816\nMvaa 164,236,208\ns_add_u32 m0, s48, 4096\ns_add_u32 s46, s45, 0x10000\nMaaa 32,240,208\nRa 168,10,40960\nCV 57,58,59\n"
"Maaa 36,244,208\nD0 2,20,46,0\nMaaa 96,248,208\nRa 172,10,43008\nMaaa 100,252,208\nv_mul_f32 v60, s57, v60\nMaaa 108,252,212\nRa 176,10,49152\nv_mul_f32 v61, s57, v61\n"
"Maaa 104,248,212\nCV 58,60,61\nMaaa 44,244,212\nRa 180,10,51200\nMaaa 40,240,212\nv_mul_f32 v62, s57, v62\nMvaa 172,236,212\nRa 184,10,57344\nv_mul_f32 v63, s57, v63\n"
"Mvaa 168,232,212\nD0 3,20,46,1024\nMvaa 108,228,212\nRa 188,10,59392\nMvaa 104,224,212\nCV 59,62,63\ns_add_u32 s6, s56, 4096\nSv 56,1,16,6,3072\nMvaa 112,224,216\n"
"v_mul_f32 v192, s57, v192\nv_mul_f32 v193, s57, v193\nMvaa 116,228,216\nCV 192,192,193\nMvaa 176,232,216\nv_mul_f32 v194, s57, v194\nMvaa 180,236,216\nv_mul_f32 v195, s57, v195\n"
"Maaa 48,240,216\nCV 193,194,195\nv_mul_f32 v196, s57, v196\nMaaa 52,244,216\nD0 4,20,46,2048\nMaaa 112,248,216\nv_mul_f32 v197, s57, v197\nMaaa 116,252,216\nCV 194,196,197\n"
"Maaa 124,252,220\nv_mul_f32 v198, s57, v198\nv_mul_f32 v199, s57, v199\nMaaa 120,248,220\nCV 195,198,199\ns_add_u32 s6, s53, 0\nSv 192,1,16,6,0\nMaaa 60,244,220\n"
"v_mul_f32 v200, s57, v200\nMaaa 56,240,220\nv_mul_f32 v201, s57, v201\nMvaa 188,236,220\nCV 200,200,201\nv_mul_f32 v202, s57, v202\nMvaa 184,232,220\nD0 5,20,46,3072\n"
"Mvaa 124,228,220\nv_mul_f32 v203, s57, v203\nMvaa 120,224,220\ns_waitcnt vmcnt(17) lgkmcnt(0)\ns_barrier\nMvaa 64,160,128\nTa 192,12,49152\nCV 201,202,203\nMvaa 68,164,128\n"
"v_mul_f32 v204, s57, v204\nMvaa 128,168,128\nTa 194,12,51200\nMvaa 132,172,128\ns_add_u32 m0, s47, 16384\ns_add_u32 s44, s44, 0x40000\nMaaa 0,176,128\nTa 196,13,49152\n"
"v_mul_f32 v205, s57, v205\nMaaa 4,180,128\nD0 6,24,44,0\nMaaa 64,184,128\nTa 198,13,51200\nMaaa 68,188,128\nCV 202,204,205\nMaaa 76,188,132\nTa 200,14,49152\n"
"v_mul_f32 v206, s57, v206\nMaaa 72,184,132\nv_mul_f32 v207, s57, v207\nMaaa 12,180,132\nTa 202,14,51200\nMaaa 8,176,132\nCV 203,206,207\ns_add_u32 s6, s53, 0\nSv 200,1,16,6,2048\n"
"Mvaa 140,172,132\nTa 204,15,49152\nv_mul_f32 v208, s57, v208\nMvaa 136,168,132\nD0 7,24,44,1024\nMvaa 76,164,132\nTa 206,15,51200\nMvaa 72,160,132\nv_mul_f32 v209, s57, v209\n"
"Mvaa 80,160,136\nTa 208,16,49152\nCV 208,208,209\nMvaa 84,164,136\nv_mul_f32 v210, s57, v210\nMvaa 144,168,136\nTa 210,16,51200\nMvaa 148,172,136\nv_mul_f32 v211, s57, v211\n"
"Maaa 16,176,136\nTa 212,17,49152\nCV 209,210,211\nMaaa 20,180,136\nD0 8,24,44,2048\nMaaa 80,184,136\nTa 214,17,51200\nMaaa 84,188,136\nv_mul_f32 v212, s57, v212\nMaaa 92,188,140\n"
"Ta 216,18,49152\nv_mul_f32 v213, s57, v213\nMaaa 88,184,140\nCV 210,212,213\nMaaa 28,180,140\nTa 218,18,51200\nMaaa 24,176,140\nv_mul_f32 v214, s57, v214\nMvaa 156,172,140\n"
"Ta 220,19,49152\nv_mul_f32 v215, s57, v215\nMvaa 152,168,140\nD0 9,24,44,3072\nMvaa 92,164,140\nTa 222,19,51200\nMvaa 88,160,140\nCV 211,214,215\ns_add_u32 s6, s53, 4096\n"
"Sv 208,1,16,6,0\nMvaa 96,160,144\nRa 224,11,32768\nv_mul_f32 v216, s57, v216\nMvaa 100,164,144\nv_mul_f32 v217, s57, v217\nMvaa 160,168,144\nRa 228,11,34816\nMvaa 164,172,144\n"
"s_add_u32 m0, s47, 32768\ns_add_u32 s44, s44, 0x40000\nMaaa 32,176,144\nRa 232,11,40960\nCV 216,216,217\nMaaa 36,180,144\nD0 6,24,44,0\nMaaa 96,184,144\nRa 236,11,43008\n"
"Maaa 100,188,144\nv_mul_f32 v218, s57, v218\nMaaa 108,188,148\nRa 240,11,49152\nv_mul_f32 v219, s57, v219\nMaaa 104,184,148\nCV 217,218,219\nMaaa 44,180,148\nRa 244,11,51200\n"
"Maaa 40,176,148\nv_mul_f32 v220, s57, v220\nMvaa 172,172,148\nRa 248,11,57344\nv_mul_f32 v221, s57, v221\nMvaa 168,168,148\nD0 7,24,44,1024\nMvaa 108,164,148\nRa 252,11,59392\n"
"Mvaa 104,160,148\nCV 218,220,221\nMvaa 112,160,152\nv_mul_f32 v222, s57, v222\nv_mul_f32 v223, s57, v223\nMvaa 116,164,152\nCV 219,222,223\ns_add_u32 s6, s53, 4096\n"
"Sv 216,1,16,6,2048\nMvaa 176,168,152\nv_mul_f32 v224, s57, v224\nMvaa 180,172,152\nv_mul_f32 v225, s57, v225\nMaaa 48,176,152\nCV 224,224,225\nv_mul_f32 v226, s57, v226\n"
"Maaa 52,180,152\nD0 8,24,44,2048\nMaaa 112,184,152\nv_mul_f32 v227, s57, v227\nMaaa 116,188,152\nCV 225,226,227\nMaaa 124,188,156\nv_mul_f32 v228, s57, v228\n"
"v_mul_f32 v229, s57, v229\nMaaa 120,184,156\nCV 226,228,229\nMaaa 60,180,156\nv_mul_f32 v230, s57, v230\nMaaa 56,176,156\nv_mul_f32 v231, s57, v231\nMvaa 188,172,156\n"
"CV 227,230,231\nMvaa 184,168,156\nD0 9,24,44,3072\ns_add_u32 s6, s53, 0\nSv 224,1,16,6,1024\nMvaa 124,164,156\nv_mul_f32 v232, s57, v232\nMvaa 120,160,156\n"
"s_waitcnt vmcnt(12) lgkmcnt(0)\ns_barrier\nMvaa 64,224,192\nTa 128,12,0\nv_mul_f32 v233, s57, v233\nMvaa 68,228,192\nCV 232,232,233\nMvaa 128,232,192\nTa 130,12,2048\n"
"Mvaa 132,236,192\ns_add_u32 m0, s48, 32768\ns_add_u32 s45, s45, 128\nMaaa 0,240,192\nTa 132,13,0\nv_mul_f32 v234, s57, v234\nMaaa 4,244,192\nD0 2,20,45,0\nMaaa 64,248,192\n"
"Ta 134,13,2048\nMaaa 68,252,192\nv_mul_f32 v235, s57, v235\nMaaa 76,252,196\nTa 136,14,0\nCV 233,234,235\nMaaa 72,248,196\nv_mul_f32 v236, s57, v236\nMaaa 12,244,196\n"
"Ta 138,14,2048\nMaaa 8,240,196\nv_mul_f32 v237, s57, v237\nMvaa 140,236,196\nTa 140,15,0\nCV 234,236,237\nMvaa 136,232,196\nD0 3,20,45,1024\nMvaa 76,228,196\nTa 142,15,2048\n"
"Mvaa 72,224,196\nv_mul_f32 v238, s57, v238\nMvaa 80,224,200\nTa 144,16,0\nv_mul_f32 v239, s57, v239\nMvaa 84,228,200\nCV 235,238,239\ns_add_u32 s6, s53, 0\nSv 232,1,16,6,3072\n"
"Mvaa 144,232,200\nTa 146,16,2048\nMvaa 148,236,200\nv_mul_f32 v240, s57, v240\nMaaa 16,240,200\nTa 148,17,0\nv_mul_f32 v241, s57, v241\nMaaa 20,244,200\nD0 4,20,45,2048\n"
"Maaa 80,248,200\nTa 150,17,2048\nMaaa 84,252,200\nCV 240,240,241\nMaaa 92,252,204\nTa 152,18,0\nv_mul_f32 v242, s57, v242\nMaaa 88,248,204\nv_mul_f32 v243, s57, v243\n"
"Maaa 28,244,204\nTa 154,18,2048\nMaaa 24,240,204\nCV 241,242,243\nMvaa 156,236,204\nTa 156,19,0\nv_mul_f32 v244, s57, v244\nMvaa 152,232,204\nD0 5,20,45,3072\nMvaa 92,228,204\n"
"Ta 158,19,2048\nMvaa 88,224,204\nv_mul_f32 v245, s57, v245\nMvaa 96,224,208\nRa 160,10,0\nCV 242,244,245\nMvaa 100,228,208\nv_mul_f32 v246, s57, v246\nMvaa 160,232,208\n"
"Ra 164,10,2048\nMvaa 164,236,208\ns_add_u32 m0, s48, 36864\ns_add_u32 s46, s45, 0x10000\nMaaa 32,240,208\nRa 168,10,8192\nv_mul_f32 v247, s57, v247\nMaaa 36,244,208\nD0 2,20,46,0\n"
"Maaa 96,248,208\nRa 172,10,10240\nMaaa 100,252,208\nCV 243,246,247\ns_add_u32 s6, s53, 4096\nSv 240,1,16,6,1024\nMaaa 108,252,212\nRa 176,10,16384\nv_mul_f32 v248, s57, v248\n"
"Maaa 104,248,212\nv_mul_f32 v249, s57, v249\nMaaa 44,244,212\nRa 180,10,18432\nMaaa 40,240,212\nCV 248,248,249\nMvaa 172,236,212\nRa 184,10,24576\nv_mul_f32 v250, s57, v250\n"
"Mvaa 168,232,212\nD0 3,20,46,1024\nMvaa 108,228,212\nRa 188,10,26624\nMvaa 104,224,212\nv_mul_f32 v251, s57, v251\nMvaa 112,224,216\nCV 249,250,251\nv_mul_f32 v252, s57, v252\n"
"Mvaa 116,228,216\nv_mul_f32 v253, s57, v253\nMvaa 176,232,216\nCV 250,252,253\nMvaa 180,236,216\nv_mul_f32 v254, s57, v254\nMaaa 48,240,216\nv_mul_f32 v255, s57, v255\n"
"CV 251,254,255\nMaaa 52,244,216\nD0 4,20,46,2048\ns_add_u32 s6, s53, 4096\nSv 248,1,16,6,3072\nMaaa 112,248,216\nMaaa 116,252,216\nMaaa 124,252,220\nMaaa 120,248,220\n"
"Maaa 60,244,220\nMaaa 56,240,220\ns_lshl3_add_u32 s90, s12, s13\ns_lshl2_add_u32 s90, s90, s11\ns_lshl2_add_u32 s90, s90, 0xc04a400\nMvaa 188,236,220\nMvaa 184,232,220\n"
"D0 5,20,46,3072\nMvaa 124,228,220\nMvaa 120,224,220\ns_waitcnt vmcnt(18) lgkmcnt(0)\ns_barrier\ns_mov_b32 s50, 2\ns_mov_b32 s88, 0\ns_mov_b32 s89, 0\n.GV121:\nMvaa 64,160,128\n"
"Ta 192,12,16384\nMvaa 68,164,128\nMvaa 128,168,128\nTa 194,12,18432\nMvaa 132,172,128\ns_add_u32 m0, s47, 49152\ns_add_u32 s44, s44, 0x40000\nMaaa 0,176,128\nTa 196,13,16384\n"
"Maaa 4,180,128\nD0 6,24,44,0\nMaaa 64,184,128\nTa 198,13,18432\nMaaa 68,188,128\nMaaa 76,188,132\nTa 200,14,16384\nMaaa 72,184,132\nMaaa 12,180,132\nTa 202,14,18432\n"
"Maaa 8,176,132\nMvaa 140,172,132\nTa 204,15,16384\nMvaa 136,168,132\nD0 7,24,44,1024\nMvaa 76,164,132\nTa 206,15,18432\nMvaa 72,160,132\nMvaa 80,160,136\nTa 208,16,16384\n"
"Mvaa 84,164,136\nMvaa 144,168,136\nTa 210,16,18432\nMvaa 148,172,136\nMaaa 16,176,136\nTa 212,17,16384\nMaaa 20,180,136\nD0 8,24,44,2048\nMaaa 80,184,136\nTa 214,17,18432\n"
"Maaa 84,188,136\nMaaa 92,188,140\nTa 216,18,16384\nMaaa 88,184,140\nMaaa 28,180,140\nTa 218,18,18432\nMaaa 24,176,140\nMvaa 156,172,140\nTa 220,19,16384\nMvaa 152,168,140\n"
"D0 9,24,44,3072\nMvaa 92,164,140\nTa 222,19,18432\nMvaa 88,160,140\nMvaa 96,160,144\nRa 224,11,0\nMvaa 100,164,144\nMvaa 160,168,144\nRa 228,11,2048\nMvaa 164,172,144\n"
"s_add_u32 m0, s47, 0\ns_add_u32 s44, s44, 0x40000\nMaaa 32,176,144\nRa 232,11,8192\nMaaa 36,180,144\nD0 6,24,44,0\nMaaa 96,184,144\nRa 236,11,10240\nMaaa 100,188,144\n"
"Maaa 108,188,148\nRa 240,11,16384\nMaaa 104,184,148\nMaaa 44,180,148\nRa 244,11,18432\nMaaa 40,176,148\nMvaa 172,172,148\nRa 248,11,24576\nMvaa 168,168,148\nD0 7,24,44,1024\n"
"Mvaa 108,164,148\nRa 252,11,26624\nMvaa 104,160,148\nMvaa 112,160,152\nMvaa 116,164,152\nMvaa 176,168,152\nMvaa 180,172,152\nMaaa 48,176,152\nMaaa 52,180,152\nD0 8,24,44,2048\n"
"Maaa 112,184,152\nMaaa 116,188,152\nMaaa 124,188,156\nMaaa 120,184,156\nMaaa 60,180,156\nMaaa 56,176,156\nMvaa 188,172,156\nMvaa 184,168,156\nD0 9,24,44,3072\nMvaa 124,164,156\n"
"Mvaa 120,160,156\ns_waitcnt vmcnt(8) lgkmcnt(0)\ns_barrier\nMvaa 64,224,192\nTa 128,12,32768\nMvaa 68,228,192\nMvaa 128,232,192\nTa 130,12,34816\nMvaa 132,236,192\n"
"s_add_u32 m0, s48, 0\ns_add_u32 s45, s45, 128\nMaaa 0,240,192\nTa 132,13,32768\nMaaa 4,244,192\nD0 2,20,45,0\nMaaa 64,248,192\nTa 134,13,34816\nMaaa 68,252,192\nMaaa 76,252,196\n"
"Ta 136,14,32768\nMaaa 72,248,196\nMaaa 12,244,196\nTa 138,14,34816\nMaaa 8,240,196\nMvaa 140,236,196\nTa 140,15,32768\nMvaa 136,232,196\nD0 3,20,45,1024\nMvaa 76,228,196\n"
"Ta 142,15,34816\nMvaa 72,224,196\nMvaa 80,224,200\nTa 144,16,32768\nMvaa 84,228,200\nMvaa 144,232,200\nTa 146,16,34816\nMvaa 148,236,200\nMaaa 16,240,200\nTa 148,17,32768\n"
"Maaa 20,244,200\nD0 4,20,45,2048\nMaaa 80,248,200\nTa 150,17,34816\nMaaa 84,252,200\nMaaa 92,252,204\nTa 152,18,32768\nMaaa 88,248,204\nMaaa 28,244,204\nTa 154,18,34816\n"
"Maaa 24,240,204\nMvaa 156,236,204\nTa 156,19,32768\nMvaa 152,232,204\nD0 5,20,45,3072\nMvaa 92,228,204\nTa 158,19,34816\nMvaa 88,224,204\nMvaa 96,224,208\nRa 160,10,32768\n"
"Mvaa 100,228,208\nMvaa 160,232,208\nRa 164,10,34816\nMvaa 164,236,208\ns_add_u32 m0, s48, 4096\ns_add_u32 s46, s45, 0x10000\nMaaa 32,240,208\nRa 168,10,40960\nMaaa 36,244,208\n"
"D0 2,20,46,0\nMaaa 96,248,208\nRa 172,10,43008\nMaaa 100,252,208\nMaaa 108,252,212\nRa 176,10,49152\nMaaa 104,248,212\nMaaa 44,244,212\nRa 180,10,51200\nMaaa 40,240,212\n"
"Mvaa 172,236,212\nRa 184,10,57344\nMvaa 168,232,212\nD0 3,20,46,1024\nMvaa 108,228,212\nRa 188,10,59392\nMvaa 104,224,212\nMvaa 112,224,216\nMvaa 116,228,216\nMvaa 176,232,216\n"
"Mvaa 180,236,216\nMaaa 48,240,216\nMaaa 52,244,216\nD0 4,20,46,2048\nMaaa 112,248,216\nMaaa 116,252,216\nMaaa 124,252,220\nMaaa 120,248,220\nMaaa 60,244,220\nMaaa 56,240,220\n"
"Mvaa 188,236,220\nMvaa 184,232,220\nD0 5,20,46,3072\nMvaa 124,228,220\nMvaa 120,224,220\ns_waitcnt vmcnt(12) lgkmcnt(0)\ns_barrier\nMvaa 64,160,128\nTa 192,12,49152\n"
"Mvaa 68,164,128\nMvaa 128,168,128\nTa 194,12,51200\nMvaa 132,172,128\ns_add_u32 m0, s47, 16384\ns_add_u32 s44, s44, 0x40000\nMaaa 0,176,128\nTa 196,13,49152\nMaaa 4,180,128\n"
"D0 6,24,44,0\nMaaa 64,184,128\nTa 198,13,51200\nMaaa 68,188,128\nMaaa 76,188,132\nTa 200,14,49152\nMaaa 72,184,132\nMaaa 12,180,132\nTa 202,14,51200\nMaaa 8,176,132\n"
"Mvaa 140,172,132\nTa 204,15,49152\nMvaa 136,168,132\nD0 7,24,44,1024\nMvaa 76,164,132\nTa 206,15,51200\nMvaa 72,160,132\nMvaa 80,160,136\nTa 208,16,49152\nMvaa 84,164,136\n"
"Mvaa 144,168,136\nTa 210,16,51200\nMvaa 148,172,136\nMaaa 16,176,136\nTa 212,17,49152\nMaaa 20,180,136\nD0 8,24,44,2048\nMaaa 80,184,136\nTa 214,17,51200\nMaaa 84,188,136\n"
"Maaa 92,188,140\nTa 216,18,49152\nMaaa 88,184,140\nMaaa 28,180,140\nTa 218,18,51200\nMaaa 24,176,140\nMvaa 156,172,140\nTa 220,19,49152\nMvaa 152,168,140\nD0 9,24,44,3072\n"
"Mvaa 92,164,140\nTa 222,19,51200\nMvaa 88,160,140\nMvaa 96,160,144\nRa 224,11,32768\nMvaa 100,164,144\nMvaa 160,168,144\nRa 228,11,34816\nMvaa 164,172,144\n"
"s_add_u32 m0, s47, 32768\ns_add_u32 s44, s44, 0x40000\nMaaa 32,176,144\nRa 232,11,40960\nMaaa 36,180,144\nD0 6,24,44,0\nMaaa 96,184,144\nRa 236,11,43008\nMaaa 100,188,144\n"
"Maaa 108,188,148\nRa 240,11,49152\nMaaa 104,184,148\nMaaa 44,180,148\nRa 244,11,51200\nMaaa 40,176,148\nMvaa 172,172,148\nRa 248,11,57344\nMvaa 168,168,148\nD0 7,24,44,1024\n"
"Mvaa 108,164,148\nRa 252,11,59392\nMvaa 104,160,148\nMvaa 112,160,152\nMvaa 116,164,152\nMvaa 176,168,152\nMvaa 180,172,152\nMaaa 48,176,152\nMaaa 52,180,152\nD0 8,24,44,2048\n"
"Maaa 112,184,152\nMaaa 116,188,152\nMaaa 124,188,156\nMaaa 120,184,156\nMaaa 60,180,156\nMaaa 56,176,156\nMvaa 188,172,156\nMvaa 184,168,156\nD0 9,24,44,3072\nMvaa 124,164,156\n"
"Mvaa 120,160,156\ns_waitcnt vmcnt(8) lgkmcnt(0)\ns_barrier\nMvaa 64,224,192\nTa 128,12,0\nMvaa 68,228,192\nMvaa 128,232,192\nTa 130,12,2048\nMvaa 132,236,192\n"
"s_add_u32 m0, s48, 32768\ns_add_u32 s45, s45, 128\nMaaa 0,240,192\nTa 132,13,0\nMaaa 4,244,192\nD0 2,20,45,0\nMaaa 64,248,192\nTa 134,13,2048\nMaaa 68,252,192\nMaaa 76,252,196\n"
"Ta 136,14,0\nMaaa 72,248,196\nMaaa 12,244,196\nTa 138,14,2048\nMaaa 8,240,196\nMvaa 140,236,196\nTa 140,15,0\nMvaa 136,232,196\nD0 3,20,45,1024\nMvaa 76,228,196\nTa 142,15,2048\n"
"Mvaa 72,224,196\nMvaa 80,224,200\nTa 144,16,0\nMvaa 84,228,200\nMvaa 144,232,200\nTa 146,16,2048\nMvaa 148,236,200\nMaaa 16,240,200\nTa 148,17,0\nMaaa 20,244,200\nD0 4,20,45,2048\n"
"Maaa 80,248,200\nTa 150,17,2048\nMaaa 84,252,200\nMaaa 92,252,204\nTa 152,18,0\nMaaa 88,248,204\nMaaa 28,244,204\nTa 154,18,2048\nMaaa 24,240,204\nMvaa 156,236,204\nTa 156,19,0\n"
"Mvaa 152,232,204\nD0 5,20,45,3072\nMvaa 92,228,204\nTa 158,19,2048\nMvaa 88,224,204\nMvaa 96,224,208\nRa 160,10,0\nMvaa 100,228,208\nMvaa 160,232,208\nRa 164,10,2048\n"
"Mvaa 164,236,208\ns_add_u32 m0, s48, 36864\ns_add_u32 s46, s45, 0x10000\nMaaa 32,240,208\nRa 168,10,8192\nMaaa 36,244,208\nD0 2,20,46,0\nMaaa 96,248,208\nRa 172,10,10240\n"
"Maaa 100,252,208\nMaaa 108,252,212\nRa 176,10,16384\nMaaa 104,248,212\nMaaa 44,244,212\nRa 180,10,18432\nMaaa 40,240,212\nMvaa 172,236,212\nRa 184,10,24576\nMvaa 168,232,212\n"
"D0 3,20,46,1024\nMvaa 108,228,212\nRa 188,10,26624\nMvaa 104,224,212\nMvaa 112,224,216\nMvaa 116,228,216\nMvaa 176,232,216\nMvaa 180,236,216\nMaaa 48,240,216\nMaaa 52,244,216\n"
"D0 4,20,46,2048\nMaaa 112,248,216\nMaaa 116,252,216\nMaaa 124,252,220\nMaaa 120,248,220\nMaaa 60,244,220\nMaaa 56,240,220\nMvaa 188,236,220\nMvaa 184,232,220\nD0 5,20,46,3072\n"
"Mvaa 124,228,220\nMvaa 120,224,220\ns_waitcnt vmcnt(12) lgkmcnt(0)\ns_barrier\ns_sub_u32 s50, s50, 1\ns_cbranch_scc0 .GV121\nMvaa 64,160,128\nTa 192,12,16384\nMvaa 68,164,128\n"
"Mvaa 128,168,128\nTa 194,12,18432\nMvaa 132,172,128\ns_add_u32 m0, s47, 49152\ns_add_u32 s44, s44, 0x40000\nMaaa 0,176,128\nTa 196,13,16384\nMaaa 4,180,128\nD0 6,24,44,0\n"
"Maaa 64,184,128\nTa 198,13,18432\nMaaa 68,188,128\nMaaa 76,188,132\nTa 200,14,16384\nMaaa 72,184,132\nbuffer_store_dword v25, off, s[16:19], s90 offset:0 sc0 sc1\nMaaa 12,180,132\n"
"Ta 202,14,18432\nMaaa 8,176,132\nMvaa 140,172,132\nTa 204,15,16384\nMvaa 136,168,132\nD0 7,24,44,1024\nMvaa 76,164,132\nTa 206,15,18432\nMvaa 72,160,132\nMvaa 80,160,136\n"
"Ta 208,16,16384\nMvaa 84,164,136\nMvaa 144,168,136\nTa 210,16,18432\nMvaa 148,172,136\nMaaa 16,176,136\nTa 212,17,16384\nMaaa 20,180,136\nD0 8,24,44,2048\nMaaa 80,184,136\n"
"Ta 214,17,18432\nMaaa 84,188,136\nMaaa 92,188,140\nTa 216,18,16384\nMaaa 88,184,140\nMaaa 28,180,140\nTa 218,18,18432\nMaaa 24,176,140\nMvaa 156,172,140\nTa 220,19,16384\n"
"Mvaa 152,168,140\nD0 9,24,44,3072\nMvaa 92,164,140\nTa 222,19,18432\nMvaa 88,160,140\nMvaa 96,160,144\nRa 224,11,0\nMvaa 100,164,144\nMvaa 160,168,144\nRa 228,11,2048\n"
"Mvaa 164,172,144\ns_add_u32 m0, s47, 0\ns_add_u32 s44, s44, 0x40000\nMaaa 32,176,144\nRa 232,11,8192\nMaaa 36,180,144\nD0 6,24,44,0\nMaaa 96,184,144\nRa 236,11,10240\n"
"Maaa 100,188,144\nMaaa 108,188,148\nRa 240,11,16384\nMaaa 104,184,148\nMaaa 44,180,148\nRa 244,11,18432\nMaaa 40,176,148\nMvaa 172,172,148\nRa 248,11,24576\nMvaa 168,168,148\n"
"D0 7,24,44,1024\nMvaa 108,164,148\nRa 252,11,26624\nMvaa 104,160,148\nMvaa 112,160,152\nMvaa 116,164,152\nMvaa 176,168,152\nMvaa 180,172,152\nMaaa 48,176,152\nMaaa 52,180,152\n"
"D0 8,24,44,2048\nMaaa 112,184,152\nMaaa 116,188,152\nMaaa 124,188,156\nMaaa 120,184,156\nMaaa 60,180,156\nMaaa 56,176,156\nMvaa 188,172,156\nMvaa 184,168,156\nD0 9,24,44,3072\n"
"Mvaa 124,164,156\nMvaa 120,160,156\ns_waitcnt vmcnt(9) lgkmcnt(0)\ns_barrier\nMvaa 64,224,192\nTa 128,12,32768\nMvaa 68,228,192\nMvaa 128,232,192\nTa 130,12,34816\n"
"Mvaa 132,236,192\ns_add_u32 m0, s48, 0\ns_add_u32 s45, s45, 128\nMaaa 0,240,192\nTa 132,13,32768\nMaaa 4,244,192\nD0 2,20,45,0\nMaaa 64,248,192\nTa 134,13,34816\nMaaa 68,252,192\n"
"Maaa 76,252,196\nTa 136,14,32768\nMaaa 72,248,196\nMaaa 12,244,196\nTa 138,14,34816\nMaaa 8,240,196\nMvaa 140,236,196\nTa 140,15,32768\nMvaa 136,232,196\nD0 3,20,45,1024\n"
"Mvaa 76,228,196\nTa 142,15,34816\nMvaa 72,224,196\nMvaa 80,224,200\nTa 144,16,32768\nMvaa 84,228,200\nMvaa 144,232,200\nTa 146,16,34816\nMvaa 148,236,200\nMaaa 16,240,200\n"
"Ta 148,17,32768\nMaaa 20,244,200\nD0 4,20,45,2048\nMaaa 80,248,200\nTa 150,17,34816\nMaaa 84,252,200\nMaaa 92,252,204\nTa 152,18,32768\nMaaa 88,248,204\nMaaa 28,244,204\n"
"Ta 154,18,34816\nMaaa 24,240,204\nMvaa 156,236,204\nTa 156,19,32768\nMvaa 152,232,204\nD0 5,20,45,3072\nMvaa 92,228,204\nTa 158,19,34816\nMvaa 88,224,204\nMvaa 96,224,208\n"
"Ra 160,10,32768\nMvaa 100,228,208\nMvaa 160,232,208\nRa 164,10,34816\nMvaa 164,236,208\ns_add_u32 m0, s48, 4096\ns_add_u32 s46, s45, 0x10000\nMaaa 32,240,208\nRa 168,10,40960\n"
"Maaa 36,244,208\nD0 2,20,46,0\nMaaa 96,248,208\nRa 172,10,43008\nMaaa 100,252,208\nMaaa 108,252,212\nRa 176,10,49152\nMaaa 104,248,212\nMaaa 44,244,212\nRa 180,10,51200\n"
"Maaa 40,240,212\nMvaa 172,236,212\nRa 184,10,57344\nMvaa 168,232,212\nD0 3,20,46,1024\nMvaa 108,228,212\nRa 188,10,59392\nMvaa 104,224,212\nMvaa 112,224,216\nMvaa 116,228,216\n"
"Mvaa 176,232,216\nMvaa 180,236,216\nMaaa 48,240,216\nMaaa 52,244,216\nD0 4,20,46,2048\nMaaa 112,248,216\nMaaa 116,252,216\nMaaa 124,252,220\nMaaa 120,248,220\nMaaa 60,244,220\n"
"Maaa 56,240,220\nMvaa 188,236,220\nMvaa 184,232,220\nD0 5,20,46,3072\nMvaa 124,228,220\nMvaa 120,224,220\ns_waitcnt vmcnt(12) lgkmcnt(0)\ns_barrier\nMvaa 64,160,128\n"
"Ta 192,12,49152\nMvaa 68,164,128\nMvaa 128,168,128\nTa 194,12,51200\nMvaa 132,172,128\ns_add_u32 m0, s47, 16384\ns_add_u32 s44, s44, 0x40000\nMaaa 0,176,128\nTa 196,13,49152\n"
"Maaa 4,180,128\nD0 6,24,44,0\nMaaa 64,184,128\nTa 198,13,51200\nMaaa 68,188,128\nMaaa 76,188,132\nTa 200,14,49152\nMaaa 72,184,132\nMaaa 12,180,132\nTa 202,14,51200\n"
"Maaa 8,176,132\nMvaa 140,172,132\nTa 204,15,49152\nMvaa 136,168,132\nD0 7,24,44,1024\nMvaa 76,164,132\nTa 206,15,51200\nMvaa 72,160,132\nMvaa 80,160,136\nTa 208,16,49152\n"
"Mvaa 84,164,136\nMvaa 144,168,136\nTa 210,16,51200\nMvaa 148,172,136\nMaaa 16,176,136\nTa 212,17,49152\nMaaa 20,180,136\nD0 8,24,44,2048\nMaaa 80,184,136\nTa 214,17,51200\n"
"Maaa 84,188,136\nMaaa 92,188,140\nTa 216,18,49152\nMaaa 88,184,140\nMaaa 28,180,140\nTa 218,18,51200\nMaaa 24,176,140\nMvaa 156,172,140\nTa 220,19,49152\nMvaa 152,168,140\n"
"D0 9,24,44,3072\nMvaa 92,164,140\nTa 222,19,51200\nMvaa 88,160,140\nMvaa 96,160,144\nRa 224,11,32768\nMvaa 100,164,144\ns_lshl_b32 s5, s73, 7\ns_add_u32 s5, s5, 0xc04ac00\n"
"v_bfe_u32 v27, v1, 2, 7\nbuffer_load_dword v27, v27, s[16:19], s5 offen sc0 sc1\nMvaa 160,168,144\nRa 228,11,34816\nMvaa 164,172,144\ns_add_u32 m0, s47, 32768\n"
"s_add_u32 s44, s44, 0x40000\nMaaa 32,176,144\nRa 232,11,40960\nMaaa 36,180,144\nD0 6,24,44,0\nMaaa 96,184,144\nRa 236,11,43008\nMaaa 100,188,144\nMaaa 108,188,148\n"
"Ra 240,11,49152\nMaaa 104,184,148\nMaaa 44,180,148\nRa 244,11,51200\nMaaa 40,176,148\nMvaa 172,172,148\nRa 248,11,57344\nMvaa 168,168,148\nD0 7,24,44,1024\nMvaa 108,164,148\n"
"Ra 252,11,59392\nMvaa 104,160,148\nMvaa 112,160,152\nMvaa 116,164,152\nMvaa 176,168,152\nMvaa 180,172,152\nMaaa 48,176,152\nMaaa 52,180,152\nD0 8,24,44,2048\nMaaa 112,184,152\n"
"Maaa 116,188,152\nMaaa 124,188,156\nMaaa 120,184,156\nMaaa 60,180,156\nMaaa 56,176,156\nMvaa 188,172,156\nMvaa 184,168,156\nD0 9,24,44,3072\nMvaa 124,164,156\nMvaa 120,160,156\n"
"s_waitcnt vmcnt(9) lgkmcnt(0)\ns_barrier\nMvaa 64,224,192\nTa 128,12,0\nMvaa 68,228,192\nMvaa 128,232,192\nTa 130,12,2048\nMvaa 132,236,192\ns_add_u32 m0, s48, 32768\n"
"s_add_u32 s45, s45, 128\nMaaa 0,240,192\nTa 132,13,0\nMaaa 4,244,192\nD0 2,20,45,0\nMaaa 64,248,192\nTa 134,13,2048\nMaaa 68,252,192\nMaaa 76,252,196\nTa 136,14,0\n"
"Maaa 72,248,196\nMaaa 12,244,196\nTa 138,14,2048\nMaaa 8,240,196\nMvaa 140,236,196\nTa 140,15,0\nMvaa 136,232,196\nD0 3,20,45,1024\nMvaa 76,228,196\nTa 142,15,2048\n"
"Mvaa 72,224,196\nMvaa 80,224,200\nTa 144,16,0\nMvaa 84,228,200\nMvaa 144,232,200\nTa 146,16,2048\nMvaa 148,236,200\nMaaa 16,240,200\nTa 148,17,0\nMaaa 20,244,200\nD0 4,20,45,2048\n"
"Maaa 80,248,200\nTa 150,17,2048\nMaaa 84,252,200\nMaaa 92,252,204\nTa 152,18,0\nMaaa 88,248,204\nMaaa 28,244,204\nTa 154,18,2048\nMaaa 24,240,204\nMvaa 156,236,204\nTa 156,19,0\n"
"Mvaa 152,232,204\nD0 5,20,45,3072\nMvaa 92,228,204\nTa 158,19,2048\nMvaa 88,224,204\nMvaa 96,224,208\nRa 160,10,0\nMvaa 100,228,208\nMvaa 160,232,208\nRa 164,10,2048\n"
"Mvaa 164,236,208\ns_add_u32 m0, s48, 36864\ns_add_u32 s46, s45, 0x10000\nMaaa 32,240,208\nRa 168,10,8192\nWV 8\nv_xor_b32 v27, s15, v27\nv_and_b32 v32, 0xffffdfff, v27\n"
"v_cmp_ne_u32 vcc, 0, v32\ns_nop 0\ns_cbranch_vccnz .S118\n.S119:\nv_cmp_ne_u32 vcc, 0, v27\ns_nop 3\ns_bcnt1_i32_b64 s4, vcc\ns_cmp_gt_u32 s4, 32\ns_cselect_b32 s4, 1, 0\n"
"Maaa 36,244,208\nD0 2,20,46,0\nMaaa 96,248,208\nRa 172,10,10240\nMaaa 100,252,208\ns_xor_b32 s4, s4, s12\nMaaa 108,252,212\nRa 176,10,16384\nMaaa 104,248,212\ns_and_b32 s4, s4, 1\n"
"Maaa 44,244,212\nRa 180,10,18432\nMaaa 40,240,212\ns_lshl_b32 s4, s4, 13\nMvaa 172,236,212\nRa 184,10,24576\nMvaa 168,232,212\nD0 3,20,46,1024\nMvaa 108,228,212\nRa 188,10,26624\n"
"Mvaa 104,224,212\ns_mul_i32 s72, s73, 25165824\nMvaa 112,224,216\ns_add_u32 s72, s72, s4\nMvaa 116,228,216\ns_and_b32 s4, s12, 1\nMvaa 176,232,216\ns_lshl_b32 s4, s4, 21\n"
"Mvaa 180,236,216\ns_add_u32 s72, s72, s4\nMaaa 48,240,216\ns_lshl3_add_u32 s2, s13, s11\nMaaa 52,244,216\nD0 4,20,46,2048\nMaaa 112,248,216\ns_lshl_b32 s2, s2, 11\n"
"Maaa 116,252,216\ns_lshl_b32 s3, s14, 22\nMaaa 124,252,220\ns_add_u32 s2, s2, s3\nMaaa 120,248,220\ns_add_u32 s2, s2, s72\nMaaa 60,244,220\ns_add_u32 s49, s2, 0xc00000\n"
"Maaa 56,240,220\nMvaa 188,236,220\nMvaa 184,232,220\nD0 5,20,46,3072\nMvaa 124,228,220\nMvaa 120,224,220\nWL 0\ns_barrier\nMvaa 64,160,128\nTa 192,12,16384\ns_mov_b32 s36, s60\n"
"Mvaa 68,164,128\ns_and_b32 s37, s61, 0xffff\nMvaa 128,168,128\nTa 194,12,18432\nMvaa 132,172,128\ns_add_u32 m0, s47, 49152\ns_add_u32 s44, s44, 0x40000\nMaaa 0,176,128\n"
"Ta 196,13,16384\nMaaa 4,180,128\nD0 6,24,44,0\nMaaa 64,184,128\nTa 198,13,18432\nMaaa 68,188,128\ns_mov_b32 s38, 0x380000\nMaaa 76,188,132\nTa 200,14,16384\nMaaa 72,184,132\n"
"s_mov_b32 s39, 0x20000\nMaaa 12,180,132\nTa 202,14,18432\nMaaa 8,176,132\ns_lshr_b32 s4, s13, 1\nMvaa 140,172,132\nTa 204,15,16384\nMvaa 136,168,132\nD0 7,24,44,1024\n"
"Mvaa 76,164,132\nTa 206,15,18432\nMvaa 72,160,132\ns_lshl2_add_u32 s4, s14, s4\nMvaa 80,160,136\nTa 208,16,16384\ns_lshl3_add_u32 s2, s12, s4\nMvaa 84,164,136\n"
"s_lshl2_add_u32 s2, s2, s11\nMvaa 144,168,136\nTa 210,16,18432\nMvaa 148,172,136\ns_mul_i32 s2, s2, 7168\nMaaa 16,176,136\nTa 212,17,16384\nMaaa 20,180,136\nD0 8,24,44,2048\n"
"Maaa 80,184,136\nTa 214,17,18432\nMaaa 84,188,136\ns_mul_i32 s3, s11, 7168\nMaaa 92,188,140\nTa 216,18,16384\nMaaa 88,184,140\ns_add_u32 s3, s3, 131072\nMaaa 28,180,140\n"
"Ta 218,18,18432\nMaaa 24,176,140\nMvaa 156,172,140\nTa 220,19,16384\nMvaa 152,168,140\nD0 9,24,44,3072\nMvaa 92,164,140\nTa 222,19,18432\nMvaa 88,160,140\nMvaa 96,160,144\n"
"Ra 224,11,0\ns_add_u32 m0, s3, 0\ns_add_u32 s5, s2, 0\nD0 1,36,5,0\nD0 1,36,5,1024\nMvaa 100,164,144\nMvaa 160,168,144\nRa 228,11,2048\nMvaa 164,172,144\ns_add_u32 m0, s47, 0\n"
"s_add_u32 s44, s44, 0x40000\nMaaa 32,176,144\nRa 232,11,8192\nMaaa 36,180,144\nD0 6,24,44,0\nMaaa 96,184,144\nRa 236,11,10240\nMaaa 100,188,144\nMaaa 108,188,148\nRa 240,11,16384\n"
"Maaa 104,184,148\nMaaa 44,180,148\nRa 244,11,18432\nMaaa 40,176,148\nMvaa 172,172,148\nRa 248,11,24576\nMvaa 168,168,148\nD0 7,24,44,1024\nMvaa 108,164,148\nRa 252,11,26624\n"
"Mvaa 104,160,148\nMvaa 112,160,152\nMvaa 116,164,152\nMvaa 176,168,152\nMvaa 180,172,152\nMaaa 48,176,152\nMaaa 52,180,152\nD0 8,24,44,2048\nMaaa 112,184,152\nMaaa 116,188,152\n"
"Maaa 124,188,156\nMaaa 120,184,156\nMaaa 60,180,156\nMaaa 56,176,156\nMvaa 188,172,156\nMvaa 184,168,156\nD0 9,24,44,3072\nMvaa 124,164,156\ns_add_u32 m0, s3, 2048\n"
"s_add_u32 s5, s2, 2048\nD0 1,36,5,0\nD0 1,36,5,1024\nMvaa 120,160,156\ns_waitcnt vmcnt(12) lgkmcnt(0)\ns_barrier\nMvaa 64,224,192\nTa 128,12,32768\nMvaa 68,228,192\n"
"Mvaa 128,232,192\nTa 130,12,34816\nMvaa 132,236,192\ns_add_u32 m0, s48, 0\ns_add_u32 s45, s45, 128\nMaaa 0,240,192\nTa 132,13,32768\nMaaa 4,244,192\nD0 2,20,45,0\nMaaa 64,248,192\n"
"Ta 134,13,34816\nMaaa 68,252,192\nMaaa 76,252,196\nTa 136,14,32768\nMaaa 72,248,196\nMaaa 12,244,196\nTa 138,14,34816\nMaaa 8,240,196\nMvaa 140,236,196\nTa 140,15,32768\n"
"Mvaa 136,232,196\nD0 3,20,45,1024\nMvaa 76,228,196\nTa 142,15,34816\nMvaa 72,224,196\nMvaa 80,224,200\nTa 144,16,32768\nMvaa 84,228,200\nMvaa 144,232,200\nTa 146,16,34816\n"
"Mvaa 148,236,200\nMaaa 16,240,200\nTa 148,17,32768\nMaaa 20,244,200\nD0 4,20,45,2048\nMaaa 80,248,200\nTa 150,17,34816\nMaaa 84,252,200\nMaaa 92,252,204\nTa 152,18,32768\n"
"Maaa 88,248,204\nMaaa 28,244,204\nTa 154,18,34816\nMaaa 24,240,204\nMvaa 156,236,204\nTa 156,19,32768\nMvaa 152,232,204\nD0 5,20,45,3072\nMvaa 92,228,204\nTa 158,19,34816\n"
"Mvaa 88,224,204\nMvaa 96,224,208\nRa 160,10,32768\nMvaa 100,228,208\nMvaa 160,232,208\nRa 164,10,34816\nMvaa 164,236,208\ns_add_u32 m0, s48, 4096\ns_add_u32 s46, s45, 0x10000\n"
"Maaa 32,240,208\nRa 168,10,40960\nMaaa 36,244,208\nD0 2,20,46,0\nMaaa 96,248,208\nRa 172,10,43008\nMaaa 100,252,208\nMaaa 108,252,212\nRa 176,10,49152\nMaaa 104,248,212\n"
"Maaa 44,244,212\nRa 180,10,51200\nMaaa 40,240,212\nMvaa 172,236,212\nRa 184,10,57344\nMvaa 168,232,212\nD0 3,20,46,1024\nMvaa 108,228,212\nRa 188,10,59392\nMvaa 104,224,212\n"
"Mvaa 112,224,216\nMvaa 116,228,216\nMvaa 176,232,216\nMvaa 180,236,216\nMaaa 48,240,216\nMaaa 52,244,216\nD0 4,20,46,2048\nMaaa 112,248,216\nMaaa 116,252,216\nMaaa 124,252,220\n"
"Maaa 120,248,220\nMaaa 60,244,220\nMaaa 56,240,220\nMvaa 188,236,220\nMvaa 184,232,220\nD0 5,20,46,3072\nMvaa 124,228,220\nMvaa 120,224,220\ns_waitcnt vmcnt(16) lgkmcnt(0)\n"
"s_barrier\nMvaa 64,160,128\nTa 192,12,49152\nMvaa 68,164,128\nMvaa 128,168,128\nTa 194,12,51200\nMvaa 132,172,128\ns_add_u32 m0, s47, 16384\ns_add_u32 s44, s44, 0x40000\n"
"Maaa 0,176,128\nTa 196,13,49152\nMaaa 4,180,128\nD0 6,24,44,0\nMaaa 64,184,128\nTa 198,13,51200\nMaaa 68,188,128\nMaaa 76,188,132\nTa 200,14,49152\nMaaa 72,184,132\n"
"Maaa 12,180,132\nTa 202,14,51200\nMaaa 8,176,132\nMvaa 140,172,132\nTa 204,15,49152\nMvaa 136,168,132\nD0 7,24,44,1024\nMvaa 76,164,132\nTa 206,15,51200\nMvaa 72,160,132\n"
"Mvaa 80,160,136\nTa 208,16,49152\nMvaa 84,164,136\nMvaa 144,168,136\nTa 210,16,51200\nMvaa 148,172,136\nMaaa 16,176,136\nTa 212,17,49152\nMaaa 20,180,136\nD0 8,24,44,2048\n"
"Maaa 80,184,136\nTa 214,17,51200\nMaaa 84,188,136\nMaaa 92,188,140\nTa 216,18,49152\nMaaa 88,184,140\nMaaa 28,180,140\nTa 218,18,51200\nMaaa 24,176,140\nMvaa 156,172,140\n"
"Ta 220,19,49152\nMvaa 152,168,140\nD0 9,24,44,3072\nMvaa 92,164,140\nTa 222,19,51200\nMvaa 88,160,140\nMvaa 96,160,144\nRa 224,11,32768\ns_add_u32 m0, s3, 4096\n"
"s_add_u32 s5, s2, 4096\nD0 1,36,5,0\nD0 1,36,5,1024\nMvaa 100,164,144\nMvaa 160,168,144\nRa 228,11,34816\nMvaa 164,172,144\ns_add_u32 m0, s47, 32768\ns_add_u32 s44, s44, 0x40000\n"
"Maaa 32,176,144\nRa 232,11,40960\nMaaa 36,180,144\nD0 6,24,44,0\nMaaa 96,184,144\nRa 236,11,43008\nMaaa 100,188,144\nMaaa 108,188,148\nRa 240,11,49152\nMaaa 104,184,148\n"
"Maaa 44,180,148\nRa 244,11,51200\nMaaa 40,176,148\nMvaa 172,172,148\nRa 248,11,57344\nMvaa 168,168,148\nD0 7,24,44,1024\nMvaa 108,164,148\nRa 252,11,59392\nMvaa 104,160,148\n"
"Mvaa 112,160,152\nMvaa 116,164,152\nMvaa 176,168,152\nMvaa 180,172,152\nMaaa 48,176,152\nMaaa 52,180,152\nD0 8,24,44,2048\nMaaa 112,184,152\nMaaa 116,188,152\nMaaa 124,188,156\n"
"Maaa 120,184,156\nMaaa 60,180,156\nMaaa 56,176,156\nMvaa 188,172,156\nMvaa 184,168,156\nD0 9,24,44,3072\nMvaa 124,164,156\ns_add_u32 m0, s3, 6144\ns_add_u32 s5, s2, 6144\n"
"D0 1,36,5,0\nMvaa 120,160,156\ns_waitcnt vmcnt(11) lgkmcnt(0)\ns_barrier\nMvaa 64,224,192\nTa 128,12,0\nMvaa 68,228,192\nMvaa 128,232,192\nTa 130,12,2048\nMvaa 132,236,192\n"
"s_add_u32 m0, s48, 32768\ns_add_u32 s45, s45, 128\nMaaa 0,240,192\nTa 132,13,0\nMaaa 4,244,192\nD0 2,20,45,0\nMaaa 64,248,192\nTa 134,13,2048\nMaaa 68,252,192\nMaaa 76,252,196\n"
"Ta 136,14,0\nMaaa 72,248,196\nMaaa 12,244,196\nTa 138,14,2048\nMaaa 8,240,196\nMvaa 140,236,196\nTa 140,15,0\nMvaa 136,232,196\nD0 3,20,45,1024\nMvaa 76,228,196\nTa 142,15,2048\n"
"Mvaa 72,224,196\nMvaa 80,224,200\nTa 144,16,0\nMvaa 84,228,200\nMvaa 144,232,200\nTa 146,16,2048\nMvaa 148,236,200\nMaaa 16,240,200\nTa 148,17,0\nMaaa 20,244,200\nD0 4,20,45,2048\n"
"Maaa 80,248,200\nTa 150,17,2048\nMaaa 84,252,200\nMaaa 92,252,204\nTa 152,18,0\nMaaa 88,248,204\nMaaa 28,244,204\nTa 154,18,2048\nMaaa 24,240,204\nMvaa 156,236,204\nTa 156,19,0\n"
"Mvaa 152,232,204\nD0 5,20,45,3072\nMvaa 92,228,204\nTa 158,19,2048\nMvaa 88,224,204\nMvaa 96,224,208\nRa 160,10,0\nMvaa 100,228,208\nMvaa 160,232,208\nRa 164,10,2048\n"
"Mvaa 164,236,208\ns_add_u32 m0, s48, 36864\ns_add_u32 s46, s45, 0x10000\nMaaa 32,240,208\nRa 168,10,8192\nMaaa 36,244,208\nD0 2,20,46,0\nMaaa 96,248,208\nRa 172,10,10240\n"
"Maaa 100,252,208\nMaaa 108,252,212\nRa 176,10,16384\nMaaa 104,248,212\nMaaa 44,244,212\nRa 180,10,18432\nMaaa 40,240,212\nMvaa 172,236,212\nRa 184,10,24576\nMvaa 168,232,212\n"
"D0 3,20,46,1024\nMvaa 108,228,212\nRa 188,10,26624\nMvaa 104,224,212\nMvaa 112,224,216\nMvaa 116,228,216\nMvaa 176,232,216\nMvaa 180,236,216\nMaaa 48,240,216\nMaaa 52,244,216\n"
"D0 4,20,46,2048\nMaaa 112,248,216\nMaaa 116,252,216\nMaaa 124,252,220\nMaaa 120,248,220\nMaaa 60,244,220\nMaaa 56,240,220\nMvaa 188,236,220\nMvaa 184,232,220\nD0 5,20,46,3072\n"
"Mvaa 124,228,220\nMvaa 120,224,220\ns_waitcnt vmcnt(15) lgkmcnt(0)\ns_barrier\nMvaa 64,160,128\nTa 192,12,16384\nMvaa 68,164,128\nMvaa 128,168,128\nTa 194,12,18432\n"
"Mvaa 132,172,128\ns_add_u32 m0, s47, 49152\ns_add_u32 s44, s44, 0x40000\nMaaa 0,176,128\nTa 196,13,16384\nMaaa 4,180,128\nD0 6,24,44,0\nMaaa 64,184,128\nTa 198,13,18432\n"
"Maaa 68,188,128\nMaaa 76,188,132\nTa 200,14,16384\nMaaa 72,184,132\nMaaa 12,180,132\nTa 202,14,18432\nMaaa 8,176,132\nMvaa 140,172,132\nTa 204,15,16384\nMvaa 136,168,132\n"
"D0 7,24,44,1024\nMvaa 76,164,132\nTa 206,15,18432\nMvaa 72,160,132\nMvaa 80,160,136\nTa 208,16,16384\nMvaa 84,164,136\nMvaa 144,168,136\nTa 210,16,18432\nMvaa 148,172,136\n"
"Maaa 16,176,136\nTa 212,17,16384\nMaaa 20,180,136\nD0 8,24,44,2048\nMaaa 80,184,136\nTa 214,17,18432\nMaaa 84,188,136\nMaaa 92,188,140\nTa 216,18,16384\nMaaa 88,184,140\n"
"Maaa 28,180,140\nTa 218,18,18432\nMaaa 24,176,140\nMvaa 156,172,140\nTa 220,19,16384\nMvaa 152,168,140\nD0 9,24,44,3072\nMvaa 92,164,140\nTa 222,19,18432\nMvaa 88,160,140\n"
"Mvaa 96,160,144\nRa 224,11,0\nMvaa 100,164,144\nMvaa 160,168,144\nRa 228,11,2048\nMvaa 164,172,144\ns_add_u32 m0, s47, 0\ns_add_u32 s44, s44, 0x40000\nMaaa 32,176,144\n"
"Ra 232,11,8192\nMaaa 36,180,144\nD0 6,24,44,0\nMaaa 96,184,144\nRa 236,11,10240\nMaaa 100,188,144\nMaaa 108,188,148\nRa 240,11,16384\nMaaa 104,184,148\nMaaa 44,180,148\n"
"Ra 244,11,18432\nMaaa 40,176,148\nMvaa 172,172,148\nRa 248,11,24576\nMvaa 168,168,148\nD0 7,24,44,1024\nMvaa 108,164,148\nRa 252,11,26624\nMvaa 104,160,148\nMvaa 112,160,152\n"
"Mvaa 116,164,152\nMvaa 176,168,152\nMvaa 180,172,152\nMaaa 48,176,152\nMaaa 52,180,152\nD0 8,24,44,2048\nMaaa 112,184,152\nMaaa 116,188,152\nMaaa 124,188,156\nMaaa 120,184,156\n"
"Maaa 60,180,156\nMaaa 56,176,156\nMvaa 188,172,156\nMvaa 184,168,156\nD0 9,24,44,3072\nMvaa 124,164,156\nMvaa 120,160,156\ns_waitcnt vmcnt(8) lgkmcnt(0)\ns_barrier\n"
"Mvaa 64,224,192\nTa 128,12,32768\nv_lshrrev_b32 v32, 2, v0\nMvaa 68,228,192\nv_mul_u32_u24 v32, 896, v32\nMvaa 128,232,192\nTa 130,12,34816\nv_and_b32 v33, 3, v0\n"
"Mvaa 132,236,192\ns_add_u32 m0, s48, 0\ns_add_u32 s45, s45, 128\nMaaa 0,240,192\nTa 132,13,32768\nv_lshl_add_u32 v32, v33, 4, v32\nMaaa 4,244,192\nD0 2,20,45,0\nMaaa 64,248,192\n"
"Ta 134,13,34816\ns_lshr_b32 s2, s11, 1\nMaaa 68,252,192\ns_mul_i32 s3, s2, 448\nMaaa 76,252,196\nTa 136,14,32768\ns_and_b32 s5, s11, 1\nMaaa 72,248,196\n"
"s_lshl3_add_u32 s3, s5, s3\nMaaa 12,244,196\nTa 138,14,34816\ns_add_u32 s3, s3, 131072\nMaaa 8,240,196\nv_add_u32 v32, s3, v32\nMvaa 140,236,196\nTa 140,15,32768\nTv 36,32,0\n"
"Mvaa 136,232,196\nD0 3,20,45,1024\nMvaa 76,228,196\nTa 142,15,34816\nTv 38,32,14336\nMvaa 72,224,196\nTv 40,32,64\nMvaa 80,224,200\nTa 144,16,32768\nTv 42,32,14400\n"
"Mvaa 84,228,200\nTv 44,32,128\nMvaa 144,232,200\nTa 146,16,34816\nTv 46,32,14464\nMvaa 148,236,200\nTv 48,32,192\nMaaa 16,240,200\nTa 148,17,32768\nTv 50,32,14528\n"
"Maaa 20,244,200\nD0 4,20,45,2048\nMaaa 80,248,200\nTa 150,17,34816\nTv 52,32,256\nMaaa 84,252,200\nTv 54,32,14592\nMaaa 92,252,204\nTa 152,18,32768\nTv 56,32,320\nMaaa 88,248,204\n"
"Tv 58,32,14656\nMaaa 28,244,204\nTa 154,18,34816\nTv 60,32,384\nMaaa 24,240,204\nTv 62,32,14720\nMvaa 156,236,204\nTa 156,19,32768\ns_mul_i32 s3, s2, 229376\nMvaa 152,232,204\n"
"D0 5,20,45,3072\nMvaa 92,228,204\nTa 158,19,34816\ns_add_u32 s3, s3, s72\nMvaa 88,224,204\ns_add_u32 s3, s3, 0x1400000\nMvaa 96,224,208\nRa 160,10,32768\ns_lshr_b32 s0, s4, 1\n"
"Mvaa 100,228,208\ns_mul_i32 s0, s0, 28\nMvaa 160,232,208\nRa 164,10,34816\ns_lshl1_add_u32 s0, s5, s0\nMvaa 164,236,208\ns_add_u32 m0, s48, 4096\ns_add_u32 s46, s45, 0x10000\n"
"Maaa 32,240,208\nRa 168,10,40960\ns_and_b32 s6, s4, 1\nMaaa 36,244,208\nD0 2,20,46,0\nMaaa 96,248,208\nRa 172,10,43008\ns_add_u32 s0, s0, s6\nMaaa 100,252,208\n"
"s_lshr_b32 s1, s4, 2\nMaaa 108,252,212\nRa 176,10,49152\ns_mul_i32 s1, s1, 56\nMaaa 104,248,212\ns_lshl2_add_u32 s1, s5, s1\nMaaa 44,244,212\nRa 180,10,51200\ns_and_b32 s6, s4, 3\n"
"Maaa 40,240,212\ns_add_u32 s1, s1, s6\nMvaa 172,236,212\nRa 184,10,57344\ns_and_b32 s4, s12, 1\nMvaa 168,232,212\nD0 3,20,46,1024\nMvaa 108,228,212\nRa 188,10,59392\n"
"s_mov_b32 s7, 0x6543210\nMvaa 104,224,212\ns_cmp_eq_u32 s4, s2\ns_cselect_b32 s6, s0, s1\ns_cselect_b32 s5, 4, 8\ns_cselect_b32 s7, 0x6352410, s7\nMvaa 112,224,216\nWL 14\n"
"s_lshr_b32 s0, s6, 4\nMvaa 116,228,216\ns_lshl_b32 s0, s0, 2\nMvaa 176,232,216\ns_lshr_b32 s0, s7, s0\ns_and_b32 s0, s0, 7\nMvaa 180,236,216\ns_lshl_b32 s0, s0, 5\n"
"Maaa 48,240,216\ns_and_b32 s1, s6, 15\ns_add_u32 s0, s0, s1\nMaaa 52,244,216\nD0 4,20,46,2048\nMaaa 112,248,216\ns_and_b32 s1, s6, 8\ns_add_u32 s0, s0, s1\nMaaa 116,252,216\n"
"s_lshl_b32 s0, s0, 10\nMaaa 124,252,220\ns_add_u32 s0, s0, s3\nSv 36,1,16,0,0\nMaaa 120,248,220\ns_add_u32 s6, s6, s5\nMaaa 60,244,220\nMaaa 56,240,220\nMvaa 188,236,220\n"
"Mvaa 184,232,220\nD0 5,20,46,3072\nMvaa 124,228,220\nMvaa 120,224,220\ns_waitcnt vmcnt(13) lgkmcnt(0)\ns_barrier\nMvaa 64,160,128\nTa 192,12,49152\nMvaa 68,164,128\n"
"s_lshr_b32 s0, s6, 4\nMvaa 128,168,128\nTa 194,12,51200\ns_lshl_b32 s0, s0, 2\nMvaa 132,172,128\ns_add_u32 m0, s47, 16384\ns_add_u32 s44, s44, 0x40000\nMaaa 0,176,128\n"
"Ta 196,13,49152\ns_lshr_b32 s0, s7, s0\nMaaa 4,180,128\nD0 6,24,44,0\nMaaa 64,184,128\nTa 198,13,51200\ns_and_b32 s0, s0, 7\nMaaa 68,188,128\ns_lshl_b32 s0, s0, 5\n"
"Maaa 76,188,132\nTa 200,14,49152\ns_and_b32 s1, s6, 15\nMaaa 72,184,132\ns_add_u32 s0, s0, s1\nMaaa 12,180,132\nTa 202,14,51200\ns_and_b32 s1, s6, 8\nMaaa 8,176,132\n"
"s_add_u32 s0, s0, s1\nMvaa 140,172,132\nTa 204,15,49152\ns_lshl_b32 s0, s0, 10\nMvaa 136,168,132\nD0 7,24,44,1024\nMvaa 76,164,132\nTa 206,15,51200\ns_add_u32 s0, s0, s3\n"
"Mvaa 72,160,132\nSv 40,1,16,0,0\nMvaa 80,160,136\nTa 208,16,49152\ns_add_u32 s6, s6, s5\nMvaa 84,164,136\nMvaa 144,168,136\nTa 210,16,51200\nMvaa 148,172,136\nMaaa 16,176,136\n"
"Ta 212,17,49152\nMaaa 20,180,136\nD0 8,24,44,2048\nMaaa 80,184,136\nTa 214,17,51200\nMaaa 84,188,136\nMaaa 92,188,140\nTa 216,18,49152\ns_lshr_b32 s0, s6, 4\nMaaa 88,184,140\n"
"s_lshl_b32 s0, s0, 2\nMaaa 28,180,140\nTa 218,18,51200\ns_lshr_b32 s0, s7, s0\nMaaa 24,176,140\ns_and_b32 s0, s0, 7\nMvaa 156,172,140\nTa 220,19,49152\ns_lshl_b32 s0, s0, 5\n"
"Mvaa 152,168,140\nD0 9,24,44,3072\nMvaa 92,164,140\nTa 222,19,51200\ns_and_b32 s1, s6, 15\nMvaa 88,160,140\ns_add_u32 s0, s0, s1\nMvaa 96,160,144\nRa 224,11,32768\n"
"s_and_b32 s1, s6, 8\nMvaa 100,164,144\ns_add_u32 s0, s0, s1\nMvaa 160,168,144\nRa 228,11,34816\ns_lshl_b32 s0, s0, 10\nMvaa 164,172,144\ns_add_u32 m0, s47, 32768\n"
"s_add_u32 s44, s44, 0x40000\nMaaa 32,176,144\nRa 232,11,40960\ns_add_u32 s0, s0, s3\nMaaa 36,180,144\nD0 6,24,44,0\nMaaa 96,184,144\nRa 236,11,43008\nSv 44,1,16,0,0\n"
"Maaa 100,188,144\ns_add_u32 s6, s6, s5\nMaaa 108,188,148\nRa 240,11,49152\nMaaa 104,184,148\nMaaa 44,180,148\nRa 244,11,51200\nMaaa 40,176,148\nMvaa 172,172,148\nRa 248,11,57344\n"
"Mvaa 168,168,148\nD0 7,24,44,1024\nMvaa 108,164,148\nRa 252,11,59392\nMvaa 104,160,148\ns_lshr_b32 s0, s6, 4\nMvaa 112,160,152\ns_lshl_b32 s0, s0, 2\ns_lshr_b32 s0, s7, s0\n"
"Mvaa 116,164,152\ns_and_b32 s0, s0, 7\nMvaa 176,168,152\ns_lshl_b32 s0, s0, 5\ns_and_b32 s1, s6, 15\nMvaa 180,172,152\ns_add_u32 s0, s0, s1\nMaaa 48,176,152\ns_and_b32 s1, s6, 8\n"
"s_add_u32 s0, s0, s1\nMaaa 52,180,152\nD0 8,24,44,2048\nMaaa 112,184,152\ns_lshl_b32 s0, s0, 10\ns_add_u32 s0, s0, s3\nMaaa 116,188,152\nSv 48,1,16,0,0\nMaaa 124,188,156\n"
"s_add_u32 s6, s6, s5\nMaaa 120,184,156\nMaaa 60,180,156\nMaaa 56,176,156\nMvaa 188,172,156\nMvaa 184,168,156\nD0 9,24,44,3072\nMvaa 124,164,156\nMvaa 120,160,156\n"
"s_waitcnt vmcnt(11) lgkmcnt(0)\ns_barrier\nMvaa 64,224,192\nTa 128,12,0\ns_lshr_b32 s0, s6, 4\nMvaa 68,228,192\ns_lshl_b32 s0, s0, 2\nMvaa 128,232,192\nTa 130,12,2048\n"
"s_lshr_b32 s0, s7, s0\nMvaa 132,236,192\ns_add_u32 m0, s48, 32768\ns_add_u32 s45, s45, 128\nMaaa 0,240,192\nTa 132,13,0\ns_and_b32 s0, s0, 7\nMaaa 4,244,192\nD0 2,20,45,0\n"
"Maaa 64,248,192\nTa 134,13,2048\ns_lshl_b32 s0, s0, 5\nMaaa 68,252,192\ns_and_b32 s1, s6, 15\nMaaa 76,252,196\nTa 136,14,0\ns_add_u32 s0, s0, s1\nMaaa 72,248,196\n"
"s_and_b32 s1, s6, 8\nMaaa 12,244,196\nTa 138,14,2048\ns_add_u32 s0, s0, s1\nMaaa 8,240,196\ns_lshl_b32 s0, s0, 10\nMvaa 140,236,196\nTa 140,15,0\ns_add_u32 s0, s0, s3\n"
"Mvaa 136,232,196\nD0 3,20,45,1024\nMvaa 76,228,196\nTa 142,15,2048\nSv 52,1,16,0,0\nMvaa 72,224,196\ns_add_u32 s6, s6, s5\nMvaa 80,224,200\nTa 144,16,0\nMvaa 84,228,200\n"
"Mvaa 144,232,200\nTa 146,16,2048\nMvaa 148,236,200\nMaaa 16,240,200\nTa 148,17,0\nMaaa 20,244,200\nD0 4,20,45,2048\nMaaa 80,248,200\nTa 150,17,2048\nMaaa 84,252,200\n"
"s_lshr_b32 s0, s6, 4\nMaaa 92,252,204\nTa 152,18,0\ns_lshl_b32 s0, s0, 2\nMaaa 88,248,204\ns_lshr_b32 s0, s7, s0\nMaaa 28,244,204\nTa 154,18,2048\ns_and_b32 s0, s0, 7\n"
"Maaa 24,240,204\ns_lshl_b32 s0, s0, 5\nMvaa 156,236,204\nTa 156,19,0\ns_and_b32 s1, s6, 15\nMvaa 152,232,204\nD0 5,20,45,3072\nMvaa 92,228,204\nTa 158,19,2048\n"
"s_add_u32 s0, s0, s1\nMvaa 88,224,204\ns_and_b32 s1, s6, 8\nMvaa 96,224,208\nRa 160,10,0\ns_add_u32 s0, s0, s1\nMvaa 100,228,208\ns_lshl_b32 s0, s0, 10\nMvaa 160,232,208\n"
"Ra 164,10,2048\ns_add_u32 s0, s0, s3\nMvaa 164,236,208\ns_add_u32 m0, s48, 36864\ns_add_u32 s46, s45, 0x10000\nMaaa 32,240,208\nRa 168,10,8192\nSv 56,1,16,0,0\nMaaa 36,244,208\n"
"D0 2,20,46,0\nMaaa 96,248,208\nRa 172,10,10240\ns_add_u32 s6, s6, s5\nMaaa 100,252,208\nMaaa 108,252,212\nRa 176,10,16384\nMaaa 104,248,212\nMaaa 44,244,212\nRa 180,10,18432\n"
"Maaa 40,240,212\nMvaa 172,236,212\nRa 184,10,24576\nMvaa 168,232,212\nD0 3,20,46,1024\nMvaa 108,228,212\nRa 188,10,26624\ns_lshr_b32 s0, s6, 4\nMvaa 104,224,212\n"
"s_lshl_b32 s0, s0, 2\nMvaa 112,224,216\ns_lshr_b32 s0, s7, s0\ns_and_b32 s0, s0, 7\nMvaa 116,228,216\ns_lshl_b32 s0, s0, 5\nMvaa 176,232,216\ns_and_b32 s1, s6, 15\n"
"s_add_u32 s0, s0, s1\nMvaa 180,236,216\ns_and_b32 s1, s6, 8\nMaaa 48,240,216\ns_add_u32 s0, s0, s1\ns_lshl_b32 s0, s0, 10\nMaaa 52,244,216\nD0 4,20,46,2048\nMaaa 112,248,216\n"
"s_add_u32 s0, s0, s3\nSv 60,1,16,0,0\nMaaa 116,252,216\ns_add_u32 s6, s6, s5\nMaaa 124,252,220\nMaaa 120,248,220\nMaaa 60,244,220\nMaaa 56,240,220\nMvaa 188,236,220\n"
"Mvaa 184,232,220\nD0 5,20,46,3072\nMvaa 124,228,220\nMvaa 120,224,220\ns_waitcnt vmcnt(17) lgkmcnt(0)\ns_barrier\nMvaa 64,160,128\nTa 192,12,16384\ns_lshr_b32 s3, s11, 1\n"
"Mvaa 68,164,128\ns_lshl_b32 s3, s3, 13\ns_add_u32 m0, s47, 49152\ns_add_u32 s44, s44, 0x40000\nMvaa 128,168,128\nTa 194,12,18432\ns_lshl_b32 s2, s11, 12\nMvaa 132,172,128\n"
"s_add_u32 s2, s2, s3\ns_add_u32 s2, s2, s72\nD0 6,24,44,0\nMaaa 0,176,128\nTa 196,13,16384\nMaaa 4,180,128\ns_lshl_b32 s3, s11, 12\ns_add_u32 s89, s3, 0x0\nMaaa 64,184,128\n"
"Ta 198,13,18432\ns_mov_b32 s88, 0xffff8000\nMaaa 68,188,128\ns_lshl_b32 s4, s13, 1\nD0 7,24,44,1024\nMaaa 76,188,132\nTa 200,14,16384\ns_sub_u32 s79, 15, s4\nMaaa 72,184,132\n"
"s_and_b32 s5, s12, 1\ns_mul_i32 s5, s5, 229376\nMaaa 12,180,132\nTa 202,14,18432\nMaaa 8,176,132\ns_add_u32 s5, s5, s2\ns_add_u32 s96, s5, 0x1310000\nD0 8,24,44,2048\n"
"Mvaa 140,172,132\nTa 204,15,16384\ns_mov_b32 s63, 0x53800000\nMvaa 136,168,132\ns_mov_b32 s64, 0x2b800000\nMvaa 76,164,132\nTa 206,15,18432\ns_mov_b32 s98, s4\nMvaa 72,160,132\n"
"s_lshl_b32 s5, s13, 5\ns_add_u32 s5, s5, s11\nD0 9,24,44,3072\nMvaa 80,160,136\nTa 208,16,16384\ns_lshl_b32 s65, s5, 9\nMvaa 84,164,136\ns_add_u32 s65, s65, s72\n"
"s_add_u32 s6, s56, 0\nMvaa 144,168,136\nTa 210,16,18432\ns_add_u32 s65, s65, 0x1580000\nMvaa 148,172,136\ns_and_b32 s66, s72, 0x2000\ns_add_u32 s66, s66, s56\n"
"buffer_load_dwordx4 v[192:195], v1, s[16:19], s6 offen offset:0 sc1\nMaaa 16,176,136\nTa 212,17,16384\nMaaa 20,180,136\ns_add_u32 s66, s66, 0x100000\ns_and_b32 s5, s12, 1\n"
"Maaa 80,184,136\nTa 214,17,18432\ns_mul_i32 s58, s5, 0x202000\nMaaa 84,188,136\ns_xor_b32 s58, s58, s72\nbuffer_load_dwordx4 v[196:199], v1, s[16:19], s6 offen offset:1024 sc1\n"
"Maaa 92,188,140\nTa 216,18,16384\ns_add_u32 s58, s58, 0x15c0000\nMaaa 88,184,140\ns_lshl_b32 s5, s5, 8\ns_add_u32 s61, s58, s5\nMaaa 28,180,140\nTa 218,18,18432\nMaaa 24,176,140\n"
"s_lshl4_add_u32 s60, s13, s61\ns_add_u32 s54, s60, 0\nbuffer_load_dwordx4 v[200:203], v1, s[16:19], s6 offen offset:2048 sc1\nMvaa 156,172,140\nTa 220,19,16384\n"
"s_xor_b32 s60, s54, 0x100\nMvaa 152,168,140\ns_cmp_eq_u32 s11, 0\ns_cselect_b32 s54, s54, 0x40000000\nMvaa 92,164,140\nTa 222,19,18432\ns_getpc_b64 s[8:9]\n.PC122:\n"
"s_add_u32 s8, s8, .HD117-.PC122\ns_addc_u32 s9, s9, 0\nMvaa 88,160,140\ns_mov_b32 s97, 0xff800000\ns_mov_b32 s90, 3\n"
"buffer_load_dwordx4 v[204:207], v1, s[16:19], s6 offen offset:3072 sc1\nMvaa 96,160,144\nRa 224,11,0\ns_lshl_b32 s5, s11, 2\nMvaa 100,164,144\ns_lshr_b32 s92, 0xf112, s5\n"
"s_add_u32 s6, s56, 4096\nMvaa 160,168,144\nRa 228,11,2048\ns_and_b32 s92, s92, 15\nMvaa 164,172,144\ns_lshr_b32 s93, s11, 1\ns_xor_b32 s93, s93, 1\n"
"buffer_load_dwordx4 v[208:211], v1, s[16:19], s6 offen offset:0 sc1\nMaaa 32,176,144\nRa 232,11,8192\nMaaa 36,180,144\ns_add_u32 s86, s2, 0x20000\ns_add_u32 s87, s2, 0x410000\n"
"Maaa 96,184,144\nRa 236,11,10240\nMaaa 100,188,144\ns_cmp_eq_u32 s13, 7\ns_cselect_b32 s5, 0xfffffffd, 64\nbuffer_load_dwordx4 v[212:215], v1, s[16:19], s6 offen offset:1024 sc1\n"
"Maaa 108,188,148\nRa 240,11,16384\ns_add_u32 s92, s92, s5\nMaaa 104,184,148\ns_add_u32 s93, s93, s5\ns_or_b32 s92, s92, 0x100\nMaaa 44,180,148\nRa 244,11,18432\n"
"v_and_b32 v27, 0x70, v1\nbuffer_load_dword v27, v27, s[16:19], s61 offen offset:128 sc0 sc1\nMaaa 40,176,148\ns_lshr_b32 s2, s13, 2\ns_bfe_u32 s3, s13, 0x10001\n"
"buffer_load_dwordx4 v[216:219], v1, s[16:19], s6 offen offset:2048 sc1\nMvaa 172,172,148\nRa 248,11,24576\ns_mul_i32 s3, s3, 17\nMvaa 168,168,148\ns_add_u32 s2, s2, s3\n"
"Mvaa 108,164,148\nRa 252,11,26624\ns_lshl2_add_u32 s3, s14, 0\nMvaa 104,160,148\ns_lshl_b32 s3, s3, 2\ns_lshr_b32 s3, 0xe420ca86, s3\n"
"buffer_load_dwordx4 v[220:223], v1, s[16:19], s6 offen offset:3072 sc1\nMvaa 112,160,152\ns_and_b32 s3, s3, 15\ns_add_u32 s2, s2, s3\nMvaa 116,164,152\ns_cmp_eq_u32 s2, 32\n"
"s_cselect_b32 s4, 16, s2\ns_add_u32 s6, s56, 16384\nMvaa 176,168,152\ns_mul_i32 s75, s4, 65536\ns_lshr_b32 s2, s13, 2\nMvaa 180,172,152\ns_bfe_u32 s3, s13, 0x10001\n"
"s_mul_i32 s3, s3, 17\nbuffer_load_dwordx4 v[224:227], v1, s[16:19], s6 offen offset:0 sc1\nMaaa 48,176,152\ns_add_u32 s2, s2, s3\nMaaa 52,180,152\ns_lshl2_add_u32 s3, s14, 1\n"
"s_lshl_b32 s3, s3, 2\nMaaa 112,184,152\ns_lshr_b32 s3, 0xe420ca86, s3\ns_and_b32 s3, s3, 15\nMaaa 116,188,152\ns_add_u32 s2, s2, s3\ns_cmp_eq_u32 s2, 32\ns_cselect_b32 s4, 16, s2\n"
"buffer_load_dwordx4 v[228:231], v1, s[16:19], s6 offen offset:1024 sc1\nMaaa 124,188,156\ns_mul_i32 s76, s4, 65536\ns_lshr_b32 s2, s13, 2\nMaaa 120,184,156\n"
"s_bfe_u32 s3, s13, 0x10001\ns_mul_i32 s3, s3, 17\nMaaa 60,180,156\ns_add_u32 s2, s2, s3\nMaaa 56,176,156\ns_lshl2_add_u32 s3, s14, 2\ns_lshl_b32 s3, s3, 2\n"
"buffer_load_dwordx4 v[232:235], v1, s[16:19], s6 offen offset:2048 sc1\nMvaa 188,172,156\ns_lshr_b32 s3, 0xe420ca86, s3\ns_and_b32 s3, s3, 15\nMvaa 184,168,156\n"
"s_add_u32 s2, s2, s3\ns_cmp_eq_u32 s2, 32\ns_cselect_b32 s4, 16, s2\nMvaa 124,164,156\ns_mul_i32 s77, s4, 65536\ns_lshr_b32 s2, s13, 2\nMvaa 120,160,156\n"
"s_waitcnt vmcnt(16) lgkmcnt(0)\ns_barrier\nMvaa 64,224,192\nTa 128,12,32768\ns_bfe_u32 s3, s13, 0x10001\nbuffer_load_dwordx4 v[236:239], v1, s[16:19], s6 offen offset:3072 sc1\n"
"Mvaa 68,228,192\ns_mul_i32 s3, s3, 17\nMvaa 128,232,192\nTa 130,12,34816\ns_add_u32 s2, s2, s3\ns_add_u32 s6, s56, 20480\nMvaa 132,236,192\ns_lshl2_add_u32 s3, s14, 3\n"
"s_lshl_b32 s3, s3, 2\nMaaa 0,240,192\nTa 132,13,32768\nbuffer_load_dwordx4 v[240:243], v1, s[16:19], s6 offen offset:0 sc1\nMaaa 4,244,192\ns_lshr_b32 s3, 0xe420ca86, s3\n"
"s_and_b32 s3, s3, 15\nMaaa 64,248,192\nTa 134,13,34816\ns_add_u32 s2, s2, s3\nMaaa 68,252,192\ns_cmp_eq_u32 s2, 32\ns_cselect_b32 s4, 16, s2\nMaaa 76,252,196\nTa 136,14,32768\n"
"s_mul_i32 s78, s4, 65536\nbuffer_load_dwordx4 v[244:247], v1, s[16:19], s6 offen offset:1024 sc1\nMaaa 72,248,196\ns_lshr_b32 s3, s11, 1\ns_lshl_b32 s3, s3, 15\nMaaa 12,244,196\n"
"Ta 138,14,34816\nMaaa 8,240,196\ns_add_u32 s2, s72, s3\ns_and_b32 s3, s11, 1\nMvaa 140,236,196\nTa 140,15,32768\ns_lshl_b32 s3, s3, 14\n"
"buffer_load_dwordx4 v[248:251], v1, s[16:19], s6 offen offset:2048 sc1\nMvaa 136,232,196\ns_add_u32 s2, s2, s3\nMvaa 76,228,196\nTa 142,15,34816\ns_add_u32 s2, s2, 0x400000\n"
"Mvaa 72,224,196\ns_mov_b32 s55, 4096\ns_add_u32 s75, s75, s2\nMvaa 80,224,200\nTa 144,16,32768\ns_add_u32 s76, s76, s2\n"
"buffer_load_dwordx4 v[252:255], v1, s[16:19], s6 offen offset:3072 sc1\nMvaa 84,228,200\ns_add_u32 s77, s77, s2\nMvaa 144,232,200\nTa 146,16,34816\ns_add_u32 s78, s78, s2\n"
"Mvaa 148,236,200\nMaaa 16,240,200\nTa 148,17,32768\nMaaa 20,244,200\nMaaa 80,248,200\nTa 150,17,34816\nMaaa 84,252,200\nMaaa 92,252,204\nTa 152,18,32768\nMaaa 88,248,204\n"
"Maaa 28,244,204\nTa 154,18,34816\nMaaa 24,240,204\nMvaa 156,236,204\nTa 156,19,32768\nMvaa 152,232,204\nMvaa 92,228,204\nTa 158,19,34816\nMvaa 88,224,204\nMvaa 96,224,208\n"
"Ra 160,10,32768\nMvaa 100,228,208\nMvaa 160,232,208\nRa 164,10,34816\nMvaa 164,236,208\nMaaa 32,240,208\nRa 168,10,40960\nMaaa 36,244,208\nMaaa 96,248,208\nRa 172,10,43008\n"
"Maaa 100,252,208\nMaaa 108,252,212\nRa 176,10,49152\nWV 10\nv_cmp_ne_u32 vcc, s15, v27\ns_nop 0\ns_cbranch_vccnz .S123\n.S124:\ns_sub_u32 s45, s86, 131072\nMaaa 104,248,212\n"
"s_add_u32 m0, s47, 0\ns_add_u32 s46, s45, 0x0\nMaaa 44,244,212\nRa 180,10,51200\nMaaa 40,240,212\nD1 1,16,46,0\nMvaa 172,236,212\nRa 184,10,57344\nMvaa 168,232,212\n"
"Mvaa 108,228,212\nRa 188,10,59392\nMvaa 104,224,212\nD1 1,16,46,1024\nMvaa 112,224,216\nMvaa 116,228,216\nMvaa 176,232,216\ns_mov_b32 s36, s68\nMvaa 180,236,216\nD1 1,16,46,2048\n"
"Maaa 48,240,216\ns_and_b32 s37, s69, 0xffff\nMaaa 52,244,216\ns_mov_b32 s38, 0x1c0000\nMaaa 112,248,216\ns_mov_b32 s39, 0x20000\ns_and_b32 s2, s74, 15\nMaaa 116,252,216\n"
"D1 1,16,46,3072\nMaaa 124,252,220\ns_mul_i32 s3, s73, 14\nMaaa 120,248,220\ns_add_u32 s2, s2, s3\nMaaa 60,244,220\ns_lshl2_add_u32 s2, s2, s11\nMaaa 56,240,220\n"
"s_mul_i32 s3, s2, 4682\ns_lshr_b32 s3, s3, 16\nMvaa 188,236,220\ns_mul_i32 s22, s3, 14\nMvaa 184,232,220\ns_sub_u32 s22, s2, s22\nMvaa 124,228,220\ns_lshl_b32 s21, s3, 5\n"
"Mvaa 120,224,220\nWL 0\ns_barrier\nMvaa 64,160,128\nTa 192,12,49152\ns_add_u32 s21, s21, 1024\nMvaa 68,164,128\ns_add_u32 m0, s47, 32768\ns_add_u32 s46, s45, 0x8000\n"
"Mvaa 128,168,128\nTa 194,12,51200\nMvaa 132,172,128\nD1 1,16,46,0\nMaaa 0,176,128\nTa 196,13,49152\nMaaa 4,180,128\ns_xor_b32 s3, s3, 28\nMaaa 64,184,128\nTa 198,13,51200\n"
"s_bfe_u32 s41, s3, 0x30002\nMaaa 68,188,128\nD1 1,16,46,1024\ns_cmp_gt_u32 s22, 6\ns_cselect_b32 s2, 1187840, 0\ns_cselect_b32 s1, 16, 0\nMaaa 76,188,132\nTa 200,14,49152\n"
"Maaa 72,184,132\ns_add_u32 s41, s41, s1\nMaaa 12,180,132\nTa 202,14,51200\nMaaa 8,176,132\nD1 1,16,46,2048\ns_lshl4_add_u32 s41, s41, 0x15c0200\nMvaa 140,172,132\nTa 204,15,49152\n"
"Mvaa 136,168,132\ns_and_b32 s1, s3, 28\nMvaa 76,164,132\nTa 206,15,51200\nMvaa 72,160,132\nD1 1,16,46,3072\nMvaa 80,160,136\nTa 208,16,49152\ns_add_u32 s3, s3, s1\n"
"Mvaa 84,164,136\ns_add_u32 m0, s47, -65536\ns_add_u32 s46, s45, 0x10000\nMvaa 144,168,136\nTa 210,16,51200\nMvaa 148,172,136\nD1 1,16,46,0\nMaaa 16,176,136\nTa 212,17,49152\n"
"Maaa 20,180,136\ns_lshl_b32 s3, s3, 11\nMaaa 80,184,136\nTa 214,17,51200\ns_add_u32 s2, s2, s3\nMaaa 84,188,136\nD1 1,16,46,1024\nMaaa 92,188,140\nTa 216,18,49152\n"
"Maaa 88,184,140\ns_lshl_b32 s3, s22, 17\nMaaa 28,180,140\nTa 218,18,51200\nMaaa 24,176,140\nD1 1,16,46,2048\ns_add_u32 s2, s2, s3\nMvaa 156,172,140\nTa 220,19,49152\n"
"Mvaa 152,168,140\ns_add_u32 s20, s2, 0xd00000\nMvaa 92,164,140\nTa 222,19,51200\nMvaa 88,160,140\nD1 1,16,46,3072\nMvaa 96,160,144\nRa 224,11,32768\ns_lshl_b32 s2, s22, 7\n"
"Mvaa 100,164,144\ns_add_u32 m0, s47, -49152\ns_add_u32 s46, s45, 0x408000\nMvaa 160,168,144\nRa 228,11,34816\nMvaa 164,172,144\nD1 1,16,46,0\nMaaa 32,176,144\nRa 232,11,40960\n"
"Maaa 36,180,144\ns_add_u32 s40, s2, 0xc04b800\nMaaa 96,184,144\nRa 236,11,43008\ns_mul_i32 s2, s21, 896\nMaaa 100,188,144\nD1 1,16,46,1024\nMaaa 108,188,148\nRa 240,11,49152\n"
"Maaa 104,184,148\ns_lshl_b32 s3, s22, 6\nMaaa 44,180,148\nRa 244,11,51200\nMaaa 40,176,148\nD1 1,16,46,2048\ns_add_u32 s22, s2, s3\nMvaa 172,172,148\nRa 248,11,57344\n"
"Mvaa 168,168,148\ns_add_u32 s23, s22, 14336\nMvaa 108,164,148\nRa 252,11,59392\nMvaa 104,160,148\nD1 1,16,46,3072\nMvaa 112,160,152\nMvaa 116,164,152\nv_add_u32 v2, 0x10000, v1\n"
"Mvaa 176,168,152\ns_lshr_b32 s2, s59, 1\nMvaa 180,172,152\nv_add_u32 v24, s2, v2\nMaaa 48,176,152\nv_xor_b32 v26, 0x1000, v24\nMaaa 52,180,152\nv_lshrrev_b32 v20, 4, v0\n"
"Maaa 112,184,152\nv_lshlrev_b32 v20, 2, v20\nMaaa 116,188,152\nv_and_b32 v0, 15, v0\nMaaa 124,188,156\nv_sub_u32 v0, v0, v20\nMaaa 120,184,156\ns_and_b32 s2, s11, 1\n"
"Maaa 60,180,156\ns_lshl_b32 s2, s2, 6\nMaaa 56,176,156\nv_subrev_u32 v3, s2, v0\nMvaa 188,172,156\ns_xor_b32 s2, s2, 64\nMvaa 184,168,156\nv_subrev_u32 v0, s2, v0\n"
"Mvaa 124,164,156\nv_mov_b32 v4, 0\nMvaa 120,160,156\ns_waitcnt vmcnt(12) lgkmcnt(0)\ns_barrier\nMvaa 64,224,192\nv_mov_b32 v5, 0\nMvaa 68,228,192\ns_add_u32 m0, s47, 49152\n"
"s_add_u32 s46, s45, 0x400000\nMvaa 72,224,196\nv_mov_b32 v6, 0\nMvaa 76,228,196\nD1 1,16,46,0\nMvaa 80,224,200\nv_mov_b32 v7, 0\nMvaa 84,228,200\nv_mov_b32 v8, 0\nMvaa 88,224,204\n"
"v_mov_b32 v9, 0\nCV 32,64,65\nMvaa 92,228,204\nD1 1,16,46,1024\nCV 33,66,67\nMvaa 96,224,208\nv_mov_b32 v10, 0\nCV 34,68,69\nMvaa 100,228,208\nv_mov_b32 v11, 0\nCV 35,70,71\n"
"Mvaa 104,224,212\nv_mov_b32 v12, 0\nCV 36,72,73\nMvaa 108,228,212\nD1 1,16,46,2048\nCV 37,74,75\nMvaa 112,224,216\nv_mov_b32 v13, 0\nCV 38,76,77\nMvaa 116,228,216\n"
"v_mov_b32 v14, 0\nCV 39,78,79\nMvaa 120,224,220\nv_mov_b32 v15, 0\nCV 40,80,81\nMvaa 124,228,220\nD1 1,16,46,3072\nCV 41,82,83\nMvaa 128,232,192\nv_mov_b32 v16, 0\nCV 42,84,85\n"
"Mvaa 132,236,192\nv_mov_b32 v17, 0\nMvaa 136,232,196\nv_mov_b32 v18, 0\nCV 43,86,87\nMvaa 140,236,196\nv_mov_b32 v19, 0\nCV 44,88,89\nMvaa 144,232,200\nCV 45,90,91\nCV 46,92,93\n"
"Mvaa 148,236,200\nCV 47,94,95\nCV 48,96,97\nMvaa 152,232,204\nCV 49,98,99\nCV 50,100,101\nMvaa 156,236,204\nCV 51,102,103\nCV 52,104,105\nMvaa 160,232,208\nCV 53,106,107\n"
"CV 54,108,109\nMvaa 164,236,208\nCV 55,110,111\nCV 56,112,113\nSv 32,1,16,75,0\nMvaa 168,232,212\nCV 57,114,115\nCV 58,116,117\nMvaa 172,236,212\nCV 59,118,119\nCV 60,120,121\n"
"Mvaa 176,232,216\nCV 61,122,123\nCV 62,124,125\nMvaa 180,236,216\nCV 63,126,127\nCV 96,128,129\nSv 36,1,16,75,1024\nMvaa 184,232,220\nCV 97,130,131\nCV 98,132,133\n"
"Mvaa 188,236,220\nCV 99,134,135\nCV 100,136,137\nMaaa 0,240,192\nCV 101,138,139\nCV 102,140,141\nMaaa 4,244,192\nCV 103,142,143\nSv 40,1,16,75,2048\nMaaa 8,240,196\n"
"CV 104,144,145\nCV 105,146,147\nMaaa 12,244,196\nCV 106,148,149\nCV 107,150,151\nMaaa 16,240,200\nCV 108,152,153\nCV 109,154,155\nMaaa 20,244,200\nCV 110,156,157\nCV 111,158,159\n"
"Sv 44,1,16,75,3072\nMaaa 24,240,204\nCV 112,160,161\nCV 113,162,163\nMaaa 28,244,204\nCV 114,164,165\nCV 115,166,167\ns_add_u32 s75, s75, s55\nMaaa 32,240,208\nCV 116,168,169\n"
"CV 117,170,171\nMaaa 36,244,208\nCV 118,172,173\nCV 119,174,175\nSv 48,1,16,75,0\nMaaa 40,240,212\nCV 120,176,177\nCV 121,178,179\nMaaa 44,244,212\nCV 122,180,181\nCV 123,182,183\n"
"Maaa 48,240,216\nCV 124,184,185\nCV 125,186,187\nMaaa 52,244,216\nCV 126,188,189\nCV 127,190,191\nSv 52,1,16,75,1024\nMaaa 56,240,220\nv_mov_b32 v86, 0\nv_mov_b32 v87, 0\n"
"Maaa 60,244,220\nv_mov_b32 v88, 0\nv_mov_b32 v89, 0\nMaaa 64,248,192\nRa 128,24,0\nAR 92,0\nAR 93,1\nMaaa 68,252,192\nSv 56,1,16,75,2048\nCV 128,92,93\nMaaa 72,248,196\n"
"Ra 132,24,1024\nAR 92,2\nMaaa 76,252,196\nAR 93,3\nCV 129,92,93\nMaaa 80,248,200\nRa 136,24,2048\nAR 92,4\nAR 93,5\nMaaa 84,252,200\nSv 60,1,16,75,3072\nCV 130,92,93\n"
"Maaa 88,248,204\nRa 140,24,3072\nAR 92,6\nMaaa 92,252,204\nAR 93,7\nCV 131,92,93\nMaaa 96,248,208\nRa 144,26,0\nAR 92,8\nAR 93,9\nMaaa 100,252,208\nCV 132,92,93\nAR 92,10\n"
"Maaa 104,248,212\nRa 148,26,1024\nAR 93,11\nMaaa 108,252,212\nCV 133,92,93\nAR 92,12\nMaaa 112,248,216\nRa 152,26,2048\nAR 93,13\nCV 134,92,93\nMaaa 116,252,216\nAR 92,14\n"
"AR 93,15\nMaaa 120,248,220\nRa 156,26,3072\nCV 135,92,93\nMaaa 124,252,220\nWL 4\nCvav 32,128,192,4\nRa 160,24,8192\nMvav 32,132,200\nMvav 32,136,208\nRa 164,24,9216\n"
"Mvav 32,140,216\nCvav 36,140,220,8\nRa 168,24,10240\nAR 92,16\nAR 93,17\nMvav 36,136,212\ns_add_u32 m0, s89, 32768\nCV 136,92,93\nAR 92,18\nAR 93,19\nMvav 36,132,204\n"
"Ra 172,24,11264\nWL 4\ns_barrier\nMvav 36,128,196\nCV 137,92,93\nAR 92,20\nAR 93,21\nCV 138,92,93\nMvav 36,144,228\nRa 176,26,8192\nAR 92,22\nAR 93,23\nSv 96,1,16,76,0\n"
"Mvav 36,148,236\ns_add_u32 s46, s86, s88\nCV 139,92,93\nAR 92,24\nMvav 36,152,244\nRa 180,26,9216\nAR 93,25\nCV 140,92,93\nAR 92,26\nMvav 36,156,252\nAR 93,27\nCV 141,92,93\n"
"AR 92,28\nMvav 32,156,248\nRa 184,26,10240\nAR 93,29\nCV 142,92,93\nAR 92,30\nMvav 32,152,240\nD1 1,16,46,0\nAR 93,31\nCV 143,92,93\nMvav 32,148,232\nRa 188,26,11264\nWL 4\n"
"AR 92,32\nAR 93,33\nMvav 32,144,224\nCV 144,92,93\nAR 92,34\nAR 93,35\nCvav 40,160,192,12\nCV 145,92,93\nAR 92,36\nAR 93,37\nCV 146,92,93\nMvav 40,164,200\nAR 92,38\nAR 93,39\n"
"CV 147,92,93\nMvav 40,168,208\nv_and_b32 v27, 0x70, v1\nbuffer_load_dword v27, v27, s[16:19], s61 offen offset:136 sc0 sc1\nAR 92,40\nMvav 40,172,216\nAR 93,41\nCV 148,92,93\n"
"AR 92,42\nAR 93,43\nCvav 44,172,220,16\nCV 149,92,93\nAR 92,44\nAR 93,45\nMvav 44,168,212\nD1 1,16,46,1024\nCV 150,92,93\nAR 92,46\nAR 93,47\nMvav 44,164,204\nWL 0\nCV 151,92,93\n"
"AR 92,48\nMvav 44,160,196\nAR 93,49\nCV 152,92,93\nAR 92,50\nAR 93,51\nMvav 44,176,228\nCV 153,92,93\nAR 92,52\nAR 93,53\nSv 100,1,16,76,1024\nMvav 44,180,236\n"
"s_add_u32 s88, s88, 32768\nEX 80,32\nMvav 44,184,244\nCV 154,92,93\nAR 92,54\nAR 93,55\nCV 155,92,93\nMvav 44,188,252\nEX 81,33\nMvav 40,188,248\nAR 92,56\nAR 93,57\nCV 156,92,93\n"
"AR 92,58\nMvav 40,184,240\nD1 1,16,46,2048\nAR 93,59\nCV 157,92,93\nMvav 40,180,232\nAR 92,60\nAR 93,61\nCV 158,92,93\nAR 92,62\nMvav 40,176,224\nAD 84,80,81\nCV 64,80,81\n"
"AR 93,63\nCV 159,92,93\nAR 92,64\nAR 93,65\nCV 160,92,93\nEX 82,34\nAR 92,66\nAR 93,67\nCV 161,92,93\nAR 92,68\nWV 6\nEX 83,35\nAR 93,69\nAD 84,84,82\nCV 162,92,93\nAR 92,70\n"
"D1 1,16,46,3072\ns_add_u32 m0, s89, 49152\nAR 93,71\nCV 163,92,93\ns_barrier\nAR 92,72\nAR 93,73\nAD 84,84,83\nCV 65,82,83\nCV 164,92,93\nAR 92,74\nRa 128,24,32768\nAR 93,75\n"
"CV 165,92,93\nSv 104,1,16,76,2048\ns_add_u32 s46, s87, s88\nEX 80,36\nbuffer_store_dword v25, off, s[16:19], s54 offset:12 sc0 sc1\nRa 132,24,33792\nAR 92,76\nAR 93,77\n"
"CV 166,92,93\nEX 81,37\nRa 136,24,34816\nAR 92,78\nAR 93,79\nCV 167,92,93\nD1 1,16,46,0\nAR 92,80\nAR 93,81\nRa 140,24,35840\nCV 168,92,93\nAR 92,82\nAR 93,83\nAD 85,80,81\n"
"CV 68,80,81\nCV 169,92,93\nRa 144,26,32768\nAR 92,84\nAR 93,85\nCV 170,92,93\nEX 82,38\nAR 92,86\nRa 148,26,33792\nAR 93,87\nCV 171,92,93\nEX 83,39\nAR 92,88\nRa 152,26,34816\n"
"AD 85,85,82\nAR 93,89\nD1 1,16,46,1024\nCV 172,92,93\nAR 92,90\nAR 93,91\nRa 156,26,35840\nCV 173,92,93\nAR 92,92\nAD 85,85,83\nCV 69,82,83\nAR 93,93\nCV 174,92,93\n"
"Ra 160,24,40960\nAR 92,94\nAR 93,95\nSv 108,1,16,76,3072\ns_xor_b32 s89, s89, 0x10000\nEX 80,40\nRa 164,24,41984\nCV 175,92,93\nAR 92,96\nAR 93,97\nWL 6\nEX 81,41\n"
"Ra 168,24,43008\nAD 84,84,80\nCV 176,92,93\nAR 92,98\nD1 1,16,46,2048\nAR 93,99\nCV 177,92,93\nRa 172,24,44032\nAR 92,100\nAR 93,101\nCV 178,92,93\nAD 84,84,81\nCV 66,80,81\n"
"AR 92,102\nCvav 48,128,192,4\nRa 176,26,40960\nAR 93,103\nCV 179,92,93\nAR 92,104\nMvav 48,132,200\nEX 82,42\nAR 93,105\nMvav 48,136,208\nRa 180,26,41984\nCV 180,92,93\nAR 92,106\n"
"Mvav 48,140,216\nWL 6\nEX 83,43\nAR 93,107\nWV 10\nv_cmp_ne_u32 vcc, s15, v27\ns_nop 0\ns_cbranch_vccnz .S141\n.S142:\nCvav 52,140,220,8\nRa 184,26,43008\nAD 84,84,82\n"
"CV 181,92,93\nMvav 52,136,212\nD1 1,16,46,3072\ns_add_u32 m0, s89, 0\nAR 92,108\nAR 93,109\nMvav 52,132,204\nRa 188,26,44032\nCV 182,92,93\nAR 92,110\nMvav 52,128,196\n"
"AD 84,84,83\nCV 67,82,83\nAR 93,111\nCV 183,92,93\nMvav 52,144,228\nRa 128,2,49152\nAR 92,112\nAR 93,113\ns_add_u32 s76, s76, s55\nSv 112,1,16,76,0\nMvav 52,148,236\n"
"s_add_u32 s46, s86, s88\nEX 80,44\nMvav 52,152,244\nRa 132,2,50176\nv_xor_b32 v24, 0x10000, v24\nCV 184,92,93\nAR 92,114\nMvav 52,156,252\nWL 6\nEX 81,45\nMvav 48,156,248\n"
"Ra 136,2,51200\nAD 85,85,80\nAR 93,115\nCV 185,92,93\nMvav 48,152,240\nD1 1,16,46,0\nAR 92,116\nAR 93,117\nMvav 48,148,232\nRa 140,2,52224\nCV 186,92,93\nAR 92,118\nAR 93,119\n"
"Mvav 48,144,224\nAD 85,85,81\nCV 70,80,81\nCV 187,92,93\nCvav 56,160,192,12\nRa 144,2,53248\nAR 92,120\nAR 93,121\nCV 188,92,93\nMvav 56,164,200\nEX 82,46\nAR 92,122\n"
"Mvav 56,168,208\nRa 148,2,54272\nv_xor_b32 v26, 0x10000, v26\nAR 93,123\nMvav 56,172,216\nWL 6\nEX 83,47\nCV 189,92,93\nCvav 60,172,220,16\nRa 152,2,55296\nAD 85,85,82\nAR 92,124\n"
"Mvav 60,168,212\nD1 1,16,46,1024\nAR 93,125\nCV 190,92,93\nAR 92,126\nMvav 60,164,204\nRa 156,2,56320\nAR 93,127\nCV 191,92,93\nMvav 60,160,196\nAD 85,85,83\nCV 71,82,83\n"
"Mvav 60,176,228\nRa 160,2,57344\nv_max_f32 v90, v84, v85\nSv 116,1,16,76,1024\nMvav 60,180,236\ns_add_u32 s88, s88, 32768\nEX 80,48\nMvav 60,184,244\nRa 164,2,58368\n"
"v_cmp_lt_f32 vcc, s63, v90\nMvav 60,188,252\nWL 8\nEX 81,49\nMvav 56,188,248\nRa 168,2,59392\ns_cbranch_vccnz .T126\n.T127:\nv_mov_b32 v20, v84\nv_mov_b32 v21, v85\n"
"v_mov_b32 v22, v20\nv_mov_b32 v23, v21\nMvav 56,184,240\nD1 1,16,46,2048\nv_permlane32_swap_b32 v20, v22\nv_permlane32_swap_b32 v21, v23\nAD 20,20,22\nAD 21,21,23\n"
"v_mov_b32 v22, v20\nv_mov_b32 v23, v21\nMvav 56,180,232\nRa 172,2,60416\nv_permlane16_swap_b32 v20, v22\nv_permlane16_swap_b32 v21, v23\nAD 20,20,22\nAD 21,21,23\n"
"v_min_f32 v90, v20, v21\nv_cmp_gt_f32 vcc, s64, v90\ns_nop 0\ns_cbranch_vccnz .T128\n.T129:\nAD 86,86,84\nMvav 56,176,224\nAD 84,80,81\nCV 72,80,81\nZaav 0,128,64\nRa 176,2,61440\n"
"Zaav 4,128,68\nEX 82,50\nZaav 12,132,68\nRa 180,2,62464\nZaav 8,132,64\nWL 8\nEX 83,51\nZaav 16,136,64\nRa 184,2,63488\nAD 84,84,82\nZaav 20,136,68\nD1 1,16,46,3072\n"
"s_add_u32 m0, s89, 16384\nZaav 28,140,68\nRa 188,2,64512\ns_barrier\nZaav 24,140,64\nAD 84,84,83\nCV 73,82,83\nZaav 32,144,64\nRa 128,24,0\nSv 120,1,16,76,2048\nZaav 36,144,68\n"
"s_add_u32 s46, s87, s88\nEX 80,52\nZaav 44,148,68\nRa 132,24,1024\nv_xor_b32 v2, 0x10000, v2\nZaav 40,148,64\nWL 8\nEX 81,53\nZaav 48,152,64\nRa 136,24,2048\nAD 87,87,85\n"
"Zaav 52,152,68\nD1 1,16,46,0\nZaav 60,156,68\nRa 140,24,3072\nZaav 56,156,64\nAD 85,80,81\nCV 76,80,81\nZaav 64,160,64\nRa 144,26,0\nZaav 68,160,68\nEX 82,54\nZaav 76,164,68\n"
"Ra 148,26,1024\nZaav 72,164,64\nWL 8\nEX 83,55\nZaav 80,168,64\nRa 152,26,2048\nAD 85,85,82\nZaav 84,168,68\nD1 1,16,46,1024\nZaav 92,172,68\nRa 156,26,3072\nZaav 88,172,64\n"
"AD 85,85,83\nCV 77,82,83\nZaav 96,176,64\nRa 160,24,8192\nSv 124,1,16,76,3072\nZaav 100,176,68\nEX 80,56\nZaav 108,180,68\nRa 164,24,9216\nZaav 104,180,64\nWL 6\nEX 81,57\n"
"Zaav 112,184,64\nRa 168,24,10240\nAD 84,84,80\nZaav 116,184,68\nD1 1,16,46,2048\nZaav 124,188,68\nRa 172,24,11264\nZaav 120,188,64\nAD 84,84,81\nCV 74,80,81\nCvav 32,128,192,4\n"
"Ra 176,26,8192\nMvav 32,132,200\nEX 82,58\nMvav 32,136,208\nRa 180,26,9216\nMvav 32,140,216\nWL 6\nEX 83,59\nCvav 36,140,220,8\nRa 184,26,10240\nAD 84,84,82\nMvav 36,136,212\n"
"D1 1,16,46,3072\ns_add_u32 m0, s89, 32768\nMvav 36,132,204\nRa 188,26,11264\nMvav 36,128,196\nAD 84,84,83\nCV 75,82,83\nMvav 36,144,228\nRa 128,2,16384\nSv 128,1,16,77,0\n"
"Mvav 36,148,236\ns_add_u32 s46, s86, s88\nEX 80,60\nMvav 36,152,244\nRa 132,2,17408\nMvav 36,156,252\nWL 6\nEX 81,61\nMvav 32,156,248\nRa 136,2,18432\nAD 85,85,80\n"
"Mvav 32,152,240\nD1 1,16,46,0\nMvav 32,148,232\nRa 140,2,19456\nMvav 32,144,224\nAD 85,85,81\nCV 78,80,81\nCvav 40,160,192,12\nRa 144,2,20480\nMvav 40,164,200\nEX 82,62\n"
"Mvav 40,168,208\nRa 148,2,21504\nMvav 40,172,216\nWL 6\nEX 83,63\nCvav 44,172,220,16\nRa 152,2,22528\nAD 85,85,82\nMvav 44,168,212\nD1 1,16,46,1024\nMvav 44,164,204\n"
"Ra 156,2,23552\nMvav 44,160,196\nAD 85,85,83\nCV 79,82,83\nMvav 44,176,228\nRa 160,2,24576\nv_max_f32 v90, v84, v85\nSv 132,1,16,77,1024\nMvav 44,180,236\n"
"s_add_u32 s88, s88, 32768\nEX 80,32\nMvav 44,184,244\nRa 164,2,25600\nv_cmp_lt_f32 vcc, s63, v90\nMvav 44,188,252\nWL 8\nEX 81,33\nMvav 40,188,248\nRa 168,2,26624\n"
"s_cbranch_vccnz .T130\n.T131:\nMvav 40,184,240\nD1 1,16,46,2048\nMvav 40,180,232\nRa 172,2,27648\nAD 86,86,84\nMvav 40,176,224\nAD 84,80,81\nCV 64,80,81\nMaav 0,128,72\n"
"Ra 176,2,28672\nv_mov_b32 v112, -1\nMaav 4,128,76\nEX 82,34\nMaav 12,132,76\nRa 180,2,29696\nMaav 8,132,72\ns_waitcnt vmcnt(17) lgkmcnt(8)\nEX 83,35\nMaav 16,136,72\n"
"Ra 184,2,30720\nAD 84,84,82\nMaav 20,136,76\nD1 1,16,46,3072\ns_add_u32 m0, s89, 49152\nMaav 28,140,76\nRa 188,2,31744\ns_barrier\nMaav 24,140,72\nAD 84,84,83\nCV 65,82,83\n"
"Maav 32,144,72\nRa 128,24,32768\nSv 136,1,16,77,2048\nMaav 36,144,76\ns_add_u32 s46, s87, s88\nEX 80,36\nMaav 44,148,76\nRa 132,24,33792\nMaav 40,148,72\nWL 8\nEX 81,37\n"
"Maav 48,152,72\nRa 136,24,34816\nAD 87,87,85\nMaav 52,152,76\nD1 1,16,46,0\nMaav 60,156,76\nRa 140,24,35840\nMaav 56,156,72\nAD 85,80,81\nCV 68,80,81\nMaav 64,160,72\n"
"Ra 144,26,32768\nv_mov_b32 v113, -1\nMaav 68,160,76\nEX 82,38\nMaav 76,164,76\nRa 148,26,33792\nMaav 72,164,72\nWL 8\nEX 83,39\nMaav 80,168,72\nRa 152,26,34816\nAD 85,85,82\n"
"Maav 84,168,76\nD1 1,16,46,1024\nMaav 92,172,76\nRa 156,26,35840\nMaav 88,172,72\nAD 85,85,83\nCV 69,82,83\nMaav 96,176,72\nRa 160,24,40960\nSv 140,1,16,77,3072\nMaav 100,176,76\n"
"s_xor_b32 s89, s89, 0x10000\nEX 80,40\nMaav 108,180,76\nRa 164,24,41984\nMaav 104,180,72\nWL 6\nEX 81,41\nMaav 112,184,72\nRa 168,24,43008\nAD 84,84,80\nMaav 116,184,76\n"
"D1 1,16,46,2048\nMaav 124,188,76\nRa 172,24,44032\nMaav 120,188,72\nAD 84,84,81\nCV 66,80,81\nCvav 48,128,192,4\nRa 176,26,40960\nv_mov_b32 v114, -1\nMvav 48,132,200\nEX 82,42\n"
"Mvav 48,136,208\nRa 180,26,41984\nMvav 48,140,216\nWL 6\nEX 83,43\nCvav 52,140,220,8\nRa 184,26,43008\nAD 84,84,82\nv_and_b32 v27, 0xf0, v1\n"
"buffer_load_dword v27, v27, s[16:19], s61 offen offset:12 sc0 sc1\nMvav 52,136,212\nD1 1,16,46,3072\ns_add_u32 m0, s89, 0\nMvav 52,132,204\nRa 188,26,44032\nMvav 52,128,196\n"
"AD 84,84,83\nCV 67,82,83\nMvav 52,144,228\nRa 128,2,49152\ns_add_u32 s77, s77, s55\nSv 144,1,16,77,0\nMvav 52,148,236\ns_add_u32 s46, s86, s88\nEX 80,44\nMvav 52,152,244\n"
"Ra 132,2,50176\nv_xor_b32 v24, 0x10000, v24\nMvav 52,156,252\nWL 6\nEX 81,45\nMvav 48,156,248\nRa 136,2,51200\nAD 85,85,80\nMvav 48,152,240\nD1 1,16,46,0\nMvav 48,148,232\n"
"Ra 140,2,52224\nMvav 48,144,224\nAD 85,85,81\nCV 70,80,81\nCvav 56,160,192,12\nRa 144,2,53248\nv_mov_b32 v115, -1\nMvav 56,164,200\nEX 82,46\nMvav 56,168,208\nRa 148,2,54272\n"
"v_xor_b32 v26, 0x10000, v26\nMvav 56,172,216\nWL 6\nEX 83,47\nCvav 60,172,220,16\nRa 152,2,55296\nAD 85,85,82\nMvav 60,168,212\nD1 1,16,46,1024\nMvav 60,164,204\nRa 156,2,56320\n"
"Mvav 60,160,196\nAD 85,85,83\nCV 71,82,83\nMvav 60,176,228\nRa 160,2,57344\nv_max_f32 v90, v84, v85\nSv 148,1,16,77,1024\nMvav 60,180,236\ns_add_u32 s88, s88, 32768\nEX 80,48\n"
"Mvav 60,184,244\nRa 164,2,58368\nv_cmp_lt_f32 vcc, s63, v90\nMvav 60,188,252\nWL 8\nEX 81,49\nMvav 56,188,248\nRa 168,2,59392\ns_cbranch_vccnz .T132\n.T133:\nMvav 56,184,240\n"
"D1 1,16,46,2048\nMvav 56,180,232\nRa 172,2,60416\nAD 86,86,84\nMvav 56,176,224\nAD 84,80,81\nCV 72,80,81\nMaav 0,128,64\nRa 176,2,61440\nMaav 4,128,68\nEX 82,50\nMaav 12,132,68\n"
"Ra 180,2,62464\nMaav 8,132,64\ns_waitcnt vmcnt(18) lgkmcnt(8)\nEX 83,51\nMaav 16,136,64\nRa 184,2,63488\nAD 84,84,82\nMaav 20,136,68\nD1 1,16,46,3072\ns_add_u32 m0, s89, 16384\n"
"Maav 28,140,68\nRa 188,2,64512\ns_barrier\nMaav 24,140,64\nAD 84,84,83\nCV 73,82,83\nMaav 32,144,64\nRa 128,24,0\nSv 152,1,16,77,2048\nMaav 36,144,68\ns_add_u32 s46, s87, s88\n"
"EX 80,52\nMaav 44,148,68\nRa 132,24,1024\nv_xor_b32 v2, 0x10000, v2\nMaav 40,148,64\nWL 8\nEX 81,53\nMaav 48,152,64\nRa 136,24,2048\nAD 87,87,85\nMaav 52,152,68\nD1 1,16,46,0\n"
"Maav 60,156,68\nRa 140,24,3072\nMaav 56,156,64\nAD 85,80,81\nCV 76,80,81\nMaav 64,160,64\nRa 144,26,0\nMaav 68,160,68\nEX 82,54\nMaav 76,164,68\nRa 148,26,1024\nMaav 72,164,64\n"
"WL 8\nEX 83,55\nMaav 80,168,64\nRa 152,26,2048\nAD 85,85,82\nMaav 84,168,68\nD1 1,16,46,1024\nMaav 92,172,68\nRa 156,26,3072\nMaav 88,172,64\nAD 85,85,83\nCV 77,82,83\n"
"Maav 96,176,64\nRa 160,24,8192\nSv 156,1,16,77,3072\nMaav 100,176,68\nEX 80,56\nMaav 108,180,68\nRa 164,24,9216\nMaav 104,180,64\nWL 6\nEX 81,57\nMaav 112,184,64\nRa 168,24,10240\n"
"AD 84,84,80\nMaav 116,184,68\nD1 1,16,46,2048\nMaav 124,188,68\nRa 172,24,11264\nMaav 120,188,64\nAD 84,84,81\nCV 74,80,81\nCvav 32,128,192,4\nRa 176,26,8192\nMvav 32,132,200\n"
"EX 82,58\nMvav 32,136,208\nRa 180,26,9216\nMvav 32,140,216\nWL 6\nEX 83,59\nCvav 36,140,220,8\nRa 184,26,10240\nAD 84,84,82\nMvav 36,136,212\nD1 1,16,46,3072\n"
"s_add_u32 m0, s89, 32768\nMvav 36,132,204\nRa 188,26,11264\nMvav 36,128,196\nAD 84,84,83\nCV 75,82,83\nMvav 36,144,228\nRa 128,2,16384\nSv 160,1,16,78,0\nMvav 36,148,236\n"
"s_add_u32 s46, s86, s88\nEX 80,60\nMvav 36,152,244\nRa 132,2,17408\nMvav 36,156,252\nWL 6\nEX 81,61\nMvav 32,156,248\nRa 136,2,18432\nAD 85,85,80\nMvav 32,152,240\nD1 1,16,46,0\n"
"Mvav 32,148,232\nRa 140,2,19456\nSv 112,1,16,49,0\nMvav 32,144,224\nAD 85,85,81\nCV 78,80,81\nCvav 40,160,192,12\nRa 144,2,20480\nMvav 40,164,200\nEX 82,62\nMvav 40,168,208\n"
"Ra 148,2,21504\nMvav 40,172,216\nWL 6\nEX 83,63\nCvav 44,172,220,16\nRa 152,2,22528\nAD 85,85,82\nMvav 44,168,212\nD1 1,16,46,1024\nMvav 44,164,204\nRa 156,2,23552\n"
"Mvav 44,160,196\nAD 85,85,83\nCV 79,82,83\nMvav 44,176,228\nRa 160,2,24576\nv_max_f32 v90, v84, v85\nSv 164,1,16,78,1024\nMvav 44,180,236\ns_add_u32 s88, s88, 32768\nEX 80,32\n"
"Mvav 44,184,244\nRa 164,2,25600\nv_cmp_lt_f32 vcc, s63, v90\nMvav 44,188,252\nWL 8\nEX 81,33\nMvav 40,188,248\nRa 168,2,26624\ns_cbranch_vccnz .T134\n.T135:\nMvav 40,184,240\n"
"D1 1,16,46,2048\nMvav 40,180,232\nRa 172,2,27648\nAD 86,86,84\nMvav 40,176,224\nAD 84,80,81\nCV 64,80,81\nMaav 0,128,72\nRa 176,2,28672\nMaav 4,128,76\nEX 82,34\nMaav 12,132,76\n"
"Ra 180,2,29696\nMaav 8,132,72\ns_waitcnt vmcnt(18) lgkmcnt(8)\nEX 83,35\nMaav 16,136,72\nRa 184,2,30720\nAD 84,84,82\nMaav 20,136,76\nD1 1,16,46,3072\ns_add_u32 m0, s89, 49152\n"
"Maav 28,140,76\nRa 188,2,31744\ns_barrier\nMaav 24,140,72\nAD 84,84,83\nCV 65,82,83\nMaav 32,144,72\nRa 128,24,32768\nSv 168,1,16,78,2048\nMaav 36,144,76\ns_add_u32 s46, s87, s88\n"
"EX 80,36\nMaav 44,148,76\nRa 132,24,33792\nMaav 40,148,72\nWL 8\nEX 81,37\nMaav 48,152,72\nRa 136,24,34816\nAD 87,87,85\nMaav 52,152,76\nD1 1,16,46,0\nMaav 60,156,76\n"
"Ra 140,24,35840\nMaav 56,156,72\nAD 85,80,81\nCV 68,80,81\nMaav 64,160,72\nRa 144,26,32768\nMaav 68,160,76\nEX 82,38\nMaav 76,164,76\nRa 148,26,33792\nMaav 72,164,72\nWL 8\n"
"EX 83,39\nMaav 80,168,72\nRa 152,26,34816\nAD 85,85,82\nMaav 84,168,76\nD1 1,16,46,1024\nMaav 92,172,76\nRa 156,26,35840\nMaav 88,172,72\nAD 85,85,83\nCV 69,82,83\nMaav 96,176,72\n"
"Ra 160,24,40960\nSv 172,1,16,78,3072\nMaav 100,176,76\ns_xor_b32 s89, s89, 0x10000\nEX 80,40\nMaav 108,180,76\nRa 164,24,41984\nMaav 104,180,72\nWL 6\nEX 81,41\nMaav 112,184,72\n"
"Ra 168,24,43008\nAD 84,84,80\nMaav 116,184,76\nD1 1,16,46,2048\nMaav 124,188,76\nRa 172,24,44032\nMaav 120,188,72\nAD 84,84,81\nCV 66,80,81\nCvav 48,128,192,4\nRa 176,26,40960\n"
"Mvav 48,132,200\nEX 82,42\nMvav 48,136,208\nRa 180,26,41984\nMvav 48,140,216\nWL 6\nEX 83,43\nv_cmp_ne_u32 vcc, s15, v27\ns_nop 0\ns_cbranch_vccnz .S138\n.S139:\n"
"Cvav 52,140,220,8\nRa 184,26,43008\nAD 84,84,82\nMvav 52,136,212\nD1 1,16,46,3072\ns_add_u32 m0, s89, 0\nMvav 52,132,204\nRa 188,26,44032\nMvav 52,128,196\nAD 84,84,83\n"
"CV 67,82,83\nMvav 52,144,228\nRa 128,2,49152\ns_add_u32 s78, s78, s55\nSv 176,1,16,78,0\nMvav 52,148,236\ns_add_u32 s46, s86, s88\nEX 80,44\nMvav 52,152,244\nRa 132,2,50176\n"
"v_xor_b32 v24, 0x10000, v24\nMvav 52,156,252\nWL 6\nEX 81,45\nMvav 48,156,248\nRa 136,2,51200\nAD 85,85,80\nMvav 48,152,240\nD1 1,16,46,0\nv_lshrrev_b32 v92, 8, v1\n"
"Mvav 48,148,232\nRa 140,2,52224\nSv 112,1,16,49,1024\nMvav 48,144,224\nAD 85,85,81\nCV 70,80,81\nCvav 56,160,192,12\nRa 144,2,53248\nv_bfe_u32 v93, v1, 6, 2\nMvav 56,164,200\n"
"s_add_u32 s49, s49, 131072\nEX 82,46\nMvav 56,168,208\nRa 148,2,54272\nv_xor_b32 v26, 0x10000, v26\nMvav 56,172,216\nWL 6\nEX 83,47\nCvav 60,172,220,16\nRa 152,2,55296\n"
"AD 85,85,82\nMvav 60,168,212\nD1 1,16,46,1024\nv_cmp_eq_u32 vcc, v92, v93\nv_and_b32 v92, 16, v1\nv_mov_b32 v93, 0x3f80\nv_cndmask_b32 v93, 0, v93, vcc\nMvav 60,164,204\n"
"Ra 156,2,56320\nMvav 60,160,196\nAD 85,85,83\nCV 71,82,83\nMvav 60,176,228\nRa 160,2,57344\nv_max_f32 v90, v84, v85\nSv 180,1,16,78,1024\nMvav 60,180,236\n"
"s_add_u32 s88, s88, 32768\nEX 80,48\nMvav 60,184,244\nRa 164,2,58368\nv_cmp_lt_f32 vcc, s63, v90\nMvav 60,188,252\nWL 8\nEX 81,49\nMvav 56,188,248\nRa 168,2,59392\n"
"s_cbranch_vccnz .T136\n.T137:\nMvav 56,184,240\nD1 1,16,46,2048\nv_lshlrev_b32 v93, v92, v93\nMvav 56,180,232\nRa 172,2,60416\nAD 86,86,84\nMvav 56,176,224\nAD 84,80,81\n"
"CV 72,80,81\nMaav 0,128,64\nRa 176,2,61440\nv_and_b32 v92, 32, v1\nMaav 4,128,68\nEX 82,50\nMaav 12,132,68\nRa 180,2,62464\nv_cmp_eq_u32 vcc, 0, v92\nv_mov_b32 v128, 0\n"
"v_mov_b32 v129, 0\nv_cndmask_b32 v130, 0, v93, vcc\nMaav 8,132,64\ns_waitcnt vmcnt(19) lgkmcnt(8)\nEX 83,51\nMaav 16,136,64\nRa 184,2,63488\nAD 84,84,82\nMaav 20,136,68\n"
"D1 1,16,46,3072\ns_add_u32 m0, s89, 16384\nMaav 28,140,68\nRa 188,2,64512\ns_barrier\nMaav 24,140,64\nAD 84,84,83\nCV 73,82,83\nMaav 32,144,64\nRa 128,24,0\nSv 184,1,16,78,2048\n"
"Maav 36,144,68\ns_add_u32 s46, s87, s88\nEX 80,52\nMaav 44,148,68\nRa 132,24,1024\nv_xor_b32 v2, 0x10000, v2\nMaav 40,148,64\nWL 8\nEX 81,53\nMaav 48,152,64\nRa 136,24,2048\n"
"AD 87,87,85\nMaav 52,152,68\nD1 1,16,46,0\nv_sub_u32 v131, v93, v130\nMaav 60,156,68\nRa 140,24,3072\nMaav 56,156,64\nAD 85,80,81\nCV 76,80,81\nMaav 64,160,64\nRa 144,26,0\n"
"v_mov_b32 v132, 0\nMaav 68,160,68\nEX 82,54\nMaav 76,164,68\nRa 148,26,1024\nMaav 72,164,64\nWL 8\nEX 83,55\nMaav 80,168,64\nRa 152,26,2048\nAD 85,85,82\nMaav 84,168,68\n"
"D1 1,16,46,1024\nMaav 92,172,68\nRa 156,26,3072\nMaav 88,172,64\nAD 85,85,83\nCV 77,82,83\nMaav 96,176,64\nRa 160,24,8192\nSv 188,1,16,78,3072\nMaav 100,176,68\nEX 80,56\n"
"Maav 108,180,68\nRa 164,24,9216\nv_mov_b32 v133, 0\nMaav 104,180,64\nWL 6\nEX 81,57\nMaav 112,184,64\nRa 168,24,10240\nAD 84,84,80\nMaav 116,184,68\nD1 1,16,46,2048\n"
"Maav 124,188,68\nRa 172,24,11264\nMaav 120,188,64\nAD 84,84,81\nCV 74,80,81\ns_mov_b32 s99, 11\ns_sub_u32 s90, s90, 3\n.p2align 6\n.A144:\nCvav 32,128,192,4\nRa 176,26,8192\n"
"Mvav 32,132,200\nEX 82,58\nMvav 32,136,208\nRa 180,26,9216\nMvav 32,140,216\nWL 6\nEX 83,59\nCvav 36,140,220,8\nRa 184,26,10240\nAD 84,84,82\nMvav 36,136,212\nD1 1,16,46,3072\n"
"s_add_u32 m0, s89, 32768\nMvav 36,132,204\nRa 188,26,11264\nMvav 36,128,196\nAD 84,84,83\nCV 75,82,83\nMvav 36,144,228\nRa 128,2,16384\nMvav 36,148,236\ns_add_u32 s46, s86, s88\n"
"EX 80,60\nMvav 36,152,244\nRa 132,2,17408\nMvav 36,156,252\nWL 6\nEX 81,61\nMvav 32,156,248\nRa 136,2,18432\nAD 85,85,80\nMvav 32,152,240\nD1 1,16,46,0\nMvav 32,148,232\n"
"Ra 140,2,19456\nSv 112,1,16,49,0\nMvav 32,144,224\nAD 85,85,81\nCV 78,80,81\nCvav 40,160,192,12\nRa 144,2,20480\nMvav 40,164,200\nEX 82,62\nMvav 40,168,208\nRa 148,2,21504\n"
"Mvav 40,172,216\nWL 6\nEX 83,63\nCvav 44,172,220,16\nRa 152,2,22528\nAD 85,85,82\nMvav 44,168,212\nD1 1,16,46,1024\nMvav 44,164,204\nRa 156,2,23552\nMvav 44,160,196\nAD 85,85,83\n"
"CV 79,82,83\nMvav 44,176,228\nRa 160,2,24576\nv_max_f32 v90, v84, v85\nMvav 44,180,236\ns_add_u32 s88, s88, 32768\nEX 80,32\nMvav 44,184,244\nRa 164,2,25600\n"
"v_cmp_lt_f32 vcc, s63, v90\nMvav 44,188,252\nWL 8\nEX 81,33\nMvav 40,188,248\nRa 168,2,26624\ns_cbranch_vccnz .T147\n.T148:\nMvav 40,184,240\nD1 1,16,46,2048\nMvav 40,180,232\n"
"Ra 172,2,27648\nAD 86,86,84\nMvav 40,176,224\nAD 84,80,81\nCV 64,80,81\nMaav 0,128,72\nRa 176,2,28672\nMaav 4,128,76\nEX 82,34\nMaav 12,132,76\nRa 180,2,29696\nMaav 8,132,72\n"
"s_waitcnt vmcnt(13) lgkmcnt(8)\nEX 83,35\nMaav 16,136,72\nRa 184,2,30720\nAD 84,84,82\nMaav 20,136,76\nD1 1,16,46,3072\ns_add_u32 m0, s89, 49152\nMaav 28,140,76\nRa 188,2,31744\n"
"s_barrier\nMaav 24,140,72\nAD 84,84,83\nCV 65,82,83\nMaav 32,144,72\nRa 128,24,32768\nMaav 36,144,76\ns_add_u32 s46, s87, s88\nEX 80,36\nMaav 44,148,76\nRa 132,24,33792\n"
"Maav 40,148,72\nWL 8\nEX 81,37\nMaav 48,152,72\nRa 136,24,34816\nAD 87,87,85\nMaav 52,152,76\nD1 1,16,46,0\nMaav 60,156,76\nRa 140,24,35840\nMaav 56,156,72\nAD 85,80,81\n"
"CV 68,80,81\nMaav 64,160,72\nRa 144,26,32768\nMaav 68,160,76\nEX 82,38\nMaav 76,164,76\nRa 148,26,33792\nMaav 72,164,72\nWL 8\nEX 83,39\nMaav 80,168,72\nRa 152,26,34816\n"
"AD 85,85,82\nMaav 84,168,76\nD1 1,16,46,1024\nMaav 92,172,76\nRa 156,26,35840\nMaav 88,172,72\nAD 85,85,83\nCV 69,82,83\nMaav 96,176,72\nRa 160,24,40960\nMaav 100,176,76\n"
"s_xor_b32 s89, s89, 0x10000\nEX 80,40\nMaav 108,180,76\nRa 164,24,41984\nMaav 104,180,72\nWL 6\nEX 81,41\nMaav 112,184,72\nRa 168,24,43008\nAD 84,84,80\nMaav 116,184,76\n"
"D1 1,16,46,2048\nMaav 124,188,76\nRa 172,24,44032\nMaav 120,188,72\nAD 84,84,81\nCV 66,80,81\nCvav 48,128,192,4\nRa 176,26,40960\nMvav 48,132,200\nEX 82,42\nMvav 48,136,208\n"
"Ra 180,26,41984\nMvav 48,140,216\nWL 6\nEX 83,43\nCvav 52,140,220,8\nRa 184,26,43008\nAD 84,84,82\nMvav 52,136,212\nD1 1,16,46,3072\ns_add_u32 m0, s89, 0\nMvav 52,132,204\n"
"Ra 188,26,44032\nMvav 52,128,196\nAD 84,84,83\nCV 67,82,83\nMvav 52,144,228\nRa 128,2,49152\nMvav 52,148,236\ns_add_u32 s46, s86, s88\nEX 80,44\nMvav 52,152,244\nRa 132,2,50176\n"
"v_xor_b32 v24, 0x10000, v24\nMvav 52,156,252\nWL 6\nEX 81,45\nMvav 48,156,248\nRa 136,2,51200\nAD 85,85,80\nMvav 48,152,240\nD1 1,16,46,0\nMvav 48,148,232\nRa 140,2,52224\n"
"Sv 112,1,16,49,1024\nMvav 48,144,224\nAD 85,85,81\nCV 70,80,81\nCvav 56,160,192,12\nRa 144,2,53248\nMvav 56,164,200\ns_add_u32 s49, s49, 131072\nEX 82,46\nMvav 56,168,208\n"
"Ra 148,2,54272\nv_xor_b32 v26, 0x10000, v26\nMvav 56,172,216\nWL 6\nEX 83,47\nCvav 60,172,220,16\nRa 152,2,55296\nAD 85,85,82\nMvav 60,168,212\nD1 1,16,46,1024\nMvav 60,164,204\n"
"Ra 156,2,56320\nMvav 60,160,196\nAD 85,85,83\nCV 71,82,83\nMvav 60,176,228\nRa 160,2,57344\nv_max_f32 v90, v84, v85\nMvav 60,180,236\ns_add_u32 s88, s88, 32768\nEX 80,48\n"
"Mvav 60,184,244\nRa 164,2,58368\nv_cmp_lt_f32 vcc, s63, v90\nMvav 60,188,252\nWL 8\nEX 81,49\nMvav 56,188,248\nRa 168,2,59392\ns_cbranch_vccnz .T149\n.T150:\nMvav 56,184,240\n"
"D1 1,16,46,2048\nMvav 56,180,232\nRa 172,2,60416\nAD 86,86,84\nMvav 56,176,224\nAD 84,80,81\nCV 72,80,81\nMaav 0,128,64\nRa 176,2,61440\nMaav 4,128,68\nEX 82,50\nMaav 12,132,68\n"
"Ra 180,2,62464\nMaav 8,132,64\ns_waitcnt vmcnt(13) lgkmcnt(8)\nEX 83,51\nMaav 16,136,64\nRa 184,2,63488\nAD 84,84,82\nMaav 20,136,68\nD1 1,16,46,3072\ns_add_u32 m0, s89, 16384\n"
"Maav 28,140,68\nRa 188,2,64512\ns_barrier\nMaav 24,140,64\nAD 84,84,83\nCV 73,82,83\nMaav 32,144,64\nRa 128,24,0\nMaav 36,144,68\ns_add_u32 s46, s87, s88\nEX 80,52\n"
"Maav 44,148,68\nRa 132,24,1024\nv_xor_b32 v2, 0x10000, v2\nMaav 40,148,64\nWL 8\nEX 81,53\nMaav 48,152,64\nRa 136,24,2048\nAD 87,87,85\nMaav 52,152,68\nD1 1,16,46,0\n"
"Maav 60,156,68\nRa 140,24,3072\nMaav 56,156,64\nAD 85,80,81\nCV 76,80,81\nMaav 64,160,64\nRa 144,26,0\nMaav 68,160,68\nEX 82,54\nMaav 76,164,68\nRa 148,26,1024\nMaav 72,164,64\n"
"WL 8\nEX 83,55\nMaav 80,168,64\nRa 152,26,2048\nAD 85,85,82\nMaav 84,168,68\nD1 1,16,46,1024\nMaav 92,172,68\nRa 156,26,3072\nMaav 88,172,64\nAD 85,85,83\nCV 77,82,83\n"
"Maav 96,176,64\nRa 160,24,8192\nMaav 100,176,68\ns_sub_u32 s90, s90, 1\nEX 80,56\nMaav 108,180,68\nRa 164,24,9216\ns_cbranch_scc1 .E145\n.E146:\nMaav 104,180,64\nWL 6\nEX 81,57\n"
"Maav 112,184,64\nRa 168,24,10240\nAD 84,84,80\nMaav 116,184,68\nD1 1,16,46,2048\ns_sub_u32 s99, s99, 1\nMaav 124,188,68\nRa 172,24,11264\nMaav 120,188,64\nAD 84,84,81\n"
"CV 74,80,81\ns_cbranch_scc0 .A144\nCvav 32,128,192,4\nRa 176,26,8192\nMvav 32,132,200\nEX 82,58\nMvav 32,136,208\nRa 180,26,9216\nMvav 32,140,216\nWL 6\nEX 83,59\n"
"Cvav 36,140,220,8\nRa 184,26,10240\nAD 84,84,82\nMvav 36,136,212\nD1 1,16,46,3072\ns_add_u32 m0, s89, 32768\nMvav 36,132,204\nRa 188,26,11264\nMvav 36,128,196\nAD 84,84,83\n"
"CV 75,82,83\ns_mov_b32 s94, s96\ns_add_u32 s95, s96, 65536\nv_and_b32 v108, 0xf0, v1\nMvav 36,144,228\nRa 128,2,16384\nMvav 36,148,236\ns_add_u32 s46, s86, s88\nEX 80,60\n"
"Mvav 36,152,244\nRa 132,2,17408\nMvav 36,156,252\nWL 6\nEX 81,61\nMvav 32,156,248\nRa 136,2,18432\nAD 85,85,80\nMvav 32,152,240\nD1 1,16,46,0\nMvav 32,148,232\nRa 140,2,19456\n"
"Sv 112,1,16,49,0\nMvav 32,144,224\nAD 85,85,81\nCV 78,80,81\nbuffer_load_dwordx4 v[100:103], v108, s[16:19], s65 offen offset:0 sc0 sc1\n"
"buffer_load_dwordx4 v[104:107], v108, s[16:19], s65 offen offset:256 sc0 sc1\nCvav 40,160,192,12\nRa 144,2,20480\nMvav 40,164,200\nEX 82,62\nMvav 40,168,208\nRa 148,2,21504\n"
"Mvav 40,172,216\nWL 6\nEX 83,63\nCvav 44,172,220,16\nRa 152,2,22528\nAD 85,85,82\nMvav 44,168,212\nD1 1,16,46,1024\nMvav 44,164,204\nRa 156,2,23552\nMvav 44,160,196\nAD 85,85,83\n"
"CV 79,82,83\nMvav 44,176,228\nRa 160,2,24576\nv_max_f32 v90, v84, v85\nMvav 44,180,236\ns_add_u32 s88, s88, 32768\nEX 80,32\nMvav 44,184,244\nRa 164,2,25600\n"
"v_cmp_lt_f32 vcc, s63, v90\nMvav 44,188,252\nWL 8\nEX 81,33\nMvav 40,188,248\nRa 168,2,26624\ns_cbranch_vccnz .T153\n.T154:\nMvav 40,184,240\nD1 1,16,46,2048\nMvav 40,180,232\n"
"Ra 172,2,27648\nAD 86,86,84\nMvav 40,176,224\nAD 84,80,81\nCV 64,80,81\nMaav 0,128,72\nRa 176,2,28672\nMaav 4,128,76\nEX 82,34\nMaav 12,132,76\nRa 180,2,29696\nMaav 8,132,72\n"
"s_waitcnt vmcnt(15) lgkmcnt(8)\nEX 83,35\nMaav 16,136,72\nRa 184,2,30720\nAD 84,84,82\nMaav 20,136,76\nD1 1,16,46,3072\ns_add_u32 m0, s89, 49152\nMaav 28,140,76\nRa 188,2,31744\n"
"s_barrier\nMaav 24,140,72\nAD 84,84,83\nCV 65,82,83\nMaav 32,144,72\nRa 128,24,32768\nMaav 36,144,76\ns_add_u32 s46, s87, s88\nEX 80,36\nMaav 44,148,76\nRa 132,24,33792\n"
"Maav 40,148,72\nWL 8\nEX 81,37\nMaav 48,152,72\nRa 136,24,34816\nAD 87,87,85\nMaav 52,152,76\nD1 1,16,46,0\nMaav 60,156,76\nRa 140,24,35840\nMaav 56,156,72\nAD 85,80,81\n"
"CV 68,80,81\nMaav 64,160,72\nRa 144,26,32768\nMaav 68,160,76\nEX 82,38\nMaav 76,164,76\nRa 148,26,33792\nMaav 72,164,72\nWL 8\nEX 83,39\nMaav 80,168,72\nRa 152,26,34816\n"
"AD 85,85,82\nMaav 84,168,76\nD1 1,16,46,1024\nMaav 92,172,76\nRa 156,26,35840\nMaav 88,172,72\nAD 85,85,83\nCV 69,82,83\nMaav 96,176,72\nRa 160,24,40960\nMaav 100,176,76\n"
"s_xor_b32 s89, s89, 0x10000\nEX 80,40\nMaav 108,180,76\nRa 164,24,41984\nMaav 104,180,72\nWL 6\nEX 81,41\nMaav 112,184,72\nRa 168,24,43008\nAD 84,84,80\nMaav 116,184,76\n"
"D1 1,16,46,2048\nMaav 124,188,76\nRa 172,24,44032\nMaav 120,188,72\nAD 84,84,81\nCV 66,80,81\nCvav 48,128,192,4\nRa 176,26,40960\nMvav 48,132,200\ns_mov_b32 s86, s94\nEX 82,42\n"
"Mvav 48,136,208\nRa 180,26,41984\nMvav 48,140,216\nWL 6\nEX 83,43\nCvav 52,140,220,8\nRa 184,26,43008\nAD 84,84,82\nMvav 52,136,212\nD1 1,16,46,3072\ns_add_u32 m0, s89, 0\n"
"Mvav 52,132,204\nRa 188,26,44032\nMvav 52,128,196\nAD 84,84,83\nCV 67,82,83\nMvav 52,144,228\nRa 128,2,49152\nMvav 52,148,236\ns_add_u32 s46, s86, s88\nEX 80,44\nMvav 52,152,244\n"
"Ra 132,2,50176\nv_xor_b32 v24, 0x10000, v24\nMvav 52,156,252\nWL 6\nEX 81,45\nMvav 48,156,248\nRa 136,2,51200\nAD 85,85,80\nMvav 48,152,240\nD1 1,16,46,0\nMvav 48,148,232\n"
"Ra 140,2,52224\nSv 112,1,16,49,1024\nMvav 48,144,224\nAD 85,85,81\nCV 70,80,81\nWV 9\nv_xor_b32 v109, s15, v102\nv_xor_b32 v110, s15, v106\nv_or_b32 v109, v109, v110\n"
"v_cmp_ne_u32 vcc, 0, v109\ns_nop 3\ns_cmp_lg_u64 vcc, 0\ns_cselect_b32 s67, 1, 0\ns_cselect_b32 s6, 0x40000000, s66\nCvav 56,160,192,12\nRa 144,2,53248\ns_add_u32 s5, s6, 0\n"
"Mvav 56,164,200\ns_add_u32 s49, s49, 131072\nEX 82,46\nMvav 56,168,208\nRa 148,2,54272\nv_xor_b32 v26, 0x10000, v26\n"
"buffer_load_dwordx4 a[192:195], v1, s[16:19], s5 offen offset:0 sc1\nMvav 56,172,216\nWL 6\nEX 83,47\nCvav 60,172,220,16\nRa 152,2,55296\nAD 85,85,82\nMvav 60,168,212\n"
"D1 1,16,46,1024\nMvav 60,164,204\nRa 156,2,56320\nMvav 60,160,196\nAD 85,85,83\nCV 71,82,83\nMvav 60,176,228\nRa 160,2,57344\nv_max_f32 v90, v84, v85\nMvav 60,180,236\n"
"s_add_u32 s88, s88, 32768\nEX 80,48\nMvav 60,184,244\nRa 164,2,58368\nv_cmp_lt_f32 vcc, s63, v90\nMvav 60,188,252\nWL 8\nEX 81,49\nMvav 56,188,248\nRa 168,2,59392\n"
"s_cbranch_vccnz .T155\n.T156:\nbuffer_load_dwordx4 a[196:199], v1, s[16:19], s5 offen offset:1024 sc1\nMvav 56,184,240\nD1 1,16,46,2048\nMvav 56,180,232\nRa 172,2,60416\n"
"AD 86,86,84\nMvav 56,176,224\nAD 84,80,81\nCV 72,80,81\nMaav 0,128,64\nRa 176,2,61440\nMaav 4,128,68\nEX 82,50\nMaav 12,132,68\nRa 180,2,62464\nMaav 8,132,64\nWL 8\nEX 83,51\n"
"Maav 16,136,64\nRa 184,2,63488\nAD 84,84,82\nbuffer_load_dwordx4 a[200:203], v1, s[16:19], s5 offen offset:2048 sc1\nMaav 20,136,68\nD1 1,16,46,3072\ns_add_u32 m0, s89, 16384\n"
"Maav 28,140,68\nRa 188,2,64512\ns_barrier\nMaav 24,140,64\nAD 84,84,83\nCV 73,82,83\nMaav 32,144,64\nRa 128,24,0\nMaav 36,144,68\ns_add_u32 s46, s87, s88\nEX 80,52\n"
"Maav 44,148,68\nRa 132,24,1024\nv_xor_b32 v2, 0x10000, v2\nMaav 40,148,64\nWL 8\nEX 81,53\nMaav 48,152,64\nRa 136,24,2048\nAD 87,87,85\n"
"buffer_load_dwordx4 a[204:207], v1, s[16:19], s5 offen offset:3072 sc1\nMaav 52,152,68\nD1 1,16,46,0\nMaav 60,156,68\nRa 140,24,3072\nMaav 56,156,64\nAD 85,80,81\nCV 76,80,81\n"
"Maav 64,160,64\nRa 144,26,0\nMaav 68,160,68\ns_cmp_eq_u32 s99, s92\nEX 82,54\nMaav 76,164,68\nRa 148,26,1024\ns_cbranch_scc1 .E151\n.E152:\ns_add_u32 s5, s6, 4096\nMaav 72,164,64\n"
"WL 8\nEX 83,55\nMaav 80,168,64\nRa 152,26,2048\nAD 85,85,82\nbuffer_load_dwordx4 a[208:211], v1, s[16:19], s5 offen offset:0 sc1\nMaav 84,168,68\nD1 1,16,46,1024\nMaav 92,172,68\n"
"Ra 156,26,3072\nMaav 88,172,64\nAD 85,85,83\nCV 77,82,83\nMaav 96,176,64\nRa 160,24,8192\nMaav 100,176,68\nEX 80,56\nMaav 108,180,68\nRa 164,24,9216\nMaav 104,180,64\nWL 6\n"
"EX 81,57\nMaav 112,184,64\nRa 168,24,10240\nAD 84,84,80\nbuffer_load_dwordx4 a[212:215], v1, s[16:19], s5 offen offset:1024 sc1\nMaav 116,184,68\nD1 1,16,46,2048\nMaav 124,188,68\n"
"Ra 172,24,11264\nMaav 120,188,64\nAD 84,84,81\nCV 74,80,81\ns_sub_u32 s99, s99, 1\nCvav 32,128,192,4\nRa 176,26,8192\nMvav 32,132,200\nEX 82,58\nMvav 32,136,208\nRa 180,26,9216\n"
"Mvav 32,140,216\nWL 6\nEX 83,59\nCvav 36,140,220,8\nRa 184,26,10240\nAD 84,84,82\nbuffer_load_dwordx4 a[216:219], v1, s[16:19], s5 offen offset:2048 sc1\nMvav 36,136,212\n"
"D1 1,16,46,3072\ns_add_u32 m0, s89, 32768\nMvav 36,132,204\nRa 188,26,11264\nMvav 36,128,196\nAD 84,84,83\nCV 75,82,83\nMvav 36,144,228\nRa 128,2,16384\nMvav 36,148,236\n"
"s_add_u32 s46, s86, s88\nEX 80,60\nMvav 36,152,244\nRa 132,2,17408\nMvav 36,156,252\nWL 6\nEX 81,61\nMvav 32,156,248\nRa 136,2,18432\nAD 85,85,80\n"
"buffer_load_dwordx4 a[220:223], v1, s[16:19], s5 offen offset:3072 sc1\nMvav 32,152,240\nD1 1,16,46,0\nMvav 32,148,232\nRa 140,2,19456\nSv 112,1,16,49,0\nMvav 32,144,224\n"
"AD 85,85,81\nCV 78,80,81\nCvav 40,160,192,12\nRa 144,2,20480\nMvav 40,164,200\ns_mov_b32 s87, s95\nEX 82,62\nMvav 40,168,208\nRa 148,2,21504\ns_add_u32 s5, s6, 16384\n"
"Mvav 40,172,216\nWL 6\nEX 83,63\nCvav 44,172,220,16\nRa 152,2,22528\nAD 85,85,82\nbuffer_load_dwordx4 a[224:227], v1, s[16:19], s5 offen offset:0 sc1\nMvav 44,168,212\n"
"D1 1,16,46,1024\nMvav 44,164,204\nRa 156,2,23552\nMvav 44,160,196\nAD 85,85,83\nCV 79,82,83\nMvav 44,176,228\nRa 160,2,24576\nv_max_f32 v90, v84, v85\nMvav 44,180,236\n"
"s_add_u32 s88, s88, 32768\nEX 80,32\nMvav 44,184,244\nRa 164,2,25600\nv_cmp_lt_f32 vcc, s63, v90\nMvav 44,188,252\nWL 8\nEX 81,33\nMvav 40,188,248\nRa 168,2,26624\n"
"s_cbranch_vccnz .T159\n.T160:\nbuffer_load_dwordx4 a[228:231], v1, s[16:19], s5 offen offset:1024 sc1\nMvav 40,184,240\nD1 1,16,46,2048\nMvav 40,180,232\nRa 172,2,27648\n"
"AD 86,86,84\nMvav 40,176,224\nAD 84,80,81\nCV 64,80,81\nMaav 0,128,72\nRa 176,2,28672\nMaav 4,128,76\nEX 82,34\nMaav 12,132,76\nRa 180,2,29696\nMaav 8,132,72\n"
"s_waitcnt vmcnt(23) lgkmcnt(8)\nEX 83,35\nMaav 16,136,72\nRa 184,2,30720\nAD 84,84,82\nbuffer_load_dwordx4 a[232:235], v1, s[16:19], s5 offen offset:2048 sc1\nMaav 20,136,76\n"
"D1 1,16,46,3072\ns_add_u32 m0, s89, 49152\nMaav 28,140,76\nRa 188,2,31744\ns_barrier\nMaav 24,140,72\nAD 84,84,83\nCV 65,82,83\nMaav 32,144,72\nRa 128,24,32768\nMaav 36,144,76\n"
"s_add_u32 s46, s87, s88\nEX 80,36\nMaav 44,148,76\nRa 132,24,33792\nMaav 40,148,72\nWL 8\nEX 81,37\nMaav 48,152,72\nRa 136,24,34816\nAD 87,87,85\n"
"buffer_load_dwordx4 a[236:239], v1, s[16:19], s5 offen offset:3072 sc1\nMaav 52,152,76\nD1 1,16,46,0\nMaav 60,156,76\nRa 140,24,35840\nMaav 56,156,72\nAD 85,80,81\nCV 68,80,81\n"
"Maav 64,160,72\nRa 144,26,32768\nMaav 68,160,76\ns_cmp_eq_u32 s99, s93\nEX 82,38\nMaav 76,164,76\nRa 148,26,33792\ns_cbranch_scc1 .E157\n.E158:\ns_add_u32 s5, s6, 20480\n"
"Maav 72,164,72\nWL 8\nEX 83,39\nMaav 80,168,72\nRa 152,26,34816\nAD 85,85,82\nbuffer_load_dwordx4 a[240:243], v1, s[16:19], s5 offen offset:0 sc1\nMaav 84,168,76\nD1 1,16,46,1024\n"
"Maav 92,172,76\nRa 156,26,35840\nMaav 88,172,72\nAD 85,85,83\nCV 69,82,83\nMaav 96,176,72\nRa 160,24,40960\nMaav 100,176,76\ns_xor_b32 s89, s89, 0x10000\nEX 80,40\n"
"Maav 108,180,76\nRa 164,24,41984\nMaav 104,180,72\nWL 6\nEX 81,41\nMaav 112,184,72\nRa 168,24,43008\nAD 84,84,80\n"
"buffer_load_dwordx4 a[244:247], v1, s[16:19], s5 offen offset:1024 sc1\nMaav 116,184,76\nD1 1,16,46,2048\nMaav 124,188,76\nRa 172,24,44032\nMaav 120,188,72\nAD 84,84,81\n"
"CV 66,80,81\nCvav 48,128,192,4\nRa 176,26,40960\nMvav 48,132,200\nEX 82,42\nMvav 48,136,208\nRa 180,26,41984\nMvav 48,140,216\nWL 6\nEX 83,43\nCvav 52,140,220,8\nRa 184,26,43008\n"
"AD 84,84,82\nbuffer_load_dwordx4 a[248:251], v1, s[16:19], s5 offen offset:2048 sc1\nMvav 52,136,212\nD1 1,16,46,3072\ns_add_u32 m0, s89, 0\nMvav 52,132,204\nRa 188,26,44032\n"
"Mvav 52,128,196\nAD 84,84,83\nCV 67,82,83\nMvav 52,144,228\nRa 128,2,49152\nMvav 52,148,236\ns_add_u32 s46, s86, s88\nEX 80,44\nMvav 52,152,244\nRa 132,2,50176\n"
"v_xor_b32 v24, 0x10000, v24\nMvav 52,156,252\nWL 6\nEX 81,45\nMvav 48,156,248\nRa 136,2,51200\nAD 85,85,80\nbuffer_load_dwordx4 a[252:255], v1, s[16:19], s5 offen offset:3072 sc1\n"
"Mvav 48,152,240\nD1 1,16,46,0\nMvav 48,148,232\nRa 140,2,52224\nSv 112,1,16,49,1024\nMvav 48,144,224\nAD 85,85,81\nCV 70,80,81\nCvav 56,160,192,12\nRa 144,2,53248\n"
"Mvav 56,164,200\ns_add_u32 s49, s49, 131072\nEX 82,46\nMvav 56,168,208\nRa 148,2,54272\nv_xor_b32 v26, 0x10000, v26\nMvav 56,172,216\nWL 6\nEX 83,47\nCvav 60,172,220,16\n"
"Ra 152,2,55296\nAD 85,85,82\nMvav 60,168,212\nD1 1,16,46,1024\nMvav 60,164,204\nRa 156,2,56320\nMvav 60,160,196\nAD 85,85,83\nCV 71,82,83\nMvav 60,176,228\nRa 160,2,57344\n"
"v_max_f32 v90, v84, v85\nMvav 60,180,236\ns_add_u32 s88, s88, 32768\nEX 80,48\nMvav 60,184,244\nRa 164,2,58368\nv_cmp_lt_f32 vcc, s63, v90\nMvav 60,188,252\nWL 8\nEX 81,49\n"
"Mvav 56,188,248\nRa 168,2,59392\ns_cbranch_vccnz .T161\n.T162:\nMvav 56,184,240\nD1 1,16,46,2048\nMvav 56,180,232\nRa 172,2,60416\nAD 86,86,84\nMvav 56,176,224\nAD 84,80,81\n"
"CV 72,80,81\nMaav 0,128,64\nRa 176,2,61440\nMaav 4,128,68\nEX 82,50\nMaav 12,132,68\nRa 180,2,62464\nMaav 8,132,64\ns_waitcnt vmcnt(22) lgkmcnt(8)\nEX 83,51\nMaav 16,136,64\n"
"Ra 184,2,63488\nAD 84,84,82\nMaav 20,136,68\nD1 1,16,46,3072\ns_add_u32 m0, s89, 16384\nMaav 28,140,68\nRa 188,2,64512\ns_barrier\nMaav 24,140,64\nAD 84,84,83\nCV 73,82,83\n"
"Maav 32,144,64\nRa 128,24,0\nMaav 36,144,68\ns_add_u32 s46, s87, s88\nEX 80,52\nMaav 44,148,68\nRa 132,24,1024\nv_xor_b32 v2, 0x10000, v2\nMaav 40,148,64\nWL 8\nEX 81,53\n"
"Maav 48,152,64\nRa 136,24,2048\nAD 87,87,85\nMaav 52,152,68\nD1 1,16,46,0\nMaav 60,156,68\nRa 140,24,3072\nMaav 56,156,64\nAD 85,80,81\nCV 76,80,81\ns_cmp_lg_u32 s67, 0\n"
"s_cbranch_scc1 .E164\n.E165:\nMaav 64,160,64\nRa 144,26,0\nMaav 68,160,68\nEX 82,54\nMaav 76,164,68\nRa 148,26,1024\nMaav 72,164,64\nWL 8\nEX 83,55\nMaav 80,168,64\n"
"Ra 152,26,2048\nAD 85,85,82\nMaav 84,168,68\nD1 1,16,46,1024\nMaav 92,172,68\nRa 156,26,3072\nMaav 88,172,64\nAD 85,85,83\nCV 77,82,83\nMaav 96,176,64\nRa 160,24,8192\n"
"Maav 100,176,68\nEX 80,56\nMaav 108,180,68\nRa 164,24,9216\nMaav 104,180,64\nWL 10\nEX 81,57\nMaav 112,184,64\nRa 168,24,10240\nAD 84,84,80\nMaav 116,184,68\nD1 1,16,46,2048\n"
"Maav 124,188,68\nRa 172,24,11264\nMaav 120,188,64\nAD 84,84,81\nCV 74,80,81\ns_cmp_lg_u32 s67, 0\ns_cbranch_scc1 .D166\n.D167:\nWV 8\nv_cmp_lt_f32 vcc, 0, v101\nSU 20,100,88\n"
"v_mul_f32 v101, 0x3e800000, v101\nv_cndmask_b32 v20, 0, v20, vcc\nv_cmp_lt_f32 vcc, 0, v105\nSU 21,104,89\nv_mul_f32 v105, 0x3e800000, v105\nv_cndmask_b32 v21, 0, v21, vcc\n"
"v_or_b32 v91, v20, v21\nv_cmp_ne_u32 vcc, 0, v91\ns_nop 0\ns_cbranch_vccnz .D168\nAD 86,86,101\nAD 87,87,105\n.D169:\nMava 0,130,192\nMava 8,128,192\nEX 82,58\nMava 12,128,196\n"
"Mava 4,130,196\nEX 83,59\nMava 16,130,200\nAD 84,84,82\nMava 24,128,200\nD1 1,16,46,3072\nMava 28,128,204\ns_add_u32 m0, s89, 32768\ns_add_u32 s46, s86, s88\nMava 20,130,204\n"
"AD 84,84,83\nCV 75,82,83\nMava 32,130,208\nRa 128,2,16384\nv_xor_b32 v3, 0x10000, v2\nMava 40,128,208\nEX 80,60\nMava 44,128,212\nRa 132,2,17408\nv_add_u32 v0, 0x20000, v1\n"
"Mava 36,130,212\nEX 81,61\nMava 48,130,216\nRa 136,2,18432\nAD 85,85,80\nMava 56,128,216\nD1 1,16,46,0\ns_sub_u32 s94, s49, 1966080\nMava 60,128,220\nRa 140,2,19456\n"
"s_add_u32 s95, s94, 1048576\nMava 52,130,220\nAD 85,85,81\nCV 78,80,81\nMava 64,130,224\nRa 144,2,20480\ns_xor_b32 s92, s94, 0x202000\nMava 72,128,224\nEX 82,62\nMava 76,128,228\n"
"Ra 148,2,21504\ns_add_u32 s93, s96, 983040\nMava 68,130,228\nEX 83,63\nMava 80,130,232\nRa 152,2,22528\nAD 85,85,82\nMava 88,128,232\nD1 1,16,46,1024\n"
"s_xor_b32 s93, s93, 0x202000\nMava 92,128,236\nRa 156,2,23552\ns_lshl_b32 s89, s11, 12\nMava 84,130,236\nAD 85,85,83\nCV 79,82,83\nMava 96,130,240\nRa 160,2,24576\n"
"v_max_f32 v90, v84, v85\nMava 104,128,240\nv_cmp_lt_f32 vcc, s63, v90\nMava 108,128,244\nRa 164,2,25600\nMava 100,130,244\nWL 8\nMava 112,130,248\nRa 168,2,26624\n"
"Mava 120,128,248\nD1 1,16,46,2048\nMava 124,128,252\nRa 172,2,27648\nMava 116,130,252\ns_cbranch_vccnz .T173\n.T174:\nAD 86,86,84\nAD 87,87,85\nMaav 0,128,72\nRa 176,2,28672\n"
"v_mov_b32 v84, v86\nv_mov_b32 v85, v87\nMaav 4,128,76\nv_permlane32_swap_b32 v86, v84\nv_permlane32_swap_b32 v87, v85\nMaav 12,132,76\nRa 180,2,29696\nAD 86,86,84\nMaav 8,132,72\n"
"WL 8\nAD 87,87,85\nMaav 16,136,72\nRa 184,2,30720\nv_mov_b32 v84, v86\nv_mov_b32 v85, v87\nMaav 20,136,76\nD1 1,16,46,3072\nv_permlane16_swap_b32 v86, v84\nMaav 28,140,76\n"
"Ra 188,2,31744\nv_permlane16_swap_b32 v87, v85\nMaav 24,140,72\nAD 86,86,84\nAD 87,87,85\nMaav 32,144,72\nRa 128,2,0\nv_rcp_f32 v86, v86\nMaav 36,144,76\nv_rcp_f32 v87, v87\n"
"Maav 44,148,76\nRa 132,2,1024\nAR 82,0\nAR 83,1\nMU 82,82,86\nMU 83,83,86\nMaav 40,148,72\nWL 8\ns_add_u32 m0, s89, 131072\ns_add_u32 s46, s93, 0\nCV 96,82,83\nAR 82,2\n"
"Maav 48,152,72\nRa 136,2,2048\nAR 83,3\nMU 82,82,86\nMU 83,83,86\nCV 97,82,83\nMaav 52,152,76\nD1 1,16,46,0\nAR 82,4\nAR 83,5\nMU 82,82,87\nMU 83,83,87\nMaav 60,156,76\n"
"Ra 140,2,3072\nCV 100,82,83\nAR 82,6\nAR 83,7\nMU 82,82,87\nMaav 56,156,72\nMU 83,83,87\nCV 101,82,83\nAR 82,8\nAR 83,9\nMU 82,82,86\nMaav 64,160,72\nRa 144,2,4096\nMU 83,83,86\n"
"CV 98,82,83\nAR 82,10\nAR 83,11\nMaav 68,160,76\nMU 82,82,86\nMU 83,83,86\nCV 99,82,83\ns_add_u32 s5, s94, 0\nSv 96,1,16,5,0\nMaav 76,164,76\nRa 148,2,5120\nAR 82,12\nAR 83,13\n"
"MU 82,82,87\nMU 83,83,87\nMaav 72,164,72\nWL 8\nCV 102,82,83\nAR 82,14\nAR 83,15\nMU 82,82,87\nMaav 80,168,72\nRa 152,2,6144\nMU 83,83,87\nCV 103,82,83\nSv 100,1,16,5,1024\n"
"AR 82,16\nMaav 84,168,76\nD1 1,16,46,1024\nAR 83,17\nMU 82,82,86\nMU 83,83,86\nCV 104,82,83\nMaav 92,172,76\nRa 156,2,7168\nAR 82,18\nAR 83,19\nMU 82,82,86\nMU 83,83,86\n"
"Maav 88,172,72\nCV 105,82,83\nAR 82,20\nAR 83,21\nMU 82,82,87\nMU 83,83,87\nMaav 96,176,72\nRa 160,2,8192\nCV 108,82,83\nAR 82,22\nAR 83,23\nMU 82,82,87\nMaav 100,176,76\n"
"MU 83,83,87\nCV 109,82,83\nAR 82,24\nAR 83,25\nMU 82,82,86\nMaav 108,180,76\nRa 164,2,9216\nMU 83,83,86\nCV 106,82,83\nAR 82,26\nAR 83,27\nMaav 104,180,72\nWL 8\nMU 82,82,86\n"
"MU 83,83,86\nCV 107,82,83\ns_add_u32 s5, s94, 131072\nMaav 112,184,72\nRa 168,2,10240\nSv 104,1,16,5,0\nAR 82,28\nAR 83,29\nMU 82,82,87\nMaav 116,184,76\nD1 1,16,46,2048\n"
"MU 83,83,87\nCV 110,82,83\nAR 82,30\nAR 83,31\nMaav 124,188,76\nRa 172,2,11264\nMU 82,82,87\nMU 83,83,87\nCV 111,82,83\nSv 108,1,16,5,1024\nMaav 120,188,72\nAR 82,32\nAR 83,33\n"
"MU 82,82,86\nZvav 136,128,96\nRa 176,2,12288\nMU 83,83,86\nCV 112,82,83\nMvav 136,132,104\nAR 82,34\nAR 83,35\nZvav 140,132,108\nRa 180,2,13312\nMU 82,82,86\nMU 83,83,86\n"
"Mvav 140,128,100\ns_waitcnt vmcnt(18) lgkmcnt(8)\nCV 113,82,83\nAR 82,36\nZvav 148,136,100\nRa 184,2,14336\nAR 83,37\nMU 82,82,87\nMvav 148,140,108\nD1 1,16,46,3072\nMU 83,83,87\n"
"CV 116,82,83\nZvav 144,140,104\nRa 188,2,15360\ns_barrier\nAR 82,38\nMvav 144,136,96\nAR 83,39\nMU 82,82,87\nMU 83,83,87\nZvav 152,144,96\nRa 128,2,32768\nCV 117,82,83\nAR 82,40\n"
"v_lshrrev_b32 v88, 8, v1\nMvav 152,148,104\nAR 83,41\nMU 82,82,86\nMU 83,83,86\nbuffer_store_dword v25, off, s[16:19], s54 offset:512 sc0 sc1\nZvav 156,148,108\nRa 132,2,33792\n"
"CV 114,82,83\nAR 82,42\nMvav 156,144,100\nWL 8\ns_add_u32 m0, s89, 114688\ns_add_u32 s46, s96, 1179648\nZvav 164,152,100\nRa 136,2,34816\nAR 83,43\nMU 82,82,86\n"
"v_lshlrev_b32 v89, 5, v88\nMvav 164,156,108\nD1 1,16,46,0\nMU 83,83,86\nCV 115,82,83\nZvav 160,156,104\nRa 140,2,35840\ns_add_u32 s5, s94, 262144\nSv 112,1,16,5,0\n"
"Mvav 160,152,96\nAR 82,44\nAR 83,45\nMU 82,82,87\nZvav 168,160,96\nRa 144,2,36864\nMU 83,83,87\nCV 118,82,83\n"
"buffer_load_dwordx4 v[64:67], v89, s[16:19], s40 offen offset:0 sc0 sc1\nMvav 168,164,104\nAR 82,46\nAR 83,47\nZvav 172,164,108\nRa 148,2,37888\nMU 82,82,87\nMU 83,83,87\n"
"Mvav 172,160,100\nWL 8\nCV 119,82,83\nSv 116,1,16,5,1024\nZvav 180,168,100\nRa 152,2,38912\nAR 82,48\nAR 83,49\n"
"buffer_load_dwordx4 v[68:71], v89, s[16:19], s40 offen offset:16 sc0 sc1\nMvav 180,172,108\nD1 1,16,46,1024\nMU 82,82,86\nMU 83,83,86\nZvav 176,172,104\nRa 156,2,39936\n"
"CV 120,82,83\nAR 82,50\nMvav 176,168,96\nAR 83,51\nMU 82,82,86\nMU 83,83,86\nZvav 184,176,96\nRa 160,2,40960\nCV 121,82,83\nAR 82,52\n"
"buffer_load_dwordx4 v[72:75], v89, s[16:19], s40 offen offset:1792 sc0 sc1\nMvav 184,180,104\nAR 83,53\nMU 82,82,87\nMU 83,83,87\nZvav 188,180,108\nRa 164,2,41984\nCV 124,82,83\n"
"AR 82,54\nMvav 188,176,100\nWL 8\nAR 83,55\nMU 82,82,87\nZvav 196,184,100\nRa 168,2,43008\nMU 83,83,87\nCV 125,82,83\n"
"buffer_load_dwordx4 v[76:79], v89, s[16:19], s40 offen offset:1808 sc0 sc1\nMvav 196,188,108\nD1 1,16,46,2048\nAR 82,56\nAR 83,57\nZvav 192,188,104\nRa 172,2,44032\nMU 82,82,86\n"
"MU 83,83,86\nMvav 192,184,96\nCV 122,82,83\nAR 82,58\nAR 83,59\nZvav 200,128,96\nRa 176,2,45056\nMU 82,82,86\nMU 83,83,86\nv_bfe_u32 v90, v1, 4, 4\nMvav 200,132,104\nCV 123,82,83\n"
"s_add_u32 s5, s94, 393216\nv_mul_u32_u24 v91, 896, v90\nZvav 204,132,108\nRa 180,2,46080\nSv 120,1,16,5,0\nAR 82,60\nMvav 204,128,100\nWL 8\nAR 83,61\nMU 82,82,87\n"
"Zvav 212,136,100\nRa 184,2,47104\nMU 83,83,87\nCV 126,82,83\nv_lshl_add_u32 v91, v88, 4, v91\nMvav 212,140,108\nD1 1,16,46,3072\nAR 82,62\nAR 83,63\nZvav 208,140,104\n"
"Ra 188,2,48128\nMU 82,82,87\nMU 83,83,87\nMvav 208,136,96\nCV 127,82,83\nSv 124,1,16,5,1024\nbuffer_load_dwordx4 v[12:15], v91, s[28:31], s22 offen offset:0\nZvav 216,144,96\n"
"Ra 128,2,49152\nAR 82,64\nAR 83,65\nMvav 216,148,104\nMU 82,82,86\nMU 83,83,86\nCV 32,82,83\nZvav 220,148,108\nRa 132,2,50176\nAR 82,66\nAR 83,67\nMvav 220,144,100\nWL 8\n"
"s_add_u32 m0, s89, 147456\ns_add_u32 s46, s93, 32768\nZvav 228,152,100\nRa 136,2,51200\nMU 82,82,86\nMU 83,83,86\nbuffer_load_dwordx4 v[16:19], v91, s[28:31], s23 offen offset:0\n"
"Mvav 228,156,108\nD1 1,16,46,0\nCV 33,82,83\nAR 82,68\nZvav 224,156,104\nRa 140,2,52224\nAR 83,69\nMU 82,82,87\nMvav 224,152,96\nMU 83,83,87\nCV 36,82,83\nAR 82,70\n"
"Zvav 232,160,96\nRa 144,2,53248\nAR 83,71\nMU 82,82,87\nv_add_u32 v90, s21, v90\nMvav 232,164,104\nMU 83,83,87\nCV 37,82,83\nv_lshlrev_b32 v88, 2, v90\nZvav 236,164,108\n"
"Ra 148,2,54272\nAR 82,72\nAR 83,73\nMvav 236,160,100\nWL 8\nMU 82,82,86\nMU 83,83,86\nZvav 244,168,100\nRa 152,2,55296\nCV 34,82,83\nAR 82,74\nMvav 244,172,108\nD1 1,16,46,1024\n"
"AR 83,75\nMU 82,82,86\nZvav 240,172,104\nRa 156,2,56320\nMU 83,83,86\nCV 35,82,83\nMvav 240,168,96\ns_add_u32 s5, s94, 524288\nSv 32,1,16,5,0\nAR 82,76\nMvav 136,176,112\n"
"Ra 160,2,57344\nAR 83,77\nMU 82,82,87\nMvav 136,180,120\nMU 83,83,87\nCV 38,82,83\nAR 82,78\nMvav 140,180,124\nRa 164,2,58368\nAR 83,79\nMU 82,82,87\nMvav 140,176,116\nWL 8\n"
"MU 83,83,87\nCV 39,82,83\nMvav 148,184,116\nRa 168,2,59392\nSv 36,1,16,5,1024\nAR 82,80\nMvav 148,188,124\nD1 1,16,46,2048\nAR 83,81\nMU 82,82,86\nMvav 144,188,120\n"
"Ra 172,2,60416\nMU 83,83,86\nCV 40,82,83\nMvav 144,184,112\nAR 82,82\nAR 83,83\nMU 82,82,86\nMvav 152,128,112\nRa 176,2,61440\nMU 83,83,86\nCV 41,82,83\nMvav 152,132,120\n"
"AR 82,84\nAR 83,85\nMvav 156,132,124\nRa 180,2,62464\nMU 82,82,87\nMU 83,83,87\nMvav 156,128,116\ns_waitcnt vmcnt(32) lgkmcnt(8)\nCV 44,82,83\nAR 82,86\nMvav 164,136,116\n"
"Ra 184,2,63488\nAR 83,87\nMU 82,82,87\nMvav 164,140,124\nD1 1,16,46,3072\nMU 83,83,87\nCV 45,82,83\nMvav 160,140,120\nRa 188,2,64512\ns_barrier\nAR 82,88\nMvav 160,136,112\n"
"AR 83,89\nMU 82,82,86\nMU 83,83,86\nMvav 168,144,112\nRa 128,3,0\nCV 42,82,83\nAR 82,90\nMvav 168,148,120\nAR 83,91\nMU 82,82,86\nMU 83,83,86\nMvav 172,148,124\nRa 132,3,1024\n"
"CV 43,82,83\ns_add_u32 s5, s94, 655360\nMvav 172,144,116\nWL 8\nSv 40,1,16,5,0\nAR 82,92\nMvav 180,152,116\nRa 136,3,2048\nAR 83,93\nMU 82,82,87\nMvav 180,156,124\nMU 83,83,87\n"
"CV 46,82,83\nAR 82,94\nMvav 176,156,120\nRa 140,3,3072\nAR 83,95\nMU 82,82,87\nMvav 176,152,112\nMU 83,83,87\nCV 47,82,83\nSv 44,1,16,5,1024\nMvav 184,160,112\nRa 144,3,4096\n"
"AR 82,96\nAR 83,97\nMvav 184,164,120\nMU 82,82,86\nMU 83,83,86\nbuffer_load_dword v27, off, s[16:19], s60 offset:512 sc0 sc1\nMvav 188,164,124\nRa 148,3,5120\nCV 48,82,83\n"
"AR 82,98\nMvav 188,160,116\nWL 8\nAR 83,99\nMU 82,82,86\nMvav 196,168,116\nRa 152,3,6144\nMU 83,83,86\nCV 49,82,83\nMvav 196,172,124\nAR 82,100\nAR 83,101\nMU 82,82,87\n"
"Mvav 192,172,120\nRa 156,3,7168\nMU 83,83,87\nMvav 192,168,112\nCV 52,82,83\nAR 82,102\nAR 83,103\nMvav 200,176,112\nRa 160,3,8192\nMU 82,82,87\nMU 83,83,87\nMvav 200,180,120\n"
"CV 53,82,83\nAR 82,104\nAR 83,105\nMvav 204,180,124\nRa 164,3,9216\nMU 82,82,86\nMU 83,83,86\nMvav 204,176,116\nWL 8\nCV 50,82,83\nMvav 212,184,116\nRa 168,3,10240\nAR 82,106\n"
"AR 83,107\nMvav 212,188,124\nMU 82,82,86\nMU 83,83,86\nCV 51,82,83\nMvav 208,188,120\nRa 172,3,11264\ns_add_u32 s5, s94, 786432\nSv 48,1,16,5,0\nMvav 208,184,112\nAR 82,108\n"
"AR 83,109\nMU 82,82,87\nMvav 216,128,112\nRa 176,3,12288\nMU 83,83,87\nCV 54,82,83\nMvav 216,132,120\nAR 82,110\nAR 83,111\nMvav 220,132,124\nRa 180,3,13312\nMU 82,82,87\n"
"MU 83,83,87\nMvav 220,128,116\nWL 8\nCV 55,82,83\nSv 52,1,16,5,1024\nMvav 228,136,116\nRa 184,3,14336\nAR 82,112\nAR 83,113\nMvav 228,140,124\nMU 82,82,86\nMU 83,83,86\n"
"CV 56,82,83\nMvav 224,140,120\nRa 188,3,15360\nAR 82,114\nMvav 224,136,112\nAR 83,115\nMU 82,82,86\nMU 83,83,86\nMvav 232,144,112\nRa 128,3,16384\nCV 57,82,83\nAR 82,116\n"
"Mvav 232,148,120\nAR 83,117\nMU 82,82,87\nMU 83,83,87\nMvav 236,148,124\nRa 132,3,17408\nCV 60,82,83\nAR 82,118\nMvav 236,144,116\nWL 8\nAR 83,119\nMvav 244,152,116\n"
"Ra 136,3,18432\nMU 82,82,87\nMU 83,83,87\nMvav 244,156,124\nCV 61,82,83\nAR 82,120\nAR 83,121\nMvav 240,156,120\nRa 140,3,19456\nMU 82,82,86\nMU 83,83,86\nMvav 240,152,112\n"
"CV 58,82,83\nAR 82,122\nAR 83,123\nMvav 136,160,32\nRa 144,3,20480\nMU 82,82,86\nMU 83,83,86\nMvav 136,164,40\nCV 59,82,83\ns_add_u32 s5, s94, 917504\nMvav 140,164,44\n"
"Ra 148,3,21504\nSv 56,1,16,5,0\nAR 82,124\nMvav 140,160,36\nWL 8\nAR 83,125\nMU 82,82,87\nMvav 148,168,36\nRa 152,3,22528\nMU 83,83,87\nCV 62,82,83\nMvav 148,172,44\nAR 82,126\n"
"AR 83,127\nMU 82,82,87\nMvav 144,172,40\nRa 156,3,23552\nMU 83,83,87\nMvav 144,168,32\nCV 63,82,83\nSv 60,1,16,5,1024\nMvav 152,176,32\nRa 160,3,24576\nWV 19\nMvav 152,180,40\n"
"v_xor_b32 v92, s15, v66\nMvav 156,180,44\nRa 164,3,25600\nMvav 156,176,36\nWL 8\nMvav 164,184,36\nRa 168,3,26624\nMvav 164,188,44\nv_xor_b32 v93, s15, v70\nMvav 160,188,40\n"
"Ra 172,3,27648\nMvav 160,184,32\nv_xor_b32 v94, s15, v74\nMvav 168,128,32\nRa 176,3,28672\nv_or3_b32 v92, v92, v93, v94\nMvav 168,132,40\nv_xor_b32 v93, s15, v78\nMvav 172,132,44\n"
"Ra 180,3,29696\nMvav 172,128,36\ns_waitcnt vmcnt(16) lgkmcnt(8)\nMvav 180,136,36\nRa 184,3,30720\nMvav 180,140,44\nv_or_b32 v92, v92, v93\nv_cmp_ne_u32 vcc, 0, v92\ns_nop 0\n"
"s_cbranch_vccnz .S182\n.S183:\nMvav 176,140,40\nRa 188,3,31744\ns_barrier\nMvav 176,136,32\ns_mov_b32 s2, 0xc040000\nMvav 184,144,32\nRa 128,3,32768\n"
"buffer_load_dword v80, v88, s[16:19], s2 offen sc0 sc1\nMvav 184,148,40\nMvav 188,148,44\nRa 132,3,33792\nMvav 188,144,36\nWL 8\nMvav 196,152,36\nRa 136,3,34816\nMvav 196,156,44\n"
"buffer_load_dword v81, v88, s[16:19], s2 offen offset:64 sc0 sc1\nMvav 192,156,40\nRa 140,3,35840\nMvav 192,152,32\nv_and_b32 v92, 0x7ff, v67\nMvav 200,160,32\nRa 144,3,36864\n"
"v_lshrrev_b32 v93, 16, v67\nWV 6\nv_cmp_ne_u32 vcc, s15, v27\ns_nop 0\ns_cbranch_vccnz .S185\n.S186:\nMvav 200,164,40\ns_add_u32 s4, s92, 0\nMvav 204,164,44\nRa 148,3,37888\n"
"Mvav 204,160,36\nWL 8\ns_add_u32 m0, s89, 16384\ns_add_u32 s46, s93, 65536\nbuffer_load_dwordx4 a[192:195], v1, s[16:19], s4 offen offset:0 sc0 sc1\nMvav 212,168,36\n"
"Ra 152,3,38912\nMvav 212,172,44\nD1 1,16,46,0\nMvav 208,172,40\nRa 156,3,39936\nMvav 208,168,32\nbuffer_load_dwordx4 a[196:199], v1, s[16:19], s4 offen offset:1024 sc0 sc1\n"
"Mvav 216,176,32\nRa 160,3,40960\nv_add_u32 v94, 16, v90\nMvav 216,180,40\ns_add_u32 s4, s92, 131072\nv_cmp_eq_u32 vcc, v92, v90\ns_nop 1\nv_cndmask_b32 v12, v12, v64, vcc\n"
"v_cndmask_b32 v13, v13, v65, vcc\nv_cndmask_b32 v14, v14, v68, vcc\nv_cndmask_b32 v15, v15, v69, vcc\nMvav 220,180,44\nRa 164,3,41984\nv_cmp_eq_u32 vcc, v93, v90\ns_nop 1\n"
"v_cndmask_b32 v12, v12, v72, vcc\nv_cndmask_b32 v13, v13, v73, vcc\nv_cndmask_b32 v14, v14, v76, vcc\nv_cndmask_b32 v15, v15, v77, vcc\nMvav 220,176,36\nWL 8\n"
"buffer_load_dwordx4 a[200:203], v1, s[16:19], s4 offen offset:0 sc0 sc1\nMvav 228,184,36\nRa 168,3,43008\nv_cmp_eq_u32 vcc, v92, v94\ns_nop 1\nv_cndmask_b32 v16, v16, v64, vcc\n"
"v_cndmask_b32 v17, v17, v65, vcc\nv_cndmask_b32 v18, v18, v68, vcc\nv_cndmask_b32 v19, v19, v69, vcc\nMvav 228,188,44\nD1 1,16,46,1024\nv_cmp_eq_u32 vcc, v93, v94\ns_nop 1\n"
"v_cndmask_b32 v16, v16, v72, vcc\nv_cndmask_b32 v17, v17, v73, vcc\nv_cndmask_b32 v18, v18, v76, vcc\nv_cndmask_b32 v19, v19, v77, vcc\nMvav 224,188,40\nRa 172,3,44032\n"
"Mvav 224,184,32\nbuffer_load_dwordx4 a[204:207], v1, s[16:19], s4 offen offset:1024 sc0 sc1\nMvav 232,128,32\nRa 176,3,45056\nMvav 232,132,40\ns_add_u32 s4, s92, 262144\n"
"Mvav 236,132,44\nRa 180,3,46080\nMvav 236,128,36\nWL 8\nbuffer_load_dwordx4 a[208:211], v1, s[16:19], s4 offen offset:0 sc0 sc1\nMvav 244,136,36\nRa 184,3,47104\nMvav 244,140,44\n"
"D1 1,16,46,2048\nMvav 240,140,40\nRa 188,3,48128\nMvav 240,136,32\nbuffer_load_dwordx4 a[212:215], v1, s[16:19], s4 offen offset:1024 sc0 sc1\nMvav 136,144,48\nRa 128,3,49152\n"
"Mvav 136,148,56\ns_add_u32 s4, s92, 393216\nMvav 140,148,60\nRa 132,3,50176\nMvav 140,144,52\nWL 8\nbuffer_load_dwordx4 a[216:219], v1, s[16:19], s4 offen offset:0 sc0 sc1\n"
"Mvav 148,152,52\nRa 136,3,51200\nMvav 148,156,60\nD1 1,16,46,3072\nMvav 144,156,56\nRa 140,3,52224\nMvav 144,152,48\n"
"buffer_load_dwordx4 a[220:223], v1, s[16:19], s4 offen offset:1024 sc0 sc1\nMvav 152,160,48\nRa 144,3,53248\nMvav 152,164,56\nMvav 156,164,60\nRa 148,3,54272\nMvav 156,160,52\n"
"WL 8\ns_add_u32 m0, s89, 0\ns_add_u32 s46, s93, 98304\nMvav 164,168,52\nRa 152,3,55296\nMvav 164,172,60\nD1 1,16,46,0\nMvav 160,172,56\nRa 156,3,56320\nMvav 160,168,48\n"
"Mvav 168,176,48\nRa 160,3,57344\nMvav 168,180,56\nMvav 172,180,60\nRa 164,3,58368\nMvav 172,176,52\nWL 8\nMvav 180,184,52\nRa 168,3,59392\nMvav 180,188,60\nD1 1,16,46,1024\n"
"Mvav 176,188,56\nRa 172,3,60416\nMvav 176,184,48\nMvav 184,128,48\nRa 176,3,61440\nMvav 184,132,56\nMvav 188,132,60\nRa 180,3,62464\nMvav 188,128,52\nWL 8\nMvav 196,136,52\n"
"Ra 184,3,63488\nMvav 196,140,60\nD1 1,16,46,2048\nMvav 192,140,56\nRa 188,3,64512\ns_barrier\nMvav 192,136,48\nMvav 200,144,48\nRa 128,0,0\nMvav 200,148,56\nMvav 204,148,60\n"
"Ra 132,0,1024\nMvav 204,144,52\nWL 8\nMvav 212,152,52\nRa 136,0,2048\nMvav 212,156,60\nD1 1,16,46,3072\nMvav 208,156,56\nRa 140,0,3072\nMvav 208,152,48\nMvav 216,160,48\n"
"Ra 144,0,4096\nMvav 216,164,56\nMvav 220,164,60\nRa 148,0,5120\nMvav 220,160,52\nWL 8\ns_add_u32 m0, s89, 32768\ns_add_u32 s46, s93, 131072\nMvav 228,168,52\nRa 152,0,6144\n"
"Mvav 228,172,60\nD1 1,16,46,0\nMvav 224,172,56\nRa 156,0,7168\nWV 5\nAR 22,195\nMvav 224,168,48\nAR 23,199\nv_max_u32 v21, v22, v23\nMvav 232,176,48\nRa 160,0,8192\nAR 22,203\n"
"Mvav 232,180,56\nAR 23,207\nv_max3_u32 v21, v21, v22, v23\nMvav 236,180,60\nRa 164,0,9216\nAR 22,211\nMvav 236,176,52\nWL 6\nAR 23,215\nv_max3_u32 v21, v21, v22, v23\n"
"Mvav 244,184,52\nRa 168,0,10240\nAR 22,219\nMvav 244,188,60\nD1 1,16,46,1024\nAR 23,223\nMvav 240,188,56\nRa 172,0,11264\nv_max3_u32 v21, v21, v22, v23\nMvav 240,184,48\n"
"v_cmp_eq_u32 vcc, -1, v21\ns_nop 0\ns_cbranch_vccnz .S176\n.S177:\nMvaa 136,128,192\nRa 176,0,12288\nMvaa 136,132,200\nv_sub_f32 v92, 1.0, v80\nMvaa 136,136,208\nRa 180,0,13312\n"
"Mvaa 136,140,216\nWL 6\nMvaa 140,140,220\nRa 184,0,14336\nv_sub_f32 v93, 1.0, v81\nMvaa 140,136,212\nD1 1,16,46,2048\nMvaa 140,132,204\nRa 188,0,15360\nMvaa 140,128,196\n"
"CV 92,92,93\nMvaa 144,144,192\nRa 128,0,16384\nv_and_b32 v93, 0xffff0000, v92\nMvaa 144,148,200\nv_lshlrev_b32 v92, 16, v92\nMvaa 144,152,208\nRa 132,0,17408\nMvaa 144,156,216\n"
"WL 6\nMvaa 148,156,220\nRa 136,0,18432\nv_lshlrev_b32 v64, 16, v12\nMvaa 148,152,212\nD1 1,16,46,3072\nMvaa 148,148,204\nRa 140,0,19456\nMvaa 148,144,196\n"
"v_and_b32 v65, 0xffff0000, v12\nMvaa 152,160,192\nRa 144,0,20480\nMU 64,64,92\nMvaa 152,164,200\nMU 65,65,92\nMvaa 152,168,208\nRa 148,0,21504\nMvaa 152,172,216\nWL 6\n"
"s_add_u32 m0, s89, 49152\ns_add_u32 s46, s93, 163840\nMvaa 156,172,220\nRa 152,0,22528\nv_lshlrev_b32 v66, 16, v13\nMvaa 156,168,212\nD1 1,16,46,0\nMvaa 156,164,204\n"
"Ra 156,0,23552\nMvaa 156,160,196\nv_and_b32 v67, 0xffff0000, v13\nMvaa 160,176,192\nRa 160,0,24576\nMU 66,66,92\nMvaa 160,180,200\ns_add_u32 s4, s92, 524288\nMvaa 160,184,208\n"
"Ra 164,0,25600\nMvaa 160,188,216\nWL 6\nbuffer_load_dwordx4 a[224:227], v1, s[16:19], s4 offen offset:0 sc0 sc1\nMvaa 164,188,220\nRa 168,0,26624\nMU 67,67,92\nMvaa 164,184,212\n"
"D1 1,16,46,1024\nMvaa 164,180,204\nRa 172,0,27648\nMvaa 164,176,196\nbuffer_load_dwordx4 a[228:231], v1, s[16:19], s4 offen offset:1024 sc0 sc1\nMvaa 168,128,192\nRa 176,0,28672\n"
"v_lshlrev_b32 v68, 16, v14\nMvaa 168,132,200\ns_add_u32 s4, s92, 655360\nMvaa 168,136,208\nRa 180,0,29696\nMvaa 168,140,216\ns_waitcnt vmcnt(8) lgkmcnt(6)\n"
"buffer_load_dwordx4 a[232:235], v1, s[16:19], s4 offen offset:0 sc0 sc1\nMvaa 172,140,220\nRa 184,0,30720\nv_and_b32 v69, 0xffff0000, v14\nMvaa 172,136,212\nD1 1,16,46,2048\n"
"Mvaa 172,132,204\nRa 188,0,31744\ns_barrier\nMvaa 172,128,196\nbuffer_load_dwordx4 a[236:239], v1, s[16:19], s4 offen offset:1024 sc0 sc1\nMvaa 176,144,192\nRa 128,2,16384\n"
"MU 68,68,92\nMvaa 176,148,200\ns_add_u32 s4, s92, 786432\nMvaa 176,152,208\nRa 132,2,17408\nMvaa 176,156,216\nWL 6\n"
"buffer_load_dwordx4 a[240:243], v1, s[16:19], s4 offen offset:0 sc0 sc1\nMvaa 180,156,220\nRa 136,2,18432\nMU 69,69,92\nMvaa 180,152,212\nD1 1,16,46,3072\nMvaa 180,148,204\n"
"Ra 140,2,19456\nMvaa 180,144,196\nbuffer_load_dwordx4 a[244:247], v1, s[16:19], s4 offen offset:1024 sc0 sc1\nMvaa 184,160,192\nRa 144,2,20480\nv_lshlrev_b32 v70, 16, v15\n"
"Mvaa 184,164,200\ns_add_u32 s4, s92, 917504\nMvaa 184,168,208\nRa 148,2,21504\nMvaa 184,172,216\nWL 6\ns_add_u32 m0, s89, 65536\ns_add_u32 s46, s93, 196608\n"
"buffer_load_dwordx4 a[248:251], v1, s[16:19], s4 offen offset:0 sc0 sc1\nMvaa 188,172,220\nRa 152,2,22528\nv_and_b32 v71, 0xffff0000, v15\nMvaa 188,168,212\nD1 1,16,46,0\n"
"Mvaa 188,164,204\nRa 156,2,23552\nMvaa 188,160,196\nbuffer_load_dwordx4 a[252:255], v1, s[16:19], s4 offen offset:1024 sc0 sc1\nMvaa 192,176,192\nRa 160,2,24576\nMU 70,70,92\n"
"Mvaa 192,180,200\nMU 71,71,92\nMvaa 192,184,208\nRa 164,2,25600\nMvaa 192,188,216\nWL 6\nMvaa 196,188,220\nRa 168,2,26624\nv_lshlrev_b32 v72, 16, v16\nMvaa 196,184,212\n"
"D1 1,16,46,1024\nMvaa 196,180,204\nRa 172,2,27648\nMvaa 196,176,196\nv_and_b32 v73, 0xffff0000, v16\nMvaa 200,128,192\nRa 176,2,28672\nMU 72,72,93\nMvaa 200,132,200\nMU 73,73,93\n"
"Mvaa 200,136,208\nRa 180,2,29696\nMvaa 200,140,216\nWL 6\nMvaa 204,140,220\nRa 184,2,30720\nv_lshlrev_b32 v74, 16, v17\nMvaa 204,136,212\nD1 1,16,46,2048\nMvaa 204,132,204\n"
"Ra 188,2,31744\nMvaa 204,128,196\nv_and_b32 v75, 0xffff0000, v17\nMvaa 208,144,192\nRa 128,2,0\nMU 74,74,93\nMvaa 208,148,200\nMU 75,75,93\nMvaa 208,152,208\nRa 132,2,1024\n"
"Mvaa 208,156,216\nWL 6\nMvaa 212,156,220\nRa 136,2,2048\nv_lshlrev_b32 v76, 16, v18\nMvaa 212,152,212\nD1 1,16,46,3072\nMvaa 212,148,204\nRa 140,2,3072\nMvaa 212,144,196\n"
"v_and_b32 v77, 0xffff0000, v18\nMvaa 216,160,192\nRa 144,2,4096\nMU 76,76,93\nMvaa 216,164,200\nv_bfe_u32 v24, v1, 4, 3\nMvaa 216,168,208\nRa 148,2,5120\nMvaa 216,172,216\nWL 6\n"
"Mvaa 220,172,220\nRa 152,2,6144\nMU 77,77,93\nMvaa 220,168,212\nv_mul_u32_u24 v24, 0xc00, v24\nMvaa 220,164,204\nRa 156,2,7168\nMvaa 220,160,196\nv_bfe_u32 v26, v1, 7, 1\n"
"Mvaa 224,176,192\nRa 160,2,8192\nv_lshlrev_b32 v78, 16, v19\nMvaa 224,180,200\nv_add_lshl_u32 v24, v24, v26, 13\nMvaa 224,184,208\nRa 164,2,9216\nMvaa 224,188,216\nWL 6\n"
"Mvaa 228,188,220\nRa 168,2,10240\nv_and_b32 v79, 0xffff0000, v19\nMvaa 228,184,212\nbuffer_load_dword v27, v24, s[16:19], s41 offen sc0 sc1\nMvaa 228,180,204\nRa 172,2,11264\n"
"Mvaa 228,176,196\nMU 78,78,93\nMvaa 232,128,192\nRa 176,2,12288\nMU 79,79,93\nMvaa 232,132,200\nMvaa 232,136,208\nRa 180,2,13312\nMvaa 232,140,216\ns_waitcnt vmcnt(8) lgkmcnt(6)\n"
"Mvaa 236,140,220\nRa 184,2,14336\nMvaa 236,136,212\nMvaa 236,132,204\nRa 188,2,15360\ns_barrier\nWV 4\nMvaa 236,128,196\nAR 22,227\nAR 23,231\nMvaa 240,144,192\nRa 128,2,32768\n"
"v_max_u32 v21, v22, v23\nMvaa 240,148,200\nAR 22,235\nAR 23,239\nMvaa 240,152,208\nRa 132,2,33792\nv_max3_u32 v21, v21, v22, v23\nMvaa 240,156,216\nWL 6\nAR 22,243\nAR 23,247\n"
"Mvaa 244,156,220\nRa 136,2,34816\nv_max3_u32 v21, v21, v22, v23\nMvaa 244,152,212\nAR 22,251\nAR 23,255\nMvaa 244,148,204\nRa 140,2,35840\nv_max3_u32 v21, v21, v22, v23\n"
"Mvaa 244,144,196\nv_cmp_eq_u32 vcc, -1, v21\ns_nop 0\ns_cbranch_vccnz .S179\n.S180:\nMvaa 136,160,224\nRa 144,2,36864\nMvaa 136,164,232\nMvaa 136,168,240\nRa 148,2,37888\n"
"Mvaa 136,172,248\nWL 6\nMvaa 140,172,252\nRa 152,2,38912\nMvaa 140,168,244\nMvaa 140,164,236\nRa 156,2,39936\nMvaa 140,160,228\nMvaa 144,176,224\nRa 160,2,40960\nMvaa 144,180,232\n"
"Mvaa 144,184,240\nRa 164,2,41984\nMvaa 144,188,248\nWL 6\nMvaa 148,188,252\nRa 168,2,43008\nMvaa 148,184,244\nMvaa 148,180,236\nRa 172,2,44032\nMvaa 148,176,228\nCV 136,136,137\n"
"Mvaa 152,128,224\nRa 176,2,45056\nCV 137,138,139\nMvaa 152,132,232\nCV 138,144,145\nMvaa 152,136,240\nRa 180,2,46080\nMvaa 152,140,248\nWL 6\nMvaa 156,140,252\nRa 184,2,47104\n"
"Mvaa 156,136,244\nCV 139,146,147\nMvaa 156,132,236\nRa 188,2,48128\ns_add_u32 s5, s95, 0\nMvaa 156,128,228\nSv 136,1,16,5,0\nMvaa 160,144,224\nRa 128,2,49152\nMvaa 160,148,232\n"
"CV 140,140,141\nMvaa 160,152,240\nRa 132,2,50176\nMvaa 160,156,248\nWL 6\nCV 141,142,143\nMvaa 164,156,252\nRa 136,2,51200\nMvaa 164,152,244\nCV 142,148,149\nMvaa 164,148,236\n"
"Ra 140,2,52224\nMvaa 164,144,228\nCV 143,150,151\nMvaa 168,160,224\nRa 144,2,53248\nSv 140,1,16,5,1024\nMvaa 168,164,232\nCV 152,152,153\nMvaa 168,168,240\nRa 148,2,54272\n"
"Mvaa 168,172,248\nWL 6\nMvaa 172,172,252\nRa 152,2,55296\nMvaa 172,168,244\nCV 153,154,155\nMvaa 172,164,236\nRa 156,2,56320\nCV 154,160,161\nMvaa 172,160,228\nCV 155,162,163\n"
"Mvaa 176,176,224\nRa 160,2,57344\nMvaa 176,180,232\ns_add_u32 s5, s95, 131072\nMvaa 176,184,240\nRa 164,2,58368\nMvaa 176,188,248\nWL 6\nSv 152,1,16,5,0\nMvaa 180,188,252\n"
"Ra 168,2,59392\nMvaa 180,184,244\nCV 156,156,157\nMvaa 180,180,236\nRa 172,2,60416\nMvaa 180,176,228\nCV 157,158,159\nMvaa 184,128,224\nRa 176,2,61440\nCV 158,164,165\n"
"Mvaa 184,132,232\nCV 159,166,167\nMvaa 184,136,240\nRa 180,2,62464\nMvaa 184,140,248\ns_waitcnt vmcnt(4) lgkmcnt(6)\nMvaa 188,140,252\nRa 184,2,63488\nMvaa 188,136,244\n"
"Sv 156,1,16,5,1024\nMvaa 188,132,236\nRa 188,2,64512\ns_barrier\nMvaa 188,128,228\nCV 168,168,169\nMvaa 192,144,224\nRa 128,3,0\nMvaa 192,148,232\nCV 169,170,171\n"
"Mvaa 192,152,240\nRa 132,3,1024\nMvaa 192,156,248\nWL 6\nCV 170,176,177\nMvaa 196,156,252\nRa 136,3,2048\nMvaa 196,152,244\nCV 171,178,179\nMvaa 196,148,236\nRa 140,3,3072\n"
"Mvaa 196,144,228\ns_add_u32 s5, s95, 262144\nMvaa 200,160,224\nRa 144,3,4096\nSv 168,1,16,5,0\nMvaa 200,164,232\nCV 172,172,173\nMvaa 200,168,240\nRa 148,3,5120\nMvaa 200,172,248\n"
"WL 6\nMvaa 204,172,252\nRa 152,3,6144\nMvaa 204,168,244\nCV 173,174,175\nMvaa 204,164,236\nRa 156,3,7168\nCV 174,180,181\nMvaa 204,160,228\nCV 175,182,183\nMvaa 208,176,224\n"
"Ra 160,3,8192\nMvaa 208,180,232\nSv 172,1,16,5,1024\nMvaa 208,184,240\nRa 164,3,9216\nMvaa 208,188,248\nWL 6\nCV 184,184,185\nMvaa 212,188,252\nRa 168,3,10240\nMvaa 212,184,244\n"
"CV 185,186,187\nMvaa 212,180,236\nRa 172,3,11264\nMvaa 212,176,228\nCV 186,192,193\nMvaa 216,128,224\nRa 176,3,12288\nCV 187,194,195\nMvaa 216,132,232\ns_add_u32 s5, s95, 393216\n"
"Mvaa 216,136,240\nRa 180,3,13312\nMvaa 216,140,248\nWL 6\nMvaa 220,140,252\nRa 184,3,14336\nMvaa 220,136,244\nSv 184,1,16,5,0\nMvaa 220,132,236\nRa 188,3,15360\nCV 188,188,189\n"
"Mvaa 220,128,228\nCV 189,190,191\nMvaa 224,144,224\nCV 190,196,197\nMvaa 224,148,232\nCV 191,198,199\nMvaa 224,152,240\nSv 188,1,16,5,1024\nMvaa 224,156,248\nWL 4\nCV 200,200,201\n"
"Mvaa 228,156,252\nCV 201,202,203\nMvaa 228,152,244\nCV 202,208,209\nMvaa 228,148,236\nCV 203,210,211\nMvaa 228,144,228\ns_add_u32 s5, s95, 524288\nMvaa 232,160,224\n"
"Sv 200,1,16,5,0\nCV 204,204,205\nMvaa 232,164,232\nCV 205,206,207\nMvaa 232,168,240\nCV 206,212,213\nMvaa 232,172,248\nWL 0\nMvaa 236,172,252\nCV 207,214,215\nMvaa 236,168,244\n"
"Sv 204,1,16,5,1024\nMvaa 236,164,236\nCV 216,216,217\nCV 217,218,219\nMvaa 236,160,228\nCV 218,224,225\nMvaa 240,176,224\nCV 219,226,227\nMvaa 240,180,232\n"
"s_add_u32 s5, s95, 655360\nMvaa 240,184,240\nSv 216,1,16,5,0\nMvaa 240,188,248\nCV 220,220,221\nCV 221,222,223\nMvaa 244,188,252\nCV 222,228,229\nMvaa 244,184,244\nCV 223,230,231\n"
"Mvaa 244,180,236\nSv 220,1,16,5,1024\nMvaa 244,176,228\nCV 232,232,233\nCV 233,234,235\nCV 234,240,241\nCV 235,242,243\ns_add_u32 s5, s95, 786432\nSv 232,1,16,5,0\nCV 236,236,237\n"
"CV 237,238,239\nCV 238,244,245\nCV 239,246,247\nSv 236,1,16,5,1024\ns_cmp_eq_u32 s13, 7\ns_cbranch_scc1 .Z191\nWV 14\nv_cmp_eq_u32 s[62:63], s15, v27\ns_mov_b32 vcc_hi, 0\n"
"s_nop 2\ns_lshr_b32 s63, s62, 8\ns_or_b32 s62, s62, s63\ns_andn2_b32 vcc_lo, 0xff, s62\ns_mov_b64 vcc, vcc\ns_cbranch_vccnz .S188\n.S189:\ns_mov_b32 s7, 262144\n"
"s_mov_b32 s3, 0xff\n.Z192:\ns_bitcmp1_b32 s3, 0\ns_cbranch_scc0 .Z194\ns_bitcmp1_b32 s63, 0\ns_cselect_b32 s2, 0x2000, 0\ns_xor_b32 s2, s2, s20\ns_add_u32 s2, s2, 0x0\n"
"buffer_load_dwordx4 v[96:99], v1, s[16:19], s2 offen offset:0 sc0 sc1\nbuffer_load_dwordx4 v[100:103], v1, s[16:19], s2 offen offset:1024 sc0 sc1\n.Z194:\ns_bitcmp1_b32 s3, 1\n"
"s_cbranch_scc0 .Z195\ns_bitcmp1_b32 s63, 1\ns_cselect_b32 s2, 0x2000, 0\ns_xor_b32 s2, s2, s20\ns_add_u32 s2, s2, 0x1800000\n"
"buffer_load_dwordx4 v[104:107], v1, s[16:19], s2 offen offset:0 sc0 sc1\nbuffer_load_dwordx4 v[108:111], v1, s[16:19], s2 offen offset:1024 sc0 sc1\n.Z195:\ns_bitcmp1_b32 s3, 2\n"
"s_cbranch_scc0 .Z196\ns_bitcmp1_b32 s63, 2\ns_cselect_b32 s2, 0x2000, 0\ns_xor_b32 s2, s2, s20\ns_add_u32 s2, s2, 0x3000000\n"
"buffer_load_dwordx4 v[112:115], v1, s[16:19], s2 offen offset:0 sc0 sc1\nbuffer_load_dwordx4 v[116:119], v1, s[16:19], s2 offen offset:1024 sc0 sc1\n.Z196:\ns_bitcmp1_b32 s3, 3\n"
"s_cbranch_scc0 .Z197\ns_bitcmp1_b32 s63, 3\ns_cselect_b32 s2, 0x2000, 0\ns_xor_b32 s2, s2, s20\ns_add_u32 s2, s2, 0x4800000\n"
"buffer_load_dwordx4 v[120:123], v1, s[16:19], s2 offen offset:0 sc0 sc1\nbuffer_load_dwordx4 v[124:127], v1, s[16:19], s2 offen offset:1024 sc0 sc1\n.Z197:\ns_bitcmp1_b32 s3, 4\n"
"s_cbranch_scc0 .Z198\ns_bitcmp1_b32 s63, 4\ns_cselect_b32 s2, 0x2000, 0\ns_xor_b32 s2, s2, s20\ns_add_u32 s2, s2, 0x6000000\n"
"buffer_load_dwordx4 v[32:35], v1, s[16:19], s2 offen offset:0 sc0 sc1\nbuffer_load_dwordx4 v[36:39], v1, s[16:19], s2 offen offset:1024 sc0 sc1\n.Z198:\ns_bitcmp1_b32 s3, 5\n"
"s_cbranch_scc0 .Z199\ns_bitcmp1_b32 s63, 5\ns_cselect_b32 s2, 0x2000, 0\ns_xor_b32 s2, s2, s20\ns_add_u32 s2, s2, 0x7800000\n"
"buffer_load_dwordx4 v[40:43], v1, s[16:19], s2 offen offset:0 sc0 sc1\nbuffer_load_dwordx4 v[44:47], v1, s[16:19], s2 offen offset:1024 sc0 sc1\n.Z199:\ns_bitcmp1_b32 s3, 6\n"
"s_cbranch_scc0 .Z200\ns_bitcmp1_b32 s63, 6\ns_cselect_b32 s2, 0x2000, 0\ns_xor_b32 s2, s2, s20\ns_add_u32 s2, s2, 0x9000000\n"
"buffer_load_dwordx4 v[48:51], v1, s[16:19], s2 offen offset:0 sc0 sc1\nbuffer_load_dwordx4 v[52:55], v1, s[16:19], s2 offen offset:1024 sc0 sc1\n.Z200:\ns_bitcmp1_b32 s3, 7\n"
"s_cbranch_scc0 .Z201\ns_bitcmp1_b32 s63, 7\ns_cselect_b32 s2, 0x2000, 0\ns_xor_b32 s2, s2, s20\ns_add_u32 s2, s2, 0xa800000\n"
"buffer_load_dwordx4 v[56:59], v1, s[16:19], s2 offen offset:0 sc0 sc1\nbuffer_load_dwordx4 v[60:63], v1, s[16:19], s2 offen offset:1024 sc0 sc1\n.Z201:\ns_mov_b32 s3, 0\nWV 0\n"
"v_max_u32 v82, v99, v103\nv_cmp_eq_u32 s[44:45], -1, v82\nv_max_u32 v82, v107, v111\nv_cmp_eq_u32 s[46:47], -1, v82\nv_max_u32 v82, v115, v119\nv_cmp_eq_u32 s[48:49], -1, v82\n"
"v_max_u32 v82, v123, v127\nv_cmp_eq_u32 s[50:51], -1, v82\nv_max_u32 v82, v35, v39\nv_cmp_eq_u32 s[52:53], -1, v82\nv_max_u32 v82, v43, v47\nv_cmp_eq_u32 s[54:55], -1, v82\n"
"v_max_u32 v82, v51, v55\nv_cmp_eq_u32 s[56:57], -1, v82\nv_max_u32 v82, v59, v63\nv_cmp_eq_u32 s[58:59], -1, v82\ns_cmp_lg_u64 s[44:45], 0\ns_cselect_b32 s2, 1, 0\n"
"s_or_b32 s3, s3, s2\ns_cmp_lg_u64 s[46:47], 0\ns_cselect_b32 s2, 2, 0\ns_or_b32 s3, s3, s2\ns_cmp_lg_u64 s[48:49], 0\ns_cselect_b32 s2, 4, 0\ns_or_b32 s3, s3, s2\n"
"s_cmp_lg_u64 s[50:51], 0\ns_cselect_b32 s2, 8, 0\ns_or_b32 s3, s3, s2\ns_cmp_lg_u64 s[52:53], 0\ns_cselect_b32 s2, 16, 0\ns_or_b32 s3, s3, s2\ns_cmp_lg_u64 s[54:55], 0\n"
"s_cselect_b32 s2, 32, 0\ns_or_b32 s3, s3, s2\ns_cmp_lg_u64 s[56:57], 0\ns_cselect_b32 s2, 64, 0\ns_or_b32 s3, s3, s2\ns_cmp_lg_u64 s[58:59], 0\ns_cselect_b32 s2, 128, 0\n"
"s_or_b32 s3, s3, s2\ns_cbranch_scc0 .Z193\ns_sub_u32 s7, s7, 1\ns_cbranch_scc0 .Z192\n.Z193:\nZvvv 136,130,96\nMvvv 136,130,104\nMvvv 136,130,112\nMvvv 136,130,120\n"
"Mvvv 136,130,32\nMvvv 136,130,40\nMvvv 136,130,48\nMvvv 136,130,56\nZvvv 140,128,96\nMvvv 140,128,104\nMvvv 140,128,112\nMvvv 140,128,120\nMvvv 140,128,32\nMvvv 140,128,40\n"
"Mvvv 140,128,48\nMvvv 140,128,56\nZvvv 144,130,100\nMvvv 144,130,108\nMvvv 144,130,116\nMvvv 144,130,124\nMvvv 144,130,36\nMvvv 144,130,44\nMvvv 144,130,52\nMvvv 144,130,60\n"
"Zvvv 148,128,100\nMvvv 148,128,108\nMvvv 148,128,116\nMvvv 148,128,124\nMvvv 148,128,36\nMvvv 148,128,44\nMvvv 148,128,52\nMvvv 148,128,60\nFM 64,80,136\nFM 65,80,137\n"
"FM 66,80,138\nFM 67,80,139\nFM 68,80,140\nFM 69,80,141\nFM 70,80,142\nFM 71,80,143\nCV 12,64,65\nCV 13,66,67\nCV 14,68,69\nCV 15,70,71\nSv 12,91,36,22,0\nFM 72,81,144\n"
"FM 73,81,145\nFM 74,81,146\nFM 75,81,147\nFM 76,81,148\nFM 77,81,149\nFM 78,81,150\nFM 79,81,151\nCV 16,72,73\nCV 17,74,75\nCV 18,76,77\nCV 19,78,79\nSv 16,91,36,23,0\n.Z191:\n"
"s_endpgm\n.HD117:\nbuffer_store_dword v25, off, s[16:19], s54 offset:4 sc0 sc1\ns_mov_b32 s90, 0\ns_swappc_b64 s[8:9], s[80:81]\nv_and_b32 v27, 0x1f0, v1\n"
"buffer_load_dword v27, v27, s[16:19], s58 offen offset:4 sc0 sc1\ns_mov_b32 s90, 0\ns_swappc_b64 s[8:9], s[80:81]\nv_cmp_ne_u32 vcc, s15, v27\ns_nop 0\ns_cbranch_vccnz .S202\n"
".S203:\ns_mov_b32 s90, 100\ns_swappc_b64 s[8:9], s[80:81]\n.S118:\ns_mov_b32 s7, 262144\n.S120:\ns_sleep 8\ns_lshl_b32 s5, s73, 7\ns_add_u32 s5, s5, 0xc04ac00\n"
"v_bfe_u32 v27, v1, 2, 7\nbuffer_load_dword v27, v27, s[16:19], s5 offen sc0 sc1\nWV 0\nv_xor_b32 v27, s15, v27\nv_and_b32 v32, 0xffffdfff, v27\nv_cmp_ne_u32 vcc, 0, v32\ns_nop 0\n"
"s_cbranch_vccz .S119\ns_sub_u32 s7, s7, 1\ns_cbranch_scc0 .S120\ns_branch .S119\n.S123:\ns_mov_b32 s7, 262144\n.S125:\ns_sleep 8\nv_and_b32 v27, 0x70, v1\n"
"buffer_load_dword v27, v27, s[16:19], s61 offen offset:128 sc0 sc1\nWV 0\nv_cmp_ne_u32 vcc, s15, v27\ns_nop 0\ns_cbranch_vccz .S124\ns_sub_u32 s7, s7, 1\ns_cbranch_scc0 .S125\n"
"s_branch .S124\n.T126:\ns_mov_b32 s62, 1\ns_call_b64 s[80:81], .RA10\ns_branch .T127\n.T128:\ns_mov_b32 s62, 1\ns_call_b64 s[80:81], .RA10\ns_branch .T129\n.T130:\n"
"s_mov_b32 s62, 0\ns_call_b64 s[80:81], .RB11\ns_branch .T131\n.T132:\ns_mov_b32 s62, 0\ns_call_b64 s[80:81], .RA10\ns_branch .T133\n.T134:\ns_mov_b32 s62, 0\n"
"s_call_b64 s[80:81], .RB11\ns_branch .T135\n.T136:\ns_mov_b32 s62, 0\ns_call_b64 s[80:81], .RA10\ns_branch .T137\n.S138:\ns_mov_b32 s7, 262144\n.S140:\ns_sleep 8\n"
"v_and_b32 v27, 0xf0, v1\nbuffer_load_dword v27, v27, s[16:19], s61 offen offset:12 sc0 sc1\nWV 0\nv_cmp_ne_u32 vcc, s15, v27\ns_nop 0\ns_cbranch_vccz .S139\ns_sub_u32 s7, s7, 1\n"
"s_cbranch_scc0 .S140\ns_branch .S139\n.S141:\ns_mov_b32 s7, 262144\n.S143:\ns_sleep 8\nv_and_b32 v27, 0x70, v1\nbuffer_load_dword v27, v27, s[16:19], s61 offen offset:136 sc0 sc1\n"
"WV 0\nv_cmp_ne_u32 vcc, s15, v27\ns_nop 0\ns_cbranch_vccz .S142\ns_sub_u32 s7, s7, 1\ns_cbranch_scc0 .S143\ns_branch .S142\n.E145:\ns_swappc_b64 s[80:81], s[8:9]\ns_branch .E146\n"
".T147:\ns_mov_b32 s62, 0\ns_call_b64 s[80:81], .RB11\ns_branch .T148\n.T149:\ns_mov_b32 s62, 0\ns_call_b64 s[80:81], .RA10\ns_branch .T150\n.E151:\nv_mov_b32 v12, s97\n"
"v_mov_b32 v13, s97\nv_mov_b32 v14, s97\nv_mov_b32 v15, s97\nv_cmpx_gt_i32 vcc, 3, v3\nv_mov_b32 v7, s97\nv_mov_b32 v19, s97\nv_cmpx_gt_i32 vcc, 2, v3\nv_mov_b32 v6, s97\n"
"v_mov_b32 v18, s97\nv_cmpx_gt_i32 vcc, 1, v3\nv_mov_b32 v5, s97\nv_mov_b32 v17, s97\nv_cmpx_gt_i32 vcc, 0, v3\nv_mov_b32 v4, s97\nv_mov_b32 v16, s97\nv_cmpx_gt_i32 vcc, -16, v3\n"
"v_mov_b32 v8, s97\nv_mov_b32 v9, s97\nv_mov_b32 v10, s97\nv_mov_b32 v11, s97\ns_mov_b64 exec, -1\ns_nop 2\ns_branch .E152\n.T153:\ns_mov_b32 s62, 0\ns_call_b64 s[80:81], .RB11\n"
"s_branch .T154\n.T155:\ns_mov_b32 s62, 0\ns_call_b64 s[80:81], .RA10\ns_branch .T156\n.E157:\nv_mov_b32 v12, s97\nv_mov_b32 v13, s97\nv_mov_b32 v14, s97\nv_mov_b32 v15, s97\n"
"v_cmpx_gt_i32 vcc, 3, v0\nv_mov_b32 v7, s97\nv_mov_b32 v19, s97\nv_cmpx_gt_i32 vcc, 2, v0\nv_mov_b32 v6, s97\nv_mov_b32 v18, s97\nv_cmpx_gt_i32 vcc, 1, v0\nv_mov_b32 v5, s97\n"
"v_mov_b32 v17, s97\nv_cmpx_gt_i32 vcc, 0, v0\nv_mov_b32 v4, s97\nv_mov_b32 v16, s97\nv_cmpx_gt_i32 vcc, -16, v0\nv_mov_b32 v8, s97\nv_mov_b32 v9, s97\nv_mov_b32 v10, s97\n"
"v_mov_b32 v11, s97\ns_mov_b64 exec, -1\ns_nop 2\ns_branch .E158\n.T159:\ns_mov_b32 s62, 0\ns_call_b64 s[80:81], .RB11\ns_branch .T160\n.T161:\ns_mov_b32 s62, 0\n"
"s_call_b64 s[80:81], .RA10\ns_branch .T162\n.E164:\nbuffer_load_dwordx4 v[100:103], v108, s[16:19], s65 offen offset:0 sc0 sc1\n"
"buffer_load_dwordx4 v[104:107], v108, s[16:19], s65 offen offset:256 sc0 sc1\ns_branch .E165\n.S170:\ns_mov_b32 s7, 262144\n.S172:\ns_sleep 8\n"
"buffer_load_dwordx4 v[100:103], v108, s[16:19], s65 offen offset:0 sc0 sc1\nbuffer_load_dwordx4 v[104:107], v108, s[16:19], s65 offen offset:256 sc0 sc1\nWV 0\n"
"v_xor_b32 v109, s15, v102\nv_xor_b32 v110, s15, v106\nv_or_b32 v109, v109, v110\nv_cmp_ne_u32 vcc, 0, v109\ns_nop 0\ns_cbranch_vccz .S171\ns_sub_u32 s7, s7, 1\n"
"s_cbranch_scc0 .S172\ns_branch .S171\n.D166:\nWV 0\nv_xor_b32 v109, s15, v102\nv_xor_b32 v110, s15, v106\nv_or_b32 v109, v109, v110\nv_cmp_ne_u32 vcc, 0, v109\ns_nop 0\n"
"s_cbranch_vccnz .S170\n.S171:\ns_add_u32 s5, s66, 0\nbuffer_load_dwordx4 a[192:195], v1, s[16:19], s5 offen offset:0 sc1\n"
"buffer_load_dwordx4 a[196:199], v1, s[16:19], s5 offen offset:1024 sc1\nbuffer_load_dwordx4 a[200:203], v1, s[16:19], s5 offen offset:2048 sc1\n"
"buffer_load_dwordx4 a[204:207], v1, s[16:19], s5 offen offset:3072 sc1\ns_add_u32 s5, s66, 4096\nbuffer_load_dwordx4 a[208:211], v1, s[16:19], s5 offen offset:0 sc1\n"
"buffer_load_dwordx4 a[212:215], v1, s[16:19], s5 offen offset:1024 sc1\nbuffer_load_dwordx4 a[216:219], v1, s[16:19], s5 offen offset:2048 sc1\n"
"buffer_load_dwordx4 a[220:223], v1, s[16:19], s5 offen offset:3072 sc1\ns_add_u32 s5, s66, 16384\nbuffer_load_dwordx4 a[224:227], v1, s[16:19], s5 offen offset:0 sc1\n"
"buffer_load_dwordx4 a[228:231], v1, s[16:19], s5 offen offset:1024 sc1\nbuffer_load_dwordx4 a[232:235], v1, s[16:19], s5 offen offset:2048 sc1\n"
"buffer_load_dwordx4 a[236:239], v1, s[16:19], s5 offen offset:3072 sc1\ns_add_u32 s5, s66, 20480\nbuffer_load_dwordx4 a[240:243], v1, s[16:19], s5 offen offset:0 sc1\n"
"buffer_load_dwordx4 a[244:247], v1, s[16:19], s5 offen offset:1024 sc1\nbuffer_load_dwordx4 a[248:251], v1, s[16:19], s5 offen offset:2048 sc1\n"
"buffer_load_dwordx4 a[252:255], v1, s[16:19], s5 offen offset:3072 sc1\nWV 0\ns_mov_b32 s67, 0\ns_branch .D167\n.D168:\nv_mov_b32 v130, 0\nv_mov_b32 v131, 0\n"
"s_mov_b32 s63, 0xff800000\ns_mov_b32 s67, 1\ns_branch .D169\n.T173:\ns_lshl3_add_u32 s62, s67, 2\ns_call_b64 s[80:81], .RB11\ns_cmp_eq_u32 s67, 0\ns_cbranch_scc1 .T174\n"
"SU 20,100,88\nv_min_f32 v20, 0, v20\nEX 20,20\ns_nop 0\nFM 86,101,20\nSU 21,104,89\nv_min_f32 v21, 0, v21\nEX 21,21\ns_nop 0\nFM 87,105,21\nAR 22,192\n"
"v_and_b32 v23, 0xffff0000, v22\nv_lshlrev_b32 v22, 16, v22\nMU 22,22,20\nMU 23,23,20\nCV 22,22,23\nAW 192,22\nAR 22,193\nv_and_b32 v23, 0xffff0000, v22\n"
"v_lshlrev_b32 v22, 16, v22\nMU 22,22,20\nMU 23,23,20\nCV 22,22,23\nAW 193,22\nAR 22,194\nv_and_b32 v23, 0xffff0000, v22\nv_lshlrev_b32 v22, 16, v22\nMU 22,22,20\nMU 23,23,20\n"
"CV 22,22,23\nAW 194,22\nAR 22,195\nv_and_b32 v23, 0xffff0000, v22\nv_lshlrev_b32 v22, 16, v22\nMU 22,22,20\nMU 23,23,20\nCV 22,22,23\nAW 195,22\nAR 22,196\n"
"v_and_b32 v23, 0xffff0000, v22\nv_lshlrev_b32 v22, 16, v22\nMU 22,22,21\nMU 23,23,21\nCV 22,22,23\nAW 196,22\nAR 22,197\nv_and_b32 v23, 0xffff0000, v22\n"
"v_lshlrev_b32 v22, 16, v22\nMU 22,22,21\nMU 23,23,21\nCV 22,22,23\nAW 197,22\nAR 22,198\nv_and_b32 v23, 0xffff0000, v22\nv_lshlrev_b32 v22, 16, v22\nMU 22,22,21\nMU 23,23,21\n"
"CV 22,22,23\nAW 198,22\nAR 22,199\nv_and_b32 v23, 0xffff0000, v22\nv_lshlrev_b32 v22, 16, v22\nMU 22,22,21\nMU 23,23,21\nCV 22,22,23\nAW 199,22\nAR 22,200\n"
"v_and_b32 v23, 0xffff0000, v22\nv_lshlrev_b32 v22, 16, v22\nMU 22,22,20\nMU 23,23,20\nCV 22,22,23\nAW 200,22\nAR 22,201\nv_and_b32 v23, 0xffff0000, v22\n"
"v_lshlrev_b32 v22, 16, v22\nMU 22,22,20\nMU 23,23,20\nCV 22,22,23\nAW 201,22\nAR 22,202\nv_and_b32 v23, 0xffff0000, v22\nv_lshlrev_b32 v22, 16, v22\nMU 22,22,20\nMU 23,23,20\n"
"CV 22,22,23\nAW 202,22\nAR 22,203\nv_and_b32 v23, 0xffff0000, v22\nv_lshlrev_b32 v22, 16, v22\nMU 22,22,20\nMU 23,23,20\nCV 22,22,23\nAW 203,22\nAR 22,204\n"
"v_and_b32 v23, 0xffff0000, v22\nv_lshlrev_b32 v22, 16, v22\nMU 22,22,21\nMU 23,23,21\nCV 22,22,23\nAW 204,22\nAR 22,205\nv_and_b32 v23, 0xffff0000, v22\n"
"v_lshlrev_b32 v22, 16, v22\nMU 22,22,21\nMU 23,23,21\nCV 22,22,23\nAW 205,22\nAR 22,206\nv_and_b32 v23, 0xffff0000, v22\nv_lshlrev_b32 v22, 16, v22\nMU 22,22,21\nMU 23,23,21\n"
"CV 22,22,23\nAW 206,22\nAR 22,207\nv_and_b32 v23, 0xffff0000, v22\nv_lshlrev_b32 v22, 16, v22\nMU 22,22,21\nMU 23,23,21\nCV 22,22,23\nAW 207,22\nAR 22,208\n"
"v_and_b32 v23, 0xffff0000, v22\nv_lshlrev_b32 v22, 16, v22\nMU 22,22,20\nMU 23,23,20\nCV 22,22,23\nAW 208,22\nAR 22,209\nv_and_b32 v23, 0xffff0000, v22\n"
"v_lshlrev_b32 v22, 16, v22\nMU 22,22,20\nMU 23,23,20\nCV 22,22,23\nAW 209,22\nAR 22,210\nv_and_b32 v23, 0xffff0000, v22\nv_lshlrev_b32 v22, 16, v22\nMU 22,22,20\nMU 23,23,20\n"
"CV 22,22,23\nAW 210,22\nAR 22,211\nv_and_b32 v23, 0xffff0000, v22\nv_lshlrev_b32 v22, 16, v22\nMU 22,22,20\nMU 23,23,20\nCV 22,22,23\nAW 211,22\nAR 22,212\n"
"v_and_b32 v23, 0xffff0000, v22\nv_lshlrev_b32 v22, 16, v22\nMU 22,22,21\nMU 23,23,21\nCV 22,22,23\nAW 212,22\nAR 22,213\nv_and_b32 v23, 0xffff0000, v22\n"
"v_lshlrev_b32 v22, 16, v22\nMU 22,22,21\nMU 23,23,21\nCV 22,22,23\nAW 213,22\nAR 22,214\nv_and_b32 v23, 0xffff0000, v22\nv_lshlrev_b32 v22, 16, v22\nMU 22,22,21\nMU 23,23,21\n"
"CV 22,22,23\nAW 214,22\nAR 22,215\nv_and_b32 v23, 0xffff0000, v22\nv_lshlrev_b32 v22, 16, v22\nMU 22,22,21\nMU 23,23,21\nCV 22,22,23\nAW 215,22\nAR 22,216\n"
"v_and_b32 v23, 0xffff0000, v22\nv_lshlrev_b32 v22, 16, v22\nMU 22,22,20\nMU 23,23,20\nCV 22,22,23\nAW 216,22\nAR 22,217\nv_and_b32 v23, 0xffff0000, v22\n"
"v_lshlrev_b32 v22, 16, v22\nMU 22,22,20\nMU 23,23,20\nCV 22,22,23\nAW 217,22\nAR 22,218\nv_and_b32 v23, 0xffff0000, v22\nv_lshlrev_b32 v22, 16, v22\nMU 22,22,20\nMU 23,23,20\n"
"CV 22,22,23\nAW 218,22\nAR 22,219\nv_and_b32 v23, 0xffff0000, v22\nv_lshlrev_b32 v22, 16, v22\nMU 22,22,20\nMU 23,23,20\nCV 22,22,23\nAW 219,22\nAR 22,220\n"
"v_and_b32 v23, 0xffff0000, v22\nv_lshlrev_b32 v22, 16, v22\nMU 22,22,21\nMU 23,23,21\nCV 22,22,23\nAW 220,22\nAR 22,221\nv_and_b32 v23, 0xffff0000, v22\n"
"v_lshlrev_b32 v22, 16, v22\nMU 22,22,21\nMU 23,23,21\nCV 22,22,23\nAW 221,22\nAR 22,222\nv_and_b32 v23, 0xffff0000, v22\nv_lshlrev_b32 v22, 16, v22\nMU 22,22,21\nMU 23,23,21\n"
"CV 22,22,23\nAW 222,22\nAR 22,223\nv_and_b32 v23, 0xffff0000, v22\nv_lshlrev_b32 v22, 16, v22\nMU 22,22,21\nMU 23,23,21\nCV 22,22,23\nAW 223,22\nAR 22,224\n"
"v_and_b32 v23, 0xffff0000, v22\nv_lshlrev_b32 v22, 16, v22\nMU 22,22,20\nMU 23,23,20\nCV 22,22,23\nAW 224,22\nAR 22,225\nv_and_b32 v23, 0xffff0000, v22\n"
"v_lshlrev_b32 v22, 16, v22\nMU 22,22,20\nMU 23,23,20\nCV 22,22,23\nAW 225,22\nAR 22,226\nv_and_b32 v23, 0xffff0000, v22\nv_lshlrev_b32 v22, 16, v22\nMU 22,22,20\nMU 23,23,20\n"
"CV 22,22,23\nAW 226,22\nAR 22,227\nv_and_b32 v23, 0xffff0000, v22\nv_lshlrev_b32 v22, 16, v22\nMU 22,22,20\nMU 23,23,20\nCV 22,22,23\nAW 227,22\nAR 22,228\n"
"v_and_b32 v23, 0xffff0000, v22\nv_lshlrev_b32 v22, 16, v22\nMU 22,22,21\nMU 23,23,21\nCV 22,22,23\nAW 228,22\nAR 22,229\nv_and_b32 v23, 0xffff0000, v22\n"
"v_lshlrev_b32 v22, 16, v22\nMU 22,22,21\nMU 23,23,21\nCV 22,22,23\nAW 229,22\nAR 22,230\nv_and_b32 v23, 0xffff0000, v22\nv_lshlrev_b32 v22, 16, v22\nMU 22,22,21\nMU 23,23,21\n"
"CV 22,22,23\nAW 230,22\nAR 22,231\nv_and_b32 v23, 0xffff0000, v22\nv_lshlrev_b32 v22, 16, v22\nMU 22,22,21\nMU 23,23,21\nCV 22,22,23\nAW 231,22\nAR 22,232\n"
"v_and_b32 v23, 0xffff0000, v22\nv_lshlrev_b32 v22, 16, v22\nMU 22,22,20\nMU 23,23,20\nCV 22,22,23\nAW 232,22\nAR 22,233\nv_and_b32 v23, 0xffff0000, v22\n"
"v_lshlrev_b32 v22, 16, v22\nMU 22,22,20\nMU 23,23,20\nCV 22,22,23\nAW 233,22\nAR 22,234\nv_and_b32 v23, 0xffff0000, v22\nv_lshlrev_b32 v22, 16, v22\nMU 22,22,20\nMU 23,23,20\n"
"CV 22,22,23\nAW 234,22\nAR 22,235\nv_and_b32 v23, 0xffff0000, v22\nv_lshlrev_b32 v22, 16, v22\nMU 22,22,20\nMU 23,23,20\nCV 22,22,23\nAW 235,22\nAR 22,236\n"
"v_and_b32 v23, 0xffff0000, v22\nv_lshlrev_b32 v22, 16, v22\nMU 22,22,21\nMU 23,23,21\nCV 22,22,23\nAW 236,22\nAR 22,237\nv_and_b32 v23, 0xffff0000, v22\n"
"v_lshlrev_b32 v22, 16, v22\nMU 22,22,21\nMU 23,23,21\nCV 22,22,23\nAW 237,22\nAR 22,238\nv_and_b32 v23, 0xffff0000, v22\nv_lshlrev_b32 v22, 16, v22\nMU 22,22,21\nMU 23,23,21\n"
"CV 22,22,23\nAW 238,22\nAR 22,239\nv_and_b32 v23, 0xffff0000, v22\nv_lshlrev_b32 v22, 16, v22\nMU 22,22,21\nMU 23,23,21\nCV 22,22,23\nAW 239,22\nAR 22,240\n"
"v_and_b32 v23, 0xffff0000, v22\nv_lshlrev_b32 v22, 16, v22\nMU 22,22,20\nMU 23,23,20\nCV 22,22,23\nAW 240,22\nAR 22,241\nv_and_b32 v23, 0xffff0000, v22\n"
"v_lshlrev_b32 v22, 16, v22\nMU 22,22,20\nMU 23,23,20\nCV 22,22,23\nAW 241,22\nAR 22,242\nv_and_b32 v23, 0xffff0000, v22\nv_lshlrev_b32 v22, 16, v22\nMU 22,22,20\nMU 23,23,20\n"
"CV 22,22,23\nAW 242,22\nAR 22,243\nv_and_b32 v23, 0xffff0000, v22\nv_lshlrev_b32 v22, 16, v22\nMU 22,22,20\nMU 23,23,20\nCV 22,22,23\nAW 243,22\nAR 22,244\n"
"v_and_b32 v23, 0xffff0000, v22\nv_lshlrev_b32 v22, 16, v22\nMU 22,22,21\nMU 23,23,21\nCV 22,22,23\nAW 244,22\nAR 22,245\nv_and_b32 v23, 0xffff0000, v22\n"
"v_lshlrev_b32 v22, 16, v22\nMU 22,22,21\nMU 23,23,21\nCV 22,22,23\nAW 245,22\nAR 22,246\nv_and_b32 v23, 0xffff0000, v22\nv_lshlrev_b32 v22, 16, v22\nMU 22,22,21\nMU 23,23,21\n"
"CV 22,22,23\nAW 246,22\nAR 22,247\nv_and_b32 v23, 0xffff0000, v22\nv_lshlrev_b32 v22, 16, v22\nMU 22,22,21\nMU 23,23,21\nCV 22,22,23\nAW 247,22\nAR 22,248\n"
"v_and_b32 v23, 0xffff0000, v22\nv_lshlrev_b32 v22, 16, v22\nMU 22,22,20\nMU 23,23,20\nCV 22,22,23\nAW 248,22\nAR 22,249\nv_and_b32 v23, 0xffff0000, v22\n"
"v_lshlrev_b32 v22, 16, v22\nMU 22,22,20\nMU 23,23,20\nCV 22,22,23\nAW 249,22\nAR 22,250\nv_and_b32 v23, 0xffff0000, v22\nv_lshlrev_b32 v22, 16, v22\nMU 22,22,20\nMU 23,23,20\n"
"CV 22,22,23\nAW 250,22\nAR 22,251\nv_and_b32 v23, 0xffff0000, v22\nv_lshlrev_b32 v22, 16, v22\nMU 22,22,20\nMU 23,23,20\nCV 22,22,23\nAW 251,22\nAR 22,252\n"
"v_and_b32 v23, 0xffff0000, v22\nv_lshlrev_b32 v22, 16, v22\nMU 22,22,21\nMU 23,23,21\nCV 22,22,23\nAW 252,22\nAR 22,253\nv_and_b32 v23, 0xffff0000, v22\n"
"v_lshlrev_b32 v22, 16, v22\nMU 22,22,21\nMU 23,23,21\nCV 22,22,23\nAW 253,22\nAR 22,254\nv_and_b32 v23, 0xffff0000, v22\nv_lshlrev_b32 v22, 16, v22\nMU 22,22,21\nMU 23,23,21\n"
"CV 22,22,23\nAW 254,22\nAR 22,255\nv_and_b32 v23, 0xffff0000, v22\nv_lshlrev_b32 v22, 16, v22\nMU 22,22,21\nMU 23,23,21\nCV 22,22,23\nAW 255,22\nv_lshrrev_b32 v22, 8, v1\n"
"v_bfe_u32 v23, v1, 6, 2\nv_cmp_eq_u32 vcc, v22, v23\nv_and_b32 v22, 16, v1\nv_mov_b32 v23, 0x3f80\nv_cndmask_b32 v23, 0, v23, vcc\nv_lshlrev_b32 v23, v22, v23\n"
"v_and_b32 v22, 32, v1\nv_cmp_eq_u32 vcc, 0, v22\nv_mov_b32 v128, 0\nv_mov_b32 v129, 0\nv_cndmask_b32 v130, 0, v23, vcc\nv_sub_u32 v131, v23, v130\nv_mov_b32 v132, 0\n"
"v_mov_b32 v133, 0\ns_nop 1\nMava 0,130,192\nMava 8,128,192\nMava 12,128,196\nMava 4,130,196\nMava 16,130,200\nMava 24,128,200\nMava 28,128,204\nMava 20,130,204\nMava 32,130,208\n"
"Mava 40,128,208\nMava 44,128,212\nMava 36,130,212\nMava 48,130,216\nMava 56,128,216\nMava 60,128,220\nMava 52,130,220\nMava 64,130,224\nMava 72,128,224\nMava 76,128,228\n"
"Mava 68,130,228\nMava 80,130,232\nMava 88,128,232\nMava 92,128,236\nMava 84,130,236\nMava 96,130,240\nMava 104,128,240\nMava 108,128,244\nMava 100,130,244\nMava 112,130,248\n"
"Mava 120,128,248\nMava 124,128,252\nMava 116,130,252\ns_nop 7\ns_branch .T174\n.S176:\ns_mov_b32 s7, 262144\n.S178:\ns_sleep 8\ns_add_u32 s4, s92, 0\n"
"buffer_load_dwordx4 a[192:195], v1, s[16:19], s4 offen offset:0 sc0 sc1\nbuffer_load_dwordx4 a[196:199], v1, s[16:19], s4 offen offset:1024 sc0 sc1\ns_add_u32 s4, s92, 131072\n"
"buffer_load_dwordx4 a[200:203], v1, s[16:19], s4 offen offset:0 sc0 sc1\nbuffer_load_dwordx4 a[204:207], v1, s[16:19], s4 offen offset:1024 sc0 sc1\ns_add_u32 s4, s92, 262144\n"
"buffer_load_dwordx4 a[208:211], v1, s[16:19], s4 offen offset:0 sc0 sc1\nbuffer_load_dwordx4 a[212:215], v1, s[16:19], s4 offen offset:1024 sc0 sc1\ns_add_u32 s4, s92, 393216\n"
"buffer_load_dwordx4 a[216:219], v1, s[16:19], s4 offen offset:0 sc0 sc1\nbuffer_load_dwordx4 a[220:223], v1, s[16:19], s4 offen offset:1024 sc0 sc1\nWV 0\nAR 22,195\nAR 23,199\n"
"v_max_u32 v21, v22, v23\nAR 22,203\nAR 23,207\nv_max3_u32 v21, v21, v22, v23\nAR 22,211\nAR 23,215\nv_max3_u32 v21, v21, v22, v23\nAR 22,219\nAR 23,223\n"
"v_max3_u32 v21, v21, v22, v23\nv_cmp_eq_u32 vcc, -1, v21\ns_nop 0\ns_cbranch_vccz .S177\ns_sub_u32 s7, s7, 1\ns_cbranch_scc0 .S178\ns_branch .S177\n.S179:\ns_mov_b32 s7, 262144\n"
".S181:\ns_sleep 8\ns_add_u32 s4, s92, 524288\nbuffer_load_dwordx4 a[224:227], v1, s[16:19], s4 offen offset:0 sc0 sc1\n"
"buffer_load_dwordx4 a[228:231], v1, s[16:19], s4 offen offset:1024 sc0 sc1\ns_add_u32 s4, s92, 655360\nbuffer_load_dwordx4 a[232:235], v1, s[16:19], s4 offen offset:0 sc0 sc1\n"
"buffer_load_dwordx4 a[236:239], v1, s[16:19], s4 offen offset:1024 sc0 sc1\ns_add_u32 s4, s92, 786432\nbuffer_load_dwordx4 a[240:243], v1, s[16:19], s4 offen offset:0 sc0 sc1\n"
"buffer_load_dwordx4 a[244:247], v1, s[16:19], s4 offen offset:1024 sc0 sc1\ns_add_u32 s4, s92, 917504\nbuffer_load_dwordx4 a[248:251], v1, s[16:19], s4 offen offset:0 sc0 sc1\n"
"buffer_load_dwordx4 a[252:255], v1, s[16:19], s4 offen offset:1024 sc0 sc1\nWV 0\nAR 22,227\nAR 23,231\nv_max_u32 v21, v22, v23\nAR 22,235\nAR 23,239\n"
"v_max3_u32 v21, v21, v22, v23\nAR 22,243\nAR 23,247\nv_max3_u32 v21, v21, v22, v23\nAR 22,251\nAR 23,255\nv_max3_u32 v21, v21, v22, v23\nv_cmp_eq_u32 vcc, -1, v21\ns_nop 0\n"
"s_cbranch_vccz .S180\ns_sub_u32 s7, s7, 1\ns_cbranch_scc0 .S181\ns_branch .S180\n.S182:\ns_mov_b32 s7, 262144\n.S184:\ns_sleep 8\n"
"buffer_load_dwordx4 v[64:67], v89, s[16:19], s40 offen offset:0 sc0 sc1\nbuffer_load_dwordx4 v[68:71], v89, s[16:19], s40 offen offset:16 sc0 sc1\n"
"buffer_load_dwordx4 v[72:75], v89, s[16:19], s40 offen offset:1792 sc0 sc1\nbuffer_load_dwordx4 v[76:79], v89, s[16:19], s40 offen offset:1808 sc0 sc1\nWV 0\n"
"v_xor_b32 v92, s15, v66\nv_xor_b32 v93, s15, v70\nv_xor_b32 v94, s15, v74\nv_or3_b32 v92, v92, v93, v94\nv_xor_b32 v93, s15, v78\nv_or_b32 v92, v92, v93\nv_cmp_ne_u32 vcc, 0, v92\n"
"s_nop 0\ns_cbranch_vccz .S183\ns_sub_u32 s7, s7, 1\ns_cbranch_scc0 .S184\ns_branch .S183\n.S185:\ns_mov_b32 s7, 262144\n.S187:\ns_sleep 8\n"
"buffer_load_dword v27, off, s[16:19], s60 offset:512 sc0 sc1\nWV 0\nv_cmp_ne_u32 vcc, s15, v27\ns_nop 0\ns_cbranch_vccz .S186\ns_sub_u32 s7, s7, 1\ns_cbranch_scc0 .S187\n"
"s_branch .S186\n.S188:\ns_mov_b32 s7, 262144\n.S190:\ns_sleep 8\nbuffer_load_dword v27, v24, s[16:19], s41 offen sc0 sc1\nWV 0\nv_cmp_eq_u32 s[62:63], s15, v27\n"
"s_mov_b32 vcc_hi, 0\ns_nop 2\ns_lshr_b32 s63, s62, 8\ns_or_b32 s62, s62, s63\ns_andn2_b32 vcc_lo, 0xff, s62\ns_mov_b64 vcc, vcc\ns_cbranch_vccz .S189\ns_sub_u32 s7, s7, 1\n"
"s_cbranch_scc0 .S190\ns_branch .S189\n.S202:\ns_mov_b32 s7, 262144\n.S204:\ns_sleep 8\nv_and_b32 v27, 0x1f0, v1\nbuffer_load_dword v27, v27, s[16:19], s58 offen offset:4 sc0 sc1\n"
"WV 0\nv_cmp_ne_u32 vcc, s15, v27\ns_nop 0\ns_cbranch_vccz .S203\ns_sub_u32 s7, s7, 1\ns_cbranch_scc0 .S204\ns_branch .S203\n.p2align 6\n.PA4:\nWV 24\ns_barrier\nTa 128,12,0\n"
"Ta 130,12,2048\nTa 132,13,0\nTa 134,13,2048\nTa 136,14,0\nTa 138,14,2048\nTa 140,15,0\nTa 142,15,2048\nTa 144,16,0\nTa 146,16,2048\nTa 148,17,0\nTa 150,17,2048\nTa 152,18,0\n"
"Ta 154,18,2048\nTa 156,19,0\nTa 158,19,2048\nRa 160,10,0\nRa 164,10,2048\nRa 168,10,8192\nRa 172,10,10240\nRa 176,10,16384\nRa 180,10,18432\nRa 184,10,24576\nRa 188,10,26624\n"
"s_waitcnt vmcnt(20) lgkmcnt(0)\ns_barrier\nZvaa 64,128,160\nTa 192,12,16384\nZvaa 96,128,164\nZvaa 128,128,168\nTa 194,12,18432\nZvaa 160,128,172\ns_add_u32 m0, s47, 49152\n"
"s_add_u32 s44, s44, 0x40000\nZaaa 0,128,176\nTa 196,13,16384\nZaaa 32,128,180\nD0 6,24,44,0\nZaaa 64,128,184\nTa 198,13,18432\nZaaa 96,128,188\nZaaa 100,132,188\nTa 200,14,16384\n"
"Zaaa 68,132,184\nZaaa 36,132,180\nTa 202,14,18432\nZaaa 4,132,176\nZvaa 164,132,172\nTa 204,15,16384\nZvaa 132,132,168\nD0 7,24,44,1024\nZvaa 100,132,164\nTa 206,15,18432\n"
"Zvaa 68,132,160\nZvaa 72,136,160\nTa 208,16,16384\nZvaa 104,136,164\nZvaa 136,136,168\nTa 210,16,18432\nZvaa 168,136,172\nZaaa 8,136,176\nTa 212,17,16384\nZaaa 40,136,180\n"
"D0 8,24,44,2048\nZaaa 72,136,184\nTa 214,17,18432\nZaaa 104,136,188\nZaaa 108,140,188\nTa 216,18,16384\nZaaa 76,140,184\nZaaa 44,140,180\nTa 218,18,18432\nZaaa 12,140,176\n"
"Zvaa 172,140,172\nTa 220,19,16384\nZvaa 140,140,168\nD0 9,24,44,3072\nZvaa 108,140,164\nTa 222,19,18432\nZvaa 76,140,160\nbuffer_load_dword v31, v24, s[24:27], s96 offen\n"
"s_add_u32 s96, s96, 0x80000\nZvaa 80,144,160\nRa 224,11,0\nZvaa 112,144,164\nZvaa 144,144,168\nRa 228,11,2048\nZvaa 176,144,172\ns_add_u32 m0, s47, 0\ns_add_u32 s44, s44, 0x40000\n"
"Zaaa 16,144,176\nRa 232,11,8192\nZaaa 48,144,180\nD0 6,24,44,0\nZaaa 80,144,184\nRa 236,11,10240\nZaaa 112,144,188\nZaaa 116,148,188\nRa 240,11,16384\nZaaa 84,148,184\n"
"Zaaa 52,148,180\nRa 244,11,18432\nZaaa 20,148,176\nZvaa 180,148,172\nRa 248,11,24576\nZvaa 148,148,168\nD0 7,24,44,1024\nZvaa 116,148,164\nRa 252,11,26624\nZvaa 84,148,160\n"
"Zvaa 88,152,160\nZvaa 120,152,164\nZvaa 152,152,168\nZvaa 184,152,172\nZaaa 24,152,176\nZaaa 56,152,180\nD0 8,24,44,2048\nZaaa 88,152,184\nZaaa 120,152,188\nZaaa 124,156,188\n"
"Zaaa 92,156,184\nZaaa 60,156,180\nZaaa 28,156,176\nZvaa 188,156,172\nZvaa 156,156,168\nD0 9,24,44,3072\nZvaa 124,156,164\nZvaa 92,156,160\ns_waitcnt vmcnt(13) lgkmcnt(0)\n"
"s_barrier\nMvaa 64,192,224\nTa 128,12,32768\nMvaa 96,192,228\nv_lshlrev_b32 v48, 16, v32\nMvaa 128,192,232\nTa 130,12,34816\nMvaa 160,192,236\ns_add_u32 m0, s48, 0\n"
"s_add_u32 s45, s45, 128\nMaaa 0,192,240\nTa 132,13,32768\nv_lshlrev_b32 v49, 16, v36\nMaaa 32,192,244\nD0 2,20,45,0\nMaaa 64,192,248\nTa 134,13,34816\nMaaa 96,192,252\n"
"v_lshlrev_b32 v50, 16, v40\nv_lshlrev_b32 v51, 16, v44\nMaaa 100,196,252\nTa 136,14,32768\nMaaa 68,196,248\nMU 52,48,50\nMaaa 36,196,244\nTa 138,14,34816\nMU 53,49,50\n"
"Maaa 4,196,240\nCV 52,52,53\nMvaa 164,196,236\nTa 140,15,32768\nMvaa 132,196,232\nD0 3,20,45,1024\nv_lshlrev_b32 v54, 16, v52\nMvaa 100,196,228\nTa 142,15,34816\nMvaa 68,196,224\n"
"v_and_b32 v55, 0xffff0000, v52\nMvaa 72,200,224\nTa 144,16,32768\nMU 56,48,51\nMvaa 104,200,228\nMU 57,49,51\nMvaa 136,200,232\nTa 146,16,34816\nMvaa 168,200,236\n"
"v_and_b32 v48, 0xffff0000, v32\nMaaa 8,200,240\nTa 148,17,32768\nv_and_b32 v49, 0xffff0000, v36\nMaaa 40,200,244\nD0 4,20,45,2048\nMaaa 72,200,248\nTa 150,17,34816\n"
"Maaa 104,200,252\nv_and_b32 v50, 0xffff0000, v40\nv_and_b32 v51, 0xffff0000, v44\nMaaa 108,204,252\nTa 152,18,32768\nMaaa 76,204,248\nMU 52,48,50\nMaaa 44,204,244\n"
"Ta 154,18,34816\nMU 53,49,50\nMaaa 12,204,240\nCV 52,52,53\nMvaa 172,204,236\nTa 156,19,32768\nMvaa 140,204,232\nD0 5,20,45,3072\nv_and_b32 v53, 0xffff0000, v52\nMvaa 108,204,228\n"
"Ta 158,19,34816\nMvaa 76,204,224\nv_lshlrev_b32 v52, 16, v52\nMvaa 80,208,224\nRa 160,10,32768\nFM 56,48,51\nMvaa 112,208,228\nFM 57,49,51\nMvaa 144,208,232\nRa 164,10,34816\n"
"Mvaa 176,208,236\ns_add_u32 m0, s48, 4096\ns_add_u32 s46, s45, 0x10000\nMaaa 16,208,240\nRa 168,10,40960\nAD 54,54,52\nMaaa 48,208,244\nD0 2,20,46,0\nMaaa 80,208,248\n"
"Ra 172,10,43008\nMaaa 112,208,252\nAD 55,55,53\nv_lshlrev_b32 v48, 16, v33\nMaaa 116,212,252\nRa 176,10,49152\nMaaa 84,212,248\nv_lshlrev_b32 v49, 16, v37\nMaaa 52,212,244\n"
"Ra 180,10,51200\nv_lshlrev_b32 v50, 16, v41\nMaaa 20,212,240\nv_lshlrev_b32 v51, 16, v45\nMvaa 180,212,236\nRa 184,10,57344\nMvaa 148,212,232\nD0 3,20,46,1024\nMU 52,48,50\n"
"Mvaa 116,212,228\nRa 188,10,59392\nMvaa 84,212,224\nMU 53,49,50\nMvaa 88,216,224\nCV 52,52,53\nv_and_b32 v53, 0xffff0000, v52\nMvaa 120,216,228\nv_lshlrev_b32 v52, 16, v52\n"
"Mvaa 152,216,232\nFM 56,48,51\nMvaa 184,216,236\nFM 57,49,51\nMaaa 24,216,240\nAD 54,54,52\nAD 55,55,53\nMaaa 56,216,244\nD0 4,20,46,2048\nMaaa 88,216,248\n"
"v_and_b32 v48, 0xffff0000, v33\nMaaa 120,216,252\nv_and_b32 v49, 0xffff0000, v37\nv_and_b32 v50, 0xffff0000, v41\nMaaa 124,220,252\nv_and_b32 v51, 0xffff0000, v45\n"
"Maaa 92,220,248\nMU 52,48,50\nMaaa 60,220,244\nMU 53,49,50\nCV 52,52,53\nMaaa 28,220,240\nv_and_b32 v53, 0xffff0000, v52\nMvaa 188,220,236\nv_lshlrev_b32 v52, 16, v52\n"
"Mvaa 156,220,232\nD0 5,20,46,3072\nFM 56,48,51\nMvaa 124,220,228\nFM 57,49,51\nMvaa 92,220,224\ns_waitcnt vmcnt(13) lgkmcnt(0)\ns_barrier\nMvaa 64,128,160\nTa 192,12,49152\n"
"AD 54,54,52\nMvaa 96,128,164\nAD 55,55,53\nMvaa 128,128,168\nTa 194,12,51200\nMvaa 160,128,172\ns_add_u32 m0, s47, 16384\ns_add_u32 s44, s44, 0x40000\nMaaa 0,128,176\n"
"Ta 196,13,49152\nv_lshlrev_b32 v48, 16, v34\nMaaa 32,128,180\nD0 6,24,44,0\nMaaa 64,128,184\nTa 198,13,51200\nMaaa 96,128,188\nv_lshlrev_b32 v49, 16, v38\n"
"v_lshlrev_b32 v50, 16, v42\nMaaa 100,132,188\nTa 200,14,49152\nMaaa 68,132,184\nv_lshlrev_b32 v51, 16, v46\nMaaa 36,132,180\nTa 202,14,51200\nMU 52,48,50\nMaaa 4,132,176\n"
"MU 53,49,50\nMvaa 164,132,172\nTa 204,15,49152\nMvaa 132,132,168\nD0 7,24,44,1024\nCV 52,52,53\nMvaa 100,132,164\nTa 206,15,51200\nMvaa 68,132,160\nv_and_b32 v53, 0xffff0000, v52\n"
"Mvaa 72,136,160\nTa 208,16,49152\nv_lshlrev_b32 v52, 16, v52\nMvaa 104,136,164\nFM 56,48,51\nMvaa 136,136,168\nTa 210,16,51200\nMvaa 168,136,172\nFM 57,49,51\nMaaa 8,136,176\n"
"Ta 212,17,49152\nAD 54,54,52\nMaaa 40,136,180\nD0 8,24,44,2048\nMaaa 72,136,184\nTa 214,17,51200\nMaaa 104,136,188\nAD 55,55,53\nv_and_b32 v48, 0xffff0000, v34\nMaaa 108,140,188\n"
"Ta 216,18,49152\nMaaa 76,140,184\nv_and_b32 v49, 0xffff0000, v38\nMaaa 44,140,180\nTa 218,18,51200\nv_and_b32 v50, 0xffff0000, v42\nMaaa 12,140,176\n"
"v_and_b32 v51, 0xffff0000, v46\nMvaa 172,140,172\nTa 220,19,49152\nMvaa 140,140,168\nD0 9,24,44,3072\nMU 52,48,50\nMvaa 108,140,164\nTa 222,19,51200\nMvaa 76,140,160\n"
"buffer_load_dword v31, v24, s[24:27], s96 offen\ns_add_u32 s96, s96, 0x80000\nMvaa 80,144,160\nRa 224,11,32768\nMU 53,49,50\nMvaa 112,144,164\nCV 52,52,53\nMvaa 144,144,168\n"
"Ra 228,11,34816\nMvaa 176,144,172\ns_add_u32 m0, s47, 32768\ns_add_u32 s44, s44, 0x40000\nMaaa 16,144,176\nRa 232,11,40960\nv_and_b32 v53, 0xffff0000, v52\nMaaa 48,144,180\n"
"D0 6,24,44,0\nMaaa 80,144,184\nRa 236,11,43008\nMaaa 112,144,188\nv_lshlrev_b32 v52, 16, v52\nFM 56,48,51\nMaaa 116,148,188\nRa 240,11,49152\nMaaa 84,148,184\nFM 57,49,51\n"
"Maaa 52,148,180\nRa 244,11,51200\nAD 54,54,52\nMaaa 20,148,176\nAD 55,55,53\nMvaa 180,148,172\nRa 248,11,57344\nMvaa 148,148,168\nD0 7,24,44,1024\nv_lshlrev_b32 v48, 16, v35\n"
"Mvaa 116,148,164\nRa 252,11,59392\nMvaa 84,148,160\nv_lshlrev_b32 v49, 16, v39\nMvaa 88,152,160\nv_lshlrev_b32 v50, 16, v43\nv_lshlrev_b32 v51, 16, v47\nMvaa 120,152,164\n"
"MU 52,48,50\nMvaa 152,152,168\nMU 53,49,50\nMvaa 184,152,172\nCV 52,52,53\nMaaa 24,152,176\nv_and_b32 v53, 0xffff0000, v52\nv_lshlrev_b32 v52, 16, v52\nMaaa 56,152,180\n"
"D0 8,24,44,2048\nMaaa 88,152,184\nFM 56,48,51\nMaaa 120,152,188\nFM 57,49,51\nAD 54,54,52\nMaaa 124,156,188\nAD 55,55,53\nMaaa 92,156,184\nv_and_b32 v48, 0xffff0000, v35\n"
"Maaa 60,156,180\nv_and_b32 v49, 0xffff0000, v39\nv_and_b32 v50, 0xffff0000, v43\nMaaa 28,156,176\nv_and_b32 v51, 0xffff0000, v47\nMvaa 188,156,172\nMU 52,48,50\nMvaa 156,156,168\n"
"D0 9,24,44,3072\nMU 53,49,50\nMvaa 124,156,164\nCV 52,52,53\nMvaa 92,156,160\ns_waitcnt vmcnt(9) lgkmcnt(0)\ns_barrier\nMvaa 64,192,224\nTa 128,12,0\n"
"v_and_b32 v53, 0xffff0000, v52\nMvaa 96,192,228\nv_lshlrev_b32 v52, 16, v52\nMvaa 128,192,232\nTa 130,12,2048\nMvaa 160,192,236\ns_add_u32 m0, s48, 32768\ns_add_u32 s45, s45, 128\n"
"Maaa 0,192,240\nTa 132,13,0\nFM 56,48,51\nMaaa 32,192,244\nD0 2,20,45,0\nMaaa 64,192,248\nTa 134,13,2048\nMaaa 96,192,252\nFM 57,49,51\nAD 54,54,52\nMaaa 100,196,252\nTa 136,14,0\n"
"Maaa 68,196,248\nAD 55,55,53\nMaaa 36,196,244\nTa 138,14,2048\nv_permlane32_swap_b32 v54, v55\nMaaa 4,196,240\nv_permlane32_swap_b32 v56, v57\nMvaa 164,196,236\nTa 140,15,0\n"
"Mvaa 132,196,232\nD0 3,20,45,1024\nAD 54,54,55\nMvaa 100,196,228\nTa 142,15,2048\nMvaa 68,196,224\nAD 56,56,57\nMvaa 72,200,224\nTa 144,16,0\nMvaa 104,200,228\n"
"v_permlane16_swap_b32 v54, v56\nMvaa 136,200,232\nTa 146,16,2048\nMvaa 168,200,236\nAD 54,54,56\nMaaa 8,200,240\nTa 148,17,0\nMaaa 40,200,244\nD0 4,20,45,2048\nMaaa 72,200,248\n"
"Ta 150,17,2048\nMaaa 104,200,252\nv_add_f32_dpp v54, v54, v54 row_ror:8 row_mask:0xf bank_mask:0xf\nMaaa 108,204,252\nTa 152,18,0\nMaaa 76,204,248\n"
"v_add_f32_dpp v54, v54, v54 row_ror:4 row_mask:0xf bank_mask:0xf\nMaaa 44,204,244\nTa 154,18,2048\nMaaa 12,204,240\n"
"v_add_f32_dpp v54, v54, v54 row_ror:2 row_mask:0xf bank_mask:0xf\nMvaa 172,204,236\nTa 156,19,0\nMvaa 140,204,232\nD0 5,20,45,3072\n"
"v_add_f32_dpp v54, v54, v54 row_ror:1 row_mask:0xf bank_mask:0xf\nMvaa 108,204,228\nTa 158,19,2048\nMvaa 76,204,224\nCV 54,54,54\nMvaa 80,208,224\nRa 160,10,0\n"
"v_lshlrev_b32 v54, 16, v54\nMvaa 112,208,228\nv_mul_f32 v55, 0xbfb8aa3b, v54\nEX 55,55\nMvaa 144,208,232\nRa 164,10,2048\nMvaa 176,208,236\ns_add_u32 m0, s48, 36864\n"
"s_add_u32 s46, s45, 0x10000\nMaaa 16,208,240\nRa 168,10,8192\nv_add_f32 v56, 0, v54\nMaaa 48,208,244\nD0 2,20,46,0\nMaaa 80,208,248\nRa 172,10,10240\nMaaa 112,208,252\n"
"v_add_f32 v55, 1.0, v55\nMaaa 116,212,252\nRa 176,10,16384\nv_rcp_f32 v55, v55\nMaaa 84,212,248\nv_ashrrev_i32 v57, 31, v56\nMaaa 52,212,244\nRa 180,10,18432\nCV 55,55,55\n"
"Maaa 20,212,240\nv_lshlrev_b32 v55, 16, v55\nMvaa 180,212,236\nRa 184,10,24576\nMvaa 148,212,232\nD0 3,20,46,1024\nv_or_b32 v57, 0x80000000, v57\nMvaa 116,212,228\n"
"Ra 188,10,26624\nMvaa 84,212,224\nv_xor_b32 v56, v56, v57\nMvaa 88,216,224\nv_and_b32 v56, 0xffff0000, v56\ns_lshl2_add_u32 s2, s10, s11\nMvaa 120,216,228\n"
"v_lshrrev_b32 v48, 5, v0\nMvaa 152,216,232\nv_lshl_add_u32 v48, s2, 1, v48\nMvaa 184,216,236\nv_sub_u32 v49, 0xffff, v48\nMaaa 24,216,240\nv_or_b32 v56, v56, v49\n"
"v_mov_b32 v57, s15\nMaaa 56,216,244\nD0 4,20,46,2048\nMaaa 88,216,248\nv_and_b32 v49, 31, v0\nMaaa 120,216,252\nv_mov_b32 v52, 0x40000000\nv_cmp_eq_u32 vcc, 16, v49\n"
"v_lshlrev_b32 v50, 3, v48\nv_lshlrev_b32 v51, 9, v49\nv_cndmask_b32 v50, v52, v50, vcc\nv_cndmask_b32 v55, v55, v54, vcc\nMaaa 124,220,252\nv_lshl_add_u32 v51, v48, 2, v51\n"
"Maaa 92,220,248\nv_and_b32 v49, 15, v0\nv_cmp_eq_u32 vcc, 0, v49\ns_mov_b32 s2, 0xc040000\ns_mov_b32 s3, 0xc044000\nv_cndmask_b32 v51, v52, v51, vcc\n"
"buffer_store_dwordx2 v[56:57], v50, s[16:19], s3 offen sc0 sc1\nbuffer_store_dword v55, v51, s[16:19], s2 offen sc0 sc1\nMaaa 60,220,244\nMaaa 28,220,240\nMvaa 188,220,236\n"
"Mvaa 156,220,232\nD0 5,20,46,3072\nMvaa 124,220,228\nMvaa 92,220,224\ns_waitcnt vmcnt(15) lgkmcnt(0)\ns_barrier\nMvaa 64,128,160\nTa 192,12,16384\nMvaa 96,128,164\n"
"Mvaa 128,128,168\nTa 194,12,18432\nMvaa 160,128,172\ns_add_u32 m0, s47, 49152\ns_add_u32 s44, s44, 0x40000\nMaaa 0,128,176\nTa 196,13,16384\nMaaa 32,128,180\nD0 6,24,44,0\n"
"Maaa 64,128,184\nTa 198,13,18432\nMaaa 96,128,188\nMaaa 100,132,188\nTa 200,14,16384\nMaaa 68,132,184\nMaaa 36,132,180\nTa 202,14,18432\nMaaa 4,132,176\nMvaa 164,132,172\n"
"Ta 204,15,16384\nMvaa 132,132,168\nD0 7,24,44,1024\nMvaa 100,132,164\nTa 206,15,18432\nMvaa 68,132,160\nMvaa 72,136,160\nTa 208,16,16384\nMvaa 104,136,164\nMvaa 136,136,168\n"
"Ta 210,16,18432\nMvaa 168,136,172\nMaaa 8,136,176\nTa 212,17,16384\nMaaa 40,136,180\nD0 8,24,44,2048\nMaaa 72,136,184\nTa 214,17,18432\nMaaa 104,136,188\nMaaa 108,140,188\n"
"Ta 216,18,16384\nMaaa 76,140,184\nMaaa 44,140,180\nTa 218,18,18432\nMaaa 12,140,176\nMvaa 172,140,172\nTa 220,19,16384\nMvaa 140,140,168\nD0 9,24,44,3072\nMvaa 108,140,164\n"
"Ta 222,19,18432\nMvaa 76,140,160\nbuffer_load_dword v31, v24, s[24:27], s96 offen\ns_add_u32 s96, s96, 0x80000\nMvaa 80,144,160\nRa 224,11,0\nMvaa 112,144,164\nMvaa 144,144,168\n"
"Ra 228,11,2048\nMvaa 176,144,172\ns_add_u32 m0, s47, 0\ns_add_u32 s44, s44, 0x40000\nMaaa 16,144,176\nRa 232,11,8192\nMaaa 48,144,180\nD0 6,24,44,0\nMaaa 80,144,184\n"
"Ra 236,11,10240\nMaaa 112,144,188\nMaaa 116,148,188\nRa 240,11,16384\nMaaa 84,148,184\nMaaa 52,148,180\nRa 244,11,18432\nMaaa 20,148,176\nMvaa 180,148,172\nRa 248,11,24576\n"
"Mvaa 148,148,168\nD0 7,24,44,1024\nMvaa 116,148,164\nRa 252,11,26624\nMvaa 84,148,160\nMvaa 88,152,160\nMvaa 120,152,164\nMvaa 152,152,168\nMvaa 184,152,172\nMaaa 24,152,176\n"
"Maaa 56,152,180\nD0 8,24,44,2048\nMaaa 88,152,184\nMaaa 120,152,188\nMaaa 124,156,188\nMaaa 92,156,184\nMaaa 60,156,180\nMaaa 28,156,176\nMvaa 188,156,172\nMvaa 156,156,168\n"
"D0 9,24,44,3072\nMvaa 124,156,164\nMvaa 92,156,160\ns_waitcnt vmcnt(9) lgkmcnt(0)\ns_barrier\nMvaa 64,192,224\nTa 128,12,32768\nMvaa 96,192,228\nMvaa 128,192,232\nTa 130,12,34816\n"
"Mvaa 160,192,236\ns_add_u32 m0, s48, 0\ns_add_u32 s45, s45, 128\nMaaa 0,192,240\nTa 132,13,32768\nMaaa 32,192,244\nD0 2,20,45,0\nMaaa 64,192,248\nTa 134,13,34816\nMaaa 96,192,252\n"
"Maaa 100,196,252\nTa 136,14,32768\nMaaa 68,196,248\nMaaa 36,196,244\nTa 138,14,34816\nMaaa 4,196,240\nMvaa 164,196,236\nTa 140,15,32768\nMvaa 132,196,232\nD0 3,20,45,1024\n"
"Mvaa 100,196,228\nTa 142,15,34816\nMvaa 68,196,224\nMvaa 72,200,224\nTa 144,16,32768\nMvaa 104,200,228\nMvaa 136,200,232\nTa 146,16,34816\nMvaa 168,200,236\nMaaa 8,200,240\n"
"Ta 148,17,32768\nMaaa 40,200,244\nD0 4,20,45,2048\nMaaa 72,200,248\nTa 150,17,34816\nMaaa 104,200,252\nMaaa 108,204,252\nTa 152,18,32768\nMaaa 76,204,248\nMaaa 44,204,244\n"
"Ta 154,18,34816\nMaaa 12,204,240\nMvaa 172,204,236\nTa 156,19,32768\nMvaa 140,204,232\nD0 5,20,45,3072\nMvaa 108,204,228\nTa 158,19,34816\nMvaa 76,204,224\nMvaa 80,208,224\n"
"Ra 160,10,32768\nMvaa 112,208,228\nMvaa 144,208,232\nRa 164,10,34816\nMvaa 176,208,236\ns_add_u32 m0, s48, 4096\ns_add_u32 s46, s45, 0x10000\nMaaa 16,208,240\nRa 168,10,40960\n"
"Maaa 48,208,244\nD0 2,20,46,0\nMaaa 80,208,248\nRa 172,10,43008\nMaaa 112,208,252\nMaaa 116,212,252\nRa 176,10,49152\nMaaa 84,212,248\nMaaa 52,212,244\nRa 180,10,51200\n"
"Maaa 20,212,240\nMvaa 180,212,236\nRa 184,10,57344\nMvaa 148,212,232\nD0 3,20,46,1024\nMvaa 116,212,228\nRa 188,10,59392\nMvaa 84,212,224\nMvaa 88,216,224\nMvaa 120,216,228\n"
"Mvaa 152,216,232\nMvaa 184,216,236\nMaaa 24,216,240\nMaaa 56,216,244\nD0 4,20,46,2048\nMaaa 88,216,248\nMaaa 120,216,252\nMaaa 124,220,252\nMaaa 92,220,248\nMaaa 60,220,244\n"
"Maaa 28,220,240\nMvaa 188,220,236\nMvaa 156,220,232\nD0 5,20,46,3072\nMvaa 124,220,228\nMvaa 92,220,224\ns_waitcnt vmcnt(13) lgkmcnt(0)\ns_barrier\nMvaa 64,128,160\n"
"Ta 192,12,49152\nMvaa 96,128,164\nMvaa 128,128,168\nTa 194,12,51200\nMvaa 160,128,172\ns_add_u32 m0, s47, 16384\ns_add_u32 s44, s44, 0x40000\nMaaa 0,128,176\nTa 196,13,49152\n"
"Maaa 32,128,180\nD0 6,24,44,0\nMaaa 64,128,184\nTa 198,13,51200\nMaaa 96,128,188\nMaaa 100,132,188\nTa 200,14,49152\nMaaa 68,132,184\nMaaa 36,132,180\nTa 202,14,51200\n"
"Maaa 4,132,176\nMvaa 164,132,172\nTa 204,15,49152\nMvaa 132,132,168\nD0 7,24,44,1024\nMvaa 100,132,164\nTa 206,15,51200\nMvaa 68,132,160\nMvaa 72,136,160\nTa 208,16,49152\n"
"Mvaa 104,136,164\nMvaa 136,136,168\nTa 210,16,51200\nMvaa 168,136,172\nMaaa 8,136,176\nTa 212,17,49152\nMaaa 40,136,180\nD0 8,24,44,2048\nMaaa 72,136,184\nTa 214,17,51200\n"
"Maaa 104,136,188\nMaaa 108,140,188\nTa 216,18,49152\nMaaa 76,140,184\nMaaa 44,140,180\nTa 218,18,51200\nMaaa 12,140,176\nMvaa 172,140,172\nTa 220,19,49152\nMvaa 140,140,168\n"
"D0 9,24,44,3072\nMvaa 108,140,164\nTa 222,19,51200\nMvaa 76,140,160\nbuffer_load_dword v31, v24, s[24:27], s96 offen\ns_add_u32 s96, s96, 0x80000\nMvaa 80,144,160\n"
"Ra 224,11,32768\nMvaa 112,144,164\nMvaa 144,144,168\nRa 228,11,34816\nMvaa 176,144,172\ns_add_u32 m0, s47, 32768\ns_add_u32 s44, s44, 0x40000\nMaaa 16,144,176\nRa 232,11,40960\n"
"Maaa 48,144,180\nD0 6,24,44,0\nMaaa 80,144,184\nRa 236,11,43008\nMaaa 112,144,188\nMaaa 116,148,188\nRa 240,11,49152\nMaaa 84,148,184\nMaaa 52,148,180\nRa 244,11,51200\n"
"Maaa 20,148,176\nMvaa 180,148,172\nRa 248,11,57344\nMvaa 148,148,168\nD0 7,24,44,1024\nMvaa 116,148,164\nRa 252,11,59392\nMvaa 84,148,160\nMvaa 88,152,160\nMvaa 120,152,164\n"
"Mvaa 152,152,168\nMvaa 184,152,172\nMaaa 24,152,176\nMaaa 56,152,180\nD0 8,24,44,2048\nMaaa 88,152,184\nMaaa 120,152,188\nMaaa 124,156,188\nMaaa 92,156,184\nMaaa 60,156,180\n"
"Maaa 28,156,176\nMvaa 188,156,172\nMvaa 156,156,168\nD0 9,24,44,3072\nMvaa 124,156,164\nMvaa 92,156,160\ns_waitcnt vmcnt(9) lgkmcnt(0)\ns_barrier\nMvaa 64,192,224\nTa 128,12,0\n"
"Mvaa 96,192,228\nMvaa 128,192,232\nTa 130,12,2048\nMvaa 160,192,236\ns_add_u32 m0, s48, 32768\ns_add_u32 s45, s45, 128\nMaaa 0,192,240\nTa 132,13,0\nMaaa 32,192,244\nD0 2,20,45,0\n"
"Maaa 64,192,248\nTa 134,13,2048\nMaaa 96,192,252\nMaaa 100,196,252\nTa 136,14,0\nMaaa 68,196,248\nMaaa 36,196,244\nTa 138,14,2048\nMaaa 4,196,240\nMvaa 164,196,236\nTa 140,15,0\n"
"Mvaa 132,196,232\nD0 3,20,45,1024\nMvaa 100,196,228\nTa 142,15,2048\nMvaa 68,196,224\nMvaa 72,200,224\nTa 144,16,0\nMvaa 104,200,228\nMvaa 136,200,232\nTa 146,16,2048\n"
"Mvaa 168,200,236\nMaaa 8,200,240\nTa 148,17,0\nMaaa 40,200,244\nD0 4,20,45,2048\nMaaa 72,200,248\nTa 150,17,2048\nMaaa 104,200,252\nMaaa 108,204,252\nTa 152,18,0\nMaaa 76,204,248\n"
"Maaa 44,204,244\nTa 154,18,2048\nMaaa 12,204,240\nMvaa 172,204,236\nTa 156,19,0\nMvaa 140,204,232\nD0 5,20,45,3072\nMvaa 108,204,228\nTa 158,19,2048\nMvaa 76,204,224\n"
"Mvaa 80,208,224\nRa 160,10,0\nMvaa 112,208,228\nMvaa 144,208,232\nRa 164,10,2048\nMvaa 176,208,236\ns_add_u32 m0, s48, 36864\ns_add_u32 s46, s45, 0x10000\nMaaa 16,208,240\n"
"Ra 168,10,8192\nMaaa 48,208,244\nD0 2,20,46,0\nMaaa 80,208,248\nRa 172,10,10240\nMaaa 112,208,252\nMaaa 116,212,252\nRa 176,10,16384\nMaaa 84,212,248\nMaaa 52,212,244\n"
"Ra 180,10,18432\nMaaa 20,212,240\nMvaa 180,212,236\nRa 184,10,24576\nMvaa 148,212,232\nD0 3,20,46,1024\nMvaa 116,212,228\nRa 188,10,26624\nMvaa 84,212,224\nMvaa 88,216,224\n"
"Mvaa 120,216,228\nMvaa 152,216,232\nMvaa 184,216,236\nMaaa 24,216,240\nMaaa 56,216,244\nD0 4,20,46,2048\nMaaa 88,216,248\nMaaa 120,216,252\nMaaa 124,220,252\nMaaa 92,220,248\n"
"Maaa 60,220,244\nMaaa 28,220,240\nMvaa 188,220,236\nMvaa 156,220,232\nD0 5,20,46,3072\nMvaa 124,220,228\nMvaa 92,220,224\ns_waitcnt vmcnt(13) lgkmcnt(0)\ns_barrier\n"
"Mvaa 64,128,160\nTa 192,12,16384\ns_and_b32 s2, s12, 1\nMvaa 96,128,164\ns_and_b32 s3, s13, 6\nMvaa 128,128,168\nTa 194,12,18432\nMvaa 160,128,172\ns_add_u32 m0, s47, 49152\n"
"s_add_u32 s44, s44, 0x40000\nMaaa 0,128,176\nTa 196,13,16384\nMaaa 32,128,180\nD0 6,24,44,0\nMaaa 64,128,184\nTa 198,13,18432\nMaaa 96,128,188\ns_add_u32 s2, s2, s3\n"
"Maaa 100,132,188\nTa 200,14,16384\ns_lshl3_add_u32 s2, s14, s2\nMaaa 68,132,184\ns_cmp_lt_u32 s2, 14\ns_cselect_b32 s5, 0, 0x40000000\nMaaa 36,132,180\nTa 202,14,18432\n"
"Maaa 4,132,176\ns_mul_i32 s4, s73, 14\nMvaa 164,132,172\nTa 204,15,16384\nMvaa 132,132,168\nD0 7,24,44,1024\nMvaa 100,132,164\nTa 206,15,18432\nMvaa 68,132,160\n"
"s_add_u32 s4, s4, s2\nMvaa 72,136,160\nTa 208,16,16384\ns_mov_b32 s36, s62\nMvaa 104,136,164\ns_and_b32 s37, s63, 0xffff\nMvaa 136,136,168\nTa 210,16,18432\nMvaa 168,136,172\n"
"s_mov_b32 s38, 0x188000\nMaaa 8,136,176\nTa 212,17,16384\nMaaa 40,136,180\nD0 8,24,44,2048\nMaaa 72,136,184\nTa 214,17,18432\nMaaa 104,136,188\ns_mov_b32 s39, 0x20000\n"
"Maaa 108,140,188\nTa 216,18,16384\ns_mov_b32 s40, s64\nMaaa 76,140,184\ns_and_b32 s41, s65, 0xffff\nMaaa 44,140,180\nTa 218,18,18432\nMaaa 12,140,176\ns_mov_b32 s42, 0x188000\n"
"Mvaa 172,140,172\nTa 220,19,16384\nMvaa 140,140,168\nD0 9,24,44,3072\nMvaa 108,140,164\nTa 222,19,18432\nMvaa 76,140,160\nbuffer_load_dword v31, v24, s[24:27], s96 offen\n"
"s_add_u32 s96, s96, 0x80000\nMvaa 80,144,160\nRa 224,11,0\ns_mov_b32 s43, 0x20000\nMvaa 112,144,164\ns_mov_b32 s92, s66\nMvaa 144,144,168\nRa 228,11,2048\nMvaa 176,144,172\n"
"s_add_u32 m0, s47, 0\ns_add_u32 s44, s44, 0x40000\nMaaa 16,144,176\nRa 232,11,8192\nMaaa 48,144,180\nD0 6,24,44,0\nMaaa 80,144,184\nRa 236,11,10240\nMaaa 112,144,188\n"
"s_and_b32 s93, s67, 0xffff\nMaaa 116,148,188\nRa 240,11,16384\ns_mov_b32 s94, 0x188000\nMaaa 84,148,184\ns_mov_b32 s95, 0x20000\nMaaa 52,148,180\nRa 244,11,18432\nMaaa 20,148,176\n"
"v_lshrrev_b32 v252, 1, v0\nMvaa 180,148,172\nRa 248,11,24576\nMvaa 148,148,168\nD0 7,24,44,1024\nMvaa 116,148,164\nRa 252,11,26624\nMvaa 84,148,160\n"
"v_mul_u32_u24 v253, 14336, v252\nMvaa 88,152,160\nv_and_b32 v254, 1, v0\nv_lshl_add_u32 v253, v254, 4, v253\nMvaa 120,152,164\nv_mov_b32 v254, 0x40000000\n"
"v_cmp_gt_u32 vcc, 56, v0\nv_lshlrev_b32 v252, 3, v252\ns_mul_i32 s2, s11, 401408\nv_cndmask_b32 v253, v254, v253, vcc\nv_cndmask_b32 v255, v254, v252, vcc\nMvaa 152,152,168\n"
"s_lshl_b32 s3, s4, 5\nMvaa 184,152,172\ns_add_u32 s2, s2, s3\nMaaa 24,152,176\ns_or_b32 s86, s2, s5\nMaaa 56,152,180\nD0 8,24,44,2048\nMaaa 88,152,184\ns_add_u32 s87, s86, 7168\n"
"Maaa 120,152,188\nv_lshrrev_b32 v251, 4, v0\nMaaa 124,156,188\nv_mul_u32_u24 v252, 3584, v251\nv_and_b32 v251, 15, v0\nMaaa 92,156,184\nv_lshl_add_u32 v252, v251, 4, v252\n"
"v_cmp_gt_u32 vcc, 14, v251\ns_mul_i32 s2, s4, 14336\ns_mul_i32 s3, s11, 224\nv_cndmask_b32 v20, v254, v252, vcc\nMaaa 60,156,180\ns_add_u32 s2, s2, s3\nMaaa 28,156,176\n"
"s_or_b32 s88, s2, s5\nMvaa 188,156,172\nMvaa 156,156,168\nD0 9,24,44,3072\nMvaa 124,156,164\nMvaa 92,156,160\ns_waitcnt vmcnt(9) lgkmcnt(0)\ns_barrier\nMvaa 64,192,224\n"
"Ta 128,12,32768\nMvaa 96,192,228\nMvaa 128,192,232\nTa 130,12,34816\nMvaa 160,192,236\ns_add_u32 m0, s48, 0\ns_add_u32 s45, s45, 128\nMaaa 0,192,240\nTa 132,13,32768\n"
"Maaa 32,192,244\nD0 2,20,45,0\nMaaa 64,192,248\nTa 134,13,34816\nMaaa 96,192,252\nMaaa 100,196,252\nTa 136,14,32768\nMaaa 68,196,248\nMaaa 36,196,244\nTa 138,14,34816\n"
"Maaa 4,196,240\nMvaa 164,196,236\nTa 140,15,32768\nMvaa 132,196,232\nD0 3,20,45,1024\nMvaa 100,196,228\nTa 142,15,34816\nMvaa 68,196,224\nMvaa 72,200,224\nTa 144,16,32768\n"
"Mvaa 104,200,228\nMvaa 136,200,232\nTa 146,16,34816\nMvaa 168,200,236\nMaaa 8,200,240\nTa 148,17,32768\nMaaa 40,200,244\nD0 4,20,45,2048\nMaaa 72,200,248\nTa 150,17,34816\n"
"Maaa 104,200,252\nMaaa 108,204,252\nTa 152,18,32768\nMaaa 76,204,248\nMaaa 44,204,244\nTa 154,18,34816\nMaaa 12,204,240\nMvaa 172,204,236\nTa 156,19,32768\nMvaa 140,204,232\n"
"D0 5,20,45,3072\nMvaa 108,204,228\nTa 158,19,34816\nMvaa 76,204,224\nMvaa 80,208,224\nRa 160,10,32768\nMvaa 112,208,228\nMvaa 144,208,232\nRa 164,10,34816\nMvaa 176,208,236\n"
"s_add_u32 m0, s48, 4096\ns_add_u32 s46, s45, 0x10000\nMaaa 16,208,240\nRa 168,10,40960\nMaaa 48,208,244\nD0 2,20,46,0\nMaaa 80,208,248\nRa 172,10,43008\nMaaa 112,208,252\n"
"Maaa 116,212,252\nRa 176,10,49152\nMaaa 84,212,248\nMaaa 52,212,244\nRa 180,10,51200\nMaaa 20,212,240\nMvaa 180,212,236\nRa 184,10,57344\nMvaa 148,212,232\nD0 3,20,46,1024\n"
"Mvaa 116,212,228\nRa 188,10,59392\nMvaa 84,212,224\nMvaa 88,216,224\nMvaa 120,216,228\nMvaa 152,216,232\nMvaa 184,216,236\nMaaa 24,216,240\nMaaa 56,216,244\nD0 4,20,46,2048\n"
"Maaa 88,216,248\nMaaa 120,216,252\nMaaa 124,220,252\nMaaa 92,220,248\nMaaa 60,220,244\nMaaa 28,220,240\nMvaa 188,220,236\nMvaa 156,220,232\nD0 5,20,46,3072\nMvaa 124,220,228\n"
"Mvaa 92,220,224\ns_waitcnt vmcnt(13) lgkmcnt(0)\ns_barrier\nMvaa 64,128,160\nTa 192,12,49152\nMvaa 96,128,164\nMvaa 128,128,168\nTa 194,12,51200\nMvaa 160,128,172\n"
"s_add_u32 m0, s47, 16384\ns_add_u32 s44, s44, 0x40000\nMaaa 0,128,176\nTa 196,13,49152\nMaaa 32,128,180\nD0 6,24,44,0\nMaaa 64,128,184\nTa 198,13,51200\nMaaa 96,128,188\n"
"Maaa 100,132,188\nTa 200,14,49152\nMaaa 68,132,184\nMaaa 36,132,180\nTa 202,14,51200\nMaaa 4,132,176\nMvaa 164,132,172\nTa 204,15,49152\nMvaa 132,132,168\nD0 7,24,44,1024\n"
"Mvaa 100,132,164\nTa 206,15,51200\nMvaa 68,132,160\nMvaa 72,136,160\nTa 208,16,49152\nMvaa 104,136,164\nMvaa 136,136,168\nTa 210,16,51200\nMvaa 168,136,172\nMaaa 8,136,176\n"
"Ta 212,17,49152\nMaaa 40,136,180\nD0 8,24,44,2048\nMaaa 72,136,184\nTa 214,17,51200\nMaaa 104,136,188\nMaaa 108,140,188\nTa 216,18,49152\nMaaa 76,140,184\nMaaa 44,140,180\n"
"Ta 218,18,51200\nMaaa 12,140,176\nMvaa 172,140,172\nTa 220,19,49152\nMvaa 140,140,168\nD0 9,24,44,3072\nMvaa 108,140,164\nTa 222,19,51200\nMvaa 76,140,160\n"
"buffer_load_dword v31, v24, s[24:27], s96 offen\ns_add_u32 s96, s96, 0x80000\nMvaa 80,144,160\nRa 224,11,32768\nMvaa 112,144,164\nMvaa 144,144,168\nRa 228,11,34816\n"
"Mvaa 176,144,172\ns_add_u32 m0, s47, 32768\ns_add_u32 s44, s44, 0x40000\nMaaa 16,144,176\nRa 232,11,40960\nMaaa 48,144,180\nD0 6,24,44,0\nMaaa 80,144,184\nRa 236,11,43008\n"
"Maaa 112,144,188\nMaaa 116,148,188\nRa 240,11,49152\nMaaa 84,148,184\nMaaa 52,148,180\nRa 244,11,51200\nMaaa 20,148,176\nMvaa 180,148,172\nRa 248,11,57344\nMvaa 148,148,168\n"
"D0 7,24,44,1024\nMvaa 116,148,164\nRa 252,11,59392\nMvaa 84,148,160\nMvaa 88,152,160\nMvaa 120,152,164\nMvaa 152,152,168\nMvaa 184,152,172\nMaaa 24,152,176\nMaaa 56,152,180\n"
"D0 8,24,44,2048\nMaaa 88,152,184\nMaaa 120,152,188\nMaaa 124,156,188\nMaaa 92,156,184\nMaaa 60,156,180\nMaaa 28,156,176\nMvaa 188,156,172\nMvaa 156,156,168\nD0 9,24,44,3072\n"
"Mvaa 124,156,164\nMvaa 92,156,160\ns_waitcnt vmcnt(9) lgkmcnt(0)\ns_barrier\nMvaa 64,192,224\nTa 128,12,0\nMvaa 96,192,228\n"
"buffer_load_dwordx4 v[192:195], v253, s[36:39], s86 offen offset:0\nMvaa 128,192,232\nTa 130,12,2048\nMvaa 160,192,236\ns_add_u32 m0, s48, 32768\ns_add_u32 s45, s45, 128\n"
"Maaa 0,192,240\nTa 132,13,0\nMaaa 32,192,244\nD0 2,20,45,0\nMaaa 64,192,248\nTa 134,13,2048\nMaaa 96,192,252\nMaaa 100,196,252\nTa 136,14,0\nMaaa 68,196,248\nMaaa 36,196,244\n"
"Ta 138,14,2048\nMaaa 4,196,240\nMvaa 164,196,236\nTa 140,15,0\nMvaa 132,196,232\nD0 3,20,45,1024\nMvaa 100,196,228\nTa 142,15,2048\nMvaa 68,196,224\nMvaa 72,200,224\nTa 144,16,0\n"
"Mvaa 104,200,228\nMvaa 136,200,232\nTa 146,16,2048\nMvaa 168,200,236\nMaaa 8,200,240\nTa 148,17,0\nMaaa 40,200,244\nD0 4,20,45,2048\nMaaa 72,200,248\nTa 150,17,2048\n"
"Maaa 104,200,252\nMaaa 108,204,252\nTa 152,18,0\nMaaa 76,204,248\nMaaa 44,204,244\nTa 154,18,2048\nMaaa 12,204,240\nMvaa 172,204,236\nTa 156,19,0\nMvaa 140,204,232\n"
"D0 5,20,45,3072\nMvaa 108,204,228\nTa 158,19,2048\nMvaa 76,204,224\nMvaa 80,208,224\nRa 160,10,0\nMvaa 112,208,228\nMvaa 144,208,232\nRa 164,10,2048\nMvaa 176,208,236\n"
"s_add_u32 m0, s48, 36864\ns_add_u32 s46, s45, 0x10000\nMaaa 16,208,240\nRa 168,10,8192\nMaaa 48,208,244\nD0 2,20,46,0\nMaaa 80,208,248\nRa 172,10,10240\nMaaa 112,208,252\n"
"Maaa 116,212,252\nRa 176,10,16384\nMaaa 84,212,248\nMaaa 52,212,244\nRa 180,10,18432\nMaaa 20,212,240\nMvaa 180,212,236\nRa 184,10,24576\nMvaa 148,212,232\nD0 3,20,46,1024\n"
"Mvaa 116,212,228\nRa 188,10,26624\nMvaa 84,212,224\nMvaa 88,216,224\nMvaa 120,216,228\nMvaa 152,216,232\nMvaa 184,216,236\nMaaa 24,216,240\nMaaa 56,216,244\nD0 4,20,46,2048\n"
"Maaa 88,216,248\nMaaa 120,216,252\nMaaa 124,220,252\nMaaa 92,220,248\nMaaa 60,220,244\nMaaa 28,220,240\nMvaa 188,220,236\nMvaa 156,220,232\nD0 5,20,46,3072\nMvaa 124,220,228\n"
"Mvaa 92,220,224\ns_waitcnt vmcnt(14) lgkmcnt(0)\ns_barrier\nMvaa 64,128,160\nTa 192,12,16384\nMvaa 96,128,164\nbuffer_load_dwordx4 v[208:211], v253, s[40:43], s86 offen offset:0\n"
"Mvaa 128,128,168\nTa 194,12,18432\nMvaa 160,128,172\ns_add_u32 m0, s47, 49152\ns_add_u32 s44, s44, 0x40000\nMaaa 0,128,176\nTa 196,13,16384\nMaaa 32,128,180\nD0 6,24,44,0\n"
"Maaa 64,128,184\nTa 198,13,18432\nMaaa 96,128,188\nMaaa 100,132,188\nTa 200,14,16384\nMaaa 68,132,184\nMaaa 36,132,180\nTa 202,14,18432\nMaaa 4,132,176\nMvaa 164,132,172\n"
"Ta 204,15,16384\nMvaa 132,132,168\nD0 7,24,44,1024\nMvaa 100,132,164\nTa 206,15,18432\nMvaa 68,132,160\nMvaa 72,136,160\nTa 208,16,16384\nMvaa 104,136,164\nMvaa 136,136,168\n"
"Ta 210,16,18432\nMvaa 168,136,172\nMaaa 8,136,176\nTa 212,17,16384\nMaaa 40,136,180\nD0 8,24,44,2048\nMaaa 72,136,184\nTa 214,17,18432\nMaaa 104,136,188\nMaaa 108,140,188\n"
"Ta 216,18,16384\nMaaa 76,140,184\nMaaa 44,140,180\nTa 218,18,18432\nMaaa 12,140,176\nMvaa 172,140,172\nTa 220,19,16384\nMvaa 140,140,168\nD0 9,24,44,3072\nMvaa 108,140,164\n"
"Ta 222,19,18432\nMvaa 76,140,160\nbuffer_load_dword v31, v24, s[24:27], s96 offen\ns_add_u32 s96, s96, 0x80000\nMvaa 80,144,160\nRa 224,11,0\nMvaa 112,144,164\nMvaa 144,144,168\n"
"Ra 228,11,2048\nMvaa 176,144,172\ns_add_u32 m0, s47, 0\ns_add_u32 s44, s44, 0x40000\nMaaa 16,144,176\nRa 232,11,8192\nMaaa 48,144,180\nD0 6,24,44,0\nMaaa 80,144,184\n"
"Ra 236,11,10240\nMaaa 112,144,188\nMaaa 116,148,188\nRa 240,11,16384\nMaaa 84,148,184\nMaaa 52,148,180\nRa 244,11,18432\nMaaa 20,148,176\nMvaa 180,148,172\nRa 248,11,24576\n"
"Mvaa 148,148,168\nD0 7,24,44,1024\nMvaa 116,148,164\nRa 252,11,26624\nMvaa 84,148,160\nMvaa 88,152,160\nMvaa 120,152,164\nMvaa 152,152,168\nMvaa 184,152,172\nMaaa 24,152,176\n"
"Maaa 56,152,180\nD0 8,24,44,2048\nMaaa 88,152,184\nMaaa 120,152,188\nMaaa 124,156,188\nMaaa 92,156,184\nMaaa 60,156,180\nMaaa 28,156,176\nMvaa 188,156,172\nMvaa 156,156,168\n"
"D0 9,24,44,3072\nMvaa 124,156,164\nMvaa 92,156,160\ns_waitcnt vmcnt(10) lgkmcnt(0)\ns_barrier\nMvaa 64,192,224\nTa 128,12,32768\ns_lshl_b32 s6, s11, 12\n"
"s_add_u32 s6, s6, 0xc044000\nMvaa 96,192,228\nbuffer_load_dwordx4 v[196:199], v253, s[36:39], s86 offen offset:3584\nMvaa 128,192,232\nTa 130,12,34816\nMvaa 160,192,236\n"
"s_add_u32 m0, s48, 0\ns_add_u32 s45, s45, 128\nMaaa 0,192,240\nTa 132,13,32768\nMaaa 32,192,244\nD0 2,20,45,0\nMaaa 64,192,248\nTa 134,13,34816\nMaaa 96,192,252\n"
"buffer_load_dwordx4 v[32:35], v1, s[16:19], s6 offen offset:0 sc0 sc1\nMaaa 100,196,252\nTa 136,14,32768\nMaaa 68,196,248\nMaaa 36,196,244\nTa 138,14,34816\nMaaa 4,196,240\n"
"buffer_load_dwordx4 v[36:39], v1, s[16:19], s6 offen offset:1024 sc0 sc1\nMvaa 164,196,236\nTa 140,15,32768\nMvaa 132,196,232\nD0 3,20,45,1024\nMvaa 100,196,228\nTa 142,15,34816\n"
"Mvaa 68,196,224\nbuffer_load_dwordx4 v[40:43], v1, s[16:19], s6 offen offset:2048 sc0 sc1\nMvaa 72,200,224\nTa 144,16,32768\nMvaa 104,200,228\nMvaa 136,200,232\nTa 146,16,34816\n"
"Mvaa 168,200,236\nbuffer_load_dwordx4 v[44:47], v1, s[16:19], s6 offen offset:3072 sc0 sc1\nMaaa 8,200,240\nTa 148,17,32768\nMaaa 40,200,244\nD0 4,20,45,2048\nMaaa 72,200,248\n"
"Ta 150,17,34816\nMaaa 104,200,252\nMaaa 108,204,252\nTa 152,18,32768\nMaaa 76,204,248\nMaaa 44,204,244\nTa 154,18,34816\nMaaa 12,204,240\nMvaa 172,204,236\nTa 156,19,32768\n"
"Mvaa 140,204,232\nD0 5,20,45,3072\nMvaa 108,204,228\nTa 158,19,34816\nMvaa 76,204,224\nMvaa 80,208,224\nRa 160,10,32768\nMvaa 112,208,228\nMvaa 144,208,232\nRa 164,10,34816\n"
"Mvaa 176,208,236\ns_add_u32 m0, s48, 4096\ns_add_u32 s46, s45, 0x10000\nMaaa 16,208,240\nRa 168,10,40960\nMaaa 48,208,244\nD0 2,20,46,0\nMaaa 80,208,248\nRa 172,10,43008\n"
"Maaa 112,208,252\nMaaa 116,212,252\nRa 176,10,49152\nMaaa 84,212,248\nMaaa 52,212,244\nRa 180,10,51200\nMaaa 20,212,240\nMvaa 180,212,236\nRa 184,10,57344\nMvaa 148,212,232\n"
"D0 3,20,46,1024\nMvaa 116,212,228\nRa 188,10,59392\nMvaa 84,212,224\nMvaa 88,216,224\nMvaa 120,216,228\nMvaa 152,216,232\nMvaa 184,216,236\nMaaa 24,216,240\nMaaa 56,216,244\n"
"D0 4,20,46,2048\nMaaa 88,216,248\nMaaa 120,216,252\nMaaa 124,220,252\nMaaa 92,220,248\nMaaa 60,220,244\nMaaa 28,220,240\nMvaa 188,220,236\nMvaa 156,220,232\nD0 5,20,46,3072\n"
"Mvaa 124,220,228\nMvaa 92,220,224\ns_waitcnt vmcnt(18) lgkmcnt(0)\ns_barrier\nMvaa 64,128,160\nTa 192,12,49152\nMvaa 96,128,164\n"
"buffer_load_dwordx4 v[212:215], v253, s[40:43], s86 offen offset:3584\nMvaa 128,128,168\nTa 194,12,51200\nMvaa 160,128,172\ns_add_u32 m0, s47, 16384\ns_add_u32 s44, s44, 0x40000\n"
"Maaa 0,128,176\nTa 196,13,49152\nMaaa 32,128,180\nD0 6,24,44,0\nMaaa 64,128,184\nTa 198,13,51200\nMaaa 96,128,188\nMaaa 100,132,188\nTa 200,14,49152\nMaaa 68,132,184\n"
"Maaa 36,132,180\nTa 202,14,51200\nMaaa 4,132,176\nMvaa 164,132,172\nTa 204,15,49152\nMvaa 132,132,168\nD0 7,24,44,1024\nMvaa 100,132,164\nTa 206,15,51200\nMvaa 68,132,160\n"
"Mvaa 72,136,160\nTa 208,16,49152\nMvaa 104,136,164\nMvaa 136,136,168\nTa 210,16,51200\nMvaa 168,136,172\nMaaa 8,136,176\nTa 212,17,49152\nMaaa 40,136,180\nD0 8,24,44,2048\n"
"Maaa 72,136,184\nTa 214,17,51200\nMaaa 104,136,188\nMaaa 108,140,188\nTa 216,18,49152\nMaaa 76,140,184\nMaaa 44,140,180\nTa 218,18,51200\nMaaa 12,140,176\nMvaa 172,140,172\n"
"Ta 220,19,49152\nMvaa 140,140,168\nD0 9,24,44,3072\nMvaa 108,140,164\nTa 222,19,51200\nMvaa 76,140,160\nbuffer_load_dword v31, v24, s[24:27], s96 offen\n"
"s_add_u32 s96, s96, 0x80000\nMvaa 80,144,160\nRa 224,11,32768\nMvaa 112,144,164\nMvaa 144,144,168\nRa 228,11,34816\nMvaa 176,144,172\ns_add_u32 m0, s47, 32768\n"
"s_add_u32 s44, s44, 0x40000\nMaaa 16,144,176\nRa 232,11,40960\nMaaa 48,144,180\nD0 6,24,44,0\nMaaa 80,144,184\nRa 236,11,43008\nMaaa 112,144,188\nMaaa 116,148,188\n"
"Ra 240,11,49152\nMaaa 84,148,184\nMaaa 52,148,180\nRa 244,11,51200\nMaaa 20,148,176\nMvaa 180,148,172\nRa 248,11,57344\nMvaa 148,148,168\nD0 7,24,44,1024\nMvaa 116,148,164\n"
"Ra 252,11,59392\nMvaa 84,148,160\nMvaa 88,152,160\nMvaa 120,152,164\nMvaa 152,152,168\nMvaa 184,152,172\nMaaa 24,152,176\nMaaa 56,152,180\nD0 8,24,44,2048\nMaaa 88,152,184\n"
"Maaa 120,152,188\nMaaa 124,156,188\nMaaa 92,156,184\nMaaa 60,156,180\nMaaa 28,156,176\nMvaa 188,156,172\nMvaa 156,156,168\nD0 9,24,44,3072\nMvaa 124,156,164\nMvaa 92,156,160\n"
"s_waitcnt vmcnt(10) lgkmcnt(0)\ns_barrier\nMvaa 64,192,224\nTa 128,12,0\nMvaa 96,192,228\nbuffer_load_dwordx4 v[200:203], v253, s[36:39], s87 offen offset:0\nMvaa 128,192,232\n"
"Ta 130,12,2048\nMvaa 160,192,236\ns_add_u32 m0, s48, 32768\ns_add_u32 s45, s45, 128\nMaaa 0,192,240\nTa 132,13,0\nMaaa 32,192,244\nD0 2,20,45,0\nMaaa 64,192,248\nTa 134,13,2048\n"
"Maaa 96,192,252\nMaaa 100,196,252\nTa 136,14,0\nMaaa 68,196,248\nMaaa 36,196,244\nTa 138,14,2048\nMaaa 4,196,240\nMvaa 164,196,236\nTa 140,15,0\nMvaa 132,196,232\nD0 3,20,45,1024\n"
"Mvaa 100,196,228\nTa 142,15,2048\nMvaa 68,196,224\nMvaa 72,200,224\nTa 144,16,0\nMvaa 104,200,228\nMvaa 136,200,232\nTa 146,16,2048\nMvaa 168,200,236\nMaaa 8,200,240\nTa 148,17,0\n"
"Maaa 40,200,244\nD0 4,20,45,2048\nMaaa 72,200,248\nTa 150,17,2048\nMaaa 104,200,252\nMaaa 108,204,252\nTa 152,18,0\nMaaa 76,204,248\nMaaa 44,204,244\nTa 154,18,2048\n"
"Maaa 12,204,240\nMvaa 172,204,236\nTa 156,19,0\nMvaa 140,204,232\nD0 5,20,45,3072\nMvaa 108,204,228\nTa 158,19,2048\nMvaa 76,204,224\nMvaa 80,208,224\nRa 160,10,0\n"
"Mvaa 112,208,228\nMvaa 144,208,232\nRa 164,10,2048\nMvaa 176,208,236\ns_add_u32 m0, s48, 36864\ns_add_u32 s46, s45, 0x10000\nMaaa 16,208,240\nRa 168,10,8192\nMaaa 48,208,244\n"
"D0 2,20,46,0\nMaaa 80,208,248\nRa 172,10,10240\nMaaa 112,208,252\nMaaa 116,212,252\nRa 176,10,16384\nMaaa 84,212,248\nMaaa 52,212,244\nRa 180,10,18432\nMaaa 20,212,240\n"
"Mvaa 180,212,236\nRa 184,10,24576\nMvaa 148,212,232\nD0 3,20,46,1024\nMvaa 116,212,228\nRa 188,10,26624\nMvaa 84,212,224\nMvaa 88,216,224\nMvaa 120,216,228\nMvaa 152,216,232\n"
"Mvaa 184,216,236\nMaaa 24,216,240\nMaaa 56,216,244\nD0 4,20,46,2048\nMaaa 88,216,248\nMaaa 120,216,252\nMaaa 124,220,252\nMaaa 92,220,248\nMaaa 60,220,244\nMaaa 28,220,240\n"
"Mvaa 188,220,236\nMvaa 156,220,232\nD0 5,20,46,3072\nMvaa 124,220,228\nMvaa 92,220,224\ns_waitcnt vmcnt(14) lgkmcnt(0)\ns_barrier\nMvaa 64,128,160\nTa 192,12,16384\n"
"v_xor_b32 v48, s15, v33\nMvaa 96,128,164\nbuffer_load_dwordx4 v[216:219], v253, s[40:43], s87 offen offset:0\nMvaa 128,128,168\nTa 194,12,18432\nMvaa 160,128,172\n"
"s_add_u32 m0, s47, 49152\ns_add_u32 s44, s44, 0x40000\nMaaa 0,128,176\nTa 196,13,16384\nMaaa 32,128,180\nD0 6,24,44,0\nMaaa 64,128,184\nTa 198,13,18432\nMaaa 96,128,188\n"
"v_xor_b32 v49, s15, v35\nMaaa 100,132,188\nTa 200,14,16384\nMaaa 68,132,184\nv_xor_b32 v50, s15, v37\nMaaa 36,132,180\nTa 202,14,18432\nMaaa 4,132,176\nv_xor_b32 v51, s15, v39\n"
"Mvaa 164,132,172\nTa 204,15,16384\nMvaa 132,132,168\nD0 7,24,44,1024\nMvaa 100,132,164\nTa 206,15,18432\nMvaa 68,132,160\nv_xor_b32 v52, s15, v41\nMvaa 72,136,160\n"
"Ta 208,16,16384\nv_xor_b32 v53, s15, v43\nMvaa 104,136,164\nv_xor_b32 v54, s15, v45\nMvaa 136,136,168\nTa 210,16,18432\nMvaa 168,136,172\nv_xor_b32 v55, s15, v47\nMaaa 8,136,176\n"
"Ta 212,17,16384\nMaaa 40,136,180\nD0 8,24,44,2048\nMaaa 72,136,184\nTa 214,17,18432\nMaaa 104,136,188\nv_or3_b32 v48, v48, v49, v50\nMaaa 108,140,188\nTa 216,18,16384\n"
"Maaa 76,140,184\nv_or3_b32 v51, v51, v52, v53\nMaaa 44,140,180\nTa 218,18,18432\nMaaa 12,140,176\nv_or3_b32 v48, v48, v54, v55\nMvaa 172,140,172\nTa 220,19,16384\n"
"Mvaa 140,140,168\nD0 9,24,44,3072\nMvaa 108,140,164\nTa 222,19,18432\nMvaa 76,140,160\nbuffer_load_dword v31, v24, s[24:27], s96 offen\ns_add_u32 s96, s96, 0x80000\n"
"Mvaa 80,144,160\nRa 224,11,0\nv_or_b32 v48, v48, v51\nMvaa 112,144,164\nv_cmp_ne_u32 vcc, 0, v48\nMvaa 144,144,168\nRa 228,11,2048\nMvaa 176,144,172\ns_add_u32 m0, s47, 0\n"
"s_add_u32 s44, s44, 0x40000\nMaaa 16,144,176\nRa 232,11,8192\nMaaa 48,144,180\nD0 6,24,44,0\nMaaa 80,144,184\nRa 236,11,10240\nMaaa 112,144,188\ns_cbranch_vccnz .S209\n.S210:\n"
"Maaa 116,148,188\nRa 240,11,16384\nMaaa 84,148,184\nv_max_u32 v48, v32, v34\nMaaa 52,148,180\nRa 244,11,18432\nMaaa 20,148,176\nv_min_u32 v49, v32, v34\nMvaa 180,148,172\n"
"Ra 248,11,24576\nMvaa 148,148,168\nD0 7,24,44,1024\nMvaa 116,148,164\nRa 252,11,26624\nMvaa 84,148,160\nv_med3_u32 v49, v48, v49, v36\nMvaa 88,152,160\nv_max_u32 v48, v48, v36\n"
"v_med3_u32 v49, v48, v49, v38\nMvaa 120,152,164\nv_max_u32 v48, v48, v38\nMvaa 152,152,168\nv_med3_u32 v49, v48, v49, v40\nMvaa 184,152,172\nv_max_u32 v48, v48, v40\n"
"Maaa 24,152,176\nv_med3_u32 v49, v48, v49, v42\nMaaa 56,152,180\nD0 8,24,44,2048\nMaaa 88,152,184\nv_max_u32 v48, v48, v42\nMaaa 120,152,188\nv_med3_u32 v49, v48, v49, v44\n"
"Maaa 124,156,188\nv_max_u32 v48, v48, v44\nMaaa 92,156,184\nv_med3_u32 v49, v48, v49, v46\nMaaa 60,156,180\nv_max_u32 v48, v48, v46\nMaaa 28,156,176\nv_mov_b32 v50, v48\n"
"Mvaa 188,156,172\nMvaa 156,156,168\nD0 9,24,44,3072\nMvaa 124,156,164\nv_max_u32_dpp v50, v50, v50 row_ror:8 row_mask:0xf bank_mask:0xf\nMvaa 92,156,160\n"
"s_waitcnt vmcnt(10) lgkmcnt(0)\ns_barrier\nMvaa 64,192,224\nTa 128,12,32768\nv_max_u32_dpp v50, v50, v50 row_ror:4 row_mask:0xf bank_mask:0xf\nMvaa 96,192,228\n"
"buffer_load_dwordx4 v[204:207], v253, s[36:39], s87 offen offset:3584\nMvaa 128,192,232\nTa 130,12,34816\nMvaa 160,192,236\ns_add_u32 m0, s48, 0\ns_add_u32 s45, s45, 128\n"
"Maaa 0,192,240\nTa 132,13,32768\nMaaa 32,192,244\nD0 2,20,45,0\nMaaa 64,192,248\nTa 134,13,34816\nMaaa 96,192,252\n"
"v_max_u32_dpp v50, v50, v50 row_ror:2 row_mask:0xf bank_mask:0xf\nMaaa 100,196,252\nTa 136,14,32768\nMaaa 68,196,248\n"
"v_max_u32_dpp v50, v50, v50 row_ror:1 row_mask:0xf bank_mask:0xf\nMaaa 36,196,244\nTa 138,14,34816\nMaaa 4,196,240\nv_mov_b32 v52, v50\nMvaa 164,196,236\nTa 140,15,32768\n"
"Mvaa 132,196,232\nD0 3,20,45,1024\nMvaa 100,196,228\nTa 142,15,34816\nMvaa 68,196,224\nv_permlane16_swap_b32 v50, v52\nMvaa 72,200,224\nTa 144,16,32768\nv_max_u32 v50, v50, v52\n"
"Mvaa 104,200,228\nv_mov_b32 v52, v50\nMvaa 136,200,232\nTa 146,16,34816\nMvaa 168,200,236\nv_permlane32_swap_b32 v50, v52\nMaaa 8,200,240\nTa 148,17,32768\nMaaa 40,200,244\n"
"D0 4,20,45,2048\nMaaa 72,200,248\nTa 150,17,34816\nMaaa 104,200,252\nv_max_u32 v50, v50, v52\nv_cmp_eq_u32 vcc, v48, v50\ns_lshl_b32 s2, s11, 3\ns_add_u32 s2, s2, 131072\n"
"v_cndmask_b32 v51, v48, v49, vcc\nMaaa 108,204,252\nTa 152,18,32768\nMaaa 76,204,248\nv_max_u32_dpp v51, v51, v51 row_ror:8 row_mask:0xf bank_mask:0xf\nMaaa 44,204,244\n"
"Ta 154,18,34816\nMaaa 12,204,240\nv_max_u32_dpp v51, v51, v51 row_ror:4 row_mask:0xf bank_mask:0xf\nMvaa 172,204,236\nTa 156,19,32768\nMvaa 140,204,232\nD0 5,20,45,3072\n"
"Mvaa 108,204,228\nTa 158,19,34816\nMvaa 76,204,224\nv_max_u32_dpp v51, v51, v51 row_ror:2 row_mask:0xf bank_mask:0xf\nMvaa 80,208,224\nRa 160,10,32768\nMvaa 112,208,228\n"
"v_max_u32_dpp v51, v51, v51 row_ror:1 row_mask:0xf bank_mask:0xf\nMvaa 144,208,232\nRa 164,10,34816\nMvaa 176,208,236\ns_add_u32 m0, s48, 4096\ns_add_u32 s46, s45, 0x10000\n"
"Maaa 16,208,240\nRa 168,10,40960\nMaaa 48,208,244\nD0 2,20,46,0\nMaaa 80,208,248\nRa 172,10,43008\nMaaa 112,208,252\nv_mov_b32 v52, v51\nMaaa 116,212,252\nRa 176,10,49152\n"
"Maaa 84,212,248\nv_permlane16_swap_b32 v51, v52\nMaaa 52,212,244\nRa 180,10,51200\nMaaa 20,212,240\nv_max_u32 v51, v51, v52\nMvaa 180,212,236\nRa 184,10,57344\nMvaa 148,212,232\n"
"D0 3,20,46,1024\nMvaa 116,212,228\nRa 188,10,59392\nMvaa 84,212,224\nv_mov_b32 v52, v51\nMvaa 88,216,224\nMvaa 120,216,228\nv_permlane32_swap_b32 v51, v52\nMvaa 152,216,232\n"
"v_max_u32 v51, v51, v52\nMvaa 184,216,236\nv_mov_b32 v52, s2\nMaaa 24,216,240\nds_write_b64 v52, v[50:51]\nMaaa 56,216,244\nD0 4,20,46,2048\nMaaa 88,216,248\n"
"v_mov_b32 v52, 131072\nMaaa 120,216,252\nMaaa 124,220,252\nMaaa 92,220,248\nMaaa 60,220,244\nMaaa 28,220,240\nMvaa 188,220,236\nMvaa 156,220,232\nD0 5,20,46,3072\n"
"Mvaa 124,220,228\nMvaa 92,220,224\ns_waitcnt vmcnt(14) lgkmcnt(0)\ns_barrier\nMvaa 64,128,160\nTa 192,12,49152\nMvaa 96,128,164\n"
"buffer_load_dwordx4 v[220:223], v253, s[40:43], s87 offen offset:3584\nMvaa 128,128,168\nTa 194,12,51200\nMvaa 160,128,172\ns_add_u32 m0, s47, 16384\ns_add_u32 s44, s44, 0x40000\n"
"Maaa 0,128,176\nTa 196,13,49152\nMaaa 32,128,180\nD0 6,24,44,0\nMaaa 64,128,184\nTa 198,13,51200\nMaaa 96,128,188\nRv 32,52,0\nMaaa 100,132,188\nTa 200,14,49152\nMaaa 68,132,184\n"
"Rv 36,52,16\nMaaa 36,132,180\nTa 202,14,51200\nMaaa 4,132,176\nMvaa 164,132,172\nTa 204,15,49152\nMvaa 132,132,168\nD0 7,24,44,1024\nMvaa 100,132,164\nTa 206,15,51200\n"
"Mvaa 68,132,160\nMvaa 72,136,160\nTa 208,16,49152\nMvaa 104,136,164\nMvaa 136,136,168\nTa 210,16,51200\nMvaa 168,136,172\nMaaa 8,136,176\nTa 212,17,49152\nMaaa 40,136,180\n"
"D0 8,24,44,2048\nMaaa 72,136,184\nTa 214,17,51200\nMaaa 104,136,188\nWL 7\nMaaa 108,140,188\nTa 216,18,49152\nMaaa 76,140,184\nv_max_u32 v48, v32, v33\nMaaa 44,140,180\n"
"Ta 218,18,51200\nMaaa 12,140,176\nv_min_u32 v49, v32, v33\nMvaa 172,140,172\nTa 220,19,49152\nMvaa 140,140,168\nD0 9,24,44,3072\nMvaa 108,140,164\nTa 222,19,51200\n"
"Mvaa 76,140,160\nbuffer_load_dword v31, v24, s[24:27], s96 offen\ns_add_u32 s96, s96, 0x80000\nMvaa 80,144,160\nRa 224,11,32768\nMvaa 112,144,164\nv_med3_u32 v49, v48, v49, v34\n"
"Mvaa 144,144,168\nRa 228,11,34816\nMvaa 176,144,172\ns_add_u32 m0, s47, 32768\ns_add_u32 s44, s44, 0x40000\nMaaa 16,144,176\nRa 232,11,40960\nMaaa 48,144,180\nD0 6,24,44,0\n"
"Maaa 80,144,184\nRa 236,11,43008\nMaaa 112,144,188\nv_max_u32 v48, v48, v34\nMaaa 116,148,188\nRa 240,11,49152\nMaaa 84,148,184\nv_med3_u32 v49, v48, v49, v35\nMaaa 52,148,180\n"
"Ra 244,11,51200\nMaaa 20,148,176\nv_max_u32 v48, v48, v35\nMvaa 180,148,172\nRa 248,11,57344\nMvaa 148,148,168\nD0 7,24,44,1024\nMvaa 116,148,164\nRa 252,11,59392\n"
"Mvaa 84,148,160\nv_med3_u32 v49, v48, v49, v36\nMvaa 88,152,160\nv_max_u32 v48, v48, v36\nMvaa 120,152,164\nv_med3_u32 v49, v48, v49, v37\nMvaa 152,152,168\n"
"v_max_u32 v48, v48, v37\nMvaa 184,152,172\nv_med3_u32 v49, v48, v49, v38\nMaaa 24,152,176\nv_max_u32 v48, v48, v38\nMaaa 56,152,180\nD0 8,24,44,2048\nMaaa 88,152,184\n"
"v_med3_u32 v49, v48, v49, v39\nMaaa 120,152,188\nv_max_u32 v48, v48, v39\nMaaa 124,156,188\nv_readfirstlane_b32 s2, v48\nMaaa 92,156,184\nv_readfirstlane_b32 s3, v49\n"
"Maaa 60,156,180\nMaaa 28,156,176\nMvaa 188,156,172\ns_andn2_b32 s2, 0x7ff, s2\nMvaa 156,156,168\nD0 9,24,44,3072\nMvaa 124,156,164\ns_andn2_b32 s3, 0x7ff, s3\nMvaa 92,156,160\n"
"s_waitcnt vmcnt(10) lgkmcnt(0)\ns_barrier\nMvaa 64,192,224\nTa 128,12,0\nMvaa 96,192,228\nbuffer_load_dwordx4 v[224:227], v20, s[92:95], s88 offen offset:0\nMvaa 128,192,232\n"
"Ta 130,12,2048\nMvaa 160,192,236\ns_add_u32 m0, s48, 32768\ns_add_u32 s45, s45, 128\nMaaa 0,192,240\nTa 132,13,0\nMaaa 32,192,244\nD0 2,20,45,0\nMaaa 64,192,248\nTa 134,13,2048\n"
"Maaa 96,192,252\ns_lshl_b32 s71, s3, 16\nMaaa 100,196,252\nTa 136,14,0\nMaaa 68,196,248\ns_or_b32 s71, s71, s2\nMaaa 36,196,244\nTa 138,14,2048\nMaaa 4,196,240\n"
"s_mul_i32 s2, s2, 896\nMvaa 164,196,236\nTa 140,15,0\nMvaa 132,196,232\nD0 3,20,45,1024\nMvaa 100,196,228\nTa 142,15,2048\nMvaa 68,196,224\ns_mul_i32 s3, s3, 896\nMvaa 72,200,224\n"
"Ta 144,16,0\nMvaa 104,200,228\ns_mul_i32 s6, s11, 224\nMvaa 136,200,232\nTa 146,16,2048\nMvaa 168,200,236\ns_add_u32 s2, s2, s6\nMaaa 8,200,240\nTa 148,17,0\nMaaa 40,200,244\n"
"D0 4,20,45,2048\nMaaa 72,200,248\nTa 150,17,2048\nMaaa 104,200,252\ns_add_u32 s3, s3, s6\nMaaa 108,204,252\nTa 152,18,0\nMaaa 76,204,248\n"
"buffer_load_dwordx2 v[240:241], v255, s[28:31], s2 offen\nMaaa 44,204,244\nTa 154,18,2048\nMaaa 12,204,240\nMvaa 172,204,236\nTa 156,19,0\nMvaa 140,204,232\nD0 5,20,45,3072\n"
"Mvaa 108,204,228\nTa 158,19,2048\nMvaa 76,204,224\nbuffer_load_dwordx2 v[242:243], v255, s[28:31], s3 offen\ns_lshl2_add_u32 s2, s10, s11\ns_cmp_eq_u32 s2, 0\n"
"s_cselect_b32 s2, 0, 0x40000000\ns_add_u32 s2, s2, 0xc048000\nMvaa 80,208,224\nRa 160,10,0\nMvaa 112,208,228\nbuffer_load_dwordx4 v[228:231], v20, s[92:95], s88 offen offset:896\n"
"Mvaa 144,208,232\nRa 164,10,2048\nMvaa 176,208,236\ns_add_u32 m0, s48, 36864\ns_add_u32 s46, s45, 0x10000\nMaaa 16,208,240\nRa 168,10,8192\nMaaa 48,208,244\nD0 2,20,46,0\n"
"Maaa 80,208,248\nRa 172,10,10240\nMaaa 112,208,252\nbuffer_store_dword v25, off, s[16:19], s2 offset:0 sc0 sc1\nMaaa 116,212,252\nRa 176,10,16384\nMaaa 84,212,248\n"
"Maaa 52,212,244\nRa 180,10,18432\nMaaa 20,212,240\nMvaa 180,212,236\nRa 184,10,24576\nMvaa 148,212,232\nD0 3,20,46,1024\nMvaa 116,212,228\nRa 188,10,26624\nMvaa 84,212,224\n"
"Mvaa 88,216,224\nMvaa 120,216,228\nMvaa 152,216,232\nMvaa 184,216,236\nMaaa 24,216,240\nMaaa 56,216,244\nD0 4,20,46,2048\nMaaa 88,216,248\nMaaa 120,216,252\nMaaa 124,220,252\n"
"Maaa 92,220,248\nMaaa 60,220,244\nMaaa 28,220,240\nMvaa 188,220,236\nMvaa 156,220,232\nD0 5,20,46,3072\nMvaa 124,220,228\nMvaa 92,220,224\ns_waitcnt vmcnt(18) lgkmcnt(0)\n"
"s_barrier\nMvaa 64,128,160\nTa 192,12,16384\nMvaa 96,128,164\nbuffer_load_dwordx4 v[232:235], v20, s[92:95], s88 offen offset:1792\nMvaa 128,128,168\nTa 194,12,18432\n"
"Mvaa 160,128,172\ns_add_u32 m0, s47, 49152\ns_add_u32 s44, s44, 0x40000\nMaaa 0,128,176\nTa 196,13,16384\nMaaa 32,128,180\nD0 6,24,44,0\nMaaa 64,128,184\nTa 198,13,18432\n"
"Maaa 96,128,188\nMaaa 100,132,188\nTa 200,14,16384\nMaaa 68,132,184\nMaaa 36,132,180\nTa 202,14,18432\nMaaa 4,132,176\nMvaa 164,132,172\nTa 204,15,16384\nMvaa 132,132,168\n"
"D0 7,24,44,1024\nMvaa 100,132,164\nTa 206,15,18432\nMvaa 68,132,160\nMvaa 72,136,160\nTa 208,16,16384\nMvaa 104,136,164\nMvaa 136,136,168\nTa 210,16,18432\nMvaa 168,136,172\n"
"Maaa 8,136,176\nTa 212,17,16384\nMaaa 40,136,180\nD0 8,24,44,2048\nMaaa 72,136,184\nTa 214,17,18432\nMaaa 104,136,188\nMaaa 108,140,188\nTa 216,18,16384\nMaaa 76,140,184\n"
"Maaa 44,140,180\nTa 218,18,18432\nMaaa 12,140,176\nMvaa 172,140,172\nTa 220,19,16384\nMvaa 140,140,168\nD0 9,24,44,3072\nMvaa 108,140,164\nTa 222,19,18432\nMvaa 76,140,160\n"
"buffer_load_dword v31, v24, s[24:27], s96 offen\ns_add_u32 s96, s96, 0x80000\nMvaa 80,144,160\nRa 224,11,0\nMvaa 112,144,164\n"
"buffer_load_dwordx4 v[236:239], v20, s[92:95], s88 offen offset:2688\ns_lshl_b32 s5, s73, 7\ns_add_u32 s5, s5, 0xc04ac00\nv_bfe_u32 v27, v1, 2, 7\n"
"buffer_load_dword v27, v27, s[16:19], s5 offen sc0 sc1\nMvaa 144,144,168\nRa 228,11,2048\nMvaa 176,144,172\ns_add_u32 m0, s47, 0\ns_add_u32 s44, s44, 0x40000\nMaaa 16,144,176\n"
"Ra 232,11,8192\nMaaa 48,144,180\nD0 6,24,44,0\nMaaa 80,144,184\nRa 236,11,10240\nMaaa 112,144,188\nMaaa 116,148,188\nRa 240,11,16384\nMaaa 84,148,184\nMaaa 52,148,180\n"
"Ra 244,11,18432\nMaaa 20,148,176\nMvaa 180,148,172\nRa 248,11,24576\nMvaa 148,148,168\nD0 7,24,44,1024\nMvaa 116,148,164\nRa 252,11,26624\nMvaa 84,148,160\nMvaa 88,152,160\n"
"Mvaa 120,152,164\nMvaa 152,152,168\nMvaa 184,152,172\nMaaa 24,152,176\nMaaa 56,152,180\nD0 8,24,44,2048\nMaaa 88,152,184\nMaaa 120,152,188\nMaaa 124,156,188\nMaaa 92,156,184\n"
"Maaa 60,156,180\nMaaa 28,156,176\nMvaa 188,156,172\nMvaa 156,156,168\nD0 9,24,44,3072\nMvaa 124,156,164\nMvaa 92,156,160\ns_waitcnt vmcnt(12) lgkmcnt(0)\ns_barrier\n"
"Mvaa 64,192,224\nTa 128,12,32768\nMvaa 96,192,228\nv_lshlrev_b32 v244, 16, v240\nMvaa 128,192,232\nTa 130,12,34816\nv_and_b32 v245, 0xffff0000, v240\nMvaa 160,192,236\n"
"s_add_u32 m0, s48, 0\ns_add_u32 s45, s45, 128\nMaaa 0,192,240\nTa 132,13,32768\nv_lshlrev_b32 v246, 16, v241\nMaaa 32,192,244\nD0 2,20,45,0\nv_and_b32 v247, 0xffff0000, v241\n"
"Maaa 64,192,248\nTa 134,13,34816\nMaaa 96,192,252\nv_lshlrev_b32 v248, 16, v242\nv_and_b32 v249, 0xffff0000, v242\nMaaa 100,196,252\nTa 136,14,32768\nv_lshlrev_b32 v250, 16, v243\n"
"Maaa 68,196,248\nv_and_b32 v251, 0xffff0000, v243\nv_lshlrev_b32 v252, 16, v192\nMaaa 36,196,244\nTa 138,14,34816\nv_and_b32 v253, 0xffff0000, v192\nMaaa 4,196,240\nMU 32,244,252\n"
"Mvaa 164,196,236\nTa 140,15,32768\nMU 33,244,253\nMvaa 132,196,232\nD0 3,20,45,1024\nMU 40,248,252\nMvaa 100,196,228\nTa 142,15,34816\nMU 41,248,253\nMvaa 68,196,224\n"
"v_lshlrev_b32 v252, 16, v193\nv_and_b32 v253, 0xffff0000, v193\nMvaa 72,200,224\nTa 144,16,32768\nMU 34,244,252\nMvaa 104,200,228\nMU 35,244,253\nMvaa 136,200,232\n"
"Ta 146,16,34816\nMU 42,248,252\nMvaa 168,200,236\nMU 43,248,253\nv_lshlrev_b32 v252, 16, v194\nMaaa 8,200,240\nTa 148,17,32768\nv_and_b32 v253, 0xffff0000, v194\nMaaa 40,200,244\n"
"D0 4,20,45,2048\nMU 36,244,252\nMaaa 72,200,248\nTa 150,17,34816\nMaaa 104,200,252\nMU 37,244,253\nMU 44,248,252\nMaaa 108,204,252\nTa 152,18,32768\nMU 45,248,253\n"
"Maaa 76,204,248\nv_lshlrev_b32 v252, 16, v195\nv_and_b32 v253, 0xffff0000, v195\nMaaa 44,204,244\nTa 154,18,34816\nMU 38,244,252\nMaaa 12,204,240\nMU 39,244,253\nMvaa 172,204,236\n"
"Ta 156,19,32768\nMU 46,248,252\nMvaa 140,204,232\nD0 5,20,45,3072\nMU 47,248,253\nMvaa 108,204,228\nTa 158,19,34816\nv_lshlrev_b32 v252, 16, v208\nMvaa 76,204,224\n"
"v_and_b32 v253, 0xffff0000, v208\nMU 48,244,252\nMvaa 80,208,224\nRa 160,10,32768\nMU 49,244,253\nMvaa 112,208,228\nMU 56,248,252\nMvaa 144,208,232\nRa 164,10,34816\n"
"MU 57,248,253\nMvaa 176,208,236\ns_add_u32 m0, s48, 4096\ns_add_u32 s46, s45, 0x10000\nMaaa 16,208,240\nRa 168,10,40960\nv_lshlrev_b32 v252, 16, v209\nMaaa 48,208,244\n"
"D0 2,20,46,0\nv_and_b32 v253, 0xffff0000, v209\nMaaa 80,208,248\nRa 172,10,43008\nMaaa 112,208,252\nMU 50,244,252\nMU 51,244,253\nMaaa 116,212,252\nRa 176,10,49152\nMU 58,248,252\n"
"Maaa 84,212,248\nMU 59,248,253\nv_lshlrev_b32 v252, 16, v210\nMaaa 52,212,244\nRa 180,10,51200\nv_and_b32 v253, 0xffff0000, v210\nMaaa 20,212,240\nMU 52,244,252\nMvaa 180,212,236\n"
"Ra 184,10,57344\nMU 53,244,253\nMvaa 148,212,232\nD0 3,20,46,1024\nMU 60,248,252\nMvaa 116,212,228\nRa 188,10,59392\nMU 61,248,253\nMvaa 84,212,224\nv_lshlrev_b32 v252, 16, v211\n"
"v_and_b32 v253, 0xffff0000, v211\nMvaa 88,216,224\nMU 54,244,252\nMU 55,244,253\nMvaa 120,216,228\nMU 62,248,252\nMvaa 152,216,232\nMU 63,248,253\nv_lshlrev_b32 v252, 16, v196\n"
"Mvaa 184,216,236\nv_and_b32 v253, 0xffff0000, v196\nFM 32,245,252\nMaaa 24,216,240\nFM 33,245,253\nFM 40,249,252\nMaaa 56,216,244\nD0 4,20,46,2048\nFM 41,249,253\nMaaa 88,216,248\n"
"v_lshlrev_b32 v252, 16, v197\nMaaa 120,216,252\nv_and_b32 v253, 0xffff0000, v197\nFM 34,245,252\nMaaa 124,220,252\nFM 35,245,253\nFM 42,249,252\nMaaa 92,220,248\nFM 43,249,253\n"
"v_lshlrev_b32 v252, 16, v198\nMaaa 60,220,244\nv_and_b32 v253, 0xffff0000, v198\nFM 36,245,252\nMaaa 28,220,240\nFM 37,245,253\nMvaa 188,220,236\nFM 44,249,252\nFM 45,249,253\n"
"Mvaa 156,220,232\nD0 5,20,46,3072\nv_lshlrev_b32 v252, 16, v199\nMvaa 124,220,228\nv_and_b32 v253, 0xffff0000, v199\nFM 38,245,252\nMvaa 92,220,224\n"
"s_waitcnt vmcnt(15) lgkmcnt(0)\ns_barrier\nMvaa 64,128,160\nTa 192,12,49152\nFM 39,245,253\nMvaa 96,128,164\nFM 46,249,252\nMvaa 128,128,168\nTa 194,12,51200\nFM 47,249,253\n"
"Mvaa 160,128,172\ns_add_u32 m0, s47, 16384\ns_add_u32 s44, s44, 0x40000\nMaaa 0,128,176\nTa 196,13,49152\nv_lshlrev_b32 v252, 16, v212\nMaaa 32,128,180\nD0 6,24,44,0\n"
"v_and_b32 v253, 0xffff0000, v212\nMaaa 64,128,184\nTa 198,13,51200\nMaaa 96,128,188\nFM 48,245,252\nFM 49,245,253\nMaaa 100,132,188\nTa 200,14,49152\nFM 56,249,252\n"
"Maaa 68,132,184\nFM 57,249,253\nv_lshlrev_b32 v252, 16, v213\nMaaa 36,132,180\nTa 202,14,51200\nv_and_b32 v253, 0xffff0000, v213\nMaaa 4,132,176\nFM 50,245,252\nMvaa 164,132,172\n"
"Ta 204,15,49152\nFM 51,245,253\nMvaa 132,132,168\nD0 7,24,44,1024\nFM 58,249,252\nMvaa 100,132,164\nTa 206,15,51200\nFM 59,249,253\nMvaa 68,132,160\nv_lshlrev_b32 v252, 16, v214\n"
"v_and_b32 v253, 0xffff0000, v214\nMvaa 72,136,160\nTa 208,16,49152\nFM 52,245,252\nMvaa 104,136,164\nFM 53,245,253\nMvaa 136,136,168\nTa 210,16,51200\nFM 60,249,252\n"
"Mvaa 168,136,172\nFM 61,249,253\nv_lshlrev_b32 v252, 16, v215\nMaaa 8,136,176\nTa 212,17,49152\nv_and_b32 v253, 0xffff0000, v215\nMaaa 40,136,180\nD0 8,24,44,2048\nFM 54,245,252\n"
"Maaa 72,136,184\nTa 214,17,51200\nMaaa 104,136,188\nFM 55,245,253\nFM 62,249,252\nMaaa 108,140,188\nTa 216,18,49152\nFM 63,249,253\nMaaa 76,140,184\nv_lshlrev_b32 v252, 16, v200\n"
"v_and_b32 v253, 0xffff0000, v200\nMaaa 44,140,180\nTa 218,18,51200\nFM 32,246,252\nMaaa 12,140,176\nFM 33,246,253\nMvaa 172,140,172\nTa 220,19,49152\nFM 40,250,252\n"
"Mvaa 140,140,168\nD0 9,24,44,3072\nFM 41,250,253\nMvaa 108,140,164\nTa 222,19,51200\nv_lshlrev_b32 v252, 16, v201\nMvaa 76,140,160\n"
"buffer_load_dword v31, v24, s[24:27], s96 offen\ns_add_u32 s96, s96, 0x80000\nMvaa 80,144,160\nRa 224,11,32768\nv_and_b32 v253, 0xffff0000, v201\nMvaa 112,144,164\nFM 34,246,252\n"
"Mvaa 144,144,168\nRa 228,11,34816\nFM 35,246,253\nMvaa 176,144,172\ns_add_u32 m0, s47, 32768\ns_add_u32 s44, s44, 0x40000\nMaaa 16,144,176\nRa 232,11,40960\nFM 42,250,252\n"
"Maaa 48,144,180\nD0 6,24,44,0\nFM 43,250,253\nMaaa 80,144,184\nRa 236,11,43008\nMaaa 112,144,188\nv_lshlrev_b32 v252, 16, v202\nv_and_b32 v253, 0xffff0000, v202\nMaaa 116,148,188\n"
"Ra 240,11,49152\nFM 36,246,252\nMaaa 84,148,184\nFM 37,246,253\nFM 44,250,252\nMaaa 52,148,180\nRa 244,11,51200\nFM 45,250,253\nMaaa 20,148,176\nv_lshlrev_b32 v252, 16, v203\n"
"Mvaa 180,148,172\nRa 248,11,57344\nv_and_b32 v253, 0xffff0000, v203\nMvaa 148,148,168\nD0 7,24,44,1024\nFM 38,246,252\nMvaa 116,148,164\nRa 252,11,59392\nFM 39,246,253\n"
"Mvaa 84,148,160\nFM 46,250,252\nFM 47,250,253\nMvaa 88,152,160\nv_lshlrev_b32 v252, 16, v216\nv_and_b32 v253, 0xffff0000, v216\nMvaa 120,152,164\nFM 48,246,252\nMvaa 152,152,168\n"
"FM 49,246,253\nFM 56,250,252\nMvaa 184,152,172\nFM 57,250,253\nv_lshlrev_b32 v252, 16, v217\nMaaa 24,152,176\nv_and_b32 v253, 0xffff0000, v217\nFM 50,246,252\nMaaa 56,152,180\n"
"D0 8,24,44,2048\nFM 51,246,253\nMaaa 88,152,184\nFM 58,250,252\nMaaa 120,152,188\nFM 59,250,253\nv_lshlrev_b32 v252, 16, v218\nMaaa 124,156,188\nv_and_b32 v253, 0xffff0000, v218\n"
"FM 52,246,252\nMaaa 92,156,184\nFM 53,246,253\nFM 60,250,252\nMaaa 60,156,180\nFM 61,250,253\nv_lshlrev_b32 v252, 16, v219\nMaaa 28,156,176\nv_and_b32 v253, 0xffff0000, v219\n"
"Mvaa 188,156,172\nFM 54,246,252\nWV 20\nv_xor_b32 v27, s15, v27\nv_and_b32 v255, 0xffffdfff, v27\nv_cmp_ne_u32 vcc, 0, v255\ns_nop 0\ns_cbranch_vccnz .S212\n.S213:\n"
"v_cmp_ne_u32 vcc, 0, v27\ns_nop 3\ns_bcnt1_i32_b64 s4, vcc\ns_cmp_gt_u32 s4, 32\ns_cselect_b32 s4, 1, 0\nMvaa 156,156,168\nD0 9,24,44,3072\ns_xor_b32 s4, s4, s12\n"
"Mvaa 124,156,164\ns_and_b32 s4, s4, 1\ns_lshl_b32 s4, s4, 13\nMvaa 92,156,160\ns_waitcnt vmcnt(9) lgkmcnt(0)\ns_barrier\nMvaa 64,192,224\nTa 128,12,0\n"
"s_mul_i32 s72, s73, 25165824\nMvaa 96,192,228\ns_add_u32 s72, s72, s4\nMvaa 128,192,232\nTa 130,12,2048\ns_and_b32 s4, s12, 1\nMvaa 160,192,236\ns_add_u32 m0, s48, 32768\n"
"s_add_u32 s45, s45, 128\nMaaa 0,192,240\nTa 132,13,0\ns_lshl_b32 s4, s4, 21\nMaaa 32,192,244\nD0 2,20,45,0\ns_add_u32 s72, s72, s4\nMaaa 64,192,248\nTa 134,13,2048\n"
"Maaa 96,192,252\ns_lshl3_add_u32 s2, s13, s11\ns_lshl_b32 s2, s2, 11\nMaaa 100,196,252\nTa 136,14,0\ns_lshl_b32 s3, s14, 22\nMaaa 68,196,248\ns_add_u32 s2, s2, s3\n"
"s_add_u32 s2, s2, s72\nMaaa 36,196,244\nTa 138,14,2048\ns_add_u32 s49, s2, 0xc00000\nMaaa 4,196,240\nFM 55,246,253\nMvaa 164,196,236\nTa 140,15,0\nFM 62,250,252\nMvaa 132,196,232\n"
"D0 3,20,45,1024\nFM 63,250,253\nMvaa 100,196,228\nTa 142,15,2048\nv_lshlrev_b32 v252, 16, v204\nMvaa 68,196,224\nv_and_b32 v253, 0xffff0000, v204\nFM 32,247,252\nMvaa 72,200,224\n"
"Ta 144,16,0\nFM 33,247,253\nMvaa 104,200,228\nFM 40,251,252\nMvaa 136,200,232\nTa 146,16,2048\nFM 41,251,253\nMvaa 168,200,236\nv_lshlrev_b32 v252, 16, v205\n"
"v_and_b32 v253, 0xffff0000, v205\nMaaa 8,200,240\nTa 148,17,0\nFM 34,247,252\nMaaa 40,200,244\nD0 4,20,45,2048\nFM 35,247,253\nMaaa 72,200,248\nTa 150,17,2048\nMaaa 104,200,252\n"
"FM 42,251,252\nFM 43,251,253\nMaaa 108,204,252\nTa 152,18,0\nv_lshlrev_b32 v252, 16, v206\nMaaa 76,204,248\nv_and_b32 v253, 0xffff0000, v206\nFM 36,247,252\nMaaa 44,204,244\n"
"Ta 154,18,2048\nFM 37,247,253\nMaaa 12,204,240\nFM 44,251,252\nMvaa 172,204,236\nTa 156,19,0\nFM 45,251,253\nMvaa 140,204,232\nD0 5,20,45,3072\nv_lshlrev_b32 v252, 16, v207\n"
"Mvaa 108,204,228\nTa 158,19,2048\nv_and_b32 v253, 0xffff0000, v207\nMvaa 76,204,224\nFM 38,247,252\nFM 39,247,253\nMvaa 80,208,224\nRa 160,10,0\nFM 46,251,252\nMvaa 112,208,228\n"
"FM 47,251,253\nMvaa 144,208,232\nRa 164,10,2048\nv_lshlrev_b32 v252, 16, v220\nMvaa 176,208,236\ns_add_u32 m0, s48, 36864\ns_add_u32 s46, s45, 0x10000\nMaaa 16,208,240\n"
"Ra 168,10,8192\nv_and_b32 v253, 0xffff0000, v220\nMaaa 48,208,244\nD0 2,20,46,0\nFM 48,247,252\nMaaa 80,208,248\nRa 172,10,10240\nMaaa 112,208,252\nFM 49,247,253\nFM 56,251,252\n"
"Maaa 116,212,252\nRa 176,10,16384\nFM 57,251,253\nMaaa 84,212,248\nv_lshlrev_b32 v252, 16, v221\nv_and_b32 v253, 0xffff0000, v221\nMaaa 52,212,244\nRa 180,10,18432\nFM 50,247,252\n"
"Maaa 20,212,240\nFM 51,247,253\nMvaa 180,212,236\nRa 184,10,24576\nFM 58,251,252\nMvaa 148,212,232\nD0 3,20,46,1024\nFM 59,251,253\nMvaa 116,212,228\nRa 188,10,26624\n"
"v_lshlrev_b32 v252, 16, v222\nMvaa 84,212,224\nv_and_b32 v253, 0xffff0000, v222\nFM 52,247,252\nMvaa 88,216,224\nFM 53,247,253\nFM 60,251,252\nMvaa 120,216,228\nFM 61,251,253\n"
"Mvaa 152,216,232\nv_lshlrev_b32 v252, 16, v223\nv_and_b32 v253, 0xffff0000, v223\nMvaa 184,216,236\nFM 54,247,252\nFM 55,247,253\nMaaa 24,216,240\nFM 62,251,252\nFM 63,251,253\n"
"Maaa 56,216,244\nD0 4,20,46,2048\nv_permlane32_swap_b32 v32, v48\nMaaa 88,216,248\nv_permlane32_swap_b32 v33, v49\nMaaa 120,216,252\nv_permlane32_swap_b32 v34, v50\n"
"v_permlane32_swap_b32 v35, v51\nMaaa 124,220,252\nv_permlane32_swap_b32 v36, v52\nv_permlane32_swap_b32 v37, v53\nMaaa 92,220,248\nv_permlane32_swap_b32 v38, v54\n"
"v_permlane32_swap_b32 v39, v55\nMaaa 60,220,244\nv_permlane32_swap_b32 v40, v56\nv_permlane32_swap_b32 v41, v57\nMaaa 28,220,240\nv_permlane32_swap_b32 v42, v58\nMvaa 188,220,236\n"
"v_permlane32_swap_b32 v43, v59\nv_permlane32_swap_b32 v44, v60\nMvaa 156,220,232\nD0 5,20,46,3072\nv_permlane32_swap_b32 v45, v61\nMvaa 124,220,228\n"
"v_permlane32_swap_b32 v46, v62\nv_permlane32_swap_b32 v47, v63\nMvaa 92,220,224\ns_waitcnt vmcnt(13) lgkmcnt(0)\ns_barrier\nMvaa 64,128,160\nTa 192,12,16384\nAD 32,32,48\n"
"Mvaa 96,128,164\nAD 33,33,49\nMvaa 128,128,168\nTa 194,12,18432\nAD 34,34,50\nMvaa 160,128,172\ns_add_u32 m0, s47, 49152\ns_add_u32 s44, s44, 0x40000\nMaaa 0,128,176\n"
"Ta 196,13,16384\nAD 35,35,51\nMaaa 32,128,180\nD0 6,24,44,0\nAD 36,36,52\nMaaa 64,128,184\nTa 198,13,18432\nMaaa 96,128,188\nAD 37,37,53\nAD 38,38,54\nMaaa 100,132,188\n"
"Ta 200,14,16384\nAD 39,39,55\nMaaa 68,132,184\nAD 40,40,56\nAD 41,41,57\nMaaa 36,132,180\nTa 202,14,18432\nAD 42,42,58\nMaaa 4,132,176\nAD 43,43,59\nMvaa 164,132,172\n"
"Ta 204,15,16384\nAD 44,44,60\nMvaa 132,132,168\nD0 7,24,44,1024\nAD 45,45,61\nMvaa 100,132,164\nTa 206,15,18432\nAD 46,46,62\nMvaa 68,132,160\nAD 47,47,63\nMvaa 72,136,160\n"
"Ta 208,16,16384\nv_permlane16_swap_b32 v32, v40\nMvaa 104,136,164\nv_permlane16_swap_b32 v33, v41\nMvaa 136,136,168\nTa 210,16,18432\nv_permlane16_swap_b32 v34, v42\n"
"Mvaa 168,136,172\nv_permlane16_swap_b32 v35, v43\nv_permlane16_swap_b32 v36, v44\nMaaa 8,136,176\nTa 212,17,16384\nv_permlane16_swap_b32 v37, v45\nMaaa 40,136,180\n"
"D0 8,24,44,2048\nv_permlane16_swap_b32 v38, v46\nMaaa 72,136,184\nTa 214,17,18432\nMaaa 104,136,188\nv_permlane16_swap_b32 v39, v47\nAD 32,32,40\nMaaa 108,140,188\n"
"Ta 216,18,16384\nAD 33,33,41\nMaaa 76,140,184\nAD 34,34,42\nAD 35,35,43\nMaaa 44,140,180\nTa 218,18,18432\nAD 36,36,44\nMaaa 12,140,176\nAD 37,37,45\nMvaa 172,140,172\n"
"Ta 220,19,16384\nAD 38,38,46\nMvaa 140,140,168\nD0 9,24,44,3072\nAD 39,39,47\nMvaa 108,140,164\nTa 222,19,18432\nv_add_f32_dpp v32, v32, v32 row_ror:8 row_mask:0xf bank_mask:0xf\n"
"Mvaa 76,140,160\nv_add_f32_dpp v33, v33, v33 row_ror:8 row_mask:0xf bank_mask:0xf\nv_add_f32_dpp v34, v34, v34 row_ror:8 row_mask:0xf bank_mask:0xf\nMvaa 80,144,160\nRa 224,11,0\n"
"v_add_f32_dpp v35, v35, v35 row_ror:8 row_mask:0xf bank_mask:0xf\nMvaa 112,144,164\nv_add_f32_dpp v36, v36, v36 row_ror:8 row_mask:0xf bank_mask:0xf\nMvaa 144,144,168\n"
"Ra 228,11,2048\nv_add_f32_dpp v37, v37, v37 row_ror:8 row_mask:0xf bank_mask:0xf\nMvaa 176,144,172\ns_add_u32 m0, s47, 0\ns_add_u32 s44, s44, 0x40000\nMaaa 16,144,176\n"
"Ra 232,11,8192\nv_add_f32_dpp v38, v38, v38 row_ror:8 row_mask:0xf bank_mask:0xf\nMaaa 48,144,180\nD0 6,24,44,0\nv_add_f32_dpp v39, v39, v39 row_ror:8 row_mask:0xf bank_mask:0xf\n"
"Maaa 80,144,184\nRa 236,11,10240\nMaaa 112,144,188\nv_add_f32_dpp v32, v32, v32 row_ror:4 row_mask:0xf bank_mask:0xf\n"
"v_add_f32_dpp v33, v33, v33 row_ror:4 row_mask:0xf bank_mask:0xf\nMaaa 116,148,188\nRa 240,11,16384\nv_add_f32_dpp v34, v34, v34 row_ror:4 row_mask:0xf bank_mask:0xf\n"
"Maaa 84,148,184\nv_add_f32_dpp v35, v35, v35 row_ror:4 row_mask:0xf bank_mask:0xf\nv_add_f32_dpp v36, v36, v36 row_ror:4 row_mask:0xf bank_mask:0xf\nMaaa 52,148,180\n"
"Ra 244,11,18432\nv_add_f32_dpp v37, v37, v37 row_ror:4 row_mask:0xf bank_mask:0xf\nMaaa 20,148,176\nv_add_f32_dpp v38, v38, v38 row_ror:4 row_mask:0xf bank_mask:0xf\n"
"Mvaa 180,148,172\nRa 248,11,24576\nv_add_f32_dpp v39, v39, v39 row_ror:4 row_mask:0xf bank_mask:0xf\nMvaa 148,148,168\nD0 7,24,44,1024\n"
"v_add_f32_dpp v32, v32, v32 row_ror:2 row_mask:0xf bank_mask:0xf\nMvaa 116,148,164\nRa 252,11,26624\nv_add_f32_dpp v33, v33, v33 row_ror:2 row_mask:0xf bank_mask:0xf\n"
"Mvaa 84,148,160\nv_add_f32_dpp v34, v34, v34 row_ror:2 row_mask:0xf bank_mask:0xf\nv_add_f32_dpp v35, v35, v35 row_ror:2 row_mask:0xf bank_mask:0xf\nMvaa 88,152,160\n"
"v_add_f32_dpp v36, v36, v36 row_ror:2 row_mask:0xf bank_mask:0xf\nv_add_f32_dpp v37, v37, v37 row_ror:2 row_mask:0xf bank_mask:0xf\nMvaa 120,152,164\n"
"v_add_f32_dpp v38, v38, v38 row_ror:2 row_mask:0xf bank_mask:0xf\nMvaa 152,152,168\nv_add_f32_dpp v39, v39, v39 row_ror:2 row_mask:0xf bank_mask:0xf\nv_lshrrev_b32 v48, 4, v0\n"
"Mvaa 184,152,172\nv_and_b32 v49, 1, v0\nv_lshlrev_b32 v49, 5, v49\nMaaa 24,152,176\nv_lshl_add_u32 v49, v48, 6, v49\ns_lshl_b32 s2, s11, 8\nMaaa 56,152,180\nD0 8,24,44,2048\n"
"s_add_u32 s2, s2, 132096\nMaaa 88,152,184\nv_add_u32 v49, s2, v49\nMaaa 120,152,188\nds_write_b128 v49, v[32:35]\nds_write_b128 v49, v[36:39] offset:16\nMaaa 124,156,188\n"
"v_and_b32 v50, 31, v0\nv_lshlrev_b32 v50, 2, v50\nMaaa 92,156,184\ns_lshl_b32 s2, s11, 10\ns_add_u32 s2, s2, 133120\nMaaa 60,156,180\nv_add_u32 v51, s2, v50\n"
"v_add_u32 v50, 132096, v50\nMaaa 28,156,176\nv_lshl_add_u32 v52, v48, 4, s2\nMvaa 188,156,172\nMvaa 156,156,168\nD0 9,24,44,3072\nMvaa 124,156,164\nMvaa 92,156,160\n"
"s_waitcnt vmcnt(8) lgkmcnt(0)\ns_barrier\nMvaa 64,192,224\nTa 128,12,32768\nds_read_b32 v32, v50 offset:0\nMvaa 96,192,228\nds_read_b32 v33, v50 offset:256\nMvaa 128,192,232\n"
"Ta 130,12,34816\nds_read_b32 v34, v50 offset:512\nMvaa 160,192,236\ns_add_u32 m0, s48, 0\ns_add_u32 s45, s45, 128\nMaaa 0,192,240\nTa 132,13,32768\n"
"ds_read_b32 v35, v50 offset:768\nMaaa 32,192,244\nD0 2,20,45,0\nMaaa 64,192,248\nTa 134,13,34816\nds_read_b32 v36, v50 offset:128\nMaaa 96,192,252\n"
"ds_read_b32 v37, v50 offset:384\nMaaa 100,196,252\nTa 136,14,32768\nds_read_b32 v38, v50 offset:640\nMaaa 68,196,248\nds_read_b32 v39, v50 offset:896\nMaaa 36,196,244\n"
"Ta 138,14,34816\nMaaa 4,196,240\nMvaa 164,196,236\nTa 140,15,32768\nMvaa 132,196,232\nD0 3,20,45,1024\nMvaa 100,196,228\nTa 142,15,34816\nMvaa 68,196,224\nMvaa 72,200,224\n"
"Ta 144,16,32768\nMvaa 104,200,228\nMvaa 136,200,232\nTa 146,16,34816\nMvaa 168,200,236\nWL 5\nMaaa 8,200,240\nTa 148,17,32768\nAD 32,32,33\nMaaa 40,200,244\nD0 4,20,45,2048\n"
"Maaa 72,200,248\nTa 150,17,34816\nAD 34,34,35\nMaaa 104,200,252\nAD 36,36,37\nMaaa 108,204,252\nTa 152,18,32768\nAD 38,38,39\nMaaa 76,204,248\nAD 32,32,34\nMaaa 44,204,244\n"
"Ta 154,18,34816\nAD 36,36,38\nMaaa 12,204,240\nCV 32,32,36\nMvaa 172,204,236\nTa 156,19,32768\nv_and_b32 v36, 0xffff0000, v32\nMvaa 140,204,232\nD0 5,20,45,3072\nMvaa 108,204,228\n"
"Ta 158,19,34816\nv_lshlrev_b32 v32, 16, v32\nMvaa 76,204,224\nv_mul_f32 v33, 0xbfb8aa3b, v32\nEX 33,33\nMvaa 80,208,224\nRa 160,10,32768\nv_add_f32 v33, 1.0, v33\n"
"Mvaa 112,208,228\nv_rcp_f32 v33, v33\nMvaa 144,208,232\nRa 164,10,34816\nMU 33,32,33\nMvaa 176,208,236\ns_add_u32 m0, s48, 4096\ns_add_u32 s46, s45, 0x10000\nMaaa 16,208,240\n"
"Ra 168,10,40960\nCV 33,33,33\nMaaa 48,208,244\nD0 2,20,46,0\nMaaa 80,208,248\nRa 172,10,43008\nv_lshlrev_b32 v33, 16, v33\nMaaa 112,208,252\nMU 33,33,36\nMaaa 116,212,252\n"
"Ra 176,10,49152\nCV 33,33,33\nMaaa 84,212,248\nv_lshlrev_b32 v33, 16, v33\nMaaa 52,212,244\nRa 180,10,51200\nds_write_b32 v51, v33\nMaaa 20,212,240\nRv 244,52,0\nMvaa 180,212,236\n"
"Ra 184,10,57344\nRv 248,52,64\nMvaa 148,212,232\nD0 3,20,46,1024\nMvaa 116,212,228\nRa 188,10,59392\ns_and_b32 s2, s12, 1\nMvaa 84,212,224\ns_and_b32 s3, s13, 6\nMvaa 88,216,224\n"
"s_add_u32 s2, s2, s3\ns_lshl3_add_u32 s2, s14, s2\nMvaa 120,216,228\ns_cmp_lt_u32 s2, 14\ns_cselect_b32 s5, 0, 0x40000000\nMvaa 152,216,232\ns_mul_i32 s4, s73, 14\n"
"s_add_u32 s4, s4, s2\nMvaa 184,216,236\nv_and_b32 v49, 15, v0\nMaaa 24,216,240\nv_lshlrev_b32 v50, 3, v49\nv_and_b32 v51, 1, v48\nMaaa 56,216,244\nD0 4,20,46,2048\n"
"Maaa 88,216,248\nv_lshl_add_u32 v50, v51, 2, v50\ns_mul_i32 s2, s11, 112\nMaaa 120,216,252\nv_add_u32 v50, s2, v50\nMaaa 124,220,252\nv_lshrrev_b32 v51, 4, v50\n"
"v_lshrrev_b32 v48, 1, v48\nMaaa 92,220,248\nv_mad_u32_u24 v51, v48, 28, v51\nMaaa 60,220,244\nv_mul_u32_u24 v51, 7168, v51\nv_and_b32 v50, 15, v50\nMaaa 28,220,240\n"
"v_lshl_add_u32 v51, v50, 2, v51\nv_cmp_gt_u32 vcc, 14, v49\ns_lshl_b32 s4, s4, 6\ns_add_u32 s4, s4, 0xc04c800\nv_cndmask_b32 v51, v254, v51, vcc\nMvaa 188,220,236\n"
"s_or_b32 s4, s4, s5\nMvaa 156,220,232\nD0 5,20,46,3072\nMvaa 124,220,228\nv_lshlrev_b32 v252, 16, v224\nv_and_b32 v253, 0xffff0000, v224\nMvaa 92,220,224\n"
"s_waitcnt vmcnt(12) lgkmcnt(0)\ns_barrier\nMvaa 64,128,160\nTa 192,12,49152\nMU 32,244,252\nMvaa 96,128,164\nMU 33,244,253\nMvaa 128,128,168\nTa 194,12,51200\nMU 40,248,252\n"
"Mvaa 160,128,172\ns_add_u32 m0, s47, 16384\ns_add_u32 s44, s44, 0x40000\nMaaa 0,128,176\nTa 196,13,49152\nMU 41,248,253\nMaaa 32,128,180\nD0 6,24,44,0\nMaaa 64,128,184\n"
"Ta 198,13,51200\nv_lshlrev_b32 v252, 16, v225\nMaaa 96,128,188\nv_and_b32 v253, 0xffff0000, v225\nMaaa 100,132,188\nTa 200,14,49152\nMU 34,244,252\nMaaa 68,132,184\nMU 35,244,253\n"
"Maaa 36,132,180\nTa 202,14,51200\nMU 42,248,252\nMaaa 4,132,176\nMU 43,248,253\nMvaa 164,132,172\nTa 204,15,49152\nv_lshlrev_b32 v252, 16, v226\nMvaa 132,132,168\nD0 7,24,44,1024\n"
"Mvaa 100,132,164\nTa 206,15,51200\nv_and_b32 v253, 0xffff0000, v226\nMvaa 68,132,160\nMU 36,244,252\nMvaa 72,136,160\nTa 208,16,49152\nMU 37,244,253\nMvaa 104,136,164\n"
"MU 44,248,252\nMvaa 136,136,168\nTa 210,16,51200\nMU 45,248,253\nMvaa 168,136,172\nv_lshlrev_b32 v252, 16, v227\nMaaa 8,136,176\nTa 212,17,49152\nv_and_b32 v253, 0xffff0000, v227\n"
"Maaa 40,136,180\nD0 8,24,44,2048\nMaaa 72,136,184\nTa 214,17,51200\nMU 38,244,252\nMaaa 104,136,188\nMU 39,244,253\nMaaa 108,140,188\nTa 216,18,49152\nMU 46,248,252\n"
"Maaa 76,140,184\nMU 47,248,253\nMaaa 44,140,180\nTa 218,18,51200\nv_lshlrev_b32 v252, 16, v228\nMaaa 12,140,176\nv_and_b32 v253, 0xffff0000, v228\nMvaa 172,140,172\n"
"Ta 220,19,49152\nFM 32,245,252\nMvaa 140,140,168\nD0 9,24,44,3072\nMvaa 108,140,164\nTa 222,19,51200\nFM 33,245,253\nMvaa 76,140,160\nFM 40,249,252\nMvaa 80,144,160\n"
"Ra 224,11,32768\nFM 41,249,253\nMvaa 112,144,164\nv_lshlrev_b32 v252, 16, v229\nMvaa 144,144,168\nRa 228,11,34816\nv_and_b32 v253, 0xffff0000, v229\nMvaa 176,144,172\n"
"s_add_u32 m0, s47, 32768\ns_add_u32 s44, s44, 0x40000\nMaaa 16,144,176\nRa 232,11,40960\nFM 34,245,252\nMaaa 48,144,180\nD0 6,24,44,0\nMaaa 80,144,184\nRa 236,11,43008\n"
"FM 35,245,253\nMaaa 112,144,188\nFM 42,249,252\nMaaa 116,148,188\nRa 240,11,49152\nFM 43,249,253\nMaaa 84,148,184\nv_lshlrev_b32 v252, 16, v230\nMaaa 52,148,180\nRa 244,11,51200\n"
"v_and_b32 v253, 0xffff0000, v230\nMaaa 20,148,176\nFM 36,245,252\nMvaa 180,148,172\nRa 248,11,57344\nFM 37,245,253\nMvaa 148,148,168\nD0 7,24,44,1024\nMvaa 116,148,164\n"
"Ra 252,11,59392\nFM 44,249,252\nMvaa 84,148,160\nFM 45,249,253\nMvaa 88,152,160\nv_lshlrev_b32 v252, 16, v231\nv_and_b32 v253, 0xffff0000, v231\nMvaa 120,152,164\nFM 38,245,252\n"
"Mvaa 152,152,168\nFM 39,245,253\nFM 46,249,252\nMvaa 184,152,172\nFM 47,249,253\nMaaa 24,152,176\nv_lshlrev_b32 v252, 16, v232\nv_and_b32 v253, 0xffff0000, v232\nMaaa 56,152,180\n"
"D0 8,24,44,2048\nMaaa 88,152,184\nFM 32,246,252\nFM 33,246,253\nMaaa 120,152,188\nFM 40,250,252\nMaaa 124,156,188\nFM 41,250,253\nv_lshlrev_b32 v252, 16, v233\nMaaa 92,156,184\n"
"v_and_b32 v253, 0xffff0000, v233\nMaaa 60,156,180\nFM 34,246,252\nFM 35,246,253\nMaaa 28,156,176\nFM 42,250,252\nMvaa 188,156,172\nFM 43,250,253\nv_lshlrev_b32 v252, 16, v234\n"
"Mvaa 156,156,168\nD0 9,24,44,3072\nMvaa 124,156,164\nv_and_b32 v253, 0xffff0000, v234\nFM 36,246,252\nMvaa 92,156,160\ns_waitcnt vmcnt(8) lgkmcnt(0)\ns_barrier\nMvaa 64,192,224\n"
"Ta 128,12,0\nFM 37,246,253\nMvaa 96,192,228\nFM 44,250,252\nMvaa 128,192,232\nTa 130,12,2048\nFM 45,250,253\nMvaa 160,192,236\ns_add_u32 m0, s48, 32768\ns_add_u32 s45, s45, 128\n"
"Maaa 0,192,240\nTa 132,13,0\nv_lshlrev_b32 v252, 16, v235\nMaaa 32,192,244\nD0 2,20,45,0\nMaaa 64,192,248\nTa 134,13,2048\nv_and_b32 v253, 0xffff0000, v235\nMaaa 96,192,252\n"
"FM 38,246,252\nMaaa 100,196,252\nTa 136,14,0\nFM 39,246,253\nMaaa 68,196,248\nFM 46,250,252\nMaaa 36,196,244\nTa 138,14,2048\nFM 47,250,253\nMaaa 4,196,240\n"
"v_lshlrev_b32 v252, 16, v236\nMvaa 164,196,236\nTa 140,15,0\nv_and_b32 v253, 0xffff0000, v236\nMvaa 132,196,232\nD0 3,20,45,1024\nMvaa 100,196,228\nTa 142,15,2048\nFM 32,247,252\n"
"Mvaa 68,196,224\nFM 33,247,253\nMvaa 72,200,224\nTa 144,16,0\nFM 40,251,252\nMvaa 104,200,228\nFM 41,251,253\nMvaa 136,200,232\nTa 146,16,2048\nv_lshlrev_b32 v252, 16, v237\n"
"Mvaa 168,200,236\nv_and_b32 v253, 0xffff0000, v237\nMaaa 8,200,240\nTa 148,17,0\nFM 34,247,252\nMaaa 40,200,244\nD0 4,20,45,2048\nMaaa 72,200,248\nTa 150,17,2048\nFM 35,247,253\n"
"Maaa 104,200,252\nFM 42,251,252\nMaaa 108,204,252\nTa 152,18,0\nFM 43,251,253\nMaaa 76,204,248\nv_lshlrev_b32 v252, 16, v238\nMaaa 44,204,244\nTa 154,18,2048\n"
"v_and_b32 v253, 0xffff0000, v238\nMaaa 12,204,240\nFM 36,247,252\nMvaa 172,204,236\nTa 156,19,0\nFM 37,247,253\nMvaa 140,204,232\nD0 5,20,45,3072\nMvaa 108,204,228\n"
"Ta 158,19,2048\nFM 44,251,252\nMvaa 76,204,224\nFM 45,251,253\nMvaa 80,208,224\nRa 160,10,0\nv_lshlrev_b32 v252, 16, v239\nMvaa 112,208,228\nv_and_b32 v253, 0xffff0000, v239\n"
"Mvaa 144,208,232\nRa 164,10,2048\nFM 38,247,252\nMvaa 176,208,236\ns_add_u32 m0, s48, 36864\ns_add_u32 s46, s45, 0x10000\nMaaa 16,208,240\nRa 168,10,8192\nFM 39,247,253\n"
"Maaa 48,208,244\nD0 2,20,46,0\nMaaa 80,208,248\nRa 172,10,10240\nFM 46,251,252\nMaaa 112,208,252\nFM 47,251,253\nMaaa 116,212,252\nRa 176,10,16384\nv_permlane32_swap_b32 v32, v40\n"
"Maaa 84,212,248\nv_permlane32_swap_b32 v33, v41\nMaaa 52,212,244\nRa 180,10,18432\nv_permlane32_swap_b32 v34, v42\nMaaa 20,212,240\nv_permlane32_swap_b32 v35, v43\n"
"Mvaa 180,212,236\nRa 184,10,24576\nv_permlane32_swap_b32 v36, v44\nMvaa 148,212,232\nD0 3,20,46,1024\nMvaa 116,212,228\nRa 188,10,26624\nv_permlane32_swap_b32 v37, v45\n"
"Mvaa 84,212,224\nv_permlane32_swap_b32 v38, v46\nMvaa 88,216,224\nv_permlane32_swap_b32 v39, v47\nAD 32,32,40\nMvaa 120,216,228\nAD 33,33,41\nMvaa 152,216,232\nAD 34,34,42\n"
"AD 35,35,43\nMvaa 184,216,236\nAD 36,36,44\nMaaa 24,216,240\nAD 37,37,45\nAD 38,38,46\nMaaa 56,216,244\nD0 4,20,46,2048\nMaaa 88,216,248\nAD 39,39,47\nMaaa 120,216,252\n"
"v_permlane16_swap_b32 v32, v36\nMaaa 124,220,252\nv_permlane16_swap_b32 v33, v37\nv_permlane16_swap_b32 v34, v38\nMaaa 92,220,248\nv_permlane16_swap_b32 v35, v39\nMaaa 60,220,244\n"
"AD 32,32,36\nAD 33,33,37\nMaaa 28,220,240\nAD 34,34,38\nMvaa 188,220,236\nAD 35,35,39\nSv 32,51,16,4,0\nMvaa 156,220,232\nD0 5,20,46,3072\nMvaa 124,220,228\nMvaa 92,220,224\n"
"s_waitcnt vmcnt(13) lgkmcnt(0)\ns_barrier\nMvaa 64,128,160\nTa 192,12,16384\nv_lshrrev_b32 v48, 4, v0\nMvaa 96,128,164\nv_and_b32 v49, 7, v0\ns_add_u32 m0, s47, 49152\n"
"s_add_u32 s44, s44, 0x40000\nMvaa 128,128,168\nTa 194,12,18432\nMvaa 160,128,172\nv_xor_b32 v49, v49, v48\nD0 6,24,44,0\nMaaa 0,128,176\nTa 196,13,16384\n"
"v_lshlrev_b32 v49, 4, v49\nMaaa 32,128,180\nv_lshrrev_b32 v48, 3, v0\nMaaa 64,128,184\nTa 198,13,18432\nMaaa 96,128,188\nv_mul_u32_u24 v48, 896, v48\nMaaa 100,132,188\n"
"Ta 200,14,16384\nv_add_u32 v40, v48, v49\nD0 7,24,44,1024\nMaaa 68,132,184\nv_xor_b32 v49, 64, v40\nMaaa 36,132,180\nTa 202,14,18432\nMaaa 4,132,176\nv_add_u32 v41, 6144, v49\n"
"Mvaa 164,132,172\nTa 204,15,16384\nv_add_u32 v42, 12288, v40\nMvaa 132,132,168\nv_add_u32 v43, 18432, v49\nD0 8,24,44,2048\nMvaa 100,132,164\nTa 206,15,18432\nMvaa 68,132,160\n"
"s_mov_b32 s2, s13\nMvaa 72,136,160\nTa 208,16,16384\ns_lshl2_add_u32 s2, s2, s11\nMvaa 104,136,164\ns_mul_i32 s52, s2, 28672\nMvaa 136,136,168\nTa 210,16,18432\nD0 9,24,44,3072\n"
"Mvaa 168,136,172\ns_lshr_b32 s2, s13, 2\nMaaa 8,136,176\nTa 212,17,16384\ns_bfe_u32 s3, s13, 0x10001\nMaaa 40,136,180\ns_mul_i32 s3, s3, 17\ns_add_u32 m0, s47, 65536\n"
"s_add_u32 s46, s51, 0\nMaaa 72,136,184\nTa 214,17,18432\nMaaa 104,136,188\ns_add_u32 s2, s2, s3\nD0 6,32,46,0\nMaaa 108,140,188\nTa 216,18,16384\ns_lshl2_add_u32 s3, s14, 0\n"
"Maaa 76,140,184\ns_lshl_b32 s3, s3, 2\nMaaa 44,140,180\nTa 218,18,18432\nMaaa 12,140,176\ns_lshr_b32 s3, 0xe420ca86, s3\nMvaa 172,140,172\nTa 220,19,16384\ns_and_b32 s3, s3, 15\n"
"D0 7,32,46,1024\nMvaa 140,140,168\ns_add_u32 s2, s2, s3\ns_cmp_eq_u32 s2, 32\ns_cselect_b32 s4, 16, s2\nMvaa 108,140,164\nTa 222,19,18432\nMvaa 76,140,160\n"
"s_mul_i32 s75, s4, 65536\nMvaa 80,144,160\nRa 224,11,0\ns_lshr_b32 s2, s13, 2\nMvaa 112,144,164\ns_bfe_u32 s3, s13, 0x10001\nD0 8,32,46,2048\nMvaa 144,144,168\nRa 228,11,2048\n"
"Mvaa 176,144,172\ns_mul_i32 s3, s3, 17\nMaaa 16,144,176\nRa 232,11,8192\ns_add_u32 s2, s2, s3\nMaaa 48,144,180\ns_lshl2_add_u32 s3, s14, 1\nMaaa 80,144,184\nRa 236,11,10240\n"
"D0 9,32,46,3072\nMaaa 112,144,188\ns_lshl_b32 s3, s3, 2\nMaaa 116,148,188\nRa 240,11,16384\ns_lshr_b32 s3, 0xe420ca86, s3\nMaaa 84,148,184\ns_and_b32 s3, s3, 15\n"
"s_add_u32 m0, s47, 81920\ns_add_u32 s46, s51, 262144\nMaaa 52,148,180\nRa 244,11,18432\nMaaa 20,148,176\ns_add_u32 s2, s2, s3\ns_cmp_eq_u32 s2, 32\ns_cselect_b32 s4, 16, s2\n"
"D0 6,32,46,0\nMvaa 180,148,172\nRa 248,11,24576\ns_mul_i32 s76, s4, 65536\nMvaa 148,148,168\ns_lshr_b32 s2, s13, 2\nMvaa 116,148,164\nRa 252,11,26624\nMvaa 84,148,160\n"
"s_bfe_u32 s3, s13, 0x10001\nMvaa 88,152,160\ns_mul_i32 s3, s3, 17\ns_add_u32 s2, s2, s3\nD0 7,32,46,1024\nMvaa 120,152,164\ns_lshl2_add_u32 s3, s14, 2\nMvaa 152,152,168\n"
"s_lshl_b32 s3, s3, 2\nMvaa 184,152,172\ns_lshr_b32 s3, 0xe420ca86, s3\nMaaa 24,152,176\ns_and_b32 s3, s3, 15\ns_add_u32 s2, s2, s3\nMaaa 56,152,180\ns_cmp_eq_u32 s2, 32\n"
"s_cselect_b32 s4, 16, s2\nD0 8,32,46,2048\nMaaa 88,152,184\ns_mul_i32 s77, s4, 65536\nMaaa 120,152,188\ns_lshr_b32 s2, s13, 2\nMaaa 124,156,188\ns_bfe_u32 s3, s13, 0x10001\n"
"s_mul_i32 s3, s3, 17\nMaaa 92,156,184\ns_add_u32 s2, s2, s3\nMaaa 60,156,180\ns_lshl2_add_u32 s3, s14, 3\nD0 9,32,46,3072\nMaaa 28,156,176\ns_lshl_b32 s3, s3, 2\nMvaa 188,156,172\n"
"s_lshr_b32 s3, 0xe420ca86, s3\ns_and_b32 s3, s3, 15\nMvaa 156,156,168\ns_add_u32 s2, s2, s3\ns_cmp_eq_u32 s2, 32\ns_cselect_b32 s4, 16, s2\nMvaa 124,156,164\n"
"s_mul_i32 s78, s4, 65536\nMvaa 92,156,160\ns_waitcnt vmcnt(12) lgkmcnt(0)\ns_barrier\nMvaa 64,192,224\nTa 128,12,32768\ns_lshr_b32 s3, s11, 1\nMvaa 96,192,228\n"
"s_lshl_b32 s3, s3, 15\ns_add_u32 m0, s47, -65536\ns_add_u32 s46, s52, 0\nMvaa 128,192,232\nTa 130,12,34816\nMvaa 160,192,236\ns_add_u32 s2, s72, s3\nD0 40,28,46,0\nMaaa 0,192,240\n"
"Ta 132,13,32768\ns_and_b32 s3, s11, 1\nMaaa 32,192,244\ns_lshl_b32 s3, s3, 12\nMaaa 64,192,248\nTa 134,13,34816\nMaaa 96,192,252\ns_add_u32 s2, s2, s3\nMaaa 100,196,252\n"
"Ta 136,14,32768\ns_add_u32 s2, s2, 0x0\nD0 41,28,46,1024\nMaaa 68,196,248\ns_mov_b32 s55, 16384\nMaaa 36,196,244\nTa 138,14,34816\nMaaa 4,196,240\ns_add_u32 s75, s75, s2\n"
"Mvaa 164,196,236\nTa 140,15,32768\ns_add_u32 s76, s76, s2\nMvaa 132,196,232\ns_add_u32 s77, s77, s2\nD0 42,28,46,2048\nMvaa 100,196,228\nTa 142,15,34816\nMvaa 68,196,224\n"
"s_add_u32 s78, s78, s2\nMvaa 72,200,224\nTa 144,16,32768\nv_add_u32 v32, 0x10000, v12\nMvaa 104,200,228\nv_add_u32 v33, 0x10000, v13\nMvaa 136,200,232\nTa 146,16,34816\n"
"D0 43,28,46,3072\nMvaa 168,200,236\nv_add_u32 v34, 0x10000, v14\nMaaa 8,200,240\nTa 148,17,32768\nv_add_u32 v35, 0x10000, v15\nMaaa 40,200,244\nv_add_u32 v36, 0x10000, v16\n"
"s_add_u32 m0, s47, 0\ns_add_u32 s46, s51, 524288\nMaaa 72,200,248\nTa 150,17,34816\nMaaa 104,200,252\nv_add_u32 v37, 0x10000, v17\nD0 6,32,46,0\nMaaa 108,204,252\nTa 152,18,32768\n"
"v_add_u32 v38, 0x10000, v18\nMaaa 76,204,248\nv_add_u32 v39, 0x10000, v19\nMaaa 44,204,244\nTa 154,18,34816\nMaaa 12,204,240\ns_add_u32 s2, s11, 1\nMvaa 172,204,236\n"
"Ta 156,19,32768\ns_lshr_b32 s2, s2, 1\nD0 7,32,46,1024\nMvaa 140,204,232\ns_lshl_b32 s2, s2, 12\nMvaa 108,204,228\nTa 158,19,34816\nMvaa 76,204,224\nv_add_u32 v44, s2, v10\n"
"Mvaa 80,208,224\nRa 160,10,32768\nv_add_u32 v45, s2, v11\nMvaa 112,208,228\nv_xor_b32 v46, 0x1000, v44\nD0 8,32,46,2048\nMvaa 144,208,232\nRa 164,10,34816\nMvaa 176,208,236\n"
"v_xor_b32 v47, 0x1000, v45\nMaaa 16,208,240\nRa 168,10,40960\ns_lshr_b32 s3, s11, 1\nMaaa 48,208,244\ns_lshl_b32 s3, s3, 13\nMaaa 80,208,248\nRa 172,10,43008\nD0 9,32,46,3072\n"
"Maaa 112,208,252\ns_lshl_b32 s2, s11, 12\nMaaa 116,212,252\nRa 176,10,49152\ns_add_u32 s2, s2, s3\nMaaa 84,212,248\ns_add_u32 s2, s2, s72\ns_add_u32 m0, s47, 16384\n"
"s_add_u32 s46, s51, 786432\nMaaa 52,212,244\nRa 180,10,51200\nMaaa 20,212,240\ns_lshl_b32 s3, s11, 12\nD0 6,32,46,0\nMvaa 180,212,236\nRa 184,10,57344\ns_add_u32 s89, s3, 0x10000\n"
"Mvaa 148,212,232\ns_mov_b32 s88, 0xffff8000\nMvaa 116,212,228\nRa 188,10,59392\nMvaa 84,212,224\ns_lshl_b32 s4, s13, 1\nMvaa 88,216,224\ns_sub_u32 s79, 15, s4\n"
"s_and_b32 s5, s12, 1\nD0 7,32,46,1024\nMvaa 120,216,228\ns_mul_i32 s5, s5, 229376\nMvaa 152,216,232\ns_add_u32 s5, s5, s2\nMvaa 184,216,236\ns_add_u32 s96, s5, 0x1310000\n"
"Maaa 24,216,240\ns_mov_b32 s63, 0x53800000\ns_mov_b32 s64, 0x2b800000\nMaaa 56,216,244\ns_mov_b32 s98, s4\nD0 8,32,46,2048\nMaaa 88,216,248\ns_lshl_b32 s5, s13, 5\n"
"Maaa 120,216,252\ns_add_u32 s5, s5, s11\nMaaa 124,220,252\ns_lshl_b32 s65, s5, 9\ns_add_u32 s65, s65, s72\nMaaa 92,220,248\ns_add_u32 s65, s65, 0x1580000\nMaaa 60,220,244\n"
"s_and_b32 s66, s72, 0x2000\nD0 9,32,46,3072\nMaaa 28,220,240\ns_add_u32 s66, s66, s56\nMvaa 188,220,236\ns_add_u32 s66, s66, 0x100000\ns_and_b32 s5, s12, 1\nMvaa 156,220,232\n"
"s_mul_i32 s58, s5, 0x202000\nMvaa 124,220,228\ns_xor_b32 s58, s58, s72\nMvaa 92,220,224\ns_waitcnt vmcnt(20) lgkmcnt(0)\ns_barrier\nMvaa 64,128,160\nTa 192,12,49152\n"
"s_add_u32 s58, s58, 0x15c0000\nMvaa 96,128,164\ns_lshl_b32 s5, s5, 8\ns_add_u32 m0, s47, -49152\ns_add_u32 s46, s52, 256\nMvaa 128,128,168\nTa 194,12,51200\nMvaa 160,128,172\n"
"s_add_u32 s61, s58, s5\nD0 40,28,46,0\nMaaa 0,128,176\nTa 196,13,49152\ns_lshl4_add_u32 s60, s13, s61\nMaaa 32,128,180\ns_add_u32 s54, s60, 128\nMaaa 64,128,184\nTa 198,13,51200\n"
"Maaa 96,128,188\ns_xor_b32 s60, s54, 0x100\ns_cmp_eq_u32 s11, 0\ns_cselect_b32 s54, s54, 0x40000000\nMaaa 100,132,188\nTa 200,14,49152\ns_getpc_b64 s[8:9]\n.PC216:\n"
"s_add_u32 s8, s8, .HD208-.PC216\ns_addc_u32 s9, s9, 0\nD0 41,28,46,1024\nMaaa 68,132,184\ns_mov_b32 s97, 0xff800000\nMaaa 36,132,180\nTa 202,14,51200\nMaaa 4,132,176\n"
"s_mov_b32 s90, 1\nMvaa 164,132,172\nTa 204,15,49152\ns_lshl_b32 s5, s11, 2\nMvaa 132,132,168\ns_lshr_b32 s92, 0xf112, s5\nD0 42,28,46,2048\nMvaa 100,132,164\nTa 206,15,51200\n"
"Mvaa 68,132,160\ns_and_b32 s92, s92, 15\nMvaa 72,136,160\nTa 208,16,49152\ns_lshr_b32 s93, s11, 1\nMvaa 104,136,164\ns_xor_b32 s93, s93, 1\nMvaa 136,136,168\nTa 210,16,51200\n"
"D0 43,28,46,3072\nMvaa 168,136,172\ns_mul_i32 s4, s79, 65536\nMaaa 8,136,176\nTa 212,17,49152\ns_sub_u32 s4, s2, s4\nMaaa 40,136,180\ns_add_u32 s94, s4, 0x20000\n"
"s_add_u32 m0, s47, 32768\ns_add_u32 s46, s51, 1048576\nMaaa 72,136,184\nTa 214,17,51200\nMaaa 104,136,188\ns_add_u32 s95, s4, 0x410000\nD0 6,32,46,0\nMaaa 108,140,188\n"
"Ta 216,18,49152\ns_add_u32 s86, s2, 0x130000\nMaaa 76,140,184\ns_add_u32 s87, s2, 0x520000\nMaaa 44,140,180\nTa 218,18,51200\nMaaa 12,140,176\ns_sub_u32 s99, s79, 1\n"
"s_cmp_eq_u32 s11, 3\ns_cselect_b32 s92, 100, s92\nMvaa 172,140,172\nTa 220,19,49152\ns_mov_b32 s36, s68\nD0 7,32,46,1024\nMvaa 140,140,168\ns_and_b32 s37, s69, 0xffff\n"
"Mvaa 108,140,164\nTa 222,19,51200\nMvaa 76,140,160\ns_mov_b32 s38, 0x1c0000\nMvaa 80,144,160\nRa 224,11,32768\ns_mov_b32 s39, 0x20000\nMvaa 112,144,164\ns_and_b32 s2, s74, 15\n"
"D0 8,32,46,2048\nMvaa 144,144,168\nRa 228,11,34816\nMvaa 176,144,172\ns_mul_i32 s3, s73, 14\nMaaa 16,144,176\nRa 232,11,40960\ns_add_u32 s2, s2, s3\nMaaa 48,144,180\n"
"s_lshl2_add_u32 s2, s2, s11\nMaaa 80,144,184\nRa 236,11,43008\nD0 9,32,46,3072\nMaaa 112,144,188\ns_mul_i32 s3, s2, 4682\nMaaa 116,148,188\nRa 240,11,49152\ns_lshr_b32 s3, s3, 16\n"
"Maaa 84,148,184\ns_mul_i32 s22, s3, 14\nMaaa 52,148,180\nRa 244,11,51200\nMaaa 20,148,176\ns_sub_u32 s22, s2, s22\nMvaa 180,148,172\nRa 248,11,57344\ns_lshl_b32 s21, s3, 5\n"
"Mvaa 148,148,168\ns_bfe_u32 s41, s3, 0x30002\ns_cmp_gt_u32 s22, 6\ns_cselect_b32 s2, 1187840, 0\ns_cselect_b32 s1, 16, 0\nMvaa 116,148,164\nRa 252,11,59392\nMvaa 84,148,160\n"
"s_add_u32 s41, s41, s1\nMvaa 88,152,160\ns_lshl4_add_u32 s41, s41, 0x15c0280\ns_and_b32 s1, s3, 28\nMvaa 120,152,164\ns_add_u32 s3, s3, s1\nMvaa 152,152,168\n"
"s_lshl_b32 s3, s3, 11\nMvaa 184,152,172\ns_add_u32 s2, s2, s3\nMaaa 24,152,176\ns_lshl_b32 s3, s22, 17\ns_add_u32 s2, s2, s3\nMaaa 56,152,180\ns_add_u32 s20, s2, 0x1100000\n"
"Maaa 88,152,184\ns_lshl_b32 s2, s22, 7\nMaaa 120,152,188\ns_add_u32 s40, s2, 0xc04b800\nMaaa 124,156,188\ns_mul_i32 s2, s21, 896\ns_lshl_b32 s3, s22, 6\nMaaa 92,156,184\n"
"s_add_u32 s22, s2, s3\nMaaa 60,156,180\ns_add_u32 s23, s22, 14336\nMaaa 28,156,176\nMvaa 188,156,172\nMvaa 156,156,168\nMvaa 124,156,164\nMvaa 92,156,160\n"
"s_waitcnt vmcnt(16) lgkmcnt(0)\ns_barrier\nMvaa 64,192,224\ns_add_u32 m0, s47, -16384\ns_add_u32 s46, s52, 128\nMvaa 68,196,224\nMvaa 72,200,224\nD0 40,28,46,0\nMvaa 76,204,224\n"
"Mvaa 80,208,224\nMvaa 84,212,224\nMvaa 88,216,224\nD0 41,28,46,1024\nCV 192,64,65\nMvaa 92,220,224\nCV 193,66,67\nCV 194,68,69\nMvaa 96,192,228\nCV 195,70,71\nCV 196,72,73\n"
"Mvaa 100,196,228\nCV 197,74,75\nMvaa 104,200,228\nD0 42,28,46,2048\nCV 198,76,77\nMvaa 108,204,228\nCV 199,78,79\nCV 200,80,81\nMvaa 112,208,228\nCV 201,82,83\nCV 202,84,85\n"
"Mvaa 116,212,228\nCV 203,86,87\nMvaa 120,216,228\nD0 43,28,46,3072\nCV 204,88,89\nMvaa 124,220,228\nCV 205,90,91\nCV 206,92,93\nMvaa 128,192,232\ns_add_u32 m0, s47, 49152\n"
"s_add_u32 s46, s51, 1310720\nMvaa 132,196,232\nCV 207,94,95\nMvaa 136,200,232\nD0 6,32,46,0\nCV 208,96,97\nMvaa 140,204,232\nCV 209,98,99\nCV 210,100,101\nMvaa 144,208,232\n"
"CV 211,102,103\nCV 212,104,105\nMvaa 148,212,232\nCV 213,106,107\nMvaa 152,216,232\nD0 7,32,46,1024\nCV 214,108,109\nMvaa 156,220,232\nCV 215,110,111\nCV 216,112,113\n"
"Mvaa 160,192,236\nCV 217,114,115\nCV 218,116,117\nMvaa 164,196,236\nCV 219,118,119\nMvaa 168,200,236\nD0 8,32,46,2048\nCV 220,120,121\nMvaa 172,204,236\nCV 221,122,123\n"
"CV 222,124,125\nMvaa 176,208,236\nCV 223,126,127\nCV 224,128,129\nMvaa 180,212,236\nCV 225,130,131\nMvaa 184,216,236\nD0 9,32,46,3072\nCV 226,132,133\nMvaa 188,220,236\n"
"CV 227,134,135\nCV 228,136,137\nMaaa 0,192,240\ns_add_u32 m0, s47, -32768\ns_add_u32 s46, s52, 384\nMaaa 4,196,240\nCV 229,138,139\nMaaa 8,200,240\nD0 40,28,46,0\nCV 230,140,141\n"
"Maaa 12,204,240\nCV 231,142,143\nCV 232,144,145\nMaaa 16,208,240\nRa 160,44,0\nCV 233,146,147\nMaaa 20,212,240\nRa 164,44,2048\nMaaa 24,216,240\nD0 41,28,46,1024\nRa 168,46,0\n"
"Maaa 28,220,240\nRa 172,46,2048\nCV 234,148,149\nMaaa 32,192,244\nTa 128,32,0\nCV 235,150,151\nMaaa 36,196,244\nTa 130,32,2048\nMaaa 40,200,244\nD0 42,28,46,2048\nTa 132,33,0\n"
"Maaa 44,204,244\nTa 134,33,2048\nCV 236,152,153\nMaaa 48,208,244\nTa 136,34,0\nCV 237,154,155\nMaaa 52,212,244\nTa 138,34,2048\nMaaa 56,216,244\nD0 43,28,46,3072\nTa 140,35,0\n"
"Maaa 60,220,244\nTa 142,35,2048\nCV 238,156,157\nMaaa 64,192,248\nTa 144,36,0\nCV 239,158,159\nMaaa 68,196,248\nTa 146,36,2048\nMaaa 72,200,248\nTa 148,37,0\nCV 240,160,161\n"
"Sv 192,1,16,75,0\nMaaa 76,204,248\nTa 150,37,2048\nCV 241,162,163\nMaaa 80,208,248\nTa 152,38,0\nCV 242,164,165\nMaaa 84,212,248\nTa 154,38,2048\nMaaa 88,216,248\nTa 156,39,0\n"
"CV 243,166,167\nMaaa 92,220,248\nTa 158,39,2048\nCV 244,168,169\nMaaa 96,192,252\nCV 245,170,171\nCV 246,172,173\nSv 196,1,16,75,1024\nMaaa 100,196,252\nCV 247,174,175\n"
"Maaa 104,200,252\nCV 248,176,177\nCV 249,178,179\nMaaa 108,204,252\nCV 250,180,181\nCV 251,182,183\nMaaa 112,208,252\nCV 252,184,185\nCV 253,186,187\nMaaa 116,212,252\n"
"CV 254,188,189\nMaaa 120,216,252\nCV 255,190,191\nSv 200,1,16,75,2048\nMaaa 124,220,252\nWL 0\nZvaa 64,128,160\nRa 224,45,0\nZvaa 96,128,164\nRa 228,45,2048\nZvaa 128,128,168\n"
"Ra 232,47,0\nZvaa 160,128,172\nRa 236,47,2048\nSv 204,1,16,75,3072\ns_lshl3_add_u32 s5, s12, s13\ns_lshl2_add_u32 s5, s5, s11\ns_lshl2_add_u32 s5, s5, 0xc04a400\n"
"buffer_load_dword v27, off, s[16:19], s5 sc0 sc1\nZvaa 164,132,172\nTa 192,32,16384\nZvaa 132,132,168\nZvaa 100,132,164\nTa 194,32,18432\nZvaa 68,132,160\nTa 196,33,16384\n"
"Zvaa 72,136,160\nTa 198,33,18432\nZvaa 104,136,164\nTa 200,34,16384\ns_add_u32 s75, s75, s55\nSv 208,1,16,75,0\nZvaa 136,136,168\nTa 202,34,18432\nZvaa 168,136,172\n"
"Zvaa 172,140,172\nTa 204,35,16384\nZvaa 140,140,168\nTa 206,35,18432\nZvaa 108,140,164\nTa 208,36,16384\nZvaa 76,140,160\nTa 210,36,18432\nSv 212,1,16,75,1024\nZvaa 80,144,160\n"
"Ta 212,37,16384\nZvaa 112,144,164\nZvaa 144,144,168\nTa 214,37,18432\nZvaa 176,144,172\nTa 216,38,16384\nZvaa 180,148,172\nTa 218,38,18432\nZvaa 148,148,168\nTa 220,39,16384\n"
"Sv 216,1,16,75,2048\nZvaa 116,148,164\nTa 222,39,18432\nZvaa 84,148,160\nZvaa 88,152,160\nZvaa 120,152,164\nZvaa 152,152,168\nZvaa 184,152,172\nSv 220,1,16,75,3072\n"
"Zvaa 188,156,172\nZvaa 156,156,168\nZvaa 124,156,164\nZvaa 92,156,160\ns_waitcnt vmcnt(17) lgkmcnt(0)\ns_barrier\nMvaa 64,192,224\nRa 160,44,49152\nMvaa 96,192,228\n"
"Ra 164,44,51200\ns_add_u32 m0, s47, 65536\ns_add_u32 s46, s51, 1572864\nD0 6,32,46,0\nMvaa 128,192,232\nRa 168,46,49152\nMvaa 160,192,236\nRa 172,46,51200\nMvaa 164,196,236\n"
"Ta 128,12,0\nMvaa 132,196,232\nD0 7,32,46,1024\nMvaa 100,196,228\nTa 130,12,2048\nMvaa 68,196,224\nTa 132,13,0\nMvaa 72,200,224\nTa 134,13,2048\nMvaa 104,200,228\nTa 136,14,0\n"
"D0 8,32,46,2048\nMvaa 136,200,232\nTa 138,14,2048\nMvaa 168,200,236\nMvaa 172,204,236\nTa 140,15,0\nMvaa 140,204,232\nTa 142,15,2048\nD0 9,32,46,3072\nMvaa 108,204,228\n"
"Ta 144,16,0\nMvaa 76,204,224\nTa 146,16,2048\ns_add_u32 m0, s47, 81920\ns_add_u32 s46, s51, 1835008\nMvaa 80,208,224\nTa 148,17,0\nMvaa 112,208,228\nD0 6,32,46,0\n"
"Mvaa 144,208,232\nTa 150,17,2048\nMvaa 176,208,236\nTa 152,18,0\nMvaa 180,212,236\nTa 154,18,2048\nMvaa 148,212,232\nTa 156,19,0\nD0 7,32,46,1024\nMvaa 116,212,228\n"
"Ta 158,19,2048\nMvaa 84,212,224\nMvaa 88,216,224\nMvaa 120,216,228\nD0 8,32,46,2048\nMvaa 152,216,232\nMvaa 184,216,236\nMvaa 188,220,236\nMvaa 156,220,232\nD0 9,32,46,3072\n"
"Mvaa 124,220,228\nMvaa 92,220,224\nWL 0\nMvaa 64,128,160\nRa 224,45,49152\nMvaa 96,128,164\nRa 228,45,51200\ns_add_u32 m0, s47, -65536\ns_add_u32 s46, s52, 512\nD0 40,28,46,0\n"
"Mvaa 128,128,168\nRa 232,47,49152\nMvaa 160,128,172\nRa 236,47,51200\nMvaa 164,132,172\nTa 192,12,16384\nMvaa 132,132,168\nD0 41,28,46,1024\nMvaa 100,132,164\nTa 194,12,18432\n"
"Mvaa 68,132,160\nTa 196,13,16384\nMvaa 72,136,160\nTa 198,13,18432\nMvaa 104,136,164\nTa 200,14,16384\nD0 42,28,46,2048\nMvaa 136,136,168\nTa 202,14,18432\nMvaa 168,136,172\n"
"Mvaa 172,140,172\nTa 204,15,16384\nMvaa 140,140,168\nTa 206,15,18432\nD0 43,28,46,3072\nMvaa 108,140,164\nTa 208,16,16384\nSv 224,1,16,76,0\nMvaa 76,140,160\nTa 210,16,18432\n"
"Mvaa 80,144,160\nTa 212,17,16384\nMvaa 112,144,164\nMvaa 144,144,168\nTa 214,17,18432\nMvaa 176,144,172\nTa 216,18,16384\nSv 228,1,16,76,1024\nMvaa 180,148,172\nTa 218,18,18432\n"
"Mvaa 148,148,168\nTa 220,19,16384\nMvaa 116,148,164\nTa 222,19,18432\nMvaa 84,148,160\nMvaa 88,152,160\nSv 232,1,16,76,2048\nMvaa 120,152,164\nMvaa 152,152,168\nMvaa 184,152,172\n"
"Mvaa 188,156,172\nMvaa 156,156,168\nSv 236,1,16,76,3072\nMvaa 124,156,164\nMvaa 92,156,160\ns_waitcnt vmcnt(16) lgkmcnt(0)\ns_barrier\nMvaa 64,192,224\nRa 160,44,16384\n"
"v_cmp_ne_u32 vcc, s15, v27\ns_nop 0\ns_cbranch_vccnz .S227\n.S228:\nMvaa 96,192,228\nRa 164,44,18432\nbuffer_store_dword v25, off, s[16:19], s54 offset:0 sc0 sc1\n"
"s_add_u32 m0, s47, 0\ns_add_u32 s46, s51, 2097152\nD0 6,32,46,0\nMvaa 128,192,232\nRa 168,46,16384\nMvaa 160,192,236\nRa 172,46,18432\nMvaa 164,196,236\nTa 128,12,32768\n"
"Mvaa 132,196,232\nD0 7,32,46,1024\nMvaa 100,196,228\nTa 130,12,34816\nMvaa 68,196,224\nTa 132,13,32768\nMvaa 72,200,224\nTa 134,13,34816\nMvaa 104,200,228\nTa 136,14,32768\n"
"D0 8,32,46,2048\nMvaa 136,200,232\nTa 138,14,34816\nMvaa 168,200,236\nMvaa 172,204,236\nTa 140,15,32768\nMvaa 140,204,232\nTa 142,15,34816\nD0 9,32,46,3072\nMvaa 108,204,228\n"
"Ta 144,16,32768\nMvaa 76,204,224\nTa 146,16,34816\ns_add_u32 m0, s47, 16384\ns_add_u32 s46, s51, 2359296\nMvaa 80,208,224\nTa 148,17,32768\nMvaa 112,208,228\nD0 6,32,46,0\n"
"Mvaa 144,208,232\nTa 150,17,34816\nMvaa 176,208,236\nTa 152,18,32768\nMvaa 180,212,236\nTa 154,18,34816\nMvaa 148,212,232\nTa 156,19,32768\nD0 7,32,46,1024\nMvaa 116,212,228\n"
"Ta 158,19,34816\nMvaa 84,212,224\nMvaa 88,216,224\nMvaa 120,216,228\nD0 8,32,46,2048\nMvaa 152,216,232\nMvaa 184,216,236\nMvaa 188,220,236\nMvaa 156,220,232\nD0 9,32,46,3072\n"
"Mvaa 124,220,228\nMvaa 92,220,224\nWL 0\nMvaa 64,128,160\nRa 224,45,16384\nMvaa 96,128,164\nRa 228,45,18432\ns_add_u32 m0, s47, -16384\ns_add_u32 s46, s52, 640\nD0 40,28,46,0\n"
"Mvaa 128,128,168\nRa 232,47,16384\nMvaa 160,128,172\nRa 236,47,18432\nMvaa 164,132,172\nTa 192,12,49152\nMvaa 132,132,168\nD0 41,28,46,1024\nMvaa 100,132,164\nTa 194,12,51200\n"
"Mvaa 68,132,160\nTa 196,13,49152\nMvaa 72,136,160\nTa 198,13,51200\nMvaa 104,136,164\nTa 200,14,49152\nD0 42,28,46,2048\nMvaa 136,136,168\nTa 202,14,51200\nMvaa 168,136,172\n"
"Mvaa 172,140,172\nTa 204,15,49152\nMvaa 140,140,168\nTa 206,15,51200\nD0 43,28,46,3072\nMvaa 108,140,164\nTa 208,16,49152\ns_add_u32 s76, s76, s55\nSv 240,1,16,76,0\n"
"Mvaa 76,140,160\nTa 210,16,51200\nMvaa 80,144,160\nTa 212,17,49152\nMvaa 112,144,164\nMvaa 144,144,168\nTa 214,17,51200\nMvaa 176,144,172\nTa 216,18,49152\nSv 244,1,16,76,1024\n"
"Mvaa 180,148,172\nTa 218,18,51200\nMvaa 148,148,168\nTa 220,19,49152\nMvaa 116,148,164\nTa 222,19,51200\nMvaa 84,148,160\nMvaa 88,152,160\nSv 248,1,16,76,2048\nMvaa 120,152,164\n"
"Mvaa 152,152,168\nMvaa 184,152,172\nMvaa 188,156,172\nMvaa 156,156,168\nSv 252,1,16,76,3072\nMvaa 124,156,164\nMvaa 92,156,160\ns_waitcnt vmcnt(25) lgkmcnt(0)\ns_barrier\n"
"Mvaa 64,192,224\nRa 160,44,32768\nAR 60,0\nAR 61,1\nMvaa 96,192,228\nRa 164,44,34816\ns_add_u32 m0, s47, 32768\ns_add_u32 s46, s51, 2621440\nD0 6,32,46,0\nMvaa 128,192,232\n"
"Ra 168,46,32768\nCV 52,60,61\nMvaa 160,192,236\nRa 172,46,34816\nAR 60,2\nMvaa 164,196,236\nTa 128,32,0\nAR 61,3\nMvaa 132,196,232\nD0 7,32,46,1024\nCV 53,60,61\nMvaa 100,196,228\n"
"Ta 130,32,2048\nAR 60,4\nMvaa 68,196,224\nTa 132,33,0\nAR 61,5\nMvaa 72,200,224\nTa 134,33,2048\nCV 54,60,61\nMvaa 104,200,228\nTa 136,34,0\nD0 8,32,46,2048\nMvaa 136,200,232\n"
"Ta 138,34,2048\nAR 60,6\nMvaa 168,200,236\nAR 61,7\nCV 55,60,61\nMvaa 172,204,236\nTa 140,35,0\nAR 60,8\nMvaa 140,204,232\nTa 142,35,2048\nD0 9,32,46,3072\nMvaa 108,204,228\n"
"Ta 144,36,0\nAR 61,9\nMvaa 76,204,224\nTa 146,36,2048\ns_add_u32 m0, s47, 49152\ns_add_u32 s46, s51, 2883584\nMvaa 80,208,224\nTa 148,37,0\nCV 56,60,61\nAR 60,10\n"
"Mvaa 112,208,228\nD0 6,32,46,0\nAR 61,11\nMvaa 144,208,232\nTa 150,37,2048\nCV 57,60,61\nMvaa 176,208,236\nTa 152,38,0\nAR 60,12\nMvaa 180,212,236\nTa 154,38,2048\nAR 61,13\n"
"Mvaa 148,212,232\nTa 156,39,0\nD0 7,32,46,1024\nMvaa 116,212,228\nTa 158,39,2048\nCV 58,60,61\nMvaa 84,212,224\nAR 60,14\nAR 61,15\nMvaa 88,216,224\nCV 59,60,61\nAR 60,16\n"
"Mvaa 120,216,228\nD0 8,32,46,2048\nAR 61,17\nMvaa 152,216,232\nCV 20,60,61\nAR 60,18\nMvaa 184,216,236\nAR 61,19\nCV 21,60,61\nMvaa 188,220,236\nAR 60,20\nAR 61,21\n"
"Mvaa 156,220,232\nD0 9,32,46,3072\nCV 22,60,61\nMvaa 124,220,228\nAR 60,22\nAR 61,23\nMvaa 92,220,224\nWL 0\nMvaa 64,128,160\nRa 224,45,32768\nCV 23,60,61\nAR 60,24\n"
"Mvaa 96,128,164\nRa 228,45,34816\ns_add_u32 m0, s47, -49152\ns_add_u32 s46, s52, 768\nD0 40,28,46,0\nMvaa 128,128,168\nRa 232,47,32768\nAR 61,25\nMvaa 160,128,172\n"
"Ra 236,47,34816\nCV 2,60,61\nMvaa 164,132,172\nTa 192,32,16384\nAR 60,26\nMvaa 132,132,168\nD0 41,28,46,1024\nAR 61,27\nMvaa 100,132,164\nTa 194,32,18432\nCV 3,60,61\n"
"Mvaa 68,132,160\nTa 196,33,16384\nAR 60,28\nMvaa 72,136,160\nTa 198,33,18432\nAR 61,29\nMvaa 104,136,164\nTa 200,34,16384\nD0 42,28,46,2048\nMvaa 136,136,168\nTa 202,34,18432\n"
"CV 4,60,61\nMvaa 168,136,172\nAR 60,30\nAR 61,31\nMvaa 172,140,172\nTa 204,35,16384\nCV 5,60,61\nMvaa 140,140,168\nTa 206,35,18432\nD0 43,28,46,3072\nMvaa 108,140,164\n"
"Ta 208,36,16384\nSv 52,1,16,77,0\nMvaa 76,140,160\nTa 210,36,18432\nMvaa 80,144,160\nTa 212,37,16384\nMvaa 112,144,164\nMvaa 144,144,168\nTa 214,37,18432\nMvaa 176,144,172\n"
"Ta 216,38,16384\nSv 56,1,16,77,1024\nMvaa 180,148,172\nTa 218,38,18432\nMvaa 148,148,168\nTa 220,39,16384\nMvaa 116,148,164\nTa 222,39,18432\nMvaa 84,148,160\nMvaa 88,152,160\n"
"Sv 20,1,16,77,2048\nMvaa 120,152,164\nMvaa 152,152,168\nMvaa 184,152,172\nMvaa 188,156,172\nMvaa 156,156,168\nSv 2,1,16,77,3072\nMvaa 124,156,164\nMvaa 92,156,160\n"
"s_waitcnt vmcnt(24) lgkmcnt(0)\ns_barrier\nMvaa 64,192,224\nRa 160,44,0\nv_and_b32 v27, 0x70, v1\nbuffer_load_dword v27, v27, s[16:19], s61 offen offset:128 sc0 sc1\n"
"Mvaa 96,192,228\nRa 164,44,2048\ns_add_u32 m0, s47, 65536\ns_add_u32 s46, s51, 3145728\nD0 6,32,46,0\nMvaa 128,192,232\nRa 168,46,0\nAR 60,32\nMvaa 160,192,236\nRa 172,46,2048\n"
"AR 61,33\nMvaa 164,196,236\nTa 128,12,0\nCV 52,60,61\nMvaa 132,196,232\nD0 7,32,46,1024\nAR 60,34\nMvaa 100,196,228\nTa 130,12,2048\nAR 61,35\nMvaa 68,196,224\nTa 132,13,0\n"
"CV 53,60,61\nMvaa 72,200,224\nTa 134,13,2048\nAR 60,36\nAR 61,37\nMvaa 104,200,228\nTa 136,14,0\nD0 8,32,46,2048\nMvaa 136,200,232\nTa 138,14,2048\nCV 54,60,61\nMvaa 168,200,236\n"
"AR 60,38\nAR 61,39\nMvaa 172,204,236\nTa 140,15,0\nCV 55,60,61\nMvaa 140,204,232\nTa 142,15,2048\nD0 9,32,46,3072\nMvaa 108,204,228\nTa 144,16,0\nAR 60,40\nMvaa 76,204,224\n"
"Ta 146,16,2048\ns_add_u32 m0, s47, 81920\ns_add_u32 s46, s51, 3407872\nMvaa 80,208,224\nTa 148,17,0\nAR 61,41\nCV 56,60,61\nMvaa 112,208,228\nD0 6,32,46,0\nAR 60,42\n"
"Mvaa 144,208,232\nTa 150,17,2048\nAR 61,43\nMvaa 176,208,236\nTa 152,18,0\nCV 57,60,61\nMvaa 180,212,236\nTa 154,18,2048\nAR 60,44\nMvaa 148,212,232\nTa 156,19,0\nD0 7,32,46,1024\n"
"Mvaa 116,212,228\nTa 158,19,2048\nAR 61,45\nMvaa 84,212,224\nCV 58,60,61\nAR 60,46\nMvaa 88,216,224\nAR 61,47\nCV 59,60,61\nAR 60,48\nMvaa 120,216,228\nD0 8,32,46,2048\nAR 61,49\n"
"Mvaa 152,216,232\nCV 20,60,61\nAR 60,50\nMvaa 184,216,236\nAR 61,51\nCV 21,60,61\nMvaa 188,220,236\nAR 60,52\nAR 61,53\nMvaa 156,220,232\nD0 9,32,46,3072\nCV 22,60,61\n"
"Mvaa 124,220,228\nAR 60,54\nAR 61,55\nMvaa 92,220,224\nWL 0\nMvaa 64,128,160\nRa 224,45,0\nCV 23,60,61\nAR 60,56\nMvaa 96,128,164\nRa 228,45,2048\ns_add_u32 s6, s56, 0\n"
"buffer_load_dwordx4 v[192:195], v1, s[16:19], s6 offen offset:0 sc1\nMvaa 128,128,168\nRa 232,47,0\nAR 61,57\nMvaa 160,128,172\nRa 236,47,2048\nCV 2,60,61\nMvaa 164,132,172\n"
"Ta 192,12,16384\nAR 60,58\nMvaa 132,132,168\nbuffer_load_dwordx4 v[196:199], v1, s[16:19], s6 offen offset:1024 sc1\nAR 61,59\nMvaa 100,132,164\nTa 194,12,18432\nCV 3,60,61\n"
"Mvaa 68,132,160\nTa 196,13,16384\nAR 60,60\nMvaa 72,136,160\nTa 198,13,18432\nAR 61,61\nCV 4,60,61\nMvaa 104,136,164\nTa 200,14,16384\n"
"buffer_load_dwordx4 v[200:203], v1, s[16:19], s6 offen offset:2048 sc1\nMvaa 136,136,168\nTa 202,14,18432\nAR 60,62\nMvaa 168,136,172\nAR 61,63\nCV 5,60,61\nMvaa 172,140,172\n"
"Ta 204,15,16384\nMvaa 140,140,168\nTa 206,15,18432\nbuffer_load_dwordx4 v[204:207], v1, s[16:19], s6 offen offset:3072 sc1\nMvaa 108,140,164\nTa 208,16,16384\n"
"s_add_u32 s77, s77, s55\nSv 52,1,16,77,0\nMvaa 76,140,160\nTa 210,16,18432\nMvaa 80,144,160\nTa 212,17,16384\nMvaa 112,144,164\nMvaa 144,144,168\nTa 214,17,18432\n"
"Mvaa 176,144,172\nTa 216,18,16384\nSv 56,1,16,77,1024\nMvaa 180,148,172\nTa 218,18,18432\nMvaa 148,148,168\nTa 220,19,16384\nMvaa 116,148,164\nTa 222,19,18432\nMvaa 84,148,160\n"
"Mvaa 88,152,160\nSv 20,1,16,77,2048\nMvaa 120,152,164\nMvaa 152,152,168\nMvaa 184,152,172\nMvaa 188,156,172\nMvaa 156,156,168\nSv 2,1,16,77,3072\nMvaa 124,156,164\n"
"Mvaa 92,156,160\ns_waitcnt vmcnt(16) lgkmcnt(0)\ns_barrier\nMvaa 64,192,224\nRa 160,44,49152\nv_cmp_ne_u32 vcc, s15, v27\ns_nop 0\ns_cbranch_vccnz .S230\n.S231:\n"
"s_sub_u32 s45, s86, 131072\ns_cmp_eq_u32 s79, 1\ns_cselect_b32 s86, s94, s86\ns_add_u32 m0, s47, -65536\ns_add_u32 s46, s45, 0x0\nMvaa 96,192,228\nRa 164,44,51200\n"
"buffer_store_dword v25, off, s[16:19], s54 offset:8 sc0 sc1\nMvaa 128,192,232\nRa 168,46,49152\nD1 1,16,46,0\nMvaa 160,192,236\nRa 172,46,51200\nAR 60,126\nMvaa 164,196,236\n"
"Ta 128,12,32768\nAR 61,127\nMvaa 132,196,232\nCV 60,60,61\nAW 127,60\nMvaa 100,196,228\nTa 130,12,34816\nD1 1,16,46,1024\nMvaa 68,196,224\nTa 132,13,32768\nAR 60,124\n"
"Mvaa 72,200,224\nTa 134,13,34816\nAR 61,125\nMvaa 104,200,228\nTa 136,14,32768\nMvaa 136,200,232\nTa 138,14,34816\nD1 1,16,46,2048\nMvaa 168,200,236\nCV 60,60,61\nAW 126,60\n"
"Mvaa 172,204,236\nTa 140,15,32768\nAR 60,122\nMvaa 140,204,232\nTa 142,15,34816\nAR 61,123\nMvaa 108,204,228\nTa 144,16,32768\nD1 1,16,46,3072\nMvaa 76,204,224\nTa 146,16,34816\n"
"CV 60,60,61\nMvaa 80,208,224\nTa 148,17,32768\ns_add_u32 s6, s56, 4096\nMvaa 112,208,228\nAW 125,60\nMvaa 144,208,232\nTa 150,17,34816\n"
"buffer_load_dwordx4 v[208:211], v1, s[16:19], s6 offen offset:0 sc1\nMvaa 176,208,236\nTa 152,18,32768\nAR 60,120\nMvaa 180,212,236\nTa 154,18,34816\nAR 61,121\nMvaa 148,212,232\n"
"Ta 156,19,32768\nCV 60,60,61\nMvaa 116,212,228\nTa 158,19,34816\nbuffer_load_dwordx4 v[212:215], v1, s[16:19], s6 offen offset:1024 sc1\nMvaa 84,212,224\nAW 124,60\nAR 60,118\n"
"Mvaa 88,216,224\nAR 61,119\nCV 60,60,61\nMvaa 120,216,228\nAW 123,60\nMvaa 152,216,232\nbuffer_load_dwordx4 v[216:219], v1, s[16:19], s6 offen offset:2048 sc1\nAR 60,116\n"
"Mvaa 184,216,236\nAR 61,117\nCV 60,60,61\nMvaa 188,220,236\nAW 122,60\nAR 60,114\nMvaa 156,220,232\nAR 61,115\nCV 60,60,61\nMvaa 124,220,228\n"
"buffer_load_dwordx4 v[220:223], v1, s[16:19], s6 offen offset:3072 sc1\nAW 121,60\nMvaa 92,220,224\nWL 0\nMvaa 64,128,160\nRa 224,45,49152\ns_add_u32 s6, s56, 16384\n"
"Mvaa 96,128,164\nRa 228,45,51200\nMvaa 128,128,168\nRa 232,47,49152\nbuffer_load_dwordx4 v[224:227], v1, s[16:19], s6 offen offset:0 sc1\nMvaa 160,128,172\nRa 236,47,51200\n"
"AR 60,112\nMvaa 164,132,172\nTa 192,12,49152\nAR 61,113\nMvaa 132,132,168\nCV 60,60,61\nAW 120,60\nMvaa 100,132,164\nTa 194,12,51200\n"
"buffer_load_dwordx4 v[228:231], v1, s[16:19], s6 offen offset:1024 sc1\nMvaa 68,132,160\nTa 196,13,49152\nAR 60,110\nMvaa 72,136,160\nTa 198,13,51200\nAR 61,111\nMvaa 104,136,164\n"
"Ta 200,14,49152\nMvaa 136,136,168\nTa 202,14,51200\nbuffer_load_dwordx4 v[232:235], v1, s[16:19], s6 offen offset:2048 sc1\nMvaa 168,136,172\nCV 60,60,61\nAW 119,60\n"
"Mvaa 172,140,172\nTa 204,15,49152\nAR 60,108\nMvaa 140,140,168\nTa 206,15,51200\nAR 61,109\nMvaa 108,140,164\nTa 208,16,49152\n"
"buffer_load_dwordx4 v[236:239], v1, s[16:19], s6 offen offset:3072 sc1\nMvaa 76,140,160\nTa 210,16,51200\nCV 60,60,61\nMvaa 80,144,160\nTa 212,17,49152\nAW 118,60\n"
"Mvaa 112,144,164\nAR 60,106\nMvaa 144,144,168\nTa 214,17,51200\nAR 61,107\nMvaa 176,144,172\nTa 216,18,49152\nCV 60,60,61\nMvaa 180,148,172\nTa 218,18,51200\nAW 117,60\n"
"Mvaa 148,148,168\nTa 220,19,49152\nAR 60,104\nMvaa 116,148,164\nTa 222,19,51200\nAR 61,105\nMvaa 84,148,160\nCV 60,60,61\nAW 116,60\nMvaa 88,152,160\nAR 60,102\nAR 61,103\n"
"Mvaa 120,152,164\nCV 60,60,61\nMvaa 152,152,168\nAW 115,60\nAR 60,100\nMvaa 184,152,172\nAR 61,101\nCV 60,60,61\nMvaa 188,156,172\nAW 114,60\nAR 60,98\nMvaa 156,156,168\nAR 61,99\n"
"CV 60,60,61\nMvaa 124,156,164\nAW 113,60\nAR 60,96\nMvaa 92,156,160\ns_waitcnt vmcnt(21) lgkmcnt(0)\ns_barrier\nMvaa 64,192,224\nRa 160,44,16384\nAR 61,97\nMvaa 96,192,228\n"
"Ra 164,44,18432\ns_add_u32 s6, s56, 20480\nbuffer_load_dwordx4 v[240:243], v1, s[16:19], s6 offen offset:0 sc1\nMvaa 128,192,232\nRa 168,46,16384\nCV 60,60,61\nMvaa 160,192,236\n"
"Ra 172,46,18432\nAW 112,60\nMvaa 164,196,236\nTa 128,32,0\nAR 60,94\nMvaa 132,196,232\nbuffer_load_dwordx4 v[244:247], v1, s[16:19], s6 offen offset:1024 sc1\nAR 61,95\n"
"Mvaa 100,196,228\nTa 130,32,2048\nCV 60,60,61\nMvaa 68,196,224\nTa 132,33,0\nAW 111,60\nMvaa 72,200,224\nTa 134,33,2048\nAR 60,92\nMvaa 104,200,228\nTa 136,34,0\n"
"buffer_load_dwordx4 v[248:251], v1, s[16:19], s6 offen offset:2048 sc1\nMvaa 136,200,232\nTa 138,34,2048\nAR 61,93\nMvaa 168,200,236\nCV 60,60,61\nAW 110,60\nMvaa 172,204,236\n"
"Ta 140,35,0\nAR 60,90\nMvaa 140,204,232\nTa 142,35,2048\nbuffer_load_dwordx4 v[252:255], v1, s[16:19], s6 offen offset:3072 sc1\nMvaa 108,204,228\nTa 144,36,0\nAR 61,91\n"
"Mvaa 76,204,224\nTa 146,36,2048\ns_add_u32 m0, s47, -32768\ns_add_u32 s46, s45, 0x8000\nMvaa 80,208,224\nTa 148,37,0\nCV 60,60,61\nMvaa 112,208,228\nD1 1,16,46,0\n"
"Mvaa 144,208,232\nTa 150,37,2048\nAW 109,60\nMvaa 176,208,236\nTa 152,38,0\nAR 60,88\nMvaa 180,212,236\nTa 154,38,2048\nAR 61,89\nMvaa 148,212,232\nTa 156,39,0\nD1 1,16,46,1024\n"
"Mvaa 116,212,228\nTa 158,39,2048\nCV 60,60,61\nMvaa 84,212,224\nAW 108,60\nAR 60,86\nMvaa 88,216,224\nAR 61,87\nCV 60,60,61\nMvaa 120,216,228\nD1 1,16,46,2048\nMvaa 152,216,232\n"
"AW 107,60\nAR 60,84\nMvaa 184,216,236\nAR 61,85\nCV 60,60,61\nMvaa 188,220,236\nAW 106,60\nAR 60,82\nMvaa 156,220,232\nD1 1,16,46,3072\nAR 61,83\nMvaa 124,220,228\nCV 60,60,61\n"
"AW 105,60\nMvaa 92,220,224\nWL 0\nMvaa 64,128,160\nRa 224,45,16384\nAR 60,80\nMvaa 96,128,164\nRa 228,45,18432\ns_add_u32 m0, s47, -16384\ns_add_u32 s46, s45, 0x400000\n"
"D1 1,16,46,0\nMvaa 128,128,168\nRa 232,47,16384\nAR 61,81\nMvaa 160,128,172\nRa 236,47,18432\nCV 60,60,61\nMvaa 164,132,172\nTa 192,32,16384\nAW 104,60\nMvaa 132,132,168\n"
"D1 1,16,46,1024\nAR 60,78\nMvaa 100,132,164\nTa 194,32,18432\nAR 61,79\nMvaa 68,132,160\nTa 196,33,16384\nCV 60,60,61\nMvaa 72,136,160\nTa 198,33,18432\nAW 103,60\n"
"Mvaa 104,136,164\nTa 200,34,16384\nD1 1,16,46,2048\nMvaa 136,136,168\nTa 202,34,18432\nAR 60,76\nMvaa 168,136,172\nAR 61,77\nCV 60,60,61\nMvaa 172,140,172\nTa 204,35,16384\n"
"AW 102,60\nMvaa 140,140,168\nTa 206,35,18432\nD1 1,16,46,3072\nMvaa 108,140,164\nTa 208,36,16384\nAR 60,74\nMvaa 76,140,160\nTa 210,36,18432\nAR 61,75\nMvaa 80,144,160\n"
"Ta 212,37,16384\nCV 60,60,61\nMvaa 112,144,164\nAW 101,60\nMvaa 144,144,168\nTa 214,37,18432\nAR 60,72\nMvaa 176,144,172\nTa 216,38,16384\nAR 61,73\nMvaa 180,148,172\n"
"Ta 218,38,18432\nCV 60,60,61\nMvaa 148,148,168\nTa 220,39,16384\nAW 100,60\nMvaa 116,148,164\nTa 222,39,18432\nAR 60,70\nMvaa 84,148,160\nAR 61,71\nCV 60,60,61\nMvaa 88,152,160\n"
"AW 99,60\nAR 60,68\nMvaa 120,152,164\nAR 61,69\nMvaa 152,152,168\nCV 60,60,61\nAW 98,60\nMvaa 184,152,172\nAR 60,66\nAR 61,67\nMvaa 188,156,172\nCV 60,60,61\nAW 97,60\n"
"Mvaa 156,156,168\nAR 60,64\nAR 61,65\nMvaa 124,156,164\nCV 60,60,61\nAW 96,60\nMvaa 92,156,160\ns_waitcnt vmcnt(20) lgkmcnt(0)\ns_barrier\nMvaa 64,192,224\nv_add_u32 v2, 0x0, v1\n"
"s_lshr_b32 s2, s59, 1\nMvaa 68,196,224\ns_add_u32 m0, s47, 0\ns_add_u32 s46, s86, 0xffff0000\nD1 1,16,46,0\nbuffer_store_dword v25, off, s[16:19], s54 offset:12 sc0 sc1\n"
"Mvaa 72,200,224\nv_add_u32 v24, s2, v2\nMvaa 76,204,224\nv_xor_b32 v26, 0x1000, v24\nv_lshrrev_b32 v20, 4, v0\nMvaa 80,208,224\nv_lshlrev_b32 v20, 2, v20\nMvaa 84,212,224\n"
"D1 1,16,46,1024\nv_and_b32 v0, 15, v0\nMvaa 88,216,224\nv_sub_u32 v0, v0, v20\nv_mul_f32 v64, s57, v64\nMvaa 92,220,224\ns_and_b32 s2, s11, 1\ns_lshl_b32 s2, s2, 6\n"
"v_mul_f32 v65, s57, v65\nMvaa 96,192,228\nv_subrev_u32 v3, s2, v0\nCV 64,64,65\nv_mul_f32 v66, s57, v66\nMvaa 100,196,228\nD1 1,16,46,2048\nv_mul_f32 v67, s57, v67\nCV 65,66,67\n"
"Mvaa 104,200,228\ns_xor_b32 s2, s2, 64\nv_subrev_u32 v0, s2, v0\nv_mul_f32 v68, s57, v68\nMvaa 108,204,228\nv_mov_b32 v4, 0\nv_mov_b32 v5, 0\nMvaa 112,208,228\nRa 128,24,0\n"
"v_mov_b32 v6, 0\nv_mul_f32 v69, s57, v69\nMvaa 116,212,228\nD1 1,16,46,3072\nCV 66,68,69\nv_mul_f32 v70, s57, v70\nMvaa 120,216,228\nRa 132,24,1024\nv_mov_b32 v7, 0\n"
"v_mul_f32 v71, s57, v71\nMvaa 124,220,228\ns_add_u32 m0, s47, 16384\ns_add_u32 s46, s45, 0x408000\nCV 67,70,71\nMvaa 128,192,232\nRa 136,24,2048\nv_mov_b32 v8, 0\n"
"v_mul_f32 v72, s57, v72\nMvaa 132,196,232\nD1 1,16,46,0\nv_mul_f32 v73, s57, v73\nMvaa 136,200,232\nRa 140,24,3072\nv_mov_b32 v9, 0\nCV 72,72,73\nMvaa 140,204,232\n"
"v_mov_b32 v10, 0\nv_mov_b32 v11, 0\nv_mul_f32 v74, s57, v74\nMvaa 144,208,232\nRa 144,26,0\nv_mov_b32 v12, 0\nv_mul_f32 v75, s57, v75\nMvaa 148,212,232\nD1 1,16,46,1024\n"
"CV 73,74,75\nv_mul_f32 v76, s57, v76\nMvaa 152,216,232\nRa 148,26,1024\nv_mov_b32 v13, 0\nMvaa 156,220,232\nv_mov_b32 v14, 0\nv_mov_b32 v15, 0\nv_mul_f32 v77, s57, v77\n"
"Mvaa 160,192,236\nRa 152,26,2048\nv_mov_b32 v16, 0\nCV 74,76,77\nMvaa 164,196,236\nD1 1,16,46,2048\nv_mul_f32 v78, s57, v78\nv_mul_f32 v79, s57, v79\nMvaa 168,200,236\n"
"Ra 156,26,3072\nv_mov_b32 v17, 0\nCV 75,78,79\nMvaa 172,204,236\nv_mov_b32 v18, 0\nv_mov_b32 v19, 0\nMvaa 176,208,236\ns_lshl_b32 s2, s11, 13\ns_add_u32 s2, s2, 0x20000\n"
"v_mul_f32 v80, s57, v80\nMvaa 180,212,236\nD1 1,16,46,3072\nv_mul_f32 v81, s57, v81\nCV 80,80,81\nMvaa 184,216,236\nv_add_u32 v22, s2, v1\nv_xor_b32 v23, 0x2000, v22\n"
"v_mul_f32 v82, s57, v82\nMvaa 188,220,236\ns_waitcnt vmcnt(25) lgkmcnt(4)\ns_cmp_eq_u32 s79, 1\ns_cbranch_scc1 .E234\n.E235:\nCvav 32,128,192,4\nRa 160,24,8192\n"
"v_mul_f32 v83, s57, v83\nCV 81,82,83\nMvav 32,132,200\nv_mul_f32 v84, s57, v84\nv_mul_f32 v85, s57, v85\nMvav 32,136,208\nRa 164,24,9216\nCV 82,84,85\nv_mul_f32 v86, s57, v86\n"
"Mvav 32,140,216\nv_mul_f32 v87, s57, v87\nCV 83,86,87\nv_mul_f32 v88, s57, v88\nCvav 36,140,220,8\nRa 168,24,10240\nv_mul_f32 v89, s57, v89\nCV 88,88,89\nMvav 36,136,212\n"
"s_add_u32 m0, s89, 32768\nv_mul_f32 v90, s57, v90\nv_mul_f32 v91, s57, v91\nMvav 36,132,204\nRa 172,24,11264\ns_waitcnt vmcnt(17) lgkmcnt(4)\ns_barrier\nMvav 36,128,196\n"
"CV 89,90,91\nv_mul_f32 v92, s57, v92\nv_mul_f32 v93, s57, v93\nMvav 36,144,228\nRa 176,26,8192\nCV 90,92,93\nv_mul_f32 v94, s57, v94\nSa 96,1,16,78,0\nMvav 36,148,236\n"
"s_add_u32 s46, s86, s88\nv_mul_f32 v95, s57, v95\nCV 91,94,95\nMvav 36,152,244\nRa 180,26,9216\nAW 192,64\nAW 193,65\nMvav 36,156,252\nAW 194,66\nAW 195,67\nMvav 32,156,248\n"
"Ra 184,26,10240\nAW 200,72\nAW 201,73\nMvav 32,152,240\nD1 1,16,46,0\nAW 202,74\nAW 203,75\nMvav 32,148,232\nRa 188,26,11264\nWL 4\nAW 208,80\nMvav 32,144,224\nAW 209,81\n"
"AW 210,82\nAW 211,83\nCvav 40,160,192,12\nAW 216,88\nAW 217,89\nAW 218,90\nSa 100,1,16,78,1024\nMvav 40,164,200\ns_cmp_eq_u32 s99, 0\nMvav 40,168,208\ns_cselect_b32 s87, s95, s87\n"
"AW 219,91\nv_mov_b32 v86, 0\nMvav 40,172,216\nv_mov_b32 v87, 0\nv_mov_b32 v88, 0\nv_mov_b32 v89, 0\nv_mul_f32 v128, s57, v128\nv_mul_f32 v129, s57, v129\nCV 128,128,129\n"
"v_mul_f32 v130, s57, v130\nCvav 44,172,220,16\nv_and_b32 v27, 0x70, v1\nbuffer_load_dword v27, v27, s[16:19], s61 offen offset:136 sc0 sc1\nv_mul_f32 v131, s57, v131\n"
"CV 129,130,131\nv_mul_f32 v132, s57, v132\nv_mul_f32 v133, s57, v133\nCV 130,132,133\nv_mul_f32 v134, s57, v134\nMvav 44,168,212\nD1 1,16,46,1024\nv_mul_f32 v135, s57, v135\n"
"CV 131,134,135\nds_write_b128 v23, v[128:131] offset:0\nv_mul_f32 v136, s57, v136\nv_mul_f32 v137, s57, v137\nCV 136,136,137\nv_mul_f32 v138, s57, v138\nMvav 44,164,204\nWL 1\n"
"v_mul_f32 v139, s57, v139\nCV 137,138,139\nv_mul_f32 v140, s57, v140\nv_mul_f32 v141, s57, v141\nCV 138,140,141\nv_mul_f32 v142, s57, v142\nMvav 44,160,196\n"
"v_mul_f32 v143, s57, v143\nCV 139,142,143\nds_write_b128 v23, v[136:139] offset:2048\nv_mul_f32 v144, s57, v144\nv_mul_f32 v145, s57, v145\nCV 144,144,145\n"
"v_mul_f32 v146, s57, v146\nv_mul_f32 v147, s57, v147\nMvav 44,176,228\nCV 145,146,147\nv_mul_f32 v148, s57, v148\nv_mul_f32 v149, s57, v149\nCV 146,148,149\n"
"v_mul_f32 v150, s57, v150\nv_mul_f32 v151, s57, v151\nCV 147,150,151\nds_write_b128 v23, v[144:147] offset:4096\nSa 104,1,16,78,2048\nMvav 44,180,236\ns_add_u32 s88, s88, 32768\n"
"EX 80,32\nv_mul_f32 v152, s57, v152\nv_mul_f32 v153, s57, v153\nCV 152,152,153\nv_mul_f32 v154, s57, v154\nMvav 44,184,244\nv_mul_f32 v155, s57, v155\nCV 153,154,155\n"
"v_mul_f32 v156, s57, v156\nv_mul_f32 v157, s57, v157\nCV 154,156,157\nv_mul_f32 v158, s57, v158\nv_mul_f32 v159, s57, v159\nCV 155,158,159\nMvav 44,188,252\nEX 81,33\n"
"ds_write_b128 v23, v[152:155] offset:6144\nv_mul_f32 v160, s57, v160\nv_mul_f32 v161, s57, v161\nCV 160,160,161\nMvav 40,188,248\nv_mul_f32 v162, s57, v162\n"
"v_mul_f32 v163, s57, v163\nCV 161,162,163\nv_mul_f32 v164, s57, v164\nv_mul_f32 v165, s57, v165\nCV 162,164,165\nv_mul_f32 v166, s57, v166\nv_mul_f32 v167, s57, v167\n"
"Mvav 40,184,240\nD1 1,16,46,2048\nCV 163,166,167\nds_write_b128 v23, v[160:163] offset:1024\nv_mul_f32 v168, s57, v168\nv_mul_f32 v169, s57, v169\nCV 168,168,169\n"
"v_mul_f32 v170, s57, v170\nv_mul_f32 v171, s57, v171\nMvav 40,180,232\nCV 169,170,171\nv_mul_f32 v172, s57, v172\nv_mul_f32 v173, s57, v173\nCV 170,172,173\n"
"v_mul_f32 v174, s57, v174\nv_mul_f32 v175, s57, v175\nCV 171,174,175\nMvav 40,176,224\nAD 84,80,81\nCV 64,80,81\nds_write_b128 v23, v[168:171] offset:3072\n"
"v_mul_f32 v176, s57, v176\nv_mul_f32 v177, s57, v177\nCV 176,176,177\nv_mul_f32 v178, s57, v178\nv_mul_f32 v179, s57, v179\nCV 177,178,179\nv_mul_f32 v180, s57, v180\n"
"v_mul_f32 v181, s57, v181\nCV 178,180,181\nv_mul_f32 v182, s57, v182\nv_mul_f32 v183, s57, v183\nCV 179,182,183\nds_write_b128 v23, v[176:179] offset:5120\nSa 108,1,16,78,3072\n"
"EX 82,34\nv_mul_f32 v184, s57, v184\nv_mul_f32 v185, s57, v185\nCV 184,184,185\nv_mul_f32 v186, s57, v186\nv_mul_f32 v187, s57, v187\nCV 185,186,187\nv_mul_f32 v188, s57, v188\n"
"v_mul_f32 v189, s57, v189\nCV 186,188,189\nv_mul_f32 v190, s57, v190\nv_mul_f32 v191, s57, v191\nCV 187,190,191\nds_write_b128 v23, v[184:187] offset:7168\n"
"s_waitcnt vmcnt(17) lgkmcnt(0)\nEX 83,35\nAD 84,84,82\nD1 1,16,46,3072\ns_add_u32 m0, s89, 49152\ns_barrier\nAD 84,84,83\nCV 65,82,83\nRa 128,24,32768\nRa 224,22,0\n"
"Ra 228,22,1024\nRa 232,22,2048\ns_add_u32 s46, s87, s88\nEX 80,36\nRa 132,24,33792\nRa 236,22,3072\nRa 240,22,4096\nRa 244,22,5120\nEX 81,37\nRa 248,22,6144\nRa 136,24,34816\n"
"Ra 252,22,7168\nv_mul_f32 v96, s57, v96\nv_mul_f32 v97, s57, v97\nD1 1,16,46,0\nCV 96,96,97\nv_mul_f32 v98, s57, v98\nRa 140,24,35840\nv_mul_f32 v99, s57, v99\nCV 97,98,99\n"
"v_mul_f32 v100, s57, v100\nAD 85,80,81\nCV 68,80,81\nv_mul_f32 v101, s57, v101\nCV 98,100,101\nRa 144,26,32768\nv_mul_f32 v102, s57, v102\nv_mul_f32 v103, s57, v103\n"
"CV 99,102,103\ns_cmp_eq_u32 s99, s93\nEX 82,38\nRa 148,26,33792\ns_cbranch_scc1 .E217\n.E218:\nAW 196,96\nAW 197,97\nEX 83,39\nAW 198,98\nRa 152,26,34816\nAD 85,85,82\nAW 199,99\n"
"v_mul_f32 v104, s57, v104\nD1 1,16,46,1024\nv_mul_f32 v105, s57, v105\nCV 104,104,105\nRa 156,26,35840\nv_mul_f32 v106, s57, v106\nv_mul_f32 v107, s57, v107\nCV 105,106,107\n"
"AD 85,85,83\nCV 69,82,83\nv_mul_f32 v108, s57, v108\nv_mul_f32 v109, s57, v109\nRa 160,24,40960\nCV 106,108,109\nv_mul_f32 v110, s57, v110\nv_mul_f32 v111, s57, v111\n"
"s_xor_b32 s89, s89, 0x10000\nEX 80,40\nRa 164,24,41984\nCV 107,110,111\nAW 204,104\nAW 205,105\nWL 6\nEX 81,41\nAW 206,106\nRa 168,24,43008\nAD 84,84,80\nAW 207,107\n"
"v_mul_f32 v112, s57, v112\nD1 1,16,46,2048\nv_mul_f32 v113, s57, v113\nCV 112,112,113\nRa 172,24,44032\nv_mul_f32 v114, s57, v114\nv_mul_f32 v115, s57, v115\nCV 113,114,115\n"
"AD 84,84,81\nCV 66,80,81\nv_mul_f32 v116, s57, v116\nv_mul_f32 v117, s57, v117\nCvav 48,128,192,4\nRa 176,26,40960\nCV 114,116,117\nv_mul_f32 v118, s57, v118\n"
"v_mul_f32 v119, s57, v119\nMvav 48,132,200\ns_cmp_eq_u32 s99, 1\nEX 82,42\nMvav 48,136,208\nRa 180,26,41984\ns_cselect_b32 s86, s94, s86\nCV 115,118,119\nAW 212,112\n"
"Mvav 48,140,216\nWL 6\nEX 83,43\nAW 213,113\nWV 8\nv_cmp_ne_u32 vcc, s15, v27\ns_nop 0\ns_cbranch_vccnz .S236\n.S237:\nCvav 52,140,220,8\nRa 184,26,43008\nAD 84,84,82\nAW 214,114\n"
"AW 215,115\nMvav 52,136,212\nD1 1,16,46,3072\ns_add_u32 m0, s89, 0\nv_mul_f32 v120, s57, v120\nMvav 52,132,204\nRa 188,26,44032\nv_mul_f32 v121, s57, v121\nCV 120,120,121\n"
"v_mul_f32 v122, s57, v122\nMvav 52,128,196\nAD 84,84,83\nCV 67,82,83\nv_mul_f32 v123, s57, v123\nCV 121,122,123\nMvav 52,144,228\nRa 128,2,49152\nv_mul_f32 v124, s57, v124\n"
"v_mul_f32 v125, s57, v125\nCV 122,124,125\ns_add_u32 s78, s78, s55\nSa 112,1,16,78,0\nMvav 52,148,236\ns_add_u32 s46, s86, s88\nEX 80,44\nMvav 52,152,244\nRa 132,2,50176\n"
"v_xor_b32 v24, 0x10000, v24\nv_mul_f32 v126, s57, v126\nv_mul_f32 v127, s57, v127\nMvav 52,156,252\nWL 6\nEX 81,45\nCV 123,126,127\nMvav 48,156,248\nRa 136,2,51200\nAD 85,85,80\n"
"AW 220,120\nAW 221,121\nMvav 48,152,240\nD1 1,16,46,0\nAW 222,122\nAW 223,123\nMvav 48,148,232\nRa 140,2,52224\nv_mov_b32 v112, -1\nv_mov_b32 v113, -1\nv_mov_b32 v114, -1\n"
"Mvav 48,144,224\nAD 85,85,81\nCV 70,80,81\nv_mov_b32 v115, -1\nCvav 56,160,192,12\nRa 144,2,53248\nSa 116,1,16,78,1024\nMvav 56,164,200\nEX 82,46\nMvav 56,168,208\nRa 148,2,54272\n"
"v_xor_b32 v26, 0x10000, v26\nMvav 56,172,216\nWL 6\nEX 83,47\nCvav 60,172,220,16\nRa 152,2,55296\nAD 85,85,82\nMvav 60,168,212\nD1 1,16,46,1024\nMvav 60,164,204\nRa 156,2,56320\n"
"Mvav 60,160,196\nAD 85,85,83\nCV 71,82,83\nMvav 60,176,228\nRa 160,2,57344\nv_max_f32 v90, v84, v85\nMvav 60,180,236\ns_add_u32 s88, s88, 32768\nEX 80,48\nMvav 60,184,244\n"
"Ra 164,2,58368\nv_cmp_lt_f32 vcc, s63, v90\nMvav 60,188,252\nWL 8\nEX 81,49\nMvav 56,188,248\nRa 168,2,59392\ns_cbranch_vccnz .T223\n.T224:\nv_mov_b32 v20, v84\n"
"v_mov_b32 v21, v85\nv_mov_b32 v22, v20\nv_mov_b32 v23, v21\nMvav 56,184,240\nD1 1,16,46,2048\nv_permlane32_swap_b32 v20, v22\nv_permlane32_swap_b32 v21, v23\nAD 20,20,22\n"
"AD 21,21,23\nv_mov_b32 v22, v20\nv_mov_b32 v23, v21\nMvav 56,180,232\nRa 172,2,60416\nv_permlane16_swap_b32 v20, v22\nv_permlane16_swap_b32 v21, v23\nAD 20,20,22\nAD 21,21,23\n"
"v_min_f32 v90, v20, v21\nv_cmp_gt_f32 vcc, s64, v90\ns_nop 0\ns_cbranch_vccnz .T225\n.T226:\nAD 86,86,84\nMvav 56,176,224\nAD 84,80,81\nCV 72,80,81\nZaav 0,128,64\nRa 176,2,61440\n"
"Sa 120,1,16,78,2048\nZaav 4,128,68\nEX 82,50\nZaav 12,132,68\nRa 180,2,62464\nZaav 8,132,64\nWL 8\nEX 83,51\nZaav 16,136,64\nRa 184,2,63488\nAD 84,84,82\nZaav 20,136,68\n"
"D1 1,16,46,3072\ns_add_u32 m0, s89, 16384\nZaav 28,140,68\nRa 188,2,64512\ns_barrier\nZaav 24,140,64\nAD 84,84,83\nCV 73,82,83\nZaav 32,144,64\nRa 128,24,0\nSa 124,1,16,78,3072\n"
"Zaav 36,144,68\ns_add_u32 s46, s87, s88\nEX 80,52\nZaav 44,148,68\nRa 132,24,1024\nv_xor_b32 v2, 0x10000, v2\nZaav 40,148,64\nWL 8\nEX 81,53\nZaav 48,152,64\nRa 136,24,2048\n"
"AD 87,87,85\nZaav 52,152,68\nD1 1,16,46,0\nZaav 60,156,68\nRa 140,24,3072\nZaav 56,156,64\nAD 85,80,81\nCV 76,80,81\nZaav 64,160,64\nRa 144,26,0\nZaav 68,160,68\n"
"s_cmp_eq_u32 s99, s92\nEX 82,54\nZaav 76,164,68\nRa 148,26,1024\ns_cbranch_scc1 .E219\n.E220:\nZaav 72,164,64\nWL 8\nEX 83,55\nZaav 80,168,64\nRa 152,26,2048\nAD 85,85,82\n"
"Zaav 84,168,68\nD1 1,16,46,1024\nZaav 92,172,68\nRa 156,26,3072\nZaav 88,172,64\nAD 85,85,83\nCV 77,82,83\nZaav 96,176,64\nRa 160,24,8192\nZaav 100,176,68\ns_sub_u32 s90, s90, 1\n"
"EX 80,56\nZaav 108,180,68\nRa 164,24,9216\ns_cbranch_scc1 .E221\n.E222:\nZaav 104,180,64\nWL 6\nEX 81,57\nZaav 112,184,64\nRa 168,24,10240\nAD 84,84,80\nv_and_b32 v27, 0xf0, v1\n"
"buffer_load_dword v27, v27, s[16:19], s61 offen offset:12 sc0 sc1\nZaav 116,184,68\nD1 1,16,46,2048\nZaav 124,188,68\nRa 172,24,11264\nZaav 120,188,64\nAD 84,84,81\nCV 74,80,81\n"
"s_sub_u32 s99, s99, 1\ns_cbranch_scc1 .SW215\n.p2align 6\n.A239:\nCvav 32,128,192,4\nRa 176,26,8192\nMvav 32,132,200\nEX 82,58\nMvav 32,136,208\nRa 180,26,9216\nMvav 32,140,216\n"
"WL 6\nEX 83,59\nCvav 36,140,220,8\nRa 184,26,10240\nAD 84,84,82\nMvav 36,136,212\nD1 1,16,46,3072\ns_add_u32 m0, s89, 32768\nMvav 36,132,204\nRa 188,26,11264\nMvav 36,128,196\n"
"AD 84,84,83\nCV 75,82,83\nMvav 36,144,228\nRa 128,2,16384\nMvav 36,148,236\ns_add_u32 s46, s86, s88\nEX 80,60\nMvav 36,152,244\nRa 132,2,17408\nMvav 36,156,252\nWL 6\nEX 81,61\n"
"Mvav 32,156,248\nRa 136,2,18432\nAD 85,85,80\nMvav 32,152,240\nD1 1,16,46,0\nMvav 32,148,232\nRa 140,2,19456\nSv 112,1,16,49,0\nMvav 32,144,224\nAD 85,85,81\nCV 78,80,81\n"
"Cvav 40,160,192,12\nRa 144,2,20480\nMvav 40,164,200\ns_cmp_eq_u32 s99, 0\nEX 82,62\nMvav 40,168,208\nRa 148,2,21504\ns_cselect_b32 s87, s95, s87\nMvav 40,172,216\nWL 6\nEX 83,63\n"
"Cvav 44,172,220,16\nRa 152,2,22528\nAD 85,85,82\nMvav 44,168,212\nD1 1,16,46,1024\nMvav 44,164,204\nRa 156,2,23552\nMvav 44,160,196\nAD 85,85,83\nCV 79,82,83\nMvav 44,176,228\n"
"Ra 160,2,24576\nv_max_f32 v90, v84, v85\nMvav 44,180,236\ns_add_u32 s88, s88, 32768\nEX 80,32\nMvav 44,184,244\nRa 164,2,25600\nv_cmp_lt_f32 vcc, s63, v90\nMvav 44,188,252\nWL 8\n"
"EX 81,33\nMvav 40,188,248\nRa 168,2,26624\ns_cbranch_vccnz .T246\n.T247:\nMvav 40,184,240\nD1 1,16,46,2048\nMvav 40,180,232\nRa 172,2,27648\nAD 86,86,84\nMvav 40,176,224\n"
"AD 84,80,81\nCV 64,80,81\nMaav 0,128,72\nRa 176,2,28672\nMaav 4,128,76\nEX 82,34\nMaav 12,132,76\nRa 180,2,29696\nMaav 8,132,72\ns_waitcnt vmcnt(13) lgkmcnt(8)\nEX 83,35\n"
"Maav 16,136,72\nRa 184,2,30720\nAD 84,84,82\nMaav 20,136,76\nD1 1,16,46,3072\ns_add_u32 m0, s89, 49152\nMaav 28,140,76\nRa 188,2,31744\ns_barrier\nMaav 24,140,72\nAD 84,84,83\n"
"CV 65,82,83\nMaav 32,144,72\nRa 128,24,32768\nMaav 36,144,76\ns_add_u32 s46, s87, s88\nEX 80,36\nMaav 44,148,76\nRa 132,24,33792\nMaav 40,148,72\nWL 8\nEX 81,37\nMaav 48,152,72\n"
"Ra 136,24,34816\nAD 87,87,85\nMaav 52,152,76\nD1 1,16,46,0\nMaav 60,156,76\nRa 140,24,35840\nMaav 56,156,72\nAD 85,80,81\nCV 68,80,81\nMaav 64,160,72\nRa 144,26,32768\n"
"Maav 68,160,76\ns_cmp_eq_u32 s99, s93\nEX 82,38\nMaav 76,164,76\nRa 148,26,33792\ns_cbranch_scc1 .E240\n.E241:\nMaav 72,164,72\nWL 8\nEX 83,39\nMaav 80,168,72\nRa 152,26,34816\n"
"AD 85,85,82\nMaav 84,168,76\nD1 1,16,46,1024\nMaav 92,172,76\nRa 156,26,35840\nMaav 88,172,72\nAD 85,85,83\nCV 69,82,83\nMaav 96,176,72\nRa 160,24,40960\nMaav 100,176,76\n"
"s_xor_b32 s89, s89, 0x10000\nEX 80,40\nMaav 108,180,76\nRa 164,24,41984\nMaav 104,180,72\nWL 6\nEX 81,41\nMaav 112,184,72\nRa 168,24,43008\nAD 84,84,80\nMaav 116,184,76\n"
"D1 1,16,46,2048\nMaav 124,188,76\nRa 172,24,44032\nMaav 120,188,72\nAD 84,84,81\nCV 66,80,81\nCvav 48,128,192,4\nRa 176,26,40960\nMvav 48,132,200\ns_cmp_eq_u32 s99, 1\nEX 82,42\n"
"Mvav 48,136,208\nRa 180,26,41984\ns_cselect_b32 s86, s94, s86\nMvav 48,140,216\nWL 6\nEX 83,43\nCvav 52,140,220,8\nRa 184,26,43008\nAD 84,84,82\nMvav 52,136,212\nD1 1,16,46,3072\n"
"s_add_u32 m0, s89, 0\nMvav 52,132,204\nRa 188,26,44032\nMvav 52,128,196\nAD 84,84,83\nCV 67,82,83\nMvav 52,144,228\nRa 128,2,49152\nMvav 52,148,236\ns_add_u32 s46, s86, s88\n"
"EX 80,44\nMvav 52,152,244\nRa 132,2,50176\nv_xor_b32 v24, 0x10000, v24\nMvav 52,156,252\nWL 6\nEX 81,45\nMvav 48,156,248\nRa 136,2,51200\nAD 85,85,80\nMvav 48,152,240\n"
"D1 1,16,46,0\nMvav 48,148,232\nRa 140,2,52224\nSv 112,1,16,49,1024\nMvav 48,144,224\nAD 85,85,81\nCV 70,80,81\nCvav 56,160,192,12\nRa 144,2,53248\nMvav 56,164,200\n"
"s_add_u32 s49, s49, 131072\nEX 82,46\nMvav 56,168,208\nRa 148,2,54272\nv_xor_b32 v26, 0x10000, v26\nMvav 56,172,216\nWL 6\nEX 83,47\nCvav 60,172,220,16\nRa 152,2,55296\n"
"AD 85,85,82\nMvav 60,168,212\nD1 1,16,46,1024\nMvav 60,164,204\nRa 156,2,56320\nMvav 60,160,196\nAD 85,85,83\nCV 71,82,83\nMvav 60,176,228\nRa 160,2,57344\n"
"v_max_f32 v90, v84, v85\nMvav 60,180,236\ns_add_u32 s88, s88, 32768\nEX 80,48\nMvav 60,184,244\nRa 164,2,58368\nv_cmp_lt_f32 vcc, s63, v90\nMvav 60,188,252\nWL 8\nEX 81,49\n"
"Mvav 56,188,248\nRa 168,2,59392\ns_cbranch_vccnz .T248\n.T249:\nMvav 56,184,240\nD1 1,16,46,2048\nMvav 56,180,232\nRa 172,2,60416\nAD 86,86,84\nMvav 56,176,224\nAD 84,80,81\n"
"CV 72,80,81\nMaav 0,128,64\nRa 176,2,61440\nMaav 4,128,68\nEX 82,50\nMaav 12,132,68\nRa 180,2,62464\nMaav 8,132,64\ns_waitcnt vmcnt(13) lgkmcnt(8)\nEX 83,51\nMaav 16,136,64\n"
"Ra 184,2,63488\nAD 84,84,82\nMaav 20,136,68\nD1 1,16,46,3072\ns_add_u32 m0, s89, 16384\nMaav 28,140,68\nRa 188,2,64512\ns_barrier\nMaav 24,140,64\nAD 84,84,83\nCV 73,82,83\n"
"Maav 32,144,64\nRa 128,24,0\nMaav 36,144,68\ns_add_u32 s46, s87, s88\nEX 80,52\nMaav 44,148,68\nRa 132,24,1024\nv_xor_b32 v2, 0x10000, v2\nMaav 40,148,64\nWL 8\nEX 81,53\n"
"Maav 48,152,64\nRa 136,24,2048\nAD 87,87,85\nMaav 52,152,68\nD1 1,16,46,0\nMaav 60,156,68\nRa 140,24,3072\nMaav 56,156,64\nAD 85,80,81\nCV 76,80,81\nMaav 64,160,64\nRa 144,26,0\n"
"Maav 68,160,68\ns_cmp_eq_u32 s99, s92\nEX 82,54\nMaav 76,164,68\nRa 148,26,1024\ns_cbranch_scc1 .E242\n.E243:\nMaav 72,164,64\nWL 8\nEX 83,55\nMaav 80,168,64\nRa 152,26,2048\n"
"AD 85,85,82\nMaav 84,168,68\nD1 1,16,46,1024\nMaav 92,172,68\nRa 156,26,3072\nMaav 88,172,64\nAD 85,85,83\nCV 77,82,83\nMaav 96,176,64\nRa 160,24,8192\nMaav 100,176,68\n"
"s_sub_u32 s90, s90, 1\nEX 80,56\nMaav 108,180,68\nRa 164,24,9216\ns_cbranch_scc1 .E244\n.E245:\nMaav 104,180,64\nWL 6\nEX 81,57\nMaav 112,184,64\nRa 168,24,10240\nAD 84,84,80\n"
"Maav 116,184,68\nD1 1,16,46,2048\ns_sub_u32 s99, s99, 1\nMaav 124,188,68\nRa 172,24,11264\nMaav 120,188,64\nAD 84,84,81\nCV 74,80,81\ns_cbranch_scc0 .A239\n.SW215:\n"
"v_mov_b32 v4, 0\nv_mov_b32 v5, 0\nv_mov_b32 v6, 0\nv_mov_b32 v7, 0\nv_mov_b32 v8, 0\nv_mov_b32 v9, 0\nv_mov_b32 v10, 0\nv_mov_b32 v11, 0\nv_mov_b32 v12, 0\nv_mov_b32 v13, 0\n"
"v_mov_b32 v14, 0\nv_mov_b32 v15, 0\nv_mov_b32 v16, 0\nv_mov_b32 v17, 0\nv_mov_b32 v18, 0\nv_mov_b32 v19, 0\ns_add_u32 s99, s98, 1\ns_mov_b32 s94, s96\ns_add_u32 s95, s96, 65536\n"
"s_or_b32 s2, s13, s11\ns_cmp_eq_u32 s2, 0\ns_cbranch_scc1 .E250\n.E251:\nCvaa 32,128,192,4\nRa 176,26,8192\nMvaa 32,132,200\nEX 82,58\nMvaa 32,136,208\nRa 180,26,9216\n"
"Mvaa 32,140,216\nWL 6\nEX 83,59\nCvaa 36,140,220,8\nRa 184,26,10240\nAD 84,84,82\nMvaa 36,136,212\nD1 1,16,46,3072\ns_add_u32 m0, s89, 32768\nMvaa 36,132,204\nRa 188,26,11264\n"
"Mvaa 36,128,196\nAD 84,84,83\nCV 75,82,83\nMvaa 36,144,228\nRa 128,2,16384\nMvaa 36,148,236\ns_add_u32 s46, s86, s88\nEX 80,60\nMvaa 36,152,244\nRa 132,2,17408\nMvaa 36,156,252\n"
"WL 6\nEX 81,61\nMvaa 32,156,248\nRa 136,2,18432\nAD 85,85,80\nMvaa 32,152,240\nD1 1,16,46,0\nMvaa 32,148,232\nRa 140,2,19456\nMvaa 32,144,224\nAD 85,85,81\nCV 78,80,81\n"
"Cvaa 40,160,192,12\nRa 144,2,20480\nMvaa 40,164,200\nEX 82,62\nMvaa 40,168,208\nRa 148,2,21504\nMvaa 40,172,216\nWL 6\nEX 83,63\nCvaa 44,172,220,16\nRa 152,2,22528\nAD 85,85,82\n"
"Mvaa 44,168,212\nD1 1,16,46,1024\nMvaa 44,164,204\nRa 156,2,23552\nMvaa 44,160,196\nAD 85,85,83\nCV 79,82,83\nMvaa 44,176,228\nRa 160,2,24576\nv_max_f32 v90, v84, v85\n"
"Mvaa 44,180,236\ns_add_u32 s88, s88, 32768\nEX 80,32\nMvaa 44,184,244\nRa 164,2,25600\nv_cmp_lt_f32 vcc, s63, v90\nMvaa 44,188,252\nWL 8\nEX 81,33\nMvaa 40,188,248\n"
"Ra 168,2,26624\ns_cbranch_vccnz .T258\n.T259:\nMvaa 40,184,240\nD1 1,16,46,2048\nMvaa 40,180,232\nRa 172,2,27648\nAD 86,86,84\nMvaa 40,176,224\nAD 84,80,81\nCV 64,80,81\n"
"Maav 0,128,72\nRa 176,2,28672\nMaav 4,128,76\nEX 82,34\nMaav 12,132,76\nRa 180,2,29696\nMaav 8,132,72\ns_waitcnt vmcnt(12) lgkmcnt(8)\nEX 83,35\nMaav 16,136,72\nRa 184,2,30720\n"
"AD 84,84,82\nMaav 20,136,76\nD1 1,16,46,3072\ns_add_u32 m0, s89, 49152\nMaav 28,140,76\nRa 188,2,31744\ns_barrier\nAR 98,0\nAR 99,1\nCV 100,98,99\nMaav 24,140,72\nAD 84,84,83\n"
"CV 65,82,83\nAR 98,2\nAR 99,3\nCV 101,98,99\nMaav 32,144,72\nRa 128,24,32768\nAR 98,8\nAR 99,9\nCV 102,98,99\nAR 98,10\nMaav 36,144,76\ns_add_u32 s46, s87, s88\nEX 80,36\n"
"AR 99,11\nMaav 44,148,76\nRa 132,24,33792\nCV 103,98,99\ns_add_u32 s5, s66, 0\nSv 100,1,16,5,0\nAR 98,4\nMaav 40,148,72\nWL 8\nEX 81,37\nAR 99,5\nCV 104,98,99\nMaav 48,152,72\n"
"Ra 136,24,34816\nAD 87,87,85\nAR 98,6\nAR 99,7\nCV 105,98,99\nMaav 52,152,76\nD1 1,16,46,0\nv_mov_b32 v96, v86\nv_mov_b32 v97, v87\nAR 98,12\nAR 99,13\nMaav 60,156,76\n"
"Ra 140,24,35840\nv_permlane32_swap_b32 v86, v96\nCV 106,98,99\nAR 98,14\nAR 99,15\nMaav 56,156,72\nAD 85,80,81\nCV 68,80,81\nCV 107,98,99\ns_add_u32 s5, s66, 0\n"
"Sv 104,1,16,5,1024\nMaav 64,160,72\nRa 144,26,32768\nv_permlane32_swap_b32 v87, v97\nAD 86,86,96\nAR 98,16\nAR 99,17\nMaav 68,160,76\ns_cmp_eq_u32 s99, s93\nEX 82,38\n"
"Maav 76,164,76\nRa 148,26,33792\ns_cbranch_scc1 .E252\n.E253:\nCV 108,98,99\nAR 98,18\nAR 99,19\nMaav 72,164,72\nWL 8\nEX 83,39\nCV 109,98,99\nAR 98,24\nMaav 80,168,72\n"
"Ra 152,26,34816\nAD 85,85,82\nAD 87,87,97\nAR 99,25\nCV 110,98,99\nMaav 84,168,76\nD1 1,16,46,1024\nv_mov_b32 v96, v86\nAR 98,26\nAR 99,27\nCV 111,98,99\nMaav 92,172,76\n"
"Ra 156,26,35840\nv_mov_b32 v97, v87\ns_add_u32 s5, s66, 0\nSv 108,1,16,5,2048\nAR 98,20\nMaav 88,172,72\nAD 85,85,83\nCV 69,82,83\nv_permlane16_swap_b32 v86, v96\nAR 99,21\n"
"CV 100,98,99\nMaav 96,176,72\nRa 160,24,40960\nv_permlane16_swap_b32 v87, v97\nAR 98,22\nAR 99,23\nCV 101,98,99\nMaav 100,176,76\ns_xor_b32 s89, s89, 0x10000\nEX 80,40\nAR 98,28\n"
"Maav 108,180,76\nRa 164,24,41984\nAD 86,86,96\nAD 87,87,97\nAR 99,29\nCV 102,98,99\nMaav 104,180,72\nWL 6\nEX 81,41\nAR 98,30\nAR 99,31\nMaav 112,184,72\nRa 168,24,43008\n"
"AD 84,84,80\nCV 103,98,99\ns_add_u32 s5, s66, 0\nSv 100,1,16,5,3072\nMaav 116,184,76\nD1 1,16,46,2048\nv_cmp_gt_u32 vcc, 0x100, v1\nv_mov_b32 v94, s15\nv_mov_b32 v95, s15\n"
"v_cndmask_b32 v92, v89, v88, vcc\nv_cndmask_b32 v93, v87, v86, vcc\nMaav 124,188,76\nRa 172,24,44032\nv_mov_b32 v86, 0\nAR 98,32\nAR 99,33\nCV 104,98,99\nMaav 120,188,72\n"
"AD 84,84,81\nCV 66,80,81\nAR 98,34\nAR 99,35\nCV 105,98,99\nCvaa 48,128,192,4\nRa 176,26,40960\nv_mov_b32 v87, 0\nv_mov_b32 v88, 0\nAR 98,40\nAR 99,41\nMvaa 48,132,200\n"
"s_cmp_eq_u32 s99, 1\nEX 82,42\nMvaa 48,136,208\nRa 180,26,41984\ns_cselect_b32 s86, s94, s86\nCV 106,98,99\nAR 98,42\nAR 99,43\nMvaa 48,140,216\nWL 6\nEX 83,43\nCV 107,98,99\n"
"Cvaa 52,140,220,8\nRa 184,26,43008\nAD 84,84,82\nv_mov_b32 v89, 0\ns_add_u32 s5, s66, 4096\nSv 104,1,16,5,0\nMvaa 52,136,212\nD1 1,16,46,3072\ns_add_u32 m0, s89, 0\nAR 98,36\n"
"AR 99,37\nCV 108,98,99\nMvaa 52,132,204\nRa 188,26,44032\ns_mov_b32 s64, 0x2b800000\nAR 98,38\nAR 99,39\nCV 109,98,99\nMvaa 52,128,196\nAD 84,84,83\nCV 67,82,83\nAR 98,44\n"
"AR 99,45\nCV 110,98,99\nMvaa 52,144,228\nRa 128,2,49152\nAR 98,46\nAR 99,47\nCV 111,98,99\nMvaa 52,148,236\ns_add_u32 s46, s86, s88\nEX 80,44\nMvaa 52,152,244\nRa 132,2,50176\n"
"v_xor_b32 v24, 0x10000, v24\ns_add_u32 s5, s66, 4096\nSv 108,1,16,5,1024\nAR 98,48\nMvaa 52,156,252\nWL 6\nEX 81,45\nAR 99,49\nCV 100,98,99\nMvaa 48,156,248\nRa 136,2,51200\n"
"AD 85,85,80\nAR 98,50\nAR 99,51\nCV 101,98,99\nMvaa 48,152,240\nD1 1,16,46,0\nAR 98,56\nAR 99,57\nCV 102,98,99\nAR 98,58\nMvaa 48,148,232\nRa 140,2,52224\nAR 99,59\nCV 103,98,99\n"
"s_add_u32 s5, s66, 4096\nSv 100,1,16,5,2048\nMvaa 48,144,224\nAD 85,85,81\nCV 70,80,81\nAR 98,52\nAR 99,53\nCV 104,98,99\nCvaa 56,160,192,12\nRa 144,2,53248\nAR 98,54\nAR 99,55\n"
"CV 105,98,99\nAR 98,60\nMvaa 56,164,200\nEX 82,46\nAR 99,61\nCV 106,98,99\nMvaa 56,168,208\nRa 148,2,54272\nv_xor_b32 v26, 0x10000, v26\nAR 98,62\nAR 99,63\nCV 107,98,99\n"
"Mvaa 56,172,216\nWL 6\nEX 83,47\ns_add_u32 s5, s66, 4096\nSv 104,1,16,5,3072\nCvaa 60,172,220,16\nRa 152,2,55296\nAD 85,85,82\nAR 98,64\nAR 99,65\nCV 108,98,99\nMvaa 60,168,212\n"
"D1 1,16,46,1024\nAR 98,66\nAR 99,67\nCV 109,98,99\nAR 98,72\nMvaa 60,164,204\nRa 156,2,56320\nAR 99,73\nCV 110,98,99\nAR 98,74\nAR 99,75\nMvaa 60,160,196\nAD 85,85,83\n"
"CV 71,82,83\nCV 111,98,99\ns_add_u32 s5, s66, 16384\nSv 108,1,16,5,0\nMvaa 60,176,228\nRa 160,2,57344\nv_max_f32 v90, v84, v85\nAR 98,68\nAR 99,69\nCV 100,98,99\nMvaa 60,180,236\n"
"s_add_u32 s88, s88, 32768\nEX 80,48\nAR 98,70\nMvaa 60,184,244\nRa 164,2,58368\nv_cmp_lt_f32 vcc, s63, v90\nMvaa 60,188,252\nWL 8\nEX 81,49\nMvaa 56,188,248\nRa 168,2,59392\n"
"s_cbranch_vccnz .T260\n.T261:\nv_mov_b32 v20, v84\nv_mov_b32 v21, v85\nv_mov_b32 v22, v20\nv_mov_b32 v23, v21\nMvaa 56,184,240\nD1 1,16,46,2048\nv_permlane32_swap_b32 v20, v22\n"
"v_permlane32_swap_b32 v21, v23\nAD 20,20,22\nAD 21,21,23\nv_mov_b32 v22, v20\nv_mov_b32 v23, v21\nMvaa 56,180,232\nRa 172,2,60416\nv_permlane16_swap_b32 v20, v22\n"
"v_permlane16_swap_b32 v21, v23\nAD 20,20,22\nAD 21,21,23\nv_min_f32 v90, v20, v21\nv_cmp_gt_f32 vcc, s64, v90\ns_nop 0\ns_cbranch_vccnz .T262\n.T263:\nAD 86,86,84\n"
"Mvaa 56,176,224\nAD 84,80,81\nCV 72,80,81\nAR 99,71\nCV 101,98,99\nAR 98,76\nZvav 128,128,64\nRa 176,2,61440\nAR 99,77\nCV 102,98,99\nAR 98,78\nAR 99,79\nZvav 132,128,68\n"
"EX 82,50\nCV 103,98,99\nZvav 140,132,68\nRa 180,2,62464\ns_add_u32 s5, s66, 16384\nSv 100,1,16,5,1024\nAR 98,80\nAR 99,81\nZvav 136,132,64\ns_waitcnt vmcnt(21) lgkmcnt(8)\n"
"EX 83,51\nCV 104,98,99\nAR 98,82\nZvav 144,136,64\nRa 184,2,63488\nAD 84,84,82\nAR 99,83\nCV 105,98,99\nAR 98,88\nZvav 148,136,68\nD1 1,16,46,3072\ns_add_u32 m0, s89, 16384\n"
"AR 99,89\nCV 106,98,99\nAR 98,90\nZvav 156,140,68\nRa 188,2,64512\ns_barrier\nAR 99,91\nCV 107,98,99\nZvav 152,140,64\nAD 84,84,83\nCV 73,82,83\ns_add_u32 s5, s66, 16384\n"
"Sv 104,1,16,5,2048\nAR 98,84\nZvav 160,144,64\nRa 128,24,0\nAR 99,85\nCV 108,98,99\nAR 98,86\nAR 99,87\nZvav 164,144,68\ns_add_u32 s46, s87, s88\nEX 80,52\nCV 109,98,99\n"
"Zvav 172,148,68\nRa 132,24,1024\nv_xor_b32 v2, 0x10000, v2\nAR 98,92\nAR 99,93\nCV 110,98,99\nZvav 168,148,64\nWL 8\nEX 81,53\nAR 98,94\nAR 99,95\nZvav 176,152,64\nRa 136,24,2048\n"
"AD 87,87,85\nCV 111,98,99\ns_add_u32 s5, s66, 16384\nSv 108,1,16,5,3072\nZvav 180,152,68\nD1 1,16,46,0\nAR 98,96\nAR 99,97\nCV 100,98,99\nAR 98,98\nZvav 188,156,68\n"
"Ra 140,24,3072\nAR 99,99\nCV 101,98,99\nAR 98,104\nAR 99,105\nZvav 184,156,64\nAD 85,80,81\nCV 76,80,81\nCV 102,98,99\nAR 98,106\nAR 99,107\nZvav 192,160,64\nRa 144,26,0\n"
"CV 103,98,99\ns_add_u32 s5, s66, 20480\nSv 100,1,16,5,0\nAR 98,100\nZvav 196,160,68\ns_cmp_eq_u32 s99, s92\nEX 82,54\nZvav 204,164,68\nRa 148,26,1024\ns_cbranch_scc1 .E254\n"
".E255:\nAR 99,101\nCV 104,98,99\nAR 98,102\nZvav 200,164,64\nWL 8\nEX 83,55\nAR 99,103\nCV 105,98,99\nZvav 208,168,64\nRa 152,26,2048\nAD 85,85,82\nAR 98,108\nAR 99,109\n"
"CV 106,98,99\nZvav 212,168,68\nD1 1,16,46,1024\nAR 98,110\nAR 99,111\nCV 107,98,99\nZvav 220,172,68\nRa 156,26,3072\ns_add_u32 s5, s66, 20480\nSv 104,1,16,5,1024\nAR 98,112\n"
"AR 99,113\nZvav 216,172,64\nAD 85,85,83\nCV 77,82,83\nCV 108,98,99\nAR 98,114\nAR 99,115\nZvav 224,176,64\nRa 160,24,8192\nCV 109,98,99\nAR 98,120\nAR 99,121\nCV 110,98,99\n"
"Zvav 228,176,68\ns_sub_u32 s90, s90, 1\nEX 80,56\nZvav 236,180,68\nRa 164,24,9216\ns_cbranch_scc1 .E256\n.E257:\nAR 98,122\nAR 99,123\nCV 111,98,99\nZvav 232,180,64\nWL 6\n"
"EX 81,57\ns_add_u32 s5, s66, 20480\nSv 108,1,16,5,2048\nZvav 240,184,64\nRa 168,24,10240\nAD 84,84,80\nAR 98,116\nAR 99,117\nCV 100,98,99\nZvav 244,184,68\nD1 1,16,46,2048\n"
"AR 98,118\nAR 99,119\nCV 101,98,99\nAR 98,124\nZvav 252,188,68\nRa 172,24,11264\nAR 99,125\nCV 102,98,99\nAR 98,126\nAR 99,127\nZvav 248,188,64\nAD 84,84,81\nCV 74,80,81\n"
"CV 103,98,99\ns_add_u32 s5, s66, 20480\nSv 100,1,16,5,3072\ns_sub_u32 s99, s99, 1\n.p2align 6\n.A264:\nCvaa 32,128,192,4\nRa 176,26,8192\nMvaa 32,132,200\nEX 82,58\n"
"Mvaa 32,136,208\nRa 180,26,9216\nMvaa 32,140,216\nWL 6\nEX 83,59\nCvaa 36,140,220,8\nRa 184,26,10240\nAD 84,84,82\nMvaa 36,136,212\nD1 1,16,46,3072\ns_add_u32 m0, s89, 32768\n"
"Mvaa 36,132,204\nRa 188,26,11264\nMvaa 36,128,196\nAD 84,84,83\nCV 75,82,83\nMvaa 36,144,228\nRa 128,2,16384\nMvaa 36,148,236\ns_add_u32 s46, s86, s88\nEX 80,60\nMvaa 36,152,244\n"
"Ra 132,2,17408\nMvaa 36,156,252\nWL 6\nEX 81,61\nMvaa 32,156,248\nRa 136,2,18432\nAD 85,85,80\nMvaa 32,152,240\nD1 1,16,46,0\nMvaa 32,148,232\nRa 140,2,19456\nSv 112,1,16,49,0\n"
"Mvaa 32,144,224\nAD 85,85,81\nCV 78,80,81\nCvaa 40,160,192,12\nRa 144,2,20480\nMvaa 40,164,200\ns_cmp_eq_u32 s99, 0\nEX 82,62\nMvaa 40,168,208\nRa 148,2,21504\n"
"s_cselect_b32 s87, s95, s87\nMvaa 40,172,216\nWL 6\nEX 83,63\nCvaa 44,172,220,16\nRa 152,2,22528\nAD 85,85,82\nMvaa 44,168,212\nD1 1,16,46,1024\nMvaa 44,164,204\nRa 156,2,23552\n"
"Mvaa 44,160,196\nAD 85,85,83\nCV 79,82,83\nMvaa 44,176,228\nRa 160,2,24576\nv_max_f32 v90, v84, v85\nMvaa 44,180,236\ns_add_u32 s88, s88, 32768\nEX 80,32\nMvaa 44,184,244\n"
"Ra 164,2,25600\nv_cmp_lt_f32 vcc, s63, v90\nMvaa 44,188,252\nWL 8\nEX 81,33\nMvaa 40,188,248\nRa 168,2,26624\ns_cbranch_vccnz .T273\n.T274:\nMvaa 40,184,240\nD1 1,16,46,2048\n"
"Mvaa 40,180,232\nRa 172,2,27648\nAD 86,86,84\nMvaa 40,176,224\nAD 84,80,81\nCV 64,80,81\nMvav 128,128,72\nRa 176,2,28672\nMvav 132,128,76\ns_cmp_eq_u32 s99, s98\nEX 82,34\n"
"Mvav 140,132,76\nRa 180,2,29696\ns_cbranch_scc1 .E271\n.E272:\nMvav 136,132,72\ns_waitcnt vmcnt(13) lgkmcnt(8)\nEX 83,35\nMvav 144,136,72\nRa 184,2,30720\nAD 84,84,82\n"
"Mvav 148,136,76\nD1 1,16,46,3072\ns_add_u32 m0, s89, 49152\nMvav 156,140,76\nRa 188,2,31744\ns_barrier\nMvav 152,140,72\nAD 84,84,83\nCV 65,82,83\nMvav 160,144,72\n"
"Ra 128,24,32768\nMvav 164,144,76\ns_add_u32 s46, s87, s88\nEX 80,36\nMvav 172,148,76\nRa 132,24,33792\nMvav 168,148,72\nWL 8\nEX 81,37\nMvav 176,152,72\nRa 136,24,34816\n"
"AD 87,87,85\nMvav 180,152,76\nD1 1,16,46,0\nMvav 188,156,76\nRa 140,24,35840\nMvav 184,156,72\nAD 85,80,81\nCV 68,80,81\nMvav 192,160,72\nRa 144,26,32768\nMvav 196,160,76\n"
"s_cmp_eq_u32 s99, s93\nEX 82,38\nMvav 204,164,76\nRa 148,26,33792\ns_cbranch_scc1 .E265\n.E266:\nMvav 200,164,72\nWL 8\nEX 83,39\nMvav 208,168,72\nRa 152,26,34816\nAD 85,85,82\n"
"Mvav 212,168,76\nD1 1,16,46,1024\nMvav 220,172,76\nRa 156,26,35840\nMvav 216,172,72\nAD 85,85,83\nCV 69,82,83\nMvav 224,176,72\nRa 160,24,40960\nMvav 228,176,76\n"
"s_xor_b32 s89, s89, 0x10000\nEX 80,40\nMvav 236,180,76\nRa 164,24,41984\nMvav 232,180,72\nWL 6\nEX 81,41\nMvav 240,184,72\nRa 168,24,43008\nAD 84,84,80\nMvav 244,184,76\n"
"D1 1,16,46,2048\nMvav 252,188,76\nRa 172,24,44032\nMvav 248,188,72\nAD 84,84,81\nCV 66,80,81\nCvaa 48,128,192,4\nRa 176,26,40960\nMvaa 48,132,200\ns_cmp_eq_u32 s99, 1\nEX 82,42\n"
"Mvaa 48,136,208\nRa 180,26,41984\ns_cselect_b32 s86, s94, s86\nMvaa 48,140,216\nWL 6\nEX 83,43\nCvaa 52,140,220,8\nRa 184,26,43008\nAD 84,84,82\nMvaa 52,136,212\nD1 1,16,46,3072\n"
"s_add_u32 m0, s89, 0\nMvaa 52,132,204\nRa 188,26,44032\nMvaa 52,128,196\nAD 84,84,83\nCV 67,82,83\nMvaa 52,144,228\nRa 128,2,49152\nMvaa 52,148,236\ns_add_u32 s46, s86, s88\n"
"EX 80,44\nMvaa 52,152,244\nRa 132,2,50176\nv_xor_b32 v24, 0x10000, v24\nMvaa 52,156,252\nWL 6\nEX 81,45\nMvaa 48,156,248\nRa 136,2,51200\nAD 85,85,80\nMvaa 48,152,240\n"
"D1 1,16,46,0\nMvaa 48,148,232\nRa 140,2,52224\nSv 112,1,16,49,1024\nMvaa 48,144,224\nAD 85,85,81\nCV 70,80,81\nCvaa 56,160,192,12\nRa 144,2,53248\nMvaa 56,164,200\n"
"s_add_u32 s49, s49, 131072\nEX 82,46\nMvaa 56,168,208\nRa 148,2,54272\nv_xor_b32 v26, 0x10000, v26\nMvaa 56,172,216\nWL 6\nEX 83,47\nCvaa 60,172,220,16\nRa 152,2,55296\n"
"AD 85,85,82\nMvaa 60,168,212\nD1 1,16,46,1024\nMvaa 60,164,204\nRa 156,2,56320\nMvaa 60,160,196\nAD 85,85,83\nCV 71,82,83\nMvaa 60,176,228\nRa 160,2,57344\n"
"v_max_f32 v90, v84, v85\nMvaa 60,180,236\ns_add_u32 s88, s88, 32768\nEX 80,48\nMvaa 60,184,244\nRa 164,2,58368\nv_cmp_lt_f32 vcc, s63, v90\nMvaa 60,188,252\nWL 8\nEX 81,49\n"
"Mvaa 56,188,248\nRa 168,2,59392\ns_cbranch_vccnz .T275\n.T276:\nMvaa 56,184,240\nD1 1,16,46,2048\nMvaa 56,180,232\nRa 172,2,60416\nAD 86,86,84\nMvaa 56,176,224\nAD 84,80,81\n"
"CV 72,80,81\nMvav 128,128,64\nRa 176,2,61440\nMvav 132,128,68\nEX 82,50\nMvav 140,132,68\nRa 180,2,62464\nMvav 136,132,64\ns_waitcnt vmcnt(13) lgkmcnt(8)\nEX 83,51\n"
"Mvav 144,136,64\nRa 184,2,63488\nAD 84,84,82\nMvav 148,136,68\nD1 1,16,46,3072\ns_add_u32 m0, s89, 16384\nMvav 156,140,68\nRa 188,2,64512\ns_barrier\nMvav 152,140,64\nAD 84,84,83\n"
"CV 73,82,83\nMvav 160,144,64\nRa 128,24,0\nMvav 164,144,68\ns_add_u32 s46, s87, s88\nEX 80,52\nMvav 172,148,68\nRa 132,24,1024\nv_xor_b32 v2, 0x10000, v2\nMvav 168,148,64\nWL 8\n"
"EX 81,53\nMvav 176,152,64\nRa 136,24,2048\nAD 87,87,85\nMvav 180,152,68\nD1 1,16,46,0\nMvav 188,156,68\nRa 140,24,3072\nMvav 184,156,64\nAD 85,80,81\nCV 76,80,81\nMvav 192,160,64\n"
"Ra 144,26,0\nMvav 196,160,68\ns_cmp_eq_u32 s99, s92\nEX 82,54\nMvav 204,164,68\nRa 148,26,1024\ns_cbranch_scc1 .E267\n.E268:\nMvav 200,164,64\nWL 8\nEX 83,55\nMvav 208,168,64\n"
"Ra 152,26,2048\nAD 85,85,82\nMvav 212,168,68\nD1 1,16,46,1024\nMvav 220,172,68\nRa 156,26,3072\nMvav 216,172,64\nAD 85,85,83\nCV 77,82,83\nMvav 224,176,64\nRa 160,24,8192\n"
"Mvav 228,176,68\ns_sub_u32 s90, s90, 1\nEX 80,56\nMvav 236,180,68\nRa 164,24,9216\ns_cbranch_scc1 .E269\n.E270:\nMvav 232,180,64\nWL 6\nEX 81,57\nMvav 240,184,64\nRa 168,24,10240\n"
"AD 84,84,80\nMvav 244,184,68\nD1 1,16,46,2048\ns_sub_u32 s99, s99, 1\nMvav 252,188,68\nRa 172,24,11264\nMvav 248,188,64\nAD 84,84,81\nCV 74,80,81\ns_cbranch_scc0 .A264\nEX 82,58\n"
"EX 83,59\nAD 84,84,82\nD1 1,16,46,3072\ns_add_u32 m0, s89, 32768\ns_add_u32 s46, s86, s88\nAD 84,84,83\nCV 75,82,83\nRa 128,2,16384\nv_xor_b32 v3, 0x10000, v2\nEX 80,60\n"
"Ra 132,2,17408\nv_add_u32 v0, 0x20000, v1\nEX 81,61\nRa 136,2,18432\nAD 85,85,80\nD1 1,16,46,0\ns_sub_u32 s94, s49, 1966080\nRa 140,2,19456\ns_add_u32 s95, s94, 1048576\n"
"AD 85,85,81\nCV 78,80,81\nRa 144,2,20480\ns_xor_b32 s92, s94, 0x202000\nEX 82,62\nRa 148,2,21504\ns_add_u32 s93, s96, 983040\nEX 83,63\nRa 152,2,22528\nAD 85,85,82\n"
"D1 1,16,46,1024\ns_xor_b32 s93, s93, 0x202000\nRa 156,2,23552\ns_lshl_b32 s89, s11, 12\nAD 85,85,83\nCV 79,82,83\nRa 160,2,24576\nv_max_f32 v90, v84, v85\n"
"v_cmp_lt_f32 vcc, s63, v90\nRa 164,2,25600\ns_cbranch_vccnz .T277\n.T278:\nAD 86,86,84\nAD 87,87,85\nWL 8\nv_mov_b32 v84, v86\nv_mov_b32 v85, v87\nRa 168,2,26624\n"
"v_permlane32_swap_b32 v86, v84\nv_permlane32_swap_b32 v87, v85\nD1 1,16,46,2048\nAD 86,86,84\nRa 172,2,27648\nAD 87,87,85\nv_mov_b32 v84, v86\nv_mov_b32 v85, v87\nMvav 128,128,72\n"
"Ra 176,2,28672\nv_permlane16_swap_b32 v86, v84\nv_permlane16_swap_b32 v87, v85\nMvav 132,128,76\nAD 86,86,84\nAD 87,87,85\nMvav 140,132,76\nRa 180,2,29696\nv_rcp_f32 v86, v86\n"
"Mvav 136,132,72\nWL 8\nv_rcp_f32 v87, v87\nMvav 144,136,72\nRa 184,2,30720\nMvav 148,136,76\nD1 1,16,46,3072\nMvav 156,140,76\nRa 188,2,31744\nMU 128,128,86\nMU 129,129,86\n"
"Mvav 152,140,72\nCV 96,128,129\nMU 130,130,86\nMU 131,131,86\nMvav 160,144,72\nRa 128,2,0\nCV 97,130,131\nMU 132,132,87\nMvav 164,144,76\nMU 133,133,87\nCV 100,132,133\n"
"MU 134,134,87\nMvav 172,148,76\nRa 132,2,1024\nMU 135,135,87\nCV 101,134,135\nMvav 168,148,72\nWL 8\ns_add_u32 m0, s89, 131072\ns_add_u32 s46, s93, 0\nMvav 176,152,72\n"
"Ra 136,2,2048\nMU 136,136,86\nMU 137,137,86\nMvav 180,152,76\nD1 1,16,46,0\nCV 98,136,137\nMU 138,138,86\nMvav 188,156,76\nRa 140,2,3072\nMU 139,139,86\nCV 99,138,139\n"
"Mvav 184,156,72\ns_add_u32 s5, s94, 0\nSv 96,1,16,5,0\nMU 140,140,87\nMvav 192,160,72\nRa 144,2,4096\nMU 141,141,87\nCV 102,140,141\nMvav 196,160,76\nMU 142,142,87\nMU 143,143,87\n"
"CV 103,142,143\nMvav 204,164,76\nRa 148,2,5120\nSv 100,1,16,5,1024\nMU 144,144,86\nMvav 200,164,72\nWL 8\nMU 145,145,86\nCV 104,144,145\nMvav 208,168,72\nRa 152,2,6144\n"
"MU 146,146,86\nMU 147,147,86\nMvav 212,168,76\nD1 1,16,46,1024\nCV 105,146,147\nMU 148,148,87\nMvav 220,172,76\nRa 156,2,7168\nMU 149,149,87\nCV 108,148,149\nMvav 216,172,72\n"
"MU 150,150,87\nMU 151,151,87\nCV 109,150,151\nMvav 224,176,72\nRa 160,2,8192\nMU 152,152,86\nMU 153,153,86\nMvav 228,176,76\nCV 106,152,153\nMU 154,154,86\nMU 155,155,86\n"
"Mvav 236,180,76\nRa 164,2,9216\nCV 107,154,155\ns_add_u32 s5, s94, 131072\nMvav 232,180,72\nWL 8\nSv 104,1,16,5,0\nMU 156,156,87\nMvav 240,184,72\nRa 168,2,10240\nMU 157,157,87\n"
"CV 110,156,157\nMvav 244,184,76\nD1 1,16,46,2048\nMU 158,158,87\nMU 159,159,87\nMvav 252,188,76\nRa 172,2,11264\nCV 111,158,159\nSv 108,1,16,5,1024\nMvav 248,188,72\n"
"MU 160,160,86\nMU 161,161,86\nZaav 0,128,96\nRa 176,2,12288\nCV 112,160,161\nMU 162,162,86\nMaav 0,132,104\nMU 163,163,86\nCV 113,162,163\nZaav 4,132,108\nRa 180,2,13312\n"
"MU 164,164,87\nMaav 4,128,100\ns_waitcnt vmcnt(18) lgkmcnt(8)\nMU 165,165,87\nZaav 12,136,100\nRa 184,2,14336\nCV 116,164,165\nMaav 12,140,108\nD1 1,16,46,3072\nMU 166,166,87\n"
"Zaav 8,140,104\nRa 188,2,15360\ns_barrier\nMaav 8,136,96\nMU 167,167,87\nCV 117,166,167\nZaav 16,144,96\nRa 128,2,32768\nMU 168,168,86\nMU 169,169,86\nv_lshrrev_b32 v88, 8, v1\n"
"v_lshlrev_b32 v89, 5, v88\nMaav 16,148,104\nCV 114,168,169\nMU 170,170,86\nbuffer_load_dwordx4 v[64:67], v89, s[16:19], s40 offen offset:0 sc0 sc1\n"
"buffer_store_dword v25, off, s[16:19], s54 offset:512 sc0 sc1\nZaav 20,148,108\nRa 132,2,33792\nMU 171,171,86\nMaav 20,144,100\nWL 8\ns_add_u32 m0, s89, 49152\n"
"s_add_u32 s46, s96, 1179648\nZaav 28,152,100\nRa 136,2,34816\nCV 115,170,171\nMaav 28,156,108\nD1 1,16,46,0\ns_add_u32 s5, s94, 262144\n"
"buffer_load_dwordx4 v[68:71], v89, s[16:19], s40 offen offset:16 sc0 sc1\nZaav 24,156,104\nRa 140,2,35840\nSv 112,1,16,5,0\nMaav 24,152,96\nMU 172,172,87\nMU 173,173,87\n"
"Zaav 32,160,96\nRa 144,2,36864\nCV 118,172,173\nMU 174,174,87\nMaav 32,164,104\nMU 175,175,87\nCV 119,174,175\n"
"buffer_load_dwordx4 v[72:75], v89, s[16:19], s40 offen offset:1792 sc0 sc1\nZaav 36,164,108\nRa 148,2,37888\nSv 116,1,16,5,1024\nMaav 36,160,100\nWL 8\nMU 176,176,86\n"
"Zaav 44,168,100\nRa 152,2,38912\nMU 177,177,86\nMaav 44,172,108\nD1 1,16,46,1024\nCV 120,176,177\nbuffer_load_dwordx4 v[76:79], v89, s[16:19], s40 offen offset:1808 sc0 sc1\n"
"Zaav 40,172,104\nRa 156,2,39936\nMU 178,178,86\nMaav 40,168,96\nMU 179,179,86\nCV 121,178,179\nZaav 48,176,96\nRa 160,2,40960\nMU 180,180,87\nMU 181,181,87\nMaav 48,180,104\n"
"CV 124,180,181\nMU 182,182,87\nv_bfe_u32 v90, v1, 4, 4\nv_mul_u32_u24 v91, 896, v90\nv_lshl_add_u32 v91, v88, 4, v91\nZaav 52,180,108\nRa 164,2,41984\nMU 183,183,87\n"
"buffer_load_dwordx4 v[12:15], v91, s[28:31], s22 offen offset:0\nMaav 52,176,100\nWL 8\nCV 125,182,183\nZaav 60,184,100\nRa 168,2,43008\nMU 184,184,86\nMaav 60,188,108\n"
"D1 1,16,46,2048\nMU 185,185,86\nZaav 56,188,104\nRa 172,2,44032\nCV 122,184,185\nbuffer_load_dwordx4 v[16:19], v91, s[28:31], s23 offen offset:0\nv_add_u32 v90, s21, v90\n"
"v_lshlrev_b32 v88, 2, v90\nMaav 56,184,96\nMU 186,186,86\nMU 187,187,86\nv_lshrrev_b32 v92, 8, v1\nv_bfe_u32 v93, v1, 6, 2\nZaav 64,128,96\nRa 176,2,45056\nCV 123,186,187\n"
"s_add_u32 s5, s94, 393216\nMaav 64,132,104\nSv 120,1,16,5,0\nMU 188,188,87\nZaav 68,132,108\nRa 180,2,46080\nMU 189,189,87\nMaav 68,128,100\nWL 8\nCV 126,188,189\nZaav 76,136,100\n"
"Ra 184,2,47104\nMU 190,190,87\nMaav 76,140,108\nD1 1,16,46,3072\nMU 191,191,87\nZaav 72,140,104\nRa 188,2,48128\nCV 127,190,191\nv_cmp_eq_u32 vcc, v92, v93\nv_and_b32 v92, 16, v1\n"
"v_mov_b32 v93, 0x3f80\nv_cndmask_b32 v93, 0, v93, vcc\nMaav 72,136,96\nSv 124,1,16,5,1024\nv_lshlrev_b32 v93, v92, v93\nv_and_b32 v92, 32, v1\nZaav 80,144,96\nRa 128,2,49152\n"
"MU 192,192,86\nMU 193,193,86\nMaav 80,148,104\nCV 32,192,193\nMU 194,194,86\nZaav 84,148,108\nRa 132,2,50176\nMU 195,195,86\nMaav 84,144,100\nWL 8\ns_add_u32 m0, s89, 147456\n"
"s_add_u32 s46, s93, 32768\nZaav 92,152,100\nRa 136,2,51200\nCV 33,194,195\nMaav 92,156,108\nD1 1,16,46,0\nMU 196,196,87\nZaav 88,156,104\nRa 140,2,52224\nMU 197,197,87\n"
"Maav 88,152,96\nCV 36,196,197\nMU 198,198,87\nZaav 96,160,96\nRa 144,2,53248\nMU 199,199,87\nCV 37,198,199\nMaav 96,164,104\nMU 200,200,86\nMU 201,201,86\nZaav 100,164,108\n"
"Ra 148,2,54272\nCV 34,200,201\nMaav 100,160,100\nWL 8\nMU 202,202,86\nZaav 108,168,100\nRa 152,2,55296\nMU 203,203,86\nMaav 108,172,108\nD1 1,16,46,1024\nCV 35,202,203\n"
"Zaav 104,172,104\nRa 156,2,56320\ns_add_u32 s5, s94, 524288\nv_cmp_eq_u32 vcc, 0, v92\nv_mov_b32 v4, 0\nv_mov_b32 v5, 0\nv_cndmask_b32 v6, 0, v93, vcc\nMaav 104,168,96\n"
"Sv 32,1,16,5,0\nMU 204,204,87\nv_sub_u32 v7, v93, v6\nv_mov_b32 v8, 0\nv_mov_b32 v9, 0\nMaav 0,176,112\nRa 160,2,57344\nMU 205,205,87\nCV 38,204,205\nMaav 0,180,120\n"
"MU 206,206,87\nMU 207,207,87\nMaav 4,180,124\nRa 164,2,58368\nCV 39,206,207\nMaav 4,176,116\nWL 8\nSv 36,1,16,5,1024\nMaav 12,184,116\nRa 168,2,59392\nMU 208,208,86\n"
"Maav 12,188,124\nD1 1,16,46,2048\nMU 209,209,86\nMaav 8,188,120\nRa 172,2,60416\nCV 40,208,209\nMaav 8,184,112\nMU 210,210,86\nMU 211,211,86\nMaav 16,128,112\nRa 176,2,61440\n"
"CV 41,210,211\nMU 212,212,87\nMaav 16,132,120\nMU 213,213,87\nCV 44,212,213\nMaav 20,132,124\nRa 180,2,62464\nMU 214,214,87\nMaav 20,128,116\ns_waitcnt vmcnt(32) lgkmcnt(8)\n"
"MU 215,215,87\nMaav 28,136,116\nRa 184,2,63488\nCV 45,214,215\nMaav 28,140,124\nD1 1,16,46,3072\nMU 216,216,86\nMaav 24,140,120\nRa 188,2,64512\ns_barrier\nMaav 24,136,112\n"
"MU 217,217,86\nCV 42,216,217\nMaav 32,144,112\nRa 128,3,0\nMU 218,218,86\nMU 219,219,86\nMaav 32,148,120\nCV 43,218,219\ns_add_u32 s5, s94, 655360\nMaav 36,148,124\nRa 132,3,1024\n"
"Sv 40,1,16,5,0\nMaav 36,144,116\nWL 8\nMU 220,220,87\nMaav 44,152,116\nRa 136,3,2048\nMU 221,221,87\nMaav 44,156,124\nCV 46,220,221\nMU 222,222,87\nMaav 40,156,120\nRa 140,3,3072\n"
"MU 223,223,87\nMaav 40,152,112\nCV 47,222,223\nSv 44,1,16,5,1024\nMaav 48,160,112\nRa 144,3,4096\nMU 224,224,86\nMaav 48,164,120\nMU 225,225,86\nCV 48,224,225\n"
"buffer_load_dword v27, off, s[16:19], s60 offset:512 sc0 sc1\nMaav 52,164,124\nRa 148,3,5120\nMU 226,226,86\nMaav 52,160,116\nWL 8\nMU 227,227,86\nMaav 60,168,116\nRa 152,3,6144\n"
"CV 49,226,227\nMaav 60,172,124\nMU 228,228,87\nMU 229,229,87\nMaav 56,172,120\nRa 156,3,7168\nCV 52,228,229\nMaav 56,168,112\nMU 230,230,87\nMU 231,231,87\nMaav 64,176,112\n"
"Ra 160,3,8192\nCV 53,230,231\nMaav 64,180,120\nMU 232,232,86\nMU 233,233,86\nMaav 68,180,124\nRa 164,3,9216\nCV 50,232,233\nMaav 68,176,116\nWL 8\nMU 234,234,86\nMaav 76,184,116\n"
"Ra 168,3,10240\nMU 235,235,86\nMaav 76,188,124\nCV 51,234,235\ns_add_u32 s5, s94, 786432\nMaav 72,188,120\nRa 172,3,11264\nSv 48,1,16,5,0\nMaav 72,184,112\nMU 236,236,87\n"
"MU 237,237,87\nMaav 80,128,112\nRa 176,3,12288\nCV 54,236,237\nMaav 80,132,120\nMU 238,238,87\nMU 239,239,87\nMaav 84,132,124\nRa 180,3,13312\nCV 55,238,239\nMaav 84,128,116\n"
"WL 8\nSv 52,1,16,5,1024\nMaav 92,136,116\nRa 184,3,14336\nMU 240,240,86\nMaav 92,140,124\nMU 241,241,86\nCV 56,240,241\nMaav 88,140,120\nRa 188,3,15360\nMU 242,242,86\n"
"Maav 88,136,112\nMU 243,243,86\nCV 57,242,243\nMaav 96,144,112\nRa 128,3,16384\nMU 244,244,87\nMaav 96,148,120\nMU 245,245,87\nCV 60,244,245\nMaav 100,148,124\nRa 132,3,17408\n"
"MU 246,246,87\nMaav 100,144,116\nWL 8\nMU 247,247,87\nMaav 108,152,116\nRa 136,3,18432\nCV 61,246,247\nMaav 108,156,124\nMU 248,248,86\nMU 249,249,86\nMaav 104,156,120\n"
"Ra 140,3,19456\nCV 58,248,249\nMaav 104,152,112\nMU 250,250,86\nMU 251,251,86\nMaav 0,160,32\nRa 144,3,20480\nCV 59,250,251\nMaav 0,164,40\ns_add_u32 s5, s94, 917504\n"
"Sv 56,1,16,5,0\nMaav 4,164,44\nRa 148,3,21504\nMU 252,252,87\nMaav 4,160,36\nWL 8\nMU 253,253,87\nMaav 12,168,36\nRa 152,3,22528\nCV 62,252,253\nMaav 12,172,44\nMU 254,254,87\n"
"MU 255,255,87\nMaav 8,172,40\nRa 156,3,23552\nCV 63,254,255\nMaav 8,168,32\nSv 60,1,16,5,1024\nMaav 16,176,32\nRa 160,3,24576\nWV 19\nMaav 16,180,40\nv_xor_b32 v92, s15, v66\n"
"Maav 20,180,44\nRa 164,3,25600\nMaav 20,176,36\nWL 8\nMaav 28,184,36\nRa 168,3,26624\nMaav 28,188,44\nv_xor_b32 v93, s15, v70\nMaav 24,188,40\nRa 172,3,27648\nMaav 24,184,32\n"
"v_xor_b32 v94, s15, v74\nMaav 32,128,32\nRa 176,3,28672\nv_or3_b32 v92, v92, v93, v94\nMaav 32,132,40\nv_xor_b32 v93, s15, v78\nMaav 36,132,44\nRa 180,3,29696\nMaav 36,128,36\n"
"s_waitcnt vmcnt(14) lgkmcnt(8)\nMaav 44,136,36\nRa 184,3,30720\nMaav 44,140,44\nv_or_b32 v92, v92, v93\nv_cmp_ne_u32 vcc, 0, v92\ns_nop 0\ns_cbranch_vccnz .S285\n.S286:\n"
"Maav 40,140,40\nRa 188,3,31744\ns_barrier\nMaav 40,136,32\ns_mov_b32 s2, 0xc040000\nMaav 48,144,32\nRa 128,3,32768\nbuffer_load_dword v80, v88, s[16:19], s2 offen sc0 sc1\n"
"Maav 48,148,40\nMaav 52,148,44\nRa 132,3,33792\nMaav 52,144,36\nWL 8\nMaav 60,152,36\nRa 136,3,34816\nMaav 60,156,44\n"
"buffer_load_dword v81, v88, s[16:19], s2 offen offset:64 sc0 sc1\nMaav 56,156,40\nRa 140,3,35840\nMaav 56,152,32\nv_and_b32 v92, 0x7ff, v67\nMaav 64,160,32\nRa 144,3,36864\n"
"v_lshrrev_b32 v93, 16, v67\nWV 6\nv_cmp_ne_u32 vcc, s15, v27\ns_nop 0\ns_cbranch_vccnz .S288\n.S289:\nMaav 64,164,40\ns_add_u32 s4, s92, 0\nMaav 68,164,44\nRa 148,3,37888\n"
"Maav 68,160,36\nWL 8\ns_add_u32 m0, s89, 81920\ns_add_u32 s46, s93, 65536\nbuffer_load_dwordx4 a[192:195], v1, s[16:19], s4 offen offset:0 sc0 sc1\nMaav 76,168,36\nRa 152,3,38912\n"
"Maav 76,172,44\nD1 1,16,46,0\nMaav 72,172,40\nRa 156,3,39936\nMaav 72,168,32\nbuffer_load_dwordx4 a[196:199], v1, s[16:19], s4 offen offset:1024 sc0 sc1\nMaav 80,176,32\n"
"Ra 160,3,40960\nv_add_u32 v94, 16, v90\nMaav 80,180,40\ns_add_u32 s4, s92, 131072\nv_cmp_eq_u32 vcc, v92, v90\ns_nop 1\nv_cndmask_b32 v12, v12, v64, vcc\n"
"v_cndmask_b32 v13, v13, v65, vcc\nv_cndmask_b32 v14, v14, v68, vcc\nv_cndmask_b32 v15, v15, v69, vcc\nMaav 84,180,44\nRa 164,3,41984\nv_cmp_eq_u32 vcc, v93, v90\ns_nop 1\n"
"v_cndmask_b32 v12, v12, v72, vcc\nv_cndmask_b32 v13, v13, v73, vcc\nv_cndmask_b32 v14, v14, v76, vcc\nv_cndmask_b32 v15, v15, v77, vcc\nMaav 84,176,36\nWL 8\n"
"buffer_load_dwordx4 a[200:203], v1, s[16:19], s4 offen offset:0 sc0 sc1\nMaav 92,184,36\nRa 168,3,43008\nv_cmp_eq_u32 vcc, v92, v94\ns_nop 1\nv_cndmask_b32 v16, v16, v64, vcc\n"
"v_cndmask_b32 v17, v17, v65, vcc\nv_cndmask_b32 v18, v18, v68, vcc\nv_cndmask_b32 v19, v19, v69, vcc\nMaav 92,188,44\nD1 1,16,46,1024\nv_cmp_eq_u32 vcc, v93, v94\ns_nop 1\n"
"v_cndmask_b32 v16, v16, v72, vcc\nv_cndmask_b32 v17, v17, v73, vcc\nv_cndmask_b32 v18, v18, v76, vcc\nv_cndmask_b32 v19, v19, v77, vcc\nMaav 88,188,40\nRa 172,3,44032\n"
"Maav 88,184,32\nbuffer_load_dwordx4 a[204:207], v1, s[16:19], s4 offen offset:1024 sc0 sc1\nMaav 96,128,32\nRa 176,3,45056\nMaav 96,132,40\ns_add_u32 s4, s92, 262144\n"
"Maav 100,132,44\nRa 180,3,46080\nMaav 100,128,36\nWL 8\nbuffer_load_dwordx4 a[208:211], v1, s[16:19], s4 offen offset:0 sc0 sc1\nMaav 108,136,36\nRa 184,3,47104\nMaav 108,140,44\n"
"D1 1,16,46,2048\nMaav 104,140,40\nRa 188,3,48128\nMaav 104,136,32\nbuffer_load_dwordx4 a[212:215], v1, s[16:19], s4 offen offset:1024 sc0 sc1\nMaav 0,144,48\nRa 128,3,49152\n"
"Maav 0,148,56\ns_add_u32 s4, s92, 393216\nMaav 4,148,60\nRa 132,3,50176\nMaav 4,144,52\nWL 8\nbuffer_load_dwordx4 a[216:219], v1, s[16:19], s4 offen offset:0 sc0 sc1\n"
"Maav 12,152,52\nRa 136,3,51200\nMaav 12,156,60\nD1 1,16,46,3072\nMaav 8,156,56\nRa 140,3,52224\nMaav 8,152,48\n"
"buffer_load_dwordx4 a[220:223], v1, s[16:19], s4 offen offset:1024 sc0 sc1\nMaav 16,160,48\nRa 144,3,53248\nMaav 16,164,56\nMaav 20,164,60\nRa 148,3,54272\nMaav 20,160,52\nWL 8\n"
"s_add_u32 m0, s89, 65536\ns_add_u32 s46, s93, 98304\nMaav 28,168,52\nRa 152,3,55296\nMaav 28,172,60\nD1 1,16,46,0\nMaav 24,172,56\nRa 156,3,56320\nMaav 24,168,48\nMaav 32,176,48\n"
"Ra 160,3,57344\nMaav 32,180,56\nMaav 36,180,60\nRa 164,3,58368\nMaav 36,176,52\nWL 8\nMaav 44,184,52\nRa 168,3,59392\nMaav 44,188,60\nD1 1,16,46,1024\nMaav 40,188,56\n"
"Ra 172,3,60416\nMaav 40,184,48\nMaav 48,128,48\nRa 176,3,61440\nMaav 48,132,56\nMaav 52,132,60\nRa 180,3,62464\nMaav 52,128,52\nWL 8\nMaav 60,136,52\nRa 184,3,63488\n"
"Maav 60,140,60\nD1 1,16,46,2048\nMaav 56,140,56\nRa 188,3,64512\ns_barrier\nMaav 56,136,48\nMaav 64,144,48\nRa 128,0,0\nMaav 64,148,56\nMaav 68,148,60\nRa 132,0,1024\n"
"Maav 68,144,52\nWL 8\nMaav 76,152,52\nRa 136,0,2048\nMaav 76,156,60\nD1 1,16,46,3072\nMaav 72,156,56\nRa 140,0,3072\nMaav 72,152,48\nMaav 80,160,48\nRa 144,0,4096\nMaav 80,164,56\n"
"Maav 84,164,60\nRa 148,0,5120\nMaav 84,160,52\nWL 8\ns_add_u32 m0, s89, 98304\ns_add_u32 s46, s93, 131072\nMaav 92,168,52\nRa 152,0,6144\nMaav 92,172,60\nD1 1,16,46,0\n"
"Maav 88,172,56\nRa 156,0,7168\nWV 5\nAR 22,195\nMaav 88,168,48\nAR 23,199\nv_max_u32 v21, v22, v23\nMaav 96,176,48\nRa 160,0,8192\nAR 22,203\nMaav 96,180,56\nAR 23,207\n"
"v_max3_u32 v21, v21, v22, v23\nMaav 100,180,60\nRa 164,0,9216\nAR 22,211\nMaav 100,176,52\nWL 6\nAR 23,215\nv_max3_u32 v21, v21, v22, v23\nMaav 108,184,52\nRa 168,0,10240\n"
"AR 22,219\nMaav 108,188,60\nD1 1,16,46,1024\nAR 23,223\nMaav 104,188,56\nRa 172,0,11264\nv_max3_u32 v21, v21, v22, v23\nMaav 104,184,48\nv_cmp_eq_u32 vcc, -1, v21\ns_nop 0\n"
"s_cbranch_vccnz .S279\n.S280:\nMaaa 0,128,192\nRa 176,0,12288\nMaaa 0,132,200\nv_sub_f32 v92, 1.0, v80\nMaaa 0,136,208\nRa 180,0,13312\nMaaa 0,140,216\nWL 6\nMaaa 4,140,220\n"
"Ra 184,0,14336\nv_sub_f32 v93, 1.0, v81\nMaaa 4,136,212\nD1 1,16,46,2048\nMaaa 4,132,204\nRa 188,0,15360\nMaaa 4,128,196\nCV 92,92,93\nMaaa 8,144,192\nRa 128,0,16384\n"
"v_and_b32 v93, 0xffff0000, v92\nMaaa 8,148,200\nv_lshlrev_b32 v92, 16, v92\nMaaa 8,152,208\nRa 132,0,17408\nMaaa 8,156,216\nWL 6\nMaaa 12,156,220\nRa 136,0,18432\n"
"v_lshlrev_b32 v64, 16, v12\nMaaa 12,152,212\nD1 1,16,46,3072\nMaaa 12,148,204\nRa 140,0,19456\nMaaa 12,144,196\nv_and_b32 v65, 0xffff0000, v12\nMaaa 16,160,192\nRa 144,0,20480\n"
"MU 64,64,92\nMaaa 16,164,200\nMU 65,65,92\nMaaa 16,168,208\nRa 148,0,21504\nMaaa 16,172,216\nWL 6\ns_add_u32 m0, s89, 114688\ns_add_u32 s46, s93, 163840\nMaaa 20,172,220\n"
"Ra 152,0,22528\nv_lshlrev_b32 v66, 16, v13\nMaaa 20,168,212\nD1 1,16,46,0\nMaaa 20,164,204\nRa 156,0,23552\nMaaa 20,160,196\nv_and_b32 v67, 0xffff0000, v13\nMaaa 24,176,192\n"
"Ra 160,0,24576\nMU 66,66,92\nMaaa 24,180,200\ns_add_u32 s4, s92, 524288\nMaaa 24,184,208\nRa 164,0,25600\nMaaa 24,188,216\nWL 6\n"
"buffer_load_dwordx4 a[224:227], v1, s[16:19], s4 offen offset:0 sc0 sc1\nMaaa 28,188,220\nRa 168,0,26624\nMU 67,67,92\nMaaa 28,184,212\nD1 1,16,46,1024\nMaaa 28,180,204\n"
"Ra 172,0,27648\nMaaa 28,176,196\nbuffer_load_dwordx4 a[228:231], v1, s[16:19], s4 offen offset:1024 sc0 sc1\nMaaa 32,128,192\nRa 176,0,28672\nv_lshlrev_b32 v68, 16, v14\n"
"Maaa 32,132,200\ns_add_u32 s4, s92, 655360\nMaaa 32,136,208\nRa 180,0,29696\nMaaa 32,140,216\ns_waitcnt vmcnt(8) lgkmcnt(6)\n"
"buffer_load_dwordx4 a[232:235], v1, s[16:19], s4 offen offset:0 sc0 sc1\nMaaa 36,140,220\nRa 184,0,30720\nv_and_b32 v69, 0xffff0000, v14\nMaaa 36,136,212\nD1 1,16,46,2048\n"
"Maaa 36,132,204\nRa 188,0,31744\ns_barrier\nMaaa 36,128,196\nbuffer_load_dwordx4 a[236:239], v1, s[16:19], s4 offen offset:1024 sc0 sc1\nMaaa 40,144,192\nRa 128,2,16384\n"
"MU 68,68,92\nMaaa 40,148,200\ns_add_u32 s4, s92, 786432\nMaaa 40,152,208\nRa 132,2,17408\nMaaa 40,156,216\nWL 6\n"
"buffer_load_dwordx4 a[240:243], v1, s[16:19], s4 offen offset:0 sc0 sc1\nMaaa 44,156,220\nRa 136,2,18432\nMU 69,69,92\nMaaa 44,152,212\nD1 1,16,46,3072\nMaaa 44,148,204\n"
"Ra 140,2,19456\nMaaa 44,144,196\nbuffer_load_dwordx4 a[244:247], v1, s[16:19], s4 offen offset:1024 sc0 sc1\nMaaa 48,160,192\nRa 144,2,20480\nv_lshlrev_b32 v70, 16, v15\n"
"Maaa 48,164,200\ns_add_u32 s4, s92, 917504\nMaaa 48,168,208\nRa 148,2,21504\nMaaa 48,172,216\nWL 6\ns_add_u32 m0, s89, 0\ns_add_u32 s46, s93, 196608\n"
"buffer_load_dwordx4 a[248:251], v1, s[16:19], s4 offen offset:0 sc0 sc1\nMaaa 52,172,220\nRa 152,2,22528\nv_and_b32 v71, 0xffff0000, v15\nMaaa 52,168,212\nD1 1,16,46,0\n"
"Maaa 52,164,204\nRa 156,2,23552\nMaaa 52,160,196\nbuffer_load_dwordx4 a[252:255], v1, s[16:19], s4 offen offset:1024 sc0 sc1\nMaaa 56,176,192\nRa 160,2,24576\nMU 70,70,92\n"
"Maaa 56,180,200\nMU 71,71,92\nMaaa 56,184,208\nRa 164,2,25600\nMaaa 56,188,216\nWL 6\nMaaa 60,188,220\nRa 168,2,26624\nv_lshlrev_b32 v72, 16, v16\nMaaa 60,184,212\n"
"D1 1,16,46,1024\nMaaa 60,180,204\nRa 172,2,27648\nMaaa 60,176,196\nv_and_b32 v73, 0xffff0000, v16\nMaaa 64,128,192\nRa 176,2,28672\nMU 72,72,93\nMaaa 64,132,200\nMU 73,73,93\n"
"Maaa 64,136,208\nRa 180,2,29696\nMaaa 64,140,216\nWL 6\nMaaa 68,140,220\nRa 184,2,30720\nv_lshlrev_b32 v74, 16, v17\nMaaa 68,136,212\nD1 1,16,46,2048\nMaaa 68,132,204\n"
"Ra 188,2,31744\nMaaa 68,128,196\nv_and_b32 v75, 0xffff0000, v17\nMaaa 72,144,192\nRa 128,2,0\nMU 74,74,93\nMaaa 72,148,200\nMU 75,75,93\nMaaa 72,152,208\nRa 132,2,1024\n"
"Maaa 72,156,216\nWL 6\nMaaa 76,156,220\nRa 136,2,2048\nv_lshlrev_b32 v76, 16, v18\nMaaa 76,152,212\nD1 1,16,46,3072\nMaaa 76,148,204\nRa 140,2,3072\nMaaa 76,144,196\n"
"v_and_b32 v77, 0xffff0000, v18\nMaaa 80,160,192\nRa 144,2,4096\nMU 76,76,93\nMaaa 80,164,200\nv_bfe_u32 v24, v1, 4, 3\nMaaa 80,168,208\nRa 148,2,5120\nMaaa 80,172,216\nWL 6\n"
"Maaa 84,172,220\nRa 152,2,6144\nMU 77,77,93\nMaaa 84,168,212\nv_mul_u32_u24 v24, 0xc00, v24\nMaaa 84,164,204\nRa 156,2,7168\nMaaa 84,160,196\nv_bfe_u32 v26, v1, 7, 1\n"
"Maaa 88,176,192\nRa 160,2,8192\nv_lshlrev_b32 v78, 16, v19\nMaaa 88,180,200\nv_add_lshl_u32 v24, v24, v26, 13\nMaaa 88,184,208\nRa 164,2,9216\nMaaa 88,188,216\nWL 6\n"
"Maaa 92,188,220\nRa 168,2,10240\nv_and_b32 v79, 0xffff0000, v19\nMaaa 92,184,212\nbuffer_load_dword v27, v24, s[16:19], s41 offen sc0 sc1\nMaaa 92,180,204\nRa 172,2,11264\n"
"Maaa 92,176,196\nMU 78,78,93\nMaaa 96,128,192\nRa 176,2,12288\nMU 79,79,93\nMaaa 96,132,200\nMaaa 96,136,208\nRa 180,2,13312\nMaaa 96,140,216\ns_waitcnt vmcnt(8) lgkmcnt(6)\n"
"Maaa 100,140,220\nRa 184,2,14336\nMaaa 100,136,212\nMaaa 100,132,204\nRa 188,2,15360\ns_barrier\nWV 4\nMaaa 100,128,196\nAR 22,227\nAR 23,231\nMaaa 104,144,192\nRa 128,2,32768\n"
"v_max_u32 v21, v22, v23\nMaaa 104,148,200\nAR 22,235\nAR 23,239\nMaaa 104,152,208\nRa 132,2,33792\nv_max3_u32 v21, v21, v22, v23\nMaaa 104,156,216\nWL 6\nAR 22,243\nAR 23,247\n"
"Maaa 108,156,220\nRa 136,2,34816\nv_max3_u32 v21, v21, v22, v23\nMaaa 108,152,212\nAR 22,251\nAR 23,255\nMaaa 108,148,204\nRa 140,2,35840\nv_max3_u32 v21, v21, v22, v23\n"
"Maaa 108,144,196\nv_cmp_eq_u32 vcc, -1, v21\ns_nop 0\ns_cbranch_vccnz .S282\n.S283:\nMaaa 0,160,224\nRa 144,2,36864\nMaaa 0,164,232\nMaaa 0,168,240\nRa 148,2,37888\n"
"Maaa 0,172,248\nWL 6\nMaaa 4,172,252\nRa 152,2,38912\nMaaa 4,168,244\nMaaa 4,164,236\nRa 156,2,39936\nMaaa 4,160,228\nMaaa 8,176,224\nRa 160,2,40960\nMaaa 8,180,232\n"
"Maaa 8,184,240\nRa 164,2,41984\nMaaa 8,188,248\nWL 6\nMaaa 12,188,252\nRa 168,2,43008\nMaaa 12,184,244\nMaaa 12,180,236\nRa 172,2,44032\nMaaa 12,176,228\nAR 82,0\nAR 83,1\n"
"Maaa 16,128,224\nRa 176,2,45056\nCV 20,82,83\nAR 82,2\nMaaa 16,132,232\nAR 83,3\nCV 21,82,83\nMaaa 16,136,240\nRa 180,2,46080\nAR 82,8\nMaaa 16,140,248\nWL 6\nAR 83,9\n"
"Maaa 20,140,252\nRa 184,2,47104\nCV 22,82,83\nMaaa 20,136,244\nAR 82,10\nAR 83,11\nMaaa 20,132,236\nRa 188,2,48128\nCV 23,82,83\ns_add_u32 s5, s95, 0\nMaaa 20,128,228\n"
"Sv 20,1,16,5,0\nAR 82,4\nMaaa 24,144,224\nRa 128,2,49152\nAR 83,5\nMaaa 24,148,232\nCV 84,82,83\nAR 82,6\nMaaa 24,152,240\nRa 132,2,50176\nAR 83,7\nMaaa 24,156,248\nWL 6\n"
"CV 85,82,83\nAR 82,12\nMaaa 28,156,252\nRa 136,2,51200\nAR 83,13\nMaaa 28,152,244\nCV 86,82,83\nAR 82,14\nMaaa 28,148,236\nRa 140,2,52224\nAR 83,15\nMaaa 28,144,228\nCV 87,82,83\n"
"Sv 84,1,16,5,1024\nMaaa 32,160,224\nRa 144,2,53248\nAR 82,16\nAR 83,17\nMaaa 32,164,232\nCV 20,82,83\nAR 82,18\nMaaa 32,168,240\nRa 148,2,54272\nAR 83,19\nMaaa 32,172,248\nWL 6\n"
"CV 21,82,83\nMaaa 36,172,252\nRa 152,2,55296\nAR 82,24\nMaaa 36,168,244\nAR 83,25\nCV 22,82,83\nMaaa 36,164,236\nRa 156,2,56320\nAR 82,26\nAR 83,27\nMaaa 36,160,228\nCV 23,82,83\n"
"s_add_u32 s5, s95, 131072\nMaaa 40,176,224\nRa 160,2,57344\nSv 20,1,16,5,0\nMaaa 40,180,232\nAR 82,20\nAR 83,21\nMaaa 40,184,240\nRa 164,2,58368\nCV 84,82,83\nMaaa 40,188,248\n"
"WL 6\nAR 82,22\nAR 83,23\nMaaa 44,188,252\nRa 168,2,59392\nCV 85,82,83\nMaaa 44,184,244\nAR 82,28\nAR 83,29\nMaaa 44,180,236\nRa 172,2,60416\nCV 86,82,83\nMaaa 44,176,228\n"
"AR 82,30\nAR 83,31\nMaaa 48,128,224\nRa 176,2,61440\nCV 87,82,83\nSv 84,1,16,5,1024\nMaaa 48,132,232\nAR 82,32\nAR 83,33\nMaaa 48,136,240\nRa 180,2,62464\nCV 20,82,83\n"
"Maaa 48,140,248\ns_waitcnt vmcnt(5) lgkmcnt(6)\nAR 82,34\nMaaa 52,140,252\nRa 184,2,63488\nAR 83,35\nMaaa 52,136,244\nCV 21,82,83\nAR 82,40\nMaaa 52,132,236\nRa 188,2,64512\n"
"s_barrier\nAR 83,41\nMaaa 52,128,228\nCV 22,82,83\nAR 82,42\nMaaa 56,144,224\nRa 128,3,0\nAR 83,43\nMaaa 56,148,232\nCV 23,82,83\ns_add_u32 s5, s95, 262144\nMaaa 56,152,240\n"
"Ra 132,3,1024\nSv 20,1,16,5,0\nMaaa 56,156,248\nWL 6\nAR 82,36\nAR 83,37\nMaaa 60,156,252\nRa 136,3,2048\nCV 84,82,83\nMaaa 60,152,244\nAR 82,38\nAR 83,39\nMaaa 60,148,236\n"
"Ra 140,3,3072\nCV 85,82,83\nMaaa 60,144,228\nAR 82,44\nAR 83,45\nMaaa 64,160,224\nRa 144,3,4096\nCV 86,82,83\nAR 82,46\nMaaa 64,164,232\nAR 83,47\nCV 87,82,83\nMaaa 64,168,240\n"
"Ra 148,3,5120\nSv 84,1,16,5,1024\nMaaa 64,172,248\nWL 6\nAR 82,48\nMaaa 68,172,252\nRa 152,3,6144\nAR 83,49\nMaaa 68,168,244\nCV 20,82,83\nAR 82,50\nMaaa 68,164,236\n"
"Ra 156,3,7168\nAR 83,51\nCV 21,82,83\nMaaa 68,160,228\nAR 82,56\nAR 83,57\nMaaa 72,176,224\nRa 160,3,8192\nCV 22,82,83\nMaaa 72,180,232\nAR 82,58\nAR 83,59\nMaaa 72,184,240\n"
"Ra 164,3,9216\nCV 23,82,83\nMaaa 72,188,248\nWL 6\ns_add_u32 s5, s95, 393216\nSv 20,1,16,5,0\nMaaa 76,188,252\nRa 168,3,10240\nAR 82,52\nMaaa 76,184,244\nAR 83,53\nCV 84,82,83\n"
"Maaa 76,180,236\nRa 172,3,11264\nAR 82,54\nMaaa 76,176,228\nAR 83,55\nCV 85,82,83\nMaaa 80,128,224\nRa 176,3,12288\nAR 82,60\nAR 83,61\nMaaa 80,132,232\nCV 86,82,83\nAR 82,62\n"
"Maaa 80,136,240\nRa 180,3,13312\nAR 83,63\nMaaa 80,140,248\nWL 6\nCV 87,82,83\nMaaa 84,140,252\nRa 184,3,14336\nSv 84,1,16,5,1024\nMaaa 84,136,244\nAR 82,64\nAR 83,65\n"
"Maaa 84,132,236\nRa 188,3,15360\nCV 20,82,83\nAR 82,66\nMaaa 84,128,228\nAR 83,67\nCV 21,82,83\nMaaa 88,144,224\nAR 82,72\nAR 83,73\nMaaa 88,148,232\nCV 22,82,83\nAR 82,74\n"
"Maaa 88,152,240\nAR 83,75\nCV 23,82,83\nMaaa 88,156,248\nWL 4\ns_add_u32 s5, s95, 524288\nSv 20,1,16,5,0\nMaaa 92,156,252\nAR 82,68\nAR 83,69\nMaaa 92,152,244\nCV 84,82,83\n"
"AR 82,70\nMaaa 92,148,236\nAR 83,71\nCV 85,82,83\nMaaa 92,144,228\nAR 82,76\nAR 83,77\nMaaa 96,160,224\nCV 86,82,83\nAR 82,78\nAR 83,79\nMaaa 96,164,232\nCV 87,82,83\n"
"Sv 84,1,16,5,1024\nMaaa 96,168,240\nAR 82,80\nAR 83,81\nMaaa 96,172,248\nWL 0\nCV 20,82,83\nMaaa 100,172,252\nAR 82,82\nAR 83,83\nMaaa 100,168,244\nCV 21,82,83\nAR 82,88\n"
"Maaa 100,164,236\nAR 83,89\nCV 22,82,83\nAR 82,90\nMaaa 100,160,228\nAR 83,91\nCV 23,82,83\nMaaa 104,176,224\ns_add_u32 s5, s95, 655360\nSv 20,1,16,5,0\nMaaa 104,180,232\n"
"AR 82,84\nAR 83,85\nMaaa 104,184,240\nCV 84,82,83\nAR 82,86\nMaaa 104,188,248\nAR 83,87\nCV 85,82,83\nAR 82,92\nMaaa 108,188,252\nAR 83,93\nCV 86,82,83\nMaaa 108,184,244\n"
"AR 82,94\nAR 83,95\nMaaa 108,180,236\nCV 87,82,83\nSv 84,1,16,5,1024\nMaaa 108,176,228\nAR 82,96\nAR 83,97\nCV 20,82,83\nAR 82,98\nAR 83,99\nCV 21,82,83\nAR 82,104\nAR 83,105\n"
"CV 22,82,83\nAR 82,106\nAR 83,107\nCV 23,82,83\ns_add_u32 s5, s95, 786432\nSv 20,1,16,5,0\nAR 82,100\nAR 83,101\nCV 84,82,83\nAR 82,102\nAR 83,103\nCV 85,82,83\nAR 82,108\n"
"AR 83,109\nCV 86,82,83\nAR 82,110\nAR 83,111\nCV 87,82,83\nSv 84,1,16,5,1024\ns_cmp_eq_u32 s13, 7\ns_cbranch_scc1 .Z294\nWV 14\nv_cmp_eq_u32 s[62:63], s15, v27\n"
"s_mov_b32 vcc_hi, 0\ns_nop 2\ns_lshr_b32 s63, s62, 8\ns_or_b32 s62, s62, s63\ns_andn2_b32 vcc_lo, 0xff, s62\ns_mov_b64 vcc, vcc\ns_cbranch_vccnz .S291\n.S292:\n"
"s_mov_b32 s7, 262144\ns_mov_b32 s3, 0xff\n.Z295:\ns_bitcmp1_b32 s3, 0\ns_cbranch_scc0 .Z297\ns_bitcmp1_b32 s63, 0\ns_cselect_b32 s2, 0x2000, 0\ns_xor_b32 s2, s2, s20\n"
"s_add_u32 s2, s2, 0x0\nbuffer_load_dwordx4 v[96:99], v1, s[16:19], s2 offen offset:0 sc0 sc1\nbuffer_load_dwordx4 v[100:103], v1, s[16:19], s2 offen offset:1024 sc0 sc1\n.Z297:\n"
"s_bitcmp1_b32 s3, 1\ns_cbranch_scc0 .Z298\ns_bitcmp1_b32 s63, 1\ns_cselect_b32 s2, 0x2000, 0\ns_xor_b32 s2, s2, s20\ns_add_u32 s2, s2, 0x1800000\n"
"buffer_load_dwordx4 v[104:107], v1, s[16:19], s2 offen offset:0 sc0 sc1\nbuffer_load_dwordx4 v[108:111], v1, s[16:19], s2 offen offset:1024 sc0 sc1\n.Z298:\ns_bitcmp1_b32 s3, 2\n"
"s_cbranch_scc0 .Z299\ns_bitcmp1_b32 s63, 2\ns_cselect_b32 s2, 0x2000, 0\ns_xor_b32 s2, s2, s20\ns_add_u32 s2, s2, 0x3000000\n"
"buffer_load_dwordx4 v[112:115], v1, s[16:19], s2 offen offset:0 sc0 sc1\nbuffer_load_dwordx4 v[116:119], v1, s[16:19], s2 offen offset:1024 sc0 sc1\n.Z299:\ns_bitcmp1_b32 s3, 3\n"
"s_cbranch_scc0 .Z300\ns_bitcmp1_b32 s63, 3\ns_cselect_b32 s2, 0x2000, 0\ns_xor_b32 s2, s2, s20\ns_add_u32 s2, s2, 0x4800000\n"
"buffer_load_dwordx4 v[120:123], v1, s[16:19], s2 offen offset:0 sc0 sc1\nbuffer_load_dwordx4 v[124:127], v1, s[16:19], s2 offen offset:1024 sc0 sc1\n.Z300:\ns_bitcmp1_b32 s3, 4\n"
"s_cbranch_scc0 .Z301\ns_bitcmp1_b32 s63, 4\ns_cselect_b32 s2, 0x2000, 0\ns_xor_b32 s2, s2, s20\ns_add_u32 s2, s2, 0x6000000\n"
"buffer_load_dwordx4 v[32:35], v1, s[16:19], s2 offen offset:0 sc0 sc1\nbuffer_load_dwordx4 v[36:39], v1, s[16:19], s2 offen offset:1024 sc0 sc1\n.Z301:\ns_bitcmp1_b32 s3, 5\n"
"s_cbranch_scc0 .Z302\ns_bitcmp1_b32 s63, 5\ns_cselect_b32 s2, 0x2000, 0\ns_xor_b32 s2, s2, s20\ns_add_u32 s2, s2, 0x7800000\n"
"buffer_load_dwordx4 v[40:43], v1, s[16:19], s2 offen offset:0 sc0 sc1\nbuffer_load_dwordx4 v[44:47], v1, s[16:19], s2 offen offset:1024 sc0 sc1\n.Z302:\ns_bitcmp1_b32 s3, 6\n"
"s_cbranch_scc0 .Z303\ns_bitcmp1_b32 s63, 6\ns_cselect_b32 s2, 0x2000, 0\ns_xor_b32 s2, s2, s20\ns_add_u32 s2, s2, 0x9000000\n"
"buffer_load_dwordx4 v[48:51], v1, s[16:19], s2 offen offset:0 sc0 sc1\nbuffer_load_dwordx4 v[52:55], v1, s[16:19], s2 offen offset:1024 sc0 sc1\n.Z303:\ns_bitcmp1_b32 s3, 7\n"
"s_cbranch_scc0 .Z304\ns_bitcmp1_b32 s63, 7\ns_cselect_b32 s2, 0x2000, 0\ns_xor_b32 s2, s2, s20\ns_add_u32 s2, s2, 0xa800000\n"
"buffer_load_dwordx4 v[56:59], v1, s[16:19], s2 offen offset:0 sc0 sc1\nbuffer_load_dwordx4 v[60:63], v1, s[16:19], s2 offen offset:1024 sc0 sc1\n.Z304:\ns_mov_b32 s3, 0\nWV 0\n"
"v_max_u32 v82, v99, v103\nv_cmp_eq_u32 s[44:45], -1, v82\nv_max_u32 v82, v107, v111\nv_cmp_eq_u32 s[46:47], -1, v82\nv_max_u32 v82, v115, v119\nv_cmp_eq_u32 s[48:49], -1, v82\n"
"v_max_u32 v82, v123, v127\nv_cmp_eq_u32 s[50:51], -1, v82\nv_max_u32 v82, v35, v39\nv_cmp_eq_u32 s[52:53], -1, v82\nv_max_u32 v82, v43, v47\nv_cmp_eq_u32 s[54:55], -1, v82\n"
"v_max_u32 v82, v51, v55\nv_cmp_eq_u32 s[56:57], -1, v82\nv_max_u32 v82, v59, v63\nv_cmp_eq_u32 s[58:59], -1, v82\ns_cmp_lg_u64 s[44:45], 0\ns_cselect_b32 s2, 1, 0\n"
"s_or_b32 s3, s3, s2\ns_cmp_lg_u64 s[46:47], 0\ns_cselect_b32 s2, 2, 0\ns_or_b32 s3, s3, s2\ns_cmp_lg_u64 s[48:49], 0\ns_cselect_b32 s2, 4, 0\ns_or_b32 s3, s3, s2\n"
"s_cmp_lg_u64 s[50:51], 0\ns_cselect_b32 s2, 8, 0\ns_or_b32 s3, s3, s2\ns_cmp_lg_u64 s[52:53], 0\ns_cselect_b32 s2, 16, 0\ns_or_b32 s3, s3, s2\ns_cmp_lg_u64 s[54:55], 0\n"
"s_cselect_b32 s2, 32, 0\ns_or_b32 s3, s3, s2\ns_cmp_lg_u64 s[56:57], 0\ns_cselect_b32 s2, 64, 0\ns_or_b32 s3, s3, s2\ns_cmp_lg_u64 s[58:59], 0\ns_cselect_b32 s2, 128, 0\n"
"s_or_b32 s3, s3, s2\ns_cbranch_scc0 .Z296\ns_sub_u32 s7, s7, 1\ns_cbranch_scc0 .Z295\n.Z296:\nZvvv 136,6,96\nMvvv 136,6,104\nMvvv 136,6,112\nMvvv 136,6,120\nMvvv 136,6,32\n"
"Mvvv 136,6,40\nMvvv 136,6,48\nMvvv 136,6,56\nZvvv 140,4,96\nMvvv 140,4,104\nMvvv 140,4,112\nMvvv 140,4,120\nMvvv 140,4,32\nMvvv 140,4,40\nMvvv 140,4,48\nMvvv 140,4,56\n"
"Zvvv 144,6,100\nMvvv 144,6,108\nMvvv 144,6,116\nMvvv 144,6,124\nMvvv 144,6,36\nMvvv 144,6,44\nMvvv 144,6,52\nMvvv 144,6,60\nZvvv 148,4,100\nMvvv 148,4,108\nMvvv 148,4,116\n"
"Mvvv 148,4,124\nMvvv 148,4,36\nMvvv 148,4,44\nMvvv 148,4,52\nMvvv 148,4,60\nFM 64,80,136\nFM 65,80,137\nFM 66,80,138\nFM 67,80,139\nFM 68,80,140\nFM 69,80,141\nFM 70,80,142\n"
"FM 71,80,143\nCV 12,64,65\nCV 13,66,67\nCV 14,68,69\nCV 15,70,71\nSv 12,91,36,22,0\nFM 72,81,144\nFM 73,81,145\nFM 74,81,146\nFM 75,81,147\nFM 76,81,148\nFM 77,81,149\n"
"FM 78,81,150\nFM 79,81,151\nCV 16,72,73\nCV 17,74,75\nCV 18,76,77\nCV 19,78,79\nSv 16,91,36,23,0\n.Z294:\ns_endpgm\n.HD208:\nv_cmp_ne_u32 vcc, s15, v27\ns_nop 0\n"
"s_cbranch_vccnz .S305\n.S306:\nbuffer_store_dword v25, off, s[16:19], s54 offset:4 sc0 sc1\ns_mov_b32 s90, 2\ns_swappc_b64 s[8:9], s[80:81]\nv_and_b32 v27, 0x1f0, v1\n"
"buffer_load_dword v27, v27, s[16:19], s58 offen offset:4 sc0 sc1\ns_mov_b32 s90, 0\ns_swappc_b64 s[8:9], s[80:81]\nv_cmp_ne_u32 vcc, s15, v27\ns_nop 0\ns_cbranch_vccnz .S308\n"
".S309:\ns_mov_b32 s90, 100\ns_swappc_b64 s[8:9], s[80:81]\n.S209:\ns_mov_b32 s7, 262144\n.S211:\ns_sleep 8\nbuffer_load_dwordx4 v[32:35], v1, s[16:19], s6 offen offset:0 sc0 sc1\n"
"buffer_load_dwordx4 v[36:39], v1, s[16:19], s6 offen offset:1024 sc0 sc1\nbuffer_load_dwordx4 v[40:43], v1, s[16:19], s6 offen offset:2048 sc0 sc1\n"
"buffer_load_dwordx4 v[44:47], v1, s[16:19], s6 offen offset:3072 sc0 sc1\nWV 0\nv_xor_b32 v48, s15, v33\nv_xor_b32 v49, s15, v35\nv_xor_b32 v50, s15, v37\nv_xor_b32 v51, s15, v39\n"
"v_xor_b32 v52, s15, v41\nv_xor_b32 v53, s15, v43\nv_xor_b32 v54, s15, v45\nv_xor_b32 v55, s15, v47\nv_or3_b32 v48, v48, v49, v50\nv_or3_b32 v51, v51, v52, v53\n"
"v_or3_b32 v48, v48, v54, v55\nv_or_b32 v48, v48, v51\nv_cmp_ne_u32 vcc, 0, v48\ns_nop 0\ns_cbranch_vccz .S210\ns_sub_u32 s7, s7, 1\ns_cbranch_scc0 .S211\ns_branch .S210\n.S212:\n"
"s_mov_b32 s7, 262144\n.S214:\ns_sleep 8\ns_lshl_b32 s5, s73, 7\ns_add_u32 s5, s5, 0xc04ac00\nv_bfe_u32 v27, v1, 2, 7\nbuffer_load_dword v27, v27, s[16:19], s5 offen sc0 sc1\nWV 0\n"
"v_xor_b32 v27, s15, v27\nv_and_b32 v255, 0xffffdfff, v27\nv_cmp_ne_u32 vcc, 0, v255\ns_nop 0\ns_cbranch_vccz .S213\ns_sub_u32 s7, s7, 1\ns_cbranch_scc0 .S214\ns_branch .S213\n"
".E217:\nv_mov_b32 v12, s97\nv_mov_b32 v13, s97\nv_mov_b32 v14, s97\nv_mov_b32 v15, s97\nv_cmpx_gt_i32 vcc, 3, v0\nv_mov_b32 v7, s97\nv_mov_b32 v19, s97\nv_cmpx_gt_i32 vcc, 2, v0\n"
"v_mov_b32 v6, s97\nv_mov_b32 v18, s97\nv_cmpx_gt_i32 vcc, 1, v0\nv_mov_b32 v5, s97\nv_mov_b32 v17, s97\nv_cmpx_gt_i32 vcc, 0, v0\nv_mov_b32 v4, s97\nv_mov_b32 v16, s97\n"
"v_cmpx_gt_i32 vcc, -16, v0\nv_mov_b32 v8, s97\nv_mov_b32 v9, s97\nv_mov_b32 v10, s97\nv_mov_b32 v11, s97\ns_mov_b64 exec, -1\ns_nop 2\ns_branch .E218\n.E219:\nv_mov_b32 v12, s97\n"
"v_mov_b32 v13, s97\nv_mov_b32 v14, s97\nv_mov_b32 v15, s97\nv_cmpx_gt_i32 vcc, 3, v3\nv_mov_b32 v7, s97\nv_mov_b32 v19, s97\nv_cmpx_gt_i32 vcc, 2, v3\nv_mov_b32 v6, s97\n"
"v_mov_b32 v18, s97\nv_cmpx_gt_i32 vcc, 1, v3\nv_mov_b32 v5, s97\nv_mov_b32 v17, s97\nv_cmpx_gt_i32 vcc, 0, v3\nv_mov_b32 v4, s97\nv_mov_b32 v16, s97\nv_cmpx_gt_i32 vcc, -16, v3\n"
"v_mov_b32 v8, s97\nv_mov_b32 v9, s97\nv_mov_b32 v10, s97\nv_mov_b32 v11, s97\ns_mov_b64 exec, -1\ns_nop 2\ns_branch .E220\n.E221:\ns_swappc_b64 s[80:81], s[8:9]\ns_branch .E222\n"
".T223:\ns_mov_b32 s62, 1\ns_call_b64 s[80:81], .RA205\ns_branch .T224\n.T225:\ns_mov_b32 s62, 1\ns_call_b64 s[80:81], .RA205\ns_branch .T226\n.S227:\ns_mov_b32 s7, 262144\n.S229:\n"
"s_sleep 8\nbuffer_load_dword v27, off, s[16:19], s5 sc0 sc1\nWV 0\nv_cmp_ne_u32 vcc, s15, v27\ns_nop 0\ns_cbranch_vccz .S228\ns_sub_u32 s7, s7, 1\ns_cbranch_scc0 .S229\n"
"s_branch .S228\n.S230:\ns_mov_b32 s7, 262144\n.S232:\ns_sleep 8\nv_and_b32 v27, 0x70, v1\nbuffer_load_dword v27, v27, s[16:19], s61 offen offset:128 sc0 sc1\nWV 0\n"
"v_cmp_ne_u32 vcc, s15, v27\ns_nop 0\ns_cbranch_vccz .S231\ns_sub_u32 s7, s7, 1\ns_cbranch_scc0 .S232\ns_branch .S231\n.E234:\ns_cmp_eq_u32 s11, 3\ns_cbranch_scc1 .P233\n"
"s_cmp_lt_u32 s11, 2\ns_cselect_b32 s64, -1.0, s64\ns_cselect_b32 s2, 64, 0\nv_subrev_u32 v20, s2, v3\nv_mov_b32 v12, s97\nv_mov_b32 v13, s97\nv_mov_b32 v14, s97\n"
"v_mov_b32 v15, s97\nv_cmpx_gt_i32 vcc, 3, v20\nv_mov_b32 v7, s97\nv_mov_b32 v19, s97\nv_cmpx_gt_i32 vcc, 2, v20\nv_mov_b32 v6, s97\nv_mov_b32 v18, s97\nv_cmpx_gt_i32 vcc, 1, v20\n"
"v_mov_b32 v5, s97\nv_mov_b32 v17, s97\nv_cmpx_gt_i32 vcc, 0, v20\nv_mov_b32 v4, s97\nv_mov_b32 v16, s97\nv_cmpx_gt_i32 vcc, -16, v20\nv_mov_b32 v8, s97\nv_mov_b32 v9, s97\n"
"v_mov_b32 v10, s97\nv_mov_b32 v11, s97\ns_mov_b64 exec, -1\ns_nop 2\n.P233:\ns_branch .E235\n.S236:\ns_mov_b32 s7, 262144\n.S238:\ns_sleep 8\nv_and_b32 v27, 0x70, v1\n"
"buffer_load_dword v27, v27, s[16:19], s61 offen offset:136 sc0 sc1\nWV 0\nv_cmp_ne_u32 vcc, s15, v27\ns_nop 0\ns_cbranch_vccz .S237\ns_sub_u32 s7, s7, 1\ns_cbranch_scc0 .S238\n"
"s_branch .S237\n.E240:\nv_mov_b32 v12, s97\nv_mov_b32 v13, s97\nv_mov_b32 v14, s97\nv_mov_b32 v15, s97\nv_cmpx_gt_i32 vcc, 3, v0\nv_mov_b32 v7, s97\nv_mov_b32 v19, s97\n"
"v_cmpx_gt_i32 vcc, 2, v0\nv_mov_b32 v6, s97\nv_mov_b32 v18, s97\nv_cmpx_gt_i32 vcc, 1, v0\nv_mov_b32 v5, s97\nv_mov_b32 v17, s97\nv_cmpx_gt_i32 vcc, 0, v0\nv_mov_b32 v4, s97\n"
"v_mov_b32 v16, s97\nv_cmpx_gt_i32 vcc, -16, v0\nv_mov_b32 v8, s97\nv_mov_b32 v9, s97\nv_mov_b32 v10, s97\nv_mov_b32 v11, s97\ns_mov_b64 exec, -1\ns_nop 2\ns_branch .E241\n.E242:\n"
"v_mov_b32 v12, s97\nv_mov_b32 v13, s97\nv_mov_b32 v14, s97\nv_mov_b32 v15, s97\nv_cmpx_gt_i32 vcc, 3, v3\nv_mov_b32 v7, s97\nv_mov_b32 v19, s97\nv_cmpx_gt_i32 vcc, 2, v3\n"
"v_mov_b32 v6, s97\nv_mov_b32 v18, s97\nv_cmpx_gt_i32 vcc, 1, v3\nv_mov_b32 v5, s97\nv_mov_b32 v17, s97\nv_cmpx_gt_i32 vcc, 0, v3\nv_mov_b32 v4, s97\nv_mov_b32 v16, s97\n"
"v_cmpx_gt_i32 vcc, -16, v3\nv_mov_b32 v8, s97\nv_mov_b32 v9, s97\nv_mov_b32 v10, s97\nv_mov_b32 v11, s97\ns_mov_b64 exec, -1\ns_nop 2\ns_branch .E243\n.E244:\n"
"s_swappc_b64 s[80:81], s[8:9]\ns_branch .E245\n.T246:\ns_mov_b32 s62, 0\ns_call_b64 s[80:81], .RB206\ns_branch .T247\n.T248:\ns_mov_b32 s62, 0\ns_call_b64 s[80:81], .RA205\n"
"s_branch .T249\n.E250:\nv_mov_b32 v12, s97\nv_mov_b32 v13, s97\nv_mov_b32 v14, s97\nv_mov_b32 v15, s97\nv_cmpx_gt_i32 vcc, 3, v3\nv_mov_b32 v7, s97\nv_mov_b32 v19, s97\n"
"v_cmpx_gt_i32 vcc, 2, v3\nv_mov_b32 v6, s97\nv_mov_b32 v18, s97\nv_cmpx_gt_i32 vcc, 1, v3\nv_mov_b32 v5, s97\nv_mov_b32 v17, s97\nv_cmpx_gt_i32 vcc, 0, v3\nv_mov_b32 v4, s97\n"
"v_mov_b32 v16, s97\nv_cmpx_gt_i32 vcc, -16, v3\nv_mov_b32 v8, s97\nv_mov_b32 v9, s97\nv_mov_b32 v10, s97\nv_mov_b32 v11, s97\ns_mov_b64 exec, -1\ns_nop 2\ns_branch .E251\n.E252:\n"
"v_mov_b32 v12, s97\nv_mov_b32 v13, s97\nv_mov_b32 v14, s97\nv_mov_b32 v15, s97\nv_cmpx_gt_i32 vcc, 3, v0\nv_mov_b32 v7, s97\nv_mov_b32 v19, s97\nv_cmpx_gt_i32 vcc, 2, v0\n"
"v_mov_b32 v6, s97\nv_mov_b32 v18, s97\nv_cmpx_gt_i32 vcc, 1, v0\nv_mov_b32 v5, s97\nv_mov_b32 v17, s97\nv_cmpx_gt_i32 vcc, 0, v0\nv_mov_b32 v4, s97\nv_mov_b32 v16, s97\n"
"v_cmpx_gt_i32 vcc, -16, v0\nv_mov_b32 v8, s97\nv_mov_b32 v9, s97\nv_mov_b32 v10, s97\nv_mov_b32 v11, s97\ns_mov_b64 exec, -1\ns_nop 2\ns_branch .E253\n.E254:\nv_mov_b32 v12, s97\n"
"v_mov_b32 v13, s97\nv_mov_b32 v14, s97\nv_mov_b32 v15, s97\nv_cmpx_gt_i32 vcc, 3, v3\nv_mov_b32 v7, s97\nv_mov_b32 v19, s97\nv_cmpx_gt_i32 vcc, 2, v3\nv_mov_b32 v6, s97\n"
"v_mov_b32 v18, s97\nv_cmpx_gt_i32 vcc, 1, v3\nv_mov_b32 v5, s97\nv_mov_b32 v17, s97\nv_cmpx_gt_i32 vcc, 0, v3\nv_mov_b32 v4, s97\nv_mov_b32 v16, s97\nv_cmpx_gt_i32 vcc, -16, v3\n"
"v_mov_b32 v8, s97\nv_mov_b32 v9, s97\nv_mov_b32 v10, s97\nv_mov_b32 v11, s97\ns_mov_b64 exec, -1\ns_nop 2\ns_branch .E255\n.E256:\ns_swappc_b64 s[80:81], s[8:9]\ns_branch .E257\n"
".T258:\ns_mov_b32 s62, 2\ns_call_b64 s[80:81], .RB206\ns_branch .T259\n.T260:\ns_mov_b32 s62, 5\ns_call_b64 s[80:81], .RA205\ns_branch .T261\n.T262:\ns_mov_b32 s62, 5\n"
"s_call_b64 s[80:81], .RA205\ns_branch .T263\n.E265:\nv_mov_b32 v12, s97\nv_mov_b32 v13, s97\nv_mov_b32 v14, s97\nv_mov_b32 v15, s97\nv_cmpx_gt_i32 vcc, 3, v0\nv_mov_b32 v7, s97\n"
"v_mov_b32 v19, s97\nv_cmpx_gt_i32 vcc, 2, v0\nv_mov_b32 v6, s97\nv_mov_b32 v18, s97\nv_cmpx_gt_i32 vcc, 1, v0\nv_mov_b32 v5, s97\nv_mov_b32 v17, s97\nv_cmpx_gt_i32 vcc, 0, v0\n"
"v_mov_b32 v4, s97\nv_mov_b32 v16, s97\nv_cmpx_gt_i32 vcc, -16, v0\nv_mov_b32 v8, s97\nv_mov_b32 v9, s97\nv_mov_b32 v10, s97\nv_mov_b32 v11, s97\ns_mov_b64 exec, -1\ns_nop 2\n"
"s_branch .E266\n.E267:\nv_mov_b32 v12, s97\nv_mov_b32 v13, s97\nv_mov_b32 v14, s97\nv_mov_b32 v15, s97\nv_cmpx_gt_i32 vcc, 3, v3\nv_mov_b32 v7, s97\nv_mov_b32 v19, s97\n"
"v_cmpx_gt_i32 vcc, 2, v3\nv_mov_b32 v6, s97\nv_mov_b32 v18, s97\nv_cmpx_gt_i32 vcc, 1, v3\nv_mov_b32 v5, s97\nv_mov_b32 v17, s97\nv_cmpx_gt_i32 vcc, 0, v3\nv_mov_b32 v4, s97\n"
"v_mov_b32 v16, s97\nv_cmpx_gt_i32 vcc, -16, v3\nv_mov_b32 v8, s97\nv_mov_b32 v9, s97\nv_mov_b32 v10, s97\nv_mov_b32 v11, s97\ns_mov_b64 exec, -1\ns_nop 2\ns_branch .E268\n.E269:\n"
"s_swappc_b64 s[80:81], s[8:9]\ns_branch .E270\n.E271:\nv_mov_b32 v91, 0x40000000\nv_cmp_gt_u32 vcc, 0x200, v1\nWV 5\ns_nop 0\nv_cndmask_b32 v91, v91, v1, vcc\nSv 92,91,16,65,0\n"
"s_branch .E272\n.T273:\ns_mov_b32 s62, 4\ns_call_b64 s[80:81], .RB206\ns_branch .T274\n.T275:\ns_mov_b32 s62, 4\ns_call_b64 s[80:81], .RA205\ns_branch .T276\n.T277:\n"
"s_mov_b32 s62, 6\ns_call_b64 s[80:81], .RB206\ns_branch .T278\n.S279:\ns_mov_b32 s7, 262144\n.S281:\ns_sleep 8\ns_add_u32 s4, s92, 0\n"
"buffer_load_dwordx4 a[192:195], v1, s[16:19], s4 offen offset:0 sc0 sc1\nbuffer_load_dwordx4 a[196:199], v1, s[16:19], s4 offen offset:1024 sc0 sc1\ns_add_u32 s4, s92, 131072\n"
"buffer_load_dwordx4 a[200:203], v1, s[16:19], s4 offen offset:0 sc0 sc1\nbuffer_load_dwordx4 a[204:207], v1, s[16:19], s4 offen offset:1024 sc0 sc1\ns_add_u32 s4, s92, 262144\n"
"buffer_load_dwordx4 a[208:211], v1, s[16:19], s4 offen offset:0 sc0 sc1\nbuffer_load_dwordx4 a[212:215], v1, s[16:19], s4 offen offset:1024 sc0 sc1\ns_add_u32 s4, s92, 393216\n"
"buffer_load_dwordx4 a[216:219], v1, s[16:19], s4 offen offset:0 sc0 sc1\nbuffer_load_dwordx4 a[220:223], v1, s[16:19], s4 offen offset:1024 sc0 sc1\nWV 0\nAR 22,195\nAR 23,199\n"
"v_max_u32 v21, v22, v23\nAR 22,203\nAR 23,207\nv_max3_u32 v21, v21, v22, v23\nAR 22,211\nAR 23,215\nv_max3_u32 v21, v21, v22, v23\nAR 22,219\nAR 23,223\n"
"v_max3_u32 v21, v21, v22, v23\nv_cmp_eq_u32 vcc, -1, v21\ns_nop 0\ns_cbranch_vccz .S280\ns_sub_u32 s7, s7, 1\ns_cbranch_scc0 .S281\ns_branch .S280\n.S282:\ns_mov_b32 s7, 262144\n"
".S284:\ns_sleep 8\ns_add_u32 s4, s92, 524288\nbuffer_load_dwordx4 a[224:227], v1, s[16:19], s4 offen offset:0 sc0 sc1\n"
"buffer_load_dwordx4 a[228:231], v1, s[16:19], s4 offen offset:1024 sc0 sc1\ns_add_u32 s4, s92, 655360\nbuffer_load_dwordx4 a[232:235], v1, s[16:19], s4 offen offset:0 sc0 sc1\n"
"buffer_load_dwordx4 a[236:239], v1, s[16:19], s4 offen offset:1024 sc0 sc1\ns_add_u32 s4, s92, 786432\nbuffer_load_dwordx4 a[240:243], v1, s[16:19], s4 offen offset:0 sc0 sc1\n"
"buffer_load_dwordx4 a[244:247], v1, s[16:19], s4 offen offset:1024 sc0 sc1\ns_add_u32 s4, s92, 917504\nbuffer_load_dwordx4 a[248:251], v1, s[16:19], s4 offen offset:0 sc0 sc1\n"
"buffer_load_dwordx4 a[252:255], v1, s[16:19], s4 offen offset:1024 sc0 sc1\nWV 0\nAR 22,227\nAR 23,231\nv_max_u32 v21, v22, v23\nAR 22,235\nAR 23,239\n"
"v_max3_u32 v21, v21, v22, v23\nAR 22,243\nAR 23,247\nv_max3_u32 v21, v21, v22, v23\nAR 22,251\nAR 23,255\nv_max3_u32 v21, v21, v22, v23\nv_cmp_eq_u32 vcc, -1, v21\ns_nop 0\n"
"s_cbranch_vccz .S283\ns_sub_u32 s7, s7, 1\ns_cbranch_scc0 .S284\ns_branch .S283\n.S285:\ns_mov_b32 s7, 262144\n.S287:\ns_sleep 8\n"
"buffer_load_dwordx4 v[64:67], v89, s[16:19], s40 offen offset:0 sc0 sc1\nbuffer_load_dwordx4 v[68:71], v89, s[16:19], s40 offen offset:16 sc0 sc1\n"
"buffer_load_dwordx4 v[72:75], v89, s[16:19], s40 offen offset:1792 sc0 sc1\nbuffer_load_dwordx4 v[76:79], v89, s[16:19], s40 offen offset:1808 sc0 sc1\nWV 0\n"
"v_xor_b32 v92, s15, v66\nv_xor_b32 v93, s15, v70\nv_xor_b32 v94, s15, v74\nv_or3_b32 v92, v92, v93, v94\nv_xor_b32 v93, s15, v78\nv_or_b32 v92, v92, v93\nv_cmp_ne_u32 vcc, 0, v92\n"
"s_nop 0\ns_cbranch_vccz .S286\ns_sub_u32 s7, s7, 1\ns_cbranch_scc0 .S287\ns_branch .S286\n.S288:\ns_mov_b32 s7, 262144\n.S290:\ns_sleep 8\n"
"buffer_load_dword v27, off, s[16:19], s60 offset:512 sc0 sc1\nWV 0\nv_cmp_ne_u32 vcc, s15, v27\ns_nop 0\ns_cbranch_vccz .S289\ns_sub_u32 s7, s7, 1\ns_cbranch_scc0 .S290\n"
"s_branch .S289\n.S291:\ns_mov_b32 s7, 262144\n.S293:\ns_sleep 8\nbuffer_load_dword v27, v24, s[16:19], s41 offen sc0 sc1\nWV 0\nv_cmp_eq_u32 s[62:63], s15, v27\n"
"s_mov_b32 vcc_hi, 0\ns_nop 2\ns_lshr_b32 s63, s62, 8\ns_or_b32 s62, s62, s63\ns_andn2_b32 vcc_lo, 0xff, s62\ns_mov_b64 vcc, vcc\ns_cbranch_vccz .S292\ns_sub_u32 s7, s7, 1\n"
"s_cbranch_scc0 .S293\ns_branch .S292\n.S305:\ns_mov_b32 s7, 262144\n.S307:\ns_sleep 8\nv_and_b32 v27, 0xf0, v1\nbuffer_load_dword v27, v27, s[16:19], s61 offen offset:12 sc0 sc1\n"
"WV 0\nv_cmp_ne_u32 vcc, s15, v27\ns_nop 0\ns_cbranch_vccz .S306\ns_sub_u32 s7, s7, 1\ns_cbranch_scc0 .S307\ns_branch .S306\n.S308:\ns_mov_b32 s7, 262144\n.S310:\ns_sleep 8\n"
"v_and_b32 v27, 0x1f0, v1\nbuffer_load_dword v27, v27, s[16:19], s58 offen offset:4 sc0 sc1\nWV 0\nv_cmp_ne_u32 vcc, s15, v27\ns_nop 0\ns_cbranch_vccz .S309\ns_sub_u32 s7, s7, 1\n"
"s_cbranch_scc0 .S310\ns_branch .S309\n.RA205:\ns_nop 7\ns_nop 3\nv_max3_f32 v22, v32, v33, v34\nv_max3_f32 v22, v22, v35, v40\nv_max3_f32 v22, v22, v41, v42\n"
"v_max_f32 v22, v22, v43\nv_mov_b32 v91, v22\ns_nop 1\nv_permlane32_swap_b32 v22, v91\nv_max_f32 v22, v22, v91\nv_mov_b32 v91, v22\ns_nop 1\nv_permlane16_swap_b32 v22, v91\n"
"v_max_f32 v22, v22, v91\nv_ceil_f32 v22, v22\nv_max3_f32 v23, v36, v37, v38\nv_max3_f32 v23, v23, v39, v44\nv_max3_f32 v23, v23, v45, v46\nv_max_f32 v23, v23, v47\n"
"v_mov_b32 v91, v23\ns_nop 1\nv_permlane32_swap_b32 v23, v91\nv_max_f32 v23, v23, v91\nv_mov_b32 v91, v23\ns_nop 1\nv_permlane16_swap_b32 v23, v91\nv_max_f32 v23, v23, v91\n"
"v_ceil_f32 v23, v23\ns_bitcmp1_b32 s62, 0\ns_cbranch_scc0 .R311\nv_cmp_lt_f32 vcc, 0xff000000, v22\ns_nop 1\nv_cndmask_b32 v22, 0, v22, vcc\nv_cmp_lt_f32 vcc, 0xff000000, v23\n"
"s_nop 1\nv_cndmask_b32 v23, 0, v23, vcc\ns_branch .R312\n.R311:\nv_max_f32 v22, 0, v22\nv_max_f32 v23, 0, v23\ns_bitcmp1_b32 s62, 3\ns_cbranch_scc0 .R312\nv_max_f32 v22, v20, v22\n"
"v_max_f32 v23, v21, v23\n.R312:\nAD 88,88,22\nAD 89,89,23\nSU 32,32,22\nSU 33,33,22\nSU 34,34,22\nSU 35,35,22\nSU 36,36,23\nSU 37,37,23\nSU 38,38,23\nSU 39,39,23\nSU 40,40,22\n"
"SU 41,41,22\nSU 42,42,22\nSU 43,43,22\nSU 44,44,23\nSU 45,45,23\nSU 46,46,23\nSU 47,47,23\nv_mov_b32 v90, v80\nv_mov_b32 v91, v81\nEX 80,32\nEX 81,33\ns_nop 0\nAD 84,80,81\n"
"CV 64,80,81\nEX 82,34\nEX 83,35\nAD 84,84,82\nAD 84,84,83\nCV 65,82,83\nEX 80,36\nEX 81,37\ns_nop 0\nAD 85,80,81\nCV 68,80,81\nEX 82,38\nEX 83,39\nAD 85,85,82\nAD 85,85,83\n"
"CV 69,82,83\nEX 80,40\nEX 81,41\nAD 84,84,80\nAD 84,84,81\nCV 66,80,81\nEX 82,42\nEX 83,43\nAD 84,84,82\nAD 84,84,83\nCV 67,82,83\nEX 80,44\nEX 81,45\nAD 85,85,80\nAD 85,85,81\n"
"CV 70,80,81\nEX 82,46\nEX 83,47\nAD 85,85,82\nAD 85,85,83\nCV 71,82,83\nv_mov_b32 v80, v90\nv_mov_b32 v81, v91\ns_bitcmp1_b32 s62, 1\ns_cbranch_scc1 .R313\nSU 48,48,22\n"
"SU 49,49,22\nSU 50,50,22\nSU 51,51,22\nSU 52,52,23\nSU 53,53,23\nSU 54,54,23\nSU 55,55,23\nSU 56,56,22\nSU 57,57,22\nSU 58,58,22\nSU 59,59,22\nSU 60,60,23\nSU 61,61,23\n"
"SU 62,62,23\nSU 63,63,23\nSU 4,4,22\nSU 5,5,22\nSU 6,6,22\nSU 7,7,22\nSU 8,8,23\nSU 9,9,23\nSU 10,10,23\nSU 11,11,23\nSU 12,12,22\nSU 13,13,22\nSU 14,14,22\nSU 15,15,22\n"
"SU 16,16,23\nSU 17,17,23\nSU 18,18,23\nSU 19,19,23\nEX 80,48\nEX 81,49\n.R313:\ns_bitcmp1_b32 s62, 0\ns_cbranch_scc0 .RS207\ns_nop 1\ns_setpc_b64 s[80:81]\n.RB206:\ns_nop 7\n"
"s_nop 3\nv_max3_f32 v22, v48, v49, v50\nv_max3_f32 v22, v22, v51, v56\nv_max3_f32 v22, v22, v57, v58\nv_max_f32 v22, v22, v59\nv_mov_b32 v91, v22\ns_nop 1\n"
"v_permlane32_swap_b32 v22, v91\nv_max_f32 v22, v22, v91\nv_mov_b32 v91, v22\ns_nop 1\nv_permlane16_swap_b32 v22, v91\nv_max_f32 v22, v22, v91\nv_ceil_f32 v22, v22\n"
"v_max3_f32 v23, v52, v53, v54\nv_max3_f32 v23, v23, v55, v60\nv_max3_f32 v23, v23, v61, v62\nv_max_f32 v23, v23, v63\nv_mov_b32 v91, v23\ns_nop 1\nv_permlane32_swap_b32 v23, v91\n"
"v_max_f32 v23, v23, v91\nv_mov_b32 v91, v23\ns_nop 1\nv_permlane16_swap_b32 v23, v91\nv_max_f32 v23, v23, v91\nv_ceil_f32 v23, v23\ns_bitcmp1_b32 s62, 0\ns_cbranch_scc0 .R316\n"
"v_cmp_lt_f32 vcc, 0xff000000, v22\nv_cndmask_b32 v22, 0, v22, vcc\nv_cmp_lt_f32 vcc, 0xff000000, v23\nv_cndmask_b32 v23, 0, v23, vcc\ns_branch .R317\n.R316:\n"
"v_max_f32 v22, 0, v22\nv_max_f32 v23, 0, v23\ns_bitcmp1_b32 s62, 3\ns_cbranch_scc0 .R317\nv_max_f32 v22, v20, v22\nv_max_f32 v23, v21, v23\n.R317:\nAD 88,88,22\nAD 89,89,23\n"
"SU 48,48,22\nSU 49,49,22\nSU 50,50,22\nSU 51,51,22\nSU 52,52,23\nSU 53,53,23\nSU 54,54,23\nSU 55,55,23\nSU 56,56,22\nSU 57,57,22\nSU 58,58,22\nSU 59,59,22\nSU 60,60,23\n"
"SU 61,61,23\nSU 62,62,23\nSU 63,63,23\nv_mov_b32 v90, v80\nv_mov_b32 v91, v81\nEX 80,48\nEX 81,49\ns_nop 0\nAD 84,80,81\nCV 72,80,81\nEX 82,50\nEX 83,51\nAD 84,84,82\nAD 84,84,83\n"
"CV 73,82,83\nEX 80,52\nEX 81,53\ns_nop 0\nAD 85,80,81\nCV 76,80,81\nEX 82,54\nEX 83,55\nAD 85,85,82\nAD 85,85,83\nCV 77,82,83\nEX 80,56\nEX 81,57\nAD 84,84,80\nAD 84,84,81\n"
"CV 74,80,81\nEX 82,58\nEX 83,59\nAD 84,84,82\nAD 84,84,83\nCV 75,82,83\nEX 80,60\nEX 81,61\nAD 85,85,80\nAD 85,85,81\nCV 78,80,81\nEX 82,62\nEX 83,63\nAD 85,85,82\nAD 85,85,83\n"
"CV 79,82,83\nv_mov_b32 v80, v90\nv_mov_b32 v81, v91\ns_bitcmp1_b32 s62, 1\ns_cbranch_scc1 .R318\nSU 32,32,22\nSU 33,33,22\nSU 34,34,22\nSU 35,35,22\nSU 36,36,23\nSU 37,37,23\n"
"SU 38,38,23\nSU 39,39,23\nSU 40,40,22\nSU 41,41,22\nSU 42,42,22\nSU 43,43,22\nSU 44,44,23\nSU 45,45,23\nSU 46,46,23\nSU 47,47,23\nSU 4,4,22\nSU 5,5,22\nSU 6,6,22\nSU 7,7,22\n"
"SU 8,8,23\nSU 9,9,23\nSU 10,10,23\nSU 11,11,23\nSU 12,12,22\nSU 13,13,22\nSU 14,14,22\nSU 15,15,22\nSU 16,16,23\nSU 17,17,23\nSU 18,18,23\nSU 19,19,23\nEX 80,32\nEX 81,33\n.R318:\n"
"s_bitcmp1_b32 s62, 0\ns_cbranch_scc0 .RS207\ns_nop 1\ns_setpc_b64 s[80:81]\n.RS207:\nv_exp_f32 v22, -v22\nv_exp_f32 v23, -v23\nMU 86,86,22\nMU 87,87,23\ns_bitcmp1_b32 s62, 2\n"
"s_cbranch_scc1 .R321\nAR 91,0\nMU 91,91,22\nAW 0,91\nAR 91,1\nMU 91,91,22\nAW 1,91\nAR 91,2\nMU 91,91,22\nAW 2,91\nAR 91,3\nMU 91,91,22\nAW 3,91\nAR 91,4\nMU 91,91,23\nAW 4,91\n"
"AR 91,5\nMU 91,91,23\nAW 5,91\nAR 91,6\nMU 91,91,23\nAW 6,91\nAR 91,7\nMU 91,91,23\nAW 7,91\nAR 91,8\nMU 91,91,22\nAW 8,91\nAR 91,9\nMU 91,91,22\nAW 9,91\nAR 91,10\nMU 91,91,22\n"
"AW 10,91\nAR 91,11\nMU 91,91,22\nAW 11,91\nAR 91,12\nMU 91,91,23\nAW 12,91\nAR 91,13\nMU 91,91,23\nAW 13,91\nAR 91,14\nMU 91,91,23\nAW 14,91\nAR 91,15\nMU 91,91,23\nAW 15,91\n"
"AR 91,16\nMU 91,91,22\nAW 16,91\nAR 91,17\nMU 91,91,22\nAW 17,91\nAR 91,18\nMU 91,91,22\nAW 18,91\nAR 91,19\nMU 91,91,22\nAW 19,91\nAR 91,20\nMU 91,91,23\nAW 20,91\nAR 91,21\n"
"MU 91,91,23\nAW 21,91\nAR 91,22\nMU 91,91,23\nAW 22,91\nAR 91,23\nMU 91,91,23\nAW 23,91\nAR 91,24\nMU 91,91,22\nAW 24,91\nAR 91,25\nMU 91,91,22\nAW 25,91\nAR 91,26\nMU 91,91,22\n"
"AW 26,91\nAR 91,27\nMU 91,91,22\nAW 27,91\nAR 91,28\nMU 91,91,23\nAW 28,91\nAR 91,29\nMU 91,91,23\nAW 29,91\nAR 91,30\nMU 91,91,23\nAW 30,91\nAR 91,31\nMU 91,91,23\nAW 31,91\n"
"AR 91,32\nMU 91,91,22\nAW 32,91\nAR 91,33\nMU 91,91,22\nAW 33,91\nAR 91,34\nMU 91,91,22\nAW 34,91\nAR 91,35\nMU 91,91,22\nAW 35,91\nAR 91,36\nMU 91,91,23\nAW 36,91\nAR 91,37\n"
"MU 91,91,23\nAW 37,91\nAR 91,38\nMU 91,91,23\nAW 38,91\nAR 91,39\nMU 91,91,23\nAW 39,91\nAR 91,40\nMU 91,91,22\nAW 40,91\nAR 91,41\nMU 91,91,22\nAW 41,91\nAR 91,42\nMU 91,91,22\n"
"AW 42,91\nAR 91,43\nMU 91,91,22\nAW 43,91\nAR 91,44\nMU 91,91,23\nAW 44,91\nAR 91,45\nMU 91,91,23\nAW 45,91\nAR 91,46\nMU 91,91,23\nAW 46,91\nAR 91,47\nMU 91,91,23\nAW 47,91\n"
"AR 91,48\nMU 91,91,22\nAW 48,91\nAR 91,49\nMU 91,91,22\nAW 49,91\nAR 91,50\nMU 91,91,22\nAW 50,91\nAR 91,51\nMU 91,91,22\nAW 51,91\nAR 91,52\nMU 91,91,23\nAW 52,91\nAR 91,53\n"
"MU 91,91,23\nAW 53,91\nAR 91,54\nMU 91,91,23\nAW 54,91\nAR 91,55\nMU 91,91,23\nAW 55,91\nAR 91,56\nMU 91,91,22\nAW 56,91\nAR 91,57\nMU 91,91,22\nAW 57,91\nAR 91,58\nMU 91,91,22\n"
"AW 58,91\nAR 91,59\nMU 91,91,22\nAW 59,91\nAR 91,60\nMU 91,91,23\nAW 60,91\nAR 91,61\nMU 91,91,23\nAW 61,91\nAR 91,62\nMU 91,91,23\nAW 62,91\nAR 91,63\nMU 91,91,23\nAW 63,91\n"
"AR 91,64\nMU 91,91,22\nAW 64,91\nAR 91,65\nMU 91,91,22\nAW 65,91\nAR 91,66\nMU 91,91,22\nAW 66,91\nAR 91,67\nMU 91,91,22\nAW 67,91\nAR 91,68\nMU 91,91,23\nAW 68,91\nAR 91,69\n"
"MU 91,91,23\nAW 69,91\nAR 91,70\nMU 91,91,23\nAW 70,91\nAR 91,71\nMU 91,91,23\nAW 71,91\nAR 91,72\nMU 91,91,22\nAW 72,91\nAR 91,73\nMU 91,91,22\nAW 73,91\nAR 91,74\nMU 91,91,22\n"
"AW 74,91\nAR 91,75\nMU 91,91,22\nAW 75,91\nAR 91,76\nMU 91,91,23\nAW 76,91\nAR 91,77\nMU 91,91,23\nAW 77,91\nAR 91,78\nMU 91,91,23\nAW 78,91\nAR 91,79\nMU 91,91,23\nAW 79,91\n"
"AR 91,80\nMU 91,91,22\nAW 80,91\nAR 91,81\nMU 91,91,22\nAW 81,91\nAR 91,82\nMU 91,91,22\nAW 82,91\nAR 91,83\nMU 91,91,22\nAW 83,91\nAR 91,84\nMU 91,91,23\nAW 84,91\nAR 91,85\n"
"MU 91,91,23\nAW 85,91\nAR 91,86\nMU 91,91,23\nAW 86,91\nAR 91,87\nMU 91,91,23\nAW 87,91\nAR 91,88\nMU 91,91,22\nAW 88,91\nAR 91,89\nMU 91,91,22\nAW 89,91\nAR 91,90\nMU 91,91,22\n"
"AW 90,91\nAR 91,91\nMU 91,91,22\nAW 91,91\nAR 91,92\nMU 91,91,23\nAW 92,91\nAR 91,93\nMU 91,91,23\nAW 93,91\nAR 91,94\nMU 91,91,23\nAW 94,91\nAR 91,95\nMU 91,91,23\nAW 95,91\n"
"AR 91,96\nMU 91,91,22\nAW 96,91\nAR 91,97\nMU 91,91,22\nAW 97,91\nAR 91,98\nMU 91,91,22\nAW 98,91\nAR 91,99\nMU 91,91,22\nAW 99,91\nAR 91,100\nMU 91,91,23\nAW 100,91\nAR 91,101\n"
"MU 91,91,23\nAW 101,91\nAR 91,102\nMU 91,91,23\nAW 102,91\nAR 91,103\nMU 91,91,23\nAW 103,91\nAR 91,104\nMU 91,91,22\nAW 104,91\nAR 91,105\nMU 91,91,22\nAW 105,91\nAR 91,106\n"
"MU 91,91,22\nAW 106,91\nAR 91,107\nMU 91,91,22\nAW 107,91\nAR 91,108\nMU 91,91,23\nAW 108,91\nAR 91,109\nMU 91,91,23\nAW 109,91\nAR 91,110\nMU 91,91,23\nAW 110,91\nAR 91,111\n"
"MU 91,91,23\nAW 111,91\nAR 91,112\nMU 91,91,22\nAW 112,91\nAR 91,113\nMU 91,91,22\nAW 113,91\nAR 91,114\nMU 91,91,22\nAW 114,91\nAR 91,115\nMU 91,91,22\nAW 115,91\nAR 91,116\n"
"MU 91,91,23\nAW 116,91\nAR 91,117\nMU 91,91,23\nAW 117,91\nAR 91,118\nMU 91,91,23\nAW 118,91\nAR 91,119\nMU 91,91,23\nAW 119,91\nAR 91,120\nMU 91,91,22\nAW 120,91\nAR 91,121\n"
"MU 91,91,22\nAW 121,91\nAR 91,122\nMU 91,91,22\nAW 122,91\nAR 91,123\nMU 91,91,22\nAW 123,91\nAR 91,124\nMU 91,91,23\nAW 124,91\nAR 91,125\nMU 91,91,23\nAW 125,91\nAR 91,126\n"
"MU 91,91,23\nAW 126,91\nAR 91,127\nMU 91,91,23\nAW 127,91\ns_nop 1\ns_setpc_b64 s[80:81]\n.R321:\nMU 128,128,22\nMU 129,129,22\nMU 130,130,22\nMU 131,131,22\nMU 132,132,23\n"
"MU 133,133,23\nMU 134,134,23\nMU 135,135,23\nMU 136,136,22\nMU 137,137,22\nMU 138,138,22\nMU 139,139,22\nMU 140,140,23\nMU 141,141,23\nMU 142,142,23\nMU 143,143,23\nMU 144,144,22\n"
"MU 145,145,22\nMU 146,146,22\nMU 147,147,22\nMU 148,148,23\nMU 149,149,23\nMU 150,150,23\nMU 151,151,23\nMU 152,152,22\nMU 153,153,22\nMU 154,154,22\nMU 155,155,22\nMU 156,156,23\n"
"MU 157,157,23\nMU 158,158,23\nMU 159,159,23\nMU 160,160,22\nMU 161,161,22\nMU 162,162,22\nMU 163,163,22\nMU 164,164,23\nMU 165,165,23\nMU 166,166,23\nMU 167,167,23\nMU 168,168,22\n"
"MU 169,169,22\nMU 170,170,22\nMU 171,171,22\nMU 172,172,23\nMU 173,173,23\nMU 174,174,23\nMU 175,175,23\nMU 176,176,22\nMU 177,177,22\nMU 178,178,22\nMU 179,179,22\nMU 180,180,23\n"
"MU 181,181,23\nMU 182,182,23\nMU 183,183,23\nMU 184,184,22\nMU 185,185,22\nMU 186,186,22\nMU 187,187,22\nMU 188,188,23\nMU 189,189,23\nMU 190,190,23\nMU 191,191,23\nMU 192,192,22\n"
"MU 193,193,22\nMU 194,194,22\nMU 195,195,22\nMU 196,196,23\nMU 197,197,23\nMU 198,198,23\nMU 199,199,23\nMU 200,200,22\nMU 201,201,22\nMU 202,202,22\nMU 203,203,22\nMU 204,204,23\n"
"MU 205,205,23\nMU 206,206,23\nMU 207,207,23\nMU 208,208,22\nMU 209,209,22\nMU 210,210,22\nMU 211,211,22\nMU 212,212,23\nMU 213,213,23\nMU 214,214,23\nMU 215,215,23\nMU 216,216,22\n"
"MU 217,217,22\nMU 218,218,22\nMU 219,219,22\nMU 220,220,23\nMU 221,221,23\nMU 222,222,23\nMU 223,223,23\nMU 224,224,22\nMU 225,225,22\nMU 226,226,22\nMU 227,227,22\nMU 228,228,23\n"
"MU 229,229,23\nMU 230,230,23\nMU 231,231,23\nMU 232,232,22\nMU 233,233,22\nMU 234,234,22\nMU 235,235,22\nMU 236,236,23\nMU 237,237,23\nMU 238,238,23\nMU 239,239,23\nMU 240,240,22\n"
"MU 241,241,22\nMU 242,242,22\nMU 243,243,22\nMU 244,244,23\nMU 245,245,23\nMU 246,246,23\nMU 247,247,23\nMU 248,248,22\nMU 249,249,22\nMU 250,250,22\nMU 251,251,22\nMU 252,252,23\n"
"MU 253,253,23\nMU 254,254,23\nMU 255,255,23\ns_nop 1\ns_setpc_b64 s[80:81]\n.p2align 6\n.PA5:\nWV 24\ns_barrier\nTa 128,12,0\nTa 130,12,2048\nTa 132,13,0\nTa 134,13,2048\n"
"Ta 136,14,0\nTa 138,14,2048\nTa 140,15,0\nTa 142,15,2048\nTa 144,16,0\nTa 146,16,2048\nTa 148,17,0\nTa 150,17,2048\nTa 152,18,0\nTa 154,18,2048\nTa 156,19,0\nTa 158,19,2048\n"
"Ra 160,10,0\nRa 164,10,2048\nRa 168,10,8192\nRa 172,10,10240\nRa 176,10,16384\nRa 180,10,18432\nRa 184,10,24576\nRa 188,10,26624\ns_waitcnt vmcnt(20) lgkmcnt(0)\ns_barrier\n"
"Zvaa 64,160,128\nTa 192,12,16384\nZvaa 68,164,128\ns_and_b32 s86, s13, 6\nZvaa 128,168,128\nTa 194,12,18432\nZvaa 132,172,128\ns_add_u32 m0, s47, 49152\n"
"s_add_u32 s44, s44, 0x40000\nZaaa 0,176,128\nTa 196,13,16384\nZaaa 4,180,128\nD0 6,24,44,0\nZaaa 64,184,128\nTa 198,13,18432\nZaaa 68,188,128\ns_and_b32 s87, s12, 1\n"
"Zaaa 76,188,132\nTa 200,14,16384\nZaaa 72,184,132\ns_add_u32 s86, s86, s87\nZaaa 12,180,132\nTa 202,14,18432\nZaaa 8,176,132\ns_lshl2_add_u32 s86, s86, s11\nZvaa 140,172,132\n"
"Ta 204,15,16384\nZvaa 136,168,132\nD0 7,24,44,1024\nZvaa 76,164,132\nTa 206,15,18432\nZvaa 72,160,132\ns_lshl_b32 s93, s73, 7\nZvaa 80,160,136\nTa 208,16,16384\nZvaa 84,164,136\n"
"s_lshl2_add_u32 s93, s86, s93\nZvaa 144,168,136\nTa 210,16,18432\nZvaa 148,172,136\ns_add_u32 s93, s93, 0xc04ac00\nZaaa 16,176,136\nTa 212,17,16384\nZaaa 20,180,136\n"
"D0 8,24,44,2048\nZaaa 80,184,136\nTa 214,17,18432\nZaaa 84,188,136\ns_lshl_b32 s86, s86, 14\nZaaa 92,188,140\nTa 216,18,16384\nZaaa 88,184,140\ns_mul_i32 s87, s73, 25165824\n"
"Zaaa 28,180,140\nTa 218,18,18432\nZaaa 24,176,140\ns_add_u32 s86, s86, s87\nZvaa 156,172,140\nTa 220,19,16384\nZvaa 152,168,140\nD0 9,24,44,3072\nZvaa 92,164,140\nTa 222,19,18432\n"
"Zvaa 88,160,140\nbuffer_load_dword v31, v24, s[24:27], s96 offen\ns_add_u32 s96, s96, 0x80000\nZvaa 96,160,144\nRa 224,11,0\nZvaa 100,164,144\ns_add_u32 s94, s86, 0x1500000\n"
"Zvaa 160,168,144\nRa 228,11,2048\nZvaa 164,172,144\ns_add_u32 m0, s47, 0\ns_add_u32 s44, s44, 0x40000\nZaaa 32,176,144\nRa 232,11,8192\nZaaa 36,180,144\nD0 6,24,44,0\n"
"Zaaa 96,184,144\nRa 236,11,10240\nZaaa 100,188,144\ns_add_u32 s95, s94, 0x2000\nZaaa 108,188,148\nRa 240,11,16384\nZaaa 104,184,148\ns_mov_b32 s92, 1024\nZaaa 44,180,148\n"
"Ra 244,11,18432\nZaaa 40,176,148\nv_lshrrev_b32 v60, 3, v0\nZvaa 172,172,148\nRa 248,11,24576\nZvaa 168,168,148\nD0 7,24,44,1024\nZvaa 108,164,148\nRa 252,11,26624\n"
"Zvaa 104,160,148\nv_and_b32 v61, 0x70, v1\nZvaa 112,160,152\nv_lshl_or_b32 v60, v60, 10, v61\nZvaa 116,164,152\nSv 2,60,16,94,0\nZvaa 176,168,152\nZvaa 180,172,152\n"
"Zaaa 48,176,152\nZaaa 52,180,152\nD0 8,24,44,2048\nZaaa 112,184,152\nZaaa 116,188,152\nSv 2,60,16,95,0\nZaaa 124,188,156\ns_add_u32 s86, s94, 512\nZaaa 120,184,156\n"
"s_load_dword s87, s[54:55], s86\nZaaa 60,180,156\nZaaa 56,176,156\nZvaa 188,172,156\nZvaa 184,168,156\nD0 9,24,44,3072\nZvaa 124,164,156\nZvaa 120,160,156\n"
"s_waitcnt vmcnt(15) lgkmcnt(0)\ns_barrier\nMvaa 64,224,192\nTa 128,12,32768\nMvaa 68,228,192\nv_lshlrev_b32 v48, 16, v32\nMvaa 128,232,192\nTa 130,12,34816\nMvaa 132,236,192\n"
"s_add_u32 m0, s48, 0\ns_add_u32 s45, s45, 128\nMaaa 0,240,192\nTa 132,13,32768\nv_lshlrev_b32 v49, 16, v36\nMaaa 4,244,192\nD0 2,20,45,0\nMaaa 64,248,192\nTa 134,13,34816\n"
"Maaa 68,252,192\nv_lshlrev_b32 v50, 16, v40\nv_lshlrev_b32 v51, 16, v44\nMaaa 76,252,196\nTa 136,14,32768\nMaaa 72,248,196\nMU 52,48,50\nMaaa 12,244,196\nTa 138,14,34816\n"
"MU 53,49,50\nMaaa 8,240,196\nCV 52,52,53\nMvaa 140,236,196\nTa 140,15,32768\nMvaa 136,232,196\nD0 3,20,45,1024\nv_lshlrev_b32 v54, 16, v52\nMvaa 76,228,196\nTa 142,15,34816\n"
"Mvaa 72,224,196\nv_and_b32 v55, 0xffff0000, v52\nMvaa 80,224,200\nTa 144,16,32768\nMU 56,48,51\nMvaa 84,228,200\nMU 57,49,51\nMvaa 144,232,200\nTa 146,16,34816\nMvaa 148,236,200\n"
"v_and_b32 v48, 0xffff0000, v32\nMaaa 16,240,200\nTa 148,17,32768\nv_and_b32 v49, 0xffff0000, v36\nMaaa 20,244,200\nD0 4,20,45,2048\nMaaa 80,248,200\nTa 150,17,34816\n"
"Maaa 84,252,200\nv_and_b32 v50, 0xffff0000, v40\nv_and_b32 v51, 0xffff0000, v44\nMaaa 92,252,204\nTa 152,18,32768\nMaaa 88,248,204\nMU 52,48,50\nMaaa 28,244,204\nTa 154,18,34816\n"
"MU 53,49,50\nMaaa 24,240,204\nCV 52,52,53\nMvaa 156,236,204\nTa 156,19,32768\nMvaa 152,232,204\nD0 5,20,45,3072\nv_and_b32 v53, 0xffff0000, v52\nMvaa 92,228,204\nTa 158,19,34816\n"
"Mvaa 88,224,204\nv_lshlrev_b32 v52, 16, v52\nMvaa 96,224,208\nRa 160,10,32768\nFM 56,48,51\nMvaa 100,228,208\nFM 57,49,51\nMvaa 160,232,208\nRa 164,10,34816\nMvaa 164,236,208\n"
"s_add_u32 m0, s48, 4096\ns_add_u32 s46, s45, 0x10000\nMaaa 32,240,208\nRa 168,10,40960\nAD 54,54,52\nMaaa 36,244,208\nD0 2,20,46,0\nMaaa 96,248,208\nRa 172,10,43008\n"
"Maaa 100,252,208\nAD 55,55,53\nv_lshlrev_b32 v48, 16, v33\nMaaa 108,252,212\nRa 176,10,49152\nMaaa 104,248,212\nv_lshlrev_b32 v49, 16, v37\nMaaa 44,244,212\nRa 180,10,51200\n"
"v_lshlrev_b32 v50, 16, v41\nMaaa 40,240,212\nv_lshlrev_b32 v51, 16, v45\nMvaa 172,236,212\nRa 184,10,57344\nMvaa 168,232,212\nD0 3,20,46,1024\nMU 52,48,50\nMvaa 108,228,212\n"
"Ra 188,10,59392\nMvaa 104,224,212\nMU 53,49,50\nMvaa 112,224,216\nCV 52,52,53\nv_and_b32 v53, 0xffff0000, v52\nMvaa 116,228,216\nv_lshlrev_b32 v52, 16, v52\nMvaa 176,232,216\n"
"FM 56,48,51\nMvaa 180,236,216\nFM 57,49,51\nMaaa 48,240,216\nAD 54,54,52\nAD 55,55,53\nMaaa 52,244,216\nD0 4,20,46,2048\nMaaa 112,248,216\nv_and_b32 v48, 0xffff0000, v33\n"
"Maaa 116,252,216\nv_and_b32 v49, 0xffff0000, v37\nv_and_b32 v50, 0xffff0000, v41\nMaaa 124,252,220\nv_and_b32 v51, 0xffff0000, v45\nMaaa 120,248,220\nMU 52,48,50\nMaaa 60,244,220\n"
"MU 53,49,50\nCV 52,52,53\nMaaa 56,240,220\nv_and_b32 v53, 0xffff0000, v52\nMvaa 188,236,220\nv_lshlrev_b32 v52, 16, v52\nMvaa 184,232,220\nD0 5,20,46,3072\nFM 56,48,51\n"
"Mvaa 124,228,220\nFM 57,49,51\nMvaa 120,224,220\ns_waitcnt vmcnt(15) lgkmcnt(0)\ns_barrier\nMvaa 64,160,128\nTa 192,12,49152\nAD 54,54,52\nMvaa 68,164,128\nAD 55,55,53\n"
"Mvaa 128,168,128\nTa 194,12,51200\nMvaa 132,172,128\ns_add_u32 m0, s47, 16384\ns_add_u32 s44, s44, 0x40000\nMaaa 0,176,128\nTa 196,13,49152\nv_lshlrev_b32 v48, 16, v34\n"
"Maaa 4,180,128\nD0 6,24,44,0\nMaaa 64,184,128\nTa 198,13,51200\nMaaa 68,188,128\nv_lshlrev_b32 v49, 16, v38\nv_lshlrev_b32 v50, 16, v42\nMaaa 76,188,132\nTa 200,14,49152\n"
"Maaa 72,184,132\nv_lshlrev_b32 v51, 16, v46\nMaaa 12,180,132\nTa 202,14,51200\nMU 52,48,50\nMaaa 8,176,132\nMU 53,49,50\nMvaa 140,172,132\nTa 204,15,49152\nMvaa 136,168,132\n"
"D0 7,24,44,1024\nCV 52,52,53\nMvaa 76,164,132\nTa 206,15,51200\nMvaa 72,160,132\nv_and_b32 v53, 0xffff0000, v52\nMvaa 80,160,136\nTa 208,16,49152\nv_lshlrev_b32 v52, 16, v52\n"
"Mvaa 84,164,136\nFM 56,48,51\nMvaa 144,168,136\nTa 210,16,51200\nMvaa 148,172,136\nFM 57,49,51\nMaaa 16,176,136\nTa 212,17,49152\nAD 54,54,52\nMaaa 20,180,136\nD0 8,24,44,2048\n"
"Maaa 80,184,136\nTa 214,17,51200\nMaaa 84,188,136\nAD 55,55,53\nv_and_b32 v48, 0xffff0000, v34\nMaaa 92,188,140\nTa 216,18,49152\nMaaa 88,184,140\nv_and_b32 v49, 0xffff0000, v38\n"
"Maaa 28,180,140\nTa 218,18,51200\nv_and_b32 v50, 0xffff0000, v42\nMaaa 24,176,140\nv_and_b32 v51, 0xffff0000, v46\nMvaa 156,172,140\nTa 220,19,49152\nMvaa 152,168,140\n"
"D0 9,24,44,3072\nMU 52,48,50\nMvaa 92,164,140\nTa 222,19,51200\nMvaa 88,160,140\nbuffer_load_dword v31, v24, s[24:27], s96 offen\ns_add_u32 s96, s96, 0x80000\nMvaa 96,160,144\n"
"Ra 224,11,32768\nMU 53,49,50\nMvaa 100,164,144\nCV 52,52,53\nMvaa 160,168,144\nRa 228,11,34816\nMvaa 164,172,144\ns_add_u32 m0, s47, 32768\ns_add_u32 s44, s44, 0x40000\n"
"Maaa 32,176,144\nRa 232,11,40960\nv_and_b32 v53, 0xffff0000, v52\nMaaa 36,180,144\nD0 6,24,44,0\nMaaa 96,184,144\nRa 236,11,43008\nMaaa 100,188,144\nv_lshlrev_b32 v52, 16, v52\n"
"FM 56,48,51\nMaaa 108,188,148\nRa 240,11,49152\nMaaa 104,184,148\nFM 57,49,51\nMaaa 44,180,148\nRa 244,11,51200\nAD 54,54,52\nMaaa 40,176,148\nAD 55,55,53\nMvaa 172,172,148\n"
"Ra 248,11,57344\nMvaa 168,168,148\nD0 7,24,44,1024\nv_lshlrev_b32 v48, 16, v35\nMvaa 108,164,148\nRa 252,11,59392\nMvaa 104,160,148\nv_lshlrev_b32 v49, 16, v39\nMvaa 112,160,152\n"
"v_lshlrev_b32 v50, 16, v43\nv_lshlrev_b32 v51, 16, v47\nMvaa 116,164,152\nMU 52,48,50\nMvaa 176,168,152\nMU 53,49,50\nMvaa 180,172,152\nCV 52,52,53\nMaaa 48,176,152\n"
"v_and_b32 v53, 0xffff0000, v52\nv_lshlrev_b32 v52, 16, v52\nMaaa 52,180,152\nD0 8,24,44,2048\nMaaa 112,184,152\nFM 56,48,51\nMaaa 116,188,152\nFM 57,49,51\nAD 54,54,52\n"
"Maaa 124,188,156\nAD 55,55,53\nMaaa 120,184,156\nv_and_b32 v48, 0xffff0000, v35\nMaaa 60,180,156\nv_and_b32 v49, 0xffff0000, v39\nv_and_b32 v50, 0xffff0000, v43\nMaaa 56,176,156\n"
"v_and_b32 v51, 0xffff0000, v47\nMvaa 188,172,156\nMU 52,48,50\nMvaa 184,168,156\nD0 9,24,44,3072\nMU 53,49,50\nMvaa 124,164,156\nCV 52,52,53\nMvaa 120,160,156\n"
"s_waitcnt vmcnt(9) lgkmcnt(0)\ns_barrier\nMvaa 64,224,192\nTa 128,12,0\nv_and_b32 v53, 0xffff0000, v52\nMvaa 68,228,192\nv_lshlrev_b32 v52, 16, v52\nMvaa 128,232,192\n"
"Ta 130,12,2048\nMvaa 132,236,192\ns_add_u32 m0, s48, 32768\ns_add_u32 s45, s45, 128\nMaaa 0,240,192\nTa 132,13,0\nFM 56,48,51\nMaaa 4,244,192\nD0 2,20,45,0\nMaaa 64,248,192\n"
"Ta 134,13,2048\nMaaa 68,252,192\nFM 57,49,51\nAD 54,54,52\nMaaa 76,252,196\nTa 136,14,0\nMaaa 72,248,196\nAD 55,55,53\nMaaa 12,244,196\nTa 138,14,2048\n"
"v_permlane32_swap_b32 v54, v55\nMaaa 8,240,196\nv_permlane32_swap_b32 v56, v57\nMvaa 140,236,196\nTa 140,15,0\nMvaa 136,232,196\nD0 3,20,45,1024\nAD 54,54,55\nMvaa 76,228,196\n"
"Ta 142,15,2048\nMvaa 72,224,196\nAD 56,56,57\nMvaa 80,224,200\nTa 144,16,0\nMvaa 84,228,200\nv_permlane16_swap_b32 v54, v56\nMvaa 144,232,200\nTa 146,16,2048\nMvaa 148,236,200\n"
"AD 54,54,56\nMaaa 16,240,200\nTa 148,17,0\nMaaa 20,244,200\nD0 4,20,45,2048\nMaaa 80,248,200\nTa 150,17,2048\nMaaa 84,252,200\n"
"v_add_f32_dpp v54, v54, v54 row_ror:8 row_mask:0xf bank_mask:0xf\nMaaa 92,252,204\nTa 152,18,0\nMaaa 88,248,204\nv_add_f32_dpp v54, v54, v54 row_ror:4 row_mask:0xf bank_mask:0xf\n"
"Maaa 28,244,204\nTa 154,18,2048\nMaaa 24,240,204\nv_add_f32_dpp v54, v54, v54 row_ror:2 row_mask:0xf bank_mask:0xf\nMvaa 156,236,204\nTa 156,19,0\nMvaa 152,232,204\n"
"D0 5,20,45,3072\nv_add_f32_dpp v54, v54, v54 row_ror:1 row_mask:0xf bank_mask:0xf\nMvaa 92,228,204\nTa 158,19,2048\nMvaa 88,224,204\nCV 54,54,54\nMvaa 96,224,208\nRa 160,10,0\n"
"v_lshlrev_b32 v54, 16, v54\nMvaa 100,228,208\nv_mul_f32 v55, 0xbfb8aa3b, v54\nEX 55,55\nMvaa 160,232,208\nRa 164,10,2048\nMvaa 164,236,208\ns_add_u32 m0, s48, 36864\n"
"s_add_u32 s46, s45, 0x10000\nMaaa 32,240,208\nRa 168,10,8192\nv_add_f32 v56, 0, v54\nMaaa 36,244,208\nD0 2,20,46,0\nMaaa 96,248,208\nRa 172,10,10240\nMaaa 100,252,208\n"
"v_add_f32 v55, 1.0, v55\nMaaa 108,252,212\nRa 176,10,16384\nv_rcp_f32 v55, v55\nMaaa 104,248,212\nv_ashrrev_i32 v57, 31, v56\nMaaa 44,244,212\nRa 180,10,18432\nCV 55,55,55\n"
"Maaa 40,240,212\nv_lshlrev_b32 v55, 16, v55\nMvaa 172,236,212\nRa 184,10,24576\nMvaa 168,232,212\nD0 3,20,46,1024\nv_or_b32 v57, 0x80000000, v57\nMvaa 108,228,212\n"
"Ra 188,10,26624\nMvaa 104,224,212\nv_xor_b32 v56, v56, v57\nMvaa 112,224,216\nv_and_b32 v56, 0xffff0000, v56\ns_lshl2_add_u32 s2, s10, s11\nMvaa 116,228,216\n"
"v_lshrrev_b32 v48, 5, v0\nMvaa 176,232,216\nv_lshl_add_u32 v48, s2, 1, v48\nMvaa 180,236,216\nv_sub_u32 v49, 0xffff, v48\nMaaa 48,240,216\nv_or_b32 v56, v56, v49\n"
"v_mov_b32 v57, s15\nMaaa 52,244,216\nD0 4,20,46,2048\nMaaa 112,248,216\nv_and_b32 v49, 31, v0\nMaaa 116,252,216\nv_mov_b32 v52, 0x40000000\nv_cmp_eq_u32 vcc, 16, v49\n"
"v_lshlrev_b32 v50, 3, v48\nv_lshlrev_b32 v51, 9, v49\nv_cndmask_b32 v50, v52, v50, vcc\nv_cndmask_b32 v55, v55, v54, vcc\nMaaa 124,252,220\nv_lshl_add_u32 v51, v48, 2, v51\n"
"Maaa 120,248,220\nv_and_b32 v49, 15, v0\nv_cmp_eq_u32 vcc, 0, v49\ns_mov_b32 s2, 0xc040000\ns_mov_b32 s3, 0xc044000\nv_cndmask_b32 v51, v52, v51, vcc\n"
"buffer_store_dwordx2 v[56:57], v50, s[16:19], s3 offen sc0 sc1\nbuffer_store_dword v55, v51, s[16:19], s2 offen sc0 sc1\nMaaa 60,244,220\nMaaa 56,240,220\nMvaa 188,236,220\n"
"Mvaa 184,232,220\nD0 5,20,46,3072\nMvaa 124,228,220\nMvaa 120,224,220\ns_waitcnt vmcnt(15) lgkmcnt(0)\ns_barrier\ns_mov_b32 s50, 2\ns_mov_b32 s88, 0\ns_mov_b32 s89, 0\n.GV326:\n"
"Mvaa 64,160,128\nTa 192,12,16384\nMvaa 68,164,128\ns_mov_b32 s86, 0x1d2c3b4a\ns_load_dword s86, s[54:55], s94\nMvaa 128,168,128\nTa 194,12,18432\ns_add_u32 s94, s94, s92\n"
"Mvaa 132,172,128\ns_add_u32 m0, s47, 49152\ns_add_u32 s44, s44, 0x40000\nMaaa 0,176,128\nTa 196,13,16384\nMaaa 4,180,128\nD0 6,24,44,0\nMaaa 64,184,128\nTa 198,13,18432\n"
"Maaa 68,188,128\ns_cmp_eq_u32 s86, 0x1d2c3b4a\ns_addc_u32 s88, s88, 0\nMaaa 76,188,132\nTa 200,14,16384\nMaaa 72,184,132\nMaaa 12,180,132\nTa 202,14,18432\nMaaa 8,176,132\n"
"s_cmp_eq_u32 s86, 0x1d2c3b4a\ns_addc_u32 s88, s88, 0\nMvaa 140,172,132\nTa 204,15,16384\nMvaa 136,168,132\nD0 7,24,44,1024\nMvaa 76,164,132\nTa 206,15,18432\nMvaa 72,160,132\n"
"s_cmp_eq_u32 s86, 0x1d2c3b4a\ns_addc_u32 s88, s88, 0\nMvaa 80,160,136\nTa 208,16,16384\nMvaa 84,164,136\nMvaa 144,168,136\nTa 210,16,18432\nMvaa 148,172,136\n"
"s_cmp_eq_u32 s86, 0x1d2c3b4a\ns_addc_u32 s88, s88, 0\nMaaa 16,176,136\nTa 212,17,16384\nMaaa 20,180,136\nD0 8,24,44,2048\nMaaa 80,184,136\nTa 214,17,18432\nMaaa 84,188,136\n"
"s_cmp_eq_u32 s86, 0x1d2c3b4a\ns_addc_u32 s88, s88, 0\nMaaa 92,188,140\nTa 216,18,16384\nMaaa 88,184,140\nMaaa 28,180,140\nTa 218,18,18432\nMaaa 24,176,140\n"
"s_cmp_eq_u32 s86, 0x1d2c3b4a\ns_addc_u32 s88, s88, 0\nMvaa 156,172,140\nTa 220,19,16384\nMvaa 152,168,140\nD0 9,24,44,3072\nMvaa 92,164,140\nTa 222,19,18432\nMvaa 88,160,140\n"
"buffer_load_dword v31, v24, s[24:27], s96 offen\ns_add_u32 s96, s96, 0x80000\ns_cmp_eq_u32 s86, 0x1d2c3b4a\ns_addc_u32 s88, s88, 0\nMvaa 96,160,144\nRa 224,11,0\nMvaa 100,164,144\n"
"Mvaa 160,168,144\nRa 228,11,2048\nMvaa 164,172,144\ns_add_u32 m0, s47, 0\ns_add_u32 s44, s44, 0x40000\ns_cmp_eq_u32 s86, 0x1d2c3b4a\ns_addc_u32 s88, s88, 0\nMaaa 32,176,144\n"
"Ra 232,11,8192\nMaaa 36,180,144\nD0 6,24,44,0\nMaaa 96,184,144\nRa 236,11,10240\nMaaa 100,188,144\ns_cmp_eq_u32 s86, 0x1d2c3b4a\ns_addc_u32 s88, s88, 0\nMaaa 108,188,148\n"
"Ra 240,11,16384\nMaaa 104,184,148\nMaaa 44,180,148\nRa 244,11,18432\nMaaa 40,176,148\ns_cmp_eq_u32 s86, 0x1d2c3b4a\ns_addc_u32 s88, s88, 0\nMvaa 172,172,148\nRa 248,11,24576\n"
"Mvaa 168,168,148\nD0 7,24,44,1024\nMvaa 108,164,148\nRa 252,11,26624\nMvaa 104,160,148\ns_cmp_eq_u32 s86, 0x1d2c3b4a\ns_addc_u32 s88, s88, 0\nMvaa 112,160,152\nMvaa 116,164,152\n"
"Mvaa 176,168,152\nMvaa 180,172,152\ns_cmp_eq_u32 s86, 0x1d2c3b4a\ns_addc_u32 s88, s88, 0\nMaaa 48,176,152\nMaaa 52,180,152\nD0 8,24,44,2048\nMaaa 112,184,152\nMaaa 116,188,152\n"
"s_cmp_eq_u32 s86, 0x1d2c3b4a\ns_addc_u32 s88, s88, 0\nMaaa 124,188,156\nMaaa 120,184,156\nMaaa 60,180,156\nMaaa 56,176,156\ns_cmp_eq_u32 s86, 0x1d2c3b4a\ns_addc_u32 s88, s88, 0\n"
"Mvaa 188,172,156\nMvaa 184,168,156\nD0 9,24,44,3072\nMvaa 124,164,156\nMvaa 120,160,156\ns_waitcnt vmcnt(9) lgkmcnt(0)\ns_barrier\nMvaa 64,224,192\nTa 128,12,32768\n"
"Mvaa 68,228,192\ns_mov_b32 s86, 0x1d2c3b4a\ns_load_dword s86, s[54:55], s95\nMvaa 128,232,192\nTa 130,12,34816\ns_add_u32 s95, s95, s92\nMvaa 132,236,192\ns_add_u32 m0, s48, 0\n"
"s_add_u32 s45, s45, 128\nMaaa 0,240,192\nTa 132,13,32768\nMaaa 4,244,192\nD0 2,20,45,0\nMaaa 64,248,192\nTa 134,13,34816\nMaaa 68,252,192\ns_cmp_eq_u32 s86, 0x1d2c3b4a\n"
"s_addc_u32 s89, s89, 0\nMaaa 76,252,196\nTa 136,14,32768\nMaaa 72,248,196\nMaaa 12,244,196\nTa 138,14,34816\nMaaa 8,240,196\ns_cmp_eq_u32 s86, 0x1d2c3b4a\ns_addc_u32 s89, s89, 0\n"
"Mvaa 140,236,196\nTa 140,15,32768\nMvaa 136,232,196\nD0 3,20,45,1024\nMvaa 76,228,196\nTa 142,15,34816\nMvaa 72,224,196\ns_cmp_eq_u32 s86, 0x1d2c3b4a\ns_addc_u32 s89, s89, 0\n"
"Mvaa 80,224,200\nTa 144,16,32768\nMvaa 84,228,200\nMvaa 144,232,200\nTa 146,16,34816\nMvaa 148,236,200\ns_cmp_eq_u32 s86, 0x1d2c3b4a\ns_addc_u32 s89, s89, 0\nMaaa 16,240,200\n"
"Ta 148,17,32768\nMaaa 20,244,200\nD0 4,20,45,2048\nMaaa 80,248,200\nTa 150,17,34816\nMaaa 84,252,200\ns_cmp_eq_u32 s86, 0x1d2c3b4a\ns_addc_u32 s89, s89, 0\nMaaa 92,252,204\n"
"Ta 152,18,32768\nMaaa 88,248,204\nMaaa 28,244,204\nTa 154,18,34816\nMaaa 24,240,204\ns_cmp_eq_u32 s86, 0x1d2c3b4a\ns_addc_u32 s89, s89, 0\nMvaa 156,236,204\nTa 156,19,32768\n"
"Mvaa 152,232,204\nD0 5,20,45,3072\nMvaa 92,228,204\nTa 158,19,34816\nMvaa 88,224,204\ns_cmp_eq_u32 s86, 0x1d2c3b4a\ns_addc_u32 s89, s89, 0\nMvaa 96,224,208\nRa 160,10,32768\n"
"Mvaa 100,228,208\nMvaa 160,232,208\nRa 164,10,34816\nMvaa 164,236,208\ns_add_u32 m0, s48, 4096\ns_add_u32 s46, s45, 0x10000\ns_cmp_eq_u32 s86, 0x1d2c3b4a\ns_addc_u32 s89, s89, 0\n"
"Maaa 32,240,208\nRa 168,10,40960\nMaaa 36,244,208\nD0 2,20,46,0\nMaaa 96,248,208\nRa 172,10,43008\nMaaa 100,252,208\ns_cmp_eq_u32 s86, 0x1d2c3b4a\ns_addc_u32 s89, s89, 0\n"
"Maaa 108,252,212\nRa 176,10,49152\nMaaa 104,248,212\nMaaa 44,244,212\nRa 180,10,51200\nMaaa 40,240,212\ns_cmp_eq_u32 s86, 0x1d2c3b4a\ns_addc_u32 s89, s89, 0\nMvaa 172,236,212\n"
"Ra 184,10,57344\nMvaa 168,232,212\nD0 3,20,46,1024\nMvaa 108,228,212\nRa 188,10,59392\nMvaa 104,224,212\ns_cmp_eq_u32 s86, 0x1d2c3b4a\ns_addc_u32 s89, s89, 0\nMvaa 112,224,216\n"
"Mvaa 116,228,216\nMvaa 176,232,216\nMvaa 180,236,216\ns_cmp_eq_u32 s86, 0x1d2c3b4a\ns_addc_u32 s89, s89, 0\nMaaa 48,240,216\nMaaa 52,244,216\nD0 4,20,46,2048\nMaaa 112,248,216\n"
"Maaa 116,252,216\ns_cmp_eq_u32 s86, 0x1d2c3b4a\ns_addc_u32 s89, s89, 0\nMaaa 124,252,220\nMaaa 120,248,220\nMaaa 60,244,220\nMaaa 56,240,220\ns_cmp_eq_u32 s86, 0x1d2c3b4a\n"
"s_addc_u32 s89, s89, 0\nMvaa 188,236,220\nMvaa 184,232,220\nD0 5,20,46,3072\nMvaa 124,228,220\nMvaa 120,224,220\ns_waitcnt vmcnt(13) lgkmcnt(0)\ns_barrier\nMvaa 64,160,128\n"
"Ta 192,12,49152\nMvaa 68,164,128\ns_mov_b32 s86, 0x1d2c3b4a\ns_load_dword s86, s[54:55], s95\nMvaa 128,168,128\nTa 194,12,51200\ns_add_u32 s95, s95, s92\nMvaa 132,172,128\n"
"s_add_u32 m0, s47, 16384\ns_add_u32 s44, s44, 0x40000\nMaaa 0,176,128\nTa 196,13,49152\nMaaa 4,180,128\nD0 6,24,44,0\nMaaa 64,184,128\nTa 198,13,51200\nMaaa 68,188,128\n"
"s_cmp_eq_u32 s86, 0x1d2c3b4a\ns_addc_u32 s89, s89, 0\nMaaa 76,188,132\nTa 200,14,49152\nMaaa 72,184,132\nMaaa 12,180,132\nTa 202,14,51200\nMaaa 8,176,132\n"
"s_cmp_eq_u32 s86, 0x1d2c3b4a\ns_addc_u32 s89, s89, 0\nMvaa 140,172,132\nTa 204,15,49152\nMvaa 136,168,132\nD0 7,24,44,1024\nMvaa 76,164,132\nTa 206,15,51200\nMvaa 72,160,132\n"
"s_cmp_eq_u32 s86, 0x1d2c3b4a\ns_addc_u32 s89, s89, 0\nMvaa 80,160,136\nTa 208,16,49152\nMvaa 84,164,136\nMvaa 144,168,136\nTa 210,16,51200\nMvaa 148,172,136\n"
"s_cmp_eq_u32 s86, 0x1d2c3b4a\ns_addc_u32 s89, s89, 0\nMaaa 16,176,136\nTa 212,17,49152\nMaaa 20,180,136\nD0 8,24,44,2048\nMaaa 80,184,136\nTa 214,17,51200\nMaaa 84,188,136\n"
"s_cmp_eq_u32 s86, 0x1d2c3b4a\ns_addc_u32 s89, s89, 0\nMaaa 92,188,140\nTa 216,18,49152\nMaaa 88,184,140\nMaaa 28,180,140\nTa 218,18,51200\nMaaa 24,176,140\n"
"s_cmp_eq_u32 s86, 0x1d2c3b4a\ns_addc_u32 s89, s89, 0\nMvaa 156,172,140\nTa 220,19,49152\nMvaa 152,168,140\nD0 9,24,44,3072\nMvaa 92,164,140\nTa 222,19,51200\nMvaa 88,160,140\n"
"buffer_load_dword v31, v24, s[24:27], s96 offen\ns_add_u32 s96, s96, 0x80000\ns_cmp_eq_u32 s86, 0x1d2c3b4a\ns_addc_u32 s89, s89, 0\nMvaa 96,160,144\nRa 224,11,32768\n"
"Mvaa 100,164,144\nMvaa 160,168,144\nRa 228,11,34816\nMvaa 164,172,144\ns_add_u32 m0, s47, 32768\ns_add_u32 s44, s44, 0x40000\ns_cmp_eq_u32 s86, 0x1d2c3b4a\ns_addc_u32 s89, s89, 0\n"
"Maaa 32,176,144\nRa 232,11,40960\nMaaa 36,180,144\nD0 6,24,44,0\nMaaa 96,184,144\nRa 236,11,43008\nMaaa 100,188,144\ns_cmp_eq_u32 s86, 0x1d2c3b4a\ns_addc_u32 s89, s89, 0\n"
"Maaa 108,188,148\nRa 240,11,49152\nMaaa 104,184,148\nMaaa 44,180,148\nRa 244,11,51200\nMaaa 40,176,148\ns_cmp_eq_u32 s86, 0x1d2c3b4a\ns_addc_u32 s89, s89, 0\nMvaa 172,172,148\n"
"Ra 248,11,57344\nMvaa 168,168,148\nD0 7,24,44,1024\nMvaa 108,164,148\nRa 252,11,59392\nMvaa 104,160,148\ns_cmp_eq_u32 s86, 0x1d2c3b4a\ns_addc_u32 s89, s89, 0\nMvaa 112,160,152\n"
"Mvaa 116,164,152\nMvaa 176,168,152\nMvaa 180,172,152\ns_cmp_eq_u32 s86, 0x1d2c3b4a\ns_addc_u32 s89, s89, 0\nMaaa 48,176,152\nMaaa 52,180,152\nD0 8,24,44,2048\nMaaa 112,184,152\n"
"Maaa 116,188,152\ns_cmp_eq_u32 s86, 0x1d2c3b4a\ns_addc_u32 s89, s89, 0\nMaaa 124,188,156\nMaaa 120,184,156\nMaaa 60,180,156\nMaaa 56,176,156\ns_cmp_eq_u32 s86, 0x1d2c3b4a\n"
"s_addc_u32 s89, s89, 0\nMvaa 188,172,156\nMvaa 184,168,156\nD0 9,24,44,3072\nMvaa 124,164,156\nMvaa 120,160,156\ns_waitcnt vmcnt(9) lgkmcnt(0)\ns_barrier\nMvaa 64,224,192\n"
"Ta 128,12,0\nMvaa 68,228,192\ns_mov_b32 s86, 0x1d2c3b4a\ns_load_dword s86, s[54:55], s94\nMvaa 128,232,192\nTa 130,12,2048\ns_add_u32 s94, s94, s92\nMvaa 132,236,192\n"
"s_add_u32 m0, s48, 32768\ns_add_u32 s45, s45, 128\nMaaa 0,240,192\nTa 132,13,0\nMaaa 4,244,192\nD0 2,20,45,0\nMaaa 64,248,192\nTa 134,13,2048\nMaaa 68,252,192\n"
"s_cmp_eq_u32 s86, 0x1d2c3b4a\ns_addc_u32 s88, s88, 0\nMaaa 76,252,196\nTa 136,14,0\nMaaa 72,248,196\nMaaa 12,244,196\nTa 138,14,2048\nMaaa 8,240,196\ns_cmp_eq_u32 s86, 0x1d2c3b4a\n"
"s_addc_u32 s88, s88, 0\nMvaa 140,236,196\nTa 140,15,0\nMvaa 136,232,196\nD0 3,20,45,1024\nMvaa 76,228,196\nTa 142,15,2048\nMvaa 72,224,196\ns_cmp_eq_u32 s86, 0x1d2c3b4a\n"
"s_addc_u32 s88, s88, 0\nMvaa 80,224,200\nTa 144,16,0\nMvaa 84,228,200\nMvaa 144,232,200\nTa 146,16,2048\nMvaa 148,236,200\ns_cmp_eq_u32 s86, 0x1d2c3b4a\ns_addc_u32 s88, s88, 0\n"
"Maaa 16,240,200\nTa 148,17,0\nMaaa 20,244,200\nD0 4,20,45,2048\nMaaa 80,248,200\nTa 150,17,2048\nMaaa 84,252,200\ns_cmp_eq_u32 s86, 0x1d2c3b4a\ns_addc_u32 s88, s88, 0\n"
"Maaa 92,252,204\nTa 152,18,0\nMaaa 88,248,204\nMaaa 28,244,204\nTa 154,18,2048\nMaaa 24,240,204\ns_cmp_eq_u32 s86, 0x1d2c3b4a\ns_addc_u32 s88, s88, 0\nMvaa 156,236,204\n"
"Ta 156,19,0\nMvaa 152,232,204\nD0 5,20,45,3072\nMvaa 92,228,204\nTa 158,19,2048\nMvaa 88,224,204\ns_cmp_eq_u32 s86, 0x1d2c3b4a\ns_addc_u32 s88, s88, 0\nMvaa 96,224,208\n"
"Ra 160,10,0\nMvaa 100,228,208\nMvaa 160,232,208\nRa 164,10,2048\nMvaa 164,236,208\ns_add_u32 m0, s48, 36864\ns_add_u32 s46, s45, 0x10000\ns_cmp_eq_u32 s86, 0x1d2c3b4a\n"
"s_addc_u32 s88, s88, 0\nMaaa 32,240,208\nRa 168,10,8192\nMaaa 36,244,208\nD0 2,20,46,0\nMaaa 96,248,208\nRa 172,10,10240\nMaaa 100,252,208\ns_cmp_eq_u32 s86, 0x1d2c3b4a\n"
"s_addc_u32 s88, s88, 0\nMaaa 108,252,212\nRa 176,10,16384\nMaaa 104,248,212\nMaaa 44,244,212\nRa 180,10,18432\nMaaa 40,240,212\ns_cmp_eq_u32 s86, 0x1d2c3b4a\n"
"s_addc_u32 s88, s88, 0\nMvaa 172,236,212\nRa 184,10,24576\nMvaa 168,232,212\nD0 3,20,46,1024\nMvaa 108,228,212\nRa 188,10,26624\nMvaa 104,224,212\ns_cmp_eq_u32 s86, 0x1d2c3b4a\n"
"s_addc_u32 s88, s88, 0\nMvaa 112,224,216\nMvaa 116,228,216\nMvaa 176,232,216\nMvaa 180,236,216\ns_cmp_eq_u32 s86, 0x1d2c3b4a\ns_addc_u32 s88, s88, 0\nMaaa 48,240,216\n"
"Maaa 52,244,216\nD0 4,20,46,2048\nMaaa 112,248,216\nMaaa 116,252,216\ns_cmp_eq_u32 s86, 0x1d2c3b4a\ns_addc_u32 s88, s88, 0\nMaaa 124,252,220\nMaaa 120,248,220\nMaaa 60,244,220\n"
"Maaa 56,240,220\ns_cmp_eq_u32 s86, 0x1d2c3b4a\ns_addc_u32 s88, s88, 0\nMvaa 188,236,220\nMvaa 184,232,220\nD0 5,20,46,3072\nMvaa 124,228,220\nMvaa 120,224,220\n"
"s_waitcnt vmcnt(13) lgkmcnt(0)\ns_barrier\ns_sub_u32 s50, s50, 1\ns_cbranch_scc0 .GV326\nMvaa 64,160,128\nTa 192,12,16384\nMvaa 68,164,128\ns_cmp_lt_u32 s89, s88\n"
"s_cselect_b32 s88, 0x2000, 0\nMvaa 128,168,128\nTa 194,12,18432\nMvaa 132,172,128\ns_add_u32 m0, s47, 49152\ns_add_u32 s44, s44, 0x40000\nMaaa 0,176,128\nTa 196,13,16384\n"
"Maaa 4,180,128\nD0 6,24,44,0\nMaaa 64,184,128\nTa 198,13,18432\nMaaa 68,188,128\nv_xor_b32 v27, s88, v25\nMaaa 76,188,132\nTa 200,14,16384\nMaaa 72,184,132\n"
"buffer_store_dword v27, off, s[16:19], s93 sc0 sc1\nMaaa 12,180,132\nTa 202,14,18432\nMaaa 8,176,132\nMvaa 140,172,132\nTa 204,15,16384\nMvaa 136,168,132\nD0 7,24,44,1024\n"
"Mvaa 76,164,132\nTa 206,15,18432\nMvaa 72,160,132\nMvaa 80,160,136\nTa 208,16,16384\nMvaa 84,164,136\nMvaa 144,168,136\nTa 210,16,18432\nMvaa 148,172,136\nMaaa 16,176,136\n"
"Ta 212,17,16384\nMaaa 20,180,136\nD0 8,24,44,2048\nMaaa 80,184,136\nTa 214,17,18432\nMaaa 84,188,136\nMaaa 92,188,140\nTa 216,18,16384\nMaaa 88,184,140\nMaaa 28,180,140\n"
"Ta 218,18,18432\nMaaa 24,176,140\nMvaa 156,172,140\nTa 220,19,16384\nMvaa 152,168,140\nD0 9,24,44,3072\nMvaa 92,164,140\nTa 222,19,18432\nMvaa 88,160,140\n"
"buffer_load_dword v31, v24, s[24:27], s96 offen\ns_add_u32 s96, s96, 0x80000\nMvaa 96,160,144\nRa 224,11,0\nMvaa 100,164,144\nMvaa 160,168,144\nRa 228,11,2048\nMvaa 164,172,144\n"
"s_add_u32 m0, s47, 0\ns_add_u32 s44, s44, 0x40000\nMaaa 32,176,144\nRa 232,11,8192\nMaaa 36,180,144\nD0 6,24,44,0\nMaaa 96,184,144\nRa 236,11,10240\nMaaa 100,188,144\n"
"Maaa 108,188,148\nRa 240,11,16384\nMaaa 104,184,148\nMaaa 44,180,148\nRa 244,11,18432\nMaaa 40,176,148\nMvaa 172,172,148\nRa 248,11,24576\nMvaa 168,168,148\nD0 7,24,44,1024\n"
"Mvaa 108,164,148\nRa 252,11,26624\nMvaa 104,160,148\nMvaa 112,160,152\nMvaa 116,164,152\nMvaa 176,168,152\nMvaa 180,172,152\nMaaa 48,176,152\nMaaa 52,180,152\nD0 8,24,44,2048\n"
"Maaa 112,184,152\nMaaa 116,188,152\nMaaa 124,188,156\nMaaa 120,184,156\nMaaa 60,180,156\nMaaa 56,176,156\nMvaa 188,172,156\nMvaa 184,168,156\nD0 9,24,44,3072\nMvaa 124,164,156\n"
"Mvaa 120,160,156\ns_waitcnt vmcnt(10) lgkmcnt(0)\ns_barrier\nMvaa 64,224,192\nTa 128,12,32768\nMvaa 68,228,192\nMvaa 128,232,192\nTa 130,12,34816\nMvaa 132,236,192\n"
"s_add_u32 m0, s48, 0\ns_add_u32 s45, s45, 128\nMaaa 0,240,192\nTa 132,13,32768\nMaaa 4,244,192\nD0 2,20,45,0\nMaaa 64,248,192\nTa 134,13,34816\nMaaa 68,252,192\nMaaa 76,252,196\n"
"Ta 136,14,32768\nMaaa 72,248,196\nMaaa 12,244,196\nTa 138,14,34816\nMaaa 8,240,196\nMvaa 140,236,196\nTa 140,15,32768\nMvaa 136,232,196\nD0 3,20,45,1024\nMvaa 76,228,196\n"
"Ta 142,15,34816\nMvaa 72,224,196\nMvaa 80,224,200\nTa 144,16,32768\nMvaa 84,228,200\nMvaa 144,232,200\nTa 146,16,34816\nMvaa 148,236,200\nMaaa 16,240,200\nTa 148,17,32768\n"
"Maaa 20,244,200\nD0 4,20,45,2048\nMaaa 80,248,200\nTa 150,17,34816\nMaaa 84,252,200\nMaaa 92,252,204\nTa 152,18,32768\nMaaa 88,248,204\nMaaa 28,244,204\nTa 154,18,34816\n"
"Maaa 24,240,204\nMvaa 156,236,204\nTa 156,19,32768\nMvaa 152,232,204\nD0 5,20,45,3072\nMvaa 92,228,204\nTa 158,19,34816\nMvaa 88,224,204\nMvaa 96,224,208\nRa 160,10,32768\n"
"Mvaa 100,228,208\nMvaa 160,232,208\nRa 164,10,34816\nMvaa 164,236,208\ns_add_u32 m0, s48, 4096\ns_add_u32 s46, s45, 0x10000\nMaaa 32,240,208\nRa 168,10,40960\nMaaa 36,244,208\n"
"D0 2,20,46,0\nMaaa 96,248,208\nRa 172,10,43008\nMaaa 100,252,208\nMaaa 108,252,212\nRa 176,10,49152\nMaaa 104,248,212\nMaaa 44,244,212\nRa 180,10,51200\nMaaa 40,240,212\n"
"Mvaa 172,236,212\nRa 184,10,57344\nMvaa 168,232,212\nD0 3,20,46,1024\nMvaa 108,228,212\nRa 188,10,59392\nMvaa 104,224,212\nMvaa 112,224,216\nMvaa 116,228,216\nMvaa 176,232,216\n"
"Mvaa 180,236,216\nMaaa 48,240,216\nMaaa 52,244,216\nD0 4,20,46,2048\nMaaa 112,248,216\nMaaa 116,252,216\nMaaa 124,252,220\nMaaa 120,248,220\nMaaa 60,244,220\nMaaa 56,240,220\n"
"Mvaa 188,236,220\nMvaa 184,232,220\nD0 5,20,46,3072\nMvaa 124,228,220\nMvaa 120,224,220\ns_waitcnt vmcnt(13) lgkmcnt(0)\ns_barrier\nMvaa 64,160,128\nTa 192,12,49152\n"
"Mvaa 68,164,128\nMvaa 128,168,128\nTa 194,12,51200\nMvaa 132,172,128\ns_add_u32 m0, s47, 16384\ns_add_u32 s44, s44, 0x40000\nMaaa 0,176,128\nTa 196,13,49152\nMaaa 4,180,128\n"
"D0 6,24,44,0\nMaaa 64,184,128\nTa 198,13,51200\nMaaa 68,188,128\nMaaa 76,188,132\nTa 200,14,49152\nMaaa 72,184,132\nMaaa 12,180,132\nTa 202,14,51200\nMaaa 8,176,132\n"
"Mvaa 140,172,132\nTa 204,15,49152\nMvaa 136,168,132\nD0 7,24,44,1024\nMvaa 76,164,132\nTa 206,15,51200\nMvaa 72,160,132\nMvaa 80,160,136\nTa 208,16,49152\nMvaa 84,164,136\n"
"Mvaa 144,168,136\nTa 210,16,51200\nMvaa 148,172,136\nMaaa 16,176,136\nTa 212,17,49152\nMaaa 20,180,136\nD0 8,24,44,2048\nMaaa 80,184,136\nTa 214,17,51200\nMaaa 84,188,136\n"
"Maaa 92,188,140\nTa 216,18,49152\nMaaa 88,184,140\nMaaa 28,180,140\nTa 218,18,51200\nMaaa 24,176,140\nMvaa 156,172,140\nTa 220,19,49152\nMvaa 152,168,140\nD0 9,24,44,3072\n"
"Mvaa 92,164,140\nTa 222,19,51200\nMvaa 88,160,140\nbuffer_load_dword v31, v24, s[24:27], s96 offen\ns_add_u32 s96, s96, 0x80000\nMvaa 96,160,144\nRa 224,11,32768\n"
"Mvaa 100,164,144\ns_lshl_b32 s5, s73, 7\ns_add_u32 s5, s5, 0xc04ac00\nv_bfe_u32 v27, v1, 2, 7\nbuffer_load_dword v27, v27, s[16:19], s5 offen sc0 sc1\nMvaa 160,168,144\n"
"Ra 228,11,34816\nMvaa 164,172,144\ns_add_u32 m0, s47, 32768\ns_add_u32 s44, s44, 0x40000\nMaaa 32,176,144\nRa 232,11,40960\nMaaa 36,180,144\nD0 6,24,44,0\nMaaa 96,184,144\n"
"Ra 236,11,43008\nMaaa 100,188,144\nMaaa 108,188,148\nRa 240,11,49152\nMaaa 104,184,148\nMaaa 44,180,148\nRa 244,11,51200\nMaaa 40,176,148\nMvaa 172,172,148\nRa 248,11,57344\n"
"Mvaa 168,168,148\nD0 7,24,44,1024\nMvaa 108,164,148\nRa 252,11,59392\nMvaa 104,160,148\nMvaa 112,160,152\nMvaa 116,164,152\nMvaa 176,168,152\nMvaa 180,172,152\nMaaa 48,176,152\n"
"Maaa 52,180,152\nD0 8,24,44,2048\nMaaa 112,184,152\nMaaa 116,188,152\nMaaa 124,188,156\nMaaa 120,184,156\nMaaa 60,180,156\nMaaa 56,176,156\nMvaa 188,172,156\nMvaa 184,168,156\n"
"D0 9,24,44,3072\nMvaa 124,164,156\nMvaa 120,160,156\ns_waitcnt vmcnt(10) lgkmcnt(0)\ns_barrier\nMvaa 64,224,192\nTa 128,12,0\nMvaa 68,228,192\nMvaa 128,232,192\nTa 130,12,2048\n"
"Mvaa 132,236,192\ns_add_u32 m0, s48, 32768\ns_add_u32 s45, s45, 128\nMaaa 0,240,192\nTa 132,13,0\nMaaa 4,244,192\nD0 2,20,45,0\nMaaa 64,248,192\nTa 134,13,2048\nMaaa 68,252,192\n"
"Maaa 76,252,196\nTa 136,14,0\nMaaa 72,248,196\nMaaa 12,244,196\nTa 138,14,2048\nMaaa 8,240,196\nMvaa 140,236,196\nTa 140,15,0\nMvaa 136,232,196\nD0 3,20,45,1024\nMvaa 76,228,196\n"
"Ta 142,15,2048\nMvaa 72,224,196\nMvaa 80,224,200\nTa 144,16,0\nMvaa 84,228,200\nMvaa 144,232,200\nTa 146,16,2048\nMvaa 148,236,200\nMaaa 16,240,200\nTa 148,17,0\nMaaa 20,244,200\n"
"D0 4,20,45,2048\nMaaa 80,248,200\nTa 150,17,2048\nMaaa 84,252,200\nMaaa 92,252,204\nTa 152,18,0\nMaaa 88,248,204\nMaaa 28,244,204\nTa 154,18,2048\nMaaa 24,240,204\n"
"Mvaa 156,236,204\nTa 156,19,0\nMvaa 152,232,204\nD0 5,20,45,3072\nMvaa 92,228,204\nTa 158,19,2048\nMvaa 88,224,204\nMvaa 96,224,208\nRa 160,10,0\nMvaa 100,228,208\n"
"Mvaa 160,232,208\nRa 164,10,2048\nMvaa 164,236,208\ns_add_u32 m0, s48, 36864\ns_add_u32 s46, s45, 0x10000\nMaaa 32,240,208\nRa 168,10,8192\nWV 8\nv_xor_b32 v27, s15, v27\n"
"v_and_b32 v32, 0xffffdfff, v27\nv_cmp_ne_u32 vcc, 0, v32\ns_nop 0\ns_cbranch_vccnz .S323\n.S324:\nv_cmp_ne_u32 vcc, 0, v27\ns_nop 3\ns_bcnt1_i32_b64 s4, vcc\ns_cmp_gt_u32 s4, 32\n"
"s_cselect_b32 s4, 1, 0\nMaaa 36,244,208\nD0 2,20,46,0\nMaaa 96,248,208\nRa 172,10,10240\nMaaa 100,252,208\ns_xor_b32 s4, s4, s12\nMaaa 108,252,212\nRa 176,10,16384\n"
"Maaa 104,248,212\ns_and_b32 s4, s4, 1\nMaaa 44,244,212\nRa 180,10,18432\nMaaa 40,240,212\ns_lshl_b32 s4, s4, 13\nMvaa 172,236,212\nRa 184,10,24576\nMvaa 168,232,212\n"
"D0 3,20,46,1024\nMvaa 108,228,212\nRa 188,10,26624\nMvaa 104,224,212\ns_mul_i32 s72, s73, 25165824\nMvaa 112,224,216\ns_add_u32 s72, s72, s4\nMvaa 116,228,216\n"
"s_and_b32 s4, s12, 1\nMvaa 176,232,216\ns_lshl_b32 s4, s4, 21\nMvaa 180,236,216\ns_add_u32 s72, s72, s4\nMaaa 48,240,216\ns_lshl3_add_u32 s2, s13, s11\nMaaa 52,244,216\n"
"D0 4,20,46,2048\nMaaa 112,248,216\ns_lshl_b32 s2, s2, 11\nMaaa 116,252,216\ns_lshl_b32 s3, s14, 22\nMaaa 124,252,220\ns_add_u32 s2, s2, s3\nMaaa 120,248,220\n"
"s_add_u32 s2, s2, s72\nMaaa 60,244,220\ns_add_u32 s49, s2, 0xc00000\nMaaa 56,240,220\nMvaa 188,236,220\nMvaa 184,232,220\nD0 5,20,46,3072\nMvaa 124,228,220\nMvaa 120,224,220\n"
"WL 0\ns_barrier\nMvaa 64,160,128\nTa 192,12,16384\ns_mov_b32 s36, s60\nMvaa 68,164,128\ns_and_b32 s37, s61, 0xffff\nMvaa 128,168,128\nTa 194,12,18432\nMvaa 132,172,128\n"
"s_add_u32 m0, s47, 49152\ns_add_u32 s44, s44, 0x40000\nMaaa 0,176,128\nTa 196,13,16384\nMaaa 4,180,128\nD0 6,24,44,0\nMaaa 64,184,128\nTa 198,13,18432\nMaaa 68,188,128\n"
"s_mov_b32 s38, 0x380000\nMaaa 76,188,132\nTa 200,14,16384\nMaaa 72,184,132\ns_mov_b32 s39, 0x20000\nMaaa 12,180,132\nTa 202,14,18432\nMaaa 8,176,132\ns_lshr_b32 s4, s13, 1\n"
"Mvaa 140,172,132\nTa 204,15,16384\nMvaa 136,168,132\nD0 7,24,44,1024\nMvaa 76,164,132\nTa 206,15,18432\nMvaa 72,160,132\ns_lshl2_add_u32 s4, s14, s4\nMvaa 80,160,136\n"
"Ta 208,16,16384\ns_lshl3_add_u32 s2, s12, s4\nMvaa 84,164,136\ns_lshl2_add_u32 s2, s2, s11\nMvaa 144,168,136\nTa 210,16,18432\nMvaa 148,172,136\ns_mul_i32 s2, s2, 7168\n"
"Maaa 16,176,136\nTa 212,17,16384\nMaaa 20,180,136\nD0 8,24,44,2048\nMaaa 80,184,136\nTa 214,17,18432\nMaaa 84,188,136\ns_mul_i32 s3, s11, 7168\nMaaa 92,188,140\nTa 216,18,16384\n"
"Maaa 88,184,140\ns_add_u32 s3, s3, 131072\nMaaa 28,180,140\nTa 218,18,18432\nMaaa 24,176,140\nMvaa 156,172,140\nTa 220,19,16384\nMvaa 152,168,140\nD0 9,24,44,3072\n"
"Mvaa 92,164,140\nTa 222,19,18432\nMvaa 88,160,140\nbuffer_load_dword v31, v24, s[24:27], s96 offen\ns_add_u32 s96, s96, 0x80000\nMvaa 96,160,144\nRa 224,11,0\ns_add_u32 m0, s3, 0\n"
"s_add_u32 s5, s2, 0\nD0 1,36,5,0\nD0 1,36,5,1024\nMvaa 100,164,144\nMvaa 160,168,144\nRa 228,11,2048\nMvaa 164,172,144\ns_add_u32 m0, s47, 0\ns_add_u32 s44, s44, 0x40000\n"
"Maaa 32,176,144\nRa 232,11,8192\nMaaa 36,180,144\nD0 6,24,44,0\nMaaa 96,184,144\nRa 236,11,10240\nMaaa 100,188,144\nMaaa 108,188,148\nRa 240,11,16384\nMaaa 104,184,148\n"
"Maaa 44,180,148\nRa 244,11,18432\nMaaa 40,176,148\nMvaa 172,172,148\nRa 248,11,24576\nMvaa 168,168,148\nD0 7,24,44,1024\nMvaa 108,164,148\nRa 252,11,26624\nMvaa 104,160,148\n"
"Mvaa 112,160,152\nMvaa 116,164,152\nMvaa 176,168,152\nMvaa 180,172,152\nMaaa 48,176,152\nMaaa 52,180,152\nD0 8,24,44,2048\nMaaa 112,184,152\nMaaa 116,188,152\nMaaa 124,188,156\n"
"Maaa 120,184,156\nMaaa 60,180,156\nMaaa 56,176,156\nMvaa 188,172,156\nMvaa 184,168,156\nD0 9,24,44,3072\nMvaa 124,164,156\ns_add_u32 m0, s3, 2048\ns_add_u32 s5, s2, 2048\n"
"D0 1,36,5,0\nD0 1,36,5,1024\nMvaa 120,160,156\ns_waitcnt vmcnt(13) lgkmcnt(0)\ns_barrier\nMvaa 64,224,192\nTa 128,12,32768\nMvaa 68,228,192\nMvaa 128,232,192\nTa 130,12,34816\n"
"Mvaa 132,236,192\ns_add_u32 m0, s48, 0\ns_add_u32 s45, s45, 128\nMaaa 0,240,192\nTa 132,13,32768\nMaaa 4,244,192\nD0 2,20,45,0\nMaaa 64,248,192\nTa 134,13,34816\nMaaa 68,252,192\n"
"Maaa 76,252,196\nTa 136,14,32768\nMaaa 72,248,196\nMaaa 12,244,196\nTa 138,14,34816\nMaaa 8,240,196\nMvaa 140,236,196\nTa 140,15,32768\nMvaa 136,232,196\nD0 3,20,45,1024\n"
"Mvaa 76,228,196\nTa 142,15,34816\nMvaa 72,224,196\nMvaa 80,224,200\nTa 144,16,32768\nMvaa 84,228,200\nMvaa 144,232,200\nTa 146,16,34816\nMvaa 148,236,200\nMaaa 16,240,200\n"
"Ta 148,17,32768\nMaaa 20,244,200\nD0 4,20,45,2048\nMaaa 80,248,200\nTa 150,17,34816\nMaaa 84,252,200\nMaaa 92,252,204\nTa 152,18,32768\nMaaa 88,248,204\nMaaa 28,244,204\n"
"Ta 154,18,34816\nMaaa 24,240,204\nMvaa 156,236,204\nTa 156,19,32768\nMvaa 152,232,204\nD0 5,20,45,3072\nMvaa 92,228,204\nTa 158,19,34816\nMvaa 88,224,204\nMvaa 96,224,208\n"
"Ra 160,10,32768\nMvaa 100,228,208\nMvaa 160,232,208\nRa 164,10,34816\nMvaa 164,236,208\ns_add_u32 m0, s48, 4096\ns_add_u32 s46, s45, 0x10000\nMaaa 32,240,208\nRa 168,10,40960\n"
"Maaa 36,244,208\nD0 2,20,46,0\nMaaa 96,248,208\nRa 172,10,43008\nMaaa 100,252,208\nMaaa 108,252,212\nRa 176,10,49152\nMaaa 104,248,212\nMaaa 44,244,212\nRa 180,10,51200\n"
"Maaa 40,240,212\nMvaa 172,236,212\nRa 184,10,57344\nMvaa 168,232,212\nD0 3,20,46,1024\nMvaa 108,228,212\nRa 188,10,59392\nMvaa 104,224,212\nMvaa 112,224,216\nMvaa 116,228,216\n"
"Mvaa 176,232,216\nMvaa 180,236,216\nMaaa 48,240,216\nMaaa 52,244,216\nD0 4,20,46,2048\nMaaa 112,248,216\nMaaa 116,252,216\nMaaa 124,252,220\nMaaa 120,248,220\nMaaa 60,244,220\n"
"Maaa 56,240,220\nMvaa 188,236,220\nMvaa 184,232,220\nD0 5,20,46,3072\nMvaa 124,228,220\nMvaa 120,224,220\ns_waitcnt vmcnt(17) lgkmcnt(0)\ns_barrier\nMvaa 64,160,128\n"
"Ta 192,12,49152\nMvaa 68,164,128\nMvaa 128,168,128\nTa 194,12,51200\nMvaa 132,172,128\ns_add_u32 m0, s47, 16384\ns_add_u32 s44, s44, 0x40000\nMaaa 0,176,128\nTa 196,13,49152\n"
"Maaa 4,180,128\nD0 6,24,44,0\nMaaa 64,184,128\nTa 198,13,51200\nMaaa 68,188,128\nMaaa 76,188,132\nTa 200,14,49152\nMaaa 72,184,132\nMaaa 12,180,132\nTa 202,14,51200\n"
"Maaa 8,176,132\nMvaa 140,172,132\nTa 204,15,49152\nMvaa 136,168,132\nD0 7,24,44,1024\nMvaa 76,164,132\nTa 206,15,51200\nMvaa 72,160,132\nMvaa 80,160,136\nTa 208,16,49152\n"
"Mvaa 84,164,136\nMvaa 144,168,136\nTa 210,16,51200\nMvaa 148,172,136\nMaaa 16,176,136\nTa 212,17,49152\nMaaa 20,180,136\nD0 8,24,44,2048\nMaaa 80,184,136\nTa 214,17,51200\n"
"Maaa 84,188,136\nMaaa 92,188,140\nTa 216,18,49152\nMaaa 88,184,140\nMaaa 28,180,140\nTa 218,18,51200\nMaaa 24,176,140\nMvaa 156,172,140\nTa 220,19,49152\nMvaa 152,168,140\n"
"D0 9,24,44,3072\nMvaa 92,164,140\nTa 222,19,51200\nMvaa 88,160,140\nbuffer_load_dword v31, v24, s[24:27], s96 offen\ns_add_u32 s96, s96, 0x80000\nMvaa 96,160,144\nRa 224,11,32768\n"
"s_add_u32 m0, s3, 4096\ns_add_u32 s5, s2, 4096\nD0 1,36,5,0\nD0 1,36,5,1024\nMvaa 100,164,144\nMvaa 160,168,144\nRa 228,11,34816\nMvaa 164,172,144\ns_add_u32 m0, s47, 32768\n"
"s_add_u32 s44, s44, 0x40000\nMaaa 32,176,144\nRa 232,11,40960\nMaaa 36,180,144\nD0 6,24,44,0\nMaaa 96,184,144\nRa 236,11,43008\nMaaa 100,188,144\nMaaa 108,188,148\n"
"Ra 240,11,49152\nMaaa 104,184,148\nMaaa 44,180,148\nRa 244,11,51200\nMaaa 40,176,148\nMvaa 172,172,148\nRa 248,11,57344\nMvaa 168,168,148\nD0 7,24,44,1024\nMvaa 108,164,148\n"
"Ra 252,11,59392\nMvaa 104,160,148\nMvaa 112,160,152\nMvaa 116,164,152\nMvaa 176,168,152\nMvaa 180,172,152\nMaaa 48,176,152\nMaaa 52,180,152\nD0 8,24,44,2048\nMaaa 112,184,152\n"
"Maaa 116,188,152\nMaaa 124,188,156\nMaaa 120,184,156\nMaaa 60,180,156\nMaaa 56,176,156\nMvaa 188,172,156\nMvaa 184,168,156\nD0 9,24,44,3072\nMvaa 124,164,156\n"
"s_add_u32 m0, s3, 6144\ns_add_u32 s5, s2, 6144\nD0 1,36,5,0\nMvaa 120,160,156\ns_waitcnt vmcnt(12) lgkmcnt(0)\ns_barrier\nMvaa 64,224,192\nTa 128,12,0\nMvaa 68,228,192\n"
"Mvaa 128,232,192\nTa 130,12,2048\nMvaa 132,236,192\ns_add_u32 m0, s48, 32768\ns_add_u32 s45, s45, 128\nMaaa 0,240,192\nTa 132,13,0\nMaaa 4,244,192\nD0 2,20,45,0\nMaaa 64,248,192\n"
"Ta 134,13,2048\nMaaa 68,252,192\nMaaa 76,252,196\nTa 136,14,0\nMaaa 72,248,196\nMaaa 12,244,196\nTa 138,14,2048\nMaaa 8,240,196\nMvaa 140,236,196\nTa 140,15,0\nMvaa 136,232,196\n"
"D0 3,20,45,1024\nMvaa 76,228,196\nTa 142,15,2048\nMvaa 72,224,196\nMvaa 80,224,200\nTa 144,16,0\nMvaa 84,228,200\nMvaa 144,232,200\nTa 146,16,2048\nMvaa 148,236,200\n"
"Maaa 16,240,200\nTa 148,17,0\nMaaa 20,244,200\nD0 4,20,45,2048\nMaaa 80,248,200\nTa 150,17,2048\nMaaa 84,252,200\nMaaa 92,252,204\nTa 152,18,0\nMaaa 88,248,204\nMaaa 28,244,204\n"
"Ta 154,18,2048\nMaaa 24,240,204\nMvaa 156,236,204\nTa 156,19,0\nMvaa 152,232,204\nD0 5,20,45,3072\nMvaa 92,228,204\nTa 158,19,2048\nMvaa 88,224,204\nMvaa 96,224,208\nRa 160,10,0\n"
"Mvaa 100,228,208\nMvaa 160,232,208\nRa 164,10,2048\nMvaa 164,236,208\ns_add_u32 m0, s48, 36864\ns_add_u32 s46, s45, 0x10000\nMaaa 32,240,208\nRa 168,10,8192\nMaaa 36,244,208\n"
"D0 2,20,46,0\nMaaa 96,248,208\nRa 172,10,10240\nMaaa 100,252,208\nMaaa 108,252,212\nRa 176,10,16384\nMaaa 104,248,212\nMaaa 44,244,212\nRa 180,10,18432\nMaaa 40,240,212\n"
"Mvaa 172,236,212\nRa 184,10,24576\nMvaa 168,232,212\nD0 3,20,46,1024\nMvaa 108,228,212\nRa 188,10,26624\nMvaa 104,224,212\nMvaa 112,224,216\nMvaa 116,228,216\nMvaa 176,232,216\n"
"Mvaa 180,236,216\nMaaa 48,240,216\nMaaa 52,244,216\nD0 4,20,46,2048\nMaaa 112,248,216\nMaaa 116,252,216\nMaaa 124,252,220\nMaaa 120,248,220\nMaaa 60,244,220\nMaaa 56,240,220\n"
"Mvaa 188,236,220\nMvaa 184,232,220\nD0 5,20,46,3072\nMvaa 124,228,220\nMvaa 120,224,220\ns_waitcnt vmcnt(16) lgkmcnt(0)\ns_barrier\nMvaa 64,160,128\nTa 192,12,16384\n"
"Mvaa 68,164,128\nMvaa 128,168,128\nTa 194,12,18432\nMvaa 132,172,128\ns_add_u32 m0, s47, 49152\ns_add_u32 s44, s44, 0x40000\nMaaa 0,176,128\nTa 196,13,16384\nMaaa 4,180,128\n"
"D0 6,24,44,0\nMaaa 64,184,128\nTa 198,13,18432\nMaaa 68,188,128\nMaaa 76,188,132\nTa 200,14,16384\nMaaa 72,184,132\nMaaa 12,180,132\nTa 202,14,18432\nMaaa 8,176,132\n"
"Mvaa 140,172,132\nTa 204,15,16384\nMvaa 136,168,132\nD0 7,24,44,1024\nMvaa 76,164,132\nTa 206,15,18432\nMvaa 72,160,132\nMvaa 80,160,136\nTa 208,16,16384\nMvaa 84,164,136\n"
"Mvaa 144,168,136\nTa 210,16,18432\nMvaa 148,172,136\nMaaa 16,176,136\nTa 212,17,16384\nMaaa 20,180,136\nD0 8,24,44,2048\nMaaa 80,184,136\nTa 214,17,18432\nMaaa 84,188,136\n"
"Maaa 92,188,140\nTa 216,18,16384\nMaaa 88,184,140\nMaaa 28,180,140\nTa 218,18,18432\nMaaa 24,176,140\nMvaa 156,172,140\nTa 220,19,16384\nMvaa 152,168,140\nD0 9,24,44,3072\n"
"Mvaa 92,164,140\nTa 222,19,18432\nMvaa 88,160,140\nMvaa 96,160,144\nRa 224,11,0\nMvaa 100,164,144\nMvaa 160,168,144\nRa 228,11,2048\nMvaa 164,172,144\ns_add_u32 m0, s47, 0\n"
"s_add_u32 s44, s44, 0x40000\nMaaa 32,176,144\nRa 232,11,8192\nMaaa 36,180,144\nD0 6,24,44,0\nMaaa 96,184,144\nRa 236,11,10240\nMaaa 100,188,144\nMaaa 108,188,148\nRa 240,11,16384\n"
"Maaa 104,184,148\nMaaa 44,180,148\nRa 244,11,18432\nMaaa 40,176,148\nMvaa 172,172,148\nRa 248,11,24576\nMvaa 168,168,148\nD0 7,24,44,1024\nMvaa 108,164,148\nRa 252,11,26624\n"
"Mvaa 104,160,148\nMvaa 112,160,152\nMvaa 116,164,152\nMvaa 176,168,152\nMvaa 180,172,152\nMaaa 48,176,152\nMaaa 52,180,152\nD0 8,24,44,2048\nMaaa 112,184,152\nMaaa 116,188,152\n"
"Maaa 124,188,156\nMaaa 120,184,156\nMaaa 60,180,156\nMaaa 56,176,156\nMvaa 188,172,156\nMvaa 184,168,156\nD0 9,24,44,3072\nMvaa 124,164,156\nMvaa 120,160,156\n"
"s_waitcnt vmcnt(8) lgkmcnt(0)\ns_barrier\nMvaa 64,224,192\nTa 128,12,32768\nv_lshrrev_b32 v32, 2, v0\nMvaa 68,228,192\nv_mul_u32_u24 v32, 896, v32\nMvaa 128,232,192\n"
"Ta 130,12,34816\nv_and_b32 v33, 3, v0\nMvaa 132,236,192\ns_add_u32 m0, s48, 0\ns_add_u32 s45, s45, 128\nMaaa 0,240,192\nTa 132,13,32768\nv_lshl_add_u32 v32, v33, 4, v32\n"
"Maaa 4,244,192\nD0 2,20,45,0\nMaaa 64,248,192\nTa 134,13,34816\ns_lshr_b32 s2, s11, 1\nMaaa 68,252,192\ns_mul_i32 s3, s2, 448\nMaaa 76,252,196\nTa 136,14,32768\n"
"s_and_b32 s5, s11, 1\nMaaa 72,248,196\ns_lshl3_add_u32 s3, s5, s3\nMaaa 12,244,196\nTa 138,14,34816\ns_add_u32 s3, s3, 131072\nMaaa 8,240,196\nv_add_u32 v32, s3, v32\n"
"Mvaa 140,236,196\nTa 140,15,32768\nTv 36,32,0\nMvaa 136,232,196\nD0 3,20,45,1024\nMvaa 76,228,196\nTa 142,15,34816\nTv 38,32,14336\nMvaa 72,224,196\nTv 40,32,64\nMvaa 80,224,200\n"
"Ta 144,16,32768\nTv 42,32,14400\nMvaa 84,228,200\nTv 44,32,128\nMvaa 144,232,200\nTa 146,16,34816\nTv 46,32,14464\nMvaa 148,236,200\nTv 48,32,192\nMaaa 16,240,200\n"
"Ta 148,17,32768\nTv 50,32,14528\nMaaa 20,244,200\nD0 4,20,45,2048\nMaaa 80,248,200\nTa 150,17,34816\nTv 52,32,256\nMaaa 84,252,200\nTv 54,32,14592\nMaaa 92,252,204\n"
"Ta 152,18,32768\nTv 56,32,320\nMaaa 88,248,204\nTv 58,32,14656\nMaaa 28,244,204\nTa 154,18,34816\nTv 60,32,384\nMaaa 24,240,204\nTv 62,32,14720\nMvaa 156,236,204\nTa 156,19,32768\n"
"s_mul_i32 s3, s2, 229376\nMvaa 152,232,204\nD0 5,20,45,3072\nMvaa 92,228,204\nTa 158,19,34816\ns_add_u32 s3, s3, s72\nMvaa 88,224,204\ns_add_u32 s3, s3, 0x1400000\n"
"Mvaa 96,224,208\nRa 160,10,32768\ns_lshr_b32 s0, s4, 1\nMvaa 100,228,208\ns_mul_i32 s0, s0, 28\nMvaa 160,232,208\nRa 164,10,34816\ns_lshl1_add_u32 s0, s5, s0\nMvaa 164,236,208\n"
"s_add_u32 m0, s48, 4096\ns_add_u32 s46, s45, 0x10000\nMaaa 32,240,208\nRa 168,10,40960\ns_and_b32 s6, s4, 1\nMaaa 36,244,208\nD0 2,20,46,0\nMaaa 96,248,208\nRa 172,10,43008\n"
"s_add_u32 s0, s0, s6\nMaaa 100,252,208\ns_lshr_b32 s1, s4, 2\nMaaa 108,252,212\nRa 176,10,49152\ns_mul_i32 s1, s1, 56\nMaaa 104,248,212\ns_lshl2_add_u32 s1, s5, s1\n"
"Maaa 44,244,212\nRa 180,10,51200\ns_and_b32 s6, s4, 3\nMaaa 40,240,212\ns_add_u32 s1, s1, s6\nMvaa 172,236,212\nRa 184,10,57344\ns_and_b32 s4, s12, 1\nMvaa 168,232,212\n"
"D0 3,20,46,1024\nMvaa 108,228,212\nRa 188,10,59392\ns_mov_b32 s7, 0x6543210\nMvaa 104,224,212\ns_cmp_eq_u32 s4, s2\ns_cselect_b32 s6, s0, s1\ns_cselect_b32 s5, 4, 8\n"
"s_cselect_b32 s7, 0x6352410, s7\nMvaa 112,224,216\nWL 14\ns_lshr_b32 s0, s6, 4\nMvaa 116,228,216\ns_lshl_b32 s0, s0, 2\nMvaa 176,232,216\ns_lshr_b32 s0, s7, s0\n"
"s_and_b32 s0, s0, 7\nMvaa 180,236,216\ns_lshl_b32 s0, s0, 5\nMaaa 48,240,216\ns_and_b32 s1, s6, 15\ns_add_u32 s0, s0, s1\nMaaa 52,244,216\nD0 4,20,46,2048\nMaaa 112,248,216\n"
"s_and_b32 s1, s6, 8\ns_add_u32 s0, s0, s1\nMaaa 116,252,216\ns_lshl_b32 s0, s0, 10\nMaaa 124,252,220\ns_add_u32 s0, s0, s3\nSv 36,1,16,0,0\nMaaa 120,248,220\ns_add_u32 s6, s6, s5\n"
"Maaa 60,244,220\nMaaa 56,240,220\nMvaa 188,236,220\nMvaa 184,232,220\nD0 5,20,46,3072\nMvaa 124,228,220\nMvaa 120,224,220\ns_waitcnt vmcnt(13) lgkmcnt(0)\ns_barrier\n"
"Mvaa 64,160,128\nTa 192,12,49152\nMvaa 68,164,128\ns_lshr_b32 s0, s6, 4\nMvaa 128,168,128\nTa 194,12,51200\ns_lshl_b32 s0, s0, 2\nMvaa 132,172,128\ns_add_u32 m0, s47, 16384\n"
"s_add_u32 s44, s44, 0x40000\nMaaa 0,176,128\nTa 196,13,49152\ns_lshr_b32 s0, s7, s0\nMaaa 4,180,128\nD0 6,24,44,0\nMaaa 64,184,128\nTa 198,13,51200\ns_and_b32 s0, s0, 7\n"
"Maaa 68,188,128\ns_lshl_b32 s0, s0, 5\nMaaa 76,188,132\nTa 200,14,49152\ns_and_b32 s1, s6, 15\nMaaa 72,184,132\ns_add_u32 s0, s0, s1\nMaaa 12,180,132\nTa 202,14,51200\n"
"s_and_b32 s1, s6, 8\nMaaa 8,176,132\ns_add_u32 s0, s0, s1\nMvaa 140,172,132\nTa 204,15,49152\ns_lshl_b32 s0, s0, 10\nMvaa 136,168,132\nD0 7,24,44,1024\nMvaa 76,164,132\n"
"Ta 206,15,51200\ns_add_u32 s0, s0, s3\nMvaa 72,160,132\nSv 40,1,16,0,0\nMvaa 80,160,136\nTa 208,16,49152\ns_add_u32 s6, s6, s5\nMvaa 84,164,136\nMvaa 144,168,136\nTa 210,16,51200\n"
"Mvaa 148,172,136\nMaaa 16,176,136\nTa 212,17,49152\nMaaa 20,180,136\nD0 8,24,44,2048\nMaaa 80,184,136\nTa 214,17,51200\nMaaa 84,188,136\nMaaa 92,188,140\nTa 216,18,49152\n"
"s_lshr_b32 s0, s6, 4\nMaaa 88,184,140\ns_lshl_b32 s0, s0, 2\nMaaa 28,180,140\nTa 218,18,51200\ns_lshr_b32 s0, s7, s0\nMaaa 24,176,140\ns_and_b32 s0, s0, 7\nMvaa 156,172,140\n"
"Ta 220,19,49152\ns_lshl_b32 s0, s0, 5\nMvaa 152,168,140\nD0 9,24,44,3072\nMvaa 92,164,140\nTa 222,19,51200\ns_and_b32 s1, s6, 15\nMvaa 88,160,140\ns_add_u32 s0, s0, s1\n"
"Mvaa 96,160,144\nRa 224,11,32768\ns_and_b32 s1, s6, 8\nMvaa 100,164,144\ns_add_u32 s0, s0, s1\nMvaa 160,168,144\nRa 228,11,34816\ns_lshl_b32 s0, s0, 10\nMvaa 164,172,144\n"
"s_add_u32 m0, s47, 32768\ns_add_u32 s44, s44, 0x40000\nMaaa 32,176,144\nRa 232,11,40960\ns_add_u32 s0, s0, s3\nMaaa 36,180,144\nD0 6,24,44,0\nMaaa 96,184,144\nRa 236,11,43008\n"
"Sv 44,1,16,0,0\nMaaa 100,188,144\ns_add_u32 s6, s6, s5\nMaaa 108,188,148\nRa 240,11,49152\nMaaa 104,184,148\nMaaa 44,180,148\nRa 244,11,51200\nMaaa 40,176,148\nMvaa 172,172,148\n"
"Ra 248,11,57344\nMvaa 168,168,148\nD0 7,24,44,1024\nMvaa 108,164,148\nRa 252,11,59392\nMvaa 104,160,148\ns_lshr_b32 s0, s6, 4\nMvaa 112,160,152\ns_lshl_b32 s0, s0, 2\n"
"s_lshr_b32 s0, s7, s0\nMvaa 116,164,152\ns_and_b32 s0, s0, 7\nMvaa 176,168,152\ns_lshl_b32 s0, s0, 5\ns_and_b32 s1, s6, 15\nMvaa 180,172,152\ns_add_u32 s0, s0, s1\n"
"Maaa 48,176,152\ns_and_b32 s1, s6, 8\ns_add_u32 s0, s0, s1\nMaaa 52,180,152\nD0 8,24,44,2048\nMaaa 112,184,152\ns_lshl_b32 s0, s0, 10\ns_add_u32 s0, s0, s3\nMaaa 116,188,152\n"
"Sv 48,1,16,0,0\nMaaa 124,188,156\ns_add_u32 s6, s6, s5\nMaaa 120,184,156\nMaaa 60,180,156\nMaaa 56,176,156\nMvaa 188,172,156\nMvaa 184,168,156\nD0 9,24,44,3072\nMvaa 124,164,156\n"
"Mvaa 120,160,156\ns_waitcnt vmcnt(11) lgkmcnt(0)\ns_barrier\nMvaa 64,224,192\nTa 128,12,0\ns_lshr_b32 s0, s6, 4\nMvaa 68,228,192\ns_lshl_b32 s0, s0, 2\nMvaa 128,232,192\n"
"Ta 130,12,2048\ns_lshr_b32 s0, s7, s0\nMvaa 132,236,192\ns_add_u32 m0, s48, 32768\ns_add_u32 s45, s45, 128\nMaaa 0,240,192\nTa 132,13,0\ns_and_b32 s0, s0, 7\nMaaa 4,244,192\n"
"D0 2,20,45,0\nMaaa 64,248,192\nTa 134,13,2048\ns_lshl_b32 s0, s0, 5\nMaaa 68,252,192\ns_and_b32 s1, s6, 15\nMaaa 76,252,196\nTa 136,14,0\ns_add_u32 s0, s0, s1\nMaaa 72,248,196\n"
"s_and_b32 s1, s6, 8\nMaaa 12,244,196\nTa 138,14,2048\ns_add_u32 s0, s0, s1\nMaaa 8,240,196\ns_lshl_b32 s0, s0, 10\nMvaa 140,236,196\nTa 140,15,0\ns_add_u32 s0, s0, s3\n"
"Mvaa 136,232,196\nD0 3,20,45,1024\nMvaa 76,228,196\nTa 142,15,2048\nSv 52,1,16,0,0\nMvaa 72,224,196\ns_add_u32 s6, s6, s5\nMvaa 80,224,200\nTa 144,16,0\nMvaa 84,228,200\n"
"Mvaa 144,232,200\nTa 146,16,2048\nMvaa 148,236,200\nMaaa 16,240,200\nTa 148,17,0\nMaaa 20,244,200\nD0 4,20,45,2048\nMaaa 80,248,200\nTa 150,17,2048\nMaaa 84,252,200\n"
"s_lshr_b32 s0, s6, 4\nMaaa 92,252,204\nTa 152,18,0\ns_lshl_b32 s0, s0, 2\nMaaa 88,248,204\ns_lshr_b32 s0, s7, s0\nMaaa 28,244,204\nTa 154,18,2048\ns_and_b32 s0, s0, 7\n"
"Maaa 24,240,204\ns_lshl_b32 s0, s0, 5\nMvaa 156,236,204\nTa 156,19,0\ns_and_b32 s1, s6, 15\nMvaa 152,232,204\nD0 5,20,45,3072\nMvaa 92,228,204\nTa 158,19,2048\n"
"s_add_u32 s0, s0, s1\nMvaa 88,224,204\ns_and_b32 s1, s6, 8\nMvaa 96,224,208\nRa 160,10,0\ns_add_u32 s0, s0, s1\nMvaa 100,228,208\ns_lshl_b32 s0, s0, 10\nMvaa 160,232,208\n"
"Ra 164,10,2048\ns_add_u32 s0, s0, s3\nMvaa 164,236,208\ns_add_u32 m0, s48, 36864\ns_add_u32 s46, s45, 0x10000\nMaaa 32,240,208\nRa 168,10,8192\nSv 56,1,16,0,0\nMaaa 36,244,208\n"
"D0 2,20,46,0\nMaaa 96,248,208\nRa 172,10,10240\ns_add_u32 s6, s6, s5\nMaaa 100,252,208\nMaaa 108,252,212\nRa 176,10,16384\nMaaa 104,248,212\nMaaa 44,244,212\nRa 180,10,18432\n"
"Maaa 40,240,212\nMvaa 172,236,212\nRa 184,10,24576\nMvaa 168,232,212\nD0 3,20,46,1024\nMvaa 108,228,212\nRa 188,10,26624\ns_lshr_b32 s0, s6, 4\nMvaa 104,224,212\n"
"s_lshl_b32 s0, s0, 2\nMvaa 112,224,216\ns_lshr_b32 s0, s7, s0\ns_and_b32 s0, s0, 7\nMvaa 116,228,216\ns_lshl_b32 s0, s0, 5\nMvaa 176,232,216\ns_and_b32 s1, s6, 15\n"
"s_add_u32 s0, s0, s1\nMvaa 180,236,216\ns_and_b32 s1, s6, 8\nMaaa 48,240,216\ns_add_u32 s0, s0, s1\ns_lshl_b32 s0, s0, 10\nMaaa 52,244,216\nD0 4,20,46,2048\nMaaa 112,248,216\n"
"s_add_u32 s0, s0, s3\nSv 60,1,16,0,0\nMaaa 116,252,216\ns_add_u32 s6, s6, s5\nMaaa 124,252,220\nMaaa 120,248,220\nMaaa 60,244,220\nMaaa 56,240,220\nMvaa 188,236,220\n"
"Mvaa 184,232,220\nD0 5,20,46,3072\nMvaa 124,228,220\nMvaa 120,224,220\ns_waitcnt vmcnt(17) lgkmcnt(0)\ns_barrier\nMvaa 64,160,128\nTa 192,12,16384\nv_lshrrev_b32 v48, 4, v0\n"
"Mvaa 68,164,128\nv_and_b32 v49, 7, v0\ns_add_u32 m0, s47, 49152\ns_add_u32 s44, s44, 0x40000\nMvaa 128,168,128\nTa 194,12,18432\nMvaa 132,172,128\nv_xor_b32 v49, v49, v48\n"
"D0 6,24,44,0\nMaaa 0,176,128\nTa 196,13,16384\nv_lshlrev_b32 v49, 4, v49\nMaaa 4,180,128\nv_lshrrev_b32 v48, 3, v0\nMaaa 64,184,128\nTa 198,13,18432\nMaaa 68,188,128\n"
"v_mul_u32_u24 v48, 896, v48\nMaaa 76,188,132\nTa 200,14,16384\nv_add_u32 v40, v48, v49\nD0 7,24,44,1024\nMaaa 72,184,132\nv_xor_b32 v49, 64, v40\nMaaa 12,180,132\nTa 202,14,18432\n"
"Maaa 8,176,132\nv_add_u32 v41, 6144, v49\nMvaa 140,172,132\nTa 204,15,16384\nv_add_u32 v42, 12288, v40\nMvaa 136,168,132\nv_add_u32 v43, 18432, v49\nD0 8,24,44,2048\n"
"Mvaa 76,164,132\nTa 206,15,18432\nMvaa 72,160,132\ns_mov_b32 s2, s13\nMvaa 80,160,136\nTa 208,16,16384\ns_lshl2_add_u32 s2, s2, s11\nMvaa 84,164,136\ns_mul_i32 s52, s2, 28672\n"
"Mvaa 144,168,136\nTa 210,16,18432\nD0 9,24,44,3072\nMvaa 148,172,136\ns_lshr_b32 s2, s13, 2\nMaaa 16,176,136\nTa 212,17,16384\ns_bfe_u32 s3, s13, 0x10001\nMaaa 20,180,136\n"
"s_mul_i32 s3, s3, 17\ns_add_u32 m0, s47, 65536\ns_add_u32 s46, s51, 0\nMaaa 80,184,136\nTa 214,17,18432\nMaaa 84,188,136\ns_add_u32 s2, s2, s3\nD0 6,32,46,0\nMaaa 92,188,140\n"
"Ta 216,18,16384\ns_lshl2_add_u32 s3, s14, 0\nMaaa 88,184,140\ns_lshl_b32 s3, s3, 2\nMaaa 28,180,140\nTa 218,18,18432\nMaaa 24,176,140\ns_lshr_b32 s3, 0xe420ca86, s3\n"
"Mvaa 156,172,140\nTa 220,19,16384\ns_and_b32 s3, s3, 15\nD0 7,32,46,1024\nMvaa 152,168,140\ns_add_u32 s2, s2, s3\ns_cmp_eq_u32 s2, 32\ns_cselect_b32 s4, 16, s2\nMvaa 92,164,140\n"
"Ta 222,19,18432\nMvaa 88,160,140\ns_mul_i32 s75, s4, 65536\nMvaa 96,160,144\nRa 224,11,0\ns_lshr_b32 s2, s13, 2\nMvaa 100,164,144\ns_bfe_u32 s3, s13, 0x10001\nD0 8,32,46,2048\n"
"Mvaa 160,168,144\nRa 228,11,2048\nMvaa 164,172,144\ns_mul_i32 s3, s3, 17\nMaaa 32,176,144\nRa 232,11,8192\ns_add_u32 s2, s2, s3\nMaaa 36,180,144\ns_lshl2_add_u32 s3, s14, 1\n"
"Maaa 96,184,144\nRa 236,11,10240\nD0 9,32,46,3072\nMaaa 100,188,144\ns_lshl_b32 s3, s3, 2\nMaaa 108,188,148\nRa 240,11,16384\ns_lshr_b32 s3, 0xe420ca86, s3\nMaaa 104,184,148\n"
"s_and_b32 s3, s3, 15\ns_add_u32 m0, s47, 81920\ns_add_u32 s46, s51, 262144\nMaaa 44,180,148\nRa 244,11,18432\nMaaa 40,176,148\ns_add_u32 s2, s2, s3\ns_cmp_eq_u32 s2, 32\n"
"s_cselect_b32 s4, 16, s2\nD0 6,32,46,0\nMvaa 172,172,148\nRa 248,11,24576\ns_mul_i32 s76, s4, 65536\nMvaa 168,168,148\ns_lshr_b32 s2, s13, 2\nMvaa 108,164,148\nRa 252,11,26624\n"
"Mvaa 104,160,148\ns_bfe_u32 s3, s13, 0x10001\nMvaa 112,160,152\ns_mul_i32 s3, s3, 17\ns_add_u32 s2, s2, s3\nD0 7,32,46,1024\nMvaa 116,164,152\ns_lshl2_add_u32 s3, s14, 2\n"
"Mvaa 176,168,152\ns_lshl_b32 s3, s3, 2\nMvaa 180,172,152\ns_lshr_b32 s3, 0xe420ca86, s3\nMaaa 48,176,152\ns_and_b32 s3, s3, 15\ns_add_u32 s2, s2, s3\nMaaa 52,180,152\n"
"s_cmp_eq_u32 s2, 32\ns_cselect_b32 s4, 16, s2\nD0 8,32,46,2048\nMaaa 112,184,152\ns_mul_i32 s77, s4, 65536\nMaaa 116,188,152\ns_lshr_b32 s2, s13, 2\nMaaa 124,188,156\n"
"s_bfe_u32 s3, s13, 0x10001\ns_mul_i32 s3, s3, 17\nMaaa 120,184,156\ns_add_u32 s2, s2, s3\nMaaa 60,180,156\ns_lshl2_add_u32 s3, s14, 3\nD0 9,32,46,3072\nMaaa 56,176,156\n"
"s_lshl_b32 s3, s3, 2\nMvaa 188,172,156\ns_lshr_b32 s3, 0xe420ca86, s3\ns_and_b32 s3, s3, 15\nMvaa 184,168,156\ns_add_u32 s2, s2, s3\ns_cmp_eq_u32 s2, 32\ns_cselect_b32 s4, 16, s2\n"
"Mvaa 124,164,156\ns_mul_i32 s78, s4, 65536\nMvaa 120,160,156\ns_waitcnt vmcnt(12) lgkmcnt(0)\ns_barrier\nMvaa 64,224,192\nTa 128,12,32768\ns_lshr_b32 s3, s11, 1\nMvaa 68,228,192\n"
"s_lshl_b32 s3, s3, 15\ns_add_u32 m0, s47, -65536\ns_add_u32 s46, s52, 0\nMvaa 128,232,192\nTa 130,12,34816\nMvaa 132,236,192\ns_add_u32 s2, s72, s3\nD0 40,28,46,0\nMaaa 0,240,192\n"
"Ta 132,13,32768\ns_and_b32 s3, s11, 1\nMaaa 4,244,192\ns_lshl_b32 s3, s3, 14\nMaaa 64,248,192\nTa 134,13,34816\nMaaa 68,252,192\ns_add_u32 s2, s2, s3\nMaaa 76,252,196\n"
"Ta 136,14,32768\ns_add_u32 s2, s2, 0x400000\nD0 41,28,46,1024\nMaaa 72,248,196\ns_mov_b32 s55, 4096\nMaaa 12,244,196\nTa 138,14,34816\nMaaa 8,240,196\ns_add_u32 s75, s75, s2\n"
"Mvaa 140,236,196\nTa 140,15,32768\ns_add_u32 s76, s76, s2\nMvaa 136,232,196\ns_add_u32 s77, s77, s2\nD0 42,28,46,2048\nMvaa 76,228,196\nTa 142,15,34816\nMvaa 72,224,196\n"
"s_add_u32 s78, s78, s2\nMvaa 80,224,200\nTa 144,16,32768\nv_add_u32 v32, 0x10000, v12\nMvaa 84,228,200\nv_add_u32 v33, 0x10000, v13\nMvaa 144,232,200\nTa 146,16,34816\n"
"D0 43,28,46,3072\nMvaa 148,236,200\nv_add_u32 v34, 0x10000, v14\nMaaa 16,240,200\nTa 148,17,32768\nv_add_u32 v35, 0x10000, v15\nMaaa 20,244,200\nv_add_u32 v36, 0x10000, v16\n"
"s_add_u32 m0, s47, 0\ns_add_u32 s46, s51, 524288\nMaaa 80,248,200\nTa 150,17,34816\nMaaa 84,252,200\nv_add_u32 v37, 0x10000, v17\nD0 6,32,46,0\nMaaa 92,252,204\nTa 152,18,32768\n"
"v_add_u32 v38, 0x10000, v18\nMaaa 88,248,204\nv_add_u32 v39, 0x10000, v19\nMaaa 28,244,204\nTa 154,18,34816\nMaaa 24,240,204\ns_add_u32 s2, s11, 1\nMvaa 156,236,204\n"
"Ta 156,19,32768\ns_lshr_b32 s2, s2, 1\nD0 7,32,46,1024\nMvaa 152,232,204\ns_lshl_b32 s2, s2, 12\nMvaa 92,228,204\nTa 158,19,34816\nMvaa 88,224,204\nv_add_u32 v44, s2, v10\n"
"Mvaa 96,224,208\nRa 160,10,32768\nv_add_u32 v45, s2, v11\nMvaa 100,228,208\nv_xor_b32 v46, 0x1000, v44\nD0 8,32,46,2048\nMvaa 160,232,208\nRa 164,10,34816\nMvaa 164,236,208\n"
"v_xor_b32 v47, 0x1000, v45\nMaaa 32,240,208\nRa 168,10,40960\ns_lshr_b32 s3, s11, 1\nMaaa 36,244,208\ns_lshl_b32 s3, s3, 13\nMaaa 96,248,208\nRa 172,10,43008\nD0 9,32,46,3072\n"
"Maaa 100,252,208\ns_lshl_b32 s2, s11, 12\nMaaa 108,252,212\nRa 176,10,49152\ns_add_u32 s2, s2, s3\nMaaa 104,248,212\ns_add_u32 s2, s2, s72\ns_add_u32 m0, s47, 16384\n"
"s_add_u32 s46, s51, 786432\nMaaa 44,244,212\nRa 180,10,51200\nMaaa 40,240,212\ns_lshl_b32 s3, s11, 12\nD0 6,32,46,0\nMvaa 172,236,212\nRa 184,10,57344\ns_add_u32 s89, s3, 0x10000\n"
"Mvaa 168,232,212\ns_mov_b32 s88, 0xffff8000\nMvaa 108,228,212\nRa 188,10,59392\nMvaa 104,224,212\ns_lshl_b32 s4, s13, 1\nMvaa 112,224,216\ns_sub_u32 s79, 15, s4\n"
"s_and_b32 s5, s12, 1\nD0 7,32,46,1024\nMvaa 116,228,216\ns_mul_i32 s5, s5, 229376\nMvaa 176,232,216\ns_add_u32 s5, s5, s2\nMvaa 180,236,216\ns_add_u32 s96, s5, 0x1310000\n"
"Maaa 48,240,216\ns_mov_b32 s63, 0x53800000\ns_mov_b32 s64, 0x2b800000\nMaaa 52,244,216\ns_mov_b32 s98, s4\nD0 8,32,46,2048\nMaaa 112,248,216\ns_lshl_b32 s5, s13, 5\n"
"Maaa 116,252,216\ns_add_u32 s5, s5, s11\nMaaa 124,252,220\ns_lshl_b32 s65, s5, 9\ns_add_u32 s65, s65, s72\nMaaa 120,248,220\ns_add_u32 s65, s65, 0x1580000\nMaaa 60,244,220\n"
"s_and_b32 s66, s72, 0x2000\nD0 9,32,46,3072\nMaaa 56,240,220\ns_add_u32 s66, s66, s56\nMvaa 188,236,220\ns_add_u32 s66, s66, 0x100000\ns_and_b32 s5, s12, 1\nMvaa 184,232,220\n"
"s_mul_i32 s58, s5, 0x202000\nMvaa 124,228,220\ns_xor_b32 s58, s58, s72\nMvaa 120,224,220\ns_waitcnt vmcnt(20) lgkmcnt(0)\ns_barrier\nMvaa 64,160,128\nTa 192,12,49152\n"
"s_add_u32 s58, s58, 0x15c0000\nMvaa 68,164,128\ns_lshl_b32 s5, s5, 8\ns_add_u32 m0, s47, -49152\ns_add_u32 s46, s52, 256\nMvaa 128,168,128\nTa 194,12,51200\nMvaa 132,172,128\n"
"s_add_u32 s61, s58, s5\nD0 40,28,46,0\nMaaa 0,176,128\nTa 196,13,49152\ns_lshl4_add_u32 s60, s13, s61\nMaaa 4,180,128\ns_add_u32 s54, s60, 128\nMaaa 64,184,128\nTa 198,13,51200\n"
"Maaa 68,188,128\ns_xor_b32 s60, s54, 0x100\ns_cmp_eq_u32 s11, 0\ns_cselect_b32 s54, s54, 0x40000000\nMaaa 76,188,132\nTa 200,14,49152\ns_getpc_b64 s[8:9]\n.PC328:\n"
"s_add_u32 s8, s8, .HD322-.PC328\ns_addc_u32 s9, s9, 0\nD0 41,28,46,1024\nMaaa 72,184,132\ns_mov_b32 s97, 0xff800000\nMaaa 12,180,132\nTa 202,14,51200\nMaaa 8,176,132\n"
"s_mov_b32 s90, 1\nMvaa 140,172,132\nTa 204,15,49152\ns_lshl_b32 s5, s11, 2\nMvaa 136,168,132\ns_lshr_b32 s92, 0xf112, s5\nD0 42,28,46,2048\nMvaa 76,164,132\nTa 206,15,51200\n"
"Mvaa 72,160,132\ns_and_b32 s92, s92, 15\nMvaa 80,160,136\nTa 208,16,49152\ns_lshr_b32 s93, s11, 1\nMvaa 84,164,136\ns_xor_b32 s93, s93, 1\nMvaa 144,168,136\nTa 210,16,51200\n"
"D0 43,28,46,3072\nMvaa 148,172,136\ns_mul_i32 s4, s79, 65536\nMaaa 16,176,136\nTa 212,17,49152\ns_sub_u32 s4, s2, s4\nMaaa 20,180,136\ns_add_u32 s94, s4, 0x20000\n"
"s_add_u32 m0, s47, 32768\ns_add_u32 s46, s51, 1048576\nMaaa 80,184,136\nTa 214,17,51200\nMaaa 84,188,136\ns_add_u32 s95, s4, 0x410000\nD0 6,32,46,0\nMaaa 92,188,140\n"
"Ta 216,18,49152\ns_add_u32 s86, s2, 0x130000\nMaaa 88,184,140\ns_add_u32 s87, s2, 0x520000\nMaaa 28,180,140\nTa 218,18,51200\nMaaa 24,176,140\ns_sub_u32 s99, s79, 1\n"
"s_cmp_eq_u32 s11, 3\ns_cselect_b32 s92, 100, s92\nMvaa 156,172,140\nTa 220,19,49152\ns_mov_b32 s36, s68\nD0 7,32,46,1024\nMvaa 152,168,140\ns_and_b32 s37, s69, 0xffff\n"
"Mvaa 92,164,140\nTa 222,19,51200\nMvaa 88,160,140\ns_mov_b32 s38, 0x1c0000\nMvaa 96,160,144\nRa 224,11,32768\ns_mov_b32 s39, 0x20000\nMvaa 100,164,144\ns_and_b32 s2, s74, 15\n"
"D0 8,32,46,2048\nMvaa 160,168,144\nRa 228,11,34816\nMvaa 164,172,144\ns_mul_i32 s3, s73, 14\nMaaa 32,176,144\nRa 232,11,40960\ns_add_u32 s2, s2, s3\nMaaa 36,180,144\n"
"s_lshl2_add_u32 s2, s2, s11\nMaaa 96,184,144\nRa 236,11,43008\nD0 9,32,46,3072\nMaaa 100,188,144\ns_mul_i32 s3, s2, 4682\nMaaa 108,188,148\nRa 240,11,49152\ns_lshr_b32 s3, s3, 16\n"
"Maaa 104,184,148\ns_mul_i32 s22, s3, 14\nMaaa 44,180,148\nRa 244,11,51200\nMaaa 40,176,148\ns_sub_u32 s22, s2, s22\nMvaa 172,172,148\nRa 248,11,57344\ns_lshl_b32 s21, s3, 5\n"
"Mvaa 168,168,148\ns_bfe_u32 s41, s3, 0x30002\ns_cmp_gt_u32 s22, 6\ns_cselect_b32 s2, 1187840, 0\ns_cselect_b32 s1, 16, 0\nMvaa 108,164,148\nRa 252,11,59392\nMvaa 104,160,148\n"
"s_add_u32 s41, s41, s1\nMvaa 112,160,152\ns_lshl4_add_u32 s41, s41, 0x15c0280\ns_and_b32 s1, s3, 28\nMvaa 116,164,152\ns_add_u32 s3, s3, s1\nMvaa 176,168,152\n"
"s_lshl_b32 s3, s3, 11\nMvaa 180,172,152\ns_add_u32 s2, s2, s3\nMaaa 48,176,152\ns_lshl_b32 s3, s22, 17\ns_add_u32 s2, s2, s3\nMaaa 52,180,152\ns_add_u32 s20, s2, 0x1100000\n"
"Maaa 112,184,152\ns_lshl_b32 s2, s22, 7\nMaaa 116,188,152\ns_add_u32 s40, s2, 0xc04b800\nMaaa 124,188,156\ns_mul_i32 s2, s21, 896\ns_lshl_b32 s3, s22, 6\nMaaa 120,184,156\n"
"s_add_u32 s22, s2, s3\nMaaa 60,180,156\ns_add_u32 s23, s22, 14336\nMaaa 56,176,156\nMvaa 188,172,156\nMvaa 184,168,156\nMvaa 124,164,156\nMvaa 120,160,156\n"
"s_waitcnt vmcnt(16) lgkmcnt(0)\ns_barrier\nMvaa 64,224,192\ns_add_u32 m0, s47, -16384\ns_add_u32 s46, s52, 128\nMvaa 68,228,192\nMvaa 72,224,196\nD0 40,28,46,0\nMvaa 76,228,196\n"
"Mvaa 80,224,200\nMvaa 84,228,200\nMvaa 88,224,204\nD0 41,28,46,1024\nCV 192,64,65\nMvaa 92,228,204\nCV 193,66,67\nCV 194,68,69\nMvaa 96,224,208\nCV 195,70,71\nCV 196,72,73\n"
"Mvaa 100,228,208\nCV 197,74,75\nMvaa 104,224,212\nD0 42,28,46,2048\nCV 198,76,77\nMvaa 108,228,212\nCV 199,78,79\nCV 200,80,81\nMvaa 112,224,216\nCV 201,82,83\nCV 202,84,85\n"
"Mvaa 116,228,216\nCV 203,86,87\nMvaa 120,224,220\nD0 43,28,46,3072\nCV 204,88,89\nMvaa 124,228,220\nCV 205,90,91\nCV 206,92,93\nMvaa 128,232,192\ns_add_u32 m0, s47, 49152\n"
"s_add_u32 s46, s51, 1310720\nMvaa 132,236,192\nCV 207,94,95\nMvaa 136,232,196\nD0 6,32,46,0\nCV 208,96,97\nMvaa 140,236,196\nCV 209,98,99\nCV 210,100,101\nMvaa 144,232,200\n"
"CV 211,102,103\nCV 212,104,105\nMvaa 148,236,200\nCV 213,106,107\nMvaa 152,232,204\nD0 7,32,46,1024\nCV 214,108,109\nMvaa 156,236,204\nCV 215,110,111\nCV 216,112,113\n"
"Mvaa 160,232,208\nCV 217,114,115\nCV 218,116,117\nMvaa 164,236,208\nCV 219,118,119\nMvaa 168,232,212\nD0 8,32,46,2048\nCV 220,120,121\nMvaa 172,236,212\nCV 221,122,123\n"
"CV 222,124,125\nMvaa 176,232,216\nCV 223,126,127\nCV 224,128,129\nMvaa 180,236,216\nCV 225,130,131\nMvaa 184,232,220\nD0 9,32,46,3072\nCV 226,132,133\nMvaa 188,236,220\n"
"CV 227,134,135\nCV 228,136,137\nMaaa 0,240,192\ns_add_u32 m0, s47, -32768\ns_add_u32 s46, s52, 384\nMaaa 4,244,192\nCV 229,138,139\nMaaa 8,240,196\nD0 40,28,46,0\nCV 230,140,141\n"
"Maaa 12,244,196\nCV 231,142,143\nCV 232,144,145\nMaaa 16,240,200\nRa 160,44,0\nCV 233,146,147\nMaaa 20,244,200\nRa 164,44,2048\nMaaa 24,240,204\nD0 41,28,46,1024\nRa 168,46,0\n"
"Maaa 28,244,204\nRa 172,46,2048\nCV 234,148,149\nMaaa 32,240,208\nTa 128,32,0\nCV 235,150,151\nMaaa 36,244,208\nTa 130,32,2048\nMaaa 40,240,212\nD0 42,28,46,2048\nTa 132,33,0\n"
"Maaa 44,244,212\nTa 134,33,2048\nCV 236,152,153\nMaaa 48,240,216\nTa 136,34,0\nCV 237,154,155\nMaaa 52,244,216\nTa 138,34,2048\nMaaa 56,240,220\nD0 43,28,46,3072\nTa 140,35,0\n"
"Maaa 60,244,220\nTa 142,35,2048\nCV 238,156,157\nMaaa 64,248,192\nTa 144,36,0\nCV 239,158,159\nMaaa 68,252,192\nTa 146,36,2048\nMaaa 72,248,196\nTa 148,37,0\nCV 240,160,161\n"
"Sv 192,1,16,75,0\nMaaa 76,252,196\nTa 150,37,2048\nCV 241,162,163\nMaaa 80,248,200\nTa 152,38,0\nCV 242,164,165\nMaaa 84,252,200\nTa 154,38,2048\nMaaa 88,248,204\nTa 156,39,0\n"
"CV 243,166,167\nMaaa 92,252,204\nTa 158,39,2048\nCV 244,168,169\nMaaa 96,248,208\nCV 245,170,171\nCV 246,172,173\nSv 196,1,16,75,1024\nMaaa 100,252,208\nCV 247,174,175\n"
"Maaa 104,248,212\nCV 248,176,177\nCV 249,178,179\nMaaa 108,252,212\nCV 250,180,181\nCV 251,182,183\nMaaa 112,248,216\nCV 252,184,185\nCV 253,186,187\nMaaa 116,252,216\n"
"CV 254,188,189\nMaaa 120,248,220\nCV 255,190,191\nSv 200,1,16,75,2048\nMaaa 124,252,220\nWL 0\nZvaa 64,128,160\nRa 224,45,0\nZvaa 96,128,164\nRa 228,45,2048\nZvaa 128,128,168\n"
"Ra 232,47,0\nZvaa 160,128,172\nRa 236,47,2048\nSv 204,1,16,75,3072\ns_lshl3_add_u32 s5, s12, s13\ns_lshl2_add_u32 s5, s5, s11\ns_lshl2_add_u32 s5, s5, 0xc04a400\n"
"buffer_load_dword v27, off, s[16:19], s5 sc0 sc1\nZvaa 164,132,172\nTa 192,32,16384\nZvaa 132,132,168\nZvaa 100,132,164\nTa 194,32,18432\nZvaa 68,132,160\nTa 196,33,16384\n"
"Zvaa 72,136,160\nTa 198,33,18432\nZvaa 104,136,164\nTa 200,34,16384\ns_add_u32 s75, s75, s55\nSv 208,1,16,75,0\nZvaa 136,136,168\nTa 202,34,18432\nZvaa 168,136,172\n"
"Zvaa 172,140,172\nTa 204,35,16384\nZvaa 140,140,168\nTa 206,35,18432\nZvaa 108,140,164\nTa 208,36,16384\nZvaa 76,140,160\nTa 210,36,18432\nSv 212,1,16,75,1024\nZvaa 80,144,160\n"
"Ta 212,37,16384\nZvaa 112,144,164\nZvaa 144,144,168\nTa 214,37,18432\nZvaa 176,144,172\nTa 216,38,16384\nZvaa 180,148,172\nTa 218,38,18432\nZvaa 148,148,168\nTa 220,39,16384\n"
"Sv 216,1,16,75,2048\nZvaa 116,148,164\nTa 222,39,18432\nZvaa 84,148,160\nZvaa 88,152,160\nZvaa 120,152,164\nZvaa 152,152,168\nZvaa 184,152,172\nSv 220,1,16,75,3072\n"
"Zvaa 188,156,172\nZvaa 156,156,168\nZvaa 124,156,164\nZvaa 92,156,160\ns_waitcnt vmcnt(17) lgkmcnt(0)\ns_barrier\nMvaa 64,192,224\nRa 160,44,49152\nMvaa 96,192,228\n"
"Ra 164,44,51200\ns_add_u32 m0, s47, 65536\ns_add_u32 s46, s51, 1572864\nD0 6,32,46,0\nMvaa 128,192,232\nRa 168,46,49152\nMvaa 160,192,236\nRa 172,46,51200\nMvaa 164,196,236\n"
"Ta 128,12,0\nMvaa 132,196,232\nD0 7,32,46,1024\nMvaa 100,196,228\nTa 130,12,2048\nMvaa 68,196,224\nTa 132,13,0\nMvaa 72,200,224\nTa 134,13,2048\nMvaa 104,200,228\nTa 136,14,0\n"
"D0 8,32,46,2048\nMvaa 136,200,232\nTa 138,14,2048\nMvaa 168,200,236\nMvaa 172,204,236\nTa 140,15,0\nMvaa 140,204,232\nTa 142,15,2048\nD0 9,32,46,3072\nMvaa 108,204,228\n"
"Ta 144,16,0\nMvaa 76,204,224\nTa 146,16,2048\ns_add_u32 m0, s47, 81920\ns_add_u32 s46, s51, 1835008\nMvaa 80,208,224\nTa 148,17,0\nMvaa 112,208,228\nD0 6,32,46,0\n"
"Mvaa 144,208,232\nTa 150,17,2048\nMvaa 176,208,236\nTa 152,18,0\nMvaa 180,212,236\nTa 154,18,2048\nMvaa 148,212,232\nTa 156,19,0\nD0 7,32,46,1024\nMvaa 116,212,228\n"
"Ta 158,19,2048\nMvaa 84,212,224\nMvaa 88,216,224\nMvaa 120,216,228\nD0 8,32,46,2048\nMvaa 152,216,232\nMvaa 184,216,236\nMvaa 188,220,236\nMvaa 156,220,232\nD0 9,32,46,3072\n"
"Mvaa 124,220,228\nMvaa 92,220,224\nWL 0\nMvaa 64,128,160\nRa 224,45,49152\nMvaa 96,128,164\nRa 228,45,51200\ns_add_u32 m0, s47, -65536\ns_add_u32 s46, s52, 512\nD0 40,28,46,0\n"
"Mvaa 128,128,168\nRa 232,47,49152\nMvaa 160,128,172\nRa 236,47,51200\nMvaa 164,132,172\nTa 192,12,16384\nMvaa 132,132,168\nD0 41,28,46,1024\nMvaa 100,132,164\nTa 194,12,18432\n"
"Mvaa 68,132,160\nTa 196,13,16384\nMvaa 72,136,160\nTa 198,13,18432\nMvaa 104,136,164\nTa 200,14,16384\nD0 42,28,46,2048\nMvaa 136,136,168\nTa 202,14,18432\nMvaa 168,136,172\n"
"Mvaa 172,140,172\nTa 204,15,16384\nMvaa 140,140,168\nTa 206,15,18432\nD0 43,28,46,3072\nMvaa 108,140,164\nTa 208,16,16384\nSv 224,1,16,76,0\nMvaa 76,140,160\nTa 210,16,18432\n"
"Mvaa 80,144,160\nTa 212,17,16384\nMvaa 112,144,164\nMvaa 144,144,168\nTa 214,17,18432\nMvaa 176,144,172\nTa 216,18,16384\nSv 228,1,16,76,1024\nMvaa 180,148,172\nTa 218,18,18432\n"
"Mvaa 148,148,168\nTa 220,19,16384\nMvaa 116,148,164\nTa 222,19,18432\nMvaa 84,148,160\nMvaa 88,152,160\nSv 232,1,16,76,2048\nMvaa 120,152,164\nMvaa 152,152,168\nMvaa 184,152,172\n"
"Mvaa 188,156,172\nMvaa 156,156,168\nSv 236,1,16,76,3072\nMvaa 124,156,164\nMvaa 92,156,160\ns_waitcnt vmcnt(16) lgkmcnt(0)\ns_barrier\nMvaa 64,192,224\nRa 160,44,16384\n"
"v_cmp_ne_u32 vcc, s15, v27\ns_nop 0\ns_cbranch_vccnz .S339\n.S340:\nMvaa 96,192,228\nRa 164,44,18432\nbuffer_store_dword v25, off, s[16:19], s54 offset:0 sc0 sc1\n"
"s_add_u32 m0, s47, 0\ns_add_u32 s46, s51, 2097152\nD0 6,32,46,0\nMvaa 128,192,232\nRa 168,46,16384\nMvaa 160,192,236\nRa 172,46,18432\nMvaa 164,196,236\nTa 128,12,32768\n"
"Mvaa 132,196,232\nD0 7,32,46,1024\nMvaa 100,196,228\nTa 130,12,34816\nMvaa 68,196,224\nTa 132,13,32768\nMvaa 72,200,224\nTa 134,13,34816\nMvaa 104,200,228\nTa 136,14,32768\n"
"D0 8,32,46,2048\nMvaa 136,200,232\nTa 138,14,34816\nMvaa 168,200,236\nMvaa 172,204,236\nTa 140,15,32768\nMvaa 140,204,232\nTa 142,15,34816\nD0 9,32,46,3072\nMvaa 108,204,228\n"
"Ta 144,16,32768\nMvaa 76,204,224\nTa 146,16,34816\ns_add_u32 m0, s47, 16384\ns_add_u32 s46, s51, 2359296\nMvaa 80,208,224\nTa 148,17,32768\nMvaa 112,208,228\nD0 6,32,46,0\n"
"Mvaa 144,208,232\nTa 150,17,34816\nMvaa 176,208,236\nTa 152,18,32768\nMvaa 180,212,236\nTa 154,18,34816\nMvaa 148,212,232\nTa 156,19,32768\nD0 7,32,46,1024\nMvaa 116,212,228\n"
"Ta 158,19,34816\nMvaa 84,212,224\nMvaa 88,216,224\nMvaa 120,216,228\nD0 8,32,46,2048\nMvaa 152,216,232\nMvaa 184,216,236\nMvaa 188,220,236\nMvaa 156,220,232\nD0 9,32,46,3072\n"
"Mvaa 124,220,228\nMvaa 92,220,224\nWL 0\nMvaa 64,128,160\nRa 224,45,16384\nMvaa 96,128,164\nRa 228,45,18432\ns_add_u32 m0, s47, -16384\ns_add_u32 s46, s52, 640\nD0 40,28,46,0\n"
"Mvaa 128,128,168\nRa 232,47,16384\nMvaa 160,128,172\nRa 236,47,18432\nMvaa 164,132,172\nTa 192,12,49152\nMvaa 132,132,168\nD0 41,28,46,1024\nMvaa 100,132,164\nTa 194,12,51200\n"
"Mvaa 68,132,160\nTa 196,13,49152\nMvaa 72,136,160\nTa 198,13,51200\nMvaa 104,136,164\nTa 200,14,49152\nD0 42,28,46,2048\nMvaa 136,136,168\nTa 202,14,51200\nMvaa 168,136,172\n"
"Mvaa 172,140,172\nTa 204,15,49152\nMvaa 140,140,168\nTa 206,15,51200\nD0 43,28,46,3072\nMvaa 108,140,164\nTa 208,16,49152\ns_add_u32 s76, s76, s55\nSv 240,1,16,76,0\n"
"Mvaa 76,140,160\nTa 210,16,51200\nMvaa 80,144,160\nTa 212,17,49152\nMvaa 112,144,164\nMvaa 144,144,168\nTa 214,17,51200\nMvaa 176,144,172\nTa 216,18,49152\nSv 244,1,16,76,1024\n"
"Mvaa 180,148,172\nTa 218,18,51200\nMvaa 148,148,168\nTa 220,19,49152\nMvaa 116,148,164\nTa 222,19,51200\nMvaa 84,148,160\nMvaa 88,152,160\nSv 248,1,16,76,2048\nMvaa 120,152,164\n"
"Mvaa 152,152,168\nMvaa 184,152,172\nMvaa 188,156,172\nMvaa 156,156,168\nSv 252,1,16,76,3072\nMvaa 124,156,164\nMvaa 92,156,160\ns_waitcnt vmcnt(25) lgkmcnt(0)\ns_barrier\n"
"Mvaa 64,192,224\nRa 160,44,32768\nAR 60,0\nAR 61,1\nMvaa 96,192,228\nRa 164,44,34816\ns_add_u32 m0, s47, 32768\ns_add_u32 s46, s51, 2621440\nD0 6,32,46,0\nMvaa 128,192,232\n"
"Ra 168,46,32768\nCV 52,60,61\nMvaa 160,192,236\nRa 172,46,34816\nAR 60,2\nMvaa 164,196,236\nTa 128,32,0\nAR 61,3\nMvaa 132,196,232\nD0 7,32,46,1024\nCV 53,60,61\nMvaa 100,196,228\n"
"Ta 130,32,2048\nAR 60,4\nMvaa 68,196,224\nTa 132,33,0\nAR 61,5\nMvaa 72,200,224\nTa 134,33,2048\nCV 54,60,61\nMvaa 104,200,228\nTa 136,34,0\nD0 8,32,46,2048\nMvaa 136,200,232\n"
"Ta 138,34,2048\nAR 60,6\nMvaa 168,200,236\nAR 61,7\nCV 55,60,61\nMvaa 172,204,236\nTa 140,35,0\nAR 60,8\nMvaa 140,204,232\nTa 142,35,2048\nD0 9,32,46,3072\nMvaa 108,204,228\n"
"Ta 144,36,0\nAR 61,9\nMvaa 76,204,224\nTa 146,36,2048\ns_add_u32 m0, s47, 49152\ns_add_u32 s46, s51, 2883584\nMvaa 80,208,224\nTa 148,37,0\nCV 56,60,61\nAR 60,10\n"
"Mvaa 112,208,228\nD0 6,32,46,0\nAR 61,11\nMvaa 144,208,232\nTa 150,37,2048\nCV 57,60,61\nMvaa 176,208,236\nTa 152,38,0\nAR 60,12\nMvaa 180,212,236\nTa 154,38,2048\nAR 61,13\n"
"Mvaa 148,212,232\nTa 156,39,0\nD0 7,32,46,1024\nMvaa 116,212,228\nTa 158,39,2048\nCV 58,60,61\nMvaa 84,212,224\nAR 60,14\nAR 61,15\nMvaa 88,216,224\nCV 59,60,61\nAR 60,16\n"
"Mvaa 120,216,228\nD0 8,32,46,2048\nAR 61,17\nMvaa 152,216,232\nCV 20,60,61\nAR 60,18\nMvaa 184,216,236\nAR 61,19\nCV 21,60,61\nMvaa 188,220,236\nAR 60,20\nAR 61,21\n"
"Mvaa 156,220,232\nD0 9,32,46,3072\nCV 22,60,61\nMvaa 124,220,228\nAR 60,22\nAR 61,23\nMvaa 92,220,224\nWL 0\nMvaa 64,128,160\nRa 224,45,32768\nCV 23,60,61\nAR 60,24\n"
"Mvaa 96,128,164\nRa 228,45,34816\ns_add_u32 m0, s47, -49152\ns_add_u32 s46, s52, 768\nD0 40,28,46,0\nMvaa 128,128,168\nRa 232,47,32768\nAR 61,25\nMvaa 160,128,172\n"
"Ra 236,47,34816\nCV 2,60,61\nMvaa 164,132,172\nTa 192,32,16384\nAR 60,26\nMvaa 132,132,168\nD0 41,28,46,1024\nAR 61,27\nMvaa 100,132,164\nTa 194,32,18432\nCV 3,60,61\n"
"Mvaa 68,132,160\nTa 196,33,16384\nAR 60,28\nMvaa 72,136,160\nTa 198,33,18432\nAR 61,29\nMvaa 104,136,164\nTa 200,34,16384\nD0 42,28,46,2048\nMvaa 136,136,168\nTa 202,34,18432\n"
"CV 4,60,61\nMvaa 168,136,172\nAR 60,30\nAR 61,31\nMvaa 172,140,172\nTa 204,35,16384\nCV 5,60,61\nMvaa 140,140,168\nTa 206,35,18432\nD0 43,28,46,3072\nMvaa 108,140,164\n"
"Ta 208,36,16384\nSv 52,1,16,77,0\nMvaa 76,140,160\nTa 210,36,18432\nMvaa 80,144,160\nTa 212,37,16384\nMvaa 112,144,164\nMvaa 144,144,168\nTa 214,37,18432\nMvaa 176,144,172\n"
"Ta 216,38,16384\nSv 56,1,16,77,1024\nMvaa 180,148,172\nTa 218,38,18432\nMvaa 148,148,168\nTa 220,39,16384\nMvaa 116,148,164\nTa 222,39,18432\nMvaa 84,148,160\nMvaa 88,152,160\n"
"Sv 20,1,16,77,2048\nMvaa 120,152,164\nMvaa 152,152,168\nMvaa 184,152,172\nMvaa 188,156,172\nMvaa 156,156,168\nSv 2,1,16,77,3072\nMvaa 124,156,164\nMvaa 92,156,160\n"
"s_waitcnt vmcnt(24) lgkmcnt(0)\ns_barrier\nMvaa 64,192,224\nRa 160,44,0\nv_and_b32 v27, 0x70, v1\nbuffer_load_dword v27, v27, s[16:19], s61 offen offset:128 sc0 sc1\n"
"Mvaa 96,192,228\nRa 164,44,2048\ns_add_u32 m0, s47, 65536\ns_add_u32 s46, s51, 3145728\nD0 6,32,46,0\nMvaa 128,192,232\nRa 168,46,0\nAR 60,32\nMvaa 160,192,236\nRa 172,46,2048\n"
"AR 61,33\nMvaa 164,196,236\nTa 128,12,0\nCV 52,60,61\nMvaa 132,196,232\nD0 7,32,46,1024\nAR 60,34\nMvaa 100,196,228\nTa 130,12,2048\nAR 61,35\nMvaa 68,196,224\nTa 132,13,0\n"
"CV 53,60,61\nMvaa 72,200,224\nTa 134,13,2048\nAR 60,36\nAR 61,37\nMvaa 104,200,228\nTa 136,14,0\nD0 8,32,46,2048\nMvaa 136,200,232\nTa 138,14,2048\nCV 54,60,61\nMvaa 168,200,236\n"
"AR 60,38\nAR 61,39\nMvaa 172,204,236\nTa 140,15,0\nCV 55,60,61\nMvaa 140,204,232\nTa 142,15,2048\nD0 9,32,46,3072\nMvaa 108,204,228\nTa 144,16,0\nAR 60,40\nMvaa 76,204,224\n"
"Ta 146,16,2048\ns_add_u32 m0, s47, 81920\ns_add_u32 s46, s51, 3407872\nMvaa 80,208,224\nTa 148,17,0\nAR 61,41\nCV 56,60,61\nMvaa 112,208,228\nD0 6,32,46,0\nAR 60,42\n"
"Mvaa 144,208,232\nTa 150,17,2048\nAR 61,43\nMvaa 176,208,236\nTa 152,18,0\nCV 57,60,61\nMvaa 180,212,236\nTa 154,18,2048\nAR 60,44\nMvaa 148,212,232\nTa 156,19,0\nD0 7,32,46,1024\n"
"Mvaa 116,212,228\nTa 158,19,2048\nAR 61,45\nMvaa 84,212,224\nCV 58,60,61\nAR 60,46\nMvaa 88,216,224\nAR 61,47\nCV 59,60,61\nAR 60,48\nMvaa 120,216,228\nD0 8,32,46,2048\nAR 61,49\n"
"Mvaa 152,216,232\nCV 20,60,61\nAR 60,50\nMvaa 184,216,236\nAR 61,51\nCV 21,60,61\nMvaa 188,220,236\nAR 60,52\nAR 61,53\nMvaa 156,220,232\nD0 9,32,46,3072\nCV 22,60,61\n"
"Mvaa 124,220,228\nAR 60,54\nAR 61,55\nMvaa 92,220,224\nWL 0\nMvaa 64,128,160\nRa 224,45,0\nCV 23,60,61\nAR 60,56\nMvaa 96,128,164\nRa 228,45,2048\ns_add_u32 s6, s56, 0\n"
"buffer_load_dwordx4 v[192:195], v1, s[16:19], s6 offen offset:0 sc1\nMvaa 128,128,168\nRa 232,47,0\nAR 61,57\nMvaa 160,128,172\nRa 236,47,2048\nCV 2,60,61\nMvaa 164,132,172\n"
"Ta 192,12,16384\nAR 60,58\nMvaa 132,132,168\nbuffer_load_dwordx4 v[196:199], v1, s[16:19], s6 offen offset:1024 sc1\nAR 61,59\nMvaa 100,132,164\nTa 194,12,18432\nCV 3,60,61\n"
"Mvaa 68,132,160\nTa 196,13,16384\nAR 60,60\nMvaa 72,136,160\nTa 198,13,18432\nAR 61,61\nCV 4,60,61\nMvaa 104,136,164\nTa 200,14,16384\n"
"buffer_load_dwordx4 v[200:203], v1, s[16:19], s6 offen offset:2048 sc1\nMvaa 136,136,168\nTa 202,14,18432\nAR 60,62\nMvaa 168,136,172\nAR 61,63\nCV 5,60,61\nMvaa 172,140,172\n"
"Ta 204,15,16384\nMvaa 140,140,168\nTa 206,15,18432\nbuffer_load_dwordx4 v[204:207], v1, s[16:19], s6 offen offset:3072 sc1\nMvaa 108,140,164\nTa 208,16,16384\n"
"s_add_u32 s77, s77, s55\nSv 52,1,16,77,0\nMvaa 76,140,160\nTa 210,16,18432\nMvaa 80,144,160\nTa 212,17,16384\nMvaa 112,144,164\nMvaa 144,144,168\nTa 214,17,18432\n"
"Mvaa 176,144,172\nTa 216,18,16384\nSv 56,1,16,77,1024\nMvaa 180,148,172\nTa 218,18,18432\nMvaa 148,148,168\nTa 220,19,16384\nMvaa 116,148,164\nTa 222,19,18432\nMvaa 84,148,160\n"
"Mvaa 88,152,160\nSv 20,1,16,77,2048\nMvaa 120,152,164\nMvaa 152,152,168\nMvaa 184,152,172\nMvaa 188,156,172\nMvaa 156,156,168\nSv 2,1,16,77,3072\nMvaa 124,156,164\n"
"Mvaa 92,156,160\ns_waitcnt vmcnt(16) lgkmcnt(0)\ns_barrier\nMvaa 64,192,224\nRa 160,44,49152\nv_cmp_ne_u32 vcc, s15, v27\ns_nop 0\ns_cbranch_vccnz .S342\n.S343:\n"
"s_sub_u32 s45, s86, 131072\ns_cmp_eq_u32 s79, 1\ns_cselect_b32 s86, s94, s86\ns_add_u32 m0, s47, -65536\ns_add_u32 s46, s45, 0x0\nMvaa 96,192,228\nRa 164,44,51200\n"
"buffer_store_dword v25, off, s[16:19], s54 offset:8 sc0 sc1\nMvaa 128,192,232\nRa 168,46,49152\nD1 1,16,46,0\nMvaa 160,192,236\nRa 172,46,51200\nAR 60,126\nMvaa 164,196,236\n"
"Ta 128,12,32768\nAR 61,127\nMvaa 132,196,232\nCV 60,60,61\nAW 127,60\nMvaa 100,196,228\nTa 130,12,34816\nD1 1,16,46,1024\nMvaa 68,196,224\nTa 132,13,32768\nAR 60,124\n"
"Mvaa 72,200,224\nTa 134,13,34816\nAR 61,125\nMvaa 104,200,228\nTa 136,14,32768\nMvaa 136,200,232\nTa 138,14,34816\nD1 1,16,46,2048\nMvaa 168,200,236\nCV 60,60,61\nAW 126,60\n"
"Mvaa 172,204,236\nTa 140,15,32768\nAR 60,122\nMvaa 140,204,232\nTa 142,15,34816\nAR 61,123\nMvaa 108,204,228\nTa 144,16,32768\nD1 1,16,46,3072\nMvaa 76,204,224\nTa 146,16,34816\n"
"CV 60,60,61\nMvaa 80,208,224\nTa 148,17,32768\ns_add_u32 s6, s56, 4096\nMvaa 112,208,228\nAW 125,60\nMvaa 144,208,232\nTa 150,17,34816\n"
"buffer_load_dwordx4 v[208:211], v1, s[16:19], s6 offen offset:0 sc1\nMvaa 176,208,236\nTa 152,18,32768\nAR 60,120\nMvaa 180,212,236\nTa 154,18,34816\nAR 61,121\nMvaa 148,212,232\n"
"Ta 156,19,32768\nCV 60,60,61\nMvaa 116,212,228\nTa 158,19,34816\nbuffer_load_dwordx4 v[212:215], v1, s[16:19], s6 offen offset:1024 sc1\nMvaa 84,212,224\nAW 124,60\nAR 60,118\n"
"Mvaa 88,216,224\nAR 61,119\nCV 60,60,61\nMvaa 120,216,228\nAW 123,60\nMvaa 152,216,232\nbuffer_load_dwordx4 v[216:219], v1, s[16:19], s6 offen offset:2048 sc1\nAR 60,116\n"
"Mvaa 184,216,236\nAR 61,117\nCV 60,60,61\nMvaa 188,220,236\nAW 122,60\nAR 60,114\nMvaa 156,220,232\nAR 61,115\nCV 60,60,61\nMvaa 124,220,228\n"
"buffer_load_dwordx4 v[220:223], v1, s[16:19], s6 offen offset:3072 sc1\nAW 121,60\nMvaa 92,220,224\nWL 0\nMvaa 64,128,160\nRa 224,45,49152\ns_add_u32 s6, s56, 16384\n"
"Mvaa 96,128,164\nRa 228,45,51200\nMvaa 128,128,168\nRa 232,47,49152\nbuffer_load_dwordx4 v[224:227], v1, s[16:19], s6 offen offset:0 sc1\nMvaa 160,128,172\nRa 236,47,51200\n"
"AR 60,112\nMvaa 164,132,172\nTa 192,12,49152\nAR 61,113\nMvaa 132,132,168\nCV 60,60,61\nAW 120,60\nMvaa 100,132,164\nTa 194,12,51200\n"
"buffer_load_dwordx4 v[228:231], v1, s[16:19], s6 offen offset:1024 sc1\nMvaa 68,132,160\nTa 196,13,49152\nAR 60,110\nMvaa 72,136,160\nTa 198,13,51200\nAR 61,111\nMvaa 104,136,164\n"
"Ta 200,14,49152\nMvaa 136,136,168\nTa 202,14,51200\nbuffer_load_dwordx4 v[232:235], v1, s[16:19], s6 offen offset:2048 sc1\nMvaa 168,136,172\nCV 60,60,61\nAW 119,60\n"
"Mvaa 172,140,172\nTa 204,15,49152\nAR 60,108\nMvaa 140,140,168\nTa 206,15,51200\nAR 61,109\nMvaa 108,140,164\nTa 208,16,49152\n"
"buffer_load_dwordx4 v[236:239], v1, s[16:19], s6 offen offset:3072 sc1\nMvaa 76,140,160\nTa 210,16,51200\nCV 60,60,61\nMvaa 80,144,160\nTa 212,17,49152\nAW 118,60\n"
"Mvaa 112,144,164\nAR 60,106\nMvaa 144,144,168\nTa 214,17,51200\nAR 61,107\nMvaa 176,144,172\nTa 216,18,49152\nCV 60,60,61\nMvaa 180,148,172\nTa 218,18,51200\nAW 117,60\n"
"Mvaa 148,148,168\nTa 220,19,49152\nAR 60,104\nMvaa 116,148,164\nTa 222,19,51200\nAR 61,105\nMvaa 84,148,160\nCV 60,60,61\nAW 116,60\nMvaa 88,152,160\nAR 60,102\nAR 61,103\n"
"Mvaa 120,152,164\nCV 60,60,61\nMvaa 152,152,168\nAW 115,60\nAR 60,100\nMvaa 184,152,172\nAR 61,101\nCV 60,60,61\nMvaa 188,156,172\nAW 114,60\nAR 60,98\nMvaa 156,156,168\nAR 61,99\n"
"CV 60,60,61\nMvaa 124,156,164\nAW 113,60\nAR 60,96\nMvaa 92,156,160\ns_waitcnt vmcnt(21) lgkmcnt(0)\ns_barrier\nMvaa 64,192,224\nRa 160,44,16384\nAR 61,97\nMvaa 96,192,228\n"
"Ra 164,44,18432\ns_add_u32 s6, s56, 20480\nbuffer_load_dwordx4 v[240:243], v1, s[16:19], s6 offen offset:0 sc1\nMvaa 128,192,232\nRa 168,46,16384\nCV 60,60,61\nMvaa 160,192,236\n"
"Ra 172,46,18432\nAW 112,60\nMvaa 164,196,236\nTa 128,32,0\nAR 60,94\nMvaa 132,196,232\nbuffer_load_dwordx4 v[244:247], v1, s[16:19], s6 offen offset:1024 sc1\nAR 61,95\n"
"Mvaa 100,196,228\nTa 130,32,2048\nCV 60,60,61\nMvaa 68,196,224\nTa 132,33,0\nAW 111,60\nMvaa 72,200,224\nTa 134,33,2048\nAR 60,92\nMvaa 104,200,228\nTa 136,34,0\n"
"buffer_load_dwordx4 v[248:251], v1, s[16:19], s6 offen offset:2048 sc1\nMvaa 136,200,232\nTa 138,34,2048\nAR 61,93\nMvaa 168,200,236\nCV 60,60,61\nAW 110,60\nMvaa 172,204,236\n"
"Ta 140,35,0\nAR 60,90\nMvaa 140,204,232\nTa 142,35,2048\nbuffer_load_dwordx4 v[252:255], v1, s[16:19], s6 offen offset:3072 sc1\nMvaa 108,204,228\nTa 144,36,0\nAR 61,91\n"
"Mvaa 76,204,224\nTa 146,36,2048\ns_add_u32 m0, s47, -32768\ns_add_u32 s46, s45, 0x8000\nMvaa 80,208,224\nTa 148,37,0\nCV 60,60,61\nMvaa 112,208,228\nD1 1,16,46,0\n"
"Mvaa 144,208,232\nTa 150,37,2048\nAW 109,60\nMvaa 176,208,236\nTa 152,38,0\nAR 60,88\nMvaa 180,212,236\nTa 154,38,2048\nAR 61,89\nMvaa 148,212,232\nTa 156,39,0\nD1 1,16,46,1024\n"
"Mvaa 116,212,228\nTa 158,39,2048\nCV 60,60,61\nMvaa 84,212,224\nAW 108,60\nAR 60,86\nMvaa 88,216,224\nAR 61,87\nCV 60,60,61\nMvaa 120,216,228\nD1 1,16,46,2048\nMvaa 152,216,232\n"
"AW 107,60\nAR 60,84\nMvaa 184,216,236\nAR 61,85\nCV 60,60,61\nMvaa 188,220,236\nAW 106,60\nAR 60,82\nMvaa 156,220,232\nD1 1,16,46,3072\nAR 61,83\nMvaa 124,220,228\nCV 60,60,61\n"
"AW 105,60\nMvaa 92,220,224\nWL 0\nMvaa 64,128,160\nRa 224,45,16384\nAR 60,80\nMvaa 96,128,164\nRa 228,45,18432\ns_add_u32 m0, s47, -16384\ns_add_u32 s46, s45, 0x400000\n"
"D1 1,16,46,0\nMvaa 128,128,168\nRa 232,47,16384\nAR 61,81\nMvaa 160,128,172\nRa 236,47,18432\nCV 60,60,61\nMvaa 164,132,172\nTa 192,32,16384\nAW 104,60\nMvaa 132,132,168\n"
"D1 1,16,46,1024\nAR 60,78\nMvaa 100,132,164\nTa 194,32,18432\nAR 61,79\nMvaa 68,132,160\nTa 196,33,16384\nCV 60,60,61\nMvaa 72,136,160\nTa 198,33,18432\nAW 103,60\n"
"Mvaa 104,136,164\nTa 200,34,16384\nD1 1,16,46,2048\nMvaa 136,136,168\nTa 202,34,18432\nAR 60,76\nMvaa 168,136,172\nAR 61,77\nCV 60,60,61\nMvaa 172,140,172\nTa 204,35,16384\n"
"AW 102,60\nMvaa 140,140,168\nTa 206,35,18432\nD1 1,16,46,3072\nMvaa 108,140,164\nTa 208,36,16384\nAR 60,74\nMvaa 76,140,160\nTa 210,36,18432\nAR 61,75\nMvaa 80,144,160\n"
"Ta 212,37,16384\nCV 60,60,61\nMvaa 112,144,164\nAW 101,60\nMvaa 144,144,168\nTa 214,37,18432\nAR 60,72\nMvaa 176,144,172\nTa 216,38,16384\nAR 61,73\nMvaa 180,148,172\n"
"Ta 218,38,18432\nCV 60,60,61\nMvaa 148,148,168\nTa 220,39,16384\nAW 100,60\nMvaa 116,148,164\nTa 222,39,18432\nAR 60,70\nMvaa 84,148,160\nAR 61,71\nCV 60,60,61\nMvaa 88,152,160\n"
"AW 99,60\nAR 60,68\nMvaa 120,152,164\nAR 61,69\nMvaa 152,152,168\nCV 60,60,61\nAW 98,60\nMvaa 184,152,172\nAR 60,66\nAR 61,67\nMvaa 188,156,172\nCV 60,60,61\nAW 97,60\n"
"Mvaa 156,156,168\nAR 60,64\nAR 61,65\nMvaa 124,156,164\nCV 60,60,61\nAW 96,60\nMvaa 92,156,160\ns_waitcnt vmcnt(20) lgkmcnt(0)\ns_barrier\nMvaa 64,192,224\nv_add_u32 v2, 0x0, v1\n"
"s_lshr_b32 s2, s59, 1\nMvaa 68,196,224\ns_add_u32 m0, s47, 0\ns_add_u32 s46, s86, 0xffff0000\nD1 1,16,46,0\nbuffer_store_dword v25, off, s[16:19], s54 offset:12 sc0 sc1\n"
"Mvaa 72,200,224\nv_add_u32 v24, s2, v2\nMvaa 76,204,224\nv_xor_b32 v26, 0x1000, v24\nv_lshrrev_b32 v20, 4, v0\nMvaa 80,208,224\nv_lshlrev_b32 v20, 2, v20\nMvaa 84,212,224\n"
"D1 1,16,46,1024\nv_and_b32 v0, 15, v0\nMvaa 88,216,224\nv_sub_u32 v0, v0, v20\nv_mul_f32 v64, s57, v64\nMvaa 92,220,224\ns_and_b32 s2, s11, 1\ns_lshl_b32 s2, s2, 6\n"
"v_mul_f32 v65, s57, v65\nMvaa 96,192,228\nv_subrev_u32 v3, s2, v0\nCV 64,64,65\nv_mul_f32 v66, s57, v66\nMvaa 100,196,228\nD1 1,16,46,2048\nv_mul_f32 v67, s57, v67\nCV 65,66,67\n"
"Mvaa 104,200,228\ns_xor_b32 s2, s2, 64\nv_subrev_u32 v0, s2, v0\nv_mul_f32 v68, s57, v68\nMvaa 108,204,228\nv_mov_b32 v4, 0\nv_mov_b32 v5, 0\nMvaa 112,208,228\nRa 128,24,0\n"
"v_mov_b32 v6, 0\nv_mul_f32 v69, s57, v69\nMvaa 116,212,228\nD1 1,16,46,3072\nCV 66,68,69\nv_mul_f32 v70, s57, v70\nMvaa 120,216,228\nRa 132,24,1024\nv_mov_b32 v7, 0\n"
"v_mul_f32 v71, s57, v71\nMvaa 124,220,228\ns_add_u32 m0, s47, 16384\ns_add_u32 s46, s45, 0x408000\nCV 67,70,71\nMvaa 128,192,232\nRa 136,24,2048\nv_mov_b32 v8, 0\n"
"v_mul_f32 v72, s57, v72\nMvaa 132,196,232\nD1 1,16,46,0\nv_mul_f32 v73, s57, v73\nMvaa 136,200,232\nRa 140,24,3072\nv_mov_b32 v9, 0\nCV 72,72,73\nMvaa 140,204,232\n"
"v_mov_b32 v10, 0\nv_mov_b32 v11, 0\nv_mul_f32 v74, s57, v74\nMvaa 144,208,232\nRa 144,26,0\nv_mov_b32 v12, 0\nv_mul_f32 v75, s57, v75\nMvaa 148,212,232\nD1 1,16,46,1024\n"
"CV 73,74,75\nv_mul_f32 v76, s57, v76\nMvaa 152,216,232\nRa 148,26,1024\nv_mov_b32 v13, 0\nMvaa 156,220,232\nv_mov_b32 v14, 0\nv_mov_b32 v15, 0\nv_mul_f32 v77, s57, v77\n"
"Mvaa 160,192,236\nRa 152,26,2048\nv_mov_b32 v16, 0\nCV 74,76,77\nMvaa 164,196,236\nD1 1,16,46,2048\nv_mul_f32 v78, s57, v78\nv_mul_f32 v79, s57, v79\nMvaa 168,200,236\n"
"Ra 156,26,3072\nv_mov_b32 v17, 0\nCV 75,78,79\nMvaa 172,204,236\nv_mov_b32 v18, 0\nv_mov_b32 v19, 0\nMvaa 176,208,236\ns_lshl_b32 s2, s11, 13\ns_add_u32 s2, s2, 0x20000\n"
"v_mul_f32 v80, s57, v80\nMvaa 180,212,236\nD1 1,16,46,3072\nv_mul_f32 v81, s57, v81\nCV 80,80,81\nMvaa 184,216,236\nv_add_u32 v22, s2, v1\nv_xor_b32 v23, 0x2000, v22\n"
"v_mul_f32 v82, s57, v82\nMvaa 188,220,236\ns_waitcnt vmcnt(25) lgkmcnt(4)\ns_cmp_eq_u32 s79, 1\ns_cbranch_scc1 .E346\n.E347:\nCvav 32,128,192,4\nRa 160,24,8192\n"
"v_mul_f32 v83, s57, v83\nCV 81,82,83\nMvav 32,132,200\nv_mul_f32 v84, s57, v84\nv_mul_f32 v85, s57, v85\nMvav 32,136,208\nRa 164,24,9216\nCV 82,84,85\nv_mul_f32 v86, s57, v86\n"
"Mvav 32,140,216\nv_mul_f32 v87, s57, v87\nCV 83,86,87\nv_mul_f32 v88, s57, v88\nCvav 36,140,220,8\nRa 168,24,10240\nv_mul_f32 v89, s57, v89\nCV 88,88,89\nMvav 36,136,212\n"
"s_add_u32 m0, s89, 32768\nv_mul_f32 v90, s57, v90\nv_mul_f32 v91, s57, v91\nMvav 36,132,204\nRa 172,24,11264\ns_waitcnt vmcnt(17) lgkmcnt(4)\ns_barrier\nMvav 36,128,196\n"
"CV 89,90,91\nv_mul_f32 v92, s57, v92\nv_mul_f32 v93, s57, v93\nMvav 36,144,228\nRa 176,26,8192\nCV 90,92,93\nv_mul_f32 v94, s57, v94\nSa 96,1,16,78,0\nMvav 36,148,236\n"
"s_add_u32 s46, s86, s88\nv_mul_f32 v95, s57, v95\nCV 91,94,95\nMvav 36,152,244\nRa 180,26,9216\nAW 192,64\nAW 193,65\nMvav 36,156,252\nAW 194,66\nAW 195,67\nMvav 32,156,248\n"
"Ra 184,26,10240\nAW 200,72\nAW 201,73\nMvav 32,152,240\nD1 1,16,46,0\nAW 202,74\nAW 203,75\nMvav 32,148,232\nRa 188,26,11264\nWL 4\nAW 208,80\nMvav 32,144,224\nAW 209,81\n"
"AW 210,82\nAW 211,83\nCvav 40,160,192,12\nAW 216,88\nAW 217,89\nAW 218,90\nSa 100,1,16,78,1024\nMvav 40,164,200\ns_cmp_eq_u32 s99, 0\nMvav 40,168,208\ns_cselect_b32 s87, s95, s87\n"
"AW 219,91\nv_mov_b32 v86, 0\nMvav 40,172,216\nv_mov_b32 v87, 0\nv_mov_b32 v88, 0\nv_mov_b32 v89, 0\nv_mul_f32 v128, s57, v128\nv_mul_f32 v129, s57, v129\nCV 128,128,129\n"
"v_mul_f32 v130, s57, v130\nCvav 44,172,220,16\nv_and_b32 v27, 0x70, v1\nbuffer_load_dword v27, v27, s[16:19], s61 offen offset:136 sc0 sc1\nv_mul_f32 v131, s57, v131\n"
"CV 129,130,131\nv_mul_f32 v132, s57, v132\nv_mul_f32 v133, s57, v133\nCV 130,132,133\nv_mul_f32 v134, s57, v134\nMvav 44,168,212\nD1 1,16,46,1024\nv_mul_f32 v135, s57, v135\n"
"CV 131,134,135\nds_write_b128 v23, v[128:131] offset:0\nv_mul_f32 v136, s57, v136\nv_mul_f32 v137, s57, v137\nCV 136,136,137\nv_mul_f32 v138, s57, v138\nMvav 44,164,204\nWL 1\n"
"v_mul_f32 v139, s57, v139\nCV 137,138,139\nv_mul_f32 v140, s57, v140\nv_mul_f32 v141, s57, v141\nCV 138,140,141\nv_mul_f32 v142, s57, v142\nMvav 44,160,196\n"
"v_mul_f32 v143, s57, v143\nCV 139,142,143\nds_write_b128 v23, v[136:139] offset:2048\nv_mul_f32 v144, s57, v144\nv_mul_f32 v145, s57, v145\nCV 144,144,145\n"
"v_mul_f32 v146, s57, v146\nv_mul_f32 v147, s57, v147\nMvav 44,176,228\nCV 145,146,147\nv_mul_f32 v148, s57, v148\nv_mul_f32 v149, s57, v149\nCV 146,148,149\n"
"v_mul_f32 v150, s57, v150\nv_mul_f32 v151, s57, v151\nCV 147,150,151\nds_write_b128 v23, v[144:147] offset:4096\nSa 104,1,16,78,2048\nMvav 44,180,236\ns_add_u32 s88, s88, 32768\n"
"EX 80,32\nv_mul_f32 v152, s57, v152\nv_mul_f32 v153, s57, v153\nCV 152,152,153\nv_mul_f32 v154, s57, v154\nMvav 44,184,244\nv_mul_f32 v155, s57, v155\nCV 153,154,155\n"
"v_mul_f32 v156, s57, v156\nv_mul_f32 v157, s57, v157\nCV 154,156,157\nv_mul_f32 v158, s57, v158\nv_mul_f32 v159, s57, v159\nCV 155,158,159\nMvav 44,188,252\nEX 81,33\n"
"ds_write_b128 v23, v[152:155] offset:6144\nv_mul_f32 v160, s57, v160\nv_mul_f32 v161, s57, v161\nCV 160,160,161\nMvav 40,188,248\nv_mul_f32 v162, s57, v162\n"
"v_mul_f32 v163, s57, v163\nCV 161,162,163\nv_mul_f32 v164, s57, v164\nv_mul_f32 v165, s57, v165\nCV 162,164,165\nv_mul_f32 v166, s57, v166\nv_mul_f32 v167, s57, v167\n"
"Mvav 40,184,240\nD1 1,16,46,2048\nCV 163,166,167\nds_write_b128 v23, v[160:163] offset:1024\nv_mul_f32 v168, s57, v168\nv_mul_f32 v169, s57, v169\nCV 168,168,169\n"
"v_mul_f32 v170, s57, v170\nv_mul_f32 v171, s57, v171\nMvav 40,180,232\nCV 169,170,171\nv_mul_f32 v172, s57, v172\nv_mul_f32 v173, s57, v173\nCV 170,172,173\n"
"v_mul_f32 v174, s57, v174\nv_mul_f32 v175, s57, v175\nCV 171,174,175\nMvav 40,176,224\nAD 84,80,81\nCV 64,80,81\nds_write_b128 v23, v[168:171] offset:3072\n"
"v_mul_f32 v176, s57, v176\nv_mul_f32 v177, s57, v177\nCV 176,176,177\nv_mul_f32 v178, s57, v178\nv_mul_f32 v179, s57, v179\nCV 177,178,179\nv_mul_f32 v180, s57, v180\n"
"v_mul_f32 v181, s57, v181\nCV 178,180,181\nv_mul_f32 v182, s57, v182\nv_mul_f32 v183, s57, v183\nCV 179,182,183\nds_write_b128 v23, v[176:179] offset:5120\nSa 108,1,16,78,3072\n"
"EX 82,34\nv_mul_f32 v184, s57, v184\nv_mul_f32 v185, s57, v185\nCV 184,184,185\nv_mul_f32 v186, s57, v186\nv_mul_f32 v187, s57, v187\nCV 185,186,187\nv_mul_f32 v188, s57, v188\n"
"v_mul_f32 v189, s57, v189\nCV 186,188,189\nv_mul_f32 v190, s57, v190\nv_mul_f32 v191, s57, v191\nCV 187,190,191\nds_write_b128 v23, v[184:187] offset:7168\n"
"s_waitcnt vmcnt(17) lgkmcnt(0)\nEX 83,35\nAD 84,84,82\nD1 1,16,46,3072\ns_add_u32 m0, s89, 49152\ns_barrier\nAD 84,84,83\nCV 65,82,83\nRa 128,24,32768\nRa 224,22,0\n"
"Ra 228,22,1024\nRa 232,22,2048\ns_add_u32 s46, s87, s88\nEX 80,36\nRa 132,24,33792\nRa 236,22,3072\nRa 240,22,4096\nRa 244,22,5120\nEX 81,37\nRa 248,22,6144\nRa 136,24,34816\n"
"Ra 252,22,7168\nv_mul_f32 v96, s57, v96\nv_mul_f32 v97, s57, v97\nD1 1,16,46,0\nCV 96,96,97\nv_mul_f32 v98, s57, v98\nRa 140,24,35840\nv_mul_f32 v99, s57, v99\nCV 97,98,99\n"
"v_mul_f32 v100, s57, v100\nAD 85,80,81\nCV 68,80,81\nv_mul_f32 v101, s57, v101\nCV 98,100,101\nRa 144,26,32768\nv_mul_f32 v102, s57, v102\nv_mul_f32 v103, s57, v103\n"
"CV 99,102,103\ns_cmp_eq_u32 s99, s93\nEX 82,38\nRa 148,26,33792\ns_cbranch_scc1 .E329\n.E330:\nAW 196,96\nAW 197,97\nEX 83,39\nAW 198,98\nRa 152,26,34816\nAD 85,85,82\nAW 199,99\n"
"v_mul_f32 v104, s57, v104\nD1 1,16,46,1024\nv_mul_f32 v105, s57, v105\nCV 104,104,105\nRa 156,26,35840\nv_mul_f32 v106, s57, v106\nv_mul_f32 v107, s57, v107\nCV 105,106,107\n"
"AD 85,85,83\nCV 69,82,83\nv_mul_f32 v108, s57, v108\nv_mul_f32 v109, s57, v109\nRa 160,24,40960\nCV 106,108,109\nv_mul_f32 v110, s57, v110\nv_mul_f32 v111, s57, v111\n"
"s_xor_b32 s89, s89, 0x10000\nEX 80,40\nRa 164,24,41984\nCV 107,110,111\nAW 204,104\nAW 205,105\nWL 6\nEX 81,41\nAW 206,106\nRa 168,24,43008\nAD 84,84,80\nAW 207,107\n"
"v_mul_f32 v112, s57, v112\nD1 1,16,46,2048\nv_mul_f32 v113, s57, v113\nCV 112,112,113\nRa 172,24,44032\nv_mul_f32 v114, s57, v114\nv_mul_f32 v115, s57, v115\nCV 113,114,115\n"
"AD 84,84,81\nCV 66,80,81\nv_mul_f32 v116, s57, v116\nv_mul_f32 v117, s57, v117\nCvav 48,128,192,4\nRa 176,26,40960\nCV 114,116,117\nv_mul_f32 v118, s57, v118\n"
"v_mul_f32 v119, s57, v119\nMvav 48,132,200\ns_cmp_eq_u32 s99, 1\nEX 82,42\nMvav 48,136,208\nRa 180,26,41984\ns_cselect_b32 s86, s94, s86\nCV 115,118,119\nAW 212,112\n"
"Mvav 48,140,216\nWL 6\nEX 83,43\nAW 213,113\nWV 8\nv_cmp_ne_u32 vcc, s15, v27\ns_nop 0\ns_cbranch_vccnz .S348\n.S349:\nCvav 52,140,220,8\nRa 184,26,43008\nAD 84,84,82\nAW 214,114\n"
"AW 215,115\nMvav 52,136,212\nD1 1,16,46,3072\ns_add_u32 m0, s89, 0\nv_mul_f32 v120, s57, v120\nMvav 52,132,204\nRa 188,26,44032\nv_mul_f32 v121, s57, v121\nCV 120,120,121\n"
"v_mul_f32 v122, s57, v122\nMvav 52,128,196\nAD 84,84,83\nCV 67,82,83\nv_mul_f32 v123, s57, v123\nCV 121,122,123\nMvav 52,144,228\nRa 128,2,49152\nv_mul_f32 v124, s57, v124\n"
"v_mul_f32 v125, s57, v125\nCV 122,124,125\ns_add_u32 s78, s78, s55\nSa 112,1,16,78,0\nMvav 52,148,236\ns_add_u32 s46, s86, s88\nEX 80,44\nMvav 52,152,244\nRa 132,2,50176\n"
"v_xor_b32 v24, 0x10000, v24\nv_mul_f32 v126, s57, v126\nv_mul_f32 v127, s57, v127\nMvav 52,156,252\nWL 6\nEX 81,45\nCV 123,126,127\nMvav 48,156,248\nRa 136,2,51200\nAD 85,85,80\n"
"AW 220,120\nAW 221,121\nMvav 48,152,240\nD1 1,16,46,0\nAW 222,122\nAW 223,123\nMvav 48,148,232\nRa 140,2,52224\nv_mov_b32 v112, -1\nv_mov_b32 v113, -1\nv_mov_b32 v114, -1\n"
"Mvav 48,144,224\nAD 85,85,81\nCV 70,80,81\nv_mov_b32 v115, -1\nCvav 56,160,192,12\nRa 144,2,53248\nSa 116,1,16,78,1024\nMvav 56,164,200\nEX 82,46\nMvav 56,168,208\nRa 148,2,54272\n"
"v_xor_b32 v26, 0x10000, v26\nMvav 56,172,216\nWL 6\nEX 83,47\nCvav 60,172,220,16\nRa 152,2,55296\nAD 85,85,82\nMvav 60,168,212\nD1 1,16,46,1024\nMvav 60,164,204\nRa 156,2,56320\n"
"Mvav 60,160,196\nAD 85,85,83\nCV 71,82,83\nMvav 60,176,228\nRa 160,2,57344\nv_max_f32 v90, v84, v85\nMvav 60,180,236\ns_add_u32 s88, s88, 32768\nEX 80,48\nMvav 60,184,244\n"
"Ra 164,2,58368\nv_cmp_lt_f32 vcc, s63, v90\nMvav 60,188,252\nWL 8\nEX 81,49\nMvav 56,188,248\nRa 168,2,59392\ns_cbranch_vccnz .T335\n.T336:\nv_mov_b32 v20, v84\n"
"v_mov_b32 v21, v85\nv_mov_b32 v22, v20\nv_mov_b32 v23, v21\nMvav 56,184,240\nD1 1,16,46,2048\nv_permlane32_swap_b32 v20, v22\nv_permlane32_swap_b32 v21, v23\nAD 20,20,22\n"
"AD 21,21,23\nv_mov_b32 v22, v20\nv_mov_b32 v23, v21\nMvav 56,180,232\nRa 172,2,60416\nv_permlane16_swap_b32 v20, v22\nv_permlane16_swap_b32 v21, v23\nAD 20,20,22\nAD 21,21,23\n"
"v_min_f32 v90, v20, v21\nv_cmp_gt_f32 vcc, s64, v90\ns_nop 0\ns_cbranch_vccnz .T337\n.T338:\nAD 86,86,84\nMvav 56,176,224\nAD 84,80,81\nCV 72,80,81\nZaav 0,128,64\nRa 176,2,61440\n"
"Sa 120,1,16,78,2048\nZaav 4,128,68\nEX 82,50\nZaav 12,132,68\nRa 180,2,62464\nZaav 8,132,64\nWL 8\nEX 83,51\nZaav 16,136,64\nRa 184,2,63488\nAD 84,84,82\nZaav 20,136,68\n"
"D1 1,16,46,3072\ns_add_u32 m0, s89, 16384\nZaav 28,140,68\nRa 188,2,64512\ns_barrier\nZaav 24,140,64\nAD 84,84,83\nCV 73,82,83\nZaav 32,144,64\nRa 128,24,0\nSa 124,1,16,78,3072\n"
"Zaav 36,144,68\ns_add_u32 s46, s87, s88\nEX 80,52\nZaav 44,148,68\nRa 132,24,1024\nv_xor_b32 v2, 0x10000, v2\nZaav 40,148,64\nWL 8\nEX 81,53\nZaav 48,152,64\nRa 136,24,2048\n"
"AD 87,87,85\nZaav 52,152,68\nD1 1,16,46,0\nZaav 60,156,68\nRa 140,24,3072\nZaav 56,156,64\nAD 85,80,81\nCV 76,80,81\nZaav 64,160,64\nRa 144,26,0\nZaav 68,160,68\n"
"s_cmp_eq_u32 s99, s92\nEX 82,54\nZaav 76,164,68\nRa 148,26,1024\ns_cbranch_scc1 .E331\n.E332:\nZaav 72,164,64\nWL 8\nEX 83,55\nZaav 80,168,64\nRa 152,26,2048\nAD 85,85,82\n"
"Zaav 84,168,68\nD1 1,16,46,1024\nZaav 92,172,68\nRa 156,26,3072\nZaav 88,172,64\nAD 85,85,83\nCV 77,82,83\nZaav 96,176,64\nRa 160,24,8192\nZaav 100,176,68\ns_sub_u32 s90, s90, 1\n"
"EX 80,56\nZaav 108,180,68\nRa 164,24,9216\ns_cbranch_scc1 .E333\n.E334:\nZaav 104,180,64\nWL 6\nEX 81,57\nZaav 112,184,64\nRa 168,24,10240\nAD 84,84,80\nv_and_b32 v27, 0xf0, v1\n"
"buffer_load_dword v27, v27, s[16:19], s61 offen offset:12 sc0 sc1\nZaav 116,184,68\nD1 1,16,46,2048\nZaav 124,188,68\nRa 172,24,11264\nZaav 120,188,64\nAD 84,84,81\nCV 74,80,81\n"
"s_sub_u32 s99, s99, 1\ns_cbranch_scc1 .SW327\n.p2align 6\n.A351:\nCvav 32,128,192,4\nRa 176,26,8192\nMvav 32,132,200\nEX 82,58\nMvav 32,136,208\nRa 180,26,9216\nMvav 32,140,216\n"
"WL 6\nEX 83,59\nCvav 36,140,220,8\nRa 184,26,10240\nAD 84,84,82\nMvav 36,136,212\nD1 1,16,46,3072\ns_add_u32 m0, s89, 32768\nMvav 36,132,204\nRa 188,26,11264\nMvav 36,128,196\n"
"AD 84,84,83\nCV 75,82,83\nMvav 36,144,228\nRa 128,2,16384\nMvav 36,148,236\ns_add_u32 s46, s86, s88\nEX 80,60\nMvav 36,152,244\nRa 132,2,17408\nMvav 36,156,252\nWL 6\nEX 81,61\n"
"Mvav 32,156,248\nRa 136,2,18432\nAD 85,85,80\nMvav 32,152,240\nD1 1,16,46,0\nMvav 32,148,232\nRa 140,2,19456\nSv 112,1,16,49,0\nMvav 32,144,224\nAD 85,85,81\nCV 78,80,81\n"
"Cvav 40,160,192,12\nRa 144,2,20480\nMvav 40,164,200\ns_cmp_eq_u32 s99, 0\nEX 82,62\nMvav 40,168,208\nRa 148,2,21504\ns_cselect_b32 s87, s95, s87\nMvav 40,172,216\nWL 6\nEX 83,63\n"
"Cvav 44,172,220,16\nRa 152,2,22528\nAD 85,85,82\nMvav 44,168,212\nD1 1,16,46,1024\nMvav 44,164,204\nRa 156,2,23552\nMvav 44,160,196\nAD 85,85,83\nCV 79,82,83\nMvav 44,176,228\n"
"Ra 160,2,24576\nv_max_f32 v90, v84, v85\nMvav 44,180,236\ns_add_u32 s88, s88, 32768\nEX 80,32\nMvav 44,184,244\nRa 164,2,25600\nv_cmp_lt_f32 vcc, s63, v90\nMvav 44,188,252\nWL 8\n"
"EX 81,33\nMvav 40,188,248\nRa 168,2,26624\ns_cbranch_vccnz .T358\n.T359:\nMvav 40,184,240\nD1 1,16,46,2048\nMvav 40,180,232\nRa 172,2,27648\nAD 86,86,84\nMvav 40,176,224\n"
"AD 84,80,81\nCV 64,80,81\nMaav 0,128,72\nRa 176,2,28672\nMaav 4,128,76\nEX 82,34\nMaav 12,132,76\nRa 180,2,29696\nMaav 8,132,72\ns_waitcnt vmcnt(13) lgkmcnt(8)\nEX 83,35\n"
"Maav 16,136,72\nRa 184,2,30720\nAD 84,84,82\nMaav 20,136,76\nD1 1,16,46,3072\ns_add_u32 m0, s89, 49152\nMaav 28,140,76\nRa 188,2,31744\ns_barrier\nMaav 24,140,72\nAD 84,84,83\n"
"CV 65,82,83\nMaav 32,144,72\nRa 128,24,32768\nMaav 36,144,76\ns_add_u32 s46, s87, s88\nEX 80,36\nMaav 44,148,76\nRa 132,24,33792\nMaav 40,148,72\nWL 8\nEX 81,37\nMaav 48,152,72\n"
"Ra 136,24,34816\nAD 87,87,85\nMaav 52,152,76\nD1 1,16,46,0\nMaav 60,156,76\nRa 140,24,35840\nMaav 56,156,72\nAD 85,80,81\nCV 68,80,81\nMaav 64,160,72\nRa 144,26,32768\n"
"Maav 68,160,76\ns_cmp_eq_u32 s99, s93\nEX 82,38\nMaav 76,164,76\nRa 148,26,33792\ns_cbranch_scc1 .E352\n.E353:\nMaav 72,164,72\nWL 8\nEX 83,39\nMaav 80,168,72\nRa 152,26,34816\n"
"AD 85,85,82\nMaav 84,168,76\nD1 1,16,46,1024\nMaav 92,172,76\nRa 156,26,35840\nMaav 88,172,72\nAD 85,85,83\nCV 69,82,83\nMaav 96,176,72\nRa 160,24,40960\nMaav 100,176,76\n"
"s_xor_b32 s89, s89, 0x10000\nEX 80,40\nMaav 108,180,76\nRa 164,24,41984\nMaav 104,180,72\nWL 6\nEX 81,41\nMaav 112,184,72\nRa 168,24,43008\nAD 84,84,80\nMaav 116,184,76\n"
"D1 1,16,46,2048\nMaav 124,188,76\nRa 172,24,44032\nMaav 120,188,72\nAD 84,84,81\nCV 66,80,81\nCvav 48,128,192,4\nRa 176,26,40960\nMvav 48,132,200\ns_cmp_eq_u32 s99, 1\nEX 82,42\n"
"Mvav 48,136,208\nRa 180,26,41984\ns_cselect_b32 s86, s94, s86\nMvav 48,140,216\nWL 6\nEX 83,43\nCvav 52,140,220,8\nRa 184,26,43008\nAD 84,84,82\nMvav 52,136,212\nD1 1,16,46,3072\n"
"s_add_u32 m0, s89, 0\nMvav 52,132,204\nRa 188,26,44032\nMvav 52,128,196\nAD 84,84,83\nCV 67,82,83\nMvav 52,144,228\nRa 128,2,49152\nMvav 52,148,236\ns_add_u32 s46, s86, s88\n"
"EX 80,44\nMvav 52,152,244\nRa 132,2,50176\nv_xor_b32 v24, 0x10000, v24\nMvav 52,156,252\nWL 6\nEX 81,45\nMvav 48,156,248\nRa 136,2,51200\nAD 85,85,80\nMvav 48,152,240\n"
"D1 1,16,46,0\nMvav 48,148,232\nRa 140,2,52224\nSv 112,1,16,49,1024\nMvav 48,144,224\nAD 85,85,81\nCV 70,80,81\nCvav 56,160,192,12\nRa 144,2,53248\nMvav 56,164,200\n"
"s_add_u32 s49, s49, 131072\nEX 82,46\nMvav 56,168,208\nRa 148,2,54272\nv_xor_b32 v26, 0x10000, v26\nMvav 56,172,216\nWL 6\nEX 83,47\nCvav 60,172,220,16\nRa 152,2,55296\n"
"AD 85,85,82\nMvav 60,168,212\nD1 1,16,46,1024\nMvav 60,164,204\nRa 156,2,56320\nMvav 60,160,196\nAD 85,85,83\nCV 71,82,83\nMvav 60,176,228\nRa 160,2,57344\n"
"v_max_f32 v90, v84, v85\nMvav 60,180,236\ns_add_u32 s88, s88, 32768\nEX 80,48\nMvav 60,184,244\nRa 164,2,58368\nv_cmp_lt_f32 vcc, s63, v90\nMvav 60,188,252\nWL 8\nEX 81,49\n"
"Mvav 56,188,248\nRa 168,2,59392\ns_cbranch_vccnz .T360\n.T361:\nMvav 56,184,240\nD1 1,16,46,2048\nMvav 56,180,232\nRa 172,2,60416\nAD 86,86,84\nMvav 56,176,224\nAD 84,80,81\n"
"CV 72,80,81\nMaav 0,128,64\nRa 176,2,61440\nMaav 4,128,68\nEX 82,50\nMaav 12,132,68\nRa 180,2,62464\nMaav 8,132,64\ns_waitcnt vmcnt(13) lgkmcnt(8)\nEX 83,51\nMaav 16,136,64\n"
"Ra 184,2,63488\nAD 84,84,82\nMaav 20,136,68\nD1 1,16,46,3072\ns_add_u32 m0, s89, 16384\nMaav 28,140,68\nRa 188,2,64512\ns_barrier\nMaav 24,140,64\nAD 84,84,83\nCV 73,82,83\n"
"Maav 32,144,64\nRa 128,24,0\nMaav 36,144,68\ns_add_u32 s46, s87, s88\nEX 80,52\nMaav 44,148,68\nRa 132,24,1024\nv_xor_b32 v2, 0x10000, v2\nMaav 40,148,64\nWL 8\nEX 81,53\n"
"Maav 48,152,64\nRa 136,24,2048\nAD 87,87,85\nMaav 52,152,68\nD1 1,16,46,0\nMaav 60,156,68\nRa 140,24,3072\nMaav 56,156,64\nAD 85,80,81\nCV 76,80,81\nMaav 64,160,64\nRa 144,26,0\n"
"Maav 68,160,68\ns_cmp_eq_u32 s99, s92\nEX 82,54\nMaav 76,164,68\nRa 148,26,1024\ns_cbranch_scc1 .E354\n.E355:\nMaav 72,164,64\nWL 8\nEX 83,55\nMaav 80,168,64\nRa 152,26,2048\n"
"AD 85,85,82\nMaav 84,168,68\nD1 1,16,46,1024\nMaav 92,172,68\nRa 156,26,3072\nMaav 88,172,64\nAD 85,85,83\nCV 77,82,83\nMaav 96,176,64\nRa 160,24,8192\nMaav 100,176,68\n"
"s_sub_u32 s90, s90, 1\nEX 80,56\nMaav 108,180,68\nRa 164,24,9216\ns_cbranch_scc1 .E356\n.E357:\nMaav 104,180,64\nWL 6\nEX 81,57\nMaav 112,184,64\nRa 168,24,10240\nAD 84,84,80\n"
"Maav 116,184,68\nD1 1,16,46,2048\ns_sub_u32 s99, s99, 1\nMaav 124,188,68\nRa 172,24,11264\nMaav 120,188,64\nAD 84,84,81\nCV 74,80,81\ns_cbranch_scc0 .A351\n.SW327:\n"
"v_mov_b32 v4, 0\nv_mov_b32 v5, 0\nv_mov_b32 v6, 0\nv_mov_b32 v7, 0\nv_mov_b32 v8, 0\nv_mov_b32 v9, 0\nv_mov_b32 v10, 0\nv_mov_b32 v11, 0\nv_mov_b32 v12, 0\nv_mov_b32 v13, 0\n"
"v_mov_b32 v14, 0\nv_mov_b32 v15, 0\nv_mov_b32 v16, 0\nv_mov_b32 v17, 0\nv_mov_b32 v18, 0\nv_mov_b32 v19, 0\ns_add_u32 s99, s98, 1\ns_mov_b32 s94, s96\ns_add_u32 s95, s96, 65536\n"
"s_or_b32 s2, s13, s11\ns_cmp_eq_u32 s2, 0\ns_cbranch_scc1 .E362\n.E363:\nCvaa 32,128,192,4\nRa 176,26,8192\nMvaa 32,132,200\nEX 82,58\nMvaa 32,136,208\nRa 180,26,9216\n"
"Mvaa 32,140,216\nWL 6\nEX 83,59\nCvaa 36,140,220,8\nRa 184,26,10240\nAD 84,84,82\nMvaa 36,136,212\nD1 1,16,46,3072\ns_add_u32 m0, s89, 32768\nMvaa 36,132,204\nRa 188,26,11264\n"
"Mvaa 36,128,196\nAD 84,84,83\nCV 75,82,83\nMvaa 36,144,228\nRa 128,2,16384\nMvaa 36,148,236\ns_add_u32 s46, s86, s88\nEX 80,60\nMvaa 36,152,244\nRa 132,2,17408\nMvaa 36,156,252\n"
"WL 6\nEX 81,61\nMvaa 32,156,248\nRa 136,2,18432\nAD 85,85,80\nMvaa 32,152,240\nD1 1,16,46,0\nMvaa 32,148,232\nRa 140,2,19456\nMvaa 32,144,224\nAD 85,85,81\nCV 78,80,81\n"
"Cvaa 40,160,192,12\nRa 144,2,20480\nMvaa 40,164,200\nEX 82,62\nMvaa 40,168,208\nRa 148,2,21504\nMvaa 40,172,216\nWL 6\nEX 83,63\nCvaa 44,172,220,16\nRa 152,2,22528\nAD 85,85,82\n"
"Mvaa 44,168,212\nD1 1,16,46,1024\nMvaa 44,164,204\nRa 156,2,23552\nMvaa 44,160,196\nAD 85,85,83\nCV 79,82,83\nMvaa 44,176,228\nRa 160,2,24576\nv_max_f32 v90, v84, v85\n"
"Mvaa 44,180,236\ns_add_u32 s88, s88, 32768\nEX 80,32\nMvaa 44,184,244\nRa 164,2,25600\nv_cmp_lt_f32 vcc, s63, v90\nMvaa 44,188,252\nWL 8\nEX 81,33\nMvaa 40,188,248\n"
"Ra 168,2,26624\ns_cbranch_vccnz .T370\n.T371:\nMvaa 40,184,240\nD1 1,16,46,2048\nMvaa 40,180,232\nRa 172,2,27648\nAD 86,86,84\nMvaa 40,176,224\nAD 84,80,81\nCV 64,80,81\n"
"Maav 0,128,72\nRa 176,2,28672\nMaav 4,128,76\nEX 82,34\nMaav 12,132,76\nRa 180,2,29696\nMaav 8,132,72\ns_waitcnt vmcnt(12) lgkmcnt(8)\nEX 83,35\nMaav 16,136,72\nRa 184,2,30720\n"
"AD 84,84,82\nMaav 20,136,76\nD1 1,16,46,3072\ns_add_u32 m0, s89, 49152\nMaav 28,140,76\nRa 188,2,31744\ns_barrier\nAR 98,0\nAR 99,1\nCV 100,98,99\nMaav 24,140,72\nAD 84,84,83\n"
"CV 65,82,83\nAR 98,2\nAR 99,3\nCV 101,98,99\nMaav 32,144,72\nRa 128,24,32768\nAR 98,8\nAR 99,9\nCV 102,98,99\nAR 98,10\nMaav 36,144,76\ns_add_u32 s46, s87, s88\nEX 80,36\n"
"AR 99,11\nMaav 44,148,76\nRa 132,24,33792\nCV 103,98,99\ns_add_u32 s5, s66, 0\nSv 100,1,16,5,0\nAR 98,4\nMaav 40,148,72\nWL 8\nEX 81,37\nAR 99,5\nCV 104,98,99\nMaav 48,152,72\n"
"Ra 136,24,34816\nAD 87,87,85\nAR 98,6\nAR 99,7\nCV 105,98,99\nMaav 52,152,76\nD1 1,16,46,0\nv_mov_b32 v96, v86\nv_mov_b32 v97, v87\nAR 98,12\nAR 99,13\nMaav 60,156,76\n"
"Ra 140,24,35840\nv_permlane32_swap_b32 v86, v96\nCV 106,98,99\nAR 98,14\nAR 99,15\nMaav 56,156,72\nAD 85,80,81\nCV 68,80,81\nCV 107,98,99\ns_add_u32 s5, s66, 0\n"
"Sv 104,1,16,5,1024\nMaav 64,160,72\nRa 144,26,32768\nv_permlane32_swap_b32 v87, v97\nAD 86,86,96\nAR 98,16\nAR 99,17\nMaav 68,160,76\ns_cmp_eq_u32 s99, s93\nEX 82,38\n"
"Maav 76,164,76\nRa 148,26,33792\ns_cbranch_scc1 .E364\n.E365:\nCV 108,98,99\nAR 98,18\nAR 99,19\nMaav 72,164,72\nWL 8\nEX 83,39\nCV 109,98,99\nAR 98,24\nMaav 80,168,72\n"
"Ra 152,26,34816\nAD 85,85,82\nAD 87,87,97\nAR 99,25\nCV 110,98,99\nMaav 84,168,76\nD1 1,16,46,1024\nv_mov_b32 v96, v86\nAR 98,26\nAR 99,27\nCV 111,98,99\nMaav 92,172,76\n"
"Ra 156,26,35840\nv_mov_b32 v97, v87\ns_add_u32 s5, s66, 0\nSv 108,1,16,5,2048\nAR 98,20\nMaav 88,172,72\nAD 85,85,83\nCV 69,82,83\nv_permlane16_swap_b32 v86, v96\nAR 99,21\n"
"CV 100,98,99\nMaav 96,176,72\nRa 160,24,40960\nv_permlane16_swap_b32 v87, v97\nAR 98,22\nAR 99,23\nCV 101,98,99\nMaav 100,176,76\ns_xor_b32 s89, s89, 0x10000\nEX 80,40\nAR 98,28\n"
"Maav 108,180,76\nRa 164,24,41984\nAD 86,86,96\nAD 87,87,97\nAR 99,29\nCV 102,98,99\nMaav 104,180,72\nWL 6\nEX 81,41\nAR 98,30\nAR 99,31\nMaav 112,184,72\nRa 168,24,43008\n"
"AD 84,84,80\nCV 103,98,99\ns_add_u32 s5, s66, 0\nSv 100,1,16,5,3072\nMaav 116,184,76\nD1 1,16,46,2048\nv_cmp_gt_u32 vcc, 0x100, v1\nv_mov_b32 v94, s15\nv_mov_b32 v95, s15\n"
"v_cndmask_b32 v92, v89, v88, vcc\nv_cndmask_b32 v93, v87, v86, vcc\nMaav 124,188,76\nRa 172,24,44032\nv_mov_b32 v86, 0\nAR 98,32\nAR 99,33\nCV 104,98,99\nMaav 120,188,72\n"
"AD 84,84,81\nCV 66,80,81\nAR 98,34\nAR 99,35\nCV 105,98,99\nCvaa 48,128,192,4\nRa 176,26,40960\nv_mov_b32 v87, 0\nv_mov_b32 v88, 0\nAR 98,40\nAR 99,41\nMvaa 48,132,200\n"
"s_cmp_eq_u32 s99, 1\nEX 82,42\nMvaa 48,136,208\nRa 180,26,41984\ns_cselect_b32 s86, s94, s86\nCV 106,98,99\nAR 98,42\nAR 99,43\nMvaa 48,140,216\nWL 6\nEX 83,43\nCV 107,98,99\n"
"Cvaa 52,140,220,8\nRa 184,26,43008\nAD 84,84,82\nv_mov_b32 v89, 0\ns_add_u32 s5, s66, 4096\nSv 104,1,16,5,0\nMvaa 52,136,212\nD1 1,16,46,3072\ns_add_u32 m0, s89, 0\nAR 98,36\n"
"AR 99,37\nCV 108,98,99\nMvaa 52,132,204\nRa 188,26,44032\ns_mov_b32 s64, 0x2b800000\nAR 98,38\nAR 99,39\nCV 109,98,99\nMvaa 52,128,196\nAD 84,84,83\nCV 67,82,83\nAR 98,44\n"
"AR 99,45\nCV 110,98,99\nMvaa 52,144,228\nRa 128,2,49152\nAR 98,46\nAR 99,47\nCV 111,98,99\nMvaa 52,148,236\ns_add_u32 s46, s86, s88\nEX 80,44\nMvaa 52,152,244\nRa 132,2,50176\n"
"v_xor_b32 v24, 0x10000, v24\ns_add_u32 s5, s66, 4096\nSv 108,1,16,5,1024\nAR 98,48\nMvaa 52,156,252\nWL 6\nEX 81,45\nAR 99,49\nCV 100,98,99\nMvaa 48,156,248\nRa 136,2,51200\n"
"AD 85,85,80\nAR 98,50\nAR 99,51\nCV 101,98,99\nMvaa 48,152,240\nD1 1,16,46,0\nAR 98,56\nAR 99,57\nCV 102,98,99\nAR 98,58\nMvaa 48,148,232\nRa 140,2,52224\nAR 99,59\nCV 103,98,99\n"
"s_add_u32 s5, s66, 4096\nSv 100,1,16,5,2048\nMvaa 48,144,224\nAD 85,85,81\nCV 70,80,81\nAR 98,52\nAR 99,53\nCV 104,98,99\nCvaa 56,160,192,12\nRa 144,2,53248\nAR 98,54\nAR 99,55\n"
"CV 105,98,99\nAR 98,60\nMvaa 56,164,200\nEX 82,46\nAR 99,61\nCV 106,98,99\nMvaa 56,168,208\nRa 148,2,54272\nv_xor_b32 v26, 0x10000, v26\nAR 98,62\nAR 99,63\nCV 107,98,99\n"
"Mvaa 56,172,216\nWL 6\nEX 83,47\ns_add_u32 s5, s66, 4096\nSv 104,1,16,5,3072\nCvaa 60,172,220,16\nRa 152,2,55296\nAD 85,85,82\nAR 98,64\nAR 99,65\nCV 108,98,99\nMvaa 60,168,212\n"
"D1 1,16,46,1024\nAR 98,66\nAR 99,67\nCV 109,98,99\nAR 98,72\nMvaa 60,164,204\nRa 156,2,56320\nAR 99,73\nCV 110,98,99\nAR 98,74\nAR 99,75\nMvaa 60,160,196\nAD 85,85,83\n"
"CV 71,82,83\nCV 111,98,99\ns_add_u32 s5, s66, 16384\nSv 108,1,16,5,0\nMvaa 60,176,228\nRa 160,2,57344\nv_max_f32 v90, v84, v85\nAR 98,68\nAR 99,69\nCV 100,98,99\nMvaa 60,180,236\n"
"s_add_u32 s88, s88, 32768\nEX 80,48\nAR 98,70\nMvaa 60,184,244\nRa 164,2,58368\nv_cmp_lt_f32 vcc, s63, v90\nMvaa 60,188,252\nWL 8\nEX 81,49\nMvaa 56,188,248\nRa 168,2,59392\n"
"s_cbranch_vccnz .T372\n.T373:\nv_mov_b32 v20, v84\nv_mov_b32 v21, v85\nv_mov_b32 v22, v20\nv_mov_b32 v23, v21\nMvaa 56,184,240\nD1 1,16,46,2048\nv_permlane32_swap_b32 v20, v22\n"
"v_permlane32_swap_b32 v21, v23\nAD 20,20,22\nAD 21,21,23\nv_mov_b32 v22, v20\nv_mov_b32 v23, v21\nMvaa 56,180,232\nRa 172,2,60416\nv_permlane16_swap_b32 v20, v22\n"
"v_permlane16_swap_b32 v21, v23\nAD 20,20,22\nAD 21,21,23\nv_min_f32 v90, v20, v21\nv_cmp_gt_f32 vcc, s64, v90\ns_nop 0\ns_cbranch_vccnz .T374\n.T375:\nAD 86,86,84\n"
"Mvaa 56,176,224\nAD 84,80,81\nCV 72,80,81\nAR 99,71\nCV 101,98,99\nAR 98,76\nZvav 128,128,64\nRa 176,2,61440\nAR 99,77\nCV 102,98,99\nAR 98,78\nAR 99,79\nZvav 132,128,68\n"
"EX 82,50\nCV 103,98,99\nZvav 140,132,68\nRa 180,2,62464\ns_add_u32 s5, s66, 16384\nSv 100,1,16,5,1024\nAR 98,80\nAR 99,81\nZvav 136,132,64\ns_waitcnt vmcnt(21) lgkmcnt(8)\n"
"EX 83,51\nCV 104,98,99\nAR 98,82\nZvav 144,136,64\nRa 184,2,63488\nAD 84,84,82\nAR 99,83\nCV 105,98,99\nAR 98,88\nZvav 148,136,68\nD1 1,16,46,3072\ns_add_u32 m0, s89, 16384\n"
"AR 99,89\nCV 106,98,99\nAR 98,90\nZvav 156,140,68\nRa 188,2,64512\ns_barrier\nAR 99,91\nCV 107,98,99\nZvav 152,140,64\nAD 84,84,83\nCV 73,82,83\ns_add_u32 s5, s66, 16384\n"
"Sv 104,1,16,5,2048\nAR 98,84\nZvav 160,144,64\nRa 128,24,0\nAR 99,85\nCV 108,98,99\nAR 98,86\nAR 99,87\nZvav 164,144,68\ns_add_u32 s46, s87, s88\nEX 80,52\nCV 109,98,99\n"
"Zvav 172,148,68\nRa 132,24,1024\nv_xor_b32 v2, 0x10000, v2\nAR 98,92\nAR 99,93\nCV 110,98,99\nZvav 168,148,64\nWL 8\nEX 81,53\nAR 98,94\nAR 99,95\nZvav 176,152,64\nRa 136,24,2048\n"
"AD 87,87,85\nCV 111,98,99\ns_add_u32 s5, s66, 16384\nSv 108,1,16,5,3072\nZvav 180,152,68\nD1 1,16,46,0\nAR 98,96\nAR 99,97\nCV 100,98,99\nAR 98,98\nZvav 188,156,68\n"
"Ra 140,24,3072\nAR 99,99\nCV 101,98,99\nAR 98,104\nAR 99,105\nZvav 184,156,64\nAD 85,80,81\nCV 76,80,81\nCV 102,98,99\nAR 98,106\nAR 99,107\nZvav 192,160,64\nRa 144,26,0\n"
"CV 103,98,99\ns_add_u32 s5, s66, 20480\nSv 100,1,16,5,0\nAR 98,100\nZvav 196,160,68\ns_cmp_eq_u32 s99, s92\nEX 82,54\nZvav 204,164,68\nRa 148,26,1024\ns_cbranch_scc1 .E366\n"
".E367:\nAR 99,101\nCV 104,98,99\nAR 98,102\nZvav 200,164,64\nWL 8\nEX 83,55\nAR 99,103\nCV 105,98,99\nZvav 208,168,64\nRa 152,26,2048\nAD 85,85,82\nAR 98,108\nAR 99,109\n"
"CV 106,98,99\nZvav 212,168,68\nD1 1,16,46,1024\nAR 98,110\nAR 99,111\nCV 107,98,99\nZvav 220,172,68\nRa 156,26,3072\ns_add_u32 s5, s66, 20480\nSv 104,1,16,5,1024\nAR 98,112\n"
"AR 99,113\nZvav 216,172,64\nAD 85,85,83\nCV 77,82,83\nCV 108,98,99\nAR 98,114\nAR 99,115\nZvav 224,176,64\nRa 160,24,8192\nCV 109,98,99\nAR 98,120\nAR 99,121\nCV 110,98,99\n"
"Zvav 228,176,68\ns_sub_u32 s90, s90, 1\nEX 80,56\nZvav 236,180,68\nRa 164,24,9216\ns_cbranch_scc1 .E368\n.E369:\nAR 98,122\nAR 99,123\nCV 111,98,99\nZvav 232,180,64\nWL 6\n"
"EX 81,57\ns_add_u32 s5, s66, 20480\nSv 108,1,16,5,2048\nZvav 240,184,64\nRa 168,24,10240\nAD 84,84,80\nAR 98,116\nAR 99,117\nCV 100,98,99\nZvav 244,184,68\nD1 1,16,46,2048\n"
"AR 98,118\nAR 99,119\nCV 101,98,99\nAR 98,124\nZvav 252,188,68\nRa 172,24,11264\nAR 99,125\nCV 102,98,99\nAR 98,126\nAR 99,127\nZvav 248,188,64\nAD 84,84,81\nCV 74,80,81\n"
"CV 103,98,99\ns_add_u32 s5, s66, 20480\nSv 100,1,16,5,3072\ns_sub_u32 s99, s99, 1\n.p2align 6\n.A376:\nCvaa 32,128,192,4\nRa 176,26,8192\nMvaa 32,132,200\nEX 82,58\n"
"Mvaa 32,136,208\nRa 180,26,9216\nMvaa 32,140,216\nWL 6\nEX 83,59\nCvaa 36,140,220,8\nRa 184,26,10240\nAD 84,84,82\nMvaa 36,136,212\nD1 1,16,46,3072\ns_add_u32 m0, s89, 32768\n"
"Mvaa 36,132,204\nRa 188,26,11264\nMvaa 36,128,196\nAD 84,84,83\nCV 75,82,83\nMvaa 36,144,228\nRa 128,2,16384\nMvaa 36,148,236\ns_add_u32 s46, s86, s88\nEX 80,60\nMvaa 36,152,244\n"
"Ra 132,2,17408\nMvaa 36,156,252\nWL 6\nEX 81,61\nMvaa 32,156,248\nRa 136,2,18432\nAD 85,85,80\nMvaa 32,152,240\nD1 1,16,46,0\nMvaa 32,148,232\nRa 140,2,19456\nSv 112,1,16,49,0\n"
"Mvaa 32,144,224\nAD 85,85,81\nCV 78,80,81\nCvaa 40,160,192,12\nRa 144,2,20480\nMvaa 40,164,200\ns_cmp_eq_u32 s99, 0\nEX 82,62\nMvaa 40,168,208\nRa 148,2,21504\n"
"s_cselect_b32 s87, s95, s87\nMvaa 40,172,216\nWL 6\nEX 83,63\nCvaa 44,172,220,16\nRa 152,2,22528\nAD 85,85,82\nMvaa 44,168,212\nD1 1,16,46,1024\nMvaa 44,164,204\nRa 156,2,23552\n"
"Mvaa 44,160,196\nAD 85,85,83\nCV 79,82,83\nMvaa 44,176,228\nRa 160,2,24576\nv_max_f32 v90, v84, v85\nMvaa 44,180,236\ns_add_u32 s88, s88, 32768\nEX 80,32\nMvaa 44,184,244\n"
"Ra 164,2,25600\nv_cmp_lt_f32 vcc, s63, v90\nMvaa 44,188,252\nWL 8\nEX 81,33\nMvaa 40,188,248\nRa 168,2,26624\ns_cbranch_vccnz .T385\n.T386:\nMvaa 40,184,240\nD1 1,16,46,2048\n"
"Mvaa 40,180,232\nRa 172,2,27648\nAD 86,86,84\nMvaa 40,176,224\nAD 84,80,81\nCV 64,80,81\nMvav 128,128,72\nRa 176,2,28672\nMvav 132,128,76\ns_cmp_eq_u32 s99, s98\nEX 82,34\n"
"Mvav 140,132,76\nRa 180,2,29696\ns_cbranch_scc1 .E383\n.E384:\nMvav 136,132,72\ns_waitcnt vmcnt(13) lgkmcnt(8)\nEX 83,35\nMvav 144,136,72\nRa 184,2,30720\nAD 84,84,82\n"
"Mvav 148,136,76\nD1 1,16,46,3072\ns_add_u32 m0, s89, 49152\nMvav 156,140,76\nRa 188,2,31744\ns_barrier\nMvav 152,140,72\nAD 84,84,83\nCV 65,82,83\nMvav 160,144,72\n"
"Ra 128,24,32768\nMvav 164,144,76\ns_add_u32 s46, s87, s88\nEX 80,36\nMvav 172,148,76\nRa 132,24,33792\nMvav 168,148,72\nWL 8\nEX 81,37\nMvav 176,152,72\nRa 136,24,34816\n"
"AD 87,87,85\nMvav 180,152,76\nD1 1,16,46,0\nMvav 188,156,76\nRa 140,24,35840\nMvav 184,156,72\nAD 85,80,81\nCV 68,80,81\nMvav 192,160,72\nRa 144,26,32768\nMvav 196,160,76\n"
"s_cmp_eq_u32 s99, s93\nEX 82,38\nMvav 204,164,76\nRa 148,26,33792\ns_cbranch_scc1 .E377\n.E378:\nMvav 200,164,72\nWL 8\nEX 83,39\nMvav 208,168,72\nRa 152,26,34816\nAD 85,85,82\n"
"Mvav 212,168,76\nD1 1,16,46,1024\nMvav 220,172,76\nRa 156,26,35840\nMvav 216,172,72\nAD 85,85,83\nCV 69,82,83\nMvav 224,176,72\nRa 160,24,40960\nMvav 228,176,76\n"
"s_xor_b32 s89, s89, 0x10000\nEX 80,40\nMvav 236,180,76\nRa 164,24,41984\nMvav 232,180,72\nWL 6\nEX 81,41\nMvav 240,184,72\nRa 168,24,43008\nAD 84,84,80\nMvav 244,184,76\n"
"D1 1,16,46,2048\nMvav 252,188,76\nRa 172,24,44032\nMvav 248,188,72\nAD 84,84,81\nCV 66,80,81\nCvaa 48,128,192,4\nRa 176,26,40960\nMvaa 48,132,200\ns_cmp_eq_u32 s99, 1\nEX 82,42\n"
"Mvaa 48,136,208\nRa 180,26,41984\ns_cselect_b32 s86, s94, s86\nMvaa 48,140,216\nWL 6\nEX 83,43\nCvaa 52,140,220,8\nRa 184,26,43008\nAD 84,84,82\nMvaa 52,136,212\nD1 1,16,46,3072\n"
"s_add_u32 m0, s89, 0\nMvaa 52,132,204\nRa 188,26,44032\nMvaa 52,128,196\nAD 84,84,83\nCV 67,82,83\nMvaa 52,144,228\nRa 128,2,49152\nMvaa 52,148,236\ns_add_u32 s46, s86, s88\n"
"EX 80,44\nMvaa 52,152,244\nRa 132,2,50176\nv_xor_b32 v24, 0x10000, v24\nMvaa 52,156,252\nWL 6\nEX 81,45\nMvaa 48,156,248\nRa 136,2,51200\nAD 85,85,80\nMvaa 48,152,240\n"
"D1 1,16,46,0\nMvaa 48,148,232\nRa 140,2,52224\nSv 112,1,16,49,1024\nMvaa 48,144,224\nAD 85,85,81\nCV 70,80,81\nCvaa 56,160,192,12\nRa 144,2,53248\nMvaa 56,164,200\n"
"s_add_u32 s49, s49, 131072\nEX 82,46\nMvaa 56,168,208\nRa 148,2,54272\nv_xor_b32 v26, 0x10000, v26\nMvaa 56,172,216\nWL 6\nEX 83,47\nCvaa 60,172,220,16\nRa 152,2,55296\n"
"AD 85,85,82\nMvaa 60,168,212\nD1 1,16,46,1024\nMvaa 60,164,204\nRa 156,2,56320\nMvaa 60,160,196\nAD 85,85,83\nCV 71,82,83\nMvaa 60,176,228\nRa 160,2,57344\n"
"v_max_f32 v90, v84, v85\nMvaa 60,180,236\ns_add_u32 s88, s88, 32768\nEX 80,48\nMvaa 60,184,244\nRa 164,2,58368\nv_cmp_lt_f32 vcc, s63, v90\nMvaa 60,188,252\nWL 8\nEX 81,49\n"
"Mvaa 56,188,248\nRa 168,2,59392\ns_cbranch_vccnz .T387\n.T388:\nMvaa 56,184,240\nD1 1,16,46,2048\nMvaa 56,180,232\nRa 172,2,60416\nAD 86,86,84\nMvaa 56,176,224\nAD 84,80,81\n"
"CV 72,80,81\nMvav 128,128,64\nRa 176,2,61440\nMvav 132,128,68\nEX 82,50\nMvav 140,132,68\nRa 180,2,62464\nMvav 136,132,64\ns_waitcnt vmcnt(13) lgkmcnt(8)\nEX 83,51\n"
"Mvav 144,136,64\nRa 184,2,63488\nAD 84,84,82\nMvav 148,136,68\nD1 1,16,46,3072\ns_add_u32 m0, s89, 16384\nMvav 156,140,68\nRa 188,2,64512\ns_barrier\nMvav 152,140,64\nAD 84,84,83\n"
"CV 73,82,83\nMvav 160,144,64\nRa 128,24,0\nMvav 164,144,68\ns_add_u32 s46, s87, s88\nEX 80,52\nMvav 172,148,68\nRa 132,24,1024\nv_xor_b32 v2, 0x10000, v2\nMvav 168,148,64\nWL 8\n"
"EX 81,53\nMvav 176,152,64\nRa 136,24,2048\nAD 87,87,85\nMvav 180,152,68\nD1 1,16,46,0\nMvav 188,156,68\nRa 140,24,3072\nMvav 184,156,64\nAD 85,80,81\nCV 76,80,81\nMvav 192,160,64\n"
"Ra 144,26,0\nMvav 196,160,68\ns_cmp_eq_u32 s99, s92\nEX 82,54\nMvav 204,164,68\nRa 148,26,1024\ns_cbranch_scc1 .E379\n.E380:\nMvav 200,164,64\nWL 8\nEX 83,55\nMvav 208,168,64\n"
"Ra 152,26,2048\nAD 85,85,82\nMvav 212,168,68\nD1 1,16,46,1024\nMvav 220,172,68\nRa 156,26,3072\nMvav 216,172,64\nAD 85,85,83\nCV 77,82,83\nMvav 224,176,64\nRa 160,24,8192\n"
"Mvav 228,176,68\ns_sub_u32 s90, s90, 1\nEX 80,56\nMvav 236,180,68\nRa 164,24,9216\ns_cbranch_scc1 .E381\n.E382:\nMvav 232,180,64\nWL 6\nEX 81,57\nMvav 240,184,64\nRa 168,24,10240\n"
"AD 84,84,80\nMvav 244,184,68\nD1 1,16,46,2048\ns_sub_u32 s99, s99, 1\nMvav 252,188,68\nRa 172,24,11264\nMvav 248,188,64\nAD 84,84,81\nCV 74,80,81\ns_cbranch_scc0 .A376\nEX 82,58\n"
"EX 83,59\nAD 84,84,82\nD1 1,16,46,3072\ns_add_u32 m0, s89, 32768\ns_add_u32 s46, s86, s88\nAD 84,84,83\nCV 75,82,83\nRa 128,2,16384\nv_xor_b32 v3, 0x10000, v2\nEX 80,60\n"
"Ra 132,2,17408\nv_add_u32 v0, 0x20000, v1\nEX 81,61\nRa 136,2,18432\nAD 85,85,80\nD1 1,16,46,0\ns_sub_u32 s94, s49, 1966080\nRa 140,2,19456\ns_add_u32 s95, s94, 1048576\n"
"AD 85,85,81\nCV 78,80,81\nRa 144,2,20480\ns_xor_b32 s92, s94, 0x202000\nEX 82,62\nRa 148,2,21504\ns_add_u32 s93, s96, 983040\nEX 83,63\nRa 152,2,22528\nAD 85,85,82\n"
"D1 1,16,46,1024\ns_xor_b32 s93, s93, 0x202000\nRa 156,2,23552\ns_lshl_b32 s89, s11, 12\nAD 85,85,83\nCV 79,82,83\nRa 160,2,24576\nv_max_f32 v90, v84, v85\n"
"v_cmp_lt_f32 vcc, s63, v90\nRa 164,2,25600\ns_cbranch_vccnz .T389\n.T390:\nAD 86,86,84\nAD 87,87,85\nWL 8\nv_mov_b32 v84, v86\nv_mov_b32 v85, v87\nRa 168,2,26624\n"
"v_permlane32_swap_b32 v86, v84\nv_permlane32_swap_b32 v87, v85\nD1 1,16,46,2048\nAD 86,86,84\nRa 172,2,27648\nAD 87,87,85\nv_mov_b32 v84, v86\nv_mov_b32 v85, v87\nMvav 128,128,72\n"
"Ra 176,2,28672\nv_permlane16_swap_b32 v86, v84\nv_permlane16_swap_b32 v87, v85\nMvav 132,128,76\nAD 86,86,84\nAD 87,87,85\nMvav 140,132,76\nRa 180,2,29696\nv_rcp_f32 v86, v86\n"
"Mvav 136,132,72\nWL 8\nv_rcp_f32 v87, v87\nMvav 144,136,72\nRa 184,2,30720\nMvav 148,136,76\nD1 1,16,46,3072\nMvav 156,140,76\nRa 188,2,31744\nMU 128,128,86\nMU 129,129,86\n"
"Mvav 152,140,72\nCV 96,128,129\nMU 130,130,86\nMU 131,131,86\nMvav 160,144,72\nRa 128,2,0\nCV 97,130,131\nMU 132,132,87\nMvav 164,144,76\nMU 133,133,87\nCV 100,132,133\n"
"MU 134,134,87\nMvav 172,148,76\nRa 132,2,1024\nMU 135,135,87\nCV 101,134,135\nMvav 168,148,72\nWL 8\ns_add_u32 m0, s89, 131072\ns_add_u32 s46, s93, 0\nMvav 176,152,72\n"
"Ra 136,2,2048\nMU 136,136,86\nMU 137,137,86\nMvav 180,152,76\nD1 1,16,46,0\nCV 98,136,137\nMU 138,138,86\nMvav 188,156,76\nRa 140,2,3072\nMU 139,139,86\nCV 99,138,139\n"
"Mvav 184,156,72\ns_add_u32 s5, s94, 0\nSv 96,1,16,5,0\nMU 140,140,87\nMvav 192,160,72\nRa 144,2,4096\nMU 141,141,87\nCV 102,140,141\nMvav 196,160,76\nMU 142,142,87\nMU 143,143,87\n"
"CV 103,142,143\nMvav 204,164,76\nRa 148,2,5120\nSv 100,1,16,5,1024\nMU 144,144,86\nMvav 200,164,72\nWL 8\nMU 145,145,86\nCV 104,144,145\nMvav 208,168,72\nRa 152,2,6144\n"
"MU 146,146,86\nMU 147,147,86\nMvav 212,168,76\nD1 1,16,46,1024\nCV 105,146,147\nMU 148,148,87\nMvav 220,172,76\nRa 156,2,7168\nMU 149,149,87\nCV 108,148,149\nMvav 216,172,72\n"
"MU 150,150,87\nMU 151,151,87\nCV 109,150,151\nMvav 224,176,72\nRa 160,2,8192\nMU 152,152,86\nMU 153,153,86\nMvav 228,176,76\nCV 106,152,153\nMU 154,154,86\nMU 155,155,86\n"
"Mvav 236,180,76\nRa 164,2,9216\nCV 107,154,155\ns_add_u32 s5, s94, 131072\nMvav 232,180,72\nWL 8\nSv 104,1,16,5,0\nMU 156,156,87\nMvav 240,184,72\nRa 168,2,10240\nMU 157,157,87\n"
"CV 110,156,157\nMvav 244,184,76\nD1 1,16,46,2048\nMU 158,158,87\nMU 159,159,87\nMvav 252,188,76\nRa 172,2,11264\nCV 111,158,159\nSv 108,1,16,5,1024\nMvav 248,188,72\n"
"MU 160,160,86\nMU 161,161,86\nZaav 0,128,96\nRa 176,2,12288\nCV 112,160,161\nMU 162,162,86\nMaav 0,132,104\nMU 163,163,86\nCV 113,162,163\nZaav 4,132,108\nRa 180,2,13312\n"
"MU 164,164,87\nMaav 4,128,100\ns_waitcnt vmcnt(18) lgkmcnt(8)\nMU 165,165,87\nZaav 12,136,100\nRa 184,2,14336\nCV 116,164,165\nMaav 12,140,108\nD1 1,16,46,3072\nMU 166,166,87\n"
"Zaav 8,140,104\nRa 188,2,15360\ns_barrier\nMaav 8,136,96\nMU 167,167,87\nCV 117,166,167\nZaav 16,144,96\nRa 128,2,32768\nMU 168,168,86\nMU 169,169,86\nv_lshrrev_b32 v88, 8, v1\n"
"v_lshlrev_b32 v89, 5, v88\nMaav 16,148,104\nCV 114,168,169\nMU 170,170,86\nbuffer_load_dwordx4 v[64:67], v89, s[16:19], s40 offen offset:0 sc0 sc1\n"
"buffer_store_dword v25, off, s[16:19], s54 offset:512 sc0 sc1\nZaav 20,148,108\nRa 132,2,33792\nMU 171,171,86\nMaav 20,144,100\nWL 8\ns_add_u32 m0, s89, 49152\n"
"s_add_u32 s46, s96, 1179648\nZaav 28,152,100\nRa 136,2,34816\nCV 115,170,171\nMaav 28,156,108\nD1 1,16,46,0\ns_add_u32 s5, s94, 262144\n"
"buffer_load_dwordx4 v[68:71], v89, s[16:19], s40 offen offset:16 sc0 sc1\nZaav 24,156,104\nRa 140,2,35840\nSv 112,1,16,5,0\nMaav 24,152,96\nMU 172,172,87\nMU 173,173,87\n"
"Zaav 32,160,96\nRa 144,2,36864\nCV 118,172,173\nMU 174,174,87\nMaav 32,164,104\nMU 175,175,87\nCV 119,174,175\n"
"buffer_load_dwordx4 v[72:75], v89, s[16:19], s40 offen offset:1792 sc0 sc1\nZaav 36,164,108\nRa 148,2,37888\nSv 116,1,16,5,1024\nMaav 36,160,100\nWL 8\nMU 176,176,86\n"
"Zaav 44,168,100\nRa 152,2,38912\nMU 177,177,86\nMaav 44,172,108\nD1 1,16,46,1024\nCV 120,176,177\nbuffer_load_dwordx4 v[76:79], v89, s[16:19], s40 offen offset:1808 sc0 sc1\n"
"Zaav 40,172,104\nRa 156,2,39936\nMU 178,178,86\nMaav 40,168,96\nMU 179,179,86\nCV 121,178,179\nZaav 48,176,96\nRa 160,2,40960\nMU 180,180,87\nMU 181,181,87\nMaav 48,180,104\n"
"CV 124,180,181\nMU 182,182,87\nv_bfe_u32 v90, v1, 4, 4\nv_mul_u32_u24 v91, 896, v90\nv_lshl_add_u32 v91, v88, 4, v91\nZaav 52,180,108\nRa 164,2,41984\nMU 183,183,87\n"
"buffer_load_dwordx4 v[12:15], v91, s[28:31], s22 offen offset:0\nMaav 52,176,100\nWL 8\nCV 125,182,183\nZaav 60,184,100\nRa 168,2,43008\nMU 184,184,86\nMaav 60,188,108\n"
"D1 1,16,46,2048\nMU 185,185,86\nZaav 56,188,104\nRa 172,2,44032\nCV 122,184,185\nbuffer_load_dwordx4 v[16:19], v91, s[28:31], s23 offen offset:0\nv_add_u32 v90, s21, v90\n"
"v_lshlrev_b32 v88, 2, v90\nMaav 56,184,96\nMU 186,186,86\nMU 187,187,86\nv_lshrrev_b32 v92, 8, v1\nv_bfe_u32 v93, v1, 6, 2\nZaav 64,128,96\nRa 176,2,45056\nCV 123,186,187\n"
"s_add_u32 s5, s94, 393216\nMaav 64,132,104\nSv 120,1,16,5,0\nMU 188,188,87\nZaav 68,132,108\nRa 180,2,46080\nMU 189,189,87\nMaav 68,128,100\nWL 8\nCV 126,188,189\nZaav 76,136,100\n"
"Ra 184,2,47104\nMU 190,190,87\nMaav 76,140,108\nD1 1,16,46,3072\nMU 191,191,87\nZaav 72,140,104\nRa 188,2,48128\nCV 127,190,191\nv_cmp_eq_u32 vcc, v92, v93\nv_and_b32 v92, 16, v1\n"
"v_mov_b32 v93, 0x3f80\nv_cndmask_b32 v93, 0, v93, vcc\nMaav 72,136,96\nSv 124,1,16,5,1024\nv_lshlrev_b32 v93, v92, v93\nv_and_b32 v92, 32, v1\nZaav 80,144,96\nRa 128,2,49152\n"
"MU 192,192,86\nMU 193,193,86\nMaav 80,148,104\nCV 32,192,193\nMU 194,194,86\nZaav 84,148,108\nRa 132,2,50176\nMU 195,195,86\nMaav 84,144,100\nWL 8\ns_add_u32 m0, s89, 147456\n"
"s_add_u32 s46, s93, 32768\nZaav 92,152,100\nRa 136,2,51200\nCV 33,194,195\nMaav 92,156,108\nD1 1,16,46,0\nMU 196,196,87\nZaav 88,156,104\nRa 140,2,52224\nMU 197,197,87\n"
"Maav 88,152,96\nCV 36,196,197\nMU 198,198,87\nZaav 96,160,96\nRa 144,2,53248\nMU 199,199,87\nCV 37,198,199\nMaav 96,164,104\nMU 200,200,86\nMU 201,201,86\nZaav 100,164,108\n"
"Ra 148,2,54272\nCV 34,200,201\nMaav 100,160,100\nWL 8\nMU 202,202,86\nZaav 108,168,100\nRa 152,2,55296\nMU 203,203,86\nMaav 108,172,108\nD1 1,16,46,1024\nCV 35,202,203\n"
"Zaav 104,172,104\nRa 156,2,56320\ns_add_u32 s5, s94, 524288\nv_cmp_eq_u32 vcc, 0, v92\nv_mov_b32 v4, 0\nv_mov_b32 v5, 0\nv_cndmask_b32 v6, 0, v93, vcc\nMaav 104,168,96\n"
"Sv 32,1,16,5,0\nMU 204,204,87\nv_sub_u32 v7, v93, v6\nv_mov_b32 v8, 0\nv_mov_b32 v9, 0\nMaav 0,176,112\nRa 160,2,57344\nMU 205,205,87\nCV 38,204,205\nMaav 0,180,120\n"
"MU 206,206,87\nMU 207,207,87\nMaav 4,180,124\nRa 164,2,58368\nCV 39,206,207\nMaav 4,176,116\nWL 8\nSv 36,1,16,5,1024\nMaav 12,184,116\nRa 168,2,59392\nMU 208,208,86\n"
"Maav 12,188,124\nD1 1,16,46,2048\nMU 209,209,86\nMaav 8,188,120\nRa 172,2,60416\nCV 40,208,209\nMaav 8,184,112\nMU 210,210,86\nMU 211,211,86\nMaav 16,128,112\nRa 176,2,61440\n"
"CV 41,210,211\nMU 212,212,87\nMaav 16,132,120\nMU 213,213,87\nCV 44,212,213\nMaav 20,132,124\nRa 180,2,62464\nMU 214,214,87\nMaav 20,128,116\ns_waitcnt vmcnt(32) lgkmcnt(8)\n"
"MU 215,215,87\nMaav 28,136,116\nRa 184,2,63488\nCV 45,214,215\nMaav 28,140,124\nD1 1,16,46,3072\nMU 216,216,86\nMaav 24,140,120\nRa 188,2,64512\ns_barrier\nMaav 24,136,112\n"
"MU 217,217,86\nCV 42,216,217\nMaav 32,144,112\nRa 128,3,0\nMU 218,218,86\nMU 219,219,86\nMaav 32,148,120\nCV 43,218,219\ns_add_u32 s5, s94, 655360\nMaav 36,148,124\nRa 132,3,1024\n"
"Sv 40,1,16,5,0\nMaav 36,144,116\nWL 8\nMU 220,220,87\nMaav 44,152,116\nRa 136,3,2048\nMU 221,221,87\nMaav 44,156,124\nCV 46,220,221\nMU 222,222,87\nMaav 40,156,120\nRa 140,3,3072\n"
"MU 223,223,87\nMaav 40,152,112\nCV 47,222,223\nSv 44,1,16,5,1024\nMaav 48,160,112\nRa 144,3,4096\nMU 224,224,86\nMaav 48,164,120\nMU 225,225,86\nCV 48,224,225\n"
"buffer_load_dword v27, off, s[16:19], s60 offset:512 sc0 sc1\nMaav 52,164,124\nRa 148,3,5120\nMU 226,226,86\nMaav 52,160,116\nWL 8\nMU 227,227,86\nMaav 60,168,116\nRa 152,3,6144\n"
"CV 49,226,227\nMaav 60,172,124\nMU 228,228,87\nMU 229,229,87\nMaav 56,172,120\nRa 156,3,7168\nCV 52,228,229\nMaav 56,168,112\nMU 230,230,87\nMU 231,231,87\nMaav 64,176,112\n"
"Ra 160,3,8192\nCV 53,230,231\nMaav 64,180,120\nMU 232,232,86\nMU 233,233,86\nMaav 68,180,124\nRa 164,3,9216\nCV 50,232,233\nMaav 68,176,116\nWL 8\nMU 234,234,86\nMaav 76,184,116\n"
"Ra 168,3,10240\nMU 235,235,86\nMaav 76,188,124\nCV 51,234,235\ns_add_u32 s5, s94, 786432\nMaav 72,188,120\nRa 172,3,11264\nSv 48,1,16,5,0\nMaav 72,184,112\nMU 236,236,87\n"
"MU 237,237,87\nMaav 80,128,112\nRa 176,3,12288\nCV 54,236,237\nMaav 80,132,120\nMU 238,238,87\nMU 239,239,87\nMaav 84,132,124\nRa 180,3,13312\nCV 55,238,239\nMaav 84,128,116\n"
"WL 8\nSv 52,1,16,5,1024\nMaav 92,136,116\nRa 184,3,14336\nMU 240,240,86\nMaav 92,140,124\nMU 241,241,86\nCV 56,240,241\nMaav 88,140,120\nRa 188,3,15360\nMU 242,242,86\n"
"Maav 88,136,112\nMU 243,243,86\nCV 57,242,243\nMaav 96,144,112\nRa 128,3,16384\nMU 244,244,87\nMaav 96,148,120\nMU 245,245,87\nCV 60,244,245\nMaav 100,148,124\nRa 132,3,17408\n"
"MU 246,246,87\nMaav 100,144,116\nWL 8\nMU 247,247,87\nMaav 108,152,116\nRa 136,3,18432\nCV 61,246,247\nMaav 108,156,124\nMU 248,248,86\nMU 249,249,86\nMaav 104,156,120\n"
"Ra 140,3,19456\nCV 58,248,249\nMaav 104,152,112\nMU 250,250,86\nMU 251,251,86\nMaav 0,160,32\nRa 144,3,20480\nCV 59,250,251\nMaav 0,164,40\ns_add_u32 s5, s94, 917504\n"
"Sv 56,1,16,5,0\nMaav 4,164,44\nRa 148,3,21504\nMU 252,252,87\nMaav 4,160,36\nWL 8\nMU 253,253,87\nMaav 12,168,36\nRa 152,3,22528\nCV 62,252,253\nMaav 12,172,44\nMU 254,254,87\n"
"MU 255,255,87\nMaav 8,172,40\nRa 156,3,23552\nCV 63,254,255\nMaav 8,168,32\nSv 60,1,16,5,1024\nMaav 16,176,32\nRa 160,3,24576\nWV 19\nMaav 16,180,40\nv_xor_b32 v92, s15, v66\n"
"Maav 20,180,44\nRa 164,3,25600\nMaav 20,176,36\nWL 8\nMaav 28,184,36\nRa 168,3,26624\nMaav 28,188,44\nv_xor_b32 v93, s15, v70\nMaav 24,188,40\nRa 172,3,27648\nMaav 24,184,32\n"
"v_xor_b32 v94, s15, v74\nMaav 32,128,32\nRa 176,3,28672\nv_or3_b32 v92, v92, v93, v94\nMaav 32,132,40\nv_xor_b32 v93, s15, v78\nMaav 36,132,44\nRa 180,3,29696\nMaav 36,128,36\n"
"s_waitcnt vmcnt(14) lgkmcnt(8)\nMaav 44,136,36\nRa 184,3,30720\nMaav 44,140,44\nv_or_b32 v92, v92, v93\nv_cmp_ne_u32 vcc, 0, v92\ns_nop 0\ns_cbranch_vccnz .S397\n.S398:\n"
"Maav 40,140,40\nRa 188,3,31744\ns_barrier\nMaav 40,136,32\ns_mov_b32 s2, 0xc040000\nMaav 48,144,32\nRa 128,3,32768\nbuffer_load_dword v80, v88, s[16:19], s2 offen sc0 sc1\n"
"Maav 48,148,40\nMaav 52,148,44\nRa 132,3,33792\nMaav 52,144,36\nWL 8\nMaav 60,152,36\nRa 136,3,34816\nMaav 60,156,44\n"
"buffer_load_dword v81, v88, s[16:19], s2 offen offset:64 sc0 sc1\nMaav 56,156,40\nRa 140,3,35840\nMaav 56,152,32\nv_and_b32 v92, 0x7ff, v67\nMaav 64,160,32\nRa 144,3,36864\n"
"v_lshrrev_b32 v93, 16, v67\nWV 6\nv_cmp_ne_u32 vcc, s15, v27\ns_nop 0\ns_cbranch_vccnz .S400\n.S401:\nMaav 64,164,40\ns_add_u32 s4, s92, 0\nMaav 68,164,44\nRa 148,3,37888\n"
"Maav 68,160,36\nWL 8\ns_add_u32 m0, s89, 81920\ns_add_u32 s46, s93, 65536\nbuffer_load_dwordx4 a[192:195], v1, s[16:19], s4 offen offset:0 sc0 sc1\nMaav 76,168,36\nRa 152,3,38912\n"
"Maav 76,172,44\nD1 1,16,46,0\nMaav 72,172,40\nRa 156,3,39936\nMaav 72,168,32\nbuffer_load_dwordx4 a[196:199], v1, s[16:19], s4 offen offset:1024 sc0 sc1\nMaav 80,176,32\n"
"Ra 160,3,40960\nv_add_u32 v94, 16, v90\nMaav 80,180,40\ns_add_u32 s4, s92, 131072\nv_cmp_eq_u32 vcc, v92, v90\ns_nop 1\nv_cndmask_b32 v12, v12, v64, vcc\n"
"v_cndmask_b32 v13, v13, v65, vcc\nv_cndmask_b32 v14, v14, v68, vcc\nv_cndmask_b32 v15, v15, v69, vcc\nMaav 84,180,44\nRa 164,3,41984\nv_cmp_eq_u32 vcc, v93, v90\ns_nop 1\n"
"v_cndmask_b32 v12, v12, v72, vcc\nv_cndmask_b32 v13, v13, v73, vcc\nv_cndmask_b32 v14, v14, v76, vcc\nv_cndmask_b32 v15, v15, v77, vcc\nMaav 84,176,36\nWL 8\n"
"buffer_load_dwordx4 a[200:203], v1, s[16:19], s4 offen offset:0 sc0 sc1\nMaav 92,184,36\nRa 168,3,43008\nv_cmp_eq_u32 vcc, v92, v94\ns_nop 1\nv_cndmask_b32 v16, v16, v64, vcc\n"
"v_cndmask_b32 v17, v17, v65, vcc\nv_cndmask_b32 v18, v18, v68, vcc\nv_cndmask_b32 v19, v19, v69, vcc\nMaav 92,188,44\nD1 1,16,46,1024\nv_cmp_eq_u32 vcc, v93, v94\ns_nop 1\n"
"v_cndmask_b32 v16, v16, v72, vcc\nv_cndmask_b32 v17, v17, v73, vcc\nv_cndmask_b32 v18, v18, v76, vcc\nv_cndmask_b32 v19, v19, v77, vcc\nMaav 88,188,40\nRa 172,3,44032\n"
"Maav 88,184,32\nbuffer_load_dwordx4 a[204:207], v1, s[16:19], s4 offen offset:1024 sc0 sc1\nMaav 96,128,32\nRa 176,3,45056\nMaav 96,132,40\ns_add_u32 s4, s92, 262144\n"
"Maav 100,132,44\nRa 180,3,46080\nMaav 100,128,36\nWL 8\nbuffer_load_dwordx4 a[208:211], v1, s[16:19], s4 offen offset:0 sc0 sc1\nMaav 108,136,36\nRa 184,3,47104\nMaav 108,140,44\n"
"D1 1,16,46,2048\nMaav 104,140,40\nRa 188,3,48128\nMaav 104,136,32\nbuffer_load_dwordx4 a[212:215], v1, s[16:19], s4 offen offset:1024 sc0 sc1\nMaav 0,144,48\nRa 128,3,49152\n"
"Maav 0,148,56\ns_add_u32 s4, s92, 393216\nMaav 4,148,60\nRa 132,3,50176\nMaav 4,144,52\nWL 8\nbuffer_load_dwordx4 a[216:219], v1, s[16:19], s4 offen offset:0 sc0 sc1\n"
"Maav 12,152,52\nRa 136,3,51200\nMaav 12,156,60\nD1 1,16,46,3072\nMaav 8,156,56\nRa 140,3,52224\nMaav 8,152,48\n"
"buffer_load_dwordx4 a[220:223], v1, s[16:19], s4 offen offset:1024 sc0 sc1\nMaav 16,160,48\nRa 144,3,53248\nMaav 16,164,56\nMaav 20,164,60\nRa 148,3,54272\nMaav 20,160,52\nWL 8\n"
"s_add_u32 m0, s89, 65536\ns_add_u32 s46, s93, 98304\nMaav 28,168,52\nRa 152,3,55296\nMaav 28,172,60\nD1 1,16,46,0\nMaav 24,172,56\nRa 156,3,56320\nMaav 24,168,48\nMaav 32,176,48\n"
"Ra 160,3,57344\nMaav 32,180,56\nMaav 36,180,60\nRa 164,3,58368\nMaav 36,176,52\nWL 8\nMaav 44,184,52\nRa 168,3,59392\nMaav 44,188,60\nD1 1,16,46,1024\nMaav 40,188,56\n"
"Ra 172,3,60416\nMaav 40,184,48\nMaav 48,128,48\nRa 176,3,61440\nMaav 48,132,56\nMaav 52,132,60\nRa 180,3,62464\nMaav 52,128,52\nWL 8\nMaav 60,136,52\nRa 184,3,63488\n"
"Maav 60,140,60\nD1 1,16,46,2048\nMaav 56,140,56\nRa 188,3,64512\ns_barrier\nMaav 56,136,48\nMaav 64,144,48\nRa 128,0,0\nMaav 64,148,56\nMaav 68,148,60\nRa 132,0,1024\n"
"Maav 68,144,52\nWL 8\nMaav 76,152,52\nRa 136,0,2048\nMaav 76,156,60\nD1 1,16,46,3072\nMaav 72,156,56\nRa 140,0,3072\nMaav 72,152,48\nMaav 80,160,48\nRa 144,0,4096\nMaav 80,164,56\n"
"Maav 84,164,60\nRa 148,0,5120\nMaav 84,160,52\nWL 8\ns_add_u32 m0, s89, 98304\ns_add_u32 s46, s93, 131072\nMaav 92,168,52\nRa 152,0,6144\nMaav 92,172,60\nD1 1,16,46,0\n"
"Maav 88,172,56\nRa 156,0,7168\nWV 5\nAR 22,195\nMaav 88,168,48\nAR 23,199\nv_max_u32 v21, v22, v23\nMaav 96,176,48\nRa 160,0,8192\nAR 22,203\nMaav 96,180,56\nAR 23,207\n"
"v_max3_u32 v21, v21, v22, v23\nMaav 100,180,60\nRa 164,0,9216\nAR 22,211\nMaav 100,176,52\nWL 6\nAR 23,215\nv_max3_u32 v21, v21, v22, v23\nMaav 108,184,52\nRa 168,0,10240\n"
"AR 22,219\nMaav 108,188,60\nD1 1,16,46,1024\nAR 23,223\nMaav 104,188,56\nRa 172,0,11264\nv_max3_u32 v21, v21, v22, v23\nMaav 104,184,48\nv_cmp_eq_u32 vcc, -1, v21\ns_nop 0\n"
"s_cbranch_vccnz .S391\n.S392:\nMaaa 0,128,192\nRa 176,0,12288\nMaaa 0,132,200\nv_sub_f32 v92, 1.0, v80\nMaaa 0,136,208\nRa 180,0,13312\nMaaa 0,140,216\nWL 6\nMaaa 4,140,220\n"
"Ra 184,0,14336\nv_sub_f32 v93, 1.0, v81\nMaaa 4,136,212\nD1 1,16,46,2048\nMaaa 4,132,204\nRa 188,0,15360\nMaaa 4,128,196\nCV 92,92,93\nMaaa 8,144,192\nRa 128,0,16384\n"
"v_and_b32 v93, 0xffff0000, v92\nMaaa 8,148,200\nv_lshlrev_b32 v92, 16, v92\nMaaa 8,152,208\nRa 132,0,17408\nMaaa 8,156,216\nWL 6\nMaaa 12,156,220\nRa 136,0,18432\n"
"v_lshlrev_b32 v64, 16, v12\nMaaa 12,152,212\nD1 1,16,46,3072\nMaaa 12,148,204\nRa 140,0,19456\nMaaa 12,144,196\nv_and_b32 v65, 0xffff0000, v12\nMaaa 16,160,192\nRa 144,0,20480\n"
"MU 64,64,92\nMaaa 16,164,200\nMU 65,65,92\nMaaa 16,168,208\nRa 148,0,21504\nMaaa 16,172,216\nWL 6\ns_add_u32 m0, s89, 114688\ns_add_u32 s46, s93, 163840\nMaaa 20,172,220\n"
"Ra 152,0,22528\nv_lshlrev_b32 v66, 16, v13\nMaaa 20,168,212\nD1 1,16,46,0\nMaaa 20,164,204\nRa 156,0,23552\nMaaa 20,160,196\nv_and_b32 v67, 0xffff0000, v13\nMaaa 24,176,192\n"
"Ra 160,0,24576\nMU 66,66,92\nMaaa 24,180,200\ns_add_u32 s4, s92, 524288\nMaaa 24,184,208\nRa 164,0,25600\nMaaa 24,188,216\nWL 6\n"
"buffer_load_dwordx4 a[224:227], v1, s[16:19], s4 offen offset:0 sc0 sc1\nMaaa 28,188,220\nRa 168,0,26624\nMU 67,67,92\nMaaa 28,184,212\nD1 1,16,46,1024\nMaaa 28,180,204\n"
"Ra 172,0,27648\nMaaa 28,176,196\nbuffer_load_dwordx4 a[228:231], v1, s[16:19], s4 offen offset:1024 sc0 sc1\nMaaa 32,128,192\nRa 176,0,28672\nv_lshlrev_b32 v68, 16, v14\n"
"Maaa 32,132,200\ns_add_u32 s4, s92, 655360\nMaaa 32,136,208\nRa 180,0,29696\nMaaa 32,140,216\ns_waitcnt vmcnt(8) lgkmcnt(6)\n"
"buffer_load_dwordx4 a[232:235], v1, s[16:19], s4 offen offset:0 sc0 sc1\nMaaa 36,140,220\nRa 184,0,30720\nv_and_b32 v69, 0xffff0000, v14\nMaaa 36,136,212\nD1 1,16,46,2048\n"
"Maaa 36,132,204\nRa 188,0,31744\ns_barrier\nMaaa 36,128,196\nbuffer_load_dwordx4 a[236:239], v1, s[16:19], s4 offen offset:1024 sc0 sc1\nMaaa 40,144,192\nRa 128,2,16384\n"
"MU 68,68,92\nMaaa 40,148,200\ns_add_u32 s4, s92, 786432\nMaaa 40,152,208\nRa 132,2,17408\nMaaa 40,156,216\nWL 6\n"
"buffer_load_dwordx4 a[240:243], v1, s[16:19], s4 offen offset:0 sc0 sc1\nMaaa 44,156,220\nRa 136,2,18432\nMU 69,69,92\nMaaa 44,152,212\nD1 1,16,46,3072\nMaaa 44,148,204\n"
"Ra 140,2,19456\nMaaa 44,144,196\nbuffer_load_dwordx4 a[244:247], v1, s[16:19], s4 offen offset:1024 sc0 sc1\nMaaa 48,160,192\nRa 144,2,20480\nv_lshlrev_b32 v70, 16, v15\n"
"Maaa 48,164,200\ns_add_u32 s4, s92, 917504\nMaaa 48,168,208\nRa 148,2,21504\nMaaa 48,172,216\nWL 6\ns_add_u32 m0, s89, 0\ns_add_u32 s46, s93, 196608\n"
"buffer_load_dwordx4 a[248:251], v1, s[16:19], s4 offen offset:0 sc0 sc1\nMaaa 52,172,220\nRa 152,2,22528\nv_and_b32 v71, 0xffff0000, v15\nMaaa 52,168,212\nD1 1,16,46,0\n"
"Maaa 52,164,204\nRa 156,2,23552\nMaaa 52,160,196\nbuffer_load_dwordx4 a[252:255], v1, s[16:19], s4 offen offset:1024 sc0 sc1\nMaaa 56,176,192\nRa 160,2,24576\nMU 70,70,92\n"
"Maaa 56,180,200\nMU 71,71,92\nMaaa 56,184,208\nRa 164,2,25600\nMaaa 56,188,216\nWL 6\nMaaa 60,188,220\nRa 168,2,26624\nv_lshlrev_b32 v72, 16, v16\nMaaa 60,184,212\n"
"D1 1,16,46,1024\nMaaa 60,180,204\nRa 172,2,27648\nMaaa 60,176,196\nv_and_b32 v73, 0xffff0000, v16\nMaaa 64,128,192\nRa 176,2,28672\nMU 72,72,93\nMaaa 64,132,200\nMU 73,73,93\n"
"Maaa 64,136,208\nRa 180,2,29696\nMaaa 64,140,216\nWL 6\nMaaa 68,140,220\nRa 184,2,30720\nv_lshlrev_b32 v74, 16, v17\nMaaa 68,136,212\nD1 1,16,46,2048\nMaaa 68,132,204\n"
"Ra 188,2,31744\nMaaa 68,128,196\nv_and_b32 v75, 0xffff0000, v17\nMaaa 72,144,192\nRa 128,2,0\nMU 74,74,93\nMaaa 72,148,200\nMU 75,75,93\nMaaa 72,152,208\nRa 132,2,1024\n"
"Maaa 72,156,216\nWL 6\nMaaa 76,156,220\nRa 136,2,2048\nv_lshlrev_b32 v76, 16, v18\nMaaa 76,152,212\nD1 1,16,46,3072\nMaaa 76,148,204\nRa 140,2,3072\nMaaa 76,144,196\n"
"v_and_b32 v77, 0xffff0000, v18\nMaaa 80,160,192\nRa 144,2,4096\nMU 76,76,93\nMaaa 80,164,200\nv_bfe_u32 v24, v1, 4, 3\nMaaa 80,168,208\nRa 148,2,5120\nMaaa 80,172,216\nWL 6\n"
"Maaa 84,172,220\nRa 152,2,6144\nMU 77,77,93\nMaaa 84,168,212\nv_mul_u32_u24 v24, 0xc00, v24\nMaaa 84,164,204\nRa 156,2,7168\nMaaa 84,160,196\nv_bfe_u32 v26, v1, 7, 1\n"
"Maaa 88,176,192\nRa 160,2,8192\nv_lshlrev_b32 v78, 16, v19\nMaaa 88,180,200\nv_add_lshl_u32 v24, v24, v26, 13\nMaaa 88,184,208\nRa 164,2,9216\nMaaa 88,188,216\nWL 6\n"
"Maaa 92,188,220\nRa 168,2,10240\nv_and_b32 v79, 0xffff0000, v19\nMaaa 92,184,212\nbuffer_load_dword v27, v24, s[16:19], s41 offen sc0 sc1\nMaaa 92,180,204\nRa 172,2,11264\n"
"Maaa 92,176,196\nMU 78,78,93\nMaaa 96,128,192\nRa 176,2,12288\nMU 79,79,93\nMaaa 96,132,200\nMaaa 96,136,208\nRa 180,2,13312\nMaaa 96,140,216\ns_waitcnt vmcnt(8) lgkmcnt(6)\n"
"Maaa 100,140,220\nRa 184,2,14336\nMaaa 100,136,212\nMaaa 100,132,204\nRa 188,2,15360\ns_barrier\nWV 4\nMaaa 100,128,196\nAR 22,227\nAR 23,231\nMaaa 104,144,192\nRa 128,2,32768\n"
"v_max_u32 v21, v22, v23\nMaaa 104,148,200\nAR 22,235\nAR 23,239\nMaaa 104,152,208\nRa 132,2,33792\nv_max3_u32 v21, v21, v22, v23\nMaaa 104,156,216\nWL 6\nAR 22,243\nAR 23,247\n"
"Maaa 108,156,220\nRa 136,2,34816\nv_max3_u32 v21, v21, v22, v23\nMaaa 108,152,212\nAR 22,251\nAR 23,255\nMaaa 108,148,204\nRa 140,2,35840\nv_max3_u32 v21, v21, v22, v23\n"
"Maaa 108,144,196\nv_cmp_eq_u32 vcc, -1, v21\ns_nop 0\ns_cbranch_vccnz .S394\n.S395:\nMaaa 0,160,224\nRa 144,2,36864\nMaaa 0,164,232\nMaaa 0,168,240\nRa 148,2,37888\n"
"Maaa 0,172,248\nWL 6\nMaaa 4,172,252\nRa 152,2,38912\nMaaa 4,168,244\nMaaa 4,164,236\nRa 156,2,39936\nMaaa 4,160,228\nMaaa 8,176,224\nRa 160,2,40960\nMaaa 8,180,232\n"
"Maaa 8,184,240\nRa 164,2,41984\nMaaa 8,188,248\nWL 6\nMaaa 12,188,252\nRa 168,2,43008\nMaaa 12,184,244\nMaaa 12,180,236\nRa 172,2,44032\nMaaa 12,176,228\nAR 82,0\nAR 83,1\n"
"Maaa 16,128,224\nRa 176,2,45056\nCV 20,82,83\nAR 82,2\nMaaa 16,132,232\nAR 83,3\nCV 21,82,83\nMaaa 16,136,240\nRa 180,2,46080\nAR 82,8\nMaaa 16,140,248\nWL 6\nAR 83,9\n"
"Maaa 20,140,252\nRa 184,2,47104\nCV 22,82,83\nMaaa 20,136,244\nAR 82,10\nAR 83,11\nMaaa 20,132,236\nRa 188,2,48128\nCV 23,82,83\ns_add_u32 s5, s95, 0\nMaaa 20,128,228\n"
"Sv 20,1,16,5,0\nAR 82,4\nMaaa 24,144,224\nRa 128,2,49152\nAR 83,5\nMaaa 24,148,232\nCV 84,82,83\nAR 82,6\nMaaa 24,152,240\nRa 132,2,50176\nAR 83,7\nMaaa 24,156,248\nWL 6\n"
"CV 85,82,83\nAR 82,12\nMaaa 28,156,252\nRa 136,2,51200\nAR 83,13\nMaaa 28,152,244\nCV 86,82,83\nAR 82,14\nMaaa 28,148,236\nRa 140,2,52224\nAR 83,15\nMaaa 28,144,228\nCV 87,82,83\n"
"Sv 84,1,16,5,1024\nMaaa 32,160,224\nRa 144,2,53248\nAR 82,16\nAR 83,17\nMaaa 32,164,232\nCV 20,82,83\nAR 82,18\nMaaa 32,168,240\nRa 148,2,54272\nAR 83,19\nMaaa 32,172,248\nWL 6\n"
"CV 21,82,83\nMaaa 36,172,252\nRa 152,2,55296\nAR 82,24\nMaaa 36,168,244\nAR 83,25\nCV 22,82,83\nMaaa 36,164,236\nRa 156,2,56320\nAR 82,26\nAR 83,27\nMaaa 36,160,228\nCV 23,82,83\n"
"s_add_u32 s5, s95, 131072\nMaaa 40,176,224\nRa 160,2,57344\nSv 20,1,16,5,0\nMaaa 40,180,232\nAR 82,20\nAR 83,21\nMaaa 40,184,240\nRa 164,2,58368\nCV 84,82,83\nMaaa 40,188,248\n"
"WL 6\nAR 82,22\nAR 83,23\nMaaa 44,188,252\nRa 168,2,59392\nCV 85,82,83\nMaaa 44,184,244\nAR 82,28\nAR 83,29\nMaaa 44,180,236\nRa 172,2,60416\nCV 86,82,83\nMaaa 44,176,228\n"
"AR 82,30\nAR 83,31\nMaaa 48,128,224\nRa 176,2,61440\nCV 87,82,83\nSv 84,1,16,5,1024\nMaaa 48,132,232\nAR 82,32\nAR 83,33\nMaaa 48,136,240\nRa 180,2,62464\nCV 20,82,83\n"
"Maaa 48,140,248\ns_waitcnt vmcnt(5) lgkmcnt(6)\nAR 82,34\nMaaa 52,140,252\nRa 184,2,63488\nAR 83,35\nMaaa 52,136,244\nCV 21,82,83\nAR 82,40\nMaaa 52,132,236\nRa 188,2,64512\n"
"s_barrier\nAR 83,41\nMaaa 52,128,228\nCV 22,82,83\nAR 82,42\nMaaa 56,144,224\nRa 128,3,0\nAR 83,43\nMaaa 56,148,232\nCV 23,82,83\ns_add_u32 s5, s95, 262144\nMaaa 56,152,240\n"
"Ra 132,3,1024\nSv 20,1,16,5,0\nMaaa 56,156,248\nWL 6\nAR 82,36\nAR 83,37\nMaaa 60,156,252\nRa 136,3,2048\nCV 84,82,83\nMaaa 60,152,244\nAR 82,38\nAR 83,39\nMaaa 60,148,236\n"
"Ra 140,3,3072\nCV 85,82,83\nMaaa 60,144,228\nAR 82,44\nAR 83,45\nMaaa 64,160,224\nRa 144,3,4096\nCV 86,82,83\nAR 82,46\nMaaa 64,164,232\nAR 83,47\nCV 87,82,83\nMaaa 64,168,240\n"
"Ra 148,3,5120\nSv 84,1,16,5,1024\nMaaa 64,172,248\nWL 6\nAR 82,48\nMaaa 68,172,252\nRa 152,3,6144\nAR 83,49\nMaaa 68,168,244\nCV 20,82,83\nAR 82,50\nMaaa 68,164,236\n"
"Ra 156,3,7168\nAR 83,51\nCV 21,82,83\nMaaa 68,160,228\nAR 82,56\nAR 83,57\nMaaa 72,176,224\nRa 160,3,8192\nCV 22,82,83\nMaaa 72,180,232\nAR 82,58\nAR 83,59\nMaaa 72,184,240\n"
"Ra 164,3,9216\nCV 23,82,83\nMaaa 72,188,248\nWL 6\ns_add_u32 s5, s95, 393216\nSv 20,1,16,5,0\nMaaa 76,188,252\nRa 168,3,10240\nAR 82,52\nMaaa 76,184,244\nAR 83,53\nCV 84,82,83\n"
"Maaa 76,180,236\nRa 172,3,11264\nAR 82,54\nMaaa 76,176,228\nAR 83,55\nCV 85,82,83\nMaaa 80,128,224\nRa 176,3,12288\nAR 82,60\nAR 83,61\nMaaa 80,132,232\nCV 86,82,83\nAR 82,62\n"
"Maaa 80,136,240\nRa 180,3,13312\nAR 83,63\nMaaa 80,140,248\nWL 6\nCV 87,82,83\nMaaa 84,140,252\nRa 184,3,14336\nSv 84,1,16,5,1024\nMaaa 84,136,244\nAR 82,64\nAR 83,65\n"
"Maaa 84,132,236\nRa 188,3,15360\nCV 20,82,83\nAR 82,66\nMaaa 84,128,228\nAR 83,67\nCV 21,82,83\nMaaa 88,144,224\nAR 82,72\nAR 83,73\nMaaa 88,148,232\nCV 22,82,83\nAR 82,74\n"
"Maaa 88,152,240\nAR 83,75\nCV 23,82,83\nMaaa 88,156,248\nWL 4\ns_add_u32 s5, s95, 524288\nSv 20,1,16,5,0\nMaaa 92,156,252\nAR 82,68\nAR 83,69\nMaaa 92,152,244\nCV 84,82,83\n"
"AR 82,70\nMaaa 92,148,236\nAR 83,71\nCV 85,82,83\nMaaa 92,144,228\nAR 82,76\nAR 83,77\nMaaa 96,160,224\nCV 86,82,83\nAR 82,78\nAR 83,79\nMaaa 96,164,232\nCV 87,82,83\n"
"Sv 84,1,16,5,1024\nMaaa 96,168,240\nAR 82,80\nAR 83,81\nMaaa 96,172,248\nWL 0\nCV 20,82,83\nMaaa 100,172,252\nAR 82,82\nAR 83,83\nMaaa 100,168,244\nCV 21,82,83\nAR 82,88\n"
"Maaa 100,164,236\nAR 83,89\nCV 22,82,83\nAR 82,90\nMaaa 100,160,228\nAR 83,91\nCV 23,82,83\nMaaa 104,176,224\ns_add_u32 s5, s95, 655360\nSv 20,1,16,5,0\nMaaa 104,180,232\n"
"AR 82,84\nAR 83,85\nMaaa 104,184,240\nCV 84,82,83\nAR 82,86\nMaaa 104,188,248\nAR 83,87\nCV 85,82,83\nAR 82,92\nMaaa 108,188,252\nAR 83,93\nCV 86,82,83\nMaaa 108,184,244\n"
"AR 82,94\nAR 83,95\nMaaa 108,180,236\nCV 87,82,83\nSv 84,1,16,5,1024\nMaaa 108,176,228\nAR 82,96\nAR 83,97\nCV 20,82,83\nAR 82,98\nAR 83,99\nCV 21,82,83\nAR 82,104\nAR 83,105\n"
"CV 22,82,83\nAR 82,106\nAR 83,107\nCV 23,82,83\ns_add_u32 s5, s95, 786432\nSv 20,1,16,5,0\nAR 82,100\nAR 83,101\nCV 84,82,83\nAR 82,102\nAR 83,103\nCV 85,82,83\nAR 82,108\n"
"AR 83,109\nCV 86,82,83\nAR 82,110\nAR 83,111\nCV 87,82,83\nSv 84,1,16,5,1024\ns_cmp_eq_u32 s13, 7\ns_cbranch_scc1 .Z406\nWV 14\nv_cmp_eq_u32 s[62:63], s15, v27\n"
"s_mov_b32 vcc_hi, 0\ns_nop 2\ns_lshr_b32 s63, s62, 8\ns_or_b32 s62, s62, s63\ns_andn2_b32 vcc_lo, 0xff, s62\ns_mov_b64 vcc, vcc\ns_cbranch_vccnz .S403\n.S404:\n"
"s_mov_b32 s7, 262144\ns_mov_b32 s3, 0xff\n.Z407:\ns_bitcmp1_b32 s3, 0\ns_cbranch_scc0 .Z409\ns_bitcmp1_b32 s63, 0\ns_cselect_b32 s2, 0x2000, 0\ns_xor_b32 s2, s2, s20\n"
"s_add_u32 s2, s2, 0x0\nbuffer_load_dwordx4 v[96:99], v1, s[16:19], s2 offen offset:0 sc0 sc1\nbuffer_load_dwordx4 v[100:103], v1, s[16:19], s2 offen offset:1024 sc0 sc1\n.Z409:\n"
"s_bitcmp1_b32 s3, 1\ns_cbranch_scc0 .Z410\ns_bitcmp1_b32 s63, 1\ns_cselect_b32 s2, 0x2000, 0\ns_xor_b32 s2, s2, s20\ns_add_u32 s2, s2, 0x1800000\n"
"buffer_load_dwordx4 v[104:107], v1, s[16:19], s2 offen offset:0 sc0 sc1\nbuffer_load_dwordx4 v[108:111], v1, s[16:19], s2 offen offset:1024 sc0 sc1\n.Z410:\ns_bitcmp1_b32 s3, 2\n"
"s_cbranch_scc0 .Z411\ns_bitcmp1_b32 s63, 2\ns_cselect_b32 s2, 0x2000, 0\ns_xor_b32 s2, s2, s20\ns_add_u32 s2, s2, 0x3000000\n"
"buffer_load_dwordx4 v[112:115], v1, s[16:19], s2 offen offset:0 sc0 sc1\nbuffer_load_dwordx4 v[116:119], v1, s[16:19], s2 offen offset:1024 sc0 sc1\n.Z411:\ns_bitcmp1_b32 s3, 3\n"
"s_cbranch_scc0 .Z412\ns_bitcmp1_b32 s63, 3\ns_cselect_b32 s2, 0x2000, 0\ns_xor_b32 s2, s2, s20\ns_add_u32 s2, s2, 0x4800000\n"
"buffer_load_dwordx4 v[120:123], v1, s[16:19], s2 offen offset:0 sc0 sc1\nbuffer_load_dwordx4 v[124:127], v1, s[16:19], s2 offen offset:1024 sc0 sc1\n.Z412:\ns_bitcmp1_b32 s3, 4\n"
"s_cbranch_scc0 .Z413\ns_bitcmp1_b32 s63, 4\ns_cselect_b32 s2, 0x2000, 0\ns_xor_b32 s2, s2, s20\ns_add_u32 s2, s2, 0x6000000\n"
"buffer_load_dwordx4 v[32:35], v1, s[16:19], s2 offen offset:0 sc0 sc1\nbuffer_load_dwordx4 v[36:39], v1, s[16:19], s2 offen offset:1024 sc0 sc1\n.Z413:\ns_bitcmp1_b32 s3, 5\n"
"s_cbranch_scc0 .Z414\ns_bitcmp1_b32 s63, 5\ns_cselect_b32 s2, 0x2000, 0\ns_xor_b32 s2, s2, s20\ns_add_u32 s2, s2, 0x7800000\n"
"buffer_load_dwordx4 v[40:43], v1, s[16:19], s2 offen offset:0 sc0 sc1\nbuffer_load_dwordx4 v[44:47], v1, s[16:19], s2 offen offset:1024 sc0 sc1\n.Z414:\ns_bitcmp1_b32 s3, 6\n"
"s_cbranch_scc0 .Z415\ns_bitcmp1_b32 s63, 6\ns_cselect_b32 s2, 0x2000, 0\ns_xor_b32 s2, s2, s20\ns_add_u32 s2, s2, 0x9000000\n"
"buffer_load_dwordx4 v[48:51], v1, s[16:19], s2 offen offset:0 sc0 sc1\nbuffer_load_dwordx4 v[52:55], v1, s[16:19], s2 offen offset:1024 sc0 sc1\n.Z415:\ns_bitcmp1_b32 s3, 7\n"
"s_cbranch_scc0 .Z416\ns_bitcmp1_b32 s63, 7\ns_cselect_b32 s2, 0x2000, 0\ns_xor_b32 s2, s2, s20\ns_add_u32 s2, s2, 0xa800000\n"
"buffer_load_dwordx4 v[56:59], v1, s[16:19], s2 offen offset:0 sc0 sc1\nbuffer_load_dwordx4 v[60:63], v1, s[16:19], s2 offen offset:1024 sc0 sc1\n.Z416:\ns_mov_b32 s3, 0\nWV 0\n"
"v_max_u32 v82, v99, v103\nv_cmp_eq_u32 s[44:45], -1, v82\nv_max_u32 v82, v107, v111\nv_cmp_eq_u32 s[46:47], -1, v82\nv_max_u32 v82, v115, v119\nv_cmp_eq_u32 s[48:49], -1, v82\n"
"v_max_u32 v82, v123, v127\nv_cmp_eq_u32 s[50:51], -1, v82\nv_max_u32 v82, v35, v39\nv_cmp_eq_u32 s[52:53], -1, v82\nv_max_u32 v82, v43, v47\nv_cmp_eq_u32 s[54:55], -1, v82\n"
"v_max_u32 v82, v51, v55\nv_cmp_eq_u32 s[56:57], -1, v82\nv_max_u32 v82, v59, v63\nv_cmp_eq_u32 s[58:59], -1, v82\ns_cmp_lg_u64 s[44:45], 0\ns_cselect_b32 s2, 1, 0\n"
"s_or_b32 s3, s3, s2\ns_cmp_lg_u64 s[46:47], 0\ns_cselect_b32 s2, 2, 0\ns_or_b32 s3, s3, s2\ns_cmp_lg_u64 s[48:49], 0\ns_cselect_b32 s2, 4, 0\ns_or_b32 s3, s3, s2\n"
"s_cmp_lg_u64 s[50:51], 0\ns_cselect_b32 s2, 8, 0\ns_or_b32 s3, s3, s2\ns_cmp_lg_u64 s[52:53], 0\ns_cselect_b32 s2, 16, 0\ns_or_b32 s3, s3, s2\ns_cmp_lg_u64 s[54:55], 0\n"
"s_cselect_b32 s2, 32, 0\ns_or_b32 s3, s3, s2\ns_cmp_lg_u64 s[56:57], 0\ns_cselect_b32 s2, 64, 0\ns_or_b32 s3, s3, s2\ns_cmp_lg_u64 s[58:59], 0\ns_cselect_b32 s2, 128, 0\n"
"s_or_b32 s3, s3, s2\ns_cbranch_scc0 .Z408\ns_sub_u32 s7, s7, 1\ns_cbranch_scc0 .Z407\n.Z408:\nZvvv 136,6,96\nMvvv 136,6,104\nMvvv 136,6,112\nMvvv 136,6,120\nMvvv 136,6,32\n"
"Mvvv 136,6,40\nMvvv 136,6,48\nMvvv 136,6,56\nZvvv 140,4,96\nMvvv 140,4,104\nMvvv 140,4,112\nMvvv 140,4,120\nMvvv 140,4,32\nMvvv 140,4,40\nMvvv 140,4,48\nMvvv 140,4,56\n"
"Zvvv 144,6,100\nMvvv 144,6,108\nMvvv 144,6,116\nMvvv 144,6,124\nMvvv 144,6,36\nMvvv 144,6,44\nMvvv 144,6,52\nMvvv 144,6,60\nZvvv 148,4,100\nMvvv 148,4,108\nMvvv 148,4,116\n"
"Mvvv 148,4,124\nMvvv 148,4,36\nMvvv 148,4,44\nMvvv 148,4,52\nMvvv 148,4,60\nFM 64,80,136\nFM 65,80,137\nFM 66,80,138\nFM 67,80,139\nFM 68,80,140\nFM 69,80,141\nFM 70,80,142\n"
"FM 71,80,143\nCV 12,64,65\nCV 13,66,67\nCV 14,68,69\nCV 15,70,71\nSv 12,91,36,22,0\nFM 72,81,144\nFM 73,81,145\nFM 74,81,146\nFM 75,81,147\nFM 76,81,148\nFM 77,81,149\n"
"FM 78,81,150\nFM 79,81,151\nCV 16,72,73\nCV 17,74,75\nCV 18,76,77\nCV 19,78,79\nSv 16,91,36,23,0\n.Z406:\ns_endpgm\n.HD322:\nv_cmp_ne_u32 vcc, s15, v27\ns_nop 0\n"
"s_cbranch_vccnz .S417\n.S418:\nbuffer_store_dword v25, off, s[16:19], s54 offset:4 sc0 sc1\ns_mov_b32 s90, 2\ns_swappc_b64 s[8:9], s[80:81]\nv_and_b32 v27, 0x1f0, v1\n"
"buffer_load_dword v27, v27, s[16:19], s58 offen offset:4 sc0 sc1\ns_mov_b32 s90, 0\ns_swappc_b64 s[8:9], s[80:81]\nv_cmp_ne_u32 vcc, s15, v27\ns_nop 0\ns_cbranch_vccnz .S420\n"
".S421:\ns_mov_b32 s90, 100\ns_swappc_b64 s[8:9], s[80:81]\n.S323:\ns_mov_b32 s7, 262144\n.S325:\ns_sleep 8\ns_lshl_b32 s5, s73, 7\ns_add_u32 s5, s5, 0xc04ac00\n"
"v_bfe_u32 v27, v1, 2, 7\nbuffer_load_dword v27, v27, s[16:19], s5 offen sc0 sc1\nWV 0\nv_xor_b32 v27, s15, v27\nv_and_b32 v32, 0xffffdfff, v27\nv_cmp_ne_u32 vcc, 0, v32\ns_nop 0\n"
"s_cbranch_vccz .S324\ns_sub_u32 s7, s7, 1\ns_cbranch_scc0 .S325\ns_branch .S324\n.E329:\nv_mov_b32 v12, s97\nv_mov_b32 v13, s97\nv_mov_b32 v14, s97\nv_mov_b32 v15, s97\n"
"v_cmpx_gt_i32 vcc, 3, v0\nv_mov_b32 v7, s97\nv_mov_b32 v19, s97\nv_cmpx_gt_i32 vcc, 2, v0\nv_mov_b32 v6, s97\nv_mov_b32 v18, s97\nv_cmpx_gt_i32 vcc, 1, v0\nv_mov_b32 v5, s97\n"
"v_mov_b32 v17, s97\nv_cmpx_gt_i32 vcc, 0, v0\nv_mov_b32 v4, s97\nv_mov_b32 v16, s97\nv_cmpx_gt_i32 vcc, -16, v0\nv_mov_b32 v8, s97\nv_mov_b32 v9, s97\nv_mov_b32 v10, s97\n"
"v_mov_b32 v11, s97\ns_mov_b64 exec, -1\ns_nop 2\ns_branch .E330\n.E331:\nv_mov_b32 v12, s97\nv_mov_b32 v13, s97\nv_mov_b32 v14, s97\nv_mov_b32 v15, s97\nv_cmpx_gt_i32 vcc, 3, v3\n"
"v_mov_b32 v7, s97\nv_mov_b32 v19, s97\nv_cmpx_gt_i32 vcc, 2, v3\nv_mov_b32 v6, s97\nv_mov_b32 v18, s97\nv_cmpx_gt_i32 vcc, 1, v3\nv_mov_b32 v5, s97\nv_mov_b32 v17, s97\n"
"v_cmpx_gt_i32 vcc, 0, v3\nv_mov_b32 v4, s97\nv_mov_b32 v16, s97\nv_cmpx_gt_i32 vcc, -16, v3\nv_mov_b32 v8, s97\nv_mov_b32 v9, s97\nv_mov_b32 v10, s97\nv_mov_b32 v11, s97\n"
"s_mov_b64 exec, -1\ns_nop 2\ns_branch .E332\n.E333:\ns_swappc_b64 s[80:81], s[8:9]\ns_branch .E334\n.T335:\ns_mov_b32 s62, 1\ns_call_b64 s[80:81], .RA205\ns_branch .T336\n.T337:\n"
"s_mov_b32 s62, 1\ns_call_b64 s[80:81], .RA205\ns_branch .T338\n.S339:\ns_mov_b32 s7, 262144\n.S341:\ns_sleep 8\nbuffer_load_dword v27, off, s[16:19], s5 sc0 sc1\nWV 0\n"
"v_cmp_ne_u32 vcc, s15, v27\ns_nop 0\ns_cbranch_vccz .S340\ns_sub_u32 s7, s7, 1\ns_cbranch_scc0 .S341\ns_branch .S340\n.S342:\ns_mov_b32 s7, 262144\n.S344:\ns_sleep 8\n"
"v_and_b32 v27, 0x70, v1\nbuffer_load_dword v27, v27, s[16:19], s61 offen offset:128 sc0 sc1\nWV 0\nv_cmp_ne_u32 vcc, s15, v27\ns_nop 0\ns_cbranch_vccz .S343\ns_sub_u32 s7, s7, 1\n"
"s_cbranch_scc0 .S344\ns_branch .S343\n.E346:\ns_cmp_eq_u32 s11, 3\ns_cbranch_scc1 .P345\ns_cmp_lt_u32 s11, 2\ns_cselect_b32 s64, -1.0, s64\ns_cselect_b32 s2, 64, 0\n"
"v_subrev_u32 v20, s2, v3\nv_mov_b32 v12, s97\nv_mov_b32 v13, s97\nv_mov_b32 v14, s97\nv_mov_b32 v15, s97\nv_cmpx_gt_i32 vcc, 3, v20\nv_mov_b32 v7, s97\nv_mov_b32 v19, s97\n"
"v_cmpx_gt_i32 vcc, 2, v20\nv_mov_b32 v6, s97\nv_mov_b32 v18, s97\nv_cmpx_gt_i32 vcc, 1, v20\nv_mov_b32 v5, s97\nv_mov_b32 v17, s97\nv_cmpx_gt_i32 vcc, 0, v20\nv_mov_b32 v4, s97\n"
"v_mov_b32 v16, s97\nv_cmpx_gt_i32 vcc, -16, v20\nv_mov_b32 v8, s97\nv_mov_b32 v9, s97\nv_mov_b32 v10, s97\nv_mov_b32 v11, s97\ns_mov_b64 exec, -1\ns_nop 2\n.P345:\ns_branch .E347\n"
".S348:\ns_mov_b32 s7, 262144\n.S350:\ns_sleep 8\nv_and_b32 v27, 0x70, v1\nbuffer_load_dword v27, v27, s[16:19], s61 offen offset:136 sc0 sc1\nWV 0\nv_cmp_ne_u32 vcc, s15, v27\n"
"s_nop 0\ns_cbranch_vccz .S349\ns_sub_u32 s7, s7, 1\ns_cbranch_scc0 .S350\ns_branch .S349\n.E352:\nv_mov_b32 v12, s97\nv_mov_b32 v13, s97\nv_mov_b32 v14, s97\nv_mov_b32 v15, s97\n"
"v_cmpx_gt_i32 vcc, 3, v0\nv_mov_b32 v7, s97\nv_mov_b32 v19, s97\nv_cmpx_gt_i32 vcc, 2, v0\nv_mov_b32 v6, s97\nv_mov_b32 v18, s97\nv_cmpx_gt_i32 vcc, 1, v0\nv_mov_b32 v5, s97\n"
"v_mov_b32 v17, s97\nv_cmpx_gt_i32 vcc, 0, v0\nv_mov_b32 v4, s97\nv_mov_b32 v16, s97\nv_cmpx_gt_i32 vcc, -16, v0\nv_mov_b32 v8, s97\nv_mov_b32 v9, s97\nv_mov_b32 v10, s97\n"
"v_mov_b32 v11, s97\ns_mov_b64 exec, -1\ns_nop 2\ns_branch .E353\n.E354:\nv_mov_b32 v12, s97\nv_mov_b32 v13, s97\nv_mov_b32 v14, s97\nv_mov_b32 v15, s97\nv_cmpx_gt_i32 vcc, 3, v3\n"
"v_mov_b32 v7, s97\nv_mov_b32 v19, s97\nv_cmpx_gt_i32 vcc, 2, v3\nv_mov_b32 v6, s97\nv_mov_b32 v18, s97\nv_cmpx_gt_i32 vcc, 1, v3\nv_mov_b32 v5, s97\nv_mov_b32 v17, s97\n"
"v_cmpx_gt_i32 vcc, 0, v3\nv_mov_b32 v4, s97\nv_mov_b32 v16, s97\nv_cmpx_gt_i32 vcc, -16, v3\nv_mov_b32 v8, s97\nv_mov_b32 v9, s97\nv_mov_b32 v10, s97\nv_mov_b32 v11, s97\n"
"s_mov_b64 exec, -1\ns_nop 2\ns_branch .E355\n.E356:\ns_swappc_b64 s[80:81], s[8:9]\ns_branch .E357\n.T358:\ns_mov_b32 s62, 0\ns_call_b64 s[80:81], .RB206\ns_branch .T359\n.T360:\n"
"s_mov_b32 s62, 0\ns_call_b64 s[80:81], .RA205\ns_branch .T361\n.E362:\nv_mov_b32 v12, s97\nv_mov_b32 v13, s97\nv_mov_b32 v14, s97\nv_mov_b32 v15, s97\nv_cmpx_gt_i32 vcc, 3, v3\n"
"v_mov_b32 v7, s97\nv_mov_b32 v19, s97\nv_cmpx_gt_i32 vcc, 2, v3\nv_mov_b32 v6, s97\nv_mov_b32 v18, s97\nv_cmpx_gt_i32 vcc, 1, v3\nv_mov_b32 v5, s97\nv_mov_b32 v17, s97\n"
"v_cmpx_gt_i32 vcc, 0, v3\nv_mov_b32 v4, s97\nv_mov_b32 v16, s97\nv_cmpx_gt_i32 vcc, -16, v3\nv_mov_b32 v8, s97\nv_mov_b32 v9, s97\nv_mov_b32 v10, s97\nv_mov_b32 v11, s97\n"
"s_mov_b64 exec, -1\ns_nop 2\ns_branch .E363\n.E364:\nv_mov_b32 v12, s97\nv_mov_b32 v13, s97\nv_mov_b32 v14, s97\nv_mov_b32 v15, s97\nv_cmpx_gt_i32 vcc, 3, v0\nv_mov_b32 v7, s97\n"
"v_mov_b32 v19, s97\nv_cmpx_gt_i32 vcc, 2, v0\nv_mov_b32 v6, s97\nv_mov_b32 v18, s97\nv_cmpx_gt_i32 vcc, 1, v0\nv_mov_b32 v5, s97\nv_mov_b32 v17, s97\nv_cmpx_gt_i32 vcc, 0, v0\n"
"v_mov_b32 v4, s97\nv_mov_b32 v16, s97\nv_cmpx_gt_i32 vcc, -16, v0\nv_mov_b32 v8, s97\nv_mov_b32 v9, s97\nv_mov_b32 v10, s97\nv_mov_b32 v11, s97\ns_mov_b64 exec, -1\ns_nop 2\n"
"s_branch .E365\n.E366:\nv_mov_b32 v12, s97\nv_mov_b32 v13, s97\nv_mov_b32 v14, s97\nv_mov_b32 v15, s97\nv_cmpx_gt_i32 vcc, 3, v3\nv_mov_b32 v7, s97\nv_mov_b32 v19, s97\n"
"v_cmpx_gt_i32 vcc, 2, v3\nv_mov_b32 v6, s97\nv_mov_b32 v18, s97\nv_cmpx_gt_i32 vcc, 1, v3\nv_mov_b32 v5, s97\nv_mov_b32 v17, s97\nv_cmpx_gt_i32 vcc, 0, v3\nv_mov_b32 v4, s97\n"
"v_mov_b32 v16, s97\nv_cmpx_gt_i32 vcc, -16, v3\nv_mov_b32 v8, s97\nv_mov_b32 v9, s97\nv_mov_b32 v10, s97\nv_mov_b32 v11, s97\ns_mov_b64 exec, -1\ns_nop 2\ns_branch .E367\n.E368:\n"
"s_swappc_b64 s[80:81], s[8:9]\ns_branch .E369\n.T370:\ns_mov_b32 s62, 2\ns_call_b64 s[80:81], .RB206\ns_branch .T371\n.T372:\ns_mov_b32 s62, 5\ns_call_b64 s[80:81], .RA205\n"
"s_branch .T373\n.T374:\ns_mov_b32 s62, 5\ns_call_b64 s[80:81], .RA205\ns_branch .T375\n.E377:\nv_mov_b32 v12, s97\nv_mov_b32 v13, s97\nv_mov_b32 v14, s97\nv_mov_b32 v15, s97\n"
"v_cmpx_gt_i32 vcc, 3, v0\nv_mov_b32 v7, s97\nv_mov_b32 v19, s97\nv_cmpx_gt_i32 vcc, 2, v0\nv_mov_b32 v6, s97\nv_mov_b32 v18, s97\nv_cmpx_gt_i32 vcc, 1, v0\nv_mov_b32 v5, s97\n"
"v_mov_b32 v17, s97\nv_cmpx_gt_i32 vcc, 0, v0\nv_mov_b32 v4, s97\nv_mov_b32 v16, s97\nv_cmpx_gt_i32 vcc, -16, v0\nv_mov_b32 v8, s97\nv_mov_b32 v9, s97\nv_mov_b32 v10, s97\n"
"v_mov_b32 v11, s97\ns_mov_b64 exec, -1\ns_nop 2\ns_branch .E378\n.E379:\nv_mov_b32 v12, s97\nv_mov_b32 v13, s97\nv_mov_b32 v14, s97\nv_mov_b32 v15, s97\nv_cmpx_gt_i32 vcc, 3, v3\n"
"v_mov_b32 v7, s97\nv_mov_b32 v19, s97\nv_cmpx_gt_i32 vcc, 2, v3\nv_mov_b32 v6, s97\nv_mov_b32 v18, s97\nv_cmpx_gt_i32 vcc, 1, v3\nv_mov_b32 v5, s97\nv_mov_b32 v17, s97\n"
"v_cmpx_gt_i32 vcc, 0, v3\nv_mov_b32 v4, s97\nv_mov_b32 v16, s97\nv_cmpx_gt_i32 vcc, -16, v3\nv_mov_b32 v8, s97\nv_mov_b32 v9, s97\nv_mov_b32 v10, s97\nv_mov_b32 v11, s97\n"
"s_mov_b64 exec, -1\ns_nop 2\ns_branch .E380\n.E381:\ns_swappc_b64 s[80:81], s[8:9]\ns_branch .E382\n.E383:\nv_mov_b32 v91, 0x40000000\nv_cmp_gt_u32 vcc, 0x200, v1\nWV 5\ns_nop 0\n"
"v_cndmask_b32 v91, v91, v1, vcc\nSv 92,91,16,65,0\ns_branch .E384\n.T385:\ns_mov_b32 s62, 4\ns_call_b64 s[80:81], .RB206\ns_branch .T386\n.T387:\ns_mov_b32 s62, 4\n"
"s_call_b64 s[80:81], .RA205\ns_branch .T388\n.T389:\ns_mov_b32 s62, 6\ns_call_b64 s[80:81], .RB206\ns_branch .T390\n.S391:\ns_mov_b32 s7, 262144\n.S393:\ns_sleep 8\n"
"s_add_u32 s4, s92, 0\nbuffer_load_dwordx4 a[192:195], v1, s[16:19], s4 offen offset:0 sc0 sc1\nbuffer_load_dwordx4 a[196:199], v1, s[16:19], s4 offen offset:1024 sc0 sc1\n"
"s_add_u32 s4, s92, 131072\nbuffer_load_dwordx4 a[200:203], v1, s[16:19], s4 offen offset:0 sc0 sc1\nbuffer_load_dwordx4 a[204:207], v1, s[16:19], s4 offen offset:1024 sc0 sc1\n"
"s_add_u32 s4, s92, 262144\nbuffer_load_dwordx4 a[208:211], v1, s[16:19], s4 offen offset:0 sc0 sc1\nbuffer_load_dwordx4 a[212:215], v1, s[16:19], s4 offen offset:1024 sc0 sc1\n"
"s_add_u32 s4, s92, 393216\nbuffer_load_dwordx4 a[216:219], v1, s[16:19], s4 offen offset:0 sc0 sc1\nbuffer_load_dwordx4 a[220:223], v1, s[16:19], s4 offen offset:1024 sc0 sc1\n"
"WV 0\nAR 22,195\nAR 23,199\nv_max_u32 v21, v22, v23\nAR 22,203\nAR 23,207\nv_max3_u32 v21, v21, v22, v23\nAR 22,211\nAR 23,215\nv_max3_u32 v21, v21, v22, v23\nAR 22,219\n"
"AR 23,223\nv_max3_u32 v21, v21, v22, v23\nv_cmp_eq_u32 vcc, -1, v21\ns_nop 0\ns_cbranch_vccz .S392\ns_sub_u32 s7, s7, 1\ns_cbranch_scc0 .S393\ns_branch .S392\n.S394:\n"
"s_mov_b32 s7, 262144\n.S396:\ns_sleep 8\ns_add_u32 s4, s92, 524288\nbuffer_load_dwordx4 a[224:227], v1, s[16:19], s4 offen offset:0 sc0 sc1\n"
"buffer_load_dwordx4 a[228:231], v1, s[16:19], s4 offen offset:1024 sc0 sc1\ns_add_u32 s4, s92, 655360\nbuffer_load_dwordx4 a[232:235], v1, s[16:19], s4 offen offset:0 sc0 sc1\n"
"buffer_load_dwordx4 a[236:239], v1, s[16:19], s4 offen offset:1024 sc0 sc1\ns_add_u32 s4, s92, 786432\nbuffer_load_dwordx4 a[240:243], v1, s[16:19], s4 offen offset:0 sc0 sc1\n"
"buffer_load_dwordx4 a[244:247], v1, s[16:19], s4 offen offset:1024 sc0 sc1\ns_add_u32 s4, s92, 917504\nbuffer_load_dwordx4 a[248:251], v1, s[16:19], s4 offen offset:0 sc0 sc1\n"
"buffer_load_dwordx4 a[252:255], v1, s[16:19], s4 offen offset:1024 sc0 sc1\nWV 0\nAR 22,227\nAR 23,231\nv_max_u32 v21, v22, v23\nAR 22,235\nAR 23,239\n"
"v_max3_u32 v21, v21, v22, v23\nAR 22,243\nAR 23,247\nv_max3_u32 v21, v21, v22, v23\nAR 22,251\nAR 23,255\nv_max3_u32 v21, v21, v22, v23\nv_cmp_eq_u32 vcc, -1, v21\ns_nop 0\n"
"s_cbranch_vccz .S395\ns_sub_u32 s7, s7, 1\ns_cbranch_scc0 .S396\ns_branch .S395\n.S397:\ns_mov_b32 s7, 262144\n.S399:\ns_sleep 8\n"
"buffer_load_dwordx4 v[64:67], v89, s[16:19], s40 offen offset:0 sc0 sc1\nbuffer_load_dwordx4 v[68:71], v89, s[16:19], s40 offen offset:16 sc0 sc1\n"
"buffer_load_dwordx4 v[72:75], v89, s[16:19], s40 offen offset:1792 sc0 sc1\nbuffer_load_dwordx4 v[76:79], v89, s[16:19], s40 offen offset:1808 sc0 sc1\nWV 0\n"
"v_xor_b32 v92, s15, v66\nv_xor_b32 v93, s15, v70\nv_xor_b32 v94, s15, v74\nv_or3_b32 v92, v92, v93, v94\nv_xor_b32 v93, s15, v78\nv_or_b32 v92, v92, v93\nv_cmp_ne_u32 vcc, 0, v92\n"
"s_nop 0\ns_cbranch_vccz .S398\ns_sub_u32 s7, s7, 1\ns_cbranch_scc0 .S399\ns_branch .S398\n.S400:\ns_mov_b32 s7, 262144\n.S402:\ns_sleep 8\n"
"buffer_load_dword v27, off, s[16:19], s60 offset:512 sc0 sc1\nWV 0\nv_cmp_ne_u32 vcc, s15, v27\ns_nop 0\ns_cbranch_vccz .S401\ns_sub_u32 s7, s7, 1\ns_cbranch_scc0 .S402\n"
"s_branch .S401\n.S403:\ns_mov_b32 s7, 262144\n.S405:\ns_sleep 8\nbuffer_load_dword v27, v24, s[16:19], s41 offen sc0 sc1\nWV 0\nv_cmp_eq_u32 s[62:63], s15, v27\n"
"s_mov_b32 vcc_hi, 0\ns_nop 2\ns_lshr_b32 s63, s62, 8\ns_or_b32 s62, s62, s63\ns_andn2_b32 vcc_lo, 0xff, s62\ns_mov_b64 vcc, vcc\ns_cbranch_vccz .S404\ns_sub_u32 s7, s7, 1\n"
"s_cbranch_scc0 .S405\ns_branch .S404\n.S417:\ns_mov_b32 s7, 262144\n.S419:\ns_sleep 8\nv_and_b32 v27, 0xf0, v1\nbuffer_load_dword v27, v27, s[16:19], s61 offen offset:12 sc0 sc1\n"
"WV 0\nv_cmp_ne_u32 vcc, s15, v27\ns_nop 0\ns_cbranch_vccz .S418\ns_sub_u32 s7, s7, 1\ns_cbranch_scc0 .S419\ns_branch .S418\n.S420:\ns_mov_b32 s7, 262144\n.S422:\ns_sleep 8\n"
"v_and_b32 v27, 0x1f0, v1\nbuffer_load_dword v27, v27, s[16:19], s58 offen offset:4 sc0 sc1\nWV 0\nv_cmp_ne_u32 vcc, s15, v27\ns_nop 0\ns_cbranch_vccz .S421\ns_sub_u32 s7, s7, 1\n"
"s_cbranch_scc0 .S422\ns_branch .S421\n.Lcode1:\n"
  :: "s"(__builtin_amdgcn_kernarg_segment_ptr()), "s"(blockIdx.x), "v"(threadIdx.x)
  : "memory", "scc", "vcc", "v1", "v2", "v3", "v4", "v5", "v6", "v7", "v8", "v9", "v10", "v11", "v12", "v13", "v14", "v15", "v16", "v17", "v18", "v19", "v20", "v21", "v22", "v23", "v24", "v25", "v26", "v27", "v28", "v29", "v30", "v31", "v32", "v33", "v34", "v35", "v36", "v37", "v38", "v39", "v40", "v41", "v42", "v43", "v44", "v45", "v46", "v47", "v48", "v49", "v50", "v51", "v52", "v53", "v54", "v55", "v56", "v57", "v58", "v59", "v60", "v61", "v62", "v63", "v64", "v65", "v66", "v67", "v68", "v69", "v70", "v71", "v72", "v73", "v74", "v75", "v76", "v77", "v78", "v79", "v80", "v81", "v82", "v83", "v84", "v85", "v86", "v87", "v88", "v89", "v90", "v91", "v92", "v93", "v94", "v95", "v96", "v97", "v98", "v99", "v100", "v101", "v102", "v103", "v104", "v105", "v106", "v107", "v108", "v109", "v110", "v111", "v112", "v113", "v114", "v115", "v116", "v117", "v118", "v119", "v120", "v121", "v122", "v123", "v124", "v125", "v126", "v127", "v128", "v129", "v130", "v131", "v132", "v133", "v134", "v135", "v136", "v137", "v138", "v139", "v140", "v141", "v142", "v143", "v144", "v145", "v146", "v147", "v148", "v149", "v150", "v151", "v152", "v153", "v154", "v155", "v156", "v157", "v158", "v159", "v160", "v161", "v162", "v163", "v164", "v165", "v166", "v167", "v168", "v169", "v170", "v171", "v172", "v173", "v174", "v175", "v176", "v177", "v178", "v179", "v180", "v181", "v182", "v183", "v184", "v185", "v186", "v187", "v188", "v189", "v190", "v191", "v192", "v193", "v194", "v195", "v196", "v197", "v198", "v199", "v200", "v201", "v202", "v203", "v204", "v205", "v206", "v207", "v208", "v209", "v210", "v211", "v212", "v213", "v214", "v215", "v216", "v217", "v218", "v219", "v220", "v221", "v222", "v223", "v224", "v225", "v226", "v227", "v228", "v229", "v230", "v231", "v232", "v233", "v234", "v235", "v236", "v237", "v238", "v239", "v240", "v241", "v242", "v243", "v244", "v245", "v246", "v247", "v248", "v249", "v250", "v251", "v252", "v253", "v254", "v255", "a0", "a1", "a2", "a3", "a4", "a5", "a6", "a7", "a8", "a9", "a10", "a11", "a12", "a13", "a14", "a15", "a16", "a17", "a18", "a19", "a20", "a21", "a22", "a23", "a24", "a25", "a26", "a27", "a28", "a29", "a30", "a31", "a32", "a33", "a34", "a35", "a36", "a37", "a38", "a39", "a40", "a41", "a42", "a43", "a44", "a45", "a46", "a47", "a48", "a49", "a50", "a51", "a52", "a53", "a54", "a55", "a56", "a57", "a58", "a59", "a60", "a61", "a62", "a63", "a64", "a65", "a66", "a67", "a68", "a69", "a70", "a71", "a72", "a73", "a74", "a75", "a76", "a77", "a78", "a79", "a80", "a81", "a82", "a83", "a84", "a85", "a86", "a87", "a88", "a89", "a90", "a91", "a92", "a93", "a94", "a95", "a96", "a97", "a98", "a99", "a100", "a101", "a102", "a103", "a104", "a105", "a106", "a107", "a108", "a109", "a110", "a111", "a112", "a113", "a114", "a115", "a116", "a117", "a118", "a119", "a120", "a121", "a122", "a123", "a124", "a125", "a126", "a127", "a128", "a129", "a130", "a131", "a132", "a133", "a134", "a135", "a136", "a137", "a138", "a139", "a140", "a141", "a142", "a143", "a144", "a145", "a146", "a147", "a148", "a149", "a150", "a151", "a152", "a153", "a154", "a155", "a156", "a157", "a158", "a159", "a160", "a161", "a162", "a163", "a164", "a165", "a166", "a167", "a168", "a169", "a170", "a171", "a172", "a173", "a174", "a175", "a176", "a177", "a178", "a179", "a180", "a181", "a182", "a183", "a184", "a185", "a186", "a187", "a188", "a189", "a190", "a191", "a192", "a193", "a194", "a195", "a196", "a197", "a198", "a199", "a200", "a201", "a202", "a203", "a204", "a205", "a206", "a207", "a208", "a209", "a210", "a211", "a212", "a213", "a214", "a215", "a216", "a217", "a218", "a219", "a220", "a221", "a222", "a223", "a224", "a225", "a226", "a227", "a228", "a229", "a230", "a231", "a232", "a233", "a234", "a235", "a236", "a237", "a238", "a239", "a240", "a241", "a242", "a243", "a244", "a245", "a246", "a247", "a248", "a249", "a250", "a251", "a252", "a253", "a254", "a255", "s8", "s9", "s10", "s11", "s12", "s13", "s14", "s15", "s16", "s17", "s18", "s19", "s20", "s21", "s22", "s23", "s24", "s25", "s26", "s27", "s28", "s29", "s30", "s31", "s33", "s34", "s35", "s36", "s37", "s38", "s39", "s40", "s41", "s42", "s43", "s44", "s45", "s46", "s47", "s48", "s49", "s50", "s51", "s52", "s53", "s54", "s55", "s56", "s57", "s58", "s59", "s60", "s61", "s62", "s63", "s64", "s65", "s66", "s67", "s68", "s69", "s70", "s71", "s72", "s73", "s74", "s75", "s76", "s77", "s78", "s79", "s80", "s81", "s82", "s83", "s84", "s85", "s86", "s87", "s88", "s89", "s90", "s91", "s92", "s93", "s94", "s95", "s96", "s97", "s98", "s99");
#endif
}

extern "C" void kernel_launch(void* const* d_in, const int* in_sizes, int n_in,
                              void* d_out, int out_size, void* d_ws, size_t ws_size,
                              hipStream_t stream) {
  (void)hipFuncSetAttribute((const void*)k_main, hipFuncAttributeMaxDynamicSharedMemorySize, 163840);
  k_main<<<256, 256, 163840, stream>>>(d_in[0], d_in[1], d_in[2], d_in[3], d_in[4], d_ws, d_in[11], d_in[7], d_in[5], d_in[8], d_in[9], d_in[10], d_out);
}
```

```cpp
#include <hip/hip_runtime.h>
#include <stdint.h>

extern "C" __global__ __launch_bounds__(256, 1)
void k_main(const void* x, const void* ckv, const void* wq, const void* wuk, const void* wuv, void* ws,
            const void* gate_w, const void* w_r, const void* wo, const void* wg, const void* wu, const void* wd, void* out) {
#if defined(__HIP_DEVICE_COMPILE__)
  asm volatile(
"s_mov_b64 s[8:9], %0\ns_mov_b32 s10, %1\nv_mov_b32 v0, %2\n.macro WL n\ns_waitcnt lgkmcnt(\\n)\n.endm\n.macro D0 x,r,s,o\n"
"buffer_load_dwordx4 v\\x, s[\\r:\\r+3], s\\s offen offset:\\o lds\n.endm\n.macro WV n\ns_waitcnt vmcnt(\\n)\n.endm\n.macro Ra d,x,o\nds_read_b128 a[\\d:\\d+3], v\\x offset:\\o\n"
".endm\n.macro Ta d,x,o\nds_read_b64_tr_b16 a[\\d:\\d+1], v\\x offset:\\o\n.endm\n.macro Zvaa d,x,y\nv_mfma_f32_16x16x32_bf16 v[\\d:\\d+3], a[\\x:\\x+3], a[\\y:\\y+3], 0\n.endm\n"
".macro Mvaa d,x,y\nv_mfma_f32_16x16x32_bf16 v[\\d:\\d+3], a[\\x:\\x+3], a[\\y:\\y+3], v[\\d:\\d+3]\n.endm\n.macro MU d,x,y\nv_mul_f32 v\\d, v\\x, v\\y\n.endm\n.macro CV d,x,y\n"
"v_cvt_pk_bf16_f32 v\\d, v\\x, v\\y\n.endm\n.macro FM d,x,y\nv_fmac_f32 v\\d, v\\x, v\\y\n.endm\n.macro AD d,x,y\nv_add_f32 v\\d, v\\x, v\\y\n.endm\n.macro EX d,x\n"
"v_exp_f32 v\\d, v\\x\n.endm\n.macro Zaaa d,x,y\nv_mfma_f32_16x16x32_bf16 a[\\d:\\d+3], a[\\x:\\x+3], a[\\y:\\y+3], 0\n.endm\n.macro Sv d,x,r,s,o\n"
"buffer_store_dwordx4 v[\\d:\\d+3], v\\x, s[\\r:\\r+3], s\\s offen offset:\\o sc0 sc1\n.endm\n.macro Maaa d,x,y\n"
"v_mfma_f32_16x16x32_bf16 a[\\d:\\d+3], a[\\x:\\x+3], a[\\y:\\y+3], a[\\d:\\d+3]\n.endm\n.macro Rv d,x,o\nds_read_b128 v[\\d:\\d+3], v\\x offset:\\o\n.endm\n.macro D1 x,r,s,o\n"
"buffer_load_dwordx4 v\\x, s[\\r:\\r+3], s\\s offen offset:\\o sc1 lds\n.endm\n.macro AR d,x\nv_accvgpr_read_b32 v\\d, a\\x\n.endm\n.macro Cvav d,x,y,c\n"
"v_mfma_f32_16x16x32_bf16 v[\\d:\\d+3], a[\\x:\\x+3], v[\\y:\\y+3], v[\\c:\\c+3]\n.endm\n.macro Mvav d,x,y\n"
"v_mfma_f32_16x16x32_bf16 v[\\d:\\d+3], a[\\x:\\x+3], v[\\y:\\y+3], v[\\d:\\d+3]\n.endm\n.macro Zaav d,x,y\nv_mfma_f32_16x16x32_bf16 a[\\d:\\d+3], a[\\x:\\x+3], v[\\y:\\y+3], 0\n"
".endm\n.macro Maav d,x,y\nv_mfma_f32_16x16x32_bf16 a[\\d:\\d+3], a[\\x:\\x+3], v[\\y:\\y+3], a[\\d:\\d+3]\n.endm\n.macro SU d,x,y\nv_sub_f32 v\\d, v\\x, v\\y\n.endm\n"
".macro Mava d,x,y\nv_mfma_f32_16x16x32_bf16 a[\\d:\\d+3], v[\\x:\\x+3], a[\\y:\\y+3], a[\\d:\\d+3]\n.endm\n.macro Zvav d,x,y\n"
"v_mfma_f32_16x16x32_bf16 v[\\d:\\d+3], a[\\x:\\x+3], v[\\y:\\y+3], 0\n.endm\n.macro Zvvv d,x,y\nv_mfma_f32_16x16x32_bf16 v[\\d:\\d+3], v[\\x:\\x+3], v[\\y:\\y+3], 0\n.endm\n"
".macro Mvvv d,x,y\nv_mfma_f32_16x16x32_bf16 v[\\d:\\d+3], v[\\x:\\x+3], v[\\y:\\y+3], v[\\d:\\d+3]\n.endm\n.macro AW d,x\nv_accvgpr_write_b32 a\\d, v\\x\n.endm\n.macro Tv d,x,o\n"
"ds_read_b64_tr_b16 v[\\d:\\d+1], v\\x offset:\\o\n.endm\n.macro Sa d,x,r,s,o\nbuffer_store_dwordx4 a[\\d:\\d+3], v\\x, s[\\r:\\r+3], s\\s offen offset:\\o sc0 sc1\n.endm\n"
".macro Cvaa d,x,y,c\nv_mfma_f32_16x16x32_bf16 v[\\d:\\d+3], a[\\x:\\x+3], a[\\y:\\y+3], v[\\c:\\c+3]\n.endm\n.Lcode0:\ns_mov_b32 s70, 0\ns_load_dwordx16 s[44:59], s[8:9], 0x0\n"
"s_load_dwordx8 s[60:67], s[8:9], 0x40\ns_load_dwordx2 s[68:69], s[8:9], 0x60\nv_lshrrev_b32 v2, 6, v0\nv_and_b32 v0, 63, v0\nv_readfirstlane_b32 s11, v2\nv_lshlrev_b32 v1, 4, v0\n"
"s_and_b32 s73, s10, 7\ns_lshr_b32 s74, s10, 3\ns_and_b32 s2, s74, 1\ns_lshl1_add_u32 s12, s73, s2\ns_bfe_u32 s13, s74, 0x30001\ns_lshr_b32 s14, s74, 4\ns_xor_b32 s14, s14, 1\n"
"v_lshrrev_b32 v32, 5, v0\ns_and_b32 s3, s11, 1\ns_lshl_b32 s2, s3, 3\nv_lshl_or_b32 v33, v32, 1, s2\nv_and_b32 v34, 31, v0\nv_xor_b32 v33, v33, v34\nv_lshlrev_b32 v33, 4, v33\n"
"v_lshl_or_b32 v6, v32, 13, v33\nv_xor_b32 v33, 64, v6\nv_add_u32 v7, 15360, v33\nv_add_u32 v8, 30720, v6\nv_add_u32 v9, 46080, v33\ns_lshl_b32 s2, s11, 16\ns_lshl_b32 s4, s12, 9\n"
"s_add_u32 s5, s2, s4\ns_getpc_b64 s[40:41]\n.PC8:\ns_sub_u32 s40, s40, .PC8-.Lcode0\ns_subb_u32 s41, s41, 0\ns_mov_b32 s42, .Lcode1-.Lcode0\ns_mov_b32 s43, 0x20000\n"
"s_lshl2_add_u32 s6, s74, s11\ns_lshl_b32 s6, s6, 7\nv_lshlrev_b32 v54, 10, v1\nbuffer_load_dword v31, v54, s[40:43], s6 offen\nv_lshrrev_b32 v32, 4, v0\nv_and_b32 v33, 7, v0\n"
"v_xor_b32 v33, v33, v32\nv_lshlrev_b32 v33, 4, v33\nv_lshrrev_b32 v32, 3, v0\nv_lshl_or_b32 v2, v32, 11, v33\nv_xor_b32 v33, 64, v2\nv_add_u32 v3, 15360, v33\n"
"v_add_u32 v4, 30720, v2\nv_add_u32 v5, 46080, v33\nv_lshrrev_b32 v144, 4, v0\nv_and_b32 v145, 7, v0\nv_xor_b32 v145, v145, v144\nv_lshlrev_b32 v145, 4, v145\n"
"v_lshrrev_b32 v144, 3, v0\nv_mul_u32_u24 v144, 896, v144\nv_add_u32 v136, v144, v145\nv_xor_b32 v145, 64, v136\nv_add_u32 v137, 6144, v145\nv_add_u32 v138, 55296, v136\n"
"v_add_u32 v139, 61440, v145\ns_lshr_b32 s2, s13, 2\ns_bfe_u32 s3, s13, 0x10001\ns_mul_i32 s3, s3, 17\ns_add_u32 s2, s2, s3\ns_lshl2_add_u32 s3, s14, s11\ns_lshl_b32 s3, s3, 2\n"
"s_lshr_b32 s3, 0xe420ca86, s3\ns_and_b32 s3, s3, 15\ns_add_u32 s2, s2, s3\ns_cmp_eq_u32 s2, 32\ns_cselect_b32 s86, 16, s2\ns_sub_u32 s2, 15, s13\ns_lshl3_add_u32 s2, s2, s11\n"
"s_mul_i32 s87, s2, 14336\ns_mov_b32 s18, 0xc0ae800\ns_mov_b32 s30, 0x1c0000\ns_mov_b32 s34, 0x380000\ns_mov_b32 s22, 0x400000\ns_mov_b32 s26, 0x800000\ns_mov_b32 s19, 0x20000\n"
"s_mov_b32 s31, 0x20000\ns_mov_b32 s35, 0x20000\ns_mov_b32 s23, 0x20000\ns_mov_b32 s27, 0x20000\nWL 0\ns_mov_b32 s28, s44\ns_and_b32 s29, s45, 0xffff\ns_mov_b32 s32, s48\n"
"s_and_b32 s33, s49, 0xffff\ns_mov_b32 s20, s46\ns_and_b32 s21, s47, 0xffff\ns_bitcmp1_b32 s13, 0\ns_cselect_b64 s[50:51], s[52:53], s[50:51]\ns_mov_b32 s24, s50\n"
"s_and_b32 s25, s51, 0xffff\ns_sub_u32 s44, s5, 0x40000\ns_mov_b32 s51, s5\ns_lshl_b32 s47, s11, 12\ns_add_u32 s47, s47, 0x10000\ns_lshl_b32 s48, s11, 13\ns_lshl_b32 s45, s86, 17\n"
"s_sub_u32 s45, s45, 128\ns_mov_b32 s52, s87\ns_cmp_eq_u32 s14, 1\ns_cbranch_scc1 .B6\ns_add_u32 m0, s47, 65536\ns_add_u32 s46, s51, 0\nD0 6,32,46,0\nD0 7,32,46,1024\n"
"D0 8,32,46,2048\nD0 9,32,46,3072\ns_add_u32 m0, s47, -65536\ns_add_u32 s46, s52, 0\nD0 136,28,46,0\nD0 137,28,46,1024\nD0 138,28,46,2048\nD0 139,28,46,3072\n"
"s_add_u32 m0, s47, 81920\ns_add_u32 s46, s51, 262144\nD0 6,32,46,0\nD0 7,32,46,1024\nD0 8,32,46,2048\nD0 9,32,46,3072\ns_branch .B7\n.B6:\ns_add_u32 m0, s47, 0\n"
"s_add_u32 s44, s44, 0x40000\nD0 6,24,44,0\nD0 7,24,44,1024\nD0 8,24,44,2048\nD0 9,24,44,3072\ns_add_u32 m0, s48, 0\ns_add_u32 s45, s45, 128\nD0 2,20,45,0\nD0 3,20,45,1024\n"
"D0 4,20,45,2048\nD0 5,20,45,3072\ns_add_u32 m0, s48, 4096\ns_add_u32 s46, s45, 0x10000\nD0 2,20,46,0\nD0 3,20,46,1024\nD0 4,20,46,2048\nD0 5,20,46,3072\n.B7:\n"
"s_add_u32 s54, s54, 0x1fffff\ns_addc_u32 s55, s55, 0\ns_and_b32 s54, s54, 0xffe00000\ns_mov_b32 s16, s54\ns_and_b32 s17, s55, 0xffff\ns_mov_b32 s2, 0xc048000\n"
"s_load_dword s15, s[54:55], s2\nv_and_b32 v48, 15, v0\nv_lshrrev_b32 v49, 4, v0\nv_lshrrev_b32 v50, 1, v48\nv_xor_b32 v50, v50, v49\nv_lshlrev_b32 v50, 4, v50\n"
"v_lshl_or_b32 v50, v48, 7, v50\ns_lshr_b32 s2, s11, 1\ns_lshl_b32 s2, s2, 12\nv_add_u32 v10, s2, v50\nv_xor_b32 v11, 64, v10\nv_lshrrev_b32 v50, 2, v48\n"
"v_lshl_add_u32 v51, v49, 3, v50\nv_and_b32 v52, 1, v49\nv_lshl_or_b32 v50, v52, 2, v50\nv_lshlrev_b32 v50, 5, v50\nv_lshl_or_b32 v50, v51, 9, v50\nv_and_b32 v52, 3, v0\n"
"v_lshl_or_b32 v50, v52, 3, v50\ns_and_b32 s2, s11, 1\ns_lshl_b32 s2, s2, 8\ns_add_u32 s2, s2, 0x10000\nv_add_u32 v12, s2, v50\nv_xor_b32 v13, 32, v12\nv_xor_b32 v14, 64, v12\n"
"v_xor_b32 v15, 96, v12\nv_xor_b32 v16, 128, v12\nv_xor_b32 v17, 160, v12\nv_xor_b32 v18, 192, v12\nv_xor_b32 v19, 224, v12\nv_lshrrev_b32 v24, 3, v0\nv_lshlrev_b32 v24, 13, v24\n"
"v_and_b32 v26, 7, v0\nv_lshl_or_b32 v24, v26, 7, v24\nv_lshrrev_b32 v26, 4, v0\nv_lshl_or_b32 v24, v26, 28, v24\nv_lshrrev_b32 v26, 5, v0\nv_lshlrev_b32 v26, 30, v26\n"
"v_lshl_or_b32 v26, v0, 7, v26\ns_and_b32 s2, s13, 6\ns_and_b32 s3, s12, 1\ns_add_u32 s2, s2, s3\ns_lshl2_add_u32 s2, s2, s11\ns_lshl_b32 s2, s2, 14\ns_lshl_b32 s3, s73, 10\n"
"s_add_u32 s2, s2, s3\ns_xor_b32 s3, s14, 1\ns_lshl_b32 s3, s3, 30\ns_add_u32 s2, s2, s3\ns_add_u32 s96, s2, 1048576\ns_and_b32 s2, s10, 127\ns_lshl2_add_u32 s2, s2, s11\n"
"s_lshl_b32 s97, s2, 12\ns_getpc_b64 s[0:1]\n.PC9:\ns_mov_b32 s2, .PA2-.PC9\ns_mov_b32 s3, .PA4-.PC9\ns_bitcmp1_b32 s13, 0\ns_cselect_b32 s2, .PA3-.PC9, s2\n"
"s_cselect_b32 s3, .PA5-.PC9, s3\ns_cmp_eq_u32 s14, 0\ns_cselect_b32 s2, s2, s3\ns_add_u32 s0, s0, s2\ns_addc_u32 s1, s1, 0\ns_setpc_b64 s[0:1]\n.p2align 12\n.PA2:\n"
"v_add_u32 v128, 0x10000, v12\nv_add_u32 v129, 0x10000, v13\nv_add_u32 v130, 0x10000, v14\nv_add_u32 v131, 0x10000, v15\nv_add_u32 v132, 0x10000, v16\nv_add_u32 v133, 0x10000, v17\n"
"v_add_u32 v134, 0x10000, v18\nv_add_u32 v135, 0x10000, v19\ns_add_u32 s2, s11, 1\ns_lshr_b32 s2, s2, 1\ns_lshl_b32 s2, s2, 12\nv_add_u32 v140, s2, v10\nv_add_u32 v141, s2, v11\n"
"v_xor_b32 v142, 0x1000, v140\nv_xor_b32 v143, 0x1000, v141\ns_add_u32 m0, s47, 0\ns_add_u32 s46, s51, 524288\nD0 6,32,46,0\nD0 7,32,46,1024\nD0 8,32,46,2048\nD0 9,32,46,3072\n"
"s_add_u32 m0, s47, -49152\ns_add_u32 s46, s52, 128\nD0 136,28,46,0\nD0 137,28,46,1024\nD0 138,28,46,2048\nD0 139,28,46,3072\ns_add_u32 m0, s47, 16384\ns_add_u32 s46, s51, 786432\n"
"D0 6,32,46,0\nD0 7,32,46,1024\nD0 8,32,46,2048\nD0 9,32,46,3072\nv_mov_b32 v116, 0x40000000\nv_cmp_gt_u32 vcc, 56, v0\ns_lshl2_add_u32 s2, s10, s11\ns_mul_i32 s2, s2, 1792\n"
"v_cndmask_b32 v117, v116, v1, vcc\ns_mov_b32 s36, s56\ns_and_b32 s37, s57, 0xffff\ns_mov_b32 s38, 0x380\ns_mov_b32 s39, 0x20000\ns_mov_b32 s40, s58\ns_and_b32 s41, s59, 0xffff\n"
"s_mov_b32 s42, 0x380\ns_mov_b32 s43, 0x20000\nbuffer_load_dwordx4 v[96:99], v117, s[28:31], s2 offen offset:0\nbuffer_load_dwordx4 v[100:103], v117, s[28:31], s2 offen offset:896\n"
"buffer_load_dwordx4 v[104:107], v117, s[36:39], 0 offen offset:0\nbuffer_load_dwordx4 v[108:111], v117, s[40:43], 0 offen offset:0\ns_add_u32 m0, s47, 32768\n"
"s_add_u32 s46, s51, 1048576\nD0 6,32,46,0\nD0 7,32,46,1024\nD0 8,32,46,2048\nD0 9,32,46,3072\ns_add_u32 m0, s47, -32768\ns_add_u32 s46, s52, 256\nD0 136,28,46,0\n"
"D0 137,28,46,1024\nD0 138,28,46,2048\nD0 139,28,46,3072\nWL 0\ns_mul_i32 s15, s15, 1664525\ns_add_u32 s15, s15, 1013904223\nv_mov_b32 v25, s15\ns_mov_b32 s57, 0x3db8aa3b\n"
"s_and_b32 s59, s11, 1\ns_lshl_b32 s59, s59, 13\ns_mul_i32 s2, s73, 25165824\ns_and_b32 s3, s12, 1\ns_lshl_b32 s3, s3, 21\ns_add_u32 s2, s2, s3\ns_lshl2_add_u32 s3, s13, s11\n"
"s_lshl_b32 s3, s3, 15\ns_add_u32 s2, s2, s3\ns_add_u32 s56, s2, 0x800000\ns_xor_b32 s53, s56, 0xc000\nWV 24\ns_barrier\nRa 160,140,0\nRa 164,140,2048\nRa 168,142,0\n"
"Ra 172,142,2048\nTa 128,128,0\nTa 130,128,2048\nTa 132,129,0\nTa 134,129,2048\nTa 136,130,0\nTa 138,130,2048\nTa 140,131,0\nTa 142,131,2048\nTa 144,132,0\nTa 146,132,2048\n"
"Ta 148,133,0\nTa 150,133,2048\nTa 152,134,0\nTa 154,134,2048\nTa 156,135,0\nTa 158,135,2048\nWL 0\nZvaa 64,128,160\nRa 224,141,0\ns_add_u32 m0, s47, 49152\n"
"s_add_u32 s46, s51, 1310720\nZvaa 32,128,164\nRa 228,141,2048\nZvaa 192,128,168\nRa 232,143,0\nD0 6,32,46,0\nZvaa 224,128,172\nRa 236,143,2048\nZvaa 228,132,172\nTa 192,128,16384\n"
"Zvaa 196,132,168\nZvaa 36,132,164\nTa 194,128,18432\nZvaa 68,132,160\nTa 196,129,16384\nD0 7,32,46,1024\nZvaa 72,136,160\nTa 198,129,18432\nZvaa 40,136,164\nTa 200,130,16384\n"
"Zvaa 200,136,168\nTa 202,130,18432\nZvaa 232,136,172\nZvaa 236,140,172\nTa 204,131,16384\nD0 8,32,46,2048\nZvaa 204,140,168\nTa 206,131,18432\nZvaa 44,140,164\nTa 208,132,16384\n"
"Zvaa 76,140,160\nTa 210,132,18432\nZvaa 80,144,160\nTa 212,133,16384\nZvaa 48,144,164\nD0 9,32,46,3072\nZvaa 208,144,168\nTa 214,133,18432\nZvaa 240,144,172\nTa 216,134,16384\n"
"Zvaa 244,148,172\nTa 218,134,18432\ns_add_u32 m0, s47, -16384\ns_add_u32 s46, s52, 384\nZvaa 212,148,168\nTa 220,135,16384\nZvaa 52,148,164\nTa 222,135,18432\nD0 136,28,46,0\n"
"Zvaa 84,148,160\nZvaa 88,152,160\nZvaa 56,152,164\nZvaa 216,152,168\nZvaa 248,152,172\nD0 137,28,46,1024\nZvaa 252,156,172\nZvaa 220,156,168\nZvaa 60,156,164\nZvaa 92,156,160\n"
"s_waitcnt vmcnt(18) lgkmcnt(0)\ns_barrier\nMvaa 64,192,224\nRa 160,140,16384\nD0 138,28,46,2048\nMvaa 32,192,228\nRa 164,140,18432\nMvaa 192,192,232\nRa 168,142,16384\n"
"Mvaa 224,192,236\nRa 172,142,18432\nWV 15\nMvaa 228,196,236\nTa 128,12,0\nMvaa 196,196,232\nD0 139,28,46,3072\nMvaa 36,196,228\nTa 130,12,2048\nv_lshlrev_b32 v112, 16, v96\n"
"Mvaa 68,196,224\nTa 132,13,0\nMvaa 72,200,224\nTa 134,13,2048\ns_add_u32 m0, s47, 65536\ns_add_u32 s46, s51, 1572864\nMvaa 40,200,228\nTa 136,14,0\nMvaa 200,200,232\n"
"Ta 138,14,2048\nD0 6,32,46,0\nMvaa 232,200,236\nv_lshlrev_b32 v113, 16, v100\nv_lshlrev_b32 v114, 16, v104\nMvaa 236,204,236\nTa 140,15,0\nMvaa 204,204,232\nTa 142,15,2048\n"
"Mvaa 44,204,228\nTa 144,16,0\nv_lshlrev_b32 v115, 16, v108\nMvaa 76,204,224\nTa 146,16,2048\nD0 7,32,46,1024\nMvaa 80,208,224\nTa 148,17,0\nMU 116,112,114\nMvaa 48,208,228\n"
"MU 117,113,114\nMvaa 208,208,232\nTa 150,17,2048\nMvaa 240,208,236\nTa 152,18,0\nCV 116,116,117\nMvaa 244,212,236\nTa 154,18,2048\nD0 8,32,46,2048\nMvaa 212,212,232\nTa 156,19,0\n"
"Mvaa 52,212,228\nTa 158,19,2048\nv_lshlrev_b32 v118, 16, v116\nMvaa 84,212,224\nv_and_b32 v119, 0xffff0000, v116\nMvaa 88,216,224\nMU 120,112,115\nMU 121,113,115\nMvaa 56,216,228\n"
"D0 9,32,46,3072\nMvaa 216,216,232\nv_and_b32 v112, 0xffff0000, v96\nMvaa 248,216,236\nv_and_b32 v113, 0xffff0000, v100\nv_and_b32 v114, 0xffff0000, v104\nMvaa 252,220,236\n"
"s_add_u32 m0, s47, 81920\ns_add_u32 s46, s51, 1835008\nMvaa 220,220,232\nv_and_b32 v115, 0xffff0000, v108\nMvaa 60,220,228\nD0 6,32,46,0\nMU 116,112,114\nMvaa 92,220,224\nWL 0\n"
"Mvaa 64,128,160\nRa 224,141,16384\nMU 117,113,114\nMvaa 32,128,164\nRa 228,141,18432\nMvaa 192,128,168\nRa 232,143,16384\nMvaa 224,128,172\nRa 236,143,18432\nD0 7,32,46,1024\n"
"Mvaa 228,132,172\nTa 192,12,16384\nMvaa 196,132,168\nCV 116,116,117\nMvaa 36,132,164\nTa 194,12,18432\nv_and_b32 v117, 0xffff0000, v116\nMvaa 68,132,160\nTa 196,13,16384\n"
"Mvaa 72,136,160\nTa 198,13,18432\nD0 8,32,46,2048\nMvaa 40,136,164\nTa 200,14,16384\nMvaa 200,136,168\nTa 202,14,18432\nMvaa 232,136,172\nv_lshlrev_b32 v116, 16, v116\n"
"FM 120,112,115\nMvaa 236,140,172\nTa 204,15,16384\nMvaa 204,140,168\nTa 206,15,18432\nD0 9,32,46,3072\nMvaa 44,140,164\nTa 208,16,16384\nFM 121,113,115\nMvaa 76,140,160\n"
"Ta 210,16,18432\nMvaa 80,144,160\nTa 212,17,16384\ns_add_u32 m0, s47, -65536\ns_add_u32 s46, s52, 512\nMvaa 48,144,164\nAD 118,118,116\nMvaa 208,144,168\nTa 214,17,18432\n"
"D0 136,28,46,0\nMvaa 240,144,172\nTa 216,18,16384\nAD 119,119,117\nMvaa 244,148,172\nTa 218,18,18432\nMvaa 212,148,168\nTa 220,19,16384\nMvaa 52,148,164\nTa 222,19,18432\n"
"v_lshlrev_b32 v112, 16, v97\nMvaa 84,148,160\nD0 137,28,46,1024\nMvaa 88,152,160\nv_lshlrev_b32 v113, 16, v101\nv_lshlrev_b32 v114, 16, v105\nMvaa 56,152,164\n"
"v_lshlrev_b32 v115, 16, v109\nMvaa 216,152,168\nMU 116,112,114\nMvaa 248,152,172\nMU 117,113,114\nCV 116,116,117\nMvaa 252,156,172\nD0 138,28,46,2048\nMvaa 220,156,168\n"
"v_and_b32 v117, 0xffff0000, v116\nMvaa 60,156,164\nv_lshlrev_b32 v116, 16, v116\nFM 120,112,115\nMvaa 92,156,160\ns_waitcnt vmcnt(15) lgkmcnt(0)\ns_barrier\nMvaa 64,192,224\n"
"Ra 160,140,32768\nFM 121,113,115\nMvaa 32,192,228\nRa 164,140,34816\nD0 139,28,46,3072\nMvaa 192,192,232\nRa 168,142,32768\nMvaa 224,192,236\nRa 172,142,34816\nAD 118,118,116\n"
"Mvaa 228,196,236\nTa 128,12,32768\ns_add_u32 m0, s47, 0\ns_add_u32 s46, s51, 2097152\nMvaa 196,196,232\nAD 119,119,117\nMvaa 36,196,228\nTa 130,12,34816\nD0 6,32,46,0\n"
"Mvaa 68,196,224\nTa 132,13,32768\nMvaa 72,200,224\nTa 134,13,34816\nv_and_b32 v112, 0xffff0000, v97\nMvaa 40,200,228\nTa 136,14,32768\nMvaa 200,200,232\nTa 138,14,34816\n"
"Mvaa 232,200,236\nD0 7,32,46,1024\nv_and_b32 v113, 0xffff0000, v101\nMvaa 236,204,236\nTa 140,15,32768\nMvaa 204,204,232\nTa 142,15,34816\nMvaa 44,204,228\nTa 144,16,32768\n"
"v_and_b32 v114, 0xffff0000, v105\nMvaa 76,204,224\nTa 146,16,34816\nMvaa 80,208,224\nTa 148,17,32768\nD0 8,32,46,2048\nMvaa 48,208,228\nv_and_b32 v115, 0xffff0000, v109\n"
"Mvaa 208,208,232\nTa 150,17,34816\nMvaa 240,208,236\nTa 152,18,32768\nMU 116,112,114\nMvaa 244,212,236\nTa 154,18,34816\nMvaa 212,212,232\nTa 156,19,32768\nD0 9,32,46,3072\n"
"Mvaa 52,212,228\nTa 158,19,34816\nMU 117,113,114\nMvaa 84,212,224\nCV 116,116,117\nMvaa 88,216,224\ns_add_u32 m0, s47, 16384\ns_add_u32 s46, s51, 2359296\nMvaa 56,216,228\n"
"v_and_b32 v117, 0xffff0000, v116\nMvaa 216,216,232\nD0 6,32,46,0\nMvaa 248,216,236\nv_lshlrev_b32 v116, 16, v116\nFM 120,112,115\nMvaa 252,220,236\nFM 121,113,115\n"
"Mvaa 220,220,232\nAD 118,118,116\nMvaa 60,220,228\nAD 119,119,117\nv_lshlrev_b32 v112, 16, v98\nMvaa 92,220,224\nWL 0\nMvaa 64,128,160\nRa 224,141,32768\nD0 7,32,46,1024\n"
"Mvaa 32,128,164\nRa 228,141,34816\nMvaa 192,128,168\nRa 232,143,32768\nMvaa 224,128,172\nRa 236,143,34816\nv_lshlrev_b32 v113, 16, v102\nMvaa 228,132,172\nTa 192,12,49152\n"
"Mvaa 196,132,168\nD0 8,32,46,2048\nMvaa 36,132,164\nTa 194,12,51200\nv_lshlrev_b32 v114, 16, v106\nMvaa 68,132,160\nTa 196,13,49152\nMvaa 72,136,160\nTa 198,13,51200\n"
"v_lshlrev_b32 v115, 16, v110\nMvaa 40,136,164\nTa 200,14,49152\nMvaa 200,136,168\nTa 202,14,51200\nD0 9,32,46,3072\nMvaa 232,136,172\nMU 116,112,114\nMU 117,113,114\n"
"Mvaa 236,140,172\nTa 204,15,49152\nMvaa 204,140,168\nTa 206,15,51200\nMvaa 44,140,164\nTa 208,16,49152\nCV 116,116,117\nMvaa 76,140,160\nTa 210,16,51200\nMvaa 80,144,160\n"
"Ta 212,17,49152\nv_and_b32 v117, 0xffff0000, v116\nMvaa 48,144,164\nv_lshlrev_b32 v116, 16, v116\nMvaa 208,144,168\nTa 214,17,51200\nMvaa 240,144,172\nTa 216,18,49152\n"
"FM 120,112,115\nMvaa 244,148,172\nTa 218,18,51200\nMvaa 212,148,168\nTa 220,19,49152\nMvaa 52,148,164\nTa 222,19,51200\nFM 121,113,115\nMvaa 84,148,160\nAD 118,118,116\n"
"Mvaa 88,152,160\nAD 119,119,117\nv_and_b32 v112, 0xffff0000, v98\nMvaa 56,152,164\nv_and_b32 v113, 0xffff0000, v102\nMvaa 216,152,168\nv_and_b32 v114, 0xffff0000, v106\n"
"Mvaa 248,152,172\nv_and_b32 v115, 0xffff0000, v110\nMU 116,112,114\nMvaa 252,156,172\nMU 117,113,114\nMvaa 220,156,168\nCV 116,116,117\nMvaa 60,156,164\n"
"v_and_b32 v117, 0xffff0000, v116\nv_lshlrev_b32 v116, 16, v116\nMvaa 92,156,160\ns_waitcnt vmcnt(12) lgkmcnt(0)\ns_barrier\nMvaa 64,192,224\nRa 160,140,49152\n"
"s_add_u32 m0, s47, 32768\ns_add_u32 s46, s51, 2621440\nMvaa 32,192,228\nRa 164,140,51200\nMvaa 192,192,232\nRa 168,142,49152\nD0 6,32,46,0\nMvaa 224,192,236\nRa 172,142,51200\n"
"FM 120,112,115\nMvaa 228,196,236\nTa 128,128,0\nMvaa 196,196,232\nFM 121,113,115\nMvaa 36,196,228\nTa 130,128,2048\nAD 118,118,116\nMvaa 68,196,224\nTa 132,129,0\nD0 7,32,46,1024\n"
"Mvaa 72,200,224\nTa 134,129,2048\nAD 119,119,117\nMvaa 40,200,228\nTa 136,130,0\nMvaa 200,200,232\nTa 138,130,2048\nMvaa 232,200,236\nv_lshlrev_b32 v112, 16, v99\n"
"v_lshlrev_b32 v113, 16, v103\nMvaa 236,204,236\nTa 140,131,0\nD0 8,32,46,2048\nMvaa 204,204,232\nTa 142,131,2048\nMvaa 44,204,228\nTa 144,132,0\nv_lshlrev_b32 v114, 16, v107\n"
"Mvaa 76,204,224\nTa 146,132,2048\nMvaa 80,208,224\nTa 148,133,0\nv_lshlrev_b32 v115, 16, v111\nMvaa 48,208,228\nD0 9,32,46,3072\nMvaa 208,208,232\nTa 150,133,2048\n"
"Mvaa 240,208,236\nTa 152,134,0\nMU 116,112,114\nMvaa 244,212,236\nTa 154,134,2048\ns_add_u32 m0, s47, -32768\ns_add_u32 s46, s52, 640\nMvaa 212,212,232\nTa 156,135,0\n"
"Mvaa 52,212,228\nTa 158,135,2048\nD0 136,28,46,0\nMvaa 84,212,224\nMU 117,113,114\nMvaa 88,216,224\nCV 116,116,117\nv_and_b32 v117, 0xffff0000, v116\nMvaa 56,216,228\n"
"v_lshlrev_b32 v116, 16, v116\nMvaa 216,216,232\nFM 120,112,115\nMvaa 248,216,236\nD0 137,28,46,1024\nFM 121,113,115\nMvaa 252,220,236\nAD 118,118,116\nMvaa 220,220,232\n"
"AD 119,119,117\nMvaa 60,220,228\nv_and_b32 v112, 0xffff0000, v99\nv_and_b32 v113, 0xffff0000, v103\nMvaa 92,220,224\nWL 0\nMvaa 64,128,160\nRa 224,141,49152\nD0 138,28,46,2048\n"
"Mvaa 32,128,164\nRa 228,141,51200\nMvaa 192,128,168\nRa 232,143,49152\nMvaa 224,128,172\nRa 236,143,51200\nv_and_b32 v114, 0xffff0000, v107\nMvaa 228,132,172\nTa 192,128,16384\n"
"Mvaa 196,132,168\nD0 139,28,46,3072\nMvaa 36,132,164\nTa 194,128,18432\nv_and_b32 v115, 0xffff0000, v111\nMvaa 68,132,160\nTa 196,129,16384\nMvaa 72,136,160\nTa 198,129,18432\n"
"s_add_u32 m0, s47, 49152\ns_add_u32 s46, s51, 2883584\nMvaa 40,136,164\nTa 200,130,16384\nMvaa 200,136,168\nTa 202,130,18432\nD0 6,32,46,0\nMvaa 232,136,172\nMU 116,112,114\n"
"MU 117,113,114\nMvaa 236,140,172\nTa 204,131,16384\nMvaa 204,140,168\nTa 206,131,18432\nMvaa 44,140,164\nTa 208,132,16384\nCV 116,116,117\nMvaa 76,140,160\nTa 210,132,18432\n"
"D0 7,32,46,1024\nMvaa 80,144,160\nTa 212,133,16384\nv_and_b32 v117, 0xffff0000, v116\nMvaa 48,144,164\nv_lshlrev_b32 v116, 16, v116\nMvaa 208,144,168\nTa 214,133,18432\n"
"Mvaa 240,144,172\nTa 216,134,16384\nFM 120,112,115\nMvaa 244,148,172\nTa 218,134,18432\nD0 8,32,46,2048\nMvaa 212,148,168\nTa 220,135,16384\nMvaa 52,148,164\nTa 222,135,18432\n"
"FM 121,113,115\nMvaa 84,148,160\nAD 118,118,116\nMvaa 88,152,160\nAD 119,119,117\nMvaa 56,152,164\nD0 9,32,46,3072\nMvaa 216,152,168\nv_permlane32_swap_b32 v118, v119\n"
"Mvaa 248,152,172\nv_permlane32_swap_b32 v120, v121\nAD 118,118,119\nMvaa 252,156,172\nAD 120,120,121\ns_and_b32 s2, s74, 3\ns_lshl2_add_u32 s2, s73, s2\ns_lshl_b32 s2, s2, 12\n"
"s_add_u32 s3, s45, 512\ns_and_b32 s3, s3, 0xfffe0000\ns_add_u32 s2, s2, s3\nbuffer_load_dword v31, v26, s[20:23], s2 offen\nMvaa 220,156,168\nMvaa 60,156,164\n"
"v_permlane16_swap_b32 v118, v120\nAD 118,118,120\nMvaa 92,156,160\ns_waitcnt vmcnt(13) lgkmcnt(0)\ns_barrier\nMvaa 64,192,224\nRa 160,140,0\ns_add_u32 m0, s47, 65536\n"
"s_add_u32 s46, s51, 3145728\nMvaa 32,192,228\nRa 164,140,2048\nMvaa 192,192,232\nRa 168,142,0\nD0 6,32,46,0\nMvaa 224,192,236\nRa 172,142,2048\n"
"v_add_f32_dpp v118, v118, v118 row_ror:8 row_mask:0xf bank_mask:0xf\nMvaa 228,196,236\nTa 128,12,0\nMvaa 196,196,232\n"
"v_add_f32_dpp v118, v118, v118 row_ror:4 row_mask:0xf bank_mask:0xf\nMvaa 36,196,228\nTa 130,12,2048\nMvaa 68,196,224\nTa 132,13,0\nD0 7,32,46,1024\nMvaa 72,200,224\n"
"Ta 134,13,2048\nv_add_f32_dpp v118, v118, v118 row_ror:2 row_mask:0xf bank_mask:0xf\nMvaa 40,200,228\nTa 136,14,0\nMvaa 200,200,232\nTa 138,14,2048\nMvaa 232,200,236\n"
"v_add_f32_dpp v118, v118, v118 row_ror:1 row_mask:0xf bank_mask:0xf\nCV 118,118,118\nMvaa 236,204,236\nTa 140,15,0\nD0 8,32,46,2048\nMvaa 204,204,232\nTa 142,15,2048\n"
"Mvaa 44,204,228\nTa 144,16,0\nv_lshlrev_b32 v118, 16, v118\nMvaa 76,204,224\nTa 146,16,2048\nMvaa 80,208,224\nTa 148,17,0\nv_mul_f32 v119, 0xbfb8aa3b, v118\nMvaa 48,208,228\n"
"D0 9,32,46,3072\nEX 119,119\nMvaa 208,208,232\nTa 150,17,2048\nMvaa 240,208,236\nTa 152,18,0\nv_add_f32 v120, 0, v118\nMvaa 244,212,236\nTa 154,18,2048\ns_add_u32 m0, s47, -16384\n"
"s_add_u32 s46, s52, 768\nMvaa 212,212,232\nTa 156,19,0\nMvaa 52,212,228\nTa 158,19,2048\nD0 136,28,46,0\nMvaa 84,212,224\nv_add_f32 v119, 1.0, v119\nv_rcp_f32 v119, v119\n"
"Mvaa 88,216,224\nv_ashrrev_i32 v121, 31, v120\nCV 119,119,119\nMvaa 56,216,228\nv_lshlrev_b32 v119, 16, v119\nMvaa 216,216,232\nv_or_b32 v121, 0x80000000, v121\nMvaa 248,216,236\n"
"D0 137,28,46,1024\nv_xor_b32 v120, v120, v121\nMvaa 252,220,236\nv_and_b32 v120, 0xffff0000, v120\nMvaa 220,220,232\ns_lshl2_add_u32 s2, s10, s11\nMvaa 60,220,228\n"
"v_lshrrev_b32 v112, 5, v0\nv_lshl_add_u32 v112, s2, 1, v112\nMvaa 92,220,224\nWL 0\nMvaa 64,128,160\nRa 224,141,0\nD0 138,28,46,2048\nMvaa 32,128,164\nRa 228,141,2048\n"
"Mvaa 192,128,168\nRa 232,143,0\nMvaa 224,128,172\nRa 236,143,2048\nv_sub_u32 v113, 0xffff, v112\nMvaa 228,132,172\nTa 192,12,16384\nMvaa 196,132,168\nD0 139,28,46,3072\n"
"Mvaa 36,132,164\nTa 194,12,18432\nv_or_b32 v120, v120, v113\nMvaa 68,132,160\nTa 196,13,16384\nMvaa 72,136,160\nTa 198,13,18432\ns_add_u32 m0, s47, 81920\n"
"s_add_u32 s46, s51, 3407872\nMvaa 40,136,164\nTa 200,14,16384\nMvaa 200,136,168\nTa 202,14,18432\nD0 6,32,46,0\nMvaa 232,136,172\nv_mov_b32 v121, s15\nv_and_b32 v113, 31, v0\n"
"Mvaa 236,140,172\nTa 204,15,16384\nMvaa 204,140,168\nTa 206,15,18432\nMvaa 44,140,164\nTa 208,16,16384\nv_mov_b32 v116, 0x40000000\nMvaa 76,140,160\nTa 210,16,18432\n"
"D0 7,32,46,1024\nMvaa 80,144,160\nTa 212,17,16384\nv_cmp_eq_u32 vcc, 16, v113\nv_lshlrev_b32 v114, 3, v112\nv_lshlrev_b32 v115, 9, v113\nv_cndmask_b32 v114, v116, v114, vcc\n"
"v_cndmask_b32 v119, v119, v118, vcc\nMvaa 48,144,164\nv_lshl_add_u32 v115, v112, 2, v115\nMvaa 208,144,168\nTa 214,17,18432\nMvaa 240,144,172\nTa 216,18,16384\n"
"v_and_b32 v113, 15, v0\nMvaa 244,148,172\nTa 218,18,18432\nD0 8,32,46,2048\nMvaa 212,148,168\nTa 220,19,16384\nv_cmp_eq_u32 vcc, 0, v113\ns_mov_b32 s2, 0xc040000\n"
"s_mov_b32 s3, 0xc044000\nv_cndmask_b32 v115, v116, v115, vcc\nbuffer_store_dwordx2 v[120:121], v114, s[16:19], s3 offen sc0 sc1\n"
"buffer_store_dword v119, v115, s[16:19], s2 offen sc0 sc1\nMvaa 52,148,164\nTa 222,19,18432\nMvaa 84,148,160\nMvaa 88,152,160\nMvaa 56,152,164\nD0 9,32,46,3072\nMvaa 216,152,168\n"
"Mvaa 248,152,172\nMvaa 252,156,172\nMvaa 220,156,168\nMvaa 60,156,164\nMvaa 92,156,160\ns_waitcnt vmcnt(15) lgkmcnt(0)\ns_barrier\nMvaa 64,192,224\nRa 160,140,32768\n"
"s_add_u32 m0, s47, 0\ns_add_u32 s44, s44, 0x40000\nMvaa 32,192,228\nRa 164,140,34816\nMvaa 192,192,232\nRa 168,142,32768\nD0 6,24,44,0\nMvaa 224,192,236\nRa 172,142,34816\n"
"Mvaa 228,196,236\nTa 128,12,32768\nMvaa 196,196,232\nMvaa 36,196,228\nTa 130,12,34816\nMvaa 68,196,224\nTa 132,13,32768\nD0 7,24,44,1024\nMvaa 72,200,224\nTa 134,13,34816\n"
"Mvaa 40,200,228\nTa 136,14,32768\nMvaa 200,200,232\nTa 138,14,34816\nMvaa 232,200,236\nMvaa 236,204,236\nTa 140,15,32768\nD0 8,24,44,2048\nMvaa 204,204,232\nTa 142,15,34816\n"
"Mvaa 44,204,228\nTa 144,16,32768\nMvaa 76,204,224\nTa 146,16,34816\nMvaa 80,208,224\nTa 148,17,32768\nMvaa 48,208,228\nD0 9,24,44,3072\nMvaa 208,208,232\nTa 150,17,34816\n"
"Mvaa 240,208,236\nTa 152,18,32768\nMvaa 244,212,236\nTa 154,18,34816\ns_add_u32 m0, s48, 0\ns_add_u32 s45, s45, 128\nMvaa 212,212,232\nTa 156,19,32768\nMvaa 52,212,228\n"
"Ta 158,19,34816\nD0 2,20,45,0\nMvaa 84,212,224\nMvaa 88,216,224\nMvaa 56,216,228\nMvaa 216,216,232\nMvaa 248,216,236\nD0 3,20,45,1024\nMvaa 252,220,236\nMvaa 220,220,232\n"
"Mvaa 60,220,228\nMvaa 92,220,224\nWL 0\nMvaa 64,128,160\nRa 224,141,32768\nD0 4,20,45,2048\nMvaa 32,128,164\nRa 228,141,34816\nMvaa 192,128,168\nRa 232,143,32768\n"
"Mvaa 224,128,172\nRa 236,143,34816\nMvaa 228,132,172\nTa 192,12,49152\nMvaa 196,132,168\nD0 5,20,45,3072\nMvaa 36,132,164\nTa 194,12,51200\nMvaa 68,132,160\nTa 196,13,49152\n"
"Mvaa 72,136,160\nTa 198,13,51200\ns_add_u32 m0, s48, 4096\ns_add_u32 s46, s45, 0x10000\nMvaa 40,136,164\nTa 200,14,49152\nMvaa 200,136,168\nTa 202,14,51200\nD0 2,20,46,0\n"
"Mvaa 232,136,172\nMvaa 236,140,172\nTa 204,15,49152\nMvaa 204,140,168\nTa 206,15,51200\nMvaa 44,140,164\nTa 208,16,49152\nMvaa 76,140,160\nTa 210,16,51200\nD0 3,20,46,1024\n"
"Mvaa 80,144,160\nTa 212,17,49152\nMvaa 48,144,164\nMvaa 208,144,168\nTa 214,17,51200\nMvaa 240,144,172\nTa 216,18,49152\nMvaa 244,148,172\nTa 218,18,51200\nD0 4,20,46,2048\n"
"Mvaa 212,148,168\nTa 220,19,49152\nMvaa 52,148,164\nTa 222,19,51200\nMvaa 84,148,160\nMvaa 88,152,160\nMvaa 56,152,164\nD0 5,20,46,3072\nMvaa 216,152,168\nMvaa 248,152,172\n"
"Mvaa 252,156,172\ns_add_u32 m0, s47, 16384\ns_add_u32 s44, s44, 0x40000\nMvaa 220,156,168\nMvaa 60,156,164\nD0 6,24,44,0\nMvaa 92,156,160\ns_waitcnt vmcnt(13) lgkmcnt(0)\n"
"s_barrier\nMvaa 64,192,224\nRa 160,140,49152\nMvaa 32,192,228\nRa 164,140,51200\nMvaa 192,192,232\nRa 168,142,49152\nMvaa 224,192,236\nRa 172,142,51200\nD0 7,24,44,1024\n"
"Mvaa 228,196,236\nTa 128,128,0\nMvaa 196,196,232\nMvaa 36,196,228\nTa 130,128,2048\nMvaa 68,196,224\nTa 132,129,0\nMvaa 72,200,224\nTa 134,129,2048\nD0 8,24,44,2048\n"
"Mvaa 40,200,228\nTa 136,130,0\nMvaa 200,200,232\nTa 138,130,2048\nMvaa 232,200,236\nMvaa 236,204,236\nTa 140,131,0\nMvaa 204,204,232\nTa 142,131,2048\nD0 9,24,44,3072\n"
"Mvaa 44,204,228\nTa 144,132,0\nMvaa 76,204,224\nTa 146,132,2048\nMvaa 80,208,224\nTa 148,133,0\ns_add_u32 m0, s47, 32768\ns_add_u32 s44, s44, 0x40000\nMvaa 48,208,228\n"
"Mvaa 208,208,232\nTa 150,133,2048\nD0 6,24,44,0\nMvaa 240,208,236\nTa 152,134,0\nMvaa 244,212,236\nTa 154,134,2048\nMvaa 212,212,232\nTa 156,135,0\nMvaa 52,212,228\n"
"Ta 158,135,2048\nMvaa 84,212,224\nD0 7,24,44,1024\nMvaa 88,216,224\nMvaa 56,216,228\nMvaa 216,216,232\nMvaa 248,216,236\nMvaa 252,220,236\nD0 8,24,44,2048\nMvaa 220,220,232\n"
"Mvaa 60,220,228\nMvaa 92,220,224\nWL 0\nMvaa 64,128,160\nRa 224,141,49152\nMvaa 32,128,164\nRa 228,141,51200\nD0 9,24,44,3072\nMvaa 192,128,168\nRa 232,143,49152\n"
"Mvaa 224,128,172\nRa 236,143,51200\nMvaa 228,132,172\nTa 192,128,16384\nMvaa 196,132,168\nMvaa 36,132,164\nTa 194,128,18432\nMvaa 68,132,160\nTa 196,129,16384\nMvaa 72,136,160\n"
"Ta 198,129,18432\nMvaa 40,136,164\nTa 200,130,16384\nMvaa 200,136,168\nTa 202,130,18432\nMvaa 232,136,172\nMvaa 236,140,172\nTa 204,131,16384\nMvaa 204,140,168\nTa 206,131,18432\n"
"Mvaa 44,140,164\nTa 208,132,16384\nMvaa 76,140,160\nTa 210,132,18432\nMvaa 80,144,160\nTa 212,133,16384\nMvaa 48,144,164\nMvaa 208,144,168\nTa 214,133,18432\nMvaa 240,144,172\n"
"Ta 216,134,16384\nMvaa 244,148,172\nTa 218,134,18432\nMvaa 212,148,168\nTa 220,135,16384\nMvaa 52,148,164\nTa 222,135,18432\nMvaa 84,148,160\nMvaa 88,152,160\nMvaa 56,152,164\n"
"Mvaa 216,152,168\nMvaa 248,152,172\nMvaa 252,156,172\nMvaa 220,156,168\nMvaa 60,156,164\nMvaa 92,156,160\ns_waitcnt vmcnt(8) lgkmcnt(0)\ns_barrier\nMvaa 64,192,224\n"
"s_add_u32 m0, s48, 32768\ns_add_u32 s45, s45, 128\nTa 128,12,0\nMvaa 68,196,224\nTa 130,12,2048\nMvaa 72,200,224\nD0 2,20,45,0\nTa 132,13,0\nMvaa 76,204,224\nTa 134,13,2048\n"
"Mvaa 80,208,224\nTa 136,14,0\nMvaa 84,212,224\nTa 138,14,2048\nMvaa 88,216,224\nTa 140,15,0\nv_mul_f32 v64, s57, v64\nMvaa 92,220,224\nD0 3,20,45,1024\nTa 142,15,2048\n"
"Mvaa 32,192,228\nTa 144,16,0\nv_mul_f32 v65, s57, v65\nMvaa 36,196,228\nTa 146,16,2048\nMvaa 40,200,228\nTa 148,17,0\nCV 64,64,65\nMvaa 44,204,228\nTa 150,17,2048\n"
"v_mul_f32 v66, s57, v66\nMvaa 48,208,228\nD0 4,20,45,2048\nTa 152,18,0\nMvaa 52,212,228\nTa 154,18,2048\nMvaa 56,216,228\nTa 156,19,0\nv_mul_f32 v67, s57, v67\nMvaa 60,220,228\n"
"Ta 158,19,2048\nCV 65,66,67\nMvaa 192,192,232\nRa 160,10,0\nv_mul_f32 v68, s57, v68\nMvaa 196,196,232\nD0 5,20,45,3072\nRa 164,10,2048\nMvaa 200,200,232\nRa 168,10,8192\n"
"v_mul_f32 v69, s57, v69\nMvaa 204,204,232\nRa 172,10,10240\nCV 66,68,69\nMvaa 208,208,232\nRa 176,10,16384\nv_mul_f32 v70, s57, v70\nMvaa 212,212,232\nRa 180,10,18432\n"
"Mvaa 216,216,232\nRa 184,10,24576\nv_mul_f32 v71, s57, v71\nMvaa 220,220,232\nRa 188,10,26624\nCV 67,70,71\nMvaa 224,192,236\nv_mul_f32 v72, s57, v72\nv_mul_f32 v73, s57, v73\n"
"Mvaa 228,196,236\nCV 72,72,73\nMvaa 232,200,236\nv_mul_f32 v74, s57, v74\nv_mul_f32 v75, s57, v75\nMvaa 236,204,236\nCV 73,74,75\nv_mul_f32 v76, s57, v76\nMvaa 240,208,236\n"
"v_mul_f32 v77, s57, v77\nCV 74,76,77\nMvaa 244,212,236\nv_mul_f32 v78, s57, v78\nMvaa 248,216,236\nv_mul_f32 v79, s57, v79\nCV 75,78,79\nMvaa 252,220,236\n"
"s_waitcnt vmcnt(8) lgkmcnt(0)\ns_barrier\nZaaa 0,128,176\nTa 192,12,16384\nv_mul_f32 v80, s57, v80\nZaaa 32,128,180\ns_add_u32 m0, s48, 36864\ns_add_u32 s46, s45, 0x10000\n"
"s_add_u32 s6, s56, 0\nSv 64,1,16,6,0\nZaaa 64,128,184\nTa 194,12,18432\nv_mul_f32 v81, s57, v81\nZaaa 96,128,188\nD0 2,20,46,0\nCV 80,80,81\nZaaa 100,132,188\nTa 196,13,16384\n"
"v_mul_f32 v82, s57, v82\nZaaa 68,132,184\nv_mul_f32 v83, s57, v83\nZaaa 36,132,180\nTa 198,13,18432\nCV 81,82,83\nZaaa 4,132,176\nD0 3,20,46,1024\nv_mul_f32 v84, s57, v84\n"
"Zaaa 8,136,176\nTa 200,14,16384\nv_mul_f32 v85, s57, v85\nZaaa 40,136,180\nCV 82,84,85\ns_add_u32 s6, s56, 0\nSv 72,1,16,6,2048\nZaaa 72,136,184\nTa 202,14,18432\n"
"v_mul_f32 v86, s57, v86\nZaaa 104,136,188\nD0 4,20,46,2048\nv_mul_f32 v87, s57, v87\nZaaa 108,140,188\nTa 204,15,16384\nCV 83,86,87\nZaaa 76,140,184\nv_mul_f32 v88, s57, v88\n"
"Zaaa 44,140,180\nTa 206,15,18432\nv_mul_f32 v89, s57, v89\nZaaa 12,140,176\nD0 5,20,46,3072\nCV 88,88,89\nZaaa 16,144,176\nTa 208,16,16384\nv_mul_f32 v90, s57, v90\n"
"Zaaa 48,144,180\nv_mul_f32 v91, s57, v91\nZaaa 80,144,184\nTa 210,16,18432\nCV 89,90,91\nZaaa 112,144,188\nv_mul_f32 v92, s57, v92\nv_mul_f32 v93, s57, v93\nZaaa 116,148,188\n"
"Ta 212,17,16384\nCV 90,92,93\nZaaa 84,148,184\nv_mul_f32 v94, s57, v94\nZaaa 52,148,180\nTa 214,17,18432\nv_mul_f32 v95, s57, v95\nZaaa 20,148,176\nCV 91,94,95\nZaaa 24,152,176\n"
"Ta 216,18,16384\nZaaa 56,152,180\ns_add_u32 s6, s56, 4096\nSv 80,1,16,6,0\nZaaa 88,152,184\nTa 218,18,18432\nZaaa 120,152,188\nZaaa 124,156,188\nTa 220,19,16384\nZaaa 92,156,184\n"
"s_add_u32 s6, s56, 4096\nSv 88,1,16,6,2048\nZaaa 60,156,180\nTa 222,19,18432\nZaaa 28,156,176\ns_add_u32 m0, s47, 49152\ns_add_u32 s44, s44, 0x40000\nZvaa 64,128,160\nRa 224,11,0\n"
"v_mul_f32 v32, s57, v32\nZvaa 96,128,164\nD0 6,24,44,0\nZvaa 128,128,168\nRa 228,11,2048\nZvaa 160,128,172\nv_mul_f32 v33, s57, v33\nZvaa 164,132,172\nRa 232,11,8192\nCV 32,32,33\n"
"Zvaa 132,132,168\nD0 7,24,44,1024\nZvaa 100,132,164\nRa 236,11,10240\nZvaa 68,132,160\nv_mul_f32 v34, s57, v34\nZvaa 72,136,160\nRa 240,11,16384\nv_mul_f32 v35, s57, v35\n"
"Zvaa 104,136,164\nD0 8,24,44,2048\nZvaa 136,136,168\nRa 244,11,18432\nZvaa 168,136,172\nCV 33,34,35\nZvaa 172,140,172\nRa 248,11,24576\nv_mul_f32 v36, s57, v36\nZvaa 140,140,168\n"
"D0 9,24,44,3072\nZvaa 108,140,164\nRa 252,11,26624\nZvaa 76,140,160\ns_add_u32 m0, s47, 0\ns_add_u32 s44, s44, 0x40000\nZvaa 80,144,160\nv_mul_f32 v37, s57, v37\nCV 34,36,37\n"
"Zvaa 112,144,164\nD0 6,24,44,0\nZvaa 144,144,168\nv_mul_f32 v38, s57, v38\nZvaa 176,144,172\nv_mul_f32 v39, s57, v39\nZvaa 180,148,172\nCV 35,38,39\nZvaa 148,148,168\n"
"D0 7,24,44,1024\ns_add_u32 s6, s56, 0\nSv 32,1,16,6,1024\nZvaa 116,148,164\nv_mul_f32 v40, s57, v40\nZvaa 84,148,160\nv_mul_f32 v41, s57, v41\nZvaa 88,152,160\nCV 40,40,41\n"
"v_mul_f32 v42, s57, v42\nZvaa 120,152,164\nD0 8,24,44,2048\nZvaa 152,152,168\nv_mul_f32 v43, s57, v43\nZvaa 184,152,172\nCV 41,42,43\nZvaa 188,156,172\nv_mul_f32 v44, s57, v44\n"
"v_mul_f32 v45, s57, v45\nZvaa 156,156,168\nD0 9,24,44,3072\nZvaa 124,156,164\nCV 42,44,45\nZvaa 92,156,160\ns_waitcnt vmcnt(11) lgkmcnt(0)\ns_barrier\nMvaa 64,192,224\n"
"Ta 128,12,32768\nv_mul_f32 v46, s57, v46\nMvaa 96,192,228\nv_mul_f32 v47, s57, v47\nMvaa 128,192,232\nTa 130,12,34816\nMvaa 160,192,236\ns_add_u32 m0, s48, 0\n"
"s_add_u32 s45, s45, 128\nMaaa 0,192,240\nTa 132,13,32768\nCV 43,46,47\nMaaa 32,192,244\nD0 2,20,45,0\ns_add_u32 s6, s56, 0\nSv 40,1,16,6,3072\nMaaa 64,192,248\nTa 134,13,34816\n"
"Maaa 96,192,252\nv_mul_f32 v48, s57, v48\nMaaa 100,196,252\nTa 136,14,32768\nv_mul_f32 v49, s57, v49\nMaaa 68,196,248\nCV 48,48,49\nMaaa 36,196,244\nTa 138,14,34816\n"
"Maaa 4,196,240\nv_mul_f32 v50, s57, v50\nMvaa 164,196,236\nTa 140,15,32768\nv_mul_f32 v51, s57, v51\nMvaa 132,196,232\nD0 3,20,45,1024\nMvaa 100,196,228\nTa 142,15,34816\n"
"Mvaa 68,196,224\nCV 49,50,51\nMvaa 72,200,224\nTa 144,16,32768\nv_mul_f32 v52, s57, v52\nMvaa 104,200,228\nv_mul_f32 v53, s57, v53\nMvaa 136,200,232\nTa 146,16,34816\n"
"Mvaa 168,200,236\nCV 50,52,53\nMaaa 8,200,240\nTa 148,17,32768\nv_mul_f32 v54, s57, v54\nMaaa 40,200,244\nD0 4,20,45,2048\nMaaa 72,200,248\nTa 150,17,34816\nMaaa 104,200,252\n"
"v_mul_f32 v55, s57, v55\nMaaa 108,204,252\nTa 152,18,32768\nCV 51,54,55\nMaaa 76,204,248\ns_add_u32 s6, s56, 4096\nSv 48,1,16,6,1024\nMaaa 44,204,244\nTa 154,18,34816\n"
"Maaa 12,204,240\nv_mul_f32 v56, s57, v56\nMvaa 172,204,236\nTa 156,19,32768\nv_mul_f32 v57, s57, v57\nMvaa 140,204,232\nD0 5,20,45,3072\nMvaa 108,204,228\nTa 158,19,34816\n"
"Mvaa 76,204,224\nCV 56,56,57\nMvaa 80,208,224\nRa 160,10,32768\nv_mul_f32 v58, s57, v58\nMvaa 112,208,228\nv_mul_f32 v59, s57, v59\nMvaa 144,208,232\nRa 164,10,34816\n"
"Mvaa 176,208,236\ns_add_u32 m0, s48, 4096\ns_add_u32 s46, s45, 0x10000\nMaaa 16,208,240\nRa 168,10,40960\nCV 57,58,59\nMaaa 48,208,244\nD0 2,20,46,0\nMaaa 80,208,248\n"
"Ra 172,10,43008\nMaaa 112,208,252\nv_mul_f32 v60, s57, v60\nMaaa 116,212,252\nRa 176,10,49152\nv_mul_f32 v61, s57, v61\nMaaa 84,212,248\nCV 58,60,61\nMaaa 52,212,244\n"
"Ra 180,10,51200\nMaaa 20,212,240\nv_mul_f32 v62, s57, v62\nMvaa 180,212,236\nRa 184,10,57344\nv_mul_f32 v63, s57, v63\nMvaa 148,212,232\nD0 3,20,46,1024\nMvaa 116,212,228\n"
"Ra 188,10,59392\nMvaa 84,212,224\nCV 59,62,63\ns_add_u32 s6, s56, 4096\nSv 56,1,16,6,3072\nMvaa 88,216,224\nv_mul_f32 v192, s57, v192\nv_mul_f32 v193, s57, v193\nMvaa 120,216,228\n"
"CV 192,192,193\nMvaa 152,216,232\nv_mul_f32 v194, s57, v194\nMvaa 184,216,236\nv_mul_f32 v195, s57, v195\nMaaa 24,216,240\nCV 193,194,195\nv_mul_f32 v196, s57, v196\n"
"Maaa 56,216,244\nD0 4,20,46,2048\nMaaa 88,216,248\nv_mul_f32 v197, s57, v197\nMaaa 120,216,252\nCV 194,196,197\nMaaa 124,220,252\nv_mul_f32 v198, s57, v198\n"
"v_mul_f32 v199, s57, v199\nMaaa 92,220,248\nCV 195,198,199\ns_add_u32 s6, s53, 0\nSv 192,1,16,6,0\nMaaa 60,220,244\nv_mul_f32 v200, s57, v200\nMaaa 28,220,240\n"
"v_mul_f32 v201, s57, v201\nMvaa 188,220,236\nCV 200,200,201\nv_mul_f32 v202, s57, v202\nMvaa 156,220,232\nD0 5,20,46,3072\nMvaa 124,220,228\nv_mul_f32 v203, s57, v203\n"
"Mvaa 92,220,224\ns_waitcnt vmcnt(17) lgkmcnt(0)\ns_barrier\nMvaa 64,128,160\nTa 192,12,49152\nCV 201,202,203\nMvaa 96,128,164\nv_mul_f32 v204, s57, v204\nMvaa 128,128,168\n"
"Ta 194,12,51200\nMvaa 160,128,172\ns_add_u32 m0, s47, 16384\ns_add_u32 s44, s44, 0x40000\nMaaa 0,128,176\nTa 196,13,49152\nv_mul_f32 v205, s57, v205\nMaaa 32,128,180\n"
"D0 6,24,44,0\nMaaa 64,128,184\nTa 198,13,51200\nMaaa 96,128,188\nCV 202,204,205\nMaaa 100,132,188\nTa 200,14,49152\nv_mul_f32 v206, s57, v206\nMaaa 68,132,184\n"
"v_mul_f32 v207, s57, v207\nMaaa 36,132,180\nTa 202,14,51200\nMaaa 4,132,176\nCV 203,206,207\ns_add_u32 s6, s53, 0\nSv 200,1,16,6,2048\nMvaa 164,132,172\nTa 204,15,49152\n"
"v_mul_f32 v208, s57, v208\nMvaa 132,132,168\nD0 7,24,44,1024\nMvaa 100,132,164\nTa 206,15,51200\nMvaa 68,132,160\nv_mul_f32 v209, s57, v209\nMvaa 72,136,160\nTa 208,16,49152\n"
"CV 208,208,209\nMvaa 104,136,164\nv_mul_f32 v210, s57, v210\nMvaa 136,136,168\nTa 210,16,51200\nMvaa 168,136,172\nv_mul_f32 v211, s57, v211\nMaaa 8,136,176\nTa 212,17,49152\n"
"CV 209,210,211\nMaaa 40,136,180\nD0 8,24,44,2048\nMaaa 72,136,184\nTa 214,17,51200\nMaaa 104,136,188\nv_mul_f32 v212, s57, v212\nMaaa 108,140,188\nTa 216,18,49152\n"
"v_mul_f32 v213, s57, v213\nMaaa 76,140,184\nCV 210,212,213\nMaaa 44,140,180\nTa 218,18,51200\nMaaa 12,140,176\nv_mul_f32 v214, s57, v214\nMvaa 172,140,172\nTa 220,19,49152\n"
"v_mul_f32 v215, s57, v215\nMvaa 140,140,168\nD0 9,24,44,3072\nMvaa 108,140,164\nTa 222,19,51200\nMvaa 76,140,160\nCV 211,214,215\ns_add_u32 s6, s53, 4096\nSv 208,1,16,6,0\n"
"Mvaa 80,144,160\nRa 224,11,32768\nv_mul_f32 v216, s57, v216\nMvaa 112,144,164\nv_mul_f32 v217, s57, v217\nMvaa 144,144,168\nRa 228,11,34816\nMvaa 176,144,172\n"
"s_add_u32 m0, s47, 32768\ns_add_u32 s44, s44, 0x40000\nMaaa 16,144,176\nRa 232,11,40960\nCV 216,216,217\nMaaa 48,144,180\nD0 6,24,44,0\nMaaa 80,144,184\nRa 236,11,43008\n"
"Maaa 112,144,188\nv_mul_f32 v218, s57, v218\nMaaa 116,148,188\nRa 240,11,49152\nv_mul_f32 v219, s57, v219\nMaaa 84,148,184\nCV 217,218,219\nMaaa 52,148,180\nRa 244,11,51200\n"
"Maaa 20,148,176\nv_mul_f32 v220, s57, v220\nMvaa 180,148,172\nRa 248,11,57344\nv_mul_f32 v221, s57, v221\nMvaa 148,148,168\nD0 7,24,44,1024\nMvaa 116,148,164\nRa 252,11,59392\n"
"Mvaa 84,148,160\nCV 218,220,221\nMvaa 88,152,160\nv_mul_f32 v222, s57, v222\nv_mul_f32 v223, s57, v223\nMvaa 120,152,164\nCV 219,222,223\ns_add_u32 s6, s53, 4096\n"
"Sv 216,1,16,6,2048\nMvaa 152,152,168\nv_mul_f32 v224, s57, v224\nMvaa 184,152,172\nv_mul_f32 v225, s57, v225\nMaaa 24,152,176\nCV 224,224,225\nv_mul_f32 v226, s57, v226\n"
"Maaa 56,152,180\nD0 8,24,44,2048\nMaaa 88,152,184\nv_mul_f32 v227, s57, v227\nMaaa 120,152,188\nCV 225,226,227\nMaaa 124,156,188\nv_mul_f32 v228, s57, v228\n"
"v_mul_f32 v229, s57, v229\nMaaa 92,156,184\nCV 226,228,229\nMaaa 60,156,180\nv_mul_f32 v230, s57, v230\nMaaa 28,156,176\nv_mul_f32 v231, s57, v231\nMvaa 188,156,172\n"
"CV 227,230,231\nMvaa 156,156,168\nD0 9,24,44,3072\ns_add_u32 s6, s53, 0\nSv 224,1,16,6,1024\nMvaa 124,156,164\nv_mul_f32 v232, s57, v232\nMvaa 92,156,160\n"
"s_waitcnt vmcnt(12) lgkmcnt(0)\ns_barrier\nMvaa 64,192,224\nTa 128,12,0\nv_mul_f32 v233, s57, v233\nMvaa 96,192,228\nCV 232,232,233\nMvaa 128,192,232\nTa 130,12,2048\n"
"Mvaa 160,192,236\ns_add_u32 m0, s48, 32768\ns_add_u32 s45, s45, 128\nMaaa 0,192,240\nTa 132,13,0\nv_mul_f32 v234, s57, v234\nMaaa 32,192,244\nD0 2,20,45,0\nMaaa 64,192,248\n"
"Ta 134,13,2048\nMaaa 96,192,252\nv_mul_f32 v235, s57, v235\nMaaa 100,196,252\nTa 136,14,0\nCV 233,234,235\nMaaa 68,196,248\nv_mul_f32 v236, s57, v236\nMaaa 36,196,244\n"
"Ta 138,14,2048\nMaaa 4,196,240\nv_mul_f32 v237, s57, v237\nMvaa 164,196,236\nTa 140,15,0\nCV 234,236,237\nMvaa 132,196,232\nD0 3,20,45,1024\nMvaa 100,196,228\nTa 142,15,2048\n"
"Mvaa 68,196,224\nv_mul_f32 v238, s57, v238\nMvaa 72,200,224\nTa 144,16,0\nv_mul_f32 v239, s57, v239\nMvaa 104,200,228\nCV 235,238,239\ns_add_u32 s6, s53, 0\nSv 232,1,16,6,3072\n"
"Mvaa 136,200,232\nTa 146,16,2048\nMvaa 168,200,236\nv_mul_f32 v240, s57, v240\nMaaa 8,200,240\nTa 148,17,0\nv_mul_f32 v241, s57, v241\nMaaa 40,200,244\nD0 4,20,45,2048\n"
"Maaa 72,200,248\nTa 150,17,2048\nMaaa 104,200,252\nCV 240,240,241\nMaaa 108,204,252\nTa 152,18,0\nv_mul_f32 v242, s57, v242\nMaaa 76,204,248\nv_mul_f32 v243, s57, v243\n"
"Maaa 44,204,244\nTa 154,18,2048\nMaaa 12,204,240\nCV 241,242,243\nMvaa 172,204,236\nTa 156,19,0\nv_mul_f32 v244, s57, v244\nMvaa 140,204,232\nD0 5,20,45,3072\nMvaa 108,204,228\n"
"Ta 158,19,2048\nMvaa 76,204,224\nv_mul_f32 v245, s57, v245\nMvaa 80,208,224\nRa 160,10,0\nCV 242,244,245\nMvaa 112,208,228\nv_mul_f32 v246, s57, v246\nMvaa 144,208,232\n"
"Ra 164,10,2048\nMvaa 176,208,236\ns_add_u32 m0, s48, 36864\ns_add_u32 s46, s45, 0x10000\nMaaa 16,208,240\nRa 168,10,8192\nv_mul_f32 v247, s57, v247\nMaaa 48,208,244\nD0 2,20,46,0\n"
"Maaa 80,208,248\nRa 172,10,10240\nMaaa 112,208,252\nCV 243,246,247\ns_add_u32 s6, s53, 4096\nSv 240,1,16,6,1024\nMaaa 116,212,252\nRa 176,10,16384\nv_mul_f32 v248, s57, v248\n"
"Maaa 84,212,248\nv_mul_f32 v249, s57, v249\nMaaa 52,212,244\nRa 180,10,18432\nMaaa 20,212,240\nCV 248,248,249\nMvaa 180,212,236\nRa 184,10,24576\nv_mul_f32 v250, s57, v250\n"
"Mvaa 148,212,232\nD0 3,20,46,1024\nMvaa 116,212,228\nRa 188,10,26624\nMvaa 84,212,224\nv_mul_f32 v251, s57, v251\nMvaa 88,216,224\nCV 249,250,251\nv_mul_f32 v252, s57, v252\n"
"Mvaa 120,216,228\nv_mul_f32 v253, s57, v253\nMvaa 152,216,232\nCV 250,252,253\nMvaa 184,216,236\nv_mul_f32 v254, s57, v254\nMaaa 24,216,240\nv_mul_f32 v255, s57, v255\n"
"CV 251,254,255\nMaaa 56,216,244\nD0 4,20,46,2048\ns_add_u32 s6, s53, 4096\nSv 248,1,16,6,3072\nMaaa 88,216,248\nMaaa 120,216,252\nMaaa 124,220,252\nMaaa 92,220,248\n"
"Maaa 60,220,244\nMaaa 28,220,240\ns_lshl3_add_u32 s90, s12, s13\ns_lshl2_add_u32 s90, s90, s11\ns_lshl2_add_u32 s90, s90, 0xc04a400\nMvaa 188,220,236\nMvaa 156,220,232\n"
"D0 5,20,46,3072\nMvaa 124,220,228\nMvaa 92,220,224\ns_waitcnt vmcnt(18) lgkmcnt(0)\ns_barrier\ns_mov_b64 s[76:77], s[62:63]\ns_mov_b64 s[78:79], s[64:65]\ns_mov_b32 s42, 1605632\n"
"s_mov_b32 s50, 1\n.GL20:\nMvaa 64,128,160\nTa 192,12,16384\nMvaa 96,128,164\nMvaa 128,128,168\nTa 194,12,18432\nMvaa 160,128,172\ns_add_u32 m0, s47, 49152\n"
"s_add_u32 s44, s44, 0x40000\nMaaa 0,128,176\nTa 196,13,16384\nMaaa 32,128,180\nD0 6,24,44,0\nMaaa 64,128,184\nTa 198,13,18432\nMaaa 96,128,188\nMaaa 100,132,188\nTa 200,14,16384\n"
"Maaa 68,132,184\nMaaa 36,132,180\nTa 202,14,18432\nMaaa 4,132,176\nMvaa 164,132,172\nTa 204,15,16384\nMvaa 132,132,168\nD0 7,24,44,1024\nMvaa 100,132,164\nTa 206,15,18432\n"
"Mvaa 68,132,160\nMvaa 72,136,160\nTa 208,16,16384\nMvaa 104,136,164\nMvaa 136,136,168\nTa 210,16,18432\nMvaa 168,136,172\nMaaa 8,136,176\nTa 212,17,16384\nMaaa 40,136,180\n"
"D0 8,24,44,2048\nMaaa 72,136,184\nTa 214,17,18432\nMaaa 104,136,188\nMaaa 108,140,188\nTa 216,18,16384\nMaaa 76,140,184\nMaaa 44,140,180\nTa 218,18,18432\nMaaa 12,140,176\n"
"Mvaa 172,140,172\nTa 220,19,16384\nMvaa 140,140,168\nD0 9,24,44,3072\nMvaa 108,140,164\nTa 222,19,18432\nMvaa 76,140,160\ns_mov_b32 s40, s76\ns_and_b32 s41, s77, 0xffff\n"
"Mvaa 80,144,160\nRa 224,11,0\nMvaa 112,144,164\nbuffer_load_dword v31, v26, s[40:43], s97 offen\ns_mov_b64 s[76:77], s[66:67]\nMvaa 144,144,168\nRa 228,11,2048\nMvaa 176,144,172\n"
"s_add_u32 m0, s47, 0\ns_add_u32 s44, s44, 0x40000\nMaaa 16,144,176\nRa 232,11,8192\nMaaa 48,144,180\nD0 6,24,44,0\nMaaa 80,144,184\nRa 236,11,10240\nMaaa 112,144,188\n"
"Maaa 116,148,188\nRa 240,11,16384\nMaaa 84,148,184\nMaaa 52,148,180\nRa 244,11,18432\nMaaa 20,148,176\nMvaa 180,148,172\nRa 248,11,24576\nMvaa 148,148,168\nD0 7,24,44,1024\n"
"Mvaa 116,148,164\nRa 252,11,26624\nMvaa 84,148,160\nMvaa 88,152,160\nMvaa 120,152,164\nMvaa 152,152,168\nMvaa 184,152,172\nMaaa 24,152,176\nMaaa 56,152,180\nD0 8,24,44,2048\n"
"Maaa 88,152,184\nMaaa 120,152,188\nMaaa 124,156,188\nMaaa 92,156,184\nMaaa 60,156,180\nMaaa 28,156,176\nMvaa 188,156,172\nMvaa 156,156,168\nD0 9,24,44,3072\nMvaa 124,156,164\n"
"Mvaa 92,156,160\ns_waitcnt vmcnt(9) lgkmcnt(0)\ns_barrier\nMvaa 64,192,224\nTa 128,12,32768\nMvaa 96,192,228\nMvaa 128,192,232\nTa 130,12,34816\nMvaa 160,192,236\n"
"s_add_u32 m0, s48, 0\ns_add_u32 s45, s45, 128\nMaaa 0,192,240\nTa 132,13,32768\nMaaa 32,192,244\nD0 2,20,45,0\nMaaa 64,192,248\nTa 134,13,34816\nMaaa 96,192,252\nMaaa 100,196,252\n"
"Ta 136,14,32768\nMaaa 68,196,248\nMaaa 36,196,244\nTa 138,14,34816\nMaaa 4,196,240\nMvaa 164,196,236\nTa 140,15,32768\nMvaa 132,196,232\nD0 3,20,45,1024\nMvaa 100,196,228\n"
"Ta 142,15,34816\nMvaa 68,196,224\nMvaa 72,200,224\nTa 144,16,32768\nMvaa 104,200,228\nMvaa 136,200,232\nTa 146,16,34816\nMvaa 168,200,236\nMaaa 8,200,240\nTa 148,17,32768\n"
"Maaa 40,200,244\nD0 4,20,45,2048\nMaaa 72,200,248\nTa 150,17,34816\nMaaa 104,200,252\nMaaa 108,204,252\nTa 152,18,32768\nMaaa 76,204,248\nMaaa 44,204,244\nTa 154,18,34816\n"
"Maaa 12,204,240\nMvaa 172,204,236\nTa 156,19,32768\nMvaa 140,204,232\nD0 5,20,45,3072\nMvaa 108,204,228\nTa 158,19,34816\nMvaa 76,204,224\nMvaa 80,208,224\nRa 160,10,32768\n"
"Mvaa 112,208,228\nMvaa 144,208,232\nRa 164,10,34816\nMvaa 176,208,236\ns_add_u32 m0, s48, 4096\ns_add_u32 s46, s45, 0x10000\nMaaa 16,208,240\nRa 168,10,40960\nMaaa 48,208,244\n"
"D0 2,20,46,0\nMaaa 80,208,248\nRa 172,10,43008\nMaaa 112,208,252\nMaaa 116,212,252\nRa 176,10,49152\nMaaa 84,212,248\nMaaa 52,212,244\nRa 180,10,51200\nMaaa 20,212,240\n"
"Mvaa 180,212,236\nRa 184,10,57344\nMvaa 148,212,232\nD0 3,20,46,1024\nMvaa 116,212,228\nRa 188,10,59392\nMvaa 84,212,224\nMvaa 88,216,224\nMvaa 120,216,228\nMvaa 152,216,232\n"
"Mvaa 184,216,236\nMaaa 24,216,240\nMaaa 56,216,244\nD0 4,20,46,2048\nMaaa 88,216,248\nMaaa 120,216,252\nMaaa 124,220,252\nMaaa 92,220,248\nMaaa 60,220,244\nMaaa 28,220,240\n"
"Mvaa 188,220,236\nMvaa 156,220,232\nD0 5,20,46,3072\nMvaa 124,220,228\nMvaa 92,220,224\ns_waitcnt vmcnt(13) lgkmcnt(0)\ns_barrier\nMvaa 64,128,160\nTa 192,12,49152\n"
"Mvaa 96,128,164\nMvaa 128,128,168\nTa 194,12,51200\nMvaa 160,128,172\ns_add_u32 m0, s47, 16384\ns_add_u32 s44, s44, 0x40000\nMaaa 0,128,176\nTa 196,13,49152\nMaaa 32,128,180\n"
"D0 6,24,44,0\nMaaa 64,128,184\nTa 198,13,51200\nMaaa 96,128,188\nMaaa 100,132,188\nTa 200,14,49152\nMaaa 68,132,184\nMaaa 36,132,180\nTa 202,14,51200\nMaaa 4,132,176\n"
"Mvaa 164,132,172\nTa 204,15,49152\nMvaa 132,132,168\nD0 7,24,44,1024\nMvaa 100,132,164\nTa 206,15,51200\nMvaa 68,132,160\nMvaa 72,136,160\nTa 208,16,49152\nMvaa 104,136,164\n"
"Mvaa 136,136,168\nTa 210,16,51200\nMvaa 168,136,172\nMaaa 8,136,176\nTa 212,17,49152\nMaaa 40,136,180\nD0 8,24,44,2048\nMaaa 72,136,184\nTa 214,17,51200\nMaaa 104,136,188\n"
"Maaa 108,140,188\nTa 216,18,49152\nMaaa 76,140,184\nMaaa 44,140,180\nTa 218,18,51200\nMaaa 12,140,176\nMvaa 172,140,172\nTa 220,19,49152\nMvaa 140,140,168\nD0 9,24,44,3072\n"
"Mvaa 108,140,164\nTa 222,19,51200\nMvaa 76,140,160\ns_mov_b32 s40, s78\ns_and_b32 s41, s79, 0xffff\nMvaa 80,144,160\nRa 224,11,32768\nMvaa 112,144,164\n"
"buffer_load_dword v31, v26, s[40:43], s97 offen\ns_mov_b64 s[78:79], s[66:67]\nMvaa 144,144,168\nRa 228,11,34816\nMvaa 176,144,172\ns_add_u32 m0, s47, 32768\n"
"s_add_u32 s44, s44, 0x40000\nMaaa 16,144,176\nRa 232,11,40960\nMaaa 48,144,180\nD0 6,24,44,0\nMaaa 80,144,184\nRa 236,11,43008\nMaaa 112,144,188\nMaaa 116,148,188\n"
"Ra 240,11,49152\nMaaa 84,148,184\nMaaa 52,148,180\nRa 244,11,51200\nMaaa 20,148,176\nMvaa 180,148,172\nRa 248,11,57344\nMvaa 148,148,168\nD0 7,24,44,1024\nMvaa 116,148,164\n"
"Ra 252,11,59392\nMvaa 84,148,160\nMvaa 88,152,160\nMvaa 120,152,164\nMvaa 152,152,168\nMvaa 184,152,172\nMaaa 24,152,176\nMaaa 56,152,180\nD0 8,24,44,2048\nMaaa 88,152,184\n"
"Maaa 120,152,188\nMaaa 124,156,188\nMaaa 92,156,184\nMaaa 60,156,180\nMaaa 28,156,176\nMvaa 188,156,172\nMvaa 156,156,168\nD0 9,24,44,3072\nMvaa 124,156,164\nMvaa 92,156,160\n"
"s_waitcnt vmcnt(9) lgkmcnt(0)\ns_barrier\nMvaa 64,192,224\nTa 128,12,0\nMvaa 96,192,228\nMvaa 128,192,232\nTa 130,12,2048\nMvaa 160,192,236\ns_add_u32 m0, s48, 32768\n"
"s_add_u32 s45, s45, 128\nMaaa 0,192,240\nTa 132,13,0\nMaaa 32,192,244\nD0 2,20,45,0\nMaaa 64,192,248\nTa 134,13,2048\nMaaa 96,192,252\nMaaa 100,196,252\nTa 136,14,0\n"
"Maaa 68,196,248\nMaaa 36,196,244\nTa 138,14,2048\nMaaa 4,196,240\nMvaa 164,196,236\nTa 140,15,0\nMvaa 132,196,232\nD0 3,20,45,1024\nMvaa 100,196,228\nTa 142,15,2048\n"
"Mvaa 68,196,224\nMvaa 72,200,224\nTa 144,16,0\nMvaa 104,200,228\nMvaa 136,200,232\nTa 146,16,2048\nMvaa 168,200,236\nMaaa 8,200,240\nTa 148,17,0\nMaaa 40,200,244\nD0 4,20,45,2048\n"
"Maaa 72,200,248\nTa 150,17,2048\nMaaa 104,200,252\nMaaa 108,204,252\nTa 152,18,0\nMaaa 76,204,248\nMaaa 44,204,244\nTa 154,18,2048\nMaaa 12,204,240\nMvaa 172,204,236\nTa 156,19,0\n"
"Mvaa 140,204,232\nD0 5,20,45,3072\nMvaa 108,204,228\nTa 158,19,2048\nMvaa 76,204,224\nMvaa 80,208,224\nRa 160,10,0\nMvaa 112,208,228\nMvaa 144,208,232\nRa 164,10,2048\n"
"Mvaa 176,208,236\ns_add_u32 m0, s48, 36864\ns_add_u32 s46, s45, 0x10000\nMaaa 16,208,240\nRa 168,10,8192\nMaaa 48,208,244\nD0 2,20,46,0\nMaaa 80,208,248\nRa 172,10,10240\n"
"Maaa 112,208,252\nMaaa 116,212,252\nRa 176,10,16384\nMaaa 84,212,248\nMaaa 52,212,244\nRa 180,10,18432\nMaaa 20,212,240\nMvaa 180,212,236\nRa 184,10,24576\nMvaa 148,212,232\n"
"D0 3,20,46,1024\nMvaa 116,212,228\nRa 188,10,26624\nMvaa 84,212,224\nMvaa 88,216,224\nMvaa 120,216,228\nMvaa 152,216,232\nMvaa 184,216,236\nMaaa 24,216,240\nMaaa 56,216,244\n"
"D0 4,20,46,2048\nMaaa 88,216,248\nMaaa 120,216,252\nMaaa 124,220,252\nMaaa 92,220,248\nMaaa 60,220,244\nMaaa 28,220,240\nMvaa 188,220,236\nMvaa 156,220,232\nD0 5,20,46,3072\n"
"s_sub_u32 s50, s50, 1\nMvaa 124,220,228\nMvaa 92,220,224\ns_waitcnt vmcnt(13) lgkmcnt(0)\ns_barrier\ns_cbranch_scc0 .GL20\nMvaa 64,128,160\nTa 192,12,16384\ns_and_b32 s2, s74, 15\n"
"Mvaa 96,128,164\ns_sub_u32 s2, s2, 2\ns_cmp_lt_u32 s2, 14\ns_cselect_b32 s5, 0, 0x40000000\nMvaa 128,128,168\nTa 194,12,18432\nMvaa 160,128,172\ns_add_u32 m0, s47, 49152\n"
"s_add_u32 s44, s44, 0x40000\nMaaa 0,128,176\nTa 196,13,16384\nMaaa 32,128,180\nD0 6,24,44,0\nMaaa 64,128,184\nTa 198,13,18432\nMaaa 96,128,188\ns_and_b32 s2, s2, 15\n"
"Maaa 100,132,188\nTa 200,14,16384\ns_mul_i32 s4, s73, 14\nMaaa 68,132,184\ns_add_u32 s4, s4, s2\nbuffer_store_dword v25, off, s[16:19], s90 offset:0 sc0 sc1\nMaaa 36,132,180\n"
"Ta 202,14,18432\nMaaa 4,132,176\ns_mov_b32 s36, s62\nMvaa 164,132,172\nTa 204,15,16384\nMvaa 132,132,168\nD0 7,24,44,1024\nMvaa 100,132,164\nTa 206,15,18432\nMvaa 68,132,160\n"
"s_and_b32 s37, s63, 0xffff\nMvaa 72,136,160\nTa 208,16,16384\ns_mov_b32 s38, 0x188000\nMvaa 104,136,164\ns_mov_b32 s39, 0x20000\nMvaa 136,136,168\nTa 210,16,18432\n"
"Mvaa 168,136,172\ns_mov_b32 s40, s64\nMaaa 8,136,176\nTa 212,17,16384\nMaaa 40,136,180\nD0 8,24,44,2048\nMaaa 72,136,184\nTa 214,17,18432\nMaaa 104,136,188\n"
"s_and_b32 s41, s65, 0xffff\nMaaa 108,140,188\nTa 216,18,16384\ns_mov_b32 s42, 0x188000\nMaaa 76,140,184\ns_mov_b32 s43, 0x20000\nMaaa 44,140,180\nTa 218,18,18432\nMaaa 12,140,176\n"
"s_mov_b32 s92, s66\nMvaa 172,140,172\nTa 220,19,16384\nMvaa 140,140,168\nD0 9,24,44,3072\nMvaa 108,140,164\nTa 222,19,18432\nMvaa 76,140,160\ns_and_b32 s93, s67, 0xffff\n"
"Mvaa 80,144,160\nRa 224,11,0\ns_mov_b32 s94, 0x188000\nMvaa 112,144,164\ns_mov_b32 s95, 0x20000\nMvaa 144,144,168\nRa 228,11,2048\nMvaa 176,144,172\ns_add_u32 m0, s47, 0\n"
"s_add_u32 s44, s44, 0x40000\nMaaa 16,144,176\nRa 232,11,8192\nMaaa 48,144,180\nD0 6,24,44,0\nMaaa 80,144,184\nRa 236,11,10240\nMaaa 112,144,188\nv_lshrrev_b32 v252, 1, v0\n"
"Maaa 116,148,188\nRa 240,11,16384\nv_mul_u32_u24 v253, 14336, v252\nMaaa 84,148,184\nv_and_b32 v254, 1, v0\nMaaa 52,148,180\nRa 244,11,18432\nMaaa 20,148,176\n"
"v_lshl_add_u32 v253, v254, 4, v253\nMvaa 180,148,172\nRa 248,11,24576\nMvaa 148,148,168\nD0 7,24,44,1024\nMvaa 116,148,164\nRa 252,11,26624\nMvaa 84,148,160\n"
"v_mov_b32 v254, 0x40000000\nv_cmp_gt_u32 vcc, 56, v0\nv_lshlrev_b32 v252, 3, v252\ns_mul_i32 s2, s11, 401408\nv_cndmask_b32 v21, v254, v253, vcc\n"
"v_cndmask_b32 v255, v254, v252, vcc\nMvaa 88,152,160\ns_lshl_b32 s3, s4, 5\ns_add_u32 s2, s2, s3\nMvaa 120,152,164\ns_or_b32 s86, s2, s5\nMvaa 152,152,168\n"
"s_add_u32 s87, s86, 7168\nMvaa 184,152,172\nv_lshrrev_b32 v251, 4, v0\nMaaa 24,152,176\nv_mul_u32_u24 v252, 3584, v251\nMaaa 56,152,180\nD0 8,24,44,2048\nMaaa 88,152,184\n"
"v_and_b32 v251, 15, v0\nMaaa 120,152,188\nv_lshl_add_u32 v252, v251, 4, v252\nv_cmp_gt_u32 vcc, 14, v251\ns_mul_i32 s2, s4, 14336\ns_mul_i32 s3, s11, 224\n"
"v_cndmask_b32 v20, v254, v252, vcc\nMaaa 124,156,188\ns_add_u32 s2, s2, s3\ns_or_b32 s88, s2, s5\nMaaa 92,156,184\nMaaa 60,156,180\nMaaa 28,156,176\nMvaa 188,156,172\n"
"Mvaa 156,156,168\nD0 9,24,44,3072\nMvaa 124,156,164\nMvaa 92,156,160\ns_waitcnt vmcnt(9) lgkmcnt(0)\ns_barrier\nMvaa 64,192,224\nTa 128,12,32768\ns_lshl_b32 s6, s11, 12\n"
"s_add_u32 s6, s6, 0xc044000\nMvaa 96,192,228\nbuffer_load_dwordx4 v[192:195], v21, s[36:39], s86 offen offset:0\nMvaa 128,192,232\nTa 130,12,34816\nMvaa 160,192,236\n"
"s_add_u32 m0, s48, 0\ns_add_u32 s45, s45, 128\nMaaa 0,192,240\nTa 132,13,32768\nMaaa 32,192,244\nD0 2,20,45,0\nMaaa 64,192,248\nTa 134,13,34816\nMaaa 96,192,252\n"
"buffer_load_dwordx4 v[32:35], v1, s[16:19], s6 offen offset:0 sc0 sc1\nMaaa 100,196,252\nTa 136,14,32768\nMaaa 68,196,248\nMaaa 36,196,244\nTa 138,14,34816\nMaaa 4,196,240\n"
"buffer_load_dwordx4 v[36:39], v1, s[16:19], s6 offen offset:1024 sc0 sc1\nMvaa 164,196,236\nTa 140,15,32768\nMvaa 132,196,232\nD0 3,20,45,1024\nMvaa 100,196,228\nTa 142,15,34816\n"
"Mvaa 68,196,224\nbuffer_load_dwordx4 v[40:43], v1, s[16:19], s6 offen offset:2048 sc0 sc1\nMvaa 72,200,224\nTa 144,16,32768\nMvaa 104,200,228\nMvaa 136,200,232\nTa 146,16,34816\n"
"Mvaa 168,200,236\nbuffer_load_dwordx4 v[44:47], v1, s[16:19], s6 offen offset:3072 sc0 sc1\nMaaa 8,200,240\nTa 148,17,32768\nMaaa 40,200,244\nD0 4,20,45,2048\nMaaa 72,200,248\n"
"Ta 150,17,34816\nMaaa 104,200,252\nMaaa 108,204,252\nTa 152,18,32768\nMaaa 76,204,248\nMaaa 44,204,244\nTa 154,18,34816\nMaaa 12,204,240\nMvaa 172,204,236\nTa 156,19,32768\n"
"Mvaa 140,204,232\nD0 5,20,45,3072\nMvaa 108,204,228\nTa 158,19,34816\nMvaa 76,204,224\nMvaa 80,208,224\nRa 160,10,32768\nMvaa 112,208,228\nMvaa 144,208,232\nRa 164,10,34816\n"
"Mvaa 176,208,236\ns_add_u32 m0, s48, 4096\ns_add_u32 s46, s45, 0x10000\nMaaa 16,208,240\nRa 168,10,40960\nMaaa 48,208,244\nD0 2,20,46,0\nMaaa 80,208,248\nRa 172,10,43008\n"
"Maaa 112,208,252\nMaaa 116,212,252\nRa 176,10,49152\nMaaa 84,212,248\nMaaa 52,212,244\nRa 180,10,51200\nMaaa 20,212,240\nMvaa 180,212,236\nRa 184,10,57344\nMvaa 148,212,232\n"
"D0 3,20,46,1024\nMvaa 116,212,228\nRa 188,10,59392\nMvaa 84,212,224\nMvaa 88,216,224\nMvaa 120,216,228\nMvaa 152,216,232\nMvaa 184,216,236\nMaaa 24,216,240\nMaaa 56,216,244\n"
"D0 4,20,46,2048\nMaaa 88,216,248\nMaaa 120,216,252\nMaaa 124,220,252\nMaaa 92,220,248\nMaaa 60,220,244\nMaaa 28,220,240\nMvaa 188,220,236\nMvaa 156,220,232\nD0 5,20,46,3072\n"
"Mvaa 124,220,228\nMvaa 92,220,224\ns_waitcnt vmcnt(17) lgkmcnt(0)\ns_barrier\nMvaa 64,128,160\nTa 192,12,49152\nMvaa 96,128,164\n"
"buffer_load_dwordx4 v[208:211], v21, s[40:43], s86 offen offset:0\nMvaa 128,128,168\nTa 194,12,51200\nMvaa 160,128,172\ns_add_u32 m0, s47, 16384\ns_add_u32 s44, s44, 0x40000\n"
"Maaa 0,128,176\nTa 196,13,49152\nMaaa 32,128,180\nD0 6,24,44,0\nMaaa 64,128,184\nTa 198,13,51200\nMaaa 96,128,188\nMaaa 100,132,188\nTa 200,14,49152\nMaaa 68,132,184\n"
"Maaa 36,132,180\nTa 202,14,51200\nMaaa 4,132,176\nMvaa 164,132,172\nTa 204,15,49152\nMvaa 132,132,168\nD0 7,24,44,1024\nMvaa 100,132,164\nTa 206,15,51200\nMvaa 68,132,160\n"
"Mvaa 72,136,160\nTa 208,16,49152\nMvaa 104,136,164\nMvaa 136,136,168\nTa 210,16,51200\nMvaa 168,136,172\nMaaa 8,136,176\nTa 212,17,49152\nMaaa 40,136,180\nD0 8,24,44,2048\n"
"Maaa 72,136,184\nTa 214,17,51200\nMaaa 104,136,188\nMaaa 108,140,188\nTa 216,18,49152\nMaaa 76,140,184\nMaaa 44,140,180\nTa 218,18,51200\nMaaa 12,140,176\nMvaa 172,140,172\n"
"Ta 220,19,49152\nMvaa 140,140,168\nD0 9,24,44,3072\nMvaa 108,140,164\nTa 222,19,51200\nMvaa 76,140,160\nMvaa 80,144,160\nRa 224,11,32768\nMvaa 112,144,164\nMvaa 144,144,168\n"
"Ra 228,11,34816\nMvaa 176,144,172\ns_add_u32 m0, s47, 32768\ns_add_u32 s44, s44, 0x40000\nMaaa 16,144,176\nRa 232,11,40960\nMaaa 48,144,180\nD0 6,24,44,0\nMaaa 80,144,184\n"
"Ra 236,11,43008\nMaaa 112,144,188\nMaaa 116,148,188\nRa 240,11,49152\nMaaa 84,148,184\nMaaa 52,148,180\nRa 244,11,51200\nMaaa 20,148,176\nMvaa 180,148,172\nRa 248,11,57344\n"
"Mvaa 148,148,168\nD0 7,24,44,1024\nMvaa 116,148,164\nRa 252,11,59392\nMvaa 84,148,160\nMvaa 88,152,160\nMvaa 120,152,164\nMvaa 152,152,168\nMvaa 184,152,172\nMaaa 24,152,176\n"
"Maaa 56,152,180\nD0 8,24,44,2048\nMaaa 88,152,184\nMaaa 120,152,188\nMaaa 124,156,188\nMaaa 92,156,184\nMaaa 60,156,180\nMaaa 28,156,176\nMvaa 188,156,172\nMvaa 156,156,168\n"
"D0 9,24,44,3072\nMvaa 124,156,164\nMvaa 92,156,160\ns_waitcnt vmcnt(9) lgkmcnt(0)\ns_barrier\nMvaa 64,192,224\nTa 128,12,0\nMvaa 96,192,228\n"
"buffer_load_dwordx4 v[196:199], v21, s[36:39], s86 offen offset:3584\nMvaa 128,192,232\nTa 130,12,2048\nMvaa 160,192,236\ns_add_u32 m0, s48, 32768\ns_add_u32 s45, s45, 128\n"
"Maaa 0,192,240\nTa 132,13,0\nMaaa 32,192,244\nD0 2,20,45,0\nMaaa 64,192,248\nTa 134,13,2048\nMaaa 96,192,252\nMaaa 100,196,252\nTa 136,14,0\nMaaa 68,196,248\nMaaa 36,196,244\n"
"Ta 138,14,2048\nMaaa 4,196,240\nMvaa 164,196,236\nTa 140,15,0\nMvaa 132,196,232\nD0 3,20,45,1024\nMvaa 100,196,228\nTa 142,15,2048\nMvaa 68,196,224\nMvaa 72,200,224\nTa 144,16,0\n"
"Mvaa 104,200,228\nMvaa 136,200,232\nTa 146,16,2048\nMvaa 168,200,236\nMaaa 8,200,240\nTa 148,17,0\nMaaa 40,200,244\nD0 4,20,45,2048\nMaaa 72,200,248\nTa 150,17,2048\n"
"Maaa 104,200,252\nMaaa 108,204,252\nTa 152,18,0\nMaaa 76,204,248\nMaaa 44,204,244\nTa 154,18,2048\nMaaa 12,204,240\nMvaa 172,204,236\nTa 156,19,0\nMvaa 140,204,232\n"
"D0 5,20,45,3072\nMvaa 108,204,228\nTa 158,19,2048\nMvaa 76,204,224\nMvaa 80,208,224\nRa 160,10,0\nMvaa 112,208,228\nMvaa 144,208,232\nRa 164,10,2048\nMvaa 176,208,236\n"
"s_add_u32 m0, s48, 36864\ns_add_u32 s46, s45, 0x10000\nMaaa 16,208,240\nRa 168,10,8192\nMaaa 48,208,244\nD0 2,20,46,0\nMaaa 80,208,248\nRa 172,10,10240\nMaaa 112,208,252\n"
"Maaa 116,212,252\nRa 176,10,16384\nMaaa 84,212,248\nMaaa 52,212,244\nRa 180,10,18432\nMaaa 20,212,240\nMvaa 180,212,236\nRa 184,10,24576\nMvaa 148,212,232\nD0 3,20,46,1024\n"
"Mvaa 116,212,228\nRa 188,10,26624\nMvaa 84,212,224\nMvaa 88,216,224\nMvaa 120,216,228\nMvaa 152,216,232\nMvaa 184,216,236\nMaaa 24,216,240\nMaaa 56,216,244\nD0 4,20,46,2048\n"
"Maaa 88,216,248\nMaaa 120,216,252\nMaaa 124,220,252\nMaaa 92,220,248\nMaaa 60,220,244\nMaaa 28,220,240\nMvaa 188,220,236\nMvaa 156,220,232\nD0 5,20,46,3072\nMvaa 124,220,228\n"
"Mvaa 92,220,224\ns_waitcnt vmcnt(13) lgkmcnt(0)\ns_barrier\nMvaa 64,128,160\nTa 192,12,16384\nMvaa 96,128,164\n"
"buffer_load_dwordx4 v[212:215], v21, s[40:43], s86 offen offset:3584\nMvaa 128,128,168\nTa 194,12,18432\nMvaa 160,128,172\ns_add_u32 m0, s47, 49152\ns_add_u32 s44, s44, 0x40000\n"
"Maaa 0,128,176\nTa 196,13,16384\nMaaa 32,128,180\nD0 6,24,44,0\nMaaa 64,128,184\nTa 198,13,18432\nMaaa 96,128,188\nv_xor_b32 v48, s15, v33\nMaaa 100,132,188\nTa 200,14,16384\n"
"Maaa 68,132,184\nv_xor_b32 v49, s15, v35\nMaaa 36,132,180\nTa 202,14,18432\nMaaa 4,132,176\nv_xor_b32 v50, s15, v37\nMvaa 164,132,172\nTa 204,15,16384\nMvaa 132,132,168\n"
"D0 7,24,44,1024\nMvaa 100,132,164\nTa 206,15,18432\nMvaa 68,132,160\nv_xor_b32 v51, s15, v39\nMvaa 72,136,160\nTa 208,16,16384\nMvaa 104,136,164\nv_xor_b32 v52, s15, v41\n"
"Mvaa 136,136,168\nTa 210,16,18432\nMvaa 168,136,172\nv_xor_b32 v53, s15, v43\nMaaa 8,136,176\nTa 212,17,16384\nMaaa 40,136,180\nD0 8,24,44,2048\nMaaa 72,136,184\nTa 214,17,18432\n"
"Maaa 104,136,188\nv_xor_b32 v54, s15, v45\nMaaa 108,140,188\nTa 216,18,16384\nMaaa 76,140,184\nv_xor_b32 v55, s15, v47\nMaaa 44,140,180\nTa 218,18,18432\nMaaa 12,140,176\n"
"v_or3_b32 v48, v48, v49, v50\nMvaa 172,140,172\nTa 220,19,16384\nMvaa 140,140,168\nD0 9,24,44,3072\nMvaa 108,140,164\nTa 222,19,18432\nMvaa 76,140,160\n"
"v_or3_b32 v51, v51, v52, v53\nMvaa 80,144,160\nRa 224,11,0\nMvaa 112,144,164\nv_or3_b32 v48, v48, v54, v55\nMvaa 144,144,168\nRa 228,11,2048\nMvaa 176,144,172\n"
"s_add_u32 m0, s47, 0\ns_add_u32 s44, s44, 0x40000\nMaaa 16,144,176\nRa 232,11,8192\nMaaa 48,144,180\nD0 6,24,44,0\nMaaa 80,144,184\nRa 236,11,10240\nMaaa 112,144,188\n"
"v_or_b32 v48, v48, v51\nMaaa 116,148,188\nRa 240,11,16384\nMaaa 84,148,184\nv_cmp_ne_u32 vcc, 0, v48\nMaaa 52,148,180\nRa 244,11,18432\nMaaa 20,148,176\ns_cbranch_vccnz .S14\n"
".S15:\nMvaa 180,148,172\nRa 248,11,24576\nMvaa 148,148,168\nD0 7,24,44,1024\nMvaa 116,148,164\nRa 252,11,26624\nMvaa 84,148,160\nv_max_u32 v48, v32, v34\nMvaa 88,152,160\n"
"v_min_u32 v49, v32, v34\nMvaa 120,152,164\nv_med3_u32 v49, v48, v49, v36\nMvaa 152,152,168\nv_max_u32 v48, v48, v36\nMvaa 184,152,172\nv_med3_u32 v49, v48, v49, v38\n"
"Maaa 24,152,176\nv_max_u32 v48, v48, v38\nMaaa 56,152,180\nD0 8,24,44,2048\nMaaa 88,152,184\nv_med3_u32 v49, v48, v49, v40\nMaaa 120,152,188\nv_max_u32 v48, v48, v40\n"
"Maaa 124,156,188\nv_med3_u32 v49, v48, v49, v42\nMaaa 92,156,184\nv_max_u32 v48, v48, v42\nMaaa 60,156,180\nv_med3_u32 v49, v48, v49, v44\nMaaa 28,156,176\n"
"v_max_u32 v48, v48, v44\nMvaa 188,156,172\nv_med3_u32 v49, v48, v49, v46\nMvaa 156,156,168\nD0 9,24,44,3072\nMvaa 124,156,164\nv_max_u32 v48, v48, v46\nMvaa 92,156,160\n"
"s_waitcnt vmcnt(9) lgkmcnt(0)\ns_barrier\nMvaa 64,192,224\nTa 128,12,32768\nMvaa 96,192,228\nbuffer_load_dwordx4 v[200:203], v21, s[36:39], s87 offen offset:0\nMvaa 128,192,232\n"
"Ta 130,12,34816\nMvaa 160,192,236\ns_add_u32 m0, s48, 0\ns_add_u32 s45, s45, 128\nMaaa 0,192,240\nTa 132,13,32768\nMaaa 32,192,244\nD0 2,20,45,0\nMaaa 64,192,248\nTa 134,13,34816\n"
"Maaa 96,192,252\nv_mov_b32 v50, v48\nMaaa 100,196,252\nTa 136,14,32768\nMaaa 68,196,248\nv_max_u32_dpp v50, v50, v50 row_ror:8 row_mask:0xf bank_mask:0xf\nMaaa 36,196,244\n"
"Ta 138,14,34816\nMaaa 4,196,240\nv_max_u32_dpp v50, v50, v50 row_ror:4 row_mask:0xf bank_mask:0xf\nMvaa 164,196,236\nTa 140,15,32768\nMvaa 132,196,232\nD0 3,20,45,1024\n"
"Mvaa 100,196,228\nTa 142,15,34816\nMvaa 68,196,224\nv_max_u32_dpp v50, v50, v50 row_ror:2 row_mask:0xf bank_mask:0xf\nMvaa 72,200,224\nTa 144,16,32768\nMvaa 104,200,228\n"
"v_max_u32_dpp v50, v50, v50 row_ror:1 row_mask:0xf bank_mask:0xf\nMvaa 136,200,232\nTa 146,16,34816\nMvaa 168,200,236\nv_mov_b32 v52, v50\nMaaa 8,200,240\nTa 148,17,32768\n"
"Maaa 40,200,244\nD0 4,20,45,2048\nMaaa 72,200,248\nTa 150,17,34816\nMaaa 104,200,252\nv_permlane16_swap_b32 v50, v52\nMaaa 108,204,252\nTa 152,18,32768\nMaaa 76,204,248\n"
"v_max_u32 v50, v50, v52\nMaaa 44,204,244\nTa 154,18,34816\nMaaa 12,204,240\nv_mov_b32 v52, v50\nMvaa 172,204,236\nTa 156,19,32768\nMvaa 140,204,232\nD0 5,20,45,3072\n"
"Mvaa 108,204,228\nTa 158,19,34816\nMvaa 76,204,224\nv_permlane32_swap_b32 v50, v52\nMvaa 80,208,224\nRa 160,10,32768\nMvaa 112,208,228\nv_max_u32 v50, v50, v52\n"
"v_cmp_eq_u32 vcc, v48, v50\ns_lshl_b32 s2, s11, 3\ns_add_u32 s2, s2, 131072\nv_cndmask_b32 v51, v48, v49, vcc\nMvaa 144,208,232\nRa 164,10,34816\nMvaa 176,208,236\n"
"s_add_u32 m0, s48, 4096\ns_add_u32 s46, s45, 0x10000\nMaaa 16,208,240\nRa 168,10,40960\nMaaa 48,208,244\nD0 2,20,46,0\nMaaa 80,208,248\nRa 172,10,43008\nMaaa 112,208,252\n"
"v_max_u32_dpp v51, v51, v51 row_ror:8 row_mask:0xf bank_mask:0xf\nMaaa 116,212,252\nRa 176,10,49152\nMaaa 84,212,248\n"
"v_max_u32_dpp v51, v51, v51 row_ror:4 row_mask:0xf bank_mask:0xf\nMaaa 52,212,244\nRa 180,10,51200\nMaaa 20,212,240\n"
"v_max_u32_dpp v51, v51, v51 row_ror:2 row_mask:0xf bank_mask:0xf\nMvaa 180,212,236\nRa 184,10,57344\nMvaa 148,212,232\nD0 3,20,46,1024\nMvaa 116,212,228\nRa 188,10,59392\n"
"Mvaa 84,212,224\nv_max_u32_dpp v51, v51, v51 row_ror:1 row_mask:0xf bank_mask:0xf\nMvaa 88,216,224\nv_mov_b32 v52, v51\nMvaa 120,216,228\nMvaa 152,216,232\n"
"v_permlane16_swap_b32 v51, v52\nMvaa 184,216,236\nv_max_u32 v51, v51, v52\nMaaa 24,216,240\nv_mov_b32 v52, v51\nMaaa 56,216,244\nD0 4,20,46,2048\nMaaa 88,216,248\n"
"v_permlane32_swap_b32 v51, v52\nMaaa 120,216,252\nv_max_u32 v51, v51, v52\nMaaa 124,220,252\nv_mov_b32 v52, s2\nMaaa 92,220,248\nds_write_b64 v52, v[50:51]\nMaaa 60,220,244\n"
"v_mov_b32 v52, 131072\nMaaa 28,220,240\nMvaa 188,220,236\nMvaa 156,220,232\nD0 5,20,46,3072\nMvaa 124,220,228\nMvaa 92,220,224\ns_waitcnt vmcnt(13) lgkmcnt(0)\ns_barrier\n"
"Mvaa 64,128,160\nTa 192,12,49152\nMvaa 96,128,164\nbuffer_load_dwordx4 v[216:219], v21, s[40:43], s87 offen offset:0\nMvaa 128,128,168\nTa 194,12,51200\nMvaa 160,128,172\n"
"s_add_u32 m0, s47, 16384\ns_add_u32 s44, s44, 0x40000\nMaaa 0,128,176\nTa 196,13,49152\nMaaa 32,128,180\nD0 6,24,44,0\nMaaa 64,128,184\nTa 198,13,51200\nMaaa 96,128,188\n"
"Rv 32,52,0\nMaaa 100,132,188\nTa 200,14,49152\nMaaa 68,132,184\nRv 36,52,16\nMaaa 36,132,180\nTa 202,14,51200\nMaaa 4,132,176\nMvaa 164,132,172\nTa 204,15,49152\nMvaa 132,132,168\n"
"D0 7,24,44,1024\nMvaa 100,132,164\nTa 206,15,51200\nMvaa 68,132,160\nMvaa 72,136,160\nTa 208,16,49152\nMvaa 104,136,164\nMvaa 136,136,168\nTa 210,16,51200\nMvaa 168,136,172\n"
"Maaa 8,136,176\nTa 212,17,49152\nMaaa 40,136,180\nD0 8,24,44,2048\nMaaa 72,136,184\nTa 214,17,51200\nMaaa 104,136,188\nWL 7\nMaaa 108,140,188\nTa 216,18,49152\nMaaa 76,140,184\n"
"v_max_u32 v48, v32, v33\nMaaa 44,140,180\nTa 218,18,51200\nMaaa 12,140,176\nv_min_u32 v49, v32, v33\nMvaa 172,140,172\nTa 220,19,49152\nMvaa 140,140,168\nD0 9,24,44,3072\n"
"Mvaa 108,140,164\nTa 222,19,51200\nMvaa 76,140,160\nv_med3_u32 v49, v48, v49, v34\nMvaa 80,144,160\nRa 224,11,32768\nMvaa 112,144,164\nv_max_u32 v48, v48, v34\nMvaa 144,144,168\n"
"Ra 228,11,34816\nMvaa 176,144,172\ns_add_u32 m0, s47, 32768\ns_add_u32 s44, s44, 0x40000\nMaaa 16,144,176\nRa 232,11,40960\nMaaa 48,144,180\nD0 6,24,44,0\nMaaa 80,144,184\n"
"Ra 236,11,43008\nMaaa 112,144,188\nv_med3_u32 v49, v48, v49, v35\nMaaa 116,148,188\nRa 240,11,49152\nMaaa 84,148,184\nv_max_u32 v48, v48, v35\nMaaa 52,148,180\nRa 244,11,51200\n"
"Maaa 20,148,176\nv_med3_u32 v49, v48, v49, v36\nMvaa 180,148,172\nRa 248,11,57344\nMvaa 148,148,168\nD0 7,24,44,1024\nMvaa 116,148,164\nRa 252,11,59392\nMvaa 84,148,160\n"
"v_max_u32 v48, v48, v36\nMvaa 88,152,160\nv_med3_u32 v49, v48, v49, v37\nMvaa 120,152,164\nv_max_u32 v48, v48, v37\nMvaa 152,152,168\nv_med3_u32 v49, v48, v49, v38\n"
"Mvaa 184,152,172\nv_max_u32 v48, v48, v38\nMaaa 24,152,176\nv_med3_u32 v49, v48, v49, v39\nMaaa 56,152,180\nD0 8,24,44,2048\nMaaa 88,152,184\nv_max_u32 v48, v48, v39\n"
"Maaa 120,152,188\nv_readfirstlane_b32 s2, v48\nMaaa 124,156,188\nv_readfirstlane_b32 s3, v49\nMaaa 92,156,184\nMaaa 60,156,180\nMaaa 28,156,176\ns_andn2_b32 s2, 0x7ff, s2\n"
"Mvaa 188,156,172\ns_andn2_b32 s3, 0x7ff, s3\nMvaa 156,156,168\nD0 9,24,44,3072\nMvaa 124,156,164\ns_lshl_b32 s71, s3, 16\nMvaa 92,156,160\ns_waitcnt vmcnt(9) lgkmcnt(0)\n"
"s_barrier\nMvaa 64,192,224\nTa 128,12,0\nMvaa 96,192,228\nbuffer_load_dwordx4 v[204:207], v21, s[36:39], s87 offen offset:3584\nMvaa 128,192,232\nTa 130,12,2048\nMvaa 160,192,236\n"
"s_add_u32 m0, s48, 32768\ns_add_u32 s45, s45, 128\nMaaa 0,192,240\nTa 132,13,0\nMaaa 32,192,244\nD0 2,20,45,0\nMaaa 64,192,248\nTa 134,13,2048\nMaaa 96,192,252\n"
"s_or_b32 s71, s71, s2\nMaaa 100,196,252\nTa 136,14,0\nMaaa 68,196,248\ns_mul_i32 s2, s2, 896\nMaaa 36,196,244\nTa 138,14,2048\nMaaa 4,196,240\ns_mul_i32 s3, s3, 896\n"
"Mvaa 164,196,236\nTa 140,15,0\nMvaa 132,196,232\nD0 3,20,45,1024\nMvaa 100,196,228\nTa 142,15,2048\nMvaa 68,196,224\ns_mul_i32 s6, s11, 224\nMvaa 72,200,224\nTa 144,16,0\n"
"Mvaa 104,200,228\ns_add_u32 s2, s2, s6\nMvaa 136,200,232\nTa 146,16,2048\nMvaa 168,200,236\ns_add_u32 s3, s3, s6\nMaaa 8,200,240\nTa 148,17,0\nMaaa 40,200,244\nD0 4,20,45,2048\n"
"Maaa 72,200,248\nTa 150,17,2048\nMaaa 104,200,252\nbuffer_load_dwordx2 v[240:241], v255, s[28:31], s2 offen\nMaaa 108,204,252\nTa 152,18,0\nMaaa 76,204,248\nMaaa 44,204,244\n"
"Ta 154,18,2048\nMaaa 12,204,240\nbuffer_load_dwordx2 v[242:243], v255, s[28:31], s3 offen\ns_lshl2_add_u32 s2, s10, s11\ns_cmp_eq_u32 s2, 512\ns_cselect_b32 s2, 0, 0x40000000\n"
"s_add_u32 s2, s2, 0xc048000\nMvaa 172,204,236\nTa 156,19,0\nMvaa 140,204,232\nD0 5,20,45,3072\nMvaa 108,204,228\nTa 158,19,2048\nMvaa 76,204,224\n"
"buffer_store_dword v25, off, s[16:19], s2 offset:0 sc0 sc1\nMvaa 80,208,224\nRa 160,10,0\nMvaa 112,208,228\nMvaa 144,208,232\nRa 164,10,2048\nMvaa 176,208,236\n"
"s_add_u32 m0, s48, 36864\ns_add_u32 s46, s45, 0x10000\nMaaa 16,208,240\nRa 168,10,8192\nMaaa 48,208,244\nD0 2,20,46,0\nMaaa 80,208,248\nRa 172,10,10240\nMaaa 112,208,252\n"
"Maaa 116,212,252\nRa 176,10,16384\nMaaa 84,212,248\nMaaa 52,212,244\nRa 180,10,18432\nMaaa 20,212,240\nMvaa 180,212,236\nRa 184,10,24576\nMvaa 148,212,232\nD0 3,20,46,1024\n"
"Mvaa 116,212,228\nRa 188,10,26624\nMvaa 84,212,224\nMvaa 88,216,224\nMvaa 120,216,228\nMvaa 152,216,232\nMvaa 184,216,236\nMaaa 24,216,240\nMaaa 56,216,244\nD0 4,20,46,2048\n"
"Maaa 88,216,248\nMaaa 120,216,252\nMaaa 124,220,252\nMaaa 92,220,248\nMaaa 60,220,244\nMaaa 28,220,240\nMvaa 188,220,236\nMvaa 156,220,232\nD0 5,20,46,3072\nMvaa 124,220,228\n"
"Mvaa 92,220,224\ns_waitcnt vmcnt(16) lgkmcnt(0)\ns_barrier\nMvaa 64,128,160\nTa 192,12,16384\nMvaa 96,128,164\n"
"buffer_load_dwordx4 v[220:223], v21, s[40:43], s87 offen offset:3584\nMvaa 128,128,168\nTa 194,12,18432\nMvaa 160,128,172\ns_add_u32 m0, s47, 49152\ns_add_u32 s44, s44, 0x40000\n"
"Maaa 0,128,176\nTa 196,13,16384\nMaaa 32,128,180\nD0 6,24,44,0\nMaaa 64,128,184\nTa 198,13,18432\nMaaa 96,128,188\nMaaa 100,132,188\nTa 200,14,16384\nMaaa 68,132,184\n"
"Maaa 36,132,180\nTa 202,14,18432\nMaaa 4,132,176\nMvaa 164,132,172\nTa 204,15,16384\nMvaa 132,132,168\nD0 7,24,44,1024\nMvaa 100,132,164\nTa 206,15,18432\nMvaa 68,132,160\n"
"Mvaa 72,136,160\nTa 208,16,16384\nMvaa 104,136,164\nMvaa 136,136,168\nTa 210,16,18432\nMvaa 168,136,172\nMaaa 8,136,176\nTa 212,17,16384\nMaaa 40,136,180\nD0 8,24,44,2048\n"
"Maaa 72,136,184\nTa 214,17,18432\nMaaa 104,136,188\nMaaa 108,140,188\nTa 216,18,16384\nMaaa 76,140,184\nMaaa 44,140,180\nTa 218,18,18432\nMaaa 12,140,176\nMvaa 172,140,172\n"
"Ta 220,19,16384\nMvaa 140,140,168\nD0 9,24,44,3072\nMvaa 108,140,164\nTa 222,19,18432\nMvaa 76,140,160\nMvaa 80,144,160\nRa 224,11,0\nMvaa 112,144,164\ns_lshl_b32 s5, s73, 8\n"
"s_add_u32 s5, s5, 0xc04ac00\nv_lshrrev_b32 v27, 2, v1\nbuffer_load_dword v27, v27, s[16:19], s5 offen sc0 sc1\nMvaa 144,144,168\nRa 228,11,2048\nMvaa 176,144,172\n"
"s_add_u32 m0, s47, 0\ns_add_u32 s44, s44, 0x40000\nMaaa 16,144,176\nRa 232,11,8192\nMaaa 48,144,180\nD0 6,24,44,0\nMaaa 80,144,184\nRa 236,11,10240\nMaaa 112,144,188\n"
"Maaa 116,148,188\nRa 240,11,16384\nMaaa 84,148,184\nMaaa 52,148,180\nRa 244,11,18432\nMaaa 20,148,176\nMvaa 180,148,172\nRa 248,11,24576\nMvaa 148,148,168\nD0 7,24,44,1024\n"
"Mvaa 116,148,164\nRa 252,11,26624\nMvaa 84,148,160\nMvaa 88,152,160\nMvaa 120,152,164\nMvaa 152,152,168\nMvaa 184,152,172\nMaaa 24,152,176\nMaaa 56,152,180\nD0 8,24,44,2048\n"
"Maaa 88,152,184\nMaaa 120,152,188\nMaaa 124,156,188\nMaaa 92,156,184\nMaaa 60,156,180\nMaaa 28,156,176\nMvaa 188,156,172\nMvaa 156,156,168\nD0 9,24,44,3072\nMvaa 124,156,164\n"
"Mvaa 92,156,160\ns_waitcnt vmcnt(10) lgkmcnt(0)\ns_barrier\nMvaa 64,192,224\nTa 128,12,32768\nMvaa 96,192,228\nbuffer_load_dwordx4 v[224:227], v20, s[92:95], s88 offen offset:0\n"
"Mvaa 128,192,232\nTa 130,12,34816\nv_lshlrev_b32 v244, 16, v240\nMvaa 160,192,236\ns_add_u32 m0, s48, 0\ns_add_u32 s45, s45, 128\nMaaa 0,192,240\nTa 132,13,32768\n"
"v_and_b32 v245, 0xffff0000, v240\nMaaa 32,192,244\nD0 2,20,45,0\nv_lshlrev_b32 v246, 16, v241\nMaaa 64,192,248\nTa 134,13,34816\nMaaa 96,192,252\nv_and_b32 v247, 0xffff0000, v241\n"
"v_lshlrev_b32 v248, 16, v242\nMaaa 100,196,252\nTa 136,14,32768\nv_and_b32 v249, 0xffff0000, v242\nMaaa 68,196,248\nv_lshlrev_b32 v250, 16, v243\nv_and_b32 v251, 0xffff0000, v243\n"
"Maaa 36,196,244\nTa 138,14,34816\nv_lshlrev_b32 v252, 16, v192\nMaaa 4,196,240\nv_and_b32 v253, 0xffff0000, v192\nMvaa 164,196,236\nTa 140,15,32768\nMU 32,244,252\n"
"Mvaa 132,196,232\nD0 3,20,45,1024\nMU 33,244,253\nMvaa 100,196,228\nTa 142,15,34816\nMU 40,248,252\nMvaa 68,196,224\nMU 41,248,253\nv_lshlrev_b32 v252, 16, v193\nMvaa 72,200,224\n"
"Ta 144,16,32768\nv_and_b32 v253, 0xffff0000, v193\nMvaa 104,200,228\nMU 34,244,252\nMvaa 136,200,232\nTa 146,16,34816\nMU 35,244,253\nMvaa 168,200,236\nMU 42,248,252\n"
"MU 43,248,253\nMaaa 8,200,240\nTa 148,17,32768\nv_lshlrev_b32 v252, 16, v194\nMaaa 40,200,244\nD0 4,20,45,2048\nv_and_b32 v253, 0xffff0000, v194\nMaaa 72,200,248\nTa 150,17,34816\n"
"Maaa 104,200,252\nMU 36,244,252\nMU 37,244,253\nMaaa 108,204,252\nTa 152,18,32768\nMU 44,248,252\nMaaa 76,204,248\nMU 45,248,253\nv_lshlrev_b32 v252, 16, v195\nMaaa 44,204,244\n"
"Ta 154,18,34816\nv_and_b32 v253, 0xffff0000, v195\nMaaa 12,204,240\nMU 38,244,252\nMvaa 172,204,236\nTa 156,19,32768\nMU 39,244,253\nMvaa 140,204,232\nD0 5,20,45,3072\n"
"MU 46,248,252\nMvaa 108,204,228\nTa 158,19,34816\nMU 47,248,253\nMvaa 76,204,224\nv_lshlrev_b32 v252, 16, v208\nv_and_b32 v253, 0xffff0000, v208\nMvaa 80,208,224\nRa 160,10,32768\n"
"MU 48,244,252\nMvaa 112,208,228\nbuffer_load_dwordx4 v[228:231], v20, s[92:95], s88 offen offset:896\nMvaa 144,208,232\nRa 164,10,34816\nMU 49,244,253\nMvaa 176,208,236\n"
"s_add_u32 m0, s48, 4096\ns_add_u32 s46, s45, 0x10000\nMaaa 16,208,240\nRa 168,10,40960\nMU 56,248,252\nMaaa 48,208,244\nD0 2,20,46,0\nMU 57,248,253\nMaaa 80,208,248\n"
"Ra 172,10,43008\nMaaa 112,208,252\nv_lshlrev_b32 v252, 16, v209\nv_and_b32 v253, 0xffff0000, v209\nMaaa 116,212,252\nRa 176,10,49152\nMU 50,244,252\nMaaa 84,212,248\n"
"MU 51,244,253\nMU 58,248,252\nMaaa 52,212,244\nRa 180,10,51200\nMU 59,248,253\nMaaa 20,212,240\nv_lshlrev_b32 v252, 16, v210\nMvaa 180,212,236\nRa 184,10,57344\n"
"v_and_b32 v253, 0xffff0000, v210\nMvaa 148,212,232\nD0 3,20,46,1024\nMU 52,244,252\nMvaa 116,212,228\nRa 188,10,59392\nMU 53,244,253\nMvaa 84,212,224\nMU 60,248,252\n"
"MU 61,248,253\nMvaa 88,216,224\nv_lshlrev_b32 v252, 16, v211\nv_and_b32 v253, 0xffff0000, v211\nMvaa 120,216,228\nMU 54,244,252\nMvaa 152,216,232\nMU 55,244,253\nMU 62,248,252\n"
"Mvaa 184,216,236\nMU 63,248,253\nv_lshlrev_b32 v252, 16, v196\nMaaa 24,216,240\nv_and_b32 v253, 0xffff0000, v196\nFM 32,245,252\nMaaa 56,216,244\nD0 4,20,46,2048\nFM 33,245,253\n"
"Maaa 88,216,248\nFM 40,249,252\nMaaa 120,216,252\nFM 41,249,253\nv_lshlrev_b32 v252, 16, v197\nMaaa 124,220,252\nv_and_b32 v253, 0xffff0000, v197\nFM 34,245,252\nMaaa 92,220,248\n"
"FM 35,245,253\nFM 42,249,252\nMaaa 60,220,244\nFM 43,249,253\nv_lshlrev_b32 v252, 16, v198\nMaaa 28,220,240\nv_and_b32 v253, 0xffff0000, v198\nMvaa 188,220,236\nFM 36,245,252\n"
"FM 37,245,253\nMvaa 156,220,232\nD0 5,20,46,3072\nFM 44,249,252\nMvaa 124,220,228\nFM 45,249,253\nv_lshlrev_b32 v252, 16, v199\nMvaa 92,220,224\ns_waitcnt vmcnt(15) lgkmcnt(0)\n"
"s_barrier\nMvaa 64,128,160\nTa 192,12,49152\nv_and_b32 v253, 0xffff0000, v199\nMvaa 96,128,164\nbuffer_load_dwordx4 v[232:235], v20, s[92:95], s88 offen offset:1792\n"
"Mvaa 128,128,168\nTa 194,12,51200\nFM 38,245,252\nMvaa 160,128,172\ns_add_u32 m0, s47, 16384\ns_add_u32 s44, s44, 0x40000\nMaaa 0,128,176\nTa 196,13,49152\nFM 39,245,253\n"
"Maaa 32,128,180\nD0 6,24,44,0\nFM 46,249,252\nMaaa 64,128,184\nTa 198,13,51200\nMaaa 96,128,188\nFM 47,249,253\nv_lshlrev_b32 v252, 16, v212\nMaaa 100,132,188\nTa 200,14,49152\n"
"v_and_b32 v253, 0xffff0000, v212\nMaaa 68,132,184\nFM 48,245,252\nFM 49,245,253\nMaaa 36,132,180\nTa 202,14,51200\nFM 56,249,252\nMaaa 4,132,176\nFM 57,249,253\nMvaa 164,132,172\n"
"Ta 204,15,49152\nv_lshlrev_b32 v252, 16, v213\nMvaa 132,132,168\nD0 7,24,44,1024\nv_and_b32 v253, 0xffff0000, v213\nMvaa 100,132,164\nTa 206,15,51200\nFM 50,245,252\n"
"Mvaa 68,132,160\nFM 51,245,253\nFM 58,249,252\nMvaa 72,136,160\nTa 208,16,49152\nFM 59,249,253\nMvaa 104,136,164\nv_lshlrev_b32 v252, 16, v214\nMvaa 136,136,168\nTa 210,16,51200\n"
"v_and_b32 v253, 0xffff0000, v214\nMvaa 168,136,172\nFM 52,245,252\nFM 53,245,253\nMaaa 8,136,176\nTa 212,17,49152\nFM 60,249,252\nMaaa 40,136,180\nD0 8,24,44,2048\nFM 61,249,253\n"
"Maaa 72,136,184\nTa 214,17,51200\nMaaa 104,136,188\nv_lshlrev_b32 v252, 16, v215\nv_and_b32 v253, 0xffff0000, v215\nMaaa 108,140,188\nTa 216,18,49152\nFM 54,245,252\n"
"Maaa 76,140,184\nFM 55,245,253\nFM 62,249,252\nMaaa 44,140,180\nTa 218,18,51200\nFM 63,249,253\nMaaa 12,140,176\nv_lshlrev_b32 v252, 16, v200\nMvaa 172,140,172\nTa 220,19,49152\n"
"v_and_b32 v253, 0xffff0000, v200\nMvaa 140,140,168\nD0 9,24,44,3072\nFM 32,246,252\nMvaa 108,140,164\nTa 222,19,51200\nFM 33,246,253\nMvaa 76,140,160\nFM 40,250,252\n"
"FM 41,250,253\nMvaa 80,144,160\nRa 224,11,32768\nv_lshlrev_b32 v252, 16, v201\nMvaa 112,144,164\nbuffer_load_dwordx4 v[236:239], v20, s[92:95], s88 offen offset:2688\n"
"Mvaa 144,144,168\nRa 228,11,34816\nv_and_b32 v253, 0xffff0000, v201\nMvaa 176,144,172\ns_add_u32 m0, s47, 32768\ns_add_u32 s44, s44, 0x40000\nMaaa 16,144,176\nRa 232,11,40960\n"
"FM 34,246,252\nMaaa 48,144,180\nD0 6,24,44,0\nFM 35,246,253\nMaaa 80,144,184\nRa 236,11,43008\nMaaa 112,144,188\nFM 42,250,252\nFM 43,250,253\nMaaa 116,148,188\nRa 240,11,49152\n"
"v_lshlrev_b32 v252, 16, v202\nMaaa 84,148,184\nv_and_b32 v253, 0xffff0000, v202\nFM 36,246,252\nMaaa 52,148,180\nRa 244,11,51200\nFM 37,246,253\nMaaa 20,148,176\nFM 44,250,252\n"
"Mvaa 180,148,172\nRa 248,11,57344\nFM 45,250,253\nMvaa 148,148,168\nD0 7,24,44,1024\nv_lshlrev_b32 v252, 16, v203\nMvaa 116,148,164\nRa 252,11,59392\n"
"v_and_b32 v253, 0xffff0000, v203\nMvaa 84,148,160\nFM 38,246,252\nFM 39,246,253\nMvaa 88,152,160\nFM 46,250,252\nFM 47,250,253\nMvaa 120,152,164\nv_lshlrev_b32 v252, 16, v216\n"
"Mvaa 152,152,168\nv_and_b32 v253, 0xffff0000, v216\nFM 48,246,252\nMvaa 184,152,172\nFM 49,246,253\nFM 56,250,252\nMaaa 24,152,176\nFM 57,250,253\nv_lshlrev_b32 v252, 16, v217\n"
"Maaa 56,152,180\nD0 8,24,44,2048\nv_and_b32 v253, 0xffff0000, v217\nMaaa 88,152,184\nFM 50,246,252\nMaaa 120,152,188\nFM 51,246,253\nFM 58,250,252\nMaaa 124,156,188\n"
"FM 59,250,253\nv_lshlrev_b32 v252, 16, v218\nMaaa 92,156,184\nv_and_b32 v253, 0xffff0000, v218\nFM 52,246,252\nMaaa 60,156,180\nFM 53,246,253\nFM 60,250,252\nMaaa 28,156,176\n"
"FM 61,250,253\nMvaa 188,156,172\nv_lshlrev_b32 v252, 16, v219\nv_and_b32 v253, 0xffff0000, v219\nMvaa 156,156,168\nD0 9,24,44,3072\nFM 54,246,252\nMvaa 124,156,164\nWV 24\n"
"v_xor_b32 v27, s15, v27\nv_and_b32 v255, 0xffffdfff, v27\nv_cmp_ne_u32 vcc, 0, v255\ns_nop 0\ns_cbranch_vccnz .S17\n.S18:\nv_cmp_ne_u32 vcc, 0, v27\ns_nop 3\n"
"s_bcnt1_i32_b64 s4, vcc\ns_cmp_gt_u32 s4, 32\ns_cselect_b32 s4, 1, 0\nMvaa 92,156,160\ns_waitcnt vmcnt(10) lgkmcnt(0)\ns_barrier\nMvaa 64,192,224\nTa 128,12,0\n"
"s_xor_b32 s4, s4, s12\nMvaa 96,192,228\ns_and_b32 s4, s4, 1\nMvaa 128,192,232\nTa 130,12,2048\ns_lshl_b32 s4, s4, 13\nMvaa 160,192,236\ns_add_u32 m0, s48, 32768\n"
"s_add_u32 s45, s45, 128\nMaaa 0,192,240\nTa 132,13,0\ns_mul_i32 s72, s73, 25165824\nMaaa 32,192,244\nD0 2,20,45,0\ns_add_u32 s72, s72, s4\nMaaa 64,192,248\nTa 134,13,2048\n"
"Maaa 96,192,252\ns_and_b32 s4, s12, 1\ns_lshl_b32 s4, s4, 21\nMaaa 100,196,252\nTa 136,14,0\ns_add_u32 s72, s72, s4\nMaaa 68,196,248\ns_lshl3_add_u32 s2, s13, s11\n"
"s_lshl_b32 s2, s2, 11\nMaaa 36,196,244\nTa 138,14,2048\ns_lshl_b32 s3, s14, 22\nMaaa 4,196,240\ns_add_u32 s2, s2, s3\nMvaa 164,196,236\nTa 140,15,0\ns_add_u32 s2, s2, s72\n"
"Mvaa 132,196,232\nD0 3,20,45,1024\ns_add_u32 s49, s2, 0xc00000\nMvaa 100,196,228\nTa 142,15,2048\nFM 55,246,253\nMvaa 68,196,224\nFM 62,250,252\nFM 63,250,253\nMvaa 72,200,224\n"
"Ta 144,16,0\nv_lshlrev_b32 v252, 16, v204\nMvaa 104,200,228\nv_and_b32 v253, 0xffff0000, v204\nMvaa 136,200,232\nTa 146,16,2048\nFM 32,247,252\nMvaa 168,200,236\nFM 33,247,253\n"
"FM 40,251,252\nMaaa 8,200,240\nTa 148,17,0\nFM 41,251,253\nMaaa 40,200,244\nD0 4,20,45,2048\nv_lshlrev_b32 v252, 16, v205\nMaaa 72,200,248\nTa 150,17,2048\nMaaa 104,200,252\n"
"v_and_b32 v253, 0xffff0000, v205\nFM 34,247,252\nMaaa 108,204,252\nTa 152,18,0\nFM 35,247,253\nMaaa 76,204,248\nFM 42,251,252\nFM 43,251,253\nMaaa 44,204,244\nTa 154,18,2048\n"
"v_lshlrev_b32 v252, 16, v206\nMaaa 12,204,240\nv_and_b32 v253, 0xffff0000, v206\nMvaa 172,204,236\nTa 156,19,0\nFM 36,247,252\nMvaa 140,204,232\nD0 5,20,45,3072\nFM 37,247,253\n"
"Mvaa 108,204,228\nTa 158,19,2048\nFM 44,251,252\nMvaa 76,204,224\nFM 45,251,253\nv_lshlrev_b32 v252, 16, v207\nMvaa 80,208,224\nRa 160,10,0\nv_and_b32 v253, 0xffff0000, v207\n"
"Mvaa 112,208,228\nFM 38,247,252\nMvaa 144,208,232\nRa 164,10,2048\nFM 39,247,253\nMvaa 176,208,236\ns_add_u32 m0, s48, 36864\ns_add_u32 s46, s45, 0x10000\nMaaa 16,208,240\n"
"Ra 168,10,8192\nFM 46,251,252\nMaaa 48,208,244\nD0 2,20,46,0\nFM 47,251,253\nMaaa 80,208,248\nRa 172,10,10240\nMaaa 112,208,252\nv_lshlrev_b32 v252, 16, v220\n"
"v_and_b32 v253, 0xffff0000, v220\nMaaa 116,212,252\nRa 176,10,16384\nFM 48,247,252\nMaaa 84,212,248\nFM 49,247,253\nFM 56,251,252\nMaaa 52,212,244\nRa 180,10,18432\nFM 57,251,253\n"
"Maaa 20,212,240\nv_lshlrev_b32 v252, 16, v221\nMvaa 180,212,236\nRa 184,10,24576\nv_and_b32 v253, 0xffff0000, v221\nMvaa 148,212,232\nD0 3,20,46,1024\nFM 50,247,252\n"
"Mvaa 116,212,228\nRa 188,10,26624\nFM 51,247,253\nMvaa 84,212,224\nFM 58,251,252\nFM 59,251,253\nMvaa 88,216,224\nv_lshlrev_b32 v252, 16, v222\nv_and_b32 v253, 0xffff0000, v222\n"
"Mvaa 120,216,228\nFM 52,247,252\nMvaa 152,216,232\nFM 53,247,253\nFM 60,251,252\nMvaa 184,216,236\nFM 61,251,253\nv_lshlrev_b32 v252, 16, v223\nMaaa 24,216,240\n"
"v_and_b32 v253, 0xffff0000, v223\nFM 54,247,252\nMaaa 56,216,244\nD0 4,20,46,2048\nFM 55,247,253\nMaaa 88,216,248\nFM 62,251,252\nMaaa 120,216,252\nFM 63,251,253\n"
"Maaa 124,220,252\nv_permlane32_swap_b32 v32, v48\nv_permlane32_swap_b32 v33, v49\nMaaa 92,220,248\nv_permlane32_swap_b32 v34, v50\nv_permlane32_swap_b32 v35, v51\nMaaa 60,220,244\n"
"v_permlane32_swap_b32 v36, v52\nv_permlane32_swap_b32 v37, v53\nMaaa 28,220,240\nv_permlane32_swap_b32 v38, v54\nMvaa 188,220,236\nv_permlane32_swap_b32 v39, v55\n"
"v_permlane32_swap_b32 v40, v56\nMvaa 156,220,232\nD0 5,20,46,3072\nv_permlane32_swap_b32 v41, v57\nMvaa 124,220,228\nv_permlane32_swap_b32 v42, v58\n"
"v_permlane32_swap_b32 v43, v59\nMvaa 92,220,224\ns_waitcnt vmcnt(13) lgkmcnt(0)\ns_barrier\nMvaa 64,128,160\nTa 192,12,16384\nv_permlane32_swap_b32 v44, v60\nMvaa 96,128,164\n"
"v_permlane32_swap_b32 v45, v61\nMvaa 128,128,168\nTa 194,12,18432\nv_permlane32_swap_b32 v46, v62\nMvaa 160,128,172\ns_add_u32 m0, s47, 49152\ns_add_u32 s44, s44, 0x40000\n"
"Maaa 0,128,176\nTa 196,13,16384\nv_permlane32_swap_b32 v47, v63\nMaaa 32,128,180\nD0 6,24,44,0\nAD 32,32,48\nMaaa 64,128,184\nTa 198,13,18432\nMaaa 96,128,188\nAD 33,33,49\n"
"AD 34,34,50\nMaaa 100,132,188\nTa 200,14,16384\nAD 35,35,51\nMaaa 68,132,184\nAD 36,36,52\nAD 37,37,53\nMaaa 36,132,180\nTa 202,14,18432\nAD 38,38,54\nMaaa 4,132,176\nAD 39,39,55\n"
"Mvaa 164,132,172\nTa 204,15,16384\nAD 40,40,56\nMvaa 132,132,168\nD0 7,24,44,1024\nAD 41,41,57\nMvaa 100,132,164\nTa 206,15,18432\nAD 42,42,58\nMvaa 68,132,160\nAD 43,43,59\n"
"AD 44,44,60\nMvaa 72,136,160\nTa 208,16,16384\nAD 45,45,61\nMvaa 104,136,164\nAD 46,46,62\nMvaa 136,136,168\nTa 210,16,18432\nAD 47,47,63\nMvaa 168,136,172\n"
"v_permlane16_swap_b32 v32, v40\nv_permlane16_swap_b32 v33, v41\nMaaa 8,136,176\nTa 212,17,16384\nv_permlane16_swap_b32 v34, v42\nMaaa 40,136,180\nD0 8,24,44,2048\n"
"v_permlane16_swap_b32 v35, v43\nMaaa 72,136,184\nTa 214,17,18432\nMaaa 104,136,188\nv_permlane16_swap_b32 v36, v44\nv_permlane16_swap_b32 v37, v45\nMaaa 108,140,188\n"
"Ta 216,18,16384\nv_permlane16_swap_b32 v38, v46\nMaaa 76,140,184\nv_permlane16_swap_b32 v39, v47\nAD 32,32,40\nMaaa 44,140,180\nTa 218,18,18432\nAD 33,33,41\nMaaa 12,140,176\n"
"AD 34,34,42\nMvaa 172,140,172\nTa 220,19,16384\nAD 35,35,43\nMvaa 140,140,168\nD0 9,24,44,3072\nAD 36,36,44\nMvaa 108,140,164\nTa 222,19,18432\nAD 37,37,45\nMvaa 76,140,160\n"
"AD 38,38,46\nAD 39,39,47\nMvaa 80,144,160\nRa 224,11,0\nv_add_f32_dpp v32, v32, v32 row_ror:8 row_mask:0xf bank_mask:0xf\nMvaa 112,144,164\n"
"v_add_f32_dpp v33, v33, v33 row_ror:8 row_mask:0xf bank_mask:0xf\nMvaa 144,144,168\nRa 228,11,2048\nv_add_f32_dpp v34, v34, v34 row_ror:8 row_mask:0xf bank_mask:0xf\n"
"Mvaa 176,144,172\ns_add_u32 m0, s47, 0\ns_add_u32 s44, s44, 0x40000\nMaaa 16,144,176\nRa 232,11,8192\nv_add_f32_dpp v35, v35, v35 row_ror:8 row_mask:0xf bank_mask:0xf\n"
"Maaa 48,144,180\nD0 6,24,44,0\nv_add_f32_dpp v36, v36, v36 row_ror:8 row_mask:0xf bank_mask:0xf\nMaaa 80,144,184\nRa 236,11,10240\nMaaa 112,144,188\n"
"v_add_f32_dpp v37, v37, v37 row_ror:8 row_mask:0xf bank_mask:0xf\nv_add_f32_dpp v38, v38, v38 row_ror:8 row_mask:0xf bank_mask:0xf\nMaaa 116,148,188\nRa 240,11,16384\n"
"v_add_f32_dpp v39, v39, v39 row_ror:8 row_mask:0xf bank_mask:0xf\nMaaa 84,148,184\nv_add_f32_dpp v32, v32, v32 row_ror:4 row_mask:0xf bank_mask:0xf\n"
"v_add_f32_dpp v33, v33, v33 row_ror:4 row_mask:0xf bank_mask:0xf\nMaaa 52,148,180\nRa 244,11,18432\nv_add_f32_dpp v34, v34, v34 row_ror:4 row_mask:0xf bank_mask:0xf\n"
"Maaa 20,148,176\nv_add_f32_dpp v35, v35, v35 row_ror:4 row_mask:0xf bank_mask:0xf\nMvaa 180,148,172\nRa 248,11,24576\n"
"v_add_f32_dpp v36, v36, v36 row_ror:4 row_mask:0xf bank_mask:0xf\nMvaa 148,148,168\nD0 7,24,44,1024\nv_add_f32_dpp v37, v37, v37 row_ror:4 row_mask:0xf bank_mask:0xf\n"
"Mvaa 116,148,164\nRa 252,11,26624\nv_add_f32_dpp v38, v38, v38 row_ror:4 row_mask:0xf bank_mask:0xf\nMvaa 84,148,160\n"
"v_add_f32_dpp v39, v39, v39 row_ror:4 row_mask:0xf bank_mask:0xf\nv_add_f32_dpp v32, v32, v32 row_ror:2 row_mask:0xf bank_mask:0xf\nMvaa 88,152,160\n"
"v_add_f32_dpp v33, v33, v33 row_ror:2 row_mask:0xf bank_mask:0xf\nv_add_f32_dpp v34, v34, v34 row_ror:2 row_mask:0xf bank_mask:0xf\nMvaa 120,152,164\n"
"v_add_f32_dpp v35, v35, v35 row_ror:2 row_mask:0xf bank_mask:0xf\nMvaa 152,152,168\nv_add_f32_dpp v36, v36, v36 row_ror:2 row_mask:0xf bank_mask:0xf\n"
"v_add_f32_dpp v37, v37, v37 row_ror:2 row_mask:0xf bank_mask:0xf\nMvaa 184,152,172\nv_add_f32_dpp v38, v38, v38 row_ror:2 row_mask:0xf bank_mask:0xf\n"
"v_add_f32_dpp v39, v39, v39 row_ror:2 row_mask:0xf bank_mask:0xf\nMaaa 24,152,176\nv_lshrrev_b32 v48, 4, v0\nv_and_b32 v49, 1, v0\nMaaa 56,152,180\nD0 8,24,44,2048\n"
"v_lshlrev_b32 v49, 5, v49\nMaaa 88,152,184\nv_lshl_add_u32 v49, v48, 6, v49\nMaaa 120,152,188\ns_lshl_b32 s2, s11, 8\ns_add_u32 s2, s2, 132096\nMaaa 124,156,188\n"
"v_add_u32 v49, s2, v49\nds_write_b128 v49, v[32:35]\nMaaa 92,156,184\nds_write_b128 v49, v[36:39] offset:16\nv_and_b32 v50, 31, v0\nMaaa 60,156,180\nv_lshlrev_b32 v50, 2, v50\n"
"s_lshl_b32 s2, s11, 10\nMaaa 28,156,176\ns_add_u32 s2, s2, 133120\nMvaa 188,156,172\nv_add_u32 v51, s2, v50\nv_add_u32 v50, 132096, v50\nMvaa 156,156,168\nD0 9,24,44,3072\n"
"v_lshl_add_u32 v52, v48, 4, s2\nMvaa 124,156,164\nMvaa 92,156,160\ns_waitcnt vmcnt(8) lgkmcnt(0)\ns_barrier\nMvaa 64,192,224\nTa 128,12,32768\nds_read_b32 v32, v50 offset:0\n"
"Mvaa 96,192,228\nds_read_b32 v33, v50 offset:256\nMvaa 128,192,232\nTa 130,12,34816\nds_read_b32 v34, v50 offset:512\nMvaa 160,192,236\ns_add_u32 m0, s48, 0\n"
"s_add_u32 s45, s45, 128\nMaaa 0,192,240\nTa 132,13,32768\nds_read_b32 v35, v50 offset:768\nMaaa 32,192,244\nD0 2,20,45,0\nMaaa 64,192,248\nTa 134,13,34816\n"
"ds_read_b32 v36, v50 offset:128\nMaaa 96,192,252\nds_read_b32 v37, v50 offset:384\nMaaa 100,196,252\nTa 136,14,32768\nds_read_b32 v38, v50 offset:640\nMaaa 68,196,248\n"
"ds_read_b32 v39, v50 offset:896\nMaaa 36,196,244\nTa 138,14,34816\nMaaa 4,196,240\nMvaa 164,196,236\nTa 140,15,32768\nMvaa 132,196,232\nD0 3,20,45,1024\nMvaa 100,196,228\n"
"Ta 142,15,34816\nMvaa 68,196,224\nMvaa 72,200,224\nTa 144,16,32768\nMvaa 104,200,228\nMvaa 136,200,232\nTa 146,16,34816\nMvaa 168,200,236\nWL 5\nMaaa 8,200,240\nTa 148,17,32768\n"
"AD 32,32,33\nMaaa 40,200,244\nD0 4,20,45,2048\nMaaa 72,200,248\nTa 150,17,34816\nAD 34,34,35\nMaaa 104,200,252\nAD 36,36,37\nMaaa 108,204,252\nTa 152,18,32768\nAD 38,38,39\n"
"Maaa 76,204,248\nAD 32,32,34\nMaaa 44,204,244\nTa 154,18,34816\nAD 36,36,38\nMaaa 12,204,240\nCV 32,32,36\nMvaa 172,204,236\nTa 156,19,32768\nv_and_b32 v36, 0xffff0000, v32\n"
"Mvaa 140,204,232\nD0 5,20,45,3072\nMvaa 108,204,228\nTa 158,19,34816\nv_lshlrev_b32 v32, 16, v32\nMvaa 76,204,224\nv_mul_f32 v33, 0xbfb8aa3b, v32\nEX 33,33\nMvaa 80,208,224\n"
"Ra 160,10,32768\nv_add_f32 v33, 1.0, v33\nMvaa 112,208,228\nv_rcp_f32 v33, v33\nMvaa 144,208,232\nRa 164,10,34816\nMU 33,32,33\nMvaa 176,208,236\ns_add_u32 m0, s48, 4096\n"
"s_add_u32 s46, s45, 0x10000\nMaaa 16,208,240\nRa 168,10,40960\nCV 33,33,33\nMaaa 48,208,244\nD0 2,20,46,0\nMaaa 80,208,248\nRa 172,10,43008\nv_lshlrev_b32 v33, 16, v33\n"
"Maaa 112,208,252\nMU 33,33,36\nMaaa 116,212,252\nRa 176,10,49152\nCV 33,33,33\nMaaa 84,212,248\nv_lshlrev_b32 v33, 16, v33\nMaaa 52,212,244\nRa 180,10,51200\n"
"ds_write_b32 v51, v33\nMaaa 20,212,240\nRv 244,52,0\nMvaa 180,212,236\nRa 184,10,57344\nRv 248,52,64\nMvaa 148,212,232\nD0 3,20,46,1024\nMvaa 116,212,228\nRa 188,10,59392\n"
"s_and_b32 s2, s74, 15\nMvaa 84,212,224\ns_sub_u32 s2, s2, 2\ns_cmp_lt_u32 s2, 14\ns_cselect_b32 s5, 0, 0x40000000\nMvaa 88,216,224\ns_and_b32 s2, s2, 15\ns_mul_i32 s4, s73, 14\n"
"Mvaa 120,216,228\ns_add_u32 s4, s4, s2\nMvaa 152,216,232\nv_and_b32 v49, 15, v0\nv_lshlrev_b32 v50, 3, v49\nMvaa 184,216,236\nv_and_b32 v51, 1, v48\nMaaa 24,216,240\n"
"v_lshl_add_u32 v50, v51, 2, v50\ns_mul_i32 s2, s11, 112\nMaaa 56,216,244\nD0 4,20,46,2048\nMaaa 88,216,248\nv_add_u32 v50, s2, v50\nv_lshrrev_b32 v51, 4, v50\nMaaa 120,216,252\n"
"v_lshrrev_b32 v48, 1, v48\nMaaa 124,220,252\nv_mad_u32_u24 v51, v48, 28, v51\nv_mul_u32_u24 v51, 7168, v51\nMaaa 92,220,248\nv_and_b32 v50, 15, v50\nMaaa 60,220,244\n"
"v_lshl_add_u32 v51, v50, 2, v51\nv_cmp_gt_u32 vcc, 14, v49\ns_lshl_b32 s4, s4, 6\ns_add_u32 s4, s4, 0xc04c800\nv_cndmask_b32 v51, v254, v51, vcc\nMaaa 28,220,240\n"
"s_or_b32 s4, s4, s5\nMvaa 188,220,236\nWL 3\nv_lshlrev_b32 v252, 16, v224\nMvaa 156,220,232\nD0 5,20,46,3072\nMvaa 124,220,228\nv_and_b32 v253, 0xffff0000, v224\nMU 32,244,252\n"
"Mvaa 92,220,224\ns_waitcnt vmcnt(12) lgkmcnt(0)\ns_barrier\nMvaa 64,128,160\nTa 192,12,49152\nMU 33,244,253\nMvaa 96,128,164\nMU 40,248,252\nMvaa 128,128,168\nTa 194,12,51200\n"
"MU 41,248,253\nMvaa 160,128,172\ns_add_u32 m0, s47, 16384\ns_add_u32 s44, s44, 0x40000\nMaaa 0,128,176\nTa 196,13,49152\nv_lshlrev_b32 v252, 16, v225\nMaaa 32,128,180\n"
"D0 6,24,44,0\nMaaa 64,128,184\nTa 198,13,51200\nv_and_b32 v253, 0xffff0000, v225\nMaaa 96,128,188\nMU 34,244,252\nMaaa 100,132,188\nTa 200,14,49152\nMU 35,244,253\n"
"Maaa 68,132,184\nMU 42,248,252\nMaaa 36,132,180\nTa 202,14,51200\nMU 43,248,253\nMaaa 4,132,176\nv_lshlrev_b32 v252, 16, v226\nMvaa 164,132,172\nTa 204,15,49152\n"
"v_and_b32 v253, 0xffff0000, v226\nMvaa 132,132,168\nD0 7,24,44,1024\nMvaa 100,132,164\nTa 206,15,51200\nMU 36,244,252\nMvaa 68,132,160\nMU 37,244,253\nMvaa 72,136,160\n"
"Ta 208,16,49152\nMU 44,248,252\nMvaa 104,136,164\nMU 45,248,253\nMvaa 136,136,168\nTa 210,16,51200\nv_lshlrev_b32 v252, 16, v227\nMvaa 168,136,172\n"
"v_and_b32 v253, 0xffff0000, v227\nMaaa 8,136,176\nTa 212,17,49152\nMU 38,244,252\nMaaa 40,136,180\nD0 8,24,44,2048\nMaaa 72,136,184\nTa 214,17,51200\nMU 39,244,253\n"
"Maaa 104,136,188\nMU 46,248,252\nMaaa 108,140,188\nTa 216,18,49152\nMU 47,248,253\nMaaa 76,140,184\nv_lshlrev_b32 v252, 16, v228\nMaaa 44,140,180\nTa 218,18,51200\n"
"v_and_b32 v253, 0xffff0000, v228\nMaaa 12,140,176\nFM 32,245,252\nMvaa 172,140,172\nTa 220,19,49152\nFM 33,245,253\nMvaa 140,140,168\nD0 9,24,44,3072\nMvaa 108,140,164\n"
"Ta 222,19,51200\nFM 40,249,252\nMvaa 76,140,160\nFM 41,249,253\nMvaa 80,144,160\nRa 224,11,32768\nv_lshlrev_b32 v252, 16, v229\nMvaa 112,144,164\nv_and_b32 v253, 0xffff0000, v229\n"
"Mvaa 144,144,168\nRa 228,11,34816\nFM 34,245,252\nMvaa 176,144,172\ns_add_u32 m0, s47, 32768\ns_add_u32 s44, s44, 0x40000\nMaaa 16,144,176\nRa 232,11,40960\nFM 35,245,253\n"
"Maaa 48,144,180\nD0 6,24,44,0\nMaaa 80,144,184\nRa 236,11,43008\nFM 42,249,252\nMaaa 112,144,188\nFM 43,249,253\nMaaa 116,148,188\nRa 240,11,49152\nv_lshlrev_b32 v252, 16, v230\n"
"Maaa 84,148,184\nv_and_b32 v253, 0xffff0000, v230\nMaaa 52,148,180\nRa 244,11,51200\nFM 36,245,252\nMaaa 20,148,176\nFM 37,245,253\nMvaa 180,148,172\nRa 248,11,57344\n"
"FM 44,249,252\nMvaa 148,148,168\nD0 7,24,44,1024\nMvaa 116,148,164\nRa 252,11,59392\nFM 45,249,253\nMvaa 84,148,160\nv_lshlrev_b32 v252, 16, v231\nMvaa 88,152,160\n"
"v_and_b32 v253, 0xffff0000, v231\nFM 38,245,252\nMvaa 120,152,164\nFM 39,245,253\nMvaa 152,152,168\nFM 46,249,252\nFM 47,249,253\nMvaa 184,152,172\nv_lshlrev_b32 v252, 16, v232\n"
"Maaa 24,152,176\nv_and_b32 v253, 0xffff0000, v232\nFM 32,246,252\nMaaa 56,152,180\nD0 8,24,44,2048\nMaaa 88,152,184\nFM 33,246,253\nFM 40,250,252\nMaaa 120,152,188\nFM 41,250,253\n"
"Maaa 124,156,188\nv_lshlrev_b32 v252, 16, v233\nv_and_b32 v253, 0xffff0000, v233\nMaaa 92,156,184\nFM 34,246,252\nMaaa 60,156,180\nFM 35,246,253\nFM 42,250,252\nMaaa 28,156,176\n"
"FM 43,250,253\nMvaa 188,156,172\nv_lshlrev_b32 v252, 16, v234\nv_and_b32 v253, 0xffff0000, v234\nMvaa 156,156,168\nD0 9,24,44,3072\nMvaa 124,156,164\nFM 36,246,252\nFM 37,246,253\n"
"Mvaa 92,156,160\ns_waitcnt vmcnt(8) lgkmcnt(0)\ns_barrier\nMvaa 64,192,224\nTa 128,12,0\nFM 44,250,252\nMvaa 96,192,228\nFM 45,250,253\nMvaa 128,192,232\nTa 130,12,2048\n"
"v_lshlrev_b32 v252, 16, v235\nMvaa 160,192,236\ns_add_u32 m0, s48, 32768\ns_add_u32 s45, s45, 128\nMaaa 0,192,240\nTa 132,13,0\nv_and_b32 v253, 0xffff0000, v235\nMaaa 32,192,244\n"
"D0 2,20,45,0\nMaaa 64,192,248\nTa 134,13,2048\nFM 38,246,252\nMaaa 96,192,252\nFM 39,246,253\nMaaa 100,196,252\nTa 136,14,0\nFM 46,250,252\nMaaa 68,196,248\nFM 47,250,253\n"
"Maaa 36,196,244\nTa 138,14,2048\nv_lshlrev_b32 v252, 16, v236\nMaaa 4,196,240\nv_and_b32 v253, 0xffff0000, v236\nMvaa 164,196,236\nTa 140,15,0\nFM 32,247,252\nMvaa 132,196,232\n"
"D0 3,20,45,1024\nMvaa 100,196,228\nTa 142,15,2048\nFM 33,247,253\nMvaa 68,196,224\nFM 40,251,252\nMvaa 72,200,224\nTa 144,16,0\nFM 41,251,253\nMvaa 104,200,228\n"
"v_lshlrev_b32 v252, 16, v237\nMvaa 136,200,232\nTa 146,16,2048\nv_and_b32 v253, 0xffff0000, v237\nMvaa 168,200,236\nFM 34,247,252\nMaaa 8,200,240\nTa 148,17,0\nFM 35,247,253\n"
"Maaa 40,200,244\nD0 4,20,45,2048\nMaaa 72,200,248\nTa 150,17,2048\nFM 42,251,252\nMaaa 104,200,252\nFM 43,251,253\nMaaa 108,204,252\nTa 152,18,0\nv_lshlrev_b32 v252, 16, v238\n"
"Maaa 76,204,248\nv_and_b32 v253, 0xffff0000, v238\nMaaa 44,204,244\nTa 154,18,2048\nFM 36,247,252\nMaaa 12,204,240\nFM 37,247,253\nMvaa 172,204,236\nTa 156,19,0\nFM 44,251,252\n"
"Mvaa 140,204,232\nD0 5,20,45,3072\nMvaa 108,204,228\nTa 158,19,2048\nFM 45,251,253\nMvaa 76,204,224\nv_lshlrev_b32 v252, 16, v239\nMvaa 80,208,224\nRa 160,10,0\n"
"v_and_b32 v253, 0xffff0000, v239\nMvaa 112,208,228\nFM 38,247,252\nMvaa 144,208,232\nRa 164,10,2048\nFM 39,247,253\nMvaa 176,208,236\ns_add_u32 m0, s48, 36864\n"
"s_add_u32 s46, s45, 0x10000\nMaaa 16,208,240\nRa 168,10,8192\nFM 46,251,252\nMaaa 48,208,244\nD0 2,20,46,0\nMaaa 80,208,248\nRa 172,10,10240\nFM 47,251,253\nMaaa 112,208,252\n"
"v_permlane32_swap_b32 v32, v40\nMaaa 116,212,252\nRa 176,10,16384\nv_permlane32_swap_b32 v33, v41\nMaaa 84,212,248\nv_permlane32_swap_b32 v34, v42\nMaaa 52,212,244\n"
"Ra 180,10,18432\nv_permlane32_swap_b32 v35, v43\nMaaa 20,212,240\nv_permlane32_swap_b32 v36, v44\nMvaa 180,212,236\nRa 184,10,24576\nv_permlane32_swap_b32 v37, v45\n"
"Mvaa 148,212,232\nD0 3,20,46,1024\nMvaa 116,212,228\nRa 188,10,26624\nv_permlane32_swap_b32 v38, v46\nMvaa 84,212,224\nv_permlane32_swap_b32 v39, v47\nMvaa 88,216,224\n"
"AD 32,32,40\nAD 33,33,41\nMvaa 120,216,228\nAD 34,34,42\nMvaa 152,216,232\nAD 35,35,43\nAD 36,36,44\nMvaa 184,216,236\nAD 37,37,45\nMaaa 24,216,240\nAD 38,38,46\nAD 39,39,47\n"
"Maaa 56,216,244\nD0 4,20,46,2048\nMaaa 88,216,248\nv_permlane16_swap_b32 v32, v36\nv_permlane16_swap_b32 v33, v37\nMaaa 120,216,252\nv_permlane16_swap_b32 v34, v38\n"
"Maaa 124,220,252\nv_permlane16_swap_b32 v35, v39\nAD 32,32,36\nMaaa 92,220,248\nAD 33,33,37\nMaaa 60,220,244\nAD 34,34,38\nAD 35,35,39\nMaaa 28,220,240\nSv 32,51,16,4,0\n"
"Mvaa 188,220,236\nMvaa 156,220,232\nD0 5,20,46,3072\nMvaa 124,220,228\nMvaa 92,220,224\ns_waitcnt vmcnt(13) lgkmcnt(0)\ns_barrier\nMvaa 64,128,160\nTa 192,12,16384\n"
"s_lshr_b32 s3, s11, 1\nMvaa 96,128,164\ns_lshl_b32 s3, s3, 13\ns_add_u32 m0, s47, 49152\ns_add_u32 s44, s44, 0x40000\nMvaa 128,128,168\nTa 194,12,18432\ns_lshl_b32 s2, s11, 12\n"
"Mvaa 160,128,172\ns_add_u32 s2, s2, s3\nD0 6,24,44,0\nMaaa 0,128,176\nTa 196,13,16384\ns_add_u32 s2, s2, s72\nMaaa 32,128,180\ns_lshl_b32 s3, s11, 12\nMaaa 64,128,184\n"
"Ta 198,13,18432\ns_add_u32 s89, s3, 0x0\nMaaa 96,128,188\ns_mov_b32 s88, 0xffff8000\nMaaa 100,132,188\nTa 200,14,16384\ns_lshl_b32 s4, s13, 1\nD0 7,24,44,1024\nMaaa 68,132,184\n"
"s_sub_u32 s79, 15, s4\ns_and_b32 s5, s12, 1\nMaaa 36,132,180\nTa 202,14,18432\nMaaa 4,132,176\ns_mul_i32 s5, s5, 229376\ns_add_u32 s5, s5, s2\nMvaa 164,132,172\nTa 204,15,16384\n"
"Mvaa 132,132,168\ns_add_u32 s96, s5, 0x1310000\ns_mov_b32 s63, 0x53800000\nD0 8,24,44,2048\nMvaa 100,132,164\nTa 206,15,18432\nMvaa 68,132,160\ns_mov_b32 s64, 0x2b800000\n"
"s_sub_u32 s98, s4, 1\nMvaa 72,136,160\nTa 208,16,16384\ns_lshl_b32 s5, s13, 5\nMvaa 104,136,164\ns_add_u32 s5, s5, s11\nMvaa 136,136,168\nTa 210,16,18432\ns_lshl_b32 s65, s5, 9\n"
"D0 9,24,44,3072\nMvaa 168,136,172\ns_add_u32 s65, s65, s72\nMaaa 8,136,176\nTa 212,17,16384\ns_add_u32 s65, s65, 0x1580000\nMaaa 40,136,180\ns_and_b32 s66, s72, 0x2000\n"
"s_add_u32 s6, s56, 0\nMaaa 72,136,184\nTa 214,17,18432\ns_add_u32 s66, s66, s56\nMaaa 104,136,188\ns_add_u32 s66, s66, 0x100000\n"
"buffer_load_dwordx4 v[192:195], v1, s[16:19], s6 offen offset:0 sc1\nMaaa 108,140,188\nTa 216,18,16384\ns_and_b32 s5, s12, 1\nMaaa 76,140,184\ns_mul_i32 s58, s5, 0x202000\n"
"s_xor_b32 s58, s58, s72\nMaaa 44,140,180\nTa 218,18,18432\nMaaa 12,140,176\ns_add_u32 s58, s58, 0x15c0000\ns_lshl_b32 s5, s5, 8\nMvaa 172,140,172\nTa 220,19,16384\n"
"buffer_load_dwordx4 v[196:199], v1, s[16:19], s6 offen offset:1024 sc1\nMvaa 140,140,168\ns_add_u32 s61, s58, s5\ns_lshl4_add_u32 s60, s13, s61\nMvaa 108,140,164\nTa 222,19,18432\n"
"Mvaa 76,140,160\ns_add_u32 s54, s60, 0\ns_xor_b32 s60, s54, 0x100\nMvaa 80,144,160\nRa 224,11,0\nMvaa 112,144,164\ns_cmp_eq_u32 s11, 0\ns_cselect_b32 s54, s54, 0x40000000\n"
"buffer_load_dwordx4 v[200:203], v1, s[16:19], s6 offen offset:2048 sc1\nMvaa 144,144,168\nRa 228,11,2048\ns_getpc_b64 s[8:9]\n.PC21:\ns_add_u32 s8, s8, .HD13-.PC21\n"
"s_addc_u32 s9, s9, 0\nMvaa 176,144,172\ns_mov_b32 s97, 0xff800000\nMaaa 16,144,176\nRa 232,11,8192\ns_mov_b32 s90, 3\nMaaa 48,144,180\ns_cmp_lt_u32 s11, 2\n"
"s_cselect_b32 s92, 2, 0\nMaaa 80,144,184\nRa 236,11,10240\ns_add_u32 s86, s2, 0x20000\nbuffer_load_dwordx4 v[204:207], v1, s[16:19], s6 offen offset:3072 sc1\nMaaa 112,144,188\n"
"s_add_u32 s87, s2, 0x410000\ns_cmp_eq_u32 s13, 7\ns_cselect_b32 s92, s92, 0\nMaaa 116,148,188\nRa 240,11,16384\ns_sub_u32 s4, s13, 2\nMaaa 84,148,184\ns_cmp_lt_u32 s4, 4\n"
"s_cselect_b32 s4, s12, 15\ns_add_u32 s6, s56, 4096\nMaaa 52,148,180\nRa 244,11,18432\ns_cmp_lt_u32 s4, 14\ns_cselect_b32 s5, s11, 1\nMaaa 20,148,176\ns_cmp_eq_u32 s5, 0\n"
"s_cselect_b32 s91, 1, 100\nbuffer_load_dwordx4 v[208:211], v1, s[16:19], s6 offen offset:0 sc1\nMvaa 180,148,172\nRa 248,11,24576\nv_and_b32 v27, 0x70, v1\n"
"buffer_load_dword v27, v27, s[16:19], s61 offen offset:128 sc0 sc1\nMvaa 148,148,168\ns_lshr_b32 s2, s13, 2\ns_bfe_u32 s3, s13, 0x10001\nMvaa 116,148,164\nRa 252,11,26624\n"
"Mvaa 84,148,160\ns_mul_i32 s3, s3, 17\ns_add_u32 s2, s2, s3\nMvaa 88,152,160\ns_lshl2_add_u32 s3, s14, 0\ns_lshl_b32 s3, s3, 2\n"
"buffer_load_dwordx4 v[212:215], v1, s[16:19], s6 offen offset:1024 sc1\nMvaa 120,152,164\ns_lshr_b32 s3, 0xe420ca86, s3\nMvaa 152,152,168\ns_and_b32 s3, s3, 15\n"
"s_add_u32 s2, s2, s3\nMvaa 184,152,172\ns_cmp_eq_u32 s2, 32\ns_cselect_b32 s4, 16, s2\nMaaa 24,152,176\ns_mul_i32 s75, s4, 65536\ns_lshr_b32 s2, s13, 2\nMaaa 56,152,180\n"
"s_bfe_u32 s3, s13, 0x10001\nbuffer_load_dwordx4 v[216:219], v1, s[16:19], s6 offen offset:2048 sc1\nMaaa 88,152,184\ns_mul_i32 s3, s3, 17\ns_add_u32 s2, s2, s3\nMaaa 120,152,188\n"
"s_lshl2_add_u32 s3, s14, 1\nMaaa 124,156,188\ns_lshl_b32 s3, s3, 2\ns_lshr_b32 s3, 0xe420ca86, s3\nMaaa 92,156,184\ns_and_b32 s3, s3, 15\ns_add_u32 s2, s2, s3\nMaaa 60,156,180\n"
"s_cmp_eq_u32 s2, 32\ns_cselect_b32 s4, 16, s2\nbuffer_load_dwordx4 v[220:223], v1, s[16:19], s6 offen offset:3072 sc1\nMaaa 28,156,176\ns_mul_i32 s76, s4, 65536\n"
"s_lshr_b32 s2, s13, 2\nMvaa 188,156,172\ns_bfe_u32 s3, s13, 0x10001\nMvaa 156,156,168\ns_mul_i32 s3, s3, 17\ns_add_u32 s2, s2, s3\nMvaa 124,156,164\ns_lshl2_add_u32 s3, s14, 2\n"
"Mvaa 92,156,160\ns_waitcnt vmcnt(13) lgkmcnt(0)\ns_barrier\nMvaa 64,192,224\nTa 128,12,32768\ns_lshl_b32 s3, s3, 2\ns_add_u32 s6, s56, 16384\n"
"buffer_load_dwordx4 v[224:227], v1, s[16:19], s6 offen offset:0 sc1\nMvaa 96,192,228\ns_lshr_b32 s3, 0xe420ca86, s3\nMvaa 128,192,232\nTa 130,12,34816\ns_and_b32 s3, s3, 15\n"
"Mvaa 160,192,236\ns_add_u32 s2, s2, s3\ns_cmp_eq_u32 s2, 32\ns_cselect_b32 s4, 16, s2\nMaaa 0,192,240\nTa 132,13,32768\ns_mul_i32 s77, s4, 65536\nMaaa 32,192,244\n"
"s_lshr_b32 s2, s13, 2\nbuffer_load_dwordx4 v[228:231], v1, s[16:19], s6 offen offset:1024 sc1\nMaaa 64,192,248\nTa 134,13,34816\ns_bfe_u32 s3, s13, 0x10001\nMaaa 96,192,252\n"
"s_mul_i32 s3, s3, 17\nMaaa 100,196,252\nTa 136,14,32768\ns_add_u32 s2, s2, s3\nMaaa 68,196,248\ns_lshl2_add_u32 s3, s14, 3\ns_lshl_b32 s3, s3, 2\nMaaa 36,196,244\nTa 138,14,34816\n"
"buffer_load_dwordx4 v[232:235], v1, s[16:19], s6 offen offset:2048 sc1\nMaaa 4,196,240\ns_lshr_b32 s3, 0xe420ca86, s3\ns_and_b32 s3, s3, 15\nMvaa 164,196,236\nTa 140,15,32768\n"
"Mvaa 132,196,232\ns_add_u32 s2, s2, s3\nMvaa 100,196,228\nTa 142,15,34816\ns_cmp_eq_u32 s2, 32\ns_cselect_b32 s4, 16, s2\nMvaa 68,196,224\ns_mul_i32 s78, s4, 65536\n"
"s_lshr_b32 s3, s11, 1\nbuffer_load_dwordx4 v[236:239], v1, s[16:19], s6 offen offset:3072 sc1\nMvaa 72,200,224\nTa 144,16,32768\ns_lshl_b32 s3, s3, 15\nMvaa 104,200,228\n"
"s_add_u32 s2, s72, s3\nMvaa 136,200,232\nTa 146,16,34816\ns_and_b32 s3, s11, 1\ns_add_u32 s6, s56, 20480\nMvaa 168,200,236\ns_lshl_b32 s3, s3, 12\nMaaa 8,200,240\nTa 148,17,32768\n"
"s_add_u32 s2, s2, s3\nbuffer_load_dwordx4 v[240:243], v1, s[16:19], s6 offen offset:0 sc1\nMaaa 40,200,244\ns_add_u32 s2, s2, 0x0\nMaaa 72,200,248\nTa 150,17,34816\n"
"s_mov_b32 s55, 16384\nMaaa 104,200,252\ns_add_u32 s75, s75, s2\nMaaa 108,204,252\nTa 152,18,32768\ns_add_u32 s76, s76, s2\nMaaa 76,204,248\ns_add_u32 s77, s77, s2\n"
"s_add_u32 s78, s78, s2\nbuffer_load_dwordx4 v[244:247], v1, s[16:19], s6 offen offset:1024 sc1\nMaaa 44,204,244\nTa 154,18,34816\nMaaa 12,204,240\nMvaa 172,204,236\n"
"Ta 156,19,32768\nMvaa 140,204,232\nMvaa 108,204,228\nTa 158,19,34816\nbuffer_load_dwordx4 v[248:251], v1, s[16:19], s6 offen offset:2048 sc1\nMvaa 76,204,224\nMvaa 80,208,224\n"
"Ra 160,10,32768\nMvaa 112,208,228\nMvaa 144,208,232\nRa 164,10,34816\nMvaa 176,208,236\nbuffer_load_dwordx4 v[252:255], v1, s[16:19], s6 offen offset:3072 sc1\nMaaa 16,208,240\n"
"Ra 168,10,40960\nMaaa 48,208,244\nMaaa 80,208,248\nRa 172,10,43008\nMaaa 112,208,252\nMaaa 116,212,252\nRa 176,10,49152\nWV 11\nv_cmp_ne_u32 vcc, s15, v27\ns_nop 0\n"
"s_cbranch_vccnz .S22\n.S23:\ns_sub_u32 s45, s86, 131072\nMaaa 84,212,248\ns_add_u32 m0, s47, 0\ns_add_u32 s46, s45, 0x0\nMaaa 52,212,244\nRa 180,10,51200\nMaaa 20,212,240\n"
"D1 1,16,46,0\nMvaa 180,212,236\nRa 184,10,57344\nMvaa 148,212,232\nMvaa 116,212,228\nRa 188,10,59392\nMvaa 84,212,224\nD1 1,16,46,1024\nMvaa 88,216,224\nMvaa 120,216,228\n"
"Mvaa 152,216,232\ns_mov_b32 s36, s68\nMvaa 184,216,236\nD1 1,16,46,2048\nMaaa 24,216,240\ns_and_b32 s37, s69, 0xffff\ns_mov_b32 s38, 0x1c0000\nMaaa 56,216,244\n"
"s_mov_b32 s39, 0x20000\nMaaa 88,216,248\ns_and_b32 s2, s74, 15\nMaaa 120,216,252\nD1 1,16,46,3072\ns_max_u32 s2, s2, 2\nMaaa 124,220,252\ns_sub_u32 s2, s2, 2\nMaaa 92,220,248\n"
"s_mul_i32 s3, s73, 14\nMaaa 60,220,244\ns_add_u32 s2, s2, s3\ns_lshl2_add_u32 s2, s2, s11\nMaaa 28,220,240\ns_mul_i32 s3, s2, 4682\nMvaa 188,220,236\ns_lshr_b32 s3, s3, 16\n"
"Mvaa 156,220,232\ns_mul_i32 s22, s3, 14\ns_sub_u32 s22, s2, s22\nMvaa 124,220,228\ns_and_b32 s21, s3, 28\nMvaa 92,220,224\nWL 0\ns_barrier\nMvaa 64,128,160\nTa 192,12,49152\n"
"s_add_u32 s21, s21, s3\nMvaa 96,128,164\ns_add_u32 m0, s47, 32768\ns_add_u32 s46, s45, 0x8000\nMvaa 128,128,168\nTa 194,12,51200\nMvaa 160,128,172\nD1 1,16,46,0\nMaaa 0,128,176\n"
"Ta 196,13,49152\ns_lshl_b32 s21, s21, 4\nMaaa 32,128,180\ns_add_u32 s21, s21, 1024\nMaaa 64,128,184\nTa 198,13,51200\nMaaa 96,128,188\nD1 1,16,46,1024\ns_xor_b32 s3, s3, 28\n"
"Maaa 100,132,188\nTa 200,14,49152\nMaaa 68,132,184\ns_bfe_u32 s41, s3, 0x30002\ns_cmp_gt_u32 s22, 6\ns_cselect_b32 s2, 1187840, 0\ns_cselect_b32 s1, 16, 0\nMaaa 36,132,180\n"
"Ta 202,14,51200\ns_add_u32 s41, s41, s1\nMaaa 4,132,176\nD1 1,16,46,2048\nMvaa 164,132,172\nTa 204,15,49152\nMvaa 132,132,168\ns_lshl4_add_u32 s41, s41, 0x15c0200\n"
"s_and_b32 s1, s3, 28\nMvaa 100,132,164\nTa 206,15,51200\nMvaa 68,132,160\nD1 1,16,46,3072\nMvaa 72,136,160\nTa 208,16,49152\ns_add_u32 s3, s3, s1\nMvaa 104,136,164\n"
"s_add_u32 m0, s47, -65536\ns_add_u32 s46, s45, 0x10000\nMvaa 136,136,168\nTa 210,16,51200\nMvaa 168,136,172\nD1 1,16,46,0\nMaaa 8,136,176\nTa 212,17,49152\ns_lshl_b32 s3, s3, 11\n"
"Maaa 40,136,180\ns_add_u32 s2, s2, s3\nMaaa 72,136,184\nTa 214,17,51200\nMaaa 104,136,188\nD1 1,16,46,1024\ns_lshl_b32 s3, s22, 17\nMaaa 108,140,188\nTa 216,18,49152\n"
"Maaa 76,140,184\ns_add_u32 s2, s2, s3\nMaaa 44,140,180\nTa 218,18,51200\ns_add_u32 s20, s2, 0xd00000\nMaaa 12,140,176\nD1 1,16,46,2048\nMvaa 172,140,172\nTa 220,19,49152\n"
"Mvaa 140,140,168\ns_lshl_b32 s2, s22, 7\ns_add_u32 s40, s2, 0xc04b800\nMvaa 108,140,164\nTa 222,19,51200\nMvaa 76,140,160\nD1 1,16,46,3072\nMvaa 80,144,160\nRa 224,11,32768\n"
"s_mul_i32 s2, s21, 896\nMvaa 112,144,164\ns_add_u32 m0, s47, -49152\ns_add_u32 s46, s45, 0x408000\nMvaa 144,144,168\nRa 228,11,34816\nMvaa 176,144,172\nD1 1,16,46,0\n"
"Maaa 16,144,176\nRa 232,11,40960\ns_lshl_b32 s3, s22, 6\nMaaa 48,144,180\ns_add_u32 s22, s2, s3\nMaaa 80,144,184\nRa 236,11,43008\nMaaa 112,144,188\nD1 1,16,46,1024\n"
"s_add_u32 s23, s22, 57344\nMaaa 116,148,188\nRa 240,11,49152\nMaaa 84,148,184\nMaaa 52,148,180\nRa 244,11,51200\nMaaa 20,148,176\nD1 1,16,46,2048\nMvaa 180,148,172\n"
"Ra 248,11,57344\nMvaa 148,148,168\nMvaa 116,148,164\nRa 252,11,59392\nMvaa 84,148,160\nD1 1,16,46,3072\nMvaa 88,152,160\nMvaa 120,152,164\nv_add_u32 v2, 0x10000, v1\n"
"Mvaa 152,152,168\ns_lshr_b32 s2, s59, 1\nMvaa 184,152,172\nv_add_u32 v24, s2, v2\nMaaa 24,152,176\nv_xor_b32 v26, 0x1000, v24\nMaaa 56,152,180\nv_lshrrev_b32 v20, 4, v0\n"
"Maaa 88,152,184\nv_lshlrev_b32 v20, 2, v20\nMaaa 120,152,188\nv_and_b32 v0, 15, v0\nMaaa 124,156,188\nv_sub_u32 v0, v0, v20\nMaaa 92,156,184\ns_lshl_b32 s2, s11, 4\n"
"Maaa 60,156,180\nv_add_u32 v3, s2, v0\nMaaa 28,156,176\nv_subrev_u32 v0, 32, v3\nMvaa 188,156,172\nv_mov_b32 v4, 0\nMvaa 156,156,168\nv_mov_b32 v5, 0\nMvaa 124,156,164\n"
"v_mov_b32 v6, 0\nMvaa 92,156,160\ns_waitcnt vmcnt(12) lgkmcnt(0)\ns_barrier\nMvaa 64,192,224\nv_mov_b32 v7, 0\nMvaa 68,196,224\ns_add_u32 m0, s47, 49152\n"
"s_add_u32 s46, s45, 0x400000\nMvaa 72,200,224\nv_mov_b32 v8, 0\nMvaa 76,204,224\nD1 1,16,46,0\nMvaa 80,208,224\nv_mov_b32 v9, 0\nMvaa 84,212,224\nv_mov_b32 v10, 0\n"
"Mvaa 88,216,224\nv_mov_b32 v11, 0\nCV 32,64,65\nMvaa 92,220,224\nD1 1,16,46,1024\nCV 33,66,67\nMvaa 96,192,228\nv_mov_b32 v12, 0\nCV 34,68,69\nMvaa 100,196,228\nv_mov_b32 v13, 0\n"
"Mvaa 104,200,228\nv_mov_b32 v14, 0\nCV 35,70,71\nMvaa 108,204,228\nD1 1,16,46,2048\nCV 36,72,73\nMvaa 112,208,228\nv_mov_b32 v15, 0\nCV 37,74,75\nMvaa 116,212,228\n"
"v_mov_b32 v16, 0\nCV 38,76,77\nMvaa 120,216,228\nv_mov_b32 v17, 0\nCV 39,78,79\nMvaa 124,220,228\nD1 1,16,46,3072\nCV 40,80,81\nMvaa 128,192,232\nv_mov_b32 v18, 0\nCV 41,82,83\n"
"Mvaa 132,196,232\nv_mov_b32 v19, 0\nMvaa 136,200,232\nCV 42,84,85\nCV 43,86,87\nMvaa 140,204,232\nCV 44,88,89\nCV 45,90,91\nMvaa 144,208,232\nCV 46,92,93\nCV 47,94,95\n"
"Mvaa 148,212,232\nCV 48,96,97\nCV 49,98,99\nMvaa 152,216,232\nCV 50,100,101\nCV 51,102,103\nMvaa 156,220,232\nCV 52,104,105\nCV 53,106,107\nMvaa 160,192,236\nCV 54,108,109\n"
"CV 55,110,111\nMvaa 164,196,236\nCV 56,112,113\nSv 32,1,16,75,0\nMvaa 168,200,236\nCV 57,114,115\nCV 58,116,117\nMvaa 172,204,236\nCV 59,118,119\nCV 60,120,121\nMvaa 176,208,236\n"
"CV 61,122,123\nCV 62,124,125\nMvaa 180,212,236\nCV 63,126,127\nCV 96,128,129\nSv 36,1,16,75,1024\nMvaa 184,216,236\nCV 97,130,131\nCV 98,132,133\nMvaa 188,220,236\nCV 99,134,135\n"
"CV 100,136,137\nMaaa 0,192,240\nCV 101,138,139\nCV 102,140,141\nMaaa 4,196,240\nCV 103,142,143\nSv 40,1,16,75,2048\nMaaa 8,200,240\nCV 104,144,145\nCV 105,146,147\n"
"Maaa 12,204,240\nCV 106,148,149\nCV 107,150,151\nMaaa 16,208,240\nCV 108,152,153\nCV 109,154,155\nMaaa 20,212,240\nCV 110,156,157\nCV 111,158,159\nSv 44,1,16,75,3072\n"
"Maaa 24,216,240\nCV 112,160,161\nCV 113,162,163\nMaaa 28,220,240\nCV 114,164,165\nCV 115,166,167\ns_add_u32 s75, s75, s55\nMaaa 32,192,244\nCV 116,168,169\nCV 117,170,171\n"
"Maaa 36,196,244\nCV 118,172,173\nSv 48,1,16,75,0\nMaaa 40,200,244\nCV 119,174,175\nCV 120,176,177\nMaaa 44,204,244\nCV 121,178,179\nCV 122,180,181\nMaaa 48,208,244\n"
"CV 123,182,183\nCV 124,184,185\nMaaa 52,212,244\nCV 125,186,187\nCV 126,188,189\nSv 52,1,16,75,1024\nMaaa 56,216,244\nCV 127,190,191\nv_mov_b32 v86, 0\nMaaa 60,220,244\n"
"v_mov_b32 v87, 0\nv_mov_b32 v88, 0\nMaaa 64,192,248\nRa 128,24,0\nv_mov_b32 v89, 0\nMaaa 68,196,248\nSv 56,1,16,75,2048\nAR 92,0\nMaaa 72,200,248\nRa 132,24,1024\nAR 93,1\n"
"Maaa 76,204,248\nCV 128,92,93\nAR 92,2\nAR 93,3\nMaaa 80,208,248\nRa 136,24,2048\nCV 129,92,93\nMaaa 84,212,248\nSv 60,1,16,75,3072\nAR 92,4\nMaaa 88,216,248\nRa 140,24,3072\n"
"AR 93,5\nCV 130,92,93\nMaaa 92,220,248\nAR 92,6\nAR 93,7\nMaaa 96,192,252\nRa 144,26,0\nCV 131,92,93\nAR 92,8\nMaaa 100,196,252\nAR 93,9\nCV 132,92,93\nMaaa 104,200,252\n"
"Ra 148,26,1024\nAR 92,10\nMaaa 108,204,252\nAR 93,11\nCV 133,92,93\nAR 92,12\nMaaa 112,208,252\nRa 152,26,2048\nAR 93,13\nMaaa 116,212,252\nCV 134,92,93\nAR 92,14\n"
"Maaa 120,216,252\nRa 156,26,3072\nAR 93,15\nCV 135,92,93\nMaaa 124,220,252\nWL 4\nCvav 32,128,192,4\nRa 160,24,8192\nMvav 32,132,200\nMvav 32,136,208\nRa 164,24,9216\n"
"Mvav 32,140,216\nCvav 36,140,220,8\nRa 168,24,10240\nAR 92,16\nAR 93,17\nMvav 36,136,212\ns_add_u32 m0, s89, 32768\nCV 136,92,93\nAR 92,18\nAR 93,19\nMvav 36,132,204\n"
"Ra 172,24,11264\nWL 4\ns_barrier\nMvav 36,128,196\nCV 137,92,93\nAR 92,20\nAR 93,21\nCV 138,92,93\nMvav 36,144,228\nRa 176,26,8192\nAR 92,22\nAR 93,23\nSv 96,1,16,76,0\n"
"Mvav 36,148,236\ns_add_u32 s46, s86, s88\nCV 139,92,93\nAR 92,24\nMvav 36,152,244\nRa 180,26,9216\nAR 93,25\nCV 140,92,93\nAR 92,26\nMvav 36,156,252\nAR 93,27\nCV 141,92,93\n"
"AR 92,28\nMvav 32,156,248\nRa 184,26,10240\nAR 93,29\nCV 142,92,93\nAR 92,30\nMvav 32,152,240\nD1 1,16,46,0\nAR 93,31\nCV 143,92,93\nMvav 32,148,232\nRa 188,26,11264\nWL 4\n"
"AR 92,32\nAR 93,33\nMvav 32,144,224\nCV 144,92,93\nAR 92,34\nAR 93,35\nCvav 40,160,192,12\nCV 145,92,93\nAR 92,36\nAR 93,37\nCV 146,92,93\nMvav 40,164,200\nAR 92,38\nAR 93,39\n"
"CV 147,92,93\nMvav 40,168,208\nv_and_b32 v27, 0x70, v1\nbuffer_load_dword v27, v27, s[16:19], s61 offen offset:136 sc0 sc1\nAR 92,40\nMvav 40,172,216\nAR 93,41\nCV 148,92,93\n"
"AR 92,42\nAR 93,43\nCvav 44,172,220,16\nCV 149,92,93\nAR 92,44\nAR 93,45\nMvav 44,168,212\nD1 1,16,46,1024\nCV 150,92,93\nAR 92,46\nAR 93,47\nMvav 44,164,204\nWL 0\nCV 151,92,93\n"
"AR 92,48\nMvav 44,160,196\nAR 93,49\nCV 152,92,93\nAR 92,50\nAR 93,51\nMvav 44,176,228\nCV 153,92,93\nAR 92,52\nAR 93,53\nSv 100,1,16,76,1024\nMvav 44,180,236\n"
"s_add_u32 s88, s88, 32768\nEX 80,32\nMvav 44,184,244\nCV 154,92,93\nAR 92,54\nAR 93,55\nCV 155,92,93\nMvav 44,188,252\nEX 81,33\nMvav 40,188,248\nAR 92,56\nAR 93,57\nCV 156,92,93\n"
"AR 92,58\nMvav 40,184,240\nD1 1,16,46,2048\nAR 93,59\nCV 157,92,93\nMvav 40,180,232\nAR 92,60\nAR 93,61\nCV 158,92,93\nAR 92,62\nMvav 40,176,224\nAD 84,80,81\nCV 64,80,81\n"
"AR 93,63\nCV 159,92,93\nAR 92,64\nAR 93,65\nCV 160,92,93\nEX 82,34\nAR 92,66\nAR 93,67\nCV 161,92,93\nAR 92,68\nWV 14\nEX 83,35\nAR 93,69\nAD 84,84,82\nCV 162,92,93\nAR 92,70\n"
"D1 1,16,46,3072\ns_add_u32 m0, s89, 49152\nAR 93,71\nCV 163,92,93\ns_barrier\nAR 92,72\nAR 93,73\nAD 84,84,83\nCV 65,82,83\nCV 164,92,93\nAR 92,74\nRa 128,24,32768\nAR 93,75\n"
"CV 165,92,93\nSv 104,1,16,76,2048\ns_add_u32 s46, s87, s88\nEX 80,36\nRa 132,24,33792\nAR 92,76\nAR 93,77\nCV 166,92,93\nEX 81,37\nRa 136,24,34816\nAR 92,78\nAR 93,79\n"
"CV 167,92,93\nD1 1,16,46,0\nAR 92,80\nAR 93,81\nRa 140,24,35840\nCV 168,92,93\nAR 92,82\nAR 93,83\nAD 85,80,81\nCV 68,80,81\nCV 169,92,93\nRa 144,26,32768\nAR 92,84\nAR 93,85\n"
"CV 170,92,93\nEX 82,38\nAR 92,86\nRa 148,26,33792\nAR 93,87\nCV 171,92,93\nEX 83,39\nAR 92,88\nRa 152,26,34816\nAD 85,85,82\nAR 93,89\nD1 1,16,46,1024\nCV 172,92,93\nAR 92,90\n"
"AR 93,91\nRa 156,26,35840\nCV 173,92,93\nAR 92,92\nAD 85,85,83\nCV 69,82,83\nAR 93,93\nCV 174,92,93\nRa 160,24,40960\nAR 92,94\nAR 93,95\nSv 108,1,16,76,3072\n"
"s_xor_b32 s89, s89, 0x10000\nEX 80,40\nRa 164,24,41984\nCV 175,92,93\nAR 92,96\nAR 93,97\nWL 6\nEX 81,41\nRa 168,24,43008\nAD 84,84,80\nCV 176,92,93\nAR 92,98\nD1 1,16,46,2048\n"
"AR 93,99\nCV 177,92,93\nRa 172,24,44032\nAR 92,100\nAR 93,101\nCV 178,92,93\nAD 84,84,81\nCV 66,80,81\nAR 92,102\nCvav 48,128,192,4\nRa 176,26,40960\nAR 93,103\nCV 179,92,93\n"
"AR 92,104\nMvav 48,132,200\nEX 82,42\nAR 93,105\nMvav 48,136,208\nRa 180,26,41984\nCV 180,92,93\nAR 92,106\nMvav 48,140,216\nWL 6\nEX 83,43\nAR 93,107\nWV 9\n"
"v_cmp_ne_u32 vcc, s15, v27\ns_nop 0\ns_cbranch_vccnz .S43\n.S44:\nCvav 52,140,220,8\nRa 184,26,43008\nAD 84,84,82\nCV 181,92,93\nMvav 52,136,212\nD1 1,16,46,3072\n"
"s_add_u32 m0, s89, 0\nAR 92,108\nAR 93,109\nMvav 52,132,204\nRa 188,26,44032\nCV 182,92,93\nAR 92,110\nMvav 52,128,196\nAD 84,84,83\nCV 67,82,83\nAR 93,111\nCV 183,92,93\n"
"Mvav 52,144,228\nRa 128,2,49152\nAR 92,112\nAR 93,113\ns_add_u32 s76, s76, s55\nSv 112,1,16,76,0\nMvav 52,148,236\ns_add_u32 s46, s86, s88\nEX 80,44\nMvav 52,152,244\n"
"Ra 132,2,50176\nv_xor_b32 v24, 0x10000, v24\nCV 184,92,93\nAR 92,114\nMvav 52,156,252\nWL 6\nEX 81,45\nMvav 48,156,248\nRa 136,2,51200\nAD 85,85,80\nAR 93,115\nCV 185,92,93\n"
"Mvav 48,152,240\nD1 1,16,46,0\nAR 92,116\nAR 93,117\nMvav 48,148,232\nRa 140,2,52224\nCV 186,92,93\nAR 92,118\nAR 93,119\nMvav 48,144,224\nAD 85,85,81\nCV 70,80,81\nCV 187,92,93\n"
"Cvav 56,160,192,12\nRa 144,2,53248\nAR 92,120\nAR 93,121\nCV 188,92,93\nMvav 56,164,200\nEX 82,46\nAR 92,122\nMvav 56,168,208\nRa 148,2,54272\nv_xor_b32 v26, 0x10000, v26\n"
"AR 93,123\nMvav 56,172,216\nWL 6\nEX 83,47\nCV 189,92,93\nCvav 60,172,220,16\nRa 152,2,55296\nAD 85,85,82\nAR 92,124\nMvav 60,168,212\nD1 1,16,46,1024\nAR 93,125\nCV 190,92,93\n"
"AR 92,126\nMvav 60,164,204\nRa 156,2,56320\nAR 93,127\nCV 191,92,93\nMvav 60,160,196\nAD 85,85,83\nCV 71,82,83\nMvav 60,176,228\nRa 160,2,57344\nv_max_f32 v90, v84, v85\n"
"Sv 116,1,16,76,1024\nMvav 60,180,236\ns_add_u32 s88, s88, 32768\nEX 80,48\nMvav 60,184,244\nRa 164,2,58368\nv_cmp_lt_f32 vcc, s63, v90\nMvav 60,188,252\nWL 8\nEX 81,49\n"
"Mvav 56,188,248\nRa 168,2,59392\ns_cbranch_vccnz .T26\n.T27:\nv_mov_b32 v20, v84\nv_mov_b32 v21, v85\nv_mov_b32 v22, v20\nv_mov_b32 v23, v21\nMvav 56,184,240\nD1 1,16,46,2048\n"
"v_permlane32_swap_b32 v20, v22\nv_permlane32_swap_b32 v21, v23\nAD 20,20,22\nAD 21,21,23\nv_mov_b32 v22, v20\nv_mov_b32 v23, v21\nMvav 56,180,232\nRa 172,2,60416\n"
"v_permlane16_swap_b32 v20, v22\nv_permlane16_swap_b32 v21, v23\nAD 20,20,22\nAD 21,21,23\nv_min_f32 v90, v20, v21\nv_cmp_gt_f32 vcc, s64, v90\ns_nop 0\ns_cbranch_vccnz .T28\n"
".T29:\nAD 86,86,84\nMvav 56,176,224\nAD 84,80,81\nCV 72,80,81\nZaav 0,128,64\nRa 176,2,61440\nZaav 4,128,68\nEX 82,50\nZaav 12,132,68\nRa 180,2,62464\nZaav 8,132,64\nWL 8\n"
"EX 83,51\nZaav 16,136,64\nRa 184,2,63488\nAD 84,84,82\nZaav 20,136,68\nD1 1,16,46,3072\ns_add_u32 m0, s89, 16384\nZaav 28,140,68\nRa 188,2,64512\ns_barrier\nZaav 24,140,64\n"
"AD 84,84,83\nCV 73,82,83\nZaav 32,144,64\nRa 128,24,0\nSv 120,1,16,76,2048\nZaav 36,144,68\ns_add_u32 s46, s87, s88\nEX 80,52\n"
"buffer_store_dword v25, off, s[16:19], s54 offset:12 sc0 sc1\nZaav 44,148,68\nRa 132,24,1024\nv_xor_b32 v2, 0x10000, v2\nZaav 40,148,64\nWL 8\nEX 81,53\nZaav 48,152,64\n"
"Ra 136,24,2048\nAD 87,87,85\nZaav 52,152,68\nD1 1,16,46,0\nZaav 60,156,68\nRa 140,24,3072\nZaav 56,156,64\nAD 85,80,81\nCV 76,80,81\nZaav 64,160,64\nRa 144,26,0\nZaav 68,160,68\n"
"EX 82,54\nZaav 76,164,68\nRa 148,26,1024\nZaav 72,164,64\nWL 8\nEX 83,55\nZaav 80,168,64\nRa 152,26,2048\nAD 85,85,82\nZaav 84,168,68\nD1 1,16,46,1024\nZaav 92,172,68\n"
"Ra 156,26,3072\nZaav 88,172,64\nAD 85,85,83\nCV 77,82,83\nZaav 96,176,64\nRa 160,24,8192\nSv 124,1,16,76,3072\nZaav 100,176,68\nEX 80,56\nZaav 108,180,68\nRa 164,24,9216\n"
"Zaav 104,180,64\nWL 6\nEX 81,57\nZaav 112,184,64\nRa 168,24,10240\nAD 84,84,80\nZaav 116,184,68\nD1 1,16,46,2048\nZaav 124,188,68\nRa 172,24,11264\nZaav 120,188,64\nAD 84,84,81\n"
"CV 74,80,81\nCvav 32,128,192,4\nRa 176,26,8192\nMvav 32,132,200\nEX 82,58\nMvav 32,136,208\nRa 180,26,9216\nMvav 32,140,216\nWL 6\nEX 83,59\nCvav 36,140,220,8\nRa 184,26,10240\n"
"AD 84,84,82\nMvav 36,136,212\nD1 1,16,46,3072\ns_add_u32 m0, s89, 32768\nMvav 36,132,204\nRa 188,26,11264\nMvav 36,128,196\nAD 84,84,83\nCV 75,82,83\nMvav 36,144,228\n"
"Ra 128,2,16384\nSv 128,1,16,77,0\nMvav 36,148,236\ns_add_u32 s46, s86, s88\nEX 80,60\nMvav 36,152,244\nRa 132,2,17408\nMvav 36,156,252\nWL 6\nEX 81,61\nMvav 32,156,248\n"
"Ra 136,2,18432\nAD 85,85,80\nMvav 32,152,240\nD1 1,16,46,0\nMvav 32,148,232\nRa 140,2,19456\nMvav 32,144,224\nAD 85,85,81\nCV 78,80,81\nCvav 40,160,192,12\nRa 144,2,20480\n"
"Mvav 40,164,200\nEX 82,62\nMvav 40,168,208\nRa 148,2,21504\nMvav 40,172,216\nWL 6\nEX 83,63\nCvav 44,172,220,16\nRa 152,2,22528\nAD 85,85,82\nMvav 44,168,212\nD1 1,16,46,1024\n"
"Mvav 44,164,204\nRa 156,2,23552\nMvav 44,160,196\nAD 85,85,83\nCV 79,82,83\nMvav 44,176,228\nRa 160,2,24576\nv_max_f32 v90, v84, v85\nSv 132,1,16,77,1024\nMvav 44,180,236\n"
"s_add_u32 s88, s88, 32768\nEX 80,32\nMvav 44,184,244\nRa 164,2,25600\nv_cmp_lt_f32 vcc, s63, v90\nMvav 44,188,252\nWL 8\nEX 81,33\nMvav 40,188,248\nRa 168,2,26624\n"
"s_cbranch_vccnz .T31\n.T32:\nMvav 40,184,240\nD1 1,16,46,2048\nMvav 40,180,232\nRa 172,2,27648\nAD 86,86,84\nMvav 40,176,224\nAD 84,80,81\nCV 64,80,81\nMaav 0,128,72\n"
"Ra 176,2,28672\nv_mov_b32 v112, -1\nMaav 4,128,76\nEX 82,34\nMaav 12,132,76\nRa 180,2,29696\nMaav 8,132,72\ns_waitcnt vmcnt(18) lgkmcnt(8)\nEX 83,35\nMaav 16,136,72\n"
"Ra 184,2,30720\nAD 84,84,82\nMaav 20,136,76\nD1 1,16,46,3072\ns_add_u32 m0, s89, 49152\nMaav 28,140,76\nRa 188,2,31744\ns_barrier\nMaav 24,140,72\nAD 84,84,83\nCV 65,82,83\n"
"Maav 32,144,72\nRa 128,24,32768\nSv 136,1,16,77,2048\nMaav 36,144,76\ns_add_u32 s46, s87, s88\nEX 80,36\nMaav 44,148,76\nRa 132,24,33792\nMaav 40,148,72\nWL 8\nEX 81,37\n"
"Maav 48,152,72\nRa 136,24,34816\nAD 87,87,85\nMaav 52,152,76\nD1 1,16,46,0\nMaav 60,156,76\nRa 140,24,35840\nMaav 56,156,72\nAD 85,80,81\nCV 68,80,81\nMaav 64,160,72\n"
"Ra 144,26,32768\nv_mov_b32 v113, -1\nMaav 68,160,76\nEX 82,38\nMaav 76,164,76\nRa 148,26,33792\nMaav 72,164,72\nWL 8\nEX 83,39\nMaav 80,168,72\nRa 152,26,34816\nAD 85,85,82\n"
"Maav 84,168,76\nD1 1,16,46,1024\nMaav 92,172,76\nRa 156,26,35840\nMaav 88,172,72\nAD 85,85,83\nCV 69,82,83\nMaav 96,176,72\nRa 160,24,40960\nSv 140,1,16,77,3072\nMaav 100,176,76\n"
"s_xor_b32 s89, s89, 0x10000\nEX 80,40\nMaav 108,180,76\nRa 164,24,41984\nMaav 104,180,72\nWL 6\nEX 81,41\nMaav 112,184,72\nRa 168,24,43008\nAD 84,84,80\nMaav 116,184,76\n"
"D1 1,16,46,2048\nMaav 124,188,76\nRa 172,24,44032\nMaav 120,188,72\nAD 84,84,81\nCV 66,80,81\nCvav 48,128,192,4\nRa 176,26,40960\nv_mov_b32 v114, -1\nMvav 48,132,200\nEX 82,42\n"
"Mvav 48,136,208\nRa 180,26,41984\nMvav 48,140,216\nWL 6\nEX 83,43\nCvav 52,140,220,8\nRa 184,26,43008\nAD 84,84,82\nv_and_b32 v27, 0x70, v1\n"
"buffer_load_dword v27, v27, s[16:19], s61 offen offset:140 sc0 sc1\nMvav 52,136,212\nD1 1,16,46,3072\ns_add_u32 m0, s89, 0\nMvav 52,132,204\nRa 188,26,44032\nMvav 52,128,196\n"
"AD 84,84,83\nCV 67,82,83\nMvav 52,144,228\nRa 128,2,49152\ns_add_u32 s77, s77, s55\nSv 144,1,16,77,0\nMvav 52,148,236\ns_add_u32 s46, s86, s88\nEX 80,44\nMvav 52,152,244\n"
"Ra 132,2,50176\nv_xor_b32 v24, 0x10000, v24\nMvav 52,156,252\nWL 6\nEX 81,45\nMvav 48,156,248\nRa 136,2,51200\nAD 85,85,80\nMvav 48,152,240\nD1 1,16,46,0\nMvav 48,148,232\n"
"Ra 140,2,52224\nMvav 48,144,224\nAD 85,85,81\nCV 70,80,81\nCvav 56,160,192,12\nRa 144,2,53248\nv_mov_b32 v115, -1\nMvav 56,164,200\nEX 82,46\nMvav 56,168,208\nRa 148,2,54272\n"
"v_xor_b32 v26, 0x10000, v26\nMvav 56,172,216\nWL 6\nEX 83,47\nCvav 60,172,220,16\nRa 152,2,55296\nAD 85,85,82\nMvav 60,168,212\nD1 1,16,46,1024\nMvav 60,164,204\nRa 156,2,56320\n"
"Mvav 60,160,196\nAD 85,85,83\nCV 71,82,83\nMvav 60,176,228\nRa 160,2,57344\nv_max_f32 v90, v84, v85\nSv 148,1,16,77,1024\nMvav 60,180,236\ns_add_u32 s88, s88, 32768\nEX 80,48\n"
"Mvav 60,184,244\nRa 164,2,58368\nv_cmp_lt_f32 vcc, s63, v90\nMvav 60,188,252\nWL 8\nEX 81,49\nMvav 56,188,248\nRa 168,2,59392\ns_cbranch_vccnz .T33\n.T34:\nMvav 56,184,240\n"
"D1 1,16,46,2048\nMvav 56,180,232\nRa 172,2,60416\nAD 86,86,84\nMvav 56,176,224\nAD 84,80,81\nCV 72,80,81\nMaav 0,128,64\nRa 176,2,61440\nMaav 4,128,68\nEX 82,50\nMaav 12,132,68\n"
"Ra 180,2,62464\nMaav 8,132,64\ns_waitcnt vmcnt(18) lgkmcnt(8)\nEX 83,51\nMaav 16,136,64\nRa 184,2,63488\nAD 84,84,82\nMaav 20,136,68\nD1 1,16,46,3072\ns_add_u32 m0, s89, 16384\n"
"Maav 28,140,68\nRa 188,2,64512\ns_barrier\nMaav 24,140,64\nAD 84,84,83\nCV 73,82,83\nMaav 32,144,64\nRa 128,24,0\nSv 152,1,16,77,2048\nMaav 36,144,68\ns_add_u32 s46, s87, s88\n"
"EX 80,52\nMaav 44,148,68\nRa 132,24,1024\nv_xor_b32 v2, 0x10000, v2\nMaav 40,148,64\nWL 8\nEX 81,53\nMaav 48,152,64\nRa 136,24,2048\nAD 87,87,85\n"
"buffer_store_dword v25, off, s[16:19], s54 offset:8 sc0 sc1\nMaav 52,152,68\nD1 1,16,46,0\nMaav 60,156,68\nRa 140,24,3072\nMaav 56,156,64\nAD 85,80,81\nCV 76,80,81\n"
"Maav 64,160,64\nRa 144,26,0\nMaav 68,160,68\nEX 82,54\nMaav 76,164,68\nRa 148,26,1024\nMaav 72,164,64\nWL 8\nEX 83,55\nMaav 80,168,64\nRa 152,26,2048\nAD 85,85,82\nMaav 84,168,68\n"
"D1 1,16,46,1024\nMaav 92,172,68\nRa 156,26,3072\nMaav 88,172,64\nAD 85,85,83\nCV 77,82,83\nMaav 96,176,64\nRa 160,24,8192\nSv 156,1,16,77,3072\nMaav 100,176,68\nEX 80,56\n"
"Maav 108,180,68\nRa 164,24,9216\nMaav 104,180,64\nWL 6\nEX 81,57\nMaav 112,184,64\nRa 168,24,10240\nAD 84,84,80\nMaav 116,184,68\nD1 1,16,46,2048\nMaav 124,188,68\n"
"Ra 172,24,11264\nMaav 120,188,64\nAD 84,84,81\nCV 74,80,81\nCvav 32,128,192,4\nRa 176,26,8192\nMvav 32,132,200\nEX 82,58\nMvav 32,136,208\nRa 180,26,9216\nMvav 32,140,216\nWL 6\n"
"EX 83,59\nCvav 36,140,220,8\nRa 184,26,10240\nAD 84,84,82\nMvav 36,136,212\nD1 1,16,46,3072\ns_add_u32 m0, s89, 32768\nMvav 36,132,204\nRa 188,26,11264\nMvav 36,128,196\n"
"AD 84,84,83\nCV 75,82,83\nMvav 36,144,228\nRa 128,2,16384\nSv 160,1,16,78,0\nMvav 36,148,236\ns_add_u32 s46, s86, s88\nEX 80,60\nMvav 36,152,244\nRa 132,2,17408\nMvav 36,156,252\n"
"WL 6\nEX 81,61\nMvav 32,156,248\nRa 136,2,18432\nAD 85,85,80\nMvav 32,152,240\nD1 1,16,46,0\nMvav 32,148,232\nRa 140,2,19456\nSv 112,1,16,49,0\nMvav 32,144,224\nAD 85,85,81\n"
"CV 78,80,81\nCvav 40,160,192,12\nRa 144,2,20480\nMvav 40,164,200\nEX 82,62\nMvav 40,168,208\nRa 148,2,21504\nMvav 40,172,216\nWL 6\nEX 83,63\nCvav 44,172,220,16\nRa 152,2,22528\n"
"AD 85,85,82\nMvav 44,168,212\nD1 1,16,46,1024\nMvav 44,164,204\nRa 156,2,23552\nMvav 44,160,196\nAD 85,85,83\nCV 79,82,83\nMvav 44,176,228\nRa 160,2,24576\n"
"v_max_f32 v90, v84, v85\nSv 164,1,16,78,1024\nMvav 44,180,236\ns_add_u32 s88, s88, 32768\nEX 80,32\nMvav 44,184,244\nRa 164,2,25600\nv_cmp_lt_f32 vcc, s63, v90\nMvav 44,188,252\n"
"WL 8\nEX 81,33\nMvav 40,188,248\nRa 168,2,26624\ns_cbranch_vccnz .T36\n.T37:\nMvav 40,184,240\nD1 1,16,46,2048\nMvav 40,180,232\nRa 172,2,27648\nAD 86,86,84\nMvav 40,176,224\n"
"AD 84,80,81\nCV 64,80,81\nMaav 0,128,72\nRa 176,2,28672\nMaav 4,128,76\nEX 82,34\nMaav 12,132,76\nRa 180,2,29696\nMaav 8,132,72\ns_waitcnt vmcnt(19) lgkmcnt(8)\nEX 83,35\n"
"Maav 16,136,72\nRa 184,2,30720\nAD 84,84,82\nMaav 20,136,76\nD1 1,16,46,3072\ns_add_u32 m0, s89, 49152\nMaav 28,140,76\nRa 188,2,31744\ns_barrier\nMaav 24,140,72\nAD 84,84,83\n"
"CV 65,82,83\nMaav 32,144,72\nRa 128,24,32768\nSv 168,1,16,78,2048\nMaav 36,144,76\ns_add_u32 s46, s87, s88\nEX 80,36\nMaav 44,148,76\nRa 132,24,33792\nMaav 40,148,72\nWL 8\n"
"EX 81,37\nMaav 48,152,72\nRa 136,24,34816\nAD 87,87,85\nMaav 52,152,76\nD1 1,16,46,0\nMaav 60,156,76\nRa 140,24,35840\nMaav 56,156,72\nAD 85,80,81\nCV 68,80,81\nMaav 64,160,72\n"
"Ra 144,26,32768\nMaav 68,160,76\nEX 82,38\nMaav 76,164,76\nRa 148,26,33792\nMaav 72,164,72\nWL 8\nEX 83,39\nMaav 80,168,72\nRa 152,26,34816\nAD 85,85,82\nMaav 84,168,76\n"
"D1 1,16,46,1024\nMaav 92,172,76\nRa 156,26,35840\nMaav 88,172,72\nAD 85,85,83\nCV 69,82,83\nMaav 96,176,72\nRa 160,24,40960\nSv 172,1,16,78,3072\nMaav 100,176,76\n"
"s_xor_b32 s89, s89, 0x10000\nEX 80,40\nMaav 108,180,76\nRa 164,24,41984\nMaav 104,180,72\nWL 6\nEX 81,41\nMaav 112,184,72\nRa 168,24,43008\nAD 84,84,80\nMaav 116,184,76\n"
"D1 1,16,46,2048\nMaav 124,188,76\nRa 172,24,44032\nMaav 120,188,72\nAD 84,84,81\nCV 66,80,81\nCvav 48,128,192,4\nRa 176,26,40960\nMvav 48,132,200\nEX 82,42\nMvav 48,136,208\n"
"Ra 180,26,41984\nMvav 48,140,216\nWL 6\nEX 83,43\nv_cmp_ne_u32 vcc, s15, v27\ns_nop 0\ns_cbranch_vccnz .S40\n.S41:\nv_and_b32 v27, 0x70, v1\n"
"buffer_load_dword v27, v27, s[16:19], s61 offen offset:12 sc0 sc1\nCvav 52,140,220,8\nRa 184,26,43008\nAD 84,84,82\nMvav 52,136,212\nD1 1,16,46,3072\ns_add_u32 m0, s89, 0\n"
"Mvav 52,132,204\nRa 188,26,44032\nMvav 52,128,196\nAD 84,84,83\nCV 67,82,83\nMvav 52,144,228\nRa 128,2,49152\ns_add_u32 s78, s78, s55\nSv 176,1,16,78,0\nMvav 52,148,236\n"
"s_add_u32 s46, s86, s88\nEX 80,44\nMvav 52,152,244\nRa 132,2,50176\nv_xor_b32 v24, 0x10000, v24\nMvav 52,156,252\nWL 6\nEX 81,45\nMvav 48,156,248\nRa 136,2,51200\nAD 85,85,80\n"
"Mvav 48,152,240\nD1 1,16,46,0\nv_lshrrev_b32 v92, 8, v1\nMvav 48,148,232\nRa 140,2,52224\nSv 112,1,16,49,1024\nMvav 48,144,224\nAD 85,85,81\nCV 70,80,81\nCvav 56,160,192,12\n"
"Ra 144,2,53248\nv_bfe_u32 v93, v1, 6, 2\nMvav 56,164,200\ns_add_u32 s49, s49, 131072\nEX 82,46\nMvav 56,168,208\nRa 148,2,54272\nv_xor_b32 v26, 0x10000, v26\nMvav 56,172,216\n"
"WL 6\nEX 83,47\nCvav 60,172,220,16\nRa 152,2,55296\nAD 85,85,82\nMvav 60,168,212\nD1 1,16,46,1024\nv_cmp_eq_u32 vcc, v92, v93\nv_and_b32 v92, 16, v1\nv_mov_b32 v93, 0x3f80\n"
"v_cndmask_b32 v93, 0, v93, vcc\nMvav 60,164,204\nRa 156,2,56320\nMvav 60,160,196\nAD 85,85,83\nCV 71,82,83\nMvav 60,176,228\nRa 160,2,57344\nv_max_f32 v90, v84, v85\n"
"Sv 180,1,16,78,1024\nMvav 60,180,236\ns_add_u32 s88, s88, 32768\nEX 80,48\nMvav 60,184,244\nRa 164,2,58368\nv_cmp_lt_f32 vcc, s63, v90\nMvav 60,188,252\nWL 8\nEX 81,49\n"
"Mvav 56,188,248\nRa 168,2,59392\ns_cbranch_vccnz .T38\n.T39:\nMvav 56,184,240\nD1 1,16,46,2048\nv_lshlrev_b32 v93, v92, v93\nMvav 56,180,232\nRa 172,2,60416\nAD 86,86,84\n"
"Mvav 56,176,224\nAD 84,80,81\nCV 72,80,81\nMaav 0,128,64\nRa 176,2,61440\nv_and_b32 v92, 32, v1\nMaav 4,128,68\nEX 82,50\nMaav 12,132,68\nRa 180,2,62464\nv_cmp_eq_u32 vcc, 0, v92\n"
"v_mov_b32 v128, 0\nv_mov_b32 v129, 0\nv_cndmask_b32 v130, 0, v93, vcc\nMaav 8,132,64\ns_waitcnt vmcnt(20) lgkmcnt(8)\nEX 83,51\nMaav 16,136,64\nRa 184,2,63488\nAD 84,84,82\n"
"Maav 20,136,68\nD1 1,16,46,3072\ns_add_u32 m0, s89, 16384\nMaav 28,140,68\nRa 188,2,64512\ns_barrier\nMaav 24,140,64\nAD 84,84,83\nCV 73,82,83\nMaav 32,144,64\nRa 128,24,0\n"
"Sv 184,1,16,78,2048\nMaav 36,144,68\ns_add_u32 s46, s87, s88\nEX 80,52\nMaav 44,148,68\nRa 132,24,1024\nv_xor_b32 v2, 0x10000, v2\nMaav 40,148,64\nWL 8\nEX 81,53\nMaav 48,152,64\n"
"Ra 136,24,2048\nAD 87,87,85\nMaav 52,152,68\nD1 1,16,46,0\nv_sub_u32 v131, v93, v130\nMaav 60,156,68\nRa 140,24,3072\nMaav 56,156,64\nAD 85,80,81\nCV 76,80,81\nMaav 64,160,64\n"
"Ra 144,26,0\nv_mov_b32 v132, 0\nMaav 68,160,68\nEX 82,54\nMaav 76,164,68\nRa 148,26,1024\nMaav 72,164,64\nWL 8\nEX 83,55\nMaav 80,168,64\nRa 152,26,2048\nAD 85,85,82\n"
"Maav 84,168,68\nD1 1,16,46,1024\nMaav 92,172,68\nRa 156,26,3072\nMaav 88,172,64\nAD 85,85,83\nCV 77,82,83\nMaav 96,176,64\nRa 160,24,8192\nSv 188,1,16,78,3072\nMaav 100,176,68\n"
"EX 80,56\nMaav 108,180,68\nRa 164,24,9216\nv_mov_b32 v133, 0\nMaav 104,180,64\nWL 6\nEX 81,57\nMaav 112,184,64\nRa 168,24,10240\nAD 84,84,80\nMaav 116,184,68\nD1 1,16,46,2048\n"
"Maav 124,188,68\nRa 172,24,11264\nMaav 120,188,64\nAD 84,84,81\nCV 74,80,81\ns_mov_b32 s99, 11\ns_sub_u32 s90, s90, 3\n.p2align 6\n.A46:\nCvav 32,128,192,4\nRa 176,26,8192\n"
"Mvav 32,132,200\nEX 82,58\nMvav 32,136,208\nRa 180,26,9216\nMvav 32,140,216\nWL 6\nEX 83,59\nCvav 36,140,220,8\nRa 184,26,10240\nAD 84,84,82\nMvav 36,136,212\nD1 1,16,46,3072\n"
"s_add_u32 m0, s89, 32768\nMvav 36,132,204\nRa 188,26,11264\nMvav 36,128,196\nAD 84,84,83\nCV 75,82,83\nMvav 36,144,228\nRa 128,2,16384\nMvav 36,148,236\ns_add_u32 s46, s86, s88\n"
"EX 80,60\nMvav 36,152,244\nRa 132,2,17408\nMvav 36,156,252\nWL 6\nEX 81,61\nMvav 32,156,248\nRa 136,2,18432\nAD 85,85,80\nMvav 32,152,240\nD1 1,16,46,0\nMvav 32,148,232\n"
"Ra 140,2,19456\nSv 112,1,16,49,0\nMvav 32,144,224\nAD 85,85,81\nCV 78,80,81\nCvav 40,160,192,12\nRa 144,2,20480\nMvav 40,164,200\nEX 82,62\nMvav 40,168,208\nRa 148,2,21504\n"
"Mvav 40,172,216\nWL 6\nEX 83,63\nCvav 44,172,220,16\nRa 152,2,22528\nAD 85,85,82\nMvav 44,168,212\nD1 1,16,46,1024\nMvav 44,164,204\nRa 156,2,23552\nMvav 44,160,196\nAD 85,85,83\n"
"CV 79,82,83\nMvav 44,176,228\nRa 160,2,24576\nv_max_f32 v90, v84, v85\nMvav 44,180,236\ns_add_u32 s88, s88, 32768\nEX 80,32\nMvav 44,184,244\nRa 164,2,25600\n"
"v_cmp_lt_f32 vcc, s63, v90\nMvav 44,188,252\nWL 8\nEX 81,33\nMvav 40,188,248\nRa 168,2,26624\ns_cbranch_vccnz .T50\n.T51:\nMvav 40,184,240\nD1 1,16,46,2048\nMvav 40,180,232\n"
"Ra 172,2,27648\nAD 86,86,84\nMvav 40,176,224\nAD 84,80,81\nCV 64,80,81\nMaav 0,128,72\nRa 176,2,28672\nMaav 4,128,76\nEX 82,34\nMaav 12,132,76\nRa 180,2,29696\nMaav 8,132,72\n"
"s_waitcnt vmcnt(13) lgkmcnt(8)\nEX 83,35\nMaav 16,136,72\nRa 184,2,30720\nAD 84,84,82\nMaav 20,136,76\nD1 1,16,46,3072\ns_add_u32 m0, s89, 49152\nMaav 28,140,76\nRa 188,2,31744\n"
"s_barrier\nMaav 24,140,72\nAD 84,84,83\nCV 65,82,83\nMaav 32,144,72\nRa 128,24,32768\nMaav 36,144,76\ns_add_u32 s46, s87, s88\nEX 80,36\nMaav 44,148,76\nRa 132,24,33792\n"
"Maav 40,148,72\nWL 8\nEX 81,37\nMaav 48,152,72\nRa 136,24,34816\nAD 87,87,85\nMaav 52,152,76\nD1 1,16,46,0\nMaav 60,156,76\nRa 140,24,35840\nMaav 56,156,72\nAD 85,80,81\n"
"CV 68,80,81\nMaav 64,160,72\nRa 144,26,32768\nMaav 68,160,76\nEX 82,38\nMaav 76,164,76\nRa 148,26,33792\nMaav 72,164,72\nWL 8\nEX 83,39\nMaav 80,168,72\nRa 152,26,34816\n"
"AD 85,85,82\nMaav 84,168,76\nD1 1,16,46,1024\nMaav 92,172,76\nRa 156,26,35840\nMaav 88,172,72\nAD 85,85,83\nCV 69,82,83\nMaav 96,176,72\nRa 160,24,40960\nMaav 100,176,76\n"
"s_xor_b32 s89, s89, 0x10000\nEX 80,40\nMaav 108,180,76\nRa 164,24,41984\nMaav 104,180,72\nWL 6\nEX 81,41\nMaav 112,184,72\nRa 168,24,43008\nAD 84,84,80\nMaav 116,184,76\n"
"D1 1,16,46,2048\nMaav 124,188,76\nRa 172,24,44032\nMaav 120,188,72\nAD 84,84,81\nCV 66,80,81\nCvav 48,128,192,4\nRa 176,26,40960\nMvav 48,132,200\nEX 82,42\nMvav 48,136,208\n"
"Ra 180,26,41984\nMvav 48,140,216\nWL 6\nEX 83,43\nCvav 52,140,220,8\nRa 184,26,43008\nAD 84,84,82\nMvav 52,136,212\nD1 1,16,46,3072\ns_add_u32 m0, s89, 0\nMvav 52,132,204\n"
"Ra 188,26,44032\nMvav 52,128,196\nAD 84,84,83\nCV 67,82,83\nMvav 52,144,228\nRa 128,2,49152\nMvav 52,148,236\ns_add_u32 s46, s86, s88\nEX 80,44\nMvav 52,152,244\nRa 132,2,50176\n"
"v_xor_b32 v24, 0x10000, v24\nMvav 52,156,252\nWL 6\nEX 81,45\nMvav 48,156,248\nRa 136,2,51200\nAD 85,85,80\nMvav 48,152,240\nD1 1,16,46,0\nMvav 48,148,232\nRa 140,2,52224\n"
"Sv 112,1,16,49,1024\nMvav 48,144,224\nAD 85,85,81\nCV 70,80,81\nCvav 56,160,192,12\nRa 144,2,53248\nMvav 56,164,200\ns_add_u32 s49, s49, 131072\nEX 82,46\nMvav 56,168,208\n"
"Ra 148,2,54272\nv_xor_b32 v26, 0x10000, v26\nMvav 56,172,216\nWL 6\nEX 83,47\nCvav 60,172,220,16\nRa 152,2,55296\nAD 85,85,82\nMvav 60,168,212\nD1 1,16,46,1024\nMvav 60,164,204\n"
"Ra 156,2,56320\nMvav 60,160,196\nAD 85,85,83\nCV 71,82,83\nMvav 60,176,228\nRa 160,2,57344\nv_max_f32 v90, v84, v85\nMvav 60,180,236\ns_add_u32 s88, s88, 32768\nEX 80,48\n"
"Mvav 60,184,244\nRa 164,2,58368\nv_cmp_lt_f32 vcc, s63, v90\nMvav 60,188,252\nWL 8\nEX 81,49\nMvav 56,188,248\nRa 168,2,59392\ns_cbranch_vccnz .T52\n.T53:\nMvav 56,184,240\n"
"D1 1,16,46,2048\nMvav 56,180,232\nRa 172,2,60416\nAD 86,86,84\nMvav 56,176,224\nAD 84,80,81\nCV 72,80,81\nMaav 0,128,64\nRa 176,2,61440\nMaav 4,128,68\nEX 82,50\nMaav 12,132,68\n"
"Ra 180,2,62464\nMaav 8,132,64\ns_waitcnt vmcnt(13) lgkmcnt(8)\nEX 83,51\nMaav 16,136,64\nRa 184,2,63488\nAD 84,84,82\nMaav 20,136,68\nD1 1,16,46,3072\ns_add_u32 m0, s89, 16384\n"
"Maav 28,140,68\nRa 188,2,64512\ns_barrier\nMaav 24,140,64\nAD 84,84,83\nCV 73,82,83\nMaav 32,144,64\nRa 128,24,0\nMaav 36,144,68\ns_add_u32 s46, s87, s88\nEX 80,52\n"
"Maav 44,148,68\nRa 132,24,1024\nv_xor_b32 v2, 0x10000, v2\nMaav 40,148,64\nWL 8\nEX 81,53\nMaav 48,152,64\nRa 136,24,2048\nAD 87,87,85\nMaav 52,152,68\nD1 1,16,46,0\n"
"Maav 60,156,68\nRa 140,24,3072\nMaav 56,156,64\nAD 85,80,81\nCV 76,80,81\nMaav 64,160,64\nRa 144,26,0\nMaav 68,160,68\nEX 82,54\nMaav 76,164,68\nRa 148,26,1024\nMaav 72,164,64\n"
"WL 8\nEX 83,55\nMaav 80,168,64\nRa 152,26,2048\nAD 85,85,82\nMaav 84,168,68\nD1 1,16,46,1024\nMaav 92,172,68\nRa 156,26,3072\nMaav 88,172,64\nAD 85,85,83\nCV 77,82,83\n"
"Maav 96,176,64\nRa 160,24,8192\nMaav 100,176,68\ns_sub_u32 s90, s90, 1\nEX 80,56\nMaav 108,180,68\nRa 164,24,9216\ns_cbranch_scc1 .E48\n.E49:\nMaav 104,180,64\nWL 6\nEX 81,57\n"
"Maav 112,184,64\nRa 168,24,10240\nAD 84,84,80\nMaav 116,184,68\nD1 1,16,46,2048\ns_sub_u32 s99, s99, 1\nMaav 124,188,68\nRa 172,24,11264\nMaav 120,188,64\nAD 84,84,81\n"
"CV 74,80,81\ns_cbranch_scc0 .A46\nCvav 32,128,192,4\nRa 176,26,8192\nMvav 32,132,200\nEX 82,58\nMvav 32,136,208\nRa 180,26,9216\nMvav 32,140,216\nWL 6\nEX 83,59\n"
"Cvav 36,140,220,8\nRa 184,26,10240\nAD 84,84,82\nMvav 36,136,212\nD1 1,16,46,3072\ns_add_u32 m0, s89, 32768\nMvav 36,132,204\nRa 188,26,11264\nMvav 36,128,196\nAD 84,84,83\n"
"CV 75,82,83\ns_mov_b32 s94, s96\ns_add_u32 s95, s96, 65536\nv_and_b32 v108, 0xf0, v1\nMvav 36,144,228\nRa 128,2,16384\nMvav 36,148,236\ns_add_u32 s46, s86, s88\nEX 80,60\n"
"Mvav 36,152,244\nRa 132,2,17408\nMvav 36,156,252\nWL 6\nEX 81,61\nMvav 32,156,248\nRa 136,2,18432\nAD 85,85,80\nMvav 32,152,240\nD1 1,16,46,0\nMvav 32,148,232\nRa 140,2,19456\n"
"Sv 112,1,16,49,0\nMvav 32,144,224\nAD 85,85,81\nCV 78,80,81\nbuffer_load_dwordx4 v[100:103], v108, s[16:19], s65 offen offset:0 sc0 sc1\n"
"buffer_load_dwordx4 v[104:107], v108, s[16:19], s65 offen offset:256 sc0 sc1\nCvav 40,160,192,12\nRa 144,2,20480\nMvav 40,164,200\nEX 82,62\nMvav 40,168,208\nRa 148,2,21504\n"
"Mvav 40,172,216\nWL 6\nEX 83,63\nCvav 44,172,220,16\nRa 152,2,22528\nAD 85,85,82\nMvav 44,168,212\nD1 1,16,46,1024\nMvav 44,164,204\nRa 156,2,23552\nMvav 44,160,196\nAD 85,85,83\n"
"CV 79,82,83\nMvav 44,176,228\nRa 160,2,24576\nv_max_f32 v90, v84, v85\nMvav 44,180,236\ns_add_u32 s88, s88, 32768\nEX 80,32\nMvav 44,184,244\nRa 164,2,25600\n"
"v_cmp_lt_f32 vcc, s63, v90\nMvav 44,188,252\nWL 8\nEX 81,33\nMvav 40,188,248\nRa 168,2,26624\ns_cbranch_vccnz .T57\n.T58:\nMvav 40,184,240\nD1 1,16,46,2048\nMvav 40,180,232\n"
"Ra 172,2,27648\nAD 86,86,84\nMvav 40,176,224\nAD 84,80,81\nCV 64,80,81\nMaav 0,128,72\nRa 176,2,28672\nMaav 4,128,76\nEX 82,34\nMaav 12,132,76\nRa 180,2,29696\nMaav 8,132,72\n"
"s_waitcnt vmcnt(15) lgkmcnt(8)\nEX 83,35\nMaav 16,136,72\nRa 184,2,30720\nAD 84,84,82\nMaav 20,136,76\nD1 1,16,46,3072\ns_add_u32 m0, s89, 49152\nMaav 28,140,76\nRa 188,2,31744\n"
"s_barrier\nMaav 24,140,72\nAD 84,84,83\nCV 65,82,83\nMaav 32,144,72\nRa 128,24,32768\nMaav 36,144,76\ns_add_u32 s46, s87, s88\nEX 80,36\nMaav 44,148,76\nRa 132,24,33792\n"
"Maav 40,148,72\nWL 8\nEX 81,37\nMaav 48,152,72\nRa 136,24,34816\nAD 87,87,85\nMaav 52,152,76\nD1 1,16,46,0\nMaav 60,156,76\nRa 140,24,35840\nMaav 56,156,72\nAD 85,80,81\n"
"CV 68,80,81\nMaav 64,160,72\nRa 144,26,32768\nMaav 68,160,76\nEX 82,38\nMaav 76,164,76\nRa 148,26,33792\nMaav 72,164,72\nWL 8\nEX 83,39\nMaav 80,168,72\nRa 152,26,34816\n"
"AD 85,85,82\nMaav 84,168,76\nD1 1,16,46,1024\nMaav 92,172,76\nRa 156,26,35840\nMaav 88,172,72\nAD 85,85,83\nCV 69,82,83\nMaav 96,176,72\nRa 160,24,40960\nMaav 100,176,76\n"
"s_xor_b32 s89, s89, 0x10000\nEX 80,40\nMaav 108,180,76\nRa 164,24,41984\nMaav 104,180,72\nWL 6\nEX 81,41\nMaav 112,184,72\nRa 168,24,43008\nAD 84,84,80\nMaav 116,184,76\n"
"D1 1,16,46,2048\nMaav 124,188,76\nRa 172,24,44032\nMaav 120,188,72\nAD 84,84,81\nCV 66,80,81\nCvav 48,128,192,4\nRa 176,26,40960\nMvav 48,132,200\ns_mov_b32 s86, s94\nEX 82,42\n"
"Mvav 48,136,208\nRa 180,26,41984\nMvav 48,140,216\nWL 6\nEX 83,43\nCvav 52,140,220,8\nRa 184,26,43008\nAD 84,84,82\nMvav 52,136,212\nD1 1,16,46,3072\ns_add_u32 m0, s89, 0\n"
"Mvav 52,132,204\nRa 188,26,44032\nMvav 52,128,196\nAD 84,84,83\nCV 67,82,83\nMvav 52,144,228\nRa 128,2,49152\nMvav 52,148,236\ns_add_u32 s46, s86, s88\nEX 80,44\nMvav 52,152,244\n"
"Ra 132,2,50176\nv_xor_b32 v24, 0x10000, v24\nMvav 52,156,252\nWL 6\nEX 81,45\nMvav 48,156,248\nRa 136,2,51200\nAD 85,85,80\nMvav 48,152,240\nD1 1,16,46,0\nMvav 48,148,232\n"
"Ra 140,2,52224\nSv 112,1,16,49,1024\nMvav 48,144,224\nAD 85,85,81\nCV 70,80,81\nWV 9\nv_xor_b32 v109, s15, v102\nv_xor_b32 v110, s15, v106\nv_or_b32 v109, v109, v110\n"
"v_cmp_ne_u32 vcc, 0, v109\ns_nop 3\ns_cmp_lg_u64 vcc, 0\ns_cselect_b32 s67, 1, 0\ns_cselect_b32 s6, 0x40000000, s66\nCvav 56,160,192,12\nRa 144,2,53248\ns_add_u32 s5, s6, 0\n"
"Mvav 56,164,200\ns_add_u32 s49, s49, 131072\nEX 82,46\nMvav 56,168,208\nRa 148,2,54272\nv_xor_b32 v26, 0x10000, v26\n"
"buffer_load_dwordx4 a[192:195], v1, s[16:19], s5 offen offset:0 sc1\nMvav 56,172,216\nWL 6\nEX 83,47\nCvav 60,172,220,16\nRa 152,2,55296\nAD 85,85,82\nMvav 60,168,212\n"
"D1 1,16,46,1024\nMvav 60,164,204\nRa 156,2,56320\nMvav 60,160,196\nAD 85,85,83\nCV 71,82,83\nMvav 60,176,228\nRa 160,2,57344\nv_max_f32 v90, v84, v85\nMvav 60,180,236\n"
"s_add_u32 s88, s88, 32768\nEX 80,48\nMvav 60,184,244\nRa 164,2,58368\nv_cmp_lt_f32 vcc, s63, v90\nMvav 60,188,252\nWL 8\nEX 81,49\nMvav 56,188,248\nRa 168,2,59392\n"
"s_cbranch_vccnz .T59\n.T60:\nbuffer_load_dwordx4 a[196:199], v1, s[16:19], s5 offen offset:1024 sc1\nMvav 56,184,240\nD1 1,16,46,2048\nMvav 56,180,232\nRa 172,2,60416\n"
"AD 86,86,84\nMvav 56,176,224\nAD 84,80,81\nCV 72,80,81\nMaav 0,128,64\nRa 176,2,61440\nMaav 4,128,68\nEX 82,50\nMaav 12,132,68\nRa 180,2,62464\nMaav 8,132,64\nWL 8\nEX 83,51\n"
"Maav 16,136,64\nRa 184,2,63488\nAD 84,84,82\nbuffer_load_dwordx4 a[200:203], v1, s[16:19], s5 offen offset:2048 sc1\nMaav 20,136,68\nD1 1,16,46,3072\ns_add_u32 m0, s89, 16384\n"
"Maav 28,140,68\nRa 188,2,64512\ns_barrier\nMaav 24,140,64\nAD 84,84,83\nCV 73,82,83\nMaav 32,144,64\nRa 128,24,0\nMaav 36,144,68\ns_add_u32 s46, s87, s88\nEX 80,52\n"
"Maav 44,148,68\nRa 132,24,1024\nv_xor_b32 v2, 0x10000, v2\nMaav 40,148,64\nWL 8\nEX 81,53\nMaav 48,152,64\nRa 136,24,2048\nAD 87,87,85\n"
"buffer_load_dwordx4 a[204:207], v1, s[16:19], s5 offen offset:3072 sc1\nMaav 52,152,68\nD1 1,16,46,0\nMaav 60,156,68\nRa 140,24,3072\nMaav 56,156,64\nAD 85,80,81\nCV 76,80,81\n"
"Maav 64,160,64\nRa 144,26,0\nMaav 68,160,68\ns_cmp_lg_u32 s92, 0\nEX 82,54\nMaav 76,164,68\nRa 148,26,1024\ns_cbranch_scc1 .E55\n.E56:\ns_add_u32 s5, s6, 4096\nMaav 72,164,64\n"
"WL 8\nEX 83,55\nMaav 80,168,64\nRa 152,26,2048\nAD 85,85,82\nbuffer_load_dwordx4 a[208:211], v1, s[16:19], s5 offen offset:0 sc1\nMaav 84,168,68\nD1 1,16,46,1024\nMaav 92,172,68\n"
"Ra 156,26,3072\nMaav 88,172,64\nAD 85,85,83\nCV 77,82,83\nMaav 96,176,64\nRa 160,24,8192\nMaav 100,176,68\nEX 80,56\nMaav 108,180,68\nRa 164,24,9216\nMaav 104,180,64\nWL 6\n"
"EX 81,57\nMaav 112,184,64\nRa 168,24,10240\nAD 84,84,80\nbuffer_load_dwordx4 a[212:215], v1, s[16:19], s5 offen offset:1024 sc1\nMaav 116,184,68\nD1 1,16,46,2048\nMaav 124,188,68\n"
"Ra 172,24,11264\nMaav 120,188,64\nAD 84,84,81\nCV 74,80,81\ns_sub_u32 s99, s99, 1\nCvav 32,128,192,4\nRa 176,26,8192\nMvav 32,132,200\nEX 82,58\nMvav 32,136,208\nRa 180,26,9216\n"
"Mvav 32,140,216\nWL 6\nEX 83,59\nCvav 36,140,220,8\nRa 184,26,10240\nAD 84,84,82\nbuffer_load_dwordx4 a[216:219], v1, s[16:19], s5 offen offset:2048 sc1\nMvav 36,136,212\n"
"D1 1,16,46,3072\ns_add_u32 m0, s89, 32768\nMvav 36,132,204\nRa 188,26,11264\nMvav 36,128,196\nAD 84,84,83\nCV 75,82,83\nMvav 36,144,228\nRa 128,2,16384\nMvav 36,148,236\n"
"s_add_u32 s46, s86, s88\nEX 80,60\nMvav 36,152,244\nRa 132,2,17408\nMvav 36,156,252\nWL 6\nEX 81,61\nMvav 32,156,248\nRa 136,2,18432\nAD 85,85,80\n"
"buffer_load_dwordx4 a[220:223], v1, s[16:19], s5 offen offset:3072 sc1\nMvav 32,152,240\nD1 1,16,46,0\nMvav 32,148,232\nRa 140,2,19456\nSv 112,1,16,49,0\nMvav 32,144,224\n"
"AD 85,85,81\nCV 78,80,81\nCvav 40,160,192,12\nRa 144,2,20480\nMvav 40,164,200\ns_mov_b32 s87, s95\nEX 82,62\nMvav 40,168,208\nRa 148,2,21504\ns_add_u32 s5, s6, 16384\n"
"Mvav 40,172,216\nWL 6\nEX 83,63\nCvav 44,172,220,16\nRa 152,2,22528\nAD 85,85,82\nbuffer_load_dwordx4 a[224:227], v1, s[16:19], s5 offen offset:0 sc1\nMvav 44,168,212\n"
"D1 1,16,46,1024\nMvav 44,164,204\nRa 156,2,23552\nMvav 44,160,196\nAD 85,85,83\nCV 79,82,83\nMvav 44,176,228\nRa 160,2,24576\nv_max_f32 v90, v84, v85\nMvav 44,180,236\n"
"s_add_u32 s88, s88, 32768\nEX 80,32\nMvav 44,184,244\nRa 164,2,25600\nv_cmp_lt_f32 vcc, s63, v90\nMvav 44,188,252\nWL 8\nEX 81,33\nMvav 40,188,248\nRa 168,2,26624\n"
"s_cbranch_vccnz .T64\n.T65:\nbuffer_load_dwordx4 a[228:231], v1, s[16:19], s5 offen offset:1024 sc1\nMvav 40,184,240\nD1 1,16,46,2048\nMvav 40,180,232\nRa 172,2,27648\n"
"AD 86,86,84\nMvav 40,176,224\nAD 84,80,81\nCV 64,80,81\nMaav 0,128,72\nRa 176,2,28672\nMaav 4,128,76\nEX 82,34\nMaav 12,132,76\nRa 180,2,29696\nMaav 8,132,72\n"
"s_waitcnt vmcnt(23) lgkmcnt(8)\nEX 83,35\nMaav 16,136,72\nRa 184,2,30720\nAD 84,84,82\nbuffer_load_dwordx4 a[232:235], v1, s[16:19], s5 offen offset:2048 sc1\nMaav 20,136,76\n"
"D1 1,16,46,3072\ns_add_u32 m0, s89, 49152\nMaav 28,140,76\nRa 188,2,31744\ns_barrier\nMaav 24,140,72\nAD 84,84,83\nCV 65,82,83\nMaav 32,144,72\nRa 128,24,32768\nMaav 36,144,76\n"
"s_add_u32 s46, s87, s88\nEX 80,36\nMaav 44,148,76\nRa 132,24,33792\nMaav 40,148,72\nWL 8\nEX 81,37\nMaav 48,152,72\nRa 136,24,34816\nAD 87,87,85\n"
"buffer_load_dwordx4 a[236:239], v1, s[16:19], s5 offen offset:3072 sc1\nMaav 52,152,76\nD1 1,16,46,0\nMaav 60,156,76\nRa 140,24,35840\nMaav 56,156,72\nAD 85,80,81\nCV 68,80,81\n"
"Maav 64,160,72\nRa 144,26,32768\nMaav 68,160,76\ns_cmp_eq_u32 s13, 7\nEX 82,38\nMaav 76,164,76\nRa 148,26,33792\ns_cbranch_scc1 .E62\n.E63:\ns_add_u32 s5, s6, 20480\n"
"Maav 72,164,72\nWL 8\nEX 83,39\nMaav 80,168,72\nRa 152,26,34816\nAD 85,85,82\nbuffer_load_dwordx4 a[240:243], v1, s[16:19], s5 offen offset:0 sc1\nMaav 84,168,76\nD1 1,16,46,1024\n"
"Maav 92,172,76\nRa 156,26,35840\nMaav 88,172,72\nAD 85,85,83\nCV 69,82,83\nMaav 96,176,72\nRa 160,24,40960\nMaav 100,176,76\ns_xor_b32 s89, s89, 0x10000\nEX 80,40\n"
"Maav 108,180,76\nRa 164,24,41984\nMaav 104,180,72\nWL 6\nEX 81,41\nMaav 112,184,72\nRa 168,24,43008\nAD 84,84,80\n"
"buffer_load_dwordx4 a[244:247], v1, s[16:19], s5 offen offset:1024 sc1\nMaav 116,184,76\nD1 1,16,46,2048\nMaav 124,188,76\nRa 172,24,44032\nMaav 120,188,72\nAD 84,84,81\n"
"CV 66,80,81\nCvav 48,128,192,4\nRa 176,26,40960\nMvav 48,132,200\nEX 82,42\nMvav 48,136,208\nRa 180,26,41984\nMvav 48,140,216\nWL 6\nEX 83,43\nCvav 52,140,220,8\nRa 184,26,43008\n"
"AD 84,84,82\nbuffer_load_dwordx4 a[248:251], v1, s[16:19], s5 offen offset:2048 sc1\nMvav 52,136,212\nD1 1,16,46,3072\ns_add_u32 m0, s89, 0\nMvav 52,132,204\nRa 188,26,44032\n"
"Mvav 52,128,196\nAD 84,84,83\nCV 67,82,83\nMvav 52,144,228\nRa 128,2,49152\nMvav 52,148,236\ns_add_u32 s46, s86, s88\nEX 80,44\nMvav 52,152,244\nRa 132,2,50176\n"
"v_xor_b32 v24, 0x10000, v24\nMvav 52,156,252\nWL 6\nEX 81,45\nMvav 48,156,248\nRa 136,2,51200\nAD 85,85,80\nbuffer_load_dwordx4 a[252:255], v1, s[16:19], s5 offen offset:3072 sc1\n"
"Mvav 48,152,240\nD1 1,16,46,0\nMvav 48,148,232\nRa 140,2,52224\nSv 112,1,16,49,1024\nMvav 48,144,224\nAD 85,85,81\nCV 70,80,81\nCvav 56,160,192,12\nRa 144,2,53248\n"
"Mvav 56,164,200\ns_add_u32 s49, s49, 131072\nEX 82,46\nMvav 56,168,208\nRa 148,2,54272\nv_xor_b32 v26, 0x10000, v26\nMvav 56,172,216\nWL 6\nEX 83,47\nCvav 60,172,220,16\n"
"Ra 152,2,55296\nAD 85,85,82\nMvav 60,168,212\nD1 1,16,46,1024\nMvav 60,164,204\nRa 156,2,56320\nMvav 60,160,196\nAD 85,85,83\nCV 71,82,83\nMvav 60,176,228\nRa 160,2,57344\n"
"v_max_f32 v90, v84, v85\nMvav 60,180,236\ns_add_u32 s88, s88, 32768\nEX 80,48\nMvav 60,184,244\nRa 164,2,58368\nv_cmp_lt_f32 vcc, s63, v90\nMvav 60,188,252\nWL 8\nEX 81,49\n"
"Mvav 56,188,248\nRa 168,2,59392\ns_cbranch_vccnz .T66\n.T67:\nMvav 56,184,240\nD1 1,16,46,2048\nMvav 56,180,232\nRa 172,2,60416\nAD 86,86,84\nMvav 56,176,224\nAD 84,80,81\n"
"CV 72,80,81\nMaav 0,128,64\nRa 176,2,61440\nMaav 4,128,68\nEX 82,50\nMaav 12,132,68\nRa 180,2,62464\nMaav 8,132,64\ns_waitcnt vmcnt(22) lgkmcnt(8)\nEX 83,51\nMaav 16,136,64\n"
"Ra 184,2,63488\nAD 84,84,82\nMaav 20,136,68\nD1 1,16,46,3072\ns_add_u32 m0, s89, 16384\nMaav 28,140,68\nRa 188,2,64512\ns_barrier\nMaav 24,140,64\nAD 84,84,83\nCV 73,82,83\n"
"Maav 32,144,64\nRa 128,24,0\nMaav 36,144,68\ns_add_u32 s46, s87, s88\nEX 80,52\nMaav 44,148,68\nRa 132,24,1024\nv_xor_b32 v2, 0x10000, v2\nMaav 40,148,64\nWL 8\nEX 81,53\n"
"Maav 48,152,64\nRa 136,24,2048\nAD 87,87,85\nMaav 52,152,68\nD1 1,16,46,0\nMaav 60,156,68\nRa 140,24,3072\nMaav 56,156,64\nAD 85,80,81\nCV 76,80,81\ns_cmp_lg_u32 s67, 0\n"
"s_cbranch_scc1 .E69\n.E70:\nMaav 64,160,64\nRa 144,26,0\nMaav 68,160,68\nEX 82,54\nMaav 76,164,68\nRa 148,26,1024\nMaav 72,164,64\nWL 8\nEX 83,55\nMaav 80,168,64\nRa 152,26,2048\n"
"AD 85,85,82\nMaav 84,168,68\nD1 1,16,46,1024\nMaav 92,172,68\nRa 156,26,3072\nMaav 88,172,64\nAD 85,85,83\nCV 77,82,83\nMaav 96,176,64\nRa 160,24,8192\nMaav 100,176,68\nEX 80,56\n"
"Maav 108,180,68\nRa 164,24,9216\nMaav 104,180,64\nWL 10\nEX 81,57\nMaav 112,184,64\nRa 168,24,10240\nAD 84,84,80\nMaav 116,184,68\nD1 1,16,46,2048\nMaav 124,188,68\n"
"Ra 172,24,11264\nMaav 120,188,64\nAD 84,84,81\nCV 74,80,81\ns_cmp_lg_u32 s67, 0\ns_cbranch_scc1 .D71\n.D72:\nWV 8\nv_cmp_lt_f32 vcc, 0, v101\nSU 20,100,88\n"
"v_mul_f32 v101, 0x3e800000, v101\nv_cndmask_b32 v20, 0, v20, vcc\nv_cmp_lt_f32 vcc, 0, v105\nSU 21,104,89\nv_mul_f32 v105, 0x3e800000, v105\nv_cndmask_b32 v21, 0, v21, vcc\n"
"v_or_b32 v91, v20, v21\nv_cmp_ne_u32 vcc, 0, v91\ns_nop 0\ns_cbranch_vccnz .D73\nAD 86,86,101\nAD 87,87,105\n.D74:\nMava 0,130,192\nMava 8,128,192\nEX 82,58\nMava 12,128,196\n"
"Mava 4,130,196\nEX 83,59\nMava 16,130,200\nAD 84,84,82\nMava 24,128,200\nD1 1,16,46,3072\nMava 28,128,204\ns_add_u32 m0, s89, 32768\ns_add_u32 s46, s86, s88\nMava 20,130,204\n"
"AD 84,84,83\nCV 75,82,83\nMava 32,130,208\nRa 128,2,16384\nv_xor_b32 v3, 0x10000, v2\nMava 40,128,208\nEX 80,60\nMava 44,128,212\nRa 132,2,17408\nv_add_u32 v0, 0x20000, v1\n"
"Mava 36,130,212\nEX 81,61\nMava 48,130,216\nRa 136,2,18432\nAD 85,85,80\nMava 56,128,216\nD1 1,16,46,0\ns_sub_u32 s94, s49, 1966080\nMava 60,128,220\nRa 140,2,19456\n"
"s_add_u32 s95, s94, 1048576\nMava 52,130,220\nAD 85,85,81\nCV 78,80,81\nMava 64,130,224\nRa 144,2,20480\ns_xor_b32 s92, s94, 0x202000\nMava 72,128,224\nEX 82,62\nMava 76,128,228\n"
"Ra 148,2,21504\ns_add_u32 s93, s96, 983040\nMava 68,130,228\nEX 83,63\nMava 80,130,232\nRa 152,2,22528\nAD 85,85,82\nMava 88,128,232\nD1 1,16,46,1024\n"
"s_xor_b32 s93, s93, 0x202000\nMava 92,128,236\nRa 156,2,23552\ns_lshl_b32 s89, s11, 12\nMava 84,130,236\nAD 85,85,83\nCV 79,82,83\nMava 96,130,240\nRa 160,2,24576\n"
"v_max_f32 v90, v84, v85\nMava 104,128,240\nv_cmp_lt_f32 vcc, s63, v90\nMava 108,128,244\nRa 164,2,25600\nMava 100,130,244\nWL 8\nMava 112,130,248\nRa 168,2,26624\n"
"Mava 120,128,248\nD1 1,16,46,2048\nMava 124,128,252\nRa 172,2,27648\nMava 116,130,252\ns_cbranch_vccnz .T78\n.T79:\nAD 86,86,84\nAD 87,87,85\nMaav 0,128,72\nRa 176,2,28672\n"
"v_mov_b32 v84, v86\nv_mov_b32 v85, v87\nMaav 4,128,76\nv_permlane32_swap_b32 v86, v84\nv_permlane32_swap_b32 v87, v85\nMaav 12,132,76\nRa 180,2,29696\nAD 86,86,84\nMaav 8,132,72\n"
"WL 8\nAD 87,87,85\nMaav 16,136,72\nRa 184,2,30720\nv_mov_b32 v84, v86\nv_mov_b32 v85, v87\nMaav 20,136,76\nD1 1,16,46,3072\nv_permlane16_swap_b32 v86, v84\nMaav 28,140,76\n"
"Ra 188,2,31744\nv_permlane16_swap_b32 v87, v85\nMaav 24,140,72\nAD 86,86,84\nAD 87,87,85\nMaav 32,144,72\nRa 128,2,0\nv_rcp_f32 v86, v86\nMaav 36,144,76\nv_rcp_f32 v87, v87\n"
"Maav 44,148,76\nRa 132,2,1024\nAR 82,0\nAR 83,1\nMU 82,82,86\nMU 83,83,86\nMaav 40,148,72\nWL 8\ns_add_u32 m0, s89, 131072\ns_add_u32 s46, s93, 0\nCV 96,82,83\nAR 82,2\n"
"Maav 48,152,72\nRa 136,2,2048\nAR 83,3\nMU 82,82,86\nMU 83,83,86\nCV 97,82,83\nMaav 52,152,76\nD1 1,16,46,0\nAR 82,4\nAR 83,5\nMU 82,82,87\nMU 83,83,87\nMaav 60,156,76\n"
"Ra 140,2,3072\nCV 100,82,83\nAR 82,6\nAR 83,7\nMU 82,82,87\nMaav 56,156,72\nMU 83,83,87\nCV 101,82,83\nAR 82,8\nAR 83,9\nMU 82,82,86\nMaav 64,160,72\nRa 144,2,4096\nMU 83,83,86\n"
"CV 98,82,83\nAR 82,10\nAR 83,11\nMaav 68,160,76\nMU 82,82,86\nMU 83,83,86\nCV 99,82,83\ns_add_u32 s5, s94, 0\nSv 96,1,16,5,0\nMaav 76,164,76\nRa 148,2,5120\nAR 82,12\nAR 83,13\n"
"MU 82,82,87\nMU 83,83,87\nMaav 72,164,72\nWL 8\nCV 102,82,83\nAR 82,14\nAR 83,15\nMU 82,82,87\nMaav 80,168,72\nRa 152,2,6144\nMU 83,83,87\nCV 103,82,83\nSv 100,1,16,5,1024\n"
"AR 82,16\nMaav 84,168,76\nD1 1,16,46,1024\nAR 83,17\nMU 82,82,86\nMU 83,83,86\nCV 104,82,83\nMaav 92,172,76\nRa 156,2,7168\nAR 82,18\nAR 83,19\nMU 82,82,86\nMU 83,83,86\n"
"Maav 88,172,72\nCV 105,82,83\nAR 82,20\nAR 83,21\nMU 82,82,87\nMU 83,83,87\nMaav 96,176,72\nRa 160,2,8192\nCV 108,82,83\nAR 82,22\nAR 83,23\nMU 82,82,87\nMaav 100,176,76\n"
"MU 83,83,87\nCV 109,82,83\nAR 82,24\nAR 83,25\nMU 82,82,86\nMaav 108,180,76\nRa 164,2,9216\nMU 83,83,86\nCV 106,82,83\nAR 82,26\nAR 83,27\nMaav 104,180,72\nWL 8\nMU 82,82,86\n"
"MU 83,83,86\nCV 107,82,83\ns_add_u32 s5, s94, 131072\nMaav 112,184,72\nRa 168,2,10240\nSv 104,1,16,5,0\nAR 82,28\nAR 83,29\nMU 82,82,87\nMaav 116,184,76\nD1 1,16,46,2048\n"
"MU 83,83,87\nCV 110,82,83\nAR 82,30\nAR 83,31\nMaav 124,188,76\nRa 172,2,11264\nMU 82,82,87\nMU 83,83,87\nCV 111,82,83\nSv 108,1,16,5,1024\nMaav 120,188,72\nAR 82,32\nAR 83,33\n"
"MU 82,82,86\nZvav 136,128,96\nRa 176,2,12288\nMU 83,83,86\nCV 112,82,83\nMvav 136,132,104\nAR 82,34\nAR 83,35\nZvav 140,132,108\nRa 180,2,13312\nMU 82,82,86\nMU 83,83,86\n"
"Mvav 140,128,100\ns_waitcnt vmcnt(18) lgkmcnt(8)\nCV 113,82,83\nAR 82,36\nZvav 148,136,100\nRa 184,2,14336\nAR 83,37\nMU 82,82,87\nMvav 148,140,108\nD1 1,16,46,3072\nMU 83,83,87\n"
"CV 116,82,83\nZvav 144,140,104\nRa 188,2,15360\ns_barrier\nAR 82,38\nMvav 144,136,96\nAR 83,39\nMU 82,82,87\nMU 83,83,87\nZvav 152,144,96\nRa 128,2,32768\nCV 117,82,83\nAR 82,40\n"
"v_lshrrev_b32 v88, 8, v1\nMvav 152,148,104\nAR 83,41\nMU 82,82,86\nMU 83,83,86\nbuffer_store_dword v25, off, s[16:19], s54 offset:512 sc0 sc1\nZvav 156,148,108\nRa 132,2,33792\n"
"CV 114,82,83\nAR 82,42\nMvav 156,144,100\nWL 8\ns_add_u32 m0, s89, 114688\ns_add_u32 s46, s96, 1179648\nZvav 164,152,100\nRa 136,2,34816\nAR 83,43\nMU 82,82,86\nMvav 164,156,108\n"
"D1 1,16,46,0\nMU 83,83,86\nCV 115,82,83\nZvav 160,156,104\nRa 140,2,35840\ns_add_u32 s5, s94, 262144\nSv 112,1,16,5,0\nMvav 160,152,96\nAR 82,44\nAR 83,45\nMU 82,82,87\n"
"Zvav 168,160,96\nRa 144,2,36864\nMU 83,83,87\nCV 118,82,83\nv_lshlrev_b32 v89, 5, v88\nMvav 168,164,104\nAR 82,46\nAR 83,47\n"
"buffer_load_dwordx4 v[64:67], v89, s[16:19], s40 offen offset:0 sc0 sc1\nZvav 172,164,108\nRa 148,2,37888\nMU 82,82,87\nMU 83,83,87\nMvav 172,160,100\nWL 8\nCV 119,82,83\n"
"Sv 116,1,16,5,1024\nZvav 180,168,100\nRa 152,2,38912\nAR 82,48\nAR 83,49\nMvav 180,172,108\nD1 1,16,46,1024\nMU 82,82,86\nMU 83,83,86\nZvav 176,172,104\nRa 156,2,39936\n"
"CV 120,82,83\nAR 82,50\nMvav 176,168,96\nAR 83,51\nMU 82,82,86\nMU 83,83,86\nZvav 184,176,96\nRa 160,2,40960\nCV 121,82,83\nAR 82,52\n"
"buffer_load_dwordx4 v[68:71], v89, s[16:19], s40 offen offset:16 sc0 sc1\nMvav 184,180,104\nAR 83,53\nMU 82,82,87\nMU 83,83,87\nZvav 188,180,108\nRa 164,2,41984\nCV 124,82,83\n"
"AR 82,54\nMvav 188,176,100\nWL 8\nAR 83,55\nMU 82,82,87\nZvav 196,184,100\nRa 168,2,43008\nMU 83,83,87\nCV 125,82,83\nMvav 196,188,108\nD1 1,16,46,2048\nAR 82,56\nAR 83,57\n"
"Zvav 192,188,104\nRa 172,2,44032\nMU 82,82,86\nMU 83,83,86\nMvav 192,184,96\nCV 122,82,83\nAR 82,58\nAR 83,59\nZvav 200,128,96\nRa 176,2,45056\nMU 82,82,86\nMU 83,83,86\n"
"buffer_load_dwordx4 v[72:75], v89, s[16:19], s40 offen offset:1792 sc0 sc1\nMvav 200,132,104\nCV 123,82,83\ns_add_u32 s5, s94, 393216\nZvav 204,132,108\nRa 180,2,46080\n"
"Sv 120,1,16,5,0\nAR 82,60\nMvav 204,128,100\nWL 8\nAR 83,61\nMU 82,82,87\nZvav 212,136,100\nRa 184,2,47104\nMU 83,83,87\nCV 126,82,83\nMvav 212,140,108\nD1 1,16,46,3072\nAR 82,62\n"
"AR 83,63\nZvav 208,140,104\nRa 188,2,48128\nMU 82,82,87\nMU 83,83,87\nMvav 208,136,96\nCV 127,82,83\nSv 124,1,16,5,1024\n"
"buffer_load_dwordx4 v[76:79], v89, s[16:19], s40 offen offset:1808 sc0 sc1\nZvav 216,144,96\nRa 128,2,49152\nAR 82,64\nAR 83,65\nMvav 216,148,104\nMU 82,82,86\nMU 83,83,86\n"
"Zvav 220,148,108\nRa 132,2,50176\nCV 32,82,83\nAR 82,66\nMvav 220,144,100\nWL 8\ns_add_u32 m0, s89, 147456\ns_add_u32 s46, s93, 32768\nZvav 228,152,100\nRa 136,2,51200\nAR 83,67\n"
"MU 82,82,86\nMvav 228,156,108\nD1 1,16,46,0\nMU 83,83,86\nCV 33,82,83\nZvav 224,156,104\nRa 140,2,52224\nAR 82,68\nv_bfe_u32 v90, v1, 4, 4\nMvav 224,152,96\nAR 83,69\nMU 82,82,87\n"
"MU 83,83,87\nZvav 232,160,96\nRa 144,2,53248\nCV 36,82,83\nAR 82,70\nv_mul_u32_u24 v91, 896, v90\nMvav 232,164,104\nAR 83,71\nMU 82,82,87\nv_lshl_add_u32 v91, v88, 4, v91\n"
"Zvav 236,164,108\nRa 148,2,54272\nMU 83,83,87\nCV 37,82,83\nMvav 236,160,100\nWL 8\nAR 82,72\nbuffer_load_dwordx4 v[12:15], v91, s[28:31], s22 offen offset:0\nZvav 244,168,100\n"
"Ra 152,2,55296\nAR 83,73\nMU 82,82,86\nMvav 244,172,108\nD1 1,16,46,1024\nMU 83,83,86\nCV 34,82,83\nZvav 240,172,104\nRa 156,2,56320\nAR 82,74\nMvav 240,168,96\nAR 83,75\n"
"MU 82,82,86\nMU 83,83,86\nMvav 136,176,112\nRa 160,2,57344\nCV 35,82,83\ns_add_u32 s5, s94, 524288\nbuffer_load_dwordx4 v[16:19], v91, s[28:31], s23 offen offset:0\n"
"Mvav 136,180,120\nSv 32,1,16,5,0\nAR 82,76\nv_add_u32 v90, s21, v90\nMvav 140,180,124\nRa 164,2,58368\nAR 83,77\nMU 82,82,87\nMvav 140,176,116\nWL 8\nMU 83,83,87\n"
"v_lshlrev_b32 v88, 2, v90\nMvav 148,184,116\nRa 168,2,59392\nCV 38,82,83\nAR 82,78\nMvav 148,188,124\nD1 1,16,46,2048\nAR 83,79\nMU 82,82,87\nMvav 144,188,120\nRa 172,2,60416\n"
"MU 83,83,87\nMvav 144,184,112\nCV 39,82,83\nSv 36,1,16,5,1024\nAR 82,80\nMvav 152,128,112\nRa 176,2,61440\nAR 83,81\nMU 82,82,86\nMvav 152,132,120\nMU 83,83,86\nCV 40,82,83\n"
"Mvav 156,132,124\nRa 180,2,62464\nAR 82,82\nAR 83,83\nMvav 156,128,116\ns_waitcnt vmcnt(32) lgkmcnt(8)\nMU 82,82,86\nMvav 164,136,116\nRa 184,2,63488\nMU 83,83,86\nCV 41,82,83\n"
"Mvav 164,140,124\nD1 1,16,46,3072\nAR 82,84\nAR 83,85\nMvav 160,140,120\nRa 188,2,64512\ns_barrier\nMvav 160,136,112\nMU 82,82,87\nMU 83,83,87\nCV 44,82,83\nMvav 168,144,112\n"
"Ra 128,3,0\nAR 82,86\nAR 83,87\nMvav 168,148,120\nMU 82,82,87\nMU 83,83,87\nMvav 172,148,124\nRa 132,3,1024\nCV 45,82,83\nAR 82,88\nMvav 172,144,116\nWL 8\n"
"s_add_u32 m0, s89, 16384\ns_add_u32 s46, s93, 65536\nMvav 180,152,116\nRa 136,3,2048\nAR 83,89\nMU 82,82,86\nMvav 180,156,124\nD1 1,16,46,0\nMU 83,83,86\nCV 42,82,83\n"
"Mvav 176,156,120\nRa 140,3,3072\nAR 82,90\nMvav 176,152,112\nAR 83,91\nMU 82,82,86\nMU 83,83,86\nMvav 184,160,112\nRa 144,3,4096\nCV 43,82,83\ns_add_u32 s5, s94, 655360\n"
"Mvav 184,164,120\nSv 40,1,16,5,0\nAR 82,92\nbuffer_load_dword v27, off, s[16:19], s60 offset:512 sc0 sc1\nMvav 188,164,124\nRa 148,3,5120\nAR 83,93\nMU 82,82,87\nMvav 188,160,116\n"
"WL 8\nMU 83,83,87\nMvav 196,168,116\nRa 152,3,6144\nCV 46,82,83\nAR 82,94\nMvav 196,172,124\nD1 1,16,46,1024\nAR 83,95\nMU 82,82,87\nMvav 192,172,120\nRa 156,3,7168\nMU 83,83,87\n"
"Mvav 192,168,112\nCV 47,82,83\nSv 44,1,16,5,1024\nMvav 200,176,112\nRa 160,3,8192\nAR 82,96\nAR 83,97\nMvav 200,180,120\nMU 82,82,86\nMU 83,83,86\nMvav 204,180,124\nRa 164,3,9216\n"
"CV 48,82,83\nAR 82,98\nMvav 204,176,116\nWL 8\nAR 83,99\nMvav 212,184,116\nRa 168,3,10240\nMU 82,82,86\nMU 83,83,86\nMvav 212,188,124\nD1 1,16,46,2048\nCV 49,82,83\nAR 82,100\n"
"Mvav 208,188,120\nRa 172,3,11264\nAR 83,101\nMvav 208,184,112\nMU 82,82,87\nMU 83,83,87\nCV 52,82,83\nMvav 216,128,112\nRa 176,3,12288\nAR 82,102\nAR 83,103\nWV 10\n"
"v_xor_b32 v92, s15, v66\nv_xor_b32 v93, s15, v70\nv_xor_b32 v94, s15, v74\nv_or3_b32 v92, v92, v93, v94\nv_xor_b32 v93, s15, v78\nMvav 216,132,120\nMU 82,82,87\nMU 83,83,87\n"
"v_or_b32 v92, v92, v93\nv_cmp_ne_u32 vcc, 0, v92\ns_nop 0\ns_cbranch_vccnz .S87\n.S88:\ns_mov_b32 s2, 0xc040000\nbuffer_load_dword v80, v88, s[16:19], s2 offen sc0 sc1\n"
"Mvav 220,132,124\nRa 180,3,13312\nCV 53,82,83\nAR 82,104\nMvav 220,128,116\nWL 8\nAR 83,105\nMvav 228,136,116\nRa 184,3,14336\nMU 82,82,86\nMU 83,83,86\nMvav 228,140,124\n"
"D1 1,16,46,3072\nCV 50,82,83\nAR 82,106\nbuffer_load_dword v81, v88, s[16:19], s2 offen offset:256 sc0 sc1\nv_and_b32 v92, 0x7ff, v67\nv_lshrrev_b32 v93, 16, v67\n"
"v_add_u32 v94, 64, v90\nMvav 224,140,120\nRa 188,3,15360\nAR 83,107\nv_cmp_eq_u32 vcc, v92, v90\ns_nop 1\nv_cndmask_b32 v12, v12, v64, vcc\nv_cndmask_b32 v13, v13, v65, vcc\n"
"v_cndmask_b32 v14, v14, v68, vcc\nv_cndmask_b32 v15, v15, v69, vcc\nMvav 224,136,112\nMU 82,82,86\nMU 83,83,86\nCV 51,82,83\nMvav 232,144,112\nRa 128,3,16384\n"
"s_add_u32 s5, s94, 786432\nSv 48,1,16,5,0\nMvav 232,148,120\nAR 82,108\nAR 83,109\nMvav 236,148,124\nRa 132,3,17408\nMU 82,82,87\nMU 83,83,87\nMvav 236,144,116\nWL 8\n"
"s_add_u32 m0, s89, 0\ns_add_u32 s46, s93, 98304\nMvav 244,152,116\nRa 136,3,18432\nCV 54,82,83\nAR 82,110\nMvav 244,156,124\nD1 1,16,46,0\nAR 83,111\nMU 82,82,87\n"
"Mvav 240,156,120\nRa 140,3,19456\nMU 83,83,87\nMvav 240,152,112\nCV 55,82,83\nSv 52,1,16,5,1024\nAR 82,112\nMvav 136,160,32\nRa 144,3,20480\nAR 83,113\nMU 82,82,86\n"
"Mvav 136,164,40\nMU 83,83,86\nCV 56,82,83\nMvav 140,164,44\nRa 148,3,21504\nAR 82,114\nAR 83,115\nMvav 140,160,36\nWL 8\nMU 82,82,86\nMvav 148,168,36\nRa 152,3,22528\nMU 83,83,86\n"
"CV 57,82,83\nMvav 148,172,44\nD1 1,16,46,1024\nAR 82,116\nAR 83,117\nMvav 144,172,40\nRa 156,3,23552\nMU 82,82,87\nv_cmp_eq_u32 vcc, v93, v90\ns_nop 1\n"
"v_cndmask_b32 v12, v12, v72, vcc\nv_cndmask_b32 v13, v13, v73, vcc\nv_cndmask_b32 v14, v14, v76, vcc\nv_cndmask_b32 v15, v15, v77, vcc\nMvav 144,168,32\nMU 83,83,87\nCV 60,82,83\n"
"AR 82,118\nMvav 152,176,32\nRa 160,3,24576\nAR 83,119\nMU 82,82,87\nMvav 152,180,40\nMU 83,83,87\nCV 61,82,83\nMvav 156,180,44\nRa 164,3,25600\nAR 82,120\nAR 83,121\n"
"Mvav 156,176,36\nWL 8\nMU 82,82,86\nv_cmp_eq_u32 vcc, v92, v94\ns_nop 1\nv_cndmask_b32 v16, v16, v64, vcc\nv_cndmask_b32 v17, v17, v65, vcc\nv_cndmask_b32 v18, v18, v68, vcc\n"
"v_cndmask_b32 v19, v19, v69, vcc\nMvav 164,184,36\nRa 168,3,26624\nMU 83,83,86\nCV 58,82,83\nMvav 164,188,44\nD1 1,16,46,2048\nAR 82,122\nAR 83,123\nMvav 160,188,40\n"
"Ra 172,3,27648\nMU 82,82,86\nMvav 160,184,32\nMU 83,83,86\nCV 59,82,83\ns_add_u32 s5, s94, 917504\nMvav 168,128,32\nRa 176,3,28672\nSv 56,1,16,5,0\nAR 82,124\nMvav 168,132,40\n"
"AR 83,125\nMU 82,82,87\nMvav 172,132,44\nRa 180,3,29696\nMU 83,83,87\nCV 62,82,83\nMvav 172,128,36\nWL 8\nAR 82,126\nMvav 180,136,36\nRa 184,3,30720\nAR 83,127\nMU 82,82,87\n"
"Mvav 180,140,44\nD1 1,16,46,3072\nMU 83,83,87\nCV 63,82,83\nMvav 176,140,40\nRa 188,3,31744\ns_barrier\nv_cmp_eq_u32 vcc, v93, v94\ns_nop 1\nv_cndmask_b32 v16, v16, v72, vcc\n"
"v_cndmask_b32 v17, v17, v73, vcc\nv_cndmask_b32 v18, v18, v76, vcc\nv_cndmask_b32 v19, v19, v77, vcc\nMvav 176,136,32\nSv 60,1,16,5,1024\nMvav 184,144,32\nRa 128,3,32768\n"
"Mvav 184,148,40\nMvav 188,148,44\nRa 132,3,33792\nMvav 188,144,36\nWL 8\ns_add_u32 m0, s89, 32768\ns_add_u32 s46, s93, 131072\nMvav 196,152,36\nRa 136,3,34816\nMvav 196,156,44\n"
"D1 1,16,46,0\nMvav 192,156,40\nRa 140,3,35840\nMvav 192,152,32\nMvav 200,160,32\nRa 144,3,36864\nWV 15\nv_cmp_ne_u32 vcc, s15, v27\ns_nop 0\ns_cbranch_vccnz .S90\n.S91:\n"
"Mvav 200,164,40\ns_add_u32 s4, s92, 0\nMvav 204,164,44\nRa 148,3,37888\nMvav 204,160,36\nWL 8\nbuffer_load_dwordx4 a[192:195], v1, s[16:19], s4 offen offset:0 sc0 sc1\n"
"Mvav 212,168,36\nRa 152,3,38912\nMvav 212,172,44\nD1 1,16,46,1024\nMvav 208,172,40\nRa 156,3,39936\nMvav 208,168,32\n"
"buffer_load_dwordx4 a[196:199], v1, s[16:19], s4 offen offset:1024 sc0 sc1\nMvav 216,176,32\nRa 160,3,40960\nMvav 216,180,40\ns_add_u32 s4, s92, 131072\nMvav 220,180,44\n"
"Ra 164,3,41984\nMvav 220,176,36\nWL 8\nbuffer_load_dwordx4 a[200:203], v1, s[16:19], s4 offen offset:0 sc0 sc1\nMvav 228,184,36\nRa 168,3,43008\nMvav 228,188,44\nD1 1,16,46,2048\n"
"Mvav 224,188,40\nRa 172,3,44032\nMvav 224,184,32\nbuffer_load_dwordx4 a[204:207], v1, s[16:19], s4 offen offset:1024 sc0 sc1\nMvav 232,128,32\nRa 176,3,45056\nMvav 232,132,40\n"
"s_add_u32 s4, s92, 262144\nMvav 236,132,44\nRa 180,3,46080\nMvav 236,128,36\nWL 8\nbuffer_load_dwordx4 a[208:211], v1, s[16:19], s4 offen offset:0 sc0 sc1\nMvav 244,136,36\n"
"Ra 184,3,47104\nMvav 244,140,44\nD1 1,16,46,3072\nMvav 240,140,40\nRa 188,3,48128\nMvav 240,136,32\nbuffer_load_dwordx4 a[212:215], v1, s[16:19], s4 offen offset:1024 sc0 sc1\n"
"Mvav 136,144,48\nRa 128,3,49152\nMvav 136,148,56\ns_add_u32 s4, s92, 393216\nMvav 140,148,60\nRa 132,3,50176\nMvav 140,144,52\nWL 8\ns_add_u32 m0, s89, 49152\n"
"s_add_u32 s46, s93, 163840\nbuffer_load_dwordx4 a[216:219], v1, s[16:19], s4 offen offset:0 sc0 sc1\nMvav 148,152,52\nRa 136,3,51200\nMvav 148,156,60\nD1 1,16,46,0\n"
"Mvav 144,156,56\nRa 140,3,52224\nMvav 144,152,48\nbuffer_load_dwordx4 a[220:223], v1, s[16:19], s4 offen offset:1024 sc0 sc1\nMvav 152,160,48\nRa 144,3,53248\nMvav 152,164,56\n"
"Mvav 156,164,60\nRa 148,3,54272\nMvav 156,160,52\nWL 8\nMvav 164,168,52\nRa 152,3,55296\nMvav 164,172,60\nD1 1,16,46,1024\nMvav 160,172,56\nRa 156,3,56320\nMvav 160,168,48\n"
"Mvav 168,176,48\nRa 160,3,57344\nMvav 168,180,56\ns_add_u32 s4, s92, 524288\nMvav 172,180,60\nRa 164,3,58368\nMvav 172,176,52\nWL 8\n"
"buffer_load_dwordx4 a[224:227], v1, s[16:19], s4 offen offset:0 sc0 sc1\nMvav 180,184,52\nRa 168,3,59392\nMvav 180,188,60\nD1 1,16,46,2048\nMvav 176,188,56\nRa 172,3,60416\n"
"Mvav 176,184,48\nbuffer_load_dwordx4 a[228:231], v1, s[16:19], s4 offen offset:1024 sc0 sc1\nMvav 184,128,48\nRa 176,3,61440\nMvav 184,132,56\ns_add_u32 s4, s92, 655360\n"
"Mvav 188,132,60\nRa 180,3,62464\nMvav 188,128,52\nWL 8\nbuffer_load_dwordx4 a[232:235], v1, s[16:19], s4 offen offset:0 sc0 sc1\nMvav 196,136,52\nRa 184,3,63488\nMvav 196,140,60\n"
"D1 1,16,46,3072\nMvav 192,140,56\nRa 188,3,64512\ns_barrier\nMvav 192,136,48\nbuffer_load_dwordx4 a[236:239], v1, s[16:19], s4 offen offset:1024 sc0 sc1\nMvav 200,144,48\n"
"Ra 128,0,0\nMvav 200,148,56\ns_add_u32 s4, s92, 786432\nMvav 204,148,60\nRa 132,0,1024\nMvav 204,144,52\nWL 8\ns_add_u32 m0, s89, 65536\ns_add_u32 s46, s93, 196608\n"
"buffer_load_dwordx4 a[240:243], v1, s[16:19], s4 offen offset:0 sc0 sc1\nMvav 212,152,52\nRa 136,0,2048\nMvav 212,156,60\nD1 1,16,46,0\nMvav 208,156,56\nRa 140,0,3072\n"
"Mvav 208,152,48\nbuffer_load_dwordx4 a[244:247], v1, s[16:19], s4 offen offset:1024 sc0 sc1\nMvav 216,160,48\nRa 144,0,4096\nMvav 216,164,56\ns_add_u32 s4, s92, 917504\n"
"Mvav 220,164,60\nRa 148,0,5120\nMvav 220,160,52\nWL 8\nbuffer_load_dwordx4 a[248:251], v1, s[16:19], s4 offen offset:0 sc0 sc1\nMvav 228,168,52\nRa 152,0,6144\nMvav 228,172,60\n"
"D1 1,16,46,1024\nMvav 224,172,56\nRa 156,0,7168\nWV 12\nAR 22,195\nMvav 224,168,48\nAR 23,199\nv_max_u32 v21, v22, v23\n"
"buffer_load_dwordx4 a[252:255], v1, s[16:19], s4 offen offset:1024 sc0 sc1\nMvav 232,176,48\nRa 160,0,8192\nAR 22,203\nMvav 232,180,56\nAR 23,207\nv_max3_u32 v21, v21, v22, v23\n"
"Mvav 236,180,60\nRa 164,0,9216\nAR 22,211\nMvav 236,176,52\nWL 6\nAR 23,215\nv_max3_u32 v21, v21, v22, v23\nMvav 244,184,52\nRa 168,0,10240\nAR 22,219\nMvav 244,188,60\n"
"D1 1,16,46,2048\nAR 23,223\nMvav 240,188,56\nRa 172,0,11264\nv_max3_u32 v21, v21, v22, v23\nMvav 240,184,48\nv_cmp_eq_u32 vcc, -1, v21\ns_nop 0\ns_cbranch_vccnz .S81\n.S82:\n"
"Mvaa 136,128,192\nRa 176,0,12288\nMvaa 136,132,200\nMvaa 136,136,208\nRa 180,0,13312\nMvaa 136,140,216\nWL 6\nMvaa 140,140,220\nRa 184,0,14336\nMvaa 140,136,212\nD1 1,16,46,3072\n"
"Mvaa 140,132,204\nRa 188,0,15360\nMvaa 140,128,196\nMvaa 144,144,192\nRa 128,0,16384\nMvaa 144,148,200\nMvaa 144,152,208\nRa 132,0,17408\nMvaa 144,156,216\nWL 6\nMvaa 148,156,220\n"
"Ra 136,0,18432\nMvaa 148,152,212\nMvaa 148,148,204\nRa 140,0,19456\nMvaa 148,144,196\nMvaa 152,160,192\nRa 144,0,20480\nMvaa 152,164,200\nMvaa 152,168,208\nRa 148,0,21504\n"
"Mvaa 152,172,216\nWL 6\nMvaa 156,172,220\nRa 152,0,22528\nMvaa 156,168,212\nMvaa 156,164,204\nRa 156,0,23552\nWV 2\nMvaa 156,160,196\nAR 22,227\nAR 23,231\nMvaa 160,176,192\n"
"Ra 160,0,24576\nv_max_u32 v21, v22, v23\nAR 22,235\nMvaa 160,180,200\nAR 23,239\nv_max3_u32 v21, v21, v22, v23\nMvaa 160,184,208\nRa 164,0,25600\nAR 22,243\nMvaa 160,188,216\n"
"WL 6\nAR 23,247\nMvaa 164,188,220\nRa 168,0,26624\nv_max3_u32 v21, v21, v22, v23\nMvaa 164,184,212\nAR 22,251\nAR 23,255\nMvaa 164,180,204\nRa 172,0,27648\n"
"v_max3_u32 v21, v21, v22, v23\nMvaa 164,176,196\nv_cmp_eq_u32 vcc, -1, v21\ns_nop 0\ns_cbranch_vccnz .S84\n.S85:\nMvaa 136,128,224\nRa 176,0,28672\nMvaa 136,132,232\n"
"Mvaa 136,136,240\nRa 180,0,29696\nMvaa 136,140,248\nWL 6\nMvaa 140,140,252\nRa 184,0,30720\nMvaa 140,136,244\nMvaa 140,132,236\nRa 188,0,31744\ns_barrier\nMvaa 140,128,228\n"
"Mvaa 144,144,224\nRa 128,2,16384\nMvaa 144,148,232\nMvaa 144,152,240\nRa 132,2,17408\nv_sub_f32 v92, 1.0, v80\nMvaa 144,156,248\nWL 6\nMvaa 148,156,252\nRa 136,2,18432\n"
"Mvaa 148,152,244\nv_sub_f32 v93, 1.0, v81\nCV 92,92,93\nMvaa 148,148,236\nRa 140,2,19456\nMvaa 148,144,228\nv_and_b32 v93, 0xffff0000, v92\nCV 136,136,137\nMvaa 152,160,224\n"
"Ra 144,2,20480\nv_lshlrev_b32 v92, 16, v92\nMvaa 152,164,232\nv_lshlrev_b32 v64, 16, v12\nCV 137,138,139\nMvaa 152,168,240\nRa 148,2,21504\nCV 138,144,145\nMvaa 152,172,248\nWL 6\n"
"CV 139,146,147\nMvaa 156,172,252\nRa 152,2,22528\nv_and_b32 v65, 0xffff0000, v12\nMvaa 156,168,244\nMU 64,64,92\ns_add_u32 s5, s95, 0\nMvaa 156,164,236\nRa 156,2,23552\n"
"Sv 136,1,16,5,0\nMvaa 156,160,228\nMU 65,65,92\nv_lshlrev_b32 v66, 16, v13\nMvaa 160,176,224\nRa 160,2,24576\nCV 140,140,141\nMvaa 160,180,232\nv_and_b32 v67, 0xffff0000, v13\n"
"CV 141,142,143\nMvaa 160,184,240\nRa 164,2,25600\nMU 66,66,92\nMvaa 160,188,248\nWL 6\nCV 142,148,149\nMvaa 164,188,252\nRa 168,2,26624\nCV 143,150,151\nMvaa 164,184,244\n"
"MU 67,67,92\nv_lshlrev_b32 v68, 16, v14\nMvaa 164,180,236\nRa 172,2,27648\nSv 140,1,16,5,1024\nMvaa 164,176,228\nv_and_b32 v69, 0xffff0000, v14\nCV 152,152,153\nMvaa 168,128,192\n"
"Ra 176,2,28672\nMU 68,68,92\nMvaa 168,132,200\nv_bfe_u32 v24, v1, 4, 3\nCV 153,154,155\nMvaa 168,136,208\nRa 180,2,29696\nCV 154,160,161\nMvaa 168,140,216\nWL 6\nCV 155,162,163\n"
"Mvaa 172,140,220\nRa 184,2,30720\nMU 69,69,92\nMvaa 172,136,212\nv_mul_u32_u24 v24, 0xc00, v24\ns_add_u32 s5, s95, 131072\nMvaa 172,132,204\nRa 188,2,31744\nSv 152,1,16,5,0\n"
"Mvaa 172,128,196\nv_bfe_u32 v26, v1, 7, 1\nv_lshlrev_b32 v70, 16, v15\nMvaa 172,144,228\nRa 128,2,0\nCV 156,156,157\nMvaa 172,148,236\nv_add_lshl_u32 v24, v24, v26, 13\n"
"CV 157,158,159\nMvaa 172,152,244\nRa 132,2,1024\nv_and_b32 v71, 0xffff0000, v15\nMvaa 172,156,252\nWL 6\nCV 158,164,165\nMvaa 168,156,248\nRa 136,2,2048\nCV 159,166,167\n"
"Mvaa 168,152,240\nbuffer_load_dword v27, v24, s[16:19], s41 offen sc0 sc1\nMU 70,70,92\nMvaa 168,148,232\nRa 140,2,3072\nSv 156,1,16,5,1024\nMvaa 168,144,224\nMU 71,71,92\n"
"Mvaa 176,160,192\nRa 144,2,4096\nv_lshlrev_b32 v72, 16, v16\nMvaa 176,164,200\nv_and_b32 v73, 0xffff0000, v16\nMvaa 176,168,208\nRa 148,2,5120\nMvaa 176,172,216\nWL 6\n"
"Mvaa 180,172,220\nRa 152,2,6144\nMU 72,72,93\nMvaa 180,168,212\nMU 73,73,93\nMvaa 180,164,204\nRa 156,2,7168\nMvaa 180,160,196\nv_lshlrev_b32 v74, 16, v17\n"
"v_and_b32 v75, 0xffff0000, v17\nMvaa 180,176,228\nRa 160,2,8192\nMvaa 180,180,236\nMU 74,74,93\nMvaa 180,184,244\nRa 164,2,9216\nMU 75,75,93\nMvaa 180,188,252\nWL 6\n"
"Mvaa 176,188,248\nRa 168,2,10240\nMvaa 176,184,240\nv_lshlrev_b32 v76, 16, v18\nv_and_b32 v77, 0xffff0000, v18\nMvaa 176,180,232\nRa 172,2,11264\nMvaa 176,176,224\nMU 76,76,93\n"
"Mvaa 184,128,192\nRa 176,2,12288\nMU 77,77,93\nMvaa 184,132,200\nv_lshlrev_b32 v78, 16, v19\nMvaa 184,136,208\nRa 180,2,13312\nMvaa 184,140,216\nWL 6\nCV 168,168,169\n"
"Mvaa 188,140,220\nRa 184,2,14336\nv_and_b32 v79, 0xffff0000, v19\nMvaa 188,136,212\nMU 78,78,93\nCV 169,170,171\nMvaa 188,132,204\nRa 188,2,15360\ns_barrier\nMvaa 188,128,196\n"
"MU 79,79,93\nCV 170,176,177\nMvaa 188,144,228\nRa 128,2,32768\nCV 171,178,179\nMvaa 188,148,236\ns_add_u32 s5, s95, 262144\nSv 168,1,16,5,0\nMvaa 188,152,244\nRa 132,2,33792\n"
"CV 172,172,173\nMvaa 188,156,252\nWL 6\nCV 173,174,175\nMvaa 184,156,248\nRa 136,2,34816\nCV 174,180,181\nMvaa 184,152,240\nCV 175,182,183\nSv 172,1,16,5,1024\nMvaa 184,148,232\n"
"Ra 140,2,35840\nMvaa 184,144,224\nMvaa 192,160,192\nRa 144,2,36864\nMvaa 192,164,200\nMvaa 192,168,208\nRa 148,2,37888\nMvaa 192,172,216\nWL 6\nMvaa 196,172,220\nRa 152,2,38912\n"
"Mvaa 196,168,212\nMvaa 196,164,204\nRa 156,2,39936\nMvaa 196,160,196\nMvaa 196,176,228\nRa 160,2,40960\nMvaa 196,180,236\nMvaa 196,184,244\nRa 164,2,41984\nMvaa 196,188,252\nWL 6\n"
"Mvaa 192,188,248\nRa 168,2,43008\nMvaa 192,184,240\nMvaa 192,180,232\nRa 172,2,44032\nMvaa 192,176,224\nMvaa 200,128,192\nRa 176,2,45056\nMvaa 200,132,200\nMvaa 200,136,208\n"
"Ra 180,2,46080\nMvaa 200,140,216\nWL 6\nCV 184,184,185\nMvaa 204,140,220\nRa 184,2,47104\nCV 185,186,187\nMvaa 204,136,212\nCV 186,192,193\nCV 187,194,195\nMvaa 204,132,204\n"
"Ra 188,2,48128\ns_add_u32 s5, s95, 393216\nMvaa 204,128,196\nSv 184,1,16,5,0\nCV 188,188,189\nMvaa 204,144,228\nRa 128,2,49152\nCV 189,190,191\nMvaa 204,148,236\nCV 190,196,197\n"
"CV 191,198,199\nMvaa 204,152,244\nRa 132,2,50176\nSv 188,1,16,5,1024\nMvaa 204,156,252\nWL 6\nMvaa 200,156,248\nRa 136,2,51200\nMvaa 200,152,240\nMvaa 200,148,232\nRa 140,2,52224\n"
"Mvaa 200,144,224\nMvaa 208,160,192\nRa 144,2,53248\nWV 5\nv_cmp_eq_u32 s[62:63], s15, v27\ns_mov_b32 vcc_hi, 0\ns_nop 2\ns_lshr_b32 s63, s62, 8\ns_or_b32 s62, s62, s63\n"
"s_andn2_b32 vcc_lo, 0xff, s62\ns_mov_b64 vcc, vcc\ns_cbranch_vccnz .S93\n.S94:\nMvaa 208,164,200\ns_bitcmp1_b32 s63, 0\ns_cselect_b32 s2, 0x2000, 0\nMvaa 208,168,208\n"
"Ra 148,2,54272\nMvaa 208,172,216\nWL 6\nMvaa 212,172,220\nRa 152,2,55296\nMvaa 212,168,212\ns_xor_b32 s2, s2, s20\nMvaa 212,164,204\nRa 156,2,56320\nMvaa 212,160,196\n"
"s_add_u32 s24, s2, 0x0\ns_bitcmp1_b32 s63, 1\ns_cselect_b32 s2, 0x2000, 0\nMvaa 212,176,228\nRa 160,2,57344\nMvaa 212,180,236\ns_xor_b32 s2, s2, s20\nMvaa 212,184,244\n"
"Ra 164,2,58368\nMvaa 212,188,252\nWL 6\nMvaa 208,188,248\nRa 168,2,59392\nMvaa 208,184,240\ns_add_u32 s25, s2, 0x1800000\ns_bitcmp1_b32 s63, 2\ns_cselect_b32 s2, 0x2000, 0\n"
"Mvaa 208,180,232\nRa 172,2,60416\nMvaa 208,176,224\ns_xor_b32 s2, s2, s20\nMvaa 216,128,192\nRa 176,2,61440\nMvaa 216,132,200\ns_add_u32 s26, s2, 0x3000000\ns_bitcmp1_b32 s63, 3\n"
"s_cselect_b32 s2, 0x2000, 0\nMvaa 216,136,208\nRa 180,2,62464\nMvaa 216,140,216\nWL 6\nCV 200,200,201\nMvaa 220,140,220\nRa 184,2,63488\nCV 201,202,203\nMvaa 220,136,212\n"
"s_xor_b32 s2, s2, s20\nCV 202,208,209\nMvaa 220,132,204\nRa 188,2,64512\ns_barrier\nMvaa 220,128,196\ns_add_u32 s27, s2, 0x4800000\ns_bitcmp1_b32 s63, 4\n"
"s_cselect_b32 s2, 0x2000, 0\nMvaa 220,144,228\nRa 128,3,0\nCV 203,210,211\nMvaa 220,148,236\ns_xor_b32 s2, s2, s20\ns_add_u32 s5, s95, 524288\nMvaa 220,152,244\nRa 132,3,1024\n"
"Sv 200,1,16,5,0\nMvaa 220,156,252\nWL 6\nCV 204,204,205\nMvaa 216,156,248\nRa 136,3,2048\nCV 205,206,207\nMvaa 216,152,240\ns_add_u32 s32, s2, 0x6000000\ns_bitcmp1_b32 s63, 5\n"
"s_cselect_b32 s2, 0x2000, 0\nMvaa 216,148,232\nRa 140,3,3072\nCV 206,212,213\nMvaa 216,144,224\ns_xor_b32 s2, s2, s20\nCV 207,214,215\nMvaa 224,160,192\nRa 144,3,4096\n"
"Sv 204,1,16,5,1024\nMvaa 224,164,200\ns_add_u32 s33, s2, 0x7800000\ns_bitcmp1_b32 s63, 6\ns_cselect_b32 s2, 0x2000, 0\nMvaa 224,168,208\nRa 148,3,5120\nMvaa 224,172,216\nWL 6\n"
"Mvaa 228,172,220\nRa 152,3,6144\nMvaa 228,168,212\ns_xor_b32 s2, s2, s20\nMvaa 228,164,204\nRa 156,3,7168\nMvaa 228,160,196\ns_add_u32 s34, s2, 0x9000000\ns_bitcmp1_b32 s63, 7\n"
"s_cselect_b32 s2, 0x2000, 0\nMvaa 228,176,228\nRa 160,3,8192\nMvaa 228,180,236\ns_xor_b32 s2, s2, s20\nMvaa 228,184,244\nRa 164,3,9216\nMvaa 228,188,252\nWL 6\nMvaa 224,188,248\n"
"Ra 168,3,10240\nMvaa 224,184,240\ns_add_u32 s35, s2, 0xa800000\nMvaa 224,180,232\nRa 172,3,11264\nMvaa 224,176,224\nMvaa 232,128,192\nRa 176,3,12288\nMvaa 232,132,200\n"
"Mvaa 232,136,208\nRa 180,3,13312\nMvaa 232,140,216\nWL 6\nCV 216,216,217\nMvaa 236,140,220\nRa 184,3,14336\nCV 217,218,219\nMvaa 236,136,212\nCV 218,224,225\nCV 219,226,227\n"
"Mvaa 236,132,204\nRa 188,3,15360\ns_add_u32 s5, s95, 655360\nMvaa 236,128,196\nSv 216,1,16,5,0\nCV 220,220,221\nMvaa 236,144,228\nCV 221,222,223\nCV 222,228,229\nMvaa 236,148,236\n"
"CV 223,230,231\nSv 220,1,16,5,1024\nMvaa 236,152,244\nMvaa 236,156,252\nWL 4\nMvaa 232,156,248\nMvaa 232,152,240\nMvaa 232,148,232\nMvaa 232,144,224\nMvaa 240,160,192\n"
"Mvaa 240,164,200\nMvaa 240,168,208\nMvaa 240,172,216\nWL 0\nMvaa 244,172,220\nMvaa 244,168,212\nMvaa 244,164,204\nMvaa 244,160,196\nMvaa 244,176,228\nMvaa 244,180,236\n"
"Mvaa 244,184,244\nMvaa 244,188,252\nMvaa 240,188,248\nMvaa 240,184,240\nMvaa 240,180,232\nMvaa 240,176,224\nCV 232,232,233\nCV 233,234,235\ns_nop 5\nCV 234,240,241\n"
"CV 235,242,243\ns_add_u32 s5, s95, 786432\nSv 232,1,16,5,0\nCV 236,236,237\nCV 237,238,239\nCV 238,244,245\nCV 239,246,247\nSv 236,1,16,5,1024\ns_cmp_eq_u32 s13, 0\n"
"s_cbranch_scc1 .Z96\nbuffer_load_dwordx4 v[96:99], v1, s[16:19], s24 offen offset:0 sc0 sc1\nbuffer_load_dwordx4 v[100:103], v1, s[16:19], s24 offen offset:1024 sc0 sc1\n"
"buffer_load_dwordx4 v[104:107], v1, s[16:19], s25 offen offset:0 sc0 sc1\nbuffer_load_dwordx4 v[108:111], v1, s[16:19], s25 offen offset:1024 sc0 sc1\n"
"buffer_load_dwordx4 v[112:115], v1, s[16:19], s26 offen offset:0 sc0 sc1\nbuffer_load_dwordx4 v[116:119], v1, s[16:19], s26 offen offset:1024 sc0 sc1\n"
"buffer_load_dwordx4 v[120:123], v1, s[16:19], s27 offen offset:0 sc0 sc1\nbuffer_load_dwordx4 v[124:127], v1, s[16:19], s27 offen offset:1024 sc0 sc1\n"
"buffer_load_dwordx4 v[32:35], v1, s[16:19], s32 offen offset:0 sc0 sc1\nbuffer_load_dwordx4 v[36:39], v1, s[16:19], s32 offen offset:1024 sc0 sc1\n"
"buffer_load_dwordx4 v[40:43], v1, s[16:19], s33 offen offset:0 sc0 sc1\nbuffer_load_dwordx4 v[44:47], v1, s[16:19], s33 offen offset:1024 sc0 sc1\n"
"buffer_load_dwordx4 v[48:51], v1, s[16:19], s34 offen offset:0 sc0 sc1\nbuffer_load_dwordx4 v[52:55], v1, s[16:19], s34 offen offset:1024 sc0 sc1\n"
"buffer_load_dwordx4 v[56:59], v1, s[16:19], s35 offen offset:0 sc0 sc1\nbuffer_load_dwordx4 v[60:63], v1, s[16:19], s35 offen offset:1024 sc0 sc1\ns_mov_b32 s7, 262144\nWV 0\n"
".Z99:\nv_max_u32 v82, v99, v103\nv_cmp_eq_u32 s[44:45], -1, v82\nv_max_u32 v82, v107, v111\nv_cmp_eq_u32 s[46:47], -1, v82\nv_max_u32 v82, v115, v119\n"
"v_cmp_eq_u32 s[48:49], -1, v82\nv_max_u32 v82, v123, v127\nv_cmp_eq_u32 s[50:51], -1, v82\nv_max_u32 v82, v35, v39\nv_cmp_eq_u32 s[52:53], -1, v82\nv_max_u32 v82, v43, v47\n"
"v_cmp_eq_u32 s[54:55], -1, v82\nv_max_u32 v82, v51, v55\nv_cmp_eq_u32 s[56:57], -1, v82\nv_max_u32 v82, v59, v63\nv_cmp_eq_u32 s[58:59], -1, v82\n"
"s_or_b64 s[0:1], s[44:45], s[46:47]\ns_or_b64 s[2:3], s[48:49], s[50:51]\ns_or_b64 s[0:1], s[0:1], s[52:53]\ns_or_b64 s[2:3], s[2:3], s[54:55]\ns_or_b64 s[0:1], s[0:1], s[56:57]\n"
"s_or_b64 s[2:3], s[2:3], s[58:59]\ns_or_b64 s[0:1], s[0:1], s[2:3]\ns_cbranch_scc1 .Z97\n.Z98:\nZvvv 136,130,96\nMvvv 136,130,104\nMvvv 136,130,112\nMvvv 136,130,120\n"
"Mvvv 136,130,32\nMvvv 136,130,40\nMvvv 136,130,48\nMvvv 136,130,56\nZvvv 140,128,96\nMvvv 140,128,104\nMvvv 140,128,112\nMvvv 140,128,120\nMvvv 140,128,32\nMvvv 140,128,40\n"
"Mvvv 140,128,48\nMvvv 140,128,56\nZvvv 144,130,100\nMvvv 144,130,108\nMvvv 144,130,116\nMvvv 144,130,124\nMvvv 144,130,36\nMvvv 144,130,44\nMvvv 144,130,52\nMvvv 144,130,60\n"
"Zvvv 148,128,100\nMvvv 148,128,108\nMvvv 148,128,116\nMvvv 148,128,124\nMvvv 148,128,36\nMvvv 148,128,44\nMvvv 148,128,52\nMvvv 148,128,60\nFM 64,80,136\nFM 65,80,137\n"
"FM 66,80,138\nFM 67,80,139\nFM 68,80,140\nFM 69,80,141\nFM 70,80,142\nFM 71,80,143\nCV 12,64,65\nCV 13,66,67\nCV 14,68,69\nCV 15,70,71\nSv 12,91,36,22,0\nFM 72,81,144\n"
"FM 73,81,145\nFM 74,81,146\nFM 75,81,147\nFM 76,81,148\nFM 77,81,149\nFM 78,81,150\nFM 79,81,151\nCV 16,72,73\nCV 17,74,75\nCV 18,76,77\nCV 19,78,79\nSv 16,91,36,23,0\n.Z96:\n"
"s_endpgm\n.HD13:\nbuffer_store_dword v25, off, s[16:19], s54 offset:4 sc0 sc1\nv_cmp_ne_u32 vcc, s15, v27\ns_nop 0\ns_cbranch_vccnz .S108\n.S109:\nv_and_b32 v27, 0x70, v1\n"
"buffer_load_dword v27, v27, s[16:19], s61 offen offset:8 sc0 sc1\ns_mov_b32 s90, 1\ns_swappc_b64 s[8:9], s[80:81]\nv_cmp_ne_u32 vcc, s15, v27\ns_nop 0\ns_cbranch_vccnz .S111\n"
".S112:\nv_and_b32 v27, 0x1f0, v1\nbuffer_load_dword v27, v27, s[16:19], s58 offen offset:4 sc0 sc1\ns_mov_b32 s90, 1\ns_swappc_b64 s[8:9], s[80:81]\nv_cmp_ne_u32 vcc, s15, v27\n"
"s_nop 0\ns_cbranch_vccnz .S114\n.S115:\ns_mov_b32 s90, s91\ns_swappc_b64 s[8:9], s[80:81]\nv_lshrrev_b32 v144, 9, v1\nv_mul_u32_u24 v144, 0x18000, v144\n"
"v_lshlrev_b32 v144, 8, v144\nv_and_b32 v145, 0x1f0, v1\nv_add_u32 v144, v144, v145\ns_mov_b32 s6, 0x15c0000\nbuffer_load_dword v136, v144, s[16:19], s6 offen offset:4 sc0 sc1\n"
"s_mov_b32 s6, 0x15c2000\nbuffer_load_dword v137, v144, s[16:19], s6 offen offset:4 sc0 sc1\ns_mov_b32 s6, 0x45c0000\n"
"buffer_load_dword v138, v144, s[16:19], s6 offen offset:4 sc0 sc1\ns_mov_b32 s6, 0x45c2000\nbuffer_load_dword v139, v144, s[16:19], s6 offen offset:4 sc0 sc1\n"
"s_mov_b32 s6, 0x75c0000\nbuffer_load_dword v140, v144, s[16:19], s6 offen offset:4 sc0 sc1\ns_mov_b32 s6, 0x75c2000\n"
"buffer_load_dword v141, v144, s[16:19], s6 offen offset:4 sc0 sc1\ns_mov_b32 s6, 0xa5c0000\nbuffer_load_dword v142, v144, s[16:19], s6 offen offset:4 sc0 sc1\n"
"s_mov_b32 s6, 0xa5c2000\nbuffer_load_dword v143, v144, s[16:19], s6 offen offset:4 sc0 sc1\ns_mov_b32 s90, 0\ns_swappc_b64 s[8:9], s[80:81]\nv_xor_b32 v136, s15, v136\n"
"v_xor_b32 v137, s15, v137\nv_xor_b32 v138, s15, v138\nv_xor_b32 v139, s15, v139\nv_xor_b32 v140, s15, v140\nv_xor_b32 v141, s15, v141\nv_xor_b32 v142, s15, v142\n"
"v_xor_b32 v143, s15, v143\nv_min_u32 v136, v136, v137\nv_min_u32 v138, v138, v139\nv_min_u32 v140, v140, v141\nv_min_u32 v142, v142, v143\nv_or3_b32 v136, v136, v138, v140\n"
"v_or_b32 v136, v136, v142\nv_cmp_ne_u32 vcc, 0, v136\ns_nop 0\ns_cbranch_vccnz .S117\n.S118:\ns_and_b32 s4, s13, 3\ns_lshl2_add_u32 s4, s12, s4\ns_lshr_b32 s5, s71, 16\n"
"s_cmp_lt_u32 s4, 28\ns_cselect_b32 s5, s71, s5\ns_cselect_b32 s3, 0, 28\ns_and_b32 s5, s5, 0x7ff\ns_sub_u32 s3, s4, s3\ns_mul_i32 s2, s4, 7168\ns_add_u32 s2, s2, 0xc04c800\n"
"s_add_u32 s6, s2, 4096\nbuffer_load_dwordx4 v[136:139], v1, s[16:19], s2 offen offset:0 sc0 sc1\nbuffer_load_dwordx4 v[140:143], v1, s[16:19], s2 offen offset:1024 sc0 sc1\n"
"buffer_load_dwordx4 v[144:147], v1, s[16:19], s2 offen offset:2048 sc0 sc1\nbuffer_load_dwordx4 v[148:151], v1, s[16:19], s2 offen offset:3072 sc0 sc1\n"
"buffer_load_dwordx4 v[152:155], v1, s[16:19], s6 offen offset:0 sc0 sc1\nbuffer_load_dwordx4 v[156:159], v1, s[16:19], s6 offen offset:1024 sc0 sc1\n"
"buffer_load_dwordx4 v[160:163], v1, s[16:19], s6 offen offset:2048 sc0 sc1\nv_lshrrev_b32 v167, 1, v1\nv_and_b32 v167, 24, v167\ns_mul_i32 s2, s5, 896\ns_lshl_b32 s3, s3, 5\n"
"s_add_u32 s2, s2, s3\ns_lshl2_add_u32 s5, s5, 0xc042000\nbuffer_load_dwordx2 v[164:165], v167, s[28:31], s2 offen\nbuffer_load_dword v166, off, s[16:19], s5 sc0 sc1\n"
"s_mov_b32 s90, 0\ns_swappc_b64 s[8:9], s[80:81]\nAD 136,136,140\nAD 137,137,141\nAD 138,138,142\nAD 139,139,143\nAD 144,144,148\nAD 145,145,149\nAD 146,146,150\nAD 147,147,151\n"
"AD 152,152,156\nAD 153,153,157\nAD 154,154,158\nAD 155,155,159\nAD 136,136,144\nAD 137,137,145\nAD 138,138,146\nAD 139,139,147\nAD 152,152,160\nAD 153,153,161\nAD 154,154,162\n"
"AD 155,155,163\nAD 136,136,152\nAD 137,137,153\nAD 138,138,154\nAD 139,139,155\nv_mov_b32 v140, v136\nv_mov_b32 v141, v137\nv_mov_b32 v142, v138\nv_mov_b32 v143, v139\n"
"v_permlane32_swap_b32 v136, v140\nv_permlane32_swap_b32 v137, v141\nv_permlane32_swap_b32 v138, v142\nv_permlane32_swap_b32 v139, v143\nAD 136,136,140\nAD 137,137,141\n"
"AD 138,138,142\nAD 139,139,143\nv_mov_b32 v140, v136\nv_mov_b32 v141, v137\nv_mov_b32 v142, v138\nv_mov_b32 v143, v139\nv_permlane16_swap_b32 v136, v140\n"
"v_permlane16_swap_b32 v137, v141\nv_permlane16_swap_b32 v138, v142\nv_permlane16_swap_b32 v139, v143\nAD 136,136,140\nAD 137,137,141\nAD 138,138,142\nAD 139,139,143\n"
"v_add_f32_dpp v136, v136, v136 row_ror:8 row_mask:0xf bank_mask:0xf\nv_add_f32_dpp v137, v137, v137 row_ror:8 row_mask:0xf bank_mask:0xf\n"
"v_add_f32_dpp v138, v138, v138 row_ror:8 row_mask:0xf bank_mask:0xf\nv_add_f32_dpp v139, v139, v139 row_ror:8 row_mask:0xf bank_mask:0xf\n"
"v_add_f32_dpp v136, v136, v136 row_ror:4 row_mask:0xf bank_mask:0xf\nv_add_f32_dpp v137, v137, v137 row_ror:4 row_mask:0xf bank_mask:0xf\n"
"v_add_f32_dpp v138, v138, v138 row_ror:4 row_mask:0xf bank_mask:0xf\nv_add_f32_dpp v139, v139, v139 row_ror:4 row_mask:0xf bank_mask:0xf\nv_mul_f32 v166, 0xbfb8aa3b, v166\n"
"EX 166,166\nCV 136,136,137\nv_add_f32 v166, 1.0, v166\nv_rcp_f32 v166, v166\nCV 138,138,139\nCV 166,166,166\nv_lshlrev_b32 v166, 16, v166\nv_and_b32 v137, 0xffff0000, v136\n"
"v_lshlrev_b32 v136, 16, v136\nv_and_b32 v139, 0xffff0000, v138\nv_lshlrev_b32 v138, 16, v138\nMU 136,136,166\nMU 137,137,166\nMU 138,138,166\nMU 139,139,166\nCV 136,136,137\n"
"CV 138,138,139\nv_and_b32 v137, 0xffff0000, v136\nv_lshlrev_b32 v136, 16, v136\nv_and_b32 v139, 0xffff0000, v138\nv_lshlrev_b32 v138, 16, v138\nv_lshlrev_b32 v140, 16, v164\n"
"v_and_b32 v141, 0xffff0000, v164\nv_lshlrev_b32 v142, 16, v165\nv_and_b32 v143, 0xffff0000, v165\nAD 136,136,140\nAD 137,137,141\nAD 138,138,142\nAD 139,139,143\nCV 136,136,137\n"
"CV 137,138,139\nv_mov_b32 v138, s15\nv_mov_b32 v139, s71\ns_and_b32 s4, s13, 3\ns_lshl2_add_u32 s4, s12, s4\ns_lshr_b32 s5, s71, 16\ns_cmp_lt_u32 s4, 28\n"
"s_cselect_b32 s5, s71, s5\ns_cselect_b32 s3, 0, 28\ns_and_b32 s5, s5, 0x7ff\ns_sub_u32 s3, s4, s3\nv_mov_b32 v140, 0x40000000\nv_cmp_gt_u32 vcc, 64, v1\ns_lshl_b32 s4, s4, 6\n"
"s_add_u32 s4, s4, 0xc04b800\nv_cndmask_b32 v140, v140, v1, vcc\nSv 136,140,16,4,0\ns_mov_b32 s90, 100\ns_swappc_b64 s[8:9], s[80:81]\n.S14:\ns_mov_b32 s7, 262144\n.S16:\n"
"buffer_load_dwordx4 v[32:35], v1, s[16:19], s6 offen offset:0 sc0 sc1\nbuffer_load_dwordx4 v[36:39], v1, s[16:19], s6 offen offset:1024 sc0 sc1\n"
"buffer_load_dwordx4 v[40:43], v1, s[16:19], s6 offen offset:2048 sc0 sc1\nbuffer_load_dwordx4 v[44:47], v1, s[16:19], s6 offen offset:3072 sc0 sc1\nWV 0\nv_xor_b32 v48, s15, v33\n"
"v_xor_b32 v49, s15, v35\nv_xor_b32 v50, s15, v37\nv_xor_b32 v51, s15, v39\nv_xor_b32 v52, s15, v41\nv_xor_b32 v53, s15, v43\nv_xor_b32 v54, s15, v45\nv_xor_b32 v55, s15, v47\n"
"v_or3_b32 v48, v48, v49, v50\nv_or3_b32 v51, v51, v52, v53\nv_or3_b32 v48, v48, v54, v55\nv_or_b32 v48, v48, v51\nv_cmp_ne_u32 vcc, 0, v48\ns_nop 0\ns_cbranch_vccz .S15\n"
"s_sleep 6\ns_sub_u32 s7, s7, 1\ns_cbranch_scc0 .S16\ns_branch .S15\n.S17:\ns_mov_b32 s7, 262144\n.S19:\ns_lshl_b32 s5, s73, 8\ns_add_u32 s5, s5, 0xc04ac00\n"
"v_lshrrev_b32 v27, 2, v1\nbuffer_load_dword v27, v27, s[16:19], s5 offen sc0 sc1\nWV 0\nv_xor_b32 v27, s15, v27\nv_and_b32 v255, 0xffffdfff, v27\nv_cmp_ne_u32 vcc, 0, v255\n"
"s_nop 0\ns_cbranch_vccz .S18\ns_sleep 6\ns_sub_u32 s7, s7, 1\ns_cbranch_scc0 .S19\ns_branch .S18\n.S22:\ns_mov_b32 s7, 262144\n.S24:\nv_and_b32 v27, 0x70, v1\n"
"buffer_load_dword v27, v27, s[16:19], s61 offen offset:128 sc0 sc1\nWV 0\nv_cmp_ne_u32 vcc, s15, v27\ns_nop 0\ns_cbranch_vccz .S23\ns_sleep 6\ns_sub_u32 s7, s7, 1\n"
"s_cbranch_scc0 .S24\ns_branch .S23\n.T26:\ns_mov_b32 s62, 1\ns_call_b64 s[80:81], .RA10\ns_branch .T27\n.T28:\ns_mov_b32 s62, 1\ns_call_b64 s[80:81], .RA10\ns_branch .T29\n.T31:\n"
"s_mov_b32 s62, 0\ns_call_b64 s[80:81], .RB11\ns_branch .T32\n.T33:\ns_mov_b32 s62, 0\ns_call_b64 s[80:81], .RA10\ns_branch .T34\n.T36:\ns_mov_b32 s62, 0\n"
"s_call_b64 s[80:81], .RB11\ns_branch .T37\n.T38:\ns_mov_b32 s62, 0\ns_call_b64 s[80:81], .RA10\ns_branch .T39\n.S40:\ns_mov_b32 s7, 262144\n.S42:\nv_and_b32 v27, 0x70, v1\n"
"buffer_load_dword v27, v27, s[16:19], s61 offen offset:140 sc0 sc1\nWV 0\nv_cmp_ne_u32 vcc, s15, v27\ns_nop 0\ns_cbranch_vccz .S41\ns_sleep 6\ns_sub_u32 s7, s7, 1\n"
"s_cbranch_scc0 .S42\ns_branch .S41\n.S43:\ns_mov_b32 s7, 262144\n.S45:\nv_and_b32 v27, 0x70, v1\nbuffer_load_dword v27, v27, s[16:19], s61 offen offset:136 sc0 sc1\nWV 0\n"
"v_cmp_ne_u32 vcc, s15, v27\ns_nop 0\ns_cbranch_vccz .S44\ns_sleep 6\ns_sub_u32 s7, s7, 1\ns_cbranch_scc0 .S45\ns_branch .S44\n.E48:\ns_swappc_b64 s[80:81], s[8:9]\ns_branch .E49\n"
".T50:\ns_mov_b32 s62, 0\ns_call_b64 s[80:81], .RB11\ns_branch .T51\n.T52:\ns_mov_b32 s62, 0\ns_call_b64 s[80:81], .RA10\ns_branch .T53\n.E55:\nv_cmpx_gt_i32 vcc, 19, v3\n"
"v_mov_b32 v15, s97\nv_cmpx_gt_i32 vcc, 18, v3\nv_mov_b32 v14, s97\nv_cmpx_gt_i32 vcc, 17, v3\nv_mov_b32 v13, s97\nv_cmpx_gt_i32 vcc, 16, v3\nv_mov_b32 v12, s97\n"
"v_cmpx_gt_i32 vcc, 3, v3\nv_mov_b32 v7, s97\nv_cmpx_gt_i32 vcc, 2, v3\nv_mov_b32 v6, s97\nv_cmpx_gt_i32 vcc, 1, v3\nv_mov_b32 v5, s97\nv_cmpx_gt_i32 vcc, 0, v3\nv_mov_b32 v4, s97\n"
"s_mov_b64 exec, -1\ns_nop 2\ns_branch .E56\n.T57:\ns_mov_b32 s62, 0\ns_call_b64 s[80:81], .RB11\ns_branch .T58\n.T59:\ns_mov_b32 s62, 0\ns_call_b64 s[80:81], .RA10\ns_branch .T60\n"
".E62:\nv_cmpx_gt_i32 vcc, 19, v0\nv_mov_b32 v15, s97\nv_cmpx_gt_i32 vcc, 18, v0\nv_mov_b32 v14, s97\nv_cmpx_gt_i32 vcc, 17, v0\nv_mov_b32 v13, s97\nv_cmpx_gt_i32 vcc, 16, v0\n"
"v_mov_b32 v12, s97\nv_cmpx_gt_i32 vcc, 3, v0\nv_mov_b32 v7, s97\nv_cmpx_gt_i32 vcc, 2, v0\nv_mov_b32 v6, s97\nv_cmpx_gt_i32 vcc, 1, v0\nv_mov_b32 v5, s97\n"
"v_cmpx_gt_i32 vcc, 0, v0\nv_mov_b32 v4, s97\ns_mov_b64 exec, -1\ns_nop 2\ns_branch .E63\n.T64:\ns_mov_b32 s62, 0\ns_call_b64 s[80:81], .RB11\ns_branch .T65\n.T66:\n"
"s_mov_b32 s62, 0\ns_call_b64 s[80:81], .RA10\ns_branch .T67\n.E69:\nbuffer_load_dwordx4 v[100:103], v108, s[16:19], s65 offen offset:0 sc0 sc1\n"
"buffer_load_dwordx4 v[104:107], v108, s[16:19], s65 offen offset:256 sc0 sc1\ns_branch .E70\n.S75:\ns_mov_b32 s7, 262144\n.S77:\n"
"buffer_load_dwordx4 v[100:103], v108, s[16:19], s65 offen offset:0 sc0 sc1\nbuffer_load_dwordx4 v[104:107], v108, s[16:19], s65 offen offset:256 sc0 sc1\nWV 0\n"
"v_xor_b32 v109, s15, v102\nv_xor_b32 v110, s15, v106\nv_or_b32 v109, v109, v110\nv_cmp_ne_u32 vcc, 0, v109\ns_nop 0\ns_cbranch_vccz .S76\ns_sleep 6\ns_sub_u32 s7, s7, 1\n"
"s_cbranch_scc0 .S77\ns_branch .S76\n.D71:\nWV 0\nv_xor_b32 v109, s15, v102\nv_xor_b32 v110, s15, v106\nv_or_b32 v109, v109, v110\nv_cmp_ne_u32 vcc, 0, v109\ns_nop 0\n"
"s_cbranch_vccnz .S75\n.S76:\ns_add_u32 s5, s66, 0\nbuffer_load_dwordx4 a[192:195], v1, s[16:19], s5 offen offset:0 sc1\n"
"buffer_load_dwordx4 a[196:199], v1, s[16:19], s5 offen offset:1024 sc1\nbuffer_load_dwordx4 a[200:203], v1, s[16:19], s5 offen offset:2048 sc1\n"
"buffer_load_dwordx4 a[204:207], v1, s[16:19], s5 offen offset:3072 sc1\ns_add_u32 s5, s66, 4096\nbuffer_load_dwordx4 a[208:211], v1, s[16:19], s5 offen offset:0 sc1\n"
"buffer_load_dwordx4 a[212:215], v1, s[16:19], s5 offen offset:1024 sc1\nbuffer_load_dwordx4 a[216:219], v1, s[16:19], s5 offen offset:2048 sc1\n"
"buffer_load_dwordx4 a[220:223], v1, s[16:19], s5 offen offset:3072 sc1\ns_add_u32 s5, s66, 16384\nbuffer_load_dwordx4 a[224:227], v1, s[16:19], s5 offen offset:0 sc1\n"
"buffer_load_dwordx4 a[228:231], v1, s[16:19], s5 offen offset:1024 sc1\nbuffer_load_dwordx4 a[232:235], v1, s[16:19], s5 offen offset:2048 sc1\n"
"buffer_load_dwordx4 a[236:239], v1, s[16:19], s5 offen offset:3072 sc1\ns_add_u32 s5, s66, 20480\nbuffer_load_dwordx4 a[240:243], v1, s[16:19], s5 offen offset:0 sc1\n"
"buffer_load_dwordx4 a[244:247], v1, s[16:19], s5 offen offset:1024 sc1\nbuffer_load_dwordx4 a[248:251], v1, s[16:19], s5 offen offset:2048 sc1\n"
"buffer_load_dwordx4 a[252:255], v1, s[16:19], s5 offen offset:3072 sc1\nWV 0\ns_mov_b32 s67, 0\ns_branch .D72\n.D73:\nv_mov_b32 v130, 0\nv_mov_b32 v131, 0\n"
"s_mov_b32 s63, 0xff800000\ns_mov_b32 s67, 1\ns_branch .D74\n.T78:\ns_lshl3_add_u32 s62, s67, 2\ns_call_b64 s[80:81], .RB11\ns_cmp_eq_u32 s67, 0\ns_cbranch_scc1 .T79\nSU 20,100,88\n"
"v_min_f32 v20, 0, v20\nEX 20,20\ns_nop 0\nFM 86,101,20\nSU 21,104,89\nv_min_f32 v21, 0, v21\nEX 21,21\ns_nop 0\nFM 87,105,21\nAR 22,192\nv_and_b32 v23, 0xffff0000, v22\n"
"v_lshlrev_b32 v22, 16, v22\nMU 22,22,20\nMU 23,23,20\nCV 22,22,23\nAW 192,22\nAR 22,193\nv_and_b32 v23, 0xffff0000, v22\nv_lshlrev_b32 v22, 16, v22\nMU 22,22,20\nMU 23,23,20\n"
"CV 22,22,23\nAW 193,22\nAR 22,194\nv_and_b32 v23, 0xffff0000, v22\nv_lshlrev_b32 v22, 16, v22\nMU 22,22,20\nMU 23,23,20\nCV 22,22,23\nAW 194,22\nAR 22,195\n"
"v_and_b32 v23, 0xffff0000, v22\nv_lshlrev_b32 v22, 16, v22\nMU 22,22,20\nMU 23,23,20\nCV 22,22,23\nAW 195,22\nAR 22,196\nv_and_b32 v23, 0xffff0000, v22\n"
"v_lshlrev_b32 v22, 16, v22\nMU 22,22,21\nMU 23,23,21\nCV 22,22,23\nAW 196,22\nAR 22,197\nv_and_b32 v23, 0xffff0000, v22\nv_lshlrev_b32 v22, 16, v22\nMU 22,22,21\nMU 23,23,21\n"
"CV 22,22,23\nAW 197,22\nAR 22,198\nv_and_b32 v23, 0xffff0000, v22\nv_lshlrev_b32 v22, 16, v22\nMU 22,22,21\nMU 23,23,21\nCV 22,22,23\nAW 198,22\nAR 22,199\n"
"v_and_b32 v23, 0xffff0000, v22\nv_lshlrev_b32 v22, 16, v22\nMU 22,22,21\nMU 23,23,21\nCV 22,22,23\nAW 199,22\nAR 22,200\nv_and_b32 v23, 0xffff0000, v22\n"
"v_lshlrev_b32 v22, 16, v22\nMU 22,22,20\nMU 23,23,20\nCV 22,22,23\nAW 200,22\nAR 22,201\nv_and_b32 v23, 0xffff0000, v22\nv_lshlrev_b32 v22, 16, v22\nMU 22,22,20\nMU 23,23,20\n"
"CV 22,22,23\nAW 201,22\nAR 22,202\nv_and_b32 v23, 0xffff0000, v22\nv_lshlrev_b32 v22, 16, v22\nMU 22,22,20\nMU 23,23,20\nCV 22,22,23\nAW 202,22\nAR 22,203\n"
"v_and_b32 v23, 0xffff0000, v22\nv_lshlrev_b32 v22, 16, v22\nMU 22,22,20\nMU 23,23,20\nCV 22,22,23\nAW 203,22\nAR 22,204\nv_and_b32 v23, 0xffff0000, v22\n"
"v_lshlrev_b32 v22, 16, v22\nMU 22,22,21\nMU 23,23,21\nCV 22,22,23\nAW 204,22\nAR 22,205\nv_and_b32 v23, 0xffff0000, v22\nv_lshlrev_b32 v22, 16, v22\nMU 22,22,21\nMU 23,23,21\n"
"CV 22,22,23\nAW 205,22\nAR 22,206\nv_and_b32 v23, 0xffff0000, v22\nv_lshlrev_b32 v22, 16, v22\nMU 22,22,21\nMU 23,23,21\nCV 22,22,23\nAW 206,22\nAR 22,207\n"
"v_and_b32 v23, 0xffff0000, v22\nv_lshlrev_b32 v22, 16, v22\nMU 22,22,21\nMU 23,23,21\nCV 22,22,23\nAW 207,22\nAR 22,208\nv_and_b32 v23, 0xffff0000, v22\n"
"v_lshlrev_b32 v22, 16, v22\nMU 22,22,20\nMU 23,23,20\nCV 22,22,23\nAW 208,22\nAR 22,209\nv_and_b32 v23, 0xffff0000, v22\nv_lshlrev_b32 v22, 16, v22\nMU 22,22,20\nMU 23,23,20\n"
"CV 22,22,23\nAW 209,22\nAR 22,210\nv_and_b32 v23, 0xffff0000, v22\nv_lshlrev_b32 v22, 16, v22\nMU 22,22,20\nMU 23,23,20\nCV 22,22,23\nAW 210,22\nAR 22,211\n"
"v_and_b32 v23, 0xffff0000, v22\nv_lshlrev_b32 v22, 16, v22\nMU 22,22,20\nMU 23,23,20\nCV 22,22,23\nAW 211,22\nAR 22,212\nv_and_b32 v23, 0xffff0000, v22\n"
"v_lshlrev_b32 v22, 16, v22\nMU 22,22,21\nMU 23,23,21\nCV 22,22,23\nAW 212,22\nAR 22,213\nv_and_b32 v23, 0xffff0000, v22\nv_lshlrev_b32 v22, 16, v22\nMU 22,22,21\nMU 23,23,21\n"
"CV 22,22,23\nAW 213,22\nAR 22,214\nv_and_b32 v23, 0xffff0000, v22\nv_lshlrev_b32 v22, 16, v22\nMU 22,22,21\nMU 23,23,21\nCV 22,22,23\nAW 214,22\nAR 22,215\n"
"v_and_b32 v23, 0xffff0000, v22\nv_lshlrev_b32 v22, 16, v22\nMU 22,22,21\nMU 23,23,21\nCV 22,22,23\nAW 215,22\nAR 22,216\nv_and_b32 v23, 0xffff0000, v22\n"
"v_lshlrev_b32 v22, 16, v22\nMU 22,22,20\nMU 23,23,20\nCV 22,22,23\nAW 216,22\nAR 22,217\nv_and_b32 v23, 0xffff0000, v22\nv_lshlrev_b32 v22, 16, v22\nMU 22,22,20\nMU 23,23,20\n"
"CV 22,22,23\nAW 217,22\nAR 22,218\nv_and_b32 v23, 0xffff0000, v22\nv_lshlrev_b32 v22, 16, v22\nMU 22,22,20\nMU 23,23,20\nCV 22,22,23\nAW 218,22\nAR 22,219\n"
"v_and_b32 v23, 0xffff0000, v22\nv_lshlrev_b32 v22, 16, v22\nMU 22,22,20\nMU 23,23,20\nCV 22,22,23\nAW 219,22\nAR 22,220\nv_and_b32 v23, 0xffff0000, v22\n"
"v_lshlrev_b32 v22, 16, v22\nMU 22,22,21\nMU 23,23,21\nCV 22,22,23\nAW 220,22\nAR 22,221\nv_and_b32 v23, 0xffff0000, v22\nv_lshlrev_b32 v22, 16, v22\nMU 22,22,21\nMU 23,23,21\n"
"CV 22,22,23\nAW 221,22\nAR 22,222\nv_and_b32 v23, 0xffff0000, v22\nv_lshlrev_b32 v22, 16, v22\nMU 22,22,21\nMU 23,23,21\nCV 22,22,23\nAW 222,22\nAR 22,223\n"
"v_and_b32 v23, 0xffff0000, v22\nv_lshlrev_b32 v22, 16, v22\nMU 22,22,21\nMU 23,23,21\nCV 22,22,23\nAW 223,22\nAR 22,224\nv_and_b32 v23, 0xffff0000, v22\n"
"v_lshlrev_b32 v22, 16, v22\nMU 22,22,20\nMU 23,23,20\nCV 22,22,23\nAW 224,22\nAR 22,225\nv_and_b32 v23, 0xffff0000, v22\nv_lshlrev_b32 v22, 16, v22\nMU 22,22,20\nMU 23,23,20\n"
"CV 22,22,23\nAW 225,22\nAR 22,226\nv_and_b32 v23, 0xffff0000, v22\nv_lshlrev_b32 v22, 16, v22\nMU 22,22,20\nMU 23,23,20\nCV 22,22,23\nAW 226,22\nAR 22,227\n"
"v_and_b32 v23, 0xffff0000, v22\nv_lshlrev_b32 v22, 16, v22\nMU 22,22,20\nMU 23,23,20\nCV 22,22,23\nAW 227,22\nAR 22,228\nv_and_b32 v23, 0xffff0000, v22\n"
"v_lshlrev_b32 v22, 16, v22\nMU 22,22,21\nMU 23,23,21\nCV 22,22,23\nAW 228,22\nAR 22,229\nv_and_b32 v23, 0xffff0000, v22\nv_lshlrev_b32 v22, 16, v22\nMU 22,22,21\nMU 23,23,21\n"
"CV 22,22,23\nAW 229,22\nAR 22,230\nv_and_b32 v23, 0xffff0000, v22\nv_lshlrev_b32 v22, 16, v22\nMU 22,22,21\nMU 23,23,21\nCV 22,22,23\nAW 230,22\nAR 22,231\n"
"v_and_b32 v23, 0xffff0000, v22\nv_lshlrev_b32 v22, 16, v22\nMU 22,22,21\nMU 23,23,21\nCV 22,22,23\nAW 231,22\nAR 22,232\nv_and_b32 v23, 0xffff0000, v22\n"
"v_lshlrev_b32 v22, 16, v22\nMU 22,22,20\nMU 23,23,20\nCV 22,22,23\nAW 232,22\nAR 22,233\nv_and_b32 v23, 0xffff0000, v22\nv_lshlrev_b32 v22, 16, v22\nMU 22,22,20\nMU 23,23,20\n"
"CV 22,22,23\nAW 233,22\nAR 22,234\nv_and_b32 v23, 0xffff0000, v22\nv_lshlrev_b32 v22, 16, v22\nMU 22,22,20\nMU 23,23,20\nCV 22,22,23\nAW 234,22\nAR 22,235\n"
"v_and_b32 v23, 0xffff0000, v22\nv_lshlrev_b32 v22, 16, v22\nMU 22,22,20\nMU 23,23,20\nCV 22,22,23\nAW 235,22\nAR 22,236\nv_and_b32 v23, 0xffff0000, v22\n"
"v_lshlrev_b32 v22, 16, v22\nMU 22,22,21\nMU 23,23,21\nCV 22,22,23\nAW 236,22\nAR 22,237\nv_and_b32 v23, 0xffff0000, v22\nv_lshlrev_b32 v22, 16, v22\nMU 22,22,21\nMU 23,23,21\n"
"CV 22,22,23\nAW 237,22\nAR 22,238\nv_and_b32 v23, 0xffff0000, v22\nv_lshlrev_b32 v22, 16, v22\nMU 22,22,21\nMU 23,23,21\nCV 22,22,23\nAW 238,22\nAR 22,239\n"
"v_and_b32 v23, 0xffff0000, v22\nv_lshlrev_b32 v22, 16, v22\nMU 22,22,21\nMU 23,23,21\nCV 22,22,23\nAW 239,22\nAR 22,240\nv_and_b32 v23, 0xffff0000, v22\n"
"v_lshlrev_b32 v22, 16, v22\nMU 22,22,20\nMU 23,23,20\nCV 22,22,23\nAW 240,22\nAR 22,241\nv_and_b32 v23, 0xffff0000, v22\nv_lshlrev_b32 v22, 16, v22\nMU 22,22,20\nMU 23,23,20\n"
"CV 22,22,23\nAW 241,22\nAR 22,242\nv_and_b32 v23, 0xffff0000, v22\nv_lshlrev_b32 v22, 16, v22\nMU 22,22,20\nMU 23,23,20\nCV 22,22,23\nAW 242,22\nAR 22,243\n"
"v_and_b32 v23, 0xffff0000, v22\nv_lshlrev_b32 v22, 16, v22\nMU 22,22,20\nMU 23,23,20\nCV 22,22,23\nAW 243,22\nAR 22,244\nv_and_b32 v23, 0xffff0000, v22\n"
"v_lshlrev_b32 v22, 16, v22\nMU 22,22,21\nMU 23,23,21\nCV 22,22,23\nAW 244,22\nAR 22,245\nv_and_b32 v23, 0xffff0000, v22\nv_lshlrev_b32 v22, 16, v22\nMU 22,22,21\nMU 23,23,21\n"
"CV 22,22,23\nAW 245,22\nAR 22,246\nv_and_b32 v23, 0xffff0000, v22\nv_lshlrev_b32 v22, 16, v22\nMU 22,22,21\nMU 23,23,21\nCV 22,22,23\nAW 246,22\nAR 22,247\n"
"v_and_b32 v23, 0xffff0000, v22\nv_lshlrev_b32 v22, 16, v22\nMU 22,22,21\nMU 23,23,21\nCV 22,22,23\nAW 247,22\nAR 22,248\nv_and_b32 v23, 0xffff0000, v22\n"
"v_lshlrev_b32 v22, 16, v22\nMU 22,22,20\nMU 23,23,20\nCV 22,22,23\nAW 248,22\nAR 22,249\nv_and_b32 v23, 0xffff0000, v22\nv_lshlrev_b32 v22, 16, v22\nMU 22,22,20\nMU 23,23,20\n"
"CV 22,22,23\nAW 249,22\nAR 22,250\nv_and_b32 v23, 0xffff0000, v22\nv_lshlrev_b32 v22, 16, v22\nMU 22,22,20\nMU 23,23,20\nCV 22,22,23\nAW 250,22\nAR 22,251\n"
"v_and_b32 v23, 0xffff0000, v22\nv_lshlrev_b32 v22, 16, v22\nMU 22,22,20\nMU 23,23,20\nCV 22,22,23\nAW 251,22\nAR 22,252\nv_and_b32 v23, 0xffff0000, v22\n"
"v_lshlrev_b32 v22, 16, v22\nMU 22,22,21\nMU 23,23,21\nCV 22,22,23\nAW 252,22\nAR 22,253\nv_and_b32 v23, 0xffff0000, v22\nv_lshlrev_b32 v22, 16, v22\nMU 22,22,21\nMU 23,23,21\n"
"CV 22,22,23\nAW 253,22\nAR 22,254\nv_and_b32 v23, 0xffff0000, v22\nv_lshlrev_b32 v22, 16, v22\nMU 22,22,21\nMU 23,23,21\nCV 22,22,23\nAW 254,22\nAR 22,255\n"
"v_and_b32 v23, 0xffff0000, v22\nv_lshlrev_b32 v22, 16, v22\nMU 22,22,21\nMU 23,23,21\nCV 22,22,23\nAW 255,22\nv_lshrrev_b32 v22, 8, v1\nv_bfe_u32 v23, v1, 6, 2\n"
"v_cmp_eq_u32 vcc, v22, v23\nv_and_b32 v22, 16, v1\nv_mov_b32 v23, 0x3f80\nv_cndmask_b32 v23, 0, v23, vcc\nv_lshlrev_b32 v23, v22, v23\nv_and_b32 v22, 32, v1\n"
"v_cmp_eq_u32 vcc, 0, v22\nv_mov_b32 v128, 0\nv_mov_b32 v129, 0\nv_cndmask_b32 v130, 0, v23, vcc\nv_sub_u32 v131, v23, v130\nv_mov_b32 v132, 0\nv_mov_b32 v133, 0\ns_nop 1\n"
"Mava 0,130,192\nMava 8,128,192\nMava 12,128,196\nMava 4,130,196\nMava 16,130,200\nMava 24,128,200\nMava 28,128,204\nMava 20,130,204\nMava 32,130,208\nMava 40,128,208\n"
"Mava 44,128,212\nMava 36,130,212\nMava 48,130,216\nMava 56,128,216\nMava 60,128,220\nMava 52,130,220\nMava 64,130,224\nMava 72,128,224\nMava 76,128,228\nMava 68,130,228\n"
"Mava 80,130,232\nMava 88,128,232\nMava 92,128,236\nMava 84,130,236\nMava 96,130,240\nMava 104,128,240\nMava 108,128,244\nMava 100,130,244\nMava 112,130,248\nMava 120,128,248\n"
"Mava 124,128,252\nMava 116,130,252\ns_nop 7\ns_branch .T79\n.S81:\ns_mov_b32 s7, 262144\n.S83:\ns_add_u32 s4, s92, 0\n"
"buffer_load_dwordx4 a[192:195], v1, s[16:19], s4 offen offset:0 sc0 sc1\nbuffer_load_dwordx4 a[196:199], v1, s[16:19], s4 offen offset:1024 sc0 sc1\ns_add_u32 s4, s92, 131072\n"
"buffer_load_dwordx4 a[200:203], v1, s[16:19], s4 offen offset:0 sc0 sc1\nbuffer_load_dwordx4 a[204:207], v1, s[16:19], s4 offen offset:1024 sc0 sc1\ns_add_u32 s4, s92, 262144\n"
"buffer_load_dwordx4 a[208:211], v1, s[16:19], s4 offen offset:0 sc0 sc1\nbuffer_load_dwordx4 a[212:215], v1, s[16:19], s4 offen offset:1024 sc0 sc1\ns_add_u32 s4, s92, 393216\n"
"buffer_load_dwordx4 a[216:219], v1, s[16:19], s4 offen offset:0 sc0 sc1\nbuffer_load_dwordx4 a[220:223], v1, s[16:19], s4 offen offset:1024 sc0 sc1\nWV 0\nAR 22,195\nAR 23,199\n"
"v_max_u32 v21, v22, v23\nAR 22,203\nAR 23,207\nv_max3_u32 v21, v21, v22, v23\nAR 22,211\nAR 23,215\nv_max3_u32 v21, v21, v22, v23\nAR 22,219\nAR 23,223\n"
"v_max3_u32 v21, v21, v22, v23\nv_cmp_eq_u32 vcc, -1, v21\ns_nop 0\ns_cbranch_vccz .S82\ns_sleep 6\ns_sub_u32 s7, s7, 1\ns_cbranch_scc0 .S83\ns_branch .S82\n.S84:\n"
"s_mov_b32 s7, 262144\n.S86:\ns_add_u32 s4, s92, 524288\nbuffer_load_dwordx4 a[224:227], v1, s[16:19], s4 offen offset:0 sc0 sc1\n"
"buffer_load_dwordx4 a[228:231], v1, s[16:19], s4 offen offset:1024 sc0 sc1\ns_add_u32 s4, s92, 655360\nbuffer_load_dwordx4 a[232:235], v1, s[16:19], s4 offen offset:0 sc0 sc1\n"
"buffer_load_dwordx4 a[236:239], v1, s[16:19], s4 offen offset:1024 sc0 sc1\ns_add_u32 s4, s92, 786432\nbuffer_load_dwordx4 a[240:243], v1, s[16:19], s4 offen offset:0 sc0 sc1\n"
"buffer_load_dwordx4 a[244:247], v1, s[16:19], s4 offen offset:1024 sc0 sc1\ns_add_u32 s4, s92, 917504\nbuffer_load_dwordx4 a[248:251], v1, s[16:19], s4 offen offset:0 sc0 sc1\n"
"buffer_load_dwordx4 a[252:255], v1, s[16:19], s4 offen offset:1024 sc0 sc1\nWV 0\nAR 22,227\nAR 23,231\nv_max_u32 v21, v22, v23\nAR 22,235\nAR 23,239\n"
"v_max3_u32 v21, v21, v22, v23\nAR 22,243\nAR 23,247\nv_max3_u32 v21, v21, v22, v23\nAR 22,251\nAR 23,255\nv_max3_u32 v21, v21, v22, v23\nv_cmp_eq_u32 vcc, -1, v21\ns_nop 0\n"
"s_cbranch_vccz .S85\ns_sleep 6\ns_sub_u32 s7, s7, 1\ns_cbranch_scc0 .S86\ns_branch .S85\n.S87:\ns_mov_b32 s7, 262144\n.S89:\n"
"buffer_load_dwordx4 v[64:67], v89, s[16:19], s40 offen offset:0 sc0 sc1\nbuffer_load_dwordx4 v[68:71], v89, s[16:19], s40 offen offset:16 sc0 sc1\n"
"buffer_load_dwordx4 v[72:75], v89, s[16:19], s40 offen offset:1792 sc0 sc1\nbuffer_load_dwordx4 v[76:79], v89, s[16:19], s40 offen offset:1808 sc0 sc1\nWV 0\n"
"v_xor_b32 v92, s15, v66\nv_xor_b32 v93, s15, v70\nv_xor_b32 v94, s15, v74\nv_or3_b32 v92, v92, v93, v94\nv_xor_b32 v93, s15, v78\nv_or_b32 v92, v92, v93\nv_cmp_ne_u32 vcc, 0, v92\n"
"s_nop 0\ns_cbranch_vccz .S88\ns_sleep 6\ns_sub_u32 s7, s7, 1\ns_cbranch_scc0 .S89\ns_branch .S88\n.S90:\ns_mov_b32 s7, 262144\n.S92:\n"
"buffer_load_dword v27, off, s[16:19], s60 offset:512 sc0 sc1\nWV 0\nv_cmp_ne_u32 vcc, s15, v27\ns_nop 0\ns_cbranch_vccz .S91\ns_sleep 6\ns_sub_u32 s7, s7, 1\ns_cbranch_scc0 .S92\n"
"s_branch .S91\n.S93:\ns_mov_b32 s7, 262144\n.S95:\nbuffer_load_dword v27, v24, s[16:19], s41 offen sc0 sc1\nWV 0\nv_cmp_eq_u32 s[62:63], s15, v27\ns_mov_b32 vcc_hi, 0\ns_nop 2\n"
"s_lshr_b32 s63, s62, 8\ns_or_b32 s62, s62, s63\ns_andn2_b32 vcc_lo, 0xff, s62\ns_mov_b64 vcc, vcc\ns_cbranch_vccz .S94\ns_sleep 6\ns_sub_u32 s7, s7, 1\ns_cbranch_scc0 .S95\n"
"s_branch .S94\n.Z97:\ns_sub_u32 s7, s7, 1\ns_cbranch_scc1 .Z98\ns_cmp_lg_u64 s[44:45], 0\ns_cbranch_scc0 .Z100\n"
"buffer_load_dwordx4 v[96:99], v1, s[16:19], s24 offen offset:0 sc0 sc1\nbuffer_load_dwordx4 v[100:103], v1, s[16:19], s24 offen offset:1024 sc0 sc1\n.Z100:\n"
"s_cmp_lg_u64 s[46:47], 0\ns_cbranch_scc0 .Z101\nbuffer_load_dwordx4 v[104:107], v1, s[16:19], s25 offen offset:0 sc0 sc1\n"
"buffer_load_dwordx4 v[108:111], v1, s[16:19], s25 offen offset:1024 sc0 sc1\n.Z101:\ns_cmp_lg_u64 s[48:49], 0\ns_cbranch_scc0 .Z102\n"
"buffer_load_dwordx4 v[112:115], v1, s[16:19], s26 offen offset:0 sc0 sc1\nbuffer_load_dwordx4 v[116:119], v1, s[16:19], s26 offen offset:1024 sc0 sc1\n.Z102:\n"
"s_cmp_lg_u64 s[50:51], 0\ns_cbranch_scc0 .Z103\nbuffer_load_dwordx4 v[120:123], v1, s[16:19], s27 offen offset:0 sc0 sc1\n"
"buffer_load_dwordx4 v[124:127], v1, s[16:19], s27 offen offset:1024 sc0 sc1\n.Z103:\ns_cmp_lg_u64 s[52:53], 0\ns_cbranch_scc0 .Z104\n"
"buffer_load_dwordx4 v[32:35], v1, s[16:19], s32 offen offset:0 sc0 sc1\nbuffer_load_dwordx4 v[36:39], v1, s[16:19], s32 offen offset:1024 sc0 sc1\n.Z104:\n"
"s_cmp_lg_u64 s[54:55], 0\ns_cbranch_scc0 .Z105\nbuffer_load_dwordx4 v[40:43], v1, s[16:19], s33 offen offset:0 sc0 sc1\n"
"buffer_load_dwordx4 v[44:47], v1, s[16:19], s33 offen offset:1024 sc0 sc1\n.Z105:\ns_cmp_lg_u64 s[56:57], 0\ns_cbranch_scc0 .Z106\n"
"buffer_load_dwordx4 v[48:51], v1, s[16:19], s34 offen offset:0 sc0 sc1\nbuffer_load_dwordx4 v[52:55], v1, s[16:19], s34 offen offset:1024 sc0 sc1\n.Z106:\n"
"s_cmp_lg_u64 s[58:59], 0\ns_cbranch_scc0 .Z107\nbuffer_load_dwordx4 v[56:59], v1, s[16:19], s35 offen offset:0 sc0 sc1\n"
"buffer_load_dwordx4 v[60:63], v1, s[16:19], s35 offen offset:1024 sc0 sc1\n.Z107:\ns_sleep 8\nWV 0\ns_branch .Z99\n.S108:\ns_mov_b32 s7, 262144\n.S110:\nv_and_b32 v27, 0x70, v1\n"
"buffer_load_dword v27, v27, s[16:19], s61 offen offset:12 sc0 sc1\nWV 0\nv_cmp_ne_u32 vcc, s15, v27\ns_nop 0\ns_cbranch_vccz .S109\ns_sleep 6\ns_sub_u32 s7, s7, 1\n"
"s_cbranch_scc0 .S110\ns_branch .S109\n.S111:\ns_mov_b32 s7, 262144\n.S113:\nv_and_b32 v27, 0x70, v1\nbuffer_load_dword v27, v27, s[16:19], s61 offen offset:8 sc0 sc1\nWV 0\n"
"v_cmp_ne_u32 vcc, s15, v27\ns_nop 0\ns_cbranch_vccz .S112\ns_sleep 6\ns_sub_u32 s7, s7, 1\ns_cbranch_scc0 .S113\ns_branch .S112\n.S114:\ns_mov_b32 s7, 262144\n.S116:\n"
"v_and_b32 v27, 0x1f0, v1\nbuffer_load_dword v27, v27, s[16:19], s58 offen offset:4 sc0 sc1\nWV 0\nv_cmp_ne_u32 vcc, s15, v27\ns_nop 0\ns_cbranch_vccz .S115\ns_sleep 6\n"
"s_sub_u32 s7, s7, 1\ns_cbranch_scc0 .S116\ns_branch .S115\n.S117:\ns_mov_b32 s7, 262144\n.S119:\nv_lshrrev_b32 v144, 9, v1\nv_mul_u32_u24 v144, 0x18000, v144\n"
"v_lshlrev_b32 v144, 8, v144\nv_and_b32 v145, 0x1f0, v1\nv_add_u32 v144, v144, v145\ns_mov_b32 s6, 0x15c0000\nbuffer_load_dword v136, v144, s[16:19], s6 offen offset:4 sc0 sc1\n"
"s_mov_b32 s6, 0x15c2000\nbuffer_load_dword v137, v144, s[16:19], s6 offen offset:4 sc0 sc1\ns_mov_b32 s6, 0x45c0000\n"
"buffer_load_dword v138, v144, s[16:19], s6 offen offset:4 sc0 sc1\ns_mov_b32 s6, 0x45c2000\nbuffer_load_dword v139, v144, s[16:19], s6 offen offset:4 sc0 sc1\n"
"s_mov_b32 s6, 0x75c0000\nbuffer_load_dword v140, v144, s[16:19], s6 offen offset:4 sc0 sc1\ns_mov_b32 s6, 0x75c2000\n"
"buffer_load_dword v141, v144, s[16:19], s6 offen offset:4 sc0 sc1\ns_mov_b32 s6, 0xa5c0000\nbuffer_load_dword v142, v144, s[16:19], s6 offen offset:4 sc0 sc1\n"
"s_mov_b32 s6, 0xa5c2000\nbuffer_load_dword v143, v144, s[16:19], s6 offen offset:4 sc0 sc1\nWV 0\nv_xor_b32 v136, s15, v136\nv_xor_b32 v137, s15, v137\nv_xor_b32 v138, s15, v138\n"
"v_xor_b32 v139, s15, v139\nv_xor_b32 v140, s15, v140\nv_xor_b32 v141, s15, v141\nv_xor_b32 v142, s15, v142\nv_xor_b32 v143, s15, v143\nv_min_u32 v136, v136, v137\n"
"v_min_u32 v138, v138, v139\nv_min_u32 v140, v140, v141\nv_min_u32 v142, v142, v143\nv_or3_b32 v136, v136, v138, v140\nv_or_b32 v136, v136, v142\nv_cmp_ne_u32 vcc, 0, v136\n"
"s_nop 0\ns_cbranch_vccz .S118\ns_sleep 6\ns_sub_u32 s7, s7, 1\ns_cbranch_scc0 .S119\ns_branch .S118\n.RA10:\ns_nop 7\ns_nop 3\nv_max3_f32 v22, v32, v33, v34\n"
"v_max3_f32 v22, v22, v35, v40\nv_max3_f32 v22, v22, v41, v42\nv_max_f32 v22, v22, v43\nv_mov_b32 v91, v22\ns_nop 1\nv_permlane32_swap_b32 v22, v91\nv_max_f32 v22, v22, v91\n"
"v_mov_b32 v91, v22\ns_nop 1\nv_permlane16_swap_b32 v22, v91\nv_max_f32 v22, v22, v91\nv_ceil_f32 v22, v22\nv_max3_f32 v23, v36, v37, v38\nv_max3_f32 v23, v23, v39, v44\n"
"v_max3_f32 v23, v23, v45, v46\nv_max_f32 v23, v23, v47\nv_mov_b32 v91, v23\ns_nop 1\nv_permlane32_swap_b32 v23, v91\nv_max_f32 v23, v23, v91\nv_mov_b32 v91, v23\ns_nop 1\n"
"v_permlane16_swap_b32 v23, v91\nv_max_f32 v23, v23, v91\nv_ceil_f32 v23, v23\ns_bitcmp1_b32 s62, 0\ns_cbranch_scc0 .R120\nv_cmp_lt_f32 vcc, 0xff000000, v22\ns_nop 1\n"
"v_cndmask_b32 v22, 0, v22, vcc\nv_cmp_lt_f32 vcc, 0xff000000, v23\ns_nop 1\nv_cndmask_b32 v23, 0, v23, vcc\ns_branch .R121\n.R120:\nv_max_f32 v22, 0, v22\nv_max_f32 v23, 0, v23\n"
"s_bitcmp1_b32 s62, 3\ns_cbranch_scc0 .R121\nv_max_f32 v22, v20, v22\nv_max_f32 v23, v21, v23\n.R121:\nAD 88,88,22\nAD 89,89,23\nSU 32,32,22\nSU 33,33,22\nSU 34,34,22\nSU 35,35,22\n"
"SU 36,36,23\nSU 37,37,23\nSU 38,38,23\nSU 39,39,23\nSU 40,40,22\nSU 41,41,22\nSU 42,42,22\nSU 43,43,22\nSU 44,44,23\nSU 45,45,23\nSU 46,46,23\nSU 47,47,23\nv_mov_b32 v90, v80\n"
"v_mov_b32 v91, v81\nEX 80,32\nEX 81,33\ns_nop 0\nAD 84,80,81\nCV 64,80,81\nEX 82,34\nEX 83,35\nAD 84,84,82\nAD 84,84,83\nCV 65,82,83\nEX 80,36\nEX 81,37\ns_nop 0\nAD 85,80,81\n"
"CV 68,80,81\nEX 82,38\nEX 83,39\nAD 85,85,82\nAD 85,85,83\nCV 69,82,83\nEX 80,40\nEX 81,41\nAD 84,84,80\nAD 84,84,81\nCV 66,80,81\nEX 82,42\nEX 83,43\nAD 84,84,82\nAD 84,84,83\n"
"CV 67,82,83\nEX 80,44\nEX 81,45\nAD 85,85,80\nAD 85,85,81\nCV 70,80,81\nEX 82,46\nEX 83,47\nAD 85,85,82\nAD 85,85,83\nCV 71,82,83\nv_mov_b32 v80, v90\nv_mov_b32 v81, v91\n"
"s_bitcmp1_b32 s62, 1\ns_cbranch_scc1 .R122\nSU 48,48,22\nSU 49,49,22\nSU 50,50,22\nSU 51,51,22\nSU 52,52,23\nSU 53,53,23\nSU 54,54,23\nSU 55,55,23\nSU 56,56,22\nSU 57,57,22\n"
"SU 58,58,22\nSU 59,59,22\nSU 60,60,23\nSU 61,61,23\nSU 62,62,23\nSU 63,63,23\nSU 4,4,22\nSU 5,5,22\nSU 6,6,22\nSU 7,7,22\nSU 8,8,23\nSU 9,9,23\nSU 10,10,23\nSU 11,11,23\n"
"SU 12,12,22\nSU 13,13,22\nSU 14,14,22\nSU 15,15,22\nSU 16,16,23\nSU 17,17,23\nSU 18,18,23\nSU 19,19,23\nEX 80,48\nEX 81,49\n.R122:\ns_bitcmp1_b32 s62, 0\ns_cbranch_scc0 .RS12\n"
"s_nop 1\ns_setpc_b64 s[80:81]\n.RB11:\ns_nop 7\ns_nop 3\nv_max3_f32 v22, v48, v49, v50\nv_max3_f32 v22, v22, v51, v56\nv_max3_f32 v22, v22, v57, v58\nv_max_f32 v22, v22, v59\n"
"v_mov_b32 v91, v22\ns_nop 1\nv_permlane32_swap_b32 v22, v91\nv_max_f32 v22, v22, v91\nv_mov_b32 v91, v22\ns_nop 1\nv_permlane16_swap_b32 v22, v91\nv_max_f32 v22, v22, v91\n"
"v_ceil_f32 v22, v22\nv_max3_f32 v23, v52, v53, v54\nv_max3_f32 v23, v23, v55, v60\nv_max3_f32 v23, v23, v61, v62\nv_max_f32 v23, v23, v63\nv_mov_b32 v91, v23\ns_nop 1\n"
"v_permlane32_swap_b32 v23, v91\nv_max_f32 v23, v23, v91\nv_mov_b32 v91, v23\ns_nop 1\nv_permlane16_swap_b32 v23, v91\nv_max_f32 v23, v23, v91\nv_ceil_f32 v23, v23\n"
"s_bitcmp1_b32 s62, 0\ns_cbranch_scc0 .R125\nv_cmp_lt_f32 vcc, 0xff000000, v22\nv_cndmask_b32 v22, 0, v22, vcc\nv_cmp_lt_f32 vcc, 0xff000000, v23\nv_cndmask_b32 v23, 0, v23, vcc\n"
"s_branch .R126\n.R125:\nv_max_f32 v22, 0, v22\nv_max_f32 v23, 0, v23\ns_bitcmp1_b32 s62, 3\ns_cbranch_scc0 .R126\nv_max_f32 v22, v20, v22\nv_max_f32 v23, v21, v23\n.R126:\n"
"AD 88,88,22\nAD 89,89,23\nSU 48,48,22\nSU 49,49,22\nSU 50,50,22\nSU 51,51,22\nSU 52,52,23\nSU 53,53,23\nSU 54,54,23\nSU 55,55,23\nSU 56,56,22\nSU 57,57,22\nSU 58,58,22\n"
"SU 59,59,22\nSU 60,60,23\nSU 61,61,23\nSU 62,62,23\nSU 63,63,23\nv_mov_b32 v90, v80\nv_mov_b32 v91, v81\nEX 80,48\nEX 81,49\ns_nop 0\nAD 84,80,81\nCV 72,80,81\nEX 82,50\nEX 83,51\n"
"AD 84,84,82\nAD 84,84,83\nCV 73,82,83\nEX 80,52\nEX 81,53\ns_nop 0\nAD 85,80,81\nCV 76,80,81\nEX 82,54\nEX 83,55\nAD 85,85,82\nAD 85,85,83\nCV 77,82,83\nEX 80,56\nEX 81,57\n"
"AD 84,84,80\nAD 84,84,81\nCV 74,80,81\nEX 82,58\nEX 83,59\nAD 84,84,82\nAD 84,84,83\nCV 75,82,83\nEX 80,60\nEX 81,61\nAD 85,85,80\nAD 85,85,81\nCV 78,80,81\nEX 82,62\nEX 83,63\n"
"AD 85,85,82\nAD 85,85,83\nCV 79,82,83\nv_mov_b32 v80, v90\nv_mov_b32 v81, v91\ns_bitcmp1_b32 s62, 1\ns_cbranch_scc1 .R127\nSU 32,32,22\nSU 33,33,22\nSU 34,34,22\nSU 35,35,22\n"
"SU 36,36,23\nSU 37,37,23\nSU 38,38,23\nSU 39,39,23\nSU 40,40,22\nSU 41,41,22\nSU 42,42,22\nSU 43,43,22\nSU 44,44,23\nSU 45,45,23\nSU 46,46,23\nSU 47,47,23\nSU 4,4,22\nSU 5,5,22\n"
"SU 6,6,22\nSU 7,7,22\nSU 8,8,23\nSU 9,9,23\nSU 10,10,23\nSU 11,11,23\nSU 12,12,22\nSU 13,13,22\nSU 14,14,22\nSU 15,15,22\nSU 16,16,23\nSU 17,17,23\nSU 18,18,23\nSU 19,19,23\n"
"EX 80,32\nEX 81,33\n.R127:\ns_bitcmp1_b32 s62, 0\ns_cbranch_scc0 .RS12\ns_nop 1\ns_setpc_b64 s[80:81]\n.RS12:\nv_exp_f32 v22, -v22\nv_exp_f32 v23, -v23\nMU 86,86,22\nMU 87,87,23\n"
"AR 91,0\nMU 91,91,22\nAW 0,91\nAR 91,1\nMU 91,91,22\nAW 1,91\nAR 91,2\nMU 91,91,22\nAW 2,91\nAR 91,3\nMU 91,91,22\nAW 3,91\nAR 91,4\nMU 91,91,23\nAW 4,91\nAR 91,5\nMU 91,91,23\n"
"AW 5,91\nAR 91,6\nMU 91,91,23\nAW 6,91\nAR 91,7\nMU 91,91,23\nAW 7,91\nAR 91,8\nMU 91,91,22\nAW 8,91\nAR 91,9\nMU 91,91,22\nAW 9,91\nAR 91,10\nMU 91,91,22\nAW 10,91\nAR 91,11\n"
"MU 91,91,22\nAW 11,91\nAR 91,12\nMU 91,91,23\nAW 12,91\nAR 91,13\nMU 91,91,23\nAW 13,91\nAR 91,14\nMU 91,91,23\nAW 14,91\nAR 91,15\nMU 91,91,23\nAW 15,91\nAR 91,16\nMU 91,91,22\n"
"AW 16,91\nAR 91,17\nMU 91,91,22\nAW 17,91\nAR 91,18\nMU 91,91,22\nAW 18,91\nAR 91,19\nMU 91,91,22\nAW 19,91\nAR 91,20\nMU 91,91,23\nAW 20,91\nAR 91,21\nMU 91,91,23\nAW 21,91\n"
"AR 91,22\nMU 91,91,23\nAW 22,91\nAR 91,23\nMU 91,91,23\nAW 23,91\nAR 91,24\nMU 91,91,22\nAW 24,91\nAR 91,25\nMU 91,91,22\nAW 25,91\nAR 91,26\nMU 91,91,22\nAW 26,91\nAR 91,27\n"
"MU 91,91,22\nAW 27,91\nAR 91,28\nMU 91,91,23\nAW 28,91\nAR 91,29\nMU 91,91,23\nAW 29,91\nAR 91,30\nMU 91,91,23\nAW 30,91\nAR 91,31\nMU 91,91,23\nAW 31,91\nAR 91,32\nMU 91,91,22\n"
"AW 32,91\nAR 91,33\nMU 91,91,22\nAW 33,91\nAR 91,34\nMU 91,91,22\nAW 34,91\nAR 91,35\nMU 91,91,22\nAW 35,91\nAR 91,36\nMU 91,91,23\nAW 36,91\nAR 91,37\nMU 91,91,23\nAW 37,91\n"
"AR 91,38\nMU 91,91,23\nAW 38,91\nAR 91,39\nMU 91,91,23\nAW 39,91\nAR 91,40\nMU 91,91,22\nAW 40,91\nAR 91,41\nMU 91,91,22\nAW 41,91\nAR 91,42\nMU 91,91,22\nAW 42,91\nAR 91,43\n"
"MU 91,91,22\nAW 43,91\nAR 91,44\nMU 91,91,23\nAW 44,91\nAR 91,45\nMU 91,91,23\nAW 45,91\nAR 91,46\nMU 91,91,23\nAW 46,91\nAR 91,47\nMU 91,91,23\nAW 47,91\nAR 91,48\nMU 91,91,22\n"
"AW 48,91\nAR 91,49\nMU 91,91,22\nAW 49,91\nAR 91,50\nMU 91,91,22\nAW 50,91\nAR 91,51\nMU 91,91,22\nAW 51,91\nAR 91,52\nMU 91,91,23\nAW 52,91\nAR 91,53\nMU 91,91,23\nAW 53,91\n"
"AR 91,54\nMU 91,91,23\nAW 54,91\nAR 91,55\nMU 91,91,23\nAW 55,91\nAR 91,56\nMU 91,91,22\nAW 56,91\nAR 91,57\nMU 91,91,22\nAW 57,91\nAR 91,58\nMU 91,91,22\nAW 58,91\nAR 91,59\n"
"MU 91,91,22\nAW 59,91\nAR 91,60\nMU 91,91,23\nAW 60,91\nAR 91,61\nMU 91,91,23\nAW 61,91\nAR 91,62\nMU 91,91,23\nAW 62,91\nAR 91,63\nMU 91,91,23\nAW 63,91\nAR 91,64\nMU 91,91,22\n"
"AW 64,91\nAR 91,65\nMU 91,91,22\nAW 65,91\nAR 91,66\nMU 91,91,22\nAW 66,91\nAR 91,67\nMU 91,91,22\nAW 67,91\nAR 91,68\nMU 91,91,23\nAW 68,91\nAR 91,69\nMU 91,91,23\nAW 69,91\n"
"AR 91,70\nMU 91,91,23\nAW 70,91\nAR 91,71\nMU 91,91,23\nAW 71,91\nAR 91,72\nMU 91,91,22\nAW 72,91\nAR 91,73\nMU 91,91,22\nAW 73,91\nAR 91,74\nMU 91,91,22\nAW 74,91\nAR 91,75\n"
"MU 91,91,22\nAW 75,91\nAR 91,76\nMU 91,91,23\nAW 76,91\nAR 91,77\nMU 91,91,23\nAW 77,91\nAR 91,78\nMU 91,91,23\nAW 78,91\nAR 91,79\nMU 91,91,23\nAW 79,91\nAR 91,80\nMU 91,91,22\n"
"AW 80,91\nAR 91,81\nMU 91,91,22\nAW 81,91\nAR 91,82\nMU 91,91,22\nAW 82,91\nAR 91,83\nMU 91,91,22\nAW 83,91\nAR 91,84\nMU 91,91,23\nAW 84,91\nAR 91,85\nMU 91,91,23\nAW 85,91\n"
"AR 91,86\nMU 91,91,23\nAW 86,91\nAR 91,87\nMU 91,91,23\nAW 87,91\nAR 91,88\nMU 91,91,22\nAW 88,91\nAR 91,89\nMU 91,91,22\nAW 89,91\nAR 91,90\nMU 91,91,22\nAW 90,91\nAR 91,91\n"
"MU 91,91,22\nAW 91,91\nAR 91,92\nMU 91,91,23\nAW 92,91\nAR 91,93\nMU 91,91,23\nAW 93,91\nAR 91,94\nMU 91,91,23\nAW 94,91\nAR 91,95\nMU 91,91,23\nAW 95,91\nAR 91,96\nMU 91,91,22\n"
"AW 96,91\nAR 91,97\nMU 91,91,22\nAW 97,91\nAR 91,98\nMU 91,91,22\nAW 98,91\nAR 91,99\nMU 91,91,22\nAW 99,91\nAR 91,100\nMU 91,91,23\nAW 100,91\nAR 91,101\nMU 91,91,23\nAW 101,91\n"
"AR 91,102\nMU 91,91,23\nAW 102,91\nAR 91,103\nMU 91,91,23\nAW 103,91\nAR 91,104\nMU 91,91,22\nAW 104,91\nAR 91,105\nMU 91,91,22\nAW 105,91\nAR 91,106\nMU 91,91,22\nAW 106,91\n"
"AR 91,107\nMU 91,91,22\nAW 107,91\nAR 91,108\nMU 91,91,23\nAW 108,91\nAR 91,109\nMU 91,91,23\nAW 109,91\nAR 91,110\nMU 91,91,23\nAW 110,91\nAR 91,111\nMU 91,91,23\nAW 111,91\n"
"AR 91,112\nMU 91,91,22\nAW 112,91\nAR 91,113\nMU 91,91,22\nAW 113,91\nAR 91,114\nMU 91,91,22\nAW 114,91\nAR 91,115\nMU 91,91,22\nAW 115,91\nAR 91,116\nMU 91,91,23\nAW 116,91\n"
"AR 91,117\nMU 91,91,23\nAW 117,91\nAR 91,118\nMU 91,91,23\nAW 118,91\nAR 91,119\nMU 91,91,23\nAW 119,91\nAR 91,120\nMU 91,91,22\nAW 120,91\nAR 91,121\nMU 91,91,22\nAW 121,91\n"
"AR 91,122\nMU 91,91,22\nAW 122,91\nAR 91,123\nMU 91,91,22\nAW 123,91\nAR 91,124\nMU 91,91,23\nAW 124,91\nAR 91,125\nMU 91,91,23\nAW 125,91\nAR 91,126\nMU 91,91,23\nAW 126,91\n"
"AR 91,127\nMU 91,91,23\nAW 127,91\ns_nop 1\ns_setpc_b64 s[80:81]\n.p2align 12\n.PA3:\nv_add_u32 v128, 0x10000, v12\nv_add_u32 v129, 0x10000, v13\nv_add_u32 v130, 0x10000, v14\n"
"v_add_u32 v131, 0x10000, v15\nv_add_u32 v132, 0x10000, v16\nv_add_u32 v133, 0x10000, v17\nv_add_u32 v134, 0x10000, v18\nv_add_u32 v135, 0x10000, v19\ns_add_u32 s2, s11, 1\n"
"s_lshr_b32 s2, s2, 1\ns_lshl_b32 s2, s2, 12\nv_add_u32 v140, s2, v10\nv_add_u32 v141, s2, v11\nv_xor_b32 v142, 0x1000, v140\nv_xor_b32 v143, 0x1000, v141\ns_add_u32 m0, s47, 0\n"
"s_add_u32 s46, s51, 524288\nD0 6,32,46,0\nD0 7,32,46,1024\nD0 8,32,46,2048\nD0 9,32,46,3072\ns_add_u32 m0, s47, -49152\ns_add_u32 s46, s52, 128\nD0 136,28,46,0\nD0 137,28,46,1024\n"
"D0 138,28,46,2048\nD0 139,28,46,3072\ns_add_u32 m0, s47, 16384\ns_add_u32 s46, s51, 786432\nD0 6,32,46,0\nD0 7,32,46,1024\nD0 8,32,46,2048\nD0 9,32,46,3072\n"
"v_mov_b32 v116, 0x40000000\nv_cmp_gt_u32 vcc, 56, v0\ns_lshl2_add_u32 s2, s10, s11\ns_mul_i32 s2, s2, 1792\nv_cndmask_b32 v117, v116, v1, vcc\ns_mov_b32 s36, s56\n"
"s_and_b32 s37, s57, 0xffff\ns_mov_b32 s38, 0x380\ns_mov_b32 s39, 0x20000\ns_mov_b32 s40, s58\ns_and_b32 s41, s59, 0xffff\ns_mov_b32 s42, 0x380\ns_mov_b32 s43, 0x20000\n"
"buffer_load_dwordx4 v[96:99], v117, s[28:31], s2 offen offset:0\nbuffer_load_dwordx4 v[100:103], v117, s[28:31], s2 offen offset:896\n"
"buffer_load_dwordx4 v[104:107], v117, s[36:39], 0 offen offset:0\nbuffer_load_dwordx4 v[108:111], v117, s[40:43], 0 offen offset:0\ns_add_u32 m0, s47, 32768\n"
"s_add_u32 s46, s51, 1048576\nD0 6,32,46,0\nD0 7,32,46,1024\nD0 8,32,46,2048\nD0 9,32,46,3072\ns_add_u32 m0, s47, -32768\ns_add_u32 s46, s52, 256\nD0 136,28,46,0\n"
"D0 137,28,46,1024\nD0 138,28,46,2048\nD0 139,28,46,3072\nWL 0\ns_mul_i32 s15, s15, 1664525\ns_add_u32 s15, s15, 1013904223\nv_mov_b32 v25, s15\ns_mov_b32 s57, 0x3db8aa3b\n"
"s_and_b32 s59, s11, 1\ns_lshl_b32 s59, s59, 13\ns_mul_i32 s2, s73, 25165824\ns_and_b32 s3, s12, 1\ns_lshl_b32 s3, s3, 21\ns_add_u32 s2, s2, s3\ns_lshl2_add_u32 s3, s13, s11\n"
"s_lshl_b32 s3, s3, 15\ns_add_u32 s2, s2, s3\ns_add_u32 s56, s2, 0x800000\ns_xor_b32 s53, s56, 0xc000\nWV 24\ns_barrier\nRa 160,140,0\nRa 164,140,2048\nRa 168,142,0\n"
"Ra 172,142,2048\nTa 128,128,0\nTa 130,128,2048\nTa 132,129,0\nTa 134,129,2048\nTa 136,130,0\nTa 138,130,2048\nTa 140,131,0\nTa 142,131,2048\nTa 144,132,0\nTa 146,132,2048\n"
"Ta 148,133,0\nTa 150,133,2048\nTa 152,134,0\nTa 154,134,2048\nTa 156,135,0\nTa 158,135,2048\nWL 0\nZvaa 64,128,160\nRa 224,141,0\ns_add_u32 m0, s47, 49152\n"
"s_add_u32 s46, s51, 1310720\nZvaa 32,128,164\nRa 228,141,2048\nZvaa 192,128,168\nRa 232,143,0\nD0 6,32,46,0\nZvaa 224,128,172\nRa 236,143,2048\nZvaa 228,132,172\nTa 192,128,16384\n"
"Zvaa 196,132,168\nZvaa 36,132,164\nTa 194,128,18432\nZvaa 68,132,160\nTa 196,129,16384\nD0 7,32,46,1024\nZvaa 72,136,160\nTa 198,129,18432\nZvaa 40,136,164\nTa 200,130,16384\n"
"Zvaa 200,136,168\nTa 202,130,18432\nZvaa 232,136,172\nZvaa 236,140,172\nTa 204,131,16384\nD0 8,32,46,2048\nZvaa 204,140,168\nTa 206,131,18432\nZvaa 44,140,164\nTa 208,132,16384\n"
"Zvaa 76,140,160\nTa 210,132,18432\nZvaa 80,144,160\nTa 212,133,16384\nZvaa 48,144,164\nD0 9,32,46,3072\nZvaa 208,144,168\nTa 214,133,18432\nZvaa 240,144,172\nTa 216,134,16384\n"
"Zvaa 244,148,172\nTa 218,134,18432\ns_add_u32 m0, s47, -16384\ns_add_u32 s46, s52, 384\nZvaa 212,148,168\nTa 220,135,16384\nZvaa 52,148,164\nTa 222,135,18432\nD0 136,28,46,0\n"
"Zvaa 84,148,160\nZvaa 88,152,160\nZvaa 56,152,164\nZvaa 216,152,168\nZvaa 248,152,172\nD0 137,28,46,1024\nZvaa 252,156,172\nZvaa 220,156,168\nZvaa 60,156,164\nZvaa 92,156,160\n"
"s_waitcnt vmcnt(18) lgkmcnt(0)\ns_barrier\nMvaa 64,192,224\nRa 160,140,16384\nD0 138,28,46,2048\nMvaa 32,192,228\nRa 164,140,18432\nMvaa 192,192,232\nRa 168,142,16384\n"
"Mvaa 224,192,236\nRa 172,142,18432\nWV 15\nMvaa 228,196,236\nTa 128,12,0\nMvaa 196,196,232\nD0 139,28,46,3072\nMvaa 36,196,228\nTa 130,12,2048\nv_lshlrev_b32 v112, 16, v96\n"
"Mvaa 68,196,224\nTa 132,13,0\nMvaa 72,200,224\nTa 134,13,2048\ns_add_u32 m0, s47, 65536\ns_add_u32 s46, s51, 1572864\nMvaa 40,200,228\nTa 136,14,0\nMvaa 200,200,232\n"
"Ta 138,14,2048\nD0 6,32,46,0\nMvaa 232,200,236\nv_lshlrev_b32 v113, 16, v100\nv_lshlrev_b32 v114, 16, v104\nMvaa 236,204,236\nTa 140,15,0\nMvaa 204,204,232\nTa 142,15,2048\n"
"Mvaa 44,204,228\nTa 144,16,0\nv_lshlrev_b32 v115, 16, v108\nMvaa 76,204,224\nTa 146,16,2048\nD0 7,32,46,1024\nMvaa 80,208,224\nTa 148,17,0\nMU 116,112,114\nMvaa 48,208,228\n"
"MU 117,113,114\nMvaa 208,208,232\nTa 150,17,2048\nMvaa 240,208,236\nTa 152,18,0\nCV 116,116,117\nMvaa 244,212,236\nTa 154,18,2048\nD0 8,32,46,2048\nMvaa 212,212,232\nTa 156,19,0\n"
"Mvaa 52,212,228\nTa 158,19,2048\nv_lshlrev_b32 v118, 16, v116\nMvaa 84,212,224\nv_and_b32 v119, 0xffff0000, v116\nMvaa 88,216,224\nMU 120,112,115\nMU 121,113,115\nMvaa 56,216,228\n"
"D0 9,32,46,3072\nMvaa 216,216,232\nv_and_b32 v112, 0xffff0000, v96\nMvaa 248,216,236\nv_and_b32 v113, 0xffff0000, v100\nv_and_b32 v114, 0xffff0000, v104\nMvaa 252,220,236\n"
"s_add_u32 m0, s47, 81920\ns_add_u32 s46, s51, 1835008\nMvaa 220,220,232\nv_and_b32 v115, 0xffff0000, v108\nMvaa 60,220,228\nD0 6,32,46,0\nMU 116,112,114\nMvaa 92,220,224\nWL 0\n"
"Mvaa 64,128,160\nRa 224,141,16384\nMU 117,113,114\nMvaa 32,128,164\nRa 228,141,18432\nMvaa 192,128,168\nRa 232,143,16384\nMvaa 224,128,172\nRa 236,143,18432\nD0 7,32,46,1024\n"
"Mvaa 228,132,172\nTa 192,12,16384\nMvaa 196,132,168\nCV 116,116,117\nMvaa 36,132,164\nTa 194,12,18432\nv_and_b32 v117, 0xffff0000, v116\nMvaa 68,132,160\nTa 196,13,16384\n"
"Mvaa 72,136,160\nTa 198,13,18432\nD0 8,32,46,2048\nMvaa 40,136,164\nTa 200,14,16384\nMvaa 200,136,168\nTa 202,14,18432\nMvaa 232,136,172\nv_lshlrev_b32 v116, 16, v116\n"
"FM 120,112,115\nMvaa 236,140,172\nTa 204,15,16384\nMvaa 204,140,168\nTa 206,15,18432\nD0 9,32,46,3072\nMvaa 44,140,164\nTa 208,16,16384\nFM 121,113,115\nMvaa 76,140,160\n"
"Ta 210,16,18432\nMvaa 80,144,160\nTa 212,17,16384\ns_add_u32 m0, s47, -65536\ns_add_u32 s46, s52, 512\nMvaa 48,144,164\nAD 118,118,116\nMvaa 208,144,168\nTa 214,17,18432\n"
"D0 136,28,46,0\nMvaa 240,144,172\nTa 216,18,16384\nAD 119,119,117\nMvaa 244,148,172\nTa 218,18,18432\nMvaa 212,148,168\nTa 220,19,16384\nMvaa 52,148,164\nTa 222,19,18432\n"
"v_lshlrev_b32 v112, 16, v97\nMvaa 84,148,160\nD0 137,28,46,1024\nMvaa 88,152,160\nv_lshlrev_b32 v113, 16, v101\nv_lshlrev_b32 v114, 16, v105\nMvaa 56,152,164\n"
"v_lshlrev_b32 v115, 16, v109\nMvaa 216,152,168\nMU 116,112,114\nMvaa 248,152,172\nMU 117,113,114\nCV 116,116,117\nMvaa 252,156,172\nD0 138,28,46,2048\nMvaa 220,156,168\n"
"v_and_b32 v117, 0xffff0000, v116\nMvaa 60,156,164\nv_lshlrev_b32 v116, 16, v116\nFM 120,112,115\nMvaa 92,156,160\ns_waitcnt vmcnt(15) lgkmcnt(0)\ns_barrier\nMvaa 64,192,224\n"
"Ra 160,140,32768\nFM 121,113,115\nMvaa 32,192,228\nRa 164,140,34816\nD0 139,28,46,3072\nMvaa 192,192,232\nRa 168,142,32768\nMvaa 224,192,236\nRa 172,142,34816\nAD 118,118,116\n"
"Mvaa 228,196,236\nTa 128,12,32768\ns_add_u32 m0, s47, 0\ns_add_u32 s46, s51, 2097152\nMvaa 196,196,232\nAD 119,119,117\nMvaa 36,196,228\nTa 130,12,34816\nD0 6,32,46,0\n"
"Mvaa 68,196,224\nTa 132,13,32768\nMvaa 72,200,224\nTa 134,13,34816\nv_and_b32 v112, 0xffff0000, v97\nMvaa 40,200,228\nTa 136,14,32768\nMvaa 200,200,232\nTa 138,14,34816\n"
"Mvaa 232,200,236\nD0 7,32,46,1024\nv_and_b32 v113, 0xffff0000, v101\nMvaa 236,204,236\nTa 140,15,32768\nMvaa 204,204,232\nTa 142,15,34816\nMvaa 44,204,228\nTa 144,16,32768\n"
"v_and_b32 v114, 0xffff0000, v105\nMvaa 76,204,224\nTa 146,16,34816\nMvaa 80,208,224\nTa 148,17,32768\nD0 8,32,46,2048\nMvaa 48,208,228\nv_and_b32 v115, 0xffff0000, v109\n"
"Mvaa 208,208,232\nTa 150,17,34816\nMvaa 240,208,236\nTa 152,18,32768\nMU 116,112,114\nMvaa 244,212,236\nTa 154,18,34816\nMvaa 212,212,232\nTa 156,19,32768\nD0 9,32,46,3072\n"
"Mvaa 52,212,228\nTa 158,19,34816\nMU 117,113,114\nMvaa 84,212,224\nCV 116,116,117\nMvaa 88,216,224\ns_add_u32 m0, s47, 16384\ns_add_u32 s46, s51, 2359296\nMvaa 56,216,228\n"
"v_and_b32 v117, 0xffff0000, v116\nMvaa 216,216,232\nD0 6,32,46,0\nMvaa 248,216,236\nv_lshlrev_b32 v116, 16, v116\nFM 120,112,115\nMvaa 252,220,236\nFM 121,113,115\n"
"Mvaa 220,220,232\nAD 118,118,116\nMvaa 60,220,228\nAD 119,119,117\nv_lshlrev_b32 v112, 16, v98\nMvaa 92,220,224\nWL 0\nMvaa 64,128,160\nRa 224,141,32768\nD0 7,32,46,1024\n"
"Mvaa 32,128,164\nRa 228,141,34816\nMvaa 192,128,168\nRa 232,143,32768\nMvaa 224,128,172\nRa 236,143,34816\nv_lshlrev_b32 v113, 16, v102\nMvaa 228,132,172\nTa 192,12,49152\n"
"Mvaa 196,132,168\nD0 8,32,46,2048\nMvaa 36,132,164\nTa 194,12,51200\nv_lshlrev_b32 v114, 16, v106\nMvaa 68,132,160\nTa 196,13,49152\nMvaa 72,136,160\nTa 198,13,51200\n"
"v_lshlrev_b32 v115, 16, v110\nMvaa 40,136,164\nTa 200,14,49152\nMvaa 200,136,168\nTa 202,14,51200\nD0 9,32,46,3072\nMvaa 232,136,172\nMU 116,112,114\nMU 117,113,114\n"
"Mvaa 236,140,172\nTa 204,15,49152\nMvaa 204,140,168\nTa 206,15,51200\nMvaa 44,140,164\nTa 208,16,49152\nCV 116,116,117\nMvaa 76,140,160\nTa 210,16,51200\nMvaa 80,144,160\n"
"Ta 212,17,49152\nv_and_b32 v117, 0xffff0000, v116\nMvaa 48,144,164\nv_lshlrev_b32 v116, 16, v116\nMvaa 208,144,168\nTa 214,17,51200\nMvaa 240,144,172\nTa 216,18,49152\n"
"FM 120,112,115\nMvaa 244,148,172\nTa 218,18,51200\nMvaa 212,148,168\nTa 220,19,49152\nMvaa 52,148,164\nTa 222,19,51200\nFM 121,113,115\nMvaa 84,148,160\nAD 118,118,116\n"
"Mvaa 88,152,160\nAD 119,119,117\nv_and_b32 v112, 0xffff0000, v98\nMvaa 56,152,164\nv_and_b32 v113, 0xffff0000, v102\nMvaa 216,152,168\nv_and_b32 v114, 0xffff0000, v106\n"
"Mvaa 248,152,172\nv_and_b32 v115, 0xffff0000, v110\nMU 116,112,114\nMvaa 252,156,172\nMU 117,113,114\nMvaa 220,156,168\nCV 116,116,117\nMvaa 60,156,164\n"
"v_and_b32 v117, 0xffff0000, v116\nv_lshlrev_b32 v116, 16, v116\nMvaa 92,156,160\ns_waitcnt vmcnt(12) lgkmcnt(0)\ns_barrier\nMvaa 64,192,224\nRa 160,140,49152\n"
"s_add_u32 m0, s47, 32768\ns_add_u32 s46, s51, 2621440\nMvaa 32,192,228\nRa 164,140,51200\nMvaa 192,192,232\nRa 168,142,49152\nD0 6,32,46,0\nMvaa 224,192,236\nRa 172,142,51200\n"
"FM 120,112,115\nMvaa 228,196,236\nTa 128,128,0\nMvaa 196,196,232\nFM 121,113,115\nMvaa 36,196,228\nTa 130,128,2048\nAD 118,118,116\nMvaa 68,196,224\nTa 132,129,0\nD0 7,32,46,1024\n"
"Mvaa 72,200,224\nTa 134,129,2048\nAD 119,119,117\nMvaa 40,200,228\nTa 136,130,0\nMvaa 200,200,232\nTa 138,130,2048\nMvaa 232,200,236\nv_lshlrev_b32 v112, 16, v99\n"
"v_lshlrev_b32 v113, 16, v103\nMvaa 236,204,236\nTa 140,131,0\nD0 8,32,46,2048\nMvaa 204,204,232\nTa 142,131,2048\nMvaa 44,204,228\nTa 144,132,0\nv_lshlrev_b32 v114, 16, v107\n"
"Mvaa 76,204,224\nTa 146,132,2048\nMvaa 80,208,224\nTa 148,133,0\nv_lshlrev_b32 v115, 16, v111\nMvaa 48,208,228\nD0 9,32,46,3072\nMvaa 208,208,232\nTa 150,133,2048\n"
"Mvaa 240,208,236\nTa 152,134,0\nMU 116,112,114\nMvaa 244,212,236\nTa 154,134,2048\ns_add_u32 m0, s47, -32768\ns_add_u32 s46, s52, 640\nMvaa 212,212,232\nTa 156,135,0\n"
"Mvaa 52,212,228\nTa 158,135,2048\nD0 136,28,46,0\nMvaa 84,212,224\nMU 117,113,114\nMvaa 88,216,224\nCV 116,116,117\nv_and_b32 v117, 0xffff0000, v116\nMvaa 56,216,228\n"
"v_lshlrev_b32 v116, 16, v116\nMvaa 216,216,232\nFM 120,112,115\nMvaa 248,216,236\nD0 137,28,46,1024\nFM 121,113,115\nMvaa 252,220,236\nAD 118,118,116\nMvaa 220,220,232\n"
"AD 119,119,117\nMvaa 60,220,228\nv_and_b32 v112, 0xffff0000, v99\nv_and_b32 v113, 0xffff0000, v103\nMvaa 92,220,224\nWL 0\nMvaa 64,128,160\nRa 224,141,49152\nD0 138,28,46,2048\n"
"Mvaa 32,128,164\nRa 228,141,51200\nMvaa 192,128,168\nRa 232,143,49152\nMvaa 224,128,172\nRa 236,143,51200\nv_and_b32 v114, 0xffff0000, v107\nMvaa 228,132,172\nTa 192,128,16384\n"
"Mvaa 196,132,168\nD0 139,28,46,3072\nMvaa 36,132,164\nTa 194,128,18432\nv_and_b32 v115, 0xffff0000, v111\nMvaa 68,132,160\nTa 196,129,16384\nMvaa 72,136,160\nTa 198,129,18432\n"
"s_add_u32 m0, s47, 49152\ns_add_u32 s46, s51, 2883584\nMvaa 40,136,164\nTa 200,130,16384\nMvaa 200,136,168\nTa 202,130,18432\nD0 6,32,46,0\nMvaa 232,136,172\nMU 116,112,114\n"
"MU 117,113,114\nMvaa 236,140,172\nTa 204,131,16384\nMvaa 204,140,168\nTa 206,131,18432\nMvaa 44,140,164\nTa 208,132,16384\nCV 116,116,117\nMvaa 76,140,160\nTa 210,132,18432\n"
"D0 7,32,46,1024\nMvaa 80,144,160\nTa 212,133,16384\nv_and_b32 v117, 0xffff0000, v116\nMvaa 48,144,164\nv_lshlrev_b32 v116, 16, v116\nMvaa 208,144,168\nTa 214,133,18432\n"
"Mvaa 240,144,172\nTa 216,134,16384\nFM 120,112,115\nMvaa 244,148,172\nTa 218,134,18432\nD0 8,32,46,2048\nMvaa 212,148,168\nTa 220,135,16384\nMvaa 52,148,164\nTa 222,135,18432\n"
"FM 121,113,115\nMvaa 84,148,160\nAD 118,118,116\nMvaa 88,152,160\nAD 119,119,117\nMvaa 56,152,164\nD0 9,32,46,3072\nMvaa 216,152,168\nv_permlane32_swap_b32 v118, v119\n"
"Mvaa 248,152,172\nv_permlane32_swap_b32 v120, v121\nAD 118,118,119\nMvaa 252,156,172\nAD 120,120,121\ns_and_b32 s2, s74, 3\ns_lshl2_add_u32 s2, s73, s2\ns_lshl_b32 s2, s2, 12\n"
"s_add_u32 s3, s45, 512\ns_and_b32 s3, s3, 0xfffe0000\ns_add_u32 s2, s2, s3\nbuffer_load_dword v31, v26, s[20:23], s2 offen\nMvaa 220,156,168\nMvaa 60,156,164\n"
"v_permlane16_swap_b32 v118, v120\nAD 118,118,120\nMvaa 92,156,160\ns_waitcnt vmcnt(13) lgkmcnt(0)\ns_barrier\nMvaa 64,192,224\nRa 160,140,0\ns_add_u32 m0, s47, 65536\n"
"s_add_u32 s46, s51, 3145728\nMvaa 32,192,228\nRa 164,140,2048\nMvaa 192,192,232\nRa 168,142,0\nD0 6,32,46,0\nMvaa 224,192,236\nRa 172,142,2048\n"
"v_add_f32_dpp v118, v118, v118 row_ror:8 row_mask:0xf bank_mask:0xf\nMvaa 228,196,236\nTa 128,12,0\nMvaa 196,196,232\n"
"v_add_f32_dpp v118, v118, v118 row_ror:4 row_mask:0xf bank_mask:0xf\nMvaa 36,196,228\nTa 130,12,2048\nMvaa 68,196,224\nTa 132,13,0\nD0 7,32,46,1024\nMvaa 72,200,224\n"
"Ta 134,13,2048\nv_add_f32_dpp v118, v118, v118 row_ror:2 row_mask:0xf bank_mask:0xf\nMvaa 40,200,228\nTa 136,14,0\nMvaa 200,200,232\nTa 138,14,2048\nMvaa 232,200,236\n"
"v_add_f32_dpp v118, v118, v118 row_ror:1 row_mask:0xf bank_mask:0xf\nCV 118,118,118\nMvaa 236,204,236\nTa 140,15,0\nD0 8,32,46,2048\nMvaa 204,204,232\nTa 142,15,2048\n"
"Mvaa 44,204,228\nTa 144,16,0\nv_lshlrev_b32 v118, 16, v118\nMvaa 76,204,224\nTa 146,16,2048\nMvaa 80,208,224\nTa 148,17,0\nv_mul_f32 v119, 0xbfb8aa3b, v118\nMvaa 48,208,228\n"
"D0 9,32,46,3072\nEX 119,119\nMvaa 208,208,232\nTa 150,17,2048\nMvaa 240,208,236\nTa 152,18,0\nv_add_f32 v120, 0, v118\nMvaa 244,212,236\nTa 154,18,2048\ns_add_u32 m0, s47, -16384\n"
"s_add_u32 s46, s52, 768\nMvaa 212,212,232\nTa 156,19,0\nMvaa 52,212,228\nTa 158,19,2048\nD0 136,28,46,0\nMvaa 84,212,224\nv_add_f32 v119, 1.0, v119\nv_rcp_f32 v119, v119\n"
"Mvaa 88,216,224\nv_ashrrev_i32 v121, 31, v120\nCV 119,119,119\nMvaa 56,216,228\nv_lshlrev_b32 v119, 16, v119\nMvaa 216,216,232\nv_or_b32 v121, 0x80000000, v121\nMvaa 248,216,236\n"
"D0 137,28,46,1024\nv_xor_b32 v120, v120, v121\nMvaa 252,220,236\nv_and_b32 v120, 0xffff0000, v120\nMvaa 220,220,232\ns_lshl2_add_u32 s2, s10, s11\nMvaa 60,220,228\n"
"v_lshrrev_b32 v112, 5, v0\nv_lshl_add_u32 v112, s2, 1, v112\nMvaa 92,220,224\nWL 0\nMvaa 64,128,160\nRa 224,141,0\nD0 138,28,46,2048\nMvaa 32,128,164\nRa 228,141,2048\n"
"Mvaa 192,128,168\nRa 232,143,0\nMvaa 224,128,172\nRa 236,143,2048\nv_sub_u32 v113, 0xffff, v112\nMvaa 228,132,172\nTa 192,12,16384\nMvaa 196,132,168\nD0 139,28,46,3072\n"
"Mvaa 36,132,164\nTa 194,12,18432\nv_or_b32 v120, v120, v113\nMvaa 68,132,160\nTa 196,13,16384\nMvaa 72,136,160\nTa 198,13,18432\ns_add_u32 m0, s47, 81920\n"
"s_add_u32 s46, s51, 3407872\nMvaa 40,136,164\nTa 200,14,16384\nMvaa 200,136,168\nTa 202,14,18432\nD0 6,32,46,0\nMvaa 232,136,172\nv_mov_b32 v121, s15\nv_and_b32 v113, 31, v0\n"
"Mvaa 236,140,172\nTa 204,15,16384\nMvaa 204,140,168\nTa 206,15,18432\nMvaa 44,140,164\nTa 208,16,16384\nv_mov_b32 v116, 0x40000000\nMvaa 76,140,160\nTa 210,16,18432\n"
"D0 7,32,46,1024\nMvaa 80,144,160\nTa 212,17,16384\nv_cmp_eq_u32 vcc, 16, v113\nv_lshlrev_b32 v114, 3, v112\nv_lshlrev_b32 v115, 9, v113\nv_cndmask_b32 v114, v116, v114, vcc\n"
"v_cndmask_b32 v119, v119, v118, vcc\nMvaa 48,144,164\nv_lshl_add_u32 v115, v112, 2, v115\nMvaa 208,144,168\nTa 214,17,18432\nMvaa 240,144,172\nTa 216,18,16384\n"
"v_and_b32 v113, 15, v0\nMvaa 244,148,172\nTa 218,18,18432\nD0 8,32,46,2048\nMvaa 212,148,168\nTa 220,19,16384\nv_cmp_eq_u32 vcc, 0, v113\ns_mov_b32 s2, 0xc040000\n"
"s_mov_b32 s3, 0xc044000\nv_cndmask_b32 v115, v116, v115, vcc\nbuffer_store_dwordx2 v[120:121], v114, s[16:19], s3 offen sc0 sc1\n"
"buffer_store_dword v119, v115, s[16:19], s2 offen sc0 sc1\nMvaa 52,148,164\nTa 222,19,18432\nMvaa 84,148,160\nMvaa 88,152,160\nMvaa 56,152,164\nD0 9,32,46,3072\nMvaa 216,152,168\n"
"Mvaa 248,152,172\nMvaa 252,156,172\nMvaa 220,156,168\nMvaa 60,156,164\nMvaa 92,156,160\ns_waitcnt vmcnt(15) lgkmcnt(0)\ns_barrier\nMvaa 64,192,224\nRa 160,140,32768\n"
"s_add_u32 m0, s47, 0\ns_add_u32 s44, s44, 0x40000\nMvaa 32,192,228\nRa 164,140,34816\nMvaa 192,192,232\nRa 168,142,32768\nD0 6,24,44,0\nMvaa 224,192,236\nRa 172,142,34816\n"
"Mvaa 228,196,236\nTa 128,12,32768\nMvaa 196,196,232\nMvaa 36,196,228\nTa 130,12,34816\nMvaa 68,196,224\nTa 132,13,32768\nD0 7,24,44,1024\nMvaa 72,200,224\nTa 134,13,34816\n"
"Mvaa 40,200,228\nTa 136,14,32768\nMvaa 200,200,232\nTa 138,14,34816\nMvaa 232,200,236\nMvaa 236,204,236\nTa 140,15,32768\nD0 8,24,44,2048\nMvaa 204,204,232\nTa 142,15,34816\n"
"Mvaa 44,204,228\nTa 144,16,32768\nMvaa 76,204,224\nTa 146,16,34816\nMvaa 80,208,224\nTa 148,17,32768\nMvaa 48,208,228\nD0 9,24,44,3072\nMvaa 208,208,232\nTa 150,17,34816\n"
"Mvaa 240,208,236\nTa 152,18,32768\nMvaa 244,212,236\nTa 154,18,34816\ns_add_u32 m0, s48, 0\ns_add_u32 s45, s45, 128\nMvaa 212,212,232\nTa 156,19,32768\nMvaa 52,212,228\n"
"Ta 158,19,34816\nD0 2,20,45,0\nMvaa 84,212,224\nMvaa 88,216,224\nMvaa 56,216,228\nMvaa 216,216,232\nMvaa 248,216,236\nD0 3,20,45,1024\nMvaa 252,220,236\nMvaa 220,220,232\n"
"Mvaa 60,220,228\nMvaa 92,220,224\nWL 0\nMvaa 64,128,160\nRa 224,141,32768\nD0 4,20,45,2048\nMvaa 32,128,164\nRa 228,141,34816\nMvaa 192,128,168\nRa 232,143,32768\n"
"Mvaa 224,128,172\nRa 236,143,34816\nMvaa 228,132,172\nTa 192,12,49152\nMvaa 196,132,168\nD0 5,20,45,3072\nMvaa 36,132,164\nTa 194,12,51200\nMvaa 68,132,160\nTa 196,13,49152\n"
"Mvaa 72,136,160\nTa 198,13,51200\ns_add_u32 m0, s48, 4096\ns_add_u32 s46, s45, 0x10000\nMvaa 40,136,164\nTa 200,14,49152\nMvaa 200,136,168\nTa 202,14,51200\nD0 2,20,46,0\n"
"Mvaa 232,136,172\nMvaa 236,140,172\nTa 204,15,49152\nMvaa 204,140,168\nTa 206,15,51200\nMvaa 44,140,164\nTa 208,16,49152\nMvaa 76,140,160\nTa 210,16,51200\nD0 3,20,46,1024\n"
"Mvaa 80,144,160\nTa 212,17,49152\nMvaa 48,144,164\nMvaa 208,144,168\nTa 214,17,51200\nMvaa 240,144,172\nTa 216,18,49152\nMvaa 244,148,172\nTa 218,18,51200\nD0 4,20,46,2048\n"
"Mvaa 212,148,168\nTa 220,19,49152\nMvaa 52,148,164\nTa 222,19,51200\nMvaa 84,148,160\nMvaa 88,152,160\nMvaa 56,152,164\nD0 5,20,46,3072\nMvaa 216,152,168\nMvaa 248,152,172\n"
"Mvaa 252,156,172\ns_add_u32 m0, s47, 16384\ns_add_u32 s44, s44, 0x40000\nMvaa 220,156,168\nMvaa 60,156,164\nD0 6,24,44,0\nMvaa 92,156,160\ns_waitcnt vmcnt(13) lgkmcnt(0)\n"
"s_barrier\nMvaa 64,192,224\nRa 160,140,49152\nMvaa 32,192,228\nRa 164,140,51200\nMvaa 192,192,232\nRa 168,142,49152\nMvaa 224,192,236\nRa 172,142,51200\nD0 7,24,44,1024\n"
"Mvaa 228,196,236\nTa 128,128,0\nMvaa 196,196,232\nMvaa 36,196,228\nTa 130,128,2048\nMvaa 68,196,224\nTa 132,129,0\nMvaa 72,200,224\nTa 134,129,2048\nD0 8,24,44,2048\n"
"Mvaa 40,200,228\nTa 136,130,0\nMvaa 200,200,232\nTa 138,130,2048\nMvaa 232,200,236\nMvaa 236,204,236\nTa 140,131,0\nMvaa 204,204,232\nTa 142,131,2048\nD0 9,24,44,3072\n"
"Mvaa 44,204,228\nTa 144,132,0\nMvaa 76,204,224\nTa 146,132,2048\nMvaa 80,208,224\nTa 148,133,0\ns_add_u32 m0, s47, 32768\ns_add_u32 s44, s44, 0x40000\nMvaa 48,208,228\n"
"Mvaa 208,208,232\nTa 150,133,2048\nD0 6,24,44,0\nMvaa 240,208,236\nTa 152,134,0\nMvaa 244,212,236\nTa 154,134,2048\nMvaa 212,212,232\nTa 156,135,0\nMvaa 52,212,228\n"
"Ta 158,135,2048\nMvaa 84,212,224\nD0 7,24,44,1024\nMvaa 88,216,224\nMvaa 56,216,228\nMvaa 216,216,232\nMvaa 248,216,236\nMvaa 252,220,236\nD0 8,24,44,2048\nMvaa 220,220,232\n"
"Mvaa 60,220,228\nMvaa 92,220,224\nWL 0\nMvaa 64,128,160\nRa 224,141,49152\nMvaa 32,128,164\nRa 228,141,51200\nD0 9,24,44,3072\nMvaa 192,128,168\nRa 232,143,49152\n"
"Mvaa 224,128,172\nRa 236,143,51200\nMvaa 228,132,172\nTa 192,128,16384\nMvaa 196,132,168\nMvaa 36,132,164\nTa 194,128,18432\nMvaa 68,132,160\nTa 196,129,16384\nMvaa 72,136,160\n"
"Ta 198,129,18432\nMvaa 40,136,164\nTa 200,130,16384\nMvaa 200,136,168\nTa 202,130,18432\nMvaa 232,136,172\nMvaa 236,140,172\nTa 204,131,16384\nMvaa 204,140,168\nTa 206,131,18432\n"
"Mvaa 44,140,164\nTa 208,132,16384\nMvaa 76,140,160\nTa 210,132,18432\nMvaa 80,144,160\nTa 212,133,16384\nMvaa 48,144,164\nMvaa 208,144,168\nTa 214,133,18432\nMvaa 240,144,172\n"
"Ta 216,134,16384\nMvaa 244,148,172\nTa 218,134,18432\nMvaa 212,148,168\nTa 220,135,16384\nMvaa 52,148,164\nTa 222,135,18432\nMvaa 84,148,160\nMvaa 88,152,160\nMvaa 56,152,164\n"
"Mvaa 216,152,168\nMvaa 248,152,172\nMvaa 252,156,172\nMvaa 220,156,168\nMvaa 60,156,164\nMvaa 92,156,160\ns_waitcnt vmcnt(8) lgkmcnt(0)\ns_barrier\nMvaa 64,192,224\n"
"s_add_u32 m0, s48, 32768\ns_add_u32 s45, s45, 128\nTa 128,12,0\nMvaa 68,196,224\nTa 130,12,2048\nMvaa 72,200,224\nD0 2,20,45,0\nTa 132,13,0\nMvaa 76,204,224\nTa 134,13,2048\n"
"Mvaa 80,208,224\nTa 136,14,0\nMvaa 84,212,224\nTa 138,14,2048\nMvaa 88,216,224\nTa 140,15,0\nv_mul_f32 v64, s57, v64\nMvaa 92,220,224\nD0 3,20,45,1024\nTa 142,15,2048\n"
"Mvaa 32,192,228\nTa 144,16,0\nv_mul_f32 v65, s57, v65\nMvaa 36,196,228\nTa 146,16,2048\nMvaa 40,200,228\nTa 148,17,0\nCV 64,64,65\nMvaa 44,204,228\nTa 150,17,2048\n"
"v_mul_f32 v66, s57, v66\nMvaa 48,208,228\nD0 4,20,45,2048\nTa 152,18,0\nMvaa 52,212,228\nTa 154,18,2048\nMvaa 56,216,228\nTa 156,19,0\nv_mul_f32 v67, s57, v67\nMvaa 60,220,228\n"
"Ta 158,19,2048\nCV 65,66,67\nMvaa 192,192,232\nRa 160,10,0\nv_mul_f32 v68, s57, v68\nMvaa 196,196,232\nD0 5,20,45,3072\nRa 164,10,2048\nMvaa 200,200,232\nRa 168,10,8192\n"
"v_mul_f32 v69, s57, v69\nMvaa 204,204,232\nRa 172,10,10240\nCV 66,68,69\nMvaa 208,208,232\nRa 176,10,16384\nv_mul_f32 v70, s57, v70\nMvaa 212,212,232\nRa 180,10,18432\n"
"Mvaa 216,216,232\nRa 184,10,24576\nv_mul_f32 v71, s57, v71\nMvaa 220,220,232\nRa 188,10,26624\nCV 67,70,71\nMvaa 224,192,236\nv_mul_f32 v72, s57, v72\nv_mul_f32 v73, s57, v73\n"
"Mvaa 228,196,236\nCV 72,72,73\nMvaa 232,200,236\nv_mul_f32 v74, s57, v74\nv_mul_f32 v75, s57, v75\nMvaa 236,204,236\nCV 73,74,75\nv_mul_f32 v76, s57, v76\nMvaa 240,208,236\n"
"v_mul_f32 v77, s57, v77\nCV 74,76,77\nMvaa 244,212,236\nv_mul_f32 v78, s57, v78\nMvaa 248,216,236\nv_mul_f32 v79, s57, v79\nCV 75,78,79\nMvaa 252,220,236\n"
"s_waitcnt vmcnt(8) lgkmcnt(0)\ns_barrier\nZaaa 0,176,128\nTa 192,12,16384\nv_mul_f32 v80, s57, v80\nZaaa 4,180,128\ns_add_u32 m0, s48, 36864\ns_add_u32 s46, s45, 0x10000\n"
"s_add_u32 s6, s56, 0\nSv 64,1,16,6,0\nZaaa 64,184,128\nTa 194,12,18432\nv_mul_f32 v81, s57, v81\nZaaa 68,188,128\nD0 2,20,46,0\nCV 80,80,81\nZaaa 76,188,132\nTa 196,13,16384\n"
"v_mul_f32 v82, s57, v82\nZaaa 72,184,132\nv_mul_f32 v83, s57, v83\nZaaa 12,180,132\nTa 198,13,18432\nCV 81,82,83\nZaaa 8,176,132\nD0 3,20,46,1024\nv_mul_f32 v84, s57, v84\n"
"Zaaa 16,176,136\nTa 200,14,16384\nv_mul_f32 v85, s57, v85\nZaaa 20,180,136\nCV 82,84,85\ns_add_u32 s6, s56, 0\nSv 72,1,16,6,2048\nZaaa 80,184,136\nTa 202,14,18432\n"
"v_mul_f32 v86, s57, v86\nZaaa 84,188,136\nD0 4,20,46,2048\nv_mul_f32 v87, s57, v87\nZaaa 92,188,140\nTa 204,15,16384\nCV 83,86,87\nZaaa 88,184,140\nv_mul_f32 v88, s57, v88\n"
"Zaaa 28,180,140\nTa 206,15,18432\nv_mul_f32 v89, s57, v89\nZaaa 24,176,140\nD0 5,20,46,3072\nCV 88,88,89\nZaaa 32,176,144\nTa 208,16,16384\nv_mul_f32 v90, s57, v90\n"
"Zaaa 36,180,144\nv_mul_f32 v91, s57, v91\nZaaa 96,184,144\nTa 210,16,18432\nCV 89,90,91\nZaaa 100,188,144\nv_mul_f32 v92, s57, v92\nv_mul_f32 v93, s57, v93\nZaaa 108,188,148\n"
"Ta 212,17,16384\nCV 90,92,93\nZaaa 104,184,148\nv_mul_f32 v94, s57, v94\nZaaa 44,180,148\nTa 214,17,18432\nv_mul_f32 v95, s57, v95\nZaaa 40,176,148\nCV 91,94,95\nZaaa 48,176,152\n"
"Ta 216,18,16384\nZaaa 52,180,152\ns_add_u32 s6, s56, 4096\nSv 80,1,16,6,0\nZaaa 112,184,152\nTa 218,18,18432\nZaaa 116,188,152\nZaaa 124,188,156\nTa 220,19,16384\n"
"Zaaa 120,184,156\ns_add_u32 s6, s56, 4096\nSv 88,1,16,6,2048\nZaaa 60,180,156\nTa 222,19,18432\nZaaa 56,176,156\ns_add_u32 m0, s47, 49152\ns_add_u32 s44, s44, 0x40000\n"
"Zvaa 64,160,128\nRa 224,11,0\nv_mul_f32 v32, s57, v32\nZvaa 68,164,128\nD0 6,24,44,0\nZvaa 128,168,128\nRa 228,11,2048\nZvaa 132,172,128\nv_mul_f32 v33, s57, v33\n"
"Zvaa 140,172,132\nRa 232,11,8192\nCV 32,32,33\nZvaa 136,168,132\nD0 7,24,44,1024\nZvaa 76,164,132\nRa 236,11,10240\nZvaa 72,160,132\nv_mul_f32 v34, s57, v34\nZvaa 80,160,136\n"
"Ra 240,11,16384\nv_mul_f32 v35, s57, v35\nZvaa 84,164,136\nD0 8,24,44,2048\nZvaa 144,168,136\nRa 244,11,18432\nZvaa 148,172,136\nCV 33,34,35\nZvaa 156,172,140\nRa 248,11,24576\n"
"v_mul_f32 v36, s57, v36\nZvaa 152,168,140\nD0 9,24,44,3072\nZvaa 92,164,140\nRa 252,11,26624\nZvaa 88,160,140\ns_add_u32 m0, s47, 0\ns_add_u32 s44, s44, 0x40000\nZvaa 96,160,144\n"
"v_mul_f32 v37, s57, v37\nCV 34,36,37\nZvaa 100,164,144\nD0 6,24,44,0\nZvaa 160,168,144\nv_mul_f32 v38, s57, v38\nZvaa 164,172,144\nv_mul_f32 v39, s57, v39\nZvaa 172,172,148\n"
"CV 35,38,39\nZvaa 168,168,148\nD0 7,24,44,1024\ns_add_u32 s6, s56, 0\nSv 32,1,16,6,1024\nZvaa 108,164,148\nv_mul_f32 v40, s57, v40\nZvaa 104,160,148\nv_mul_f32 v41, s57, v41\n"
"Zvaa 112,160,152\nCV 40,40,41\nv_mul_f32 v42, s57, v42\nZvaa 116,164,152\nD0 8,24,44,2048\nZvaa 176,168,152\nv_mul_f32 v43, s57, v43\nZvaa 180,172,152\nCV 41,42,43\n"
"Zvaa 188,172,156\nv_mul_f32 v44, s57, v44\nv_mul_f32 v45, s57, v45\nZvaa 184,168,156\nD0 9,24,44,3072\nZvaa 124,164,156\nCV 42,44,45\nZvaa 120,160,156\n"
"s_waitcnt vmcnt(11) lgkmcnt(0)\ns_barrier\nMvaa 64,224,192\nTa 128,12,32768\nv_mul_f32 v46, s57, v46\nMvaa 68,228,192\nv_mul_f32 v47, s57, v47\nMvaa 128,232,192\nTa 130,12,34816\n"
"Mvaa 132,236,192\ns_add_u32 m0, s48, 0\ns_add_u32 s45, s45, 128\nMaaa 0,240,192\nTa 132,13,32768\nCV 43,46,47\nMaaa 4,244,192\nD0 2,20,45,0\ns_add_u32 s6, s56, 0\n"
"Sv 40,1,16,6,3072\nMaaa 64,248,192\nTa 134,13,34816\nMaaa 68,252,192\nv_mul_f32 v48, s57, v48\nMaaa 76,252,196\nTa 136,14,32768\nv_mul_f32 v49, s57, v49\nMaaa 72,248,196\n"
"CV 48,48,49\nMaaa 12,244,196\nTa 138,14,34816\nMaaa 8,240,196\nv_mul_f32 v50, s57, v50\nMvaa 140,236,196\nTa 140,15,32768\nv_mul_f32 v51, s57, v51\nMvaa 136,232,196\n"
"D0 3,20,45,1024\nMvaa 76,228,196\nTa 142,15,34816\nMvaa 72,224,196\nCV 49,50,51\nMvaa 80,224,200\nTa 144,16,32768\nv_mul_f32 v52, s57, v52\nMvaa 84,228,200\n"
"v_mul_f32 v53, s57, v53\nMvaa 144,232,200\nTa 146,16,34816\nMvaa 148,236,200\nCV 50,52,53\nMaaa 16,240,200\nTa 148,17,32768\nv_mul_f32 v54, s57, v54\nMaaa 20,244,200\n"
"D0 4,20,45,2048\nMaaa 80,248,200\nTa 150,17,34816\nMaaa 84,252,200\nv_mul_f32 v55, s57, v55\nMaaa 92,252,204\nTa 152,18,32768\nCV 51,54,55\nMaaa 88,248,204\n"
"s_add_u32 s6, s56, 4096\nSv 48,1,16,6,1024\nMaaa 28,244,204\nTa 154,18,34816\nMaaa 24,240,204\nv_mul_f32 v56, s57, v56\nMvaa 156,236,204\nTa 156,19,32768\nv_mul_f32 v57, s57, v57\n"
"Mvaa 152,232,204\nD0 5,20,45,3072\nMvaa 92,228,204\nTa 158,19,34816\nMvaa 88,224,204\nCV 56,56,57\nMvaa 96,224,208\nRa 160,10,32768\nv_mul_f32 v58, s57, v58\nMvaa 100,228,208\n"
"v_mul_f32 v59, s57, v59\nMvaa 160,232,208\nRa 164,10,34816\nMvaa 164,236,208\ns_add_u32 m0, s48, 4096\ns_add_u32 s46, s45, 0x10000\nMaaa 32,240,208\nRa 168,10,40960\nCV 57,58,59\n"
"Maaa 36,244,208\nD0 2,20,46,0\nMaaa 96,248,208\nRa 172,10,43008\nMaaa 100,252,208\nv_mul_f32 v60, s57, v60\nMaaa 108,252,212\nRa 176,10,49152\nv_mul_f32 v61, s57, v61\n"
"Maaa 104,248,212\nCV 58,60,61\nMaaa 44,244,212\nRa 180,10,51200\nMaaa 40,240,212\nv_mul_f32 v62, s57, v62\nMvaa 172,236,212\nRa 184,10,57344\nv_mul_f32 v63, s57, v63\n"
"Mvaa 168,232,212\nD0 3,20,46,1024\nMvaa 108,228,212\nRa 188,10,59392\nMvaa 104,224,212\nCV 59,62,63\ns_add_u32 s6, s56, 4096\nSv 56,1,16,6,3072\nMvaa 112,224,216\n"
"v_mul_f32 v192, s57, v192\nv_mul_f32 v193, s57, v193\nMvaa 116,228,216\nCV 192,192,193\nMvaa 176,232,216\nv_mul_f32 v194, s57, v194\nMvaa 180,236,216\nv_mul_f32 v195, s57, v195\n"
"Maaa 48,240,216\nCV 193,194,195\nv_mul_f32 v196, s57, v196\nMaaa 52,244,216\nD0 4,20,46,2048\nMaaa 112,248,216\nv_mul_f32 v197, s57, v197\nMaaa 116,252,216\nCV 194,196,197\n"
"Maaa 124,252,220\nv_mul_f32 v198, s57, v198\nv_mul_f32 v199, s57, v199\nMaaa 120,248,220\nCV 195,198,199\ns_add_u32 s6, s53, 0\nSv 192,1,16,6,0\nMaaa 60,244,220\n"
"v_mul_f32 v200, s57, v200\nMaaa 56,240,220\nv_mul_f32 v201, s57, v201\nMvaa 188,236,220\nCV 200,200,201\nv_mul_f32 v202, s57, v202\nMvaa 184,232,220\nD0 5,20,46,3072\n"
"Mvaa 124,228,220\nv_mul_f32 v203, s57, v203\nMvaa 120,224,220\ns_waitcnt vmcnt(17) lgkmcnt(0)\ns_barrier\nMvaa 64,160,128\nTa 192,12,49152\nCV 201,202,203\nMvaa 68,164,128\n"
"v_mul_f32 v204, s57, v204\nMvaa 128,168,128\nTa 194,12,51200\nMvaa 132,172,128\ns_add_u32 m0, s47, 16384\ns_add_u32 s44, s44, 0x40000\nMaaa 0,176,128\nTa 196,13,49152\n"
"v_mul_f32 v205, s57, v205\nMaaa 4,180,128\nD0 6,24,44,0\nMaaa 64,184,128\nTa 198,13,51200\nMaaa 68,188,128\nCV 202,204,205\nMaaa 76,188,132\nTa 200,14,49152\n"
"v_mul_f32 v206, s57, v206\nMaaa 72,184,132\nv_mul_f32 v207, s57, v207\nMaaa 12,180,132\nTa 202,14,51200\nMaaa 8,176,132\nCV 203,206,207\ns_add_u32 s6, s53, 0\nSv 200,1,16,6,2048\n"
"Mvaa 140,172,132\nTa 204,15,49152\nv_mul_f32 v208, s57, v208\nMvaa 136,168,132\nD0 7,24,44,1024\nMvaa 76,164,132\nTa 206,15,51200\nMvaa 72,160,132\nv_mul_f32 v209, s57, v209\n"
"Mvaa 80,160,136\nTa 208,16,49152\nCV 208,208,209\nMvaa 84,164,136\nv_mul_f32 v210, s57, v210\nMvaa 144,168,136\nTa 210,16,51200\nMvaa 148,172,136\nv_mul_f32 v211, s57, v211\n"
"Maaa 16,176,136\nTa 212,17,49152\nCV 209,210,211\nMaaa 20,180,136\nD0 8,24,44,2048\nMaaa 80,184,136\nTa 214,17,51200\nMaaa 84,188,136\nv_mul_f32 v212, s57, v212\nMaaa 92,188,140\n"
"Ta 216,18,49152\nv_mul_f32 v213, s57, v213\nMaaa 88,184,140\nCV 210,212,213\nMaaa 28,180,140\nTa 218,18,51200\nMaaa 24,176,140\nv_mul_f32 v214, s57, v214\nMvaa 156,172,140\n"
"Ta 220,19,49152\nv_mul_f32 v215, s57, v215\nMvaa 152,168,140\nD0 9,24,44,3072\nMvaa 92,164,140\nTa 222,19,51200\nMvaa 88,160,140\nCV 211,214,215\ns_add_u32 s6, s53, 4096\n"
"Sv 208,1,16,6,0\nMvaa 96,160,144\nRa 224,11,32768\nv_mul_f32 v216, s57, v216\nMvaa 100,164,144\nv_mul_f32 v217, s57, v217\nMvaa 160,168,144\nRa 228,11,34816\nMvaa 164,172,144\n"
"s_add_u32 m0, s47, 32768\ns_add_u32 s44, s44, 0x40000\nMaaa 32,176,144\nRa 232,11,40960\nCV 216,216,217\nMaaa 36,180,144\nD0 6,24,44,0\nMaaa 96,184,144\nRa 236,11,43008\n"
"Maaa 100,188,144\nv_mul_f32 v218, s57, v218\nMaaa 108,188,148\nRa 240,11,49152\nv_mul_f32 v219, s57, v219\nMaaa 104,184,148\nCV 217,218,219\nMaaa 44,180,148\nRa 244,11,51200\n"
"Maaa 40,176,148\nv_mul_f32 v220, s57, v220\nMvaa 172,172,148\nRa 248,11,57344\nv_mul_f32 v221, s57, v221\nMvaa 168,168,148\nD0 7,24,44,1024\nMvaa 108,164,148\nRa 252,11,59392\n"
"Mvaa 104,160,148\nCV 218,220,221\nMvaa 112,160,152\nv_mul_f32 v222, s57, v222\nv_mul_f32 v223, s57, v223\nMvaa 116,164,152\nCV 219,222,223\ns_add_u32 s6, s53, 4096\n"
"Sv 216,1,16,6,2048\nMvaa 176,168,152\nv_mul_f32 v224, s57, v224\nMvaa 180,172,152\nv_mul_f32 v225, s57, v225\nMaaa 48,176,152\nCV 224,224,225\nv_mul_f32 v226, s57, v226\n"
"Maaa 52,180,152\nD0 8,24,44,2048\nMaaa 112,184,152\nv_mul_f32 v227, s57, v227\nMaaa 116,188,152\nCV 225,226,227\nMaaa 124,188,156\nv_mul_f32 v228, s57, v228\n"
"v_mul_f32 v229, s57, v229\nMaaa 120,184,156\nCV 226,228,229\nMaaa 60,180,156\nv_mul_f32 v230, s57, v230\nMaaa 56,176,156\nv_mul_f32 v231, s57, v231\nMvaa 188,172,156\n"
"CV 227,230,231\nMvaa 184,168,156\nD0 9,24,44,3072\ns_add_u32 s6, s53, 0\nSv 224,1,16,6,1024\nMvaa 124,164,156\nv_mul_f32 v232, s57, v232\nMvaa 120,160,156\n"
"s_waitcnt vmcnt(12) lgkmcnt(0)\ns_barrier\nMvaa 64,224,192\nTa 128,12,0\nv_mul_f32 v233, s57, v233\nMvaa 68,228,192\nCV 232,232,233\nMvaa 128,232,192\nTa 130,12,2048\n"
"Mvaa 132,236,192\ns_add_u32 m0, s48, 32768\ns_add_u32 s45, s45, 128\nMaaa 0,240,192\nTa 132,13,0\nv_mul_f32 v234, s57, v234\nMaaa 4,244,192\nD0 2,20,45,0\nMaaa 64,248,192\n"
"Ta 134,13,2048\nMaaa 68,252,192\nv_mul_f32 v235, s57, v235\nMaaa 76,252,196\nTa 136,14,0\nCV 233,234,235\nMaaa 72,248,196\nv_mul_f32 v236, s57, v236\nMaaa 12,244,196\n"
"Ta 138,14,2048\nMaaa 8,240,196\nv_mul_f32 v237, s57, v237\nMvaa 140,236,196\nTa 140,15,0\nCV 234,236,237\nMvaa 136,232,196\nD0 3,20,45,1024\nMvaa 76,228,196\nTa 142,15,2048\n"
"Mvaa 72,224,196\nv_mul_f32 v238, s57, v238\nMvaa 80,224,200\nTa 144,16,0\nv_mul_f32 v239, s57, v239\nMvaa 84,228,200\nCV 235,238,239\ns_add_u32 s6, s53, 0\nSv 232,1,16,6,3072\n"
"Mvaa 144,232,200\nTa 146,16,2048\nMvaa 148,236,200\nv_mul_f32 v240, s57, v240\nMaaa 16,240,200\nTa 148,17,0\nv_mul_f32 v241, s57, v241\nMaaa 20,244,200\nD0 4,20,45,2048\n"
"Maaa 80,248,200\nTa 150,17,2048\nMaaa 84,252,200\nCV 240,240,241\nMaaa 92,252,204\nTa 152,18,0\nv_mul_f32 v242, s57, v242\nMaaa 88,248,204\nv_mul_f32 v243, s57, v243\n"
"Maaa 28,244,204\nTa 154,18,2048\nMaaa 24,240,204\nCV 241,242,243\nMvaa 156,236,204\nTa 156,19,0\nv_mul_f32 v244, s57, v244\nMvaa 152,232,204\nD0 5,20,45,3072\nMvaa 92,228,204\n"
"Ta 158,19,2048\nMvaa 88,224,204\nv_mul_f32 v245, s57, v245\nMvaa 96,224,208\nRa 160,10,0\nCV 242,244,245\nMvaa 100,228,208\nv_mul_f32 v246, s57, v246\nMvaa 160,232,208\n"
"Ra 164,10,2048\nMvaa 164,236,208\ns_add_u32 m0, s48, 36864\ns_add_u32 s46, s45, 0x10000\nMaaa 32,240,208\nRa 168,10,8192\nv_mul_f32 v247, s57, v247\nMaaa 36,244,208\nD0 2,20,46,0\n"
"Maaa 96,248,208\nRa 172,10,10240\nMaaa 100,252,208\nCV 243,246,247\ns_add_u32 s6, s53, 4096\nSv 240,1,16,6,1024\nMaaa 108,252,212\nRa 176,10,16384\nv_mul_f32 v248, s57, v248\n"
"Maaa 104,248,212\nv_mul_f32 v249, s57, v249\nMaaa 44,244,212\nRa 180,10,18432\nMaaa 40,240,212\nCV 248,248,249\nMvaa 172,236,212\nRa 184,10,24576\nv_mul_f32 v250, s57, v250\n"
"Mvaa 168,232,212\nD0 3,20,46,1024\nMvaa 108,228,212\nRa 188,10,26624\nMvaa 104,224,212\nv_mul_f32 v251, s57, v251\nMvaa 112,224,216\nCV 249,250,251\nv_mul_f32 v252, s57, v252\n"
"Mvaa 116,228,216\nv_mul_f32 v253, s57, v253\nMvaa 176,232,216\nCV 250,252,253\nMvaa 180,236,216\nv_mul_f32 v254, s57, v254\nMaaa 48,240,216\nv_mul_f32 v255, s57, v255\n"
"CV 251,254,255\nMaaa 52,244,216\nD0 4,20,46,2048\ns_add_u32 s6, s53, 4096\nSv 248,1,16,6,3072\nMaaa 112,248,216\nMaaa 116,252,216\nMaaa 124,252,220\nMaaa 120,248,220\n"
"Maaa 60,244,220\nMaaa 56,240,220\ns_lshl3_add_u32 s90, s12, s13\ns_lshl2_add_u32 s90, s90, s11\ns_lshl2_add_u32 s90, s90, 0xc04a400\nMvaa 188,236,220\nMvaa 184,232,220\n"
"D0 5,20,46,3072\nMvaa 124,228,220\nMvaa 120,224,220\ns_waitcnt vmcnt(18) lgkmcnt(0)\ns_barrier\ns_mov_b64 s[76:77], s[62:63]\ns_mov_b64 s[78:79], s[64:65]\ns_mov_b32 s42, 1605632\n"
"s_mov_b32 s50, 1\n.GL138:\nMvaa 64,160,128\nTa 192,12,16384\nMvaa 68,164,128\nMvaa 128,168,128\nTa 194,12,18432\nMvaa 132,172,128\ns_add_u32 m0, s47, 49152\n"
"s_add_u32 s44, s44, 0x40000\nMaaa 0,176,128\nTa 196,13,16384\nMaaa 4,180,128\nD0 6,24,44,0\nMaaa 64,184,128\nTa 198,13,18432\nMaaa 68,188,128\nMaaa 76,188,132\nTa 200,14,16384\n"
"Maaa 72,184,132\nMaaa 12,180,132\nTa 202,14,18432\nMaaa 8,176,132\nMvaa 140,172,132\nTa 204,15,16384\nMvaa 136,168,132\nD0 7,24,44,1024\nMvaa 76,164,132\nTa 206,15,18432\n"
"Mvaa 72,160,132\nMvaa 80,160,136\nTa 208,16,16384\nMvaa 84,164,136\nMvaa 144,168,136\nTa 210,16,18432\nMvaa 148,172,136\nMaaa 16,176,136\nTa 212,17,16384\nMaaa 20,180,136\n"
"D0 8,24,44,2048\nMaaa 80,184,136\nTa 214,17,18432\nMaaa 84,188,136\nMaaa 92,188,140\nTa 216,18,16384\nMaaa 88,184,140\nMaaa 28,180,140\nTa 218,18,18432\nMaaa 24,176,140\n"
"Mvaa 156,172,140\nTa 220,19,16384\nMvaa 152,168,140\nD0 9,24,44,3072\nMvaa 92,164,140\nTa 222,19,18432\nMvaa 88,160,140\ns_mov_b32 s40, s76\ns_and_b32 s41, s77, 0xffff\n"
"Mvaa 96,160,144\nRa 224,11,0\nMvaa 100,164,144\nbuffer_load_dword v31, v26, s[40:43], s97 offen\ns_mov_b64 s[76:77], s[66:67]\nMvaa 160,168,144\nRa 228,11,2048\nMvaa 164,172,144\n"
"s_add_u32 m0, s47, 0\ns_add_u32 s44, s44, 0x40000\nMaaa 32,176,144\nRa 232,11,8192\nMaaa 36,180,144\nD0 6,24,44,0\nMaaa 96,184,144\nRa 236,11,10240\nMaaa 100,188,144\n"
"Maaa 108,188,148\nRa 240,11,16384\nMaaa 104,184,148\nMaaa 44,180,148\nRa 244,11,18432\nMaaa 40,176,148\nMvaa 172,172,148\nRa 248,11,24576\nMvaa 168,168,148\nD0 7,24,44,1024\n"
"Mvaa 108,164,148\nRa 252,11,26624\nMvaa 104,160,148\nMvaa 112,160,152\nMvaa 116,164,152\nMvaa 176,168,152\nMvaa 180,172,152\nMaaa 48,176,152\nMaaa 52,180,152\nD0 8,24,44,2048\n"
"Maaa 112,184,152\nMaaa 116,188,152\nMaaa 124,188,156\nMaaa 120,184,156\nMaaa 60,180,156\nMaaa 56,176,156\nMvaa 188,172,156\nMvaa 184,168,156\nD0 9,24,44,3072\nMvaa 124,164,156\n"
"Mvaa 120,160,156\ns_waitcnt vmcnt(9) lgkmcnt(0)\ns_barrier\nMvaa 64,224,192\nTa 128,12,32768\nMvaa 68,228,192\nMvaa 128,232,192\nTa 130,12,34816\nMvaa 132,236,192\n"
"s_add_u32 m0, s48, 0\ns_add_u32 s45, s45, 128\nMaaa 0,240,192\nTa 132,13,32768\nMaaa 4,244,192\nD0 2,20,45,0\nMaaa 64,248,192\nTa 134,13,34816\nMaaa 68,252,192\nMaaa 76,252,196\n"
"Ta 136,14,32768\nMaaa 72,248,196\nMaaa 12,244,196\nTa 138,14,34816\nMaaa 8,240,196\nMvaa 140,236,196\nTa 140,15,32768\nMvaa 136,232,196\nD0 3,20,45,1024\nMvaa 76,228,196\n"
"Ta 142,15,34816\nMvaa 72,224,196\nMvaa 80,224,200\nTa 144,16,32768\nMvaa 84,228,200\nMvaa 144,232,200\nTa 146,16,34816\nMvaa 148,236,200\nMaaa 16,240,200\nTa 148,17,32768\n"
"Maaa 20,244,200\nD0 4,20,45,2048\nMaaa 80,248,200\nTa 150,17,34816\nMaaa 84,252,200\nMaaa 92,252,204\nTa 152,18,32768\nMaaa 88,248,204\nMaaa 28,244,204\nTa 154,18,34816\n"
"Maaa 24,240,204\nMvaa 156,236,204\nTa 156,19,32768\nMvaa 152,232,204\nD0 5,20,45,3072\nMvaa 92,228,204\nTa 158,19,34816\nMvaa 88,224,204\nMvaa 96,224,208\nRa 160,10,32768\n"
"Mvaa 100,228,208\nMvaa 160,232,208\nRa 164,10,34816\nMvaa 164,236,208\ns_add_u32 m0, s48, 4096\ns_add_u32 s46, s45, 0x10000\nMaaa 32,240,208\nRa 168,10,40960\nMaaa 36,244,208\n"
"D0 2,20,46,0\nMaaa 96,248,208\nRa 172,10,43008\nMaaa 100,252,208\nMaaa 108,252,212\nRa 176,10,49152\nMaaa 104,248,212\nMaaa 44,244,212\nRa 180,10,51200\nMaaa 40,240,212\n"
"Mvaa 172,236,212\nRa 184,10,57344\nMvaa 168,232,212\nD0 3,20,46,1024\nMvaa 108,228,212\nRa 188,10,59392\nMvaa 104,224,212\nMvaa 112,224,216\nMvaa 116,228,216\nMvaa 176,232,216\n"
"Mvaa 180,236,216\nMaaa 48,240,216\nMaaa 52,244,216\nD0 4,20,46,2048\nMaaa 112,248,216\nMaaa 116,252,216\nMaaa 124,252,220\nMaaa 120,248,220\nMaaa 60,244,220\nMaaa 56,240,220\n"
"Mvaa 188,236,220\nMvaa 184,232,220\nD0 5,20,46,3072\nMvaa 124,228,220\nMvaa 120,224,220\ns_waitcnt vmcnt(13) lgkmcnt(0)\ns_barrier\nMvaa 64,160,128\nTa 192,12,49152\n"
"Mvaa 68,164,128\nMvaa 128,168,128\nTa 194,12,51200\nMvaa 132,172,128\ns_add_u32 m0, s47, 16384\ns_add_u32 s44, s44, 0x40000\nMaaa 0,176,128\nTa 196,13,49152\nMaaa 4,180,128\n"
"D0 6,24,44,0\nMaaa 64,184,128\nTa 198,13,51200\nMaaa 68,188,128\nMaaa 76,188,132\nTa 200,14,49152\nMaaa 72,184,132\nMaaa 12,180,132\nTa 202,14,51200\nMaaa 8,176,132\n"
"Mvaa 140,172,132\nTa 204,15,49152\nMvaa 136,168,132\nD0 7,24,44,1024\nMvaa 76,164,132\nTa 206,15,51200\nMvaa 72,160,132\nMvaa 80,160,136\nTa 208,16,49152\nMvaa 84,164,136\n"
"Mvaa 144,168,136\nTa 210,16,51200\nMvaa 148,172,136\nMaaa 16,176,136\nTa 212,17,49152\nMaaa 20,180,136\nD0 8,24,44,2048\nMaaa 80,184,136\nTa 214,17,51200\nMaaa 84,188,136\n"
"Maaa 92,188,140\nTa 216,18,49152\nMaaa 88,184,140\nMaaa 28,180,140\nTa 218,18,51200\nMaaa 24,176,140\nMvaa 156,172,140\nTa 220,19,49152\nMvaa 152,168,140\nD0 9,24,44,3072\n"
"Mvaa 92,164,140\nTa 222,19,51200\nMvaa 88,160,140\ns_mov_b32 s40, s78\ns_and_b32 s41, s79, 0xffff\nMvaa 96,160,144\nRa 224,11,32768\nMvaa 100,164,144\n"
"buffer_load_dword v31, v26, s[40:43], s97 offen\ns_mov_b64 s[78:79], s[66:67]\nMvaa 160,168,144\nRa 228,11,34816\nMvaa 164,172,144\ns_add_u32 m0, s47, 32768\n"
"s_add_u32 s44, s44, 0x40000\nMaaa 32,176,144\nRa 232,11,40960\nMaaa 36,180,144\nD0 6,24,44,0\nMaaa 96,184,144\nRa 236,11,43008\nMaaa 100,188,144\nMaaa 108,188,148\n"
"Ra 240,11,49152\nMaaa 104,184,148\nMaaa 44,180,148\nRa 244,11,51200\nMaaa 40,176,148\nMvaa 172,172,148\nRa 248,11,57344\nMvaa 168,168,148\nD0 7,24,44,1024\nMvaa 108,164,148\n"
"Ra 252,11,59392\nMvaa 104,160,148\nMvaa 112,160,152\nMvaa 116,164,152\nMvaa 176,168,152\nMvaa 180,172,152\nMaaa 48,176,152\nMaaa 52,180,152\nD0 8,24,44,2048\nMaaa 112,184,152\n"
"Maaa 116,188,152\nMaaa 124,188,156\nMaaa 120,184,156\nMaaa 60,180,156\nMaaa 56,176,156\nMvaa 188,172,156\nMvaa 184,168,156\nD0 9,24,44,3072\nMvaa 124,164,156\nMvaa 120,160,156\n"
"s_waitcnt vmcnt(9) lgkmcnt(0)\ns_barrier\nMvaa 64,224,192\nTa 128,12,0\nMvaa 68,228,192\nMvaa 128,232,192\nTa 130,12,2048\nMvaa 132,236,192\ns_add_u32 m0, s48, 32768\n"
"s_add_u32 s45, s45, 128\nMaaa 0,240,192\nTa 132,13,0\nMaaa 4,244,192\nD0 2,20,45,0\nMaaa 64,248,192\nTa 134,13,2048\nMaaa 68,252,192\nMaaa 76,252,196\nTa 136,14,0\n"
"Maaa 72,248,196\nMaaa 12,244,196\nTa 138,14,2048\nMaaa 8,240,196\nMvaa 140,236,196\nTa 140,15,0\nMvaa 136,232,196\nD0 3,20,45,1024\nMvaa 76,228,196\nTa 142,15,2048\n"
"Mvaa 72,224,196\nMvaa 80,224,200\nTa 144,16,0\nMvaa 84,228,200\nMvaa 144,232,200\nTa 146,16,2048\nMvaa 148,236,200\nMaaa 16,240,200\nTa 148,17,0\nMaaa 20,244,200\nD0 4,20,45,2048\n"
"Maaa 80,248,200\nTa 150,17,2048\nMaaa 84,252,200\nMaaa 92,252,204\nTa 152,18,0\nMaaa 88,248,204\nMaaa 28,244,204\nTa 154,18,2048\nMaaa 24,240,204\nMvaa 156,236,204\nTa 156,19,0\n"
"Mvaa 152,232,204\nD0 5,20,45,3072\nMvaa 92,228,204\nTa 158,19,2048\nMvaa 88,224,204\nMvaa 96,224,208\nRa 160,10,0\nMvaa 100,228,208\nMvaa 160,232,208\nRa 164,10,2048\n"
"Mvaa 164,236,208\ns_add_u32 m0, s48, 36864\ns_add_u32 s46, s45, 0x10000\nMaaa 32,240,208\nRa 168,10,8192\nMaaa 36,244,208\nD0 2,20,46,0\nMaaa 96,248,208\nRa 172,10,10240\n"
"Maaa 100,252,208\nMaaa 108,252,212\nRa 176,10,16384\nMaaa 104,248,212\nMaaa 44,244,212\nRa 180,10,18432\nMaaa 40,240,212\nMvaa 172,236,212\nRa 184,10,24576\nMvaa 168,232,212\n"
"D0 3,20,46,1024\nMvaa 108,228,212\nRa 188,10,26624\nMvaa 104,224,212\nMvaa 112,224,216\nMvaa 116,228,216\nMvaa 176,232,216\nMvaa 180,236,216\nMaaa 48,240,216\nMaaa 52,244,216\n"
"D0 4,20,46,2048\nMaaa 112,248,216\nMaaa 116,252,216\nMaaa 124,252,220\nMaaa 120,248,220\nMaaa 60,244,220\nMaaa 56,240,220\nMvaa 188,236,220\nMvaa 184,232,220\nD0 5,20,46,3072\n"
"s_sub_u32 s50, s50, 1\nMvaa 124,228,220\nMvaa 120,224,220\ns_waitcnt vmcnt(13) lgkmcnt(0)\ns_barrier\ns_cbranch_scc0 .GL138\nMvaa 64,160,128\nTa 192,12,16384\n"
"s_and_b32 s2, s74, 15\nMvaa 68,164,128\ns_sub_u32 s2, s2, 2\ns_cmp_lt_u32 s2, 14\ns_cselect_b32 s5, 0, 0x40000000\nMvaa 128,168,128\nTa 194,12,18432\nMvaa 132,172,128\n"
"s_add_u32 m0, s47, 49152\ns_add_u32 s44, s44, 0x40000\nMaaa 0,176,128\nTa 196,13,16384\nMaaa 4,180,128\nD0 6,24,44,0\nMaaa 64,184,128\nTa 198,13,18432\nMaaa 68,188,128\n"
"s_and_b32 s2, s2, 15\nMaaa 76,188,132\nTa 200,14,16384\ns_mul_i32 s4, s73, 14\nMaaa 72,184,132\ns_add_u32 s4, s4, s2\nbuffer_store_dword v25, off, s[16:19], s90 offset:0 sc0 sc1\n"
"Maaa 12,180,132\nTa 202,14,18432\nMaaa 8,176,132\ns_mov_b32 s36, s62\nMvaa 140,172,132\nTa 204,15,16384\nMvaa 136,168,132\nD0 7,24,44,1024\nMvaa 76,164,132\nTa 206,15,18432\n"
"Mvaa 72,160,132\ns_and_b32 s37, s63, 0xffff\nMvaa 80,160,136\nTa 208,16,16384\ns_mov_b32 s38, 0x188000\nMvaa 84,164,136\ns_mov_b32 s39, 0x20000\nMvaa 144,168,136\nTa 210,16,18432\n"
"Mvaa 148,172,136\ns_mov_b32 s40, s64\nMaaa 16,176,136\nTa 212,17,16384\nMaaa 20,180,136\nD0 8,24,44,2048\nMaaa 80,184,136\nTa 214,17,18432\nMaaa 84,188,136\n"
"s_and_b32 s41, s65, 0xffff\nMaaa 92,188,140\nTa 216,18,16384\ns_mov_b32 s42, 0x188000\nMaaa 88,184,140\ns_mov_b32 s43, 0x20000\nMaaa 28,180,140\nTa 218,18,18432\nMaaa 24,176,140\n"
"s_mov_b32 s92, s66\nMvaa 156,172,140\nTa 220,19,16384\nMvaa 152,168,140\nD0 9,24,44,3072\nMvaa 92,164,140\nTa 222,19,18432\nMvaa 88,160,140\ns_and_b32 s93, s67, 0xffff\n"
"Mvaa 96,160,144\nRa 224,11,0\ns_mov_b32 s94, 0x188000\nMvaa 100,164,144\ns_mov_b32 s95, 0x20000\nMvaa 160,168,144\nRa 228,11,2048\nMvaa 164,172,144\ns_add_u32 m0, s47, 0\n"
"s_add_u32 s44, s44, 0x40000\nMaaa 32,176,144\nRa 232,11,8192\nMaaa 36,180,144\nD0 6,24,44,0\nMaaa 96,184,144\nRa 236,11,10240\nMaaa 100,188,144\nv_lshrrev_b32 v252, 1, v0\n"
"Maaa 108,188,148\nRa 240,11,16384\nv_mul_u32_u24 v253, 14336, v252\nMaaa 104,184,148\nv_and_b32 v254, 1, v0\nMaaa 44,180,148\nRa 244,11,18432\nMaaa 40,176,148\n"
"v_lshl_add_u32 v253, v254, 4, v253\nMvaa 172,172,148\nRa 248,11,24576\nMvaa 168,168,148\nD0 7,24,44,1024\nMvaa 108,164,148\nRa 252,11,26624\nMvaa 104,160,148\n"
"v_mov_b32 v254, 0x40000000\nv_cmp_gt_u32 vcc, 56, v0\nv_lshlrev_b32 v252, 3, v252\ns_mul_i32 s2, s11, 401408\nv_cndmask_b32 v21, v254, v253, vcc\n"
"v_cndmask_b32 v255, v254, v252, vcc\nMvaa 112,160,152\ns_lshl_b32 s3, s4, 5\ns_add_u32 s2, s2, s3\nMvaa 116,164,152\ns_or_b32 s86, s2, s5\nMvaa 176,168,152\n"
"s_add_u32 s87, s86, 7168\nMvaa 180,172,152\nv_lshrrev_b32 v251, 4, v0\nMaaa 48,176,152\nv_mul_u32_u24 v252, 3584, v251\nMaaa 52,180,152\nD0 8,24,44,2048\nMaaa 112,184,152\n"
"v_and_b32 v251, 15, v0\nMaaa 116,188,152\nv_lshl_add_u32 v252, v251, 4, v252\nv_cmp_gt_u32 vcc, 14, v251\ns_mul_i32 s2, s4, 14336\ns_mul_i32 s3, s11, 224\n"
"v_cndmask_b32 v20, v254, v252, vcc\nMaaa 124,188,156\ns_add_u32 s2, s2, s3\ns_or_b32 s88, s2, s5\nMaaa 120,184,156\nMaaa 60,180,156\nMaaa 56,176,156\nMvaa 188,172,156\n"
"Mvaa 184,168,156\nD0 9,24,44,3072\nMvaa 124,164,156\nMvaa 120,160,156\ns_waitcnt vmcnt(9) lgkmcnt(0)\ns_barrier\nMvaa 64,224,192\nTa 128,12,32768\ns_lshl_b32 s6, s11, 12\n"
"s_add_u32 s6, s6, 0xc044000\nMvaa 68,228,192\nbuffer_load_dwordx4 v[192:195], v21, s[36:39], s86 offen offset:0\nMvaa 128,232,192\nTa 130,12,34816\nMvaa 132,236,192\n"
"s_add_u32 m0, s48, 0\ns_add_u32 s45, s45, 128\nMaaa 0,240,192\nTa 132,13,32768\nMaaa 4,244,192\nD0 2,20,45,0\nMaaa 64,248,192\nTa 134,13,34816\nMaaa 68,252,192\n"
"buffer_load_dwordx4 v[32:35], v1, s[16:19], s6 offen offset:0 sc0 sc1\nMaaa 76,252,196\nTa 136,14,32768\nMaaa 72,248,196\nMaaa 12,244,196\nTa 138,14,34816\nMaaa 8,240,196\n"
"buffer_load_dwordx4 v[36:39], v1, s[16:19], s6 offen offset:1024 sc0 sc1\nMvaa 140,236,196\nTa 140,15,32768\nMvaa 136,232,196\nD0 3,20,45,1024\nMvaa 76,228,196\nTa 142,15,34816\n"
"Mvaa 72,224,196\nbuffer_load_dwordx4 v[40:43], v1, s[16:19], s6 offen offset:2048 sc0 sc1\nMvaa 80,224,200\nTa 144,16,32768\nMvaa 84,228,200\nMvaa 144,232,200\nTa 146,16,34816\n"
"Mvaa 148,236,200\nbuffer_load_dwordx4 v[44:47], v1, s[16:19], s6 offen offset:3072 sc0 sc1\nMaaa 16,240,200\nTa 148,17,32768\nMaaa 20,244,200\nD0 4,20,45,2048\nMaaa 80,248,200\n"
"Ta 150,17,34816\nMaaa 84,252,200\nMaaa 92,252,204\nTa 152,18,32768\nMaaa 88,248,204\nMaaa 28,244,204\nTa 154,18,34816\nMaaa 24,240,204\nMvaa 156,236,204\nTa 156,19,32768\n"
"Mvaa 152,232,204\nD0 5,20,45,3072\nMvaa 92,228,204\nTa 158,19,34816\nMvaa 88,224,204\nMvaa 96,224,208\nRa 160,10,32768\nMvaa 100,228,208\nMvaa 160,232,208\nRa 164,10,34816\n"
"Mvaa 164,236,208\ns_add_u32 m0, s48, 4096\ns_add_u32 s46, s45, 0x10000\nMaaa 32,240,208\nRa 168,10,40960\nMaaa 36,244,208\nD0 2,20,46,0\nMaaa 96,248,208\nRa 172,10,43008\n"
"Maaa 100,252,208\nMaaa 108,252,212\nRa 176,10,49152\nMaaa 104,248,212\nMaaa 44,244,212\nRa 180,10,51200\nMaaa 40,240,212\nMvaa 172,236,212\nRa 184,10,57344\nMvaa 168,232,212\n"
"D0 3,20,46,1024\nMvaa 108,228,212\nRa 188,10,59392\nMvaa 104,224,212\nMvaa 112,224,216\nMvaa 116,228,216\nMvaa 176,232,216\nMvaa 180,236,216\nMaaa 48,240,216\nMaaa 52,244,216\n"
"D0 4,20,46,2048\nMaaa 112,248,216\nMaaa 116,252,216\nMaaa 124,252,220\nMaaa 120,248,220\nMaaa 60,244,220\nMaaa 56,240,220\nMvaa 188,236,220\nMvaa 184,232,220\nD0 5,20,46,3072\n"
"Mvaa 124,228,220\nMvaa 120,224,220\ns_waitcnt vmcnt(17) lgkmcnt(0)\ns_barrier\nMvaa 64,160,128\nTa 192,12,49152\nMvaa 68,164,128\n"
"buffer_load_dwordx4 v[208:211], v21, s[40:43], s86 offen offset:0\nMvaa 128,168,128\nTa 194,12,51200\nMvaa 132,172,128\ns_add_u32 m0, s47, 16384\ns_add_u32 s44, s44, 0x40000\n"
"Maaa 0,176,128\nTa 196,13,49152\nMaaa 4,180,128\nD0 6,24,44,0\nMaaa 64,184,128\nTa 198,13,51200\nMaaa 68,188,128\nMaaa 76,188,132\nTa 200,14,49152\nMaaa 72,184,132\n"
"Maaa 12,180,132\nTa 202,14,51200\nMaaa 8,176,132\nMvaa 140,172,132\nTa 204,15,49152\nMvaa 136,168,132\nD0 7,24,44,1024\nMvaa 76,164,132\nTa 206,15,51200\nMvaa 72,160,132\n"
"Mvaa 80,160,136\nTa 208,16,49152\nMvaa 84,164,136\nMvaa 144,168,136\nTa 210,16,51200\nMvaa 148,172,136\nMaaa 16,176,136\nTa 212,17,49152\nMaaa 20,180,136\nD0 8,24,44,2048\n"
"Maaa 80,184,136\nTa 214,17,51200\nMaaa 84,188,136\nMaaa 92,188,140\nTa 216,18,49152\nMaaa 88,184,140\nMaaa 28,180,140\nTa 218,18,51200\nMaaa 24,176,140\nMvaa 156,172,140\n"
"Ta 220,19,49152\nMvaa 152,168,140\nD0 9,24,44,3072\nMvaa 92,164,140\nTa 222,19,51200\nMvaa 88,160,140\nMvaa 96,160,144\nRa 224,11,32768\nMvaa 100,164,144\nMvaa 160,168,144\n"
"Ra 228,11,34816\nMvaa 164,172,144\ns_add_u32 m0, s47, 32768\ns_add_u32 s44, s44, 0x40000\nMaaa 32,176,144\nRa 232,11,40960\nMaaa 36,180,144\nD0 6,24,44,0\nMaaa 96,184,144\n"
"Ra 236,11,43008\nMaaa 100,188,144\nMaaa 108,188,148\nRa 240,11,49152\nMaaa 104,184,148\nMaaa 44,180,148\nRa 244,11,51200\nMaaa 40,176,148\nMvaa 172,172,148\nRa 248,11,57344\n"
"Mvaa 168,168,148\nD0 7,24,44,1024\nMvaa 108,164,148\nRa 252,11,59392\nMvaa 104,160,148\nMvaa 112,160,152\nMvaa 116,164,152\nMvaa 176,168,152\nMvaa 180,172,152\nMaaa 48,176,152\n"
"Maaa 52,180,152\nD0 8,24,44,2048\nMaaa 112,184,152\nMaaa 116,188,152\nMaaa 124,188,156\nMaaa 120,184,156\nMaaa 60,180,156\nMaaa 56,176,156\nMvaa 188,172,156\nMvaa 184,168,156\n"
"D0 9,24,44,3072\nMvaa 124,164,156\nMvaa 120,160,156\ns_waitcnt vmcnt(9) lgkmcnt(0)\ns_barrier\nMvaa 64,224,192\nTa 128,12,0\nMvaa 68,228,192\n"
"buffer_load_dwordx4 v[196:199], v21, s[36:39], s86 offen offset:3584\nMvaa 128,232,192\nTa 130,12,2048\nMvaa 132,236,192\ns_add_u32 m0, s48, 32768\ns_add_u32 s45, s45, 128\n"
"Maaa 0,240,192\nTa 132,13,0\nMaaa 4,244,192\nD0 2,20,45,0\nMaaa 64,248,192\nTa 134,13,2048\nMaaa 68,252,192\nMaaa 76,252,196\nTa 136,14,0\nMaaa 72,248,196\nMaaa 12,244,196\n"
"Ta 138,14,2048\nMaaa 8,240,196\nMvaa 140,236,196\nTa 140,15,0\nMvaa 136,232,196\nD0 3,20,45,1024\nMvaa 76,228,196\nTa 142,15,2048\nMvaa 72,224,196\nMvaa 80,224,200\nTa 144,16,0\n"
"Mvaa 84,228,200\nMvaa 144,232,200\nTa 146,16,2048\nMvaa 148,236,200\nMaaa 16,240,200\nTa 148,17,0\nMaaa 20,244,200\nD0 4,20,45,2048\nMaaa 80,248,200\nTa 150,17,2048\n"
"Maaa 84,252,200\nMaaa 92,252,204\nTa 152,18,0\nMaaa 88,248,204\nMaaa 28,244,204\nTa 154,18,2048\nMaaa 24,240,204\nMvaa 156,236,204\nTa 156,19,0\nMvaa 152,232,204\nD0 5,20,45,3072\n"
"Mvaa 92,228,204\nTa 158,19,2048\nMvaa 88,224,204\nMvaa 96,224,208\nRa 160,10,0\nMvaa 100,228,208\nMvaa 160,232,208\nRa 164,10,2048\nMvaa 164,236,208\ns_add_u32 m0, s48, 36864\n"
"s_add_u32 s46, s45, 0x10000\nMaaa 32,240,208\nRa 168,10,8192\nMaaa 36,244,208\nD0 2,20,46,0\nMaaa 96,248,208\nRa 172,10,10240\nMaaa 100,252,208\nMaaa 108,252,212\nRa 176,10,16384\n"
"Maaa 104,248,212\nMaaa 44,244,212\nRa 180,10,18432\nMaaa 40,240,212\nMvaa 172,236,212\nRa 184,10,24576\nMvaa 168,232,212\nD0 3,20,46,1024\nMvaa 108,228,212\nRa 188,10,26624\n"
"Mvaa 104,224,212\nMvaa 112,224,216\nMvaa 116,228,216\nMvaa 176,232,216\nMvaa 180,236,216\nMaaa 48,240,216\nMaaa 52,244,216\nD0 4,20,46,2048\nMaaa 112,248,216\nMaaa 116,252,216\n"
"Maaa 124,252,220\nMaaa 120,248,220\nMaaa 60,244,220\nMaaa 56,240,220\nMvaa 188,236,220\nMvaa 184,232,220\nD0 5,20,46,3072\nMvaa 124,228,220\nMvaa 120,224,220\n"
"s_waitcnt vmcnt(13) lgkmcnt(0)\ns_barrier\nMvaa 64,160,128\nTa 192,12,16384\nMvaa 68,164,128\nbuffer_load_dwordx4 v[212:215], v21, s[40:43], s86 offen offset:3584\n"
"Mvaa 128,168,128\nTa 194,12,18432\nMvaa 132,172,128\ns_add_u32 m0, s47, 49152\ns_add_u32 s44, s44, 0x40000\nMaaa 0,176,128\nTa 196,13,16384\nMaaa 4,180,128\nD0 6,24,44,0\n"
"Maaa 64,184,128\nTa 198,13,18432\nMaaa 68,188,128\nv_xor_b32 v48, s15, v33\nMaaa 76,188,132\nTa 200,14,16384\nMaaa 72,184,132\nv_xor_b32 v49, s15, v35\nMaaa 12,180,132\n"
"Ta 202,14,18432\nMaaa 8,176,132\nv_xor_b32 v50, s15, v37\nMvaa 140,172,132\nTa 204,15,16384\nMvaa 136,168,132\nD0 7,24,44,1024\nMvaa 76,164,132\nTa 206,15,18432\nMvaa 72,160,132\n"
"v_xor_b32 v51, s15, v39\nMvaa 80,160,136\nTa 208,16,16384\nMvaa 84,164,136\nv_xor_b32 v52, s15, v41\nMvaa 144,168,136\nTa 210,16,18432\nMvaa 148,172,136\nv_xor_b32 v53, s15, v43\n"
"Maaa 16,176,136\nTa 212,17,16384\nMaaa 20,180,136\nD0 8,24,44,2048\nMaaa 80,184,136\nTa 214,17,18432\nMaaa 84,188,136\nv_xor_b32 v54, s15, v45\nMaaa 92,188,140\nTa 216,18,16384\n"
"Maaa 88,184,140\nv_xor_b32 v55, s15, v47\nMaaa 28,180,140\nTa 218,18,18432\nMaaa 24,176,140\nv_or3_b32 v48, v48, v49, v50\nMvaa 156,172,140\nTa 220,19,16384\nMvaa 152,168,140\n"
"D0 9,24,44,3072\nMvaa 92,164,140\nTa 222,19,18432\nMvaa 88,160,140\nv_or3_b32 v51, v51, v52, v53\nMvaa 96,160,144\nRa 224,11,0\nMvaa 100,164,144\nv_or3_b32 v48, v48, v54, v55\n"
"Mvaa 160,168,144\nRa 228,11,2048\nMvaa 164,172,144\ns_add_u32 m0, s47, 0\ns_add_u32 s44, s44, 0x40000\nMaaa 32,176,144\nRa 232,11,8192\nMaaa 36,180,144\nD0 6,24,44,0\n"
"Maaa 96,184,144\nRa 236,11,10240\nMaaa 100,188,144\nv_or_b32 v48, v48, v51\nMaaa 108,188,148\nRa 240,11,16384\nMaaa 104,184,148\nv_cmp_ne_u32 vcc, 0, v48\nMaaa 44,180,148\n"
"Ra 244,11,18432\nMaaa 40,176,148\ns_cbranch_vccnz .S132\n.S133:\nMvaa 172,172,148\nRa 248,11,24576\nMvaa 168,168,148\nD0 7,24,44,1024\nMvaa 108,164,148\nRa 252,11,26624\n"
"Mvaa 104,160,148\nv_max_u32 v48, v32, v34\nMvaa 112,160,152\nv_min_u32 v49, v32, v34\nMvaa 116,164,152\nv_med3_u32 v49, v48, v49, v36\nMvaa 176,168,152\nv_max_u32 v48, v48, v36\n"
"Mvaa 180,172,152\nv_med3_u32 v49, v48, v49, v38\nMaaa 48,176,152\nv_max_u32 v48, v48, v38\nMaaa 52,180,152\nD0 8,24,44,2048\nMaaa 112,184,152\nv_med3_u32 v49, v48, v49, v40\n"
"Maaa 116,188,152\nv_max_u32 v48, v48, v40\nMaaa 124,188,156\nv_med3_u32 v49, v48, v49, v42\nMaaa 120,184,156\nv_max_u32 v48, v48, v42\nMaaa 60,180,156\n"
"v_med3_u32 v49, v48, v49, v44\nMaaa 56,176,156\nv_max_u32 v48, v48, v44\nMvaa 188,172,156\nv_med3_u32 v49, v48, v49, v46\nMvaa 184,168,156\nD0 9,24,44,3072\nMvaa 124,164,156\n"
"v_max_u32 v48, v48, v46\nMvaa 120,160,156\ns_waitcnt vmcnt(9) lgkmcnt(0)\ns_barrier\nMvaa 64,224,192\nTa 128,12,32768\nMvaa 68,228,192\n"
"buffer_load_dwordx4 v[200:203], v21, s[36:39], s87 offen offset:0\nMvaa 128,232,192\nTa 130,12,34816\nMvaa 132,236,192\ns_add_u32 m0, s48, 0\ns_add_u32 s45, s45, 128\n"
"Maaa 0,240,192\nTa 132,13,32768\nMaaa 4,244,192\nD0 2,20,45,0\nMaaa 64,248,192\nTa 134,13,34816\nMaaa 68,252,192\nv_mov_b32 v50, v48\nMaaa 76,252,196\nTa 136,14,32768\n"
"Maaa 72,248,196\nv_max_u32_dpp v50, v50, v50 row_ror:8 row_mask:0xf bank_mask:0xf\nMaaa 12,244,196\nTa 138,14,34816\nMaaa 8,240,196\n"
"v_max_u32_dpp v50, v50, v50 row_ror:4 row_mask:0xf bank_mask:0xf\nMvaa 140,236,196\nTa 140,15,32768\nMvaa 136,232,196\nD0 3,20,45,1024\nMvaa 76,228,196\nTa 142,15,34816\n"
"Mvaa 72,224,196\nv_max_u32_dpp v50, v50, v50 row_ror:2 row_mask:0xf bank_mask:0xf\nMvaa 80,224,200\nTa 144,16,32768\nMvaa 84,228,200\n"
"v_max_u32_dpp v50, v50, v50 row_ror:1 row_mask:0xf bank_mask:0xf\nMvaa 144,232,200\nTa 146,16,34816\nMvaa 148,236,200\nv_mov_b32 v52, v50\nMaaa 16,240,200\nTa 148,17,32768\n"
"Maaa 20,244,200\nD0 4,20,45,2048\nMaaa 80,248,200\nTa 150,17,34816\nMaaa 84,252,200\nv_permlane16_swap_b32 v50, v52\nMaaa 92,252,204\nTa 152,18,32768\nMaaa 88,248,204\n"
"v_max_u32 v50, v50, v52\nMaaa 28,244,204\nTa 154,18,34816\nMaaa 24,240,204\nv_mov_b32 v52, v50\nMvaa 156,236,204\nTa 156,19,32768\nMvaa 152,232,204\nD0 5,20,45,3072\n"
"Mvaa 92,228,204\nTa 158,19,34816\nMvaa 88,224,204\nv_permlane32_swap_b32 v50, v52\nMvaa 96,224,208\nRa 160,10,32768\nMvaa 100,228,208\nv_max_u32 v50, v50, v52\n"
"v_cmp_eq_u32 vcc, v48, v50\ns_lshl_b32 s2, s11, 3\ns_add_u32 s2, s2, 131072\nv_cndmask_b32 v51, v48, v49, vcc\nMvaa 160,232,208\nRa 164,10,34816\nMvaa 164,236,208\n"
"s_add_u32 m0, s48, 4096\ns_add_u32 s46, s45, 0x10000\nMaaa 32,240,208\nRa 168,10,40960\nMaaa 36,244,208\nD0 2,20,46,0\nMaaa 96,248,208\nRa 172,10,43008\nMaaa 100,252,208\n"
"v_max_u32_dpp v51, v51, v51 row_ror:8 row_mask:0xf bank_mask:0xf\nMaaa 108,252,212\nRa 176,10,49152\nMaaa 104,248,212\n"
"v_max_u32_dpp v51, v51, v51 row_ror:4 row_mask:0xf bank_mask:0xf\nMaaa 44,244,212\nRa 180,10,51200\nMaaa 40,240,212\n"
"v_max_u32_dpp v51, v51, v51 row_ror:2 row_mask:0xf bank_mask:0xf\nMvaa 172,236,212\nRa 184,10,57344\nMvaa 168,232,212\nD0 3,20,46,1024\nMvaa 108,228,212\nRa 188,10,59392\n"
"Mvaa 104,224,212\nv_max_u32_dpp v51, v51, v51 row_ror:1 row_mask:0xf bank_mask:0xf\nMvaa 112,224,216\nv_mov_b32 v52, v51\nMvaa 116,228,216\nMvaa 176,232,216\n"
"v_permlane16_swap_b32 v51, v52\nMvaa 180,236,216\nv_max_u32 v51, v51, v52\nMaaa 48,240,216\nv_mov_b32 v52, v51\nMaaa 52,244,216\nD0 4,20,46,2048\nMaaa 112,248,216\n"
"v_permlane32_swap_b32 v51, v52\nMaaa 116,252,216\nv_max_u32 v51, v51, v52\nMaaa 124,252,220\nv_mov_b32 v52, s2\nMaaa 120,248,220\nds_write_b64 v52, v[50:51]\nMaaa 60,244,220\n"
"v_mov_b32 v52, 131072\nMaaa 56,240,220\nMvaa 188,236,220\nMvaa 184,232,220\nD0 5,20,46,3072\nMvaa 124,228,220\nMvaa 120,224,220\ns_waitcnt vmcnt(13) lgkmcnt(0)\ns_barrier\n"
"Mvaa 64,160,128\nTa 192,12,49152\nMvaa 68,164,128\nbuffer_load_dwordx4 v[216:219], v21, s[40:43], s87 offen offset:0\nMvaa 128,168,128\nTa 194,12,51200\nMvaa 132,172,128\n"
"s_add_u32 m0, s47, 16384\ns_add_u32 s44, s44, 0x40000\nMaaa 0,176,128\nTa 196,13,49152\nMaaa 4,180,128\nD0 6,24,44,0\nMaaa 64,184,128\nTa 198,13,51200\nMaaa 68,188,128\n"
"Rv 32,52,0\nMaaa 76,188,132\nTa 200,14,49152\nMaaa 72,184,132\nRv 36,52,16\nMaaa 12,180,132\nTa 202,14,51200\nMaaa 8,176,132\nMvaa 140,172,132\nTa 204,15,49152\nMvaa 136,168,132\n"
"D0 7,24,44,1024\nMvaa 76,164,132\nTa 206,15,51200\nMvaa 72,160,132\nMvaa 80,160,136\nTa 208,16,49152\nMvaa 84,164,136\nMvaa 144,168,136\nTa 210,16,51200\nMvaa 148,172,136\n"
"Maaa 16,176,136\nTa 212,17,49152\nMaaa 20,180,136\nD0 8,24,44,2048\nMaaa 80,184,136\nTa 214,17,51200\nMaaa 84,188,136\nWL 7\nMaaa 92,188,140\nTa 216,18,49152\nMaaa 88,184,140\n"
"v_max_u32 v48, v32, v33\nMaaa 28,180,140\nTa 218,18,51200\nMaaa 24,176,140\nv_min_u32 v49, v32, v33\nMvaa 156,172,140\nTa 220,19,49152\nMvaa 152,168,140\nD0 9,24,44,3072\n"
"Mvaa 92,164,140\nTa 222,19,51200\nMvaa 88,160,140\nv_med3_u32 v49, v48, v49, v34\nMvaa 96,160,144\nRa 224,11,32768\nMvaa 100,164,144\nv_max_u32 v48, v48, v34\nMvaa 160,168,144\n"
"Ra 228,11,34816\nMvaa 164,172,144\ns_add_u32 m0, s47, 32768\ns_add_u32 s44, s44, 0x40000\nMaaa 32,176,144\nRa 232,11,40960\nMaaa 36,180,144\nD0 6,24,44,0\nMaaa 96,184,144\n"
"Ra 236,11,43008\nMaaa 100,188,144\nv_med3_u32 v49, v48, v49, v35\nMaaa 108,188,148\nRa 240,11,49152\nMaaa 104,184,148\nv_max_u32 v48, v48, v35\nMaaa 44,180,148\nRa 244,11,51200\n"
"Maaa 40,176,148\nv_med3_u32 v49, v48, v49, v36\nMvaa 172,172,148\nRa 248,11,57344\nMvaa 168,168,148\nD0 7,24,44,1024\nMvaa 108,164,148\nRa 252,11,59392\nMvaa 104,160,148\n"
"v_max_u32 v48, v48, v36\nMvaa 112,160,152\nv_med3_u32 v49, v48, v49, v37\nMvaa 116,164,152\nv_max_u32 v48, v48, v37\nMvaa 176,168,152\nv_med3_u32 v49, v48, v49, v38\n"
"Mvaa 180,172,152\nv_max_u32 v48, v48, v38\nMaaa 48,176,152\nv_med3_u32 v49, v48, v49, v39\nMaaa 52,180,152\nD0 8,24,44,2048\nMaaa 112,184,152\nv_max_u32 v48, v48, v39\n"
"Maaa 116,188,152\nv_readfirstlane_b32 s2, v48\nMaaa 124,188,156\nv_readfirstlane_b32 s3, v49\nMaaa 120,184,156\nMaaa 60,180,156\nMaaa 56,176,156\ns_andn2_b32 s2, 0x7ff, s2\n"
"Mvaa 188,172,156\ns_andn2_b32 s3, 0x7ff, s3\nMvaa 184,168,156\nD0 9,24,44,3072\nMvaa 124,164,156\ns_lshl_b32 s71, s3, 16\nMvaa 120,160,156\ns_waitcnt vmcnt(9) lgkmcnt(0)\n"
"s_barrier\nMvaa 64,224,192\nTa 128,12,0\nMvaa 68,228,192\nbuffer_load_dwordx4 v[204:207], v21, s[36:39], s87 offen offset:3584\nMvaa 128,232,192\nTa 130,12,2048\nMvaa 132,236,192\n"
"s_add_u32 m0, s48, 32768\ns_add_u32 s45, s45, 128\nMaaa 0,240,192\nTa 132,13,0\nMaaa 4,244,192\nD0 2,20,45,0\nMaaa 64,248,192\nTa 134,13,2048\nMaaa 68,252,192\n"
"s_or_b32 s71, s71, s2\nMaaa 76,252,196\nTa 136,14,0\nMaaa 72,248,196\ns_mul_i32 s2, s2, 896\nMaaa 12,244,196\nTa 138,14,2048\nMaaa 8,240,196\ns_mul_i32 s3, s3, 896\n"
"Mvaa 140,236,196\nTa 140,15,0\nMvaa 136,232,196\nD0 3,20,45,1024\nMvaa 76,228,196\nTa 142,15,2048\nMvaa 72,224,196\ns_mul_i32 s6, s11, 224\nMvaa 80,224,200\nTa 144,16,0\n"
"Mvaa 84,228,200\ns_add_u32 s2, s2, s6\nMvaa 144,232,200\nTa 146,16,2048\nMvaa 148,236,200\ns_add_u32 s3, s3, s6\nMaaa 16,240,200\nTa 148,17,0\nMaaa 20,244,200\nD0 4,20,45,2048\n"
"Maaa 80,248,200\nTa 150,17,2048\nMaaa 84,252,200\nbuffer_load_dwordx2 v[240:241], v255, s[28:31], s2 offen\nMaaa 92,252,204\nTa 152,18,0\nMaaa 88,248,204\nMaaa 28,244,204\n"
"Ta 154,18,2048\nMaaa 24,240,204\nbuffer_load_dwordx2 v[242:243], v255, s[28:31], s3 offen\ns_lshl2_add_u32 s2, s10, s11\ns_cmp_eq_u32 s2, 512\ns_cselect_b32 s2, 0, 0x40000000\n"
"s_add_u32 s2, s2, 0xc048000\nMvaa 156,236,204\nTa 156,19,0\nMvaa 152,232,204\nD0 5,20,45,3072\nMvaa 92,228,204\nTa 158,19,2048\nMvaa 88,224,204\n"
"buffer_store_dword v25, off, s[16:19], s2 offset:0 sc0 sc1\nMvaa 96,224,208\nRa 160,10,0\nMvaa 100,228,208\nMvaa 160,232,208\nRa 164,10,2048\nMvaa 164,236,208\n"
"s_add_u32 m0, s48, 36864\ns_add_u32 s46, s45, 0x10000\nMaaa 32,240,208\nRa 168,10,8192\nMaaa 36,244,208\nD0 2,20,46,0\nMaaa 96,248,208\nRa 172,10,10240\nMaaa 100,252,208\n"
"Maaa 108,252,212\nRa 176,10,16384\nMaaa 104,248,212\nMaaa 44,244,212\nRa 180,10,18432\nMaaa 40,240,212\nMvaa 172,236,212\nRa 184,10,24576\nMvaa 168,232,212\nD0 3,20,46,1024\n"
"Mvaa 108,228,212\nRa 188,10,26624\nMvaa 104,224,212\nMvaa 112,224,216\nMvaa 116,228,216\nMvaa 176,232,216\nMvaa 180,236,216\nMaaa 48,240,216\nMaaa 52,244,216\nD0 4,20,46,2048\n"
"Maaa 112,248,216\nMaaa 116,252,216\nMaaa 124,252,220\nMaaa 120,248,220\nMaaa 60,244,220\nMaaa 56,240,220\nMvaa 188,236,220\nMvaa 184,232,220\nD0 5,20,46,3072\nMvaa 124,228,220\n"
"Mvaa 120,224,220\ns_waitcnt vmcnt(16) lgkmcnt(0)\ns_barrier\nMvaa 64,160,128\nTa 192,12,16384\nMvaa 68,164,128\n"
"buffer_load_dwordx4 v[220:223], v21, s[40:43], s87 offen offset:3584\nMvaa 128,168,128\nTa 194,12,18432\nMvaa 132,172,128\ns_add_u32 m0, s47, 49152\ns_add_u32 s44, s44, 0x40000\n"
"Maaa 0,176,128\nTa 196,13,16384\nMaaa 4,180,128\nD0 6,24,44,0\nMaaa 64,184,128\nTa 198,13,18432\nMaaa 68,188,128\nMaaa 76,188,132\nTa 200,14,16384\nMaaa 72,184,132\n"
"Maaa 12,180,132\nTa 202,14,18432\nMaaa 8,176,132\nMvaa 140,172,132\nTa 204,15,16384\nMvaa 136,168,132\nD0 7,24,44,1024\nMvaa 76,164,132\nTa 206,15,18432\nMvaa 72,160,132\n"
"Mvaa 80,160,136\nTa 208,16,16384\nMvaa 84,164,136\nMvaa 144,168,136\nTa 210,16,18432\nMvaa 148,172,136\nMaaa 16,176,136\nTa 212,17,16384\nMaaa 20,180,136\nD0 8,24,44,2048\n"
"Maaa 80,184,136\nTa 214,17,18432\nMaaa 84,188,136\nMaaa 92,188,140\nTa 216,18,16384\nMaaa 88,184,140\nMaaa 28,180,140\nTa 218,18,18432\nMaaa 24,176,140\nMvaa 156,172,140\n"
"Ta 220,19,16384\nMvaa 152,168,140\nD0 9,24,44,3072\nMvaa 92,164,140\nTa 222,19,18432\nMvaa 88,160,140\nMvaa 96,160,144\nRa 224,11,0\nMvaa 100,164,144\ns_lshl_b32 s5, s73, 8\n"
"s_add_u32 s5, s5, 0xc04ac00\nv_lshrrev_b32 v27, 2, v1\nbuffer_load_dword v27, v27, s[16:19], s5 offen sc0 sc1\nMvaa 160,168,144\nRa 228,11,2048\nMvaa 164,172,144\n"
"s_add_u32 m0, s47, 0\ns_add_u32 s44, s44, 0x40000\nMaaa 32,176,144\nRa 232,11,8192\nMaaa 36,180,144\nD0 6,24,44,0\nMaaa 96,184,144\nRa 236,11,10240\nMaaa 100,188,144\n"
"Maaa 108,188,148\nRa 240,11,16384\nMaaa 104,184,148\nMaaa 44,180,148\nRa 244,11,18432\nMaaa 40,176,148\nMvaa 172,172,148\nRa 248,11,24576\nMvaa 168,168,148\nD0 7,24,44,1024\n"
"Mvaa 108,164,148\nRa 252,11,26624\nMvaa 104,160,148\nMvaa 112,160,152\nMvaa 116,164,152\nMvaa 176,168,152\nMvaa 180,172,152\nMaaa 48,176,152\nMaaa 52,180,152\nD0 8,24,44,2048\n"
"Maaa 112,184,152\nMaaa 116,188,152\nMaaa 124,188,156\nMaaa 120,184,156\nMaaa 60,180,156\nMaaa 56,176,156\nMvaa 188,172,156\nMvaa 184,168,156\nD0 9,24,44,3072\nMvaa 124,164,156\n"
"Mvaa 120,160,156\ns_waitcnt vmcnt(10) lgkmcnt(0)\ns_barrier\nMvaa 64,224,192\nTa 128,12,32768\nMvaa 68,228,192\nbuffer_load_dwordx4 v[224:227], v20, s[92:95], s88 offen offset:0\n"
"Mvaa 128,232,192\nTa 130,12,34816\nv_lshlrev_b32 v244, 16, v240\nMvaa 132,236,192\ns_add_u32 m0, s48, 0\ns_add_u32 s45, s45, 128\nMaaa 0,240,192\nTa 132,13,32768\n"
"v_and_b32 v245, 0xffff0000, v240\nMaaa 4,244,192\nD0 2,20,45,0\nv_lshlrev_b32 v246, 16, v241\nMaaa 64,248,192\nTa 134,13,34816\nMaaa 68,252,192\nv_and_b32 v247, 0xffff0000, v241\n"
"v_lshlrev_b32 v248, 16, v242\nMaaa 76,252,196\nTa 136,14,32768\nv_and_b32 v249, 0xffff0000, v242\nMaaa 72,248,196\nv_lshlrev_b32 v250, 16, v243\nv_and_b32 v251, 0xffff0000, v243\n"
"Maaa 12,244,196\nTa 138,14,34816\nv_lshlrev_b32 v252, 16, v192\nMaaa 8,240,196\nv_and_b32 v253, 0xffff0000, v192\nMvaa 140,236,196\nTa 140,15,32768\nMU 32,244,252\n"
"Mvaa 136,232,196\nD0 3,20,45,1024\nMU 33,244,253\nMvaa 76,228,196\nTa 142,15,34816\nMU 40,248,252\nMvaa 72,224,196\nMU 41,248,253\nv_lshlrev_b32 v252, 16, v193\nMvaa 80,224,200\n"
"Ta 144,16,32768\nv_and_b32 v253, 0xffff0000, v193\nMvaa 84,228,200\nMU 34,244,252\nMvaa 144,232,200\nTa 146,16,34816\nMU 35,244,253\nMvaa 148,236,200\nMU 42,248,252\n"
"MU 43,248,253\nMaaa 16,240,200\nTa 148,17,32768\nv_lshlrev_b32 v252, 16, v194\nMaaa 20,244,200\nD0 4,20,45,2048\nv_and_b32 v253, 0xffff0000, v194\nMaaa 80,248,200\n"
"Ta 150,17,34816\nMaaa 84,252,200\nMU 36,244,252\nMU 37,244,253\nMaaa 92,252,204\nTa 152,18,32768\nMU 44,248,252\nMaaa 88,248,204\nMU 45,248,253\nv_lshlrev_b32 v252, 16, v195\n"
"Maaa 28,244,204\nTa 154,18,34816\nv_and_b32 v253, 0xffff0000, v195\nMaaa 24,240,204\nMU 38,244,252\nMvaa 156,236,204\nTa 156,19,32768\nMU 39,244,253\nMvaa 152,232,204\n"
"D0 5,20,45,3072\nMU 46,248,252\nMvaa 92,228,204\nTa 158,19,34816\nMU 47,248,253\nMvaa 88,224,204\nv_lshlrev_b32 v252, 16, v208\nv_and_b32 v253, 0xffff0000, v208\nMvaa 96,224,208\n"
"Ra 160,10,32768\nMU 48,244,252\nMvaa 100,228,208\nbuffer_load_dwordx4 v[228:231], v20, s[92:95], s88 offen offset:896\nMvaa 160,232,208\nRa 164,10,34816\nMU 49,244,253\n"
"Mvaa 164,236,208\ns_add_u32 m0, s48, 4096\ns_add_u32 s46, s45, 0x10000\nMaaa 32,240,208\nRa 168,10,40960\nMU 56,248,252\nMaaa 36,244,208\nD0 2,20,46,0\nMU 57,248,253\n"
"Maaa 96,248,208\nRa 172,10,43008\nMaaa 100,252,208\nv_lshlrev_b32 v252, 16, v209\nv_and_b32 v253, 0xffff0000, v209\nMaaa 108,252,212\nRa 176,10,49152\nMU 50,244,252\n"
"Maaa 104,248,212\nMU 51,244,253\nMU 58,248,252\nMaaa 44,244,212\nRa 180,10,51200\nMU 59,248,253\nMaaa 40,240,212\nv_lshlrev_b32 v252, 16, v210\nMvaa 172,236,212\nRa 184,10,57344\n"
"v_and_b32 v253, 0xffff0000, v210\nMvaa 168,232,212\nD0 3,20,46,1024\nMU 52,244,252\nMvaa 108,228,212\nRa 188,10,59392\nMU 53,244,253\nMvaa 104,224,212\nMU 60,248,252\n"
"MU 61,248,253\nMvaa 112,224,216\nv_lshlrev_b32 v252, 16, v211\nv_and_b32 v253, 0xffff0000, v211\nMvaa 116,228,216\nMU 54,244,252\nMvaa 176,232,216\nMU 55,244,253\nMU 62,248,252\n"
"Mvaa 180,236,216\nMU 63,248,253\nv_lshlrev_b32 v252, 16, v196\nMaaa 48,240,216\nv_and_b32 v253, 0xffff0000, v196\nFM 32,245,252\nMaaa 52,244,216\nD0 4,20,46,2048\nFM 33,245,253\n"
"Maaa 112,248,216\nFM 40,249,252\nMaaa 116,252,216\nFM 41,249,253\nv_lshlrev_b32 v252, 16, v197\nMaaa 124,252,220\nv_and_b32 v253, 0xffff0000, v197\nFM 34,245,252\n"
"Maaa 120,248,220\nFM 35,245,253\nFM 42,249,252\nMaaa 60,244,220\nFM 43,249,253\nv_lshlrev_b32 v252, 16, v198\nMaaa 56,240,220\nv_and_b32 v253, 0xffff0000, v198\nMvaa 188,236,220\n"
"FM 36,245,252\nFM 37,245,253\nMvaa 184,232,220\nD0 5,20,46,3072\nFM 44,249,252\nMvaa 124,228,220\nFM 45,249,253\nv_lshlrev_b32 v252, 16, v199\nMvaa 120,224,220\n"
"s_waitcnt vmcnt(15) lgkmcnt(0)\ns_barrier\nMvaa 64,160,128\nTa 192,12,49152\nv_and_b32 v253, 0xffff0000, v199\nMvaa 68,164,128\n"
"buffer_load_dwordx4 v[232:235], v20, s[92:95], s88 offen offset:1792\nMvaa 128,168,128\nTa 194,12,51200\nFM 38,245,252\nMvaa 132,172,128\ns_add_u32 m0, s47, 16384\n"
"s_add_u32 s44, s44, 0x40000\nMaaa 0,176,128\nTa 196,13,49152\nFM 39,245,253\nMaaa 4,180,128\nD0 6,24,44,0\nFM 46,249,252\nMaaa 64,184,128\nTa 198,13,51200\nMaaa 68,188,128\n"
"FM 47,249,253\nv_lshlrev_b32 v252, 16, v212\nMaaa 76,188,132\nTa 200,14,49152\nv_and_b32 v253, 0xffff0000, v212\nMaaa 72,184,132\nFM 48,245,252\nFM 49,245,253\nMaaa 12,180,132\n"
"Ta 202,14,51200\nFM 56,249,252\nMaaa 8,176,132\nFM 57,249,253\nMvaa 140,172,132\nTa 204,15,49152\nv_lshlrev_b32 v252, 16, v213\nMvaa 136,168,132\nD0 7,24,44,1024\n"
"v_and_b32 v253, 0xffff0000, v213\nMvaa 76,164,132\nTa 206,15,51200\nFM 50,245,252\nMvaa 72,160,132\nFM 51,245,253\nFM 58,249,252\nMvaa 80,160,136\nTa 208,16,49152\nFM 59,249,253\n"
"Mvaa 84,164,136\nv_lshlrev_b32 v252, 16, v214\nMvaa 144,168,136\nTa 210,16,51200\nv_and_b32 v253, 0xffff0000, v214\nMvaa 148,172,136\nFM 52,245,252\nFM 53,245,253\n"
"Maaa 16,176,136\nTa 212,17,49152\nFM 60,249,252\nMaaa 20,180,136\nD0 8,24,44,2048\nFM 61,249,253\nMaaa 80,184,136\nTa 214,17,51200\nMaaa 84,188,136\nv_lshlrev_b32 v252, 16, v215\n"
"v_and_b32 v253, 0xffff0000, v215\nMaaa 92,188,140\nTa 216,18,49152\nFM 54,245,252\nMaaa 88,184,140\nFM 55,245,253\nFM 62,249,252\nMaaa 28,180,140\nTa 218,18,51200\nFM 63,249,253\n"
"Maaa 24,176,140\nv_lshlrev_b32 v252, 16, v200\nMvaa 156,172,140\nTa 220,19,49152\nv_and_b32 v253, 0xffff0000, v200\nMvaa 152,168,140\nD0 9,24,44,3072\nFM 32,246,252\n"
"Mvaa 92,164,140\nTa 222,19,51200\nFM 33,246,253\nMvaa 88,160,140\nFM 40,250,252\nFM 41,250,253\nMvaa 96,160,144\nRa 224,11,32768\nv_lshlrev_b32 v252, 16, v201\nMvaa 100,164,144\n"
"buffer_load_dwordx4 v[236:239], v20, s[92:95], s88 offen offset:2688\nMvaa 160,168,144\nRa 228,11,34816\nv_and_b32 v253, 0xffff0000, v201\nMvaa 164,172,144\n"
"s_add_u32 m0, s47, 32768\ns_add_u32 s44, s44, 0x40000\nMaaa 32,176,144\nRa 232,11,40960\nFM 34,246,252\nMaaa 36,180,144\nD0 6,24,44,0\nFM 35,246,253\nMaaa 96,184,144\n"
"Ra 236,11,43008\nMaaa 100,188,144\nFM 42,250,252\nFM 43,250,253\nMaaa 108,188,148\nRa 240,11,49152\nv_lshlrev_b32 v252, 16, v202\nMaaa 104,184,148\n"
"v_and_b32 v253, 0xffff0000, v202\nFM 36,246,252\nMaaa 44,180,148\nRa 244,11,51200\nFM 37,246,253\nMaaa 40,176,148\nFM 44,250,252\nMvaa 172,172,148\nRa 248,11,57344\nFM 45,250,253\n"
"Mvaa 168,168,148\nD0 7,24,44,1024\nv_lshlrev_b32 v252, 16, v203\nMvaa 108,164,148\nRa 252,11,59392\nv_and_b32 v253, 0xffff0000, v203\nMvaa 104,160,148\nFM 38,246,252\n"
"FM 39,246,253\nMvaa 112,160,152\nFM 46,250,252\nFM 47,250,253\nMvaa 116,164,152\nv_lshlrev_b32 v252, 16, v216\nMvaa 176,168,152\nv_and_b32 v253, 0xffff0000, v216\nFM 48,246,252\n"
"Mvaa 180,172,152\nFM 49,246,253\nFM 56,250,252\nMaaa 48,176,152\nFM 57,250,253\nv_lshlrev_b32 v252, 16, v217\nMaaa 52,180,152\nD0 8,24,44,2048\nv_and_b32 v253, 0xffff0000, v217\n"
"Maaa 112,184,152\nFM 50,246,252\nMaaa 116,188,152\nFM 51,246,253\nFM 58,250,252\nMaaa 124,188,156\nFM 59,250,253\nv_lshlrev_b32 v252, 16, v218\nMaaa 120,184,156\n"
"v_and_b32 v253, 0xffff0000, v218\nFM 52,246,252\nMaaa 60,180,156\nFM 53,246,253\nFM 60,250,252\nMaaa 56,176,156\nFM 61,250,253\nMvaa 188,172,156\nv_lshlrev_b32 v252, 16, v219\n"
"v_and_b32 v253, 0xffff0000, v219\nMvaa 184,168,156\nD0 9,24,44,3072\nFM 54,246,252\nMvaa 124,164,156\nWV 24\nv_xor_b32 v27, s15, v27\nv_and_b32 v255, 0xffffdfff, v27\n"
"v_cmp_ne_u32 vcc, 0, v255\ns_nop 0\ns_cbranch_vccnz .S135\n.S136:\nv_cmp_ne_u32 vcc, 0, v27\ns_nop 3\ns_bcnt1_i32_b64 s4, vcc\ns_cmp_gt_u32 s4, 32\ns_cselect_b32 s4, 1, 0\n"
"Mvaa 120,160,156\ns_waitcnt vmcnt(10) lgkmcnt(0)\ns_barrier\nMvaa 64,224,192\nTa 128,12,0\ns_xor_b32 s4, s4, s12\nMvaa 68,228,192\ns_and_b32 s4, s4, 1\nMvaa 128,232,192\n"
"Ta 130,12,2048\ns_lshl_b32 s4, s4, 13\nMvaa 132,236,192\ns_add_u32 m0, s48, 32768\ns_add_u32 s45, s45, 128\nMaaa 0,240,192\nTa 132,13,0\ns_mul_i32 s72, s73, 25165824\n"
"Maaa 4,244,192\nD0 2,20,45,0\ns_add_u32 s72, s72, s4\nMaaa 64,248,192\nTa 134,13,2048\nMaaa 68,252,192\ns_and_b32 s4, s12, 1\ns_lshl_b32 s4, s4, 21\nMaaa 76,252,196\nTa 136,14,0\n"
"s_add_u32 s72, s72, s4\nMaaa 72,248,196\ns_lshl3_add_u32 s2, s13, s11\ns_lshl_b32 s2, s2, 11\nMaaa 12,244,196\nTa 138,14,2048\ns_lshl_b32 s3, s14, 22\nMaaa 8,240,196\n"
"s_add_u32 s2, s2, s3\nMvaa 140,236,196\nTa 140,15,0\ns_add_u32 s2, s2, s72\nMvaa 136,232,196\nD0 3,20,45,1024\ns_add_u32 s49, s2, 0xc00000\nMvaa 76,228,196\nTa 142,15,2048\n"
"FM 55,246,253\nMvaa 72,224,196\nFM 62,250,252\nFM 63,250,253\nMvaa 80,224,200\nTa 144,16,0\nv_lshlrev_b32 v252, 16, v204\nMvaa 84,228,200\nv_and_b32 v253, 0xffff0000, v204\n"
"Mvaa 144,232,200\nTa 146,16,2048\nFM 32,247,252\nMvaa 148,236,200\nFM 33,247,253\nFM 40,251,252\nMaaa 16,240,200\nTa 148,17,0\nFM 41,251,253\nMaaa 20,244,200\nD0 4,20,45,2048\n"
"v_lshlrev_b32 v252, 16, v205\nMaaa 80,248,200\nTa 150,17,2048\nMaaa 84,252,200\nv_and_b32 v253, 0xffff0000, v205\nFM 34,247,252\nMaaa 92,252,204\nTa 152,18,0\nFM 35,247,253\n"
"Maaa 88,248,204\nFM 42,251,252\nFM 43,251,253\nMaaa 28,244,204\nTa 154,18,2048\nv_lshlrev_b32 v252, 16, v206\nMaaa 24,240,204\nv_and_b32 v253, 0xffff0000, v206\nMvaa 156,236,204\n"
"Ta 156,19,0\nFM 36,247,252\nMvaa 152,232,204\nD0 5,20,45,3072\nFM 37,247,253\nMvaa 92,228,204\nTa 158,19,2048\nFM 44,251,252\nMvaa 88,224,204\nFM 45,251,253\n"
"v_lshlrev_b32 v252, 16, v207\nMvaa 96,224,208\nRa 160,10,0\nv_and_b32 v253, 0xffff0000, v207\nMvaa 100,228,208\nFM 38,247,252\nMvaa 160,232,208\nRa 164,10,2048\nFM 39,247,253\n"
"Mvaa 164,236,208\ns_add_u32 m0, s48, 36864\ns_add_u32 s46, s45, 0x10000\nMaaa 32,240,208\nRa 168,10,8192\nFM 46,251,252\nMaaa 36,244,208\nD0 2,20,46,0\nFM 47,251,253\n"
"Maaa 96,248,208\nRa 172,10,10240\nMaaa 100,252,208\nv_lshlrev_b32 v252, 16, v220\nv_and_b32 v253, 0xffff0000, v220\nMaaa 108,252,212\nRa 176,10,16384\nFM 48,247,252\n"
"Maaa 104,248,212\nFM 49,247,253\nFM 56,251,252\nMaaa 44,244,212\nRa 180,10,18432\nFM 57,251,253\nMaaa 40,240,212\nv_lshlrev_b32 v252, 16, v221\nMvaa 172,236,212\nRa 184,10,24576\n"
"v_and_b32 v253, 0xffff0000, v221\nMvaa 168,232,212\nD0 3,20,46,1024\nFM 50,247,252\nMvaa 108,228,212\nRa 188,10,26624\nFM 51,247,253\nMvaa 104,224,212\nFM 58,251,252\n"
"FM 59,251,253\nMvaa 112,224,216\nv_lshlrev_b32 v252, 16, v222\nv_and_b32 v253, 0xffff0000, v222\nMvaa 116,228,216\nFM 52,247,252\nMvaa 176,232,216\nFM 53,247,253\nFM 60,251,252\n"
"Mvaa 180,236,216\nFM 61,251,253\nv_lshlrev_b32 v252, 16, v223\nMaaa 48,240,216\nv_and_b32 v253, 0xffff0000, v223\nFM 54,247,252\nMaaa 52,244,216\nD0 4,20,46,2048\nFM 55,247,253\n"
"Maaa 112,248,216\nFM 62,251,252\nMaaa 116,252,216\nFM 63,251,253\nMaaa 124,252,220\nv_permlane32_swap_b32 v32, v48\nv_permlane32_swap_b32 v33, v49\nMaaa 120,248,220\n"
"v_permlane32_swap_b32 v34, v50\nv_permlane32_swap_b32 v35, v51\nMaaa 60,244,220\nv_permlane32_swap_b32 v36, v52\nv_permlane32_swap_b32 v37, v53\nMaaa 56,240,220\n"
"v_permlane32_swap_b32 v38, v54\nMvaa 188,236,220\nv_permlane32_swap_b32 v39, v55\nv_permlane32_swap_b32 v40, v56\nMvaa 184,232,220\nD0 5,20,46,3072\n"
"v_permlane32_swap_b32 v41, v57\nMvaa 124,228,220\nv_permlane32_swap_b32 v42, v58\nv_permlane32_swap_b32 v43, v59\nMvaa 120,224,220\ns_waitcnt vmcnt(13) lgkmcnt(0)\ns_barrier\n"
"Mvaa 64,160,128\nTa 192,12,16384\nv_permlane32_swap_b32 v44, v60\nMvaa 68,164,128\nv_permlane32_swap_b32 v45, v61\nMvaa 128,168,128\nTa 194,12,18432\n"
"v_permlane32_swap_b32 v46, v62\nMvaa 132,172,128\ns_add_u32 m0, s47, 49152\ns_add_u32 s44, s44, 0x40000\nMaaa 0,176,128\nTa 196,13,16384\nv_permlane32_swap_b32 v47, v63\n"
"Maaa 4,180,128\nD0 6,24,44,0\nAD 32,32,48\nMaaa 64,184,128\nTa 198,13,18432\nMaaa 68,188,128\nAD 33,33,49\nAD 34,34,50\nMaaa 76,188,132\nTa 200,14,16384\nAD 35,35,51\n"
"Maaa 72,184,132\nAD 36,36,52\nAD 37,37,53\nMaaa 12,180,132\nTa 202,14,18432\nAD 38,38,54\nMaaa 8,176,132\nAD 39,39,55\nMvaa 140,172,132\nTa 204,15,16384\nAD 40,40,56\n"
"Mvaa 136,168,132\nD0 7,24,44,1024\nAD 41,41,57\nMvaa 76,164,132\nTa 206,15,18432\nAD 42,42,58\nMvaa 72,160,132\nAD 43,43,59\nAD 44,44,60\nMvaa 80,160,136\nTa 208,16,16384\n"
"AD 45,45,61\nMvaa 84,164,136\nAD 46,46,62\nMvaa 144,168,136\nTa 210,16,18432\nAD 47,47,63\nMvaa 148,172,136\nv_permlane16_swap_b32 v32, v40\nv_permlane16_swap_b32 v33, v41\n"
"Maaa 16,176,136\nTa 212,17,16384\nv_permlane16_swap_b32 v34, v42\nMaaa 20,180,136\nD0 8,24,44,2048\nv_permlane16_swap_b32 v35, v43\nMaaa 80,184,136\nTa 214,17,18432\n"
"Maaa 84,188,136\nv_permlane16_swap_b32 v36, v44\nv_permlane16_swap_b32 v37, v45\nMaaa 92,188,140\nTa 216,18,16384\nv_permlane16_swap_b32 v38, v46\nMaaa 88,184,140\n"
"v_permlane16_swap_b32 v39, v47\nAD 32,32,40\nMaaa 28,180,140\nTa 218,18,18432\nAD 33,33,41\nMaaa 24,176,140\nAD 34,34,42\nMvaa 156,172,140\nTa 220,19,16384\nAD 35,35,43\n"
"Mvaa 152,168,140\nD0 9,24,44,3072\nAD 36,36,44\nMvaa 92,164,140\nTa 222,19,18432\nAD 37,37,45\nMvaa 88,160,140\nAD 38,38,46\nAD 39,39,47\nMvaa 96,160,144\nRa 224,11,0\n"
"v_add_f32_dpp v32, v32, v32 row_ror:8 row_mask:0xf bank_mask:0xf\nMvaa 100,164,144\nv_add_f32_dpp v33, v33, v33 row_ror:8 row_mask:0xf bank_mask:0xf\nMvaa 160,168,144\n"
"Ra 228,11,2048\nv_add_f32_dpp v34, v34, v34 row_ror:8 row_mask:0xf bank_mask:0xf\nMvaa 164,172,144\ns_add_u32 m0, s47, 0\ns_add_u32 s44, s44, 0x40000\nMaaa 32,176,144\n"
"Ra 232,11,8192\nv_add_f32_dpp v35, v35, v35 row_ror:8 row_mask:0xf bank_mask:0xf\nMaaa 36,180,144\nD0 6,24,44,0\nv_add_f32_dpp v36, v36, v36 row_ror:8 row_mask:0xf bank_mask:0xf\n"
"Maaa 96,184,144\nRa 236,11,10240\nMaaa 100,188,144\nv_add_f32_dpp v37, v37, v37 row_ror:8 row_mask:0xf bank_mask:0xf\n"
"v_add_f32_dpp v38, v38, v38 row_ror:8 row_mask:0xf bank_mask:0xf\nMaaa 108,188,148\nRa 240,11,16384\nv_add_f32_dpp v39, v39, v39 row_ror:8 row_mask:0xf bank_mask:0xf\n"
"Maaa 104,184,148\nv_add_f32_dpp v32, v32, v32 row_ror:4 row_mask:0xf bank_mask:0xf\nv_add_f32_dpp v33, v33, v33 row_ror:4 row_mask:0xf bank_mask:0xf\nMaaa 44,180,148\n"
"Ra 244,11,18432\nv_add_f32_dpp v34, v34, v34 row_ror:4 row_mask:0xf bank_mask:0xf\nMaaa 40,176,148\nv_add_f32_dpp v35, v35, v35 row_ror:4 row_mask:0xf bank_mask:0xf\n"
"Mvaa 172,172,148\nRa 248,11,24576\nv_add_f32_dpp v36, v36, v36 row_ror:4 row_mask:0xf bank_mask:0xf\nMvaa 168,168,148\nD0 7,24,44,1024\n"
"v_add_f32_dpp v37, v37, v37 row_ror:4 row_mask:0xf bank_mask:0xf\nMvaa 108,164,148\nRa 252,11,26624\nv_add_f32_dpp v38, v38, v38 row_ror:4 row_mask:0xf bank_mask:0xf\n"
"Mvaa 104,160,148\nv_add_f32_dpp v39, v39, v39 row_ror:4 row_mask:0xf bank_mask:0xf\nv_add_f32_dpp v32, v32, v32 row_ror:2 row_mask:0xf bank_mask:0xf\nMvaa 112,160,152\n"
"v_add_f32_dpp v33, v33, v33 row_ror:2 row_mask:0xf bank_mask:0xf\nv_add_f32_dpp v34, v34, v34 row_ror:2 row_mask:0xf bank_mask:0xf\nMvaa 116,164,152\n"
"v_add_f32_dpp v35, v35, v35 row_ror:2 row_mask:0xf bank_mask:0xf\nMvaa 176,168,152\nv_add_f32_dpp v36, v36, v36 row_ror:2 row_mask:0xf bank_mask:0xf\n"
"v_add_f32_dpp v37, v37, v37 row_ror:2 row_mask:0xf bank_mask:0xf\nMvaa 180,172,152\nv_add_f32_dpp v38, v38, v38 row_ror:2 row_mask:0xf bank_mask:0xf\n"
"v_add_f32_dpp v39, v39, v39 row_ror:2 row_mask:0xf bank_mask:0xf\nMaaa 48,176,152\nv_lshrrev_b32 v48, 4, v0\nv_and_b32 v49, 1, v0\nMaaa 52,180,152\nD0 8,24,44,2048\n"
"v_lshlrev_b32 v49, 5, v49\nMaaa 112,184,152\nv_lshl_add_u32 v49, v48, 6, v49\nMaaa 116,188,152\ns_lshl_b32 s2, s11, 8\ns_add_u32 s2, s2, 132096\nMaaa 124,188,156\n"
"v_add_u32 v49, s2, v49\nds_write_b128 v49, v[32:35]\nMaaa 120,184,156\nds_write_b128 v49, v[36:39] offset:16\nv_and_b32 v50, 31, v0\nMaaa 60,180,156\nv_lshlrev_b32 v50, 2, v50\n"
"s_lshl_b32 s2, s11, 10\nMaaa 56,176,156\ns_add_u32 s2, s2, 133120\nMvaa 188,172,156\nv_add_u32 v51, s2, v50\nv_add_u32 v50, 132096, v50\nMvaa 184,168,156\nD0 9,24,44,3072\n"
"v_lshl_add_u32 v52, v48, 4, s2\nMvaa 124,164,156\nMvaa 120,160,156\ns_waitcnt vmcnt(8) lgkmcnt(0)\ns_barrier\nMvaa 64,224,192\nTa 128,12,32768\nds_read_b32 v32, v50 offset:0\n"
"Mvaa 68,228,192\nds_read_b32 v33, v50 offset:256\nMvaa 128,232,192\nTa 130,12,34816\nds_read_b32 v34, v50 offset:512\nMvaa 132,236,192\ns_add_u32 m0, s48, 0\n"
"s_add_u32 s45, s45, 128\nMaaa 0,240,192\nTa 132,13,32768\nds_read_b32 v35, v50 offset:768\nMaaa 4,244,192\nD0 2,20,45,0\nMaaa 64,248,192\nTa 134,13,34816\n"
"ds_read_b32 v36, v50 offset:128\nMaaa 68,252,192\nds_read_b32 v37, v50 offset:384\nMaaa 76,252,196\nTa 136,14,32768\nds_read_b32 v38, v50 offset:640\nMaaa 72,248,196\n"
"ds_read_b32 v39, v50 offset:896\nMaaa 12,244,196\nTa 138,14,34816\nMaaa 8,240,196\nMvaa 140,236,196\nTa 140,15,32768\nMvaa 136,232,196\nD0 3,20,45,1024\nMvaa 76,228,196\n"
"Ta 142,15,34816\nMvaa 72,224,196\nMvaa 80,224,200\nTa 144,16,32768\nMvaa 84,228,200\nMvaa 144,232,200\nTa 146,16,34816\nMvaa 148,236,200\nWL 5\nMaaa 16,240,200\nTa 148,17,32768\n"
"AD 32,32,33\nMaaa 20,244,200\nD0 4,20,45,2048\nMaaa 80,248,200\nTa 150,17,34816\nAD 34,34,35\nMaaa 84,252,200\nAD 36,36,37\nMaaa 92,252,204\nTa 152,18,32768\nAD 38,38,39\n"
"Maaa 88,248,204\nAD 32,32,34\nMaaa 28,244,204\nTa 154,18,34816\nAD 36,36,38\nMaaa 24,240,204\nCV 32,32,36\nMvaa 156,236,204\nTa 156,19,32768\nv_and_b32 v36, 0xffff0000, v32\n"
"Mvaa 152,232,204\nD0 5,20,45,3072\nMvaa 92,228,204\nTa 158,19,34816\nv_lshlrev_b32 v32, 16, v32\nMvaa 88,224,204\nv_mul_f32 v33, 0xbfb8aa3b, v32\nEX 33,33\nMvaa 96,224,208\n"
"Ra 160,10,32768\nv_add_f32 v33, 1.0, v33\nMvaa 100,228,208\nv_rcp_f32 v33, v33\nMvaa 160,232,208\nRa 164,10,34816\nMU 33,32,33\nMvaa 164,236,208\ns_add_u32 m0, s48, 4096\n"
"s_add_u32 s46, s45, 0x10000\nMaaa 32,240,208\nRa 168,10,40960\nCV 33,33,33\nMaaa 36,244,208\nD0 2,20,46,0\nMaaa 96,248,208\nRa 172,10,43008\nv_lshlrev_b32 v33, 16, v33\n"
"Maaa 100,252,208\nMU 33,33,36\nMaaa 108,252,212\nRa 176,10,49152\nCV 33,33,33\nMaaa 104,248,212\nv_lshlrev_b32 v33, 16, v33\nMaaa 44,244,212\nRa 180,10,51200\n"
"ds_write_b32 v51, v33\nMaaa 40,240,212\nRv 244,52,0\nMvaa 172,236,212\nRa 184,10,57344\nRv 248,52,64\nMvaa 168,232,212\nD0 3,20,46,1024\nMvaa 108,228,212\nRa 188,10,59392\n"
"s_and_b32 s2, s74, 15\nMvaa 104,224,212\ns_sub_u32 s2, s2, 2\ns_cmp_lt_u32 s2, 14\ns_cselect_b32 s5, 0, 0x40000000\nMvaa 112,224,216\ns_and_b32 s2, s2, 15\ns_mul_i32 s4, s73, 14\n"
"Mvaa 116,228,216\ns_add_u32 s4, s4, s2\nMvaa 176,232,216\nv_and_b32 v49, 15, v0\nv_lshlrev_b32 v50, 3, v49\nMvaa 180,236,216\nv_and_b32 v51, 1, v48\nMaaa 48,240,216\n"
"v_lshl_add_u32 v50, v51, 2, v50\ns_mul_i32 s2, s11, 112\nMaaa 52,244,216\nD0 4,20,46,2048\nMaaa 112,248,216\nv_add_u32 v50, s2, v50\nv_lshrrev_b32 v51, 4, v50\nMaaa 116,252,216\n"
"v_lshrrev_b32 v48, 1, v48\nMaaa 124,252,220\nv_mad_u32_u24 v51, v48, 28, v51\nv_mul_u32_u24 v51, 7168, v51\nMaaa 120,248,220\nv_and_b32 v50, 15, v50\nMaaa 60,244,220\n"
"v_lshl_add_u32 v51, v50, 2, v51\nv_cmp_gt_u32 vcc, 14, v49\ns_lshl_b32 s4, s4, 6\ns_add_u32 s4, s4, 0xc04c800\nv_cndmask_b32 v51, v254, v51, vcc\nMaaa 56,240,220\n"
"s_or_b32 s4, s4, s5\nMvaa 188,236,220\nWL 3\nv_lshlrev_b32 v252, 16, v224\nMvaa 184,232,220\nD0 5,20,46,3072\nMvaa 124,228,220\nv_and_b32 v253, 0xffff0000, v224\nMU 32,244,252\n"
"Mvaa 120,224,220\ns_waitcnt vmcnt(12) lgkmcnt(0)\ns_barrier\nMvaa 64,160,128\nTa 192,12,49152\nMU 33,244,253\nMvaa 68,164,128\nMU 40,248,252\nMvaa 128,168,128\nTa 194,12,51200\n"
"MU 41,248,253\nMvaa 132,172,128\ns_add_u32 m0, s47, 16384\ns_add_u32 s44, s44, 0x40000\nMaaa 0,176,128\nTa 196,13,49152\nv_lshlrev_b32 v252, 16, v225\nMaaa 4,180,128\n"
"D0 6,24,44,0\nMaaa 64,184,128\nTa 198,13,51200\nv_and_b32 v253, 0xffff0000, v225\nMaaa 68,188,128\nMU 34,244,252\nMaaa 76,188,132\nTa 200,14,49152\nMU 35,244,253\nMaaa 72,184,132\n"
"MU 42,248,252\nMaaa 12,180,132\nTa 202,14,51200\nMU 43,248,253\nMaaa 8,176,132\nv_lshlrev_b32 v252, 16, v226\nMvaa 140,172,132\nTa 204,15,49152\nv_and_b32 v253, 0xffff0000, v226\n"
"Mvaa 136,168,132\nD0 7,24,44,1024\nMvaa 76,164,132\nTa 206,15,51200\nMU 36,244,252\nMvaa 72,160,132\nMU 37,244,253\nMvaa 80,160,136\nTa 208,16,49152\nMU 44,248,252\n"
"Mvaa 84,164,136\nMU 45,248,253\nMvaa 144,168,136\nTa 210,16,51200\nv_lshlrev_b32 v252, 16, v227\nMvaa 148,172,136\nv_and_b32 v253, 0xffff0000, v227\nMaaa 16,176,136\n"
"Ta 212,17,49152\nMU 38,244,252\nMaaa 20,180,136\nD0 8,24,44,2048\nMaaa 80,184,136\nTa 214,17,51200\nMU 39,244,253\nMaaa 84,188,136\nMU 46,248,252\nMaaa 92,188,140\n"
"Ta 216,18,49152\nMU 47,248,253\nMaaa 88,184,140\nv_lshlrev_b32 v252, 16, v228\nMaaa 28,180,140\nTa 218,18,51200\nv_and_b32 v253, 0xffff0000, v228\nMaaa 24,176,140\nFM 32,245,252\n"
"Mvaa 156,172,140\nTa 220,19,49152\nFM 33,245,253\nMvaa 152,168,140\nD0 9,24,44,3072\nMvaa 92,164,140\nTa 222,19,51200\nFM 40,249,252\nMvaa 88,160,140\nFM 41,249,253\n"
"Mvaa 96,160,144\nRa 224,11,32768\nv_lshlrev_b32 v252, 16, v229\nMvaa 100,164,144\nv_and_b32 v253, 0xffff0000, v229\nMvaa 160,168,144\nRa 228,11,34816\nFM 34,245,252\n"
"Mvaa 164,172,144\ns_add_u32 m0, s47, 32768\ns_add_u32 s44, s44, 0x40000\nMaaa 32,176,144\nRa 232,11,40960\nFM 35,245,253\nMaaa 36,180,144\nD0 6,24,44,0\nMaaa 96,184,144\n"
"Ra 236,11,43008\nFM 42,249,252\nMaaa 100,188,144\nFM 43,249,253\nMaaa 108,188,148\nRa 240,11,49152\nv_lshlrev_b32 v252, 16, v230\nMaaa 104,184,148\n"
"v_and_b32 v253, 0xffff0000, v230\nMaaa 44,180,148\nRa 244,11,51200\nFM 36,245,252\nMaaa 40,176,148\nFM 37,245,253\nMvaa 172,172,148\nRa 248,11,57344\nFM 44,249,252\n"
"Mvaa 168,168,148\nD0 7,24,44,1024\nMvaa 108,164,148\nRa 252,11,59392\nFM 45,249,253\nMvaa 104,160,148\nv_lshlrev_b32 v252, 16, v231\nMvaa 112,160,152\n"
"v_and_b32 v253, 0xffff0000, v231\nFM 38,245,252\nMvaa 116,164,152\nFM 39,245,253\nMvaa 176,168,152\nFM 46,249,252\nFM 47,249,253\nMvaa 180,172,152\nv_lshlrev_b32 v252, 16, v232\n"
"Maaa 48,176,152\nv_and_b32 v253, 0xffff0000, v232\nFM 32,246,252\nMaaa 52,180,152\nD0 8,24,44,2048\nMaaa 112,184,152\nFM 33,246,253\nFM 40,250,252\nMaaa 116,188,152\n"
"FM 41,250,253\nMaaa 124,188,156\nv_lshlrev_b32 v252, 16, v233\nv_and_b32 v253, 0xffff0000, v233\nMaaa 120,184,156\nFM 34,246,252\nMaaa 60,180,156\nFM 35,246,253\nFM 42,250,252\n"
"Maaa 56,176,156\nFM 43,250,253\nMvaa 188,172,156\nv_lshlrev_b32 v252, 16, v234\nv_and_b32 v253, 0xffff0000, v234\nMvaa 184,168,156\nD0 9,24,44,3072\nMvaa 124,164,156\n"
"FM 36,246,252\nFM 37,246,253\nMvaa 120,160,156\ns_waitcnt vmcnt(8) lgkmcnt(0)\ns_barrier\nMvaa 64,224,192\nTa 128,12,0\nFM 44,250,252\nMvaa 68,228,192\nFM 45,250,253\n"
"Mvaa 128,232,192\nTa 130,12,2048\nv_lshlrev_b32 v252, 16, v235\nMvaa 132,236,192\ns_add_u32 m0, s48, 32768\ns_add_u32 s45, s45, 128\nMaaa 0,240,192\nTa 132,13,0\n"
"v_and_b32 v253, 0xffff0000, v235\nMaaa 4,244,192\nD0 2,20,45,0\nMaaa 64,248,192\nTa 134,13,2048\nFM 38,246,252\nMaaa 68,252,192\nFM 39,246,253\nMaaa 76,252,196\nTa 136,14,0\n"
"FM 46,250,252\nMaaa 72,248,196\nFM 47,250,253\nMaaa 12,244,196\nTa 138,14,2048\nv_lshlrev_b32 v252, 16, v236\nMaaa 8,240,196\nv_and_b32 v253, 0xffff0000, v236\nMvaa 140,236,196\n"
"Ta 140,15,0\nFM 32,247,252\nMvaa 136,232,196\nD0 3,20,45,1024\nMvaa 76,228,196\nTa 142,15,2048\nFM 33,247,253\nMvaa 72,224,196\nFM 40,251,252\nMvaa 80,224,200\nTa 144,16,0\n"
"FM 41,251,253\nMvaa 84,228,200\nv_lshlrev_b32 v252, 16, v237\nMvaa 144,232,200\nTa 146,16,2048\nv_and_b32 v253, 0xffff0000, v237\nMvaa 148,236,200\nFM 34,247,252\nMaaa 16,240,200\n"
"Ta 148,17,0\nFM 35,247,253\nMaaa 20,244,200\nD0 4,20,45,2048\nMaaa 80,248,200\nTa 150,17,2048\nFM 42,251,252\nMaaa 84,252,200\nFM 43,251,253\nMaaa 92,252,204\nTa 152,18,0\n"
"v_lshlrev_b32 v252, 16, v238\nMaaa 88,248,204\nv_and_b32 v253, 0xffff0000, v238\nMaaa 28,244,204\nTa 154,18,2048\nFM 36,247,252\nMaaa 24,240,204\nFM 37,247,253\nMvaa 156,236,204\n"
"Ta 156,19,0\nFM 44,251,252\nMvaa 152,232,204\nD0 5,20,45,3072\nMvaa 92,228,204\nTa 158,19,2048\nFM 45,251,253\nMvaa 88,224,204\nv_lshlrev_b32 v252, 16, v239\nMvaa 96,224,208\n"
"Ra 160,10,0\nv_and_b32 v253, 0xffff0000, v239\nMvaa 100,228,208\nFM 38,247,252\nMvaa 160,232,208\nRa 164,10,2048\nFM 39,247,253\nMvaa 164,236,208\ns_add_u32 m0, s48, 36864\n"
"s_add_u32 s46, s45, 0x10000\nMaaa 32,240,208\nRa 168,10,8192\nFM 46,251,252\nMaaa 36,244,208\nD0 2,20,46,0\nMaaa 96,248,208\nRa 172,10,10240\nFM 47,251,253\nMaaa 100,252,208\n"
"v_permlane32_swap_b32 v32, v40\nMaaa 108,252,212\nRa 176,10,16384\nv_permlane32_swap_b32 v33, v41\nMaaa 104,248,212\nv_permlane32_swap_b32 v34, v42\nMaaa 44,244,212\n"
"Ra 180,10,18432\nv_permlane32_swap_b32 v35, v43\nMaaa 40,240,212\nv_permlane32_swap_b32 v36, v44\nMvaa 172,236,212\nRa 184,10,24576\nv_permlane32_swap_b32 v37, v45\n"
"Mvaa 168,232,212\nD0 3,20,46,1024\nMvaa 108,228,212\nRa 188,10,26624\nv_permlane32_swap_b32 v38, v46\nMvaa 104,224,212\nv_permlane32_swap_b32 v39, v47\nMvaa 112,224,216\n"
"AD 32,32,40\nAD 33,33,41\nMvaa 116,228,216\nAD 34,34,42\nMvaa 176,232,216\nAD 35,35,43\nAD 36,36,44\nMvaa 180,236,216\nAD 37,37,45\nMaaa 48,240,216\nAD 38,38,46\nAD 39,39,47\n"
"Maaa 52,244,216\nD0 4,20,46,2048\nMaaa 112,248,216\nv_permlane16_swap_b32 v32, v36\nv_permlane16_swap_b32 v33, v37\nMaaa 116,252,216\nv_permlane16_swap_b32 v34, v38\n"
"Maaa 124,252,220\nv_permlane16_swap_b32 v35, v39\nAD 32,32,36\nMaaa 120,248,220\nAD 33,33,37\nMaaa 60,244,220\nAD 34,34,38\nAD 35,35,39\nMaaa 56,240,220\nSv 32,51,16,4,0\n"
"Mvaa 188,236,220\nMvaa 184,232,220\nD0 5,20,46,3072\nMvaa 124,228,220\nMvaa 120,224,220\ns_waitcnt vmcnt(13) lgkmcnt(0)\ns_barrier\nMvaa 64,160,128\nTa 192,12,16384\n"
"s_lshr_b32 s3, s11, 1\nMvaa 68,164,128\ns_lshl_b32 s3, s3, 13\ns_add_u32 m0, s47, 49152\ns_add_u32 s44, s44, 0x40000\nMvaa 128,168,128\nTa 194,12,18432\ns_lshl_b32 s2, s11, 12\n"
"Mvaa 132,172,128\ns_add_u32 s2, s2, s3\nD0 6,24,44,0\nMaaa 0,176,128\nTa 196,13,16384\ns_add_u32 s2, s2, s72\nMaaa 4,180,128\ns_lshl_b32 s3, s11, 12\nMaaa 64,184,128\n"
"Ta 198,13,18432\ns_add_u32 s89, s3, 0x0\nMaaa 68,188,128\ns_mov_b32 s88, 0xffff8000\nMaaa 76,188,132\nTa 200,14,16384\ns_lshl_b32 s4, s13, 1\nD0 7,24,44,1024\nMaaa 72,184,132\n"
"s_sub_u32 s79, 15, s4\ns_and_b32 s5, s12, 1\nMaaa 12,180,132\nTa 202,14,18432\nMaaa 8,176,132\ns_mul_i32 s5, s5, 229376\ns_add_u32 s5, s5, s2\nMvaa 140,172,132\nTa 204,15,16384\n"
"Mvaa 136,168,132\ns_add_u32 s96, s5, 0x1310000\ns_mov_b32 s63, 0x53800000\nD0 8,24,44,2048\nMvaa 76,164,132\nTa 206,15,18432\nMvaa 72,160,132\ns_mov_b32 s64, 0x2b800000\n"
"s_sub_u32 s98, s4, 1\nMvaa 80,160,136\nTa 208,16,16384\ns_lshl_b32 s5, s13, 5\nMvaa 84,164,136\ns_add_u32 s5, s5, s11\nMvaa 144,168,136\nTa 210,16,18432\ns_lshl_b32 s65, s5, 9\n"
"D0 9,24,44,3072\nMvaa 148,172,136\ns_add_u32 s65, s65, s72\nMaaa 16,176,136\nTa 212,17,16384\ns_add_u32 s65, s65, 0x1580000\nMaaa 20,180,136\ns_and_b32 s66, s72, 0x2000\n"
"s_add_u32 s6, s56, 0\nMaaa 80,184,136\nTa 214,17,18432\ns_add_u32 s66, s66, s56\nMaaa 84,188,136\ns_add_u32 s66, s66, 0x100000\n"
"buffer_load_dwordx4 v[192:195], v1, s[16:19], s6 offen offset:0 sc1\nMaaa 92,188,140\nTa 216,18,16384\ns_and_b32 s5, s12, 1\nMaaa 88,184,140\ns_mul_i32 s58, s5, 0x202000\n"
"s_xor_b32 s58, s58, s72\nMaaa 28,180,140\nTa 218,18,18432\nMaaa 24,176,140\ns_add_u32 s58, s58, 0x15c0000\ns_lshl_b32 s5, s5, 8\nMvaa 156,172,140\nTa 220,19,16384\n"
"buffer_load_dwordx4 v[196:199], v1, s[16:19], s6 offen offset:1024 sc1\nMvaa 152,168,140\ns_add_u32 s61, s58, s5\ns_lshl4_add_u32 s60, s13, s61\nMvaa 92,164,140\nTa 222,19,18432\n"
"Mvaa 88,160,140\ns_add_u32 s54, s60, 0\ns_xor_b32 s60, s54, 0x100\nMvaa 96,160,144\nRa 224,11,0\nMvaa 100,164,144\ns_cmp_eq_u32 s11, 0\ns_cselect_b32 s54, s54, 0x40000000\n"
"buffer_load_dwordx4 v[200:203], v1, s[16:19], s6 offen offset:2048 sc1\nMvaa 160,168,144\nRa 228,11,2048\ns_getpc_b64 s[8:9]\n.PC139:\ns_add_u32 s8, s8, .HD131-.PC139\n"
"s_addc_u32 s9, s9, 0\nMvaa 164,172,144\ns_mov_b32 s97, 0xff800000\nMaaa 32,176,144\nRa 232,11,8192\ns_mov_b32 s90, 3\nMaaa 36,180,144\ns_cmp_lt_u32 s11, 2\n"
"s_cselect_b32 s92, 2, 0\nMaaa 96,184,144\nRa 236,11,10240\ns_add_u32 s86, s2, 0x20000\nbuffer_load_dwordx4 v[204:207], v1, s[16:19], s6 offen offset:3072 sc1\nMaaa 100,188,144\n"
"s_add_u32 s87, s2, 0x410000\ns_cmp_eq_u32 s13, 7\ns_cselect_b32 s92, s92, 0\nMaaa 108,188,148\nRa 240,11,16384\ns_sub_u32 s4, s13, 2\nMaaa 104,184,148\ns_cmp_lt_u32 s4, 4\n"
"s_cselect_b32 s4, s12, 15\ns_add_u32 s6, s56, 4096\nMaaa 44,180,148\nRa 244,11,18432\ns_cmp_lt_u32 s4, 14\ns_cselect_b32 s5, s11, 1\nMaaa 40,176,148\ns_cmp_eq_u32 s5, 0\n"
"s_cselect_b32 s91, 1, 100\nbuffer_load_dwordx4 v[208:211], v1, s[16:19], s6 offen offset:0 sc1\nMvaa 172,172,148\nRa 248,11,24576\nv_and_b32 v27, 0x70, v1\n"
"buffer_load_dword v27, v27, s[16:19], s61 offen offset:128 sc0 sc1\nMvaa 168,168,148\ns_lshr_b32 s2, s13, 2\ns_bfe_u32 s3, s13, 0x10001\nMvaa 108,164,148\nRa 252,11,26624\n"
"Mvaa 104,160,148\ns_mul_i32 s3, s3, 17\ns_add_u32 s2, s2, s3\nMvaa 112,160,152\ns_lshl2_add_u32 s3, s14, 0\ns_lshl_b32 s3, s3, 2\n"
"buffer_load_dwordx4 v[212:215], v1, s[16:19], s6 offen offset:1024 sc1\nMvaa 116,164,152\ns_lshr_b32 s3, 0xe420ca86, s3\nMvaa 176,168,152\ns_and_b32 s3, s3, 15\n"
"s_add_u32 s2, s2, s3\nMvaa 180,172,152\ns_cmp_eq_u32 s2, 32\ns_cselect_b32 s4, 16, s2\nMaaa 48,176,152\ns_mul_i32 s75, s4, 65536\ns_lshr_b32 s2, s13, 2\nMaaa 52,180,152\n"
"s_bfe_u32 s3, s13, 0x10001\nbuffer_load_dwordx4 v[216:219], v1, s[16:19], s6 offen offset:2048 sc1\nMaaa 112,184,152\ns_mul_i32 s3, s3, 17\ns_add_u32 s2, s2, s3\nMaaa 116,188,152\n"
"s_lshl2_add_u32 s3, s14, 1\nMaaa 124,188,156\ns_lshl_b32 s3, s3, 2\ns_lshr_b32 s3, 0xe420ca86, s3\nMaaa 120,184,156\ns_and_b32 s3, s3, 15\ns_add_u32 s2, s2, s3\nMaaa 60,180,156\n"
"s_cmp_eq_u32 s2, 32\ns_cselect_b32 s4, 16, s2\nbuffer_load_dwordx4 v[220:223], v1, s[16:19], s6 offen offset:3072 sc1\nMaaa 56,176,156\ns_mul_i32 s76, s4, 65536\n"
"s_lshr_b32 s2, s13, 2\nMvaa 188,172,156\ns_bfe_u32 s3, s13, 0x10001\nMvaa 184,168,156\ns_mul_i32 s3, s3, 17\ns_add_u32 s2, s2, s3\nMvaa 124,164,156\ns_lshl2_add_u32 s3, s14, 2\n"
"Mvaa 120,160,156\ns_waitcnt vmcnt(13) lgkmcnt(0)\ns_barrier\nMvaa 64,224,192\nTa 128,12,32768\ns_lshl_b32 s3, s3, 2\ns_add_u32 s6, s56, 16384\n"
"buffer_load_dwordx4 v[224:227], v1, s[16:19], s6 offen offset:0 sc1\nMvaa 68,228,192\ns_lshr_b32 s3, 0xe420ca86, s3\nMvaa 128,232,192\nTa 130,12,34816\ns_and_b32 s3, s3, 15\n"
"Mvaa 132,236,192\ns_add_u32 s2, s2, s3\ns_cmp_eq_u32 s2, 32\ns_cselect_b32 s4, 16, s2\nMaaa 0,240,192\nTa 132,13,32768\ns_mul_i32 s77, s4, 65536\nMaaa 4,244,192\n"
"s_lshr_b32 s2, s13, 2\nbuffer_load_dwordx4 v[228:231], v1, s[16:19], s6 offen offset:1024 sc1\nMaaa 64,248,192\nTa 134,13,34816\ns_bfe_u32 s3, s13, 0x10001\nMaaa 68,252,192\n"
"s_mul_i32 s3, s3, 17\nMaaa 76,252,196\nTa 136,14,32768\ns_add_u32 s2, s2, s3\nMaaa 72,248,196\ns_lshl2_add_u32 s3, s14, 3\ns_lshl_b32 s3, s3, 2\nMaaa 12,244,196\nTa 138,14,34816\n"
"buffer_load_dwordx4 v[232:235], v1, s[16:19], s6 offen offset:2048 sc1\nMaaa 8,240,196\ns_lshr_b32 s3, 0xe420ca86, s3\ns_and_b32 s3, s3, 15\nMvaa 140,236,196\nTa 140,15,32768\n"
"Mvaa 136,232,196\ns_add_u32 s2, s2, s3\nMvaa 76,228,196\nTa 142,15,34816\ns_cmp_eq_u32 s2, 32\ns_cselect_b32 s4, 16, s2\nMvaa 72,224,196\ns_mul_i32 s78, s4, 65536\n"
"s_lshr_b32 s3, s11, 1\nbuffer_load_dwordx4 v[236:239], v1, s[16:19], s6 offen offset:3072 sc1\nMvaa 80,224,200\nTa 144,16,32768\ns_lshl_b32 s3, s3, 15\nMvaa 84,228,200\n"
"s_add_u32 s2, s72, s3\nMvaa 144,232,200\nTa 146,16,34816\ns_and_b32 s3, s11, 1\ns_add_u32 s6, s56, 20480\nMvaa 148,236,200\ns_lshl_b32 s3, s3, 14\nMaaa 16,240,200\n"
"Ta 148,17,32768\ns_add_u32 s2, s2, s3\nbuffer_load_dwordx4 v[240:243], v1, s[16:19], s6 offen offset:0 sc1\nMaaa 20,244,200\ns_add_u32 s2, s2, 0x400000\nMaaa 80,248,200\n"
"Ta 150,17,34816\ns_mov_b32 s55, 4096\nMaaa 84,252,200\ns_add_u32 s75, s75, s2\nMaaa 92,252,204\nTa 152,18,32768\ns_add_u32 s76, s76, s2\nMaaa 88,248,204\ns_add_u32 s77, s77, s2\n"
"s_add_u32 s78, s78, s2\nbuffer_load_dwordx4 v[244:247], v1, s[16:19], s6 offen offset:1024 sc1\nMaaa 28,244,204\nTa 154,18,34816\nMaaa 24,240,204\nMvaa 156,236,204\n"
"Ta 156,19,32768\nMvaa 152,232,204\nMvaa 92,228,204\nTa 158,19,34816\nbuffer_load_dwordx4 v[248:251], v1, s[16:19], s6 offen offset:2048 sc1\nMvaa 88,224,204\nMvaa 96,224,208\n"
"Ra 160,10,32768\nMvaa 100,228,208\nMvaa 160,232,208\nRa 164,10,34816\nMvaa 164,236,208\nbuffer_load_dwordx4 v[252:255], v1, s[16:19], s6 offen offset:3072 sc1\nMaaa 32,240,208\n"
"Ra 168,10,40960\nMaaa 36,244,208\nMaaa 96,248,208\nRa 172,10,43008\nMaaa 100,252,208\nMaaa 108,252,212\nRa 176,10,49152\nWV 11\nv_cmp_ne_u32 vcc, s15, v27\ns_nop 0\n"
"s_cbranch_vccnz .S140\n.S141:\ns_sub_u32 s45, s86, 131072\nMaaa 104,248,212\ns_add_u32 m0, s47, 0\ns_add_u32 s46, s45, 0x0\nMaaa 44,244,212\nRa 180,10,51200\nMaaa 40,240,212\n"
"D1 1,16,46,0\nMvaa 172,236,212\nRa 184,10,57344\nMvaa 168,232,212\nMvaa 108,228,212\nRa 188,10,59392\nMvaa 104,224,212\nD1 1,16,46,1024\nMvaa 112,224,216\nMvaa 116,228,216\n"
"Mvaa 176,232,216\ns_mov_b32 s36, s68\nMvaa 180,236,216\nD1 1,16,46,2048\nMaaa 48,240,216\ns_and_b32 s37, s69, 0xffff\ns_mov_b32 s38, 0x1c0000\nMaaa 52,244,216\n"
"s_mov_b32 s39, 0x20000\nMaaa 112,248,216\ns_and_b32 s2, s74, 15\nMaaa 116,252,216\nD1 1,16,46,3072\ns_max_u32 s2, s2, 2\nMaaa 124,252,220\ns_sub_u32 s2, s2, 2\nMaaa 120,248,220\n"
"s_mul_i32 s3, s73, 14\nMaaa 60,244,220\ns_add_u32 s2, s2, s3\ns_lshl2_add_u32 s2, s2, s11\nMaaa 56,240,220\ns_mul_i32 s3, s2, 4682\nMvaa 188,236,220\ns_lshr_b32 s3, s3, 16\n"
"Mvaa 184,232,220\ns_mul_i32 s22, s3, 14\ns_sub_u32 s22, s2, s22\nMvaa 124,228,220\ns_and_b32 s21, s3, 28\nMvaa 120,224,220\nWL 0\ns_barrier\nMvaa 64,160,128\nTa 192,12,49152\n"
"s_add_u32 s21, s21, s3\nMvaa 68,164,128\ns_add_u32 m0, s47, 32768\ns_add_u32 s46, s45, 0x8000\nMvaa 128,168,128\nTa 194,12,51200\nMvaa 132,172,128\nD1 1,16,46,0\nMaaa 0,176,128\n"
"Ta 196,13,49152\ns_lshl_b32 s21, s21, 4\nMaaa 4,180,128\ns_add_u32 s21, s21, 1024\nMaaa 64,184,128\nTa 198,13,51200\nMaaa 68,188,128\nD1 1,16,46,1024\ns_xor_b32 s3, s3, 28\n"
"Maaa 76,188,132\nTa 200,14,49152\nMaaa 72,184,132\ns_bfe_u32 s41, s3, 0x30002\ns_cmp_gt_u32 s22, 6\ns_cselect_b32 s2, 1187840, 0\ns_cselect_b32 s1, 16, 0\nMaaa 12,180,132\n"
"Ta 202,14,51200\ns_add_u32 s41, s41, s1\nMaaa 8,176,132\nD1 1,16,46,2048\nMvaa 140,172,132\nTa 204,15,49152\nMvaa 136,168,132\ns_lshl4_add_u32 s41, s41, 0x15c0200\n"
"s_and_b32 s1, s3, 28\nMvaa 76,164,132\nTa 206,15,51200\nMvaa 72,160,132\nD1 1,16,46,3072\nMvaa 80,160,136\nTa 208,16,49152\ns_add_u32 s3, s3, s1\nMvaa 84,164,136\n"
"s_add_u32 m0, s47, -65536\ns_add_u32 s46, s45, 0x10000\nMvaa 144,168,136\nTa 210,16,51200\nMvaa 148,172,136\nD1 1,16,46,0\nMaaa 16,176,136\nTa 212,17,49152\ns_lshl_b32 s3, s3, 11\n"
"Maaa 20,180,136\ns_add_u32 s2, s2, s3\nMaaa 80,184,136\nTa 214,17,51200\nMaaa 84,188,136\nD1 1,16,46,1024\ns_lshl_b32 s3, s22, 17\nMaaa 92,188,140\nTa 216,18,49152\n"
"Maaa 88,184,140\ns_add_u32 s2, s2, s3\nMaaa 28,180,140\nTa 218,18,51200\ns_add_u32 s20, s2, 0xd00000\nMaaa 24,176,140\nD1 1,16,46,2048\nMvaa 156,172,140\nTa 220,19,49152\n"
"Mvaa 152,168,140\ns_lshl_b32 s2, s22, 7\ns_add_u32 s40, s2, 0xc04b800\nMvaa 92,164,140\nTa 222,19,51200\nMvaa 88,160,140\nD1 1,16,46,3072\nMvaa 96,160,144\nRa 224,11,32768\n"
"s_mul_i32 s2, s21, 896\nMvaa 100,164,144\ns_add_u32 m0, s47, -49152\ns_add_u32 s46, s45, 0x408000\nMvaa 160,168,144\nRa 228,11,34816\nMvaa 164,172,144\nD1 1,16,46,0\n"
"Maaa 32,176,144\nRa 232,11,40960\ns_lshl_b32 s3, s22, 6\nMaaa 36,180,144\ns_add_u32 s22, s2, s3\nMaaa 96,184,144\nRa 236,11,43008\nMaaa 100,188,144\nD1 1,16,46,1024\n"
"s_add_u32 s23, s22, 57344\nMaaa 108,188,148\nRa 240,11,49152\nMaaa 104,184,148\nMaaa 44,180,148\nRa 244,11,51200\nMaaa 40,176,148\nD1 1,16,46,2048\nMvaa 172,172,148\n"
"Ra 248,11,57344\nMvaa 168,168,148\nMvaa 108,164,148\nRa 252,11,59392\nMvaa 104,160,148\nD1 1,16,46,3072\nMvaa 112,160,152\nMvaa 116,164,152\nv_add_u32 v2, 0x10000, v1\n"
"Mvaa 176,168,152\ns_lshr_b32 s2, s59, 1\nMvaa 180,172,152\nv_add_u32 v24, s2, v2\nMaaa 48,176,152\nv_xor_b32 v26, 0x1000, v24\nMaaa 52,180,152\nv_lshrrev_b32 v20, 4, v0\n"
"Maaa 112,184,152\nv_lshlrev_b32 v20, 2, v20\nMaaa 116,188,152\nv_and_b32 v0, 15, v0\nMaaa 124,188,156\nv_sub_u32 v0, v0, v20\nMaaa 120,184,156\ns_lshl_b32 s2, s11, 4\n"
"Maaa 60,180,156\nv_add_u32 v3, s2, v0\nMaaa 56,176,156\nv_subrev_u32 v0, 32, v3\nMvaa 188,172,156\nv_mov_b32 v4, 0\nMvaa 184,168,156\nv_mov_b32 v5, 0\nMvaa 124,164,156\n"
"v_mov_b32 v6, 0\nMvaa 120,160,156\ns_waitcnt vmcnt(12) lgkmcnt(0)\ns_barrier\nMvaa 64,224,192\nv_mov_b32 v7, 0\nMvaa 68,228,192\ns_add_u32 m0, s47, 49152\n"
"s_add_u32 s46, s45, 0x400000\nMvaa 72,224,196\nv_mov_b32 v8, 0\nMvaa 76,228,196\nD1 1,16,46,0\nMvaa 80,224,200\nv_mov_b32 v9, 0\nMvaa 84,228,200\nv_mov_b32 v10, 0\n"
"Mvaa 88,224,204\nv_mov_b32 v11, 0\nCV 32,64,65\nMvaa 92,228,204\nD1 1,16,46,1024\nCV 33,66,67\nMvaa 96,224,208\nv_mov_b32 v12, 0\nCV 34,68,69\nMvaa 100,228,208\nv_mov_b32 v13, 0\n"
"Mvaa 104,224,212\nv_mov_b32 v14, 0\nCV 35,70,71\nMvaa 108,228,212\nD1 1,16,46,2048\nCV 36,72,73\nMvaa 112,224,216\nv_mov_b32 v15, 0\nCV 37,74,75\nMvaa 116,228,216\n"
"v_mov_b32 v16, 0\nCV 38,76,77\nMvaa 120,224,220\nv_mov_b32 v17, 0\nCV 39,78,79\nMvaa 124,228,220\nD1 1,16,46,3072\nCV 40,80,81\nMvaa 128,232,192\nv_mov_b32 v18, 0\nCV 41,82,83\n"
"Mvaa 132,236,192\nv_mov_b32 v19, 0\nMvaa 136,232,196\nCV 42,84,85\nCV 43,86,87\nMvaa 140,236,196\nCV 44,88,89\nCV 45,90,91\nMvaa 144,232,200\nCV 46,92,93\nCV 47,94,95\n"
"Mvaa 148,236,200\nCV 48,96,97\nCV 49,98,99\nMvaa 152,232,204\nCV 50,100,101\nCV 51,102,103\nMvaa 156,236,204\nCV 52,104,105\nCV 53,106,107\nMvaa 160,232,208\nCV 54,108,109\n"
"CV 55,110,111\nMvaa 164,236,208\nCV 56,112,113\nSv 32,1,16,75,0\nMvaa 168,232,212\nCV 57,114,115\nCV 58,116,117\nMvaa 172,236,212\nCV 59,118,119\nCV 60,120,121\nMvaa 176,232,216\n"
"CV 61,122,123\nCV 62,124,125\nMvaa 180,236,216\nCV 63,126,127\nCV 96,128,129\nSv 36,1,16,75,1024\nMvaa 184,232,220\nCV 97,130,131\nCV 98,132,133\nMvaa 188,236,220\nCV 99,134,135\n"
"CV 100,136,137\nMaaa 0,240,192\nCV 101,138,139\nCV 102,140,141\nMaaa 4,244,192\nCV 103,142,143\nSv 40,1,16,75,2048\nMaaa 8,240,196\nCV 104,144,145\nCV 105,146,147\n"
"Maaa 12,244,196\nCV 106,148,149\nCV 107,150,151\nMaaa 16,240,200\nCV 108,152,153\nCV 109,154,155\nMaaa 20,244,200\nCV 110,156,157\nCV 111,158,159\nSv 44,1,16,75,3072\n"
"Maaa 24,240,204\nCV 112,160,161\nCV 113,162,163\nMaaa 28,244,204\nCV 114,164,165\nCV 115,166,167\ns_add_u32 s75, s75, s55\nMaaa 32,240,208\nCV 116,168,169\nCV 117,170,171\n"
"Maaa 36,244,208\nCV 118,172,173\nSv 48,1,16,75,0\nMaaa 40,240,212\nCV 119,174,175\nCV 120,176,177\nMaaa 44,244,212\nCV 121,178,179\nCV 122,180,181\nMaaa 48,240,216\n"
"CV 123,182,183\nCV 124,184,185\nMaaa 52,244,216\nCV 125,186,187\nCV 126,188,189\nSv 52,1,16,75,1024\nMaaa 56,240,220\nCV 127,190,191\nv_mov_b32 v86, 0\nMaaa 60,244,220\n"
"v_mov_b32 v87, 0\nv_mov_b32 v88, 0\nMaaa 64,248,192\nRa 128,24,0\nv_mov_b32 v89, 0\nMaaa 68,252,192\nSv 56,1,16,75,2048\nAR 92,0\nMaaa 72,248,196\nRa 132,24,1024\nAR 93,1\n"
"Maaa 76,252,196\nCV 128,92,93\nAR 92,2\nAR 93,3\nMaaa 80,248,200\nRa 136,24,2048\nCV 129,92,93\nMaaa 84,252,200\nSv 60,1,16,75,3072\nAR 92,4\nMaaa 88,248,204\nRa 140,24,3072\n"
"AR 93,5\nCV 130,92,93\nMaaa 92,252,204\nAR 92,6\nAR 93,7\nMaaa 96,248,208\nRa 144,26,0\nCV 131,92,93\nAR 92,8\nMaaa 100,252,208\nAR 93,9\nCV 132,92,93\nMaaa 104,248,212\n"
"Ra 148,26,1024\nAR 92,10\nMaaa 108,252,212\nAR 93,11\nCV 133,92,93\nAR 92,12\nMaaa 112,248,216\nRa 152,26,2048\nAR 93,13\nMaaa 116,252,216\nCV 134,92,93\nAR 92,14\n"
"Maaa 120,248,220\nRa 156,26,3072\nAR 93,15\nCV 135,92,93\nMaaa 124,252,220\nWL 4\nCvav 32,128,192,4\nRa 160,24,8192\nMvav 32,132,200\nMvav 32,136,208\nRa 164,24,9216\n"
"Mvav 32,140,216\nCvav 36,140,220,8\nRa 168,24,10240\nAR 92,16\nAR 93,17\nMvav 36,136,212\ns_add_u32 m0, s89, 32768\nCV 136,92,93\nAR 92,18\nAR 93,19\nMvav 36,132,204\n"
"Ra 172,24,11264\nWL 4\ns_barrier\nMvav 36,128,196\nCV 137,92,93\nAR 92,20\nAR 93,21\nCV 138,92,93\nMvav 36,144,228\nRa 176,26,8192\nAR 92,22\nAR 93,23\nSv 96,1,16,76,0\n"
"Mvav 36,148,236\ns_add_u32 s46, s86, s88\nCV 139,92,93\nAR 92,24\nMvav 36,152,244\nRa 180,26,9216\nAR 93,25\nCV 140,92,93\nAR 92,26\nMvav 36,156,252\nAR 93,27\nCV 141,92,93\n"
"AR 92,28\nMvav 32,156,248\nRa 184,26,10240\nAR 93,29\nCV 142,92,93\nAR 92,30\nMvav 32,152,240\nD1 1,16,46,0\nAR 93,31\nCV 143,92,93\nMvav 32,148,232\nRa 188,26,11264\nWL 4\n"
"AR 92,32\nAR 93,33\nMvav 32,144,224\nCV 144,92,93\nAR 92,34\nAR 93,35\nCvav 40,160,192,12\nCV 145,92,93\nAR 92,36\nAR 93,37\nCV 146,92,93\nMvav 40,164,200\nAR 92,38\nAR 93,39\n"
"CV 147,92,93\nMvav 40,168,208\nv_and_b32 v27, 0x70, v1\nbuffer_load_dword v27, v27, s[16:19], s61 offen offset:136 sc0 sc1\nAR 92,40\nMvav 40,172,216\nAR 93,41\nCV 148,92,93\n"
"AR 92,42\nAR 93,43\nCvav 44,172,220,16\nCV 149,92,93\nAR 92,44\nAR 93,45\nMvav 44,168,212\nD1 1,16,46,1024\nCV 150,92,93\nAR 92,46\nAR 93,47\nMvav 44,164,204\nWL 0\nCV 151,92,93\n"
"AR 92,48\nMvav 44,160,196\nAR 93,49\nCV 152,92,93\nAR 92,50\nAR 93,51\nMvav 44,176,228\nCV 153,92,93\nAR 92,52\nAR 93,53\nSv 100,1,16,76,1024\nMvav 44,180,236\n"
"s_add_u32 s88, s88, 32768\nEX 80,32\nMvav 44,184,244\nCV 154,92,93\nAR 92,54\nAR 93,55\nCV 155,92,93\nMvav 44,188,252\nEX 81,33\nMvav 40,188,248\nAR 92,56\nAR 93,57\nCV 156,92,93\n"
"AR 92,58\nMvav 40,184,240\nD1 1,16,46,2048\nAR 93,59\nCV 157,92,93\nMvav 40,180,232\nAR 92,60\nAR 93,61\nCV 158,92,93\nAR 92,62\nMvav 40,176,224\nAD 84,80,81\nCV 64,80,81\n"
"AR 93,63\nCV 159,92,93\nAR 92,64\nAR 93,65\nCV 160,92,93\nEX 82,34\nAR 92,66\nAR 93,67\nCV 161,92,93\nAR 92,68\nWV 14\nEX 83,35\nAR 93,69\nAD 84,84,82\nCV 162,92,93\nAR 92,70\n"
"D1 1,16,46,3072\ns_add_u32 m0, s89, 49152\nAR 93,71\nCV 163,92,93\ns_barrier\nAR 92,72\nAR 93,73\nAD 84,84,83\nCV 65,82,83\nCV 164,92,93\nAR 92,74\nRa 128,24,32768\nAR 93,75\n"
"CV 165,92,93\nSv 104,1,16,76,2048\ns_add_u32 s46, s87, s88\nEX 80,36\nRa 132,24,33792\nAR 92,76\nAR 93,77\nCV 166,92,93\nEX 81,37\nRa 136,24,34816\nAR 92,78\nAR 93,79\n"
"CV 167,92,93\nD1 1,16,46,0\nAR 92,80\nAR 93,81\nRa 140,24,35840\nCV 168,92,93\nAR 92,82\nAR 93,83\nAD 85,80,81\nCV 68,80,81\nCV 169,92,93\nRa 144,26,32768\nAR 92,84\nAR 93,85\n"
"CV 170,92,93\nEX 82,38\nAR 92,86\nRa 148,26,33792\nAR 93,87\nCV 171,92,93\nEX 83,39\nAR 92,88\nRa 152,26,34816\nAD 85,85,82\nAR 93,89\nD1 1,16,46,1024\nCV 172,92,93\nAR 92,90\n"
"AR 93,91\nRa 156,26,35840\nCV 173,92,93\nAR 92,92\nAD 85,85,83\nCV 69,82,83\nAR 93,93\nCV 174,92,93\nRa 160,24,40960\nAR 92,94\nAR 93,95\nSv 108,1,16,76,3072\n"
"s_xor_b32 s89, s89, 0x10000\nEX 80,40\nRa 164,24,41984\nCV 175,92,93\nAR 92,96\nAR 93,97\nWL 6\nEX 81,41\nRa 168,24,43008\nAD 84,84,80\nCV 176,92,93\nAR 92,98\nD1 1,16,46,2048\n"
"AR 93,99\nCV 177,92,93\nRa 172,24,44032\nAR 92,100\nAR 93,101\nCV 178,92,93\nAD 84,84,81\nCV 66,80,81\nAR 92,102\nCvav 48,128,192,4\nRa 176,26,40960\nAR 93,103\nCV 179,92,93\n"
"AR 92,104\nMvav 48,132,200\nEX 82,42\nAR 93,105\nMvav 48,136,208\nRa 180,26,41984\nCV 180,92,93\nAR 92,106\nMvav 48,140,216\nWL 6\nEX 83,43\nAR 93,107\nWV 9\n"
"v_cmp_ne_u32 vcc, s15, v27\ns_nop 0\ns_cbranch_vccnz .S161\n.S162:\nCvav 52,140,220,8\nRa 184,26,43008\nAD 84,84,82\nCV 181,92,93\nMvav 52,136,212\nD1 1,16,46,3072\n"
"s_add_u32 m0, s89, 0\nAR 92,108\nAR 93,109\nMvav 52,132,204\nRa 188,26,44032\nCV 182,92,93\nAR 92,110\nMvav 52,128,196\nAD 84,84,83\nCV 67,82,83\nAR 93,111\nCV 183,92,93\n"
"Mvav 52,144,228\nRa 128,2,49152\nAR 92,112\nAR 93,113\ns_add_u32 s76, s76, s55\nSv 112,1,16,76,0\nMvav 52,148,236\ns_add_u32 s46, s86, s88\nEX 80,44\nMvav 52,152,244\n"
"Ra 132,2,50176\nv_xor_b32 v24, 0x10000, v24\nCV 184,92,93\nAR 92,114\nMvav 52,156,252\nWL 6\nEX 81,45\nMvav 48,156,248\nRa 136,2,51200\nAD 85,85,80\nAR 93,115\nCV 185,92,93\n"
"Mvav 48,152,240\nD1 1,16,46,0\nAR 92,116\nAR 93,117\nMvav 48,148,232\nRa 140,2,52224\nCV 186,92,93\nAR 92,118\nAR 93,119\nMvav 48,144,224\nAD 85,85,81\nCV 70,80,81\nCV 187,92,93\n"
"Cvav 56,160,192,12\nRa 144,2,53248\nAR 92,120\nAR 93,121\nCV 188,92,93\nMvav 56,164,200\nEX 82,46\nAR 92,122\nMvav 56,168,208\nRa 148,2,54272\nv_xor_b32 v26, 0x10000, v26\n"
"AR 93,123\nMvav 56,172,216\nWL 6\nEX 83,47\nCV 189,92,93\nCvav 60,172,220,16\nRa 152,2,55296\nAD 85,85,82\nAR 92,124\nMvav 60,168,212\nD1 1,16,46,1024\nAR 93,125\nCV 190,92,93\n"
"AR 92,126\nMvav 60,164,204\nRa 156,2,56320\nAR 93,127\nCV 191,92,93\nMvav 60,160,196\nAD 85,85,83\nCV 71,82,83\nMvav 60,176,228\nRa 160,2,57344\nv_max_f32 v90, v84, v85\n"
"Sv 116,1,16,76,1024\nMvav 60,180,236\ns_add_u32 s88, s88, 32768\nEX 80,48\nMvav 60,184,244\nRa 164,2,58368\nv_cmp_lt_f32 vcc, s63, v90\nMvav 60,188,252\nWL 8\nEX 81,49\n"
"Mvav 56,188,248\nRa 168,2,59392\ns_cbranch_vccnz .T144\n.T145:\nv_mov_b32 v20, v84\nv_mov_b32 v21, v85\nv_mov_b32 v22, v20\nv_mov_b32 v23, v21\nMvav 56,184,240\nD1 1,16,46,2048\n"
"v_permlane32_swap_b32 v20, v22\nv_permlane32_swap_b32 v21, v23\nAD 20,20,22\nAD 21,21,23\nv_mov_b32 v22, v20\nv_mov_b32 v23, v21\nMvav 56,180,232\nRa 172,2,60416\n"
"v_permlane16_swap_b32 v20, v22\nv_permlane16_swap_b32 v21, v23\nAD 20,20,22\nAD 21,21,23\nv_min_f32 v90, v20, v21\nv_cmp_gt_f32 vcc, s64, v90\ns_nop 0\ns_cbranch_vccnz .T146\n"
".T147:\nAD 86,86,84\nMvav 56,176,224\nAD 84,80,81\nCV 72,80,81\nZaav 0,128,64\nRa 176,2,61440\nZaav 4,128,68\nEX 82,50\nZaav 12,132,68\nRa 180,2,62464\nZaav 8,132,64\nWL 8\n"
"EX 83,51\nZaav 16,136,64\nRa 184,2,63488\nAD 84,84,82\nZaav 20,136,68\nD1 1,16,46,3072\ns_add_u32 m0, s89, 16384\nZaav 28,140,68\nRa 188,2,64512\ns_barrier\nZaav 24,140,64\n"
"AD 84,84,83\nCV 73,82,83\nZaav 32,144,64\nRa 128,24,0\nSv 120,1,16,76,2048\nZaav 36,144,68\ns_add_u32 s46, s87, s88\nEX 80,52\n"
"buffer_store_dword v25, off, s[16:19], s54 offset:12 sc0 sc1\nZaav 44,148,68\nRa 132,24,1024\nv_xor_b32 v2, 0x10000, v2\nZaav 40,148,64\nWL 8\nEX 81,53\nZaav 48,152,64\n"
"Ra 136,24,2048\nAD 87,87,85\nZaav 52,152,68\nD1 1,16,46,0\nZaav 60,156,68\nRa 140,24,3072\nZaav 56,156,64\nAD 85,80,81\nCV 76,80,81\nZaav 64,160,64\nRa 144,26,0\nZaav 68,160,68\n"
"EX 82,54\nZaav 76,164,68\nRa 148,26,1024\nZaav 72,164,64\nWL 8\nEX 83,55\nZaav 80,168,64\nRa 152,26,2048\nAD 85,85,82\nZaav 84,168,68\nD1 1,16,46,1024\nZaav 92,172,68\n"
"Ra 156,26,3072\nZaav 88,172,64\nAD 85,85,83\nCV 77,82,83\nZaav 96,176,64\nRa 160,24,8192\nSv 124,1,16,76,3072\nZaav 100,176,68\nEX 80,56\nZaav 108,180,68\nRa 164,24,9216\n"
"Zaav 104,180,64\nWL 6\nEX 81,57\nZaav 112,184,64\nRa 168,24,10240\nAD 84,84,80\nZaav 116,184,68\nD1 1,16,46,2048\nZaav 124,188,68\nRa 172,24,11264\nZaav 120,188,64\nAD 84,84,81\n"
"CV 74,80,81\nCvav 32,128,192,4\nRa 176,26,8192\nMvav 32,132,200\nEX 82,58\nMvav 32,136,208\nRa 180,26,9216\nMvav 32,140,216\nWL 6\nEX 83,59\nCvav 36,140,220,8\nRa 184,26,10240\n"
"AD 84,84,82\nMvav 36,136,212\nD1 1,16,46,3072\ns_add_u32 m0, s89, 32768\nMvav 36,132,204\nRa 188,26,11264\nMvav 36,128,196\nAD 84,84,83\nCV 75,82,83\nMvav 36,144,228\n"
"Ra 128,2,16384\nSv 128,1,16,77,0\nMvav 36,148,236\ns_add_u32 s46, s86, s88\nEX 80,60\nMvav 36,152,244\nRa 132,2,17408\nMvav 36,156,252\nWL 6\nEX 81,61\nMvav 32,156,248\n"
"Ra 136,2,18432\nAD 85,85,80\nMvav 32,152,240\nD1 1,16,46,0\nMvav 32,148,232\nRa 140,2,19456\nMvav 32,144,224\nAD 85,85,81\nCV 78,80,81\nCvav 40,160,192,12\nRa 144,2,20480\n"
"Mvav 40,164,200\nEX 82,62\nMvav 40,168,208\nRa 148,2,21504\nMvav 40,172,216\nWL 6\nEX 83,63\nCvav 44,172,220,16\nRa 152,2,22528\nAD 85,85,82\nMvav 44,168,212\nD1 1,16,46,1024\n"
"Mvav 44,164,204\nRa 156,2,23552\nMvav 44,160,196\nAD 85,85,83\nCV 79,82,83\nMvav 44,176,228\nRa 160,2,24576\nv_max_f32 v90, v84, v85\nSv 132,1,16,77,1024\nMvav 44,180,236\n"
"s_add_u32 s88, s88, 32768\nEX 80,32\nMvav 44,184,244\nRa 164,2,25600\nv_cmp_lt_f32 vcc, s63, v90\nMvav 44,188,252\nWL 8\nEX 81,33\nMvav 40,188,248\nRa 168,2,26624\n"
"s_cbranch_vccnz .T149\n.T150:\nMvav 40,184,240\nD1 1,16,46,2048\nMvav 40,180,232\nRa 172,2,27648\nAD 86,86,84\nMvav 40,176,224\nAD 84,80,81\nCV 64,80,81\nMaav 0,128,72\n"
"Ra 176,2,28672\nv_mov_b32 v112, -1\nMaav 4,128,76\nEX 82,34\nMaav 12,132,76\nRa 180,2,29696\nMaav 8,132,72\ns_waitcnt vmcnt(18) lgkmcnt(8)\nEX 83,35\nMaav 16,136,72\n"
"Ra 184,2,30720\nAD 84,84,82\nMaav 20,136,76\nD1 1,16,46,3072\ns_add_u32 m0, s89, 49152\nMaav 28,140,76\nRa 188,2,31744\ns_barrier\nMaav 24,140,72\nAD 84,84,83\nCV 65,82,83\n"
"Maav 32,144,72\nRa 128,24,32768\nSv 136,1,16,77,2048\nMaav 36,144,76\ns_add_u32 s46, s87, s88\nEX 80,36\nMaav 44,148,76\nRa 132,24,33792\nMaav 40,148,72\nWL 8\nEX 81,37\n"
"Maav 48,152,72\nRa 136,24,34816\nAD 87,87,85\nMaav 52,152,76\nD1 1,16,46,0\nMaav 60,156,76\nRa 140,24,35840\nMaav 56,156,72\nAD 85,80,81\nCV 68,80,81\nMaav 64,160,72\n"
"Ra 144,26,32768\nv_mov_b32 v113, -1\nMaav 68,160,76\nEX 82,38\nMaav 76,164,76\nRa 148,26,33792\nMaav 72,164,72\nWL 8\nEX 83,39\nMaav 80,168,72\nRa 152,26,34816\nAD 85,85,82\n"
"Maav 84,168,76\nD1 1,16,46,1024\nMaav 92,172,76\nRa 156,26,35840\nMaav 88,172,72\nAD 85,85,83\nCV 69,82,83\nMaav 96,176,72\nRa 160,24,40960\nSv 140,1,16,77,3072\nMaav 100,176,76\n"
"s_xor_b32 s89, s89, 0x10000\nEX 80,40\nMaav 108,180,76\nRa 164,24,41984\nMaav 104,180,72\nWL 6\nEX 81,41\nMaav 112,184,72\nRa 168,24,43008\nAD 84,84,80\nMaav 116,184,76\n"
"D1 1,16,46,2048\nMaav 124,188,76\nRa 172,24,44032\nMaav 120,188,72\nAD 84,84,81\nCV 66,80,81\nCvav 48,128,192,4\nRa 176,26,40960\nv_mov_b32 v114, -1\nMvav 48,132,200\nEX 82,42\n"
"Mvav 48,136,208\nRa 180,26,41984\nMvav 48,140,216\nWL 6\nEX 83,43\nCvav 52,140,220,8\nRa 184,26,43008\nAD 84,84,82\nv_and_b32 v27, 0x70, v1\n"
"buffer_load_dword v27, v27, s[16:19], s61 offen offset:140 sc0 sc1\nMvav 52,136,212\nD1 1,16,46,3072\ns_add_u32 m0, s89, 0\nMvav 52,132,204\nRa 188,26,44032\nMvav 52,128,196\n"
"AD 84,84,83\nCV 67,82,83\nMvav 52,144,228\nRa 128,2,49152\ns_add_u32 s77, s77, s55\nSv 144,1,16,77,0\nMvav 52,148,236\ns_add_u32 s46, s86, s88\nEX 80,44\nMvav 52,152,244\n"
"Ra 132,2,50176\nv_xor_b32 v24, 0x10000, v24\nMvav 52,156,252\nWL 6\nEX 81,45\nMvav 48,156,248\nRa 136,2,51200\nAD 85,85,80\nMvav 48,152,240\nD1 1,16,46,0\nMvav 48,148,232\n"
"Ra 140,2,52224\nMvav 48,144,224\nAD 85,85,81\nCV 70,80,81\nCvav 56,160,192,12\nRa 144,2,53248\nv_mov_b32 v115, -1\nMvav 56,164,200\nEX 82,46\nMvav 56,168,208\nRa 148,2,54272\n"
"v_xor_b32 v26, 0x10000, v26\nMvav 56,172,216\nWL 6\nEX 83,47\nCvav 60,172,220,16\nRa 152,2,55296\nAD 85,85,82\nMvav 60,168,212\nD1 1,16,46,1024\nMvav 60,164,204\nRa 156,2,56320\n"
"Mvav 60,160,196\nAD 85,85,83\nCV 71,82,83\nMvav 60,176,228\nRa 160,2,57344\nv_max_f32 v90, v84, v85\nSv 148,1,16,77,1024\nMvav 60,180,236\ns_add_u32 s88, s88, 32768\nEX 80,48\n"
"Mvav 60,184,244\nRa 164,2,58368\nv_cmp_lt_f32 vcc, s63, v90\nMvav 60,188,252\nWL 8\nEX 81,49\nMvav 56,188,248\nRa 168,2,59392\ns_cbranch_vccnz .T151\n.T152:\nMvav 56,184,240\n"
"D1 1,16,46,2048\nMvav 56,180,232\nRa 172,2,60416\nAD 86,86,84\nMvav 56,176,224\nAD 84,80,81\nCV 72,80,81\nMaav 0,128,64\nRa 176,2,61440\nMaav 4,128,68\nEX 82,50\nMaav 12,132,68\n"
"Ra 180,2,62464\nMaav 8,132,64\ns_waitcnt vmcnt(18) lgkmcnt(8)\nEX 83,51\nMaav 16,136,64\nRa 184,2,63488\nAD 84,84,82\nMaav 20,136,68\nD1 1,16,46,3072\ns_add_u32 m0, s89, 16384\n"
"Maav 28,140,68\nRa 188,2,64512\ns_barrier\nMaav 24,140,64\nAD 84,84,83\nCV 73,82,83\nMaav 32,144,64\nRa 128,24,0\nSv 152,1,16,77,2048\nMaav 36,144,68\ns_add_u32 s46, s87, s88\n"
"EX 80,52\nMaav 44,148,68\nRa 132,24,1024\nv_xor_b32 v2, 0x10000, v2\nMaav 40,148,64\nWL 8\nEX 81,53\nMaav 48,152,64\nRa 136,24,2048\nAD 87,87,85\n"
"buffer_store_dword v25, off, s[16:19], s54 offset:8 sc0 sc1\nMaav 52,152,68\nD1 1,16,46,0\nMaav 60,156,68\nRa 140,24,3072\nMaav 56,156,64\nAD 85,80,81\nCV 76,80,81\n"
"Maav 64,160,64\nRa 144,26,0\nMaav 68,160,68\nEX 82,54\nMaav 76,164,68\nRa 148,26,1024\nMaav 72,164,64\nWL 8\nEX 83,55\nMaav 80,168,64\nRa 152,26,2048\nAD 85,85,82\nMaav 84,168,68\n"
"D1 1,16,46,1024\nMaav 92,172,68\nRa 156,26,3072\nMaav 88,172,64\nAD 85,85,83\nCV 77,82,83\nMaav 96,176,64\nRa 160,24,8192\nSv 156,1,16,77,3072\nMaav 100,176,68\nEX 80,56\n"
"Maav 108,180,68\nRa 164,24,9216\nMaav 104,180,64\nWL 6\nEX 81,57\nMaav 112,184,64\nRa 168,24,10240\nAD 84,84,80\nMaav 116,184,68\nD1 1,16,46,2048\nMaav 124,188,68\n"
"Ra 172,24,11264\nMaav 120,188,64\nAD 84,84,81\nCV 74,80,81\nCvav 32,128,192,4\nRa 176,26,8192\nMvav 32,132,200\nEX 82,58\nMvav 32,136,208\nRa 180,26,9216\nMvav 32,140,216\nWL 6\n"
"EX 83,59\nCvav 36,140,220,8\nRa 184,26,10240\nAD 84,84,82\nMvav 36,136,212\nD1 1,16,46,3072\ns_add_u32 m0, s89, 32768\nMvav 36,132,204\nRa 188,26,11264\nMvav 36,128,196\n"
"AD 84,84,83\nCV 75,82,83\nMvav 36,144,228\nRa 128,2,16384\nSv 160,1,16,78,0\nMvav 36,148,236\ns_add_u32 s46, s86, s88\nEX 80,60\nMvav 36,152,244\nRa 132,2,17408\nMvav 36,156,252\n"
"WL 6\nEX 81,61\nMvav 32,156,248\nRa 136,2,18432\nAD 85,85,80\nMvav 32,152,240\nD1 1,16,46,0\nMvav 32,148,232\nRa 140,2,19456\nSv 112,1,16,49,0\nMvav 32,144,224\nAD 85,85,81\n"
"CV 78,80,81\nCvav 40,160,192,12\nRa 144,2,20480\nMvav 40,164,200\nEX 82,62\nMvav 40,168,208\nRa 148,2,21504\nMvav 40,172,216\nWL 6\nEX 83,63\nCvav 44,172,220,16\nRa 152,2,22528\n"
"AD 85,85,82\nMvav 44,168,212\nD1 1,16,46,1024\nMvav 44,164,204\nRa 156,2,23552\nMvav 44,160,196\nAD 85,85,83\nCV 79,82,83\nMvav 44,176,228\nRa 160,2,24576\n"
"v_max_f32 v90, v84, v85\nSv 164,1,16,78,1024\nMvav 44,180,236\ns_add_u32 s88, s88, 32768\nEX 80,32\nMvav 44,184,244\nRa 164,2,25600\nv_cmp_lt_f32 vcc, s63, v90\nMvav 44,188,252\n"
"WL 8\nEX 81,33\nMvav 40,188,248\nRa 168,2,26624\ns_cbranch_vccnz .T154\n.T155:\nMvav 40,184,240\nD1 1,16,46,2048\nMvav 40,180,232\nRa 172,2,27648\nAD 86,86,84\nMvav 40,176,224\n"
"AD 84,80,81\nCV 64,80,81\nMaav 0,128,72\nRa 176,2,28672\nMaav 4,128,76\nEX 82,34\nMaav 12,132,76\nRa 180,2,29696\nMaav 8,132,72\ns_waitcnt vmcnt(19) lgkmcnt(8)\nEX 83,35\n"
"Maav 16,136,72\nRa 184,2,30720\nAD 84,84,82\nMaav 20,136,76\nD1 1,16,46,3072\ns_add_u32 m0, s89, 49152\nMaav 28,140,76\nRa 188,2,31744\ns_barrier\nMaav 24,140,72\nAD 84,84,83\n"
"CV 65,82,83\nMaav 32,144,72\nRa 128,24,32768\nSv 168,1,16,78,2048\nMaav 36,144,76\ns_add_u32 s46, s87, s88\nEX 80,36\nMaav 44,148,76\nRa 132,24,33792\nMaav 40,148,72\nWL 8\n"
"EX 81,37\nMaav 48,152,72\nRa 136,24,34816\nAD 87,87,85\nMaav 52,152,76\nD1 1,16,46,0\nMaav 60,156,76\nRa 140,24,35840\nMaav 56,156,72\nAD 85,80,81\nCV 68,80,81\nMaav 64,160,72\n"
"Ra 144,26,32768\nMaav 68,160,76\nEX 82,38\nMaav 76,164,76\nRa 148,26,33792\nMaav 72,164,72\nWL 8\nEX 83,39\nMaav 80,168,72\nRa 152,26,34816\nAD 85,85,82\nMaav 84,168,76\n"
"D1 1,16,46,1024\nMaav 92,172,76\nRa 156,26,35840\nMaav 88,172,72\nAD 85,85,83\nCV 69,82,83\nMaav 96,176,72\nRa 160,24,40960\nSv 172,1,16,78,3072\nMaav 100,176,76\n"
"s_xor_b32 s89, s89, 0x10000\nEX 80,40\nMaav 108,180,76\nRa 164,24,41984\nMaav 104,180,72\nWL 6\nEX 81,41\nMaav 112,184,72\nRa 168,24,43008\nAD 84,84,80\nMaav 116,184,76\n"
"D1 1,16,46,2048\nMaav 124,188,76\nRa 172,24,44032\nMaav 120,188,72\nAD 84,84,81\nCV 66,80,81\nCvav 48,128,192,4\nRa 176,26,40960\nMvav 48,132,200\nEX 82,42\nMvav 48,136,208\n"
"Ra 180,26,41984\nMvav 48,140,216\nWL 6\nEX 83,43\nv_cmp_ne_u32 vcc, s15, v27\ns_nop 0\ns_cbranch_vccnz .S158\n.S159:\nv_and_b32 v27, 0x70, v1\n"
"buffer_load_dword v27, v27, s[16:19], s61 offen offset:12 sc0 sc1\nCvav 52,140,220,8\nRa 184,26,43008\nAD 84,84,82\nMvav 52,136,212\nD1 1,16,46,3072\ns_add_u32 m0, s89, 0\n"
"Mvav 52,132,204\nRa 188,26,44032\nMvav 52,128,196\nAD 84,84,83\nCV 67,82,83\nMvav 52,144,228\nRa 128,2,49152\ns_add_u32 s78, s78, s55\nSv 176,1,16,78,0\nMvav 52,148,236\n"
"s_add_u32 s46, s86, s88\nEX 80,44\nMvav 52,152,244\nRa 132,2,50176\nv_xor_b32 v24, 0x10000, v24\nMvav 52,156,252\nWL 6\nEX 81,45\nMvav 48,156,248\nRa 136,2,51200\nAD 85,85,80\n"
"Mvav 48,152,240\nD1 1,16,46,0\nv_lshrrev_b32 v92, 8, v1\nMvav 48,148,232\nRa 140,2,52224\nSv 112,1,16,49,1024\nMvav 48,144,224\nAD 85,85,81\nCV 70,80,81\nCvav 56,160,192,12\n"
"Ra 144,2,53248\nv_bfe_u32 v93, v1, 6, 2\nMvav 56,164,200\ns_add_u32 s49, s49, 131072\nEX 82,46\nMvav 56,168,208\nRa 148,2,54272\nv_xor_b32 v26, 0x10000, v26\nMvav 56,172,216\n"
"WL 6\nEX 83,47\nCvav 60,172,220,16\nRa 152,2,55296\nAD 85,85,82\nMvav 60,168,212\nD1 1,16,46,1024\nv_cmp_eq_u32 vcc, v92, v93\nv_and_b32 v92, 16, v1\nv_mov_b32 v93, 0x3f80\n"
"v_cndmask_b32 v93, 0, v93, vcc\nMvav 60,164,204\nRa 156,2,56320\nMvav 60,160,196\nAD 85,85,83\nCV 71,82,83\nMvav 60,176,228\nRa 160,2,57344\nv_max_f32 v90, v84, v85\n"
"Sv 180,1,16,78,1024\nMvav 60,180,236\ns_add_u32 s88, s88, 32768\nEX 80,48\nMvav 60,184,244\nRa 164,2,58368\nv_cmp_lt_f32 vcc, s63, v90\nMvav 60,188,252\nWL 8\nEX 81,49\n"
"Mvav 56,188,248\nRa 168,2,59392\ns_cbranch_vccnz .T156\n.T157:\nMvav 56,184,240\nD1 1,16,46,2048\nv_lshlrev_b32 v93, v92, v93\nMvav 56,180,232\nRa 172,2,60416\nAD 86,86,84\n"
"Mvav 56,176,224\nAD 84,80,81\nCV 72,80,81\nMaav 0,128,64\nRa 176,2,61440\nv_and_b32 v92, 32, v1\nMaav 4,128,68\nEX 82,50\nMaav 12,132,68\nRa 180,2,62464\nv_cmp_eq_u32 vcc, 0, v92\n"
"v_mov_b32 v128, 0\nv_mov_b32 v129, 0\nv_cndmask_b32 v130, 0, v93, vcc\nMaav 8,132,64\ns_waitcnt vmcnt(20) lgkmcnt(8)\nEX 83,51\nMaav 16,136,64\nRa 184,2,63488\nAD 84,84,82\n"
"Maav 20,136,68\nD1 1,16,46,3072\ns_add_u32 m0, s89, 16384\nMaav 28,140,68\nRa 188,2,64512\ns_barrier\nMaav 24,140,64\nAD 84,84,83\nCV 73,82,83\nMaav 32,144,64\nRa 128,24,0\n"
"Sv 184,1,16,78,2048\nMaav 36,144,68\ns_add_u32 s46, s87, s88\nEX 80,52\nMaav 44,148,68\nRa 132,24,1024\nv_xor_b32 v2, 0x10000, v2\nMaav 40,148,64\nWL 8\nEX 81,53\nMaav 48,152,64\n"
"Ra 136,24,2048\nAD 87,87,85\nMaav 52,152,68\nD1 1,16,46,0\nv_sub_u32 v131, v93, v130\nMaav 60,156,68\nRa 140,24,3072\nMaav 56,156,64\nAD 85,80,81\nCV 76,80,81\nMaav 64,160,64\n"
"Ra 144,26,0\nv_mov_b32 v132, 0\nMaav 68,160,68\nEX 82,54\nMaav 76,164,68\nRa 148,26,1024\nMaav 72,164,64\nWL 8\nEX 83,55\nMaav 80,168,64\nRa 152,26,2048\nAD 85,85,82\n"
"Maav 84,168,68\nD1 1,16,46,1024\nMaav 92,172,68\nRa 156,26,3072\nMaav 88,172,64\nAD 85,85,83\nCV 77,82,83\nMaav 96,176,64\nRa 160,24,8192\nSv 188,1,16,78,3072\nMaav 100,176,68\n"
"EX 80,56\nMaav 108,180,68\nRa 164,24,9216\nv_mov_b32 v133, 0\nMaav 104,180,64\nWL 6\nEX 81,57\nMaav 112,184,64\nRa 168,24,10240\nAD 84,84,80\nMaav 116,184,68\nD1 1,16,46,2048\n"
"Maav 124,188,68\nRa 172,24,11264\nMaav 120,188,64\nAD 84,84,81\nCV 74,80,81\ns_mov_b32 s99, 11\ns_sub_u32 s90, s90, 3\n.p2align 6\n.A164:\nCvav 32,128,192,4\nRa 176,26,8192\n"
"Mvav 32,132,200\nEX 82,58\nMvav 32,136,208\nRa 180,26,9216\nMvav 32,140,216\nWL 6\nEX 83,59\nCvav 36,140,220,8\nRa 184,26,10240\nAD 84,84,82\nMvav 36,136,212\nD1 1,16,46,3072\n"
"s_add_u32 m0, s89, 32768\nMvav 36,132,204\nRa 188,26,11264\nMvav 36,128,196\nAD 84,84,83\nCV 75,82,83\nMvav 36,144,228\nRa 128,2,16384\nMvav 36,148,236\ns_add_u32 s46, s86, s88\n"
"EX 80,60\nMvav 36,152,244\nRa 132,2,17408\nMvav 36,156,252\nWL 6\nEX 81,61\nMvav 32,156,248\nRa 136,2,18432\nAD 85,85,80\nMvav 32,152,240\nD1 1,16,46,0\nMvav 32,148,232\n"
"Ra 140,2,19456\nSv 112,1,16,49,0\nMvav 32,144,224\nAD 85,85,81\nCV 78,80,81\nCvav 40,160,192,12\nRa 144,2,20480\nMvav 40,164,200\nEX 82,62\nMvav 40,168,208\nRa 148,2,21504\n"
"Mvav 40,172,216\nWL 6\nEX 83,63\nCvav 44,172,220,16\nRa 152,2,22528\nAD 85,85,82\nMvav 44,168,212\nD1 1,16,46,1024\nMvav 44,164,204\nRa 156,2,23552\nMvav 44,160,196\nAD 85,85,83\n"
"CV 79,82,83\nMvav 44,176,228\nRa 160,2,24576\nv_max_f32 v90, v84, v85\nMvav 44,180,236\ns_add_u32 s88, s88, 32768\nEX 80,32\nMvav 44,184,244\nRa 164,2,25600\n"
"v_cmp_lt_f32 vcc, s63, v90\nMvav 44,188,252\nWL 8\nEX 81,33\nMvav 40,188,248\nRa 168,2,26624\ns_cbranch_vccnz .T168\n.T169:\nMvav 40,184,240\nD1 1,16,46,2048\nMvav 40,180,232\n"
"Ra 172,2,27648\nAD 86,86,84\nMvav 40,176,224\nAD 84,80,81\nCV 64,80,81\nMaav 0,128,72\nRa 176,2,28672\nMaav 4,128,76\nEX 82,34\nMaav 12,132,76\nRa 180,2,29696\nMaav 8,132,72\n"
"s_waitcnt vmcnt(13) lgkmcnt(8)\nEX 83,35\nMaav 16,136,72\nRa 184,2,30720\nAD 84,84,82\nMaav 20,136,76\nD1 1,16,46,3072\ns_add_u32 m0, s89, 49152\nMaav 28,140,76\nRa 188,2,31744\n"
"s_barrier\nMaav 24,140,72\nAD 84,84,83\nCV 65,82,83\nMaav 32,144,72\nRa 128,24,32768\nMaav 36,144,76\ns_add_u32 s46, s87, s88\nEX 80,36\nMaav 44,148,76\nRa 132,24,33792\n"
"Maav 40,148,72\nWL 8\nEX 81,37\nMaav 48,152,72\nRa 136,24,34816\nAD 87,87,85\nMaav 52,152,76\nD1 1,16,46,0\nMaav 60,156,76\nRa 140,24,35840\nMaav 56,156,72\nAD 85,80,81\n"
"CV 68,80,81\nMaav 64,160,72\nRa 144,26,32768\nMaav 68,160,76\nEX 82,38\nMaav 76,164,76\nRa 148,26,33792\nMaav 72,164,72\nWL 8\nEX 83,39\nMaav 80,168,72\nRa 152,26,34816\n"
"AD 85,85,82\nMaav 84,168,76\nD1 1,16,46,1024\nMaav 92,172,76\nRa 156,26,35840\nMaav 88,172,72\nAD 85,85,83\nCV 69,82,83\nMaav 96,176,72\nRa 160,24,40960\nMaav 100,176,76\n"
"s_xor_b32 s89, s89, 0x10000\nEX 80,40\nMaav 108,180,76\nRa 164,24,41984\nMaav 104,180,72\nWL 6\nEX 81,41\nMaav 112,184,72\nRa 168,24,43008\nAD 84,84,80\nMaav 116,184,76\n"
"D1 1,16,46,2048\nMaav 124,188,76\nRa 172,24,44032\nMaav 120,188,72\nAD 84,84,81\nCV 66,80,81\nCvav 48,128,192,4\nRa 176,26,40960\nMvav 48,132,200\nEX 82,42\nMvav 48,136,208\n"
"Ra 180,26,41984\nMvav 48,140,216\nWL 6\nEX 83,43\nCvav 52,140,220,8\nRa 184,26,43008\nAD 84,84,82\nMvav 52,136,212\nD1 1,16,46,3072\ns_add_u32 m0, s89, 0\nMvav 52,132,204\n"
"Ra 188,26,44032\nMvav 52,128,196\nAD 84,84,83\nCV 67,82,83\nMvav 52,144,228\nRa 128,2,49152\nMvav 52,148,236\ns_add_u32 s46, s86, s88\nEX 80,44\nMvav 52,152,244\nRa 132,2,50176\n"
"v_xor_b32 v24, 0x10000, v24\nMvav 52,156,252\nWL 6\nEX 81,45\nMvav 48,156,248\nRa 136,2,51200\nAD 85,85,80\nMvav 48,152,240\nD1 1,16,46,0\nMvav 48,148,232\nRa 140,2,52224\n"
"Sv 112,1,16,49,1024\nMvav 48,144,224\nAD 85,85,81\nCV 70,80,81\nCvav 56,160,192,12\nRa 144,2,53248\nMvav 56,164,200\ns_add_u32 s49, s49, 131072\nEX 82,46\nMvav 56,168,208\n"
"Ra 148,2,54272\nv_xor_b32 v26, 0x10000, v26\nMvav 56,172,216\nWL 6\nEX 83,47\nCvav 60,172,220,16\nRa 152,2,55296\nAD 85,85,82\nMvav 60,168,212\nD1 1,16,46,1024\nMvav 60,164,204\n"
"Ra 156,2,56320\nMvav 60,160,196\nAD 85,85,83\nCV 71,82,83\nMvav 60,176,228\nRa 160,2,57344\nv_max_f32 v90, v84, v85\nMvav 60,180,236\ns_add_u32 s88, s88, 32768\nEX 80,48\n"
"Mvav 60,184,244\nRa 164,2,58368\nv_cmp_lt_f32 vcc, s63, v90\nMvav 60,188,252\nWL 8\nEX 81,49\nMvav 56,188,248\nRa 168,2,59392\ns_cbranch_vccnz .T170\n.T171:\nMvav 56,184,240\n"
"D1 1,16,46,2048\nMvav 56,180,232\nRa 172,2,60416\nAD 86,86,84\nMvav 56,176,224\nAD 84,80,81\nCV 72,80,81\nMaav 0,128,64\nRa 176,2,61440\nMaav 4,128,68\nEX 82,50\nMaav 12,132,68\n"
"Ra 180,2,62464\nMaav 8,132,64\ns_waitcnt vmcnt(13) lgkmcnt(8)\nEX 83,51\nMaav 16,136,64\nRa 184,2,63488\nAD 84,84,82\nMaav 20,136,68\nD1 1,16,46,3072\ns_add_u32 m0, s89, 16384\n"
"Maav 28,140,68\nRa 188,2,64512\ns_barrier\nMaav 24,140,64\nAD 84,84,83\nCV 73,82,83\nMaav 32,144,64\nRa 128,24,0\nMaav 36,144,68\ns_add_u32 s46, s87, s88\nEX 80,52\n"
"Maav 44,148,68\nRa 132,24,1024\nv_xor_b32 v2, 0x10000, v2\nMaav 40,148,64\nWL 8\nEX 81,53\nMaav 48,152,64\nRa 136,24,2048\nAD 87,87,85\nMaav 52,152,68\nD1 1,16,46,0\n"
"Maav 60,156,68\nRa 140,24,3072\nMaav 56,156,64\nAD 85,80,81\nCV 76,80,81\nMaav 64,160,64\nRa 144,26,0\nMaav 68,160,68\nEX 82,54\nMaav 76,164,68\nRa 148,26,1024\nMaav 72,164,64\n"
"WL 8\nEX 83,55\nMaav 80,168,64\nRa 152,26,2048\nAD 85,85,82\nMaav 84,168,68\nD1 1,16,46,1024\nMaav 92,172,68\nRa 156,26,3072\nMaav 88,172,64\nAD 85,85,83\nCV 77,82,83\n"
"Maav 96,176,64\nRa 160,24,8192\nMaav 100,176,68\ns_sub_u32 s90, s90, 1\nEX 80,56\nMaav 108,180,68\nRa 164,24,9216\ns_cbranch_scc1 .E166\n.E167:\nMaav 104,180,64\nWL 6\nEX 81,57\n"
"Maav 112,184,64\nRa 168,24,10240\nAD 84,84,80\nMaav 116,184,68\nD1 1,16,46,2048\ns_sub_u32 s99, s99, 1\nMaav 124,188,68\nRa 172,24,11264\nMaav 120,188,64\nAD 84,84,81\n"
"CV 74,80,81\ns_cbranch_scc0 .A164\nCvav 32,128,192,4\nRa 176,26,8192\nMvav 32,132,200\nEX 82,58\nMvav 32,136,208\nRa 180,26,9216\nMvav 32,140,216\nWL 6\nEX 83,59\n"
"Cvav 36,140,220,8\nRa 184,26,10240\nAD 84,84,82\nMvav 36,136,212\nD1 1,16,46,3072\ns_add_u32 m0, s89, 32768\nMvav 36,132,204\nRa 188,26,11264\nMvav 36,128,196\nAD 84,84,83\n"
"CV 75,82,83\ns_mov_b32 s94, s96\ns_add_u32 s95, s96, 65536\nv_and_b32 v108, 0xf0, v1\nMvav 36,144,228\nRa 128,2,16384\nMvav 36,148,236\ns_add_u32 s46, s86, s88\nEX 80,60\n"
"Mvav 36,152,244\nRa 132,2,17408\nMvav 36,156,252\nWL 6\nEX 81,61\nMvav 32,156,248\nRa 136,2,18432\nAD 85,85,80\nMvav 32,152,240\nD1 1,16,46,0\nMvav 32,148,232\nRa 140,2,19456\n"
"Sv 112,1,16,49,0\nMvav 32,144,224\nAD 85,85,81\nCV 78,80,81\nbuffer_load_dwordx4 v[100:103], v108, s[16:19], s65 offen offset:0 sc0 sc1\n"
"buffer_load_dwordx4 v[104:107], v108, s[16:19], s65 offen offset:256 sc0 sc1\nCvav 40,160,192,12\nRa 144,2,20480\nMvav 40,164,200\nEX 82,62\nMvav 40,168,208\nRa 148,2,21504\n"
"Mvav 40,172,216\nWL 6\nEX 83,63\nCvav 44,172,220,16\nRa 152,2,22528\nAD 85,85,82\nMvav 44,168,212\nD1 1,16,46,1024\nMvav 44,164,204\nRa 156,2,23552\nMvav 44,160,196\nAD 85,85,83\n"
"CV 79,82,83\nMvav 44,176,228\nRa 160,2,24576\nv_max_f32 v90, v84, v85\nMvav 44,180,236\ns_add_u32 s88, s88, 32768\nEX 80,32\nMvav 44,184,244\nRa 164,2,25600\n"
"v_cmp_lt_f32 vcc, s63, v90\nMvav 44,188,252\nWL 8\nEX 81,33\nMvav 40,188,248\nRa 168,2,26624\ns_cbranch_vccnz .T175\n.T176:\nMvav 40,184,240\nD1 1,16,46,2048\nMvav 40,180,232\n"
"Ra 172,2,27648\nAD 86,86,84\nMvav 40,176,224\nAD 84,80,81\nCV 64,80,81\nMaav 0,128,72\nRa 176,2,28672\nMaav 4,128,76\nEX 82,34\nMaav 12,132,76\nRa 180,2,29696\nMaav 8,132,72\n"
"s_waitcnt vmcnt(15) lgkmcnt(8)\nEX 83,35\nMaav 16,136,72\nRa 184,2,30720\nAD 84,84,82\nMaav 20,136,76\nD1 1,16,46,3072\ns_add_u32 m0, s89, 49152\nMaav 28,140,76\nRa 188,2,31744\n"
"s_barrier\nMaav 24,140,72\nAD 84,84,83\nCV 65,82,83\nMaav 32,144,72\nRa 128,24,32768\nMaav 36,144,76\ns_add_u32 s46, s87, s88\nEX 80,36\nMaav 44,148,76\nRa 132,24,33792\n"
"Maav 40,148,72\nWL 8\nEX 81,37\nMaav 48,152,72\nRa 136,24,34816\nAD 87,87,85\nMaav 52,152,76\nD1 1,16,46,0\nMaav 60,156,76\nRa 140,24,35840\nMaav 56,156,72\nAD 85,80,81\n"
"CV 68,80,81\nMaav 64,160,72\nRa 144,26,32768\nMaav 68,160,76\nEX 82,38\nMaav 76,164,76\nRa 148,26,33792\nMaav 72,164,72\nWL 8\nEX 83,39\nMaav 80,168,72\nRa 152,26,34816\n"
"AD 85,85,82\nMaav 84,168,76\nD1 1,16,46,1024\nMaav 92,172,76\nRa 156,26,35840\nMaav 88,172,72\nAD 85,85,83\nCV 69,82,83\nMaav 96,176,72\nRa 160,24,40960\nMaav 100,176,76\n"
"s_xor_b32 s89, s89, 0x10000\nEX 80,40\nMaav 108,180,76\nRa 164,24,41984\nMaav 104,180,72\nWL 6\nEX 81,41\nMaav 112,184,72\nRa 168,24,43008\nAD 84,84,80\nMaav 116,184,76\n"
"D1 1,16,46,2048\nMaav 124,188,76\nRa 172,24,44032\nMaav 120,188,72\nAD 84,84,81\nCV 66,80,81\nCvav 48,128,192,4\nRa 176,26,40960\nMvav 48,132,200\ns_mov_b32 s86, s94\nEX 82,42\n"
"Mvav 48,136,208\nRa 180,26,41984\nMvav 48,140,216\nWL 6\nEX 83,43\nCvav 52,140,220,8\nRa 184,26,43008\nAD 84,84,82\nMvav 52,136,212\nD1 1,16,46,3072\ns_add_u32 m0, s89, 0\n"
"Mvav 52,132,204\nRa 188,26,44032\nMvav 52,128,196\nAD 84,84,83\nCV 67,82,83\nMvav 52,144,228\nRa 128,2,49152\nMvav 52,148,236\ns_add_u32 s46, s86, s88\nEX 80,44\nMvav 52,152,244\n"
"Ra 132,2,50176\nv_xor_b32 v24, 0x10000, v24\nMvav 52,156,252\nWL 6\nEX 81,45\nMvav 48,156,248\nRa 136,2,51200\nAD 85,85,80\nMvav 48,152,240\nD1 1,16,46,0\nMvav 48,148,232\n"
"Ra 140,2,52224\nSv 112,1,16,49,1024\nMvav 48,144,224\nAD 85,85,81\nCV 70,80,81\nWV 9\nv_xor_b32 v109, s15, v102\nv_xor_b32 v110, s15, v106\nv_or_b32 v109, v109, v110\n"
"v_cmp_ne_u32 vcc, 0, v109\ns_nop 3\ns_cmp_lg_u64 vcc, 0\ns_cselect_b32 s67, 1, 0\ns_cselect_b32 s6, 0x40000000, s66\nCvav 56,160,192,12\nRa 144,2,53248\ns_add_u32 s5, s6, 0\n"
"Mvav 56,164,200\ns_add_u32 s49, s49, 131072\nEX 82,46\nMvav 56,168,208\nRa 148,2,54272\nv_xor_b32 v26, 0x10000, v26\n"
"buffer_load_dwordx4 a[192:195], v1, s[16:19], s5 offen offset:0 sc1\nMvav 56,172,216\nWL 6\nEX 83,47\nCvav 60,172,220,16\nRa 152,2,55296\nAD 85,85,82\nMvav 60,168,212\n"
"D1 1,16,46,1024\nMvav 60,164,204\nRa 156,2,56320\nMvav 60,160,196\nAD 85,85,83\nCV 71,82,83\nMvav 60,176,228\nRa 160,2,57344\nv_max_f32 v90, v84, v85\nMvav 60,180,236\n"
"s_add_u32 s88, s88, 32768\nEX 80,48\nMvav 60,184,244\nRa 164,2,58368\nv_cmp_lt_f32 vcc, s63, v90\nMvav 60,188,252\nWL 8\nEX 81,49\nMvav 56,188,248\nRa 168,2,59392\n"
"s_cbranch_vccnz .T177\n.T178:\nbuffer_load_dwordx4 a[196:199], v1, s[16:19], s5 offen offset:1024 sc1\nMvav 56,184,240\nD1 1,16,46,2048\nMvav 56,180,232\nRa 172,2,60416\n"
"AD 86,86,84\nMvav 56,176,224\nAD 84,80,81\nCV 72,80,81\nMaav 0,128,64\nRa 176,2,61440\nMaav 4,128,68\nEX 82,50\nMaav 12,132,68\nRa 180,2,62464\nMaav 8,132,64\nWL 8\nEX 83,51\n"
"Maav 16,136,64\nRa 184,2,63488\nAD 84,84,82\nbuffer_load_dwordx4 a[200:203], v1, s[16:19], s5 offen offset:2048 sc1\nMaav 20,136,68\nD1 1,16,46,3072\ns_add_u32 m0, s89, 16384\n"
"Maav 28,140,68\nRa 188,2,64512\ns_barrier\nMaav 24,140,64\nAD 84,84,83\nCV 73,82,83\nMaav 32,144,64\nRa 128,24,0\nMaav 36,144,68\ns_add_u32 s46, s87, s88\nEX 80,52\n"
"Maav 44,148,68\nRa 132,24,1024\nv_xor_b32 v2, 0x10000, v2\nMaav 40,148,64\nWL 8\nEX 81,53\nMaav 48,152,64\nRa 136,24,2048\nAD 87,87,85\n"
"buffer_load_dwordx4 a[204:207], v1, s[16:19], s5 offen offset:3072 sc1\nMaav 52,152,68\nD1 1,16,46,0\nMaav 60,156,68\nRa 140,24,3072\nMaav 56,156,64\nAD 85,80,81\nCV 76,80,81\n"
"Maav 64,160,64\nRa 144,26,0\nMaav 68,160,68\ns_cmp_lg_u32 s92, 0\nEX 82,54\nMaav 76,164,68\nRa 148,26,1024\ns_cbranch_scc1 .E173\n.E174:\ns_add_u32 s5, s6, 4096\nMaav 72,164,64\n"
"WL 8\nEX 83,55\nMaav 80,168,64\nRa 152,26,2048\nAD 85,85,82\nbuffer_load_dwordx4 a[208:211], v1, s[16:19], s5 offen offset:0 sc1\nMaav 84,168,68\nD1 1,16,46,1024\nMaav 92,172,68\n"
"Ra 156,26,3072\nMaav 88,172,64\nAD 85,85,83\nCV 77,82,83\nMaav 96,176,64\nRa 160,24,8192\nMaav 100,176,68\nEX 80,56\nMaav 108,180,68\nRa 164,24,9216\nMaav 104,180,64\nWL 6\n"
"EX 81,57\nMaav 112,184,64\nRa 168,24,10240\nAD 84,84,80\nbuffer_load_dwordx4 a[212:215], v1, s[16:19], s5 offen offset:1024 sc1\nMaav 116,184,68\nD1 1,16,46,2048\nMaav 124,188,68\n"
"Ra 172,24,11264\nMaav 120,188,64\nAD 84,84,81\nCV 74,80,81\ns_sub_u32 s99, s99, 1\nCvav 32,128,192,4\nRa 176,26,8192\nMvav 32,132,200\nEX 82,58\nMvav 32,136,208\nRa 180,26,9216\n"
"Mvav 32,140,216\nWL 6\nEX 83,59\nCvav 36,140,220,8\nRa 184,26,10240\nAD 84,84,82\nbuffer_load_dwordx4 a[216:219], v1, s[16:19], s5 offen offset:2048 sc1\nMvav 36,136,212\n"
"D1 1,16,46,3072\ns_add_u32 m0, s89, 32768\nMvav 36,132,204\nRa 188,26,11264\nMvav 36,128,196\nAD 84,84,83\nCV 75,82,83\nMvav 36,144,228\nRa 128,2,16384\nMvav 36,148,236\n"
"s_add_u32 s46, s86, s88\nEX 80,60\nMvav 36,152,244\nRa 132,2,17408\nMvav 36,156,252\nWL 6\nEX 81,61\nMvav 32,156,248\nRa 136,2,18432\nAD 85,85,80\n"
"buffer_load_dwordx4 a[220:223], v1, s[16:19], s5 offen offset:3072 sc1\nMvav 32,152,240\nD1 1,16,46,0\nMvav 32,148,232\nRa 140,2,19456\nSv 112,1,16,49,0\nMvav 32,144,224\n"
"AD 85,85,81\nCV 78,80,81\nCvav 40,160,192,12\nRa 144,2,20480\nMvav 40,164,200\ns_mov_b32 s87, s95\nEX 82,62\nMvav 40,168,208\nRa 148,2,21504\ns_add_u32 s5, s6, 16384\n"
"Mvav 40,172,216\nWL 6\nEX 83,63\nCvav 44,172,220,16\nRa 152,2,22528\nAD 85,85,82\nbuffer_load_dwordx4 a[224:227], v1, s[16:19], s5 offen offset:0 sc1\nMvav 44,168,212\n"
"D1 1,16,46,1024\nMvav 44,164,204\nRa 156,2,23552\nMvav 44,160,196\nAD 85,85,83\nCV 79,82,83\nMvav 44,176,228\nRa 160,2,24576\nv_max_f32 v90, v84, v85\nMvav 44,180,236\n"
"s_add_u32 s88, s88, 32768\nEX 80,32\nMvav 44,184,244\nRa 164,2,25600\nv_cmp_lt_f32 vcc, s63, v90\nMvav 44,188,252\nWL 8\nEX 81,33\nMvav 40,188,248\nRa 168,2,26624\n"
"s_cbranch_vccnz .T182\n.T183:\nbuffer_load_dwordx4 a[228:231], v1, s[16:19], s5 offen offset:1024 sc1\nMvav 40,184,240\nD1 1,16,46,2048\nMvav 40,180,232\nRa 172,2,27648\n"
"AD 86,86,84\nMvav 40,176,224\nAD 84,80,81\nCV 64,80,81\nMaav 0,128,72\nRa 176,2,28672\nMaav 4,128,76\nEX 82,34\nMaav 12,132,76\nRa 180,2,29696\nMaav 8,132,72\n"
"s_waitcnt vmcnt(23) lgkmcnt(8)\nEX 83,35\nMaav 16,136,72\nRa 184,2,30720\nAD 84,84,82\nbuffer_load_dwordx4 a[232:235], v1, s[16:19], s5 offen offset:2048 sc1\nMaav 20,136,76\n"
"D1 1,16,46,3072\ns_add_u32 m0, s89, 49152\nMaav 28,140,76\nRa 188,2,31744\ns_barrier\nMaav 24,140,72\nAD 84,84,83\nCV 65,82,83\nMaav 32,144,72\nRa 128,24,32768\nMaav 36,144,76\n"
"s_add_u32 s46, s87, s88\nEX 80,36\nMaav 44,148,76\nRa 132,24,33792\nMaav 40,148,72\nWL 8\nEX 81,37\nMaav 48,152,72\nRa 136,24,34816\nAD 87,87,85\n"
"buffer_load_dwordx4 a[236:239], v1, s[16:19], s5 offen offset:3072 sc1\nMaav 52,152,76\nD1 1,16,46,0\nMaav 60,156,76\nRa 140,24,35840\nMaav 56,156,72\nAD 85,80,81\nCV 68,80,81\n"
"Maav 64,160,72\nRa 144,26,32768\nMaav 68,160,76\ns_cmp_eq_u32 s13, 7\nEX 82,38\nMaav 76,164,76\nRa 148,26,33792\ns_cbranch_scc1 .E180\n.E181:\ns_add_u32 s5, s6, 20480\n"
"Maav 72,164,72\nWL 8\nEX 83,39\nMaav 80,168,72\nRa 152,26,34816\nAD 85,85,82\nbuffer_load_dwordx4 a[240:243], v1, s[16:19], s5 offen offset:0 sc1\nMaav 84,168,76\nD1 1,16,46,1024\n"
"Maav 92,172,76\nRa 156,26,35840\nMaav 88,172,72\nAD 85,85,83\nCV 69,82,83\nMaav 96,176,72\nRa 160,24,40960\nMaav 100,176,76\ns_xor_b32 s89, s89, 0x10000\nEX 80,40\n"
"Maav 108,180,76\nRa 164,24,41984\nMaav 104,180,72\nWL 6\nEX 81,41\nMaav 112,184,72\nRa 168,24,43008\nAD 84,84,80\n"
"buffer_load_dwordx4 a[244:247], v1, s[16:19], s5 offen offset:1024 sc1\nMaav 116,184,76\nD1 1,16,46,2048\nMaav 124,188,76\nRa 172,24,44032\nMaav 120,188,72\nAD 84,84,81\n"
"CV 66,80,81\nCvav 48,128,192,4\nRa 176,26,40960\nMvav 48,132,200\nEX 82,42\nMvav 48,136,208\nRa 180,26,41984\nMvav 48,140,216\nWL 6\nEX 83,43\nCvav 52,140,220,8\nRa 184,26,43008\n"
"AD 84,84,82\nbuffer_load_dwordx4 a[248:251], v1, s[16:19], s5 offen offset:2048 sc1\nMvav 52,136,212\nD1 1,16,46,3072\ns_add_u32 m0, s89, 0\nMvav 52,132,204\nRa 188,26,44032\n"
"Mvav 52,128,196\nAD 84,84,83\nCV 67,82,83\nMvav 52,144,228\nRa 128,2,49152\nMvav 52,148,236\ns_add_u32 s46, s86, s88\nEX 80,44\nMvav 52,152,244\nRa 132,2,50176\n"
"v_xor_b32 v24, 0x10000, v24\nMvav 52,156,252\nWL 6\nEX 81,45\nMvav 48,156,248\nRa 136,2,51200\nAD 85,85,80\nbuffer_load_dwordx4 a[252:255], v1, s[16:19], s5 offen offset:3072 sc1\n"
"Mvav 48,152,240\nD1 1,16,46,0\nMvav 48,148,232\nRa 140,2,52224\nSv 112,1,16,49,1024\nMvav 48,144,224\nAD 85,85,81\nCV 70,80,81\nCvav 56,160,192,12\nRa 144,2,53248\n"
"Mvav 56,164,200\ns_add_u32 s49, s49, 131072\nEX 82,46\nMvav 56,168,208\nRa 148,2,54272\nv_xor_b32 v26, 0x10000, v26\nMvav 56,172,216\nWL 6\nEX 83,47\nCvav 60,172,220,16\n"
"Ra 152,2,55296\nAD 85,85,82\nMvav 60,168,212\nD1 1,16,46,1024\nMvav 60,164,204\nRa 156,2,56320\nMvav 60,160,196\nAD 85,85,83\nCV 71,82,83\nMvav 60,176,228\nRa 160,2,57344\n"
"v_max_f32 v90, v84, v85\nMvav 60,180,236\ns_add_u32 s88, s88, 32768\nEX 80,48\nMvav 60,184,244\nRa 164,2,58368\nv_cmp_lt_f32 vcc, s63, v90\nMvav 60,188,252\nWL 8\nEX 81,49\n"
"Mvav 56,188,248\nRa 168,2,59392\ns_cbranch_vccnz .T184\n.T185:\nMvav 56,184,240\nD1 1,16,46,2048\nMvav 56,180,232\nRa 172,2,60416\nAD 86,86,84\nMvav 56,176,224\nAD 84,80,81\n"
"CV 72,80,81\nMaav 0,128,64\nRa 176,2,61440\nMaav 4,128,68\nEX 82,50\nMaav 12,132,68\nRa 180,2,62464\nMaav 8,132,64\ns_waitcnt vmcnt(22) lgkmcnt(8)\nEX 83,51\nMaav 16,136,64\n"
"Ra 184,2,63488\nAD 84,84,82\nMaav 20,136,68\nD1 1,16,46,3072\ns_add_u32 m0, s89, 16384\nMaav 28,140,68\nRa 188,2,64512\ns_barrier\nMaav 24,140,64\nAD 84,84,83\nCV 73,82,83\n"
"Maav 32,144,64\nRa 128,24,0\nMaav 36,144,68\ns_add_u32 s46, s87, s88\nEX 80,52\nMaav 44,148,68\nRa 132,24,1024\nv_xor_b32 v2, 0x10000, v2\nMaav 40,148,64\nWL 8\nEX 81,53\n"
"Maav 48,152,64\nRa 136,24,2048\nAD 87,87,85\nMaav 52,152,68\nD1 1,16,46,0\nMaav 60,156,68\nRa 140,24,3072\nMaav 56,156,64\nAD 85,80,81\nCV 76,80,81\ns_cmp_lg_u32 s67, 0\n"
"s_cbranch_scc1 .E187\n.E188:\nMaav 64,160,64\nRa 144,26,0\nMaav 68,160,68\nEX 82,54\nMaav 76,164,68\nRa 148,26,1024\nMaav 72,164,64\nWL 8\nEX 83,55\nMaav 80,168,64\n"
"Ra 152,26,2048\nAD 85,85,82\nMaav 84,168,68\nD1 1,16,46,1024\nMaav 92,172,68\nRa 156,26,3072\nMaav 88,172,64\nAD 85,85,83\nCV 77,82,83\nMaav 96,176,64\nRa 160,24,8192\n"
"Maav 100,176,68\nEX 80,56\nMaav 108,180,68\nRa 164,24,9216\nMaav 104,180,64\nWL 10\nEX 81,57\nMaav 112,184,64\nRa 168,24,10240\nAD 84,84,80\nMaav 116,184,68\nD1 1,16,46,2048\n"
"Maav 124,188,68\nRa 172,24,11264\nMaav 120,188,64\nAD 84,84,81\nCV 74,80,81\ns_cmp_lg_u32 s67, 0\ns_cbranch_scc1 .D189\n.D190:\nWV 8\nv_cmp_lt_f32 vcc, 0, v101\nSU 20,100,88\n"
"v_mul_f32 v101, 0x3e800000, v101\nv_cndmask_b32 v20, 0, v20, vcc\nv_cmp_lt_f32 vcc, 0, v105\nSU 21,104,89\nv_mul_f32 v105, 0x3e800000, v105\nv_cndmask_b32 v21, 0, v21, vcc\n"
"v_or_b32 v91, v20, v21\nv_cmp_ne_u32 vcc, 0, v91\ns_nop 0\ns_cbranch_vccnz .D191\nAD 86,86,101\nAD 87,87,105\n.D192:\nMava 0,130,192\nMava 8,128,192\nEX 82,58\nMava 12,128,196\n"
"Mava 4,130,196\nEX 83,59\nMava 16,130,200\nAD 84,84,82\nMava 24,128,200\nD1 1,16,46,3072\nMava 28,128,204\ns_add_u32 m0, s89, 32768\ns_add_u32 s46, s86, s88\nMava 20,130,204\n"
"AD 84,84,83\nCV 75,82,83\nMava 32,130,208\nRa 128,2,16384\nv_xor_b32 v3, 0x10000, v2\nMava 40,128,208\nEX 80,60\nMava 44,128,212\nRa 132,2,17408\nv_add_u32 v0, 0x20000, v1\n"
"Mava 36,130,212\nEX 81,61\nMava 48,130,216\nRa 136,2,18432\nAD 85,85,80\nMava 56,128,216\nD1 1,16,46,0\ns_sub_u32 s94, s49, 1966080\nMava 60,128,220\nRa 140,2,19456\n"
"s_add_u32 s95, s94, 1048576\nMava 52,130,220\nAD 85,85,81\nCV 78,80,81\nMava 64,130,224\nRa 144,2,20480\ns_xor_b32 s92, s94, 0x202000\nMava 72,128,224\nEX 82,62\nMava 76,128,228\n"
"Ra 148,2,21504\ns_add_u32 s93, s96, 983040\nMava 68,130,228\nEX 83,63\nMava 80,130,232\nRa 152,2,22528\nAD 85,85,82\nMava 88,128,232\nD1 1,16,46,1024\n"
"s_xor_b32 s93, s93, 0x202000\nMava 92,128,236\nRa 156,2,23552\ns_lshl_b32 s89, s11, 12\nMava 84,130,236\nAD 85,85,83\nCV 79,82,83\nMava 96,130,240\nRa 160,2,24576\n"
"v_max_f32 v90, v84, v85\nMava 104,128,240\nv_cmp_lt_f32 vcc, s63, v90\nMava 108,128,244\nRa 164,2,25600\nMava 100,130,244\nWL 8\nMava 112,130,248\nRa 168,2,26624\n"
"Mava 120,128,248\nD1 1,16,46,2048\nMava 124,128,252\nRa 172,2,27648\nMava 116,130,252\ns_cbranch_vccnz .T196\n.T197:\nAD 86,86,84\nAD 87,87,85\nMaav 0,128,72\nRa 176,2,28672\n"
"v_mov_b32 v84, v86\nv_mov_b32 v85, v87\nMaav 4,128,76\nv_permlane32_swap_b32 v86, v84\nv_permlane32_swap_b32 v87, v85\nMaav 12,132,76\nRa 180,2,29696\nAD 86,86,84\nMaav 8,132,72\n"
"WL 8\nAD 87,87,85\nMaav 16,136,72\nRa 184,2,30720\nv_mov_b32 v84, v86\nv_mov_b32 v85, v87\nMaav 20,136,76\nD1 1,16,46,3072\nv_permlane16_swap_b32 v86, v84\nMaav 28,140,76\n"
"Ra 188,2,31744\nv_permlane16_swap_b32 v87, v85\nMaav 24,140,72\nAD 86,86,84\nAD 87,87,85\nMaav 32,144,72\nRa 128,2,0\nv_rcp_f32 v86, v86\nMaav 36,144,76\nv_rcp_f32 v87, v87\n"
"Maav 44,148,76\nRa 132,2,1024\nAR 82,0\nAR 83,1\nMU 82,82,86\nMU 83,83,86\nMaav 40,148,72\nWL 8\ns_add_u32 m0, s89, 131072\ns_add_u32 s46, s93, 0\nCV 96,82,83\nAR 82,2\n"
"Maav 48,152,72\nRa 136,2,2048\nAR 83,3\nMU 82,82,86\nMU 83,83,86\nCV 97,82,83\nMaav 52,152,76\nD1 1,16,46,0\nAR 82,4\nAR 83,5\nMU 82,82,87\nMU 83,83,87\nMaav 60,156,76\n"
"Ra 140,2,3072\nCV 100,82,83\nAR 82,6\nAR 83,7\nMU 82,82,87\nMaav 56,156,72\nMU 83,83,87\nCV 101,82,83\nAR 82,8\nAR 83,9\nMU 82,82,86\nMaav 64,160,72\nRa 144,2,4096\nMU 83,83,86\n"
"CV 98,82,83\nAR 82,10\nAR 83,11\nMaav 68,160,76\nMU 82,82,86\nMU 83,83,86\nCV 99,82,83\ns_add_u32 s5, s94, 0\nSv 96,1,16,5,0\nMaav 76,164,76\nRa 148,2,5120\nAR 82,12\nAR 83,13\n"
"MU 82,82,87\nMU 83,83,87\nMaav 72,164,72\nWL 8\nCV 102,82,83\nAR 82,14\nAR 83,15\nMU 82,82,87\nMaav 80,168,72\nRa 152,2,6144\nMU 83,83,87\nCV 103,82,83\nSv 100,1,16,5,1024\n"
"AR 82,16\nMaav 84,168,76\nD1 1,16,46,1024\nAR 83,17\nMU 82,82,86\nMU 83,83,86\nCV 104,82,83\nMaav 92,172,76\nRa 156,2,7168\nAR 82,18\nAR 83,19\nMU 82,82,86\nMU 83,83,86\n"
"Maav 88,172,72\nCV 105,82,83\nAR 82,20\nAR 83,21\nMU 82,82,87\nMU 83,83,87\nMaav 96,176,72\nRa 160,2,8192\nCV 108,82,83\nAR 82,22\nAR 83,23\nMU 82,82,87\nMaav 100,176,76\n"
"MU 83,83,87\nCV 109,82,83\nAR 82,24\nAR 83,25\nMU 82,82,86\nMaav 108,180,76\nRa 164,2,9216\nMU 83,83,86\nCV 106,82,83\nAR 82,26\nAR 83,27\nMaav 104,180,72\nWL 8\nMU 82,82,86\n"
"MU 83,83,86\nCV 107,82,83\ns_add_u32 s5, s94, 131072\nMaav 112,184,72\nRa 168,2,10240\nSv 104,1,16,5,0\nAR 82,28\nAR 83,29\nMU 82,82,87\nMaav 116,184,76\nD1 1,16,46,2048\n"
"MU 83,83,87\nCV 110,82,83\nAR 82,30\nAR 83,31\nMaav 124,188,76\nRa 172,2,11264\nMU 82,82,87\nMU 83,83,87\nCV 111,82,83\nSv 108,1,16,5,1024\nMaav 120,188,72\nAR 82,32\nAR 83,33\n"
"MU 82,82,86\nZvav 136,128,96\nRa 176,2,12288\nMU 83,83,86\nCV 112,82,83\nMvav 136,132,104\nAR 82,34\nAR 83,35\nZvav 140,132,108\nRa 180,2,13312\nMU 82,82,86\nMU 83,83,86\n"
"Mvav 140,128,100\ns_waitcnt vmcnt(18) lgkmcnt(8)\nCV 113,82,83\nAR 82,36\nZvav 148,136,100\nRa 184,2,14336\nAR 83,37\nMU 82,82,87\nMvav 148,140,108\nD1 1,16,46,3072\nMU 83,83,87\n"
"CV 116,82,83\nZvav 144,140,104\nRa 188,2,15360\ns_barrier\nAR 82,38\nMvav 144,136,96\nAR 83,39\nMU 82,82,87\nMU 83,83,87\nZvav 152,144,96\nRa 128,2,32768\nCV 117,82,83\nAR 82,40\n"
"v_lshrrev_b32 v88, 8, v1\nMvav 152,148,104\nAR 83,41\nMU 82,82,86\nMU 83,83,86\nbuffer_store_dword v25, off, s[16:19], s54 offset:512 sc0 sc1\nZvav 156,148,108\nRa 132,2,33792\n"
"CV 114,82,83\nAR 82,42\nMvav 156,144,100\nWL 8\ns_add_u32 m0, s89, 114688\ns_add_u32 s46, s96, 1179648\nZvav 164,152,100\nRa 136,2,34816\nAR 83,43\nMU 82,82,86\nMvav 164,156,108\n"
"D1 1,16,46,0\nMU 83,83,86\nCV 115,82,83\nZvav 160,156,104\nRa 140,2,35840\ns_add_u32 s5, s94, 262144\nSv 112,1,16,5,0\nMvav 160,152,96\nAR 82,44\nAR 83,45\nMU 82,82,87\n"
"Zvav 168,160,96\nRa 144,2,36864\nMU 83,83,87\nCV 118,82,83\nv_lshlrev_b32 v89, 5, v88\nMvav 168,164,104\nAR 82,46\nAR 83,47\n"
"buffer_load_dwordx4 v[64:67], v89, s[16:19], s40 offen offset:0 sc0 sc1\nZvav 172,164,108\nRa 148,2,37888\nMU 82,82,87\nMU 83,83,87\nMvav 172,160,100\nWL 8\nCV 119,82,83\n"
"Sv 116,1,16,5,1024\nZvav 180,168,100\nRa 152,2,38912\nAR 82,48\nAR 83,49\nMvav 180,172,108\nD1 1,16,46,1024\nMU 82,82,86\nMU 83,83,86\nZvav 176,172,104\nRa 156,2,39936\n"
"CV 120,82,83\nAR 82,50\nMvav 176,168,96\nAR 83,51\nMU 82,82,86\nMU 83,83,86\nZvav 184,176,96\nRa 160,2,40960\nCV 121,82,83\nAR 82,52\n"
"buffer_load_dwordx4 v[68:71], v89, s[16:19], s40 offen offset:16 sc0 sc1\nMvav 184,180,104\nAR 83,53\nMU 82,82,87\nMU 83,83,87\nZvav 188,180,108\nRa 164,2,41984\nCV 124,82,83\n"
"AR 82,54\nMvav 188,176,100\nWL 8\nAR 83,55\nMU 82,82,87\nZvav 196,184,100\nRa 168,2,43008\nMU 83,83,87\nCV 125,82,83\nMvav 196,188,108\nD1 1,16,46,2048\nAR 82,56\nAR 83,57\n"
"Zvav 192,188,104\nRa 172,2,44032\nMU 82,82,86\nMU 83,83,86\nMvav 192,184,96\nCV 122,82,83\nAR 82,58\nAR 83,59\nZvav 200,128,96\nRa 176,2,45056\nMU 82,82,86\nMU 83,83,86\n"
"buffer_load_dwordx4 v[72:75], v89, s[16:19], s40 offen offset:1792 sc0 sc1\nMvav 200,132,104\nCV 123,82,83\ns_add_u32 s5, s94, 393216\nZvav 204,132,108\nRa 180,2,46080\n"
"Sv 120,1,16,5,0\nAR 82,60\nMvav 204,128,100\nWL 8\nAR 83,61\nMU 82,82,87\nZvav 212,136,100\nRa 184,2,47104\nMU 83,83,87\nCV 126,82,83\nMvav 212,140,108\nD1 1,16,46,3072\nAR 82,62\n"
"AR 83,63\nZvav 208,140,104\nRa 188,2,48128\nMU 82,82,87\nMU 83,83,87\nMvav 208,136,96\nCV 127,82,83\nSv 124,1,16,5,1024\n"
"buffer_load_dwordx4 v[76:79], v89, s[16:19], s40 offen offset:1808 sc0 sc1\nZvav 216,144,96\nRa 128,2,49152\nAR 82,64\nAR 83,65\nMvav 216,148,104\nMU 82,82,86\nMU 83,83,86\n"
"Zvav 220,148,108\nRa 132,2,50176\nCV 32,82,83\nAR 82,66\nMvav 220,144,100\nWL 8\ns_add_u32 m0, s89, 147456\ns_add_u32 s46, s93, 32768\nZvav 228,152,100\nRa 136,2,51200\nAR 83,67\n"
"MU 82,82,86\nMvav 228,156,108\nD1 1,16,46,0\nMU 83,83,86\nCV 33,82,83\nZvav 224,156,104\nRa 140,2,52224\nAR 82,68\nv_bfe_u32 v90, v1, 4, 4\nMvav 224,152,96\nAR 83,69\nMU 82,82,87\n"
"MU 83,83,87\nZvav 232,160,96\nRa 144,2,53248\nCV 36,82,83\nAR 82,70\nv_mul_u32_u24 v91, 896, v90\nMvav 232,164,104\nAR 83,71\nMU 82,82,87\nv_lshl_add_u32 v91, v88, 4, v91\n"
"Zvav 236,164,108\nRa 148,2,54272\nMU 83,83,87\nCV 37,82,83\nMvav 236,160,100\nWL 8\nAR 82,72\nbuffer_load_dwordx4 v[12:15], v91, s[28:31], s22 offen offset:0\nZvav 244,168,100\n"
"Ra 152,2,55296\nAR 83,73\nMU 82,82,86\nMvav 244,172,108\nD1 1,16,46,1024\nMU 83,83,86\nCV 34,82,83\nZvav 240,172,104\nRa 156,2,56320\nAR 82,74\nMvav 240,168,96\nAR 83,75\n"
"MU 82,82,86\nMU 83,83,86\nMvav 136,176,112\nRa 160,2,57344\nCV 35,82,83\ns_add_u32 s5, s94, 524288\nbuffer_load_dwordx4 v[16:19], v91, s[28:31], s23 offen offset:0\n"
"Mvav 136,180,120\nSv 32,1,16,5,0\nAR 82,76\nv_add_u32 v90, s21, v90\nMvav 140,180,124\nRa 164,2,58368\nAR 83,77\nMU 82,82,87\nMvav 140,176,116\nWL 8\nMU 83,83,87\n"
"v_lshlrev_b32 v88, 2, v90\nMvav 148,184,116\nRa 168,2,59392\nCV 38,82,83\nAR 82,78\nMvav 148,188,124\nD1 1,16,46,2048\nAR 83,79\nMU 82,82,87\nMvav 144,188,120\nRa 172,2,60416\n"
"MU 83,83,87\nMvav 144,184,112\nCV 39,82,83\nSv 36,1,16,5,1024\nAR 82,80\nMvav 152,128,112\nRa 176,2,61440\nAR 83,81\nMU 82,82,86\nMvav 152,132,120\nMU 83,83,86\nCV 40,82,83\n"
"Mvav 156,132,124\nRa 180,2,62464\nAR 82,82\nAR 83,83\nMvav 156,128,116\ns_waitcnt vmcnt(32) lgkmcnt(8)\nMU 82,82,86\nMvav 164,136,116\nRa 184,2,63488\nMU 83,83,86\nCV 41,82,83\n"
"Mvav 164,140,124\nD1 1,16,46,3072\nAR 82,84\nAR 83,85\nMvav 160,140,120\nRa 188,2,64512\ns_barrier\nMvav 160,136,112\nMU 82,82,87\nMU 83,83,87\nCV 44,82,83\nMvav 168,144,112\n"
"Ra 128,3,0\nAR 82,86\nAR 83,87\nMvav 168,148,120\nMU 82,82,87\nMU 83,83,87\nMvav 172,148,124\nRa 132,3,1024\nCV 45,82,83\nAR 82,88\nMvav 172,144,116\nWL 8\n"
"s_add_u32 m0, s89, 16384\ns_add_u32 s46, s93, 65536\nMvav 180,152,116\nRa 136,3,2048\nAR 83,89\nMU 82,82,86\nMvav 180,156,124\nD1 1,16,46,0\nMU 83,83,86\nCV 42,82,83\n"
"Mvav 176,156,120\nRa 140,3,3072\nAR 82,90\nMvav 176,152,112\nAR 83,91\nMU 82,82,86\nMU 83,83,86\nMvav 184,160,112\nRa 144,3,4096\nCV 43,82,83\ns_add_u32 s5, s94, 655360\n"
"Mvav 184,164,120\nSv 40,1,16,5,0\nAR 82,92\nbuffer_load_dword v27, off, s[16:19], s60 offset:512 sc0 sc1\nMvav 188,164,124\nRa 148,3,5120\nAR 83,93\nMU 82,82,87\nMvav 188,160,116\n"
"WL 8\nMU 83,83,87\nMvav 196,168,116\nRa 152,3,6144\nCV 46,82,83\nAR 82,94\nMvav 196,172,124\nD1 1,16,46,1024\nAR 83,95\nMU 82,82,87\nMvav 192,172,120\nRa 156,3,7168\nMU 83,83,87\n"
"Mvav 192,168,112\nCV 47,82,83\nSv 44,1,16,5,1024\nMvav 200,176,112\nRa 160,3,8192\nAR 82,96\nAR 83,97\nMvav 200,180,120\nMU 82,82,86\nMU 83,83,86\nMvav 204,180,124\nRa 164,3,9216\n"
"CV 48,82,83\nAR 82,98\nMvav 204,176,116\nWL 8\nAR 83,99\nMvav 212,184,116\nRa 168,3,10240\nMU 82,82,86\nMU 83,83,86\nMvav 212,188,124\nD1 1,16,46,2048\nCV 49,82,83\nAR 82,100\n"
"Mvav 208,188,120\nRa 172,3,11264\nAR 83,101\nMvav 208,184,112\nMU 82,82,87\nMU 83,83,87\nCV 52,82,83\nMvav 216,128,112\nRa 176,3,12288\nAR 82,102\nAR 83,103\nWV 10\n"
"v_xor_b32 v92, s15, v66\nv_xor_b32 v93, s15, v70\nv_xor_b32 v94, s15, v74\nv_or3_b32 v92, v92, v93, v94\nv_xor_b32 v93, s15, v78\nMvav 216,132,120\nMU 82,82,87\nMU 83,83,87\n"
"v_or_b32 v92, v92, v93\nv_cmp_ne_u32 vcc, 0, v92\ns_nop 0\ns_cbranch_vccnz .S205\n.S206:\ns_mov_b32 s2, 0xc040000\nbuffer_load_dword v80, v88, s[16:19], s2 offen sc0 sc1\n"
"Mvav 220,132,124\nRa 180,3,13312\nCV 53,82,83\nAR 82,104\nMvav 220,128,116\nWL 8\nAR 83,105\nMvav 228,136,116\nRa 184,3,14336\nMU 82,82,86\nMU 83,83,86\nMvav 228,140,124\n"
"D1 1,16,46,3072\nCV 50,82,83\nAR 82,106\nbuffer_load_dword v81, v88, s[16:19], s2 offen offset:256 sc0 sc1\nv_and_b32 v92, 0x7ff, v67\nv_lshrrev_b32 v93, 16, v67\n"
"v_add_u32 v94, 64, v90\nMvav 224,140,120\nRa 188,3,15360\nAR 83,107\nv_cmp_eq_u32 vcc, v92, v90\ns_nop 1\nv_cndmask_b32 v12, v12, v64, vcc\nv_cndmask_b32 v13, v13, v65, vcc\n"
"v_cndmask_b32 v14, v14, v68, vcc\nv_cndmask_b32 v15, v15, v69, vcc\nMvav 224,136,112\nMU 82,82,86\nMU 83,83,86\nCV 51,82,83\nMvav 232,144,112\nRa 128,3,16384\n"
"s_add_u32 s5, s94, 786432\nSv 48,1,16,5,0\nMvav 232,148,120\nAR 82,108\nAR 83,109\nMvav 236,148,124\nRa 132,3,17408\nMU 82,82,87\nMU 83,83,87\nMvav 236,144,116\nWL 8\n"
"s_add_u32 m0, s89, 0\ns_add_u32 s46, s93, 98304\nMvav 244,152,116\nRa 136,3,18432\nCV 54,82,83\nAR 82,110\nMvav 244,156,124\nD1 1,16,46,0\nAR 83,111\nMU 82,82,87\n"
"Mvav 240,156,120\nRa 140,3,19456\nMU 83,83,87\nMvav 240,152,112\nCV 55,82,83\nSv 52,1,16,5,1024\nAR 82,112\nMvav 136,160,32\nRa 144,3,20480\nAR 83,113\nMU 82,82,86\n"
"Mvav 136,164,40\nMU 83,83,86\nCV 56,82,83\nMvav 140,164,44\nRa 148,3,21504\nAR 82,114\nAR 83,115\nMvav 140,160,36\nWL 8\nMU 82,82,86\nMvav 148,168,36\nRa 152,3,22528\nMU 83,83,86\n"
"CV 57,82,83\nMvav 148,172,44\nD1 1,16,46,1024\nAR 82,116\nAR 83,117\nMvav 144,172,40\nRa 156,3,23552\nMU 82,82,87\nv_cmp_eq_u32 vcc, v93, v90\ns_nop 1\n"
"v_cndmask_b32 v12, v12, v72, vcc\nv_cndmask_b32 v13, v13, v73, vcc\nv_cndmask_b32 v14, v14, v76, vcc\nv_cndmask_b32 v15, v15, v77, vcc\nMvav 144,168,32\nMU 83,83,87\nCV 60,82,83\n"
"AR 82,118\nMvav 152,176,32\nRa 160,3,24576\nAR 83,119\nMU 82,82,87\nMvav 152,180,40\nMU 83,83,87\nCV 61,82,83\nMvav 156,180,44\nRa 164,3,25600\nAR 82,120\nAR 83,121\n"
"Mvav 156,176,36\nWL 8\nMU 82,82,86\nv_cmp_eq_u32 vcc, v92, v94\ns_nop 1\nv_cndmask_b32 v16, v16, v64, vcc\nv_cndmask_b32 v17, v17, v65, vcc\nv_cndmask_b32 v18, v18, v68, vcc\n"
"v_cndmask_b32 v19, v19, v69, vcc\nMvav 164,184,36\nRa 168,3,26624\nMU 83,83,86\nCV 58,82,83\nMvav 164,188,44\nD1 1,16,46,2048\nAR 82,122\nAR 83,123\nMvav 160,188,40\n"
"Ra 172,3,27648\nMU 82,82,86\nMvav 160,184,32\nMU 83,83,86\nCV 59,82,83\ns_add_u32 s5, s94, 917504\nMvav 168,128,32\nRa 176,3,28672\nSv 56,1,16,5,0\nAR 82,124\nMvav 168,132,40\n"
"AR 83,125\nMU 82,82,87\nMvav 172,132,44\nRa 180,3,29696\nMU 83,83,87\nCV 62,82,83\nMvav 172,128,36\nWL 8\nAR 82,126\nMvav 180,136,36\nRa 184,3,30720\nAR 83,127\nMU 82,82,87\n"
"Mvav 180,140,44\nD1 1,16,46,3072\nMU 83,83,87\nCV 63,82,83\nMvav 176,140,40\nRa 188,3,31744\ns_barrier\nv_cmp_eq_u32 vcc, v93, v94\ns_nop 1\nv_cndmask_b32 v16, v16, v72, vcc\n"
"v_cndmask_b32 v17, v17, v73, vcc\nv_cndmask_b32 v18, v18, v76, vcc\nv_cndmask_b32 v19, v19, v77, vcc\nMvav 176,136,32\nSv 60,1,16,5,1024\nMvav 184,144,32\nRa 128,3,32768\n"
"Mvav 184,148,40\nMvav 188,148,44\nRa 132,3,33792\nMvav 188,144,36\nWL 8\ns_add_u32 m0, s89, 32768\ns_add_u32 s46, s93, 131072\nMvav 196,152,36\nRa 136,3,34816\nMvav 196,156,44\n"
"D1 1,16,46,0\nMvav 192,156,40\nRa 140,3,35840\nMvav 192,152,32\nMvav 200,160,32\nRa 144,3,36864\nWV 15\nv_cmp_ne_u32 vcc, s15, v27\ns_nop 0\ns_cbranch_vccnz .S208\n.S209:\n"
"Mvav 200,164,40\ns_add_u32 s4, s92, 0\nMvav 204,164,44\nRa 148,3,37888\nMvav 204,160,36\nWL 8\nbuffer_load_dwordx4 a[192:195], v1, s[16:19], s4 offen offset:0 sc0 sc1\n"
"Mvav 212,168,36\nRa 152,3,38912\nMvav 212,172,44\nD1 1,16,46,1024\nMvav 208,172,40\nRa 156,3,39936\nMvav 208,168,32\n"
"buffer_load_dwordx4 a[196:199], v1, s[16:19], s4 offen offset:1024 sc0 sc1\nMvav 216,176,32\nRa 160,3,40960\nMvav 216,180,40\ns_add_u32 s4, s92, 131072\nMvav 220,180,44\n"
"Ra 164,3,41984\nMvav 220,176,36\nWL 8\nbuffer_load_dwordx4 a[200:203], v1, s[16:19], s4 offen offset:0 sc0 sc1\nMvav 228,184,36\nRa 168,3,43008\nMvav 228,188,44\nD1 1,16,46,2048\n"
"Mvav 224,188,40\nRa 172,3,44032\nMvav 224,184,32\nbuffer_load_dwordx4 a[204:207], v1, s[16:19], s4 offen offset:1024 sc0 sc1\nMvav 232,128,32\nRa 176,3,45056\nMvav 232,132,40\n"
"s_add_u32 s4, s92, 262144\nMvav 236,132,44\nRa 180,3,46080\nMvav 236,128,36\nWL 8\nbuffer_load_dwordx4 a[208:211], v1, s[16:19], s4 offen offset:0 sc0 sc1\nMvav 244,136,36\n"
"Ra 184,3,47104\nMvav 244,140,44\nD1 1,16,46,3072\nMvav 240,140,40\nRa 188,3,48128\nMvav 240,136,32\nbuffer_load_dwordx4 a[212:215], v1, s[16:19], s4 offen offset:1024 sc0 sc1\n"
"Mvav 136,144,48\nRa 128,3,49152\nMvav 136,148,56\ns_add_u32 s4, s92, 393216\nMvav 140,148,60\nRa 132,3,50176\nMvav 140,144,52\nWL 8\ns_add_u32 m0, s89, 49152\n"
"s_add_u32 s46, s93, 163840\nbuffer_load_dwordx4 a[216:219], v1, s[16:19], s4 offen offset:0 sc0 sc1\nMvav 148,152,52\nRa 136,3,51200\nMvav 148,156,60\nD1 1,16,46,0\n"
"Mvav 144,156,56\nRa 140,3,52224\nMvav 144,152,48\nbuffer_load_dwordx4 a[220:223], v1, s[16:19], s4 offen offset:1024 sc0 sc1\nMvav 152,160,48\nRa 144,3,53248\nMvav 152,164,56\n"
"Mvav 156,164,60\nRa 148,3,54272\nMvav 156,160,52\nWL 8\nMvav 164,168,52\nRa 152,3,55296\nMvav 164,172,60\nD1 1,16,46,1024\nMvav 160,172,56\nRa 156,3,56320\nMvav 160,168,48\n"
"Mvav 168,176,48\nRa 160,3,57344\nMvav 168,180,56\ns_add_u32 s4, s92, 524288\nMvav 172,180,60\nRa 164,3,58368\nMvav 172,176,52\nWL 8\n"
"buffer_load_dwordx4 a[224:227], v1, s[16:19], s4 offen offset:0 sc0 sc1\nMvav 180,184,52\nRa 168,3,59392\nMvav 180,188,60\nD1 1,16,46,2048\nMvav 176,188,56\nRa 172,3,60416\n"
"Mvav 176,184,48\nbuffer_load_dwordx4 a[228:231], v1, s[16:19], s4 offen offset:1024 sc0 sc1\nMvav 184,128,48\nRa 176,3,61440\nMvav 184,132,56\ns_add_u32 s4, s92, 655360\n"
"Mvav 188,132,60\nRa 180,3,62464\nMvav 188,128,52\nWL 8\nbuffer_load_dwordx4 a[232:235], v1, s[16:19], s4 offen offset:0 sc0 sc1\nMvav 196,136,52\nRa 184,3,63488\nMvav 196,140,60\n"
"D1 1,16,46,3072\nMvav 192,140,56\nRa 188,3,64512\ns_barrier\nMvav 192,136,48\nbuffer_load_dwordx4 a[236:239], v1, s[16:19], s4 offen offset:1024 sc0 sc1\nMvav 200,144,48\n"
"Ra 128,0,0\nMvav 200,148,56\ns_add_u32 s4, s92, 786432\nMvav 204,148,60\nRa 132,0,1024\nMvav 204,144,52\nWL 8\ns_add_u32 m0, s89, 65536\ns_add_u32 s46, s93, 196608\n"
"buffer_load_dwordx4 a[240:243], v1, s[16:19], s4 offen offset:0 sc0 sc1\nMvav 212,152,52\nRa 136,0,2048\nMvav 212,156,60\nD1 1,16,46,0\nMvav 208,156,56\nRa 140,0,3072\n"
"Mvav 208,152,48\nbuffer_load_dwordx4 a[244:247], v1, s[16:19], s4 offen offset:1024 sc0 sc1\nMvav 216,160,48\nRa 144,0,4096\nMvav 216,164,56\ns_add_u32 s4, s92, 917504\n"
"Mvav 220,164,60\nRa 148,0,5120\nMvav 220,160,52\nWL 8\nbuffer_load_dwordx4 a[248:251], v1, s[16:19], s4 offen offset:0 sc0 sc1\nMvav 228,168,52\nRa 152,0,6144\nMvav 228,172,60\n"
"D1 1,16,46,1024\nMvav 224,172,56\nRa 156,0,7168\nWV 12\nAR 22,195\nMvav 224,168,48\nAR 23,199\nv_max_u32 v21, v22, v23\n"
"buffer_load_dwordx4 a[252:255], v1, s[16:19], s4 offen offset:1024 sc0 sc1\nMvav 232,176,48\nRa 160,0,8192\nAR 22,203\nMvav 232,180,56\nAR 23,207\nv_max3_u32 v21, v21, v22, v23\n"
"Mvav 236,180,60\nRa 164,0,9216\nAR 22,211\nMvav 236,176,52\nWL 6\nAR 23,215\nv_max3_u32 v21, v21, v22, v23\nMvav 244,184,52\nRa 168,0,10240\nAR 22,219\nMvav 244,188,60\n"
"D1 1,16,46,2048\nAR 23,223\nMvav 240,188,56\nRa 172,0,11264\nv_max3_u32 v21, v21, v22, v23\nMvav 240,184,48\nv_cmp_eq_u32 vcc, -1, v21\ns_nop 0\ns_cbranch_vccnz .S199\n.S200:\n"
"Mvaa 136,128,192\nRa 176,0,12288\nMvaa 136,132,200\nMvaa 136,136,208\nRa 180,0,13312\nMvaa 136,140,216\nWL 6\nMvaa 140,140,220\nRa 184,0,14336\nMvaa 140,136,212\nD1 1,16,46,3072\n"
"Mvaa 140,132,204\nRa 188,0,15360\nMvaa 140,128,196\nMvaa 144,144,192\nRa 128,0,16384\nMvaa 144,148,200\nMvaa 144,152,208\nRa 132,0,17408\nMvaa 144,156,216\nWL 6\nMvaa 148,156,220\n"
"Ra 136,0,18432\nMvaa 148,152,212\nMvaa 148,148,204\nRa 140,0,19456\nMvaa 148,144,196\nMvaa 152,160,192\nRa 144,0,20480\nMvaa 152,164,200\nMvaa 152,168,208\nRa 148,0,21504\n"
"Mvaa 152,172,216\nWL 6\nMvaa 156,172,220\nRa 152,0,22528\nMvaa 156,168,212\nMvaa 156,164,204\nRa 156,0,23552\nWV 2\nMvaa 156,160,196\nAR 22,227\nAR 23,231\nMvaa 160,176,192\n"
"Ra 160,0,24576\nv_max_u32 v21, v22, v23\nAR 22,235\nMvaa 160,180,200\nAR 23,239\nv_max3_u32 v21, v21, v22, v23\nMvaa 160,184,208\nRa 164,0,25600\nAR 22,243\nMvaa 160,188,216\n"
"WL 6\nAR 23,247\nMvaa 164,188,220\nRa 168,0,26624\nv_max3_u32 v21, v21, v22, v23\nMvaa 164,184,212\nAR 22,251\nAR 23,255\nMvaa 164,180,204\nRa 172,0,27648\n"
"v_max3_u32 v21, v21, v22, v23\nMvaa 164,176,196\nv_cmp_eq_u32 vcc, -1, v21\ns_nop 0\ns_cbranch_vccnz .S202\n.S203:\nMvaa 136,128,224\nRa 176,0,28672\nMvaa 136,132,232\n"
"Mvaa 136,136,240\nRa 180,0,29696\nMvaa 136,140,248\nWL 6\nMvaa 140,140,252\nRa 184,0,30720\nMvaa 140,136,244\nMvaa 140,132,236\nRa 188,0,31744\ns_barrier\nMvaa 140,128,228\n"
"Mvaa 144,144,224\nRa 128,2,16384\nMvaa 144,148,232\nMvaa 144,152,240\nRa 132,2,17408\nv_sub_f32 v92, 1.0, v80\nMvaa 144,156,248\nWL 6\nMvaa 148,156,252\nRa 136,2,18432\n"
"Mvaa 148,152,244\nv_sub_f32 v93, 1.0, v81\nCV 92,92,93\nMvaa 148,148,236\nRa 140,2,19456\nMvaa 148,144,228\nv_and_b32 v93, 0xffff0000, v92\nCV 136,136,137\nMvaa 152,160,224\n"
"Ra 144,2,20480\nv_lshlrev_b32 v92, 16, v92\nMvaa 152,164,232\nv_lshlrev_b32 v64, 16, v12\nCV 137,138,139\nMvaa 152,168,240\nRa 148,2,21504\nCV 138,144,145\nMvaa 152,172,248\nWL 6\n"
"CV 139,146,147\nMvaa 156,172,252\nRa 152,2,22528\nv_and_b32 v65, 0xffff0000, v12\nMvaa 156,168,244\nMU 64,64,92\ns_add_u32 s5, s95, 0\nMvaa 156,164,236\nRa 156,2,23552\n"
"Sv 136,1,16,5,0\nMvaa 156,160,228\nMU 65,65,92\nv_lshlrev_b32 v66, 16, v13\nMvaa 160,176,224\nRa 160,2,24576\nCV 140,140,141\nMvaa 160,180,232\nv_and_b32 v67, 0xffff0000, v13\n"
"CV 141,142,143\nMvaa 160,184,240\nRa 164,2,25600\nMU 66,66,92\nMvaa 160,188,248\nWL 6\nCV 142,148,149\nMvaa 164,188,252\nRa 168,2,26624\nCV 143,150,151\nMvaa 164,184,244\n"
"MU 67,67,92\nv_lshlrev_b32 v68, 16, v14\nMvaa 164,180,236\nRa 172,2,27648\nSv 140,1,16,5,1024\nMvaa 164,176,228\nv_and_b32 v69, 0xffff0000, v14\nCV 152,152,153\nMvaa 168,128,192\n"
"Ra 176,2,28672\nMU 68,68,92\nMvaa 168,132,200\nv_bfe_u32 v24, v1, 4, 3\nCV 153,154,155\nMvaa 168,136,208\nRa 180,2,29696\nCV 154,160,161\nMvaa 168,140,216\nWL 6\nCV 155,162,163\n"
"Mvaa 172,140,220\nRa 184,2,30720\nMU 69,69,92\nMvaa 172,136,212\nv_mul_u32_u24 v24, 0xc00, v24\ns_add_u32 s5, s95, 131072\nMvaa 172,132,204\nRa 188,2,31744\nSv 152,1,16,5,0\n"
"Mvaa 172,128,196\nv_bfe_u32 v26, v1, 7, 1\nv_lshlrev_b32 v70, 16, v15\nMvaa 172,144,228\nRa 128,2,0\nCV 156,156,157\nMvaa 172,148,236\nv_add_lshl_u32 v24, v24, v26, 13\n"
"CV 157,158,159\nMvaa 172,152,244\nRa 132,2,1024\nv_and_b32 v71, 0xffff0000, v15\nMvaa 172,156,252\nWL 6\nCV 158,164,165\nMvaa 168,156,248\nRa 136,2,2048\nCV 159,166,167\n"
"Mvaa 168,152,240\nbuffer_load_dword v27, v24, s[16:19], s41 offen sc0 sc1\nMU 70,70,92\nMvaa 168,148,232\nRa 140,2,3072\nSv 156,1,16,5,1024\nMvaa 168,144,224\nMU 71,71,92\n"
"Mvaa 176,160,192\nRa 144,2,4096\nv_lshlrev_b32 v72, 16, v16\nMvaa 176,164,200\nv_and_b32 v73, 0xffff0000, v16\nMvaa 176,168,208\nRa 148,2,5120\nMvaa 176,172,216\nWL 6\n"
"Mvaa 180,172,220\nRa 152,2,6144\nMU 72,72,93\nMvaa 180,168,212\nMU 73,73,93\nMvaa 180,164,204\nRa 156,2,7168\nMvaa 180,160,196\nv_lshlrev_b32 v74, 16, v17\n"
"v_and_b32 v75, 0xffff0000, v17\nMvaa 180,176,228\nRa 160,2,8192\nMvaa 180,180,236\nMU 74,74,93\nMvaa 180,184,244\nRa 164,2,9216\nMU 75,75,93\nMvaa 180,188,252\nWL 6\n"
"Mvaa 176,188,248\nRa 168,2,10240\nMvaa 176,184,240\nv_lshlrev_b32 v76, 16, v18\nv_and_b32 v77, 0xffff0000, v18\nMvaa 176,180,232\nRa 172,2,11264\nMvaa 176,176,224\nMU 76,76,93\n"
"Mvaa 184,128,192\nRa 176,2,12288\nMU 77,77,93\nMvaa 184,132,200\nv_lshlrev_b32 v78, 16, v19\nMvaa 184,136,208\nRa 180,2,13312\nMvaa 184,140,216\nWL 6\nCV 168,168,169\n"
"Mvaa 188,140,220\nRa 184,2,14336\nv_and_b32 v79, 0xffff0000, v19\nMvaa 188,136,212\nMU 78,78,93\nCV 169,170,171\nMvaa 188,132,204\nRa 188,2,15360\ns_barrier\nMvaa 188,128,196\n"
"MU 79,79,93\nCV 170,176,177\nMvaa 188,144,228\nRa 128,2,32768\nCV 171,178,179\nMvaa 188,148,236\ns_add_u32 s5, s95, 262144\nSv 168,1,16,5,0\nMvaa 188,152,244\nRa 132,2,33792\n"
"CV 172,172,173\nMvaa 188,156,252\nWL 6\nCV 173,174,175\nMvaa 184,156,248\nRa 136,2,34816\nCV 174,180,181\nMvaa 184,152,240\nCV 175,182,183\nSv 172,1,16,5,1024\nMvaa 184,148,232\n"
"Ra 140,2,35840\nMvaa 184,144,224\nMvaa 192,160,192\nRa 144,2,36864\nMvaa 192,164,200\nMvaa 192,168,208\nRa 148,2,37888\nMvaa 192,172,216\nWL 6\nMvaa 196,172,220\nRa 152,2,38912\n"
"Mvaa 196,168,212\nMvaa 196,164,204\nRa 156,2,39936\nMvaa 196,160,196\nMvaa 196,176,228\nRa 160,2,40960\nMvaa 196,180,236\nMvaa 196,184,244\nRa 164,2,41984\nMvaa 196,188,252\nWL 6\n"
"Mvaa 192,188,248\nRa 168,2,43008\nMvaa 192,184,240\nMvaa 192,180,232\nRa 172,2,44032\nMvaa 192,176,224\nMvaa 200,128,192\nRa 176,2,45056\nMvaa 200,132,200\nMvaa 200,136,208\n"
"Ra 180,2,46080\nMvaa 200,140,216\nWL 6\nCV 184,184,185\nMvaa 204,140,220\nRa 184,2,47104\nCV 185,186,187\nMvaa 204,136,212\nCV 186,192,193\nCV 187,194,195\nMvaa 204,132,204\n"
"Ra 188,2,48128\ns_add_u32 s5, s95, 393216\nMvaa 204,128,196\nSv 184,1,16,5,0\nCV 188,188,189\nMvaa 204,144,228\nRa 128,2,49152\nCV 189,190,191\nMvaa 204,148,236\nCV 190,196,197\n"
"CV 191,198,199\nMvaa 204,152,244\nRa 132,2,50176\nSv 188,1,16,5,1024\nMvaa 204,156,252\nWL 6\nMvaa 200,156,248\nRa 136,2,51200\nMvaa 200,152,240\nMvaa 200,148,232\nRa 140,2,52224\n"
"Mvaa 200,144,224\nMvaa 208,160,192\nRa 144,2,53248\nWV 5\nv_cmp_eq_u32 s[62:63], s15, v27\ns_mov_b32 vcc_hi, 0\ns_nop 2\ns_lshr_b32 s63, s62, 8\ns_or_b32 s62, s62, s63\n"
"s_andn2_b32 vcc_lo, 0xff, s62\ns_mov_b64 vcc, vcc\ns_cbranch_vccnz .S211\n.S212:\nMvaa 208,164,200\ns_bitcmp1_b32 s63, 0\ns_cselect_b32 s2, 0x2000, 0\nMvaa 208,168,208\n"
"Ra 148,2,54272\nMvaa 208,172,216\nWL 6\nMvaa 212,172,220\nRa 152,2,55296\nMvaa 212,168,212\ns_xor_b32 s2, s2, s20\nMvaa 212,164,204\nRa 156,2,56320\nMvaa 212,160,196\n"
"s_add_u32 s24, s2, 0x0\ns_bitcmp1_b32 s63, 1\ns_cselect_b32 s2, 0x2000, 0\nMvaa 212,176,228\nRa 160,2,57344\nMvaa 212,180,236\ns_xor_b32 s2, s2, s20\nMvaa 212,184,244\n"
"Ra 164,2,58368\nMvaa 212,188,252\nWL 6\nMvaa 208,188,248\nRa 168,2,59392\nMvaa 208,184,240\ns_add_u32 s25, s2, 0x1800000\ns_bitcmp1_b32 s63, 2\ns_cselect_b32 s2, 0x2000, 0\n"
"Mvaa 208,180,232\nRa 172,2,60416\nMvaa 208,176,224\ns_xor_b32 s2, s2, s20\nMvaa 216,128,192\nRa 176,2,61440\nMvaa 216,132,200\ns_add_u32 s26, s2, 0x3000000\ns_bitcmp1_b32 s63, 3\n"
"s_cselect_b32 s2, 0x2000, 0\nMvaa 216,136,208\nRa 180,2,62464\nMvaa 216,140,216\nWL 6\nCV 200,200,201\nMvaa 220,140,220\nRa 184,2,63488\nCV 201,202,203\nMvaa 220,136,212\n"
"s_xor_b32 s2, s2, s20\nCV 202,208,209\nMvaa 220,132,204\nRa 188,2,64512\ns_barrier\nMvaa 220,128,196\ns_add_u32 s27, s2, 0x4800000\ns_bitcmp1_b32 s63, 4\n"
"s_cselect_b32 s2, 0x2000, 0\nMvaa 220,144,228\nRa 128,3,0\nCV 203,210,211\nMvaa 220,148,236\ns_xor_b32 s2, s2, s20\ns_add_u32 s5, s95, 524288\nMvaa 220,152,244\nRa 132,3,1024\n"
"Sv 200,1,16,5,0\nMvaa 220,156,252\nWL 6\nCV 204,204,205\nMvaa 216,156,248\nRa 136,3,2048\nCV 205,206,207\nMvaa 216,152,240\ns_add_u32 s32, s2, 0x6000000\ns_bitcmp1_b32 s63, 5\n"
"s_cselect_b32 s2, 0x2000, 0\nMvaa 216,148,232\nRa 140,3,3072\nCV 206,212,213\nMvaa 216,144,224\ns_xor_b32 s2, s2, s20\nCV 207,214,215\nMvaa 224,160,192\nRa 144,3,4096\n"
"Sv 204,1,16,5,1024\nMvaa 224,164,200\ns_add_u32 s33, s2, 0x7800000\ns_bitcmp1_b32 s63, 6\ns_cselect_b32 s2, 0x2000, 0\nMvaa 224,168,208\nRa 148,3,5120\nMvaa 224,172,216\nWL 6\n"
"Mvaa 228,172,220\nRa 152,3,6144\nMvaa 228,168,212\ns_xor_b32 s2, s2, s20\nMvaa 228,164,204\nRa 156,3,7168\nMvaa 228,160,196\ns_add_u32 s34, s2, 0x9000000\ns_bitcmp1_b32 s63, 7\n"
"s_cselect_b32 s2, 0x2000, 0\nMvaa 228,176,228\nRa 160,3,8192\nMvaa 228,180,236\ns_xor_b32 s2, s2, s20\nMvaa 228,184,244\nRa 164,3,9216\nMvaa 228,188,252\nWL 6\nMvaa 224,188,248\n"
"Ra 168,3,10240\nMvaa 224,184,240\ns_add_u32 s35, s2, 0xa800000\nMvaa 224,180,232\nRa 172,3,11264\nMvaa 224,176,224\nMvaa 232,128,192\nRa 176,3,12288\nMvaa 232,132,200\n"
"Mvaa 232,136,208\nRa 180,3,13312\nMvaa 232,140,216\nWL 6\nCV 216,216,217\nMvaa 236,140,220\nRa 184,3,14336\nCV 217,218,219\nMvaa 236,136,212\nCV 218,224,225\nCV 219,226,227\n"
"Mvaa 236,132,204\nRa 188,3,15360\ns_add_u32 s5, s95, 655360\nMvaa 236,128,196\nSv 216,1,16,5,0\nCV 220,220,221\nMvaa 236,144,228\nCV 221,222,223\nCV 222,228,229\nMvaa 236,148,236\n"
"CV 223,230,231\nSv 220,1,16,5,1024\nMvaa 236,152,244\nMvaa 236,156,252\nWL 4\nMvaa 232,156,248\nMvaa 232,152,240\nMvaa 232,148,232\nMvaa 232,144,224\nMvaa 240,160,192\n"
"Mvaa 240,164,200\nMvaa 240,168,208\nMvaa 240,172,216\nWL 0\nMvaa 244,172,220\nMvaa 244,168,212\nMvaa 244,164,204\nMvaa 244,160,196\nMvaa 244,176,228\nMvaa 244,180,236\n"
"Mvaa 244,184,244\nMvaa 244,188,252\nMvaa 240,188,248\nMvaa 240,184,240\nMvaa 240,180,232\nMvaa 240,176,224\nCV 232,232,233\nCV 233,234,235\ns_nop 5\nCV 234,240,241\n"
"CV 235,242,243\ns_add_u32 s5, s95, 786432\nSv 232,1,16,5,0\nCV 236,236,237\nCV 237,238,239\nCV 238,244,245\nCV 239,246,247\nSv 236,1,16,5,1024\ns_cmp_eq_u32 s13, 0\n"
"s_cbranch_scc1 .Z214\nbuffer_load_dwordx4 v[96:99], v1, s[16:19], s24 offen offset:0 sc0 sc1\nbuffer_load_dwordx4 v[100:103], v1, s[16:19], s24 offen offset:1024 sc0 sc1\n"
"buffer_load_dwordx4 v[104:107], v1, s[16:19], s25 offen offset:0 sc0 sc1\nbuffer_load_dwordx4 v[108:111], v1, s[16:19], s25 offen offset:1024 sc0 sc1\n"
"buffer_load_dwordx4 v[112:115], v1, s[16:19], s26 offen offset:0 sc0 sc1\nbuffer_load_dwordx4 v[116:119], v1, s[16:19], s26 offen offset:1024 sc0 sc1\n"
"buffer_load_dwordx4 v[120:123], v1, s[16:19], s27 offen offset:0 sc0 sc1\nbuffer_load_dwordx4 v[124:127], v1, s[16:19], s27 offen offset:1024 sc0 sc1\n"
"buffer_load_dwordx4 v[32:35], v1, s[16:19], s32 offen offset:0 sc0 sc1\nbuffer_load_dwordx4 v[36:39], v1, s[16:19], s32 offen offset:1024 sc0 sc1\n"
"buffer_load_dwordx4 v[40:43], v1, s[16:19], s33 offen offset:0 sc0 sc1\nbuffer_load_dwordx4 v[44:47], v1, s[16:19], s33 offen offset:1024 sc0 sc1\n"
"buffer_load_dwordx4 v[48:51], v1, s[16:19], s34 offen offset:0 sc0 sc1\nbuffer_load_dwordx4 v[52:55], v1, s[16:19], s34 offen offset:1024 sc0 sc1\n"
"buffer_load_dwordx4 v[56:59], v1, s[16:19], s35 offen offset:0 sc0 sc1\nbuffer_load_dwordx4 v[60:63], v1, s[16:19], s35 offen offset:1024 sc0 sc1\ns_mov_b32 s7, 262144\nWV 0\n"
".Z217:\nv_max_u32 v82, v99, v103\nv_cmp_eq_u32 s[44:45], -1, v82\nv_max_u32 v82, v107, v111\nv_cmp_eq_u32 s[46:47], -1, v82\nv_max_u32 v82, v115, v119\n"
"v_cmp_eq_u32 s[48:49], -1, v82\nv_max_u32 v82, v123, v127\nv_cmp_eq_u32 s[50:51], -1, v82\nv_max_u32 v82, v35, v39\nv_cmp_eq_u32 s[52:53], -1, v82\nv_max_u32 v82, v43, v47\n"
"v_cmp_eq_u32 s[54:55], -1, v82\nv_max_u32 v82, v51, v55\nv_cmp_eq_u32 s[56:57], -1, v82\nv_max_u32 v82, v59, v63\nv_cmp_eq_u32 s[58:59], -1, v82\n"
"s_or_b64 s[0:1], s[44:45], s[46:47]\ns_or_b64 s[2:3], s[48:49], s[50:51]\ns_or_b64 s[0:1], s[0:1], s[52:53]\ns_or_b64 s[2:3], s[2:3], s[54:55]\ns_or_b64 s[0:1], s[0:1], s[56:57]\n"
"s_or_b64 s[2:3], s[2:3], s[58:59]\ns_or_b64 s[0:1], s[0:1], s[2:3]\ns_cbranch_scc1 .Z215\n.Z216:\nZvvv 136,130,96\nMvvv 136,130,104\nMvvv 136,130,112\nMvvv 136,130,120\n"
"Mvvv 136,130,32\nMvvv 136,130,40\nMvvv 136,130,48\nMvvv 136,130,56\nZvvv 140,128,96\nMvvv 140,128,104\nMvvv 140,128,112\nMvvv 140,128,120\nMvvv 140,128,32\nMvvv 140,128,40\n"
"Mvvv 140,128,48\nMvvv 140,128,56\nZvvv 144,130,100\nMvvv 144,130,108\nMvvv 144,130,116\nMvvv 144,130,124\nMvvv 144,130,36\nMvvv 144,130,44\nMvvv 144,130,52\nMvvv 144,130,60\n"
"Zvvv 148,128,100\nMvvv 148,128,108\nMvvv 148,128,116\nMvvv 148,128,124\nMvvv 148,128,36\nMvvv 148,128,44\nMvvv 148,128,52\nMvvv 148,128,60\nFM 64,80,136\nFM 65,80,137\n"
"FM 66,80,138\nFM 67,80,139\nFM 68,80,140\nFM 69,80,141\nFM 70,80,142\nFM 71,80,143\nCV 12,64,65\nCV 13,66,67\nCV 14,68,69\nCV 15,70,71\nSv 12,91,36,22,0\nFM 72,81,144\n"
"FM 73,81,145\nFM 74,81,146\nFM 75,81,147\nFM 76,81,148\nFM 77,81,149\nFM 78,81,150\nFM 79,81,151\nCV 16,72,73\nCV 17,74,75\nCV 18,76,77\nCV 19,78,79\nSv 16,91,36,23,0\n.Z214:\n"
"s_endpgm\n.HD131:\nbuffer_store_dword v25, off, s[16:19], s54 offset:4 sc0 sc1\nv_cmp_ne_u32 vcc, s15, v27\ns_nop 0\ns_cbranch_vccnz .S226\n.S227:\nv_and_b32 v27, 0x70, v1\n"
"buffer_load_dword v27, v27, s[16:19], s61 offen offset:8 sc0 sc1\ns_mov_b32 s90, 1\ns_swappc_b64 s[8:9], s[80:81]\nv_cmp_ne_u32 vcc, s15, v27\ns_nop 0\ns_cbranch_vccnz .S229\n"
".S230:\nv_and_b32 v27, 0x1f0, v1\nbuffer_load_dword v27, v27, s[16:19], s58 offen offset:4 sc0 sc1\ns_mov_b32 s90, 1\ns_swappc_b64 s[8:9], s[80:81]\nv_cmp_ne_u32 vcc, s15, v27\n"
"s_nop 0\ns_cbranch_vccnz .S232\n.S233:\ns_mov_b32 s90, s91\ns_swappc_b64 s[8:9], s[80:81]\nv_lshrrev_b32 v144, 9, v1\nv_mul_u32_u24 v144, 0x18000, v144\n"
"v_lshlrev_b32 v144, 8, v144\nv_and_b32 v145, 0x1f0, v1\nv_add_u32 v144, v144, v145\ns_mov_b32 s6, 0x15c0000\nbuffer_load_dword v136, v144, s[16:19], s6 offen offset:4 sc0 sc1\n"
"s_mov_b32 s6, 0x15c2000\nbuffer_load_dword v137, v144, s[16:19], s6 offen offset:4 sc0 sc1\ns_mov_b32 s6, 0x45c0000\n"
"buffer_load_dword v138, v144, s[16:19], s6 offen offset:4 sc0 sc1\ns_mov_b32 s6, 0x45c2000\nbuffer_load_dword v139, v144, s[16:19], s6 offen offset:4 sc0 sc1\n"
"s_mov_b32 s6, 0x75c0000\nbuffer_load_dword v140, v144, s[16:19], s6 offen offset:4 sc0 sc1\ns_mov_b32 s6, 0x75c2000\n"
"buffer_load_dword v141, v144, s[16:19], s6 offen offset:4 sc0 sc1\ns_mov_b32 s6, 0xa5c0000\nbuffer_load_dword v142, v144, s[16:19], s6 offen offset:4 sc0 sc1\n"
"s_mov_b32 s6, 0xa5c2000\nbuffer_load_dword v143, v144, s[16:19], s6 offen offset:4 sc0 sc1\ns_mov_b32 s90, 0\ns_swappc_b64 s[8:9], s[80:81]\nv_xor_b32 v136, s15, v136\n"
"v_xor_b32 v137, s15, v137\nv_xor_b32 v138, s15, v138\nv_xor_b32 v139, s15, v139\nv_xor_b32 v140, s15, v140\nv_xor_b32 v141, s15, v141\nv_xor_b32 v142, s15, v142\n"
"v_xor_b32 v143, s15, v143\nv_min_u32 v136, v136, v137\nv_min_u32 v138, v138, v139\nv_min_u32 v140, v140, v141\nv_min_u32 v142, v142, v143\nv_or3_b32 v136, v136, v138, v140\n"
"v_or_b32 v136, v136, v142\nv_cmp_ne_u32 vcc, 0, v136\ns_nop 0\ns_cbranch_vccnz .S235\n.S236:\ns_and_b32 s4, s13, 3\ns_lshl2_add_u32 s4, s12, s4\ns_lshr_b32 s5, s71, 16\n"
"s_cmp_lt_u32 s4, 28\ns_cselect_b32 s5, s71, s5\ns_cselect_b32 s3, 0, 28\ns_and_b32 s5, s5, 0x7ff\ns_sub_u32 s3, s4, s3\ns_mul_i32 s2, s4, 7168\ns_add_u32 s2, s2, 0xc04c800\n"
"s_add_u32 s6, s2, 4096\nbuffer_load_dwordx4 v[136:139], v1, s[16:19], s2 offen offset:0 sc0 sc1\nbuffer_load_dwordx4 v[140:143], v1, s[16:19], s2 offen offset:1024 sc0 sc1\n"
"buffer_load_dwordx4 v[144:147], v1, s[16:19], s2 offen offset:2048 sc0 sc1\nbuffer_load_dwordx4 v[148:151], v1, s[16:19], s2 offen offset:3072 sc0 sc1\n"
"buffer_load_dwordx4 v[152:155], v1, s[16:19], s6 offen offset:0 sc0 sc1\nbuffer_load_dwordx4 v[156:159], v1, s[16:19], s6 offen offset:1024 sc0 sc1\n"
"buffer_load_dwordx4 v[160:163], v1, s[16:19], s6 offen offset:2048 sc0 sc1\nv_lshrrev_b32 v167, 1, v1\nv_and_b32 v167, 24, v167\ns_mul_i32 s2, s5, 896\ns_lshl_b32 s3, s3, 5\n"
"s_add_u32 s2, s2, s3\ns_lshl2_add_u32 s5, s5, 0xc042000\nbuffer_load_dwordx2 v[164:165], v167, s[28:31], s2 offen\nbuffer_load_dword v166, off, s[16:19], s5 sc0 sc1\n"
"s_mov_b32 s90, 0\ns_swappc_b64 s[8:9], s[80:81]\nAD 136,136,140\nAD 137,137,141\nAD 138,138,142\nAD 139,139,143\nAD 144,144,148\nAD 145,145,149\nAD 146,146,150\nAD 147,147,151\n"
"AD 152,152,156\nAD 153,153,157\nAD 154,154,158\nAD 155,155,159\nAD 136,136,144\nAD 137,137,145\nAD 138,138,146\nAD 139,139,147\nAD 152,152,160\nAD 153,153,161\nAD 154,154,162\n"
"AD 155,155,163\nAD 136,136,152\nAD 137,137,153\nAD 138,138,154\nAD 139,139,155\nv_mov_b32 v140, v136\nv_mov_b32 v141, v137\nv_mov_b32 v142, v138\nv_mov_b32 v143, v139\n"
"v_permlane32_swap_b32 v136, v140\nv_permlane32_swap_b32 v137, v141\nv_permlane32_swap_b32 v138, v142\nv_permlane32_swap_b32 v139, v143\nAD 136,136,140\nAD 137,137,141\n"
"AD 138,138,142\nAD 139,139,143\nv_mov_b32 v140, v136\nv_mov_b32 v141, v137\nv_mov_b32 v142, v138\nv_mov_b32 v143, v139\nv_permlane16_swap_b32 v136, v140\n"
"v_permlane16_swap_b32 v137, v141\nv_permlane16_swap_b32 v138, v142\nv_permlane16_swap_b32 v139, v143\nAD 136,136,140\nAD 137,137,141\nAD 138,138,142\nAD 139,139,143\n"
"v_add_f32_dpp v136, v136, v136 row_ror:8 row_mask:0xf bank_mask:0xf\nv_add_f32_dpp v137, v137, v137 row_ror:8 row_mask:0xf bank_mask:0xf\n"
"v_add_f32_dpp v138, v138, v138 row_ror:8 row_mask:0xf bank_mask:0xf\nv_add_f32_dpp v139, v139, v139 row_ror:8 row_mask:0xf bank_mask:0xf\n"
"v_add_f32_dpp v136, v136, v136 row_ror:4 row_mask:0xf bank_mask:0xf\nv_add_f32_dpp v137, v137, v137 row_ror:4 row_mask:0xf bank_mask:0xf\n"
"v_add_f32_dpp v138, v138, v138 row_ror:4 row_mask:0xf bank_mask:0xf\nv_add_f32_dpp v139, v139, v139 row_ror:4 row_mask:0xf bank_mask:0xf\nv_mul_f32 v166, 0xbfb8aa3b, v166\n"
"EX 166,166\nCV 136,136,137\nv_add_f32 v166, 1.0, v166\nv_rcp_f32 v166, v166\nCV 138,138,139\nCV 166,166,166\nv_lshlrev_b32 v166, 16, v166\nv_and_b32 v137, 0xffff0000, v136\n"
"v_lshlrev_b32 v136, 16, v136\nv_and_b32 v139, 0xffff0000, v138\nv_lshlrev_b32 v138, 16, v138\nMU 136,136,166\nMU 137,137,166\nMU 138,138,166\nMU 139,139,166\nCV 136,136,137\n"
"CV 138,138,139\nv_and_b32 v137, 0xffff0000, v136\nv_lshlrev_b32 v136, 16, v136\nv_and_b32 v139, 0xffff0000, v138\nv_lshlrev_b32 v138, 16, v138\nv_lshlrev_b32 v140, 16, v164\n"
"v_and_b32 v141, 0xffff0000, v164\nv_lshlrev_b32 v142, 16, v165\nv_and_b32 v143, 0xffff0000, v165\nAD 136,136,140\nAD 137,137,141\nAD 138,138,142\nAD 139,139,143\nCV 136,136,137\n"
"CV 137,138,139\nv_mov_b32 v138, s15\nv_mov_b32 v139, s71\ns_and_b32 s4, s13, 3\ns_lshl2_add_u32 s4, s12, s4\ns_lshr_b32 s5, s71, 16\ns_cmp_lt_u32 s4, 28\n"
"s_cselect_b32 s5, s71, s5\ns_cselect_b32 s3, 0, 28\ns_and_b32 s5, s5, 0x7ff\ns_sub_u32 s3, s4, s3\nv_mov_b32 v140, 0x40000000\nv_cmp_gt_u32 vcc, 64, v1\ns_lshl_b32 s4, s4, 6\n"
"s_add_u32 s4, s4, 0xc04b800\nv_cndmask_b32 v140, v140, v1, vcc\nSv 136,140,16,4,0\ns_mov_b32 s90, 100\ns_swappc_b64 s[8:9], s[80:81]\n.S132:\ns_mov_b32 s7, 262144\n.S134:\n"
"buffer_load_dwordx4 v[32:35], v1, s[16:19], s6 offen offset:0 sc0 sc1\nbuffer_load_dwordx4 v[36:39], v1, s[16:19], s6 offen offset:1024 sc0 sc1\n"
"buffer_load_dwordx4 v[40:43], v1, s[16:19], s6 offen offset:2048 sc0 sc1\nbuffer_load_dwordx4 v[44:47], v1, s[16:19], s6 offen offset:3072 sc0 sc1\nWV 0\nv_xor_b32 v48, s15, v33\n"
"v_xor_b32 v49, s15, v35\nv_xor_b32 v50, s15, v37\nv_xor_b32 v51, s15, v39\nv_xor_b32 v52, s15, v41\nv_xor_b32 v53, s15, v43\nv_xor_b32 v54, s15, v45\nv_xor_b32 v55, s15, v47\n"
"v_or3_b32 v48, v48, v49, v50\nv_or3_b32 v51, v51, v52, v53\nv_or3_b32 v48, v48, v54, v55\nv_or_b32 v48, v48, v51\nv_cmp_ne_u32 vcc, 0, v48\ns_nop 0\ns_cbranch_vccz .S133\n"
"s_sleep 6\ns_sub_u32 s7, s7, 1\ns_cbranch_scc0 .S134\ns_branch .S133\n.S135:\ns_mov_b32 s7, 262144\n.S137:\ns_lshl_b32 s5, s73, 8\ns_add_u32 s5, s5, 0xc04ac00\n"
"v_lshrrev_b32 v27, 2, v1\nbuffer_load_dword v27, v27, s[16:19], s5 offen sc0 sc1\nWV 0\nv_xor_b32 v27, s15, v27\nv_and_b32 v255, 0xffffdfff, v27\nv_cmp_ne_u32 vcc, 0, v255\n"
"s_nop 0\ns_cbranch_vccz .S136\ns_sleep 6\ns_sub_u32 s7, s7, 1\ns_cbranch_scc0 .S137\ns_branch .S136\n.S140:\ns_mov_b32 s7, 262144\n.S142:\nv_and_b32 v27, 0x70, v1\n"
"buffer_load_dword v27, v27, s[16:19], s61 offen offset:128 sc0 sc1\nWV 0\nv_cmp_ne_u32 vcc, s15, v27\ns_nop 0\ns_cbranch_vccz .S141\ns_sleep 6\ns_sub_u32 s7, s7, 1\n"
"s_cbranch_scc0 .S142\ns_branch .S141\n.T144:\ns_mov_b32 s62, 1\ns_call_b64 s[80:81], .RA10\ns_branch .T145\n.T146:\ns_mov_b32 s62, 1\ns_call_b64 s[80:81], .RA10\ns_branch .T147\n"
".T149:\ns_mov_b32 s62, 0\ns_call_b64 s[80:81], .RB11\ns_branch .T150\n.T151:\ns_mov_b32 s62, 0\ns_call_b64 s[80:81], .RA10\ns_branch .T152\n.T154:\ns_mov_b32 s62, 0\n"
"s_call_b64 s[80:81], .RB11\ns_branch .T155\n.T156:\ns_mov_b32 s62, 0\ns_call_b64 s[80:81], .RA10\ns_branch .T157\n.S158:\ns_mov_b32 s7, 262144\n.S160:\nv_and_b32 v27, 0x70, v1\n"
"buffer_load_dword v27, v27, s[16:19], s61 offen offset:140 sc0 sc1\nWV 0\nv_cmp_ne_u32 vcc, s15, v27\ns_nop 0\ns_cbranch_vccz .S159\ns_sleep 6\ns_sub_u32 s7, s7, 1\n"
"s_cbranch_scc0 .S160\ns_branch .S159\n.S161:\ns_mov_b32 s7, 262144\n.S163:\nv_and_b32 v27, 0x70, v1\nbuffer_load_dword v27, v27, s[16:19], s61 offen offset:136 sc0 sc1\nWV 0\n"
"v_cmp_ne_u32 vcc, s15, v27\ns_nop 0\ns_cbranch_vccz .S162\ns_sleep 6\ns_sub_u32 s7, s7, 1\ns_cbranch_scc0 .S163\ns_branch .S162\n.E166:\ns_swappc_b64 s[80:81], s[8:9]\n"
"s_branch .E167\n.T168:\ns_mov_b32 s62, 0\ns_call_b64 s[80:81], .RB11\ns_branch .T169\n.T170:\ns_mov_b32 s62, 0\ns_call_b64 s[80:81], .RA10\ns_branch .T171\n.E173:\n"
"v_cmpx_gt_i32 vcc, 19, v3\nv_mov_b32 v15, s97\nv_cmpx_gt_i32 vcc, 18, v3\nv_mov_b32 v14, s97\nv_cmpx_gt_i32 vcc, 17, v3\nv_mov_b32 v13, s97\nv_cmpx_gt_i32 vcc, 16, v3\n"
"v_mov_b32 v12, s97\nv_cmpx_gt_i32 vcc, 3, v3\nv_mov_b32 v7, s97\nv_cmpx_gt_i32 vcc, 2, v3\nv_mov_b32 v6, s97\nv_cmpx_gt_i32 vcc, 1, v3\nv_mov_b32 v5, s97\n"
"v_cmpx_gt_i32 vcc, 0, v3\nv_mov_b32 v4, s97\ns_mov_b64 exec, -1\ns_nop 2\ns_branch .E174\n.T175:\ns_mov_b32 s62, 0\ns_call_b64 s[80:81], .RB11\ns_branch .T176\n.T177:\n"
"s_mov_b32 s62, 0\ns_call_b64 s[80:81], .RA10\ns_branch .T178\n.E180:\nv_cmpx_gt_i32 vcc, 19, v0\nv_mov_b32 v15, s97\nv_cmpx_gt_i32 vcc, 18, v0\nv_mov_b32 v14, s97\n"
"v_cmpx_gt_i32 vcc, 17, v0\nv_mov_b32 v13, s97\nv_cmpx_gt_i32 vcc, 16, v0\nv_mov_b32 v12, s97\nv_cmpx_gt_i32 vcc, 3, v0\nv_mov_b32 v7, s97\nv_cmpx_gt_i32 vcc, 2, v0\n"
"v_mov_b32 v6, s97\nv_cmpx_gt_i32 vcc, 1, v0\nv_mov_b32 v5, s97\nv_cmpx_gt_i32 vcc, 0, v0\nv_mov_b32 v4, s97\ns_mov_b64 exec, -1\ns_nop 2\ns_branch .E181\n.T182:\ns_mov_b32 s62, 0\n"
"s_call_b64 s[80:81], .RB11\ns_branch .T183\n.T184:\ns_mov_b32 s62, 0\ns_call_b64 s[80:81], .RA10\ns_branch .T185\n.E187:\n"
"buffer_load_dwordx4 v[100:103], v108, s[16:19], s65 offen offset:0 sc0 sc1\nbuffer_load_dwordx4 v[104:107], v108, s[16:19], s65 offen offset:256 sc0 sc1\ns_branch .E188\n.S193:\n"
"s_mov_b32 s7, 262144\n.S195:\nbuffer_load_dwordx4 v[100:103], v108, s[16:19], s65 offen offset:0 sc0 sc1\n"
"buffer_load_dwordx4 v[104:107], v108, s[16:19], s65 offen offset:256 sc0 sc1\nWV 0\nv_xor_b32 v109, s15, v102\nv_xor_b32 v110, s15, v106\nv_or_b32 v109, v109, v110\n"
"v_cmp_ne_u32 vcc, 0, v109\ns_nop 0\ns_cbranch_vccz .S194\ns_sleep 6\ns_sub_u32 s7, s7, 1\ns_cbranch_scc0 .S195\ns_branch .S194\n.D189:\nWV 0\nv_xor_b32 v109, s15, v102\n"
"v_xor_b32 v110, s15, v106\nv_or_b32 v109, v109, v110\nv_cmp_ne_u32 vcc, 0, v109\ns_nop 0\ns_cbranch_vccnz .S193\n.S194:\ns_add_u32 s5, s66, 0\n"
"buffer_load_dwordx4 a[192:195], v1, s[16:19], s5 offen offset:0 sc1\nbuffer_load_dwordx4 a[196:199], v1, s[16:19], s5 offen offset:1024 sc1\n"
"buffer_load_dwordx4 a[200:203], v1, s[16:19], s5 offen offset:2048 sc1\nbuffer_load_dwordx4 a[204:207], v1, s[16:19], s5 offen offset:3072 sc1\ns_add_u32 s5, s66, 4096\n"
"buffer_load_dwordx4 a[208:211], v1, s[16:19], s5 offen offset:0 sc1\nbuffer_load_dwordx4 a[212:215], v1, s[16:19], s5 offen offset:1024 sc1\n"
"buffer_load_dwordx4 a[216:219], v1, s[16:19], s5 offen offset:2048 sc1\nbuffer_load_dwordx4 a[220:223], v1, s[16:19], s5 offen offset:3072 sc1\ns_add_u32 s5, s66, 16384\n"
"buffer_load_dwordx4 a[224:227], v1, s[16:19], s5 offen offset:0 sc1\nbuffer_load_dwordx4 a[228:231], v1, s[16:19], s5 offen offset:1024 sc1\n"
"buffer_load_dwordx4 a[232:235], v1, s[16:19], s5 offen offset:2048 sc1\nbuffer_load_dwordx4 a[236:239], v1, s[16:19], s5 offen offset:3072 sc1\ns_add_u32 s5, s66, 20480\n"
"buffer_load_dwordx4 a[240:243], v1, s[16:19], s5 offen offset:0 sc1\nbuffer_load_dwordx4 a[244:247], v1, s[16:19], s5 offen offset:1024 sc1\n"
"buffer_load_dwordx4 a[248:251], v1, s[16:19], s5 offen offset:2048 sc1\nbuffer_load_dwordx4 a[252:255], v1, s[16:19], s5 offen offset:3072 sc1\nWV 0\ns_mov_b32 s67, 0\n"
"s_branch .D190\n.D191:\nv_mov_b32 v130, 0\nv_mov_b32 v131, 0\ns_mov_b32 s63, 0xff800000\ns_mov_b32 s67, 1\ns_branch .D192\n.T196:\ns_lshl3_add_u32 s62, s67, 2\n"
"s_call_b64 s[80:81], .RB11\ns_cmp_eq_u32 s67, 0\ns_cbranch_scc1 .T197\nSU 20,100,88\nv_min_f32 v20, 0, v20\nEX 20,20\ns_nop 0\nFM 86,101,20\nSU 21,104,89\nv_min_f32 v21, 0, v21\n"
"EX 21,21\ns_nop 0\nFM 87,105,21\nAR 22,192\nv_and_b32 v23, 0xffff0000, v22\nv_lshlrev_b32 v22, 16, v22\nMU 22,22,20\nMU 23,23,20\nCV 22,22,23\nAW 192,22\nAR 22,193\n"
"v_and_b32 v23, 0xffff0000, v22\nv_lshlrev_b32 v22, 16, v22\nMU 22,22,20\nMU 23,23,20\nCV 22,22,23\nAW 193,22\nAR 22,194\nv_and_b32 v23, 0xffff0000, v22\n"
"v_lshlrev_b32 v22, 16, v22\nMU 22,22,20\nMU 23,23,20\nCV 22,22,23\nAW 194,22\nAR 22,195\nv_and_b32 v23, 0xffff0000, v22\nv_lshlrev_b32 v22, 16, v22\nMU 22,22,20\nMU 23,23,20\n"
"CV 22,22,23\nAW 195,22\nAR 22,196\nv_and_b32 v23, 0xffff0000, v22\nv_lshlrev_b32 v22, 16, v22\nMU 22,22,21\nMU 23,23,21\nCV 22,22,23\nAW 196,22\nAR 22,197\n"
"v_and_b32 v23, 0xffff0000, v22\nv_lshlrev_b32 v22, 16, v22\nMU 22,22,21\nMU 23,23,21\nCV 22,22,23\nAW 197,22\nAR 22,198\nv_and_b32 v23, 0xffff0000, v22\n"
"v_lshlrev_b32 v22, 16, v22\nMU 22,22,21\nMU 23,23,21\nCV 22,22,23\nAW 198,22\nAR 22,199\nv_and_b32 v23, 0xffff0000, v22\nv_lshlrev_b32 v22, 16, v22\nMU 22,22,21\nMU 23,23,21\n"
"CV 22,22,23\nAW 199,22\nAR 22,200\nv_and_b32 v23, 0xffff0000, v22\nv_lshlrev_b32 v22, 16, v22\nMU 22,22,20\nMU 23,23,20\nCV 22,22,23\nAW 200,22\nAR 22,201\n"
"v_and_b32 v23, 0xffff0000, v22\nv_lshlrev_b32 v22, 16, v22\nMU 22,22,20\nMU 23,23,20\nCV 22,22,23\nAW 201,22\nAR 22,202\nv_and_b32 v23, 0xffff0000, v22\n"
"v_lshlrev_b32 v22, 16, v22\nMU 22,22,20\nMU 23,23,20\nCV 22,22,23\nAW 202,22\nAR 22,203\nv_and_b32 v23, 0xffff0000, v22\nv_lshlrev_b32 v22, 16, v22\nMU 22,22,20\nMU 23,23,20\n"
"CV 22,22,23\nAW 203,22\nAR 22,204\nv_and_b32 v23, 0xffff0000, v22\nv_lshlrev_b32 v22, 16, v22\nMU 22,22,21\nMU 23,23,21\nCV 22,22,23\nAW 204,22\nAR 22,205\n"
"v_and_b32 v23, 0xffff0000, v22\nv_lshlrev_b32 v22, 16, v22\nMU 22,22,21\nMU 23,23,21\nCV 22,22,23\nAW 205,22\nAR 22,206\nv_and_b32 v23, 0xffff0000, v22\n"
"v_lshlrev_b32 v22, 16, v22\nMU 22,22,21\nMU 23,23,21\nCV 22,22,23\nAW 206,22\nAR 22,207\nv_and_b32 v23, 0xffff0000, v22\nv_lshlrev_b32 v22, 16, v22\nMU 22,22,21\nMU 23,23,21\n"
"CV 22,22,23\nAW 207,22\nAR 22,208\nv_and_b32 v23, 0xffff0000, v22\nv_lshlrev_b32 v22, 16, v22\nMU 22,22,20\nMU 23,23,20\nCV 22,22,23\nAW 208,22\nAR 22,209\n"
"v_and_b32 v23, 0xffff0000, v22\nv_lshlrev_b32 v22, 16, v22\nMU 22,22,20\nMU 23,23,20\nCV 22,22,23\nAW 209,22\nAR 22,210\nv_and_b32 v23, 0xffff0000, v22\n"
"v_lshlrev_b32 v22, 16, v22\nMU 22,22,20\nMU 23,23,20\nCV 22,22,23\nAW 210,22\nAR 22,211\nv_and_b32 v23, 0xffff0000, v22\nv_lshlrev_b32 v22, 16, v22\nMU 22,22,20\nMU 23,23,20\n"
"CV 22,22,23\nAW 211,22\nAR 22,212\nv_and_b32 v23, 0xffff0000, v22\nv_lshlrev_b32 v22, 16, v22\nMU 22,22,21\nMU 23,23,21\nCV 22,22,23\nAW 212,22\nAR 22,213\n"
"v_and_b32 v23, 0xffff0000, v22\nv_lshlrev_b32 v22, 16, v22\nMU 22,22,21\nMU 23,23,21\nCV 22,22,23\nAW 213,22\nAR 22,214\nv_and_b32 v23, 0xffff0000, v22\n"
"v_lshlrev_b32 v22, 16, v22\nMU 22,22,21\nMU 23,23,21\nCV 22,22,23\nAW 214,22\nAR 22,215\nv_and_b32 v23, 0xffff0000, v22\nv_lshlrev_b32 v22, 16, v22\nMU 22,22,21\nMU 23,23,21\n"
"CV 22,22,23\nAW 215,22\nAR 22,216\nv_and_b32 v23, 0xffff0000, v22\nv_lshlrev_b32 v22, 16, v22\nMU 22,22,20\nMU 23,23,20\nCV 22,22,23\nAW 216,22\nAR 22,217\n"
"v_and_b32 v23, 0xffff0000, v22\nv_lshlrev_b32 v22, 16, v22\nMU 22,22,20\nMU 23,23,20\nCV 22,22,23\nAW 217,22\nAR 22,218\nv_and_b32 v23, 0xffff0000, v22\n"
"v_lshlrev_b32 v22, 16, v22\nMU 22,22,20\nMU 23,23,20\nCV 22,22,23\nAW 218,22\nAR 22,219\nv_and_b32 v23, 0xffff0000, v22\nv_lshlrev_b32 v22, 16, v22\nMU 22,22,20\nMU 23,23,20\n"
"CV 22,22,23\nAW 219,22\nAR 22,220\nv_and_b32 v23, 0xffff0000, v22\nv_lshlrev_b32 v22, 16, v22\nMU 22,22,21\nMU 23,23,21\nCV 22,22,23\nAW 220,22\nAR 22,221\n"
"v_and_b32 v23, 0xffff0000, v22\nv_lshlrev_b32 v22, 16, v22\nMU 22,22,21\nMU 23,23,21\nCV 22,22,23\nAW 221,22\nAR 22,222\nv_and_b32 v23, 0xffff0000, v22\n"
"v_lshlrev_b32 v22, 16, v22\nMU 22,22,21\nMU 23,23,21\nCV 22,22,23\nAW 222,22\nAR 22,223\nv_and_b32 v23, 0xffff0000, v22\nv_lshlrev_b32 v22, 16, v22\nMU 22,22,21\nMU 23,23,21\n"
"CV 22,22,23\nAW 223,22\nAR 22,224\nv_and_b32 v23, 0xffff0000, v22\nv_lshlrev_b32 v22, 16, v22\nMU 22,22,20\nMU 23,23,20\nCV 22,22,23\nAW 224,22\nAR 22,225\n"
"v_and_b32 v23, 0xffff0000, v22\nv_lshlrev_b32 v22, 16, v22\nMU 22,22,20\nMU 23,23,20\nCV 22,22,23\nAW 225,22\nAR 22,226\nv_and_b32 v23, 0xffff0000, v22\n"
"v_lshlrev_b32 v22, 16, v22\nMU 22,22,20\nMU 23,23,20\nCV 22,22,23\nAW 226,22\nAR 22,227\nv_and_b32 v23, 0xffff0000, v22\nv_lshlrev_b32 v22, 16, v22\nMU 22,22,20\nMU 23,23,20\n"
"CV 22,22,23\nAW 227,22\nAR 22,228\nv_and_b32 v23, 0xffff0000, v22\nv_lshlrev_b32 v22, 16, v22\nMU 22,22,21\nMU 23,23,21\nCV 22,22,23\nAW 228,22\nAR 22,229\n"
"v_and_b32 v23, 0xffff0000, v22\nv_lshlrev_b32 v22, 16, v22\nMU 22,22,21\nMU 23,23,21\nCV 22,22,23\nAW 229,22\nAR 22,230\nv_and_b32 v23, 0xffff0000, v22\n"
"v_lshlrev_b32 v22, 16, v22\nMU 22,22,21\nMU 23,23,21\nCV 22,22,23\nAW 230,22\nAR 22,231\nv_and_b32 v23, 0xffff0000, v22\nv_lshlrev_b32 v22, 16, v22\nMU 22,22,21\nMU 23,23,21\n"
"CV 22,22,23\nAW 231,22\nAR 22,232\nv_and_b32 v23, 0xffff0000, v22\nv_lshlrev_b32 v22, 16, v22\nMU 22,22,20\nMU 23,23,20\nCV 22,22,23\nAW 232,22\nAR 22,233\n"
"v_and_b32 v23, 0xffff0000, v22\nv_lshlrev_b32 v22, 16, v22\nMU 22,22,20\nMU 23,23,20\nCV 22,22,23\nAW 233,22\nAR 22,234\nv_and_b32 v23, 0xffff0000, v22\n"
"v_lshlrev_b32 v22, 16, v22\nMU 22,22,20\nMU 23,23,20\nCV 22,22,23\nAW 234,22\nAR 22,235\nv_and_b32 v23, 0xffff0000, v22\nv_lshlrev_b32 v22, 16, v22\nMU 22,22,20\nMU 23,23,20\n"
"CV 22,22,23\nAW 235,22\nAR 22,236\nv_and_b32 v23, 0xffff0000, v22\nv_lshlrev_b32 v22, 16, v22\nMU 22,22,21\nMU 23,23,21\nCV 22,22,23\nAW 236,22\nAR 22,237\n"
"v_and_b32 v23, 0xffff0000, v22\nv_lshlrev_b32 v22, 16, v22\nMU 22,22,21\nMU 23,23,21\nCV 22,22,23\nAW 237,22\nAR 22,238\nv_and_b32 v23, 0xffff0000, v22\n"
"v_lshlrev_b32 v22, 16, v22\nMU 22,22,21\nMU 23,23,21\nCV 22,22,23\nAW 238,22\nAR 22,239\nv_and_b32 v23, 0xffff0000, v22\nv_lshlrev_b32 v22, 16, v22\nMU 22,22,21\nMU 23,23,21\n"
"CV 22,22,23\nAW 239,22\nAR 22,240\nv_and_b32 v23, 0xffff0000, v22\nv_lshlrev_b32 v22, 16, v22\nMU 22,22,20\nMU 23,23,20\nCV 22,22,23\nAW 240,22\nAR 22,241\n"
"v_and_b32 v23, 0xffff0000, v22\nv_lshlrev_b32 v22, 16, v22\nMU 22,22,20\nMU 23,23,20\nCV 22,22,23\nAW 241,22\nAR 22,242\nv_and_b32 v23, 0xffff0000, v22\n"
"v_lshlrev_b32 v22, 16, v22\nMU 22,22,20\nMU 23,23,20\nCV 22,22,23\nAW 242,22\nAR 22,243\nv_and_b32 v23, 0xffff0000, v22\nv_lshlrev_b32 v22, 16, v22\nMU 22,22,20\nMU 23,23,20\n"
"CV 22,22,23\nAW 243,22\nAR 22,244\nv_and_b32 v23, 0xffff0000, v22\nv_lshlrev_b32 v22, 16, v22\nMU 22,22,21\nMU 23,23,21\nCV 22,22,23\nAW 244,22\nAR 22,245\n"
"v_and_b32 v23, 0xffff0000, v22\nv_lshlrev_b32 v22, 16, v22\nMU 22,22,21\nMU 23,23,21\nCV 22,22,23\nAW 245,22\nAR 22,246\nv_and_b32 v23, 0xffff0000, v22\n"
"v_lshlrev_b32 v22, 16, v22\nMU 22,22,21\nMU 23,23,21\nCV 22,22,23\nAW 246,22\nAR 22,247\nv_and_b32 v23, 0xffff0000, v22\nv_lshlrev_b32 v22, 16, v22\nMU 22,22,21\nMU 23,23,21\n"
"CV 22,22,23\nAW 247,22\nAR 22,248\nv_and_b32 v23, 0xffff0000, v22\nv_lshlrev_b32 v22, 16, v22\nMU 22,22,20\nMU 23,23,20\nCV 22,22,23\nAW 248,22\nAR 22,249\n"
"v_and_b32 v23, 0xffff0000, v22\nv_lshlrev_b32 v22, 16, v22\nMU 22,22,20\nMU 23,23,20\nCV 22,22,23\nAW 249,22\nAR 22,250\nv_and_b32 v23, 0xffff0000, v22\n"
"v_lshlrev_b32 v22, 16, v22\nMU 22,22,20\nMU 23,23,20\nCV 22,22,23\nAW 250,22\nAR 22,251\nv_and_b32 v23, 0xffff0000, v22\nv_lshlrev_b32 v22, 16, v22\nMU 22,22,20\nMU 23,23,20\n"
"CV 22,22,23\nAW 251,22\nAR 22,252\nv_and_b32 v23, 0xffff0000, v22\nv_lshlrev_b32 v22, 16, v22\nMU 22,22,21\nMU 23,23,21\nCV 22,22,23\nAW 252,22\nAR 22,253\n"
"v_and_b32 v23, 0xffff0000, v22\nv_lshlrev_b32 v22, 16, v22\nMU 22,22,21\nMU 23,23,21\nCV 22,22,23\nAW 253,22\nAR 22,254\nv_and_b32 v23, 0xffff0000, v22\n"
"v_lshlrev_b32 v22, 16, v22\nMU 22,22,21\nMU 23,23,21\nCV 22,22,23\nAW 254,22\nAR 22,255\nv_and_b32 v23, 0xffff0000, v22\nv_lshlrev_b32 v22, 16, v22\nMU 22,22,21\nMU 23,23,21\n"
"CV 22,22,23\nAW 255,22\nv_lshrrev_b32 v22, 8, v1\nv_bfe_u32 v23, v1, 6, 2\nv_cmp_eq_u32 vcc, v22, v23\nv_and_b32 v22, 16, v1\nv_mov_b32 v23, 0x3f80\n"
"v_cndmask_b32 v23, 0, v23, vcc\nv_lshlrev_b32 v23, v22, v23\nv_and_b32 v22, 32, v1\nv_cmp_eq_u32 vcc, 0, v22\nv_mov_b32 v128, 0\nv_mov_b32 v129, 0\n"
"v_cndmask_b32 v130, 0, v23, vcc\nv_sub_u32 v131, v23, v130\nv_mov_b32 v132, 0\nv_mov_b32 v133, 0\ns_nop 1\nMava 0,130,192\nMava 8,128,192\nMava 12,128,196\nMava 4,130,196\n"
"Mava 16,130,200\nMava 24,128,200\nMava 28,128,204\nMava 20,130,204\nMava 32,130,208\nMava 40,128,208\nMava 44,128,212\nMava 36,130,212\nMava 48,130,216\nMava 56,128,216\n"
"Mava 60,128,220\nMava 52,130,220\nMava 64,130,224\nMava 72,128,224\nMava 76,128,228\nMava 68,130,228\nMava 80,130,232\nMava 88,128,232\nMava 92,128,236\nMava 84,130,236\n"
"Mava 96,130,240\nMava 104,128,240\nMava 108,128,244\nMava 100,130,244\nMava 112,130,248\nMava 120,128,248\nMava 124,128,252\nMava 116,130,252\ns_nop 7\ns_branch .T197\n.S199:\n"
"s_mov_b32 s7, 262144\n.S201:\ns_add_u32 s4, s92, 0\nbuffer_load_dwordx4 a[192:195], v1, s[16:19], s4 offen offset:0 sc0 sc1\n"
"buffer_load_dwordx4 a[196:199], v1, s[16:19], s4 offen offset:1024 sc0 sc1\ns_add_u32 s4, s92, 131072\nbuffer_load_dwordx4 a[200:203], v1, s[16:19], s4 offen offset:0 sc0 sc1\n"
"buffer_load_dwordx4 a[204:207], v1, s[16:19], s4 offen offset:1024 sc0 sc1\ns_add_u32 s4, s92, 262144\nbuffer_load_dwordx4 a[208:211], v1, s[16:19], s4 offen offset:0 sc0 sc1\n"
"buffer_load_dwordx4 a[212:215], v1, s[16:19], s4 offen offset:1024 sc0 sc1\ns_add_u32 s4, s92, 393216\nbuffer_load_dwordx4 a[216:219], v1, s[16:19], s4 offen offset:0 sc0 sc1\n"
"buffer_load_dwordx4 a[220:223], v1, s[16:19], s4 offen offset:1024 sc0 sc1\nWV 0\nAR 22,195\nAR 23,199\nv_max_u32 v21, v22, v23\nAR 22,203\nAR 23,207\n"
"v_max3_u32 v21, v21, v22, v23\nAR 22,211\nAR 23,215\nv_max3_u32 v21, v21, v22, v23\nAR 22,219\nAR 23,223\nv_max3_u32 v21, v21, v22, v23\nv_cmp_eq_u32 vcc, -1, v21\ns_nop 0\n"
"s_cbranch_vccz .S200\ns_sleep 6\ns_sub_u32 s7, s7, 1\ns_cbranch_scc0 .S201\ns_branch .S200\n.S202:\ns_mov_b32 s7, 262144\n.S204:\ns_add_u32 s4, s92, 524288\n"
"buffer_load_dwordx4 a[224:227], v1, s[16:19], s4 offen offset:0 sc0 sc1\nbuffer_load_dwordx4 a[228:231], v1, s[16:19], s4 offen offset:1024 sc0 sc1\ns_add_u32 s4, s92, 655360\n"
"buffer_load_dwordx4 a[232:235], v1, s[16:19], s4 offen offset:0 sc0 sc1\nbuffer_load_dwordx4 a[236:239], v1, s[16:19], s4 offen offset:1024 sc0 sc1\ns_add_u32 s4, s92, 786432\n"
"buffer_load_dwordx4 a[240:243], v1, s[16:19], s4 offen offset:0 sc0 sc1\nbuffer_load_dwordx4 a[244:247], v1, s[16:19], s4 offen offset:1024 sc0 sc1\ns_add_u32 s4, s92, 917504\n"
"buffer_load_dwordx4 a[248:251], v1, s[16:19], s4 offen offset:0 sc0 sc1\nbuffer_load_dwordx4 a[252:255], v1, s[16:19], s4 offen offset:1024 sc0 sc1\nWV 0\nAR 22,227\nAR 23,231\n"
"v_max_u32 v21, v22, v23\nAR 22,235\nAR 23,239\nv_max3_u32 v21, v21, v22, v23\nAR 22,243\nAR 23,247\nv_max3_u32 v21, v21, v22, v23\nAR 22,251\nAR 23,255\n"
"v_max3_u32 v21, v21, v22, v23\nv_cmp_eq_u32 vcc, -1, v21\ns_nop 0\ns_cbranch_vccz .S203\ns_sleep 6\ns_sub_u32 s7, s7, 1\ns_cbranch_scc0 .S204\ns_branch .S203\n.S205:\n"
"s_mov_b32 s7, 262144\n.S207:\nbuffer_load_dwordx4 v[64:67], v89, s[16:19], s40 offen offset:0 sc0 sc1\nbuffer_load_dwordx4 v[68:71], v89, s[16:19], s40 offen offset:16 sc0 sc1\n"
"buffer_load_dwordx4 v[72:75], v89, s[16:19], s40 offen offset:1792 sc0 sc1\nbuffer_load_dwordx4 v[76:79], v89, s[16:19], s40 offen offset:1808 sc0 sc1\nWV 0\n"
"v_xor_b32 v92, s15, v66\nv_xor_b32 v93, s15, v70\nv_xor_b32 v94, s15, v74\nv_or3_b32 v92, v92, v93, v94\nv_xor_b32 v93, s15, v78\nv_or_b32 v92, v92, v93\nv_cmp_ne_u32 vcc, 0, v92\n"
"s_nop 0\ns_cbranch_vccz .S206\ns_sleep 6\ns_sub_u32 s7, s7, 1\ns_cbranch_scc0 .S207\ns_branch .S206\n.S208:\ns_mov_b32 s7, 262144\n.S210:\n"
"buffer_load_dword v27, off, s[16:19], s60 offset:512 sc0 sc1\nWV 0\nv_cmp_ne_u32 vcc, s15, v27\ns_nop 0\ns_cbranch_vccz .S209\ns_sleep 6\ns_sub_u32 s7, s7, 1\n"
"s_cbranch_scc0 .S210\ns_branch .S209\n.S211:\ns_mov_b32 s7, 262144\n.S213:\nbuffer_load_dword v27, v24, s[16:19], s41 offen sc0 sc1\nWV 0\nv_cmp_eq_u32 s[62:63], s15, v27\n"
"s_mov_b32 vcc_hi, 0\ns_nop 2\ns_lshr_b32 s63, s62, 8\ns_or_b32 s62, s62, s63\ns_andn2_b32 vcc_lo, 0xff, s62\ns_mov_b64 vcc, vcc\ns_cbranch_vccz .S212\ns_sleep 6\n"
"s_sub_u32 s7, s7, 1\ns_cbranch_scc0 .S213\ns_branch .S212\n.Z215:\ns_sub_u32 s7, s7, 1\ns_cbranch_scc1 .Z216\ns_cmp_lg_u64 s[44:45], 0\ns_cbranch_scc0 .Z218\n"
"buffer_load_dwordx4 v[96:99], v1, s[16:19], s24 offen offset:0 sc0 sc1\nbuffer_load_dwordx4 v[100:103], v1, s[16:19], s24 offen offset:1024 sc0 sc1\n.Z218:\n"
"s_cmp_lg_u64 s[46:47], 0\ns_cbranch_scc0 .Z219\nbuffer_load_dwordx4 v[104:107], v1, s[16:19], s25 offen offset:0 sc0 sc1\n"
"buffer_load_dwordx4 v[108:111], v1, s[16:19], s25 offen offset:1024 sc0 sc1\n.Z219:\ns_cmp_lg_u64 s[48:49], 0\ns_cbranch_scc0 .Z220\n"
"buffer_load_dwordx4 v[112:115], v1, s[16:19], s26 offen offset:0 sc0 sc1\nbuffer_load_dwordx4 v[116:119], v1, s[16:19], s26 offen offset:1024 sc0 sc1\n.Z220:\n"
"s_cmp_lg_u64 s[50:51], 0\ns_cbranch_scc0 .Z221\nbuffer_load_dwordx4 v[120:123], v1, s[16:19], s27 offen offset:0 sc0 sc1\n"
"buffer_load_dwordx4 v[124:127], v1, s[16:19], s27 offen offset:1024 sc0 sc1\n.Z221:\ns_cmp_lg_u64 s[52:53], 0\ns_cbranch_scc0 .Z222\n"
"buffer_load_dwordx4 v[32:35], v1, s[16:19], s32 offen offset:0 sc0 sc1\nbuffer_load_dwordx4 v[36:39], v1, s[16:19], s32 offen offset:1024 sc0 sc1\n.Z222:\n"
"s_cmp_lg_u64 s[54:55], 0\ns_cbranch_scc0 .Z223\nbuffer_load_dwordx4 v[40:43], v1, s[16:19], s33 offen offset:0 sc0 sc1\n"
"buffer_load_dwordx4 v[44:47], v1, s[16:19], s33 offen offset:1024 sc0 sc1\n.Z223:\ns_cmp_lg_u64 s[56:57], 0\ns_cbranch_scc0 .Z224\n"
"buffer_load_dwordx4 v[48:51], v1, s[16:19], s34 offen offset:0 sc0 sc1\nbuffer_load_dwordx4 v[52:55], v1, s[16:19], s34 offen offset:1024 sc0 sc1\n.Z224:\n"
"s_cmp_lg_u64 s[58:59], 0\ns_cbranch_scc0 .Z225\nbuffer_load_dwordx4 v[56:59], v1, s[16:19], s35 offen offset:0 sc0 sc1\n"
"buffer_load_dwordx4 v[60:63], v1, s[16:19], s35 offen offset:1024 sc0 sc1\n.Z225:\ns_sleep 8\nWV 0\ns_branch .Z217\n.S226:\ns_mov_b32 s7, 262144\n.S228:\nv_and_b32 v27, 0x70, v1\n"
"buffer_load_dword v27, v27, s[16:19], s61 offen offset:12 sc0 sc1\nWV 0\nv_cmp_ne_u32 vcc, s15, v27\ns_nop 0\ns_cbranch_vccz .S227\ns_sleep 6\ns_sub_u32 s7, s7, 1\n"
"s_cbranch_scc0 .S228\ns_branch .S227\n.S229:\ns_mov_b32 s7, 262144\n.S231:\nv_and_b32 v27, 0x70, v1\nbuffer_load_dword v27, v27, s[16:19], s61 offen offset:8 sc0 sc1\nWV 0\n"
"v_cmp_ne_u32 vcc, s15, v27\ns_nop 0\ns_cbranch_vccz .S230\ns_sleep 6\ns_sub_u32 s7, s7, 1\ns_cbranch_scc0 .S231\ns_branch .S230\n.S232:\ns_mov_b32 s7, 262144\n.S234:\n"
"v_and_b32 v27, 0x1f0, v1\nbuffer_load_dword v27, v27, s[16:19], s58 offen offset:4 sc0 sc1\nWV 0\nv_cmp_ne_u32 vcc, s15, v27\ns_nop 0\ns_cbranch_vccz .S233\ns_sleep 6\n"
"s_sub_u32 s7, s7, 1\ns_cbranch_scc0 .S234\ns_branch .S233\n.S235:\ns_mov_b32 s7, 262144\n.S237:\nv_lshrrev_b32 v144, 9, v1\nv_mul_u32_u24 v144, 0x18000, v144\n"
"v_lshlrev_b32 v144, 8, v144\nv_and_b32 v145, 0x1f0, v1\nv_add_u32 v144, v144, v145\ns_mov_b32 s6, 0x15c0000\nbuffer_load_dword v136, v144, s[16:19], s6 offen offset:4 sc0 sc1\n"
"s_mov_b32 s6, 0x15c2000\nbuffer_load_dword v137, v144, s[16:19], s6 offen offset:4 sc0 sc1\ns_mov_b32 s6, 0x45c0000\n"
"buffer_load_dword v138, v144, s[16:19], s6 offen offset:4 sc0 sc1\ns_mov_b32 s6, 0x45c2000\nbuffer_load_dword v139, v144, s[16:19], s6 offen offset:4 sc0 sc1\n"
"s_mov_b32 s6, 0x75c0000\nbuffer_load_dword v140, v144, s[16:19], s6 offen offset:4 sc0 sc1\ns_mov_b32 s6, 0x75c2000\n"
"buffer_load_dword v141, v144, s[16:19], s6 offen offset:4 sc0 sc1\ns_mov_b32 s6, 0xa5c0000\nbuffer_load_dword v142, v144, s[16:19], s6 offen offset:4 sc0 sc1\n"
"s_mov_b32 s6, 0xa5c2000\nbuffer_load_dword v143, v144, s[16:19], s6 offen offset:4 sc0 sc1\nWV 0\nv_xor_b32 v136, s15, v136\nv_xor_b32 v137, s15, v137\nv_xor_b32 v138, s15, v138\n"
"v_xor_b32 v139, s15, v139\nv_xor_b32 v140, s15, v140\nv_xor_b32 v141, s15, v141\nv_xor_b32 v142, s15, v142\nv_xor_b32 v143, s15, v143\nv_min_u32 v136, v136, v137\n"
"v_min_u32 v138, v138, v139\nv_min_u32 v140, v140, v141\nv_min_u32 v142, v142, v143\nv_or3_b32 v136, v136, v138, v140\nv_or_b32 v136, v136, v142\nv_cmp_ne_u32 vcc, 0, v136\n"
"s_nop 0\ns_cbranch_vccz .S236\ns_sleep 6\ns_sub_u32 s7, s7, 1\ns_cbranch_scc0 .S237\ns_branch .S236\n.p2align 12\n.PA4:\ns_add_u32 m0, s47, 16384\ns_add_u32 s44, s44, 0x40000\n"
"D0 6,24,44,0\nD0 7,24,44,1024\nD0 8,24,44,2048\nD0 9,24,44,3072\ns_and_b32 s86, s74, 15\ns_lshl2_add_u32 s86, s86, s11\ns_mov_b32 s88, 0\ns_lshl_b32 s93, s73, 8\n"
"s_lshl2_add_u32 s93, s86, s93\ns_add_u32 s93, s93, 0xc04ac00\ns_lshl_b32 s86, s86, 14\ns_mul_i32 s91, s73, 25165824\ns_add_u32 s86, s86, s91\ns_add_u32 s94, s86, 0x1480000\n"
"s_add_u32 s95, s94, 0x2000\ns_add_u32 s86, s94, 512\ns_load_dword s87, s[54:55], s86\ns_add_u32 m0, s48, 32768\ns_add_u32 s45, s45, 128\nD0 2,20,45,0\nD0 3,20,45,1024\n"
"D0 4,20,45,2048\nD0 5,20,45,3072\ns_add_u32 m0, s48, 36864\ns_add_u32 s46, s45, 0x10000\nD0 2,20,46,0\nD0 3,20,46,1024\nD0 4,20,46,2048\nD0 5,20,46,3072\ns_add_u32 m0, s47, 32768\n"
"s_add_u32 s44, s44, 0x40000\nD0 6,24,44,0\nD0 7,24,44,1024\nD0 8,24,44,2048\nD0 9,24,44,3072\nv_mov_b32 v52, 0x40000000\nv_cmp_gt_u32 vcc, 56, v0\ns_lshl2_add_u32 s2, s10, s11\n"
"s_mul_i32 s2, s2, 1792\nv_cndmask_b32 v53, v52, v1, vcc\ns_mov_b32 s36, s56\ns_and_b32 s37, s57, 0xffff\ns_mov_b32 s38, 0x380\ns_mov_b32 s39, 0x20000\ns_mov_b32 s40, s58\n"
"s_and_b32 s41, s59, 0xffff\ns_mov_b32 s42, 0x380\ns_mov_b32 s43, 0x20000\nbuffer_load_dwordx4 v[32:35], v53, s[28:31], s2 offen offset:0\n"
"buffer_load_dwordx4 v[36:39], v53, s[28:31], s2 offen offset:896\nbuffer_load_dwordx4 v[40:43], v53, s[36:39], 0 offen offset:0\n"
"buffer_load_dwordx4 v[44:47], v53, s[40:43], 0 offen offset:0\nWL 0\ns_mul_i32 s15, s15, 1664525\ns_add_u32 s15, s15, 1013904223\nv_mov_b32 v25, s15\ns_mov_b32 s57, 0x3db8aa3b\n"
"s_and_b32 s59, s11, 1\ns_lshl_b32 s59, s59, 13\ns_mul_i32 s2, s73, 25165824\ns_and_b32 s3, s12, 1\ns_lshl_b32 s3, s3, 21\ns_add_u32 s2, s2, s3\ns_lshl2_add_u32 s3, s13, s11\n"
"s_lshl_b32 s3, s3, 15\ns_add_u32 s2, s2, s3\ns_add_u32 s56, s2, 0x800000\ns_xor_b32 s53, s56, 0xc000\nbuffer_load_dword v31, v24, s[24:27], s96 offen\ns_add_u32 s96, s96, 0x80000\n"
"v_lshrrev_b32 v60, 3, v0\nv_and_b32 v61, 0x70, v1\nv_lshl_or_b32 v60, v60, 10, v61\nSv 2,60,16,94,0\nSv 2,60,16,95,0\nWV 23\ns_barrier\nTa 128,12,0\nTa 130,12,2048\nTa 132,13,0\n"
"Ta 134,13,2048\nTa 136,14,0\nTa 138,14,2048\nTa 140,15,0\nTa 142,15,2048\nTa 144,16,0\nTa 146,16,2048\nTa 148,17,0\nTa 150,17,2048\nTa 152,18,0\nTa 154,18,2048\nTa 156,19,0\n"
"Ta 158,19,2048\nRa 160,10,0\nRa 164,10,2048\nRa 168,10,8192\nRa 172,10,10240\nRa 176,10,16384\nRa 180,10,18432\nRa 184,10,24576\nRa 188,10,26624\ns_waitcnt vmcnt(19) lgkmcnt(0)\n"
"s_barrier\nZvaa 64,128,160\nTa 192,12,16384\nZvaa 96,128,164\ns_mov_b32 s36, s60\nZvaa 128,128,168\nTa 194,12,18432\nZvaa 160,128,172\ns_add_u32 m0, s47, 49152\n"
"s_add_u32 s44, s44, 0x40000\nZaaa 0,128,176\nTa 196,13,16384\nZaaa 32,128,180\nD0 6,24,44,0\nZaaa 64,128,184\nTa 198,13,18432\nZaaa 96,128,188\ns_and_b32 s37, s61, 0xffff\n"
"Zaaa 100,132,188\nTa 200,14,16384\nZaaa 68,132,184\ns_mov_b32 s38, 0x380000\nZaaa 36,132,180\nTa 202,14,18432\nZaaa 4,132,176\ns_mov_b32 s39, 0x20000\nZvaa 164,132,172\n"
"Ta 204,15,16384\nZvaa 132,132,168\nD0 7,24,44,1024\nZvaa 100,132,164\nTa 206,15,18432\nZvaa 68,132,160\ns_lshl3_add_u32 s75, s12, s13\nZvaa 72,136,160\nTa 208,16,16384\n"
"Zvaa 104,136,164\ns_lshl2_add_u32 s75, s75, s11\nZvaa 136,136,168\nTa 210,16,18432\nZvaa 168,136,172\ns_mul_i32 s75, s75, 7168\nZaaa 8,136,176\nTa 212,17,16384\nZaaa 40,136,180\n"
"D0 8,24,44,2048\nZaaa 72,136,184\nTa 214,17,18432\nZaaa 104,136,188\ns_mul_i32 s76, s11, 7168\nZaaa 108,140,188\nTa 216,18,16384\nZaaa 76,140,184\ns_add_u32 s76, s76, 131072\n"
"Zaaa 44,140,180\nTa 218,18,18432\nZaaa 12,140,176\ns_mov_b32 s42, 1605632\nZvaa 172,140,172\nTa 220,19,16384\nZvaa 140,140,168\nD0 9,24,44,3072\nZvaa 108,140,164\nTa 222,19,18432\n"
"Zvaa 76,140,160\nbuffer_load_dword v31, v24, s[24:27], s96 offen\ns_add_u32 s96, s96, 0x80000\nZvaa 80,144,160\nRa 224,11,0\nZvaa 112,144,164\nZvaa 144,144,168\nRa 228,11,2048\n"
"Zvaa 176,144,172\ns_add_u32 m0, s47, 0\ns_add_u32 s44, s44, 0x40000\nZaaa 16,144,176\nRa 232,11,8192\nZaaa 48,144,180\nD0 6,24,44,0\nZaaa 80,144,184\nRa 236,11,10240\n"
"Zaaa 112,144,188\nZaaa 116,148,188\nRa 240,11,16384\nZaaa 84,148,184\ns_and_b32 s78, s74, 3\ns_lshl2_add_u32 s78, s73, s78\ns_lshl_b32 s78, s78, 12\ns_add_u32 s79, s45, 512\n"
"s_and_b32 s79, s79, 0xfffe0000\ns_add_u32 s78, s78, s79\nbuffer_load_dword v31, v26, s[20:23], s78 offen\nZaaa 52,148,180\nRa 244,11,18432\nZaaa 20,148,176\nZvaa 180,148,172\n"
"Ra 248,11,24576\nZvaa 148,148,168\nD0 7,24,44,1024\nZvaa 116,148,164\nRa 252,11,26624\nZvaa 84,148,160\nZvaa 88,152,160\nZvaa 120,152,164\nZvaa 152,152,168\nZvaa 184,152,172\n"
"Zaaa 24,152,176\nZaaa 56,152,180\nD0 8,24,44,2048\nZaaa 88,152,184\nZaaa 120,152,188\nZaaa 124,156,188\nZaaa 92,156,184\nZaaa 60,156,180\nZaaa 28,156,176\nZvaa 188,156,172\n"
"Zvaa 156,156,168\nD0 9,24,44,3072\nZvaa 124,156,164\nZvaa 92,156,160\ns_waitcnt vmcnt(17) lgkmcnt(0)\ns_barrier\nMvaa 64,192,224\nTa 128,12,32768\nWV 13\nMvaa 96,192,228\n"
"v_lshlrev_b32 v48, 16, v32\nMvaa 128,192,232\nTa 130,12,34816\nMvaa 160,192,236\ns_add_u32 m0, s48, 0\ns_add_u32 s45, s45, 128\nMaaa 0,192,240\nTa 132,13,32768\n"
"v_lshlrev_b32 v49, 16, v36\nMaaa 32,192,244\nD0 2,20,45,0\nMaaa 64,192,248\nTa 134,13,34816\nMaaa 96,192,252\nv_lshlrev_b32 v50, 16, v40\nv_lshlrev_b32 v51, 16, v44\n"
"Maaa 100,196,252\nTa 136,14,32768\nMaaa 68,196,248\nMU 52,48,50\nMaaa 36,196,244\nTa 138,14,34816\nMU 53,49,50\nMaaa 4,196,240\nCV 52,52,53\nMvaa 164,196,236\nTa 140,15,32768\n"
"Mvaa 132,196,232\nD0 3,20,45,1024\nv_lshlrev_b32 v54, 16, v52\nMvaa 100,196,228\nTa 142,15,34816\nMvaa 68,196,224\nv_and_b32 v55, 0xffff0000, v52\nMvaa 72,200,224\n"
"Ta 144,16,32768\nMU 56,48,51\nMvaa 104,200,228\nMU 57,49,51\nMvaa 136,200,232\nTa 146,16,34816\nMvaa 168,200,236\nv_and_b32 v48, 0xffff0000, v32\nMaaa 8,200,240\nTa 148,17,32768\n"
"v_and_b32 v49, 0xffff0000, v36\nMaaa 40,200,244\nD0 4,20,45,2048\nMaaa 72,200,248\nTa 150,17,34816\nMaaa 104,200,252\nv_and_b32 v50, 0xffff0000, v40\n"
"v_and_b32 v51, 0xffff0000, v44\nMaaa 108,204,252\nTa 152,18,32768\nMaaa 76,204,248\nMU 52,48,50\nMaaa 44,204,244\nTa 154,18,34816\nMU 53,49,50\nMaaa 12,204,240\nCV 52,52,53\n"
"Mvaa 172,204,236\nTa 156,19,32768\nMvaa 140,204,232\nD0 5,20,45,3072\nv_and_b32 v53, 0xffff0000, v52\nMvaa 108,204,228\nTa 158,19,34816\nMvaa 76,204,224\n"
"v_lshlrev_b32 v52, 16, v52\nMvaa 80,208,224\nRa 160,10,32768\nFM 56,48,51\nMvaa 112,208,228\nFM 57,49,51\nMvaa 144,208,232\nRa 164,10,34816\nMvaa 176,208,236\n"
"s_add_u32 m0, s48, 4096\ns_add_u32 s46, s45, 0x10000\nMaaa 16,208,240\nRa 168,10,40960\nAD 54,54,52\nMaaa 48,208,244\nD0 2,20,46,0\nMaaa 80,208,248\nRa 172,10,43008\n"
"Maaa 112,208,252\nAD 55,55,53\nv_lshlrev_b32 v48, 16, v33\nMaaa 116,212,252\nRa 176,10,49152\nMaaa 84,212,248\nv_lshlrev_b32 v49, 16, v37\nMaaa 52,212,244\nRa 180,10,51200\n"
"v_lshlrev_b32 v50, 16, v41\nMaaa 20,212,240\nv_lshlrev_b32 v51, 16, v45\nMvaa 180,212,236\nRa 184,10,57344\nMvaa 148,212,232\nD0 3,20,46,1024\nMU 52,48,50\nMvaa 116,212,228\n"
"Ra 188,10,59392\nMvaa 84,212,224\nMU 53,49,50\nMvaa 88,216,224\nCV 52,52,53\nv_and_b32 v53, 0xffff0000, v52\nMvaa 120,216,228\nv_lshlrev_b32 v52, 16, v52\nMvaa 152,216,232\n"
"FM 56,48,51\nMvaa 184,216,236\nFM 57,49,51\nMaaa 24,216,240\nAD 54,54,52\nAD 55,55,53\nMaaa 56,216,244\nD0 4,20,46,2048\nMaaa 88,216,248\nv_and_b32 v48, 0xffff0000, v33\n"
"Maaa 120,216,252\nv_and_b32 v49, 0xffff0000, v37\nv_and_b32 v50, 0xffff0000, v41\nMaaa 124,220,252\nv_and_b32 v51, 0xffff0000, v45\nMaaa 92,220,248\nMU 52,48,50\nMaaa 60,220,244\n"
"MU 53,49,50\nCV 52,52,53\nMaaa 28,220,240\nv_and_b32 v53, 0xffff0000, v52\nMvaa 188,220,236\nv_lshlrev_b32 v52, 16, v52\nMvaa 156,220,232\nD0 5,20,46,3072\nFM 56,48,51\n"
"Mvaa 124,220,228\nFM 57,49,51\nMvaa 92,220,224\ns_waitcnt vmcnt(14) lgkmcnt(0)\ns_barrier\nMvaa 64,128,160\nTa 192,12,49152\nAD 54,54,52\nMvaa 96,128,164\nAD 55,55,53\n"
"Mvaa 128,128,168\nTa 194,12,51200\nMvaa 160,128,172\ns_add_u32 m0, s47, 16384\ns_add_u32 s44, s44, 0x40000\nMaaa 0,128,176\nTa 196,13,49152\nv_lshlrev_b32 v48, 16, v34\n"
"Maaa 32,128,180\nD0 6,24,44,0\nMaaa 64,128,184\nTa 198,13,51200\nMaaa 96,128,188\nv_lshlrev_b32 v49, 16, v38\nv_lshlrev_b32 v50, 16, v42\nMaaa 100,132,188\nTa 200,14,49152\n"
"Maaa 68,132,184\nv_lshlrev_b32 v51, 16, v46\nMaaa 36,132,180\nTa 202,14,51200\nMU 52,48,50\nMaaa 4,132,176\nMU 53,49,50\nMvaa 164,132,172\nTa 204,15,49152\nMvaa 132,132,168\n"
"D0 7,24,44,1024\nCV 52,52,53\nMvaa 100,132,164\nTa 206,15,51200\nMvaa 68,132,160\nv_and_b32 v53, 0xffff0000, v52\nMvaa 72,136,160\nTa 208,16,49152\nv_lshlrev_b32 v52, 16, v52\n"
"Mvaa 104,136,164\nFM 56,48,51\nMvaa 136,136,168\nTa 210,16,51200\nMvaa 168,136,172\nFM 57,49,51\nMaaa 8,136,176\nTa 212,17,49152\nAD 54,54,52\nMaaa 40,136,180\nD0 8,24,44,2048\n"
"Maaa 72,136,184\nTa 214,17,51200\nMaaa 104,136,188\nAD 55,55,53\nv_and_b32 v48, 0xffff0000, v34\nMaaa 108,140,188\nTa 216,18,49152\nMaaa 76,140,184\n"
"v_and_b32 v49, 0xffff0000, v38\nMaaa 44,140,180\nTa 218,18,51200\nv_and_b32 v50, 0xffff0000, v42\nMaaa 12,140,176\nv_and_b32 v51, 0xffff0000, v46\nMvaa 172,140,172\n"
"Ta 220,19,49152\nMvaa 140,140,168\nD0 9,24,44,3072\nMU 52,48,50\nMvaa 108,140,164\nTa 222,19,51200\nMvaa 76,140,160\nbuffer_load_dword v31, v24, s[24:27], s96 offen\n"
"s_add_u32 s96, s96, 0x80000\nMvaa 80,144,160\nRa 224,11,32768\nMU 53,49,50\nMvaa 112,144,164\nCV 52,52,53\nMvaa 144,144,168\nRa 228,11,34816\nMvaa 176,144,172\n"
"s_add_u32 m0, s47, 32768\ns_add_u32 s44, s44, 0x40000\nMaaa 16,144,176\nRa 232,11,40960\nv_and_b32 v53, 0xffff0000, v52\nMaaa 48,144,180\nD0 6,24,44,0\nMaaa 80,144,184\n"
"Ra 236,11,43008\nMaaa 112,144,188\nv_lshlrev_b32 v52, 16, v52\nFM 56,48,51\nMaaa 116,148,188\nRa 240,11,49152\nMaaa 84,148,184\nFM 57,49,51\nMaaa 52,148,180\nRa 244,11,51200\n"
"AD 54,54,52\nMaaa 20,148,176\nAD 55,55,53\nMvaa 180,148,172\nRa 248,11,57344\nMvaa 148,148,168\nD0 7,24,44,1024\nv_lshlrev_b32 v48, 16, v35\nMvaa 116,148,164\nRa 252,11,59392\n"
"Mvaa 84,148,160\nv_lshlrev_b32 v49, 16, v39\nMvaa 88,152,160\nv_lshlrev_b32 v50, 16, v43\nv_lshlrev_b32 v51, 16, v47\nMvaa 120,152,164\nMU 52,48,50\nMvaa 152,152,168\nMU 53,49,50\n"
"Mvaa 184,152,172\nCV 52,52,53\nMaaa 24,152,176\nv_and_b32 v53, 0xffff0000, v52\nv_lshlrev_b32 v52, 16, v52\nMaaa 56,152,180\nD0 8,24,44,2048\nMaaa 88,152,184\nFM 56,48,51\n"
"Maaa 120,152,188\nFM 57,49,51\nAD 54,54,52\nMaaa 124,156,188\nAD 55,55,53\nMaaa 92,156,184\nv_and_b32 v48, 0xffff0000, v35\nMaaa 60,156,180\nv_and_b32 v49, 0xffff0000, v39\n"
"v_and_b32 v50, 0xffff0000, v43\nMaaa 28,156,176\nv_and_b32 v51, 0xffff0000, v47\nMvaa 188,156,172\nMU 52,48,50\nMvaa 156,156,168\nD0 9,24,44,3072\nMU 53,49,50\nMvaa 124,156,164\n"
"CV 52,52,53\nMvaa 92,156,160\ns_waitcnt vmcnt(9) lgkmcnt(0)\ns_barrier\nMvaa 64,192,224\nTa 128,12,0\nv_and_b32 v53, 0xffff0000, v52\nMvaa 96,192,228\nv_lshlrev_b32 v52, 16, v52\n"
"Mvaa 128,192,232\nTa 130,12,2048\nMvaa 160,192,236\ns_add_u32 m0, s48, 32768\ns_add_u32 s45, s45, 128\nMaaa 0,192,240\nTa 132,13,0\nFM 56,48,51\nMaaa 32,192,244\nD0 2,20,45,0\n"
"Maaa 64,192,248\nTa 134,13,2048\nMaaa 96,192,252\nFM 57,49,51\nAD 54,54,52\nMaaa 100,196,252\nTa 136,14,0\nMaaa 68,196,248\nAD 55,55,53\nMaaa 36,196,244\nTa 138,14,2048\n"
"v_permlane32_swap_b32 v54, v55\nMaaa 4,196,240\nv_permlane32_swap_b32 v56, v57\nMvaa 164,196,236\nTa 140,15,0\nMvaa 132,196,232\nD0 3,20,45,1024\nAD 54,54,55\nMvaa 100,196,228\n"
"Ta 142,15,2048\nMvaa 68,196,224\nAD 56,56,57\nMvaa 72,200,224\nTa 144,16,0\nMvaa 104,200,228\nv_permlane16_swap_b32 v54, v56\nMvaa 136,200,232\nTa 146,16,2048\nMvaa 168,200,236\n"
"AD 54,54,56\nMaaa 8,200,240\nTa 148,17,0\nMaaa 40,200,244\nD0 4,20,45,2048\nMaaa 72,200,248\nTa 150,17,2048\nMaaa 104,200,252\n"
"v_add_f32_dpp v54, v54, v54 row_ror:8 row_mask:0xf bank_mask:0xf\nMaaa 108,204,252\nTa 152,18,0\nMaaa 76,204,248\nv_add_f32_dpp v54, v54, v54 row_ror:4 row_mask:0xf bank_mask:0xf\n"
"Maaa 44,204,244\nTa 154,18,2048\nMaaa 12,204,240\nv_add_f32_dpp v54, v54, v54 row_ror:2 row_mask:0xf bank_mask:0xf\nMvaa 172,204,236\nTa 156,19,0\nMvaa 140,204,232\n"
"D0 5,20,45,3072\nv_add_f32_dpp v54, v54, v54 row_ror:1 row_mask:0xf bank_mask:0xf\nMvaa 108,204,228\nTa 158,19,2048\nMvaa 76,204,224\nCV 54,54,54\nMvaa 80,208,224\nRa 160,10,0\n"
"v_lshlrev_b32 v54, 16, v54\nMvaa 112,208,228\nv_mul_f32 v55, 0xbfb8aa3b, v54\nEX 55,55\nMvaa 144,208,232\nRa 164,10,2048\nMvaa 176,208,236\ns_add_u32 m0, s48, 36864\n"
"s_add_u32 s46, s45, 0x10000\nMaaa 16,208,240\nRa 168,10,8192\nv_add_f32 v56, 0, v54\nMaaa 48,208,244\nD0 2,20,46,0\nMaaa 80,208,248\nRa 172,10,10240\nMaaa 112,208,252\n"
"v_add_f32 v55, 1.0, v55\nMaaa 116,212,252\nRa 176,10,16384\nv_rcp_f32 v55, v55\nMaaa 84,212,248\nv_ashrrev_i32 v57, 31, v56\nMaaa 52,212,244\nRa 180,10,18432\nCV 55,55,55\n"
"Maaa 20,212,240\nv_lshlrev_b32 v55, 16, v55\nMvaa 180,212,236\nRa 184,10,24576\nMvaa 148,212,232\nD0 3,20,46,1024\nv_or_b32 v57, 0x80000000, v57\nMvaa 116,212,228\n"
"Ra 188,10,26624\nMvaa 84,212,224\nv_xor_b32 v56, v56, v57\nMvaa 88,216,224\nv_and_b32 v56, 0xffff0000, v56\ns_lshl2_add_u32 s2, s10, s11\nMvaa 120,216,228\n"
"v_lshrrev_b32 v48, 5, v0\nMvaa 152,216,232\nv_lshl_add_u32 v48, s2, 1, v48\nMvaa 184,216,236\nv_sub_u32 v49, 0xffff, v48\nMaaa 24,216,240\nv_or_b32 v56, v56, v49\n"
"v_mov_b32 v57, s15\nMaaa 56,216,244\nD0 4,20,46,2048\nMaaa 88,216,248\nv_and_b32 v49, 31, v0\nMaaa 120,216,252\nv_mov_b32 v52, 0x40000000\nv_cmp_eq_u32 vcc, 16, v49\n"
"v_lshlrev_b32 v50, 3, v48\nv_lshlrev_b32 v51, 9, v49\nv_cndmask_b32 v50, v52, v50, vcc\nv_cndmask_b32 v55, v55, v54, vcc\nMaaa 124,220,252\nv_lshl_add_u32 v51, v48, 2, v51\n"
"Maaa 92,220,248\nv_and_b32 v49, 15, v0\nv_cmp_eq_u32 vcc, 0, v49\ns_mov_b32 s2, 0xc040000\ns_mov_b32 s3, 0xc044000\nv_cndmask_b32 v51, v52, v51, vcc\n"
"buffer_store_dwordx2 v[56:57], v50, s[16:19], s3 offen sc0 sc1\nbuffer_store_dword v55, v51, s[16:19], s2 offen sc0 sc1\nMaaa 60,220,244\nMaaa 28,220,240\nMvaa 188,220,236\n"
"Mvaa 156,220,232\nD0 5,20,46,3072\nMvaa 124,220,228\nMvaa 92,220,224\ns_waitcnt vmcnt(15) lgkmcnt(0)\ns_barrier\ns_mov_b32 s50, 3\n.GL245:\nMvaa 64,128,160\nTa 192,12,16384\n"
"Mvaa 96,128,164\ns_mov_b32 s86, 0x1d2c3b4a\ns_load_dword s86, s[54:55], s94\nMvaa 128,128,168\nTa 194,12,18432\ns_add_u32 s94, s94, 1024\nMvaa 160,128,172\n"
"s_add_u32 m0, s47, 49152\ns_add_u32 s44, s44, 0x40000\nMaaa 0,128,176\nTa 196,13,16384\nMaaa 32,128,180\nD0 6,24,44,0\nMaaa 64,128,184\nTa 198,13,18432\nMaaa 96,128,188\n"
"s_cmp_eq_u32 s86, 0x1d2c3b4a\ns_addc_u32 s88, s88, 0\nMaaa 100,132,188\nTa 200,14,16384\nMaaa 68,132,184\nMaaa 36,132,180\nTa 202,14,18432\nMaaa 4,132,176\n"
"s_cmp_eq_u32 s86, 0x1d2c3b4a\ns_addc_u32 s88, s88, 0\nMvaa 164,132,172\nTa 204,15,16384\nMvaa 132,132,168\nD0 7,24,44,1024\nMvaa 100,132,164\nTa 206,15,18432\nMvaa 68,132,160\n"
"s_cmp_eq_u32 s86, 0x1d2c3b4a\ns_addc_u32 s88, s88, 0\nMvaa 72,136,160\nTa 208,16,16384\nMvaa 104,136,164\nMvaa 136,136,168\nTa 210,16,18432\nMvaa 168,136,172\n"
"s_cmp_eq_u32 s86, 0x1d2c3b4a\ns_addc_u32 s88, s88, 0\nMaaa 8,136,176\nTa 212,17,16384\nMaaa 40,136,180\nD0 8,24,44,2048\nMaaa 72,136,184\nTa 214,17,18432\nMaaa 104,136,188\n"
"s_cmp_eq_u32 s86, 0x1d2c3b4a\ns_addc_u32 s88, s88, 0\nMaaa 108,140,188\nTa 216,18,16384\nMaaa 76,140,184\nMaaa 44,140,180\nTa 218,18,18432\nMaaa 12,140,176\n"
"s_cmp_eq_u32 s86, 0x1d2c3b4a\ns_addc_u32 s88, s88, 0\nMvaa 172,140,172\nTa 220,19,16384\nMvaa 140,140,168\nD0 9,24,44,3072\nMvaa 108,140,164\nTa 222,19,18432\nMvaa 76,140,160\n"
"buffer_load_dword v31, v24, s[24:27], s96 offen\ns_add_u32 s96, s96, 0x80000\ns_cmp_eq_u32 s86, 0x1d2c3b4a\ns_addc_u32 s88, s88, 0\nMvaa 80,144,160\nRa 224,11,0\n"
"s_mov_b32 m0, s76\ns_add_u32 s76, s76, 1024\nMvaa 112,144,164\nD0 1,36,75,0\ns_add_u32 s75, s75, 1024\nMvaa 144,144,168\nRa 228,11,2048\nMvaa 176,144,172\ns_add_u32 m0, s47, 0\n"
"s_add_u32 s44, s44, 0x40000\ns_cmp_eq_u32 s86, 0x1d2c3b4a\ns_addc_u32 s88, s88, 0\nMaaa 16,144,176\nRa 232,11,8192\nMaaa 48,144,180\nD0 6,24,44,0\nMaaa 80,144,184\n"
"Ra 236,11,10240\nMaaa 112,144,188\ns_cmp_eq_u32 s86, 0x1d2c3b4a\ns_addc_u32 s88, s88, 0\nMaaa 116,148,188\nRa 240,11,16384\nMaaa 84,148,184\nMaaa 52,148,180\nRa 244,11,18432\n"
"Maaa 20,148,176\ns_cmp_eq_u32 s86, 0x1d2c3b4a\ns_addc_u32 s88, s88, 0\nMvaa 180,148,172\nRa 248,11,24576\nMvaa 148,148,168\nD0 7,24,44,1024\nMvaa 116,148,164\nRa 252,11,26624\n"
"Mvaa 84,148,160\ns_cmp_eq_u32 s86, 0x1d2c3b4a\ns_addc_u32 s88, s88, 0\nMvaa 88,152,160\nMvaa 120,152,164\nMvaa 152,152,168\nMvaa 184,152,172\ns_cmp_eq_u32 s86, 0x1d2c3b4a\n"
"s_addc_u32 s88, s88, 0\nMaaa 24,152,176\nMaaa 56,152,180\nD0 8,24,44,2048\nMaaa 88,152,184\nMaaa 120,152,188\ns_cmp_eq_u32 s86, 0x1d2c3b4a\ns_addc_u32 s88, s88, 0\n"
"Maaa 124,156,188\nMaaa 92,156,184\nMaaa 60,156,180\nMaaa 28,156,176\ns_cmp_eq_u32 s86, 0x1d2c3b4a\ns_addc_u32 s88, s88, 0\nMvaa 188,156,172\nMvaa 156,156,168\nD0 9,24,44,3072\n"
"Mvaa 124,156,164\nMvaa 92,156,160\ns_waitcnt vmcnt(10) lgkmcnt(0)\ns_barrier\nMvaa 64,192,224\nTa 128,12,32768\nMvaa 96,192,228\ns_mov_b32 s86, 0x1d2c3b4a\n"
"s_load_dword s86, s[54:55], s95\nMvaa 128,192,232\nTa 130,12,34816\ns_add_u32 s95, s95, 1024\nMvaa 160,192,236\ns_add_u32 m0, s48, 0\ns_add_u32 s45, s45, 128\nMaaa 0,192,240\n"
"Ta 132,13,32768\nMaaa 32,192,244\nD0 2,20,45,0\nMaaa 64,192,248\nTa 134,13,34816\nMaaa 96,192,252\ns_cmp_eq_u32 s86, 0x1d2c3b4a\ns_subb_u32 s88, s88, 0\nMaaa 100,196,252\n"
"Ta 136,14,32768\nMaaa 68,196,248\nMaaa 36,196,244\nTa 138,14,34816\nMaaa 4,196,240\ns_cmp_eq_u32 s86, 0x1d2c3b4a\ns_subb_u32 s88, s88, 0\nMvaa 164,196,236\nTa 140,15,32768\n"
"Mvaa 132,196,232\nD0 3,20,45,1024\nMvaa 100,196,228\nTa 142,15,34816\nMvaa 68,196,224\ns_cmp_eq_u32 s86, 0x1d2c3b4a\ns_subb_u32 s88, s88, 0\nMvaa 72,200,224\nTa 144,16,32768\n"
"Mvaa 104,200,228\nMvaa 136,200,232\nTa 146,16,34816\nMvaa 168,200,236\ns_cmp_eq_u32 s86, 0x1d2c3b4a\ns_subb_u32 s88, s88, 0\nMaaa 8,200,240\nTa 148,17,32768\nMaaa 40,200,244\n"
"D0 4,20,45,2048\nMaaa 72,200,248\nTa 150,17,34816\nMaaa 104,200,252\ns_cmp_eq_u32 s86, 0x1d2c3b4a\ns_subb_u32 s88, s88, 0\nMaaa 108,204,252\nTa 152,18,32768\nMaaa 76,204,248\n"
"Maaa 44,204,244\nTa 154,18,34816\nMaaa 12,204,240\ns_cmp_eq_u32 s86, 0x1d2c3b4a\ns_subb_u32 s88, s88, 0\nMvaa 172,204,236\nTa 156,19,32768\nMvaa 140,204,232\nD0 5,20,45,3072\n"
"Mvaa 108,204,228\nTa 158,19,34816\nMvaa 76,204,224\ns_cmp_eq_u32 s86, 0x1d2c3b4a\ns_subb_u32 s88, s88, 0\nMvaa 80,208,224\nRa 160,10,32768\nMvaa 112,208,228\nMvaa 144,208,232\n"
"Ra 164,10,34816\nMvaa 176,208,236\ns_add_u32 m0, s48, 4096\ns_add_u32 s46, s45, 0x10000\ns_cmp_eq_u32 s86, 0x1d2c3b4a\ns_subb_u32 s88, s88, 0\nMaaa 16,208,240\nRa 168,10,40960\n"
"Maaa 48,208,244\nD0 2,20,46,0\nMaaa 80,208,248\nRa 172,10,43008\nMaaa 112,208,252\ns_cmp_eq_u32 s86, 0x1d2c3b4a\ns_subb_u32 s88, s88, 0\nMaaa 116,212,252\nRa 176,10,49152\n"
"Maaa 84,212,248\nMaaa 52,212,244\nRa 180,10,51200\nMaaa 20,212,240\ns_cmp_eq_u32 s86, 0x1d2c3b4a\ns_subb_u32 s88, s88, 0\nMvaa 180,212,236\nRa 184,10,57344\nMvaa 148,212,232\n"
"D0 3,20,46,1024\nMvaa 116,212,228\nRa 188,10,59392\nMvaa 84,212,224\ns_cmp_eq_u32 s86, 0x1d2c3b4a\ns_subb_u32 s88, s88, 0\nMvaa 88,216,224\nMvaa 120,216,228\nMvaa 152,216,232\n"
"Mvaa 184,216,236\ns_cmp_eq_u32 s86, 0x1d2c3b4a\ns_subb_u32 s88, s88, 0\nMaaa 24,216,240\nMaaa 56,216,244\nD0 4,20,46,2048\nMaaa 88,216,248\nMaaa 120,216,252\n"
"s_cmp_eq_u32 s86, 0x1d2c3b4a\ns_subb_u32 s88, s88, 0\nMaaa 124,220,252\nMaaa 92,220,248\nMaaa 60,220,244\nMaaa 28,220,240\ns_cmp_eq_u32 s86, 0x1d2c3b4a\ns_subb_u32 s88, s88, 0\n"
"Mvaa 188,220,236\nMvaa 156,220,232\nD0 5,20,46,3072\nMvaa 124,220,228\nMvaa 92,220,224\ns_waitcnt vmcnt(14) lgkmcnt(0)\ns_barrier\nMvaa 64,128,160\nTa 192,12,49152\n"
"Mvaa 96,128,164\ns_mov_b32 s86, 0x1d2c3b4a\ns_load_dword s86, s[54:55], s95\nMvaa 128,128,168\nTa 194,12,51200\ns_add_u32 s95, s95, 1024\nMvaa 160,128,172\n"
"s_add_u32 m0, s47, 16384\ns_add_u32 s44, s44, 0x40000\nMaaa 0,128,176\nTa 196,13,49152\nMaaa 32,128,180\nD0 6,24,44,0\nMaaa 64,128,184\nTa 198,13,51200\nMaaa 96,128,188\n"
"s_cmp_eq_u32 s86, 0x1d2c3b4a\ns_subb_u32 s88, s88, 0\nMaaa 100,132,188\nTa 200,14,49152\nMaaa 68,132,184\nMaaa 36,132,180\nTa 202,14,51200\nMaaa 4,132,176\n"
"s_cmp_eq_u32 s86, 0x1d2c3b4a\ns_subb_u32 s88, s88, 0\nMvaa 164,132,172\nTa 204,15,49152\nMvaa 132,132,168\nD0 7,24,44,1024\nMvaa 100,132,164\nTa 206,15,51200\nMvaa 68,132,160\n"
"s_cmp_eq_u32 s86, 0x1d2c3b4a\ns_subb_u32 s88, s88, 0\nMvaa 72,136,160\nTa 208,16,49152\nMvaa 104,136,164\nMvaa 136,136,168\nTa 210,16,51200\nMvaa 168,136,172\n"
"s_cmp_eq_u32 s86, 0x1d2c3b4a\ns_subb_u32 s88, s88, 0\nMaaa 8,136,176\nTa 212,17,49152\nMaaa 40,136,180\nD0 8,24,44,2048\nMaaa 72,136,184\nTa 214,17,51200\nMaaa 104,136,188\n"
"s_cmp_eq_u32 s86, 0x1d2c3b4a\ns_subb_u32 s88, s88, 0\nMaaa 108,140,188\nTa 216,18,49152\nMaaa 76,140,184\nMaaa 44,140,180\nTa 218,18,51200\nMaaa 12,140,176\n"
"s_cmp_eq_u32 s86, 0x1d2c3b4a\ns_subb_u32 s88, s88, 0\nMvaa 172,140,172\nTa 220,19,49152\nMvaa 140,140,168\nD0 9,24,44,3072\nMvaa 108,140,164\nTa 222,19,51200\nMvaa 76,140,160\n"
"buffer_load_dword v31, v24, s[24:27], s96 offen\ns_add_u32 s96, s96, 0x80000\ns_cmp_eq_u32 s86, 0x1d2c3b4a\ns_subb_u32 s88, s88, 0\nMvaa 80,144,160\nRa 224,11,32768\n"
"s_mov_b32 m0, s76\ns_add_u32 s76, s76, 1024\nMvaa 112,144,164\nD0 1,36,75,0\ns_add_u32 s75, s75, 1024\nMvaa 144,144,168\nRa 228,11,34816\nMvaa 176,144,172\n"
"s_add_u32 m0, s47, 32768\ns_add_u32 s44, s44, 0x40000\ns_cmp_eq_u32 s86, 0x1d2c3b4a\ns_subb_u32 s88, s88, 0\nMaaa 16,144,176\nRa 232,11,40960\nMaaa 48,144,180\nD0 6,24,44,0\n"
"Maaa 80,144,184\nRa 236,11,43008\nMaaa 112,144,188\ns_cmp_eq_u32 s86, 0x1d2c3b4a\ns_subb_u32 s88, s88, 0\nMaaa 116,148,188\nRa 240,11,49152\nMaaa 84,148,184\nMaaa 52,148,180\n"
"Ra 244,11,51200\nMaaa 20,148,176\ns_cmp_eq_u32 s86, 0x1d2c3b4a\ns_subb_u32 s88, s88, 0\nMvaa 180,148,172\nRa 248,11,57344\nMvaa 148,148,168\nD0 7,24,44,1024\nMvaa 116,148,164\n"
"Ra 252,11,59392\nMvaa 84,148,160\ns_cmp_eq_u32 s86, 0x1d2c3b4a\ns_subb_u32 s88, s88, 0\nMvaa 88,152,160\nMvaa 120,152,164\nMvaa 152,152,168\nMvaa 184,152,172\n"
"s_cmp_eq_u32 s86, 0x1d2c3b4a\ns_subb_u32 s88, s88, 0\nMaaa 24,152,176\nMaaa 56,152,180\nD0 8,24,44,2048\nMaaa 88,152,184\nMaaa 120,152,188\ns_cmp_eq_u32 s86, 0x1d2c3b4a\n"
"s_subb_u32 s88, s88, 0\nMaaa 124,156,188\nMaaa 92,156,184\nMaaa 60,156,180\nMaaa 28,156,176\ns_cmp_eq_u32 s86, 0x1d2c3b4a\ns_subb_u32 s88, s88, 0\nMvaa 188,156,172\n"
"Mvaa 156,156,168\nD0 9,24,44,3072\nMvaa 124,156,164\nMvaa 92,156,160\ns_waitcnt vmcnt(10) lgkmcnt(0)\ns_barrier\nMvaa 64,192,224\nTa 128,12,0\nMvaa 96,192,228\n"
"s_mov_b32 s86, 0x1d2c3b4a\ns_load_dword s86, s[54:55], s94\nMvaa 128,192,232\nTa 130,12,2048\ns_add_u32 s94, s94, 1024\nMvaa 160,192,236\ns_add_u32 m0, s48, 32768\n"
"s_add_u32 s45, s45, 128\nMaaa 0,192,240\nTa 132,13,0\nMaaa 32,192,244\nD0 2,20,45,0\nMaaa 64,192,248\nTa 134,13,2048\nMaaa 96,192,252\ns_cmp_eq_u32 s86, 0x1d2c3b4a\n"
"s_addc_u32 s88, s88, 0\nMaaa 100,196,252\nTa 136,14,0\nMaaa 68,196,248\nMaaa 36,196,244\nTa 138,14,2048\nMaaa 4,196,240\ns_cmp_eq_u32 s86, 0x1d2c3b4a\ns_addc_u32 s88, s88, 0\n"
"Mvaa 164,196,236\nTa 140,15,0\nMvaa 132,196,232\nD0 3,20,45,1024\nMvaa 100,196,228\nTa 142,15,2048\nMvaa 68,196,224\ns_cmp_eq_u32 s86, 0x1d2c3b4a\ns_addc_u32 s88, s88, 0\n"
"Mvaa 72,200,224\nTa 144,16,0\nMvaa 104,200,228\nMvaa 136,200,232\nTa 146,16,2048\nMvaa 168,200,236\ns_cmp_eq_u32 s86, 0x1d2c3b4a\ns_addc_u32 s88, s88, 0\nMaaa 8,200,240\n"
"Ta 148,17,0\nMaaa 40,200,244\nD0 4,20,45,2048\nMaaa 72,200,248\nTa 150,17,2048\nMaaa 104,200,252\ns_cmp_eq_u32 s86, 0x1d2c3b4a\ns_addc_u32 s88, s88, 0\nMaaa 108,204,252\n"
"Ta 152,18,0\nMaaa 76,204,248\nMaaa 44,204,244\nTa 154,18,2048\nMaaa 12,204,240\ns_cmp_eq_u32 s86, 0x1d2c3b4a\ns_addc_u32 s88, s88, 0\nMvaa 172,204,236\nTa 156,19,0\n"
"Mvaa 140,204,232\nD0 5,20,45,3072\nMvaa 108,204,228\nTa 158,19,2048\nMvaa 76,204,224\ns_cmp_eq_u32 s86, 0x1d2c3b4a\ns_addc_u32 s88, s88, 0\nMvaa 80,208,224\nRa 160,10,0\n"
"Mvaa 112,208,228\nMvaa 144,208,232\nRa 164,10,2048\nMvaa 176,208,236\ns_add_u32 m0, s48, 36864\ns_add_u32 s46, s45, 0x10000\ns_cmp_eq_u32 s86, 0x1d2c3b4a\ns_addc_u32 s88, s88, 0\n"
"Maaa 16,208,240\nRa 168,10,8192\nMaaa 48,208,244\nD0 2,20,46,0\nMaaa 80,208,248\nRa 172,10,10240\nMaaa 112,208,252\ns_cmp_eq_u32 s86, 0x1d2c3b4a\ns_addc_u32 s88, s88, 0\n"
"Maaa 116,212,252\nRa 176,10,16384\nMaaa 84,212,248\nMaaa 52,212,244\nRa 180,10,18432\nMaaa 20,212,240\ns_cmp_eq_u32 s86, 0x1d2c3b4a\ns_addc_u32 s88, s88, 0\nMvaa 180,212,236\n"
"Ra 184,10,24576\nMvaa 148,212,232\nD0 3,20,46,1024\nMvaa 116,212,228\nRa 188,10,26624\nMvaa 84,212,224\ns_cmp_eq_u32 s86, 0x1d2c3b4a\ns_addc_u32 s88, s88, 0\nMvaa 88,216,224\n"
"Mvaa 120,216,228\nMvaa 152,216,232\nMvaa 184,216,236\ns_cmp_eq_u32 s86, 0x1d2c3b4a\ns_addc_u32 s88, s88, 0\nMaaa 24,216,240\nMaaa 56,216,244\nD0 4,20,46,2048\nMaaa 88,216,248\n"
"Maaa 120,216,252\ns_cmp_eq_u32 s86, 0x1d2c3b4a\ns_addc_u32 s88, s88, 0\nMaaa 124,220,252\nMaaa 92,220,248\nMaaa 60,220,244\nMaaa 28,220,240\ns_cmp_eq_u32 s86, 0x1d2c3b4a\n"
"s_addc_u32 s88, s88, 0\nMvaa 188,220,236\ns_xor_b32 s94, s94, 0x2000\ns_xor_b32 s95, s95, 0x2000\ns_sub_u32 s88, 0, s88\nMvaa 156,220,232\nD0 5,20,46,3072\ns_sub_u32 s50, s50, 1\n"
"Mvaa 124,220,228\nMvaa 92,220,224\ns_waitcnt vmcnt(14) lgkmcnt(0)\ns_barrier\ns_cbranch_scc0 .GL245\nMvaa 64,128,160\nTa 192,12,16384\nMvaa 96,128,164\ns_cmp_gt_i32 s88, 0\n"
"s_cselect_b32 s88, 0x2000, 0\nMvaa 128,128,168\nTa 194,12,18432\nMvaa 160,128,172\ns_add_u32 m0, s47, 49152\ns_add_u32 s44, s44, 0x40000\nMaaa 0,128,176\nTa 196,13,16384\n"
"Maaa 32,128,180\nD0 6,24,44,0\nMaaa 64,128,184\nTa 198,13,18432\nMaaa 96,128,188\nv_xor_b32 v27, s88, v25\nMaaa 100,132,188\nTa 200,14,16384\nMaaa 68,132,184\n"
"buffer_store_dword v27, off, s[16:19], s93 sc0 sc1\nMaaa 36,132,180\nTa 202,14,18432\nMaaa 4,132,176\nMvaa 164,132,172\nTa 204,15,16384\nMvaa 132,132,168\nD0 7,24,44,1024\n"
"Mvaa 100,132,164\nTa 206,15,18432\nMvaa 68,132,160\nMvaa 72,136,160\nTa 208,16,16384\nMvaa 104,136,164\nMvaa 136,136,168\nTa 210,16,18432\nMvaa 168,136,172\nMaaa 8,136,176\n"
"Ta 212,17,16384\nMaaa 40,136,180\nD0 8,24,44,2048\nMaaa 72,136,184\nTa 214,17,18432\nMaaa 104,136,188\nMaaa 108,140,188\nTa 216,18,16384\nMaaa 76,140,184\nMaaa 44,140,180\n"
"Ta 218,18,18432\nMaaa 12,140,176\nMvaa 172,140,172\nTa 220,19,16384\nMvaa 140,140,168\nD0 9,24,44,3072\nMvaa 108,140,164\nTa 222,19,18432\nMvaa 76,140,160\n"
"buffer_load_dword v31, v24, s[24:27], s96 offen\ns_add_u32 s96, s96, 0x80000\nMvaa 80,144,160\nRa 224,11,0\nMvaa 112,144,164\nMvaa 144,144,168\nRa 228,11,2048\nMvaa 176,144,172\n"
"s_add_u32 m0, s47, 0\ns_add_u32 s44, s44, 0x40000\nMaaa 16,144,176\nRa 232,11,8192\nMaaa 48,144,180\nD0 6,24,44,0\nMaaa 80,144,184\nRa 236,11,10240\nMaaa 112,144,188\n"
"Maaa 116,148,188\nRa 240,11,16384\nMaaa 84,148,184\nMaaa 52,148,180\nRa 244,11,18432\nMaaa 20,148,176\nMvaa 180,148,172\nRa 248,11,24576\nMvaa 148,148,168\nD0 7,24,44,1024\n"
"Mvaa 116,148,164\nRa 252,11,26624\nMvaa 84,148,160\nMvaa 88,152,160\nMvaa 120,152,164\nMvaa 152,152,168\nMvaa 184,152,172\nMaaa 24,152,176\nMaaa 56,152,180\nD0 8,24,44,2048\n"
"Maaa 88,152,184\nMaaa 120,152,188\nMaaa 124,156,188\nMaaa 92,156,184\nMaaa 60,156,180\nMaaa 28,156,176\nMvaa 188,156,172\nMvaa 156,156,168\nD0 9,24,44,3072\nMvaa 124,156,164\n"
"Mvaa 92,156,160\ns_waitcnt vmcnt(10) lgkmcnt(0)\ns_barrier\nMvaa 64,192,224\nTa 128,12,32768\nMvaa 96,192,228\nMvaa 128,192,232\nTa 130,12,34816\nMvaa 160,192,236\n"
"s_add_u32 m0, s48, 0\ns_add_u32 s45, s45, 128\nMaaa 0,192,240\nTa 132,13,32768\nMaaa 32,192,244\nD0 2,20,45,0\nMaaa 64,192,248\nTa 134,13,34816\nMaaa 96,192,252\nMaaa 100,196,252\n"
"Ta 136,14,32768\nMaaa 68,196,248\nMaaa 36,196,244\nTa 138,14,34816\nMaaa 4,196,240\nMvaa 164,196,236\nTa 140,15,32768\nMvaa 132,196,232\nD0 3,20,45,1024\nMvaa 100,196,228\n"
"Ta 142,15,34816\nMvaa 68,196,224\nMvaa 72,200,224\nTa 144,16,32768\nMvaa 104,200,228\nMvaa 136,200,232\nTa 146,16,34816\nMvaa 168,200,236\nMaaa 8,200,240\nTa 148,17,32768\n"
"Maaa 40,200,244\nD0 4,20,45,2048\nMaaa 72,200,248\nTa 150,17,34816\nMaaa 104,200,252\nMaaa 108,204,252\nTa 152,18,32768\nMaaa 76,204,248\nMaaa 44,204,244\nTa 154,18,34816\n"
"Maaa 12,204,240\nMvaa 172,204,236\nTa 156,19,32768\nMvaa 140,204,232\nD0 5,20,45,3072\nMvaa 108,204,228\nTa 158,19,34816\nMvaa 76,204,224\nMvaa 80,208,224\nRa 160,10,32768\n"
"Mvaa 112,208,228\nMvaa 144,208,232\nRa 164,10,34816\nMvaa 176,208,236\ns_add_u32 m0, s48, 4096\ns_add_u32 s46, s45, 0x10000\nMaaa 16,208,240\nRa 168,10,40960\nMaaa 48,208,244\n"
"D0 2,20,46,0\nMaaa 80,208,248\nRa 172,10,43008\nMaaa 112,208,252\nMaaa 116,212,252\nRa 176,10,49152\nMaaa 84,212,248\nMaaa 52,212,244\nRa 180,10,51200\nMaaa 20,212,240\n"
"Mvaa 180,212,236\nRa 184,10,57344\nMvaa 148,212,232\nD0 3,20,46,1024\nMvaa 116,212,228\nRa 188,10,59392\nMvaa 84,212,224\nMvaa 88,216,224\nMvaa 120,216,228\nMvaa 152,216,232\n"
"Mvaa 184,216,236\nMaaa 24,216,240\nMaaa 56,216,244\nD0 4,20,46,2048\nMaaa 88,216,248\nMaaa 120,216,252\nMaaa 124,220,252\nMaaa 92,220,248\nMaaa 60,220,244\nMaaa 28,220,240\n"
"Mvaa 188,220,236\nMvaa 156,220,232\nD0 5,20,46,3072\nMvaa 124,220,228\nMvaa 92,220,224\ns_waitcnt vmcnt(13) lgkmcnt(0)\ns_barrier\nMvaa 64,128,160\nTa 192,12,49152\n"
"Mvaa 96,128,164\nMvaa 128,128,168\nTa 194,12,51200\nMvaa 160,128,172\ns_add_u32 m0, s47, 16384\ns_add_u32 s44, s44, 0x40000\nMaaa 0,128,176\nTa 196,13,49152\nMaaa 32,128,180\n"
"D0 6,24,44,0\nMaaa 64,128,184\nTa 198,13,51200\nMaaa 96,128,188\nMaaa 100,132,188\nTa 200,14,49152\nMaaa 68,132,184\nMaaa 36,132,180\nTa 202,14,51200\nMaaa 4,132,176\n"
"Mvaa 164,132,172\nTa 204,15,49152\nMvaa 132,132,168\nD0 7,24,44,1024\nMvaa 100,132,164\nTa 206,15,51200\nMvaa 68,132,160\nMvaa 72,136,160\nTa 208,16,49152\nMvaa 104,136,164\n"
"Mvaa 136,136,168\nTa 210,16,51200\nMvaa 168,136,172\nMaaa 8,136,176\nTa 212,17,49152\nMaaa 40,136,180\nD0 8,24,44,2048\nMaaa 72,136,184\nTa 214,17,51200\nMaaa 104,136,188\n"
"Maaa 108,140,188\nTa 216,18,49152\nMaaa 76,140,184\nMaaa 44,140,180\nTa 218,18,51200\nMaaa 12,140,176\nMvaa 172,140,172\nTa 220,19,49152\nMvaa 140,140,168\nD0 9,24,44,3072\n"
"Mvaa 108,140,164\nTa 222,19,51200\nMvaa 76,140,160\nbuffer_load_dword v31, v24, s[24:27], s96 offen\ns_add_u32 s96, s96, 0x80000\nMvaa 80,144,160\nRa 224,11,32768\n"
"Mvaa 112,144,164\nMvaa 144,144,168\nRa 228,11,34816\nMvaa 176,144,172\ns_add_u32 m0, s47, 32768\ns_add_u32 s44, s44, 0x40000\nMaaa 16,144,176\nRa 232,11,40960\nMaaa 48,144,180\n"
"D0 6,24,44,0\nMaaa 80,144,184\nRa 236,11,43008\nMaaa 112,144,188\nMaaa 116,148,188\nRa 240,11,49152\nMaaa 84,148,184\ns_lshl_b32 s5, s73, 8\ns_add_u32 s5, s5, 0xc04ac00\n"
"v_lshrrev_b32 v27, 2, v1\nbuffer_load_dword v27, v27, s[16:19], s5 offen sc0 sc1\nMaaa 52,148,180\nRa 244,11,51200\nMaaa 20,148,176\nMvaa 180,148,172\nRa 248,11,57344\n"
"Mvaa 148,148,168\nD0 7,24,44,1024\nMvaa 116,148,164\nRa 252,11,59392\nMvaa 84,148,160\nMvaa 88,152,160\nMvaa 120,152,164\nMvaa 152,152,168\nMvaa 184,152,172\nMaaa 24,152,176\n"
"Maaa 56,152,180\nD0 8,24,44,2048\nMaaa 88,152,184\nMaaa 120,152,188\nMaaa 124,156,188\nMaaa 92,156,184\nMaaa 60,156,180\nMaaa 28,156,176\nMvaa 188,156,172\nMvaa 156,156,168\n"
"D0 9,24,44,3072\nMvaa 124,156,164\nMvaa 92,156,160\ns_waitcnt vmcnt(10) lgkmcnt(0)\ns_barrier\nMvaa 64,192,224\nTa 128,12,0\nMvaa 96,192,228\nMvaa 128,192,232\nTa 130,12,2048\n"
"Mvaa 160,192,236\ns_add_u32 m0, s48, 32768\ns_add_u32 s45, s45, 128\nMaaa 0,192,240\nTa 132,13,0\nMaaa 32,192,244\nD0 2,20,45,0\nMaaa 64,192,248\nTa 134,13,2048\nMaaa 96,192,252\n"
"Maaa 100,196,252\nTa 136,14,0\nMaaa 68,196,248\nMaaa 36,196,244\nTa 138,14,2048\nMaaa 4,196,240\nMvaa 164,196,236\nTa 140,15,0\nMvaa 132,196,232\nD0 3,20,45,1024\n"
"Mvaa 100,196,228\nTa 142,15,2048\nMvaa 68,196,224\nMvaa 72,200,224\nTa 144,16,0\nMvaa 104,200,228\nMvaa 136,200,232\nTa 146,16,2048\nMvaa 168,200,236\nMaaa 8,200,240\nTa 148,17,0\n"
"Maaa 40,200,244\nD0 4,20,45,2048\nMaaa 72,200,248\nTa 150,17,2048\nMaaa 104,200,252\nMaaa 108,204,252\nTa 152,18,0\nMaaa 76,204,248\nMaaa 44,204,244\nTa 154,18,2048\n"
"Maaa 12,204,240\nMvaa 172,204,236\nTa 156,19,0\nMvaa 140,204,232\nD0 5,20,45,3072\nMvaa 108,204,228\nTa 158,19,2048\nMvaa 76,204,224\nMvaa 80,208,224\nRa 160,10,0\n"
"Mvaa 112,208,228\nMvaa 144,208,232\nRa 164,10,2048\nMvaa 176,208,236\ns_add_u32 m0, s48, 36864\ns_add_u32 s46, s45, 0x10000\nMaaa 16,208,240\nRa 168,10,8192\nMaaa 48,208,244\n"
"D0 2,20,46,0\nMaaa 80,208,248\nRa 172,10,10240\nMaaa 112,208,252\nMaaa 116,212,252\nRa 176,10,16384\nMaaa 84,212,248\nMaaa 52,212,244\nRa 180,10,18432\nMaaa 20,212,240\n"
"Mvaa 180,212,236\nRa 184,10,24576\nMvaa 148,212,232\nD0 3,20,46,1024\nMvaa 116,212,228\nRa 188,10,26624\nMvaa 84,212,224\nMvaa 88,216,224\nMvaa 120,216,228\nMvaa 152,216,232\n"
"Mvaa 184,216,236\nMaaa 24,216,240\nMaaa 56,216,244\nD0 4,20,46,2048\nMaaa 88,216,248\nMaaa 120,216,252\nMaaa 124,220,252\nMaaa 92,220,248\nMaaa 60,220,244\nMaaa 28,220,240\n"
"Mvaa 188,220,236\nMvaa 156,220,232\nD0 5,20,46,3072\nMvaa 124,220,228\nMvaa 92,220,224\ns_waitcnt vmcnt(14) lgkmcnt(0)\ns_barrier\nMvaa 64,128,160\nTa 192,12,16384\nWV 11\n"
"v_xor_b32 v27, s15, v27\nv_and_b32 v32, 0xffffdfff, v27\nv_cmp_ne_u32 vcc, 0, v32\ns_nop 0\ns_cbranch_vccnz .S242\n.S243:\nv_cmp_ne_u32 vcc, 0, v27\ns_nop 3\n"
"s_bcnt1_i32_b64 s4, vcc\ns_cmp_gt_u32 s4, 32\ns_cselect_b32 s4, 1, 0\nMvaa 96,128,164\ns_xor_b32 s4, s4, s12\nMvaa 128,128,168\nTa 194,12,18432\nMvaa 160,128,172\n"
"s_add_u32 m0, s47, 49152\ns_add_u32 s44, s44, 0x40000\nMaaa 0,128,176\nTa 196,13,16384\nMaaa 32,128,180\nD0 6,24,44,0\nMaaa 64,128,184\nTa 198,13,18432\nMaaa 96,128,188\n"
"s_and_b32 s4, s4, 1\nMaaa 100,132,188\nTa 200,14,16384\nMaaa 68,132,184\ns_lshl_b32 s4, s4, 13\nMaaa 36,132,180\nTa 202,14,18432\nMaaa 4,132,176\ns_mul_i32 s72, s73, 25165824\n"
"Mvaa 164,132,172\nTa 204,15,16384\nMvaa 132,132,168\nD0 7,24,44,1024\nMvaa 100,132,164\nTa 206,15,18432\nMvaa 68,132,160\ns_add_u32 s72, s72, s4\nMvaa 72,136,160\nTa 208,16,16384\n"
"Mvaa 104,136,164\ns_and_b32 s4, s12, 1\nMvaa 136,136,168\nTa 210,16,18432\nMvaa 168,136,172\ns_lshl_b32 s4, s4, 21\nMaaa 8,136,176\nTa 212,17,16384\nMaaa 40,136,180\n"
"D0 8,24,44,2048\nMaaa 72,136,184\nTa 214,17,18432\nMaaa 104,136,188\ns_add_u32 s72, s72, s4\nMaaa 108,140,188\nTa 216,18,16384\nMaaa 76,140,184\ns_lshl3_add_u32 s2, s13, s11\n"
"Maaa 44,140,180\nTa 218,18,18432\nMaaa 12,140,176\ns_lshl_b32 s2, s2, 11\nMvaa 172,140,172\nTa 220,19,16384\nMvaa 140,140,168\nD0 9,24,44,3072\nMvaa 108,140,164\nTa 222,19,18432\n"
"Mvaa 76,140,160\nbuffer_load_dword v31, v24, s[24:27], s96 offen\ns_add_u32 s96, s96, 0x80000\nMvaa 80,144,160\nRa 224,11,0\nMvaa 112,144,164\ns_lshl_b32 s3, s14, 22\n"
"Mvaa 144,144,168\nRa 228,11,2048\nMvaa 176,144,172\ns_add_u32 m0, s47, 0\ns_add_u32 s44, s44, 0x40000\nMaaa 16,144,176\nRa 232,11,8192\nMaaa 48,144,180\nD0 6,24,44,0\n"
"Maaa 80,144,184\nRa 236,11,10240\nMaaa 112,144,188\ns_add_u32 s2, s2, s3\nMaaa 116,148,188\nRa 240,11,16384\nMaaa 84,148,184\ns_add_u32 s2, s2, s72\nMaaa 52,148,180\n"
"Ra 244,11,18432\nMaaa 20,148,176\ns_add_u32 s49, s2, 0xc00000\nMvaa 180,148,172\nRa 248,11,24576\nMvaa 148,148,168\nD0 7,24,44,1024\nMvaa 116,148,164\nRa 252,11,26624\n"
"Mvaa 84,148,160\nMvaa 88,152,160\nMvaa 120,152,164\nMvaa 152,152,168\nMvaa 184,152,172\nMaaa 24,152,176\nMaaa 56,152,180\nD0 8,24,44,2048\nMaaa 88,152,184\nMaaa 120,152,188\n"
"Maaa 124,156,188\nMaaa 92,156,184\nMaaa 60,156,180\nMaaa 28,156,176\nMvaa 188,156,172\nMvaa 156,156,168\nD0 9,24,44,3072\nMvaa 124,156,164\nMvaa 92,156,160\n"
"s_waitcnt vmcnt(9) lgkmcnt(0)\ns_barrier\nMvaa 64,192,224\nTa 128,12,32768\ns_mov_b32 s4, s13\nMvaa 96,192,228\nv_lshrrev_b32 v32, 2, v0\nMvaa 128,192,232\nTa 130,12,34816\n"
"v_mul_u32_u24 v32, 896, v32\nMvaa 160,192,236\ns_add_u32 m0, s48, 0\ns_add_u32 s45, s45, 128\nMaaa 0,192,240\nTa 132,13,32768\nv_and_b32 v33, 3, v0\nMaaa 32,192,244\nD0 2,20,45,0\n"
"Maaa 64,192,248\nTa 134,13,34816\nv_lshl_add_u32 v32, v33, 4, v32\nMaaa 96,192,252\ns_lshr_b32 s2, s11, 1\nMaaa 100,196,252\nTa 136,14,32768\ns_mul_i32 s3, s2, 448\n"
"Maaa 68,196,248\ns_and_b32 s5, s11, 1\nMaaa 36,196,244\nTa 138,14,34816\ns_lshl3_add_u32 s3, s5, s3\nMaaa 4,196,240\ns_add_u32 s3, s3, 131072\nMvaa 164,196,236\nTa 140,15,32768\n"
"v_add_u32 v32, s3, v32\nMvaa 132,196,232\nD0 3,20,45,1024\nMvaa 100,196,228\nTa 142,15,34816\nTv 36,32,0\nMvaa 68,196,224\nTv 38,32,14336\nMvaa 72,200,224\nTa 144,16,32768\n"
"Tv 40,32,64\nMvaa 104,200,228\nTv 42,32,14400\nMvaa 136,200,232\nTa 146,16,34816\nTv 44,32,128\nMvaa 168,200,236\nTv 46,32,14464\nMaaa 8,200,240\nTa 148,17,32768\nTv 48,32,192\n"
"Maaa 40,200,244\nD0 4,20,45,2048\nMaaa 72,200,248\nTa 150,17,34816\nTv 50,32,14528\nMaaa 104,200,252\nTv 52,32,256\nMaaa 108,204,252\nTa 152,18,32768\nTv 54,32,14592\n"
"Maaa 76,204,248\nTv 56,32,320\nMaaa 44,204,244\nTa 154,18,34816\nTv 58,32,14656\nMaaa 12,204,240\nTv 60,32,384\nMvaa 172,204,236\nTa 156,19,32768\nTv 62,32,14720\n"
"Mvaa 140,204,232\nD0 5,20,45,3072\nMvaa 108,204,228\nTa 158,19,34816\ns_mul_i32 s3, s2, 229376\nMvaa 76,204,224\ns_add_u32 s3, s3, s72\nMvaa 80,208,224\nRa 160,10,32768\n"
"s_add_u32 s3, s3, 0x1400000\nMvaa 112,208,228\ns_lshr_b32 s0, s4, 1\nMvaa 144,208,232\nRa 164,10,34816\ns_mul_i32 s0, s0, 28\nMvaa 176,208,236\ns_add_u32 m0, s48, 4096\n"
"s_add_u32 s46, s45, 0x10000\nMaaa 16,208,240\nRa 168,10,40960\ns_lshl1_add_u32 s0, s5, s0\nMaaa 48,208,244\nD0 2,20,46,0\nMaaa 80,208,248\nRa 172,10,43008\ns_and_b32 s6, s4, 1\n"
"Maaa 112,208,252\ns_add_u32 s0, s0, s6\nMaaa 116,212,252\nRa 176,10,49152\ns_lshr_b32 s77, s4, 2\nMaaa 84,212,248\ns_mul_i32 s1, s77, 16\nMaaa 52,212,244\nRa 180,10,51200\n"
"s_lshl2_add_u32 s1, s5, s1\nMaaa 20,212,240\ns_and_b32 s6, s4, 3\nMvaa 180,212,236\nRa 184,10,57344\ns_add_u32 s1, s1, s6\nMvaa 148,212,232\nD0 3,20,46,1024\nMvaa 116,212,228\n"
"Ra 188,10,59392\ns_lshl1_add_u32 s77, s5, s77\nMvaa 84,212,224\ns_lshl2_add_u32 s77, s77, s6\nMvaa 88,216,224\ns_add_u32 s77, s77, 32\ns_and_b32 s4, s12, 1\nMvaa 120,216,228\n"
"s_mov_b32 s7, 0x6543210\ns_cmp_eq_u32 s4, s2\ns_cselect_b32 s6, s0, s1\ns_cselect_b32 s5, 4, 8\ns_cselect_b32 s7, 0x6352410, s7\nMvaa 152,216,232\nWL 14\ns_lshr_b32 s0, s6, 4\n"
"Mvaa 184,216,236\ns_lshl_b32 s0, s0, 2\nMaaa 24,216,240\ns_lshr_b32 s0, s7, s0\ns_and_b32 s0, s0, 7\nMaaa 56,216,244\nD0 4,20,46,2048\nMaaa 88,216,248\ns_lshl_b32 s0, s0, 5\n"
"s_and_b32 s1, s6, 15\nMaaa 120,216,252\ns_add_u32 s0, s0, s1\nMaaa 124,220,252\ns_and_b32 s1, s6, 8\ns_add_u32 s0, s0, s1\nMaaa 92,220,248\ns_lshl_b32 s0, s0, 10\nMaaa 60,220,244\n"
"s_add_u32 s0, s0, s3\nSv 36,1,16,0,0\nMaaa 28,220,240\ns_add_u32 s6, s6, s5\nMvaa 188,220,236\nMvaa 156,220,232\nD0 5,20,46,3072\nMvaa 124,220,228\nMvaa 92,220,224\n"
"s_waitcnt vmcnt(14) lgkmcnt(0)\ns_barrier\nMvaa 64,128,160\nTa 192,12,49152\nMvaa 96,128,164\nMvaa 128,128,168\nTa 194,12,51200\nMvaa 160,128,172\ns_add_u32 m0, s47, 16384\n"
"s_add_u32 s44, s44, 0x40000\nMaaa 0,128,176\nTa 196,13,49152\ns_lshr_b32 s0, s6, 4\nMaaa 32,128,180\nD0 6,24,44,0\nMaaa 64,128,184\nTa 198,13,51200\ns_lshl_b32 s0, s0, 2\n"
"Maaa 96,128,188\ns_lshr_b32 s0, s7, s0\nMaaa 100,132,188\nTa 200,14,49152\ns_and_b32 s0, s0, 7\nMaaa 68,132,184\ns_lshl_b32 s0, s0, 5\nMaaa 36,132,180\nTa 202,14,51200\n"
"s_and_b32 s1, s6, 15\nMaaa 4,132,176\ns_add_u32 s0, s0, s1\nMvaa 164,132,172\nTa 204,15,49152\ns_and_b32 s1, s6, 8\nMvaa 132,132,168\nD0 7,24,44,1024\nMvaa 100,132,164\n"
"Ta 206,15,51200\ns_add_u32 s0, s0, s1\nMvaa 68,132,160\ns_lshl_b32 s0, s0, 10\nMvaa 72,136,160\nTa 208,16,49152\ns_add_u32 s0, s0, s3\nMvaa 104,136,164\nSv 40,1,16,0,0\n"
"Mvaa 136,136,168\nTa 210,16,51200\ns_add_u32 s6, s6, s5\nMvaa 168,136,172\nMaaa 8,136,176\nTa 212,17,49152\nMaaa 40,136,180\nD0 8,24,44,2048\nMaaa 72,136,184\nTa 214,17,51200\n"
"Maaa 104,136,188\nMaaa 108,140,188\nTa 216,18,49152\nMaaa 76,140,184\nMaaa 44,140,180\nTa 218,18,51200\ns_cmp_eq_u32 s5, 8\ns_cselect_b32 s6, s77, s6\ns_cselect_b32 s5, 16, s5\n"
"Maaa 12,140,176\ns_lshr_b32 s0, s6, 4\nMvaa 172,140,172\nTa 220,19,49152\ns_lshl_b32 s0, s0, 2\nMvaa 140,140,168\nD0 9,24,44,3072\nMvaa 108,140,164\nTa 222,19,51200\n"
"s_lshr_b32 s0, s7, s0\nMvaa 76,140,160\ns_and_b32 s0, s0, 7\nMvaa 80,144,160\nRa 224,11,32768\ns_lshl_b32 s0, s0, 5\nMvaa 112,144,164\ns_and_b32 s1, s6, 15\nMvaa 144,144,168\n"
"Ra 228,11,34816\ns_add_u32 s0, s0, s1\nMvaa 176,144,172\ns_add_u32 m0, s47, 32768\ns_add_u32 s44, s44, 0x40000\nMaaa 16,144,176\nRa 232,11,40960\ns_and_b32 s1, s6, 8\n"
"Maaa 48,144,180\nD0 6,24,44,0\nMaaa 80,144,184\nRa 236,11,43008\ns_add_u32 s0, s0, s1\nMaaa 112,144,188\ns_lshl_b32 s0, s0, 10\nMaaa 116,148,188\nRa 240,11,49152\n"
"s_add_u32 s0, s0, s3\nMaaa 84,148,184\nSv 44,1,16,0,0\nMaaa 52,148,180\nRa 244,11,51200\ns_add_u32 s6, s6, s5\nMaaa 20,148,176\nMvaa 180,148,172\nRa 248,11,57344\n"
"Mvaa 148,148,168\nD0 7,24,44,1024\nMvaa 116,148,164\nRa 252,11,59392\nMvaa 84,148,160\nMvaa 88,152,160\nMvaa 120,152,164\nMvaa 152,152,168\ns_lshr_b32 s0, s6, 4\n"
"s_lshl_b32 s0, s0, 2\nMvaa 184,152,172\ns_lshr_b32 s0, s7, s0\nMaaa 24,152,176\ns_and_b32 s0, s0, 7\ns_lshl_b32 s0, s0, 5\nMaaa 56,152,180\nD0 8,24,44,2048\nMaaa 88,152,184\n"
"s_and_b32 s1, s6, 15\ns_add_u32 s0, s0, s1\nMaaa 120,152,188\ns_and_b32 s1, s6, 8\nMaaa 124,156,188\ns_add_u32 s0, s0, s1\ns_lshl_b32 s0, s0, 10\nMaaa 92,156,184\n"
"s_add_u32 s0, s0, s3\nMaaa 60,156,180\nSv 48,1,16,0,0\ns_add_u32 s6, s6, s5\nMaaa 28,156,176\nMvaa 188,156,172\nMvaa 156,156,168\nD0 9,24,44,3072\nMvaa 124,156,164\n"
"Mvaa 92,156,160\ns_waitcnt vmcnt(11) lgkmcnt(0)\ns_barrier\nMvaa 64,192,224\nTa 128,12,0\nMvaa 96,192,228\nMvaa 128,192,232\nTa 130,12,2048\ns_lshr_b32 s0, s6, 4\n"
"Mvaa 160,192,236\ns_add_u32 m0, s48, 32768\ns_add_u32 s45, s45, 128\nMaaa 0,192,240\nTa 132,13,0\ns_lshl_b32 s0, s0, 2\nMaaa 32,192,244\nD0 2,20,45,0\nMaaa 64,192,248\n"
"Ta 134,13,2048\ns_lshr_b32 s0, s7, s0\nMaaa 96,192,252\ns_and_b32 s0, s0, 7\nMaaa 100,196,252\nTa 136,14,0\ns_lshl_b32 s0, s0, 5\nMaaa 68,196,248\ns_and_b32 s1, s6, 15\n"
"Maaa 36,196,244\nTa 138,14,2048\ns_add_u32 s0, s0, s1\nMaaa 4,196,240\ns_and_b32 s1, s6, 8\nMvaa 164,196,236\nTa 140,15,0\ns_add_u32 s0, s0, s1\nMvaa 132,196,232\nD0 3,20,45,1024\n"
"Mvaa 100,196,228\nTa 142,15,2048\ns_lshl_b32 s0, s0, 10\nMvaa 68,196,224\ns_add_u32 s0, s0, s3\nMvaa 72,200,224\nTa 144,16,0\nSv 52,1,16,0,0\nMvaa 104,200,228\n"
"s_add_u32 s6, s6, s5\nMvaa 136,200,232\nTa 146,16,2048\nMvaa 168,200,236\nMaaa 8,200,240\nTa 148,17,0\nMaaa 40,200,244\nD0 4,20,45,2048\nMaaa 72,200,248\nTa 150,17,2048\n"
"Maaa 104,200,252\nMaaa 108,204,252\nTa 152,18,0\nMaaa 76,204,248\ns_lshr_b32 s0, s6, 4\nMaaa 44,204,244\nTa 154,18,2048\ns_lshl_b32 s0, s0, 2\nMaaa 12,204,240\n"
"s_lshr_b32 s0, s7, s0\nMvaa 172,204,236\nTa 156,19,0\ns_and_b32 s0, s0, 7\nMvaa 140,204,232\nD0 5,20,45,3072\nMvaa 108,204,228\nTa 158,19,2048\ns_lshl_b32 s0, s0, 5\n"
"Mvaa 76,204,224\ns_and_b32 s1, s6, 15\nMvaa 80,208,224\nRa 160,10,0\ns_add_u32 s0, s0, s1\nMvaa 112,208,228\ns_and_b32 s1, s6, 8\nMvaa 144,208,232\nRa 164,10,2048\n"
"s_add_u32 s0, s0, s1\nMvaa 176,208,236\ns_add_u32 m0, s48, 36864\ns_add_u32 s46, s45, 0x10000\nMaaa 16,208,240\nRa 168,10,8192\ns_lshl_b32 s0, s0, 10\nMaaa 48,208,244\n"
"D0 2,20,46,0\nMaaa 80,208,248\nRa 172,10,10240\ns_add_u32 s0, s0, s3\nMaaa 112,208,252\nSv 56,1,16,0,0\nMaaa 116,212,252\nRa 176,10,16384\ns_add_u32 s6, s6, s5\nMaaa 84,212,248\n"
"Maaa 52,212,244\nRa 180,10,18432\nMaaa 20,212,240\nMvaa 180,212,236\nRa 184,10,24576\nMvaa 148,212,232\nD0 3,20,46,1024\nMvaa 116,212,228\nRa 188,10,26624\nMvaa 84,212,224\n"
"Mvaa 88,216,224\ns_lshr_b32 s0, s6, 4\ns_lshl_b32 s0, s0, 2\nMvaa 120,216,228\ns_lshr_b32 s0, s7, s0\nMvaa 152,216,232\ns_and_b32 s0, s0, 7\ns_lshl_b32 s0, s0, 5\n"
"Mvaa 184,216,236\ns_and_b32 s1, s6, 15\nMaaa 24,216,240\ns_add_u32 s0, s0, s1\ns_and_b32 s1, s6, 8\nMaaa 56,216,244\nD0 4,20,46,2048\nMaaa 88,216,248\ns_add_u32 s0, s0, s1\n"
"s_lshl_b32 s0, s0, 10\nMaaa 120,216,252\ns_add_u32 s0, s0, s3\nMaaa 124,220,252\nSv 60,1,16,0,0\ns_add_u32 s6, s6, s5\nMaaa 92,220,248\nMaaa 60,220,244\nMaaa 28,220,240\n"
"Mvaa 188,220,236\nMvaa 156,220,232\nD0 5,20,46,3072\nMvaa 124,220,228\nMvaa 92,220,224\ns_waitcnt vmcnt(17) lgkmcnt(0)\ns_barrier\nMvaa 64,128,160\nTa 192,12,16384\n"
"v_lshrrev_b32 v48, 4, v0\nMvaa 96,128,164\nv_and_b32 v49, 7, v0\ns_add_u32 m0, s47, 49152\ns_add_u32 s44, s44, 0x40000\nMvaa 128,128,168\nTa 194,12,18432\nMvaa 160,128,172\n"
"v_xor_b32 v49, v49, v48\nD0 6,24,44,0\nMaaa 0,128,176\nTa 196,13,16384\nv_lshlrev_b32 v49, 4, v49\nMaaa 32,128,180\nv_lshrrev_b32 v48, 3, v0\nMaaa 64,128,184\nTa 198,13,18432\n"
"Maaa 96,128,188\nv_mul_u32_u24 v48, 896, v48\nMaaa 100,132,188\nTa 200,14,16384\nv_add_u32 v40, v48, v49\nD0 7,24,44,1024\nMaaa 68,132,184\nv_xor_b32 v49, 64, v40\n"
"Maaa 36,132,180\nTa 202,14,18432\nMaaa 4,132,176\nv_add_u32 v41, 6144, v49\nMvaa 164,132,172\nTa 204,15,16384\nv_add_u32 v42, 55296, v40\nMvaa 132,132,168\n"
"v_add_u32 v43, 61440, v49\nD0 8,24,44,2048\nMvaa 100,132,164\nTa 206,15,18432\nMvaa 68,132,160\ns_mov_b32 s2, s13\nMvaa 72,136,160\nTa 208,16,16384\ns_lshl3_add_u32 s2, s2, s11\n"
"Mvaa 104,136,164\ns_mul_i32 s52, s2, 14336\nMvaa 136,136,168\nTa 210,16,18432\nD0 9,24,44,3072\nMvaa 168,136,172\ns_lshr_b32 s2, s13, 2\nMaaa 8,136,176\nTa 212,17,16384\n"
"s_bfe_u32 s3, s13, 0x10001\nMaaa 40,136,180\ns_mul_i32 s3, s3, 17\ns_add_u32 m0, s47, 65536\ns_add_u32 s46, s51, 0\nMaaa 72,136,184\nTa 214,17,18432\nMaaa 104,136,188\n"
"s_add_u32 s2, s2, s3\nD0 6,32,46,0\nMaaa 108,140,188\nTa 216,18,16384\ns_lshl2_add_u32 s3, s14, 0\nMaaa 76,140,184\ns_lshl_b32 s3, s3, 2\nMaaa 44,140,180\nTa 218,18,18432\n"
"Maaa 12,140,176\ns_lshr_b32 s3, 0xe420ca86, s3\nMvaa 172,140,172\nTa 220,19,16384\ns_and_b32 s3, s3, 15\nD0 7,32,46,1024\nMvaa 140,140,168\ns_add_u32 s2, s2, s3\n"
"s_cmp_eq_u32 s2, 32\ns_cselect_b32 s4, 16, s2\nMvaa 108,140,164\nTa 222,19,18432\nMvaa 76,140,160\ns_mul_i32 s75, s4, 65536\nMvaa 80,144,160\nRa 224,11,0\ns_lshr_b32 s2, s13, 2\n"
"Mvaa 112,144,164\ns_bfe_u32 s3, s13, 0x10001\nD0 8,32,46,2048\nMvaa 144,144,168\nRa 228,11,2048\nMvaa 176,144,172\ns_mul_i32 s3, s3, 17\nMaaa 16,144,176\nRa 232,11,8192\n"
"s_add_u32 s2, s2, s3\nMaaa 48,144,180\ns_lshl2_add_u32 s3, s14, 1\nMaaa 80,144,184\nRa 236,11,10240\nD0 9,32,46,3072\nMaaa 112,144,188\ns_lshl_b32 s3, s3, 2\nMaaa 116,148,188\n"
"Ra 240,11,16384\ns_lshr_b32 s3, 0xe420ca86, s3\nMaaa 84,148,184\ns_and_b32 s3, s3, 15\ns_add_u32 m0, s47, 81920\ns_add_u32 s46, s51, 262144\nMaaa 52,148,180\nRa 244,11,18432\n"
"Maaa 20,148,176\ns_add_u32 s2, s2, s3\ns_cmp_eq_u32 s2, 32\ns_cselect_b32 s4, 16, s2\nD0 6,32,46,0\nMvaa 180,148,172\nRa 248,11,24576\ns_mul_i32 s76, s4, 65536\nMvaa 148,148,168\n"
"s_lshr_b32 s2, s13, 2\nMvaa 116,148,164\nRa 252,11,26624\nMvaa 84,148,160\ns_bfe_u32 s3, s13, 0x10001\nMvaa 88,152,160\ns_mul_i32 s3, s3, 17\ns_add_u32 s2, s2, s3\n"
"D0 7,32,46,1024\nMvaa 120,152,164\ns_lshl2_add_u32 s3, s14, 2\nMvaa 152,152,168\ns_lshl_b32 s3, s3, 2\nMvaa 184,152,172\ns_lshr_b32 s3, 0xe420ca86, s3\nMaaa 24,152,176\n"
"s_and_b32 s3, s3, 15\ns_add_u32 s2, s2, s3\nMaaa 56,152,180\ns_cmp_eq_u32 s2, 32\ns_cselect_b32 s4, 16, s2\nD0 8,32,46,2048\nMaaa 88,152,184\ns_mul_i32 s77, s4, 65536\n"
"Maaa 120,152,188\ns_lshr_b32 s2, s13, 2\nMaaa 124,156,188\ns_bfe_u32 s3, s13, 0x10001\ns_mul_i32 s3, s3, 17\nMaaa 92,156,184\ns_add_u32 s2, s2, s3\nMaaa 60,156,180\n"
"s_lshl2_add_u32 s3, s14, 3\nD0 9,32,46,3072\nMaaa 28,156,176\ns_lshl_b32 s3, s3, 2\nMvaa 188,156,172\ns_lshr_b32 s3, 0xe420ca86, s3\ns_and_b32 s3, s3, 15\nMvaa 156,156,168\n"
"s_add_u32 s2, s2, s3\ns_cmp_eq_u32 s2, 32\ns_cselect_b32 s4, 16, s2\nMvaa 124,156,164\ns_mul_i32 s78, s4, 65536\nMvaa 92,156,160\ns_waitcnt vmcnt(12) lgkmcnt(0)\ns_barrier\n"
"Mvaa 64,192,224\nTa 128,12,32768\ns_lshr_b32 s3, s11, 1\nMvaa 96,192,228\ns_lshl_b32 s3, s3, 15\ns_add_u32 m0, s47, -65536\ns_add_u32 s46, s52, 0\nMvaa 128,192,232\n"
"Ta 130,12,34816\nMvaa 160,192,236\ns_add_u32 s2, s72, s3\nD0 40,28,46,0\nMaaa 0,192,240\nTa 132,13,32768\ns_and_b32 s3, s11, 1\nMaaa 32,192,244\ns_lshl_b32 s3, s3, 12\n"
"Maaa 64,192,248\nTa 134,13,34816\nMaaa 96,192,252\ns_add_u32 s2, s2, s3\nMaaa 100,196,252\nTa 136,14,32768\ns_add_u32 s2, s2, 0x0\nD0 41,28,46,1024\nMaaa 68,196,248\n"
"s_mov_b32 s55, 16384\nMaaa 36,196,244\nTa 138,14,34816\nMaaa 4,196,240\ns_add_u32 s75, s75, s2\nMvaa 164,196,236\nTa 140,15,32768\ns_add_u32 s76, s76, s2\nMvaa 132,196,232\n"
"s_add_u32 s77, s77, s2\nD0 42,28,46,2048\nMvaa 100,196,228\nTa 142,15,34816\nMvaa 68,196,224\ns_add_u32 s78, s78, s2\nMvaa 72,200,224\nTa 144,16,32768\n"
"v_add_u32 v32, 0x10000, v12\nMvaa 104,200,228\nv_add_u32 v33, 0x10000, v13\nMvaa 136,200,232\nTa 146,16,34816\nD0 43,28,46,3072\nMvaa 168,200,236\nv_add_u32 v34, 0x10000, v14\n"
"Maaa 8,200,240\nTa 148,17,32768\nv_add_u32 v35, 0x10000, v15\nMaaa 40,200,244\nv_add_u32 v36, 0x10000, v16\ns_add_u32 m0, s47, 0\ns_add_u32 s46, s51, 524288\nMaaa 72,200,248\n"
"Ta 150,17,34816\nMaaa 104,200,252\nv_add_u32 v37, 0x10000, v17\nD0 6,32,46,0\nMaaa 108,204,252\nTa 152,18,32768\nv_add_u32 v38, 0x10000, v18\nMaaa 76,204,248\n"
"v_add_u32 v39, 0x10000, v19\nMaaa 44,204,244\nTa 154,18,34816\nMaaa 12,204,240\ns_add_u32 s2, s11, 1\nMvaa 172,204,236\nTa 156,19,32768\ns_lshr_b32 s2, s2, 1\nD0 7,32,46,1024\n"
"Mvaa 140,204,232\ns_lshl_b32 s2, s2, 12\nMvaa 108,204,228\nTa 158,19,34816\nMvaa 76,204,224\nv_add_u32 v44, s2, v10\nMvaa 80,208,224\nRa 160,10,32768\nv_add_u32 v45, s2, v11\n"
"Mvaa 112,208,228\nv_xor_b32 v46, 0x1000, v44\nD0 8,32,46,2048\nMvaa 144,208,232\nRa 164,10,34816\nMvaa 176,208,236\nv_xor_b32 v47, 0x1000, v45\nMaaa 16,208,240\nRa 168,10,40960\n"
"s_lshr_b32 s3, s11, 1\nMaaa 48,208,244\ns_lshl_b32 s3, s3, 13\nMaaa 80,208,248\nRa 172,10,43008\nD0 9,32,46,3072\nMaaa 112,208,252\ns_lshl_b32 s2, s11, 12\nMaaa 116,212,252\n"
"Ra 176,10,49152\ns_add_u32 s2, s2, s3\nMaaa 84,212,248\ns_add_u32 s2, s2, s72\ns_add_u32 m0, s47, 16384\ns_add_u32 s46, s51, 786432\nMaaa 52,212,244\nRa 180,10,51200\n"
"Maaa 20,212,240\ns_lshl_b32 s3, s11, 12\nD0 6,32,46,0\nMvaa 180,212,236\nRa 184,10,57344\ns_add_u32 s89, s3, 0x10000\nMvaa 148,212,232\ns_mov_b32 s88, 0xffff8000\n"
"Mvaa 116,212,228\nRa 188,10,59392\nMvaa 84,212,224\ns_lshl_b32 s4, s13, 1\nMvaa 88,216,224\ns_sub_u32 s79, 15, s4\ns_and_b32 s5, s12, 1\nD0 7,32,46,1024\nMvaa 120,216,228\n"
"s_mul_i32 s5, s5, 229376\nMvaa 152,216,232\ns_add_u32 s5, s5, s2\nMvaa 184,216,236\ns_add_u32 s96, s5, 0x1310000\nMaaa 24,216,240\ns_mov_b32 s63, 0x53800000\n"
"s_mov_b32 s64, 0x2b800000\nMaaa 56,216,244\ns_sub_u32 s98, s4, 1\nD0 8,32,46,2048\nMaaa 88,216,248\ns_lshl_b32 s5, s13, 5\nMaaa 120,216,252\ns_add_u32 s5, s5, s11\n"
"Maaa 124,220,252\ns_lshl_b32 s65, s5, 9\ns_add_u32 s65, s65, s72\nMaaa 92,220,248\ns_add_u32 s65, s65, 0x1580000\nMaaa 60,220,244\ns_and_b32 s66, s72, 0x2000\nD0 9,32,46,3072\n"
"Maaa 28,220,240\ns_add_u32 s66, s66, s56\nMvaa 188,220,236\ns_add_u32 s66, s66, 0x100000\ns_and_b32 s5, s12, 1\nMvaa 156,220,232\ns_mul_i32 s58, s5, 0x202000\nMvaa 124,220,228\n"
"s_xor_b32 s58, s58, s72\nMvaa 92,220,224\ns_waitcnt vmcnt(20) lgkmcnt(0)\ns_barrier\nMvaa 64,128,160\nTa 192,12,49152\ns_add_u32 s58, s58, 0x15c0000\nMvaa 96,128,164\n"
"s_lshl_b32 s5, s5, 8\ns_add_u32 m0, s47, -49152\ns_add_u32 s46, s52, 256\nMvaa 128,128,168\nTa 194,12,51200\nMvaa 160,128,172\ns_add_u32 s61, s58, s5\nD0 40,28,46,0\n"
"Maaa 0,128,176\nTa 196,13,49152\ns_lshl4_add_u32 s60, s13, s61\nMaaa 32,128,180\ns_add_u32 s54, s60, 128\nMaaa 64,128,184\nTa 198,13,51200\nMaaa 96,128,188\n"
"s_xor_b32 s60, s54, 0x100\ns_cmp_eq_u32 s11, 0\ns_cselect_b32 s54, s54, 0x40000000\nMaaa 100,132,188\nTa 200,14,49152\ns_getpc_b64 s[8:9]\n.PC248:\n"
"s_add_u32 s8, s8, .HD241-.PC248\ns_addc_u32 s9, s9, 0\nD0 41,28,46,1024\nMaaa 68,132,184\ns_mov_b32 s97, 0xff800000\nMaaa 36,132,180\nTa 202,14,51200\nMaaa 4,132,176\n"
"s_mov_b32 s90, 1\ns_cmp_lt_u32 s11, 2\ns_cselect_b32 s92, 2, 0\nMvaa 164,132,172\nTa 204,15,49152\ns_mul_i32 s4, s79, 65536\nMvaa 132,132,168\ns_sub_u32 s4, s2, s4\n"
"D0 42,28,46,2048\nMvaa 100,132,164\nTa 206,15,51200\nMvaa 68,132,160\ns_add_u32 s94, s4, 0x20000\nMvaa 72,136,160\nTa 208,16,49152\ns_add_u32 s95, s4, 0x410000\nMvaa 104,136,164\n"
"s_add_u32 s86, s2, 0x130000\nMvaa 136,136,168\nTa 210,16,51200\nD0 43,28,46,3072\nMvaa 168,136,172\ns_add_u32 s87, s2, 0x520000\nMaaa 8,136,176\nTa 212,17,49152\n"
"s_sub_u32 s99, s79, 2\nMaaa 40,136,180\ns_cmp_eq_u32 s13, 7\ns_cselect_b32 s93, 1.0, 0\ns_add_u32 m0, s47, 32768\ns_add_u32 s46, s51, 1048576\nMaaa 72,136,184\nTa 214,17,51200\n"
"Maaa 104,136,188\ns_mov_b32 s36, s68\nD0 6,32,46,0\nMaaa 108,140,188\nTa 216,18,49152\ns_and_b32 s37, s69, 0xffff\nMaaa 76,140,184\ns_mov_b32 s38, 0x1c0000\nMaaa 44,140,180\n"
"Ta 218,18,51200\nMaaa 12,140,176\ns_mov_b32 s39, 0x20000\nMvaa 172,140,172\nTa 220,19,49152\ns_and_b32 s2, s74, 15\nD0 7,32,46,1024\nMvaa 140,140,168\ns_max_u32 s2, s2, 2\n"
"Mvaa 108,140,164\nTa 222,19,51200\nMvaa 76,140,160\ns_sub_u32 s2, s2, 2\nMvaa 80,144,160\nRa 224,11,32768\ns_mul_i32 s3, s73, 14\nMvaa 112,144,164\ns_add_u32 s2, s2, s3\n"
"D0 8,32,46,2048\nMvaa 144,144,168\nRa 228,11,34816\nMvaa 176,144,172\ns_lshl2_add_u32 s2, s2, s11\nMaaa 16,144,176\nRa 232,11,40960\ns_mul_i32 s3, s2, 4682\nMaaa 48,144,180\n"
"s_lshr_b32 s3, s3, 16\nMaaa 80,144,184\nRa 236,11,43008\nD0 9,32,46,3072\nMaaa 112,144,188\ns_mul_i32 s22, s3, 14\nMaaa 116,148,188\nRa 240,11,49152\ns_sub_u32 s22, s2, s22\n"
"Maaa 84,148,184\ns_and_b32 s21, s3, 28\nMaaa 52,148,180\nRa 244,11,51200\nMaaa 20,148,176\ns_add_u32 s21, s21, s3\nMvaa 180,148,172\nRa 248,11,57344\ns_lshl_b32 s21, s21, 4\n"
"Mvaa 148,148,168\ns_bfe_u32 s41, s3, 0x30002\ns_cmp_gt_u32 s22, 6\ns_cselect_b32 s2, 1187840, 0\ns_cselect_b32 s1, 16, 0\nMvaa 116,148,164\nRa 252,11,59392\nMvaa 84,148,160\n"
"s_add_u32 s41, s41, s1\nMvaa 88,152,160\ns_lshl4_add_u32 s41, s41, 0x15c0280\ns_and_b32 s1, s3, 28\nMvaa 120,152,164\ns_add_u32 s3, s3, s1\nMvaa 152,152,168\n"
"s_lshl_b32 s3, s3, 11\nMvaa 184,152,172\ns_add_u32 s2, s2, s3\nMaaa 24,152,176\ns_lshl_b32 s3, s22, 17\ns_add_u32 s2, s2, s3\nMaaa 56,152,180\ns_add_u32 s20, s2, 0x1100000\n"
"Maaa 88,152,184\ns_lshl_b32 s2, s22, 7\nMaaa 120,152,188\ns_add_u32 s40, s2, 0xc04b800\nMaaa 124,156,188\ns_mul_i32 s2, s21, 896\ns_lshl_b32 s3, s22, 6\nMaaa 92,156,184\n"
"s_add_u32 s22, s2, s3\nMaaa 60,156,180\ns_add_u32 s23, s22, 57344\nMaaa 28,156,176\nMvaa 188,156,172\nMvaa 156,156,168\nMvaa 124,156,164\nMvaa 92,156,160\n"
"s_waitcnt vmcnt(16) lgkmcnt(0)\ns_barrier\nMvaa 64,192,224\ns_add_u32 m0, s47, -16384\ns_add_u32 s46, s52, 128\nMvaa 68,196,224\nMvaa 72,200,224\nD0 40,28,46,0\nMvaa 76,204,224\n"
"Mvaa 80,208,224\nMvaa 84,212,224\nMvaa 88,216,224\nD0 41,28,46,1024\nCV 192,64,65\nMvaa 92,220,224\nCV 193,66,67\nCV 194,68,69\nMvaa 96,192,228\nCV 195,70,71\nCV 196,72,73\n"
"Mvaa 100,196,228\nCV 197,74,75\nMvaa 104,200,228\nD0 42,28,46,2048\nCV 198,76,77\nMvaa 108,204,228\nCV 199,78,79\nCV 200,80,81\nMvaa 112,208,228\nCV 201,82,83\nCV 202,84,85\n"
"Mvaa 116,212,228\nCV 203,86,87\nMvaa 120,216,228\nD0 43,28,46,3072\nCV 204,88,89\nMvaa 124,220,228\nCV 205,90,91\nCV 206,92,93\nMvaa 128,192,232\ns_add_u32 m0, s47, 49152\n"
"s_add_u32 s46, s51, 1310720\nMvaa 132,196,232\nCV 207,94,95\nMvaa 136,200,232\nD0 6,32,46,0\nCV 208,96,97\nMvaa 140,204,232\nCV 209,98,99\nCV 210,100,101\nMvaa 144,208,232\n"
"CV 211,102,103\nCV 212,104,105\nMvaa 148,212,232\nCV 213,106,107\nMvaa 152,216,232\nD0 7,32,46,1024\nCV 214,108,109\nMvaa 156,220,232\nCV 215,110,111\nCV 216,112,113\n"
"Mvaa 160,192,236\nCV 217,114,115\nCV 218,116,117\nMvaa 164,196,236\nCV 219,118,119\nMvaa 168,200,236\nD0 8,32,46,2048\nCV 220,120,121\nMvaa 172,204,236\nCV 221,122,123\n"
"CV 222,124,125\nMvaa 176,208,236\nCV 223,126,127\nCV 224,128,129\nMvaa 180,212,236\nCV 225,130,131\nMvaa 184,216,236\nD0 9,32,46,3072\nCV 226,132,133\nMvaa 188,220,236\n"
"CV 227,134,135\nCV 228,136,137\nMaaa 0,192,240\ns_add_u32 m0, s47, -32768\ns_add_u32 s46, s52, 384\nMaaa 4,196,240\nCV 229,138,139\nMaaa 8,200,240\nD0 40,28,46,0\nCV 230,140,141\n"
"Maaa 12,204,240\nCV 231,142,143\nCV 232,144,145\nMaaa 16,208,240\nRa 160,44,0\nCV 233,146,147\nMaaa 20,212,240\nRa 164,44,2048\nMaaa 24,216,240\nD0 41,28,46,1024\nRa 168,46,0\n"
"Maaa 28,220,240\nRa 172,46,2048\nCV 234,148,149\nMaaa 32,192,244\nTa 128,32,0\nCV 235,150,151\nMaaa 36,196,244\nTa 130,32,2048\nMaaa 40,200,244\nD0 42,28,46,2048\nTa 132,33,0\n"
"Maaa 44,204,244\nTa 134,33,2048\nCV 236,152,153\nMaaa 48,208,244\nTa 136,34,0\nCV 237,154,155\nMaaa 52,212,244\nTa 138,34,2048\nMaaa 56,216,244\nD0 43,28,46,3072\nTa 140,35,0\n"
"Maaa 60,220,244\nTa 142,35,2048\nCV 238,156,157\nMaaa 64,192,248\nTa 144,36,0\nCV 239,158,159\nMaaa 68,196,248\nTa 146,36,2048\nMaaa 72,200,248\nTa 148,37,0\nCV 240,160,161\n"
"Sv 192,1,16,75,0\nMaaa 76,204,248\nTa 150,37,2048\nCV 241,162,163\nMaaa 80,208,248\nTa 152,38,0\nCV 242,164,165\nMaaa 84,212,248\nTa 154,38,2048\nMaaa 88,216,248\nTa 156,39,0\n"
"CV 243,166,167\nMaaa 92,220,248\nTa 158,39,2048\nCV 244,168,169\nMaaa 96,192,252\nCV 245,170,171\nCV 246,172,173\nSv 196,1,16,75,1024\nMaaa 100,196,252\nCV 247,174,175\n"
"Maaa 104,200,252\nCV 248,176,177\nCV 249,178,179\nMaaa 108,204,252\nCV 250,180,181\nCV 251,182,183\nMaaa 112,208,252\nCV 252,184,185\nCV 253,186,187\nMaaa 116,212,252\n"
"CV 254,188,189\nMaaa 120,216,252\nCV 255,190,191\nSv 200,1,16,75,2048\nMaaa 124,220,252\nWL 0\nZvaa 64,128,160\nRa 224,45,0\nZvaa 96,128,164\nRa 228,45,2048\nZvaa 128,128,168\n"
"Ra 232,47,0\nZvaa 160,128,172\nRa 236,47,2048\nSv 204,1,16,75,3072\ns_lshl3_add_u32 s5, s12, s13\ns_lshl2_add_u32 s5, s5, s11\ns_lshl2_add_u32 s5, s5, 0xc04a400\n"
"buffer_load_dword v27, off, s[16:19], s5 sc0 sc1\nZvaa 164,132,172\nTa 192,32,16384\nZvaa 132,132,168\nZvaa 100,132,164\nTa 194,32,18432\nZvaa 68,132,160\nTa 196,33,16384\n"
"Zvaa 72,136,160\nTa 198,33,18432\nZvaa 104,136,164\nTa 200,34,16384\ns_add_u32 s75, s75, s55\nSv 208,1,16,75,0\nZvaa 136,136,168\nTa 202,34,18432\nZvaa 168,136,172\n"
"Zvaa 172,140,172\nTa 204,35,16384\nZvaa 140,140,168\nTa 206,35,18432\nZvaa 108,140,164\nTa 208,36,16384\nZvaa 76,140,160\nTa 210,36,18432\nSv 212,1,16,75,1024\nZvaa 80,144,160\n"
"Ta 212,37,16384\nZvaa 112,144,164\nZvaa 144,144,168\nTa 214,37,18432\nZvaa 176,144,172\nTa 216,38,16384\nZvaa 180,148,172\nTa 218,38,18432\nZvaa 148,148,168\nTa 220,39,16384\n"
"Sv 216,1,16,75,2048\nZvaa 116,148,164\nTa 222,39,18432\nZvaa 84,148,160\nZvaa 88,152,160\nZvaa 120,152,164\nZvaa 152,152,168\nZvaa 184,152,172\nSv 220,1,16,75,3072\n"
"Zvaa 188,156,172\nZvaa 156,156,168\nZvaa 124,156,164\nZvaa 92,156,160\ns_waitcnt vmcnt(17) lgkmcnt(0)\ns_barrier\nMvaa 64,192,224\nRa 160,44,49152\nMvaa 96,192,228\n"
"Ra 164,44,51200\ns_add_u32 m0, s47, 65536\ns_add_u32 s46, s51, 1572864\nD0 6,32,46,0\nMvaa 128,192,232\nRa 168,46,49152\nMvaa 160,192,236\nRa 172,46,51200\nMvaa 164,196,236\n"
"Ta 128,12,0\nMvaa 132,196,232\nD0 7,32,46,1024\nMvaa 100,196,228\nTa 130,12,2048\nMvaa 68,196,224\nTa 132,13,0\nMvaa 72,200,224\nTa 134,13,2048\nMvaa 104,200,228\nTa 136,14,0\n"
"D0 8,32,46,2048\nMvaa 136,200,232\nTa 138,14,2048\nMvaa 168,200,236\nMvaa 172,204,236\nTa 140,15,0\nMvaa 140,204,232\nTa 142,15,2048\nD0 9,32,46,3072\nMvaa 108,204,228\n"
"Ta 144,16,0\nMvaa 76,204,224\nTa 146,16,2048\ns_add_u32 m0, s47, 81920\ns_add_u32 s46, s51, 1835008\nMvaa 80,208,224\nTa 148,17,0\nMvaa 112,208,228\nD0 6,32,46,0\n"
"Mvaa 144,208,232\nTa 150,17,2048\nMvaa 176,208,236\nTa 152,18,0\nMvaa 180,212,236\nTa 154,18,2048\nMvaa 148,212,232\nTa 156,19,0\nD0 7,32,46,1024\nMvaa 116,212,228\n"
"Ta 158,19,2048\nMvaa 84,212,224\nMvaa 88,216,224\nMvaa 120,216,228\nD0 8,32,46,2048\nMvaa 152,216,232\nMvaa 184,216,236\nMvaa 188,220,236\nMvaa 156,220,232\nD0 9,32,46,3072\n"
"Mvaa 124,220,228\nMvaa 92,220,224\nWL 0\nMvaa 64,128,160\nRa 224,45,49152\nMvaa 96,128,164\nRa 228,45,51200\ns_add_u32 m0, s47, -65536\ns_add_u32 s46, s52, 512\nD0 40,28,46,0\n"
"Mvaa 128,128,168\nRa 232,47,49152\nMvaa 160,128,172\nRa 236,47,51200\nMvaa 164,132,172\nTa 192,12,16384\nMvaa 132,132,168\nD0 41,28,46,1024\nMvaa 100,132,164\nTa 194,12,18432\n"
"Mvaa 68,132,160\nTa 196,13,16384\nMvaa 72,136,160\nTa 198,13,18432\nMvaa 104,136,164\nTa 200,14,16384\nD0 42,28,46,2048\nMvaa 136,136,168\nTa 202,14,18432\nMvaa 168,136,172\n"
"Mvaa 172,140,172\nTa 204,15,16384\nMvaa 140,140,168\nTa 206,15,18432\nD0 43,28,46,3072\nMvaa 108,140,164\nTa 208,16,16384\nSv 224,1,16,76,0\nMvaa 76,140,160\nTa 210,16,18432\n"
"Mvaa 80,144,160\nTa 212,17,16384\nMvaa 112,144,164\nMvaa 144,144,168\nTa 214,17,18432\nMvaa 176,144,172\nTa 216,18,16384\nSv 228,1,16,76,1024\nMvaa 180,148,172\nTa 218,18,18432\n"
"Mvaa 148,148,168\nTa 220,19,16384\nMvaa 116,148,164\nTa 222,19,18432\nMvaa 84,148,160\nMvaa 88,152,160\nSv 232,1,16,76,2048\nMvaa 120,152,164\nMvaa 152,152,168\nMvaa 184,152,172\n"
"Mvaa 188,156,172\nMvaa 156,156,168\nSv 236,1,16,76,3072\nMvaa 124,156,164\nMvaa 92,156,160\ns_waitcnt vmcnt(16) lgkmcnt(0)\ns_barrier\nMvaa 64,192,224\nRa 160,44,16384\n"
"v_cmp_ne_u32 vcc, s15, v27\ns_nop 0\ns_cbranch_vccnz .S260\n.S261:\nMvaa 96,192,228\nRa 164,44,18432\nbuffer_store_dword v25, off, s[16:19], s54 offset:0 sc0 sc1\n"
"s_add_u32 m0, s47, 0\ns_add_u32 s46, s51, 2097152\nD0 6,32,46,0\nMvaa 128,192,232\nRa 168,46,16384\nMvaa 160,192,236\nRa 172,46,18432\nMvaa 164,196,236\nTa 128,12,32768\n"
"Mvaa 132,196,232\nD0 7,32,46,1024\nMvaa 100,196,228\nTa 130,12,34816\nMvaa 68,196,224\nTa 132,13,32768\nMvaa 72,200,224\nTa 134,13,34816\nMvaa 104,200,228\nTa 136,14,32768\n"
"D0 8,32,46,2048\nMvaa 136,200,232\nTa 138,14,34816\nMvaa 168,200,236\nMvaa 172,204,236\nTa 140,15,32768\nMvaa 140,204,232\nTa 142,15,34816\nD0 9,32,46,3072\nMvaa 108,204,228\n"
"Ta 144,16,32768\nMvaa 76,204,224\nTa 146,16,34816\ns_add_u32 m0, s47, 16384\ns_add_u32 s46, s51, 2359296\nMvaa 80,208,224\nTa 148,17,32768\nMvaa 112,208,228\nD0 6,32,46,0\n"
"Mvaa 144,208,232\nTa 150,17,34816\nMvaa 176,208,236\nTa 152,18,32768\nMvaa 180,212,236\nTa 154,18,34816\nMvaa 148,212,232\nTa 156,19,32768\nD0 7,32,46,1024\nMvaa 116,212,228\n"
"Ta 158,19,34816\nMvaa 84,212,224\nMvaa 88,216,224\nMvaa 120,216,228\nD0 8,32,46,2048\nMvaa 152,216,232\nMvaa 184,216,236\nMvaa 188,220,236\nMvaa 156,220,232\nD0 9,32,46,3072\n"
"Mvaa 124,220,228\nMvaa 92,220,224\nWL 0\nMvaa 64,128,160\nRa 224,45,16384\nMvaa 96,128,164\nRa 228,45,18432\ns_add_u32 m0, s47, -16384\ns_add_u32 s46, s52, 640\nD0 40,28,46,0\n"
"Mvaa 128,128,168\nRa 232,47,16384\nMvaa 160,128,172\nRa 236,47,18432\nMvaa 164,132,172\nTa 192,12,49152\nMvaa 132,132,168\nD0 41,28,46,1024\nMvaa 100,132,164\nTa 194,12,51200\n"
"Mvaa 68,132,160\nTa 196,13,49152\nMvaa 72,136,160\nTa 198,13,51200\nMvaa 104,136,164\nTa 200,14,49152\nD0 42,28,46,2048\nMvaa 136,136,168\nTa 202,14,51200\nMvaa 168,136,172\n"
"Mvaa 172,140,172\nTa 204,15,49152\nMvaa 140,140,168\nTa 206,15,51200\nD0 43,28,46,3072\nMvaa 108,140,164\nTa 208,16,49152\ns_add_u32 s76, s76, s55\nSv 240,1,16,76,0\n"
"Mvaa 76,140,160\nTa 210,16,51200\nMvaa 80,144,160\nTa 212,17,49152\nMvaa 112,144,164\nMvaa 144,144,168\nTa 214,17,51200\nMvaa 176,144,172\nTa 216,18,49152\nSv 244,1,16,76,1024\n"
"Mvaa 180,148,172\nTa 218,18,51200\nMvaa 148,148,168\nTa 220,19,49152\nMvaa 116,148,164\nTa 222,19,51200\nMvaa 84,148,160\nMvaa 88,152,160\nSv 248,1,16,76,2048\nMvaa 120,152,164\n"
"Mvaa 152,152,168\nMvaa 184,152,172\nMvaa 188,156,172\nMvaa 156,156,168\nSv 252,1,16,76,3072\nMvaa 124,156,164\nMvaa 92,156,160\ns_waitcnt vmcnt(25) lgkmcnt(0)\ns_barrier\n"
"Mvaa 64,192,224\nRa 160,44,32768\nAR 60,0\nAR 61,1\nMvaa 96,192,228\nRa 164,44,34816\ns_add_u32 m0, s47, 32768\ns_add_u32 s46, s51, 2621440\nD0 6,32,46,0\nMvaa 128,192,232\n"
"Ra 168,46,32768\nCV 52,60,61\nMvaa 160,192,236\nRa 172,46,34816\nAR 60,2\nMvaa 164,196,236\nTa 128,32,0\nAR 61,3\nMvaa 132,196,232\nD0 7,32,46,1024\nCV 53,60,61\nMvaa 100,196,228\n"
"Ta 130,32,2048\nAR 60,4\nMvaa 68,196,224\nTa 132,33,0\nAR 61,5\nMvaa 72,200,224\nTa 134,33,2048\nCV 54,60,61\nMvaa 104,200,228\nTa 136,34,0\nD0 8,32,46,2048\nMvaa 136,200,232\n"
"Ta 138,34,2048\nAR 60,6\nMvaa 168,200,236\nAR 61,7\nCV 55,60,61\nMvaa 172,204,236\nTa 140,35,0\nAR 60,8\nMvaa 140,204,232\nTa 142,35,2048\nD0 9,32,46,3072\nMvaa 108,204,228\n"
"Ta 144,36,0\nAR 61,9\nMvaa 76,204,224\nTa 146,36,2048\ns_add_u32 m0, s47, 49152\ns_add_u32 s46, s51, 2883584\nMvaa 80,208,224\nTa 148,37,0\nCV 56,60,61\nAR 60,10\n"
"Mvaa 112,208,228\nD0 6,32,46,0\nAR 61,11\nMvaa 144,208,232\nTa 150,37,2048\nCV 57,60,61\nMvaa 176,208,236\nTa 152,38,0\nAR 60,12\nMvaa 180,212,236\nTa 154,38,2048\nAR 61,13\n"
"Mvaa 148,212,232\nTa 156,39,0\nD0 7,32,46,1024\nMvaa 116,212,228\nTa 158,39,2048\nCV 58,60,61\nMvaa 84,212,224\nAR 60,14\nAR 61,15\nMvaa 88,216,224\nCV 59,60,61\nAR 60,16\n"
"Mvaa 120,216,228\nD0 8,32,46,2048\nAR 61,17\nMvaa 152,216,232\nCV 20,60,61\nAR 60,18\nMvaa 184,216,236\nAR 61,19\nCV 21,60,61\nMvaa 188,220,236\nAR 60,20\nAR 61,21\n"
"Mvaa 156,220,232\nD0 9,32,46,3072\nCV 22,60,61\nMvaa 124,220,228\nAR 60,22\nAR 61,23\nMvaa 92,220,224\nWL 0\nMvaa 64,128,160\nRa 224,45,32768\nCV 23,60,61\nAR 60,24\n"
"Mvaa 96,128,164\nRa 228,45,34816\ns_add_u32 m0, s47, -49152\ns_add_u32 s46, s52, 768\nD0 40,28,46,0\nMvaa 128,128,168\nRa 232,47,32768\nAR 61,25\nMvaa 160,128,172\n"
"Ra 236,47,34816\nCV 2,60,61\nMvaa 164,132,172\nTa 192,32,16384\nAR 60,26\nMvaa 132,132,168\nD0 41,28,46,1024\nAR 61,27\nMvaa 100,132,164\nTa 194,32,18432\nCV 3,60,61\n"
"Mvaa 68,132,160\nTa 196,33,16384\nAR 60,28\nMvaa 72,136,160\nTa 198,33,18432\nAR 61,29\nMvaa 104,136,164\nTa 200,34,16384\nD0 42,28,46,2048\nMvaa 136,136,168\nTa 202,34,18432\n"
"CV 4,60,61\nMvaa 168,136,172\nAR 60,30\nAR 61,31\nMvaa 172,140,172\nTa 204,35,16384\nCV 5,60,61\nMvaa 140,140,168\nTa 206,35,18432\nD0 43,28,46,3072\nMvaa 108,140,164\n"
"Ta 208,36,16384\nSv 52,1,16,77,0\nMvaa 76,140,160\nTa 210,36,18432\nMvaa 80,144,160\nTa 212,37,16384\nMvaa 112,144,164\nMvaa 144,144,168\nTa 214,37,18432\nMvaa 176,144,172\n"
"Ta 216,38,16384\nSv 56,1,16,77,1024\nMvaa 180,148,172\nTa 218,38,18432\nMvaa 148,148,168\nTa 220,39,16384\nMvaa 116,148,164\nTa 222,39,18432\nMvaa 84,148,160\nMvaa 88,152,160\n"
"Sv 20,1,16,77,2048\nMvaa 120,152,164\nMvaa 152,152,168\nMvaa 184,152,172\nMvaa 188,156,172\nMvaa 156,156,168\nSv 2,1,16,77,3072\nMvaa 124,156,164\nMvaa 92,156,160\n"
"s_waitcnt vmcnt(24) lgkmcnt(0)\ns_barrier\nMvaa 64,192,224\nRa 160,44,0\nv_and_b32 v27, 0x70, v1\nbuffer_load_dword v27, v27, s[16:19], s61 offen offset:128 sc0 sc1\n"
"Mvaa 96,192,228\nRa 164,44,2048\ns_add_u32 m0, s47, 65536\ns_add_u32 s46, s51, 3145728\nD0 6,32,46,0\nMvaa 128,192,232\nRa 168,46,0\nAR 60,32\nMvaa 160,192,236\nRa 172,46,2048\n"
"AR 61,33\nMvaa 164,196,236\nTa 128,12,0\nCV 52,60,61\nMvaa 132,196,232\nD0 7,32,46,1024\nAR 60,34\nMvaa 100,196,228\nTa 130,12,2048\nAR 61,35\nMvaa 68,196,224\nTa 132,13,0\n"
"CV 53,60,61\nMvaa 72,200,224\nTa 134,13,2048\nAR 60,36\nAR 61,37\nMvaa 104,200,228\nTa 136,14,0\nD0 8,32,46,2048\nMvaa 136,200,232\nTa 138,14,2048\nCV 54,60,61\nMvaa 168,200,236\n"
"AR 60,38\nAR 61,39\nMvaa 172,204,236\nTa 140,15,0\nCV 55,60,61\nMvaa 140,204,232\nTa 142,15,2048\nD0 9,32,46,3072\nMvaa 108,204,228\nTa 144,16,0\nAR 60,40\nMvaa 76,204,224\n"
"Ta 146,16,2048\ns_add_u32 m0, s47, 81920\ns_add_u32 s46, s51, 3407872\nMvaa 80,208,224\nTa 148,17,0\nAR 61,41\nCV 56,60,61\nMvaa 112,208,228\nD0 6,32,46,0\nAR 60,42\n"
"Mvaa 144,208,232\nTa 150,17,2048\nAR 61,43\nMvaa 176,208,236\nTa 152,18,0\nCV 57,60,61\nMvaa 180,212,236\nTa 154,18,2048\nAR 60,44\nMvaa 148,212,232\nTa 156,19,0\nD0 7,32,46,1024\n"
"Mvaa 116,212,228\nTa 158,19,2048\nAR 61,45\nMvaa 84,212,224\nCV 58,60,61\nAR 60,46\nMvaa 88,216,224\nAR 61,47\nCV 59,60,61\nAR 60,48\nMvaa 120,216,228\nD0 8,32,46,2048\nAR 61,49\n"
"Mvaa 152,216,232\nCV 20,60,61\nAR 60,50\nMvaa 184,216,236\nAR 61,51\nCV 21,60,61\nMvaa 188,220,236\nAR 60,52\nAR 61,53\nMvaa 156,220,232\nD0 9,32,46,3072\nCV 22,60,61\n"
"Mvaa 124,220,228\nAR 60,54\nAR 61,55\nMvaa 92,220,224\nWL 0\nMvaa 64,128,160\nRa 224,45,0\nCV 23,60,61\nAR 60,56\nMvaa 96,128,164\nRa 228,45,2048\ns_add_u32 s6, s56, 0\n"
"buffer_load_dwordx4 v[192:195], v1, s[16:19], s6 offen offset:0 sc1\nMvaa 128,128,168\nRa 232,47,0\nAR 61,57\nMvaa 160,128,172\nRa 236,47,2048\nCV 2,60,61\nMvaa 164,132,172\n"
"Ta 192,12,16384\nAR 60,58\nMvaa 132,132,168\nbuffer_load_dwordx4 v[196:199], v1, s[16:19], s6 offen offset:1024 sc1\nAR 61,59\nMvaa 100,132,164\nTa 194,12,18432\nCV 3,60,61\n"
"Mvaa 68,132,160\nTa 196,13,16384\nAR 60,60\nMvaa 72,136,160\nTa 198,13,18432\nAR 61,61\nCV 4,60,61\nMvaa 104,136,164\nTa 200,14,16384\n"
"buffer_load_dwordx4 v[200:203], v1, s[16:19], s6 offen offset:2048 sc1\nMvaa 136,136,168\nTa 202,14,18432\nAR 60,62\nMvaa 168,136,172\nAR 61,63\nCV 5,60,61\nMvaa 172,140,172\n"
"Ta 204,15,16384\nMvaa 140,140,168\nTa 206,15,18432\nbuffer_load_dwordx4 v[204:207], v1, s[16:19], s6 offen offset:3072 sc1\nMvaa 108,140,164\nTa 208,16,16384\n"
"s_add_u32 s77, s77, s55\nSv 52,1,16,77,0\nMvaa 76,140,160\nTa 210,16,18432\nMvaa 80,144,160\nTa 212,17,16384\nMvaa 112,144,164\nMvaa 144,144,168\nTa 214,17,18432\n"
"Mvaa 176,144,172\nTa 216,18,16384\nSv 56,1,16,77,1024\nMvaa 180,148,172\nTa 218,18,18432\nMvaa 148,148,168\nTa 220,19,16384\nMvaa 116,148,164\nTa 222,19,18432\nMvaa 84,148,160\n"
"Mvaa 88,152,160\nSv 20,1,16,77,2048\nMvaa 120,152,164\nMvaa 152,152,168\nMvaa 184,152,172\nMvaa 188,156,172\nMvaa 156,156,168\nSv 2,1,16,77,3072\nMvaa 124,156,164\n"
"Mvaa 92,156,160\ns_waitcnt vmcnt(16) lgkmcnt(0)\ns_barrier\nMvaa 64,192,224\nRa 160,44,49152\nv_cmp_ne_u32 vcc, s15, v27\ns_nop 0\ns_cbranch_vccnz .S263\n.S264:\n"
"s_sub_u32 s45, s86, 131072\ns_cmp_eq_u32 s79, 1\ns_cselect_b32 s86, s94, s86\ns_add_u32 m0, s47, -65536\ns_add_u32 s46, s45, 0x0\nMvaa 96,192,228\nRa 164,44,51200\n"
"buffer_store_dword v25, off, s[16:19], s54 offset:8 sc0 sc1\nMvaa 128,192,232\nRa 168,46,49152\nD1 1,16,46,0\nMvaa 160,192,236\nRa 172,46,51200\nAR 60,126\nMvaa 164,196,236\n"
"Ta 128,12,32768\nAR 61,127\nMvaa 132,196,232\nCV 60,60,61\nAW 127,60\nMvaa 100,196,228\nTa 130,12,34816\nD1 1,16,46,1024\nMvaa 68,196,224\nTa 132,13,32768\nAR 60,124\n"
"Mvaa 72,200,224\nTa 134,13,34816\nAR 61,125\nMvaa 104,200,228\nTa 136,14,32768\nMvaa 136,200,232\nTa 138,14,34816\nD1 1,16,46,2048\nMvaa 168,200,236\nCV 60,60,61\nAW 126,60\n"
"Mvaa 172,204,236\nTa 140,15,32768\nAR 60,122\nMvaa 140,204,232\nTa 142,15,34816\nAR 61,123\nMvaa 108,204,228\nTa 144,16,32768\nD1 1,16,46,3072\nMvaa 76,204,224\nTa 146,16,34816\n"
"CV 60,60,61\nMvaa 80,208,224\nTa 148,17,32768\ns_add_u32 s6, s56, 4096\nMvaa 112,208,228\nAW 125,60\nMvaa 144,208,232\nTa 150,17,34816\n"
"buffer_load_dwordx4 v[208:211], v1, s[16:19], s6 offen offset:0 sc1\nMvaa 176,208,236\nTa 152,18,32768\nAR 60,120\nMvaa 180,212,236\nTa 154,18,34816\nAR 61,121\nMvaa 148,212,232\n"
"Ta 156,19,32768\nCV 60,60,61\nMvaa 116,212,228\nTa 158,19,34816\nbuffer_load_dwordx4 v[212:215], v1, s[16:19], s6 offen offset:1024 sc1\nMvaa 84,212,224\nAW 124,60\nAR 60,118\n"
"Mvaa 88,216,224\nAR 61,119\nCV 60,60,61\nMvaa 120,216,228\nAW 123,60\nMvaa 152,216,232\nbuffer_load_dwordx4 v[216:219], v1, s[16:19], s6 offen offset:2048 sc1\nAR 60,116\n"
"Mvaa 184,216,236\nAR 61,117\nCV 60,60,61\nMvaa 188,220,236\nAW 122,60\nAR 60,114\nMvaa 156,220,232\nAR 61,115\nCV 60,60,61\nMvaa 124,220,228\n"
"buffer_load_dwordx4 v[220:223], v1, s[16:19], s6 offen offset:3072 sc1\nAW 121,60\nMvaa 92,220,224\nWL 0\nMvaa 64,128,160\nRa 224,45,49152\ns_add_u32 s6, s56, 16384\n"
"Mvaa 96,128,164\nRa 228,45,51200\nMvaa 128,128,168\nRa 232,47,49152\nbuffer_load_dwordx4 v[224:227], v1, s[16:19], s6 offen offset:0 sc1\nMvaa 160,128,172\nRa 236,47,51200\n"
"AR 60,112\nMvaa 164,132,172\nTa 192,12,49152\nAR 61,113\nMvaa 132,132,168\nCV 60,60,61\nAW 120,60\nMvaa 100,132,164\nTa 194,12,51200\n"
"buffer_load_dwordx4 v[228:231], v1, s[16:19], s6 offen offset:1024 sc1\nMvaa 68,132,160\nTa 196,13,49152\nAR 60,110\nMvaa 72,136,160\nTa 198,13,51200\nAR 61,111\nMvaa 104,136,164\n"
"Ta 200,14,49152\nMvaa 136,136,168\nTa 202,14,51200\nbuffer_load_dwordx4 v[232:235], v1, s[16:19], s6 offen offset:2048 sc1\nMvaa 168,136,172\nCV 60,60,61\nAW 119,60\n"
"Mvaa 172,140,172\nTa 204,15,49152\nAR 60,108\nMvaa 140,140,168\nTa 206,15,51200\nAR 61,109\nMvaa 108,140,164\nTa 208,16,49152\n"
"buffer_load_dwordx4 v[236:239], v1, s[16:19], s6 offen offset:3072 sc1\nMvaa 76,140,160\nTa 210,16,51200\nCV 60,60,61\nMvaa 80,144,160\nTa 212,17,49152\nAW 118,60\n"
"Mvaa 112,144,164\nAR 60,106\nMvaa 144,144,168\nTa 214,17,51200\nAR 61,107\nMvaa 176,144,172\nTa 216,18,49152\nCV 60,60,61\nMvaa 180,148,172\nTa 218,18,51200\nAW 117,60\n"
"Mvaa 148,148,168\nTa 220,19,49152\nAR 60,104\nMvaa 116,148,164\nTa 222,19,51200\nAR 61,105\nMvaa 84,148,160\nCV 60,60,61\nAW 116,60\nMvaa 88,152,160\nAR 60,102\nAR 61,103\n"
"Mvaa 120,152,164\nCV 60,60,61\nMvaa 152,152,168\nAW 115,60\nAR 60,100\nMvaa 184,152,172\nAR 61,101\nCV 60,60,61\nMvaa 188,156,172\nAW 114,60\nAR 60,98\nMvaa 156,156,168\nAR 61,99\n"
"CV 60,60,61\nMvaa 124,156,164\nAW 113,60\nAR 60,96\nMvaa 92,156,160\ns_waitcnt vmcnt(21) lgkmcnt(0)\ns_barrier\nMvaa 64,192,224\nRa 160,44,16384\nAR 61,97\nMvaa 96,192,228\n"
"Ra 164,44,18432\ns_add_u32 s6, s56, 20480\nbuffer_load_dwordx4 v[240:243], v1, s[16:19], s6 offen offset:0 sc1\nMvaa 128,192,232\nRa 168,46,16384\nCV 60,60,61\nMvaa 160,192,236\n"
"Ra 172,46,18432\nAW 112,60\nMvaa 164,196,236\nTa 128,32,0\nAR 60,94\nMvaa 132,196,232\nbuffer_load_dwordx4 v[244:247], v1, s[16:19], s6 offen offset:1024 sc1\nAR 61,95\n"
"Mvaa 100,196,228\nTa 130,32,2048\nCV 60,60,61\nMvaa 68,196,224\nTa 132,33,0\nAW 111,60\nMvaa 72,200,224\nTa 134,33,2048\nAR 60,92\nMvaa 104,200,228\nTa 136,34,0\n"
"buffer_load_dwordx4 v[248:251], v1, s[16:19], s6 offen offset:2048 sc1\nMvaa 136,200,232\nTa 138,34,2048\nAR 61,93\nMvaa 168,200,236\nCV 60,60,61\nAW 110,60\nMvaa 172,204,236\n"
"Ta 140,35,0\nAR 60,90\nMvaa 140,204,232\nTa 142,35,2048\nbuffer_load_dwordx4 v[252:255], v1, s[16:19], s6 offen offset:3072 sc1\nMvaa 108,204,228\nTa 144,36,0\nAR 61,91\n"
"Mvaa 76,204,224\nTa 146,36,2048\ns_add_u32 m0, s47, -32768\ns_add_u32 s46, s45, 0x8000\nMvaa 80,208,224\nTa 148,37,0\nCV 60,60,61\nMvaa 112,208,228\nD1 1,16,46,0\n"
"Mvaa 144,208,232\nTa 150,37,2048\nAW 109,60\nMvaa 176,208,236\nTa 152,38,0\nAR 60,88\nMvaa 180,212,236\nTa 154,38,2048\nAR 61,89\nMvaa 148,212,232\nTa 156,39,0\nD1 1,16,46,1024\n"
"Mvaa 116,212,228\nTa 158,39,2048\nCV 60,60,61\nMvaa 84,212,224\nAW 108,60\nAR 60,86\nMvaa 88,216,224\nAR 61,87\nCV 60,60,61\nMvaa 120,216,228\nD1 1,16,46,2048\nMvaa 152,216,232\n"
"AW 107,60\nAR 60,84\nMvaa 184,216,236\nAR 61,85\nCV 60,60,61\nMvaa 188,220,236\nAW 106,60\nAR 60,82\nMvaa 156,220,232\nD1 1,16,46,3072\nAR 61,83\nMvaa 124,220,228\nCV 60,60,61\n"
"AW 105,60\nMvaa 92,220,224\nWL 0\nMvaa 64,128,160\nRa 224,45,16384\nAR 60,80\nMvaa 96,128,164\nRa 228,45,18432\ns_add_u32 m0, s47, -16384\ns_add_u32 s46, s45, 0x400000\n"
"D1 1,16,46,0\nMvaa 128,128,168\nRa 232,47,16384\nAR 61,81\nMvaa 160,128,172\nRa 236,47,18432\nCV 60,60,61\nMvaa 164,132,172\nTa 192,32,16384\nAW 104,60\nMvaa 132,132,168\n"
"D1 1,16,46,1024\nAR 60,78\nMvaa 100,132,164\nTa 194,32,18432\nAR 61,79\nMvaa 68,132,160\nTa 196,33,16384\nCV 60,60,61\nMvaa 72,136,160\nTa 198,33,18432\nAW 103,60\n"
"Mvaa 104,136,164\nTa 200,34,16384\nD1 1,16,46,2048\nMvaa 136,136,168\nTa 202,34,18432\nAR 60,76\nMvaa 168,136,172\nAR 61,77\nCV 60,60,61\nMvaa 172,140,172\nTa 204,35,16384\n"
"AW 102,60\nMvaa 140,140,168\nTa 206,35,18432\nD1 1,16,46,3072\nMvaa 108,140,164\nTa 208,36,16384\nAR 60,74\nMvaa 76,140,160\nTa 210,36,18432\nAR 61,75\nMvaa 80,144,160\n"
"Ta 212,37,16384\nCV 60,60,61\nMvaa 112,144,164\nAW 101,60\nMvaa 144,144,168\nTa 214,37,18432\nAR 60,72\nMvaa 176,144,172\nTa 216,38,16384\nAR 61,73\nMvaa 180,148,172\n"
"Ta 218,38,18432\nCV 60,60,61\nMvaa 148,148,168\nTa 220,39,16384\nAW 100,60\nMvaa 116,148,164\nTa 222,39,18432\nAR 60,70\nMvaa 84,148,160\nAR 61,71\nCV 60,60,61\nMvaa 88,152,160\n"
"AW 99,60\nAR 60,68\nMvaa 120,152,164\nAR 61,69\nMvaa 152,152,168\nCV 60,60,61\nAW 98,60\nMvaa 184,152,172\nAR 60,66\nAR 61,67\nMvaa 188,156,172\nCV 60,60,61\nAW 97,60\n"
"Mvaa 156,156,168\nAR 60,64\nAR 61,65\nMvaa 124,156,164\nCV 60,60,61\nAW 96,60\nMvaa 92,156,160\ns_waitcnt vmcnt(20) lgkmcnt(0)\ns_barrier\nMvaa 64,192,224\nv_add_u32 v2, 0x0, v1\n"
"s_lshr_b32 s2, s59, 1\nMvaa 68,196,224\ns_add_u32 m0, s47, 0\ns_add_u32 s46, s86, 0xffff0000\nD1 1,16,46,0\nbuffer_store_dword v25, off, s[16:19], s54 offset:12 sc0 sc1\n"
"Mvaa 72,200,224\nv_add_u32 v24, s2, v2\nMvaa 76,204,224\nv_xor_b32 v26, 0x1000, v24\nMvaa 80,208,224\nv_lshrrev_b32 v20, 4, v0\nv_lshlrev_b32 v20, 2, v20\nMvaa 84,212,224\n"
"D1 1,16,46,1024\nMvaa 88,216,224\nv_and_b32 v0, 15, v0\nv_mul_f32 v64, s57, v64\nMvaa 92,220,224\nv_sub_u32 v0, v0, v20\nv_mul_f32 v65, s57, v65\nCV 64,64,65\nMvaa 96,192,228\n"
"s_lshl_b32 s2, s11, 4\nv_add_u32 v3, s2, v0\nv_mul_f32 v66, s57, v66\nMvaa 100,196,228\nD1 1,16,46,2048\nv_mul_f32 v67, s57, v67\nCV 65,66,67\nMvaa 104,200,228\n"
"v_subrev_u32 v0, 32, v3\nv_mul_f32 v68, s57, v68\nv_mul_f32 v69, s57, v69\nMvaa 108,204,228\nv_mov_b32 v4, 0\nv_mov_b32 v5, 0\nMvaa 112,208,228\nRa 128,24,0\nv_mov_b32 v6, 0\n"
"CV 66,68,69\nMvaa 116,212,228\nD1 1,16,46,3072\nv_mul_f32 v70, s57, v70\nv_mul_f32 v71, s57, v71\nMvaa 120,216,228\nRa 132,24,1024\nv_mov_b32 v7, 0\nCV 67,70,71\nMvaa 124,220,228\n"
"s_add_u32 m0, s47, 16384\ns_add_u32 s46, s45, 0x408000\nv_mul_f32 v72, s57, v72\nMvaa 128,192,232\nRa 136,24,2048\nv_mov_b32 v8, 0\nv_mul_f32 v73, s57, v73\nMvaa 132,196,232\n"
"D1 1,16,46,0\nCV 72,72,73\nMvaa 136,200,232\nRa 140,24,3072\nv_mov_b32 v9, 0\nv_mul_f32 v74, s57, v74\nMvaa 140,204,232\nv_mov_b32 v10, 0\nv_mov_b32 v11, 0\n"
"v_mul_f32 v75, s57, v75\nMvaa 144,208,232\nRa 144,26,0\nv_mov_b32 v12, 0\nCV 73,74,75\nMvaa 148,212,232\nD1 1,16,46,1024\nv_mul_f32 v76, s57, v76\nv_mul_f32 v77, s57, v77\n"
"Mvaa 152,216,232\nRa 148,26,1024\nv_mov_b32 v13, 0\nMvaa 156,220,232\nv_mov_b32 v14, 0\nv_mov_b32 v15, 0\nCV 74,76,77\nMvaa 160,192,236\nRa 152,26,2048\nv_mov_b32 v16, 0\n"
"v_mul_f32 v78, s57, v78\nMvaa 164,196,236\nD1 1,16,46,2048\nv_mul_f32 v79, s57, v79\nCV 75,78,79\nMvaa 168,200,236\nRa 156,26,3072\nv_mov_b32 v17, 0\nv_mul_f32 v80, s57, v80\n"
"Mvaa 172,204,236\nv_mov_b32 v18, 0\nv_mov_b32 v19, 0\nMvaa 176,208,236\ns_lshl_b32 s2, s11, 13\ns_add_u32 s2, s2, 0x20000\nv_mul_f32 v81, s57, v81\nMvaa 180,212,236\n"
"D1 1,16,46,3072\nCV 80,80,81\nv_mul_f32 v82, s57, v82\nMvaa 184,216,236\nv_add_u32 v22, s2, v1\nv_xor_b32 v23, 0x2000, v22\nv_mul_f32 v83, s57, v83\nMvaa 188,220,236\n"
"s_waitcnt vmcnt(25) lgkmcnt(4)\ns_cmp_eq_u32 s79, 1\ns_cbranch_scc1 .E267\n.E268:\nCvav 32,128,192,4\nRa 160,24,8192\nCV 81,82,83\nv_mul_f32 v84, s57, v84\nMvav 32,132,200\n"
"v_mul_f32 v85, s57, v85\nCV 82,84,85\nMvav 32,136,208\nRa 164,24,9216\nv_mul_f32 v86, s57, v86\nv_mul_f32 v87, s57, v87\nMvav 32,140,216\nCV 83,86,87\nv_mul_f32 v88, s57, v88\n"
"v_mul_f32 v89, s57, v89\nCvav 36,140,220,8\nRa 168,24,10240\nCV 88,88,89\nv_mul_f32 v90, s57, v90\nMvav 36,136,212\ns_add_u32 m0, s89, 32768\nv_mul_f32 v91, s57, v91\nCV 89,90,91\n"
"Mvav 36,132,204\nRa 172,24,11264\ns_waitcnt vmcnt(17) lgkmcnt(4)\ns_barrier\nMvav 36,128,196\nv_mul_f32 v92, s57, v92\nv_mul_f32 v93, s57, v93\nCV 90,92,93\nMvav 36,144,228\n"
"Ra 176,26,8192\nv_mul_f32 v94, s57, v94\nv_mul_f32 v95, s57, v95\nSa 96,1,16,78,0\nMvav 36,148,236\ns_add_u32 s46, s86, s88\nCV 91,94,95\nAW 192,64\nMvav 36,152,244\n"
"Ra 180,26,9216\nAW 193,65\nAW 194,66\nMvav 36,156,252\nAW 195,67\nAW 200,72\nMvav 32,156,248\nRa 184,26,10240\nAW 201,73\nAW 202,74\nMvav 32,152,240\nD1 1,16,46,0\nAW 203,75\n"
"AW 208,80\nMvav 32,148,232\nRa 188,26,11264\nWL 4\nAW 209,81\nMvav 32,144,224\nAW 210,82\nAW 211,83\nAW 216,88\nCvav 40,160,192,12\nAW 217,89\nAW 218,90\nAW 219,91\n"
"Sa 100,1,16,78,1024\nMvav 40,164,200\ns_cmp_eq_u32 s99, -1\nMvav 40,168,208\ns_cselect_b32 s87, s95, s87\nv_mov_b32 v86, 0\nv_mov_b32 v87, 0\nMvav 40,172,216\nv_mov_b32 v88, 0\n"
"v_mov_b32 v89, 0\nv_mul_f32 v128, s57, v128\nv_mul_f32 v129, s57, v129\nCV 128,128,129\nv_mul_f32 v130, s57, v130\nv_mul_f32 v131, s57, v131\nCvav 44,172,220,16\n"
"v_and_b32 v27, 0x70, v1\nbuffer_load_dword v27, v27, s[16:19], s61 offen offset:136 sc0 sc1\nCV 129,130,131\nv_mul_f32 v132, s57, v132\nv_mul_f32 v133, s57, v133\nCV 130,132,133\n"
"v_mul_f32 v134, s57, v134\nv_mul_f32 v135, s57, v135\nMvav 44,168,212\nD1 1,16,46,1024\nCV 131,134,135\nds_write_b128 v23, v[128:131] offset:0\nv_mul_f32 v136, s57, v136\n"
"v_mul_f32 v137, s57, v137\nCV 136,136,137\nv_mul_f32 v138, s57, v138\nMvav 44,164,204\nWL 1\nv_mul_f32 v139, s57, v139\nCV 137,138,139\nv_mul_f32 v140, s57, v140\n"
"v_mul_f32 v141, s57, v141\nCV 138,140,141\nv_mul_f32 v142, s57, v142\nv_mul_f32 v143, s57, v143\nMvav 44,160,196\nCV 139,142,143\nds_write_b128 v23, v[136:139] offset:2048\n"
"v_mul_f32 v144, s57, v144\nv_mul_f32 v145, s57, v145\nCV 144,144,145\nv_mul_f32 v146, s57, v146\nv_mul_f32 v147, s57, v147\nMvav 44,176,228\nCV 145,146,147\n"
"v_mul_f32 v148, s57, v148\nv_mul_f32 v149, s57, v149\nCV 146,148,149\nv_mul_f32 v150, s57, v150\nv_mul_f32 v151, s57, v151\nCV 147,150,151\n"
"ds_write_b128 v23, v[144:147] offset:4096\nSa 104,1,16,78,2048\nMvav 44,180,236\ns_add_u32 s88, s88, 32768\nEX 80,32\nv_mul_f32 v152, s57, v152\nv_mul_f32 v153, s57, v153\n"
"CV 152,152,153\nv_mul_f32 v154, s57, v154\nv_mul_f32 v155, s57, v155\nMvav 44,184,244\nCV 153,154,155\nv_mul_f32 v156, s57, v156\nv_mul_f32 v157, s57, v157\nCV 154,156,157\n"
"v_mul_f32 v158, s57, v158\nv_mul_f32 v159, s57, v159\nCV 155,158,159\nMvav 44,188,252\nEX 81,33\nds_write_b128 v23, v[152:155] offset:6144\nv_mul_f32 v160, s57, v160\n"
"v_mul_f32 v161, s57, v161\nCV 160,160,161\nv_mul_f32 v162, s57, v162\nMvav 40,188,248\nv_mul_f32 v163, s57, v163\nCV 161,162,163\nv_mul_f32 v164, s57, v164\n"
"v_mul_f32 v165, s57, v165\nCV 162,164,165\nv_mul_f32 v166, s57, v166\nv_mul_f32 v167, s57, v167\nMvav 40,184,240\nD1 1,16,46,2048\nCV 163,166,167\n"
"ds_write_b128 v23, v[160:163] offset:1024\nv_mul_f32 v168, s57, v168\nv_mul_f32 v169, s57, v169\nCV 168,168,169\nv_mul_f32 v170, s57, v170\nv_mul_f32 v171, s57, v171\n"
"Mvav 40,180,232\nCV 169,170,171\nv_mul_f32 v172, s57, v172\nv_mul_f32 v173, s57, v173\nCV 170,172,173\nv_mul_f32 v174, s57, v174\nv_mul_f32 v175, s57, v175\nCV 171,174,175\n"
"Mvav 40,176,224\nAD 84,80,81\nCV 64,80,81\nds_write_b128 v23, v[168:171] offset:3072\nv_mul_f32 v176, s57, v176\nv_mul_f32 v177, s57, v177\nCV 176,176,177\n"
"v_mul_f32 v178, s57, v178\nv_mul_f32 v179, s57, v179\nCV 177,178,179\nv_mul_f32 v180, s57, v180\nv_mul_f32 v181, s57, v181\nCV 178,180,181\nv_mul_f32 v182, s57, v182\n"
"v_mul_f32 v183, s57, v183\nCV 179,182,183\nds_write_b128 v23, v[176:179] offset:5120\nSa 108,1,16,78,3072\nEX 82,34\nv_mul_f32 v184, s57, v184\nv_mul_f32 v185, s57, v185\n"
"CV 184,184,185\nv_mul_f32 v186, s57, v186\nv_mul_f32 v187, s57, v187\nCV 185,186,187\nv_mul_f32 v188, s57, v188\nv_mul_f32 v189, s57, v189\nCV 186,188,189\n"
"v_mul_f32 v190, s57, v190\nv_mul_f32 v191, s57, v191\nCV 187,190,191\nds_write_b128 v23, v[184:187] offset:7168\ns_waitcnt vmcnt(17) lgkmcnt(0)\nEX 83,35\nAD 84,84,82\n"
"D1 1,16,46,3072\ns_add_u32 m0, s89, 49152\ns_barrier\nAD 84,84,83\nCV 65,82,83\nRa 128,24,32768\nRa 224,22,0\nRa 228,22,1024\nRa 232,22,2048\ns_add_u32 s46, s87, s88\nEX 80,36\n"
"Ra 132,24,33792\nRa 236,22,3072\nRa 240,22,4096\nRa 244,22,5120\nEX 81,37\nRa 248,22,6144\nRa 136,24,34816\nRa 252,22,7168\nv_mul_f32 v96, s57, v96\nv_mul_f32 v97, s57, v97\n"
"D1 1,16,46,0\nCV 96,96,97\nv_mul_f32 v98, s57, v98\nRa 140,24,35840\nv_mul_f32 v99, s57, v99\nCV 97,98,99\nv_mul_f32 v100, s57, v100\nAD 85,80,81\nCV 68,80,81\n"
"v_mul_f32 v101, s57, v101\nCV 98,100,101\nRa 144,26,32768\nv_mul_f32 v102, s57, v102\nv_mul_f32 v103, s57, v103\nCV 99,102,103\ns_cmp_eq_u32 s99, -1\nEX 82,38\nRa 148,26,33792\n"
"s_cbranch_scc1 .E250\n.E251:\nAW 196,96\nAW 197,97\nEX 83,39\nAW 198,98\nRa 152,26,34816\nAD 85,85,82\nAW 199,99\nv_mul_f32 v104, s57, v104\nD1 1,16,46,1024\n"
"v_mul_f32 v105, s57, v105\nCV 104,104,105\nRa 156,26,35840\nv_mul_f32 v106, s57, v106\nv_mul_f32 v107, s57, v107\nCV 105,106,107\nAD 85,85,83\nCV 69,82,83\n"
"v_mul_f32 v108, s57, v108\nv_mul_f32 v109, s57, v109\nRa 160,24,40960\nCV 106,108,109\nv_mul_f32 v110, s57, v110\nv_mul_f32 v111, s57, v111\ns_xor_b32 s89, s89, 0x10000\nEX 80,40\n"
"Ra 164,24,41984\nCV 107,110,111\nAW 204,104\nAW 205,105\nWL 6\nEX 81,41\nAW 206,106\nRa 168,24,43008\nAD 84,84,80\nAW 207,107\nv_mul_f32 v112, s57, v112\nD1 1,16,46,2048\n"
"v_mul_f32 v113, s57, v113\nCV 112,112,113\nRa 172,24,44032\nv_mul_f32 v114, s57, v114\nv_mul_f32 v115, s57, v115\nCV 113,114,115\nAD 84,84,81\nCV 66,80,81\n"
"v_mul_f32 v116, s57, v116\nv_mul_f32 v117, s57, v117\nCvav 48,128,192,4\nRa 176,26,40960\nCV 114,116,117\nv_mul_f32 v118, s57, v118\nv_mul_f32 v119, s57, v119\nMvav 48,132,200\n"
"s_cmp_eq_u32 s99, 0\nEX 82,42\nMvav 48,136,208\nRa 180,26,41984\ns_cselect_b32 s86, s94, s86\nCV 115,118,119\nAW 212,112\nMvav 48,140,216\nWL 6\nEX 83,43\nAW 213,113\nWV 8\n"
"v_cmp_ne_u32 vcc, s15, v27\ns_nop 0\ns_cbranch_vccnz .S269\n.S270:\nCvav 52,140,220,8\nRa 184,26,43008\nAD 84,84,82\nAW 214,114\nAW 215,115\nMvav 52,136,212\nD1 1,16,46,3072\n"
"s_add_u32 m0, s89, 0\nv_mul_f32 v120, s57, v120\nMvav 52,132,204\nRa 188,26,44032\nv_mul_f32 v121, s57, v121\nCV 120,120,121\nv_mul_f32 v122, s57, v122\nMvav 52,128,196\n"
"AD 84,84,83\nCV 67,82,83\nv_mul_f32 v123, s57, v123\nCV 121,122,123\nMvav 52,144,228\nRa 128,2,49152\nv_mul_f32 v124, s57, v124\nv_mul_f32 v125, s57, v125\nCV 122,124,125\n"
"s_add_u32 s78, s78, s55\nSa 112,1,16,78,0\nMvav 52,148,236\ns_add_u32 s46, s86, s88\nEX 80,44\nMvav 52,152,244\nRa 132,2,50176\nv_xor_b32 v24, 0x10000, v24\n"
"v_mul_f32 v126, s57, v126\nv_mul_f32 v127, s57, v127\nMvav 52,156,252\nWL 6\nEX 81,45\nCV 123,126,127\nMvav 48,156,248\nRa 136,2,51200\nAD 85,85,80\nAW 220,120\nAW 221,121\n"
"Mvav 48,152,240\nD1 1,16,46,0\nAW 222,122\nAW 223,123\nMvav 48,148,232\nRa 140,2,52224\nv_mov_b32 v112, -1\nv_mov_b32 v113, -1\nv_mov_b32 v114, -1\nMvav 48,144,224\nAD 85,85,81\n"
"CV 70,80,81\nv_mov_b32 v115, -1\nCvav 56,160,192,12\nRa 144,2,53248\nSa 116,1,16,78,1024\nMvav 56,164,200\nEX 82,46\nMvav 56,168,208\nRa 148,2,54272\nv_xor_b32 v26, 0x10000, v26\n"
"Mvav 56,172,216\nWL 6\nEX 83,47\nCvav 60,172,220,16\nRa 152,2,55296\nAD 85,85,82\nMvav 60,168,212\nD1 1,16,46,1024\nMvav 60,164,204\nRa 156,2,56320\nMvav 60,160,196\nAD 85,85,83\n"
"CV 71,82,83\nMvav 60,176,228\nRa 160,2,57344\nv_max_f32 v90, v84, v85\nMvav 60,180,236\ns_add_u32 s88, s88, 32768\nEX 80,48\nMvav 60,184,244\nRa 164,2,58368\n"
"v_cmp_lt_f32 vcc, s63, v90\nMvav 60,188,252\nWL 8\nEX 81,49\nMvav 56,188,248\nRa 168,2,59392\ns_cbranch_vccnz .T256\n.T257:\nv_add_f32 v20, s93, v84\nv_mov_b32 v21, v85\n"
"v_mov_b32 v22, v20\nv_mov_b32 v23, v21\nMvav 56,184,240\nD1 1,16,46,2048\nv_permlane32_swap_b32 v20, v22\nv_permlane32_swap_b32 v21, v23\nAD 20,20,22\nAD 21,21,23\n"
"v_mov_b32 v22, v20\nv_mov_b32 v23, v21\nMvav 56,180,232\nRa 172,2,60416\nv_permlane16_swap_b32 v20, v22\nv_permlane16_swap_b32 v21, v23\nAD 20,20,22\nAD 21,21,23\n"
"v_min_f32 v90, v20, v21\nv_cmp_gt_f32 vcc, s64, v90\ns_nop 0\ns_cbranch_vccnz .T258\n.T259:\nAD 86,86,84\nMvav 56,176,224\nAD 84,80,81\nCV 72,80,81\nZaav 0,128,64\nRa 176,2,61440\n"
"Sa 120,1,16,78,2048\nZaav 4,128,68\nEX 82,50\nZaav 12,132,68\nRa 180,2,62464\nZaav 8,132,64\nWL 8\nEX 83,51\nZaav 16,136,64\nRa 184,2,63488\nAD 84,84,82\nZaav 20,136,68\n"
"D1 1,16,46,3072\ns_add_u32 m0, s89, 16384\nZaav 28,140,68\nRa 188,2,64512\ns_barrier\nZaav 24,140,64\nAD 84,84,83\nCV 73,82,83\nZaav 32,144,64\nRa 128,24,0\nSa 124,1,16,78,3072\n"
"Zaav 36,144,68\ns_add_u32 s46, s87, s88\nEX 80,52\nZaav 44,148,68\nRa 132,24,1024\nv_xor_b32 v2, 0x10000, v2\nZaav 40,148,64\nWL 8\nEX 81,53\nZaav 48,152,64\nRa 136,24,2048\n"
"AD 87,87,85\nZaav 52,152,68\nD1 1,16,46,0\nZaav 60,156,68\nRa 140,24,3072\nZaav 56,156,64\nAD 85,80,81\nCV 76,80,81\nZaav 64,160,64\nRa 144,26,0\nZaav 68,160,68\n"
"s_cmp_lt_u32 s99, s92\nEX 82,54\nZaav 76,164,68\nRa 148,26,1024\ns_cbranch_scc1 .E252\n.E253:\nZaav 72,164,64\nWL 8\nEX 83,55\nZaav 80,168,64\nRa 152,26,2048\nAD 85,85,82\n"
"Zaav 84,168,68\nD1 1,16,46,1024\nZaav 92,172,68\nRa 156,26,3072\nZaav 88,172,64\nAD 85,85,83\nCV 77,82,83\nZaav 96,176,64\nRa 160,24,8192\nZaav 100,176,68\ns_sub_u32 s90, s90, 1\n"
"EX 80,56\nZaav 108,180,68\nRa 164,24,9216\ns_cbranch_scc1 .E254\n.E255:\nZaav 104,180,64\nWL 6\nEX 81,57\nZaav 112,184,64\nRa 168,24,10240\nAD 84,84,80\nv_and_b32 v27, 0x70, v1\n"
"buffer_load_dword v27, v27, s[16:19], s61 offen offset:140 sc0 sc1\nZaav 116,184,68\nD1 1,16,46,2048\nZaav 124,188,68\nRa 172,24,11264\nZaav 120,188,64\nAD 84,84,81\nCV 74,80,81\n"
"s_sub_u32 s99, s99, 1\ns_cmp_eq_u32 s13, 7\ns_cbranch_scc1 .SW246\n.p2align 6\n.A272:\nCvav 32,128,192,4\nRa 176,26,8192\nMvav 32,132,200\nEX 82,58\nMvav 32,136,208\n"
"Ra 180,26,9216\nMvav 32,140,216\nWL 6\nEX 83,59\nCvav 36,140,220,8\nRa 184,26,10240\nAD 84,84,82\nMvav 36,136,212\nD1 1,16,46,3072\ns_add_u32 m0, s89, 32768\nMvav 36,132,204\n"
"Ra 188,26,11264\nMvav 36,128,196\nAD 84,84,83\nCV 75,82,83\nMvav 36,144,228\nRa 128,2,16384\nMvav 36,148,236\ns_add_u32 s46, s86, s88\nEX 80,60\nMvav 36,152,244\nRa 132,2,17408\n"
"Mvav 36,156,252\nWL 6\nEX 81,61\nMvav 32,156,248\nRa 136,2,18432\nAD 85,85,80\nMvav 32,152,240\nD1 1,16,46,0\nMvav 32,148,232\nRa 140,2,19456\nSv 112,1,16,49,0\nMvav 32,144,224\n"
"AD 85,85,81\nCV 78,80,81\nCvav 40,160,192,12\nRa 144,2,20480\nMvav 40,164,200\nEX 82,62\nMvav 40,168,208\nRa 148,2,21504\nMvav 40,172,216\nWL 6\nEX 83,63\nCvav 44,172,220,16\n"
"Ra 152,2,22528\nAD 85,85,82\nMvav 44,168,212\nD1 1,16,46,1024\nMvav 44,164,204\nRa 156,2,23552\nMvav 44,160,196\nAD 85,85,83\nCV 79,82,83\nMvav 44,176,228\nRa 160,2,24576\n"
"v_max_f32 v90, v84, v85\nMvav 44,180,236\ns_add_u32 s88, s88, 32768\nEX 80,32\nMvav 44,184,244\nRa 164,2,25600\nv_cmp_lt_f32 vcc, s63, v90\nMvav 44,188,252\nWL 8\nEX 81,33\n"
"Mvav 40,188,248\nRa 168,2,26624\ns_cbranch_vccnz .T280\n.T281:\nMvav 40,184,240\nD1 1,16,46,2048\nMvav 40,180,232\nRa 172,2,27648\nAD 86,86,84\nMvav 40,176,224\nAD 84,80,81\n"
"CV 64,80,81\nMaav 0,128,72\nRa 176,2,28672\nMaav 4,128,76\nEX 82,34\nMaav 12,132,76\nRa 180,2,29696\nMaav 8,132,72\ns_waitcnt vmcnt(13) lgkmcnt(8)\nEX 83,35\nMaav 16,136,72\n"
"Ra 184,2,30720\nAD 84,84,82\nMaav 20,136,76\nD1 1,16,46,3072\ns_add_u32 m0, s89, 49152\nMaav 28,140,76\nRa 188,2,31744\ns_barrier\nMaav 24,140,72\nAD 84,84,83\nCV 65,82,83\n"
"Maav 32,144,72\nRa 128,24,32768\nMaav 36,144,76\ns_add_u32 s46, s87, s88\nEX 80,36\nMaav 44,148,76\nRa 132,24,33792\nMaav 40,148,72\nWL 8\nEX 81,37\nMaav 48,152,72\n"
"Ra 136,24,34816\nAD 87,87,85\nMaav 52,152,76\nD1 1,16,46,0\nMaav 60,156,76\nRa 140,24,35840\nMaav 56,156,72\nAD 85,80,81\nCV 68,80,81\nMaav 64,160,72\nRa 144,26,32768\n"
"Maav 68,160,76\ns_cmp_eq_u32 s99, 0\nEX 82,38\nMaav 76,164,76\nRa 148,26,33792\ns_cbranch_scc1 .E274\n.E275:\nMaav 72,164,72\nWL 8\nEX 83,39\nMaav 80,168,72\nRa 152,26,34816\n"
"AD 85,85,82\nMaav 84,168,76\nD1 1,16,46,1024\nMaav 92,172,76\nRa 156,26,35840\nMaav 88,172,72\nAD 85,85,83\nCV 69,82,83\nMaav 96,176,72\nRa 160,24,40960\nMaav 100,176,76\n"
"s_xor_b32 s89, s89, 0x10000\nEX 80,40\nMaav 108,180,76\nRa 164,24,41984\nMaav 104,180,72\nWL 6\nEX 81,41\nMaav 112,184,72\nRa 168,24,43008\nAD 84,84,80\nMaav 116,184,76\n"
"D1 1,16,46,2048\nMaav 124,188,76\nRa 172,24,44032\nMaav 120,188,72\nAD 84,84,81\nCV 66,80,81\nCvav 48,128,192,4\nRa 176,26,40960\nMvav 48,132,200\ns_cmp_eq_u32 s99, 0\nEX 82,42\n"
"Mvav 48,136,208\nRa 180,26,41984\ns_cselect_b32 s86, s94, s86\nMvav 48,140,216\nWL 6\nEX 83,43\nCvav 52,140,220,8\nRa 184,26,43008\nAD 84,84,82\nMvav 52,136,212\nD1 1,16,46,3072\n"
"s_add_u32 m0, s89, 0\nMvav 52,132,204\nRa 188,26,44032\nMvav 52,128,196\nAD 84,84,83\nCV 67,82,83\nMvav 52,144,228\nRa 128,2,49152\nMvav 52,148,236\ns_add_u32 s46, s86, s88\n"
"EX 80,44\nMvav 52,152,244\nRa 132,2,50176\nv_xor_b32 v24, 0x10000, v24\nMvav 52,156,252\nWL 6\nEX 81,45\nMvav 48,156,248\nRa 136,2,51200\nAD 85,85,80\nMvav 48,152,240\n"
"D1 1,16,46,0\nMvav 48,148,232\nRa 140,2,52224\nSv 112,1,16,49,1024\nMvav 48,144,224\nAD 85,85,81\nCV 70,80,81\nCvav 56,160,192,12\nRa 144,2,53248\nMvav 56,164,200\n"
"s_add_u32 s49, s49, 131072\nEX 82,46\nMvav 56,168,208\nRa 148,2,54272\nv_xor_b32 v26, 0x10000, v26\nMvav 56,172,216\nWL 6\nEX 83,47\nCvav 60,172,220,16\nRa 152,2,55296\n"
"AD 85,85,82\nMvav 60,168,212\nD1 1,16,46,1024\nMvav 60,164,204\nRa 156,2,56320\nMvav 60,160,196\nAD 85,85,83\nCV 71,82,83\nMvav 60,176,228\nRa 160,2,57344\n"
"v_max_f32 v90, v84, v85\nMvav 60,180,236\ns_add_u32 s88, s88, 32768\nEX 80,48\nMvav 60,184,244\nRa 164,2,58368\nv_cmp_lt_f32 vcc, s63, v90\nMvav 60,188,252\nWL 8\nEX 81,49\n"
"Mvav 56,188,248\nRa 168,2,59392\ns_cbranch_vccnz .T282\n.T283:\nMvav 56,184,240\nD1 1,16,46,2048\nMvav 56,180,232\nRa 172,2,60416\nAD 86,86,84\nMvav 56,176,224\nAD 84,80,81\n"
"CV 72,80,81\nMaav 0,128,64\nRa 176,2,61440\nMaav 4,128,68\nEX 82,50\nMaav 12,132,68\nRa 180,2,62464\nMaav 8,132,64\ns_waitcnt vmcnt(13) lgkmcnt(8)\nEX 83,51\nMaav 16,136,64\n"
"Ra 184,2,63488\nAD 84,84,82\nMaav 20,136,68\nD1 1,16,46,3072\ns_add_u32 m0, s89, 16384\nMaav 28,140,68\nRa 188,2,64512\ns_barrier\nMaav 24,140,64\nAD 84,84,83\nCV 73,82,83\n"
"Maav 32,144,64\nRa 128,24,0\nMaav 36,144,68\ns_add_u32 s46, s87, s88\nEX 80,52\nMaav 44,148,68\nRa 132,24,1024\nv_xor_b32 v2, 0x10000, v2\nMaav 40,148,64\nWL 8\nEX 81,53\n"
"Maav 48,152,64\nRa 136,24,2048\nAD 87,87,85\nMaav 52,152,68\nD1 1,16,46,0\nMaav 60,156,68\nRa 140,24,3072\nMaav 56,156,64\nAD 85,80,81\nCV 76,80,81\nMaav 64,160,64\nRa 144,26,0\n"
"Maav 68,160,68\ns_cmp_lt_u32 s99, s92\nEX 82,54\nMaav 76,164,68\nRa 148,26,1024\ns_cbranch_scc1 .E276\n.E277:\nMaav 72,164,64\nWL 8\nEX 83,55\nMaav 80,168,64\nRa 152,26,2048\n"
"AD 85,85,82\nMaav 84,168,68\nD1 1,16,46,1024\nMaav 92,172,68\nRa 156,26,3072\nMaav 88,172,64\nAD 85,85,83\nCV 77,82,83\nMaav 96,176,64\nRa 160,24,8192\nMaav 100,176,68\n"
"s_sub_u32 s90, s90, 1\nEX 80,56\nMaav 108,180,68\nRa 164,24,9216\ns_cbranch_scc1 .E278\n.E279:\nMaav 104,180,64\nWL 6\nEX 81,57\nMaav 112,184,64\nRa 168,24,10240\nAD 84,84,80\n"
"Maav 116,184,68\nD1 1,16,46,2048\ns_sub_u32 s99, s99, 1\nMaav 124,188,68\nRa 172,24,11264\nMaav 120,188,64\nAD 84,84,81\nCV 74,80,81\ns_cbranch_scc0 .A272\nRa 176,26,8192\n"
"EX 82,58\nRa 180,26,9216\nWL 6\nEX 83,59\nCvav 36,140,220,8\nRa 184,26,10240\nAD 84,84,82\nMvav 36,136,212\nD1 1,16,46,3072\ns_add_u32 m0, s89, 32768\nMvav 36,132,204\n"
"Ra 188,26,11264\nMvav 36,128,196\nAD 84,84,83\nCV 75,82,83\nMvav 36,144,228\nRa 128,2,16384\nMvav 36,148,236\ns_add_u32 s46, s86, s88\nEX 80,60\nMvav 36,152,244\nRa 132,2,17408\n"
"Mvav 36,156,252\nEX 81,61\nRa 136,2,18432\nAD 85,85,80\nD1 1,16,46,0\nRa 140,2,19456\nSv 112,1,16,49,0\nAD 85,85,81\nCV 78,80,81\nRa 144,2,20480\ns_mov_b32 s87, s95\nEX 82,62\n"
"Ra 148,2,21504\nWL 6\nEX 83,63\nCvav 44,172,220,16\nRa 152,2,22528\nAD 85,85,82\nMvav 44,168,212\nD1 1,16,46,1024\nMvav 44,164,204\nRa 156,2,23552\nMvav 44,160,196\nAD 85,85,83\n"
"CV 79,82,83\nMvav 44,176,228\nRa 160,2,24576\nv_max_f32 v90, v84, v85\nMvav 44,180,236\ns_add_u32 s88, s88, 32768\nMvav 44,184,244\nRa 164,2,25600\nv_cmp_lt_f32 vcc, s63, v90\n"
"Mvav 44,188,252\nWL 8\nRa 168,2,26624\ns_cbranch_vccnz .T287\n.T288:\nD1 1,16,46,2048\nRa 172,2,27648\nAD 86,86,84\nMaav 0,128,72\nRa 176,2,28672\nMaav 4,128,76\nMaav 12,132,76\n"
"Ra 180,2,29696\nMaav 8,132,72\ns_waitcnt vmcnt(13) lgkmcnt(8)\nMaav 16,136,72\nRa 184,2,30720\nMaav 20,136,76\nD1 1,16,46,3072\ns_add_u32 m0, s89, 49152\nMaav 28,140,76\n"
"Ra 188,2,31744\ns_barrier\nMaav 24,140,72\nMaav 32,144,72\nRa 128,24,32768\nMaav 36,144,76\ns_add_u32 s46, s87, s88\nEX 80,36\nMaav 44,148,76\nRa 132,24,33792\nMaav 40,148,72\n"
"WL 8\nEX 81,37\nMaav 48,152,72\nRa 136,24,34816\nAD 87,87,85\nMaav 52,152,76\nD1 1,16,46,0\nMaav 60,156,76\nRa 140,24,35840\nMaav 56,156,72\nAD 85,80,81\nCV 68,80,81\n"
"Maav 64,160,72\nRa 144,26,32768\nMaav 68,160,76\ns_nop 0\nEX 82,38\nMaav 76,164,76\nRa 148,26,33792\nv_cmpx_gt_i32 vcc, 19, v0\nv_mov_b32 v19, s97\nv_cmpx_gt_i32 vcc, 18, v0\n"
"v_mov_b32 v18, s97\nv_cmpx_gt_i32 vcc, 17, v0\nv_mov_b32 v17, s97\nv_cmpx_gt_i32 vcc, 16, v0\nv_mov_b32 v16, s97\nv_cmpx_gt_i32 vcc, 3, v0\nv_mov_b32 v11, s97\n"
"v_cmpx_gt_i32 vcc, 2, v0\nv_mov_b32 v10, s97\nv_cmpx_gt_i32 vcc, 1, v0\nv_mov_b32 v9, s97\nv_cmpx_gt_i32 vcc, 0, v0\nv_mov_b32 v8, s97\ns_mov_b64 exec, -1\ns_nop 2\ns_nop 0\n"
"Maav 72,164,72\nWL 8\nEX 83,39\nMaav 80,168,72\nRa 152,26,34816\nAD 85,85,82\nMaav 84,168,76\nD1 1,16,46,1024\nMaav 92,172,76\nRa 156,26,35840\nMaav 88,172,72\nAD 85,85,83\n"
"CV 69,82,83\nMaav 96,176,72\nRa 160,24,40960\nMaav 100,176,76\ns_xor_b32 s89, s89, 0x10000\nMaav 108,180,76\nRa 164,24,41984\nMaav 104,180,72\nWL 10\nMaav 112,184,72\n"
"Ra 168,24,43008\nMaav 116,184,76\nD1 1,16,46,2048\nMaav 124,188,76\nRa 172,24,44032\nMaav 120,188,72\nRa 176,26,40960\nRa 180,26,41984\nWL 6\nCvav 52,140,220,8\nRa 184,26,43008\n"
"Mvav 52,136,212\nD1 1,16,46,3072\ns_add_u32 m0, s89, 0\nMvav 52,132,204\nRa 188,26,44032\nMvav 52,128,196\nMvav 52,144,228\nRa 128,2,49152\nMvav 52,148,236\n"
"s_add_u32 s46, s86, s88\nEX 80,44\nMvav 52,152,244\nRa 132,2,50176\nv_xor_b32 v24, 0x10000, v24\nMvav 52,156,252\nEX 81,45\nRa 136,2,51200\nAD 85,85,80\nD1 1,16,46,0\n"
"Ra 140,2,52224\nSv 112,1,16,49,1024\nAD 85,85,81\nCV 70,80,81\nRa 144,2,53248\ns_add_u32 s49, s49, 131072\nEX 82,46\nRa 148,2,54272\nv_xor_b32 v26, 0x10000, v26\nWL 6\nEX 83,47\n"
"Cvav 60,172,220,16\nRa 152,2,55296\nAD 85,85,82\nMvav 60,168,212\nD1 1,16,46,1024\nMvav 60,164,204\nRa 156,2,56320\nMvav 60,160,196\nAD 85,85,83\nCV 71,82,83\nMvav 60,176,228\n"
"Ra 160,2,57344\nMvav 60,180,236\ns_add_u32 s88, s88, 32768\nMvav 60,184,244\nRa 164,2,58368\nv_cmp_lt_f32 vcc, s63, v85\nMvav 60,188,252\nWL 8\nRa 168,2,59392\n"
"s_cbranch_vccnz .T289\n.T290:\nD1 1,16,46,2048\nRa 172,2,60416\nRa 176,2,61440\nMaav 4,128,68\nMaav 12,132,68\nRa 180,2,62464\ns_waitcnt vmcnt(13) lgkmcnt(8)\nRa 184,2,63488\n"
"Maav 20,136,68\nD1 1,16,46,3072\ns_add_u32 m0, s89, 16384\nMaav 28,140,68\nRa 188,2,64512\ns_barrier\nRa 128,24,0\nMaav 36,144,68\ns_add_u32 s46, s87, s88\nEX 80,52\n"
"Maav 44,148,68\nRa 132,24,1024\nv_xor_b32 v2, 0x10000, v2\nWL 8\nEX 81,53\nRa 136,24,2048\nAD 87,87,85\nMaav 52,152,68\nD1 1,16,46,0\nMaav 60,156,68\nRa 140,24,3072\nAD 85,80,81\n"
"CV 76,80,81\nRa 144,26,0\nMaav 68,160,68\nEX 82,54\nMaav 76,164,68\nRa 148,26,1024\nWL 8\nEX 83,55\nRa 152,26,2048\nAD 85,85,82\nMaav 84,168,68\nD1 1,16,46,1024\nMaav 92,172,68\n"
"Ra 156,26,3072\nAD 85,85,83\nCV 77,82,83\nRa 160,24,8192\nMaav 100,176,68\ns_sub_u32 s90, s90, 1\nMaav 108,180,68\nRa 164,24,9216\ns_cbranch_scc1 .E285\n.E286:\nWL 6\n"
"Ra 168,24,10240\nMaav 116,184,68\nD1 1,16,46,2048\nMaav 124,188,68\nRa 172,24,11264\n.SW246:\nv_mov_b32 v4, 0\nv_mov_b32 v5, 0\nv_mov_b32 v6, 0\nv_mov_b32 v7, 0\nv_mov_b32 v8, 0\n"
"v_mov_b32 v9, 0\nv_mov_b32 v10, 0\nv_mov_b32 v11, 0\nv_mov_b32 v12, 0\nv_mov_b32 v13, 0\nv_mov_b32 v14, 0\nv_mov_b32 v15, 0\nv_mov_b32 v16, 0\nv_mov_b32 v17, 0\nv_mov_b32 v18, 0\n"
"v_mov_b32 v19, 0\ns_add_u32 s99, s98, 1\ns_mov_b32 s94, s96\ns_add_u32 s95, s96, 65536\ns_lshl2_add_u32 s2, s13, s11\ns_cmp_lt_u32 s2, 2\ns_cbranch_scc1 .E291\n.E292:\n"
"Cvaa 32,128,192,4\nRa 176,26,8192\nMvaa 32,132,200\nMvaa 32,136,208\nRa 180,26,9216\nMvaa 32,140,216\nWL 6\nCvaa 36,140,220,8\nRa 184,26,10240\nMvaa 36,136,212\nD1 1,16,46,3072\n"
"s_add_u32 m0, s89, 32768\nMvaa 36,132,204\nRa 188,26,11264\nMvaa 36,128,196\nMvaa 36,144,228\nRa 128,2,16384\nMvaa 36,148,236\ns_add_u32 s46, s86, s88\nEX 80,60\nMvaa 36,152,244\n"
"Ra 132,2,17408\nMvaa 36,156,252\nWL 6\nEX 81,61\nMvaa 32,156,248\nRa 136,2,18432\nAD 85,85,80\nMvaa 32,152,240\nD1 1,16,46,0\nMvaa 32,148,232\nRa 140,2,19456\nMvaa 32,144,224\n"
"AD 85,85,81\nCV 78,80,81\nCvaa 40,160,192,12\nRa 144,2,20480\nMvaa 40,164,200\nEX 82,62\nMvaa 40,168,208\nRa 148,2,21504\nMvaa 40,172,216\nWL 6\nEX 83,63\nCvaa 44,172,220,16\n"
"Ra 152,2,22528\nAD 85,85,82\nMvaa 44,168,212\nD1 1,16,46,1024\nMvaa 44,164,204\nRa 156,2,23552\nMvaa 44,160,196\nAD 85,85,83\nCV 79,82,83\nMvaa 44,176,228\nRa 160,2,24576\n"
"Mvaa 44,180,236\ns_add_u32 s88, s88, 32768\nEX 80,32\nMvaa 44,184,244\nRa 164,2,25600\nv_cmp_lt_f32 vcc, s63, v85\nMvaa 44,188,252\nWL 8\nEX 81,33\nMvaa 40,188,248\n"
"Ra 168,2,26624\ns_cbranch_vccnz .T300\n.T301:\nMvaa 40,184,240\nD1 1,16,46,2048\nMvaa 40,180,232\nRa 172,2,27648\nMvaa 40,176,224\nAD 84,80,81\nCV 64,80,81\nRa 176,2,28672\n"
"Maav 4,128,76\nEX 82,34\nMaav 12,132,76\nRa 180,2,29696\ns_waitcnt vmcnt(12) lgkmcnt(8)\nEX 83,35\nRa 184,2,30720\nAD 84,84,82\nMaav 20,136,76\nD1 1,16,46,3072\n"
"s_add_u32 m0, s89, 49152\nMaav 28,140,76\nRa 188,2,31744\ns_barrier\nAR 98,0\nAR 99,1\nCV 100,98,99\nAD 84,84,83\nCV 65,82,83\nAR 98,2\nAR 99,3\nCV 101,98,99\nRa 128,24,32768\n"
"AR 98,8\nAR 99,9\nCV 102,98,99\nAR 98,10\nMaav 36,144,76\ns_add_u32 s46, s87, s88\nEX 80,36\nAR 99,11\nMaav 44,148,76\nRa 132,24,33792\nCV 103,98,99\ns_add_u32 s5, s66, 0\n"
"Sv 100,1,16,5,0\nAR 98,4\nWL 8\nEX 81,37\nAR 99,5\nCV 104,98,99\nRa 136,24,34816\nAD 87,87,85\nAR 98,6\nAR 99,7\nCV 105,98,99\nMaav 52,152,76\nD1 1,16,46,0\nv_mov_b32 v96, v86\n"
"v_mov_b32 v97, v87\nAR 98,12\nAR 99,13\nMaav 60,156,76\nRa 140,24,35840\nv_permlane32_swap_b32 v86, v96\nCV 106,98,99\nAR 98,14\nAR 99,15\nAD 85,80,81\nCV 68,80,81\nCV 107,98,99\n"
"s_add_u32 s5, s66, 0\nSv 104,1,16,5,1024\nRa 144,26,32768\nv_permlane32_swap_b32 v87, v97\nAD 86,86,96\nAR 98,16\nAR 99,17\nMaav 68,160,76\ns_cmp_eq_u32 s99, 0\nEX 82,38\n"
"Maav 76,164,76\nRa 148,26,33792\ns_cbranch_scc1 .E294\n.E295:\nCV 108,98,99\nAR 98,18\nAR 99,19\nWL 8\nEX 83,39\nCV 109,98,99\nAR 98,24\nRa 152,26,34816\nAD 85,85,82\nAD 87,87,97\n"
"AR 99,25\nCV 110,98,99\nMaav 84,168,76\nD1 1,16,46,1024\nv_mov_b32 v96, v86\nAR 98,26\nAR 99,27\nCV 111,98,99\nMaav 92,172,76\nRa 156,26,35840\nv_mov_b32 v97, v87\n"
"s_add_u32 s5, s66, 0\nSv 108,1,16,5,2048\nAR 98,20\nAD 85,85,83\nCV 69,82,83\nv_permlane16_swap_b32 v86, v96\nAR 99,21\nCV 100,98,99\nRa 160,24,40960\n"
"v_permlane16_swap_b32 v87, v97\nAR 98,22\nAR 99,23\nCV 101,98,99\nMaav 100,176,76\ns_xor_b32 s89, s89, 0x10000\nEX 80,40\nAR 98,28\nMaav 108,180,76\nRa 164,24,41984\nAD 86,86,96\n"
"AD 87,87,97\nAR 99,29\nCV 102,98,99\nWL 6\nEX 81,41\nAR 98,30\nAR 99,31\nRa 168,24,43008\nAD 84,84,80\nCV 103,98,99\ns_add_u32 s5, s66, 0\nSv 100,1,16,5,3072\nMaav 116,184,76\n"
"D1 1,16,46,2048\nv_cmp_gt_u32 vcc, 0x100, v1\nv_mov_b32 v94, s15\nv_mov_b32 v95, s15\nv_cndmask_b32 v92, v89, v88, vcc\nv_cndmask_b32 v93, v87, v86, vcc\nMaav 124,188,76\n"
"Ra 172,24,44032\nv_mov_b32 v86, 0\nAR 98,32\nAR 99,33\nCV 104,98,99\nAD 84,84,81\nCV 66,80,81\nAR 98,34\nAR 99,35\nCV 105,98,99\nCvaa 48,128,192,4\nRa 176,26,40960\n"
"v_mov_b32 v87, 0\nv_mov_b32 v88, 0\nAR 98,40\nAR 99,41\nMvaa 48,132,200\ns_cmp_eq_u32 s99, 0\nEX 82,42\nMvaa 48,136,208\nRa 180,26,41984\ns_cselect_b32 s86, s94, s86\n"
"CV 106,98,99\nAR 98,42\nAR 99,43\nMvaa 48,140,216\nWL 6\nEX 83,43\nCV 107,98,99\nCvaa 52,140,220,8\nRa 184,26,43008\nAD 84,84,82\nv_mov_b32 v89, 0\ns_add_u32 s5, s66, 4096\n"
"Sv 104,1,16,5,0\nMvaa 52,136,212\nD1 1,16,46,3072\ns_add_u32 m0, s89, 0\nAR 98,36\nAR 99,37\nCV 108,98,99\nMvaa 52,132,204\nRa 188,26,44032\nAR 98,38\nAR 99,39\nCV 109,98,99\n"
"AR 98,44\nMvaa 52,128,196\nAD 84,84,83\nCV 67,82,83\nAR 99,45\nCV 110,98,99\nAR 98,46\nMvaa 52,144,228\nRa 128,2,49152\nAR 99,47\nCV 111,98,99\ns_add_u32 s5, s66, 4096\n"
"Sv 108,1,16,5,1024\nMvaa 52,148,236\ns_add_u32 s46, s86, s88\nEX 80,44\nAR 98,48\nMvaa 52,152,244\nRa 132,2,50176\nv_xor_b32 v24, 0x10000, v24\nAR 99,49\nCV 100,98,99\nAR 98,50\n"
"Mvaa 52,156,252\nWL 6\nEX 81,45\nAR 99,51\nCV 101,98,99\nMvaa 48,156,248\nRa 136,2,51200\nAD 85,85,80\nAR 98,56\nAR 99,57\nCV 102,98,99\nMvaa 48,152,240\nD1 1,16,46,0\nAR 98,58\n"
"AR 99,59\nCV 103,98,99\nMvaa 48,148,232\nRa 140,2,52224\ns_add_u32 s5, s66, 4096\nSv 100,1,16,5,2048\nAR 98,52\nAR 99,53\nMvaa 48,144,224\nAD 85,85,81\nCV 70,80,81\nCV 104,98,99\n"
"AR 98,54\nAR 99,55\nCvaa 56,160,192,12\nRa 144,2,53248\nCV 105,98,99\nAR 98,60\nAR 99,61\nCV 106,98,99\nMvaa 56,164,200\nEX 82,46\nAR 98,62\nAR 99,63\nMvaa 56,168,208\n"
"Ra 148,2,54272\nv_xor_b32 v26, 0x10000, v26\nCV 107,98,99\ns_add_u32 s5, s66, 4096\nSv 104,1,16,5,3072\nMvaa 56,172,216\nWL 6\nEX 83,47\nAR 98,64\nAR 99,65\nCvaa 60,172,220,16\n"
"Ra 152,2,55296\nAD 85,85,82\nCV 108,98,99\nAR 98,66\nAR 99,67\nMvaa 60,168,212\nD1 1,16,46,1024\nCV 109,98,99\nAR 98,72\nAR 99,73\nCV 110,98,99\nMvaa 60,164,204\nRa 156,2,56320\n"
"AR 98,74\nAR 99,75\nCV 111,98,99\nMvaa 60,160,196\nAD 85,85,83\nCV 71,82,83\ns_add_u32 s5, s66, 16384\nSv 108,1,16,5,0\nAR 98,68\nMvaa 60,176,228\nRa 160,2,57344\n"
"v_max_f32 v90, v84, v85\nAR 99,69\nCV 100,98,99\nAR 98,70\nMvaa 60,180,236\ns_add_u32 s88, s88, 32768\nEX 80,48\nAR 99,71\nMvaa 60,184,244\nRa 164,2,58368\n"
"v_cmp_lt_f32 vcc, s63, v90\nMvaa 60,188,252\nWL 8\nEX 81,49\nMvaa 56,188,248\nRa 168,2,59392\ns_cbranch_vccnz .T302\n.T303:\nv_mov_b32 v20, v84\nv_mov_b32 v21, v85\n"
"v_mov_b32 v22, v20\nv_mov_b32 v23, v21\nMvaa 56,184,240\nD1 1,16,46,2048\nv_permlane32_swap_b32 v20, v22\nv_permlane32_swap_b32 v21, v23\nAD 20,20,22\nAD 21,21,23\n"
"v_mov_b32 v22, v20\nv_mov_b32 v23, v21\nMvaa 56,180,232\nRa 172,2,60416\nv_permlane16_swap_b32 v20, v22\nv_permlane16_swap_b32 v21, v23\nAD 20,20,22\nAD 21,21,23\n"
"v_min_f32 v90, v20, v21\nv_cmp_gt_f32 vcc, s64, v90\ns_nop 0\ns_cbranch_vccnz .T304\n.T305:\nAD 86,86,84\nMvaa 56,176,224\nAD 84,80,81\nCV 72,80,81\nCV 101,98,99\nAR 98,76\n"
"AR 99,77\nZvav 128,128,64\nRa 176,2,61440\nCV 102,98,99\nAR 98,78\nAR 99,79\nCV 103,98,99\nZvav 132,128,68\nEX 82,50\ns_add_u32 s5, s66, 16384\nSv 100,1,16,5,1024\n"
"Zvav 140,132,68\nRa 180,2,62464\nAR 98,80\nAR 99,81\nCV 104,98,99\nAR 98,82\nZvav 136,132,64\ns_waitcnt vmcnt(21) lgkmcnt(8)\nEX 83,51\nAR 99,83\nCV 105,98,99\nZvav 144,136,64\n"
"Ra 184,2,63488\nAD 84,84,82\nAR 98,88\nAR 99,89\nCV 106,98,99\nZvav 148,136,68\nD1 1,16,46,3072\ns_add_u32 m0, s89, 16384\nAR 98,90\nAR 99,91\nCV 107,98,99\nZvav 156,140,68\n"
"Ra 188,2,64512\ns_barrier\ns_add_u32 s5, s66, 16384\nSv 104,1,16,5,2048\nAR 98,84\nZvav 152,140,64\nAD 84,84,83\nCV 73,82,83\nAR 99,85\nCV 108,98,99\nAR 98,86\nZvav 160,144,64\n"
"Ra 128,24,0\nAR 99,87\nCV 109,98,99\nAR 98,92\nAR 99,93\nZvav 164,144,68\ns_add_u32 s46, s87, s88\nEX 80,52\nCV 110,98,99\nZvav 172,148,68\nRa 132,24,1024\n"
"v_xor_b32 v2, 0x10000, v2\nAR 98,94\nAR 99,95\nCV 111,98,99\nZvav 168,148,64\nWL 8\nEX 81,53\ns_add_u32 s5, s66, 16384\nSv 108,1,16,5,3072\nZvav 176,152,64\nRa 136,24,2048\n"
"AD 87,87,85\nAR 98,96\nAR 99,97\nCV 100,98,99\nZvav 180,152,68\nD1 1,16,46,0\nAR 98,98\nAR 99,99\nCV 101,98,99\nAR 98,104\nZvav 188,156,68\nRa 140,24,3072\nAR 99,105\n"
"CV 102,98,99\nAR 98,106\nAR 99,107\nZvav 184,156,64\nAD 85,80,81\nCV 76,80,81\nCV 103,98,99\ns_add_u32 s5, s66, 20480\nSv 100,1,16,5,0\nZvav 192,160,64\nRa 144,26,0\nAR 98,100\n"
"AR 99,101\nCV 104,98,99\nAR 98,102\nZvav 196,160,68\ns_cmp_lt_u32 s99, s92\nEX 82,54\nZvav 204,164,68\nRa 148,26,1024\ns_cbranch_scc1 .E296\n.E297:\nAR 99,103\nCV 105,98,99\n"
"AR 98,108\nZvav 200,164,64\nWL 8\nEX 83,55\nAR 99,109\nCV 106,98,99\nZvav 208,168,64\nRa 152,26,2048\nAD 85,85,82\nAR 98,110\nAR 99,111\nCV 107,98,99\nZvav 212,168,68\n"
"D1 1,16,46,1024\ns_add_u32 s5, s66, 20480\nSv 104,1,16,5,1024\nAR 98,112\nAR 99,113\nZvav 220,172,68\nRa 156,26,3072\nCV 108,98,99\nAR 98,114\nAR 99,115\nCV 109,98,99\n"
"Zvav 216,172,64\nAD 85,85,83\nCV 77,82,83\nAR 98,120\nAR 99,121\nCV 110,98,99\nZvav 224,176,64\nRa 160,24,8192\nAR 98,122\nAR 99,123\nCV 111,98,99\nZvav 228,176,68\n"
"s_sub_u32 s90, s90, 1\nEX 80,56\nZvav 236,180,68\nRa 164,24,9216\ns_cbranch_scc1 .E298\n.E299:\ns_add_u32 s5, s66, 20480\nSv 108,1,16,5,2048\nAR 98,116\nZvav 232,180,64\nWL 6\n"
"EX 81,57\nAR 99,117\nCV 100,98,99\nZvav 240,184,64\nRa 168,24,10240\nAD 84,84,80\nAR 98,118\nAR 99,119\nCV 101,98,99\nZvav 244,184,68\nD1 1,16,46,2048\nAR 98,124\nAR 99,125\n"
"CV 102,98,99\nAR 98,126\nZvav 252,188,68\nRa 172,24,11264\nAR 99,127\nCV 103,98,99\ns_add_u32 s5, s66, 20480\nSv 100,1,16,5,3072\nZvav 248,188,64\nAD 84,84,81\nCV 74,80,81\n"
"s_sub_u32 s99, s99, 1\ns_cbranch_scc1 .LE247\n.p2align 6\n.A306:\nCvaa 32,128,192,4\nRa 176,26,8192\nMvaa 32,132,200\nEX 82,58\nMvaa 32,136,208\nRa 180,26,9216\nMvaa 32,140,216\n"
"WL 6\nEX 83,59\nCvaa 36,140,220,8\nRa 184,26,10240\nAD 84,84,82\nMvaa 36,136,212\nD1 1,16,46,3072\ns_add_u32 m0, s89, 32768\nMvaa 36,132,204\nRa 188,26,11264\nMvaa 36,128,196\n"
"AD 84,84,83\nCV 75,82,83\nMvaa 36,144,228\nRa 128,2,16384\nMvaa 36,148,236\ns_add_u32 s46, s86, s88\nEX 80,60\nMvaa 36,152,244\nRa 132,2,17408\nMvaa 36,156,252\nWL 6\nEX 81,61\n"
"Mvaa 32,156,248\nRa 136,2,18432\nAD 85,85,80\nMvaa 32,152,240\nD1 1,16,46,0\nMvaa 32,148,232\nRa 140,2,19456\nSv 112,1,16,49,0\nMvaa 32,144,224\nAD 85,85,81\nCV 78,80,81\n"
"Cvaa 40,160,192,12\nRa 144,2,20480\nMvaa 40,164,200\nEX 82,62\nMvaa 40,168,208\nRa 148,2,21504\nMvaa 40,172,216\nWL 6\nEX 83,63\nCvaa 44,172,220,16\nRa 152,2,22528\nAD 85,85,82\n"
"Mvaa 44,168,212\nD1 1,16,46,1024\nMvaa 44,164,204\nRa 156,2,23552\nMvaa 44,160,196\nAD 85,85,83\nCV 79,82,83\nMvaa 44,176,228\nRa 160,2,24576\nv_max_f32 v90, v84, v85\n"
"Mvaa 44,180,236\ns_add_u32 s88, s88, 32768\nEX 80,32\nMvaa 44,184,244\nRa 164,2,25600\nv_cmp_lt_f32 vcc, s63, v90\nMvaa 44,188,252\nWL 8\nEX 81,33\nMvaa 40,188,248\n"
"Ra 168,2,26624\ns_cbranch_vccnz .T316\n.T317:\nMvaa 40,184,240\nD1 1,16,46,2048\nMvaa 40,180,232\nRa 172,2,27648\nAD 86,86,84\nMvaa 40,176,224\nAD 84,80,81\nCV 64,80,81\n"
"Mvav 128,128,72\nRa 176,2,28672\nMvav 132,128,76\ns_cmp_eq_u32 s99, s98\nEX 82,34\nMvav 140,132,76\nRa 180,2,29696\ns_cbranch_scc1 .E314\n.E315:\nMvav 136,132,72\n"
"s_waitcnt vmcnt(13) lgkmcnt(8)\nEX 83,35\nMvav 144,136,72\nRa 184,2,30720\nAD 84,84,82\nMvav 148,136,76\nD1 1,16,46,3072\ns_add_u32 m0, s89, 49152\nMvav 156,140,76\n"
"Ra 188,2,31744\ns_barrier\nMvav 152,140,72\nAD 84,84,83\nCV 65,82,83\nMvav 160,144,72\nRa 128,24,32768\nMvav 164,144,76\ns_add_u32 s46, s87, s88\nEX 80,36\nMvav 172,148,76\n"
"Ra 132,24,33792\nMvav 168,148,72\nWL 8\nEX 81,37\nMvav 176,152,72\nRa 136,24,34816\nAD 87,87,85\nMvav 180,152,76\nD1 1,16,46,0\nMvav 188,156,76\nRa 140,24,35840\nMvav 184,156,72\n"
"AD 85,80,81\nCV 68,80,81\nMvav 192,160,72\nRa 144,26,32768\nMvav 196,160,76\ns_cmp_eq_u32 s99, 0\nEX 82,38\nMvav 204,164,76\nRa 148,26,33792\ns_cbranch_scc1 .E308\n.E309:\n"
"Mvav 200,164,72\nWL 8\nEX 83,39\nMvav 208,168,72\nRa 152,26,34816\nAD 85,85,82\nMvav 212,168,76\nD1 1,16,46,1024\nMvav 220,172,76\nRa 156,26,35840\nMvav 216,172,72\nAD 85,85,83\n"
"CV 69,82,83\nMvav 224,176,72\nRa 160,24,40960\nMvav 228,176,76\ns_xor_b32 s89, s89, 0x10000\nEX 80,40\nMvav 236,180,76\nRa 164,24,41984\nMvav 232,180,72\nWL 6\nEX 81,41\n"
"Mvav 240,184,72\nRa 168,24,43008\nAD 84,84,80\nMvav 244,184,76\nD1 1,16,46,2048\nMvav 252,188,76\nRa 172,24,44032\nMvav 248,188,72\nAD 84,84,81\nCV 66,80,81\nCvaa 48,128,192,4\n"
"Ra 176,26,40960\nMvaa 48,132,200\ns_cmp_eq_u32 s99, 0\nEX 82,42\nMvaa 48,136,208\nRa 180,26,41984\ns_cselect_b32 s86, s94, s86\nMvaa 48,140,216\nWL 6\nEX 83,43\nCvaa 52,140,220,8\n"
"Ra 184,26,43008\nAD 84,84,82\nMvaa 52,136,212\nD1 1,16,46,3072\ns_add_u32 m0, s89, 0\nMvaa 52,132,204\nRa 188,26,44032\nMvaa 52,128,196\nAD 84,84,83\nCV 67,82,83\nMvaa 52,144,228\n"
"Ra 128,2,49152\nMvaa 52,148,236\ns_add_u32 s46, s86, s88\nEX 80,44\nMvaa 52,152,244\nRa 132,2,50176\nv_xor_b32 v24, 0x10000, v24\nMvaa 52,156,252\nWL 6\nEX 81,45\nMvaa 48,156,248\n"
"Ra 136,2,51200\nAD 85,85,80\nMvaa 48,152,240\nD1 1,16,46,0\nMvaa 48,148,232\nRa 140,2,52224\nSv 112,1,16,49,1024\nMvaa 48,144,224\nAD 85,85,81\nCV 70,80,81\nCvaa 56,160,192,12\n"
"Ra 144,2,53248\nMvaa 56,164,200\ns_add_u32 s49, s49, 131072\nEX 82,46\nMvaa 56,168,208\nRa 148,2,54272\nv_xor_b32 v26, 0x10000, v26\nMvaa 56,172,216\nWL 6\nEX 83,47\n"
"Cvaa 60,172,220,16\nRa 152,2,55296\nAD 85,85,82\nMvaa 60,168,212\nD1 1,16,46,1024\nMvaa 60,164,204\nRa 156,2,56320\nMvaa 60,160,196\nAD 85,85,83\nCV 71,82,83\nMvaa 60,176,228\n"
"Ra 160,2,57344\nv_max_f32 v90, v84, v85\nMvaa 60,180,236\ns_add_u32 s88, s88, 32768\nEX 80,48\nMvaa 60,184,244\nRa 164,2,58368\nv_cmp_lt_f32 vcc, s63, v90\nMvaa 60,188,252\nWL 8\n"
"EX 81,49\nMvaa 56,188,248\nRa 168,2,59392\ns_cbranch_vccnz .T318\n.T319:\nMvaa 56,184,240\nD1 1,16,46,2048\nMvaa 56,180,232\nRa 172,2,60416\nAD 86,86,84\nMvaa 56,176,224\n"
"AD 84,80,81\nCV 72,80,81\nMvav 128,128,64\nRa 176,2,61440\nMvav 132,128,68\nEX 82,50\nMvav 140,132,68\nRa 180,2,62464\nMvav 136,132,64\ns_waitcnt vmcnt(13) lgkmcnt(8)\nEX 83,51\n"
"Mvav 144,136,64\nRa 184,2,63488\nAD 84,84,82\nMvav 148,136,68\nD1 1,16,46,3072\ns_add_u32 m0, s89, 16384\nMvav 156,140,68\nRa 188,2,64512\ns_barrier\nMvav 152,140,64\nAD 84,84,83\n"
"CV 73,82,83\nMvav 160,144,64\nRa 128,24,0\nMvav 164,144,68\ns_add_u32 s46, s87, s88\nEX 80,52\nMvav 172,148,68\nRa 132,24,1024\nv_xor_b32 v2, 0x10000, v2\nMvav 168,148,64\nWL 8\n"
"EX 81,53\nMvav 176,152,64\nRa 136,24,2048\nAD 87,87,85\nMvav 180,152,68\nD1 1,16,46,0\nMvav 188,156,68\nRa 140,24,3072\nMvav 184,156,64\nAD 85,80,81\nCV 76,80,81\nMvav 192,160,64\n"
"Ra 144,26,0\nMvav 196,160,68\ns_cmp_lt_u32 s99, s92\nEX 82,54\nMvav 204,164,68\nRa 148,26,1024\ns_cbranch_scc1 .E310\n.E311:\nMvav 200,164,64\nWL 8\nEX 83,55\nMvav 208,168,64\n"
"Ra 152,26,2048\nAD 85,85,82\nMvav 212,168,68\nD1 1,16,46,1024\nMvav 220,172,68\nRa 156,26,3072\nMvav 216,172,64\nAD 85,85,83\nCV 77,82,83\nMvav 224,176,64\nRa 160,24,8192\n"
"Mvav 228,176,68\ns_sub_u32 s90, s90, 1\nEX 80,56\nMvav 236,180,68\nRa 164,24,9216\ns_cbranch_scc1 .E312\n.E313:\nMvav 232,180,64\nWL 6\nEX 81,57\nMvav 240,184,64\nRa 168,24,10240\n"
"AD 84,84,80\nMvav 244,184,68\nD1 1,16,46,2048\ns_sub_u32 s99, s99, 1\nMvav 252,188,68\nRa 172,24,11264\nMvav 248,188,64\nAD 84,84,81\nCV 74,80,81\ns_cbranch_scc0 .A306\n.LE247:\n"
"Ra 176,26,8192\nEX 82,58\nRa 180,26,9216\nWL 6\nEX 83,59\nCvaa 36,140,220,8\nRa 184,26,10240\nAD 84,84,82\nMvaa 36,136,212\nD1 1,16,46,3072\ns_add_u32 m0, s89, 32768\n"
"Mvaa 36,132,204\nRa 188,26,11264\nMvaa 36,128,196\nAD 84,84,83\nCV 75,82,83\nMvaa 36,144,228\nRa 128,2,16384\nMvaa 36,148,236\ns_add_u32 s46, s86, s88\nEX 80,60\nMvaa 36,152,244\n"
"Ra 132,2,17408\nMvaa 36,156,252\nEX 81,61\nRa 136,2,18432\nAD 85,85,80\nD1 1,16,46,0\nRa 140,2,19456\nSv 112,1,16,49,0\nAD 85,85,81\nCV 78,80,81\nRa 144,2,20480\n"
"s_mov_b32 s87, s95\nEX 82,62\nRa 148,2,21504\nWL 6\nEX 83,63\nCvaa 44,172,220,16\nRa 152,2,22528\nAD 85,85,82\nMvaa 44,168,212\nD1 1,16,46,1024\nMvaa 44,164,204\nRa 156,2,23552\n"
"Mvaa 44,160,196\nAD 85,85,83\nCV 79,82,83\nMvaa 44,176,228\nRa 160,2,24576\nv_max_f32 v90, v84, v85\nMvaa 44,180,236\ns_add_u32 s88, s88, 32768\nMvaa 44,184,244\nRa 164,2,25600\n"
"v_cmp_lt_f32 vcc, s63, v90\nMvaa 44,188,252\nWL 8\nRa 168,2,26624\ns_cbranch_vccnz .T323\n.T324:\nD1 1,16,46,2048\nRa 172,2,27648\nAD 86,86,84\nMvav 128,128,72\nRa 176,2,28672\n"
"Mvav 132,128,76\ns_cmp_eq_u32 s99, s98\nMvav 140,132,76\nRa 180,2,29696\ns_cbranch_scc1 .E321\n.E322:\nMvav 136,132,72\ns_waitcnt vmcnt(13) lgkmcnt(8)\nMvav 144,136,72\n"
"Ra 184,2,30720\nMvav 148,136,76\nD1 1,16,46,3072\ns_add_u32 m0, s89, 49152\nMvav 156,140,76\nRa 188,2,31744\ns_barrier\nMvav 152,140,72\nMvav 160,144,72\nRa 128,24,32768\n"
"Mvav 164,144,76\ns_add_u32 s46, s87, s88\nEX 80,36\nMvav 172,148,76\nRa 132,24,33792\nMvav 168,148,72\nWL 8\nEX 81,37\nMvav 176,152,72\nRa 136,24,34816\nAD 87,87,85\n"
"Mvav 180,152,76\nD1 1,16,46,0\nMvav 188,156,76\nRa 140,24,35840\nMvav 184,156,72\nAD 85,80,81\nCV 68,80,81\nMvav 192,160,72\nRa 144,26,32768\nMvav 196,160,76\ns_nop 0\nEX 82,38\n"
"Mvav 204,164,76\nRa 148,26,33792\nv_cmpx_gt_i32 vcc, 19, v0\nv_mov_b32 v19, s97\nv_cmpx_gt_i32 vcc, 18, v0\nv_mov_b32 v18, s97\nv_cmpx_gt_i32 vcc, 17, v0\nv_mov_b32 v17, s97\n"
"v_cmpx_gt_i32 vcc, 16, v0\nv_mov_b32 v16, s97\nv_cmpx_gt_i32 vcc, 3, v0\nv_mov_b32 v11, s97\nv_cmpx_gt_i32 vcc, 2, v0\nv_mov_b32 v10, s97\nv_cmpx_gt_i32 vcc, 1, v0\n"
"v_mov_b32 v9, s97\nv_cmpx_gt_i32 vcc, 0, v0\nv_mov_b32 v8, s97\ns_mov_b64 exec, -1\ns_nop 2\ns_nop 0\nMvav 200,164,72\nWL 8\nEX 83,39\nMvav 208,168,72\nRa 152,26,34816\n"
"AD 85,85,82\nMvav 212,168,76\nD1 1,16,46,1024\nMvav 220,172,76\nRa 156,26,35840\nMvav 216,172,72\nAD 85,85,83\nCV 69,82,83\nMvav 224,176,72\nRa 160,24,40960\nMvav 228,176,76\n"
"s_xor_b32 s89, s89, 0x10000\nMvav 236,180,76\nRa 164,24,41984\nMvav 232,180,72\nWL 10\nMvav 240,184,72\nRa 168,24,43008\nMvav 244,184,76\nD1 1,16,46,2048\nMvav 252,188,76\n"
"Ra 172,24,44032\nMvav 248,188,72\nRa 176,26,40960\nRa 180,26,41984\nWL 6\nCvaa 52,140,220,8\nRa 184,26,43008\nMvaa 52,136,212\nD1 1,16,46,3072\ns_add_u32 m0, s89, 0\n"
"Mvaa 52,132,204\nRa 188,26,44032\nMvaa 52,128,196\nMvaa 52,144,228\nRa 128,2,49152\nMvaa 52,148,236\ns_add_u32 s46, s86, s88\nEX 80,44\nMvaa 52,152,244\nRa 132,2,50176\n"
"v_xor_b32 v24, 0x10000, v24\nMvaa 52,156,252\nEX 81,45\nRa 136,2,51200\nAD 85,85,80\nD1 1,16,46,0\nRa 140,2,52224\nSv 112,1,16,49,1024\nAD 85,85,81\nCV 70,80,81\nRa 144,2,53248\n"
"s_add_u32 s49, s49, 131072\nEX 82,46\nRa 148,2,54272\nv_xor_b32 v26, 0x10000, v26\nWL 6\nEX 83,47\nCvaa 60,172,220,16\nRa 152,2,55296\nAD 85,85,82\nMvaa 60,168,212\n"
"D1 1,16,46,1024\nMvaa 60,164,204\nRa 156,2,56320\nMvaa 60,160,196\nAD 85,85,83\nCV 71,82,83\nMvaa 60,176,228\nRa 160,2,57344\nMvaa 60,180,236\ns_add_u32 s88, s88, 32768\n"
"Mvaa 60,184,244\nRa 164,2,58368\nv_cmp_lt_f32 vcc, s63, v85\nMvaa 60,188,252\nWL 8\nRa 168,2,59392\ns_cbranch_vccnz .T325\n.T326:\nD1 1,16,46,2048\nRa 172,2,60416\nRa 176,2,61440\n"
"Mvav 132,128,68\nMvav 140,132,68\nRa 180,2,62464\ns_waitcnt vmcnt(13) lgkmcnt(8)\nRa 184,2,63488\nMvav 148,136,68\nD1 1,16,46,3072\ns_add_u32 m0, s89, 16384\nMvav 156,140,68\n"
"Ra 188,2,64512\ns_barrier\nRa 128,24,0\nMvav 164,144,68\ns_add_u32 s46, s87, s88\nEX 80,52\nMvav 172,148,68\nRa 132,24,1024\nv_xor_b32 v2, 0x10000, v2\nWL 8\nEX 81,53\n"
"Ra 136,24,2048\nAD 87,87,85\nMvav 180,152,68\nD1 1,16,46,0\nMvav 188,156,68\nRa 140,24,3072\nAD 85,80,81\nCV 76,80,81\nRa 144,26,0\nMvav 196,160,68\nEX 82,54\nMvav 204,164,68\n"
"Ra 148,26,1024\nWL 8\nEX 83,55\nRa 152,26,2048\nAD 85,85,82\nMvav 212,168,68\nD1 1,16,46,1024\nMvav 220,172,68\nRa 156,26,3072\nAD 85,85,83\nCV 77,82,83\nRa 160,24,8192\n"
"Mvav 228,176,68\nMvav 236,180,68\nRa 164,24,9216\nWL 10\nRa 168,24,10240\nMvav 244,184,68\nD1 1,16,46,2048\nMvav 252,188,68\nRa 172,24,11264\nD1 1,16,46,3072\n"
"s_add_u32 m0, s89, 32768\ns_add_u32 s46, s86, s88\nRa 128,2,16384\nv_xor_b32 v3, 0x10000, v2\nEX 80,60\nRa 132,2,17408\nv_add_u32 v0, 0x20000, v1\nEX 81,61\nRa 136,2,18432\n"
"AD 85,85,80\nD1 1,16,46,0\ns_sub_u32 s94, s49, 1966080\nRa 140,2,19456\ns_add_u32 s95, s94, 1048576\nAD 85,85,81\nCV 78,80,81\nRa 144,2,20480\ns_xor_b32 s92, s94, 0x202000\n"
"EX 82,62\nRa 148,2,21504\ns_add_u32 s93, s96, 983040\nEX 83,63\nRa 152,2,22528\nAD 85,85,82\nD1 1,16,46,1024\ns_xor_b32 s93, s93, 0x202000\nRa 156,2,23552\n"
"s_lshl_b32 s89, s11, 12\nAD 85,85,83\nCV 79,82,83\nRa 160,2,24576\nv_cmp_lt_f32 vcc, s63, v85\nRa 164,2,25600\ns_cbranch_vccnz .T327\n.T328:\nAD 87,87,85\nWL 8\n"
"v_mov_b32 v84, v86\nv_mov_b32 v85, v87\nRa 168,2,26624\nv_permlane32_swap_b32 v86, v84\nv_permlane32_swap_b32 v87, v85\nD1 1,16,46,2048\nAD 86,86,84\nRa 172,2,27648\nAD 87,87,85\n"
"v_mov_b32 v84, v86\nv_mov_b32 v85, v87\nRa 176,2,28672\nv_permlane16_swap_b32 v86, v84\nv_permlane16_swap_b32 v87, v85\nMvav 132,128,76\nAD 86,86,84\nAD 87,87,85\nMvav 140,132,76\n"
"Ra 180,2,29696\nv_rcp_f32 v86, v86\nWL 8\nv_rcp_f32 v87, v87\nRa 184,2,30720\nMvav 148,136,76\nD1 1,16,46,3072\nMvav 156,140,76\nRa 188,2,31744\nMU 128,128,86\nMU 129,129,86\n"
"CV 96,128,129\nMU 130,130,86\nMU 131,131,86\nRa 128,2,0\nCV 97,130,131\nMU 132,132,87\nMvav 164,144,76\nMU 133,133,87\nCV 100,132,133\nMU 134,134,87\nMvav 172,148,76\n"
"Ra 132,2,1024\nMU 135,135,87\nCV 101,134,135\nWL 8\ns_add_u32 m0, s89, 131072\ns_add_u32 s46, s93, 0\nRa 136,2,2048\nMU 136,136,86\nMU 137,137,86\nMvav 180,152,76\nD1 1,16,46,0\n"
"CV 98,136,137\nMU 138,138,86\nMvav 188,156,76\nRa 140,2,3072\nMU 139,139,86\nCV 99,138,139\ns_add_u32 s5, s94, 0\nSv 96,1,16,5,0\nMU 140,140,87\nRa 144,2,4096\nMU 141,141,87\n"
"CV 102,140,141\nMvav 196,160,76\nMU 142,142,87\nMU 143,143,87\nCV 103,142,143\nMvav 204,164,76\nRa 148,2,5120\nSv 100,1,16,5,1024\nMU 144,144,86\nWL 8\nMU 145,145,86\n"
"CV 104,144,145\nRa 152,2,6144\nMU 146,146,86\nMU 147,147,86\nMvav 212,168,76\nD1 1,16,46,1024\nCV 105,146,147\nMU 148,148,87\nMvav 220,172,76\nRa 156,2,7168\nMU 149,149,87\n"
"CV 108,148,149\nMU 150,150,87\nMU 151,151,87\nCV 109,150,151\nRa 160,2,8192\nMU 152,152,86\nMU 153,153,86\nMvav 228,176,76\nCV 106,152,153\nMU 154,154,86\nMU 155,155,86\n"
"Mvav 236,180,76\nRa 164,2,9216\nCV 107,154,155\ns_add_u32 s5, s94, 131072\nWL 8\nSv 104,1,16,5,0\nMU 156,156,87\nRa 168,2,10240\nMU 157,157,87\nCV 110,156,157\nMvav 244,184,76\n"
"D1 1,16,46,2048\nMU 158,158,87\nMU 159,159,87\nMvav 252,188,76\nRa 172,2,11264\nCV 111,158,159\nSv 108,1,16,5,1024\nMU 160,160,86\nMU 161,161,86\nZaav 0,128,96\nRa 176,2,12288\n"
"CV 112,160,161\nMU 162,162,86\nMaav 0,132,104\nMU 163,163,86\nCV 113,162,163\nZaav 4,132,108\nRa 180,2,13312\nMU 164,164,87\nMaav 4,128,100\ns_waitcnt vmcnt(18) lgkmcnt(8)\n"
"MU 165,165,87\nZaav 12,136,100\nRa 184,2,14336\nCV 116,164,165\nMaav 12,140,108\nD1 1,16,46,3072\nMU 166,166,87\nZaav 8,140,104\nRa 188,2,15360\ns_barrier\nMaav 8,136,96\n"
"MU 167,167,87\nCV 117,166,167\nZaav 16,144,96\nRa 128,2,32768\nMU 168,168,86\nMU 169,169,86\nv_lshrrev_b32 v88, 8, v1\nMaav 16,148,104\nCV 114,168,169\nMU 170,170,86\n"
"v_lshlrev_b32 v89, 5, v88\nbuffer_store_dword v25, off, s[16:19], s54 offset:512 sc0 sc1\nZaav 20,148,108\nRa 132,2,33792\nMU 171,171,86\n"
"buffer_load_dwordx4 v[64:67], v89, s[16:19], s40 offen offset:0 sc0 sc1\nMaav 20,144,100\nWL 8\ns_add_u32 m0, s89, 49152\ns_add_u32 s46, s96, 1179648\nZaav 28,152,100\n"
"Ra 136,2,34816\nCV 115,170,171\nMaav 28,156,108\nD1 1,16,46,0\ns_add_u32 s5, s94, 262144\nZaav 24,156,104\nRa 140,2,35840\nSv 112,1,16,5,0\n"
"buffer_load_dwordx4 v[68:71], v89, s[16:19], s40 offen offset:16 sc0 sc1\nMaav 24,152,96\nMU 172,172,87\nMU 173,173,87\nZaav 32,160,96\nRa 144,2,36864\nCV 118,172,173\n"
"MU 174,174,87\nMaav 32,164,104\nMU 175,175,87\nCV 119,174,175\nZaav 36,164,108\nRa 148,2,37888\nSv 116,1,16,5,1024\n"
"buffer_load_dwordx4 v[72:75], v89, s[16:19], s40 offen offset:1792 sc0 sc1\nMaav 36,160,100\nWL 8\nMU 176,176,86\nZaav 44,168,100\nRa 152,2,38912\nMU 177,177,86\nMaav 44,172,108\n"
"D1 1,16,46,1024\nCV 120,176,177\nZaav 40,172,104\nRa 156,2,39936\nMU 178,178,86\nbuffer_load_dwordx4 v[76:79], v89, s[16:19], s40 offen offset:1808 sc0 sc1\nMaav 40,168,96\n"
"MU 179,179,86\nCV 121,178,179\nZaav 48,176,96\nRa 160,2,40960\nMU 180,180,87\nMU 181,181,87\nMaav 48,180,104\nCV 124,180,181\nMU 182,182,87\nZaav 52,180,108\nRa 164,2,41984\n"
"MU 183,183,87\nv_bfe_u32 v90, v1, 4, 4\nMaav 52,176,100\nWL 8\nCV 125,182,183\nv_mul_u32_u24 v91, 896, v90\nZaav 60,184,100\nRa 168,2,43008\nMU 184,184,86\n"
"v_lshl_add_u32 v91, v88, 4, v91\nMaav 60,188,108\nD1 1,16,46,2048\nMU 185,185,86\nbuffer_load_dwordx4 v[12:15], v91, s[28:31], s22 offen offset:0\nZaav 56,188,104\nRa 172,2,44032\n"
"CV 122,184,185\nMaav 56,184,96\nMU 186,186,86\nMU 187,187,86\nZaav 64,128,96\nRa 176,2,45056\nCV 123,186,187\ns_add_u32 s5, s94, 393216\nMaav 64,132,104\nSv 120,1,16,5,0\n"
"MU 188,188,87\nbuffer_load_dwordx4 v[16:19], v91, s[28:31], s23 offen offset:0\nZaav 68,132,108\nRa 180,2,46080\nMU 189,189,87\nv_add_u32 v90, s21, v90\nMaav 68,128,100\nWL 8\n"
"CV 126,188,189\nv_lshlrev_b32 v88, 2, v90\nZaav 76,136,100\nRa 184,2,47104\nMU 190,190,87\nv_lshrrev_b32 v92, 8, v1\nMaav 76,140,108\nD1 1,16,46,3072\nMU 191,191,87\n"
"v_bfe_u32 v93, v1, 6, 2\nZaav 72,140,104\nRa 188,2,48128\nCV 127,190,191\nMaav 72,136,96\nSv 124,1,16,5,1024\nv_cmp_eq_u32 vcc, v92, v93\nv_and_b32 v92, 16, v1\n"
"v_mov_b32 v93, 0x3f80\nv_cndmask_b32 v93, 0, v93, vcc\nZaav 80,144,96\nRa 128,2,49152\nMU 192,192,86\nv_lshlrev_b32 v93, v92, v93\nv_and_b32 v92, 32, v1\nMaav 80,148,104\n"
"MU 193,193,86\nCV 32,192,193\nZaav 84,148,108\nRa 132,2,50176\nMU 194,194,86\nMaav 84,144,100\nWL 8\ns_add_u32 m0, s89, 147456\ns_add_u32 s46, s93, 32768\nZaav 92,152,100\n"
"Ra 136,2,51200\nMU 195,195,86\nMaav 92,156,108\nD1 1,16,46,0\nCV 33,194,195\nZaav 88,156,104\nRa 140,2,52224\nMU 196,196,87\nMaav 88,152,96\nMU 197,197,87\nCV 36,196,197\n"
"Zaav 96,160,96\nRa 144,2,53248\nMU 198,198,87\nMaav 96,164,104\nMU 199,199,87\nv_cmp_eq_u32 vcc, 0, v92\nv_mov_b32 v4, 0\nv_mov_b32 v5, 0\nv_cndmask_b32 v6, 0, v93, vcc\n"
"Zaav 100,164,108\nRa 148,2,54272\nCV 37,198,199\nv_sub_u32 v7, v93, v6\nMaav 100,160,100\nWL 8\nMU 200,200,86\nv_mov_b32 v8, 0\nZaav 108,168,100\nRa 152,2,55296\nMU 201,201,86\n"
"v_mov_b32 v9, 0\nMaav 108,172,108\nD1 1,16,46,1024\nCV 34,200,201\nZaav 104,172,104\nRa 156,2,56320\nMU 202,202,86\nMaav 104,168,96\nMU 203,203,86\nCV 35,202,203\nMaav 0,176,112\n"
"Ra 160,2,57344\ns_add_u32 s5, s94, 524288\nMaav 0,180,120\nSv 32,1,16,5,0\nMU 204,204,87\nMaav 4,180,124\nRa 164,2,58368\nMU 205,205,87\nMaav 4,176,116\nWL 8\nCV 38,204,205\n"
"Maav 12,184,116\nRa 168,2,59392\nMU 206,206,87\nMaav 12,188,124\nD1 1,16,46,2048\nMU 207,207,87\nMaav 8,188,120\nRa 172,2,60416\nCV 39,206,207\nMaav 8,184,112\nSv 36,1,16,5,1024\n"
"MU 208,208,86\nMaav 16,128,112\nRa 176,2,61440\nMU 209,209,86\nMaav 16,132,120\nCV 40,208,209\nMaav 20,132,124\nRa 180,2,62464\nMU 210,210,86\nMaav 20,128,116\n"
"s_waitcnt vmcnt(32) lgkmcnt(8)\nMU 211,211,86\nMaav 28,136,116\nRa 184,2,63488\nCV 41,210,211\nMaav 28,140,124\nD1 1,16,46,3072\nMU 212,212,87\nMaav 24,140,120\nRa 188,2,64512\n"
"s_barrier\nMaav 24,136,112\nMU 213,213,87\nCV 44,212,213\nMaav 32,144,112\nRa 128,3,0\nMU 214,214,87\nMaav 32,148,120\nMU 215,215,87\nCV 45,214,215\nMaav 36,148,124\n"
"Ra 132,3,1024\nMU 216,216,86\nMaav 36,144,116\nWL 8\ns_add_u32 m0, s89, 81920\ns_add_u32 s46, s93, 65536\nMaav 44,152,116\nRa 136,3,2048\nMU 217,217,86\nMaav 44,156,124\n"
"D1 1,16,46,0\nCV 42,216,217\nMaav 40,156,120\nRa 140,3,3072\nMU 218,218,86\nMaav 40,152,112\nMU 219,219,86\nCV 43,218,219\nMaav 48,160,112\nRa 144,3,4096\n"
"s_add_u32 s5, s94, 655360\nMaav 48,164,120\nSv 40,1,16,5,0\nbuffer_load_dword v27, off, s[16:19], s60 offset:512 sc0 sc1\nMaav 52,164,124\nRa 148,3,5120\nMU 220,220,87\n"
"Maav 52,160,116\nWL 8\nMU 221,221,87\nMaav 60,168,116\nRa 152,3,6144\nCV 46,220,221\nMaav 60,172,124\nD1 1,16,46,1024\nMU 222,222,87\nMaav 56,172,120\nRa 156,3,7168\n"
"MU 223,223,87\nMaav 56,168,112\nCV 47,222,223\nSv 44,1,16,5,1024\nMaav 64,176,112\nRa 160,3,8192\nMU 224,224,86\nMaav 64,180,120\nMU 225,225,86\nCV 48,224,225\nMaav 68,180,124\n"
"Ra 164,3,9216\nMU 226,226,86\nMaav 68,176,116\nWL 8\nMU 227,227,86\nMaav 76,184,116\nRa 168,3,10240\nCV 49,226,227\nMaav 76,188,124\nD1 1,16,46,2048\nMU 228,228,87\n"
"Maav 72,188,120\nRa 172,3,11264\nMU 229,229,87\nMaav 72,184,112\nCV 52,228,229\nMU 230,230,87\nMaav 80,128,112\nRa 176,3,12288\nMU 231,231,87\nWV 14\nv_xor_b32 v92, s15, v66\n"
"v_xor_b32 v93, s15, v70\nv_xor_b32 v94, s15, v74\nv_or3_b32 v92, v92, v93, v94\nv_xor_b32 v93, s15, v78\nv_or_b32 v92, v92, v93\nMaav 80,132,120\nCV 53,230,231\n"
"v_cmp_ne_u32 vcc, 0, v92\ns_nop 0\ns_cbranch_vccnz .S335\n.S336:\ns_mov_b32 s2, 0xc040000\nbuffer_load_dword v80, v88, s[16:19], s2 offen sc0 sc1\nMaav 84,132,124\nRa 180,3,13312\n"
"MU 232,232,86\nMaav 84,128,116\nWL 8\nMU 233,233,86\nMaav 92,136,116\nRa 184,3,14336\nCV 50,232,233\nMaav 92,140,124\nD1 1,16,46,3072\nMU 234,234,86\n"
"buffer_load_dword v81, v88, s[16:19], s2 offen offset:256 sc0 sc1\nv_and_b32 v92, 0x7ff, v67\nv_lshrrev_b32 v93, 16, v67\nv_add_u32 v94, 64, v90\nMaav 88,140,120\nRa 188,3,15360\n"
"MU 235,235,86\nMaav 88,136,112\nCV 51,234,235\ns_add_u32 s5, s94, 786432\nMaav 96,144,112\nRa 128,3,16384\nSv 48,1,16,5,0\nMaav 96,148,120\nMU 236,236,87\nMU 237,237,87\n"
"Maav 100,148,124\nRa 132,3,17408\nCV 54,236,237\nMaav 100,144,116\nWL 8\ns_add_u32 m0, s89, 65536\ns_add_u32 s46, s93, 98304\nMaav 108,152,116\nRa 136,3,18432\nMU 238,238,87\n"
"Maav 108,156,124\nD1 1,16,46,0\nMU 239,239,87\nMaav 104,156,120\nRa 140,3,19456\nCV 55,238,239\nMaav 104,152,112\nSv 52,1,16,5,1024\nMU 240,240,86\nMaav 0,160,32\nRa 144,3,20480\n"
"MU 241,241,86\nv_cmp_eq_u32 vcc, v92, v90\ns_nop 1\nv_cndmask_b32 v12, v12, v64, vcc\nv_cndmask_b32 v13, v13, v65, vcc\nv_cndmask_b32 v14, v14, v68, vcc\n"
"v_cndmask_b32 v15, v15, v69, vcc\nMaav 0,164,40\nCV 56,240,241\nv_cmp_eq_u32 vcc, v93, v90\ns_nop 1\nv_cndmask_b32 v12, v12, v72, vcc\nv_cndmask_b32 v13, v13, v73, vcc\n"
"v_cndmask_b32 v14, v14, v76, vcc\nv_cndmask_b32 v15, v15, v77, vcc\nMaav 4,164,44\nRa 148,3,21504\nMU 242,242,86\nMaav 4,160,36\nWL 8\nMU 243,243,86\nMaav 12,168,36\n"
"Ra 152,3,22528\nCV 57,242,243\nMaav 12,172,44\nD1 1,16,46,1024\nMU 244,244,87\nMaav 8,172,40\nRa 156,3,23552\nMU 245,245,87\nMaav 8,168,32\nCV 60,244,245\nMU 246,246,87\n"
"Maav 16,176,32\nRa 160,3,24576\nMU 247,247,87\nMaav 16,180,40\nCV 61,246,247\nMU 248,248,86\nMaav 20,180,44\nRa 164,3,25600\nMU 249,249,86\nMaav 20,176,36\nWL 8\nCV 58,248,249\n"
"Maav 28,184,36\nRa 168,3,26624\nMU 250,250,86\nMaav 28,188,44\nD1 1,16,46,2048\nMU 251,251,86\nMaav 24,188,40\nRa 172,3,27648\nCV 59,250,251\nMaav 24,184,32\n"
"s_add_u32 s5, s94, 917504\nSv 56,1,16,5,0\nMaav 32,128,32\nRa 176,3,28672\nMU 252,252,87\nv_cmp_eq_u32 vcc, v92, v94\ns_nop 1\nv_cndmask_b32 v16, v16, v64, vcc\n"
"v_cndmask_b32 v17, v17, v65, vcc\nv_cndmask_b32 v18, v18, v68, vcc\nv_cndmask_b32 v19, v19, v69, vcc\nMaav 32,132,40\nMU 253,253,87\nv_cmp_eq_u32 vcc, v93, v94\ns_nop 1\n"
"v_cndmask_b32 v16, v16, v72, vcc\nv_cndmask_b32 v17, v17, v73, vcc\nv_cndmask_b32 v18, v18, v76, vcc\nv_cndmask_b32 v19, v19, v77, vcc\nMaav 36,132,44\nRa 180,3,29696\n"
"CV 62,252,253\nMaav 36,128,36\ns_waitcnt vmcnt(22) lgkmcnt(8)\nMU 254,254,87\nMaav 44,136,36\nRa 184,3,30720\nMU 255,255,87\nMaav 44,140,44\nD1 1,16,46,3072\nCV 63,254,255\n"
"Maav 40,140,40\nRa 188,3,31744\ns_barrier\nMaav 40,136,32\nSv 60,1,16,5,1024\nMaav 48,144,32\nRa 128,3,32768\nMaav 48,148,40\nMaav 52,148,44\nRa 132,3,33792\nMaav 52,144,36\nWL 8\n"
"s_add_u32 m0, s89, 98304\ns_add_u32 s46, s93, 131072\nMaav 60,152,36\nRa 136,3,34816\nMaav 60,156,44\nD1 1,16,46,0\nMaav 56,156,40\nRa 140,3,35840\nMaav 56,152,32\nMaav 64,160,32\n"
"Ra 144,3,36864\nWV 15\nv_cmp_ne_u32 vcc, s15, v27\ns_nop 0\ns_cbranch_vccnz .S338\n.S339:\nMaav 64,164,40\ns_add_u32 s4, s92, 0\nMaav 68,164,44\nRa 148,3,37888\nMaav 68,160,36\n"
"WL 8\nbuffer_load_dwordx4 a[192:195], v1, s[16:19], s4 offen offset:0 sc0 sc1\nMaav 76,168,36\nRa 152,3,38912\nMaav 76,172,44\nD1 1,16,46,1024\nMaav 72,172,40\nRa 156,3,39936\n"
"Maav 72,168,32\nbuffer_load_dwordx4 a[196:199], v1, s[16:19], s4 offen offset:1024 sc0 sc1\nMaav 80,176,32\nRa 160,3,40960\nMaav 80,180,40\ns_add_u32 s4, s92, 131072\n"
"Maav 84,180,44\nRa 164,3,41984\nMaav 84,176,36\nWL 8\nbuffer_load_dwordx4 a[200:203], v1, s[16:19], s4 offen offset:0 sc0 sc1\nMaav 92,184,36\nRa 168,3,43008\nMaav 92,188,44\n"
"D1 1,16,46,2048\nMaav 88,188,40\nRa 172,3,44032\nMaav 88,184,32\nbuffer_load_dwordx4 a[204:207], v1, s[16:19], s4 offen offset:1024 sc0 sc1\nMaav 96,128,32\nRa 176,3,45056\n"
"Maav 96,132,40\ns_add_u32 s4, s92, 262144\nMaav 100,132,44\nRa 180,3,46080\nMaav 100,128,36\nWL 8\nbuffer_load_dwordx4 a[208:211], v1, s[16:19], s4 offen offset:0 sc0 sc1\n"
"Maav 108,136,36\nRa 184,3,47104\nMaav 108,140,44\nD1 1,16,46,3072\nMaav 104,140,40\nRa 188,3,48128\nMaav 104,136,32\n"
"buffer_load_dwordx4 a[212:215], v1, s[16:19], s4 offen offset:1024 sc0 sc1\nMaav 0,144,48\nRa 128,3,49152\nMaav 0,148,56\ns_add_u32 s4, s92, 393216\nMaav 4,148,60\nRa 132,3,50176\n"
"Maav 4,144,52\nWL 8\ns_add_u32 m0, s89, 114688\ns_add_u32 s46, s93, 163840\nbuffer_load_dwordx4 a[216:219], v1, s[16:19], s4 offen offset:0 sc0 sc1\nMaav 12,152,52\n"
"Ra 136,3,51200\nMaav 12,156,60\nD1 1,16,46,0\nMaav 8,156,56\nRa 140,3,52224\nMaav 8,152,48\nbuffer_load_dwordx4 a[220:223], v1, s[16:19], s4 offen offset:1024 sc0 sc1\n"
"Maav 16,160,48\nRa 144,3,53248\nMaav 16,164,56\nMaav 20,164,60\nRa 148,3,54272\nMaav 20,160,52\nWL 8\nMaav 28,168,52\nRa 152,3,55296\nMaav 28,172,60\nD1 1,16,46,1024\n"
"Maav 24,172,56\nRa 156,3,56320\nMaav 24,168,48\nMaav 32,176,48\nRa 160,3,57344\nMaav 32,180,56\ns_add_u32 s4, s92, 524288\nMaav 36,180,60\nRa 164,3,58368\nMaav 36,176,52\nWL 8\n"
"buffer_load_dwordx4 a[224:227], v1, s[16:19], s4 offen offset:0 sc0 sc1\nMaav 44,184,52\nRa 168,3,59392\nMaav 44,188,60\nD1 1,16,46,2048\nMaav 40,188,56\nRa 172,3,60416\n"
"Maav 40,184,48\nbuffer_load_dwordx4 a[228:231], v1, s[16:19], s4 offen offset:1024 sc0 sc1\nMaav 48,128,48\nRa 176,3,61440\nMaav 48,132,56\ns_add_u32 s4, s92, 655360\n"
"Maav 52,132,60\nRa 180,3,62464\nMaav 52,128,52\nWL 8\nbuffer_load_dwordx4 a[232:235], v1, s[16:19], s4 offen offset:0 sc0 sc1\nMaav 60,136,52\nRa 184,3,63488\nMaav 60,140,60\n"
"D1 1,16,46,3072\nMaav 56,140,56\nRa 188,3,64512\ns_barrier\nMaav 56,136,48\nbuffer_load_dwordx4 a[236:239], v1, s[16:19], s4 offen offset:1024 sc0 sc1\nMaav 64,144,48\nRa 128,0,0\n"
"Maav 64,148,56\ns_add_u32 s4, s92, 786432\nMaav 68,148,60\nRa 132,0,1024\nMaav 68,144,52\nWL 8\ns_add_u32 m0, s89, 0\ns_add_u32 s46, s93, 196608\n"
"buffer_load_dwordx4 a[240:243], v1, s[16:19], s4 offen offset:0 sc0 sc1\nMaav 76,152,52\nRa 136,0,2048\nMaav 76,156,60\nD1 1,16,46,0\nMaav 72,156,56\nRa 140,0,3072\n"
"Maav 72,152,48\nbuffer_load_dwordx4 a[244:247], v1, s[16:19], s4 offen offset:1024 sc0 sc1\nMaav 80,160,48\nRa 144,0,4096\nMaav 80,164,56\ns_add_u32 s4, s92, 917504\n"
"Maav 84,164,60\nRa 148,0,5120\nMaav 84,160,52\nWL 8\nbuffer_load_dwordx4 a[248:251], v1, s[16:19], s4 offen offset:0 sc0 sc1\nMaav 92,168,52\nRa 152,0,6144\nMaav 92,172,60\n"
"D1 1,16,46,1024\nMaav 88,172,56\nRa 156,0,7168\nWV 12\nAR 22,195\nMaav 88,168,48\nAR 23,199\nv_max_u32 v21, v22, v23\n"
"buffer_load_dwordx4 a[252:255], v1, s[16:19], s4 offen offset:1024 sc0 sc1\nMaav 96,176,48\nRa 160,0,8192\nAR 22,203\nMaav 96,180,56\nAR 23,207\nv_max3_u32 v21, v21, v22, v23\n"
"Maav 100,180,60\nRa 164,0,9216\nAR 22,211\nMaav 100,176,52\nWL 6\nAR 23,215\nv_max3_u32 v21, v21, v22, v23\nMaav 108,184,52\nRa 168,0,10240\nAR 22,219\nMaav 108,188,60\n"
"D1 1,16,46,2048\nAR 23,223\nMaav 104,188,56\nRa 172,0,11264\nv_max3_u32 v21, v21, v22, v23\nMaav 104,184,48\nv_cmp_eq_u32 vcc, -1, v21\ns_nop 0\ns_cbranch_vccnz .S329\n.S330:\n"
"Maaa 0,128,192\nRa 176,0,12288\nMaaa 0,132,200\nMaaa 0,136,208\nRa 180,0,13312\nMaaa 0,140,216\nWL 6\nMaaa 4,140,220\nRa 184,0,14336\nMaaa 4,136,212\nD1 1,16,46,3072\n"
"Maaa 4,132,204\nRa 188,0,15360\nMaaa 4,128,196\nMaaa 8,144,192\nRa 128,0,16384\nMaaa 8,148,200\nMaaa 8,152,208\nRa 132,0,17408\nMaaa 8,156,216\nWL 6\nMaaa 12,156,220\n"
"Ra 136,0,18432\nMaaa 12,152,212\nMaaa 12,148,204\nRa 140,0,19456\nMaaa 12,144,196\nMaaa 16,160,192\nRa 144,0,20480\nMaaa 16,164,200\nMaaa 16,168,208\nRa 148,0,21504\n"
"Maaa 16,172,216\nWL 6\nMaaa 20,172,220\nRa 152,0,22528\nMaaa 20,168,212\nMaaa 20,164,204\nRa 156,0,23552\nWV 2\nMaaa 20,160,196\nAR 22,227\nAR 23,231\nMaaa 24,176,192\n"
"Ra 160,0,24576\nv_max_u32 v21, v22, v23\nAR 22,235\nMaaa 24,180,200\nAR 23,239\nv_max3_u32 v21, v21, v22, v23\nMaaa 24,184,208\nRa 164,0,25600\nAR 22,243\nMaaa 24,188,216\nWL 6\n"
"AR 23,247\nMaaa 28,188,220\nRa 168,0,26624\nv_max3_u32 v21, v21, v22, v23\nMaaa 28,184,212\nAR 22,251\nAR 23,255\nMaaa 28,180,204\nRa 172,0,27648\nv_max3_u32 v21, v21, v22, v23\n"
"Maaa 28,176,196\nv_cmp_eq_u32 vcc, -1, v21\ns_nop 0\ns_cbranch_vccnz .S332\n.S333:\nMaaa 0,128,224\nRa 176,0,28672\nMaaa 0,132,232\nMaaa 0,136,240\nRa 180,0,29696\nMaaa 0,140,248\n"
"WL 6\nMaaa 4,140,252\nRa 184,0,30720\nMaaa 4,136,244\nMaaa 4,132,236\nRa 188,0,31744\ns_barrier\nMaaa 4,128,228\nMaaa 8,144,224\nRa 128,2,16384\nMaaa 8,148,232\nMaaa 8,152,240\n"
"Ra 132,2,17408\nv_sub_f32 v92, 1.0, v80\nMaaa 8,156,248\nWL 6\nMaaa 12,156,252\nRa 136,2,18432\nMaaa 12,152,244\nv_sub_f32 v93, 1.0, v81\nCV 92,92,93\nMaaa 12,148,236\n"
"Ra 140,2,19456\nMaaa 12,144,228\nv_and_b32 v93, 0xffff0000, v92\nAR 82,0\nMaaa 16,160,224\nRa 144,2,20480\nv_lshlrev_b32 v92, 16, v92\nMaaa 16,164,232\nv_lshlrev_b32 v64, 16, v12\n"
"AR 83,1\nMaaa 16,168,240\nRa 148,2,21504\nCV 128,82,83\nMaaa 16,172,248\nWL 6\nAR 82,2\nMaaa 20,172,252\nRa 152,2,22528\nv_and_b32 v65, 0xffff0000, v12\nMaaa 20,168,244\n"
"MU 64,64,92\nAR 83,3\nMaaa 20,164,236\nRa 156,2,23552\nCV 129,82,83\nMaaa 20,160,228\nMU 65,65,92\nv_lshlrev_b32 v66, 16, v13\nMaaa 24,176,224\nRa 160,2,24576\nAR 82,8\n"
"Maaa 24,180,232\nv_and_b32 v67, 0xffff0000, v13\nAR 83,9\nMaaa 24,184,240\nRa 164,2,25600\nMU 66,66,92\nMaaa 24,188,248\nWL 6\nCV 130,82,83\nMaaa 28,188,252\nRa 168,2,26624\n"
"AR 82,10\nMaaa 28,184,244\nMU 67,67,92\nv_lshlrev_b32 v68, 16, v14\nMaaa 28,180,236\nRa 172,2,27648\nAR 83,11\nMaaa 28,176,228\nv_and_b32 v69, 0xffff0000, v14\nCV 131,82,83\n"
"Maaa 32,128,192\nRa 176,2,28672\nMU 68,68,92\nMaaa 32,132,200\nv_bfe_u32 v24, v1, 4, 3\ns_add_u32 s5, s95, 0\nMaaa 32,136,208\nRa 180,2,29696\nSv 128,1,16,5,0\nMaaa 32,140,216\n"
"WL 6\nAR 82,4\nMaaa 36,140,220\nRa 184,2,30720\nMU 69,69,92\nMaaa 36,136,212\nv_mul_u32_u24 v24, 0xc00, v24\nAR 83,5\nMaaa 36,132,204\nRa 188,2,31744\nCV 132,82,83\n"
"Maaa 36,128,196\nv_bfe_u32 v26, v1, 7, 1\nv_lshlrev_b32 v70, 16, v15\nMaaa 36,144,228\nRa 128,2,0\nAR 82,6\nMaaa 36,148,236\nv_add_lshl_u32 v24, v24, v26, 13\nAR 83,7\n"
"Maaa 36,152,244\nRa 132,2,1024\nv_and_b32 v71, 0xffff0000, v15\nMaaa 36,156,252\nWL 6\nCV 133,82,83\nMaaa 32,156,248\nRa 136,2,2048\nAR 82,12\nMaaa 32,152,240\n"
"buffer_load_dword v27, v24, s[16:19], s41 offen sc0 sc1\nMU 70,70,92\nMaaa 32,148,232\nRa 140,2,3072\nAR 83,13\nMaaa 32,144,224\nMU 71,71,92\nCV 134,82,83\nMaaa 40,160,192\n"
"Ra 144,2,4096\nv_lshlrev_b32 v72, 16, v16\nMaaa 40,164,200\nv_and_b32 v73, 0xffff0000, v16\nAR 82,14\nMaaa 40,168,208\nRa 148,2,5120\nAR 83,15\nMaaa 40,172,216\nWL 6\n"
"CV 135,82,83\nMaaa 44,172,220\nRa 152,2,6144\nMU 72,72,93\nMaaa 44,168,212\nMU 73,73,93\nSv 132,1,16,5,1024\nMaaa 44,164,204\nRa 156,2,7168\nAR 82,16\nMaaa 44,160,196\n"
"v_lshlrev_b32 v74, 16, v17\nv_and_b32 v75, 0xffff0000, v17\nMaaa 44,176,228\nRa 160,2,8192\nAR 83,17\nMaaa 44,180,236\nMU 74,74,93\nCV 128,82,83\nMaaa 44,184,244\nRa 164,2,9216\n"
"MU 75,75,93\nMaaa 44,188,252\nWL 6\nAR 82,18\nMaaa 40,188,248\nRa 168,2,10240\nAR 83,19\nMaaa 40,184,240\nv_lshlrev_b32 v76, 16, v18\nv_and_b32 v77, 0xffff0000, v18\n"
"Maaa 40,180,232\nRa 172,2,11264\nCV 129,82,83\nMaaa 40,176,224\nMU 76,76,93\nAR 82,24\nMaaa 48,128,192\nRa 176,2,12288\nMU 77,77,93\nMaaa 48,132,200\nv_lshlrev_b32 v78, 16, v19\n"
"AR 83,25\nMaaa 48,136,208\nRa 180,2,13312\nCV 130,82,83\nMaaa 48,140,216\nWL 6\nAR 82,26\nMaaa 52,140,220\nRa 184,2,14336\nv_and_b32 v79, 0xffff0000, v19\nMaaa 52,136,212\n"
"MU 78,78,93\nAR 83,27\nMaaa 52,132,204\nRa 188,2,15360\ns_barrier\nMaaa 52,128,196\nMU 79,79,93\nCV 131,82,83\nMaaa 52,144,228\nRa 128,2,32768\ns_add_u32 s5, s95, 131072\n"
"Maaa 52,148,236\nSv 128,1,16,5,0\nAR 82,20\nMaaa 52,152,244\nRa 132,2,33792\nAR 83,21\nMaaa 52,156,252\nWL 6\nCV 132,82,83\nMaaa 48,156,248\nRa 136,2,34816\nAR 82,22\n"
"Maaa 48,152,240\nAR 83,23\nCV 133,82,83\nMaaa 48,148,232\nRa 140,2,35840\nAR 82,28\nMaaa 48,144,224\nAR 83,29\nCV 134,82,83\nMaaa 56,160,192\nRa 144,2,36864\nAR 82,30\n"
"Maaa 56,164,200\nAR 83,31\nCV 135,82,83\nMaaa 56,168,208\nRa 148,2,37888\nSv 132,1,16,5,1024\nMaaa 56,172,216\nWL 6\nAR 82,32\nMaaa 60,172,220\nRa 152,2,38912\nAR 83,33\n"
"Maaa 60,168,212\nCV 128,82,83\nAR 82,34\nMaaa 60,164,204\nRa 156,2,39936\nAR 83,35\nMaaa 60,160,196\nCV 129,82,83\nAR 82,40\nMaaa 60,176,228\nRa 160,2,40960\nAR 83,41\n"
"Maaa 60,180,236\nCV 130,82,83\nAR 82,42\nMaaa 60,184,244\nRa 164,2,41984\nAR 83,43\nMaaa 60,188,252\nWL 6\nCV 131,82,83\nMaaa 56,188,248\nRa 168,2,43008\n"
"s_add_u32 s5, s95, 262144\nMaaa 56,184,240\nSv 128,1,16,5,0\nAR 82,36\nMaaa 56,180,232\nRa 172,2,44032\nAR 83,37\nMaaa 56,176,224\nCV 132,82,83\nAR 82,38\nMaaa 64,128,192\n"
"Ra 176,2,45056\nAR 83,39\nMaaa 64,132,200\nCV 133,82,83\nAR 82,44\nMaaa 64,136,208\nRa 180,2,46080\nAR 83,45\nMaaa 64,140,216\nWL 6\nCV 134,82,83\nMaaa 68,140,220\nRa 184,2,47104\n"
"AR 82,46\nMaaa 68,136,212\nAR 83,47\nCV 135,82,83\nMaaa 68,132,204\nRa 188,2,48128\nSv 132,1,16,5,1024\nMaaa 68,128,196\nAR 82,48\nAR 83,49\nMaaa 68,144,228\nRa 128,2,49152\n"
"CV 128,82,83\nMaaa 68,148,236\nAR 82,50\nAR 83,51\nMaaa 68,152,244\nRa 132,2,50176\nCV 129,82,83\nMaaa 68,156,252\nWL 6\nAR 82,56\nMaaa 64,156,248\nRa 136,2,51200\nAR 83,57\n"
"Maaa 64,152,240\nCV 130,82,83\nAR 82,58\nMaaa 64,148,232\nRa 140,2,52224\nAR 83,59\nMaaa 64,144,224\nCV 131,82,83\ns_add_u32 s5, s95, 393216\nMaaa 72,160,192\nRa 144,2,53248\n"
"WV 5\nv_cmp_eq_u32 s[62:63], s15, v27\ns_mov_b32 vcc_hi, 0\ns_nop 2\ns_lshr_b32 s63, s62, 8\ns_or_b32 s62, s62, s63\ns_andn2_b32 vcc_lo, 0xff, s62\ns_mov_b64 vcc, vcc\n"
"s_cbranch_vccnz .S341\n.S342:\nMaaa 72,164,200\ns_bitcmp1_b32 s63, 0\ns_cselect_b32 s2, 0x2000, 0\nMaaa 72,168,208\nRa 148,2,54272\nSv 128,1,16,5,0\nMaaa 72,172,216\nWL 6\n"
"AR 82,52\nMaaa 76,172,220\nRa 152,2,55296\nAR 83,53\nMaaa 76,168,212\ns_xor_b32 s2, s2, s20\nCV 132,82,83\nMaaa 76,164,204\nRa 156,2,56320\nAR 82,54\nMaaa 76,160,196\n"
"s_add_u32 s24, s2, 0x0\ns_bitcmp1_b32 s63, 1\ns_cselect_b32 s2, 0x2000, 0\nMaaa 76,176,228\nRa 160,2,57344\nAR 83,55\nMaaa 76,180,236\ns_xor_b32 s2, s2, s20\nCV 133,82,83\n"
"Maaa 76,184,244\nRa 164,2,58368\nAR 82,60\nMaaa 76,188,252\nWL 6\nAR 83,61\nMaaa 72,188,248\nRa 168,2,59392\nCV 134,82,83\nMaaa 72,184,240\ns_add_u32 s25, s2, 0x1800000\n"
"s_bitcmp1_b32 s63, 2\ns_cselect_b32 s2, 0x2000, 0\nMaaa 72,180,232\nRa 172,2,60416\nAR 82,62\nMaaa 72,176,224\ns_xor_b32 s2, s2, s20\nAR 83,63\nMaaa 80,128,192\nRa 176,2,61440\n"
"CV 135,82,83\nMaaa 80,132,200\ns_add_u32 s26, s2, 0x3000000\ns_bitcmp1_b32 s63, 3\ns_cselect_b32 s2, 0x2000, 0\nMaaa 80,136,208\nRa 180,2,62464\nSv 132,1,16,5,1024\n"
"Maaa 80,140,216\nWL 6\nAR 82,64\nMaaa 84,140,220\nRa 184,2,63488\nAR 83,65\nMaaa 84,136,212\ns_xor_b32 s2, s2, s20\nCV 128,82,83\nMaaa 84,132,204\nRa 188,2,64512\ns_barrier\n"
"Maaa 84,128,196\ns_add_u32 s27, s2, 0x4800000\ns_bitcmp1_b32 s63, 4\ns_cselect_b32 s2, 0x2000, 0\nMaaa 84,144,228\nRa 128,3,0\nAR 82,66\nMaaa 84,148,236\ns_xor_b32 s2, s2, s20\n"
"AR 83,67\nMaaa 84,152,244\nRa 132,3,1024\nCV 129,82,83\nMaaa 84,156,252\nWL 6\nAR 82,72\nMaaa 80,156,248\nRa 136,3,2048\nAR 83,73\nMaaa 80,152,240\ns_add_u32 s32, s2, 0x6000000\n"
"s_bitcmp1_b32 s63, 5\ns_cselect_b32 s2, 0x2000, 0\nMaaa 80,148,232\nRa 140,3,3072\nCV 130,82,83\nMaaa 80,144,224\ns_xor_b32 s2, s2, s20\nAR 82,74\nMaaa 88,160,192\nRa 144,3,4096\n"
"AR 83,75\nMaaa 88,164,200\ns_add_u32 s33, s2, 0x7800000\ns_bitcmp1_b32 s63, 6\ns_cselect_b32 s2, 0x2000, 0\nMaaa 88,168,208\nRa 148,3,5120\nCV 131,82,83\nMaaa 88,172,216\nWL 6\n"
"s_add_u32 s5, s95, 524288\nMaaa 92,172,220\nRa 152,3,6144\nSv 128,1,16,5,0\nMaaa 92,168,212\ns_xor_b32 s2, s2, s20\nAR 82,68\nMaaa 92,164,204\nRa 156,3,7168\nAR 83,69\n"
"Maaa 92,160,196\ns_add_u32 s34, s2, 0x9000000\ns_bitcmp1_b32 s63, 7\ns_cselect_b32 s2, 0x2000, 0\nMaaa 92,176,228\nRa 160,3,8192\nCV 132,82,83\nMaaa 92,180,236\n"
"s_xor_b32 s2, s2, s20\nAR 82,70\nMaaa 92,184,244\nRa 164,3,9216\nAR 83,71\nMaaa 92,188,252\nWL 6\nCV 133,82,83\nMaaa 88,188,248\nRa 168,3,10240\nAR 82,76\nMaaa 88,184,240\n"
"s_add_u32 s35, s2, 0xa800000\nAR 83,77\nMaaa 88,180,232\nRa 172,3,11264\nCV 134,82,83\nMaaa 88,176,224\nAR 82,78\nAR 83,79\nMaaa 96,128,192\nRa 176,3,12288\nCV 135,82,83\n"
"Maaa 96,132,200\nSv 132,1,16,5,1024\nMaaa 96,136,208\nRa 180,3,13312\nMaaa 96,140,216\nWL 6\nAR 82,80\nMaaa 100,140,220\nRa 184,3,14336\nAR 83,81\nMaaa 100,136,212\nCV 128,82,83\n"
"AR 82,82\nMaaa 100,132,204\nRa 188,3,15360\nAR 83,83\nMaaa 100,128,196\nCV 129,82,83\nAR 82,88\nMaaa 100,144,228\nAR 83,89\nCV 130,82,83\nMaaa 100,148,236\nAR 82,90\nAR 83,91\n"
"Maaa 100,152,244\nCV 131,82,83\ns_add_u32 s5, s95, 655360\nMaaa 100,156,252\nWL 4\nSv 128,1,16,5,0\nMaaa 96,156,248\nAR 82,84\nAR 83,85\nMaaa 96,152,240\nCV 132,82,83\nAR 82,86\n"
"Maaa 96,148,232\nAR 83,87\nCV 133,82,83\nMaaa 96,144,224\nAR 82,92\nAR 83,93\nMaaa 104,160,192\nCV 134,82,83\nAR 82,94\nMaaa 104,164,200\nAR 83,95\nCV 135,82,83\nMaaa 104,168,208\n"
"Sv 132,1,16,5,1024\nMaaa 104,172,216\nWL 0\nMaaa 108,172,220\nMaaa 108,168,212\nMaaa 108,164,204\nMaaa 108,160,196\nMaaa 108,176,228\nMaaa 108,180,236\nMaaa 108,184,244\n"
"Maaa 108,188,252\nMaaa 104,188,248\nMaaa 104,184,240\nMaaa 104,180,232\nMaaa 104,176,224\nAR 82,96\nAR 83,97\nCV 128,82,83\nAR 82,98\nAR 83,99\nCV 129,82,83\ns_nop 1\nAR 82,104\n"
"AR 83,105\nCV 130,82,83\nAR 82,106\nAR 83,107\nCV 131,82,83\ns_add_u32 s5, s95, 786432\nSv 128,1,16,5,0\nAR 82,100\nAR 83,101\nCV 132,82,83\nAR 82,102\nAR 83,103\nCV 133,82,83\n"
"AR 82,108\nAR 83,109\nCV 134,82,83\nAR 82,110\nAR 83,111\nCV 135,82,83\nSv 132,1,16,5,1024\ns_cmp_eq_u32 s13, 0\ns_cbranch_scc1 .Z344\n"
"buffer_load_dwordx4 v[96:99], v1, s[16:19], s24 offen offset:0 sc0 sc1\nbuffer_load_dwordx4 v[100:103], v1, s[16:19], s24 offen offset:1024 sc0 sc1\n"
"buffer_load_dwordx4 v[104:107], v1, s[16:19], s25 offen offset:0 sc0 sc1\nbuffer_load_dwordx4 v[108:111], v1, s[16:19], s25 offen offset:1024 sc0 sc1\n"
"buffer_load_dwordx4 v[112:115], v1, s[16:19], s26 offen offset:0 sc0 sc1\nbuffer_load_dwordx4 v[116:119], v1, s[16:19], s26 offen offset:1024 sc0 sc1\n"
"buffer_load_dwordx4 v[120:123], v1, s[16:19], s27 offen offset:0 sc0 sc1\nbuffer_load_dwordx4 v[124:127], v1, s[16:19], s27 offen offset:1024 sc0 sc1\n"
"buffer_load_dwordx4 v[32:35], v1, s[16:19], s32 offen offset:0 sc0 sc1\nbuffer_load_dwordx4 v[36:39], v1, s[16:19], s32 offen offset:1024 sc0 sc1\n"
"buffer_load_dwordx4 v[40:43], v1, s[16:19], s33 offen offset:0 sc0 sc1\nbuffer_load_dwordx4 v[44:47], v1, s[16:19], s33 offen offset:1024 sc0 sc1\n"
"buffer_load_dwordx4 v[48:51], v1, s[16:19], s34 offen offset:0 sc0 sc1\nbuffer_load_dwordx4 v[52:55], v1, s[16:19], s34 offen offset:1024 sc0 sc1\n"
"buffer_load_dwordx4 v[56:59], v1, s[16:19], s35 offen offset:0 sc0 sc1\nbuffer_load_dwordx4 v[60:63], v1, s[16:19], s35 offen offset:1024 sc0 sc1\ns_mov_b32 s7, 262144\nWV 0\n"
".Z347:\nv_max_u32 v82, v99, v103\nv_cmp_eq_u32 s[44:45], -1, v82\nv_max_u32 v82, v107, v111\nv_cmp_eq_u32 s[46:47], -1, v82\nv_max_u32 v82, v115, v119\n"
"v_cmp_eq_u32 s[48:49], -1, v82\nv_max_u32 v82, v123, v127\nv_cmp_eq_u32 s[50:51], -1, v82\nv_max_u32 v82, v35, v39\nv_cmp_eq_u32 s[52:53], -1, v82\nv_max_u32 v82, v43, v47\n"
"v_cmp_eq_u32 s[54:55], -1, v82\nv_max_u32 v82, v51, v55\nv_cmp_eq_u32 s[56:57], -1, v82\nv_max_u32 v82, v59, v63\nv_cmp_eq_u32 s[58:59], -1, v82\n"
"s_or_b64 s[0:1], s[44:45], s[46:47]\ns_or_b64 s[2:3], s[48:49], s[50:51]\ns_or_b64 s[0:1], s[0:1], s[52:53]\ns_or_b64 s[2:3], s[2:3], s[54:55]\ns_or_b64 s[0:1], s[0:1], s[56:57]\n"
"s_or_b64 s[2:3], s[2:3], s[58:59]\ns_or_b64 s[0:1], s[0:1], s[2:3]\ns_cbranch_scc1 .Z345\n.Z346:\nZvvv 136,6,96\nMvvv 136,6,104\nMvvv 136,6,112\nMvvv 136,6,120\nMvvv 136,6,32\n"
"Mvvv 136,6,40\nMvvv 136,6,48\nMvvv 136,6,56\nZvvv 140,4,96\nMvvv 140,4,104\nMvvv 140,4,112\nMvvv 140,4,120\nMvvv 140,4,32\nMvvv 140,4,40\nMvvv 140,4,48\nMvvv 140,4,56\n"
"Zvvv 144,6,100\nMvvv 144,6,108\nMvvv 144,6,116\nMvvv 144,6,124\nMvvv 144,6,36\nMvvv 144,6,44\nMvvv 144,6,52\nMvvv 144,6,60\nZvvv 148,4,100\nMvvv 148,4,108\nMvvv 148,4,116\n"
"Mvvv 148,4,124\nMvvv 148,4,36\nMvvv 148,4,44\nMvvv 148,4,52\nMvvv 148,4,60\nFM 64,80,136\nFM 65,80,137\nFM 66,80,138\nFM 67,80,139\nFM 68,80,140\nFM 69,80,141\nFM 70,80,142\n"
"FM 71,80,143\nCV 12,64,65\nCV 13,66,67\nCV 14,68,69\nCV 15,70,71\nSv 12,91,36,22,0\nFM 72,81,144\nFM 73,81,145\nFM 74,81,146\nFM 75,81,147\nFM 76,81,148\nFM 77,81,149\n"
"FM 78,81,150\nFM 79,81,151\nCV 16,72,73\nCV 17,74,75\nCV 18,76,77\nCV 19,78,79\nSv 16,91,36,23,0\n.Z344:\ns_endpgm\n.HD241:\nv_cmp_ne_u32 vcc, s15, v27\ns_nop 0\n"
"s_cbranch_vccnz .S356\n.S357:\nbuffer_store_dword v25, off, s[16:19], s54 offset:4 sc0 sc1\nv_and_b32 v27, 0x70, v1\n"
"buffer_load_dword v27, v27, s[16:19], s61 offen offset:12 sc0 sc1\ns_mov_b32 s90, 1\ns_swappc_b64 s[8:9], s[80:81]\nv_cmp_ne_u32 vcc, s15, v27\ns_nop 0\ns_cbranch_vccnz .S359\n"
".S360:\nv_and_b32 v27, 0x70, v1\nbuffer_load_dword v27, v27, s[16:19], s61 offen offset:8 sc0 sc1\ns_mov_b32 s90, 1\ns_swappc_b64 s[8:9], s[80:81]\nv_cmp_ne_u32 vcc, s15, v27\n"
"s_nop 0\ns_cbranch_vccnz .S362\n.S363:\nv_and_b32 v27, 0x1f0, v1\nbuffer_load_dword v27, v27, s[16:19], s58 offen offset:4 sc0 sc1\ns_mov_b32 s90, 1\n"
"s_swappc_b64 s[8:9], s[80:81]\nv_cmp_ne_u32 vcc, s15, v27\ns_nop 0\ns_cbranch_vccnz .S365\n.S366:\ns_mov_b32 s90, 100\ns_swappc_b64 s[8:9], s[80:81]\n.S242:\ns_mov_b32 s7, 262144\n"
".S244:\ns_lshl_b32 s5, s73, 8\ns_add_u32 s5, s5, 0xc04ac00\nv_lshrrev_b32 v27, 2, v1\nbuffer_load_dword v27, v27, s[16:19], s5 offen sc0 sc1\nWV 0\nv_xor_b32 v27, s15, v27\n"
"v_and_b32 v32, 0xffffdfff, v27\nv_cmp_ne_u32 vcc, 0, v32\ns_nop 0\ns_cbranch_vccz .S243\ns_sleep 6\ns_sub_u32 s7, s7, 1\ns_cbranch_scc0 .S244\ns_branch .S243\n.E250:\n"
"v_cmpx_gt_i32 vcc, 19, v0\nv_mov_b32 v19, s97\nv_cmpx_gt_i32 vcc, 18, v0\nv_mov_b32 v18, s97\nv_cmpx_gt_i32 vcc, 17, v0\nv_mov_b32 v17, s97\nv_cmpx_gt_i32 vcc, 16, v0\n"
"v_mov_b32 v16, s97\nv_cmpx_gt_i32 vcc, 3, v0\nv_mov_b32 v11, s97\nv_cmpx_gt_i32 vcc, 2, v0\nv_mov_b32 v10, s97\nv_cmpx_gt_i32 vcc, 1, v0\nv_mov_b32 v9, s97\n"
"v_cmpx_gt_i32 vcc, 0, v0\nv_mov_b32 v8, s97\ns_mov_b64 exec, -1\ns_nop 2\ns_branch .E251\n.E252:\ns_cmp_eq_u32 s99, 0\ns_cbranch_scc1 .K249\nv_cmpx_gt_i32 vcc, 19, v3\n"
"v_mov_b32 v15, s97\nv_cmpx_gt_i32 vcc, 18, v3\nv_mov_b32 v14, s97\nv_cmpx_gt_i32 vcc, 17, v3\nv_mov_b32 v13, s97\nv_cmpx_gt_i32 vcc, 16, v3\nv_mov_b32 v12, s97\n"
"v_cmpx_gt_i32 vcc, 3, v3\nv_mov_b32 v7, s97\nv_cmpx_gt_i32 vcc, 2, v3\nv_mov_b32 v6, s97\nv_cmpx_gt_i32 vcc, 1, v3\nv_mov_b32 v5, s97\nv_cmpx_gt_i32 vcc, 0, v3\nv_mov_b32 v4, s97\n"
"s_mov_b64 exec, -1\ns_nop 2\ns_branch .E253\n.K249:\nv_cmpx_gt_i32 vcc, 19, v3\nv_mov_b32 v19, s97\nv_cmpx_gt_i32 vcc, 18, v3\nv_mov_b32 v18, s97\nv_cmpx_gt_i32 vcc, 17, v3\n"
"v_mov_b32 v17, s97\nv_cmpx_gt_i32 vcc, 16, v3\nv_mov_b32 v16, s97\nv_cmpx_gt_i32 vcc, 3, v3\nv_mov_b32 v11, s97\nv_cmpx_gt_i32 vcc, 2, v3\nv_mov_b32 v10, s97\n"
"v_cmpx_gt_i32 vcc, 1, v3\nv_mov_b32 v9, s97\nv_cmpx_gt_i32 vcc, 0, v3\nv_mov_b32 v8, s97\ns_mov_b64 exec, -1\ns_nop 2\ns_branch .E253\n.E254:\ns_swappc_b64 s[80:81], s[8:9]\n"
"s_branch .E255\n.T256:\ns_mov_b32 s62, 1\ns_call_b64 s[80:81], .RA238\ns_branch .T257\n.T258:\ns_mov_b32 s62, 1\ns_call_b64 s[80:81], .RA238\ns_branch .T259\n.S260:\n"
"s_mov_b32 s7, 262144\n.S262:\nbuffer_load_dword v27, off, s[16:19], s5 sc0 sc1\nWV 0\nv_cmp_ne_u32 vcc, s15, v27\ns_nop 0\ns_cbranch_vccz .S261\ns_sleep 6\ns_sub_u32 s7, s7, 1\n"
"s_cbranch_scc0 .S262\ns_branch .S261\n.S263:\ns_mov_b32 s7, 262144\n.S265:\nv_and_b32 v27, 0x70, v1\nbuffer_load_dword v27, v27, s[16:19], s61 offen offset:128 sc0 sc1\nWV 0\n"
"v_cmp_ne_u32 vcc, s15, v27\ns_nop 0\ns_cbranch_vccz .S264\ns_sleep 6\ns_sub_u32 s7, s7, 1\ns_cbranch_scc0 .S265\ns_branch .S264\n.E267:\nv_mov_b32 v4, s97\nv_mov_b32 v5, s97\n"
"v_mov_b32 v6, s97\nv_mov_b32 v7, s97\nv_mov_b32 v12, s97\nv_mov_b32 v13, s97\nv_mov_b32 v14, s97\nv_mov_b32 v15, s97\nv_cmpx_gt_i32 vcc, 19, v3\nv_mov_b32 v19, s97\n"
"v_cmpx_gt_i32 vcc, 18, v3\nv_mov_b32 v18, s97\nv_cmpx_gt_i32 vcc, 17, v3\nv_mov_b32 v17, s97\nv_cmpx_gt_i32 vcc, 16, v3\nv_mov_b32 v16, s97\nv_cmpx_gt_i32 vcc, 3, v3\n"
"v_mov_b32 v11, s97\nv_cmpx_gt_i32 vcc, 2, v3\nv_mov_b32 v10, s97\nv_cmpx_gt_i32 vcc, 1, v3\nv_mov_b32 v9, s97\nv_cmpx_gt_i32 vcc, 0, v3\nv_mov_b32 v8, s97\ns_mov_b64 exec, -1\n"
"s_nop 2\ns_branch .E268\n.S269:\ns_mov_b32 s7, 262144\n.S271:\nv_and_b32 v27, 0x70, v1\nbuffer_load_dword v27, v27, s[16:19], s61 offen offset:136 sc0 sc1\nWV 0\n"
"v_cmp_ne_u32 vcc, s15, v27\ns_nop 0\ns_cbranch_vccz .S270\ns_sleep 6\ns_sub_u32 s7, s7, 1\ns_cbranch_scc0 .S271\ns_branch .S270\n.E274:\nv_cmpx_gt_i32 vcc, 19, v0\n"
"v_mov_b32 v15, s97\nv_cmpx_gt_i32 vcc, 18, v0\nv_mov_b32 v14, s97\nv_cmpx_gt_i32 vcc, 17, v0\nv_mov_b32 v13, s97\nv_cmpx_gt_i32 vcc, 16, v0\nv_mov_b32 v12, s97\n"
"v_cmpx_gt_i32 vcc, 3, v0\nv_mov_b32 v7, s97\nv_cmpx_gt_i32 vcc, 2, v0\nv_mov_b32 v6, s97\nv_cmpx_gt_i32 vcc, 1, v0\nv_mov_b32 v5, s97\nv_cmpx_gt_i32 vcc, 0, v0\nv_mov_b32 v4, s97\n"
"s_mov_b64 exec, -1\ns_nop 2\ns_branch .E275\n.E276:\ns_cmp_eq_u32 s99, 0\ns_cbranch_scc1 .K273\nv_cmpx_gt_i32 vcc, 19, v3\nv_mov_b32 v15, s97\nv_cmpx_gt_i32 vcc, 18, v3\n"
"v_mov_b32 v14, s97\nv_cmpx_gt_i32 vcc, 17, v3\nv_mov_b32 v13, s97\nv_cmpx_gt_i32 vcc, 16, v3\nv_mov_b32 v12, s97\nv_cmpx_gt_i32 vcc, 3, v3\nv_mov_b32 v7, s97\n"
"v_cmpx_gt_i32 vcc, 2, v3\nv_mov_b32 v6, s97\nv_cmpx_gt_i32 vcc, 1, v3\nv_mov_b32 v5, s97\nv_cmpx_gt_i32 vcc, 0, v3\nv_mov_b32 v4, s97\ns_mov_b64 exec, -1\ns_nop 2\ns_branch .E277\n"
".K273:\nv_cmpx_gt_i32 vcc, 19, v3\nv_mov_b32 v19, s97\nv_cmpx_gt_i32 vcc, 18, v3\nv_mov_b32 v18, s97\nv_cmpx_gt_i32 vcc, 17, v3\nv_mov_b32 v17, s97\nv_cmpx_gt_i32 vcc, 16, v3\n"
"v_mov_b32 v16, s97\nv_cmpx_gt_i32 vcc, 3, v3\nv_mov_b32 v11, s97\nv_cmpx_gt_i32 vcc, 2, v3\nv_mov_b32 v10, s97\nv_cmpx_gt_i32 vcc, 1, v3\nv_mov_b32 v9, s97\n"
"v_cmpx_gt_i32 vcc, 0, v3\nv_mov_b32 v8, s97\ns_mov_b64 exec, -1\ns_nop 2\ns_branch .E277\n.E278:\ns_swappc_b64 s[80:81], s[8:9]\ns_branch .E279\n.T280:\ns_mov_b32 s62, 0\n"
"s_call_b64 s[80:81], .RB239\ns_branch .T281\n.T282:\ns_mov_b32 s62, 0\ns_call_b64 s[80:81], .RA238\ns_branch .T283\n.E285:\ns_swappc_b64 s[80:81], s[8:9]\ns_branch .E286\n.T287:\n"
"s_mov_b32 s62, 0\ns_call_b64 s[80:81], .RB239\ns_branch .T288\n.T289:\ns_mov_b32 s62, 0\ns_call_b64 s[80:81], .RA238\ns_branch .T290\n.E291:\nv_cmpx_gt_i32 vcc, 19, v3\n"
"v_mov_b32 v15, s97\nv_cmpx_gt_i32 vcc, 18, v3\nv_mov_b32 v14, s97\nv_cmpx_gt_i32 vcc, 17, v3\nv_mov_b32 v13, s97\nv_cmpx_gt_i32 vcc, 16, v3\nv_mov_b32 v12, s97\n"
"v_cmpx_gt_i32 vcc, 3, v3\nv_mov_b32 v7, s97\nv_cmpx_gt_i32 vcc, 2, v3\nv_mov_b32 v6, s97\nv_cmpx_gt_i32 vcc, 1, v3\nv_mov_b32 v5, s97\nv_cmpx_gt_i32 vcc, 0, v3\nv_mov_b32 v4, s97\n"
"s_mov_b64 exec, -1\ns_nop 2\ns_branch .E292\n.E294:\nv_cmpx_gt_i32 vcc, 19, v0\nv_mov_b32 v15, s97\nv_cmpx_gt_i32 vcc, 18, v0\nv_mov_b32 v14, s97\nv_cmpx_gt_i32 vcc, 17, v0\n"
"v_mov_b32 v13, s97\nv_cmpx_gt_i32 vcc, 16, v0\nv_mov_b32 v12, s97\nv_cmpx_gt_i32 vcc, 3, v0\nv_mov_b32 v7, s97\nv_cmpx_gt_i32 vcc, 2, v0\nv_mov_b32 v6, s97\n"
"v_cmpx_gt_i32 vcc, 1, v0\nv_mov_b32 v5, s97\nv_cmpx_gt_i32 vcc, 0, v0\nv_mov_b32 v4, s97\ns_mov_b64 exec, -1\ns_nop 2\ns_branch .E295\n.E296:\ns_cmp_eq_u32 s99, 0\n"
"s_cbranch_scc1 .K293\nv_cmpx_gt_i32 vcc, 19, v3\nv_mov_b32 v15, s97\nv_cmpx_gt_i32 vcc, 18, v3\nv_mov_b32 v14, s97\nv_cmpx_gt_i32 vcc, 17, v3\nv_mov_b32 v13, s97\n"
"v_cmpx_gt_i32 vcc, 16, v3\nv_mov_b32 v12, s97\nv_cmpx_gt_i32 vcc, 3, v3\nv_mov_b32 v7, s97\nv_cmpx_gt_i32 vcc, 2, v3\nv_mov_b32 v6, s97\nv_cmpx_gt_i32 vcc, 1, v3\n"
"v_mov_b32 v5, s97\nv_cmpx_gt_i32 vcc, 0, v3\nv_mov_b32 v4, s97\ns_mov_b64 exec, -1\ns_nop 2\ns_branch .E297\n.K293:\nv_cmpx_gt_i32 vcc, 19, v3\nv_mov_b32 v19, s97\n"
"v_cmpx_gt_i32 vcc, 18, v3\nv_mov_b32 v18, s97\nv_cmpx_gt_i32 vcc, 17, v3\nv_mov_b32 v17, s97\nv_cmpx_gt_i32 vcc, 16, v3\nv_mov_b32 v16, s97\nv_cmpx_gt_i32 vcc, 3, v3\n"
"v_mov_b32 v11, s97\nv_cmpx_gt_i32 vcc, 2, v3\nv_mov_b32 v10, s97\nv_cmpx_gt_i32 vcc, 1, v3\nv_mov_b32 v9, s97\nv_cmpx_gt_i32 vcc, 0, v3\nv_mov_b32 v8, s97\ns_mov_b64 exec, -1\n"
"s_nop 2\ns_branch .E297\n.E298:\ns_swappc_b64 s[80:81], s[8:9]\ns_branch .E299\n.T300:\ns_mov_b32 s62, 2\ns_call_b64 s[80:81], .RB239\ns_branch .T301\n.T302:\ns_mov_b32 s62, 5\n"
"s_call_b64 s[80:81], .RA238\ns_branch .T303\n.T304:\ns_mov_b32 s62, 5\ns_call_b64 s[80:81], .RA238\ns_branch .T305\n.E308:\nv_cmpx_gt_i32 vcc, 19, v0\nv_mov_b32 v15, s97\n"
"v_cmpx_gt_i32 vcc, 18, v0\nv_mov_b32 v14, s97\nv_cmpx_gt_i32 vcc, 17, v0\nv_mov_b32 v13, s97\nv_cmpx_gt_i32 vcc, 16, v0\nv_mov_b32 v12, s97\nv_cmpx_gt_i32 vcc, 3, v0\n"
"v_mov_b32 v7, s97\nv_cmpx_gt_i32 vcc, 2, v0\nv_mov_b32 v6, s97\nv_cmpx_gt_i32 vcc, 1, v0\nv_mov_b32 v5, s97\nv_cmpx_gt_i32 vcc, 0, v0\nv_mov_b32 v4, s97\ns_mov_b64 exec, -1\n"
"s_nop 2\ns_branch .E309\n.E310:\ns_cmp_eq_u32 s99, 0\ns_cbranch_scc1 .K307\nv_cmpx_gt_i32 vcc, 19, v3\nv_mov_b32 v15, s97\nv_cmpx_gt_i32 vcc, 18, v3\nv_mov_b32 v14, s97\n"
"v_cmpx_gt_i32 vcc, 17, v3\nv_mov_b32 v13, s97\nv_cmpx_gt_i32 vcc, 16, v3\nv_mov_b32 v12, s97\nv_cmpx_gt_i32 vcc, 3, v3\nv_mov_b32 v7, s97\nv_cmpx_gt_i32 vcc, 2, v3\n"
"v_mov_b32 v6, s97\nv_cmpx_gt_i32 vcc, 1, v3\nv_mov_b32 v5, s97\nv_cmpx_gt_i32 vcc, 0, v3\nv_mov_b32 v4, s97\ns_mov_b64 exec, -1\ns_nop 2\ns_branch .E311\n.K307:\n"
"v_cmpx_gt_i32 vcc, 19, v3\nv_mov_b32 v19, s97\nv_cmpx_gt_i32 vcc, 18, v3\nv_mov_b32 v18, s97\nv_cmpx_gt_i32 vcc, 17, v3\nv_mov_b32 v17, s97\nv_cmpx_gt_i32 vcc, 16, v3\n"
"v_mov_b32 v16, s97\nv_cmpx_gt_i32 vcc, 3, v3\nv_mov_b32 v11, s97\nv_cmpx_gt_i32 vcc, 2, v3\nv_mov_b32 v10, s97\nv_cmpx_gt_i32 vcc, 1, v3\nv_mov_b32 v9, s97\n"
"v_cmpx_gt_i32 vcc, 0, v3\nv_mov_b32 v8, s97\ns_mov_b64 exec, -1\ns_nop 2\ns_branch .E311\n.E312:\ns_swappc_b64 s[80:81], s[8:9]\ns_branch .E313\n.E314:\nv_mov_b32 v91, 0x40000000\n"
"v_cmp_gt_u32 vcc, 0x200, v1\nWV 5\ns_nop 0\nv_cndmask_b32 v91, v91, v1, vcc\nSv 92,91,16,65,0\ns_branch .E315\n.T316:\ns_mov_b32 s62, 4\ns_call_b64 s[80:81], .RB239\n"
"s_branch .T317\n.T318:\ns_mov_b32 s62, 4\ns_call_b64 s[80:81], .RA238\ns_branch .T319\n.E321:\nv_mov_b32 v91, 0x40000000\nv_cmp_gt_u32 vcc, 0x200, v1\nWV 5\ns_nop 0\n"
"v_cndmask_b32 v91, v91, v1, vcc\nSv 92,91,16,65,0\ns_branch .E322\n.T323:\ns_mov_b32 s62, 4\ns_call_b64 s[80:81], .RB239\ns_branch .T324\n.T325:\ns_mov_b32 s62, 4\n"
"s_call_b64 s[80:81], .RA238\ns_branch .T326\n.T327:\ns_mov_b32 s62, 6\ns_call_b64 s[80:81], .RB239\ns_branch .T328\n.S329:\ns_mov_b32 s7, 262144\n.S331:\ns_add_u32 s4, s92, 0\n"
"buffer_load_dwordx4 a[192:195], v1, s[16:19], s4 offen offset:0 sc0 sc1\nbuffer_load_dwordx4 a[196:199], v1, s[16:19], s4 offen offset:1024 sc0 sc1\ns_add_u32 s4, s92, 131072\n"
"buffer_load_dwordx4 a[200:203], v1, s[16:19], s4 offen offset:0 sc0 sc1\nbuffer_load_dwordx4 a[204:207], v1, s[16:19], s4 offen offset:1024 sc0 sc1\ns_add_u32 s4, s92, 262144\n"
"buffer_load_dwordx4 a[208:211], v1, s[16:19], s4 offen offset:0 sc0 sc1\nbuffer_load_dwordx4 a[212:215], v1, s[16:19], s4 offen offset:1024 sc0 sc1\ns_add_u32 s4, s92, 393216\n"
"buffer_load_dwordx4 a[216:219], v1, s[16:19], s4 offen offset:0 sc0 sc1\nbuffer_load_dwordx4 a[220:223], v1, s[16:19], s4 offen offset:1024 sc0 sc1\nWV 0\nAR 22,195\nAR 23,199\n"
"v_max_u32 v21, v22, v23\nAR 22,203\nAR 23,207\nv_max3_u32 v21, v21, v22, v23\nAR 22,211\nAR 23,215\nv_max3_u32 v21, v21, v22, v23\nAR 22,219\nAR 23,223\n"
"v_max3_u32 v21, v21, v22, v23\nv_cmp_eq_u32 vcc, -1, v21\ns_nop 0\ns_cbranch_vccz .S330\ns_sleep 6\ns_sub_u32 s7, s7, 1\ns_cbranch_scc0 .S331\ns_branch .S330\n.S332:\n"
"s_mov_b32 s7, 262144\n.S334:\ns_add_u32 s4, s92, 524288\nbuffer_load_dwordx4 a[224:227], v1, s[16:19], s4 offen offset:0 sc0 sc1\n"
"buffer_load_dwordx4 a[228:231], v1, s[16:19], s4 offen offset:1024 sc0 sc1\ns_add_u32 s4, s92, 655360\nbuffer_load_dwordx4 a[232:235], v1, s[16:19], s4 offen offset:0 sc0 sc1\n"
"buffer_load_dwordx4 a[236:239], v1, s[16:19], s4 offen offset:1024 sc0 sc1\ns_add_u32 s4, s92, 786432\nbuffer_load_dwordx4 a[240:243], v1, s[16:19], s4 offen offset:0 sc0 sc1\n"
"buffer_load_dwordx4 a[244:247], v1, s[16:19], s4 offen offset:1024 sc0 sc1\ns_add_u32 s4, s92, 917504\nbuffer_load_dwordx4 a[248:251], v1, s[16:19], s4 offen offset:0 sc0 sc1\n"
"buffer_load_dwordx4 a[252:255], v1, s[16:19], s4 offen offset:1024 sc0 sc1\nWV 0\nAR 22,227\nAR 23,231\nv_max_u32 v21, v22, v23\nAR 22,235\nAR 23,239\n"
"v_max3_u32 v21, v21, v22, v23\nAR 22,243\nAR 23,247\nv_max3_u32 v21, v21, v22, v23\nAR 22,251\nAR 23,255\nv_max3_u32 v21, v21, v22, v23\nv_cmp_eq_u32 vcc, -1, v21\ns_nop 0\n"
"s_cbranch_vccz .S333\ns_sleep 6\ns_sub_u32 s7, s7, 1\ns_cbranch_scc0 .S334\ns_branch .S333\n.S335:\ns_mov_b32 s7, 262144\n.S337:\n"
"buffer_load_dwordx4 v[64:67], v89, s[16:19], s40 offen offset:0 sc0 sc1\nbuffer_load_dwordx4 v[68:71], v89, s[16:19], s40 offen offset:16 sc0 sc1\n"
"buffer_load_dwordx4 v[72:75], v89, s[16:19], s40 offen offset:1792 sc0 sc1\nbuffer_load_dwordx4 v[76:79], v89, s[16:19], s40 offen offset:1808 sc0 sc1\nWV 0\n"
"v_xor_b32 v92, s15, v66\nv_xor_b32 v93, s15, v70\nv_xor_b32 v94, s15, v74\nv_or3_b32 v92, v92, v93, v94\nv_xor_b32 v93, s15, v78\nv_or_b32 v92, v92, v93\nv_cmp_ne_u32 vcc, 0, v92\n"
"s_nop 0\ns_cbranch_vccz .S336\ns_sleep 6\ns_sub_u32 s7, s7, 1\ns_cbranch_scc0 .S337\ns_branch .S336\n.S338:\ns_mov_b32 s7, 262144\n.S340:\n"
"buffer_load_dword v27, off, s[16:19], s60 offset:512 sc0 sc1\nWV 0\nv_cmp_ne_u32 vcc, s15, v27\ns_nop 0\ns_cbranch_vccz .S339\ns_sleep 6\ns_sub_u32 s7, s7, 1\n"
"s_cbranch_scc0 .S340\ns_branch .S339\n.S341:\ns_mov_b32 s7, 262144\n.S343:\nbuffer_load_dword v27, v24, s[16:19], s41 offen sc0 sc1\nWV 0\nv_cmp_eq_u32 s[62:63], s15, v27\n"
"s_mov_b32 vcc_hi, 0\ns_nop 2\ns_lshr_b32 s63, s62, 8\ns_or_b32 s62, s62, s63\ns_andn2_b32 vcc_lo, 0xff, s62\ns_mov_b64 vcc, vcc\ns_cbranch_vccz .S342\ns_sleep 6\n"
"s_sub_u32 s7, s7, 1\ns_cbranch_scc0 .S343\ns_branch .S342\n.Z345:\ns_sub_u32 s7, s7, 1\ns_cbranch_scc1 .Z346\ns_cmp_lg_u64 s[44:45], 0\ns_cbranch_scc0 .Z348\n"
"buffer_load_dwordx4 v[96:99], v1, s[16:19], s24 offen offset:0 sc0 sc1\nbuffer_load_dwordx4 v[100:103], v1, s[16:19], s24 offen offset:1024 sc0 sc1\n.Z348:\n"
"s_cmp_lg_u64 s[46:47], 0\ns_cbranch_scc0 .Z349\nbuffer_load_dwordx4 v[104:107], v1, s[16:19], s25 offen offset:0 sc0 sc1\n"
"buffer_load_dwordx4 v[108:111], v1, s[16:19], s25 offen offset:1024 sc0 sc1\n.Z349:\ns_cmp_lg_u64 s[48:49], 0\ns_cbranch_scc0 .Z350\n"
"buffer_load_dwordx4 v[112:115], v1, s[16:19], s26 offen offset:0 sc0 sc1\nbuffer_load_dwordx4 v[116:119], v1, s[16:19], s26 offen offset:1024 sc0 sc1\n.Z350:\n"
"s_cmp_lg_u64 s[50:51], 0\ns_cbranch_scc0 .Z351\nbuffer_load_dwordx4 v[120:123], v1, s[16:19], s27 offen offset:0 sc0 sc1\n"
"buffer_load_dwordx4 v[124:127], v1, s[16:19], s27 offen offset:1024 sc0 sc1\n.Z351:\ns_cmp_lg_u64 s[52:53], 0\ns_cbranch_scc0 .Z352\n"
"buffer_load_dwordx4 v[32:35], v1, s[16:19], s32 offen offset:0 sc0 sc1\nbuffer_load_dwordx4 v[36:39], v1, s[16:19], s32 offen offset:1024 sc0 sc1\n.Z352:\n"
"s_cmp_lg_u64 s[54:55], 0\ns_cbranch_scc0 .Z353\nbuffer_load_dwordx4 v[40:43], v1, s[16:19], s33 offen offset:0 sc0 sc1\n"
"buffer_load_dwordx4 v[44:47], v1, s[16:19], s33 offen offset:1024 sc0 sc1\n.Z353:\ns_cmp_lg_u64 s[56:57], 0\ns_cbranch_scc0 .Z354\n"
"buffer_load_dwordx4 v[48:51], v1, s[16:19], s34 offen offset:0 sc0 sc1\nbuffer_load_dwordx4 v[52:55], v1, s[16:19], s34 offen offset:1024 sc0 sc1\n.Z354:\n"
"s_cmp_lg_u64 s[58:59], 0\ns_cbranch_scc0 .Z355\nbuffer_load_dwordx4 v[56:59], v1, s[16:19], s35 offen offset:0 sc0 sc1\n"
"buffer_load_dwordx4 v[60:63], v1, s[16:19], s35 offen offset:1024 sc0 sc1\n.Z355:\ns_sleep 8\nWV 0\ns_branch .Z347\n.S356:\ns_mov_b32 s7, 262144\n.S358:\nv_and_b32 v27, 0x70, v1\n"
"buffer_load_dword v27, v27, s[16:19], s61 offen offset:140 sc0 sc1\nWV 0\nv_cmp_ne_u32 vcc, s15, v27\ns_nop 0\ns_cbranch_vccz .S357\ns_sleep 6\ns_sub_u32 s7, s7, 1\n"
"s_cbranch_scc0 .S358\ns_branch .S357\n.S359:\ns_mov_b32 s7, 262144\n.S361:\nv_and_b32 v27, 0x70, v1\nbuffer_load_dword v27, v27, s[16:19], s61 offen offset:12 sc0 sc1\nWV 0\n"
"v_cmp_ne_u32 vcc, s15, v27\ns_nop 0\ns_cbranch_vccz .S360\ns_sleep 6\ns_sub_u32 s7, s7, 1\ns_cbranch_scc0 .S361\ns_branch .S360\n.S362:\ns_mov_b32 s7, 262144\n.S364:\n"
"v_and_b32 v27, 0x70, v1\nbuffer_load_dword v27, v27, s[16:19], s61 offen offset:8 sc0 sc1\nWV 0\nv_cmp_ne_u32 vcc, s15, v27\ns_nop 0\ns_cbranch_vccz .S363\ns_sleep 6\n"
"s_sub_u32 s7, s7, 1\ns_cbranch_scc0 .S364\ns_branch .S363\n.S365:\ns_mov_b32 s7, 262144\n.S367:\nv_and_b32 v27, 0x1f0, v1\n"
"buffer_load_dword v27, v27, s[16:19], s58 offen offset:4 sc0 sc1\nWV 0\nv_cmp_ne_u32 vcc, s15, v27\ns_nop 0\ns_cbranch_vccz .S366\ns_sleep 6\ns_sub_u32 s7, s7, 1\n"
"s_cbranch_scc0 .S367\ns_branch .S366\n.RA238:\ns_nop 7\ns_nop 3\nv_max3_f32 v22, v32, v33, v34\nv_max3_f32 v22, v22, v35, v40\nv_max3_f32 v22, v22, v41, v42\n"
"v_max_f32 v22, v22, v43\nv_mov_b32 v91, v22\ns_nop 1\nv_permlane32_swap_b32 v22, v91\nv_max_f32 v22, v22, v91\nv_mov_b32 v91, v22\ns_nop 1\nv_permlane16_swap_b32 v22, v91\n"
"v_max_f32 v22, v22, v91\nv_ceil_f32 v22, v22\nv_max3_f32 v23, v36, v37, v38\nv_max3_f32 v23, v23, v39, v44\nv_max3_f32 v23, v23, v45, v46\nv_max_f32 v23, v23, v47\n"
"v_mov_b32 v91, v23\ns_nop 1\nv_permlane32_swap_b32 v23, v91\nv_max_f32 v23, v23, v91\nv_mov_b32 v91, v23\ns_nop 1\nv_permlane16_swap_b32 v23, v91\nv_max_f32 v23, v23, v91\n"
"v_ceil_f32 v23, v23\ns_bitcmp1_b32 s62, 0\ns_cbranch_scc0 .R368\nv_cmp_lt_f32 vcc, 0xff000000, v22\ns_nop 1\nv_cndmask_b32 v22, 0, v22, vcc\nv_cmp_lt_f32 vcc, 0xff000000, v23\n"
"s_nop 1\nv_cndmask_b32 v23, 0, v23, vcc\ns_branch .R369\n.R368:\nv_max_f32 v22, 0, v22\nv_max_f32 v23, 0, v23\ns_bitcmp1_b32 s62, 3\ns_cbranch_scc0 .R369\nv_max_f32 v22, v20, v22\n"
"v_max_f32 v23, v21, v23\n.R369:\nAD 88,88,22\nAD 89,89,23\nSU 32,32,22\nSU 33,33,22\nSU 34,34,22\nSU 35,35,22\nSU 36,36,23\nSU 37,37,23\nSU 38,38,23\nSU 39,39,23\nSU 40,40,22\n"
"SU 41,41,22\nSU 42,42,22\nSU 43,43,22\nSU 44,44,23\nSU 45,45,23\nSU 46,46,23\nSU 47,47,23\nv_mov_b32 v90, v80\nv_mov_b32 v91, v81\nEX 80,32\nEX 81,33\ns_nop 0\nAD 84,80,81\n"
"CV 64,80,81\nEX 82,34\nEX 83,35\nAD 84,84,82\nAD 84,84,83\nCV 65,82,83\nEX 80,36\nEX 81,37\ns_nop 0\nAD 85,80,81\nCV 68,80,81\nEX 82,38\nEX 83,39\nAD 85,85,82\nAD 85,85,83\n"
"CV 69,82,83\nEX 80,40\nEX 81,41\nAD 84,84,80\nAD 84,84,81\nCV 66,80,81\nEX 82,42\nEX 83,43\nAD 84,84,82\nAD 84,84,83\nCV 67,82,83\nEX 80,44\nEX 81,45\nAD 85,85,80\nAD 85,85,81\n"
"CV 70,80,81\nEX 82,46\nEX 83,47\nAD 85,85,82\nAD 85,85,83\nCV 71,82,83\nv_mov_b32 v80, v90\nv_mov_b32 v81, v91\ns_bitcmp1_b32 s62, 1\ns_cbranch_scc1 .R370\nSU 48,48,22\n"
"SU 49,49,22\nSU 50,50,22\nSU 51,51,22\nSU 52,52,23\nSU 53,53,23\nSU 54,54,23\nSU 55,55,23\nSU 56,56,22\nSU 57,57,22\nSU 58,58,22\nSU 59,59,22\nSU 60,60,23\nSU 61,61,23\n"
"SU 62,62,23\nSU 63,63,23\nSU 4,4,22\nSU 5,5,22\nSU 6,6,22\nSU 7,7,22\nSU 8,8,23\nSU 9,9,23\nSU 10,10,23\nSU 11,11,23\nSU 12,12,22\nSU 13,13,22\nSU 14,14,22\nSU 15,15,22\n"
"SU 16,16,23\nSU 17,17,23\nSU 18,18,23\nSU 19,19,23\nEX 80,48\nEX 81,49\n.R370:\ns_bitcmp1_b32 s62, 0\ns_cbranch_scc0 .RS240\ns_nop 1\ns_setpc_b64 s[80:81]\n.RB239:\ns_nop 7\n"
"s_nop 3\nv_max3_f32 v22, v48, v49, v50\nv_max3_f32 v22, v22, v51, v56\nv_max3_f32 v22, v22, v57, v58\nv_max_f32 v22, v22, v59\nv_mov_b32 v91, v22\ns_nop 1\n"
"v_permlane32_swap_b32 v22, v91\nv_max_f32 v22, v22, v91\nv_mov_b32 v91, v22\ns_nop 1\nv_permlane16_swap_b32 v22, v91\nv_max_f32 v22, v22, v91\nv_ceil_f32 v22, v22\n"
"v_max3_f32 v23, v52, v53, v54\nv_max3_f32 v23, v23, v55, v60\nv_max3_f32 v23, v23, v61, v62\nv_max_f32 v23, v23, v63\nv_mov_b32 v91, v23\ns_nop 1\nv_permlane32_swap_b32 v23, v91\n"
"v_max_f32 v23, v23, v91\nv_mov_b32 v91, v23\ns_nop 1\nv_permlane16_swap_b32 v23, v91\nv_max_f32 v23, v23, v91\nv_ceil_f32 v23, v23\ns_bitcmp1_b32 s62, 0\ns_cbranch_scc0 .R373\n"
"v_cmp_lt_f32 vcc, 0xff000000, v22\nv_cndmask_b32 v22, 0, v22, vcc\nv_cmp_lt_f32 vcc, 0xff000000, v23\nv_cndmask_b32 v23, 0, v23, vcc\ns_branch .R374\n.R373:\n"
"v_max_f32 v22, 0, v22\nv_max_f32 v23, 0, v23\ns_bitcmp1_b32 s62, 3\ns_cbranch_scc0 .R374\nv_max_f32 v22, v20, v22\nv_max_f32 v23, v21, v23\n.R374:\nAD 88,88,22\nAD 89,89,23\n"
"SU 48,48,22\nSU 49,49,22\nSU 50,50,22\nSU 51,51,22\nSU 52,52,23\nSU 53,53,23\nSU 54,54,23\nSU 55,55,23\nSU 56,56,22\nSU 57,57,22\nSU 58,58,22\nSU 59,59,22\nSU 60,60,23\n"
"SU 61,61,23\nSU 62,62,23\nSU 63,63,23\nv_mov_b32 v90, v80\nv_mov_b32 v91, v81\nEX 80,48\nEX 81,49\ns_nop 0\nAD 84,80,81\nCV 72,80,81\nEX 82,50\nEX 83,51\nAD 84,84,82\nAD 84,84,83\n"
"CV 73,82,83\nEX 80,52\nEX 81,53\ns_nop 0\nAD 85,80,81\nCV 76,80,81\nEX 82,54\nEX 83,55\nAD 85,85,82\nAD 85,85,83\nCV 77,82,83\nEX 80,56\nEX 81,57\nAD 84,84,80\nAD 84,84,81\n"
"CV 74,80,81\nEX 82,58\nEX 83,59\nAD 84,84,82\nAD 84,84,83\nCV 75,82,83\nEX 80,60\nEX 81,61\nAD 85,85,80\nAD 85,85,81\nCV 78,80,81\nEX 82,62\nEX 83,63\nAD 85,85,82\nAD 85,85,83\n"
"CV 79,82,83\nv_mov_b32 v80, v90\nv_mov_b32 v81, v91\ns_bitcmp1_b32 s62, 1\ns_cbranch_scc1 .R375\nSU 32,32,22\nSU 33,33,22\nSU 34,34,22\nSU 35,35,22\nSU 36,36,23\nSU 37,37,23\n"
"SU 38,38,23\nSU 39,39,23\nSU 40,40,22\nSU 41,41,22\nSU 42,42,22\nSU 43,43,22\nSU 44,44,23\nSU 45,45,23\nSU 46,46,23\nSU 47,47,23\nSU 4,4,22\nSU 5,5,22\nSU 6,6,22\nSU 7,7,22\n"
"SU 8,8,23\nSU 9,9,23\nSU 10,10,23\nSU 11,11,23\nSU 12,12,22\nSU 13,13,22\nSU 14,14,22\nSU 15,15,22\nSU 16,16,23\nSU 17,17,23\nSU 18,18,23\nSU 19,19,23\nEX 80,32\nEX 81,33\n.R375:\n"
"s_bitcmp1_b32 s62, 0\ns_cbranch_scc0 .RS240\ns_nop 1\ns_setpc_b64 s[80:81]\n.RS240:\nv_exp_f32 v22, -v22\nv_exp_f32 v23, -v23\nMU 86,86,22\nMU 87,87,23\ns_bitcmp1_b32 s62, 2\n"
"s_cbranch_scc1 .R378\nAR 91,0\nMU 91,91,22\nAW 0,91\nAR 91,1\nMU 91,91,22\nAW 1,91\nAR 91,2\nMU 91,91,22\nAW 2,91\nAR 91,3\nMU 91,91,22\nAW 3,91\nAR 91,4\nMU 91,91,23\nAW 4,91\n"
"AR 91,5\nMU 91,91,23\nAW 5,91\nAR 91,6\nMU 91,91,23\nAW 6,91\nAR 91,7\nMU 91,91,23\nAW 7,91\nAR 91,8\nMU 91,91,22\nAW 8,91\nAR 91,9\nMU 91,91,22\nAW 9,91\nAR 91,10\nMU 91,91,22\n"
"AW 10,91\nAR 91,11\nMU 91,91,22\nAW 11,91\nAR 91,12\nMU 91,91,23\nAW 12,91\nAR 91,13\nMU 91,91,23\nAW 13,91\nAR 91,14\nMU 91,91,23\nAW 14,91\nAR 91,15\nMU 91,91,23\nAW 15,91\n"
"AR 91,16\nMU 91,91,22\nAW 16,91\nAR 91,17\nMU 91,91,22\nAW 17,91\nAR 91,18\nMU 91,91,22\nAW 18,91\nAR 91,19\nMU 91,91,22\nAW 19,91\nAR 91,20\nMU 91,91,23\nAW 20,91\nAR 91,21\n"
"MU 91,91,23\nAW 21,91\nAR 91,22\nMU 91,91,23\nAW 22,91\nAR 91,23\nMU 91,91,23\nAW 23,91\nAR 91,24\nMU 91,91,22\nAW 24,91\nAR 91,25\nMU 91,91,22\nAW 25,91\nAR 91,26\nMU 91,91,22\n"
"AW 26,91\nAR 91,27\nMU 91,91,22\nAW 27,91\nAR 91,28\nMU 91,91,23\nAW 28,91\nAR 91,29\nMU 91,91,23\nAW 29,91\nAR 91,30\nMU 91,91,23\nAW 30,91\nAR 91,31\nMU 91,91,23\nAW 31,91\n"
"AR 91,32\nMU 91,91,22\nAW 32,91\nAR 91,33\nMU 91,91,22\nAW 33,91\nAR 91,34\nMU 91,91,22\nAW 34,91\nAR 91,35\nMU 91,91,22\nAW 35,91\nAR 91,36\nMU 91,91,23\nAW 36,91\nAR 91,37\n"
"MU 91,91,23\nAW 37,91\nAR 91,38\nMU 91,91,23\nAW 38,91\nAR 91,39\nMU 91,91,23\nAW 39,91\nAR 91,40\nMU 91,91,22\nAW 40,91\nAR 91,41\nMU 91,91,22\nAW 41,91\nAR 91,42\nMU 91,91,22\n"
"AW 42,91\nAR 91,43\nMU 91,91,22\nAW 43,91\nAR 91,44\nMU 91,91,23\nAW 44,91\nAR 91,45\nMU 91,91,23\nAW 45,91\nAR 91,46\nMU 91,91,23\nAW 46,91\nAR 91,47\nMU 91,91,23\nAW 47,91\n"
"AR 91,48\nMU 91,91,22\nAW 48,91\nAR 91,49\nMU 91,91,22\nAW 49,91\nAR 91,50\nMU 91,91,22\nAW 50,91\nAR 91,51\nMU 91,91,22\nAW 51,91\nAR 91,52\nMU 91,91,23\nAW 52,91\nAR 91,53\n"
"MU 91,91,23\nAW 53,91\nAR 91,54\nMU 91,91,23\nAW 54,91\nAR 91,55\nMU 91,91,23\nAW 55,91\nAR 91,56\nMU 91,91,22\nAW 56,91\nAR 91,57\nMU 91,91,22\nAW 57,91\nAR 91,58\nMU 91,91,22\n"
"AW 58,91\nAR 91,59\nMU 91,91,22\nAW 59,91\nAR 91,60\nMU 91,91,23\nAW 60,91\nAR 91,61\nMU 91,91,23\nAW 61,91\nAR 91,62\nMU 91,91,23\nAW 62,91\nAR 91,63\nMU 91,91,23\nAW 63,91\n"
"AR 91,64\nMU 91,91,22\nAW 64,91\nAR 91,65\nMU 91,91,22\nAW 65,91\nAR 91,66\nMU 91,91,22\nAW 66,91\nAR 91,67\nMU 91,91,22\nAW 67,91\nAR 91,68\nMU 91,91,23\nAW 68,91\nAR 91,69\n"
"MU 91,91,23\nAW 69,91\nAR 91,70\nMU 91,91,23\nAW 70,91\nAR 91,71\nMU 91,91,23\nAW 71,91\nAR 91,72\nMU 91,91,22\nAW 72,91\nAR 91,73\nMU 91,91,22\nAW 73,91\nAR 91,74\nMU 91,91,22\n"
"AW 74,91\nAR 91,75\nMU 91,91,22\nAW 75,91\nAR 91,76\nMU 91,91,23\nAW 76,91\nAR 91,77\nMU 91,91,23\nAW 77,91\nAR 91,78\nMU 91,91,23\nAW 78,91\nAR 91,79\nMU 91,91,23\nAW 79,91\n"
"AR 91,80\nMU 91,91,22\nAW 80,91\nAR 91,81\nMU 91,91,22\nAW 81,91\nAR 91,82\nMU 91,91,22\nAW 82,91\nAR 91,83\nMU 91,91,22\nAW 83,91\nAR 91,84\nMU 91,91,23\nAW 84,91\nAR 91,85\n"
"MU 91,91,23\nAW 85,91\nAR 91,86\nMU 91,91,23\nAW 86,91\nAR 91,87\nMU 91,91,23\nAW 87,91\nAR 91,88\nMU 91,91,22\nAW 88,91\nAR 91,89\nMU 91,91,22\nAW 89,91\nAR 91,90\nMU 91,91,22\n"
"AW 90,91\nAR 91,91\nMU 91,91,22\nAW 91,91\nAR 91,92\nMU 91,91,23\nAW 92,91\nAR 91,93\nMU 91,91,23\nAW 93,91\nAR 91,94\nMU 91,91,23\nAW 94,91\nAR 91,95\nMU 91,91,23\nAW 95,91\n"
"AR 91,96\nMU 91,91,22\nAW 96,91\nAR 91,97\nMU 91,91,22\nAW 97,91\nAR 91,98\nMU 91,91,22\nAW 98,91\nAR 91,99\nMU 91,91,22\nAW 99,91\nAR 91,100\nMU 91,91,23\nAW 100,91\nAR 91,101\n"
"MU 91,91,23\nAW 101,91\nAR 91,102\nMU 91,91,23\nAW 102,91\nAR 91,103\nMU 91,91,23\nAW 103,91\nAR 91,104\nMU 91,91,22\nAW 104,91\nAR 91,105\nMU 91,91,22\nAW 105,91\nAR 91,106\n"
"MU 91,91,22\nAW 106,91\nAR 91,107\nMU 91,91,22\nAW 107,91\nAR 91,108\nMU 91,91,23\nAW 108,91\nAR 91,109\nMU 91,91,23\nAW 109,91\nAR 91,110\nMU 91,91,23\nAW 110,91\nAR 91,111\n"
"MU 91,91,23\nAW 111,91\nAR 91,112\nMU 91,91,22\nAW 112,91\nAR 91,113\nMU 91,91,22\nAW 113,91\nAR 91,114\nMU 91,91,22\nAW 114,91\nAR 91,115\nMU 91,91,22\nAW 115,91\nAR 91,116\n"
"MU 91,91,23\nAW 116,91\nAR 91,117\nMU 91,91,23\nAW 117,91\nAR 91,118\nMU 91,91,23\nAW 118,91\nAR 91,119\nMU 91,91,23\nAW 119,91\nAR 91,120\nMU 91,91,22\nAW 120,91\nAR 91,121\n"
"MU 91,91,22\nAW 121,91\nAR 91,122\nMU 91,91,22\nAW 122,91\nAR 91,123\nMU 91,91,22\nAW 123,91\nAR 91,124\nMU 91,91,23\nAW 124,91\nAR 91,125\nMU 91,91,23\nAW 125,91\nAR 91,126\n"
"MU 91,91,23\nAW 126,91\nAR 91,127\nMU 91,91,23\nAW 127,91\ns_nop 1\ns_setpc_b64 s[80:81]\n.R378:\nMU 128,128,22\nMU 129,129,22\nMU 130,130,22\nMU 131,131,22\nMU 132,132,23\n"
"MU 133,133,23\nMU 134,134,23\nMU 135,135,23\nMU 136,136,22\nMU 137,137,22\nMU 138,138,22\nMU 139,139,22\nMU 140,140,23\nMU 141,141,23\nMU 142,142,23\nMU 143,143,23\nMU 144,144,22\n"
"MU 145,145,22\nMU 146,146,22\nMU 147,147,22\nMU 148,148,23\nMU 149,149,23\nMU 150,150,23\nMU 151,151,23\nMU 152,152,22\nMU 153,153,22\nMU 154,154,22\nMU 155,155,22\nMU 156,156,23\n"
"MU 157,157,23\nMU 158,158,23\nMU 159,159,23\nMU 160,160,22\nMU 161,161,22\nMU 162,162,22\nMU 163,163,22\nMU 164,164,23\nMU 165,165,23\nMU 166,166,23\nMU 167,167,23\nMU 168,168,22\n"
"MU 169,169,22\nMU 170,170,22\nMU 171,171,22\nMU 172,172,23\nMU 173,173,23\nMU 174,174,23\nMU 175,175,23\nMU 176,176,22\nMU 177,177,22\nMU 178,178,22\nMU 179,179,22\nMU 180,180,23\n"
"MU 181,181,23\nMU 182,182,23\nMU 183,183,23\nMU 184,184,22\nMU 185,185,22\nMU 186,186,22\nMU 187,187,22\nMU 188,188,23\nMU 189,189,23\nMU 190,190,23\nMU 191,191,23\nMU 192,192,22\n"
"MU 193,193,22\nMU 194,194,22\nMU 195,195,22\nMU 196,196,23\nMU 197,197,23\nMU 198,198,23\nMU 199,199,23\nMU 200,200,22\nMU 201,201,22\nMU 202,202,22\nMU 203,203,22\nMU 204,204,23\n"
"MU 205,205,23\nMU 206,206,23\nMU 207,207,23\nMU 208,208,22\nMU 209,209,22\nMU 210,210,22\nMU 211,211,22\nMU 212,212,23\nMU 213,213,23\nMU 214,214,23\nMU 215,215,23\nMU 216,216,22\n"
"MU 217,217,22\nMU 218,218,22\nMU 219,219,22\nMU 220,220,23\nMU 221,221,23\nMU 222,222,23\nMU 223,223,23\nMU 224,224,22\nMU 225,225,22\nMU 226,226,22\nMU 227,227,22\nMU 228,228,23\n"
"MU 229,229,23\nMU 230,230,23\nMU 231,231,23\nMU 232,232,22\nMU 233,233,22\nMU 234,234,22\nMU 235,235,22\nMU 236,236,23\nMU 237,237,23\nMU 238,238,23\nMU 239,239,23\nMU 240,240,22\n"
"MU 241,241,22\nMU 242,242,22\nMU 243,243,22\nMU 244,244,23\nMU 245,245,23\nMU 246,246,23\nMU 247,247,23\nMU 248,248,22\nMU 249,249,22\nMU 250,250,22\nMU 251,251,22\nMU 252,252,23\n"
"MU 253,253,23\nMU 254,254,23\nMU 255,255,23\ns_nop 1\ns_setpc_b64 s[80:81]\n.p2align 12\n.PA5:\ns_add_u32 m0, s47, 16384\ns_add_u32 s44, s44, 0x40000\nD0 6,24,44,0\n"
"D0 7,24,44,1024\nD0 8,24,44,2048\nD0 9,24,44,3072\ns_and_b32 s86, s74, 15\ns_lshl2_add_u32 s86, s86, s11\ns_mov_b32 s88, 0\ns_lshl_b32 s93, s73, 8\ns_lshl2_add_u32 s93, s86, s93\n"
"s_add_u32 s93, s93, 0xc04ac00\ns_lshl_b32 s86, s86, 14\ns_mul_i32 s91, s73, 25165824\ns_add_u32 s86, s86, s91\ns_add_u32 s94, s86, 0x1480000\ns_add_u32 s95, s94, 0x2000\n"
"s_add_u32 s86, s94, 512\ns_load_dword s87, s[54:55], s86\ns_add_u32 m0, s48, 32768\ns_add_u32 s45, s45, 128\nD0 2,20,45,0\nD0 3,20,45,1024\nD0 4,20,45,2048\nD0 5,20,45,3072\n"
"s_add_u32 m0, s48, 36864\ns_add_u32 s46, s45, 0x10000\nD0 2,20,46,0\nD0 3,20,46,1024\nD0 4,20,46,2048\nD0 5,20,46,3072\ns_add_u32 m0, s47, 32768\ns_add_u32 s44, s44, 0x40000\n"
"D0 6,24,44,0\nD0 7,24,44,1024\nD0 8,24,44,2048\nD0 9,24,44,3072\nv_mov_b32 v52, 0x40000000\nv_cmp_gt_u32 vcc, 56, v0\ns_lshl2_add_u32 s2, s10, s11\ns_mul_i32 s2, s2, 1792\n"
"v_cndmask_b32 v53, v52, v1, vcc\ns_mov_b32 s36, s56\ns_and_b32 s37, s57, 0xffff\ns_mov_b32 s38, 0x380\ns_mov_b32 s39, 0x20000\ns_mov_b32 s40, s58\ns_and_b32 s41, s59, 0xffff\n"
"s_mov_b32 s42, 0x380\ns_mov_b32 s43, 0x20000\nbuffer_load_dwordx4 v[32:35], v53, s[28:31], s2 offen offset:0\nbuffer_load_dwordx4 v[36:39], v53, s[28:31], s2 offen offset:896\n"
"buffer_load_dwordx4 v[40:43], v53, s[36:39], 0 offen offset:0\nbuffer_load_dwordx4 v[44:47], v53, s[40:43], 0 offen offset:0\nWL 0\ns_mul_i32 s15, s15, 1664525\n"
"s_add_u32 s15, s15, 1013904223\nv_mov_b32 v25, s15\ns_mov_b32 s57, 0x3db8aa3b\ns_and_b32 s59, s11, 1\ns_lshl_b32 s59, s59, 13\ns_mul_i32 s2, s73, 25165824\ns_and_b32 s3, s12, 1\n"
"s_lshl_b32 s3, s3, 21\ns_add_u32 s2, s2, s3\ns_lshl2_add_u32 s3, s13, s11\ns_lshl_b32 s3, s3, 15\ns_add_u32 s2, s2, s3\ns_add_u32 s56, s2, 0x800000\ns_xor_b32 s53, s56, 0xc000\n"
"buffer_load_dword v31, v24, s[24:27], s96 offen\ns_add_u32 s96, s96, 0x80000\nv_lshrrev_b32 v60, 3, v0\nv_and_b32 v61, 0x70, v1\nv_lshl_or_b32 v60, v60, 10, v61\nSv 2,60,16,94,0\n"
"Sv 2,60,16,95,0\nWV 23\ns_barrier\nTa 128,12,0\nTa 130,12,2048\nTa 132,13,0\nTa 134,13,2048\nTa 136,14,0\nTa 138,14,2048\nTa 140,15,0\nTa 142,15,2048\nTa 144,16,0\nTa 146,16,2048\n"
"Ta 148,17,0\nTa 150,17,2048\nTa 152,18,0\nTa 154,18,2048\nTa 156,19,0\nTa 158,19,2048\nRa 160,10,0\nRa 164,10,2048\nRa 168,10,8192\nRa 172,10,10240\nRa 176,10,16384\n"
"Ra 180,10,18432\nRa 184,10,24576\nRa 188,10,26624\ns_waitcnt vmcnt(19) lgkmcnt(0)\ns_barrier\nZvaa 64,160,128\nTa 192,12,16384\nZvaa 68,164,128\ns_mov_b32 s36, s60\n"
"Zvaa 128,168,128\nTa 194,12,18432\nZvaa 132,172,128\ns_add_u32 m0, s47, 49152\ns_add_u32 s44, s44, 0x40000\nZaaa 0,176,128\nTa 196,13,16384\nZaaa 4,180,128\nD0 6,24,44,0\n"
"Zaaa 64,184,128\nTa 198,13,18432\nZaaa 68,188,128\ns_and_b32 s37, s61, 0xffff\nZaaa 76,188,132\nTa 200,14,16384\nZaaa 72,184,132\ns_mov_b32 s38, 0x380000\nZaaa 12,180,132\n"
"Ta 202,14,18432\nZaaa 8,176,132\ns_mov_b32 s39, 0x20000\nZvaa 140,172,132\nTa 204,15,16384\nZvaa 136,168,132\nD0 7,24,44,1024\nZvaa 76,164,132\nTa 206,15,18432\nZvaa 72,160,132\n"
"s_lshl3_add_u32 s75, s12, s13\nZvaa 80,160,136\nTa 208,16,16384\nZvaa 84,164,136\ns_lshl2_add_u32 s75, s75, s11\nZvaa 144,168,136\nTa 210,16,18432\nZvaa 148,172,136\n"
"s_mul_i32 s75, s75, 7168\nZaaa 16,176,136\nTa 212,17,16384\nZaaa 20,180,136\nD0 8,24,44,2048\nZaaa 80,184,136\nTa 214,17,18432\nZaaa 84,188,136\ns_mul_i32 s76, s11, 7168\n"
"Zaaa 92,188,140\nTa 216,18,16384\nZaaa 88,184,140\ns_add_u32 s76, s76, 131072\nZaaa 28,180,140\nTa 218,18,18432\nZaaa 24,176,140\ns_mov_b32 s42, 1605632\nZvaa 156,172,140\n"
"Ta 220,19,16384\nZvaa 152,168,140\nD0 9,24,44,3072\nZvaa 92,164,140\nTa 222,19,18432\nZvaa 88,160,140\nbuffer_load_dword v31, v24, s[24:27], s96 offen\n"
"s_add_u32 s96, s96, 0x80000\nZvaa 96,160,144\nRa 224,11,0\nZvaa 100,164,144\nZvaa 160,168,144\nRa 228,11,2048\nZvaa 164,172,144\ns_add_u32 m0, s47, 0\ns_add_u32 s44, s44, 0x40000\n"
"Zaaa 32,176,144\nRa 232,11,8192\nZaaa 36,180,144\nD0 6,24,44,0\nZaaa 96,184,144\nRa 236,11,10240\nZaaa 100,188,144\nZaaa 108,188,148\nRa 240,11,16384\nZaaa 104,184,148\n"
"s_and_b32 s78, s74, 3\ns_lshl2_add_u32 s78, s73, s78\ns_lshl_b32 s78, s78, 12\ns_add_u32 s79, s45, 512\ns_and_b32 s79, s79, 0xfffe0000\ns_add_u32 s78, s78, s79\n"
"buffer_load_dword v31, v26, s[20:23], s78 offen\nZaaa 44,180,148\nRa 244,11,18432\nZaaa 40,176,148\nZvaa 172,172,148\nRa 248,11,24576\nZvaa 168,168,148\nD0 7,24,44,1024\n"
"Zvaa 108,164,148\nRa 252,11,26624\nZvaa 104,160,148\nZvaa 112,160,152\nZvaa 116,164,152\nZvaa 176,168,152\nZvaa 180,172,152\nZaaa 48,176,152\nZaaa 52,180,152\nD0 8,24,44,2048\n"
"Zaaa 112,184,152\nZaaa 116,188,152\nZaaa 124,188,156\nZaaa 120,184,156\nZaaa 60,180,156\nZaaa 56,176,156\nZvaa 188,172,156\nZvaa 184,168,156\nD0 9,24,44,3072\nZvaa 124,164,156\n"
"Zvaa 120,160,156\ns_waitcnt vmcnt(17) lgkmcnt(0)\ns_barrier\nMvaa 64,224,192\nTa 128,12,32768\nWV 13\nMvaa 68,228,192\nv_lshlrev_b32 v48, 16, v32\nMvaa 128,232,192\n"
"Ta 130,12,34816\nMvaa 132,236,192\ns_add_u32 m0, s48, 0\ns_add_u32 s45, s45, 128\nMaaa 0,240,192\nTa 132,13,32768\nv_lshlrev_b32 v49, 16, v36\nMaaa 4,244,192\nD0 2,20,45,0\n"
"Maaa 64,248,192\nTa 134,13,34816\nMaaa 68,252,192\nv_lshlrev_b32 v50, 16, v40\nv_lshlrev_b32 v51, 16, v44\nMaaa 76,252,196\nTa 136,14,32768\nMaaa 72,248,196\nMU 52,48,50\n"
"Maaa 12,244,196\nTa 138,14,34816\nMU 53,49,50\nMaaa 8,240,196\nCV 52,52,53\nMvaa 140,236,196\nTa 140,15,32768\nMvaa 136,232,196\nD0 3,20,45,1024\nv_lshlrev_b32 v54, 16, v52\n"
"Mvaa 76,228,196\nTa 142,15,34816\nMvaa 72,224,196\nv_and_b32 v55, 0xffff0000, v52\nMvaa 80,224,200\nTa 144,16,32768\nMU 56,48,51\nMvaa 84,228,200\nMU 57,49,51\nMvaa 144,232,200\n"
"Ta 146,16,34816\nMvaa 148,236,200\nv_and_b32 v48, 0xffff0000, v32\nMaaa 16,240,200\nTa 148,17,32768\nv_and_b32 v49, 0xffff0000, v36\nMaaa 20,244,200\nD0 4,20,45,2048\n"
"Maaa 80,248,200\nTa 150,17,34816\nMaaa 84,252,200\nv_and_b32 v50, 0xffff0000, v40\nv_and_b32 v51, 0xffff0000, v44\nMaaa 92,252,204\nTa 152,18,32768\nMaaa 88,248,204\nMU 52,48,50\n"
"Maaa 28,244,204\nTa 154,18,34816\nMU 53,49,50\nMaaa 24,240,204\nCV 52,52,53\nMvaa 156,236,204\nTa 156,19,32768\nMvaa 152,232,204\nD0 5,20,45,3072\nv_and_b32 v53, 0xffff0000, v52\n"
"Mvaa 92,228,204\nTa 158,19,34816\nMvaa 88,224,204\nv_lshlrev_b32 v52, 16, v52\nMvaa 96,224,208\nRa 160,10,32768\nFM 56,48,51\nMvaa 100,228,208\nFM 57,49,51\nMvaa 160,232,208\n"
"Ra 164,10,34816\nMvaa 164,236,208\ns_add_u32 m0, s48, 4096\ns_add_u32 s46, s45, 0x10000\nMaaa 32,240,208\nRa 168,10,40960\nAD 54,54,52\nMaaa 36,244,208\nD0 2,20,46,0\n"
"Maaa 96,248,208\nRa 172,10,43008\nMaaa 100,252,208\nAD 55,55,53\nv_lshlrev_b32 v48, 16, v33\nMaaa 108,252,212\nRa 176,10,49152\nMaaa 104,248,212\nv_lshlrev_b32 v49, 16, v37\n"
"Maaa 44,244,212\nRa 180,10,51200\nv_lshlrev_b32 v50, 16, v41\nMaaa 40,240,212\nv_lshlrev_b32 v51, 16, v45\nMvaa 172,236,212\nRa 184,10,57344\nMvaa 168,232,212\nD0 3,20,46,1024\n"
"MU 52,48,50\nMvaa 108,228,212\nRa 188,10,59392\nMvaa 104,224,212\nMU 53,49,50\nMvaa 112,224,216\nCV 52,52,53\nv_and_b32 v53, 0xffff0000, v52\nMvaa 116,228,216\n"
"v_lshlrev_b32 v52, 16, v52\nMvaa 176,232,216\nFM 56,48,51\nMvaa 180,236,216\nFM 57,49,51\nMaaa 48,240,216\nAD 54,54,52\nAD 55,55,53\nMaaa 52,244,216\nD0 4,20,46,2048\n"
"Maaa 112,248,216\nv_and_b32 v48, 0xffff0000, v33\nMaaa 116,252,216\nv_and_b32 v49, 0xffff0000, v37\nv_and_b32 v50, 0xffff0000, v41\nMaaa 124,252,220\n"
"v_and_b32 v51, 0xffff0000, v45\nMaaa 120,248,220\nMU 52,48,50\nMaaa 60,244,220\nMU 53,49,50\nCV 52,52,53\nMaaa 56,240,220\nv_and_b32 v53, 0xffff0000, v52\nMvaa 188,236,220\n"
"v_lshlrev_b32 v52, 16, v52\nMvaa 184,232,220\nD0 5,20,46,3072\nFM 56,48,51\nMvaa 124,228,220\nFM 57,49,51\nMvaa 120,224,220\ns_waitcnt vmcnt(14) lgkmcnt(0)\ns_barrier\n"
"Mvaa 64,160,128\nTa 192,12,49152\nAD 54,54,52\nMvaa 68,164,128\nAD 55,55,53\nMvaa 128,168,128\nTa 194,12,51200\nMvaa 132,172,128\ns_add_u32 m0, s47, 16384\n"
"s_add_u32 s44, s44, 0x40000\nMaaa 0,176,128\nTa 196,13,49152\nv_lshlrev_b32 v48, 16, v34\nMaaa 4,180,128\nD0 6,24,44,0\nMaaa 64,184,128\nTa 198,13,51200\nMaaa 68,188,128\n"
"v_lshlrev_b32 v49, 16, v38\nv_lshlrev_b32 v50, 16, v42\nMaaa 76,188,132\nTa 200,14,49152\nMaaa 72,184,132\nv_lshlrev_b32 v51, 16, v46\nMaaa 12,180,132\nTa 202,14,51200\n"
"MU 52,48,50\nMaaa 8,176,132\nMU 53,49,50\nMvaa 140,172,132\nTa 204,15,49152\nMvaa 136,168,132\nD0 7,24,44,1024\nCV 52,52,53\nMvaa 76,164,132\nTa 206,15,51200\nMvaa 72,160,132\n"
"v_and_b32 v53, 0xffff0000, v52\nMvaa 80,160,136\nTa 208,16,49152\nv_lshlrev_b32 v52, 16, v52\nMvaa 84,164,136\nFM 56,48,51\nMvaa 144,168,136\nTa 210,16,51200\nMvaa 148,172,136\n"
"FM 57,49,51\nMaaa 16,176,136\nTa 212,17,49152\nAD 54,54,52\nMaaa 20,180,136\nD0 8,24,44,2048\nMaaa 80,184,136\nTa 214,17,51200\nMaaa 84,188,136\nAD 55,55,53\n"
"v_and_b32 v48, 0xffff0000, v34\nMaaa 92,188,140\nTa 216,18,49152\nMaaa 88,184,140\nv_and_b32 v49, 0xffff0000, v38\nMaaa 28,180,140\nTa 218,18,51200\n"
"v_and_b32 v50, 0xffff0000, v42\nMaaa 24,176,140\nv_and_b32 v51, 0xffff0000, v46\nMvaa 156,172,140\nTa 220,19,49152\nMvaa 152,168,140\nD0 9,24,44,3072\nMU 52,48,50\n"
"Mvaa 92,164,140\nTa 222,19,51200\nMvaa 88,160,140\nbuffer_load_dword v31, v24, s[24:27], s96 offen\ns_add_u32 s96, s96, 0x80000\nMvaa 96,160,144\nRa 224,11,32768\nMU 53,49,50\n"
"Mvaa 100,164,144\nCV 52,52,53\nMvaa 160,168,144\nRa 228,11,34816\nMvaa 164,172,144\ns_add_u32 m0, s47, 32768\ns_add_u32 s44, s44, 0x40000\nMaaa 32,176,144\nRa 232,11,40960\n"
"v_and_b32 v53, 0xffff0000, v52\nMaaa 36,180,144\nD0 6,24,44,0\nMaaa 96,184,144\nRa 236,11,43008\nMaaa 100,188,144\nv_lshlrev_b32 v52, 16, v52\nFM 56,48,51\nMaaa 108,188,148\n"
"Ra 240,11,49152\nMaaa 104,184,148\nFM 57,49,51\nMaaa 44,180,148\nRa 244,11,51200\nAD 54,54,52\nMaaa 40,176,148\nAD 55,55,53\nMvaa 172,172,148\nRa 248,11,57344\nMvaa 168,168,148\n"
"D0 7,24,44,1024\nv_lshlrev_b32 v48, 16, v35\nMvaa 108,164,148\nRa 252,11,59392\nMvaa 104,160,148\nv_lshlrev_b32 v49, 16, v39\nMvaa 112,160,152\nv_lshlrev_b32 v50, 16, v43\n"
"v_lshlrev_b32 v51, 16, v47\nMvaa 116,164,152\nMU 52,48,50\nMvaa 176,168,152\nMU 53,49,50\nMvaa 180,172,152\nCV 52,52,53\nMaaa 48,176,152\nv_and_b32 v53, 0xffff0000, v52\n"
"v_lshlrev_b32 v52, 16, v52\nMaaa 52,180,152\nD0 8,24,44,2048\nMaaa 112,184,152\nFM 56,48,51\nMaaa 116,188,152\nFM 57,49,51\nAD 54,54,52\nMaaa 124,188,156\nAD 55,55,53\n"
"Maaa 120,184,156\nv_and_b32 v48, 0xffff0000, v35\nMaaa 60,180,156\nv_and_b32 v49, 0xffff0000, v39\nv_and_b32 v50, 0xffff0000, v43\nMaaa 56,176,156\nv_and_b32 v51, 0xffff0000, v47\n"
"Mvaa 188,172,156\nMU 52,48,50\nMvaa 184,168,156\nD0 9,24,44,3072\nMU 53,49,50\nMvaa 124,164,156\nCV 52,52,53\nMvaa 120,160,156\ns_waitcnt vmcnt(9) lgkmcnt(0)\ns_barrier\n"
"Mvaa 64,224,192\nTa 128,12,0\nv_and_b32 v53, 0xffff0000, v52\nMvaa 68,228,192\nv_lshlrev_b32 v52, 16, v52\nMvaa 128,232,192\nTa 130,12,2048\nMvaa 132,236,192\n"
"s_add_u32 m0, s48, 32768\ns_add_u32 s45, s45, 128\nMaaa 0,240,192\nTa 132,13,0\nFM 56,48,51\nMaaa 4,244,192\nD0 2,20,45,0\nMaaa 64,248,192\nTa 134,13,2048\nMaaa 68,252,192\n"
"FM 57,49,51\nAD 54,54,52\nMaaa 76,252,196\nTa 136,14,0\nMaaa 72,248,196\nAD 55,55,53\nMaaa 12,244,196\nTa 138,14,2048\nv_permlane32_swap_b32 v54, v55\nMaaa 8,240,196\n"
"v_permlane32_swap_b32 v56, v57\nMvaa 140,236,196\nTa 140,15,0\nMvaa 136,232,196\nD0 3,20,45,1024\nAD 54,54,55\nMvaa 76,228,196\nTa 142,15,2048\nMvaa 72,224,196\nAD 56,56,57\n"
"Mvaa 80,224,200\nTa 144,16,0\nMvaa 84,228,200\nv_permlane16_swap_b32 v54, v56\nMvaa 144,232,200\nTa 146,16,2048\nMvaa 148,236,200\nAD 54,54,56\nMaaa 16,240,200\nTa 148,17,0\n"
"Maaa 20,244,200\nD0 4,20,45,2048\nMaaa 80,248,200\nTa 150,17,2048\nMaaa 84,252,200\nv_add_f32_dpp v54, v54, v54 row_ror:8 row_mask:0xf bank_mask:0xf\nMaaa 92,252,204\nTa 152,18,0\n"
"Maaa 88,248,204\nv_add_f32_dpp v54, v54, v54 row_ror:4 row_mask:0xf bank_mask:0xf\nMaaa 28,244,204\nTa 154,18,2048\nMaaa 24,240,204\n"
"v_add_f32_dpp v54, v54, v54 row_ror:2 row_mask:0xf bank_mask:0xf\nMvaa 156,236,204\nTa 156,19,0\nMvaa 152,232,204\nD0 5,20,45,3072\n"
"v_add_f32_dpp v54, v54, v54 row_ror:1 row_mask:0xf bank_mask:0xf\nMvaa 92,228,204\nTa 158,19,2048\nMvaa 88,224,204\nCV 54,54,54\nMvaa 96,224,208\nRa 160,10,0\n"
"v_lshlrev_b32 v54, 16, v54\nMvaa 100,228,208\nv_mul_f32 v55, 0xbfb8aa3b, v54\nEX 55,55\nMvaa 160,232,208\nRa 164,10,2048\nMvaa 164,236,208\ns_add_u32 m0, s48, 36864\n"
"s_add_u32 s46, s45, 0x10000\nMaaa 32,240,208\nRa 168,10,8192\nv_add_f32 v56, 0, v54\nMaaa 36,244,208\nD0 2,20,46,0\nMaaa 96,248,208\nRa 172,10,10240\nMaaa 100,252,208\n"
"v_add_f32 v55, 1.0, v55\nMaaa 108,252,212\nRa 176,10,16384\nv_rcp_f32 v55, v55\nMaaa 104,248,212\nv_ashrrev_i32 v57, 31, v56\nMaaa 44,244,212\nRa 180,10,18432\nCV 55,55,55\n"
"Maaa 40,240,212\nv_lshlrev_b32 v55, 16, v55\nMvaa 172,236,212\nRa 184,10,24576\nMvaa 168,232,212\nD0 3,20,46,1024\nv_or_b32 v57, 0x80000000, v57\nMvaa 108,228,212\n"
"Ra 188,10,26624\nMvaa 104,224,212\nv_xor_b32 v56, v56, v57\nMvaa 112,224,216\nv_and_b32 v56, 0xffff0000, v56\ns_lshl2_add_u32 s2, s10, s11\nMvaa 116,228,216\n"
"v_lshrrev_b32 v48, 5, v0\nMvaa 176,232,216\nv_lshl_add_u32 v48, s2, 1, v48\nMvaa 180,236,216\nv_sub_u32 v49, 0xffff, v48\nMaaa 48,240,216\nv_or_b32 v56, v56, v49\n"
"v_mov_b32 v57, s15\nMaaa 52,244,216\nD0 4,20,46,2048\nMaaa 112,248,216\nv_and_b32 v49, 31, v0\nMaaa 116,252,216\nv_mov_b32 v52, 0x40000000\nv_cmp_eq_u32 vcc, 16, v49\n"
"v_lshlrev_b32 v50, 3, v48\nv_lshlrev_b32 v51, 9, v49\nv_cndmask_b32 v50, v52, v50, vcc\nv_cndmask_b32 v55, v55, v54, vcc\nMaaa 124,252,220\nv_lshl_add_u32 v51, v48, 2, v51\n"
"Maaa 120,248,220\nv_and_b32 v49, 15, v0\nv_cmp_eq_u32 vcc, 0, v49\ns_mov_b32 s2, 0xc040000\ns_mov_b32 s3, 0xc044000\nv_cndmask_b32 v51, v52, v51, vcc\n"
"buffer_store_dwordx2 v[56:57], v50, s[16:19], s3 offen sc0 sc1\nbuffer_store_dword v55, v51, s[16:19], s2 offen sc0 sc1\nMaaa 60,244,220\nMaaa 56,240,220\nMvaa 188,236,220\n"
"Mvaa 184,232,220\nD0 5,20,46,3072\nMvaa 124,228,220\nMvaa 120,224,220\ns_waitcnt vmcnt(15) lgkmcnt(0)\ns_barrier\ns_mov_b32 s50, 3\n.GL383:\nMvaa 64,160,128\nTa 192,12,16384\n"
"Mvaa 68,164,128\ns_mov_b32 s86, 0x1d2c3b4a\ns_load_dword s86, s[54:55], s94\nMvaa 128,168,128\nTa 194,12,18432\ns_add_u32 s94, s94, 1024\nMvaa 132,172,128\n"
"s_add_u32 m0, s47, 49152\ns_add_u32 s44, s44, 0x40000\nMaaa 0,176,128\nTa 196,13,16384\nMaaa 4,180,128\nD0 6,24,44,0\nMaaa 64,184,128\nTa 198,13,18432\nMaaa 68,188,128\n"
"s_cmp_eq_u32 s86, 0x1d2c3b4a\ns_addc_u32 s88, s88, 0\nMaaa 76,188,132\nTa 200,14,16384\nMaaa 72,184,132\nMaaa 12,180,132\nTa 202,14,18432\nMaaa 8,176,132\n"
"s_cmp_eq_u32 s86, 0x1d2c3b4a\ns_addc_u32 s88, s88, 0\nMvaa 140,172,132\nTa 204,15,16384\nMvaa 136,168,132\nD0 7,24,44,1024\nMvaa 76,164,132\nTa 206,15,18432\nMvaa 72,160,132\n"
"s_cmp_eq_u32 s86, 0x1d2c3b4a\ns_addc_u32 s88, s88, 0\nMvaa 80,160,136\nTa 208,16,16384\nMvaa 84,164,136\nMvaa 144,168,136\nTa 210,16,18432\nMvaa 148,172,136\n"
"s_cmp_eq_u32 s86, 0x1d2c3b4a\ns_addc_u32 s88, s88, 0\nMaaa 16,176,136\nTa 212,17,16384\nMaaa 20,180,136\nD0 8,24,44,2048\nMaaa 80,184,136\nTa 214,17,18432\nMaaa 84,188,136\n"
"s_cmp_eq_u32 s86, 0x1d2c3b4a\ns_addc_u32 s88, s88, 0\nMaaa 92,188,140\nTa 216,18,16384\nMaaa 88,184,140\nMaaa 28,180,140\nTa 218,18,18432\nMaaa 24,176,140\n"
"s_cmp_eq_u32 s86, 0x1d2c3b4a\ns_addc_u32 s88, s88, 0\nMvaa 156,172,140\nTa 220,19,16384\nMvaa 152,168,140\nD0 9,24,44,3072\nMvaa 92,164,140\nTa 222,19,18432\nMvaa 88,160,140\n"
"buffer_load_dword v31, v24, s[24:27], s96 offen\ns_add_u32 s96, s96, 0x80000\ns_cmp_eq_u32 s86, 0x1d2c3b4a\ns_addc_u32 s88, s88, 0\nMvaa 96,160,144\nRa 224,11,0\n"
"s_mov_b32 m0, s76\ns_add_u32 s76, s76, 1024\nMvaa 100,164,144\nD0 1,36,75,0\ns_add_u32 s75, s75, 1024\nMvaa 160,168,144\nRa 228,11,2048\nMvaa 164,172,144\ns_add_u32 m0, s47, 0\n"
"s_add_u32 s44, s44, 0x40000\ns_cmp_eq_u32 s86, 0x1d2c3b4a\ns_addc_u32 s88, s88, 0\nMaaa 32,176,144\nRa 232,11,8192\nMaaa 36,180,144\nD0 6,24,44,0\nMaaa 96,184,144\n"
"Ra 236,11,10240\nMaaa 100,188,144\ns_cmp_eq_u32 s86, 0x1d2c3b4a\ns_addc_u32 s88, s88, 0\nMaaa 108,188,148\nRa 240,11,16384\nMaaa 104,184,148\nMaaa 44,180,148\nRa 244,11,18432\n"
"Maaa 40,176,148\ns_cmp_eq_u32 s86, 0x1d2c3b4a\ns_addc_u32 s88, s88, 0\nMvaa 172,172,148\nRa 248,11,24576\nMvaa 168,168,148\nD0 7,24,44,1024\nMvaa 108,164,148\nRa 252,11,26624\n"
"Mvaa 104,160,148\ns_cmp_eq_u32 s86, 0x1d2c3b4a\ns_addc_u32 s88, s88, 0\nMvaa 112,160,152\nMvaa 116,164,152\nMvaa 176,168,152\nMvaa 180,172,152\ns_cmp_eq_u32 s86, 0x1d2c3b4a\n"
"s_addc_u32 s88, s88, 0\nMaaa 48,176,152\nMaaa 52,180,152\nD0 8,24,44,2048\nMaaa 112,184,152\nMaaa 116,188,152\ns_cmp_eq_u32 s86, 0x1d2c3b4a\ns_addc_u32 s88, s88, 0\n"
"Maaa 124,188,156\nMaaa 120,184,156\nMaaa 60,180,156\nMaaa 56,176,156\ns_cmp_eq_u32 s86, 0x1d2c3b4a\ns_addc_u32 s88, s88, 0\nMvaa 188,172,156\nMvaa 184,168,156\nD0 9,24,44,3072\n"
"Mvaa 124,164,156\nMvaa 120,160,156\ns_waitcnt vmcnt(10) lgkmcnt(0)\ns_barrier\nMvaa 64,224,192\nTa 128,12,32768\nMvaa 68,228,192\ns_mov_b32 s86, 0x1d2c3b4a\n"
"s_load_dword s86, s[54:55], s95\nMvaa 128,232,192\nTa 130,12,34816\ns_add_u32 s95, s95, 1024\nMvaa 132,236,192\ns_add_u32 m0, s48, 0\ns_add_u32 s45, s45, 128\nMaaa 0,240,192\n"
"Ta 132,13,32768\nMaaa 4,244,192\nD0 2,20,45,0\nMaaa 64,248,192\nTa 134,13,34816\nMaaa 68,252,192\ns_cmp_eq_u32 s86, 0x1d2c3b4a\ns_subb_u32 s88, s88, 0\nMaaa 76,252,196\n"
"Ta 136,14,32768\nMaaa 72,248,196\nMaaa 12,244,196\nTa 138,14,34816\nMaaa 8,240,196\ns_cmp_eq_u32 s86, 0x1d2c3b4a\ns_subb_u32 s88, s88, 0\nMvaa 140,236,196\nTa 140,15,32768\n"
"Mvaa 136,232,196\nD0 3,20,45,1024\nMvaa 76,228,196\nTa 142,15,34816\nMvaa 72,224,196\ns_cmp_eq_u32 s86, 0x1d2c3b4a\ns_subb_u32 s88, s88, 0\nMvaa 80,224,200\nTa 144,16,32768\n"
"Mvaa 84,228,200\nMvaa 144,232,200\nTa 146,16,34816\nMvaa 148,236,200\ns_cmp_eq_u32 s86, 0x1d2c3b4a\ns_subb_u32 s88, s88, 0\nMaaa 16,240,200\nTa 148,17,32768\nMaaa 20,244,200\n"
"D0 4,20,45,2048\nMaaa 80,248,200\nTa 150,17,34816\nMaaa 84,252,200\ns_cmp_eq_u32 s86, 0x1d2c3b4a\ns_subb_u32 s88, s88, 0\nMaaa 92,252,204\nTa 152,18,32768\nMaaa 88,248,204\n"
"Maaa 28,244,204\nTa 154,18,34816\nMaaa 24,240,204\ns_cmp_eq_u32 s86, 0x1d2c3b4a\ns_subb_u32 s88, s88, 0\nMvaa 156,236,204\nTa 156,19,32768\nMvaa 152,232,204\nD0 5,20,45,3072\n"
"Mvaa 92,228,204\nTa 158,19,34816\nMvaa 88,224,204\ns_cmp_eq_u32 s86, 0x1d2c3b4a\ns_subb_u32 s88, s88, 0\nMvaa 96,224,208\nRa 160,10,32768\nMvaa 100,228,208\nMvaa 160,232,208\n"
"Ra 164,10,34816\nMvaa 164,236,208\ns_add_u32 m0, s48, 4096\ns_add_u32 s46, s45, 0x10000\ns_cmp_eq_u32 s86, 0x1d2c3b4a\ns_subb_u32 s88, s88, 0\nMaaa 32,240,208\nRa 168,10,40960\n"
"Maaa 36,244,208\nD0 2,20,46,0\nMaaa 96,248,208\nRa 172,10,43008\nMaaa 100,252,208\ns_cmp_eq_u32 s86, 0x1d2c3b4a\ns_subb_u32 s88, s88, 0\nMaaa 108,252,212\nRa 176,10,49152\n"
"Maaa 104,248,212\nMaaa 44,244,212\nRa 180,10,51200\nMaaa 40,240,212\ns_cmp_eq_u32 s86, 0x1d2c3b4a\ns_subb_u32 s88, s88, 0\nMvaa 172,236,212\nRa 184,10,57344\nMvaa 168,232,212\n"
"D0 3,20,46,1024\nMvaa 108,228,212\nRa 188,10,59392\nMvaa 104,224,212\ns_cmp_eq_u32 s86, 0x1d2c3b4a\ns_subb_u32 s88, s88, 0\nMvaa 112,224,216\nMvaa 116,228,216\nMvaa 176,232,216\n"
"Mvaa 180,236,216\ns_cmp_eq_u32 s86, 0x1d2c3b4a\ns_subb_u32 s88, s88, 0\nMaaa 48,240,216\nMaaa 52,244,216\nD0 4,20,46,2048\nMaaa 112,248,216\nMaaa 116,252,216\n"
"s_cmp_eq_u32 s86, 0x1d2c3b4a\ns_subb_u32 s88, s88, 0\nMaaa 124,252,220\nMaaa 120,248,220\nMaaa 60,244,220\nMaaa 56,240,220\ns_cmp_eq_u32 s86, 0x1d2c3b4a\ns_subb_u32 s88, s88, 0\n"
"Mvaa 188,236,220\nMvaa 184,232,220\nD0 5,20,46,3072\nMvaa 124,228,220\nMvaa 120,224,220\ns_waitcnt vmcnt(14) lgkmcnt(0)\ns_barrier\nMvaa 64,160,128\nTa 192,12,49152\n"
"Mvaa 68,164,128\ns_mov_b32 s86, 0x1d2c3b4a\ns_load_dword s86, s[54:55], s95\nMvaa 128,168,128\nTa 194,12,51200\ns_add_u32 s95, s95, 1024\nMvaa 132,172,128\n"
"s_add_u32 m0, s47, 16384\ns_add_u32 s44, s44, 0x40000\nMaaa 0,176,128\nTa 196,13,49152\nMaaa 4,180,128\nD0 6,24,44,0\nMaaa 64,184,128\nTa 198,13,51200\nMaaa 68,188,128\n"
"s_cmp_eq_u32 s86, 0x1d2c3b4a\ns_subb_u32 s88, s88, 0\nMaaa 76,188,132\nTa 200,14,49152\nMaaa 72,184,132\nMaaa 12,180,132\nTa 202,14,51200\nMaaa 8,176,132\n"
"s_cmp_eq_u32 s86, 0x1d2c3b4a\ns_subb_u32 s88, s88, 0\nMvaa 140,172,132\nTa 204,15,49152\nMvaa 136,168,132\nD0 7,24,44,1024\nMvaa 76,164,132\nTa 206,15,51200\nMvaa 72,160,132\n"
"s_cmp_eq_u32 s86, 0x1d2c3b4a\ns_subb_u32 s88, s88, 0\nMvaa 80,160,136\nTa 208,16,49152\nMvaa 84,164,136\nMvaa 144,168,136\nTa 210,16,51200\nMvaa 148,172,136\n"
"s_cmp_eq_u32 s86, 0x1d2c3b4a\ns_subb_u32 s88, s88, 0\nMaaa 16,176,136\nTa 212,17,49152\nMaaa 20,180,136\nD0 8,24,44,2048\nMaaa 80,184,136\nTa 214,17,51200\nMaaa 84,188,136\n"
"s_cmp_eq_u32 s86, 0x1d2c3b4a\ns_subb_u32 s88, s88, 0\nMaaa 92,188,140\nTa 216,18,49152\nMaaa 88,184,140\nMaaa 28,180,140\nTa 218,18,51200\nMaaa 24,176,140\n"
"s_cmp_eq_u32 s86, 0x1d2c3b4a\ns_subb_u32 s88, s88, 0\nMvaa 156,172,140\nTa 220,19,49152\nMvaa 152,168,140\nD0 9,24,44,3072\nMvaa 92,164,140\nTa 222,19,51200\nMvaa 88,160,140\n"
"buffer_load_dword v31, v24, s[24:27], s96 offen\ns_add_u32 s96, s96, 0x80000\ns_cmp_eq_u32 s86, 0x1d2c3b4a\ns_subb_u32 s88, s88, 0\nMvaa 96,160,144\nRa 224,11,32768\n"
"s_mov_b32 m0, s76\ns_add_u32 s76, s76, 1024\nMvaa 100,164,144\nD0 1,36,75,0\ns_add_u32 s75, s75, 1024\nMvaa 160,168,144\nRa 228,11,34816\nMvaa 164,172,144\n"
"s_add_u32 m0, s47, 32768\ns_add_u32 s44, s44, 0x40000\ns_cmp_eq_u32 s86, 0x1d2c3b4a\ns_subb_u32 s88, s88, 0\nMaaa 32,176,144\nRa 232,11,40960\nMaaa 36,180,144\nD0 6,24,44,0\n"
"Maaa 96,184,144\nRa 236,11,43008\nMaaa 100,188,144\ns_cmp_eq_u32 s86, 0x1d2c3b4a\ns_subb_u32 s88, s88, 0\nMaaa 108,188,148\nRa 240,11,49152\nMaaa 104,184,148\nMaaa 44,180,148\n"
"Ra 244,11,51200\nMaaa 40,176,148\ns_cmp_eq_u32 s86, 0x1d2c3b4a\ns_subb_u32 s88, s88, 0\nMvaa 172,172,148\nRa 248,11,57344\nMvaa 168,168,148\nD0 7,24,44,1024\nMvaa 108,164,148\n"
"Ra 252,11,59392\nMvaa 104,160,148\ns_cmp_eq_u32 s86, 0x1d2c3b4a\ns_subb_u32 s88, s88, 0\nMvaa 112,160,152\nMvaa 116,164,152\nMvaa 176,168,152\nMvaa 180,172,152\n"
"s_cmp_eq_u32 s86, 0x1d2c3b4a\ns_subb_u32 s88, s88, 0\nMaaa 48,176,152\nMaaa 52,180,152\nD0 8,24,44,2048\nMaaa 112,184,152\nMaaa 116,188,152\ns_cmp_eq_u32 s86, 0x1d2c3b4a\n"
"s_subb_u32 s88, s88, 0\nMaaa 124,188,156\nMaaa 120,184,156\nMaaa 60,180,156\nMaaa 56,176,156\ns_cmp_eq_u32 s86, 0x1d2c3b4a\ns_subb_u32 s88, s88, 0\nMvaa 188,172,156\n"
"Mvaa 184,168,156\nD0 9,24,44,3072\nMvaa 124,164,156\nMvaa 120,160,156\ns_waitcnt vmcnt(10) lgkmcnt(0)\ns_barrier\nMvaa 64,224,192\nTa 128,12,0\nMvaa 68,228,192\n"
"s_mov_b32 s86, 0x1d2c3b4a\ns_load_dword s86, s[54:55], s94\nMvaa 128,232,192\nTa 130,12,2048\ns_add_u32 s94, s94, 1024\nMvaa 132,236,192\ns_add_u32 m0, s48, 32768\n"
"s_add_u32 s45, s45, 128\nMaaa 0,240,192\nTa 132,13,0\nMaaa 4,244,192\nD0 2,20,45,0\nMaaa 64,248,192\nTa 134,13,2048\nMaaa 68,252,192\ns_cmp_eq_u32 s86, 0x1d2c3b4a\n"
"s_addc_u32 s88, s88, 0\nMaaa 76,252,196\nTa 136,14,0\nMaaa 72,248,196\nMaaa 12,244,196\nTa 138,14,2048\nMaaa 8,240,196\ns_cmp_eq_u32 s86, 0x1d2c3b4a\ns_addc_u32 s88, s88, 0\n"
"Mvaa 140,236,196\nTa 140,15,0\nMvaa 136,232,196\nD0 3,20,45,1024\nMvaa 76,228,196\nTa 142,15,2048\nMvaa 72,224,196\ns_cmp_eq_u32 s86, 0x1d2c3b4a\ns_addc_u32 s88, s88, 0\n"
"Mvaa 80,224,200\nTa 144,16,0\nMvaa 84,228,200\nMvaa 144,232,200\nTa 146,16,2048\nMvaa 148,236,200\ns_cmp_eq_u32 s86, 0x1d2c3b4a\ns_addc_u32 s88, s88, 0\nMaaa 16,240,200\n"
"Ta 148,17,0\nMaaa 20,244,200\nD0 4,20,45,2048\nMaaa 80,248,200\nTa 150,17,2048\nMaaa 84,252,200\ns_cmp_eq_u32 s86, 0x1d2c3b4a\ns_addc_u32 s88, s88, 0\nMaaa 92,252,204\n"
"Ta 152,18,0\nMaaa 88,248,204\nMaaa 28,244,204\nTa 154,18,2048\nMaaa 24,240,204\ns_cmp_eq_u32 s86, 0x1d2c3b4a\ns_addc_u32 s88, s88, 0\nMvaa 156,236,204\nTa 156,19,0\n"
"Mvaa 152,232,204\nD0 5,20,45,3072\nMvaa 92,228,204\nTa 158,19,2048\nMvaa 88,224,204\ns_cmp_eq_u32 s86, 0x1d2c3b4a\ns_addc_u32 s88, s88, 0\nMvaa 96,224,208\nRa 160,10,0\n"
"Mvaa 100,228,208\nMvaa 160,232,208\nRa 164,10,2048\nMvaa 164,236,208\ns_add_u32 m0, s48, 36864\ns_add_u32 s46, s45, 0x10000\ns_cmp_eq_u32 s86, 0x1d2c3b4a\ns_addc_u32 s88, s88, 0\n"
"Maaa 32,240,208\nRa 168,10,8192\nMaaa 36,244,208\nD0 2,20,46,0\nMaaa 96,248,208\nRa 172,10,10240\nMaaa 100,252,208\ns_cmp_eq_u32 s86, 0x1d2c3b4a\ns_addc_u32 s88, s88, 0\n"
"Maaa 108,252,212\nRa 176,10,16384\nMaaa 104,248,212\nMaaa 44,244,212\nRa 180,10,18432\nMaaa 40,240,212\ns_cmp_eq_u32 s86, 0x1d2c3b4a\ns_addc_u32 s88, s88, 0\nMvaa 172,236,212\n"
"Ra 184,10,24576\nMvaa 168,232,212\nD0 3,20,46,1024\nMvaa 108,228,212\nRa 188,10,26624\nMvaa 104,224,212\ns_cmp_eq_u32 s86, 0x1d2c3b4a\ns_addc_u32 s88, s88, 0\nMvaa 112,224,216\n"
"Mvaa 116,228,216\nMvaa 176,232,216\nMvaa 180,236,216\ns_cmp_eq_u32 s86, 0x1d2c3b4a\ns_addc_u32 s88, s88, 0\nMaaa 48,240,216\nMaaa 52,244,216\nD0 4,20,46,2048\nMaaa 112,248,216\n"
"Maaa 116,252,216\ns_cmp_eq_u32 s86, 0x1d2c3b4a\ns_addc_u32 s88, s88, 0\nMaaa 124,252,220\nMaaa 120,248,220\nMaaa 60,244,220\nMaaa 56,240,220\ns_cmp_eq_u32 s86, 0x1d2c3b4a\n"
"s_addc_u32 s88, s88, 0\nMvaa 188,236,220\ns_xor_b32 s94, s94, 0x2000\ns_xor_b32 s95, s95, 0x2000\ns_sub_u32 s88, 0, s88\nMvaa 184,232,220\nD0 5,20,46,3072\ns_sub_u32 s50, s50, 1\n"
"Mvaa 124,228,220\nMvaa 120,224,220\ns_waitcnt vmcnt(14) lgkmcnt(0)\ns_barrier\ns_cbranch_scc0 .GL383\nMvaa 64,160,128\nTa 192,12,16384\nMvaa 68,164,128\ns_cmp_gt_i32 s88, 0\n"
"s_cselect_b32 s88, 0x2000, 0\nMvaa 128,168,128\nTa 194,12,18432\nMvaa 132,172,128\ns_add_u32 m0, s47, 49152\ns_add_u32 s44, s44, 0x40000\nMaaa 0,176,128\nTa 196,13,16384\n"
"Maaa 4,180,128\nD0 6,24,44,0\nMaaa 64,184,128\nTa 198,13,18432\nMaaa 68,188,128\nv_xor_b32 v27, s88, v25\nMaaa 76,188,132\nTa 200,14,16384\nMaaa 72,184,132\n"
"buffer_store_dword v27, off, s[16:19], s93 sc0 sc1\nMaaa 12,180,132\nTa 202,14,18432\nMaaa 8,176,132\nMvaa 140,172,132\nTa 204,15,16384\nMvaa 136,168,132\nD0 7,24,44,1024\n"
"Mvaa 76,164,132\nTa 206,15,18432\nMvaa 72,160,132\nMvaa 80,160,136\nTa 208,16,16384\nMvaa 84,164,136\nMvaa 144,168,136\nTa 210,16,18432\nMvaa 148,172,136\nMaaa 16,176,136\n"
"Ta 212,17,16384\nMaaa 20,180,136\nD0 8,24,44,2048\nMaaa 80,184,136\nTa 214,17,18432\nMaaa 84,188,136\nMaaa 92,188,140\nTa 216,18,16384\nMaaa 88,184,140\nMaaa 28,180,140\n"
"Ta 218,18,18432\nMaaa 24,176,140\nMvaa 156,172,140\nTa 220,19,16384\nMvaa 152,168,140\nD0 9,24,44,3072\nMvaa 92,164,140\nTa 222,19,18432\nMvaa 88,160,140\n"
"buffer_load_dword v31, v24, s[24:27], s96 offen\ns_add_u32 s96, s96, 0x80000\nMvaa 96,160,144\nRa 224,11,0\nMvaa 100,164,144\nMvaa 160,168,144\nRa 228,11,2048\nMvaa 164,172,144\n"
"s_add_u32 m0, s47, 0\ns_add_u32 s44, s44, 0x40000\nMaaa 32,176,144\nRa 232,11,8192\nMaaa 36,180,144\nD0 6,24,44,0\nMaaa 96,184,144\nRa 236,11,10240\nMaaa 100,188,144\n"
"Maaa 108,188,148\nRa 240,11,16384\nMaaa 104,184,148\nMaaa 44,180,148\nRa 244,11,18432\nMaaa 40,176,148\nMvaa 172,172,148\nRa 248,11,24576\nMvaa 168,168,148\nD0 7,24,44,1024\n"
"Mvaa 108,164,148\nRa 252,11,26624\nMvaa 104,160,148\nMvaa 112,160,152\nMvaa 116,164,152\nMvaa 176,168,152\nMvaa 180,172,152\nMaaa 48,176,152\nMaaa 52,180,152\nD0 8,24,44,2048\n"
"Maaa 112,184,152\nMaaa 116,188,152\nMaaa 124,188,156\nMaaa 120,184,156\nMaaa 60,180,156\nMaaa 56,176,156\nMvaa 188,172,156\nMvaa 184,168,156\nD0 9,24,44,3072\nMvaa 124,164,156\n"
"Mvaa 120,160,156\ns_waitcnt vmcnt(10) lgkmcnt(0)\ns_barrier\nMvaa 64,224,192\nTa 128,12,32768\nMvaa 68,228,192\nMvaa 128,232,192\nTa 130,12,34816\nMvaa 132,236,192\n"
"s_add_u32 m0, s48, 0\ns_add_u32 s45, s45, 128\nMaaa 0,240,192\nTa 132,13,32768\nMaaa 4,244,192\nD0 2,20,45,0\nMaaa 64,248,192\nTa 134,13,34816\nMaaa 68,252,192\nMaaa 76,252,196\n"
"Ta 136,14,32768\nMaaa 72,248,196\nMaaa 12,244,196\nTa 138,14,34816\nMaaa 8,240,196\nMvaa 140,236,196\nTa 140,15,32768\nMvaa 136,232,196\nD0 3,20,45,1024\nMvaa 76,228,196\n"
"Ta 142,15,34816\nMvaa 72,224,196\nMvaa 80,224,200\nTa 144,16,32768\nMvaa 84,228,200\nMvaa 144,232,200\nTa 146,16,34816\nMvaa 148,236,200\nMaaa 16,240,200\nTa 148,17,32768\n"
"Maaa 20,244,200\nD0 4,20,45,2048\nMaaa 80,248,200\nTa 150,17,34816\nMaaa 84,252,200\nMaaa 92,252,204\nTa 152,18,32768\nMaaa 88,248,204\nMaaa 28,244,204\nTa 154,18,34816\n"
"Maaa 24,240,204\nMvaa 156,236,204\nTa 156,19,32768\nMvaa 152,232,204\nD0 5,20,45,3072\nMvaa 92,228,204\nTa 158,19,34816\nMvaa 88,224,204\nMvaa 96,224,208\nRa 160,10,32768\n"
"Mvaa 100,228,208\nMvaa 160,232,208\nRa 164,10,34816\nMvaa 164,236,208\ns_add_u32 m0, s48, 4096\ns_add_u32 s46, s45, 0x10000\nMaaa 32,240,208\nRa 168,10,40960\nMaaa 36,244,208\n"
"D0 2,20,46,0\nMaaa 96,248,208\nRa 172,10,43008\nMaaa 100,252,208\nMaaa 108,252,212\nRa 176,10,49152\nMaaa 104,248,212\nMaaa 44,244,212\nRa 180,10,51200\nMaaa 40,240,212\n"
"Mvaa 172,236,212\nRa 184,10,57344\nMvaa 168,232,212\nD0 3,20,46,1024\nMvaa 108,228,212\nRa 188,10,59392\nMvaa 104,224,212\nMvaa 112,224,216\nMvaa 116,228,216\nMvaa 176,232,216\n"
"Mvaa 180,236,216\nMaaa 48,240,216\nMaaa 52,244,216\nD0 4,20,46,2048\nMaaa 112,248,216\nMaaa 116,252,216\nMaaa 124,252,220\nMaaa 120,248,220\nMaaa 60,244,220\nMaaa 56,240,220\n"
"Mvaa 188,236,220\nMvaa 184,232,220\nD0 5,20,46,3072\nMvaa 124,228,220\nMvaa 120,224,220\ns_waitcnt vmcnt(13) lgkmcnt(0)\ns_barrier\nMvaa 64,160,128\nTa 192,12,49152\n"
"Mvaa 68,164,128\nMvaa 128,168,128\nTa 194,12,51200\nMvaa 132,172,128\ns_add_u32 m0, s47, 16384\ns_add_u32 s44, s44, 0x40000\nMaaa 0,176,128\nTa 196,13,49152\nMaaa 4,180,128\n"
"D0 6,24,44,0\nMaaa 64,184,128\nTa 198,13,51200\nMaaa 68,188,128\nMaaa 76,188,132\nTa 200,14,49152\nMaaa 72,184,132\nMaaa 12,180,132\nTa 202,14,51200\nMaaa 8,176,132\n"
"Mvaa 140,172,132\nTa 204,15,49152\nMvaa 136,168,132\nD0 7,24,44,1024\nMvaa 76,164,132\nTa 206,15,51200\nMvaa 72,160,132\nMvaa 80,160,136\nTa 208,16,49152\nMvaa 84,164,136\n"
"Mvaa 144,168,136\nTa 210,16,51200\nMvaa 148,172,136\nMaaa 16,176,136\nTa 212,17,49152\nMaaa 20,180,136\nD0 8,24,44,2048\nMaaa 80,184,136\nTa 214,17,51200\nMaaa 84,188,136\n"
"Maaa 92,188,140\nTa 216,18,49152\nMaaa 88,184,140\nMaaa 28,180,140\nTa 218,18,51200\nMaaa 24,176,140\nMvaa 156,172,140\nTa 220,19,49152\nMvaa 152,168,140\nD0 9,24,44,3072\n"
"Mvaa 92,164,140\nTa 222,19,51200\nMvaa 88,160,140\nbuffer_load_dword v31, v24, s[24:27], s96 offen\ns_add_u32 s96, s96, 0x80000\nMvaa 96,160,144\nRa 224,11,32768\n"
"Mvaa 100,164,144\nMvaa 160,168,144\nRa 228,11,34816\nMvaa 164,172,144\ns_add_u32 m0, s47, 32768\ns_add_u32 s44, s44, 0x40000\nMaaa 32,176,144\nRa 232,11,40960\nMaaa 36,180,144\n"
"D0 6,24,44,0\nMaaa 96,184,144\nRa 236,11,43008\nMaaa 100,188,144\nMaaa 108,188,148\nRa 240,11,49152\nMaaa 104,184,148\ns_lshl_b32 s5, s73, 8\ns_add_u32 s5, s5, 0xc04ac00\n"
"v_lshrrev_b32 v27, 2, v1\nbuffer_load_dword v27, v27, s[16:19], s5 offen sc0 sc1\nMaaa 44,180,148\nRa 244,11,51200\nMaaa 40,176,148\nMvaa 172,172,148\nRa 248,11,57344\n"
"Mvaa 168,168,148\nD0 7,24,44,1024\nMvaa 108,164,148\nRa 252,11,59392\nMvaa 104,160,148\nMvaa 112,160,152\nMvaa 116,164,152\nMvaa 176,168,152\nMvaa 180,172,152\nMaaa 48,176,152\n"
"Maaa 52,180,152\nD0 8,24,44,2048\nMaaa 112,184,152\nMaaa 116,188,152\nMaaa 124,188,156\nMaaa 120,184,156\nMaaa 60,180,156\nMaaa 56,176,156\nMvaa 188,172,156\nMvaa 184,168,156\n"
"D0 9,24,44,3072\nMvaa 124,164,156\nMvaa 120,160,156\ns_waitcnt vmcnt(10) lgkmcnt(0)\ns_barrier\nMvaa 64,224,192\nTa 128,12,0\nMvaa 68,228,192\nMvaa 128,232,192\nTa 130,12,2048\n"
"Mvaa 132,236,192\ns_add_u32 m0, s48, 32768\ns_add_u32 s45, s45, 128\nMaaa 0,240,192\nTa 132,13,0\nMaaa 4,244,192\nD0 2,20,45,0\nMaaa 64,248,192\nTa 134,13,2048\nMaaa 68,252,192\n"
"Maaa 76,252,196\nTa 136,14,0\nMaaa 72,248,196\nMaaa 12,244,196\nTa 138,14,2048\nMaaa 8,240,196\nMvaa 140,236,196\nTa 140,15,0\nMvaa 136,232,196\nD0 3,20,45,1024\nMvaa 76,228,196\n"
"Ta 142,15,2048\nMvaa 72,224,196\nMvaa 80,224,200\nTa 144,16,0\nMvaa 84,228,200\nMvaa 144,232,200\nTa 146,16,2048\nMvaa 148,236,200\nMaaa 16,240,200\nTa 148,17,0\nMaaa 20,244,200\n"
"D0 4,20,45,2048\nMaaa 80,248,200\nTa 150,17,2048\nMaaa 84,252,200\nMaaa 92,252,204\nTa 152,18,0\nMaaa 88,248,204\nMaaa 28,244,204\nTa 154,18,2048\nMaaa 24,240,204\n"
"Mvaa 156,236,204\nTa 156,19,0\nMvaa 152,232,204\nD0 5,20,45,3072\nMvaa 92,228,204\nTa 158,19,2048\nMvaa 88,224,204\nMvaa 96,224,208\nRa 160,10,0\nMvaa 100,228,208\n"
"Mvaa 160,232,208\nRa 164,10,2048\nMvaa 164,236,208\ns_add_u32 m0, s48, 36864\ns_add_u32 s46, s45, 0x10000\nMaaa 32,240,208\nRa 168,10,8192\nMaaa 36,244,208\nD0 2,20,46,0\n"
"Maaa 96,248,208\nRa 172,10,10240\nMaaa 100,252,208\nMaaa 108,252,212\nRa 176,10,16384\nMaaa 104,248,212\nMaaa 44,244,212\nRa 180,10,18432\nMaaa 40,240,212\nMvaa 172,236,212\n"
"Ra 184,10,24576\nMvaa 168,232,212\nD0 3,20,46,1024\nMvaa 108,228,212\nRa 188,10,26624\nMvaa 104,224,212\nMvaa 112,224,216\nMvaa 116,228,216\nMvaa 176,232,216\nMvaa 180,236,216\n"
"Maaa 48,240,216\nMaaa 52,244,216\nD0 4,20,46,2048\nMaaa 112,248,216\nMaaa 116,252,216\nMaaa 124,252,220\nMaaa 120,248,220\nMaaa 60,244,220\nMaaa 56,240,220\nMvaa 188,236,220\n"
"Mvaa 184,232,220\nD0 5,20,46,3072\nMvaa 124,228,220\nMvaa 120,224,220\ns_waitcnt vmcnt(14) lgkmcnt(0)\ns_barrier\nMvaa 64,160,128\nTa 192,12,16384\nWV 11\nv_xor_b32 v27, s15, v27\n"
"v_and_b32 v32, 0xffffdfff, v27\nv_cmp_ne_u32 vcc, 0, v32\ns_nop 0\ns_cbranch_vccnz .S380\n.S381:\nv_cmp_ne_u32 vcc, 0, v27\ns_nop 3\ns_bcnt1_i32_b64 s4, vcc\ns_cmp_gt_u32 s4, 32\n"
"s_cselect_b32 s4, 1, 0\nMvaa 68,164,128\ns_xor_b32 s4, s4, s12\nMvaa 128,168,128\nTa 194,12,18432\nMvaa 132,172,128\ns_add_u32 m0, s47, 49152\ns_add_u32 s44, s44, 0x40000\n"
"Maaa 0,176,128\nTa 196,13,16384\nMaaa 4,180,128\nD0 6,24,44,0\nMaaa 64,184,128\nTa 198,13,18432\nMaaa 68,188,128\ns_and_b32 s4, s4, 1\nMaaa 76,188,132\nTa 200,14,16384\n"
"Maaa 72,184,132\ns_lshl_b32 s4, s4, 13\nMaaa 12,180,132\nTa 202,14,18432\nMaaa 8,176,132\ns_mul_i32 s72, s73, 25165824\nMvaa 140,172,132\nTa 204,15,16384\nMvaa 136,168,132\n"
"D0 7,24,44,1024\nMvaa 76,164,132\nTa 206,15,18432\nMvaa 72,160,132\ns_add_u32 s72, s72, s4\nMvaa 80,160,136\nTa 208,16,16384\nMvaa 84,164,136\ns_and_b32 s4, s12, 1\n"
"Mvaa 144,168,136\nTa 210,16,18432\nMvaa 148,172,136\ns_lshl_b32 s4, s4, 21\nMaaa 16,176,136\nTa 212,17,16384\nMaaa 20,180,136\nD0 8,24,44,2048\nMaaa 80,184,136\nTa 214,17,18432\n"
"Maaa 84,188,136\ns_add_u32 s72, s72, s4\nMaaa 92,188,140\nTa 216,18,16384\nMaaa 88,184,140\ns_lshl3_add_u32 s2, s13, s11\nMaaa 28,180,140\nTa 218,18,18432\nMaaa 24,176,140\n"
"s_lshl_b32 s2, s2, 11\nMvaa 156,172,140\nTa 220,19,16384\nMvaa 152,168,140\nD0 9,24,44,3072\nMvaa 92,164,140\nTa 222,19,18432\nMvaa 88,160,140\n"
"buffer_load_dword v31, v24, s[24:27], s96 offen\ns_add_u32 s96, s96, 0x80000\nMvaa 96,160,144\nRa 224,11,0\nMvaa 100,164,144\ns_lshl_b32 s3, s14, 22\nMvaa 160,168,144\n"
"Ra 228,11,2048\nMvaa 164,172,144\ns_add_u32 m0, s47, 0\ns_add_u32 s44, s44, 0x40000\nMaaa 32,176,144\nRa 232,11,8192\nMaaa 36,180,144\nD0 6,24,44,0\nMaaa 96,184,144\n"
"Ra 236,11,10240\nMaaa 100,188,144\ns_add_u32 s2, s2, s3\nMaaa 108,188,148\nRa 240,11,16384\nMaaa 104,184,148\ns_add_u32 s2, s2, s72\nMaaa 44,180,148\nRa 244,11,18432\n"
"Maaa 40,176,148\ns_add_u32 s49, s2, 0xc00000\nMvaa 172,172,148\nRa 248,11,24576\nMvaa 168,168,148\nD0 7,24,44,1024\nMvaa 108,164,148\nRa 252,11,26624\nMvaa 104,160,148\n"
"Mvaa 112,160,152\nMvaa 116,164,152\nMvaa 176,168,152\nMvaa 180,172,152\nMaaa 48,176,152\nMaaa 52,180,152\nD0 8,24,44,2048\nMaaa 112,184,152\nMaaa 116,188,152\nMaaa 124,188,156\n"
"Maaa 120,184,156\nMaaa 60,180,156\nMaaa 56,176,156\nMvaa 188,172,156\nMvaa 184,168,156\nD0 9,24,44,3072\nMvaa 124,164,156\nMvaa 120,160,156\ns_waitcnt vmcnt(9) lgkmcnt(0)\n"
"s_barrier\nMvaa 64,224,192\nTa 128,12,32768\ns_mov_b32 s4, s13\nMvaa 68,228,192\nv_lshrrev_b32 v32, 2, v0\nMvaa 128,232,192\nTa 130,12,34816\nv_mul_u32_u24 v32, 896, v32\n"
"Mvaa 132,236,192\ns_add_u32 m0, s48, 0\ns_add_u32 s45, s45, 128\nMaaa 0,240,192\nTa 132,13,32768\nv_and_b32 v33, 3, v0\nMaaa 4,244,192\nD0 2,20,45,0\nMaaa 64,248,192\n"
"Ta 134,13,34816\nv_lshl_add_u32 v32, v33, 4, v32\nMaaa 68,252,192\ns_lshr_b32 s2, s11, 1\nMaaa 76,252,196\nTa 136,14,32768\ns_mul_i32 s3, s2, 448\nMaaa 72,248,196\n"
"s_and_b32 s5, s11, 1\nMaaa 12,244,196\nTa 138,14,34816\ns_lshl3_add_u32 s3, s5, s3\nMaaa 8,240,196\ns_add_u32 s3, s3, 131072\nMvaa 140,236,196\nTa 140,15,32768\n"
"v_add_u32 v32, s3, v32\nMvaa 136,232,196\nD0 3,20,45,1024\nMvaa 76,228,196\nTa 142,15,34816\nTv 36,32,0\nMvaa 72,224,196\nTv 38,32,14336\nMvaa 80,224,200\nTa 144,16,32768\n"
"Tv 40,32,64\nMvaa 84,228,200\nTv 42,32,14400\nMvaa 144,232,200\nTa 146,16,34816\nTv 44,32,128\nMvaa 148,236,200\nTv 46,32,14464\nMaaa 16,240,200\nTa 148,17,32768\nTv 48,32,192\n"
"Maaa 20,244,200\nD0 4,20,45,2048\nMaaa 80,248,200\nTa 150,17,34816\nTv 50,32,14528\nMaaa 84,252,200\nTv 52,32,256\nMaaa 92,252,204\nTa 152,18,32768\nTv 54,32,14592\n"
"Maaa 88,248,204\nTv 56,32,320\nMaaa 28,244,204\nTa 154,18,34816\nTv 58,32,14656\nMaaa 24,240,204\nTv 60,32,384\nMvaa 156,236,204\nTa 156,19,32768\nTv 62,32,14720\n"
"Mvaa 152,232,204\nD0 5,20,45,3072\nMvaa 92,228,204\nTa 158,19,34816\ns_mul_i32 s3, s2, 229376\nMvaa 88,224,204\ns_add_u32 s3, s3, s72\nMvaa 96,224,208\nRa 160,10,32768\n"
"s_add_u32 s3, s3, 0x1400000\nMvaa 100,228,208\ns_lshr_b32 s0, s4, 1\nMvaa 160,232,208\nRa 164,10,34816\ns_mul_i32 s0, s0, 28\nMvaa 164,236,208\ns_add_u32 m0, s48, 4096\n"
"s_add_u32 s46, s45, 0x10000\nMaaa 32,240,208\nRa 168,10,40960\ns_lshl1_add_u32 s0, s5, s0\nMaaa 36,244,208\nD0 2,20,46,0\nMaaa 96,248,208\nRa 172,10,43008\ns_and_b32 s6, s4, 1\n"
"Maaa 100,252,208\ns_add_u32 s0, s0, s6\nMaaa 108,252,212\nRa 176,10,49152\ns_lshr_b32 s77, s4, 2\nMaaa 104,248,212\ns_mul_i32 s1, s77, 16\nMaaa 44,244,212\nRa 180,10,51200\n"
"s_lshl2_add_u32 s1, s5, s1\nMaaa 40,240,212\ns_and_b32 s6, s4, 3\nMvaa 172,236,212\nRa 184,10,57344\ns_add_u32 s1, s1, s6\nMvaa 168,232,212\nD0 3,20,46,1024\nMvaa 108,228,212\n"
"Ra 188,10,59392\ns_lshl1_add_u32 s77, s5, s77\nMvaa 104,224,212\ns_lshl2_add_u32 s77, s77, s6\nMvaa 112,224,216\ns_add_u32 s77, s77, 32\ns_and_b32 s4, s12, 1\nMvaa 116,228,216\n"
"s_mov_b32 s7, 0x6543210\ns_cmp_eq_u32 s4, s2\ns_cselect_b32 s6, s0, s1\ns_cselect_b32 s5, 4, 8\ns_cselect_b32 s7, 0x6352410, s7\nMvaa 176,232,216\nWL 14\ns_lshr_b32 s0, s6, 4\n"
"Mvaa 180,236,216\ns_lshl_b32 s0, s0, 2\nMaaa 48,240,216\ns_lshr_b32 s0, s7, s0\ns_and_b32 s0, s0, 7\nMaaa 52,244,216\nD0 4,20,46,2048\nMaaa 112,248,216\ns_lshl_b32 s0, s0, 5\n"
"s_and_b32 s1, s6, 15\nMaaa 116,252,216\ns_add_u32 s0, s0, s1\nMaaa 124,252,220\ns_and_b32 s1, s6, 8\ns_add_u32 s0, s0, s1\nMaaa 120,248,220\ns_lshl_b32 s0, s0, 10\n"
"Maaa 60,244,220\ns_add_u32 s0, s0, s3\nSv 36,1,16,0,0\nMaaa 56,240,220\ns_add_u32 s6, s6, s5\nMvaa 188,236,220\nMvaa 184,232,220\nD0 5,20,46,3072\nMvaa 124,228,220\n"
"Mvaa 120,224,220\ns_waitcnt vmcnt(14) lgkmcnt(0)\ns_barrier\nMvaa 64,160,128\nTa 192,12,49152\nMvaa 68,164,128\nMvaa 128,168,128\nTa 194,12,51200\nMvaa 132,172,128\n"
"s_add_u32 m0, s47, 16384\ns_add_u32 s44, s44, 0x40000\nMaaa 0,176,128\nTa 196,13,49152\ns_lshr_b32 s0, s6, 4\nMaaa 4,180,128\nD0 6,24,44,0\nMaaa 64,184,128\nTa 198,13,51200\n"
"s_lshl_b32 s0, s0, 2\nMaaa 68,188,128\ns_lshr_b32 s0, s7, s0\nMaaa 76,188,132\nTa 200,14,49152\ns_and_b32 s0, s0, 7\nMaaa 72,184,132\ns_lshl_b32 s0, s0, 5\nMaaa 12,180,132\n"
"Ta 202,14,51200\ns_and_b32 s1, s6, 15\nMaaa 8,176,132\ns_add_u32 s0, s0, s1\nMvaa 140,172,132\nTa 204,15,49152\ns_and_b32 s1, s6, 8\nMvaa 136,168,132\nD0 7,24,44,1024\n"
"Mvaa 76,164,132\nTa 206,15,51200\ns_add_u32 s0, s0, s1\nMvaa 72,160,132\ns_lshl_b32 s0, s0, 10\nMvaa 80,160,136\nTa 208,16,49152\ns_add_u32 s0, s0, s3\nMvaa 84,164,136\n"
"Sv 40,1,16,0,0\nMvaa 144,168,136\nTa 210,16,51200\ns_add_u32 s6, s6, s5\nMvaa 148,172,136\nMaaa 16,176,136\nTa 212,17,49152\nMaaa 20,180,136\nD0 8,24,44,2048\nMaaa 80,184,136\n"
"Ta 214,17,51200\nMaaa 84,188,136\nMaaa 92,188,140\nTa 216,18,49152\nMaaa 88,184,140\nMaaa 28,180,140\nTa 218,18,51200\ns_cmp_eq_u32 s5, 8\ns_cselect_b32 s6, s77, s6\n"
"s_cselect_b32 s5, 16, s5\nMaaa 24,176,140\ns_lshr_b32 s0, s6, 4\nMvaa 156,172,140\nTa 220,19,49152\ns_lshl_b32 s0, s0, 2\nMvaa 152,168,140\nD0 9,24,44,3072\nMvaa 92,164,140\n"
"Ta 222,19,51200\ns_lshr_b32 s0, s7, s0\nMvaa 88,160,140\ns_and_b32 s0, s0, 7\nMvaa 96,160,144\nRa 224,11,32768\ns_lshl_b32 s0, s0, 5\nMvaa 100,164,144\ns_and_b32 s1, s6, 15\n"
"Mvaa 160,168,144\nRa 228,11,34816\ns_add_u32 s0, s0, s1\nMvaa 164,172,144\ns_add_u32 m0, s47, 32768\ns_add_u32 s44, s44, 0x40000\nMaaa 32,176,144\nRa 232,11,40960\n"
"s_and_b32 s1, s6, 8\nMaaa 36,180,144\nD0 6,24,44,0\nMaaa 96,184,144\nRa 236,11,43008\ns_add_u32 s0, s0, s1\nMaaa 100,188,144\ns_lshl_b32 s0, s0, 10\nMaaa 108,188,148\n"
"Ra 240,11,49152\ns_add_u32 s0, s0, s3\nMaaa 104,184,148\nSv 44,1,16,0,0\nMaaa 44,180,148\nRa 244,11,51200\ns_add_u32 s6, s6, s5\nMaaa 40,176,148\nMvaa 172,172,148\n"
"Ra 248,11,57344\nMvaa 168,168,148\nD0 7,24,44,1024\nMvaa 108,164,148\nRa 252,11,59392\nMvaa 104,160,148\nMvaa 112,160,152\nMvaa 116,164,152\nMvaa 176,168,152\n"
"s_lshr_b32 s0, s6, 4\ns_lshl_b32 s0, s0, 2\nMvaa 180,172,152\ns_lshr_b32 s0, s7, s0\nMaaa 48,176,152\ns_and_b32 s0, s0, 7\ns_lshl_b32 s0, s0, 5\nMaaa 52,180,152\nD0 8,24,44,2048\n"
"Maaa 112,184,152\ns_and_b32 s1, s6, 15\ns_add_u32 s0, s0, s1\nMaaa 116,188,152\ns_and_b32 s1, s6, 8\nMaaa 124,188,156\ns_add_u32 s0, s0, s1\ns_lshl_b32 s0, s0, 10\n"
"Maaa 120,184,156\ns_add_u32 s0, s0, s3\nMaaa 60,180,156\nSv 48,1,16,0,0\ns_add_u32 s6, s6, s5\nMaaa 56,176,156\nMvaa 188,172,156\nMvaa 184,168,156\nD0 9,24,44,3072\n"
"Mvaa 124,164,156\nMvaa 120,160,156\ns_waitcnt vmcnt(11) lgkmcnt(0)\ns_barrier\nMvaa 64,224,192\nTa 128,12,0\nMvaa 68,228,192\nMvaa 128,232,192\nTa 130,12,2048\n"
"s_lshr_b32 s0, s6, 4\nMvaa 132,236,192\ns_add_u32 m0, s48, 32768\ns_add_u32 s45, s45, 128\nMaaa 0,240,192\nTa 132,13,0\ns_lshl_b32 s0, s0, 2\nMaaa 4,244,192\nD0 2,20,45,0\n"
"Maaa 64,248,192\nTa 134,13,2048\ns_lshr_b32 s0, s7, s0\nMaaa 68,252,192\ns_and_b32 s0, s0, 7\nMaaa 76,252,196\nTa 136,14,0\ns_lshl_b32 s0, s0, 5\nMaaa 72,248,196\n"
"s_and_b32 s1, s6, 15\nMaaa 12,244,196\nTa 138,14,2048\ns_add_u32 s0, s0, s1\nMaaa 8,240,196\ns_and_b32 s1, s6, 8\nMvaa 140,236,196\nTa 140,15,0\ns_add_u32 s0, s0, s1\n"
"Mvaa 136,232,196\nD0 3,20,45,1024\nMvaa 76,228,196\nTa 142,15,2048\ns_lshl_b32 s0, s0, 10\nMvaa 72,224,196\ns_add_u32 s0, s0, s3\nMvaa 80,224,200\nTa 144,16,0\nSv 52,1,16,0,0\n"
"Mvaa 84,228,200\ns_add_u32 s6, s6, s5\nMvaa 144,232,200\nTa 146,16,2048\nMvaa 148,236,200\nMaaa 16,240,200\nTa 148,17,0\nMaaa 20,244,200\nD0 4,20,45,2048\nMaaa 80,248,200\n"
"Ta 150,17,2048\nMaaa 84,252,200\nMaaa 92,252,204\nTa 152,18,0\nMaaa 88,248,204\ns_lshr_b32 s0, s6, 4\nMaaa 28,244,204\nTa 154,18,2048\ns_lshl_b32 s0, s0, 2\nMaaa 24,240,204\n"
"s_lshr_b32 s0, s7, s0\nMvaa 156,236,204\nTa 156,19,0\ns_and_b32 s0, s0, 7\nMvaa 152,232,204\nD0 5,20,45,3072\nMvaa 92,228,204\nTa 158,19,2048\ns_lshl_b32 s0, s0, 5\n"
"Mvaa 88,224,204\ns_and_b32 s1, s6, 15\nMvaa 96,224,208\nRa 160,10,0\ns_add_u32 s0, s0, s1\nMvaa 100,228,208\ns_and_b32 s1, s6, 8\nMvaa 160,232,208\nRa 164,10,2048\n"
"s_add_u32 s0, s0, s1\nMvaa 164,236,208\ns_add_u32 m0, s48, 36864\ns_add_u32 s46, s45, 0x10000\nMaaa 32,240,208\nRa 168,10,8192\ns_lshl_b32 s0, s0, 10\nMaaa 36,244,208\n"
"D0 2,20,46,0\nMaaa 96,248,208\nRa 172,10,10240\ns_add_u32 s0, s0, s3\nMaaa 100,252,208\nSv 56,1,16,0,0\nMaaa 108,252,212\nRa 176,10,16384\ns_add_u32 s6, s6, s5\nMaaa 104,248,212\n"
"Maaa 44,244,212\nRa 180,10,18432\nMaaa 40,240,212\nMvaa 172,236,212\nRa 184,10,24576\nMvaa 168,232,212\nD0 3,20,46,1024\nMvaa 108,228,212\nRa 188,10,26624\nMvaa 104,224,212\n"
"Mvaa 112,224,216\ns_lshr_b32 s0, s6, 4\ns_lshl_b32 s0, s0, 2\nMvaa 116,228,216\ns_lshr_b32 s0, s7, s0\nMvaa 176,232,216\ns_and_b32 s0, s0, 7\ns_lshl_b32 s0, s0, 5\n"
"Mvaa 180,236,216\ns_and_b32 s1, s6, 15\nMaaa 48,240,216\ns_add_u32 s0, s0, s1\ns_and_b32 s1, s6, 8\nMaaa 52,244,216\nD0 4,20,46,2048\nMaaa 112,248,216\ns_add_u32 s0, s0, s1\n"
"s_lshl_b32 s0, s0, 10\nMaaa 116,252,216\ns_add_u32 s0, s0, s3\nMaaa 124,252,220\nSv 60,1,16,0,0\ns_add_u32 s6, s6, s5\nMaaa 120,248,220\nMaaa 60,244,220\nMaaa 56,240,220\n"
"Mvaa 188,236,220\nMvaa 184,232,220\nD0 5,20,46,3072\nMvaa 124,228,220\nMvaa 120,224,220\ns_waitcnt vmcnt(17) lgkmcnt(0)\ns_barrier\nMvaa 64,160,128\nTa 192,12,16384\n"
"v_lshrrev_b32 v48, 4, v0\nMvaa 68,164,128\nv_and_b32 v49, 7, v0\ns_add_u32 m0, s47, 49152\ns_add_u32 s44, s44, 0x40000\nMvaa 128,168,128\nTa 194,12,18432\nMvaa 132,172,128\n"
"v_xor_b32 v49, v49, v48\nD0 6,24,44,0\nMaaa 0,176,128\nTa 196,13,16384\nv_lshlrev_b32 v49, 4, v49\nMaaa 4,180,128\nv_lshrrev_b32 v48, 3, v0\nMaaa 64,184,128\nTa 198,13,18432\n"
"Maaa 68,188,128\nv_mul_u32_u24 v48, 896, v48\nMaaa 76,188,132\nTa 200,14,16384\nv_add_u32 v40, v48, v49\nD0 7,24,44,1024\nMaaa 72,184,132\nv_xor_b32 v49, 64, v40\nMaaa 12,180,132\n"
"Ta 202,14,18432\nMaaa 8,176,132\nv_add_u32 v41, 6144, v49\nMvaa 140,172,132\nTa 204,15,16384\nv_add_u32 v42, 55296, v40\nMvaa 136,168,132\nv_add_u32 v43, 61440, v49\n"
"D0 8,24,44,2048\nMvaa 76,164,132\nTa 206,15,18432\nMvaa 72,160,132\ns_mov_b32 s2, s13\nMvaa 80,160,136\nTa 208,16,16384\ns_lshl3_add_u32 s2, s2, s11\nMvaa 84,164,136\n"
"s_mul_i32 s52, s2, 14336\nMvaa 144,168,136\nTa 210,16,18432\nD0 9,24,44,3072\nMvaa 148,172,136\ns_lshr_b32 s2, s13, 2\nMaaa 16,176,136\nTa 212,17,16384\n"
"s_bfe_u32 s3, s13, 0x10001\nMaaa 20,180,136\ns_mul_i32 s3, s3, 17\ns_add_u32 m0, s47, 65536\ns_add_u32 s46, s51, 0\nMaaa 80,184,136\nTa 214,17,18432\nMaaa 84,188,136\n"
"s_add_u32 s2, s2, s3\nD0 6,32,46,0\nMaaa 92,188,140\nTa 216,18,16384\ns_lshl2_add_u32 s3, s14, 0\nMaaa 88,184,140\ns_lshl_b32 s3, s3, 2\nMaaa 28,180,140\nTa 218,18,18432\n"
"Maaa 24,176,140\ns_lshr_b32 s3, 0xe420ca86, s3\nMvaa 156,172,140\nTa 220,19,16384\ns_and_b32 s3, s3, 15\nD0 7,32,46,1024\nMvaa 152,168,140\ns_add_u32 s2, s2, s3\n"
"s_cmp_eq_u32 s2, 32\ns_cselect_b32 s4, 16, s2\nMvaa 92,164,140\nTa 222,19,18432\nMvaa 88,160,140\ns_mul_i32 s75, s4, 65536\nMvaa 96,160,144\nRa 224,11,0\ns_lshr_b32 s2, s13, 2\n"
"Mvaa 100,164,144\ns_bfe_u32 s3, s13, 0x10001\nD0 8,32,46,2048\nMvaa 160,168,144\nRa 228,11,2048\nMvaa 164,172,144\ns_mul_i32 s3, s3, 17\nMaaa 32,176,144\nRa 232,11,8192\n"
"s_add_u32 s2, s2, s3\nMaaa 36,180,144\ns_lshl2_add_u32 s3, s14, 1\nMaaa 96,184,144\nRa 236,11,10240\nD0 9,32,46,3072\nMaaa 100,188,144\ns_lshl_b32 s3, s3, 2\nMaaa 108,188,148\n"
"Ra 240,11,16384\ns_lshr_b32 s3, 0xe420ca86, s3\nMaaa 104,184,148\ns_and_b32 s3, s3, 15\ns_add_u32 m0, s47, 81920\ns_add_u32 s46, s51, 262144\nMaaa 44,180,148\nRa 244,11,18432\n"
"Maaa 40,176,148\ns_add_u32 s2, s2, s3\ns_cmp_eq_u32 s2, 32\ns_cselect_b32 s4, 16, s2\nD0 6,32,46,0\nMvaa 172,172,148\nRa 248,11,24576\ns_mul_i32 s76, s4, 65536\nMvaa 168,168,148\n"
"s_lshr_b32 s2, s13, 2\nMvaa 108,164,148\nRa 252,11,26624\nMvaa 104,160,148\ns_bfe_u32 s3, s13, 0x10001\nMvaa 112,160,152\ns_mul_i32 s3, s3, 17\ns_add_u32 s2, s2, s3\n"
"D0 7,32,46,1024\nMvaa 116,164,152\ns_lshl2_add_u32 s3, s14, 2\nMvaa 176,168,152\ns_lshl_b32 s3, s3, 2\nMvaa 180,172,152\ns_lshr_b32 s3, 0xe420ca86, s3\nMaaa 48,176,152\n"
"s_and_b32 s3, s3, 15\ns_add_u32 s2, s2, s3\nMaaa 52,180,152\ns_cmp_eq_u32 s2, 32\ns_cselect_b32 s4, 16, s2\nD0 8,32,46,2048\nMaaa 112,184,152\ns_mul_i32 s77, s4, 65536\n"
"Maaa 116,188,152\ns_lshr_b32 s2, s13, 2\nMaaa 124,188,156\ns_bfe_u32 s3, s13, 0x10001\ns_mul_i32 s3, s3, 17\nMaaa 120,184,156\ns_add_u32 s2, s2, s3\nMaaa 60,180,156\n"
"s_lshl2_add_u32 s3, s14, 3\nD0 9,32,46,3072\nMaaa 56,176,156\ns_lshl_b32 s3, s3, 2\nMvaa 188,172,156\ns_lshr_b32 s3, 0xe420ca86, s3\ns_and_b32 s3, s3, 15\nMvaa 184,168,156\n"
"s_add_u32 s2, s2, s3\ns_cmp_eq_u32 s2, 32\ns_cselect_b32 s4, 16, s2\nMvaa 124,164,156\ns_mul_i32 s78, s4, 65536\nMvaa 120,160,156\ns_waitcnt vmcnt(12) lgkmcnt(0)\ns_barrier\n"
"Mvaa 64,224,192\nTa 128,12,32768\ns_lshr_b32 s3, s11, 1\nMvaa 68,228,192\ns_lshl_b32 s3, s3, 15\ns_add_u32 m0, s47, -65536\ns_add_u32 s46, s52, 0\nMvaa 128,232,192\n"
"Ta 130,12,34816\nMvaa 132,236,192\ns_add_u32 s2, s72, s3\nD0 40,28,46,0\nMaaa 0,240,192\nTa 132,13,32768\ns_and_b32 s3, s11, 1\nMaaa 4,244,192\ns_lshl_b32 s3, s3, 14\n"
"Maaa 64,248,192\nTa 134,13,34816\nMaaa 68,252,192\ns_add_u32 s2, s2, s3\nMaaa 76,252,196\nTa 136,14,32768\ns_add_u32 s2, s2, 0x400000\nD0 41,28,46,1024\nMaaa 72,248,196\n"
"s_mov_b32 s55, 4096\nMaaa 12,244,196\nTa 138,14,34816\nMaaa 8,240,196\ns_add_u32 s75, s75, s2\nMvaa 140,236,196\nTa 140,15,32768\ns_add_u32 s76, s76, s2\nMvaa 136,232,196\n"
"s_add_u32 s77, s77, s2\nD0 42,28,46,2048\nMvaa 76,228,196\nTa 142,15,34816\nMvaa 72,224,196\ns_add_u32 s78, s78, s2\nMvaa 80,224,200\nTa 144,16,32768\nv_add_u32 v32, 0x10000, v12\n"
"Mvaa 84,228,200\nv_add_u32 v33, 0x10000, v13\nMvaa 144,232,200\nTa 146,16,34816\nD0 43,28,46,3072\nMvaa 148,236,200\nv_add_u32 v34, 0x10000, v14\nMaaa 16,240,200\nTa 148,17,32768\n"
"v_add_u32 v35, 0x10000, v15\nMaaa 20,244,200\nv_add_u32 v36, 0x10000, v16\ns_add_u32 m0, s47, 0\ns_add_u32 s46, s51, 524288\nMaaa 80,248,200\nTa 150,17,34816\nMaaa 84,252,200\n"
"v_add_u32 v37, 0x10000, v17\nD0 6,32,46,0\nMaaa 92,252,204\nTa 152,18,32768\nv_add_u32 v38, 0x10000, v18\nMaaa 88,248,204\nv_add_u32 v39, 0x10000, v19\nMaaa 28,244,204\n"
"Ta 154,18,34816\nMaaa 24,240,204\ns_add_u32 s2, s11, 1\nMvaa 156,236,204\nTa 156,19,32768\ns_lshr_b32 s2, s2, 1\nD0 7,32,46,1024\nMvaa 152,232,204\ns_lshl_b32 s2, s2, 12\n"
"Mvaa 92,228,204\nTa 158,19,34816\nMvaa 88,224,204\nv_add_u32 v44, s2, v10\nMvaa 96,224,208\nRa 160,10,32768\nv_add_u32 v45, s2, v11\nMvaa 100,228,208\nv_xor_b32 v46, 0x1000, v44\n"
"D0 8,32,46,2048\nMvaa 160,232,208\nRa 164,10,34816\nMvaa 164,236,208\nv_xor_b32 v47, 0x1000, v45\nMaaa 32,240,208\nRa 168,10,40960\ns_lshr_b32 s3, s11, 1\nMaaa 36,244,208\n"
"s_lshl_b32 s3, s3, 13\nMaaa 96,248,208\nRa 172,10,43008\nD0 9,32,46,3072\nMaaa 100,252,208\ns_lshl_b32 s2, s11, 12\nMaaa 108,252,212\nRa 176,10,49152\ns_add_u32 s2, s2, s3\n"
"Maaa 104,248,212\ns_add_u32 s2, s2, s72\ns_add_u32 m0, s47, 16384\ns_add_u32 s46, s51, 786432\nMaaa 44,244,212\nRa 180,10,51200\nMaaa 40,240,212\ns_lshl_b32 s3, s11, 12\n"
"D0 6,32,46,0\nMvaa 172,236,212\nRa 184,10,57344\ns_add_u32 s89, s3, 0x10000\nMvaa 168,232,212\ns_mov_b32 s88, 0xffff8000\nMvaa 108,228,212\nRa 188,10,59392\nMvaa 104,224,212\n"
"s_lshl_b32 s4, s13, 1\nMvaa 112,224,216\ns_sub_u32 s79, 15, s4\ns_and_b32 s5, s12, 1\nD0 7,32,46,1024\nMvaa 116,228,216\ns_mul_i32 s5, s5, 229376\nMvaa 176,232,216\n"
"s_add_u32 s5, s5, s2\nMvaa 180,236,216\ns_add_u32 s96, s5, 0x1310000\nMaaa 48,240,216\ns_mov_b32 s63, 0x53800000\ns_mov_b32 s64, 0x2b800000\nMaaa 52,244,216\ns_sub_u32 s98, s4, 1\n"
"D0 8,32,46,2048\nMaaa 112,248,216\ns_lshl_b32 s5, s13, 5\nMaaa 116,252,216\ns_add_u32 s5, s5, s11\nMaaa 124,252,220\ns_lshl_b32 s65, s5, 9\ns_add_u32 s65, s65, s72\n"
"Maaa 120,248,220\ns_add_u32 s65, s65, 0x1580000\nMaaa 60,244,220\ns_and_b32 s66, s72, 0x2000\nD0 9,32,46,3072\nMaaa 56,240,220\ns_add_u32 s66, s66, s56\nMvaa 188,236,220\n"
"s_add_u32 s66, s66, 0x100000\ns_and_b32 s5, s12, 1\nMvaa 184,232,220\ns_mul_i32 s58, s5, 0x202000\nMvaa 124,228,220\ns_xor_b32 s58, s58, s72\nMvaa 120,224,220\n"
"s_waitcnt vmcnt(20) lgkmcnt(0)\ns_barrier\nMvaa 64,160,128\nTa 192,12,49152\ns_add_u32 s58, s58, 0x15c0000\nMvaa 68,164,128\ns_lshl_b32 s5, s5, 8\ns_add_u32 m0, s47, -49152\n"
"s_add_u32 s46, s52, 256\nMvaa 128,168,128\nTa 194,12,51200\nMvaa 132,172,128\ns_add_u32 s61, s58, s5\nD0 40,28,46,0\nMaaa 0,176,128\nTa 196,13,49152\n"
"s_lshl4_add_u32 s60, s13, s61\nMaaa 4,180,128\ns_add_u32 s54, s60, 128\nMaaa 64,184,128\nTa 198,13,51200\nMaaa 68,188,128\ns_xor_b32 s60, s54, 0x100\ns_cmp_eq_u32 s11, 0\n"
"s_cselect_b32 s54, s54, 0x40000000\nMaaa 76,188,132\nTa 200,14,49152\ns_getpc_b64 s[8:9]\n.PC386:\ns_add_u32 s8, s8, .HD379-.PC386\ns_addc_u32 s9, s9, 0\nD0 41,28,46,1024\n"
"Maaa 72,184,132\ns_mov_b32 s97, 0xff800000\nMaaa 12,180,132\nTa 202,14,51200\nMaaa 8,176,132\ns_mov_b32 s90, 1\ns_cmp_lt_u32 s11, 2\ns_cselect_b32 s92, 2, 0\nMvaa 140,172,132\n"
"Ta 204,15,49152\ns_mul_i32 s4, s79, 65536\nMvaa 136,168,132\ns_sub_u32 s4, s2, s4\nD0 42,28,46,2048\nMvaa 76,164,132\nTa 206,15,51200\nMvaa 72,160,132\ns_add_u32 s94, s4, 0x20000\n"
"Mvaa 80,160,136\nTa 208,16,49152\ns_add_u32 s95, s4, 0x410000\nMvaa 84,164,136\ns_add_u32 s86, s2, 0x130000\nMvaa 144,168,136\nTa 210,16,51200\nD0 43,28,46,3072\nMvaa 148,172,136\n"
"s_add_u32 s87, s2, 0x520000\nMaaa 16,176,136\nTa 212,17,49152\ns_sub_u32 s99, s79, 2\nMaaa 20,180,136\ns_cmp_eq_u32 s13, 7\ns_cselect_b32 s93, 1.0, 0\ns_add_u32 m0, s47, 32768\n"
"s_add_u32 s46, s51, 1048576\nMaaa 80,184,136\nTa 214,17,51200\nMaaa 84,188,136\ns_mov_b32 s36, s68\nD0 6,32,46,0\nMaaa 92,188,140\nTa 216,18,49152\ns_and_b32 s37, s69, 0xffff\n"
"Maaa 88,184,140\ns_mov_b32 s38, 0x1c0000\nMaaa 28,180,140\nTa 218,18,51200\nMaaa 24,176,140\ns_mov_b32 s39, 0x20000\nMvaa 156,172,140\nTa 220,19,49152\ns_and_b32 s2, s74, 15\n"
"D0 7,32,46,1024\nMvaa 152,168,140\ns_max_u32 s2, s2, 2\nMvaa 92,164,140\nTa 222,19,51200\nMvaa 88,160,140\ns_sub_u32 s2, s2, 2\nMvaa 96,160,144\nRa 224,11,32768\n"
"s_mul_i32 s3, s73, 14\nMvaa 100,164,144\ns_add_u32 s2, s2, s3\nD0 8,32,46,2048\nMvaa 160,168,144\nRa 228,11,34816\nMvaa 164,172,144\ns_lshl2_add_u32 s2, s2, s11\nMaaa 32,176,144\n"
"Ra 232,11,40960\ns_mul_i32 s3, s2, 4682\nMaaa 36,180,144\ns_lshr_b32 s3, s3, 16\nMaaa 96,184,144\nRa 236,11,43008\nD0 9,32,46,3072\nMaaa 100,188,144\ns_mul_i32 s22, s3, 14\n"
"Maaa 108,188,148\nRa 240,11,49152\ns_sub_u32 s22, s2, s22\nMaaa 104,184,148\ns_and_b32 s21, s3, 28\nMaaa 44,180,148\nRa 244,11,51200\nMaaa 40,176,148\ns_add_u32 s21, s21, s3\n"
"Mvaa 172,172,148\nRa 248,11,57344\ns_lshl_b32 s21, s21, 4\nMvaa 168,168,148\ns_bfe_u32 s41, s3, 0x30002\ns_cmp_gt_u32 s22, 6\ns_cselect_b32 s2, 1187840, 0\n"
"s_cselect_b32 s1, 16, 0\nMvaa 108,164,148\nRa 252,11,59392\nMvaa 104,160,148\ns_add_u32 s41, s41, s1\nMvaa 112,160,152\ns_lshl4_add_u32 s41, s41, 0x15c0280\ns_and_b32 s1, s3, 28\n"
"Mvaa 116,164,152\ns_add_u32 s3, s3, s1\nMvaa 176,168,152\ns_lshl_b32 s3, s3, 11\nMvaa 180,172,152\ns_add_u32 s2, s2, s3\nMaaa 48,176,152\ns_lshl_b32 s3, s22, 17\n"
"s_add_u32 s2, s2, s3\nMaaa 52,180,152\ns_add_u32 s20, s2, 0x1100000\nMaaa 112,184,152\ns_lshl_b32 s2, s22, 7\nMaaa 116,188,152\ns_add_u32 s40, s2, 0xc04b800\nMaaa 124,188,156\n"
"s_mul_i32 s2, s21, 896\ns_lshl_b32 s3, s22, 6\nMaaa 120,184,156\ns_add_u32 s22, s2, s3\nMaaa 60,180,156\ns_add_u32 s23, s22, 57344\nMaaa 56,176,156\nMvaa 188,172,156\n"
"Mvaa 184,168,156\nMvaa 124,164,156\nMvaa 120,160,156\ns_waitcnt vmcnt(16) lgkmcnt(0)\ns_barrier\nMvaa 64,224,192\ns_add_u32 m0, s47, -16384\ns_add_u32 s46, s52, 128\n"
"Mvaa 68,228,192\nMvaa 72,224,196\nD0 40,28,46,0\nMvaa 76,228,196\nMvaa 80,224,200\nMvaa 84,228,200\nMvaa 88,224,204\nD0 41,28,46,1024\nCV 192,64,65\nMvaa 92,228,204\nCV 193,66,67\n"
"CV 194,68,69\nMvaa 96,224,208\nCV 195,70,71\nCV 196,72,73\nMvaa 100,228,208\nCV 197,74,75\nMvaa 104,224,212\nD0 42,28,46,2048\nCV 198,76,77\nMvaa 108,228,212\nCV 199,78,79\n"
"CV 200,80,81\nMvaa 112,224,216\nCV 201,82,83\nCV 202,84,85\nMvaa 116,228,216\nCV 203,86,87\nMvaa 120,224,220\nD0 43,28,46,3072\nCV 204,88,89\nMvaa 124,228,220\nCV 205,90,91\n"
"CV 206,92,93\nMvaa 128,232,192\ns_add_u32 m0, s47, 49152\ns_add_u32 s46, s51, 1310720\nMvaa 132,236,192\nCV 207,94,95\nMvaa 136,232,196\nD0 6,32,46,0\nCV 208,96,97\n"
"Mvaa 140,236,196\nCV 209,98,99\nCV 210,100,101\nMvaa 144,232,200\nCV 211,102,103\nCV 212,104,105\nMvaa 148,236,200\nCV 213,106,107\nMvaa 152,232,204\nD0 7,32,46,1024\n"
"CV 214,108,109\nMvaa 156,236,204\nCV 215,110,111\nCV 216,112,113\nMvaa 160,232,208\nCV 217,114,115\nCV 218,116,117\nMvaa 164,236,208\nCV 219,118,119\nMvaa 168,232,212\n"
"D0 8,32,46,2048\nCV 220,120,121\nMvaa 172,236,212\nCV 221,122,123\nCV 222,124,125\nMvaa 176,232,216\nCV 223,126,127\nCV 224,128,129\nMvaa 180,236,216\nCV 225,130,131\n"
"Mvaa 184,232,220\nD0 9,32,46,3072\nCV 226,132,133\nMvaa 188,236,220\nCV 227,134,135\nCV 228,136,137\nMaaa 0,240,192\ns_add_u32 m0, s47, -32768\ns_add_u32 s46, s52, 384\n"
"Maaa 4,244,192\nCV 229,138,139\nMaaa 8,240,196\nD0 40,28,46,0\nCV 230,140,141\nMaaa 12,244,196\nCV 231,142,143\nCV 232,144,145\nMaaa 16,240,200\nRa 160,44,0\nCV 233,146,147\n"
"Maaa 20,244,200\nRa 164,44,2048\nMaaa 24,240,204\nD0 41,28,46,1024\nRa 168,46,0\nMaaa 28,244,204\nRa 172,46,2048\nCV 234,148,149\nMaaa 32,240,208\nTa 128,32,0\nCV 235,150,151\n"
"Maaa 36,244,208\nTa 130,32,2048\nMaaa 40,240,212\nD0 42,28,46,2048\nTa 132,33,0\nMaaa 44,244,212\nTa 134,33,2048\nCV 236,152,153\nMaaa 48,240,216\nTa 136,34,0\nCV 237,154,155\n"
"Maaa 52,244,216\nTa 138,34,2048\nMaaa 56,240,220\nD0 43,28,46,3072\nTa 140,35,0\nMaaa 60,244,220\nTa 142,35,2048\nCV 238,156,157\nMaaa 64,248,192\nTa 144,36,0\nCV 239,158,159\n"
"Maaa 68,252,192\nTa 146,36,2048\nMaaa 72,248,196\nTa 148,37,0\nCV 240,160,161\nSv 192,1,16,75,0\nMaaa 76,252,196\nTa 150,37,2048\nCV 241,162,163\nMaaa 80,248,200\nTa 152,38,0\n"
"CV 242,164,165\nMaaa 84,252,200\nTa 154,38,2048\nMaaa 88,248,204\nTa 156,39,0\nCV 243,166,167\nMaaa 92,252,204\nTa 158,39,2048\nCV 244,168,169\nMaaa 96,248,208\nCV 245,170,171\n"
"CV 246,172,173\nSv 196,1,16,75,1024\nMaaa 100,252,208\nCV 247,174,175\nMaaa 104,248,212\nCV 248,176,177\nCV 249,178,179\nMaaa 108,252,212\nCV 250,180,181\nCV 251,182,183\n"
"Maaa 112,248,216\nCV 252,184,185\nCV 253,186,187\nMaaa 116,252,216\nCV 254,188,189\nMaaa 120,248,220\nCV 255,190,191\nSv 200,1,16,75,2048\nMaaa 124,252,220\nWL 0\nZvaa 64,128,160\n"
"Ra 224,45,0\nZvaa 96,128,164\nRa 228,45,2048\nZvaa 128,128,168\nRa 232,47,0\nZvaa 160,128,172\nRa 236,47,2048\nSv 204,1,16,75,3072\ns_lshl3_add_u32 s5, s12, s13\n"
"s_lshl2_add_u32 s5, s5, s11\ns_lshl2_add_u32 s5, s5, 0xc04a400\nbuffer_load_dword v27, off, s[16:19], s5 sc0 sc1\nZvaa 164,132,172\nTa 192,32,16384\nZvaa 132,132,168\n"
"Zvaa 100,132,164\nTa 194,32,18432\nZvaa 68,132,160\nTa 196,33,16384\nZvaa 72,136,160\nTa 198,33,18432\nZvaa 104,136,164\nTa 200,34,16384\ns_add_u32 s75, s75, s55\n"
"Sv 208,1,16,75,0\nZvaa 136,136,168\nTa 202,34,18432\nZvaa 168,136,172\nZvaa 172,140,172\nTa 204,35,16384\nZvaa 140,140,168\nTa 206,35,18432\nZvaa 108,140,164\nTa 208,36,16384\n"
"Zvaa 76,140,160\nTa 210,36,18432\nSv 212,1,16,75,1024\nZvaa 80,144,160\nTa 212,37,16384\nZvaa 112,144,164\nZvaa 144,144,168\nTa 214,37,18432\nZvaa 176,144,172\nTa 216,38,16384\n"
"Zvaa 180,148,172\nTa 218,38,18432\nZvaa 148,148,168\nTa 220,39,16384\nSv 216,1,16,75,2048\nZvaa 116,148,164\nTa 222,39,18432\nZvaa 84,148,160\nZvaa 88,152,160\nZvaa 120,152,164\n"
"Zvaa 152,152,168\nZvaa 184,152,172\nSv 220,1,16,75,3072\nZvaa 188,156,172\nZvaa 156,156,168\nZvaa 124,156,164\nZvaa 92,156,160\ns_waitcnt vmcnt(17) lgkmcnt(0)\ns_barrier\n"
"Mvaa 64,192,224\nRa 160,44,49152\nMvaa 96,192,228\nRa 164,44,51200\ns_add_u32 m0, s47, 65536\ns_add_u32 s46, s51, 1572864\nD0 6,32,46,0\nMvaa 128,192,232\nRa 168,46,49152\n"
"Mvaa 160,192,236\nRa 172,46,51200\nMvaa 164,196,236\nTa 128,12,0\nMvaa 132,196,232\nD0 7,32,46,1024\nMvaa 100,196,228\nTa 130,12,2048\nMvaa 68,196,224\nTa 132,13,0\n"
"Mvaa 72,200,224\nTa 134,13,2048\nMvaa 104,200,228\nTa 136,14,0\nD0 8,32,46,2048\nMvaa 136,200,232\nTa 138,14,2048\nMvaa 168,200,236\nMvaa 172,204,236\nTa 140,15,0\n"
"Mvaa 140,204,232\nTa 142,15,2048\nD0 9,32,46,3072\nMvaa 108,204,228\nTa 144,16,0\nMvaa 76,204,224\nTa 146,16,2048\ns_add_u32 m0, s47, 81920\ns_add_u32 s46, s51, 1835008\n"
"Mvaa 80,208,224\nTa 148,17,0\nMvaa 112,208,228\nD0 6,32,46,0\nMvaa 144,208,232\nTa 150,17,2048\nMvaa 176,208,236\nTa 152,18,0\nMvaa 180,212,236\nTa 154,18,2048\nMvaa 148,212,232\n"
"Ta 156,19,0\nD0 7,32,46,1024\nMvaa 116,212,228\nTa 158,19,2048\nMvaa 84,212,224\nMvaa 88,216,224\nMvaa 120,216,228\nD0 8,32,46,2048\nMvaa 152,216,232\nMvaa 184,216,236\n"
"Mvaa 188,220,236\nMvaa 156,220,232\nD0 9,32,46,3072\nMvaa 124,220,228\nMvaa 92,220,224\nWL 0\nMvaa 64,128,160\nRa 224,45,49152\nMvaa 96,128,164\nRa 228,45,51200\n"
"s_add_u32 m0, s47, -65536\ns_add_u32 s46, s52, 512\nD0 40,28,46,0\nMvaa 128,128,168\nRa 232,47,49152\nMvaa 160,128,172\nRa 236,47,51200\nMvaa 164,132,172\nTa 192,12,16384\n"
"Mvaa 132,132,168\nD0 41,28,46,1024\nMvaa 100,132,164\nTa 194,12,18432\nMvaa 68,132,160\nTa 196,13,16384\nMvaa 72,136,160\nTa 198,13,18432\nMvaa 104,136,164\nTa 200,14,16384\n"
"D0 42,28,46,2048\nMvaa 136,136,168\nTa 202,14,18432\nMvaa 168,136,172\nMvaa 172,140,172\nTa 204,15,16384\nMvaa 140,140,168\nTa 206,15,18432\nD0 43,28,46,3072\nMvaa 108,140,164\n"
"Ta 208,16,16384\nSv 224,1,16,76,0\nMvaa 76,140,160\nTa 210,16,18432\nMvaa 80,144,160\nTa 212,17,16384\nMvaa 112,144,164\nMvaa 144,144,168\nTa 214,17,18432\nMvaa 176,144,172\n"
"Ta 216,18,16384\nSv 228,1,16,76,1024\nMvaa 180,148,172\nTa 218,18,18432\nMvaa 148,148,168\nTa 220,19,16384\nMvaa 116,148,164\nTa 222,19,18432\nMvaa 84,148,160\nMvaa 88,152,160\n"
"Sv 232,1,16,76,2048\nMvaa 120,152,164\nMvaa 152,152,168\nMvaa 184,152,172\nMvaa 188,156,172\nMvaa 156,156,168\nSv 236,1,16,76,3072\nMvaa 124,156,164\nMvaa 92,156,160\n"
"s_waitcnt vmcnt(16) lgkmcnt(0)\ns_barrier\nMvaa 64,192,224\nRa 160,44,16384\nv_cmp_ne_u32 vcc, s15, v27\ns_nop 0\ns_cbranch_vccnz .S398\n.S399:\nMvaa 96,192,228\nRa 164,44,18432\n"
"buffer_store_dword v25, off, s[16:19], s54 offset:0 sc0 sc1\ns_add_u32 m0, s47, 0\ns_add_u32 s46, s51, 2097152\nD0 6,32,46,0\nMvaa 128,192,232\nRa 168,46,16384\nMvaa 160,192,236\n"
"Ra 172,46,18432\nMvaa 164,196,236\nTa 128,12,32768\nMvaa 132,196,232\nD0 7,32,46,1024\nMvaa 100,196,228\nTa 130,12,34816\nMvaa 68,196,224\nTa 132,13,32768\nMvaa 72,200,224\n"
"Ta 134,13,34816\nMvaa 104,200,228\nTa 136,14,32768\nD0 8,32,46,2048\nMvaa 136,200,232\nTa 138,14,34816\nMvaa 168,200,236\nMvaa 172,204,236\nTa 140,15,32768\nMvaa 140,204,232\n"
"Ta 142,15,34816\nD0 9,32,46,3072\nMvaa 108,204,228\nTa 144,16,32768\nMvaa 76,204,224\nTa 146,16,34816\ns_add_u32 m0, s47, 16384\ns_add_u32 s46, s51, 2359296\nMvaa 80,208,224\n"
"Ta 148,17,32768\nMvaa 112,208,228\nD0 6,32,46,0\nMvaa 144,208,232\nTa 150,17,34816\nMvaa 176,208,236\nTa 152,18,32768\nMvaa 180,212,236\nTa 154,18,34816\nMvaa 148,212,232\n"
"Ta 156,19,32768\nD0 7,32,46,1024\nMvaa 116,212,228\nTa 158,19,34816\nMvaa 84,212,224\nMvaa 88,216,224\nMvaa 120,216,228\nD0 8,32,46,2048\nMvaa 152,216,232\nMvaa 184,216,236\n"
"Mvaa 188,220,236\nMvaa 156,220,232\nD0 9,32,46,3072\nMvaa 124,220,228\nMvaa 92,220,224\nWL 0\nMvaa 64,128,160\nRa 224,45,16384\nMvaa 96,128,164\nRa 228,45,18432\n"
"s_add_u32 m0, s47, -16384\ns_add_u32 s46, s52, 640\nD0 40,28,46,0\nMvaa 128,128,168\nRa 232,47,16384\nMvaa 160,128,172\nRa 236,47,18432\nMvaa 164,132,172\nTa 192,12,49152\n"
"Mvaa 132,132,168\nD0 41,28,46,1024\nMvaa 100,132,164\nTa 194,12,51200\nMvaa 68,132,160\nTa 196,13,49152\nMvaa 72,136,160\nTa 198,13,51200\nMvaa 104,136,164\nTa 200,14,49152\n"
"D0 42,28,46,2048\nMvaa 136,136,168\nTa 202,14,51200\nMvaa 168,136,172\nMvaa 172,140,172\nTa 204,15,49152\nMvaa 140,140,168\nTa 206,15,51200\nD0 43,28,46,3072\nMvaa 108,140,164\n"
"Ta 208,16,49152\ns_add_u32 s76, s76, s55\nSv 240,1,16,76,0\nMvaa 76,140,160\nTa 210,16,51200\nMvaa 80,144,160\nTa 212,17,49152\nMvaa 112,144,164\nMvaa 144,144,168\n"
"Ta 214,17,51200\nMvaa 176,144,172\nTa 216,18,49152\nSv 244,1,16,76,1024\nMvaa 180,148,172\nTa 218,18,51200\nMvaa 148,148,168\nTa 220,19,49152\nMvaa 116,148,164\nTa 222,19,51200\n"
"Mvaa 84,148,160\nMvaa 88,152,160\nSv 248,1,16,76,2048\nMvaa 120,152,164\nMvaa 152,152,168\nMvaa 184,152,172\nMvaa 188,156,172\nMvaa 156,156,168\nSv 252,1,16,76,3072\n"
"Mvaa 124,156,164\nMvaa 92,156,160\ns_waitcnt vmcnt(25) lgkmcnt(0)\ns_barrier\nMvaa 64,192,224\nRa 160,44,32768\nAR 60,0\nAR 61,1\nMvaa 96,192,228\nRa 164,44,34816\n"
"s_add_u32 m0, s47, 32768\ns_add_u32 s46, s51, 2621440\nD0 6,32,46,0\nMvaa 128,192,232\nRa 168,46,32768\nCV 52,60,61\nMvaa 160,192,236\nRa 172,46,34816\nAR 60,2\nMvaa 164,196,236\n"
"Ta 128,32,0\nAR 61,3\nMvaa 132,196,232\nD0 7,32,46,1024\nCV 53,60,61\nMvaa 100,196,228\nTa 130,32,2048\nAR 60,4\nMvaa 68,196,224\nTa 132,33,0\nAR 61,5\nMvaa 72,200,224\n"
"Ta 134,33,2048\nCV 54,60,61\nMvaa 104,200,228\nTa 136,34,0\nD0 8,32,46,2048\nMvaa 136,200,232\nTa 138,34,2048\nAR 60,6\nMvaa 168,200,236\nAR 61,7\nCV 55,60,61\nMvaa 172,204,236\n"
"Ta 140,35,0\nAR 60,8\nMvaa 140,204,232\nTa 142,35,2048\nD0 9,32,46,3072\nMvaa 108,204,228\nTa 144,36,0\nAR 61,9\nMvaa 76,204,224\nTa 146,36,2048\ns_add_u32 m0, s47, 49152\n"
"s_add_u32 s46, s51, 2883584\nMvaa 80,208,224\nTa 148,37,0\nCV 56,60,61\nAR 60,10\nMvaa 112,208,228\nD0 6,32,46,0\nAR 61,11\nMvaa 144,208,232\nTa 150,37,2048\nCV 57,60,61\n"
"Mvaa 176,208,236\nTa 152,38,0\nAR 60,12\nMvaa 180,212,236\nTa 154,38,2048\nAR 61,13\nMvaa 148,212,232\nTa 156,39,0\nD0 7,32,46,1024\nMvaa 116,212,228\nTa 158,39,2048\nCV 58,60,61\n"
"Mvaa 84,212,224\nAR 60,14\nAR 61,15\nMvaa 88,216,224\nCV 59,60,61\nAR 60,16\nMvaa 120,216,228\nD0 8,32,46,2048\nAR 61,17\nMvaa 152,216,232\nCV 20,60,61\nAR 60,18\n"
"Mvaa 184,216,236\nAR 61,19\nCV 21,60,61\nMvaa 188,220,236\nAR 60,20\nAR 61,21\nMvaa 156,220,232\nD0 9,32,46,3072\nCV 22,60,61\nMvaa 124,220,228\nAR 60,22\nAR 61,23\n"
"Mvaa 92,220,224\nWL 0\nMvaa 64,128,160\nRa 224,45,32768\nCV 23,60,61\nAR 60,24\nMvaa 96,128,164\nRa 228,45,34816\ns_add_u32 m0, s47, -49152\ns_add_u32 s46, s52, 768\n"
"D0 40,28,46,0\nMvaa 128,128,168\nRa 232,47,32768\nAR 61,25\nMvaa 160,128,172\nRa 236,47,34816\nCV 2,60,61\nMvaa 164,132,172\nTa 192,32,16384\nAR 60,26\nMvaa 132,132,168\n"
"D0 41,28,46,1024\nAR 61,27\nMvaa 100,132,164\nTa 194,32,18432\nCV 3,60,61\nMvaa 68,132,160\nTa 196,33,16384\nAR 60,28\nMvaa 72,136,160\nTa 198,33,18432\nAR 61,29\n"
"Mvaa 104,136,164\nTa 200,34,16384\nD0 42,28,46,2048\nMvaa 136,136,168\nTa 202,34,18432\nCV 4,60,61\nMvaa 168,136,172\nAR 60,30\nAR 61,31\nMvaa 172,140,172\nTa 204,35,16384\n"
"CV 5,60,61\nMvaa 140,140,168\nTa 206,35,18432\nD0 43,28,46,3072\nMvaa 108,140,164\nTa 208,36,16384\nSv 52,1,16,77,0\nMvaa 76,140,160\nTa 210,36,18432\nMvaa 80,144,160\n"
"Ta 212,37,16384\nMvaa 112,144,164\nMvaa 144,144,168\nTa 214,37,18432\nMvaa 176,144,172\nTa 216,38,16384\nSv 56,1,16,77,1024\nMvaa 180,148,172\nTa 218,38,18432\nMvaa 148,148,168\n"
"Ta 220,39,16384\nMvaa 116,148,164\nTa 222,39,18432\nMvaa 84,148,160\nMvaa 88,152,160\nSv 20,1,16,77,2048\nMvaa 120,152,164\nMvaa 152,152,168\nMvaa 184,152,172\nMvaa 188,156,172\n"
"Mvaa 156,156,168\nSv 2,1,16,77,3072\nMvaa 124,156,164\nMvaa 92,156,160\ns_waitcnt vmcnt(24) lgkmcnt(0)\ns_barrier\nMvaa 64,192,224\nRa 160,44,0\nv_and_b32 v27, 0x70, v1\n"
"buffer_load_dword v27, v27, s[16:19], s61 offen offset:128 sc0 sc1\nMvaa 96,192,228\nRa 164,44,2048\ns_add_u32 m0, s47, 65536\ns_add_u32 s46, s51, 3145728\nD0 6,32,46,0\n"
"Mvaa 128,192,232\nRa 168,46,0\nAR 60,32\nMvaa 160,192,236\nRa 172,46,2048\nAR 61,33\nMvaa 164,196,236\nTa 128,12,0\nCV 52,60,61\nMvaa 132,196,232\nD0 7,32,46,1024\nAR 60,34\n"
"Mvaa 100,196,228\nTa 130,12,2048\nAR 61,35\nMvaa 68,196,224\nTa 132,13,0\nCV 53,60,61\nMvaa 72,200,224\nTa 134,13,2048\nAR 60,36\nAR 61,37\nMvaa 104,200,228\nTa 136,14,0\n"
"D0 8,32,46,2048\nMvaa 136,200,232\nTa 138,14,2048\nCV 54,60,61\nMvaa 168,200,236\nAR 60,38\nAR 61,39\nMvaa 172,204,236\nTa 140,15,0\nCV 55,60,61\nMvaa 140,204,232\nTa 142,15,2048\n"
"D0 9,32,46,3072\nMvaa 108,204,228\nTa 144,16,0\nAR 60,40\nMvaa 76,204,224\nTa 146,16,2048\ns_add_u32 m0, s47, 81920\ns_add_u32 s46, s51, 3407872\nMvaa 80,208,224\nTa 148,17,0\n"
"AR 61,41\nCV 56,60,61\nMvaa 112,208,228\nD0 6,32,46,0\nAR 60,42\nMvaa 144,208,232\nTa 150,17,2048\nAR 61,43\nMvaa 176,208,236\nTa 152,18,0\nCV 57,60,61\nMvaa 180,212,236\n"
"Ta 154,18,2048\nAR 60,44\nMvaa 148,212,232\nTa 156,19,0\nD0 7,32,46,1024\nMvaa 116,212,228\nTa 158,19,2048\nAR 61,45\nMvaa 84,212,224\nCV 58,60,61\nAR 60,46\nMvaa 88,216,224\n"
"AR 61,47\nCV 59,60,61\nAR 60,48\nMvaa 120,216,228\nD0 8,32,46,2048\nAR 61,49\nMvaa 152,216,232\nCV 20,60,61\nAR 60,50\nMvaa 184,216,236\nAR 61,51\nCV 21,60,61\nMvaa 188,220,236\n"
"AR 60,52\nAR 61,53\nMvaa 156,220,232\nD0 9,32,46,3072\nCV 22,60,61\nMvaa 124,220,228\nAR 60,54\nAR 61,55\nMvaa 92,220,224\nWL 0\nMvaa 64,128,160\nRa 224,45,0\nCV 23,60,61\n"
"AR 60,56\nMvaa 96,128,164\nRa 228,45,2048\ns_add_u32 s6, s56, 0\nbuffer_load_dwordx4 v[192:195], v1, s[16:19], s6 offen offset:0 sc1\nMvaa 128,128,168\nRa 232,47,0\nAR 61,57\n"
"Mvaa 160,128,172\nRa 236,47,2048\nCV 2,60,61\nMvaa 164,132,172\nTa 192,12,16384\nAR 60,58\nMvaa 132,132,168\n"
"buffer_load_dwordx4 v[196:199], v1, s[16:19], s6 offen offset:1024 sc1\nAR 61,59\nMvaa 100,132,164\nTa 194,12,18432\nCV 3,60,61\nMvaa 68,132,160\nTa 196,13,16384\nAR 60,60\n"
"Mvaa 72,136,160\nTa 198,13,18432\nAR 61,61\nCV 4,60,61\nMvaa 104,136,164\nTa 200,14,16384\nbuffer_load_dwordx4 v[200:203], v1, s[16:19], s6 offen offset:2048 sc1\n"
"Mvaa 136,136,168\nTa 202,14,18432\nAR 60,62\nMvaa 168,136,172\nAR 61,63\nCV 5,60,61\nMvaa 172,140,172\nTa 204,15,16384\nMvaa 140,140,168\nTa 206,15,18432\n"
"buffer_load_dwordx4 v[204:207], v1, s[16:19], s6 offen offset:3072 sc1\nMvaa 108,140,164\nTa 208,16,16384\ns_add_u32 s77, s77, s55\nSv 52,1,16,77,0\nMvaa 76,140,160\n"
"Ta 210,16,18432\nMvaa 80,144,160\nTa 212,17,16384\nMvaa 112,144,164\nMvaa 144,144,168\nTa 214,17,18432\nMvaa 176,144,172\nTa 216,18,16384\nSv 56,1,16,77,1024\nMvaa 180,148,172\n"
"Ta 218,18,18432\nMvaa 148,148,168\nTa 220,19,16384\nMvaa 116,148,164\nTa 222,19,18432\nMvaa 84,148,160\nMvaa 88,152,160\nSv 20,1,16,77,2048\nMvaa 120,152,164\nMvaa 152,152,168\n"
"Mvaa 184,152,172\nMvaa 188,156,172\nMvaa 156,156,168\nSv 2,1,16,77,3072\nMvaa 124,156,164\nMvaa 92,156,160\ns_waitcnt vmcnt(16) lgkmcnt(0)\ns_barrier\nMvaa 64,192,224\n"
"Ra 160,44,49152\nv_cmp_ne_u32 vcc, s15, v27\ns_nop 0\ns_cbranch_vccnz .S401\n.S402:\ns_sub_u32 s45, s86, 131072\ns_cmp_eq_u32 s79, 1\ns_cselect_b32 s86, s94, s86\n"
"s_add_u32 m0, s47, -65536\ns_add_u32 s46, s45, 0x0\nMvaa 96,192,228\nRa 164,44,51200\nbuffer_store_dword v25, off, s[16:19], s54 offset:8 sc0 sc1\nMvaa 128,192,232\n"
"Ra 168,46,49152\nD1 1,16,46,0\nMvaa 160,192,236\nRa 172,46,51200\nAR 60,126\nMvaa 164,196,236\nTa 128,12,32768\nAR 61,127\nMvaa 132,196,232\nCV 60,60,61\nAW 127,60\n"
"Mvaa 100,196,228\nTa 130,12,34816\nD1 1,16,46,1024\nMvaa 68,196,224\nTa 132,13,32768\nAR 60,124\nMvaa 72,200,224\nTa 134,13,34816\nAR 61,125\nMvaa 104,200,228\nTa 136,14,32768\n"
"Mvaa 136,200,232\nTa 138,14,34816\nD1 1,16,46,2048\nMvaa 168,200,236\nCV 60,60,61\nAW 126,60\nMvaa 172,204,236\nTa 140,15,32768\nAR 60,122\nMvaa 140,204,232\nTa 142,15,34816\n"
"AR 61,123\nMvaa 108,204,228\nTa 144,16,32768\nD1 1,16,46,3072\nMvaa 76,204,224\nTa 146,16,34816\nCV 60,60,61\nMvaa 80,208,224\nTa 148,17,32768\ns_add_u32 s6, s56, 4096\n"
"Mvaa 112,208,228\nAW 125,60\nMvaa 144,208,232\nTa 150,17,34816\nbuffer_load_dwordx4 v[208:211], v1, s[16:19], s6 offen offset:0 sc1\nMvaa 176,208,236\nTa 152,18,32768\nAR 60,120\n"
"Mvaa 180,212,236\nTa 154,18,34816\nAR 61,121\nMvaa 148,212,232\nTa 156,19,32768\nCV 60,60,61\nMvaa 116,212,228\nTa 158,19,34816\n"
"buffer_load_dwordx4 v[212:215], v1, s[16:19], s6 offen offset:1024 sc1\nMvaa 84,212,224\nAW 124,60\nAR 60,118\nMvaa 88,216,224\nAR 61,119\nCV 60,60,61\nMvaa 120,216,228\n"
"AW 123,60\nMvaa 152,216,232\nbuffer_load_dwordx4 v[216:219], v1, s[16:19], s6 offen offset:2048 sc1\nAR 60,116\nMvaa 184,216,236\nAR 61,117\nCV 60,60,61\nMvaa 188,220,236\n"
"AW 122,60\nAR 60,114\nMvaa 156,220,232\nAR 61,115\nCV 60,60,61\nMvaa 124,220,228\nbuffer_load_dwordx4 v[220:223], v1, s[16:19], s6 offen offset:3072 sc1\nAW 121,60\n"
"Mvaa 92,220,224\nWL 0\nMvaa 64,128,160\nRa 224,45,49152\ns_add_u32 s6, s56, 16384\nMvaa 96,128,164\nRa 228,45,51200\nMvaa 128,128,168\nRa 232,47,49152\n"
"buffer_load_dwordx4 v[224:227], v1, s[16:19], s6 offen offset:0 sc1\nMvaa 160,128,172\nRa 236,47,51200\nAR 60,112\nMvaa 164,132,172\nTa 192,12,49152\nAR 61,113\nMvaa 132,132,168\n"
"CV 60,60,61\nAW 120,60\nMvaa 100,132,164\nTa 194,12,51200\nbuffer_load_dwordx4 v[228:231], v1, s[16:19], s6 offen offset:1024 sc1\nMvaa 68,132,160\nTa 196,13,49152\nAR 60,110\n"
"Mvaa 72,136,160\nTa 198,13,51200\nAR 61,111\nMvaa 104,136,164\nTa 200,14,49152\nMvaa 136,136,168\nTa 202,14,51200\n"
"buffer_load_dwordx4 v[232:235], v1, s[16:19], s6 offen offset:2048 sc1\nMvaa 168,136,172\nCV 60,60,61\nAW 119,60\nMvaa 172,140,172\nTa 204,15,49152\nAR 60,108\nMvaa 140,140,168\n"
"Ta 206,15,51200\nAR 61,109\nMvaa 108,140,164\nTa 208,16,49152\nbuffer_load_dwordx4 v[236:239], v1, s[16:19], s6 offen offset:3072 sc1\nMvaa 76,140,160\nTa 210,16,51200\n"
"CV 60,60,61\nMvaa 80,144,160\nTa 212,17,49152\nAW 118,60\nMvaa 112,144,164\nAR 60,106\nMvaa 144,144,168\nTa 214,17,51200\nAR 61,107\nMvaa 176,144,172\nTa 216,18,49152\n"
"CV 60,60,61\nMvaa 180,148,172\nTa 218,18,51200\nAW 117,60\nMvaa 148,148,168\nTa 220,19,49152\nAR 60,104\nMvaa 116,148,164\nTa 222,19,51200\nAR 61,105\nMvaa 84,148,160\n"
"CV 60,60,61\nAW 116,60\nMvaa 88,152,160\nAR 60,102\nAR 61,103\nMvaa 120,152,164\nCV 60,60,61\nMvaa 152,152,168\nAW 115,60\nAR 60,100\nMvaa 184,152,172\nAR 61,101\nCV 60,60,61\n"
"Mvaa 188,156,172\nAW 114,60\nAR 60,98\nMvaa 156,156,168\nAR 61,99\nCV 60,60,61\nMvaa 124,156,164\nAW 113,60\nAR 60,96\nMvaa 92,156,160\ns_waitcnt vmcnt(21) lgkmcnt(0)\ns_barrier\n"
"Mvaa 64,192,224\nRa 160,44,16384\nAR 61,97\nMvaa 96,192,228\nRa 164,44,18432\ns_add_u32 s6, s56, 20480\nbuffer_load_dwordx4 v[240:243], v1, s[16:19], s6 offen offset:0 sc1\n"
"Mvaa 128,192,232\nRa 168,46,16384\nCV 60,60,61\nMvaa 160,192,236\nRa 172,46,18432\nAW 112,60\nMvaa 164,196,236\nTa 128,32,0\nAR 60,94\nMvaa 132,196,232\n"
"buffer_load_dwordx4 v[244:247], v1, s[16:19], s6 offen offset:1024 sc1\nAR 61,95\nMvaa 100,196,228\nTa 130,32,2048\nCV 60,60,61\nMvaa 68,196,224\nTa 132,33,0\nAW 111,60\n"
"Mvaa 72,200,224\nTa 134,33,2048\nAR 60,92\nMvaa 104,200,228\nTa 136,34,0\nbuffer_load_dwordx4 v[248:251], v1, s[16:19], s6 offen offset:2048 sc1\nMvaa 136,200,232\nTa 138,34,2048\n"
"AR 61,93\nMvaa 168,200,236\nCV 60,60,61\nAW 110,60\nMvaa 172,204,236\nTa 140,35,0\nAR 60,90\nMvaa 140,204,232\nTa 142,35,2048\n"
"buffer_load_dwordx4 v[252:255], v1, s[16:19], s6 offen offset:3072 sc1\nMvaa 108,204,228\nTa 144,36,0\nAR 61,91\nMvaa 76,204,224\nTa 146,36,2048\ns_add_u32 m0, s47, -32768\n"
"s_add_u32 s46, s45, 0x8000\nMvaa 80,208,224\nTa 148,37,0\nCV 60,60,61\nMvaa 112,208,228\nD1 1,16,46,0\nMvaa 144,208,232\nTa 150,37,2048\nAW 109,60\nMvaa 176,208,236\nTa 152,38,0\n"
"AR 60,88\nMvaa 180,212,236\nTa 154,38,2048\nAR 61,89\nMvaa 148,212,232\nTa 156,39,0\nD1 1,16,46,1024\nMvaa 116,212,228\nTa 158,39,2048\nCV 60,60,61\nMvaa 84,212,224\nAW 108,60\n"
"AR 60,86\nMvaa 88,216,224\nAR 61,87\nCV 60,60,61\nMvaa 120,216,228\nD1 1,16,46,2048\nMvaa 152,216,232\nAW 107,60\nAR 60,84\nMvaa 184,216,236\nAR 61,85\nCV 60,60,61\n"
"Mvaa 188,220,236\nAW 106,60\nAR 60,82\nMvaa 156,220,232\nD1 1,16,46,3072\nAR 61,83\nMvaa 124,220,228\nCV 60,60,61\nAW 105,60\nMvaa 92,220,224\nWL 0\nMvaa 64,128,160\n"
"Ra 224,45,16384\nAR 60,80\nMvaa 96,128,164\nRa 228,45,18432\ns_add_u32 m0, s47, -16384\ns_add_u32 s46, s45, 0x400000\nD1 1,16,46,0\nMvaa 128,128,168\nRa 232,47,16384\nAR 61,81\n"
"Mvaa 160,128,172\nRa 236,47,18432\nCV 60,60,61\nMvaa 164,132,172\nTa 192,32,16384\nAW 104,60\nMvaa 132,132,168\nD1 1,16,46,1024\nAR 60,78\nMvaa 100,132,164\nTa 194,32,18432\n"
"AR 61,79\nMvaa 68,132,160\nTa 196,33,16384\nCV 60,60,61\nMvaa 72,136,160\nTa 198,33,18432\nAW 103,60\nMvaa 104,136,164\nTa 200,34,16384\nD1 1,16,46,2048\nMvaa 136,136,168\n"
"Ta 202,34,18432\nAR 60,76\nMvaa 168,136,172\nAR 61,77\nCV 60,60,61\nMvaa 172,140,172\nTa 204,35,16384\nAW 102,60\nMvaa 140,140,168\nTa 206,35,18432\nD1 1,16,46,3072\n"
"Mvaa 108,140,164\nTa 208,36,16384\nAR 60,74\nMvaa 76,140,160\nTa 210,36,18432\nAR 61,75\nMvaa 80,144,160\nTa 212,37,16384\nCV 60,60,61\nMvaa 112,144,164\nAW 101,60\n"
"Mvaa 144,144,168\nTa 214,37,18432\nAR 60,72\nMvaa 176,144,172\nTa 216,38,16384\nAR 61,73\nMvaa 180,148,172\nTa 218,38,18432\nCV 60,60,61\nMvaa 148,148,168\nTa 220,39,16384\n"
"AW 100,60\nMvaa 116,148,164\nTa 222,39,18432\nAR 60,70\nMvaa 84,148,160\nAR 61,71\nCV 60,60,61\nMvaa 88,152,160\nAW 99,60\nAR 60,68\nMvaa 120,152,164\nAR 61,69\nMvaa 152,152,168\n"
"CV 60,60,61\nAW 98,60\nMvaa 184,152,172\nAR 60,66\nAR 61,67\nMvaa 188,156,172\nCV 60,60,61\nAW 97,60\nMvaa 156,156,168\nAR 60,64\nAR 61,65\nMvaa 124,156,164\nCV 60,60,61\n"
"AW 96,60\nMvaa 92,156,160\ns_waitcnt vmcnt(20) lgkmcnt(0)\ns_barrier\nMvaa 64,192,224\nv_add_u32 v2, 0x0, v1\ns_lshr_b32 s2, s59, 1\nMvaa 68,196,224\ns_add_u32 m0, s47, 0\n"
"s_add_u32 s46, s86, 0xffff0000\nD1 1,16,46,0\nbuffer_store_dword v25, off, s[16:19], s54 offset:12 sc0 sc1\nMvaa 72,200,224\nv_add_u32 v24, s2, v2\nMvaa 76,204,224\n"
"v_xor_b32 v26, 0x1000, v24\nMvaa 80,208,224\nv_lshrrev_b32 v20, 4, v0\nv_lshlrev_b32 v20, 2, v20\nMvaa 84,212,224\nD1 1,16,46,1024\nMvaa 88,216,224\nv_and_b32 v0, 15, v0\n"
"v_mul_f32 v64, s57, v64\nMvaa 92,220,224\nv_sub_u32 v0, v0, v20\nv_mul_f32 v65, s57, v65\nCV 64,64,65\nMvaa 96,192,228\ns_lshl_b32 s2, s11, 4\nv_add_u32 v3, s2, v0\n"
"v_mul_f32 v66, s57, v66\nMvaa 100,196,228\nD1 1,16,46,2048\nv_mul_f32 v67, s57, v67\nCV 65,66,67\nMvaa 104,200,228\nv_subrev_u32 v0, 32, v3\nv_mul_f32 v68, s57, v68\n"
"v_mul_f32 v69, s57, v69\nMvaa 108,204,228\nv_mov_b32 v4, 0\nv_mov_b32 v5, 0\nMvaa 112,208,228\nRa 128,24,0\nv_mov_b32 v6, 0\nCV 66,68,69\nMvaa 116,212,228\nD1 1,16,46,3072\n"
"v_mul_f32 v70, s57, v70\nv_mul_f32 v71, s57, v71\nMvaa 120,216,228\nRa 132,24,1024\nv_mov_b32 v7, 0\nCV 67,70,71\nMvaa 124,220,228\ns_add_u32 m0, s47, 16384\n"
"s_add_u32 s46, s45, 0x408000\nv_mul_f32 v72, s57, v72\nMvaa 128,192,232\nRa 136,24,2048\nv_mov_b32 v8, 0\nv_mul_f32 v73, s57, v73\nMvaa 132,196,232\nD1 1,16,46,0\nCV 72,72,73\n"
"Mvaa 136,200,232\nRa 140,24,3072\nv_mov_b32 v9, 0\nv_mul_f32 v74, s57, v74\nMvaa 140,204,232\nv_mov_b32 v10, 0\nv_mov_b32 v11, 0\nv_mul_f32 v75, s57, v75\nMvaa 144,208,232\n"
"Ra 144,26,0\nv_mov_b32 v12, 0\nCV 73,74,75\nMvaa 148,212,232\nD1 1,16,46,1024\nv_mul_f32 v76, s57, v76\nv_mul_f32 v77, s57, v77\nMvaa 152,216,232\nRa 148,26,1024\n"
"v_mov_b32 v13, 0\nMvaa 156,220,232\nv_mov_b32 v14, 0\nv_mov_b32 v15, 0\nCV 74,76,77\nMvaa 160,192,236\nRa 152,26,2048\nv_mov_b32 v16, 0\nv_mul_f32 v78, s57, v78\nMvaa 164,196,236\n"
"D1 1,16,46,2048\nv_mul_f32 v79, s57, v79\nCV 75,78,79\nMvaa 168,200,236\nRa 156,26,3072\nv_mov_b32 v17, 0\nv_mul_f32 v80, s57, v80\nMvaa 172,204,236\nv_mov_b32 v18, 0\n"
"v_mov_b32 v19, 0\nMvaa 176,208,236\ns_lshl_b32 s2, s11, 13\ns_add_u32 s2, s2, 0x20000\nv_mul_f32 v81, s57, v81\nMvaa 180,212,236\nD1 1,16,46,3072\nCV 80,80,81\n"
"v_mul_f32 v82, s57, v82\nMvaa 184,216,236\nv_add_u32 v22, s2, v1\nv_xor_b32 v23, 0x2000, v22\nv_mul_f32 v83, s57, v83\nMvaa 188,220,236\ns_waitcnt vmcnt(25) lgkmcnt(4)\n"
"s_cmp_eq_u32 s79, 1\ns_cbranch_scc1 .E405\n.E406:\nCvav 32,128,192,4\nRa 160,24,8192\nCV 81,82,83\nv_mul_f32 v84, s57, v84\nMvav 32,132,200\nv_mul_f32 v85, s57, v85\nCV 82,84,85\n"
"Mvav 32,136,208\nRa 164,24,9216\nv_mul_f32 v86, s57, v86\nv_mul_f32 v87, s57, v87\nMvav 32,140,216\nCV 83,86,87\nv_mul_f32 v88, s57, v88\nv_mul_f32 v89, s57, v89\n"
"Cvav 36,140,220,8\nRa 168,24,10240\nCV 88,88,89\nv_mul_f32 v90, s57, v90\nMvav 36,136,212\ns_add_u32 m0, s89, 32768\nv_mul_f32 v91, s57, v91\nCV 89,90,91\nMvav 36,132,204\n"
"Ra 172,24,11264\ns_waitcnt vmcnt(17) lgkmcnt(4)\ns_barrier\nMvav 36,128,196\nv_mul_f32 v92, s57, v92\nv_mul_f32 v93, s57, v93\nCV 90,92,93\nMvav 36,144,228\nRa 176,26,8192\n"
"v_mul_f32 v94, s57, v94\nv_mul_f32 v95, s57, v95\nSa 96,1,16,78,0\nMvav 36,148,236\ns_add_u32 s46, s86, s88\nCV 91,94,95\nAW 192,64\nMvav 36,152,244\nRa 180,26,9216\nAW 193,65\n"
"AW 194,66\nMvav 36,156,252\nAW 195,67\nAW 200,72\nMvav 32,156,248\nRa 184,26,10240\nAW 201,73\nAW 202,74\nMvav 32,152,240\nD1 1,16,46,0\nAW 203,75\nAW 208,80\nMvav 32,148,232\n"
"Ra 188,26,11264\nWL 4\nAW 209,81\nMvav 32,144,224\nAW 210,82\nAW 211,83\nAW 216,88\nCvav 40,160,192,12\nAW 217,89\nAW 218,90\nAW 219,91\nSa 100,1,16,78,1024\nMvav 40,164,200\n"
"s_cmp_eq_u32 s99, -1\nMvav 40,168,208\ns_cselect_b32 s87, s95, s87\nv_mov_b32 v86, 0\nv_mov_b32 v87, 0\nMvav 40,172,216\nv_mov_b32 v88, 0\nv_mov_b32 v89, 0\n"
"v_mul_f32 v128, s57, v128\nv_mul_f32 v129, s57, v129\nCV 128,128,129\nv_mul_f32 v130, s57, v130\nv_mul_f32 v131, s57, v131\nCvav 44,172,220,16\nv_and_b32 v27, 0x70, v1\n"
"buffer_load_dword v27, v27, s[16:19], s61 offen offset:136 sc0 sc1\nCV 129,130,131\nv_mul_f32 v132, s57, v132\nv_mul_f32 v133, s57, v133\nCV 130,132,133\n"
"v_mul_f32 v134, s57, v134\nv_mul_f32 v135, s57, v135\nMvav 44,168,212\nD1 1,16,46,1024\nCV 131,134,135\nds_write_b128 v23, v[128:131] offset:0\nv_mul_f32 v136, s57, v136\n"
"v_mul_f32 v137, s57, v137\nCV 136,136,137\nv_mul_f32 v138, s57, v138\nMvav 44,164,204\nWL 1\nv_mul_f32 v139, s57, v139\nCV 137,138,139\nv_mul_f32 v140, s57, v140\n"
"v_mul_f32 v141, s57, v141\nCV 138,140,141\nv_mul_f32 v142, s57, v142\nv_mul_f32 v143, s57, v143\nMvav 44,160,196\nCV 139,142,143\nds_write_b128 v23, v[136:139] offset:2048\n"
"v_mul_f32 v144, s57, v144\nv_mul_f32 v145, s57, v145\nCV 144,144,145\nv_mul_f32 v146, s57, v146\nv_mul_f32 v147, s57, v147\nMvav 44,176,228\nCV 145,146,147\n"
"v_mul_f32 v148, s57, v148\nv_mul_f32 v149, s57, v149\nCV 146,148,149\nv_mul_f32 v150, s57, v150\nv_mul_f32 v151, s57, v151\nCV 147,150,151\n"
"ds_write_b128 v23, v[144:147] offset:4096\nSa 104,1,16,78,2048\nMvav 44,180,236\ns_add_u32 s88, s88, 32768\nEX 80,32\nv_mul_f32 v152, s57, v152\nv_mul_f32 v153, s57, v153\n"
"CV 152,152,153\nv_mul_f32 v154, s57, v154\nv_mul_f32 v155, s57, v155\nMvav 44,184,244\nCV 153,154,155\nv_mul_f32 v156, s57, v156\nv_mul_f32 v157, s57, v157\nCV 154,156,157\n"
"v_mul_f32 v158, s57, v158\nv_mul_f32 v159, s57, v159\nCV 155,158,159\nMvav 44,188,252\nEX 81,33\nds_write_b128 v23, v[152:155] offset:6144\nv_mul_f32 v160, s57, v160\n"
"v_mul_f32 v161, s57, v161\nCV 160,160,161\nv_mul_f32 v162, s57, v162\nMvav 40,188,248\nv_mul_f32 v163, s57, v163\nCV 161,162,163\nv_mul_f32 v164, s57, v164\n"
"v_mul_f32 v165, s57, v165\nCV 162,164,165\nv_mul_f32 v166, s57, v166\nv_mul_f32 v167, s57, v167\nMvav 40,184,240\nD1 1,16,46,2048\nCV 163,166,167\n"
"ds_write_b128 v23, v[160:163] offset:1024\nv_mul_f32 v168, s57, v168\nv_mul_f32 v169, s57, v169\nCV 168,168,169\nv_mul_f32 v170, s57, v170\nv_mul_f32 v171, s57, v171\n"
"Mvav 40,180,232\nCV 169,170,171\nv_mul_f32 v172, s57, v172\nv_mul_f32 v173, s57, v173\nCV 170,172,173\nv_mul_f32 v174, s57, v174\nv_mul_f32 v175, s57, v175\nCV 171,174,175\n"
"Mvav 40,176,224\nAD 84,80,81\nCV 64,80,81\nds_write_b128 v23, v[168:171] offset:3072\nv_mul_f32 v176, s57, v176\nv_mul_f32 v177, s57, v177\nCV 176,176,177\n"
"v_mul_f32 v178, s57, v178\nv_mul_f32 v179, s57, v179\nCV 177,178,179\nv_mul_f32 v180, s57, v180\nv_mul_f32 v181, s57, v181\nCV 178,180,181\nv_mul_f32 v182, s57, v182\n"
"v_mul_f32 v183, s57, v183\nCV 179,182,183\nds_write_b128 v23, v[176:179] offset:5120\nSa 108,1,16,78,3072\nEX 82,34\nv_mul_f32 v184, s57, v184\nv_mul_f32 v185, s57, v185\n"
"CV 184,184,185\nv_mul_f32 v186, s57, v186\nv_mul_f32 v187, s57, v187\nCV 185,186,187\nv_mul_f32 v188, s57, v188\nv_mul_f32 v189, s57, v189\nCV 186,188,189\n"
"v_mul_f32 v190, s57, v190\nv_mul_f32 v191, s57, v191\nCV 187,190,191\nds_write_b128 v23, v[184:187] offset:7168\ns_waitcnt vmcnt(17) lgkmcnt(0)\nEX 83,35\nAD 84,84,82\n"
"D1 1,16,46,3072\ns_add_u32 m0, s89, 49152\ns_barrier\nAD 84,84,83\nCV 65,82,83\nRa 128,24,32768\nRa 224,22,0\nRa 228,22,1024\nRa 232,22,2048\ns_add_u32 s46, s87, s88\nEX 80,36\n"
"Ra 132,24,33792\nRa 236,22,3072\nRa 240,22,4096\nRa 244,22,5120\nEX 81,37\nRa 248,22,6144\nRa 136,24,34816\nRa 252,22,7168\nv_mul_f32 v96, s57, v96\nv_mul_f32 v97, s57, v97\n"
"D1 1,16,46,0\nCV 96,96,97\nv_mul_f32 v98, s57, v98\nRa 140,24,35840\nv_mul_f32 v99, s57, v99\nCV 97,98,99\nv_mul_f32 v100, s57, v100\nAD 85,80,81\nCV 68,80,81\n"
"v_mul_f32 v101, s57, v101\nCV 98,100,101\nRa 144,26,32768\nv_mul_f32 v102, s57, v102\nv_mul_f32 v103, s57, v103\nCV 99,102,103\ns_cmp_eq_u32 s99, -1\nEX 82,38\nRa 148,26,33792\n"
"s_cbranch_scc1 .E388\n.E389:\nAW 196,96\nAW 197,97\nEX 83,39\nAW 198,98\nRa 152,26,34816\nAD 85,85,82\nAW 199,99\nv_mul_f32 v104, s57, v104\nD1 1,16,46,1024\n"
"v_mul_f32 v105, s57, v105\nCV 104,104,105\nRa 156,26,35840\nv_mul_f32 v106, s57, v106\nv_mul_f32 v107, s57, v107\nCV 105,106,107\nAD 85,85,83\nCV 69,82,83\n"
"v_mul_f32 v108, s57, v108\nv_mul_f32 v109, s57, v109\nRa 160,24,40960\nCV 106,108,109\nv_mul_f32 v110, s57, v110\nv_mul_f32 v111, s57, v111\ns_xor_b32 s89, s89, 0x10000\nEX 80,40\n"
"Ra 164,24,41984\nCV 107,110,111\nAW 204,104\nAW 205,105\nWL 6\nEX 81,41\nAW 206,106\nRa 168,24,43008\nAD 84,84,80\nAW 207,107\nv_mul_f32 v112, s57, v112\nD1 1,16,46,2048\n"
"v_mul_f32 v113, s57, v113\nCV 112,112,113\nRa 172,24,44032\nv_mul_f32 v114, s57, v114\nv_mul_f32 v115, s57, v115\nCV 113,114,115\nAD 84,84,81\nCV 66,80,81\n"
"v_mul_f32 v116, s57, v116\nv_mul_f32 v117, s57, v117\nCvav 48,128,192,4\nRa 176,26,40960\nCV 114,116,117\nv_mul_f32 v118, s57, v118\nv_mul_f32 v119, s57, v119\nMvav 48,132,200\n"
"s_cmp_eq_u32 s99, 0\nEX 82,42\nMvav 48,136,208\nRa 180,26,41984\ns_cselect_b32 s86, s94, s86\nCV 115,118,119\nAW 212,112\nMvav 48,140,216\nWL 6\nEX 83,43\nAW 213,113\nWV 8\n"
"v_cmp_ne_u32 vcc, s15, v27\ns_nop 0\ns_cbranch_vccnz .S407\n.S408:\nCvav 52,140,220,8\nRa 184,26,43008\nAD 84,84,82\nAW 214,114\nAW 215,115\nMvav 52,136,212\nD1 1,16,46,3072\n"
"s_add_u32 m0, s89, 0\nv_mul_f32 v120, s57, v120\nMvav 52,132,204\nRa 188,26,44032\nv_mul_f32 v121, s57, v121\nCV 120,120,121\nv_mul_f32 v122, s57, v122\nMvav 52,128,196\n"
"AD 84,84,83\nCV 67,82,83\nv_mul_f32 v123, s57, v123\nCV 121,122,123\nMvav 52,144,228\nRa 128,2,49152\nv_mul_f32 v124, s57, v124\nv_mul_f32 v125, s57, v125\nCV 122,124,125\n"
"s_add_u32 s78, s78, s55\nSa 112,1,16,78,0\nMvav 52,148,236\ns_add_u32 s46, s86, s88\nEX 80,44\nMvav 52,152,244\nRa 132,2,50176\nv_xor_b32 v24, 0x10000, v24\n"
"v_mul_f32 v126, s57, v126\nv_mul_f32 v127, s57, v127\nMvav 52,156,252\nWL 6\nEX 81,45\nCV 123,126,127\nMvav 48,156,248\nRa 136,2,51200\nAD 85,85,80\nAW 220,120\nAW 221,121\n"
"Mvav 48,152,240\nD1 1,16,46,0\nAW 222,122\nAW 223,123\nMvav 48,148,232\nRa 140,2,52224\nv_mov_b32 v112, -1\nv_mov_b32 v113, -1\nv_mov_b32 v114, -1\nMvav 48,144,224\nAD 85,85,81\n"
"CV 70,80,81\nv_mov_b32 v115, -1\nCvav 56,160,192,12\nRa 144,2,53248\nSa 116,1,16,78,1024\nMvav 56,164,200\nEX 82,46\nMvav 56,168,208\nRa 148,2,54272\nv_xor_b32 v26, 0x10000, v26\n"
"Mvav 56,172,216\nWL 6\nEX 83,47\nCvav 60,172,220,16\nRa 152,2,55296\nAD 85,85,82\nMvav 60,168,212\nD1 1,16,46,1024\nMvav 60,164,204\nRa 156,2,56320\nMvav 60,160,196\nAD 85,85,83\n"
"CV 71,82,83\nMvav 60,176,228\nRa 160,2,57344\nv_max_f32 v90, v84, v85\nMvav 60,180,236\ns_add_u32 s88, s88, 32768\nEX 80,48\nMvav 60,184,244\nRa 164,2,58368\n"
"v_cmp_lt_f32 vcc, s63, v90\nMvav 60,188,252\nWL 8\nEX 81,49\nMvav 56,188,248\nRa 168,2,59392\ns_cbranch_vccnz .T394\n.T395:\nv_add_f32 v20, s93, v84\nv_mov_b32 v21, v85\n"
"v_mov_b32 v22, v20\nv_mov_b32 v23, v21\nMvav 56,184,240\nD1 1,16,46,2048\nv_permlane32_swap_b32 v20, v22\nv_permlane32_swap_b32 v21, v23\nAD 20,20,22\nAD 21,21,23\n"
"v_mov_b32 v22, v20\nv_mov_b32 v23, v21\nMvav 56,180,232\nRa 172,2,60416\nv_permlane16_swap_b32 v20, v22\nv_permlane16_swap_b32 v21, v23\nAD 20,20,22\nAD 21,21,23\n"
"v_min_f32 v90, v20, v21\nv_cmp_gt_f32 vcc, s64, v90\ns_nop 0\ns_cbranch_vccnz .T396\n.T397:\nAD 86,86,84\nMvav 56,176,224\nAD 84,80,81\nCV 72,80,81\nZaav 0,128,64\nRa 176,2,61440\n"
"Sa 120,1,16,78,2048\nZaav 4,128,68\nEX 82,50\nZaav 12,132,68\nRa 180,2,62464\nZaav 8,132,64\nWL 8\nEX 83,51\nZaav 16,136,64\nRa 184,2,63488\nAD 84,84,82\nZaav 20,136,68\n"
"D1 1,16,46,3072\ns_add_u32 m0, s89, 16384\nZaav 28,140,68\nRa 188,2,64512\ns_barrier\nZaav 24,140,64\nAD 84,84,83\nCV 73,82,83\nZaav 32,144,64\nRa 128,24,0\nSa 124,1,16,78,3072\n"
"Zaav 36,144,68\ns_add_u32 s46, s87, s88\nEX 80,52\nZaav 44,148,68\nRa 132,24,1024\nv_xor_b32 v2, 0x10000, v2\nZaav 40,148,64\nWL 8\nEX 81,53\nZaav 48,152,64\nRa 136,24,2048\n"
"AD 87,87,85\nZaav 52,152,68\nD1 1,16,46,0\nZaav 60,156,68\nRa 140,24,3072\nZaav 56,156,64\nAD 85,80,81\nCV 76,80,81\nZaav 64,160,64\nRa 144,26,0\nZaav 68,160,68\n"
"s_cmp_lt_u32 s99, s92\nEX 82,54\nZaav 76,164,68\nRa 148,26,1024\ns_cbranch_scc1 .E390\n.E391:\nZaav 72,164,64\nWL 8\nEX 83,55\nZaav 80,168,64\nRa 152,26,2048\nAD 85,85,82\n"
"Zaav 84,168,68\nD1 1,16,46,1024\nZaav 92,172,68\nRa 156,26,3072\nZaav 88,172,64\nAD 85,85,83\nCV 77,82,83\nZaav 96,176,64\nRa 160,24,8192\nZaav 100,176,68\ns_sub_u32 s90, s90, 1\n"
"EX 80,56\nZaav 108,180,68\nRa 164,24,9216\ns_cbranch_scc1 .E392\n.E393:\nZaav 104,180,64\nWL 6\nEX 81,57\nZaav 112,184,64\nRa 168,24,10240\nAD 84,84,80\nv_and_b32 v27, 0x70, v1\n"
"buffer_load_dword v27, v27, s[16:19], s61 offen offset:140 sc0 sc1\nZaav 116,184,68\nD1 1,16,46,2048\nZaav 124,188,68\nRa 172,24,11264\nZaav 120,188,64\nAD 84,84,81\nCV 74,80,81\n"
"s_sub_u32 s99, s99, 1\ns_cmp_eq_u32 s13, 7\ns_cbranch_scc1 .SW384\n.p2align 6\n.A410:\nCvav 32,128,192,4\nRa 176,26,8192\nMvav 32,132,200\nEX 82,58\nMvav 32,136,208\n"
"Ra 180,26,9216\nMvav 32,140,216\nWL 6\nEX 83,59\nCvav 36,140,220,8\nRa 184,26,10240\nAD 84,84,82\nMvav 36,136,212\nD1 1,16,46,3072\ns_add_u32 m0, s89, 32768\nMvav 36,132,204\n"
"Ra 188,26,11264\nMvav 36,128,196\nAD 84,84,83\nCV 75,82,83\nMvav 36,144,228\nRa 128,2,16384\nMvav 36,148,236\ns_add_u32 s46, s86, s88\nEX 80,60\nMvav 36,152,244\nRa 132,2,17408\n"
"Mvav 36,156,252\nWL 6\nEX 81,61\nMvav 32,156,248\nRa 136,2,18432\nAD 85,85,80\nMvav 32,152,240\nD1 1,16,46,0\nMvav 32,148,232\nRa 140,2,19456\nSv 112,1,16,49,0\nMvav 32,144,224\n"
"AD 85,85,81\nCV 78,80,81\nCvav 40,160,192,12\nRa 144,2,20480\nMvav 40,164,200\nEX 82,62\nMvav 40,168,208\nRa 148,2,21504\nMvav 40,172,216\nWL 6\nEX 83,63\nCvav 44,172,220,16\n"
"Ra 152,2,22528\nAD 85,85,82\nMvav 44,168,212\nD1 1,16,46,1024\nMvav 44,164,204\nRa 156,2,23552\nMvav 44,160,196\nAD 85,85,83\nCV 79,82,83\nMvav 44,176,228\nRa 160,2,24576\n"
"v_max_f32 v90, v84, v85\nMvav 44,180,236\ns_add_u32 s88, s88, 32768\nEX 80,32\nMvav 44,184,244\nRa 164,2,25600\nv_cmp_lt_f32 vcc, s63, v90\nMvav 44,188,252\nWL 8\nEX 81,33\n"
"Mvav 40,188,248\nRa 168,2,26624\ns_cbranch_vccnz .T418\n.T419:\nMvav 40,184,240\nD1 1,16,46,2048\nMvav 40,180,232\nRa 172,2,27648\nAD 86,86,84\nMvav 40,176,224\nAD 84,80,81\n"
"CV 64,80,81\nMaav 0,128,72\nRa 176,2,28672\nMaav 4,128,76\nEX 82,34\nMaav 12,132,76\nRa 180,2,29696\nMaav 8,132,72\ns_waitcnt vmcnt(13) lgkmcnt(8)\nEX 83,35\nMaav 16,136,72\n"
"Ra 184,2,30720\nAD 84,84,82\nMaav 20,136,76\nD1 1,16,46,3072\ns_add_u32 m0, s89, 49152\nMaav 28,140,76\nRa 188,2,31744\ns_barrier\nMaav 24,140,72\nAD 84,84,83\nCV 65,82,83\n"
"Maav 32,144,72\nRa 128,24,32768\nMaav 36,144,76\ns_add_u32 s46, s87, s88\nEX 80,36\nMaav 44,148,76\nRa 132,24,33792\nMaav 40,148,72\nWL 8\nEX 81,37\nMaav 48,152,72\n"
"Ra 136,24,34816\nAD 87,87,85\nMaav 52,152,76\nD1 1,16,46,0\nMaav 60,156,76\nRa 140,24,35840\nMaav 56,156,72\nAD 85,80,81\nCV 68,80,81\nMaav 64,160,72\nRa 144,26,32768\n"
"Maav 68,160,76\ns_cmp_eq_u32 s99, 0\nEX 82,38\nMaav 76,164,76\nRa 148,26,33792\ns_cbranch_scc1 .E412\n.E413:\nMaav 72,164,72\nWL 8\nEX 83,39\nMaav 80,168,72\nRa 152,26,34816\n"
"AD 85,85,82\nMaav 84,168,76\nD1 1,16,46,1024\nMaav 92,172,76\nRa 156,26,35840\nMaav 88,172,72\nAD 85,85,83\nCV 69,82,83\nMaav 96,176,72\nRa 160,24,40960\nMaav 100,176,76\n"
"s_xor_b32 s89, s89, 0x10000\nEX 80,40\nMaav 108,180,76\nRa 164,24,41984\nMaav 104,180,72\nWL 6\nEX 81,41\nMaav 112,184,72\nRa 168,24,43008\nAD 84,84,80\nMaav 116,184,76\n"
"D1 1,16,46,2048\nMaav 124,188,76\nRa 172,24,44032\nMaav 120,188,72\nAD 84,84,81\nCV 66,80,81\nCvav 48,128,192,4\nRa 176,26,40960\nMvav 48,132,200\ns_cmp_eq_u32 s99, 0\nEX 82,42\n"
"Mvav 48,136,208\nRa 180,26,41984\ns_cselect_b32 s86, s94, s86\nMvav 48,140,216\nWL 6\nEX 83,43\nCvav 52,140,220,8\nRa 184,26,43008\nAD 84,84,82\nMvav 52,136,212\nD1 1,16,46,3072\n"
"s_add_u32 m0, s89, 0\nMvav 52,132,204\nRa 188,26,44032\nMvav 52,128,196\nAD 84,84,83\nCV 67,82,83\nMvav 52,144,228\nRa 128,2,49152\nMvav 52,148,236\ns_add_u32 s46, s86, s88\n"
"EX 80,44\nMvav 52,152,244\nRa 132,2,50176\nv_xor_b32 v24, 0x10000, v24\nMvav 52,156,252\nWL 6\nEX 81,45\nMvav 48,156,248\nRa 136,2,51200\nAD 85,85,80\nMvav 48,152,240\n"
"D1 1,16,46,0\nMvav 48,148,232\nRa 140,2,52224\nSv 112,1,16,49,1024\nMvav 48,144,224\nAD 85,85,81\nCV 70,80,81\nCvav 56,160,192,12\nRa 144,2,53248\nMvav 56,164,200\n"
"s_add_u32 s49, s49, 131072\nEX 82,46\nMvav 56,168,208\nRa 148,2,54272\nv_xor_b32 v26, 0x10000, v26\nMvav 56,172,216\nWL 6\nEX 83,47\nCvav 60,172,220,16\nRa 152,2,55296\n"
"AD 85,85,82\nMvav 60,168,212\nD1 1,16,46,1024\nMvav 60,164,204\nRa 156,2,56320\nMvav 60,160,196\nAD 85,85,83\nCV 71,82,83\nMvav 60,176,228\nRa 160,2,57344\n"
"v_max_f32 v90, v84, v85\nMvav 60,180,236\ns_add_u32 s88, s88, 32768\nEX 80,48\nMvav 60,184,244\nRa 164,2,58368\nv_cmp_lt_f32 vcc, s63, v90\nMvav 60,188,252\nWL 8\nEX 81,49\n"
"Mvav 56,188,248\nRa 168,2,59392\ns_cbranch_vccnz .T420\n.T421:\nMvav 56,184,240\nD1 1,16,46,2048\nMvav 56,180,232\nRa 172,2,60416\nAD 86,86,84\nMvav 56,176,224\nAD 84,80,81\n"
"CV 72,80,81\nMaav 0,128,64\nRa 176,2,61440\nMaav 4,128,68\nEX 82,50\nMaav 12,132,68\nRa 180,2,62464\nMaav 8,132,64\ns_waitcnt vmcnt(13) lgkmcnt(8)\nEX 83,51\nMaav 16,136,64\n"
"Ra 184,2,63488\nAD 84,84,82\nMaav 20,136,68\nD1 1,16,46,3072\ns_add_u32 m0, s89, 16384\nMaav 28,140,68\nRa 188,2,64512\ns_barrier\nMaav 24,140,64\nAD 84,84,83\nCV 73,82,83\n"
"Maav 32,144,64\nRa 128,24,0\nMaav 36,144,68\ns_add_u32 s46, s87, s88\nEX 80,52\nMaav 44,148,68\nRa 132,24,1024\nv_xor_b32 v2, 0x10000, v2\nMaav 40,148,64\nWL 8\nEX 81,53\n"
"Maav 48,152,64\nRa 136,24,2048\nAD 87,87,85\nMaav 52,152,68\nD1 1,16,46,0\nMaav 60,156,68\nRa 140,24,3072\nMaav 56,156,64\nAD 85,80,81\nCV 76,80,81\nMaav 64,160,64\nRa 144,26,0\n"
"Maav 68,160,68\ns_cmp_lt_u32 s99, s92\nEX 82,54\nMaav 76,164,68\nRa 148,26,1024\ns_cbranch_scc1 .E414\n.E415:\nMaav 72,164,64\nWL 8\nEX 83,55\nMaav 80,168,64\nRa 152,26,2048\n"
"AD 85,85,82\nMaav 84,168,68\nD1 1,16,46,1024\nMaav 92,172,68\nRa 156,26,3072\nMaav 88,172,64\nAD 85,85,83\nCV 77,82,83\nMaav 96,176,64\nRa 160,24,8192\nMaav 100,176,68\n"
"s_sub_u32 s90, s90, 1\nEX 80,56\nMaav 108,180,68\nRa 164,24,9216\ns_cbranch_scc1 .E416\n.E417:\nMaav 104,180,64\nWL 6\nEX 81,57\nMaav 112,184,64\nRa 168,24,10240\nAD 84,84,80\n"
"Maav 116,184,68\nD1 1,16,46,2048\ns_sub_u32 s99, s99, 1\nMaav 124,188,68\nRa 172,24,11264\nMaav 120,188,64\nAD 84,84,81\nCV 74,80,81\ns_cbranch_scc0 .A410\nRa 176,26,8192\n"
"EX 82,58\nRa 180,26,9216\nWL 6\nEX 83,59\nCvav 36,140,220,8\nRa 184,26,10240\nAD 84,84,82\nMvav 36,136,212\nD1 1,16,46,3072\ns_add_u32 m0, s89, 32768\nMvav 36,132,204\n"
"Ra 188,26,11264\nMvav 36,128,196\nAD 84,84,83\nCV 75,82,83\nMvav 36,144,228\nRa 128,2,16384\nMvav 36,148,236\ns_add_u32 s46, s86, s88\nEX 80,60\nMvav 36,152,244\nRa 132,2,17408\n"
"Mvav 36,156,252\nEX 81,61\nRa 136,2,18432\nAD 85,85,80\nD1 1,16,46,0\nRa 140,2,19456\nSv 112,1,16,49,0\nAD 85,85,81\nCV 78,80,81\nRa 144,2,20480\ns_mov_b32 s87, s95\nEX 82,62\n"
"Ra 148,2,21504\nWL 6\nEX 83,63\nCvav 44,172,220,16\nRa 152,2,22528\nAD 85,85,82\nMvav 44,168,212\nD1 1,16,46,1024\nMvav 44,164,204\nRa 156,2,23552\nMvav 44,160,196\nAD 85,85,83\n"
"CV 79,82,83\nMvav 44,176,228\nRa 160,2,24576\nv_max_f32 v90, v84, v85\nMvav 44,180,236\ns_add_u32 s88, s88, 32768\nMvav 44,184,244\nRa 164,2,25600\nv_cmp_lt_f32 vcc, s63, v90\n"
"Mvav 44,188,252\nWL 8\nRa 168,2,26624\ns_cbranch_vccnz .T425\n.T426:\nD1 1,16,46,2048\nRa 172,2,27648\nAD 86,86,84\nMaav 0,128,72\nRa 176,2,28672\nMaav 4,128,76\nMaav 12,132,76\n"
"Ra 180,2,29696\nMaav 8,132,72\ns_waitcnt vmcnt(13) lgkmcnt(8)\nMaav 16,136,72\nRa 184,2,30720\nMaav 20,136,76\nD1 1,16,46,3072\ns_add_u32 m0, s89, 49152\nMaav 28,140,76\n"
"Ra 188,2,31744\ns_barrier\nMaav 24,140,72\nMaav 32,144,72\nRa 128,24,32768\nMaav 36,144,76\ns_add_u32 s46, s87, s88\nEX 80,36\nMaav 44,148,76\nRa 132,24,33792\nMaav 40,148,72\n"
"WL 8\nEX 81,37\nMaav 48,152,72\nRa 136,24,34816\nAD 87,87,85\nMaav 52,152,76\nD1 1,16,46,0\nMaav 60,156,76\nRa 140,24,35840\nMaav 56,156,72\nAD 85,80,81\nCV 68,80,81\n"
"Maav 64,160,72\nRa 144,26,32768\nMaav 68,160,76\ns_nop 0\nEX 82,38\nMaav 76,164,76\nRa 148,26,33792\nv_cmpx_gt_i32 vcc, 19, v0\nv_mov_b32 v19, s97\nv_cmpx_gt_i32 vcc, 18, v0\n"
"v_mov_b32 v18, s97\nv_cmpx_gt_i32 vcc, 17, v0\nv_mov_b32 v17, s97\nv_cmpx_gt_i32 vcc, 16, v0\nv_mov_b32 v16, s97\nv_cmpx_gt_i32 vcc, 3, v0\nv_mov_b32 v11, s97\n"
"v_cmpx_gt_i32 vcc, 2, v0\nv_mov_b32 v10, s97\nv_cmpx_gt_i32 vcc, 1, v0\nv_mov_b32 v9, s97\nv_cmpx_gt_i32 vcc, 0, v0\nv_mov_b32 v8, s97\ns_mov_b64 exec, -1\ns_nop 2\ns_nop 0\n"
"Maav 72,164,72\nWL 8\nEX 83,39\nMaav 80,168,72\nRa 152,26,34816\nAD 85,85,82\nMaav 84,168,76\nD1 1,16,46,1024\nMaav 92,172,76\nRa 156,26,35840\nMaav 88,172,72\nAD 85,85,83\n"
"CV 69,82,83\nMaav 96,176,72\nRa 160,24,40960\nMaav 100,176,76\ns_xor_b32 s89, s89, 0x10000\nMaav 108,180,76\nRa 164,24,41984\nMaav 104,180,72\nWL 10\nMaav 112,184,72\n"
"Ra 168,24,43008\nMaav 116,184,76\nD1 1,16,46,2048\nMaav 124,188,76\nRa 172,24,44032\nMaav 120,188,72\nRa 176,26,40960\nRa 180,26,41984\nWL 6\nCvav 52,140,220,8\nRa 184,26,43008\n"
"Mvav 52,136,212\nD1 1,16,46,3072\ns_add_u32 m0, s89, 0\nMvav 52,132,204\nRa 188,26,44032\nMvav 52,128,196\nMvav 52,144,228\nRa 128,2,49152\nMvav 52,148,236\n"
"s_add_u32 s46, s86, s88\nEX 80,44\nMvav 52,152,244\nRa 132,2,50176\nv_xor_b32 v24, 0x10000, v24\nMvav 52,156,252\nEX 81,45\nRa 136,2,51200\nAD 85,85,80\nD1 1,16,46,0\n"
"Ra 140,2,52224\nSv 112,1,16,49,1024\nAD 85,85,81\nCV 70,80,81\nRa 144,2,53248\ns_add_u32 s49, s49, 131072\nEX 82,46\nRa 148,2,54272\nv_xor_b32 v26, 0x10000, v26\nWL 6\nEX 83,47\n"
"Cvav 60,172,220,16\nRa 152,2,55296\nAD 85,85,82\nMvav 60,168,212\nD1 1,16,46,1024\nMvav 60,164,204\nRa 156,2,56320\nMvav 60,160,196\nAD 85,85,83\nCV 71,82,83\nMvav 60,176,228\n"
"Ra 160,2,57344\nMvav 60,180,236\ns_add_u32 s88, s88, 32768\nMvav 60,184,244\nRa 164,2,58368\nv_cmp_lt_f32 vcc, s63, v85\nMvav 60,188,252\nWL 8\nRa 168,2,59392\n"
"s_cbranch_vccnz .T427\n.T428:\nD1 1,16,46,2048\nRa 172,2,60416\nRa 176,2,61440\nMaav 4,128,68\nMaav 12,132,68\nRa 180,2,62464\ns_waitcnt vmcnt(13) lgkmcnt(8)\nRa 184,2,63488\n"
"Maav 20,136,68\nD1 1,16,46,3072\ns_add_u32 m0, s89, 16384\nMaav 28,140,68\nRa 188,2,64512\ns_barrier\nRa 128,24,0\nMaav 36,144,68\ns_add_u32 s46, s87, s88\nEX 80,52\n"
"Maav 44,148,68\nRa 132,24,1024\nv_xor_b32 v2, 0x10000, v2\nWL 8\nEX 81,53\nRa 136,24,2048\nAD 87,87,85\nMaav 52,152,68\nD1 1,16,46,0\nMaav 60,156,68\nRa 140,24,3072\nAD 85,80,81\n"
"CV 76,80,81\nRa 144,26,0\nMaav 68,160,68\nEX 82,54\nMaav 76,164,68\nRa 148,26,1024\nWL 8\nEX 83,55\nRa 152,26,2048\nAD 85,85,82\nMaav 84,168,68\nD1 1,16,46,1024\nMaav 92,172,68\n"
"Ra 156,26,3072\nAD 85,85,83\nCV 77,82,83\nRa 160,24,8192\nMaav 100,176,68\ns_sub_u32 s90, s90, 1\nMaav 108,180,68\nRa 164,24,9216\ns_cbranch_scc1 .E423\n.E424:\nWL 6\n"
"Ra 168,24,10240\nMaav 116,184,68\nD1 1,16,46,2048\nMaav 124,188,68\nRa 172,24,11264\n.SW384:\nv_mov_b32 v4, 0\nv_mov_b32 v5, 0\nv_mov_b32 v6, 0\nv_mov_b32 v7, 0\nv_mov_b32 v8, 0\n"
"v_mov_b32 v9, 0\nv_mov_b32 v10, 0\nv_mov_b32 v11, 0\nv_mov_b32 v12, 0\nv_mov_b32 v13, 0\nv_mov_b32 v14, 0\nv_mov_b32 v15, 0\nv_mov_b32 v16, 0\nv_mov_b32 v17, 0\nv_mov_b32 v18, 0\n"
"v_mov_b32 v19, 0\ns_add_u32 s99, s98, 1\ns_mov_b32 s94, s96\ns_add_u32 s95, s96, 65536\ns_lshl2_add_u32 s2, s13, s11\ns_cmp_lt_u32 s2, 2\ns_cbranch_scc1 .E429\n.E430:\n"
"Cvaa 32,128,192,4\nRa 176,26,8192\nMvaa 32,132,200\nMvaa 32,136,208\nRa 180,26,9216\nMvaa 32,140,216\nWL 6\nCvaa 36,140,220,8\nRa 184,26,10240\nMvaa 36,136,212\nD1 1,16,46,3072\n"
"s_add_u32 m0, s89, 32768\nMvaa 36,132,204\nRa 188,26,11264\nMvaa 36,128,196\nMvaa 36,144,228\nRa 128,2,16384\nMvaa 36,148,236\ns_add_u32 s46, s86, s88\nEX 80,60\nMvaa 36,152,244\n"
"Ra 132,2,17408\nMvaa 36,156,252\nWL 6\nEX 81,61\nMvaa 32,156,248\nRa 136,2,18432\nAD 85,85,80\nMvaa 32,152,240\nD1 1,16,46,0\nMvaa 32,148,232\nRa 140,2,19456\nMvaa 32,144,224\n"
"AD 85,85,81\nCV 78,80,81\nCvaa 40,160,192,12\nRa 144,2,20480\nMvaa 40,164,200\nEX 82,62\nMvaa 40,168,208\nRa 148,2,21504\nMvaa 40,172,216\nWL 6\nEX 83,63\nCvaa 44,172,220,16\n"
"Ra 152,2,22528\nAD 85,85,82\nMvaa 44,168,212\nD1 1,16,46,1024\nMvaa 44,164,204\nRa 156,2,23552\nMvaa 44,160,196\nAD 85,85,83\nCV 79,82,83\nMvaa 44,176,228\nRa 160,2,24576\n"
"Mvaa 44,180,236\ns_add_u32 s88, s88, 32768\nEX 80,32\nMvaa 44,184,244\nRa 164,2,25600\nv_cmp_lt_f32 vcc, s63, v85\nMvaa 44,188,252\nWL 8\nEX 81,33\nMvaa 40,188,248\n"
"Ra 168,2,26624\ns_cbranch_vccnz .T438\n.T439:\nMvaa 40,184,240\nD1 1,16,46,2048\nMvaa 40,180,232\nRa 172,2,27648\nMvaa 40,176,224\nAD 84,80,81\nCV 64,80,81\nRa 176,2,28672\n"
"Maav 4,128,76\nEX 82,34\nMaav 12,132,76\nRa 180,2,29696\ns_waitcnt vmcnt(12) lgkmcnt(8)\nEX 83,35\nRa 184,2,30720\nAD 84,84,82\nMaav 20,136,76\nD1 1,16,46,3072\n"
"s_add_u32 m0, s89, 49152\nMaav 28,140,76\nRa 188,2,31744\ns_barrier\nAR 98,0\nAR 99,1\nCV 100,98,99\nAD 84,84,83\nCV 65,82,83\nAR 98,2\nAR 99,3\nCV 101,98,99\nRa 128,24,32768\n"
"AR 98,8\nAR 99,9\nCV 102,98,99\nAR 98,10\nMaav 36,144,76\ns_add_u32 s46, s87, s88\nEX 80,36\nAR 99,11\nMaav 44,148,76\nRa 132,24,33792\nCV 103,98,99\ns_add_u32 s5, s66, 0\n"
"Sv 100,1,16,5,0\nAR 98,4\nWL 8\nEX 81,37\nAR 99,5\nCV 104,98,99\nRa 136,24,34816\nAD 87,87,85\nAR 98,6\nAR 99,7\nCV 105,98,99\nMaav 52,152,76\nD1 1,16,46,0\nv_mov_b32 v96, v86\n"
"v_mov_b32 v97, v87\nAR 98,12\nAR 99,13\nMaav 60,156,76\nRa 140,24,35840\nv_permlane32_swap_b32 v86, v96\nCV 106,98,99\nAR 98,14\nAR 99,15\nAD 85,80,81\nCV 68,80,81\nCV 107,98,99\n"
"s_add_u32 s5, s66, 0\nSv 104,1,16,5,1024\nRa 144,26,32768\nv_permlane32_swap_b32 v87, v97\nAD 86,86,96\nAR 98,16\nAR 99,17\nMaav 68,160,76\ns_cmp_eq_u32 s99, 0\nEX 82,38\n"
"Maav 76,164,76\nRa 148,26,33792\ns_cbranch_scc1 .E432\n.E433:\nCV 108,98,99\nAR 98,18\nAR 99,19\nWL 8\nEX 83,39\nCV 109,98,99\nAR 98,24\nRa 152,26,34816\nAD 85,85,82\nAD 87,87,97\n"
"AR 99,25\nCV 110,98,99\nMaav 84,168,76\nD1 1,16,46,1024\nv_mov_b32 v96, v86\nAR 98,26\nAR 99,27\nCV 111,98,99\nMaav 92,172,76\nRa 156,26,35840\nv_mov_b32 v97, v87\n"
"s_add_u32 s5, s66, 0\nSv 108,1,16,5,2048\nAR 98,20\nAD 85,85,83\nCV 69,82,83\nv_permlane16_swap_b32 v86, v96\nAR 99,21\nCV 100,98,99\nRa 160,24,40960\n"
"v_permlane16_swap_b32 v87, v97\nAR 98,22\nAR 99,23\nCV 101,98,99\nMaav 100,176,76\ns_xor_b32 s89, s89, 0x10000\nEX 80,40\nAR 98,28\nMaav 108,180,76\nRa 164,24,41984\nAD 86,86,96\n"
"AD 87,87,97\nAR 99,29\nCV 102,98,99\nWL 6\nEX 81,41\nAR 98,30\nAR 99,31\nRa 168,24,43008\nAD 84,84,80\nCV 103,98,99\ns_add_u32 s5, s66, 0\nSv 100,1,16,5,3072\nMaav 116,184,76\n"
"D1 1,16,46,2048\nv_cmp_gt_u32 vcc, 0x100, v1\nv_mov_b32 v94, s15\nv_mov_b32 v95, s15\nv_cndmask_b32 v92, v89, v88, vcc\nv_cndmask_b32 v93, v87, v86, vcc\nMaav 124,188,76\n"
"Ra 172,24,44032\nv_mov_b32 v86, 0\nAR 98,32\nAR 99,33\nCV 104,98,99\nAD 84,84,81\nCV 66,80,81\nAR 98,34\nAR 99,35\nCV 105,98,99\nCvaa 48,128,192,4\nRa 176,26,40960\n"
"v_mov_b32 v87, 0\nv_mov_b32 v88, 0\nAR 98,40\nAR 99,41\nMvaa 48,132,200\ns_cmp_eq_u32 s99, 0\nEX 82,42\nMvaa 48,136,208\nRa 180,26,41984\ns_cselect_b32 s86, s94, s86\n"
"CV 106,98,99\nAR 98,42\nAR 99,43\nMvaa 48,140,216\nWL 6\nEX 83,43\nCV 107,98,99\nCvaa 52,140,220,8\nRa 184,26,43008\nAD 84,84,82\nv_mov_b32 v89, 0\ns_add_u32 s5, s66, 4096\n"
"Sv 104,1,16,5,0\nMvaa 52,136,212\nD1 1,16,46,3072\ns_add_u32 m0, s89, 0\nAR 98,36\nAR 99,37\nCV 108,98,99\nMvaa 52,132,204\nRa 188,26,44032\nAR 98,38\nAR 99,39\nCV 109,98,99\n"
"AR 98,44\nMvaa 52,128,196\nAD 84,84,83\nCV 67,82,83\nAR 99,45\nCV 110,98,99\nAR 98,46\nMvaa 52,144,228\nRa 128,2,49152\nAR 99,47\nCV 111,98,99\ns_add_u32 s5, s66, 4096\n"
"Sv 108,1,16,5,1024\nMvaa 52,148,236\ns_add_u32 s46, s86, s88\nEX 80,44\nAR 98,48\nMvaa 52,152,244\nRa 132,2,50176\nv_xor_b32 v24, 0x10000, v24\nAR 99,49\nCV 100,98,99\nAR 98,50\n"
"Mvaa 52,156,252\nWL 6\nEX 81,45\nAR 99,51\nCV 101,98,99\nMvaa 48,156,248\nRa 136,2,51200\nAD 85,85,80\nAR 98,56\nAR 99,57\nCV 102,98,99\nMvaa 48,152,240\nD1 1,16,46,0\nAR 98,58\n"
"AR 99,59\nCV 103,98,99\nMvaa 48,148,232\nRa 140,2,52224\ns_add_u32 s5, s66, 4096\nSv 100,1,16,5,2048\nAR 98,52\nAR 99,53\nMvaa 48,144,224\nAD 85,85,81\nCV 70,80,81\nCV 104,98,99\n"
"AR 98,54\nAR 99,55\nCvaa 56,160,192,12\nRa 144,2,53248\nCV 105,98,99\nAR 98,60\nAR 99,61\nCV 106,98,99\nMvaa 56,164,200\nEX 82,46\nAR 98,62\nAR 99,63\nMvaa 56,168,208\n"
"Ra 148,2,54272\nv_xor_b32 v26, 0x10000, v26\nCV 107,98,99\ns_add_u32 s5, s66, 4096\nSv 104,1,16,5,3072\nMvaa 56,172,216\nWL 6\nEX 83,47\nAR 98,64\nAR 99,65\nCvaa 60,172,220,16\n"
"Ra 152,2,55296\nAD 85,85,82\nCV 108,98,99\nAR 98,66\nAR 99,67\nMvaa 60,168,212\nD1 1,16,46,1024\nCV 109,98,99\nAR 98,72\nAR 99,73\nCV 110,98,99\nMvaa 60,164,204\nRa 156,2,56320\n"
"AR 98,74\nAR 99,75\nCV 111,98,99\nMvaa 60,160,196\nAD 85,85,83\nCV 71,82,83\ns_add_u32 s5, s66, 16384\nSv 108,1,16,5,0\nAR 98,68\nMvaa 60,176,228\nRa 160,2,57344\n"
"v_max_f32 v90, v84, v85\nAR 99,69\nCV 100,98,99\nAR 98,70\nMvaa 60,180,236\ns_add_u32 s88, s88, 32768\nEX 80,48\nAR 99,71\nMvaa 60,184,244\nRa 164,2,58368\n"
"v_cmp_lt_f32 vcc, s63, v90\nMvaa 60,188,252\nWL 8\nEX 81,49\nMvaa 56,188,248\nRa 168,2,59392\ns_cbranch_vccnz .T440\n.T441:\nv_mov_b32 v20, v84\nv_mov_b32 v21, v85\n"
"v_mov_b32 v22, v20\nv_mov_b32 v23, v21\nMvaa 56,184,240\nD1 1,16,46,2048\nv_permlane32_swap_b32 v20, v22\nv_permlane32_swap_b32 v21, v23\nAD 20,20,22\nAD 21,21,23\n"
"v_mov_b32 v22, v20\nv_mov_b32 v23, v21\nMvaa 56,180,232\nRa 172,2,60416\nv_permlane16_swap_b32 v20, v22\nv_permlane16_swap_b32 v21, v23\nAD 20,20,22\nAD 21,21,23\n"
"v_min_f32 v90, v20, v21\nv_cmp_gt_f32 vcc, s64, v90\ns_nop 0\ns_cbranch_vccnz .T442\n.T443:\nAD 86,86,84\nMvaa 56,176,224\nAD 84,80,81\nCV 72,80,81\nCV 101,98,99\nAR 98,76\n"
"AR 99,77\nZvav 128,128,64\nRa 176,2,61440\nCV 102,98,99\nAR 98,78\nAR 99,79\nCV 103,98,99\nZvav 132,128,68\nEX 82,50\ns_add_u32 s5, s66, 16384\nSv 100,1,16,5,1024\n"
"Zvav 140,132,68\nRa 180,2,62464\nAR 98,80\nAR 99,81\nCV 104,98,99\nAR 98,82\nZvav 136,132,64\ns_waitcnt vmcnt(21) lgkmcnt(8)\nEX 83,51\nAR 99,83\nCV 105,98,99\nZvav 144,136,64\n"
"Ra 184,2,63488\nAD 84,84,82\nAR 98,88\nAR 99,89\nCV 106,98,99\nZvav 148,136,68\nD1 1,16,46,3072\ns_add_u32 m0, s89, 16384\nAR 98,90\nAR 99,91\nCV 107,98,99\nZvav 156,140,68\n"
"Ra 188,2,64512\ns_barrier\ns_add_u32 s5, s66, 16384\nSv 104,1,16,5,2048\nAR 98,84\nZvav 152,140,64\nAD 84,84,83\nCV 73,82,83\nAR 99,85\nCV 108,98,99\nAR 98,86\nZvav 160,144,64\n"
"Ra 128,24,0\nAR 99,87\nCV 109,98,99\nAR 98,92\nAR 99,93\nZvav 164,144,68\ns_add_u32 s46, s87, s88\nEX 80,52\nCV 110,98,99\nZvav 172,148,68\nRa 132,24,1024\n"
"v_xor_b32 v2, 0x10000, v2\nAR 98,94\nAR 99,95\nCV 111,98,99\nZvav 168,148,64\nWL 8\nEX 81,53\ns_add_u32 s5, s66, 16384\nSv 108,1,16,5,3072\nZvav 176,152,64\nRa 136,24,2048\n"
"AD 87,87,85\nAR 98,96\nAR 99,97\nCV 100,98,99\nZvav 180,152,68\nD1 1,16,46,0\nAR 98,98\nAR 99,99\nCV 101,98,99\nAR 98,104\nZvav 188,156,68\nRa 140,24,3072\nAR 99,105\n"
"CV 102,98,99\nAR 98,106\nAR 99,107\nZvav 184,156,64\nAD 85,80,81\nCV 76,80,81\nCV 103,98,99\ns_add_u32 s5, s66, 20480\nSv 100,1,16,5,0\nZvav 192,160,64\nRa 144,26,0\nAR 98,100\n"
"AR 99,101\nCV 104,98,99\nAR 98,102\nZvav 196,160,68\ns_cmp_lt_u32 s99, s92\nEX 82,54\nZvav 204,164,68\nRa 148,26,1024\ns_cbranch_scc1 .E434\n.E435:\nAR 99,103\nCV 105,98,99\n"
"AR 98,108\nZvav 200,164,64\nWL 8\nEX 83,55\nAR 99,109\nCV 106,98,99\nZvav 208,168,64\nRa 152,26,2048\nAD 85,85,82\nAR 98,110\nAR 99,111\nCV 107,98,99\nZvav 212,168,68\n"
"D1 1,16,46,1024\ns_add_u32 s5, s66, 20480\nSv 104,1,16,5,1024\nAR 98,112\nAR 99,113\nZvav 220,172,68\nRa 156,26,3072\nCV 108,98,99\nAR 98,114\nAR 99,115\nCV 109,98,99\n"
"Zvav 216,172,64\nAD 85,85,83\nCV 77,82,83\nAR 98,120\nAR 99,121\nCV 110,98,99\nZvav 224,176,64\nRa 160,24,8192\nAR 98,122\nAR 99,123\nCV 111,98,99\nZvav 228,176,68\n"
"s_sub_u32 s90, s90, 1\nEX 80,56\nZvav 236,180,68\nRa 164,24,9216\ns_cbranch_scc1 .E436\n.E437:\ns_add_u32 s5, s66, 20480\nSv 108,1,16,5,2048\nAR 98,116\nZvav 232,180,64\nWL 6\n"
"EX 81,57\nAR 99,117\nCV 100,98,99\nZvav 240,184,64\nRa 168,24,10240\nAD 84,84,80\nAR 98,118\nAR 99,119\nCV 101,98,99\nZvav 244,184,68\nD1 1,16,46,2048\nAR 98,124\nAR 99,125\n"
"CV 102,98,99\nAR 98,126\nZvav 252,188,68\nRa 172,24,11264\nAR 99,127\nCV 103,98,99\ns_add_u32 s5, s66, 20480\nSv 100,1,16,5,3072\nZvav 248,188,64\nAD 84,84,81\nCV 74,80,81\n"
"s_sub_u32 s99, s99, 1\ns_cbranch_scc1 .LE385\n.p2align 6\n.A444:\nCvaa 32,128,192,4\nRa 176,26,8192\nMvaa 32,132,200\nEX 82,58\nMvaa 32,136,208\nRa 180,26,9216\nMvaa 32,140,216\n"
"WL 6\nEX 83,59\nCvaa 36,140,220,8\nRa 184,26,10240\nAD 84,84,82\nMvaa 36,136,212\nD1 1,16,46,3072\ns_add_u32 m0, s89, 32768\nMvaa 36,132,204\nRa 188,26,11264\nMvaa 36,128,196\n"
"AD 84,84,83\nCV 75,82,83\nMvaa 36,144,228\nRa 128,2,16384\nMvaa 36,148,236\ns_add_u32 s46, s86, s88\nEX 80,60\nMvaa 36,152,244\nRa 132,2,17408\nMvaa 36,156,252\nWL 6\nEX 81,61\n"
"Mvaa 32,156,248\nRa 136,2,18432\nAD 85,85,80\nMvaa 32,152,240\nD1 1,16,46,0\nMvaa 32,148,232\nRa 140,2,19456\nSv 112,1,16,49,0\nMvaa 32,144,224\nAD 85,85,81\nCV 78,80,81\n"
"Cvaa 40,160,192,12\nRa 144,2,20480\nMvaa 40,164,200\nEX 82,62\nMvaa 40,168,208\nRa 148,2,21504\nMvaa 40,172,216\nWL 6\nEX 83,63\nCvaa 44,172,220,16\nRa 152,2,22528\nAD 85,85,82\n"
"Mvaa 44,168,212\nD1 1,16,46,1024\nMvaa 44,164,204\nRa 156,2,23552\nMvaa 44,160,196\nAD 85,85,83\nCV 79,82,83\nMvaa 44,176,228\nRa 160,2,24576\nv_max_f32 v90, v84, v85\n"
"Mvaa 44,180,236\ns_add_u32 s88, s88, 32768\nEX 80,32\nMvaa 44,184,244\nRa 164,2,25600\nv_cmp_lt_f32 vcc, s63, v90\nMvaa 44,188,252\nWL 8\nEX 81,33\nMvaa 40,188,248\n"
"Ra 168,2,26624\ns_cbranch_vccnz .T454\n.T455:\nMvaa 40,184,240\nD1 1,16,46,2048\nMvaa 40,180,232\nRa 172,2,27648\nAD 86,86,84\nMvaa 40,176,224\nAD 84,80,81\nCV 64,80,81\n"
"Mvav 128,128,72\nRa 176,2,28672\nMvav 132,128,76\ns_cmp_eq_u32 s99, s98\nEX 82,34\nMvav 140,132,76\nRa 180,2,29696\ns_cbranch_scc1 .E452\n.E453:\nMvav 136,132,72\n"
"s_waitcnt vmcnt(13) lgkmcnt(8)\nEX 83,35\nMvav 144,136,72\nRa 184,2,30720\nAD 84,84,82\nMvav 148,136,76\nD1 1,16,46,3072\ns_add_u32 m0, s89, 49152\nMvav 156,140,76\n"
"Ra 188,2,31744\ns_barrier\nMvav 152,140,72\nAD 84,84,83\nCV 65,82,83\nMvav 160,144,72\nRa 128,24,32768\nMvav 164,144,76\ns_add_u32 s46, s87, s88\nEX 80,36\nMvav 172,148,76\n"
"Ra 132,24,33792\nMvav 168,148,72\nWL 8\nEX 81,37\nMvav 176,152,72\nRa 136,24,34816\nAD 87,87,85\nMvav 180,152,76\nD1 1,16,46,0\nMvav 188,156,76\nRa 140,24,35840\nMvav 184,156,72\n"
"AD 85,80,81\nCV 68,80,81\nMvav 192,160,72\nRa 144,26,32768\nMvav 196,160,76\ns_cmp_eq_u32 s99, 0\nEX 82,38\nMvav 204,164,76\nRa 148,26,33792\ns_cbranch_scc1 .E446\n.E447:\n"
"Mvav 200,164,72\nWL 8\nEX 83,39\nMvav 208,168,72\nRa 152,26,34816\nAD 85,85,82\nMvav 212,168,76\nD1 1,16,46,1024\nMvav 220,172,76\nRa 156,26,35840\nMvav 216,172,72\nAD 85,85,83\n"
"CV 69,82,83\nMvav 224,176,72\nRa 160,24,40960\nMvav 228,176,76\ns_xor_b32 s89, s89, 0x10000\nEX 80,40\nMvav 236,180,76\nRa 164,24,41984\nMvav 232,180,72\nWL 6\nEX 81,41\n"
"Mvav 240,184,72\nRa 168,24,43008\nAD 84,84,80\nMvav 244,184,76\nD1 1,16,46,2048\nMvav 252,188,76\nRa 172,24,44032\nMvav 248,188,72\nAD 84,84,81\nCV 66,80,81\nCvaa 48,128,192,4\n"
"Ra 176,26,40960\nMvaa 48,132,200\ns_cmp_eq_u32 s99, 0\nEX 82,42\nMvaa 48,136,208\nRa 180,26,41984\ns_cselect_b32 s86, s94, s86\nMvaa 48,140,216\nWL 6\nEX 83,43\nCvaa 52,140,220,8\n"
"Ra 184,26,43008\nAD 84,84,82\nMvaa 52,136,212\nD1 1,16,46,3072\ns_add_u32 m0, s89, 0\nMvaa 52,132,204\nRa 188,26,44032\nMvaa 52,128,196\nAD 84,84,83\nCV 67,82,83\nMvaa 52,144,228\n"
"Ra 128,2,49152\nMvaa 52,148,236\ns_add_u32 s46, s86, s88\nEX 80,44\nMvaa 52,152,244\nRa 132,2,50176\nv_xor_b32 v24, 0x10000, v24\nMvaa 52,156,252\nWL 6\nEX 81,45\nMvaa 48,156,248\n"
"Ra 136,2,51200\nAD 85,85,80\nMvaa 48,152,240\nD1 1,16,46,0\nMvaa 48,148,232\nRa 140,2,52224\nSv 112,1,16,49,1024\nMvaa 48,144,224\nAD 85,85,81\nCV 70,80,81\nCvaa 56,160,192,12\n"
"Ra 144,2,53248\nMvaa 56,164,200\ns_add_u32 s49, s49, 131072\nEX 82,46\nMvaa 56,168,208\nRa 148,2,54272\nv_xor_b32 v26, 0x10000, v26\nMvaa 56,172,216\nWL 6\nEX 83,47\n"
"Cvaa 60,172,220,16\nRa 152,2,55296\nAD 85,85,82\nMvaa 60,168,212\nD1 1,16,46,1024\nMvaa 60,164,204\nRa 156,2,56320\nMvaa 60,160,196\nAD 85,85,83\nCV 71,82,83\nMvaa 60,176,228\n"
"Ra 160,2,57344\nv_max_f32 v90, v84, v85\nMvaa 60,180,236\ns_add_u32 s88, s88, 32768\nEX 80,48\nMvaa 60,184,244\nRa 164,2,58368\nv_cmp_lt_f32 vcc, s63, v90\nMvaa 60,188,252\nWL 8\n"
"EX 81,49\nMvaa 56,188,248\nRa 168,2,59392\ns_cbranch_vccnz .T456\n.T457:\nMvaa 56,184,240\nD1 1,16,46,2048\nMvaa 56,180,232\nRa 172,2,60416\nAD 86,86,84\nMvaa 56,176,224\n"
"AD 84,80,81\nCV 72,80,81\nMvav 128,128,64\nRa 176,2,61440\nMvav 132,128,68\nEX 82,50\nMvav 140,132,68\nRa 180,2,62464\nMvav 136,132,64\ns_waitcnt vmcnt(13) lgkmcnt(8)\nEX 83,51\n"
"Mvav 144,136,64\nRa 184,2,63488\nAD 84,84,82\nMvav 148,136,68\nD1 1,16,46,3072\ns_add_u32 m0, s89, 16384\nMvav 156,140,68\nRa 188,2,64512\ns_barrier\nMvav 152,140,64\nAD 84,84,83\n"
"CV 73,82,83\nMvav 160,144,64\nRa 128,24,0\nMvav 164,144,68\ns_add_u32 s46, s87, s88\nEX 80,52\nMvav 172,148,68\nRa 132,24,1024\nv_xor_b32 v2, 0x10000, v2\nMvav 168,148,64\nWL 8\n"
"EX 81,53\nMvav 176,152,64\nRa 136,24,2048\nAD 87,87,85\nMvav 180,152,68\nD1 1,16,46,0\nMvav 188,156,68\nRa 140,24,3072\nMvav 184,156,64\nAD 85,80,81\nCV 76,80,81\nMvav 192,160,64\n"
"Ra 144,26,0\nMvav 196,160,68\ns_cmp_lt_u32 s99, s92\nEX 82,54\nMvav 204,164,68\nRa 148,26,1024\ns_cbranch_scc1 .E448\n.E449:\nMvav 200,164,64\nWL 8\nEX 83,55\nMvav 208,168,64\n"
"Ra 152,26,2048\nAD 85,85,82\nMvav 212,168,68\nD1 1,16,46,1024\nMvav 220,172,68\nRa 156,26,3072\nMvav 216,172,64\nAD 85,85,83\nCV 77,82,83\nMvav 224,176,64\nRa 160,24,8192\n"
"Mvav 228,176,68\ns_sub_u32 s90, s90, 1\nEX 80,56\nMvav 236,180,68\nRa 164,24,9216\ns_cbranch_scc1 .E450\n.E451:\nMvav 232,180,64\nWL 6\nEX 81,57\nMvav 240,184,64\nRa 168,24,10240\n"
"AD 84,84,80\nMvav 244,184,68\nD1 1,16,46,2048\ns_sub_u32 s99, s99, 1\nMvav 252,188,68\nRa 172,24,11264\nMvav 248,188,64\nAD 84,84,81\nCV 74,80,81\ns_cbranch_scc0 .A444\n.LE385:\n"
"Ra 176,26,8192\nEX 82,58\nRa 180,26,9216\nWL 6\nEX 83,59\nCvaa 36,140,220,8\nRa 184,26,10240\nAD 84,84,82\nMvaa 36,136,212\nD1 1,16,46,3072\ns_add_u32 m0, s89, 32768\n"
"Mvaa 36,132,204\nRa 188,26,11264\nMvaa 36,128,196\nAD 84,84,83\nCV 75,82,83\nMvaa 36,144,228\nRa 128,2,16384\nMvaa 36,148,236\ns_add_u32 s46, s86, s88\nEX 80,60\nMvaa 36,152,244\n"
"Ra 132,2,17408\nMvaa 36,156,252\nEX 81,61\nRa 136,2,18432\nAD 85,85,80\nD1 1,16,46,0\nRa 140,2,19456\nSv 112,1,16,49,0\nAD 85,85,81\nCV 78,80,81\nRa 144,2,20480\n"
"s_mov_b32 s87, s95\nEX 82,62\nRa 148,2,21504\nWL 6\nEX 83,63\nCvaa 44,172,220,16\nRa 152,2,22528\nAD 85,85,82\nMvaa 44,168,212\nD1 1,16,46,1024\nMvaa 44,164,204\nRa 156,2,23552\n"
"Mvaa 44,160,196\nAD 85,85,83\nCV 79,82,83\nMvaa 44,176,228\nRa 160,2,24576\nv_max_f32 v90, v84, v85\nMvaa 44,180,236\ns_add_u32 s88, s88, 32768\nMvaa 44,184,244\nRa 164,2,25600\n"
"v_cmp_lt_f32 vcc, s63, v90\nMvaa 44,188,252\nWL 8\nRa 168,2,26624\ns_cbranch_vccnz .T461\n.T462:\nD1 1,16,46,2048\nRa 172,2,27648\nAD 86,86,84\nMvav 128,128,72\nRa 176,2,28672\n"
"Mvav 132,128,76\ns_cmp_eq_u32 s99, s98\nMvav 140,132,76\nRa 180,2,29696\ns_cbranch_scc1 .E459\n.E460:\nMvav 136,132,72\ns_waitcnt vmcnt(13) lgkmcnt(8)\nMvav 144,136,72\n"
"Ra 184,2,30720\nMvav 148,136,76\nD1 1,16,46,3072\ns_add_u32 m0, s89, 49152\nMvav 156,140,76\nRa 188,2,31744\ns_barrier\nMvav 152,140,72\nMvav 160,144,72\nRa 128,24,32768\n"
"Mvav 164,144,76\ns_add_u32 s46, s87, s88\nEX 80,36\nMvav 172,148,76\nRa 132,24,33792\nMvav 168,148,72\nWL 8\nEX 81,37\nMvav 176,152,72\nRa 136,24,34816\nAD 87,87,85\n"
"Mvav 180,152,76\nD1 1,16,46,0\nMvav 188,156,76\nRa 140,24,35840\nMvav 184,156,72\nAD 85,80,81\nCV 68,80,81\nMvav 192,160,72\nRa 144,26,32768\nMvav 196,160,76\ns_nop 0\nEX 82,38\n"
"Mvav 204,164,76\nRa 148,26,33792\nv_cmpx_gt_i32 vcc, 19, v0\nv_mov_b32 v19, s97\nv_cmpx_gt_i32 vcc, 18, v0\nv_mov_b32 v18, s97\nv_cmpx_gt_i32 vcc, 17, v0\nv_mov_b32 v17, s97\n"
"v_cmpx_gt_i32 vcc, 16, v0\nv_mov_b32 v16, s97\nv_cmpx_gt_i32 vcc, 3, v0\nv_mov_b32 v11, s97\nv_cmpx_gt_i32 vcc, 2, v0\nv_mov_b32 v10, s97\nv_cmpx_gt_i32 vcc, 1, v0\n"
"v_mov_b32 v9, s97\nv_cmpx_gt_i32 vcc, 0, v0\nv_mov_b32 v8, s97\ns_mov_b64 exec, -1\ns_nop 2\ns_nop 0\nMvav 200,164,72\nWL 8\nEX 83,39\nMvav 208,168,72\nRa 152,26,34816\n"
"AD 85,85,82\nMvav 212,168,76\nD1 1,16,46,1024\nMvav 220,172,76\nRa 156,26,35840\nMvav 216,172,72\nAD 85,85,83\nCV 69,82,83\nMvav 224,176,72\nRa 160,24,40960\nMvav 228,176,76\n"
"s_xor_b32 s89, s89, 0x10000\nMvav 236,180,76\nRa 164,24,41984\nMvav 232,180,72\nWL 10\nMvav 240,184,72\nRa 168,24,43008\nMvav 244,184,76\nD1 1,16,46,2048\nMvav 252,188,76\n"
"Ra 172,24,44032\nMvav 248,188,72\nRa 176,26,40960\nRa 180,26,41984\nWL 6\nCvaa 52,140,220,8\nRa 184,26,43008\nMvaa 52,136,212\nD1 1,16,46,3072\ns_add_u32 m0, s89, 0\n"
"Mvaa 52,132,204\nRa 188,26,44032\nMvaa 52,128,196\nMvaa 52,144,228\nRa 128,2,49152\nMvaa 52,148,236\ns_add_u32 s46, s86, s88\nEX 80,44\nMvaa 52,152,244\nRa 132,2,50176\n"
"v_xor_b32 v24, 0x10000, v24\nMvaa 52,156,252\nEX 81,45\nRa 136,2,51200\nAD 85,85,80\nD1 1,16,46,0\nRa 140,2,52224\nSv 112,1,16,49,1024\nAD 85,85,81\nCV 70,80,81\nRa 144,2,53248\n"
"s_add_u32 s49, s49, 131072\nEX 82,46\nRa 148,2,54272\nv_xor_b32 v26, 0x10000, v26\nWL 6\nEX 83,47\nCvaa 60,172,220,16\nRa 152,2,55296\nAD 85,85,82\nMvaa 60,168,212\n"
"D1 1,16,46,1024\nMvaa 60,164,204\nRa 156,2,56320\nMvaa 60,160,196\nAD 85,85,83\nCV 71,82,83\nMvaa 60,176,228\nRa 160,2,57344\nMvaa 60,180,236\ns_add_u32 s88, s88, 32768\n"
"Mvaa 60,184,244\nRa 164,2,58368\nv_cmp_lt_f32 vcc, s63, v85\nMvaa 60,188,252\nWL 8\nRa 168,2,59392\ns_cbranch_vccnz .T463\n.T464:\nD1 1,16,46,2048\nRa 172,2,60416\nRa 176,2,61440\n"
"Mvav 132,128,68\nMvav 140,132,68\nRa 180,2,62464\ns_waitcnt vmcnt(13) lgkmcnt(8)\nRa 184,2,63488\nMvav 148,136,68\nD1 1,16,46,3072\ns_add_u32 m0, s89, 16384\nMvav 156,140,68\n"
"Ra 188,2,64512\ns_barrier\nRa 128,24,0\nMvav 164,144,68\ns_add_u32 s46, s87, s88\nEX 80,52\nMvav 172,148,68\nRa 132,24,1024\nv_xor_b32 v2, 0x10000, v2\nWL 8\nEX 81,53\n"
"Ra 136,24,2048\nAD 87,87,85\nMvav 180,152,68\nD1 1,16,46,0\nMvav 188,156,68\nRa 140,24,3072\nAD 85,80,81\nCV 76,80,81\nRa 144,26,0\nMvav 196,160,68\nEX 82,54\nMvav 204,164,68\n"
"Ra 148,26,1024\nWL 8\nEX 83,55\nRa 152,26,2048\nAD 85,85,82\nMvav 212,168,68\nD1 1,16,46,1024\nMvav 220,172,68\nRa 156,26,3072\nAD 85,85,83\nCV 77,82,83\nRa 160,24,8192\n"
"Mvav 228,176,68\nMvav 236,180,68\nRa 164,24,9216\nWL 10\nRa 168,24,10240\nMvav 244,184,68\nD1 1,16,46,2048\nMvav 252,188,68\nRa 172,24,11264\nD1 1,16,46,3072\n"
"s_add_u32 m0, s89, 32768\ns_add_u32 s46, s86, s88\nRa 128,2,16384\nv_xor_b32 v3, 0x10000, v2\nEX 80,60\nRa 132,2,17408\nv_add_u32 v0, 0x20000, v1\nEX 81,61\nRa 136,2,18432\n"
"AD 85,85,80\nD1 1,16,46,0\ns_sub_u32 s94, s49, 1966080\nRa 140,2,19456\ns_add_u32 s95, s94, 1048576\nAD 85,85,81\nCV 78,80,81\nRa 144,2,20480\ns_xor_b32 s92, s94, 0x202000\n"
"EX 82,62\nRa 148,2,21504\ns_add_u32 s93, s96, 983040\nEX 83,63\nRa 152,2,22528\nAD 85,85,82\nD1 1,16,46,1024\ns_xor_b32 s93, s93, 0x202000\nRa 156,2,23552\n"
"s_lshl_b32 s89, s11, 12\nAD 85,85,83\nCV 79,82,83\nRa 160,2,24576\nv_cmp_lt_f32 vcc, s63, v85\nRa 164,2,25600\ns_cbranch_vccnz .T465\n.T466:\nAD 87,87,85\nWL 8\n"
"v_mov_b32 v84, v86\nv_mov_b32 v85, v87\nRa 168,2,26624\nv_permlane32_swap_b32 v86, v84\nv_permlane32_swap_b32 v87, v85\nD1 1,16,46,2048\nAD 86,86,84\nRa 172,2,27648\nAD 87,87,85\n"
"v_mov_b32 v84, v86\nv_mov_b32 v85, v87\nRa 176,2,28672\nv_permlane16_swap_b32 v86, v84\nv_permlane16_swap_b32 v87, v85\nMvav 132,128,76\nAD 86,86,84\nAD 87,87,85\nMvav 140,132,76\n"
"Ra 180,2,29696\nv_rcp_f32 v86, v86\nWL 8\nv_rcp_f32 v87, v87\nRa 184,2,30720\nMvav 148,136,76\nD1 1,16,46,3072\nMvav 156,140,76\nRa 188,2,31744\nMU 128,128,86\nMU 129,129,86\n"
"CV 96,128,129\nMU 130,130,86\nMU 131,131,86\nRa 128,2,0\nCV 97,130,131\nMU 132,132,87\nMvav 164,144,76\nMU 133,133,87\nCV 100,132,133\nMU 134,134,87\nMvav 172,148,76\n"
"Ra 132,2,1024\nMU 135,135,87\nCV 101,134,135\nWL 8\ns_add_u32 m0, s89, 131072\ns_add_u32 s46, s93, 0\nRa 136,2,2048\nMU 136,136,86\nMU 137,137,86\nMvav 180,152,76\nD1 1,16,46,0\n"
"CV 98,136,137\nMU 138,138,86\nMvav 188,156,76\nRa 140,2,3072\nMU 139,139,86\nCV 99,138,139\ns_add_u32 s5, s94, 0\nSv 96,1,16,5,0\nMU 140,140,87\nRa 144,2,4096\nMU 141,141,87\n"
"CV 102,140,141\nMvav 196,160,76\nMU 142,142,87\nMU 143,143,87\nCV 103,142,143\nMvav 204,164,76\nRa 148,2,5120\nSv 100,1,16,5,1024\nMU 144,144,86\nWL 8\nMU 145,145,86\n"
"CV 104,144,145\nRa 152,2,6144\nMU 146,146,86\nMU 147,147,86\nMvav 212,168,76\nD1 1,16,46,1024\nCV 105,146,147\nMU 148,148,87\nMvav 220,172,76\nRa 156,2,7168\nMU 149,149,87\n"
"CV 108,148,149\nMU 150,150,87\nMU 151,151,87\nCV 109,150,151\nRa 160,2,8192\nMU 152,152,86\nMU 153,153,86\nMvav 228,176,76\nCV 106,152,153\nMU 154,154,86\nMU 155,155,86\n"
"Mvav 236,180,76\nRa 164,2,9216\nCV 107,154,155\ns_add_u32 s5, s94, 131072\nWL 8\nSv 104,1,16,5,0\nMU 156,156,87\nRa 168,2,10240\nMU 157,157,87\nCV 110,156,157\nMvav 244,184,76\n"
"D1 1,16,46,2048\nMU 158,158,87\nMU 159,159,87\nMvav 252,188,76\nRa 172,2,11264\nCV 111,158,159\nSv 108,1,16,5,1024\nMU 160,160,86\nMU 161,161,86\nZaav 0,128,96\nRa 176,2,12288\n"
"CV 112,160,161\nMU 162,162,86\nMaav 0,132,104\nMU 163,163,86\nCV 113,162,163\nZaav 4,132,108\nRa 180,2,13312\nMU 164,164,87\nMaav 4,128,100\ns_waitcnt vmcnt(18) lgkmcnt(8)\n"
"MU 165,165,87\nZaav 12,136,100\nRa 184,2,14336\nCV 116,164,165\nMaav 12,140,108\nD1 1,16,46,3072\nMU 166,166,87\nZaav 8,140,104\nRa 188,2,15360\ns_barrier\nMaav 8,136,96\n"
"MU 167,167,87\nCV 117,166,167\nZaav 16,144,96\nRa 128,2,32768\nMU 168,168,86\nMU 169,169,86\nv_lshrrev_b32 v88, 8, v1\nMaav 16,148,104\nCV 114,168,169\nMU 170,170,86\n"
"v_lshlrev_b32 v89, 5, v88\nbuffer_store_dword v25, off, s[16:19], s54 offset:512 sc0 sc1\nZaav 20,148,108\nRa 132,2,33792\nMU 171,171,86\n"
"buffer_load_dwordx4 v[64:67], v89, s[16:19], s40 offen offset:0 sc0 sc1\nMaav 20,144,100\nWL 8\ns_add_u32 m0, s89, 49152\ns_add_u32 s46, s96, 1179648\nZaav 28,152,100\n"
"Ra 136,2,34816\nCV 115,170,171\nMaav 28,156,108\nD1 1,16,46,0\ns_add_u32 s5, s94, 262144\nZaav 24,156,104\nRa 140,2,35840\nSv 112,1,16,5,0\n"
"buffer_load_dwordx4 v[68:71], v89, s[16:19], s40 offen offset:16 sc0 sc1\nMaav 24,152,96\nMU 172,172,87\nMU 173,173,87\nZaav 32,160,96\nRa 144,2,36864\nCV 118,172,173\n"
"MU 174,174,87\nMaav 32,164,104\nMU 175,175,87\nCV 119,174,175\nZaav 36,164,108\nRa 148,2,37888\nSv 116,1,16,5,1024\n"
"buffer_load_dwordx4 v[72:75], v89, s[16:19], s40 offen offset:1792 sc0 sc1\nMaav 36,160,100\nWL 8\nMU 176,176,86\nZaav 44,168,100\nRa 152,2,38912\nMU 177,177,86\nMaav 44,172,108\n"
"D1 1,16,46,1024\nCV 120,176,177\nZaav 40,172,104\nRa 156,2,39936\nMU 178,178,86\nbuffer_load_dwordx4 v[76:79], v89, s[16:19], s40 offen offset:1808 sc0 sc1\nMaav 40,168,96\n"
"MU 179,179,86\nCV 121,178,179\nZaav 48,176,96\nRa 160,2,40960\nMU 180,180,87\nMU 181,181,87\nMaav 48,180,104\nCV 124,180,181\nMU 182,182,87\nZaav 52,180,108\nRa 164,2,41984\n"
"MU 183,183,87\nv_bfe_u32 v90, v1, 4, 4\nMaav 52,176,100\nWL 8\nCV 125,182,183\nv_mul_u32_u24 v91, 896, v90\nZaav 60,184,100\nRa 168,2,43008\nMU 184,184,86\n"
"v_lshl_add_u32 v91, v88, 4, v91\nMaav 60,188,108\nD1 1,16,46,2048\nMU 185,185,86\nbuffer_load_dwordx4 v[12:15], v91, s[28:31], s22 offen offset:0\nZaav 56,188,104\nRa 172,2,44032\n"
"CV 122,184,185\nMaav 56,184,96\nMU 186,186,86\nMU 187,187,86\nZaav 64,128,96\nRa 176,2,45056\nCV 123,186,187\ns_add_u32 s5, s94, 393216\nMaav 64,132,104\nSv 120,1,16,5,0\n"
"MU 188,188,87\nbuffer_load_dwordx4 v[16:19], v91, s[28:31], s23 offen offset:0\nZaav 68,132,108\nRa 180,2,46080\nMU 189,189,87\nv_add_u32 v90, s21, v90\nMaav 68,128,100\nWL 8\n"
"CV 126,188,189\nv_lshlrev_b32 v88, 2, v90\nZaav 76,136,100\nRa 184,2,47104\nMU 190,190,87\nv_lshrrev_b32 v92, 8, v1\nMaav 76,140,108\nD1 1,16,46,3072\nMU 191,191,87\n"
"v_bfe_u32 v93, v1, 6, 2\nZaav 72,140,104\nRa 188,2,48128\nCV 127,190,191\nMaav 72,136,96\nSv 124,1,16,5,1024\nv_cmp_eq_u32 vcc, v92, v93\nv_and_b32 v92, 16, v1\n"
"v_mov_b32 v93, 0x3f80\nv_cndmask_b32 v93, 0, v93, vcc\nZaav 80,144,96\nRa 128,2,49152\nMU 192,192,86\nv_lshlrev_b32 v93, v92, v93\nv_and_b32 v92, 32, v1\nMaav 80,148,104\n"
"MU 193,193,86\nCV 32,192,193\nZaav 84,148,108\nRa 132,2,50176\nMU 194,194,86\nMaav 84,144,100\nWL 8\ns_add_u32 m0, s89, 147456\ns_add_u32 s46, s93, 32768\nZaav 92,152,100\n"
"Ra 136,2,51200\nMU 195,195,86\nMaav 92,156,108\nD1 1,16,46,0\nCV 33,194,195\nZaav 88,156,104\nRa 140,2,52224\nMU 196,196,87\nMaav 88,152,96\nMU 197,197,87\nCV 36,196,197\n"
"Zaav 96,160,96\nRa 144,2,53248\nMU 198,198,87\nMaav 96,164,104\nMU 199,199,87\nv_cmp_eq_u32 vcc, 0, v92\nv_mov_b32 v4, 0\nv_mov_b32 v5, 0\nv_cndmask_b32 v6, 0, v93, vcc\n"
"Zaav 100,164,108\nRa 148,2,54272\nCV 37,198,199\nv_sub_u32 v7, v93, v6\nMaav 100,160,100\nWL 8\nMU 200,200,86\nv_mov_b32 v8, 0\nZaav 108,168,100\nRa 152,2,55296\nMU 201,201,86\n"
"v_mov_b32 v9, 0\nMaav 108,172,108\nD1 1,16,46,1024\nCV 34,200,201\nZaav 104,172,104\nRa 156,2,56320\nMU 202,202,86\nMaav 104,168,96\nMU 203,203,86\nCV 35,202,203\nMaav 0,176,112\n"
"Ra 160,2,57344\ns_add_u32 s5, s94, 524288\nMaav 0,180,120\nSv 32,1,16,5,0\nMU 204,204,87\nMaav 4,180,124\nRa 164,2,58368\nMU 205,205,87\nMaav 4,176,116\nWL 8\nCV 38,204,205\n"
"Maav 12,184,116\nRa 168,2,59392\nMU 206,206,87\nMaav 12,188,124\nD1 1,16,46,2048\nMU 207,207,87\nMaav 8,188,120\nRa 172,2,60416\nCV 39,206,207\nMaav 8,184,112\nSv 36,1,16,5,1024\n"
"MU 208,208,86\nMaav 16,128,112\nRa 176,2,61440\nMU 209,209,86\nMaav 16,132,120\nCV 40,208,209\nMaav 20,132,124\nRa 180,2,62464\nMU 210,210,86\nMaav 20,128,116\n"
"s_waitcnt vmcnt(32) lgkmcnt(8)\nMU 211,211,86\nMaav 28,136,116\nRa 184,2,63488\nCV 41,210,211\nMaav 28,140,124\nD1 1,16,46,3072\nMU 212,212,87\nMaav 24,140,120\nRa 188,2,64512\n"
"s_barrier\nMaav 24,136,112\nMU 213,213,87\nCV 44,212,213\nMaav 32,144,112\nRa 128,3,0\nMU 214,214,87\nMaav 32,148,120\nMU 215,215,87\nCV 45,214,215\nMaav 36,148,124\n"
"Ra 132,3,1024\nMU 216,216,86\nMaav 36,144,116\nWL 8\ns_add_u32 m0, s89, 81920\ns_add_u32 s46, s93, 65536\nMaav 44,152,116\nRa 136,3,2048\nMU 217,217,86\nMaav 44,156,124\n"
"D1 1,16,46,0\nCV 42,216,217\nMaav 40,156,120\nRa 140,3,3072\nMU 218,218,86\nMaav 40,152,112\nMU 219,219,86\nCV 43,218,219\nMaav 48,160,112\nRa 144,3,4096\n"
"s_add_u32 s5, s94, 655360\nMaav 48,164,120\nSv 40,1,16,5,0\nbuffer_load_dword v27, off, s[16:19], s60 offset:512 sc0 sc1\nMaav 52,164,124\nRa 148,3,5120\nMU 220,220,87\n"
"Maav 52,160,116\nWL 8\nMU 221,221,87\nMaav 60,168,116\nRa 152,3,6144\nCV 46,220,221\nMaav 60,172,124\nD1 1,16,46,1024\nMU 222,222,87\nMaav 56,172,120\nRa 156,3,7168\n"
"MU 223,223,87\nMaav 56,168,112\nCV 47,222,223\nSv 44,1,16,5,1024\nMaav 64,176,112\nRa 160,3,8192\nMU 224,224,86\nMaav 64,180,120\nMU 225,225,86\nCV 48,224,225\nMaav 68,180,124\n"
"Ra 164,3,9216\nMU 226,226,86\nMaav 68,176,116\nWL 8\nMU 227,227,86\nMaav 76,184,116\nRa 168,3,10240\nCV 49,226,227\nMaav 76,188,124\nD1 1,16,46,2048\nMU 228,228,87\n"
"Maav 72,188,120\nRa 172,3,11264\nMU 229,229,87\nMaav 72,184,112\nCV 52,228,229\nMU 230,230,87\nMaav 80,128,112\nRa 176,3,12288\nMU 231,231,87\nWV 14\nv_xor_b32 v92, s15, v66\n"
"v_xor_b32 v93, s15, v70\nv_xor_b32 v94, s15, v74\nv_or3_b32 v92, v92, v93, v94\nv_xor_b32 v93, s15, v78\nv_or_b32 v92, v92, v93\nMaav 80,132,120\nCV 53,230,231\n"
"v_cmp_ne_u32 vcc, 0, v92\ns_nop 0\ns_cbranch_vccnz .S473\n.S474:\ns_mov_b32 s2, 0xc040000\nbuffer_load_dword v80, v88, s[16:19], s2 offen sc0 sc1\nMaav 84,132,124\nRa 180,3,13312\n"
"MU 232,232,86\nMaav 84,128,116\nWL 8\nMU 233,233,86\nMaav 92,136,116\nRa 184,3,14336\nCV 50,232,233\nMaav 92,140,124\nD1 1,16,46,3072\nMU 234,234,86\n"
"buffer_load_dword v81, v88, s[16:19], s2 offen offset:256 sc0 sc1\nv_and_b32 v92, 0x7ff, v67\nv_lshrrev_b32 v93, 16, v67\nv_add_u32 v94, 64, v90\nMaav 88,140,120\nRa 188,3,15360\n"
"MU 235,235,86\nMaav 88,136,112\nCV 51,234,235\ns_add_u32 s5, s94, 786432\nMaav 96,144,112\nRa 128,3,16384\nSv 48,1,16,5,0\nMaav 96,148,120\nMU 236,236,87\nMU 237,237,87\n"
"Maav 100,148,124\nRa 132,3,17408\nCV 54,236,237\nMaav 100,144,116\nWL 8\ns_add_u32 m0, s89, 65536\ns_add_u32 s46, s93, 98304\nMaav 108,152,116\nRa 136,3,18432\nMU 238,238,87\n"
"Maav 108,156,124\nD1 1,16,46,0\nMU 239,239,87\nMaav 104,156,120\nRa 140,3,19456\nCV 55,238,239\nMaav 104,152,112\nSv 52,1,16,5,1024\nMU 240,240,86\nMaav 0,160,32\nRa 144,3,20480\n"
"MU 241,241,86\nv_cmp_eq_u32 vcc, v92, v90\ns_nop 1\nv_cndmask_b32 v12, v12, v64, vcc\nv_cndmask_b32 v13, v13, v65, vcc\nv_cndmask_b32 v14, v14, v68, vcc\n"
"v_cndmask_b32 v15, v15, v69, vcc\nMaav 0,164,40\nCV 56,240,241\nv_cmp_eq_u32 vcc, v93, v90\ns_nop 1\nv_cndmask_b32 v12, v12, v72, vcc\nv_cndmask_b32 v13, v13, v73, vcc\n"
"v_cndmask_b32 v14, v14, v76, vcc\nv_cndmask_b32 v15, v15, v77, vcc\nMaav 4,164,44\nRa 148,3,21504\nMU 242,242,86\nMaav 4,160,36\nWL 8\nMU 243,243,86\nMaav 12,168,36\n"
"Ra 152,3,22528\nCV 57,242,243\nMaav 12,172,44\nD1 1,16,46,1024\nMU 244,244,87\nMaav 8,172,40\nRa 156,3,23552\nMU 245,245,87\nMaav 8,168,32\nCV 60,244,245\nMU 246,246,87\n"
"Maav 16,176,32\nRa 160,3,24576\nMU 247,247,87\nMaav 16,180,40\nCV 61,246,247\nMU 248,248,86\nMaav 20,180,44\nRa 164,3,25600\nMU 249,249,86\nMaav 20,176,36\nWL 8\nCV 58,248,249\n"
"Maav 28,184,36\nRa 168,3,26624\nMU 250,250,86\nMaav 28,188,44\nD1 1,16,46,2048\nMU 251,251,86\nMaav 24,188,40\nRa 172,3,27648\nCV 59,250,251\nMaav 24,184,32\n"
"s_add_u32 s5, s94, 917504\nSv 56,1,16,5,0\nMaav 32,128,32\nRa 176,3,28672\nMU 252,252,87\nv_cmp_eq_u32 vcc, v92, v94\ns_nop 1\nv_cndmask_b32 v16, v16, v64, vcc\n"
"v_cndmask_b32 v17, v17, v65, vcc\nv_cndmask_b32 v18, v18, v68, vcc\nv_cndmask_b32 v19, v19, v69, vcc\nMaav 32,132,40\nMU 253,253,87\nv_cmp_eq_u32 vcc, v93, v94\ns_nop 1\n"
"v_cndmask_b32 v16, v16, v72, vcc\nv_cndmask_b32 v17, v17, v73, vcc\nv_cndmask_b32 v18, v18, v76, vcc\nv_cndmask_b32 v19, v19, v77, vcc\nMaav 36,132,44\nRa 180,3,29696\n"
"CV 62,252,253\nMaav 36,128,36\ns_waitcnt vmcnt(22) lgkmcnt(8)\nMU 254,254,87\nMaav 44,136,36\nRa 184,3,30720\nMU 255,255,87\nMaav 44,140,44\nD1 1,16,46,3072\nCV 63,254,255\n"
"Maav 40,140,40\nRa 188,3,31744\ns_barrier\nMaav 40,136,32\nSv 60,1,16,5,1024\nMaav 48,144,32\nRa 128,3,32768\nMaav 48,148,40\nMaav 52,148,44\nRa 132,3,33792\nMaav 52,144,36\nWL 8\n"
"s_add_u32 m0, s89, 98304\ns_add_u32 s46, s93, 131072\nMaav 60,152,36\nRa 136,3,34816\nMaav 60,156,44\nD1 1,16,46,0\nMaav 56,156,40\nRa 140,3,35840\nMaav 56,152,32\nMaav 64,160,32\n"
"Ra 144,3,36864\nWV 15\nv_cmp_ne_u32 vcc, s15, v27\ns_nop 0\ns_cbranch_vccnz .S476\n.S477:\nMaav 64,164,40\ns_add_u32 s4, s92, 0\nMaav 68,164,44\nRa 148,3,37888\nMaav 68,160,36\n"
"WL 8\nbuffer_load_dwordx4 a[192:195], v1, s[16:19], s4 offen offset:0 sc0 sc1\nMaav 76,168,36\nRa 152,3,38912\nMaav 76,172,44\nD1 1,16,46,1024\nMaav 72,172,40\nRa 156,3,39936\n"
"Maav 72,168,32\nbuffer_load_dwordx4 a[196:199], v1, s[16:19], s4 offen offset:1024 sc0 sc1\nMaav 80,176,32\nRa 160,3,40960\nMaav 80,180,40\ns_add_u32 s4, s92, 131072\n"
"Maav 84,180,44\nRa 164,3,41984\nMaav 84,176,36\nWL 8\nbuffer_load_dwordx4 a[200:203], v1, s[16:19], s4 offen offset:0 sc0 sc1\nMaav 92,184,36\nRa 168,3,43008\nMaav 92,188,44\n"
"D1 1,16,46,2048\nMaav 88,188,40\nRa 172,3,44032\nMaav 88,184,32\nbuffer_load_dwordx4 a[204:207], v1, s[16:19], s4 offen offset:1024 sc0 sc1\nMaav 96,128,32\nRa 176,3,45056\n"
"Maav 96,132,40\ns_add_u32 s4, s92, 262144\nMaav 100,132,44\nRa 180,3,46080\nMaav 100,128,36\nWL 8\nbuffer_load_dwordx4 a[208:211], v1, s[16:19], s4 offen offset:0 sc0 sc1\n"
"Maav 108,136,36\nRa 184,3,47104\nMaav 108,140,44\nD1 1,16,46,3072\nMaav 104,140,40\nRa 188,3,48128\nMaav 104,136,32\n"
"buffer_load_dwordx4 a[212:215], v1, s[16:19], s4 offen offset:1024 sc0 sc1\nMaav 0,144,48\nRa 128,3,49152\nMaav 0,148,56\ns_add_u32 s4, s92, 393216\nMaav 4,148,60\nRa 132,3,50176\n"
"Maav 4,144,52\nWL 8\ns_add_u32 m0, s89, 114688\ns_add_u32 s46, s93, 163840\nbuffer_load_dwordx4 a[216:219], v1, s[16:19], s4 offen offset:0 sc0 sc1\nMaav 12,152,52\n"
"Ra 136,3,51200\nMaav 12,156,60\nD1 1,16,46,0\nMaav 8,156,56\nRa 140,3,52224\nMaav 8,152,48\nbuffer_load_dwordx4 a[220:223], v1, s[16:19], s4 offen offset:1024 sc0 sc1\n"
"Maav 16,160,48\nRa 144,3,53248\nMaav 16,164,56\nMaav 20,164,60\nRa 148,3,54272\nMaav 20,160,52\nWL 8\nMaav 28,168,52\nRa 152,3,55296\nMaav 28,172,60\nD1 1,16,46,1024\n"
"Maav 24,172,56\nRa 156,3,56320\nMaav 24,168,48\nMaav 32,176,48\nRa 160,3,57344\nMaav 32,180,56\ns_add_u32 s4, s92, 524288\nMaav 36,180,60\nRa 164,3,58368\nMaav 36,176,52\nWL 8\n"
"buffer_load_dwordx4 a[224:227], v1, s[16:19], s4 offen offset:0 sc0 sc1\nMaav 44,184,52\nRa 168,3,59392\nMaav 44,188,60\nD1 1,16,46,2048\nMaav 40,188,56\nRa 172,3,60416\n"
"Maav 40,184,48\nbuffer_load_dwordx4 a[228:231], v1, s[16:19], s4 offen offset:1024 sc0 sc1\nMaav 48,128,48\nRa 176,3,61440\nMaav 48,132,56\ns_add_u32 s4, s92, 655360\n"
"Maav 52,132,60\nRa 180,3,62464\nMaav 52,128,52\nWL 8\nbuffer_load_dwordx4 a[232:235], v1, s[16:19], s4 offen offset:0 sc0 sc1\nMaav 60,136,52\nRa 184,3,63488\nMaav 60,140,60\n"
"D1 1,16,46,3072\nMaav 56,140,56\nRa 188,3,64512\ns_barrier\nMaav 56,136,48\nbuffer_load_dwordx4 a[236:239], v1, s[16:19], s4 offen offset:1024 sc0 sc1\nMaav 64,144,48\nRa 128,0,0\n"
"Maav 64,148,56\ns_add_u32 s4, s92, 786432\nMaav 68,148,60\nRa 132,0,1024\nMaav 68,144,52\nWL 8\ns_add_u32 m0, s89, 0\ns_add_u32 s46, s93, 196608\n"
"buffer_load_dwordx4 a[240:243], v1, s[16:19], s4 offen offset:0 sc0 sc1\nMaav 76,152,52\nRa 136,0,2048\nMaav 76,156,60\nD1 1,16,46,0\nMaav 72,156,56\nRa 140,0,3072\n"
"Maav 72,152,48\nbuffer_load_dwordx4 a[244:247], v1, s[16:19], s4 offen offset:1024 sc0 sc1\nMaav 80,160,48\nRa 144,0,4096\nMaav 80,164,56\ns_add_u32 s4, s92, 917504\n"
"Maav 84,164,60\nRa 148,0,5120\nMaav 84,160,52\nWL 8\nbuffer_load_dwordx4 a[248:251], v1, s[16:19], s4 offen offset:0 sc0 sc1\nMaav 92,168,52\nRa 152,0,6144\nMaav 92,172,60\n"
"D1 1,16,46,1024\nMaav 88,172,56\nRa 156,0,7168\nWV 12\nAR 22,195\nMaav 88,168,48\nAR 23,199\nv_max_u32 v21, v22, v23\n"
"buffer_load_dwordx4 a[252:255], v1, s[16:19], s4 offen offset:1024 sc0 sc1\nMaav 96,176,48\nRa 160,0,8192\nAR 22,203\nMaav 96,180,56\nAR 23,207\nv_max3_u32 v21, v21, v22, v23\n"
"Maav 100,180,60\nRa 164,0,9216\nAR 22,211\nMaav 100,176,52\nWL 6\nAR 23,215\nv_max3_u32 v21, v21, v22, v23\nMaav 108,184,52\nRa 168,0,10240\nAR 22,219\nMaav 108,188,60\n"
"D1 1,16,46,2048\nAR 23,223\nMaav 104,188,56\nRa 172,0,11264\nv_max3_u32 v21, v21, v22, v23\nMaav 104,184,48\nv_cmp_eq_u32 vcc, -1, v21\ns_nop 0\ns_cbranch_vccnz .S467\n.S468:\n"
"Maaa 0,128,192\nRa 176,0,12288\nMaaa 0,132,200\nMaaa 0,136,208\nRa 180,0,13312\nMaaa 0,140,216\nWL 6\nMaaa 4,140,220\nRa 184,0,14336\nMaaa 4,136,212\nD1 1,16,46,3072\n"
"Maaa 4,132,204\nRa 188,0,15360\nMaaa 4,128,196\nMaaa 8,144,192\nRa 128,0,16384\nMaaa 8,148,200\nMaaa 8,152,208\nRa 132,0,17408\nMaaa 8,156,216\nWL 6\nMaaa 12,156,220\n"
"Ra 136,0,18432\nMaaa 12,152,212\nMaaa 12,148,204\nRa 140,0,19456\nMaaa 12,144,196\nMaaa 16,160,192\nRa 144,0,20480\nMaaa 16,164,200\nMaaa 16,168,208\nRa 148,0,21504\n"
"Maaa 16,172,216\nWL 6\nMaaa 20,172,220\nRa 152,0,22528\nMaaa 20,168,212\nMaaa 20,164,204\nRa 156,0,23552\nWV 2\nMaaa 20,160,196\nAR 22,227\nAR 23,231\nMaaa 24,176,192\n"
"Ra 160,0,24576\nv_max_u32 v21, v22, v23\nAR 22,235\nMaaa 24,180,200\nAR 23,239\nv_max3_u32 v21, v21, v22, v23\nMaaa 24,184,208\nRa 164,0,25600\nAR 22,243\nMaaa 24,188,216\nWL 6\n"
"AR 23,247\nMaaa 28,188,220\nRa 168,0,26624\nv_max3_u32 v21, v21, v22, v23\nMaaa 28,184,212\nAR 22,251\nAR 23,255\nMaaa 28,180,204\nRa 172,0,27648\nv_max3_u32 v21, v21, v22, v23\n"
"Maaa 28,176,196\nv_cmp_eq_u32 vcc, -1, v21\ns_nop 0\ns_cbranch_vccnz .S470\n.S471:\nMaaa 0,128,224\nRa 176,0,28672\nMaaa 0,132,232\nMaaa 0,136,240\nRa 180,0,29696\nMaaa 0,140,248\n"
"WL 6\nMaaa 4,140,252\nRa 184,0,30720\nMaaa 4,136,244\nMaaa 4,132,236\nRa 188,0,31744\ns_barrier\nMaaa 4,128,228\nMaaa 8,144,224\nRa 128,2,16384\nMaaa 8,148,232\nMaaa 8,152,240\n"
"Ra 132,2,17408\nv_sub_f32 v92, 1.0, v80\nMaaa 8,156,248\nWL 6\nMaaa 12,156,252\nRa 136,2,18432\nMaaa 12,152,244\nv_sub_f32 v93, 1.0, v81\nCV 92,92,93\nMaaa 12,148,236\n"
"Ra 140,2,19456\nMaaa 12,144,228\nv_and_b32 v93, 0xffff0000, v92\nAR 82,0\nMaaa 16,160,224\nRa 144,2,20480\nv_lshlrev_b32 v92, 16, v92\nMaaa 16,164,232\nv_lshlrev_b32 v64, 16, v12\n"
"AR 83,1\nMaaa 16,168,240\nRa 148,2,21504\nCV 128,82,83\nMaaa 16,172,248\nWL 6\nAR 82,2\nMaaa 20,172,252\nRa 152,2,22528\nv_and_b32 v65, 0xffff0000, v12\nMaaa 20,168,244\n"
"MU 64,64,92\nAR 83,3\nMaaa 20,164,236\nRa 156,2,23552\nCV 129,82,83\nMaaa 20,160,228\nMU 65,65,92\nv_lshlrev_b32 v66, 16, v13\nMaaa 24,176,224\nRa 160,2,24576\nAR 82,8\n"
"Maaa 24,180,232\nv_and_b32 v67, 0xffff0000, v13\nAR 83,9\nMaaa 24,184,240\nRa 164,2,25600\nMU 66,66,92\nMaaa 24,188,248\nWL 6\nCV 130,82,83\nMaaa 28,188,252\nRa 168,2,26624\n"
"AR 82,10\nMaaa 28,184,244\nMU 67,67,92\nv_lshlrev_b32 v68, 16, v14\nMaaa 28,180,236\nRa 172,2,27648\nAR 83,11\nMaaa 28,176,228\nv_and_b32 v69, 0xffff0000, v14\nCV 131,82,83\n"
"Maaa 32,128,192\nRa 176,2,28672\nMU 68,68,92\nMaaa 32,132,200\nv_bfe_u32 v24, v1, 4, 3\ns_add_u32 s5, s95, 0\nMaaa 32,136,208\nRa 180,2,29696\nSv 128,1,16,5,0\nMaaa 32,140,216\n"
"WL 6\nAR 82,4\nMaaa 36,140,220\nRa 184,2,30720\nMU 69,69,92\nMaaa 36,136,212\nv_mul_u32_u24 v24, 0xc00, v24\nAR 83,5\nMaaa 36,132,204\nRa 188,2,31744\nCV 132,82,83\n"
"Maaa 36,128,196\nv_bfe_u32 v26, v1, 7, 1\nv_lshlrev_b32 v70, 16, v15\nMaaa 36,144,228\nRa 128,2,0\nAR 82,6\nMaaa 36,148,236\nv_add_lshl_u32 v24, v24, v26, 13\nAR 83,7\n"
"Maaa 36,152,244\nRa 132,2,1024\nv_and_b32 v71, 0xffff0000, v15\nMaaa 36,156,252\nWL 6\nCV 133,82,83\nMaaa 32,156,248\nRa 136,2,2048\nAR 82,12\nMaaa 32,152,240\n"
"buffer_load_dword v27, v24, s[16:19], s41 offen sc0 sc1\nMU 70,70,92\nMaaa 32,148,232\nRa 140,2,3072\nAR 83,13\nMaaa 32,144,224\nMU 71,71,92\nCV 134,82,83\nMaaa 40,160,192\n"
"Ra 144,2,4096\nv_lshlrev_b32 v72, 16, v16\nMaaa 40,164,200\nv_and_b32 v73, 0xffff0000, v16\nAR 82,14\nMaaa 40,168,208\nRa 148,2,5120\nAR 83,15\nMaaa 40,172,216\nWL 6\n"
"CV 135,82,83\nMaaa 44,172,220\nRa 152,2,6144\nMU 72,72,93\nMaaa 44,168,212\nMU 73,73,93\nSv 132,1,16,5,1024\nMaaa 44,164,204\nRa 156,2,7168\nAR 82,16\nMaaa 44,160,196\n"
"v_lshlrev_b32 v74, 16, v17\nv_and_b32 v75, 0xffff0000, v17\nMaaa 44,176,228\nRa 160,2,8192\nAR 83,17\nMaaa 44,180,236\nMU 74,74,93\nCV 128,82,83\nMaaa 44,184,244\nRa 164,2,9216\n"
"MU 75,75,93\nMaaa 44,188,252\nWL 6\nAR 82,18\nMaaa 40,188,248\nRa 168,2,10240\nAR 83,19\nMaaa 40,184,240\nv_lshlrev_b32 v76, 16, v18\nv_and_b32 v77, 0xffff0000, v18\n"
"Maaa 40,180,232\nRa 172,2,11264\nCV 129,82,83\nMaaa 40,176,224\nMU 76,76,93\nAR 82,24\nMaaa 48,128,192\nRa 176,2,12288\nMU 77,77,93\nMaaa 48,132,200\nv_lshlrev_b32 v78, 16, v19\n"
"AR 83,25\nMaaa 48,136,208\nRa 180,2,13312\nCV 130,82,83\nMaaa 48,140,216\nWL 6\nAR 82,26\nMaaa 52,140,220\nRa 184,2,14336\nv_and_b32 v79, 0xffff0000, v19\nMaaa 52,136,212\n"
"MU 78,78,93\nAR 83,27\nMaaa 52,132,204\nRa 188,2,15360\ns_barrier\nMaaa 52,128,196\nMU 79,79,93\nCV 131,82,83\nMaaa 52,144,228\nRa 128,2,32768\ns_add_u32 s5, s95, 131072\n"
"Maaa 52,148,236\nSv 128,1,16,5,0\nAR 82,20\nMaaa 52,152,244\nRa 132,2,33792\nAR 83,21\nMaaa 52,156,252\nWL 6\nCV 132,82,83\nMaaa 48,156,248\nRa 136,2,34816\nAR 82,22\n"
"Maaa 48,152,240\nAR 83,23\nCV 133,82,83\nMaaa 48,148,232\nRa 140,2,35840\nAR 82,28\nMaaa 48,144,224\nAR 83,29\nCV 134,82,83\nMaaa 56,160,192\nRa 144,2,36864\nAR 82,30\n"
"Maaa 56,164,200\nAR 83,31\nCV 135,82,83\nMaaa 56,168,208\nRa 148,2,37888\nSv 132,1,16,5,1024\nMaaa 56,172,216\nWL 6\nAR 82,32\nMaaa 60,172,220\nRa 152,2,38912\nAR 83,33\n"
"Maaa 60,168,212\nCV 128,82,83\nAR 82,34\nMaaa 60,164,204\nRa 156,2,39936\nAR 83,35\nMaaa 60,160,196\nCV 129,82,83\nAR 82,40\nMaaa 60,176,228\nRa 160,2,40960\nAR 83,41\n"
"Maaa 60,180,236\nCV 130,82,83\nAR 82,42\nMaaa 60,184,244\nRa 164,2,41984\nAR 83,43\nMaaa 60,188,252\nWL 6\nCV 131,82,83\nMaaa 56,188,248\nRa 168,2,43008\n"
"s_add_u32 s5, s95, 262144\nMaaa 56,184,240\nSv 128,1,16,5,0\nAR 82,36\nMaaa 56,180,232\nRa 172,2,44032\nAR 83,37\nMaaa 56,176,224\nCV 132,82,83\nAR 82,38\nMaaa 64,128,192\n"
"Ra 176,2,45056\nAR 83,39\nMaaa 64,132,200\nCV 133,82,83\nAR 82,44\nMaaa 64,136,208\nRa 180,2,46080\nAR 83,45\nMaaa 64,140,216\nWL 6\nCV 134,82,83\nMaaa 68,140,220\nRa 184,2,47104\n"
"AR 82,46\nMaaa 68,136,212\nAR 83,47\nCV 135,82,83\nMaaa 68,132,204\nRa 188,2,48128\nSv 132,1,16,5,1024\nMaaa 68,128,196\nAR 82,48\nAR 83,49\nMaaa 68,144,228\nRa 128,2,49152\n"
"CV 128,82,83\nMaaa 68,148,236\nAR 82,50\nAR 83,51\nMaaa 68,152,244\nRa 132,2,50176\nCV 129,82,83\nMaaa 68,156,252\nWL 6\nAR 82,56\nMaaa 64,156,248\nRa 136,2,51200\nAR 83,57\n"
"Maaa 64,152,240\nCV 130,82,83\nAR 82,58\nMaaa 64,148,232\nRa 140,2,52224\nAR 83,59\nMaaa 64,144,224\nCV 131,82,83\ns_add_u32 s5, s95, 393216\nMaaa 72,160,192\nRa 144,2,53248\n"
"WV 5\nv_cmp_eq_u32 s[62:63], s15, v27\ns_mov_b32 vcc_hi, 0\ns_nop 2\ns_lshr_b32 s63, s62, 8\ns_or_b32 s62, s62, s63\ns_andn2_b32 vcc_lo, 0xff, s62\ns_mov_b64 vcc, vcc\n"
"s_cbranch_vccnz .S479\n.S480:\nMaaa 72,164,200\ns_bitcmp1_b32 s63, 0\ns_cselect_b32 s2, 0x2000, 0\nMaaa 72,168,208\nRa 148,2,54272\nSv 128,1,16,5,0\nMaaa 72,172,216\nWL 6\n"
"AR 82,52\nMaaa 76,172,220\nRa 152,2,55296\nAR 83,53\nMaaa 76,168,212\ns_xor_b32 s2, s2, s20\nCV 132,82,83\nMaaa 76,164,204\nRa 156,2,56320\nAR 82,54\nMaaa 76,160,196\n"
"s_add_u32 s24, s2, 0x0\ns_bitcmp1_b32 s63, 1\ns_cselect_b32 s2, 0x2000, 0\nMaaa 76,176,228\nRa 160,2,57344\nAR 83,55\nMaaa 76,180,236\ns_xor_b32 s2, s2, s20\nCV 133,82,83\n"
"Maaa 76,184,244\nRa 164,2,58368\nAR 82,60\nMaaa 76,188,252\nWL 6\nAR 83,61\nMaaa 72,188,248\nRa 168,2,59392\nCV 134,82,83\nMaaa 72,184,240\ns_add_u32 s25, s2, 0x1800000\n"
"s_bitcmp1_b32 s63, 2\ns_cselect_b32 s2, 0x2000, 0\nMaaa 72,180,232\nRa 172,2,60416\nAR 82,62\nMaaa 72,176,224\ns_xor_b32 s2, s2, s20\nAR 83,63\nMaaa 80,128,192\nRa 176,2,61440\n"
"CV 135,82,83\nMaaa 80,132,200\ns_add_u32 s26, s2, 0x3000000\ns_bitcmp1_b32 s63, 3\ns_cselect_b32 s2, 0x2000, 0\nMaaa 80,136,208\nRa 180,2,62464\nSv 132,1,16,5,1024\n"
"Maaa 80,140,216\nWL 6\nAR 82,64\nMaaa 84,140,220\nRa 184,2,63488\nAR 83,65\nMaaa 84,136,212\ns_xor_b32 s2, s2, s20\nCV 128,82,83\nMaaa 84,132,204\nRa 188,2,64512\ns_barrier\n"
"Maaa 84,128,196\ns_add_u32 s27, s2, 0x4800000\ns_bitcmp1_b32 s63, 4\ns_cselect_b32 s2, 0x2000, 0\nMaaa 84,144,228\nRa 128,3,0\nAR 82,66\nMaaa 84,148,236\ns_xor_b32 s2, s2, s20\n"
"AR 83,67\nMaaa 84,152,244\nRa 132,3,1024\nCV 129,82,83\nMaaa 84,156,252\nWL 6\nAR 82,72\nMaaa 80,156,248\nRa 136,3,2048\nAR 83,73\nMaaa 80,152,240\ns_add_u32 s32, s2, 0x6000000\n"
"s_bitcmp1_b32 s63, 5\ns_cselect_b32 s2, 0x2000, 0\nMaaa 80,148,232\nRa 140,3,3072\nCV 130,82,83\nMaaa 80,144,224\ns_xor_b32 s2, s2, s20\nAR 82,74\nMaaa 88,160,192\nRa 144,3,4096\n"
"AR 83,75\nMaaa 88,164,200\ns_add_u32 s33, s2, 0x7800000\ns_bitcmp1_b32 s63, 6\ns_cselect_b32 s2, 0x2000, 0\nMaaa 88,168,208\nRa 148,3,5120\nCV 131,82,83\nMaaa 88,172,216\nWL 6\n"
"s_add_u32 s5, s95, 524288\nMaaa 92,172,220\nRa 152,3,6144\nSv 128,1,16,5,0\nMaaa 92,168,212\ns_xor_b32 s2, s2, s20\nAR 82,68\nMaaa 92,164,204\nRa 156,3,7168\nAR 83,69\n"
"Maaa 92,160,196\ns_add_u32 s34, s2, 0x9000000\ns_bitcmp1_b32 s63, 7\ns_cselect_b32 s2, 0x2000, 0\nMaaa 92,176,228\nRa 160,3,8192\nCV 132,82,83\nMaaa 92,180,236\n"
"s_xor_b32 s2, s2, s20\nAR 82,70\nMaaa 92,184,244\nRa 164,3,9216\nAR 83,71\nMaaa 92,188,252\nWL 6\nCV 133,82,83\nMaaa 88,188,248\nRa 168,3,10240\nAR 82,76\nMaaa 88,184,240\n"
"s_add_u32 s35, s2, 0xa800000\nAR 83,77\nMaaa 88,180,232\nRa 172,3,11264\nCV 134,82,83\nMaaa 88,176,224\nAR 82,78\nAR 83,79\nMaaa 96,128,192\nRa 176,3,12288\nCV 135,82,83\n"
"Maaa 96,132,200\nSv 132,1,16,5,1024\nMaaa 96,136,208\nRa 180,3,13312\nMaaa 96,140,216\nWL 6\nAR 82,80\nMaaa 100,140,220\nRa 184,3,14336\nAR 83,81\nMaaa 100,136,212\nCV 128,82,83\n"
"AR 82,82\nMaaa 100,132,204\nRa 188,3,15360\nAR 83,83\nMaaa 100,128,196\nCV 129,82,83\nAR 82,88\nMaaa 100,144,228\nAR 83,89\nCV 130,82,83\nMaaa 100,148,236\nAR 82,90\nAR 83,91\n"
"Maaa 100,152,244\nCV 131,82,83\ns_add_u32 s5, s95, 655360\nMaaa 100,156,252\nWL 4\nSv 128,1,16,5,0\nMaaa 96,156,248\nAR 82,84\nAR 83,85\nMaaa 96,152,240\nCV 132,82,83\nAR 82,86\n"
"Maaa 96,148,232\nAR 83,87\nCV 133,82,83\nMaaa 96,144,224\nAR 82,92\nAR 83,93\nMaaa 104,160,192\nCV 134,82,83\nAR 82,94\nMaaa 104,164,200\nAR 83,95\nCV 135,82,83\nMaaa 104,168,208\n"
"Sv 132,1,16,5,1024\nMaaa 104,172,216\nWL 0\nMaaa 108,172,220\nMaaa 108,168,212\nMaaa 108,164,204\nMaaa 108,160,196\nMaaa 108,176,228\nMaaa 108,180,236\nMaaa 108,184,244\n"
"Maaa 108,188,252\nMaaa 104,188,248\nMaaa 104,184,240\nMaaa 104,180,232\nMaaa 104,176,224\nAR 82,96\nAR 83,97\nCV 128,82,83\nAR 82,98\nAR 83,99\nCV 129,82,83\ns_nop 1\nAR 82,104\n"
"AR 83,105\nCV 130,82,83\nAR 82,106\nAR 83,107\nCV 131,82,83\ns_add_u32 s5, s95, 786432\nSv 128,1,16,5,0\nAR 82,100\nAR 83,101\nCV 132,82,83\nAR 82,102\nAR 83,103\nCV 133,82,83\n"
"AR 82,108\nAR 83,109\nCV 134,82,83\nAR 82,110\nAR 83,111\nCV 135,82,83\nSv 132,1,16,5,1024\ns_cmp_eq_u32 s13, 0\ns_cbranch_scc1 .Z482\n"
"buffer_load_dwordx4 v[96:99], v1, s[16:19], s24 offen offset:0 sc0 sc1\nbuffer_load_dwordx4 v[100:103], v1, s[16:19], s24 offen offset:1024 sc0 sc1\n"
"buffer_load_dwordx4 v[104:107], v1, s[16:19], s25 offen offset:0 sc0 sc1\nbuffer_load_dwordx4 v[108:111], v1, s[16:19], s25 offen offset:1024 sc0 sc1\n"
"buffer_load_dwordx4 v[112:115], v1, s[16:19], s26 offen offset:0 sc0 sc1\nbuffer_load_dwordx4 v[116:119], v1, s[16:19], s26 offen offset:1024 sc0 sc1\n"
"buffer_load_dwordx4 v[120:123], v1, s[16:19], s27 offen offset:0 sc0 sc1\nbuffer_load_dwordx4 v[124:127], v1, s[16:19], s27 offen offset:1024 sc0 sc1\n"
"buffer_load_dwordx4 v[32:35], v1, s[16:19], s32 offen offset:0 sc0 sc1\nbuffer_load_dwordx4 v[36:39], v1, s[16:19], s32 offen offset:1024 sc0 sc1\n"
"buffer_load_dwordx4 v[40:43], v1, s[16:19], s33 offen offset:0 sc0 sc1\nbuffer_load_dwordx4 v[44:47], v1, s[16:19], s33 offen offset:1024 sc0 sc1\n"
"buffer_load_dwordx4 v[48:51], v1, s[16:19], s34 offen offset:0 sc0 sc1\nbuffer_load_dwordx4 v[52:55], v1, s[16:19], s34 offen offset:1024 sc0 sc1\n"
"buffer_load_dwordx4 v[56:59], v1, s[16:19], s35 offen offset:0 sc0 sc1\nbuffer_load_dwordx4 v[60:63], v1, s[16:19], s35 offen offset:1024 sc0 sc1\ns_mov_b32 s7, 262144\nWV 0\n"
".Z485:\nv_max_u32 v82, v99, v103\nv_cmp_eq_u32 s[44:45], -1, v82\nv_max_u32 v82, v107, v111\nv_cmp_eq_u32 s[46:47], -1, v82\nv_max_u32 v82, v115, v119\n"
"v_cmp_eq_u32 s[48:49], -1, v82\nv_max_u32 v82, v123, v127\nv_cmp_eq_u32 s[50:51], -1, v82\nv_max_u32 v82, v35, v39\nv_cmp_eq_u32 s[52:53], -1, v82\nv_max_u32 v82, v43, v47\n"
"v_cmp_eq_u32 s[54:55], -1, v82\nv_max_u32 v82, v51, v55\nv_cmp_eq_u32 s[56:57], -1, v82\nv_max_u32 v82, v59, v63\nv_cmp_eq_u32 s[58:59], -1, v82\n"
"s_or_b64 s[0:1], s[44:45], s[46:47]\ns_or_b64 s[2:3], s[48:49], s[50:51]\ns_or_b64 s[0:1], s[0:1], s[52:53]\ns_or_b64 s[2:3], s[2:3], s[54:55]\ns_or_b64 s[0:1], s[0:1], s[56:57]\n"
"s_or_b64 s[2:3], s[2:3], s[58:59]\ns_or_b64 s[0:1], s[0:1], s[2:3]\ns_cbranch_scc1 .Z483\n.Z484:\nZvvv 136,6,96\nMvvv 136,6,104\nMvvv 136,6,112\nMvvv 136,6,120\nMvvv 136,6,32\n"
"Mvvv 136,6,40\nMvvv 136,6,48\nMvvv 136,6,56\nZvvv 140,4,96\nMvvv 140,4,104\nMvvv 140,4,112\nMvvv 140,4,120\nMvvv 140,4,32\nMvvv 140,4,40\nMvvv 140,4,48\nMvvv 140,4,56\n"
"Zvvv 144,6,100\nMvvv 144,6,108\nMvvv 144,6,116\nMvvv 144,6,124\nMvvv 144,6,36\nMvvv 144,6,44\nMvvv 144,6,52\nMvvv 144,6,60\nZvvv 148,4,100\nMvvv 148,4,108\nMvvv 148,4,116\n"
"Mvvv 148,4,124\nMvvv 148,4,36\nMvvv 148,4,44\nMvvv 148,4,52\nMvvv 148,4,60\nFM 64,80,136\nFM 65,80,137\nFM 66,80,138\nFM 67,80,139\nFM 68,80,140\nFM 69,80,141\nFM 70,80,142\n"
"FM 71,80,143\nCV 12,64,65\nCV 13,66,67\nCV 14,68,69\nCV 15,70,71\nSv 12,91,36,22,0\nFM 72,81,144\nFM 73,81,145\nFM 74,81,146\nFM 75,81,147\nFM 76,81,148\nFM 77,81,149\n"
"FM 78,81,150\nFM 79,81,151\nCV 16,72,73\nCV 17,74,75\nCV 18,76,77\nCV 19,78,79\nSv 16,91,36,23,0\n.Z482:\ns_endpgm\n.HD379:\nv_cmp_ne_u32 vcc, s15, v27\ns_nop 0\n"
"s_cbranch_vccnz .S494\n.S495:\nbuffer_store_dword v25, off, s[16:19], s54 offset:4 sc0 sc1\nv_and_b32 v27, 0x70, v1\n"
"buffer_load_dword v27, v27, s[16:19], s61 offen offset:12 sc0 sc1\ns_mov_b32 s90, 1\ns_swappc_b64 s[8:9], s[80:81]\nv_cmp_ne_u32 vcc, s15, v27\ns_nop 0\ns_cbranch_vccnz .S497\n"
".S498:\nv_and_b32 v27, 0x70, v1\nbuffer_load_dword v27, v27, s[16:19], s61 offen offset:8 sc0 sc1\ns_mov_b32 s90, 1\ns_swappc_b64 s[8:9], s[80:81]\nv_cmp_ne_u32 vcc, s15, v27\n"
"s_nop 0\ns_cbranch_vccnz .S500\n.S501:\nv_and_b32 v27, 0x1f0, v1\nbuffer_load_dword v27, v27, s[16:19], s58 offen offset:4 sc0 sc1\ns_mov_b32 s90, 1\n"
"s_swappc_b64 s[8:9], s[80:81]\nv_cmp_ne_u32 vcc, s15, v27\ns_nop 0\ns_cbranch_vccnz .S503\n.S504:\ns_mov_b32 s90, 100\ns_swappc_b64 s[8:9], s[80:81]\n.S380:\ns_mov_b32 s7, 262144\n"
".S382:\ns_lshl_b32 s5, s73, 8\ns_add_u32 s5, s5, 0xc04ac00\nv_lshrrev_b32 v27, 2, v1\nbuffer_load_dword v27, v27, s[16:19], s5 offen sc0 sc1\nWV 0\nv_xor_b32 v27, s15, v27\n"
"v_and_b32 v32, 0xffffdfff, v27\nv_cmp_ne_u32 vcc, 0, v32\ns_nop 0\ns_cbranch_vccz .S381\ns_sleep 6\ns_sub_u32 s7, s7, 1\ns_cbranch_scc0 .S382\ns_branch .S381\n.E388:\n"
"v_cmpx_gt_i32 vcc, 19, v0\nv_mov_b32 v19, s97\nv_cmpx_gt_i32 vcc, 18, v0\nv_mov_b32 v18, s97\nv_cmpx_gt_i32 vcc, 17, v0\nv_mov_b32 v17, s97\nv_cmpx_gt_i32 vcc, 16, v0\n"
"v_mov_b32 v16, s97\nv_cmpx_gt_i32 vcc, 3, v0\nv_mov_b32 v11, s97\nv_cmpx_gt_i32 vcc, 2, v0\nv_mov_b32 v10, s97\nv_cmpx_gt_i32 vcc, 1, v0\nv_mov_b32 v9, s97\n"
"v_cmpx_gt_i32 vcc, 0, v0\nv_mov_b32 v8, s97\ns_mov_b64 exec, -1\ns_nop 2\ns_branch .E389\n.E390:\ns_cmp_eq_u32 s99, 0\ns_cbranch_scc1 .K387\nv_cmpx_gt_i32 vcc, 19, v3\n"
"v_mov_b32 v15, s97\nv_cmpx_gt_i32 vcc, 18, v3\nv_mov_b32 v14, s97\nv_cmpx_gt_i32 vcc, 17, v3\nv_mov_b32 v13, s97\nv_cmpx_gt_i32 vcc, 16, v3\nv_mov_b32 v12, s97\n"
"v_cmpx_gt_i32 vcc, 3, v3\nv_mov_b32 v7, s97\nv_cmpx_gt_i32 vcc, 2, v3\nv_mov_b32 v6, s97\nv_cmpx_gt_i32 vcc, 1, v3\nv_mov_b32 v5, s97\nv_cmpx_gt_i32 vcc, 0, v3\nv_mov_b32 v4, s97\n"
"s_mov_b64 exec, -1\ns_nop 2\ns_branch .E391\n.K387:\nv_cmpx_gt_i32 vcc, 19, v3\nv_mov_b32 v19, s97\nv_cmpx_gt_i32 vcc, 18, v3\nv_mov_b32 v18, s97\nv_cmpx_gt_i32 vcc, 17, v3\n"
"v_mov_b32 v17, s97\nv_cmpx_gt_i32 vcc, 16, v3\nv_mov_b32 v16, s97\nv_cmpx_gt_i32 vcc, 3, v3\nv_mov_b32 v11, s97\nv_cmpx_gt_i32 vcc, 2, v3\nv_mov_b32 v10, s97\n"
"v_cmpx_gt_i32 vcc, 1, v3\nv_mov_b32 v9, s97\nv_cmpx_gt_i32 vcc, 0, v3\nv_mov_b32 v8, s97\ns_mov_b64 exec, -1\ns_nop 2\ns_branch .E391\n.E392:\ns_swappc_b64 s[80:81], s[8:9]\n"
"s_branch .E393\n.T394:\ns_mov_b32 s62, 1\ns_call_b64 s[80:81], .RA238\ns_branch .T395\n.T396:\ns_mov_b32 s62, 1\ns_call_b64 s[80:81], .RA238\ns_branch .T397\n.S398:\n"
"s_mov_b32 s7, 262144\n.S400:\nbuffer_load_dword v27, off, s[16:19], s5 sc0 sc1\nWV 0\nv_cmp_ne_u32 vcc, s15, v27\ns_nop 0\ns_cbranch_vccz .S399\ns_sleep 6\ns_sub_u32 s7, s7, 1\n"
"s_cbranch_scc0 .S400\ns_branch .S399\n.S401:\ns_mov_b32 s7, 262144\n.S403:\nv_and_b32 v27, 0x70, v1\nbuffer_load_dword v27, v27, s[16:19], s61 offen offset:128 sc0 sc1\nWV 0\n"
"v_cmp_ne_u32 vcc, s15, v27\ns_nop 0\ns_cbranch_vccz .S402\ns_sleep 6\ns_sub_u32 s7, s7, 1\ns_cbranch_scc0 .S403\ns_branch .S402\n.E405:\nv_mov_b32 v4, s97\nv_mov_b32 v5, s97\n"
"v_mov_b32 v6, s97\nv_mov_b32 v7, s97\nv_mov_b32 v12, s97\nv_mov_b32 v13, s97\nv_mov_b32 v14, s97\nv_mov_b32 v15, s97\nv_cmpx_gt_i32 vcc, 19, v3\nv_mov_b32 v19, s97\n"
"v_cmpx_gt_i32 vcc, 18, v3\nv_mov_b32 v18, s97\nv_cmpx_gt_i32 vcc, 17, v3\nv_mov_b32 v17, s97\nv_cmpx_gt_i32 vcc, 16, v3\nv_mov_b32 v16, s97\nv_cmpx_gt_i32 vcc, 3, v3\n"
"v_mov_b32 v11, s97\nv_cmpx_gt_i32 vcc, 2, v3\nv_mov_b32 v10, s97\nv_cmpx_gt_i32 vcc, 1, v3\nv_mov_b32 v9, s97\nv_cmpx_gt_i32 vcc, 0, v3\nv_mov_b32 v8, s97\ns_mov_b64 exec, -1\n"
"s_nop 2\ns_branch .E406\n.S407:\ns_mov_b32 s7, 262144\n.S409:\nv_and_b32 v27, 0x70, v1\nbuffer_load_dword v27, v27, s[16:19], s61 offen offset:136 sc0 sc1\nWV 0\n"
"v_cmp_ne_u32 vcc, s15, v27\ns_nop 0\ns_cbranch_vccz .S408\ns_sleep 6\ns_sub_u32 s7, s7, 1\ns_cbranch_scc0 .S409\ns_branch .S408\n.E412:\nv_cmpx_gt_i32 vcc, 19, v0\n"
"v_mov_b32 v15, s97\nv_cmpx_gt_i32 vcc, 18, v0\nv_mov_b32 v14, s97\nv_cmpx_gt_i32 vcc, 17, v0\nv_mov_b32 v13, s97\nv_cmpx_gt_i32 vcc, 16, v0\nv_mov_b32 v12, s97\n"
"v_cmpx_gt_i32 vcc, 3, v0\nv_mov_b32 v7, s97\nv_cmpx_gt_i32 vcc, 2, v0\nv_mov_b32 v6, s97\nv_cmpx_gt_i32 vcc, 1, v0\nv_mov_b32 v5, s97\nv_cmpx_gt_i32 vcc, 0, v0\nv_mov_b32 v4, s97\n"
"s_mov_b64 exec, -1\ns_nop 2\ns_branch .E413\n.E414:\ns_cmp_eq_u32 s99, 0\ns_cbranch_scc1 .K411\nv_cmpx_gt_i32 vcc, 19, v3\nv_mov_b32 v15, s97\nv_cmpx_gt_i32 vcc, 18, v3\n"
"v_mov_b32 v14, s97\nv_cmpx_gt_i32 vcc, 17, v3\nv_mov_b32 v13, s97\nv_cmpx_gt_i32 vcc, 16, v3\nv_mov_b32 v12, s97\nv_cmpx_gt_i32 vcc, 3, v3\nv_mov_b32 v7, s97\n"
"v_cmpx_gt_i32 vcc, 2, v3\nv_mov_b32 v6, s97\nv_cmpx_gt_i32 vcc, 1, v3\nv_mov_b32 v5, s97\nv_cmpx_gt_i32 vcc, 0, v3\nv_mov_b32 v4, s97\ns_mov_b64 exec, -1\ns_nop 2\ns_branch .E415\n"
".K411:\nv_cmpx_gt_i32 vcc, 19, v3\nv_mov_b32 v19, s97\nv_cmpx_gt_i32 vcc, 18, v3\nv_mov_b32 v18, s97\nv_cmpx_gt_i32 vcc, 17, v3\nv_mov_b32 v17, s97\nv_cmpx_gt_i32 vcc, 16, v3\n"
"v_mov_b32 v16, s97\nv_cmpx_gt_i32 vcc, 3, v3\nv_mov_b32 v11, s97\nv_cmpx_gt_i32 vcc, 2, v3\nv_mov_b32 v10, s97\nv_cmpx_gt_i32 vcc, 1, v3\nv_mov_b32 v9, s97\n"
"v_cmpx_gt_i32 vcc, 0, v3\nv_mov_b32 v8, s97\ns_mov_b64 exec, -1\ns_nop 2\ns_branch .E415\n.E416:\ns_swappc_b64 s[80:81], s[8:9]\ns_branch .E417\n.T418:\ns_mov_b32 s62, 0\n"
"s_call_b64 s[80:81], .RB239\ns_branch .T419\n.T420:\ns_mov_b32 s62, 0\ns_call_b64 s[80:81], .RA238\ns_branch .T421\n.E423:\ns_swappc_b64 s[80:81], s[8:9]\ns_branch .E424\n.T425:\n"
"s_mov_b32 s62, 0\ns_call_b64 s[80:81], .RB239\ns_branch .T426\n.T427:\ns_mov_b32 s62, 0\ns_call_b64 s[80:81], .RA238\ns_branch .T428\n.E429:\nv_cmpx_gt_i32 vcc, 19, v3\n"
"v_mov_b32 v15, s97\nv_cmpx_gt_i32 vcc, 18, v3\nv_mov_b32 v14, s97\nv_cmpx_gt_i32 vcc, 17, v3\nv_mov_b32 v13, s97\nv_cmpx_gt_i32 vcc, 16, v3\nv_mov_b32 v12, s97\n"
"v_cmpx_gt_i32 vcc, 3, v3\nv_mov_b32 v7, s97\nv_cmpx_gt_i32 vcc, 2, v3\nv_mov_b32 v6, s97\nv_cmpx_gt_i32 vcc, 1, v3\nv_mov_b32 v5, s97\nv_cmpx_gt_i32 vcc, 0, v3\nv_mov_b32 v4, s97\n"
"s_mov_b64 exec, -1\ns_nop 2\ns_branch .E430\n.E432:\nv_cmpx_gt_i32 vcc, 19, v0\nv_mov_b32 v15, s97\nv_cmpx_gt_i32 vcc, 18, v0\nv_mov_b32 v14, s97\nv_cmpx_gt_i32 vcc, 17, v0\n"
"v_mov_b32 v13, s97\nv_cmpx_gt_i32 vcc, 16, v0\nv_mov_b32 v12, s97\nv_cmpx_gt_i32 vcc, 3, v0\nv_mov_b32 v7, s97\nv_cmpx_gt_i32 vcc, 2, v0\nv_mov_b32 v6, s97\n"
"v_cmpx_gt_i32 vcc, 1, v0\nv_mov_b32 v5, s97\nv_cmpx_gt_i32 vcc, 0, v0\nv_mov_b32 v4, s97\ns_mov_b64 exec, -1\ns_nop 2\ns_branch .E433\n.E434:\ns_cmp_eq_u32 s99, 0\n"
"s_cbranch_scc1 .K431\nv_cmpx_gt_i32 vcc, 19, v3\nv_mov_b32 v15, s97\nv_cmpx_gt_i32 vcc, 18, v3\nv_mov_b32 v14, s97\nv_cmpx_gt_i32 vcc, 17, v3\nv_mov_b32 v13, s97\n"
"v_cmpx_gt_i32 vcc, 16, v3\nv_mov_b32 v12, s97\nv_cmpx_gt_i32 vcc, 3, v3\nv_mov_b32 v7, s97\nv_cmpx_gt_i32 vcc, 2, v3\nv_mov_b32 v6, s97\nv_cmpx_gt_i32 vcc, 1, v3\n"
"v_mov_b32 v5, s97\nv_cmpx_gt_i32 vcc, 0, v3\nv_mov_b32 v4, s97\ns_mov_b64 exec, -1\ns_nop 2\ns_branch .E435\n.K431:\nv_cmpx_gt_i32 vcc, 19, v3\nv_mov_b32 v19, s97\n"
"v_cmpx_gt_i32 vcc, 18, v3\nv_mov_b32 v18, s97\nv_cmpx_gt_i32 vcc, 17, v3\nv_mov_b32 v17, s97\nv_cmpx_gt_i32 vcc, 16, v3\nv_mov_b32 v16, s97\nv_cmpx_gt_i32 vcc, 3, v3\n"
"v_mov_b32 v11, s97\nv_cmpx_gt_i32 vcc, 2, v3\nv_mov_b32 v10, s97\nv_cmpx_gt_i32 vcc, 1, v3\nv_mov_b32 v9, s97\nv_cmpx_gt_i32 vcc, 0, v3\nv_mov_b32 v8, s97\ns_mov_b64 exec, -1\n"
"s_nop 2\ns_branch .E435\n.E436:\ns_swappc_b64 s[80:81], s[8:9]\ns_branch .E437\n.T438:\ns_mov_b32 s62, 2\ns_call_b64 s[80:81], .RB239\ns_branch .T439\n.T440:\ns_mov_b32 s62, 5\n"
"s_call_b64 s[80:81], .RA238\ns_branch .T441\n.T442:\ns_mov_b32 s62, 5\ns_call_b64 s[80:81], .RA238\ns_branch .T443\n.E446:\nv_cmpx_gt_i32 vcc, 19, v0\nv_mov_b32 v15, s97\n"
"v_cmpx_gt_i32 vcc, 18, v0\nv_mov_b32 v14, s97\nv_cmpx_gt_i32 vcc, 17, v0\nv_mov_b32 v13, s97\nv_cmpx_gt_i32 vcc, 16, v0\nv_mov_b32 v12, s97\nv_cmpx_gt_i32 vcc, 3, v0\n"
"v_mov_b32 v7, s97\nv_cmpx_gt_i32 vcc, 2, v0\nv_mov_b32 v6, s97\nv_cmpx_gt_i32 vcc, 1, v0\nv_mov_b32 v5, s97\nv_cmpx_gt_i32 vcc, 0, v0\nv_mov_b32 v4, s97\ns_mov_b64 exec, -1\n"
"s_nop 2\ns_branch .E447\n.E448:\ns_cmp_eq_u32 s99, 0\ns_cbranch_scc1 .K445\nv_cmpx_gt_i32 vcc, 19, v3\nv_mov_b32 v15, s97\nv_cmpx_gt_i32 vcc, 18, v3\nv_mov_b32 v14, s97\n"
"v_cmpx_gt_i32 vcc, 17, v3\nv_mov_b32 v13, s97\nv_cmpx_gt_i32 vcc, 16, v3\nv_mov_b32 v12, s97\nv_cmpx_gt_i32 vcc, 3, v3\nv_mov_b32 v7, s97\nv_cmpx_gt_i32 vcc, 2, v3\n"
"v_mov_b32 v6, s97\nv_cmpx_gt_i32 vcc, 1, v3\nv_mov_b32 v5, s97\nv_cmpx_gt_i32 vcc, 0, v3\nv_mov_b32 v4, s97\ns_mov_b64 exec, -1\ns_nop 2\ns_branch .E449\n.K445:\n"
"v_cmpx_gt_i32 vcc, 19, v3\nv_mov_b32 v19, s97\nv_cmpx_gt_i32 vcc, 18, v3\nv_mov_b32 v18, s97\nv_cmpx_gt_i32 vcc, 17, v3\nv_mov_b32 v17, s97\nv_cmpx_gt_i32 vcc, 16, v3\n"
"v_mov_b32 v16, s97\nv_cmpx_gt_i32 vcc, 3, v3\nv_mov_b32 v11, s97\nv_cmpx_gt_i32 vcc, 2, v3\nv_mov_b32 v10, s97\nv_cmpx_gt_i32 vcc, 1, v3\nv_mov_b32 v9, s97\n"
"v_cmpx_gt_i32 vcc, 0, v3\nv_mov_b32 v8, s97\ns_mov_b64 exec, -1\ns_nop 2\ns_branch .E449\n.E450:\ns_swappc_b64 s[80:81], s[8:9]\ns_branch .E451\n.E452:\nv_mov_b32 v91, 0x40000000\n"
"v_cmp_gt_u32 vcc, 0x200, v1\nWV 5\ns_nop 0\nv_cndmask_b32 v91, v91, v1, vcc\nSv 92,91,16,65,0\ns_branch .E453\n.T454:\ns_mov_b32 s62, 4\ns_call_b64 s[80:81], .RB239\n"
"s_branch .T455\n.T456:\ns_mov_b32 s62, 4\ns_call_b64 s[80:81], .RA238\ns_branch .T457\n.E459:\nv_mov_b32 v91, 0x40000000\nv_cmp_gt_u32 vcc, 0x200, v1\n"
"v_cndmask_b32 v91, v91, v1, vcc\nSv 92,91,16,65,0\ns_branch .E460\n.T461:\ns_mov_b32 s62, 4\ns_call_b64 s[80:81], .RB239\ns_branch .T462\n.T463:\ns_mov_b32 s62, 4\n"
"s_call_b64 s[80:81], .RA238\ns_branch .T464\n.T465:\ns_mov_b32 s62, 6\ns_call_b64 s[80:81], .RB239\ns_branch .T466\n.S467:\ns_mov_b32 s7, 262144\n.S469:\ns_add_u32 s4, s92, 0\n"
"buffer_load_dwordx4 a[192:195], v1, s[16:19], s4 offen offset:0 sc0 sc1\nbuffer_load_dwordx4 a[196:199], v1, s[16:19], s4 offen offset:1024 sc0 sc1\ns_add_u32 s4, s92, 131072\n"
"buffer_load_dwordx4 a[200:203], v1, s[16:19], s4 offen offset:0 sc0 sc1\nbuffer_load_dwordx4 a[204:207], v1, s[16:19], s4 offen offset:1024 sc0 sc1\ns_add_u32 s4, s92, 262144\n"
"buffer_load_dwordx4 a[208:211], v1, s[16:19], s4 offen offset:0 sc0 sc1\nbuffer_load_dwordx4 a[212:215], v1, s[16:19], s4 offen offset:1024 sc0 sc1\ns_add_u32 s4, s92, 393216\n"
"buffer_load_dwordx4 a[216:219], v1, s[16:19], s4 offen offset:0 sc0 sc1\nbuffer_load_dwordx4 a[220:223], v1, s[16:19], s4 offen offset:1024 sc0 sc1\nWV 0\nAR 22,195\nAR 23,199\n"
"v_max_u32 v21, v22, v23\nAR 22,203\nAR 23,207\nv_max3_u32 v21, v21, v22, v23\nAR 22,211\nAR 23,215\nv_max3_u32 v21, v21, v22, v23\nAR 22,219\nAR 23,223\n"
"v_max3_u32 v21, v21, v22, v23\nv_cmp_eq_u32 vcc, -1, v21\ns_nop 0\ns_cbranch_vccz .S468\ns_sleep 6\ns_sub_u32 s7, s7, 1\ns_cbranch_scc0 .S469\ns_branch .S468\n.S470:\n"
"s_mov_b32 s7, 262144\n.S472:\ns_add_u32 s4, s92, 524288\nbuffer_load_dwordx4 a[224:227], v1, s[16:19], s4 offen offset:0 sc0 sc1\n"
"buffer_load_dwordx4 a[228:231], v1, s[16:19], s4 offen offset:1024 sc0 sc1\ns_add_u32 s4, s92, 655360\nbuffer_load_dwordx4 a[232:235], v1, s[16:19], s4 offen offset:0 sc0 sc1\n"
"buffer_load_dwordx4 a[236:239], v1, s[16:19], s4 offen offset:1024 sc0 sc1\ns_add_u32 s4, s92, 786432\nbuffer_load_dwordx4 a[240:243], v1, s[16:19], s4 offen offset:0 sc0 sc1\n"
"buffer_load_dwordx4 a[244:247], v1, s[16:19], s4 offen offset:1024 sc0 sc1\ns_add_u32 s4, s92, 917504\nbuffer_load_dwordx4 a[248:251], v1, s[16:19], s4 offen offset:0 sc0 sc1\n"
"buffer_load_dwordx4 a[252:255], v1, s[16:19], s4 offen offset:1024 sc0 sc1\nWV 0\nAR 22,227\nAR 23,231\nv_max_u32 v21, v22, v23\nAR 22,235\nAR 23,239\n"
"v_max3_u32 v21, v21, v22, v23\nAR 22,243\nAR 23,247\nv_max3_u32 v21, v21, v22, v23\nAR 22,251\nAR 23,255\nv_max3_u32 v21, v21, v22, v23\nv_cmp_eq_u32 vcc, -1, v21\ns_nop 0\n"
"s_cbranch_vccz .S471\ns_sleep 6\ns_sub_u32 s7, s7, 1\ns_cbranch_scc0 .S472\ns_branch .S471\n.S473:\ns_mov_b32 s7, 262144\n.S475:\n"
"buffer_load_dwordx4 v[64:67], v89, s[16:19], s40 offen offset:0 sc0 sc1\nbuffer_load_dwordx4 v[68:71], v89, s[16:19], s40 offen offset:16 sc0 sc1\n"
"buffer_load_dwordx4 v[72:75], v89, s[16:19], s40 offen offset:1792 sc0 sc1\nbuffer_load_dwordx4 v[76:79], v89, s[16:19], s40 offen offset:1808 sc0 sc1\nWV 0\n"
"v_xor_b32 v92, s15, v66\nv_xor_b32 v93, s15, v70\nv_xor_b32 v94, s15, v74\nv_or3_b32 v92, v92, v93, v94\nv_xor_b32 v93, s15, v78\nv_or_b32 v92, v92, v93\nv_cmp_ne_u32 vcc, 0, v92\n"
"s_nop 0\ns_cbranch_vccz .S474\ns_sleep 6\ns_sub_u32 s7, s7, 1\ns_cbranch_scc0 .S475\ns_branch .S474\n.S476:\ns_mov_b32 s7, 262144\n.S478:\n"
"buffer_load_dword v27, off, s[16:19], s60 offset:512 sc0 sc1\nWV 0\nv_cmp_ne_u32 vcc, s15, v27\ns_nop 0\ns_cbranch_vccz .S477\ns_sleep 6\ns_sub_u32 s7, s7, 1\n"
"s_cbranch_scc0 .S478\ns_branch .S477\n.S479:\ns_mov_b32 s7, 262144\n.S481:\nbuffer_load_dword v27, v24, s[16:19], s41 offen sc0 sc1\nWV 0\nv_cmp_eq_u32 s[62:63], s15, v27\n"
"s_mov_b32 vcc_hi, 0\ns_nop 2\ns_lshr_b32 s63, s62, 8\ns_or_b32 s62, s62, s63\ns_andn2_b32 vcc_lo, 0xff, s62\ns_mov_b64 vcc, vcc\ns_cbranch_vccz .S480\ns_sleep 6\n"
"s_sub_u32 s7, s7, 1\ns_cbranch_scc0 .S481\ns_branch .S480\n.Z483:\ns_sub_u32 s7, s7, 1\ns_cbranch_scc1 .Z484\ns_cmp_lg_u64 s[44:45], 0\ns_cbranch_scc0 .Z486\n"
"buffer_load_dwordx4 v[96:99], v1, s[16:19], s24 offen offset:0 sc0 sc1\nbuffer_load_dwordx4 v[100:103], v1, s[16:19], s24 offen offset:1024 sc0 sc1\n.Z486:\n"
"s_cmp_lg_u64 s[46:47], 0\ns_cbranch_scc0 .Z487\nbuffer_load_dwordx4 v[104:107], v1, s[16:19], s25 offen offset:0 sc0 sc1\n"
"buffer_load_dwordx4 v[108:111], v1, s[16:19], s25 offen offset:1024 sc0 sc1\n.Z487:\ns_cmp_lg_u64 s[48:49], 0\ns_cbranch_scc0 .Z488\n"
"buffer_load_dwordx4 v[112:115], v1, s[16:19], s26 offen offset:0 sc0 sc1\nbuffer_load_dwordx4 v[116:119], v1, s[16:19], s26 offen offset:1024 sc0 sc1\n.Z488:\n"
"s_cmp_lg_u64 s[50:51], 0\ns_cbranch_scc0 .Z489\nbuffer_load_dwordx4 v[120:123], v1, s[16:19], s27 offen offset:0 sc0 sc1\n"
"buffer_load_dwordx4 v[124:127], v1, s[16:19], s27 offen offset:1024 sc0 sc1\n.Z489:\ns_cmp_lg_u64 s[52:53], 0\ns_cbranch_scc0 .Z490\n"
"buffer_load_dwordx4 v[32:35], v1, s[16:19], s32 offen offset:0 sc0 sc1\nbuffer_load_dwordx4 v[36:39], v1, s[16:19], s32 offen offset:1024 sc0 sc1\n.Z490:\n"
"s_cmp_lg_u64 s[54:55], 0\ns_cbranch_scc0 .Z491\nbuffer_load_dwordx4 v[40:43], v1, s[16:19], s33 offen offset:0 sc0 sc1\n"
"buffer_load_dwordx4 v[44:47], v1, s[16:19], s33 offen offset:1024 sc0 sc1\n.Z491:\ns_cmp_lg_u64 s[56:57], 0\ns_cbranch_scc0 .Z492\n"
"buffer_load_dwordx4 v[48:51], v1, s[16:19], s34 offen offset:0 sc0 sc1\nbuffer_load_dwordx4 v[52:55], v1, s[16:19], s34 offen offset:1024 sc0 sc1\n.Z492:\n"
"s_cmp_lg_u64 s[58:59], 0\ns_cbranch_scc0 .Z493\nbuffer_load_dwordx4 v[56:59], v1, s[16:19], s35 offen offset:0 sc0 sc1\n"
"buffer_load_dwordx4 v[60:63], v1, s[16:19], s35 offen offset:1024 sc0 sc1\n.Z493:\ns_sleep 8\nWV 0\ns_branch .Z485\n.S494:\ns_mov_b32 s7, 262144\n.S496:\nv_and_b32 v27, 0x70, v1\n"
"buffer_load_dword v27, v27, s[16:19], s61 offen offset:140 sc0 sc1\nWV 0\nv_cmp_ne_u32 vcc, s15, v27\ns_nop 0\ns_cbranch_vccz .S495\ns_sleep 6\ns_sub_u32 s7, s7, 1\n"
"s_cbranch_scc0 .S496\ns_branch .S495\n.S497:\ns_mov_b32 s7, 262144\n.S499:\nv_and_b32 v27, 0x70, v1\nbuffer_load_dword v27, v27, s[16:19], s61 offen offset:12 sc0 sc1\nWV 0\n"
"v_cmp_ne_u32 vcc, s15, v27\ns_nop 0\ns_cbranch_vccz .S498\ns_sleep 6\ns_sub_u32 s7, s7, 1\ns_cbranch_scc0 .S499\ns_branch .S498\n.S500:\ns_mov_b32 s7, 262144\n.S502:\n"
"v_and_b32 v27, 0x70, v1\nbuffer_load_dword v27, v27, s[16:19], s61 offen offset:8 sc0 sc1\nWV 0\nv_cmp_ne_u32 vcc, s15, v27\ns_nop 0\ns_cbranch_vccz .S501\ns_sleep 6\n"
"s_sub_u32 s7, s7, 1\ns_cbranch_scc0 .S502\ns_branch .S501\n.S503:\ns_mov_b32 s7, 262144\n.S505:\nv_and_b32 v27, 0x1f0, v1\n"
"buffer_load_dword v27, v27, s[16:19], s58 offen offset:4 sc0 sc1\nWV 0\nv_cmp_ne_u32 vcc, s15, v27\ns_nop 0\ns_cbranch_vccz .S504\ns_sleep 6\ns_sub_u32 s7, s7, 1\n"
"s_cbranch_scc0 .S505\ns_branch .S504\n.Lcode1:\n"
  :: "s"(__builtin_amdgcn_kernarg_segment_ptr()), "s"(blockIdx.x), "v"(threadIdx.x)
  : "memory", "scc", "vcc", "v1", "v2", "v3", "v4", "v5", "v6", "v7", "v8", "v9", "v10", "v11", "v12", "v13", "v14", "v15", "v16", "v17", "v18", "v19", "v20", "v21", "v22", "v23", "v24", "v25", "v26", "v27", "v28", "v29", "v30", "v31", "v32", "v33", "v34", "v35", "v36", "v37", "v38", "v39", "v40", "v41", "v42", "v43", "v44", "v45", "v46", "v47", "v48", "v49", "v50", "v51", "v52", "v53", "v54", "v55", "v56", "v57", "v58", "v59", "v60", "v61", "v62", "v63", "v64", "v65", "v66", "v67", "v68", "v69", "v70", "v71", "v72", "v73", "v74", "v75", "v76", "v77", "v78", "v79", "v80", "v81", "v82", "v83", "v84", "v85", "v86", "v87", "v88", "v89", "v90", "v91", "v92", "v93", "v94", "v95", "v96", "v97", "v98", "v99", "v100", "v101", "v102", "v103", "v104", "v105", "v106", "v107", "v108", "v109", "v110", "v111", "v112", "v113", "v114", "v115", "v116", "v117", "v118", "v119", "v120", "v121", "v122", "v123", "v124", "v125", "v126", "v127", "v128", "v129", "v130", "v131", "v132", "v133", "v134", "v135", "v136", "v137", "v138", "v139", "v140", "v141", "v142", "v143", "v144", "v145", "v146", "v147", "v148", "v149", "v150", "v151", "v152", "v153", "v154", "v155", "v156", "v157", "v158", "v159", "v160", "v161", "v162", "v163", "v164", "v165", "v166", "v167", "v168", "v169", "v170", "v171", "v172", "v173", "v174", "v175", "v176", "v177", "v178", "v179", "v180", "v181", "v182", "v183", "v184", "v185", "v186", "v187", "v188", "v189", "v190", "v191", "v192", "v193", "v194", "v195", "v196", "v197", "v198", "v199", "v200", "v201", "v202", "v203", "v204", "v205", "v206", "v207", "v208", "v209", "v210", "v211", "v212", "v213", "v214", "v215", "v216", "v217", "v218", "v219", "v220", "v221", "v222", "v223", "v224", "v225", "v226", "v227", "v228", "v229", "v230", "v231", "v232", "v233", "v234", "v235", "v236", "v237", "v238", "v239", "v240", "v241", "v242", "v243", "v244", "v245", "v246", "v247", "v248", "v249", "v250", "v251", "v252", "v253", "v254", "v255", "a0", "a1", "a2", "a3", "a4", "a5", "a6", "a7", "a8", "a9", "a10", "a11", "a12", "a13", "a14", "a15", "a16", "a17", "a18", "a19", "a20", "a21", "a22", "a23", "a24", "a25", "a26", "a27", "a28", "a29", "a30", "a31", "a32", "a33", "a34", "a35", "a36", "a37", "a38", "a39", "a40", "a41", "a42", "a43", "a44", "a45", "a46", "a47", "a48", "a49", "a50", "a51", "a52", "a53", "a54", "a55", "a56", "a57", "a58", "a59", "a60", "a61", "a62", "a63", "a64", "a65", "a66", "a67", "a68", "a69", "a70", "a71", "a72", "a73", "a74", "a75", "a76", "a77", "a78", "a79", "a80", "a81", "a82", "a83", "a84", "a85", "a86", "a87", "a88", "a89", "a90", "a91", "a92", "a93", "a94", "a95", "a96", "a97", "a98", "a99", "a100", "a101", "a102", "a103", "a104", "a105", "a106", "a107", "a108", "a109", "a110", "a111", "a112", "a113", "a114", "a115", "a116", "a117", "a118", "a119", "a120", "a121", "a122", "a123", "a124", "a125", "a126", "a127", "a128", "a129", "a130", "a131", "a132", "a133", "a134", "a135", "a136", "a137", "a138", "a139", "a140", "a141", "a142", "a143", "a144", "a145", "a146", "a147", "a148", "a149", "a150", "a151", "a152", "a153", "a154", "a155", "a156", "a157", "a158", "a159", "a160", "a161", "a162", "a163", "a164", "a165", "a166", "a167", "a168", "a169", "a170", "a171", "a172", "a173", "a174", "a175", "a176", "a177", "a178", "a179", "a180", "a181", "a182", "a183", "a184", "a185", "a186", "a187", "a188", "a189", "a190", "a191", "a192", "a193", "a194", "a195", "a196", "a197", "a198", "a199", "a200", "a201", "a202", "a203", "a204", "a205", "a206", "a207", "a208", "a209", "a210", "a211", "a212", "a213", "a214", "a215", "a216", "a217", "a218", "a219", "a220", "a221", "a222", "a223", "a224", "a225", "a226", "a227", "a228", "a229", "a230", "a231", "a232", "a233", "a234", "a235", "a236", "a237", "a238", "a239", "a240", "a241", "a242", "a243", "a244", "a245", "a246", "a247", "a248", "a249", "a250", "a251", "a252", "a253", "a254", "a255", "s8", "s9", "s10", "s11", "s12", "s13", "s14", "s15", "s16", "s17", "s18", "s19", "s20", "s21", "s22", "s23", "s24", "s25", "s26", "s27", "s28", "s29", "s30", "s31", "s33", "s34", "s35", "s36", "s37", "s38", "s39", "s40", "s41", "s42", "s43", "s44", "s45", "s46", "s47", "s48", "s49", "s50", "s51", "s52", "s53", "s54", "s55", "s56", "s57", "s58", "s59", "s60", "s61", "s62", "s63", "s64", "s65", "s66", "s67", "s68", "s69", "s70", "s71", "s72", "s73", "s74", "s75", "s76", "s77", "s78", "s79", "s80", "s81", "s82", "s83", "s84", "s85", "s86", "s87", "s88", "s89", "s90", "s91", "s92", "s93", "s94", "s95", "s96", "s97", "s98", "s99");
#endif
}

extern "C" void kernel_launch(void* const* d_in, const int* in_sizes, int n_in,
                              void* d_out, int out_size, void* d_ws, size_t ws_size,
                              hipStream_t stream) {
  (void)hipFuncSetAttribute((const void*)k_main, hipFuncAttributeMaxDynamicSharedMemorySize, 163840);
  k_main<<<256, 256, 163840, stream>>>(d_in[0], d_in[1], d_in[2], d_in[3], d_in[4], d_ws, d_in[11], d_in[7], d_in[5], d_in[8], d_in[9], d_in[10], d_out);
}
```

```cpp
#include <hip/hip_runtime.h>
#include <stdint.h>

extern "C" __global__ __launch_bounds__(256, 1)
void k_main(const void* x, const void* ckv, const void* wq, const void* wuk, const void* wuv, void* ws,
            const void* gate_w, const void* w_r, const void* wo, const void* wg, const void* wu, const void* wd, void* out) {
#if defined(__HIP_DEVICE_COMPILE__)
  asm volatile(
"s_mov_b64 s[8:9], %0\ns_mov_b32 s10, %1\nv_mov_b32 v0, %2\n.macro WL n\ns_waitcnt lgkmcnt(\\n)\n.endm\n.macro D0 x,r,s,o\n"
"buffer_load_dwordx4 v\\x, s[\\r:\\r+3], s\\s offen offset:\\o lds\n.endm\n.macro WV n\ns_waitcnt vmcnt(\\n)\n.endm\n.macro Ra d,x,o\nds_read_b128 a[\\d:\\d+3], v\\x offset:\\o\n"
".endm\n.macro Ta d,x,o\nds_read_b64_tr_b16 a[\\d:\\d+1], v\\x offset:\\o\n.endm\n.macro Zvaa d,x,y\nv_mfma_f32_16x16x32_bf16 v[\\d:\\d+3], a[\\x:\\x+3], a[\\y:\\y+3], 0\n.endm\n"
".macro Mvaa d,x,y\nv_mfma_f32_16x16x32_bf16 v[\\d:\\d+3], a[\\x:\\x+3], a[\\y:\\y+3], v[\\d:\\d+3]\n.endm\n.macro Zaaa d,x,y\n"
"v_mfma_f32_16x16x32_bf16 a[\\d:\\d+3], a[\\x:\\x+3], a[\\y:\\y+3], 0\n.endm\n.macro Maaa d,x,y\nv_mfma_f32_16x16x32_bf16 a[\\d:\\d+3], a[\\x:\\x+3], a[\\y:\\y+3], a[\\d:\\d+3]\n"
".endm\n.macro MU d,x,y\nv_mul_f32 v\\d, v\\x, v\\y\n.endm\n.macro CV d,x,y\nv_cvt_pk_bf16_f32 v\\d, v\\x, v\\y\n.endm\n.macro FM d,x,y\nv_fmac_f32 v\\d, v\\x, v\\y\n.endm\n"
".macro AD d,x,y\nv_add_f32 v\\d, v\\x, v\\y\n.endm\n.macro EX d,x\nv_exp_f32 v\\d, v\\x\n.endm\n.macro AR d,x\nv_accvgpr_read_b32 v\\d, a\\x\n.endm\n.macro Sv d,x,r,s,o\n"
"buffer_store_dwordx4 v[\\d:\\d+3], v\\x, s[\\r:\\r+3], s\\s offen offset:\\o sc0 sc1\n.endm\n.macro Rv d,x,o\nds_read_b128 v[\\d:\\d+3], v\\x offset:\\o\n.endm\n.macro D1 x,r,s,o\n"
"buffer_load_dwordx4 v\\x, s[\\r:\\r+3], s\\s offen offset:\\o sc1 lds\n.endm\n.macro Cvav d,x,y,c\nv_mfma_f32_16x16x32_bf16 v[\\d:\\d+3], a[\\x:\\x+3], v[\\y:\\y+3], v[\\c:\\c+3]\n"
".endm\n.macro Mvav d,x,y\nv_mfma_f32_16x16x32_bf16 v[\\d:\\d+3], a[\\x:\\x+3], v[\\y:\\y+3], v[\\d:\\d+3]\n.endm\n.macro Zaav d,x,y\n"
"v_mfma_f32_16x16x32_bf16 a[\\d:\\d+3], a[\\x:\\x+3], v[\\y:\\y+3], 0\n.endm\n.macro Maav d,x,y\nv_mfma_f32_16x16x32_bf16 a[\\d:\\d+3], a[\\x:\\x+3], v[\\y:\\y+3], a[\\d:\\d+3]\n"
".endm\n.macro SU d,x,y\nv_sub_f32 v\\d, v\\x, v\\y\n.endm\n.macro Mava d,x,y\nv_mfma_f32_16x16x32_bf16 a[\\d:\\d+3], v[\\x:\\x+3], a[\\y:\\y+3], a[\\d:\\d+3]\n.endm\n"
".macro Zvav d,x,y\nv_mfma_f32_16x16x32_bf16 v[\\d:\\d+3], a[\\x:\\x+3], v[\\y:\\y+3], 0\n.endm\n.macro Zvvv d,x,y\n"
"v_mfma_f32_16x16x32_bf16 v[\\d:\\d+3], v[\\x:\\x+3], v[\\y:\\y+3], 0\n.endm\n.macro Mvvv d,x,y\nv_mfma_f32_16x16x32_bf16 v[\\d:\\d+3], v[\\x:\\x+3], v[\\y:\\y+3], v[\\d:\\d+3]\n"
".endm\n.macro AW d,x\nv_accvgpr_write_b32 a\\d, v\\x\n.endm\n.macro Tv d,x,o\nds_read_b64_tr_b16 v[\\d:\\d+1], v\\x offset:\\o\n.endm\n.macro Sa d,x,r,s,o\n"
"buffer_store_dwordx4 a[\\d:\\d+3], v\\x, s[\\r:\\r+3], s\\s offen offset:\\o sc0 sc1\n.endm\n.macro Cvaa d,x,y,c\n"
"v_mfma_f32_16x16x32_bf16 v[\\d:\\d+3], a[\\x:\\x+3], a[\\y:\\y+3], v[\\c:\\c+3]\n.endm\n.Lcode0:\ns_mov_b32 s70, 0\ns_load_dwordx16 s[44:59], s[8:9], 0x0\n"
"s_load_dwordx8 s[60:67], s[8:9], 0x40\ns_load_dwordx2 s[68:69], s[8:9], 0x60\nv_lshrrev_b32 v2, 6, v0\nv_and_b32 v0, 63, v0\nv_readfirstlane_b32 s11, v2\nv_lshlrev_b32 v1, 4, v0\n"
"s_and_b32 s73, s10, 7\ns_lshr_b32 s74, s10, 3\ns_and_b32 s2, s74, 1\ns_lshl1_add_u32 s12, s73, s2\ns_bfe_u32 s13, s74, 0x30001\ns_lshr_b32 s14, s74, 4\ns_xor_b32 s14, s14, 1\n"
"v_lshrrev_b32 v32, 5, v0\ns_and_b32 s3, s11, 1\ns_lshl_b32 s2, s3, 3\nv_lshl_or_b32 v33, v32, 1, s2\nv_and_b32 v34, 31, v0\nv_xor_b32 v33, v33, v34\nv_lshlrev_b32 v33, 4, v33\n"
"v_lshl_or_b32 v6, v32, 13, v33\nv_xor_b32 v33, 64, v6\nv_add_u32 v7, 15360, v33\nv_add_u32 v8, 30720, v6\nv_add_u32 v9, 46080, v33\ns_lshl_b32 s2, s11, 16\ns_lshl_b32 s4, s12, 9\n"
"s_add_u32 s5, s2, s4\ns_getpc_b64 s[40:41]\n.PC8:\ns_sub_u32 s40, s40, .PC8-.Lcode0\ns_subb_u32 s41, s41, 0\ns_mov_b32 s42, .Lcode1-.Lcode0\ns_mov_b32 s43, 0x20000\n"
"s_lshl2_add_u32 s6, s74, s11\ns_lshl_b32 s6, s6, 7\nv_lshlrev_b32 v54, 10, v1\nbuffer_load_dword v31, v54, s[40:43], s6 offen\nv_lshrrev_b32 v32, 4, v0\nv_and_b32 v33, 7, v0\n"
"v_xor_b32 v33, v33, v32\nv_lshlrev_b32 v33, 4, v33\nv_lshrrev_b32 v32, 3, v0\nv_lshl_or_b32 v2, v32, 11, v33\nv_xor_b32 v33, 64, v2\nv_add_u32 v3, 15360, v33\n"
"v_add_u32 v4, 30720, v2\nv_add_u32 v5, 46080, v33\nv_lshrrev_b32 v144, 4, v0\nv_and_b32 v145, 7, v0\nv_xor_b32 v145, v145, v144\nv_lshlrev_b32 v145, 4, v145\n"
"v_lshrrev_b32 v144, 3, v0\nv_mul_u32_u24 v144, 896, v144\nv_add_u32 v136, v144, v145\nv_xor_b32 v145, 64, v136\nv_add_u32 v137, 6144, v145\nv_add_u32 v138, 55296, v136\n"
"v_add_u32 v139, 61440, v145\ns_lshr_b32 s88, s11, 1\ns_lshr_b32 s2, s13, 2\ns_bfe_u32 s3, s13, 0x10001\ns_mul_i32 s3, s3, 17\ns_add_u32 s2, s2, s3\ns_lshl2_add_u32 s3, s14, s88\n"
"s_lshl_b32 s3, s3, 2\ns_lshr_b32 s3, 0xe420ca86, s3\ns_and_b32 s3, s3, 15\ns_add_u32 s2, s2, s3\ns_cmp_eq_u32 s2, 32\ns_cselect_b32 s86, 16, s2\ns_add_u32 s88, s88, 2\n"
"s_lshr_b32 s2, s13, 2\ns_bfe_u32 s3, s13, 0x10001\ns_mul_i32 s3, s3, 17\ns_add_u32 s2, s2, s3\ns_lshl2_add_u32 s3, s14, s88\ns_lshl_b32 s3, s3, 2\ns_lshr_b32 s3, 0xe420ca86, s3\n"
"s_and_b32 s3, s3, 15\ns_add_u32 s2, s2, s3\ns_cmp_eq_u32 s2, 32\ns_cselect_b32 s88, 16, s2\ns_and_b32 s89, s11, 1\ns_lshl1_add_u32 s86, s86, s89\ns_lshl1_add_u32 s88, s88, s89\n"
"s_sub_u32 s2, 15, s13\ns_lshl3_add_u32 s2, s2, s11\ns_mul_i32 s87, s2, 14336\ns_mov_b32 s18, 0xc0ae800\ns_mov_b32 s30, 0x1c0000\ns_mov_b32 s34, 0x380000\ns_mov_b32 s22, 0x400000\n"
"s_mov_b32 s26, 0x800000\ns_mov_b32 s19, 0x20000\ns_mov_b32 s31, 0x20000\ns_mov_b32 s35, 0x20000\ns_mov_b32 s23, 0x20000\ns_mov_b32 s27, 0x20000\nWL 0\ns_mov_b32 s28, s44\n"
"s_and_b32 s29, s45, 0xffff\ns_mov_b32 s32, s48\ns_and_b32 s33, s49, 0xffff\ns_mov_b32 s20, s46\ns_and_b32 s21, s47, 0xffff\ns_bitcmp1_b32 s13, 0\n"
"s_cselect_b64 s[50:51], s[52:53], s[50:51]\ns_mov_b32 s24, s50\ns_and_b32 s25, s51, 0xffff\ns_sub_u32 s44, s5, 0x40000\ns_mov_b32 s51, s5\ns_lshl_b32 s47, s11, 12\n"
"s_add_u32 s47, s47, 0x10000\ns_lshl_b32 s45, s86, 16\ns_sub_u32 s45, s45, 128\ns_lshl_b32 s48, s88, 16\ns_sub_u32 s48, s48, 128\ns_mov_b32 s52, s87\ns_cmp_eq_u32 s14, 1\n"
"s_cbranch_scc1 .B6\ns_add_u32 m0, s47, 65536\ns_add_u32 s46, s51, 0\nD0 6,32,46,0\nD0 7,32,46,1024\nD0 8,32,46,2048\nD0 9,32,46,3072\ns_add_u32 m0, s47, -65536\n"
"s_add_u32 s46, s52, 0\nD0 136,28,46,0\nD0 137,28,46,1024\nD0 138,28,46,2048\nD0 139,28,46,3072\ns_add_u32 m0, s47, 81920\ns_add_u32 s46, s51, 262144\nD0 6,32,46,0\n"
"D0 7,32,46,1024\nD0 8,32,46,2048\nD0 9,32,46,3072\ns_branch .B7\n.B6:\ns_add_u32 m0, s47, 0\ns_add_u32 s44, s44, 0x40000\nD0 6,24,44,0\nD0 7,24,44,1024\nD0 8,24,44,2048\n"
"D0 9,24,44,3072\ns_add_u32 m0, s47, -65536\ns_add_u32 s45, s45, 128\nD0 2,20,45,0\nD0 3,20,45,1024\nD0 4,20,45,2048\nD0 5,20,45,3072\ns_add_u32 m0, s47, 16384\n"
"s_add_u32 s44, s44, 0x40000\nD0 6,24,44,0\nD0 7,24,44,1024\nD0 8,24,44,2048\nD0 9,24,44,3072\n.B7:\ns_add_u32 s54, s54, 0x1fffff\ns_addc_u32 s55, s55, 0\n"
"s_and_b32 s54, s54, 0xffe00000\ns_mov_b32 s16, s54\ns_and_b32 s17, s55, 0xffff\ns_mov_b32 s2, 0xc048000\ns_load_dword s15, s[54:55], s2\nv_and_b32 v48, 15, v0\n"
"v_lshrrev_b32 v49, 4, v0\nv_lshrrev_b32 v50, 1, v48\nv_xor_b32 v50, v50, v49\nv_lshlrev_b32 v50, 4, v50\nv_lshl_or_b32 v50, v48, 7, v50\ns_lshr_b32 s2, s11, 1\n"
"s_lshl_b32 s2, s2, 12\nv_add_u32 v10, s2, v50\nv_xor_b32 v11, 64, v10\nv_lshrrev_b32 v50, 2, v48\nv_lshl_add_u32 v51, v49, 3, v50\nv_and_b32 v52, 1, v49\n"
"v_lshl_or_b32 v50, v52, 2, v50\nv_lshlrev_b32 v50, 5, v50\nv_lshl_or_b32 v50, v51, 9, v50\nv_and_b32 v52, 3, v0\nv_lshl_or_b32 v50, v52, 3, v50\ns_and_b32 s2, s11, 1\n"
"s_lshl_b32 s2, s2, 8\ns_add_u32 s2, s2, 0x10000\nv_add_u32 v12, s2, v50\nv_xor_b32 v13, 32, v12\nv_xor_b32 v14, 64, v12\nv_xor_b32 v15, 96, v12\nv_xor_b32 v16, 128, v12\n"
"v_xor_b32 v17, 160, v12\nv_xor_b32 v18, 192, v12\nv_xor_b32 v19, 224, v12\nv_lshrrev_b32 v24, 3, v0\nv_lshlrev_b32 v24, 13, v24\nv_and_b32 v26, 7, v0\n"
"v_lshl_or_b32 v24, v26, 7, v24\nv_lshrrev_b32 v26, 4, v0\nv_lshl_or_b32 v24, v26, 28, v24\nv_lshrrev_b32 v26, 5, v0\nv_lshlrev_b32 v26, 30, v26\nv_lshl_or_b32 v26, v0, 7, v26\n"
"s_and_b32 s2, s13, 6\ns_and_b32 s3, s12, 1\ns_add_u32 s2, s2, s3\ns_lshl2_add_u32 s2, s2, s11\ns_lshl_b32 s2, s2, 14\ns_lshl_b32 s3, s73, 10\ns_add_u32 s2, s2, s3\n"
"s_xor_b32 s3, s14, 1\ns_lshl_b32 s3, s3, 30\ns_add_u32 s2, s2, s3\ns_add_u32 s96, s2, 1048576\ns_and_b32 s2, s10, 127\ns_lshl2_add_u32 s2, s2, s11\ns_lshl_b32 s97, s2, 12\n"
"s_getpc_b64 s[0:1]\n.PC9:\ns_mov_b32 s2, .PA2-.PC9\ns_mov_b32 s3, .PA4-.PC9\ns_bitcmp1_b32 s13, 0\ns_cselect_b32 s2, .PA3-.PC9, s2\ns_cselect_b32 s3, .PA5-.PC9, s3\n"
"s_cmp_eq_u32 s14, 0\ns_cselect_b32 s2, s2, s3\ns_add_u32 s0, s0, s2\ns_addc_u32 s1, s1, 0\ns_setpc_b64 s[0:1]\n.p2align 12\n.PA2:\nv_add_u32 v128, 0x10000, v12\n"
"v_add_u32 v129, 0x10000, v13\nv_add_u32 v130, 0x10000, v14\nv_add_u32 v131, 0x10000, v15\nv_add_u32 v132, 0x10000, v16\nv_add_u32 v133, 0x10000, v17\nv_add_u32 v134, 0x10000, v18\n"
"v_add_u32 v135, 0x10000, v19\ns_add_u32 s2, s11, 1\ns_lshr_b32 s2, s2, 1\ns_lshl_b32 s2, s2, 12\nv_add_u32 v140, s2, v10\nv_add_u32 v141, s2, v11\nv_xor_b32 v142, 0x1000, v140\n"
"v_xor_b32 v143, 0x1000, v141\ns_add_u32 m0, s47, 0\ns_add_u32 s46, s51, 524288\nD0 6,32,46,0\nD0 7,32,46,1024\nD0 8,32,46,2048\nD0 9,32,46,3072\ns_add_u32 m0, s47, -49152\n"
"s_add_u32 s46, s52, 128\nD0 136,28,46,0\nD0 137,28,46,1024\nD0 138,28,46,2048\nD0 139,28,46,3072\ns_add_u32 m0, s47, 16384\ns_add_u32 s46, s51, 786432\nD0 6,32,46,0\n"
"D0 7,32,46,1024\nD0 8,32,46,2048\nD0 9,32,46,3072\nv_mov_b32 v116, 0x40000000\nv_cmp_gt_u32 vcc, 56, v0\ns_lshl2_add_u32 s2, s10, s11\ns_mul_i32 s2, s2, 1792\n"
"v_cndmask_b32 v117, v116, v1, vcc\ns_mov_b32 s36, s56\ns_and_b32 s37, s57, 0xffff\ns_mov_b32 s38, 0x380\ns_mov_b32 s39, 0x20000\ns_mov_b32 s40, s58\ns_and_b32 s41, s59, 0xffff\n"
"s_mov_b32 s42, 0x380\ns_mov_b32 s43, 0x20000\nbuffer_load_dwordx4 v[96:99], v117, s[28:31], s2 offen offset:0\nbuffer_load_dwordx4 v[100:103], v117, s[28:31], s2 offen offset:896\n"
"buffer_load_dwordx4 v[104:107], v117, s[36:39], 0 offen offset:0\nbuffer_load_dwordx4 v[108:111], v117, s[40:43], 0 offen offset:0\ns_add_u32 m0, s47, 32768\n"
"s_add_u32 s46, s51, 1048576\nD0 6,32,46,0\nD0 7,32,46,1024\nD0 8,32,46,2048\nD0 9,32,46,3072\ns_add_u32 m0, s47, -32768\ns_add_u32 s46, s52, 256\nD0 136,28,46,0\n"
"D0 137,28,46,1024\nD0 138,28,46,2048\nD0 139,28,46,3072\nWL 0\ns_mul_i32 s15, s15, 1664525\ns_add_u32 s15, s15, 1013904223\nv_mov_b32 v25, s15\ns_mov_b32 s57, 0x3db8aa3b\n"
"s_and_b32 s59, s11, 1\ns_lshl_b32 s59, s59, 13\ns_mul_i32 s2, s73, 25165824\ns_and_b32 s3, s12, 1\ns_lshl_b32 s3, s3, 21\ns_add_u32 s2, s2, s3\ns_lshl2_add_u32 s3, s13, s11\n"
"s_lshl_b32 s3, s3, 15\ns_add_u32 s2, s2, s3\ns_add_u32 s56, s2, 0x800000\ns_xor_b32 s53, s56, 0xc000\nWV 24\ns_barrier\nRa 192,140,0\nRa 196,141,0\nRa 200,140,2048\n"
"Ra 204,141,2048\nRa 208,142,0\nRa 212,143,0\nRa 216,142,2048\nRa 220,143,2048\nTa 160,128,0\nTa 162,128,2048\nTa 164,128,16384\nTa 166,128,18432\nTa 168,129,0\nTa 170,129,2048\n"
"Ta 172,129,16384\nTa 174,129,18432\nTa 176,130,0\nTa 178,130,2048\nTa 180,130,16384\nTa 182,130,18432\nTa 184,131,0\nTa 186,131,2048\nTa 188,131,16384\nTa 190,131,18432\nWL 0\n"
"Zvaa 32,160,192\nTa 224,132,0\ns_add_u32 m0, s47, 49152\ns_add_u32 s46, s51, 1310720\nMvaa 32,164,196\nTa 226,132,2048\nZaaa 96,164,204\nD0 6,32,46,0\nMaaa 96,160,200\n"
"Ta 228,132,16384\nZvaa 192,160,208\nTa 230,132,18432\nMvaa 192,164,212\nZvaa 224,164,220\nTa 232,133,0\nMvaa 224,160,216\nTa 234,133,2048\nD0 7,32,46,1024\nZvaa 228,168,216\n"
"Mvaa 228,172,220\nTa 236,133,16384\nZvaa 196,172,212\nTa 238,133,18432\nMvaa 196,168,208\nZaaa 100,168,200\nTa 240,134,0\nD0 8,32,46,2048\nMaaa 100,172,204\nTa 242,134,2048\n"
"Zvaa 36,172,196\nMvaa 36,168,192\nTa 244,134,16384\nZvaa 40,176,192\nTa 246,134,18432\nMvaa 40,180,196\nD0 9,32,46,3072\nZaaa 104,180,204\nTa 248,135,0\nMaaa 104,176,200\n"
"Ta 250,135,2048\nZvaa 200,176,208\ns_add_u32 m0, s47, -16384\ns_add_u32 s46, s52, 384\nMvaa 200,180,212\nTa 252,135,16384\nZvaa 232,180,220\nTa 254,135,18432\nD0 136,28,46,0\n"
"Mvaa 232,176,216\nZvaa 236,184,216\nMvaa 236,188,220\nZvaa 204,188,212\nMvaa 204,184,208\nD0 137,28,46,1024\nZaaa 108,184,200\nMaaa 108,188,204\nZvaa 44,188,196\nMvaa 44,184,192\n"
"s_waitcnt vmcnt(18) lgkmcnt(0)\ns_barrier\nZvaa 48,224,192\nRa 128,140,16384\nD0 138,28,46,2048\nMvaa 48,228,196\nRa 132,141,16384\nZaaa 112,228,204\nRa 136,140,18432\n"
"Maaa 112,224,200\nRa 140,141,18432\nZvaa 208,224,208\nRa 144,142,16384\nWV 15\nMvaa 208,228,212\nRa 148,143,16384\nD0 139,28,46,3072\nZvaa 240,228,220\nRa 152,142,18432\n"
"Mvaa 240,224,216\nRa 156,143,18432\nZvaa 244,232,216\nTa 160,12,0\ns_add_u32 m0, s47, 65536\ns_add_u32 s46, s51, 1572864\nMvaa 244,236,220\nTa 162,12,2048\nZvaa 212,236,212\n"
"Ta 164,12,16384\nD0 6,32,46,0\nMvaa 212,232,208\nTa 166,12,18432\nZaaa 116,232,200\nTa 168,13,0\nv_lshlrev_b32 v112, 16, v96\nMaaa 116,236,204\nTa 170,13,2048\nZvaa 52,236,196\n"
"Ta 172,13,16384\nMvaa 52,232,192\nTa 174,13,18432\nD0 7,32,46,1024\nZvaa 56,240,192\nTa 176,14,0\nv_lshlrev_b32 v113, 16, v100\nMvaa 56,244,196\nTa 178,14,2048\nZaaa 120,244,204\n"
"Ta 180,14,16384\nMaaa 120,240,200\nTa 182,14,18432\nZvaa 216,240,208\nTa 184,15,0\nD0 8,32,46,2048\nMvaa 216,244,212\nTa 186,15,2048\nZvaa 248,244,220\nTa 188,15,16384\n"
"Mvaa 248,240,216\nTa 190,15,18432\nZvaa 252,248,216\nv_lshlrev_b32 v114, 16, v104\nv_lshlrev_b32 v115, 16, v108\nMvaa 252,252,220\nD0 9,32,46,3072\nZvaa 220,252,212\n"
"MU 116,112,114\nMvaa 220,248,208\nMU 117,113,114\nZaaa 124,248,200\ns_add_u32 m0, s47, 81920\ns_add_u32 s46, s51, 1835008\nMaaa 124,252,204\nCV 116,116,117\nZvaa 60,252,196\n"
"D0 6,32,46,0\nMvaa 60,248,192\nWL 0\nMvaa 32,160,128\nTa 224,16,0\nv_lshlrev_b32 v118, 16, v116\nMvaa 32,164,132\nTa 226,16,2048\nMaaa 96,164,140\n"
"v_and_b32 v119, 0xffff0000, v116\nMaaa 96,160,136\nTa 228,16,16384\nD0 7,32,46,1024\nMvaa 192,160,144\nTa 230,16,18432\nMU 120,112,115\nMvaa 192,164,148\nMU 121,113,115\n"
"Mvaa 224,164,156\nTa 232,17,0\nMvaa 224,160,152\nTa 234,17,2048\nMvaa 228,168,152\nD0 8,32,46,2048\nv_and_b32 v112, 0xffff0000, v96\nMvaa 228,172,156\nTa 236,17,16384\n"
"Mvaa 196,172,148\nTa 238,17,18432\nMvaa 196,168,144\nv_and_b32 v113, 0xffff0000, v100\nMaaa 100,168,136\nTa 240,18,0\nv_and_b32 v114, 0xffff0000, v104\nMaaa 100,172,140\n"
"Ta 242,18,2048\nD0 9,32,46,3072\nMvaa 36,172,132\nv_and_b32 v115, 0xffff0000, v108\nMvaa 36,168,128\nTa 244,18,16384\nMvaa 40,176,128\nTa 246,18,18432\nMU 116,112,114\n"
"Mvaa 40,180,132\nMU 117,113,114\nMaaa 104,180,140\nTa 248,19,0\nMaaa 104,176,136\nTa 250,19,2048\nMvaa 200,176,144\nCV 116,116,117\nv_and_b32 v117, 0xffff0000, v116\n"
"Mvaa 200,180,148\nTa 252,19,16384\nMvaa 232,180,156\nTa 254,19,18432\nMvaa 232,176,152\nv_lshlrev_b32 v116, 16, v116\nMvaa 236,184,152\nFM 120,112,115\nFM 121,113,115\n"
"Mvaa 236,188,156\nAD 118,118,116\nMvaa 204,188,148\nAD 119,119,117\nMvaa 204,184,144\nv_lshlrev_b32 v112, 16, v97\nMaaa 108,184,136\nv_lshlrev_b32 v113, 16, v101\n"
"v_lshlrev_b32 v114, 16, v105\nMaaa 108,188,140\nv_lshlrev_b32 v115, 16, v109\nMvaa 44,188,132\nMU 116,112,114\nMvaa 44,184,128\ns_waitcnt vmcnt(12) lgkmcnt(0)\ns_barrier\n"
"Mvaa 48,224,128\nRa 192,140,32768\ns_add_u32 m0, s47, -49152\ns_add_u32 s46, s52, 512\nMvaa 48,228,132\nRa 196,141,32768\nMaaa 112,228,140\nRa 200,140,34816\nD0 136,28,46,0\n"
"Maaa 112,224,136\nRa 204,141,34816\nMvaa 208,224,144\nRa 208,142,32768\nMU 117,113,114\nMvaa 208,228,148\nRa 212,143,32768\nMvaa 240,228,156\nRa 216,142,34816\nMvaa 240,224,152\n"
"Ra 220,143,34816\nD0 137,28,46,1024\nMvaa 244,232,152\nTa 160,12,32768\nCV 116,116,117\nMvaa 244,236,156\nTa 162,12,34816\nMvaa 212,236,148\nTa 164,12,49152\nMvaa 212,232,144\n"
"Ta 166,12,51200\nMaaa 116,232,136\nTa 168,13,32768\nD0 138,28,46,2048\nMaaa 116,236,140\nTa 170,13,34816\nMvaa 52,236,132\nTa 172,13,49152\nMvaa 52,232,128\nTa 174,13,51200\n"
"Mvaa 56,240,128\nTa 176,14,32768\nv_and_b32 v117, 0xffff0000, v116\nMvaa 56,244,132\nTa 178,14,34816\nD0 139,28,46,3072\nMaaa 120,244,140\nTa 180,14,49152\nMaaa 120,240,136\n"
"Ta 182,14,51200\nMvaa 216,240,144\nTa 184,15,32768\ns_add_u32 m0, s47, 0\ns_add_u32 s46, s51, 2097152\nMvaa 216,244,148\nTa 186,15,34816\nMvaa 248,244,156\nTa 188,15,49152\n"
"D0 6,32,46,0\nMvaa 248,240,152\nTa 190,15,51200\nMvaa 252,248,152\nv_lshlrev_b32 v116, 16, v116\nFM 120,112,115\nMvaa 252,252,156\nFM 121,113,115\nMvaa 220,252,148\n"
"AD 118,118,116\nMvaa 220,248,144\nD0 7,32,46,1024\nMaaa 124,248,136\nAD 119,119,117\nv_and_b32 v112, 0xffff0000, v97\nMaaa 124,252,140\nv_and_b32 v113, 0xffff0000, v101\n"
"Mvaa 60,252,132\nv_and_b32 v114, 0xffff0000, v105\nMvaa 60,248,128\nWL 0\nMvaa 32,160,192\nTa 224,16,32768\nD0 8,32,46,2048\nMvaa 32,164,196\nTa 226,16,34816\nMaaa 96,164,204\n"
"v_and_b32 v115, 0xffff0000, v109\nMaaa 96,160,200\nTa 228,16,49152\nMvaa 192,160,208\nTa 230,16,51200\nMU 116,112,114\nMvaa 192,164,212\nD0 9,32,46,3072\nMvaa 224,164,220\n"
"Ta 232,17,32768\nMvaa 224,160,216\nTa 234,17,34816\nMvaa 228,168,216\ns_add_u32 m0, s47, 16384\ns_add_u32 s46, s51, 2359296\nMvaa 228,172,220\nTa 236,17,49152\nMvaa 196,172,212\n"
"Ta 238,17,51200\nD0 6,32,46,0\nMvaa 196,168,208\nMU 117,113,114\nMaaa 100,168,200\nTa 240,18,32768\nCV 116,116,117\nMaaa 100,172,204\nTa 242,18,34816\nMvaa 36,172,196\n"
"v_and_b32 v117, 0xffff0000, v116\nMvaa 36,168,192\nTa 244,18,49152\nD0 7,32,46,1024\nMvaa 40,176,192\nTa 246,18,51200\nv_lshlrev_b32 v116, 16, v116\nMvaa 40,180,196\n"
"FM 120,112,115\nMaaa 104,180,204\nTa 248,19,32768\nMaaa 104,176,200\nTa 250,19,34816\nMvaa 200,176,208\nD0 8,32,46,2048\nFM 121,113,115\nMvaa 200,180,212\nTa 252,19,49152\n"
"Mvaa 232,180,220\nTa 254,19,51200\nMvaa 232,176,216\nAD 118,118,116\nMvaa 236,184,216\nAD 119,119,117\nv_lshlrev_b32 v112, 16, v98\nMvaa 236,188,220\nD0 9,32,46,3072\n"
"Mvaa 204,188,212\nv_lshlrev_b32 v113, 16, v102\nMvaa 204,184,208\nv_lshlrev_b32 v114, 16, v106\nMaaa 108,184,200\nv_lshlrev_b32 v115, 16, v110\nMU 116,112,114\nMaaa 108,188,204\n"
"MU 117,113,114\nMvaa 44,188,196\nCV 116,116,117\nMvaa 44,184,192\ns_waitcnt vmcnt(12) lgkmcnt(0)\ns_barrier\nMvaa 48,224,192\nRa 128,140,49152\ns_add_u32 m0, s47, -32768\n"
"s_add_u32 s46, s52, 640\nMvaa 48,228,196\nRa 132,141,49152\nMaaa 112,228,204\nRa 136,140,51200\nD0 136,28,46,0\nMaaa 112,224,200\nRa 140,141,51200\nMvaa 208,224,208\n"
"Ra 144,142,49152\nv_and_b32 v117, 0xffff0000, v116\nMvaa 208,228,212\nRa 148,143,49152\nMvaa 240,228,220\nRa 152,142,51200\nMvaa 240,224,216\nRa 156,143,51200\nD0 137,28,46,1024\n"
"Mvaa 244,232,216\nTa 160,128,0\nv_lshlrev_b32 v116, 16, v116\nMvaa 244,236,220\nTa 162,128,2048\nMvaa 212,236,212\nTa 164,128,16384\nMvaa 212,232,208\nTa 166,128,18432\n"
"Maaa 116,232,200\nTa 168,129,0\nD0 138,28,46,2048\nMaaa 116,236,204\nTa 170,129,2048\nMvaa 52,236,196\nTa 172,129,16384\nMvaa 52,232,192\nTa 174,129,18432\nMvaa 56,240,192\n"
"Ta 176,130,0\nFM 120,112,115\nMvaa 56,244,196\nTa 178,130,2048\nD0 139,28,46,3072\nMaaa 120,244,204\nTa 180,130,16384\nMaaa 120,240,200\nTa 182,130,18432\nMvaa 216,240,208\n"
"Ta 184,131,0\ns_add_u32 m0, s47, 32768\ns_add_u32 s46, s51, 2621440\nMvaa 216,244,212\nTa 186,131,2048\nMvaa 248,244,220\nTa 188,131,16384\nD0 6,32,46,0\nMvaa 248,240,216\n"
"Ta 190,131,18432\nMvaa 252,248,216\nFM 121,113,115\nAD 118,118,116\nMvaa 252,252,220\nAD 119,119,117\nMvaa 220,252,212\nv_and_b32 v112, 0xffff0000, v98\nMvaa 220,248,208\n"
"D0 7,32,46,1024\nMaaa 124,248,200\nv_and_b32 v113, 0xffff0000, v102\nv_and_b32 v114, 0xffff0000, v106\nMaaa 124,252,204\nv_and_b32 v115, 0xffff0000, v110\nMvaa 60,252,196\n"
"MU 116,112,114\nMvaa 60,248,192\nWL 0\nMvaa 32,160,128\nTa 224,132,0\nD0 8,32,46,2048\nMvaa 32,164,132\nTa 226,132,2048\nMaaa 96,164,140\nMU 117,113,114\nMaaa 96,160,136\n"
"Ta 228,132,16384\nMvaa 192,160,144\nTa 230,132,18432\nCV 116,116,117\nMvaa 192,164,148\nD0 9,32,46,3072\nMvaa 224,164,156\nTa 232,133,0\nMvaa 224,160,152\nTa 234,133,2048\n"
"Mvaa 228,168,152\ns_add_u32 m0, s47, 49152\ns_add_u32 s46, s51, 2883584\nMvaa 228,172,156\nTa 236,133,16384\nMvaa 196,172,148\nTa 238,133,18432\nD0 6,32,46,0\nMvaa 196,168,144\n"
"v_and_b32 v117, 0xffff0000, v116\nMaaa 100,168,136\nTa 240,134,0\nv_lshlrev_b32 v116, 16, v116\nMaaa 100,172,140\nTa 242,134,2048\nMvaa 36,172,132\nFM 120,112,115\n"
"Mvaa 36,168,128\nTa 244,134,16384\nD0 7,32,46,1024\nMvaa 40,176,128\nTa 246,134,18432\nFM 121,113,115\nMvaa 40,180,132\nAD 118,118,116\nMaaa 104,180,140\nTa 248,135,0\n"
"Maaa 104,176,136\nTa 250,135,2048\nMvaa 200,176,144\nD0 8,32,46,2048\nAD 119,119,117\nMvaa 200,180,148\nTa 252,135,16384\nMvaa 232,180,156\nTa 254,135,18432\nMvaa 232,176,152\n"
"v_lshlrev_b32 v112, 16, v99\nMvaa 236,184,152\nv_lshlrev_b32 v113, 16, v103\nv_lshlrev_b32 v114, 16, v107\nMvaa 236,188,156\nD0 9,32,46,3072\nMvaa 204,188,148\n"
"v_lshlrev_b32 v115, 16, v111\nMvaa 204,184,144\nMU 116,112,114\nMaaa 108,184,136\nMU 117,113,114\nCV 116,116,117\ns_and_b32 s78, s74, 3\ns_lshl2_add_u32 s78, s73, s78\n"
"s_lshl_b32 s78, s78, 12\ns_bitcmp1_b32 s11, 0\ns_cselect_b32 s79, s48, s45\ns_add_u32 s79, s79, 512\ns_and_b32 s79, s79, 0xfffe0000\ns_add_u32 s78, s78, s79\n"
"buffer_load_dword v31, v26, s[20:23], s78 offen\nMaaa 108,188,140\nv_and_b32 v117, 0xffff0000, v116\nMvaa 44,188,132\nv_lshlrev_b32 v116, 16, v116\nMvaa 44,184,128\n"
"s_waitcnt vmcnt(13) lgkmcnt(0)\ns_barrier\nMvaa 48,224,128\nRa 192,140,16384\ns_add_u32 m0, s47, -16384\ns_add_u32 s46, s52, 768\nMvaa 48,228,132\nRa 196,141,16384\n"
"Maaa 112,228,140\nRa 200,140,18432\nD0 136,28,46,0\nMaaa 112,224,136\nRa 204,141,18432\nMvaa 208,224,144\nRa 208,142,16384\nFM 120,112,115\nMvaa 208,228,148\nRa 212,143,16384\n"
"Mvaa 240,228,156\nRa 216,142,18432\nMvaa 240,224,152\nRa 220,143,18432\nD0 137,28,46,1024\nMvaa 244,232,152\nTa 160,12,0\nFM 121,113,115\nMvaa 244,236,156\nTa 162,12,2048\n"
"Mvaa 212,236,148\nTa 164,12,16384\nMvaa 212,232,144\nTa 166,12,18432\nMaaa 116,232,136\nTa 168,13,0\nD0 138,28,46,2048\nMaaa 116,236,140\nTa 170,13,2048\nMvaa 52,236,132\n"
"Ta 172,13,16384\nMvaa 52,232,128\nTa 174,13,18432\nMvaa 56,240,128\nTa 176,14,0\nAD 118,118,116\nMvaa 56,244,132\nTa 178,14,2048\nD0 139,28,46,3072\nMaaa 120,244,140\n"
"Ta 180,14,16384\nMaaa 120,240,136\nTa 182,14,18432\nMvaa 216,240,144\nTa 184,15,0\ns_add_u32 m0, s47, 65536\ns_add_u32 s46, s51, 3145728\nMvaa 216,244,148\nTa 186,15,2048\n"
"Mvaa 248,244,156\nTa 188,15,16384\nD0 6,32,46,0\nMvaa 248,240,152\nTa 190,15,18432\nMvaa 252,248,152\nAD 119,119,117\nv_and_b32 v112, 0xffff0000, v99\nMvaa 252,252,156\n"
"v_and_b32 v113, 0xffff0000, v103\nMvaa 220,252,148\nv_and_b32 v114, 0xffff0000, v107\nMvaa 220,248,144\nD0 7,32,46,1024\nMaaa 124,248,136\nv_and_b32 v115, 0xffff0000, v111\n"
"MU 116,112,114\nMaaa 124,252,140\nMU 117,113,114\nMvaa 60,252,132\nCV 116,116,117\nMvaa 60,248,128\nWL 0\nMvaa 32,160,192\nTa 224,16,0\nD0 8,32,46,2048\nMvaa 32,164,196\n"
"Ta 226,16,2048\nMaaa 96,164,204\nv_and_b32 v117, 0xffff0000, v116\nMaaa 96,160,200\nTa 228,16,16384\nMvaa 192,160,208\nTa 230,16,18432\nv_lshlrev_b32 v116, 16, v116\n"
"Mvaa 192,164,212\nD0 9,32,46,3072\nMvaa 224,164,220\nTa 232,17,0\nMvaa 224,160,216\nTa 234,17,2048\nMvaa 228,168,216\ns_add_u32 m0, s47, 81920\ns_add_u32 s46, s51, 3407872\n"
"Mvaa 228,172,220\nTa 236,17,16384\nMvaa 196,172,212\nTa 238,17,18432\nD0 6,32,46,0\nMvaa 196,168,208\nFM 120,112,115\nMaaa 100,168,200\nTa 240,18,0\nFM 121,113,115\n"
"Maaa 100,172,204\nTa 242,18,2048\nMvaa 36,172,196\nAD 118,118,116\nMvaa 36,168,192\nTa 244,18,16384\nD0 7,32,46,1024\nMvaa 40,176,192\nTa 246,18,18432\nAD 119,119,117\n"
"Mvaa 40,180,196\ns_nop 0\nv_permlane32_swap_b32 v118, v119\nMaaa 104,180,204\nTa 248,19,0\nMaaa 104,176,200\nTa 250,19,2048\nMvaa 200,176,208\nD0 8,32,46,2048\n"
"v_permlane32_swap_b32 v120, v121\nMvaa 200,180,212\nTa 252,19,16384\nMvaa 232,180,220\nTa 254,19,18432\nMvaa 232,176,216\nAD 118,118,119\nMvaa 236,184,216\nAD 120,120,121\n"
"Mvaa 236,188,220\nD0 9,32,46,3072\nMvaa 204,188,212\nv_permlane16_swap_b32 v118, v120\nMvaa 204,184,208\nAD 118,118,120\nMaaa 108,184,200\nMaaa 108,188,204\n"
"v_add_f32_dpp v118, v118, v118 row_ror:8 row_mask:0xf bank_mask:0xf\nMvaa 44,188,196\nMvaa 44,184,192\ns_waitcnt vmcnt(13) lgkmcnt(0)\ns_barrier\nMvaa 48,224,192\n"
"Ra 128,140,32768\ns_add_u32 m0, s47, -65536\ns_add_u32 s45, s45, 128\nMvaa 48,228,196\nRa 132,141,32768\nMaaa 112,228,204\nRa 136,140,34816\nD0 2,20,45,0\nMaaa 112,224,200\n"
"Ra 140,141,34816\nMvaa 208,224,208\nRa 144,142,32768\nv_add_f32_dpp v118, v118, v118 row_ror:4 row_mask:0xf bank_mask:0xf\nMvaa 208,228,212\nRa 148,143,32768\nMvaa 240,228,220\n"
"Ra 152,142,34816\nMvaa 240,224,216\nRa 156,143,34816\nD0 3,20,45,1024\nMvaa 244,232,216\nTa 160,12,32768\nv_add_f32_dpp v118, v118, v118 row_ror:2 row_mask:0xf bank_mask:0xf\n"
"Mvaa 244,236,220\nTa 162,12,34816\nMvaa 212,236,212\nTa 164,12,49152\nMvaa 212,232,208\nTa 166,12,51200\nMaaa 116,232,200\nTa 168,13,32768\nD0 4,20,45,2048\nMaaa 116,236,204\n"
"Ta 170,13,34816\nMvaa 52,236,196\nTa 172,13,49152\nMvaa 52,232,192\nTa 174,13,51200\nMvaa 56,240,192\nTa 176,14,32768\n"
"v_add_f32_dpp v118, v118, v118 row_ror:1 row_mask:0xf bank_mask:0xf\nMvaa 56,244,196\nTa 178,14,34816\nD0 5,20,45,3072\nMaaa 120,244,204\nTa 180,14,49152\nMaaa 120,240,200\n"
"Ta 182,14,51200\nMvaa 216,240,208\nTa 184,15,32768\ns_add_u32 m0, s47, 0\ns_add_u32 s44, s44, 0x40000\nMvaa 216,244,212\nTa 186,15,34816\nMvaa 248,244,220\nTa 188,15,49152\n"
"D0 6,24,44,0\nMvaa 248,240,216\nTa 190,15,51200\nMvaa 252,248,216\nCV 118,118,118\nv_lshlrev_b32 v118, 16, v118\nMvaa 252,252,220\nv_mul_f32 v119, 0xbfb8aa3b, v118\nEX 119,119\n"
"Mvaa 220,252,212\nv_add_f32 v120, 0, v118\nMvaa 220,248,208\nD0 7,24,44,1024\nMaaa 124,248,200\nv_add_f32 v119, 1.0, v119\nMaaa 124,252,204\nv_rcp_f32 v119, v119\nMvaa 60,252,196\n"
"v_ashrrev_i32 v121, 31, v120\nMvaa 60,248,192\nWL 0\nMvaa 32,160,128\nTa 224,16,32768\nD0 8,24,44,2048\nMvaa 32,164,132\nTa 226,16,34816\nMaaa 96,164,140\nCV 119,119,119\n"
"Maaa 96,160,136\nTa 228,16,49152\nMvaa 192,160,144\nTa 230,16,51200\nv_lshlrev_b32 v119, 16, v119\nMvaa 192,164,148\nD0 9,24,44,3072\nMvaa 224,164,156\nTa 232,17,32768\n"
"Mvaa 224,160,152\nTa 234,17,34816\nMvaa 228,168,152\ns_add_u32 m0, s47, 16384\ns_add_u32 s44, s44, 0x40000\nMvaa 228,172,156\nTa 236,17,49152\nMvaa 196,172,148\nTa 238,17,51200\n"
"D0 6,24,44,0\nMvaa 196,168,144\nv_or_b32 v121, 0x80000000, v121\nMaaa 100,168,136\nTa 240,18,32768\nv_xor_b32 v120, v120, v121\nMaaa 100,172,140\nTa 242,18,34816\nMvaa 36,172,132\n"
"v_and_b32 v120, 0xffff0000, v120\nMvaa 36,168,128\nTa 244,18,49152\nD0 7,24,44,1024\nMvaa 40,176,128\nTa 246,18,51200\ns_lshl2_add_u32 s2, s10, s11\nMvaa 40,180,132\n"
"v_lshrrev_b32 v112, 5, v0\nMaaa 104,180,140\nTa 248,19,32768\nMaaa 104,176,136\nTa 250,19,34816\nMvaa 200,176,144\nD0 8,24,44,2048\nv_lshl_add_u32 v112, s2, 1, v112\n"
"Mvaa 200,180,148\nTa 252,19,49152\nMvaa 232,180,156\nTa 254,19,51200\nMvaa 232,176,152\nv_sub_u32 v113, 0xffff, v112\nMvaa 236,184,152\nv_or_b32 v120, v120, v113\n"
"v_mov_b32 v121, s15\nMvaa 236,188,156\nD0 9,24,44,3072\nMvaa 204,188,148\nv_and_b32 v113, 31, v0\nMvaa 204,184,144\nv_mov_b32 v116, 0x40000000\nv_cmp_eq_u32 vcc, 16, v113\n"
"v_lshlrev_b32 v114, 2, v112\nv_lshlrev_b32 v115, 10, v113\nv_cndmask_b32 v114, v116, v114, vcc\nv_cndmask_b32 v120, v119, v120, vcc\nMaaa 108,184,136\n"
"v_lshl_add_u32 v115, v112, 3, v115\nv_and_b32 v113, 15, v0\nMaaa 108,188,140\nv_cmp_eq_u32 vcc, 0, v113\ns_mov_b32 s2, 0xc040000\ns_mov_b32 s3, 0xc03e000\n"
"v_cndmask_b32 v115, v116, v115, vcc\nbuffer_store_dwordx2 v[120:121], v115, s[16:19], s2 offen sc0 sc1\nbuffer_store_dword v118, v114, s[16:19], s3 offen sc0 sc1\nMvaa 44,188,132\n"
"Mvaa 44,184,128\ns_waitcnt vmcnt(14) lgkmcnt(0)\ns_barrier\nMvaa 48,224,128\nRa 192,140,49152\ns_add_u32 m0, s47, -49152\ns_add_u32 s48, s48, 128\nMvaa 48,228,132\n"
"Ra 196,141,49152\nMaaa 112,228,140\nRa 200,140,51200\nD0 2,20,48,0\nMaaa 112,224,136\nRa 204,141,51200\nMvaa 208,224,144\nRa 208,142,49152\nMvaa 208,228,148\nRa 212,143,49152\n"
"Mvaa 240,228,156\nRa 216,142,51200\nMvaa 240,224,152\nRa 220,143,51200\nD0 3,20,48,1024\nMvaa 244,232,152\nTa 160,128,0\nMvaa 244,236,156\nTa 162,128,2048\nMvaa 212,236,148\n"
"Ta 164,128,16384\nMvaa 212,232,144\nTa 166,128,18432\nMaaa 116,232,136\nTa 168,129,0\nD0 4,20,48,2048\nMaaa 116,236,140\nTa 170,129,2048\nMvaa 52,236,132\nTa 172,129,16384\n"
"Mvaa 52,232,128\nTa 174,129,18432\nMvaa 56,240,128\nTa 176,130,0\nMvaa 56,244,132\nTa 178,130,2048\nD0 5,20,48,3072\nMaaa 120,244,140\nTa 180,130,16384\nMaaa 120,240,136\n"
"Ta 182,130,18432\nMvaa 216,240,144\nTa 184,131,0\ns_add_u32 m0, s47, -32768\ns_add_u32 s45, s45, 128\nMvaa 216,244,148\nTa 186,131,2048\nMvaa 248,244,156\nTa 188,131,16384\n"
"D0 2,20,45,0\nMvaa 248,240,152\nTa 190,131,18432\nMvaa 252,248,152\nMvaa 252,252,156\nMvaa 220,252,148\nMvaa 220,248,144\nD0 3,20,45,1024\nMaaa 124,248,136\nMaaa 124,252,140\n"
"Mvaa 60,252,132\nMvaa 60,248,128\nWL 0\nMvaa 32,160,192\nTa 224,132,0\nD0 4,20,45,2048\nMvaa 32,164,196\nTa 226,132,2048\nMvaa 36,172,196\nMvaa 36,168,192\nTa 228,132,16384\n"
"Mvaa 40,176,192\nTa 230,132,18432\nMvaa 40,180,196\nD0 5,20,45,3072\nMvaa 44,188,196\nTa 232,133,0\nMvaa 44,184,192\nTa 234,133,2048\nMaaa 108,184,200\ns_add_u32 m0, s47, 32768\n"
"s_add_u32 s44, s44, 0x40000\nMaaa 108,188,204\nTa 236,133,16384\nMaaa 104,180,204\nTa 238,133,18432\nD0 6,24,44,0\nMaaa 104,176,200\nv_mul_f32 v32, s57, v32\n"
"v_mul_f32 v33, s57, v33\nMaaa 100,168,200\nTa 240,134,0\nCV 32,32,33\nMaaa 100,172,204\nTa 242,134,2048\nv_mul_f32 v34, s57, v34\nMaaa 96,164,204\nv_mul_f32 v35, s57, v35\n"
"CV 33,34,35\nMaaa 96,160,200\nTa 244,134,16384\nD0 7,24,44,1024\nMvaa 192,160,208\nTa 246,134,18432\nv_mul_f32 v36, s57, v36\nMvaa 192,164,212\nv_mul_f32 v37, s57, v37\n"
"Mvaa 196,172,212\nTa 248,135,0\nCV 34,36,37\nMvaa 196,168,208\nTa 250,135,2048\nv_mul_f32 v38, s57, v38\nMvaa 200,176,208\nD0 8,24,44,2048\nv_mul_f32 v39, s57, v39\n"
"Mvaa 200,180,212\nTa 252,135,16384\nCV 35,38,39\nMvaa 204,188,212\nTa 254,135,18432\nv_mul_f32 v40, s57, v40\nMvaa 204,184,208\nv_mul_f32 v41, s57, v41\nCV 40,40,41\n"
"Mvaa 236,184,216\nv_mul_f32 v42, s57, v42\nv_mul_f32 v43, s57, v43\nMvaa 236,188,220\nD0 9,24,44,3072\nMvaa 232,180,220\nCV 41,42,43\nv_mul_f32 v44, s57, v44\nMvaa 232,176,216\n"
"v_mul_f32 v45, s57, v45\nCV 42,44,45\nMvaa 228,168,216\nv_mul_f32 v46, s57, v46\nv_mul_f32 v47, s57, v47\nMvaa 228,172,220\nCV 43,46,47\nAR 20,96\nMvaa 224,164,220\nAR 21,97\n"
"v_mul_f32 v20, s57, v20\nMvaa 224,160,216\ns_waitcnt vmcnt(14) lgkmcnt(0)\ns_barrier\nMvaa 48,224,192\ns_add_u32 m0, s47, 49152\ns_add_u32 s44, s44, 0x40000\nRa 128,10,0\n"
"Mvaa 48,228,196\nRa 132,11,0\nMvaa 52,236,196\nD0 6,24,44,0\nRa 136,10,2048\nMvaa 52,232,192\nRa 140,11,2048\nv_mul_f32 v21, s57, v21\nMvaa 56,240,192\nRa 144,10,8192\n"
"CV 36,20,21\nMvaa 56,244,196\nRa 148,11,8192\nAR 20,98\nMvaa 60,252,196\nD0 7,24,44,1024\nRa 152,10,10240\nMvaa 60,248,192\nRa 156,11,10240\nAR 21,99\nMaaa 124,248,200\n"
"Ta 160,12,0\nv_mul_f32 v20, s57, v20\nMaaa 124,252,204\nTa 162,12,2048\nMaaa 120,244,204\nD0 8,24,44,2048\nTa 164,12,16384\nMaaa 120,240,200\nTa 166,12,18432\n"
"v_mul_f32 v21, s57, v21\nMaaa 116,232,200\nTa 168,13,0\nCV 37,20,21\nMaaa 116,236,204\nTa 170,13,2048\nAR 20,100\nMaaa 112,228,204\nD0 9,24,44,3072\nTa 172,13,16384\n"
"Maaa 112,224,200\nTa 174,13,18432\nAR 21,101\nMvaa 208,224,208\ns_add_u32 m0, s47, -16384\ns_add_u32 s48, s48, 128\nTa 176,14,0\nMvaa 208,228,212\nTa 178,14,2048\n"
"Mvaa 212,236,212\nD0 2,20,48,0\nTa 180,14,16384\nMvaa 212,232,208\nTa 182,14,18432\nv_mul_f32 v20, s57, v20\nMvaa 216,240,208\nTa 184,15,0\nv_mul_f32 v21, s57, v21\n"
"Mvaa 216,244,212\nTa 186,15,2048\nCV 38,20,21\nMvaa 220,252,212\nD0 3,20,48,1024\nTa 188,15,16384\nMvaa 220,248,208\nTa 190,15,18432\nAR 20,102\nMvaa 252,248,216\nAR 21,103\n"
"v_mul_f32 v20, s57, v20\nMvaa 252,252,220\nv_mul_f32 v21, s57, v21\nMvaa 248,244,220\nD0 4,20,48,2048\nCV 39,20,21\nMvaa 248,240,216\nAR 20,104\nAR 21,105\nMvaa 244,232,216\n"
"v_mul_f32 v20, s57, v20\nv_mul_f32 v21, s57, v21\nMvaa 244,236,220\nCV 44,20,21\nAR 20,106\nMvaa 240,228,220\nD0 5,20,48,3072\nAR 21,107\nMvaa 240,224,216\nWL 0\ns_barrier\n"
"Zvaa 64,160,128\nTa 224,16,0\nv_mul_f32 v20, s57, v20\nMvaa 64,164,132\nTa 226,16,2048\nZvaa 96,164,140\ns_add_u32 m0, s47, -65536\ns_add_u32 s45, s45, 128\nMvaa 96,160,136\n"
"Ta 228,16,16384\nv_mul_f32 v21, s57, v21\nZvaa 128,160,144\nTa 230,16,18432\nCV 45,20,21\nMvaa 128,164,148\nD0 2,20,45,0\nAR 20,108\nZvaa 160,164,156\nTa 232,17,0\nAR 21,109\n"
"Mvaa 160,160,152\nTa 234,17,2048\nv_mul_f32 v20, s57, v20\nZvaa 164,168,152\nv_mul_f32 v21, s57, v21\nCV 46,20,21\nMvaa 164,172,156\nTa 236,17,16384\ns_add_u32 s6, s56, 0\n"
"Sv 32,1,16,6,0\nZvaa 132,172,148\nTa 238,17,18432\nAR 20,110\nMvaa 132,168,144\nAR 21,111\nv_mul_f32 v20, s57, v20\nZvaa 100,168,136\nTa 240,18,0\nv_mul_f32 v21, s57, v21\n"
"Mvaa 100,172,140\nTa 242,18,2048\nD0 3,20,45,1024\nZvaa 68,172,132\nCV 47,20,21\nv_mul_f32 v48, s57, v48\nMvaa 68,168,128\nTa 244,18,16384\nv_mul_f32 v49, s57, v49\n"
"Zvaa 72,176,128\nTa 246,18,18432\nCV 48,48,49\nMvaa 72,180,132\nv_mul_f32 v50, s57, v50\nZvaa 104,180,140\nTa 248,19,0\nv_mul_f32 v51, s57, v51\nMvaa 104,176,136\nTa 250,19,2048\n"
"CV 49,50,51\nZvaa 136,176,144\nv_mul_f32 v52, s57, v52\nv_mul_f32 v53, s57, v53\nMvaa 136,180,148\nTa 252,19,16384\nD0 4,20,45,2048\nZvaa 168,180,156\nTa 254,19,18432\n"
"CV 50,52,53\nMvaa 168,176,152\nv_mul_f32 v54, s57, v54\nv_mul_f32 v55, s57, v55\nZvaa 172,184,152\nCV 51,54,55\nv_mul_f32 v56, s57, v56\nMvaa 172,188,156\nv_mul_f32 v57, s57, v57\n"
"s_add_u32 s6, s56, 0\nSv 40,1,16,6,2048\nZvaa 140,188,148\nCV 56,56,57\nv_mul_f32 v58, s57, v58\nMvaa 140,184,144\nv_mul_f32 v59, s57, v59\nCV 57,58,59\nZvaa 108,184,136\n"
"v_mul_f32 v60, s57, v60\nv_mul_f32 v61, s57, v61\nMvaa 108,188,140\nD0 5,20,45,3072\nCV 58,60,61\nZvaa 76,188,132\nv_mul_f32 v62, s57, v62\nv_mul_f32 v63, s57, v63\n"
"Mvaa 76,184,128\ns_waitcnt vmcnt(18) lgkmcnt(0)\ns_barrier\nZvaa 80,224,128\nRa 192,10,16384\nCV 59,62,63\nMvaa 80,228,132\nAR 20,112\nZvaa 112,228,140\nRa 196,11,16384\n"
"s_add_u32 m0, s47, 0\ns_add_u32 s44, s44, 0x40000\nMvaa 112,224,136\nAR 21,113\nv_mul_f32 v20, s57, v20\nZvaa 144,224,144\nRa 200,10,18432\nv_mul_f32 v21, s57, v21\n"
"Mvaa 144,228,148\nD0 6,24,44,0\nCV 52,20,21\nZvaa 176,228,156\nRa 204,11,18432\nAR 20,114\nMvaa 176,224,152\nAR 21,115\nv_mul_f32 v20, s57, v20\nZvaa 180,232,152\nRa 208,10,24576\n"
"v_mul_f32 v21, s57, v21\nMvaa 180,236,156\nCV 53,20,21\ns_add_u32 s6, s56, 0\nSv 36,1,16,6,1024\nZvaa 148,236,148\nRa 212,11,24576\nAR 20,116\nMvaa 148,232,144\nAR 21,117\n"
"v_mul_f32 v20, s57, v20\nZvaa 116,232,136\nRa 216,10,26624\nv_mul_f32 v21, s57, v21\nMvaa 116,236,140\nD0 7,24,44,1024\nCV 54,20,21\nZvaa 84,236,132\nRa 220,11,26624\nAR 20,118\n"
"Mvaa 84,232,128\nAR 21,119\nv_mul_f32 v20, s57, v20\nZvaa 88,240,128\nv_mul_f32 v21, s57, v21\nCV 55,20,21\nMvaa 88,244,132\nAR 20,120\nZvaa 120,244,140\nAR 21,121\n"
"v_mul_f32 v20, s57, v20\nMvaa 120,240,136\nv_mul_f32 v21, s57, v21\nCV 60,20,21\nZvaa 152,240,144\nAR 20,122\nAR 21,123\nMvaa 152,244,148\nD0 8,24,44,2048\n"
"v_mul_f32 v20, s57, v20\nZvaa 184,244,156\nv_mul_f32 v21, s57, v21\nCV 61,20,21\nMvaa 184,240,152\nAR 20,124\nAR 21,125\nZvaa 188,248,152\nv_mul_f32 v20, s57, v20\n"
"v_mul_f32 v21, s57, v21\nMvaa 188,252,156\nCV 62,20,21\ns_add_u32 s6, s56, 0\nSv 44,1,16,6,3072\nZvaa 156,252,148\nAR 20,126\nAR 21,127\nMvaa 156,248,144\nv_mul_f32 v20, s57, v20\n"
"v_mul_f32 v21, s57, v21\nZvaa 124,248,136\nCV 63,20,21\nv_mul_f32 v192, s57, v192\nMvaa 124,252,140\nD0 9,24,44,3072\nv_mul_f32 v193, s57, v193\nZvaa 92,252,132\nCV 192,192,193\n"
"v_mul_f32 v194, s57, v194\nMvaa 92,248,128\nWL 0\nZaaa 16,224,192\nRa 128,10,32768\nv_mul_f32 v195, s57, v195\nMaaa 16,228,196\nCV 193,194,195\nv_mul_f32 v196, s57, v196\n"
"Zaaa 48,228,204\nRa 132,11,32768\ns_add_u32 m0, s47, 16384\ns_add_u32 s44, s44, 0x40000\nMaaa 48,224,200\nv_mul_f32 v197, s57, v197\nCV 194,196,197\nZaaa 80,224,208\n"
"Ra 136,10,34816\nv_mul_f32 v198, s57, v198\nMaaa 80,228,212\nD0 6,24,44,0\nv_mul_f32 v199, s57, v199\nZaaa 112,228,220\nRa 140,11,34816\nCV 195,198,199\nMaaa 112,224,216\n"
"v_mul_f32 v200, s57, v200\nv_mul_f32 v201, s57, v201\nZaaa 116,232,216\nRa 144,10,40960\nCV 200,200,201\nMaaa 116,236,220\nv_mul_f32 v202, s57, v202\nv_mul_f32 v203, s57, v203\n"
"s_add_u32 s6, s56, 4096\nSv 48,1,16,6,0\nZaaa 84,236,212\nRa 148,11,40960\nCV 201,202,203\nMaaa 84,232,208\nv_mul_f32 v204, s57, v204\nv_mul_f32 v205, s57, v205\nZaaa 52,232,200\n"
"Ra 152,10,43008\nCV 202,204,205\nMaaa 52,236,204\nD0 7,24,44,1024\nv_mul_f32 v206, s57, v206\nZaaa 20,236,196\nRa 156,11,43008\nv_mul_f32 v207, s57, v207\nMaaa 20,232,192\n"
"CV 203,206,207\nv_mul_f32 v208, s57, v208\nZaaa 24,240,192\nv_mul_f32 v209, s57, v209\nCV 208,208,209\nMaaa 24,244,196\nv_mul_f32 v210, s57, v210\nv_mul_f32 v211, s57, v211\n"
"Zaaa 56,244,204\nCV 209,210,211\nv_mul_f32 v212, s57, v212\nMaaa 56,240,200\nv_mul_f32 v213, s57, v213\nCV 210,212,213\nZaaa 88,240,208\nv_mul_f32 v214, s57, v214\n"
"v_mul_f32 v215, s57, v215\nMaaa 88,244,212\nD0 8,24,44,2048\nCV 211,214,215\nZaaa 120,244,220\nv_mul_f32 v216, s57, v216\nv_mul_f32 v217, s57, v217\nMaaa 120,240,216\n"
"CV 216,216,217\nv_mul_f32 v218, s57, v218\nZaaa 124,248,216\nv_mul_f32 v219, s57, v219\nCV 217,218,219\nMaaa 124,252,220\nv_mul_f32 v220, s57, v220\nv_mul_f32 v221, s57, v221\n"
"s_add_u32 s6, s56, 4096\nSv 56,1,16,6,2048\nZaaa 92,252,212\nCV 218,220,221\nv_mul_f32 v222, s57, v222\nMaaa 92,248,208\nv_mul_f32 v223, s57, v223\nCV 219,222,223\n"
"Zaaa 60,248,200\nv_mul_f32 v224, s57, v224\nv_mul_f32 v225, s57, v225\nMaaa 60,252,204\nD0 9,24,44,3072\nCV 224,224,225\nZaaa 28,252,196\nv_mul_f32 v226, s57, v226\n"
"v_mul_f32 v227, s57, v227\nMaaa 28,248,192\ns_waitcnt vmcnt(22) lgkmcnt(0)\ns_barrier\nZaaa 0,160,192\nTa 224,16,32768\nCV 225,226,227\nMaaa 0,164,196\nTa 226,16,34816\n"
"v_mul_f32 v228, s57, v228\nZaaa 32,164,204\ns_add_u32 m0, s47, -49152\ns_add_u32 s48, s48, 128\nMaaa 32,160,200\nTa 228,16,49152\nv_mul_f32 v229, s57, v229\nZaaa 64,160,208\n"
"Ta 230,16,51200\nCV 226,228,229\nMaaa 64,164,212\nD0 2,20,48,0\nv_mul_f32 v230, s57, v230\nZaaa 96,164,220\nTa 232,17,32768\nv_mul_f32 v231, s57, v231\nMaaa 96,160,216\n"
"Ta 234,17,34816\nCV 227,230,231\nZaaa 100,168,216\nv_mul_f32 v232, s57, v232\nv_mul_f32 v233, s57, v233\nMaaa 100,172,220\nTa 236,17,49152\nCV 232,232,233\n"
"s_add_u32 s6, s56, 4096\nSv 52,1,16,6,1024\nZaaa 68,172,212\nTa 238,17,51200\nv_mul_f32 v234, s57, v234\nMaaa 68,168,208\nv_mul_f32 v235, s57, v235\nCV 233,234,235\n"
"Zaaa 36,168,200\nTa 240,18,32768\nv_mul_f32 v236, s57, v236\nMaaa 36,172,204\nTa 242,18,34816\nD0 3,20,48,1024\nZaaa 4,172,196\nv_mul_f32 v237, s57, v237\nCV 234,236,237\n"
"Maaa 4,168,192\nTa 244,18,49152\nv_mul_f32 v238, s57, v238\nZaaa 8,176,192\nTa 246,18,51200\nv_mul_f32 v239, s57, v239\nMaaa 8,180,196\nCV 235,238,239\nv_mul_f32 v240, s57, v240\n"
"Zaaa 40,180,204\nTa 248,19,32768\nv_mul_f32 v241, s57, v241\nMaaa 40,176,200\nTa 250,19,34816\nCV 240,240,241\nZaaa 72,176,208\nv_mul_f32 v242, s57, v242\n"
"v_mul_f32 v243, s57, v243\nMaaa 72,180,212\nTa 252,19,49152\nD0 4,20,48,2048\nZaaa 104,180,220\nTa 254,19,51200\nCV 241,242,243\nMaaa 104,176,216\nv_mul_f32 v244, s57, v244\n"
"v_mul_f32 v245, s57, v245\nZaaa 108,184,216\nCV 242,244,245\nv_mul_f32 v246, s57, v246\nMaaa 108,188,220\nv_mul_f32 v247, s57, v247\nCV 243,246,247\ns_add_u32 s6, s56, 4096\n"
"Sv 60,1,16,6,3072\nZaaa 76,188,212\nv_mul_f32 v248, s57, v248\nv_mul_f32 v249, s57, v249\nMaaa 76,184,208\nCV 248,248,249\nv_mul_f32 v250, s57, v250\nZaaa 44,184,200\n"
"v_mul_f32 v251, s57, v251\nCV 249,250,251\nMaaa 44,188,204\nD0 5,20,48,3072\nv_mul_f32 v252, s57, v252\nZaaa 12,188,196\nv_mul_f32 v253, s57, v253\nCV 250,252,253\n"
"Maaa 12,184,192\nWL 0\nMvaa 80,224,128\nTa 160,12,32768\nv_mul_f32 v254, s57, v254\nMvaa 80,228,132\nTa 162,12,34816\nv_mul_f32 v255, s57, v255\nMvaa 112,228,140\n"
"s_add_u32 m0, s47, -32768\ns_add_u32 s45, s45, 128\nMvaa 112,224,136\nTa 164,12,49152\nCV 251,254,255\nMvaa 144,224,144\nTa 166,12,51200\nMvaa 144,228,148\nD0 2,20,45,0\n"
"Mvaa 176,228,156\nTa 168,13,32768\nMvaa 176,224,152\nTa 170,13,34816\nMvaa 180,232,152\nMvaa 180,236,156\nTa 172,13,49152\ns_add_u32 s6, s53, 0\nSv 192,1,16,6,0\nMvaa 148,236,148\n"
"Ta 174,13,51200\nMvaa 148,232,144\nMvaa 116,232,136\nTa 176,14,32768\nMvaa 116,236,140\nTa 178,14,34816\nD0 3,20,45,1024\nMvaa 84,236,132\nMvaa 84,232,128\nTa 180,14,49152\n"
"Mvaa 88,240,128\nTa 182,14,51200\nMvaa 88,244,132\nMvaa 120,244,140\nTa 184,15,32768\nMvaa 120,240,136\nTa 186,15,34816\nMvaa 152,240,144\nMvaa 152,244,148\nTa 188,15,49152\n"
"D0 4,20,45,2048\nMvaa 184,244,156\nTa 190,15,51200\nMvaa 184,240,152\nMvaa 188,248,152\nMvaa 188,252,156\ns_add_u32 s6, s53, 0\nSv 200,1,16,6,2048\nMvaa 156,252,148\n"
"Mvaa 156,248,144\nMvaa 124,248,136\nMvaa 124,252,140\nD0 5,20,45,3072\nMvaa 92,252,132\nMvaa 92,248,128\ns_waitcnt vmcnt(24) lgkmcnt(0)\ns_barrier\nMvaa 64,160,128\n"
"Ra 192,10,49152\nMvaa 64,164,132\nMvaa 96,164,140\nRa 196,11,49152\ns_add_u32 m0, s47, 32768\ns_add_u32 s44, s44, 0x40000\nMvaa 96,160,136\nMvaa 128,160,144\nRa 200,10,51200\n"
"Mvaa 128,164,148\nD0 6,24,44,0\nMvaa 160,164,156\nRa 204,11,51200\nMvaa 160,160,152\nMvaa 164,168,152\nRa 208,10,57344\nMvaa 164,172,156\ns_add_u32 s6, s53, 4096\nSv 208,1,16,6,0\n"
"Mvaa 132,172,148\nRa 212,11,57344\nMvaa 132,168,144\nMvaa 100,168,136\nRa 216,10,59392\nMvaa 100,172,140\nD0 7,24,44,1024\nMvaa 68,172,132\nRa 220,11,59392\nMvaa 68,168,128\n"
"Mvaa 72,176,128\nMvaa 72,180,132\nMvaa 104,180,140\nMvaa 104,176,136\nMvaa 136,176,144\nMvaa 136,180,148\nD0 8,24,44,2048\nMvaa 168,180,156\nMvaa 168,176,152\nMvaa 172,184,152\n"
"Mvaa 172,188,156\ns_add_u32 s6, s53, 4096\nSv 216,1,16,6,2048\nMvaa 140,188,148\nMvaa 140,184,144\nMvaa 108,184,136\nMvaa 108,188,140\nD0 9,24,44,3072\nMvaa 76,188,132\n"
"Mvaa 76,184,128\nWL 0\nMaaa 0,160,192\nRa 128,10,0\nMaaa 0,164,196\nMaaa 32,164,204\nRa 132,11,0\ns_add_u32 m0, s47, 49152\ns_add_u32 s44, s44, 0x40000\nMaaa 32,160,200\n"
"Maaa 64,160,208\nRa 136,10,2048\nMaaa 64,164,212\nD0 6,24,44,0\nMaaa 96,164,220\nRa 140,11,2048\nMaaa 96,160,216\nMaaa 100,168,216\nRa 144,10,8192\nMaaa 100,172,220\n"
"s_add_u32 s6, s53, 0\nSv 224,1,16,6,1024\nMaaa 68,172,212\nRa 148,11,8192\nMaaa 68,168,208\nMaaa 36,168,200\nRa 152,10,10240\nMaaa 36,172,204\nD0 7,24,44,1024\nMaaa 4,172,196\n"
"Ra 156,11,10240\nMaaa 4,168,192\nMaaa 8,176,192\nMaaa 8,180,196\nMaaa 40,180,204\nMaaa 40,176,200\nMaaa 72,176,208\nMaaa 72,180,212\nD0 8,24,44,2048\nMaaa 104,180,220\n"
"Maaa 104,176,216\nMaaa 108,184,216\nMaaa 108,188,220\ns_add_u32 s6, s53, 0\nSv 232,1,16,6,3072\nMaaa 76,188,212\nMaaa 76,184,208\nMaaa 44,184,200\nMaaa 44,188,204\n"
"D0 9,24,44,3072\nMaaa 12,188,196\nMaaa 12,184,192\ns_waitcnt vmcnt(24) lgkmcnt(0)\ns_barrier\nMaaa 16,224,192\nTa 160,12,0\nMaaa 16,228,196\nTa 162,12,2048\nMaaa 48,228,204\n"
"s_add_u32 m0, s47, -16384\ns_add_u32 s48, s48, 128\nMaaa 48,224,200\nTa 164,12,16384\nMaaa 80,224,208\nTa 166,12,18432\nMaaa 80,228,212\nD0 2,20,48,0\nMaaa 112,228,220\n"
"Ta 168,13,0\nMaaa 112,224,216\nTa 170,13,2048\nMaaa 116,232,216\nMaaa 116,236,220\nTa 172,13,16384\ns_add_u32 s6, s53, 4096\nSv 240,1,16,6,1024\nMaaa 84,236,212\nTa 174,13,18432\n"
"Maaa 84,232,208\nMaaa 52,232,200\nTa 176,14,0\nMaaa 52,236,204\nTa 178,14,2048\nD0 3,20,48,1024\nMaaa 20,236,196\nMaaa 20,232,192\nTa 180,14,16384\nMaaa 24,240,192\n"
"Ta 182,14,18432\nMaaa 24,244,196\ns_add_u32 s6, s53, 4096\nSv 248,1,16,6,3072\nMaaa 56,244,204\nTa 184,15,0\nMaaa 56,240,200\nTa 186,15,2048\nMaaa 88,240,208\nMaaa 88,244,212\n"
"Ta 188,15,16384\nD0 4,20,48,2048\nMaaa 120,244,220\nTa 190,15,18432\nMaaa 120,240,216\nMaaa 124,248,216\nMaaa 124,252,220\nMaaa 92,252,212\nMaaa 92,248,208\n"
"s_lshl3_add_u32 s90, s12, s13\ns_lshl2_add_u32 s90, s90, s11\ns_lshl2_add_u32 s90, s90, 0xc04a400\nMaaa 60,248,200\nMaaa 60,252,204\nD0 5,20,48,3072\nMaaa 28,252,196\n"
"Maaa 28,248,192\nWL 0\ns_mov_b64 s[76:77], s[62:63]\ns_mov_b64 s[78:79], s[64:65]\ns_mov_b32 s42, 1605632\ns_mov_b32 s50, 1\n.GL20:\nMvaa 64,160,128\nTa 224,16,0\nMvaa 64,164,132\n"
"Ta 226,16,2048\nMvaa 96,164,140\ns_add_u32 m0, s47, -65536\ns_add_u32 s45, s45, 128\nMvaa 96,160,136\nTa 228,16,16384\nMvaa 128,160,144\nTa 230,16,18432\nMvaa 128,164,148\n"
"D0 2,20,45,0\nMvaa 160,164,156\nTa 232,17,0\nMvaa 160,160,152\nTa 234,17,2048\nMvaa 164,168,152\nMvaa 164,172,156\nTa 236,17,16384\nMvaa 132,172,148\nTa 238,17,18432\n"
"Mvaa 132,168,144\nMvaa 100,168,136\nTa 240,18,0\nMvaa 100,172,140\nTa 242,18,2048\nD0 3,20,45,1024\nMvaa 68,172,132\nMvaa 68,168,128\nTa 244,18,16384\nMvaa 72,176,128\n"
"Ta 246,18,18432\nMvaa 72,180,132\nMvaa 104,180,140\nTa 248,19,0\nMvaa 104,176,136\nTa 250,19,2048\nMvaa 136,176,144\nMvaa 136,180,148\nTa 252,19,16384\nD0 4,20,45,2048\n"
"Mvaa 168,180,156\nTa 254,19,18432\nMvaa 168,176,152\nMvaa 172,184,152\nMvaa 172,188,156\nMvaa 140,188,148\nMvaa 140,184,144\ns_mov_b32 s40, s76\ns_and_b32 s41, s77, 0xffff\n"
"Mvaa 108,184,136\nMvaa 108,188,140\nD0 5,20,45,3072\nMvaa 76,188,132\nMvaa 76,184,128\ns_waitcnt vmcnt(17) lgkmcnt(0)\ns_barrier\nMvaa 80,224,128\nRa 192,10,16384\n"
"Mvaa 80,228,132\nbuffer_load_dword v31, v26, s[40:43], s97 offen\ns_mov_b64 s[76:77], s[66:67]\nMvaa 112,228,140\nRa 196,11,16384\ns_add_u32 m0, s47, 0\n"
"s_add_u32 s44, s44, 0x40000\nMvaa 112,224,136\nMvaa 144,224,144\nRa 200,10,18432\nMvaa 144,228,148\nD0 6,24,44,0\nMvaa 176,228,156\nRa 204,11,18432\nMvaa 176,224,152\n"
"Mvaa 180,232,152\nRa 208,10,24576\nMvaa 180,236,156\nMvaa 148,236,148\nRa 212,11,24576\nMvaa 148,232,144\nMvaa 116,232,136\nRa 216,10,26624\nMvaa 116,236,140\nD0 7,24,44,1024\n"
"Mvaa 84,236,132\nRa 220,11,26624\nMvaa 84,232,128\nMvaa 88,240,128\nMvaa 88,244,132\nMvaa 120,244,140\nMvaa 120,240,136\nMvaa 152,240,144\nMvaa 152,244,148\nD0 8,24,44,2048\n"
"Mvaa 184,244,156\nMvaa 184,240,152\nMvaa 188,248,152\nMvaa 188,252,156\nMvaa 156,252,148\nMvaa 156,248,144\nMvaa 124,248,136\nMvaa 124,252,140\nD0 9,24,44,3072\nMvaa 92,252,132\n"
"Mvaa 92,248,128\nWL 0\nMaaa 16,224,192\nRa 128,10,32768\nMaaa 16,228,196\nMaaa 48,228,204\nRa 132,11,32768\ns_add_u32 m0, s47, 16384\ns_add_u32 s44, s44, 0x40000\nMaaa 48,224,200\n"
"Maaa 80,224,208\nRa 136,10,34816\nMaaa 80,228,212\nD0 6,24,44,0\nMaaa 112,228,220\nRa 140,11,34816\nMaaa 112,224,216\nMaaa 116,232,216\nRa 144,10,40960\nMaaa 116,236,220\n"
"Maaa 84,236,212\nRa 148,11,40960\nMaaa 84,232,208\nMaaa 52,232,200\nRa 152,10,43008\nMaaa 52,236,204\nD0 7,24,44,1024\nMaaa 20,236,196\nRa 156,11,43008\nMaaa 20,232,192\n"
"Maaa 24,240,192\nMaaa 24,244,196\nMaaa 56,244,204\nMaaa 56,240,200\nMaaa 88,240,208\nMaaa 88,244,212\nD0 8,24,44,2048\nMaaa 120,244,220\nMaaa 120,240,216\nMaaa 124,248,216\n"
"Maaa 124,252,220\nMaaa 92,252,212\nMaaa 92,248,208\nMaaa 60,248,200\nMaaa 60,252,204\nD0 9,24,44,3072\nMaaa 28,252,196\nMaaa 28,248,192\ns_waitcnt vmcnt(17) lgkmcnt(0)\ns_barrier\n"
"Maaa 0,160,192\nTa 224,16,32768\nMaaa 0,164,196\nTa 226,16,34816\nMaaa 32,164,204\ns_add_u32 m0, s47, -49152\ns_add_u32 s48, s48, 128\nMaaa 32,160,200\nTa 228,16,49152\n"
"Maaa 64,160,208\nTa 230,16,51200\nMaaa 64,164,212\nD0 2,20,48,0\nMaaa 96,164,220\nTa 232,17,32768\nMaaa 96,160,216\nTa 234,17,34816\nMaaa 100,168,216\nMaaa 100,172,220\n"
"Ta 236,17,49152\nMaaa 68,172,212\nTa 238,17,51200\nMaaa 68,168,208\nMaaa 36,168,200\nTa 240,18,32768\nMaaa 36,172,204\nTa 242,18,34816\nD0 3,20,48,1024\nMaaa 4,172,196\n"
"Maaa 4,168,192\nTa 244,18,49152\nMaaa 8,176,192\nTa 246,18,51200\nMaaa 8,180,196\nMaaa 40,180,204\nTa 248,19,32768\nMaaa 40,176,200\nTa 250,19,34816\nMaaa 72,176,208\n"
"Maaa 72,180,212\nTa 252,19,49152\nD0 4,20,48,2048\nMaaa 104,180,220\nTa 254,19,51200\nMaaa 104,176,216\nMaaa 108,184,216\nMaaa 108,188,220\nMaaa 76,188,212\nMaaa 76,184,208\n"
"Maaa 44,184,200\nMaaa 44,188,204\nD0 5,20,48,3072\nMaaa 12,188,196\nMaaa 12,184,192\nWL 0\nMvaa 80,224,128\nTa 160,12,32768\nMvaa 80,228,132\nTa 162,12,34816\nMvaa 112,228,140\n"
"s_add_u32 m0, s47, -32768\ns_add_u32 s45, s45, 128\nMvaa 112,224,136\nTa 164,12,49152\nMvaa 144,224,144\nTa 166,12,51200\nMvaa 144,228,148\nD0 2,20,45,0\nMvaa 176,228,156\n"
"Ta 168,13,32768\nMvaa 176,224,152\nTa 170,13,34816\nMvaa 180,232,152\nMvaa 180,236,156\nTa 172,13,49152\nMvaa 148,236,148\nTa 174,13,51200\nMvaa 148,232,144\nMvaa 116,232,136\n"
"Ta 176,14,32768\nMvaa 116,236,140\nTa 178,14,34816\nD0 3,20,45,1024\nMvaa 84,236,132\nMvaa 84,232,128\nTa 180,14,49152\nMvaa 88,240,128\nTa 182,14,51200\nMvaa 88,244,132\n"
"Mvaa 120,244,140\nTa 184,15,32768\nMvaa 120,240,136\nTa 186,15,34816\nMvaa 152,240,144\nMvaa 152,244,148\nTa 188,15,49152\nD0 4,20,45,2048\nMvaa 184,244,156\nTa 190,15,51200\n"
"Mvaa 184,240,152\nMvaa 188,248,152\nMvaa 188,252,156\nMvaa 156,252,148\nMvaa 156,248,144\ns_mov_b32 s40, s78\ns_and_b32 s41, s79, 0xffff\nMvaa 124,248,136\nMvaa 124,252,140\n"
"D0 5,20,45,3072\nMvaa 92,252,132\nMvaa 92,248,128\ns_waitcnt vmcnt(17) lgkmcnt(0)\ns_barrier\nMvaa 64,160,128\nRa 192,10,49152\nMvaa 64,164,132\n"
"buffer_load_dword v31, v26, s[40:43], s97 offen\ns_mov_b64 s[78:79], s[66:67]\nMvaa 96,164,140\nRa 196,11,49152\ns_add_u32 m0, s47, 32768\ns_add_u32 s44, s44, 0x40000\n"
"Mvaa 96,160,136\nMvaa 128,160,144\nRa 200,10,51200\nMvaa 128,164,148\nD0 6,24,44,0\nMvaa 160,164,156\nRa 204,11,51200\nMvaa 160,160,152\nMvaa 164,168,152\nRa 208,10,57344\n"
"Mvaa 164,172,156\nMvaa 132,172,148\nRa 212,11,57344\nMvaa 132,168,144\nMvaa 100,168,136\nRa 216,10,59392\nMvaa 100,172,140\nD0 7,24,44,1024\nMvaa 68,172,132\nRa 220,11,59392\n"
"Mvaa 68,168,128\nMvaa 72,176,128\nMvaa 72,180,132\nMvaa 104,180,140\nMvaa 104,176,136\nMvaa 136,176,144\nMvaa 136,180,148\nD0 8,24,44,2048\nMvaa 168,180,156\nMvaa 168,176,152\n"
"Mvaa 172,184,152\nMvaa 172,188,156\nMvaa 140,188,148\nMvaa 140,184,144\nMvaa 108,184,136\nMvaa 108,188,140\nD0 9,24,44,3072\nMvaa 76,188,132\nMvaa 76,184,128\nWL 0\n"
"Maaa 0,160,192\nRa 128,10,0\nMaaa 0,164,196\nMaaa 32,164,204\nRa 132,11,0\ns_add_u32 m0, s47, 49152\ns_add_u32 s44, s44, 0x40000\nMaaa 32,160,200\nMaaa 64,160,208\nRa 136,10,2048\n"
"Maaa 64,164,212\nD0 6,24,44,0\nMaaa 96,164,220\nRa 140,11,2048\nMaaa 96,160,216\nMaaa 100,168,216\nRa 144,10,8192\nMaaa 100,172,220\nMaaa 68,172,212\nRa 148,11,8192\n"
"Maaa 68,168,208\nMaaa 36,168,200\nRa 152,10,10240\nMaaa 36,172,204\nD0 7,24,44,1024\nMaaa 4,172,196\nRa 156,11,10240\nMaaa 4,168,192\nMaaa 8,176,192\nMaaa 8,180,196\n"
"Maaa 40,180,204\nMaaa 40,176,200\nMaaa 72,176,208\nMaaa 72,180,212\nD0 8,24,44,2048\nMaaa 104,180,220\nMaaa 104,176,216\nMaaa 108,184,216\nMaaa 108,188,220\nMaaa 76,188,212\n"
"Maaa 76,184,208\nMaaa 44,184,200\nMaaa 44,188,204\nD0 9,24,44,3072\nMaaa 12,188,196\nMaaa 12,184,192\ns_waitcnt vmcnt(17) lgkmcnt(0)\ns_barrier\nMaaa 16,224,192\nTa 160,12,0\n"
"Maaa 16,228,196\nTa 162,12,2048\nMaaa 48,228,204\ns_add_u32 m0, s47, -16384\ns_add_u32 s48, s48, 128\nMaaa 48,224,200\nTa 164,12,16384\nMaaa 80,224,208\nTa 166,12,18432\n"
"Maaa 80,228,212\nD0 2,20,48,0\nMaaa 112,228,220\nTa 168,13,0\nMaaa 112,224,216\nTa 170,13,2048\nMaaa 116,232,216\nMaaa 116,236,220\nTa 172,13,16384\nMaaa 84,236,212\n"
"Ta 174,13,18432\nMaaa 84,232,208\nMaaa 52,232,200\nTa 176,14,0\nMaaa 52,236,204\nTa 178,14,2048\nD0 3,20,48,1024\nMaaa 20,236,196\nMaaa 20,232,192\nTa 180,14,16384\n"
"Maaa 24,240,192\nTa 182,14,18432\nMaaa 24,244,196\nMaaa 56,244,204\nTa 184,15,0\nMaaa 56,240,200\nTa 186,15,2048\nMaaa 88,240,208\nMaaa 88,244,212\nTa 188,15,16384\n"
"D0 4,20,48,2048\nMaaa 120,244,220\nTa 190,15,18432\nMaaa 120,240,216\nMaaa 124,248,216\nMaaa 124,252,220\nMaaa 92,252,212\nMaaa 92,248,208\nMaaa 60,248,200\nMaaa 60,252,204\n"
"D0 5,20,48,3072\ns_sub_u32 s50, s50, 1\nMaaa 28,252,196\nMaaa 28,248,192\nWL 0\ns_cbranch_scc0 .GL20\nMvaa 64,160,128\nTa 224,16,0\nMvaa 64,164,132\nTa 226,16,2048\n"
"Mvaa 96,164,140\ns_add_u32 m0, s47, -65536\ns_add_u32 s45, s45, 128\nMvaa 96,160,136\nTa 228,16,16384\nMvaa 128,160,144\nTa 230,16,18432\nMvaa 128,164,148\nD0 2,20,45,0\n"
"Mvaa 160,164,156\nTa 232,17,0\nMvaa 160,160,152\nTa 234,17,2048\nMvaa 164,168,152\ns_and_b32 s2, s74, 15\nMvaa 164,172,156\nTa 236,17,16384\n"
"buffer_store_dword v25, off, s[16:19], s90 offset:0 sc0 sc1\nMvaa 132,172,148\nTa 238,17,18432\nMvaa 132,168,144\ns_sub_u32 s2, s2, 2\ns_cmp_lt_u32 s2, 14\n"
"s_cselect_b32 s5, 0, 0x40000000\nMvaa 100,168,136\nTa 240,18,0\nMvaa 100,172,140\nTa 242,18,2048\nD0 3,20,45,1024\nMvaa 68,172,132\ns_and_b32 s2, s2, 15\nMvaa 68,168,128\n"
"Ta 244,18,16384\nMvaa 72,176,128\nTa 246,18,18432\nMvaa 72,180,132\ns_mul_i32 s4, s73, 14\nMvaa 104,180,140\nTa 248,19,0\nMvaa 104,176,136\nTa 250,19,2048\nMvaa 136,176,144\n"
"s_add_u32 s4, s4, s2\nMvaa 136,180,148\nTa 252,19,16384\nD0 4,20,45,2048\nMvaa 168,180,156\nTa 254,19,18432\nMvaa 168,176,152\ns_mov_b32 s36, s62\nMvaa 172,184,152\n"
"s_and_b32 s37, s63, 0xffff\nMvaa 172,188,156\ns_mov_b32 s38, 0x188000\nMvaa 140,188,148\ns_mov_b32 s39, 0x20000\nMvaa 140,184,144\ns_mov_b32 s40, s64\nMvaa 108,184,136\n"
"s_and_b32 s41, s65, 0xffff\nMvaa 108,188,140\nD0 5,20,45,3072\nMvaa 76,188,132\ns_mov_b32 s42, 0x188000\nMvaa 76,184,128\ns_waitcnt vmcnt(18) lgkmcnt(0)\ns_barrier\n"
"Mvaa 80,224,128\nRa 192,10,16384\nMvaa 80,228,132\ns_mov_b32 s43, 0x20000\nMvaa 112,228,140\nRa 196,11,16384\ns_add_u32 m0, s47, 0\ns_add_u32 s44, s44, 0x40000\nMvaa 112,224,136\n"
"s_mov_b32 s92, s66\nMvaa 144,224,144\nRa 200,10,18432\nMvaa 144,228,148\nD0 6,24,44,0\nMvaa 176,228,156\nRa 204,11,18432\nMvaa 176,224,152\ns_and_b32 s93, s67, 0xffff\n"
"Mvaa 180,232,152\nRa 208,10,24576\nMvaa 180,236,156\ns_mov_b32 s94, 0x188000\nMvaa 148,236,148\nRa 212,11,24576\nMvaa 148,232,144\ns_mov_b32 s95, 0x20000\nMvaa 116,232,136\n"
"Ra 216,10,26624\nMvaa 116,236,140\nD0 7,24,44,1024\nMvaa 84,236,132\nRa 220,11,26624\nMvaa 84,232,128\nv_lshrrev_b32 v252, 1, v0\nMvaa 88,240,128\nv_mul_u32_u24 v253, 14336, v252\n"
"Mvaa 88,244,132\nv_and_b32 v254, 1, v0\nMvaa 120,244,140\nv_lshl_add_u32 v253, v254, 4, v253\nMvaa 120,240,136\nv_mov_b32 v254, 0x40000000\nv_cmp_gt_u32 vcc, 56, v0\n"
"v_lshlrev_b32 v252, 3, v252\ns_mul_i32 s2, s11, 401408\nv_cndmask_b32 v21, v254, v253, vcc\nv_cndmask_b32 v255, v254, v252, vcc\nMvaa 152,240,144\ns_lshl_b32 s3, s4, 5\n"
"Mvaa 152,244,148\nD0 8,24,44,2048\nMvaa 184,244,156\ns_add_u32 s2, s2, s3\nMvaa 184,240,152\ns_or_b32 s86, s2, s5\nMvaa 188,248,152\ns_add_u32 s87, s86, 7168\nMvaa 188,252,156\n"
"v_lshrrev_b32 v251, 4, v0\nMvaa 156,252,148\nv_mul_u32_u24 v252, 3584, v251\nMvaa 156,248,144\nv_and_b32 v251, 15, v0\nMvaa 124,248,136\nv_lshl_add_u32 v252, v251, 4, v252\n"
"v_cmp_gt_u32 vcc, 14, v251\ns_mul_i32 s2, s4, 14336\ns_mul_i32 s3, s11, 224\nv_cndmask_b32 v20, v254, v252, vcc\nMvaa 124,252,140\nD0 9,24,44,3072\nMvaa 92,252,132\n"
"s_add_u32 s2, s2, s3\nMvaa 92,248,128\nWL 0\nMaaa 16,224,192\nRa 128,10,32768\nMaaa 16,228,196\ns_or_b32 s88, s2, s5\nMaaa 48,228,204\nRa 132,11,32768\ns_add_u32 m0, s47, 16384\n"
"s_add_u32 s44, s44, 0x40000\nMaaa 48,224,200\nMaaa 80,224,208\nRa 136,10,34816\nMaaa 80,228,212\nD0 6,24,44,0\nMaaa 112,228,220\nRa 140,11,34816\nMaaa 112,224,216\n"
"Maaa 116,232,216\nRa 144,10,40960\nMaaa 116,236,220\nMaaa 84,236,212\nRa 148,11,40960\nMaaa 84,232,208\nMaaa 52,232,200\nRa 152,10,43008\nMaaa 52,236,204\nD0 7,24,44,1024\n"
"Maaa 20,236,196\nRa 156,11,43008\nMaaa 20,232,192\nMaaa 24,240,192\nMaaa 24,244,196\nMaaa 56,244,204\nMaaa 56,240,200\nMaaa 88,240,208\nMaaa 88,244,212\nD0 8,24,44,2048\n"
"Maaa 120,244,220\nMaaa 120,240,216\nMaaa 124,248,216\nMaaa 124,252,220\nMaaa 92,252,212\nMaaa 92,248,208\nMaaa 60,248,200\nMaaa 60,252,204\nD0 9,24,44,3072\nMaaa 28,252,196\n"
"Maaa 28,248,192\ns_waitcnt vmcnt(17) lgkmcnt(0)\ns_barrier\nMaaa 0,160,192\nTa 224,16,32768\nMaaa 0,164,196\nTa 226,16,34816\n"
"buffer_load_dwordx4 v[192:195], v21, s[36:39], s86 offen offset:0\nMaaa 32,164,204\ns_add_u32 m0, s47, -49152\ns_add_u32 s48, s48, 128\nMaaa 32,160,200\nTa 228,16,49152\n"
"s_lshl_b32 s6, s11, 12\ns_add_u32 s6, s6, 0xc044000\nMaaa 64,160,208\nTa 230,16,51200\nMaaa 64,164,212\nD0 2,20,48,0\nMaaa 96,164,220\nTa 232,17,32768\nMaaa 96,160,216\n"
"Ta 234,17,34816\nMaaa 100,168,216\nbuffer_load_dwordx4 v[32:35], v1, s[16:19], s6 offen offset:0 sc0 sc1\nMaaa 100,172,220\nTa 236,17,49152\nMaaa 68,172,212\nTa 238,17,51200\n"
"Maaa 68,168,208\nMaaa 36,168,200\nTa 240,18,32768\nMaaa 36,172,204\nTa 242,18,34816\nD0 3,20,48,1024\nMaaa 4,172,196\n"
"buffer_load_dwordx4 v[36:39], v1, s[16:19], s6 offen offset:1024 sc0 sc1\nMaaa 4,168,192\nTa 244,18,49152\nMaaa 8,176,192\nTa 246,18,51200\nMaaa 8,180,196\nMaaa 40,180,204\n"
"Ta 248,19,32768\nMaaa 40,176,200\nTa 250,19,34816\nMaaa 72,176,208\nbuffer_load_dwordx4 v[40:43], v1, s[16:19], s6 offen offset:2048 sc0 sc1\nMaaa 72,180,212\nTa 252,19,49152\n"
"D0 4,20,48,2048\nMaaa 104,180,220\nTa 254,19,51200\nMaaa 104,176,216\nMaaa 108,184,216\nbuffer_load_dwordx4 v[44:47], v1, s[16:19], s6 offen offset:3072 sc0 sc1\nMaaa 108,188,220\n"
"Maaa 76,188,212\nMaaa 76,184,208\nMaaa 44,184,200\nMaaa 44,188,204\nD0 5,20,48,3072\nMaaa 12,188,196\nMaaa 12,184,192\nWL 0\nMvaa 80,224,128\nTa 160,12,32768\nMvaa 80,228,132\n"
"Ta 162,12,34816\nMvaa 112,228,140\ns_add_u32 m0, s47, -32768\ns_add_u32 s45, s45, 128\nMvaa 112,224,136\nTa 164,12,49152\nMvaa 144,224,144\nTa 166,12,51200\nMvaa 144,228,148\n"
"D0 2,20,45,0\nMvaa 176,228,156\nTa 168,13,32768\nMvaa 176,224,152\nTa 170,13,34816\nMvaa 180,232,152\nMvaa 180,236,156\nTa 172,13,49152\nMvaa 148,236,148\nTa 174,13,51200\n"
"Mvaa 148,232,144\nMvaa 116,232,136\nTa 176,14,32768\nMvaa 116,236,140\nTa 178,14,34816\nD0 3,20,45,1024\nMvaa 84,236,132\nMvaa 84,232,128\nTa 180,14,49152\nMvaa 88,240,128\n"
"Ta 182,14,51200\nMvaa 88,244,132\nMvaa 120,244,140\nTa 184,15,32768\nMvaa 120,240,136\nTa 186,15,34816\nMvaa 152,240,144\nMvaa 152,244,148\nTa 188,15,49152\nD0 4,20,45,2048\n"
"Mvaa 184,244,156\nTa 190,15,51200\nMvaa 184,240,152\nMvaa 188,248,152\nMvaa 188,252,156\nMvaa 156,252,148\nMvaa 156,248,144\nMvaa 124,248,136\nMvaa 124,252,140\nD0 5,20,45,3072\n"
"Mvaa 92,252,132\nMvaa 92,248,128\ns_waitcnt vmcnt(21) lgkmcnt(0)\ns_barrier\nMvaa 64,160,128\nRa 192,10,49152\nMvaa 64,164,132\n"
"buffer_load_dwordx4 v[208:211], v21, s[40:43], s86 offen offset:0\nMvaa 96,164,140\nRa 196,11,49152\ns_add_u32 m0, s47, 32768\ns_add_u32 s44, s44, 0x40000\nMvaa 96,160,136\n"
"Mvaa 128,160,144\nRa 200,10,51200\nMvaa 128,164,148\nD0 6,24,44,0\nMvaa 160,164,156\nRa 204,11,51200\nMvaa 160,160,152\nMvaa 164,168,152\nRa 208,10,57344\nMvaa 164,172,156\n"
"Mvaa 132,172,148\nRa 212,11,57344\nMvaa 132,168,144\nMvaa 100,168,136\nRa 216,10,59392\nMvaa 100,172,140\nD0 7,24,44,1024\nMvaa 68,172,132\nRa 220,11,59392\nMvaa 68,168,128\n"
"Mvaa 72,176,128\nMvaa 72,180,132\nMvaa 104,180,140\nMvaa 104,176,136\nMvaa 136,176,144\nMvaa 136,180,148\nD0 8,24,44,2048\nMvaa 168,180,156\nMvaa 168,176,152\nMvaa 172,184,152\n"
"Mvaa 172,188,156\nMvaa 140,188,148\nMvaa 140,184,144\nMvaa 108,184,136\nMvaa 108,188,140\nD0 9,24,44,3072\nMvaa 76,188,132\nMvaa 76,184,128\nWL 0\nMaaa 0,160,192\nRa 128,10,0\n"
"Maaa 0,164,196\nMaaa 32,164,204\nRa 132,11,0\ns_add_u32 m0, s47, 49152\ns_add_u32 s44, s44, 0x40000\nMaaa 32,160,200\nMaaa 64,160,208\nRa 136,10,2048\nMaaa 64,164,212\n"
"D0 6,24,44,0\nMaaa 96,164,220\nRa 140,11,2048\nMaaa 96,160,216\nMaaa 100,168,216\nRa 144,10,8192\nMaaa 100,172,220\nMaaa 68,172,212\nRa 148,11,8192\nMaaa 68,168,208\n"
"Maaa 36,168,200\nRa 152,10,10240\nMaaa 36,172,204\nD0 7,24,44,1024\nMaaa 4,172,196\nRa 156,11,10240\nMaaa 4,168,192\nMaaa 8,176,192\nMaaa 8,180,196\nMaaa 40,180,204\n"
"Maaa 40,176,200\nMaaa 72,176,208\nMaaa 72,180,212\nD0 8,24,44,2048\nMaaa 104,180,220\nMaaa 104,176,216\nMaaa 108,184,216\nMaaa 108,188,220\nMaaa 76,188,212\nMaaa 76,184,208\n"
"Maaa 44,184,200\nMaaa 44,188,204\nD0 9,24,44,3072\nMaaa 12,188,196\nMaaa 12,184,192\ns_waitcnt vmcnt(14) lgkmcnt(0)\ns_barrier\nMaaa 16,224,192\nTa 160,12,0\nMaaa 16,228,196\n"
"Ta 162,12,2048\nbuffer_load_dwordx4 v[196:199], v21, s[36:39], s86 offen offset:3584\nMaaa 48,228,204\ns_add_u32 m0, s47, -16384\ns_add_u32 s48, s48, 128\nMaaa 48,224,200\n"
"Ta 164,12,16384\nMaaa 80,224,208\nTa 166,12,18432\nMaaa 80,228,212\nD0 2,20,48,0\nMaaa 112,228,220\nTa 168,13,0\nMaaa 112,224,216\nTa 170,13,2048\nMaaa 116,232,216\n"
"v_xor_b32 v48, s15, v33\nMaaa 116,236,220\nTa 172,13,16384\nMaaa 84,236,212\nTa 174,13,18432\nMaaa 84,232,208\nv_xor_b32 v49, s15, v35\nMaaa 52,232,200\nTa 176,14,0\n"
"Maaa 52,236,204\nTa 178,14,2048\nD0 3,20,48,1024\nMaaa 20,236,196\nv_xor_b32 v50, s15, v37\nMaaa 20,232,192\nTa 180,14,16384\nMaaa 24,240,192\nTa 182,14,18432\nMaaa 24,244,196\n"
"v_xor_b32 v51, s15, v39\nMaaa 56,244,204\nTa 184,15,0\nMaaa 56,240,200\nTa 186,15,2048\nMaaa 88,240,208\nv_xor_b32 v52, s15, v41\nMaaa 88,244,212\nTa 188,15,16384\n"
"D0 4,20,48,2048\nMaaa 120,244,220\nTa 190,15,18432\nMaaa 120,240,216\nv_xor_b32 v53, s15, v43\nMaaa 124,248,216\nv_xor_b32 v54, s15, v45\nMaaa 124,252,220\n"
"v_xor_b32 v55, s15, v47\nMaaa 92,252,212\nv_or3_b32 v48, v48, v49, v50\nMaaa 92,248,208\nv_or3_b32 v51, v51, v52, v53\nMaaa 60,248,200\nv_or3_b32 v48, v48, v54, v55\n"
"Maaa 60,252,204\nD0 5,20,48,3072\nMaaa 28,252,196\nv_or_b32 v48, v48, v51\nMaaa 28,248,192\nWL 0\nMvaa 64,160,128\nTa 224,16,0\nMvaa 64,164,132\nTa 226,16,2048\nMvaa 96,164,140\n"
"s_add_u32 m0, s47, -65536\ns_add_u32 s45, s45, 128\nMvaa 96,160,136\nTa 228,16,16384\nMvaa 128,160,144\nTa 230,16,18432\nMvaa 128,164,148\nD0 2,20,45,0\nMvaa 160,164,156\n"
"Ta 232,17,0\nMvaa 160,160,152\nTa 234,17,2048\nMvaa 164,168,152\nv_cmp_ne_u32 vcc, 0, v48\nMvaa 164,172,156\nTa 236,17,16384\nMvaa 132,172,148\nTa 238,17,18432\nMvaa 132,168,144\n"
"s_cbranch_vccnz .S14\n.S15:\nMvaa 100,168,136\nTa 240,18,0\nMvaa 100,172,140\nTa 242,18,2048\nD0 3,20,45,1024\nMvaa 68,172,132\nv_max_u32 v48, v32, v34\nMvaa 68,168,128\n"
"Ta 244,18,16384\nMvaa 72,176,128\nTa 246,18,18432\nMvaa 72,180,132\nv_min_u32 v49, v32, v34\nMvaa 104,180,140\nTa 248,19,0\nMvaa 104,176,136\nTa 250,19,2048\nMvaa 136,176,144\n"
"v_med3_u32 v49, v48, v49, v36\nMvaa 136,180,148\nTa 252,19,16384\nD0 4,20,45,2048\nMvaa 168,180,156\nTa 254,19,18432\nMvaa 168,176,152\nv_max_u32 v48, v48, v36\nMvaa 172,184,152\n"
"v_med3_u32 v49, v48, v49, v38\nMvaa 172,188,156\nv_max_u32 v48, v48, v38\nMvaa 140,188,148\nv_med3_u32 v49, v48, v49, v40\nMvaa 140,184,144\nv_max_u32 v48, v48, v40\n"
"Mvaa 108,184,136\nv_med3_u32 v49, v48, v49, v42\nMvaa 108,188,140\nD0 5,20,45,3072\nMvaa 76,188,132\nv_max_u32 v48, v48, v42\nMvaa 76,184,128\ns_waitcnt vmcnt(18) lgkmcnt(0)\n"
"s_barrier\nMvaa 80,224,128\nRa 192,10,16384\nMvaa 80,228,132\nbuffer_load_dwordx4 v[212:215], v21, s[40:43], s86 offen offset:3584\nMvaa 112,228,140\nRa 196,11,16384\n"
"s_add_u32 m0, s47, 0\ns_add_u32 s44, s44, 0x40000\nMvaa 112,224,136\nv_med3_u32 v49, v48, v49, v44\nMvaa 144,224,144\nRa 200,10,18432\nMvaa 144,228,148\nD0 6,24,44,0\n"
"Mvaa 176,228,156\nRa 204,11,18432\nMvaa 176,224,152\nv_max_u32 v48, v48, v44\nMvaa 180,232,152\nRa 208,10,24576\nMvaa 180,236,156\nv_med3_u32 v49, v48, v49, v46\nMvaa 148,236,148\n"
"Ra 212,11,24576\nMvaa 148,232,144\nv_max_u32 v48, v48, v46\nMvaa 116,232,136\nRa 216,10,26624\nMvaa 116,236,140\nD0 7,24,44,1024\nMvaa 84,236,132\nRa 220,11,26624\n"
"Mvaa 84,232,128\nv_mov_b32 v50, v48\nMvaa 88,240,128\nMvaa 88,244,132\nv_max_u32_dpp v50, v50, v50 row_ror:8 row_mask:0xf bank_mask:0xf\nMvaa 120,244,140\nMvaa 120,240,136\n"
"v_max_u32_dpp v50, v50, v50 row_ror:4 row_mask:0xf bank_mask:0xf\nMvaa 152,240,144\nMvaa 152,244,148\nD0 8,24,44,2048\nMvaa 184,244,156\n"
"v_max_u32_dpp v50, v50, v50 row_ror:2 row_mask:0xf bank_mask:0xf\nMvaa 184,240,152\nMvaa 188,248,152\nv_max_u32_dpp v50, v50, v50 row_ror:1 row_mask:0xf bank_mask:0xf\n"
"Mvaa 188,252,156\nv_mov_b32 v52, v50\nMvaa 156,252,148\nMvaa 156,248,144\nv_permlane16_swap_b32 v50, v52\nMvaa 124,248,136\nv_max_u32 v50, v50, v52\nMvaa 124,252,140\n"
"D0 9,24,44,3072\nMvaa 92,252,132\nv_mov_b32 v52, v50\nMvaa 92,248,128\nWL 0\nMaaa 16,224,192\nRa 128,10,32768\nMaaa 16,228,196\nv_permlane32_swap_b32 v50, v52\nMaaa 48,228,204\n"
"Ra 132,11,32768\ns_add_u32 m0, s47, 16384\ns_add_u32 s44, s44, 0x40000\nMaaa 48,224,200\nv_max_u32 v50, v50, v52\nv_cmp_eq_u32 vcc, v48, v50\ns_lshl_b32 s2, s11, 3\n"
"s_add_u32 s2, s2, 131072\nv_cndmask_b32 v51, v48, v49, vcc\nMaaa 80,224,208\nRa 136,10,34816\nMaaa 80,228,212\nD0 6,24,44,0\nMaaa 112,228,220\nRa 140,11,34816\nMaaa 112,224,216\n"
"v_max_u32_dpp v51, v51, v51 row_ror:8 row_mask:0xf bank_mask:0xf\nMaaa 116,232,216\nRa 144,10,40960\nMaaa 116,236,220\n"
"v_max_u32_dpp v51, v51, v51 row_ror:4 row_mask:0xf bank_mask:0xf\nMaaa 84,236,212\nRa 148,11,40960\nMaaa 84,232,208\n"
"v_max_u32_dpp v51, v51, v51 row_ror:2 row_mask:0xf bank_mask:0xf\nMaaa 52,232,200\nRa 152,10,43008\nMaaa 52,236,204\nD0 7,24,44,1024\nMaaa 20,236,196\nRa 156,11,43008\n"
"Maaa 20,232,192\nv_max_u32_dpp v51, v51, v51 row_ror:1 row_mask:0xf bank_mask:0xf\nMaaa 24,240,192\nv_mov_b32 v52, v51\nMaaa 24,244,196\nMaaa 56,244,204\n"
"v_permlane16_swap_b32 v51, v52\nMaaa 56,240,200\nv_max_u32 v51, v51, v52\nMaaa 88,240,208\nv_mov_b32 v52, v51\nMaaa 88,244,212\nD0 8,24,44,2048\nMaaa 120,244,220\n"
"v_permlane32_swap_b32 v51, v52\nMaaa 120,240,216\nv_max_u32 v51, v51, v52\nMaaa 124,248,216\nv_mov_b32 v52, s2\nMaaa 124,252,220\nds_write_b64 v52, v[50:51]\nMaaa 92,252,212\n"
"v_mov_b32 v52, 131072\nMaaa 92,248,208\nMaaa 60,248,200\nMaaa 60,252,204\nD0 9,24,44,3072\nMaaa 28,252,196\nMaaa 28,248,192\ns_waitcnt vmcnt(18) lgkmcnt(0)\ns_barrier\n"
"Maaa 0,160,192\nTa 224,16,32768\nMaaa 0,164,196\nTa 226,16,34816\nbuffer_load_dwordx4 v[200:203], v21, s[36:39], s87 offen offset:0\nMaaa 32,164,204\ns_add_u32 m0, s47, -49152\n"
"s_add_u32 s48, s48, 128\nMaaa 32,160,200\nTa 228,16,49152\nMaaa 64,160,208\nTa 230,16,51200\nMaaa 64,164,212\nD0 2,20,48,0\nMaaa 96,164,220\nTa 232,17,32768\nMaaa 96,160,216\n"
"Ta 234,17,34816\nMaaa 100,168,216\nRv 32,52,0\nMaaa 100,172,220\nTa 236,17,49152\nMaaa 68,172,212\nTa 238,17,51200\nMaaa 68,168,208\nRv 36,52,16\nMaaa 36,168,200\nTa 240,18,32768\n"
"Maaa 36,172,204\nTa 242,18,34816\nD0 3,20,48,1024\nMaaa 4,172,196\nMaaa 4,168,192\nTa 244,18,49152\nMaaa 8,176,192\nTa 246,18,51200\nMaaa 8,180,196\nMaaa 40,180,204\n"
"Ta 248,19,32768\nMaaa 40,176,200\nTa 250,19,34816\nMaaa 72,176,208\nMaaa 72,180,212\nTa 252,19,49152\nD0 4,20,48,2048\nMaaa 104,180,220\nTa 254,19,51200\nMaaa 104,176,216\nWL 11\n"
"Maaa 108,184,216\nv_max_u32 v48, v32, v33\nMaaa 108,188,220\nv_min_u32 v49, v32, v33\nMaaa 76,188,212\nv_med3_u32 v49, v48, v49, v34\nMaaa 76,184,208\nv_max_u32 v48, v48, v34\n"
"Maaa 44,184,200\nv_med3_u32 v49, v48, v49, v35\nMaaa 44,188,204\nD0 5,20,48,3072\nMaaa 12,188,196\nv_max_u32 v48, v48, v35\nMaaa 12,184,192\nWL 0\nMvaa 80,224,128\n"
"Ta 160,12,32768\nMvaa 80,228,132\nTa 162,12,34816\nMvaa 112,228,140\ns_add_u32 m0, s47, -32768\ns_add_u32 s45, s45, 128\nMvaa 112,224,136\nTa 164,12,49152\nMvaa 144,224,144\n"
"Ta 166,12,51200\nMvaa 144,228,148\nD0 2,20,45,0\nMvaa 176,228,156\nTa 168,13,32768\nMvaa 176,224,152\nTa 170,13,34816\nMvaa 180,232,152\nv_med3_u32 v49, v48, v49, v36\n"
"Mvaa 180,236,156\nTa 172,13,49152\nMvaa 148,236,148\nTa 174,13,51200\nMvaa 148,232,144\nv_max_u32 v48, v48, v36\nMvaa 116,232,136\nTa 176,14,32768\nMvaa 116,236,140\n"
"Ta 178,14,34816\nD0 3,20,45,1024\nMvaa 84,236,132\nv_med3_u32 v49, v48, v49, v37\nMvaa 84,232,128\nTa 180,14,49152\nMvaa 88,240,128\nTa 182,14,51200\nMvaa 88,244,132\n"
"v_max_u32 v48, v48, v37\nMvaa 120,244,140\nTa 184,15,32768\nMvaa 120,240,136\nTa 186,15,34816\nMvaa 152,240,144\nv_med3_u32 v49, v48, v49, v38\nMvaa 152,244,148\nTa 188,15,49152\n"
"D0 4,20,45,2048\nMvaa 184,244,156\nTa 190,15,51200\nMvaa 184,240,152\nv_max_u32 v48, v48, v38\nMvaa 188,248,152\nv_med3_u32 v49, v48, v49, v39\nMvaa 188,252,156\n"
"v_max_u32 v48, v48, v39\nMvaa 156,252,148\nv_readfirstlane_b32 s2, v48\nMvaa 156,248,144\nv_readfirstlane_b32 s3, v49\nMvaa 124,248,136\nMvaa 124,252,140\nD0 5,20,45,3072\n"
"Mvaa 92,252,132\ns_andn2_b32 s2, 0x7ff, s2\nMvaa 92,248,128\ns_waitcnt vmcnt(18) lgkmcnt(0)\ns_barrier\nMvaa 64,160,128\nRa 192,10,49152\nMvaa 64,164,132\n"
"buffer_load_dwordx4 v[216:219], v21, s[40:43], s87 offen offset:0\nMvaa 96,164,140\nRa 196,11,49152\ns_add_u32 m0, s47, 32768\ns_add_u32 s44, s44, 0x40000\nMvaa 96,160,136\n"
"s_andn2_b32 s3, 0x7ff, s3\nMvaa 128,160,144\nRa 200,10,51200\nMvaa 128,164,148\nD0 6,24,44,0\nMvaa 160,164,156\nRa 204,11,51200\nMvaa 160,160,152\ns_lshl_b32 s71, s3, 16\n"
"Mvaa 164,168,152\nRa 208,10,57344\nMvaa 164,172,156\ns_or_b32 s71, s71, s2\nMvaa 132,172,148\nRa 212,11,57344\nMvaa 132,168,144\ns_mul_i32 s2, s2, 896\nMvaa 100,168,136\n"
"Ra 216,10,59392\nMvaa 100,172,140\nD0 7,24,44,1024\nMvaa 68,172,132\nRa 220,11,59392\nMvaa 68,168,128\ns_mul_i32 s3, s3, 896\nMvaa 72,176,128\ns_mul_i32 s6, s11, 224\n"
"Mvaa 72,180,132\ns_add_u32 s2, s2, s6\nMvaa 104,180,140\ns_add_u32 s3, s3, s6\nMvaa 104,176,136\nbuffer_load_dwordx2 v[240:241], v255, s[28:31], s2 offen\nMvaa 136,176,144\n"
"Mvaa 136,180,148\nD0 8,24,44,2048\nMvaa 168,180,156\nMvaa 168,176,152\nbuffer_load_dwordx2 v[242:243], v255, s[28:31], s3 offen\ns_lshl2_add_u32 s2, s10, s11\n"
"s_cmp_eq_u32 s2, 512\ns_cselect_b32 s2, 0, 0x40000000\ns_add_u32 s2, s2, 0xc048000\nMvaa 172,184,152\nMvaa 172,188,156\nMvaa 140,188,148\nMvaa 140,184,144\n"
"buffer_store_dword v25, off, s[16:19], s2 offset:0 sc0 sc1\nMvaa 108,184,136\nMvaa 108,188,140\nD0 9,24,44,3072\nMvaa 76,188,132\nMvaa 76,184,128\nWL 0\nMaaa 0,160,192\n"
"Ra 128,10,0\nMaaa 0,164,196\nMaaa 32,164,204\nRa 132,11,0\ns_add_u32 m0, s47, 49152\ns_add_u32 s44, s44, 0x40000\nMaaa 32,160,200\nMaaa 64,160,208\nRa 136,10,2048\n"
"Maaa 64,164,212\nD0 6,24,44,0\nMaaa 96,164,220\nRa 140,11,2048\nMaaa 96,160,216\nMaaa 100,168,216\nRa 144,10,8192\nMaaa 100,172,220\nMaaa 68,172,212\nRa 148,11,8192\n"
"Maaa 68,168,208\nMaaa 36,168,200\nRa 152,10,10240\nMaaa 36,172,204\nD0 7,24,44,1024\nMaaa 4,172,196\nRa 156,11,10240\nMaaa 4,168,192\nMaaa 8,176,192\nMaaa 8,180,196\n"
"Maaa 40,180,204\nMaaa 40,176,200\nMaaa 72,176,208\nMaaa 72,180,212\nD0 8,24,44,2048\nMaaa 104,180,220\nMaaa 104,176,216\nMaaa 108,184,216\nMaaa 108,188,220\nMaaa 76,188,212\n"
"Maaa 76,184,208\nMaaa 44,184,200\nMaaa 44,188,204\nD0 9,24,44,3072\nMaaa 12,188,196\nMaaa 12,184,192\ns_waitcnt vmcnt(21) lgkmcnt(0)\ns_barrier\nMaaa 16,224,192\nTa 160,12,0\n"
"Maaa 16,228,196\nTa 162,12,2048\nbuffer_load_dwordx4 v[204:207], v21, s[36:39], s87 offen offset:3584\nMaaa 48,228,204\ns_add_u32 m0, s47, -16384\ns_add_u32 s48, s48, 128\n"
"s_lshl_b32 s5, s73, 8\ns_add_u32 s5, s5, 0xc04ac00\nv_lshrrev_b32 v27, 2, v1\nbuffer_load_dword v27, v27, s[16:19], s5 offen sc0 sc1\nMaaa 48,224,200\nTa 164,12,16384\n"
"Maaa 80,224,208\nTa 166,12,18432\nMaaa 80,228,212\nD0 2,20,48,0\nMaaa 112,228,220\nTa 168,13,0\nMaaa 112,224,216\nTa 170,13,2048\nMaaa 116,232,216\nMaaa 116,236,220\n"
"Ta 172,13,16384\nMaaa 84,236,212\nTa 174,13,18432\nMaaa 84,232,208\nMaaa 52,232,200\nTa 176,14,0\nMaaa 52,236,204\nTa 178,14,2048\nD0 3,20,48,1024\nMaaa 20,236,196\n"
"Maaa 20,232,192\nTa 180,14,16384\nMaaa 24,240,192\nTa 182,14,18432\nMaaa 24,244,196\nMaaa 56,244,204\nTa 184,15,0\nMaaa 56,240,200\nTa 186,15,2048\nMaaa 88,240,208\n"
"Maaa 88,244,212\nTa 188,15,16384\nD0 4,20,48,2048\nMaaa 120,244,220\nTa 190,15,18432\nMaaa 120,240,216\nMaaa 124,248,216\nMaaa 124,252,220\nMaaa 92,252,212\nMaaa 92,248,208\n"
"Maaa 60,248,200\nMaaa 60,252,204\nD0 5,20,48,3072\nMaaa 28,252,196\nMaaa 28,248,192\nWL 0\nMvaa 64,160,128\nTa 224,16,0\nMvaa 64,164,132\nTa 226,16,2048\nMvaa 96,164,140\n"
"s_add_u32 m0, s47, -65536\ns_add_u32 s45, s45, 128\nMvaa 96,160,136\nTa 228,16,16384\nMvaa 128,160,144\nTa 230,16,18432\nMvaa 128,164,148\nD0 2,20,45,0\nMvaa 160,164,156\n"
"Ta 232,17,0\nMvaa 160,160,152\nTa 234,17,2048\nMvaa 164,168,152\nMvaa 164,172,156\nTa 236,17,16384\nMvaa 132,172,148\nTa 238,17,18432\nMvaa 132,168,144\nMvaa 100,168,136\n"
"Ta 240,18,0\nMvaa 100,172,140\nTa 242,18,2048\nD0 3,20,45,1024\nMvaa 68,172,132\nMvaa 68,168,128\nTa 244,18,16384\nMvaa 72,176,128\nTa 246,18,18432\nMvaa 72,180,132\n"
"Mvaa 104,180,140\nTa 248,19,0\nMvaa 104,176,136\nTa 250,19,2048\nMvaa 136,176,144\nMvaa 136,180,148\nTa 252,19,16384\nD0 4,20,45,2048\nMvaa 168,180,156\nTa 254,19,18432\n"
"Mvaa 168,176,152\nMvaa 172,184,152\nMvaa 172,188,156\nMvaa 140,188,148\nMvaa 140,184,144\nMvaa 108,184,136\nMvaa 108,188,140\nD0 5,20,45,3072\nMvaa 76,188,132\nMvaa 76,184,128\n"
"s_waitcnt vmcnt(22) lgkmcnt(0)\ns_barrier\nMvaa 80,224,128\nRa 192,10,16384\nWV 16\nMvaa 80,228,132\nbuffer_load_dwordx4 v[220:223], v21, s[40:43], s87 offen offset:3584\n"
"Mvaa 112,228,140\nRa 196,11,16384\ns_add_u32 m0, s47, 0\ns_add_u32 s44, s44, 0x40000\nMvaa 112,224,136\nv_lshlrev_b32 v244, 16, v240\nv_and_b32 v245, 0xffff0000, v240\n"
"Mvaa 144,224,144\nRa 200,10,18432\nv_lshlrev_b32 v246, 16, v241\nMvaa 144,228,148\nD0 6,24,44,0\nv_and_b32 v247, 0xffff0000, v241\nMvaa 176,228,156\nRa 204,11,18432\n"
"v_lshlrev_b32 v248, 16, v242\nMvaa 176,224,152\nv_and_b32 v249, 0xffff0000, v242\nv_lshlrev_b32 v250, 16, v243\nMvaa 180,232,152\nRa 208,10,24576\n"
"v_and_b32 v251, 0xffff0000, v243\nMvaa 180,236,156\nv_lshlrev_b32 v252, 16, v192\nMvaa 148,236,148\nRa 212,11,24576\nv_and_b32 v253, 0xffff0000, v192\nMvaa 148,232,144\n"
"MU 32,244,252\nMU 33,244,253\nMvaa 116,232,136\nRa 216,10,26624\nMU 40,248,252\nMvaa 116,236,140\nD0 7,24,44,1024\nMU 41,248,253\nMvaa 84,236,132\nRa 220,11,26624\n"
"v_lshlrev_b32 v252, 16, v193\nMvaa 84,232,128\nv_and_b32 v253, 0xffff0000, v193\nMU 34,244,252\nMvaa 88,240,128\nMU 35,244,253\nMU 42,248,252\nMvaa 88,244,132\nMU 43,248,253\n"
"Mvaa 120,244,140\nv_lshlrev_b32 v252, 16, v194\nv_and_b32 v253, 0xffff0000, v194\nMvaa 120,240,136\nMU 36,244,252\nMU 37,244,253\nMvaa 152,240,144\nMU 44,248,252\nMU 45,248,253\n"
"Mvaa 152,244,148\nD0 8,24,44,2048\nv_lshlrev_b32 v252, 16, v195\nMvaa 184,244,156\nv_and_b32 v253, 0xffff0000, v195\nMU 38,244,252\nMvaa 184,240,152\nMU 39,244,253\nMU 46,248,252\n"
"Mvaa 188,248,152\nMU 47,248,253\nv_lshlrev_b32 v252, 16, v208\nMvaa 188,252,156\nv_and_b32 v253, 0xffff0000, v208\nMvaa 156,252,148\nMU 48,244,252\nMU 49,244,253\n"
"Mvaa 156,248,144\nMU 56,248,252\nMU 57,248,253\nMvaa 124,248,136\nv_lshlrev_b32 v252, 16, v209\nv_and_b32 v253, 0xffff0000, v209\nMvaa 124,252,140\nD0 9,24,44,3072\nMU 50,244,252\n"
"Mvaa 92,252,132\nMU 51,244,253\nMU 58,248,252\nMvaa 92,248,128\nWL 0\nMaaa 16,224,192\nRa 128,10,32768\nMU 59,248,253\nMaaa 16,228,196\nv_lshlrev_b32 v252, 16, v210\n"
"Maaa 48,228,204\nRa 132,11,32768\ns_add_u32 m0, s47, 16384\ns_add_u32 s44, s44, 0x40000\nMaaa 48,224,200\nv_and_b32 v253, 0xffff0000, v210\nMU 52,244,252\nMaaa 80,224,208\n"
"Ra 136,10,34816\nMU 53,244,253\nMaaa 80,228,212\nD0 6,24,44,0\nMU 60,248,252\nMaaa 112,228,220\nRa 140,11,34816\nMU 61,248,253\nMaaa 112,224,216\nv_lshlrev_b32 v252, 16, v211\n"
"v_and_b32 v253, 0xffff0000, v211\nMaaa 116,232,216\nRa 144,10,40960\nMU 54,244,252\nMaaa 116,236,220\nMU 55,244,253\nMaaa 84,236,212\nRa 148,11,40960\nMU 62,248,252\n"
"Maaa 84,232,208\nMU 63,248,253\nv_lshlrev_b32 v252, 16, v196\nMaaa 52,232,200\nRa 152,10,43008\nv_and_b32 v253, 0xffff0000, v196\nMaaa 52,236,204\nD0 7,24,44,1024\nFM 32,245,252\n"
"Maaa 20,236,196\nRa 156,11,43008\nFM 33,245,253\nMaaa 20,232,192\nFM 40,249,252\nFM 41,249,253\nMaaa 24,240,192\nv_lshlrev_b32 v252, 16, v197\nv_and_b32 v253, 0xffff0000, v197\n"
"Maaa 24,244,196\nFM 34,245,252\nMaaa 56,244,204\nFM 35,245,253\nFM 42,249,252\nMaaa 56,240,200\nFM 43,249,253\nv_lshlrev_b32 v252, 16, v198\nMaaa 88,240,208\n"
"v_and_b32 v253, 0xffff0000, v198\nFM 36,245,252\nMaaa 88,244,212\nD0 8,24,44,2048\nFM 37,245,253\nMaaa 120,244,220\nFM 44,249,252\nFM 45,249,253\nMaaa 120,240,216\n"
"v_lshlrev_b32 v252, 16, v199\nv_and_b32 v253, 0xffff0000, v199\nMaaa 124,248,216\nFM 38,245,252\nFM 39,245,253\nMaaa 124,252,220\nFM 46,249,252\nMaaa 92,252,212\nFM 47,249,253\n"
"v_lshlrev_b32 v252, 16, v212\nMaaa 92,248,208\nv_and_b32 v253, 0xffff0000, v212\nFM 48,245,252\nMaaa 60,248,200\nFM 49,245,253\nFM 56,249,252\nMaaa 60,252,204\nD0 9,24,44,3072\n"
"FM 57,249,253\nMaaa 28,252,196\nv_lshlrev_b32 v252, 16, v213\nv_and_b32 v253, 0xffff0000, v213\nMaaa 28,248,192\ns_waitcnt vmcnt(19) lgkmcnt(0)\ns_barrier\nMaaa 0,160,192\n"
"Ta 224,16,32768\nFM 50,245,252\nMaaa 0,164,196\nTa 226,16,34816\nbuffer_load_dwordx4 v[224:227], v20, s[92:95], s88 offen offset:0\nMaaa 32,164,204\ns_add_u32 m0, s47, -49152\n"
"s_add_u32 s48, s48, 128\nMaaa 32,160,200\nTa 228,16,49152\nFM 51,245,253\nMaaa 64,160,208\nTa 230,16,51200\nFM 58,249,252\nMaaa 64,164,212\nD0 2,20,48,0\nFM 59,249,253\n"
"Maaa 96,164,220\nTa 232,17,32768\nv_lshlrev_b32 v252, 16, v214\nMaaa 96,160,216\nTa 234,17,34816\nv_and_b32 v253, 0xffff0000, v214\nMaaa 100,168,216\nFM 52,245,252\nFM 53,245,253\n"
"Maaa 100,172,220\nTa 236,17,49152\nMaaa 68,172,212\nTa 238,17,51200\nFM 60,249,252\nMaaa 68,168,208\nFM 61,249,253\nv_lshlrev_b32 v252, 16, v215\nMaaa 36,168,200\nTa 240,18,32768\n"
"v_and_b32 v253, 0xffff0000, v215\nMaaa 36,172,204\nTa 242,18,34816\nD0 3,20,48,1024\nMaaa 4,172,196\nFM 54,245,252\nFM 55,245,253\nMaaa 4,168,192\nTa 244,18,49152\nFM 62,249,252\n"
"Maaa 8,176,192\nTa 246,18,51200\nFM 63,249,253\nMaaa 8,180,196\nv_lshlrev_b32 v252, 16, v200\nMaaa 40,180,204\nTa 248,19,32768\nv_and_b32 v253, 0xffff0000, v200\nMaaa 40,176,200\n"
"Ta 250,19,34816\nFM 32,246,252\nMaaa 72,176,208\nFM 33,246,253\nFM 40,250,252\nMaaa 72,180,212\nTa 252,19,49152\nD0 4,20,48,2048\nMaaa 104,180,220\nTa 254,19,51200\nFM 41,250,253\n"
"Maaa 104,176,216\nv_lshlrev_b32 v252, 16, v201\nv_and_b32 v253, 0xffff0000, v201\nMaaa 108,184,216\nFM 34,246,252\nFM 35,246,253\nMaaa 108,188,220\nFM 42,250,252\nMaaa 76,188,212\n"
"FM 43,250,253\nv_lshlrev_b32 v252, 16, v202\nMaaa 76,184,208\nv_and_b32 v253, 0xffff0000, v202\nFM 36,246,252\nMaaa 44,184,200\nFM 37,246,253\nFM 44,250,252\nMaaa 44,188,204\n"
"D0 5,20,48,3072\nFM 45,250,253\nMaaa 12,188,196\nv_lshlrev_b32 v252, 16, v203\nv_and_b32 v253, 0xffff0000, v203\nMaaa 12,184,192\nWL 0\nMvaa 80,224,128\nTa 160,12,32768\n"
"FM 38,246,252\nMvaa 80,228,132\nTa 162,12,34816\nbuffer_load_dwordx4 v[228:231], v20, s[92:95], s88 offen offset:896\nMvaa 112,228,140\ns_add_u32 m0, s47, -32768\n"
"s_add_u32 s45, s45, 128\nMvaa 112,224,136\nTa 164,12,49152\nFM 39,246,253\nMvaa 144,224,144\nTa 166,12,51200\nFM 46,250,252\nMvaa 144,228,148\nD0 2,20,45,0\nFM 47,250,253\n"
"Mvaa 176,228,156\nTa 168,13,32768\nv_lshlrev_b32 v252, 16, v216\nMvaa 176,224,152\nTa 170,13,34816\nv_and_b32 v253, 0xffff0000, v216\nMvaa 180,232,152\nFM 48,246,252\n"
"FM 49,246,253\nMvaa 180,236,156\nTa 172,13,49152\nMvaa 148,236,148\nTa 174,13,51200\nFM 56,250,252\nMvaa 148,232,144\nFM 57,250,253\nv_lshlrev_b32 v252, 16, v217\n"
"Mvaa 116,232,136\nTa 176,14,32768\nv_and_b32 v253, 0xffff0000, v217\nMvaa 116,236,140\nTa 178,14,34816\nD0 3,20,45,1024\nMvaa 84,236,132\nFM 50,246,252\nFM 51,246,253\n"
"Mvaa 84,232,128\nTa 180,14,49152\nFM 58,250,252\nMvaa 88,240,128\nTa 182,14,51200\nFM 59,250,253\nMvaa 88,244,132\nv_lshlrev_b32 v252, 16, v218\nMvaa 120,244,140\nTa 184,15,32768\n"
"v_and_b32 v253, 0xffff0000, v218\nMvaa 120,240,136\nTa 186,15,34816\nFM 52,246,252\nMvaa 152,240,144\nFM 53,246,253\nFM 60,250,252\nMvaa 152,244,148\nTa 188,15,49152\n"
"D0 4,20,45,2048\nMvaa 184,244,156\nTa 190,15,51200\nFM 61,250,253\nMvaa 184,240,152\nv_lshlrev_b32 v252, 16, v219\nv_and_b32 v253, 0xffff0000, v219\nMvaa 188,248,152\n"
"FM 54,246,252\nWV 26\nv_xor_b32 v27, s15, v27\nv_and_b32 v255, 0xffffdfff, v27\nv_cmp_ne_u32 vcc, 0, v255\ns_nop 0\ns_cbranch_vccnz .S17\n.S18:\nv_cmp_ne_u32 vcc, 0, v27\ns_nop 3\n"
"s_bcnt1_i32_b64 s4, vcc\ns_cmp_gt_u32 s4, 32\ns_cselect_b32 s4, 1, 0\nMvaa 188,252,156\ns_xor_b32 s4, s4, s12\nMvaa 156,252,148\ns_and_b32 s4, s4, 1\ns_lshl_b32 s4, s4, 13\n"
"Mvaa 156,248,144\ns_mul_i32 s72, s73, 25165824\ns_add_u32 s72, s72, s4\nMvaa 124,248,136\ns_and_b32 s4, s12, 1\ns_lshl_b32 s4, s4, 21\nMvaa 124,252,140\nD0 5,20,45,3072\n"
"s_add_u32 s72, s72, s4\nMvaa 92,252,132\ns_lshl3_add_u32 s2, s13, s11\ns_lshl_b32 s2, s2, 11\nMvaa 92,248,128\ns_waitcnt vmcnt(18) lgkmcnt(0)\ns_barrier\nMvaa 64,160,128\n"
"Ra 192,10,49152\ns_lshl_b32 s3, s14, 22\nMvaa 64,164,132\nbuffer_load_dwordx4 v[232:235], v20, s[92:95], s88 offen offset:1792\nMvaa 96,164,140\nRa 196,11,49152\n"
"s_add_u32 m0, s47, 32768\ns_add_u32 s44, s44, 0x40000\nMvaa 96,160,136\ns_add_u32 s2, s2, s3\ns_add_u32 s2, s2, s72\nMvaa 128,160,144\nRa 200,10,51200\n"
"s_add_u32 s49, s2, 0xc00000\nMvaa 128,164,148\nD0 6,24,44,0\nFM 55,246,253\nMvaa 160,164,156\nRa 204,11,51200\nFM 62,250,252\nMvaa 160,160,152\nFM 63,250,253\n"
"v_lshlrev_b32 v252, 16, v204\nMvaa 164,168,152\nRa 208,10,57344\nv_and_b32 v253, 0xffff0000, v204\nMvaa 164,172,156\nFM 32,247,252\nMvaa 132,172,148\nRa 212,11,57344\n"
"FM 33,247,253\nMvaa 132,168,144\nFM 40,251,252\nFM 41,251,253\nMvaa 100,168,136\nRa 216,10,59392\nv_lshlrev_b32 v252, 16, v205\nMvaa 100,172,140\nD0 7,24,44,1024\n"
"v_and_b32 v253, 0xffff0000, v205\nMvaa 68,172,132\nRa 220,11,59392\nFM 34,247,252\nMvaa 68,168,128\nFM 35,247,253\nFM 42,251,252\nMvaa 72,176,128\nFM 43,251,253\n"
"v_lshlrev_b32 v252, 16, v206\nMvaa 72,180,132\nv_and_b32 v253, 0xffff0000, v206\nMvaa 104,180,140\nFM 36,247,252\nFM 37,247,253\nMvaa 104,176,136\nFM 44,251,252\nFM 45,251,253\n"
"Mvaa 136,176,144\nv_lshlrev_b32 v252, 16, v207\nv_and_b32 v253, 0xffff0000, v207\nMvaa 136,180,148\nD0 8,24,44,2048\nFM 38,247,252\nMvaa 168,180,156\nFM 39,247,253\nFM 46,251,252\n"
"Mvaa 168,176,152\nFM 47,251,253\nv_lshlrev_b32 v252, 16, v220\nMvaa 172,184,152\nv_and_b32 v253, 0xffff0000, v220\nFM 48,247,252\nMvaa 172,188,156\nFM 49,247,253\n"
"Mvaa 140,188,148\nFM 56,251,252\nFM 57,251,253\nMvaa 140,184,144\nv_lshlrev_b32 v252, 16, v221\nv_and_b32 v253, 0xffff0000, v221\nMvaa 108,184,136\nFM 50,247,252\nFM 51,247,253\n"
"Mvaa 108,188,140\nD0 9,24,44,3072\nFM 58,251,252\nMvaa 76,188,132\nFM 59,251,253\nv_lshlrev_b32 v252, 16, v222\nMvaa 76,184,128\nWL 0\nMaaa 0,160,192\nRa 128,10,0\n"
"v_and_b32 v253, 0xffff0000, v222\nMaaa 0,164,196\nbuffer_load_dwordx4 v[236:239], v20, s[92:95], s88 offen offset:2688\nMaaa 32,164,204\nRa 132,11,0\ns_add_u32 m0, s47, 49152\n"
"s_add_u32 s44, s44, 0x40000\nMaaa 32,160,200\nFM 52,247,252\nFM 53,247,253\nMaaa 64,160,208\nRa 136,10,2048\nFM 60,251,252\nMaaa 64,164,212\nD0 6,24,44,0\nFM 61,251,253\n"
"Maaa 96,164,220\nRa 140,11,2048\nv_lshlrev_b32 v252, 16, v223\nMaaa 96,160,216\nv_and_b32 v253, 0xffff0000, v223\nFM 54,247,252\nMaaa 100,168,216\nRa 144,10,8192\nFM 55,247,253\n"
"Maaa 100,172,220\nFM 62,251,252\nMaaa 68,172,212\nRa 148,11,8192\nFM 63,251,253\nMaaa 68,168,208\nv_permlane32_swap_b32 v32, v48\nv_permlane32_swap_b32 v33, v49\nMaaa 36,168,200\n"
"Ra 152,10,10240\nv_permlane32_swap_b32 v34, v50\nMaaa 36,172,204\nD0 7,24,44,1024\nv_permlane32_swap_b32 v35, v51\nMaaa 4,172,196\nRa 156,11,10240\nv_permlane32_swap_b32 v36, v52\n"
"Maaa 4,168,192\nv_permlane32_swap_b32 v37, v53\nv_permlane32_swap_b32 v38, v54\nMaaa 8,176,192\nv_permlane32_swap_b32 v39, v55\nv_permlane32_swap_b32 v40, v56\nMaaa 8,180,196\n"
"v_permlane32_swap_b32 v41, v57\nMaaa 40,180,204\nv_permlane32_swap_b32 v42, v58\nv_permlane32_swap_b32 v43, v59\nMaaa 40,176,200\nv_permlane32_swap_b32 v44, v60\n"
"v_permlane32_swap_b32 v45, v61\nMaaa 72,176,208\nv_permlane32_swap_b32 v46, v62\nv_permlane32_swap_b32 v47, v63\nMaaa 72,180,212\nD0 8,24,44,2048\nAD 32,32,48\nMaaa 104,180,220\n"
"AD 33,33,49\nAD 34,34,50\nMaaa 104,176,216\nAD 35,35,51\nAD 36,36,52\nMaaa 108,184,216\nAD 37,37,53\nAD 38,38,54\nMaaa 108,188,220\nAD 39,39,55\nMaaa 76,188,212\nAD 40,40,56\n"
"AD 41,41,57\nMaaa 76,184,208\nAD 42,42,58\nAD 43,43,59\nMaaa 44,184,200\nAD 44,44,60\nAD 45,45,61\nMaaa 44,188,204\nD0 9,24,44,3072\nAD 46,46,62\nMaaa 12,188,196\nAD 47,47,63\n"
"Maaa 12,184,192\ns_waitcnt vmcnt(20) lgkmcnt(0)\ns_barrier\nMaaa 16,224,192\nTa 160,12,0\nv_permlane16_swap_b32 v32, v40\nMaaa 16,228,196\nTa 162,12,2048\nMaaa 48,228,204\n"
"s_add_u32 m0, s47, -16384\ns_add_u32 s48, s48, 128\nMaaa 48,224,200\nTa 164,12,16384\nv_permlane16_swap_b32 v33, v41\nMaaa 80,224,208\nTa 166,12,18432\n"
"v_permlane16_swap_b32 v34, v42\nMaaa 80,228,212\nD0 2,20,48,0\nv_permlane16_swap_b32 v35, v43\nMaaa 112,228,220\nTa 168,13,0\nv_permlane16_swap_b32 v36, v44\nMaaa 112,224,216\n"
"Ta 170,13,2048\nv_permlane16_swap_b32 v37, v45\nMaaa 116,232,216\nv_permlane16_swap_b32 v38, v46\nv_permlane16_swap_b32 v39, v47\nMaaa 116,236,220\nTa 172,13,16384\n"
"Maaa 84,236,212\nTa 174,13,18432\nAD 32,32,40\nMaaa 84,232,208\nAD 33,33,41\nAD 34,34,42\nMaaa 52,232,200\nTa 176,14,0\nAD 35,35,43\nMaaa 52,236,204\nTa 178,14,2048\n"
"D0 3,20,48,1024\nMaaa 20,236,196\nAD 36,36,44\nAD 37,37,45\nMaaa 20,232,192\nTa 180,14,16384\nAD 38,38,46\nMaaa 24,240,192\nTa 182,14,18432\nAD 39,39,47\nMaaa 24,244,196\n"
"v_add_f32_dpp v32, v32, v32 row_ror:8 row_mask:0xf bank_mask:0xf\nMaaa 56,244,204\nTa 184,15,0\nv_add_f32_dpp v33, v33, v33 row_ror:8 row_mask:0xf bank_mask:0xf\nMaaa 56,240,200\n"
"Ta 186,15,2048\nv_add_f32_dpp v34, v34, v34 row_ror:8 row_mask:0xf bank_mask:0xf\nMaaa 88,240,208\nv_add_f32_dpp v35, v35, v35 row_ror:8 row_mask:0xf bank_mask:0xf\n"
"v_add_f32_dpp v36, v36, v36 row_ror:8 row_mask:0xf bank_mask:0xf\nMaaa 88,244,212\nTa 188,15,16384\nD0 4,20,48,2048\nMaaa 120,244,220\nTa 190,15,18432\n"
"v_add_f32_dpp v37, v37, v37 row_ror:8 row_mask:0xf bank_mask:0xf\nMaaa 120,240,216\nv_add_f32_dpp v38, v38, v38 row_ror:8 row_mask:0xf bank_mask:0xf\n"
"v_add_f32_dpp v39, v39, v39 row_ror:8 row_mask:0xf bank_mask:0xf\nMaaa 124,248,216\nv_add_f32_dpp v32, v32, v32 row_ror:4 row_mask:0xf bank_mask:0xf\n"
"v_add_f32_dpp v33, v33, v33 row_ror:4 row_mask:0xf bank_mask:0xf\nMaaa 124,252,220\nv_add_f32_dpp v34, v34, v34 row_ror:4 row_mask:0xf bank_mask:0xf\nMaaa 92,252,212\n"
"v_add_f32_dpp v35, v35, v35 row_ror:4 row_mask:0xf bank_mask:0xf\nv_add_f32_dpp v36, v36, v36 row_ror:4 row_mask:0xf bank_mask:0xf\nMaaa 92,248,208\n"
"v_add_f32_dpp v37, v37, v37 row_ror:4 row_mask:0xf bank_mask:0xf\nv_add_f32_dpp v38, v38, v38 row_ror:4 row_mask:0xf bank_mask:0xf\nMaaa 60,248,200\n"
"v_add_f32_dpp v39, v39, v39 row_ror:4 row_mask:0xf bank_mask:0xf\nv_add_f32_dpp v32, v32, v32 row_ror:2 row_mask:0xf bank_mask:0xf\nMaaa 60,252,204\nD0 5,20,48,3072\n"
"v_add_f32_dpp v33, v33, v33 row_ror:2 row_mask:0xf bank_mask:0xf\nMaaa 28,252,196\nv_add_f32_dpp v34, v34, v34 row_ror:2 row_mask:0xf bank_mask:0xf\n"
"v_add_f32_dpp v35, v35, v35 row_ror:2 row_mask:0xf bank_mask:0xf\nMaaa 28,248,192\nWL 0\nMvaa 64,160,128\nTa 224,16,0\n"
"v_add_f32_dpp v36, v36, v36 row_ror:2 row_mask:0xf bank_mask:0xf\nMvaa 64,164,132\nTa 226,16,2048\nMvaa 96,164,140\ns_add_u32 m0, s47, -65536\ns_add_u32 s45, s45, 128\n"
"Mvaa 96,160,136\nTa 228,16,16384\nv_add_f32_dpp v37, v37, v37 row_ror:2 row_mask:0xf bank_mask:0xf\nMvaa 128,160,144\nTa 230,16,18432\n"
"v_add_f32_dpp v38, v38, v38 row_ror:2 row_mask:0xf bank_mask:0xf\nMvaa 128,164,148\nD0 2,20,45,0\nv_add_f32_dpp v39, v39, v39 row_ror:2 row_mask:0xf bank_mask:0xf\n"
"Mvaa 160,164,156\nTa 232,17,0\nv_lshrrev_b32 v48, 4, v0\nMvaa 160,160,152\nTa 234,17,2048\nv_and_b32 v49, 1, v0\nMvaa 164,168,152\nv_lshlrev_b32 v49, 5, v49\n"
"v_lshl_add_u32 v49, v48, 6, v49\nMvaa 164,172,156\nTa 236,17,16384\nMvaa 132,172,148\nTa 238,17,18432\ns_lshl_b32 s2, s11, 8\nMvaa 132,168,144\ns_add_u32 s2, s2, 132096\n"
"v_add_u32 v49, s2, v49\nMvaa 100,168,136\nTa 240,18,0\nds_write_b128 v49, v[32:35]\nMvaa 100,172,140\nTa 242,18,2048\nD0 3,20,45,1024\nMvaa 68,172,132\n"
"ds_write_b128 v49, v[36:39] offset:16\nv_and_b32 v50, 31, v0\nMvaa 68,168,128\nTa 244,18,16384\nv_lshlrev_b32 v50, 2, v50\nMvaa 72,176,128\nTa 246,18,18432\n"
"s_lshl_b32 s2, s11, 10\nMvaa 72,180,132\ns_add_u32 s2, s2, 133120\nMvaa 104,180,140\nTa 248,19,0\nv_add_u32 v51, s2, v50\nMvaa 104,176,136\nTa 250,19,2048\n"
"v_add_u32 v50, 132096, v50\nMvaa 136,176,144\nv_lshl_add_u32 v52, v48, 4, s2\nMvaa 136,180,148\nTa 252,19,16384\nD0 4,20,45,2048\nMvaa 168,180,156\nTa 254,19,18432\n"
"Mvaa 168,176,152\nMvaa 172,184,152\nMvaa 172,188,156\nMvaa 140,188,148\nMvaa 140,184,144\nMvaa 108,184,136\nMvaa 108,188,140\nD0 5,20,45,3072\nMvaa 76,188,132\nMvaa 76,184,128\n"
"s_waitcnt vmcnt(18) lgkmcnt(0)\ns_barrier\nMvaa 80,224,128\nRa 192,10,16384\nds_read_b32 v32, v50 offset:0\nMvaa 80,228,132\nds_read_b32 v33, v50 offset:256\nMvaa 112,228,140\n"
"Ra 196,11,16384\ns_add_u32 m0, s47, 0\ns_add_u32 s44, s44, 0x40000\nMvaa 112,224,136\nds_read_b32 v34, v50 offset:512\nds_read_b32 v35, v50 offset:768\nMvaa 144,224,144\n"
"Ra 200,10,18432\nMvaa 144,228,148\nD0 6,24,44,0\nMvaa 176,228,156\nRa 204,11,18432\nds_read_b32 v36, v50 offset:128\nMvaa 176,224,152\nds_read_b32 v37, v50 offset:384\n"
"Mvaa 180,232,152\nRa 208,10,24576\nds_read_b32 v38, v50 offset:640\nMvaa 180,236,156\nds_read_b32 v39, v50 offset:896\nMvaa 148,236,148\nRa 212,11,24576\nMvaa 148,232,144\n"
"Mvaa 116,232,136\nRa 216,10,26624\nMvaa 116,236,140\nD0 7,24,44,1024\nMvaa 84,236,132\nRa 220,11,26624\nMvaa 84,232,128\nMvaa 88,240,128\nMvaa 88,244,132\nMvaa 120,244,140\n"
"Mvaa 120,240,136\nWL 3\nAD 32,32,33\nMvaa 152,240,144\nAD 34,34,35\nMvaa 152,244,148\nD0 8,24,44,2048\nMvaa 184,244,156\nAD 36,36,37\nAD 38,38,39\nMvaa 184,240,152\nAD 32,32,34\n"
"Mvaa 188,248,152\nAD 36,36,38\nCV 32,32,36\nMvaa 188,252,156\nv_and_b32 v36, 0xffff0000, v32\nMvaa 156,252,148\nv_lshlrev_b32 v32, 16, v32\nMvaa 156,248,144\n"
"v_mul_f32 v33, 0xbfb8aa3b, v32\nMvaa 124,248,136\nEX 33,33\nMvaa 124,252,140\nD0 9,24,44,3072\nMvaa 92,252,132\nv_add_f32 v33, 1.0, v33\nMvaa 92,248,128\nWL 0\nMaaa 16,224,192\n"
"Ra 128,10,32768\nMaaa 16,228,196\nv_rcp_f32 v33, v33\nMaaa 48,228,204\nRa 132,11,32768\ns_add_u32 m0, s47, 16384\ns_add_u32 s44, s44, 0x40000\nMaaa 48,224,200\nMU 33,32,33\n"
"CV 33,33,33\nMaaa 80,224,208\nRa 136,10,34816\nMaaa 80,228,212\nD0 6,24,44,0\nMaaa 112,228,220\nRa 140,11,34816\nv_lshlrev_b32 v33, 16, v33\nMaaa 112,224,216\nMU 33,33,36\n"
"Maaa 116,232,216\nRa 144,10,40960\nCV 33,33,33\nMaaa 116,236,220\nv_lshlrev_b32 v33, 16, v33\nMaaa 84,236,212\nRa 148,11,40960\nMaaa 84,232,208\nds_write_b32 v51, v33\n"
"Rv 244,52,0\nMaaa 52,232,200\nRa 152,10,43008\nMaaa 52,236,204\nD0 7,24,44,1024\nMaaa 20,236,196\nRa 156,11,43008\nRv 248,52,64\nMaaa 20,232,192\ns_and_b32 s2, s74, 15\n"
"Maaa 24,240,192\ns_sub_u32 s2, s2, 2\nMaaa 24,244,196\ns_cmp_lt_u32 s2, 14\ns_cselect_b32 s5, 0, 0x40000000\nMaaa 56,244,204\ns_and_b32 s2, s2, 15\nMaaa 56,240,200\n"
"s_mul_i32 s4, s73, 14\ns_add_u32 s4, s4, s2\nMaaa 88,240,208\nv_and_b32 v49, 15, v0\nMaaa 88,244,212\nD0 8,24,44,2048\nMaaa 120,244,220\nv_lshlrev_b32 v50, 3, v49\n"
"v_and_b32 v51, 1, v48\nMaaa 120,240,216\nv_lshl_add_u32 v50, v51, 2, v50\nMaaa 124,248,216\ns_mul_i32 s2, s11, 112\nv_add_u32 v50, s2, v50\nMaaa 124,252,220\n"
"v_lshrrev_b32 v51, 4, v50\nMaaa 92,252,212\nv_lshrrev_b32 v48, 1, v48\nMaaa 92,248,208\nv_mad_u32_u24 v51, v48, 28, v51\nv_mul_u32_u24 v51, 7168, v51\nMaaa 60,248,200\n"
"v_and_b32 v50, 15, v50\nMaaa 60,252,204\nD0 9,24,44,3072\nMaaa 28,252,196\nv_lshl_add_u32 v51, v50, 2, v51\nv_cmp_gt_u32 vcc, 14, v49\ns_lshl_b32 s4, s4, 6\n"
"s_add_u32 s4, s4, 0xc04c800\nv_cndmask_b32 v51, v254, v51, vcc\nMaaa 28,248,192\ns_waitcnt vmcnt(16) lgkmcnt(1)\ns_barrier\nMaaa 0,160,192\nTa 224,16,32768\ns_or_b32 s4, s4, s5\n"
"Maaa 0,164,196\nTa 226,16,34816\nMaaa 32,164,204\ns_add_u32 m0, s47, -49152\ns_add_u32 s48, s48, 128\nMaaa 32,160,200\nTa 228,16,49152\nWL 3\nMaaa 64,160,208\nTa 230,16,51200\n"
"Maaa 64,164,212\nD0 2,20,48,0\nMaaa 96,164,220\nTa 232,17,32768\nv_lshlrev_b32 v252, 16, v224\nMaaa 96,160,216\nTa 234,17,34816\nMaaa 100,168,216\n"
"v_and_b32 v253, 0xffff0000, v224\nMU 32,244,252\nMaaa 100,172,220\nTa 236,17,49152\nMaaa 68,172,212\nTa 238,17,51200\nMaaa 68,168,208\nMU 33,244,253\nMU 40,248,252\n"
"Maaa 36,168,200\nTa 240,18,32768\nMaaa 36,172,204\nTa 242,18,34816\nD0 3,20,48,1024\nMaaa 4,172,196\nMU 41,248,253\nv_lshlrev_b32 v252, 16, v225\nMaaa 4,168,192\nTa 244,18,49152\n"
"Maaa 8,176,192\nTa 246,18,51200\nv_and_b32 v253, 0xffff0000, v225\nMaaa 8,180,196\nMU 34,244,252\nMaaa 40,180,204\nTa 248,19,32768\nMaaa 40,176,200\nTa 250,19,34816\n"
"MU 35,244,253\nMaaa 72,176,208\nMU 42,248,252\nMaaa 72,180,212\nTa 252,19,49152\nD0 4,20,48,2048\nMaaa 104,180,220\nTa 254,19,51200\nMU 43,248,253\nMaaa 104,176,216\n"
"v_lshlrev_b32 v252, 16, v226\nMaaa 108,184,216\nv_and_b32 v253, 0xffff0000, v226\nMU 36,244,252\nMaaa 108,188,220\nMU 37,244,253\nMaaa 76,188,212\nMU 44,248,252\nMaaa 76,184,208\n"
"MU 45,248,253\nv_lshlrev_b32 v252, 16, v227\nMaaa 44,184,200\nv_and_b32 v253, 0xffff0000, v227\nMaaa 44,188,204\nD0 5,20,48,3072\nMaaa 12,188,196\nMU 38,244,252\nMU 39,244,253\n"
"Maaa 12,184,192\nWL 0\nMvaa 80,224,128\nTa 160,12,32768\nMU 46,248,252\nMvaa 80,228,132\nTa 162,12,34816\nMvaa 112,228,140\ns_add_u32 m0, s47, -32768\ns_add_u32 s45, s45, 128\n"
"Mvaa 112,224,136\nTa 164,12,49152\nMU 47,248,253\nMvaa 144,224,144\nTa 166,12,51200\nMvaa 144,228,148\nD0 2,20,45,0\nMvaa 176,228,156\nTa 168,13,32768\n"
"v_lshlrev_b32 v252, 16, v228\nMvaa 176,224,152\nTa 170,13,34816\nMvaa 180,232,152\nv_and_b32 v253, 0xffff0000, v228\nFM 32,245,252\nMvaa 180,236,156\nTa 172,13,49152\n"
"Mvaa 148,236,148\nTa 174,13,51200\nMvaa 148,232,144\nFM 33,245,253\nFM 40,249,252\nMvaa 116,232,136\nTa 176,14,32768\nMvaa 116,236,140\nTa 178,14,34816\nD0 3,20,45,1024\n"
"Mvaa 84,236,132\nFM 41,249,253\nv_lshlrev_b32 v252, 16, v229\nMvaa 84,232,128\nTa 180,14,49152\nMvaa 88,240,128\nTa 182,14,51200\nv_and_b32 v253, 0xffff0000, v229\n"
"Mvaa 88,244,132\nFM 34,245,252\nMvaa 120,244,140\nTa 184,15,32768\nMvaa 120,240,136\nTa 186,15,34816\nFM 35,245,253\nMvaa 152,240,144\nFM 42,249,252\nMvaa 152,244,148\n"
"Ta 188,15,49152\nD0 4,20,45,2048\nMvaa 184,244,156\nTa 190,15,51200\nFM 43,249,253\nMvaa 184,240,152\nv_lshlrev_b32 v252, 16, v230\nMvaa 188,248,152\n"
"v_and_b32 v253, 0xffff0000, v230\nFM 36,245,252\nMvaa 188,252,156\nFM 37,245,253\nMvaa 156,252,148\nFM 44,249,252\nMvaa 156,248,144\nFM 45,249,253\nv_lshlrev_b32 v252, 16, v231\n"
"Mvaa 124,248,136\nv_and_b32 v253, 0xffff0000, v231\nMvaa 124,252,140\nD0 5,20,45,3072\nMvaa 92,252,132\nFM 38,245,252\nFM 39,245,253\nMvaa 92,248,128\n"
"s_waitcnt vmcnt(16) lgkmcnt(0)\ns_barrier\nMvaa 64,160,128\nRa 192,10,49152\nFM 46,249,252\nMvaa 64,164,132\nFM 47,249,253\nMvaa 96,164,140\nRa 196,11,49152\n"
"s_add_u32 m0, s47, 32768\ns_add_u32 s44, s44, 0x40000\nMvaa 96,160,136\nv_lshlrev_b32 v252, 16, v232\nv_and_b32 v253, 0xffff0000, v232\nMvaa 128,160,144\nRa 200,10,51200\n"
"Mvaa 128,164,148\nD0 6,24,44,0\nMvaa 160,164,156\nRa 204,11,51200\nFM 32,246,252\nMvaa 160,160,152\nFM 33,246,253\nMvaa 164,168,152\nRa 208,10,57344\nFM 40,250,252\n"
"Mvaa 164,172,156\nFM 41,250,253\nMvaa 132,172,148\nRa 212,11,57344\nMvaa 132,168,144\nv_lshlrev_b32 v252, 16, v233\nv_and_b32 v253, 0xffff0000, v233\nMvaa 100,168,136\n"
"Ra 216,10,59392\nMvaa 100,172,140\nD0 7,24,44,1024\nMvaa 68,172,132\nRa 220,11,59392\nFM 34,246,252\nMvaa 68,168,128\nFM 35,246,253\nMvaa 72,176,128\nFM 42,250,252\nFM 43,250,253\n"
"Mvaa 72,180,132\nv_lshlrev_b32 v252, 16, v234\nMvaa 104,180,140\nv_and_b32 v253, 0xffff0000, v234\nMvaa 104,176,136\nFM 36,246,252\nFM 37,246,253\nMvaa 136,176,144\nFM 44,250,252\n"
"Mvaa 136,180,148\nD0 8,24,44,2048\nMvaa 168,180,156\nFM 45,250,253\nv_lshlrev_b32 v252, 16, v235\nMvaa 168,176,152\nv_and_b32 v253, 0xffff0000, v235\nMvaa 172,184,152\n"
"FM 38,246,252\nFM 39,246,253\nMvaa 172,188,156\nFM 46,250,252\nMvaa 140,188,148\nFM 47,250,253\nMvaa 140,184,144\nv_lshlrev_b32 v252, 16, v236\nv_and_b32 v253, 0xffff0000, v236\n"
"Mvaa 108,184,136\nFM 32,247,252\nMvaa 108,188,140\nD0 9,24,44,3072\nMvaa 76,188,132\nFM 33,247,253\nFM 40,251,252\nMvaa 76,184,128\nWL 0\nMaaa 0,160,192\nRa 128,10,0\n"
"FM 41,251,253\nMaaa 0,164,196\nv_lshlrev_b32 v252, 16, v237\nMaaa 32,164,204\nRa 132,11,0\ns_add_u32 m0, s47, 49152\ns_add_u32 s44, s44, 0x40000\nMaaa 32,160,200\n"
"v_and_b32 v253, 0xffff0000, v237\nFM 34,247,252\nMaaa 64,160,208\nRa 136,10,2048\nMaaa 64,164,212\nD0 6,24,44,0\nMaaa 96,164,220\nRa 140,11,2048\nFM 35,247,253\nMaaa 96,160,216\n"
"FM 42,251,252\nMaaa 100,168,216\nRa 144,10,8192\nFM 43,251,253\nMaaa 100,172,220\nv_lshlrev_b32 v252, 16, v238\nMaaa 68,172,212\nRa 148,11,8192\nMaaa 68,168,208\n"
"v_and_b32 v253, 0xffff0000, v238\nFM 36,247,252\nMaaa 36,168,200\nRa 152,10,10240\nMaaa 36,172,204\nD0 7,24,44,1024\nMaaa 4,172,196\nRa 156,11,10240\nFM 37,247,253\n"
"Maaa 4,168,192\nFM 44,251,252\nMaaa 8,176,192\nFM 45,251,253\nv_lshlrev_b32 v252, 16, v239\nMaaa 8,180,196\nv_and_b32 v253, 0xffff0000, v239\nMaaa 40,180,204\nFM 38,247,252\n"
"Maaa 40,176,200\nFM 39,247,253\nFM 46,251,252\nMaaa 72,176,208\nFM 47,251,253\nMaaa 72,180,212\nD0 8,24,44,2048\nMaaa 104,180,220\nv_permlane32_swap_b32 v32, v40\n"
"v_permlane32_swap_b32 v33, v41\nMaaa 104,176,216\nv_permlane32_swap_b32 v34, v42\nMaaa 108,184,216\nv_permlane32_swap_b32 v35, v43\nv_permlane32_swap_b32 v36, v44\n"
"Maaa 108,188,220\nv_permlane32_swap_b32 v37, v45\nMaaa 76,188,212\nv_permlane32_swap_b32 v38, v46\nMaaa 76,184,208\nv_permlane32_swap_b32 v39, v47\nAD 32,32,40\nMaaa 44,184,200\n"
"AD 33,33,41\nMaaa 44,188,204\nD0 9,24,44,3072\nMaaa 12,188,196\nAD 34,34,42\nAD 35,35,43\nMaaa 12,184,192\nWV 16\ns_barrier\nMaaa 16,224,192\nTa 160,12,0\nAD 36,36,44\n"
"Maaa 16,228,196\nTa 162,12,2048\nMaaa 48,228,204\ns_add_u32 m0, s47, -16384\ns_add_u32 s48, s48, 128\nMaaa 48,224,200\nTa 164,12,16384\nAD 37,37,45\nMaaa 80,224,208\n"
"Ta 166,12,18432\nMaaa 80,228,212\nD0 2,20,48,0\nMaaa 112,228,220\nTa 168,13,0\nAD 38,38,46\nMaaa 112,224,216\nTa 170,13,2048\nMaaa 116,232,216\nAD 39,39,47\nMaaa 116,236,220\n"
"Ta 172,13,16384\nMaaa 84,236,212\nTa 174,13,18432\nMaaa 84,232,208\nv_permlane16_swap_b32 v32, v36\nv_permlane16_swap_b32 v33, v37\nMaaa 52,232,200\nTa 176,14,0\nMaaa 52,236,204\n"
"Ta 178,14,2048\nD0 3,20,48,1024\nMaaa 20,236,196\nv_permlane16_swap_b32 v34, v38\nv_permlane16_swap_b32 v35, v39\nMaaa 20,232,192\nTa 180,14,16384\nMaaa 24,240,192\n"
"Ta 182,14,18432\nAD 32,32,36\nMaaa 24,244,196\nAD 33,33,37\nMaaa 56,244,204\nTa 184,15,0\nMaaa 56,240,200\nTa 186,15,2048\nAD 34,34,38\nMaaa 88,240,208\nAD 35,35,39\n"
"Maaa 88,244,212\nTa 188,15,16384\nD0 4,20,48,2048\nMaaa 120,244,220\nTa 190,15,18432\nSv 32,51,16,4,0\nMaaa 120,240,216\nMaaa 124,248,216\nMaaa 124,252,220\nMaaa 92,252,212\n"
"Maaa 92,248,208\nMaaa 60,248,200\nMaaa 60,252,204\nD0 5,20,48,3072\nMaaa 28,252,196\nMaaa 28,248,192\nWL 0\nMvaa 64,160,128\nTa 224,16,0\ns_lshr_b32 s3, s11, 1\nMvaa 64,164,132\n"
"Ta 226,16,2048\ns_add_u32 s6, s56, 0\nMvaa 96,164,140\ns_lshl_b32 s3, s3, 13\nMvaa 96,160,136\nTa 228,16,16384\n"
"buffer_load_dwordx4 v[192:195], v1, s[16:19], s6 offen offset:0 sc1\nMvaa 128,160,144\nTa 230,16,18432\nMvaa 128,164,148\ns_lshl_b32 s2, s11, 12\nMvaa 160,164,156\nTa 232,17,0\n"
"Mvaa 160,160,152\nTa 234,17,2048\nMvaa 164,168,152\ns_add_u32 s2, s2, s3\ns_add_u32 s2, s2, s72\nMvaa 164,172,156\nTa 236,17,16384\n"
"buffer_load_dwordx4 v[196:199], v1, s[16:19], s6 offen offset:1024 sc1\nMvaa 132,172,148\nTa 238,17,18432\nMvaa 132,168,144\ns_lshl_b32 s3, s11, 12\nMvaa 100,168,136\nTa 240,18,0\n"
"Mvaa 100,172,140\nTa 242,18,2048\nMvaa 68,172,132\ns_add_u32 s89, s3, 0x0\nMvaa 68,168,128\nTa 244,18,16384\n"
"buffer_load_dwordx4 v[200:203], v1, s[16:19], s6 offen offset:2048 sc1\nMvaa 72,176,128\nTa 246,18,18432\ns_mov_b32 s88, 0xffff8000\nMvaa 72,180,132\ns_lshl_b32 s4, s13, 1\n"
"Mvaa 104,180,140\nTa 248,19,0\nMvaa 104,176,136\nTa 250,19,2048\nMvaa 136,176,144\ns_sub_u32 s79, 15, s4\nMvaa 136,180,148\nTa 252,19,16384\n"
"buffer_load_dwordx4 v[204:207], v1, s[16:19], s6 offen offset:3072 sc1\nMvaa 168,180,156\nTa 254,19,18432\nMvaa 168,176,152\ns_add_u32 s96, s2, 0x1310000\nMvaa 172,184,152\n"
"s_mov_b32 s63, 0x53800000\ns_mov_b32 s64, 0x2b800000\nMvaa 172,188,156\ns_sub_u32 s98, s4, 1\ns_add_u32 s6, s56, 4096\nMvaa 140,188,148\ns_lshl_b32 s5, s13, 5\nMvaa 140,184,144\n"
"s_add_u32 s5, s5, s11\nbuffer_load_dwordx4 v[208:211], v1, s[16:19], s6 offen offset:0 sc1\nMvaa 108,184,136\ns_lshl_b32 s65, s5, 9\nMvaa 108,188,140\ns_add_u32 s65, s65, s72\n"
"Mvaa 76,188,132\ns_add_u32 s65, s65, 0x1580000\nMvaa 76,184,128\ns_waitcnt vmcnt(18) lgkmcnt(0)\ns_barrier\nMvaa 80,224,128\nRa 192,10,16384\ns_and_b32 s66, s72, 0x2000\n"
"Mvaa 80,228,132\ns_add_u32 s66, s66, s56\nbuffer_load_dwordx4 v[212:215], v1, s[16:19], s6 offen offset:1024 sc1\nMvaa 112,228,140\nRa 196,11,16384\nMvaa 112,224,136\n"
"s_add_u32 s66, s66, 0x100000\nMvaa 144,224,144\nRa 200,10,18432\nMvaa 144,228,148\ns_and_b32 s5, s12, 1\nMvaa 176,228,156\nRa 204,11,18432\nMvaa 176,224,152\n"
"s_mul_i32 s58, s5, 0x202000\nbuffer_load_dwordx4 v[216:219], v1, s[16:19], s6 offen offset:2048 sc1\nMvaa 180,232,152\nRa 208,10,24576\ns_xor_b32 s58, s58, s72\nMvaa 180,236,156\n"
"s_add_u32 s58, s58, 0x15c0000\nMvaa 148,236,148\nRa 212,11,24576\nMvaa 148,232,144\ns_lshl_b32 s5, s5, 8\nMvaa 116,232,136\nRa 216,10,26624\nMvaa 116,236,140\n"
"s_add_u32 s61, s58, s5\nbuffer_load_dwordx4 v[220:223], v1, s[16:19], s6 offen offset:3072 sc1\nMvaa 84,236,132\nRa 220,11,26624\nMvaa 84,232,128\ns_lshl4_add_u32 s60, s13, s61\n"
"Mvaa 88,240,128\ns_add_u32 s54, s60, 0\ns_xor_b32 s60, s54, 0x100\nMvaa 88,244,132\ns_cmp_eq_u32 s11, 0\ns_cselect_b32 s54, s54, 0x40000000\ns_add_u32 s6, s56, 16384\n"
"Mvaa 120,244,140\ns_getpc_b64 s[8:9]\n.PC36:\ns_add_u32 s8, s8, .HD13-.PC36\ns_addc_u32 s9, s9, 0\nMvaa 120,240,136\ns_mov_b32 s97, 0xff800000\n"
"buffer_load_dwordx4 v[224:227], v1, s[16:19], s6 offen offset:0 sc1\nMvaa 152,240,144\ns_mov_b32 s90, 3\ns_cmp_lt_u32 s11, 2\ns_cselect_b32 s92, 2, 0\nMvaa 152,244,148\n"
"s_add_u32 s86, s2, 0x20000\nMvaa 184,244,156\ns_add_u32 s87, s2, 0x410000\ns_cmp_eq_u32 s13, 7\ns_cselect_b32 s92, s92, 0\nMvaa 184,240,152\ns_sub_u32 s4, s13, 2\n"
"s_cmp_lt_u32 s4, 4\ns_cselect_b32 s4, s12, 15\nMvaa 188,248,152\ns_cmp_lt_u32 s4, 14\ns_cselect_b32 s5, s11, 1\nMvaa 188,252,156\ns_cmp_eq_u32 s5, 0\ns_cselect_b32 s91, 1, 100\n"
"buffer_load_dwordx4 v[228:231], v1, s[16:19], s6 offen offset:1024 sc1\nMvaa 156,252,148\nv_and_b32 v27, 0x70, v1\n"
"buffer_load_dword v27, v27, s[16:19], s61 offen offset:128 sc0 sc1\nMvaa 156,248,144\ns_lshr_b32 s2, s13, 2\ns_bfe_u32 s3, s13, 0x10001\nMvaa 124,248,136\ns_mul_i32 s3, s3, 17\n"
"s_add_u32 s2, s2, s3\nMvaa 124,252,140\ns_lshl2_add_u32 s3, s14, 0\nMvaa 92,252,132\ns_lshl_b32 s3, s3, 2\ns_lshr_b32 s3, 0xe420ca86, s3\nMvaa 92,248,128\nWL 0\nMaaa 16,224,192\n"
"Ra 128,10,32768\ns_and_b32 s3, s3, 15\nbuffer_load_dwordx4 v[232:235], v1, s[16:19], s6 offen offset:2048 sc1\nMaaa 16,228,196\ns_add_u32 s2, s2, s3\ns_cmp_eq_u32 s2, 32\n"
"s_cselect_b32 s4, 16, s2\nMaaa 48,228,204\nRa 132,11,32768\ns_mul_i32 s75, s4, 65536\nMaaa 48,224,200\ns_lshr_b32 s2, s13, 2\ns_bfe_u32 s3, s13, 0x10001\nMaaa 80,224,208\n"
"Ra 136,10,34816\nMaaa 80,228,212\ns_mul_i32 s3, s3, 17\ns_add_u32 s2, s2, s3\nMaaa 112,228,220\nRa 140,11,34816\ns_lshl2_add_u32 s3, s14, 1\n"
"buffer_load_dwordx4 v[236:239], v1, s[16:19], s6 offen offset:3072 sc1\nMaaa 112,224,216\ns_lshl_b32 s3, s3, 2\nMaaa 116,232,216\nRa 144,10,40960\ns_lshr_b32 s3, 0xe420ca86, s3\n"
"Maaa 116,236,220\ns_and_b32 s3, s3, 15\ns_add_u32 s2, s2, s3\nMaaa 84,236,212\nRa 148,11,40960\ns_cmp_eq_u32 s2, 32\ns_cselect_b32 s4, 16, s2\ns_add_u32 s6, s56, 20480\n"
"Maaa 84,232,208\ns_mul_i32 s76, s4, 65536\ns_lshr_b32 s2, s13, 2\nMaaa 52,232,200\nRa 152,10,43008\ns_bfe_u32 s3, s13, 0x10001\n"
"buffer_load_dwordx4 v[240:243], v1, s[16:19], s6 offen offset:0 sc1\nMaaa 52,236,204\ns_mul_i32 s3, s3, 17\nMaaa 20,236,196\nRa 156,11,43008\ns_add_u32 s2, s2, s3\n"
"Maaa 20,232,192\ns_lshl2_add_u32 s3, s14, 2\ns_lshl_b32 s3, s3, 2\nMaaa 24,240,192\ns_lshr_b32 s3, 0xe420ca86, s3\ns_and_b32 s3, s3, 15\nMaaa 24,244,196\ns_add_u32 s2, s2, s3\n"
"s_cmp_eq_u32 s2, 32\ns_cselect_b32 s4, 16, s2\nMaaa 56,244,204\ns_mul_i32 s77, s4, 65536\ns_lshr_b32 s2, s13, 2\n"
"buffer_load_dwordx4 v[244:247], v1, s[16:19], s6 offen offset:1024 sc1\nMaaa 56,240,200\ns_bfe_u32 s3, s13, 0x10001\ns_mul_i32 s3, s3, 17\nMaaa 88,240,208\ns_add_u32 s2, s2, s3\n"
"Maaa 88,244,212\ns_lshl2_add_u32 s3, s14, 3\ns_lshl_b32 s3, s3, 2\nMaaa 120,244,220\ns_lshr_b32 s3, 0xe420ca86, s3\ns_and_b32 s3, s3, 15\nMaaa 120,240,216\ns_add_u32 s2, s2, s3\n"
"s_cmp_eq_u32 s2, 32\ns_cselect_b32 s4, 16, s2\nMaaa 124,248,216\ns_mul_i32 s78, s4, 65536\ns_lshr_b32 s3, s11, 1\n"
"buffer_load_dwordx4 v[248:251], v1, s[16:19], s6 offen offset:2048 sc1\nMaaa 124,252,220\ns_lshl_b32 s3, s3, 15\ns_add_u32 s2, s72, s3\nMaaa 92,252,212\ns_and_b32 s3, s11, 1\n"
"Maaa 92,248,208\ns_lshl_b32 s3, s3, 12\ns_add_u32 s2, s2, s3\nMaaa 60,248,200\ns_add_u32 s2, s2, 0x0\ns_mov_b32 s55, 16384\nMaaa 60,252,204\ns_add_u32 s75, s75, s2\n"
"Maaa 28,252,196\ns_add_u32 s76, s76, s2\ns_add_u32 s77, s77, s2\nbuffer_load_dwordx4 v[252:255], v1, s[16:19], s6 offen offset:3072 sc1\nMaaa 28,248,192\nWV 22\ns_barrier\n"
"Maaa 0,160,192\nTa 224,16,32768\ns_add_u32 s78, s78, s2\nMaaa 0,164,196\nTa 226,16,34816\nMaaa 32,164,204\nMaaa 32,160,200\nTa 228,16,49152\nMaaa 64,160,208\nTa 230,16,51200\n"
"Maaa 64,164,212\nMaaa 96,164,220\nTa 232,17,32768\nMaaa 96,160,216\nTa 234,17,34816\nMaaa 100,168,216\nWV 6\nv_cmp_ne_u32 vcc, s15, v27\ns_nop 0\ns_cbranch_vccnz .S37\n.S38:\n"
"s_sub_u32 s45, s86, 131072\nMaaa 100,172,220\nTa 236,17,49152\ns_add_u32 m0, s47, 0\ns_add_u32 s46, s45, 0x0\nMaaa 68,172,212\nTa 238,17,51200\nMaaa 68,168,208\nD1 1,16,46,0\n"
"s_mov_b32 s36, s68\nMaaa 36,168,200\nTa 240,18,32768\nMaaa 36,172,204\nTa 242,18,34816\nMaaa 4,172,196\ns_and_b32 s37, s69, 0xffff\nMaaa 4,168,192\nTa 244,18,49152\n"
"Maaa 8,176,192\nTa 246,18,51200\nD1 1,16,46,1024\nMaaa 8,180,196\ns_mov_b32 s38, 0x1c0000\nMaaa 40,180,204\nTa 248,19,32768\nMaaa 40,176,200\nTa 250,19,34816\nMaaa 72,176,208\n"
"s_mov_b32 s39, 0x20000\nMaaa 72,180,212\nTa 252,19,49152\nD1 1,16,46,2048\nMaaa 104,180,220\nTa 254,19,51200\ns_and_b32 s2, s74, 15\nMaaa 104,176,216\ns_max_u32 s2, s2, 2\n"
"Maaa 108,184,216\ns_sub_u32 s2, s2, 2\nMaaa 108,188,220\ns_mul_i32 s3, s73, 14\nMaaa 76,188,212\nD1 1,16,46,3072\nMaaa 76,184,208\ns_add_u32 s2, s2, s3\n"
"s_lshl2_add_u32 s2, s2, s11\nMaaa 44,184,200\ns_mul_i32 s3, s2, 4682\nMaaa 44,188,204\ns_lshr_b32 s3, s3, 16\nMaaa 12,188,196\ns_mul_i32 s22, s3, 14\nMaaa 12,184,192\nWL 0\n"
"Mvaa 80,224,128\nTa 160,12,32768\ns_add_u32 m0, s47, -65536\ns_add_u32 s46, s45, 0x10000\nD1 1,16,46,0\nMvaa 80,228,132\nTa 162,12,34816\nMvaa 84,236,132\ns_sub_u32 s22, s2, s22\n"
"Mvaa 84,232,128\nTa 164,12,49152\nMvaa 88,240,128\nTa 166,12,51200\nMvaa 88,244,132\nD1 1,16,46,1024\nMvaa 92,252,132\nTa 168,13,32768\ns_and_b32 s21, s3, 28\nMvaa 92,248,128\n"
"Ta 170,13,34816\nMvaa 124,248,136\ns_add_u32 s21, s21, s3\nMvaa 124,252,140\nTa 172,13,49152\nMvaa 120,244,140\nTa 174,13,51200\nD1 1,16,46,2048\nMvaa 120,240,136\n"
"s_lshl_b32 s21, s21, 4\ns_add_u32 s21, s21, 1024\nMvaa 116,232,136\nTa 176,14,32768\nCV 40,80,81\nMvaa 116,236,140\nTa 178,14,34816\nMvaa 112,228,140\ns_xor_b32 s3, s3, 28\n"
"CV 41,82,83\nMvaa 112,224,136\nTa 180,14,49152\nD1 1,16,46,3072\nMvaa 144,224,144\nTa 182,14,51200\ns_bfe_u32 s41, s3, 0x30002\nMvaa 144,228,148\ns_cmp_gt_u32 s22, 6\n"
"s_cselect_b32 s2, 1179648, 0\ns_cselect_b32 s1, 16, 0\nMvaa 148,236,148\nTa 184,15,32768\nMvaa 148,232,144\nTa 186,15,34816\nCV 42,84,85\nMvaa 152,240,144\ns_add_u32 s41, s41, s1\n"
"Mvaa 152,244,148\nTa 188,15,49152\nCV 43,86,87\nMvaa 156,252,148\nTa 190,15,51200\ns_lshl4_add_u32 s41, s41, 0xc048400\nMvaa 156,248,144\ns_and_b32 s1, s3, 28\nCV 44,88,89\n"
"Mvaa 188,248,152\ns_add_u32 s3, s3, s1\nMvaa 188,252,156\ns_lshl_b32 s3, s3, 11\nMvaa 184,244,156\ns_add_u32 s2, s2, s3\nCV 45,90,91\nMvaa 184,240,152\ns_lshl_b32 s3, s22, 17\n"
"s_add_u32 s2, s2, s3\nMvaa 180,232,152\ns_add_u32 s20, s2, 0xce0000\nCV 46,92,93\nMvaa 180,236,156\ns_lshl_b32 s2, s22, 7\nMvaa 176,228,156\ns_add_u32 s40, s2, 0xc04b800\n"
"CV 47,94,95\nMvaa 176,224,152\nWL 0\ns_barrier\nMvaa 64,160,128\nRa 192,10,49152\ns_add_u32 m0, s47, 32768\ns_add_u32 s46, s45, 0x8000\nMvaa 64,164,132\ns_mul_i32 s2, s21, 896\n"
"Mvaa 68,172,132\nRa 196,11,49152\nD1 1,16,46,0\nMvaa 68,168,128\ns_lshl_b32 s3, s22, 6\nCV 60,120,121\nMvaa 72,176,128\nRa 200,10,51200\nMvaa 72,180,132\ns_add_u32 s22, s2, s3\n"
"CV 61,122,123\nMvaa 76,188,132\nRa 204,11,51200\ns_add_u32 s23, s22, 57344\nMvaa 76,184,128\nD1 1,16,46,1024\nCV 62,124,125\nMvaa 108,184,136\nRa 208,10,57344\nMvaa 108,188,140\n"
"CV 63,126,127\nMvaa 104,180,140\nRa 212,11,57344\nCV 56,112,113\nMvaa 104,176,136\nCV 57,114,115\nMvaa 100,168,136\nRa 216,10,59392\nD1 1,16,46,2048\nMvaa 100,172,140\n"
"CV 58,116,117\nMvaa 96,164,140\nRa 220,11,59392\nCV 59,118,119\nMvaa 96,160,136\nCV 32,64,65\nMvaa 128,160,144\nCV 33,66,67\nCV 34,68,69\nMvaa 128,164,148\nD1 1,16,46,3072\n"
"Mvaa 132,172,148\nCV 35,70,71\nMvaa 132,168,144\nCV 36,72,73\nCV 37,74,75\nMvaa 136,176,144\ns_add_u32 m0, s47, 49152\ns_add_u32 s46, s45, 0x400000\nMvaa 136,180,148\nCV 38,76,77\n"
"CV 39,78,79\nMvaa 140,188,148\nD1 1,16,46,0\nMvaa 140,184,144\nCV 52,104,105\nCV 53,106,107\nMvaa 172,184,152\nCV 54,108,109\nMvaa 172,188,156\nCV 55,110,111\nMvaa 168,180,156\n"
"CV 48,96,97\nCV 49,98,99\nMvaa 168,176,152\nD1 1,16,46,1024\nMvaa 164,168,152\nCV 50,100,101\nCV 51,102,103\nMvaa 164,172,156\nCV 104,144,145\nMvaa 160,164,156\nCV 105,146,147\n"
"CV 106,148,149\nMvaa 160,160,152\nWL 0\nMaaa 0,160,192\nD1 1,16,46,2048\nSv 32,1,16,75,0\nMaaa 0,164,196\nCV 107,150,151\nMaaa 4,172,196\nCV 108,152,153\nCV 109,154,155\n"
"Maaa 4,168,192\nCV 110,156,157\nCV 111,158,159\nMaaa 8,176,192\nCV 124,184,185\nCV 125,186,187\nMaaa 8,180,196\nD1 1,16,46,3072\nMaaa 12,188,196\nCV 126,188,189\nCV 127,190,191\n"
"Maaa 12,184,192\nCV 120,176,177\nCV 121,178,179\nMaaa 44,184,200\ns_add_u32 m0, s47, -49152\ns_add_u32 s46, s45, 0x408000\nSv 36,1,16,75,1024\nMaaa 44,188,204\nCV 122,180,181\n"
"Maaa 40,180,204\nD1 1,16,46,0\nCV 123,182,183\nMaaa 40,176,200\nCV 96,128,129\nCV 97,130,131\nMaaa 36,168,200\nCV 98,132,133\nCV 99,134,135\nMaaa 36,172,204\nCV 100,136,137\n"
"Maaa 32,164,204\nCV 101,138,139\nCV 102,140,141\nMaaa 32,160,200\nD1 1,16,46,1024\nCV 103,142,143\nMaaa 64,160,208\nSv 40,1,16,75,2048\nCV 116,168,169\nMaaa 64,164,212\n"
"CV 117,170,171\nMaaa 68,172,212\nCV 118,172,173\nCV 119,174,175\nMaaa 68,168,208\nCV 112,160,161\nCV 113,162,163\nMaaa 72,176,208\nD1 1,16,46,2048\nCV 114,164,165\n"
"Maaa 72,180,212\nCV 115,166,167\nMaaa 76,188,212\nv_add_u32 v2, 0x10000, v1\ns_lshr_b32 s2, s59, 1\nMaaa 76,184,208\nv_add_u32 v24, s2, v2\nv_xor_b32 v26, 0x1000, v24\n"
"Maaa 108,184,216\nSv 44,1,16,75,3072\nv_lshrrev_b32 v20, 4, v0\nMaaa 108,188,220\nD1 1,16,46,3072\nMaaa 104,180,220\nv_lshlrev_b32 v20, 2, v20\nv_and_b32 v0, 15, v0\n"
"Maaa 104,176,216\nv_sub_u32 v0, v0, v20\ns_lshl_b32 s2, s11, 4\nMaaa 100,168,216\nv_add_u32 v3, s2, v0\nv_subrev_u32 v0, 32, v3\nMaaa 100,172,220\nv_mov_b32 v4, 0\n"
"Maaa 96,164,220\nv_mov_b32 v5, 0\nv_mov_b32 v6, 0\nMaaa 96,160,216\nWV 20\ns_barrier\nMaaa 16,224,192\ns_add_u32 s75, s75, s55\nSv 48,1,16,75,0\nMaaa 16,228,196\nv_mov_b32 v7, 0\n"
"Maaa 20,236,196\nv_mov_b32 v8, 0\nv_mov_b32 v9, 0\nMaaa 20,232,192\nv_mov_b32 v10, 0\nv_mov_b32 v11, 0\nMaaa 24,240,192\nv_mov_b32 v12, 0\nv_mov_b32 v13, 0\nMaaa 24,244,196\n"
"v_mov_b32 v14, 0\nMaaa 28,252,196\nv_mov_b32 v15, 0\nv_mov_b32 v16, 0\nMaaa 28,248,192\nv_mov_b32 v17, 0\nv_mov_b32 v18, 0\nMaaa 60,248,200\nSv 52,1,16,75,1024\nv_mov_b32 v19, 0\n"
"Maaa 60,252,204\nv_mov_b32 v86, 0\nMaaa 56,244,204\nv_mov_b32 v87, 0\nv_mov_b32 v88, 0\nMaaa 56,240,200\nv_mov_b32 v89, 0\nAR 92,0\nAR 93,1\nMaaa 52,232,200\nCV 128,92,93\n"
"AR 92,2\nAR 93,3\nMaaa 52,236,204\nCV 129,92,93\nAR 92,4\nAR 93,5\nMaaa 48,228,204\nRa 128,24,0\nCV 130,92,93\nAR 92,6\nMaaa 48,224,200\nAR 93,7\nCV 131,92,93\nAR 92,8\n"
"Maaa 80,224,208\nSv 56,1,16,75,2048\nRa 132,24,1024\nAR 93,9\nMaaa 80,228,212\nCV 132,92,93\nAR 92,10\nAR 93,11\nMaaa 84,236,212\nRa 136,24,2048\nCV 133,92,93\nAR 92,12\n"
"Maaa 84,232,208\nAR 93,13\nCV 134,92,93\nAR 92,14\nMaaa 88,240,208\nRa 140,24,3072\nAR 93,15\nCV 135,92,93\nMaaa 88,244,212\nAR 92,40\nAR 93,41\nCV 148,92,93\nMaaa 92,252,212\n"
"Ra 144,26,0\nAR 92,42\nAR 93,43\nMaaa 92,248,208\nCV 149,92,93\nAR 92,44\nAR 93,45\nMaaa 124,248,216\nSv 60,1,16,75,3072\nRa 148,26,1024\nCV 150,92,93\nMaaa 124,252,220\nAR 92,46\n"
"AR 93,47\nCV 151,92,93\nMaaa 120,244,220\nRa 152,26,2048\nAR 92,32\nAR 93,33\nMaaa 120,240,216\nCV 144,92,93\nAR 92,34\nAR 93,35\nMaaa 116,232,216\nRa 156,26,3072\nCV 145,92,93\n"
"AR 92,36\nMaaa 116,236,220\nAR 93,37\nCV 146,92,93\nAR 92,38\nMaaa 112,228,220\nAR 93,39\nCV 147,92,93\nAR 92,16\nMaaa 112,224,216\nWL 4\nCvav 32,128,192,4\nRa 160,24,8192\n"
"AR 93,17\nCV 136,92,93\nMvav 32,132,200\nAR 92,18\nAR 93,19\nCV 137,92,93\nMvav 32,136,208\nRa 164,24,9216\nAR 92,20\nAR 93,21\nMvav 32,140,216\nCV 138,92,93\nAR 92,22\nAR 93,23\n"
"Cvav 36,140,220,8\nRa 168,24,10240\nCV 139,92,93\nAR 92,24\nMvav 36,136,212\ns_add_u32 m0, s89, 32768\nAR 93,25\nCV 140,92,93\nMvav 36,132,204\nRa 172,24,11264\nWL 4\ns_barrier\n"
"Mvav 36,128,196\nAR 92,26\nAR 93,27\nCV 141,92,93\nMvav 36,144,228\nRa 176,26,8192\nAR 92,28\nAR 93,29\nSv 96,1,16,76,0\nMvav 36,148,236\ns_add_u32 s46, s86, s88\nCV 142,92,93\n"
"AR 92,30\nMvav 36,152,244\nRa 180,26,9216\nAR 93,31\nCV 143,92,93\nMvav 36,156,252\nAR 92,56\nAR 93,57\nCV 156,92,93\nMvav 32,156,248\nRa 184,26,10240\nAR 92,58\nAR 93,59\n"
"Mvav 32,152,240\nD1 1,16,46,0\nCV 157,92,93\nAR 92,60\nMvav 32,148,232\nRa 188,26,11264\nWL 4\nAR 93,61\nMvav 32,144,224\nCV 158,92,93\nAR 92,62\nAR 93,63\nCvav 40,160,192,12\n"
"CV 159,92,93\nAR 92,48\nAR 93,49\nMvav 40,164,200\nCV 152,92,93\nAR 92,50\nAR 93,51\nMvav 40,168,208\nCV 153,92,93\nAR 92,52\nAR 93,53\nv_and_b32 v27, 0x70, v1\n"
"buffer_load_dword v27, v27, s[16:19], s61 offen offset:136 sc0 sc1\nMvav 40,172,216\nCV 154,92,93\nAR 92,54\nAR 93,55\nCvav 44,172,220,16\nCV 155,92,93\nAR 92,64\nAR 93,65\n"
"Mvav 44,168,212\nD1 1,16,46,1024\nCV 160,92,93\nAR 92,66\nMvav 44,164,204\nWL 0\nAR 93,67\nCV 161,92,93\nMvav 44,160,196\nAR 92,68\nAR 93,69\nCV 162,92,93\nMvav 44,176,228\n"
"AR 92,70\nAR 93,71\nCV 163,92,93\nSv 100,1,16,76,1024\nMvav 44,180,236\ns_add_u32 s88, s88, 32768\nEX 80,32\nMvav 44,184,244\nAR 92,72\nAR 93,73\nCV 164,92,93\nMvav 44,188,252\n"
"EX 81,33\nMvav 40,188,248\nAR 92,74\nAR 93,75\nCV 165,92,93\nMvav 40,184,240\nD1 1,16,46,2048\nAR 92,76\nAR 93,77\nMvav 40,180,232\nCV 166,92,93\nAR 92,78\nAR 93,79\n"
"Mvav 40,176,224\nAD 84,80,81\nCV 64,80,81\nCV 167,92,93\nAR 92,80\nAR 93,81\nCV 168,92,93\nEX 82,34\nAR 92,82\nAR 93,83\nCV 169,92,93\nAR 92,84\nWV 17\nEX 83,35\nAD 84,84,82\n"
"AR 93,85\nCV 170,92,93\nD1 1,16,46,3072\ns_add_u32 m0, s89, 49152\nAR 92,86\ns_barrier\nAR 93,87\nCV 171,92,93\nAD 84,84,83\nCV 65,82,83\nAR 92,88\nRa 128,24,32768\nAR 93,89\n"
"CV 172,92,93\nSv 104,1,16,76,2048\ns_add_u32 s46, s87, s88\nEX 80,36\nRa 132,24,33792\nAR 92,90\nAR 93,91\nEX 81,37\nRa 136,24,34816\nCV 173,92,93\nAR 92,92\nD1 1,16,46,0\n"
"AR 93,93\nCV 174,92,93\nRa 140,24,35840\nAR 92,94\nAR 93,95\nAD 85,80,81\nCV 68,80,81\nCV 175,92,93\nRa 144,26,32768\nAR 92,104\nAR 93,105\nEX 82,38\nCV 180,92,93\n"
"Ra 148,26,33792\nAR 92,106\nAR 93,107\nEX 83,39\nRa 152,26,34816\nAD 85,85,82\nCV 181,92,93\nD1 1,16,46,1024\nAR 92,108\nAR 93,109\nRa 156,26,35840\nCV 182,92,93\nAR 92,110\n"
"AD 85,85,83\nCV 69,82,83\nAR 93,111\nRa 160,24,40960\nCV 183,92,93\nAR 92,96\nSv 108,1,16,76,3072\ns_xor_b32 s89, s89, 0x10000\nEX 80,40\nRa 164,24,41984\nAR 93,97\nCV 176,92,93\n"
"WL 6\nEX 81,41\nRa 168,24,43008\nAD 84,84,80\nAR 92,98\nD1 1,16,46,2048\nAR 93,99\nCV 177,92,93\nRa 172,24,44032\nAR 92,100\nAR 93,101\nAD 84,84,81\nCV 66,80,81\nCV 178,92,93\n"
"Cvav 48,128,192,4\nRa 176,26,40960\nAR 92,102\nAR 93,103\nMvav 48,132,200\nEX 82,42\nCV 179,92,93\nMvav 48,136,208\nRa 180,26,41984\nAR 92,120\nAR 93,121\nMvav 48,140,216\nWL 6\n"
"EX 83,43\nWV 9\nv_cmp_ne_u32 vcc, s15, v27\ns_nop 0\ns_cbranch_vccnz .S40\n.S41:\nCvav 52,140,220,8\nRa 184,26,43008\nAD 84,84,82\nCV 188,92,93\nMvav 52,136,212\nD1 1,16,46,3072\n"
"s_add_u32 m0, s89, 0\nAR 92,122\nMvav 52,132,204\nRa 188,26,44032\nAR 93,123\nCV 189,92,93\nMvav 52,128,196\nAD 84,84,83\nCV 67,82,83\nAR 92,124\nMvav 52,144,228\nRa 128,2,49152\n"
"AR 93,125\nCV 190,92,93\ns_add_u32 s76, s76, s55\nSv 112,1,16,76,0\nMvav 52,148,236\ns_add_u32 s46, s86, s88\nEX 80,44\nMvav 52,152,244\nRa 132,2,50176\n"
"v_xor_b32 v24, 0x10000, v24\nAR 92,126\nMvav 52,156,252\nWL 6\nEX 81,45\nMvav 48,156,248\nRa 136,2,51200\nAD 85,85,80\nAR 93,127\nMvav 48,152,240\nD1 1,16,46,0\nCV 191,92,93\n"
"AR 92,112\nMvav 48,148,232\nRa 140,2,52224\nAR 93,113\nCV 184,92,93\nMvav 48,144,224\nAD 85,85,81\nCV 70,80,81\nAR 92,114\nCvav 56,160,192,12\nRa 144,2,53248\nAR 93,115\n"
"CV 185,92,93\nMvav 56,164,200\nEX 82,46\nAR 92,116\nMvav 56,168,208\nRa 148,2,54272\nv_xor_b32 v26, 0x10000, v26\nAR 93,117\nMvav 56,172,216\nWL 6\nEX 83,47\nCvav 60,172,220,16\n"
"Ra 152,2,55296\nAD 85,85,82\nCV 186,92,93\nMvav 60,168,212\nD1 1,16,46,1024\nAR 92,118\nAR 93,119\nMvav 60,164,204\nRa 156,2,56320\nCV 187,92,93\nMvav 60,160,196\nAD 85,85,83\n"
"CV 71,82,83\nMvav 60,176,228\nRa 160,2,57344\nv_max_f32 v90, v84, v85\nSv 116,1,16,76,1024\nMvav 60,180,236\ns_add_u32 s88, s88, 32768\nEX 80,48\nMvav 60,184,244\nRa 164,2,58368\n"
"v_cmp_lt_f32 vcc, s63, v90\nMvav 60,188,252\nWL 8\nEX 81,49\nMvav 56,188,248\nRa 168,2,59392\ns_cbranch_vccnz .T22\n.T23:\nv_mov_b32 v20, v84\nv_mov_b32 v21, v85\n"
"v_mov_b32 v22, v20\nv_mov_b32 v23, v21\nMvav 56,184,240\nD1 1,16,46,2048\nv_permlane32_swap_b32 v20, v22\nv_permlane32_swap_b32 v21, v23\nAD 20,20,22\nAD 21,21,23\n"
"v_mov_b32 v22, v20\nv_mov_b32 v23, v21\nMvav 56,180,232\nRa 172,2,60416\nv_permlane16_swap_b32 v20, v22\nv_permlane16_swap_b32 v21, v23\nAD 20,20,22\nAD 21,21,23\n"
"v_min_f32 v90, v20, v21\nv_cmp_gt_f32 vcc, s64, v90\ns_nop 0\ns_cbranch_vccnz .T24\n.T25:\nAD 86,86,84\nMvav 56,176,224\nAD 84,80,81\nCV 72,80,81\nZaav 0,128,64\nRa 176,2,61440\n"
"Zaav 4,128,68\nEX 82,50\nZaav 12,132,68\nRa 180,2,62464\nZaav 8,132,64\nWL 8\nEX 83,51\nZaav 16,136,64\nRa 184,2,63488\nAD 84,84,82\nZaav 20,136,68\nD1 1,16,46,3072\n"
"s_add_u32 m0, s89, 16384\nZaav 28,140,68\nRa 188,2,64512\ns_barrier\nZaav 24,140,64\nAD 84,84,83\nCV 73,82,83\nZaav 32,144,64\nRa 128,24,0\nSv 120,1,16,76,2048\nZaav 36,144,68\n"
"s_add_u32 s46, s87, s88\nEX 80,52\nbuffer_store_dword v25, off, s[16:19], s54 offset:12 sc0 sc1\nZaav 44,148,68\nRa 132,24,1024\nv_xor_b32 v2, 0x10000, v2\nZaav 40,148,64\nWL 8\n"
"EX 81,53\nZaav 48,152,64\nRa 136,24,2048\nAD 87,87,85\nZaav 52,152,68\nD1 1,16,46,0\nZaav 60,156,68\nRa 140,24,3072\nZaav 56,156,64\nAD 85,80,81\nCV 76,80,81\nZaav 64,160,64\n"
"Ra 144,26,0\nZaav 68,160,68\nEX 82,54\nZaav 76,164,68\nRa 148,26,1024\nZaav 72,164,64\nWL 8\nEX 83,55\nZaav 80,168,64\nRa 152,26,2048\nAD 85,85,82\nZaav 84,168,68\n"
"D1 1,16,46,1024\nZaav 92,172,68\nRa 156,26,3072\nZaav 88,172,64\nAD 85,85,83\nCV 77,82,83\nZaav 96,176,64\nRa 160,24,8192\nSv 124,1,16,76,3072\nZaav 100,176,68\nEX 80,56\n"
"Zaav 108,180,68\nRa 164,24,9216\nZaav 104,180,64\nWL 6\nEX 81,57\nZaav 112,184,64\nRa 168,24,10240\nAD 84,84,80\nZaav 116,184,68\nD1 1,16,46,2048\nZaav 124,188,68\n"
"Ra 172,24,11264\nZaav 120,188,64\nAD 84,84,81\nCV 74,80,81\nCvav 32,128,192,4\nRa 176,26,8192\nMvav 32,132,200\nEX 82,58\nMvav 32,136,208\nRa 180,26,9216\nMvav 32,140,216\nWL 6\n"
"EX 83,59\nCvav 36,140,220,8\nRa 184,26,10240\nAD 84,84,82\nMvav 36,136,212\nD1 1,16,46,3072\ns_add_u32 m0, s89, 32768\nMvav 36,132,204\nRa 188,26,11264\nMvav 36,128,196\n"
"AD 84,84,83\nCV 75,82,83\nMvav 36,144,228\nRa 128,2,16384\nSv 128,1,16,77,0\nMvav 36,148,236\ns_add_u32 s46, s86, s88\nEX 80,60\nMvav 36,152,244\nRa 132,2,17408\nMvav 36,156,252\n"
"WL 6\nEX 81,61\nMvav 32,156,248\nRa 136,2,18432\nAD 85,85,80\nMvav 32,152,240\nD1 1,16,46,0\nMvav 32,148,232\nRa 140,2,19456\nMvav 32,144,224\nAD 85,85,81\nCV 78,80,81\n"
"Cvav 40,160,192,12\nRa 144,2,20480\nMvav 40,164,200\nEX 82,62\nMvav 40,168,208\nRa 148,2,21504\nMvav 40,172,216\nWL 6\nEX 83,63\nCvav 44,172,220,16\nRa 152,2,22528\nAD 85,85,82\n"
"Mvav 44,168,212\nD1 1,16,46,1024\nMvav 44,164,204\nRa 156,2,23552\nMvav 44,160,196\nAD 85,85,83\nCV 79,82,83\nMvav 44,176,228\nRa 160,2,24576\nv_max_f32 v90, v84, v85\n"
"Sv 132,1,16,77,1024\nMvav 44,180,236\ns_add_u32 s88, s88, 32768\nEX 80,32\nMvav 44,184,244\nRa 164,2,25600\nv_cmp_lt_f32 vcc, s63, v90\nMvav 44,188,252\nWL 8\nEX 81,33\n"
"Mvav 40,188,248\nRa 168,2,26624\ns_cbranch_vccnz .T27\n.T28:\nMvav 40,184,240\nD1 1,16,46,2048\nMvav 40,180,232\nRa 172,2,27648\nAD 86,86,84\nMvav 40,176,224\nAD 84,80,81\n"
"CV 64,80,81\nMaav 0,128,72\nRa 176,2,28672\nv_mov_b32 v112, -1\nMaav 4,128,76\nEX 82,34\nMaav 12,132,76\nRa 180,2,29696\nMaav 8,132,72\ns_waitcnt vmcnt(18) lgkmcnt(8)\nEX 83,35\n"
"Maav 16,136,72\nRa 184,2,30720\nAD 84,84,82\nMaav 20,136,76\nD1 1,16,46,3072\ns_add_u32 m0, s89, 49152\nMaav 28,140,76\nRa 188,2,31744\ns_barrier\nMaav 24,140,72\nAD 84,84,83\n"
"CV 65,82,83\nMaav 32,144,72\nRa 128,24,32768\nSv 136,1,16,77,2048\nMaav 36,144,76\ns_add_u32 s46, s87, s88\nEX 80,36\nMaav 44,148,76\nRa 132,24,33792\nMaav 40,148,72\nWL 8\n"
"EX 81,37\nMaav 48,152,72\nRa 136,24,34816\nAD 87,87,85\nMaav 52,152,76\nD1 1,16,46,0\nMaav 60,156,76\nRa 140,24,35840\nMaav 56,156,72\nAD 85,80,81\nCV 68,80,81\nMaav 64,160,72\n"
"Ra 144,26,32768\nv_mov_b32 v113, -1\nMaav 68,160,76\nEX 82,38\nMaav 76,164,76\nRa 148,26,33792\nMaav 72,164,72\nWL 8\nEX 83,39\nMaav 80,168,72\nRa 152,26,34816\nAD 85,85,82\n"
"Maav 84,168,76\nD1 1,16,46,1024\nMaav 92,172,76\nRa 156,26,35840\nMaav 88,172,72\nAD 85,85,83\nCV 69,82,83\nMaav 96,176,72\nRa 160,24,40960\nSv 140,1,16,77,3072\nMaav 100,176,76\n"
"s_xor_b32 s89, s89, 0x10000\nEX 80,40\nMaav 108,180,76\nRa 164,24,41984\nMaav 104,180,72\nWL 6\nEX 81,41\nMaav 112,184,72\nRa 168,24,43008\nAD 84,84,80\nMaav 116,184,76\n"
"D1 1,16,46,2048\nMaav 124,188,76\nRa 172,24,44032\nMaav 120,188,72\nAD 84,84,81\nCV 66,80,81\nCvav 48,128,192,4\nRa 176,26,40960\nv_mov_b32 v114, -1\nMvav 48,132,200\nEX 82,42\n"
"Mvav 48,136,208\nRa 180,26,41984\nMvav 48,140,216\nWL 6\nEX 83,43\nCvav 52,140,220,8\nRa 184,26,43008\nAD 84,84,82\nv_and_b32 v27, 0x70, v1\n"
"buffer_load_dword v27, v27, s[16:19], s61 offen offset:140 sc0 sc1\nMvav 52,136,212\nD1 1,16,46,3072\ns_add_u32 m0, s89, 0\nMvav 52,132,204\nRa 188,26,44032\nMvav 52,128,196\n"
"AD 84,84,83\nCV 67,82,83\nMvav 52,144,228\nRa 128,2,49152\ns_add_u32 s77, s77, s55\nSv 144,1,16,77,0\nMvav 52,148,236\ns_add_u32 s46, s86, s88\nEX 80,44\nMvav 52,152,244\n"
"Ra 132,2,50176\nv_xor_b32 v24, 0x10000, v24\nMvav 52,156,252\nWL 6\nEX 81,45\nMvav 48,156,248\nRa 136,2,51200\nAD 85,85,80\nMvav 48,152,240\nD1 1,16,46,0\nMvav 48,148,232\n"
"Ra 140,2,52224\nMvav 48,144,224\nAD 85,85,81\nCV 70,80,81\nCvav 56,160,192,12\nRa 144,2,53248\nv_mov_b32 v115, -1\nMvav 56,164,200\nEX 82,46\nMvav 56,168,208\nRa 148,2,54272\n"
"v_xor_b32 v26, 0x10000, v26\nMvav 56,172,216\nWL 6\nEX 83,47\nCvav 60,172,220,16\nRa 152,2,55296\nAD 85,85,82\nMvav 60,168,212\nD1 1,16,46,1024\nMvav 60,164,204\nRa 156,2,56320\n"
"Mvav 60,160,196\nAD 85,85,83\nCV 71,82,83\nMvav 60,176,228\nRa 160,2,57344\nv_max_f32 v90, v84, v85\nSv 148,1,16,77,1024\nMvav 60,180,236\ns_add_u32 s88, s88, 32768\nEX 80,48\n"
"Mvav 60,184,244\nRa 164,2,58368\nv_cmp_lt_f32 vcc, s63, v90\nMvav 60,188,252\nWL 8\nEX 81,49\nMvav 56,188,248\nRa 168,2,59392\ns_cbranch_vccnz .T29\n.T30:\nMvav 56,184,240\n"
"D1 1,16,46,2048\nMvav 56,180,232\nRa 172,2,60416\nAD 86,86,84\nMvav 56,176,224\nAD 84,80,81\nCV 72,80,81\nMaav 0,128,64\nRa 176,2,61440\nMaav 4,128,68\nEX 82,50\nMaav 12,132,68\n"
"Ra 180,2,62464\nMaav 8,132,64\ns_waitcnt vmcnt(18) lgkmcnt(8)\nEX 83,51\nMaav 16,136,64\nRa 184,2,63488\nAD 84,84,82\nMaav 20,136,68\nD1 1,16,46,3072\ns_add_u32 m0, s89, 16384\n"
"Maav 28,140,68\nRa 188,2,64512\ns_barrier\nMaav 24,140,64\nAD 84,84,83\nCV 73,82,83\nMaav 32,144,64\nRa 128,24,0\nSv 152,1,16,77,2048\nMaav 36,144,68\ns_add_u32 s46, s87, s88\n"
"EX 80,52\nMaav 44,148,68\nRa 132,24,1024\nv_xor_b32 v2, 0x10000, v2\nMaav 40,148,64\nWL 8\nEX 81,53\nMaav 48,152,64\nRa 136,24,2048\nAD 87,87,85\n"
"buffer_store_dword v25, off, s[16:19], s54 offset:8 sc0 sc1\nMaav 52,152,68\nD1 1,16,46,0\nMaav 60,156,68\nRa 140,24,3072\nMaav 56,156,64\nAD 85,80,81\nCV 76,80,81\n"
"Maav 64,160,64\nRa 144,26,0\nMaav 68,160,68\nEX 82,54\nMaav 76,164,68\nRa 148,26,1024\nMaav 72,164,64\nWL 8\nEX 83,55\nMaav 80,168,64\nRa 152,26,2048\nAD 85,85,82\nMaav 84,168,68\n"
"D1 1,16,46,1024\nMaav 92,172,68\nRa 156,26,3072\nMaav 88,172,64\nAD 85,85,83\nCV 77,82,83\nMaav 96,176,64\nRa 160,24,8192\nSv 156,1,16,77,3072\nMaav 100,176,68\nEX 80,56\n"
"Maav 108,180,68\nRa 164,24,9216\nMaav 104,180,64\nWL 6\nEX 81,57\nMaav 112,184,64\nRa 168,24,10240\nAD 84,84,80\nMaav 116,184,68\nD1 1,16,46,2048\nMaav 124,188,68\n"
"Ra 172,24,11264\nMaav 120,188,64\nAD 84,84,81\nCV 74,80,81\nCvav 32,128,192,4\nRa 176,26,8192\nMvav 32,132,200\nEX 82,58\nMvav 32,136,208\nRa 180,26,9216\nMvav 32,140,216\nWL 6\n"
"EX 83,59\nCvav 36,140,220,8\nRa 184,26,10240\nAD 84,84,82\nMvav 36,136,212\nD1 1,16,46,3072\ns_add_u32 m0, s89, 32768\nMvav 36,132,204\nRa 188,26,11264\nMvav 36,128,196\n"
"AD 84,84,83\nCV 75,82,83\nMvav 36,144,228\nRa 128,2,16384\nSv 160,1,16,78,0\nMvav 36,148,236\ns_add_u32 s46, s86, s88\nEX 80,60\nMvav 36,152,244\nRa 132,2,17408\nMvav 36,156,252\n"
"WL 6\nEX 81,61\nMvav 32,156,248\nRa 136,2,18432\nAD 85,85,80\nMvav 32,152,240\nD1 1,16,46,0\nMvav 32,148,232\nRa 140,2,19456\nSv 112,1,16,49,0\nMvav 32,144,224\nAD 85,85,81\n"
"CV 78,80,81\nCvav 40,160,192,12\nRa 144,2,20480\nMvav 40,164,200\nEX 82,62\nMvav 40,168,208\nRa 148,2,21504\nMvav 40,172,216\nWL 6\nEX 83,63\nCvav 44,172,220,16\nRa 152,2,22528\n"
"AD 85,85,82\nMvav 44,168,212\nD1 1,16,46,1024\nMvav 44,164,204\nRa 156,2,23552\nMvav 44,160,196\nAD 85,85,83\nCV 79,82,83\nMvav 44,176,228\nRa 160,2,24576\n"
"v_max_f32 v90, v84, v85\nSv 164,1,16,78,1024\nMvav 44,180,236\ns_add_u32 s88, s88, 32768\nEX 80,32\nMvav 44,184,244\nRa 164,2,25600\nv_cmp_lt_f32 vcc, s63, v90\nMvav 44,188,252\n"
"WL 8\nEX 81,33\nMvav 40,188,248\nRa 168,2,26624\ns_cbranch_vccnz .T32\n.T33:\nMvav 40,184,240\nD1 1,16,46,2048\nMvav 40,180,232\nRa 172,2,27648\nAD 86,86,84\nMvav 40,176,224\n"
"AD 84,80,81\nCV 64,80,81\nMaav 0,128,72\nRa 176,2,28672\nMaav 4,128,76\nEX 82,34\nMaav 12,132,76\nRa 180,2,29696\nMaav 8,132,72\ns_waitcnt vmcnt(19) lgkmcnt(8)\nEX 83,35\n"
"Maav 16,136,72\nRa 184,2,30720\nAD 84,84,82\nMaav 20,136,76\nD1 1,16,46,3072\ns_add_u32 m0, s89, 49152\nMaav 28,140,76\nRa 188,2,31744\ns_barrier\nMaav 24,140,72\nAD 84,84,83\n"
"CV 65,82,83\nMaav 32,144,72\nRa 128,24,32768\nSv 168,1,16,78,2048\nMaav 36,144,76\ns_add_u32 s46, s87, s88\nEX 80,36\nMaav 44,148,76\nRa 132,24,33792\nMaav 40,148,72\nWL 8\n"
"EX 81,37\nMaav 48,152,72\nRa 136,24,34816\nAD 87,87,85\nMaav 52,152,76\nD1 1,16,46,0\nMaav 60,156,76\nRa 140,24,35840\nMaav 56,156,72\nAD 85,80,81\nCV 68,80,81\nMaav 64,160,72\n"
"Ra 144,26,32768\nMaav 68,160,76\nEX 82,38\nMaav 76,164,76\nRa 148,26,33792\nMaav 72,164,72\nWL 8\nEX 83,39\nMaav 80,168,72\nRa 152,26,34816\nAD 85,85,82\nMaav 84,168,76\n"
"D1 1,16,46,1024\nMaav 92,172,76\nRa 156,26,35840\nMaav 88,172,72\nAD 85,85,83\nCV 69,82,83\nMaav 96,176,72\nRa 160,24,40960\nSv 172,1,16,78,3072\nMaav 100,176,76\n"
"s_xor_b32 s89, s89, 0x10000\nEX 80,40\nMaav 108,180,76\nRa 164,24,41984\nMaav 104,180,72\nWL 6\nEX 81,41\nMaav 112,184,72\nRa 168,24,43008\nAD 84,84,80\nMaav 116,184,76\n"
"D1 1,16,46,2048\nMaav 124,188,76\nRa 172,24,44032\nMaav 120,188,72\nAD 84,84,81\nCV 66,80,81\nCvav 48,128,192,4\nRa 176,26,40960\nMvav 48,132,200\nEX 82,42\nMvav 48,136,208\n"
"Ra 180,26,41984\nMvav 48,140,216\nWL 6\nEX 83,43\nv_cmp_ne_u32 vcc, s15, v27\ns_nop 0\ns_cbranch_vccnz .S43\n.S44:\nv_and_b32 v27, 0x70, v1\n"
"buffer_load_dword v27, v27, s[16:19], s61 offen offset:12 sc0 sc1\nCvav 52,140,220,8\nRa 184,26,43008\nAD 84,84,82\nMvav 52,136,212\nD1 1,16,46,3072\ns_add_u32 m0, s89, 0\n"
"Mvav 52,132,204\nRa 188,26,44032\nMvav 52,128,196\nAD 84,84,83\nCV 67,82,83\nMvav 52,144,228\nRa 128,2,49152\ns_add_u32 s78, s78, s55\nSv 176,1,16,78,0\nMvav 52,148,236\n"
"s_add_u32 s46, s86, s88\nEX 80,44\nMvav 52,152,244\nRa 132,2,50176\nv_xor_b32 v24, 0x10000, v24\nMvav 52,156,252\nWL 6\nEX 81,45\nMvav 48,156,248\nRa 136,2,51200\nAD 85,85,80\n"
"Mvav 48,152,240\nD1 1,16,46,0\nv_lshrrev_b32 v92, 8, v1\nMvav 48,148,232\nRa 140,2,52224\nSv 112,1,16,49,1024\nMvav 48,144,224\nAD 85,85,81\nCV 70,80,81\nCvav 56,160,192,12\n"
"Ra 144,2,53248\nv_bfe_u32 v93, v1, 6, 2\nMvav 56,164,200\ns_add_u32 s49, s49, 131072\nEX 82,46\nMvav 56,168,208\nRa 148,2,54272\nv_xor_b32 v26, 0x10000, v26\nMvav 56,172,216\n"
"WL 6\nEX 83,47\nCvav 60,172,220,16\nRa 152,2,55296\nAD 85,85,82\nMvav 60,168,212\nD1 1,16,46,1024\nv_cmp_eq_u32 vcc, v92, v93\nv_and_b32 v92, 16, v1\nv_mov_b32 v93, 0x3f80\n"
"v_cndmask_b32 v93, 0, v93, vcc\nMvav 60,164,204\nRa 156,2,56320\nMvav 60,160,196\nAD 85,85,83\nCV 71,82,83\nMvav 60,176,228\nRa 160,2,57344\nv_max_f32 v90, v84, v85\n"
"Sv 180,1,16,78,1024\nMvav 60,180,236\ns_add_u32 s88, s88, 32768\nEX 80,48\nMvav 60,184,244\nRa 164,2,58368\nv_cmp_lt_f32 vcc, s63, v90\nMvav 60,188,252\nWL 8\nEX 81,49\n"
"Mvav 56,188,248\nRa 168,2,59392\ns_cbranch_vccnz .T34\n.T35:\nMvav 56,184,240\nD1 1,16,46,2048\nv_lshlrev_b32 v93, v92, v93\nMvav 56,180,232\nRa 172,2,60416\nAD 86,86,84\n"
"Mvav 56,176,224\nAD 84,80,81\nCV 72,80,81\nMaav 0,128,64\nRa 176,2,61440\nv_and_b32 v92, 32, v1\nMaav 4,128,68\nEX 82,50\nMaav 12,132,68\nRa 180,2,62464\nv_cmp_eq_u32 vcc, 0, v92\n"
"v_mov_b32 v128, 0\nv_mov_b32 v129, 0\nv_cndmask_b32 v130, 0, v93, vcc\nMaav 8,132,64\ns_waitcnt vmcnt(20) lgkmcnt(8)\nEX 83,51\nMaav 16,136,64\nRa 184,2,63488\nAD 84,84,82\n"
"Maav 20,136,68\nD1 1,16,46,3072\ns_add_u32 m0, s89, 16384\nMaav 28,140,68\nRa 188,2,64512\ns_barrier\nMaav 24,140,64\nAD 84,84,83\nCV 73,82,83\nMaav 32,144,64\nRa 128,24,0\n"
"Sv 184,1,16,78,2048\nMaav 36,144,68\ns_add_u32 s46, s87, s88\nEX 80,52\nMaav 44,148,68\nRa 132,24,1024\nv_xor_b32 v2, 0x10000, v2\nMaav 40,148,64\nWL 8\nEX 81,53\nMaav 48,152,64\n"
"Ra 136,24,2048\nAD 87,87,85\nMaav 52,152,68\nD1 1,16,46,0\nv_sub_u32 v131, v93, v130\nMaav 60,156,68\nRa 140,24,3072\nMaav 56,156,64\nAD 85,80,81\nCV 76,80,81\nMaav 64,160,64\n"
"Ra 144,26,0\nv_mov_b32 v132, 0\nMaav 68,160,68\nEX 82,54\nMaav 76,164,68\nRa 148,26,1024\nMaav 72,164,64\nWL 8\nEX 83,55\nMaav 80,168,64\nRa 152,26,2048\nAD 85,85,82\n"
"Maav 84,168,68\nD1 1,16,46,1024\nMaav 92,172,68\nRa 156,26,3072\nMaav 88,172,64\nAD 85,85,83\nCV 77,82,83\nMaav 96,176,64\nRa 160,24,8192\nSv 188,1,16,78,3072\nMaav 100,176,68\n"
"EX 80,56\nMaav 108,180,68\nRa 164,24,9216\nv_mov_b32 v133, 0\nMaav 104,180,64\nWL 6\nEX 81,57\nMaav 112,184,64\nRa 168,24,10240\nAD 84,84,80\nMaav 116,184,68\nD1 1,16,46,2048\n"
"Maav 124,188,68\nRa 172,24,11264\nMaav 120,188,64\nAD 84,84,81\nCV 74,80,81\ns_mov_b32 s99, 11\ns_sub_u32 s90, s90, 3\n.p2align 6\n.A59:\nCvav 32,128,192,4\nRa 176,26,8192\n"
"Mvav 32,132,200\nEX 82,58\nMvav 32,136,208\nRa 180,26,9216\nMvav 32,140,216\nWL 6\nEX 83,59\nCvav 36,140,220,8\nRa 184,26,10240\nAD 84,84,82\nMvav 36,136,212\nD1 1,16,46,3072\n"
"s_add_u32 m0, s89, 32768\nMvav 36,132,204\nRa 188,26,11264\nMvav 36,128,196\nAD 84,84,83\nCV 75,82,83\nMvav 36,144,228\nRa 128,2,16384\nMvav 36,148,236\ns_add_u32 s46, s86, s88\n"
"EX 80,60\nMvav 36,152,244\nRa 132,2,17408\nMvav 36,156,252\nWL 6\nEX 81,61\nMvav 32,156,248\nRa 136,2,18432\nAD 85,85,80\nMvav 32,152,240\nD1 1,16,46,0\nMvav 32,148,232\n"
"Ra 140,2,19456\nSv 112,1,16,49,0\nMvav 32,144,224\nAD 85,85,81\nCV 78,80,81\nCvav 40,160,192,12\nRa 144,2,20480\nMvav 40,164,200\nEX 82,62\nMvav 40,168,208\nRa 148,2,21504\n"
"Mvav 40,172,216\nWL 6\nEX 83,63\nCvav 44,172,220,16\nRa 152,2,22528\nAD 85,85,82\nMvav 44,168,212\nD1 1,16,46,1024\nMvav 44,164,204\nRa 156,2,23552\nMvav 44,160,196\nAD 85,85,83\n"
"CV 79,82,83\nMvav 44,176,228\nRa 160,2,24576\nv_max_f32 v90, v84, v85\nMvav 44,180,236\ns_add_u32 s88, s88, 32768\nEX 80,32\nMvav 44,184,244\nRa 164,2,25600\n"
"v_cmp_lt_f32 vcc, s63, v90\nMvav 44,188,252\nWL 8\nEX 81,33\nMvav 40,188,248\nRa 168,2,26624\ns_cbranch_vccnz .T63\n.T64:\nMvav 40,184,240\nD1 1,16,46,2048\nMvav 40,180,232\n"
"Ra 172,2,27648\nAD 86,86,84\nMvav 40,176,224\nAD 84,80,81\nCV 64,80,81\nMaav 0,128,72\nRa 176,2,28672\nMaav 4,128,76\nEX 82,34\nMaav 12,132,76\nRa 180,2,29696\nMaav 8,132,72\n"
"s_waitcnt vmcnt(13) lgkmcnt(8)\nEX 83,35\nMaav 16,136,72\nRa 184,2,30720\nAD 84,84,82\nMaav 20,136,76\nD1 1,16,46,3072\ns_add_u32 m0, s89, 49152\nMaav 28,140,76\nRa 188,2,31744\n"
"s_barrier\nMaav 24,140,72\nAD 84,84,83\nCV 65,82,83\nMaav 32,144,72\nRa 128,24,32768\nMaav 36,144,76\ns_add_u32 s46, s87, s88\nEX 80,36\nMaav 44,148,76\nRa 132,24,33792\n"
"Maav 40,148,72\nWL 8\nEX 81,37\nMaav 48,152,72\nRa 136,24,34816\nAD 87,87,85\nMaav 52,152,76\nD1 1,16,46,0\nMaav 60,156,76\nRa 140,24,35840\nMaav 56,156,72\nAD 85,80,81\n"
"CV 68,80,81\nMaav 64,160,72\nRa 144,26,32768\nMaav 68,160,76\nEX 82,38\nMaav 76,164,76\nRa 148,26,33792\nMaav 72,164,72\nWL 8\nEX 83,39\nMaav 80,168,72\nRa 152,26,34816\n"
"AD 85,85,82\nMaav 84,168,76\nD1 1,16,46,1024\nMaav 92,172,76\nRa 156,26,35840\nMaav 88,172,72\nAD 85,85,83\nCV 69,82,83\nMaav 96,176,72\nRa 160,24,40960\nMaav 100,176,76\n"
"s_xor_b32 s89, s89, 0x10000\nEX 80,40\nMaav 108,180,76\nRa 164,24,41984\nMaav 104,180,72\nWL 6\nEX 81,41\nMaav 112,184,72\nRa 168,24,43008\nAD 84,84,80\nMaav 116,184,76\n"
"D1 1,16,46,2048\nMaav 124,188,76\nRa 172,24,44032\nMaav 120,188,72\nAD 84,84,81\nCV 66,80,81\nCvav 48,128,192,4\nRa 176,26,40960\nMvav 48,132,200\nEX 82,42\nMvav 48,136,208\n"
"Ra 180,26,41984\nMvav 48,140,216\nWL 6\nEX 83,43\nCvav 52,140,220,8\nRa 184,26,43008\nAD 84,84,82\nMvav 52,136,212\nD1 1,16,46,3072\ns_add_u32 m0, s89, 0\nMvav 52,132,204\n"
"Ra 188,26,44032\nMvav 52,128,196\nAD 84,84,83\nCV 67,82,83\nMvav 52,144,228\nRa 128,2,49152\nMvav 52,148,236\ns_add_u32 s46, s86, s88\nEX 80,44\nMvav 52,152,244\nRa 132,2,50176\n"
"v_xor_b32 v24, 0x10000, v24\nMvav 52,156,252\nWL 6\nEX 81,45\nMvav 48,156,248\nRa 136,2,51200\nAD 85,85,80\nMvav 48,152,240\nD1 1,16,46,0\nMvav 48,148,232\nRa 140,2,52224\n"
"Sv 112,1,16,49,1024\nMvav 48,144,224\nAD 85,85,81\nCV 70,80,81\nCvav 56,160,192,12\nRa 144,2,53248\nMvav 56,164,200\ns_add_u32 s49, s49, 131072\nEX 82,46\nMvav 56,168,208\n"
"Ra 148,2,54272\nv_xor_b32 v26, 0x10000, v26\nMvav 56,172,216\nWL 6\nEX 83,47\nCvav 60,172,220,16\nRa 152,2,55296\nAD 85,85,82\nMvav 60,168,212\nD1 1,16,46,1024\nMvav 60,164,204\n"
"Ra 156,2,56320\nMvav 60,160,196\nAD 85,85,83\nCV 71,82,83\nMvav 60,176,228\nRa 160,2,57344\nv_max_f32 v90, v84, v85\nMvav 60,180,236\ns_add_u32 s88, s88, 32768\nEX 80,48\n"
"Mvav 60,184,244\nRa 164,2,58368\nv_cmp_lt_f32 vcc, s63, v90\nMvav 60,188,252\nWL 8\nEX 81,49\nMvav 56,188,248\nRa 168,2,59392\ns_cbranch_vccnz .T65\n.T66:\nMvav 56,184,240\n"
"D1 1,16,46,2048\nMvav 56,180,232\nRa 172,2,60416\nAD 86,86,84\nMvav 56,176,224\nAD 84,80,81\nCV 72,80,81\nMaav 0,128,64\nRa 176,2,61440\nMaav 4,128,68\nEX 82,50\nMaav 12,132,68\n"
"Ra 180,2,62464\nMaav 8,132,64\ns_waitcnt vmcnt(13) lgkmcnt(8)\nEX 83,51\nMaav 16,136,64\nRa 184,2,63488\nAD 84,84,82\nMaav 20,136,68\nD1 1,16,46,3072\ns_add_u32 m0, s89, 16384\n"
"Maav 28,140,68\nRa 188,2,64512\ns_barrier\nMaav 24,140,64\nAD 84,84,83\nCV 73,82,83\nMaav 32,144,64\nRa 128,24,0\nMaav 36,144,68\ns_add_u32 s46, s87, s88\nEX 80,52\n"
"Maav 44,148,68\nRa 132,24,1024\nv_xor_b32 v2, 0x10000, v2\nMaav 40,148,64\nWL 8\nEX 81,53\nMaav 48,152,64\nRa 136,24,2048\nAD 87,87,85\nMaav 52,152,68\nD1 1,16,46,0\n"
"Maav 60,156,68\nRa 140,24,3072\nMaav 56,156,64\nAD 85,80,81\nCV 76,80,81\nMaav 64,160,64\nRa 144,26,0\nMaav 68,160,68\nEX 82,54\nMaav 76,164,68\nRa 148,26,1024\nMaav 72,164,64\n"
"WL 8\nEX 83,55\nMaav 80,168,64\nRa 152,26,2048\nAD 85,85,82\nMaav 84,168,68\nD1 1,16,46,1024\nMaav 92,172,68\nRa 156,26,3072\nMaav 88,172,64\nAD 85,85,83\nCV 77,82,83\n"
"Maav 96,176,64\nRa 160,24,8192\nMaav 100,176,68\ns_sub_u32 s90, s90, 1\nEX 80,56\nMaav 108,180,68\nRa 164,24,9216\ns_cbranch_scc1 .E61\n.E62:\nMaav 104,180,64\nWL 6\nEX 81,57\n"
"Maav 112,184,64\nRa 168,24,10240\nAD 84,84,80\nMaav 116,184,68\nD1 1,16,46,2048\ns_sub_u32 s99, s99, 1\nMaav 124,188,68\nRa 172,24,11264\nMaav 120,188,64\nAD 84,84,81\n"
"CV 74,80,81\ns_cbranch_scc0 .A59\ns_branch .A67\n.E61:\ns_swappc_b64 s[80:81], s[8:9]\ns_branch .E62\n.HD13:\nbuffer_store_dword v25, off, s[16:19], s54 offset:4 sc0 sc1\n"
"v_cmp_ne_u32 vcc, s15, v27\ns_nop 0\ns_cbranch_vccnz .S46\n.S47:\nv_and_b32 v27, 0x70, v1\nbuffer_load_dword v27, v27, s[16:19], s61 offen offset:8 sc0 sc1\ns_mov_b32 s90, 1\n"
"s_swappc_b64 s[8:9], s[80:81]\nv_cmp_ne_u32 vcc, s15, v27\ns_nop 0\ns_cbranch_vccnz .S49\n.S50:\nv_and_b32 v27, 0xf0, v1\n"
"buffer_load_dword v27, v27, s[16:19], s61 offen offset:4 sc0 sc1\ns_mov_b32 s90, 1\ns_swappc_b64 s[8:9], s[80:81]\nv_cmp_ne_u32 vcc, s15, v27\ns_nop 0\ns_cbranch_vccnz .S52\n"
".S53:\ns_mov_b32 s90, s91\ns_swappc_b64 s[8:9], s[80:81]\ns_branch .FF55\n.A67:\nCvav 32,128,192,4\nRa 176,26,8192\nMvav 32,132,200\nEX 82,58\nMvav 32,136,208\nRa 180,26,9216\n"
"Mvav 32,140,216\nWL 6\nEX 83,59\nCvav 36,140,220,8\nRa 184,26,10240\nAD 84,84,82\nMvav 36,136,212\nD1 1,16,46,3072\ns_add_u32 m0, s89, 32768\nMvav 36,132,204\nRa 188,26,11264\n"
"Mvav 36,128,196\nAD 84,84,83\nCV 75,82,83\ns_mov_b32 s94, s96\ns_add_u32 s95, s96, 65536\nv_and_b32 v108, 0xf0, v1\nMvav 36,144,228\nRa 128,2,16384\nMvav 36,148,236\n"
"s_add_u32 s46, s86, s88\nEX 80,60\nMvav 36,152,244\nRa 132,2,17408\nMvav 36,156,252\nWL 6\nEX 81,61\nMvav 32,156,248\nRa 136,2,18432\nAD 85,85,80\nMvav 32,152,240\nD1 1,16,46,0\n"
"Mvav 32,148,232\nRa 140,2,19456\nSv 112,1,16,49,0\nMvav 32,144,224\nAD 85,85,81\nCV 78,80,81\nbuffer_load_dwordx4 v[100:103], v108, s[16:19], s65 offen offset:0 sc0 sc1\n"
"buffer_load_dwordx4 v[104:107], v108, s[16:19], s65 offen offset:256 sc0 sc1\nCvav 40,160,192,12\nRa 144,2,20480\nMvav 40,164,200\nEX 82,62\nMvav 40,168,208\nRa 148,2,21504\n"
"Mvav 40,172,216\nWL 6\nEX 83,63\nCvav 44,172,220,16\nRa 152,2,22528\nAD 85,85,82\nMvav 44,168,212\nD1 1,16,46,1024\nMvav 44,164,204\nRa 156,2,23552\nMvav 44,160,196\nAD 85,85,83\n"
"CV 79,82,83\nMvav 44,176,228\nRa 160,2,24576\nv_max_f32 v90, v84, v85\nMvav 44,180,236\ns_add_u32 s88, s88, 32768\nEX 80,32\nMvav 44,184,244\nRa 164,2,25600\n"
"v_cmp_lt_f32 vcc, s63, v90\nMvav 44,188,252\nWL 8\nEX 81,33\nMvav 40,188,248\nRa 168,2,26624\ns_cbranch_vccnz .T71\n.T72:\nMvav 40,184,240\nD1 1,16,46,2048\nMvav 40,180,232\n"
"Ra 172,2,27648\nAD 86,86,84\nMvav 40,176,224\nAD 84,80,81\nCV 64,80,81\nMaav 0,128,72\nRa 176,2,28672\nMaav 4,128,76\nEX 82,34\nMaav 12,132,76\nRa 180,2,29696\nMaav 8,132,72\n"
"s_waitcnt vmcnt(15) lgkmcnt(8)\nEX 83,35\nMaav 16,136,72\nRa 184,2,30720\nAD 84,84,82\nMaav 20,136,76\nD1 1,16,46,3072\ns_add_u32 m0, s89, 49152\nMaav 28,140,76\nRa 188,2,31744\n"
"s_barrier\nMaav 24,140,72\nAD 84,84,83\nCV 65,82,83\nMaav 32,144,72\nRa 128,24,32768\nMaav 36,144,76\ns_add_u32 s46, s87, s88\nEX 80,36\nMaav 44,148,76\nRa 132,24,33792\n"
"Maav 40,148,72\nWL 8\nEX 81,37\nMaav 48,152,72\nRa 136,24,34816\nAD 87,87,85\nMaav 52,152,76\nD1 1,16,46,0\nMaav 60,156,76\nRa 140,24,35840\nMaav 56,156,72\nAD 85,80,81\n"
"CV 68,80,81\nMaav 64,160,72\nRa 144,26,32768\nMaav 68,160,76\nEX 82,38\nMaav 76,164,76\nRa 148,26,33792\nMaav 72,164,72\nWL 8\nEX 83,39\nMaav 80,168,72\nRa 152,26,34816\n"
"AD 85,85,82\nMaav 84,168,76\nD1 1,16,46,1024\nMaav 92,172,76\nRa 156,26,35840\nMaav 88,172,72\nAD 85,85,83\nCV 69,82,83\nMaav 96,176,72\nRa 160,24,40960\nMaav 100,176,76\n"
"s_xor_b32 s89, s89, 0x10000\nEX 80,40\nMaav 108,180,76\nRa 164,24,41984\nMaav 104,180,72\nWL 6\nEX 81,41\nMaav 112,184,72\nRa 168,24,43008\nAD 84,84,80\nMaav 116,184,76\n"
"D1 1,16,46,2048\nMaav 124,188,76\nRa 172,24,44032\nMaav 120,188,72\nAD 84,84,81\nCV 66,80,81\nCvav 48,128,192,4\nRa 176,26,40960\nMvav 48,132,200\ns_mov_b32 s86, s94\nEX 82,42\n"
"Mvav 48,136,208\nRa 180,26,41984\nMvav 48,140,216\nWL 6\nEX 83,43\nCvav 52,140,220,8\nRa 184,26,43008\nAD 84,84,82\nMvav 52,136,212\nD1 1,16,46,3072\ns_add_u32 m0, s89, 0\n"
"Mvav 52,132,204\nRa 188,26,44032\nMvav 52,128,196\nAD 84,84,83\nCV 67,82,83\nMvav 52,144,228\nRa 128,2,49152\nMvav 52,148,236\ns_add_u32 s46, s86, s88\nEX 80,44\nMvav 52,152,244\n"
"Ra 132,2,50176\nv_xor_b32 v24, 0x10000, v24\nMvav 52,156,252\nWL 6\nEX 81,45\nMvav 48,156,248\nRa 136,2,51200\nAD 85,85,80\nMvav 48,152,240\nD1 1,16,46,0\nMvav 48,148,232\n"
"Ra 140,2,52224\nSv 112,1,16,49,1024\nMvav 48,144,224\nAD 85,85,81\nCV 70,80,81\nWV 9\nv_xor_b32 v109, s15, v102\nv_xor_b32 v110, s15, v106\nv_or_b32 v109, v109, v110\n"
"v_cmp_ne_u32 vcc, 0, v109\ns_nop 3\ns_cmp_lg_u64 vcc, 0\ns_cselect_b32 s67, 1, 0\ns_cselect_b32 s6, 0x40000000, s66\nCvav 56,160,192,12\nRa 144,2,53248\ns_add_u32 s5, s6, 0\n"
"Mvav 56,164,200\ns_add_u32 s49, s49, 131072\nEX 82,46\nMvav 56,168,208\nRa 148,2,54272\nv_xor_b32 v26, 0x10000, v26\n"
"buffer_load_dwordx4 a[192:195], v1, s[16:19], s5 offen offset:0 sc1\nMvav 56,172,216\nWL 6\nEX 83,47\nCvav 60,172,220,16\nRa 152,2,55296\nAD 85,85,82\nMvav 60,168,212\n"
"D1 1,16,46,1024\nMvav 60,164,204\nRa 156,2,56320\nMvav 60,160,196\nAD 85,85,83\nCV 71,82,83\nMvav 60,176,228\nRa 160,2,57344\nv_max_f32 v90, v84, v85\nMvav 60,180,236\n"
"s_add_u32 s88, s88, 32768\nEX 80,48\nMvav 60,184,244\nRa 164,2,58368\nv_cmp_lt_f32 vcc, s63, v90\nMvav 60,188,252\nWL 8\nEX 81,49\nMvav 56,188,248\nRa 168,2,59392\n"
"s_cbranch_vccnz .T73\n.T74:\nbuffer_load_dwordx4 a[196:199], v1, s[16:19], s5 offen offset:1024 sc1\nMvav 56,184,240\nD1 1,16,46,2048\nMvav 56,180,232\nRa 172,2,60416\n"
"AD 86,86,84\nMvav 56,176,224\nAD 84,80,81\nCV 72,80,81\nMaav 0,128,64\nRa 176,2,61440\nMaav 4,128,68\nEX 82,50\nMaav 12,132,68\nRa 180,2,62464\nMaav 8,132,64\nWL 8\nEX 83,51\n"
"Maav 16,136,64\nRa 184,2,63488\nAD 84,84,82\nbuffer_load_dwordx4 a[200:203], v1, s[16:19], s5 offen offset:2048 sc1\nMaav 20,136,68\nD1 1,16,46,3072\ns_add_u32 m0, s89, 16384\n"
"Maav 28,140,68\nRa 188,2,64512\ns_barrier\nMaav 24,140,64\nAD 84,84,83\nCV 73,82,83\nMaav 32,144,64\nRa 128,24,0\nMaav 36,144,68\ns_add_u32 s46, s87, s88\nEX 80,52\n"
"Maav 44,148,68\nRa 132,24,1024\nv_xor_b32 v2, 0x10000, v2\nMaav 40,148,64\nWL 8\nEX 81,53\nMaav 48,152,64\nRa 136,24,2048\nAD 87,87,85\n"
"buffer_load_dwordx4 a[204:207], v1, s[16:19], s5 offen offset:3072 sc1\nMaav 52,152,68\nD1 1,16,46,0\nMaav 60,156,68\nRa 140,24,3072\nMaav 56,156,64\nAD 85,80,81\nCV 76,80,81\n"
"Maav 64,160,64\nRa 144,26,0\nMaav 68,160,68\ns_cmp_lg_u32 s92, 0\nEX 82,54\nMaav 76,164,68\nRa 148,26,1024\ns_cbranch_scc1 .E69\n.E70:\ns_add_u32 s5, s6, 4096\nMaav 72,164,64\n"
"WL 8\nEX 83,55\nMaav 80,168,64\nRa 152,26,2048\nAD 85,85,82\nbuffer_load_dwordx4 a[208:211], v1, s[16:19], s5 offen offset:0 sc1\nMaav 84,168,68\nD1 1,16,46,1024\nMaav 92,172,68\n"
"Ra 156,26,3072\nMaav 88,172,64\nAD 85,85,83\nCV 77,82,83\nMaav 96,176,64\nRa 160,24,8192\nMaav 100,176,68\nEX 80,56\nMaav 108,180,68\nRa 164,24,9216\nMaav 104,180,64\nWL 6\n"
"EX 81,57\nMaav 112,184,64\nRa 168,24,10240\nAD 84,84,80\nbuffer_load_dwordx4 a[212:215], v1, s[16:19], s5 offen offset:1024 sc1\nMaav 116,184,68\nD1 1,16,46,2048\nMaav 124,188,68\n"
"Ra 172,24,11264\nMaav 120,188,64\nAD 84,84,81\nCV 74,80,81\ns_sub_u32 s99, s99, 1\nCvav 32,128,192,4\nRa 176,26,8192\nMvav 32,132,200\nEX 82,58\nMvav 32,136,208\nRa 180,26,9216\n"
"Mvav 32,140,216\nWL 6\nEX 83,59\nCvav 36,140,220,8\nRa 184,26,10240\nAD 84,84,82\nbuffer_load_dwordx4 a[216:219], v1, s[16:19], s5 offen offset:2048 sc1\nMvav 36,136,212\n"
"D1 1,16,46,3072\ns_add_u32 m0, s89, 32768\nMvav 36,132,204\nRa 188,26,11264\nMvav 36,128,196\nAD 84,84,83\nCV 75,82,83\nMvav 36,144,228\nRa 128,2,16384\nMvav 36,148,236\n"
"s_add_u32 s46, s86, s88\nEX 80,60\nMvav 36,152,244\nRa 132,2,17408\nMvav 36,156,252\nWL 6\nEX 81,61\nMvav 32,156,248\nRa 136,2,18432\nAD 85,85,80\n"
"buffer_load_dwordx4 a[220:223], v1, s[16:19], s5 offen offset:3072 sc1\nMvav 32,152,240\nD1 1,16,46,0\nMvav 32,148,232\nRa 140,2,19456\nMvav 32,144,224\nAD 85,85,81\nCV 78,80,81\n"
"Cvav 40,160,192,12\nRa 144,2,20480\nMvav 40,164,200\ns_mov_b32 s87, s95\nEX 82,62\nMvav 40,168,208\nRa 148,2,21504\ns_add_u32 s5, s6, 16384\nMvav 40,172,216\nWL 6\nEX 83,63\n"
"Cvav 44,172,220,16\nRa 152,2,22528\nAD 85,85,82\nbuffer_load_dwordx4 a[224:227], v1, s[16:19], s5 offen offset:0 sc1\nMvav 44,168,212\nD1 1,16,46,1024\nMvav 44,164,204\n"
"Ra 156,2,23552\nMvav 44,160,196\nAD 85,85,83\nCV 79,82,83\nMvav 44,176,228\nRa 160,2,24576\nv_max_f32 v90, v84, v85\nMvav 44,180,236\ns_add_u32 s88, s88, 32768\nEX 80,32\n"
"Mvav 44,184,244\nRa 164,2,25600\nv_cmp_lt_f32 vcc, s63, v90\nMvav 44,188,252\nWL 8\nEX 81,33\nMvav 40,188,248\nRa 168,2,26624\ns_cbranch_vccnz .T78\n.T79:\n"
"buffer_load_dwordx4 a[228:231], v1, s[16:19], s5 offen offset:1024 sc1\nMvav 40,184,240\nD1 1,16,46,2048\nMvav 40,180,232\nRa 172,2,27648\nAD 86,86,84\nMvav 40,176,224\n"
"AD 84,80,81\nCV 64,80,81\nMaav 0,128,72\nRa 176,2,28672\nMaav 4,128,76\nEX 82,34\nMaav 12,132,76\nRa 180,2,29696\nMaav 8,132,72\ns_waitcnt vmcnt(22) lgkmcnt(8)\nEX 83,35\n"
"Maav 16,136,72\nRa 184,2,30720\nAD 84,84,82\nbuffer_load_dwordx4 a[232:235], v1, s[16:19], s5 offen offset:2048 sc1\nMaav 20,136,76\nD1 1,16,46,3072\ns_add_u32 m0, s89, 49152\n"
"Maav 28,140,76\nRa 188,2,31744\ns_barrier\nMaav 24,140,72\nAD 84,84,83\nCV 65,82,83\nMaav 32,144,72\nRa 128,24,32768\nMaav 36,144,76\ns_add_u32 s46, s87, s88\nEX 80,36\n"
"Maav 44,148,76\nRa 132,24,33792\nMaav 40,148,72\nWL 8\nEX 81,37\nMaav 48,152,72\nRa 136,24,34816\nAD 87,87,85\n"
"buffer_load_dwordx4 a[236:239], v1, s[16:19], s5 offen offset:3072 sc1\nMaav 52,152,76\nD1 1,16,46,0\nMaav 60,156,76\nRa 140,24,35840\nMaav 56,156,72\nAD 85,80,81\nCV 68,80,81\n"
"Maav 64,160,72\nRa 144,26,32768\nMaav 68,160,76\ns_cmp_eq_u32 s13, 7\nEX 82,38\nMaav 76,164,76\nRa 148,26,33792\ns_cbranch_scc1 .E76\n.E77:\ns_add_u32 s5, s6, 20480\n"
"Maav 72,164,72\nWL 8\nEX 83,39\nMaav 80,168,72\nRa 152,26,34816\nAD 85,85,82\nbuffer_load_dwordx4 a[240:243], v1, s[16:19], s5 offen offset:0 sc1\nMaav 84,168,76\nD1 1,16,46,1024\n"
"Maav 92,172,76\nRa 156,26,35840\nMaav 88,172,72\nAD 85,85,83\nCV 69,82,83\nMaav 96,176,72\nRa 160,24,40960\nMaav 100,176,76\ns_xor_b32 s89, s89, 0x10000\nEX 80,40\n"
"Maav 108,180,76\nRa 164,24,41984\nMaav 104,180,72\nWL 6\nEX 81,41\nMaav 112,184,72\nRa 168,24,43008\nAD 84,84,80\n"
"buffer_load_dwordx4 a[244:247], v1, s[16:19], s5 offen offset:1024 sc1\nMaav 116,184,76\nD1 1,16,46,2048\nMaav 124,188,76\nRa 172,24,44032\nMaav 120,188,72\nAD 84,84,81\n"
"CV 66,80,81\nCvav 48,128,192,4\nRa 176,26,40960\nMvav 48,132,200\nEX 82,42\nMvav 48,136,208\nRa 180,26,41984\nMvav 48,140,216\nWL 6\nEX 83,43\nCvav 52,140,220,8\nRa 184,26,43008\n"
"AD 84,84,82\nbuffer_load_dwordx4 a[248:251], v1, s[16:19], s5 offen offset:2048 sc1\nMvav 52,136,212\nD1 1,16,46,3072\ns_add_u32 m0, s89, 0\nMvav 52,132,204\nRa 188,26,44032\n"
"Mvav 52,128,196\nAD 84,84,83\nCV 67,82,83\nMvav 52,144,228\nRa 128,2,49152\nMvav 52,148,236\ns_add_u32 s46, s86, s88\nEX 80,44\nMvav 52,152,244\nRa 132,2,50176\n"
"v_xor_b32 v24, 0x10000, v24\nMvav 52,156,252\nWL 6\nEX 81,45\nMvav 48,156,248\nRa 136,2,51200\nAD 85,85,80\nbuffer_load_dwordx4 a[252:255], v1, s[16:19], s5 offen offset:3072 sc1\n"
"Mvav 48,152,240\nD1 1,16,46,0\nMvav 48,148,232\nRa 140,2,52224\nMvav 48,144,224\nAD 85,85,81\nCV 70,80,81\nCvav 56,160,192,12\nRa 144,2,53248\nMvav 56,164,200\nEX 82,46\n"
"Mvav 56,168,208\nRa 148,2,54272\nv_xor_b32 v26, 0x10000, v26\nMvav 56,172,216\nWL 6\nEX 83,47\nCvav 60,172,220,16\nRa 152,2,55296\nAD 85,85,82\nMvav 60,168,212\nD1 1,16,46,1024\n"
"Mvav 60,164,204\nRa 156,2,56320\nMvav 60,160,196\nAD 85,85,83\nCV 71,82,83\nMvav 60,176,228\nRa 160,2,57344\nv_max_f32 v90, v84, v85\nMvav 60,180,236\ns_add_u32 s88, s88, 32768\n"
"EX 80,48\nMvav 60,184,244\nRa 164,2,58368\nv_cmp_lt_f32 vcc, s63, v90\nMvav 60,188,252\nWL 8\nEX 81,49\nMvav 56,188,248\nRa 168,2,59392\ns_cbranch_vccnz .T80\n.T81:\n"
"Mvav 56,184,240\nD1 1,16,46,2048\nMvav 56,180,232\nRa 172,2,60416\nAD 86,86,84\nMvav 56,176,224\nAD 84,80,81\nCV 72,80,81\nMaav 0,128,64\nRa 176,2,61440\nMaav 4,128,68\nEX 82,50\n"
"Maav 12,132,68\nRa 180,2,62464\nMaav 8,132,64\ns_waitcnt vmcnt(20) lgkmcnt(8)\nEX 83,51\nMaav 16,136,64\nRa 184,2,63488\nAD 84,84,82\nMaav 20,136,68\nD1 1,16,46,3072\n"
"s_add_u32 m0, s89, 16384\nMaav 28,140,68\nRa 188,2,64512\ns_barrier\nMaav 24,140,64\nAD 84,84,83\nCV 73,82,83\nMaav 32,144,64\nRa 128,24,0\nMaav 36,144,68\n"
"s_add_u32 s46, s87, s88\nEX 80,52\nMaav 44,148,68\nRa 132,24,1024\nv_xor_b32 v2, 0x10000, v2\nMaav 40,148,64\nWL 8\nEX 81,53\nMaav 48,152,64\nRa 136,24,2048\nAD 87,87,85\n"
"Maav 52,152,68\nD1 1,16,46,0\nMaav 60,156,68\nRa 140,24,3072\nMaav 56,156,64\nAD 85,80,81\nCV 76,80,81\ns_cmp_lg_u32 s67, 0\ns_cbranch_scc1 .E83\n.E84:\nMaav 64,160,64\n"
"Ra 144,26,0\nMaav 68,160,68\nEX 82,54\nMaav 76,164,68\nRa 148,26,1024\nMaav 72,164,64\nWL 8\nEX 83,55\nMaav 80,168,64\nRa 152,26,2048\nAD 85,85,82\nMaav 84,168,68\n"
"D1 1,16,46,1024\nMaav 92,172,68\nRa 156,26,3072\nMaav 88,172,64\nAD 85,85,83\nCV 77,82,83\nMaav 96,176,64\nRa 160,24,8192\nMaav 100,176,68\nEX 80,56\nMaav 108,180,68\n"
"Ra 164,24,9216\nMaav 104,180,64\nWL 10\nEX 81,57\nMaav 112,184,64\nRa 168,24,10240\nAD 84,84,80\nMaav 116,184,68\nD1 1,16,46,2048\nMaav 124,188,68\nRa 172,24,11264\n"
"Maav 120,188,64\nAD 84,84,81\nCV 74,80,81\ns_cmp_lg_u32 s67, 0\ns_cbranch_scc1 .D85\n.D86:\nWV 7\nv_cmp_lt_f32 vcc, 0, v101\nSU 20,100,88\nv_mul_f32 v101, 0x3e800000, v101\n"
"v_cndmask_b32 v20, 0, v20, vcc\nv_cmp_lt_f32 vcc, 0, v105\nSU 21,104,89\nv_mul_f32 v105, 0x3e800000, v105\nv_cndmask_b32 v21, 0, v21, vcc\nv_or_b32 v91, v20, v21\n"
"v_cmp_ne_u32 vcc, 0, v91\ns_nop 0\ns_cbranch_vccnz .D87\nAD 86,86,101\nAD 87,87,105\n.D88:\nMava 0,130,192\nMava 8,128,192\nEX 82,58\nMava 12,128,196\nMava 4,130,196\nEX 83,59\n"
"Mava 16,130,200\nAD 84,84,82\nMava 24,128,200\nD1 1,16,46,3072\nMava 28,128,204\ns_add_u32 m0, s89, 32768\ns_add_u32 s46, s86, s88\nMava 20,130,204\nAD 84,84,83\nCV 75,82,83\n"
"Mava 32,130,208\nRa 128,2,16384\nv_xor_b32 v3, 0x10000, v2\nv_add_u32 v0, 0x20000, v1\nMava 40,128,208\nEX 80,60\nMava 44,128,212\nRa 132,2,17408\ns_sub_u32 s94, s49, 1835008\n"
"Mava 36,130,212\nEX 81,61\nMava 48,130,216\nRa 136,2,18432\nAD 85,85,80\ns_xor_b32 s92, s94, 0x202000\nMava 56,128,216\nD1 1,16,46,0\ns_lshl_b32 s89, s11, 12\nMava 60,128,220\n"
"Ra 140,2,19456\ns_lshl1_add_u32 s2, s12, 0\nMava 52,130,220\nAD 85,85,81\nCV 78,80,81\nMava 64,130,224\nRa 144,2,20480\ns_lshl3_add_u32 s2, s2, s13\n"
"s_lshl4_add_u32 s2, s2, 0xc048200\nMava 72,128,224\nEX 82,62\nMava 76,128,228\nRa 148,2,21504\ns_and_b32 s3, s72, 0x2000\nMava 68,130,228\nEX 83,63\nMava 80,130,232\n"
"Ra 152,2,22528\nAD 85,85,82\nMava 88,128,232\nD1 1,16,46,1024\ns_cmp_eq_u32 s11, 1\ns_cselect_b32 s54, s2, s54\ns_cselect_b32 s3, s3, 0\nMava 92,128,236\nRa 156,2,23552\n"
"v_xor_b32 v25, s3, v25\nMava 84,130,236\nAD 85,85,83\nCV 79,82,83\nMava 96,130,240\nRa 160,2,24576\nv_max_f32 v90, v84, v85\nMava 104,128,240\nv_cmp_lt_f32 vcc, s63, v90\n"
"Mava 108,128,244\nRa 164,2,25600\nMava 100,130,244\nWL 8\nMava 112,130,248\nRa 168,2,26624\nMava 120,128,248\nD1 1,16,46,2048\nMava 124,128,252\nRa 172,2,27648\nMava 116,130,252\n"
"s_cbranch_vccnz .T92\n.T93:\nAD 86,86,84\nAD 87,87,85\nMaav 0,128,72\nRa 176,2,28672\nv_mov_b32 v84, v86\nv_mov_b32 v85, v87\nMaav 4,128,76\nv_permlane32_swap_b32 v86, v84\n"
"v_permlane32_swap_b32 v87, v85\nMaav 12,132,76\nRa 180,2,29696\nAD 86,86,84\nMaav 8,132,72\nWL 8\nAD 87,87,85\nMaav 16,136,72\nRa 184,2,30720\nv_mov_b32 v84, v86\n"
"v_mov_b32 v85, v87\nMaav 20,136,76\nD1 1,16,46,3072\nv_permlane16_swap_b32 v86, v84\nMaav 28,140,76\nRa 188,2,31744\nv_permlane16_swap_b32 v87, v85\nMaav 24,140,72\nAD 86,86,84\n"
"AD 87,87,85\nMaav 32,144,72\nRa 128,2,0\nv_rcp_f32 v86, v86\nMaav 36,144,76\nv_rcp_f32 v87, v87\nMaav 44,148,76\nRa 132,2,1024\nAR 82,0\nAR 83,1\nMU 82,82,86\nMU 83,83,86\n"
"Maav 40,148,72\nWL 8\ns_add_u32 m0, s89, 131072\ns_add_u32 s46, s96, 1212416\nCV 96,82,83\nAR 82,2\nMaav 48,152,72\nRa 136,2,2048\nAR 83,3\nMU 82,82,86\nMU 83,83,86\nCV 97,82,83\n"
"Maav 52,152,76\nD1 1,16,46,0\nAR 82,4\nAR 83,5\nMU 82,82,87\nMaav 60,156,76\nRa 140,2,3072\nMU 83,83,87\nCV 100,82,83\nAR 82,6\nAR 83,7\nMaav 56,156,72\nMU 82,82,87\nMU 83,83,87\n"
"CV 101,82,83\nAR 82,8\nAR 83,9\nMaav 64,160,72\nRa 144,2,4096\nMU 82,82,86\nMU 83,83,86\nCV 98,82,83\nAR 82,10\nMaav 68,160,76\nAR 83,11\nMU 82,82,86\nMU 83,83,86\nCV 99,82,83\n"
"Maav 76,164,76\nRa 148,2,5120\nAR 82,12\nAR 83,13\nMU 82,82,87\nMU 83,83,87\nMaav 72,164,72\nWL 8\nCV 102,82,83\nAR 82,14\nAR 83,15\nMU 82,82,87\nMaav 80,168,72\nRa 152,2,6144\n"
"MU 83,83,87\nCV 103,82,83\nAR 82,16\nAR 83,17\nMaav 84,168,76\nD1 1,16,46,1024\nMU 82,82,86\nMU 83,83,86\nCV 104,82,83\nMaav 92,172,76\nRa 156,2,7168\nAR 82,18\nAR 83,19\n"
"MU 82,82,86\nMU 83,83,86\nMaav 88,172,72\nCV 105,82,83\nAR 82,20\nAR 83,21\nMU 82,82,87\nMU 83,83,87\nMaav 96,176,72\nRa 160,2,8192\nCV 108,82,83\nAR 82,22\nAR 83,23\nMU 82,82,87\n"
"Maav 100,176,76\nMU 83,83,87\nCV 109,82,83\nAR 82,24\nAR 83,25\nMaav 108,180,76\nRa 164,2,9216\nMU 82,82,86\nMU 83,83,86\nCV 106,82,83\nAR 82,26\nMaav 104,180,72\nWL 8\nAR 83,27\n"
"MU 82,82,86\nMU 83,83,86\nCV 107,82,83\nMaav 112,184,72\nRa 168,2,10240\nAR 82,28\nAR 83,29\nMU 82,82,87\nMU 83,83,87\nMaav 116,184,76\nD1 1,16,46,2048\nCV 110,82,83\nAR 82,30\n"
"AR 83,31\nMaav 124,188,76\nRa 172,2,11264\nMU 82,82,87\nMU 83,83,87\nCV 111,82,83\nMaav 120,188,72\nAR 82,32\nAR 83,33\nMU 82,82,86\nZvav 128,128,96\nRa 176,2,12288\nMU 83,83,86\n"
"CV 112,82,83\nAR 82,34\nMvav 128,132,104\nAR 83,35\nMU 82,82,86\nMU 83,83,86\nZvav 132,132,108\nRa 180,2,13312\nCV 113,82,83\nAR 82,36\nMvav 132,128,100\nWL 8\nAR 83,37\n"
"MU 82,82,87\nMU 83,83,87\nZvav 140,136,100\nRa 184,2,14336\nCV 116,82,83\nAR 82,38\nMvav 140,140,108\nD1 1,16,46,3072\nAR 83,39\nMU 82,82,87\nZvav 136,140,104\nRa 188,2,15360\n"
"s_barrier\nMU 83,83,87\nCV 117,82,83\nMvav 136,136,96\nAR 82,40\nAR 83,41\nMU 82,82,86\nZvav 144,144,96\nRa 128,2,32768\nMU 83,83,86\nCV 114,82,83\nAR 82,42\nMvav 144,148,104\n"
"AR 83,43\nMU 82,82,86\nMU 83,83,86\nbuffer_store_dword v25, off, s[16:19], s54 offset:512 sc0 sc1\nZvav 148,148,108\nRa 132,2,33792\nCV 115,82,83\nAR 82,44\nMvav 148,144,100\n"
"WL 8\ns_add_u32 m0, s89, 114688\ns_add_u32 s46, s96, 1179648\nAR 83,45\nZvav 156,152,100\nRa 136,2,34816\nMU 82,82,87\nMU 83,83,87\nMvav 156,156,108\nD1 1,16,46,0\nCV 118,82,83\n"
"AR 82,46\nZvav 152,156,104\nRa 140,2,35840\nAR 83,47\nMU 82,82,87\nMU 83,83,87\nMvav 152,152,96\nCV 119,82,83\nAR 82,48\nAR 83,49\nZvav 64,160,96\nRa 144,2,36864\nMU 82,82,86\n"
"MU 83,83,86\nCV 120,82,83\nMvav 64,164,104\nAR 82,50\nAR 83,51\nMU 82,82,86\nZvav 68,164,108\nRa 148,2,37888\nMU 83,83,86\nCV 121,82,83\nMvav 68,160,100\nWL 8\nAR 82,52\nAR 83,53\n"
"MU 82,82,87\nZvav 76,168,100\nRa 152,2,38912\nMU 83,83,87\nCV 124,82,83\nMvav 76,172,108\nD1 1,16,46,1024\nAR 82,54\nAR 83,55\nZvav 72,172,104\nRa 156,2,39936\nMU 82,82,87\n"
"MU 83,83,87\nCV 125,82,83\nMvav 72,168,96\nAR 82,56\nAR 83,57\nMU 82,82,86\nZvav 4,176,96\nRa 160,2,40960\nMU 83,83,86\nCV 122,82,83\nAR 82,58\nMvav 4,180,104\nAR 83,59\n"
"MU 82,82,86\nMU 83,83,86\nZvav 8,180,108\nRa 164,2,41984\nCV 123,82,83\nAR 82,60\nMvav 8,176,100\nWL 8\nAR 83,61\nMU 82,82,87\nMU 83,83,87\nZvav 16,184,100\nRa 168,2,43008\n"
"CV 126,82,83\nAR 82,62\nMvav 16,188,108\nD1 1,16,46,2048\nAR 83,63\nMU 82,82,87\nZvav 12,188,104\nRa 172,2,44032\nMU 83,83,87\nCV 127,82,83\nMvav 12,184,96\nAR 82,64\nAR 83,65\n"
"MU 82,82,86\nMvav 128,128,112\nRa 176,2,45056\nMU 83,83,86\nCV 32,82,83\nAR 82,66\nMvav 128,132,120\nAR 83,67\nMU 82,82,86\nMU 83,83,86\nMvav 132,132,124\nRa 180,2,46080\n"
"CV 33,82,83\nAR 82,68\nMvav 132,128,116\nWL 8\nAR 83,69\nMU 82,82,87\nMvav 140,136,116\nRa 184,2,47104\nMU 83,83,87\nCV 36,82,83\nAR 82,70\nMvav 140,140,124\nD1 1,16,46,3072\n"
"AR 83,71\nMU 82,82,87\nMvav 136,140,120\nRa 188,2,48128\nMU 83,83,87\nCV 37,82,83\nMvav 136,136,112\nAR 82,72\nAR 83,73\nMU 82,82,86\nMU 83,83,86\nMvav 144,144,112\n"
"Ra 128,2,49152\nCV 34,82,83\nAR 82,74\nMvav 144,148,120\nAR 83,75\nMU 82,82,86\nMU 83,83,86\nMvav 148,148,124\nRa 132,2,50176\nCV 35,82,83\nAR 82,76\nAR 83,77\nMvav 148,144,116\n"
"WL 8\ns_add_u32 m0, s89, 147456\ns_add_u32 s46, s96, 1245184\nMvav 156,152,116\nRa 136,2,51200\nMU 82,82,87\nMU 83,83,87\nMvav 156,156,124\nD1 1,16,46,0\nCV 38,82,83\nAR 82,78\n"
"AR 83,79\nMvav 152,156,120\nRa 140,2,52224\nMU 82,82,87\nMU 83,83,87\nMvav 152,152,112\nCV 39,82,83\nAR 82,80\nAR 83,81\nMvav 64,160,112\nRa 144,2,53248\nMU 82,82,86\nMU 83,83,86\n"
"CV 40,82,83\nMvav 64,164,120\nAR 82,82\nAR 83,83\nMU 82,82,86\nMvav 68,164,124\nRa 148,2,54272\nMU 83,83,86\nCV 41,82,83\nMvav 68,160,116\nWL 8\nAR 82,84\nAR 83,85\n"
"Mvav 76,168,116\nRa 152,2,55296\nMU 82,82,87\nMU 83,83,87\nCV 44,82,83\nMvav 76,172,124\nD1 1,16,46,1024\nAR 82,86\nAR 83,87\nMvav 72,172,120\nRa 156,2,56320\nMU 82,82,87\n"
"MU 83,83,87\nMvav 72,168,112\nCV 45,82,83\nAR 82,88\nAR 83,89\nMU 82,82,86\nMvav 4,176,112\nRa 160,2,57344\nMU 83,83,86\nCV 42,82,83\nMvav 4,180,120\nAR 82,90\nAR 83,91\n"
"MU 82,82,86\nMvav 8,180,124\nRa 164,2,58368\nMU 83,83,86\nCV 43,82,83\nAR 82,92\nMvav 8,176,116\nWL 8\nAR 83,93\nMU 82,82,87\nMvav 16,184,116\nRa 168,2,59392\nMU 83,83,87\n"
"CV 46,82,83\nMvav 16,188,124\nD1 1,16,46,2048\nAR 82,94\nAR 83,95\nMU 82,82,87\nMvav 12,188,120\nRa 172,2,60416\nMU 83,83,87\nCV 47,82,83\nMvav 12,184,112\nAR 82,96\nAR 83,97\n"
"MU 82,82,86\nMvav 128,128,32\nRa 176,2,61440\nMU 83,83,86\nCV 48,82,83\nAR 82,98\nMvav 128,132,40\nAR 83,99\nMU 82,82,86\nMU 83,83,86\nMvav 132,132,44\nRa 180,2,62464\n"
"CV 49,82,83\nAR 82,100\nMvav 132,128,36\ns_waitcnt vmcnt(16) lgkmcnt(8)\nAR 83,101\nMU 82,82,87\nMU 83,83,87\nMvav 140,136,36\nRa 184,2,63488\nCV 52,82,83\nAR 82,102\n"
"Mvav 140,140,44\nD1 1,16,46,3072\nAR 83,103\nMU 82,82,87\nMvav 136,140,40\nRa 188,2,64512\ns_barrier\nMU 83,83,87\nCV 53,82,83\nMvav 136,136,32\nAR 82,104\nAR 83,105\nMU 82,82,86\n"
"Mvav 144,144,32\nRa 128,3,0\nMU 83,83,86\nCV 50,82,83\nAR 82,106\nMvav 144,148,40\nAR 83,107\nMU 82,82,86\nMU 83,83,86\nMvav 148,148,44\nRa 132,3,1024\nCV 51,82,83\nAR 82,108\n"
"Mvav 148,144,36\nWL 8\ns_add_u32 m0, s89, 16384\ns_add_u32 s46, s96, 1277952\nAR 83,109\nMvav 156,152,36\nRa 136,3,2048\nMU 82,82,87\nMU 83,83,87\nMvav 156,156,44\nD1 1,16,46,0\n"
"CV 54,82,83\nAR 82,110\nMvav 152,156,40\nRa 140,3,3072\nAR 83,111\nMU 82,82,87\nMU 83,83,87\nMvav 152,152,32\nCV 55,82,83\nAR 82,112\nAR 83,113\nMvav 64,160,32\nRa 144,3,4096\n"
"MU 82,82,86\nMU 83,83,86\nCV 56,82,83\nMvav 64,164,40\nAR 82,114\nAR 83,115\nMU 82,82,86\nMvav 68,164,44\nRa 148,3,5120\nMU 83,83,86\nCV 57,82,83\nMvav 68,160,36\nWL 8\nAR 82,116\n"
"AR 83,117\nMU 82,82,87\nMvav 76,168,36\nRa 152,3,6144\nMU 83,83,87\nCV 60,82,83\nMvav 76,172,44\nD1 1,16,46,1024\nAR 82,118\nAR 83,119\nMvav 72,172,40\nRa 156,3,7168\nMU 82,82,87\n"
"MU 83,83,87\nCV 61,82,83\nMvav 72,168,32\nAR 82,120\nAR 83,121\nMU 82,82,86\nMvav 4,176,32\nRa 160,3,8192\nMU 83,83,86\nCV 58,82,83\nAR 82,122\nMvav 4,180,40\nAR 83,123\n"
"MU 82,82,86\nMU 83,83,86\nMvav 8,180,44\nRa 164,3,9216\nCV 59,82,83\nAR 82,124\nMvav 8,176,36\nWL 8\nAR 83,125\nMU 82,82,87\nMU 83,83,87\nMvav 16,184,36\nRa 168,3,10240\n"
"CV 62,82,83\nAR 82,126\nMvav 16,188,44\nD1 1,16,46,2048\nAR 83,127\nMU 82,82,87\nMvav 12,188,40\nRa 172,3,11264\nMU 83,83,87\nCV 63,82,83\nMvav 12,184,32\nMvav 128,128,48\n"
"Ra 176,3,12288\nMvav 128,132,56\nMvav 132,132,60\nRa 180,3,13312\nMvav 132,128,52\nWL 8\nMvav 140,136,52\nRa 184,3,14336\nMvav 140,140,60\nD1 1,16,46,3072\nMvav 136,140,56\n"
"Ra 188,3,15360\nMvav 136,136,48\nMvav 144,144,48\nRa 128,3,16384\nMvav 144,148,56\nbuffer_load_dword v27, off, s[16:19], s60 offset:512 sc0 sc1\nMvav 148,148,60\nRa 132,3,17408\n"
"CV 132,132,133\nMvav 148,144,52\nWL 8\ns_add_u32 m0, s89, 0\ns_add_u32 s46, s96, 1310720\nMvav 156,152,52\nRa 136,3,18432\nCV 133,134,135\nMvav 156,156,60\nD1 1,16,46,0\n"
"CV 134,140,141\nMvav 152,156,56\nRa 140,3,19456\nCV 135,142,143\nMvav 152,152,48\ns_add_u32 s5, s94, 0\nSv 132,1,16,5,1024\nMvav 64,160,48\nRa 144,3,20480\nMvav 64,164,56\n"
"Mvav 68,164,60\nRa 148,3,21504\nMvav 68,160,52\nWL 8\nCV 128,128,129\nMvav 76,168,52\nRa 152,3,22528\nCV 129,130,131\nMvav 76,172,60\nD1 1,16,46,1024\nCV 130,136,137\n"
"Mvav 72,172,56\nRa 156,3,23552\nCV 131,138,139\nMvav 72,168,48\nSv 128,1,16,5,0\nMvav 4,176,48\nRa 160,3,24576\nMvav 4,180,56\nMvav 8,180,60\nRa 164,3,25600\nMvav 8,176,52\nWL 6\n"
"CV 148,148,149\nMvav 16,184,52\nRa 168,3,26624\nCV 149,150,151\nMvav 16,188,60\nD1 1,16,46,2048\nCV 150,156,157\nMvav 12,188,56\nRa 172,3,27648\nCV 151,158,159\nMvav 12,184,48\n"
"s_add_u32 s5, s94, 131072\nSv 148,1,16,5,1024\nZvav 176,128,96\nRa 176,3,28672\nMvav 176,132,104\nMvav 176,136,112\nRa 180,3,29696\nMvav 176,140,120\n"
"s_waitcnt vmcnt(15) lgkmcnt(6)\nCV 144,144,145\nZvav 180,140,124\nRa 184,3,30720\nCV 145,146,147\nMvav 180,136,116\nD1 1,16,46,3072\nCV 146,152,153\nMvav 180,132,108\n"
"Ra 188,3,31744\ns_barrier\nMvav 180,128,100\nCV 147,154,155\nSv 144,1,16,5,0\nMvav 180,144,36\nRa 128,3,32768\nMvav 180,148,44\nMvav 180,152,52\nRa 132,3,33792\nMvav 180,156,60\n"
"WL 6\ns_add_u32 m0, s89, 32768\ns_add_u32 s46, s96, 1343488\nMvav 176,156,56\nRa 136,3,34816\nCV 68,68,69\nMvav 176,152,48\nD1 1,16,46,0\nCV 69,70,71\nMvav 176,148,40\n"
"Ra 140,3,35840\nCV 70,76,77\nMvav 176,144,32\nCV 71,78,79\ns_add_u32 s5, s94, 262144\nZvav 184,160,96\nRa 144,3,36864\nSv 68,1,16,5,1024\nMvav 184,164,104\nMvav 184,168,112\n"
"Ra 148,3,37888\nMvav 184,172,120\nWL 6\nZvav 188,172,124\nRa 152,3,38912\nCV 64,64,65\nMvav 188,168,116\nD1 1,16,46,1024\nCV 65,66,67\nMvav 188,164,108\nRa 156,3,39936\n"
"CV 66,72,73\nMvav 188,160,100\nCV 67,74,75\nSv 64,1,16,5,0\nMvav 188,176,36\nRa 160,3,40960\nMvav 188,180,44\nMvav 188,184,52\nRa 164,3,41984\nMvav 188,188,60\nWL 6\nCV 8,8,9\n"
"Mvav 184,188,56\nRa 168,3,43008\nCV 9,10,11\nMvav 184,184,48\nD1 1,16,46,2048\nCV 10,16,17\nMvav 184,180,40\nRa 172,3,44032\nCV 11,18,19\nMvav 184,176,32\n"
"s_add_u32 s5, s94, 393216\nSv 8,1,16,5,1024\nZvav 192,128,96\nRa 176,3,45056\nv_lshrrev_b32 v88, 8, v1\nMvav 192,132,104\nv_lshlrev_b32 v89, 5, v88\nMvav 192,136,112\n"
"Ra 180,3,46080\nMvav 192,140,120\nWL 6\nCV 4,4,5\nZvav 196,140,124\nRa 184,3,47104\nv_bfe_u32 v90, v1, 4, 4\nMvav 196,136,116\nD1 1,16,46,3072\nCV 5,6,7\nMvav 196,132,108\n"
"Ra 188,3,48128\nCV 6,12,13\nMvav 196,128,100\nv_mul_u32_u24 v91, 896, v90\nCV 7,14,15\nMvav 196,144,36\nRa 128,3,49152\nv_lshl_add_u32 v91, v88, 4, v91\nWV 15\n"
"v_cmp_ne_u32 vcc, s15, v27\ns_nop 0\ns_cbranch_vccnz .S119\n.S120:\nMvav 196,148,44\ns_add_u32 s4, s92, 0\nSv 4,1,16,5,0\nMvav 196,152,52\nRa 132,3,50176\nMvav 196,156,60\nWL 6\n"
"s_add_u32 m0, s89, 49152\ns_add_u32 s46, s96, 1376256\nbuffer_load_dwordx4 v[208:211], v1, s[16:19], s4 offen offset:0 sc0 sc1\nMvav 192,156,56\nRa 136,3,51200\n"
"v_add_u32 v90, s21, v90\nMvav 192,152,48\nD1 1,16,46,0\nCV 180,180,181\nMvav 192,148,40\nRa 140,3,52224\nCV 181,182,183\nMvav 192,144,32\n"
"buffer_load_dwordx4 v[212:215], v1, s[16:19], s4 offen offset:1024 sc0 sc1\nCV 182,188,189\nZvav 200,160,96\nRa 144,3,53248\nv_lshlrev_b32 v88, 3, v90\nMvav 200,164,104\n"
"v_add_u32 v94, 64, v90\nCV 183,190,191\nMvav 200,168,112\nRa 148,3,54272\ns_add_u32 s5, s94, 524288\nMvav 200,172,120\nWL 6\nSv 180,1,16,5,1024\nZvav 204,172,124\nRa 152,3,55296\n"
"s_mov_b32 s63, 0xc040000\nMvav 204,168,116\nD1 1,16,46,1024\nMvav 204,164,108\nRa 156,3,56320\nMvav 204,160,100\nv_lshrrev_b32 v92, 8, v1\nCV 176,176,177\nMvav 204,176,36\n"
"Ra 160,3,57344\nv_bfe_u32 v93, v1, 6, 2\nMvav 204,180,44\nv_cmp_eq_u32 vcc, v92, v93\nv_and_b32 v92, 16, v1\nv_mov_b32 v93, 0x3f80\nv_cndmask_b32 v93, 0, v93, vcc\n"
"Mvav 204,184,52\nRa 164,3,58368\nCV 177,178,179\nMvav 204,188,60\nWL 6\nCV 178,184,185\nMvav 200,188,56\nRa 168,3,59392\nv_lshlrev_b32 v93, v92, v93\nMvav 200,184,48\n"
"D1 1,16,46,2048\nCV 179,186,187\nMvav 200,180,40\nRa 172,3,60416\nSv 176,1,16,5,0\nMvav 200,176,32\nv_and_b32 v92, 32, v1\nv_cmp_eq_u32 vcc, 0, v92\nv_mov_b32 v4, 0\n"
"v_mov_b32 v5, 0\nv_cndmask_b32 v6, 0, v93, vcc\nZvav 160,128,96\nRa 176,3,61440\nv_sub_u32 v7, v93, v6\nMvav 160,132,104\nv_mov_b32 v8, 0\nMvav 160,136,112\nRa 180,3,62464\n"
"CV 196,196,197\nMvav 160,140,120\nWL 6\nCV 197,198,199\nZvav 164,140,124\nRa 184,3,63488\nv_mov_b32 v9, 0\nMvav 164,136,116\nD1 1,16,46,3072\nCV 198,204,205\nMvav 164,132,108\n"
"Ra 188,3,64512\ns_barrier\nMvav 164,128,100\nCV 199,206,207\ns_add_u32 s5, s94, 655360\nMvav 164,144,36\nRa 128,0,0\nSv 196,1,16,5,1024\nMvav 164,148,44\n"
"s_add_u32 s4, s92, 131072\nMvav 164,152,52\nRa 132,0,1024\nMvav 164,156,60\nWL 6\ns_add_u32 m0, s89, 65536\ns_add_u32 s46, s96, 1409024\n"
"buffer_load_dwordx4 v[216:219], v1, s[16:19], s4 offen offset:0 sc0 sc1\nMvav 160,156,56\nRa 136,0,2048\nCV 192,192,193\nMvav 160,152,48\nD1 1,16,46,0\nCV 193,194,195\n"
"Mvav 160,148,40\nRa 140,0,3072\nCV 194,200,201\nMvav 160,144,32\nbuffer_load_dwordx4 v[220:223], v1, s[16:19], s4 offen offset:1024 sc0 sc1\nCV 195,202,203\nZvav 168,160,96\n"
"Ra 144,0,4096\nWV 9\nv_max_u32 v21, v211, v215\nv_cmp_eq_u32 vcc, -1, v21\ns_nop 0\ns_cbranch_vccnz .S95\n.S96:\nMvav 168,164,104\nv_lshlrev_b32 v176, 16, v208\nSv 192,1,16,5,0\n"
"Mvav 168,168,112\nRa 148,0,5120\nMvav 168,172,120\nWL 6\nv_and_b32 v177, 0xffff0000, v208\nZvav 172,172,124\nRa 152,0,6144\nMvav 172,168,116\nD1 1,16,46,1024\nMvav 172,164,108\n"
"Ra 156,0,7168\nv_lshlrev_b32 v178, 16, v209\nMvav 172,160,100\nv_and_b32 v179, 0xffff0000, v209\nMvav 172,176,36\nRa 160,0,8192\nv_lshlrev_b32 v180, 16, v212\nMvav 172,180,44\n"
"v_and_b32 v181, 0xffff0000, v212\nMvav 172,184,52\nRa 164,0,9216\nMvav 172,188,60\nWL 6\nv_lshlrev_b32 v182, 16, v213\nMvav 168,188,56\nRa 168,0,10240\nMvav 168,184,48\n"
"D1 1,16,46,2048\nMvav 168,180,40\nRa 172,0,11264\nv_and_b32 v183, 0xffff0000, v213\nMvav 168,176,32\nv_lshlrev_b32 v184, 16, v210\nMvav 176,128,96\nRa 176,0,12288\n"
"v_and_b32 v185, 0xffff0000, v210\nMvav 176,132,104\nv_lshlrev_b32 v186, 16, v211\nCV 164,164,165\nMvav 176,136,112\nRa 180,0,13312\nCV 165,166,167\nMvav 176,140,120\nWL 6\n"
"CV 166,172,173\nMvav 180,140,124\nRa 184,0,14336\nCV 167,174,175\nMvav 180,136,116\nD1 1,16,46,3072\ns_add_u32 s5, s94, 786432\nMvav 180,132,108\nRa 188,0,15360\n"
"v_and_b32 v187, 0xffff0000, v211\nMvav 180,128,100\nv_lshlrev_b32 v188, 16, v214\nSv 164,1,16,5,1024\nMvav 180,144,36\nRa 128,0,16384\nMvav 180,148,44\n"
"v_and_b32 v189, 0xffff0000, v214\ns_add_u32 s4, s92, 262144\nMvav 180,152,52\nRa 132,0,17408\nMvav 180,156,60\nWL 6\nv_lshlrev_b32 v190, 16, v215\n"
"buffer_load_dwordx4 v[224:227], v1, s[16:19], s4 offen offset:0 sc0 sc1\nMvav 176,156,56\nRa 136,0,18432\nCV 160,160,161\nMvav 176,152,48\nv_and_b32 v191, 0xffff0000, v215\n"
"CV 161,162,163\nMvav 176,148,40\nRa 140,0,19456\nCV 162,168,169\nMvav 176,144,32\nbuffer_load_dwordx4 v[228:231], v1, s[16:19], s4 offen offset:1024 sc0 sc1\nCV 163,170,171\n"
"Mvav 184,160,96\nRa 144,0,20480\nWV 7\nv_max_u32 v21, v219, v223\nv_cmp_eq_u32 vcc, -1, v21\ns_nop 0\ns_cbranch_vccnz .S98\n.S99:\nMvav 184,164,104\nv_lshlrev_b32 v192, 16, v216\n"
"Sv 160,1,16,5,0\nMvav 184,168,112\nRa 148,0,21504\nMvav 184,172,120\nWL 6\nMvav 188,172,124\nRa 152,0,22528\nv_and_b32 v193, 0xffff0000, v216\nMvav 188,168,116\n"
"v_lshlrev_b32 v194, 16, v217\nMvav 188,164,108\nRa 156,0,23552\nMvav 188,160,100\nv_and_b32 v195, 0xffff0000, v217\nMvav 188,176,36\nRa 160,0,24576\nv_lshlrev_b32 v196, 16, v220\n"
"Mvav 188,180,44\nv_and_b32 v197, 0xffff0000, v220\nMvav 188,184,52\nRa 164,0,25600\nMvav 188,188,60\nWL 6\nMvav 184,188,56\nRa 168,0,26624\nv_lshlrev_b32 v198, 16, v221\n"
"Mvav 184,184,48\nv_and_b32 v199, 0xffff0000, v221\nMvav 184,180,40\nRa 172,0,27648\nMvav 184,176,32\nv_lshlrev_b32 v200, 16, v218\nMvav 192,128,96\nRa 176,0,28672\n"
"v_and_b32 v201, 0xffff0000, v218\nMvav 192,132,104\nv_lshlrev_b32 v202, 16, v219\nCV 180,180,181\nMvav 192,136,112\nRa 180,0,29696\nCV 181,182,183\nMvav 192,140,120\nWL 6\n"
"CV 182,188,189\nMvav 196,140,124\nRa 184,0,30720\nCV 183,190,191\nMvav 196,136,116\nv_and_b32 v203, 0xffff0000, v219\ns_add_u32 s5, s94, 917504\nMvav 196,132,108\nRa 188,0,31744\n"
"s_barrier\nMvav 196,128,100\nv_lshlrev_b32 v204, 16, v222\nSv 180,1,16,5,1024\nMvav 196,144,36\nRa 128,2,16384\nv_and_b32 v205, 0xffff0000, v222\nMvav 196,148,44\n"
"v_lshlrev_b32 v206, 16, v223\ns_add_u32 s4, s92, 393216\nMvav 196,152,52\nRa 132,2,17408\nMvav 196,156,60\nWL 6\n"
"buffer_load_dwordx4 v[208:211], v1, s[16:19], s4 offen offset:0 sc0 sc1\nMvav 192,156,56\nRa 136,2,18432\nCV 176,176,177\nMvav 192,152,48\nv_and_b32 v207, 0xffff0000, v223\n"
"CV 177,178,179\nMvav 192,148,40\nRa 140,2,19456\nCV 178,184,185\nMvav 192,144,32\nbuffer_load_dwordx4 v[212:215], v1, s[16:19], s4 offen offset:1024 sc0 sc1\nCV 179,186,187\n"
"Mvav 200,160,96\nRa 144,2,20480\nWV 4\nv_max_u32 v21, v227, v231\nv_cmp_eq_u32 vcc, -1, v21\ns_nop 0\ns_cbranch_vccnz .S101\n.S102:\nMvav 200,164,104\n"
"v_lshlrev_b32 v160, 16, v224\nSv 176,1,16,5,0\nMvav 200,168,112\nRa 148,2,21504\nMvav 200,172,120\nWL 6\nMvav 204,172,124\nRa 152,2,22528\nv_and_b32 v161, 0xffff0000, v224\n"
"Mvav 204,168,116\nv_lshlrev_b32 v162, 16, v225\nMvav 204,164,108\nRa 156,2,23552\nMvav 204,160,100\nv_and_b32 v163, 0xffff0000, v225\nMvav 204,176,36\nRa 160,2,24576\n"
"v_lshlrev_b32 v164, 16, v228\nMvav 204,180,44\nv_and_b32 v165, 0xffff0000, v228\nMvav 204,184,52\nRa 164,2,25600\nMvav 204,188,60\nWL 6\nMvav 200,188,56\nRa 168,2,26624\n"
"v_lshlrev_b32 v166, 16, v229\nMvav 200,184,48\nv_and_b32 v167, 0xffff0000, v229\nMvav 200,180,40\nRa 172,2,27648\nMvav 200,176,32\nv_lshlrev_b32 v168, 16, v226\nMvav 160,128,96\n"
"Ra 176,2,28672\nv_and_b32 v169, 0xffff0000, v226\nMvav 160,132,104\nv_lshlrev_b32 v170, 16, v227\nCV 196,196,197\nMvav 160,136,112\nRa 180,2,29696\nCV 197,198,199\n"
"Mvav 160,140,120\nWL 6\nCV 198,204,205\nMvav 164,140,124\nRa 184,2,30720\nv_and_b32 v24, 0x70, v1\nMvav 164,136,116\nv_and_b32 v171, 0xffff0000, v227\nCV 199,206,207\n"
"Mvav 164,132,108\nRa 188,2,31744\ns_add_u32 s5, s94, 1048576\nMvav 164,128,100\nv_lshlrev_b32 v172, 16, v230\nv_lshlrev_b32 v24, 5, v24\nMvav 164,144,36\nRa 128,2,0\n"
"v_and_b32 v173, 0xffff0000, v230\nMvav 164,148,44\nv_lshlrev_b32 v174, 16, v231\ns_add_u32 s4, s92, 524288\nMvav 164,152,52\nRa 132,2,1024\n"
"buffer_load_dword v27, v24, s[16:19], s41 offen sc0 sc1\nMvav 164,156,60\nWL 6\nbuffer_load_dwordx4 v[216:219], v1, s[16:19], s4 offen offset:0 sc0 sc1\nMvav 160,156,56\n"
"Ra 136,2,2048\nSv 196,1,16,5,1024\nMvav 160,152,48\nv_and_b32 v175, 0xffff0000, v231\nMvav 160,148,40\nRa 140,2,3072\nMvav 160,144,32\n"
"buffer_load_dwordx4 v[220:223], v1, s[16:19], s4 offen offset:1024 sc0 sc1\nMvav 168,160,96\nRa 144,2,4096\nWV 5\nv_max_u32 v21, v211, v215\nv_cmp_eq_u32 vcc, -1, v21\ns_nop 0\n"
"s_cbranch_vccnz .S104\n.S105:\nMvav 168,164,104\nv_lshlrev_b32 v176, 16, v208\nCV 192,192,193\nMvav 168,168,112\nRa 148,2,5120\nCV 193,194,195\nMvav 168,172,120\nWL 6\n"
"CV 194,200,201\nMvav 172,172,124\nRa 152,2,6144\nv_and_b32 v177, 0xffff0000, v208\nMvav 172,168,116\nv_lshlrev_b32 v178, 16, v209\nCV 195,202,203\nMvav 172,164,108\nRa 156,2,7168\n"
"Sv 192,1,16,5,0\nMvav 172,160,100\nv_and_b32 v179, 0xffff0000, v209\nMvav 172,176,36\nRa 160,2,8192\nv_lshlrev_b32 v180, 16, v212\nMvav 172,180,44\n"
"v_and_b32 v181, 0xffff0000, v212\nMvav 172,184,52\nRa 164,2,9216\nMvav 172,188,60\nWL 6\nMvav 168,188,56\nRa 168,2,10240\nv_lshlrev_b32 v182, 16, v213\nMvav 168,184,48\n"
"v_and_b32 v183, 0xffff0000, v213\nMvav 168,180,40\nRa 172,2,11264\nMvav 168,176,32\nv_lshlrev_b32 v184, 16, v210\nMvav 176,128,96\nRa 176,2,12288\n"
"v_and_b32 v185, 0xffff0000, v210\nMvav 176,132,104\nv_lshlrev_b32 v186, 16, v211\nCV 164,164,165\nMvav 176,136,112\nRa 180,2,13312\nCV 165,166,167\nMvav 176,140,120\nWL 6\n"
"CV 166,172,173\nMvav 180,140,124\nRa 184,2,14336\nCV 167,174,175\nMvav 180,136,116\nv_and_b32 v187, 0xffff0000, v211\ns_add_u32 s5, s94, 1179648\nMvav 180,132,108\nRa 188,2,15360\n"
"s_barrier\nMvav 180,128,100\nv_lshlrev_b32 v188, 16, v214\nSv 164,1,16,5,1024\nMvav 180,144,36\nRa 128,2,32768\nv_and_b32 v189, 0xffff0000, v214\nMvav 180,148,44\n"
"v_lshlrev_b32 v190, 16, v215\ns_add_u32 s4, s92, 655360\nMvav 180,152,52\nRa 132,2,33792\nMvav 180,156,60\nWL 6\n"
"buffer_load_dwordx4 v[224:227], v1, s[16:19], s4 offen offset:0 sc0 sc1\nMvav 176,156,56\nRa 136,2,34816\nCV 160,160,161\nMvav 176,152,48\nv_and_b32 v191, 0xffff0000, v215\n"
"CV 161,162,163\nMvav 176,148,40\nRa 140,2,35840\nCV 162,168,169\nMvav 176,144,32\nbuffer_load_dwordx4 v[228:231], v1, s[16:19], s4 offen offset:1024 sc0 sc1\nCV 163,170,171\n"
"Mvav 184,160,96\nRa 144,2,36864\nWV 4\nv_max_u32 v21, v219, v223\nv_cmp_eq_u32 vcc, -1, v21\ns_nop 0\ns_cbranch_vccnz .S107\n.S108:\nMvav 184,164,104\n"
"v_lshlrev_b32 v192, 16, v216\nSv 160,1,16,5,0\nMvav 184,168,112\nRa 148,2,37888\nMvav 184,172,120\nWL 6\nMvav 188,172,124\nRa 152,2,38912\nv_and_b32 v193, 0xffff0000, v216\n"
"Mvav 188,168,116\nv_lshlrev_b32 v194, 16, v217\nMvav 188,164,108\nRa 156,2,39936\nMvav 188,160,100\nv_and_b32 v195, 0xffff0000, v217\nMvav 188,176,36\nRa 160,2,40960\n"
"v_lshlrev_b32 v196, 16, v220\nMvav 188,180,44\nv_and_b32 v197, 0xffff0000, v220\nMvav 188,184,52\nRa 164,2,41984\nMvav 188,188,60\nWL 6\nMvav 184,188,56\nRa 168,2,43008\n"
"v_lshlrev_b32 v198, 16, v221\nMvav 184,184,48\nv_and_b32 v199, 0xffff0000, v221\nMvav 184,180,40\nRa 172,2,44032\nMvav 184,176,32\nv_lshlrev_b32 v200, 16, v218\nMvav 192,128,96\n"
"Ra 176,2,45056\nv_and_b32 v201, 0xffff0000, v218\nMvav 192,132,104\nv_lshlrev_b32 v202, 16, v219\nCV 180,180,181\nMvav 192,136,112\nRa 180,2,46080\nCV 181,182,183\n"
"Mvav 192,140,120\nWL 6\nCV 182,188,189\nMvav 196,140,124\nRa 184,2,47104\nCV 183,190,191\nMvav 196,136,116\nv_and_b32 v203, 0xffff0000, v219\ns_add_u32 s5, s94, 1310720\n"
"Mvav 196,132,108\nRa 188,2,48128\nSv 180,1,16,5,1024\nMvav 196,128,100\nv_lshlrev_b32 v204, 16, v222\nMvav 196,144,36\nRa 128,2,49152\nv_and_b32 v205, 0xffff0000, v222\n"
"Mvav 196,148,44\nv_lshlrev_b32 v206, 16, v223\ns_add_u32 s4, s92, 786432\nMvav 196,152,52\nRa 132,2,50176\nCV 176,176,177\nMvav 196,156,60\nWL 6\n"
"buffer_load_dwordx4 v[208:211], v1, s[16:19], s4 offen offset:0 sc0 sc1\nMvav 192,156,56\nRa 136,2,51200\nCV 177,178,179\nMvav 192,152,48\nv_and_b32 v207, 0xffff0000, v223\n"
"CV 178,184,185\nMvav 192,148,40\nRa 140,2,52224\nCV 179,186,187\nMvav 192,144,32\nbuffer_load_dwordx4 v[212:215], v1, s[16:19], s4 offen offset:1024 sc0 sc1\nSv 176,1,16,5,0\n"
"Mvav 200,160,96\nRa 144,2,53248\nWV 5\nv_max_u32 v21, v227, v231\nv_cmp_eq_u32 vcc, -1, v21\ns_nop 0\ns_cbranch_vccnz .S110\n.S111:\nMvav 200,164,104\n"
"v_lshlrev_b32 v160, 16, v224\nv_xor_b32 v27, s15, v27\nv_and_b32 v26, 0xffffdfff, v27\nv_cmp_ne_u32 s[64:65], 0, v27\nv_cmp_ne_u32 vcc, 0, v26\ns_nop 0\ns_cbranch_vccnz .S122\n"
".S123:\nMvav 200,168,112\nRa 148,2,54272\ns_bitcmp1_b32 s64, 0\ns_cselect_b32 s2, 0x2000, 0\nMvav 200,172,120\nWL 6\nMvav 204,172,124\nRa 152,2,55296\n"
"v_and_b32 v161, 0xffff0000, v224\nMvav 204,168,116\nv_lshlrev_b32 v162, 16, v225\nMvav 204,164,108\nRa 156,2,56320\ns_xor_b32 s2, s2, s20\nMvav 204,160,100\n"
"v_and_b32 v163, 0xffff0000, v225\nMvav 204,176,36\nRa 160,2,57344\nv_lshlrev_b32 v164, 16, v228\nMvav 204,180,44\nv_and_b32 v165, 0xffff0000, v228\nMvav 204,184,52\n"
"Ra 164,2,58368\nMvav 204,188,60\nWL 6\ns_add_u32 s24, s2, 0x0\nMvav 200,188,56\nRa 168,2,59392\nv_lshlrev_b32 v166, 16, v229\nMvav 200,184,48\nv_and_b32 v167, 0xffff0000, v229\n"
"s_bitcmp1_b32 s64, 1\ns_cselect_b32 s2, 0x2000, 0\nMvav 200,180,40\nRa 172,2,60416\nMvav 200,176,32\nv_lshlrev_b32 v168, 16, v226\nMvav 160,128,96\nRa 176,2,61440\n"
"v_and_b32 v169, 0xffff0000, v226\nMvav 160,132,104\nv_lshlrev_b32 v170, 16, v227\nCV 196,196,197\nMvav 160,136,112\nRa 180,2,62464\nCV 197,198,199\nMvav 160,140,120\nWL 6\n"
"CV 198,204,205\nMvav 164,140,124\nRa 184,2,63488\nCV 199,206,207\nMvav 164,136,116\nv_and_b32 v171, 0xffff0000, v227\ns_add_u32 s5, s94, 1441792\nMvav 164,132,108\nRa 188,2,64512\n"
"s_barrier\nMvav 164,128,100\nv_lshlrev_b32 v172, 16, v230\nSv 196,1,16,5,1024\nMvav 164,144,36\nRa 128,3,0\nv_and_b32 v173, 0xffff0000, v230\nMvav 164,148,44\n"
"v_lshlrev_b32 v174, 16, v231\nMvav 164,152,52\nRa 132,3,1024\nMvav 164,156,60\nWL 6\ns_xor_b32 s2, s2, s20\nMvav 160,156,56\nRa 136,3,2048\nCV 192,192,193\nMvav 160,152,48\n"
"v_and_b32 v175, 0xffff0000, v231\nCV 193,194,195\nMvav 160,148,40\nRa 140,3,3072\nCV 194,200,201\nMvav 160,144,32\ns_add_u32 s25, s2, 0x1800000\ns_bitcmp1_b32 s64, 2\n"
"s_cselect_b32 s2, 0x2000, 0\nMvav 168,160,96\nRa 144,3,4096\nWV 2\nv_max_u32 v21, v211, v215\nv_cmp_eq_u32 vcc, -1, v21\ns_nop 0\ns_cbranch_vccnz .S113\n.S114:\nMvav 168,164,104\n"
"v_lshlrev_b32 v176, 16, v208\nCV 195,202,203\nMvav 168,168,112\nRa 148,3,5120\nSv 192,1,16,5,0\nMvav 168,172,120\nWL 6\nMvav 172,172,124\nRa 152,3,6144\n"
"v_and_b32 v177, 0xffff0000, v208\nMvav 172,168,116\nv_lshlrev_b32 v178, 16, v209\nMvav 172,164,108\nRa 156,3,7168\ns_xor_b32 s2, s2, s20\nMvav 172,160,100\n"
"v_and_b32 v179, 0xffff0000, v209\nMvav 172,176,36\nRa 160,3,8192\nv_lshlrev_b32 v180, 16, v212\nMvav 172,180,44\nv_and_b32 v181, 0xffff0000, v212\nMvav 172,184,52\nRa 164,3,9216\n"
"Mvav 172,188,60\nWL 6\ns_add_u32 s26, s2, 0x3000000\nMvav 168,188,56\nRa 168,3,10240\nv_lshlrev_b32 v182, 16, v213\nMvav 168,184,48\nv_and_b32 v183, 0xffff0000, v213\n"
"s_bitcmp1_b32 s64, 3\ns_cselect_b32 s2, 0x2000, 0\nMvav 168,180,40\nRa 172,3,11264\nMvav 168,176,32\nv_lshlrev_b32 v184, 16, v210\nMvav 176,128,96\nRa 176,3,12288\n"
"s_xor_b32 s2, s2, s20\nMvav 176,132,104\nv_and_b32 v185, 0xffff0000, v210\nCV 164,164,165\nMvav 176,136,112\nRa 180,3,13312\nCV 165,166,167\nMvav 176,140,120\nWL 6\n"
"CV 166,172,173\nMvav 180,140,124\nRa 184,3,14336\nCV 167,174,175\nMvav 180,136,116\nv_lshlrev_b32 v186, 16, v211\ns_add_u32 s5, s94, 1572864\nMvav 180,132,108\nRa 188,3,15360\n"
"s_add_u32 s27, s2, 0x4800000\nMvav 180,128,100\nv_and_b32 v187, 0xffff0000, v211\ns_bitcmp1_b32 s64, 4\ns_cselect_b32 s2, 0x2000, 0\nMvav 180,144,36\nv_lshlrev_b32 v188, 16, v214\n"
"Sv 164,1,16,5,1024\nMvav 180,148,44\nv_and_b32 v189, 0xffff0000, v214\nMvav 180,152,52\nv_lshlrev_b32 v190, 16, v215\nMvav 180,156,60\nWL 4\ns_xor_b32 s2, s2, s20\n"
"Mvav 176,156,56\nv_and_b32 v191, 0xffff0000, v215\nCV 160,160,161\nMvav 176,152,48\ns_add_u32 s32, s2, 0x6000000\ns_bitcmp1_b32 s64, 5\ns_cselect_b32 s2, 0x2000, 0\n"
"Mvav 176,148,40\ns_xor_b32 s2, s2, s20\nCV 161,162,163\nMvav 176,144,32\ns_add_u32 s33, s2, 0x7800000\ns_bitcmp1_b32 s64, 6\ns_cselect_b32 s2, 0x2000, 0\nMvav 184,160,96\n"
"s_xor_b32 s2, s2, s20\ns_add_u32 s34, s2, 0x9000000\nMvav 184,164,104\ns_bitcmp1_b32 s64, 7\ns_cselect_b32 s2, 0x2000, 0\nMvav 184,168,112\ns_xor_b32 s2, s2, s20\nCV 162,168,169\n"
"Mvav 184,172,120\nWL 0\nCV 163,170,171\nMvav 188,172,124\ns_add_u32 s35, s2, 0xa800000\nSv 160,1,16,5,0\nMvav 188,168,116\nMvav 188,164,108\nMvav 188,160,100\nMvav 188,176,36\n"
"Mvav 188,180,44\nMvav 188,184,52\nMvav 188,188,60\nMvav 184,188,56\nMvav 184,184,48\nMvav 184,180,40\nMvav 184,176,32\nCV 180,180,181\nCV 181,182,183\ns_nop 1\nCV 182,188,189\n"
"CV 183,190,191\ns_add_u32 s5, s94, 1703936\nSv 180,1,16,5,1024\nCV 176,176,177\nCV 177,178,179\nCV 178,184,185\nCV 179,186,187\nSv 176,1,16,5,0\ns_cmp_eq_u32 s13, 0\n"
"s_cbranch_scc1 .Z125\nbuffer_load_dwordx4 v[12:15], v91, s[28:31], s22 offen offset:0\nbuffer_load_dwordx4 v[16:19], v91, s[28:31], s23 offen offset:0\n"
"buffer_load_dwordx4 v[64:67], v89, s[16:19], s40 offen offset:0 sc0 sc1\nbuffer_load_dwordx4 v[68:71], v89, s[16:19], s40 offen offset:16 sc0 sc1\n"
"buffer_load_dwordx4 v[72:75], v89, s[16:19], s40 offen offset:1792 sc0 sc1\nbuffer_load_dwordx4 v[76:79], v89, s[16:19], s40 offen offset:1808 sc0 sc1\n"
"buffer_load_dwordx2 v[80:81], v88, s[16:19], s63 offen offset:0 sc0 sc1\nbuffer_load_dwordx2 v[82:83], v88, s[16:19], s63 offen offset:512 sc0 sc1\n"
"buffer_load_dwordx4 v[96:99], v1, s[16:19], s24 offen offset:0 sc0 sc1\nbuffer_load_dwordx4 v[100:103], v1, s[16:19], s24 offen offset:1024 sc0 sc1\n"
"buffer_load_dwordx4 v[104:107], v1, s[16:19], s25 offen offset:0 sc0 sc1\nbuffer_load_dwordx4 v[108:111], v1, s[16:19], s25 offen offset:1024 sc0 sc1\n"
"buffer_load_dwordx4 v[112:115], v1, s[16:19], s26 offen offset:0 sc0 sc1\nbuffer_load_dwordx4 v[116:119], v1, s[16:19], s26 offen offset:1024 sc0 sc1\n"
"buffer_load_dwordx4 v[120:123], v1, s[16:19], s27 offen offset:0 sc0 sc1\nbuffer_load_dwordx4 v[124:127], v1, s[16:19], s27 offen offset:1024 sc0 sc1\n"
"buffer_load_dwordx4 v[32:35], v1, s[16:19], s32 offen offset:0 sc0 sc1\nbuffer_load_dwordx4 v[36:39], v1, s[16:19], s32 offen offset:1024 sc0 sc1\n"
"buffer_load_dwordx4 v[40:43], v1, s[16:19], s33 offen offset:0 sc0 sc1\nbuffer_load_dwordx4 v[44:47], v1, s[16:19], s33 offen offset:1024 sc0 sc1\n"
"buffer_load_dwordx4 v[48:51], v1, s[16:19], s34 offen offset:0 sc0 sc1\nbuffer_load_dwordx4 v[52:55], v1, s[16:19], s34 offen offset:1024 sc0 sc1\n"
"buffer_load_dwordx4 v[56:59], v1, s[16:19], s35 offen offset:0 sc0 sc1\nbuffer_load_dwordx4 v[60:63], v1, s[16:19], s35 offen offset:1024 sc0 sc1\nWV 16\nv_xor_b32 v66, s15, v66\n"
"v_xor_b32 v70, s15, v70\nv_xor_b32 v74, s15, v74\nv_xor_b32 v78, s15, v78\nv_xor_b32 v81, s15, v81\nv_xor_b32 v83, s15, v83\nv_or3_b32 v92, v66, v70, v74\n"
"v_or3_b32 v92, v92, v78, v81\nv_or_b32 v92, v92, v83\nv_cmp_ne_u32 vcc, 0, v92\ns_nop 0\ns_cbranch_vccnz .S116\n.S117:\nv_and_b32 v92, 0x7ff, v67\nv_lshrrev_b32 v93, 16, v67\n"
"v_cmp_eq_u32 s[44:45], v92, v90\nv_cmp_eq_u32 s[46:47], v93, v90\nv_cmp_eq_u32 s[48:49], v92, v94\nv_cmp_eq_u32 s[50:51], v93, v94\nv_cndmask_b32 v12, v12, v64, s[44:45]\n"
"v_cndmask_b32 v13, v13, v65, s[44:45]\nv_cndmask_b32 v14, v14, v68, s[44:45]\nv_cndmask_b32 v15, v15, v69, s[44:45]\nv_cndmask_b32 v12, v12, v72, s[46:47]\n"
"v_cndmask_b32 v13, v13, v73, s[46:47]\nv_cndmask_b32 v14, v14, v76, s[46:47]\nv_cndmask_b32 v15, v15, v77, s[46:47]\nv_cndmask_b32 v16, v16, v64, s[48:49]\n"
"v_cndmask_b32 v17, v17, v65, s[48:49]\nv_cndmask_b32 v18, v18, v68, s[48:49]\nv_cndmask_b32 v19, v19, v69, s[48:49]\nv_cndmask_b32 v16, v16, v72, s[50:51]\n"
"v_cndmask_b32 v17, v17, v73, s[50:51]\nv_cndmask_b32 v18, v18, v76, s[50:51]\nv_cndmask_b32 v19, v19, v77, s[50:51]\nv_sub_f32 v92, 1.0, v80\nv_sub_f32 v93, 1.0, v82\nCV 92,92,93\n"
"v_and_b32 v93, 0xffff0000, v92\nv_lshlrev_b32 v92, 16, v92\nv_lshlrev_b32 v64, 16, v12\nv_and_b32 v65, 0xffff0000, v12\nMU 64,64,92\nMU 65,65,92\nv_lshlrev_b32 v66, 16, v13\n"
"v_and_b32 v67, 0xffff0000, v13\nMU 66,66,92\nMU 67,67,92\nv_lshlrev_b32 v68, 16, v14\nv_and_b32 v69, 0xffff0000, v14\nMU 68,68,92\nMU 69,69,92\nv_lshlrev_b32 v70, 16, v15\n"
"v_and_b32 v71, 0xffff0000, v15\nMU 70,70,92\nMU 71,71,92\nv_lshlrev_b32 v72, 16, v16\nv_and_b32 v73, 0xffff0000, v16\nMU 72,72,93\nMU 73,73,93\nv_lshlrev_b32 v74, 16, v17\n"
"v_and_b32 v75, 0xffff0000, v17\nMU 74,74,93\nMU 75,75,93\nv_lshlrev_b32 v76, 16, v18\nv_and_b32 v77, 0xffff0000, v18\nMU 76,76,93\nMU 77,77,93\nv_lshlrev_b32 v78, 16, v19\n"
"v_and_b32 v79, 0xffff0000, v19\nMU 78,78,93\nMU 79,79,93\ns_mov_b32 s7, 262144\nWV 0\n.Z128:\nv_max_u32 v84, v99, v103\nv_cmp_eq_u32 s[44:45], -1, v84\nv_max_u32 v84, v107, v111\n"
"v_cmp_eq_u32 s[46:47], -1, v84\nv_max_u32 v84, v115, v119\nv_cmp_eq_u32 s[48:49], -1, v84\nv_max_u32 v84, v123, v127\nv_cmp_eq_u32 s[50:51], -1, v84\nv_max_u32 v84, v35, v39\n"
"v_cmp_eq_u32 s[52:53], -1, v84\nv_max_u32 v84, v43, v47\nv_cmp_eq_u32 s[54:55], -1, v84\nv_max_u32 v84, v51, v55\nv_cmp_eq_u32 s[56:57], -1, v84\nv_max_u32 v84, v59, v63\n"
"v_cmp_eq_u32 s[58:59], -1, v84\ns_or_b64 s[0:1], s[44:45], s[46:47]\ns_or_b64 s[2:3], s[48:49], s[50:51]\ns_or_b64 s[0:1], s[0:1], s[52:53]\ns_or_b64 s[2:3], s[2:3], s[54:55]\n"
"s_or_b64 s[0:1], s[0:1], s[56:57]\ns_or_b64 s[2:3], s[2:3], s[58:59]\ns_or_b64 s[0:1], s[0:1], s[2:3]\ns_cbranch_scc1 .Z126\n.Z127:\nZvvv 136,6,96\nMvvv 136,6,104\nMvvv 136,6,112\n"
"Mvvv 136,6,120\nMvvv 136,6,32\nMvvv 136,6,40\nMvvv 136,6,48\nMvvv 136,6,56\nZvvv 140,4,96\nMvvv 140,4,104\nMvvv 140,4,112\nMvvv 140,4,120\nMvvv 140,4,32\nMvvv 140,4,40\n"
"Mvvv 140,4,48\nMvvv 140,4,56\nZvvv 144,6,100\nMvvv 144,6,108\nMvvv 144,6,116\nMvvv 144,6,124\nMvvv 144,6,36\nMvvv 144,6,44\nMvvv 144,6,52\nMvvv 144,6,60\nZvvv 148,4,100\n"
"Mvvv 148,4,108\nMvvv 148,4,116\nMvvv 148,4,124\nMvvv 148,4,36\nMvvv 148,4,44\nMvvv 148,4,52\nMvvv 148,4,60\nFM 64,80,136\nFM 65,80,137\nFM 66,80,138\nFM 67,80,139\nFM 68,80,140\n"
"FM 69,80,141\nFM 70,80,142\nFM 71,80,143\nCV 12,64,65\nCV 13,66,67\nCV 14,68,69\nCV 15,70,71\nSv 12,91,36,22,0\nFM 72,82,144\nFM 73,82,145\nFM 74,82,146\nFM 75,82,147\n"
"FM 76,82,148\nFM 77,82,149\nFM 78,82,150\nFM 79,82,151\nCV 16,72,73\nCV 17,74,75\nCV 18,76,77\nCV 19,78,79\nSv 16,91,36,23,0\n.Z125:\ns_endpgm\n.E69:\nv_cmpx_gt_i32 vcc, 19, v3\n"
"v_mov_b32 v15, s97\nv_cmpx_gt_i32 vcc, 18, v3\nv_mov_b32 v14, s97\nv_cmpx_gt_i32 vcc, 17, v3\nv_mov_b32 v13, s97\nv_cmpx_gt_i32 vcc, 16, v3\nv_mov_b32 v12, s97\n"
"v_cmpx_gt_i32 vcc, 3, v3\nv_mov_b32 v7, s97\nv_cmpx_gt_i32 vcc, 2, v3\nv_mov_b32 v6, s97\nv_cmpx_gt_i32 vcc, 1, v3\nv_mov_b32 v5, s97\nv_cmpx_gt_i32 vcc, 0, v3\nv_mov_b32 v4, s97\n"
"s_mov_b64 exec, -1\ns_nop 2\ns_branch .E70\n.E76:\nv_cmpx_gt_i32 vcc, 19, v0\nv_mov_b32 v15, s97\nv_cmpx_gt_i32 vcc, 18, v0\nv_mov_b32 v14, s97\nv_cmpx_gt_i32 vcc, 17, v0\n"
"v_mov_b32 v13, s97\nv_cmpx_gt_i32 vcc, 16, v0\nv_mov_b32 v12, s97\nv_cmpx_gt_i32 vcc, 3, v0\nv_mov_b32 v7, s97\nv_cmpx_gt_i32 vcc, 2, v0\nv_mov_b32 v6, s97\n"
"v_cmpx_gt_i32 vcc, 1, v0\nv_mov_b32 v5, s97\nv_cmpx_gt_i32 vcc, 0, v0\nv_mov_b32 v4, s97\ns_mov_b64 exec, -1\ns_nop 2\ns_branch .E77\n.E83:\n"
"buffer_load_dwordx4 v[100:103], v108, s[16:19], s65 offen offset:0 sc0 sc1\nbuffer_load_dwordx4 v[104:107], v108, s[16:19], s65 offen offset:256 sc0 sc1\ns_branch .E84\n.S14:\n"
"s_mov_b32 s7, 262144\n.S16:\nbuffer_load_dwordx4 v[32:35], v1, s[16:19], s6 offen offset:0 sc0 sc1\nbuffer_load_dwordx4 v[36:39], v1, s[16:19], s6 offen offset:1024 sc0 sc1\n"
"buffer_load_dwordx4 v[40:43], v1, s[16:19], s6 offen offset:2048 sc0 sc1\nbuffer_load_dwordx4 v[44:47], v1, s[16:19], s6 offen offset:3072 sc0 sc1\nWV 0\nv_xor_b32 v48, s15, v33\n"
"v_xor_b32 v49, s15, v35\nv_xor_b32 v50, s15, v37\nv_xor_b32 v51, s15, v39\nv_xor_b32 v52, s15, v41\nv_xor_b32 v53, s15, v43\nv_xor_b32 v54, s15, v45\nv_xor_b32 v55, s15, v47\n"
"v_or3_b32 v48, v48, v49, v50\nv_or3_b32 v51, v51, v52, v53\nv_or3_b32 v48, v48, v54, v55\nv_or_b32 v48, v48, v51\nv_cmp_ne_u32 vcc, 0, v48\ns_nop 0\ns_cbranch_vccz .S15\n"
"s_sleep 6\ns_sub_u32 s7, s7, 1\ns_cbranch_scc0 .S16\ns_branch .S15\n.S17:\ns_mov_b32 s7, 262144\n.S19:\ns_lshl_b32 s5, s73, 8\ns_add_u32 s5, s5, 0xc04ac00\n"
"v_lshrrev_b32 v27, 2, v1\nbuffer_load_dword v27, v27, s[16:19], s5 offen sc0 sc1\nWV 0\nv_xor_b32 v27, s15, v27\nv_and_b32 v255, 0xffffdfff, v27\nv_cmp_ne_u32 vcc, 0, v255\n"
"s_nop 0\ns_cbranch_vccz .S18\ns_sleep 6\ns_sub_u32 s7, s7, 1\ns_cbranch_scc0 .S19\ns_branch .S18\n.T22:\ns_mov_b32 s62, 1\ns_call_b64 s[80:81], .RA10\ns_branch .T23\n.T24:\n"
"s_mov_b32 s62, 1\ns_call_b64 s[80:81], .RA10\ns_branch .T25\n.T27:\ns_mov_b32 s62, 0\ns_call_b64 s[80:81], .RB11\ns_branch .T28\n.T29:\ns_mov_b32 s62, 0\n"
"s_call_b64 s[80:81], .RA10\ns_branch .T30\n.T32:\ns_mov_b32 s62, 0\ns_call_b64 s[80:81], .RB11\ns_branch .T33\n.T34:\ns_mov_b32 s62, 0\ns_call_b64 s[80:81], .RA10\ns_branch .T35\n"
".S37:\ns_mov_b32 s7, 262144\n.S39:\nv_and_b32 v27, 0x70, v1\nbuffer_load_dword v27, v27, s[16:19], s61 offen offset:128 sc0 sc1\nWV 0\nv_cmp_ne_u32 vcc, s15, v27\ns_nop 0\n"
"s_cbranch_vccz .S38\ns_sleep 6\ns_sub_u32 s7, s7, 1\ns_cbranch_scc0 .S39\ns_branch .S38\n.S40:\ns_mov_b32 s7, 262144\n.S42:\nv_and_b32 v27, 0x70, v1\n"
"buffer_load_dword v27, v27, s[16:19], s61 offen offset:136 sc0 sc1\nWV 0\nv_cmp_ne_u32 vcc, s15, v27\ns_nop 0\ns_cbranch_vccz .S41\ns_sleep 6\ns_sub_u32 s7, s7, 1\n"
"s_cbranch_scc0 .S42\ns_branch .S41\n.S43:\ns_mov_b32 s7, 262144\n.S45:\nv_and_b32 v27, 0x70, v1\nbuffer_load_dword v27, v27, s[16:19], s61 offen offset:140 sc0 sc1\nWV 0\n"
"v_cmp_ne_u32 vcc, s15, v27\ns_nop 0\ns_cbranch_vccz .S44\ns_sleep 6\ns_sub_u32 s7, s7, 1\ns_cbranch_scc0 .S45\ns_branch .S44\n.S46:\ns_mov_b32 s7, 262144\n.S48:\n"
"v_and_b32 v27, 0x70, v1\nbuffer_load_dword v27, v27, s[16:19], s61 offen offset:12 sc0 sc1\nWV 0\nv_cmp_ne_u32 vcc, s15, v27\ns_nop 0\ns_cbranch_vccz .S47\ns_sleep 6\n"
"s_sub_u32 s7, s7, 1\ns_cbranch_scc0 .S48\ns_branch .S47\n.S49:\ns_mov_b32 s7, 262144\n.S51:\nv_and_b32 v27, 0x70, v1\n"
"buffer_load_dword v27, v27, s[16:19], s61 offen offset:8 sc0 sc1\nWV 0\nv_cmp_ne_u32 vcc, s15, v27\ns_nop 0\ns_cbranch_vccz .S50\ns_sleep 6\ns_sub_u32 s7, s7, 1\n"
"s_cbranch_scc0 .S51\ns_branch .S50\n.S52:\ns_mov_b32 s7, 262144\n.S54:\nv_and_b32 v27, 0xf0, v1\nbuffer_load_dword v27, v27, s[16:19], s61 offen offset:4 sc0 sc1\nWV 0\n"
"v_cmp_ne_u32 vcc, s15, v27\ns_nop 0\ns_cbranch_vccz .S53\ns_sleep 6\ns_sub_u32 s7, s7, 1\ns_cbranch_scc0 .S54\ns_branch .S53\n.S56:\ns_mov_b32 s7, 262144\n.S58:\n"
"v_lshrrev_b32 v144, 9, v1\nv_mul_u32_u24 v144, 0x18000, v144\nv_lshlrev_b32 v144, 8, v144\nv_and_b32 v145, 0x1f0, v1\nv_add_u32 v144, v144, v145\ns_mov_b32 s6, 0x15c0000\n"
"buffer_load_dword v136, v144, s[16:19], s6 offen offset:4 sc0 sc1\ns_mov_b32 s6, 0x15c2000\nbuffer_load_dword v137, v144, s[16:19], s6 offen offset:4 sc0 sc1\n"
"s_mov_b32 s6, 0x45c0000\nbuffer_load_dword v138, v144, s[16:19], s6 offen offset:4 sc0 sc1\ns_mov_b32 s6, 0x45c2000\n"
"buffer_load_dword v139, v144, s[16:19], s6 offen offset:4 sc0 sc1\ns_mov_b32 s6, 0x75c0000\nbuffer_load_dword v140, v144, s[16:19], s6 offen offset:4 sc0 sc1\n"
"s_mov_b32 s6, 0x75c2000\nbuffer_load_dword v141, v144, s[16:19], s6 offen offset:4 sc0 sc1\ns_mov_b32 s6, 0xa5c0000\n"
"buffer_load_dword v142, v144, s[16:19], s6 offen offset:4 sc0 sc1\ns_mov_b32 s6, 0xa5c2000\nbuffer_load_dword v143, v144, s[16:19], s6 offen offset:4 sc0 sc1\nWV 0\n"
"v_xor_b32 v136, s15, v136\nv_xor_b32 v137, s15, v137\nv_xor_b32 v138, s15, v138\nv_xor_b32 v139, s15, v139\nv_xor_b32 v140, s15, v140\nv_xor_b32 v141, s15, v141\n"
"v_xor_b32 v142, s15, v142\nv_xor_b32 v143, s15, v143\nv_min_u32 v136, v136, v137\nv_min_u32 v138, v138, v139\nv_min_u32 v140, v140, v141\nv_min_u32 v142, v142, v143\n"
"v_or3_b32 v136, v136, v138, v140\nv_or_b32 v136, v136, v142\nv_cmp_ne_u32 vcc, 0, v136\ns_nop 0\ns_cbranch_vccz .S57\ns_sleep 6\ns_sub_u32 s7, s7, 1\ns_cbranch_scc0 .S58\n"
"s_branch .S57\n.FF55:\nv_lshrrev_b32 v144, 9, v1\nv_mul_u32_u24 v144, 0x18000, v144\nv_lshlrev_b32 v144, 8, v144\nv_and_b32 v145, 0x1f0, v1\nv_add_u32 v144, v144, v145\n"
"s_mov_b32 s6, 0x15c0000\nbuffer_load_dword v136, v144, s[16:19], s6 offen offset:4 sc0 sc1\ns_mov_b32 s6, 0x15c2000\n"
"buffer_load_dword v137, v144, s[16:19], s6 offen offset:4 sc0 sc1\ns_mov_b32 s6, 0x45c0000\nbuffer_load_dword v138, v144, s[16:19], s6 offen offset:4 sc0 sc1\n"
"s_mov_b32 s6, 0x45c2000\nbuffer_load_dword v139, v144, s[16:19], s6 offen offset:4 sc0 sc1\ns_mov_b32 s6, 0x75c0000\n"
"buffer_load_dword v140, v144, s[16:19], s6 offen offset:4 sc0 sc1\ns_mov_b32 s6, 0x75c2000\nbuffer_load_dword v141, v144, s[16:19], s6 offen offset:4 sc0 sc1\n"
"s_mov_b32 s6, 0xa5c0000\nbuffer_load_dword v142, v144, s[16:19], s6 offen offset:4 sc0 sc1\ns_mov_b32 s6, 0xa5c2000\n"
"buffer_load_dword v143, v144, s[16:19], s6 offen offset:4 sc0 sc1\ns_mov_b32 s90, 0\ns_swappc_b64 s[8:9], s[80:81]\nv_xor_b32 v136, s15, v136\nv_xor_b32 v137, s15, v137\n"
"v_xor_b32 v138, s15, v138\nv_xor_b32 v139, s15, v139\nv_xor_b32 v140, s15, v140\nv_xor_b32 v141, s15, v141\nv_xor_b32 v142, s15, v142\nv_xor_b32 v143, s15, v143\n"
"v_min_u32 v136, v136, v137\nv_min_u32 v138, v138, v139\nv_min_u32 v140, v140, v141\nv_min_u32 v142, v142, v143\nv_or3_b32 v136, v136, v138, v140\nv_or_b32 v136, v136, v142\n"
"v_cmp_ne_u32 vcc, 0, v136\ns_nop 0\ns_cbranch_vccnz .S56\n.S57:\ns_and_b32 s4, s13, 3\ns_lshl2_add_u32 s4, s12, s4\ns_lshr_b32 s5, s71, 16\ns_cmp_lt_u32 s4, 28\n"
"s_cselect_b32 s5, s71, s5\ns_cselect_b32 s3, 0, 28\ns_and_b32 s5, s5, 0x7ff\ns_sub_u32 s3, s4, s3\ns_mul_i32 s2, s4, 7168\ns_add_u32 s2, s2, 0xc04c800\ns_add_u32 s6, s2, 4096\n"
"buffer_load_dwordx4 v[136:139], v1, s[16:19], s2 offen offset:0 sc0 sc1\nbuffer_load_dwordx4 v[140:143], v1, s[16:19], s2 offen offset:1024 sc0 sc1\n"
"buffer_load_dwordx4 v[144:147], v1, s[16:19], s2 offen offset:2048 sc0 sc1\nbuffer_load_dwordx4 v[148:151], v1, s[16:19], s2 offen offset:3072 sc0 sc1\n"
"buffer_load_dwordx4 v[152:155], v1, s[16:19], s6 offen offset:0 sc0 sc1\nbuffer_load_dwordx4 v[156:159], v1, s[16:19], s6 offen offset:1024 sc0 sc1\n"
"buffer_load_dwordx4 v[160:163], v1, s[16:19], s6 offen offset:2048 sc0 sc1\nv_lshrrev_b32 v167, 1, v1\nv_and_b32 v167, 24, v167\ns_mul_i32 s2, s5, 896\ns_lshl_b32 s3, s3, 5\n"
"s_add_u32 s2, s2, s3\ns_lshl2_add_u32 s5, s5, 0xc03e000\nbuffer_load_dwordx2 v[164:165], v167, s[28:31], s2 offen\nbuffer_load_dword v166, off, s[16:19], s5 sc0 sc1\n"
"s_mov_b32 s90, 0\ns_swappc_b64 s[8:9], s[80:81]\nAD 136,136,140\nAD 137,137,141\nAD 138,138,142\nAD 139,139,143\nAD 144,144,148\nAD 145,145,149\nAD 146,146,150\nAD 147,147,151\n"
"AD 152,152,156\nAD 153,153,157\nAD 154,154,158\nAD 155,155,159\nAD 136,136,144\nAD 137,137,145\nAD 138,138,146\nAD 139,139,147\nAD 152,152,160\nAD 153,153,161\nAD 154,154,162\n"
"AD 155,155,163\nAD 136,136,152\nAD 137,137,153\nAD 138,138,154\nAD 139,139,155\nv_mov_b32 v140, v136\nv_mov_b32 v141, v137\nv_mov_b32 v142, v138\nv_mov_b32 v143, v139\n"
"v_permlane32_swap_b32 v136, v140\nv_permlane32_swap_b32 v137, v141\nv_permlane32_swap_b32 v138, v142\nv_permlane32_swap_b32 v139, v143\nAD 136,136,140\nAD 137,137,141\n"
"AD 138,138,142\nAD 139,139,143\nv_mov_b32 v140, v136\nv_mov_b32 v141, v137\nv_mov_b32 v142, v138\nv_mov_b32 v143, v139\nv_permlane16_swap_b32 v136, v140\n"
"v_permlane16_swap_b32 v137, v141\nv_permlane16_swap_b32 v138, v142\nv_permlane16_swap_b32 v139, v143\nAD 136,136,140\nAD 137,137,141\nAD 138,138,142\nAD 139,139,143\n"
"v_add_f32_dpp v136, v136, v136 row_ror:8 row_mask:0xf bank_mask:0xf\nv_add_f32_dpp v137, v137, v137 row_ror:8 row_mask:0xf bank_mask:0xf\n"
"v_add_f32_dpp v138, v138, v138 row_ror:8 row_mask:0xf bank_mask:0xf\nv_add_f32_dpp v139, v139, v139 row_ror:8 row_mask:0xf bank_mask:0xf\n"
"v_add_f32_dpp v136, v136, v136 row_ror:4 row_mask:0xf bank_mask:0xf\nv_add_f32_dpp v137, v137, v137 row_ror:4 row_mask:0xf bank_mask:0xf\n"
"v_add_f32_dpp v138, v138, v138 row_ror:4 row_mask:0xf bank_mask:0xf\nv_add_f32_dpp v139, v139, v139 row_ror:4 row_mask:0xf bank_mask:0xf\nv_mul_f32 v166, 0xbfb8aa3b, v166\n"
"EX 166,166\nCV 136,136,137\nv_add_f32 v166, 1.0, v166\nv_rcp_f32 v166, v166\nCV 138,138,139\nCV 166,166,166\nv_lshlrev_b32 v166, 16, v166\nv_and_b32 v137, 0xffff0000, v136\n"
"v_lshlrev_b32 v136, 16, v136\nv_and_b32 v139, 0xffff0000, v138\nv_lshlrev_b32 v138, 16, v138\nMU 136,136,166\nMU 137,137,166\nMU 138,138,166\nMU 139,139,166\nCV 136,136,137\n"
"CV 138,138,139\nv_and_b32 v137, 0xffff0000, v136\nv_lshlrev_b32 v136, 16, v136\nv_and_b32 v139, 0xffff0000, v138\nv_lshlrev_b32 v138, 16, v138\nv_lshlrev_b32 v140, 16, v164\n"
"v_and_b32 v141, 0xffff0000, v164\nv_lshlrev_b32 v142, 16, v165\nv_and_b32 v143, 0xffff0000, v165\nAD 136,136,140\nAD 137,137,141\nAD 138,138,142\nAD 139,139,143\nCV 136,136,137\n"
"CV 137,138,139\nv_mov_b32 v138, s15\nv_mov_b32 v139, s71\ns_and_b32 s4, s13, 3\ns_lshl2_add_u32 s4, s12, s4\ns_lshr_b32 s5, s71, 16\ns_cmp_lt_u32 s4, 28\n"
"s_cselect_b32 s5, s71, s5\ns_cselect_b32 s3, 0, 28\ns_and_b32 s5, s5, 0x7ff\ns_sub_u32 s3, s4, s3\nv_mov_b32 v140, 0x40000000\nv_cmp_gt_u32 vcc, 64, v1\ns_lshl_b32 s4, s4, 6\n"
"s_add_u32 s4, s4, 0xc04b800\nv_cndmask_b32 v140, v140, v1, vcc\nSv 136,140,16,4,0\ns_mov_b32 s90, 100\ns_swappc_b64 s[8:9], s[80:81]\n.T63:\ns_mov_b32 s62, 0\n"
"s_call_b64 s[80:81], .RB11\ns_branch .T64\n.T65:\ns_mov_b32 s62, 0\ns_call_b64 s[80:81], .RA10\ns_branch .T66\n.T71:\ns_mov_b32 s62, 0\ns_call_b64 s[80:81], .RB11\ns_branch .T72\n"
".T73:\ns_mov_b32 s62, 0\ns_call_b64 s[80:81], .RA10\ns_branch .T74\n.T78:\ns_mov_b32 s62, 0\ns_call_b64 s[80:81], .RB11\ns_branch .T79\n.T80:\ns_mov_b32 s62, 0\n"
"s_call_b64 s[80:81], .RA10\ns_branch .T81\n.S89:\ns_mov_b32 s7, 262144\n.S91:\nbuffer_load_dwordx4 v[100:103], v108, s[16:19], s65 offen offset:0 sc0 sc1\n"
"buffer_load_dwordx4 v[104:107], v108, s[16:19], s65 offen offset:256 sc0 sc1\nWV 0\nv_xor_b32 v109, s15, v102\nv_xor_b32 v110, s15, v106\nv_or_b32 v109, v109, v110\n"
"v_cmp_ne_u32 vcc, 0, v109\ns_nop 0\ns_cbranch_vccz .S90\ns_sleep 6\ns_sub_u32 s7, s7, 1\ns_cbranch_scc0 .S91\ns_branch .S90\n.D85:\nWV 0\nv_xor_b32 v109, s15, v102\n"
"v_xor_b32 v110, s15, v106\nv_or_b32 v109, v109, v110\nv_cmp_ne_u32 vcc, 0, v109\ns_nop 0\ns_cbranch_vccnz .S89\n.S90:\ns_add_u32 s5, s66, 0\n"
"buffer_load_dwordx4 a[192:195], v1, s[16:19], s5 offen offset:0 sc1\nbuffer_load_dwordx4 a[196:199], v1, s[16:19], s5 offen offset:1024 sc1\n"
"buffer_load_dwordx4 a[200:203], v1, s[16:19], s5 offen offset:2048 sc1\nbuffer_load_dwordx4 a[204:207], v1, s[16:19], s5 offen offset:3072 sc1\ns_add_u32 s5, s66, 4096\n"
"buffer_load_dwordx4 a[208:211], v1, s[16:19], s5 offen offset:0 sc1\nbuffer_load_dwordx4 a[212:215], v1, s[16:19], s5 offen offset:1024 sc1\n"
"buffer_load_dwordx4 a[216:219], v1, s[16:19], s5 offen offset:2048 sc1\nbuffer_load_dwordx4 a[220:223], v1, s[16:19], s5 offen offset:3072 sc1\ns_add_u32 s5, s66, 16384\n"
"buffer_load_dwordx4 a[224:227], v1, s[16:19], s5 offen offset:0 sc1\nbuffer_load_dwordx4 a[228:231], v1, s[16:19], s5 offen offset:1024 sc1\n"
"buffer_load_dwordx4 a[232:235], v1, s[16:19], s5 offen offset:2048 sc1\nbuffer_load_dwordx4 a[236:239], v1, s[16:19], s5 offen offset:3072 sc1\ns_add_u32 s5, s66, 20480\n"
"buffer_load_dwordx4 a[240:243], v1, s[16:19], s5 offen offset:0 sc1\nbuffer_load_dwordx4 a[244:247], v1, s[16:19], s5 offen offset:1024 sc1\n"
"buffer_load_dwordx4 a[248:251], v1, s[16:19], s5 offen offset:2048 sc1\nbuffer_load_dwordx4 a[252:255], v1, s[16:19], s5 offen offset:3072 sc1\nWV 0\ns_mov_b32 s67, 0\n"
"s_branch .D86\n.D87:\nv_mov_b32 v130, 0\nv_mov_b32 v131, 0\ns_mov_b32 s63, 0xff800000\ns_mov_b32 s67, 1\ns_branch .D88\n.T92:\ns_lshl3_add_u32 s62, s67, 2\n"
"s_call_b64 s[80:81], .RB11\ns_cmp_eq_u32 s67, 0\ns_cbranch_scc1 .T93\nSU 20,100,88\nv_min_f32 v20, 0, v20\nEX 20,20\ns_nop 0\nFM 86,101,20\nSU 21,104,89\nv_min_f32 v21, 0, v21\n"
"EX 21,21\ns_nop 0\nFM 87,105,21\nAR 22,192\nv_and_b32 v23, 0xffff0000, v22\nv_lshlrev_b32 v22, 16, v22\nMU 22,22,20\nMU 23,23,20\nCV 22,22,23\nAW 192,22\nAR 22,193\n"
"v_and_b32 v23, 0xffff0000, v22\nv_lshlrev_b32 v22, 16, v22\nMU 22,22,20\nMU 23,23,20\nCV 22,22,23\nAW 193,22\nAR 22,194\nv_and_b32 v23, 0xffff0000, v22\n"
"v_lshlrev_b32 v22, 16, v22\nMU 22,22,20\nMU 23,23,20\nCV 22,22,23\nAW 194,22\nAR 22,195\nv_and_b32 v23, 0xffff0000, v22\nv_lshlrev_b32 v22, 16, v22\nMU 22,22,20\nMU 23,23,20\n"
"CV 22,22,23\nAW 195,22\nAR 22,196\nv_and_b32 v23, 0xffff0000, v22\nv_lshlrev_b32 v22, 16, v22\nMU 22,22,21\nMU 23,23,21\nCV 22,22,23\nAW 196,22\nAR 22,197\n"
"v_and_b32 v23, 0xffff0000, v22\nv_lshlrev_b32 v22, 16, v22\nMU 22,22,21\nMU 23,23,21\nCV 22,22,23\nAW 197,22\nAR 22,198\nv_and_b32 v23, 0xffff0000, v22\n"
"v_lshlrev_b32 v22, 16, v22\nMU 22,22,21\nMU 23,23,21\nCV 22,22,23\nAW 198,22\nAR 22,199\nv_and_b32 v23, 0xffff0000, v22\nv_lshlrev_b32 v22, 16, v22\nMU 22,22,21\nMU 23,23,21\n"
"CV 22,22,23\nAW 199,22\nAR 22,200\nv_and_b32 v23, 0xffff0000, v22\nv_lshlrev_b32 v22, 16, v22\nMU 22,22,20\nMU 23,23,20\nCV 22,22,23\nAW 200,22\nAR 22,201\n"
"v_and_b32 v23, 0xffff0000, v22\nv_lshlrev_b32 v22, 16, v22\nMU 22,22,20\nMU 23,23,20\nCV 22,22,23\nAW 201,22\nAR 22,202\nv_and_b32 v23, 0xffff0000, v22\n"
"v_lshlrev_b32 v22, 16, v22\nMU 22,22,20\nMU 23,23,20\nCV 22,22,23\nAW 202,22\nAR 22,203\nv_and_b32 v23, 0xffff0000, v22\nv_lshlrev_b32 v22, 16, v22\nMU 22,22,20\nMU 23,23,20\n"
"CV 22,22,23\nAW 203,22\nAR 22,204\nv_and_b32 v23, 0xffff0000, v22\nv_lshlrev_b32 v22, 16, v22\nMU 22,22,21\nMU 23,23,21\nCV 22,22,23\nAW 204,22\nAR 22,205\n"
"v_and_b32 v23, 0xffff0000, v22\nv_lshlrev_b32 v22, 16, v22\nMU 22,22,21\nMU 23,23,21\nCV 22,22,23\nAW 205,22\nAR 22,206\nv_and_b32 v23, 0xffff0000, v22\n"
"v_lshlrev_b32 v22, 16, v22\nMU 22,22,21\nMU 23,23,21\nCV 22,22,23\nAW 206,22\nAR 22,207\nv_and_b32 v23, 0xffff0000, v22\nv_lshlrev_b32 v22, 16, v22\nMU 22,22,21\nMU 23,23,21\n"
"CV 22,22,23\nAW 207,22\nAR 22,208\nv_and_b32 v23, 0xffff0000, v22\nv_lshlrev_b32 v22, 16, v22\nMU 22,22,20\nMU 23,23,20\nCV 22,22,23\nAW 208,22\nAR 22,209\n"
"v_and_b32 v23, 0xffff0000, v22\nv_lshlrev_b32 v22, 16, v22\nMU 22,22,20\nMU 23,23,20\nCV 22,22,23\nAW 209,22\nAR 22,210\nv_and_b32 v23, 0xffff0000, v22\n"
"v_lshlrev_b32 v22, 16, v22\nMU 22,22,20\nMU 23,23,20\nCV 22,22,23\nAW 210,22\nAR 22,211\nv_and_b32 v23, 0xffff0000, v22\nv_lshlrev_b32 v22, 16, v22\nMU 22,22,20\nMU 23,23,20\n"
"CV 22,22,23\nAW 211,22\nAR 22,212\nv_and_b32 v23, 0xffff0000, v22\nv_lshlrev_b32 v22, 16, v22\nMU 22,22,21\nMU 23,23,21\nCV 22,22,23\nAW 212,22\nAR 22,213\n"
"v_and_b32 v23, 0xffff0000, v22\nv_lshlrev_b32 v22, 16, v22\nMU 22,22,21\nMU 23,23,21\nCV 22,22,23\nAW 213,22\nAR 22,214\nv_and_b32 v23, 0xffff0000, v22\n"
"v_lshlrev_b32 v22, 16, v22\nMU 22,22,21\nMU 23,23,21\nCV 22,22,23\nAW 214,22\nAR 22,215\nv_and_b32 v23, 0xffff0000, v22\nv_lshlrev_b32 v22, 16, v22\nMU 22,22,21\nMU 23,23,21\n"
"CV 22,22,23\nAW 215,22\nAR 22,216\nv_and_b32 v23, 0xffff0000, v22\nv_lshlrev_b32 v22, 16, v22\nMU 22,22,20\nMU 23,23,20\nCV 22,22,23\nAW 216,22\nAR 22,217\n"
"v_and_b32 v23, 0xffff0000, v22\nv_lshlrev_b32 v22, 16, v22\nMU 22,22,20\nMU 23,23,20\nCV 22,22,23\nAW 217,22\nAR 22,218\nv_and_b32 v23, 0xffff0000, v22\n"
"v_lshlrev_b32 v22, 16, v22\nMU 22,22,20\nMU 23,23,20\nCV 22,22,23\nAW 218,22\nAR 22,219\nv_and_b32 v23, 0xffff0000, v22\nv_lshlrev_b32 v22, 16, v22\nMU 22,22,20\nMU 23,23,20\n"
"CV 22,22,23\nAW 219,22\nAR 22,220\nv_and_b32 v23, 0xffff0000, v22\nv_lshlrev_b32 v22, 16, v22\nMU 22,22,21\nMU 23,23,21\nCV 22,22,23\nAW 220,22\nAR 22,221\n"
"v_and_b32 v23, 0xffff0000, v22\nv_lshlrev_b32 v22, 16, v22\nMU 22,22,21\nMU 23,23,21\nCV 22,22,23\nAW 221,22\nAR 22,222\nv_and_b32 v23, 0xffff0000, v22\n"
"v_lshlrev_b32 v22, 16, v22\nMU 22,22,21\nMU 23,23,21\nCV 22,22,23\nAW 222,22\nAR 22,223\nv_and_b32 v23, 0xffff0000, v22\nv_lshlrev_b32 v22, 16, v22\nMU 22,22,21\nMU 23,23,21\n"
"CV 22,22,23\nAW 223,22\nAR 22,224\nv_and_b32 v23, 0xffff0000, v22\nv_lshlrev_b32 v22, 16, v22\nMU 22,22,20\nMU 23,23,20\nCV 22,22,23\nAW 224,22\nAR 22,225\n"
"v_and_b32 v23, 0xffff0000, v22\nv_lshlrev_b32 v22, 16, v22\nMU 22,22,20\nMU 23,23,20\nCV 22,22,23\nAW 225,22\nAR 22,226\nv_and_b32 v23, 0xffff0000, v22\n"
"v_lshlrev_b32 v22, 16, v22\nMU 22,22,20\nMU 23,23,20\nCV 22,22,23\nAW 226,22\nAR 22,227\nv_and_b32 v23, 0xffff0000, v22\nv_lshlrev_b32 v22, 16, v22\nMU 22,22,20\nMU 23,23,20\n"
"CV 22,22,23\nAW 227,22\nAR 22,228\nv_and_b32 v23, 0xffff0000, v22\nv_lshlrev_b32 v22, 16, v22\nMU 22,22,21\nMU 23,23,21\nCV 22,22,23\nAW 228,22\nAR 22,229\n"
"v_and_b32 v23, 0xffff0000, v22\nv_lshlrev_b32 v22, 16, v22\nMU 22,22,21\nMU 23,23,21\nCV 22,22,23\nAW 229,22\nAR 22,230\nv_and_b32 v23, 0xffff0000, v22\n"
"v_lshlrev_b32 v22, 16, v22\nMU 22,22,21\nMU 23,23,21\nCV 22,22,23\nAW 230,22\nAR 22,231\nv_and_b32 v23, 0xffff0000, v22\nv_lshlrev_b32 v22, 16, v22\nMU 22,22,21\nMU 23,23,21\n"
"CV 22,22,23\nAW 231,22\nAR 22,232\nv_and_b32 v23, 0xffff0000, v22\nv_lshlrev_b32 v22, 16, v22\nMU 22,22,20\nMU 23,23,20\nCV 22,22,23\nAW 232,22\nAR 22,233\n"
"v_and_b32 v23, 0xffff0000, v22\nv_lshlrev_b32 v22, 16, v22\nMU 22,22,20\nMU 23,23,20\nCV 22,22,23\nAW 233,22\nAR 22,234\nv_and_b32 v23, 0xffff0000, v22\n"
"v_lshlrev_b32 v22, 16, v22\nMU 22,22,20\nMU 23,23,20\nCV 22,22,23\nAW 234,22\nAR 22,235\nv_and_b32 v23, 0xffff0000, v22\nv_lshlrev_b32 v22, 16, v22\nMU 22,22,20\nMU 23,23,20\n"
"CV 22,22,23\nAW 235,22\nAR 22,236\nv_and_b32 v23, 0xffff0000, v22\nv_lshlrev_b32 v22, 16, v22\nMU 22,22,21\nMU 23,23,21\nCV 22,22,23\nAW 236,22\nAR 22,237\n"
"v_and_b32 v23, 0xffff0000, v22\nv_lshlrev_b32 v22, 16, v22\nMU 22,22,21\nMU 23,23,21\nCV 22,22,23\nAW 237,22\nAR 22,238\nv_and_b32 v23, 0xffff0000, v22\n"
"v_lshlrev_b32 v22, 16, v22\nMU 22,22,21\nMU 23,23,21\nCV 22,22,23\nAW 238,22\nAR 22,239\nv_and_b32 v23, 0xffff0000, v22\nv_lshlrev_b32 v22, 16, v22\nMU 22,22,21\nMU 23,23,21\n"
"CV 22,22,23\nAW 239,22\nAR 22,240\nv_and_b32 v23, 0xffff0000, v22\nv_lshlrev_b32 v22, 16, v22\nMU 22,22,20\nMU 23,23,20\nCV 22,22,23\nAW 240,22\nAR 22,241\n"
"v_and_b32 v23, 0xffff0000, v22\nv_lshlrev_b32 v22, 16, v22\nMU 22,22,20\nMU 23,23,20\nCV 22,22,23\nAW 241,22\nAR 22,242\nv_and_b32 v23, 0xffff0000, v22\n"
"v_lshlrev_b32 v22, 16, v22\nMU 22,22,20\nMU 23,23,20\nCV 22,22,23\nAW 242,22\nAR 22,243\nv_and_b32 v23, 0xffff0000, v22\nv_lshlrev_b32 v22, 16, v22\nMU 22,22,20\nMU 23,23,20\n"
"CV 22,22,23\nAW 243,22\nAR 22,244\nv_and_b32 v23, 0xffff0000, v22\nv_lshlrev_b32 v22, 16, v22\nMU 22,22,21\nMU 23,23,21\nCV 22,22,23\nAW 244,22\nAR 22,245\n"
"v_and_b32 v23, 0xffff0000, v22\nv_lshlrev_b32 v22, 16, v22\nMU 22,22,21\nMU 23,23,21\nCV 22,22,23\nAW 245,22\nAR 22,246\nv_and_b32 v23, 0xffff0000, v22\n"
"v_lshlrev_b32 v22, 16, v22\nMU 22,22,21\nMU 23,23,21\nCV 22,22,23\nAW 246,22\nAR 22,247\nv_and_b32 v23, 0xffff0000, v22\nv_lshlrev_b32 v22, 16, v22\nMU 22,22,21\nMU 23,23,21\n"
"CV 22,22,23\nAW 247,22\nAR 22,248\nv_and_b32 v23, 0xffff0000, v22\nv_lshlrev_b32 v22, 16, v22\nMU 22,22,20\nMU 23,23,20\nCV 22,22,23\nAW 248,22\nAR 22,249\n"
"v_and_b32 v23, 0xffff0000, v22\nv_lshlrev_b32 v22, 16, v22\nMU 22,22,20\nMU 23,23,20\nCV 22,22,23\nAW 249,22\nAR 22,250\nv_and_b32 v23, 0xffff0000, v22\n"
"v_lshlrev_b32 v22, 16, v22\nMU 22,22,20\nMU 23,23,20\nCV 22,22,23\nAW 250,22\nAR 22,251\nv_and_b32 v23, 0xffff0000, v22\nv_lshlrev_b32 v22, 16, v22\nMU 22,22,20\nMU 23,23,20\n"
"CV 22,22,23\nAW 251,22\nAR 22,252\nv_and_b32 v23, 0xffff0000, v22\nv_lshlrev_b32 v22, 16, v22\nMU 22,22,21\nMU 23,23,21\nCV 22,22,23\nAW 252,22\nAR 22,253\n"
"v_and_b32 v23, 0xffff0000, v22\nv_lshlrev_b32 v22, 16, v22\nMU 22,22,21\nMU 23,23,21\nCV 22,22,23\nAW 253,22\nAR 22,254\nv_and_b32 v23, 0xffff0000, v22\n"
"v_lshlrev_b32 v22, 16, v22\nMU 22,22,21\nMU 23,23,21\nCV 22,22,23\nAW 254,22\nAR 22,255\nv_and_b32 v23, 0xffff0000, v22\nv_lshlrev_b32 v22, 16, v22\nMU 22,22,21\nMU 23,23,21\n"
"CV 22,22,23\nAW 255,22\nv_lshrrev_b32 v22, 8, v1\nv_bfe_u32 v23, v1, 6, 2\nv_cmp_eq_u32 vcc, v22, v23\nv_and_b32 v22, 16, v1\nv_mov_b32 v23, 0x3f80\n"
"v_cndmask_b32 v23, 0, v23, vcc\nv_lshlrev_b32 v23, v22, v23\nv_and_b32 v22, 32, v1\nv_cmp_eq_u32 vcc, 0, v22\nv_mov_b32 v128, 0\nv_mov_b32 v129, 0\n"
"v_cndmask_b32 v130, 0, v23, vcc\nv_sub_u32 v131, v23, v130\nv_mov_b32 v132, 0\nv_mov_b32 v133, 0\ns_nop 1\nMava 0,130,192\nMava 8,128,192\nMava 12,128,196\nMava 4,130,196\n"
"Mava 16,130,200\nMava 24,128,200\nMava 28,128,204\nMava 20,130,204\nMava 32,130,208\nMava 40,128,208\nMava 44,128,212\nMava 36,130,212\nMava 48,130,216\nMava 56,128,216\n"
"Mava 60,128,220\nMava 52,130,220\nMava 64,130,224\nMava 72,128,224\nMava 76,128,228\nMava 68,130,228\nMava 80,130,232\nMava 88,128,232\nMava 92,128,236\nMava 84,130,236\n"
"Mava 96,130,240\nMava 104,128,240\nMava 108,128,244\nMava 100,130,244\nMava 112,130,248\nMava 120,128,248\nMava 124,128,252\nMava 116,130,252\ns_nop 7\ns_branch .T93\n.S95:\n"
"s_mov_b32 s7, 262144\n.S97:\ns_add_u32 s0, s92, 0\nbuffer_load_dwordx4 v[208:211], v1, s[16:19], s0 offen offset:0 sc0 sc1\n"
"buffer_load_dwordx4 v[212:215], v1, s[16:19], s0 offen offset:1024 sc0 sc1\nWV 0\nv_max_u32 v21, v211, v215\nv_cmp_eq_u32 vcc, -1, v21\ns_nop 0\ns_cbranch_vccz .S96\ns_sleep 6\n"
"s_sub_u32 s7, s7, 1\ns_cbranch_scc0 .S97\ns_branch .S96\n.S98:\ns_mov_b32 s7, 262144\n.S100:\ns_add_u32 s0, s92, 131072\n"
"buffer_load_dwordx4 v[216:219], v1, s[16:19], s0 offen offset:0 sc0 sc1\nbuffer_load_dwordx4 v[220:223], v1, s[16:19], s0 offen offset:1024 sc0 sc1\nWV 0\n"
"v_max_u32 v21, v219, v223\nv_cmp_eq_u32 vcc, -1, v21\ns_nop 0\ns_cbranch_vccz .S99\ns_sleep 6\ns_sub_u32 s7, s7, 1\ns_cbranch_scc0 .S100\ns_branch .S99\n.S101:\n"
"s_mov_b32 s7, 262144\n.S103:\ns_add_u32 s0, s92, 262144\nbuffer_load_dwordx4 v[224:227], v1, s[16:19], s0 offen offset:0 sc0 sc1\n"
"buffer_load_dwordx4 v[228:231], v1, s[16:19], s0 offen offset:1024 sc0 sc1\nWV 0\nv_max_u32 v21, v227, v231\nv_cmp_eq_u32 vcc, -1, v21\ns_nop 0\ns_cbranch_vccz .S102\ns_sleep 6\n"
"s_sub_u32 s7, s7, 1\ns_cbranch_scc0 .S103\ns_branch .S102\n.S104:\ns_mov_b32 s7, 262144\n.S106:\ns_add_u32 s0, s92, 393216\n"
"buffer_load_dwordx4 v[208:211], v1, s[16:19], s0 offen offset:0 sc0 sc1\nbuffer_load_dwordx4 v[212:215], v1, s[16:19], s0 offen offset:1024 sc0 sc1\nWV 0\n"
"v_max_u32 v21, v211, v215\nv_cmp_eq_u32 vcc, -1, v21\ns_nop 0\ns_cbranch_vccz .S105\ns_sleep 6\ns_sub_u32 s7, s7, 1\ns_cbranch_scc0 .S106\ns_branch .S105\n.S107:\n"
"s_mov_b32 s7, 262144\n.S109:\ns_add_u32 s0, s92, 524288\nbuffer_load_dwordx4 v[216:219], v1, s[16:19], s0 offen offset:0 sc0 sc1\n"
"buffer_load_dwordx4 v[220:223], v1, s[16:19], s0 offen offset:1024 sc0 sc1\nWV 0\nv_max_u32 v21, v219, v223\nv_cmp_eq_u32 vcc, -1, v21\ns_nop 0\ns_cbranch_vccz .S108\ns_sleep 6\n"
"s_sub_u32 s7, s7, 1\ns_cbranch_scc0 .S109\ns_branch .S108\n.S110:\ns_mov_b32 s7, 262144\n.S112:\ns_add_u32 s0, s92, 655360\n"
"buffer_load_dwordx4 v[224:227], v1, s[16:19], s0 offen offset:0 sc0 sc1\nbuffer_load_dwordx4 v[228:231], v1, s[16:19], s0 offen offset:1024 sc0 sc1\nWV 0\n"
"v_max_u32 v21, v227, v231\nv_cmp_eq_u32 vcc, -1, v21\ns_nop 0\ns_cbranch_vccz .S111\ns_sleep 6\ns_sub_u32 s7, s7, 1\ns_cbranch_scc0 .S112\ns_branch .S111\n.S113:\n"
"s_mov_b32 s7, 262144\n.S115:\ns_add_u32 s0, s92, 786432\nbuffer_load_dwordx4 v[208:211], v1, s[16:19], s0 offen offset:0 sc0 sc1\n"
"buffer_load_dwordx4 v[212:215], v1, s[16:19], s0 offen offset:1024 sc0 sc1\nWV 0\nv_max_u32 v21, v211, v215\nv_cmp_eq_u32 vcc, -1, v21\ns_nop 0\ns_cbranch_vccz .S114\ns_sleep 6\n"
"s_sub_u32 s7, s7, 1\ns_cbranch_scc0 .S115\ns_branch .S114\n.S116:\ns_mov_b32 s7, 262144\n.S118:\nbuffer_load_dwordx4 v[64:67], v89, s[16:19], s40 offen offset:0 sc0 sc1\n"
"buffer_load_dwordx4 v[68:71], v89, s[16:19], s40 offen offset:16 sc0 sc1\nbuffer_load_dwordx4 v[72:75], v89, s[16:19], s40 offen offset:1792 sc0 sc1\n"
"buffer_load_dwordx4 v[76:79], v89, s[16:19], s40 offen offset:1808 sc0 sc1\nbuffer_load_dwordx2 v[80:81], v88, s[16:19], s63 offen offset:0 sc0 sc1\n"
"buffer_load_dwordx2 v[82:83], v88, s[16:19], s63 offen offset:512 sc0 sc1\nWV 0\nv_xor_b32 v66, s15, v66\nv_xor_b32 v70, s15, v70\nv_xor_b32 v74, s15, v74\n"
"v_xor_b32 v78, s15, v78\nv_xor_b32 v81, s15, v81\nv_xor_b32 v83, s15, v83\nv_or3_b32 v92, v66, v70, v74\nv_or3_b32 v92, v92, v78, v81\nv_or_b32 v92, v92, v83\n"
"v_cmp_ne_u32 vcc, 0, v92\ns_nop 0\ns_cbranch_vccz .S117\ns_sleep 6\ns_sub_u32 s7, s7, 1\ns_cbranch_scc0 .S118\ns_branch .S117\n.S119:\ns_mov_b32 s7, 262144\n.S121:\n"
"buffer_load_dword v27, off, s[16:19], s60 offset:512 sc0 sc1\nWV 0\nv_cmp_ne_u32 vcc, s15, v27\ns_nop 0\ns_cbranch_vccz .S120\ns_sleep 6\ns_sub_u32 s7, s7, 1\n"
"s_cbranch_scc0 .S121\ns_branch .S120\n.S122:\ns_mov_b32 s7, 262144\n.S124:\nbuffer_load_dword v27, v24, s[16:19], s41 offen sc0 sc1\nWV 0\nv_xor_b32 v27, s15, v27\n"
"v_and_b32 v26, 0xffffdfff, v27\nv_cmp_ne_u32 s[64:65], 0, v27\nv_cmp_ne_u32 vcc, 0, v26\ns_nop 0\ns_cbranch_vccz .S123\ns_sleep 6\ns_sub_u32 s7, s7, 1\ns_cbranch_scc0 .S124\n"
"s_branch .S123\n.Z126:\ns_sub_u32 s7, s7, 1\ns_cbranch_scc1 .Z127\ns_cmp_lg_u64 s[44:45], 0\ns_cbranch_scc0 .Z129\n"
"buffer_load_dwordx4 v[96:99], v1, s[16:19], s24 offen offset:0 sc0 sc1\nbuffer_load_dwordx4 v[100:103], v1, s[16:19], s24 offen offset:1024 sc0 sc1\n.Z129:\n"
"s_cmp_lg_u64 s[46:47], 0\ns_cbranch_scc0 .Z130\nbuffer_load_dwordx4 v[104:107], v1, s[16:19], s25 offen offset:0 sc0 sc1\n"
"buffer_load_dwordx4 v[108:111], v1, s[16:19], s25 offen offset:1024 sc0 sc1\n.Z130:\ns_cmp_lg_u64 s[48:49], 0\ns_cbranch_scc0 .Z131\n"
"buffer_load_dwordx4 v[112:115], v1, s[16:19], s26 offen offset:0 sc0 sc1\nbuffer_load_dwordx4 v[116:119], v1, s[16:19], s26 offen offset:1024 sc0 sc1\n.Z131:\n"
"s_cmp_lg_u64 s[50:51], 0\ns_cbranch_scc0 .Z132\nbuffer_load_dwordx4 v[120:123], v1, s[16:19], s27 offen offset:0 sc0 sc1\n"
"buffer_load_dwordx4 v[124:127], v1, s[16:19], s27 offen offset:1024 sc0 sc1\n.Z132:\ns_cmp_lg_u64 s[52:53], 0\ns_cbranch_scc0 .Z133\n"
"buffer_load_dwordx4 v[32:35], v1, s[16:19], s32 offen offset:0 sc0 sc1\nbuffer_load_dwordx4 v[36:39], v1, s[16:19], s32 offen offset:1024 sc0 sc1\n.Z133:\n"
"s_cmp_lg_u64 s[54:55], 0\ns_cbranch_scc0 .Z134\nbuffer_load_dwordx4 v[40:43], v1, s[16:19], s33 offen offset:0 sc0 sc1\n"
"buffer_load_dwordx4 v[44:47], v1, s[16:19], s33 offen offset:1024 sc0 sc1\n.Z134:\ns_cmp_lg_u64 s[56:57], 0\ns_cbranch_scc0 .Z135\n"
"buffer_load_dwordx4 v[48:51], v1, s[16:19], s34 offen offset:0 sc0 sc1\nbuffer_load_dwordx4 v[52:55], v1, s[16:19], s34 offen offset:1024 sc0 sc1\n.Z135:\n"
"s_cmp_lg_u64 s[58:59], 0\ns_cbranch_scc0 .Z136\nbuffer_load_dwordx4 v[56:59], v1, s[16:19], s35 offen offset:0 sc0 sc1\n"
"buffer_load_dwordx4 v[60:63], v1, s[16:19], s35 offen offset:1024 sc0 sc1\n.Z136:\ns_sleep 8\nWV 0\ns_branch .Z128\n.RA10:\ns_nop 7\ns_nop 3\nv_max3_f32 v22, v32, v33, v34\n"
"v_max3_f32 v22, v22, v35, v40\nv_max3_f32 v22, v22, v41, v42\nv_max_f32 v22, v22, v43\nv_mov_b32 v91, v22\ns_nop 1\nv_permlane32_swap_b32 v22, v91\nv_max_f32 v22, v22, v91\n"
"v_mov_b32 v91, v22\ns_nop 1\nv_permlane16_swap_b32 v22, v91\nv_max_f32 v22, v22, v91\nv_ceil_f32 v22, v22\nv_max3_f32 v23, v36, v37, v38\nv_max3_f32 v23, v23, v39, v44\n"
"v_max3_f32 v23, v23, v45, v46\nv_max_f32 v23, v23, v47\nv_mov_b32 v91, v23\ns_nop 1\nv_permlane32_swap_b32 v23, v91\nv_max_f32 v23, v23, v91\nv_mov_b32 v91, v23\ns_nop 1\n"
"v_permlane16_swap_b32 v23, v91\nv_max_f32 v23, v23, v91\nv_ceil_f32 v23, v23\ns_bitcmp1_b32 s62, 0\ns_cbranch_scc0 .R137\nv_cmp_lt_f32 vcc, 0xff000000, v22\ns_nop 1\n"
"v_cndmask_b32 v22, 0, v22, vcc\nv_cmp_lt_f32 vcc, 0xff000000, v23\ns_nop 1\nv_cndmask_b32 v23, 0, v23, vcc\ns_branch .R138\n.R137:\nv_max_f32 v22, 0, v22\nv_max_f32 v23, 0, v23\n"
"s_bitcmp1_b32 s62, 3\ns_cbranch_scc0 .R138\nv_max_f32 v22, v20, v22\nv_max_f32 v23, v21, v23\n.R138:\nAD 88,88,22\nAD 89,89,23\nSU 32,32,22\nSU 33,33,22\nSU 34,34,22\nSU 35,35,22\n"
"SU 36,36,23\nSU 37,37,23\nSU 38,38,23\nSU 39,39,23\nSU 40,40,22\nSU 41,41,22\nSU 42,42,22\nSU 43,43,22\nSU 44,44,23\nSU 45,45,23\nSU 46,46,23\nSU 47,47,23\nv_mov_b32 v90, v80\n"
"v_mov_b32 v91, v81\nEX 80,32\nEX 81,33\ns_nop 0\nAD 84,80,81\nCV 64,80,81\nEX 82,34\nEX 83,35\nAD 84,84,82\nAD 84,84,83\nCV 65,82,83\nEX 80,36\nEX 81,37\ns_nop 0\nAD 85,80,81\n"
"CV 68,80,81\nEX 82,38\nEX 83,39\nAD 85,85,82\nAD 85,85,83\nCV 69,82,83\nEX 80,40\nEX 81,41\nAD 84,84,80\nAD 84,84,81\nCV 66,80,81\nEX 82,42\nEX 83,43\nAD 84,84,82\nAD 84,84,83\n"
"CV 67,82,83\nEX 80,44\nEX 81,45\nAD 85,85,80\nAD 85,85,81\nCV 70,80,81\nEX 82,46\nEX 83,47\nAD 85,85,82\nAD 85,85,83\nCV 71,82,83\nv_mov_b32 v80, v90\nv_mov_b32 v81, v91\n"
"s_bitcmp1_b32 s62, 1\ns_cbranch_scc1 .R139\nSU 48,48,22\nSU 49,49,22\nSU 50,50,22\nSU 51,51,22\nSU 52,52,23\nSU 53,53,23\nSU 54,54,23\nSU 55,55,23\nSU 56,56,22\nSU 57,57,22\n"
"SU 58,58,22\nSU 59,59,22\nSU 60,60,23\nSU 61,61,23\nSU 62,62,23\nSU 63,63,23\nSU 4,4,22\nSU 5,5,22\nSU 6,6,22\nSU 7,7,22\nSU 8,8,23\nSU 9,9,23\nSU 10,10,23\nSU 11,11,23\n"
"SU 12,12,22\nSU 13,13,22\nSU 14,14,22\nSU 15,15,22\nSU 16,16,23\nSU 17,17,23\nSU 18,18,23\nSU 19,19,23\nEX 80,48\nEX 81,49\n.R139:\ns_bitcmp1_b32 s62, 0\ns_cbranch_scc0 .RS12\n"
"s_nop 1\ns_setpc_b64 s[80:81]\n.RB11:\ns_nop 7\ns_nop 3\nv_max3_f32 v22, v48, v49, v50\nv_max3_f32 v22, v22, v51, v56\nv_max3_f32 v22, v22, v57, v58\nv_max_f32 v22, v22, v59\n"
"v_mov_b32 v91, v22\ns_nop 1\nv_permlane32_swap_b32 v22, v91\nv_max_f32 v22, v22, v91\nv_mov_b32 v91, v22\ns_nop 1\nv_permlane16_swap_b32 v22, v91\nv_max_f32 v22, v22, v91\n"
"v_ceil_f32 v22, v22\nv_max3_f32 v23, v52, v53, v54\nv_max3_f32 v23, v23, v55, v60\nv_max3_f32 v23, v23, v61, v62\nv_max_f32 v23, v23, v63\nv_mov_b32 v91, v23\ns_nop 1\n"
"v_permlane32_swap_b32 v23, v91\nv_max_f32 v23, v23, v91\nv_mov_b32 v91, v23\ns_nop 1\nv_permlane16_swap_b32 v23, v91\nv_max_f32 v23, v23, v91\nv_ceil_f32 v23, v23\n"
"s_bitcmp1_b32 s62, 0\ns_cbranch_scc0 .R142\nv_cmp_lt_f32 vcc, 0xff000000, v22\nv_cndmask_b32 v22, 0, v22, vcc\nv_cmp_lt_f32 vcc, 0xff000000, v23\nv_cndmask_b32 v23, 0, v23, vcc\n"
"s_branch .R143\n.R142:\nv_max_f32 v22, 0, v22\nv_max_f32 v23, 0, v23\ns_bitcmp1_b32 s62, 3\ns_cbranch_scc0 .R143\nv_max_f32 v22, v20, v22\nv_max_f32 v23, v21, v23\n.R143:\n"
"AD 88,88,22\nAD 89,89,23\nSU 48,48,22\nSU 49,49,22\nSU 50,50,22\nSU 51,51,22\nSU 52,52,23\nSU 53,53,23\nSU 54,54,23\nSU 55,55,23\nSU 56,56,22\nSU 57,57,22\nSU 58,58,22\n"
"SU 59,59,22\nSU 60,60,23\nSU 61,61,23\nSU 62,62,23\nSU 63,63,23\nv_mov_b32 v90, v80\nv_mov_b32 v91, v81\nEX 80,48\nEX 81,49\ns_nop 0\nAD 84,80,81\nCV 72,80,81\nEX 82,50\nEX 83,51\n"
"AD 84,84,82\nAD 84,84,83\nCV 73,82,83\nEX 80,52\nEX 81,53\ns_nop 0\nAD 85,80,81\nCV 76,80,81\nEX 82,54\nEX 83,55\nAD 85,85,82\nAD 85,85,83\nCV 77,82,83\nEX 80,56\nEX 81,57\n"
"AD 84,84,80\nAD 84,84,81\nCV 74,80,81\nEX 82,58\nEX 83,59\nAD 84,84,82\nAD 84,84,83\nCV 75,82,83\nEX 80,60\nEX 81,61\nAD 85,85,80\nAD 85,85,81\nCV 78,80,81\nEX 82,62\nEX 83,63\n"
"AD 85,85,82\nAD 85,85,83\nCV 79,82,83\nv_mov_b32 v80, v90\nv_mov_b32 v81, v91\ns_bitcmp1_b32 s62, 1\ns_cbranch_scc1 .R144\nSU 32,32,22\nSU 33,33,22\nSU 34,34,22\nSU 35,35,22\n"
"SU 36,36,23\nSU 37,37,23\nSU 38,38,23\nSU 39,39,23\nSU 40,40,22\nSU 41,41,22\nSU 42,42,22\nSU 43,43,22\nSU 44,44,23\nSU 45,45,23\nSU 46,46,23\nSU 47,47,23\nSU 4,4,22\nSU 5,5,22\n"
"SU 6,6,22\nSU 7,7,22\nSU 8,8,23\nSU 9,9,23\nSU 10,10,23\nSU 11,11,23\nSU 12,12,22\nSU 13,13,22\nSU 14,14,22\nSU 15,15,22\nSU 16,16,23\nSU 17,17,23\nSU 18,18,23\nSU 19,19,23\n"
"EX 80,32\nEX 81,33\n.R144:\ns_bitcmp1_b32 s62, 0\ns_cbranch_scc0 .RS12\ns_nop 1\ns_setpc_b64 s[80:81]\n.RS12:\nv_exp_f32 v22, -v22\nv_exp_f32 v23, -v23\nMU 86,86,22\nMU 87,87,23\n"
"AR 91,0\nMU 91,91,22\nAW 0,91\nAR 91,1\nMU 91,91,22\nAW 1,91\nAR 91,2\nMU 91,91,22\nAW 2,91\nAR 91,3\nMU 91,91,22\nAW 3,91\nAR 91,4\nMU 91,91,23\nAW 4,91\nAR 91,5\nMU 91,91,23\n"
"AW 5,91\nAR 91,6\nMU 91,91,23\nAW 6,91\nAR 91,7\nMU 91,91,23\nAW 7,91\nAR 91,8\nMU 91,91,22\nAW 8,91\nAR 91,9\nMU 91,91,22\nAW 9,91\nAR 91,10\nMU 91,91,22\nAW 10,91\nAR 91,11\n"
"MU 91,91,22\nAW 11,91\nAR 91,12\nMU 91,91,23\nAW 12,91\nAR 91,13\nMU 91,91,23\nAW 13,91\nAR 91,14\nMU 91,91,23\nAW 14,91\nAR 91,15\nMU 91,91,23\nAW 15,91\nAR 91,16\nMU 91,91,22\n"
"AW 16,91\nAR 91,17\nMU 91,91,22\nAW 17,91\nAR 91,18\nMU 91,91,22\nAW 18,91\nAR 91,19\nMU 91,91,22\nAW 19,91\nAR 91,20\nMU 91,91,23\nAW 20,91\nAR 91,21\nMU 91,91,23\nAW 21,91\n"
"AR 91,22\nMU 91,91,23\nAW 22,91\nAR 91,23\nMU 91,91,23\nAW 23,91\nAR 91,24\nMU 91,91,22\nAW 24,91\nAR 91,25\nMU 91,91,22\nAW 25,91\nAR 91,26\nMU 91,91,22\nAW 26,91\nAR 91,27\n"
"MU 91,91,22\nAW 27,91\nAR 91,28\nMU 91,91,23\nAW 28,91\nAR 91,29\nMU 91,91,23\nAW 29,91\nAR 91,30\nMU 91,91,23\nAW 30,91\nAR 91,31\nMU 91,91,23\nAW 31,91\nAR 91,32\nMU 91,91,22\n"
"AW 32,91\nAR 91,33\nMU 91,91,22\nAW 33,91\nAR 91,34\nMU 91,91,22\nAW 34,91\nAR 91,35\nMU 91,91,22\nAW 35,91\nAR 91,36\nMU 91,91,23\nAW 36,91\nAR 91,37\nMU 91,91,23\nAW 37,91\n"
"AR 91,38\nMU 91,91,23\nAW 38,91\nAR 91,39\nMU 91,91,23\nAW 39,91\nAR 91,40\nMU 91,91,22\nAW 40,91\nAR 91,41\nMU 91,91,22\nAW 41,91\nAR 91,42\nMU 91,91,22\nAW 42,91\nAR 91,43\n"
"MU 91,91,22\nAW 43,91\nAR 91,44\nMU 91,91,23\nAW 44,91\nAR 91,45\nMU 91,91,23\nAW 45,91\nAR 91,46\nMU 91,91,23\nAW 46,91\nAR 91,47\nMU 91,91,23\nAW 47,91\nAR 91,48\nMU 91,91,22\n"
"AW 48,91\nAR 91,49\nMU 91,91,22\nAW 49,91\nAR 91,50\nMU 91,91,22\nAW 50,91\nAR 91,51\nMU 91,91,22\nAW 51,91\nAR 91,52\nMU 91,91,23\nAW 52,91\nAR 91,53\nMU 91,91,23\nAW 53,91\n"
"AR 91,54\nMU 91,91,23\nAW 54,91\nAR 91,55\nMU 91,91,23\nAW 55,91\nAR 91,56\nMU 91,91,22\nAW 56,91\nAR 91,57\nMU 91,91,22\nAW 57,91\nAR 91,58\nMU 91,91,22\nAW 58,91\nAR 91,59\n"
"MU 91,91,22\nAW 59,91\nAR 91,60\nMU 91,91,23\nAW 60,91\nAR 91,61\nMU 91,91,23\nAW 61,91\nAR 91,62\nMU 91,91,23\nAW 62,91\nAR 91,63\nMU 91,91,23\nAW 63,91\nAR 91,64\nMU 91,91,22\n"
"AW 64,91\nAR 91,65\nMU 91,91,22\nAW 65,91\nAR 91,66\nMU 91,91,22\nAW 66,91\nAR 91,67\nMU 91,91,22\nAW 67,91\nAR 91,68\nMU 91,91,23\nAW 68,91\nAR 91,69\nMU 91,91,23\nAW 69,91\n"
"AR 91,70\nMU 91,91,23\nAW 70,91\nAR 91,71\nMU 91,91,23\nAW 71,91\nAR 91,72\nMU 91,91,22\nAW 72,91\nAR 91,73\nMU 91,91,22\nAW 73,91\nAR 91,74\nMU 91,91,22\nAW 74,91\nAR 91,75\n"
"MU 91,91,22\nAW 75,91\nAR 91,76\nMU 91,91,23\nAW 76,91\nAR 91,77\nMU 91,91,23\nAW 77,91\nAR 91,78\nMU 91,91,23\nAW 78,91\nAR 91,79\nMU 91,91,23\nAW 79,91\nAR 91,80\nMU 91,91,22\n"
"AW 80,91\nAR 91,81\nMU 91,91,22\nAW 81,91\nAR 91,82\nMU 91,91,22\nAW 82,91\nAR 91,83\nMU 91,91,22\nAW 83,91\nAR 91,84\nMU 91,91,23\nAW 84,91\nAR 91,85\nMU 91,91,23\nAW 85,91\n"
"AR 91,86\nMU 91,91,23\nAW 86,91\nAR 91,87\nMU 91,91,23\nAW 87,91\nAR 91,88\nMU 91,91,22\nAW 88,91\nAR 91,89\nMU 91,91,22\nAW 89,91\nAR 91,90\nMU 91,91,22\nAW 90,91\nAR 91,91\n"
"MU 91,91,22\nAW 91,91\nAR 91,92\nMU 91,91,23\nAW 92,91\nAR 91,93\nMU 91,91,23\nAW 93,91\nAR 91,94\nMU 91,91,23\nAW 94,91\nAR 91,95\nMU 91,91,23\nAW 95,91\nAR 91,96\nMU 91,91,22\n"
"AW 96,91\nAR 91,97\nMU 91,91,22\nAW 97,91\nAR 91,98\nMU 91,91,22\nAW 98,91\nAR 91,99\nMU 91,91,22\nAW 99,91\nAR 91,100\nMU 91,91,23\nAW 100,91\nAR 91,101\nMU 91,91,23\nAW 101,91\n"
"AR 91,102\nMU 91,91,23\nAW 102,91\nAR 91,103\nMU 91,91,23\nAW 103,91\nAR 91,104\nMU 91,91,22\nAW 104,91\nAR 91,105\nMU 91,91,22\nAW 105,91\nAR 91,106\nMU 91,91,22\nAW 106,91\n"
"AR 91,107\nMU 91,91,22\nAW 107,91\nAR 91,108\nMU 91,91,23\nAW 108,91\nAR 91,109\nMU 91,91,23\nAW 109,91\nAR 91,110\nMU 91,91,23\nAW 110,91\nAR 91,111\nMU 91,91,23\nAW 111,91\n"
"AR 91,112\nMU 91,91,22\nAW 112,91\nAR 91,113\nMU 91,91,22\nAW 113,91\nAR 91,114\nMU 91,91,22\nAW 114,91\nAR 91,115\nMU 91,91,22\nAW 115,91\nAR 91,116\nMU 91,91,23\nAW 116,91\n"
"AR 91,117\nMU 91,91,23\nAW 117,91\nAR 91,118\nMU 91,91,23\nAW 118,91\nAR 91,119\nMU 91,91,23\nAW 119,91\nAR 91,120\nMU 91,91,22\nAW 120,91\nAR 91,121\nMU 91,91,22\nAW 121,91\n"
"AR 91,122\nMU 91,91,22\nAW 122,91\nAR 91,123\nMU 91,91,22\nAW 123,91\nAR 91,124\nMU 91,91,23\nAW 124,91\nAR 91,125\nMU 91,91,23\nAW 125,91\nAR 91,126\nMU 91,91,23\nAW 126,91\n"
"AR 91,127\nMU 91,91,23\nAW 127,91\ns_nop 1\ns_setpc_b64 s[80:81]\n.p2align 12\n.PA3:\nv_add_u32 v128, 0x10000, v12\nv_add_u32 v129, 0x10000, v13\nv_add_u32 v130, 0x10000, v14\n"
"v_add_u32 v131, 0x10000, v15\nv_add_u32 v132, 0x10000, v16\nv_add_u32 v133, 0x10000, v17\nv_add_u32 v134, 0x10000, v18\nv_add_u32 v135, 0x10000, v19\ns_add_u32 s2, s11, 1\n"
"s_lshr_b32 s2, s2, 1\ns_lshl_b32 s2, s2, 12\nv_add_u32 v140, s2, v10\nv_add_u32 v141, s2, v11\nv_xor_b32 v142, 0x1000, v140\nv_xor_b32 v143, 0x1000, v141\ns_add_u32 m0, s47, 0\n"
"s_add_u32 s46, s51, 524288\nD0 6,32,46,0\nD0 7,32,46,1024\nD0 8,32,46,2048\nD0 9,32,46,3072\ns_add_u32 m0, s47, -49152\ns_add_u32 s46, s52, 128\nD0 136,28,46,0\nD0 137,28,46,1024\n"
"D0 138,28,46,2048\nD0 139,28,46,3072\ns_add_u32 m0, s47, 16384\ns_add_u32 s46, s51, 786432\nD0 6,32,46,0\nD0 7,32,46,1024\nD0 8,32,46,2048\nD0 9,32,46,3072\n"
"v_mov_b32 v116, 0x40000000\nv_cmp_gt_u32 vcc, 56, v0\ns_lshl2_add_u32 s2, s10, s11\ns_mul_i32 s2, s2, 1792\nv_cndmask_b32 v117, v116, v1, vcc\ns_mov_b32 s36, s56\n"
"s_and_b32 s37, s57, 0xffff\ns_mov_b32 s38, 0x380\ns_mov_b32 s39, 0x20000\ns_mov_b32 s40, s58\ns_and_b32 s41, s59, 0xffff\ns_mov_b32 s42, 0x380\ns_mov_b32 s43, 0x20000\n"
"buffer_load_dwordx4 v[96:99], v117, s[28:31], s2 offen offset:0\nbuffer_load_dwordx4 v[100:103], v117, s[28:31], s2 offen offset:896\n"
"buffer_load_dwordx4 v[104:107], v117, s[36:39], 0 offen offset:0\nbuffer_load_dwordx4 v[108:111], v117, s[40:43], 0 offen offset:0\ns_add_u32 m0, s47, 32768\n"
"s_add_u32 s46, s51, 1048576\nD0 6,32,46,0\nD0 7,32,46,1024\nD0 8,32,46,2048\nD0 9,32,46,3072\ns_add_u32 m0, s47, -32768\ns_add_u32 s46, s52, 256\nD0 136,28,46,0\n"
"D0 137,28,46,1024\nD0 138,28,46,2048\nD0 139,28,46,3072\nWL 0\ns_mul_i32 s15, s15, 1664525\ns_add_u32 s15, s15, 1013904223\nv_mov_b32 v25, s15\ns_mov_b32 s57, 0x3db8aa3b\n"
"s_and_b32 s59, s11, 1\ns_lshl_b32 s59, s59, 13\ns_mul_i32 s2, s73, 25165824\ns_and_b32 s3, s12, 1\ns_lshl_b32 s3, s3, 21\ns_add_u32 s2, s2, s3\ns_lshl2_add_u32 s3, s13, s11\n"
"s_lshl_b32 s3, s3, 15\ns_add_u32 s2, s2, s3\ns_add_u32 s56, s2, 0x800000\ns_xor_b32 s53, s56, 0xc000\nWV 24\ns_barrier\nRa 192,140,0\nRa 196,141,0\nRa 200,140,2048\n"
"Ra 204,141,2048\nRa 208,142,0\nRa 212,143,0\nRa 216,142,2048\nRa 220,143,2048\nTa 160,128,0\nTa 162,128,2048\nTa 164,128,16384\nTa 166,128,18432\nTa 168,129,0\nTa 170,129,2048\n"
"Ta 172,129,16384\nTa 174,129,18432\nTa 176,130,0\nTa 178,130,2048\nTa 180,130,16384\nTa 182,130,18432\nTa 184,131,0\nTa 186,131,2048\nTa 188,131,16384\nTa 190,131,18432\nWL 0\n"
"Zvaa 32,160,192\nTa 224,132,0\ns_add_u32 m0, s47, 49152\ns_add_u32 s46, s51, 1310720\nMvaa 32,164,196\nTa 226,132,2048\nZaaa 96,164,204\nD0 6,32,46,0\nMaaa 96,160,200\n"
"Ta 228,132,16384\nZvaa 192,160,208\nTa 230,132,18432\nMvaa 192,164,212\nZvaa 224,164,220\nTa 232,133,0\nMvaa 224,160,216\nTa 234,133,2048\nD0 7,32,46,1024\nZvaa 228,168,216\n"
"Mvaa 228,172,220\nTa 236,133,16384\nZvaa 196,172,212\nTa 238,133,18432\nMvaa 196,168,208\nZaaa 100,168,200\nTa 240,134,0\nD0 8,32,46,2048\nMaaa 100,172,204\nTa 242,134,2048\n"
"Zvaa 36,172,196\nMvaa 36,168,192\nTa 244,134,16384\nZvaa 40,176,192\nTa 246,134,18432\nMvaa 40,180,196\nD0 9,32,46,3072\nZaaa 104,180,204\nTa 248,135,0\nMaaa 104,176,200\n"
"Ta 250,135,2048\nZvaa 200,176,208\ns_add_u32 m0, s47, -16384\ns_add_u32 s46, s52, 384\nMvaa 200,180,212\nTa 252,135,16384\nZvaa 232,180,220\nTa 254,135,18432\nD0 136,28,46,0\n"
"Mvaa 232,176,216\nZvaa 236,184,216\nMvaa 236,188,220\nZvaa 204,188,212\nMvaa 204,184,208\nD0 137,28,46,1024\nZaaa 108,184,200\nMaaa 108,188,204\nZvaa 44,188,196\nMvaa 44,184,192\n"
"s_waitcnt vmcnt(18) lgkmcnt(0)\ns_barrier\nZvaa 48,224,192\nRa 128,140,16384\nD0 138,28,46,2048\nMvaa 48,228,196\nRa 132,141,16384\nZaaa 112,228,204\nRa 136,140,18432\n"
"Maaa 112,224,200\nRa 140,141,18432\nZvaa 208,224,208\nRa 144,142,16384\nWV 15\nMvaa 208,228,212\nRa 148,143,16384\nD0 139,28,46,3072\nZvaa 240,228,220\nRa 152,142,18432\n"
"Mvaa 240,224,216\nRa 156,143,18432\nZvaa 244,232,216\nTa 160,12,0\ns_add_u32 m0, s47, 65536\ns_add_u32 s46, s51, 1572864\nMvaa 244,236,220\nTa 162,12,2048\nZvaa 212,236,212\n"
"Ta 164,12,16384\nD0 6,32,46,0\nMvaa 212,232,208\nTa 166,12,18432\nZaaa 116,232,200\nTa 168,13,0\nv_lshlrev_b32 v112, 16, v96\nMaaa 116,236,204\nTa 170,13,2048\nZvaa 52,236,196\n"
"Ta 172,13,16384\nMvaa 52,232,192\nTa 174,13,18432\nD0 7,32,46,1024\nZvaa 56,240,192\nTa 176,14,0\nv_lshlrev_b32 v113, 16, v100\nMvaa 56,244,196\nTa 178,14,2048\nZaaa 120,244,204\n"
"Ta 180,14,16384\nMaaa 120,240,200\nTa 182,14,18432\nZvaa 216,240,208\nTa 184,15,0\nD0 8,32,46,2048\nMvaa 216,244,212\nTa 186,15,2048\nZvaa 248,244,220\nTa 188,15,16384\n"
"Mvaa 248,240,216\nTa 190,15,18432\nZvaa 252,248,216\nv_lshlrev_b32 v114, 16, v104\nv_lshlrev_b32 v115, 16, v108\nMvaa 252,252,220\nD0 9,32,46,3072\nZvaa 220,252,212\n"
"MU 116,112,114\nMvaa 220,248,208\nMU 117,113,114\nZaaa 124,248,200\ns_add_u32 m0, s47, 81920\ns_add_u32 s46, s51, 1835008\nMaaa 124,252,204\nCV 116,116,117\nZvaa 60,252,196\n"
"D0 6,32,46,0\nMvaa 60,248,192\nWL 0\nMvaa 32,160,128\nTa 224,16,0\nv_lshlrev_b32 v118, 16, v116\nMvaa 32,164,132\nTa 226,16,2048\nMaaa 96,164,140\n"
"v_and_b32 v119, 0xffff0000, v116\nMaaa 96,160,136\nTa 228,16,16384\nD0 7,32,46,1024\nMvaa 192,160,144\nTa 230,16,18432\nMU 120,112,115\nMvaa 192,164,148\nMU 121,113,115\n"
"Mvaa 224,164,156\nTa 232,17,0\nMvaa 224,160,152\nTa 234,17,2048\nMvaa 228,168,152\nD0 8,32,46,2048\nv_and_b32 v112, 0xffff0000, v96\nMvaa 228,172,156\nTa 236,17,16384\n"
"Mvaa 196,172,148\nTa 238,17,18432\nMvaa 196,168,144\nv_and_b32 v113, 0xffff0000, v100\nMaaa 100,168,136\nTa 240,18,0\nv_and_b32 v114, 0xffff0000, v104\nMaaa 100,172,140\n"
"Ta 242,18,2048\nD0 9,32,46,3072\nMvaa 36,172,132\nv_and_b32 v115, 0xffff0000, v108\nMvaa 36,168,128\nTa 244,18,16384\nMvaa 40,176,128\nTa 246,18,18432\nMU 116,112,114\n"
"Mvaa 40,180,132\nMU 117,113,114\nMaaa 104,180,140\nTa 248,19,0\nMaaa 104,176,136\nTa 250,19,2048\nMvaa 200,176,144\nCV 116,116,117\nv_and_b32 v117, 0xffff0000, v116\n"
"Mvaa 200,180,148\nTa 252,19,16384\nMvaa 232,180,156\nTa 254,19,18432\nMvaa 232,176,152\nv_lshlrev_b32 v116, 16, v116\nMvaa 236,184,152\nFM 120,112,115\nFM 121,113,115\n"
"Mvaa 236,188,156\nAD 118,118,116\nMvaa 204,188,148\nAD 119,119,117\nMvaa 204,184,144\nv_lshlrev_b32 v112, 16, v97\nMaaa 108,184,136\nv_lshlrev_b32 v113, 16, v101\n"
"v_lshlrev_b32 v114, 16, v105\nMaaa 108,188,140\nv_lshlrev_b32 v115, 16, v109\nMvaa 44,188,132\nMU 116,112,114\nMvaa 44,184,128\ns_waitcnt vmcnt(12) lgkmcnt(0)\ns_barrier\n"
"Mvaa 48,224,128\nRa 192,140,32768\ns_add_u32 m0, s47, -49152\ns_add_u32 s46, s52, 512\nMvaa 48,228,132\nRa 196,141,32768\nMaaa 112,228,140\nRa 200,140,34816\nD0 136,28,46,0\n"
"Maaa 112,224,136\nRa 204,141,34816\nMvaa 208,224,144\nRa 208,142,32768\nMU 117,113,114\nMvaa 208,228,148\nRa 212,143,32768\nMvaa 240,228,156\nRa 216,142,34816\nMvaa 240,224,152\n"
"Ra 220,143,34816\nD0 137,28,46,1024\nMvaa 244,232,152\nTa 160,12,32768\nCV 116,116,117\nMvaa 244,236,156\nTa 162,12,34816\nMvaa 212,236,148\nTa 164,12,49152\nMvaa 212,232,144\n"
"Ta 166,12,51200\nMaaa 116,232,136\nTa 168,13,32768\nD0 138,28,46,2048\nMaaa 116,236,140\nTa 170,13,34816\nMvaa 52,236,132\nTa 172,13,49152\nMvaa 52,232,128\nTa 174,13,51200\n"
"Mvaa 56,240,128\nTa 176,14,32768\nv_and_b32 v117, 0xffff0000, v116\nMvaa 56,244,132\nTa 178,14,34816\nD0 139,28,46,3072\nMaaa 120,244,140\nTa 180,14,49152\nMaaa 120,240,136\n"
"Ta 182,14,51200\nMvaa 216,240,144\nTa 184,15,32768\ns_add_u32 m0, s47, 0\ns_add_u32 s46, s51, 2097152\nMvaa 216,244,148\nTa 186,15,34816\nMvaa 248,244,156\nTa 188,15,49152\n"
"D0 6,32,46,0\nMvaa 248,240,152\nTa 190,15,51200\nMvaa 252,248,152\nv_lshlrev_b32 v116, 16, v116\nFM 120,112,115\nMvaa 252,252,156\nFM 121,113,115\nMvaa 220,252,148\n"
"AD 118,118,116\nMvaa 220,248,144\nD0 7,32,46,1024\nMaaa 124,248,136\nAD 119,119,117\nv_and_b32 v112, 0xffff0000, v97\nMaaa 124,252,140\nv_and_b32 v113, 0xffff0000, v101\n"
"Mvaa 60,252,132\nv_and_b32 v114, 0xffff0000, v105\nMvaa 60,248,128\nWL 0\nMvaa 32,160,192\nTa 224,16,32768\nD0 8,32,46,2048\nMvaa 32,164,196\nTa 226,16,34816\nMaaa 96,164,204\n"
"v_and_b32 v115, 0xffff0000, v109\nMaaa 96,160,200\nTa 228,16,49152\nMvaa 192,160,208\nTa 230,16,51200\nMU 116,112,114\nMvaa 192,164,212\nD0 9,32,46,3072\nMvaa 224,164,220\n"
"Ta 232,17,32768\nMvaa 224,160,216\nTa 234,17,34816\nMvaa 228,168,216\ns_add_u32 m0, s47, 16384\ns_add_u32 s46, s51, 2359296\nMvaa 228,172,220\nTa 236,17,49152\nMvaa 196,172,212\n"
"Ta 238,17,51200\nD0 6,32,46,0\nMvaa 196,168,208\nMU 117,113,114\nMaaa 100,168,200\nTa 240,18,32768\nCV 116,116,117\nMaaa 100,172,204\nTa 242,18,34816\nMvaa 36,172,196\n"
"v_and_b32 v117, 0xffff0000, v116\nMvaa 36,168,192\nTa 244,18,49152\nD0 7,32,46,1024\nMvaa 40,176,192\nTa 246,18,51200\nv_lshlrev_b32 v116, 16, v116\nMvaa 40,180,196\n"
"FM 120,112,115\nMaaa 104,180,204\nTa 248,19,32768\nMaaa 104,176,200\nTa 250,19,34816\nMvaa 200,176,208\nD0 8,32,46,2048\nFM 121,113,115\nMvaa 200,180,212\nTa 252,19,49152\n"
"Mvaa 232,180,220\nTa 254,19,51200\nMvaa 232,176,216\nAD 118,118,116\nMvaa 236,184,216\nAD 119,119,117\nv_lshlrev_b32 v112, 16, v98\nMvaa 236,188,220\nD0 9,32,46,3072\n"
"Mvaa 204,188,212\nv_lshlrev_b32 v113, 16, v102\nMvaa 204,184,208\nv_lshlrev_b32 v114, 16, v106\nMaaa 108,184,200\nv_lshlrev_b32 v115, 16, v110\nMU 116,112,114\nMaaa 108,188,204\n"
"MU 117,113,114\nMvaa 44,188,196\nCV 116,116,117\nMvaa 44,184,192\ns_waitcnt vmcnt(12) lgkmcnt(0)\ns_barrier\nMvaa 48,224,192\nRa 128,140,49152\ns_add_u32 m0, s47, -32768\n"
"s_add_u32 s46, s52, 640\nMvaa 48,228,196\nRa 132,141,49152\nMaaa 112,228,204\nRa 136,140,51200\nD0 136,28,46,0\nMaaa 112,224,200\nRa 140,141,51200\nMvaa 208,224,208\n"
"Ra 144,142,49152\nv_and_b32 v117, 0xffff0000, v116\nMvaa 208,228,212\nRa 148,143,49152\nMvaa 240,228,220\nRa 152,142,51200\nMvaa 240,224,216\nRa 156,143,51200\nD0 137,28,46,1024\n"
"Mvaa 244,232,216\nTa 160,128,0\nv_lshlrev_b32 v116, 16, v116\nMvaa 244,236,220\nTa 162,128,2048\nMvaa 212,236,212\nTa 164,128,16384\nMvaa 212,232,208\nTa 166,128,18432\n"
"Maaa 116,232,200\nTa 168,129,0\nD0 138,28,46,2048\nMaaa 116,236,204\nTa 170,129,2048\nMvaa 52,236,196\nTa 172,129,16384\nMvaa 52,232,192\nTa 174,129,18432\nMvaa 56,240,192\n"
"Ta 176,130,0\nFM 120,112,115\nMvaa 56,244,196\nTa 178,130,2048\nD0 139,28,46,3072\nMaaa 120,244,204\nTa 180,130,16384\nMaaa 120,240,200\nTa 182,130,18432\nMvaa 216,240,208\n"
"Ta 184,131,0\ns_add_u32 m0, s47, 32768\ns_add_u32 s46, s51, 2621440\nMvaa 216,244,212\nTa 186,131,2048\nMvaa 248,244,220\nTa 188,131,16384\nD0 6,32,46,0\nMvaa 248,240,216\n"
"Ta 190,131,18432\nMvaa 252,248,216\nFM 121,113,115\nAD 118,118,116\nMvaa 252,252,220\nAD 119,119,117\nMvaa 220,252,212\nv_and_b32 v112, 0xffff0000, v98\nMvaa 220,248,208\n"
"D0 7,32,46,1024\nMaaa 124,248,200\nv_and_b32 v113, 0xffff0000, v102\nv_and_b32 v114, 0xffff0000, v106\nMaaa 124,252,204\nv_and_b32 v115, 0xffff0000, v110\nMvaa 60,252,196\n"
"MU 116,112,114\nMvaa 60,248,192\nWL 0\nMvaa 32,160,128\nTa 224,132,0\nD0 8,32,46,2048\nMvaa 32,164,132\nTa 226,132,2048\nMaaa 96,164,140\nMU 117,113,114\nMaaa 96,160,136\n"
"Ta 228,132,16384\nMvaa 192,160,144\nTa 230,132,18432\nCV 116,116,117\nMvaa 192,164,148\nD0 9,32,46,3072\nMvaa 224,164,156\nTa 232,133,0\nMvaa 224,160,152\nTa 234,133,2048\n"
"Mvaa 228,168,152\ns_add_u32 m0, s47, 49152\ns_add_u32 s46, s51, 2883584\nMvaa 228,172,156\nTa 236,133,16384\nMvaa 196,172,148\nTa 238,133,18432\nD0 6,32,46,0\nMvaa 196,168,144\n"
"v_and_b32 v117, 0xffff0000, v116\nMaaa 100,168,136\nTa 240,134,0\nv_lshlrev_b32 v116, 16, v116\nMaaa 100,172,140\nTa 242,134,2048\nMvaa 36,172,132\nFM 120,112,115\n"
"Mvaa 36,168,128\nTa 244,134,16384\nD0 7,32,46,1024\nMvaa 40,176,128\nTa 246,134,18432\nFM 121,113,115\nMvaa 40,180,132\nAD 118,118,116\nMaaa 104,180,140\nTa 248,135,0\n"
"Maaa 104,176,136\nTa 250,135,2048\nMvaa 200,176,144\nD0 8,32,46,2048\nAD 119,119,117\nMvaa 200,180,148\nTa 252,135,16384\nMvaa 232,180,156\nTa 254,135,18432\nMvaa 232,176,152\n"
"v_lshlrev_b32 v112, 16, v99\nMvaa 236,184,152\nv_lshlrev_b32 v113, 16, v103\nv_lshlrev_b32 v114, 16, v107\nMvaa 236,188,156\nD0 9,32,46,3072\nMvaa 204,188,148\n"
"v_lshlrev_b32 v115, 16, v111\nMvaa 204,184,144\nMU 116,112,114\nMaaa 108,184,136\nMU 117,113,114\nCV 116,116,117\ns_and_b32 s78, s74, 3\ns_lshl2_add_u32 s78, s73, s78\n"
"s_lshl_b32 s78, s78, 12\ns_bitcmp1_b32 s11, 0\ns_cselect_b32 s79, s48, s45\ns_add_u32 s79, s79, 512\ns_and_b32 s79, s79, 0xfffe0000\ns_add_u32 s78, s78, s79\n"
"buffer_load_dword v31, v26, s[20:23], s78 offen\nMaaa 108,188,140\nv_and_b32 v117, 0xffff0000, v116\nMvaa 44,188,132\nv_lshlrev_b32 v116, 16, v116\nMvaa 44,184,128\n"
"s_waitcnt vmcnt(13) lgkmcnt(0)\ns_barrier\nMvaa 48,224,128\nRa 192,140,16384\ns_add_u32 m0, s47, -16384\ns_add_u32 s46, s52, 768\nMvaa 48,228,132\nRa 196,141,16384\n"
"Maaa 112,228,140\nRa 200,140,18432\nD0 136,28,46,0\nMaaa 112,224,136\nRa 204,141,18432\nMvaa 208,224,144\nRa 208,142,16384\nFM 120,112,115\nMvaa 208,228,148\nRa 212,143,16384\n"
"Mvaa 240,228,156\nRa 216,142,18432\nMvaa 240,224,152\nRa 220,143,18432\nD0 137,28,46,1024\nMvaa 244,232,152\nTa 160,12,0\nFM 121,113,115\nMvaa 244,236,156\nTa 162,12,2048\n"
"Mvaa 212,236,148\nTa 164,12,16384\nMvaa 212,232,144\nTa 166,12,18432\nMaaa 116,232,136\nTa 168,13,0\nD0 138,28,46,2048\nMaaa 116,236,140\nTa 170,13,2048\nMvaa 52,236,132\n"
"Ta 172,13,16384\nMvaa 52,232,128\nTa 174,13,18432\nMvaa 56,240,128\nTa 176,14,0\nAD 118,118,116\nMvaa 56,244,132\nTa 178,14,2048\nD0 139,28,46,3072\nMaaa 120,244,140\n"
"Ta 180,14,16384\nMaaa 120,240,136\nTa 182,14,18432\nMvaa 216,240,144\nTa 184,15,0\ns_add_u32 m0, s47, 65536\ns_add_u32 s46, s51, 3145728\nMvaa 216,244,148\nTa 186,15,2048\n"
"Mvaa 248,244,156\nTa 188,15,16384\nD0 6,32,46,0\nMvaa 248,240,152\nTa 190,15,18432\nMvaa 252,248,152\nAD 119,119,117\nv_and_b32 v112, 0xffff0000, v99\nMvaa 252,252,156\n"
"v_and_b32 v113, 0xffff0000, v103\nMvaa 220,252,148\nv_and_b32 v114, 0xffff0000, v107\nMvaa 220,248,144\nD0 7,32,46,1024\nMaaa 124,248,136\nv_and_b32 v115, 0xffff0000, v111\n"
"MU 116,112,114\nMaaa 124,252,140\nMU 117,113,114\nMvaa 60,252,132\nCV 116,116,117\nMvaa 60,248,128\nWL 0\nMvaa 32,160,192\nTa 224,16,0\nD0 8,32,46,2048\nMvaa 32,164,196\n"
"Ta 226,16,2048\nMaaa 96,164,204\nv_and_b32 v117, 0xffff0000, v116\nMaaa 96,160,200\nTa 228,16,16384\nMvaa 192,160,208\nTa 230,16,18432\nv_lshlrev_b32 v116, 16, v116\n"
"Mvaa 192,164,212\nD0 9,32,46,3072\nMvaa 224,164,220\nTa 232,17,0\nMvaa 224,160,216\nTa 234,17,2048\nMvaa 228,168,216\ns_add_u32 m0, s47, 81920\ns_add_u32 s46, s51, 3407872\n"
"Mvaa 228,172,220\nTa 236,17,16384\nMvaa 196,172,212\nTa 238,17,18432\nD0 6,32,46,0\nMvaa 196,168,208\nFM 120,112,115\nMaaa 100,168,200\nTa 240,18,0\nFM 121,113,115\n"
"Maaa 100,172,204\nTa 242,18,2048\nMvaa 36,172,196\nAD 118,118,116\nMvaa 36,168,192\nTa 244,18,16384\nD0 7,32,46,1024\nMvaa 40,176,192\nTa 246,18,18432\nAD 119,119,117\n"
"Mvaa 40,180,196\ns_nop 0\nv_permlane32_swap_b32 v118, v119\nMaaa 104,180,204\nTa 248,19,0\nMaaa 104,176,200\nTa 250,19,2048\nMvaa 200,176,208\nD0 8,32,46,2048\n"
"v_permlane32_swap_b32 v120, v121\nMvaa 200,180,212\nTa 252,19,16384\nMvaa 232,180,220\nTa 254,19,18432\nMvaa 232,176,216\nAD 118,118,119\nMvaa 236,184,216\nAD 120,120,121\n"
"Mvaa 236,188,220\nD0 9,32,46,3072\nMvaa 204,188,212\nv_permlane16_swap_b32 v118, v120\nMvaa 204,184,208\nAD 118,118,120\nMaaa 108,184,200\nMaaa 108,188,204\n"
"v_add_f32_dpp v118, v118, v118 row_ror:8 row_mask:0xf bank_mask:0xf\nMvaa 44,188,196\nMvaa 44,184,192\ns_waitcnt vmcnt(13) lgkmcnt(0)\ns_barrier\nMvaa 48,224,192\n"
"Ra 128,140,32768\ns_add_u32 m0, s47, -65536\ns_add_u32 s45, s45, 128\nMvaa 48,228,196\nRa 132,141,32768\nMaaa 112,228,204\nRa 136,140,34816\nD0 2,20,45,0\nMaaa 112,224,200\n"
"Ra 140,141,34816\nMvaa 208,224,208\nRa 144,142,32768\nv_add_f32_dpp v118, v118, v118 row_ror:4 row_mask:0xf bank_mask:0xf\nMvaa 208,228,212\nRa 148,143,32768\nMvaa 240,228,220\n"
"Ra 152,142,34816\nMvaa 240,224,216\nRa 156,143,34816\nD0 3,20,45,1024\nMvaa 244,232,216\nTa 160,12,32768\nv_add_f32_dpp v118, v118, v118 row_ror:2 row_mask:0xf bank_mask:0xf\n"
"Mvaa 244,236,220\nTa 162,12,34816\nMvaa 212,236,212\nTa 164,12,49152\nMvaa 212,232,208\nTa 166,12,51200\nMaaa 116,232,200\nTa 168,13,32768\nD0 4,20,45,2048\nMaaa 116,236,204\n"
"Ta 170,13,34816\nMvaa 52,236,196\nTa 172,13,49152\nMvaa 52,232,192\nTa 174,13,51200\nMvaa 56,240,192\nTa 176,14,32768\n"
"v_add_f32_dpp v118, v118, v118 row_ror:1 row_mask:0xf bank_mask:0xf\nMvaa 56,244,196\nTa 178,14,34816\nD0 5,20,45,3072\nMaaa 120,244,204\nTa 180,14,49152\nMaaa 120,240,200\n"
"Ta 182,14,51200\nMvaa 216,240,208\nTa 184,15,32768\ns_add_u32 m0, s47, 0\ns_add_u32 s44, s44, 0x40000\nMvaa 216,244,212\nTa 186,15,34816\nMvaa 248,244,220\nTa 188,15,49152\n"
"D0 6,24,44,0\nMvaa 248,240,216\nTa 190,15,51200\nMvaa 252,248,216\nCV 118,118,118\nv_lshlrev_b32 v118, 16, v118\nMvaa 252,252,220\nv_mul_f32 v119, 0xbfb8aa3b, v118\nEX 119,119\n"
"Mvaa 220,252,212\nv_add_f32 v120, 0, v118\nMvaa 220,248,208\nD0 7,24,44,1024\nMaaa 124,248,200\nv_add_f32 v119, 1.0, v119\nMaaa 124,252,204\nv_rcp_f32 v119, v119\nMvaa 60,252,196\n"
"v_ashrrev_i32 v121, 31, v120\nMvaa 60,248,192\nWL 0\nMvaa 32,160,128\nTa 224,16,32768\nD0 8,24,44,2048\nMvaa 32,164,132\nTa 226,16,34816\nMaaa 96,164,140\nCV 119,119,119\n"
"Maaa 96,160,136\nTa 228,16,49152\nMvaa 192,160,144\nTa 230,16,51200\nv_lshlrev_b32 v119, 16, v119\nMvaa 192,164,148\nD0 9,24,44,3072\nMvaa 224,164,156\nTa 232,17,32768\n"
"Mvaa 224,160,152\nTa 234,17,34816\nMvaa 228,168,152\ns_add_u32 m0, s47, 16384\ns_add_u32 s44, s44, 0x40000\nMvaa 228,172,156\nTa 236,17,49152\nMvaa 196,172,148\nTa 238,17,51200\n"
"D0 6,24,44,0\nMvaa 196,168,144\nv_or_b32 v121, 0x80000000, v121\nMaaa 100,168,136\nTa 240,18,32768\nv_xor_b32 v120, v120, v121\nMaaa 100,172,140\nTa 242,18,34816\nMvaa 36,172,132\n"
"v_and_b32 v120, 0xffff0000, v120\nMvaa 36,168,128\nTa 244,18,49152\nD0 7,24,44,1024\nMvaa 40,176,128\nTa 246,18,51200\ns_lshl2_add_u32 s2, s10, s11\nMvaa 40,180,132\n"
"v_lshrrev_b32 v112, 5, v0\nMaaa 104,180,140\nTa 248,19,32768\nMaaa 104,176,136\nTa 250,19,34816\nMvaa 200,176,144\nD0 8,24,44,2048\nv_lshl_add_u32 v112, s2, 1, v112\n"
"Mvaa 200,180,148\nTa 252,19,49152\nMvaa 232,180,156\nTa 254,19,51200\nMvaa 232,176,152\nv_sub_u32 v113, 0xffff, v112\nMvaa 236,184,152\nv_or_b32 v120, v120, v113\n"
"v_mov_b32 v121, s15\nMvaa 236,188,156\nD0 9,24,44,3072\nMvaa 204,188,148\nv_and_b32 v113, 31, v0\nMvaa 204,184,144\nv_mov_b32 v116, 0x40000000\nv_cmp_eq_u32 vcc, 16, v113\n"
"v_lshlrev_b32 v114, 2, v112\nv_lshlrev_b32 v115, 10, v113\nv_cndmask_b32 v114, v116, v114, vcc\nv_cndmask_b32 v120, v119, v120, vcc\nMaaa 108,184,136\n"
"v_lshl_add_u32 v115, v112, 3, v115\nv_and_b32 v113, 15, v0\nMaaa 108,188,140\nv_cmp_eq_u32 vcc, 0, v113\ns_mov_b32 s2, 0xc040000\ns_mov_b32 s3, 0xc03e000\n"
"v_cndmask_b32 v115, v116, v115, vcc\nbuffer_store_dwordx2 v[120:121], v115, s[16:19], s2 offen sc0 sc1\nbuffer_store_dword v118, v114, s[16:19], s3 offen sc0 sc1\nMvaa 44,188,132\n"
"Mvaa 44,184,128\ns_waitcnt vmcnt(14) lgkmcnt(0)\ns_barrier\nMvaa 48,224,128\nRa 192,140,49152\ns_add_u32 m0, s47, -49152\ns_add_u32 s48, s48, 128\nMvaa 48,228,132\n"
"Ra 196,141,49152\nMaaa 112,228,140\nRa 200,140,51200\nD0 2,20,48,0\nMaaa 112,224,136\nRa 204,141,51200\nMvaa 208,224,144\nRa 208,142,49152\nMvaa 208,228,148\nRa 212,143,49152\n"
"Mvaa 240,228,156\nRa 216,142,51200\nMvaa 240,224,152\nRa 220,143,51200\nD0 3,20,48,1024\nMvaa 244,232,152\nTa 160,128,0\nMvaa 244,236,156\nTa 162,128,2048\nMvaa 212,236,148\n"
"Ta 164,128,16384\nMvaa 212,232,144\nTa 166,128,18432\nMaaa 116,232,136\nTa 168,129,0\nD0 4,20,48,2048\nMaaa 116,236,140\nTa 170,129,2048\nMvaa 52,236,132\nTa 172,129,16384\n"
"Mvaa 52,232,128\nTa 174,129,18432\nMvaa 56,240,128\nTa 176,130,0\nMvaa 56,244,132\nTa 178,130,2048\nD0 5,20,48,3072\nMaaa 120,244,140\nTa 180,130,16384\nMaaa 120,240,136\n"
"Ta 182,130,18432\nMvaa 216,240,144\nTa 184,131,0\ns_add_u32 m0, s47, -32768\ns_add_u32 s45, s45, 128\nMvaa 216,244,148\nTa 186,131,2048\nMvaa 248,244,156\nTa 188,131,16384\n"
"D0 2,20,45,0\nMvaa 248,240,152\nTa 190,131,18432\nMvaa 252,248,152\nMvaa 252,252,156\nMvaa 220,252,148\nMvaa 220,248,144\nD0 3,20,45,1024\nMaaa 124,248,136\nMaaa 124,252,140\n"
"Mvaa 60,252,132\nMvaa 60,248,128\nWL 0\nMvaa 32,160,192\nTa 224,132,0\nD0 4,20,45,2048\nMvaa 32,164,196\nTa 226,132,2048\nMvaa 36,172,196\nMvaa 36,168,192\nTa 228,132,16384\n"
"Mvaa 40,176,192\nTa 230,132,18432\nMvaa 40,180,196\nD0 5,20,45,3072\nMvaa 44,188,196\nTa 232,133,0\nMvaa 44,184,192\nTa 234,133,2048\nMaaa 108,184,200\ns_add_u32 m0, s47, 32768\n"
"s_add_u32 s44, s44, 0x40000\nMaaa 108,188,204\nTa 236,133,16384\nMaaa 104,180,204\nTa 238,133,18432\nD0 6,24,44,0\nMaaa 104,176,200\nv_mul_f32 v32, s57, v32\n"
"v_mul_f32 v33, s57, v33\nMaaa 100,168,200\nTa 240,134,0\nCV 32,32,33\nMaaa 100,172,204\nTa 242,134,2048\nv_mul_f32 v34, s57, v34\nMaaa 96,164,204\nv_mul_f32 v35, s57, v35\n"
"CV 33,34,35\nMaaa 96,160,200\nTa 244,134,16384\nD0 7,24,44,1024\nMvaa 192,160,208\nTa 246,134,18432\nv_mul_f32 v36, s57, v36\nMvaa 192,164,212\nv_mul_f32 v37, s57, v37\n"
"Mvaa 196,172,212\nTa 248,135,0\nCV 34,36,37\nMvaa 196,168,208\nTa 250,135,2048\nv_mul_f32 v38, s57, v38\nMvaa 200,176,208\nD0 8,24,44,2048\nv_mul_f32 v39, s57, v39\n"
"Mvaa 200,180,212\nTa 252,135,16384\nCV 35,38,39\nMvaa 204,188,212\nTa 254,135,18432\nv_mul_f32 v40, s57, v40\nMvaa 204,184,208\nv_mul_f32 v41, s57, v41\nCV 40,40,41\n"
"Mvaa 236,184,216\nv_mul_f32 v42, s57, v42\nv_mul_f32 v43, s57, v43\nMvaa 236,188,220\nD0 9,24,44,3072\nMvaa 232,180,220\nCV 41,42,43\nv_mul_f32 v44, s57, v44\nMvaa 232,176,216\n"
"v_mul_f32 v45, s57, v45\nCV 42,44,45\nMvaa 228,168,216\nv_mul_f32 v46, s57, v46\nv_mul_f32 v47, s57, v47\nMvaa 228,172,220\nCV 43,46,47\nAR 20,96\nMvaa 224,164,220\nAR 21,97\n"
"v_mul_f32 v20, s57, v20\nMvaa 224,160,216\ns_waitcnt vmcnt(14) lgkmcnt(0)\ns_barrier\nMvaa 48,224,192\ns_add_u32 m0, s47, 49152\ns_add_u32 s44, s44, 0x40000\nRa 128,10,0\n"
"Mvaa 48,228,196\nRa 132,11,0\nMvaa 52,236,196\nD0 6,24,44,0\nRa 136,10,2048\nMvaa 52,232,192\nRa 140,11,2048\nv_mul_f32 v21, s57, v21\nMvaa 56,240,192\nRa 144,10,8192\n"
"CV 36,20,21\nMvaa 56,244,196\nRa 148,11,8192\nAR 20,98\nMvaa 60,252,196\nD0 7,24,44,1024\nRa 152,10,10240\nMvaa 60,248,192\nRa 156,11,10240\nAR 21,99\nMaaa 124,248,200\n"
"Ta 160,12,0\nv_mul_f32 v20, s57, v20\nMaaa 124,252,204\nTa 162,12,2048\nMaaa 120,244,204\nD0 8,24,44,2048\nTa 164,12,16384\nMaaa 120,240,200\nTa 166,12,18432\n"
"v_mul_f32 v21, s57, v21\nMaaa 116,232,200\nTa 168,13,0\nCV 37,20,21\nMaaa 116,236,204\nTa 170,13,2048\nAR 20,100\nMaaa 112,228,204\nD0 9,24,44,3072\nTa 172,13,16384\n"
"Maaa 112,224,200\nTa 174,13,18432\nAR 21,101\nMvaa 208,224,208\ns_add_u32 m0, s47, -16384\ns_add_u32 s48, s48, 128\nTa 176,14,0\nMvaa 208,228,212\nTa 178,14,2048\n"
"Mvaa 212,236,212\nD0 2,20,48,0\nTa 180,14,16384\nMvaa 212,232,208\nTa 182,14,18432\nv_mul_f32 v20, s57, v20\nMvaa 216,240,208\nTa 184,15,0\nv_mul_f32 v21, s57, v21\n"
"Mvaa 216,244,212\nTa 186,15,2048\nCV 38,20,21\nMvaa 220,252,212\nD0 3,20,48,1024\nTa 188,15,16384\nMvaa 220,248,208\nTa 190,15,18432\nAR 20,102\nMvaa 252,248,216\nAR 21,103\n"
"v_mul_f32 v20, s57, v20\nMvaa 252,252,220\nv_mul_f32 v21, s57, v21\nMvaa 248,244,220\nD0 4,20,48,2048\nCV 39,20,21\nMvaa 248,240,216\nAR 20,104\nAR 21,105\nMvaa 244,232,216\n"
"v_mul_f32 v20, s57, v20\nv_mul_f32 v21, s57, v21\nMvaa 244,236,220\nCV 44,20,21\nAR 20,106\nMvaa 240,228,220\nD0 5,20,48,3072\nAR 21,107\nMvaa 240,224,216\nWL 0\ns_barrier\n"
"Zvaa 64,128,160\nTa 224,16,0\nv_mul_f32 v20, s57, v20\nMvaa 64,132,164\nTa 226,16,2048\nZvaa 68,140,164\ns_add_u32 m0, s47, -65536\ns_add_u32 s45, s45, 128\nMvaa 68,136,160\n"
"Ta 228,16,16384\nv_mul_f32 v21, s57, v21\nZvaa 128,144,160\nTa 230,16,18432\nCV 45,20,21\nMvaa 128,148,164\nD0 2,20,45,0\nAR 20,108\nZvaa 132,156,164\nTa 232,17,0\nAR 21,109\n"
"Mvaa 132,152,160\nTa 234,17,2048\nv_mul_f32 v20, s57, v20\nZvaa 140,152,168\nv_mul_f32 v21, s57, v21\nCV 46,20,21\nMvaa 140,156,172\nTa 236,17,16384\ns_add_u32 s6, s56, 0\n"
"Sv 32,1,16,6,0\nZvaa 136,148,172\nTa 238,17,18432\nAR 20,110\nMvaa 136,144,168\nAR 21,111\nv_mul_f32 v20, s57, v20\nZvaa 76,136,168\nTa 240,18,0\nv_mul_f32 v21, s57, v21\n"
"Mvaa 76,140,172\nTa 242,18,2048\nD0 3,20,45,1024\nZvaa 72,132,172\nCV 47,20,21\nv_mul_f32 v48, s57, v48\nMvaa 72,128,168\nTa 244,18,16384\nv_mul_f32 v49, s57, v49\n"
"Zvaa 80,128,176\nTa 246,18,18432\nCV 48,48,49\nMvaa 80,132,180\nv_mul_f32 v50, s57, v50\nZvaa 84,140,180\nTa 248,19,0\nv_mul_f32 v51, s57, v51\nMvaa 84,136,176\nTa 250,19,2048\n"
"CV 49,50,51\nZvaa 144,144,176\nv_mul_f32 v52, s57, v52\nv_mul_f32 v53, s57, v53\nMvaa 144,148,180\nTa 252,19,16384\nD0 4,20,45,2048\nZvaa 148,156,180\nTa 254,19,18432\n"
"CV 50,52,53\nMvaa 148,152,176\nv_mul_f32 v54, s57, v54\nv_mul_f32 v55, s57, v55\nZvaa 156,152,184\nCV 51,54,55\nv_mul_f32 v56, s57, v56\nMvaa 156,156,188\nv_mul_f32 v57, s57, v57\n"
"s_add_u32 s6, s56, 0\nSv 40,1,16,6,2048\nZvaa 152,148,188\nCV 56,56,57\nv_mul_f32 v58, s57, v58\nMvaa 152,144,184\nv_mul_f32 v59, s57, v59\nCV 57,58,59\nZvaa 92,136,184\n"
"v_mul_f32 v60, s57, v60\nv_mul_f32 v61, s57, v61\nMvaa 92,140,188\nD0 5,20,45,3072\nCV 58,60,61\nZvaa 88,132,188\nv_mul_f32 v62, s57, v62\nv_mul_f32 v63, s57, v63\n"
"Mvaa 88,128,184\ns_waitcnt vmcnt(18) lgkmcnt(0)\ns_barrier\nZvaa 96,128,224\nRa 192,10,16384\nCV 59,62,63\nMvaa 96,132,228\nAR 20,112\nZvaa 100,140,228\nRa 196,11,16384\n"
"s_add_u32 m0, s47, 0\ns_add_u32 s44, s44, 0x40000\nMvaa 100,136,224\nAR 21,113\nv_mul_f32 v20, s57, v20\nZvaa 160,144,224\nRa 200,10,18432\nv_mul_f32 v21, s57, v21\n"
"Mvaa 160,148,228\nD0 6,24,44,0\nCV 52,20,21\nZvaa 164,156,228\nRa 204,11,18432\nAR 20,114\nMvaa 164,152,224\nAR 21,115\nv_mul_f32 v20, s57, v20\nZvaa 172,152,232\nRa 208,10,24576\n"
"v_mul_f32 v21, s57, v21\nMvaa 172,156,236\nCV 53,20,21\ns_add_u32 s6, s56, 0\nSv 36,1,16,6,1024\nZvaa 168,148,236\nRa 212,11,24576\nAR 20,116\nMvaa 168,144,232\nAR 21,117\n"
"v_mul_f32 v20, s57, v20\nZvaa 108,136,232\nRa 216,10,26624\nv_mul_f32 v21, s57, v21\nMvaa 108,140,236\nD0 7,24,44,1024\nCV 54,20,21\nZvaa 104,132,236\nRa 220,11,26624\nAR 20,118\n"
"Mvaa 104,128,232\nAR 21,119\nv_mul_f32 v20, s57, v20\nZvaa 112,128,240\nv_mul_f32 v21, s57, v21\nCV 55,20,21\nMvaa 112,132,244\nAR 20,120\nZvaa 116,140,244\nAR 21,121\n"
"v_mul_f32 v20, s57, v20\nMvaa 116,136,240\nv_mul_f32 v21, s57, v21\nCV 60,20,21\nZvaa 176,144,240\nAR 20,122\nAR 21,123\nMvaa 176,148,244\nD0 8,24,44,2048\n"
"v_mul_f32 v20, s57, v20\nZvaa 180,156,244\nv_mul_f32 v21, s57, v21\nCV 61,20,21\nMvaa 180,152,240\nAR 20,124\nAR 21,125\nZvaa 188,152,248\nv_mul_f32 v20, s57, v20\n"
"v_mul_f32 v21, s57, v21\nMvaa 188,156,252\nCV 62,20,21\ns_add_u32 s6, s56, 0\nSv 44,1,16,6,3072\nZvaa 184,148,252\nAR 20,126\nAR 21,127\nMvaa 184,144,248\nv_mul_f32 v20, s57, v20\n"
"v_mul_f32 v21, s57, v21\nZvaa 124,136,248\nCV 63,20,21\nv_mul_f32 v192, s57, v192\nMvaa 124,140,252\nD0 9,24,44,3072\nv_mul_f32 v193, s57, v193\nZvaa 120,132,252\nCV 192,192,193\n"
"v_mul_f32 v194, s57, v194\nMvaa 120,128,248\nWL 0\nZaaa 32,192,224\nRa 128,10,32768\nv_mul_f32 v195, s57, v195\nMaaa 32,196,228\nCV 193,194,195\nv_mul_f32 v196, s57, v196\n"
"Zaaa 36,204,228\nRa 132,11,32768\ns_add_u32 m0, s47, 16384\ns_add_u32 s44, s44, 0x40000\nMaaa 36,200,224\nv_mul_f32 v197, s57, v197\nCV 194,196,197\nZaaa 96,208,224\n"
"Ra 136,10,34816\nv_mul_f32 v198, s57, v198\nMaaa 96,212,228\nD0 6,24,44,0\nv_mul_f32 v199, s57, v199\nZaaa 100,220,228\nRa 140,11,34816\nCV 195,198,199\nMaaa 100,216,224\n"
"v_mul_f32 v200, s57, v200\nv_mul_f32 v201, s57, v201\nZaaa 108,216,232\nRa 144,10,40960\nCV 200,200,201\nMaaa 108,220,236\nv_mul_f32 v202, s57, v202\nv_mul_f32 v203, s57, v203\n"
"s_add_u32 s6, s56, 4096\nSv 48,1,16,6,0\nZaaa 104,212,236\nRa 148,11,40960\nCV 201,202,203\nMaaa 104,208,232\nv_mul_f32 v204, s57, v204\nv_mul_f32 v205, s57, v205\n"
"Zaaa 44,200,232\nRa 152,10,43008\nCV 202,204,205\nMaaa 44,204,236\nD0 7,24,44,1024\nv_mul_f32 v206, s57, v206\nZaaa 40,196,236\nRa 156,11,43008\nv_mul_f32 v207, s57, v207\n"
"Maaa 40,192,232\nCV 203,206,207\nv_mul_f32 v208, s57, v208\nZaaa 48,192,240\nv_mul_f32 v209, s57, v209\nCV 208,208,209\nMaaa 48,196,244\nv_mul_f32 v210, s57, v210\n"
"v_mul_f32 v211, s57, v211\nZaaa 52,204,244\nCV 209,210,211\nv_mul_f32 v212, s57, v212\nMaaa 52,200,240\nv_mul_f32 v213, s57, v213\nCV 210,212,213\nZaaa 112,208,240\n"
"v_mul_f32 v214, s57, v214\nv_mul_f32 v215, s57, v215\nMaaa 112,212,244\nD0 8,24,44,2048\nCV 211,214,215\nZaaa 116,220,244\nv_mul_f32 v216, s57, v216\nv_mul_f32 v217, s57, v217\n"
"Maaa 116,216,240\nCV 216,216,217\nv_mul_f32 v218, s57, v218\nZaaa 124,216,248\nv_mul_f32 v219, s57, v219\nCV 217,218,219\nMaaa 124,220,252\nv_mul_f32 v220, s57, v220\n"
"v_mul_f32 v221, s57, v221\ns_add_u32 s6, s56, 4096\nSv 56,1,16,6,2048\nZaaa 120,212,252\nCV 218,220,221\nv_mul_f32 v222, s57, v222\nMaaa 120,208,248\nv_mul_f32 v223, s57, v223\n"
"CV 219,222,223\nZaaa 60,200,248\nv_mul_f32 v224, s57, v224\nv_mul_f32 v225, s57, v225\nMaaa 60,204,252\nD0 9,24,44,3072\nCV 224,224,225\nZaaa 56,196,252\n"
"v_mul_f32 v226, s57, v226\nv_mul_f32 v227, s57, v227\nMaaa 56,192,248\ns_waitcnt vmcnt(22) lgkmcnt(0)\ns_barrier\nZaaa 0,192,160\nTa 224,16,32768\nCV 225,226,227\nMaaa 0,196,164\n"
"Ta 226,16,34816\nv_mul_f32 v228, s57, v228\nZaaa 4,204,164\ns_add_u32 m0, s47, -49152\ns_add_u32 s48, s48, 128\nMaaa 4,200,160\nTa 228,16,49152\nv_mul_f32 v229, s57, v229\n"
"Zaaa 64,208,160\nTa 230,16,51200\nCV 226,228,229\nMaaa 64,212,164\nD0 2,20,48,0\nv_mul_f32 v230, s57, v230\nZaaa 68,220,164\nTa 232,17,32768\nv_mul_f32 v231, s57, v231\n"
"Maaa 68,216,160\nTa 234,17,34816\nCV 227,230,231\nZaaa 76,216,168\nv_mul_f32 v232, s57, v232\nv_mul_f32 v233, s57, v233\nMaaa 76,220,172\nTa 236,17,49152\nCV 232,232,233\n"
"s_add_u32 s6, s56, 4096\nSv 52,1,16,6,1024\nZaaa 72,212,172\nTa 238,17,51200\nv_mul_f32 v234, s57, v234\nMaaa 72,208,168\nv_mul_f32 v235, s57, v235\nCV 233,234,235\n"
"Zaaa 12,200,168\nTa 240,18,32768\nv_mul_f32 v236, s57, v236\nMaaa 12,204,172\nTa 242,18,34816\nD0 3,20,48,1024\nZaaa 8,196,172\nv_mul_f32 v237, s57, v237\nCV 234,236,237\n"
"Maaa 8,192,168\nTa 244,18,49152\nv_mul_f32 v238, s57, v238\nZaaa 16,192,176\nTa 246,18,51200\nv_mul_f32 v239, s57, v239\nMaaa 16,196,180\nCV 235,238,239\n"
"v_mul_f32 v240, s57, v240\nZaaa 20,204,180\nTa 248,19,32768\nv_mul_f32 v241, s57, v241\nMaaa 20,200,176\nTa 250,19,34816\nCV 240,240,241\nZaaa 80,208,176\n"
"v_mul_f32 v242, s57, v242\nv_mul_f32 v243, s57, v243\nMaaa 80,212,180\nTa 252,19,49152\nD0 4,20,48,2048\nZaaa 84,220,180\nTa 254,19,51200\nCV 241,242,243\nMaaa 84,216,176\n"
"v_mul_f32 v244, s57, v244\nv_mul_f32 v245, s57, v245\nZaaa 92,216,184\nCV 242,244,245\nv_mul_f32 v246, s57, v246\nMaaa 92,220,188\nv_mul_f32 v247, s57, v247\nCV 243,246,247\n"
"s_add_u32 s6, s56, 4096\nSv 60,1,16,6,3072\nZaaa 88,212,188\nv_mul_f32 v248, s57, v248\nv_mul_f32 v249, s57, v249\nMaaa 88,208,184\nCV 248,248,249\nv_mul_f32 v250, s57, v250\n"
"Zaaa 28,200,184\nv_mul_f32 v251, s57, v251\nCV 249,250,251\nMaaa 28,204,188\nD0 5,20,48,3072\nv_mul_f32 v252, s57, v252\nZaaa 24,196,188\nv_mul_f32 v253, s57, v253\n"
"CV 250,252,253\nMaaa 24,192,184\nWL 0\nMvaa 96,128,224\nTa 160,12,32768\nv_mul_f32 v254, s57, v254\nMvaa 96,132,228\nTa 162,12,34816\nv_mul_f32 v255, s57, v255\nMvaa 100,140,228\n"
"s_add_u32 m0, s47, -32768\ns_add_u32 s45, s45, 128\nMvaa 100,136,224\nTa 164,12,49152\nCV 251,254,255\nMvaa 160,144,224\nTa 166,12,51200\nMvaa 160,148,228\nD0 2,20,45,0\n"
"Mvaa 164,156,228\nTa 168,13,32768\nMvaa 164,152,224\nTa 170,13,34816\nMvaa 172,152,232\nMvaa 172,156,236\nTa 172,13,49152\ns_add_u32 s6, s53, 0\nSv 192,1,16,6,0\nMvaa 168,148,236\n"
"Ta 174,13,51200\nMvaa 168,144,232\nMvaa 108,136,232\nTa 176,14,32768\nMvaa 108,140,236\nTa 178,14,34816\nD0 3,20,45,1024\nMvaa 104,132,236\nMvaa 104,128,232\nTa 180,14,49152\n"
"Mvaa 112,128,240\nTa 182,14,51200\nMvaa 112,132,244\nMvaa 116,140,244\nTa 184,15,32768\nMvaa 116,136,240\nTa 186,15,34816\nMvaa 176,144,240\nMvaa 176,148,244\nTa 188,15,49152\n"
"D0 4,20,45,2048\nMvaa 180,156,244\nTa 190,15,51200\nMvaa 180,152,240\nMvaa 188,152,248\nMvaa 188,156,252\ns_add_u32 s6, s53, 0\nSv 200,1,16,6,2048\nMvaa 184,148,252\n"
"Mvaa 184,144,248\nMvaa 124,136,248\nMvaa 124,140,252\nD0 5,20,45,3072\nMvaa 120,132,252\nMvaa 120,128,248\ns_waitcnt vmcnt(24) lgkmcnt(0)\ns_barrier\nMvaa 64,128,160\n"
"Ra 192,10,49152\nMvaa 64,132,164\nMvaa 68,140,164\nRa 196,11,49152\ns_add_u32 m0, s47, 32768\ns_add_u32 s44, s44, 0x40000\nMvaa 68,136,160\nMvaa 128,144,160\nRa 200,10,51200\n"
"Mvaa 128,148,164\nD0 6,24,44,0\nMvaa 132,156,164\nRa 204,11,51200\nMvaa 132,152,160\nMvaa 140,152,168\nRa 208,10,57344\nMvaa 140,156,172\ns_add_u32 s6, s53, 4096\nSv 208,1,16,6,0\n"
"Mvaa 136,148,172\nRa 212,11,57344\nMvaa 136,144,168\nMvaa 76,136,168\nRa 216,10,59392\nMvaa 76,140,172\nD0 7,24,44,1024\nMvaa 72,132,172\nRa 220,11,59392\nMvaa 72,128,168\n"
"Mvaa 80,128,176\nMvaa 80,132,180\nMvaa 84,140,180\nMvaa 84,136,176\nMvaa 144,144,176\nMvaa 144,148,180\nD0 8,24,44,2048\nMvaa 148,156,180\nMvaa 148,152,176\nMvaa 156,152,184\n"
"Mvaa 156,156,188\ns_add_u32 s6, s53, 4096\nSv 216,1,16,6,2048\nMvaa 152,148,188\nMvaa 152,144,184\nMvaa 92,136,184\nMvaa 92,140,188\nD0 9,24,44,3072\nMvaa 88,132,188\n"
"Mvaa 88,128,184\nWL 0\nMaaa 0,192,160\nRa 128,10,0\nMaaa 0,196,164\nMaaa 4,204,164\nRa 132,11,0\ns_add_u32 m0, s47, 49152\ns_add_u32 s44, s44, 0x40000\nMaaa 4,200,160\n"
"Maaa 64,208,160\nRa 136,10,2048\nMaaa 64,212,164\nD0 6,24,44,0\nMaaa 68,220,164\nRa 140,11,2048\nMaaa 68,216,160\nMaaa 76,216,168\nRa 144,10,8192\nMaaa 76,220,172\n"
"s_add_u32 s6, s53, 0\nSv 224,1,16,6,1024\nMaaa 72,212,172\nRa 148,11,8192\nMaaa 72,208,168\nMaaa 12,200,168\nRa 152,10,10240\nMaaa 12,204,172\nD0 7,24,44,1024\nMaaa 8,196,172\n"
"Ra 156,11,10240\nMaaa 8,192,168\nMaaa 16,192,176\nMaaa 16,196,180\nMaaa 20,204,180\nMaaa 20,200,176\nMaaa 80,208,176\nMaaa 80,212,180\nD0 8,24,44,2048\nMaaa 84,220,180\n"
"Maaa 84,216,176\nMaaa 92,216,184\nMaaa 92,220,188\ns_add_u32 s6, s53, 0\nSv 232,1,16,6,3072\nMaaa 88,212,188\nMaaa 88,208,184\nMaaa 28,200,184\nMaaa 28,204,188\nD0 9,24,44,3072\n"
"Maaa 24,196,188\nMaaa 24,192,184\ns_waitcnt vmcnt(24) lgkmcnt(0)\ns_barrier\nMaaa 32,192,224\nTa 160,12,0\nMaaa 32,196,228\nTa 162,12,2048\nMaaa 36,204,228\n"
"s_add_u32 m0, s47, -16384\ns_add_u32 s48, s48, 128\nMaaa 36,200,224\nTa 164,12,16384\nMaaa 96,208,224\nTa 166,12,18432\nMaaa 96,212,228\nD0 2,20,48,0\nMaaa 100,220,228\n"
"Ta 168,13,0\nMaaa 100,216,224\nTa 170,13,2048\nMaaa 108,216,232\nMaaa 108,220,236\nTa 172,13,16384\ns_add_u32 s6, s53, 4096\nSv 240,1,16,6,1024\nMaaa 104,212,236\nTa 174,13,18432\n"
"Maaa 104,208,232\nMaaa 44,200,232\nTa 176,14,0\nMaaa 44,204,236\nTa 178,14,2048\nD0 3,20,48,1024\nMaaa 40,196,236\nMaaa 40,192,232\nTa 180,14,16384\nMaaa 48,192,240\n"
"Ta 182,14,18432\nMaaa 48,196,244\ns_add_u32 s6, s53, 4096\nSv 248,1,16,6,3072\nMaaa 52,204,244\nTa 184,15,0\nMaaa 52,200,240\nTa 186,15,2048\nMaaa 112,208,240\nMaaa 112,212,244\n"
"Ta 188,15,16384\nD0 4,20,48,2048\nMaaa 116,220,244\nTa 190,15,18432\nMaaa 116,216,240\nMaaa 124,216,248\nMaaa 124,220,252\nMaaa 120,212,252\nMaaa 120,208,248\n"
"s_lshl3_add_u32 s90, s12, s13\ns_lshl2_add_u32 s90, s90, s11\ns_lshl2_add_u32 s90, s90, 0xc04a400\nMaaa 60,200,248\nMaaa 60,204,252\nD0 5,20,48,3072\nMaaa 56,196,252\n"
"Maaa 56,192,248\nWL 0\ns_mov_b64 s[76:77], s[62:63]\ns_mov_b64 s[78:79], s[64:65]\ns_mov_b32 s42, 1605632\ns_mov_b32 s50, 1\n.GL155:\nMvaa 64,128,160\nTa 224,16,0\n"
"Mvaa 64,132,164\nTa 226,16,2048\nMvaa 68,140,164\ns_add_u32 m0, s47, -65536\ns_add_u32 s45, s45, 128\nMvaa 68,136,160\nTa 228,16,16384\nMvaa 128,144,160\nTa 230,16,18432\n"
"Mvaa 128,148,164\nD0 2,20,45,0\nMvaa 132,156,164\nTa 232,17,0\nMvaa 132,152,160\nTa 234,17,2048\nMvaa 140,152,168\nMvaa 140,156,172\nTa 236,17,16384\nMvaa 136,148,172\n"
"Ta 238,17,18432\nMvaa 136,144,168\nMvaa 76,136,168\nTa 240,18,0\nMvaa 76,140,172\nTa 242,18,2048\nD0 3,20,45,1024\nMvaa 72,132,172\nMvaa 72,128,168\nTa 244,18,16384\n"
"Mvaa 80,128,176\nTa 246,18,18432\nMvaa 80,132,180\nMvaa 84,140,180\nTa 248,19,0\nMvaa 84,136,176\nTa 250,19,2048\nMvaa 144,144,176\nMvaa 144,148,180\nTa 252,19,16384\n"
"D0 4,20,45,2048\nMvaa 148,156,180\nTa 254,19,18432\nMvaa 148,152,176\nMvaa 156,152,184\nMvaa 156,156,188\nMvaa 152,148,188\nMvaa 152,144,184\ns_mov_b32 s40, s76\n"
"s_and_b32 s41, s77, 0xffff\nMvaa 92,136,184\nMvaa 92,140,188\nD0 5,20,45,3072\nMvaa 88,132,188\nMvaa 88,128,184\ns_waitcnt vmcnt(17) lgkmcnt(0)\ns_barrier\nMvaa 96,128,224\n"
"Ra 192,10,16384\nMvaa 96,132,228\nbuffer_load_dword v31, v26, s[40:43], s97 offen\ns_mov_b64 s[76:77], s[66:67]\nMvaa 100,140,228\nRa 196,11,16384\ns_add_u32 m0, s47, 0\n"
"s_add_u32 s44, s44, 0x40000\nMvaa 100,136,224\nMvaa 160,144,224\nRa 200,10,18432\nMvaa 160,148,228\nD0 6,24,44,0\nMvaa 164,156,228\nRa 204,11,18432\nMvaa 164,152,224\n"
"Mvaa 172,152,232\nRa 208,10,24576\nMvaa 172,156,236\nMvaa 168,148,236\nRa 212,11,24576\nMvaa 168,144,232\nMvaa 108,136,232\nRa 216,10,26624\nMvaa 108,140,236\nD0 7,24,44,1024\n"
"Mvaa 104,132,236\nRa 220,11,26624\nMvaa 104,128,232\nMvaa 112,128,240\nMvaa 112,132,244\nMvaa 116,140,244\nMvaa 116,136,240\nMvaa 176,144,240\nMvaa 176,148,244\nD0 8,24,44,2048\n"
"Mvaa 180,156,244\nMvaa 180,152,240\nMvaa 188,152,248\nMvaa 188,156,252\nMvaa 184,148,252\nMvaa 184,144,248\nMvaa 124,136,248\nMvaa 124,140,252\nD0 9,24,44,3072\nMvaa 120,132,252\n"
"Mvaa 120,128,248\nWL 0\nMaaa 32,192,224\nRa 128,10,32768\nMaaa 32,196,228\nMaaa 36,204,228\nRa 132,11,32768\ns_add_u32 m0, s47, 16384\ns_add_u32 s44, s44, 0x40000\n"
"Maaa 36,200,224\nMaaa 96,208,224\nRa 136,10,34816\nMaaa 96,212,228\nD0 6,24,44,0\nMaaa 100,220,228\nRa 140,11,34816\nMaaa 100,216,224\nMaaa 108,216,232\nRa 144,10,40960\n"
"Maaa 108,220,236\nMaaa 104,212,236\nRa 148,11,40960\nMaaa 104,208,232\nMaaa 44,200,232\nRa 152,10,43008\nMaaa 44,204,236\nD0 7,24,44,1024\nMaaa 40,196,236\nRa 156,11,43008\n"
"Maaa 40,192,232\nMaaa 48,192,240\nMaaa 48,196,244\nMaaa 52,204,244\nMaaa 52,200,240\nMaaa 112,208,240\nMaaa 112,212,244\nD0 8,24,44,2048\nMaaa 116,220,244\nMaaa 116,216,240\n"
"Maaa 124,216,248\nMaaa 124,220,252\nMaaa 120,212,252\nMaaa 120,208,248\nMaaa 60,200,248\nMaaa 60,204,252\nD0 9,24,44,3072\nMaaa 56,196,252\nMaaa 56,192,248\n"
"s_waitcnt vmcnt(17) lgkmcnt(0)\ns_barrier\nMaaa 0,192,160\nTa 224,16,32768\nMaaa 0,196,164\nTa 226,16,34816\nMaaa 4,204,164\ns_add_u32 m0, s47, -49152\ns_add_u32 s48, s48, 128\n"
"Maaa 4,200,160\nTa 228,16,49152\nMaaa 64,208,160\nTa 230,16,51200\nMaaa 64,212,164\nD0 2,20,48,0\nMaaa 68,220,164\nTa 232,17,32768\nMaaa 68,216,160\nTa 234,17,34816\n"
"Maaa 76,216,168\nMaaa 76,220,172\nTa 236,17,49152\nMaaa 72,212,172\nTa 238,17,51200\nMaaa 72,208,168\nMaaa 12,200,168\nTa 240,18,32768\nMaaa 12,204,172\nTa 242,18,34816\n"
"D0 3,20,48,1024\nMaaa 8,196,172\nMaaa 8,192,168\nTa 244,18,49152\nMaaa 16,192,176\nTa 246,18,51200\nMaaa 16,196,180\nMaaa 20,204,180\nTa 248,19,32768\nMaaa 20,200,176\n"
"Ta 250,19,34816\nMaaa 80,208,176\nMaaa 80,212,180\nTa 252,19,49152\nD0 4,20,48,2048\nMaaa 84,220,180\nTa 254,19,51200\nMaaa 84,216,176\nMaaa 92,216,184\nMaaa 92,220,188\n"
"Maaa 88,212,188\nMaaa 88,208,184\nMaaa 28,200,184\nMaaa 28,204,188\nD0 5,20,48,3072\nMaaa 24,196,188\nMaaa 24,192,184\nWL 0\nMvaa 96,128,224\nTa 160,12,32768\nMvaa 96,132,228\n"
"Ta 162,12,34816\nMvaa 100,140,228\ns_add_u32 m0, s47, -32768\ns_add_u32 s45, s45, 128\nMvaa 100,136,224\nTa 164,12,49152\nMvaa 160,144,224\nTa 166,12,51200\nMvaa 160,148,228\n"
"D0 2,20,45,0\nMvaa 164,156,228\nTa 168,13,32768\nMvaa 164,152,224\nTa 170,13,34816\nMvaa 172,152,232\nMvaa 172,156,236\nTa 172,13,49152\nMvaa 168,148,236\nTa 174,13,51200\n"
"Mvaa 168,144,232\nMvaa 108,136,232\nTa 176,14,32768\nMvaa 108,140,236\nTa 178,14,34816\nD0 3,20,45,1024\nMvaa 104,132,236\nMvaa 104,128,232\nTa 180,14,49152\nMvaa 112,128,240\n"
"Ta 182,14,51200\nMvaa 112,132,244\nMvaa 116,140,244\nTa 184,15,32768\nMvaa 116,136,240\nTa 186,15,34816\nMvaa 176,144,240\nMvaa 176,148,244\nTa 188,15,49152\nD0 4,20,45,2048\n"
"Mvaa 180,156,244\nTa 190,15,51200\nMvaa 180,152,240\nMvaa 188,152,248\nMvaa 188,156,252\nMvaa 184,148,252\nMvaa 184,144,248\ns_mov_b32 s40, s78\ns_and_b32 s41, s79, 0xffff\n"
"Mvaa 124,136,248\nMvaa 124,140,252\nD0 5,20,45,3072\nMvaa 120,132,252\nMvaa 120,128,248\ns_waitcnt vmcnt(17) lgkmcnt(0)\ns_barrier\nMvaa 64,128,160\nRa 192,10,49152\n"
"Mvaa 64,132,164\nbuffer_load_dword v31, v26, s[40:43], s97 offen\ns_mov_b64 s[78:79], s[66:67]\nMvaa 68,140,164\nRa 196,11,49152\ns_add_u32 m0, s47, 32768\n"
"s_add_u32 s44, s44, 0x40000\nMvaa 68,136,160\nMvaa 128,144,160\nRa 200,10,51200\nMvaa 128,148,164\nD0 6,24,44,0\nMvaa 132,156,164\nRa 204,11,51200\nMvaa 132,152,160\n"
"Mvaa 140,152,168\nRa 208,10,57344\nMvaa 140,156,172\nMvaa 136,148,172\nRa 212,11,57344\nMvaa 136,144,168\nMvaa 76,136,168\nRa 216,10,59392\nMvaa 76,140,172\nD0 7,24,44,1024\n"
"Mvaa 72,132,172\nRa 220,11,59392\nMvaa 72,128,168\nMvaa 80,128,176\nMvaa 80,132,180\nMvaa 84,140,180\nMvaa 84,136,176\nMvaa 144,144,176\nMvaa 144,148,180\nD0 8,24,44,2048\n"
"Mvaa 148,156,180\nMvaa 148,152,176\nMvaa 156,152,184\nMvaa 156,156,188\nMvaa 152,148,188\nMvaa 152,144,184\nMvaa 92,136,184\nMvaa 92,140,188\nD0 9,24,44,3072\nMvaa 88,132,188\n"
"Mvaa 88,128,184\nWL 0\nMaaa 0,192,160\nRa 128,10,0\nMaaa 0,196,164\nMaaa 4,204,164\nRa 132,11,0\ns_add_u32 m0, s47, 49152\ns_add_u32 s44, s44, 0x40000\nMaaa 4,200,160\n"
"Maaa 64,208,160\nRa 136,10,2048\nMaaa 64,212,164\nD0 6,24,44,0\nMaaa 68,220,164\nRa 140,11,2048\nMaaa 68,216,160\nMaaa 76,216,168\nRa 144,10,8192\nMaaa 76,220,172\n"
"Maaa 72,212,172\nRa 148,11,8192\nMaaa 72,208,168\nMaaa 12,200,168\nRa 152,10,10240\nMaaa 12,204,172\nD0 7,24,44,1024\nMaaa 8,196,172\nRa 156,11,10240\nMaaa 8,192,168\n"
"Maaa 16,192,176\nMaaa 16,196,180\nMaaa 20,204,180\nMaaa 20,200,176\nMaaa 80,208,176\nMaaa 80,212,180\nD0 8,24,44,2048\nMaaa 84,220,180\nMaaa 84,216,176\nMaaa 92,216,184\n"
"Maaa 92,220,188\nMaaa 88,212,188\nMaaa 88,208,184\nMaaa 28,200,184\nMaaa 28,204,188\nD0 9,24,44,3072\nMaaa 24,196,188\nMaaa 24,192,184\ns_waitcnt vmcnt(17) lgkmcnt(0)\ns_barrier\n"
"Maaa 32,192,224\nTa 160,12,0\nMaaa 32,196,228\nTa 162,12,2048\nMaaa 36,204,228\ns_add_u32 m0, s47, -16384\ns_add_u32 s48, s48, 128\nMaaa 36,200,224\nTa 164,12,16384\n"
"Maaa 96,208,224\nTa 166,12,18432\nMaaa 96,212,228\nD0 2,20,48,0\nMaaa 100,220,228\nTa 168,13,0\nMaaa 100,216,224\nTa 170,13,2048\nMaaa 108,216,232\nMaaa 108,220,236\n"
"Ta 172,13,16384\nMaaa 104,212,236\nTa 174,13,18432\nMaaa 104,208,232\nMaaa 44,200,232\nTa 176,14,0\nMaaa 44,204,236\nTa 178,14,2048\nD0 3,20,48,1024\nMaaa 40,196,236\n"
"Maaa 40,192,232\nTa 180,14,16384\nMaaa 48,192,240\nTa 182,14,18432\nMaaa 48,196,244\nMaaa 52,204,244\nTa 184,15,0\nMaaa 52,200,240\nTa 186,15,2048\nMaaa 112,208,240\n"
"Maaa 112,212,244\nTa 188,15,16384\nD0 4,20,48,2048\nMaaa 116,220,244\nTa 190,15,18432\nMaaa 116,216,240\nMaaa 124,216,248\nMaaa 124,220,252\nMaaa 120,212,252\nMaaa 120,208,248\n"
"Maaa 60,200,248\nMaaa 60,204,252\nD0 5,20,48,3072\ns_sub_u32 s50, s50, 1\nMaaa 56,196,252\nMaaa 56,192,248\nWL 0\ns_cbranch_scc0 .GL155\nMvaa 64,128,160\nTa 224,16,0\n"
"Mvaa 64,132,164\nTa 226,16,2048\nMvaa 68,140,164\ns_add_u32 m0, s47, -65536\ns_add_u32 s45, s45, 128\nMvaa 68,136,160\nTa 228,16,16384\nMvaa 128,144,160\nTa 230,16,18432\n"
"Mvaa 128,148,164\nD0 2,20,45,0\nMvaa 132,156,164\nTa 232,17,0\nMvaa 132,152,160\nTa 234,17,2048\nMvaa 140,152,168\ns_and_b32 s2, s74, 15\nMvaa 140,156,172\nTa 236,17,16384\n"
"buffer_store_dword v25, off, s[16:19], s90 offset:0 sc0 sc1\nMvaa 136,148,172\nTa 238,17,18432\nMvaa 136,144,168\ns_sub_u32 s2, s2, 2\ns_cmp_lt_u32 s2, 14\n"
"s_cselect_b32 s5, 0, 0x40000000\nMvaa 76,136,168\nTa 240,18,0\nMvaa 76,140,172\nTa 242,18,2048\nD0 3,20,45,1024\nMvaa 72,132,172\ns_and_b32 s2, s2, 15\nMvaa 72,128,168\n"
"Ta 244,18,16384\nMvaa 80,128,176\nTa 246,18,18432\nMvaa 80,132,180\ns_mul_i32 s4, s73, 14\nMvaa 84,140,180\nTa 248,19,0\nMvaa 84,136,176\nTa 250,19,2048\nMvaa 144,144,176\n"
"s_add_u32 s4, s4, s2\nMvaa 144,148,180\nTa 252,19,16384\nD0 4,20,45,2048\nMvaa 148,156,180\nTa 254,19,18432\nMvaa 148,152,176\ns_mov_b32 s36, s62\nMvaa 156,152,184\n"
"s_and_b32 s37, s63, 0xffff\nMvaa 156,156,188\ns_mov_b32 s38, 0x188000\nMvaa 152,148,188\ns_mov_b32 s39, 0x20000\nMvaa 152,144,184\ns_mov_b32 s40, s64\nMvaa 92,136,184\n"
"s_and_b32 s41, s65, 0xffff\nMvaa 92,140,188\nD0 5,20,45,3072\nMvaa 88,132,188\ns_mov_b32 s42, 0x188000\nMvaa 88,128,184\ns_waitcnt vmcnt(18) lgkmcnt(0)\ns_barrier\n"
"Mvaa 96,128,224\nRa 192,10,16384\nMvaa 96,132,228\ns_mov_b32 s43, 0x20000\nMvaa 100,140,228\nRa 196,11,16384\ns_add_u32 m0, s47, 0\ns_add_u32 s44, s44, 0x40000\nMvaa 100,136,224\n"
"s_mov_b32 s92, s66\nMvaa 160,144,224\nRa 200,10,18432\nMvaa 160,148,228\nD0 6,24,44,0\nMvaa 164,156,228\nRa 204,11,18432\nMvaa 164,152,224\ns_and_b32 s93, s67, 0xffff\n"
"Mvaa 172,152,232\nRa 208,10,24576\nMvaa 172,156,236\ns_mov_b32 s94, 0x188000\nMvaa 168,148,236\nRa 212,11,24576\nMvaa 168,144,232\ns_mov_b32 s95, 0x20000\nMvaa 108,136,232\n"
"Ra 216,10,26624\nMvaa 108,140,236\nD0 7,24,44,1024\nMvaa 104,132,236\nRa 220,11,26624\nMvaa 104,128,232\nv_lshrrev_b32 v252, 1, v0\nMvaa 112,128,240\n"
"v_mul_u32_u24 v253, 14336, v252\nMvaa 112,132,244\nv_and_b32 v254, 1, v0\nMvaa 116,140,244\nv_lshl_add_u32 v253, v254, 4, v253\nMvaa 116,136,240\nv_mov_b32 v254, 0x40000000\n"
"v_cmp_gt_u32 vcc, 56, v0\nv_lshlrev_b32 v252, 3, v252\ns_mul_i32 s2, s11, 401408\nv_cndmask_b32 v21, v254, v253, vcc\nv_cndmask_b32 v255, v254, v252, vcc\nMvaa 176,144,240\n"
"s_lshl_b32 s3, s4, 5\nMvaa 176,148,244\nD0 8,24,44,2048\nMvaa 180,156,244\ns_add_u32 s2, s2, s3\nMvaa 180,152,240\ns_or_b32 s86, s2, s5\nMvaa 188,152,248\n"
"s_add_u32 s87, s86, 7168\nMvaa 188,156,252\nv_lshrrev_b32 v251, 4, v0\nMvaa 184,148,252\nv_mul_u32_u24 v252, 3584, v251\nMvaa 184,144,248\nv_and_b32 v251, 15, v0\n"
"Mvaa 124,136,248\nv_lshl_add_u32 v252, v251, 4, v252\nv_cmp_gt_u32 vcc, 14, v251\ns_mul_i32 s2, s4, 14336\ns_mul_i32 s3, s11, 224\nv_cndmask_b32 v20, v254, v252, vcc\n"
"Mvaa 124,140,252\nD0 9,24,44,3072\nMvaa 120,132,252\ns_add_u32 s2, s2, s3\nMvaa 120,128,248\nWL 0\nMaaa 32,192,224\nRa 128,10,32768\nMaaa 32,196,228\ns_or_b32 s88, s2, s5\n"
"Maaa 36,204,228\nRa 132,11,32768\ns_add_u32 m0, s47, 16384\ns_add_u32 s44, s44, 0x40000\nMaaa 36,200,224\nMaaa 96,208,224\nRa 136,10,34816\nMaaa 96,212,228\nD0 6,24,44,0\n"
"Maaa 100,220,228\nRa 140,11,34816\nMaaa 100,216,224\nMaaa 108,216,232\nRa 144,10,40960\nMaaa 108,220,236\nMaaa 104,212,236\nRa 148,11,40960\nMaaa 104,208,232\nMaaa 44,200,232\n"
"Ra 152,10,43008\nMaaa 44,204,236\nD0 7,24,44,1024\nMaaa 40,196,236\nRa 156,11,43008\nMaaa 40,192,232\nMaaa 48,192,240\nMaaa 48,196,244\nMaaa 52,204,244\nMaaa 52,200,240\n"
"Maaa 112,208,240\nMaaa 112,212,244\nD0 8,24,44,2048\nMaaa 116,220,244\nMaaa 116,216,240\nMaaa 124,216,248\nMaaa 124,220,252\nMaaa 120,212,252\nMaaa 120,208,248\nMaaa 60,200,248\n"
"Maaa 60,204,252\nD0 9,24,44,3072\nMaaa 56,196,252\nMaaa 56,192,248\ns_waitcnt vmcnt(17) lgkmcnt(0)\ns_barrier\nMaaa 0,192,160\nTa 224,16,32768\nMaaa 0,196,164\nTa 226,16,34816\n"
"buffer_load_dwordx4 v[192:195], v21, s[36:39], s86 offen offset:0\nMaaa 4,204,164\ns_add_u32 m0, s47, -49152\ns_add_u32 s48, s48, 128\nMaaa 4,200,160\nTa 228,16,49152\n"
"s_lshl_b32 s6, s11, 12\ns_add_u32 s6, s6, 0xc044000\nMaaa 64,208,160\nTa 230,16,51200\nMaaa 64,212,164\nD0 2,20,48,0\nMaaa 68,220,164\nTa 232,17,32768\nMaaa 68,216,160\n"
"Ta 234,17,34816\nMaaa 76,216,168\nbuffer_load_dwordx4 v[32:35], v1, s[16:19], s6 offen offset:0 sc0 sc1\nMaaa 76,220,172\nTa 236,17,49152\nMaaa 72,212,172\nTa 238,17,51200\n"
"Maaa 72,208,168\nMaaa 12,200,168\nTa 240,18,32768\nMaaa 12,204,172\nTa 242,18,34816\nD0 3,20,48,1024\nMaaa 8,196,172\n"
"buffer_load_dwordx4 v[36:39], v1, s[16:19], s6 offen offset:1024 sc0 sc1\nMaaa 8,192,168\nTa 244,18,49152\nMaaa 16,192,176\nTa 246,18,51200\nMaaa 16,196,180\nMaaa 20,204,180\n"
"Ta 248,19,32768\nMaaa 20,200,176\nTa 250,19,34816\nMaaa 80,208,176\nbuffer_load_dwordx4 v[40:43], v1, s[16:19], s6 offen offset:2048 sc0 sc1\nMaaa 80,212,180\nTa 252,19,49152\n"
"D0 4,20,48,2048\nMaaa 84,220,180\nTa 254,19,51200\nMaaa 84,216,176\nMaaa 92,216,184\nbuffer_load_dwordx4 v[44:47], v1, s[16:19], s6 offen offset:3072 sc0 sc1\nMaaa 92,220,188\n"
"Maaa 88,212,188\nMaaa 88,208,184\nMaaa 28,200,184\nMaaa 28,204,188\nD0 5,20,48,3072\nMaaa 24,196,188\nMaaa 24,192,184\nWL 0\nMvaa 96,128,224\nTa 160,12,32768\nMvaa 96,132,228\n"
"Ta 162,12,34816\nMvaa 100,140,228\ns_add_u32 m0, s47, -32768\ns_add_u32 s45, s45, 128\nMvaa 100,136,224\nTa 164,12,49152\nMvaa 160,144,224\nTa 166,12,51200\nMvaa 160,148,228\n"
"D0 2,20,45,0\nMvaa 164,156,228\nTa 168,13,32768\nMvaa 164,152,224\nTa 170,13,34816\nMvaa 172,152,232\nMvaa 172,156,236\nTa 172,13,49152\nMvaa 168,148,236\nTa 174,13,51200\n"
"Mvaa 168,144,232\nMvaa 108,136,232\nTa 176,14,32768\nMvaa 108,140,236\nTa 178,14,34816\nD0 3,20,45,1024\nMvaa 104,132,236\nMvaa 104,128,232\nTa 180,14,49152\nMvaa 112,128,240\n"
"Ta 182,14,51200\nMvaa 112,132,244\nMvaa 116,140,244\nTa 184,15,32768\nMvaa 116,136,240\nTa 186,15,34816\nMvaa 176,144,240\nMvaa 176,148,244\nTa 188,15,49152\nD0 4,20,45,2048\n"
"Mvaa 180,156,244\nTa 190,15,51200\nMvaa 180,152,240\nMvaa 188,152,248\nMvaa 188,156,252\nMvaa 184,148,252\nMvaa 184,144,248\nMvaa 124,136,248\nMvaa 124,140,252\nD0 5,20,45,3072\n"
"Mvaa 120,132,252\nMvaa 120,128,248\ns_waitcnt vmcnt(21) lgkmcnt(0)\ns_barrier\nMvaa 64,128,160\nRa 192,10,49152\nMvaa 64,132,164\n"
"buffer_load_dwordx4 v[208:211], v21, s[40:43], s86 offen offset:0\nMvaa 68,140,164\nRa 196,11,49152\ns_add_u32 m0, s47, 32768\ns_add_u32 s44, s44, 0x40000\nMvaa 68,136,160\n"
"Mvaa 128,144,160\nRa 200,10,51200\nMvaa 128,148,164\nD0 6,24,44,0\nMvaa 132,156,164\nRa 204,11,51200\nMvaa 132,152,160\nMvaa 140,152,168\nRa 208,10,57344\nMvaa 140,156,172\n"
"Mvaa 136,148,172\nRa 212,11,57344\nMvaa 136,144,168\nMvaa 76,136,168\nRa 216,10,59392\nMvaa 76,140,172\nD0 7,24,44,1024\nMvaa 72,132,172\nRa 220,11,59392\nMvaa 72,128,168\n"
"Mvaa 80,128,176\nMvaa 80,132,180\nMvaa 84,140,180\nMvaa 84,136,176\nMvaa 144,144,176\nMvaa 144,148,180\nD0 8,24,44,2048\nMvaa 148,156,180\nMvaa 148,152,176\nMvaa 156,152,184\n"
"Mvaa 156,156,188\nMvaa 152,148,188\nMvaa 152,144,184\nMvaa 92,136,184\nMvaa 92,140,188\nD0 9,24,44,3072\nMvaa 88,132,188\nMvaa 88,128,184\nWL 0\nMaaa 0,192,160\nRa 128,10,0\n"
"Maaa 0,196,164\nMaaa 4,204,164\nRa 132,11,0\ns_add_u32 m0, s47, 49152\ns_add_u32 s44, s44, 0x40000\nMaaa 4,200,160\nMaaa 64,208,160\nRa 136,10,2048\nMaaa 64,212,164\nD0 6,24,44,0\n"
"Maaa 68,220,164\nRa 140,11,2048\nMaaa 68,216,160\nMaaa 76,216,168\nRa 144,10,8192\nMaaa 76,220,172\nMaaa 72,212,172\nRa 148,11,8192\nMaaa 72,208,168\nMaaa 12,200,168\n"
"Ra 152,10,10240\nMaaa 12,204,172\nD0 7,24,44,1024\nMaaa 8,196,172\nRa 156,11,10240\nMaaa 8,192,168\nMaaa 16,192,176\nMaaa 16,196,180\nMaaa 20,204,180\nMaaa 20,200,176\n"
"Maaa 80,208,176\nMaaa 80,212,180\nD0 8,24,44,2048\nMaaa 84,220,180\nMaaa 84,216,176\nMaaa 92,216,184\nMaaa 92,220,188\nMaaa 88,212,188\nMaaa 88,208,184\nMaaa 28,200,184\n"
"Maaa 28,204,188\nD0 9,24,44,3072\nMaaa 24,196,188\nMaaa 24,192,184\ns_waitcnt vmcnt(14) lgkmcnt(0)\ns_barrier\nMaaa 32,192,224\nTa 160,12,0\nMaaa 32,196,228\nTa 162,12,2048\n"
"buffer_load_dwordx4 v[196:199], v21, s[36:39], s86 offen offset:3584\nMaaa 36,204,228\ns_add_u32 m0, s47, -16384\ns_add_u32 s48, s48, 128\nMaaa 36,200,224\nTa 164,12,16384\n"
"Maaa 96,208,224\nTa 166,12,18432\nMaaa 96,212,228\nD0 2,20,48,0\nMaaa 100,220,228\nTa 168,13,0\nMaaa 100,216,224\nTa 170,13,2048\nMaaa 108,216,232\nv_xor_b32 v48, s15, v33\n"
"Maaa 108,220,236\nTa 172,13,16384\nMaaa 104,212,236\nTa 174,13,18432\nMaaa 104,208,232\nv_xor_b32 v49, s15, v35\nMaaa 44,200,232\nTa 176,14,0\nMaaa 44,204,236\nTa 178,14,2048\n"
"D0 3,20,48,1024\nMaaa 40,196,236\nv_xor_b32 v50, s15, v37\nMaaa 40,192,232\nTa 180,14,16384\nMaaa 48,192,240\nTa 182,14,18432\nMaaa 48,196,244\nv_xor_b32 v51, s15, v39\n"
"Maaa 52,204,244\nTa 184,15,0\nMaaa 52,200,240\nTa 186,15,2048\nMaaa 112,208,240\nv_xor_b32 v52, s15, v41\nMaaa 112,212,244\nTa 188,15,16384\nD0 4,20,48,2048\nMaaa 116,220,244\n"
"Ta 190,15,18432\nMaaa 116,216,240\nv_xor_b32 v53, s15, v43\nMaaa 124,216,248\nv_xor_b32 v54, s15, v45\nMaaa 124,220,252\nv_xor_b32 v55, s15, v47\nMaaa 120,212,252\n"
"v_or3_b32 v48, v48, v49, v50\nMaaa 120,208,248\nv_or3_b32 v51, v51, v52, v53\nMaaa 60,200,248\nv_or3_b32 v48, v48, v54, v55\nMaaa 60,204,252\nD0 5,20,48,3072\nMaaa 56,196,252\n"
"v_or_b32 v48, v48, v51\nMaaa 56,192,248\nWL 0\nMvaa 64,128,160\nTa 224,16,0\nMvaa 64,132,164\nTa 226,16,2048\nMvaa 68,140,164\ns_add_u32 m0, s47, -65536\ns_add_u32 s45, s45, 128\n"
"Mvaa 68,136,160\nTa 228,16,16384\nMvaa 128,144,160\nTa 230,16,18432\nMvaa 128,148,164\nD0 2,20,45,0\nMvaa 132,156,164\nTa 232,17,0\nMvaa 132,152,160\nTa 234,17,2048\n"
"Mvaa 140,152,168\nv_cmp_ne_u32 vcc, 0, v48\nMvaa 140,156,172\nTa 236,17,16384\nMvaa 136,148,172\nTa 238,17,18432\nMvaa 136,144,168\ns_cbranch_vccnz .S149\n.S150:\nMvaa 76,136,168\n"
"Ta 240,18,0\nMvaa 76,140,172\nTa 242,18,2048\nD0 3,20,45,1024\nMvaa 72,132,172\nv_max_u32 v48, v32, v34\nMvaa 72,128,168\nTa 244,18,16384\nMvaa 80,128,176\nTa 246,18,18432\n"
"Mvaa 80,132,180\nv_min_u32 v49, v32, v34\nMvaa 84,140,180\nTa 248,19,0\nMvaa 84,136,176\nTa 250,19,2048\nMvaa 144,144,176\nv_med3_u32 v49, v48, v49, v36\nMvaa 144,148,180\n"
"Ta 252,19,16384\nD0 4,20,45,2048\nMvaa 148,156,180\nTa 254,19,18432\nMvaa 148,152,176\nv_max_u32 v48, v48, v36\nMvaa 156,152,184\nv_med3_u32 v49, v48, v49, v38\nMvaa 156,156,188\n"
"v_max_u32 v48, v48, v38\nMvaa 152,148,188\nv_med3_u32 v49, v48, v49, v40\nMvaa 152,144,184\nv_max_u32 v48, v48, v40\nMvaa 92,136,184\nv_med3_u32 v49, v48, v49, v42\n"
"Mvaa 92,140,188\nD0 5,20,45,3072\nMvaa 88,132,188\nv_max_u32 v48, v48, v42\nMvaa 88,128,184\ns_waitcnt vmcnt(18) lgkmcnt(0)\ns_barrier\nMvaa 96,128,224\nRa 192,10,16384\n"
"Mvaa 96,132,228\nbuffer_load_dwordx4 v[212:215], v21, s[40:43], s86 offen offset:3584\nMvaa 100,140,228\nRa 196,11,16384\ns_add_u32 m0, s47, 0\ns_add_u32 s44, s44, 0x40000\n"
"Mvaa 100,136,224\nv_med3_u32 v49, v48, v49, v44\nMvaa 160,144,224\nRa 200,10,18432\nMvaa 160,148,228\nD0 6,24,44,0\nMvaa 164,156,228\nRa 204,11,18432\nMvaa 164,152,224\n"
"v_max_u32 v48, v48, v44\nMvaa 172,152,232\nRa 208,10,24576\nMvaa 172,156,236\nv_med3_u32 v49, v48, v49, v46\nMvaa 168,148,236\nRa 212,11,24576\nMvaa 168,144,232\n"
"v_max_u32 v48, v48, v46\nMvaa 108,136,232\nRa 216,10,26624\nMvaa 108,140,236\nD0 7,24,44,1024\nMvaa 104,132,236\nRa 220,11,26624\nMvaa 104,128,232\nv_mov_b32 v50, v48\n"
"Mvaa 112,128,240\nMvaa 112,132,244\nv_max_u32_dpp v50, v50, v50 row_ror:8 row_mask:0xf bank_mask:0xf\nMvaa 116,140,244\nMvaa 116,136,240\n"
"v_max_u32_dpp v50, v50, v50 row_ror:4 row_mask:0xf bank_mask:0xf\nMvaa 176,144,240\nMvaa 176,148,244\nD0 8,24,44,2048\nMvaa 180,156,244\n"
"v_max_u32_dpp v50, v50, v50 row_ror:2 row_mask:0xf bank_mask:0xf\nMvaa 180,152,240\nMvaa 188,152,248\nv_max_u32_dpp v50, v50, v50 row_ror:1 row_mask:0xf bank_mask:0xf\n"
"Mvaa 188,156,252\nv_mov_b32 v52, v50\nMvaa 184,148,252\nMvaa 184,144,248\nv_permlane16_swap_b32 v50, v52\nMvaa 124,136,248\nv_max_u32 v50, v50, v52\nMvaa 124,140,252\n"
"D0 9,24,44,3072\nMvaa 120,132,252\nv_mov_b32 v52, v50\nMvaa 120,128,248\nWL 0\nMaaa 32,192,224\nRa 128,10,32768\nMaaa 32,196,228\nv_permlane32_swap_b32 v50, v52\nMaaa 36,204,228\n"
"Ra 132,11,32768\ns_add_u32 m0, s47, 16384\ns_add_u32 s44, s44, 0x40000\nMaaa 36,200,224\nv_max_u32 v50, v50, v52\nv_cmp_eq_u32 vcc, v48, v50\ns_lshl_b32 s2, s11, 3\n"
"s_add_u32 s2, s2, 131072\nv_cndmask_b32 v51, v48, v49, vcc\nMaaa 96,208,224\nRa 136,10,34816\nMaaa 96,212,228\nD0 6,24,44,0\nMaaa 100,220,228\nRa 140,11,34816\nMaaa 100,216,224\n"
"v_max_u32_dpp v51, v51, v51 row_ror:8 row_mask:0xf bank_mask:0xf\nMaaa 108,216,232\nRa 144,10,40960\nMaaa 108,220,236\n"
"v_max_u32_dpp v51, v51, v51 row_ror:4 row_mask:0xf bank_mask:0xf\nMaaa 104,212,236\nRa 148,11,40960\nMaaa 104,208,232\n"
"v_max_u32_dpp v51, v51, v51 row_ror:2 row_mask:0xf bank_mask:0xf\nMaaa 44,200,232\nRa 152,10,43008\nMaaa 44,204,236\nD0 7,24,44,1024\nMaaa 40,196,236\nRa 156,11,43008\n"
"Maaa 40,192,232\nv_max_u32_dpp v51, v51, v51 row_ror:1 row_mask:0xf bank_mask:0xf\nMaaa 48,192,240\nv_mov_b32 v52, v51\nMaaa 48,196,244\nMaaa 52,204,244\n"
"v_permlane16_swap_b32 v51, v52\nMaaa 52,200,240\nv_max_u32 v51, v51, v52\nMaaa 112,208,240\nv_mov_b32 v52, v51\nMaaa 112,212,244\nD0 8,24,44,2048\nMaaa 116,220,244\n"
"v_permlane32_swap_b32 v51, v52\nMaaa 116,216,240\nv_max_u32 v51, v51, v52\nMaaa 124,216,248\nv_mov_b32 v52, s2\nMaaa 124,220,252\nds_write_b64 v52, v[50:51]\nMaaa 120,212,252\n"
"v_mov_b32 v52, 131072\nMaaa 120,208,248\nMaaa 60,200,248\nMaaa 60,204,252\nD0 9,24,44,3072\nMaaa 56,196,252\nMaaa 56,192,248\ns_waitcnt vmcnt(18) lgkmcnt(0)\ns_barrier\n"
"Maaa 0,192,160\nTa 224,16,32768\nMaaa 0,196,164\nTa 226,16,34816\nbuffer_load_dwordx4 v[200:203], v21, s[36:39], s87 offen offset:0\nMaaa 4,204,164\ns_add_u32 m0, s47, -49152\n"
"s_add_u32 s48, s48, 128\nMaaa 4,200,160\nTa 228,16,49152\nMaaa 64,208,160\nTa 230,16,51200\nMaaa 64,212,164\nD0 2,20,48,0\nMaaa 68,220,164\nTa 232,17,32768\nMaaa 68,216,160\n"
"Ta 234,17,34816\nMaaa 76,216,168\nRv 32,52,0\nMaaa 76,220,172\nTa 236,17,49152\nMaaa 72,212,172\nTa 238,17,51200\nMaaa 72,208,168\nRv 36,52,16\nMaaa 12,200,168\nTa 240,18,32768\n"
"Maaa 12,204,172\nTa 242,18,34816\nD0 3,20,48,1024\nMaaa 8,196,172\nMaaa 8,192,168\nTa 244,18,49152\nMaaa 16,192,176\nTa 246,18,51200\nMaaa 16,196,180\nMaaa 20,204,180\n"
"Ta 248,19,32768\nMaaa 20,200,176\nTa 250,19,34816\nMaaa 80,208,176\nMaaa 80,212,180\nTa 252,19,49152\nD0 4,20,48,2048\nMaaa 84,220,180\nTa 254,19,51200\nMaaa 84,216,176\nWL 11\n"
"Maaa 92,216,184\nv_max_u32 v48, v32, v33\nMaaa 92,220,188\nv_min_u32 v49, v32, v33\nMaaa 88,212,188\nv_med3_u32 v49, v48, v49, v34\nMaaa 88,208,184\nv_max_u32 v48, v48, v34\n"
"Maaa 28,200,184\nv_med3_u32 v49, v48, v49, v35\nMaaa 28,204,188\nD0 5,20,48,3072\nMaaa 24,196,188\nv_max_u32 v48, v48, v35\nMaaa 24,192,184\nWL 0\nMvaa 96,128,224\n"
"Ta 160,12,32768\nMvaa 96,132,228\nTa 162,12,34816\nMvaa 100,140,228\ns_add_u32 m0, s47, -32768\ns_add_u32 s45, s45, 128\nMvaa 100,136,224\nTa 164,12,49152\nMvaa 160,144,224\n"
"Ta 166,12,51200\nMvaa 160,148,228\nD0 2,20,45,0\nMvaa 164,156,228\nTa 168,13,32768\nMvaa 164,152,224\nTa 170,13,34816\nMvaa 172,152,232\nv_med3_u32 v49, v48, v49, v36\n"
"Mvaa 172,156,236\nTa 172,13,49152\nMvaa 168,148,236\nTa 174,13,51200\nMvaa 168,144,232\nv_max_u32 v48, v48, v36\nMvaa 108,136,232\nTa 176,14,32768\nMvaa 108,140,236\n"
"Ta 178,14,34816\nD0 3,20,45,1024\nMvaa 104,132,236\nv_med3_u32 v49, v48, v49, v37\nMvaa 104,128,232\nTa 180,14,49152\nMvaa 112,128,240\nTa 182,14,51200\nMvaa 112,132,244\n"
"v_max_u32 v48, v48, v37\nMvaa 116,140,244\nTa 184,15,32768\nMvaa 116,136,240\nTa 186,15,34816\nMvaa 176,144,240\nv_med3_u32 v49, v48, v49, v38\nMvaa 176,148,244\nTa 188,15,49152\n"
"D0 4,20,45,2048\nMvaa 180,156,244\nTa 190,15,51200\nMvaa 180,152,240\nv_max_u32 v48, v48, v38\nMvaa 188,152,248\nv_med3_u32 v49, v48, v49, v39\nMvaa 188,156,252\n"
"v_max_u32 v48, v48, v39\nMvaa 184,148,252\nv_readfirstlane_b32 s2, v48\nMvaa 184,144,248\nv_readfirstlane_b32 s3, v49\nMvaa 124,136,248\nMvaa 124,140,252\nD0 5,20,45,3072\n"
"Mvaa 120,132,252\ns_andn2_b32 s2, 0x7ff, s2\nMvaa 120,128,248\ns_waitcnt vmcnt(18) lgkmcnt(0)\ns_barrier\nMvaa 64,128,160\nRa 192,10,49152\nMvaa 64,132,164\n"
"buffer_load_dwordx4 v[216:219], v21, s[40:43], s87 offen offset:0\nMvaa 68,140,164\nRa 196,11,49152\ns_add_u32 m0, s47, 32768\ns_add_u32 s44, s44, 0x40000\nMvaa 68,136,160\n"
"s_andn2_b32 s3, 0x7ff, s3\nMvaa 128,144,160\nRa 200,10,51200\nMvaa 128,148,164\nD0 6,24,44,0\nMvaa 132,156,164\nRa 204,11,51200\nMvaa 132,152,160\ns_lshl_b32 s71, s3, 16\n"
"Mvaa 140,152,168\nRa 208,10,57344\nMvaa 140,156,172\ns_or_b32 s71, s71, s2\nMvaa 136,148,172\nRa 212,11,57344\nMvaa 136,144,168\ns_mul_i32 s2, s2, 896\nMvaa 76,136,168\n"
"Ra 216,10,59392\nMvaa 76,140,172\nD0 7,24,44,1024\nMvaa 72,132,172\nRa 220,11,59392\nMvaa 72,128,168\ns_mul_i32 s3, s3, 896\nMvaa 80,128,176\ns_mul_i32 s6, s11, 224\n"
"Mvaa 80,132,180\ns_add_u32 s2, s2, s6\nMvaa 84,140,180\ns_add_u32 s3, s3, s6\nMvaa 84,136,176\nbuffer_load_dwordx2 v[240:241], v255, s[28:31], s2 offen\nMvaa 144,144,176\n"
"Mvaa 144,148,180\nD0 8,24,44,2048\nMvaa 148,156,180\nMvaa 148,152,176\nbuffer_load_dwordx2 v[242:243], v255, s[28:31], s3 offen\ns_lshl2_add_u32 s2, s10, s11\n"
"s_cmp_eq_u32 s2, 512\ns_cselect_b32 s2, 0, 0x40000000\ns_add_u32 s2, s2, 0xc048000\nMvaa 156,152,184\nMvaa 156,156,188\nMvaa 152,148,188\nMvaa 152,144,184\n"
"buffer_store_dword v25, off, s[16:19], s2 offset:0 sc0 sc1\nMvaa 92,136,184\nMvaa 92,140,188\nD0 9,24,44,3072\nMvaa 88,132,188\nMvaa 88,128,184\nWL 0\nMaaa 0,192,160\nRa 128,10,0\n"
"Maaa 0,196,164\nMaaa 4,204,164\nRa 132,11,0\ns_add_u32 m0, s47, 49152\ns_add_u32 s44, s44, 0x40000\nMaaa 4,200,160\nMaaa 64,208,160\nRa 136,10,2048\nMaaa 64,212,164\nD0 6,24,44,0\n"
"Maaa 68,220,164\nRa 140,11,2048\nMaaa 68,216,160\nMaaa 76,216,168\nRa 144,10,8192\nMaaa 76,220,172\nMaaa 72,212,172\nRa 148,11,8192\nMaaa 72,208,168\nMaaa 12,200,168\n"
"Ra 152,10,10240\nMaaa 12,204,172\nD0 7,24,44,1024\nMaaa 8,196,172\nRa 156,11,10240\nMaaa 8,192,168\nMaaa 16,192,176\nMaaa 16,196,180\nMaaa 20,204,180\nMaaa 20,200,176\n"
"Maaa 80,208,176\nMaaa 80,212,180\nD0 8,24,44,2048\nMaaa 84,220,180\nMaaa 84,216,176\nMaaa 92,216,184\nMaaa 92,220,188\nMaaa 88,212,188\nMaaa 88,208,184\nMaaa 28,200,184\n"
"Maaa 28,204,188\nD0 9,24,44,3072\nMaaa 24,196,188\nMaaa 24,192,184\ns_waitcnt vmcnt(21) lgkmcnt(0)\ns_barrier\nMaaa 32,192,224\nTa 160,12,0\nMaaa 32,196,228\nTa 162,12,2048\n"
"buffer_load_dwordx4 v[204:207], v21, s[36:39], s87 offen offset:3584\nMaaa 36,204,228\ns_add_u32 m0, s47, -16384\ns_add_u32 s48, s48, 128\ns_lshl_b32 s5, s73, 8\n"
"s_add_u32 s5, s5, 0xc04ac00\nv_lshrrev_b32 v27, 2, v1\nbuffer_load_dword v27, v27, s[16:19], s5 offen sc0 sc1\nMaaa 36,200,224\nTa 164,12,16384\nMaaa 96,208,224\nTa 166,12,18432\n"
"Maaa 96,212,228\nD0 2,20,48,0\nMaaa 100,220,228\nTa 168,13,0\nMaaa 100,216,224\nTa 170,13,2048\nMaaa 108,216,232\nMaaa 108,220,236\nTa 172,13,16384\nMaaa 104,212,236\n"
"Ta 174,13,18432\nMaaa 104,208,232\nMaaa 44,200,232\nTa 176,14,0\nMaaa 44,204,236\nTa 178,14,2048\nD0 3,20,48,1024\nMaaa 40,196,236\nMaaa 40,192,232\nTa 180,14,16384\n"
"Maaa 48,192,240\nTa 182,14,18432\nMaaa 48,196,244\nMaaa 52,204,244\nTa 184,15,0\nMaaa 52,200,240\nTa 186,15,2048\nMaaa 112,208,240\nMaaa 112,212,244\nTa 188,15,16384\n"
"D0 4,20,48,2048\nMaaa 116,220,244\nTa 190,15,18432\nMaaa 116,216,240\nMaaa 124,216,248\nMaaa 124,220,252\nMaaa 120,212,252\nMaaa 120,208,248\nMaaa 60,200,248\nMaaa 60,204,252\n"
"D0 5,20,48,3072\nMaaa 56,196,252\nMaaa 56,192,248\nWL 0\nMvaa 64,128,160\nTa 224,16,0\nMvaa 64,132,164\nTa 226,16,2048\nMvaa 68,140,164\ns_add_u32 m0, s47, -65536\n"
"s_add_u32 s45, s45, 128\nMvaa 68,136,160\nTa 228,16,16384\nMvaa 128,144,160\nTa 230,16,18432\nMvaa 128,148,164\nD0 2,20,45,0\nMvaa 132,156,164\nTa 232,17,0\nMvaa 132,152,160\n"
"Ta 234,17,2048\nMvaa 140,152,168\nMvaa 140,156,172\nTa 236,17,16384\nMvaa 136,148,172\nTa 238,17,18432\nMvaa 136,144,168\nMvaa 76,136,168\nTa 240,18,0\nMvaa 76,140,172\n"
"Ta 242,18,2048\nD0 3,20,45,1024\nMvaa 72,132,172\nMvaa 72,128,168\nTa 244,18,16384\nMvaa 80,128,176\nTa 246,18,18432\nMvaa 80,132,180\nMvaa 84,140,180\nTa 248,19,0\n"
"Mvaa 84,136,176\nTa 250,19,2048\nMvaa 144,144,176\nMvaa 144,148,180\nTa 252,19,16384\nD0 4,20,45,2048\nMvaa 148,156,180\nTa 254,19,18432\nMvaa 148,152,176\nMvaa 156,152,184\n"
"Mvaa 156,156,188\nMvaa 152,148,188\nMvaa 152,144,184\nMvaa 92,136,184\nMvaa 92,140,188\nD0 5,20,45,3072\nMvaa 88,132,188\nMvaa 88,128,184\ns_waitcnt vmcnt(22) lgkmcnt(0)\n"
"s_barrier\nMvaa 96,128,224\nRa 192,10,16384\nWV 16\nMvaa 96,132,228\nbuffer_load_dwordx4 v[220:223], v21, s[40:43], s87 offen offset:3584\nMvaa 100,140,228\nRa 196,11,16384\n"
"s_add_u32 m0, s47, 0\ns_add_u32 s44, s44, 0x40000\nMvaa 100,136,224\nv_lshlrev_b32 v244, 16, v240\nv_and_b32 v245, 0xffff0000, v240\nMvaa 160,144,224\nRa 200,10,18432\n"
"v_lshlrev_b32 v246, 16, v241\nMvaa 160,148,228\nD0 6,24,44,0\nv_and_b32 v247, 0xffff0000, v241\nMvaa 164,156,228\nRa 204,11,18432\nv_lshlrev_b32 v248, 16, v242\nMvaa 164,152,224\n"
"v_and_b32 v249, 0xffff0000, v242\nv_lshlrev_b32 v250, 16, v243\nMvaa 172,152,232\nRa 208,10,24576\nv_and_b32 v251, 0xffff0000, v243\nMvaa 172,156,236\n"
"v_lshlrev_b32 v252, 16, v192\nMvaa 168,148,236\nRa 212,11,24576\nv_and_b32 v253, 0xffff0000, v192\nMvaa 168,144,232\nMU 32,244,252\nMU 33,244,253\nMvaa 108,136,232\n"
"Ra 216,10,26624\nMU 40,248,252\nMvaa 108,140,236\nD0 7,24,44,1024\nMU 41,248,253\nMvaa 104,132,236\nRa 220,11,26624\nv_lshlrev_b32 v252, 16, v193\nMvaa 104,128,232\n"
"v_and_b32 v253, 0xffff0000, v193\nMU 34,244,252\nMvaa 112,128,240\nMU 35,244,253\nMU 42,248,252\nMvaa 112,132,244\nMU 43,248,253\nMvaa 116,140,244\nv_lshlrev_b32 v252, 16, v194\n"
"v_and_b32 v253, 0xffff0000, v194\nMvaa 116,136,240\nMU 36,244,252\nMU 37,244,253\nMvaa 176,144,240\nMU 44,248,252\nMU 45,248,253\nMvaa 176,148,244\nD0 8,24,44,2048\n"
"v_lshlrev_b32 v252, 16, v195\nMvaa 180,156,244\nv_and_b32 v253, 0xffff0000, v195\nMU 38,244,252\nMvaa 180,152,240\nMU 39,244,253\nMU 46,248,252\nMvaa 188,152,248\nMU 47,248,253\n"
"v_lshlrev_b32 v252, 16, v208\nMvaa 188,156,252\nv_and_b32 v253, 0xffff0000, v208\nMvaa 184,148,252\nMU 48,244,252\nMU 49,244,253\nMvaa 184,144,248\nMU 56,248,252\nMU 57,248,253\n"
"Mvaa 124,136,248\nv_lshlrev_b32 v252, 16, v209\nv_and_b32 v253, 0xffff0000, v209\nMvaa 124,140,252\nD0 9,24,44,3072\nMU 50,244,252\nMvaa 120,132,252\nMU 51,244,253\nMU 58,248,252\n"
"Mvaa 120,128,248\nWL 0\nMaaa 32,192,224\nRa 128,10,32768\nMU 59,248,253\nMaaa 32,196,228\nv_lshlrev_b32 v252, 16, v210\nMaaa 36,204,228\nRa 132,11,32768\ns_add_u32 m0, s47, 16384\n"
"s_add_u32 s44, s44, 0x40000\nMaaa 36,200,224\nv_and_b32 v253, 0xffff0000, v210\nMU 52,244,252\nMaaa 96,208,224\nRa 136,10,34816\nMU 53,244,253\nMaaa 96,212,228\nD0 6,24,44,0\n"
"MU 60,248,252\nMaaa 100,220,228\nRa 140,11,34816\nMU 61,248,253\nMaaa 100,216,224\nv_lshlrev_b32 v252, 16, v211\nv_and_b32 v253, 0xffff0000, v211\nMaaa 108,216,232\n"
"Ra 144,10,40960\nMU 54,244,252\nMaaa 108,220,236\nMU 55,244,253\nMaaa 104,212,236\nRa 148,11,40960\nMU 62,248,252\nMaaa 104,208,232\nMU 63,248,253\nv_lshlrev_b32 v252, 16, v196\n"
"Maaa 44,200,232\nRa 152,10,43008\nv_and_b32 v253, 0xffff0000, v196\nMaaa 44,204,236\nD0 7,24,44,1024\nFM 32,245,252\nMaaa 40,196,236\nRa 156,11,43008\nFM 33,245,253\n"
"Maaa 40,192,232\nFM 40,249,252\nFM 41,249,253\nMaaa 48,192,240\nv_lshlrev_b32 v252, 16, v197\nv_and_b32 v253, 0xffff0000, v197\nMaaa 48,196,244\nFM 34,245,252\nMaaa 52,204,244\n"
"FM 35,245,253\nFM 42,249,252\nMaaa 52,200,240\nFM 43,249,253\nv_lshlrev_b32 v252, 16, v198\nMaaa 112,208,240\nv_and_b32 v253, 0xffff0000, v198\nFM 36,245,252\nMaaa 112,212,244\n"
"D0 8,24,44,2048\nFM 37,245,253\nMaaa 116,220,244\nFM 44,249,252\nFM 45,249,253\nMaaa 116,216,240\nv_lshlrev_b32 v252, 16, v199\nv_and_b32 v253, 0xffff0000, v199\nMaaa 124,216,248\n"
"FM 38,245,252\nFM 39,245,253\nMaaa 124,220,252\nFM 46,249,252\nMaaa 120,212,252\nFM 47,249,253\nv_lshlrev_b32 v252, 16, v212\nMaaa 120,208,248\nv_and_b32 v253, 0xffff0000, v212\n"
"FM 48,245,252\nMaaa 60,200,248\nFM 49,245,253\nFM 56,249,252\nMaaa 60,204,252\nD0 9,24,44,3072\nFM 57,249,253\nMaaa 56,196,252\nv_lshlrev_b32 v252, 16, v213\n"
"v_and_b32 v253, 0xffff0000, v213\nMaaa 56,192,248\ns_waitcnt vmcnt(19) lgkmcnt(0)\ns_barrier\nMaaa 0,192,160\nTa 224,16,32768\nFM 50,245,252\nMaaa 0,196,164\nTa 226,16,34816\n"
"buffer_load_dwordx4 v[224:227], v20, s[92:95], s88 offen offset:0\nMaaa 4,204,164\ns_add_u32 m0, s47, -49152\ns_add_u32 s48, s48, 128\nMaaa 4,200,160\nTa 228,16,49152\n"
"FM 51,245,253\nMaaa 64,208,160\nTa 230,16,51200\nFM 58,249,252\nMaaa 64,212,164\nD0 2,20,48,0\nFM 59,249,253\nMaaa 68,220,164\nTa 232,17,32768\nv_lshlrev_b32 v252, 16, v214\n"
"Maaa 68,216,160\nTa 234,17,34816\nv_and_b32 v253, 0xffff0000, v214\nMaaa 76,216,168\nFM 52,245,252\nFM 53,245,253\nMaaa 76,220,172\nTa 236,17,49152\nMaaa 72,212,172\n"
"Ta 238,17,51200\nFM 60,249,252\nMaaa 72,208,168\nFM 61,249,253\nv_lshlrev_b32 v252, 16, v215\nMaaa 12,200,168\nTa 240,18,32768\nv_and_b32 v253, 0xffff0000, v215\nMaaa 12,204,172\n"
"Ta 242,18,34816\nD0 3,20,48,1024\nMaaa 8,196,172\nFM 54,245,252\nFM 55,245,253\nMaaa 8,192,168\nTa 244,18,49152\nFM 62,249,252\nMaaa 16,192,176\nTa 246,18,51200\nFM 63,249,253\n"
"Maaa 16,196,180\nv_lshlrev_b32 v252, 16, v200\nMaaa 20,204,180\nTa 248,19,32768\nv_and_b32 v253, 0xffff0000, v200\nMaaa 20,200,176\nTa 250,19,34816\nFM 32,246,252\n"
"Maaa 80,208,176\nFM 33,246,253\nFM 40,250,252\nMaaa 80,212,180\nTa 252,19,49152\nD0 4,20,48,2048\nMaaa 84,220,180\nTa 254,19,51200\nFM 41,250,253\nMaaa 84,216,176\n"
"v_lshlrev_b32 v252, 16, v201\nv_and_b32 v253, 0xffff0000, v201\nMaaa 92,216,184\nFM 34,246,252\nFM 35,246,253\nMaaa 92,220,188\nFM 42,250,252\nMaaa 88,212,188\nFM 43,250,253\n"
"v_lshlrev_b32 v252, 16, v202\nMaaa 88,208,184\nv_and_b32 v253, 0xffff0000, v202\nFM 36,246,252\nMaaa 28,200,184\nFM 37,246,253\nFM 44,250,252\nMaaa 28,204,188\nD0 5,20,48,3072\n"
"FM 45,250,253\nMaaa 24,196,188\nv_lshlrev_b32 v252, 16, v203\nv_and_b32 v253, 0xffff0000, v203\nMaaa 24,192,184\nWL 0\nMvaa 96,128,224\nTa 160,12,32768\nFM 38,246,252\n"
"Mvaa 96,132,228\nTa 162,12,34816\nbuffer_load_dwordx4 v[228:231], v20, s[92:95], s88 offen offset:896\nMvaa 100,140,228\ns_add_u32 m0, s47, -32768\ns_add_u32 s45, s45, 128\n"
"Mvaa 100,136,224\nTa 164,12,49152\nFM 39,246,253\nMvaa 160,144,224\nTa 166,12,51200\nFM 46,250,252\nMvaa 160,148,228\nD0 2,20,45,0\nFM 47,250,253\nMvaa 164,156,228\n"
"Ta 168,13,32768\nv_lshlrev_b32 v252, 16, v216\nMvaa 164,152,224\nTa 170,13,34816\nv_and_b32 v253, 0xffff0000, v216\nMvaa 172,152,232\nFM 48,246,252\nFM 49,246,253\n"
"Mvaa 172,156,236\nTa 172,13,49152\nMvaa 168,148,236\nTa 174,13,51200\nFM 56,250,252\nMvaa 168,144,232\nFM 57,250,253\nv_lshlrev_b32 v252, 16, v217\nMvaa 108,136,232\n"
"Ta 176,14,32768\nv_and_b32 v253, 0xffff0000, v217\nMvaa 108,140,236\nTa 178,14,34816\nD0 3,20,45,1024\nMvaa 104,132,236\nFM 50,246,252\nFM 51,246,253\nMvaa 104,128,232\n"
"Ta 180,14,49152\nFM 58,250,252\nMvaa 112,128,240\nTa 182,14,51200\nFM 59,250,253\nMvaa 112,132,244\nv_lshlrev_b32 v252, 16, v218\nMvaa 116,140,244\nTa 184,15,32768\n"
"v_and_b32 v253, 0xffff0000, v218\nMvaa 116,136,240\nTa 186,15,34816\nFM 52,246,252\nMvaa 176,144,240\nFM 53,246,253\nFM 60,250,252\nMvaa 176,148,244\nTa 188,15,49152\n"
"D0 4,20,45,2048\nMvaa 180,156,244\nTa 190,15,51200\nFM 61,250,253\nMvaa 180,152,240\nv_lshlrev_b32 v252, 16, v219\nv_and_b32 v253, 0xffff0000, v219\nMvaa 188,152,248\n"
"FM 54,246,252\nWV 26\nv_xor_b32 v27, s15, v27\nv_and_b32 v255, 0xffffdfff, v27\nv_cmp_ne_u32 vcc, 0, v255\ns_nop 0\ns_cbranch_vccnz .S152\n.S153:\nv_cmp_ne_u32 vcc, 0, v27\n"
"s_nop 3\ns_bcnt1_i32_b64 s4, vcc\ns_cmp_gt_u32 s4, 32\ns_cselect_b32 s4, 1, 0\nMvaa 188,156,252\ns_xor_b32 s4, s4, s12\nMvaa 184,148,252\ns_and_b32 s4, s4, 1\n"
"s_lshl_b32 s4, s4, 13\nMvaa 184,144,248\ns_mul_i32 s72, s73, 25165824\ns_add_u32 s72, s72, s4\nMvaa 124,136,248\ns_and_b32 s4, s12, 1\ns_lshl_b32 s4, s4, 21\nMvaa 124,140,252\n"
"D0 5,20,45,3072\ns_add_u32 s72, s72, s4\nMvaa 120,132,252\ns_lshl3_add_u32 s2, s13, s11\ns_lshl_b32 s2, s2, 11\nMvaa 120,128,248\ns_waitcnt vmcnt(18) lgkmcnt(0)\ns_barrier\n"
"Mvaa 64,128,160\nRa 192,10,49152\ns_lshl_b32 s3, s14, 22\nMvaa 64,132,164\nbuffer_load_dwordx4 v[232:235], v20, s[92:95], s88 offen offset:1792\nMvaa 68,140,164\nRa 196,11,49152\n"
"s_add_u32 m0, s47, 32768\ns_add_u32 s44, s44, 0x40000\nMvaa 68,136,160\ns_add_u32 s2, s2, s3\ns_add_u32 s2, s2, s72\nMvaa 128,144,160\nRa 200,10,51200\n"
"s_add_u32 s49, s2, 0xc00000\nMvaa 128,148,164\nD0 6,24,44,0\nFM 55,246,253\nMvaa 132,156,164\nRa 204,11,51200\nFM 62,250,252\nMvaa 132,152,160\nFM 63,250,253\n"
"v_lshlrev_b32 v252, 16, v204\nMvaa 140,152,168\nRa 208,10,57344\nv_and_b32 v253, 0xffff0000, v204\nMvaa 140,156,172\nFM 32,247,252\nMvaa 136,148,172\nRa 212,11,57344\n"
"FM 33,247,253\nMvaa 136,144,168\nFM 40,251,252\nFM 41,251,253\nMvaa 76,136,168\nRa 216,10,59392\nv_lshlrev_b32 v252, 16, v205\nMvaa 76,140,172\nD0 7,24,44,1024\n"
"v_and_b32 v253, 0xffff0000, v205\nMvaa 72,132,172\nRa 220,11,59392\nFM 34,247,252\nMvaa 72,128,168\nFM 35,247,253\nFM 42,251,252\nMvaa 80,128,176\nFM 43,251,253\n"
"v_lshlrev_b32 v252, 16, v206\nMvaa 80,132,180\nv_and_b32 v253, 0xffff0000, v206\nMvaa 84,140,180\nFM 36,247,252\nFM 37,247,253\nMvaa 84,136,176\nFM 44,251,252\nFM 45,251,253\n"
"Mvaa 144,144,176\nv_lshlrev_b32 v252, 16, v207\nv_and_b32 v253, 0xffff0000, v207\nMvaa 144,148,180\nD0 8,24,44,2048\nFM 38,247,252\nMvaa 148,156,180\nFM 39,247,253\nFM 46,251,252\n"
"Mvaa 148,152,176\nFM 47,251,253\nv_lshlrev_b32 v252, 16, v220\nMvaa 156,152,184\nv_and_b32 v253, 0xffff0000, v220\nFM 48,247,252\nMvaa 156,156,188\nFM 49,247,253\n"
"Mvaa 152,148,188\nFM 56,251,252\nFM 57,251,253\nMvaa 152,144,184\nv_lshlrev_b32 v252, 16, v221\nv_and_b32 v253, 0xffff0000, v221\nMvaa 92,136,184\nFM 50,247,252\nFM 51,247,253\n"
"Mvaa 92,140,188\nD0 9,24,44,3072\nFM 58,251,252\nMvaa 88,132,188\nFM 59,251,253\nv_lshlrev_b32 v252, 16, v222\nMvaa 88,128,184\nWL 0\nMaaa 0,192,160\nRa 128,10,0\n"
"v_and_b32 v253, 0xffff0000, v222\nMaaa 0,196,164\nbuffer_load_dwordx4 v[236:239], v20, s[92:95], s88 offen offset:2688\nMaaa 4,204,164\nRa 132,11,0\ns_add_u32 m0, s47, 49152\n"
"s_add_u32 s44, s44, 0x40000\nMaaa 4,200,160\nFM 52,247,252\nFM 53,247,253\nMaaa 64,208,160\nRa 136,10,2048\nFM 60,251,252\nMaaa 64,212,164\nD0 6,24,44,0\nFM 61,251,253\n"
"Maaa 68,220,164\nRa 140,11,2048\nv_lshlrev_b32 v252, 16, v223\nMaaa 68,216,160\nv_and_b32 v253, 0xffff0000, v223\nFM 54,247,252\nMaaa 76,216,168\nRa 144,10,8192\nFM 55,247,253\n"
"Maaa 76,220,172\nFM 62,251,252\nMaaa 72,212,172\nRa 148,11,8192\nFM 63,251,253\nMaaa 72,208,168\nv_permlane32_swap_b32 v32, v48\nv_permlane32_swap_b32 v33, v49\nMaaa 12,200,168\n"
"Ra 152,10,10240\nv_permlane32_swap_b32 v34, v50\nMaaa 12,204,172\nD0 7,24,44,1024\nv_permlane32_swap_b32 v35, v51\nMaaa 8,196,172\nRa 156,11,10240\nv_permlane32_swap_b32 v36, v52\n"
"Maaa 8,192,168\nv_permlane32_swap_b32 v37, v53\nv_permlane32_swap_b32 v38, v54\nMaaa 16,192,176\nv_permlane32_swap_b32 v39, v55\nv_permlane32_swap_b32 v40, v56\nMaaa 16,196,180\n"
"v_permlane32_swap_b32 v41, v57\nMaaa 20,204,180\nv_permlane32_swap_b32 v42, v58\nv_permlane32_swap_b32 v43, v59\nMaaa 20,200,176\nv_permlane32_swap_b32 v44, v60\n"
"v_permlane32_swap_b32 v45, v61\nMaaa 80,208,176\nv_permlane32_swap_b32 v46, v62\nv_permlane32_swap_b32 v47, v63\nMaaa 80,212,180\nD0 8,24,44,2048\nAD 32,32,48\nMaaa 84,220,180\n"
"AD 33,33,49\nAD 34,34,50\nMaaa 84,216,176\nAD 35,35,51\nAD 36,36,52\nMaaa 92,216,184\nAD 37,37,53\nAD 38,38,54\nMaaa 92,220,188\nAD 39,39,55\nMaaa 88,212,188\nAD 40,40,56\n"
"AD 41,41,57\nMaaa 88,208,184\nAD 42,42,58\nAD 43,43,59\nMaaa 28,200,184\nAD 44,44,60\nAD 45,45,61\nMaaa 28,204,188\nD0 9,24,44,3072\nAD 46,46,62\nMaaa 24,196,188\nAD 47,47,63\n"
"Maaa 24,192,184\ns_waitcnt vmcnt(20) lgkmcnt(0)\ns_barrier\nMaaa 32,192,224\nTa 160,12,0\nv_permlane16_swap_b32 v32, v40\nMaaa 32,196,228\nTa 162,12,2048\nMaaa 36,204,228\n"
"s_add_u32 m0, s47, -16384\ns_add_u32 s48, s48, 128\nMaaa 36,200,224\nTa 164,12,16384\nv_permlane16_swap_b32 v33, v41\nMaaa 96,208,224\nTa 166,12,18432\n"
"v_permlane16_swap_b32 v34, v42\nMaaa 96,212,228\nD0 2,20,48,0\nv_permlane16_swap_b32 v35, v43\nMaaa 100,220,228\nTa 168,13,0\nv_permlane16_swap_b32 v36, v44\nMaaa 100,216,224\n"
"Ta 170,13,2048\nv_permlane16_swap_b32 v37, v45\nMaaa 108,216,232\nv_permlane16_swap_b32 v38, v46\nv_permlane16_swap_b32 v39, v47\nMaaa 108,220,236\nTa 172,13,16384\n"
"Maaa 104,212,236\nTa 174,13,18432\nAD 32,32,40\nMaaa 104,208,232\nAD 33,33,41\nAD 34,34,42\nMaaa 44,200,232\nTa 176,14,0\nAD 35,35,43\nMaaa 44,204,236\nTa 178,14,2048\n"
"D0 3,20,48,1024\nMaaa 40,196,236\nAD 36,36,44\nAD 37,37,45\nMaaa 40,192,232\nTa 180,14,16384\nAD 38,38,46\nMaaa 48,192,240\nTa 182,14,18432\nAD 39,39,47\nMaaa 48,196,244\n"
"v_add_f32_dpp v32, v32, v32 row_ror:8 row_mask:0xf bank_mask:0xf\nMaaa 52,204,244\nTa 184,15,0\nv_add_f32_dpp v33, v33, v33 row_ror:8 row_mask:0xf bank_mask:0xf\nMaaa 52,200,240\n"
"Ta 186,15,2048\nv_add_f32_dpp v34, v34, v34 row_ror:8 row_mask:0xf bank_mask:0xf\nMaaa 112,208,240\nv_add_f32_dpp v35, v35, v35 row_ror:8 row_mask:0xf bank_mask:0xf\n"
"v_add_f32_dpp v36, v36, v36 row_ror:8 row_mask:0xf bank_mask:0xf\nMaaa 112,212,244\nTa 188,15,16384\nD0 4,20,48,2048\nMaaa 116,220,244\nTa 190,15,18432\n"
"v_add_f32_dpp v37, v37, v37 row_ror:8 row_mask:0xf bank_mask:0xf\nMaaa 116,216,240\nv_add_f32_dpp v38, v38, v38 row_ror:8 row_mask:0xf bank_mask:0xf\n"
"v_add_f32_dpp v39, v39, v39 row_ror:8 row_mask:0xf bank_mask:0xf\nMaaa 124,216,248\nv_add_f32_dpp v32, v32, v32 row_ror:4 row_mask:0xf bank_mask:0xf\n"
"v_add_f32_dpp v33, v33, v33 row_ror:4 row_mask:0xf bank_mask:0xf\nMaaa 124,220,252\nv_add_f32_dpp v34, v34, v34 row_ror:4 row_mask:0xf bank_mask:0xf\nMaaa 120,212,252\n"
"v_add_f32_dpp v35, v35, v35 row_ror:4 row_mask:0xf bank_mask:0xf\nv_add_f32_dpp v36, v36, v36 row_ror:4 row_mask:0xf bank_mask:0xf\nMaaa 120,208,248\n"
"v_add_f32_dpp v37, v37, v37 row_ror:4 row_mask:0xf bank_mask:0xf\nv_add_f32_dpp v38, v38, v38 row_ror:4 row_mask:0xf bank_mask:0xf\nMaaa 60,200,248\n"
"v_add_f32_dpp v39, v39, v39 row_ror:4 row_mask:0xf bank_mask:0xf\nv_add_f32_dpp v32, v32, v32 row_ror:2 row_mask:0xf bank_mask:0xf\nMaaa 60,204,252\nD0 5,20,48,3072\n"
"v_add_f32_dpp v33, v33, v33 row_ror:2 row_mask:0xf bank_mask:0xf\nMaaa 56,196,252\nv_add_f32_dpp v34, v34, v34 row_ror:2 row_mask:0xf bank_mask:0xf\n"
"v_add_f32_dpp v35, v35, v35 row_ror:2 row_mask:0xf bank_mask:0xf\nMaaa 56,192,248\nWL 0\nMvaa 64,128,160\nTa 224,16,0\n"
"v_add_f32_dpp v36, v36, v36 row_ror:2 row_mask:0xf bank_mask:0xf\nMvaa 64,132,164\nTa 226,16,2048\nMvaa 68,140,164\ns_add_u32 m0, s47, -65536\ns_add_u32 s45, s45, 128\n"
"Mvaa 68,136,160\nTa 228,16,16384\nv_add_f32_dpp v37, v37, v37 row_ror:2 row_mask:0xf bank_mask:0xf\nMvaa 128,144,160\nTa 230,16,18432\n"
"v_add_f32_dpp v38, v38, v38 row_ror:2 row_mask:0xf bank_mask:0xf\nMvaa 128,148,164\nD0 2,20,45,0\nv_add_f32_dpp v39, v39, v39 row_ror:2 row_mask:0xf bank_mask:0xf\n"
"Mvaa 132,156,164\nTa 232,17,0\nv_lshrrev_b32 v48, 4, v0\nMvaa 132,152,160\nTa 234,17,2048\nv_and_b32 v49, 1, v0\nMvaa 140,152,168\nv_lshlrev_b32 v49, 5, v49\n"
"v_lshl_add_u32 v49, v48, 6, v49\nMvaa 140,156,172\nTa 236,17,16384\nMvaa 136,148,172\nTa 238,17,18432\ns_lshl_b32 s2, s11, 8\nMvaa 136,144,168\ns_add_u32 s2, s2, 132096\n"
"v_add_u32 v49, s2, v49\nMvaa 76,136,168\nTa 240,18,0\nds_write_b128 v49, v[32:35]\nMvaa 76,140,172\nTa 242,18,2048\nD0 3,20,45,1024\nMvaa 72,132,172\n"
"ds_write_b128 v49, v[36:39] offset:16\nv_and_b32 v50, 31, v0\nMvaa 72,128,168\nTa 244,18,16384\nv_lshlrev_b32 v50, 2, v50\nMvaa 80,128,176\nTa 246,18,18432\n"
"s_lshl_b32 s2, s11, 10\nMvaa 80,132,180\ns_add_u32 s2, s2, 133120\nMvaa 84,140,180\nTa 248,19,0\nv_add_u32 v51, s2, v50\nMvaa 84,136,176\nTa 250,19,2048\n"
"v_add_u32 v50, 132096, v50\nMvaa 144,144,176\nv_lshl_add_u32 v52, v48, 4, s2\nMvaa 144,148,180\nTa 252,19,16384\nD0 4,20,45,2048\nMvaa 148,156,180\nTa 254,19,18432\n"
"Mvaa 148,152,176\nMvaa 156,152,184\nMvaa 156,156,188\nMvaa 152,148,188\nMvaa 152,144,184\nMvaa 92,136,184\nMvaa 92,140,188\nD0 5,20,45,3072\nMvaa 88,132,188\nMvaa 88,128,184\n"
"s_waitcnt vmcnt(18) lgkmcnt(0)\ns_barrier\nMvaa 96,128,224\nRa 192,10,16384\nds_read_b32 v32, v50 offset:0\nMvaa 96,132,228\nds_read_b32 v33, v50 offset:256\nMvaa 100,140,228\n"
"Ra 196,11,16384\ns_add_u32 m0, s47, 0\ns_add_u32 s44, s44, 0x40000\nMvaa 100,136,224\nds_read_b32 v34, v50 offset:512\nds_read_b32 v35, v50 offset:768\nMvaa 160,144,224\n"
"Ra 200,10,18432\nMvaa 160,148,228\nD0 6,24,44,0\nMvaa 164,156,228\nRa 204,11,18432\nds_read_b32 v36, v50 offset:128\nMvaa 164,152,224\nds_read_b32 v37, v50 offset:384\n"
"Mvaa 172,152,232\nRa 208,10,24576\nds_read_b32 v38, v50 offset:640\nMvaa 172,156,236\nds_read_b32 v39, v50 offset:896\nMvaa 168,148,236\nRa 212,11,24576\nMvaa 168,144,232\n"
"Mvaa 108,136,232\nRa 216,10,26624\nMvaa 108,140,236\nD0 7,24,44,1024\nMvaa 104,132,236\nRa 220,11,26624\nMvaa 104,128,232\nMvaa 112,128,240\nMvaa 112,132,244\nMvaa 116,140,244\n"
"Mvaa 116,136,240\nWL 3\nAD 32,32,33\nMvaa 176,144,240\nAD 34,34,35\nMvaa 176,148,244\nD0 8,24,44,2048\nMvaa 180,156,244\nAD 36,36,37\nAD 38,38,39\nMvaa 180,152,240\nAD 32,32,34\n"
"Mvaa 188,152,248\nAD 36,36,38\nCV 32,32,36\nMvaa 188,156,252\nv_and_b32 v36, 0xffff0000, v32\nMvaa 184,148,252\nv_lshlrev_b32 v32, 16, v32\nMvaa 184,144,248\n"
"v_mul_f32 v33, 0xbfb8aa3b, v32\nMvaa 124,136,248\nEX 33,33\nMvaa 124,140,252\nD0 9,24,44,3072\nMvaa 120,132,252\nv_add_f32 v33, 1.0, v33\nMvaa 120,128,248\nWL 0\nMaaa 32,192,224\n"
"Ra 128,10,32768\nMaaa 32,196,228\nv_rcp_f32 v33, v33\nMaaa 36,204,228\nRa 132,11,32768\ns_add_u32 m0, s47, 16384\ns_add_u32 s44, s44, 0x40000\nMaaa 36,200,224\nMU 33,32,33\n"
"CV 33,33,33\nMaaa 96,208,224\nRa 136,10,34816\nMaaa 96,212,228\nD0 6,24,44,0\nMaaa 100,220,228\nRa 140,11,34816\nv_lshlrev_b32 v33, 16, v33\nMaaa 100,216,224\nMU 33,33,36\n"
"Maaa 108,216,232\nRa 144,10,40960\nCV 33,33,33\nMaaa 108,220,236\nv_lshlrev_b32 v33, 16, v33\nMaaa 104,212,236\nRa 148,11,40960\nMaaa 104,208,232\nds_write_b32 v51, v33\n"
"Rv 244,52,0\nMaaa 44,200,232\nRa 152,10,43008\nMaaa 44,204,236\nD0 7,24,44,1024\nMaaa 40,196,236\nRa 156,11,43008\nRv 248,52,64\nMaaa 40,192,232\ns_and_b32 s2, s74, 15\n"
"Maaa 48,192,240\ns_sub_u32 s2, s2, 2\nMaaa 48,196,244\ns_cmp_lt_u32 s2, 14\ns_cselect_b32 s5, 0, 0x40000000\nMaaa 52,204,244\ns_and_b32 s2, s2, 15\nMaaa 52,200,240\n"
"s_mul_i32 s4, s73, 14\ns_add_u32 s4, s4, s2\nMaaa 112,208,240\nv_and_b32 v49, 15, v0\nMaaa 112,212,244\nD0 8,24,44,2048\nMaaa 116,220,244\nv_lshlrev_b32 v50, 3, v49\n"
"v_and_b32 v51, 1, v48\nMaaa 116,216,240\nv_lshl_add_u32 v50, v51, 2, v50\nMaaa 124,216,248\ns_mul_i32 s2, s11, 112\nv_add_u32 v50, s2, v50\nMaaa 124,220,252\n"
"v_lshrrev_b32 v51, 4, v50\nMaaa 120,212,252\nv_lshrrev_b32 v48, 1, v48\nMaaa 120,208,248\nv_mad_u32_u24 v51, v48, 28, v51\nv_mul_u32_u24 v51, 7168, v51\nMaaa 60,200,248\n"
"v_and_b32 v50, 15, v50\nMaaa 60,204,252\nD0 9,24,44,3072\nMaaa 56,196,252\nv_lshl_add_u32 v51, v50, 2, v51\nv_cmp_gt_u32 vcc, 14, v49\ns_lshl_b32 s4, s4, 6\n"
"s_add_u32 s4, s4, 0xc04c800\nv_cndmask_b32 v51, v254, v51, vcc\nMaaa 56,192,248\ns_waitcnt vmcnt(16) lgkmcnt(1)\ns_barrier\nMaaa 0,192,160\nTa 224,16,32768\ns_or_b32 s4, s4, s5\n"
"Maaa 0,196,164\nTa 226,16,34816\nMaaa 4,204,164\ns_add_u32 m0, s47, -49152\ns_add_u32 s48, s48, 128\nMaaa 4,200,160\nTa 228,16,49152\nWL 3\nMaaa 64,208,160\nTa 230,16,51200\n"
"Maaa 64,212,164\nD0 2,20,48,0\nMaaa 68,220,164\nTa 232,17,32768\nv_lshlrev_b32 v252, 16, v224\nMaaa 68,216,160\nTa 234,17,34816\nMaaa 76,216,168\nv_and_b32 v253, 0xffff0000, v224\n"
"MU 32,244,252\nMaaa 76,220,172\nTa 236,17,49152\nMaaa 72,212,172\nTa 238,17,51200\nMaaa 72,208,168\nMU 33,244,253\nMU 40,248,252\nMaaa 12,200,168\nTa 240,18,32768\n"
"Maaa 12,204,172\nTa 242,18,34816\nD0 3,20,48,1024\nMaaa 8,196,172\nMU 41,248,253\nv_lshlrev_b32 v252, 16, v225\nMaaa 8,192,168\nTa 244,18,49152\nMaaa 16,192,176\nTa 246,18,51200\n"
"v_and_b32 v253, 0xffff0000, v225\nMaaa 16,196,180\nMU 34,244,252\nMaaa 20,204,180\nTa 248,19,32768\nMaaa 20,200,176\nTa 250,19,34816\nMU 35,244,253\nMaaa 80,208,176\n"
"MU 42,248,252\nMaaa 80,212,180\nTa 252,19,49152\nD0 4,20,48,2048\nMaaa 84,220,180\nTa 254,19,51200\nMU 43,248,253\nMaaa 84,216,176\nv_lshlrev_b32 v252, 16, v226\nMaaa 92,216,184\n"
"v_and_b32 v253, 0xffff0000, v226\nMU 36,244,252\nMaaa 92,220,188\nMU 37,244,253\nMaaa 88,212,188\nMU 44,248,252\nMaaa 88,208,184\nMU 45,248,253\nv_lshlrev_b32 v252, 16, v227\n"
"Maaa 28,200,184\nv_and_b32 v253, 0xffff0000, v227\nMaaa 28,204,188\nD0 5,20,48,3072\nMaaa 24,196,188\nMU 38,244,252\nMU 39,244,253\nMaaa 24,192,184\nWL 0\nMvaa 96,128,224\n"
"Ta 160,12,32768\nMU 46,248,252\nMvaa 96,132,228\nTa 162,12,34816\nMvaa 100,140,228\ns_add_u32 m0, s47, -32768\ns_add_u32 s45, s45, 128\nMvaa 100,136,224\nTa 164,12,49152\n"
"MU 47,248,253\nMvaa 160,144,224\nTa 166,12,51200\nMvaa 160,148,228\nD0 2,20,45,0\nMvaa 164,156,228\nTa 168,13,32768\nv_lshlrev_b32 v252, 16, v228\nMvaa 164,152,224\n"
"Ta 170,13,34816\nMvaa 172,152,232\nv_and_b32 v253, 0xffff0000, v228\nFM 32,245,252\nMvaa 172,156,236\nTa 172,13,49152\nMvaa 168,148,236\nTa 174,13,51200\nMvaa 168,144,232\n"
"FM 33,245,253\nFM 40,249,252\nMvaa 108,136,232\nTa 176,14,32768\nMvaa 108,140,236\nTa 178,14,34816\nD0 3,20,45,1024\nMvaa 104,132,236\nFM 41,249,253\nv_lshlrev_b32 v252, 16, v229\n"
"Mvaa 104,128,232\nTa 180,14,49152\nMvaa 112,128,240\nTa 182,14,51200\nv_and_b32 v253, 0xffff0000, v229\nMvaa 112,132,244\nFM 34,245,252\nMvaa 116,140,244\nTa 184,15,32768\n"
"Mvaa 116,136,240\nTa 186,15,34816\nFM 35,245,253\nMvaa 176,144,240\nFM 42,249,252\nMvaa 176,148,244\nTa 188,15,49152\nD0 4,20,45,2048\nMvaa 180,156,244\nTa 190,15,51200\n"
"FM 43,249,253\nMvaa 180,152,240\nv_lshlrev_b32 v252, 16, v230\nMvaa 188,152,248\nv_and_b32 v253, 0xffff0000, v230\nFM 36,245,252\nMvaa 188,156,252\nFM 37,245,253\n"
"Mvaa 184,148,252\nFM 44,249,252\nMvaa 184,144,248\nFM 45,249,253\nv_lshlrev_b32 v252, 16, v231\nMvaa 124,136,248\nv_and_b32 v253, 0xffff0000, v231\nMvaa 124,140,252\n"
"D0 5,20,45,3072\nMvaa 120,132,252\nFM 38,245,252\nFM 39,245,253\nMvaa 120,128,248\ns_waitcnt vmcnt(16) lgkmcnt(0)\ns_barrier\nMvaa 64,128,160\nRa 192,10,49152\nFM 46,249,252\n"
"Mvaa 64,132,164\nFM 47,249,253\nMvaa 68,140,164\nRa 196,11,49152\ns_add_u32 m0, s47, 32768\ns_add_u32 s44, s44, 0x40000\nMvaa 68,136,160\nv_lshlrev_b32 v252, 16, v232\n"
"v_and_b32 v253, 0xffff0000, v232\nMvaa 128,144,160\nRa 200,10,51200\nMvaa 128,148,164\nD0 6,24,44,0\nMvaa 132,156,164\nRa 204,11,51200\nFM 32,246,252\nMvaa 132,152,160\n"
"FM 33,246,253\nMvaa 140,152,168\nRa 208,10,57344\nFM 40,250,252\nMvaa 140,156,172\nFM 41,250,253\nMvaa 136,148,172\nRa 212,11,57344\nMvaa 136,144,168\n"
"v_lshlrev_b32 v252, 16, v233\nv_and_b32 v253, 0xffff0000, v233\nMvaa 76,136,168\nRa 216,10,59392\nMvaa 76,140,172\nD0 7,24,44,1024\nMvaa 72,132,172\nRa 220,11,59392\n"
"FM 34,246,252\nMvaa 72,128,168\nFM 35,246,253\nMvaa 80,128,176\nFM 42,250,252\nFM 43,250,253\nMvaa 80,132,180\nv_lshlrev_b32 v252, 16, v234\nMvaa 84,140,180\n"
"v_and_b32 v253, 0xffff0000, v234\nMvaa 84,136,176\nFM 36,246,252\nFM 37,246,253\nMvaa 144,144,176\nFM 44,250,252\nMvaa 144,148,180\nD0 8,24,44,2048\nMvaa 148,156,180\n"
"FM 45,250,253\nv_lshlrev_b32 v252, 16, v235\nMvaa 148,152,176\nv_and_b32 v253, 0xffff0000, v235\nMvaa 156,152,184\nFM 38,246,252\nFM 39,246,253\nMvaa 156,156,188\nFM 46,250,252\n"
"Mvaa 152,148,188\nFM 47,250,253\nMvaa 152,144,184\nv_lshlrev_b32 v252, 16, v236\nv_and_b32 v253, 0xffff0000, v236\nMvaa 92,136,184\nFM 32,247,252\nMvaa 92,140,188\n"
"D0 9,24,44,3072\nMvaa 88,132,188\nFM 33,247,253\nFM 40,251,252\nMvaa 88,128,184\nWL 0\nMaaa 0,192,160\nRa 128,10,0\nFM 41,251,253\nMaaa 0,196,164\nv_lshlrev_b32 v252, 16, v237\n"
"Maaa 4,204,164\nRa 132,11,0\ns_add_u32 m0, s47, 49152\ns_add_u32 s44, s44, 0x40000\nMaaa 4,200,160\nv_and_b32 v253, 0xffff0000, v237\nFM 34,247,252\nMaaa 64,208,160\n"
"Ra 136,10,2048\nMaaa 64,212,164\nD0 6,24,44,0\nMaaa 68,220,164\nRa 140,11,2048\nFM 35,247,253\nMaaa 68,216,160\nFM 42,251,252\nMaaa 76,216,168\nRa 144,10,8192\nFM 43,251,253\n"
"Maaa 76,220,172\nv_lshlrev_b32 v252, 16, v238\nMaaa 72,212,172\nRa 148,11,8192\nMaaa 72,208,168\nv_and_b32 v253, 0xffff0000, v238\nFM 36,247,252\nMaaa 12,200,168\nRa 152,10,10240\n"
"Maaa 12,204,172\nD0 7,24,44,1024\nMaaa 8,196,172\nRa 156,11,10240\nFM 37,247,253\nMaaa 8,192,168\nFM 44,251,252\nMaaa 16,192,176\nFM 45,251,253\nv_lshlrev_b32 v252, 16, v239\n"
"Maaa 16,196,180\nv_and_b32 v253, 0xffff0000, v239\nMaaa 20,204,180\nFM 38,247,252\nMaaa 20,200,176\nFM 39,247,253\nFM 46,251,252\nMaaa 80,208,176\nFM 47,251,253\nMaaa 80,212,180\n"
"D0 8,24,44,2048\nMaaa 84,220,180\nv_permlane32_swap_b32 v32, v40\nv_permlane32_swap_b32 v33, v41\nMaaa 84,216,176\nv_permlane32_swap_b32 v34, v42\nMaaa 92,216,184\n"
"v_permlane32_swap_b32 v35, v43\nv_permlane32_swap_b32 v36, v44\nMaaa 92,220,188\nv_permlane32_swap_b32 v37, v45\nMaaa 88,212,188\nv_permlane32_swap_b32 v38, v46\nMaaa 88,208,184\n"
"v_permlane32_swap_b32 v39, v47\nAD 32,32,40\nMaaa 28,200,184\nAD 33,33,41\nMaaa 28,204,188\nD0 9,24,44,3072\nMaaa 24,196,188\nAD 34,34,42\nAD 35,35,43\nMaaa 24,192,184\nWV 16\n"
"s_barrier\nMaaa 32,192,224\nTa 160,12,0\nAD 36,36,44\nMaaa 32,196,228\nTa 162,12,2048\nMaaa 36,204,228\ns_add_u32 m0, s47, -16384\ns_add_u32 s48, s48, 128\nMaaa 36,200,224\n"
"Ta 164,12,16384\nAD 37,37,45\nMaaa 96,208,224\nTa 166,12,18432\nMaaa 96,212,228\nD0 2,20,48,0\nMaaa 100,220,228\nTa 168,13,0\nAD 38,38,46\nMaaa 100,216,224\nTa 170,13,2048\n"
"Maaa 108,216,232\nAD 39,39,47\nMaaa 108,220,236\nTa 172,13,16384\nMaaa 104,212,236\nTa 174,13,18432\nMaaa 104,208,232\nv_permlane16_swap_b32 v32, v36\n"
"v_permlane16_swap_b32 v33, v37\nMaaa 44,200,232\nTa 176,14,0\nMaaa 44,204,236\nTa 178,14,2048\nD0 3,20,48,1024\nMaaa 40,196,236\nv_permlane16_swap_b32 v34, v38\n"
"v_permlane16_swap_b32 v35, v39\nMaaa 40,192,232\nTa 180,14,16384\nMaaa 48,192,240\nTa 182,14,18432\nAD 32,32,36\nMaaa 48,196,244\nAD 33,33,37\nMaaa 52,204,244\nTa 184,15,0\n"
"Maaa 52,200,240\nTa 186,15,2048\nAD 34,34,38\nMaaa 112,208,240\nAD 35,35,39\nMaaa 112,212,244\nTa 188,15,16384\nD0 4,20,48,2048\nMaaa 116,220,244\nTa 190,15,18432\n"
"Sv 32,51,16,4,0\nMaaa 116,216,240\nMaaa 124,216,248\nMaaa 124,220,252\nMaaa 120,212,252\nMaaa 120,208,248\nMaaa 60,200,248\nMaaa 60,204,252\nD0 5,20,48,3072\nMaaa 56,196,252\n"
"Maaa 56,192,248\nWL 0\nMvaa 64,128,160\nTa 224,16,0\ns_lshr_b32 s3, s11, 1\nMvaa 64,132,164\nTa 226,16,2048\ns_add_u32 s6, s56, 0\nMvaa 68,140,164\ns_lshl_b32 s3, s3, 13\n"
"Mvaa 68,136,160\nTa 228,16,16384\nbuffer_load_dwordx4 v[192:195], v1, s[16:19], s6 offen offset:0 sc1\nMvaa 128,144,160\nTa 230,16,18432\nMvaa 128,148,164\ns_lshl_b32 s2, s11, 12\n"
"Mvaa 132,156,164\nTa 232,17,0\nMvaa 132,152,160\nTa 234,17,2048\nMvaa 140,152,168\ns_add_u32 s2, s2, s3\ns_add_u32 s2, s2, s72\nMvaa 140,156,172\nTa 236,17,16384\n"
"buffer_load_dwordx4 v[196:199], v1, s[16:19], s6 offen offset:1024 sc1\nMvaa 136,148,172\nTa 238,17,18432\nMvaa 136,144,168\ns_lshl_b32 s3, s11, 12\nMvaa 76,136,168\nTa 240,18,0\n"
"Mvaa 76,140,172\nTa 242,18,2048\nMvaa 72,132,172\ns_add_u32 s89, s3, 0x0\nMvaa 72,128,168\nTa 244,18,16384\nbuffer_load_dwordx4 v[200:203], v1, s[16:19], s6 offen offset:2048 sc1\n"
"Mvaa 80,128,176\nTa 246,18,18432\ns_mov_b32 s88, 0xffff8000\nMvaa 80,132,180\ns_lshl_b32 s4, s13, 1\nMvaa 84,140,180\nTa 248,19,0\nMvaa 84,136,176\nTa 250,19,2048\n"
"Mvaa 144,144,176\ns_sub_u32 s79, 15, s4\nMvaa 144,148,180\nTa 252,19,16384\nbuffer_load_dwordx4 v[204:207], v1, s[16:19], s6 offen offset:3072 sc1\nMvaa 148,156,180\n"
"Ta 254,19,18432\nMvaa 148,152,176\ns_add_u32 s96, s2, 0x1310000\nMvaa 156,152,184\ns_mov_b32 s63, 0x53800000\ns_mov_b32 s64, 0x2b800000\nMvaa 156,156,188\ns_sub_u32 s98, s4, 1\n"
"s_add_u32 s6, s56, 4096\nMvaa 152,148,188\ns_lshl_b32 s5, s13, 5\nMvaa 152,144,184\ns_add_u32 s5, s5, s11\nbuffer_load_dwordx4 v[208:211], v1, s[16:19], s6 offen offset:0 sc1\n"
"Mvaa 92,136,184\ns_lshl_b32 s65, s5, 9\nMvaa 92,140,188\ns_add_u32 s65, s65, s72\nMvaa 88,132,188\ns_add_u32 s65, s65, 0x1580000\nMvaa 88,128,184\ns_waitcnt vmcnt(18) lgkmcnt(0)\n"
"s_barrier\nMvaa 96,128,224\nRa 192,10,16384\ns_and_b32 s66, s72, 0x2000\nMvaa 96,132,228\ns_add_u32 s66, s66, s56\n"
"buffer_load_dwordx4 v[212:215], v1, s[16:19], s6 offen offset:1024 sc1\nMvaa 100,140,228\nRa 196,11,16384\nMvaa 100,136,224\ns_add_u32 s66, s66, 0x100000\nMvaa 160,144,224\n"
"Ra 200,10,18432\nMvaa 160,148,228\ns_and_b32 s5, s12, 1\nMvaa 164,156,228\nRa 204,11,18432\nMvaa 164,152,224\ns_mul_i32 s58, s5, 0x202000\n"
"buffer_load_dwordx4 v[216:219], v1, s[16:19], s6 offen offset:2048 sc1\nMvaa 172,152,232\nRa 208,10,24576\ns_xor_b32 s58, s58, s72\nMvaa 172,156,236\n"
"s_add_u32 s58, s58, 0x15c0000\nMvaa 168,148,236\nRa 212,11,24576\nMvaa 168,144,232\ns_lshl_b32 s5, s5, 8\nMvaa 108,136,232\nRa 216,10,26624\nMvaa 108,140,236\n"
"s_add_u32 s61, s58, s5\nbuffer_load_dwordx4 v[220:223], v1, s[16:19], s6 offen offset:3072 sc1\nMvaa 104,132,236\nRa 220,11,26624\nMvaa 104,128,232\ns_lshl4_add_u32 s60, s13, s61\n"
"Mvaa 112,128,240\ns_add_u32 s54, s60, 0\ns_xor_b32 s60, s54, 0x100\nMvaa 112,132,244\ns_cmp_eq_u32 s11, 0\ns_cselect_b32 s54, s54, 0x40000000\ns_add_u32 s6, s56, 16384\n"
"Mvaa 116,140,244\ns_getpc_b64 s[8:9]\n.PC171:\ns_add_u32 s8, s8, .HD148-.PC171\ns_addc_u32 s9, s9, 0\nMvaa 116,136,240\ns_mov_b32 s97, 0xff800000\n"
"buffer_load_dwordx4 v[224:227], v1, s[16:19], s6 offen offset:0 sc1\nMvaa 176,144,240\ns_mov_b32 s90, 3\ns_cmp_lt_u32 s11, 2\ns_cselect_b32 s92, 2, 0\nMvaa 176,148,244\n"
"s_add_u32 s86, s2, 0x20000\nMvaa 180,156,244\ns_add_u32 s87, s2, 0x410000\ns_cmp_eq_u32 s13, 7\ns_cselect_b32 s92, s92, 0\nMvaa 180,152,240\ns_sub_u32 s4, s13, 2\n"
"s_cmp_lt_u32 s4, 4\ns_cselect_b32 s4, s12, 15\nMvaa 188,152,248\ns_cmp_lt_u32 s4, 14\ns_cselect_b32 s5, s11, 1\nMvaa 188,156,252\ns_cmp_eq_u32 s5, 0\ns_cselect_b32 s91, 1, 100\n"
"buffer_load_dwordx4 v[228:231], v1, s[16:19], s6 offen offset:1024 sc1\nMvaa 184,148,252\nv_and_b32 v27, 0x70, v1\n"
"buffer_load_dword v27, v27, s[16:19], s61 offen offset:128 sc0 sc1\nMvaa 184,144,248\ns_lshr_b32 s2, s13, 2\ns_bfe_u32 s3, s13, 0x10001\nMvaa 124,136,248\ns_mul_i32 s3, s3, 17\n"
"s_add_u32 s2, s2, s3\nMvaa 124,140,252\ns_lshl2_add_u32 s3, s14, 0\nMvaa 120,132,252\ns_lshl_b32 s3, s3, 2\ns_lshr_b32 s3, 0xe420ca86, s3\nMvaa 120,128,248\nWL 0\nMaaa 32,192,224\n"
"Ra 128,10,32768\ns_and_b32 s3, s3, 15\nbuffer_load_dwordx4 v[232:235], v1, s[16:19], s6 offen offset:2048 sc1\nMaaa 32,196,228\ns_add_u32 s2, s2, s3\ns_cmp_eq_u32 s2, 32\n"
"s_cselect_b32 s4, 16, s2\nMaaa 36,204,228\nRa 132,11,32768\ns_mul_i32 s75, s4, 65536\nMaaa 36,200,224\ns_lshr_b32 s2, s13, 2\ns_bfe_u32 s3, s13, 0x10001\nMaaa 96,208,224\n"
"Ra 136,10,34816\nMaaa 96,212,228\ns_mul_i32 s3, s3, 17\ns_add_u32 s2, s2, s3\nMaaa 100,220,228\nRa 140,11,34816\ns_lshl2_add_u32 s3, s14, 1\n"
"buffer_load_dwordx4 v[236:239], v1, s[16:19], s6 offen offset:3072 sc1\nMaaa 100,216,224\ns_lshl_b32 s3, s3, 2\nMaaa 108,216,232\nRa 144,10,40960\ns_lshr_b32 s3, 0xe420ca86, s3\n"
"Maaa 108,220,236\ns_and_b32 s3, s3, 15\ns_add_u32 s2, s2, s3\nMaaa 104,212,236\nRa 148,11,40960\ns_cmp_eq_u32 s2, 32\ns_cselect_b32 s4, 16, s2\ns_add_u32 s6, s56, 20480\n"
"Maaa 104,208,232\ns_mul_i32 s76, s4, 65536\ns_lshr_b32 s2, s13, 2\nMaaa 44,200,232\nRa 152,10,43008\ns_bfe_u32 s3, s13, 0x10001\n"
"buffer_load_dwordx4 v[240:243], v1, s[16:19], s6 offen offset:0 sc1\nMaaa 44,204,236\ns_mul_i32 s3, s3, 17\nMaaa 40,196,236\nRa 156,11,43008\ns_add_u32 s2, s2, s3\n"
"Maaa 40,192,232\ns_lshl2_add_u32 s3, s14, 2\ns_lshl_b32 s3, s3, 2\nMaaa 48,192,240\ns_lshr_b32 s3, 0xe420ca86, s3\ns_and_b32 s3, s3, 15\nMaaa 48,196,244\ns_add_u32 s2, s2, s3\n"
"s_cmp_eq_u32 s2, 32\ns_cselect_b32 s4, 16, s2\nMaaa 52,204,244\ns_mul_i32 s77, s4, 65536\ns_lshr_b32 s2, s13, 2\n"
"buffer_load_dwordx4 v[244:247], v1, s[16:19], s6 offen offset:1024 sc1\nMaaa 52,200,240\ns_bfe_u32 s3, s13, 0x10001\ns_mul_i32 s3, s3, 17\nMaaa 112,208,240\ns_add_u32 s2, s2, s3\n"
"Maaa 112,212,244\ns_lshl2_add_u32 s3, s14, 3\ns_lshl_b32 s3, s3, 2\nMaaa 116,220,244\ns_lshr_b32 s3, 0xe420ca86, s3\ns_and_b32 s3, s3, 15\nMaaa 116,216,240\ns_add_u32 s2, s2, s3\n"
"s_cmp_eq_u32 s2, 32\ns_cselect_b32 s4, 16, s2\nMaaa 124,216,248\ns_mul_i32 s78, s4, 65536\ns_lshr_b32 s3, s11, 1\n"
"buffer_load_dwordx4 v[248:251], v1, s[16:19], s6 offen offset:2048 sc1\nMaaa 124,220,252\ns_lshl_b32 s3, s3, 15\ns_add_u32 s2, s72, s3\nMaaa 120,212,252\ns_and_b32 s3, s11, 1\n"
"Maaa 120,208,248\ns_lshl_b32 s3, s3, 14\ns_add_u32 s2, s2, s3\nMaaa 60,200,248\ns_add_u32 s2, s2, 0x400000\ns_mov_b32 s55, 4096\nMaaa 60,204,252\ns_add_u32 s75, s75, s2\n"
"Maaa 56,196,252\ns_add_u32 s76, s76, s2\ns_add_u32 s77, s77, s2\nbuffer_load_dwordx4 v[252:255], v1, s[16:19], s6 offen offset:3072 sc1\nMaaa 56,192,248\nWV 22\ns_barrier\n"
"Maaa 0,192,160\nTa 224,16,32768\ns_add_u32 s78, s78, s2\nMaaa 0,196,164\nTa 226,16,34816\nMaaa 4,204,164\nMaaa 4,200,160\nTa 228,16,49152\nMaaa 64,208,160\nTa 230,16,51200\n"
"Maaa 64,212,164\nMaaa 68,220,164\nTa 232,17,32768\nMaaa 68,216,160\nTa 234,17,34816\nMaaa 76,216,168\nWV 6\nv_cmp_ne_u32 vcc, s15, v27\ns_nop 0\ns_cbranch_vccnz .S172\n.S173:\n"
"s_sub_u32 s45, s86, 131072\nMaaa 76,220,172\nTa 236,17,49152\ns_add_u32 m0, s47, 0\ns_add_u32 s46, s45, 0x0\nMaaa 72,212,172\nTa 238,17,51200\nMaaa 72,208,168\nD1 1,16,46,0\n"
"s_mov_b32 s36, s68\nMaaa 12,200,168\nTa 240,18,32768\nMaaa 12,204,172\nTa 242,18,34816\nMaaa 8,196,172\ns_and_b32 s37, s69, 0xffff\nMaaa 8,192,168\nTa 244,18,49152\n"
"Maaa 16,192,176\nTa 246,18,51200\nD1 1,16,46,1024\nMaaa 16,196,180\ns_mov_b32 s38, 0x1c0000\nMaaa 20,204,180\nTa 248,19,32768\nMaaa 20,200,176\nTa 250,19,34816\nMaaa 80,208,176\n"
"s_mov_b32 s39, 0x20000\nMaaa 80,212,180\nTa 252,19,49152\nD1 1,16,46,2048\nMaaa 84,220,180\nTa 254,19,51200\ns_and_b32 s2, s74, 15\nMaaa 84,216,176\ns_max_u32 s2, s2, 2\n"
"Maaa 92,216,184\ns_sub_u32 s2, s2, 2\nMaaa 92,220,188\ns_mul_i32 s3, s73, 14\nMaaa 88,212,188\nD1 1,16,46,3072\nMaaa 88,208,184\ns_add_u32 s2, s2, s3\ns_lshl2_add_u32 s2, s2, s11\n"
"Maaa 28,200,184\ns_mul_i32 s3, s2, 4682\nMaaa 28,204,188\ns_lshr_b32 s3, s3, 16\nMaaa 24,196,188\ns_mul_i32 s22, s3, 14\nMaaa 24,192,184\nWL 0\nMvaa 96,128,224\nTa 160,12,32768\n"
"s_add_u32 m0, s47, -65536\ns_add_u32 s46, s45, 0x10000\nD1 1,16,46,0\nMvaa 96,132,228\nTa 162,12,34816\nMvaa 100,140,228\ns_sub_u32 s22, s2, s22\nMvaa 100,136,224\n"
"Ta 164,12,49152\nMvaa 108,136,232\nTa 166,12,51200\nMvaa 108,140,236\nD1 1,16,46,1024\nMvaa 104,132,236\nTa 168,13,32768\ns_and_b32 s21, s3, 28\nMvaa 104,128,232\nTa 170,13,34816\n"
"Mvaa 112,128,240\ns_add_u32 s21, s21, s3\nMvaa 112,132,244\nTa 172,13,49152\nMvaa 116,140,244\nTa 174,13,51200\nD1 1,16,46,2048\nMvaa 116,136,240\ns_lshl_b32 s21, s21, 4\n"
"s_add_u32 s21, s21, 1024\nMvaa 124,136,248\nTa 176,14,32768\nCV 48,96,97\nMvaa 124,140,252\nTa 178,14,34816\nMvaa 120,132,252\ns_xor_b32 s3, s3, 28\nCV 49,98,99\nMvaa 120,128,248\n"
"Ta 180,14,49152\nD1 1,16,46,3072\nMvaa 160,144,224\nTa 182,14,51200\ns_bfe_u32 s41, s3, 0x30002\nMvaa 160,148,228\ns_cmp_gt_u32 s22, 6\ns_cselect_b32 s2, 1179648, 0\n"
"s_cselect_b32 s1, 16, 0\nMvaa 164,156,228\nTa 184,15,32768\nMvaa 164,152,224\nTa 186,15,34816\nCV 50,100,101\nMvaa 172,152,232\ns_add_u32 s41, s41, s1\nMvaa 172,156,236\n"
"Ta 188,15,49152\nCV 51,102,103\nMvaa 168,148,236\nTa 190,15,51200\ns_lshl4_add_u32 s41, s41, 0xc048400\nMvaa 168,144,232\ns_and_b32 s1, s3, 28\nCV 52,104,105\nMvaa 176,144,240\n"
"s_add_u32 s3, s3, s1\nMvaa 176,148,244\ns_lshl_b32 s3, s3, 11\nMvaa 180,156,244\ns_add_u32 s2, s2, s3\nCV 53,106,107\nMvaa 180,152,240\ns_lshl_b32 s3, s22, 17\n"
"s_add_u32 s2, s2, s3\nMvaa 188,152,248\ns_add_u32 s20, s2, 0xce0000\nCV 54,108,109\nMvaa 188,156,252\ns_lshl_b32 s2, s22, 7\nMvaa 184,148,252\ns_add_u32 s40, s2, 0xc04b800\n"
"CV 55,110,111\nMvaa 184,144,248\nWL 0\ns_barrier\nMvaa 64,128,160\nRa 192,10,49152\ns_add_u32 m0, s47, 32768\ns_add_u32 s46, s45, 0x8000\nMvaa 64,132,164\ns_mul_i32 s2, s21, 896\n"
"Mvaa 68,140,164\nRa 196,11,49152\nD1 1,16,46,0\nMvaa 68,136,160\ns_lshl_b32 s3, s22, 6\nCV 56,112,113\nMvaa 76,136,168\nRa 200,10,51200\nMvaa 76,140,172\ns_add_u32 s22, s2, s3\n"
"CV 57,114,115\nMvaa 72,132,172\nRa 204,11,51200\ns_add_u32 s23, s22, 57344\nMvaa 72,128,168\nD1 1,16,46,1024\nCV 58,116,117\nMvaa 80,128,176\nRa 208,10,57344\nMvaa 80,132,180\n"
"CV 59,118,119\nMvaa 84,140,180\nRa 212,11,57344\nCV 60,120,121\nMvaa 84,136,176\nCV 61,122,123\nMvaa 92,136,184\nRa 216,10,59392\nD1 1,16,46,2048\nMvaa 92,140,188\nCV 62,124,125\n"
"Mvaa 88,132,188\nRa 220,11,59392\nCV 63,126,127\nMvaa 88,128,184\nCV 32,64,65\nMvaa 128,144,160\nCV 33,66,67\nCV 34,68,69\nMvaa 128,148,164\nD1 1,16,46,3072\nMvaa 132,156,164\n"
"CV 35,70,71\nMvaa 132,152,160\nCV 36,72,73\nCV 37,74,75\nMvaa 140,152,168\ns_add_u32 m0, s47, 49152\ns_add_u32 s46, s45, 0x400000\nMvaa 140,156,172\nCV 38,76,77\nCV 39,78,79\n"
"Mvaa 136,148,172\nD1 1,16,46,0\nMvaa 136,144,168\nCV 40,80,81\nCV 41,82,83\nMvaa 144,144,176\nCV 42,84,85\nMvaa 144,148,180\nCV 43,86,87\nMvaa 148,156,180\nCV 44,88,89\n"
"CV 45,90,91\nMvaa 148,152,176\nD1 1,16,46,1024\nMvaa 156,152,184\nCV 46,92,93\nCV 47,94,95\nMvaa 156,156,188\nCV 112,160,161\nMvaa 152,148,188\nCV 113,162,163\nCV 114,164,165\n"
"Mvaa 152,144,184\nWL 0\nMaaa 0,192,160\nD1 1,16,46,2048\nSv 32,1,16,75,0\nMaaa 0,196,164\nCV 115,166,167\nMaaa 4,204,164\nCV 116,168,169\nCV 117,170,171\nMaaa 4,200,160\n"
"CV 118,172,173\nCV 119,174,175\nMaaa 12,200,168\nCV 120,176,177\nCV 121,178,179\nMaaa 12,204,172\nD1 1,16,46,3072\nMaaa 8,196,172\nCV 122,180,181\nCV 123,182,183\nMaaa 8,192,168\n"
"CV 124,184,185\nCV 125,186,187\nMaaa 16,192,176\ns_add_u32 m0, s47, -49152\ns_add_u32 s46, s45, 0x408000\nSv 36,1,16,75,1024\nMaaa 16,196,180\nCV 126,188,189\nMaaa 20,204,180\n"
"D1 1,16,46,0\nCV 127,190,191\nMaaa 20,200,176\nCV 96,128,129\nCV 97,130,131\nMaaa 28,200,184\nCV 98,132,133\nCV 99,134,135\nMaaa 28,204,188\nCV 100,136,137\nMaaa 24,196,188\n"
"CV 101,138,139\nCV 102,140,141\nMaaa 24,192,184\nD1 1,16,46,1024\nCV 103,142,143\nMaaa 64,208,160\nSv 40,1,16,75,2048\nCV 104,144,145\nMaaa 64,212,164\nCV 105,146,147\n"
"Maaa 68,220,164\nCV 106,148,149\nCV 107,150,151\nMaaa 68,216,160\nCV 108,152,153\nCV 109,154,155\nMaaa 76,216,168\nD1 1,16,46,2048\nCV 110,156,157\nMaaa 76,220,172\n"
"CV 111,158,159\nMaaa 72,212,172\nv_add_u32 v2, 0x10000, v1\ns_lshr_b32 s2, s59, 1\nMaaa 72,208,168\nv_add_u32 v24, s2, v2\nv_xor_b32 v26, 0x1000, v24\nMaaa 80,208,176\n"
"Sv 44,1,16,75,3072\nv_lshrrev_b32 v20, 4, v0\nMaaa 80,212,180\nD1 1,16,46,3072\nMaaa 84,220,180\nv_lshlrev_b32 v20, 2, v20\nv_and_b32 v0, 15, v0\nMaaa 84,216,176\n"
"v_sub_u32 v0, v0, v20\ns_lshl_b32 s2, s11, 4\nMaaa 92,216,184\nv_add_u32 v3, s2, v0\nv_subrev_u32 v0, 32, v3\nMaaa 92,220,188\nv_mov_b32 v4, 0\nMaaa 88,212,188\nv_mov_b32 v5, 0\n"
"v_mov_b32 v6, 0\nMaaa 88,208,184\nWV 20\ns_barrier\nMaaa 32,192,224\ns_add_u32 s75, s75, s55\nSv 48,1,16,75,0\nMaaa 32,196,228\nv_mov_b32 v7, 0\nMaaa 36,204,228\nv_mov_b32 v8, 0\n"
"v_mov_b32 v9, 0\nMaaa 36,200,224\nv_mov_b32 v10, 0\nv_mov_b32 v11, 0\nMaaa 44,200,232\nv_mov_b32 v12, 0\nv_mov_b32 v13, 0\nMaaa 44,204,236\nv_mov_b32 v14, 0\nMaaa 40,196,236\n"
"v_mov_b32 v15, 0\nv_mov_b32 v16, 0\nMaaa 40,192,232\nv_mov_b32 v17, 0\nv_mov_b32 v18, 0\nMaaa 48,192,240\nSv 52,1,16,75,1024\nv_mov_b32 v19, 0\nMaaa 48,196,244\nv_mov_b32 v86, 0\n"
"Maaa 52,204,244\nv_mov_b32 v87, 0\nv_mov_b32 v88, 0\nMaaa 52,200,240\nv_mov_b32 v89, 0\nAR 92,0\nAR 93,1\nMaaa 60,200,248\nCV 128,92,93\nAR 92,2\nAR 93,3\nMaaa 60,204,252\n"
"CV 129,92,93\nAR 92,4\nAR 93,5\nMaaa 56,196,252\nRa 128,24,0\nCV 130,92,93\nAR 92,6\nMaaa 56,192,248\nAR 93,7\nCV 131,92,93\nAR 92,8\nMaaa 96,208,224\nSv 56,1,16,75,2048\n"
"Ra 132,24,1024\nAR 93,9\nMaaa 96,212,228\nCV 132,92,93\nAR 92,10\nAR 93,11\nMaaa 100,220,228\nRa 136,24,2048\nCV 133,92,93\nAR 92,12\nMaaa 100,216,224\nAR 93,13\nCV 134,92,93\n"
"AR 92,14\nMaaa 108,216,232\nRa 140,24,3072\nAR 93,15\nCV 135,92,93\nMaaa 108,220,236\nAR 92,16\nAR 93,17\nCV 136,92,93\nMaaa 104,212,236\nRa 144,26,0\nAR 92,18\nAR 93,19\n"
"Maaa 104,208,232\nCV 137,92,93\nAR 92,20\nAR 93,21\nMaaa 112,208,240\nSv 60,1,16,75,3072\nRa 148,26,1024\nCV 138,92,93\nMaaa 112,212,244\nAR 92,22\nAR 93,23\nCV 139,92,93\n"
"Maaa 116,220,244\nRa 152,26,2048\nAR 92,24\nAR 93,25\nMaaa 116,216,240\nCV 140,92,93\nAR 92,26\nAR 93,27\nMaaa 124,216,248\nRa 156,26,3072\nCV 141,92,93\nAR 92,28\n"
"Maaa 124,220,252\nAR 93,29\nCV 142,92,93\nAR 92,30\nMaaa 120,212,252\nAR 93,31\nCV 143,92,93\nAR 92,32\nMaaa 120,208,248\nWL 4\nCvav 32,128,192,4\nRa 160,24,8192\nAR 93,33\n"
"CV 144,92,93\nMvav 32,132,200\nAR 92,34\nAR 93,35\nCV 145,92,93\nMvav 32,136,208\nRa 164,24,9216\nAR 92,36\nAR 93,37\nMvav 32,140,216\nCV 146,92,93\nAR 92,38\nAR 93,39\n"
"Cvav 36,140,220,8\nRa 168,24,10240\nCV 147,92,93\nAR 92,40\nMvav 36,136,212\ns_add_u32 m0, s89, 32768\nAR 93,41\nCV 148,92,93\nMvav 36,132,204\nRa 172,24,11264\nWL 4\ns_barrier\n"
"Mvav 36,128,196\nAR 92,42\nAR 93,43\nCV 149,92,93\nMvav 36,144,228\nRa 176,26,8192\nAR 92,44\nAR 93,45\nSv 96,1,16,76,0\nMvav 36,148,236\ns_add_u32 s46, s86, s88\nCV 150,92,93\n"
"AR 92,46\nMvav 36,152,244\nRa 180,26,9216\nAR 93,47\nCV 151,92,93\nMvav 36,156,252\nAR 92,48\nAR 93,49\nCV 152,92,93\nMvav 32,156,248\nRa 184,26,10240\nAR 92,50\nAR 93,51\n"
"Mvav 32,152,240\nD1 1,16,46,0\nCV 153,92,93\nAR 92,52\nMvav 32,148,232\nRa 188,26,11264\nWL 4\nAR 93,53\nMvav 32,144,224\nCV 154,92,93\nAR 92,54\nAR 93,55\nCvav 40,160,192,12\n"
"CV 155,92,93\nAR 92,56\nAR 93,57\nMvav 40,164,200\nCV 156,92,93\nAR 92,58\nAR 93,59\nMvav 40,168,208\nCV 157,92,93\nAR 92,60\nAR 93,61\nv_and_b32 v27, 0x70, v1\n"
"buffer_load_dword v27, v27, s[16:19], s61 offen offset:136 sc0 sc1\nMvav 40,172,216\nCV 158,92,93\nAR 92,62\nAR 93,63\nCvav 44,172,220,16\nCV 159,92,93\nAR 92,64\nAR 93,65\n"
"Mvav 44,168,212\nD1 1,16,46,1024\nCV 160,92,93\nAR 92,66\nMvav 44,164,204\nWL 0\nAR 93,67\nCV 161,92,93\nMvav 44,160,196\nAR 92,68\nAR 93,69\nCV 162,92,93\nMvav 44,176,228\n"
"AR 92,70\nAR 93,71\nCV 163,92,93\nSv 100,1,16,76,1024\nMvav 44,180,236\ns_add_u32 s88, s88, 32768\nEX 80,32\nMvav 44,184,244\nAR 92,72\nAR 93,73\nCV 164,92,93\nMvav 44,188,252\n"
"EX 81,33\nMvav 40,188,248\nAR 92,74\nAR 93,75\nCV 165,92,93\nMvav 40,184,240\nD1 1,16,46,2048\nAR 92,76\nAR 93,77\nMvav 40,180,232\nCV 166,92,93\nAR 92,78\nAR 93,79\n"
"Mvav 40,176,224\nAD 84,80,81\nCV 64,80,81\nCV 167,92,93\nAR 92,80\nAR 93,81\nCV 168,92,93\nEX 82,34\nAR 92,82\nAR 93,83\nCV 169,92,93\nAR 92,84\nWV 17\nEX 83,35\nAD 84,84,82\n"
"AR 93,85\nCV 170,92,93\nD1 1,16,46,3072\ns_add_u32 m0, s89, 49152\nAR 92,86\ns_barrier\nAR 93,87\nCV 171,92,93\nAD 84,84,83\nCV 65,82,83\nAR 92,88\nRa 128,24,32768\nAR 93,89\n"
"CV 172,92,93\nSv 104,1,16,76,2048\ns_add_u32 s46, s87, s88\nEX 80,36\nRa 132,24,33792\nAR 92,90\nAR 93,91\nEX 81,37\nRa 136,24,34816\nCV 173,92,93\nAR 92,92\nD1 1,16,46,0\n"
"AR 93,93\nCV 174,92,93\nRa 140,24,35840\nAR 92,94\nAR 93,95\nAD 85,80,81\nCV 68,80,81\nCV 175,92,93\nRa 144,26,32768\nAR 92,96\nAR 93,97\nEX 82,38\nCV 176,92,93\nRa 148,26,33792\n"
"AR 92,98\nAR 93,99\nEX 83,39\nRa 152,26,34816\nAD 85,85,82\nCV 177,92,93\nD1 1,16,46,1024\nAR 92,100\nAR 93,101\nRa 156,26,35840\nCV 178,92,93\nAR 92,102\nAD 85,85,83\n"
"CV 69,82,83\nAR 93,103\nRa 160,24,40960\nCV 179,92,93\nAR 92,104\nSv 108,1,16,76,3072\ns_xor_b32 s89, s89, 0x10000\nEX 80,40\nRa 164,24,41984\nAR 93,105\nCV 180,92,93\nWL 6\n"
"EX 81,41\nRa 168,24,43008\nAD 84,84,80\nAR 92,106\nD1 1,16,46,2048\nAR 93,107\nCV 181,92,93\nRa 172,24,44032\nAR 92,108\nAR 93,109\nAD 84,84,81\nCV 66,80,81\nCV 182,92,93\n"
"Cvav 48,128,192,4\nRa 176,26,40960\nAR 92,110\nAR 93,111\nMvav 48,132,200\nEX 82,42\nCV 183,92,93\nMvav 48,136,208\nRa 180,26,41984\nAR 92,112\nAR 93,113\nMvav 48,140,216\nWL 6\n"
"EX 83,43\nWV 9\nv_cmp_ne_u32 vcc, s15, v27\ns_nop 0\ns_cbranch_vccnz .S175\n.S176:\nCvav 52,140,220,8\nRa 184,26,43008\nAD 84,84,82\nCV 184,92,93\nMvav 52,136,212\n"
"D1 1,16,46,3072\ns_add_u32 m0, s89, 0\nAR 92,114\nMvav 52,132,204\nRa 188,26,44032\nAR 93,115\nCV 185,92,93\nMvav 52,128,196\nAD 84,84,83\nCV 67,82,83\nAR 92,116\nMvav 52,144,228\n"
"Ra 128,2,49152\nAR 93,117\nCV 186,92,93\ns_add_u32 s76, s76, s55\nSv 112,1,16,76,0\nMvav 52,148,236\ns_add_u32 s46, s86, s88\nEX 80,44\nMvav 52,152,244\nRa 132,2,50176\n"
"v_xor_b32 v24, 0x10000, v24\nAR 92,118\nMvav 52,156,252\nWL 6\nEX 81,45\nMvav 48,156,248\nRa 136,2,51200\nAD 85,85,80\nAR 93,119\nMvav 48,152,240\nD1 1,16,46,0\nCV 187,92,93\n"
"AR 92,120\nMvav 48,148,232\nRa 140,2,52224\nAR 93,121\nCV 188,92,93\nMvav 48,144,224\nAD 85,85,81\nCV 70,80,81\nAR 92,122\nCvav 56,160,192,12\nRa 144,2,53248\nAR 93,123\n"
"CV 189,92,93\nMvav 56,164,200\nEX 82,46\nAR 92,124\nMvav 56,168,208\nRa 148,2,54272\nv_xor_b32 v26, 0x10000, v26\nAR 93,125\nMvav 56,172,216\nWL 6\nEX 83,47\nCvav 60,172,220,16\n"
"Ra 152,2,55296\nAD 85,85,82\nCV 190,92,93\nMvav 60,168,212\nD1 1,16,46,1024\nAR 92,126\nAR 93,127\nMvav 60,164,204\nRa 156,2,56320\nCV 191,92,93\nMvav 60,160,196\nAD 85,85,83\n"
"CV 71,82,83\nMvav 60,176,228\nRa 160,2,57344\nv_max_f32 v90, v84, v85\nSv 116,1,16,76,1024\nMvav 60,180,236\ns_add_u32 s88, s88, 32768\nEX 80,48\nMvav 60,184,244\nRa 164,2,58368\n"
"v_cmp_lt_f32 vcc, s63, v90\nMvav 60,188,252\nWL 8\nEX 81,49\nMvav 56,188,248\nRa 168,2,59392\ns_cbranch_vccnz .T157\n.T158:\nv_mov_b32 v20, v84\nv_mov_b32 v21, v85\n"
"v_mov_b32 v22, v20\nv_mov_b32 v23, v21\nMvav 56,184,240\nD1 1,16,46,2048\nv_permlane32_swap_b32 v20, v22\nv_permlane32_swap_b32 v21, v23\nAD 20,20,22\nAD 21,21,23\n"
"v_mov_b32 v22, v20\nv_mov_b32 v23, v21\nMvav 56,180,232\nRa 172,2,60416\nv_permlane16_swap_b32 v20, v22\nv_permlane16_swap_b32 v21, v23\nAD 20,20,22\nAD 21,21,23\n"
"v_min_f32 v90, v20, v21\nv_cmp_gt_f32 vcc, s64, v90\ns_nop 0\ns_cbranch_vccnz .T159\n.T160:\nAD 86,86,84\nMvav 56,176,224\nAD 84,80,81\nCV 72,80,81\nZaav 0,128,64\nRa 176,2,61440\n"
"Zaav 4,128,68\nEX 82,50\nZaav 12,132,68\nRa 180,2,62464\nZaav 8,132,64\nWL 8\nEX 83,51\nZaav 16,136,64\nRa 184,2,63488\nAD 84,84,82\nZaav 20,136,68\nD1 1,16,46,3072\n"
"s_add_u32 m0, s89, 16384\nZaav 28,140,68\nRa 188,2,64512\ns_barrier\nZaav 24,140,64\nAD 84,84,83\nCV 73,82,83\nZaav 32,144,64\nRa 128,24,0\nSv 120,1,16,76,2048\nZaav 36,144,68\n"
"s_add_u32 s46, s87, s88\nEX 80,52\nbuffer_store_dword v25, off, s[16:19], s54 offset:12 sc0 sc1\nZaav 44,148,68\nRa 132,24,1024\nv_xor_b32 v2, 0x10000, v2\nZaav 40,148,64\nWL 8\n"
"EX 81,53\nZaav 48,152,64\nRa 136,24,2048\nAD 87,87,85\nZaav 52,152,68\nD1 1,16,46,0\nZaav 60,156,68\nRa 140,24,3072\nZaav 56,156,64\nAD 85,80,81\nCV 76,80,81\nZaav 64,160,64\n"
"Ra 144,26,0\nZaav 68,160,68\nEX 82,54\nZaav 76,164,68\nRa 148,26,1024\nZaav 72,164,64\nWL 8\nEX 83,55\nZaav 80,168,64\nRa 152,26,2048\nAD 85,85,82\nZaav 84,168,68\n"
"D1 1,16,46,1024\nZaav 92,172,68\nRa 156,26,3072\nZaav 88,172,64\nAD 85,85,83\nCV 77,82,83\nZaav 96,176,64\nRa 160,24,8192\nSv 124,1,16,76,3072\nZaav 100,176,68\nEX 80,56\n"
"Zaav 108,180,68\nRa 164,24,9216\nZaav 104,180,64\nWL 6\nEX 81,57\nZaav 112,184,64\nRa 168,24,10240\nAD 84,84,80\nZaav 116,184,68\nD1 1,16,46,2048\nZaav 124,188,68\n"
"Ra 172,24,11264\nZaav 120,188,64\nAD 84,84,81\nCV 74,80,81\nCvav 32,128,192,4\nRa 176,26,8192\nMvav 32,132,200\nEX 82,58\nMvav 32,136,208\nRa 180,26,9216\nMvav 32,140,216\nWL 6\n"
"EX 83,59\nCvav 36,140,220,8\nRa 184,26,10240\nAD 84,84,82\nMvav 36,136,212\nD1 1,16,46,3072\ns_add_u32 m0, s89, 32768\nMvav 36,132,204\nRa 188,26,11264\nMvav 36,128,196\n"
"AD 84,84,83\nCV 75,82,83\nMvav 36,144,228\nRa 128,2,16384\nSv 128,1,16,77,0\nMvav 36,148,236\ns_add_u32 s46, s86, s88\nEX 80,60\nMvav 36,152,244\nRa 132,2,17408\nMvav 36,156,252\n"
"WL 6\nEX 81,61\nMvav 32,156,248\nRa 136,2,18432\nAD 85,85,80\nMvav 32,152,240\nD1 1,16,46,0\nMvav 32,148,232\nRa 140,2,19456\nMvav 32,144,224\nAD 85,85,81\nCV 78,80,81\n"
"Cvav 40,160,192,12\nRa 144,2,20480\nMvav 40,164,200\nEX 82,62\nMvav 40,168,208\nRa 148,2,21504\nMvav 40,172,216\nWL 6\nEX 83,63\nCvav 44,172,220,16\nRa 152,2,22528\nAD 85,85,82\n"
"Mvav 44,168,212\nD1 1,16,46,1024\nMvav 44,164,204\nRa 156,2,23552\nMvav 44,160,196\nAD 85,85,83\nCV 79,82,83\nMvav 44,176,228\nRa 160,2,24576\nv_max_f32 v90, v84, v85\n"
"Sv 132,1,16,77,1024\nMvav 44,180,236\ns_add_u32 s88, s88, 32768\nEX 80,32\nMvav 44,184,244\nRa 164,2,25600\nv_cmp_lt_f32 vcc, s63, v90\nMvav 44,188,252\nWL 8\nEX 81,33\n"
"Mvav 40,188,248\nRa 168,2,26624\ns_cbranch_vccnz .T162\n.T163:\nMvav 40,184,240\nD1 1,16,46,2048\nMvav 40,180,232\nRa 172,2,27648\nAD 86,86,84\nMvav 40,176,224\nAD 84,80,81\n"
"CV 64,80,81\nMaav 0,128,72\nRa 176,2,28672\nv_mov_b32 v112, -1\nMaav 4,128,76\nEX 82,34\nMaav 12,132,76\nRa 180,2,29696\nMaav 8,132,72\ns_waitcnt vmcnt(18) lgkmcnt(8)\nEX 83,35\n"
"Maav 16,136,72\nRa 184,2,30720\nAD 84,84,82\nMaav 20,136,76\nD1 1,16,46,3072\ns_add_u32 m0, s89, 49152\nMaav 28,140,76\nRa 188,2,31744\ns_barrier\nMaav 24,140,72\nAD 84,84,83\n"
"CV 65,82,83\nMaav 32,144,72\nRa 128,24,32768\nSv 136,1,16,77,2048\nMaav 36,144,76\ns_add_u32 s46, s87, s88\nEX 80,36\nMaav 44,148,76\nRa 132,24,33792\nMaav 40,148,72\nWL 8\n"
"EX 81,37\nMaav 48,152,72\nRa 136,24,34816\nAD 87,87,85\nMaav 52,152,76\nD1 1,16,46,0\nMaav 60,156,76\nRa 140,24,35840\nMaav 56,156,72\nAD 85,80,81\nCV 68,80,81\nMaav 64,160,72\n"
"Ra 144,26,32768\nv_mov_b32 v113, -1\nMaav 68,160,76\nEX 82,38\nMaav 76,164,76\nRa 148,26,33792\nMaav 72,164,72\nWL 8\nEX 83,39\nMaav 80,168,72\nRa 152,26,34816\nAD 85,85,82\n"
"Maav 84,168,76\nD1 1,16,46,1024\nMaav 92,172,76\nRa 156,26,35840\nMaav 88,172,72\nAD 85,85,83\nCV 69,82,83\nMaav 96,176,72\nRa 160,24,40960\nSv 140,1,16,77,3072\nMaav 100,176,76\n"
"s_xor_b32 s89, s89, 0x10000\nEX 80,40\nMaav 108,180,76\nRa 164,24,41984\nMaav 104,180,72\nWL 6\nEX 81,41\nMaav 112,184,72\nRa 168,24,43008\nAD 84,84,80\nMaav 116,184,76\n"
"D1 1,16,46,2048\nMaav 124,188,76\nRa 172,24,44032\nMaav 120,188,72\nAD 84,84,81\nCV 66,80,81\nCvav 48,128,192,4\nRa 176,26,40960\nv_mov_b32 v114, -1\nMvav 48,132,200\nEX 82,42\n"
"Mvav 48,136,208\nRa 180,26,41984\nMvav 48,140,216\nWL 6\nEX 83,43\nCvav 52,140,220,8\nRa 184,26,43008\nAD 84,84,82\nv_and_b32 v27, 0x70, v1\n"
"buffer_load_dword v27, v27, s[16:19], s61 offen offset:140 sc0 sc1\nMvav 52,136,212\nD1 1,16,46,3072\ns_add_u32 m0, s89, 0\nMvav 52,132,204\nRa 188,26,44032\nMvav 52,128,196\n"
"AD 84,84,83\nCV 67,82,83\nMvav 52,144,228\nRa 128,2,49152\ns_add_u32 s77, s77, s55\nSv 144,1,16,77,0\nMvav 52,148,236\ns_add_u32 s46, s86, s88\nEX 80,44\nMvav 52,152,244\n"
"Ra 132,2,50176\nv_xor_b32 v24, 0x10000, v24\nMvav 52,156,252\nWL 6\nEX 81,45\nMvav 48,156,248\nRa 136,2,51200\nAD 85,85,80\nMvav 48,152,240\nD1 1,16,46,0\nMvav 48,148,232\n"
"Ra 140,2,52224\nMvav 48,144,224\nAD 85,85,81\nCV 70,80,81\nCvav 56,160,192,12\nRa 144,2,53248\nv_mov_b32 v115, -1\nMvav 56,164,200\nEX 82,46\nMvav 56,168,208\nRa 148,2,54272\n"
"v_xor_b32 v26, 0x10000, v26\nMvav 56,172,216\nWL 6\nEX 83,47\nCvav 60,172,220,16\nRa 152,2,55296\nAD 85,85,82\nMvav 60,168,212\nD1 1,16,46,1024\nMvav 60,164,204\nRa 156,2,56320\n"
"Mvav 60,160,196\nAD 85,85,83\nCV 71,82,83\nMvav 60,176,228\nRa 160,2,57344\nv_max_f32 v90, v84, v85\nSv 148,1,16,77,1024\nMvav 60,180,236\ns_add_u32 s88, s88, 32768\nEX 80,48\n"
"Mvav 60,184,244\nRa 164,2,58368\nv_cmp_lt_f32 vcc, s63, v90\nMvav 60,188,252\nWL 8\nEX 81,49\nMvav 56,188,248\nRa 168,2,59392\ns_cbranch_vccnz .T164\n.T165:\nMvav 56,184,240\n"
"D1 1,16,46,2048\nMvav 56,180,232\nRa 172,2,60416\nAD 86,86,84\nMvav 56,176,224\nAD 84,80,81\nCV 72,80,81\nMaav 0,128,64\nRa 176,2,61440\nMaav 4,128,68\nEX 82,50\nMaav 12,132,68\n"
"Ra 180,2,62464\nMaav 8,132,64\ns_waitcnt vmcnt(18) lgkmcnt(8)\nEX 83,51\nMaav 16,136,64\nRa 184,2,63488\nAD 84,84,82\nMaav 20,136,68\nD1 1,16,46,3072\ns_add_u32 m0, s89, 16384\n"
"Maav 28,140,68\nRa 188,2,64512\ns_barrier\nMaav 24,140,64\nAD 84,84,83\nCV 73,82,83\nMaav 32,144,64\nRa 128,24,0\nSv 152,1,16,77,2048\nMaav 36,144,68\ns_add_u32 s46, s87, s88\n"
"EX 80,52\nMaav 44,148,68\nRa 132,24,1024\nv_xor_b32 v2, 0x10000, v2\nMaav 40,148,64\nWL 8\nEX 81,53\nMaav 48,152,64\nRa 136,24,2048\nAD 87,87,85\n"
"buffer_store_dword v25, off, s[16:19], s54 offset:8 sc0 sc1\nMaav 52,152,68\nD1 1,16,46,0\nMaav 60,156,68\nRa 140,24,3072\nMaav 56,156,64\nAD 85,80,81\nCV 76,80,81\n"
"Maav 64,160,64\nRa 144,26,0\nMaav 68,160,68\nEX 82,54\nMaav 76,164,68\nRa 148,26,1024\nMaav 72,164,64\nWL 8\nEX 83,55\nMaav 80,168,64\nRa 152,26,2048\nAD 85,85,82\nMaav 84,168,68\n"
"D1 1,16,46,1024\nMaav 92,172,68\nRa 156,26,3072\nMaav 88,172,64\nAD 85,85,83\nCV 77,82,83\nMaav 96,176,64\nRa 160,24,8192\nSv 156,1,16,77,3072\nMaav 100,176,68\nEX 80,56\n"
"Maav 108,180,68\nRa 164,24,9216\nMaav 104,180,64\nWL 6\nEX 81,57\nMaav 112,184,64\nRa 168,24,10240\nAD 84,84,80\nMaav 116,184,68\nD1 1,16,46,2048\nMaav 124,188,68\n"
"Ra 172,24,11264\nMaav 120,188,64\nAD 84,84,81\nCV 74,80,81\nCvav 32,128,192,4\nRa 176,26,8192\nMvav 32,132,200\nEX 82,58\nMvav 32,136,208\nRa 180,26,9216\nMvav 32,140,216\nWL 6\n"
"EX 83,59\nCvav 36,140,220,8\nRa 184,26,10240\nAD 84,84,82\nMvav 36,136,212\nD1 1,16,46,3072\ns_add_u32 m0, s89, 32768\nMvav 36,132,204\nRa 188,26,11264\nMvav 36,128,196\n"
"AD 84,84,83\nCV 75,82,83\nMvav 36,144,228\nRa 128,2,16384\nSv 160,1,16,78,0\nMvav 36,148,236\ns_add_u32 s46, s86, s88\nEX 80,60\nMvav 36,152,244\nRa 132,2,17408\nMvav 36,156,252\n"
"WL 6\nEX 81,61\nMvav 32,156,248\nRa 136,2,18432\nAD 85,85,80\nMvav 32,152,240\nD1 1,16,46,0\nMvav 32,148,232\nRa 140,2,19456\nSv 112,1,16,49,0\nMvav 32,144,224\nAD 85,85,81\n"
"CV 78,80,81\nCvav 40,160,192,12\nRa 144,2,20480\nMvav 40,164,200\nEX 82,62\nMvav 40,168,208\nRa 148,2,21504\nMvav 40,172,216\nWL 6\nEX 83,63\nCvav 44,172,220,16\nRa 152,2,22528\n"
"AD 85,85,82\nMvav 44,168,212\nD1 1,16,46,1024\nMvav 44,164,204\nRa 156,2,23552\nMvav 44,160,196\nAD 85,85,83\nCV 79,82,83\nMvav 44,176,228\nRa 160,2,24576\n"
"v_max_f32 v90, v84, v85\nSv 164,1,16,78,1024\nMvav 44,180,236\ns_add_u32 s88, s88, 32768\nEX 80,32\nMvav 44,184,244\nRa 164,2,25600\nv_cmp_lt_f32 vcc, s63, v90\nMvav 44,188,252\n"
"WL 8\nEX 81,33\nMvav 40,188,248\nRa 168,2,26624\ns_cbranch_vccnz .T167\n.T168:\nMvav 40,184,240\nD1 1,16,46,2048\nMvav 40,180,232\nRa 172,2,27648\nAD 86,86,84\nMvav 40,176,224\n"
"AD 84,80,81\nCV 64,80,81\nMaav 0,128,72\nRa 176,2,28672\nMaav 4,128,76\nEX 82,34\nMaav 12,132,76\nRa 180,2,29696\nMaav 8,132,72\ns_waitcnt vmcnt(19) lgkmcnt(8)\nEX 83,35\n"
"Maav 16,136,72\nRa 184,2,30720\nAD 84,84,82\nMaav 20,136,76\nD1 1,16,46,3072\ns_add_u32 m0, s89, 49152\nMaav 28,140,76\nRa 188,2,31744\ns_barrier\nMaav 24,140,72\nAD 84,84,83\n"
"CV 65,82,83\nMaav 32,144,72\nRa 128,24,32768\nSv 168,1,16,78,2048\nMaav 36,144,76\ns_add_u32 s46, s87, s88\nEX 80,36\nMaav 44,148,76\nRa 132,24,33792\nMaav 40,148,72\nWL 8\n"
"EX 81,37\nMaav 48,152,72\nRa 136,24,34816\nAD 87,87,85\nMaav 52,152,76\nD1 1,16,46,0\nMaav 60,156,76\nRa 140,24,35840\nMaav 56,156,72\nAD 85,80,81\nCV 68,80,81\nMaav 64,160,72\n"
"Ra 144,26,32768\nMaav 68,160,76\nEX 82,38\nMaav 76,164,76\nRa 148,26,33792\nMaav 72,164,72\nWL 8\nEX 83,39\nMaav 80,168,72\nRa 152,26,34816\nAD 85,85,82\nMaav 84,168,76\n"
"D1 1,16,46,1024\nMaav 92,172,76\nRa 156,26,35840\nMaav 88,172,72\nAD 85,85,83\nCV 69,82,83\nMaav 96,176,72\nRa 160,24,40960\nSv 172,1,16,78,3072\nMaav 100,176,76\n"
"s_xor_b32 s89, s89, 0x10000\nEX 80,40\nMaav 108,180,76\nRa 164,24,41984\nMaav 104,180,72\nWL 6\nEX 81,41\nMaav 112,184,72\nRa 168,24,43008\nAD 84,84,80\nMaav 116,184,76\n"
"D1 1,16,46,2048\nMaav 124,188,76\nRa 172,24,44032\nMaav 120,188,72\nAD 84,84,81\nCV 66,80,81\nCvav 48,128,192,4\nRa 176,26,40960\nMvav 48,132,200\nEX 82,42\nMvav 48,136,208\n"
"Ra 180,26,41984\nMvav 48,140,216\nWL 6\nEX 83,43\nv_cmp_ne_u32 vcc, s15, v27\ns_nop 0\ns_cbranch_vccnz .S178\n.S179:\nv_and_b32 v27, 0x70, v1\n"
"buffer_load_dword v27, v27, s[16:19], s61 offen offset:12 sc0 sc1\nCvav 52,140,220,8\nRa 184,26,43008\nAD 84,84,82\nMvav 52,136,212\nD1 1,16,46,3072\ns_add_u32 m0, s89, 0\n"
"Mvav 52,132,204\nRa 188,26,44032\nMvav 52,128,196\nAD 84,84,83\nCV 67,82,83\nMvav 52,144,228\nRa 128,2,49152\ns_add_u32 s78, s78, s55\nSv 176,1,16,78,0\nMvav 52,148,236\n"
"s_add_u32 s46, s86, s88\nEX 80,44\nMvav 52,152,244\nRa 132,2,50176\nv_xor_b32 v24, 0x10000, v24\nMvav 52,156,252\nWL 6\nEX 81,45\nMvav 48,156,248\nRa 136,2,51200\nAD 85,85,80\n"
"Mvav 48,152,240\nD1 1,16,46,0\nv_lshrrev_b32 v92, 8, v1\nMvav 48,148,232\nRa 140,2,52224\nSv 112,1,16,49,1024\nMvav 48,144,224\nAD 85,85,81\nCV 70,80,81\nCvav 56,160,192,12\n"
"Ra 144,2,53248\nv_bfe_u32 v93, v1, 6, 2\nMvav 56,164,200\ns_add_u32 s49, s49, 131072\nEX 82,46\nMvav 56,168,208\nRa 148,2,54272\nv_xor_b32 v26, 0x10000, v26\nMvav 56,172,216\n"
"WL 6\nEX 83,47\nCvav 60,172,220,16\nRa 152,2,55296\nAD 85,85,82\nMvav 60,168,212\nD1 1,16,46,1024\nv_cmp_eq_u32 vcc, v92, v93\nv_and_b32 v92, 16, v1\nv_mov_b32 v93, 0x3f80\n"
"v_cndmask_b32 v93, 0, v93, vcc\nMvav 60,164,204\nRa 156,2,56320\nMvav 60,160,196\nAD 85,85,83\nCV 71,82,83\nMvav 60,176,228\nRa 160,2,57344\nv_max_f32 v90, v84, v85\n"
"Sv 180,1,16,78,1024\nMvav 60,180,236\ns_add_u32 s88, s88, 32768\nEX 80,48\nMvav 60,184,244\nRa 164,2,58368\nv_cmp_lt_f32 vcc, s63, v90\nMvav 60,188,252\nWL 8\nEX 81,49\n"
"Mvav 56,188,248\nRa 168,2,59392\ns_cbranch_vccnz .T169\n.T170:\nMvav 56,184,240\nD1 1,16,46,2048\nv_lshlrev_b32 v93, v92, v93\nMvav 56,180,232\nRa 172,2,60416\nAD 86,86,84\n"
"Mvav 56,176,224\nAD 84,80,81\nCV 72,80,81\nMaav 0,128,64\nRa 176,2,61440\nv_and_b32 v92, 32, v1\nMaav 4,128,68\nEX 82,50\nMaav 12,132,68\nRa 180,2,62464\nv_cmp_eq_u32 vcc, 0, v92\n"
"v_mov_b32 v128, 0\nv_mov_b32 v129, 0\nv_cndmask_b32 v130, 0, v93, vcc\nMaav 8,132,64\ns_waitcnt vmcnt(20) lgkmcnt(8)\nEX 83,51\nMaav 16,136,64\nRa 184,2,63488\nAD 84,84,82\n"
"Maav 20,136,68\nD1 1,16,46,3072\ns_add_u32 m0, s89, 16384\nMaav 28,140,68\nRa 188,2,64512\ns_barrier\nMaav 24,140,64\nAD 84,84,83\nCV 73,82,83\nMaav 32,144,64\nRa 128,24,0\n"
"Sv 184,1,16,78,2048\nMaav 36,144,68\ns_add_u32 s46, s87, s88\nEX 80,52\nMaav 44,148,68\nRa 132,24,1024\nv_xor_b32 v2, 0x10000, v2\nMaav 40,148,64\nWL 8\nEX 81,53\nMaav 48,152,64\n"
"Ra 136,24,2048\nAD 87,87,85\nMaav 52,152,68\nD1 1,16,46,0\nv_sub_u32 v131, v93, v130\nMaav 60,156,68\nRa 140,24,3072\nMaav 56,156,64\nAD 85,80,81\nCV 76,80,81\nMaav 64,160,64\n"
"Ra 144,26,0\nv_mov_b32 v132, 0\nMaav 68,160,68\nEX 82,54\nMaav 76,164,68\nRa 148,26,1024\nMaav 72,164,64\nWL 8\nEX 83,55\nMaav 80,168,64\nRa 152,26,2048\nAD 85,85,82\n"
"Maav 84,168,68\nD1 1,16,46,1024\nMaav 92,172,68\nRa 156,26,3072\nMaav 88,172,64\nAD 85,85,83\nCV 77,82,83\nMaav 96,176,64\nRa 160,24,8192\nSv 188,1,16,78,3072\nMaav 100,176,68\n"
"EX 80,56\nMaav 108,180,68\nRa 164,24,9216\nv_mov_b32 v133, 0\nMaav 104,180,64\nWL 6\nEX 81,57\nMaav 112,184,64\nRa 168,24,10240\nAD 84,84,80\nMaav 116,184,68\nD1 1,16,46,2048\n"
"Maav 124,188,68\nRa 172,24,11264\nMaav 120,188,64\nAD 84,84,81\nCV 74,80,81\ns_mov_b32 s99, 11\ns_sub_u32 s90, s90, 3\n.p2align 6\n.A194:\nCvav 32,128,192,4\nRa 176,26,8192\n"
"Mvav 32,132,200\nEX 82,58\nMvav 32,136,208\nRa 180,26,9216\nMvav 32,140,216\nWL 6\nEX 83,59\nCvav 36,140,220,8\nRa 184,26,10240\nAD 84,84,82\nMvav 36,136,212\nD1 1,16,46,3072\n"
"s_add_u32 m0, s89, 32768\nMvav 36,132,204\nRa 188,26,11264\nMvav 36,128,196\nAD 84,84,83\nCV 75,82,83\nMvav 36,144,228\nRa 128,2,16384\nMvav 36,148,236\ns_add_u32 s46, s86, s88\n"
"EX 80,60\nMvav 36,152,244\nRa 132,2,17408\nMvav 36,156,252\nWL 6\nEX 81,61\nMvav 32,156,248\nRa 136,2,18432\nAD 85,85,80\nMvav 32,152,240\nD1 1,16,46,0\nMvav 32,148,232\n"
"Ra 140,2,19456\nSv 112,1,16,49,0\nMvav 32,144,224\nAD 85,85,81\nCV 78,80,81\nCvav 40,160,192,12\nRa 144,2,20480\nMvav 40,164,200\nEX 82,62\nMvav 40,168,208\nRa 148,2,21504\n"
"Mvav 40,172,216\nWL 6\nEX 83,63\nCvav 44,172,220,16\nRa 152,2,22528\nAD 85,85,82\nMvav 44,168,212\nD1 1,16,46,1024\nMvav 44,164,204\nRa 156,2,23552\nMvav 44,160,196\nAD 85,85,83\n"
"CV 79,82,83\nMvav 44,176,228\nRa 160,2,24576\nv_max_f32 v90, v84, v85\nMvav 44,180,236\ns_add_u32 s88, s88, 32768\nEX 80,32\nMvav 44,184,244\nRa 164,2,25600\n"
"v_cmp_lt_f32 vcc, s63, v90\nMvav 44,188,252\nWL 8\nEX 81,33\nMvav 40,188,248\nRa 168,2,26624\ns_cbranch_vccnz .T198\n.T199:\nMvav 40,184,240\nD1 1,16,46,2048\nMvav 40,180,232\n"
"Ra 172,2,27648\nAD 86,86,84\nMvav 40,176,224\nAD 84,80,81\nCV 64,80,81\nMaav 0,128,72\nRa 176,2,28672\nMaav 4,128,76\nEX 82,34\nMaav 12,132,76\nRa 180,2,29696\nMaav 8,132,72\n"
"s_waitcnt vmcnt(13) lgkmcnt(8)\nEX 83,35\nMaav 16,136,72\nRa 184,2,30720\nAD 84,84,82\nMaav 20,136,76\nD1 1,16,46,3072\ns_add_u32 m0, s89, 49152\nMaav 28,140,76\nRa 188,2,31744\n"
"s_barrier\nMaav 24,140,72\nAD 84,84,83\nCV 65,82,83\nMaav 32,144,72\nRa 128,24,32768\nMaav 36,144,76\ns_add_u32 s46, s87, s88\nEX 80,36\nMaav 44,148,76\nRa 132,24,33792\n"
"Maav 40,148,72\nWL 8\nEX 81,37\nMaav 48,152,72\nRa 136,24,34816\nAD 87,87,85\nMaav 52,152,76\nD1 1,16,46,0\nMaav 60,156,76\nRa 140,24,35840\nMaav 56,156,72\nAD 85,80,81\n"
"CV 68,80,81\nMaav 64,160,72\nRa 144,26,32768\nMaav 68,160,76\nEX 82,38\nMaav 76,164,76\nRa 148,26,33792\nMaav 72,164,72\nWL 8\nEX 83,39\nMaav 80,168,72\nRa 152,26,34816\n"
"AD 85,85,82\nMaav 84,168,76\nD1 1,16,46,1024\nMaav 92,172,76\nRa 156,26,35840\nMaav 88,172,72\nAD 85,85,83\nCV 69,82,83\nMaav 96,176,72\nRa 160,24,40960\nMaav 100,176,76\n"
"s_xor_b32 s89, s89, 0x10000\nEX 80,40\nMaav 108,180,76\nRa 164,24,41984\nMaav 104,180,72\nWL 6\nEX 81,41\nMaav 112,184,72\nRa 168,24,43008\nAD 84,84,80\nMaav 116,184,76\n"
"D1 1,16,46,2048\nMaav 124,188,76\nRa 172,24,44032\nMaav 120,188,72\nAD 84,84,81\nCV 66,80,81\nCvav 48,128,192,4\nRa 176,26,40960\nMvav 48,132,200\nEX 82,42\nMvav 48,136,208\n"
"Ra 180,26,41984\nMvav 48,140,216\nWL 6\nEX 83,43\nCvav 52,140,220,8\nRa 184,26,43008\nAD 84,84,82\nMvav 52,136,212\nD1 1,16,46,3072\ns_add_u32 m0, s89, 0\nMvav 52,132,204\n"
"Ra 188,26,44032\nMvav 52,128,196\nAD 84,84,83\nCV 67,82,83\nMvav 52,144,228\nRa 128,2,49152\nMvav 52,148,236\ns_add_u32 s46, s86, s88\nEX 80,44\nMvav 52,152,244\nRa 132,2,50176\n"
"v_xor_b32 v24, 0x10000, v24\nMvav 52,156,252\nWL 6\nEX 81,45\nMvav 48,156,248\nRa 136,2,51200\nAD 85,85,80\nMvav 48,152,240\nD1 1,16,46,0\nMvav 48,148,232\nRa 140,2,52224\n"
"Sv 112,1,16,49,1024\nMvav 48,144,224\nAD 85,85,81\nCV 70,80,81\nCvav 56,160,192,12\nRa 144,2,53248\nMvav 56,164,200\ns_add_u32 s49, s49, 131072\nEX 82,46\nMvav 56,168,208\n"
"Ra 148,2,54272\nv_xor_b32 v26, 0x10000, v26\nMvav 56,172,216\nWL 6\nEX 83,47\nCvav 60,172,220,16\nRa 152,2,55296\nAD 85,85,82\nMvav 60,168,212\nD1 1,16,46,1024\nMvav 60,164,204\n"
"Ra 156,2,56320\nMvav 60,160,196\nAD 85,85,83\nCV 71,82,83\nMvav 60,176,228\nRa 160,2,57344\nv_max_f32 v90, v84, v85\nMvav 60,180,236\ns_add_u32 s88, s88, 32768\nEX 80,48\n"
"Mvav 60,184,244\nRa 164,2,58368\nv_cmp_lt_f32 vcc, s63, v90\nMvav 60,188,252\nWL 8\nEX 81,49\nMvav 56,188,248\nRa 168,2,59392\ns_cbranch_vccnz .T200\n.T201:\nMvav 56,184,240\n"
"D1 1,16,46,2048\nMvav 56,180,232\nRa 172,2,60416\nAD 86,86,84\nMvav 56,176,224\nAD 84,80,81\nCV 72,80,81\nMaav 0,128,64\nRa 176,2,61440\nMaav 4,128,68\nEX 82,50\nMaav 12,132,68\n"
"Ra 180,2,62464\nMaav 8,132,64\ns_waitcnt vmcnt(13) lgkmcnt(8)\nEX 83,51\nMaav 16,136,64\nRa 184,2,63488\nAD 84,84,82\nMaav 20,136,68\nD1 1,16,46,3072\ns_add_u32 m0, s89, 16384\n"
"Maav 28,140,68\nRa 188,2,64512\ns_barrier\nMaav 24,140,64\nAD 84,84,83\nCV 73,82,83\nMaav 32,144,64\nRa 128,24,0\nMaav 36,144,68\ns_add_u32 s46, s87, s88\nEX 80,52\n"
"Maav 44,148,68\nRa 132,24,1024\nv_xor_b32 v2, 0x10000, v2\nMaav 40,148,64\nWL 8\nEX 81,53\nMaav 48,152,64\nRa 136,24,2048\nAD 87,87,85\nMaav 52,152,68\nD1 1,16,46,0\n"
"Maav 60,156,68\nRa 140,24,3072\nMaav 56,156,64\nAD 85,80,81\nCV 76,80,81\nMaav 64,160,64\nRa 144,26,0\nMaav 68,160,68\nEX 82,54\nMaav 76,164,68\nRa 148,26,1024\nMaav 72,164,64\n"
"WL 8\nEX 83,55\nMaav 80,168,64\nRa 152,26,2048\nAD 85,85,82\nMaav 84,168,68\nD1 1,16,46,1024\nMaav 92,172,68\nRa 156,26,3072\nMaav 88,172,64\nAD 85,85,83\nCV 77,82,83\n"
"Maav 96,176,64\nRa 160,24,8192\nMaav 100,176,68\ns_sub_u32 s90, s90, 1\nEX 80,56\nMaav 108,180,68\nRa 164,24,9216\ns_cbranch_scc1 .E196\n.E197:\nMaav 104,180,64\nWL 6\nEX 81,57\n"
"Maav 112,184,64\nRa 168,24,10240\nAD 84,84,80\nMaav 116,184,68\nD1 1,16,46,2048\ns_sub_u32 s99, s99, 1\nMaav 124,188,68\nRa 172,24,11264\nMaav 120,188,64\nAD 84,84,81\n"
"CV 74,80,81\ns_cbranch_scc0 .A194\ns_branch .A202\n.E196:\ns_swappc_b64 s[80:81], s[8:9]\ns_branch .E197\n.HD148:\nbuffer_store_dword v25, off, s[16:19], s54 offset:4 sc0 sc1\n"
"v_cmp_ne_u32 vcc, s15, v27\ns_nop 0\ns_cbranch_vccnz .S181\n.S182:\nv_and_b32 v27, 0x70, v1\nbuffer_load_dword v27, v27, s[16:19], s61 offen offset:8 sc0 sc1\ns_mov_b32 s90, 1\n"
"s_swappc_b64 s[8:9], s[80:81]\nv_cmp_ne_u32 vcc, s15, v27\ns_nop 0\ns_cbranch_vccnz .S184\n.S185:\nv_and_b32 v27, 0xf0, v1\n"
"buffer_load_dword v27, v27, s[16:19], s61 offen offset:4 sc0 sc1\ns_mov_b32 s90, 1\ns_swappc_b64 s[8:9], s[80:81]\nv_cmp_ne_u32 vcc, s15, v27\ns_nop 0\ns_cbranch_vccnz .S187\n"
".S188:\ns_mov_b32 s90, s91\ns_swappc_b64 s[8:9], s[80:81]\ns_branch .FF190\n.A202:\nCvav 32,128,192,4\nRa 176,26,8192\nMvav 32,132,200\nEX 82,58\nMvav 32,136,208\nRa 180,26,9216\n"
"Mvav 32,140,216\nWL 6\nEX 83,59\nCvav 36,140,220,8\nRa 184,26,10240\nAD 84,84,82\nMvav 36,136,212\nD1 1,16,46,3072\ns_add_u32 m0, s89, 32768\nMvav 36,132,204\nRa 188,26,11264\n"
"Mvav 36,128,196\nAD 84,84,83\nCV 75,82,83\ns_mov_b32 s94, s96\ns_add_u32 s95, s96, 65536\nv_and_b32 v108, 0xf0, v1\nMvav 36,144,228\nRa 128,2,16384\nMvav 36,148,236\n"
"s_add_u32 s46, s86, s88\nEX 80,60\nMvav 36,152,244\nRa 132,2,17408\nMvav 36,156,252\nWL 6\nEX 81,61\nMvav 32,156,248\nRa 136,2,18432\nAD 85,85,80\nMvav 32,152,240\nD1 1,16,46,0\n"
"Mvav 32,148,232\nRa 140,2,19456\nSv 112,1,16,49,0\nMvav 32,144,224\nAD 85,85,81\nCV 78,80,81\nbuffer_load_dwordx4 v[100:103], v108, s[16:19], s65 offen offset:0 sc0 sc1\n"
"buffer_load_dwordx4 v[104:107], v108, s[16:19], s65 offen offset:256 sc0 sc1\nCvav 40,160,192,12\nRa 144,2,20480\nMvav 40,164,200\nEX 82,62\nMvav 40,168,208\nRa 148,2,21504\n"
"Mvav 40,172,216\nWL 6\nEX 83,63\nCvav 44,172,220,16\nRa 152,2,22528\nAD 85,85,82\nMvav 44,168,212\nD1 1,16,46,1024\nMvav 44,164,204\nRa 156,2,23552\nMvav 44,160,196\nAD 85,85,83\n"
"CV 79,82,83\nMvav 44,176,228\nRa 160,2,24576\nv_max_f32 v90, v84, v85\nMvav 44,180,236\ns_add_u32 s88, s88, 32768\nEX 80,32\nMvav 44,184,244\nRa 164,2,25600\n"
"v_cmp_lt_f32 vcc, s63, v90\nMvav 44,188,252\nWL 8\nEX 81,33\nMvav 40,188,248\nRa 168,2,26624\ns_cbranch_vccnz .T206\n.T207:\nMvav 40,184,240\nD1 1,16,46,2048\nMvav 40,180,232\n"
"Ra 172,2,27648\nAD 86,86,84\nMvav 40,176,224\nAD 84,80,81\nCV 64,80,81\nMaav 0,128,72\nRa 176,2,28672\nMaav 4,128,76\nEX 82,34\nMaav 12,132,76\nRa 180,2,29696\nMaav 8,132,72\n"
"s_waitcnt vmcnt(15) lgkmcnt(8)\nEX 83,35\nMaav 16,136,72\nRa 184,2,30720\nAD 84,84,82\nMaav 20,136,76\nD1 1,16,46,3072\ns_add_u32 m0, s89, 49152\nMaav 28,140,76\nRa 188,2,31744\n"
"s_barrier\nMaav 24,140,72\nAD 84,84,83\nCV 65,82,83\nMaav 32,144,72\nRa 128,24,32768\nMaav 36,144,76\ns_add_u32 s46, s87, s88\nEX 80,36\nMaav 44,148,76\nRa 132,24,33792\n"
"Maav 40,148,72\nWL 8\nEX 81,37\nMaav 48,152,72\nRa 136,24,34816\nAD 87,87,85\nMaav 52,152,76\nD1 1,16,46,0\nMaav 60,156,76\nRa 140,24,35840\nMaav 56,156,72\nAD 85,80,81\n"
"CV 68,80,81\nMaav 64,160,72\nRa 144,26,32768\nMaav 68,160,76\nEX 82,38\nMaav 76,164,76\nRa 148,26,33792\nMaav 72,164,72\nWL 8\nEX 83,39\nMaav 80,168,72\nRa 152,26,34816\n"
"AD 85,85,82\nMaav 84,168,76\nD1 1,16,46,1024\nMaav 92,172,76\nRa 156,26,35840\nMaav 88,172,72\nAD 85,85,83\nCV 69,82,83\nMaav 96,176,72\nRa 160,24,40960\nMaav 100,176,76\n"
"s_xor_b32 s89, s89, 0x10000\nEX 80,40\nMaav 108,180,76\nRa 164,24,41984\nMaav 104,180,72\nWL 6\nEX 81,41\nMaav 112,184,72\nRa 168,24,43008\nAD 84,84,80\nMaav 116,184,76\n"
"D1 1,16,46,2048\nMaav 124,188,76\nRa 172,24,44032\nMaav 120,188,72\nAD 84,84,81\nCV 66,80,81\nCvav 48,128,192,4\nRa 176,26,40960\nMvav 48,132,200\ns_mov_b32 s86, s94\nEX 82,42\n"
"Mvav 48,136,208\nRa 180,26,41984\nMvav 48,140,216\nWL 6\nEX 83,43\nCvav 52,140,220,8\nRa 184,26,43008\nAD 84,84,82\nMvav 52,136,212\nD1 1,16,46,3072\ns_add_u32 m0, s89, 0\n"
"Mvav 52,132,204\nRa 188,26,44032\nMvav 52,128,196\nAD 84,84,83\nCV 67,82,83\nMvav 52,144,228\nRa 128,2,49152\nMvav 52,148,236\ns_add_u32 s46, s86, s88\nEX 80,44\nMvav 52,152,244\n"
"Ra 132,2,50176\nv_xor_b32 v24, 0x10000, v24\nMvav 52,156,252\nWL 6\nEX 81,45\nMvav 48,156,248\nRa 136,2,51200\nAD 85,85,80\nMvav 48,152,240\nD1 1,16,46,0\nMvav 48,148,232\n"
"Ra 140,2,52224\nSv 112,1,16,49,1024\nMvav 48,144,224\nAD 85,85,81\nCV 70,80,81\nWV 9\nv_xor_b32 v109, s15, v102\nv_xor_b32 v110, s15, v106\nv_or_b32 v109, v109, v110\n"
"v_cmp_ne_u32 vcc, 0, v109\ns_nop 3\ns_cmp_lg_u64 vcc, 0\ns_cselect_b32 s67, 1, 0\ns_cselect_b32 s6, 0x40000000, s66\nCvav 56,160,192,12\nRa 144,2,53248\ns_add_u32 s5, s6, 0\n"
"Mvav 56,164,200\ns_add_u32 s49, s49, 131072\nEX 82,46\nMvav 56,168,208\nRa 148,2,54272\nv_xor_b32 v26, 0x10000, v26\n"
"buffer_load_dwordx4 a[192:195], v1, s[16:19], s5 offen offset:0 sc1\nMvav 56,172,216\nWL 6\nEX 83,47\nCvav 60,172,220,16\nRa 152,2,55296\nAD 85,85,82\nMvav 60,168,212\n"
"D1 1,16,46,1024\nMvav 60,164,204\nRa 156,2,56320\nMvav 60,160,196\nAD 85,85,83\nCV 71,82,83\nMvav 60,176,228\nRa 160,2,57344\nv_max_f32 v90, v84, v85\nMvav 60,180,236\n"
"s_add_u32 s88, s88, 32768\nEX 80,48\nMvav 60,184,244\nRa 164,2,58368\nv_cmp_lt_f32 vcc, s63, v90\nMvav 60,188,252\nWL 8\nEX 81,49\nMvav 56,188,248\nRa 168,2,59392\n"
"s_cbranch_vccnz .T208\n.T209:\nbuffer_load_dwordx4 a[196:199], v1, s[16:19], s5 offen offset:1024 sc1\nMvav 56,184,240\nD1 1,16,46,2048\nMvav 56,180,232\nRa 172,2,60416\n"
"AD 86,86,84\nMvav 56,176,224\nAD 84,80,81\nCV 72,80,81\nMaav 0,128,64\nRa 176,2,61440\nMaav 4,128,68\nEX 82,50\nMaav 12,132,68\nRa 180,2,62464\nMaav 8,132,64\nWL 8\nEX 83,51\n"
"Maav 16,136,64\nRa 184,2,63488\nAD 84,84,82\nbuffer_load_dwordx4 a[200:203], v1, s[16:19], s5 offen offset:2048 sc1\nMaav 20,136,68\nD1 1,16,46,3072\ns_add_u32 m0, s89, 16384\n"
"Maav 28,140,68\nRa 188,2,64512\ns_barrier\nMaav 24,140,64\nAD 84,84,83\nCV 73,82,83\nMaav 32,144,64\nRa 128,24,0\nMaav 36,144,68\ns_add_u32 s46, s87, s88\nEX 80,52\n"
"Maav 44,148,68\nRa 132,24,1024\nv_xor_b32 v2, 0x10000, v2\nMaav 40,148,64\nWL 8\nEX 81,53\nMaav 48,152,64\nRa 136,24,2048\nAD 87,87,85\n"
"buffer_load_dwordx4 a[204:207], v1, s[16:19], s5 offen offset:3072 sc1\nMaav 52,152,68\nD1 1,16,46,0\nMaav 60,156,68\nRa 140,24,3072\nMaav 56,156,64\nAD 85,80,81\nCV 76,80,81\n"
"Maav 64,160,64\nRa 144,26,0\nMaav 68,160,68\ns_cmp_lg_u32 s92, 0\nEX 82,54\nMaav 76,164,68\nRa 148,26,1024\ns_cbranch_scc1 .E204\n.E205:\ns_add_u32 s5, s6, 4096\nMaav 72,164,64\n"
"WL 8\nEX 83,55\nMaav 80,168,64\nRa 152,26,2048\nAD 85,85,82\nbuffer_load_dwordx4 a[208:211], v1, s[16:19], s5 offen offset:0 sc1\nMaav 84,168,68\nD1 1,16,46,1024\nMaav 92,172,68\n"
"Ra 156,26,3072\nMaav 88,172,64\nAD 85,85,83\nCV 77,82,83\nMaav 96,176,64\nRa 160,24,8192\nMaav 100,176,68\nEX 80,56\nMaav 108,180,68\nRa 164,24,9216\nMaav 104,180,64\nWL 6\n"
"EX 81,57\nMaav 112,184,64\nRa 168,24,10240\nAD 84,84,80\nbuffer_load_dwordx4 a[212:215], v1, s[16:19], s5 offen offset:1024 sc1\nMaav 116,184,68\nD1 1,16,46,2048\nMaav 124,188,68\n"
"Ra 172,24,11264\nMaav 120,188,64\nAD 84,84,81\nCV 74,80,81\ns_sub_u32 s99, s99, 1\nCvav 32,128,192,4\nRa 176,26,8192\nMvav 32,132,200\nEX 82,58\nMvav 32,136,208\nRa 180,26,9216\n"
"Mvav 32,140,216\nWL 6\nEX 83,59\nCvav 36,140,220,8\nRa 184,26,10240\nAD 84,84,82\nbuffer_load_dwordx4 a[216:219], v1, s[16:19], s5 offen offset:2048 sc1\nMvav 36,136,212\n"
"D1 1,16,46,3072\ns_add_u32 m0, s89, 32768\nMvav 36,132,204\nRa 188,26,11264\nMvav 36,128,196\nAD 84,84,83\nCV 75,82,83\nMvav 36,144,228\nRa 128,2,16384\nMvav 36,148,236\n"
"s_add_u32 s46, s86, s88\nEX 80,60\nMvav 36,152,244\nRa 132,2,17408\nMvav 36,156,252\nWL 6\nEX 81,61\nMvav 32,156,248\nRa 136,2,18432\nAD 85,85,80\n"
"buffer_load_dwordx4 a[220:223], v1, s[16:19], s5 offen offset:3072 sc1\nMvav 32,152,240\nD1 1,16,46,0\nMvav 32,148,232\nRa 140,2,19456\nMvav 32,144,224\nAD 85,85,81\nCV 78,80,81\n"
"Cvav 40,160,192,12\nRa 144,2,20480\nMvav 40,164,200\ns_mov_b32 s87, s95\nEX 82,62\nMvav 40,168,208\nRa 148,2,21504\ns_add_u32 s5, s6, 16384\nMvav 40,172,216\nWL 6\nEX 83,63\n"
"Cvav 44,172,220,16\nRa 152,2,22528\nAD 85,85,82\nbuffer_load_dwordx4 a[224:227], v1, s[16:19], s5 offen offset:0 sc1\nMvav 44,168,212\nD1 1,16,46,1024\nMvav 44,164,204\n"
"Ra 156,2,23552\nMvav 44,160,196\nAD 85,85,83\nCV 79,82,83\nMvav 44,176,228\nRa 160,2,24576\nv_max_f32 v90, v84, v85\nMvav 44,180,236\ns_add_u32 s88, s88, 32768\nEX 80,32\n"
"Mvav 44,184,244\nRa 164,2,25600\nv_cmp_lt_f32 vcc, s63, v90\nMvav 44,188,252\nWL 8\nEX 81,33\nMvav 40,188,248\nRa 168,2,26624\ns_cbranch_vccnz .T213\n.T214:\n"
"buffer_load_dwordx4 a[228:231], v1, s[16:19], s5 offen offset:1024 sc1\nMvav 40,184,240\nD1 1,16,46,2048\nMvav 40,180,232\nRa 172,2,27648\nAD 86,86,84\nMvav 40,176,224\n"
"AD 84,80,81\nCV 64,80,81\nMaav 0,128,72\nRa 176,2,28672\nMaav 4,128,76\nEX 82,34\nMaav 12,132,76\nRa 180,2,29696\nMaav 8,132,72\ns_waitcnt vmcnt(22) lgkmcnt(8)\nEX 83,35\n"
"Maav 16,136,72\nRa 184,2,30720\nAD 84,84,82\nbuffer_load_dwordx4 a[232:235], v1, s[16:19], s5 offen offset:2048 sc1\nMaav 20,136,76\nD1 1,16,46,3072\ns_add_u32 m0, s89, 49152\n"
"Maav 28,140,76\nRa 188,2,31744\ns_barrier\nMaav 24,140,72\nAD 84,84,83\nCV 65,82,83\nMaav 32,144,72\nRa 128,24,32768\nMaav 36,144,76\ns_add_u32 s46, s87, s88\nEX 80,36\n"
"Maav 44,148,76\nRa 132,24,33792\nMaav 40,148,72\nWL 8\nEX 81,37\nMaav 48,152,72\nRa 136,24,34816\nAD 87,87,85\n"
"buffer_load_dwordx4 a[236:239], v1, s[16:19], s5 offen offset:3072 sc1\nMaav 52,152,76\nD1 1,16,46,0\nMaav 60,156,76\nRa 140,24,35840\nMaav 56,156,72\nAD 85,80,81\nCV 68,80,81\n"
"Maav 64,160,72\nRa 144,26,32768\nMaav 68,160,76\ns_cmp_eq_u32 s13, 7\nEX 82,38\nMaav 76,164,76\nRa 148,26,33792\ns_cbranch_scc1 .E211\n.E212:\ns_add_u32 s5, s6, 20480\n"
"Maav 72,164,72\nWL 8\nEX 83,39\nMaav 80,168,72\nRa 152,26,34816\nAD 85,85,82\nbuffer_load_dwordx4 a[240:243], v1, s[16:19], s5 offen offset:0 sc1\nMaav 84,168,76\nD1 1,16,46,1024\n"
"Maav 92,172,76\nRa 156,26,35840\nMaav 88,172,72\nAD 85,85,83\nCV 69,82,83\nMaav 96,176,72\nRa 160,24,40960\nMaav 100,176,76\ns_xor_b32 s89, s89, 0x10000\nEX 80,40\n"
"Maav 108,180,76\nRa 164,24,41984\nMaav 104,180,72\nWL 6\nEX 81,41\nMaav 112,184,72\nRa 168,24,43008\nAD 84,84,80\n"
"buffer_load_dwordx4 a[244:247], v1, s[16:19], s5 offen offset:1024 sc1\nMaav 116,184,76\nD1 1,16,46,2048\nMaav 124,188,76\nRa 172,24,44032\nMaav 120,188,72\nAD 84,84,81\n"
"CV 66,80,81\nCvav 48,128,192,4\nRa 176,26,40960\nMvav 48,132,200\nEX 82,42\nMvav 48,136,208\nRa 180,26,41984\nMvav 48,140,216\nWL 6\nEX 83,43\nCvav 52,140,220,8\nRa 184,26,43008\n"
"AD 84,84,82\nbuffer_load_dwordx4 a[248:251], v1, s[16:19], s5 offen offset:2048 sc1\nMvav 52,136,212\nD1 1,16,46,3072\ns_add_u32 m0, s89, 0\nMvav 52,132,204\nRa 188,26,44032\n"
"Mvav 52,128,196\nAD 84,84,83\nCV 67,82,83\nMvav 52,144,228\nRa 128,2,49152\nMvav 52,148,236\ns_add_u32 s46, s86, s88\nEX 80,44\nMvav 52,152,244\nRa 132,2,50176\n"
"v_xor_b32 v24, 0x10000, v24\nMvav 52,156,252\nWL 6\nEX 81,45\nMvav 48,156,248\nRa 136,2,51200\nAD 85,85,80\nbuffer_load_dwordx4 a[252:255], v1, s[16:19], s5 offen offset:3072 sc1\n"
"Mvav 48,152,240\nD1 1,16,46,0\nMvav 48,148,232\nRa 140,2,52224\nMvav 48,144,224\nAD 85,85,81\nCV 70,80,81\nCvav 56,160,192,12\nRa 144,2,53248\nMvav 56,164,200\nEX 82,46\n"
"Mvav 56,168,208\nRa 148,2,54272\nv_xor_b32 v26, 0x10000, v26\nMvav 56,172,216\nWL 6\nEX 83,47\nCvav 60,172,220,16\nRa 152,2,55296\nAD 85,85,82\nMvav 60,168,212\nD1 1,16,46,1024\n"
"Mvav 60,164,204\nRa 156,2,56320\nMvav 60,160,196\nAD 85,85,83\nCV 71,82,83\nMvav 60,176,228\nRa 160,2,57344\nv_max_f32 v90, v84, v85\nMvav 60,180,236\ns_add_u32 s88, s88, 32768\n"
"EX 80,48\nMvav 60,184,244\nRa 164,2,58368\nv_cmp_lt_f32 vcc, s63, v90\nMvav 60,188,252\nWL 8\nEX 81,49\nMvav 56,188,248\nRa 168,2,59392\ns_cbranch_vccnz .T215\n.T216:\n"
"Mvav 56,184,240\nD1 1,16,46,2048\nMvav 56,180,232\nRa 172,2,60416\nAD 86,86,84\nMvav 56,176,224\nAD 84,80,81\nCV 72,80,81\nMaav 0,128,64\nRa 176,2,61440\nMaav 4,128,68\nEX 82,50\n"
"Maav 12,132,68\nRa 180,2,62464\nMaav 8,132,64\ns_waitcnt vmcnt(20) lgkmcnt(8)\nEX 83,51\nMaav 16,136,64\nRa 184,2,63488\nAD 84,84,82\nMaav 20,136,68\nD1 1,16,46,3072\n"
"s_add_u32 m0, s89, 16384\nMaav 28,140,68\nRa 188,2,64512\ns_barrier\nMaav 24,140,64\nAD 84,84,83\nCV 73,82,83\nMaav 32,144,64\nRa 128,24,0\nMaav 36,144,68\n"
"s_add_u32 s46, s87, s88\nEX 80,52\nMaav 44,148,68\nRa 132,24,1024\nv_xor_b32 v2, 0x10000, v2\nMaav 40,148,64\nWL 8\nEX 81,53\nMaav 48,152,64\nRa 136,24,2048\nAD 87,87,85\n"
"Maav 52,152,68\nD1 1,16,46,0\nMaav 60,156,68\nRa 140,24,3072\nMaav 56,156,64\nAD 85,80,81\nCV 76,80,81\ns_cmp_lg_u32 s67, 0\ns_cbranch_scc1 .E218\n.E219:\nMaav 64,160,64\n"
"Ra 144,26,0\nMaav 68,160,68\nEX 82,54\nMaav 76,164,68\nRa 148,26,1024\nMaav 72,164,64\nWL 8\nEX 83,55\nMaav 80,168,64\nRa 152,26,2048\nAD 85,85,82\nMaav 84,168,68\n"
"D1 1,16,46,1024\nMaav 92,172,68\nRa 156,26,3072\nMaav 88,172,64\nAD 85,85,83\nCV 77,82,83\nMaav 96,176,64\nRa 160,24,8192\nMaav 100,176,68\nEX 80,56\nMaav 108,180,68\n"
"Ra 164,24,9216\nMaav 104,180,64\nWL 10\nEX 81,57\nMaav 112,184,64\nRa 168,24,10240\nAD 84,84,80\nMaav 116,184,68\nD1 1,16,46,2048\nMaav 124,188,68\nRa 172,24,11264\n"
"Maav 120,188,64\nAD 84,84,81\nCV 74,80,81\ns_cmp_lg_u32 s67, 0\ns_cbranch_scc1 .D220\n.D221:\nWV 7\nv_cmp_lt_f32 vcc, 0, v101\nSU 20,100,88\nv_mul_f32 v101, 0x3e800000, v101\n"
"v_cndmask_b32 v20, 0, v20, vcc\nv_cmp_lt_f32 vcc, 0, v105\nSU 21,104,89\nv_mul_f32 v105, 0x3e800000, v105\nv_cndmask_b32 v21, 0, v21, vcc\nv_or_b32 v91, v20, v21\n"
"v_cmp_ne_u32 vcc, 0, v91\ns_nop 0\ns_cbranch_vccnz .D222\nAD 86,86,101\nAD 87,87,105\n.D223:\nMava 0,130,192\nMava 8,128,192\nEX 82,58\nMava 12,128,196\nMava 4,130,196\nEX 83,59\n"
"Mava 16,130,200\nAD 84,84,82\nMava 24,128,200\nD1 1,16,46,3072\nMava 28,128,204\ns_add_u32 m0, s89, 32768\ns_add_u32 s46, s86, s88\nMava 20,130,204\nAD 84,84,83\nCV 75,82,83\n"
"Mava 32,130,208\nRa 128,2,16384\nv_xor_b32 v3, 0x10000, v2\nv_add_u32 v0, 0x20000, v1\nMava 40,128,208\nEX 80,60\nMava 44,128,212\nRa 132,2,17408\ns_sub_u32 s94, s49, 1835008\n"
"Mava 36,130,212\nEX 81,61\nMava 48,130,216\nRa 136,2,18432\nAD 85,85,80\ns_xor_b32 s92, s94, 0x202000\nMava 56,128,216\nD1 1,16,46,0\ns_lshl_b32 s89, s11, 12\nMava 60,128,220\n"
"Ra 140,2,19456\ns_lshl1_add_u32 s2, s12, 0\nMava 52,130,220\nAD 85,85,81\nCV 78,80,81\nMava 64,130,224\nRa 144,2,20480\ns_lshl3_add_u32 s2, s2, s13\n"
"s_lshl4_add_u32 s2, s2, 0xc048200\nMava 72,128,224\nEX 82,62\nMava 76,128,228\nRa 148,2,21504\ns_and_b32 s3, s72, 0x2000\nMava 68,130,228\nEX 83,63\nMava 80,130,232\n"
"Ra 152,2,22528\nAD 85,85,82\nMava 88,128,232\nD1 1,16,46,1024\ns_cmp_eq_u32 s11, 1\ns_cselect_b32 s54, s2, s54\ns_cselect_b32 s3, s3, 0\nMava 92,128,236\nRa 156,2,23552\n"
"v_xor_b32 v25, s3, v25\nMava 84,130,236\nAD 85,85,83\nCV 79,82,83\nMava 96,130,240\nRa 160,2,24576\nv_max_f32 v90, v84, v85\nMava 104,128,240\nv_cmp_lt_f32 vcc, s63, v90\n"
"Mava 108,128,244\nRa 164,2,25600\nMava 100,130,244\nWL 8\nMava 112,130,248\nRa 168,2,26624\nMava 120,128,248\nD1 1,16,46,2048\nMava 124,128,252\nRa 172,2,27648\nMava 116,130,252\n"
"s_cbranch_vccnz .T227\n.T228:\nAD 86,86,84\nAD 87,87,85\nMaav 0,128,72\nRa 176,2,28672\nv_mov_b32 v84, v86\nv_mov_b32 v85, v87\nMaav 4,128,76\nv_permlane32_swap_b32 v86, v84\n"
"v_permlane32_swap_b32 v87, v85\nMaav 12,132,76\nRa 180,2,29696\nAD 86,86,84\nMaav 8,132,72\nWL 8\nAD 87,87,85\nMaav 16,136,72\nRa 184,2,30720\nv_mov_b32 v84, v86\n"
"v_mov_b32 v85, v87\nMaav 20,136,76\nD1 1,16,46,3072\nv_permlane16_swap_b32 v86, v84\nMaav 28,140,76\nRa 188,2,31744\nv_permlane16_swap_b32 v87, v85\nMaav 24,140,72\nAD 86,86,84\n"
"AD 87,87,85\nMaav 32,144,72\nRa 128,2,0\nv_rcp_f32 v86, v86\nMaav 36,144,76\nv_rcp_f32 v87, v87\nMaav 44,148,76\nRa 132,2,1024\nAR 82,0\nAR 83,1\nMU 82,82,86\nMU 83,83,86\n"
"Maav 40,148,72\nWL 8\ns_add_u32 m0, s89, 131072\ns_add_u32 s46, s96, 1212416\nCV 96,82,83\nAR 82,2\nMaav 48,152,72\nRa 136,2,2048\nAR 83,3\nMU 82,82,86\nMU 83,83,86\nCV 97,82,83\n"
"Maav 52,152,76\nD1 1,16,46,0\nAR 82,4\nAR 83,5\nMU 82,82,87\nMaav 60,156,76\nRa 140,2,3072\nMU 83,83,87\nCV 100,82,83\nAR 82,6\nAR 83,7\nMaav 56,156,72\nMU 82,82,87\nMU 83,83,87\n"
"CV 101,82,83\nAR 82,8\nAR 83,9\nMaav 64,160,72\nRa 144,2,4096\nMU 82,82,86\nMU 83,83,86\nCV 98,82,83\nAR 82,10\nMaav 68,160,76\nAR 83,11\nMU 82,82,86\nMU 83,83,86\nCV 99,82,83\n"
"Maav 76,164,76\nRa 148,2,5120\nAR 82,12\nAR 83,13\nMU 82,82,87\nMU 83,83,87\nMaav 72,164,72\nWL 8\nCV 102,82,83\nAR 82,14\nAR 83,15\nMU 82,82,87\nMaav 80,168,72\nRa 152,2,6144\n"
"MU 83,83,87\nCV 103,82,83\nAR 82,16\nAR 83,17\nMaav 84,168,76\nD1 1,16,46,1024\nMU 82,82,86\nMU 83,83,86\nCV 104,82,83\nMaav 92,172,76\nRa 156,2,7168\nAR 82,18\nAR 83,19\n"
"MU 82,82,86\nMU 83,83,86\nMaav 88,172,72\nCV 105,82,83\nAR 82,20\nAR 83,21\nMU 82,82,87\nMU 83,83,87\nMaav 96,176,72\nRa 160,2,8192\nCV 108,82,83\nAR 82,22\nAR 83,23\nMU 82,82,87\n"
"Maav 100,176,76\nMU 83,83,87\nCV 109,82,83\nAR 82,24\nAR 83,25\nMaav 108,180,76\nRa 164,2,9216\nMU 82,82,86\nMU 83,83,86\nCV 106,82,83\nAR 82,26\nMaav 104,180,72\nWL 8\nAR 83,27\n"
"MU 82,82,86\nMU 83,83,86\nCV 107,82,83\nMaav 112,184,72\nRa 168,2,10240\nAR 82,28\nAR 83,29\nMU 82,82,87\nMU 83,83,87\nMaav 116,184,76\nD1 1,16,46,2048\nCV 110,82,83\nAR 82,30\n"
"AR 83,31\nMaav 124,188,76\nRa 172,2,11264\nMU 82,82,87\nMU 83,83,87\nCV 111,82,83\nMaav 120,188,72\nAR 82,32\nAR 83,33\nMU 82,82,86\nZvav 128,128,96\nRa 176,2,12288\nMU 83,83,86\n"
"CV 112,82,83\nAR 82,34\nMvav 128,132,104\nAR 83,35\nMU 82,82,86\nMU 83,83,86\nZvav 132,132,108\nRa 180,2,13312\nCV 113,82,83\nAR 82,36\nMvav 132,128,100\nWL 8\nAR 83,37\n"
"MU 82,82,87\nMU 83,83,87\nZvav 140,136,100\nRa 184,2,14336\nCV 116,82,83\nAR 82,38\nMvav 140,140,108\nD1 1,16,46,3072\nAR 83,39\nMU 82,82,87\nZvav 136,140,104\nRa 188,2,15360\n"
"s_barrier\nMU 83,83,87\nCV 117,82,83\nMvav 136,136,96\nAR 82,40\nAR 83,41\nMU 82,82,86\nZvav 144,144,96\nRa 128,2,32768\nMU 83,83,86\nCV 114,82,83\nAR 82,42\nMvav 144,148,104\n"
"AR 83,43\nMU 82,82,86\nMU 83,83,86\nbuffer_store_dword v25, off, s[16:19], s54 offset:512 sc0 sc1\nZvav 148,148,108\nRa 132,2,33792\nCV 115,82,83\nAR 82,44\nMvav 148,144,100\n"
"WL 8\ns_add_u32 m0, s89, 114688\ns_add_u32 s46, s96, 1179648\nAR 83,45\nZvav 156,152,100\nRa 136,2,34816\nMU 82,82,87\nMU 83,83,87\nMvav 156,156,108\nD1 1,16,46,0\nCV 118,82,83\n"
"AR 82,46\nZvav 152,156,104\nRa 140,2,35840\nAR 83,47\nMU 82,82,87\nMU 83,83,87\nMvav 152,152,96\nCV 119,82,83\nAR 82,48\nAR 83,49\nZvav 64,160,96\nRa 144,2,36864\nMU 82,82,86\n"
"MU 83,83,86\nCV 120,82,83\nMvav 64,164,104\nAR 82,50\nAR 83,51\nMU 82,82,86\nZvav 68,164,108\nRa 148,2,37888\nMU 83,83,86\nCV 121,82,83\nMvav 68,160,100\nWL 8\nAR 82,52\nAR 83,53\n"
"MU 82,82,87\nZvav 76,168,100\nRa 152,2,38912\nMU 83,83,87\nCV 124,82,83\nMvav 76,172,108\nD1 1,16,46,1024\nAR 82,54\nAR 83,55\nZvav 72,172,104\nRa 156,2,39936\nMU 82,82,87\n"
"MU 83,83,87\nCV 125,82,83\nMvav 72,168,96\nAR 82,56\nAR 83,57\nMU 82,82,86\nZvav 4,176,96\nRa 160,2,40960\nMU 83,83,86\nCV 122,82,83\nAR 82,58\nMvav 4,180,104\nAR 83,59\n"
"MU 82,82,86\nMU 83,83,86\nZvav 8,180,108\nRa 164,2,41984\nCV 123,82,83\nAR 82,60\nMvav 8,176,100\nWL 8\nAR 83,61\nMU 82,82,87\nMU 83,83,87\nZvav 16,184,100\nRa 168,2,43008\n"
"CV 126,82,83\nAR 82,62\nMvav 16,188,108\nD1 1,16,46,2048\nAR 83,63\nMU 82,82,87\nZvav 12,188,104\nRa 172,2,44032\nMU 83,83,87\nCV 127,82,83\nMvav 12,184,96\nAR 82,64\nAR 83,65\n"
"MU 82,82,86\nMvav 128,128,112\nRa 176,2,45056\nMU 83,83,86\nCV 32,82,83\nAR 82,66\nMvav 128,132,120\nAR 83,67\nMU 82,82,86\nMU 83,83,86\nMvav 132,132,124\nRa 180,2,46080\n"
"CV 33,82,83\nAR 82,68\nMvav 132,128,116\nWL 8\nAR 83,69\nMU 82,82,87\nMvav 140,136,116\nRa 184,2,47104\nMU 83,83,87\nCV 36,82,83\nAR 82,70\nMvav 140,140,124\nD1 1,16,46,3072\n"
"AR 83,71\nMU 82,82,87\nMvav 136,140,120\nRa 188,2,48128\nMU 83,83,87\nCV 37,82,83\nMvav 136,136,112\nAR 82,72\nAR 83,73\nMU 82,82,86\nMU 83,83,86\nMvav 144,144,112\n"
"Ra 128,2,49152\nCV 34,82,83\nAR 82,74\nMvav 144,148,120\nAR 83,75\nMU 82,82,86\nMU 83,83,86\nMvav 148,148,124\nRa 132,2,50176\nCV 35,82,83\nAR 82,76\nAR 83,77\nMvav 148,144,116\n"
"WL 8\ns_add_u32 m0, s89, 147456\ns_add_u32 s46, s96, 1245184\nMvav 156,152,116\nRa 136,2,51200\nMU 82,82,87\nMU 83,83,87\nMvav 156,156,124\nD1 1,16,46,0\nCV 38,82,83\nAR 82,78\n"
"AR 83,79\nMvav 152,156,120\nRa 140,2,52224\nMU 82,82,87\nMU 83,83,87\nMvav 152,152,112\nCV 39,82,83\nAR 82,80\nAR 83,81\nMvav 64,160,112\nRa 144,2,53248\nMU 82,82,86\nMU 83,83,86\n"
"CV 40,82,83\nMvav 64,164,120\nAR 82,82\nAR 83,83\nMU 82,82,86\nMvav 68,164,124\nRa 148,2,54272\nMU 83,83,86\nCV 41,82,83\nMvav 68,160,116\nWL 8\nAR 82,84\nAR 83,85\n"
"Mvav 76,168,116\nRa 152,2,55296\nMU 82,82,87\nMU 83,83,87\nCV 44,82,83\nMvav 76,172,124\nD1 1,16,46,1024\nAR 82,86\nAR 83,87\nMvav 72,172,120\nRa 156,2,56320\nMU 82,82,87\n"
"MU 83,83,87\nMvav 72,168,112\nCV 45,82,83\nAR 82,88\nAR 83,89\nMU 82,82,86\nMvav 4,176,112\nRa 160,2,57344\nMU 83,83,86\nCV 42,82,83\nMvav 4,180,120\nAR 82,90\nAR 83,91\n"
"MU 82,82,86\nMvav 8,180,124\nRa 164,2,58368\nMU 83,83,86\nCV 43,82,83\nAR 82,92\nMvav 8,176,116\nWL 8\nAR 83,93\nMU 82,82,87\nMvav 16,184,116\nRa 168,2,59392\nMU 83,83,87\n"
"CV 46,82,83\nMvav 16,188,124\nD1 1,16,46,2048\nAR 82,94\nAR 83,95\nMU 82,82,87\nMvav 12,188,120\nRa 172,2,60416\nMU 83,83,87\nCV 47,82,83\nMvav 12,184,112\nAR 82,96\nAR 83,97\n"
"MU 82,82,86\nMvav 128,128,32\nRa 176,2,61440\nMU 83,83,86\nCV 48,82,83\nAR 82,98\nMvav 128,132,40\nAR 83,99\nMU 82,82,86\nMU 83,83,86\nMvav 132,132,44\nRa 180,2,62464\n"
"CV 49,82,83\nAR 82,100\nMvav 132,128,36\ns_waitcnt vmcnt(16) lgkmcnt(8)\nAR 83,101\nMU 82,82,87\nMU 83,83,87\nMvav 140,136,36\nRa 184,2,63488\nCV 52,82,83\nAR 82,102\n"
"Mvav 140,140,44\nD1 1,16,46,3072\nAR 83,103\nMU 82,82,87\nMvav 136,140,40\nRa 188,2,64512\ns_barrier\nMU 83,83,87\nCV 53,82,83\nMvav 136,136,32\nAR 82,104\nAR 83,105\nMU 82,82,86\n"
"Mvav 144,144,32\nRa 128,3,0\nMU 83,83,86\nCV 50,82,83\nAR 82,106\nMvav 144,148,40\nAR 83,107\nMU 82,82,86\nMU 83,83,86\nMvav 148,148,44\nRa 132,3,1024\nCV 51,82,83\nAR 82,108\n"
"Mvav 148,144,36\nWL 8\ns_add_u32 m0, s89, 16384\ns_add_u32 s46, s96, 1277952\nAR 83,109\nMvav 156,152,36\nRa 136,3,2048\nMU 82,82,87\nMU 83,83,87\nMvav 156,156,44\nD1 1,16,46,0\n"
"CV 54,82,83\nAR 82,110\nMvav 152,156,40\nRa 140,3,3072\nAR 83,111\nMU 82,82,87\nMU 83,83,87\nMvav 152,152,32\nCV 55,82,83\nAR 82,112\nAR 83,113\nMvav 64,160,32\nRa 144,3,4096\n"
"MU 82,82,86\nMU 83,83,86\nCV 56,82,83\nMvav 64,164,40\nAR 82,114\nAR 83,115\nMU 82,82,86\nMvav 68,164,44\nRa 148,3,5120\nMU 83,83,86\nCV 57,82,83\nMvav 68,160,36\nWL 8\nAR 82,116\n"
"AR 83,117\nMU 82,82,87\nMvav 76,168,36\nRa 152,3,6144\nMU 83,83,87\nCV 60,82,83\nMvav 76,172,44\nD1 1,16,46,1024\nAR 82,118\nAR 83,119\nMvav 72,172,40\nRa 156,3,7168\nMU 82,82,87\n"
"MU 83,83,87\nCV 61,82,83\nMvav 72,168,32\nAR 82,120\nAR 83,121\nMU 82,82,86\nMvav 4,176,32\nRa 160,3,8192\nMU 83,83,86\nCV 58,82,83\nAR 82,122\nMvav 4,180,40\nAR 83,123\n"
"MU 82,82,86\nMU 83,83,86\nMvav 8,180,44\nRa 164,3,9216\nCV 59,82,83\nAR 82,124\nMvav 8,176,36\nWL 8\nAR 83,125\nMU 82,82,87\nMU 83,83,87\nMvav 16,184,36\nRa 168,3,10240\n"
"CV 62,82,83\nAR 82,126\nMvav 16,188,44\nD1 1,16,46,2048\nAR 83,127\nMU 82,82,87\nMvav 12,188,40\nRa 172,3,11264\nMU 83,83,87\nCV 63,82,83\nMvav 12,184,32\nMvav 128,128,48\n"
"Ra 176,3,12288\nMvav 128,132,56\nMvav 132,132,60\nRa 180,3,13312\nMvav 132,128,52\nWL 8\nMvav 140,136,52\nRa 184,3,14336\nMvav 140,140,60\nD1 1,16,46,3072\nMvav 136,140,56\n"
"Ra 188,3,15360\nMvav 136,136,48\nMvav 144,144,48\nRa 128,3,16384\nMvav 144,148,56\nbuffer_load_dword v27, off, s[16:19], s60 offset:512 sc0 sc1\nMvav 148,148,60\nRa 132,3,17408\n"
"CV 132,132,133\nMvav 148,144,52\nWL 8\ns_add_u32 m0, s89, 0\ns_add_u32 s46, s96, 1310720\nMvav 156,152,52\nRa 136,3,18432\nCV 133,134,135\nMvav 156,156,60\nD1 1,16,46,0\n"
"CV 134,140,141\nMvav 152,156,56\nRa 140,3,19456\nCV 135,142,143\nMvav 152,152,48\ns_add_u32 s5, s94, 0\nSv 132,1,16,5,1024\nMvav 64,160,48\nRa 144,3,20480\nMvav 64,164,56\n"
"Mvav 68,164,60\nRa 148,3,21504\nMvav 68,160,52\nWL 8\nCV 128,128,129\nMvav 76,168,52\nRa 152,3,22528\nCV 129,130,131\nMvav 76,172,60\nD1 1,16,46,1024\nCV 130,136,137\n"
"Mvav 72,172,56\nRa 156,3,23552\nCV 131,138,139\nMvav 72,168,48\nSv 128,1,16,5,0\nMvav 4,176,48\nRa 160,3,24576\nMvav 4,180,56\nMvav 8,180,60\nRa 164,3,25600\nMvav 8,176,52\nWL 6\n"
"CV 148,148,149\nMvav 16,184,52\nRa 168,3,26624\nCV 149,150,151\nMvav 16,188,60\nD1 1,16,46,2048\nCV 150,156,157\nMvav 12,188,56\nRa 172,3,27648\nCV 151,158,159\nMvav 12,184,48\n"
"s_add_u32 s5, s94, 131072\nSv 148,1,16,5,1024\nZvav 176,128,96\nRa 176,3,28672\nMvav 176,132,104\nMvav 176,136,112\nRa 180,3,29696\nMvav 176,140,120\n"
"s_waitcnt vmcnt(15) lgkmcnt(6)\nCV 144,144,145\nZvav 180,140,124\nRa 184,3,30720\nCV 145,146,147\nMvav 180,136,116\nD1 1,16,46,3072\nCV 146,152,153\nMvav 180,132,108\n"
"Ra 188,3,31744\ns_barrier\nMvav 180,128,100\nCV 147,154,155\nSv 144,1,16,5,0\nMvav 180,144,36\nRa 128,3,32768\nMvav 180,148,44\nMvav 180,152,52\nRa 132,3,33792\nMvav 180,156,60\n"
"WL 6\ns_add_u32 m0, s89, 32768\ns_add_u32 s46, s96, 1343488\nMvav 176,156,56\nRa 136,3,34816\nCV 68,68,69\nMvav 176,152,48\nD1 1,16,46,0\nCV 69,70,71\nMvav 176,148,40\n"
"Ra 140,3,35840\nCV 70,76,77\nMvav 176,144,32\nCV 71,78,79\ns_add_u32 s5, s94, 262144\nZvav 184,160,96\nRa 144,3,36864\nSv 68,1,16,5,1024\nMvav 184,164,104\nMvav 184,168,112\n"
"Ra 148,3,37888\nMvav 184,172,120\nWL 6\nZvav 188,172,124\nRa 152,3,38912\nCV 64,64,65\nMvav 188,168,116\nD1 1,16,46,1024\nCV 65,66,67\nMvav 188,164,108\nRa 156,3,39936\n"
"CV 66,72,73\nMvav 188,160,100\nCV 67,74,75\nSv 64,1,16,5,0\nMvav 188,176,36\nRa 160,3,40960\nMvav 188,180,44\nMvav 188,184,52\nRa 164,3,41984\nMvav 188,188,60\nWL 6\nCV 8,8,9\n"
"Mvav 184,188,56\nRa 168,3,43008\nCV 9,10,11\nMvav 184,184,48\nD1 1,16,46,2048\nCV 10,16,17\nMvav 184,180,40\nRa 172,3,44032\nCV 11,18,19\nMvav 184,176,32\n"
"s_add_u32 s5, s94, 393216\nSv 8,1,16,5,1024\nZvav 192,128,96\nRa 176,3,45056\nv_lshrrev_b32 v88, 8, v1\nMvav 192,132,104\nv_lshlrev_b32 v89, 5, v88\nMvav 192,136,112\n"
"Ra 180,3,46080\nMvav 192,140,120\nWL 6\nCV 4,4,5\nZvav 196,140,124\nRa 184,3,47104\nv_bfe_u32 v90, v1, 4, 4\nMvav 196,136,116\nD1 1,16,46,3072\nCV 5,6,7\nMvav 196,132,108\n"
"Ra 188,3,48128\nCV 6,12,13\nMvav 196,128,100\nv_mul_u32_u24 v91, 896, v90\nCV 7,14,15\nMvav 196,144,36\nRa 128,3,49152\nv_lshl_add_u32 v91, v88, 4, v91\nWV 15\n"
"v_cmp_ne_u32 vcc, s15, v27\ns_nop 0\ns_cbranch_vccnz .S254\n.S255:\nMvav 196,148,44\ns_add_u32 s4, s92, 0\nSv 4,1,16,5,0\nMvav 196,152,52\nRa 132,3,50176\nMvav 196,156,60\nWL 6\n"
"s_add_u32 m0, s89, 49152\ns_add_u32 s46, s96, 1376256\nbuffer_load_dwordx4 v[208:211], v1, s[16:19], s4 offen offset:0 sc0 sc1\nMvav 192,156,56\nRa 136,3,51200\n"
"v_add_u32 v90, s21, v90\nMvav 192,152,48\nD1 1,16,46,0\nCV 180,180,181\nMvav 192,148,40\nRa 140,3,52224\nCV 181,182,183\nMvav 192,144,32\n"
"buffer_load_dwordx4 v[212:215], v1, s[16:19], s4 offen offset:1024 sc0 sc1\nCV 182,188,189\nZvav 200,160,96\nRa 144,3,53248\nv_lshlrev_b32 v88, 3, v90\nMvav 200,164,104\n"
"v_add_u32 v94, 64, v90\nCV 183,190,191\nMvav 200,168,112\nRa 148,3,54272\ns_add_u32 s5, s94, 524288\nMvav 200,172,120\nWL 6\nSv 180,1,16,5,1024\nZvav 204,172,124\nRa 152,3,55296\n"
"s_mov_b32 s63, 0xc040000\nMvav 204,168,116\nD1 1,16,46,1024\nMvav 204,164,108\nRa 156,3,56320\nMvav 204,160,100\nv_lshrrev_b32 v92, 8, v1\nCV 176,176,177\nMvav 204,176,36\n"
"Ra 160,3,57344\nv_bfe_u32 v93, v1, 6, 2\nMvav 204,180,44\nv_cmp_eq_u32 vcc, v92, v93\nv_and_b32 v92, 16, v1\nv_mov_b32 v93, 0x3f80\nv_cndmask_b32 v93, 0, v93, vcc\n"
"Mvav 204,184,52\nRa 164,3,58368\nCV 177,178,179\nMvav 204,188,60\nWL 6\nCV 178,184,185\nMvav 200,188,56\nRa 168,3,59392\nv_lshlrev_b32 v93, v92, v93\nMvav 200,184,48\n"
"D1 1,16,46,2048\nCV 179,186,187\nMvav 200,180,40\nRa 172,3,60416\nSv 176,1,16,5,0\nMvav 200,176,32\nv_and_b32 v92, 32, v1\nv_cmp_eq_u32 vcc, 0, v92\nv_mov_b32 v4, 0\n"
"v_mov_b32 v5, 0\nv_cndmask_b32 v6, 0, v93, vcc\nZvav 160,128,96\nRa 176,3,61440\nv_sub_u32 v7, v93, v6\nMvav 160,132,104\nv_mov_b32 v8, 0\nMvav 160,136,112\nRa 180,3,62464\n"
"CV 196,196,197\nMvav 160,140,120\nWL 6\nCV 197,198,199\nZvav 164,140,124\nRa 184,3,63488\nv_mov_b32 v9, 0\nMvav 164,136,116\nD1 1,16,46,3072\nCV 198,204,205\nMvav 164,132,108\n"
"Ra 188,3,64512\ns_barrier\nMvav 164,128,100\nCV 199,206,207\ns_add_u32 s5, s94, 655360\nMvav 164,144,36\nRa 128,0,0\nSv 196,1,16,5,1024\nMvav 164,148,44\n"
"s_add_u32 s4, s92, 131072\nMvav 164,152,52\nRa 132,0,1024\nMvav 164,156,60\nWL 6\ns_add_u32 m0, s89, 65536\ns_add_u32 s46, s96, 1409024\n"
"buffer_load_dwordx4 v[216:219], v1, s[16:19], s4 offen offset:0 sc0 sc1\nMvav 160,156,56\nRa 136,0,2048\nCV 192,192,193\nMvav 160,152,48\nD1 1,16,46,0\nCV 193,194,195\n"
"Mvav 160,148,40\nRa 140,0,3072\nCV 194,200,201\nMvav 160,144,32\nbuffer_load_dwordx4 v[220:223], v1, s[16:19], s4 offen offset:1024 sc0 sc1\nCV 195,202,203\nZvav 168,160,96\n"
"Ra 144,0,4096\nWV 9\nv_max_u32 v21, v211, v215\nv_cmp_eq_u32 vcc, -1, v21\ns_nop 0\ns_cbranch_vccnz .S230\n.S231:\nMvav 168,164,104\nv_lshlrev_b32 v176, 16, v208\nSv 192,1,16,5,0\n"
"Mvav 168,168,112\nRa 148,0,5120\nMvav 168,172,120\nWL 6\nv_and_b32 v177, 0xffff0000, v208\nZvav 172,172,124\nRa 152,0,6144\nMvav 172,168,116\nD1 1,16,46,1024\nMvav 172,164,108\n"
"Ra 156,0,7168\nv_lshlrev_b32 v178, 16, v209\nMvav 172,160,100\nv_and_b32 v179, 0xffff0000, v209\nMvav 172,176,36\nRa 160,0,8192\nv_lshlrev_b32 v180, 16, v212\nMvav 172,180,44\n"
"v_and_b32 v181, 0xffff0000, v212\nMvav 172,184,52\nRa 164,0,9216\nMvav 172,188,60\nWL 6\nv_lshlrev_b32 v182, 16, v213\nMvav 168,188,56\nRa 168,0,10240\nMvav 168,184,48\n"
"D1 1,16,46,2048\nMvav 168,180,40\nRa 172,0,11264\nv_and_b32 v183, 0xffff0000, v213\nMvav 168,176,32\nv_lshlrev_b32 v184, 16, v210\nMvav 176,128,96\nRa 176,0,12288\n"
"v_and_b32 v185, 0xffff0000, v210\nMvav 176,132,104\nv_lshlrev_b32 v186, 16, v211\nCV 164,164,165\nMvav 176,136,112\nRa 180,0,13312\nCV 165,166,167\nMvav 176,140,120\nWL 6\n"
"CV 166,172,173\nMvav 180,140,124\nRa 184,0,14336\nCV 167,174,175\nMvav 180,136,116\nD1 1,16,46,3072\ns_add_u32 s5, s94, 786432\nMvav 180,132,108\nRa 188,0,15360\n"
"v_and_b32 v187, 0xffff0000, v211\nMvav 180,128,100\nv_lshlrev_b32 v188, 16, v214\nSv 164,1,16,5,1024\nMvav 180,144,36\nRa 128,0,16384\nMvav 180,148,44\n"
"v_and_b32 v189, 0xffff0000, v214\ns_add_u32 s4, s92, 262144\nMvav 180,152,52\nRa 132,0,17408\nMvav 180,156,60\nWL 6\nv_lshlrev_b32 v190, 16, v215\n"
"buffer_load_dwordx4 v[224:227], v1, s[16:19], s4 offen offset:0 sc0 sc1\nMvav 176,156,56\nRa 136,0,18432\nCV 160,160,161\nMvav 176,152,48\nv_and_b32 v191, 0xffff0000, v215\n"
"CV 161,162,163\nMvav 176,148,40\nRa 140,0,19456\nCV 162,168,169\nMvav 176,144,32\nbuffer_load_dwordx4 v[228:231], v1, s[16:19], s4 offen offset:1024 sc0 sc1\nCV 163,170,171\n"
"Mvav 184,160,96\nRa 144,0,20480\nWV 7\nv_max_u32 v21, v219, v223\nv_cmp_eq_u32 vcc, -1, v21\ns_nop 0\ns_cbranch_vccnz .S233\n.S234:\nMvav 184,164,104\n"
"v_lshlrev_b32 v192, 16, v216\nSv 160,1,16,5,0\nMvav 184,168,112\nRa 148,0,21504\nMvav 184,172,120\nWL 6\nMvav 188,172,124\nRa 152,0,22528\nv_and_b32 v193, 0xffff0000, v216\n"
"Mvav 188,168,116\nv_lshlrev_b32 v194, 16, v217\nMvav 188,164,108\nRa 156,0,23552\nMvav 188,160,100\nv_and_b32 v195, 0xffff0000, v217\nMvav 188,176,36\nRa 160,0,24576\n"
"v_lshlrev_b32 v196, 16, v220\nMvav 188,180,44\nv_and_b32 v197, 0xffff0000, v220\nMvav 188,184,52\nRa 164,0,25600\nMvav 188,188,60\nWL 6\nMvav 184,188,56\nRa 168,0,26624\n"
"v_lshlrev_b32 v198, 16, v221\nMvav 184,184,48\nv_and_b32 v199, 0xffff0000, v221\nMvav 184,180,40\nRa 172,0,27648\nMvav 184,176,32\nv_lshlrev_b32 v200, 16, v218\nMvav 192,128,96\n"
"Ra 176,0,28672\nv_and_b32 v201, 0xffff0000, v218\nMvav 192,132,104\nv_lshlrev_b32 v202, 16, v219\nCV 180,180,181\nMvav 192,136,112\nRa 180,0,29696\nCV 181,182,183\n"
"Mvav 192,140,120\nWL 6\nCV 182,188,189\nMvav 196,140,124\nRa 184,0,30720\nCV 183,190,191\nMvav 196,136,116\nv_and_b32 v203, 0xffff0000, v219\ns_add_u32 s5, s94, 917504\n"
"Mvav 196,132,108\nRa 188,0,31744\ns_barrier\nMvav 196,128,100\nv_lshlrev_b32 v204, 16, v222\nSv 180,1,16,5,1024\nMvav 196,144,36\nRa 128,2,16384\nv_and_b32 v205, 0xffff0000, v222\n"
"Mvav 196,148,44\nv_lshlrev_b32 v206, 16, v223\ns_add_u32 s4, s92, 393216\nMvav 196,152,52\nRa 132,2,17408\nMvav 196,156,60\nWL 6\n"
"buffer_load_dwordx4 v[208:211], v1, s[16:19], s4 offen offset:0 sc0 sc1\nMvav 192,156,56\nRa 136,2,18432\nCV 176,176,177\nMvav 192,152,48\nv_and_b32 v207, 0xffff0000, v223\n"
"CV 177,178,179\nMvav 192,148,40\nRa 140,2,19456\nCV 178,184,185\nMvav 192,144,32\nbuffer_load_dwordx4 v[212:215], v1, s[16:19], s4 offen offset:1024 sc0 sc1\nCV 179,186,187\n"
"Mvav 200,160,96\nRa 144,2,20480\nWV 4\nv_max_u32 v21, v227, v231\nv_cmp_eq_u32 vcc, -1, v21\ns_nop 0\ns_cbranch_vccnz .S236\n.S237:\nMvav 200,164,104\n"
"v_lshlrev_b32 v160, 16, v224\nSv 176,1,16,5,0\nMvav 200,168,112\nRa 148,2,21504\nMvav 200,172,120\nWL 6\nMvav 204,172,124\nRa 152,2,22528\nv_and_b32 v161, 0xffff0000, v224\n"
"Mvav 204,168,116\nv_lshlrev_b32 v162, 16, v225\nMvav 204,164,108\nRa 156,2,23552\nMvav 204,160,100\nv_and_b32 v163, 0xffff0000, v225\nMvav 204,176,36\nRa 160,2,24576\n"
"v_lshlrev_b32 v164, 16, v228\nMvav 204,180,44\nv_and_b32 v165, 0xffff0000, v228\nMvav 204,184,52\nRa 164,2,25600\nMvav 204,188,60\nWL 6\nMvav 200,188,56\nRa 168,2,26624\n"
"v_lshlrev_b32 v166, 16, v229\nMvav 200,184,48\nv_and_b32 v167, 0xffff0000, v229\nMvav 200,180,40\nRa 172,2,27648\nMvav 200,176,32\nv_lshlrev_b32 v168, 16, v226\nMvav 160,128,96\n"
"Ra 176,2,28672\nv_and_b32 v169, 0xffff0000, v226\nMvav 160,132,104\nv_lshlrev_b32 v170, 16, v227\nCV 196,196,197\nMvav 160,136,112\nRa 180,2,29696\nCV 197,198,199\n"
"Mvav 160,140,120\nWL 6\nCV 198,204,205\nMvav 164,140,124\nRa 184,2,30720\nv_and_b32 v24, 0x70, v1\nMvav 164,136,116\nv_and_b32 v171, 0xffff0000, v227\nCV 199,206,207\n"
"Mvav 164,132,108\nRa 188,2,31744\ns_add_u32 s5, s94, 1048576\nMvav 164,128,100\nv_lshlrev_b32 v172, 16, v230\nv_lshlrev_b32 v24, 5, v24\nMvav 164,144,36\nRa 128,2,0\n"
"v_and_b32 v173, 0xffff0000, v230\nMvav 164,148,44\nv_lshlrev_b32 v174, 16, v231\ns_add_u32 s4, s92, 524288\nMvav 164,152,52\nRa 132,2,1024\n"
"buffer_load_dword v27, v24, s[16:19], s41 offen sc0 sc1\nMvav 164,156,60\nWL 6\nbuffer_load_dwordx4 v[216:219], v1, s[16:19], s4 offen offset:0 sc0 sc1\nMvav 160,156,56\n"
"Ra 136,2,2048\nSv 196,1,16,5,1024\nMvav 160,152,48\nv_and_b32 v175, 0xffff0000, v231\nMvav 160,148,40\nRa 140,2,3072\nMvav 160,144,32\n"
"buffer_load_dwordx4 v[220:223], v1, s[16:19], s4 offen offset:1024 sc0 sc1\nMvav 168,160,96\nRa 144,2,4096\nWV 5\nv_max_u32 v21, v211, v215\nv_cmp_eq_u32 vcc, -1, v21\ns_nop 0\n"
"s_cbranch_vccnz .S239\n.S240:\nMvav 168,164,104\nv_lshlrev_b32 v176, 16, v208\nCV 192,192,193\nMvav 168,168,112\nRa 148,2,5120\nCV 193,194,195\nMvav 168,172,120\nWL 6\n"
"CV 194,200,201\nMvav 172,172,124\nRa 152,2,6144\nv_and_b32 v177, 0xffff0000, v208\nMvav 172,168,116\nv_lshlrev_b32 v178, 16, v209\nCV 195,202,203\nMvav 172,164,108\nRa 156,2,7168\n"
"Sv 192,1,16,5,0\nMvav 172,160,100\nv_and_b32 v179, 0xffff0000, v209\nMvav 172,176,36\nRa 160,2,8192\nv_lshlrev_b32 v180, 16, v212\nMvav 172,180,44\n"
"v_and_b32 v181, 0xffff0000, v212\nMvav 172,184,52\nRa 164,2,9216\nMvav 172,188,60\nWL 6\nMvav 168,188,56\nRa 168,2,10240\nv_lshlrev_b32 v182, 16, v213\nMvav 168,184,48\n"
"v_and_b32 v183, 0xffff0000, v213\nMvav 168,180,40\nRa 172,2,11264\nMvav 168,176,32\nv_lshlrev_b32 v184, 16, v210\nMvav 176,128,96\nRa 176,2,12288\n"
"v_and_b32 v185, 0xffff0000, v210\nMvav 176,132,104\nv_lshlrev_b32 v186, 16, v211\nCV 164,164,165\nMvav 176,136,112\nRa 180,2,13312\nCV 165,166,167\nMvav 176,140,120\nWL 6\n"
"CV 166,172,173\nMvav 180,140,124\nRa 184,2,14336\nCV 167,174,175\nMvav 180,136,116\nv_and_b32 v187, 0xffff0000, v211\ns_add_u32 s5, s94, 1179648\nMvav 180,132,108\nRa 188,2,15360\n"
"s_barrier\nMvav 180,128,100\nv_lshlrev_b32 v188, 16, v214\nSv 164,1,16,5,1024\nMvav 180,144,36\nRa 128,2,32768\nv_and_b32 v189, 0xffff0000, v214\nMvav 180,148,44\n"
"v_lshlrev_b32 v190, 16, v215\ns_add_u32 s4, s92, 655360\nMvav 180,152,52\nRa 132,2,33792\nMvav 180,156,60\nWL 6\n"
"buffer_load_dwordx4 v[224:227], v1, s[16:19], s4 offen offset:0 sc0 sc1\nMvav 176,156,56\nRa 136,2,34816\nCV 160,160,161\nMvav 176,152,48\nv_and_b32 v191, 0xffff0000, v215\n"
"CV 161,162,163\nMvav 176,148,40\nRa 140,2,35840\nCV 162,168,169\nMvav 176,144,32\nbuffer_load_dwordx4 v[228:231], v1, s[16:19], s4 offen offset:1024 sc0 sc1\nCV 163,170,171\n"
"Mvav 184,160,96\nRa 144,2,36864\nWV 4\nv_max_u32 v21, v219, v223\nv_cmp_eq_u32 vcc, -1, v21\ns_nop 0\ns_cbranch_vccnz .S242\n.S243:\nMvav 184,164,104\n"
"v_lshlrev_b32 v192, 16, v216\nSv 160,1,16,5,0\nMvav 184,168,112\nRa 148,2,37888\nMvav 184,172,120\nWL 6\nMvav 188,172,124\nRa 152,2,38912\nv_and_b32 v193, 0xffff0000, v216\n"
"Mvav 188,168,116\nv_lshlrev_b32 v194, 16, v217\nMvav 188,164,108\nRa 156,2,39936\nMvav 188,160,100\nv_and_b32 v195, 0xffff0000, v217\nMvav 188,176,36\nRa 160,2,40960\n"
"v_lshlrev_b32 v196, 16, v220\nMvav 188,180,44\nv_and_b32 v197, 0xffff0000, v220\nMvav 188,184,52\nRa 164,2,41984\nMvav 188,188,60\nWL 6\nMvav 184,188,56\nRa 168,2,43008\n"
"v_lshlrev_b32 v198, 16, v221\nMvav 184,184,48\nv_and_b32 v199, 0xffff0000, v221\nMvav 184,180,40\nRa 172,2,44032\nMvav 184,176,32\nv_lshlrev_b32 v200, 16, v218\nMvav 192,128,96\n"
"Ra 176,2,45056\nv_and_b32 v201, 0xffff0000, v218\nMvav 192,132,104\nv_lshlrev_b32 v202, 16, v219\nCV 180,180,181\nMvav 192,136,112\nRa 180,2,46080\nCV 181,182,183\n"
"Mvav 192,140,120\nWL 6\nCV 182,188,189\nMvav 196,140,124\nRa 184,2,47104\nCV 183,190,191\nMvav 196,136,116\nv_and_b32 v203, 0xffff0000, v219\ns_add_u32 s5, s94, 1310720\n"
"Mvav 196,132,108\nRa 188,2,48128\nSv 180,1,16,5,1024\nMvav 196,128,100\nv_lshlrev_b32 v204, 16, v222\nMvav 196,144,36\nRa 128,2,49152\nv_and_b32 v205, 0xffff0000, v222\n"
"Mvav 196,148,44\nv_lshlrev_b32 v206, 16, v223\ns_add_u32 s4, s92, 786432\nMvav 196,152,52\nRa 132,2,50176\nCV 176,176,177\nMvav 196,156,60\nWL 6\n"
"buffer_load_dwordx4 v[208:211], v1, s[16:19], s4 offen offset:0 sc0 sc1\nMvav 192,156,56\nRa 136,2,51200\nCV 177,178,179\nMvav 192,152,48\nv_and_b32 v207, 0xffff0000, v223\n"
"CV 178,184,185\nMvav 192,148,40\nRa 140,2,52224\nCV 179,186,187\nMvav 192,144,32\nbuffer_load_dwordx4 v[212:215], v1, s[16:19], s4 offen offset:1024 sc0 sc1\nSv 176,1,16,5,0\n"
"Mvav 200,160,96\nRa 144,2,53248\nWV 5\nv_max_u32 v21, v227, v231\nv_cmp_eq_u32 vcc, -1, v21\ns_nop 0\ns_cbranch_vccnz .S245\n.S246:\nMvav 200,164,104\n"
"v_lshlrev_b32 v160, 16, v224\nv_xor_b32 v27, s15, v27\nv_and_b32 v26, 0xffffdfff, v27\nv_cmp_ne_u32 s[64:65], 0, v27\nv_cmp_ne_u32 vcc, 0, v26\ns_nop 0\ns_cbranch_vccnz .S257\n"
".S258:\nMvav 200,168,112\nRa 148,2,54272\ns_bitcmp1_b32 s64, 0\ns_cselect_b32 s2, 0x2000, 0\nMvav 200,172,120\nWL 6\nMvav 204,172,124\nRa 152,2,55296\n"
"v_and_b32 v161, 0xffff0000, v224\nMvav 204,168,116\nv_lshlrev_b32 v162, 16, v225\nMvav 204,164,108\nRa 156,2,56320\ns_xor_b32 s2, s2, s20\nMvav 204,160,100\n"
"v_and_b32 v163, 0xffff0000, v225\nMvav 204,176,36\nRa 160,2,57344\nv_lshlrev_b32 v164, 16, v228\nMvav 204,180,44\nv_and_b32 v165, 0xffff0000, v228\nMvav 204,184,52\n"
"Ra 164,2,58368\nMvav 204,188,60\nWL 6\ns_add_u32 s24, s2, 0x0\nMvav 200,188,56\nRa 168,2,59392\nv_lshlrev_b32 v166, 16, v229\nMvav 200,184,48\nv_and_b32 v167, 0xffff0000, v229\n"
"s_bitcmp1_b32 s64, 1\ns_cselect_b32 s2, 0x2000, 0\nMvav 200,180,40\nRa 172,2,60416\nMvav 200,176,32\nv_lshlrev_b32 v168, 16, v226\nMvav 160,128,96\nRa 176,2,61440\n"
"v_and_b32 v169, 0xffff0000, v226\nMvav 160,132,104\nv_lshlrev_b32 v170, 16, v227\nCV 196,196,197\nMvav 160,136,112\nRa 180,2,62464\nCV 197,198,199\nMvav 160,140,120\nWL 6\n"
"CV 198,204,205\nMvav 164,140,124\nRa 184,2,63488\nCV 199,206,207\nMvav 164,136,116\nv_and_b32 v171, 0xffff0000, v227\ns_add_u32 s5, s94, 1441792\nMvav 164,132,108\nRa 188,2,64512\n"
"s_barrier\nMvav 164,128,100\nv_lshlrev_b32 v172, 16, v230\nSv 196,1,16,5,1024\nMvav 164,144,36\nRa 128,3,0\nv_and_b32 v173, 0xffff0000, v230\nMvav 164,148,44\n"
"v_lshlrev_b32 v174, 16, v231\nMvav 164,152,52\nRa 132,3,1024\nMvav 164,156,60\nWL 6\ns_xor_b32 s2, s2, s20\nMvav 160,156,56\nRa 136,3,2048\nCV 192,192,193\nMvav 160,152,48\n"
"v_and_b32 v175, 0xffff0000, v231\nCV 193,194,195\nMvav 160,148,40\nRa 140,3,3072\nCV 194,200,201\nMvav 160,144,32\ns_add_u32 s25, s2, 0x1800000\ns_bitcmp1_b32 s64, 2\n"
"s_cselect_b32 s2, 0x2000, 0\nMvav 168,160,96\nRa 144,3,4096\nWV 2\nv_max_u32 v21, v211, v215\nv_cmp_eq_u32 vcc, -1, v21\ns_nop 0\ns_cbranch_vccnz .S248\n.S249:\nMvav 168,164,104\n"
"v_lshlrev_b32 v176, 16, v208\nCV 195,202,203\nMvav 168,168,112\nRa 148,3,5120\nSv 192,1,16,5,0\nMvav 168,172,120\nWL 6\nMvav 172,172,124\nRa 152,3,6144\n"
"v_and_b32 v177, 0xffff0000, v208\nMvav 172,168,116\nv_lshlrev_b32 v178, 16, v209\nMvav 172,164,108\nRa 156,3,7168\ns_xor_b32 s2, s2, s20\nMvav 172,160,100\n"
"v_and_b32 v179, 0xffff0000, v209\nMvav 172,176,36\nRa 160,3,8192\nv_lshlrev_b32 v180, 16, v212\nMvav 172,180,44\nv_and_b32 v181, 0xffff0000, v212\nMvav 172,184,52\nRa 164,3,9216\n"
"Mvav 172,188,60\nWL 6\ns_add_u32 s26, s2, 0x3000000\nMvav 168,188,56\nRa 168,3,10240\nv_lshlrev_b32 v182, 16, v213\nMvav 168,184,48\nv_and_b32 v183, 0xffff0000, v213\n"
"s_bitcmp1_b32 s64, 3\ns_cselect_b32 s2, 0x2000, 0\nMvav 168,180,40\nRa 172,3,11264\nMvav 168,176,32\nv_lshlrev_b32 v184, 16, v210\nMvav 176,128,96\nRa 176,3,12288\n"
"s_xor_b32 s2, s2, s20\nMvav 176,132,104\nv_and_b32 v185, 0xffff0000, v210\nCV 164,164,165\nMvav 176,136,112\nRa 180,3,13312\nCV 165,166,167\nMvav 176,140,120\nWL 6\n"
"CV 166,172,173\nMvav 180,140,124\nRa 184,3,14336\nCV 167,174,175\nMvav 180,136,116\nv_lshlrev_b32 v186, 16, v211\ns_add_u32 s5, s94, 1572864\nMvav 180,132,108\nRa 188,3,15360\n"
"s_add_u32 s27, s2, 0x4800000\nMvav 180,128,100\nv_and_b32 v187, 0xffff0000, v211\ns_bitcmp1_b32 s64, 4\ns_cselect_b32 s2, 0x2000, 0\nMvav 180,144,36\nv_lshlrev_b32 v188, 16, v214\n"
"Sv 164,1,16,5,1024\nMvav 180,148,44\nv_and_b32 v189, 0xffff0000, v214\nMvav 180,152,52\nv_lshlrev_b32 v190, 16, v215\nMvav 180,156,60\nWL 4\ns_xor_b32 s2, s2, s20\n"
"Mvav 176,156,56\nv_and_b32 v191, 0xffff0000, v215\nCV 160,160,161\nMvav 176,152,48\ns_add_u32 s32, s2, 0x6000000\ns_bitcmp1_b32 s64, 5\ns_cselect_b32 s2, 0x2000, 0\n"
"Mvav 176,148,40\ns_xor_b32 s2, s2, s20\nCV 161,162,163\nMvav 176,144,32\ns_add_u32 s33, s2, 0x7800000\ns_bitcmp1_b32 s64, 6\ns_cselect_b32 s2, 0x2000, 0\nMvav 184,160,96\n"
"s_xor_b32 s2, s2, s20\ns_add_u32 s34, s2, 0x9000000\nMvav 184,164,104\ns_bitcmp1_b32 s64, 7\ns_cselect_b32 s2, 0x2000, 0\nMvav 184,168,112\ns_xor_b32 s2, s2, s20\nCV 162,168,169\n"
"Mvav 184,172,120\nWL 0\nCV 163,170,171\nMvav 188,172,124\ns_add_u32 s35, s2, 0xa800000\nSv 160,1,16,5,0\nMvav 188,168,116\nMvav 188,164,108\nMvav 188,160,100\nMvav 188,176,36\n"
"Mvav 188,180,44\nMvav 188,184,52\nMvav 188,188,60\nMvav 184,188,56\nMvav 184,184,48\nMvav 184,180,40\nMvav 184,176,32\nCV 180,180,181\nCV 181,182,183\ns_nop 1\nCV 182,188,189\n"
"CV 183,190,191\ns_add_u32 s5, s94, 1703936\nSv 180,1,16,5,1024\nCV 176,176,177\nCV 177,178,179\nCV 178,184,185\nCV 179,186,187\nSv 176,1,16,5,0\ns_cmp_eq_u32 s13, 0\n"
"s_cbranch_scc1 .Z260\nbuffer_load_dwordx4 v[12:15], v91, s[28:31], s22 offen offset:0\nbuffer_load_dwordx4 v[16:19], v91, s[28:31], s23 offen offset:0\n"
"buffer_load_dwordx4 v[64:67], v89, s[16:19], s40 offen offset:0 sc0 sc1\nbuffer_load_dwordx4 v[68:71], v89, s[16:19], s40 offen offset:16 sc0 sc1\n"
"buffer_load_dwordx4 v[72:75], v89, s[16:19], s40 offen offset:1792 sc0 sc1\nbuffer_load_dwordx4 v[76:79], v89, s[16:19], s40 offen offset:1808 sc0 sc1\n"
"buffer_load_dwordx2 v[80:81], v88, s[16:19], s63 offen offset:0 sc0 sc1\nbuffer_load_dwordx2 v[82:83], v88, s[16:19], s63 offen offset:512 sc0 sc1\n"
"buffer_load_dwordx4 v[96:99], v1, s[16:19], s24 offen offset:0 sc0 sc1\nbuffer_load_dwordx4 v[100:103], v1, s[16:19], s24 offen offset:1024 sc0 sc1\n"
"buffer_load_dwordx4 v[104:107], v1, s[16:19], s25 offen offset:0 sc0 sc1\nbuffer_load_dwordx4 v[108:111], v1, s[16:19], s25 offen offset:1024 sc0 sc1\n"
"buffer_load_dwordx4 v[112:115], v1, s[16:19], s26 offen offset:0 sc0 sc1\nbuffer_load_dwordx4 v[116:119], v1, s[16:19], s26 offen offset:1024 sc0 sc1\n"
"buffer_load_dwordx4 v[120:123], v1, s[16:19], s27 offen offset:0 sc0 sc1\nbuffer_load_dwordx4 v[124:127], v1, s[16:19], s27 offen offset:1024 sc0 sc1\n"
"buffer_load_dwordx4 v[32:35], v1, s[16:19], s32 offen offset:0 sc0 sc1\nbuffer_load_dwordx4 v[36:39], v1, s[16:19], s32 offen offset:1024 sc0 sc1\n"
"buffer_load_dwordx4 v[40:43], v1, s[16:19], s33 offen offset:0 sc0 sc1\nbuffer_load_dwordx4 v[44:47], v1, s[16:19], s33 offen offset:1024 sc0 sc1\n"
"buffer_load_dwordx4 v[48:51], v1, s[16:19], s34 offen offset:0 sc0 sc1\nbuffer_load_dwordx4 v[52:55], v1, s[16:19], s34 offen offset:1024 sc0 sc1\n"
"buffer_load_dwordx4 v[56:59], v1, s[16:19], s35 offen offset:0 sc0 sc1\nbuffer_load_dwordx4 v[60:63], v1, s[16:19], s35 offen offset:1024 sc0 sc1\nWV 16\nv_xor_b32 v66, s15, v66\n"
"v_xor_b32 v70, s15, v70\nv_xor_b32 v74, s15, v74\nv_xor_b32 v78, s15, v78\nv_xor_b32 v81, s15, v81\nv_xor_b32 v83, s15, v83\nv_or3_b32 v92, v66, v70, v74\n"
"v_or3_b32 v92, v92, v78, v81\nv_or_b32 v92, v92, v83\nv_cmp_ne_u32 vcc, 0, v92\ns_nop 0\ns_cbranch_vccnz .S251\n.S252:\nv_and_b32 v92, 0x7ff, v67\nv_lshrrev_b32 v93, 16, v67\n"
"v_cmp_eq_u32 s[44:45], v92, v90\nv_cmp_eq_u32 s[46:47], v93, v90\nv_cmp_eq_u32 s[48:49], v92, v94\nv_cmp_eq_u32 s[50:51], v93, v94\nv_cndmask_b32 v12, v12, v64, s[44:45]\n"
"v_cndmask_b32 v13, v13, v65, s[44:45]\nv_cndmask_b32 v14, v14, v68, s[44:45]\nv_cndmask_b32 v15, v15, v69, s[44:45]\nv_cndmask_b32 v12, v12, v72, s[46:47]\n"
"v_cndmask_b32 v13, v13, v73, s[46:47]\nv_cndmask_b32 v14, v14, v76, s[46:47]\nv_cndmask_b32 v15, v15, v77, s[46:47]\nv_cndmask_b32 v16, v16, v64, s[48:49]\n"
"v_cndmask_b32 v17, v17, v65, s[48:49]\nv_cndmask_b32 v18, v18, v68, s[48:49]\nv_cndmask_b32 v19, v19, v69, s[48:49]\nv_cndmask_b32 v16, v16, v72, s[50:51]\n"
"v_cndmask_b32 v17, v17, v73, s[50:51]\nv_cndmask_b32 v18, v18, v76, s[50:51]\nv_cndmask_b32 v19, v19, v77, s[50:51]\nv_sub_f32 v92, 1.0, v80\nv_sub_f32 v93, 1.0, v82\nCV 92,92,93\n"
"v_and_b32 v93, 0xffff0000, v92\nv_lshlrev_b32 v92, 16, v92\nv_lshlrev_b32 v64, 16, v12\nv_and_b32 v65, 0xffff0000, v12\nMU 64,64,92\nMU 65,65,92\nv_lshlrev_b32 v66, 16, v13\n"
"v_and_b32 v67, 0xffff0000, v13\nMU 66,66,92\nMU 67,67,92\nv_lshlrev_b32 v68, 16, v14\nv_and_b32 v69, 0xffff0000, v14\nMU 68,68,92\nMU 69,69,92\nv_lshlrev_b32 v70, 16, v15\n"
"v_and_b32 v71, 0xffff0000, v15\nMU 70,70,92\nMU 71,71,92\nv_lshlrev_b32 v72, 16, v16\nv_and_b32 v73, 0xffff0000, v16\nMU 72,72,93\nMU 73,73,93\nv_lshlrev_b32 v74, 16, v17\n"
"v_and_b32 v75, 0xffff0000, v17\nMU 74,74,93\nMU 75,75,93\nv_lshlrev_b32 v76, 16, v18\nv_and_b32 v77, 0xffff0000, v18\nMU 76,76,93\nMU 77,77,93\nv_lshlrev_b32 v78, 16, v19\n"
"v_and_b32 v79, 0xffff0000, v19\nMU 78,78,93\nMU 79,79,93\ns_mov_b32 s7, 262144\nWV 0\n.Z263:\nv_max_u32 v84, v99, v103\nv_cmp_eq_u32 s[44:45], -1, v84\nv_max_u32 v84, v107, v111\n"
"v_cmp_eq_u32 s[46:47], -1, v84\nv_max_u32 v84, v115, v119\nv_cmp_eq_u32 s[48:49], -1, v84\nv_max_u32 v84, v123, v127\nv_cmp_eq_u32 s[50:51], -1, v84\nv_max_u32 v84, v35, v39\n"
"v_cmp_eq_u32 s[52:53], -1, v84\nv_max_u32 v84, v43, v47\nv_cmp_eq_u32 s[54:55], -1, v84\nv_max_u32 v84, v51, v55\nv_cmp_eq_u32 s[56:57], -1, v84\nv_max_u32 v84, v59, v63\n"
"v_cmp_eq_u32 s[58:59], -1, v84\ns_or_b64 s[0:1], s[44:45], s[46:47]\ns_or_b64 s[2:3], s[48:49], s[50:51]\ns_or_b64 s[0:1], s[0:1], s[52:53]\ns_or_b64 s[2:3], s[2:3], s[54:55]\n"
"s_or_b64 s[0:1], s[0:1], s[56:57]\ns_or_b64 s[2:3], s[2:3], s[58:59]\ns_or_b64 s[0:1], s[0:1], s[2:3]\ns_cbranch_scc1 .Z261\n.Z262:\nZvvv 136,6,96\nMvvv 136,6,104\nMvvv 136,6,112\n"
"Mvvv 136,6,120\nMvvv 136,6,32\nMvvv 136,6,40\nMvvv 136,6,48\nMvvv 136,6,56\nZvvv 140,4,96\nMvvv 140,4,104\nMvvv 140,4,112\nMvvv 140,4,120\nMvvv 140,4,32\nMvvv 140,4,40\n"
"Mvvv 140,4,48\nMvvv 140,4,56\nZvvv 144,6,100\nMvvv 144,6,108\nMvvv 144,6,116\nMvvv 144,6,124\nMvvv 144,6,36\nMvvv 144,6,44\nMvvv 144,6,52\nMvvv 144,6,60\nZvvv 148,4,100\n"
"Mvvv 148,4,108\nMvvv 148,4,116\nMvvv 148,4,124\nMvvv 148,4,36\nMvvv 148,4,44\nMvvv 148,4,52\nMvvv 148,4,60\nFM 64,80,136\nFM 65,80,137\nFM 66,80,138\nFM 67,80,139\nFM 68,80,140\n"
"FM 69,80,141\nFM 70,80,142\nFM 71,80,143\nCV 12,64,65\nCV 13,66,67\nCV 14,68,69\nCV 15,70,71\nSv 12,91,36,22,0\nFM 72,82,144\nFM 73,82,145\nFM 74,82,146\nFM 75,82,147\n"
"FM 76,82,148\nFM 77,82,149\nFM 78,82,150\nFM 79,82,151\nCV 16,72,73\nCV 17,74,75\nCV 18,76,77\nCV 19,78,79\nSv 16,91,36,23,0\n.Z260:\ns_endpgm\n.E204:\nv_cmpx_gt_i32 vcc, 19, v3\n"
"v_mov_b32 v15, s97\nv_cmpx_gt_i32 vcc, 18, v3\nv_mov_b32 v14, s97\nv_cmpx_gt_i32 vcc, 17, v3\nv_mov_b32 v13, s97\nv_cmpx_gt_i32 vcc, 16, v3\nv_mov_b32 v12, s97\n"
"v_cmpx_gt_i32 vcc, 3, v3\nv_mov_b32 v7, s97\nv_cmpx_gt_i32 vcc, 2, v3\nv_mov_b32 v6, s97\nv_cmpx_gt_i32 vcc, 1, v3\nv_mov_b32 v5, s97\nv_cmpx_gt_i32 vcc, 0, v3\nv_mov_b32 v4, s97\n"
"s_mov_b64 exec, -1\ns_nop 2\ns_branch .E205\n.E211:\nv_cmpx_gt_i32 vcc, 19, v0\nv_mov_b32 v15, s97\nv_cmpx_gt_i32 vcc, 18, v0\nv_mov_b32 v14, s97\nv_cmpx_gt_i32 vcc, 17, v0\n"
"v_mov_b32 v13, s97\nv_cmpx_gt_i32 vcc, 16, v0\nv_mov_b32 v12, s97\nv_cmpx_gt_i32 vcc, 3, v0\nv_mov_b32 v7, s97\nv_cmpx_gt_i32 vcc, 2, v0\nv_mov_b32 v6, s97\n"
"v_cmpx_gt_i32 vcc, 1, v0\nv_mov_b32 v5, s97\nv_cmpx_gt_i32 vcc, 0, v0\nv_mov_b32 v4, s97\ns_mov_b64 exec, -1\ns_nop 2\ns_branch .E212\n.E218:\n"
"buffer_load_dwordx4 v[100:103], v108, s[16:19], s65 offen offset:0 sc0 sc1\nbuffer_load_dwordx4 v[104:107], v108, s[16:19], s65 offen offset:256 sc0 sc1\ns_branch .E219\n.S149:\n"
"s_mov_b32 s7, 262144\n.S151:\nbuffer_load_dwordx4 v[32:35], v1, s[16:19], s6 offen offset:0 sc0 sc1\nbuffer_load_dwordx4 v[36:39], v1, s[16:19], s6 offen offset:1024 sc0 sc1\n"
"buffer_load_dwordx4 v[40:43], v1, s[16:19], s6 offen offset:2048 sc0 sc1\nbuffer_load_dwordx4 v[44:47], v1, s[16:19], s6 offen offset:3072 sc0 sc1\nWV 0\nv_xor_b32 v48, s15, v33\n"
"v_xor_b32 v49, s15, v35\nv_xor_b32 v50, s15, v37\nv_xor_b32 v51, s15, v39\nv_xor_b32 v52, s15, v41\nv_xor_b32 v53, s15, v43\nv_xor_b32 v54, s15, v45\nv_xor_b32 v55, s15, v47\n"
"v_or3_b32 v48, v48, v49, v50\nv_or3_b32 v51, v51, v52, v53\nv_or3_b32 v48, v48, v54, v55\nv_or_b32 v48, v48, v51\nv_cmp_ne_u32 vcc, 0, v48\ns_nop 0\ns_cbranch_vccz .S150\n"
"s_sleep 6\ns_sub_u32 s7, s7, 1\ns_cbranch_scc0 .S151\ns_branch .S150\n.S152:\ns_mov_b32 s7, 262144\n.S154:\ns_lshl_b32 s5, s73, 8\ns_add_u32 s5, s5, 0xc04ac00\n"
"v_lshrrev_b32 v27, 2, v1\nbuffer_load_dword v27, v27, s[16:19], s5 offen sc0 sc1\nWV 0\nv_xor_b32 v27, s15, v27\nv_and_b32 v255, 0xffffdfff, v27\nv_cmp_ne_u32 vcc, 0, v255\n"
"s_nop 0\ns_cbranch_vccz .S153\ns_sleep 6\ns_sub_u32 s7, s7, 1\ns_cbranch_scc0 .S154\ns_branch .S153\n.T157:\ns_mov_b32 s62, 1\ns_call_b64 s[80:81], .RA10\ns_branch .T158\n.T159:\n"
"s_mov_b32 s62, 1\ns_call_b64 s[80:81], .RA10\ns_branch .T160\n.T162:\ns_mov_b32 s62, 0\ns_call_b64 s[80:81], .RB11\ns_branch .T163\n.T164:\ns_mov_b32 s62, 0\n"
"s_call_b64 s[80:81], .RA10\ns_branch .T165\n.T167:\ns_mov_b32 s62, 0\ns_call_b64 s[80:81], .RB11\ns_branch .T168\n.T169:\ns_mov_b32 s62, 0\ns_call_b64 s[80:81], .RA10\n"
"s_branch .T170\n.S172:\ns_mov_b32 s7, 262144\n.S174:\nv_and_b32 v27, 0x70, v1\nbuffer_load_dword v27, v27, s[16:19], s61 offen offset:128 sc0 sc1\nWV 0\n"
"v_cmp_ne_u32 vcc, s15, v27\ns_nop 0\ns_cbranch_vccz .S173\ns_sleep 6\ns_sub_u32 s7, s7, 1\ns_cbranch_scc0 .S174\ns_branch .S173\n.S175:\ns_mov_b32 s7, 262144\n.S177:\n"
"v_and_b32 v27, 0x70, v1\nbuffer_load_dword v27, v27, s[16:19], s61 offen offset:136 sc0 sc1\nWV 0\nv_cmp_ne_u32 vcc, s15, v27\ns_nop 0\ns_cbranch_vccz .S176\ns_sleep 6\n"
"s_sub_u32 s7, s7, 1\ns_cbranch_scc0 .S177\ns_branch .S176\n.S178:\ns_mov_b32 s7, 262144\n.S180:\nv_and_b32 v27, 0x70, v1\n"
"buffer_load_dword v27, v27, s[16:19], s61 offen offset:140 sc0 sc1\nWV 0\nv_cmp_ne_u32 vcc, s15, v27\ns_nop 0\ns_cbranch_vccz .S179\ns_sleep 6\ns_sub_u32 s7, s7, 1\n"
"s_cbranch_scc0 .S180\ns_branch .S179\n.S181:\ns_mov_b32 s7, 262144\n.S183:\nv_and_b32 v27, 0x70, v1\nbuffer_load_dword v27, v27, s[16:19], s61 offen offset:12 sc0 sc1\nWV 0\n"
"v_cmp_ne_u32 vcc, s15, v27\ns_nop 0\ns_cbranch_vccz .S182\ns_sleep 6\ns_sub_u32 s7, s7, 1\ns_cbranch_scc0 .S183\ns_branch .S182\n.S184:\ns_mov_b32 s7, 262144\n.S186:\n"
"v_and_b32 v27, 0x70, v1\nbuffer_load_dword v27, v27, s[16:19], s61 offen offset:8 sc0 sc1\nWV 0\nv_cmp_ne_u32 vcc, s15, v27\ns_nop 0\ns_cbranch_vccz .S185\ns_sleep 6\n"
"s_sub_u32 s7, s7, 1\ns_cbranch_scc0 .S186\ns_branch .S185\n.S187:\ns_mov_b32 s7, 262144\n.S189:\nv_and_b32 v27, 0xf0, v1\n"
"buffer_load_dword v27, v27, s[16:19], s61 offen offset:4 sc0 sc1\nWV 0\nv_cmp_ne_u32 vcc, s15, v27\ns_nop 0\ns_cbranch_vccz .S188\ns_sleep 6\ns_sub_u32 s7, s7, 1\n"
"s_cbranch_scc0 .S189\ns_branch .S188\n.S191:\ns_mov_b32 s7, 262144\n.S193:\nv_lshrrev_b32 v144, 9, v1\nv_mul_u32_u24 v144, 0x18000, v144\nv_lshlrev_b32 v144, 8, v144\n"
"v_and_b32 v145, 0x1f0, v1\nv_add_u32 v144, v144, v145\ns_mov_b32 s6, 0x15c0000\nbuffer_load_dword v136, v144, s[16:19], s6 offen offset:4 sc0 sc1\ns_mov_b32 s6, 0x15c2000\n"
"buffer_load_dword v137, v144, s[16:19], s6 offen offset:4 sc0 sc1\ns_mov_b32 s6, 0x45c0000\nbuffer_load_dword v138, v144, s[16:19], s6 offen offset:4 sc0 sc1\n"
"s_mov_b32 s6, 0x45c2000\nbuffer_load_dword v139, v144, s[16:19], s6 offen offset:4 sc0 sc1\ns_mov_b32 s6, 0x75c0000\n"
"buffer_load_dword v140, v144, s[16:19], s6 offen offset:4 sc0 sc1\ns_mov_b32 s6, 0x75c2000\nbuffer_load_dword v141, v144, s[16:19], s6 offen offset:4 sc0 sc1\n"
"s_mov_b32 s6, 0xa5c0000\nbuffer_load_dword v142, v144, s[16:19], s6 offen offset:4 sc0 sc1\ns_mov_b32 s6, 0xa5c2000\n"
"buffer_load_dword v143, v144, s[16:19], s6 offen offset:4 sc0 sc1\nWV 0\nv_xor_b32 v136, s15, v136\nv_xor_b32 v137, s15, v137\nv_xor_b32 v138, s15, v138\n"
"v_xor_b32 v139, s15, v139\nv_xor_b32 v140, s15, v140\nv_xor_b32 v141, s15, v141\nv_xor_b32 v142, s15, v142\nv_xor_b32 v143, s15, v143\nv_min_u32 v136, v136, v137\n"
"v_min_u32 v138, v138, v139\nv_min_u32 v140, v140, v141\nv_min_u32 v142, v142, v143\nv_or3_b32 v136, v136, v138, v140\nv_or_b32 v136, v136, v142\nv_cmp_ne_u32 vcc, 0, v136\n"
"s_nop 0\ns_cbranch_vccz .S192\ns_sleep 6\ns_sub_u32 s7, s7, 1\ns_cbranch_scc0 .S193\ns_branch .S192\n.FF190:\nv_lshrrev_b32 v144, 9, v1\nv_mul_u32_u24 v144, 0x18000, v144\n"
"v_lshlrev_b32 v144, 8, v144\nv_and_b32 v145, 0x1f0, v1\nv_add_u32 v144, v144, v145\ns_mov_b32 s6, 0x15c0000\nbuffer_load_dword v136, v144, s[16:19], s6 offen offset:4 sc0 sc1\n"
"s_mov_b32 s6, 0x15c2000\nbuffer_load_dword v137, v144, s[16:19], s6 offen offset:4 sc0 sc1\ns_mov_b32 s6, 0x45c0000\n"
"buffer_load_dword v138, v144, s[16:19], s6 offen offset:4 sc0 sc1\ns_mov_b32 s6, 0x45c2000\nbuffer_load_dword v139, v144, s[16:19], s6 offen offset:4 sc0 sc1\n"
"s_mov_b32 s6, 0x75c0000\nbuffer_load_dword v140, v144, s[16:19], s6 offen offset:4 sc0 sc1\ns_mov_b32 s6, 0x75c2000\n"
"buffer_load_dword v141, v144, s[16:19], s6 offen offset:4 sc0 sc1\ns_mov_b32 s6, 0xa5c0000\nbuffer_load_dword v142, v144, s[16:19], s6 offen offset:4 sc0 sc1\n"
"s_mov_b32 s6, 0xa5c2000\nbuffer_load_dword v143, v144, s[16:19], s6 offen offset:4 sc0 sc1\ns_mov_b32 s90, 0\ns_swappc_b64 s[8:9], s[80:81]\nv_xor_b32 v136, s15, v136\n"
"v_xor_b32 v137, s15, v137\nv_xor_b32 v138, s15, v138\nv_xor_b32 v139, s15, v139\nv_xor_b32 v140, s15, v140\nv_xor_b32 v141, s15, v141\nv_xor_b32 v142, s15, v142\n"
"v_xor_b32 v143, s15, v143\nv_min_u32 v136, v136, v137\nv_min_u32 v138, v138, v139\nv_min_u32 v140, v140, v141\nv_min_u32 v142, v142, v143\nv_or3_b32 v136, v136, v138, v140\n"
"v_or_b32 v136, v136, v142\nv_cmp_ne_u32 vcc, 0, v136\ns_nop 0\ns_cbranch_vccnz .S191\n.S192:\ns_and_b32 s4, s13, 3\ns_lshl2_add_u32 s4, s12, s4\ns_lshr_b32 s5, s71, 16\n"
"s_cmp_lt_u32 s4, 28\ns_cselect_b32 s5, s71, s5\ns_cselect_b32 s3, 0, 28\ns_and_b32 s5, s5, 0x7ff\ns_sub_u32 s3, s4, s3\ns_mul_i32 s2, s4, 7168\ns_add_u32 s2, s2, 0xc04c800\n"
"s_add_u32 s6, s2, 4096\nbuffer_load_dwordx4 v[136:139], v1, s[16:19], s2 offen offset:0 sc0 sc1\nbuffer_load_dwordx4 v[140:143], v1, s[16:19], s2 offen offset:1024 sc0 sc1\n"
"buffer_load_dwordx4 v[144:147], v1, s[16:19], s2 offen offset:2048 sc0 sc1\nbuffer_load_dwordx4 v[148:151], v1, s[16:19], s2 offen offset:3072 sc0 sc1\n"
"buffer_load_dwordx4 v[152:155], v1, s[16:19], s6 offen offset:0 sc0 sc1\nbuffer_load_dwordx4 v[156:159], v1, s[16:19], s6 offen offset:1024 sc0 sc1\n"
"buffer_load_dwordx4 v[160:163], v1, s[16:19], s6 offen offset:2048 sc0 sc1\nv_lshrrev_b32 v167, 1, v1\nv_and_b32 v167, 24, v167\ns_mul_i32 s2, s5, 896\ns_lshl_b32 s3, s3, 5\n"
"s_add_u32 s2, s2, s3\ns_lshl2_add_u32 s5, s5, 0xc03e000\nbuffer_load_dwordx2 v[164:165], v167, s[28:31], s2 offen\nbuffer_load_dword v166, off, s[16:19], s5 sc0 sc1\n"
"s_mov_b32 s90, 0\ns_swappc_b64 s[8:9], s[80:81]\nAD 136,136,140\nAD 137,137,141\nAD 138,138,142\nAD 139,139,143\nAD 144,144,148\nAD 145,145,149\nAD 146,146,150\nAD 147,147,151\n"
"AD 152,152,156\nAD 153,153,157\nAD 154,154,158\nAD 155,155,159\nAD 136,136,144\nAD 137,137,145\nAD 138,138,146\nAD 139,139,147\nAD 152,152,160\nAD 153,153,161\nAD 154,154,162\n"
"AD 155,155,163\nAD 136,136,152\nAD 137,137,153\nAD 138,138,154\nAD 139,139,155\nv_mov_b32 v140, v136\nv_mov_b32 v141, v137\nv_mov_b32 v142, v138\nv_mov_b32 v143, v139\n"
"v_permlane32_swap_b32 v136, v140\nv_permlane32_swap_b32 v137, v141\nv_permlane32_swap_b32 v138, v142\nv_permlane32_swap_b32 v139, v143\nAD 136,136,140\nAD 137,137,141\n"
"AD 138,138,142\nAD 139,139,143\nv_mov_b32 v140, v136\nv_mov_b32 v141, v137\nv_mov_b32 v142, v138\nv_mov_b32 v143, v139\nv_permlane16_swap_b32 v136, v140\n"
"v_permlane16_swap_b32 v137, v141\nv_permlane16_swap_b32 v138, v142\nv_permlane16_swap_b32 v139, v143\nAD 136,136,140\nAD 137,137,141\nAD 138,138,142\nAD 139,139,143\n"
"v_add_f32_dpp v136, v136, v136 row_ror:8 row_mask:0xf bank_mask:0xf\nv_add_f32_dpp v137, v137, v137 row_ror:8 row_mask:0xf bank_mask:0xf\n"
"v_add_f32_dpp v138, v138, v138 row_ror:8 row_mask:0xf bank_mask:0xf\nv_add_f32_dpp v139, v139, v139 row_ror:8 row_mask:0xf bank_mask:0xf\n"
"v_add_f32_dpp v136, v136, v136 row_ror:4 row_mask:0xf bank_mask:0xf\nv_add_f32_dpp v137, v137, v137 row_ror:4 row_mask:0xf bank_mask:0xf\n"
"v_add_f32_dpp v138, v138, v138 row_ror:4 row_mask:0xf bank_mask:0xf\nv_add_f32_dpp v139, v139, v139 row_ror:4 row_mask:0xf bank_mask:0xf\nv_mul_f32 v166, 0xbfb8aa3b, v166\n"
"EX 166,166\nCV 136,136,137\nv_add_f32 v166, 1.0, v166\nv_rcp_f32 v166, v166\nCV 138,138,139\nCV 166,166,166\nv_lshlrev_b32 v166, 16, v166\nv_and_b32 v137, 0xffff0000, v136\n"
"v_lshlrev_b32 v136, 16, v136\nv_and_b32 v139, 0xffff0000, v138\nv_lshlrev_b32 v138, 16, v138\nMU 136,136,166\nMU 137,137,166\nMU 138,138,166\nMU 139,139,166\nCV 136,136,137\n"
"CV 138,138,139\nv_and_b32 v137, 0xffff0000, v136\nv_lshlrev_b32 v136, 16, v136\nv_and_b32 v139, 0xffff0000, v138\nv_lshlrev_b32 v138, 16, v138\nv_lshlrev_b32 v140, 16, v164\n"
"v_and_b32 v141, 0xffff0000, v164\nv_lshlrev_b32 v142, 16, v165\nv_and_b32 v143, 0xffff0000, v165\nAD 136,136,140\nAD 137,137,141\nAD 138,138,142\nAD 139,139,143\nCV 136,136,137\n"
"CV 137,138,139\nv_mov_b32 v138, s15\nv_mov_b32 v139, s71\ns_and_b32 s4, s13, 3\ns_lshl2_add_u32 s4, s12, s4\ns_lshr_b32 s5, s71, 16\ns_cmp_lt_u32 s4, 28\n"
"s_cselect_b32 s5, s71, s5\ns_cselect_b32 s3, 0, 28\ns_and_b32 s5, s5, 0x7ff\ns_sub_u32 s3, s4, s3\nv_mov_b32 v140, 0x40000000\nv_cmp_gt_u32 vcc, 64, v1\ns_lshl_b32 s4, s4, 6\n"
"s_add_u32 s4, s4, 0xc04b800\nv_cndmask_b32 v140, v140, v1, vcc\nSv 136,140,16,4,0\ns_mov_b32 s90, 100\ns_swappc_b64 s[8:9], s[80:81]\n.T198:\ns_mov_b32 s62, 0\n"
"s_call_b64 s[80:81], .RB11\ns_branch .T199\n.T200:\ns_mov_b32 s62, 0\ns_call_b64 s[80:81], .RA10\ns_branch .T201\n.T206:\ns_mov_b32 s62, 0\ns_call_b64 s[80:81], .RB11\n"
"s_branch .T207\n.T208:\ns_mov_b32 s62, 0\ns_call_b64 s[80:81], .RA10\ns_branch .T209\n.T213:\ns_mov_b32 s62, 0\ns_call_b64 s[80:81], .RB11\ns_branch .T214\n.T215:\n"
"s_mov_b32 s62, 0\ns_call_b64 s[80:81], .RA10\ns_branch .T216\n.S224:\ns_mov_b32 s7, 262144\n.S226:\nbuffer_load_dwordx4 v[100:103], v108, s[16:19], s65 offen offset:0 sc0 sc1\n"
"buffer_load_dwordx4 v[104:107], v108, s[16:19], s65 offen offset:256 sc0 sc1\nWV 0\nv_xor_b32 v109, s15, v102\nv_xor_b32 v110, s15, v106\nv_or_b32 v109, v109, v110\n"
"v_cmp_ne_u32 vcc, 0, v109\ns_nop 0\ns_cbranch_vccz .S225\ns_sleep 6\ns_sub_u32 s7, s7, 1\ns_cbranch_scc0 .S226\ns_branch .S225\n.D220:\nWV 0\nv_xor_b32 v109, s15, v102\n"
"v_xor_b32 v110, s15, v106\nv_or_b32 v109, v109, v110\nv_cmp_ne_u32 vcc, 0, v109\ns_nop 0\ns_cbranch_vccnz .S224\n.S225:\ns_add_u32 s5, s66, 0\n"
"buffer_load_dwordx4 a[192:195], v1, s[16:19], s5 offen offset:0 sc1\nbuffer_load_dwordx4 a[196:199], v1, s[16:19], s5 offen offset:1024 sc1\n"
"buffer_load_dwordx4 a[200:203], v1, s[16:19], s5 offen offset:2048 sc1\nbuffer_load_dwordx4 a[204:207], v1, s[16:19], s5 offen offset:3072 sc1\ns_add_u32 s5, s66, 4096\n"
"buffer_load_dwordx4 a[208:211], v1, s[16:19], s5 offen offset:0 sc1\nbuffer_load_dwordx4 a[212:215], v1, s[16:19], s5 offen offset:1024 sc1\n"
"buffer_load_dwordx4 a[216:219], v1, s[16:19], s5 offen offset:2048 sc1\nbuffer_load_dwordx4 a[220:223], v1, s[16:19], s5 offen offset:3072 sc1\ns_add_u32 s5, s66, 16384\n"
"buffer_load_dwordx4 a[224:227], v1, s[16:19], s5 offen offset:0 sc1\nbuffer_load_dwordx4 a[228:231], v1, s[16:19], s5 offen offset:1024 sc1\n"
"buffer_load_dwordx4 a[232:235], v1, s[16:19], s5 offen offset:2048 sc1\nbuffer_load_dwordx4 a[236:239], v1, s[16:19], s5 offen offset:3072 sc1\ns_add_u32 s5, s66, 20480\n"
"buffer_load_dwordx4 a[240:243], v1, s[16:19], s5 offen offset:0 sc1\nbuffer_load_dwordx4 a[244:247], v1, s[16:19], s5 offen offset:1024 sc1\n"
"buffer_load_dwordx4 a[248:251], v1, s[16:19], s5 offen offset:2048 sc1\nbuffer_load_dwordx4 a[252:255], v1, s[16:19], s5 offen offset:3072 sc1\nWV 0\ns_mov_b32 s67, 0\n"
"s_branch .D221\n.D222:\nv_mov_b32 v130, 0\nv_mov_b32 v131, 0\ns_mov_b32 s63, 0xff800000\ns_mov_b32 s67, 1\ns_branch .D223\n.T227:\ns_lshl3_add_u32 s62, s67, 2\n"
"s_call_b64 s[80:81], .RB11\ns_cmp_eq_u32 s67, 0\ns_cbranch_scc1 .T228\nSU 20,100,88\nv_min_f32 v20, 0, v20\nEX 20,20\ns_nop 0\nFM 86,101,20\nSU 21,104,89\nv_min_f32 v21, 0, v21\n"
"EX 21,21\ns_nop 0\nFM 87,105,21\nAR 22,192\nv_and_b32 v23, 0xffff0000, v22\nv_lshlrev_b32 v22, 16, v22\nMU 22,22,20\nMU 23,23,20\nCV 22,22,23\nAW 192,22\nAR 22,193\n"
"v_and_b32 v23, 0xffff0000, v22\nv_lshlrev_b32 v22, 16, v22\nMU 22,22,20\nMU 23,23,20\nCV 22,22,23\nAW 193,22\nAR 22,194\nv_and_b32 v23, 0xffff0000, v22\n"
"v_lshlrev_b32 v22, 16, v22\nMU 22,22,20\nMU 23,23,20\nCV 22,22,23\nAW 194,22\nAR 22,195\nv_and_b32 v23, 0xffff0000, v22\nv_lshlrev_b32 v22, 16, v22\nMU 22,22,20\nMU 23,23,20\n"
"CV 22,22,23\nAW 195,22\nAR 22,196\nv_and_b32 v23, 0xffff0000, v22\nv_lshlrev_b32 v22, 16, v22\nMU 22,22,21\nMU 23,23,21\nCV 22,22,23\nAW 196,22\nAR 22,197\n"
"v_and_b32 v23, 0xffff0000, v22\nv_lshlrev_b32 v22, 16, v22\nMU 22,22,21\nMU 23,23,21\nCV 22,22,23\nAW 197,22\nAR 22,198\nv_and_b32 v23, 0xffff0000, v22\n"
"v_lshlrev_b32 v22, 16, v22\nMU 22,22,21\nMU 23,23,21\nCV 22,22,23\nAW 198,22\nAR 22,199\nv_and_b32 v23, 0xffff0000, v22\nv_lshlrev_b32 v22, 16, v22\nMU 22,22,21\nMU 23,23,21\n"
"CV 22,22,23\nAW 199,22\nAR 22,200\nv_and_b32 v23, 0xffff0000, v22\nv_lshlrev_b32 v22, 16, v22\nMU 22,22,20\nMU 23,23,20\nCV 22,22,23\nAW 200,22\nAR 22,201\n"
"v_and_b32 v23, 0xffff0000, v22\nv_lshlrev_b32 v22, 16, v22\nMU 22,22,20\nMU 23,23,20\nCV 22,22,23\nAW 201,22\nAR 22,202\nv_and_b32 v23, 0xffff0000, v22\n"
"v_lshlrev_b32 v22, 16, v22\nMU 22,22,20\nMU 23,23,20\nCV 22,22,23\nAW 202,22\nAR 22,203\nv_and_b32 v23, 0xffff0000, v22\nv_lshlrev_b32 v22, 16, v22\nMU 22,22,20\nMU 23,23,20\n"
"CV 22,22,23\nAW 203,22\nAR 22,204\nv_and_b32 v23, 0xffff0000, v22\nv_lshlrev_b32 v22, 16, v22\nMU 22,22,21\nMU 23,23,21\nCV 22,22,23\nAW 204,22\nAR 22,205\n"
"v_and_b32 v23, 0xffff0000, v22\nv_lshlrev_b32 v22, 16, v22\nMU 22,22,21\nMU 23,23,21\nCV 22,22,23\nAW 205,22\nAR 22,206\nv_and_b32 v23, 0xffff0000, v22\n"
"v_lshlrev_b32 v22, 16, v22\nMU 22,22,21\nMU 23,23,21\nCV 22,22,23\nAW 206,22\nAR 22,207\nv_and_b32 v23, 0xffff0000, v22\nv_lshlrev_b32 v22, 16, v22\nMU 22,22,21\nMU 23,23,21\n"
"CV 22,22,23\nAW 207,22\nAR 22,208\nv_and_b32 v23, 0xffff0000, v22\nv_lshlrev_b32 v22, 16, v22\nMU 22,22,20\nMU 23,23,20\nCV 22,22,23\nAW 208,22\nAR 22,209\n"
"v_and_b32 v23, 0xffff0000, v22\nv_lshlrev_b32 v22, 16, v22\nMU 22,22,20\nMU 23,23,20\nCV 22,22,23\nAW 209,22\nAR 22,210\nv_and_b32 v23, 0xffff0000, v22\n"
"v_lshlrev_b32 v22, 16, v22\nMU 22,22,20\nMU 23,23,20\nCV 22,22,23\nAW 210,22\nAR 22,211\nv_and_b32 v23, 0xffff0000, v22\nv_lshlrev_b32 v22, 16, v22\nMU 22,22,20\nMU 23,23,20\n"
"CV 22,22,23\nAW 211,22\nAR 22,212\nv_and_b32 v23, 0xffff0000, v22\nv_lshlrev_b32 v22, 16, v22\nMU 22,22,21\nMU 23,23,21\nCV 22,22,23\nAW 212,22\nAR 22,213\n"
"v_and_b32 v23, 0xffff0000, v22\nv_lshlrev_b32 v22, 16, v22\nMU 22,22,21\nMU 23,23,21\nCV 22,22,23\nAW 213,22\nAR 22,214\nv_and_b32 v23, 0xffff0000, v22\n"
"v_lshlrev_b32 v22, 16, v22\nMU 22,22,21\nMU 23,23,21\nCV 22,22,23\nAW 214,22\nAR 22,215\nv_and_b32 v23, 0xffff0000, v22\nv_lshlrev_b32 v22, 16, v22\nMU 22,22,21\nMU 23,23,21\n"
"CV 22,22,23\nAW 215,22\nAR 22,216\nv_and_b32 v23, 0xffff0000, v22\nv_lshlrev_b32 v22, 16, v22\nMU 22,22,20\nMU 23,23,20\nCV 22,22,23\nAW 216,22\nAR 22,217\n"
"v_and_b32 v23, 0xffff0000, v22\nv_lshlrev_b32 v22, 16, v22\nMU 22,22,20\nMU 23,23,20\nCV 22,22,23\nAW 217,22\nAR 22,218\nv_and_b32 v23, 0xffff0000, v22\n"
"v_lshlrev_b32 v22, 16, v22\nMU 22,22,20\nMU 23,23,20\nCV 22,22,23\nAW 218,22\nAR 22,219\nv_and_b32 v23, 0xffff0000, v22\nv_lshlrev_b32 v22, 16, v22\nMU 22,22,20\nMU 23,23,20\n"
"CV 22,22,23\nAW 219,22\nAR 22,220\nv_and_b32 v23, 0xffff0000, v22\nv_lshlrev_b32 v22, 16, v22\nMU 22,22,21\nMU 23,23,21\nCV 22,22,23\nAW 220,22\nAR 22,221\n"
"v_and_b32 v23, 0xffff0000, v22\nv_lshlrev_b32 v22, 16, v22\nMU 22,22,21\nMU 23,23,21\nCV 22,22,23\nAW 221,22\nAR 22,222\nv_and_b32 v23, 0xffff0000, v22\n"
"v_lshlrev_b32 v22, 16, v22\nMU 22,22,21\nMU 23,23,21\nCV 22,22,23\nAW 222,22\nAR 22,223\nv_and_b32 v23, 0xffff0000, v22\nv_lshlrev_b32 v22, 16, v22\nMU 22,22,21\nMU 23,23,21\n"
"CV 22,22,23\nAW 223,22\nAR 22,224\nv_and_b32 v23, 0xffff0000, v22\nv_lshlrev_b32 v22, 16, v22\nMU 22,22,20\nMU 23,23,20\nCV 22,22,23\nAW 224,22\nAR 22,225\n"
"v_and_b32 v23, 0xffff0000, v22\nv_lshlrev_b32 v22, 16, v22\nMU 22,22,20\nMU 23,23,20\nCV 22,22,23\nAW 225,22\nAR 22,226\nv_and_b32 v23, 0xffff0000, v22\n"
"v_lshlrev_b32 v22, 16, v22\nMU 22,22,20\nMU 23,23,20\nCV 22,22,23\nAW 226,22\nAR 22,227\nv_and_b32 v23, 0xffff0000, v22\nv_lshlrev_b32 v22, 16, v22\nMU 22,22,20\nMU 23,23,20\n"
"CV 22,22,23\nAW 227,22\nAR 22,228\nv_and_b32 v23, 0xffff0000, v22\nv_lshlrev_b32 v22, 16, v22\nMU 22,22,21\nMU 23,23,21\nCV 22,22,23\nAW 228,22\nAR 22,229\n"
"v_and_b32 v23, 0xffff0000, v22\nv_lshlrev_b32 v22, 16, v22\nMU 22,22,21\nMU 23,23,21\nCV 22,22,23\nAW 229,22\nAR 22,230\nv_and_b32 v23, 0xffff0000, v22\n"
"v_lshlrev_b32 v22, 16, v22\nMU 22,22,21\nMU 23,23,21\nCV 22,22,23\nAW 230,22\nAR 22,231\nv_and_b32 v23, 0xffff0000, v22\nv_lshlrev_b32 v22, 16, v22\nMU 22,22,21\nMU 23,23,21\n"
"CV 22,22,23\nAW 231,22\nAR 22,232\nv_and_b32 v23, 0xffff0000, v22\nv_lshlrev_b32 v22, 16, v22\nMU 22,22,20\nMU 23,23,20\nCV 22,22,23\nAW 232,22\nAR 22,233\n"
"v_and_b32 v23, 0xffff0000, v22\nv_lshlrev_b32 v22, 16, v22\nMU 22,22,20\nMU 23,23,20\nCV 22,22,23\nAW 233,22\nAR 22,234\nv_and_b32 v23, 0xffff0000, v22\n"
"v_lshlrev_b32 v22, 16, v22\nMU 22,22,20\nMU 23,23,20\nCV 22,22,23\nAW 234,22\nAR 22,235\nv_and_b32 v23, 0xffff0000, v22\nv_lshlrev_b32 v22, 16, v22\nMU 22,22,20\nMU 23,23,20\n"
"CV 22,22,23\nAW 235,22\nAR 22,236\nv_and_b32 v23, 0xffff0000, v22\nv_lshlrev_b32 v22, 16, v22\nMU 22,22,21\nMU 23,23,21\nCV 22,22,23\nAW 236,22\nAR 22,237\n"
"v_and_b32 v23, 0xffff0000, v22\nv_lshlrev_b32 v22, 16, v22\nMU 22,22,21\nMU 23,23,21\nCV 22,22,23\nAW 237,22\nAR 22,238\nv_and_b32 v23, 0xffff0000, v22\n"
"v_lshlrev_b32 v22, 16, v22\nMU 22,22,21\nMU 23,23,21\nCV 22,22,23\nAW 238,22\nAR 22,239\nv_and_b32 v23, 0xffff0000, v22\nv_lshlrev_b32 v22, 16, v22\nMU 22,22,21\nMU 23,23,21\n"
"CV 22,22,23\nAW 239,22\nAR 22,240\nv_and_b32 v23, 0xffff0000, v22\nv_lshlrev_b32 v22, 16, v22\nMU 22,22,20\nMU 23,23,20\nCV 22,22,23\nAW 240,22\nAR 22,241\n"
"v_and_b32 v23, 0xffff0000, v22\nv_lshlrev_b32 v22, 16, v22\nMU 22,22,20\nMU 23,23,20\nCV 22,22,23\nAW 241,22\nAR 22,242\nv_and_b32 v23, 0xffff0000, v22\n"
"v_lshlrev_b32 v22, 16, v22\nMU 22,22,20\nMU 23,23,20\nCV 22,22,23\nAW 242,22\nAR 22,243\nv_and_b32 v23, 0xffff0000, v22\nv_lshlrev_b32 v22, 16, v22\nMU 22,22,20\nMU 23,23,20\n"
"CV 22,22,23\nAW 243,22\nAR 22,244\nv_and_b32 v23, 0xffff0000, v22\nv_lshlrev_b32 v22, 16, v22\nMU 22,22,21\nMU 23,23,21\nCV 22,22,23\nAW 244,22\nAR 22,245\n"
"v_and_b32 v23, 0xffff0000, v22\nv_lshlrev_b32 v22, 16, v22\nMU 22,22,21\nMU 23,23,21\nCV 22,22,23\nAW 245,22\nAR 22,246\nv_and_b32 v23, 0xffff0000, v22\n"
"v_lshlrev_b32 v22, 16, v22\nMU 22,22,21\nMU 23,23,21\nCV 22,22,23\nAW 246,22\nAR 22,247\nv_and_b32 v23, 0xffff0000, v22\nv_lshlrev_b32 v22, 16, v22\nMU 22,22,21\nMU 23,23,21\n"
"CV 22,22,23\nAW 247,22\nAR 22,248\nv_and_b32 v23, 0xffff0000, v22\nv_lshlrev_b32 v22, 16, v22\nMU 22,22,20\nMU 23,23,20\nCV 22,22,23\nAW 248,22\nAR 22,249\n"
"v_and_b32 v23, 0xffff0000, v22\nv_lshlrev_b32 v22, 16, v22\nMU 22,22,20\nMU 23,23,20\nCV 22,22,23\nAW 249,22\nAR 22,250\nv_and_b32 v23, 0xffff0000, v22\n"
"v_lshlrev_b32 v22, 16, v22\nMU 22,22,20\nMU 23,23,20\nCV 22,22,23\nAW 250,22\nAR 22,251\nv_and_b32 v23, 0xffff0000, v22\nv_lshlrev_b32 v22, 16, v22\nMU 22,22,20\nMU 23,23,20\n"
"CV 22,22,23\nAW 251,22\nAR 22,252\nv_and_b32 v23, 0xffff0000, v22\nv_lshlrev_b32 v22, 16, v22\nMU 22,22,21\nMU 23,23,21\nCV 22,22,23\nAW 252,22\nAR 22,253\n"
"v_and_b32 v23, 0xffff0000, v22\nv_lshlrev_b32 v22, 16, v22\nMU 22,22,21\nMU 23,23,21\nCV 22,22,23\nAW 253,22\nAR 22,254\nv_and_b32 v23, 0xffff0000, v22\n"
"v_lshlrev_b32 v22, 16, v22\nMU 22,22,21\nMU 23,23,21\nCV 22,22,23\nAW 254,22\nAR 22,255\nv_and_b32 v23, 0xffff0000, v22\nv_lshlrev_b32 v22, 16, v22\nMU 22,22,21\nMU 23,23,21\n"
"CV 22,22,23\nAW 255,22\nv_lshrrev_b32 v22, 8, v1\nv_bfe_u32 v23, v1, 6, 2\nv_cmp_eq_u32 vcc, v22, v23\nv_and_b32 v22, 16, v1\nv_mov_b32 v23, 0x3f80\n"
"v_cndmask_b32 v23, 0, v23, vcc\nv_lshlrev_b32 v23, v22, v23\nv_and_b32 v22, 32, v1\nv_cmp_eq_u32 vcc, 0, v22\nv_mov_b32 v128, 0\nv_mov_b32 v129, 0\n"
"v_cndmask_b32 v130, 0, v23, vcc\nv_sub_u32 v131, v23, v130\nv_mov_b32 v132, 0\nv_mov_b32 v133, 0\ns_nop 1\nMava 0,130,192\nMava 8,128,192\nMava 12,128,196\nMava 4,130,196\n"
"Mava 16,130,200\nMava 24,128,200\nMava 28,128,204\nMava 20,130,204\nMava 32,130,208\nMava 40,128,208\nMava 44,128,212\nMava 36,130,212\nMava 48,130,216\nMava 56,128,216\n"
"Mava 60,128,220\nMava 52,130,220\nMava 64,130,224\nMava 72,128,224\nMava 76,128,228\nMava 68,130,228\nMava 80,130,232\nMava 88,128,232\nMava 92,128,236\nMava 84,130,236\n"
"Mava 96,130,240\nMava 104,128,240\nMava 108,128,244\nMava 100,130,244\nMava 112,130,248\nMava 120,128,248\nMava 124,128,252\nMava 116,130,252\ns_nop 7\ns_branch .T228\n.S230:\n"
"s_mov_b32 s7, 262144\n.S232:\ns_add_u32 s0, s92, 0\nbuffer_load_dwordx4 v[208:211], v1, s[16:19], s0 offen offset:0 sc0 sc1\n"
"buffer_load_dwordx4 v[212:215], v1, s[16:19], s0 offen offset:1024 sc0 sc1\nWV 0\nv_max_u32 v21, v211, v215\nv_cmp_eq_u32 vcc, -1, v21\ns_nop 0\ns_cbranch_vccz .S231\ns_sleep 6\n"
"s_sub_u32 s7, s7, 1\ns_cbranch_scc0 .S232\ns_branch .S231\n.S233:\ns_mov_b32 s7, 262144\n.S235:\ns_add_u32 s0, s92, 131072\n"
"buffer_load_dwordx4 v[216:219], v1, s[16:19], s0 offen offset:0 sc0 sc1\nbuffer_load_dwordx4 v[220:223], v1, s[16:19], s0 offen offset:1024 sc0 sc1\nWV 0\n"
"v_max_u32 v21, v219, v223\nv_cmp_eq_u32 vcc, -1, v21\ns_nop 0\ns_cbranch_vccz .S234\ns_sleep 6\ns_sub_u32 s7, s7, 1\ns_cbranch_scc0 .S235\ns_branch .S234\n.S236:\n"
"s_mov_b32 s7, 262144\n.S238:\ns_add_u32 s0, s92, 262144\nbuffer_load_dwordx4 v[224:227], v1, s[16:19], s0 offen offset:0 sc0 sc1\n"
"buffer_load_dwordx4 v[228:231], v1, s[16:19], s0 offen offset:1024 sc0 sc1\nWV 0\nv_max_u32 v21, v227, v231\nv_cmp_eq_u32 vcc, -1, v21\ns_nop 0\ns_cbranch_vccz .S237\ns_sleep 6\n"
"s_sub_u32 s7, s7, 1\ns_cbranch_scc0 .S238\ns_branch .S237\n.S239:\ns_mov_b32 s7, 262144\n.S241:\ns_add_u32 s0, s92, 393216\n"
"buffer_load_dwordx4 v[208:211], v1, s[16:19], s0 offen offset:0 sc0 sc1\nbuffer_load_dwordx4 v[212:215], v1, s[16:19], s0 offen offset:1024 sc0 sc1\nWV 0\n"
"v_max_u32 v21, v211, v215\nv_cmp_eq_u32 vcc, -1, v21\ns_nop 0\ns_cbranch_vccz .S240\ns_sleep 6\ns_sub_u32 s7, s7, 1\ns_cbranch_scc0 .S241\ns_branch .S240\n.S242:\n"
"s_mov_b32 s7, 262144\n.S244:\ns_add_u32 s0, s92, 524288\nbuffer_load_dwordx4 v[216:219], v1, s[16:19], s0 offen offset:0 sc0 sc1\n"
"buffer_load_dwordx4 v[220:223], v1, s[16:19], s0 offen offset:1024 sc0 sc1\nWV 0\nv_max_u32 v21, v219, v223\nv_cmp_eq_u32 vcc, -1, v21\ns_nop 0\ns_cbranch_vccz .S243\ns_sleep 6\n"
"s_sub_u32 s7, s7, 1\ns_cbranch_scc0 .S244\ns_branch .S243\n.S245:\ns_mov_b32 s7, 262144\n.S247:\ns_add_u32 s0, s92, 655360\n"
"buffer_load_dwordx4 v[224:227], v1, s[16:19], s0 offen offset:0 sc0 sc1\nbuffer_load_dwordx4 v[228:231], v1, s[16:19], s0 offen offset:1024 sc0 sc1\nWV 0\n"
"v_max_u32 v21, v227, v231\nv_cmp_eq_u32 vcc, -1, v21\ns_nop 0\ns_cbranch_vccz .S246\ns_sleep 6\ns_sub_u32 s7, s7, 1\ns_cbranch_scc0 .S247\ns_branch .S246\n.S248:\n"
"s_mov_b32 s7, 262144\n.S250:\ns_add_u32 s0, s92, 786432\nbuffer_load_dwordx4 v[208:211], v1, s[16:19], s0 offen offset:0 sc0 sc1\n"
"buffer_load_dwordx4 v[212:215], v1, s[16:19], s0 offen offset:1024 sc0 sc1\nWV 0\nv_max_u32 v21, v211, v215\nv_cmp_eq_u32 vcc, -1, v21\ns_nop 0\ns_cbranch_vccz .S249\ns_sleep 6\n"
"s_sub_u32 s7, s7, 1\ns_cbranch_scc0 .S250\ns_branch .S249\n.S251:\ns_mov_b32 s7, 262144\n.S253:\nbuffer_load_dwordx4 v[64:67], v89, s[16:19], s40 offen offset:0 sc0 sc1\n"
"buffer_load_dwordx4 v[68:71], v89, s[16:19], s40 offen offset:16 sc0 sc1\nbuffer_load_dwordx4 v[72:75], v89, s[16:19], s40 offen offset:1792 sc0 sc1\n"
"buffer_load_dwordx4 v[76:79], v89, s[16:19], s40 offen offset:1808 sc0 sc1\nbuffer_load_dwordx2 v[80:81], v88, s[16:19], s63 offen offset:0 sc0 sc1\n"
"buffer_load_dwordx2 v[82:83], v88, s[16:19], s63 offen offset:512 sc0 sc1\nWV 0\nv_xor_b32 v66, s15, v66\nv_xor_b32 v70, s15, v70\nv_xor_b32 v74, s15, v74\n"
"v_xor_b32 v78, s15, v78\nv_xor_b32 v81, s15, v81\nv_xor_b32 v83, s15, v83\nv_or3_b32 v92, v66, v70, v74\nv_or3_b32 v92, v92, v78, v81\nv_or_b32 v92, v92, v83\n"
"v_cmp_ne_u32 vcc, 0, v92\ns_nop 0\ns_cbranch_vccz .S252\ns_sleep 6\ns_sub_u32 s7, s7, 1\ns_cbranch_scc0 .S253\ns_branch .S252\n.S254:\ns_mov_b32 s7, 262144\n.S256:\n"
"buffer_load_dword v27, off, s[16:19], s60 offset:512 sc0 sc1\nWV 0\nv_cmp_ne_u32 vcc, s15, v27\ns_nop 0\ns_cbranch_vccz .S255\ns_sleep 6\ns_sub_u32 s7, s7, 1\n"
"s_cbranch_scc0 .S256\ns_branch .S255\n.S257:\ns_mov_b32 s7, 262144\n.S259:\nbuffer_load_dword v27, v24, s[16:19], s41 offen sc0 sc1\nWV 0\nv_xor_b32 v27, s15, v27\n"
"v_and_b32 v26, 0xffffdfff, v27\nv_cmp_ne_u32 s[64:65], 0, v27\nv_cmp_ne_u32 vcc, 0, v26\ns_nop 0\ns_cbranch_vccz .S258\ns_sleep 6\ns_sub_u32 s7, s7, 1\ns_cbranch_scc0 .S259\n"
"s_branch .S258\n.Z261:\ns_sub_u32 s7, s7, 1\ns_cbranch_scc1 .Z262\ns_cmp_lg_u64 s[44:45], 0\ns_cbranch_scc0 .Z264\n"
"buffer_load_dwordx4 v[96:99], v1, s[16:19], s24 offen offset:0 sc0 sc1\nbuffer_load_dwordx4 v[100:103], v1, s[16:19], s24 offen offset:1024 sc0 sc1\n.Z264:\n"
"s_cmp_lg_u64 s[46:47], 0\ns_cbranch_scc0 .Z265\nbuffer_load_dwordx4 v[104:107], v1, s[16:19], s25 offen offset:0 sc0 sc1\n"
"buffer_load_dwordx4 v[108:111], v1, s[16:19], s25 offen offset:1024 sc0 sc1\n.Z265:\ns_cmp_lg_u64 s[48:49], 0\ns_cbranch_scc0 .Z266\n"
"buffer_load_dwordx4 v[112:115], v1, s[16:19], s26 offen offset:0 sc0 sc1\nbuffer_load_dwordx4 v[116:119], v1, s[16:19], s26 offen offset:1024 sc0 sc1\n.Z266:\n"
"s_cmp_lg_u64 s[50:51], 0\ns_cbranch_scc0 .Z267\nbuffer_load_dwordx4 v[120:123], v1, s[16:19], s27 offen offset:0 sc0 sc1\n"
"buffer_load_dwordx4 v[124:127], v1, s[16:19], s27 offen offset:1024 sc0 sc1\n.Z267:\ns_cmp_lg_u64 s[52:53], 0\ns_cbranch_scc0 .Z268\n"
"buffer_load_dwordx4 v[32:35], v1, s[16:19], s32 offen offset:0 sc0 sc1\nbuffer_load_dwordx4 v[36:39], v1, s[16:19], s32 offen offset:1024 sc0 sc1\n.Z268:\n"
"s_cmp_lg_u64 s[54:55], 0\ns_cbranch_scc0 .Z269\nbuffer_load_dwordx4 v[40:43], v1, s[16:19], s33 offen offset:0 sc0 sc1\n"
"buffer_load_dwordx4 v[44:47], v1, s[16:19], s33 offen offset:1024 sc0 sc1\n.Z269:\ns_cmp_lg_u64 s[56:57], 0\ns_cbranch_scc0 .Z270\n"
"buffer_load_dwordx4 v[48:51], v1, s[16:19], s34 offen offset:0 sc0 sc1\nbuffer_load_dwordx4 v[52:55], v1, s[16:19], s34 offen offset:1024 sc0 sc1\n.Z270:\n"
"s_cmp_lg_u64 s[58:59], 0\ns_cbranch_scc0 .Z271\nbuffer_load_dwordx4 v[56:59], v1, s[16:19], s35 offen offset:0 sc0 sc1\n"
"buffer_load_dwordx4 v[60:63], v1, s[16:19], s35 offen offset:1024 sc0 sc1\n.Z271:\ns_sleep 8\nWV 0\ns_branch .Z263\n.p2align 12\n.PA4:\ns_add_u32 m0, s47, -49152\n"
"s_add_u32 s48, s48, 128\nD0 2,20,48,0\nD0 3,20,48,1024\nD0 4,20,48,2048\nD0 5,20,48,3072\ns_and_b32 s86, s74, 15\ns_lshl2_add_u32 s86, s86, s11\ns_mov_b32 s88, 0\n"
"s_lshl_b32 s93, s73, 8\ns_lshl2_add_u32 s93, s86, s93\ns_add_u32 s93, s93, 0xc04ac00\ns_lshl_b32 s86, s86, 14\ns_mul_i32 s91, s73, 25165824\ns_add_u32 s86, s86, s91\n"
"s_and_b32 s92, s74, 1\ns_lshl_b32 s92, s92, 13\ns_add_u32 s86, s86, s92\ns_add_u32 s94, s86, 0x1480000\ns_xor_b32 s95, s94, 0x2000\ns_add_u32 s86, s94, 512\n"
"s_load_dword s87, s[54:55], s86\ns_add_u32 m0, s47, -32768\ns_add_u32 s45, s45, 128\nD0 2,20,45,0\nD0 3,20,45,1024\nD0 4,20,45,2048\nD0 5,20,45,3072\ns_add_u32 m0, s47, 32768\n"
"s_add_u32 s44, s44, 0x40000\nD0 6,24,44,0\nD0 7,24,44,1024\nD0 8,24,44,2048\nD0 9,24,44,3072\ns_add_u32 m0, s47, 49152\ns_add_u32 s44, s44, 0x40000\nD0 6,24,44,0\nD0 7,24,44,1024\n"
"D0 8,24,44,2048\nD0 9,24,44,3072\ns_add_u32 m0, s47, -16384\ns_add_u32 s48, s48, 128\nD0 2,20,48,0\nD0 3,20,48,1024\nD0 4,20,48,2048\nD0 5,20,48,3072\nv_mov_b32 v52, 0x40000000\n"
"v_cmp_gt_u32 vcc, 56, v0\ns_lshl2_add_u32 s2, s10, s11\ns_mul_i32 s2, s2, 1792\nv_cndmask_b32 v53, v52, v1, vcc\ns_mov_b32 s36, s56\ns_and_b32 s37, s57, 0xffff\n"
"s_mov_b32 s38, 0x380\ns_mov_b32 s39, 0x20000\ns_mov_b32 s40, s58\ns_and_b32 s41, s59, 0xffff\ns_mov_b32 s42, 0x380\ns_mov_b32 s43, 0x20000\n"
"buffer_load_dwordx4 v[32:35], v53, s[28:31], s2 offen offset:0\nbuffer_load_dwordx4 v[36:39], v53, s[28:31], s2 offen offset:896\n"
"buffer_load_dwordx4 v[40:43], v53, s[36:39], 0 offen offset:0\nbuffer_load_dwordx4 v[44:47], v53, s[40:43], 0 offen offset:0\nWL 0\ns_mul_i32 s15, s15, 1664525\n"
"s_add_u32 s15, s15, 1013904223\nv_mov_b32 v25, s15\ns_mov_b32 s57, 0x3db8aa3b\ns_and_b32 s59, s11, 1\ns_lshl_b32 s59, s59, 13\ns_mul_i32 s2, s73, 25165824\ns_and_b32 s3, s12, 1\n"
"s_lshl_b32 s3, s3, 21\ns_add_u32 s2, s2, s3\ns_lshl2_add_u32 s3, s13, s11\ns_lshl_b32 s3, s3, 15\ns_add_u32 s2, s2, s3\ns_add_u32 s56, s2, 0x800000\ns_xor_b32 s53, s56, 0xc000\n"
"buffer_load_dword v31, v24, s[24:27], s96 offen\ns_add_u32 s96, s96, 0x80000\nbuffer_load_dword v31, v24, s[24:27], s96 offen\ns_add_u32 s96, s96, 0x80000\n"
"v_lshrrev_b32 v60, 3, v0\nv_and_b32 v61, 0x70, v1\nv_lshl_or_b32 v60, v60, 10, v61\nSv 2,60,16,94,0\nSv 2,60,16,95,0\nWV 28\ns_barrier\nRa 128,10,0\nRa 132,11,0\nRa 136,10,2048\n"
"Ra 140,11,2048\nRa 144,10,8192\nRa 148,11,8192\nRa 152,10,10240\nRa 156,11,10240\nTa 160,12,0\nTa 162,12,2048\nTa 164,12,16384\nTa 166,12,18432\nTa 168,13,0\nTa 170,13,2048\n"
"Ta 172,13,16384\nTa 174,13,18432\nTa 176,14,0\nTa 178,14,2048\nTa 180,14,16384\nTa 182,14,18432\nTa 184,15,0\nTa 186,15,2048\nTa 188,15,16384\nTa 190,15,18432\nWL 0\ns_barrier\n"
"Zvaa 64,160,128\nTa 224,16,0\nMvaa 64,164,132\nTa 226,16,2048\nZvaa 96,164,140\ns_add_u32 m0, s47, -65536\ns_add_u32 s45, s45, 128\nMvaa 96,160,136\nTa 228,16,16384\n"
"Zvaa 128,160,144\nTa 230,16,18432\nMvaa 128,164,148\nD0 2,20,45,0\nZvaa 160,164,156\nTa 232,17,0\nMvaa 160,160,152\nTa 234,17,2048\nZvaa 164,168,152\ns_mov_b32 s36, s60\n"
"Mvaa 164,172,156\nTa 236,17,16384\nZvaa 132,172,148\nTa 238,17,18432\nMvaa 132,168,144\ns_and_b32 s37, s61, 0xffff\nZvaa 100,168,136\nTa 240,18,0\nMvaa 100,172,140\n"
"Ta 242,18,2048\nD0 3,20,45,1024\nZvaa 68,172,132\ns_mov_b32 s38, 0x380000\nMvaa 68,168,128\nTa 244,18,16384\nZvaa 72,176,128\nTa 246,18,18432\nMvaa 72,180,132\n"
"s_mov_b32 s39, 0x20000\nZvaa 104,180,140\nTa 248,19,0\nMvaa 104,176,136\nTa 250,19,2048\nZvaa 136,176,144\ns_lshl3_add_u32 s75, s12, s13\nMvaa 136,180,148\nTa 252,19,16384\n"
"D0 4,20,45,2048\nZvaa 168,180,156\nTa 254,19,18432\nMvaa 168,176,152\ns_lshl2_add_u32 s75, s75, s11\nZvaa 172,184,152\ns_mul_i32 s75, s75, 7168\nMvaa 172,188,156\n"
"s_mul_i32 s76, s11, 7168\nZvaa 140,188,148\ns_add_u32 s76, s76, 131072\nMvaa 140,184,144\ns_mov_b32 s42, 1605632\nZvaa 108,184,136\nMvaa 108,188,140\nD0 5,20,45,3072\n"
"Zvaa 76,188,132\nMvaa 76,184,128\ns_waitcnt vmcnt(24) lgkmcnt(0)\ns_barrier\nZvaa 80,224,128\nRa 192,10,16384\nMvaa 80,228,132\nZvaa 112,228,140\nRa 196,11,16384\n"
"s_add_u32 m0, s47, 0\ns_add_u32 s44, s44, 0x40000\nMvaa 112,224,136\nZvaa 144,224,144\nRa 200,10,18432\nMvaa 144,228,148\nD0 6,24,44,0\nZvaa 176,228,156\nRa 204,11,18432\n"
"Mvaa 176,224,152\nZvaa 180,232,152\nRa 208,10,24576\nMvaa 180,236,156\ns_and_b32 s78, s74, 3\ns_lshl2_add_u32 s78, s73, s78\ns_lshl_b32 s78, s78, 12\ns_bitcmp1_b32 s11, 0\n"
"s_cselect_b32 s79, s48, s45\ns_add_u32 s79, s79, 512\ns_and_b32 s79, s79, 0xfffe0000\ns_add_u32 s78, s78, s79\nbuffer_load_dword v31, v26, s[20:23], s78 offen\nZvaa 148,236,148\n"
"Ra 212,11,24576\nMvaa 148,232,144\nZvaa 116,232,136\nRa 216,10,26624\nMvaa 116,236,140\nD0 7,24,44,1024\nZvaa 84,236,132\nRa 220,11,26624\nMvaa 84,232,128\nZvaa 88,240,128\n"
"Mvaa 88,244,132\nZvaa 120,244,140\nMvaa 120,240,136\nZvaa 152,240,144\nMvaa 152,244,148\nD0 8,24,44,2048\nZvaa 184,244,156\nMvaa 184,240,152\nZvaa 188,248,152\nMvaa 188,252,156\n"
"Zvaa 156,252,148\nMvaa 156,248,144\nZvaa 124,248,136\nMvaa 124,252,140\nD0 9,24,44,3072\nZvaa 92,252,132\nMvaa 92,248,128\nWL 0\nZaaa 16,224,192\nRa 128,10,32768\nMaaa 16,228,196\n"
"Zaaa 48,228,204\nRa 132,11,32768\ns_add_u32 m0, s47, 16384\ns_add_u32 s44, s44, 0x40000\nMaaa 48,224,200\nZaaa 80,224,208\nRa 136,10,34816\nMaaa 80,228,212\nD0 6,24,44,0\n"
"Zaaa 112,228,220\nRa 140,11,34816\nMaaa 112,224,216\nZaaa 116,232,216\nRa 144,10,40960\nMaaa 116,236,220\nZaaa 84,236,212\nRa 148,11,40960\nMaaa 84,232,208\nZaaa 52,232,200\n"
"Ra 152,10,43008\nMaaa 52,236,204\nD0 7,24,44,1024\nZaaa 20,236,196\nRa 156,11,43008\nMaaa 20,232,192\nZaaa 24,240,192\nMaaa 24,244,196\nZaaa 56,244,204\nMaaa 56,240,200\n"
"Zaaa 88,240,208\nMaaa 88,244,212\nD0 8,24,44,2048\nZaaa 120,244,220\nMaaa 120,240,216\nZaaa 124,248,216\nMaaa 124,252,220\nZaaa 92,252,212\nMaaa 92,248,208\nZaaa 60,248,200\n"
"Maaa 60,252,204\nD0 9,24,44,3072\nZaaa 28,252,196\nMaaa 28,248,192\ns_waitcnt vmcnt(25) lgkmcnt(0)\ns_barrier\nZaaa 0,160,192\nTa 224,16,32768\nWV 17\nMaaa 0,164,196\n"
"Ta 226,16,34816\nZaaa 32,164,204\ns_add_u32 m0, s47, -49152\ns_add_u32 s48, s48, 128\nMaaa 32,160,200\nTa 228,16,49152\nv_lshlrev_b32 v48, 16, v32\nZaaa 64,160,208\n"
"Ta 230,16,51200\nbuffer_load_dword v31, v24, s[24:27], s96 offen\ns_add_u32 s96, s96, 0x80000\nMaaa 64,164,212\nD0 2,20,48,0\nv_lshlrev_b32 v49, 16, v36\nZaaa 96,164,220\n"
"Ta 232,17,32768\nMaaa 96,160,216\nTa 234,17,34816\nv_lshlrev_b32 v50, 16, v40\nZaaa 100,168,216\nv_lshlrev_b32 v51, 16, v44\nMaaa 100,172,220\nTa 236,17,49152\nZaaa 68,172,212\n"
"Ta 238,17,51200\nMU 52,48,50\nMaaa 68,168,208\nMU 53,49,50\nZaaa 36,168,200\nTa 240,18,32768\nCV 52,52,53\nMaaa 36,172,204\nTa 242,18,34816\nD0 3,20,48,1024\nZaaa 4,172,196\n"
"v_lshlrev_b32 v54, 16, v52\nv_and_b32 v55, 0xffff0000, v52\nMaaa 4,168,192\nTa 244,18,49152\nZaaa 8,176,192\nTa 246,18,51200\nMU 56,48,51\nMaaa 8,180,196\nMU 57,49,51\n"
"Zaaa 40,180,204\nTa 248,19,32768\nMaaa 40,176,200\nTa 250,19,34816\nv_and_b32 v48, 0xffff0000, v32\nZaaa 72,176,208\nv_and_b32 v49, 0xffff0000, v36\nMaaa 72,180,212\n"
"Ta 252,19,49152\nD0 4,20,48,2048\nZaaa 104,180,220\nTa 254,19,51200\nMaaa 104,176,216\nv_and_b32 v50, 0xffff0000, v40\nv_and_b32 v51, 0xffff0000, v44\nZaaa 108,184,216\n"
"MU 52,48,50\nMaaa 108,188,220\nMU 53,49,50\nZaaa 76,188,212\nCV 52,52,53\nv_and_b32 v53, 0xffff0000, v52\nMaaa 76,184,208\nv_lshlrev_b32 v52, 16, v52\nZaaa 44,184,200\n"
"FM 56,48,51\nFM 57,49,51\nMaaa 44,188,204\nD0 5,20,48,3072\nZaaa 12,188,196\nAD 54,54,52\nAD 55,55,53\nMaaa 12,184,192\nWL 0\nMvaa 80,224,128\nTa 160,12,32768\n"
"v_lshlrev_b32 v48, 16, v33\nMvaa 80,228,132\nTa 162,12,34816\nMvaa 112,228,140\ns_add_u32 m0, s47, -32768\ns_add_u32 s45, s45, 128\nMvaa 112,224,136\nTa 164,12,49152\n"
"v_lshlrev_b32 v49, 16, v37\nMvaa 144,224,144\nTa 166,12,51200\nMvaa 144,228,148\nD0 2,20,45,0\nv_lshlrev_b32 v50, 16, v41\nMvaa 176,228,156\nTa 168,13,32768\nMvaa 176,224,152\n"
"Ta 170,13,34816\nv_lshlrev_b32 v51, 16, v45\nMvaa 180,232,152\nMU 52,48,50\nMvaa 180,236,156\nTa 172,13,49152\nMvaa 148,236,148\nTa 174,13,51200\nMU 53,49,50\nMvaa 148,232,144\n"
"CV 52,52,53\nMvaa 116,232,136\nTa 176,14,32768\nv_and_b32 v53, 0xffff0000, v52\nMvaa 116,236,140\nTa 178,14,34816\nD0 3,20,45,1024\nMvaa 84,236,132\nv_lshlrev_b32 v52, 16, v52\n"
"FM 56,48,51\nMvaa 84,232,128\nTa 180,14,49152\nMvaa 88,240,128\nTa 182,14,51200\nFM 57,49,51\nMvaa 88,244,132\nAD 54,54,52\nMvaa 120,244,140\nTa 184,15,32768\nMvaa 120,240,136\n"
"Ta 186,15,34816\nAD 55,55,53\nMvaa 152,240,144\nv_and_b32 v48, 0xffff0000, v33\nMvaa 152,244,148\nTa 188,15,49152\nD0 4,20,45,2048\nMvaa 184,244,156\nTa 190,15,51200\n"
"Mvaa 184,240,152\nv_and_b32 v49, 0xffff0000, v37\nv_and_b32 v50, 0xffff0000, v41\nMvaa 188,248,152\nv_and_b32 v51, 0xffff0000, v45\nMvaa 188,252,156\nMU 52,48,50\n"
"Mvaa 156,252,148\nMU 53,49,50\nCV 52,52,53\nMvaa 156,248,144\nv_and_b32 v53, 0xffff0000, v52\nMvaa 124,248,136\nv_lshlrev_b32 v52, 16, v52\nFM 56,48,51\nMvaa 124,252,140\n"
"D0 5,20,45,3072\nMvaa 92,252,132\nFM 57,49,51\nAD 54,54,52\nMvaa 92,248,128\ns_waitcnt vmcnt(18) lgkmcnt(0)\ns_barrier\nMvaa 64,160,128\nRa 192,10,49152\nAD 55,55,53\n"
"Mvaa 64,164,132\nv_lshlrev_b32 v48, 16, v34\nMvaa 96,164,140\nRa 196,11,49152\ns_add_u32 m0, s47, 32768\ns_add_u32 s44, s44, 0x40000\nMvaa 96,160,136\nv_lshlrev_b32 v49, 16, v38\n"
"v_lshlrev_b32 v50, 16, v42\nMvaa 128,160,144\nRa 200,10,51200\nMvaa 128,164,148\nD0 6,24,44,0\nv_lshlrev_b32 v51, 16, v46\nMvaa 160,164,156\nRa 204,11,51200\nMvaa 160,160,152\n"
"MU 52,48,50\nMU 53,49,50\nMvaa 164,168,152\nRa 208,10,57344\nMvaa 164,172,156\nCV 52,52,53\nMvaa 132,172,148\nRa 212,11,57344\nv_and_b32 v53, 0xffff0000, v52\nMvaa 132,168,144\n"
"v_lshlrev_b32 v52, 16, v52\nMvaa 100,168,136\nRa 216,10,59392\nFM 56,48,51\nMvaa 100,172,140\nD0 7,24,44,1024\nMvaa 68,172,132\nRa 220,11,59392\nFM 57,49,51\nMvaa 68,168,128\n"
"AD 54,54,52\nMvaa 72,176,128\nAD 55,55,53\nv_and_b32 v48, 0xffff0000, v34\nMvaa 72,180,132\nv_and_b32 v49, 0xffff0000, v38\nMvaa 104,180,140\nv_and_b32 v50, 0xffff0000, v42\n"
"Mvaa 104,176,136\nv_and_b32 v51, 0xffff0000, v46\nMU 52,48,50\nMvaa 136,176,144\nMU 53,49,50\nMvaa 136,180,148\nD0 8,24,44,2048\nCV 52,52,53\nMvaa 168,180,156\n"
"v_and_b32 v53, 0xffff0000, v52\nMvaa 168,176,152\nv_lshlrev_b32 v52, 16, v52\nFM 56,48,51\nMvaa 172,184,152\nFM 57,49,51\nMvaa 172,188,156\nAD 54,54,52\nMvaa 140,188,148\n"
"AD 55,55,53\nv_lshlrev_b32 v48, 16, v35\nMvaa 140,184,144\nv_lshlrev_b32 v49, 16, v39\nMvaa 108,184,136\nv_lshlrev_b32 v50, 16, v43\nv_lshlrev_b32 v51, 16, v47\nMvaa 108,188,140\n"
"D0 9,24,44,3072\nMvaa 76,188,132\nMU 52,48,50\nMU 53,49,50\nMvaa 76,184,128\nWL 0\nMaaa 0,160,192\nRa 128,10,0\nCV 52,52,53\nMaaa 0,164,196\nv_and_b32 v53, 0xffff0000, v52\n"
"Maaa 32,164,204\nRa 132,11,0\ns_add_u32 m0, s47, 49152\ns_add_u32 s44, s44, 0x40000\nMaaa 32,160,200\nv_lshlrev_b32 v52, 16, v52\nFM 56,48,51\nMaaa 64,160,208\nRa 136,10,2048\n"
"Maaa 64,164,212\nD0 6,24,44,0\nFM 57,49,51\nMaaa 96,164,220\nRa 140,11,2048\nMaaa 96,160,216\nAD 54,54,52\nAD 55,55,53\nMaaa 100,168,216\nRa 144,10,8192\nMaaa 100,172,220\n"
"v_and_b32 v48, 0xffff0000, v35\nMaaa 68,172,212\nRa 148,11,8192\nv_and_b32 v49, 0xffff0000, v39\nMaaa 68,168,208\nv_and_b32 v50, 0xffff0000, v43\nMaaa 36,168,200\nRa 152,10,10240\n"
"v_and_b32 v51, 0xffff0000, v47\nMaaa 36,172,204\nD0 7,24,44,1024\nMaaa 4,172,196\nRa 156,11,10240\nMU 52,48,50\nMaaa 4,168,192\nMU 53,49,50\nMaaa 8,176,192\nCV 52,52,53\n"
"v_and_b32 v53, 0xffff0000, v52\nMaaa 8,180,196\nv_lshlrev_b32 v52, 16, v52\nMaaa 40,180,204\nFM 56,48,51\nMaaa 40,176,200\nFM 57,49,51\nAD 54,54,52\nMaaa 72,176,208\nAD 55,55,53\n"
"Maaa 72,180,212\nD0 8,24,44,2048\nv_permlane32_swap_b32 v54, v55\nMaaa 104,180,220\nv_permlane32_swap_b32 v56, v57\nMaaa 104,176,216\nAD 54,54,55\nAD 56,56,57\nMaaa 108,184,216\n"
"Maaa 108,188,220\nv_permlane16_swap_b32 v54, v56\nMaaa 76,188,212\nAD 54,54,56\nMaaa 76,184,208\nMaaa 44,184,200\nv_add_f32_dpp v54, v54, v54 row_ror:8 row_mask:0xf bank_mask:0xf\n"
"Maaa 44,188,204\nD0 9,24,44,3072\nMaaa 12,188,196\nv_add_f32_dpp v54, v54, v54 row_ror:4 row_mask:0xf bank_mask:0xf\nMaaa 12,184,192\ns_waitcnt vmcnt(12) lgkmcnt(0)\ns_barrier\n"
"Maaa 16,224,192\nTa 160,12,0\nv_add_f32_dpp v54, v54, v54 row_ror:2 row_mask:0xf bank_mask:0xf\nMaaa 16,228,196\nTa 162,12,2048\nMaaa 48,228,204\ns_add_u32 m0, s47, -16384\n"
"s_add_u32 s48, s48, 128\nMaaa 48,224,200\nTa 164,12,16384\nv_add_f32_dpp v54, v54, v54 row_ror:1 row_mask:0xf bank_mask:0xf\nMaaa 80,224,208\nTa 166,12,18432\n"
"buffer_load_dword v31, v24, s[24:27], s96 offen\ns_add_u32 s96, s96, 0x80000\nMaaa 80,228,212\nD0 2,20,48,0\nCV 54,54,54\nMaaa 112,228,220\nTa 168,13,0\nMaaa 112,224,216\n"
"Ta 170,13,2048\nv_lshlrev_b32 v54, 16, v54\nMaaa 116,232,216\nv_mul_f32 v55, 0xbfb8aa3b, v54\nEX 55,55\nMaaa 116,236,220\nTa 172,13,16384\nMaaa 84,236,212\nTa 174,13,18432\n"
"v_add_f32 v56, 0, v54\nMaaa 84,232,208\nv_add_f32 v55, 1.0, v55\nv_rcp_f32 v55, v55\nMaaa 52,232,200\nTa 176,14,0\nv_ashrrev_i32 v57, 31, v56\nMaaa 52,236,204\nTa 178,14,2048\n"
"D0 3,20,48,1024\nMaaa 20,236,196\nCV 55,55,55\nv_lshlrev_b32 v55, 16, v55\nMaaa 20,232,192\nTa 180,14,16384\nMaaa 24,240,192\nTa 182,14,18432\nv_or_b32 v57, 0x80000000, v57\n"
"Maaa 24,244,196\nv_xor_b32 v56, v56, v57\nMaaa 56,244,204\nTa 184,15,0\nMaaa 56,240,200\nTa 186,15,2048\nv_and_b32 v56, 0xffff0000, v56\nMaaa 88,240,208\n"
"s_lshl2_add_u32 s2, s10, s11\nMaaa 88,244,212\nTa 188,15,16384\nD0 4,20,48,2048\nMaaa 120,244,220\nTa 190,15,18432\nMaaa 120,240,216\nv_lshrrev_b32 v48, 5, v0\n"
"v_lshl_add_u32 v48, s2, 1, v48\nMaaa 124,248,216\nv_sub_u32 v49, 0xffff, v48\nMaaa 124,252,220\nv_or_b32 v56, v56, v49\nMaaa 92,252,212\nv_mov_b32 v57, s15\nv_and_b32 v49, 31, v0\n"
"Maaa 92,248,208\nv_mov_b32 v52, 0x40000000\nv_cmp_eq_u32 vcc, 16, v49\nv_lshlrev_b32 v50, 2, v48\nv_lshlrev_b32 v51, 10, v49\nv_cndmask_b32 v50, v52, v50, vcc\n"
"v_cndmask_b32 v56, v55, v56, vcc\nMaaa 60,248,200\nv_lshl_add_u32 v51, v48, 3, v51\nv_and_b32 v49, 15, v0\nMaaa 60,252,204\nD0 5,20,48,3072\nv_cmp_eq_u32 vcc, 0, v49\n"
"s_mov_b32 s2, 0xc040000\ns_mov_b32 s3, 0xc03e000\nv_cndmask_b32 v51, v52, v51, vcc\nbuffer_store_dwordx2 v[56:57], v51, s[16:19], s2 offen sc0 sc1\n"
"buffer_store_dword v54, v50, s[16:19], s3 offen sc0 sc1\nMaaa 28,252,196\nMaaa 28,248,192\nWL 0\ns_mov_b32 s50, 3\n.GL279:\nMvaa 64,160,128\nTa 224,16,0\nMvaa 64,164,132\n"
"Ta 226,16,2048\nMvaa 96,164,140\nTa 228,16,16384\ns_add_u32 m0, s47, -65536\ns_add_u32 s45, s45, 128\nMvaa 96,160,136\nTa 230,16,18432\nMvaa 128,160,144\nTa 232,17,0\n"
"Mvaa 128,164,148\nTa 234,17,2048\nD0 2,20,45,0\nMvaa 160,164,156\nTa 236,17,16384\nMvaa 160,160,152\nTa 238,17,18432\nMvaa 164,168,152\nTa 240,18,0\nMvaa 164,172,156\n"
"Ta 242,18,2048\nMvaa 132,172,148\nTa 244,18,16384\nMvaa 132,168,144\nTa 246,18,18432\nMvaa 100,168,136\nTa 248,19,0\nMvaa 100,172,140\nTa 250,19,2048\nD0 3,20,45,1024\n"
"Mvaa 68,172,132\nTa 252,19,16384\nMvaa 68,168,128\nTa 254,19,18432\nMvaa 72,176,128\nRa 192,10,16384\nMvaa 72,180,132\nRa 196,11,16384\nMvaa 104,180,140\nRa 200,10,18432\n"
"Mvaa 104,176,136\nRa 204,11,18432\nMvaa 136,176,144\nRa 208,10,24576\nMvaa 136,180,148\nRa 212,11,24576\nD0 4,20,45,2048\nMvaa 168,180,156\nRa 216,10,26624\nMvaa 168,176,152\n"
"Ra 220,11,26624\nMvaa 172,184,152\nMvaa 172,188,156\nMvaa 140,188,148\nMvaa 140,184,144\nMvaa 108,184,136\nMvaa 108,188,140\nD0 5,20,45,3072\nMvaa 76,188,132\nMvaa 76,184,128\n"
"s_waitcnt vmcnt(18) lgkmcnt(8)\ns_barrier\nMvaa 80,224,128\nMvaa 80,228,132\ns_mov_b32 s86, 0x1d2c3b4a\ns_load_dword s86, s[54:55], s94\nMvaa 112,228,140\ns_add_u32 m0, s47, 0\n"
"s_add_u32 s44, s44, 0x40000\ns_add_u32 s94, s94, 1024\nMvaa 112,224,136\nMvaa 144,224,144\nMvaa 144,228,148\nD0 6,24,44,0\nMvaa 176,228,156\nMvaa 176,224,152\nMvaa 180,232,152\n"
"Mvaa 180,236,156\nMvaa 148,236,148\nMvaa 148,232,144\ns_cmp_eq_u32 s86, 0x1d2c3b4a\ns_addc_u32 s88, s88, 0\nMvaa 116,232,136\nMvaa 116,236,140\nD0 7,24,44,1024\n"
"s_cmp_eq_u32 s86, 0x1d2c3b4a\ns_addc_u32 s88, s88, 0\nMvaa 84,236,132\nMvaa 84,232,128\ns_cmp_eq_u32 s86, 0x1d2c3b4a\ns_addc_u32 s88, s88, 0\nMvaa 88,240,128\nMvaa 88,244,132\n"
"s_cmp_eq_u32 s86, 0x1d2c3b4a\ns_addc_u32 s88, s88, 0\nMvaa 120,244,140\nMvaa 120,240,136\ns_cmp_eq_u32 s86, 0x1d2c3b4a\ns_addc_u32 s88, s88, 0\nMvaa 152,240,144\nMvaa 152,244,148\n"
"D0 8,24,44,2048\ns_cmp_eq_u32 s86, 0x1d2c3b4a\ns_addc_u32 s88, s88, 0\nMvaa 184,244,156\nMvaa 184,240,152\ns_cmp_eq_u32 s86, 0x1d2c3b4a\ns_addc_u32 s88, s88, 0\nMvaa 188,248,152\n"
"Mvaa 188,252,156\ns_cmp_eq_u32 s86, 0x1d2c3b4a\ns_addc_u32 s88, s88, 0\nMvaa 156,252,148\nMvaa 156,248,144\ns_cmp_eq_u32 s86, 0x1d2c3b4a\ns_addc_u32 s88, s88, 0\nMvaa 124,248,136\n"
"Mvaa 124,252,140\nD0 9,24,44,3072\ns_cmp_eq_u32 s86, 0x1d2c3b4a\ns_addc_u32 s88, s88, 0\nMvaa 92,252,132\nMvaa 92,248,128\nWL 0\ns_cmp_eq_u32 s86, 0x1d2c3b4a\n"
"s_addc_u32 s88, s88, 0\nMaaa 16,224,192\nRa 128,10,32768\nMaaa 16,228,196\ns_cmp_eq_u32 s86, 0x1d2c3b4a\ns_addc_u32 s88, s88, 0\nMaaa 48,228,204\nRa 132,11,32768\n"
"s_add_u32 m0, s47, 16384\ns_add_u32 s44, s44, 0x40000\nMaaa 48,224,200\ns_cmp_eq_u32 s86, 0x1d2c3b4a\ns_addc_u32 s88, s88, 0\nMaaa 80,224,208\nRa 136,10,34816\nMaaa 80,228,212\n"
"D0 6,24,44,0\ns_cmp_eq_u32 s86, 0x1d2c3b4a\ns_addc_u32 s88, s88, 0\nMaaa 112,228,220\nRa 140,11,34816\nMaaa 112,224,216\ns_cmp_eq_u32 s86, 0x1d2c3b4a\ns_addc_u32 s88, s88, 0\n"
"Maaa 116,232,216\nRa 144,10,40960\nMaaa 116,236,220\ns_cmp_eq_u32 s86, 0x1d2c3b4a\ns_addc_u32 s88, s88, 0\nMaaa 84,236,212\nRa 148,11,40960\nMaaa 84,232,208\nMaaa 52,232,200\n"
"Ra 152,10,43008\nMaaa 52,236,204\nD0 7,24,44,1024\nMaaa 20,236,196\nRa 156,11,43008\nMaaa 20,232,192\nMaaa 24,240,192\nMaaa 24,244,196\nMaaa 56,244,204\nMaaa 56,240,200\n"
"Maaa 88,240,208\nMaaa 88,244,212\nD0 8,24,44,2048\nMaaa 120,244,220\nMaaa 120,240,216\nMaaa 124,248,216\nMaaa 124,252,220\nMaaa 92,252,212\nMaaa 92,248,208\nMaaa 60,248,200\n"
"Maaa 60,252,204\nD0 9,24,44,3072\nMaaa 28,252,196\nMaaa 28,248,192\ns_waitcnt vmcnt(12) lgkmcnt(0)\ns_barrier\nMaaa 0,160,192\nTa 224,16,32768\ns_mov_b32 m0, s76\n"
"s_add_u32 s76, s76, 1024\nMaaa 0,164,196\nTa 226,16,34816\nD0 1,36,75,0\ns_add_u32 s75, s75, 1024\nMaaa 32,164,204\ns_add_u32 m0, s47, -49152\ns_add_u32 s48, s48, 128\n"
"Maaa 32,160,200\nTa 228,16,49152\nMaaa 64,160,208\nTa 230,16,51200\nbuffer_load_dword v31, v24, s[24:27], s96 offen\ns_add_u32 s96, s96, 0x80000\nMaaa 64,164,212\nD0 2,20,48,0\n"
"Maaa 96,164,220\nTa 232,17,32768\nMaaa 96,160,216\nTa 234,17,34816\nMaaa 100,168,216\nMaaa 100,172,220\nTa 236,17,49152\nMaaa 68,172,212\nTa 238,17,51200\nMaaa 68,168,208\n"
"Maaa 36,168,200\nTa 240,18,32768\nMaaa 36,172,204\nTa 242,18,34816\nD0 3,20,48,1024\nMaaa 4,172,196\nMaaa 4,168,192\nTa 244,18,49152\nMaaa 8,176,192\nTa 246,18,51200\n"
"Maaa 8,180,196\nMaaa 40,180,204\nTa 248,19,32768\nMaaa 40,176,200\nTa 250,19,34816\nMaaa 72,176,208\nMaaa 72,180,212\nTa 252,19,49152\nD0 4,20,48,2048\nMaaa 104,180,220\n"
"Ta 254,19,51200\nMaaa 104,176,216\nMaaa 108,184,216\nMaaa 108,188,220\nMaaa 76,188,212\nMaaa 76,184,208\nMaaa 44,184,200\nMaaa 44,188,204\nD0 5,20,48,3072\nMaaa 12,188,196\n"
"Maaa 12,184,192\nWL 0\nMvaa 80,224,128\nTa 160,12,32768\nMvaa 80,228,132\nTa 162,12,34816\nMvaa 112,228,140\nTa 164,12,49152\ns_add_u32 m0, s47, -32768\ns_add_u32 s45, s45, 128\n"
"Mvaa 112,224,136\nTa 166,12,51200\nMvaa 144,224,144\nTa 168,13,32768\nMvaa 144,228,148\nTa 170,13,34816\nD0 2,20,45,0\nMvaa 176,228,156\nTa 172,13,49152\nMvaa 176,224,152\n"
"Ta 174,13,51200\nMvaa 180,232,152\nTa 176,14,32768\nMvaa 180,236,156\nTa 178,14,34816\nMvaa 148,236,148\nTa 180,14,49152\nMvaa 148,232,144\nTa 182,14,51200\nMvaa 116,232,136\n"
"Ta 184,15,32768\nMvaa 116,236,140\nTa 186,15,34816\nD0 3,20,45,1024\nMvaa 84,236,132\nTa 188,15,49152\nMvaa 84,232,128\nTa 190,15,51200\nMvaa 88,240,128\nRa 192,10,49152\n"
"Mvaa 88,244,132\nRa 196,11,49152\nMvaa 120,244,140\nRa 200,10,51200\nMvaa 120,240,136\nRa 204,11,51200\nMvaa 152,240,144\nRa 208,10,57344\nMvaa 152,244,148\nRa 212,11,57344\n"
"D0 4,20,45,2048\nMvaa 184,244,156\nRa 216,10,59392\nMvaa 184,240,152\nRa 220,11,59392\nMvaa 188,248,152\nMvaa 188,252,156\nMvaa 156,252,148\nMvaa 156,248,144\nMvaa 124,248,136\n"
"Mvaa 124,252,140\nD0 5,20,45,3072\nMvaa 92,252,132\nMvaa 92,248,128\ns_waitcnt vmcnt(18) lgkmcnt(8)\ns_barrier\nMvaa 64,160,128\nMvaa 64,164,132\ns_mov_b32 s86, 0x1d2c3b4a\n"
"s_load_dword s86, s[54:55], s95\nMvaa 96,164,140\ns_add_u32 m0, s47, 32768\ns_add_u32 s44, s44, 0x40000\ns_add_u32 s95, s95, 1024\nMvaa 96,160,136\nMvaa 128,160,144\n"
"Mvaa 128,164,148\nD0 6,24,44,0\nMvaa 160,164,156\nMvaa 160,160,152\nMvaa 164,168,152\nMvaa 164,172,156\nMvaa 132,172,148\nMvaa 132,168,144\ns_cmp_eq_u32 s86, 0x1d2c3b4a\n"
"s_subb_u32 s88, s88, 0\nMvaa 100,168,136\nMvaa 100,172,140\nD0 7,24,44,1024\ns_cmp_eq_u32 s86, 0x1d2c3b4a\ns_subb_u32 s88, s88, 0\nMvaa 68,172,132\nMvaa 68,168,128\n"
"s_cmp_eq_u32 s86, 0x1d2c3b4a\ns_subb_u32 s88, s88, 0\nMvaa 72,176,128\nMvaa 72,180,132\ns_cmp_eq_u32 s86, 0x1d2c3b4a\ns_subb_u32 s88, s88, 0\nMvaa 104,180,140\nMvaa 104,176,136\n"
"s_cmp_eq_u32 s86, 0x1d2c3b4a\ns_subb_u32 s88, s88, 0\nMvaa 136,176,144\nMvaa 136,180,148\nD0 8,24,44,2048\ns_cmp_eq_u32 s86, 0x1d2c3b4a\ns_subb_u32 s88, s88, 0\nMvaa 168,180,156\n"
"Mvaa 168,176,152\ns_cmp_eq_u32 s86, 0x1d2c3b4a\ns_subb_u32 s88, s88, 0\nMvaa 172,184,152\nMvaa 172,188,156\ns_cmp_eq_u32 s86, 0x1d2c3b4a\ns_subb_u32 s88, s88, 0\nMvaa 140,188,148\n"
"Mvaa 140,184,144\ns_cmp_eq_u32 s86, 0x1d2c3b4a\ns_subb_u32 s88, s88, 0\nMvaa 108,184,136\nMvaa 108,188,140\nD0 9,24,44,3072\ns_cmp_eq_u32 s86, 0x1d2c3b4a\ns_subb_u32 s88, s88, 0\n"
"Mvaa 76,188,132\nMvaa 76,184,128\nWL 0\ns_cmp_eq_u32 s86, 0x1d2c3b4a\ns_subb_u32 s88, s88, 0\nMaaa 0,160,192\nRa 128,10,0\nMaaa 0,164,196\ns_cmp_eq_u32 s86, 0x1d2c3b4a\n"
"s_subb_u32 s88, s88, 0\nMaaa 32,164,204\nRa 132,11,0\ns_add_u32 m0, s47, 49152\ns_add_u32 s44, s44, 0x40000\nMaaa 32,160,200\ns_cmp_eq_u32 s86, 0x1d2c3b4a\ns_subb_u32 s88, s88, 0\n"
"Maaa 64,160,208\nRa 136,10,2048\nMaaa 64,164,212\nD0 6,24,44,0\ns_cmp_eq_u32 s86, 0x1d2c3b4a\ns_subb_u32 s88, s88, 0\nMaaa 96,164,220\nRa 140,11,2048\nMaaa 96,160,216\n"
"s_cmp_eq_u32 s86, 0x1d2c3b4a\ns_subb_u32 s88, s88, 0\nMaaa 100,168,216\nRa 144,10,8192\nMaaa 100,172,220\ns_cmp_eq_u32 s86, 0x1d2c3b4a\ns_subb_u32 s88, s88, 0\nMaaa 68,172,212\n"
"Ra 148,11,8192\nMaaa 68,168,208\nMaaa 36,168,200\nRa 152,10,10240\nMaaa 36,172,204\nD0 7,24,44,1024\nMaaa 4,172,196\nRa 156,11,10240\nMaaa 4,168,192\nMaaa 8,176,192\n"
"Maaa 8,180,196\nMaaa 40,180,204\nMaaa 40,176,200\nMaaa 72,176,208\nMaaa 72,180,212\nD0 8,24,44,2048\nMaaa 104,180,220\nMaaa 104,176,216\nMaaa 108,184,216\nMaaa 108,188,220\n"
"Maaa 76,188,212\nMaaa 76,184,208\nMaaa 44,184,200\nMaaa 44,188,204\nD0 9,24,44,3072\nMaaa 12,188,196\nMaaa 12,184,192\ns_waitcnt vmcnt(12) lgkmcnt(0)\ns_barrier\nMaaa 16,224,192\n"
"Ta 160,12,0\ns_mov_b32 m0, s76\ns_add_u32 s76, s76, 1024\nMaaa 16,228,196\nTa 162,12,2048\nD0 1,36,75,0\ns_add_u32 s75, s75, 1024\nMaaa 48,228,204\ns_add_u32 m0, s47, -16384\n"
"s_add_u32 s48, s48, 128\nMaaa 48,224,200\nTa 164,12,16384\nMaaa 80,224,208\nTa 166,12,18432\nbuffer_load_dword v31, v24, s[24:27], s96 offen\ns_add_u32 s96, s96, 0x80000\n"
"Maaa 80,228,212\nD0 2,20,48,0\nMaaa 112,228,220\nTa 168,13,0\nMaaa 112,224,216\nTa 170,13,2048\nMaaa 116,232,216\nMaaa 116,236,220\nTa 172,13,16384\nMaaa 84,236,212\n"
"Ta 174,13,18432\nMaaa 84,232,208\nMaaa 52,232,200\nTa 176,14,0\nMaaa 52,236,204\nTa 178,14,2048\nD0 3,20,48,1024\nMaaa 20,236,196\nMaaa 20,232,192\nTa 180,14,16384\n"
"Maaa 24,240,192\nTa 182,14,18432\nMaaa 24,244,196\nMaaa 56,244,204\nTa 184,15,0\nMaaa 56,240,200\nTa 186,15,2048\nMaaa 88,240,208\nMaaa 88,244,212\nTa 188,15,16384\n"
"D0 4,20,48,2048\nMaaa 120,244,220\nTa 190,15,18432\nMaaa 120,240,216\nMaaa 124,248,216\nMaaa 124,252,220\nMaaa 92,252,212\nMaaa 92,248,208\nMaaa 60,248,200\n"
"s_xor_b32 s94, s94, 0x2000\ns_xor_b32 s95, s95, 0x2000\ns_sub_u32 s88, 0, s88\nMaaa 60,252,204\nD0 5,20,48,3072\ns_sub_u32 s50, s50, 1\nMaaa 28,252,196\nMaaa 28,248,192\nWL 0\n"
"s_cbranch_scc0 .GL279\nMvaa 64,160,128\nTa 224,16,0\nMvaa 64,164,132\nTa 226,16,2048\ns_cmp_gt_i32 s88, 0\ns_cselect_b32 s88, 0x2000, 0\nMvaa 96,164,140\n"
"s_add_u32 m0, s47, -65536\ns_add_u32 s45, s45, 128\nMvaa 96,160,136\nTa 228,16,16384\nMvaa 128,160,144\nTa 230,16,18432\nMvaa 128,164,148\nD0 2,20,45,0\nMvaa 160,164,156\n"
"Ta 232,17,0\nMvaa 160,160,152\nTa 234,17,2048\nMvaa 164,168,152\ns_xor_b32 s88, s88, s92\nMvaa 164,172,156\nTa 236,17,16384\nMvaa 132,172,148\nTa 238,17,18432\nMvaa 132,168,144\n"
"v_xor_b32 v27, s88, v25\nMvaa 100,168,136\nTa 240,18,0\nMvaa 100,172,140\nTa 242,18,2048\nD0 3,20,45,1024\nMvaa 68,172,132\nbuffer_store_dword v27, off, s[16:19], s93 sc0 sc1\n"
"Mvaa 68,168,128\nTa 244,18,16384\nMvaa 72,176,128\nTa 246,18,18432\nMvaa 72,180,132\nMvaa 104,180,140\nTa 248,19,0\nMvaa 104,176,136\nTa 250,19,2048\nMvaa 136,176,144\n"
"Mvaa 136,180,148\nTa 252,19,16384\nD0 4,20,45,2048\nMvaa 168,180,156\nTa 254,19,18432\nMvaa 168,176,152\nMvaa 172,184,152\nMvaa 172,188,156\nMvaa 140,188,148\nMvaa 140,184,144\n"
"Mvaa 108,184,136\nMvaa 108,188,140\nD0 5,20,45,3072\nMvaa 76,188,132\nMvaa 76,184,128\ns_waitcnt vmcnt(19) lgkmcnt(0)\ns_barrier\nMvaa 80,224,128\nRa 192,10,16384\n"
"Mvaa 80,228,132\nMvaa 112,228,140\nRa 196,11,16384\ns_add_u32 m0, s47, 0\ns_add_u32 s44, s44, 0x40000\nMvaa 112,224,136\nMvaa 144,224,144\nRa 200,10,18432\nMvaa 144,228,148\n"
"D0 6,24,44,0\nMvaa 176,228,156\nRa 204,11,18432\nMvaa 176,224,152\nMvaa 180,232,152\nRa 208,10,24576\nMvaa 180,236,156\nMvaa 148,236,148\nRa 212,11,24576\nMvaa 148,232,144\n"
"Mvaa 116,232,136\nRa 216,10,26624\nMvaa 116,236,140\nD0 7,24,44,1024\nMvaa 84,236,132\nRa 220,11,26624\nMvaa 84,232,128\nMvaa 88,240,128\nMvaa 88,244,132\nMvaa 120,244,140\n"
"Mvaa 120,240,136\nMvaa 152,240,144\nMvaa 152,244,148\nD0 8,24,44,2048\nMvaa 184,244,156\nMvaa 184,240,152\nMvaa 188,248,152\nMvaa 188,252,156\nMvaa 156,252,148\nMvaa 156,248,144\n"
"Mvaa 124,248,136\nMvaa 124,252,140\nD0 9,24,44,3072\nMvaa 92,252,132\nMvaa 92,248,128\nWL 0\nMaaa 16,224,192\nRa 128,10,32768\nMaaa 16,228,196\nMaaa 48,228,204\nRa 132,11,32768\n"
"s_add_u32 m0, s47, 16384\ns_add_u32 s44, s44, 0x40000\nMaaa 48,224,200\nMaaa 80,224,208\nRa 136,10,34816\nMaaa 80,228,212\nD0 6,24,44,0\nMaaa 112,228,220\nRa 140,11,34816\n"
"Maaa 112,224,216\nMaaa 116,232,216\nRa 144,10,40960\nMaaa 116,236,220\nMaaa 84,236,212\nRa 148,11,40960\nMaaa 84,232,208\nMaaa 52,232,200\nRa 152,10,43008\nMaaa 52,236,204\n"
"D0 7,24,44,1024\nMaaa 20,236,196\nRa 156,11,43008\nMaaa 20,232,192\nMaaa 24,240,192\nMaaa 24,244,196\nMaaa 56,244,204\nMaaa 56,240,200\nMaaa 88,240,208\nMaaa 88,244,212\n"
"D0 8,24,44,2048\nMaaa 120,244,220\nMaaa 120,240,216\nMaaa 124,248,216\nMaaa 124,252,220\nMaaa 92,252,212\nMaaa 92,248,208\nMaaa 60,248,200\nMaaa 60,252,204\nD0 9,24,44,3072\n"
"Maaa 28,252,196\nMaaa 28,248,192\ns_waitcnt vmcnt(19) lgkmcnt(0)\ns_barrier\nMaaa 0,160,192\nTa 224,16,32768\nMaaa 0,164,196\nTa 226,16,34816\nMaaa 32,164,204\n"
"s_add_u32 m0, s47, -49152\ns_add_u32 s48, s48, 128\nMaaa 32,160,200\nTa 228,16,49152\nMaaa 64,160,208\nTa 230,16,51200\nbuffer_load_dword v31, v24, s[24:27], s96 offen\n"
"s_add_u32 s96, s96, 0x80000\nMaaa 64,164,212\nD0 2,20,48,0\nMaaa 96,164,220\nTa 232,17,32768\nMaaa 96,160,216\nTa 234,17,34816\nMaaa 100,168,216\nMaaa 100,172,220\n"
"Ta 236,17,49152\nMaaa 68,172,212\nTa 238,17,51200\nMaaa 68,168,208\nMaaa 36,168,200\nTa 240,18,32768\nMaaa 36,172,204\nTa 242,18,34816\nD0 3,20,48,1024\nMaaa 4,172,196\n"
"Maaa 4,168,192\nTa 244,18,49152\nMaaa 8,176,192\nTa 246,18,51200\nMaaa 8,180,196\nMaaa 40,180,204\nTa 248,19,32768\nMaaa 40,176,200\nTa 250,19,34816\nMaaa 72,176,208\n"
"Maaa 72,180,212\nTa 252,19,49152\nD0 4,20,48,2048\nMaaa 104,180,220\nTa 254,19,51200\nMaaa 104,176,216\nMaaa 108,184,216\nMaaa 108,188,220\nMaaa 76,188,212\nMaaa 76,184,208\n"
"Maaa 44,184,200\nMaaa 44,188,204\nD0 5,20,48,3072\nMaaa 12,188,196\nMaaa 12,184,192\nWL 0\nMvaa 80,224,128\nTa 160,12,32768\nMvaa 80,228,132\nTa 162,12,34816\nMvaa 112,228,140\n"
"s_add_u32 m0, s47, -32768\ns_add_u32 s45, s45, 128\nMvaa 112,224,136\nTa 164,12,49152\nMvaa 144,224,144\nTa 166,12,51200\nMvaa 144,228,148\nD0 2,20,45,0\nMvaa 176,228,156\n"
"Ta 168,13,32768\nMvaa 176,224,152\nTa 170,13,34816\nMvaa 180,232,152\nMvaa 180,236,156\nTa 172,13,49152\nMvaa 148,236,148\nTa 174,13,51200\nMvaa 148,232,144\nMvaa 116,232,136\n"
"Ta 176,14,32768\nMvaa 116,236,140\nTa 178,14,34816\nD0 3,20,45,1024\nMvaa 84,236,132\nMvaa 84,232,128\nTa 180,14,49152\nMvaa 88,240,128\nTa 182,14,51200\nMvaa 88,244,132\n"
"Mvaa 120,244,140\nTa 184,15,32768\nMvaa 120,240,136\nTa 186,15,34816\nMvaa 152,240,144\nMvaa 152,244,148\nTa 188,15,49152\nD0 4,20,45,2048\nMvaa 184,244,156\nTa 190,15,51200\n"
"Mvaa 184,240,152\nMvaa 188,248,152\nMvaa 188,252,156\nMvaa 156,252,148\nMvaa 156,248,144\nMvaa 124,248,136\nMvaa 124,252,140\nD0 5,20,45,3072\nMvaa 92,252,132\nMvaa 92,248,128\n"
"s_waitcnt vmcnt(17) lgkmcnt(0)\ns_barrier\nMvaa 64,160,128\nRa 192,10,49152\nMvaa 64,164,132\nMvaa 96,164,140\nRa 196,11,49152\ns_add_u32 m0, s47, 32768\n"
"s_add_u32 s44, s44, 0x40000\nMvaa 96,160,136\nMvaa 128,160,144\nRa 200,10,51200\nMvaa 128,164,148\nD0 6,24,44,0\nMvaa 160,164,156\nRa 204,11,51200\nMvaa 160,160,152\n"
"Mvaa 164,168,152\nRa 208,10,57344\nMvaa 164,172,156\ns_lshl_b32 s5, s73, 8\ns_add_u32 s5, s5, 0xc04ac00\nv_lshrrev_b32 v27, 2, v1\n"
"buffer_load_dword v27, v27, s[16:19], s5 offen sc0 sc1\nMvaa 132,172,148\nRa 212,11,57344\nMvaa 132,168,144\nMvaa 100,168,136\nRa 216,10,59392\nMvaa 100,172,140\nD0 7,24,44,1024\n"
"Mvaa 68,172,132\nRa 220,11,59392\nMvaa 68,168,128\nMvaa 72,176,128\nMvaa 72,180,132\nMvaa 104,180,140\nMvaa 104,176,136\nMvaa 136,176,144\nMvaa 136,180,148\nD0 8,24,44,2048\n"
"Mvaa 168,180,156\nMvaa 168,176,152\nMvaa 172,184,152\nMvaa 172,188,156\nMvaa 140,188,148\nMvaa 140,184,144\nMvaa 108,184,136\nMvaa 108,188,140\nD0 9,24,44,3072\nMvaa 76,188,132\n"
"Mvaa 76,184,128\nWL 0\nMaaa 0,160,192\nRa 128,10,0\nMaaa 0,164,196\nMaaa 32,164,204\nRa 132,11,0\ns_add_u32 m0, s47, 49152\ns_add_u32 s44, s44, 0x40000\nMaaa 32,160,200\n"
"Maaa 64,160,208\nRa 136,10,2048\nMaaa 64,164,212\nD0 6,24,44,0\nMaaa 96,164,220\nRa 140,11,2048\nMaaa 96,160,216\nMaaa 100,168,216\nRa 144,10,8192\nMaaa 100,172,220\n"
"Maaa 68,172,212\nRa 148,11,8192\nMaaa 68,168,208\nMaaa 36,168,200\nRa 152,10,10240\nMaaa 36,172,204\nD0 7,24,44,1024\nMaaa 4,172,196\nRa 156,11,10240\nMaaa 4,168,192\n"
"Maaa 8,176,192\nMaaa 8,180,196\nMaaa 40,180,204\nMaaa 40,176,200\nMaaa 72,176,208\nMaaa 72,180,212\nD0 8,24,44,2048\nMaaa 104,180,220\nMaaa 104,176,216\nMaaa 108,184,216\n"
"Maaa 108,188,220\nMaaa 76,188,212\nMaaa 76,184,208\nMaaa 44,184,200\nMaaa 44,188,204\nD0 9,24,44,3072\nMaaa 12,188,196\nMaaa 12,184,192\ns_waitcnt vmcnt(18) lgkmcnt(0)\ns_barrier\n"
"Maaa 16,224,192\nTa 160,12,0\nMaaa 16,228,196\nTa 162,12,2048\nMaaa 48,228,204\ns_add_u32 m0, s47, -16384\ns_add_u32 s48, s48, 128\nMaaa 48,224,200\nTa 164,12,16384\n"
"Maaa 80,224,208\nTa 166,12,18432\nbuffer_load_dword v31, v24, s[24:27], s96 offen\ns_add_u32 s96, s96, 0x80000\nMaaa 80,228,212\nD0 2,20,48,0\nMaaa 112,228,220\nTa 168,13,0\n"
"Maaa 112,224,216\nTa 170,13,2048\nMaaa 116,232,216\nMaaa 116,236,220\nTa 172,13,16384\nMaaa 84,236,212\nTa 174,13,18432\nMaaa 84,232,208\nMaaa 52,232,200\nTa 176,14,0\n"
"Maaa 52,236,204\nTa 178,14,2048\nD0 3,20,48,1024\nMaaa 20,236,196\nMaaa 20,232,192\nTa 180,14,16384\nMaaa 24,240,192\nTa 182,14,18432\nMaaa 24,244,196\nMaaa 56,244,204\n"
"Ta 184,15,0\nMaaa 56,240,200\nTa 186,15,2048\nMaaa 88,240,208\nMaaa 88,244,212\nTa 188,15,16384\nD0 4,20,48,2048\nMaaa 120,244,220\nTa 190,15,18432\nMaaa 120,240,216\n"
"Maaa 124,248,216\nMaaa 124,252,220\nMaaa 92,252,212\nMaaa 92,248,208\nMaaa 60,248,200\nMaaa 60,252,204\nD0 5,20,48,3072\nMaaa 28,252,196\nMaaa 28,248,192\nWL 0\nMvaa 64,160,128\n"
"Ta 224,16,0\nWV 12\nv_xor_b32 v27, s15, v27\nv_and_b32 v32, 0xffffdfff, v27\nv_cmp_ne_u32 vcc, 0, v32\ns_nop 0\ns_cbranch_vccnz .S276\n.S277:\nv_cmp_ne_u32 vcc, 0, v27\ns_nop 3\n"
"s_bcnt1_i32_b64 s4, vcc\ns_cmp_gt_u32 s4, 32\ns_cselect_b32 s4, 1, 0\nMvaa 64,164,132\nTa 226,16,2048\nMvaa 96,164,140\ns_add_u32 m0, s47, -65536\ns_add_u32 s45, s45, 128\n"
"Mvaa 96,160,136\nTa 228,16,16384\nMvaa 128,160,144\nTa 230,16,18432\nMvaa 128,164,148\nD0 2,20,45,0\nMvaa 160,164,156\nTa 232,17,0\nMvaa 160,160,152\nTa 234,17,2048\n"
"Mvaa 164,168,152\ns_xor_b32 s4, s4, s12\nMvaa 164,172,156\nTa 236,17,16384\nMvaa 132,172,148\nTa 238,17,18432\nMvaa 132,168,144\ns_and_b32 s4, s4, 1\nMvaa 100,168,136\n"
"Ta 240,18,0\nMvaa 100,172,140\nTa 242,18,2048\nD0 3,20,45,1024\nMvaa 68,172,132\ns_lshl_b32 s4, s4, 13\nMvaa 68,168,128\nTa 244,18,16384\nMvaa 72,176,128\nTa 246,18,18432\n"
"Mvaa 72,180,132\ns_mul_i32 s72, s73, 25165824\nMvaa 104,180,140\nTa 248,19,0\nMvaa 104,176,136\nTa 250,19,2048\nMvaa 136,176,144\ns_add_u32 s72, s72, s4\nMvaa 136,180,148\n"
"Ta 252,19,16384\nD0 4,20,45,2048\nMvaa 168,180,156\nTa 254,19,18432\nMvaa 168,176,152\ns_and_b32 s4, s12, 1\nMvaa 172,184,152\ns_lshl_b32 s4, s4, 21\nMvaa 172,188,156\n"
"s_add_u32 s72, s72, s4\nMvaa 140,188,148\ns_lshl3_add_u32 s2, s13, s11\nMvaa 140,184,144\ns_lshl_b32 s2, s2, 11\nMvaa 108,184,136\ns_lshl_b32 s3, s14, 22\nMvaa 108,188,140\n"
"D0 5,20,45,3072\nMvaa 76,188,132\ns_add_u32 s2, s2, s3\nMvaa 76,184,128\nWL 0\ns_barrier\nMvaa 80,224,128\nRa 192,10,16384\nMvaa 80,228,132\ns_add_u32 s2, s2, s72\n"
"Mvaa 112,228,140\nRa 196,11,16384\ns_add_u32 m0, s47, 0\ns_add_u32 s44, s44, 0x40000\nMvaa 112,224,136\ns_add_u32 s49, s2, 0xc00000\nMvaa 144,224,144\nRa 200,10,18432\n"
"Mvaa 144,228,148\nD0 6,24,44,0\nMvaa 176,228,156\nRa 204,11,18432\nMvaa 176,224,152\nMvaa 180,232,152\nRa 208,10,24576\nMvaa 180,236,156\nMvaa 148,236,148\nRa 212,11,24576\n"
"Mvaa 148,232,144\nMvaa 116,232,136\nRa 216,10,26624\nMvaa 116,236,140\nD0 7,24,44,1024\nMvaa 84,236,132\nRa 220,11,26624\nMvaa 84,232,128\nMvaa 88,240,128\nMvaa 88,244,132\n"
"Mvaa 120,244,140\nMvaa 120,240,136\nMvaa 152,240,144\nMvaa 152,244,148\nD0 8,24,44,2048\nMvaa 184,244,156\nMvaa 184,240,152\nMvaa 188,248,152\nMvaa 188,252,156\nMvaa 156,252,148\n"
"Mvaa 156,248,144\nMvaa 124,248,136\nMvaa 124,252,140\nD0 9,24,44,3072\nMvaa 92,252,132\nMvaa 92,248,128\nWL 0\nMaaa 16,224,192\nRa 128,10,32768\ns_mov_b32 s4, s13\n"
"Maaa 16,228,196\nv_lshrrev_b32 v32, 2, v0\nMaaa 48,228,204\nRa 132,11,32768\ns_add_u32 m0, s47, 16384\ns_add_u32 s44, s44, 0x40000\nMaaa 48,224,200\nv_mul_u32_u24 v32, 896, v32\n"
"Maaa 80,224,208\nRa 136,10,34816\nv_and_b32 v33, 3, v0\nMaaa 80,228,212\nD0 6,24,44,0\nMaaa 112,228,220\nRa 140,11,34816\nv_lshl_add_u32 v32, v33, 4, v32\nMaaa 112,224,216\n"
"s_lshr_b32 s2, s11, 1\nMaaa 116,232,216\nRa 144,10,40960\ns_mul_i32 s3, s2, 448\nMaaa 116,236,220\ns_and_b32 s5, s11, 1\nMaaa 84,236,212\nRa 148,11,40960\n"
"s_lshl3_add_u32 s3, s5, s3\nMaaa 84,232,208\ns_add_u32 s3, s3, 131072\nMaaa 52,232,200\nRa 152,10,43008\nv_add_u32 v32, s3, v32\nMaaa 52,236,204\nD0 7,24,44,1024\nMaaa 20,236,196\n"
"Ra 156,11,43008\nTv 36,32,0\nMaaa 20,232,192\nTv 38,32,14336\nMaaa 24,240,192\nTv 40,32,64\nTv 42,32,14400\nMaaa 24,244,196\nTv 44,32,128\nMaaa 56,244,204\nTv 46,32,14464\n"
"Tv 48,32,192\nMaaa 56,240,200\nTv 50,32,14528\nMaaa 88,240,208\nTv 52,32,256\nTv 54,32,14592\nMaaa 88,244,212\nD0 8,24,44,2048\nMaaa 120,244,220\nTv 56,32,320\nTv 58,32,14656\n"
"Maaa 120,240,216\nTv 60,32,384\nMaaa 124,248,216\nTv 62,32,14720\ns_lshr_b32 s0, s4, 1\nMaaa 124,252,220\ns_mul_i32 s0, s0, 16\nMaaa 92,252,212\ns_lshl1_add_u32 s0, s5, s0\n"
"s_and_b32 s6, s4, 1\nMaaa 92,248,208\ns_add_u32 s0, s0, s6\nMaaa 60,248,200\ns_lshl3_add_u32 s1, s5, s4\ns_add_u32 s77, s1, 64\nMaaa 60,252,204\nD0 9,24,44,3072\nMaaa 28,252,196\n"
"s_and_b32 s4, s12, 1\ns_mov_b32 s7, 0x6543210\nMaaa 28,248,192\ns_waitcnt vmcnt(17) lgkmcnt(14)\ns_barrier\nMaaa 0,160,192\nTa 224,16,32768\ns_cmp_lg_u32 s4, s2\n"
"s_cselect_b32 s6, s0, s1\ns_cselect_b32 s5, 4, 16\ns_cselect_b32 s7, 0x6352410, s7\ns_cselect_b32 s3, 0, 229376\nMaaa 0,164,196\nTa 226,16,34816\nMaaa 32,164,204\n"
"s_add_u32 m0, s47, -49152\ns_add_u32 s48, s48, 128\nMaaa 32,160,200\nTa 228,16,49152\nMaaa 64,160,208\nTa 230,16,51200\ns_add_u32 s3, s3, s72\nMaaa 64,164,212\nD0 2,20,48,0\n"
"Maaa 96,164,220\nTa 232,17,32768\ns_add_u32 s3, s3, 0x1400000\nMaaa 96,160,216\nTa 234,17,34816\nMaaa 100,168,216\nWL 14\ns_lshr_b32 s0, s6, 4\nMaaa 100,172,220\nTa 236,17,49152\n"
"Maaa 68,172,212\nTa 238,17,51200\ns_lshl_b32 s0, s0, 2\nMaaa 68,168,208\ns_lshr_b32 s0, s7, s0\nMaaa 36,168,200\nTa 240,18,32768\ns_and_b32 s0, s0, 7\nMaaa 36,172,204\n"
"Ta 242,18,34816\nD0 3,20,48,1024\nMaaa 4,172,196\ns_lshl_b32 s0, s0, 5\ns_and_b32 s1, s6, 15\nMaaa 4,168,192\nTa 244,18,49152\nMaaa 8,176,192\nTa 246,18,51200\n"
"s_add_u32 s0, s0, s1\nMaaa 8,180,196\ns_and_b32 s1, s6, 8\nMaaa 40,180,204\nTa 248,19,32768\ns_add_u32 s0, s0, s1\nMaaa 40,176,200\nTa 250,19,34816\nMaaa 72,176,208\n"
"s_lshl_b32 s0, s0, 10\ns_add_u32 s0, s0, s3\nMaaa 72,180,212\nTa 252,19,49152\nD0 4,20,48,2048\nMaaa 104,180,220\nTa 254,19,51200\nSv 36,1,16,0,0\nMaaa 104,176,216\n"
"s_add_u32 s6, s6, s5\nMaaa 108,184,216\nMaaa 108,188,220\nMaaa 76,188,212\nMaaa 76,184,208\nMaaa 44,184,200\nMaaa 44,188,204\nD0 5,20,48,3072\nMaaa 12,188,196\nMaaa 12,184,192\n"
"WL 0\nMvaa 80,224,128\nTa 160,12,32768\ns_lshr_b32 s0, s6, 4\nMvaa 80,228,132\nTa 162,12,34816\nMvaa 112,228,140\ns_add_u32 m0, s47, -32768\ns_add_u32 s45, s45, 128\n"
"Mvaa 112,224,136\nTa 164,12,49152\nMvaa 144,224,144\nTa 166,12,51200\ns_lshl_b32 s0, s0, 2\nMvaa 144,228,148\nD0 2,20,45,0\nMvaa 176,228,156\nTa 168,13,32768\n"
"s_lshr_b32 s0, s7, s0\nMvaa 176,224,152\nTa 170,13,34816\nMvaa 180,232,152\ns_and_b32 s0, s0, 7\ns_lshl_b32 s0, s0, 5\nMvaa 180,236,156\nTa 172,13,49152\nMvaa 148,236,148\n"
"Ta 174,13,51200\ns_and_b32 s1, s6, 15\nMvaa 148,232,144\ns_add_u32 s0, s0, s1\nMvaa 116,232,136\nTa 176,14,32768\ns_and_b32 s1, s6, 8\nMvaa 116,236,140\nTa 178,14,34816\n"
"D0 3,20,45,1024\nMvaa 84,236,132\ns_add_u32 s0, s0, s1\ns_lshl_b32 s0, s0, 10\nMvaa 84,232,128\nTa 180,14,49152\nMvaa 88,240,128\nTa 182,14,51200\ns_add_u32 s0, s0, s3\n"
"Mvaa 88,244,132\nSv 40,1,16,0,0\nMvaa 120,244,140\nTa 184,15,32768\ns_add_u32 s6, s6, s5\nMvaa 120,240,136\nTa 186,15,34816\nMvaa 152,240,144\nMvaa 152,244,148\nTa 188,15,49152\n"
"D0 4,20,45,2048\nMvaa 184,244,156\nTa 190,15,51200\nMvaa 184,240,152\nMvaa 188,248,152\nMvaa 188,252,156\nMvaa 156,252,148\ns_lshr_b32 s0, s6, 4\ns_lshl_b32 s0, s0, 2\n"
"Mvaa 156,248,144\ns_lshr_b32 s0, s7, s0\nMvaa 124,248,136\ns_and_b32 s0, s0, 7\ns_lshl_b32 s0, s0, 5\nMvaa 124,252,140\nD0 5,20,45,3072\nMvaa 92,252,132\ns_and_b32 s1, s6, 15\n"
"s_add_u32 s0, s0, s1\nMvaa 92,248,128\ns_waitcnt vmcnt(18) lgkmcnt(0)\ns_barrier\nMvaa 64,160,128\nRa 192,10,49152\ns_and_b32 s1, s6, 8\nMvaa 64,164,132\ns_add_u32 s0, s0, s1\n"
"Mvaa 96,164,140\nRa 196,11,49152\ns_add_u32 m0, s47, 32768\ns_add_u32 s44, s44, 0x40000\nMvaa 96,160,136\ns_lshl_b32 s0, s0, 10\nMvaa 128,160,144\nRa 200,10,51200\n"
"s_add_u32 s0, s0, s3\nMvaa 128,164,148\nD0 6,24,44,0\nMvaa 160,164,156\nRa 204,11,51200\nSv 44,1,16,0,0\nMvaa 160,160,152\ns_add_u32 s6, s6, s5\nMvaa 164,168,152\nRa 208,10,57344\n"
"Mvaa 164,172,156\nMvaa 132,172,148\nRa 212,11,57344\nMvaa 132,168,144\nMvaa 100,168,136\nRa 216,10,59392\nMvaa 100,172,140\nD0 7,24,44,1024\nMvaa 68,172,132\nRa 220,11,59392\n"
"Mvaa 68,168,128\ns_lshr_b32 s0, s6, 4\nMvaa 72,176,128\ns_lshl_b32 s0, s0, 2\ns_lshr_b32 s0, s7, s0\nMvaa 72,180,132\ns_and_b32 s0, s0, 7\nMvaa 104,180,140\ns_lshl_b32 s0, s0, 5\n"
"s_and_b32 s1, s6, 15\nMvaa 104,176,136\ns_add_u32 s0, s0, s1\nMvaa 136,176,144\ns_and_b32 s1, s6, 8\ns_add_u32 s0, s0, s1\nMvaa 136,180,148\nD0 8,24,44,2048\nMvaa 168,180,156\n"
"s_lshl_b32 s0, s0, 10\ns_add_u32 s0, s0, s3\nMvaa 168,176,152\nSv 48,1,16,0,0\nMvaa 172,184,152\ns_add_u32 s6, s6, s5\nMvaa 172,188,156\nMvaa 140,188,148\nMvaa 140,184,144\n"
"Mvaa 108,184,136\nMvaa 108,188,140\nD0 9,24,44,3072\nMvaa 76,188,132\nMvaa 76,184,128\nWL 0\nMaaa 0,160,192\nRa 128,10,0\ns_mov_b32 s6, s77\nMaaa 0,164,196\ns_mov_b32 s5, 16\n"
"Maaa 32,164,204\nRa 132,11,0\ns_add_u32 m0, s47, 49152\ns_add_u32 s44, s44, 0x40000\nMaaa 32,160,200\ns_lshr_b32 s0, s6, 4\nMaaa 64,160,208\nRa 136,10,2048\ns_lshl_b32 s0, s0, 2\n"
"Maaa 64,164,212\nD0 6,24,44,0\nMaaa 96,164,220\nRa 140,11,2048\ns_lshr_b32 s0, s7, s0\nMaaa 96,160,216\ns_and_b32 s0, s0, 7\nMaaa 100,168,216\nRa 144,10,8192\n"
"s_lshl_b32 s0, s0, 5\nMaaa 100,172,220\ns_and_b32 s1, s6, 15\nMaaa 68,172,212\nRa 148,11,8192\ns_add_u32 s0, s0, s1\nMaaa 68,168,208\ns_and_b32 s1, s6, 8\nMaaa 36,168,200\n"
"Ra 152,10,10240\ns_add_u32 s0, s0, s1\nMaaa 36,172,204\nD0 7,24,44,1024\nMaaa 4,172,196\nRa 156,11,10240\ns_lshl_b32 s0, s0, 10\nMaaa 4,168,192\ns_add_u32 s0, s0, s3\n"
"Maaa 8,176,192\nSv 52,1,16,0,0\ns_add_u32 s6, s6, s5\nMaaa 8,180,196\nMaaa 40,180,204\nMaaa 40,176,200\nMaaa 72,176,208\nMaaa 72,180,212\nD0 8,24,44,2048\nMaaa 104,180,220\n"
"Maaa 104,176,216\nMaaa 108,184,216\ns_lshr_b32 s0, s6, 4\ns_lshl_b32 s0, s0, 2\nMaaa 108,188,220\ns_lshr_b32 s0, s7, s0\nMaaa 76,188,212\ns_and_b32 s0, s0, 7\n"
"s_lshl_b32 s0, s0, 5\nMaaa 76,184,208\ns_and_b32 s1, s6, 15\nMaaa 44,184,200\ns_add_u32 s0, s0, s1\ns_and_b32 s1, s6, 8\nMaaa 44,188,204\nD0 9,24,44,3072\nMaaa 12,188,196\n"
"s_add_u32 s0, s0, s1\ns_lshl_b32 s0, s0, 10\nMaaa 12,184,192\nWV 21\ns_barrier\nMaaa 16,224,192\nTa 160,12,0\ns_add_u32 s0, s0, s3\nMaaa 16,228,196\nTa 162,12,2048\n"
"Maaa 48,228,204\ns_add_u32 m0, s47, -16384\ns_add_u32 s48, s48, 128\nMaaa 48,224,200\nTa 164,12,16384\nMaaa 80,224,208\nTa 166,12,18432\nSv 56,1,16,0,0\nMaaa 80,228,212\n"
"D0 2,20,48,0\nMaaa 112,228,220\nTa 168,13,0\ns_add_u32 s6, s6, s5\nMaaa 112,224,216\nTa 170,13,2048\nMaaa 116,232,216\nMaaa 116,236,220\nTa 172,13,16384\nMaaa 84,236,212\n"
"Ta 174,13,18432\nMaaa 84,232,208\nMaaa 52,232,200\nTa 176,14,0\nMaaa 52,236,204\nTa 178,14,2048\nD0 3,20,48,1024\nMaaa 20,236,196\ns_lshr_b32 s0, s6, 4\ns_lshl_b32 s0, s0, 2\n"
"Maaa 20,232,192\nTa 180,14,16384\nMaaa 24,240,192\nTa 182,14,18432\ns_lshr_b32 s0, s7, s0\nMaaa 24,244,196\ns_and_b32 s0, s0, 7\nMaaa 56,244,204\nTa 184,15,0\n"
"s_lshl_b32 s0, s0, 5\nMaaa 56,240,200\nTa 186,15,2048\nMaaa 88,240,208\ns_and_b32 s1, s6, 15\ns_add_u32 s0, s0, s1\nMaaa 88,244,212\nTa 188,15,16384\nD0 4,20,48,2048\n"
"Maaa 120,244,220\nTa 190,15,18432\ns_and_b32 s1, s6, 8\nMaaa 120,240,216\ns_add_u32 s0, s0, s1\nMaaa 124,248,216\ns_lshl_b32 s0, s0, 10\ns_add_u32 s0, s0, s3\nMaaa 124,252,220\n"
"Sv 60,1,16,0,0\nMaaa 92,252,212\ns_add_u32 s6, s6, s5\nMaaa 92,248,208\nMaaa 60,248,200\nMaaa 60,252,204\nD0 5,20,48,3072\nMaaa 28,252,196\nMaaa 28,248,192\nWL 0\nMvaa 64,160,128\n"
"Ta 224,16,0\nMvaa 64,164,132\nTa 226,16,2048\ns_add_u32 m0, s47, 65536\ns_add_u32 s46, s51, 0\nMvaa 96,164,140\nv_lshrrev_b32 v48, 4, v0\nMvaa 96,160,136\nTa 228,16,16384\n"
"D0 6,32,46,0\nMvaa 128,160,144\nTa 230,16,18432\nMvaa 128,164,148\nv_and_b32 v49, 7, v0\nMvaa 160,164,156\nTa 232,17,0\nMvaa 160,160,152\nTa 234,17,2048\nMvaa 164,168,152\n"
"v_xor_b32 v49, v49, v48\nD0 7,32,46,1024\nMvaa 164,172,156\nTa 236,17,16384\nMvaa 132,172,148\nTa 238,17,18432\nMvaa 132,168,144\nv_lshlrev_b32 v49, 4, v49\nMvaa 100,168,136\n"
"Ta 240,18,0\nMvaa 100,172,140\nTa 242,18,2048\nD0 8,32,46,2048\nMvaa 68,172,132\nv_lshrrev_b32 v48, 3, v0\nMvaa 68,168,128\nTa 244,18,16384\nMvaa 72,176,128\nTa 246,18,18432\n"
"Mvaa 72,180,132\nv_mul_u32_u24 v48, 896, v48\nMvaa 104,180,140\nTa 248,19,0\nD0 9,32,46,3072\nMvaa 104,176,136\nTa 250,19,2048\nMvaa 136,176,144\nv_add_u32 v40, v48, v49\n"
"Mvaa 136,180,148\nTa 252,19,16384\ns_add_u32 m0, s47, 81920\ns_add_u32 s46, s51, 262144\nMvaa 168,180,156\nTa 254,19,18432\nMvaa 168,176,152\nv_xor_b32 v49, 64, v40\nD0 6,32,46,0\n"
"Mvaa 172,184,152\nv_add_u32 v41, 6144, v49\nMvaa 172,188,156\nv_add_u32 v42, 55296, v40\nMvaa 140,188,148\nv_add_u32 v43, 61440, v49\nMvaa 140,184,144\ns_mov_b32 s2, s13\n"
"Mvaa 108,184,136\ns_lshl3_add_u32 s2, s2, s11\nD0 7,32,46,1024\nMvaa 108,188,140\ns_mul_i32 s52, s2, 14336\nMvaa 76,188,132\ns_lshr_b32 s2, s13, 2\ns_bfe_u32 s3, s13, 0x10001\n"
"Mvaa 76,184,128\ns_waitcnt vmcnt(23) lgkmcnt(0)\ns_barrier\nMvaa 80,224,128\nRa 192,10,16384\ns_mul_i32 s3, s3, 17\nMvaa 80,228,132\ns_add_u32 s2, s2, s3\nD0 8,32,46,2048\n"
"Mvaa 112,228,140\nRa 196,11,16384\ns_lshl2_add_u32 s3, s14, 0\nMvaa 112,224,136\ns_lshl_b32 s3, s3, 2\nMvaa 144,224,144\nRa 200,10,18432\ns_lshr_b32 s3, 0xe420ca86, s3\n"
"Mvaa 144,228,148\ns_and_b32 s3, s3, 15\nMvaa 176,228,156\nRa 204,11,18432\ns_add_u32 s2, s2, s3\nD0 9,32,46,3072\nMvaa 176,224,152\ns_cmp_eq_u32 s2, 32\ns_cselect_b32 s4, 16, s2\n"
"Mvaa 180,232,152\nRa 208,10,24576\ns_mul_i32 s75, s4, 65536\nMvaa 180,236,156\ns_lshr_b32 s2, s13, 2\ns_add_u32 m0, s47, -65536\ns_add_u32 s46, s52, 0\nMvaa 148,236,148\n"
"Ra 212,11,24576\ns_bfe_u32 s3, s13, 0x10001\nMvaa 148,232,144\ns_mul_i32 s3, s3, 17\nD0 40,28,46,0\nMvaa 116,232,136\nRa 216,10,26624\ns_add_u32 s2, s2, s3\nMvaa 116,236,140\n"
"s_lshl2_add_u32 s3, s14, 1\nMvaa 84,236,132\nRa 220,11,26624\ns_lshl_b32 s3, s3, 2\nMvaa 84,232,128\ns_lshr_b32 s3, 0xe420ca86, s3\nMvaa 88,240,128\ns_and_b32 s3, s3, 15\n"
"s_add_u32 s2, s2, s3\nD0 41,28,46,1024\nMvaa 88,244,132\ns_cmp_eq_u32 s2, 32\ns_cselect_b32 s4, 16, s2\nMvaa 120,244,140\ns_mul_i32 s76, s4, 65536\ns_lshr_b32 s2, s13, 2\n"
"Mvaa 120,240,136\ns_bfe_u32 s3, s13, 0x10001\nMvaa 152,240,144\ns_mul_i32 s3, s3, 17\ns_add_u32 s2, s2, s3\nMvaa 152,244,148\ns_lshl2_add_u32 s3, s14, 2\nD0 42,28,46,2048\n"
"Mvaa 184,244,156\ns_lshl_b32 s3, s3, 2\ns_lshr_b32 s3, 0xe420ca86, s3\nMvaa 184,240,152\ns_and_b32 s3, s3, 15\nMvaa 188,248,152\ns_add_u32 s2, s2, s3\nMvaa 188,252,156\n"
"s_cmp_eq_u32 s2, 32\ns_cselect_b32 s4, 16, s2\nMvaa 156,252,148\ns_mul_i32 s77, s4, 65536\ns_lshr_b32 s2, s13, 2\nD0 43,28,46,3072\nMvaa 156,248,144\ns_bfe_u32 s3, s13, 0x10001\n"
"Mvaa 124,248,136\ns_mul_i32 s3, s3, 17\ns_add_u32 s2, s2, s3\nMvaa 124,252,140\ns_lshl2_add_u32 s3, s14, 3\nMvaa 92,252,132\ns_lshl_b32 s3, s3, 2\ns_lshr_b32 s3, 0xe420ca86, s3\n"
"Mvaa 92,248,128\nWL 0\nMaaa 16,224,192\nRa 128,10,32768\ns_and_b32 s3, s3, 15\ns_add_u32 m0, s47, 0\ns_add_u32 s46, s51, 524288\nD0 6,32,46,0\nMaaa 16,228,196\n"
"s_add_u32 s2, s2, s3\ns_cmp_eq_u32 s2, 32\ns_cselect_b32 s4, 16, s2\nMaaa 48,228,204\nRa 132,11,32768\ns_mul_i32 s78, s4, 65536\nMaaa 48,224,200\ns_lshr_b32 s3, s11, 1\n"
"Maaa 80,224,208\nRa 136,10,34816\ns_lshl_b32 s3, s3, 15\nMaaa 80,228,212\ns_add_u32 s2, s72, s3\nD0 7,32,46,1024\nMaaa 112,228,220\nRa 140,11,34816\ns_and_b32 s3, s11, 1\n"
"Maaa 112,224,216\ns_lshl_b32 s3, s3, 12\nMaaa 116,232,216\nRa 144,10,40960\ns_add_u32 s2, s2, s3\nMaaa 116,236,220\ns_add_u32 s2, s2, 0x0\nMaaa 84,236,212\nRa 148,11,40960\n"
"s_mov_b32 s55, 16384\nD0 8,32,46,2048\nMaaa 84,232,208\ns_add_u32 s75, s75, s2\nMaaa 52,232,200\nRa 152,10,43008\ns_add_u32 s76, s76, s2\nMaaa 52,236,204\ns_add_u32 s77, s77, s2\n"
"Maaa 20,236,196\nRa 156,11,43008\ns_add_u32 s78, s78, s2\nMaaa 20,232,192\nv_add_u32 v32, 0x10000, v12\nD0 9,32,46,3072\nMaaa 24,240,192\nv_add_u32 v33, 0x10000, v13\n"
"v_add_u32 v34, 0x10000, v14\nMaaa 24,244,196\nv_add_u32 v35, 0x10000, v15\nMaaa 56,244,204\nv_add_u32 v36, 0x10000, v16\nv_add_u32 v37, 0x10000, v17\ns_add_u32 m0, s47, 16384\n"
"s_add_u32 s46, s51, 786432\nMaaa 56,240,200\nv_add_u32 v38, 0x10000, v18\nMaaa 88,240,208\nv_add_u32 v39, 0x10000, v19\ns_add_u32 s2, s11, 1\nD0 6,32,46,0\nMaaa 88,244,212\n"
"s_lshr_b32 s2, s2, 1\nMaaa 120,244,220\ns_lshl_b32 s2, s2, 12\nv_add_u32 v44, s2, v10\nMaaa 120,240,216\nv_add_u32 v45, s2, v11\nMaaa 124,248,216\nv_xor_b32 v46, 0x1000, v44\n"
"v_xor_b32 v47, 0x1000, v45\nMaaa 124,252,220\ns_lshr_b32 s3, s11, 1\nD0 7,32,46,1024\nMaaa 92,252,212\ns_lshl_b32 s3, s3, 13\ns_lshl_b32 s2, s11, 12\nMaaa 92,248,208\n"
"s_add_u32 s2, s2, s3\nMaaa 60,248,200\ns_add_u32 s2, s2, s72\ns_lshl_b32 s3, s11, 12\nMaaa 60,252,204\ns_add_u32 s89, s3, 0x10000\nMaaa 28,252,196\ns_mov_b32 s88, 0xffff8000\n"
"s_lshl_b32 s4, s13, 1\nD0 8,32,46,2048\nMaaa 28,248,192\ns_waitcnt vmcnt(25) lgkmcnt(0)\ns_barrier\nMaaa 0,160,192\nTa 224,16,32768\ns_sub_u32 s79, 15, s4\nMaaa 0,164,196\n"
"Ta 226,16,34816\nMaaa 32,164,204\ns_add_u32 s96, s2, 0x1310000\ns_mov_b32 s63, 0x53800000\nMaaa 32,160,200\nTa 228,16,49152\nD0 9,32,46,3072\nMaaa 64,160,208\nTa 230,16,51200\n"
"s_mov_b32 s64, 0x2b800000\nMaaa 64,164,212\ns_sub_u32 s98, s4, 1\nMaaa 96,164,220\nTa 232,17,32768\ns_lshl_b32 s5, s13, 5\ns_add_u32 m0, s47, -32768\ns_add_u32 s46, s52, 128\n"
"Maaa 96,160,216\nTa 234,17,34816\nMaaa 100,168,216\ns_add_u32 s5, s5, s11\ns_lshl_b32 s65, s5, 9\nD0 40,28,46,0\nMaaa 100,172,220\nTa 236,17,49152\nMaaa 68,172,212\n"
"Ta 238,17,51200\ns_add_u32 s65, s65, s72\nMaaa 68,168,208\ns_add_u32 s65, s65, 0x1580000\nMaaa 36,168,200\nTa 240,18,32768\ns_and_b32 s66, s72, 0x2000\nMaaa 36,172,204\n"
"Ta 242,18,34816\nD0 41,28,46,1024\nMaaa 4,172,196\ns_add_u32 s66, s66, s56\ns_add_u32 s66, s66, 0x100000\nMaaa 4,168,192\nTa 244,18,49152\nMaaa 8,176,192\nTa 246,18,51200\n"
"s_and_b32 s5, s12, 1\nMaaa 8,180,196\ns_mul_i32 s58, s5, 0x202000\nMaaa 40,180,204\nTa 248,19,32768\ns_xor_b32 s58, s58, s72\nD0 42,28,46,2048\nMaaa 40,176,200\nTa 250,19,34816\n"
"Maaa 72,176,208\ns_add_u32 s58, s58, 0x15c0000\ns_lshl_b32 s5, s5, 8\nMaaa 72,180,212\nTa 252,19,49152\nMaaa 104,180,220\nTa 254,19,51200\ns_add_u32 s61, s58, s5\n"
"Maaa 104,176,216\ns_lshl4_add_u32 s60, s13, s61\nD0 43,28,46,3072\nMaaa 108,184,216\ns_add_u32 s54, s60, 128\ns_xor_b32 s60, s54, 0x100\nMaaa 108,188,220\ns_cmp_eq_u32 s11, 0\n"
"s_cselect_b32 s54, s54, 0x40000000\nMaaa 76,188,212\ns_getpc_b64 s[8:9]\n.PC293:\ns_add_u32 s8, s8, .HD275-.PC293\ns_addc_u32 s9, s9, 0\ns_add_u32 m0, s47, -49152\n"
"s_add_u32 s46, s52, 256\nMaaa 76,184,208\ns_mov_b32 s97, 0xff800000\nMaaa 44,184,200\ns_mov_b32 s90, 1\nD0 40,28,46,0\nMaaa 44,188,204\ns_cmp_lt_u32 s11, 2\n"
"s_cselect_b32 s92, 2, 0\nMaaa 12,188,196\ns_mul_i32 s4, s79, 65536\ns_sub_u32 s4, s2, s4\nMaaa 12,184,192\nWL 0\nMvaa 80,224,128\nTa 160,12,32768\ns_add_u32 s94, s4, 0x20000\n"
"Mvaa 80,228,132\nTa 162,12,34816\nD0 41,28,46,1024\nMvaa 84,236,132\ns_add_u32 s95, s4, 0x410000\ns_add_u32 s86, s2, 0x130000\nMvaa 84,232,128\nTa 164,12,49152\nMvaa 88,240,128\n"
"Ta 166,12,51200\ns_add_u32 s87, s2, 0x520000\nMvaa 88,244,132\ns_sub_u32 s99, s79, 2\ns_cmp_eq_u32 s13, 7\ns_cselect_b32 s93, 1.0, 0\nMvaa 92,252,132\nTa 168,13,32768\n"
"s_mov_b32 s36, s68\nD0 42,28,46,2048\nMvaa 92,248,128\nTa 170,13,34816\nMvaa 124,248,136\ns_and_b32 s37, s69, 0xffff\ns_mov_b32 s38, 0x1c0000\nMvaa 124,252,140\nTa 172,13,49152\n"
"CV 200,80,81\nMvaa 120,244,140\nTa 174,13,51200\ns_mov_b32 s39, 0x20000\nMvaa 120,240,136\ns_and_b32 s2, s74, 15\nD0 43,28,46,3072\nMvaa 116,232,136\nTa 176,14,32768\n"
"s_max_u32 s2, s2, 2\nMvaa 116,236,140\nTa 178,14,34816\nMvaa 112,228,140\ns_sub_u32 s2, s2, 2\ns_mul_i32 s3, s73, 14\nMvaa 112,224,136\nTa 180,14,49152\nCV 201,82,83\n"
"Mvaa 144,224,144\nTa 182,14,51200\ns_add_u32 s2, s2, s3\nMvaa 144,228,148\ns_lshl2_add_u32 s2, s2, s11\nMvaa 148,236,148\nTa 184,15,32768\ns_mul_i32 s3, s2, 4682\n"
"Mvaa 148,232,144\nTa 186,15,34816\nCV 202,84,85\nMvaa 152,240,144\ns_lshr_b32 s3, s3, 16\ns_mul_i32 s22, s3, 14\nMvaa 152,244,148\nTa 188,15,49152\nCV 203,86,87\nMvaa 156,252,148\n"
"Ta 190,15,51200\ns_sub_u32 s22, s2, s22\nMvaa 156,248,144\ns_and_b32 s21, s3, 28\nMvaa 188,248,152\ns_add_u32 s21, s21, s3\ns_lshl_b32 s21, s21, 4\nMvaa 188,252,156\n"
"s_bfe_u32 s41, s3, 0x30002\ns_cmp_gt_u32 s22, 6\ns_cselect_b32 s2, 1179648, 0\ns_cselect_b32 s1, 16, 0\nMvaa 184,244,156\ns_add_u32 s41, s41, s1\n"
"s_lshl4_add_u32 s41, s41, 0xc048480\nMvaa 184,240,152\ns_and_b32 s1, s3, 28\nCV 204,88,89\nMvaa 180,232,152\ns_add_u32 s3, s3, s1\ns_lshl_b32 s3, s3, 11\nMvaa 180,236,156\n"
"s_add_u32 s2, s2, s3\nMvaa 176,228,156\ns_lshl_b32 s3, s22, 17\ns_add_u32 s2, s2, s3\nMvaa 176,224,152\ns_waitcnt vmcnt(16) lgkmcnt(0)\ns_barrier\nMvaa 64,160,128\n"
"Ra 192,10,49152\ns_add_u32 s20, s2, 0x10e0000\ns_add_u32 m0, s47, 32768\ns_add_u32 s46, s51, 1048576\nMvaa 64,164,132\ns_lshl_b32 s2, s22, 7\nMvaa 68,172,132\nRa 196,11,49152\n"
"s_add_u32 s40, s2, 0xc04b800\nD0 6,32,46,0\nMvaa 68,168,128\ns_mul_i32 s2, s21, 896\nCV 205,90,91\nMvaa 72,176,128\nRa 200,10,51200\ns_lshl_b32 s3, s22, 6\nMvaa 72,180,132\n"
"s_add_u32 s22, s2, s3\nCV 206,92,93\nMvaa 76,188,132\nRa 204,11,51200\ns_add_u32 s23, s22, 57344\nMvaa 76,184,128\nD0 7,32,46,1024\nMvaa 108,184,136\nRa 208,10,57344\n"
"CV 207,94,95\nMvaa 108,188,140\nCV 220,120,121\nCV 221,122,123\nMvaa 104,180,140\nRa 212,11,57344\nMvaa 104,176,136\nCV 222,124,125\nCV 223,126,127\nMvaa 100,168,136\n"
"Ra 216,10,59392\nD0 8,32,46,2048\ns_lshl3_add_u32 s5, s12, s13\ns_lshl2_add_u32 s5, s5, s11\ns_lshl2_add_u32 s5, s5, 0xc04a400\nbuffer_load_dword v27, off, s[16:19], s5 sc0 sc1\n"
"Mvaa 100,172,140\nCV 216,112,113\nMvaa 96,164,140\nRa 220,11,59392\nCV 217,114,115\nMvaa 96,160,136\nCV 218,116,117\nCV 219,118,119\nMvaa 128,160,144\nCV 192,64,65\nCV 193,66,67\n"
"Mvaa 128,164,148\nD0 9,32,46,3072\nMvaa 132,172,148\nCV 194,68,69\nCV 195,70,71\nMvaa 132,168,144\nCV 196,72,73\nCV 197,74,75\nMvaa 136,176,144\ns_add_u32 m0, s47, 49152\n"
"s_add_u32 s46, s51, 1310720\nMvaa 136,180,148\nCV 198,76,77\nCV 199,78,79\nMvaa 140,188,148\nD0 6,32,46,0\nCV 212,104,105\nMvaa 140,184,144\nCV 213,106,107\nMvaa 172,184,152\n"
"CV 214,108,109\nCV 215,110,111\nMvaa 172,188,156\nCV 208,96,97\nCV 209,98,99\nMvaa 168,180,156\nCV 210,100,101\nMvaa 168,176,152\nD0 7,32,46,1024\nCV 211,102,103\n"
"Mvaa 164,168,152\nCV 232,144,145\nMvaa 164,172,156\nCV 233,146,147\nSv 192,1,16,75,0\nMvaa 160,164,156\nCV 234,148,149\nMvaa 160,160,152\nWL 0\nMaaa 0,160,192\nD0 8,32,46,2048\n"
"Ra 128,44,0\nMaaa 0,164,196\nCV 235,150,151\nMaaa 4,172,196\nRa 132,45,0\nMaaa 4,168,192\nCV 236,152,153\nMaaa 8,176,192\nRa 136,44,2048\nMaaa 8,180,196\nD0 9,32,46,3072\n"
"Maaa 12,188,196\nRa 140,45,2048\nSv 196,1,16,75,1024\nMaaa 12,184,192\nCV 237,154,155\nMaaa 44,184,200\nRa 144,46,0\nMaaa 44,188,204\nCV 238,156,157\nMaaa 40,180,204\nRa 148,47,0\n"
"Maaa 40,176,200\nCV 239,158,159\nMaaa 36,168,200\nRa 152,46,2048\nMaaa 36,172,204\nCV 252,184,185\nMaaa 32,164,204\nRa 156,47,2048\nMaaa 32,160,200\nCV 253,186,187\n"
"Sv 200,1,16,75,2048\nMaaa 64,160,208\nCV 254,188,189\nCV 255,190,191\nMaaa 64,164,212\nCV 248,176,177\nMaaa 68,172,212\nCV 249,178,179\nMaaa 68,168,208\nCV 250,180,181\n"
"Maaa 72,176,208\nCV 251,182,183\nMaaa 72,180,212\nCV 224,128,129\nMaaa 76,188,212\nCV 225,130,131\nMaaa 76,184,208\nCV 226,132,133\nMaaa 108,184,216\nCV 227,134,135\n"
"Sv 204,1,16,75,3072\nMaaa 108,188,220\nCV 228,136,137\nMaaa 104,180,220\nCV 229,138,139\nMaaa 104,176,216\nCV 230,140,141\nMaaa 100,168,216\nCV 231,142,143\nMaaa 100,172,220\n"
"CV 244,168,169\nMaaa 96,164,220\nCV 245,170,171\nMaaa 96,160,216\ns_barrier\nMaaa 16,224,192\ns_add_u32 m0, s47, -16384\ns_add_u32 s46, s52, 384\nTa 160,32,0\nMaaa 16,228,196\n"
"Ta 162,32,2048\nMaaa 20,236,196\nD0 40,28,46,0\ns_add_u32 s75, s75, s55\nSv 208,1,16,75,0\nMaaa 20,232,192\nTa 164,32,16384\nMaaa 24,240,192\nTa 166,32,18432\nMaaa 24,244,196\n"
"CV 246,172,173\nMaaa 28,252,196\nTa 168,33,0\nMaaa 28,248,192\nD0 41,28,46,1024\nTa 170,33,2048\nMaaa 60,248,200\nCV 247,174,175\nMaaa 60,252,204\nTa 172,33,16384\n"
"Maaa 56,244,204\nTa 174,33,18432\nMaaa 56,240,200\nCV 240,160,161\nSv 212,1,16,75,1024\nMaaa 52,232,200\nD0 42,28,46,2048\nTa 176,34,0\nMaaa 52,236,204\nTa 178,34,2048\n"
"Maaa 48,228,204\nCV 241,162,163\nMaaa 48,224,200\nTa 180,34,16384\nMaaa 80,224,208\nTa 182,34,18432\nCV 242,164,165\nMaaa 80,228,212\nD0 43,28,46,3072\nMaaa 84,236,212\n"
"Ta 184,35,0\nMaaa 84,232,208\nTa 186,35,2048\nMaaa 88,240,208\nCV 243,166,167\nSv 216,1,16,75,2048\nMaaa 88,244,212\nTa 188,35,16384\nMaaa 92,252,212\nTa 190,35,18432\n"
"Maaa 92,248,208\nMaaa 124,248,216\nMaaa 124,252,220\nMaaa 120,244,220\nMaaa 120,240,216\nMaaa 116,232,216\nMaaa 116,236,220\nSv 220,1,16,75,3072\nMaaa 112,228,220\n"
"Maaa 112,224,216\nWL 0\nZvaa 64,160,128\nTa 224,36,0\nAR 60,0\nMvaa 64,164,132\nTa 226,36,2048\nZvaa 96,164,140\nAR 61,1\nMvaa 96,160,136\nTa 228,36,16384\nZvaa 128,160,144\n"
"Ta 230,36,18432\nMvaa 128,164,148\nCV 52,60,61\nZvaa 160,164,156\nTa 232,37,0\nSv 224,1,16,76,0\nMvaa 160,160,152\nTa 234,37,2048\nZvaa 164,168,152\nAR 60,2\nMvaa 164,172,156\n"
"Ta 236,37,16384\nZvaa 132,172,148\nTa 238,37,18432\nMvaa 132,168,144\nAR 61,3\nZvaa 100,168,136\nTa 240,38,0\nMvaa 100,172,140\nTa 242,38,2048\nZvaa 68,172,132\n"
"Sv 228,1,16,76,1024\nMvaa 68,168,128\nTa 244,38,16384\nZvaa 72,176,128\nTa 246,38,18432\nCV 53,60,61\nMvaa 72,180,132\nAR 60,4\nZvaa 104,180,140\nTa 248,39,0\nMvaa 104,176,136\n"
"Ta 250,39,2048\nZvaa 136,176,144\nAR 61,5\nMvaa 136,180,148\nTa 252,39,16384\nZvaa 168,180,156\nTa 254,39,18432\nSv 232,1,16,76,2048\nMvaa 168,176,152\nCV 54,60,61\n"
"Zvaa 172,184,152\nAR 60,6\nMvaa 172,188,156\nAR 61,7\nZvaa 140,188,148\nCV 55,60,61\nMvaa 140,184,144\nAR 60,8\nZvaa 108,184,136\nAR 61,9\nMvaa 108,188,140\nSv 236,1,16,76,3072\n"
"Zvaa 76,188,132\nCV 56,60,61\nMvaa 76,184,128\ns_waitcnt vmcnt(29) lgkmcnt(0)\ns_barrier\nZvaa 80,224,128\nRa 192,44,32768\nAR 60,10\nMvaa 80,228,132\nRa 196,45,32768\n"
"s_add_u32 m0, s47, 65536\ns_add_u32 s46, s51, 1572864\nD0 6,32,46,0\nZvaa 112,228,140\nRa 200,44,34816\nMvaa 112,224,136\nRa 204,45,34816\nZvaa 144,224,144\nRa 208,46,32768\n"
"Mvaa 144,228,148\nRa 212,47,32768\nD0 7,32,46,1024\nZvaa 176,228,156\nRa 216,46,34816\nMvaa 176,224,152\nRa 220,47,34816\nZvaa 180,232,152\nTa 160,12,0\nMvaa 180,236,156\n"
"Ta 162,12,2048\nD0 8,32,46,2048\nZvaa 148,236,148\nTa 164,12,16384\nMvaa 148,232,144\nTa 166,12,18432\nZvaa 116,232,136\nTa 168,13,0\nMvaa 116,236,140\nTa 170,13,2048\n"
"D0 9,32,46,3072\nZvaa 84,236,132\nTa 172,13,16384\nMvaa 84,232,128\nTa 174,13,18432\ns_add_u32 m0, s47, 81920\ns_add_u32 s46, s51, 1835008\nZvaa 88,240,128\nTa 176,14,0\nAR 61,11\n"
"Mvaa 88,244,132\nTa 178,14,2048\nD0 6,32,46,0\nZvaa 120,244,140\nTa 180,14,16384\nMvaa 120,240,136\nTa 182,14,18432\nZvaa 152,240,144\nTa 184,15,0\nMvaa 152,244,148\n"
"Ta 186,15,2048\nD0 7,32,46,1024\nZvaa 184,244,156\nTa 188,15,16384\nMvaa 184,240,152\nTa 190,15,18432\nZvaa 188,248,152\nCV 57,60,61\nMvaa 188,252,156\nAR 60,12\nD0 8,32,46,2048\n"
"Zvaa 156,252,148\nAR 61,13\nMvaa 156,248,144\nCV 58,60,61\nZvaa 124,248,136\nAR 60,14\nMvaa 124,252,140\nAR 61,15\nD0 9,32,46,3072\nZvaa 92,252,132\nCV 59,60,61\nMvaa 92,248,128\n"
"WL 0\nMvaa 64,160,192\nTa 224,16,0\nAR 60,16\nMvaa 64,164,196\nTa 226,16,2048\ns_add_u32 m0, s47, -65536\ns_add_u32 s46, s52, 512\nD0 40,28,46,0\nMvaa 96,164,204\nAR 61,17\n"
"Mvaa 96,160,200\nTa 228,16,16384\nMvaa 128,160,208\nTa 230,16,18432\nMvaa 128,164,212\nCV 20,60,61\nD0 41,28,46,1024\nMvaa 160,164,220\nTa 232,17,0\nMvaa 160,160,216\n"
"Ta 234,17,2048\nMvaa 164,168,216\nAR 60,18\nMvaa 164,172,220\nTa 236,17,16384\nD0 42,28,46,2048\nMvaa 132,172,212\nTa 238,17,18432\nMvaa 132,168,208\nAR 61,19\nMvaa 100,168,200\n"
"Ta 240,18,0\nMvaa 100,172,204\nTa 242,18,2048\nD0 43,28,46,3072\nMvaa 68,172,196\ns_add_u32 s76, s76, s55\nSv 240,1,16,76,0\nMvaa 68,168,192\nTa 244,18,16384\nMvaa 72,176,192\n"
"Ta 246,18,18432\nCV 21,60,61\nMvaa 72,180,196\nAR 60,20\nMvaa 104,180,204\nTa 248,19,0\nMvaa 104,176,200\nTa 250,19,2048\nMvaa 136,176,208\nSv 244,1,16,76,1024\nMvaa 136,180,212\n"
"Ta 252,19,16384\nMvaa 168,180,220\nTa 254,19,18432\nMvaa 168,176,216\nAR 61,21\nMvaa 172,184,216\nCV 22,60,61\nMvaa 172,188,220\nAR 60,22\nMvaa 140,188,212\nSv 248,1,16,76,2048\n"
"Mvaa 140,184,208\nAR 61,23\nMvaa 108,184,200\nCV 23,60,61\nMvaa 108,188,204\nAR 60,24\nMvaa 76,188,196\nAR 61,25\nMvaa 76,184,192\ns_waitcnt vmcnt(19) lgkmcnt(0)\ns_barrier\n"
"Mvaa 80,224,192\nRa 128,44,16384\nCV 2,60,61\nv_cmp_ne_u32 vcc, s15, v27\ns_nop 0\ns_cbranch_vccnz .S294\n.S295:\nMvaa 80,228,196\nRa 132,45,16384\n"
"buffer_store_dword v25, off, s[16:19], s54 offset:0 sc0 sc1\ns_add_u32 m0, s47, 0\ns_add_u32 s46, s51, 2097152\nD0 6,32,46,0\nMvaa 112,228,204\nRa 136,44,18432\nMvaa 112,224,200\n"
"Ra 140,45,18432\nMvaa 144,224,208\nRa 144,46,16384\nMvaa 144,228,212\nRa 148,47,16384\nD0 7,32,46,1024\nMvaa 176,228,220\nRa 152,46,18432\nMvaa 176,224,216\nRa 156,47,18432\n"
"Mvaa 180,232,216\nTa 160,12,32768\nMvaa 180,236,220\nTa 162,12,34816\nD0 8,32,46,2048\nMvaa 148,236,212\nTa 164,12,49152\nMvaa 148,232,208\nTa 166,12,51200\nMvaa 116,232,200\n"
"Ta 168,13,32768\nMvaa 116,236,204\nTa 170,13,34816\nD0 9,32,46,3072\nMvaa 84,236,196\nTa 172,13,49152\nMvaa 84,232,192\nTa 174,13,51200\ns_add_u32 m0, s47, 16384\n"
"s_add_u32 s46, s51, 2359296\nMvaa 88,240,192\nTa 176,14,32768\nAR 60,26\nMvaa 88,244,196\nTa 178,14,34816\nD0 6,32,46,0\nMvaa 120,244,204\nTa 180,14,49152\nMvaa 120,240,200\n"
"Ta 182,14,51200\nMvaa 152,240,208\nTa 184,15,32768\nMvaa 152,244,212\nTa 186,15,34816\nD0 7,32,46,1024\nMvaa 184,244,220\nTa 188,15,49152\nMvaa 184,240,216\nTa 190,15,51200\n"
"Mvaa 188,248,216\nAR 61,27\nMvaa 188,252,220\nCV 3,60,61\nD0 8,32,46,2048\nMvaa 156,252,212\nAR 60,28\nMvaa 156,248,208\nAR 61,29\nMvaa 124,248,200\nCV 4,60,61\nMvaa 124,252,204\n"
"AR 60,30\nD0 9,32,46,3072\nMvaa 92,252,196\nAR 61,31\nMvaa 92,248,192\nWL 0\nMvaa 64,160,128\nTa 224,16,32768\nCV 5,60,61\nMvaa 64,164,132\nTa 226,16,34816\n"
"s_add_u32 m0, s47, -32768\ns_add_u32 s46, s52, 640\nD0 40,28,46,0\nMvaa 96,164,140\nMvaa 96,160,136\nTa 228,16,49152\nMvaa 128,160,144\nTa 230,16,51200\nMvaa 128,164,148\n"
"D0 41,28,46,1024\nMvaa 160,164,156\nTa 232,17,32768\nMvaa 160,160,152\nTa 234,17,34816\nMvaa 164,168,152\nMvaa 164,172,156\nTa 236,17,49152\nD0 42,28,46,2048\nMvaa 132,172,148\n"
"Ta 238,17,51200\nMvaa 132,168,144\nMvaa 100,168,136\nTa 240,18,32768\nMvaa 100,172,140\nTa 242,18,34816\nD0 43,28,46,3072\nMvaa 68,172,132\nSv 252,1,16,76,3072\nMvaa 68,168,128\n"
"Ta 244,18,49152\nMvaa 72,176,128\nTa 246,18,51200\nMvaa 72,180,132\nMvaa 104,180,140\nTa 248,19,32768\nMvaa 104,176,136\nTa 250,19,34816\nMvaa 136,176,144\nSv 52,1,16,77,0\n"
"Mvaa 136,180,148\nTa 252,19,49152\nMvaa 168,180,156\nTa 254,19,51200\nMvaa 168,176,152\nMvaa 172,184,152\nMvaa 172,188,156\nMvaa 140,188,148\nSv 56,1,16,77,1024\nMvaa 140,184,144\n"
"Mvaa 108,184,136\nMvaa 108,188,140\nMvaa 76,188,132\nMvaa 76,184,128\ns_waitcnt vmcnt(23) lgkmcnt(0)\ns_barrier\nMvaa 80,224,128\nRa 192,44,49152\nMvaa 80,228,132\n"
"Ra 196,45,49152\ns_add_u32 m0, s47, 32768\ns_add_u32 s46, s51, 2621440\nD0 6,32,46,0\nMvaa 112,228,140\nRa 200,44,51200\nMvaa 112,224,136\nRa 204,45,51200\nMvaa 144,224,144\n"
"Ra 208,46,49152\nMvaa 144,228,148\nRa 212,47,49152\nD0 7,32,46,1024\nMvaa 176,228,156\nRa 216,46,51200\nMvaa 176,224,152\nRa 220,47,51200\nMvaa 180,232,152\nTa 160,32,0\n"
"Mvaa 180,236,156\nTa 162,32,2048\nD0 8,32,46,2048\nMvaa 148,236,148\nTa 164,32,16384\nMvaa 148,232,144\nTa 166,32,18432\nMvaa 116,232,136\nTa 168,33,0\nMvaa 116,236,140\n"
"Ta 170,33,2048\nD0 9,32,46,3072\nMvaa 84,236,132\nTa 172,33,16384\nMvaa 84,232,128\nTa 174,33,18432\ns_add_u32 m0, s47, 49152\ns_add_u32 s46, s51, 2883584\nMvaa 88,240,128\n"
"Ta 176,34,0\nMvaa 88,244,132\nTa 178,34,2048\nD0 6,32,46,0\nMvaa 120,244,140\nTa 180,34,16384\nMvaa 120,240,136\nTa 182,34,18432\nMvaa 152,240,144\nTa 184,35,0\nMvaa 152,244,148\n"
"Ta 186,35,2048\nD0 7,32,46,1024\nMvaa 184,244,156\nTa 188,35,16384\nMvaa 184,240,152\nTa 190,35,18432\nMvaa 188,248,152\nMvaa 188,252,156\nD0 8,32,46,2048\nMvaa 156,252,148\n"
"Mvaa 156,248,144\nMvaa 124,248,136\nMvaa 124,252,140\nD0 9,32,46,3072\nMvaa 92,252,132\nMvaa 92,248,128\nWL 0\nMvaa 64,160,192\nTa 224,36,0\nMvaa 64,164,196\nTa 226,36,2048\n"
"s_add_u32 m0, s47, -49152\ns_add_u32 s46, s52, 768\nD0 40,28,46,0\nMvaa 96,164,204\nMvaa 96,160,200\nTa 228,36,16384\nMvaa 128,160,208\nTa 230,36,18432\nMvaa 128,164,212\n"
"D0 41,28,46,1024\nMvaa 160,164,220\nTa 232,37,0\nMvaa 160,160,216\nTa 234,37,2048\nMvaa 164,168,216\nMvaa 164,172,220\nTa 236,37,16384\nD0 42,28,46,2048\nMvaa 132,172,212\n"
"Ta 238,37,18432\nMvaa 132,168,208\nMvaa 100,168,200\nTa 240,38,0\nMvaa 100,172,204\nTa 242,38,2048\nD0 43,28,46,3072\nMvaa 68,172,196\nSv 20,1,16,77,2048\nMvaa 68,168,192\n"
"Ta 244,38,16384\nMvaa 72,176,192\nTa 246,38,18432\nMvaa 72,180,196\nMvaa 104,180,204\nTa 248,39,0\nMvaa 104,176,200\nTa 250,39,2048\nMvaa 136,176,208\nSv 2,1,16,77,3072\n"
"Mvaa 136,180,212\nTa 252,39,16384\nMvaa 168,180,220\nTa 254,39,18432\nAR 60,32\nAR 61,33\nCV 52,60,61\nAR 60,34\nAR 61,35\nCV 53,60,61\nAR 60,36\nAR 61,37\nCV 54,60,61\nAR 60,38\n"
"AR 61,39\nMvaa 168,176,216\nCV 55,60,61\nAR 60,40\nAR 61,41\nCV 56,60,61\nAR 60,42\nAR 61,43\nCV 57,60,61\nAR 60,44\nAR 61,45\nCV 58,60,61\nAR 60,46\nAR 61,47\nMvaa 172,184,216\n"
"CV 59,60,61\nAR 60,48\nAR 61,49\nCV 20,60,61\nAR 60,50\nAR 61,51\nCV 21,60,61\nAR 60,52\nAR 61,53\nCV 22,60,61\nAR 60,54\nAR 61,55\nCV 23,60,61\nMvaa 172,188,220\nAR 60,56\n"
"AR 61,57\nCV 2,60,61\nAR 60,58\nAR 61,59\nCV 3,60,61\nAR 60,60\nAR 61,61\nCV 4,60,61\nAR 60,62\nAR 61,63\nCV 5,60,61\nMvaa 140,188,212\ns_add_u32 s77, s77, s55\nSv 52,1,16,77,0\n"
"Mvaa 140,184,208\nAR 60,126\nAR 61,127\nMvaa 108,184,200\nCV 60,60,61\nAW 127,60\nMvaa 108,188,204\nAR 60,124\nMvaa 76,188,196\nAR 61,125\nCV 60,60,61\nMvaa 76,184,192\n"
"s_waitcnt vmcnt(17) lgkmcnt(0)\ns_barrier\nMvaa 80,224,192\nRa 128,44,0\nv_and_b32 v27, 0x70, v1\nbuffer_load_dword v27, v27, s[16:19], s61 offen offset:128 sc0 sc1\n"
"Mvaa 80,228,196\nRa 132,45,0\nbuffer_store_dword v25, off, s[16:19], s54 offset:8 sc0 sc1\ns_add_u32 m0, s47, 65536\ns_add_u32 s46, s51, 3145728\nD0 6,32,46,0\nMvaa 112,228,204\n"
"Ra 136,44,2048\nAW 126,60\nMvaa 112,224,200\nRa 140,45,2048\nAR 60,122\nMvaa 144,224,208\nRa 144,46,0\nAR 61,123\nMvaa 144,228,212\nRa 148,47,0\nD0 7,32,46,1024\nMvaa 176,228,220\n"
"Ra 152,46,2048\nCV 60,60,61\nMvaa 176,224,216\nRa 156,47,2048\nAW 125,60\nMvaa 180,232,216\nTa 160,12,0\nAR 60,120\nMvaa 180,236,220\nTa 162,12,2048\nD0 8,32,46,2048\n"
"Mvaa 148,236,212\nTa 164,12,16384\nAR 61,121\nMvaa 148,232,208\nTa 166,12,18432\nCV 60,60,61\nMvaa 116,232,200\nTa 168,13,0\nAW 124,60\nMvaa 116,236,204\nTa 170,13,2048\n"
"D0 9,32,46,3072\nMvaa 84,236,196\nTa 172,13,16384\nAR 60,118\nMvaa 84,232,192\nTa 174,13,18432\ns_add_u32 m0, s47, 81920\ns_add_u32 s46, s51, 3407872\nMvaa 88,240,192\n"
"Ta 176,14,0\nAR 61,119\nMvaa 88,244,196\nTa 178,14,2048\nD0 6,32,46,0\nMvaa 120,244,204\nTa 180,14,16384\nCV 60,60,61\nMvaa 120,240,200\nTa 182,14,18432\nAW 123,60\n"
"Mvaa 152,240,208\nTa 184,15,0\nAR 60,116\nMvaa 152,244,212\nTa 186,15,2048\nD0 7,32,46,1024\nMvaa 184,244,220\nTa 188,15,16384\nAR 61,117\nMvaa 184,240,216\nTa 190,15,18432\n"
"CV 60,60,61\nMvaa 188,248,216\nAW 122,60\nAR 60,114\nMvaa 188,252,220\nD0 8,32,46,2048\nMvaa 156,252,212\nAR 61,115\nCV 60,60,61\nMvaa 156,248,208\nAW 121,60\nAR 60,112\n"
"Mvaa 124,248,200\nAR 61,113\nCV 60,60,61\nMvaa 124,252,204\nD0 9,32,46,3072\nMvaa 92,252,196\nAW 120,60\nAR 60,110\nMvaa 92,248,192\nWL 0\nMvaa 64,160,128\nTa 224,16,0\nAR 61,111\n"
"Mvaa 64,164,132\nTa 226,16,2048\ns_add_u32 s6, s56, 0\nbuffer_load_dwordx4 v[192:195], v1, s[16:19], s6 offen offset:0 sc1\nMvaa 96,164,140\nCV 60,60,61\nAW 119,60\n"
"Mvaa 96,160,136\nTa 228,16,16384\nAR 60,108\nMvaa 128,160,144\nTa 230,16,18432\nAR 61,109\nMvaa 128,164,148\n"
"buffer_load_dwordx4 v[196:199], v1, s[16:19], s6 offen offset:1024 sc1\nMvaa 160,164,156\nTa 232,17,0\nCV 60,60,61\nMvaa 160,160,152\nTa 234,17,2048\nAW 118,60\nMvaa 164,168,152\n"
"AR 60,106\nAR 61,107\nMvaa 164,172,156\nTa 236,17,16384\nbuffer_load_dwordx4 v[200:203], v1, s[16:19], s6 offen offset:2048 sc1\nMvaa 132,172,148\nTa 238,17,18432\nCV 60,60,61\n"
"Mvaa 132,168,144\nAW 117,60\nAR 60,104\nMvaa 100,168,136\nTa 240,18,0\nAR 61,105\nMvaa 100,172,140\nTa 242,18,2048\n"
"buffer_load_dwordx4 v[204:207], v1, s[16:19], s6 offen offset:3072 sc1\nMvaa 68,172,132\nSv 56,1,16,77,1024\nCV 60,60,61\nMvaa 68,168,128\nTa 244,18,16384\nAW 116,60\n"
"Mvaa 72,176,128\nTa 246,18,18432\nAR 60,102\nMvaa 72,180,132\nAR 61,103\nMvaa 104,180,140\nTa 248,19,0\nCV 60,60,61\nMvaa 104,176,136\nTa 250,19,2048\nAW 115,60\nMvaa 136,176,144\n"
"Sv 20,1,16,77,2048\nAR 60,100\nMvaa 136,180,148\nTa 252,19,16384\nMvaa 168,180,156\nTa 254,19,18432\nAR 61,101\nMvaa 168,176,152\nCV 60,60,61\nAW 114,60\nMvaa 172,184,152\n"
"AR 60,98\nAR 61,99\nMvaa 172,188,156\nCV 60,60,61\nMvaa 140,188,148\nSv 2,1,16,77,3072\nAW 113,60\nMvaa 140,184,144\nAR 60,96\nAR 61,97\nMvaa 108,184,136\nCV 60,60,61\nAW 112,60\n"
"Mvaa 108,188,140\nAR 60,94\nMvaa 76,188,132\nAR 61,95\nCV 60,60,61\nMvaa 76,184,128\ns_waitcnt vmcnt(16) lgkmcnt(0)\ns_barrier\nMvaa 80,224,128\nRa 192,44,32768\n"
"v_cmp_ne_u32 vcc, s15, v27\ns_nop 0\ns_cbranch_vccnz .S297\n.S298:\ns_sub_u32 s45, s86, 131072\ns_cmp_eq_u32 s79, 1\ns_cselect_b32 s86, s94, s86\ns_add_u32 m0, s47, -65536\n"
"s_add_u32 s46, s45, 0x0\nMvaa 80,228,132\nRa 196,45,32768\nMvaa 112,228,140\nRa 200,44,34816\nD1 1,16,46,0\nMvaa 112,224,136\nRa 204,45,34816\nAW 111,60\nMvaa 144,224,144\n"
"Ra 208,46,32768\nAR 60,92\nMvaa 144,228,148\nRa 212,47,32768\nMvaa 176,228,156\nRa 216,46,34816\nD1 1,16,46,1024\nMvaa 176,224,152\nRa 220,47,34816\nAR 61,93\nMvaa 180,232,152\n"
"Ta 160,12,32768\nCV 60,60,61\nMvaa 180,236,156\nTa 162,12,34816\nMvaa 148,236,148\nTa 164,12,49152\nD1 1,16,46,2048\nMvaa 148,232,144\nTa 166,12,51200\nAW 110,60\n"
"Mvaa 116,232,136\nTa 168,13,32768\nAR 60,90\nMvaa 116,236,140\nTa 170,13,34816\nMvaa 84,236,132\nTa 172,13,49152\nD1 1,16,46,3072\nMvaa 84,232,128\nTa 174,13,51200\nAR 61,91\n"
"Mvaa 88,240,128\nTa 176,14,32768\ns_add_u32 s6, s56, 4096\nMvaa 88,244,132\nTa 178,14,34816\nMvaa 120,244,140\nTa 180,14,49152\n"
"buffer_load_dwordx4 v[208:211], v1, s[16:19], s6 offen offset:0 sc1\nMvaa 120,240,136\nTa 182,14,51200\nCV 60,60,61\nMvaa 152,240,144\nTa 184,15,32768\nAW 109,60\n"
"Mvaa 152,244,148\nTa 186,15,34816\nMvaa 184,244,156\nTa 188,15,49152\nbuffer_load_dwordx4 v[212:215], v1, s[16:19], s6 offen offset:1024 sc1\nMvaa 184,240,152\nTa 190,15,51200\n"
"AR 60,88\nMvaa 188,248,152\nAR 61,89\nCV 60,60,61\nMvaa 188,252,156\nAW 108,60\nMvaa 156,252,148\nbuffer_load_dwordx4 v[216:219], v1, s[16:19], s6 offen offset:2048 sc1\nAR 60,86\n"
"Mvaa 156,248,144\nAR 61,87\nCV 60,60,61\nMvaa 124,248,136\nAW 107,60\nAR 60,84\nMvaa 124,252,140\nAR 61,85\nMvaa 92,252,132\n"
"buffer_load_dwordx4 v[220:223], v1, s[16:19], s6 offen offset:3072 sc1\nCV 60,60,61\nMvaa 92,248,128\nWL 0\nMvaa 64,160,192\nTa 224,16,32768\ns_add_u32 s6, s56, 16384\n"
"Mvaa 64,164,196\nTa 226,16,34816\nMvaa 96,164,204\nbuffer_load_dwordx4 v[224:227], v1, s[16:19], s6 offen offset:0 sc1\nAW 106,60\nMvaa 96,160,200\nTa 228,16,49152\nAR 60,82\n"
"Mvaa 128,160,208\nTa 230,16,51200\nAR 61,83\nMvaa 128,164,212\nCV 60,60,61\nMvaa 160,164,220\nTa 232,17,32768\n"
"buffer_load_dwordx4 v[228:231], v1, s[16:19], s6 offen offset:1024 sc1\nMvaa 160,160,216\nTa 234,17,34816\nAW 105,60\nMvaa 164,168,216\nAR 60,80\nAR 61,81\nMvaa 164,172,220\n"
"Ta 236,17,49152\nMvaa 132,172,212\nTa 238,17,51200\nbuffer_load_dwordx4 v[232:235], v1, s[16:19], s6 offen offset:2048 sc1\nMvaa 132,168,208\nCV 60,60,61\nAW 104,60\n"
"Mvaa 100,168,200\nTa 240,18,32768\nAR 60,78\nMvaa 100,172,204\nTa 242,18,34816\nMvaa 68,172,196\nbuffer_load_dwordx4 v[236:239], v1, s[16:19], s6 offen offset:3072 sc1\nAR 61,79\n"
"Mvaa 68,168,192\nTa 244,18,49152\nCV 60,60,61\nMvaa 72,176,192\nTa 246,18,51200\nAW 103,60\nMvaa 72,180,196\nAR 60,76\nMvaa 104,180,204\nTa 248,19,32768\nAR 61,77\n"
"Mvaa 104,176,200\nTa 250,19,34816\nCV 60,60,61\nMvaa 136,176,208\nAW 102,60\nAR 60,74\nMvaa 136,180,212\nTa 252,19,49152\nMvaa 168,180,220\nTa 254,19,51200\nAR 61,75\n"
"Mvaa 168,176,216\nCV 60,60,61\nAW 101,60\nMvaa 172,184,216\nAR 60,72\nAR 61,73\nMvaa 172,188,220\nCV 60,60,61\nMvaa 140,188,212\nAW 100,60\nAR 60,70\nMvaa 140,184,208\nAR 61,71\n"
"CV 60,60,61\nMvaa 108,184,200\nAW 99,60\nAR 60,68\nMvaa 108,188,204\nAR 61,69\nMvaa 76,188,196\nCV 60,60,61\nAW 98,60\nMvaa 76,184,192\ns_waitcnt vmcnt(19) lgkmcnt(0)\ns_barrier\n"
"Mvaa 80,224,192\nRa 128,44,16384\nAR 60,66\nMvaa 80,228,196\nRa 132,45,16384\ns_add_u32 s6, s56, 20480\nbuffer_load_dwordx4 v[240:243], v1, s[16:19], s6 offen offset:0 sc1\n"
"Mvaa 112,228,204\nRa 136,44,18432\nAR 61,67\nMvaa 112,224,200\nRa 140,45,18432\nCV 60,60,61\nMvaa 144,224,208\nRa 144,46,16384\nAW 97,60\nMvaa 144,228,212\nRa 148,47,16384\n"
"buffer_load_dwordx4 v[244:247], v1, s[16:19], s6 offen offset:1024 sc1\nMvaa 176,228,220\nRa 152,46,18432\nAR 60,64\nMvaa 176,224,216\nRa 156,47,18432\nAR 61,65\nMvaa 180,232,216\n"
"Ta 160,32,0\nCV 60,60,61\nMvaa 180,236,220\nTa 162,32,2048\nbuffer_load_dwordx4 v[248:251], v1, s[16:19], s6 offen offset:2048 sc1\nMvaa 148,236,212\nTa 164,32,16384\nAW 96,60\n"
"Mvaa 148,232,208\nTa 166,32,18432\nv_add_u32 v2, 0x0, v1\ns_lshr_b32 s2, s59, 1\nMvaa 116,232,200\nTa 168,33,0\nv_add_u32 v24, s2, v2\nMvaa 116,236,204\nTa 170,33,2048\n"
"buffer_load_dwordx4 v[252:255], v1, s[16:19], s6 offen offset:3072 sc1\nMvaa 84,236,196\nTa 172,33,16384\nv_xor_b32 v26, 0x1000, v24\nMvaa 84,232,192\nTa 174,33,18432\n"
"s_add_u32 m0, s47, -32768\ns_add_u32 s46, s45, 0x8000\nMvaa 88,240,192\nTa 176,34,0\nv_lshrrev_b32 v20, 4, v0\nv_lshlrev_b32 v20, 2, v20\nMvaa 88,244,196\nTa 178,34,2048\n"
"D1 1,16,46,0\nMvaa 120,244,204\nTa 180,34,16384\nv_and_b32 v0, 15, v0\nMvaa 120,240,200\nTa 182,34,18432\nv_sub_u32 v0, v0, v20\nMvaa 152,240,208\nTa 184,35,0\n"
"s_lshl_b32 s2, s11, 4\nMvaa 152,244,212\nTa 186,35,2048\nD1 1,16,46,1024\nMvaa 184,244,220\nTa 188,35,16384\nv_add_u32 v3, s2, v0\nv_subrev_u32 v0, 32, v3\nMvaa 184,240,216\n"
"Ta 190,35,18432\ns_lshl_b32 s2, s11, 13\nMvaa 188,248,216\ns_add_u32 s2, s2, 0x20000\nv_add_u32 v22, s2, v1\nMvaa 188,252,220\nD1 1,16,46,2048\nv_xor_b32 v23, 0x2000, v22\n"
"Mvaa 156,252,212\nv_mov_b32 v4, 0\nv_mov_b32 v5, 0\nMvaa 156,248,208\nv_mov_b32 v6, 0\nv_mov_b32 v7, 0\nv_mov_b32 v8, 0\nMvaa 124,248,200\nv_mov_b32 v9, 0\nv_mov_b32 v10, 0\n"
"Mvaa 124,252,204\nD1 1,16,46,3072\nv_mov_b32 v11, 0\nMvaa 92,252,196\nv_mov_b32 v12, 0\nv_mov_b32 v13, 0\nMvaa 92,248,192\nWL 0\nMvaa 64,160,128\nTa 224,36,0\nv_mov_b32 v14, 0\n"
"v_mov_b32 v15, 0\nMvaa 64,164,132\nTa 226,36,2048\ns_add_u32 m0, s47, -16384\ns_add_u32 s46, s45, 0x400000\nD1 1,16,46,0\nMvaa 68,172,132\nv_mov_b32 v16, 0\nv_mov_b32 v17, 0\n"
"Mvaa 68,168,128\nTa 228,36,16384\nv_mov_b32 v18, 0\nMvaa 72,176,128\nTa 230,36,18432\nv_mov_b32 v19, 0\nMvaa 72,180,132\nD1 1,16,46,1024\nMvaa 76,188,132\nTa 232,37,0\n"
"Mvaa 76,184,128\nTa 234,37,2048\nMvaa 108,184,136\nv_mul_f32 v64, s57, v64\nv_mul_f32 v65, s57, v65\nMvaa 108,188,140\nTa 236,37,16384\nD1 1,16,46,2048\nMvaa 104,180,140\n"
"Ta 238,37,18432\nCV 64,64,65\nMvaa 104,176,136\nv_mul_f32 v66, s57, v66\nv_mul_f32 v67, s57, v67\nMvaa 100,168,136\nTa 240,38,0\nCV 65,66,67\nMvaa 100,172,140\nTa 242,38,2048\n"
"D1 1,16,46,3072\nMvaa 96,164,140\nv_mul_f32 v68, s57, v68\nv_mul_f32 v69, s57, v69\nMvaa 96,160,136\nTa 244,38,16384\nCV 66,68,69\nMvaa 128,160,144\nTa 246,38,18432\n"
"v_mul_f32 v70, s57, v70\nMvaa 128,164,148\nv_mul_f32 v71, s57, v71\nCV 67,70,71\nMvaa 132,172,148\nTa 248,39,0\nAW 192,64\nMvaa 132,168,144\nTa 250,39,2048\nAW 193,65\n"
"Mvaa 136,176,144\nAW 194,66\nAW 195,67\nMvaa 136,180,148\nTa 252,39,16384\nv_mul_f32 v72, s57, v72\nMvaa 140,188,148\nTa 254,39,18432\nv_mul_f32 v73, s57, v73\nMvaa 140,184,144\n"
"CV 72,72,73\nv_mul_f32 v74, s57, v74\nMvaa 172,184,152\nv_mul_f32 v75, s57, v75\nCV 73,74,75\nMvaa 172,188,156\nv_mul_f32 v76, s57, v76\nv_mul_f32 v77, s57, v77\nMvaa 168,180,156\n"
"CV 74,76,77\nv_mul_f32 v78, s57, v78\nMvaa 168,176,152\nv_mul_f32 v79, s57, v79\nCV 75,78,79\nMvaa 164,168,152\nAW 200,72\nAW 201,73\nMvaa 164,172,156\nAW 202,74\nAW 203,75\n"
"Mvaa 160,164,156\nv_mul_f32 v96, s57, v96\nv_mul_f32 v97, s57, v97\nMvaa 160,160,152\ns_waitcnt vmcnt(20) lgkmcnt(0)\ns_barrier\nMvaa 80,224,128\nCV 96,96,97\n"
"v_mul_f32 v98, s57, v98\nMvaa 80,228,132\nbuffer_store_dword v25, off, s[16:19], s54 offset:12 sc0 sc1\ns_add_u32 m0, s47, 0\ns_add_u32 s46, s86, 0xffff0000\nD1 1,16,46,0\n"
"Mvaa 84,236,132\nv_mul_f32 v99, s57, v99\nCV 97,98,99\nMvaa 84,232,128\nv_mul_f32 v100, s57, v100\nv_mul_f32 v101, s57, v101\nMvaa 88,240,128\nCV 98,100,101\n"
"v_mul_f32 v102, s57, v102\nMvaa 88,244,132\nD1 1,16,46,1024\nv_mul_f32 v103, s57, v103\nMvaa 92,252,132\nCV 99,102,103\nAW 196,96\nMvaa 92,248,128\nAW 197,97\nAW 198,98\n"
"Mvaa 124,248,136\nAW 199,99\nv_mul_f32 v128, s57, v128\nMvaa 124,252,140\nD1 1,16,46,2048\nv_mul_f32 v129, s57, v129\nMvaa 120,244,140\nCV 128,128,129\nv_mul_f32 v130, s57, v130\n"
"Mvaa 120,240,136\nv_mul_f32 v131, s57, v131\nCV 129,130,131\nMvaa 116,232,136\nRa 160,24,0\nv_mul_f32 v132, s57, v132\nMvaa 116,236,140\nD1 1,16,46,3072\n"
"v_mul_f32 v133, s57, v133\nMvaa 112,228,140\nRa 164,24,1024\nCV 130,132,133\nMvaa 112,224,136\ns_add_u32 m0, s47, 16384\ns_add_u32 s46, s45, 0x408000\nMvaa 144,224,144\n"
"Ra 168,24,2048\nv_mul_f32 v134, s57, v134\nMvaa 144,228,148\nD1 1,16,46,0\nv_mul_f32 v135, s57, v135\nMvaa 148,236,148\nRa 172,24,3072\nCV 131,134,135\nMvaa 148,232,144\n"
"ds_write_b128 v23, v[128:131] offset:0\nv_mul_f32 v80, s57, v80\nMvaa 152,240,144\nRa 176,26,0\nv_mul_f32 v81, s57, v81\nMvaa 152,244,148\nD1 1,16,46,1024\nCV 80,80,81\n"
"Mvaa 156,252,148\nRa 180,26,1024\nv_mul_f32 v82, s57, v82\nMvaa 156,248,144\nv_mul_f32 v83, s57, v83\nCV 81,82,83\nMvaa 188,248,152\nRa 184,26,2048\nv_mul_f32 v84, s57, v84\n"
"Mvaa 188,252,156\nD1 1,16,46,2048\nv_mul_f32 v85, s57, v85\nMvaa 184,244,156\nRa 188,26,3072\nCV 82,84,85\nMvaa 184,240,152\nv_mul_f32 v86, s57, v86\nv_mul_f32 v87, s57, v87\n"
"Mvaa 180,232,152\nCV 83,86,87\nAW 208,80\nMvaa 180,236,156\nD1 1,16,46,3072\nAW 209,81\nMvaa 176,228,156\nAW 210,82\nAW 211,83\nMvaa 176,224,152\ns_waitcnt vmcnt(25) lgkmcnt(5)\n"
"s_cmp_eq_u32 s79, 1\ns_cbranch_scc1 .E300\n.E301:\nCvav 32,160,192,4\nRa 128,24,8192\nv_mul_f32 v88, s57, v88\nMvav 32,164,200\nv_mul_f32 v89, s57, v89\nCV 88,88,89\n"
"Mvav 32,168,208\nRa 132,24,9216\nv_mul_f32 v90, s57, v90\nMvav 32,172,216\nv_mul_f32 v91, s57, v91\nCV 89,90,91\nCvav 36,172,220,8\nRa 136,24,10240\nv_mul_f32 v92, s57, v92\n"
"Mvav 36,168,212\ns_add_u32 m0, s89, 32768\nv_mul_f32 v93, s57, v93\nMvav 36,164,204\nRa 140,24,11264\ns_waitcnt vmcnt(17) lgkmcnt(4)\ns_barrier\nMvav 36,160,196\nCV 90,92,93\n"
"v_mul_f32 v94, s57, v94\nMvav 36,176,228\nRa 144,26,8192\nSa 96,1,16,78,0\nMvav 36,180,236\ns_add_u32 s46, s86, s88\nv_mul_f32 v95, s57, v95\nMvav 36,184,244\nRa 148,26,9216\n"
"CV 91,94,95\nMvav 36,188,252\nAW 216,88\nAW 217,89\nMvav 32,188,248\nRa 152,26,10240\nAW 218,90\nMvav 32,184,240\nD1 1,16,46,0\nAW 219,91\nMvav 32,180,232\nRa 156,26,11264\nWL 4\n"
"Mvav 32,176,224\nv_mov_b32 v86, 0\nv_mov_b32 v87, 0\nCvav 40,128,192,12\nSa 100,1,16,78,1024\nv_mov_b32 v88, 0\nMvav 40,132,200\ns_cmp_eq_u32 s99, -1\nMvav 40,136,208\n"
"s_cselect_b32 s87, s95, s87\nv_mov_b32 v89, 0\nv_mul_f32 v136, s57, v136\nv_mul_f32 v137, s57, v137\nCV 136,136,137\nv_mul_f32 v138, s57, v138\nv_mul_f32 v139, s57, v139\n"
"Mvav 40,140,216\nCV 137,138,139\nv_mul_f32 v140, s57, v140\nv_mul_f32 v141, s57, v141\nCV 138,140,141\nv_mul_f32 v142, s57, v142\nv_mul_f32 v143, s57, v143\nCvav 44,140,220,16\n"
"v_and_b32 v27, 0x70, v1\nbuffer_load_dword v27, v27, s[16:19], s61 offen offset:136 sc0 sc1\nCV 139,142,143\nds_write_b128 v23, v[136:139] offset:2048\nv_mul_f32 v144, s57, v144\n"
"v_mul_f32 v145, s57, v145\nCV 144,144,145\nMvav 44,136,212\nD1 1,16,46,1024\nv_mul_f32 v146, s57, v146\nv_mul_f32 v147, s57, v147\nCV 145,146,147\nv_mul_f32 v148, s57, v148\n"
"v_mul_f32 v149, s57, v149\nMvav 44,132,204\nWL 1\nCV 146,148,149\nv_mul_f32 v150, s57, v150\nv_mul_f32 v151, s57, v151\nCV 147,150,151\nds_write_b128 v23, v[144:147] offset:4096\n"
"v_mul_f32 v152, s57, v152\nMvav 44,128,196\nv_mul_f32 v153, s57, v153\nCV 152,152,153\nv_mul_f32 v154, s57, v154\nv_mul_f32 v155, s57, v155\nCV 153,154,155\n"
"v_mul_f32 v156, s57, v156\nMvav 44,144,228\nSa 104,1,16,78,2048\nv_mul_f32 v157, s57, v157\nCV 154,156,157\nv_mul_f32 v158, s57, v158\nv_mul_f32 v159, s57, v159\nCV 155,158,159\n"
"ds_write_b128 v23, v[152:155] offset:6144\nMvav 44,148,236\ns_add_u32 s88, s88, 32768\nEX 80,32\nv_mul_f32 v160, s57, v160\nv_mul_f32 v161, s57, v161\nCV 160,160,161\n"
"Mvav 44,152,244\nv_mul_f32 v162, s57, v162\nv_mul_f32 v163, s57, v163\nCV 161,162,163\nv_mul_f32 v164, s57, v164\nv_mul_f32 v165, s57, v165\nCV 162,164,165\n"
"v_mul_f32 v166, s57, v166\nMvav 44,156,252\nEX 81,33\nv_mul_f32 v167, s57, v167\nCV 163,166,167\nds_write_b128 v23, v[160:163] offset:1024\nMvav 40,156,248\n"
"v_mul_f32 v168, s57, v168\nv_mul_f32 v169, s57, v169\nCV 168,168,169\nv_mul_f32 v170, s57, v170\nv_mul_f32 v171, s57, v171\nCV 169,170,171\nv_mul_f32 v172, s57, v172\n"
"Mvav 40,152,240\nD1 1,16,46,2048\nv_mul_f32 v173, s57, v173\nCV 170,172,173\nv_mul_f32 v174, s57, v174\nv_mul_f32 v175, s57, v175\nCV 171,174,175\nMvav 40,148,232\n"
"ds_write_b128 v23, v[168:171] offset:3072\nv_mul_f32 v176, s57, v176\nv_mul_f32 v177, s57, v177\nCV 176,176,177\nv_mul_f32 v178, s57, v178\nv_mul_f32 v179, s57, v179\n"
"CV 177,178,179\nMvav 40,144,224\nAD 84,80,81\nCV 64,80,81\nv_mul_f32 v180, s57, v180\nv_mul_f32 v181, s57, v181\nCV 178,180,181\nv_mul_f32 v182, s57, v182\nSa 108,1,16,78,3072\n"
"v_mul_f32 v183, s57, v183\nCV 179,182,183\nds_write_b128 v23, v[176:179] offset:5120\nv_mul_f32 v184, s57, v184\nv_mul_f32 v185, s57, v185\nCV 184,184,185\nEX 82,34\n"
"v_mul_f32 v186, s57, v186\nv_mul_f32 v187, s57, v187\nCV 185,186,187\nv_mul_f32 v188, s57, v188\nv_mul_f32 v189, s57, v189\nCV 186,188,189\nv_mul_f32 v190, s57, v190\n"
"v_mul_f32 v191, s57, v191\nCV 187,190,191\nds_write_b128 v23, v[184:187] offset:7168\ns_waitcnt vmcnt(17) lgkmcnt(0)\nEX 83,35\nAD 84,84,82\nD1 1,16,46,3072\n"
"s_add_u32 m0, s89, 49152\ns_barrier\nAD 84,84,83\nCV 65,82,83\nRa 128,24,32768\nRa 224,22,0\nRa 228,22,1024\ns_add_u32 s46, s87, s88\nEX 80,36\nRa 132,24,33792\nRa 232,22,2048\n"
"Ra 236,22,3072\nRa 240,22,4096\nEX 81,37\nRa 136,24,34816\nRa 244,22,5120\nRa 248,22,6144\nRa 252,22,7168\nD1 1,16,46,0\nv_mul_f32 v104, s57, v104\nv_mul_f32 v105, s57, v105\n"
"Ra 140,24,35840\nCV 104,104,105\nv_mul_f32 v106, s57, v106\nv_mul_f32 v107, s57, v107\nAD 85,80,81\nCV 68,80,81\nCV 105,106,107\nRa 144,26,32768\nv_mul_f32 v108, s57, v108\n"
"v_mul_f32 v109, s57, v109\nCV 106,108,109\ns_cmp_eq_u32 s99, -1\nEX 82,38\nRa 148,26,33792\ns_cbranch_scc1 .E283\n.E284:\nv_mul_f32 v110, s57, v110\nEX 83,39\n"
"v_mul_f32 v111, s57, v111\nRa 152,26,34816\nAD 85,85,82\nCV 107,110,111\nD1 1,16,46,1024\nAW 204,104\nAW 205,105\nAW 206,106\nRa 156,26,35840\nAW 207,107\n"
"v_mul_f32 v112, s57, v112\nAD 85,85,83\nCV 69,82,83\nv_mul_f32 v113, s57, v113\nCV 112,112,113\nRa 160,24,40960\nv_mul_f32 v114, s57, v114\nv_mul_f32 v115, s57, v115\n"
"s_xor_b32 s89, s89, 0x10000\nEX 80,40\nRa 164,24,41984\nCV 113,114,115\nv_mul_f32 v116, s57, v116\nv_mul_f32 v117, s57, v117\nWL 6\nEX 81,41\nRa 168,24,43008\nAD 84,84,80\n"
"CV 114,116,117\nv_mul_f32 v118, s57, v118\nD1 1,16,46,2048\nv_mul_f32 v119, s57, v119\nCV 115,118,119\nRa 172,24,44032\nAW 212,112\nAW 213,113\nAW 214,114\nAD 84,84,81\n"
"CV 66,80,81\nAW 215,115\nCvav 48,128,192,4\nRa 176,26,40960\nv_mul_f32 v120, s57, v120\nv_mul_f32 v121, s57, v121\nCV 120,120,121\nMvav 48,132,200\ns_cmp_eq_u32 s99, 0\nEX 82,42\n"
"Mvav 48,136,208\nRa 180,26,41984\ns_cselect_b32 s86, s94, s86\nv_mul_f32 v122, s57, v122\nMvav 48,140,216\nWL 6\nEX 83,43\nWV 8\nv_cmp_ne_u32 vcc, s15, v27\ns_nop 0\n"
"s_cbranch_vccnz .S302\n.S303:\nCvav 52,140,220,8\nRa 184,26,43008\nAD 84,84,82\nv_mul_f32 v123, s57, v123\nMvav 52,136,212\nD1 1,16,46,3072\ns_add_u32 m0, s89, 0\nCV 121,122,123\n"
"v_mul_f32 v124, s57, v124\nMvav 52,132,204\nRa 188,26,44032\nv_mul_f32 v125, s57, v125\nCV 122,124,125\nMvav 52,128,196\nAD 84,84,83\nCV 67,82,83\nv_mul_f32 v126, s57, v126\n"
"v_mul_f32 v127, s57, v127\nMvav 52,144,228\nRa 128,2,49152\ns_add_u32 s78, s78, s55\nSa 112,1,16,78,0\nMvav 52,148,236\ns_add_u32 s46, s86, s88\nEX 80,44\nMvav 52,152,244\n"
"Ra 132,2,50176\nv_xor_b32 v24, 0x10000, v24\nCV 123,126,127\nAW 220,120\nMvav 52,156,252\nWL 6\nEX 81,45\nMvav 48,156,248\nRa 136,2,51200\nAD 85,85,80\nAW 221,121\nAW 222,122\n"
"Mvav 48,152,240\nD1 1,16,46,0\nAW 223,123\nv_mov_b32 v112, -1\nMvav 48,148,232\nRa 140,2,52224\nv_mov_b32 v113, -1\nv_mov_b32 v114, -1\nv_mov_b32 v115, -1\nMvav 48,144,224\n"
"AD 85,85,81\nCV 70,80,81\nCvav 56,160,192,12\nRa 144,2,53248\nSa 116,1,16,78,1024\nMvav 56,164,200\nEX 82,46\nMvav 56,168,208\nRa 148,2,54272\nv_xor_b32 v26, 0x10000, v26\n"
"Mvav 56,172,216\nWL 6\nEX 83,47\nCvav 60,172,220,16\nRa 152,2,55296\nAD 85,85,82\nMvav 60,168,212\nD1 1,16,46,1024\nMvav 60,164,204\nRa 156,2,56320\nMvav 60,160,196\nAD 85,85,83\n"
"CV 71,82,83\nMvav 60,176,228\nRa 160,2,57344\nv_max_f32 v90, v84, v85\nMvav 60,180,236\ns_add_u32 s88, s88, 32768\nEX 80,48\nMvav 60,184,244\nRa 164,2,58368\n"
"v_cmp_lt_f32 vcc, s63, v90\nMvav 60,188,252\nWL 8\nEX 81,49\nMvav 56,188,248\nRa 168,2,59392\ns_cbranch_vccnz .T289\n.T290:\nv_add_f32 v20, s93, v84\nv_mov_b32 v21, v85\n"
"v_mov_b32 v22, v20\nv_mov_b32 v23, v21\nMvav 56,184,240\nD1 1,16,46,2048\nv_permlane32_swap_b32 v20, v22\nv_permlane32_swap_b32 v21, v23\nAD 20,20,22\nAD 21,21,23\n"
"v_mov_b32 v22, v20\nv_mov_b32 v23, v21\nMvav 56,180,232\nRa 172,2,60416\nv_permlane16_swap_b32 v20, v22\nv_permlane16_swap_b32 v21, v23\nAD 20,20,22\nAD 21,21,23\n"
"v_min_f32 v90, v20, v21\nv_cmp_gt_f32 vcc, s64, v90\ns_nop 0\ns_cbranch_vccnz .T291\n.T292:\nAD 86,86,84\nMvav 56,176,224\nAD 84,80,81\nCV 72,80,81\nZaav 0,128,64\nRa 176,2,61440\n"
"Sa 120,1,16,78,2048\nZaav 4,128,68\nEX 82,50\nZaav 12,132,68\nRa 180,2,62464\nZaav 8,132,64\nWL 8\nEX 83,51\nZaav 16,136,64\nRa 184,2,63488\nAD 84,84,82\nZaav 20,136,68\n"
"D1 1,16,46,3072\ns_add_u32 m0, s89, 16384\nZaav 28,140,68\nRa 188,2,64512\ns_barrier\nZaav 24,140,64\nAD 84,84,83\nCV 73,82,83\nZaav 32,144,64\nRa 128,24,0\nSa 124,1,16,78,3072\n"
"Zaav 36,144,68\ns_add_u32 s46, s87, s88\nEX 80,52\nZaav 44,148,68\nRa 132,24,1024\nv_xor_b32 v2, 0x10000, v2\nZaav 40,148,64\nWL 8\nEX 81,53\nZaav 48,152,64\nRa 136,24,2048\n"
"AD 87,87,85\nZaav 52,152,68\nD1 1,16,46,0\nZaav 60,156,68\nRa 140,24,3072\nZaav 56,156,64\nAD 85,80,81\nCV 76,80,81\nZaav 64,160,64\nRa 144,26,0\nZaav 68,160,68\n"
"s_cmp_lt_u32 s99, s92\nEX 82,54\nZaav 76,164,68\nRa 148,26,1024\ns_cbranch_scc1 .E285\n.E286:\nZaav 72,164,64\nWL 8\nEX 83,55\nZaav 80,168,64\nRa 152,26,2048\nAD 85,85,82\n"
"Zaav 84,168,68\nD1 1,16,46,1024\nZaav 92,172,68\nRa 156,26,3072\nZaav 88,172,64\nAD 85,85,83\nCV 77,82,83\nZaav 96,176,64\nRa 160,24,8192\nZaav 100,176,68\ns_sub_u32 s90, s90, 1\n"
"EX 80,56\nZaav 108,180,68\nRa 164,24,9216\ns_cbranch_scc1 .E287\n.E288:\nZaav 104,180,64\nWL 6\nEX 81,57\nZaav 112,184,64\nRa 168,24,10240\nAD 84,84,80\nv_and_b32 v27, 0x70, v1\n"
"buffer_load_dword v27, v27, s[16:19], s61 offen offset:140 sc0 sc1\nZaav 116,184,68\nD1 1,16,46,2048\nZaav 124,188,68\nRa 172,24,11264\nZaav 120,188,64\nAD 84,84,81\nCV 74,80,81\n"
"s_sub_u32 s99, s99, 1\ns_cmp_eq_u32 s13, 7\ns_cbranch_scc1 .SW280\n.p2align 6\n.A317:\nCvav 32,128,192,4\nRa 176,26,8192\nMvav 32,132,200\nEX 82,58\nMvav 32,136,208\n"
"Ra 180,26,9216\nMvav 32,140,216\nWL 6\nEX 83,59\nCvav 36,140,220,8\nRa 184,26,10240\nAD 84,84,82\nMvav 36,136,212\nD1 1,16,46,3072\ns_add_u32 m0, s89, 32768\nMvav 36,132,204\n"
"Ra 188,26,11264\nMvav 36,128,196\nAD 84,84,83\nCV 75,82,83\nMvav 36,144,228\nRa 128,2,16384\nMvav 36,148,236\ns_add_u32 s46, s86, s88\nEX 80,60\nMvav 36,152,244\nRa 132,2,17408\n"
"Mvav 36,156,252\nWL 6\nEX 81,61\nMvav 32,156,248\nRa 136,2,18432\nAD 85,85,80\nMvav 32,152,240\nD1 1,16,46,0\nMvav 32,148,232\nRa 140,2,19456\nSv 112,1,16,49,0\nMvav 32,144,224\n"
"AD 85,85,81\nCV 78,80,81\nCvav 40,160,192,12\nRa 144,2,20480\nMvav 40,164,200\nEX 82,62\nMvav 40,168,208\nRa 148,2,21504\nMvav 40,172,216\nWL 6\nEX 83,63\nCvav 44,172,220,16\n"
"Ra 152,2,22528\nAD 85,85,82\nMvav 44,168,212\nD1 1,16,46,1024\nMvav 44,164,204\nRa 156,2,23552\nMvav 44,160,196\nAD 85,85,83\nCV 79,82,83\nMvav 44,176,228\nRa 160,2,24576\n"
"v_max_f32 v90, v84, v85\nMvav 44,180,236\ns_add_u32 s88, s88, 32768\nEX 80,32\nMvav 44,184,244\nRa 164,2,25600\nv_cmp_lt_f32 vcc, s63, v90\nMvav 44,188,252\nWL 8\nEX 81,33\n"
"Mvav 40,188,248\nRa 168,2,26624\ns_cbranch_vccnz .T325\n.T326:\nMvav 40,184,240\nD1 1,16,46,2048\nMvav 40,180,232\nRa 172,2,27648\nAD 86,86,84\nMvav 40,176,224\nAD 84,80,81\n"
"CV 64,80,81\nMaav 0,128,72\nRa 176,2,28672\nMaav 4,128,76\nEX 82,34\nMaav 12,132,76\nRa 180,2,29696\nMaav 8,132,72\ns_waitcnt vmcnt(13) lgkmcnt(8)\nEX 83,35\nMaav 16,136,72\n"
"Ra 184,2,30720\nAD 84,84,82\nMaav 20,136,76\nD1 1,16,46,3072\ns_add_u32 m0, s89, 49152\nMaav 28,140,76\nRa 188,2,31744\ns_barrier\nMaav 24,140,72\nAD 84,84,83\nCV 65,82,83\n"
"Maav 32,144,72\nRa 128,24,32768\nMaav 36,144,76\ns_add_u32 s46, s87, s88\nEX 80,36\nMaav 44,148,76\nRa 132,24,33792\nMaav 40,148,72\nWL 8\nEX 81,37\nMaav 48,152,72\n"
"Ra 136,24,34816\nAD 87,87,85\nMaav 52,152,76\nD1 1,16,46,0\nMaav 60,156,76\nRa 140,24,35840\nMaav 56,156,72\nAD 85,80,81\nCV 68,80,81\nMaav 64,160,72\nRa 144,26,32768\n"
"Maav 68,160,76\ns_cmp_eq_u32 s99, 0\nEX 82,38\nMaav 76,164,76\nRa 148,26,33792\ns_cbranch_scc1 .E319\n.E320:\nMaav 72,164,72\nWL 8\nEX 83,39\nMaav 80,168,72\nRa 152,26,34816\n"
"AD 85,85,82\nMaav 84,168,76\nD1 1,16,46,1024\nMaav 92,172,76\nRa 156,26,35840\nMaav 88,172,72\nAD 85,85,83\nCV 69,82,83\nMaav 96,176,72\nRa 160,24,40960\nMaav 100,176,76\n"
"s_xor_b32 s89, s89, 0x10000\nEX 80,40\nMaav 108,180,76\nRa 164,24,41984\nMaav 104,180,72\nWL 6\nEX 81,41\nMaav 112,184,72\nRa 168,24,43008\nAD 84,84,80\nMaav 116,184,76\n"
"D1 1,16,46,2048\nMaav 124,188,76\nRa 172,24,44032\nMaav 120,188,72\nAD 84,84,81\nCV 66,80,81\nCvav 48,128,192,4\nRa 176,26,40960\nMvav 48,132,200\ns_cmp_eq_u32 s99, 0\nEX 82,42\n"
"Mvav 48,136,208\nRa 180,26,41984\ns_cselect_b32 s86, s94, s86\nMvav 48,140,216\nWL 6\nEX 83,43\nCvav 52,140,220,8\nRa 184,26,43008\nAD 84,84,82\nMvav 52,136,212\nD1 1,16,46,3072\n"
"s_add_u32 m0, s89, 0\nMvav 52,132,204\nRa 188,26,44032\nMvav 52,128,196\nAD 84,84,83\nCV 67,82,83\nMvav 52,144,228\nRa 128,2,49152\nMvav 52,148,236\ns_add_u32 s46, s86, s88\n"
"EX 80,44\nMvav 52,152,244\nRa 132,2,50176\nv_xor_b32 v24, 0x10000, v24\nMvav 52,156,252\nWL 6\nEX 81,45\nMvav 48,156,248\nRa 136,2,51200\nAD 85,85,80\nMvav 48,152,240\n"
"D1 1,16,46,0\nMvav 48,148,232\nRa 140,2,52224\nSv 112,1,16,49,1024\nMvav 48,144,224\nAD 85,85,81\nCV 70,80,81\nCvav 56,160,192,12\nRa 144,2,53248\nMvav 56,164,200\n"
"s_add_u32 s49, s49, 131072\nEX 82,46\nMvav 56,168,208\nRa 148,2,54272\nv_xor_b32 v26, 0x10000, v26\nMvav 56,172,216\nWL 6\nEX 83,47\nCvav 60,172,220,16\nRa 152,2,55296\n"
"AD 85,85,82\nMvav 60,168,212\nD1 1,16,46,1024\nMvav 60,164,204\nRa 156,2,56320\nMvav 60,160,196\nAD 85,85,83\nCV 71,82,83\nMvav 60,176,228\nRa 160,2,57344\n"
"v_max_f32 v90, v84, v85\nMvav 60,180,236\ns_add_u32 s88, s88, 32768\nEX 80,48\nMvav 60,184,244\nRa 164,2,58368\nv_cmp_lt_f32 vcc, s63, v90\nMvav 60,188,252\nWL 8\nEX 81,49\n"
"Mvav 56,188,248\nRa 168,2,59392\ns_cbranch_vccnz .T327\n.T328:\nMvav 56,184,240\nD1 1,16,46,2048\nMvav 56,180,232\nRa 172,2,60416\nAD 86,86,84\nMvav 56,176,224\nAD 84,80,81\n"
"CV 72,80,81\nMaav 0,128,64\nRa 176,2,61440\nMaav 4,128,68\nEX 82,50\nMaav 12,132,68\nRa 180,2,62464\nMaav 8,132,64\ns_waitcnt vmcnt(13) lgkmcnt(8)\nEX 83,51\nMaav 16,136,64\n"
"Ra 184,2,63488\nAD 84,84,82\nMaav 20,136,68\nD1 1,16,46,3072\ns_add_u32 m0, s89, 16384\nMaav 28,140,68\nRa 188,2,64512\ns_barrier\nMaav 24,140,64\nAD 84,84,83\nCV 73,82,83\n"
"Maav 32,144,64\nRa 128,24,0\nMaav 36,144,68\ns_add_u32 s46, s87, s88\nEX 80,52\nMaav 44,148,68\nRa 132,24,1024\nv_xor_b32 v2, 0x10000, v2\nMaav 40,148,64\nWL 8\nEX 81,53\n"
"Maav 48,152,64\nRa 136,24,2048\nAD 87,87,85\nMaav 52,152,68\nD1 1,16,46,0\nMaav 60,156,68\nRa 140,24,3072\nMaav 56,156,64\nAD 85,80,81\nCV 76,80,81\nMaav 64,160,64\nRa 144,26,0\n"
"Maav 68,160,68\ns_cmp_lt_u32 s99, s92\nEX 82,54\nMaav 76,164,68\nRa 148,26,1024\ns_cbranch_scc1 .E321\n.E322:\nMaav 72,164,64\nWL 8\nEX 83,55\nMaav 80,168,64\nRa 152,26,2048\n"
"AD 85,85,82\nMaav 84,168,68\nD1 1,16,46,1024\nMaav 92,172,68\nRa 156,26,3072\nMaav 88,172,64\nAD 85,85,83\nCV 77,82,83\nMaav 96,176,64\nRa 160,24,8192\nMaav 100,176,68\n"
"s_sub_u32 s90, s90, 1\nEX 80,56\nMaav 108,180,68\nRa 164,24,9216\ns_cbranch_scc1 .E323\n.E324:\nMaav 104,180,64\nWL 6\nEX 81,57\nMaav 112,184,64\nRa 168,24,10240\nAD 84,84,80\n"
"Maav 116,184,68\nD1 1,16,46,2048\ns_sub_u32 s99, s99, 1\nMaav 124,188,68\nRa 172,24,11264\nMaav 120,188,64\nAD 84,84,81\nCV 74,80,81\ns_cbranch_scc0 .A317\ns_branch .A329\n.E319:\n"
"v_cmpx_gt_i32 vcc, 19, v0\nv_mov_b32 v15, s97\nv_cmpx_gt_i32 vcc, 18, v0\nv_mov_b32 v14, s97\nv_cmpx_gt_i32 vcc, 17, v0\nv_mov_b32 v13, s97\nv_cmpx_gt_i32 vcc, 16, v0\n"
"v_mov_b32 v12, s97\nv_cmpx_gt_i32 vcc, 3, v0\nv_mov_b32 v7, s97\nv_cmpx_gt_i32 vcc, 2, v0\nv_mov_b32 v6, s97\nv_cmpx_gt_i32 vcc, 1, v0\nv_mov_b32 v5, s97\n"
"v_cmpx_gt_i32 vcc, 0, v0\nv_mov_b32 v4, s97\ns_mov_b64 exec, -1\ns_nop 2\ns_branch .E320\n.E321:\ns_cmp_eq_u32 s99, 0\ns_cbranch_scc1 .K318\nv_cmpx_gt_i32 vcc, 19, v3\n"
"v_mov_b32 v15, s97\nv_cmpx_gt_i32 vcc, 18, v3\nv_mov_b32 v14, s97\nv_cmpx_gt_i32 vcc, 17, v3\nv_mov_b32 v13, s97\nv_cmpx_gt_i32 vcc, 16, v3\nv_mov_b32 v12, s97\n"
"v_cmpx_gt_i32 vcc, 3, v3\nv_mov_b32 v7, s97\nv_cmpx_gt_i32 vcc, 2, v3\nv_mov_b32 v6, s97\nv_cmpx_gt_i32 vcc, 1, v3\nv_mov_b32 v5, s97\nv_cmpx_gt_i32 vcc, 0, v3\nv_mov_b32 v4, s97\n"
"s_mov_b64 exec, -1\ns_nop 2\ns_branch .E322\n.K318:\nv_cmpx_gt_i32 vcc, 19, v3\nv_mov_b32 v19, s97\nv_cmpx_gt_i32 vcc, 18, v3\nv_mov_b32 v18, s97\nv_cmpx_gt_i32 vcc, 17, v3\n"
"v_mov_b32 v17, s97\nv_cmpx_gt_i32 vcc, 16, v3\nv_mov_b32 v16, s97\nv_cmpx_gt_i32 vcc, 3, v3\nv_mov_b32 v11, s97\nv_cmpx_gt_i32 vcc, 2, v3\nv_mov_b32 v10, s97\n"
"v_cmpx_gt_i32 vcc, 1, v3\nv_mov_b32 v9, s97\nv_cmpx_gt_i32 vcc, 0, v3\nv_mov_b32 v8, s97\ns_mov_b64 exec, -1\ns_nop 2\ns_branch .E322\n.E323:\ns_swappc_b64 s[80:81], s[8:9]\n"
"s_branch .E324\n.HD275:\nv_cmp_ne_u32 vcc, s15, v27\ns_nop 0\ns_cbranch_vccnz .S305\n.S306:\nbuffer_store_dword v25, off, s[16:19], s54 offset:4 sc0 sc1\nv_and_b32 v27, 0x70, v1\n"
"buffer_load_dword v27, v27, s[16:19], s61 offen offset:12 sc0 sc1\ns_mov_b32 s90, 1\ns_swappc_b64 s[8:9], s[80:81]\nv_cmp_ne_u32 vcc, s15, v27\ns_nop 0\ns_cbranch_vccnz .S308\n"
".S309:\nv_and_b32 v27, 0x70, v1\nbuffer_load_dword v27, v27, s[16:19], s61 offen offset:8 sc0 sc1\ns_mov_b32 s90, 1\ns_swappc_b64 s[8:9], s[80:81]\nv_cmp_ne_u32 vcc, s15, v27\n"
"s_nop 0\ns_cbranch_vccnz .S311\n.S312:\nv_and_b32 v27, 0xf0, v1\nbuffer_load_dword v27, v27, s[16:19], s61 offen offset:4 sc0 sc1\ns_mov_b32 s90, 1\ns_swappc_b64 s[8:9], s[80:81]\n"
"v_cmp_ne_u32 vcc, s15, v27\ns_nop 0\ns_cbranch_vccnz .S314\n.S315:\ns_mov_b32 s90, 100\ns_swappc_b64 s[8:9], s[80:81]\n.A329:\nRa 176,26,8192\nEX 82,58\nRa 180,26,9216\nWL 6\n"
"EX 83,59\nCvav 36,140,220,8\nRa 184,26,10240\nAD 84,84,82\nMvav 36,136,212\nD1 1,16,46,3072\ns_add_u32 m0, s89, 32768\nMvav 36,132,204\nRa 188,26,11264\nMvav 36,128,196\n"
"AD 84,84,83\nCV 75,82,83\nMvav 36,144,228\nRa 128,2,16384\nMvav 36,148,236\ns_add_u32 s46, s86, s88\nEX 80,60\nMvav 36,152,244\nRa 132,2,17408\nMvav 36,156,252\nEX 81,61\n"
"Ra 136,2,18432\nAD 85,85,80\nD1 1,16,46,0\nRa 140,2,19456\nSv 112,1,16,49,0\nAD 85,85,81\nCV 78,80,81\nRa 144,2,20480\ns_mov_b32 s87, s95\nEX 82,62\nRa 148,2,21504\nWL 6\n"
"EX 83,63\nCvav 44,172,220,16\nRa 152,2,22528\nAD 85,85,82\nMvav 44,168,212\nD1 1,16,46,1024\nMvav 44,164,204\nRa 156,2,23552\nMvav 44,160,196\nAD 85,85,83\nCV 79,82,83\n"
"Mvav 44,176,228\nRa 160,2,24576\nv_max_f32 v90, v84, v85\nMvav 44,180,236\ns_add_u32 s88, s88, 32768\nMvav 44,184,244\nRa 164,2,25600\nv_cmp_lt_f32 vcc, s63, v90\nMvav 44,188,252\n"
"WL 8\nRa 168,2,26624\ns_cbranch_vccnz .T333\n.T334:\nD1 1,16,46,2048\nRa 172,2,27648\nAD 86,86,84\nMaav 0,128,72\nRa 176,2,28672\nMaav 4,128,76\nMaav 12,132,76\nRa 180,2,29696\n"
"Maav 8,132,72\ns_waitcnt vmcnt(13) lgkmcnt(8)\nMaav 16,136,72\nRa 184,2,30720\nMaav 20,136,76\nD1 1,16,46,3072\ns_add_u32 m0, s89, 49152\nMaav 28,140,76\nRa 188,2,31744\n"
"s_barrier\nMaav 24,140,72\nMaav 32,144,72\nRa 128,24,32768\nMaav 36,144,76\ns_add_u32 s46, s87, s88\nEX 80,36\nMaav 44,148,76\nRa 132,24,33792\nMaav 40,148,72\nWL 8\nEX 81,37\n"
"Maav 48,152,72\nRa 136,24,34816\nAD 87,87,85\nMaav 52,152,76\nD1 1,16,46,0\nMaav 60,156,76\nRa 140,24,35840\nMaav 56,156,72\nAD 85,80,81\nCV 68,80,81\nMaav 64,160,72\n"
"Ra 144,26,32768\nMaav 68,160,76\ns_nop 0\nEX 82,38\nMaav 76,164,76\nRa 148,26,33792\nv_cmpx_gt_i32 vcc, 19, v0\nv_mov_b32 v19, s97\nv_cmpx_gt_i32 vcc, 18, v0\nv_mov_b32 v18, s97\n"
"v_cmpx_gt_i32 vcc, 17, v0\nv_mov_b32 v17, s97\nv_cmpx_gt_i32 vcc, 16, v0\nv_mov_b32 v16, s97\nv_cmpx_gt_i32 vcc, 3, v0\nv_mov_b32 v11, s97\nv_cmpx_gt_i32 vcc, 2, v0\n"
"v_mov_b32 v10, s97\nv_cmpx_gt_i32 vcc, 1, v0\nv_mov_b32 v9, s97\nv_cmpx_gt_i32 vcc, 0, v0\nv_mov_b32 v8, s97\ns_mov_b64 exec, -1\ns_nop 2\ns_nop 0\nMaav 72,164,72\nWL 8\nEX 83,39\n"
"Maav 80,168,72\nRa 152,26,34816\nAD 85,85,82\nMaav 84,168,76\nD1 1,16,46,1024\nMaav 92,172,76\nRa 156,26,35840\nMaav 88,172,72\nAD 85,85,83\nCV 69,82,83\nMaav 96,176,72\n"
"Ra 160,24,40960\nMaav 100,176,76\ns_xor_b32 s89, s89, 0x10000\nMaav 108,180,76\nRa 164,24,41984\nMaav 104,180,72\nWL 10\nMaav 112,184,72\nRa 168,24,43008\nMaav 116,184,76\n"
"D1 1,16,46,2048\nMaav 124,188,76\nRa 172,24,44032\nMaav 120,188,72\nRa 176,26,40960\nRa 180,26,41984\nWL 6\nCvav 52,140,220,8\nRa 184,26,43008\nMvav 52,136,212\nD1 1,16,46,3072\n"
"s_add_u32 m0, s89, 0\nMvav 52,132,204\nRa 188,26,44032\nMvav 52,128,196\nMvav 52,144,228\nRa 128,2,49152\nMvav 52,148,236\ns_add_u32 s46, s86, s88\nEX 80,44\nMvav 52,152,244\n"
"Ra 132,2,50176\nv_xor_b32 v24, 0x10000, v24\nMvav 52,156,252\nEX 81,45\nRa 136,2,51200\nAD 85,85,80\nD1 1,16,46,0\nRa 140,2,52224\nSv 112,1,16,49,1024\nAD 85,85,81\nCV 70,80,81\n"
"Ra 144,2,53248\ns_add_u32 s49, s49, 131072\nEX 82,46\nRa 148,2,54272\nv_xor_b32 v26, 0x10000, v26\nWL 6\nEX 83,47\nCvav 60,172,220,16\nRa 152,2,55296\nAD 85,85,82\n"
"Mvav 60,168,212\nD1 1,16,46,1024\nMvav 60,164,204\nRa 156,2,56320\nMvav 60,160,196\nAD 85,85,83\nCV 71,82,83\nMvav 60,176,228\nRa 160,2,57344\nMvav 60,180,236\n"
"s_add_u32 s88, s88, 32768\nMvav 60,184,244\nRa 164,2,58368\nv_cmp_lt_f32 vcc, s63, v85\nMvav 60,188,252\nWL 8\nRa 168,2,59392\ns_cbranch_vccnz .T335\n.T336:\nD1 1,16,46,2048\n"
"Ra 172,2,60416\nRa 176,2,61440\nMaav 4,128,68\nMaav 12,132,68\nRa 180,2,62464\ns_waitcnt vmcnt(13) lgkmcnt(8)\nRa 184,2,63488\nMaav 20,136,68\nD1 1,16,46,3072\n"
"s_add_u32 m0, s89, 16384\nMaav 28,140,68\nRa 188,2,64512\ns_barrier\nRa 128,24,0\nMaav 36,144,68\ns_add_u32 s46, s87, s88\nEX 80,52\nMaav 44,148,68\nRa 132,24,1024\n"
"v_xor_b32 v2, 0x10000, v2\nWL 8\nEX 81,53\nRa 136,24,2048\nAD 87,87,85\nMaav 52,152,68\nD1 1,16,46,0\nMaav 60,156,68\nRa 140,24,3072\nAD 85,80,81\nCV 76,80,81\nRa 144,26,0\n"
"Maav 68,160,68\nEX 82,54\nMaav 76,164,68\nRa 148,26,1024\nWL 8\nEX 83,55\nRa 152,26,2048\nAD 85,85,82\nMaav 84,168,68\nD1 1,16,46,1024\nMaav 92,172,68\nRa 156,26,3072\n"
"AD 85,85,83\nCV 77,82,83\nRa 160,24,8192\nMaav 100,176,68\ns_sub_u32 s90, s90, 1\nMaav 108,180,68\nRa 164,24,9216\ns_cbranch_scc1 .E331\n.E332:\nWL 6\nRa 168,24,10240\n"
"Maav 116,184,68\nD1 1,16,46,2048\nMaav 124,188,68\nRa 172,24,11264\n.SW280:\nv_mov_b32 v4, 0\nv_mov_b32 v5, 0\nv_mov_b32 v6, 0\nv_mov_b32 v7, 0\nv_mov_b32 v8, 0\nv_mov_b32 v9, 0\n"
"v_mov_b32 v10, 0\nv_mov_b32 v11, 0\nv_mov_b32 v12, 0\nv_mov_b32 v13, 0\nv_mov_b32 v14, 0\nv_mov_b32 v15, 0\nv_mov_b32 v16, 0\nv_mov_b32 v17, 0\nv_mov_b32 v18, 0\nv_mov_b32 v19, 0\n"
"s_add_u32 s99, s98, 1\ns_mov_b32 s94, s96\ns_add_u32 s95, s96, 65536\ns_lshl2_add_u32 s2, s13, s11\ns_cmp_lt_u32 s2, 2\ns_cbranch_scc1 .E337\n.E338:\nCvaa 32,128,192,4\n"
"Ra 176,26,8192\nMvaa 32,132,200\nMvaa 32,136,208\nRa 180,26,9216\nMvaa 32,140,216\nWL 6\nCvaa 36,140,220,8\nRa 184,26,10240\nMvaa 36,136,212\nD1 1,16,46,3072\n"
"s_add_u32 m0, s89, 32768\nMvaa 36,132,204\nRa 188,26,11264\nMvaa 36,128,196\nMvaa 36,144,228\nRa 128,2,16384\nMvaa 36,148,236\ns_add_u32 s46, s86, s88\nEX 80,60\nMvaa 36,152,244\n"
"Ra 132,2,17408\nMvaa 36,156,252\nWL 6\nEX 81,61\nMvaa 32,156,248\nRa 136,2,18432\nAD 85,85,80\nMvaa 32,152,240\nD1 1,16,46,0\nMvaa 32,148,232\nRa 140,2,19456\nMvaa 32,144,224\n"
"AD 85,85,81\nCV 78,80,81\nCvaa 40,160,192,12\nRa 144,2,20480\nMvaa 40,164,200\nEX 82,62\nMvaa 40,168,208\nRa 148,2,21504\nMvaa 40,172,216\nWL 6\nEX 83,63\nCvaa 44,172,220,16\n"
"Ra 152,2,22528\nAD 85,85,82\nMvaa 44,168,212\nD1 1,16,46,1024\nMvaa 44,164,204\nRa 156,2,23552\nMvaa 44,160,196\nAD 85,85,83\nCV 79,82,83\nMvaa 44,176,228\nRa 160,2,24576\n"
"Mvaa 44,180,236\ns_add_u32 s88, s88, 32768\nEX 80,32\nMvaa 44,184,244\nRa 164,2,25600\nv_cmp_lt_f32 vcc, s63, v85\nMvaa 44,188,252\nWL 8\nEX 81,33\nMvaa 40,188,248\n"
"Ra 168,2,26624\ns_cbranch_vccnz .T346\n.T347:\nMvaa 40,184,240\nD1 1,16,46,2048\nMvaa 40,180,232\nRa 172,2,27648\nMvaa 40,176,224\nAD 84,80,81\nCV 64,80,81\nRa 176,2,28672\n"
"Maav 4,128,76\nEX 82,34\nMaav 12,132,76\nRa 180,2,29696\ns_waitcnt vmcnt(12) lgkmcnt(8)\nEX 83,35\nRa 184,2,30720\nAD 84,84,82\nMaav 20,136,76\nD1 1,16,46,3072\n"
"s_add_u32 m0, s89, 49152\nMaav 28,140,76\nRa 188,2,31744\ns_barrier\nAR 98,0\nAR 99,1\nCV 100,98,99\nAD 84,84,83\nCV 65,82,83\nAR 98,2\nAR 99,3\nCV 101,98,99\nRa 128,24,32768\n"
"AR 98,8\nAR 99,9\nCV 102,98,99\nAR 98,10\nMaav 36,144,76\ns_add_u32 s46, s87, s88\nEX 80,36\nAR 99,11\nMaav 44,148,76\nRa 132,24,33792\nCV 103,98,99\ns_add_u32 s5, s66, 0\n"
"Sv 100,1,16,5,0\nAR 98,4\nWL 8\nEX 81,37\nAR 99,5\nCV 104,98,99\nRa 136,24,34816\nAD 87,87,85\nAR 98,6\nAR 99,7\nCV 105,98,99\nMaav 52,152,76\nD1 1,16,46,0\nv_mov_b32 v96, v86\n"
"v_mov_b32 v97, v87\nAR 98,12\nAR 99,13\nMaav 60,156,76\nRa 140,24,35840\nv_permlane32_swap_b32 v86, v96\nCV 106,98,99\nAR 98,14\nAR 99,15\nAD 85,80,81\nCV 68,80,81\nCV 107,98,99\n"
"s_add_u32 s5, s66, 0\nSv 104,1,16,5,1024\nRa 144,26,32768\nv_permlane32_swap_b32 v87, v97\nAD 86,86,96\nAR 98,16\nAR 99,17\nMaav 68,160,76\ns_cmp_eq_u32 s99, 0\nEX 82,38\n"
"Maav 76,164,76\nRa 148,26,33792\ns_cbranch_scc1 .E340\n.E341:\nCV 108,98,99\nAR 98,18\nAR 99,19\nWL 8\nEX 83,39\nCV 109,98,99\nAR 98,24\nRa 152,26,34816\nAD 85,85,82\nAD 87,87,97\n"
"AR 99,25\nCV 110,98,99\nMaav 84,168,76\nD1 1,16,46,1024\nv_mov_b32 v96, v86\nAR 98,26\nAR 99,27\nCV 111,98,99\nMaav 92,172,76\nRa 156,26,35840\nv_mov_b32 v97, v87\n"
"s_add_u32 s5, s66, 0\nSv 108,1,16,5,2048\nAR 98,20\nAD 85,85,83\nCV 69,82,83\nv_permlane16_swap_b32 v86, v96\nAR 99,21\nCV 100,98,99\nRa 160,24,40960\n"
"v_permlane16_swap_b32 v87, v97\nAR 98,22\nAR 99,23\nCV 101,98,99\nMaav 100,176,76\ns_xor_b32 s89, s89, 0x10000\nEX 80,40\nAR 98,28\nMaav 108,180,76\nRa 164,24,41984\nAD 86,86,96\n"
"AD 87,87,97\nAR 99,29\nCV 102,98,99\nWL 6\nEX 81,41\nAR 98,30\nAR 99,31\nRa 168,24,43008\nAD 84,84,80\nCV 103,98,99\ns_add_u32 s5, s66, 0\nSv 100,1,16,5,3072\nMaav 116,184,76\n"
"D1 1,16,46,2048\nv_cmp_gt_u32 vcc, 0x100, v1\nv_mov_b32 v94, s15\nv_mov_b32 v95, s15\nv_cndmask_b32 v92, v89, v88, vcc\nv_cndmask_b32 v93, v87, v86, vcc\nMaav 124,188,76\n"
"Ra 172,24,44032\nv_mov_b32 v86, 0\nAR 98,32\nAR 99,33\nCV 104,98,99\nAD 84,84,81\nCV 66,80,81\nAR 98,34\nAR 99,35\nCV 105,98,99\nCvaa 48,128,192,4\nRa 176,26,40960\n"
"v_mov_b32 v87, 0\nv_mov_b32 v88, 0\nAR 98,40\nAR 99,41\nMvaa 48,132,200\ns_cmp_eq_u32 s99, 0\nEX 82,42\nMvaa 48,136,208\nRa 180,26,41984\ns_cselect_b32 s86, s94, s86\n"
"CV 106,98,99\nAR 98,42\nAR 99,43\nMvaa 48,140,216\nWL 6\nEX 83,43\nCV 107,98,99\nCvaa 52,140,220,8\nRa 184,26,43008\nAD 84,84,82\nv_mov_b32 v89, 0\ns_add_u32 s5, s66, 4096\n"
"Sv 104,1,16,5,0\nMvaa 52,136,212\nD1 1,16,46,3072\ns_add_u32 m0, s89, 0\nAR 98,36\nAR 99,37\nCV 108,98,99\nMvaa 52,132,204\nRa 188,26,44032\nAR 98,38\nAR 99,39\nCV 109,98,99\n"
"AR 98,44\nMvaa 52,128,196\nAD 84,84,83\nCV 67,82,83\nAR 99,45\nCV 110,98,99\nAR 98,46\nMvaa 52,144,228\nRa 128,2,49152\nAR 99,47\nCV 111,98,99\ns_add_u32 s5, s66, 4096\n"
"Sv 108,1,16,5,1024\nMvaa 52,148,236\ns_add_u32 s46, s86, s88\nEX 80,44\nAR 98,48\nMvaa 52,152,244\nRa 132,2,50176\nv_xor_b32 v24, 0x10000, v24\nAR 99,49\nCV 100,98,99\nAR 98,50\n"
"Mvaa 52,156,252\nWL 6\nEX 81,45\nAR 99,51\nCV 101,98,99\nMvaa 48,156,248\nRa 136,2,51200\nAD 85,85,80\nAR 98,56\nAR 99,57\nCV 102,98,99\nMvaa 48,152,240\nD1 1,16,46,0\nAR 98,58\n"
"AR 99,59\nCV 103,98,99\nMvaa 48,148,232\nRa 140,2,52224\ns_add_u32 s5, s66, 4096\nSv 100,1,16,5,2048\nAR 98,52\nAR 99,53\nMvaa 48,144,224\nAD 85,85,81\nCV 70,80,81\nCV 104,98,99\n"
"AR 98,54\nAR 99,55\nCvaa 56,160,192,12\nRa 144,2,53248\nCV 105,98,99\nAR 98,60\nAR 99,61\nCV 106,98,99\nMvaa 56,164,200\nEX 82,46\nAR 98,62\nAR 99,63\nMvaa 56,168,208\n"
"Ra 148,2,54272\nv_xor_b32 v26, 0x10000, v26\nCV 107,98,99\ns_add_u32 s5, s66, 4096\nSv 104,1,16,5,3072\nMvaa 56,172,216\nWL 6\nEX 83,47\nAR 98,64\nAR 99,65\nCvaa 60,172,220,16\n"
"Ra 152,2,55296\nAD 85,85,82\nCV 108,98,99\nAR 98,66\nAR 99,67\nMvaa 60,168,212\nD1 1,16,46,1024\nCV 109,98,99\nAR 98,72\nAR 99,73\nCV 110,98,99\nMvaa 60,164,204\nRa 156,2,56320\n"
"AR 98,74\nAR 99,75\nCV 111,98,99\nMvaa 60,160,196\nAD 85,85,83\nCV 71,82,83\ns_add_u32 s5, s66, 16384\nSv 108,1,16,5,0\nAR 98,68\nMvaa 60,176,228\nRa 160,2,57344\n"
"v_max_f32 v90, v84, v85\nAR 99,69\nCV 100,98,99\nAR 98,70\nMvaa 60,180,236\ns_add_u32 s88, s88, 32768\nEX 80,48\nAR 99,71\nMvaa 60,184,244\nRa 164,2,58368\n"
"v_cmp_lt_f32 vcc, s63, v90\nMvaa 60,188,252\nWL 8\nEX 81,49\nMvaa 56,188,248\nRa 168,2,59392\ns_cbranch_vccnz .T348\n.T349:\nv_mov_b32 v20, v84\nv_mov_b32 v21, v85\n"
"v_mov_b32 v22, v20\nv_mov_b32 v23, v21\nMvaa 56,184,240\nD1 1,16,46,2048\nv_permlane32_swap_b32 v20, v22\nv_permlane32_swap_b32 v21, v23\nAD 20,20,22\nAD 21,21,23\n"
"v_mov_b32 v22, v20\nv_mov_b32 v23, v21\nMvaa 56,180,232\nRa 172,2,60416\nv_permlane16_swap_b32 v20, v22\nv_permlane16_swap_b32 v21, v23\nAD 20,20,22\nAD 21,21,23\n"
"v_min_f32 v90, v20, v21\nv_cmp_gt_f32 vcc, s64, v90\ns_nop 0\ns_cbranch_vccnz .T350\n.T351:\nAD 86,86,84\nMvaa 56,176,224\nAD 84,80,81\nCV 72,80,81\nCV 101,98,99\nAR 98,76\n"
"AR 99,77\nZvav 128,128,64\nRa 176,2,61440\nCV 102,98,99\nAR 98,78\nAR 99,79\nCV 103,98,99\nZvav 132,128,68\nEX 82,50\ns_add_u32 s5, s66, 16384\nSv 100,1,16,5,1024\n"
"Zvav 140,132,68\nRa 180,2,62464\nAR 98,80\nAR 99,81\nCV 104,98,99\nAR 98,82\nZvav 136,132,64\ns_waitcnt vmcnt(21) lgkmcnt(8)\nEX 83,51\nAR 99,83\nCV 105,98,99\nZvav 144,136,64\n"
"Ra 184,2,63488\nAD 84,84,82\nAR 98,88\nAR 99,89\nCV 106,98,99\nZvav 148,136,68\nD1 1,16,46,3072\ns_add_u32 m0, s89, 16384\nAR 98,90\nAR 99,91\nCV 107,98,99\nZvav 156,140,68\n"
"Ra 188,2,64512\ns_barrier\ns_add_u32 s5, s66, 16384\nSv 104,1,16,5,2048\nAR 98,84\nZvav 152,140,64\nAD 84,84,83\nCV 73,82,83\nAR 99,85\nCV 108,98,99\nAR 98,86\nZvav 160,144,64\n"
"Ra 128,24,0\nAR 99,87\nCV 109,98,99\nAR 98,92\nAR 99,93\nZvav 164,144,68\ns_add_u32 s46, s87, s88\nEX 80,52\nCV 110,98,99\nZvav 172,148,68\nRa 132,24,1024\n"
"v_xor_b32 v2, 0x10000, v2\nAR 98,94\nAR 99,95\nCV 111,98,99\nZvav 168,148,64\nWL 8\nEX 81,53\ns_add_u32 s5, s66, 16384\nSv 108,1,16,5,3072\nZvav 176,152,64\nRa 136,24,2048\n"
"AD 87,87,85\nAR 98,96\nAR 99,97\nCV 100,98,99\nZvav 180,152,68\nD1 1,16,46,0\nAR 98,98\nAR 99,99\nCV 101,98,99\nAR 98,104\nZvav 188,156,68\nRa 140,24,3072\nAR 99,105\n"
"CV 102,98,99\nAR 98,106\nAR 99,107\nZvav 184,156,64\nAD 85,80,81\nCV 76,80,81\nCV 103,98,99\ns_add_u32 s5, s66, 20480\nSv 100,1,16,5,0\nZvav 192,160,64\nRa 144,26,0\nAR 98,100\n"
"AR 99,101\nCV 104,98,99\nAR 98,102\nZvav 196,160,68\ns_cmp_lt_u32 s99, s92\nEX 82,54\nZvav 204,164,68\nRa 148,26,1024\ns_cbranch_scc1 .E342\n.E343:\nAR 99,103\nCV 105,98,99\n"
"AR 98,108\nZvav 200,164,64\nWL 8\nEX 83,55\nAR 99,109\nCV 106,98,99\nZvav 208,168,64\nRa 152,26,2048\nAD 85,85,82\nAR 98,110\nAR 99,111\nCV 107,98,99\nZvav 212,168,68\n"
"D1 1,16,46,1024\ns_add_u32 s5, s66, 20480\nSv 104,1,16,5,1024\nAR 98,112\nAR 99,113\nZvav 220,172,68\nRa 156,26,3072\nCV 108,98,99\nAR 98,114\nAR 99,115\nCV 109,98,99\n"
"Zvav 216,172,64\nAD 85,85,83\nCV 77,82,83\nAR 98,120\nAR 99,121\nCV 110,98,99\nZvav 224,176,64\nRa 160,24,8192\nAR 98,122\nAR 99,123\nCV 111,98,99\nZvav 228,176,68\n"
"s_sub_u32 s90, s90, 1\nEX 80,56\nZvav 236,180,68\nRa 164,24,9216\ns_cbranch_scc1 .E344\n.E345:\ns_add_u32 s5, s66, 20480\nSv 108,1,16,5,2048\nAR 98,116\nZvav 232,180,64\nWL 6\n"
"EX 81,57\nAR 99,117\nCV 100,98,99\nZvav 240,184,64\nRa 168,24,10240\nAD 84,84,80\nAR 98,118\nAR 99,119\nCV 101,98,99\nZvav 244,184,68\nD1 1,16,46,2048\nAR 98,124\nAR 99,125\n"
"CV 102,98,99\nAR 98,126\nZvav 252,188,68\nRa 172,24,11264\nAR 99,127\nCV 103,98,99\ns_add_u32 s5, s66, 20480\nSv 100,1,16,5,3072\nZvav 248,188,64\nAD 84,84,81\nCV 74,80,81\n"
"s_sub_u32 s99, s99, 1\ns_cbranch_scc1 .LE281\n.p2align 6\n.A352:\nCvaa 32,128,192,4\nRa 176,26,8192\nMvaa 32,132,200\nEX 82,58\nMvaa 32,136,208\nRa 180,26,9216\nMvaa 32,140,216\n"
"WL 6\nEX 83,59\nCvaa 36,140,220,8\nRa 184,26,10240\nAD 84,84,82\nMvaa 36,136,212\nD1 1,16,46,3072\ns_add_u32 m0, s89, 32768\nMvaa 36,132,204\nRa 188,26,11264\nMvaa 36,128,196\n"
"AD 84,84,83\nCV 75,82,83\nMvaa 36,144,228\nRa 128,2,16384\nMvaa 36,148,236\ns_add_u32 s46, s86, s88\nEX 80,60\nMvaa 36,152,244\nRa 132,2,17408\nMvaa 36,156,252\nWL 6\nEX 81,61\n"
"Mvaa 32,156,248\nRa 136,2,18432\nAD 85,85,80\nMvaa 32,152,240\nD1 1,16,46,0\nMvaa 32,148,232\nRa 140,2,19456\nSv 112,1,16,49,0\nMvaa 32,144,224\nAD 85,85,81\nCV 78,80,81\n"
"Cvaa 40,160,192,12\nRa 144,2,20480\nMvaa 40,164,200\nEX 82,62\nMvaa 40,168,208\nRa 148,2,21504\nMvaa 40,172,216\nWL 6\nEX 83,63\nCvaa 44,172,220,16\nRa 152,2,22528\nAD 85,85,82\n"
"Mvaa 44,168,212\nD1 1,16,46,1024\nMvaa 44,164,204\nRa 156,2,23552\nMvaa 44,160,196\nAD 85,85,83\nCV 79,82,83\nMvaa 44,176,228\nRa 160,2,24576\nv_max_f32 v90, v84, v85\n"
"Mvaa 44,180,236\ns_add_u32 s88, s88, 32768\nEX 80,32\nMvaa 44,184,244\nRa 164,2,25600\nv_cmp_lt_f32 vcc, s63, v90\nMvaa 44,188,252\nWL 8\nEX 81,33\nMvaa 40,188,248\n"
"Ra 168,2,26624\ns_cbranch_vccnz .T362\n.T363:\nMvaa 40,184,240\nD1 1,16,46,2048\nMvaa 40,180,232\nRa 172,2,27648\nAD 86,86,84\nMvaa 40,176,224\nAD 84,80,81\nCV 64,80,81\n"
"Mvav 128,128,72\nRa 176,2,28672\nMvav 132,128,76\ns_cmp_eq_u32 s99, s98\nEX 82,34\nMvav 140,132,76\nRa 180,2,29696\ns_cbranch_scc1 .E360\n.E361:\nMvav 136,132,72\n"
"s_waitcnt vmcnt(13) lgkmcnt(8)\nEX 83,35\nMvav 144,136,72\nRa 184,2,30720\nAD 84,84,82\nMvav 148,136,76\nD1 1,16,46,3072\ns_add_u32 m0, s89, 49152\nMvav 156,140,76\n"
"Ra 188,2,31744\ns_barrier\nMvav 152,140,72\nAD 84,84,83\nCV 65,82,83\nMvav 160,144,72\nRa 128,24,32768\nMvav 164,144,76\ns_add_u32 s46, s87, s88\nEX 80,36\nMvav 172,148,76\n"
"Ra 132,24,33792\nMvav 168,148,72\nWL 8\nEX 81,37\nMvav 176,152,72\nRa 136,24,34816\nAD 87,87,85\nMvav 180,152,76\nD1 1,16,46,0\nMvav 188,156,76\nRa 140,24,35840\nMvav 184,156,72\n"
"AD 85,80,81\nCV 68,80,81\nMvav 192,160,72\nRa 144,26,32768\nMvav 196,160,76\ns_cmp_eq_u32 s99, 0\nEX 82,38\nMvav 204,164,76\nRa 148,26,33792\ns_cbranch_scc1 .E354\n.E355:\n"
"Mvav 200,164,72\nWL 8\nEX 83,39\nMvav 208,168,72\nRa 152,26,34816\nAD 85,85,82\nMvav 212,168,76\nD1 1,16,46,1024\nMvav 220,172,76\nRa 156,26,35840\nMvav 216,172,72\nAD 85,85,83\n"
"CV 69,82,83\nMvav 224,176,72\nRa 160,24,40960\nMvav 228,176,76\ns_xor_b32 s89, s89, 0x10000\nEX 80,40\nMvav 236,180,76\nRa 164,24,41984\nMvav 232,180,72\nWL 6\nEX 81,41\n"
"Mvav 240,184,72\nRa 168,24,43008\nAD 84,84,80\nMvav 244,184,76\nD1 1,16,46,2048\nMvav 252,188,76\nRa 172,24,44032\nMvav 248,188,72\nAD 84,84,81\nCV 66,80,81\nCvaa 48,128,192,4\n"
"Ra 176,26,40960\nMvaa 48,132,200\ns_cmp_eq_u32 s99, 0\nEX 82,42\nMvaa 48,136,208\nRa 180,26,41984\ns_cselect_b32 s86, s94, s86\nMvaa 48,140,216\nWL 6\nEX 83,43\nCvaa 52,140,220,8\n"
"Ra 184,26,43008\nAD 84,84,82\nMvaa 52,136,212\nD1 1,16,46,3072\ns_add_u32 m0, s89, 0\nMvaa 52,132,204\nRa 188,26,44032\nMvaa 52,128,196\nAD 84,84,83\nCV 67,82,83\nMvaa 52,144,228\n"
"Ra 128,2,49152\nMvaa 52,148,236\ns_add_u32 s46, s86, s88\nEX 80,44\nMvaa 52,152,244\nRa 132,2,50176\nv_xor_b32 v24, 0x10000, v24\nMvaa 52,156,252\nWL 6\nEX 81,45\nMvaa 48,156,248\n"
"Ra 136,2,51200\nAD 85,85,80\nMvaa 48,152,240\nD1 1,16,46,0\nMvaa 48,148,232\nRa 140,2,52224\nSv 112,1,16,49,1024\nMvaa 48,144,224\nAD 85,85,81\nCV 70,80,81\nCvaa 56,160,192,12\n"
"Ra 144,2,53248\nMvaa 56,164,200\ns_add_u32 s49, s49, 131072\nEX 82,46\nMvaa 56,168,208\nRa 148,2,54272\nv_xor_b32 v26, 0x10000, v26\nMvaa 56,172,216\nWL 6\nEX 83,47\n"
"Cvaa 60,172,220,16\nRa 152,2,55296\nAD 85,85,82\nMvaa 60,168,212\nD1 1,16,46,1024\nMvaa 60,164,204\nRa 156,2,56320\nMvaa 60,160,196\nAD 85,85,83\nCV 71,82,83\nMvaa 60,176,228\n"
"Ra 160,2,57344\nv_max_f32 v90, v84, v85\nMvaa 60,180,236\ns_add_u32 s88, s88, 32768\nEX 80,48\nMvaa 60,184,244\nRa 164,2,58368\nv_cmp_lt_f32 vcc, s63, v90\nMvaa 60,188,252\nWL 8\n"
"EX 81,49\nMvaa 56,188,248\nRa 168,2,59392\ns_cbranch_vccnz .T364\n.T365:\nMvaa 56,184,240\nD1 1,16,46,2048\nMvaa 56,180,232\nRa 172,2,60416\nAD 86,86,84\nMvaa 56,176,224\n"
"AD 84,80,81\nCV 72,80,81\nMvav 128,128,64\nRa 176,2,61440\nMvav 132,128,68\nEX 82,50\nMvav 140,132,68\nRa 180,2,62464\nMvav 136,132,64\ns_waitcnt vmcnt(13) lgkmcnt(8)\nEX 83,51\n"
"Mvav 144,136,64\nRa 184,2,63488\nAD 84,84,82\nMvav 148,136,68\nD1 1,16,46,3072\ns_add_u32 m0, s89, 16384\nMvav 156,140,68\nRa 188,2,64512\ns_barrier\nMvav 152,140,64\nAD 84,84,83\n"
"CV 73,82,83\nMvav 160,144,64\nRa 128,24,0\nMvav 164,144,68\ns_add_u32 s46, s87, s88\nEX 80,52\nMvav 172,148,68\nRa 132,24,1024\nv_xor_b32 v2, 0x10000, v2\nMvav 168,148,64\nWL 8\n"
"EX 81,53\nMvav 176,152,64\nRa 136,24,2048\nAD 87,87,85\nMvav 180,152,68\nD1 1,16,46,0\nMvav 188,156,68\nRa 140,24,3072\nMvav 184,156,64\nAD 85,80,81\nCV 76,80,81\nMvav 192,160,64\n"
"Ra 144,26,0\nMvav 196,160,68\ns_cmp_lt_u32 s99, s92\nEX 82,54\nMvav 204,164,68\nRa 148,26,1024\ns_cbranch_scc1 .E356\n.E357:\nMvav 200,164,64\nWL 8\nEX 83,55\nMvav 208,168,64\n"
"Ra 152,26,2048\nAD 85,85,82\nMvav 212,168,68\nD1 1,16,46,1024\nMvav 220,172,68\nRa 156,26,3072\nMvav 216,172,64\nAD 85,85,83\nCV 77,82,83\nMvav 224,176,64\nRa 160,24,8192\n"
"Mvav 228,176,68\ns_sub_u32 s90, s90, 1\nEX 80,56\nMvav 236,180,68\nRa 164,24,9216\ns_cbranch_scc1 .E358\n.E359:\nMvav 232,180,64\nWL 6\nEX 81,57\nMvav 240,184,64\nRa 168,24,10240\n"
"AD 84,84,80\nMvav 244,184,68\nD1 1,16,46,2048\ns_sub_u32 s99, s99, 1\nMvav 252,188,68\nRa 172,24,11264\nMvav 248,188,64\nAD 84,84,81\nCV 74,80,81\ns_cbranch_scc0 .A352\n"
"s_branch .A366\n.E354:\nv_cmpx_gt_i32 vcc, 19, v0\nv_mov_b32 v15, s97\nv_cmpx_gt_i32 vcc, 18, v0\nv_mov_b32 v14, s97\nv_cmpx_gt_i32 vcc, 17, v0\nv_mov_b32 v13, s97\n"
"v_cmpx_gt_i32 vcc, 16, v0\nv_mov_b32 v12, s97\nv_cmpx_gt_i32 vcc, 3, v0\nv_mov_b32 v7, s97\nv_cmpx_gt_i32 vcc, 2, v0\nv_mov_b32 v6, s97\nv_cmpx_gt_i32 vcc, 1, v0\n"
"v_mov_b32 v5, s97\nv_cmpx_gt_i32 vcc, 0, v0\nv_mov_b32 v4, s97\ns_mov_b64 exec, -1\ns_nop 2\ns_branch .E355\n.E356:\ns_cmp_eq_u32 s99, 0\ns_cbranch_scc1 .K353\n"
"v_cmpx_gt_i32 vcc, 19, v3\nv_mov_b32 v15, s97\nv_cmpx_gt_i32 vcc, 18, v3\nv_mov_b32 v14, s97\nv_cmpx_gt_i32 vcc, 17, v3\nv_mov_b32 v13, s97\nv_cmpx_gt_i32 vcc, 16, v3\n"
"v_mov_b32 v12, s97\nv_cmpx_gt_i32 vcc, 3, v3\nv_mov_b32 v7, s97\nv_cmpx_gt_i32 vcc, 2, v3\nv_mov_b32 v6, s97\nv_cmpx_gt_i32 vcc, 1, v3\nv_mov_b32 v5, s97\n"
"v_cmpx_gt_i32 vcc, 0, v3\nv_mov_b32 v4, s97\ns_mov_b64 exec, -1\ns_nop 2\ns_branch .E357\n.K353:\nv_cmpx_gt_i32 vcc, 19, v3\nv_mov_b32 v19, s97\nv_cmpx_gt_i32 vcc, 18, v3\n"
"v_mov_b32 v18, s97\nv_cmpx_gt_i32 vcc, 17, v3\nv_mov_b32 v17, s97\nv_cmpx_gt_i32 vcc, 16, v3\nv_mov_b32 v16, s97\nv_cmpx_gt_i32 vcc, 3, v3\nv_mov_b32 v11, s97\n"
"v_cmpx_gt_i32 vcc, 2, v3\nv_mov_b32 v10, s97\nv_cmpx_gt_i32 vcc, 1, v3\nv_mov_b32 v9, s97\nv_cmpx_gt_i32 vcc, 0, v3\nv_mov_b32 v8, s97\ns_mov_b64 exec, -1\ns_nop 2\n"
"s_branch .E357\n.E358:\ns_swappc_b64 s[80:81], s[8:9]\ns_branch .E359\n.E360:\nv_mov_b32 v91, 0x40000000\nv_cmp_gt_u32 vcc, 0x200, v1\nWV 5\ns_nop 0\n"
"v_cndmask_b32 v91, v91, v1, vcc\nSv 92,91,16,65,0\ns_branch .E361\n.A366:\n.LE281:\nRa 176,26,8192\nEX 82,58\nRa 180,26,9216\nWL 6\nEX 83,59\nCvaa 36,140,220,8\nRa 184,26,10240\n"
"AD 84,84,82\nMvaa 36,136,212\nD1 1,16,46,3072\ns_add_u32 m0, s89, 32768\nMvaa 36,132,204\nRa 188,26,11264\nMvaa 36,128,196\nAD 84,84,83\nCV 75,82,83\nMvaa 36,144,228\n"
"Ra 128,2,16384\nMvaa 36,148,236\ns_add_u32 s46, s86, s88\nEX 80,60\nMvaa 36,152,244\nRa 132,2,17408\nMvaa 36,156,252\nEX 81,61\nRa 136,2,18432\nAD 85,85,80\nD1 1,16,46,0\n"
"Ra 140,2,19456\nAD 85,85,81\nCV 78,80,81\nRa 144,2,20480\ns_mov_b32 s87, s95\nEX 82,62\nRa 148,2,21504\nWL 6\nEX 83,63\nCvaa 44,172,220,16\nRa 152,2,22528\nAD 85,85,82\n"
"Mvaa 44,168,212\nD1 1,16,46,1024\nMvaa 44,164,204\nRa 156,2,23552\nMvaa 44,160,196\nAD 85,85,83\nCV 79,82,83\nMvaa 44,176,228\nRa 160,2,24576\nv_max_f32 v90, v84, v85\n"
"Mvaa 44,180,236\ns_add_u32 s88, s88, 32768\nMvaa 44,184,244\nRa 164,2,25600\nv_cmp_lt_f32 vcc, s63, v90\nMvaa 44,188,252\nWL 8\nRa 168,2,26624\ns_cbranch_vccnz .T370\n.T371:\n"
"D1 1,16,46,2048\nRa 172,2,27648\nAD 86,86,84\nMvav 128,128,72\nRa 176,2,28672\nMvav 132,128,76\ns_cmp_eq_u32 s99, s98\nMvav 140,132,76\nRa 180,2,29696\ns_cbranch_scc1 .E368\n"
".E369:\nMvav 136,132,72\ns_waitcnt vmcnt(12) lgkmcnt(8)\nMvav 144,136,72\nRa 184,2,30720\nMvav 148,136,76\nD1 1,16,46,3072\ns_add_u32 m0, s89, 49152\nMvav 156,140,76\n"
"Ra 188,2,31744\ns_barrier\nMvav 152,140,72\nMvav 160,144,72\nRa 128,24,32768\nMvav 164,144,76\ns_add_u32 s46, s87, s88\nEX 80,36\nMvav 172,148,76\nRa 132,24,33792\n"
"Mvav 168,148,72\nWL 8\nEX 81,37\nMvav 176,152,72\nRa 136,24,34816\nAD 87,87,85\nMvav 180,152,76\nD1 1,16,46,0\nMvav 188,156,76\nRa 140,24,35840\nMvav 184,156,72\nAD 85,80,81\n"
"CV 68,80,81\nMvav 192,160,72\nRa 144,26,32768\nMvav 196,160,76\ns_nop 0\nEX 82,38\nMvav 204,164,76\nRa 148,26,33792\nv_cmpx_gt_i32 vcc, 19, v0\nv_mov_b32 v19, s97\n"
"v_cmpx_gt_i32 vcc, 18, v0\nv_mov_b32 v18, s97\nv_cmpx_gt_i32 vcc, 17, v0\nv_mov_b32 v17, s97\nv_cmpx_gt_i32 vcc, 16, v0\nv_mov_b32 v16, s97\nv_cmpx_gt_i32 vcc, 3, v0\n"
"v_mov_b32 v11, s97\nv_cmpx_gt_i32 vcc, 2, v0\nv_mov_b32 v10, s97\nv_cmpx_gt_i32 vcc, 1, v0\nv_mov_b32 v9, s97\nv_cmpx_gt_i32 vcc, 0, v0\nv_mov_b32 v8, s97\ns_mov_b64 exec, -1\n"
"s_nop 2\ns_nop 0\nMvav 200,164,72\nWL 8\nEX 83,39\nMvav 208,168,72\nRa 152,26,34816\nAD 85,85,82\nMvav 212,168,76\nD1 1,16,46,1024\nMvav 220,172,76\nRa 156,26,35840\n"
"Mvav 216,172,72\nAD 85,85,83\nCV 69,82,83\nMvav 224,176,72\nRa 160,24,40960\nMvav 228,176,76\ns_xor_b32 s89, s89, 0x10000\nMvav 236,180,76\nRa 164,24,41984\nMvav 232,180,72\n"
"WL 10\nMvav 240,184,72\nRa 168,24,43008\nMvav 244,184,76\nD1 1,16,46,2048\nMvav 252,188,76\nRa 172,24,44032\nMvav 248,188,72\nRa 176,26,40960\nRa 180,26,41984\nWL 6\n"
"Cvaa 52,140,220,8\nRa 184,26,43008\nMvaa 52,136,212\nD1 1,16,46,3072\ns_add_u32 m0, s89, 0\nMvaa 52,132,204\nRa 188,26,44032\nMvaa 52,128,196\nMvaa 52,144,228\nRa 128,2,49152\n"
"Mvaa 52,148,236\ns_add_u32 s46, s86, s88\nEX 80,44\nMvaa 52,152,244\nRa 132,2,50176\nv_xor_b32 v24, 0x10000, v24\nMvaa 52,156,252\nEX 81,45\nRa 136,2,51200\nAD 85,85,80\n"
"D1 1,16,46,0\nRa 140,2,52224\nAD 85,85,81\nCV 70,80,81\nRa 144,2,53248\nEX 82,46\nRa 148,2,54272\nv_xor_b32 v26, 0x10000, v26\nWL 6\nEX 83,47\nCvaa 60,172,220,16\nRa 152,2,55296\n"
"AD 85,85,82\nMvaa 60,168,212\nD1 1,16,46,1024\nMvaa 60,164,204\nRa 156,2,56320\nMvaa 60,160,196\nAD 85,85,83\nCV 71,82,83\nMvaa 60,176,228\nRa 160,2,57344\nMvaa 60,180,236\n"
"s_add_u32 s88, s88, 32768\nMvaa 60,184,244\nRa 164,2,58368\nv_cmp_lt_f32 vcc, s63, v85\nMvaa 60,188,252\nWL 8\nRa 168,2,59392\ns_cbranch_vccnz .T372\n.T373:\nD1 1,16,46,2048\n"
"Ra 172,2,60416\nRa 176,2,61440\nMvav 132,128,68\nMvav 140,132,68\nRa 180,2,62464\ns_waitcnt vmcnt(11) lgkmcnt(8)\nRa 184,2,63488\nMvav 148,136,68\nD1 1,16,46,3072\n"
"s_add_u32 m0, s89, 16384\nMvav 156,140,68\nRa 188,2,64512\ns_barrier\nRa 128,24,0\nMvav 164,144,68\ns_add_u32 s46, s87, s88\nEX 80,52\nMvav 172,148,68\nRa 132,24,1024\n"
"v_xor_b32 v2, 0x10000, v2\nWL 8\nEX 81,53\nRa 136,24,2048\nAD 87,87,85\nMvav 180,152,68\nD1 1,16,46,0\nMvav 188,156,68\nRa 140,24,3072\nAD 85,80,81\nCV 76,80,81\nRa 144,26,0\n"
"Mvav 196,160,68\nEX 82,54\nMvav 204,164,68\nRa 148,26,1024\nWL 8\nEX 83,55\nRa 152,26,2048\nAD 85,85,82\nMvav 212,168,68\nD1 1,16,46,1024\nMvav 220,172,68\nRa 156,26,3072\n"
"AD 85,85,83\nCV 77,82,83\nRa 160,24,8192\nMvav 228,176,68\nMvav 236,180,68\nRa 164,24,9216\nWL 10\nRa 168,24,10240\nMvav 244,184,68\nD1 1,16,46,2048\nMvav 252,188,68\n"
"Ra 172,24,11264\nD1 1,16,46,3072\ns_add_u32 m0, s89, 32768\ns_add_u32 s46, s86, s88\nRa 128,2,16384\nv_xor_b32 v3, 0x10000, v2\nv_add_u32 v0, 0x20000, v1\nEX 80,60\n"
"Ra 132,2,17408\ns_sub_u32 s94, s49, 1835008\nEX 81,61\nRa 136,2,18432\nAD 85,85,80\ns_xor_b32 s92, s94, 0x202000\nD1 1,16,46,0\ns_lshl_b32 s89, s11, 12\nRa 140,2,19456\n"
"s_lshl1_add_u32 s2, s12, 1\nAD 85,85,81\nCV 78,80,81\nRa 144,2,20480\ns_lshl3_add_u32 s2, s2, s13\nEX 82,62\nRa 148,2,21504\ns_lshl4_add_u32 s2, s2, 0xc048200\nEX 83,63\n"
"Ra 152,2,22528\nAD 85,85,82\nD1 1,16,46,1024\nRa 156,2,23552\ns_and_b32 s3, s72, 0x2000\nAD 85,85,83\nCV 79,82,83\nRa 160,2,24576\ns_cmp_eq_u32 s11, 1\ns_cselect_b32 s54, s2, s54\n"
"s_cselect_b32 s3, s3, 0\nv_cmp_lt_f32 vcc, s63, v85\nRa 164,2,25600\ns_cbranch_vccnz .T374\n.T375:\nAD 87,87,85\nWL 8\nv_mov_b32 v84, v86\nv_mov_b32 v85, v87\nRa 168,2,26624\n"
"v_permlane32_swap_b32 v86, v84\nv_permlane32_swap_b32 v87, v85\nD1 1,16,46,2048\nAD 86,86,84\nRa 172,2,27648\nAD 87,87,85\nv_mov_b32 v84, v86\nv_mov_b32 v85, v87\nRa 176,2,28672\n"
"v_permlane16_swap_b32 v86, v84\nv_permlane16_swap_b32 v87, v85\nMvav 132,128,76\nAD 86,86,84\nAD 87,87,85\nMvav 140,132,76\nRa 180,2,29696\nv_rcp_f32 v86, v86\nWL 8\n"
"v_rcp_f32 v87, v87\nRa 184,2,30720\nv_xor_b32 v25, s3, v25\nMvav 148,136,76\nD1 1,16,46,3072\nMvav 156,140,76\nRa 188,2,31744\nMU 128,128,86\nMU 129,129,86\nCV 96,128,129\n"
"MU 130,130,86\nMU 131,131,86\nRa 128,2,0\nCV 97,130,131\nMU 132,132,87\nMvav 164,144,76\nMU 133,133,87\nCV 100,132,133\nMvav 172,148,76\nRa 132,2,1024\nMU 134,134,87\n"
"MU 135,135,87\nWL 8\ns_add_u32 m0, s89, 131072\ns_add_u32 s46, s96, 1212416\nRa 136,2,2048\nCV 101,134,135\nMU 136,136,86\nMvav 180,152,76\nD1 1,16,46,0\nMU 137,137,86\n"
"Mvav 188,156,76\nRa 140,2,3072\nCV 98,136,137\nMU 138,138,86\nMU 139,139,86\nCV 99,138,139\nMU 140,140,87\nRa 144,2,4096\nMU 141,141,87\nCV 102,140,141\nMvav 196,160,76\n"
"MU 142,142,87\nMU 143,143,87\nMvav 204,164,76\nRa 148,2,5120\nCV 103,142,143\nMU 144,144,86\nWL 8\nMU 145,145,86\nCV 104,144,145\nRa 152,2,6144\nMU 146,146,86\nMU 147,147,86\n"
"Mvav 212,168,76\nD1 1,16,46,1024\nCV 105,146,147\nMvav 220,172,76\nRa 156,2,7168\nMU 148,148,87\nMU 149,149,87\nCV 108,148,149\nMU 150,150,87\nMU 151,151,87\nRa 160,2,8192\n"
"CV 109,150,151\nMU 152,152,86\nMvav 228,176,76\nMU 153,153,86\nCV 106,152,153\nMvav 236,180,76\nRa 164,2,9216\nMU 154,154,86\nMU 155,155,86\nWL 8\nCV 107,154,155\nMU 156,156,87\n"
"Ra 168,2,10240\nMU 157,157,87\nCV 110,156,157\nMvav 244,184,76\nD1 1,16,46,2048\nMU 158,158,87\nMvav 252,188,76\nRa 172,2,11264\nMU 159,159,87\nCV 111,158,159\nMU 160,160,86\n"
"MU 161,161,86\nZvav 128,128,96\nRa 176,2,12288\nCV 112,160,161\nMU 162,162,86\nMvav 128,132,104\nMU 163,163,86\nCV 113,162,163\nZvav 132,132,108\nRa 180,2,13312\nMU 164,164,87\n"
"Mvav 132,128,100\ns_waitcnt vmcnt(15) lgkmcnt(8)\nMU 165,165,87\nZvav 140,136,100\nRa 184,2,14336\nCV 116,164,165\nMU 166,166,87\nMvav 140,140,108\nD1 1,16,46,3072\nMU 167,167,87\n"
"Zvav 136,140,104\nRa 188,2,15360\ns_barrier\nMvav 136,136,96\nCV 117,166,167\nMU 168,168,86\nMU 169,169,86\nZvav 144,144,96\nRa 128,2,32768\nCV 114,168,169\nMvav 144,148,104\n"
"MU 170,170,86\nMU 171,171,86\nbuffer_store_dword v25, off, s[16:19], s54 offset:512 sc0 sc1\nZvav 148,148,108\nRa 132,2,33792\nCV 115,170,171\nMU 172,172,87\nMvav 148,144,100\n"
"WL 8\ns_add_u32 m0, s89, 49152\ns_add_u32 s46, s96, 1179648\nZvav 156,152,100\nRa 136,2,34816\nMU 173,173,87\nMvav 156,156,108\nD1 1,16,46,0\nCV 118,172,173\nMU 174,174,87\n"
"Zvav 152,156,104\nRa 140,2,35840\nMU 175,175,87\nMvav 152,152,96\nCV 119,174,175\nMU 176,176,86\nZvav 64,160,96\nRa 144,2,36864\nMU 177,177,86\nCV 120,176,177\nMvav 64,164,104\n"
"MU 178,178,86\nMU 179,179,86\nZvav 68,164,108\nRa 148,2,37888\nCV 121,178,179\nMvav 68,160,100\nWL 8\nMU 180,180,87\nZvav 76,168,100\nRa 152,2,38912\nMU 181,181,87\n"
"CV 124,180,181\nMvav 76,172,108\nD1 1,16,46,1024\nMU 182,182,87\nZvav 72,172,104\nRa 156,2,39936\nMU 183,183,87\nMvav 72,168,96\nCV 125,182,183\nMU 184,184,86\nMU 185,185,86\n"
"Zvav 4,176,96\nRa 160,2,40960\nCV 122,184,185\nMvav 4,180,104\nMU 186,186,86\nMU 187,187,86\nZvav 8,180,108\nRa 164,2,41984\nCV 123,186,187\nMU 188,188,87\nMvav 8,176,100\nWL 8\n"
"MU 189,189,87\nZvav 16,184,100\nRa 168,2,43008\nCV 126,188,189\nMvav 16,188,108\nD1 1,16,46,2048\nMU 190,190,87\nMU 191,191,87\nZvav 12,188,104\nRa 172,2,44032\nCV 127,190,191\n"
"Mvav 12,184,96\nMU 192,192,86\nMU 193,193,86\nMvav 128,128,112\nRa 176,2,45056\nCV 32,192,193\nMU 194,194,86\nMvav 128,132,120\nMU 195,195,86\nCV 33,194,195\nMvav 132,132,124\n"
"Ra 180,2,46080\nMU 196,196,87\nMvav 132,128,116\nWL 8\nMU 197,197,87\nMvav 140,136,116\nRa 184,2,47104\nCV 36,196,197\nMU 198,198,87\nMvav 140,140,124\nD1 1,16,46,3072\n"
"MU 199,199,87\nMvav 136,140,120\nRa 188,2,48128\nCV 37,198,199\nMvav 136,136,112\nMU 200,200,86\nMU 201,201,86\nCV 34,200,201\nMvav 144,144,112\nRa 128,2,49152\nMU 202,202,86\n"
"Mvav 144,148,120\nMU 203,203,86\nCV 35,202,203\nMvav 148,148,124\nRa 132,2,50176\nMU 204,204,87\nMU 205,205,87\nMvav 148,144,116\nWL 8\ns_add_u32 m0, s89, 147456\n"
"s_add_u32 s46, s96, 1245184\nMvav 156,152,116\nRa 136,2,51200\nCV 38,204,205\nMvav 156,156,124\nD1 1,16,46,0\nMU 206,206,87\nMU 207,207,87\nMvav 152,156,120\nRa 140,2,52224\n"
"CV 39,206,207\nMvav 152,152,112\nMU 208,208,86\nMU 209,209,86\nMvav 64,160,112\nRa 144,2,53248\nCV 40,208,209\nMU 210,210,86\nMvav 64,164,120\nMU 211,211,86\nCV 41,210,211\n"
"Mvav 68,164,124\nRa 148,2,54272\nMU 212,212,87\nMvav 68,160,116\nWL 8\nMU 213,213,87\nMvav 76,168,116\nRa 152,2,55296\nCV 44,212,213\nMU 214,214,87\nMvav 76,172,124\n"
"D1 1,16,46,1024\nMU 215,215,87\nMvav 72,172,120\nRa 156,2,56320\nCV 45,214,215\nMvav 72,168,112\nMU 216,216,86\nMU 217,217,86\nCV 42,216,217\nMvav 4,176,112\nRa 160,2,57344\n"
"MU 218,218,86\nMvav 4,180,120\nMU 219,219,86\nCV 43,218,219\nMvav 8,180,124\nRa 164,2,58368\nMU 220,220,87\nMU 221,221,87\nMvav 8,176,116\nWL 8\nCV 46,220,221\nMvav 16,184,116\n"
"Ra 168,2,59392\nMU 222,222,87\nMvav 16,188,124\nD1 1,16,46,2048\nMU 223,223,87\nCV 47,222,223\nMvav 12,188,120\nRa 172,2,60416\nMU 224,224,86\nMvav 12,184,112\nMU 225,225,86\n"
"CV 48,224,225\nMvav 128,128,32\nRa 176,2,61440\nMU 226,226,86\nMU 227,227,86\nMvav 128,132,40\nCV 49,226,227\nMU 228,228,87\nMvav 132,132,44\nRa 180,2,62464\nMU 229,229,87\n"
"Mvav 132,128,36\ns_waitcnt vmcnt(16) lgkmcnt(8)\nCV 52,228,229\nMvav 140,136,36\nRa 184,2,63488\nMU 230,230,87\nMU 231,231,87\nMvav 140,140,44\nD1 1,16,46,3072\nCV 53,230,231\n"
"Mvav 136,140,40\nRa 188,2,64512\ns_barrier\nMvav 136,136,32\nMU 232,232,86\nMU 233,233,86\nCV 50,232,233\nMvav 144,144,32\nRa 128,3,0\nMU 234,234,86\nMvav 144,148,40\n"
"MU 235,235,86\nCV 51,234,235\nMvav 148,148,44\nRa 132,3,1024\nMU 236,236,87\nMU 237,237,87\nMvav 148,144,36\nWL 8\ns_add_u32 m0, s89, 81920\ns_add_u32 s46, s96, 1277952\n"
"Mvav 156,152,36\nRa 136,3,2048\nCV 54,236,237\nMvav 156,156,44\nD1 1,16,46,0\nMU 238,238,87\nMU 239,239,87\nMvav 152,156,40\nRa 140,3,3072\nCV 55,238,239\nMvav 152,152,32\n"
"MU 240,240,86\nMU 241,241,86\nMvav 64,160,32\nRa 144,3,4096\nCV 56,240,241\nMU 242,242,86\nMvav 64,164,40\nMU 243,243,86\nCV 57,242,243\nMvav 68,164,44\nRa 148,3,5120\n"
"MU 244,244,87\nMvav 68,160,36\nWL 8\nMU 245,245,87\nMvav 76,168,36\nRa 152,3,6144\nCV 60,244,245\nMU 246,246,87\nMvav 76,172,44\nD1 1,16,46,1024\nMU 247,247,87\nMvav 72,172,40\n"
"Ra 156,3,7168\nCV 61,246,247\nMvav 72,168,32\nMU 248,248,86\nMU 249,249,86\nCV 58,248,249\nMvav 4,176,32\nRa 160,3,8192\nMU 250,250,86\nMvav 4,180,40\nMU 251,251,86\n"
"CV 59,250,251\nMvav 8,180,44\nRa 164,3,9216\nMU 252,252,87\nMU 253,253,87\nMvav 8,176,36\nWL 8\nCV 62,252,253\nMvav 16,184,36\nRa 168,3,10240\nMU 254,254,87\nMvav 16,188,44\n"
"D1 1,16,46,2048\nMU 255,255,87\nCV 63,254,255\nMvav 12,188,40\nRa 172,3,11264\nMvav 12,184,32\nMvav 128,128,48\nRa 176,3,12288\nMvav 128,132,56\nMvav 132,132,60\nRa 180,3,13312\n"
"Mvav 132,128,52\nWL 8\nMvav 140,136,52\nRa 184,3,14336\nMvav 140,140,60\nD1 1,16,46,3072\nMvav 136,140,56\nRa 188,3,15360\nMvav 136,136,48\nMvav 144,144,48\nRa 128,3,16384\n"
"Mvav 144,148,56\nbuffer_load_dword v27, off, s[16:19], s60 offset:512 sc0 sc1\nMvav 148,148,60\nRa 132,3,17408\nCV 132,132,133\nMvav 148,144,52\nWL 8\ns_add_u32 m0, s89, 65536\n"
"s_add_u32 s46, s96, 1310720\nMvav 156,152,52\nRa 136,3,18432\nCV 133,134,135\nMvav 156,156,60\nD1 1,16,46,0\nCV 134,140,141\nMvav 152,156,56\nRa 140,3,19456\nCV 135,142,143\n"
"Mvav 152,152,48\ns_add_u32 s5, s94, 0\nSv 132,1,16,5,1024\nMvav 64,160,48\nRa 144,3,20480\nMvav 64,164,56\nMvav 68,164,60\nRa 148,3,21504\nMvav 68,160,52\nWL 8\nCV 128,128,129\n"
"Mvav 76,168,52\nRa 152,3,22528\nCV 129,130,131\nMvav 76,172,60\nD1 1,16,46,1024\nCV 130,136,137\nMvav 72,172,56\nRa 156,3,23552\nCV 131,138,139\nMvav 72,168,48\nSv 128,1,16,5,0\n"
"Mvav 4,176,48\nRa 160,3,24576\nMvav 4,180,56\nMvav 8,180,60\nRa 164,3,25600\nMvav 8,176,52\nWL 6\nCV 148,148,149\nMvav 16,184,52\nRa 168,3,26624\nCV 149,150,151\nMvav 16,188,60\n"
"D1 1,16,46,2048\nCV 150,156,157\nMvav 12,188,56\nRa 172,3,27648\nCV 151,158,159\nMvav 12,184,48\ns_add_u32 s5, s94, 131072\nSv 148,1,16,5,1024\nZvav 176,128,96\nRa 176,3,28672\n"
"Mvav 176,132,104\nMvav 176,136,112\nRa 180,3,29696\nMvav 176,140,120\ns_waitcnt vmcnt(15) lgkmcnt(6)\nCV 144,144,145\nZvav 180,140,124\nRa 184,3,30720\nCV 145,146,147\n"
"Mvav 180,136,116\nD1 1,16,46,3072\nCV 146,152,153\nMvav 180,132,108\nRa 188,3,31744\ns_barrier\nMvav 180,128,100\nCV 147,154,155\nSv 144,1,16,5,0\nMvav 180,144,36\nRa 128,3,32768\n"
"Mvav 180,148,44\nMvav 180,152,52\nRa 132,3,33792\nMvav 180,156,60\nWL 6\ns_add_u32 m0, s89, 98304\ns_add_u32 s46, s96, 1343488\nMvav 176,156,56\nRa 136,3,34816\nCV 68,68,69\n"
"Mvav 176,152,48\nD1 1,16,46,0\nCV 69,70,71\nMvav 176,148,40\nRa 140,3,35840\nCV 70,76,77\nMvav 176,144,32\nCV 71,78,79\ns_add_u32 s5, s94, 262144\nZvav 184,160,96\nRa 144,3,36864\n"
"Sv 68,1,16,5,1024\nMvav 184,164,104\nMvav 184,168,112\nRa 148,3,37888\nMvav 184,172,120\nWL 6\nZvav 188,172,124\nRa 152,3,38912\nCV 64,64,65\nMvav 188,168,116\nD1 1,16,46,1024\n"
"CV 65,66,67\nMvav 188,164,108\nRa 156,3,39936\nCV 66,72,73\nMvav 188,160,100\nCV 67,74,75\nSv 64,1,16,5,0\nMvav 188,176,36\nRa 160,3,40960\nMvav 188,180,44\nMvav 188,184,52\n"
"Ra 164,3,41984\nMvav 188,188,60\nWL 6\nCV 8,8,9\nMvav 184,188,56\nRa 168,3,43008\nCV 9,10,11\nMvav 184,184,48\nD1 1,16,46,2048\nCV 10,16,17\nMvav 184,180,40\nRa 172,3,44032\n"
"CV 11,18,19\nMvav 184,176,32\ns_add_u32 s5, s94, 393216\nSv 8,1,16,5,1024\nZvav 192,128,96\nRa 176,3,45056\nv_lshrrev_b32 v88, 8, v1\nMvav 192,132,104\nv_lshlrev_b32 v89, 5, v88\n"
"Mvav 192,136,112\nRa 180,3,46080\nMvav 192,140,120\nWL 6\nCV 4,4,5\nZvav 196,140,124\nRa 184,3,47104\nv_bfe_u32 v90, v1, 4, 4\nMvav 196,136,116\nD1 1,16,46,3072\nCV 5,6,7\n"
"Mvav 196,132,108\nRa 188,3,48128\nCV 6,12,13\nMvav 196,128,100\nv_mul_u32_u24 v91, 896, v90\nCV 7,14,15\nMvav 196,144,36\nRa 128,3,49152\nv_lshl_add_u32 v91, v88, 4, v91\nWV 15\n"
"v_cmp_ne_u32 vcc, s15, v27\ns_nop 0\ns_cbranch_vccnz .S400\n.S401:\nMvav 196,148,44\ns_add_u32 s4, s92, 0\nSv 4,1,16,5,0\nMvav 196,152,52\nRa 132,3,50176\nMvav 196,156,60\nWL 6\n"
"s_add_u32 m0, s89, 114688\ns_add_u32 s46, s96, 1376256\nbuffer_load_dwordx4 v[208:211], v1, s[16:19], s4 offen offset:0 sc0 sc1\nMvav 192,156,56\nRa 136,3,51200\n"
"v_add_u32 v90, s21, v90\nMvav 192,152,48\nD1 1,16,46,0\nCV 180,180,181\nMvav 192,148,40\nRa 140,3,52224\nCV 181,182,183\nMvav 192,144,32\n"
"buffer_load_dwordx4 v[212:215], v1, s[16:19], s4 offen offset:1024 sc0 sc1\nCV 182,188,189\nZvav 200,160,96\nRa 144,3,53248\nv_lshlrev_b32 v88, 3, v90\nMvav 200,164,104\n"
"v_add_u32 v94, 64, v90\nCV 183,190,191\nMvav 200,168,112\nRa 148,3,54272\ns_add_u32 s5, s94, 524288\nMvav 200,172,120\nWL 6\nSv 180,1,16,5,1024\nZvav 204,172,124\nRa 152,3,55296\n"
"s_mov_b32 s63, 0xc040000\nMvav 204,168,116\nD1 1,16,46,1024\nMvav 204,164,108\nRa 156,3,56320\nMvav 204,160,100\nv_lshrrev_b32 v92, 8, v1\nCV 176,176,177\nMvav 204,176,36\n"
"Ra 160,3,57344\nv_bfe_u32 v93, v1, 6, 2\nMvav 204,180,44\nv_cmp_eq_u32 vcc, v92, v93\nv_and_b32 v92, 16, v1\nv_mov_b32 v93, 0x3f80\nv_cndmask_b32 v93, 0, v93, vcc\n"
"Mvav 204,184,52\nRa 164,3,58368\nCV 177,178,179\nMvav 204,188,60\nWL 6\nCV 178,184,185\nMvav 200,188,56\nRa 168,3,59392\nv_lshlrev_b32 v93, v92, v93\nMvav 200,184,48\n"
"D1 1,16,46,2048\nCV 179,186,187\nMvav 200,180,40\nRa 172,3,60416\nSv 176,1,16,5,0\nMvav 200,176,32\nv_and_b32 v92, 32, v1\nv_cmp_eq_u32 vcc, 0, v92\nv_mov_b32 v4, 0\n"
"v_mov_b32 v5, 0\nv_cndmask_b32 v6, 0, v93, vcc\nZvav 160,128,96\nRa 176,3,61440\nv_sub_u32 v7, v93, v6\nMvav 160,132,104\nv_mov_b32 v8, 0\nMvav 160,136,112\nRa 180,3,62464\n"
"CV 196,196,197\nMvav 160,140,120\nWL 6\nCV 197,198,199\nZvav 164,140,124\nRa 184,3,63488\nv_mov_b32 v9, 0\nMvav 164,136,116\nD1 1,16,46,3072\nCV 198,204,205\nMvav 164,132,108\n"
"Ra 188,3,64512\ns_barrier\nMvav 164,128,100\nCV 199,206,207\ns_add_u32 s5, s94, 655360\nMvav 164,144,36\nRa 128,0,0\nSv 196,1,16,5,1024\nMvav 164,148,44\n"
"s_add_u32 s4, s92, 131072\nMvav 164,152,52\nRa 132,0,1024\nMvav 164,156,60\nWL 6\ns_add_u32 m0, s89, 0\ns_add_u32 s46, s96, 1409024\n"
"buffer_load_dwordx4 v[216:219], v1, s[16:19], s4 offen offset:0 sc0 sc1\nMvav 160,156,56\nRa 136,0,2048\nCV 192,192,193\nMvav 160,152,48\nD1 1,16,46,0\nCV 193,194,195\n"
"Mvav 160,148,40\nRa 140,0,3072\nCV 194,200,201\nMvav 160,144,32\nbuffer_load_dwordx4 v[220:223], v1, s[16:19], s4 offen offset:1024 sc0 sc1\nCV 195,202,203\nZvav 168,160,96\n"
"Ra 144,0,4096\nWV 9\nv_max_u32 v21, v211, v215\nv_cmp_eq_u32 vcc, -1, v21\ns_nop 0\ns_cbranch_vccnz .S376\n.S377:\nMvav 168,164,104\nv_lshlrev_b32 v176, 16, v208\nSv 192,1,16,5,0\n"
"Mvav 168,168,112\nRa 148,0,5120\nMvav 168,172,120\nWL 6\nv_and_b32 v177, 0xffff0000, v208\nZvav 172,172,124\nRa 152,0,6144\nMvav 172,168,116\nD1 1,16,46,1024\nMvav 172,164,108\n"
"Ra 156,0,7168\nv_lshlrev_b32 v178, 16, v209\nMvav 172,160,100\nv_and_b32 v179, 0xffff0000, v209\nMvav 172,176,36\nRa 160,0,8192\nv_lshlrev_b32 v180, 16, v212\nMvav 172,180,44\n"
"v_and_b32 v181, 0xffff0000, v212\nMvav 172,184,52\nRa 164,0,9216\nMvav 172,188,60\nWL 6\nv_lshlrev_b32 v182, 16, v213\nMvav 168,188,56\nRa 168,0,10240\nMvav 168,184,48\n"
"D1 1,16,46,2048\nMvav 168,180,40\nRa 172,0,11264\nv_and_b32 v183, 0xffff0000, v213\nMvav 168,176,32\nv_lshlrev_b32 v184, 16, v210\nMvav 176,128,96\nRa 176,0,12288\n"
"v_and_b32 v185, 0xffff0000, v210\nMvav 176,132,104\nv_lshlrev_b32 v186, 16, v211\nCV 164,164,165\nMvav 176,136,112\nRa 180,0,13312\nCV 165,166,167\nMvav 176,140,120\nWL 6\n"
"CV 166,172,173\nMvav 180,140,124\nRa 184,0,14336\nCV 167,174,175\nMvav 180,136,116\nD1 1,16,46,3072\ns_add_u32 s5, s94, 786432\nMvav 180,132,108\nRa 188,0,15360\n"
"v_and_b32 v187, 0xffff0000, v211\nMvav 180,128,100\nv_lshlrev_b32 v188, 16, v214\nSv 164,1,16,5,1024\nMvav 180,144,36\nRa 128,0,16384\nMvav 180,148,44\n"
"v_and_b32 v189, 0xffff0000, v214\ns_add_u32 s4, s92, 262144\nMvav 180,152,52\nRa 132,0,17408\nMvav 180,156,60\nWL 6\nv_lshlrev_b32 v190, 16, v215\n"
"buffer_load_dwordx4 v[224:227], v1, s[16:19], s4 offen offset:0 sc0 sc1\nMvav 176,156,56\nRa 136,0,18432\nCV 160,160,161\nMvav 176,152,48\nv_and_b32 v191, 0xffff0000, v215\n"
"CV 161,162,163\nMvav 176,148,40\nRa 140,0,19456\nCV 162,168,169\nMvav 176,144,32\nbuffer_load_dwordx4 v[228:231], v1, s[16:19], s4 offen offset:1024 sc0 sc1\nCV 163,170,171\n"
"Mvav 184,160,96\nRa 144,0,20480\nWV 7\nv_max_u32 v21, v219, v223\nv_cmp_eq_u32 vcc, -1, v21\ns_nop 0\ns_cbranch_vccnz .S379\n.S380:\nMvav 184,164,104\n"
"v_lshlrev_b32 v192, 16, v216\nSv 160,1,16,5,0\nMvav 184,168,112\nRa 148,0,21504\nMvav 184,172,120\nWL 6\nMvav 188,172,124\nRa 152,0,22528\nv_and_b32 v193, 0xffff0000, v216\n"
"Mvav 188,168,116\nv_lshlrev_b32 v194, 16, v217\nMvav 188,164,108\nRa 156,0,23552\nMvav 188,160,100\nv_and_b32 v195, 0xffff0000, v217\nMvav 188,176,36\nRa 160,0,24576\n"
"v_lshlrev_b32 v196, 16, v220\nMvav 188,180,44\nv_and_b32 v197, 0xffff0000, v220\nMvav 188,184,52\nRa 164,0,25600\nMvav 188,188,60\nWL 6\nMvav 184,188,56\nRa 168,0,26624\n"
"v_lshlrev_b32 v198, 16, v221\nMvav 184,184,48\nv_and_b32 v199, 0xffff0000, v221\nMvav 184,180,40\nRa 172,0,27648\nMvav 184,176,32\nv_lshlrev_b32 v200, 16, v218\nMvav 192,128,96\n"
"Ra 176,0,28672\nv_and_b32 v201, 0xffff0000, v218\nMvav 192,132,104\nv_lshlrev_b32 v202, 16, v219\nCV 180,180,181\nMvav 192,136,112\nRa 180,0,29696\nCV 181,182,183\n"
"Mvav 192,140,120\nWL 6\nCV 182,188,189\nMvav 196,140,124\nRa 184,0,30720\nCV 183,190,191\nMvav 196,136,116\nv_and_b32 v203, 0xffff0000, v219\ns_add_u32 s5, s94, 917504\n"
"Mvav 196,132,108\nRa 188,0,31744\ns_barrier\nMvav 196,128,100\nv_lshlrev_b32 v204, 16, v222\nSv 180,1,16,5,1024\nMvav 196,144,36\nRa 128,2,16384\nv_and_b32 v205, 0xffff0000, v222\n"
"Mvav 196,148,44\nv_lshlrev_b32 v206, 16, v223\ns_add_u32 s4, s92, 393216\nMvav 196,152,52\nRa 132,2,17408\nMvav 196,156,60\nWL 6\n"
"buffer_load_dwordx4 v[208:211], v1, s[16:19], s4 offen offset:0 sc0 sc1\nMvav 192,156,56\nRa 136,2,18432\nCV 176,176,177\nMvav 192,152,48\nv_and_b32 v207, 0xffff0000, v223\n"
"CV 177,178,179\nMvav 192,148,40\nRa 140,2,19456\nCV 178,184,185\nMvav 192,144,32\nbuffer_load_dwordx4 v[212:215], v1, s[16:19], s4 offen offset:1024 sc0 sc1\nCV 179,186,187\n"
"Mvav 200,160,96\nRa 144,2,20480\nWV 4\nv_max_u32 v21, v227, v231\nv_cmp_eq_u32 vcc, -1, v21\ns_nop 0\ns_cbranch_vccnz .S382\n.S383:\nMvav 200,164,104\n"
"v_lshlrev_b32 v160, 16, v224\nSv 176,1,16,5,0\nMvav 200,168,112\nRa 148,2,21504\nMvav 200,172,120\nWL 6\nMvav 204,172,124\nRa 152,2,22528\nv_and_b32 v161, 0xffff0000, v224\n"
"Mvav 204,168,116\nv_lshlrev_b32 v162, 16, v225\nMvav 204,164,108\nRa 156,2,23552\nMvav 204,160,100\nv_and_b32 v163, 0xffff0000, v225\nMvav 204,176,36\nRa 160,2,24576\n"
"v_lshlrev_b32 v164, 16, v228\nMvav 204,180,44\nv_and_b32 v165, 0xffff0000, v228\nMvav 204,184,52\nRa 164,2,25600\nMvav 204,188,60\nWL 6\nMvav 200,188,56\nRa 168,2,26624\n"
"v_lshlrev_b32 v166, 16, v229\nMvav 200,184,48\nv_and_b32 v167, 0xffff0000, v229\nMvav 200,180,40\nRa 172,2,27648\nMvav 200,176,32\nv_lshlrev_b32 v168, 16, v226\nMvav 160,128,96\n"
"Ra 176,2,28672\nv_and_b32 v169, 0xffff0000, v226\nMvav 160,132,104\nv_lshlrev_b32 v170, 16, v227\nCV 196,196,197\nMvav 160,136,112\nRa 180,2,29696\nCV 197,198,199\n"
"Mvav 160,140,120\nWL 6\nCV 198,204,205\nMvav 164,140,124\nRa 184,2,30720\nv_and_b32 v24, 0x70, v1\nMvav 164,136,116\nv_and_b32 v171, 0xffff0000, v227\nCV 199,206,207\n"
"Mvav 164,132,108\nRa 188,2,31744\ns_add_u32 s5, s94, 1048576\nMvav 164,128,100\nv_lshlrev_b32 v172, 16, v230\nv_lshlrev_b32 v24, 5, v24\nMvav 164,144,36\nRa 128,2,0\n"
"v_and_b32 v173, 0xffff0000, v230\nMvav 164,148,44\nv_lshlrev_b32 v174, 16, v231\ns_add_u32 s4, s92, 524288\nMvav 164,152,52\nRa 132,2,1024\n"
"buffer_load_dword v27, v24, s[16:19], s41 offen sc0 sc1\nMvav 164,156,60\nWL 6\nbuffer_load_dwordx4 v[216:219], v1, s[16:19], s4 offen offset:0 sc0 sc1\nMvav 160,156,56\n"
"Ra 136,2,2048\nSv 196,1,16,5,1024\nMvav 160,152,48\nv_and_b32 v175, 0xffff0000, v231\nMvav 160,148,40\nRa 140,2,3072\nMvav 160,144,32\n"
"buffer_load_dwordx4 v[220:223], v1, s[16:19], s4 offen offset:1024 sc0 sc1\nMvav 168,160,96\nRa 144,2,4096\nWV 5\nv_max_u32 v21, v211, v215\nv_cmp_eq_u32 vcc, -1, v21\ns_nop 0\n"
"s_cbranch_vccnz .S385\n.S386:\nMvav 168,164,104\nv_lshlrev_b32 v176, 16, v208\nCV 192,192,193\nMvav 168,168,112\nRa 148,2,5120\nCV 193,194,195\nMvav 168,172,120\nWL 6\n"
"CV 194,200,201\nMvav 172,172,124\nRa 152,2,6144\nv_and_b32 v177, 0xffff0000, v208\nMvav 172,168,116\nv_lshlrev_b32 v178, 16, v209\nCV 195,202,203\nMvav 172,164,108\nRa 156,2,7168\n"
"Sv 192,1,16,5,0\nMvav 172,160,100\nv_and_b32 v179, 0xffff0000, v209\nMvav 172,176,36\nRa 160,2,8192\nv_lshlrev_b32 v180, 16, v212\nMvav 172,180,44\n"
"v_and_b32 v181, 0xffff0000, v212\nMvav 172,184,52\nRa 164,2,9216\nMvav 172,188,60\nWL 6\nMvav 168,188,56\nRa 168,2,10240\nv_lshlrev_b32 v182, 16, v213\nMvav 168,184,48\n"
"v_and_b32 v183, 0xffff0000, v213\nMvav 168,180,40\nRa 172,2,11264\nMvav 168,176,32\nv_lshlrev_b32 v184, 16, v210\nMvav 176,128,96\nRa 176,2,12288\n"
"v_and_b32 v185, 0xffff0000, v210\nMvav 176,132,104\nv_lshlrev_b32 v186, 16, v211\nCV 164,164,165\nMvav 176,136,112\nRa 180,2,13312\nCV 165,166,167\nMvav 176,140,120\nWL 6\n"
"CV 166,172,173\nMvav 180,140,124\nRa 184,2,14336\nCV 167,174,175\nMvav 180,136,116\nv_and_b32 v187, 0xffff0000, v211\ns_add_u32 s5, s94, 1179648\nMvav 180,132,108\nRa 188,2,15360\n"
"s_barrier\nMvav 180,128,100\nv_lshlrev_b32 v188, 16, v214\nSv 164,1,16,5,1024\nMvav 180,144,36\nRa 128,2,32768\nv_and_b32 v189, 0xffff0000, v214\nMvav 180,148,44\n"
"v_lshlrev_b32 v190, 16, v215\ns_add_u32 s4, s92, 655360\nMvav 180,152,52\nRa 132,2,33792\nMvav 180,156,60\nWL 6\n"
"buffer_load_dwordx4 v[224:227], v1, s[16:19], s4 offen offset:0 sc0 sc1\nMvav 176,156,56\nRa 136,2,34816\nCV 160,160,161\nMvav 176,152,48\nv_and_b32 v191, 0xffff0000, v215\n"
"CV 161,162,163\nMvav 176,148,40\nRa 140,2,35840\nCV 162,168,169\nMvav 176,144,32\nbuffer_load_dwordx4 v[228:231], v1, s[16:19], s4 offen offset:1024 sc0 sc1\nCV 163,170,171\n"
"Mvav 184,160,96\nRa 144,2,36864\nWV 4\nv_max_u32 v21, v219, v223\nv_cmp_eq_u32 vcc, -1, v21\ns_nop 0\ns_cbranch_vccnz .S388\n.S389:\nMvav 184,164,104\n"
"v_lshlrev_b32 v192, 16, v216\nSv 160,1,16,5,0\nMvav 184,168,112\nRa 148,2,37888\nMvav 184,172,120\nWL 6\nMvav 188,172,124\nRa 152,2,38912\nv_and_b32 v193, 0xffff0000, v216\n"
"Mvav 188,168,116\nv_lshlrev_b32 v194, 16, v217\nMvav 188,164,108\nRa 156,2,39936\nMvav 188,160,100\nv_and_b32 v195, 0xffff0000, v217\nMvav 188,176,36\nRa 160,2,40960\n"
"v_lshlrev_b32 v196, 16, v220\nMvav 188,180,44\nv_and_b32 v197, 0xffff0000, v220\nMvav 188,184,52\nRa 164,2,41984\nMvav 188,188,60\nWL 6\nMvav 184,188,56\nRa 168,2,43008\n"
"v_lshlrev_b32 v198, 16, v221\nMvav 184,184,48\nv_and_b32 v199, 0xffff0000, v221\nMvav 184,180,40\nRa 172,2,44032\nMvav 184,176,32\nv_lshlrev_b32 v200, 16, v218\nMvav 192,128,96\n"
"Ra 176,2,45056\nv_and_b32 v201, 0xffff0000, v218\nMvav 192,132,104\nv_lshlrev_b32 v202, 16, v219\nCV 180,180,181\nMvav 192,136,112\nRa 180,2,46080\nCV 181,182,183\n"
"Mvav 192,140,120\nWL 6\nCV 182,188,189\nMvav 196,140,124\nRa 184,2,47104\nCV 183,190,191\nMvav 196,136,116\nv_and_b32 v203, 0xffff0000, v219\ns_add_u32 s5, s94, 1310720\n"
"Mvav 196,132,108\nRa 188,2,48128\nSv 180,1,16,5,1024\nMvav 196,128,100\nv_lshlrev_b32 v204, 16, v222\nMvav 196,144,36\nRa 128,2,49152\nv_and_b32 v205, 0xffff0000, v222\n"
"Mvav 196,148,44\nv_lshlrev_b32 v206, 16, v223\ns_add_u32 s4, s92, 786432\nMvav 196,152,52\nRa 132,2,50176\nCV 176,176,177\nMvav 196,156,60\nWL 6\n"
"buffer_load_dwordx4 v[208:211], v1, s[16:19], s4 offen offset:0 sc0 sc1\nMvav 192,156,56\nRa 136,2,51200\nCV 177,178,179\nMvav 192,152,48\nv_and_b32 v207, 0xffff0000, v223\n"
"CV 178,184,185\nMvav 192,148,40\nRa 140,2,52224\nCV 179,186,187\nMvav 192,144,32\nbuffer_load_dwordx4 v[212:215], v1, s[16:19], s4 offen offset:1024 sc0 sc1\nSv 176,1,16,5,0\n"
"Mvav 200,160,96\nRa 144,2,53248\nWV 5\nv_max_u32 v21, v227, v231\nv_cmp_eq_u32 vcc, -1, v21\ns_nop 0\ns_cbranch_vccnz .S391\n.S392:\nMvav 200,164,104\n"
"v_lshlrev_b32 v160, 16, v224\nv_xor_b32 v27, s15, v27\nv_and_b32 v26, 0xffffdfff, v27\nv_cmp_ne_u32 s[64:65], 0, v27\nv_cmp_ne_u32 vcc, 0, v26\ns_nop 0\ns_cbranch_vccnz .S403\n"
".S404:\nMvav 200,168,112\nRa 148,2,54272\ns_bitcmp1_b32 s64, 0\ns_cselect_b32 s2, 0x2000, 0\nMvav 200,172,120\nWL 6\nMvav 204,172,124\nRa 152,2,55296\n"
"v_and_b32 v161, 0xffff0000, v224\nMvav 204,168,116\nv_lshlrev_b32 v162, 16, v225\nMvav 204,164,108\nRa 156,2,56320\ns_xor_b32 s2, s2, s20\nMvav 204,160,100\n"
"v_and_b32 v163, 0xffff0000, v225\nMvav 204,176,36\nRa 160,2,57344\nv_lshlrev_b32 v164, 16, v228\nMvav 204,180,44\nv_and_b32 v165, 0xffff0000, v228\nMvav 204,184,52\n"
"Ra 164,2,58368\nMvav 204,188,60\nWL 6\ns_add_u32 s24, s2, 0x0\nMvav 200,188,56\nRa 168,2,59392\nv_lshlrev_b32 v166, 16, v229\nMvav 200,184,48\nv_and_b32 v167, 0xffff0000, v229\n"
"s_bitcmp1_b32 s64, 1\ns_cselect_b32 s2, 0x2000, 0\nMvav 200,180,40\nRa 172,2,60416\nMvav 200,176,32\nv_lshlrev_b32 v168, 16, v226\nMvav 160,128,96\nRa 176,2,61440\n"
"v_and_b32 v169, 0xffff0000, v226\nMvav 160,132,104\nv_lshlrev_b32 v170, 16, v227\nCV 196,196,197\nMvav 160,136,112\nRa 180,2,62464\nCV 197,198,199\nMvav 160,140,120\nWL 6\n"
"CV 198,204,205\nMvav 164,140,124\nRa 184,2,63488\nCV 199,206,207\nMvav 164,136,116\nv_and_b32 v171, 0xffff0000, v227\ns_add_u32 s5, s94, 1441792\nMvav 164,132,108\nRa 188,2,64512\n"
"s_barrier\nMvav 164,128,100\nv_lshlrev_b32 v172, 16, v230\nSv 196,1,16,5,1024\nMvav 164,144,36\nRa 128,3,0\nv_and_b32 v173, 0xffff0000, v230\nMvav 164,148,44\n"
"v_lshlrev_b32 v174, 16, v231\nMvav 164,152,52\nRa 132,3,1024\nMvav 164,156,60\nWL 6\ns_xor_b32 s2, s2, s20\nMvav 160,156,56\nRa 136,3,2048\nCV 192,192,193\nMvav 160,152,48\n"
"v_and_b32 v175, 0xffff0000, v231\nCV 193,194,195\nMvav 160,148,40\nRa 140,3,3072\nCV 194,200,201\nMvav 160,144,32\ns_add_u32 s25, s2, 0x1800000\ns_bitcmp1_b32 s64, 2\n"
"s_cselect_b32 s2, 0x2000, 0\nMvav 168,160,96\nRa 144,3,4096\nWV 2\nv_max_u32 v21, v211, v215\nv_cmp_eq_u32 vcc, -1, v21\ns_nop 0\ns_cbranch_vccnz .S394\n.S395:\nMvav 168,164,104\n"
"v_lshlrev_b32 v176, 16, v208\nCV 195,202,203\nMvav 168,168,112\nRa 148,3,5120\nSv 192,1,16,5,0\nMvav 168,172,120\nWL 6\nMvav 172,172,124\nRa 152,3,6144\n"
"v_and_b32 v177, 0xffff0000, v208\nMvav 172,168,116\nv_lshlrev_b32 v178, 16, v209\nMvav 172,164,108\nRa 156,3,7168\ns_xor_b32 s2, s2, s20\nMvav 172,160,100\n"
"v_and_b32 v179, 0xffff0000, v209\nMvav 172,176,36\nRa 160,3,8192\nv_lshlrev_b32 v180, 16, v212\nMvav 172,180,44\nv_and_b32 v181, 0xffff0000, v212\nMvav 172,184,52\nRa 164,3,9216\n"
"Mvav 172,188,60\nWL 6\ns_add_u32 s26, s2, 0x3000000\nMvav 168,188,56\nRa 168,3,10240\nv_lshlrev_b32 v182, 16, v213\nMvav 168,184,48\nv_and_b32 v183, 0xffff0000, v213\n"
"s_bitcmp1_b32 s64, 3\ns_cselect_b32 s2, 0x2000, 0\nMvav 168,180,40\nRa 172,3,11264\nMvav 168,176,32\nv_lshlrev_b32 v184, 16, v210\nMvav 176,128,96\nRa 176,3,12288\n"
"s_xor_b32 s2, s2, s20\nMvav 176,132,104\nv_and_b32 v185, 0xffff0000, v210\nCV 164,164,165\nMvav 176,136,112\nRa 180,3,13312\nCV 165,166,167\nMvav 176,140,120\nWL 6\n"
"CV 166,172,173\nMvav 180,140,124\nRa 184,3,14336\nCV 167,174,175\nMvav 180,136,116\nv_lshlrev_b32 v186, 16, v211\ns_add_u32 s5, s94, 1572864\nMvav 180,132,108\nRa 188,3,15360\n"
"s_add_u32 s27, s2, 0x4800000\nMvav 180,128,100\nv_and_b32 v187, 0xffff0000, v211\ns_bitcmp1_b32 s64, 4\ns_cselect_b32 s2, 0x2000, 0\nMvav 180,144,36\nv_lshlrev_b32 v188, 16, v214\n"
"Sv 164,1,16,5,1024\nMvav 180,148,44\nv_and_b32 v189, 0xffff0000, v214\nMvav 180,152,52\nv_lshlrev_b32 v190, 16, v215\nMvav 180,156,60\nWL 4\ns_xor_b32 s2, s2, s20\n"
"Mvav 176,156,56\nv_and_b32 v191, 0xffff0000, v215\nCV 160,160,161\nMvav 176,152,48\ns_add_u32 s32, s2, 0x6000000\ns_bitcmp1_b32 s64, 5\ns_cselect_b32 s2, 0x2000, 0\n"
"Mvav 176,148,40\ns_xor_b32 s2, s2, s20\nCV 161,162,163\nMvav 176,144,32\ns_add_u32 s33, s2, 0x7800000\ns_bitcmp1_b32 s64, 6\ns_cselect_b32 s2, 0x2000, 0\nMvav 184,160,96\n"
"s_xor_b32 s2, s2, s20\ns_add_u32 s34, s2, 0x9000000\nMvav 184,164,104\ns_bitcmp1_b32 s64, 7\ns_cselect_b32 s2, 0x2000, 0\nMvav 184,168,112\ns_xor_b32 s2, s2, s20\nCV 162,168,169\n"
"Mvav 184,172,120\nWL 0\nCV 163,170,171\nMvav 188,172,124\ns_add_u32 s35, s2, 0xa800000\nSv 160,1,16,5,0\nMvav 188,168,116\nMvav 188,164,108\nMvav 188,160,100\nMvav 188,176,36\n"
"Mvav 188,180,44\nMvav 188,184,52\nMvav 188,188,60\nMvav 184,188,56\nMvav 184,184,48\nMvav 184,180,40\nMvav 184,176,32\nCV 180,180,181\nCV 181,182,183\ns_nop 1\nCV 182,188,189\n"
"CV 183,190,191\ns_add_u32 s5, s94, 1703936\nSv 180,1,16,5,1024\nCV 176,176,177\nCV 177,178,179\nCV 178,184,185\nCV 179,186,187\nSv 176,1,16,5,0\ns_cmp_eq_u32 s13, 0\n"
"s_cbranch_scc1 .Z406\nbuffer_load_dwordx4 v[12:15], v91, s[28:31], s22 offen offset:0\nbuffer_load_dwordx4 v[16:19], v91, s[28:31], s23 offen offset:0\n"
"buffer_load_dwordx4 v[64:67], v89, s[16:19], s40 offen offset:0 sc0 sc1\nbuffer_load_dwordx4 v[68:71], v89, s[16:19], s40 offen offset:16 sc0 sc1\n"
"buffer_load_dwordx4 v[72:75], v89, s[16:19], s40 offen offset:1792 sc0 sc1\nbuffer_load_dwordx4 v[76:79], v89, s[16:19], s40 offen offset:1808 sc0 sc1\n"
"buffer_load_dwordx2 v[80:81], v88, s[16:19], s63 offen offset:0 sc0 sc1\nbuffer_load_dwordx2 v[82:83], v88, s[16:19], s63 offen offset:512 sc0 sc1\n"
"buffer_load_dwordx4 v[96:99], v1, s[16:19], s24 offen offset:0 sc0 sc1\nbuffer_load_dwordx4 v[100:103], v1, s[16:19], s24 offen offset:1024 sc0 sc1\n"
"buffer_load_dwordx4 v[104:107], v1, s[16:19], s25 offen offset:0 sc0 sc1\nbuffer_load_dwordx4 v[108:111], v1, s[16:19], s25 offen offset:1024 sc0 sc1\n"
"buffer_load_dwordx4 v[112:115], v1, s[16:19], s26 offen offset:0 sc0 sc1\nbuffer_load_dwordx4 v[116:119], v1, s[16:19], s26 offen offset:1024 sc0 sc1\n"
"buffer_load_dwordx4 v[120:123], v1, s[16:19], s27 offen offset:0 sc0 sc1\nbuffer_load_dwordx4 v[124:127], v1, s[16:19], s27 offen offset:1024 sc0 sc1\n"
"buffer_load_dwordx4 v[32:35], v1, s[16:19], s32 offen offset:0 sc0 sc1\nbuffer_load_dwordx4 v[36:39], v1, s[16:19], s32 offen offset:1024 sc0 sc1\n"
"buffer_load_dwordx4 v[40:43], v1, s[16:19], s33 offen offset:0 sc0 sc1\nbuffer_load_dwordx4 v[44:47], v1, s[16:19], s33 offen offset:1024 sc0 sc1\n"
"buffer_load_dwordx4 v[48:51], v1, s[16:19], s34 offen offset:0 sc0 sc1\nbuffer_load_dwordx4 v[52:55], v1, s[16:19], s34 offen offset:1024 sc0 sc1\n"
"buffer_load_dwordx4 v[56:59], v1, s[16:19], s35 offen offset:0 sc0 sc1\nbuffer_load_dwordx4 v[60:63], v1, s[16:19], s35 offen offset:1024 sc0 sc1\nWV 16\nv_xor_b32 v66, s15, v66\n"
"v_xor_b32 v70, s15, v70\nv_xor_b32 v74, s15, v74\nv_xor_b32 v78, s15, v78\nv_xor_b32 v81, s15, v81\nv_xor_b32 v83, s15, v83\nv_or3_b32 v92, v66, v70, v74\n"
"v_or3_b32 v92, v92, v78, v81\nv_or_b32 v92, v92, v83\nv_cmp_ne_u32 vcc, 0, v92\ns_nop 0\ns_cbranch_vccnz .S397\n.S398:\nv_and_b32 v92, 0x7ff, v67\nv_lshrrev_b32 v93, 16, v67\n"
"v_cmp_eq_u32 s[44:45], v92, v90\nv_cmp_eq_u32 s[46:47], v93, v90\nv_cmp_eq_u32 s[48:49], v92, v94\nv_cmp_eq_u32 s[50:51], v93, v94\nv_cndmask_b32 v12, v12, v64, s[44:45]\n"
"v_cndmask_b32 v13, v13, v65, s[44:45]\nv_cndmask_b32 v14, v14, v68, s[44:45]\nv_cndmask_b32 v15, v15, v69, s[44:45]\nv_cndmask_b32 v12, v12, v72, s[46:47]\n"
"v_cndmask_b32 v13, v13, v73, s[46:47]\nv_cndmask_b32 v14, v14, v76, s[46:47]\nv_cndmask_b32 v15, v15, v77, s[46:47]\nv_cndmask_b32 v16, v16, v64, s[48:49]\n"
"v_cndmask_b32 v17, v17, v65, s[48:49]\nv_cndmask_b32 v18, v18, v68, s[48:49]\nv_cndmask_b32 v19, v19, v69, s[48:49]\nv_cndmask_b32 v16, v16, v72, s[50:51]\n"
"v_cndmask_b32 v17, v17, v73, s[50:51]\nv_cndmask_b32 v18, v18, v76, s[50:51]\nv_cndmask_b32 v19, v19, v77, s[50:51]\nv_sub_f32 v92, 1.0, v80\nv_sub_f32 v93, 1.0, v82\nCV 92,92,93\n"
"v_and_b32 v93, 0xffff0000, v92\nv_lshlrev_b32 v92, 16, v92\nv_lshlrev_b32 v64, 16, v12\nv_and_b32 v65, 0xffff0000, v12\nMU 64,64,92\nMU 65,65,92\nv_lshlrev_b32 v66, 16, v13\n"
"v_and_b32 v67, 0xffff0000, v13\nMU 66,66,92\nMU 67,67,92\nv_lshlrev_b32 v68, 16, v14\nv_and_b32 v69, 0xffff0000, v14\nMU 68,68,92\nMU 69,69,92\nv_lshlrev_b32 v70, 16, v15\n"
"v_and_b32 v71, 0xffff0000, v15\nMU 70,70,92\nMU 71,71,92\nv_lshlrev_b32 v72, 16, v16\nv_and_b32 v73, 0xffff0000, v16\nMU 72,72,93\nMU 73,73,93\nv_lshlrev_b32 v74, 16, v17\n"
"v_and_b32 v75, 0xffff0000, v17\nMU 74,74,93\nMU 75,75,93\nv_lshlrev_b32 v76, 16, v18\nv_and_b32 v77, 0xffff0000, v18\nMU 76,76,93\nMU 77,77,93\nv_lshlrev_b32 v78, 16, v19\n"
"v_and_b32 v79, 0xffff0000, v19\nMU 78,78,93\nMU 79,79,93\ns_mov_b32 s7, 262144\nWV 0\n.Z409:\nv_max_u32 v84, v99, v103\nv_cmp_eq_u32 s[44:45], -1, v84\nv_max_u32 v84, v107, v111\n"
"v_cmp_eq_u32 s[46:47], -1, v84\nv_max_u32 v84, v115, v119\nv_cmp_eq_u32 s[48:49], -1, v84\nv_max_u32 v84, v123, v127\nv_cmp_eq_u32 s[50:51], -1, v84\nv_max_u32 v84, v35, v39\n"
"v_cmp_eq_u32 s[52:53], -1, v84\nv_max_u32 v84, v43, v47\nv_cmp_eq_u32 s[54:55], -1, v84\nv_max_u32 v84, v51, v55\nv_cmp_eq_u32 s[56:57], -1, v84\nv_max_u32 v84, v59, v63\n"
"v_cmp_eq_u32 s[58:59], -1, v84\ns_or_b64 s[0:1], s[44:45], s[46:47]\ns_or_b64 s[2:3], s[48:49], s[50:51]\ns_or_b64 s[0:1], s[0:1], s[52:53]\ns_or_b64 s[2:3], s[2:3], s[54:55]\n"
"s_or_b64 s[0:1], s[0:1], s[56:57]\ns_or_b64 s[2:3], s[2:3], s[58:59]\ns_or_b64 s[0:1], s[0:1], s[2:3]\ns_cbranch_scc1 .Z407\n.Z408:\nZvvv 136,6,96\nMvvv 136,6,104\nMvvv 136,6,112\n"
"Mvvv 136,6,120\nMvvv 136,6,32\nMvvv 136,6,40\nMvvv 136,6,48\nMvvv 136,6,56\nZvvv 140,4,96\nMvvv 140,4,104\nMvvv 140,4,112\nMvvv 140,4,120\nMvvv 140,4,32\nMvvv 140,4,40\n"
"Mvvv 140,4,48\nMvvv 140,4,56\nZvvv 144,6,100\nMvvv 144,6,108\nMvvv 144,6,116\nMvvv 144,6,124\nMvvv 144,6,36\nMvvv 144,6,44\nMvvv 144,6,52\nMvvv 144,6,60\nZvvv 148,4,100\n"
"Mvvv 148,4,108\nMvvv 148,4,116\nMvvv 148,4,124\nMvvv 148,4,36\nMvvv 148,4,44\nMvvv 148,4,52\nMvvv 148,4,60\nFM 64,80,136\nFM 65,80,137\nFM 66,80,138\nFM 67,80,139\nFM 68,80,140\n"
"FM 69,80,141\nFM 70,80,142\nFM 71,80,143\nCV 12,64,65\nCV 13,66,67\nCV 14,68,69\nCV 15,70,71\nSv 12,91,36,22,0\nFM 72,82,144\nFM 73,82,145\nFM 74,82,146\nFM 75,82,147\n"
"FM 76,82,148\nFM 77,82,149\nFM 78,82,150\nFM 79,82,151\nCV 16,72,73\nCV 17,74,75\nCV 18,76,77\nCV 19,78,79\nSv 16,91,36,23,0\n.Z406:\ns_endpgm\n.E368:\nv_mov_b32 v91, 0x40000000\n"
"v_cmp_gt_u32 vcc, 0x200, v1\nWV 4\ns_nop 0\nv_cndmask_b32 v91, v91, v1, vcc\nSv 92,91,16,65,0\ns_branch .E369\n.S276:\ns_mov_b32 s7, 262144\n.S278:\ns_lshl_b32 s5, s73, 8\n"
"s_add_u32 s5, s5, 0xc04ac00\nv_lshrrev_b32 v27, 2, v1\nbuffer_load_dword v27, v27, s[16:19], s5 offen sc0 sc1\nWV 0\nv_xor_b32 v27, s15, v27\nv_and_b32 v32, 0xffffdfff, v27\n"
"v_cmp_ne_u32 vcc, 0, v32\ns_nop 0\ns_cbranch_vccz .S277\ns_sleep 6\ns_sub_u32 s7, s7, 1\ns_cbranch_scc0 .S278\ns_branch .S277\n.T289:\ns_mov_b32 s62, 1\n"
"s_call_b64 s[80:81], .RA272\ns_branch .T290\n.T291:\ns_mov_b32 s62, 1\ns_call_b64 s[80:81], .RA272\ns_branch .T292\n.S294:\ns_mov_b32 s7, 262144\n.S296:\n"
"buffer_load_dword v27, off, s[16:19], s5 sc0 sc1\nWV 0\nv_cmp_ne_u32 vcc, s15, v27\ns_nop 0\ns_cbranch_vccz .S295\ns_sleep 6\ns_sub_u32 s7, s7, 1\ns_cbranch_scc0 .S296\n"
"s_branch .S295\n.S297:\ns_mov_b32 s7, 262144\n.S299:\nv_and_b32 v27, 0x70, v1\nbuffer_load_dword v27, v27, s[16:19], s61 offen offset:128 sc0 sc1\nWV 0\n"
"v_cmp_ne_u32 vcc, s15, v27\ns_nop 0\ns_cbranch_vccz .S298\ns_sleep 6\ns_sub_u32 s7, s7, 1\ns_cbranch_scc0 .S299\ns_branch .S298\n.S302:\ns_mov_b32 s7, 262144\n.S304:\n"
"v_and_b32 v27, 0x70, v1\nbuffer_load_dword v27, v27, s[16:19], s61 offen offset:136 sc0 sc1\nWV 0\nv_cmp_ne_u32 vcc, s15, v27\ns_nop 0\ns_cbranch_vccz .S303\ns_sleep 6\n"
"s_sub_u32 s7, s7, 1\ns_cbranch_scc0 .S304\ns_branch .S303\n.S305:\ns_mov_b32 s7, 262144\n.S307:\nv_and_b32 v27, 0x70, v1\n"
"buffer_load_dword v27, v27, s[16:19], s61 offen offset:140 sc0 sc1\nWV 0\nv_cmp_ne_u32 vcc, s15, v27\ns_nop 0\ns_cbranch_vccz .S306\ns_sleep 6\ns_sub_u32 s7, s7, 1\n"
"s_cbranch_scc0 .S307\ns_branch .S306\n.S308:\ns_mov_b32 s7, 262144\n.S310:\nv_and_b32 v27, 0x70, v1\nbuffer_load_dword v27, v27, s[16:19], s61 offen offset:12 sc0 sc1\nWV 0\n"
"v_cmp_ne_u32 vcc, s15, v27\ns_nop 0\ns_cbranch_vccz .S309\ns_sleep 6\ns_sub_u32 s7, s7, 1\ns_cbranch_scc0 .S310\ns_branch .S309\n.S311:\ns_mov_b32 s7, 262144\n.S313:\n"
"v_and_b32 v27, 0x70, v1\nbuffer_load_dword v27, v27, s[16:19], s61 offen offset:8 sc0 sc1\nWV 0\nv_cmp_ne_u32 vcc, s15, v27\ns_nop 0\ns_cbranch_vccz .S312\ns_sleep 6\n"
"s_sub_u32 s7, s7, 1\ns_cbranch_scc0 .S313\ns_branch .S312\n.S314:\ns_mov_b32 s7, 262144\n.S316:\nv_and_b32 v27, 0xf0, v1\n"
"buffer_load_dword v27, v27, s[16:19], s61 offen offset:4 sc0 sc1\nWV 0\nv_cmp_ne_u32 vcc, s15, v27\ns_nop 0\ns_cbranch_vccz .S315\ns_sleep 6\ns_sub_u32 s7, s7, 1\n"
"s_cbranch_scc0 .S316\ns_branch .S315\n.E283:\nv_cmpx_gt_i32 vcc, 19, v0\nv_mov_b32 v19, s97\nv_cmpx_gt_i32 vcc, 18, v0\nv_mov_b32 v18, s97\nv_cmpx_gt_i32 vcc, 17, v0\n"
"v_mov_b32 v17, s97\nv_cmpx_gt_i32 vcc, 16, v0\nv_mov_b32 v16, s97\nv_cmpx_gt_i32 vcc, 3, v0\nv_mov_b32 v11, s97\nv_cmpx_gt_i32 vcc, 2, v0\nv_mov_b32 v10, s97\n"
"v_cmpx_gt_i32 vcc, 1, v0\nv_mov_b32 v9, s97\nv_cmpx_gt_i32 vcc, 0, v0\nv_mov_b32 v8, s97\ns_mov_b64 exec, -1\ns_nop 2\ns_branch .E284\n.E285:\ns_cmp_eq_u32 s99, 0\n"
"s_cbranch_scc1 .K282\nv_cmpx_gt_i32 vcc, 19, v3\nv_mov_b32 v15, s97\nv_cmpx_gt_i32 vcc, 18, v3\nv_mov_b32 v14, s97\nv_cmpx_gt_i32 vcc, 17, v3\nv_mov_b32 v13, s97\n"
"v_cmpx_gt_i32 vcc, 16, v3\nv_mov_b32 v12, s97\nv_cmpx_gt_i32 vcc, 3, v3\nv_mov_b32 v7, s97\nv_cmpx_gt_i32 vcc, 2, v3\nv_mov_b32 v6, s97\nv_cmpx_gt_i32 vcc, 1, v3\n"
"v_mov_b32 v5, s97\nv_cmpx_gt_i32 vcc, 0, v3\nv_mov_b32 v4, s97\ns_mov_b64 exec, -1\ns_nop 2\ns_branch .E286\n.K282:\nv_cmpx_gt_i32 vcc, 19, v3\nv_mov_b32 v19, s97\n"
"v_cmpx_gt_i32 vcc, 18, v3\nv_mov_b32 v18, s97\nv_cmpx_gt_i32 vcc, 17, v3\nv_mov_b32 v17, s97\nv_cmpx_gt_i32 vcc, 16, v3\nv_mov_b32 v16, s97\nv_cmpx_gt_i32 vcc, 3, v3\n"
"v_mov_b32 v11, s97\nv_cmpx_gt_i32 vcc, 2, v3\nv_mov_b32 v10, s97\nv_cmpx_gt_i32 vcc, 1, v3\nv_mov_b32 v9, s97\nv_cmpx_gt_i32 vcc, 0, v3\nv_mov_b32 v8, s97\ns_mov_b64 exec, -1\n"
"s_nop 2\ns_branch .E286\n.E287:\ns_swappc_b64 s[80:81], s[8:9]\ns_branch .E288\n.E300:\nv_mov_b32 v4, s97\nv_mov_b32 v5, s97\nv_mov_b32 v6, s97\nv_mov_b32 v7, s97\n"
"v_mov_b32 v12, s97\nv_mov_b32 v13, s97\nv_mov_b32 v14, s97\nv_mov_b32 v15, s97\nv_cmpx_gt_i32 vcc, 19, v3\nv_mov_b32 v19, s97\nv_cmpx_gt_i32 vcc, 18, v3\nv_mov_b32 v18, s97\n"
"v_cmpx_gt_i32 vcc, 17, v3\nv_mov_b32 v17, s97\nv_cmpx_gt_i32 vcc, 16, v3\nv_mov_b32 v16, s97\nv_cmpx_gt_i32 vcc, 3, v3\nv_mov_b32 v11, s97\nv_cmpx_gt_i32 vcc, 2, v3\n"
"v_mov_b32 v10, s97\nv_cmpx_gt_i32 vcc, 1, v3\nv_mov_b32 v9, s97\nv_cmpx_gt_i32 vcc, 0, v3\nv_mov_b32 v8, s97\ns_mov_b64 exec, -1\ns_nop 2\ns_branch .E301\n.T325:\n"
"s_mov_b32 s62, 0\ns_call_b64 s[80:81], .RB273\ns_branch .T326\n.T327:\ns_mov_b32 s62, 0\ns_call_b64 s[80:81], .RA272\ns_branch .T328\n.T333:\ns_mov_b32 s62, 0\n"
"s_call_b64 s[80:81], .RB273\ns_branch .T334\n.T335:\ns_mov_b32 s62, 0\ns_call_b64 s[80:81], .RA272\ns_branch .T336\n.T346:\ns_mov_b32 s62, 2\ns_call_b64 s[80:81], .RB273\n"
"s_branch .T347\n.T348:\ns_mov_b32 s62, 5\ns_call_b64 s[80:81], .RA272\ns_branch .T349\n.T350:\ns_mov_b32 s62, 5\ns_call_b64 s[80:81], .RA272\ns_branch .T351\n.E331:\n"
"s_swappc_b64 s[80:81], s[8:9]\ns_branch .E332\n.E337:\nv_cmpx_gt_i32 vcc, 19, v3\nv_mov_b32 v15, s97\nv_cmpx_gt_i32 vcc, 18, v3\nv_mov_b32 v14, s97\nv_cmpx_gt_i32 vcc, 17, v3\n"
"v_mov_b32 v13, s97\nv_cmpx_gt_i32 vcc, 16, v3\nv_mov_b32 v12, s97\nv_cmpx_gt_i32 vcc, 3, v3\nv_mov_b32 v7, s97\nv_cmpx_gt_i32 vcc, 2, v3\nv_mov_b32 v6, s97\n"
"v_cmpx_gt_i32 vcc, 1, v3\nv_mov_b32 v5, s97\nv_cmpx_gt_i32 vcc, 0, v3\nv_mov_b32 v4, s97\ns_mov_b64 exec, -1\ns_nop 2\ns_branch .E338\n.E340:\nv_cmpx_gt_i32 vcc, 19, v0\n"
"v_mov_b32 v15, s97\nv_cmpx_gt_i32 vcc, 18, v0\nv_mov_b32 v14, s97\nv_cmpx_gt_i32 vcc, 17, v0\nv_mov_b32 v13, s97\nv_cmpx_gt_i32 vcc, 16, v0\nv_mov_b32 v12, s97\n"
"v_cmpx_gt_i32 vcc, 3, v0\nv_mov_b32 v7, s97\nv_cmpx_gt_i32 vcc, 2, v0\nv_mov_b32 v6, s97\nv_cmpx_gt_i32 vcc, 1, v0\nv_mov_b32 v5, s97\nv_cmpx_gt_i32 vcc, 0, v0\nv_mov_b32 v4, s97\n"
"s_mov_b64 exec, -1\ns_nop 2\ns_branch .E341\n.E342:\ns_cmp_eq_u32 s99, 0\ns_cbranch_scc1 .K339\nv_cmpx_gt_i32 vcc, 19, v3\nv_mov_b32 v15, s97\nv_cmpx_gt_i32 vcc, 18, v3\n"
"v_mov_b32 v14, s97\nv_cmpx_gt_i32 vcc, 17, v3\nv_mov_b32 v13, s97\nv_cmpx_gt_i32 vcc, 16, v3\nv_mov_b32 v12, s97\nv_cmpx_gt_i32 vcc, 3, v3\nv_mov_b32 v7, s97\n"
"v_cmpx_gt_i32 vcc, 2, v3\nv_mov_b32 v6, s97\nv_cmpx_gt_i32 vcc, 1, v3\nv_mov_b32 v5, s97\nv_cmpx_gt_i32 vcc, 0, v3\nv_mov_b32 v4, s97\ns_mov_b64 exec, -1\ns_nop 2\ns_branch .E343\n"
".K339:\nv_cmpx_gt_i32 vcc, 19, v3\nv_mov_b32 v19, s97\nv_cmpx_gt_i32 vcc, 18, v3\nv_mov_b32 v18, s97\nv_cmpx_gt_i32 vcc, 17, v3\nv_mov_b32 v17, s97\nv_cmpx_gt_i32 vcc, 16, v3\n"
"v_mov_b32 v16, s97\nv_cmpx_gt_i32 vcc, 3, v3\nv_mov_b32 v11, s97\nv_cmpx_gt_i32 vcc, 2, v3\nv_mov_b32 v10, s97\nv_cmpx_gt_i32 vcc, 1, v3\nv_mov_b32 v9, s97\n"
"v_cmpx_gt_i32 vcc, 0, v3\nv_mov_b32 v8, s97\ns_mov_b64 exec, -1\ns_nop 2\ns_branch .E343\n.E344:\ns_swappc_b64 s[80:81], s[8:9]\ns_branch .E345\n.T362:\ns_mov_b32 s62, 4\n"
"s_call_b64 s[80:81], .RB273\ns_branch .T363\n.T364:\ns_mov_b32 s62, 4\ns_call_b64 s[80:81], .RA272\ns_branch .T365\n.T370:\ns_mov_b32 s62, 4\ns_call_b64 s[80:81], .RB273\n"
"s_branch .T371\n.T372:\ns_mov_b32 s62, 4\ns_call_b64 s[80:81], .RA272\ns_branch .T373\n.T374:\ns_mov_b32 s62, 6\ns_call_b64 s[80:81], .RB273\ns_branch .T375\n.S376:\n"
"s_mov_b32 s7, 262144\n.S378:\ns_add_u32 s0, s92, 0\nbuffer_load_dwordx4 v[208:211], v1, s[16:19], s0 offen offset:0 sc0 sc1\n"
"buffer_load_dwordx4 v[212:215], v1, s[16:19], s0 offen offset:1024 sc0 sc1\nWV 0\nv_max_u32 v21, v211, v215\nv_cmp_eq_u32 vcc, -1, v21\ns_nop 0\ns_cbranch_vccz .S377\ns_sleep 6\n"
"s_sub_u32 s7, s7, 1\ns_cbranch_scc0 .S378\ns_branch .S377\n.S379:\ns_mov_b32 s7, 262144\n.S381:\ns_add_u32 s0, s92, 131072\n"
"buffer_load_dwordx4 v[216:219], v1, s[16:19], s0 offen offset:0 sc0 sc1\nbuffer_load_dwordx4 v[220:223], v1, s[16:19], s0 offen offset:1024 sc0 sc1\nWV 0\n"
"v_max_u32 v21, v219, v223\nv_cmp_eq_u32 vcc, -1, v21\ns_nop 0\ns_cbranch_vccz .S380\ns_sleep 6\ns_sub_u32 s7, s7, 1\ns_cbranch_scc0 .S381\ns_branch .S380\n.S382:\n"
"s_mov_b32 s7, 262144\n.S384:\ns_add_u32 s0, s92, 262144\nbuffer_load_dwordx4 v[224:227], v1, s[16:19], s0 offen offset:0 sc0 sc1\n"
"buffer_load_dwordx4 v[228:231], v1, s[16:19], s0 offen offset:1024 sc0 sc1\nWV 0\nv_max_u32 v21, v227, v231\nv_cmp_eq_u32 vcc, -1, v21\ns_nop 0\ns_cbranch_vccz .S383\ns_sleep 6\n"
"s_sub_u32 s7, s7, 1\ns_cbranch_scc0 .S384\ns_branch .S383\n.S385:\ns_mov_b32 s7, 262144\n.S387:\ns_add_u32 s0, s92, 393216\n"
"buffer_load_dwordx4 v[208:211], v1, s[16:19], s0 offen offset:0 sc0 sc1\nbuffer_load_dwordx4 v[212:215], v1, s[16:19], s0 offen offset:1024 sc0 sc1\nWV 0\n"
"v_max_u32 v21, v211, v215\nv_cmp_eq_u32 vcc, -1, v21\ns_nop 0\ns_cbranch_vccz .S386\ns_sleep 6\ns_sub_u32 s7, s7, 1\ns_cbranch_scc0 .S387\ns_branch .S386\n.S388:\n"
"s_mov_b32 s7, 262144\n.S390:\ns_add_u32 s0, s92, 524288\nbuffer_load_dwordx4 v[216:219], v1, s[16:19], s0 offen offset:0 sc0 sc1\n"
"buffer_load_dwordx4 v[220:223], v1, s[16:19], s0 offen offset:1024 sc0 sc1\nWV 0\nv_max_u32 v21, v219, v223\nv_cmp_eq_u32 vcc, -1, v21\ns_nop 0\ns_cbranch_vccz .S389\ns_sleep 6\n"
"s_sub_u32 s7, s7, 1\ns_cbranch_scc0 .S390\ns_branch .S389\n.S391:\ns_mov_b32 s7, 262144\n.S393:\ns_add_u32 s0, s92, 655360\n"
"buffer_load_dwordx4 v[224:227], v1, s[16:19], s0 offen offset:0 sc0 sc1\nbuffer_load_dwordx4 v[228:231], v1, s[16:19], s0 offen offset:1024 sc0 sc1\nWV 0\n"
"v_max_u32 v21, v227, v231\nv_cmp_eq_u32 vcc, -1, v21\ns_nop 0\ns_cbranch_vccz .S392\ns_sleep 6\ns_sub_u32 s7, s7, 1\ns_cbranch_scc0 .S393\ns_branch .S392\n.S394:\n"
"s_mov_b32 s7, 262144\n.S396:\ns_add_u32 s0, s92, 786432\nbuffer_load_dwordx4 v[208:211], v1, s[16:19], s0 offen offset:0 sc0 sc1\n"
"buffer_load_dwordx4 v[212:215], v1, s[16:19], s0 offen offset:1024 sc0 sc1\nWV 0\nv_max_u32 v21, v211, v215\nv_cmp_eq_u32 vcc, -1, v21\ns_nop 0\ns_cbranch_vccz .S395\ns_sleep 6\n"
"s_sub_u32 s7, s7, 1\ns_cbranch_scc0 .S396\ns_branch .S395\n.S397:\ns_mov_b32 s7, 262144\n.S399:\nbuffer_load_dwordx4 v[64:67], v89, s[16:19], s40 offen offset:0 sc0 sc1\n"
"buffer_load_dwordx4 v[68:71], v89, s[16:19], s40 offen offset:16 sc0 sc1\nbuffer_load_dwordx4 v[72:75], v89, s[16:19], s40 offen offset:1792 sc0 sc1\n"
"buffer_load_dwordx4 v[76:79], v89, s[16:19], s40 offen offset:1808 sc0 sc1\nbuffer_load_dwordx2 v[80:81], v88, s[16:19], s63 offen offset:0 sc0 sc1\n"
"buffer_load_dwordx2 v[82:83], v88, s[16:19], s63 offen offset:512 sc0 sc1\nWV 0\nv_xor_b32 v66, s15, v66\nv_xor_b32 v70, s15, v70\nv_xor_b32 v74, s15, v74\n"
"v_xor_b32 v78, s15, v78\nv_xor_b32 v81, s15, v81\nv_xor_b32 v83, s15, v83\nv_or3_b32 v92, v66, v70, v74\nv_or3_b32 v92, v92, v78, v81\nv_or_b32 v92, v92, v83\n"
"v_cmp_ne_u32 vcc, 0, v92\ns_nop 0\ns_cbranch_vccz .S398\ns_sleep 6\ns_sub_u32 s7, s7, 1\ns_cbranch_scc0 .S399\ns_branch .S398\n.S400:\ns_mov_b32 s7, 262144\n.S402:\n"
"buffer_load_dword v27, off, s[16:19], s60 offset:512 sc0 sc1\nWV 0\nv_cmp_ne_u32 vcc, s15, v27\ns_nop 0\ns_cbranch_vccz .S401\ns_sleep 6\ns_sub_u32 s7, s7, 1\n"
"s_cbranch_scc0 .S402\ns_branch .S401\n.S403:\ns_mov_b32 s7, 262144\n.S405:\nbuffer_load_dword v27, v24, s[16:19], s41 offen sc0 sc1\nWV 0\nv_xor_b32 v27, s15, v27\n"
"v_and_b32 v26, 0xffffdfff, v27\nv_cmp_ne_u32 s[64:65], 0, v27\nv_cmp_ne_u32 vcc, 0, v26\ns_nop 0\ns_cbranch_vccz .S404\ns_sleep 6\ns_sub_u32 s7, s7, 1\ns_cbranch_scc0 .S405\n"
"s_branch .S404\n.Z407:\ns_sub_u32 s7, s7, 1\ns_cbranch_scc1 .Z408\ns_cmp_lg_u64 s[44:45], 0\ns_cbranch_scc0 .Z410\n"
"buffer_load_dwordx4 v[96:99], v1, s[16:19], s24 offen offset:0 sc0 sc1\nbuffer_load_dwordx4 v[100:103], v1, s[16:19], s24 offen offset:1024 sc0 sc1\n.Z410:\n"
"s_cmp_lg_u64 s[46:47], 0\ns_cbranch_scc0 .Z411\nbuffer_load_dwordx4 v[104:107], v1, s[16:19], s25 offen offset:0 sc0 sc1\n"
"buffer_load_dwordx4 v[108:111], v1, s[16:19], s25 offen offset:1024 sc0 sc1\n.Z411:\ns_cmp_lg_u64 s[48:49], 0\ns_cbranch_scc0 .Z412\n"
"buffer_load_dwordx4 v[112:115], v1, s[16:19], s26 offen offset:0 sc0 sc1\nbuffer_load_dwordx4 v[116:119], v1, s[16:19], s26 offen offset:1024 sc0 sc1\n.Z412:\n"
"s_cmp_lg_u64 s[50:51], 0\ns_cbranch_scc0 .Z413\nbuffer_load_dwordx4 v[120:123], v1, s[16:19], s27 offen offset:0 sc0 sc1\n"
"buffer_load_dwordx4 v[124:127], v1, s[16:19], s27 offen offset:1024 sc0 sc1\n.Z413:\ns_cmp_lg_u64 s[52:53], 0\ns_cbranch_scc0 .Z414\n"
"buffer_load_dwordx4 v[32:35], v1, s[16:19], s32 offen offset:0 sc0 sc1\nbuffer_load_dwordx4 v[36:39], v1, s[16:19], s32 offen offset:1024 sc0 sc1\n.Z414:\n"
"s_cmp_lg_u64 s[54:55], 0\ns_cbranch_scc0 .Z415\nbuffer_load_dwordx4 v[40:43], v1, s[16:19], s33 offen offset:0 sc0 sc1\n"
"buffer_load_dwordx4 v[44:47], v1, s[16:19], s33 offen offset:1024 sc0 sc1\n.Z415:\ns_cmp_lg_u64 s[56:57], 0\ns_cbranch_scc0 .Z416\n"
"buffer_load_dwordx4 v[48:51], v1, s[16:19], s34 offen offset:0 sc0 sc1\nbuffer_load_dwordx4 v[52:55], v1, s[16:19], s34 offen offset:1024 sc0 sc1\n.Z416:\n"
"s_cmp_lg_u64 s[58:59], 0\ns_cbranch_scc0 .Z417\nbuffer_load_dwordx4 v[56:59], v1, s[16:19], s35 offen offset:0 sc0 sc1\n"
"buffer_load_dwordx4 v[60:63], v1, s[16:19], s35 offen offset:1024 sc0 sc1\n.Z417:\ns_sleep 8\nWV 0\ns_branch .Z409\n.RA272:\ns_nop 7\ns_nop 3\nv_max3_f32 v22, v32, v33, v34\n"
"v_max3_f32 v22, v22, v35, v40\nv_max3_f32 v22, v22, v41, v42\nv_max_f32 v22, v22, v43\nv_mov_b32 v91, v22\ns_nop 1\nv_permlane32_swap_b32 v22, v91\nv_max_f32 v22, v22, v91\n"
"v_mov_b32 v91, v22\ns_nop 1\nv_permlane16_swap_b32 v22, v91\nv_max_f32 v22, v22, v91\nv_ceil_f32 v22, v22\nv_max3_f32 v23, v36, v37, v38\nv_max3_f32 v23, v23, v39, v44\n"
"v_max3_f32 v23, v23, v45, v46\nv_max_f32 v23, v23, v47\nv_mov_b32 v91, v23\ns_nop 1\nv_permlane32_swap_b32 v23, v91\nv_max_f32 v23, v23, v91\nv_mov_b32 v91, v23\ns_nop 1\n"
"v_permlane16_swap_b32 v23, v91\nv_max_f32 v23, v23, v91\nv_ceil_f32 v23, v23\ns_bitcmp1_b32 s62, 0\ns_cbranch_scc0 .R418\nv_cmp_lt_f32 vcc, 0xff000000, v22\ns_nop 1\n"
"v_cndmask_b32 v22, 0, v22, vcc\nv_cmp_lt_f32 vcc, 0xff000000, v23\ns_nop 1\nv_cndmask_b32 v23, 0, v23, vcc\ns_branch .R419\n.R418:\nv_max_f32 v22, 0, v22\nv_max_f32 v23, 0, v23\n"
"s_bitcmp1_b32 s62, 3\ns_cbranch_scc0 .R419\nv_max_f32 v22, v20, v22\nv_max_f32 v23, v21, v23\n.R419:\nAD 88,88,22\nAD 89,89,23\nSU 32,32,22\nSU 33,33,22\nSU 34,34,22\nSU 35,35,22\n"
"SU 36,36,23\nSU 37,37,23\nSU 38,38,23\nSU 39,39,23\nSU 40,40,22\nSU 41,41,22\nSU 42,42,22\nSU 43,43,22\nSU 44,44,23\nSU 45,45,23\nSU 46,46,23\nSU 47,47,23\nv_mov_b32 v90, v80\n"
"v_mov_b32 v91, v81\nEX 80,32\nEX 81,33\ns_nop 0\nAD 84,80,81\nCV 64,80,81\nEX 82,34\nEX 83,35\nAD 84,84,82\nAD 84,84,83\nCV 65,82,83\nEX 80,36\nEX 81,37\ns_nop 0\nAD 85,80,81\n"
"CV 68,80,81\nEX 82,38\nEX 83,39\nAD 85,85,82\nAD 85,85,83\nCV 69,82,83\nEX 80,40\nEX 81,41\nAD 84,84,80\nAD 84,84,81\nCV 66,80,81\nEX 82,42\nEX 83,43\nAD 84,84,82\nAD 84,84,83\n"
"CV 67,82,83\nEX 80,44\nEX 81,45\nAD 85,85,80\nAD 85,85,81\nCV 70,80,81\nEX 82,46\nEX 83,47\nAD 85,85,82\nAD 85,85,83\nCV 71,82,83\nv_mov_b32 v80, v90\nv_mov_b32 v81, v91\n"
"s_bitcmp1_b32 s62, 1\ns_cbranch_scc1 .R420\nSU 48,48,22\nSU 49,49,22\nSU 50,50,22\nSU 51,51,22\nSU 52,52,23\nSU 53,53,23\nSU 54,54,23\nSU 55,55,23\nSU 56,56,22\nSU 57,57,22\n"
"SU 58,58,22\nSU 59,59,22\nSU 60,60,23\nSU 61,61,23\nSU 62,62,23\nSU 63,63,23\nSU 4,4,22\nSU 5,5,22\nSU 6,6,22\nSU 7,7,22\nSU 8,8,23\nSU 9,9,23\nSU 10,10,23\nSU 11,11,23\n"
"SU 12,12,22\nSU 13,13,22\nSU 14,14,22\nSU 15,15,22\nSU 16,16,23\nSU 17,17,23\nSU 18,18,23\nSU 19,19,23\nEX 80,48\nEX 81,49\n.R420:\ns_bitcmp1_b32 s62, 0\ns_cbranch_scc0 .RS274\n"
"s_nop 1\ns_setpc_b64 s[80:81]\n.RB273:\ns_nop 7\ns_nop 3\nv_max3_f32 v22, v48, v49, v50\nv_max3_f32 v22, v22, v51, v56\nv_max3_f32 v22, v22, v57, v58\nv_max_f32 v22, v22, v59\n"
"v_mov_b32 v91, v22\ns_nop 1\nv_permlane32_swap_b32 v22, v91\nv_max_f32 v22, v22, v91\nv_mov_b32 v91, v22\ns_nop 1\nv_permlane16_swap_b32 v22, v91\nv_max_f32 v22, v22, v91\n"
"v_ceil_f32 v22, v22\nv_max3_f32 v23, v52, v53, v54\nv_max3_f32 v23, v23, v55, v60\nv_max3_f32 v23, v23, v61, v62\nv_max_f32 v23, v23, v63\nv_mov_b32 v91, v23\ns_nop 1\n"
"v_permlane32_swap_b32 v23, v91\nv_max_f32 v23, v23, v91\nv_mov_b32 v91, v23\ns_nop 1\nv_permlane16_swap_b32 v23, v91\nv_max_f32 v23, v23, v91\nv_ceil_f32 v23, v23\n"
"s_bitcmp1_b32 s62, 0\ns_cbranch_scc0 .R423\nv_cmp_lt_f32 vcc, 0xff000000, v22\nv_cndmask_b32 v22, 0, v22, vcc\nv_cmp_lt_f32 vcc, 0xff000000, v23\nv_cndmask_b32 v23, 0, v23, vcc\n"
"s_branch .R424\n.R423:\nv_max_f32 v22, 0, v22\nv_max_f32 v23, 0, v23\ns_bitcmp1_b32 s62, 3\ns_cbranch_scc0 .R424\nv_max_f32 v22, v20, v22\nv_max_f32 v23, v21, v23\n.R424:\n"
"AD 88,88,22\nAD 89,89,23\nSU 48,48,22\nSU 49,49,22\nSU 50,50,22\nSU 51,51,22\nSU 52,52,23\nSU 53,53,23\nSU 54,54,23\nSU 55,55,23\nSU 56,56,22\nSU 57,57,22\nSU 58,58,22\n"
"SU 59,59,22\nSU 60,60,23\nSU 61,61,23\nSU 62,62,23\nSU 63,63,23\nv_mov_b32 v90, v80\nv_mov_b32 v91, v81\nEX 80,48\nEX 81,49\ns_nop 0\nAD 84,80,81\nCV 72,80,81\nEX 82,50\nEX 83,51\n"
"AD 84,84,82\nAD 84,84,83\nCV 73,82,83\nEX 80,52\nEX 81,53\ns_nop 0\nAD 85,80,81\nCV 76,80,81\nEX 82,54\nEX 83,55\nAD 85,85,82\nAD 85,85,83\nCV 77,82,83\nEX 80,56\nEX 81,57\n"
"AD 84,84,80\nAD 84,84,81\nCV 74,80,81\nEX 82,58\nEX 83,59\nAD 84,84,82\nAD 84,84,83\nCV 75,82,83\nEX 80,60\nEX 81,61\nAD 85,85,80\nAD 85,85,81\nCV 78,80,81\nEX 82,62\nEX 83,63\n"
"AD 85,85,82\nAD 85,85,83\nCV 79,82,83\nv_mov_b32 v80, v90\nv_mov_b32 v81, v91\ns_bitcmp1_b32 s62, 1\ns_cbranch_scc1 .R425\nSU 32,32,22\nSU 33,33,22\nSU 34,34,22\nSU 35,35,22\n"
"SU 36,36,23\nSU 37,37,23\nSU 38,38,23\nSU 39,39,23\nSU 40,40,22\nSU 41,41,22\nSU 42,42,22\nSU 43,43,22\nSU 44,44,23\nSU 45,45,23\nSU 46,46,23\nSU 47,47,23\nSU 4,4,22\nSU 5,5,22\n"
"SU 6,6,22\nSU 7,7,22\nSU 8,8,23\nSU 9,9,23\nSU 10,10,23\nSU 11,11,23\nSU 12,12,22\nSU 13,13,22\nSU 14,14,22\nSU 15,15,22\nSU 16,16,23\nSU 17,17,23\nSU 18,18,23\nSU 19,19,23\n"
"EX 80,32\nEX 81,33\n.R425:\ns_bitcmp1_b32 s62, 0\ns_cbranch_scc0 .RS274\ns_nop 1\ns_setpc_b64 s[80:81]\n.RS274:\nv_exp_f32 v22, -v22\nv_exp_f32 v23, -v23\nMU 86,86,22\n"
"MU 87,87,23\ns_bitcmp1_b32 s62, 2\ns_cbranch_scc1 .R428\nAR 91,0\nMU 91,91,22\nAW 0,91\nAR 91,1\nMU 91,91,22\nAW 1,91\nAR 91,2\nMU 91,91,22\nAW 2,91\nAR 91,3\nMU 91,91,22\n"
"AW 3,91\nAR 91,4\nMU 91,91,23\nAW 4,91\nAR 91,5\nMU 91,91,23\nAW 5,91\nAR 91,6\nMU 91,91,23\nAW 6,91\nAR 91,7\nMU 91,91,23\nAW 7,91\nAR 91,8\nMU 91,91,22\nAW 8,91\nAR 91,9\n"
"MU 91,91,22\nAW 9,91\nAR 91,10\nMU 91,91,22\nAW 10,91\nAR 91,11\nMU 91,91,22\nAW 11,91\nAR 91,12\nMU 91,91,23\nAW 12,91\nAR 91,13\nMU 91,91,23\nAW 13,91\nAR 91,14\nMU 91,91,23\n"
"AW 14,91\nAR 91,15\nMU 91,91,23\nAW 15,91\nAR 91,16\nMU 91,91,22\nAW 16,91\nAR 91,17\nMU 91,91,22\nAW 17,91\nAR 91,18\nMU 91,91,22\nAW 18,91\nAR 91,19\nMU 91,91,22\nAW 19,91\n"
"AR 91,20\nMU 91,91,23\nAW 20,91\nAR 91,21\nMU 91,91,23\nAW 21,91\nAR 91,22\nMU 91,91,23\nAW 22,91\nAR 91,23\nMU 91,91,23\nAW 23,91\nAR 91,24\nMU 91,91,22\nAW 24,91\nAR 91,25\n"
"MU 91,91,22\nAW 25,91\nAR 91,26\nMU 91,91,22\nAW 26,91\nAR 91,27\nMU 91,91,22\nAW 27,91\nAR 91,28\nMU 91,91,23\nAW 28,91\nAR 91,29\nMU 91,91,23\nAW 29,91\nAR 91,30\nMU 91,91,23\n"
"AW 30,91\nAR 91,31\nMU 91,91,23\nAW 31,91\nAR 91,32\nMU 91,91,22\nAW 32,91\nAR 91,33\nMU 91,91,22\nAW 33,91\nAR 91,34\nMU 91,91,22\nAW 34,91\nAR 91,35\nMU 91,91,22\nAW 35,91\n"
"AR 91,36\nMU 91,91,23\nAW 36,91\nAR 91,37\nMU 91,91,23\nAW 37,91\nAR 91,38\nMU 91,91,23\nAW 38,91\nAR 91,39\nMU 91,91,23\nAW 39,91\nAR 91,40\nMU 91,91,22\nAW 40,91\nAR 91,41\n"
"MU 91,91,22\nAW 41,91\nAR 91,42\nMU 91,91,22\nAW 42,91\nAR 91,43\nMU 91,91,22\nAW 43,91\nAR 91,44\nMU 91,91,23\nAW 44,91\nAR 91,45\nMU 91,91,23\nAW 45,91\nAR 91,46\nMU 91,91,23\n"
"AW 46,91\nAR 91,47\nMU 91,91,23\nAW 47,91\nAR 91,48\nMU 91,91,22\nAW 48,91\nAR 91,49\nMU 91,91,22\nAW 49,91\nAR 91,50\nMU 91,91,22\nAW 50,91\nAR 91,51\nMU 91,91,22\nAW 51,91\n"
"AR 91,52\nMU 91,91,23\nAW 52,91\nAR 91,53\nMU 91,91,23\nAW 53,91\nAR 91,54\nMU 91,91,23\nAW 54,91\nAR 91,55\nMU 91,91,23\nAW 55,91\nAR 91,56\nMU 91,91,22\nAW 56,91\nAR 91,57\n"
"MU 91,91,22\nAW 57,91\nAR 91,58\nMU 91,91,22\nAW 58,91\nAR 91,59\nMU 91,91,22\nAW 59,91\nAR 91,60\nMU 91,91,23\nAW 60,91\nAR 91,61\nMU 91,91,23\nAW 61,91\nAR 91,62\nMU 91,91,23\n"
"AW 62,91\nAR 91,63\nMU 91,91,23\nAW 63,91\nAR 91,64\nMU 91,91,22\nAW 64,91\nAR 91,65\nMU 91,91,22\nAW 65,91\nAR 91,66\nMU 91,91,22\nAW 66,91\nAR 91,67\nMU 91,91,22\nAW 67,91\n"
"AR 91,68\nMU 91,91,23\nAW 68,91\nAR 91,69\nMU 91,91,23\nAW 69,91\nAR 91,70\nMU 91,91,23\nAW 70,91\nAR 91,71\nMU 91,91,23\nAW 71,91\nAR 91,72\nMU 91,91,22\nAW 72,91\nAR 91,73\n"
"MU 91,91,22\nAW 73,91\nAR 91,74\nMU 91,91,22\nAW 74,91\nAR 91,75\nMU 91,91,22\nAW 75,91\nAR 91,76\nMU 91,91,23\nAW 76,91\nAR 91,77\nMU 91,91,23\nAW 77,91\nAR 91,78\nMU 91,91,23\n"
"AW 78,91\nAR 91,79\nMU 91,91,23\nAW 79,91\nAR 91,80\nMU 91,91,22\nAW 80,91\nAR 91,81\nMU 91,91,22\nAW 81,91\nAR 91,82\nMU 91,91,22\nAW 82,91\nAR 91,83\nMU 91,91,22\nAW 83,91\n"
"AR 91,84\nMU 91,91,23\nAW 84,91\nAR 91,85\nMU 91,91,23\nAW 85,91\nAR 91,86\nMU 91,91,23\nAW 86,91\nAR 91,87\nMU 91,91,23\nAW 87,91\nAR 91,88\nMU 91,91,22\nAW 88,91\nAR 91,89\n"
"MU 91,91,22\nAW 89,91\nAR 91,90\nMU 91,91,22\nAW 90,91\nAR 91,91\nMU 91,91,22\nAW 91,91\nAR 91,92\nMU 91,91,23\nAW 92,91\nAR 91,93\nMU 91,91,23\nAW 93,91\nAR 91,94\nMU 91,91,23\n"
"AW 94,91\nAR 91,95\nMU 91,91,23\nAW 95,91\nAR 91,96\nMU 91,91,22\nAW 96,91\nAR 91,97\nMU 91,91,22\nAW 97,91\nAR 91,98\nMU 91,91,22\nAW 98,91\nAR 91,99\nMU 91,91,22\nAW 99,91\n"
"AR 91,100\nMU 91,91,23\nAW 100,91\nAR 91,101\nMU 91,91,23\nAW 101,91\nAR 91,102\nMU 91,91,23\nAW 102,91\nAR 91,103\nMU 91,91,23\nAW 103,91\nAR 91,104\nMU 91,91,22\nAW 104,91\n"
"AR 91,105\nMU 91,91,22\nAW 105,91\nAR 91,106\nMU 91,91,22\nAW 106,91\nAR 91,107\nMU 91,91,22\nAW 107,91\nAR 91,108\nMU 91,91,23\nAW 108,91\nAR 91,109\nMU 91,91,23\nAW 109,91\n"
"AR 91,110\nMU 91,91,23\nAW 110,91\nAR 91,111\nMU 91,91,23\nAW 111,91\nAR 91,112\nMU 91,91,22\nAW 112,91\nAR 91,113\nMU 91,91,22\nAW 113,91\nAR 91,114\nMU 91,91,22\nAW 114,91\n"
"AR 91,115\nMU 91,91,22\nAW 115,91\nAR 91,116\nMU 91,91,23\nAW 116,91\nAR 91,117\nMU 91,91,23\nAW 117,91\nAR 91,118\nMU 91,91,23\nAW 118,91\nAR 91,119\nMU 91,91,23\nAW 119,91\n"
"AR 91,120\nMU 91,91,22\nAW 120,91\nAR 91,121\nMU 91,91,22\nAW 121,91\nAR 91,122\nMU 91,91,22\nAW 122,91\nAR 91,123\nMU 91,91,22\nAW 123,91\nAR 91,124\nMU 91,91,23\nAW 124,91\n"
"AR 91,125\nMU 91,91,23\nAW 125,91\nAR 91,126\nMU 91,91,23\nAW 126,91\nAR 91,127\nMU 91,91,23\nAW 127,91\ns_nop 1\ns_setpc_b64 s[80:81]\n.R428:\nMU 128,128,22\nMU 129,129,22\n"
"MU 130,130,22\nMU 131,131,22\nMU 132,132,23\nMU 133,133,23\nMU 134,134,23\nMU 135,135,23\nMU 136,136,22\nMU 137,137,22\nMU 138,138,22\nMU 139,139,22\nMU 140,140,23\nMU 141,141,23\n"
"MU 142,142,23\nMU 143,143,23\nMU 144,144,22\nMU 145,145,22\nMU 146,146,22\nMU 147,147,22\nMU 148,148,23\nMU 149,149,23\nMU 150,150,23\nMU 151,151,23\nMU 152,152,22\nMU 153,153,22\n"
"MU 154,154,22\nMU 155,155,22\nMU 156,156,23\nMU 157,157,23\nMU 158,158,23\nMU 159,159,23\nMU 160,160,22\nMU 161,161,22\nMU 162,162,22\nMU 163,163,22\nMU 164,164,23\nMU 165,165,23\n"
"MU 166,166,23\nMU 167,167,23\nMU 168,168,22\nMU 169,169,22\nMU 170,170,22\nMU 171,171,22\nMU 172,172,23\nMU 173,173,23\nMU 174,174,23\nMU 175,175,23\nMU 176,176,22\nMU 177,177,22\n"
"MU 178,178,22\nMU 179,179,22\nMU 180,180,23\nMU 181,181,23\nMU 182,182,23\nMU 183,183,23\nMU 184,184,22\nMU 185,185,22\nMU 186,186,22\nMU 187,187,22\nMU 188,188,23\nMU 189,189,23\n"
"MU 190,190,23\nMU 191,191,23\nMU 192,192,22\nMU 193,193,22\nMU 194,194,22\nMU 195,195,22\nMU 196,196,23\nMU 197,197,23\nMU 198,198,23\nMU 199,199,23\nMU 200,200,22\nMU 201,201,22\n"
"MU 202,202,22\nMU 203,203,22\nMU 204,204,23\nMU 205,205,23\nMU 206,206,23\nMU 207,207,23\nMU 208,208,22\nMU 209,209,22\nMU 210,210,22\nMU 211,211,22\nMU 212,212,23\nMU 213,213,23\n"
"MU 214,214,23\nMU 215,215,23\nMU 216,216,22\nMU 217,217,22\nMU 218,218,22\nMU 219,219,22\nMU 220,220,23\nMU 221,221,23\nMU 222,222,23\nMU 223,223,23\nMU 224,224,22\nMU 225,225,22\n"
"MU 226,226,22\nMU 227,227,22\nMU 228,228,23\nMU 229,229,23\nMU 230,230,23\nMU 231,231,23\nMU 232,232,22\nMU 233,233,22\nMU 234,234,22\nMU 235,235,22\nMU 236,236,23\nMU 237,237,23\n"
"MU 238,238,23\nMU 239,239,23\nMU 240,240,22\nMU 241,241,22\nMU 242,242,22\nMU 243,243,22\nMU 244,244,23\nMU 245,245,23\nMU 246,246,23\nMU 247,247,23\nMU 248,248,22\nMU 249,249,22\n"
"MU 250,250,22\nMU 251,251,22\nMU 252,252,23\nMU 253,253,23\nMU 254,254,23\nMU 255,255,23\ns_nop 1\ns_setpc_b64 s[80:81]\n.p2align 12\n.PA5:\ns_add_u32 m0, s47, -49152\n"
"s_add_u32 s48, s48, 128\nD0 2,20,48,0\nD0 3,20,48,1024\nD0 4,20,48,2048\nD0 5,20,48,3072\ns_and_b32 s86, s74, 15\ns_lshl2_add_u32 s86, s86, s11\ns_mov_b32 s88, 0\n"
"s_lshl_b32 s93, s73, 8\ns_lshl2_add_u32 s93, s86, s93\ns_add_u32 s93, s93, 0xc04ac00\ns_lshl_b32 s86, s86, 14\ns_mul_i32 s91, s73, 25165824\ns_add_u32 s86, s86, s91\n"
"s_and_b32 s92, s74, 1\ns_lshl_b32 s92, s92, 13\ns_add_u32 s86, s86, s92\ns_add_u32 s94, s86, 0x1480000\ns_xor_b32 s95, s94, 0x2000\ns_add_u32 s86, s94, 512\n"
"s_load_dword s87, s[54:55], s86\ns_add_u32 m0, s47, -32768\ns_add_u32 s45, s45, 128\nD0 2,20,45,0\nD0 3,20,45,1024\nD0 4,20,45,2048\nD0 5,20,45,3072\ns_add_u32 m0, s47, 32768\n"
"s_add_u32 s44, s44, 0x40000\nD0 6,24,44,0\nD0 7,24,44,1024\nD0 8,24,44,2048\nD0 9,24,44,3072\ns_add_u32 m0, s47, 49152\ns_add_u32 s44, s44, 0x40000\nD0 6,24,44,0\nD0 7,24,44,1024\n"
"D0 8,24,44,2048\nD0 9,24,44,3072\ns_add_u32 m0, s47, -16384\ns_add_u32 s48, s48, 128\nD0 2,20,48,0\nD0 3,20,48,1024\nD0 4,20,48,2048\nD0 5,20,48,3072\nv_mov_b32 v52, 0x40000000\n"
"v_cmp_gt_u32 vcc, 56, v0\ns_lshl2_add_u32 s2, s10, s11\ns_mul_i32 s2, s2, 1792\nv_cndmask_b32 v53, v52, v1, vcc\ns_mov_b32 s36, s56\ns_and_b32 s37, s57, 0xffff\n"
"s_mov_b32 s38, 0x380\ns_mov_b32 s39, 0x20000\ns_mov_b32 s40, s58\ns_and_b32 s41, s59, 0xffff\ns_mov_b32 s42, 0x380\ns_mov_b32 s43, 0x20000\n"
"buffer_load_dwordx4 v[32:35], v53, s[28:31], s2 offen offset:0\nbuffer_load_dwordx4 v[36:39], v53, s[28:31], s2 offen offset:896\n"
"buffer_load_dwordx4 v[40:43], v53, s[36:39], 0 offen offset:0\nbuffer_load_dwordx4 v[44:47], v53, s[40:43], 0 offen offset:0\nWL 0\ns_mul_i32 s15, s15, 1664525\n"
"s_add_u32 s15, s15, 1013904223\nv_mov_b32 v25, s15\ns_mov_b32 s57, 0x3db8aa3b\ns_and_b32 s59, s11, 1\ns_lshl_b32 s59, s59, 13\ns_mul_i32 s2, s73, 25165824\ns_and_b32 s3, s12, 1\n"
"s_lshl_b32 s3, s3, 21\ns_add_u32 s2, s2, s3\ns_lshl2_add_u32 s3, s13, s11\ns_lshl_b32 s3, s3, 15\ns_add_u32 s2, s2, s3\ns_add_u32 s56, s2, 0x800000\ns_xor_b32 s53, s56, 0xc000\n"
"buffer_load_dword v31, v24, s[24:27], s96 offen\ns_add_u32 s96, s96, 0x80000\nbuffer_load_dword v31, v24, s[24:27], s96 offen\ns_add_u32 s96, s96, 0x80000\n"
"v_lshrrev_b32 v60, 3, v0\nv_and_b32 v61, 0x70, v1\nv_lshl_or_b32 v60, v60, 10, v61\nSv 2,60,16,94,0\nSv 2,60,16,95,0\nWV 28\ns_barrier\nRa 128,10,0\nRa 132,11,0\nRa 136,10,2048\n"
"Ra 140,11,2048\nRa 144,10,8192\nRa 148,11,8192\nRa 152,10,10240\nRa 156,11,10240\nTa 160,12,0\nTa 162,12,2048\nTa 164,12,16384\nTa 166,12,18432\nTa 168,13,0\nTa 170,13,2048\n"
"Ta 172,13,16384\nTa 174,13,18432\nTa 176,14,0\nTa 178,14,2048\nTa 180,14,16384\nTa 182,14,18432\nTa 184,15,0\nTa 186,15,2048\nTa 188,15,16384\nTa 190,15,18432\nWL 0\ns_barrier\n"
"Zvaa 64,128,160\nTa 224,16,0\nMvaa 64,132,164\nTa 226,16,2048\nZvaa 68,140,164\ns_add_u32 m0, s47, -65536\ns_add_u32 s45, s45, 128\nMvaa 68,136,160\nTa 228,16,16384\n"
"Zvaa 128,144,160\nTa 230,16,18432\nMvaa 128,148,164\nD0 2,20,45,0\nZvaa 132,156,164\nTa 232,17,0\nMvaa 132,152,160\nTa 234,17,2048\nZvaa 140,152,168\ns_mov_b32 s36, s60\n"
"Mvaa 140,156,172\nTa 236,17,16384\nZvaa 136,148,172\nTa 238,17,18432\nMvaa 136,144,168\ns_and_b32 s37, s61, 0xffff\nZvaa 76,136,168\nTa 240,18,0\nMvaa 76,140,172\nTa 242,18,2048\n"
"D0 3,20,45,1024\nZvaa 72,132,172\ns_mov_b32 s38, 0x380000\nMvaa 72,128,168\nTa 244,18,16384\nZvaa 80,128,176\nTa 246,18,18432\nMvaa 80,132,180\ns_mov_b32 s39, 0x20000\n"
"Zvaa 84,140,180\nTa 248,19,0\nMvaa 84,136,176\nTa 250,19,2048\nZvaa 144,144,176\ns_lshl3_add_u32 s75, s12, s13\nMvaa 144,148,180\nTa 252,19,16384\nD0 4,20,45,2048\n"
"Zvaa 148,156,180\nTa 254,19,18432\nMvaa 148,152,176\ns_lshl2_add_u32 s75, s75, s11\nZvaa 156,152,184\ns_mul_i32 s75, s75, 7168\nMvaa 156,156,188\ns_mul_i32 s76, s11, 7168\n"
"Zvaa 152,148,188\ns_add_u32 s76, s76, 131072\nMvaa 152,144,184\ns_mov_b32 s42, 1605632\nZvaa 92,136,184\nMvaa 92,140,188\nD0 5,20,45,3072\nZvaa 88,132,188\nMvaa 88,128,184\n"
"s_waitcnt vmcnt(24) lgkmcnt(0)\ns_barrier\nZvaa 96,128,224\nRa 192,10,16384\nMvaa 96,132,228\nZvaa 100,140,228\nRa 196,11,16384\ns_add_u32 m0, s47, 0\ns_add_u32 s44, s44, 0x40000\n"
"Mvaa 100,136,224\nZvaa 160,144,224\nRa 200,10,18432\nMvaa 160,148,228\nD0 6,24,44,0\nZvaa 164,156,228\nRa 204,11,18432\nMvaa 164,152,224\nZvaa 172,152,232\nRa 208,10,24576\n"
"Mvaa 172,156,236\ns_and_b32 s78, s74, 3\ns_lshl2_add_u32 s78, s73, s78\ns_lshl_b32 s78, s78, 12\ns_bitcmp1_b32 s11, 0\ns_cselect_b32 s79, s48, s45\ns_add_u32 s79, s79, 512\n"
"s_and_b32 s79, s79, 0xfffe0000\ns_add_u32 s78, s78, s79\nbuffer_load_dword v31, v26, s[20:23], s78 offen\nZvaa 168,148,236\nRa 212,11,24576\nMvaa 168,144,232\nZvaa 108,136,232\n"
"Ra 216,10,26624\nMvaa 108,140,236\nD0 7,24,44,1024\nZvaa 104,132,236\nRa 220,11,26624\nMvaa 104,128,232\nZvaa 112,128,240\nMvaa 112,132,244\nZvaa 116,140,244\nMvaa 116,136,240\n"
"Zvaa 176,144,240\nMvaa 176,148,244\nD0 8,24,44,2048\nZvaa 180,156,244\nMvaa 180,152,240\nZvaa 188,152,248\nMvaa 188,156,252\nZvaa 184,148,252\nMvaa 184,144,248\nZvaa 124,136,248\n"
"Mvaa 124,140,252\nD0 9,24,44,3072\nZvaa 120,132,252\nMvaa 120,128,248\nWL 0\nZaaa 32,192,224\nRa 128,10,32768\nMaaa 32,196,228\nZaaa 36,204,228\nRa 132,11,32768\n"
"s_add_u32 m0, s47, 16384\ns_add_u32 s44, s44, 0x40000\nMaaa 36,200,224\nZaaa 96,208,224\nRa 136,10,34816\nMaaa 96,212,228\nD0 6,24,44,0\nZaaa 100,220,228\nRa 140,11,34816\n"
"Maaa 100,216,224\nZaaa 108,216,232\nRa 144,10,40960\nMaaa 108,220,236\nZaaa 104,212,236\nRa 148,11,40960\nMaaa 104,208,232\nZaaa 44,200,232\nRa 152,10,43008\nMaaa 44,204,236\n"
"D0 7,24,44,1024\nZaaa 40,196,236\nRa 156,11,43008\nMaaa 40,192,232\nZaaa 48,192,240\nMaaa 48,196,244\nZaaa 52,204,244\nMaaa 52,200,240\nZaaa 112,208,240\nMaaa 112,212,244\n"
"D0 8,24,44,2048\nZaaa 116,220,244\nMaaa 116,216,240\nZaaa 124,216,248\nMaaa 124,220,252\nZaaa 120,212,252\nMaaa 120,208,248\nZaaa 60,200,248\nMaaa 60,204,252\nD0 9,24,44,3072\n"
"Zaaa 56,196,252\nMaaa 56,192,248\ns_waitcnt vmcnt(25) lgkmcnt(0)\ns_barrier\nZaaa 0,192,160\nTa 224,16,32768\nWV 17\nMaaa 0,196,164\nTa 226,16,34816\nZaaa 4,204,164\n"
"s_add_u32 m0, s47, -49152\ns_add_u32 s48, s48, 128\nMaaa 4,200,160\nTa 228,16,49152\nv_lshlrev_b32 v48, 16, v32\nZaaa 64,208,160\nTa 230,16,51200\n"
"buffer_load_dword v31, v24, s[24:27], s96 offen\ns_add_u32 s96, s96, 0x80000\nMaaa 64,212,164\nD0 2,20,48,0\nv_lshlrev_b32 v49, 16, v36\nZaaa 68,220,164\nTa 232,17,32768\n"
"Maaa 68,216,160\nTa 234,17,34816\nv_lshlrev_b32 v50, 16, v40\nZaaa 76,216,168\nv_lshlrev_b32 v51, 16, v44\nMaaa 76,220,172\nTa 236,17,49152\nZaaa 72,212,172\nTa 238,17,51200\n"
"MU 52,48,50\nMaaa 72,208,168\nMU 53,49,50\nZaaa 12,200,168\nTa 240,18,32768\nCV 52,52,53\nMaaa 12,204,172\nTa 242,18,34816\nD0 3,20,48,1024\nZaaa 8,196,172\n"
"v_lshlrev_b32 v54, 16, v52\nv_and_b32 v55, 0xffff0000, v52\nMaaa 8,192,168\nTa 244,18,49152\nZaaa 16,192,176\nTa 246,18,51200\nMU 56,48,51\nMaaa 16,196,180\nMU 57,49,51\n"
"Zaaa 20,204,180\nTa 248,19,32768\nMaaa 20,200,176\nTa 250,19,34816\nv_and_b32 v48, 0xffff0000, v32\nZaaa 80,208,176\nv_and_b32 v49, 0xffff0000, v36\nMaaa 80,212,180\n"
"Ta 252,19,49152\nD0 4,20,48,2048\nZaaa 84,220,180\nTa 254,19,51200\nMaaa 84,216,176\nv_and_b32 v50, 0xffff0000, v40\nv_and_b32 v51, 0xffff0000, v44\nZaaa 92,216,184\nMU 52,48,50\n"
"Maaa 92,220,188\nMU 53,49,50\nZaaa 88,212,188\nCV 52,52,53\nv_and_b32 v53, 0xffff0000, v52\nMaaa 88,208,184\nv_lshlrev_b32 v52, 16, v52\nZaaa 28,200,184\nFM 56,48,51\nFM 57,49,51\n"
"Maaa 28,204,188\nD0 5,20,48,3072\nZaaa 24,196,188\nAD 54,54,52\nAD 55,55,53\nMaaa 24,192,184\nWL 0\nMvaa 96,128,224\nTa 160,12,32768\nv_lshlrev_b32 v48, 16, v33\nMvaa 96,132,228\n"
"Ta 162,12,34816\nMvaa 100,140,228\ns_add_u32 m0, s47, -32768\ns_add_u32 s45, s45, 128\nMvaa 100,136,224\nTa 164,12,49152\nv_lshlrev_b32 v49, 16, v37\nMvaa 160,144,224\n"
"Ta 166,12,51200\nMvaa 160,148,228\nD0 2,20,45,0\nv_lshlrev_b32 v50, 16, v41\nMvaa 164,156,228\nTa 168,13,32768\nMvaa 164,152,224\nTa 170,13,34816\nv_lshlrev_b32 v51, 16, v45\n"
"Mvaa 172,152,232\nMU 52,48,50\nMvaa 172,156,236\nTa 172,13,49152\nMvaa 168,148,236\nTa 174,13,51200\nMU 53,49,50\nMvaa 168,144,232\nCV 52,52,53\nMvaa 108,136,232\nTa 176,14,32768\n"
"v_and_b32 v53, 0xffff0000, v52\nMvaa 108,140,236\nTa 178,14,34816\nD0 3,20,45,1024\nMvaa 104,132,236\nv_lshlrev_b32 v52, 16, v52\nFM 56,48,51\nMvaa 104,128,232\nTa 180,14,49152\n"
"Mvaa 112,128,240\nTa 182,14,51200\nFM 57,49,51\nMvaa 112,132,244\nAD 54,54,52\nMvaa 116,140,244\nTa 184,15,32768\nMvaa 116,136,240\nTa 186,15,34816\nAD 55,55,53\nMvaa 176,144,240\n"
"v_and_b32 v48, 0xffff0000, v33\nMvaa 176,148,244\nTa 188,15,49152\nD0 4,20,45,2048\nMvaa 180,156,244\nTa 190,15,51200\nMvaa 180,152,240\nv_and_b32 v49, 0xffff0000, v37\n"
"v_and_b32 v50, 0xffff0000, v41\nMvaa 188,152,248\nv_and_b32 v51, 0xffff0000, v45\nMvaa 188,156,252\nMU 52,48,50\nMvaa 184,148,252\nMU 53,49,50\nCV 52,52,53\nMvaa 184,144,248\n"
"v_and_b32 v53, 0xffff0000, v52\nMvaa 124,136,248\nv_lshlrev_b32 v52, 16, v52\nFM 56,48,51\nMvaa 124,140,252\nD0 5,20,45,3072\nMvaa 120,132,252\nFM 57,49,51\nAD 54,54,52\n"
"Mvaa 120,128,248\ns_waitcnt vmcnt(18) lgkmcnt(0)\ns_barrier\nMvaa 64,128,160\nRa 192,10,49152\nAD 55,55,53\nMvaa 64,132,164\nv_lshlrev_b32 v48, 16, v34\nMvaa 68,140,164\n"
"Ra 196,11,49152\ns_add_u32 m0, s47, 32768\ns_add_u32 s44, s44, 0x40000\nMvaa 68,136,160\nv_lshlrev_b32 v49, 16, v38\nv_lshlrev_b32 v50, 16, v42\nMvaa 128,144,160\nRa 200,10,51200\n"
"Mvaa 128,148,164\nD0 6,24,44,0\nv_lshlrev_b32 v51, 16, v46\nMvaa 132,156,164\nRa 204,11,51200\nMvaa 132,152,160\nMU 52,48,50\nMU 53,49,50\nMvaa 140,152,168\nRa 208,10,57344\n"
"Mvaa 140,156,172\nCV 52,52,53\nMvaa 136,148,172\nRa 212,11,57344\nv_and_b32 v53, 0xffff0000, v52\nMvaa 136,144,168\nv_lshlrev_b32 v52, 16, v52\nMvaa 76,136,168\nRa 216,10,59392\n"
"FM 56,48,51\nMvaa 76,140,172\nD0 7,24,44,1024\nMvaa 72,132,172\nRa 220,11,59392\nFM 57,49,51\nMvaa 72,128,168\nAD 54,54,52\nMvaa 80,128,176\nAD 55,55,53\n"
"v_and_b32 v48, 0xffff0000, v34\nMvaa 80,132,180\nv_and_b32 v49, 0xffff0000, v38\nMvaa 84,140,180\nv_and_b32 v50, 0xffff0000, v42\nMvaa 84,136,176\nv_and_b32 v51, 0xffff0000, v46\n"
"MU 52,48,50\nMvaa 144,144,176\nMU 53,49,50\nMvaa 144,148,180\nD0 8,24,44,2048\nCV 52,52,53\nMvaa 148,156,180\nv_and_b32 v53, 0xffff0000, v52\nMvaa 148,152,176\n"
"v_lshlrev_b32 v52, 16, v52\nFM 56,48,51\nMvaa 156,152,184\nFM 57,49,51\nMvaa 156,156,188\nAD 54,54,52\nMvaa 152,148,188\nAD 55,55,53\nv_lshlrev_b32 v48, 16, v35\nMvaa 152,144,184\n"
"v_lshlrev_b32 v49, 16, v39\nMvaa 92,136,184\nv_lshlrev_b32 v50, 16, v43\nv_lshlrev_b32 v51, 16, v47\nMvaa 92,140,188\nD0 9,24,44,3072\nMvaa 88,132,188\nMU 52,48,50\nMU 53,49,50\n"
"Mvaa 88,128,184\nWL 0\nMaaa 0,192,160\nRa 128,10,0\nCV 52,52,53\nMaaa 0,196,164\nv_and_b32 v53, 0xffff0000, v52\nMaaa 4,204,164\nRa 132,11,0\ns_add_u32 m0, s47, 49152\n"
"s_add_u32 s44, s44, 0x40000\nMaaa 4,200,160\nv_lshlrev_b32 v52, 16, v52\nFM 56,48,51\nMaaa 64,208,160\nRa 136,10,2048\nMaaa 64,212,164\nD0 6,24,44,0\nFM 57,49,51\nMaaa 68,220,164\n"
"Ra 140,11,2048\nMaaa 68,216,160\nAD 54,54,52\nAD 55,55,53\nMaaa 76,216,168\nRa 144,10,8192\nMaaa 76,220,172\nv_and_b32 v48, 0xffff0000, v35\nMaaa 72,212,172\nRa 148,11,8192\n"
"v_and_b32 v49, 0xffff0000, v39\nMaaa 72,208,168\nv_and_b32 v50, 0xffff0000, v43\nMaaa 12,200,168\nRa 152,10,10240\nv_and_b32 v51, 0xffff0000, v47\nMaaa 12,204,172\n"
"D0 7,24,44,1024\nMaaa 8,196,172\nRa 156,11,10240\nMU 52,48,50\nMaaa 8,192,168\nMU 53,49,50\nMaaa 16,192,176\nCV 52,52,53\nv_and_b32 v53, 0xffff0000, v52\nMaaa 16,196,180\n"
"v_lshlrev_b32 v52, 16, v52\nMaaa 20,204,180\nFM 56,48,51\nMaaa 20,200,176\nFM 57,49,51\nAD 54,54,52\nMaaa 80,208,176\nAD 55,55,53\nMaaa 80,212,180\nD0 8,24,44,2048\n"
"v_permlane32_swap_b32 v54, v55\nMaaa 84,220,180\nv_permlane32_swap_b32 v56, v57\nMaaa 84,216,176\nAD 54,54,55\nAD 56,56,57\nMaaa 92,216,184\nMaaa 92,220,188\n"
"v_permlane16_swap_b32 v54, v56\nMaaa 88,212,188\nAD 54,54,56\nMaaa 88,208,184\nMaaa 28,200,184\nv_add_f32_dpp v54, v54, v54 row_ror:8 row_mask:0xf bank_mask:0xf\nMaaa 28,204,188\n"
"D0 9,24,44,3072\nMaaa 24,196,188\nv_add_f32_dpp v54, v54, v54 row_ror:4 row_mask:0xf bank_mask:0xf\nMaaa 24,192,184\ns_waitcnt vmcnt(12) lgkmcnt(0)\ns_barrier\nMaaa 32,192,224\n"
"Ta 160,12,0\nv_add_f32_dpp v54, v54, v54 row_ror:2 row_mask:0xf bank_mask:0xf\nMaaa 32,196,228\nTa 162,12,2048\nMaaa 36,204,228\ns_add_u32 m0, s47, -16384\n"
"s_add_u32 s48, s48, 128\nMaaa 36,200,224\nTa 164,12,16384\nv_add_f32_dpp v54, v54, v54 row_ror:1 row_mask:0xf bank_mask:0xf\nMaaa 96,208,224\nTa 166,12,18432\n"
"buffer_load_dword v31, v24, s[24:27], s96 offen\ns_add_u32 s96, s96, 0x80000\nMaaa 96,212,228\nD0 2,20,48,0\nCV 54,54,54\nMaaa 100,220,228\nTa 168,13,0\nMaaa 100,216,224\n"
"Ta 170,13,2048\nv_lshlrev_b32 v54, 16, v54\nMaaa 108,216,232\nv_mul_f32 v55, 0xbfb8aa3b, v54\nEX 55,55\nMaaa 108,220,236\nTa 172,13,16384\nMaaa 104,212,236\nTa 174,13,18432\n"
"v_add_f32 v56, 0, v54\nMaaa 104,208,232\nv_add_f32 v55, 1.0, v55\nv_rcp_f32 v55, v55\nMaaa 44,200,232\nTa 176,14,0\nv_ashrrev_i32 v57, 31, v56\nMaaa 44,204,236\nTa 178,14,2048\n"
"D0 3,20,48,1024\nMaaa 40,196,236\nCV 55,55,55\nv_lshlrev_b32 v55, 16, v55\nMaaa 40,192,232\nTa 180,14,16384\nMaaa 48,192,240\nTa 182,14,18432\nv_or_b32 v57, 0x80000000, v57\n"
"Maaa 48,196,244\nv_xor_b32 v56, v56, v57\nMaaa 52,204,244\nTa 184,15,0\nMaaa 52,200,240\nTa 186,15,2048\nv_and_b32 v56, 0xffff0000, v56\nMaaa 112,208,240\n"
"s_lshl2_add_u32 s2, s10, s11\nMaaa 112,212,244\nTa 188,15,16384\nD0 4,20,48,2048\nMaaa 116,220,244\nTa 190,15,18432\nMaaa 116,216,240\nv_lshrrev_b32 v48, 5, v0\n"
"v_lshl_add_u32 v48, s2, 1, v48\nMaaa 124,216,248\nv_sub_u32 v49, 0xffff, v48\nMaaa 124,220,252\nv_or_b32 v56, v56, v49\nMaaa 120,212,252\nv_mov_b32 v57, s15\n"
"v_and_b32 v49, 31, v0\nMaaa 120,208,248\nv_mov_b32 v52, 0x40000000\nv_cmp_eq_u32 vcc, 16, v49\nv_lshlrev_b32 v50, 2, v48\nv_lshlrev_b32 v51, 10, v49\n"
"v_cndmask_b32 v50, v52, v50, vcc\nv_cndmask_b32 v56, v55, v56, vcc\nMaaa 60,200,248\nv_lshl_add_u32 v51, v48, 3, v51\nv_and_b32 v49, 15, v0\nMaaa 60,204,252\nD0 5,20,48,3072\n"
"v_cmp_eq_u32 vcc, 0, v49\ns_mov_b32 s2, 0xc040000\ns_mov_b32 s3, 0xc03e000\nv_cndmask_b32 v51, v52, v51, vcc\nbuffer_store_dwordx2 v[56:57], v51, s[16:19], s2 offen sc0 sc1\n"
"buffer_store_dword v54, v50, s[16:19], s3 offen sc0 sc1\nMaaa 56,196,252\nMaaa 56,192,248\nWL 0\ns_mov_b32 s50, 3\n.GL433:\nMvaa 64,128,160\nTa 224,16,0\nMvaa 64,132,164\n"
"Ta 226,16,2048\nMvaa 68,140,164\nTa 228,16,16384\ns_add_u32 m0, s47, -65536\ns_add_u32 s45, s45, 128\nMvaa 68,136,160\nTa 230,16,18432\nMvaa 128,144,160\nTa 232,17,0\n"
"Mvaa 128,148,164\nTa 234,17,2048\nD0 2,20,45,0\nMvaa 132,156,164\nTa 236,17,16384\nMvaa 132,152,160\nTa 238,17,18432\nMvaa 140,152,168\nTa 240,18,0\nMvaa 140,156,172\n"
"Ta 242,18,2048\nMvaa 136,148,172\nTa 244,18,16384\nMvaa 136,144,168\nTa 246,18,18432\nMvaa 76,136,168\nTa 248,19,0\nMvaa 76,140,172\nTa 250,19,2048\nD0 3,20,45,1024\n"
"Mvaa 72,132,172\nTa 252,19,16384\nMvaa 72,128,168\nTa 254,19,18432\nMvaa 80,128,176\nRa 192,10,16384\nMvaa 80,132,180\nRa 196,11,16384\nMvaa 84,140,180\nRa 200,10,18432\n"
"Mvaa 84,136,176\nRa 204,11,18432\nMvaa 144,144,176\nRa 208,10,24576\nMvaa 144,148,180\nRa 212,11,24576\nD0 4,20,45,2048\nMvaa 148,156,180\nRa 216,10,26624\nMvaa 148,152,176\n"
"Ra 220,11,26624\nMvaa 156,152,184\nMvaa 156,156,188\nMvaa 152,148,188\nMvaa 152,144,184\nMvaa 92,136,184\nMvaa 92,140,188\nD0 5,20,45,3072\nMvaa 88,132,188\nMvaa 88,128,184\n"
"s_waitcnt vmcnt(18) lgkmcnt(8)\ns_barrier\nMvaa 96,128,224\nMvaa 96,132,228\ns_mov_b32 s86, 0x1d2c3b4a\ns_load_dword s86, s[54:55], s94\nMvaa 100,140,228\ns_add_u32 m0, s47, 0\n"
"s_add_u32 s44, s44, 0x40000\ns_add_u32 s94, s94, 1024\nMvaa 100,136,224\nMvaa 160,144,224\nMvaa 160,148,228\nD0 6,24,44,0\nMvaa 164,156,228\nMvaa 164,152,224\nMvaa 172,152,232\n"
"Mvaa 172,156,236\nMvaa 168,148,236\nMvaa 168,144,232\ns_cmp_eq_u32 s86, 0x1d2c3b4a\ns_addc_u32 s88, s88, 0\nMvaa 108,136,232\nMvaa 108,140,236\nD0 7,24,44,1024\n"
"s_cmp_eq_u32 s86, 0x1d2c3b4a\ns_addc_u32 s88, s88, 0\nMvaa 104,132,236\nMvaa 104,128,232\ns_cmp_eq_u32 s86, 0x1d2c3b4a\ns_addc_u32 s88, s88, 0\nMvaa 112,128,240\nMvaa 112,132,244\n"
"s_cmp_eq_u32 s86, 0x1d2c3b4a\ns_addc_u32 s88, s88, 0\nMvaa 116,140,244\nMvaa 116,136,240\ns_cmp_eq_u32 s86, 0x1d2c3b4a\ns_addc_u32 s88, s88, 0\nMvaa 176,144,240\nMvaa 176,148,244\n"
"D0 8,24,44,2048\ns_cmp_eq_u32 s86, 0x1d2c3b4a\ns_addc_u32 s88, s88, 0\nMvaa 180,156,244\nMvaa 180,152,240\ns_cmp_eq_u32 s86, 0x1d2c3b4a\ns_addc_u32 s88, s88, 0\nMvaa 188,152,248\n"
"Mvaa 188,156,252\ns_cmp_eq_u32 s86, 0x1d2c3b4a\ns_addc_u32 s88, s88, 0\nMvaa 184,148,252\nMvaa 184,144,248\ns_cmp_eq_u32 s86, 0x1d2c3b4a\ns_addc_u32 s88, s88, 0\nMvaa 124,136,248\n"
"Mvaa 124,140,252\nD0 9,24,44,3072\ns_cmp_eq_u32 s86, 0x1d2c3b4a\ns_addc_u32 s88, s88, 0\nMvaa 120,132,252\nMvaa 120,128,248\nWL 0\ns_cmp_eq_u32 s86, 0x1d2c3b4a\n"
"s_addc_u32 s88, s88, 0\nMaaa 32,192,224\nRa 128,10,32768\nMaaa 32,196,228\ns_cmp_eq_u32 s86, 0x1d2c3b4a\ns_addc_u32 s88, s88, 0\nMaaa 36,204,228\nRa 132,11,32768\n"
"s_add_u32 m0, s47, 16384\ns_add_u32 s44, s44, 0x40000\nMaaa 36,200,224\ns_cmp_eq_u32 s86, 0x1d2c3b4a\ns_addc_u32 s88, s88, 0\nMaaa 96,208,224\nRa 136,10,34816\nMaaa 96,212,228\n"
"D0 6,24,44,0\ns_cmp_eq_u32 s86, 0x1d2c3b4a\ns_addc_u32 s88, s88, 0\nMaaa 100,220,228\nRa 140,11,34816\nMaaa 100,216,224\ns_cmp_eq_u32 s86, 0x1d2c3b4a\ns_addc_u32 s88, s88, 0\n"
"Maaa 108,216,232\nRa 144,10,40960\nMaaa 108,220,236\ns_cmp_eq_u32 s86, 0x1d2c3b4a\ns_addc_u32 s88, s88, 0\nMaaa 104,212,236\nRa 148,11,40960\nMaaa 104,208,232\nMaaa 44,200,232\n"
"Ra 152,10,43008\nMaaa 44,204,236\nD0 7,24,44,1024\nMaaa 40,196,236\nRa 156,11,43008\nMaaa 40,192,232\nMaaa 48,192,240\nMaaa 48,196,244\nMaaa 52,204,244\nMaaa 52,200,240\n"
"Maaa 112,208,240\nMaaa 112,212,244\nD0 8,24,44,2048\nMaaa 116,220,244\nMaaa 116,216,240\nMaaa 124,216,248\nMaaa 124,220,252\nMaaa 120,212,252\nMaaa 120,208,248\nMaaa 60,200,248\n"
"Maaa 60,204,252\nD0 9,24,44,3072\nMaaa 56,196,252\nMaaa 56,192,248\ns_waitcnt vmcnt(12) lgkmcnt(0)\ns_barrier\nMaaa 0,192,160\nTa 224,16,32768\ns_mov_b32 m0, s76\n"
"s_add_u32 s76, s76, 1024\nMaaa 0,196,164\nTa 226,16,34816\nD0 1,36,75,0\ns_add_u32 s75, s75, 1024\nMaaa 4,204,164\ns_add_u32 m0, s47, -49152\ns_add_u32 s48, s48, 128\n"
"Maaa 4,200,160\nTa 228,16,49152\nMaaa 64,208,160\nTa 230,16,51200\nbuffer_load_dword v31, v24, s[24:27], s96 offen\ns_add_u32 s96, s96, 0x80000\nMaaa 64,212,164\nD0 2,20,48,0\n"
"Maaa 68,220,164\nTa 232,17,32768\nMaaa 68,216,160\nTa 234,17,34816\nMaaa 76,216,168\nMaaa 76,220,172\nTa 236,17,49152\nMaaa 72,212,172\nTa 238,17,51200\nMaaa 72,208,168\n"
"Maaa 12,200,168\nTa 240,18,32768\nMaaa 12,204,172\nTa 242,18,34816\nD0 3,20,48,1024\nMaaa 8,196,172\nMaaa 8,192,168\nTa 244,18,49152\nMaaa 16,192,176\nTa 246,18,51200\n"
"Maaa 16,196,180\nMaaa 20,204,180\nTa 248,19,32768\nMaaa 20,200,176\nTa 250,19,34816\nMaaa 80,208,176\nMaaa 80,212,180\nTa 252,19,49152\nD0 4,20,48,2048\nMaaa 84,220,180\n"
"Ta 254,19,51200\nMaaa 84,216,176\nMaaa 92,216,184\nMaaa 92,220,188\nMaaa 88,212,188\nMaaa 88,208,184\nMaaa 28,200,184\nMaaa 28,204,188\nD0 5,20,48,3072\nMaaa 24,196,188\n"
"Maaa 24,192,184\nWL 0\nMvaa 96,128,224\nTa 160,12,32768\nMvaa 96,132,228\nTa 162,12,34816\nMvaa 100,140,228\nTa 164,12,49152\ns_add_u32 m0, s47, -32768\ns_add_u32 s45, s45, 128\n"
"Mvaa 100,136,224\nTa 166,12,51200\nMvaa 160,144,224\nTa 168,13,32768\nMvaa 160,148,228\nTa 170,13,34816\nD0 2,20,45,0\nMvaa 164,156,228\nTa 172,13,49152\nMvaa 164,152,224\n"
"Ta 174,13,51200\nMvaa 172,152,232\nTa 176,14,32768\nMvaa 172,156,236\nTa 178,14,34816\nMvaa 168,148,236\nTa 180,14,49152\nMvaa 168,144,232\nTa 182,14,51200\nMvaa 108,136,232\n"
"Ta 184,15,32768\nMvaa 108,140,236\nTa 186,15,34816\nD0 3,20,45,1024\nMvaa 104,132,236\nTa 188,15,49152\nMvaa 104,128,232\nTa 190,15,51200\nMvaa 112,128,240\nRa 192,10,49152\n"
"Mvaa 112,132,244\nRa 196,11,49152\nMvaa 116,140,244\nRa 200,10,51200\nMvaa 116,136,240\nRa 204,11,51200\nMvaa 176,144,240\nRa 208,10,57344\nMvaa 176,148,244\nRa 212,11,57344\n"
"D0 4,20,45,2048\nMvaa 180,156,244\nRa 216,10,59392\nMvaa 180,152,240\nRa 220,11,59392\nMvaa 188,152,248\nMvaa 188,156,252\nMvaa 184,148,252\nMvaa 184,144,248\nMvaa 124,136,248\n"
"Mvaa 124,140,252\nD0 5,20,45,3072\nMvaa 120,132,252\nMvaa 120,128,248\ns_waitcnt vmcnt(18) lgkmcnt(8)\ns_barrier\nMvaa 64,128,160\nMvaa 64,132,164\ns_mov_b32 s86, 0x1d2c3b4a\n"
"s_load_dword s86, s[54:55], s95\nMvaa 68,140,164\ns_add_u32 m0, s47, 32768\ns_add_u32 s44, s44, 0x40000\ns_add_u32 s95, s95, 1024\nMvaa 68,136,160\nMvaa 128,144,160\n"
"Mvaa 128,148,164\nD0 6,24,44,0\nMvaa 132,156,164\nMvaa 132,152,160\nMvaa 140,152,168\nMvaa 140,156,172\nMvaa 136,148,172\nMvaa 136,144,168\ns_cmp_eq_u32 s86, 0x1d2c3b4a\n"
"s_subb_u32 s88, s88, 0\nMvaa 76,136,168\nMvaa 76,140,172\nD0 7,24,44,1024\ns_cmp_eq_u32 s86, 0x1d2c3b4a\ns_subb_u32 s88, s88, 0\nMvaa 72,132,172\nMvaa 72,128,168\n"
"s_cmp_eq_u32 s86, 0x1d2c3b4a\ns_subb_u32 s88, s88, 0\nMvaa 80,128,176\nMvaa 80,132,180\ns_cmp_eq_u32 s86, 0x1d2c3b4a\ns_subb_u32 s88, s88, 0\nMvaa 84,140,180\nMvaa 84,136,176\n"
"s_cmp_eq_u32 s86, 0x1d2c3b4a\ns_subb_u32 s88, s88, 0\nMvaa 144,144,176\nMvaa 144,148,180\nD0 8,24,44,2048\ns_cmp_eq_u32 s86, 0x1d2c3b4a\ns_subb_u32 s88, s88, 0\nMvaa 148,156,180\n"
"Mvaa 148,152,176\ns_cmp_eq_u32 s86, 0x1d2c3b4a\ns_subb_u32 s88, s88, 0\nMvaa 156,152,184\nMvaa 156,156,188\ns_cmp_eq_u32 s86, 0x1d2c3b4a\ns_subb_u32 s88, s88, 0\nMvaa 152,148,188\n"
"Mvaa 152,144,184\ns_cmp_eq_u32 s86, 0x1d2c3b4a\ns_subb_u32 s88, s88, 0\nMvaa 92,136,184\nMvaa 92,140,188\nD0 9,24,44,3072\ns_cmp_eq_u32 s86, 0x1d2c3b4a\ns_subb_u32 s88, s88, 0\n"
"Mvaa 88,132,188\nMvaa 88,128,184\nWL 0\ns_cmp_eq_u32 s86, 0x1d2c3b4a\ns_subb_u32 s88, s88, 0\nMaaa 0,192,160\nRa 128,10,0\nMaaa 0,196,164\ns_cmp_eq_u32 s86, 0x1d2c3b4a\n"
"s_subb_u32 s88, s88, 0\nMaaa 4,204,164\nRa 132,11,0\ns_add_u32 m0, s47, 49152\ns_add_u32 s44, s44, 0x40000\nMaaa 4,200,160\ns_cmp_eq_u32 s86, 0x1d2c3b4a\ns_subb_u32 s88, s88, 0\n"
"Maaa 64,208,160\nRa 136,10,2048\nMaaa 64,212,164\nD0 6,24,44,0\ns_cmp_eq_u32 s86, 0x1d2c3b4a\ns_subb_u32 s88, s88, 0\nMaaa 68,220,164\nRa 140,11,2048\nMaaa 68,216,160\n"
"s_cmp_eq_u32 s86, 0x1d2c3b4a\ns_subb_u32 s88, s88, 0\nMaaa 76,216,168\nRa 144,10,8192\nMaaa 76,220,172\ns_cmp_eq_u32 s86, 0x1d2c3b4a\ns_subb_u32 s88, s88, 0\nMaaa 72,212,172\n"
"Ra 148,11,8192\nMaaa 72,208,168\nMaaa 12,200,168\nRa 152,10,10240\nMaaa 12,204,172\nD0 7,24,44,1024\nMaaa 8,196,172\nRa 156,11,10240\nMaaa 8,192,168\nMaaa 16,192,176\n"
"Maaa 16,196,180\nMaaa 20,204,180\nMaaa 20,200,176\nMaaa 80,208,176\nMaaa 80,212,180\nD0 8,24,44,2048\nMaaa 84,220,180\nMaaa 84,216,176\nMaaa 92,216,184\nMaaa 92,220,188\n"
"Maaa 88,212,188\nMaaa 88,208,184\nMaaa 28,200,184\nMaaa 28,204,188\nD0 9,24,44,3072\nMaaa 24,196,188\nMaaa 24,192,184\ns_waitcnt vmcnt(12) lgkmcnt(0)\ns_barrier\nMaaa 32,192,224\n"
"Ta 160,12,0\ns_mov_b32 m0, s76\ns_add_u32 s76, s76, 1024\nMaaa 32,196,228\nTa 162,12,2048\nD0 1,36,75,0\ns_add_u32 s75, s75, 1024\nMaaa 36,204,228\ns_add_u32 m0, s47, -16384\n"
"s_add_u32 s48, s48, 128\nMaaa 36,200,224\nTa 164,12,16384\nMaaa 96,208,224\nTa 166,12,18432\nbuffer_load_dword v31, v24, s[24:27], s96 offen\ns_add_u32 s96, s96, 0x80000\n"
"Maaa 96,212,228\nD0 2,20,48,0\nMaaa 100,220,228\nTa 168,13,0\nMaaa 100,216,224\nTa 170,13,2048\nMaaa 108,216,232\nMaaa 108,220,236\nTa 172,13,16384\nMaaa 104,212,236\n"
"Ta 174,13,18432\nMaaa 104,208,232\nMaaa 44,200,232\nTa 176,14,0\nMaaa 44,204,236\nTa 178,14,2048\nD0 3,20,48,1024\nMaaa 40,196,236\nMaaa 40,192,232\nTa 180,14,16384\n"
"Maaa 48,192,240\nTa 182,14,18432\nMaaa 48,196,244\nMaaa 52,204,244\nTa 184,15,0\nMaaa 52,200,240\nTa 186,15,2048\nMaaa 112,208,240\nMaaa 112,212,244\nTa 188,15,16384\n"
"D0 4,20,48,2048\nMaaa 116,220,244\nTa 190,15,18432\nMaaa 116,216,240\nMaaa 124,216,248\nMaaa 124,220,252\nMaaa 120,212,252\nMaaa 120,208,248\nMaaa 60,200,248\n"
"s_xor_b32 s94, s94, 0x2000\ns_xor_b32 s95, s95, 0x2000\ns_sub_u32 s88, 0, s88\nMaaa 60,204,252\nD0 5,20,48,3072\ns_sub_u32 s50, s50, 1\nMaaa 56,196,252\nMaaa 56,192,248\nWL 0\n"
"s_cbranch_scc0 .GL433\nMvaa 64,128,160\nTa 224,16,0\nMvaa 64,132,164\nTa 226,16,2048\ns_cmp_gt_i32 s88, 0\ns_cselect_b32 s88, 0x2000, 0\nMvaa 68,140,164\n"
"s_add_u32 m0, s47, -65536\ns_add_u32 s45, s45, 128\nMvaa 68,136,160\nTa 228,16,16384\nMvaa 128,144,160\nTa 230,16,18432\nMvaa 128,148,164\nD0 2,20,45,0\nMvaa 132,156,164\n"
"Ta 232,17,0\nMvaa 132,152,160\nTa 234,17,2048\nMvaa 140,152,168\ns_xor_b32 s88, s88, s92\nMvaa 140,156,172\nTa 236,17,16384\nMvaa 136,148,172\nTa 238,17,18432\nMvaa 136,144,168\n"
"v_xor_b32 v27, s88, v25\nMvaa 76,136,168\nTa 240,18,0\nMvaa 76,140,172\nTa 242,18,2048\nD0 3,20,45,1024\nMvaa 72,132,172\nbuffer_store_dword v27, off, s[16:19], s93 sc0 sc1\n"
"Mvaa 72,128,168\nTa 244,18,16384\nMvaa 80,128,176\nTa 246,18,18432\nMvaa 80,132,180\nMvaa 84,140,180\nTa 248,19,0\nMvaa 84,136,176\nTa 250,19,2048\nMvaa 144,144,176\n"
"Mvaa 144,148,180\nTa 252,19,16384\nD0 4,20,45,2048\nMvaa 148,156,180\nTa 254,19,18432\nMvaa 148,152,176\nMvaa 156,152,184\nMvaa 156,156,188\nMvaa 152,148,188\nMvaa 152,144,184\n"
"Mvaa 92,136,184\nMvaa 92,140,188\nD0 5,20,45,3072\nMvaa 88,132,188\nMvaa 88,128,184\ns_waitcnt vmcnt(19) lgkmcnt(0)\ns_barrier\nMvaa 96,128,224\nRa 192,10,16384\nMvaa 96,132,228\n"
"Mvaa 100,140,228\nRa 196,11,16384\ns_add_u32 m0, s47, 0\ns_add_u32 s44, s44, 0x40000\nMvaa 100,136,224\nMvaa 160,144,224\nRa 200,10,18432\nMvaa 160,148,228\nD0 6,24,44,0\n"
"Mvaa 164,156,228\nRa 204,11,18432\nMvaa 164,152,224\nMvaa 172,152,232\nRa 208,10,24576\nMvaa 172,156,236\nMvaa 168,148,236\nRa 212,11,24576\nMvaa 168,144,232\nMvaa 108,136,232\n"
"Ra 216,10,26624\nMvaa 108,140,236\nD0 7,24,44,1024\nMvaa 104,132,236\nRa 220,11,26624\nMvaa 104,128,232\nMvaa 112,128,240\nMvaa 112,132,244\nMvaa 116,140,244\nMvaa 116,136,240\n"
"Mvaa 176,144,240\nMvaa 176,148,244\nD0 8,24,44,2048\nMvaa 180,156,244\nMvaa 180,152,240\nMvaa 188,152,248\nMvaa 188,156,252\nMvaa 184,148,252\nMvaa 184,144,248\nMvaa 124,136,248\n"
"Mvaa 124,140,252\nD0 9,24,44,3072\nMvaa 120,132,252\nMvaa 120,128,248\nWL 0\nMaaa 32,192,224\nRa 128,10,32768\nMaaa 32,196,228\nMaaa 36,204,228\nRa 132,11,32768\n"
"s_add_u32 m0, s47, 16384\ns_add_u32 s44, s44, 0x40000\nMaaa 36,200,224\nMaaa 96,208,224\nRa 136,10,34816\nMaaa 96,212,228\nD0 6,24,44,0\nMaaa 100,220,228\nRa 140,11,34816\n"
"Maaa 100,216,224\nMaaa 108,216,232\nRa 144,10,40960\nMaaa 108,220,236\nMaaa 104,212,236\nRa 148,11,40960\nMaaa 104,208,232\nMaaa 44,200,232\nRa 152,10,43008\nMaaa 44,204,236\n"
"D0 7,24,44,1024\nMaaa 40,196,236\nRa 156,11,43008\nMaaa 40,192,232\nMaaa 48,192,240\nMaaa 48,196,244\nMaaa 52,204,244\nMaaa 52,200,240\nMaaa 112,208,240\nMaaa 112,212,244\n"
"D0 8,24,44,2048\nMaaa 116,220,244\nMaaa 116,216,240\nMaaa 124,216,248\nMaaa 124,220,252\nMaaa 120,212,252\nMaaa 120,208,248\nMaaa 60,200,248\nMaaa 60,204,252\nD0 9,24,44,3072\n"
"Maaa 56,196,252\nMaaa 56,192,248\ns_waitcnt vmcnt(19) lgkmcnt(0)\ns_barrier\nMaaa 0,192,160\nTa 224,16,32768\nMaaa 0,196,164\nTa 226,16,34816\nMaaa 4,204,164\n"
"s_add_u32 m0, s47, -49152\ns_add_u32 s48, s48, 128\nMaaa 4,200,160\nTa 228,16,49152\nMaaa 64,208,160\nTa 230,16,51200\nbuffer_load_dword v31, v24, s[24:27], s96 offen\n"
"s_add_u32 s96, s96, 0x80000\nMaaa 64,212,164\nD0 2,20,48,0\nMaaa 68,220,164\nTa 232,17,32768\nMaaa 68,216,160\nTa 234,17,34816\nMaaa 76,216,168\nMaaa 76,220,172\nTa 236,17,49152\n"
"Maaa 72,212,172\nTa 238,17,51200\nMaaa 72,208,168\nMaaa 12,200,168\nTa 240,18,32768\nMaaa 12,204,172\nTa 242,18,34816\nD0 3,20,48,1024\nMaaa 8,196,172\nMaaa 8,192,168\n"
"Ta 244,18,49152\nMaaa 16,192,176\nTa 246,18,51200\nMaaa 16,196,180\nMaaa 20,204,180\nTa 248,19,32768\nMaaa 20,200,176\nTa 250,19,34816\nMaaa 80,208,176\nMaaa 80,212,180\n"
"Ta 252,19,49152\nD0 4,20,48,2048\nMaaa 84,220,180\nTa 254,19,51200\nMaaa 84,216,176\nMaaa 92,216,184\nMaaa 92,220,188\nMaaa 88,212,188\nMaaa 88,208,184\nMaaa 28,200,184\n"
"Maaa 28,204,188\nD0 5,20,48,3072\nMaaa 24,196,188\nMaaa 24,192,184\nWL 0\nMvaa 96,128,224\nTa 160,12,32768\nMvaa 96,132,228\nTa 162,12,34816\nMvaa 100,140,228\n"
"s_add_u32 m0, s47, -32768\ns_add_u32 s45, s45, 128\nMvaa 100,136,224\nTa 164,12,49152\nMvaa 160,144,224\nTa 166,12,51200\nMvaa 160,148,228\nD0 2,20,45,0\nMvaa 164,156,228\n"
"Ta 168,13,32768\nMvaa 164,152,224\nTa 170,13,34816\nMvaa 172,152,232\nMvaa 172,156,236\nTa 172,13,49152\nMvaa 168,148,236\nTa 174,13,51200\nMvaa 168,144,232\nMvaa 108,136,232\n"
"Ta 176,14,32768\nMvaa 108,140,236\nTa 178,14,34816\nD0 3,20,45,1024\nMvaa 104,132,236\nMvaa 104,128,232\nTa 180,14,49152\nMvaa 112,128,240\nTa 182,14,51200\nMvaa 112,132,244\n"
"Mvaa 116,140,244\nTa 184,15,32768\nMvaa 116,136,240\nTa 186,15,34816\nMvaa 176,144,240\nMvaa 176,148,244\nTa 188,15,49152\nD0 4,20,45,2048\nMvaa 180,156,244\nTa 190,15,51200\n"
"Mvaa 180,152,240\nMvaa 188,152,248\nMvaa 188,156,252\nMvaa 184,148,252\nMvaa 184,144,248\nMvaa 124,136,248\nMvaa 124,140,252\nD0 5,20,45,3072\nMvaa 120,132,252\nMvaa 120,128,248\n"
"s_waitcnt vmcnt(17) lgkmcnt(0)\ns_barrier\nMvaa 64,128,160\nRa 192,10,49152\nMvaa 64,132,164\nMvaa 68,140,164\nRa 196,11,49152\ns_add_u32 m0, s47, 32768\n"
"s_add_u32 s44, s44, 0x40000\nMvaa 68,136,160\nMvaa 128,144,160\nRa 200,10,51200\nMvaa 128,148,164\nD0 6,24,44,0\nMvaa 132,156,164\nRa 204,11,51200\nMvaa 132,152,160\n"
"Mvaa 140,152,168\nRa 208,10,57344\nMvaa 140,156,172\ns_lshl_b32 s5, s73, 8\ns_add_u32 s5, s5, 0xc04ac00\nv_lshrrev_b32 v27, 2, v1\n"
"buffer_load_dword v27, v27, s[16:19], s5 offen sc0 sc1\nMvaa 136,148,172\nRa 212,11,57344\nMvaa 136,144,168\nMvaa 76,136,168\nRa 216,10,59392\nMvaa 76,140,172\nD0 7,24,44,1024\n"
"Mvaa 72,132,172\nRa 220,11,59392\nMvaa 72,128,168\nMvaa 80,128,176\nMvaa 80,132,180\nMvaa 84,140,180\nMvaa 84,136,176\nMvaa 144,144,176\nMvaa 144,148,180\nD0 8,24,44,2048\n"
"Mvaa 148,156,180\nMvaa 148,152,176\nMvaa 156,152,184\nMvaa 156,156,188\nMvaa 152,148,188\nMvaa 152,144,184\nMvaa 92,136,184\nMvaa 92,140,188\nD0 9,24,44,3072\nMvaa 88,132,188\n"
"Mvaa 88,128,184\nWL 0\nMaaa 0,192,160\nRa 128,10,0\nMaaa 0,196,164\nMaaa 4,204,164\nRa 132,11,0\ns_add_u32 m0, s47, 49152\ns_add_u32 s44, s44, 0x40000\nMaaa 4,200,160\n"
"Maaa 64,208,160\nRa 136,10,2048\nMaaa 64,212,164\nD0 6,24,44,0\nMaaa 68,220,164\nRa 140,11,2048\nMaaa 68,216,160\nMaaa 76,216,168\nRa 144,10,8192\nMaaa 76,220,172\n"
"Maaa 72,212,172\nRa 148,11,8192\nMaaa 72,208,168\nMaaa 12,200,168\nRa 152,10,10240\nMaaa 12,204,172\nD0 7,24,44,1024\nMaaa 8,196,172\nRa 156,11,10240\nMaaa 8,192,168\n"
"Maaa 16,192,176\nMaaa 16,196,180\nMaaa 20,204,180\nMaaa 20,200,176\nMaaa 80,208,176\nMaaa 80,212,180\nD0 8,24,44,2048\nMaaa 84,220,180\nMaaa 84,216,176\nMaaa 92,216,184\n"
"Maaa 92,220,188\nMaaa 88,212,188\nMaaa 88,208,184\nMaaa 28,200,184\nMaaa 28,204,188\nD0 9,24,44,3072\nMaaa 24,196,188\nMaaa 24,192,184\ns_waitcnt vmcnt(18) lgkmcnt(0)\ns_barrier\n"
"Maaa 32,192,224\nTa 160,12,0\nMaaa 32,196,228\nTa 162,12,2048\nMaaa 36,204,228\ns_add_u32 m0, s47, -16384\ns_add_u32 s48, s48, 128\nMaaa 36,200,224\nTa 164,12,16384\n"
"Maaa 96,208,224\nTa 166,12,18432\nbuffer_load_dword v31, v24, s[24:27], s96 offen\ns_add_u32 s96, s96, 0x80000\nMaaa 96,212,228\nD0 2,20,48,0\nMaaa 100,220,228\nTa 168,13,0\n"
"Maaa 100,216,224\nTa 170,13,2048\nMaaa 108,216,232\nMaaa 108,220,236\nTa 172,13,16384\nMaaa 104,212,236\nTa 174,13,18432\nMaaa 104,208,232\nMaaa 44,200,232\nTa 176,14,0\n"
"Maaa 44,204,236\nTa 178,14,2048\nD0 3,20,48,1024\nMaaa 40,196,236\nMaaa 40,192,232\nTa 180,14,16384\nMaaa 48,192,240\nTa 182,14,18432\nMaaa 48,196,244\nMaaa 52,204,244\n"
"Ta 184,15,0\nMaaa 52,200,240\nTa 186,15,2048\nMaaa 112,208,240\nMaaa 112,212,244\nTa 188,15,16384\nD0 4,20,48,2048\nMaaa 116,220,244\nTa 190,15,18432\nMaaa 116,216,240\n"
"Maaa 124,216,248\nMaaa 124,220,252\nMaaa 120,212,252\nMaaa 120,208,248\nMaaa 60,200,248\nMaaa 60,204,252\nD0 5,20,48,3072\nMaaa 56,196,252\nMaaa 56,192,248\nWL 0\nMvaa 64,128,160\n"
"Ta 224,16,0\nWV 12\nv_xor_b32 v27, s15, v27\nv_and_b32 v32, 0xffffdfff, v27\nv_cmp_ne_u32 vcc, 0, v32\ns_nop 0\ns_cbranch_vccnz .S430\n.S431:\nv_cmp_ne_u32 vcc, 0, v27\ns_nop 3\n"
"s_bcnt1_i32_b64 s4, vcc\ns_cmp_gt_u32 s4, 32\ns_cselect_b32 s4, 1, 0\nMvaa 64,132,164\nTa 226,16,2048\nMvaa 68,140,164\ns_add_u32 m0, s47, -65536\ns_add_u32 s45, s45, 128\n"
"Mvaa 68,136,160\nTa 228,16,16384\nMvaa 128,144,160\nTa 230,16,18432\nMvaa 128,148,164\nD0 2,20,45,0\nMvaa 132,156,164\nTa 232,17,0\nMvaa 132,152,160\nTa 234,17,2048\n"
"Mvaa 140,152,168\ns_xor_b32 s4, s4, s12\nMvaa 140,156,172\nTa 236,17,16384\nMvaa 136,148,172\nTa 238,17,18432\nMvaa 136,144,168\ns_and_b32 s4, s4, 1\nMvaa 76,136,168\nTa 240,18,0\n"
"Mvaa 76,140,172\nTa 242,18,2048\nD0 3,20,45,1024\nMvaa 72,132,172\ns_lshl_b32 s4, s4, 13\nMvaa 72,128,168\nTa 244,18,16384\nMvaa 80,128,176\nTa 246,18,18432\nMvaa 80,132,180\n"
"s_mul_i32 s72, s73, 25165824\nMvaa 84,140,180\nTa 248,19,0\nMvaa 84,136,176\nTa 250,19,2048\nMvaa 144,144,176\ns_add_u32 s72, s72, s4\nMvaa 144,148,180\nTa 252,19,16384\n"
"D0 4,20,45,2048\nMvaa 148,156,180\nTa 254,19,18432\nMvaa 148,152,176\ns_and_b32 s4, s12, 1\nMvaa 156,152,184\ns_lshl_b32 s4, s4, 21\nMvaa 156,156,188\ns_add_u32 s72, s72, s4\n"
"Mvaa 152,148,188\ns_lshl3_add_u32 s2, s13, s11\nMvaa 152,144,184\ns_lshl_b32 s2, s2, 11\nMvaa 92,136,184\ns_lshl_b32 s3, s14, 22\nMvaa 92,140,188\nD0 5,20,45,3072\n"
"Mvaa 88,132,188\ns_add_u32 s2, s2, s3\nMvaa 88,128,184\nWL 0\ns_barrier\nMvaa 96,128,224\nRa 192,10,16384\nMvaa 96,132,228\ns_add_u32 s2, s2, s72\nMvaa 100,140,228\n"
"Ra 196,11,16384\ns_add_u32 m0, s47, 0\ns_add_u32 s44, s44, 0x40000\nMvaa 100,136,224\ns_add_u32 s49, s2, 0xc00000\nMvaa 160,144,224\nRa 200,10,18432\nMvaa 160,148,228\n"
"D0 6,24,44,0\nMvaa 164,156,228\nRa 204,11,18432\nMvaa 164,152,224\nMvaa 172,152,232\nRa 208,10,24576\nMvaa 172,156,236\nMvaa 168,148,236\nRa 212,11,24576\nMvaa 168,144,232\n"
"Mvaa 108,136,232\nRa 216,10,26624\nMvaa 108,140,236\nD0 7,24,44,1024\nMvaa 104,132,236\nRa 220,11,26624\nMvaa 104,128,232\nMvaa 112,128,240\nMvaa 112,132,244\nMvaa 116,140,244\n"
"Mvaa 116,136,240\nMvaa 176,144,240\nMvaa 176,148,244\nD0 8,24,44,2048\nMvaa 180,156,244\nMvaa 180,152,240\nMvaa 188,152,248\nMvaa 188,156,252\nMvaa 184,148,252\nMvaa 184,144,248\n"
"Mvaa 124,136,248\nMvaa 124,140,252\nD0 9,24,44,3072\nMvaa 120,132,252\nMvaa 120,128,248\nWL 0\nMaaa 32,192,224\nRa 128,10,32768\ns_mov_b32 s4, s13\nMaaa 32,196,228\n"
"v_lshrrev_b32 v32, 2, v0\nMaaa 36,204,228\nRa 132,11,32768\ns_add_u32 m0, s47, 16384\ns_add_u32 s44, s44, 0x40000\nMaaa 36,200,224\nv_mul_u32_u24 v32, 896, v32\nMaaa 96,208,224\n"
"Ra 136,10,34816\nv_and_b32 v33, 3, v0\nMaaa 96,212,228\nD0 6,24,44,0\nMaaa 100,220,228\nRa 140,11,34816\nv_lshl_add_u32 v32, v33, 4, v32\nMaaa 100,216,224\ns_lshr_b32 s2, s11, 1\n"
"Maaa 108,216,232\nRa 144,10,40960\ns_mul_i32 s3, s2, 448\nMaaa 108,220,236\ns_and_b32 s5, s11, 1\nMaaa 104,212,236\nRa 148,11,40960\ns_lshl3_add_u32 s3, s5, s3\nMaaa 104,208,232\n"
"s_add_u32 s3, s3, 131072\nMaaa 44,200,232\nRa 152,10,43008\nv_add_u32 v32, s3, v32\nMaaa 44,204,236\nD0 7,24,44,1024\nMaaa 40,196,236\nRa 156,11,43008\nTv 36,32,0\n"
"Maaa 40,192,232\nTv 38,32,14336\nMaaa 48,192,240\nTv 40,32,64\nTv 42,32,14400\nMaaa 48,196,244\nTv 44,32,128\nMaaa 52,204,244\nTv 46,32,14464\nTv 48,32,192\nMaaa 52,200,240\n"
"Tv 50,32,14528\nMaaa 112,208,240\nTv 52,32,256\nTv 54,32,14592\nMaaa 112,212,244\nD0 8,24,44,2048\nMaaa 116,220,244\nTv 56,32,320\nTv 58,32,14656\nMaaa 116,216,240\nTv 60,32,384\n"
"Maaa 124,216,248\nTv 62,32,14720\ns_lshr_b32 s0, s4, 1\nMaaa 124,220,252\ns_mul_i32 s0, s0, 16\nMaaa 120,212,252\ns_lshl1_add_u32 s0, s5, s0\ns_and_b32 s6, s4, 1\n"
"Maaa 120,208,248\ns_add_u32 s0, s0, s6\nMaaa 60,200,248\ns_lshl3_add_u32 s1, s5, s4\ns_add_u32 s77, s1, 64\nMaaa 60,204,252\nD0 9,24,44,3072\nMaaa 56,196,252\n"
"s_and_b32 s4, s12, 1\ns_mov_b32 s7, 0x6543210\nMaaa 56,192,248\ns_waitcnt vmcnt(17) lgkmcnt(14)\ns_barrier\nMaaa 0,192,160\nTa 224,16,32768\ns_cmp_lg_u32 s4, s2\n"
"s_cselect_b32 s6, s0, s1\ns_cselect_b32 s5, 4, 16\ns_cselect_b32 s7, 0x6352410, s7\ns_cselect_b32 s3, 0, 229376\nMaaa 0,196,164\nTa 226,16,34816\nMaaa 4,204,164\n"
"s_add_u32 m0, s47, -49152\ns_add_u32 s48, s48, 128\nMaaa 4,200,160\nTa 228,16,49152\nMaaa 64,208,160\nTa 230,16,51200\ns_add_u32 s3, s3, s72\nMaaa 64,212,164\nD0 2,20,48,0\n"
"Maaa 68,220,164\nTa 232,17,32768\ns_add_u32 s3, s3, 0x1400000\nMaaa 68,216,160\nTa 234,17,34816\nMaaa 76,216,168\nWL 14\ns_lshr_b32 s0, s6, 4\nMaaa 76,220,172\nTa 236,17,49152\n"
"Maaa 72,212,172\nTa 238,17,51200\ns_lshl_b32 s0, s0, 2\nMaaa 72,208,168\ns_lshr_b32 s0, s7, s0\nMaaa 12,200,168\nTa 240,18,32768\ns_and_b32 s0, s0, 7\nMaaa 12,204,172\n"
"Ta 242,18,34816\nD0 3,20,48,1024\nMaaa 8,196,172\ns_lshl_b32 s0, s0, 5\ns_and_b32 s1, s6, 15\nMaaa 8,192,168\nTa 244,18,49152\nMaaa 16,192,176\nTa 246,18,51200\n"
"s_add_u32 s0, s0, s1\nMaaa 16,196,180\ns_and_b32 s1, s6, 8\nMaaa 20,204,180\nTa 248,19,32768\ns_add_u32 s0, s0, s1\nMaaa 20,200,176\nTa 250,19,34816\nMaaa 80,208,176\n"
"s_lshl_b32 s0, s0, 10\ns_add_u32 s0, s0, s3\nMaaa 80,212,180\nTa 252,19,49152\nD0 4,20,48,2048\nMaaa 84,220,180\nTa 254,19,51200\nSv 36,1,16,0,0\nMaaa 84,216,176\n"
"s_add_u32 s6, s6, s5\nMaaa 92,216,184\nMaaa 92,220,188\nMaaa 88,212,188\nMaaa 88,208,184\nMaaa 28,200,184\nMaaa 28,204,188\nD0 5,20,48,3072\nMaaa 24,196,188\nMaaa 24,192,184\n"
"WL 0\nMvaa 96,128,224\nTa 160,12,32768\ns_lshr_b32 s0, s6, 4\nMvaa 96,132,228\nTa 162,12,34816\nMvaa 100,140,228\ns_add_u32 m0, s47, -32768\ns_add_u32 s45, s45, 128\n"
"Mvaa 100,136,224\nTa 164,12,49152\nMvaa 160,144,224\nTa 166,12,51200\ns_lshl_b32 s0, s0, 2\nMvaa 160,148,228\nD0 2,20,45,0\nMvaa 164,156,228\nTa 168,13,32768\n"
"s_lshr_b32 s0, s7, s0\nMvaa 164,152,224\nTa 170,13,34816\nMvaa 172,152,232\ns_and_b32 s0, s0, 7\ns_lshl_b32 s0, s0, 5\nMvaa 172,156,236\nTa 172,13,49152\nMvaa 168,148,236\n"
"Ta 174,13,51200\ns_and_b32 s1, s6, 15\nMvaa 168,144,232\ns_add_u32 s0, s0, s1\nMvaa 108,136,232\nTa 176,14,32768\ns_and_b32 s1, s6, 8\nMvaa 108,140,236\nTa 178,14,34816\n"
"D0 3,20,45,1024\nMvaa 104,132,236\ns_add_u32 s0, s0, s1\ns_lshl_b32 s0, s0, 10\nMvaa 104,128,232\nTa 180,14,49152\nMvaa 112,128,240\nTa 182,14,51200\ns_add_u32 s0, s0, s3\n"
"Mvaa 112,132,244\nSv 40,1,16,0,0\nMvaa 116,140,244\nTa 184,15,32768\ns_add_u32 s6, s6, s5\nMvaa 116,136,240\nTa 186,15,34816\nMvaa 176,144,240\nMvaa 176,148,244\nTa 188,15,49152\n"
"D0 4,20,45,2048\nMvaa 180,156,244\nTa 190,15,51200\nMvaa 180,152,240\nMvaa 188,152,248\nMvaa 188,156,252\nMvaa 184,148,252\ns_lshr_b32 s0, s6, 4\ns_lshl_b32 s0, s0, 2\n"
"Mvaa 184,144,248\ns_lshr_b32 s0, s7, s0\nMvaa 124,136,248\ns_and_b32 s0, s0, 7\ns_lshl_b32 s0, s0, 5\nMvaa 124,140,252\nD0 5,20,45,3072\nMvaa 120,132,252\ns_and_b32 s1, s6, 15\n"
"s_add_u32 s0, s0, s1\nMvaa 120,128,248\ns_waitcnt vmcnt(18) lgkmcnt(0)\ns_barrier\nMvaa 64,128,160\nRa 192,10,49152\ns_and_b32 s1, s6, 8\nMvaa 64,132,164\ns_add_u32 s0, s0, s1\n"
"Mvaa 68,140,164\nRa 196,11,49152\ns_add_u32 m0, s47, 32768\ns_add_u32 s44, s44, 0x40000\nMvaa 68,136,160\ns_lshl_b32 s0, s0, 10\nMvaa 128,144,160\nRa 200,10,51200\n"
"s_add_u32 s0, s0, s3\nMvaa 128,148,164\nD0 6,24,44,0\nMvaa 132,156,164\nRa 204,11,51200\nSv 44,1,16,0,0\nMvaa 132,152,160\ns_add_u32 s6, s6, s5\nMvaa 140,152,168\nRa 208,10,57344\n"
"Mvaa 140,156,172\nMvaa 136,148,172\nRa 212,11,57344\nMvaa 136,144,168\nMvaa 76,136,168\nRa 216,10,59392\nMvaa 76,140,172\nD0 7,24,44,1024\nMvaa 72,132,172\nRa 220,11,59392\n"
"Mvaa 72,128,168\ns_lshr_b32 s0, s6, 4\nMvaa 80,128,176\ns_lshl_b32 s0, s0, 2\ns_lshr_b32 s0, s7, s0\nMvaa 80,132,180\ns_and_b32 s0, s0, 7\nMvaa 84,140,180\ns_lshl_b32 s0, s0, 5\n"
"s_and_b32 s1, s6, 15\nMvaa 84,136,176\ns_add_u32 s0, s0, s1\nMvaa 144,144,176\ns_and_b32 s1, s6, 8\ns_add_u32 s0, s0, s1\nMvaa 144,148,180\nD0 8,24,44,2048\nMvaa 148,156,180\n"
"s_lshl_b32 s0, s0, 10\ns_add_u32 s0, s0, s3\nMvaa 148,152,176\nSv 48,1,16,0,0\nMvaa 156,152,184\ns_add_u32 s6, s6, s5\nMvaa 156,156,188\nMvaa 152,148,188\nMvaa 152,144,184\n"
"Mvaa 92,136,184\nMvaa 92,140,188\nD0 9,24,44,3072\nMvaa 88,132,188\nMvaa 88,128,184\nWL 0\nMaaa 0,192,160\nRa 128,10,0\ns_mov_b32 s6, s77\nMaaa 0,196,164\ns_mov_b32 s5, 16\n"
"Maaa 4,204,164\nRa 132,11,0\ns_add_u32 m0, s47, 49152\ns_add_u32 s44, s44, 0x40000\nMaaa 4,200,160\ns_lshr_b32 s0, s6, 4\nMaaa 64,208,160\nRa 136,10,2048\ns_lshl_b32 s0, s0, 2\n"
"Maaa 64,212,164\nD0 6,24,44,0\nMaaa 68,220,164\nRa 140,11,2048\ns_lshr_b32 s0, s7, s0\nMaaa 68,216,160\ns_and_b32 s0, s0, 7\nMaaa 76,216,168\nRa 144,10,8192\ns_lshl_b32 s0, s0, 5\n"
"Maaa 76,220,172\ns_and_b32 s1, s6, 15\nMaaa 72,212,172\nRa 148,11,8192\ns_add_u32 s0, s0, s1\nMaaa 72,208,168\ns_and_b32 s1, s6, 8\nMaaa 12,200,168\nRa 152,10,10240\n"
"s_add_u32 s0, s0, s1\nMaaa 12,204,172\nD0 7,24,44,1024\nMaaa 8,196,172\nRa 156,11,10240\ns_lshl_b32 s0, s0, 10\nMaaa 8,192,168\ns_add_u32 s0, s0, s3\nMaaa 16,192,176\n"
"Sv 52,1,16,0,0\ns_add_u32 s6, s6, s5\nMaaa 16,196,180\nMaaa 20,204,180\nMaaa 20,200,176\nMaaa 80,208,176\nMaaa 80,212,180\nD0 8,24,44,2048\nMaaa 84,220,180\nMaaa 84,216,176\n"
"Maaa 92,216,184\ns_lshr_b32 s0, s6, 4\ns_lshl_b32 s0, s0, 2\nMaaa 92,220,188\ns_lshr_b32 s0, s7, s0\nMaaa 88,212,188\ns_and_b32 s0, s0, 7\ns_lshl_b32 s0, s0, 5\nMaaa 88,208,184\n"
"s_and_b32 s1, s6, 15\nMaaa 28,200,184\ns_add_u32 s0, s0, s1\ns_and_b32 s1, s6, 8\nMaaa 28,204,188\nD0 9,24,44,3072\nMaaa 24,196,188\ns_add_u32 s0, s0, s1\ns_lshl_b32 s0, s0, 10\n"
"Maaa 24,192,184\nWV 21\ns_barrier\nMaaa 32,192,224\nTa 160,12,0\ns_add_u32 s0, s0, s3\nMaaa 32,196,228\nTa 162,12,2048\nMaaa 36,204,228\ns_add_u32 m0, s47, -16384\n"
"s_add_u32 s48, s48, 128\nMaaa 36,200,224\nTa 164,12,16384\nMaaa 96,208,224\nTa 166,12,18432\nSv 56,1,16,0,0\nMaaa 96,212,228\nD0 2,20,48,0\nMaaa 100,220,228\nTa 168,13,0\n"
"s_add_u32 s6, s6, s5\nMaaa 100,216,224\nTa 170,13,2048\nMaaa 108,216,232\nMaaa 108,220,236\nTa 172,13,16384\nMaaa 104,212,236\nTa 174,13,18432\nMaaa 104,208,232\nMaaa 44,200,232\n"
"Ta 176,14,0\nMaaa 44,204,236\nTa 178,14,2048\nD0 3,20,48,1024\nMaaa 40,196,236\ns_lshr_b32 s0, s6, 4\ns_lshl_b32 s0, s0, 2\nMaaa 40,192,232\nTa 180,14,16384\nMaaa 48,192,240\n"
"Ta 182,14,18432\ns_lshr_b32 s0, s7, s0\nMaaa 48,196,244\ns_and_b32 s0, s0, 7\nMaaa 52,204,244\nTa 184,15,0\ns_lshl_b32 s0, s0, 5\nMaaa 52,200,240\nTa 186,15,2048\n"
"Maaa 112,208,240\ns_and_b32 s1, s6, 15\ns_add_u32 s0, s0, s1\nMaaa 112,212,244\nTa 188,15,16384\nD0 4,20,48,2048\nMaaa 116,220,244\nTa 190,15,18432\ns_and_b32 s1, s6, 8\n"
"Maaa 116,216,240\ns_add_u32 s0, s0, s1\nMaaa 124,216,248\ns_lshl_b32 s0, s0, 10\ns_add_u32 s0, s0, s3\nMaaa 124,220,252\nSv 60,1,16,0,0\nMaaa 120,212,252\ns_add_u32 s6, s6, s5\n"
"Maaa 120,208,248\nMaaa 60,200,248\nMaaa 60,204,252\nD0 5,20,48,3072\nMaaa 56,196,252\nMaaa 56,192,248\nWL 0\nMvaa 64,128,160\nTa 224,16,0\nMvaa 64,132,164\nTa 226,16,2048\n"
"s_add_u32 m0, s47, 65536\ns_add_u32 s46, s51, 0\nMvaa 68,140,164\nv_lshrrev_b32 v48, 4, v0\nMvaa 68,136,160\nTa 228,16,16384\nD0 6,32,46,0\nMvaa 128,144,160\nTa 230,16,18432\n"
"Mvaa 128,148,164\nv_and_b32 v49, 7, v0\nMvaa 132,156,164\nTa 232,17,0\nMvaa 132,152,160\nTa 234,17,2048\nMvaa 140,152,168\nv_xor_b32 v49, v49, v48\nD0 7,32,46,1024\n"
"Mvaa 140,156,172\nTa 236,17,16384\nMvaa 136,148,172\nTa 238,17,18432\nMvaa 136,144,168\nv_lshlrev_b32 v49, 4, v49\nMvaa 76,136,168\nTa 240,18,0\nMvaa 76,140,172\nTa 242,18,2048\n"
"D0 8,32,46,2048\nMvaa 72,132,172\nv_lshrrev_b32 v48, 3, v0\nMvaa 72,128,168\nTa 244,18,16384\nMvaa 80,128,176\nTa 246,18,18432\nMvaa 80,132,180\nv_mul_u32_u24 v48, 896, v48\n"
"Mvaa 84,140,180\nTa 248,19,0\nD0 9,32,46,3072\nMvaa 84,136,176\nTa 250,19,2048\nMvaa 144,144,176\nv_add_u32 v40, v48, v49\nMvaa 144,148,180\nTa 252,19,16384\n"
"s_add_u32 m0, s47, 81920\ns_add_u32 s46, s51, 262144\nMvaa 148,156,180\nTa 254,19,18432\nMvaa 148,152,176\nv_xor_b32 v49, 64, v40\nD0 6,32,46,0\nMvaa 156,152,184\n"
"v_add_u32 v41, 6144, v49\nMvaa 156,156,188\nv_add_u32 v42, 55296, v40\nMvaa 152,148,188\nv_add_u32 v43, 61440, v49\nMvaa 152,144,184\ns_mov_b32 s2, s13\nMvaa 92,136,184\n"
"s_lshl3_add_u32 s2, s2, s11\nD0 7,32,46,1024\nMvaa 92,140,188\ns_mul_i32 s52, s2, 14336\nMvaa 88,132,188\ns_lshr_b32 s2, s13, 2\ns_bfe_u32 s3, s13, 0x10001\nMvaa 88,128,184\n"
"s_waitcnt vmcnt(23) lgkmcnt(0)\ns_barrier\nMvaa 96,128,224\nRa 192,10,16384\ns_mul_i32 s3, s3, 17\nMvaa 96,132,228\ns_add_u32 s2, s2, s3\nD0 8,32,46,2048\nMvaa 100,140,228\n"
"Ra 196,11,16384\ns_lshl2_add_u32 s3, s14, 0\nMvaa 100,136,224\ns_lshl_b32 s3, s3, 2\nMvaa 160,144,224\nRa 200,10,18432\ns_lshr_b32 s3, 0xe420ca86, s3\nMvaa 160,148,228\n"
"s_and_b32 s3, s3, 15\nMvaa 164,156,228\nRa 204,11,18432\ns_add_u32 s2, s2, s3\nD0 9,32,46,3072\nMvaa 164,152,224\ns_cmp_eq_u32 s2, 32\ns_cselect_b32 s4, 16, s2\nMvaa 172,152,232\n"
"Ra 208,10,24576\ns_mul_i32 s75, s4, 65536\nMvaa 172,156,236\ns_lshr_b32 s2, s13, 2\ns_add_u32 m0, s47, -65536\ns_add_u32 s46, s52, 0\nMvaa 168,148,236\nRa 212,11,24576\n"
"s_bfe_u32 s3, s13, 0x10001\nMvaa 168,144,232\ns_mul_i32 s3, s3, 17\nD0 40,28,46,0\nMvaa 108,136,232\nRa 216,10,26624\ns_add_u32 s2, s2, s3\nMvaa 108,140,236\n"
"s_lshl2_add_u32 s3, s14, 1\nMvaa 104,132,236\nRa 220,11,26624\ns_lshl_b32 s3, s3, 2\nMvaa 104,128,232\ns_lshr_b32 s3, 0xe420ca86, s3\nMvaa 112,128,240\ns_and_b32 s3, s3, 15\n"
"s_add_u32 s2, s2, s3\nD0 41,28,46,1024\nMvaa 112,132,244\ns_cmp_eq_u32 s2, 32\ns_cselect_b32 s4, 16, s2\nMvaa 116,140,244\ns_mul_i32 s76, s4, 65536\ns_lshr_b32 s2, s13, 2\n"
"Mvaa 116,136,240\ns_bfe_u32 s3, s13, 0x10001\nMvaa 176,144,240\ns_mul_i32 s3, s3, 17\ns_add_u32 s2, s2, s3\nMvaa 176,148,244\ns_lshl2_add_u32 s3, s14, 2\nD0 42,28,46,2048\n"
"Mvaa 180,156,244\ns_lshl_b32 s3, s3, 2\ns_lshr_b32 s3, 0xe420ca86, s3\nMvaa 180,152,240\ns_and_b32 s3, s3, 15\nMvaa 188,152,248\ns_add_u32 s2, s2, s3\nMvaa 188,156,252\n"
"s_cmp_eq_u32 s2, 32\ns_cselect_b32 s4, 16, s2\nMvaa 184,148,252\ns_mul_i32 s77, s4, 65536\ns_lshr_b32 s2, s13, 2\nD0 43,28,46,3072\nMvaa 184,144,248\ns_bfe_u32 s3, s13, 0x10001\n"
"Mvaa 124,136,248\ns_mul_i32 s3, s3, 17\ns_add_u32 s2, s2, s3\nMvaa 124,140,252\ns_lshl2_add_u32 s3, s14, 3\nMvaa 120,132,252\ns_lshl_b32 s3, s3, 2\ns_lshr_b32 s3, 0xe420ca86, s3\n"
"Mvaa 120,128,248\nWL 0\nMaaa 32,192,224\nRa 128,10,32768\ns_and_b32 s3, s3, 15\ns_add_u32 m0, s47, 0\ns_add_u32 s46, s51, 524288\nD0 6,32,46,0\nMaaa 32,196,228\n"
"s_add_u32 s2, s2, s3\ns_cmp_eq_u32 s2, 32\ns_cselect_b32 s4, 16, s2\nMaaa 36,204,228\nRa 132,11,32768\ns_mul_i32 s78, s4, 65536\nMaaa 36,200,224\ns_lshr_b32 s3, s11, 1\n"
"Maaa 96,208,224\nRa 136,10,34816\ns_lshl_b32 s3, s3, 15\nMaaa 96,212,228\ns_add_u32 s2, s72, s3\nD0 7,32,46,1024\nMaaa 100,220,228\nRa 140,11,34816\ns_and_b32 s3, s11, 1\n"
"Maaa 100,216,224\ns_lshl_b32 s3, s3, 14\nMaaa 108,216,232\nRa 144,10,40960\ns_add_u32 s2, s2, s3\nMaaa 108,220,236\ns_add_u32 s2, s2, 0x400000\nMaaa 104,212,236\nRa 148,11,40960\n"
"s_mov_b32 s55, 4096\nD0 8,32,46,2048\nMaaa 104,208,232\ns_add_u32 s75, s75, s2\nMaaa 44,200,232\nRa 152,10,43008\ns_add_u32 s76, s76, s2\nMaaa 44,204,236\ns_add_u32 s77, s77, s2\n"
"Maaa 40,196,236\nRa 156,11,43008\ns_add_u32 s78, s78, s2\nMaaa 40,192,232\nv_add_u32 v32, 0x10000, v12\nD0 9,32,46,3072\nMaaa 48,192,240\nv_add_u32 v33, 0x10000, v13\n"
"v_add_u32 v34, 0x10000, v14\nMaaa 48,196,244\nv_add_u32 v35, 0x10000, v15\nMaaa 52,204,244\nv_add_u32 v36, 0x10000, v16\nv_add_u32 v37, 0x10000, v17\ns_add_u32 m0, s47, 16384\n"
"s_add_u32 s46, s51, 786432\nMaaa 52,200,240\nv_add_u32 v38, 0x10000, v18\nMaaa 112,208,240\nv_add_u32 v39, 0x10000, v19\ns_add_u32 s2, s11, 1\nD0 6,32,46,0\nMaaa 112,212,244\n"
"s_lshr_b32 s2, s2, 1\nMaaa 116,220,244\ns_lshl_b32 s2, s2, 12\nv_add_u32 v44, s2, v10\nMaaa 116,216,240\nv_add_u32 v45, s2, v11\nMaaa 124,216,248\nv_xor_b32 v46, 0x1000, v44\n"
"v_xor_b32 v47, 0x1000, v45\nMaaa 124,220,252\ns_lshr_b32 s3, s11, 1\nD0 7,32,46,1024\nMaaa 120,212,252\ns_lshl_b32 s3, s3, 13\ns_lshl_b32 s2, s11, 12\nMaaa 120,208,248\n"
"s_add_u32 s2, s2, s3\nMaaa 60,200,248\ns_add_u32 s2, s2, s72\ns_lshl_b32 s3, s11, 12\nMaaa 60,204,252\ns_add_u32 s89, s3, 0x10000\nMaaa 56,196,252\ns_mov_b32 s88, 0xffff8000\n"
"s_lshl_b32 s4, s13, 1\nD0 8,32,46,2048\nMaaa 56,192,248\ns_waitcnt vmcnt(25) lgkmcnt(0)\ns_barrier\nMaaa 0,192,160\nTa 224,16,32768\ns_sub_u32 s79, 15, s4\nMaaa 0,196,164\n"
"Ta 226,16,34816\nMaaa 4,204,164\ns_add_u32 s96, s2, 0x1310000\ns_mov_b32 s63, 0x53800000\nMaaa 4,200,160\nTa 228,16,49152\nD0 9,32,46,3072\nMaaa 64,208,160\nTa 230,16,51200\n"
"s_mov_b32 s64, 0x2b800000\nMaaa 64,212,164\ns_sub_u32 s98, s4, 1\nMaaa 68,220,164\nTa 232,17,32768\ns_lshl_b32 s5, s13, 5\ns_add_u32 m0, s47, -32768\ns_add_u32 s46, s52, 128\n"
"Maaa 68,216,160\nTa 234,17,34816\nMaaa 76,216,168\ns_add_u32 s5, s5, s11\ns_lshl_b32 s65, s5, 9\nD0 40,28,46,0\nMaaa 76,220,172\nTa 236,17,49152\nMaaa 72,212,172\nTa 238,17,51200\n"
"s_add_u32 s65, s65, s72\nMaaa 72,208,168\ns_add_u32 s65, s65, 0x1580000\nMaaa 12,200,168\nTa 240,18,32768\ns_and_b32 s66, s72, 0x2000\nMaaa 12,204,172\nTa 242,18,34816\n"
"D0 41,28,46,1024\nMaaa 8,196,172\ns_add_u32 s66, s66, s56\ns_add_u32 s66, s66, 0x100000\nMaaa 8,192,168\nTa 244,18,49152\nMaaa 16,192,176\nTa 246,18,51200\ns_and_b32 s5, s12, 1\n"
"Maaa 16,196,180\ns_mul_i32 s58, s5, 0x202000\nMaaa 20,204,180\nTa 248,19,32768\ns_xor_b32 s58, s58, s72\nD0 42,28,46,2048\nMaaa 20,200,176\nTa 250,19,34816\nMaaa 80,208,176\n"
"s_add_u32 s58, s58, 0x15c0000\ns_lshl_b32 s5, s5, 8\nMaaa 80,212,180\nTa 252,19,49152\nMaaa 84,220,180\nTa 254,19,51200\ns_add_u32 s61, s58, s5\nMaaa 84,216,176\n"
"s_lshl4_add_u32 s60, s13, s61\nD0 43,28,46,3072\nMaaa 92,216,184\ns_add_u32 s54, s60, 128\ns_xor_b32 s60, s54, 0x100\nMaaa 92,220,188\ns_cmp_eq_u32 s11, 0\n"
"s_cselect_b32 s54, s54, 0x40000000\nMaaa 88,212,188\ns_getpc_b64 s[8:9]\n.PC447:\ns_add_u32 s8, s8, .HD429-.PC447\ns_addc_u32 s9, s9, 0\ns_add_u32 m0, s47, -49152\n"
"s_add_u32 s46, s52, 256\nMaaa 88,208,184\ns_mov_b32 s97, 0xff800000\nMaaa 28,200,184\ns_mov_b32 s90, 1\nD0 40,28,46,0\nMaaa 28,204,188\ns_cmp_lt_u32 s11, 2\n"
"s_cselect_b32 s92, 2, 0\nMaaa 24,196,188\ns_mul_i32 s4, s79, 65536\ns_sub_u32 s4, s2, s4\nMaaa 24,192,184\nWL 0\nMvaa 96,128,224\nTa 160,12,32768\ns_add_u32 s94, s4, 0x20000\n"
"Mvaa 96,132,228\nTa 162,12,34816\nD0 41,28,46,1024\nMvaa 100,140,228\ns_add_u32 s95, s4, 0x410000\ns_add_u32 s86, s2, 0x130000\nMvaa 100,136,224\nTa 164,12,49152\n"
"Mvaa 108,136,232\nTa 166,12,51200\ns_add_u32 s87, s2, 0x520000\nMvaa 108,140,236\ns_sub_u32 s99, s79, 2\ns_cmp_eq_u32 s13, 7\ns_cselect_b32 s93, 1.0, 0\nMvaa 104,132,236\n"
"Ta 168,13,32768\ns_mov_b32 s36, s68\nD0 42,28,46,2048\nMvaa 104,128,232\nTa 170,13,34816\nMvaa 112,128,240\ns_and_b32 s37, s69, 0xffff\ns_mov_b32 s38, 0x1c0000\nMvaa 112,132,244\n"
"Ta 172,13,49152\nCV 208,96,97\nMvaa 116,140,244\nTa 174,13,51200\ns_mov_b32 s39, 0x20000\nMvaa 116,136,240\ns_and_b32 s2, s74, 15\nD0 43,28,46,3072\nMvaa 124,136,248\n"
"Ta 176,14,32768\ns_max_u32 s2, s2, 2\nMvaa 124,140,252\nTa 178,14,34816\nMvaa 120,132,252\ns_sub_u32 s2, s2, 2\ns_mul_i32 s3, s73, 14\nMvaa 120,128,248\nTa 180,14,49152\n"
"CV 209,98,99\nMvaa 160,144,224\nTa 182,14,51200\ns_add_u32 s2, s2, s3\nMvaa 160,148,228\ns_lshl2_add_u32 s2, s2, s11\nMvaa 164,156,228\nTa 184,15,32768\ns_mul_i32 s3, s2, 4682\n"
"Mvaa 164,152,224\nTa 186,15,34816\nCV 210,100,101\nMvaa 172,152,232\ns_lshr_b32 s3, s3, 16\ns_mul_i32 s22, s3, 14\nMvaa 172,156,236\nTa 188,15,49152\nCV 211,102,103\n"
"Mvaa 168,148,236\nTa 190,15,51200\ns_sub_u32 s22, s2, s22\nMvaa 168,144,232\ns_and_b32 s21, s3, 28\nMvaa 176,144,240\ns_add_u32 s21, s21, s3\ns_lshl_b32 s21, s21, 4\n"
"Mvaa 176,148,244\ns_bfe_u32 s41, s3, 0x30002\ns_cmp_gt_u32 s22, 6\ns_cselect_b32 s2, 1179648, 0\ns_cselect_b32 s1, 16, 0\nMvaa 180,156,244\ns_add_u32 s41, s41, s1\n"
"s_lshl4_add_u32 s41, s41, 0xc048480\nMvaa 180,152,240\ns_and_b32 s1, s3, 28\nCV 212,104,105\nMvaa 188,152,248\ns_add_u32 s3, s3, s1\ns_lshl_b32 s3, s3, 11\nMvaa 188,156,252\n"
"s_add_u32 s2, s2, s3\nMvaa 184,148,252\ns_lshl_b32 s3, s22, 17\ns_add_u32 s2, s2, s3\nMvaa 184,144,248\ns_waitcnt vmcnt(16) lgkmcnt(0)\ns_barrier\nMvaa 64,128,160\n"
"Ra 192,10,49152\ns_add_u32 s20, s2, 0x10e0000\ns_add_u32 m0, s47, 32768\ns_add_u32 s46, s51, 1048576\nMvaa 64,132,164\ns_lshl_b32 s2, s22, 7\nMvaa 68,140,164\nRa 196,11,49152\n"
"s_add_u32 s40, s2, 0xc04b800\nD0 6,32,46,0\nMvaa 68,136,160\ns_mul_i32 s2, s21, 896\nCV 213,106,107\nMvaa 76,136,168\nRa 200,10,51200\ns_lshl_b32 s3, s22, 6\nMvaa 76,140,172\n"
"s_add_u32 s22, s2, s3\nCV 214,108,109\nMvaa 72,132,172\nRa 204,11,51200\ns_add_u32 s23, s22, 57344\nMvaa 72,128,168\nD0 7,32,46,1024\nMvaa 80,128,176\nRa 208,10,57344\n"
"CV 215,110,111\nMvaa 80,132,180\nCV 216,112,113\nCV 217,114,115\nMvaa 84,140,180\nRa 212,11,57344\nMvaa 84,136,176\nCV 218,116,117\nCV 219,118,119\nMvaa 92,136,184\n"
"Ra 216,10,59392\nD0 8,32,46,2048\ns_lshl3_add_u32 s5, s12, s13\ns_lshl2_add_u32 s5, s5, s11\ns_lshl2_add_u32 s5, s5, 0xc04a400\nbuffer_load_dword v27, off, s[16:19], s5 sc0 sc1\n"
"Mvaa 92,140,188\nCV 220,120,121\nMvaa 88,132,188\nRa 220,11,59392\nCV 221,122,123\nMvaa 88,128,184\nCV 222,124,125\nCV 223,126,127\nMvaa 128,144,160\nCV 192,64,65\nCV 193,66,67\n"
"Mvaa 128,148,164\nD0 9,32,46,3072\nMvaa 132,156,164\nCV 194,68,69\nCV 195,70,71\nMvaa 132,152,160\nCV 196,72,73\nCV 197,74,75\nMvaa 140,152,168\ns_add_u32 m0, s47, 49152\n"
"s_add_u32 s46, s51, 1310720\nMvaa 140,156,172\nCV 198,76,77\nCV 199,78,79\nMvaa 136,148,172\nD0 6,32,46,0\nCV 200,80,81\nMvaa 136,144,168\nCV 201,82,83\nMvaa 144,144,176\n"
"CV 202,84,85\nCV 203,86,87\nMvaa 144,148,180\nCV 204,88,89\nCV 205,90,91\nMvaa 148,156,180\nCV 206,92,93\nMvaa 148,152,176\nD0 7,32,46,1024\nCV 207,94,95\nMvaa 156,152,184\n"
"CV 240,160,161\nMvaa 156,156,188\nCV 241,162,163\nSv 192,1,16,75,0\nMvaa 152,148,188\nCV 242,164,165\nMvaa 152,144,184\nWL 0\nMaaa 0,192,160\nD0 8,32,46,2048\nRa 128,44,0\n"
"Maaa 0,196,164\nCV 243,166,167\nMaaa 4,204,164\nRa 132,45,0\nMaaa 4,200,160\nCV 244,168,169\nMaaa 12,200,168\nRa 136,44,2048\nMaaa 12,204,172\nD0 9,32,46,3072\nMaaa 8,196,172\n"
"Ra 140,45,2048\nSv 196,1,16,75,1024\nMaaa 8,192,168\nCV 245,170,171\nMaaa 16,192,176\nRa 144,46,0\nMaaa 16,196,180\nCV 246,172,173\nMaaa 20,204,180\nRa 148,47,0\nMaaa 20,200,176\n"
"CV 247,174,175\nMaaa 28,200,184\nRa 152,46,2048\nMaaa 28,204,188\nCV 248,176,177\nMaaa 24,196,188\nRa 156,47,2048\nMaaa 24,192,184\nCV 249,178,179\nSv 200,1,16,75,2048\n"
"Maaa 64,208,160\nCV 250,180,181\nCV 251,182,183\nMaaa 64,212,164\nCV 252,184,185\nMaaa 68,220,164\nCV 253,186,187\nMaaa 68,216,160\nCV 254,188,189\nMaaa 76,216,168\n"
"CV 255,190,191\nMaaa 76,220,172\nCV 224,128,129\nMaaa 72,212,172\nCV 225,130,131\nMaaa 72,208,168\nCV 226,132,133\nMaaa 80,208,176\nCV 227,134,135\nSv 204,1,16,75,3072\n"
"Maaa 80,212,180\nCV 228,136,137\nMaaa 84,220,180\nCV 229,138,139\nMaaa 84,216,176\nCV 230,140,141\nMaaa 92,216,184\nCV 231,142,143\nMaaa 92,220,188\nCV 232,144,145\n"
"Maaa 88,212,188\nCV 233,146,147\nMaaa 88,208,184\ns_barrier\nMaaa 32,192,224\ns_add_u32 m0, s47, -16384\ns_add_u32 s46, s52, 384\nTa 160,32,0\nMaaa 32,196,228\nTa 162,32,2048\n"
"Maaa 36,204,228\nD0 40,28,46,0\ns_add_u32 s75, s75, s55\nSv 208,1,16,75,0\nMaaa 36,200,224\nTa 164,32,16384\nMaaa 44,200,232\nTa 166,32,18432\nMaaa 44,204,236\nCV 234,148,149\n"
"Maaa 40,196,236\nTa 168,33,0\nMaaa 40,192,232\nD0 41,28,46,1024\nTa 170,33,2048\nMaaa 48,192,240\nCV 235,150,151\nMaaa 48,196,244\nTa 172,33,16384\nMaaa 52,204,244\n"
"Ta 174,33,18432\nMaaa 52,200,240\nCV 236,152,153\nSv 212,1,16,75,1024\nMaaa 60,200,248\nD0 42,28,46,2048\nTa 176,34,0\nMaaa 60,204,252\nTa 178,34,2048\nMaaa 56,196,252\n"
"CV 237,154,155\nMaaa 56,192,248\nTa 180,34,16384\nMaaa 96,208,224\nTa 182,34,18432\nCV 238,156,157\nMaaa 96,212,228\nD0 43,28,46,3072\nMaaa 100,220,228\nTa 184,35,0\n"
"Maaa 100,216,224\nTa 186,35,2048\nMaaa 108,216,232\nCV 239,158,159\nSv 216,1,16,75,2048\nMaaa 108,220,236\nTa 188,35,16384\nMaaa 104,212,236\nTa 190,35,18432\nMaaa 104,208,232\n"
"Maaa 112,208,240\nMaaa 112,212,244\nMaaa 116,220,244\nMaaa 116,216,240\nMaaa 124,216,248\nMaaa 124,220,252\nSv 220,1,16,75,3072\nMaaa 120,212,252\nMaaa 120,208,248\nWL 0\n"
"Zvaa 64,160,128\nTa 224,36,0\nAR 60,0\nMvaa 64,164,132\nTa 226,36,2048\nZvaa 96,164,140\nAR 61,1\nMvaa 96,160,136\nTa 228,36,16384\nZvaa 128,160,144\nTa 230,36,18432\n"
"Mvaa 128,164,148\nCV 52,60,61\nZvaa 160,164,156\nTa 232,37,0\nSv 224,1,16,76,0\nMvaa 160,160,152\nTa 234,37,2048\nZvaa 164,168,152\nAR 60,2\nMvaa 164,172,156\nTa 236,37,16384\n"
"Zvaa 132,172,148\nTa 238,37,18432\nMvaa 132,168,144\nAR 61,3\nZvaa 100,168,136\nTa 240,38,0\nMvaa 100,172,140\nTa 242,38,2048\nZvaa 68,172,132\nSv 228,1,16,76,1024\n"
"Mvaa 68,168,128\nTa 244,38,16384\nZvaa 72,176,128\nTa 246,38,18432\nCV 53,60,61\nMvaa 72,180,132\nAR 60,4\nZvaa 104,180,140\nTa 248,39,0\nMvaa 104,176,136\nTa 250,39,2048\n"
"Zvaa 136,176,144\nAR 61,5\nMvaa 136,180,148\nTa 252,39,16384\nZvaa 168,180,156\nTa 254,39,18432\nSv 232,1,16,76,2048\nMvaa 168,176,152\nCV 54,60,61\nZvaa 172,184,152\nAR 60,6\n"
"Mvaa 172,188,156\nAR 61,7\nZvaa 140,188,148\nCV 55,60,61\nMvaa 140,184,144\nAR 60,8\nZvaa 108,184,136\nAR 61,9\nMvaa 108,188,140\nSv 236,1,16,76,3072\nZvaa 76,188,132\n"
"CV 56,60,61\nMvaa 76,184,128\ns_waitcnt vmcnt(29) lgkmcnt(0)\ns_barrier\nZvaa 80,224,128\nRa 192,44,32768\nAR 60,10\nMvaa 80,228,132\nRa 196,45,32768\ns_add_u32 m0, s47, 65536\n"
"s_add_u32 s46, s51, 1572864\nD0 6,32,46,0\nZvaa 112,228,140\nRa 200,44,34816\nMvaa 112,224,136\nRa 204,45,34816\nZvaa 144,224,144\nRa 208,46,32768\nMvaa 144,228,148\n"
"Ra 212,47,32768\nD0 7,32,46,1024\nZvaa 176,228,156\nRa 216,46,34816\nMvaa 176,224,152\nRa 220,47,34816\nZvaa 180,232,152\nTa 160,12,0\nMvaa 180,236,156\nTa 162,12,2048\n"
"D0 8,32,46,2048\nZvaa 148,236,148\nTa 164,12,16384\nMvaa 148,232,144\nTa 166,12,18432\nZvaa 116,232,136\nTa 168,13,0\nMvaa 116,236,140\nTa 170,13,2048\nD0 9,32,46,3072\n"
"Zvaa 84,236,132\nTa 172,13,16384\nMvaa 84,232,128\nTa 174,13,18432\ns_add_u32 m0, s47, 81920\ns_add_u32 s46, s51, 1835008\nZvaa 88,240,128\nTa 176,14,0\nAR 61,11\nMvaa 88,244,132\n"
"Ta 178,14,2048\nD0 6,32,46,0\nZvaa 120,244,140\nTa 180,14,16384\nMvaa 120,240,136\nTa 182,14,18432\nZvaa 152,240,144\nTa 184,15,0\nMvaa 152,244,148\nTa 186,15,2048\n"
"D0 7,32,46,1024\nZvaa 184,244,156\nTa 188,15,16384\nMvaa 184,240,152\nTa 190,15,18432\nZvaa 188,248,152\nCV 57,60,61\nMvaa 188,252,156\nAR 60,12\nD0 8,32,46,2048\n"
"Zvaa 156,252,148\nAR 61,13\nMvaa 156,248,144\nCV 58,60,61\nZvaa 124,248,136\nAR 60,14\nMvaa 124,252,140\nAR 61,15\nD0 9,32,46,3072\nZvaa 92,252,132\nCV 59,60,61\nMvaa 92,248,128\n"
"WL 0\nMvaa 64,160,192\nTa 224,16,0\nAR 60,16\nMvaa 64,164,196\nTa 226,16,2048\ns_add_u32 m0, s47, -65536\ns_add_u32 s46, s52, 512\nD0 40,28,46,0\nMvaa 96,164,204\nAR 61,17\n"
"Mvaa 96,160,200\nTa 228,16,16384\nMvaa 128,160,208\nTa 230,16,18432\nMvaa 128,164,212\nCV 20,60,61\nD0 41,28,46,1024\nMvaa 160,164,220\nTa 232,17,0\nMvaa 160,160,216\n"
"Ta 234,17,2048\nMvaa 164,168,216\nAR 60,18\nMvaa 164,172,220\nTa 236,17,16384\nD0 42,28,46,2048\nMvaa 132,172,212\nTa 238,17,18432\nMvaa 132,168,208\nAR 61,19\nMvaa 100,168,200\n"
"Ta 240,18,0\nMvaa 100,172,204\nTa 242,18,2048\nD0 43,28,46,3072\nMvaa 68,172,196\ns_add_u32 s76, s76, s55\nSv 240,1,16,76,0\nMvaa 68,168,192\nTa 244,18,16384\nMvaa 72,176,192\n"
"Ta 246,18,18432\nCV 21,60,61\nMvaa 72,180,196\nAR 60,20\nMvaa 104,180,204\nTa 248,19,0\nMvaa 104,176,200\nTa 250,19,2048\nMvaa 136,176,208\nSv 244,1,16,76,1024\nMvaa 136,180,212\n"
"Ta 252,19,16384\nMvaa 168,180,220\nTa 254,19,18432\nMvaa 168,176,216\nAR 61,21\nMvaa 172,184,216\nCV 22,60,61\nMvaa 172,188,220\nAR 60,22\nMvaa 140,188,212\nSv 248,1,16,76,2048\n"
"Mvaa 140,184,208\nAR 61,23\nMvaa 108,184,200\nCV 23,60,61\nMvaa 108,188,204\nAR 60,24\nMvaa 76,188,196\nAR 61,25\nMvaa 76,184,192\ns_waitcnt vmcnt(19) lgkmcnt(0)\ns_barrier\n"
"Mvaa 80,224,192\nRa 128,44,16384\nCV 2,60,61\nv_cmp_ne_u32 vcc, s15, v27\ns_nop 0\ns_cbranch_vccnz .S448\n.S449:\nMvaa 80,228,196\nRa 132,45,16384\n"
"buffer_store_dword v25, off, s[16:19], s54 offset:0 sc0 sc1\ns_add_u32 m0, s47, 0\ns_add_u32 s46, s51, 2097152\nD0 6,32,46,0\nMvaa 112,228,204\nRa 136,44,18432\nMvaa 112,224,200\n"
"Ra 140,45,18432\nMvaa 144,224,208\nRa 144,46,16384\nMvaa 144,228,212\nRa 148,47,16384\nD0 7,32,46,1024\nMvaa 176,228,220\nRa 152,46,18432\nMvaa 176,224,216\nRa 156,47,18432\n"
"Mvaa 180,232,216\nTa 160,12,32768\nMvaa 180,236,220\nTa 162,12,34816\nD0 8,32,46,2048\nMvaa 148,236,212\nTa 164,12,49152\nMvaa 148,232,208\nTa 166,12,51200\nMvaa 116,232,200\n"
"Ta 168,13,32768\nMvaa 116,236,204\nTa 170,13,34816\nD0 9,32,46,3072\nMvaa 84,236,196\nTa 172,13,49152\nMvaa 84,232,192\nTa 174,13,51200\ns_add_u32 m0, s47, 16384\n"
"s_add_u32 s46, s51, 2359296\nMvaa 88,240,192\nTa 176,14,32768\nAR 60,26\nMvaa 88,244,196\nTa 178,14,34816\nD0 6,32,46,0\nMvaa 120,244,204\nTa 180,14,49152\nMvaa 120,240,200\n"
"Ta 182,14,51200\nMvaa 152,240,208\nTa 184,15,32768\nMvaa 152,244,212\nTa 186,15,34816\nD0 7,32,46,1024\nMvaa 184,244,220\nTa 188,15,49152\nMvaa 184,240,216\nTa 190,15,51200\n"
"Mvaa 188,248,216\nAR 61,27\nMvaa 188,252,220\nCV 3,60,61\nD0 8,32,46,2048\nMvaa 156,252,212\nAR 60,28\nMvaa 156,248,208\nAR 61,29\nMvaa 124,248,200\nCV 4,60,61\nMvaa 124,252,204\n"
"AR 60,30\nD0 9,32,46,3072\nMvaa 92,252,196\nAR 61,31\nMvaa 92,248,192\nWL 0\nMvaa 64,160,128\nTa 224,16,32768\nCV 5,60,61\nMvaa 64,164,132\nTa 226,16,34816\n"
"s_add_u32 m0, s47, -32768\ns_add_u32 s46, s52, 640\nD0 40,28,46,0\nMvaa 96,164,140\nMvaa 96,160,136\nTa 228,16,49152\nMvaa 128,160,144\nTa 230,16,51200\nMvaa 128,164,148\n"
"D0 41,28,46,1024\nMvaa 160,164,156\nTa 232,17,32768\nMvaa 160,160,152\nTa 234,17,34816\nMvaa 164,168,152\nMvaa 164,172,156\nTa 236,17,49152\nD0 42,28,46,2048\nMvaa 132,172,148\n"
"Ta 238,17,51200\nMvaa 132,168,144\nMvaa 100,168,136\nTa 240,18,32768\nMvaa 100,172,140\nTa 242,18,34816\nD0 43,28,46,3072\nMvaa 68,172,132\nSv 252,1,16,76,3072\nMvaa 68,168,128\n"
"Ta 244,18,49152\nMvaa 72,176,128\nTa 246,18,51200\nMvaa 72,180,132\nMvaa 104,180,140\nTa 248,19,32768\nMvaa 104,176,136\nTa 250,19,34816\nMvaa 136,176,144\nSv 52,1,16,77,0\n"
"Mvaa 136,180,148\nTa 252,19,49152\nMvaa 168,180,156\nTa 254,19,51200\nMvaa 168,176,152\nMvaa 172,184,152\nMvaa 172,188,156\nMvaa 140,188,148\nSv 56,1,16,77,1024\nMvaa 140,184,144\n"
"Mvaa 108,184,136\nMvaa 108,188,140\nMvaa 76,188,132\nMvaa 76,184,128\ns_waitcnt vmcnt(23) lgkmcnt(0)\ns_barrier\nMvaa 80,224,128\nRa 192,44,49152\nMvaa 80,228,132\n"
"Ra 196,45,49152\ns_add_u32 m0, s47, 32768\ns_add_u32 s46, s51, 2621440\nD0 6,32,46,0\nMvaa 112,228,140\nRa 200,44,51200\nMvaa 112,224,136\nRa 204,45,51200\nMvaa 144,224,144\n"
"Ra 208,46,49152\nMvaa 144,228,148\nRa 212,47,49152\nD0 7,32,46,1024\nMvaa 176,228,156\nRa 216,46,51200\nMvaa 176,224,152\nRa 220,47,51200\nMvaa 180,232,152\nTa 160,32,0\n"
"Mvaa 180,236,156\nTa 162,32,2048\nD0 8,32,46,2048\nMvaa 148,236,148\nTa 164,32,16384\nMvaa 148,232,144\nTa 166,32,18432\nMvaa 116,232,136\nTa 168,33,0\nMvaa 116,236,140\n"
"Ta 170,33,2048\nD0 9,32,46,3072\nMvaa 84,236,132\nTa 172,33,16384\nMvaa 84,232,128\nTa 174,33,18432\ns_add_u32 m0, s47, 49152\ns_add_u32 s46, s51, 2883584\nMvaa 88,240,128\n"
"Ta 176,34,0\nMvaa 88,244,132\nTa 178,34,2048\nD0 6,32,46,0\nMvaa 120,244,140\nTa 180,34,16384\nMvaa 120,240,136\nTa 182,34,18432\nMvaa 152,240,144\nTa 184,35,0\nMvaa 152,244,148\n"
"Ta 186,35,2048\nD0 7,32,46,1024\nMvaa 184,244,156\nTa 188,35,16384\nMvaa 184,240,152\nTa 190,35,18432\nMvaa 188,248,152\nMvaa 188,252,156\nD0 8,32,46,2048\nMvaa 156,252,148\n"
"Mvaa 156,248,144\nMvaa 124,248,136\nMvaa 124,252,140\nD0 9,32,46,3072\nMvaa 92,252,132\nMvaa 92,248,128\nWL 0\nMvaa 64,160,192\nTa 224,36,0\nMvaa 64,164,196\nTa 226,36,2048\n"
"s_add_u32 m0, s47, -49152\ns_add_u32 s46, s52, 768\nD0 40,28,46,0\nMvaa 96,164,204\nMvaa 96,160,200\nTa 228,36,16384\nMvaa 128,160,208\nTa 230,36,18432\nMvaa 128,164,212\n"
"D0 41,28,46,1024\nMvaa 160,164,220\nTa 232,37,0\nMvaa 160,160,216\nTa 234,37,2048\nMvaa 164,168,216\nMvaa 164,172,220\nTa 236,37,16384\nD0 42,28,46,2048\nMvaa 132,172,212\n"
"Ta 238,37,18432\nMvaa 132,168,208\nMvaa 100,168,200\nTa 240,38,0\nMvaa 100,172,204\nTa 242,38,2048\nD0 43,28,46,3072\nMvaa 68,172,196\nSv 20,1,16,77,2048\nMvaa 68,168,192\n"
"Ta 244,38,16384\nMvaa 72,176,192\nTa 246,38,18432\nMvaa 72,180,196\nMvaa 104,180,204\nTa 248,39,0\nMvaa 104,176,200\nTa 250,39,2048\nMvaa 136,176,208\nSv 2,1,16,77,3072\n"
"Mvaa 136,180,212\nTa 252,39,16384\nMvaa 168,180,220\nTa 254,39,18432\nAR 60,32\nAR 61,33\nCV 52,60,61\nAR 60,34\nAR 61,35\nCV 53,60,61\nAR 60,36\nAR 61,37\nCV 54,60,61\nAR 60,38\n"
"AR 61,39\nMvaa 168,176,216\nCV 55,60,61\nAR 60,40\nAR 61,41\nCV 56,60,61\nAR 60,42\nAR 61,43\nCV 57,60,61\nAR 60,44\nAR 61,45\nCV 58,60,61\nAR 60,46\nAR 61,47\nMvaa 172,184,216\n"
"CV 59,60,61\nAR 60,48\nAR 61,49\nCV 20,60,61\nAR 60,50\nAR 61,51\nCV 21,60,61\nAR 60,52\nAR 61,53\nCV 22,60,61\nAR 60,54\nAR 61,55\nCV 23,60,61\nMvaa 172,188,220\nAR 60,56\n"
"AR 61,57\nCV 2,60,61\nAR 60,58\nAR 61,59\nCV 3,60,61\nAR 60,60\nAR 61,61\nCV 4,60,61\nAR 60,62\nAR 61,63\nCV 5,60,61\nMvaa 140,188,212\ns_add_u32 s77, s77, s55\nSv 52,1,16,77,0\n"
"Mvaa 140,184,208\nAR 60,126\nAR 61,127\nMvaa 108,184,200\nCV 60,60,61\nAW 127,60\nMvaa 108,188,204\nAR 60,124\nMvaa 76,188,196\nAR 61,125\nCV 60,60,61\nMvaa 76,184,192\n"
"s_waitcnt vmcnt(17) lgkmcnt(0)\ns_barrier\nMvaa 80,224,192\nRa 128,44,0\nv_and_b32 v27, 0x70, v1\nbuffer_load_dword v27, v27, s[16:19], s61 offen offset:128 sc0 sc1\n"
"Mvaa 80,228,196\nRa 132,45,0\nbuffer_store_dword v25, off, s[16:19], s54 offset:8 sc0 sc1\ns_add_u32 m0, s47, 65536\ns_add_u32 s46, s51, 3145728\nD0 6,32,46,0\nMvaa 112,228,204\n"
"Ra 136,44,2048\nAW 126,60\nMvaa 112,224,200\nRa 140,45,2048\nAR 60,122\nMvaa 144,224,208\nRa 144,46,0\nAR 61,123\nMvaa 144,228,212\nRa 148,47,0\nD0 7,32,46,1024\nMvaa 176,228,220\n"
"Ra 152,46,2048\nCV 60,60,61\nMvaa 176,224,216\nRa 156,47,2048\nAW 125,60\nMvaa 180,232,216\nTa 160,12,0\nAR 60,120\nMvaa 180,236,220\nTa 162,12,2048\nD0 8,32,46,2048\n"
"Mvaa 148,236,212\nTa 164,12,16384\nAR 61,121\nMvaa 148,232,208\nTa 166,12,18432\nCV 60,60,61\nMvaa 116,232,200\nTa 168,13,0\nAW 124,60\nMvaa 116,236,204\nTa 170,13,2048\n"
"D0 9,32,46,3072\nMvaa 84,236,196\nTa 172,13,16384\nAR 60,118\nMvaa 84,232,192\nTa 174,13,18432\ns_add_u32 m0, s47, 81920\ns_add_u32 s46, s51, 3407872\nMvaa 88,240,192\n"
"Ta 176,14,0\nAR 61,119\nMvaa 88,244,196\nTa 178,14,2048\nD0 6,32,46,0\nMvaa 120,244,204\nTa 180,14,16384\nCV 60,60,61\nMvaa 120,240,200\nTa 182,14,18432\nAW 123,60\n"
"Mvaa 152,240,208\nTa 184,15,0\nAR 60,116\nMvaa 152,244,212\nTa 186,15,2048\nD0 7,32,46,1024\nMvaa 184,244,220\nTa 188,15,16384\nAR 61,117\nMvaa 184,240,216\nTa 190,15,18432\n"
"CV 60,60,61\nMvaa 188,248,216\nAW 122,60\nAR 60,114\nMvaa 188,252,220\nD0 8,32,46,2048\nMvaa 156,252,212\nAR 61,115\nCV 60,60,61\nMvaa 156,248,208\nAW 121,60\nAR 60,112\n"
"Mvaa 124,248,200\nAR 61,113\nCV 60,60,61\nMvaa 124,252,204\nD0 9,32,46,3072\nMvaa 92,252,196\nAW 120,60\nAR 60,110\nMvaa 92,248,192\nWL 0\nMvaa 64,160,128\nTa 224,16,0\nAR 61,111\n"
"Mvaa 64,164,132\nTa 226,16,2048\ns_add_u32 s6, s56, 0\nbuffer_load_dwordx4 v[192:195], v1, s[16:19], s6 offen offset:0 sc1\nMvaa 96,164,140\nCV 60,60,61\nAW 119,60\n"
"Mvaa 96,160,136\nTa 228,16,16384\nAR 60,108\nMvaa 128,160,144\nTa 230,16,18432\nAR 61,109\nMvaa 128,164,148\n"
"buffer_load_dwordx4 v[196:199], v1, s[16:19], s6 offen offset:1024 sc1\nMvaa 160,164,156\nTa 232,17,0\nCV 60,60,61\nMvaa 160,160,152\nTa 234,17,2048\nAW 118,60\nMvaa 164,168,152\n"
"AR 60,106\nAR 61,107\nMvaa 164,172,156\nTa 236,17,16384\nbuffer_load_dwordx4 v[200:203], v1, s[16:19], s6 offen offset:2048 sc1\nMvaa 132,172,148\nTa 238,17,18432\nCV 60,60,61\n"
"Mvaa 132,168,144\nAW 117,60\nAR 60,104\nMvaa 100,168,136\nTa 240,18,0\nAR 61,105\nMvaa 100,172,140\nTa 242,18,2048\n"
"buffer_load_dwordx4 v[204:207], v1, s[16:19], s6 offen offset:3072 sc1\nMvaa 68,172,132\nSv 56,1,16,77,1024\nCV 60,60,61\nMvaa 68,168,128\nTa 244,18,16384\nAW 116,60\n"
"Mvaa 72,176,128\nTa 246,18,18432\nAR 60,102\nMvaa 72,180,132\nAR 61,103\nMvaa 104,180,140\nTa 248,19,0\nCV 60,60,61\nMvaa 104,176,136\nTa 250,19,2048\nAW 115,60\nMvaa 136,176,144\n"
"Sv 20,1,16,77,2048\nAR 60,100\nMvaa 136,180,148\nTa 252,19,16384\nMvaa 168,180,156\nTa 254,19,18432\nAR 61,101\nMvaa 168,176,152\nCV 60,60,61\nAW 114,60\nMvaa 172,184,152\n"
"AR 60,98\nAR 61,99\nMvaa 172,188,156\nCV 60,60,61\nMvaa 140,188,148\nSv 2,1,16,77,3072\nAW 113,60\nMvaa 140,184,144\nAR 60,96\nAR 61,97\nMvaa 108,184,136\nCV 60,60,61\nAW 112,60\n"
"Mvaa 108,188,140\nAR 60,94\nMvaa 76,188,132\nAR 61,95\nCV 60,60,61\nMvaa 76,184,128\ns_waitcnt vmcnt(16) lgkmcnt(0)\ns_barrier\nMvaa 80,224,128\nRa 192,44,32768\n"
"v_cmp_ne_u32 vcc, s15, v27\ns_nop 0\ns_cbranch_vccnz .S451\n.S452:\ns_sub_u32 s45, s86, 131072\ns_cmp_eq_u32 s79, 1\ns_cselect_b32 s86, s94, s86\ns_add_u32 m0, s47, -65536\n"
"s_add_u32 s46, s45, 0x0\nMvaa 80,228,132\nRa 196,45,32768\nMvaa 112,228,140\nRa 200,44,34816\nD1 1,16,46,0\nMvaa 112,224,136\nRa 204,45,34816\nAW 111,60\nMvaa 144,224,144\n"
"Ra 208,46,32768\nAR 60,92\nMvaa 144,228,148\nRa 212,47,32768\nMvaa 176,228,156\nRa 216,46,34816\nD1 1,16,46,1024\nMvaa 176,224,152\nRa 220,47,34816\nAR 61,93\nMvaa 180,232,152\n"
"Ta 160,12,32768\nCV 60,60,61\nMvaa 180,236,156\nTa 162,12,34816\nMvaa 148,236,148\nTa 164,12,49152\nD1 1,16,46,2048\nMvaa 148,232,144\nTa 166,12,51200\nAW 110,60\n"
"Mvaa 116,232,136\nTa 168,13,32768\nAR 60,90\nMvaa 116,236,140\nTa 170,13,34816\nMvaa 84,236,132\nTa 172,13,49152\nD1 1,16,46,3072\nMvaa 84,232,128\nTa 174,13,51200\nAR 61,91\n"
"Mvaa 88,240,128\nTa 176,14,32768\ns_add_u32 s6, s56, 4096\nMvaa 88,244,132\nTa 178,14,34816\nMvaa 120,244,140\nTa 180,14,49152\n"
"buffer_load_dwordx4 v[208:211], v1, s[16:19], s6 offen offset:0 sc1\nMvaa 120,240,136\nTa 182,14,51200\nCV 60,60,61\nMvaa 152,240,144\nTa 184,15,32768\nAW 109,60\n"
"Mvaa 152,244,148\nTa 186,15,34816\nMvaa 184,244,156\nTa 188,15,49152\nbuffer_load_dwordx4 v[212:215], v1, s[16:19], s6 offen offset:1024 sc1\nMvaa 184,240,152\nTa 190,15,51200\n"
"AR 60,88\nMvaa 188,248,152\nAR 61,89\nCV 60,60,61\nMvaa 188,252,156\nAW 108,60\nMvaa 156,252,148\nbuffer_load_dwordx4 v[216:219], v1, s[16:19], s6 offen offset:2048 sc1\nAR 60,86\n"
"Mvaa 156,248,144\nAR 61,87\nCV 60,60,61\nMvaa 124,248,136\nAW 107,60\nAR 60,84\nMvaa 124,252,140\nAR 61,85\nMvaa 92,252,132\n"
"buffer_load_dwordx4 v[220:223], v1, s[16:19], s6 offen offset:3072 sc1\nCV 60,60,61\nMvaa 92,248,128\nWL 0\nMvaa 64,160,192\nTa 224,16,32768\ns_add_u32 s6, s56, 16384\n"
"Mvaa 64,164,196\nTa 226,16,34816\nMvaa 96,164,204\nbuffer_load_dwordx4 v[224:227], v1, s[16:19], s6 offen offset:0 sc1\nAW 106,60\nMvaa 96,160,200\nTa 228,16,49152\nAR 60,82\n"
"Mvaa 128,160,208\nTa 230,16,51200\nAR 61,83\nMvaa 128,164,212\nCV 60,60,61\nMvaa 160,164,220\nTa 232,17,32768\n"
"buffer_load_dwordx4 v[228:231], v1, s[16:19], s6 offen offset:1024 sc1\nMvaa 160,160,216\nTa 234,17,34816\nAW 105,60\nMvaa 164,168,216\nAR 60,80\nAR 61,81\nMvaa 164,172,220\n"
"Ta 236,17,49152\nMvaa 132,172,212\nTa 238,17,51200\nbuffer_load_dwordx4 v[232:235], v1, s[16:19], s6 offen offset:2048 sc1\nMvaa 132,168,208\nCV 60,60,61\nAW 104,60\n"
"Mvaa 100,168,200\nTa 240,18,32768\nAR 60,78\nMvaa 100,172,204\nTa 242,18,34816\nMvaa 68,172,196\nbuffer_load_dwordx4 v[236:239], v1, s[16:19], s6 offen offset:3072 sc1\nAR 61,79\n"
"Mvaa 68,168,192\nTa 244,18,49152\nCV 60,60,61\nMvaa 72,176,192\nTa 246,18,51200\nAW 103,60\nMvaa 72,180,196\nAR 60,76\nMvaa 104,180,204\nTa 248,19,32768\nAR 61,77\n"
"Mvaa 104,176,200\nTa 250,19,34816\nCV 60,60,61\nMvaa 136,176,208\nAW 102,60\nAR 60,74\nMvaa 136,180,212\nTa 252,19,49152\nMvaa 168,180,220\nTa 254,19,51200\nAR 61,75\n"
"Mvaa 168,176,216\nCV 60,60,61\nAW 101,60\nMvaa 172,184,216\nAR 60,72\nAR 61,73\nMvaa 172,188,220\nCV 60,60,61\nMvaa 140,188,212\nAW 100,60\nAR 60,70\nMvaa 140,184,208\nAR 61,71\n"
"CV 60,60,61\nMvaa 108,184,200\nAW 99,60\nAR 60,68\nMvaa 108,188,204\nAR 61,69\nMvaa 76,188,196\nCV 60,60,61\nAW 98,60\nMvaa 76,184,192\ns_waitcnt vmcnt(19) lgkmcnt(0)\ns_barrier\n"
"Mvaa 80,224,192\nRa 128,44,16384\nAR 60,66\nMvaa 80,228,196\nRa 132,45,16384\ns_add_u32 s6, s56, 20480\nbuffer_load_dwordx4 v[240:243], v1, s[16:19], s6 offen offset:0 sc1\n"
"Mvaa 112,228,204\nRa 136,44,18432\nAR 61,67\nMvaa 112,224,200\nRa 140,45,18432\nCV 60,60,61\nMvaa 144,224,208\nRa 144,46,16384\nAW 97,60\nMvaa 144,228,212\nRa 148,47,16384\n"
"buffer_load_dwordx4 v[244:247], v1, s[16:19], s6 offen offset:1024 sc1\nMvaa 176,228,220\nRa 152,46,18432\nAR 60,64\nMvaa 176,224,216\nRa 156,47,18432\nAR 61,65\nMvaa 180,232,216\n"
"Ta 160,32,0\nCV 60,60,61\nMvaa 180,236,220\nTa 162,32,2048\nbuffer_load_dwordx4 v[248:251], v1, s[16:19], s6 offen offset:2048 sc1\nMvaa 148,236,212\nTa 164,32,16384\nAW 96,60\n"
"Mvaa 148,232,208\nTa 166,32,18432\nv_add_u32 v2, 0x0, v1\ns_lshr_b32 s2, s59, 1\nMvaa 116,232,200\nTa 168,33,0\nv_add_u32 v24, s2, v2\nMvaa 116,236,204\nTa 170,33,2048\n"
"buffer_load_dwordx4 v[252:255], v1, s[16:19], s6 offen offset:3072 sc1\nMvaa 84,236,196\nTa 172,33,16384\nv_xor_b32 v26, 0x1000, v24\nMvaa 84,232,192\nTa 174,33,18432\n"
"s_add_u32 m0, s47, -32768\ns_add_u32 s46, s45, 0x8000\nMvaa 88,240,192\nTa 176,34,0\nv_lshrrev_b32 v20, 4, v0\nv_lshlrev_b32 v20, 2, v20\nMvaa 88,244,196\nTa 178,34,2048\n"
"D1 1,16,46,0\nMvaa 120,244,204\nTa 180,34,16384\nv_and_b32 v0, 15, v0\nMvaa 120,240,200\nTa 182,34,18432\nv_sub_u32 v0, v0, v20\nMvaa 152,240,208\nTa 184,35,0\n"
"s_lshl_b32 s2, s11, 4\nMvaa 152,244,212\nTa 186,35,2048\nD1 1,16,46,1024\nMvaa 184,244,220\nTa 188,35,16384\nv_add_u32 v3, s2, v0\nv_subrev_u32 v0, 32, v3\nMvaa 184,240,216\n"
"Ta 190,35,18432\ns_lshl_b32 s2, s11, 13\nMvaa 188,248,216\ns_add_u32 s2, s2, 0x20000\nv_add_u32 v22, s2, v1\nMvaa 188,252,220\nD1 1,16,46,2048\nv_xor_b32 v23, 0x2000, v22\n"
"Mvaa 156,252,212\nv_mov_b32 v4, 0\nv_mov_b32 v5, 0\nMvaa 156,248,208\nv_mov_b32 v6, 0\nv_mov_b32 v7, 0\nv_mov_b32 v8, 0\nMvaa 124,248,200\nv_mov_b32 v9, 0\nv_mov_b32 v10, 0\n"
"Mvaa 124,252,204\nD1 1,16,46,3072\nv_mov_b32 v11, 0\nMvaa 92,252,196\nv_mov_b32 v12, 0\nv_mov_b32 v13, 0\nMvaa 92,248,192\nWL 0\nMvaa 64,160,128\nTa 224,36,0\nv_mov_b32 v14, 0\n"
"v_mov_b32 v15, 0\nMvaa 64,164,132\nTa 226,36,2048\ns_add_u32 m0, s47, -16384\ns_add_u32 s46, s45, 0x400000\nD1 1,16,46,0\nMvaa 68,172,132\nv_mov_b32 v16, 0\nv_mov_b32 v17, 0\n"
"Mvaa 68,168,128\nTa 228,36,16384\nv_mov_b32 v18, 0\nMvaa 72,176,128\nTa 230,36,18432\nv_mov_b32 v19, 0\nMvaa 72,180,132\nD1 1,16,46,1024\nMvaa 76,188,132\nTa 232,37,0\n"
"Mvaa 76,184,128\nTa 234,37,2048\nMvaa 108,184,136\nv_mul_f32 v64, s57, v64\nv_mul_f32 v65, s57, v65\nMvaa 108,188,140\nTa 236,37,16384\nD1 1,16,46,2048\nMvaa 104,180,140\n"
"Ta 238,37,18432\nCV 64,64,65\nMvaa 104,176,136\nv_mul_f32 v66, s57, v66\nv_mul_f32 v67, s57, v67\nMvaa 100,168,136\nTa 240,38,0\nCV 65,66,67\nMvaa 100,172,140\nTa 242,38,2048\n"
"D1 1,16,46,3072\nMvaa 96,164,140\nv_mul_f32 v68, s57, v68\nv_mul_f32 v69, s57, v69\nMvaa 96,160,136\nTa 244,38,16384\nCV 66,68,69\nMvaa 128,160,144\nTa 246,38,18432\n"
"v_mul_f32 v70, s57, v70\nMvaa 128,164,148\nv_mul_f32 v71, s57, v71\nCV 67,70,71\nMvaa 132,172,148\nTa 248,39,0\nAW 192,64\nMvaa 132,168,144\nTa 250,39,2048\nAW 193,65\n"
"Mvaa 136,176,144\nAW 194,66\nAW 195,67\nMvaa 136,180,148\nTa 252,39,16384\nv_mul_f32 v72, s57, v72\nMvaa 140,188,148\nTa 254,39,18432\nv_mul_f32 v73, s57, v73\nMvaa 140,184,144\n"
"CV 72,72,73\nv_mul_f32 v74, s57, v74\nMvaa 172,184,152\nv_mul_f32 v75, s57, v75\nCV 73,74,75\nMvaa 172,188,156\nv_mul_f32 v76, s57, v76\nv_mul_f32 v77, s57, v77\nMvaa 168,180,156\n"
"CV 74,76,77\nv_mul_f32 v78, s57, v78\nMvaa 168,176,152\nv_mul_f32 v79, s57, v79\nCV 75,78,79\nMvaa 164,168,152\nAW 200,72\nAW 201,73\nMvaa 164,172,156\nAW 202,74\nAW 203,75\n"
"Mvaa 160,164,156\nv_mul_f32 v96, s57, v96\nv_mul_f32 v97, s57, v97\nMvaa 160,160,152\ns_waitcnt vmcnt(20) lgkmcnt(0)\ns_barrier\nMvaa 80,224,128\nCV 96,96,97\n"
"v_mul_f32 v98, s57, v98\nMvaa 80,228,132\nbuffer_store_dword v25, off, s[16:19], s54 offset:12 sc0 sc1\ns_add_u32 m0, s47, 0\ns_add_u32 s46, s86, 0xffff0000\nD1 1,16,46,0\n"
"Mvaa 84,236,132\nv_mul_f32 v99, s57, v99\nCV 97,98,99\nMvaa 84,232,128\nv_mul_f32 v100, s57, v100\nv_mul_f32 v101, s57, v101\nMvaa 88,240,128\nCV 98,100,101\n"
"v_mul_f32 v102, s57, v102\nMvaa 88,244,132\nD1 1,16,46,1024\nv_mul_f32 v103, s57, v103\nMvaa 92,252,132\nCV 99,102,103\nAW 196,96\nMvaa 92,248,128\nAW 197,97\nAW 198,98\n"
"Mvaa 124,248,136\nAW 199,99\nv_mul_f32 v128, s57, v128\nMvaa 124,252,140\nD1 1,16,46,2048\nv_mul_f32 v129, s57, v129\nMvaa 120,244,140\nCV 128,128,129\nv_mul_f32 v130, s57, v130\n"
"Mvaa 120,240,136\nv_mul_f32 v131, s57, v131\nCV 129,130,131\nMvaa 116,232,136\nRa 160,24,0\nv_mul_f32 v132, s57, v132\nMvaa 116,236,140\nD1 1,16,46,3072\n"
"v_mul_f32 v133, s57, v133\nMvaa 112,228,140\nRa 164,24,1024\nCV 130,132,133\nMvaa 112,224,136\ns_add_u32 m0, s47, 16384\ns_add_u32 s46, s45, 0x408000\nMvaa 144,224,144\n"
"Ra 168,24,2048\nv_mul_f32 v134, s57, v134\nMvaa 144,228,148\nD1 1,16,46,0\nv_mul_f32 v135, s57, v135\nMvaa 148,236,148\nRa 172,24,3072\nCV 131,134,135\nMvaa 148,232,144\n"
"ds_write_b128 v23, v[128:131] offset:0\nv_mul_f32 v80, s57, v80\nMvaa 152,240,144\nRa 176,26,0\nv_mul_f32 v81, s57, v81\nMvaa 152,244,148\nD1 1,16,46,1024\nCV 80,80,81\n"
"Mvaa 156,252,148\nRa 180,26,1024\nv_mul_f32 v82, s57, v82\nMvaa 156,248,144\nv_mul_f32 v83, s57, v83\nCV 81,82,83\nMvaa 188,248,152\nRa 184,26,2048\nv_mul_f32 v84, s57, v84\n"
"Mvaa 188,252,156\nD1 1,16,46,2048\nv_mul_f32 v85, s57, v85\nMvaa 184,244,156\nRa 188,26,3072\nCV 82,84,85\nMvaa 184,240,152\nv_mul_f32 v86, s57, v86\nv_mul_f32 v87, s57, v87\n"
"Mvaa 180,232,152\nCV 83,86,87\nAW 208,80\nMvaa 180,236,156\nD1 1,16,46,3072\nAW 209,81\nMvaa 176,228,156\nAW 210,82\nAW 211,83\nMvaa 176,224,152\ns_waitcnt vmcnt(25) lgkmcnt(5)\n"
"s_cmp_eq_u32 s79, 1\ns_cbranch_scc1 .E454\n.E455:\nCvav 32,160,192,4\nRa 128,24,8192\nv_mul_f32 v88, s57, v88\nMvav 32,164,200\nv_mul_f32 v89, s57, v89\nCV 88,88,89\n"
"Mvav 32,168,208\nRa 132,24,9216\nv_mul_f32 v90, s57, v90\nMvav 32,172,216\nv_mul_f32 v91, s57, v91\nCV 89,90,91\nCvav 36,172,220,8\nRa 136,24,10240\nv_mul_f32 v92, s57, v92\n"
"Mvav 36,168,212\ns_add_u32 m0, s89, 32768\nv_mul_f32 v93, s57, v93\nMvav 36,164,204\nRa 140,24,11264\ns_waitcnt vmcnt(17) lgkmcnt(4)\ns_barrier\nMvav 36,160,196\nCV 90,92,93\n"
"v_mul_f32 v94, s57, v94\nMvav 36,176,228\nRa 144,26,8192\nSa 96,1,16,78,0\nMvav 36,180,236\ns_add_u32 s46, s86, s88\nv_mul_f32 v95, s57, v95\nMvav 36,184,244\nRa 148,26,9216\n"
"CV 91,94,95\nMvav 36,188,252\nAW 216,88\nAW 217,89\nMvav 32,188,248\nRa 152,26,10240\nAW 218,90\nMvav 32,184,240\nD1 1,16,46,0\nAW 219,91\nMvav 32,180,232\nRa 156,26,11264\nWL 4\n"
"Mvav 32,176,224\nv_mov_b32 v86, 0\nv_mov_b32 v87, 0\nCvav 40,128,192,12\nSa 100,1,16,78,1024\nv_mov_b32 v88, 0\nMvav 40,132,200\ns_cmp_eq_u32 s99, -1\nMvav 40,136,208\n"
"s_cselect_b32 s87, s95, s87\nv_mov_b32 v89, 0\nv_mul_f32 v136, s57, v136\nv_mul_f32 v137, s57, v137\nCV 136,136,137\nv_mul_f32 v138, s57, v138\nv_mul_f32 v139, s57, v139\n"
"Mvav 40,140,216\nCV 137,138,139\nv_mul_f32 v140, s57, v140\nv_mul_f32 v141, s57, v141\nCV 138,140,141\nv_mul_f32 v142, s57, v142\nv_mul_f32 v143, s57, v143\nCvav 44,140,220,16\n"
"v_and_b32 v27, 0x70, v1\nbuffer_load_dword v27, v27, s[16:19], s61 offen offset:136 sc0 sc1\nCV 139,142,143\nds_write_b128 v23, v[136:139] offset:2048\nv_mul_f32 v144, s57, v144\n"
"v_mul_f32 v145, s57, v145\nCV 144,144,145\nMvav 44,136,212\nD1 1,16,46,1024\nv_mul_f32 v146, s57, v146\nv_mul_f32 v147, s57, v147\nCV 145,146,147\nv_mul_f32 v148, s57, v148\n"
"v_mul_f32 v149, s57, v149\nMvav 44,132,204\nWL 1\nCV 146,148,149\nv_mul_f32 v150, s57, v150\nv_mul_f32 v151, s57, v151\nCV 147,150,151\nds_write_b128 v23, v[144:147] offset:4096\n"
"v_mul_f32 v152, s57, v152\nMvav 44,128,196\nv_mul_f32 v153, s57, v153\nCV 152,152,153\nv_mul_f32 v154, s57, v154\nv_mul_f32 v155, s57, v155\nCV 153,154,155\n"
"v_mul_f32 v156, s57, v156\nMvav 44,144,228\nSa 104,1,16,78,2048\nv_mul_f32 v157, s57, v157\nCV 154,156,157\nv_mul_f32 v158, s57, v158\nv_mul_f32 v159, s57, v159\nCV 155,158,159\n"
"ds_write_b128 v23, v[152:155] offset:6144\nMvav 44,148,236\ns_add_u32 s88, s88, 32768\nEX 80,32\nv_mul_f32 v160, s57, v160\nv_mul_f32 v161, s57, v161\nCV 160,160,161\n"
"Mvav 44,152,244\nv_mul_f32 v162, s57, v162\nv_mul_f32 v163, s57, v163\nCV 161,162,163\nv_mul_f32 v164, s57, v164\nv_mul_f32 v165, s57, v165\nCV 162,164,165\n"
"v_mul_f32 v166, s57, v166\nMvav 44,156,252\nEX 81,33\nv_mul_f32 v167, s57, v167\nCV 163,166,167\nds_write_b128 v23, v[160:163] offset:1024\nMvav 40,156,248\n"
"v_mul_f32 v168, s57, v168\nv_mul_f32 v169, s57, v169\nCV 168,168,169\nv_mul_f32 v170, s57, v170\nv_mul_f32 v171, s57, v171\nCV 169,170,171\nv_mul_f32 v172, s57, v172\n"
"Mvav 40,152,240\nD1 1,16,46,2048\nv_mul_f32 v173, s57, v173\nCV 170,172,173\nv_mul_f32 v174, s57, v174\nv_mul_f32 v175, s57, v175\nCV 171,174,175\nMvav 40,148,232\n"
"ds_write_b128 v23, v[168:171] offset:3072\nv_mul_f32 v176, s57, v176\nv_mul_f32 v177, s57, v177\nCV 176,176,177\nv_mul_f32 v178, s57, v178\nv_mul_f32 v179, s57, v179\n"
"CV 177,178,179\nMvav 40,144,224\nAD 84,80,81\nCV 64,80,81\nv_mul_f32 v180, s57, v180\nv_mul_f32 v181, s57, v181\nCV 178,180,181\nv_mul_f32 v182, s57, v182\nSa 108,1,16,78,3072\n"
"v_mul_f32 v183, s57, v183\nCV 179,182,183\nds_write_b128 v23, v[176:179] offset:5120\nv_mul_f32 v184, s57, v184\nv_mul_f32 v185, s57, v185\nCV 184,184,185\nEX 82,34\n"
"v_mul_f32 v186, s57, v186\nv_mul_f32 v187, s57, v187\nCV 185,186,187\nv_mul_f32 v188, s57, v188\nv_mul_f32 v189, s57, v189\nCV 186,188,189\nv_mul_f32 v190, s57, v190\n"
"v_mul_f32 v191, s57, v191\nCV 187,190,191\nds_write_b128 v23, v[184:187] offset:7168\ns_waitcnt vmcnt(17) lgkmcnt(0)\nEX 83,35\nAD 84,84,82\nD1 1,16,46,3072\n"
"s_add_u32 m0, s89, 49152\ns_barrier\nAD 84,84,83\nCV 65,82,83\nRa 128,24,32768\nRa 224,22,0\nRa 228,22,1024\ns_add_u32 s46, s87, s88\nEX 80,36\nRa 132,24,33792\nRa 232,22,2048\n"
"Ra 236,22,3072\nRa 240,22,4096\nEX 81,37\nRa 136,24,34816\nRa 244,22,5120\nRa 248,22,6144\nRa 252,22,7168\nD1 1,16,46,0\nv_mul_f32 v104, s57, v104\nv_mul_f32 v105, s57, v105\n"
"Ra 140,24,35840\nCV 104,104,105\nv_mul_f32 v106, s57, v106\nv_mul_f32 v107, s57, v107\nAD 85,80,81\nCV 68,80,81\nCV 105,106,107\nRa 144,26,32768\nv_mul_f32 v108, s57, v108\n"
"v_mul_f32 v109, s57, v109\nCV 106,108,109\ns_cmp_eq_u32 s99, -1\nEX 82,38\nRa 148,26,33792\ns_cbranch_scc1 .E437\n.E438:\nv_mul_f32 v110, s57, v110\nEX 83,39\n"
"v_mul_f32 v111, s57, v111\nRa 152,26,34816\nAD 85,85,82\nCV 107,110,111\nD1 1,16,46,1024\nAW 204,104\nAW 205,105\nAW 206,106\nRa 156,26,35840\nAW 207,107\n"
"v_mul_f32 v112, s57, v112\nAD 85,85,83\nCV 69,82,83\nv_mul_f32 v113, s57, v113\nCV 112,112,113\nRa 160,24,40960\nv_mul_f32 v114, s57, v114\nv_mul_f32 v115, s57, v115\n"
"s_xor_b32 s89, s89, 0x10000\nEX 80,40\nRa 164,24,41984\nCV 113,114,115\nv_mul_f32 v116, s57, v116\nv_mul_f32 v117, s57, v117\nWL 6\nEX 81,41\nRa 168,24,43008\nAD 84,84,80\n"
"CV 114,116,117\nv_mul_f32 v118, s57, v118\nD1 1,16,46,2048\nv_mul_f32 v119, s57, v119\nCV 115,118,119\nRa 172,24,44032\nAW 212,112\nAW 213,113\nAW 214,114\nAD 84,84,81\n"
"CV 66,80,81\nAW 215,115\nCvav 48,128,192,4\nRa 176,26,40960\nv_mul_f32 v120, s57, v120\nv_mul_f32 v121, s57, v121\nCV 120,120,121\nMvav 48,132,200\ns_cmp_eq_u32 s99, 0\nEX 82,42\n"
"Mvav 48,136,208\nRa 180,26,41984\ns_cselect_b32 s86, s94, s86\nv_mul_f32 v122, s57, v122\nMvav 48,140,216\nWL 6\nEX 83,43\nWV 8\nv_cmp_ne_u32 vcc, s15, v27\ns_nop 0\n"
"s_cbranch_vccnz .S456\n.S457:\nCvav 52,140,220,8\nRa 184,26,43008\nAD 84,84,82\nv_mul_f32 v123, s57, v123\nMvav 52,136,212\nD1 1,16,46,3072\ns_add_u32 m0, s89, 0\nCV 121,122,123\n"
"v_mul_f32 v124, s57, v124\nMvav 52,132,204\nRa 188,26,44032\nv_mul_f32 v125, s57, v125\nCV 122,124,125\nMvav 52,128,196\nAD 84,84,83\nCV 67,82,83\nv_mul_f32 v126, s57, v126\n"
"v_mul_f32 v127, s57, v127\nMvav 52,144,228\nRa 128,2,49152\ns_add_u32 s78, s78, s55\nSa 112,1,16,78,0\nMvav 52,148,236\ns_add_u32 s46, s86, s88\nEX 80,44\nMvav 52,152,244\n"
"Ra 132,2,50176\nv_xor_b32 v24, 0x10000, v24\nCV 123,126,127\nAW 220,120\nMvav 52,156,252\nWL 6\nEX 81,45\nMvav 48,156,248\nRa 136,2,51200\nAD 85,85,80\nAW 221,121\nAW 222,122\n"
"Mvav 48,152,240\nD1 1,16,46,0\nAW 223,123\nv_mov_b32 v112, -1\nMvav 48,148,232\nRa 140,2,52224\nv_mov_b32 v113, -1\nv_mov_b32 v114, -1\nv_mov_b32 v115, -1\nMvav 48,144,224\n"
"AD 85,85,81\nCV 70,80,81\nCvav 56,160,192,12\nRa 144,2,53248\nSa 116,1,16,78,1024\nMvav 56,164,200\nEX 82,46\nMvav 56,168,208\nRa 148,2,54272\nv_xor_b32 v26, 0x10000, v26\n"
"Mvav 56,172,216\nWL 6\nEX 83,47\nCvav 60,172,220,16\nRa 152,2,55296\nAD 85,85,82\nMvav 60,168,212\nD1 1,16,46,1024\nMvav 60,164,204\nRa 156,2,56320\nMvav 60,160,196\nAD 85,85,83\n"
"CV 71,82,83\nMvav 60,176,228\nRa 160,2,57344\nv_max_f32 v90, v84, v85\nMvav 60,180,236\ns_add_u32 s88, s88, 32768\nEX 80,48\nMvav 60,184,244\nRa 164,2,58368\n"
"v_cmp_lt_f32 vcc, s63, v90\nMvav 60,188,252\nWL 8\nEX 81,49\nMvav 56,188,248\nRa 168,2,59392\ns_cbranch_vccnz .T443\n.T444:\nv_add_f32 v20, s93, v84\nv_mov_b32 v21, v85\n"
"v_mov_b32 v22, v20\nv_mov_b32 v23, v21\nMvav 56,184,240\nD1 1,16,46,2048\nv_permlane32_swap_b32 v20, v22\nv_permlane32_swap_b32 v21, v23\nAD 20,20,22\nAD 21,21,23\n"
"v_mov_b32 v22, v20\nv_mov_b32 v23, v21\nMvav 56,180,232\nRa 172,2,60416\nv_permlane16_swap_b32 v20, v22\nv_permlane16_swap_b32 v21, v23\nAD 20,20,22\nAD 21,21,23\n"
"v_min_f32 v90, v20, v21\nv_cmp_gt_f32 vcc, s64, v90\ns_nop 0\ns_cbranch_vccnz .T445\n.T446:\nAD 86,86,84\nMvav 56,176,224\nAD 84,80,81\nCV 72,80,81\nZaav 0,128,64\nRa 176,2,61440\n"
"Sa 120,1,16,78,2048\nZaav 4,128,68\nEX 82,50\nZaav 12,132,68\nRa 180,2,62464\nZaav 8,132,64\nWL 8\nEX 83,51\nZaav 16,136,64\nRa 184,2,63488\nAD 84,84,82\nZaav 20,136,68\n"
"D1 1,16,46,3072\ns_add_u32 m0, s89, 16384\nZaav 28,140,68\nRa 188,2,64512\ns_barrier\nZaav 24,140,64\nAD 84,84,83\nCV 73,82,83\nZaav 32,144,64\nRa 128,24,0\nSa 124,1,16,78,3072\n"
"Zaav 36,144,68\ns_add_u32 s46, s87, s88\nEX 80,52\nZaav 44,148,68\nRa 132,24,1024\nv_xor_b32 v2, 0x10000, v2\nZaav 40,148,64\nWL 8\nEX 81,53\nZaav 48,152,64\nRa 136,24,2048\n"
"AD 87,87,85\nZaav 52,152,68\nD1 1,16,46,0\nZaav 60,156,68\nRa 140,24,3072\nZaav 56,156,64\nAD 85,80,81\nCV 76,80,81\nZaav 64,160,64\nRa 144,26,0\nZaav 68,160,68\n"
"s_cmp_lt_u32 s99, s92\nEX 82,54\nZaav 76,164,68\nRa 148,26,1024\ns_cbranch_scc1 .E439\n.E440:\nZaav 72,164,64\nWL 8\nEX 83,55\nZaav 80,168,64\nRa 152,26,2048\nAD 85,85,82\n"
"Zaav 84,168,68\nD1 1,16,46,1024\nZaav 92,172,68\nRa 156,26,3072\nZaav 88,172,64\nAD 85,85,83\nCV 77,82,83\nZaav 96,176,64\nRa 160,24,8192\nZaav 100,176,68\ns_sub_u32 s90, s90, 1\n"
"EX 80,56\nZaav 108,180,68\nRa 164,24,9216\ns_cbranch_scc1 .E441\n.E442:\nZaav 104,180,64\nWL 6\nEX 81,57\nZaav 112,184,64\nRa 168,24,10240\nAD 84,84,80\nv_and_b32 v27, 0x70, v1\n"
"buffer_load_dword v27, v27, s[16:19], s61 offen offset:140 sc0 sc1\nZaav 116,184,68\nD1 1,16,46,2048\nZaav 124,188,68\nRa 172,24,11264\nZaav 120,188,64\nAD 84,84,81\nCV 74,80,81\n"
"s_sub_u32 s99, s99, 1\ns_cmp_eq_u32 s13, 7\ns_cbranch_scc1 .SW434\n.p2align 6\n.A471:\nCvav 32,128,192,4\nRa 176,26,8192\nMvav 32,132,200\nEX 82,58\nMvav 32,136,208\n"
"Ra 180,26,9216\nMvav 32,140,216\nWL 6\nEX 83,59\nCvav 36,140,220,8\nRa 184,26,10240\nAD 84,84,82\nMvav 36,136,212\nD1 1,16,46,3072\ns_add_u32 m0, s89, 32768\nMvav 36,132,204\n"
"Ra 188,26,11264\nMvav 36,128,196\nAD 84,84,83\nCV 75,82,83\nMvav 36,144,228\nRa 128,2,16384\nMvav 36,148,236\ns_add_u32 s46, s86, s88\nEX 80,60\nMvav 36,152,244\nRa 132,2,17408\n"
"Mvav 36,156,252\nWL 6\nEX 81,61\nMvav 32,156,248\nRa 136,2,18432\nAD 85,85,80\nMvav 32,152,240\nD1 1,16,46,0\nMvav 32,148,232\nRa 140,2,19456\nSv 112,1,16,49,0\nMvav 32,144,224\n"
"AD 85,85,81\nCV 78,80,81\nCvav 40,160,192,12\nRa 144,2,20480\nMvav 40,164,200\nEX 82,62\nMvav 40,168,208\nRa 148,2,21504\nMvav 40,172,216\nWL 6\nEX 83,63\nCvav 44,172,220,16\n"
"Ra 152,2,22528\nAD 85,85,82\nMvav 44,168,212\nD1 1,16,46,1024\nMvav 44,164,204\nRa 156,2,23552\nMvav 44,160,196\nAD 85,85,83\nCV 79,82,83\nMvav 44,176,228\nRa 160,2,24576\n"
"v_max_f32 v90, v84, v85\nMvav 44,180,236\ns_add_u32 s88, s88, 32768\nEX 80,32\nMvav 44,184,244\nRa 164,2,25600\nv_cmp_lt_f32 vcc, s63, v90\nMvav 44,188,252\nWL 8\nEX 81,33\n"
"Mvav 40,188,248\nRa 168,2,26624\ns_cbranch_vccnz .T479\n.T480:\nMvav 40,184,240\nD1 1,16,46,2048\nMvav 40,180,232\nRa 172,2,27648\nAD 86,86,84\nMvav 40,176,224\nAD 84,80,81\n"
"CV 64,80,81\nMaav 0,128,72\nRa 176,2,28672\nMaav 4,128,76\nEX 82,34\nMaav 12,132,76\nRa 180,2,29696\nMaav 8,132,72\ns_waitcnt vmcnt(13) lgkmcnt(8)\nEX 83,35\nMaav 16,136,72\n"
"Ra 184,2,30720\nAD 84,84,82\nMaav 20,136,76\nD1 1,16,46,3072\ns_add_u32 m0, s89, 49152\nMaav 28,140,76\nRa 188,2,31744\ns_barrier\nMaav 24,140,72\nAD 84,84,83\nCV 65,82,83\n"
"Maav 32,144,72\nRa 128,24,32768\nMaav 36,144,76\ns_add_u32 s46, s87, s88\nEX 80,36\nMaav 44,148,76\nRa 132,24,33792\nMaav 40,148,72\nWL 8\nEX 81,37\nMaav 48,152,72\n"
"Ra 136,24,34816\nAD 87,87,85\nMaav 52,152,76\nD1 1,16,46,0\nMaav 60,156,76\nRa 140,24,35840\nMaav 56,156,72\nAD 85,80,81\nCV 68,80,81\nMaav 64,160,72\nRa 144,26,32768\n"
"Maav 68,160,76\ns_cmp_eq_u32 s99, 0\nEX 82,38\nMaav 76,164,76\nRa 148,26,33792\ns_cbranch_scc1 .E473\n.E474:\nMaav 72,164,72\nWL 8\nEX 83,39\nMaav 80,168,72\nRa 152,26,34816\n"
"AD 85,85,82\nMaav 84,168,76\nD1 1,16,46,1024\nMaav 92,172,76\nRa 156,26,35840\nMaav 88,172,72\nAD 85,85,83\nCV 69,82,83\nMaav 96,176,72\nRa 160,24,40960\nMaav 100,176,76\n"
"s_xor_b32 s89, s89, 0x10000\nEX 80,40\nMaav 108,180,76\nRa 164,24,41984\nMaav 104,180,72\nWL 6\nEX 81,41\nMaav 112,184,72\nRa 168,24,43008\nAD 84,84,80\nMaav 116,184,76\n"
"D1 1,16,46,2048\nMaav 124,188,76\nRa 172,24,44032\nMaav 120,188,72\nAD 84,84,81\nCV 66,80,81\nCvav 48,128,192,4\nRa 176,26,40960\nMvav 48,132,200\ns_cmp_eq_u32 s99, 0\nEX 82,42\n"
"Mvav 48,136,208\nRa 180,26,41984\ns_cselect_b32 s86, s94, s86\nMvav 48,140,216\nWL 6\nEX 83,43\nCvav 52,140,220,8\nRa 184,26,43008\nAD 84,84,82\nMvav 52,136,212\nD1 1,16,46,3072\n"
"s_add_u32 m0, s89, 0\nMvav 52,132,204\nRa 188,26,44032\nMvav 52,128,196\nAD 84,84,83\nCV 67,82,83\nMvav 52,144,228\nRa 128,2,49152\nMvav 52,148,236\ns_add_u32 s46, s86, s88\n"
"EX 80,44\nMvav 52,152,244\nRa 132,2,50176\nv_xor_b32 v24, 0x10000, v24\nMvav 52,156,252\nWL 6\nEX 81,45\nMvav 48,156,248\nRa 136,2,51200\nAD 85,85,80\nMvav 48,152,240\n"
"D1 1,16,46,0\nMvav 48,148,232\nRa 140,2,52224\nSv 112,1,16,49,1024\nMvav 48,144,224\nAD 85,85,81\nCV 70,80,81\nCvav 56,160,192,12\nRa 144,2,53248\nMvav 56,164,200\n"
"s_add_u32 s49, s49, 131072\nEX 82,46\nMvav 56,168,208\nRa 148,2,54272\nv_xor_b32 v26, 0x10000, v26\nMvav 56,172,216\nWL 6\nEX 83,47\nCvav 60,172,220,16\nRa 152,2,55296\n"
"AD 85,85,82\nMvav 60,168,212\nD1 1,16,46,1024\nMvav 60,164,204\nRa 156,2,56320\nMvav 60,160,196\nAD 85,85,83\nCV 71,82,83\nMvav 60,176,228\nRa 160,2,57344\n"
"v_max_f32 v90, v84, v85\nMvav 60,180,236\ns_add_u32 s88, s88, 32768\nEX 80,48\nMvav 60,184,244\nRa 164,2,58368\nv_cmp_lt_f32 vcc, s63, v90\nMvav 60,188,252\nWL 8\nEX 81,49\n"
"Mvav 56,188,248\nRa 168,2,59392\ns_cbranch_vccnz .T481\n.T482:\nMvav 56,184,240\nD1 1,16,46,2048\nMvav 56,180,232\nRa 172,2,60416\nAD 86,86,84\nMvav 56,176,224\nAD 84,80,81\n"
"CV 72,80,81\nMaav 0,128,64\nRa 176,2,61440\nMaav 4,128,68\nEX 82,50\nMaav 12,132,68\nRa 180,2,62464\nMaav 8,132,64\ns_waitcnt vmcnt(13) lgkmcnt(8)\nEX 83,51\nMaav 16,136,64\n"
"Ra 184,2,63488\nAD 84,84,82\nMaav 20,136,68\nD1 1,16,46,3072\ns_add_u32 m0, s89, 16384\nMaav 28,140,68\nRa 188,2,64512\ns_barrier\nMaav 24,140,64\nAD 84,84,83\nCV 73,82,83\n"
"Maav 32,144,64\nRa 128,24,0\nMaav 36,144,68\ns_add_u32 s46, s87, s88\nEX 80,52\nMaav 44,148,68\nRa 132,24,1024\nv_xor_b32 v2, 0x10000, v2\nMaav 40,148,64\nWL 8\nEX 81,53\n"
"Maav 48,152,64\nRa 136,24,2048\nAD 87,87,85\nMaav 52,152,68\nD1 1,16,46,0\nMaav 60,156,68\nRa 140,24,3072\nMaav 56,156,64\nAD 85,80,81\nCV 76,80,81\nMaav 64,160,64\nRa 144,26,0\n"
"Maav 68,160,68\ns_cmp_lt_u32 s99, s92\nEX 82,54\nMaav 76,164,68\nRa 148,26,1024\ns_cbranch_scc1 .E475\n.E476:\nMaav 72,164,64\nWL 8\nEX 83,55\nMaav 80,168,64\nRa 152,26,2048\n"
"AD 85,85,82\nMaav 84,168,68\nD1 1,16,46,1024\nMaav 92,172,68\nRa 156,26,3072\nMaav 88,172,64\nAD 85,85,83\nCV 77,82,83\nMaav 96,176,64\nRa 160,24,8192\nMaav 100,176,68\n"
"s_sub_u32 s90, s90, 1\nEX 80,56\nMaav 108,180,68\nRa 164,24,9216\ns_cbranch_scc1 .E477\n.E478:\nMaav 104,180,64\nWL 6\nEX 81,57\nMaav 112,184,64\nRa 168,24,10240\nAD 84,84,80\n"
"Maav 116,184,68\nD1 1,16,46,2048\ns_sub_u32 s99, s99, 1\nMaav 124,188,68\nRa 172,24,11264\nMaav 120,188,64\nAD 84,84,81\nCV 74,80,81\ns_cbranch_scc0 .A471\ns_branch .A483\n.E473:\n"
"v_cmpx_gt_i32 vcc, 19, v0\nv_mov_b32 v15, s97\nv_cmpx_gt_i32 vcc, 18, v0\nv_mov_b32 v14, s97\nv_cmpx_gt_i32 vcc, 17, v0\nv_mov_b32 v13, s97\nv_cmpx_gt_i32 vcc, 16, v0\n"
"v_mov_b32 v12, s97\nv_cmpx_gt_i32 vcc, 3, v0\nv_mov_b32 v7, s97\nv_cmpx_gt_i32 vcc, 2, v0\nv_mov_b32 v6, s97\nv_cmpx_gt_i32 vcc, 1, v0\nv_mov_b32 v5, s97\n"
"v_cmpx_gt_i32 vcc, 0, v0\nv_mov_b32 v4, s97\ns_mov_b64 exec, -1\ns_nop 2\ns_branch .E474\n.E475:\ns_cmp_eq_u32 s99, 0\ns_cbranch_scc1 .K472\nv_cmpx_gt_i32 vcc, 19, v3\n"
"v_mov_b32 v15, s97\nv_cmpx_gt_i32 vcc, 18, v3\nv_mov_b32 v14, s97\nv_cmpx_gt_i32 vcc, 17, v3\nv_mov_b32 v13, s97\nv_cmpx_gt_i32 vcc, 16, v3\nv_mov_b32 v12, s97\n"
"v_cmpx_gt_i32 vcc, 3, v3\nv_mov_b32 v7, s97\nv_cmpx_gt_i32 vcc, 2, v3\nv_mov_b32 v6, s97\nv_cmpx_gt_i32 vcc, 1, v3\nv_mov_b32 v5, s97\nv_cmpx_gt_i32 vcc, 0, v3\nv_mov_b32 v4, s97\n"
"s_mov_b64 exec, -1\ns_nop 2\ns_branch .E476\n.K472:\nv_cmpx_gt_i32 vcc, 19, v3\nv_mov_b32 v19, s97\nv_cmpx_gt_i32 vcc, 18, v3\nv_mov_b32 v18, s97\nv_cmpx_gt_i32 vcc, 17, v3\n"
"v_mov_b32 v17, s97\nv_cmpx_gt_i32 vcc, 16, v3\nv_mov_b32 v16, s97\nv_cmpx_gt_i32 vcc, 3, v3\nv_mov_b32 v11, s97\nv_cmpx_gt_i32 vcc, 2, v3\nv_mov_b32 v10, s97\n"
"v_cmpx_gt_i32 vcc, 1, v3\nv_mov_b32 v9, s97\nv_cmpx_gt_i32 vcc, 0, v3\nv_mov_b32 v8, s97\ns_mov_b64 exec, -1\ns_nop 2\ns_branch .E476\n.E477:\ns_swappc_b64 s[80:81], s[8:9]\n"
"s_branch .E478\n.HD429:\nv_cmp_ne_u32 vcc, s15, v27\ns_nop 0\ns_cbranch_vccnz .S459\n.S460:\nbuffer_store_dword v25, off, s[16:19], s54 offset:4 sc0 sc1\nv_and_b32 v27, 0x70, v1\n"
"buffer_load_dword v27, v27, s[16:19], s61 offen offset:12 sc0 sc1\ns_mov_b32 s90, 1\ns_swappc_b64 s[8:9], s[80:81]\nv_cmp_ne_u32 vcc, s15, v27\ns_nop 0\ns_cbranch_vccnz .S462\n"
".S463:\nv_and_b32 v27, 0x70, v1\nbuffer_load_dword v27, v27, s[16:19], s61 offen offset:8 sc0 sc1\ns_mov_b32 s90, 1\ns_swappc_b64 s[8:9], s[80:81]\nv_cmp_ne_u32 vcc, s15, v27\n"
"s_nop 0\ns_cbranch_vccnz .S465\n.S466:\nv_and_b32 v27, 0xf0, v1\nbuffer_load_dword v27, v27, s[16:19], s61 offen offset:4 sc0 sc1\ns_mov_b32 s90, 1\ns_swappc_b64 s[8:9], s[80:81]\n"
"v_cmp_ne_u32 vcc, s15, v27\ns_nop 0\ns_cbranch_vccnz .S468\n.S469:\ns_mov_b32 s90, 100\ns_swappc_b64 s[8:9], s[80:81]\n.A483:\nRa 176,26,8192\nEX 82,58\nRa 180,26,9216\nWL 6\n"
"EX 83,59\nCvav 36,140,220,8\nRa 184,26,10240\nAD 84,84,82\nMvav 36,136,212\nD1 1,16,46,3072\ns_add_u32 m0, s89, 32768\nMvav 36,132,204\nRa 188,26,11264\nMvav 36,128,196\n"
"AD 84,84,83\nCV 75,82,83\nMvav 36,144,228\nRa 128,2,16384\nMvav 36,148,236\ns_add_u32 s46, s86, s88\nEX 80,60\nMvav 36,152,244\nRa 132,2,17408\nMvav 36,156,252\nEX 81,61\n"
"Ra 136,2,18432\nAD 85,85,80\nD1 1,16,46,0\nRa 140,2,19456\nSv 112,1,16,49,0\nAD 85,85,81\nCV 78,80,81\nRa 144,2,20480\ns_mov_b32 s87, s95\nEX 82,62\nRa 148,2,21504\nWL 6\n"
"EX 83,63\nCvav 44,172,220,16\nRa 152,2,22528\nAD 85,85,82\nMvav 44,168,212\nD1 1,16,46,1024\nMvav 44,164,204\nRa 156,2,23552\nMvav 44,160,196\nAD 85,85,83\nCV 79,82,83\n"
"Mvav 44,176,228\nRa 160,2,24576\nv_max_f32 v90, v84, v85\nMvav 44,180,236\ns_add_u32 s88, s88, 32768\nMvav 44,184,244\nRa 164,2,25600\nv_cmp_lt_f32 vcc, s63, v90\nMvav 44,188,252\n"
"WL 8\nRa 168,2,26624\ns_cbranch_vccnz .T487\n.T488:\nD1 1,16,46,2048\nRa 172,2,27648\nAD 86,86,84\nMaav 0,128,72\nRa 176,2,28672\nMaav 4,128,76\nMaav 12,132,76\nRa 180,2,29696\n"
"Maav 8,132,72\ns_waitcnt vmcnt(13) lgkmcnt(8)\nMaav 16,136,72\nRa 184,2,30720\nMaav 20,136,76\nD1 1,16,46,3072\ns_add_u32 m0, s89, 49152\nMaav 28,140,76\nRa 188,2,31744\n"
"s_barrier\nMaav 24,140,72\nMaav 32,144,72\nRa 128,24,32768\nMaav 36,144,76\ns_add_u32 s46, s87, s88\nEX 80,36\nMaav 44,148,76\nRa 132,24,33792\nMaav 40,148,72\nWL 8\nEX 81,37\n"
"Maav 48,152,72\nRa 136,24,34816\nAD 87,87,85\nMaav 52,152,76\nD1 1,16,46,0\nMaav 60,156,76\nRa 140,24,35840\nMaav 56,156,72\nAD 85,80,81\nCV 68,80,81\nMaav 64,160,72\n"
"Ra 144,26,32768\nMaav 68,160,76\ns_nop 0\nEX 82,38\nMaav 76,164,76\nRa 148,26,33792\nv_cmpx_gt_i32 vcc, 19, v0\nv_mov_b32 v19, s97\nv_cmpx_gt_i32 vcc, 18, v0\nv_mov_b32 v18, s97\n"
"v_cmpx_gt_i32 vcc, 17, v0\nv_mov_b32 v17, s97\nv_cmpx_gt_i32 vcc, 16, v0\nv_mov_b32 v16, s97\nv_cmpx_gt_i32 vcc, 3, v0\nv_mov_b32 v11, s97\nv_cmpx_gt_i32 vcc, 2, v0\n"
"v_mov_b32 v10, s97\nv_cmpx_gt_i32 vcc, 1, v0\nv_mov_b32 v9, s97\nv_cmpx_gt_i32 vcc, 0, v0\nv_mov_b32 v8, s97\ns_mov_b64 exec, -1\ns_nop 2\ns_nop 0\nMaav 72,164,72\nWL 8\nEX 83,39\n"
"Maav 80,168,72\nRa 152,26,34816\nAD 85,85,82\nMaav 84,168,76\nD1 1,16,46,1024\nMaav 92,172,76\nRa 156,26,35840\nMaav 88,172,72\nAD 85,85,83\nCV 69,82,83\nMaav 96,176,72\n"
"Ra 160,24,40960\nMaav 100,176,76\ns_xor_b32 s89, s89, 0x10000\nMaav 108,180,76\nRa 164,24,41984\nMaav 104,180,72\nWL 10\nMaav 112,184,72\nRa 168,24,43008\nMaav 116,184,76\n"
"D1 1,16,46,2048\nMaav 124,188,76\nRa 172,24,44032\nMaav 120,188,72\nRa 176,26,40960\nRa 180,26,41984\nWL 6\nCvav 52,140,220,8\nRa 184,26,43008\nMvav 52,136,212\nD1 1,16,46,3072\n"
"s_add_u32 m0, s89, 0\nMvav 52,132,204\nRa 188,26,44032\nMvav 52,128,196\nMvav 52,144,228\nRa 128,2,49152\nMvav 52,148,236\ns_add_u32 s46, s86, s88\nEX 80,44\nMvav 52,152,244\n"
"Ra 132,2,50176\nv_xor_b32 v24, 0x10000, v24\nMvav 52,156,252\nEX 81,45\nRa 136,2,51200\nAD 85,85,80\nD1 1,16,46,0\nRa 140,2,52224\nSv 112,1,16,49,1024\nAD 85,85,81\nCV 70,80,81\n"
"Ra 144,2,53248\ns_add_u32 s49, s49, 131072\nEX 82,46\nRa 148,2,54272\nv_xor_b32 v26, 0x10000, v26\nWL 6\nEX 83,47\nCvav 60,172,220,16\nRa 152,2,55296\nAD 85,85,82\n"
"Mvav 60,168,212\nD1 1,16,46,1024\nMvav 60,164,204\nRa 156,2,56320\nMvav 60,160,196\nAD 85,85,83\nCV 71,82,83\nMvav 60,176,228\nRa 160,2,57344\nMvav 60,180,236\n"
"s_add_u32 s88, s88, 32768\nMvav 60,184,244\nRa 164,2,58368\nv_cmp_lt_f32 vcc, s63, v85\nMvav 60,188,252\nWL 8\nRa 168,2,59392\ns_cbranch_vccnz .T489\n.T490:\nD1 1,16,46,2048\n"
"Ra 172,2,60416\nRa 176,2,61440\nMaav 4,128,68\nMaav 12,132,68\nRa 180,2,62464\ns_waitcnt vmcnt(13) lgkmcnt(8)\nRa 184,2,63488\nMaav 20,136,68\nD1 1,16,46,3072\n"
"s_add_u32 m0, s89, 16384\nMaav 28,140,68\nRa 188,2,64512\ns_barrier\nRa 128,24,0\nMaav 36,144,68\ns_add_u32 s46, s87, s88\nEX 80,52\nMaav 44,148,68\nRa 132,24,1024\n"
"v_xor_b32 v2, 0x10000, v2\nWL 8\nEX 81,53\nRa 136,24,2048\nAD 87,87,85\nMaav 52,152,68\nD1 1,16,46,0\nMaav 60,156,68\nRa 140,24,3072\nAD 85,80,81\nCV 76,80,81\nRa 144,26,0\n"
"Maav 68,160,68\nEX 82,54\nMaav 76,164,68\nRa 148,26,1024\nWL 8\nEX 83,55\nRa 152,26,2048\nAD 85,85,82\nMaav 84,168,68\nD1 1,16,46,1024\nMaav 92,172,68\nRa 156,26,3072\n"
"AD 85,85,83\nCV 77,82,83\nRa 160,24,8192\nMaav 100,176,68\ns_sub_u32 s90, s90, 1\nMaav 108,180,68\nRa 164,24,9216\ns_cbranch_scc1 .E485\n.E486:\nWL 6\nRa 168,24,10240\n"
"Maav 116,184,68\nD1 1,16,46,2048\nMaav 124,188,68\nRa 172,24,11264\n.SW434:\nv_mov_b32 v4, 0\nv_mov_b32 v5, 0\nv_mov_b32 v6, 0\nv_mov_b32 v7, 0\nv_mov_b32 v8, 0\nv_mov_b32 v9, 0\n"
"v_mov_b32 v10, 0\nv_mov_b32 v11, 0\nv_mov_b32 v12, 0\nv_mov_b32 v13, 0\nv_mov_b32 v14, 0\nv_mov_b32 v15, 0\nv_mov_b32 v16, 0\nv_mov_b32 v17, 0\nv_mov_b32 v18, 0\nv_mov_b32 v19, 0\n"
"s_add_u32 s99, s98, 1\ns_mov_b32 s94, s96\ns_add_u32 s95, s96, 65536\ns_lshl2_add_u32 s2, s13, s11\ns_cmp_lt_u32 s2, 2\ns_cbranch_scc1 .E491\n.E492:\nCvaa 32,128,192,4\n"
"Ra 176,26,8192\nMvaa 32,132,200\nMvaa 32,136,208\nRa 180,26,9216\nMvaa 32,140,216\nWL 6\nCvaa 36,140,220,8\nRa 184,26,10240\nMvaa 36,136,212\nD1 1,16,46,3072\n"
"s_add_u32 m0, s89, 32768\nMvaa 36,132,204\nRa 188,26,11264\nMvaa 36,128,196\nMvaa 36,144,228\nRa 128,2,16384\nMvaa 36,148,236\ns_add_u32 s46, s86, s88\nEX 80,60\nMvaa 36,152,244\n"
"Ra 132,2,17408\nMvaa 36,156,252\nWL 6\nEX 81,61\nMvaa 32,156,248\nRa 136,2,18432\nAD 85,85,80\nMvaa 32,152,240\nD1 1,16,46,0\nMvaa 32,148,232\nRa 140,2,19456\nMvaa 32,144,224\n"
"AD 85,85,81\nCV 78,80,81\nCvaa 40,160,192,12\nRa 144,2,20480\nMvaa 40,164,200\nEX 82,62\nMvaa 40,168,208\nRa 148,2,21504\nMvaa 40,172,216\nWL 6\nEX 83,63\nCvaa 44,172,220,16\n"
"Ra 152,2,22528\nAD 85,85,82\nMvaa 44,168,212\nD1 1,16,46,1024\nMvaa 44,164,204\nRa 156,2,23552\nMvaa 44,160,196\nAD 85,85,83\nCV 79,82,83\nMvaa 44,176,228\nRa 160,2,24576\n"
"Mvaa 44,180,236\ns_add_u32 s88, s88, 32768\nEX 80,32\nMvaa 44,184,244\nRa 164,2,25600\nv_cmp_lt_f32 vcc, s63, v85\nMvaa 44,188,252\nWL 8\nEX 81,33\nMvaa 40,188,248\n"
"Ra 168,2,26624\ns_cbranch_vccnz .T500\n.T501:\nMvaa 40,184,240\nD1 1,16,46,2048\nMvaa 40,180,232\nRa 172,2,27648\nMvaa 40,176,224\nAD 84,80,81\nCV 64,80,81\nRa 176,2,28672\n"
"Maav 4,128,76\nEX 82,34\nMaav 12,132,76\nRa 180,2,29696\ns_waitcnt vmcnt(12) lgkmcnt(8)\nEX 83,35\nRa 184,2,30720\nAD 84,84,82\nMaav 20,136,76\nD1 1,16,46,3072\n"
"s_add_u32 m0, s89, 49152\nMaav 28,140,76\nRa 188,2,31744\ns_barrier\nAR 98,0\nAR 99,1\nCV 100,98,99\nAD 84,84,83\nCV 65,82,83\nAR 98,2\nAR 99,3\nCV 101,98,99\nRa 128,24,32768\n"
"AR 98,8\nAR 99,9\nCV 102,98,99\nAR 98,10\nMaav 36,144,76\ns_add_u32 s46, s87, s88\nEX 80,36\nAR 99,11\nMaav 44,148,76\nRa 132,24,33792\nCV 103,98,99\ns_add_u32 s5, s66, 0\n"
"Sv 100,1,16,5,0\nAR 98,4\nWL 8\nEX 81,37\nAR 99,5\nCV 104,98,99\nRa 136,24,34816\nAD 87,87,85\nAR 98,6\nAR 99,7\nCV 105,98,99\nMaav 52,152,76\nD1 1,16,46,0\nv_mov_b32 v96, v86\n"
"v_mov_b32 v97, v87\nAR 98,12\nAR 99,13\nMaav 60,156,76\nRa 140,24,35840\nv_permlane32_swap_b32 v86, v96\nCV 106,98,99\nAR 98,14\nAR 99,15\nAD 85,80,81\nCV 68,80,81\nCV 107,98,99\n"
"s_add_u32 s5, s66, 0\nSv 104,1,16,5,1024\nRa 144,26,32768\nv_permlane32_swap_b32 v87, v97\nAD 86,86,96\nAR 98,16\nAR 99,17\nMaav 68,160,76\ns_cmp_eq_u32 s99, 0\nEX 82,38\n"
"Maav 76,164,76\nRa 148,26,33792\ns_cbranch_scc1 .E494\n.E495:\nCV 108,98,99\nAR 98,18\nAR 99,19\nWL 8\nEX 83,39\nCV 109,98,99\nAR 98,24\nRa 152,26,34816\nAD 85,85,82\nAD 87,87,97\n"
"AR 99,25\nCV 110,98,99\nMaav 84,168,76\nD1 1,16,46,1024\nv_mov_b32 v96, v86\nAR 98,26\nAR 99,27\nCV 111,98,99\nMaav 92,172,76\nRa 156,26,35840\nv_mov_b32 v97, v87\n"
"s_add_u32 s5, s66, 0\nSv 108,1,16,5,2048\nAR 98,20\nAD 85,85,83\nCV 69,82,83\nv_permlane16_swap_b32 v86, v96\nAR 99,21\nCV 100,98,99\nRa 160,24,40960\n"
"v_permlane16_swap_b32 v87, v97\nAR 98,22\nAR 99,23\nCV 101,98,99\nMaav 100,176,76\ns_xor_b32 s89, s89, 0x10000\nEX 80,40\nAR 98,28\nMaav 108,180,76\nRa 164,24,41984\nAD 86,86,96\n"
"AD 87,87,97\nAR 99,29\nCV 102,98,99\nWL 6\nEX 81,41\nAR 98,30\nAR 99,31\nRa 168,24,43008\nAD 84,84,80\nCV 103,98,99\ns_add_u32 s5, s66, 0\nSv 100,1,16,5,3072\nMaav 116,184,76\n"
"D1 1,16,46,2048\nv_cmp_gt_u32 vcc, 0x100, v1\nv_mov_b32 v94, s15\nv_mov_b32 v95, s15\nv_cndmask_b32 v92, v89, v88, vcc\nv_cndmask_b32 v93, v87, v86, vcc\nMaav 124,188,76\n"
"Ra 172,24,44032\nv_mov_b32 v86, 0\nAR 98,32\nAR 99,33\nCV 104,98,99\nAD 84,84,81\nCV 66,80,81\nAR 98,34\nAR 99,35\nCV 105,98,99\nCvaa 48,128,192,4\nRa 176,26,40960\n"
"v_mov_b32 v87, 0\nv_mov_b32 v88, 0\nAR 98,40\nAR 99,41\nMvaa 48,132,200\ns_cmp_eq_u32 s99, 0\nEX 82,42\nMvaa 48,136,208\nRa 180,26,41984\ns_cselect_b32 s86, s94, s86\n"
"CV 106,98,99\nAR 98,42\nAR 99,43\nMvaa 48,140,216\nWL 6\nEX 83,43\nCV 107,98,99\nCvaa 52,140,220,8\nRa 184,26,43008\nAD 84,84,82\nv_mov_b32 v89, 0\ns_add_u32 s5, s66, 4096\n"
"Sv 104,1,16,5,0\nMvaa 52,136,212\nD1 1,16,46,3072\ns_add_u32 m0, s89, 0\nAR 98,36\nAR 99,37\nCV 108,98,99\nMvaa 52,132,204\nRa 188,26,44032\nAR 98,38\nAR 99,39\nCV 109,98,99\n"
"AR 98,44\nMvaa 52,128,196\nAD 84,84,83\nCV 67,82,83\nAR 99,45\nCV 110,98,99\nAR 98,46\nMvaa 52,144,228\nRa 128,2,49152\nAR 99,47\nCV 111,98,99\ns_add_u32 s5, s66, 4096\n"
"Sv 108,1,16,5,1024\nMvaa 52,148,236\ns_add_u32 s46, s86, s88\nEX 80,44\nAR 98,48\nMvaa 52,152,244\nRa 132,2,50176\nv_xor_b32 v24, 0x10000, v24\nAR 99,49\nCV 100,98,99\nAR 98,50\n"
"Mvaa 52,156,252\nWL 6\nEX 81,45\nAR 99,51\nCV 101,98,99\nMvaa 48,156,248\nRa 136,2,51200\nAD 85,85,80\nAR 98,56\nAR 99,57\nCV 102,98,99\nMvaa 48,152,240\nD1 1,16,46,0\nAR 98,58\n"
"AR 99,59\nCV 103,98,99\nMvaa 48,148,232\nRa 140,2,52224\ns_add_u32 s5, s66, 4096\nSv 100,1,16,5,2048\nAR 98,52\nAR 99,53\nMvaa 48,144,224\nAD 85,85,81\nCV 70,80,81\nCV 104,98,99\n"
"AR 98,54\nAR 99,55\nCvaa 56,160,192,12\nRa 144,2,53248\nCV 105,98,99\nAR 98,60\nAR 99,61\nCV 106,98,99\nMvaa 56,164,200\nEX 82,46\nAR 98,62\nAR 99,63\nMvaa 56,168,208\n"
"Ra 148,2,54272\nv_xor_b32 v26, 0x10000, v26\nCV 107,98,99\ns_add_u32 s5, s66, 4096\nSv 104,1,16,5,3072\nMvaa 56,172,216\nWL 6\nEX 83,47\nAR 98,64\nAR 99,65\nCvaa 60,172,220,16\n"
"Ra 152,2,55296\nAD 85,85,82\nCV 108,98,99\nAR 98,66\nAR 99,67\nMvaa 60,168,212\nD1 1,16,46,1024\nCV 109,98,99\nAR 98,72\nAR 99,73\nCV 110,98,99\nMvaa 60,164,204\nRa 156,2,56320\n"
"AR 98,74\nAR 99,75\nCV 111,98,99\nMvaa 60,160,196\nAD 85,85,83\nCV 71,82,83\ns_add_u32 s5, s66, 16384\nSv 108,1,16,5,0\nAR 98,68\nMvaa 60,176,228\nRa 160,2,57344\n"
"v_max_f32 v90, v84, v85\nAR 99,69\nCV 100,98,99\nAR 98,70\nMvaa 60,180,236\ns_add_u32 s88, s88, 32768\nEX 80,48\nAR 99,71\nMvaa 60,184,244\nRa 164,2,58368\n"
"v_cmp_lt_f32 vcc, s63, v90\nMvaa 60,188,252\nWL 8\nEX 81,49\nMvaa 56,188,248\nRa 168,2,59392\ns_cbranch_vccnz .T502\n.T503:\nv_mov_b32 v20, v84\nv_mov_b32 v21, v85\n"
"v_mov_b32 v22, v20\nv_mov_b32 v23, v21\nMvaa 56,184,240\nD1 1,16,46,2048\nv_permlane32_swap_b32 v20, v22\nv_permlane32_swap_b32 v21, v23\nAD 20,20,22\nAD 21,21,23\n"
"v_mov_b32 v22, v20\nv_mov_b32 v23, v21\nMvaa 56,180,232\nRa 172,2,60416\nv_permlane16_swap_b32 v20, v22\nv_permlane16_swap_b32 v21, v23\nAD 20,20,22\nAD 21,21,23\n"
"v_min_f32 v90, v20, v21\nv_cmp_gt_f32 vcc, s64, v90\ns_nop 0\ns_cbranch_vccnz .T504\n.T505:\nAD 86,86,84\nMvaa 56,176,224\nAD 84,80,81\nCV 72,80,81\nCV 101,98,99\nAR 98,76\n"
"AR 99,77\nZvav 128,128,64\nRa 176,2,61440\nCV 102,98,99\nAR 98,78\nAR 99,79\nCV 103,98,99\nZvav 132,128,68\nEX 82,50\ns_add_u32 s5, s66, 16384\nSv 100,1,16,5,1024\n"
"Zvav 140,132,68\nRa 180,2,62464\nAR 98,80\nAR 99,81\nCV 104,98,99\nAR 98,82\nZvav 136,132,64\ns_waitcnt vmcnt(21) lgkmcnt(8)\nEX 83,51\nAR 99,83\nCV 105,98,99\nZvav 144,136,64\n"
"Ra 184,2,63488\nAD 84,84,82\nAR 98,88\nAR 99,89\nCV 106,98,99\nZvav 148,136,68\nD1 1,16,46,3072\ns_add_u32 m0, s89, 16384\nAR 98,90\nAR 99,91\nCV 107,98,99\nZvav 156,140,68\n"
"Ra 188,2,64512\ns_barrier\ns_add_u32 s5, s66, 16384\nSv 104,1,16,5,2048\nAR 98,84\nZvav 152,140,64\nAD 84,84,83\nCV 73,82,83\nAR 99,85\nCV 108,98,99\nAR 98,86\nZvav 160,144,64\n"
"Ra 128,24,0\nAR 99,87\nCV 109,98,99\nAR 98,92\nAR 99,93\nZvav 164,144,68\ns_add_u32 s46, s87, s88\nEX 80,52\nCV 110,98,99\nZvav 172,148,68\nRa 132,24,1024\n"
"v_xor_b32 v2, 0x10000, v2\nAR 98,94\nAR 99,95\nCV 111,98,99\nZvav 168,148,64\nWL 8\nEX 81,53\ns_add_u32 s5, s66, 16384\nSv 108,1,16,5,3072\nZvav 176,152,64\nRa 136,24,2048\n"
"AD 87,87,85\nAR 98,96\nAR 99,97\nCV 100,98,99\nZvav 180,152,68\nD1 1,16,46,0\nAR 98,98\nAR 99,99\nCV 101,98,99\nAR 98,104\nZvav 188,156,68\nRa 140,24,3072\nAR 99,105\n"
"CV 102,98,99\nAR 98,106\nAR 99,107\nZvav 184,156,64\nAD 85,80,81\nCV 76,80,81\nCV 103,98,99\ns_add_u32 s5, s66, 20480\nSv 100,1,16,5,0\nZvav 192,160,64\nRa 144,26,0\nAR 98,100\n"
"AR 99,101\nCV 104,98,99\nAR 98,102\nZvav 196,160,68\ns_cmp_lt_u32 s99, s92\nEX 82,54\nZvav 204,164,68\nRa 148,26,1024\ns_cbranch_scc1 .E496\n.E497:\nAR 99,103\nCV 105,98,99\n"
"AR 98,108\nZvav 200,164,64\nWL 8\nEX 83,55\nAR 99,109\nCV 106,98,99\nZvav 208,168,64\nRa 152,26,2048\nAD 85,85,82\nAR 98,110\nAR 99,111\nCV 107,98,99\nZvav 212,168,68\n"
"D1 1,16,46,1024\ns_add_u32 s5, s66, 20480\nSv 104,1,16,5,1024\nAR 98,112\nAR 99,113\nZvav 220,172,68\nRa 156,26,3072\nCV 108,98,99\nAR 98,114\nAR 99,115\nCV 109,98,99\n"
"Zvav 216,172,64\nAD 85,85,83\nCV 77,82,83\nAR 98,120\nAR 99,121\nCV 110,98,99\nZvav 224,176,64\nRa 160,24,8192\nAR 98,122\nAR 99,123\nCV 111,98,99\nZvav 228,176,68\n"
"s_sub_u32 s90, s90, 1\nEX 80,56\nZvav 236,180,68\nRa 164,24,9216\ns_cbranch_scc1 .E498\n.E499:\ns_add_u32 s5, s66, 20480\nSv 108,1,16,5,2048\nAR 98,116\nZvav 232,180,64\nWL 6\n"
"EX 81,57\nAR 99,117\nCV 100,98,99\nZvav 240,184,64\nRa 168,24,10240\nAD 84,84,80\nAR 98,118\nAR 99,119\nCV 101,98,99\nZvav 244,184,68\nD1 1,16,46,2048\nAR 98,124\nAR 99,125\n"
"CV 102,98,99\nAR 98,126\nZvav 252,188,68\nRa 172,24,11264\nAR 99,127\nCV 103,98,99\ns_add_u32 s5, s66, 20480\nSv 100,1,16,5,3072\nZvav 248,188,64\nAD 84,84,81\nCV 74,80,81\n"
"s_sub_u32 s99, s99, 1\ns_cbranch_scc1 .LE435\n.p2align 6\n.A506:\nCvaa 32,128,192,4\nRa 176,26,8192\nMvaa 32,132,200\nEX 82,58\nMvaa 32,136,208\nRa 180,26,9216\nMvaa 32,140,216\n"
"WL 6\nEX 83,59\nCvaa 36,140,220,8\nRa 184,26,10240\nAD 84,84,82\nMvaa 36,136,212\nD1 1,16,46,3072\ns_add_u32 m0, s89, 32768\nMvaa 36,132,204\nRa 188,26,11264\nMvaa 36,128,196\n"
"AD 84,84,83\nCV 75,82,83\nMvaa 36,144,228\nRa 128,2,16384\nMvaa 36,148,236\ns_add_u32 s46, s86, s88\nEX 80,60\nMvaa 36,152,244\nRa 132,2,17408\nMvaa 36,156,252\nWL 6\nEX 81,61\n"
"Mvaa 32,156,248\nRa 136,2,18432\nAD 85,85,80\nMvaa 32,152,240\nD1 1,16,46,0\nMvaa 32,148,232\nRa 140,2,19456\nSv 112,1,16,49,0\nMvaa 32,144,224\nAD 85,85,81\nCV 78,80,81\n"
"Cvaa 40,160,192,12\nRa 144,2,20480\nMvaa 40,164,200\nEX 82,62\nMvaa 40,168,208\nRa 148,2,21504\nMvaa 40,172,216\nWL 6\nEX 83,63\nCvaa 44,172,220,16\nRa 152,2,22528\nAD 85,85,82\n"
"Mvaa 44,168,212\nD1 1,16,46,1024\nMvaa 44,164,204\nRa 156,2,23552\nMvaa 44,160,196\nAD 85,85,83\nCV 79,82,83\nMvaa 44,176,228\nRa 160,2,24576\nv_max_f32 v90, v84, v85\n"
"Mvaa 44,180,236\ns_add_u32 s88, s88, 32768\nEX 80,32\nMvaa 44,184,244\nRa 164,2,25600\nv_cmp_lt_f32 vcc, s63, v90\nMvaa 44,188,252\nWL 8\nEX 81,33\nMvaa 40,188,248\n"
"Ra 168,2,26624\ns_cbranch_vccnz .T516\n.T517:\nMvaa 40,184,240\nD1 1,16,46,2048\nMvaa 40,180,232\nRa 172,2,27648\nAD 86,86,84\nMvaa 40,176,224\nAD 84,80,81\nCV 64,80,81\n"
"Mvav 128,128,72\nRa 176,2,28672\nMvav 132,128,76\ns_cmp_eq_u32 s99, s98\nEX 82,34\nMvav 140,132,76\nRa 180,2,29696\ns_cbranch_scc1 .E514\n.E515:\nMvav 136,132,72\n"
"s_waitcnt vmcnt(13) lgkmcnt(8)\nEX 83,35\nMvav 144,136,72\nRa 184,2,30720\nAD 84,84,82\nMvav 148,136,76\nD1 1,16,46,3072\ns_add_u32 m0, s89, 49152\nMvav 156,140,76\n"
"Ra 188,2,31744\ns_barrier\nMvav 152,140,72\nAD 84,84,83\nCV 65,82,83\nMvav 160,144,72\nRa 128,24,32768\nMvav 164,144,76\ns_add_u32 s46, s87, s88\nEX 80,36\nMvav 172,148,76\n"
"Ra 132,24,33792\nMvav 168,148,72\nWL 8\nEX 81,37\nMvav 176,152,72\nRa 136,24,34816\nAD 87,87,85\nMvav 180,152,76\nD1 1,16,46,0\nMvav 188,156,76\nRa 140,24,35840\nMvav 184,156,72\n"
"AD 85,80,81\nCV 68,80,81\nMvav 192,160,72\nRa 144,26,32768\nMvav 196,160,76\ns_cmp_eq_u32 s99, 0\nEX 82,38\nMvav 204,164,76\nRa 148,26,33792\ns_cbranch_scc1 .E508\n.E509:\n"
"Mvav 200,164,72\nWL 8\nEX 83,39\nMvav 208,168,72\nRa 152,26,34816\nAD 85,85,82\nMvav 212,168,76\nD1 1,16,46,1024\nMvav 220,172,76\nRa 156,26,35840\nMvav 216,172,72\nAD 85,85,83\n"
"CV 69,82,83\nMvav 224,176,72\nRa 160,24,40960\nMvav 228,176,76\ns_xor_b32 s89, s89, 0x10000\nEX 80,40\nMvav 236,180,76\nRa 164,24,41984\nMvav 232,180,72\nWL 6\nEX 81,41\n"
"Mvav 240,184,72\nRa 168,24,43008\nAD 84,84,80\nMvav 244,184,76\nD1 1,16,46,2048\nMvav 252,188,76\nRa 172,24,44032\nMvav 248,188,72\nAD 84,84,81\nCV 66,80,81\nCvaa 48,128,192,4\n"
"Ra 176,26,40960\nMvaa 48,132,200\ns_cmp_eq_u32 s99, 0\nEX 82,42\nMvaa 48,136,208\nRa 180,26,41984\ns_cselect_b32 s86, s94, s86\nMvaa 48,140,216\nWL 6\nEX 83,43\nCvaa 52,140,220,8\n"
"Ra 184,26,43008\nAD 84,84,82\nMvaa 52,136,212\nD1 1,16,46,3072\ns_add_u32 m0, s89, 0\nMvaa 52,132,204\nRa 188,26,44032\nMvaa 52,128,196\nAD 84,84,83\nCV 67,82,83\nMvaa 52,144,228\n"
"Ra 128,2,49152\nMvaa 52,148,236\ns_add_u32 s46, s86, s88\nEX 80,44\nMvaa 52,152,244\nRa 132,2,50176\nv_xor_b32 v24, 0x10000, v24\nMvaa 52,156,252\nWL 6\nEX 81,45\nMvaa 48,156,248\n"
"Ra 136,2,51200\nAD 85,85,80\nMvaa 48,152,240\nD1 1,16,46,0\nMvaa 48,148,232\nRa 140,2,52224\nSv 112,1,16,49,1024\nMvaa 48,144,224\nAD 85,85,81\nCV 70,80,81\nCvaa 56,160,192,12\n"
"Ra 144,2,53248\nMvaa 56,164,200\ns_add_u32 s49, s49, 131072\nEX 82,46\nMvaa 56,168,208\nRa 148,2,54272\nv_xor_b32 v26, 0x10000, v26\nMvaa 56,172,216\nWL 6\nEX 83,47\n"
"Cvaa 60,172,220,16\nRa 152,2,55296\nAD 85,85,82\nMvaa 60,168,212\nD1 1,16,46,1024\nMvaa 60,164,204\nRa 156,2,56320\nMvaa 60,160,196\nAD 85,85,83\nCV 71,82,83\nMvaa 60,176,228\n"
"Ra 160,2,57344\nv_max_f32 v90, v84, v85\nMvaa 60,180,236\ns_add_u32 s88, s88, 32768\nEX 80,48\nMvaa 60,184,244\nRa 164,2,58368\nv_cmp_lt_f32 vcc, s63, v90\nMvaa 60,188,252\nWL 8\n"
"EX 81,49\nMvaa 56,188,248\nRa 168,2,59392\ns_cbranch_vccnz .T518\n.T519:\nMvaa 56,184,240\nD1 1,16,46,2048\nMvaa 56,180,232\nRa 172,2,60416\nAD 86,86,84\nMvaa 56,176,224\n"
"AD 84,80,81\nCV 72,80,81\nMvav 128,128,64\nRa 176,2,61440\nMvav 132,128,68\nEX 82,50\nMvav 140,132,68\nRa 180,2,62464\nMvav 136,132,64\ns_waitcnt vmcnt(13) lgkmcnt(8)\nEX 83,51\n"
"Mvav 144,136,64\nRa 184,2,63488\nAD 84,84,82\nMvav 148,136,68\nD1 1,16,46,3072\ns_add_u32 m0, s89, 16384\nMvav 156,140,68\nRa 188,2,64512\ns_barrier\nMvav 152,140,64\nAD 84,84,83\n"
"CV 73,82,83\nMvav 160,144,64\nRa 128,24,0\nMvav 164,144,68\ns_add_u32 s46, s87, s88\nEX 80,52\nMvav 172,148,68\nRa 132,24,1024\nv_xor_b32 v2, 0x10000, v2\nMvav 168,148,64\nWL 8\n"
"EX 81,53\nMvav 176,152,64\nRa 136,24,2048\nAD 87,87,85\nMvav 180,152,68\nD1 1,16,46,0\nMvav 188,156,68\nRa 140,24,3072\nMvav 184,156,64\nAD 85,80,81\nCV 76,80,81\nMvav 192,160,64\n"
"Ra 144,26,0\nMvav 196,160,68\ns_cmp_lt_u32 s99, s92\nEX 82,54\nMvav 204,164,68\nRa 148,26,1024\ns_cbranch_scc1 .E510\n.E511:\nMvav 200,164,64\nWL 8\nEX 83,55\nMvav 208,168,64\n"
"Ra 152,26,2048\nAD 85,85,82\nMvav 212,168,68\nD1 1,16,46,1024\nMvav 220,172,68\nRa 156,26,3072\nMvav 216,172,64\nAD 85,85,83\nCV 77,82,83\nMvav 224,176,64\nRa 160,24,8192\n"
"Mvav 228,176,68\ns_sub_u32 s90, s90, 1\nEX 80,56\nMvav 236,180,68\nRa 164,24,9216\ns_cbranch_scc1 .E512\n.E513:\nMvav 232,180,64\nWL 6\nEX 81,57\nMvav 240,184,64\nRa 168,24,10240\n"
"AD 84,84,80\nMvav 244,184,68\nD1 1,16,46,2048\ns_sub_u32 s99, s99, 1\nMvav 252,188,68\nRa 172,24,11264\nMvav 248,188,64\nAD 84,84,81\nCV 74,80,81\ns_cbranch_scc0 .A506\n"
"s_branch .A520\n.E508:\nv_cmpx_gt_i32 vcc, 19, v0\nv_mov_b32 v15, s97\nv_cmpx_gt_i32 vcc, 18, v0\nv_mov_b32 v14, s97\nv_cmpx_gt_i32 vcc, 17, v0\nv_mov_b32 v13, s97\n"
"v_cmpx_gt_i32 vcc, 16, v0\nv_mov_b32 v12, s97\nv_cmpx_gt_i32 vcc, 3, v0\nv_mov_b32 v7, s97\nv_cmpx_gt_i32 vcc, 2, v0\nv_mov_b32 v6, s97\nv_cmpx_gt_i32 vcc, 1, v0\n"
"v_mov_b32 v5, s97\nv_cmpx_gt_i32 vcc, 0, v0\nv_mov_b32 v4, s97\ns_mov_b64 exec, -1\ns_nop 2\ns_branch .E509\n.E510:\ns_cmp_eq_u32 s99, 0\ns_cbranch_scc1 .K507\n"
"v_cmpx_gt_i32 vcc, 19, v3\nv_mov_b32 v15, s97\nv_cmpx_gt_i32 vcc, 18, v3\nv_mov_b32 v14, s97\nv_cmpx_gt_i32 vcc, 17, v3\nv_mov_b32 v13, s97\nv_cmpx_gt_i32 vcc, 16, v3\n"
"v_mov_b32 v12, s97\nv_cmpx_gt_i32 vcc, 3, v3\nv_mov_b32 v7, s97\nv_cmpx_gt_i32 vcc, 2, v3\nv_mov_b32 v6, s97\nv_cmpx_gt_i32 vcc, 1, v3\nv_mov_b32 v5, s97\n"
"v_cmpx_gt_i32 vcc, 0, v3\nv_mov_b32 v4, s97\ns_mov_b64 exec, -1\ns_nop 2\ns_branch .E511\n.K507:\nv_cmpx_gt_i32 vcc, 19, v3\nv_mov_b32 v19, s97\nv_cmpx_gt_i32 vcc, 18, v3\n"
"v_mov_b32 v18, s97\nv_cmpx_gt_i32 vcc, 17, v3\nv_mov_b32 v17, s97\nv_cmpx_gt_i32 vcc, 16, v3\nv_mov_b32 v16, s97\nv_cmpx_gt_i32 vcc, 3, v3\nv_mov_b32 v11, s97\n"
"v_cmpx_gt_i32 vcc, 2, v3\nv_mov_b32 v10, s97\nv_cmpx_gt_i32 vcc, 1, v3\nv_mov_b32 v9, s97\nv_cmpx_gt_i32 vcc, 0, v3\nv_mov_b32 v8, s97\ns_mov_b64 exec, -1\ns_nop 2\n"
"s_branch .E511\n.E512:\ns_swappc_b64 s[80:81], s[8:9]\ns_branch .E513\n.E514:\nv_mov_b32 v91, 0x40000000\nv_cmp_gt_u32 vcc, 0x200, v1\nWV 5\ns_nop 0\n"
"v_cndmask_b32 v91, v91, v1, vcc\nSv 92,91,16,65,0\ns_branch .E515\n.A520:\n.LE435:\nRa 176,26,8192\nEX 82,58\nRa 180,26,9216\nWL 6\nEX 83,59\nCvaa 36,140,220,8\nRa 184,26,10240\n"
"AD 84,84,82\nMvaa 36,136,212\nD1 1,16,46,3072\ns_add_u32 m0, s89, 32768\nMvaa 36,132,204\nRa 188,26,11264\nMvaa 36,128,196\nAD 84,84,83\nCV 75,82,83\nMvaa 36,144,228\n"
"Ra 128,2,16384\nMvaa 36,148,236\ns_add_u32 s46, s86, s88\nEX 80,60\nMvaa 36,152,244\nRa 132,2,17408\nMvaa 36,156,252\nEX 81,61\nRa 136,2,18432\nAD 85,85,80\nD1 1,16,46,0\n"
"Ra 140,2,19456\nAD 85,85,81\nCV 78,80,81\nRa 144,2,20480\ns_mov_b32 s87, s95\nEX 82,62\nRa 148,2,21504\nWL 6\nEX 83,63\nCvaa 44,172,220,16\nRa 152,2,22528\nAD 85,85,82\n"
"Mvaa 44,168,212\nD1 1,16,46,1024\nMvaa 44,164,204\nRa 156,2,23552\nMvaa 44,160,196\nAD 85,85,83\nCV 79,82,83\nMvaa 44,176,228\nRa 160,2,24576\nv_max_f32 v90, v84, v85\n"
"Mvaa 44,180,236\ns_add_u32 s88, s88, 32768\nMvaa 44,184,244\nRa 164,2,25600\nv_cmp_lt_f32 vcc, s63, v90\nMvaa 44,188,252\nWL 8\nRa 168,2,26624\ns_cbranch_vccnz .T524\n.T525:\n"
"D1 1,16,46,2048\nRa 172,2,27648\nAD 86,86,84\nMvav 128,128,72\nRa 176,2,28672\nMvav 132,128,76\ns_cmp_eq_u32 s99, s98\nMvav 140,132,76\nRa 180,2,29696\ns_cbranch_scc1 .E522\n"
".E523:\nMvav 136,132,72\ns_waitcnt vmcnt(12) lgkmcnt(8)\nMvav 144,136,72\nRa 184,2,30720\nMvav 148,136,76\nD1 1,16,46,3072\ns_add_u32 m0, s89, 49152\nMvav 156,140,76\n"
"Ra 188,2,31744\ns_barrier\nMvav 152,140,72\nMvav 160,144,72\nRa 128,24,32768\nMvav 164,144,76\ns_add_u32 s46, s87, s88\nEX 80,36\nMvav 172,148,76\nRa 132,24,33792\n"
"Mvav 168,148,72\nWL 8\nEX 81,37\nMvav 176,152,72\nRa 136,24,34816\nAD 87,87,85\nMvav 180,152,76\nD1 1,16,46,0\nMvav 188,156,76\nRa 140,24,35840\nMvav 184,156,72\nAD 85,80,81\n"
"CV 68,80,81\nMvav 192,160,72\nRa 144,26,32768\nMvav 196,160,76\ns_nop 0\nEX 82,38\nMvav 204,164,76\nRa 148,26,33792\nv_cmpx_gt_i32 vcc, 19, v0\nv_mov_b32 v19, s97\n"
"v_cmpx_gt_i32 vcc, 18, v0\nv_mov_b32 v18, s97\nv_cmpx_gt_i32 vcc, 17, v0\nv_mov_b32 v17, s97\nv_cmpx_gt_i32 vcc, 16, v0\nv_mov_b32 v16, s97\nv_cmpx_gt_i32 vcc, 3, v0\n"
"v_mov_b32 v11, s97\nv_cmpx_gt_i32 vcc, 2, v0\nv_mov_b32 v10, s97\nv_cmpx_gt_i32 vcc, 1, v0\nv_mov_b32 v9, s97\nv_cmpx_gt_i32 vcc, 0, v0\nv_mov_b32 v8, s97\ns_mov_b64 exec, -1\n"
"s_nop 2\ns_nop 0\nMvav 200,164,72\nWL 8\nEX 83,39\nMvav 208,168,72\nRa 152,26,34816\nAD 85,85,82\nMvav 212,168,76\nD1 1,16,46,1024\nMvav 220,172,76\nRa 156,26,35840\n"
"Mvav 216,172,72\nAD 85,85,83\nCV 69,82,83\nMvav 224,176,72\nRa 160,24,40960\nMvav 228,176,76\ns_xor_b32 s89, s89, 0x10000\nMvav 236,180,76\nRa 164,24,41984\nMvav 232,180,72\n"
"WL 10\nMvav 240,184,72\nRa 168,24,43008\nMvav 244,184,76\nD1 1,16,46,2048\nMvav 252,188,76\nRa 172,24,44032\nMvav 248,188,72\nRa 176,26,40960\nRa 180,26,41984\nWL 6\n"
"Cvaa 52,140,220,8\nRa 184,26,43008\nMvaa 52,136,212\nD1 1,16,46,3072\ns_add_u32 m0, s89, 0\nMvaa 52,132,204\nRa 188,26,44032\nMvaa 52,128,196\nMvaa 52,144,228\nRa 128,2,49152\n"
"Mvaa 52,148,236\ns_add_u32 s46, s86, s88\nEX 80,44\nMvaa 52,152,244\nRa 132,2,50176\nv_xor_b32 v24, 0x10000, v24\nMvaa 52,156,252\nEX 81,45\nRa 136,2,51200\nAD 85,85,80\n"
"D1 1,16,46,0\nRa 140,2,52224\nAD 85,85,81\nCV 70,80,81\nRa 144,2,53248\nEX 82,46\nRa 148,2,54272\nv_xor_b32 v26, 0x10000, v26\nWL 6\nEX 83,47\nCvaa 60,172,220,16\nRa 152,2,55296\n"
"AD 85,85,82\nMvaa 60,168,212\nD1 1,16,46,1024\nMvaa 60,164,204\nRa 156,2,56320\nMvaa 60,160,196\nAD 85,85,83\nCV 71,82,83\nMvaa 60,176,228\nRa 160,2,57344\nMvaa 60,180,236\n"
"s_add_u32 s88, s88, 32768\nMvaa 60,184,244\nRa 164,2,58368\nv_cmp_lt_f32 vcc, s63, v85\nMvaa 60,188,252\nWL 8\nRa 168,2,59392\ns_cbranch_vccnz .T526\n.T527:\nD1 1,16,46,2048\n"
"Ra 172,2,60416\nRa 176,2,61440\nMvav 132,128,68\nMvav 140,132,68\nRa 180,2,62464\ns_waitcnt vmcnt(11) lgkmcnt(8)\nRa 184,2,63488\nMvav 148,136,68\nD1 1,16,46,3072\n"
"s_add_u32 m0, s89, 16384\nMvav 156,140,68\nRa 188,2,64512\ns_barrier\nRa 128,24,0\nMvav 164,144,68\ns_add_u32 s46, s87, s88\nEX 80,52\nMvav 172,148,68\nRa 132,24,1024\n"
"v_xor_b32 v2, 0x10000, v2\nWL 8\nEX 81,53\nRa 136,24,2048\nAD 87,87,85\nMvav 180,152,68\nD1 1,16,46,0\nMvav 188,156,68\nRa 140,24,3072\nAD 85,80,81\nCV 76,80,81\nRa 144,26,0\n"
"Mvav 196,160,68\nEX 82,54\nMvav 204,164,68\nRa 148,26,1024\nWL 8\nEX 83,55\nRa 152,26,2048\nAD 85,85,82\nMvav 212,168,68\nD1 1,16,46,1024\nMvav 220,172,68\nRa 156,26,3072\n"
"AD 85,85,83\nCV 77,82,83\nRa 160,24,8192\nMvav 228,176,68\nMvav 236,180,68\nRa 164,24,9216\nWL 10\nRa 168,24,10240\nMvav 244,184,68\nD1 1,16,46,2048\nMvav 252,188,68\n"
"Ra 172,24,11264\nD1 1,16,46,3072\ns_add_u32 m0, s89, 32768\ns_add_u32 s46, s86, s88\nRa 128,2,16384\nv_xor_b32 v3, 0x10000, v2\nv_add_u32 v0, 0x20000, v1\nEX 80,60\n"
"Ra 132,2,17408\ns_sub_u32 s94, s49, 1835008\nEX 81,61\nRa 136,2,18432\nAD 85,85,80\ns_xor_b32 s92, s94, 0x202000\nD1 1,16,46,0\ns_lshl_b32 s89, s11, 12\nRa 140,2,19456\n"
"s_lshl1_add_u32 s2, s12, 1\nAD 85,85,81\nCV 78,80,81\nRa 144,2,20480\ns_lshl3_add_u32 s2, s2, s13\nEX 82,62\nRa 148,2,21504\ns_lshl4_add_u32 s2, s2, 0xc048200\nEX 83,63\n"
"Ra 152,2,22528\nAD 85,85,82\nD1 1,16,46,1024\nRa 156,2,23552\ns_and_b32 s3, s72, 0x2000\nAD 85,85,83\nCV 79,82,83\nRa 160,2,24576\ns_cmp_eq_u32 s11, 1\ns_cselect_b32 s54, s2, s54\n"
"s_cselect_b32 s3, s3, 0\nv_cmp_lt_f32 vcc, s63, v85\nRa 164,2,25600\ns_cbranch_vccnz .T528\n.T529:\nAD 87,87,85\nWL 8\nv_mov_b32 v84, v86\nv_mov_b32 v85, v87\nRa 168,2,26624\n"
"v_permlane32_swap_b32 v86, v84\nv_permlane32_swap_b32 v87, v85\nD1 1,16,46,2048\nAD 86,86,84\nRa 172,2,27648\nAD 87,87,85\nv_mov_b32 v84, v86\nv_mov_b32 v85, v87\nRa 176,2,28672\n"
"v_permlane16_swap_b32 v86, v84\nv_permlane16_swap_b32 v87, v85\nMvav 132,128,76\nAD 86,86,84\nAD 87,87,85\nMvav 140,132,76\nRa 180,2,29696\nv_rcp_f32 v86, v86\nWL 8\n"
"v_rcp_f32 v87, v87\nRa 184,2,30720\nv_xor_b32 v25, s3, v25\nMvav 148,136,76\nD1 1,16,46,3072\nMvav 156,140,76\nRa 188,2,31744\nMU 128,128,86\nMU 129,129,86\nCV 96,128,129\n"
"MU 130,130,86\nMU 131,131,86\nRa 128,2,0\nCV 97,130,131\nMU 132,132,87\nMvav 164,144,76\nMU 133,133,87\nCV 100,132,133\nMvav 172,148,76\nRa 132,2,1024\nMU 134,134,87\n"
"MU 135,135,87\nWL 8\ns_add_u32 m0, s89, 131072\ns_add_u32 s46, s96, 1212416\nRa 136,2,2048\nCV 101,134,135\nMU 136,136,86\nMvav 180,152,76\nD1 1,16,46,0\nMU 137,137,86\n"
"Mvav 188,156,76\nRa 140,2,3072\nCV 98,136,137\nMU 138,138,86\nMU 139,139,86\nCV 99,138,139\nMU 140,140,87\nRa 144,2,4096\nMU 141,141,87\nCV 102,140,141\nMvav 196,160,76\n"
"MU 142,142,87\nMU 143,143,87\nMvav 204,164,76\nRa 148,2,5120\nCV 103,142,143\nMU 144,144,86\nWL 8\nMU 145,145,86\nCV 104,144,145\nRa 152,2,6144\nMU 146,146,86\nMU 147,147,86\n"
"Mvav 212,168,76\nD1 1,16,46,1024\nCV 105,146,147\nMvav 220,172,76\nRa 156,2,7168\nMU 148,148,87\nMU 149,149,87\nCV 108,148,149\nMU 150,150,87\nMU 151,151,87\nRa 160,2,8192\n"
"CV 109,150,151\nMU 152,152,86\nMvav 228,176,76\nMU 153,153,86\nCV 106,152,153\nMvav 236,180,76\nRa 164,2,9216\nMU 154,154,86\nMU 155,155,86\nWL 8\nCV 107,154,155\nMU 156,156,87\n"
"Ra 168,2,10240\nMU 157,157,87\nCV 110,156,157\nMvav 244,184,76\nD1 1,16,46,2048\nMU 158,158,87\nMvav 252,188,76\nRa 172,2,11264\nMU 159,159,87\nCV 111,158,159\nMU 160,160,86\n"
"MU 161,161,86\nZvav 128,128,96\nRa 176,2,12288\nCV 112,160,161\nMU 162,162,86\nMvav 128,132,104\nMU 163,163,86\nCV 113,162,163\nZvav 132,132,108\nRa 180,2,13312\nMU 164,164,87\n"
"Mvav 132,128,100\ns_waitcnt vmcnt(15) lgkmcnt(8)\nMU 165,165,87\nZvav 140,136,100\nRa 184,2,14336\nCV 116,164,165\nMU 166,166,87\nMvav 140,140,108\nD1 1,16,46,3072\nMU 167,167,87\n"
"Zvav 136,140,104\nRa 188,2,15360\ns_barrier\nMvav 136,136,96\nCV 117,166,167\nMU 168,168,86\nMU 169,169,86\nZvav 144,144,96\nRa 128,2,32768\nCV 114,168,169\nMvav 144,148,104\n"
"MU 170,170,86\nMU 171,171,86\nbuffer_store_dword v25, off, s[16:19], s54 offset:512 sc0 sc1\nZvav 148,148,108\nRa 132,2,33792\nCV 115,170,171\nMU 172,172,87\nMvav 148,144,100\n"
"WL 8\ns_add_u32 m0, s89, 49152\ns_add_u32 s46, s96, 1179648\nZvav 156,152,100\nRa 136,2,34816\nMU 173,173,87\nMvav 156,156,108\nD1 1,16,46,0\nCV 118,172,173\nMU 174,174,87\n"
"Zvav 152,156,104\nRa 140,2,35840\nMU 175,175,87\nMvav 152,152,96\nCV 119,174,175\nMU 176,176,86\nZvav 64,160,96\nRa 144,2,36864\nMU 177,177,86\nCV 120,176,177\nMvav 64,164,104\n"
"MU 178,178,86\nMU 179,179,86\nZvav 68,164,108\nRa 148,2,37888\nCV 121,178,179\nMvav 68,160,100\nWL 8\nMU 180,180,87\nZvav 76,168,100\nRa 152,2,38912\nMU 181,181,87\n"
"CV 124,180,181\nMvav 76,172,108\nD1 1,16,46,1024\nMU 182,182,87\nZvav 72,172,104\nRa 156,2,39936\nMU 183,183,87\nMvav 72,168,96\nCV 125,182,183\nMU 184,184,86\nMU 185,185,86\n"
"Zvav 4,176,96\nRa 160,2,40960\nCV 122,184,185\nMvav 4,180,104\nMU 186,186,86\nMU 187,187,86\nZvav 8,180,108\nRa 164,2,41984\nCV 123,186,187\nMU 188,188,87\nMvav 8,176,100\nWL 8\n"
"MU 189,189,87\nZvav 16,184,100\nRa 168,2,43008\nCV 126,188,189\nMvav 16,188,108\nD1 1,16,46,2048\nMU 190,190,87\nMU 191,191,87\nZvav 12,188,104\nRa 172,2,44032\nCV 127,190,191\n"
"Mvav 12,184,96\nMU 192,192,86\nMU 193,193,86\nMvav 128,128,112\nRa 176,2,45056\nCV 32,192,193\nMU 194,194,86\nMvav 128,132,120\nMU 195,195,86\nCV 33,194,195\nMvav 132,132,124\n"
"Ra 180,2,46080\nMU 196,196,87\nMvav 132,128,116\nWL 8\nMU 197,197,87\nMvav 140,136,116\nRa 184,2,47104\nCV 36,196,197\nMU 198,198,87\nMvav 140,140,124\nD1 1,16,46,3072\n"
"MU 199,199,87\nMvav 136,140,120\nRa 188,2,48128\nCV 37,198,199\nMvav 136,136,112\nMU 200,200,86\nMU 201,201,86\nCV 34,200,201\nMvav 144,144,112\nRa 128,2,49152\nMU 202,202,86\n"
"Mvav 144,148,120\nMU 203,203,86\nCV 35,202,203\nMvav 148,148,124\nRa 132,2,50176\nMU 204,204,87\nMU 205,205,87\nMvav 148,144,116\nWL 8\ns_add_u32 m0, s89, 147456\n"
"s_add_u32 s46, s96, 1245184\nMvav 156,152,116\nRa 136,2,51200\nCV 38,204,205\nMvav 156,156,124\nD1 1,16,46,0\nMU 206,206,87\nMU 207,207,87\nMvav 152,156,120\nRa 140,2,52224\n"
"CV 39,206,207\nMvav 152,152,112\nMU 208,208,86\nMU 209,209,86\nMvav 64,160,112\nRa 144,2,53248\nCV 40,208,209\nMU 210,210,86\nMvav 64,164,120\nMU 211,211,86\nCV 41,210,211\n"
"Mvav 68,164,124\nRa 148,2,54272\nMU 212,212,87\nMvav 68,160,116\nWL 8\nMU 213,213,87\nMvav 76,168,116\nRa 152,2,55296\nCV 44,212,213\nMU 214,214,87\nMvav 76,172,124\n"
"D1 1,16,46,1024\nMU 215,215,87\nMvav 72,172,120\nRa 156,2,56320\nCV 45,214,215\nMvav 72,168,112\nMU 216,216,86\nMU 217,217,86\nCV 42,216,217\nMvav 4,176,112\nRa 160,2,57344\n"
"MU 218,218,86\nMvav 4,180,120\nMU 219,219,86\nCV 43,218,219\nMvav 8,180,124\nRa 164,2,58368\nMU 220,220,87\nMU 221,221,87\nMvav 8,176,116\nWL 8\nCV 46,220,221\nMvav 16,184,116\n"
"Ra 168,2,59392\nMU 222,222,87\nMvav 16,188,124\nD1 1,16,46,2048\nMU 223,223,87\nCV 47,222,223\nMvav 12,188,120\nRa 172,2,60416\nMU 224,224,86\nMvav 12,184,112\nMU 225,225,86\n"
"CV 48,224,225\nMvav 128,128,32\nRa 176,2,61440\nMU 226,226,86\nMU 227,227,86\nMvav 128,132,40\nCV 49,226,227\nMU 228,228,87\nMvav 132,132,44\nRa 180,2,62464\nMU 229,229,87\n"
"Mvav 132,128,36\ns_waitcnt vmcnt(16) lgkmcnt(8)\nCV 52,228,229\nMvav 140,136,36\nRa 184,2,63488\nMU 230,230,87\nMU 231,231,87\nMvav 140,140,44\nD1 1,16,46,3072\nCV 53,230,231\n"
"Mvav 136,140,40\nRa 188,2,64512\ns_barrier\nMvav 136,136,32\nMU 232,232,86\nMU 233,233,86\nCV 50,232,233\nMvav 144,144,32\nRa 128,3,0\nMU 234,234,86\nMvav 144,148,40\n"
"MU 235,235,86\nCV 51,234,235\nMvav 148,148,44\nRa 132,3,1024\nMU 236,236,87\nMU 237,237,87\nMvav 148,144,36\nWL 8\ns_add_u32 m0, s89, 81920\ns_add_u32 s46, s96, 1277952\n"
"Mvav 156,152,36\nRa 136,3,2048\nCV 54,236,237\nMvav 156,156,44\nD1 1,16,46,0\nMU 238,238,87\nMU 239,239,87\nMvav 152,156,40\nRa 140,3,3072\nCV 55,238,239\nMvav 152,152,32\n"
"MU 240,240,86\nMU 241,241,86\nMvav 64,160,32\nRa 144,3,4096\nCV 56,240,241\nMU 242,242,86\nMvav 64,164,40\nMU 243,243,86\nCV 57,242,243\nMvav 68,164,44\nRa 148,3,5120\n"
"MU 244,244,87\nMvav 68,160,36\nWL 8\nMU 245,245,87\nMvav 76,168,36\nRa 152,3,6144\nCV 60,244,245\nMU 246,246,87\nMvav 76,172,44\nD1 1,16,46,1024\nMU 247,247,87\nMvav 72,172,40\n"
"Ra 156,3,7168\nCV 61,246,247\nMvav 72,168,32\nMU 248,248,86\nMU 249,249,86\nCV 58,248,249\nMvav 4,176,32\nRa 160,3,8192\nMU 250,250,86\nMvav 4,180,40\nMU 251,251,86\n"
"CV 59,250,251\nMvav 8,180,44\nRa 164,3,9216\nMU 252,252,87\nMU 253,253,87\nMvav 8,176,36\nWL 8\nCV 62,252,253\nMvav 16,184,36\nRa 168,3,10240\nMU 254,254,87\nMvav 16,188,44\n"
"D1 1,16,46,2048\nMU 255,255,87\nCV 63,254,255\nMvav 12,188,40\nRa 172,3,11264\nMvav 12,184,32\nMvav 128,128,48\nRa 176,3,12288\nMvav 128,132,56\nMvav 132,132,60\nRa 180,3,13312\n"
"Mvav 132,128,52\nWL 8\nMvav 140,136,52\nRa 184,3,14336\nMvav 140,140,60\nD1 1,16,46,3072\nMvav 136,140,56\nRa 188,3,15360\nMvav 136,136,48\nMvav 144,144,48\nRa 128,3,16384\n"
"Mvav 144,148,56\nbuffer_load_dword v27, off, s[16:19], s60 offset:512 sc0 sc1\nMvav 148,148,60\nRa 132,3,17408\nCV 132,132,133\nMvav 148,144,52\nWL 8\ns_add_u32 m0, s89, 65536\n"
"s_add_u32 s46, s96, 1310720\nMvav 156,152,52\nRa 136,3,18432\nCV 133,134,135\nMvav 156,156,60\nD1 1,16,46,0\nCV 134,140,141\nMvav 152,156,56\nRa 140,3,19456\nCV 135,142,143\n"
"Mvav 152,152,48\ns_add_u32 s5, s94, 0\nSv 132,1,16,5,1024\nMvav 64,160,48\nRa 144,3,20480\nMvav 64,164,56\nMvav 68,164,60\nRa 148,3,21504\nMvav 68,160,52\nWL 8\nCV 128,128,129\n"
"Mvav 76,168,52\nRa 152,3,22528\nCV 129,130,131\nMvav 76,172,60\nD1 1,16,46,1024\nCV 130,136,137\nMvav 72,172,56\nRa 156,3,23552\nCV 131,138,139\nMvav 72,168,48\nSv 128,1,16,5,0\n"
"Mvav 4,176,48\nRa 160,3,24576\nMvav 4,180,56\nMvav 8,180,60\nRa 164,3,25600\nMvav 8,176,52\nWL 6\nCV 148,148,149\nMvav 16,184,52\nRa 168,3,26624\nCV 149,150,151\nMvav 16,188,60\n"
"D1 1,16,46,2048\nCV 150,156,157\nMvav 12,188,56\nRa 172,3,27648\nCV 151,158,159\nMvav 12,184,48\ns_add_u32 s5, s94, 131072\nSv 148,1,16,5,1024\nZvav 176,128,96\nRa 176,3,28672\n"
"Mvav 176,132,104\nMvav 176,136,112\nRa 180,3,29696\nMvav 176,140,120\ns_waitcnt vmcnt(15) lgkmcnt(6)\nCV 144,144,145\nZvav 180,140,124\nRa 184,3,30720\nCV 145,146,147\n"
"Mvav 180,136,116\nD1 1,16,46,3072\nCV 146,152,153\nMvav 180,132,108\nRa 188,3,31744\ns_barrier\nMvav 180,128,100\nCV 147,154,155\nSv 144,1,16,5,0\nMvav 180,144,36\nRa 128,3,32768\n"
"Mvav 180,148,44\nMvav 180,152,52\nRa 132,3,33792\nMvav 180,156,60\nWL 6\ns_add_u32 m0, s89, 98304\ns_add_u32 s46, s96, 1343488\nMvav 176,156,56\nRa 136,3,34816\nCV 68,68,69\n"
"Mvav 176,152,48\nD1 1,16,46,0\nCV 69,70,71\nMvav 176,148,40\nRa 140,3,35840\nCV 70,76,77\nMvav 176,144,32\nCV 71,78,79\ns_add_u32 s5, s94, 262144\nZvav 184,160,96\nRa 144,3,36864\n"
"Sv 68,1,16,5,1024\nMvav 184,164,104\nMvav 184,168,112\nRa 148,3,37888\nMvav 184,172,120\nWL 6\nZvav 188,172,124\nRa 152,3,38912\nCV 64,64,65\nMvav 188,168,116\nD1 1,16,46,1024\n"
"CV 65,66,67\nMvav 188,164,108\nRa 156,3,39936\nCV 66,72,73\nMvav 188,160,100\nCV 67,74,75\nSv 64,1,16,5,0\nMvav 188,176,36\nRa 160,3,40960\nMvav 188,180,44\nMvav 188,184,52\n"
"Ra 164,3,41984\nMvav 188,188,60\nWL 6\nCV 8,8,9\nMvav 184,188,56\nRa 168,3,43008\nCV 9,10,11\nMvav 184,184,48\nD1 1,16,46,2048\nCV 10,16,17\nMvav 184,180,40\nRa 172,3,44032\n"
"CV 11,18,19\nMvav 184,176,32\ns_add_u32 s5, s94, 393216\nSv 8,1,16,5,1024\nZvav 192,128,96\nRa 176,3,45056\nv_lshrrev_b32 v88, 8, v1\nMvav 192,132,104\nv_lshlrev_b32 v89, 5, v88\n"
"Mvav 192,136,112\nRa 180,3,46080\nMvav 192,140,120\nWL 6\nCV 4,4,5\nZvav 196,140,124\nRa 184,3,47104\nv_bfe_u32 v90, v1, 4, 4\nMvav 196,136,116\nD1 1,16,46,3072\nCV 5,6,7\n"
"Mvav 196,132,108\nRa 188,3,48128\nCV 6,12,13\nMvav 196,128,100\nv_mul_u32_u24 v91, 896, v90\nCV 7,14,15\nMvav 196,144,36\nRa 128,3,49152\nv_lshl_add_u32 v91, v88, 4, v91\nWV 15\n"
"v_cmp_ne_u32 vcc, s15, v27\ns_nop 0\ns_cbranch_vccnz .S554\n.S555:\nMvav 196,148,44\ns_add_u32 s4, s92, 0\nSv 4,1,16,5,0\nMvav 196,152,52\nRa 132,3,50176\nMvav 196,156,60\nWL 6\n"
"s_add_u32 m0, s89, 114688\ns_add_u32 s46, s96, 1376256\nbuffer_load_dwordx4 v[208:211], v1, s[16:19], s4 offen offset:0 sc0 sc1\nMvav 192,156,56\nRa 136,3,51200\n"
"v_add_u32 v90, s21, v90\nMvav 192,152,48\nD1 1,16,46,0\nCV 180,180,181\nMvav 192,148,40\nRa 140,3,52224\nCV 181,182,183\nMvav 192,144,32\n"
"buffer_load_dwordx4 v[212:215], v1, s[16:19], s4 offen offset:1024 sc0 sc1\nCV 182,188,189\nZvav 200,160,96\nRa 144,3,53248\nv_lshlrev_b32 v88, 3, v90\nMvav 200,164,104\n"
"v_add_u32 v94, 64, v90\nCV 183,190,191\nMvav 200,168,112\nRa 148,3,54272\ns_add_u32 s5, s94, 524288\nMvav 200,172,120\nWL 6\nSv 180,1,16,5,1024\nZvav 204,172,124\nRa 152,3,55296\n"
"s_mov_b32 s63, 0xc040000\nMvav 204,168,116\nD1 1,16,46,1024\nMvav 204,164,108\nRa 156,3,56320\nMvav 204,160,100\nv_lshrrev_b32 v92, 8, v1\nCV 176,176,177\nMvav 204,176,36\n"
"Ra 160,3,57344\nv_bfe_u32 v93, v1, 6, 2\nMvav 204,180,44\nv_cmp_eq_u32 vcc, v92, v93\nv_and_b32 v92, 16, v1\nv_mov_b32 v93, 0x3f80\nv_cndmask_b32 v93, 0, v93, vcc\n"
"Mvav 204,184,52\nRa 164,3,58368\nCV 177,178,179\nMvav 204,188,60\nWL 6\nCV 178,184,185\nMvav 200,188,56\nRa 168,3,59392\nv_lshlrev_b32 v93, v92, v93\nMvav 200,184,48\n"
"D1 1,16,46,2048\nCV 179,186,187\nMvav 200,180,40\nRa 172,3,60416\nSv 176,1,16,5,0\nMvav 200,176,32\nv_and_b32 v92, 32, v1\nv_cmp_eq_u32 vcc, 0, v92\nv_mov_b32 v4, 0\n"
"v_mov_b32 v5, 0\nv_cndmask_b32 v6, 0, v93, vcc\nZvav 160,128,96\nRa 176,3,61440\nv_sub_u32 v7, v93, v6\nMvav 160,132,104\nv_mov_b32 v8, 0\nMvav 160,136,112\nRa 180,3,62464\n"
"CV 196,196,197\nMvav 160,140,120\nWL 6\nCV 197,198,199\nZvav 164,140,124\nRa 184,3,63488\nv_mov_b32 v9, 0\nMvav 164,136,116\nD1 1,16,46,3072\nCV 198,204,205\nMvav 164,132,108\n"
"Ra 188,3,64512\ns_barrier\nMvav 164,128,100\nCV 199,206,207\ns_add_u32 s5, s94, 655360\nMvav 164,144,36\nRa 128,0,0\nSv 196,1,16,5,1024\nMvav 164,148,44\n"
"s_add_u32 s4, s92, 131072\nMvav 164,152,52\nRa 132,0,1024\nMvav 164,156,60\nWL 6\ns_add_u32 m0, s89, 0\ns_add_u32 s46, s96, 1409024\n"
"buffer_load_dwordx4 v[216:219], v1, s[16:19], s4 offen offset:0 sc0 sc1\nMvav 160,156,56\nRa 136,0,2048\nCV 192,192,193\nMvav 160,152,48\nD1 1,16,46,0\nCV 193,194,195\n"
"Mvav 160,148,40\nRa 140,0,3072\nCV 194,200,201\nMvav 160,144,32\nbuffer_load_dwordx4 v[220:223], v1, s[16:19], s4 offen offset:1024 sc0 sc1\nCV 195,202,203\nZvav 168,160,96\n"
"Ra 144,0,4096\nWV 9\nv_max_u32 v21, v211, v215\nv_cmp_eq_u32 vcc, -1, v21\ns_nop 0\ns_cbranch_vccnz .S530\n.S531:\nMvav 168,164,104\nv_lshlrev_b32 v176, 16, v208\nSv 192,1,16,5,0\n"
"Mvav 168,168,112\nRa 148,0,5120\nMvav 168,172,120\nWL 6\nv_and_b32 v177, 0xffff0000, v208\nZvav 172,172,124\nRa 152,0,6144\nMvav 172,168,116\nD1 1,16,46,1024\nMvav 172,164,108\n"
"Ra 156,0,7168\nv_lshlrev_b32 v178, 16, v209\nMvav 172,160,100\nv_and_b32 v179, 0xffff0000, v209\nMvav 172,176,36\nRa 160,0,8192\nv_lshlrev_b32 v180, 16, v212\nMvav 172,180,44\n"
"v_and_b32 v181, 0xffff0000, v212\nMvav 172,184,52\nRa 164,0,9216\nMvav 172,188,60\nWL 6\nv_lshlrev_b32 v182, 16, v213\nMvav 168,188,56\nRa 168,0,10240\nMvav 168,184,48\n"
"D1 1,16,46,2048\nMvav 168,180,40\nRa 172,0,11264\nv_and_b32 v183, 0xffff0000, v213\nMvav 168,176,32\nv_lshlrev_b32 v184, 16, v210\nMvav 176,128,96\nRa 176,0,12288\n"
"v_and_b32 v185, 0xffff0000, v210\nMvav 176,132,104\nv_lshlrev_b32 v186, 16, v211\nCV 164,164,165\nMvav 176,136,112\nRa 180,0,13312\nCV 165,166,167\nMvav 176,140,120\nWL 6\n"
"CV 166,172,173\nMvav 180,140,124\nRa 184,0,14336\nCV 167,174,175\nMvav 180,136,116\nD1 1,16,46,3072\ns_add_u32 s5, s94, 786432\nMvav 180,132,108\nRa 188,0,15360\n"
"v_and_b32 v187, 0xffff0000, v211\nMvav 180,128,100\nv_lshlrev_b32 v188, 16, v214\nSv 164,1,16,5,1024\nMvav 180,144,36\nRa 128,0,16384\nMvav 180,148,44\n"
"v_and_b32 v189, 0xffff0000, v214\ns_add_u32 s4, s92, 262144\nMvav 180,152,52\nRa 132,0,17408\nMvav 180,156,60\nWL 6\nv_lshlrev_b32 v190, 16, v215\n"
"buffer_load_dwordx4 v[224:227], v1, s[16:19], s4 offen offset:0 sc0 sc1\nMvav 176,156,56\nRa 136,0,18432\nCV 160,160,161\nMvav 176,152,48\nv_and_b32 v191, 0xffff0000, v215\n"
"CV 161,162,163\nMvav 176,148,40\nRa 140,0,19456\nCV 162,168,169\nMvav 176,144,32\nbuffer_load_dwordx4 v[228:231], v1, s[16:19], s4 offen offset:1024 sc0 sc1\nCV 163,170,171\n"
"Mvav 184,160,96\nRa 144,0,20480\nWV 7\nv_max_u32 v21, v219, v223\nv_cmp_eq_u32 vcc, -1, v21\ns_nop 0\ns_cbranch_vccnz .S533\n.S534:\nMvav 184,164,104\n"
"v_lshlrev_b32 v192, 16, v216\nSv 160,1,16,5,0\nMvav 184,168,112\nRa 148,0,21504\nMvav 184,172,120\nWL 6\nMvav 188,172,124\nRa 152,0,22528\nv_and_b32 v193, 0xffff0000, v216\n"
"Mvav 188,168,116\nv_lshlrev_b32 v194, 16, v217\nMvav 188,164,108\nRa 156,0,23552\nMvav 188,160,100\nv_and_b32 v195, 0xffff0000, v217\nMvav 188,176,36\nRa 160,0,24576\n"
"v_lshlrev_b32 v196, 16, v220\nMvav 188,180,44\nv_and_b32 v197, 0xffff0000, v220\nMvav 188,184,52\nRa 164,0,25600\nMvav 188,188,60\nWL 6\nMvav 184,188,56\nRa 168,0,26624\n"
"v_lshlrev_b32 v198, 16, v221\nMvav 184,184,48\nv_and_b32 v199, 0xffff0000, v221\nMvav 184,180,40\nRa 172,0,27648\nMvav 184,176,32\nv_lshlrev_b32 v200, 16, v218\nMvav 192,128,96\n"
"Ra 176,0,28672\nv_and_b32 v201, 0xffff0000, v218\nMvav 192,132,104\nv_lshlrev_b32 v202, 16, v219\nCV 180,180,181\nMvav 192,136,112\nRa 180,0,29696\nCV 181,182,183\n"
"Mvav 192,140,120\nWL 6\nCV 182,188,189\nMvav 196,140,124\nRa 184,0,30720\nCV 183,190,191\nMvav 196,136,116\nv_and_b32 v203, 0xffff0000, v219\ns_add_u32 s5, s94, 917504\n"
"Mvav 196,132,108\nRa 188,0,31744\ns_barrier\nMvav 196,128,100\nv_lshlrev_b32 v204, 16, v222\nSv 180,1,16,5,1024\nMvav 196,144,36\nRa 128,2,16384\nv_and_b32 v205, 0xffff0000, v222\n"
"Mvav 196,148,44\nv_lshlrev_b32 v206, 16, v223\ns_add_u32 s4, s92, 393216\nMvav 196,152,52\nRa 132,2,17408\nMvav 196,156,60\nWL 6\n"
"buffer_load_dwordx4 v[208:211], v1, s[16:19], s4 offen offset:0 sc0 sc1\nMvav 192,156,56\nRa 136,2,18432\nCV 176,176,177\nMvav 192,152,48\nv_and_b32 v207, 0xffff0000, v223\n"
"CV 177,178,179\nMvav 192,148,40\nRa 140,2,19456\nCV 178,184,185\nMvav 192,144,32\nbuffer_load_dwordx4 v[212:215], v1, s[16:19], s4 offen offset:1024 sc0 sc1\nCV 179,186,187\n"
"Mvav 200,160,96\nRa 144,2,20480\nWV 4\nv_max_u32 v21, v227, v231\nv_cmp_eq_u32 vcc, -1, v21\ns_nop 0\ns_cbranch_vccnz .S536\n.S537:\nMvav 200,164,104\n"
"v_lshlrev_b32 v160, 16, v224\nSv 176,1,16,5,0\nMvav 200,168,112\nRa 148,2,21504\nMvav 200,172,120\nWL 6\nMvav 204,172,124\nRa 152,2,22528\nv_and_b32 v161, 0xffff0000, v224\n"
"Mvav 204,168,116\nv_lshlrev_b32 v162, 16, v225\nMvav 204,164,108\nRa 156,2,23552\nMvav 204,160,100\nv_and_b32 v163, 0xffff0000, v225\nMvav 204,176,36\nRa 160,2,24576\n"
"v_lshlrev_b32 v164, 16, v228\nMvav 204,180,44\nv_and_b32 v165, 0xffff0000, v228\nMvav 204,184,52\nRa 164,2,25600\nMvav 204,188,60\nWL 6\nMvav 200,188,56\nRa 168,2,26624\n"
"v_lshlrev_b32 v166, 16, v229\nMvav 200,184,48\nv_and_b32 v167, 0xffff0000, v229\nMvav 200,180,40\nRa 172,2,27648\nMvav 200,176,32\nv_lshlrev_b32 v168, 16, v226\nMvav 160,128,96\n"
"Ra 176,2,28672\nv_and_b32 v169, 0xffff0000, v226\nMvav 160,132,104\nv_lshlrev_b32 v170, 16, v227\nCV 196,196,197\nMvav 160,136,112\nRa 180,2,29696\nCV 197,198,199\n"
"Mvav 160,140,120\nWL 6\nCV 198,204,205\nMvav 164,140,124\nRa 184,2,30720\nv_and_b32 v24, 0x70, v1\nMvav 164,136,116\nv_and_b32 v171, 0xffff0000, v227\nCV 199,206,207\n"
"Mvav 164,132,108\nRa 188,2,31744\ns_add_u32 s5, s94, 1048576\nMvav 164,128,100\nv_lshlrev_b32 v172, 16, v230\nv_lshlrev_b32 v24, 5, v24\nMvav 164,144,36\nRa 128,2,0\n"
"v_and_b32 v173, 0xffff0000, v230\nMvav 164,148,44\nv_lshlrev_b32 v174, 16, v231\ns_add_u32 s4, s92, 524288\nMvav 164,152,52\nRa 132,2,1024\n"
"buffer_load_dword v27, v24, s[16:19], s41 offen sc0 sc1\nMvav 164,156,60\nWL 6\nbuffer_load_dwordx4 v[216:219], v1, s[16:19], s4 offen offset:0 sc0 sc1\nMvav 160,156,56\n"
"Ra 136,2,2048\nSv 196,1,16,5,1024\nMvav 160,152,48\nv_and_b32 v175, 0xffff0000, v231\nMvav 160,148,40\nRa 140,2,3072\nMvav 160,144,32\n"
"buffer_load_dwordx4 v[220:223], v1, s[16:19], s4 offen offset:1024 sc0 sc1\nMvav 168,160,96\nRa 144,2,4096\nWV 5\nv_max_u32 v21, v211, v215\nv_cmp_eq_u32 vcc, -1, v21\ns_nop 0\n"
"s_cbranch_vccnz .S539\n.S540:\nMvav 168,164,104\nv_lshlrev_b32 v176, 16, v208\nCV 192,192,193\nMvav 168,168,112\nRa 148,2,5120\nCV 193,194,195\nMvav 168,172,120\nWL 6\n"
"CV 194,200,201\nMvav 172,172,124\nRa 152,2,6144\nv_and_b32 v177, 0xffff0000, v208\nMvav 172,168,116\nv_lshlrev_b32 v178, 16, v209\nCV 195,202,203\nMvav 172,164,108\nRa 156,2,7168\n"
"Sv 192,1,16,5,0\nMvav 172,160,100\nv_and_b32 v179, 0xffff0000, v209\nMvav 172,176,36\nRa 160,2,8192\nv_lshlrev_b32 v180, 16, v212\nMvav 172,180,44\n"
"v_and_b32 v181, 0xffff0000, v212\nMvav 172,184,52\nRa 164,2,9216\nMvav 172,188,60\nWL 6\nMvav 168,188,56\nRa 168,2,10240\nv_lshlrev_b32 v182, 16, v213\nMvav 168,184,48\n"
"v_and_b32 v183, 0xffff0000, v213\nMvav 168,180,40\nRa 172,2,11264\nMvav 168,176,32\nv_lshlrev_b32 v184, 16, v210\nMvav 176,128,96\nRa 176,2,12288\n"
"v_and_b32 v185, 0xffff0000, v210\nMvav 176,132,104\nv_lshlrev_b32 v186, 16, v211\nCV 164,164,165\nMvav 176,136,112\nRa 180,2,13312\nCV 165,166,167\nMvav 176,140,120\nWL 6\n"
"CV 166,172,173\nMvav 180,140,124\nRa 184,2,14336\nCV 167,174,175\nMvav 180,136,116\nv_and_b32 v187, 0xffff0000, v211\ns_add_u32 s5, s94, 1179648\nMvav 180,132,108\nRa 188,2,15360\n"
"s_barrier\nMvav 180,128,100\nv_lshlrev_b32 v188, 16, v214\nSv 164,1,16,5,1024\nMvav 180,144,36\nRa 128,2,32768\nv_and_b32 v189, 0xffff0000, v214\nMvav 180,148,44\n"
"v_lshlrev_b32 v190, 16, v215\ns_add_u32 s4, s92, 655360\nMvav 180,152,52\nRa 132,2,33792\nMvav 180,156,60\nWL 6\n"
"buffer_load_dwordx4 v[224:227], v1, s[16:19], s4 offen offset:0 sc0 sc1\nMvav 176,156,56\nRa 136,2,34816\nCV 160,160,161\nMvav 176,152,48\nv_and_b32 v191, 0xffff0000, v215\n"
"CV 161,162,163\nMvav 176,148,40\nRa 140,2,35840\nCV 162,168,169\nMvav 176,144,32\nbuffer_load_dwordx4 v[228:231], v1, s[16:19], s4 offen offset:1024 sc0 sc1\nCV 163,170,171\n"
"Mvav 184,160,96\nRa 144,2,36864\nWV 4\nv_max_u32 v21, v219, v223\nv_cmp_eq_u32 vcc, -1, v21\ns_nop 0\ns_cbranch_vccnz .S542\n.S543:\nMvav 184,164,104\n"
"v_lshlrev_b32 v192, 16, v216\nSv 160,1,16,5,0\nMvav 184,168,112\nRa 148,2,37888\nMvav 184,172,120\nWL 6\nMvav 188,172,124\nRa 152,2,38912\nv_and_b32 v193, 0xffff0000, v216\n"
"Mvav 188,168,116\nv_lshlrev_b32 v194, 16, v217\nMvav 188,164,108\nRa 156,2,39936\nMvav 188,160,100\nv_and_b32 v195, 0xffff0000, v217\nMvav 188,176,36\nRa 160,2,40960\n"
"v_lshlrev_b32 v196, 16, v220\nMvav 188,180,44\nv_and_b32 v197, 0xffff0000, v220\nMvav 188,184,52\nRa 164,2,41984\nMvav 188,188,60\nWL 6\nMvav 184,188,56\nRa 168,2,43008\n"
"v_lshlrev_b32 v198, 16, v221\nMvav 184,184,48\nv_and_b32 v199, 0xffff0000, v221\nMvav 184,180,40\nRa 172,2,44032\nMvav 184,176,32\nv_lshlrev_b32 v200, 16, v218\nMvav 192,128,96\n"
"Ra 176,2,45056\nv_and_b32 v201, 0xffff0000, v218\nMvav 192,132,104\nv_lshlrev_b32 v202, 16, v219\nCV 180,180,181\nMvav 192,136,112\nRa 180,2,46080\nCV 181,182,183\n"
"Mvav 192,140,120\nWL 6\nCV 182,188,189\nMvav 196,140,124\nRa 184,2,47104\nCV 183,190,191\nMvav 196,136,116\nv_and_b32 v203, 0xffff0000, v219\ns_add_u32 s5, s94, 1310720\n"
"Mvav 196,132,108\nRa 188,2,48128\nSv 180,1,16,5,1024\nMvav 196,128,100\nv_lshlrev_b32 v204, 16, v222\nMvav 196,144,36\nRa 128,2,49152\nv_and_b32 v205, 0xffff0000, v222\n"
"Mvav 196,148,44\nv_lshlrev_b32 v206, 16, v223\ns_add_u32 s4, s92, 786432\nMvav 196,152,52\nRa 132,2,50176\nCV 176,176,177\nMvav 196,156,60\nWL 6\n"
"buffer_load_dwordx4 v[208:211], v1, s[16:19], s4 offen offset:0 sc0 sc1\nMvav 192,156,56\nRa 136,2,51200\nCV 177,178,179\nMvav 192,152,48\nv_and_b32 v207, 0xffff0000, v223\n"
"CV 178,184,185\nMvav 192,148,40\nRa 140,2,52224\nCV 179,186,187\nMvav 192,144,32\nbuffer_load_dwordx4 v[212:215], v1, s[16:19], s4 offen offset:1024 sc0 sc1\nSv 176,1,16,5,0\n"
"Mvav 200,160,96\nRa 144,2,53248\nWV 5\nv_max_u32 v21, v227, v231\nv_cmp_eq_u32 vcc, -1, v21\ns_nop 0\ns_cbranch_vccnz .S545\n.S546:\nMvav 200,164,104\n"
"v_lshlrev_b32 v160, 16, v224\nv_xor_b32 v27, s15, v27\nv_and_b32 v26, 0xffffdfff, v27\nv_cmp_ne_u32 s[64:65], 0, v27\nv_cmp_ne_u32 vcc, 0, v26\ns_nop 0\ns_cbranch_vccnz .S557\n"
".S558:\nMvav 200,168,112\nRa 148,2,54272\ns_bitcmp1_b32 s64, 0\ns_cselect_b32 s2, 0x2000, 0\nMvav 200,172,120\nWL 6\nMvav 204,172,124\nRa 152,2,55296\n"
"v_and_b32 v161, 0xffff0000, v224\nMvav 204,168,116\nv_lshlrev_b32 v162, 16, v225\nMvav 204,164,108\nRa 156,2,56320\ns_xor_b32 s2, s2, s20\nMvav 204,160,100\n"
"v_and_b32 v163, 0xffff0000, v225\nMvav 204,176,36\nRa 160,2,57344\nv_lshlrev_b32 v164, 16, v228\nMvav 204,180,44\nv_and_b32 v165, 0xffff0000, v228\nMvav 204,184,52\n"
"Ra 164,2,58368\nMvav 204,188,60\nWL 6\ns_add_u32 s24, s2, 0x0\nMvav 200,188,56\nRa 168,2,59392\nv_lshlrev_b32 v166, 16, v229\nMvav 200,184,48\nv_and_b32 v167, 0xffff0000, v229\n"
"s_bitcmp1_b32 s64, 1\ns_cselect_b32 s2, 0x2000, 0\nMvav 200,180,40\nRa 172,2,60416\nMvav 200,176,32\nv_lshlrev_b32 v168, 16, v226\nMvav 160,128,96\nRa 176,2,61440\n"
"v_and_b32 v169, 0xffff0000, v226\nMvav 160,132,104\nv_lshlrev_b32 v170, 16, v227\nCV 196,196,197\nMvav 160,136,112\nRa 180,2,62464\nCV 197,198,199\nMvav 160,140,120\nWL 6\n"
"CV 198,204,205\nMvav 164,140,124\nRa 184,2,63488\nCV 199,206,207\nMvav 164,136,116\nv_and_b32 v171, 0xffff0000, v227\ns_add_u32 s5, s94, 1441792\nMvav 164,132,108\nRa 188,2,64512\n"
"s_barrier\nMvav 164,128,100\nv_lshlrev_b32 v172, 16, v230\nSv 196,1,16,5,1024\nMvav 164,144,36\nRa 128,3,0\nv_and_b32 v173, 0xffff0000, v230\nMvav 164,148,44\n"
"v_lshlrev_b32 v174, 16, v231\nMvav 164,152,52\nRa 132,3,1024\nMvav 164,156,60\nWL 6\ns_xor_b32 s2, s2, s20\nMvav 160,156,56\nRa 136,3,2048\nCV 192,192,193\nMvav 160,152,48\n"
"v_and_b32 v175, 0xffff0000, v231\nCV 193,194,195\nMvav 160,148,40\nRa 140,3,3072\nCV 194,200,201\nMvav 160,144,32\ns_add_u32 s25, s2, 0x1800000\ns_bitcmp1_b32 s64, 2\n"
"s_cselect_b32 s2, 0x2000, 0\nMvav 168,160,96\nRa 144,3,4096\nWV 2\nv_max_u32 v21, v211, v215\nv_cmp_eq_u32 vcc, -1, v21\ns_nop 0\ns_cbranch_vccnz .S548\n.S549:\nMvav 168,164,104\n"
"v_lshlrev_b32 v176, 16, v208\nCV 195,202,203\nMvav 168,168,112\nRa 148,3,5120\nSv 192,1,16,5,0\nMvav 168,172,120\nWL 6\nMvav 172,172,124\nRa 152,3,6144\n"
"v_and_b32 v177, 0xffff0000, v208\nMvav 172,168,116\nv_lshlrev_b32 v178, 16, v209\nMvav 172,164,108\nRa 156,3,7168\ns_xor_b32 s2, s2, s20\nMvav 172,160,100\n"
"v_and_b32 v179, 0xffff0000, v209\nMvav 172,176,36\nRa 160,3,8192\nv_lshlrev_b32 v180, 16, v212\nMvav 172,180,44\nv_and_b32 v181, 0xffff0000, v212\nMvav 172,184,52\nRa 164,3,9216\n"
"Mvav 172,188,60\nWL 6\ns_add_u32 s26, s2, 0x3000000\nMvav 168,188,56\nRa 168,3,10240\nv_lshlrev_b32 v182, 16, v213\nMvav 168,184,48\nv_and_b32 v183, 0xffff0000, v213\n"
"s_bitcmp1_b32 s64, 3\ns_cselect_b32 s2, 0x2000, 0\nMvav 168,180,40\nRa 172,3,11264\nMvav 168,176,32\nv_lshlrev_b32 v184, 16, v210\nMvav 176,128,96\nRa 176,3,12288\n"
"s_xor_b32 s2, s2, s20\nMvav 176,132,104\nv_and_b32 v185, 0xffff0000, v210\nCV 164,164,165\nMvav 176,136,112\nRa 180,3,13312\nCV 165,166,167\nMvav 176,140,120\nWL 6\n"
"CV 166,172,173\nMvav 180,140,124\nRa 184,3,14336\nCV 167,174,175\nMvav 180,136,116\nv_lshlrev_b32 v186, 16, v211\ns_add_u32 s5, s94, 1572864\nMvav 180,132,108\nRa 188,3,15360\n"
"s_add_u32 s27, s2, 0x4800000\nMvav 180,128,100\nv_and_b32 v187, 0xffff0000, v211\ns_bitcmp1_b32 s64, 4\ns_cselect_b32 s2, 0x2000, 0\nMvav 180,144,36\nv_lshlrev_b32 v188, 16, v214\n"
"Sv 164,1,16,5,1024\nMvav 180,148,44\nv_and_b32 v189, 0xffff0000, v214\nMvav 180,152,52\nv_lshlrev_b32 v190, 16, v215\nMvav 180,156,60\nWL 4\ns_xor_b32 s2, s2, s20\n"
"Mvav 176,156,56\nv_and_b32 v191, 0xffff0000, v215\nCV 160,160,161\nMvav 176,152,48\ns_add_u32 s32, s2, 0x6000000\ns_bitcmp1_b32 s64, 5\ns_cselect_b32 s2, 0x2000, 0\n"
"Mvav 176,148,40\ns_xor_b32 s2, s2, s20\nCV 161,162,163\nMvav 176,144,32\ns_add_u32 s33, s2, 0x7800000\ns_bitcmp1_b32 s64, 6\ns_cselect_b32 s2, 0x2000, 0\nMvav 184,160,96\n"
"s_xor_b32 s2, s2, s20\ns_add_u32 s34, s2, 0x9000000\nMvav 184,164,104\ns_bitcmp1_b32 s64, 7\ns_cselect_b32 s2, 0x2000, 0\nMvav 184,168,112\ns_xor_b32 s2, s2, s20\nCV 162,168,169\n"
"Mvav 184,172,120\nWL 0\nCV 163,170,171\nMvav 188,172,124\ns_add_u32 s35, s2, 0xa800000\nSv 160,1,16,5,0\nMvav 188,168,116\nMvav 188,164,108\nMvav 188,160,100\nMvav 188,176,36\n"
"Mvav 188,180,44\nMvav 188,184,52\nMvav 188,188,60\nMvav 184,188,56\nMvav 184,184,48\nMvav 184,180,40\nMvav 184,176,32\nCV 180,180,181\nCV 181,182,183\ns_nop 1\nCV 182,188,189\n"
"CV 183,190,191\ns_add_u32 s5, s94, 1703936\nSv 180,1,16,5,1024\nCV 176,176,177\nCV 177,178,179\nCV 178,184,185\nCV 179,186,187\nSv 176,1,16,5,0\ns_cmp_eq_u32 s13, 0\n"
"s_cbranch_scc1 .Z560\nbuffer_load_dwordx4 v[12:15], v91, s[28:31], s22 offen offset:0\nbuffer_load_dwordx4 v[16:19], v91, s[28:31], s23 offen offset:0\n"
"buffer_load_dwordx4 v[64:67], v89, s[16:19], s40 offen offset:0 sc0 sc1\nbuffer_load_dwordx4 v[68:71], v89, s[16:19], s40 offen offset:16 sc0 sc1\n"
"buffer_load_dwordx4 v[72:75], v89, s[16:19], s40 offen offset:1792 sc0 sc1\nbuffer_load_dwordx4 v[76:79], v89, s[16:19], s40 offen offset:1808 sc0 sc1\n"
"buffer_load_dwordx2 v[80:81], v88, s[16:19], s63 offen offset:0 sc0 sc1\nbuffer_load_dwordx2 v[82:83], v88, s[16:19], s63 offen offset:512 sc0 sc1\n"
"buffer_load_dwordx4 v[96:99], v1, s[16:19], s24 offen offset:0 sc0 sc1\nbuffer_load_dwordx4 v[100:103], v1, s[16:19], s24 offen offset:1024 sc0 sc1\n"
"buffer_load_dwordx4 v[104:107], v1, s[16:19], s25 offen offset:0 sc0 sc1\nbuffer_load_dwordx4 v[108:111], v1, s[16:19], s25 offen offset:1024 sc0 sc1\n"
"buffer_load_dwordx4 v[112:115], v1, s[16:19], s26 offen offset:0 sc0 sc1\nbuffer_load_dwordx4 v[116:119], v1, s[16:19], s26 offen offset:1024 sc0 sc1\n"
"buffer_load_dwordx4 v[120:123], v1, s[16:19], s27 offen offset:0 sc0 sc1\nbuffer_load_dwordx4 v[124:127], v1, s[16:19], s27 offen offset:1024 sc0 sc1\n"
"buffer_load_dwordx4 v[32:35], v1, s[16:19], s32 offen offset:0 sc0 sc1\nbuffer_load_dwordx4 v[36:39], v1, s[16:19], s32 offen offset:1024 sc0 sc1\n"
"buffer_load_dwordx4 v[40:43], v1, s[16:19], s33 offen offset:0 sc0 sc1\nbuffer_load_dwordx4 v[44:47], v1, s[16:19], s33 offen offset:1024 sc0 sc1\n"
"buffer_load_dwordx4 v[48:51], v1, s[16:19], s34 offen offset:0 sc0 sc1\nbuffer_load_dwordx4 v[52:55], v1, s[16:19], s34 offen offset:1024 sc0 sc1\n"
"buffer_load_dwordx4 v[56:59], v1, s[16:19], s35 offen offset:0 sc0 sc1\nbuffer_load_dwordx4 v[60:63], v1, s[16:19], s35 offen offset:1024 sc0 sc1\nWV 16\nv_xor_b32 v66, s15, v66\n"
"v_xor_b32 v70, s15, v70\nv_xor_b32 v74, s15, v74\nv_xor_b32 v78, s15, v78\nv_xor_b32 v81, s15, v81\nv_xor_b32 v83, s15, v83\nv_or3_b32 v92, v66, v70, v74\n"
"v_or3_b32 v92, v92, v78, v81\nv_or_b32 v92, v92, v83\nv_cmp_ne_u32 vcc, 0, v92\ns_nop 0\ns_cbranch_vccnz .S551\n.S552:\nv_and_b32 v92, 0x7ff, v67\nv_lshrrev_b32 v93, 16, v67\n"
"v_cmp_eq_u32 s[44:45], v92, v90\nv_cmp_eq_u32 s[46:47], v93, v90\nv_cmp_eq_u32 s[48:49], v92, v94\nv_cmp_eq_u32 s[50:51], v93, v94\nv_cndmask_b32 v12, v12, v64, s[44:45]\n"
"v_cndmask_b32 v13, v13, v65, s[44:45]\nv_cndmask_b32 v14, v14, v68, s[44:45]\nv_cndmask_b32 v15, v15, v69, s[44:45]\nv_cndmask_b32 v12, v12, v72, s[46:47]\n"
"v_cndmask_b32 v13, v13, v73, s[46:47]\nv_cndmask_b32 v14, v14, v76, s[46:47]\nv_cndmask_b32 v15, v15, v77, s[46:47]\nv_cndmask_b32 v16, v16, v64, s[48:49]\n"
"v_cndmask_b32 v17, v17, v65, s[48:49]\nv_cndmask_b32 v18, v18, v68, s[48:49]\nv_cndmask_b32 v19, v19, v69, s[48:49]\nv_cndmask_b32 v16, v16, v72, s[50:51]\n"
"v_cndmask_b32 v17, v17, v73, s[50:51]\nv_cndmask_b32 v18, v18, v76, s[50:51]\nv_cndmask_b32 v19, v19, v77, s[50:51]\nv_sub_f32 v92, 1.0, v80\nv_sub_f32 v93, 1.0, v82\nCV 92,92,93\n"
"v_and_b32 v93, 0xffff0000, v92\nv_lshlrev_b32 v92, 16, v92\nv_lshlrev_b32 v64, 16, v12\nv_and_b32 v65, 0xffff0000, v12\nMU 64,64,92\nMU 65,65,92\nv_lshlrev_b32 v66, 16, v13\n"
"v_and_b32 v67, 0xffff0000, v13\nMU 66,66,92\nMU 67,67,92\nv_lshlrev_b32 v68, 16, v14\nv_and_b32 v69, 0xffff0000, v14\nMU 68,68,92\nMU 69,69,92\nv_lshlrev_b32 v70, 16, v15\n"
"v_and_b32 v71, 0xffff0000, v15\nMU 70,70,92\nMU 71,71,92\nv_lshlrev_b32 v72, 16, v16\nv_and_b32 v73, 0xffff0000, v16\nMU 72,72,93\nMU 73,73,93\nv_lshlrev_b32 v74, 16, v17\n"
"v_and_b32 v75, 0xffff0000, v17\nMU 74,74,93\nMU 75,75,93\nv_lshlrev_b32 v76, 16, v18\nv_and_b32 v77, 0xffff0000, v18\nMU 76,76,93\nMU 77,77,93\nv_lshlrev_b32 v78, 16, v19\n"
"v_and_b32 v79, 0xffff0000, v19\nMU 78,78,93\nMU 79,79,93\ns_mov_b32 s7, 262144\nWV 0\n.Z563:\nv_max_u32 v84, v99, v103\nv_cmp_eq_u32 s[44:45], -1, v84\nv_max_u32 v84, v107, v111\n"
"v_cmp_eq_u32 s[46:47], -1, v84\nv_max_u32 v84, v115, v119\nv_cmp_eq_u32 s[48:49], -1, v84\nv_max_u32 v84, v123, v127\nv_cmp_eq_u32 s[50:51], -1, v84\nv_max_u32 v84, v35, v39\n"
"v_cmp_eq_u32 s[52:53], -1, v84\nv_max_u32 v84, v43, v47\nv_cmp_eq_u32 s[54:55], -1, v84\nv_max_u32 v84, v51, v55\nv_cmp_eq_u32 s[56:57], -1, v84\nv_max_u32 v84, v59, v63\n"
"v_cmp_eq_u32 s[58:59], -1, v84\ns_or_b64 s[0:1], s[44:45], s[46:47]\ns_or_b64 s[2:3], s[48:49], s[50:51]\ns_or_b64 s[0:1], s[0:1], s[52:53]\ns_or_b64 s[2:3], s[2:3], s[54:55]\n"
"s_or_b64 s[0:1], s[0:1], s[56:57]\ns_or_b64 s[2:3], s[2:3], s[58:59]\ns_or_b64 s[0:1], s[0:1], s[2:3]\ns_cbranch_scc1 .Z561\n.Z562:\nZvvv 136,6,96\nMvvv 136,6,104\nMvvv 136,6,112\n"
"Mvvv 136,6,120\nMvvv 136,6,32\nMvvv 136,6,40\nMvvv 136,6,48\nMvvv 136,6,56\nZvvv 140,4,96\nMvvv 140,4,104\nMvvv 140,4,112\nMvvv 140,4,120\nMvvv 140,4,32\nMvvv 140,4,40\n"
"Mvvv 140,4,48\nMvvv 140,4,56\nZvvv 144,6,100\nMvvv 144,6,108\nMvvv 144,6,116\nMvvv 144,6,124\nMvvv 144,6,36\nMvvv 144,6,44\nMvvv 144,6,52\nMvvv 144,6,60\nZvvv 148,4,100\n"
"Mvvv 148,4,108\nMvvv 148,4,116\nMvvv 148,4,124\nMvvv 148,4,36\nMvvv 148,4,44\nMvvv 148,4,52\nMvvv 148,4,60\nFM 64,80,136\nFM 65,80,137\nFM 66,80,138\nFM 67,80,139\nFM 68,80,140\n"
"FM 69,80,141\nFM 70,80,142\nFM 71,80,143\nCV 12,64,65\nCV 13,66,67\nCV 14,68,69\nCV 15,70,71\nSv 12,91,36,22,0\nFM 72,82,144\nFM 73,82,145\nFM 74,82,146\nFM 75,82,147\n"
"FM 76,82,148\nFM 77,82,149\nFM 78,82,150\nFM 79,82,151\nCV 16,72,73\nCV 17,74,75\nCV 18,76,77\nCV 19,78,79\nSv 16,91,36,23,0\n.Z560:\ns_endpgm\n.E522:\nv_mov_b32 v91, 0x40000000\n"
"v_cmp_gt_u32 vcc, 0x200, v1\nv_cndmask_b32 v91, v91, v1, vcc\nSv 92,91,16,65,0\ns_branch .E523\n.S430:\ns_mov_b32 s7, 262144\n.S432:\ns_lshl_b32 s5, s73, 8\n"
"s_add_u32 s5, s5, 0xc04ac00\nv_lshrrev_b32 v27, 2, v1\nbuffer_load_dword v27, v27, s[16:19], s5 offen sc0 sc1\nWV 0\nv_xor_b32 v27, s15, v27\nv_and_b32 v32, 0xffffdfff, v27\n"
"v_cmp_ne_u32 vcc, 0, v32\ns_nop 0\ns_cbranch_vccz .S431\ns_sleep 6\ns_sub_u32 s7, s7, 1\ns_cbranch_scc0 .S432\ns_branch .S431\n.T443:\ns_mov_b32 s62, 1\n"
"s_call_b64 s[80:81], .RA272\ns_branch .T444\n.T445:\ns_mov_b32 s62, 1\ns_call_b64 s[80:81], .RA272\ns_branch .T446\n.S448:\ns_mov_b32 s7, 262144\n.S450:\n"
"buffer_load_dword v27, off, s[16:19], s5 sc0 sc1\nWV 0\nv_cmp_ne_u32 vcc, s15, v27\ns_nop 0\ns_cbranch_vccz .S449\ns_sleep 6\ns_sub_u32 s7, s7, 1\ns_cbranch_scc0 .S450\n"
"s_branch .S449\n.S451:\ns_mov_b32 s7, 262144\n.S453:\nv_and_b32 v27, 0x70, v1\nbuffer_load_dword v27, v27, s[16:19], s61 offen offset:128 sc0 sc1\nWV 0\n"
"v_cmp_ne_u32 vcc, s15, v27\ns_nop 0\ns_cbranch_vccz .S452\ns_sleep 6\ns_sub_u32 s7, s7, 1\ns_cbranch_scc0 .S453\ns_branch .S452\n.S456:\ns_mov_b32 s7, 262144\n.S458:\n"
"v_and_b32 v27, 0x70, v1\nbuffer_load_dword v27, v27, s[16:19], s61 offen offset:136 sc0 sc1\nWV 0\nv_cmp_ne_u32 vcc, s15, v27\ns_nop 0\ns_cbranch_vccz .S457\ns_sleep 6\n"
"s_sub_u32 s7, s7, 1\ns_cbranch_scc0 .S458\ns_branch .S457\n.S459:\ns_mov_b32 s7, 262144\n.S461:\nv_and_b32 v27, 0x70, v1\n"
"buffer_load_dword v27, v27, s[16:19], s61 offen offset:140 sc0 sc1\nWV 0\nv_cmp_ne_u32 vcc, s15, v27\ns_nop 0\ns_cbranch_vccz .S460\ns_sleep 6\ns_sub_u32 s7, s7, 1\n"
"s_cbranch_scc0 .S461\ns_branch .S460\n.S462:\ns_mov_b32 s7, 262144\n.S464:\nv_and_b32 v27, 0x70, v1\nbuffer_load_dword v27, v27, s[16:19], s61 offen offset:12 sc0 sc1\nWV 0\n"
"v_cmp_ne_u32 vcc, s15, v27\ns_nop 0\ns_cbranch_vccz .S463\ns_sleep 6\ns_sub_u32 s7, s7, 1\ns_cbranch_scc0 .S464\ns_branch .S463\n.S465:\ns_mov_b32 s7, 262144\n.S467:\n"
"v_and_b32 v27, 0x70, v1\nbuffer_load_dword v27, v27, s[16:19], s61 offen offset:8 sc0 sc1\nWV 0\nv_cmp_ne_u32 vcc, s15, v27\ns_nop 0\ns_cbranch_vccz .S466\ns_sleep 6\n"
"s_sub_u32 s7, s7, 1\ns_cbranch_scc0 .S467\ns_branch .S466\n.S468:\ns_mov_b32 s7, 262144\n.S470:\nv_and_b32 v27, 0xf0, v1\n"
"buffer_load_dword v27, v27, s[16:19], s61 offen offset:4 sc0 sc1\nWV 0\nv_cmp_ne_u32 vcc, s15, v27\ns_nop 0\ns_cbranch_vccz .S469\ns_sleep 6\ns_sub_u32 s7, s7, 1\n"
"s_cbranch_scc0 .S470\ns_branch .S469\n.E437:\nv_cmpx_gt_i32 vcc, 19, v0\nv_mov_b32 v19, s97\nv_cmpx_gt_i32 vcc, 18, v0\nv_mov_b32 v18, s97\nv_cmpx_gt_i32 vcc, 17, v0\n"
"v_mov_b32 v17, s97\nv_cmpx_gt_i32 vcc, 16, v0\nv_mov_b32 v16, s97\nv_cmpx_gt_i32 vcc, 3, v0\nv_mov_b32 v11, s97\nv_cmpx_gt_i32 vcc, 2, v0\nv_mov_b32 v10, s97\n"
"v_cmpx_gt_i32 vcc, 1, v0\nv_mov_b32 v9, s97\nv_cmpx_gt_i32 vcc, 0, v0\nv_mov_b32 v8, s97\ns_mov_b64 exec, -1\ns_nop 2\ns_branch .E438\n.E439:\ns_cmp_eq_u32 s99, 0\n"
"s_cbranch_scc1 .K436\nv_cmpx_gt_i32 vcc, 19, v3\nv_mov_b32 v15, s97\nv_cmpx_gt_i32 vcc, 18, v3\nv_mov_b32 v14, s97\nv_cmpx_gt_i32 vcc, 17, v3\nv_mov_b32 v13, s97\n"
"v_cmpx_gt_i32 vcc, 16, v3\nv_mov_b32 v12, s97\nv_cmpx_gt_i32 vcc, 3, v3\nv_mov_b32 v7, s97\nv_cmpx_gt_i32 vcc, 2, v3\nv_mov_b32 v6, s97\nv_cmpx_gt_i32 vcc, 1, v3\n"
"v_mov_b32 v5, s97\nv_cmpx_gt_i32 vcc, 0, v3\nv_mov_b32 v4, s97\ns_mov_b64 exec, -1\ns_nop 2\ns_branch .E440\n.K436:\nv_cmpx_gt_i32 vcc, 19, v3\nv_mov_b32 v19, s97\n"
"v_cmpx_gt_i32 vcc, 18, v3\nv_mov_b32 v18, s97\nv_cmpx_gt_i32 vcc, 17, v3\nv_mov_b32 v17, s97\nv_cmpx_gt_i32 vcc, 16, v3\nv_mov_b32 v16, s97\nv_cmpx_gt_i32 vcc, 3, v3\n"
"v_mov_b32 v11, s97\nv_cmpx_gt_i32 vcc, 2, v3\nv_mov_b32 v10, s97\nv_cmpx_gt_i32 vcc, 1, v3\nv_mov_b32 v9, s97\nv_cmpx_gt_i32 vcc, 0, v3\nv_mov_b32 v8, s97\ns_mov_b64 exec, -1\n"
"s_nop 2\ns_branch .E440\n.E441:\ns_swappc_b64 s[80:81], s[8:9]\ns_branch .E442\n.E454:\nv_mov_b32 v4, s97\nv_mov_b32 v5, s97\nv_mov_b32 v6, s97\nv_mov_b32 v7, s97\n"
"v_mov_b32 v12, s97\nv_mov_b32 v13, s97\nv_mov_b32 v14, s97\nv_mov_b32 v15, s97\nv_cmpx_gt_i32 vcc, 19, v3\nv_mov_b32 v19, s97\nv_cmpx_gt_i32 vcc, 18, v3\nv_mov_b32 v18, s97\n"
"v_cmpx_gt_i32 vcc, 17, v3\nv_mov_b32 v17, s97\nv_cmpx_gt_i32 vcc, 16, v3\nv_mov_b32 v16, s97\nv_cmpx_gt_i32 vcc, 3, v3\nv_mov_b32 v11, s97\nv_cmpx_gt_i32 vcc, 2, v3\n"
"v_mov_b32 v10, s97\nv_cmpx_gt_i32 vcc, 1, v3\nv_mov_b32 v9, s97\nv_cmpx_gt_i32 vcc, 0, v3\nv_mov_b32 v8, s97\ns_mov_b64 exec, -1\ns_nop 2\ns_branch .E455\n.T479:\n"
"s_mov_b32 s62, 0\ns_call_b64 s[80:81], .RB273\ns_branch .T480\n.T481:\ns_mov_b32 s62, 0\ns_call_b64 s[80:81], .RA272\ns_branch .T482\n.T487:\ns_mov_b32 s62, 0\n"
"s_call_b64 s[80:81], .RB273\ns_branch .T488\n.T489:\ns_mov_b32 s62, 0\ns_call_b64 s[80:81], .RA272\ns_branch .T490\n.T500:\ns_mov_b32 s62, 2\ns_call_b64 s[80:81], .RB273\n"
"s_branch .T501\n.T502:\ns_mov_b32 s62, 5\ns_call_b64 s[80:81], .RA272\ns_branch .T503\n.T504:\ns_mov_b32 s62, 5\ns_call_b64 s[80:81], .RA272\ns_branch .T505\n.E485:\n"
"s_swappc_b64 s[80:81], s[8:9]\ns_branch .E486\n.E491:\nv_cmpx_gt_i32 vcc, 19, v3\nv_mov_b32 v15, s97\nv_cmpx_gt_i32 vcc, 18, v3\nv_mov_b32 v14, s97\nv_cmpx_gt_i32 vcc, 17, v3\n"
"v_mov_b32 v13, s97\nv_cmpx_gt_i32 vcc, 16, v3\nv_mov_b32 v12, s97\nv_cmpx_gt_i32 vcc, 3, v3\nv_mov_b32 v7, s97\nv_cmpx_gt_i32 vcc, 2, v3\nv_mov_b32 v6, s97\n"
"v_cmpx_gt_i32 vcc, 1, v3\nv_mov_b32 v5, s97\nv_cmpx_gt_i32 vcc, 0, v3\nv_mov_b32 v4, s97\ns_mov_b64 exec, -1\ns_nop 2\ns_branch .E492\n.E494:\nv_cmpx_gt_i32 vcc, 19, v0\n"
"v_mov_b32 v15, s97\nv_cmpx_gt_i32 vcc, 18, v0\nv_mov_b32 v14, s97\nv_cmpx_gt_i32 vcc, 17, v0\nv_mov_b32 v13, s97\nv_cmpx_gt_i32 vcc, 16, v0\nv_mov_b32 v12, s97\n"
"v_cmpx_gt_i32 vcc, 3, v0\nv_mov_b32 v7, s97\nv_cmpx_gt_i32 vcc, 2, v0\nv_mov_b32 v6, s97\nv_cmpx_gt_i32 vcc, 1, v0\nv_mov_b32 v5, s97\nv_cmpx_gt_i32 vcc, 0, v0\nv_mov_b32 v4, s97\n"
"s_mov_b64 exec, -1\ns_nop 2\ns_branch .E495\n.E496:\ns_cmp_eq_u32 s99, 0\ns_cbranch_scc1 .K493\nv_cmpx_gt_i32 vcc, 19, v3\nv_mov_b32 v15, s97\nv_cmpx_gt_i32 vcc, 18, v3\n"
"v_mov_b32 v14, s97\nv_cmpx_gt_i32 vcc, 17, v3\nv_mov_b32 v13, s97\nv_cmpx_gt_i32 vcc, 16, v3\nv_mov_b32 v12, s97\nv_cmpx_gt_i32 vcc, 3, v3\nv_mov_b32 v7, s97\n"
"v_cmpx_gt_i32 vcc, 2, v3\nv_mov_b32 v6, s97\nv_cmpx_gt_i32 vcc, 1, v3\nv_mov_b32 v5, s97\nv_cmpx_gt_i32 vcc, 0, v3\nv_mov_b32 v4, s97\ns_mov_b64 exec, -1\ns_nop 2\ns_branch .E497\n"
".K493:\nv_cmpx_gt_i32 vcc, 19, v3\nv_mov_b32 v19, s97\nv_cmpx_gt_i32 vcc, 18, v3\nv_mov_b32 v18, s97\nv_cmpx_gt_i32 vcc, 17, v3\nv_mov_b32 v17, s97\nv_cmpx_gt_i32 vcc, 16, v3\n"
"v_mov_b32 v16, s97\nv_cmpx_gt_i32 vcc, 3, v3\nv_mov_b32 v11, s97\nv_cmpx_gt_i32 vcc, 2, v3\nv_mov_b32 v10, s97\nv_cmpx_gt_i32 vcc, 1, v3\nv_mov_b32 v9, s97\n"
"v_cmpx_gt_i32 vcc, 0, v3\nv_mov_b32 v8, s97\ns_mov_b64 exec, -1\ns_nop 2\ns_branch .E497\n.E498:\ns_swappc_b64 s[80:81], s[8:9]\ns_branch .E499\n.T516:\ns_mov_b32 s62, 4\n"
"s_call_b64 s[80:81], .RB273\ns_branch .T517\n.T518:\ns_mov_b32 s62, 4\ns_call_b64 s[80:81], .RA272\ns_branch .T519\n.T524:\ns_mov_b32 s62, 4\ns_call_b64 s[80:81], .RB273\n"
"s_branch .T525\n.T526:\ns_mov_b32 s62, 4\ns_call_b64 s[80:81], .RA272\ns_branch .T527\n.T528:\ns_mov_b32 s62, 6\ns_call_b64 s[80:81], .RB273\ns_branch .T529\n.S530:\n"
"s_mov_b32 s7, 262144\n.S532:\ns_add_u32 s0, s92, 0\nbuffer_load_dwordx4 v[208:211], v1, s[16:19], s0 offen offset:0 sc0 sc1\n"
"buffer_load_dwordx4 v[212:215], v1, s[16:19], s0 offen offset:1024 sc0 sc1\nWV 0\nv_max_u32 v21, v211, v215\nv_cmp_eq_u32 vcc, -1, v21\ns_nop 0\ns_cbranch_vccz .S531\ns_sleep 6\n"
"s_sub_u32 s7, s7, 1\ns_cbranch_scc0 .S532\ns_branch .S531\n.S533:\ns_mov_b32 s7, 262144\n.S535:\ns_add_u32 s0, s92, 131072\n"
"buffer_load_dwordx4 v[216:219], v1, s[16:19], s0 offen offset:0 sc0 sc1\nbuffer_load_dwordx4 v[220:223], v1, s[16:19], s0 offen offset:1024 sc0 sc1\nWV 0\n"
"v_max_u32 v21, v219, v223\nv_cmp_eq_u32 vcc, -1, v21\ns_nop 0\ns_cbranch_vccz .S534\ns_sleep 6\ns_sub_u32 s7, s7, 1\ns_cbranch_scc0 .S535\ns_branch .S534\n.S536:\n"
"s_mov_b32 s7, 262144\n.S538:\ns_add_u32 s0, s92, 262144\nbuffer_load_dwordx4 v[224:227], v1, s[16:19], s0 offen offset:0 sc0 sc1\n"
"buffer_load_dwordx4 v[228:231], v1, s[16:19], s0 offen offset:1024 sc0 sc1\nWV 0\nv_max_u32 v21, v227, v231\nv_cmp_eq_u32 vcc, -1, v21\ns_nop 0\ns_cbranch_vccz .S537\ns_sleep 6\n"
"s_sub_u32 s7, s7, 1\ns_cbranch_scc0 .S538\ns_branch .S537\n.S539:\ns_mov_b32 s7, 262144\n.S541:\ns_add_u32 s0, s92, 393216\n"
"buffer_load_dwordx4 v[208:211], v1, s[16:19], s0 offen offset:0 sc0 sc1\nbuffer_load_dwordx4 v[212:215], v1, s[16:19], s0 offen offset:1024 sc0 sc1\nWV 0\n"
"v_max_u32 v21, v211, v215\nv_cmp_eq_u32 vcc, -1, v21\ns_nop 0\ns_cbranch_vccz .S540\ns_sleep 6\ns_sub_u32 s7, s7, 1\ns_cbranch_scc0 .S541\ns_branch .S540\n.S542:\n"
"s_mov_b32 s7, 262144\n.S544:\ns_add_u32 s0, s92, 524288\nbuffer_load_dwordx4 v[216:219], v1, s[16:19], s0 offen offset:0 sc0 sc1\n"
"buffer_load_dwordx4 v[220:223], v1, s[16:19], s0 offen offset:1024 sc0 sc1\nWV 0\nv_max_u32 v21, v219, v223\nv_cmp_eq_u32 vcc, -1, v21\ns_nop 0\ns_cbranch_vccz .S543\ns_sleep 6\n"
"s_sub_u32 s7, s7, 1\ns_cbranch_scc0 .S544\ns_branch .S543\n.S545:\ns_mov_b32 s7, 262144\n.S547:\ns_add_u32 s0, s92, 655360\n"
"buffer_load_dwordx4 v[224:227], v1, s[16:19], s0 offen offset:0 sc0 sc1\nbuffer_load_dwordx4 v[228:231], v1, s[16:19], s0 offen offset:1024 sc0 sc1\nWV 0\n"
"v_max_u32 v21, v227, v231\nv_cmp_eq_u32 vcc, -1, v21\ns_nop 0\ns_cbranch_vccz .S546\ns_sleep 6\ns_sub_u32 s7, s7, 1\ns_cbranch_scc0 .S547\ns_branch .S546\n.S548:\n"
"s_mov_b32 s7, 262144\n.S550:\ns_add_u32 s0, s92, 786432\nbuffer_load_dwordx4 v[208:211], v1, s[16:19], s0 offen offset:0 sc0 sc1\n"
"buffer_load_dwordx4 v[212:215], v1, s[16:19], s0 offen offset:1024 sc0 sc1\nWV 0\nv_max_u32 v21, v211, v215\nv_cmp_eq_u32 vcc, -1, v21\ns_nop 0\ns_cbranch_vccz .S549\ns_sleep 6\n"
"s_sub_u32 s7, s7, 1\ns_cbranch_scc0 .S550\ns_branch .S549\n.S551:\ns_mov_b32 s7, 262144\n.S553:\nbuffer_load_dwordx4 v[64:67], v89, s[16:19], s40 offen offset:0 sc0 sc1\n"
"buffer_load_dwordx4 v[68:71], v89, s[16:19], s40 offen offset:16 sc0 sc1\nbuffer_load_dwordx4 v[72:75], v89, s[16:19], s40 offen offset:1792 sc0 sc1\n"
"buffer_load_dwordx4 v[76:79], v89, s[16:19], s40 offen offset:1808 sc0 sc1\nbuffer_load_dwordx2 v[80:81], v88, s[16:19], s63 offen offset:0 sc0 sc1\n"
"buffer_load_dwordx2 v[82:83], v88, s[16:19], s63 offen offset:512 sc0 sc1\nWV 0\nv_xor_b32 v66, s15, v66\nv_xor_b32 v70, s15, v70\nv_xor_b32 v74, s15, v74\n"
"v_xor_b32 v78, s15, v78\nv_xor_b32 v81, s15, v81\nv_xor_b32 v83, s15, v83\nv_or3_b32 v92, v66, v70, v74\nv_or3_b32 v92, v92, v78, v81\nv_or_b32 v92, v92, v83\n"
"v_cmp_ne_u32 vcc, 0, v92\ns_nop 0\ns_cbranch_vccz .S552\ns_sleep 6\ns_sub_u32 s7, s7, 1\ns_cbranch_scc0 .S553\ns_branch .S552\n.S554:\ns_mov_b32 s7, 262144\n.S556:\n"
"buffer_load_dword v27, off, s[16:19], s60 offset:512 sc0 sc1\nWV 0\nv_cmp_ne_u32 vcc, s15, v27\ns_nop 0\ns_cbranch_vccz .S555\ns_sleep 6\ns_sub_u32 s7, s7, 1\n"
"s_cbranch_scc0 .S556\ns_branch .S555\n.S557:\ns_mov_b32 s7, 262144\n.S559:\nbuffer_load_dword v27, v24, s[16:19], s41 offen sc0 sc1\nWV 0\nv_xor_b32 v27, s15, v27\n"
"v_and_b32 v26, 0xffffdfff, v27\nv_cmp_ne_u32 s[64:65], 0, v27\nv_cmp_ne_u32 vcc, 0, v26\ns_nop 0\ns_cbranch_vccz .S558\ns_sleep 6\ns_sub_u32 s7, s7, 1\ns_cbranch_scc0 .S559\n"
"s_branch .S558\n.Z561:\ns_sub_u32 s7, s7, 1\ns_cbranch_scc1 .Z562\ns_cmp_lg_u64 s[44:45], 0\ns_cbranch_scc0 .Z564\n"
"buffer_load_dwordx4 v[96:99], v1, s[16:19], s24 offen offset:0 sc0 sc1\nbuffer_load_dwordx4 v[100:103], v1, s[16:19], s24 offen offset:1024 sc0 sc1\n.Z564:\n"
"s_cmp_lg_u64 s[46:47], 0\ns_cbranch_scc0 .Z565\nbuffer_load_dwordx4 v[104:107], v1, s[16:19], s25 offen offset:0 sc0 sc1\n"
"buffer_load_dwordx4 v[108:111], v1, s[16:19], s25 offen offset:1024 sc0 sc1\n.Z565:\ns_cmp_lg_u64 s[48:49], 0\ns_cbranch_scc0 .Z566\n"
"buffer_load_dwordx4 v[112:115], v1, s[16:19], s26 offen offset:0 sc0 sc1\nbuffer_load_dwordx4 v[116:119], v1, s[16:19], s26 offen offset:1024 sc0 sc1\n.Z566:\n"
"s_cmp_lg_u64 s[50:51], 0\ns_cbranch_scc0 .Z567\nbuffer_load_dwordx4 v[120:123], v1, s[16:19], s27 offen offset:0 sc0 sc1\n"
"buffer_load_dwordx4 v[124:127], v1, s[16:19], s27 offen offset:1024 sc0 sc1\n.Z567:\ns_cmp_lg_u64 s[52:53], 0\ns_cbranch_scc0 .Z568\n"
"buffer_load_dwordx4 v[32:35], v1, s[16:19], s32 offen offset:0 sc0 sc1\nbuffer_load_dwordx4 v[36:39], v1, s[16:19], s32 offen offset:1024 sc0 sc1\n.Z568:\n"
"s_cmp_lg_u64 s[54:55], 0\ns_cbranch_scc0 .Z569\nbuffer_load_dwordx4 v[40:43], v1, s[16:19], s33 offen offset:0 sc0 sc1\n"
"buffer_load_dwordx4 v[44:47], v1, s[16:19], s33 offen offset:1024 sc0 sc1\n.Z569:\ns_cmp_lg_u64 s[56:57], 0\ns_cbranch_scc0 .Z570\n"
"buffer_load_dwordx4 v[48:51], v1, s[16:19], s34 offen offset:0 sc0 sc1\nbuffer_load_dwordx4 v[52:55], v1, s[16:19], s34 offen offset:1024 sc0 sc1\n.Z570:\n"
"s_cmp_lg_u64 s[58:59], 0\ns_cbranch_scc0 .Z571\nbuffer_load_dwordx4 v[56:59], v1, s[16:19], s35 offen offset:0 sc0 sc1\n"
"buffer_load_dwordx4 v[60:63], v1, s[16:19], s35 offen offset:1024 sc0 sc1\n.Z571:\ns_sleep 8\nWV 0\ns_branch .Z563\n.Lcode1:\n"
  :: "s"(__builtin_amdgcn_kernarg_segment_ptr()), "s"(blockIdx.x), "v"(threadIdx.x)
  : "memory", "scc", "vcc", "v1", "v2", "v3", "v4", "v5", "v6", "v7", "v8", "v9", "v10", "v11", "v12", "v13", "v14", "v15", "v16", "v17", "v18", "v19", "v20", "v21", "v22", "v23", "v24", "v25", "v26", "v27", "v28", "v29", "v30", "v31", "v32", "v33", "v34", "v35", "v36", "v37", "v38", "v39", "v40", "v41", "v42", "v43", "v44", "v45", "v46", "v47", "v48", "v49", "v50", "v51", "v52", "v53", "v54", "v55", "v56", "v57", "v58", "v59", "v60", "v61", "v62", "v63", "v64", "v65", "v66", "v67", "v68", "v69", "v70", "v71", "v72", "v73", "v74", "v75", "v76", "v77", "v78", "v79", "v80", "v81", "v82", "v83", "v84", "v85", "v86", "v87", "v88", "v89", "v90", "v91", "v92", "v93", "v94", "v95", "v96", "v97", "v98", "v99", "v100", "v101", "v102", "v103", "v104", "v105", "v106", "v107", "v108", "v109", "v110", "v111", "v112", "v113", "v114", "v115", "v116", "v117", "v118", "v119", "v120", "v121", "v122", "v123", "v124", "v125", "v126", "v127", "v128", "v129", "v130", "v131", "v132", "v133", "v134", "v135", "v136", "v137", "v138", "v139", "v140", "v141", "v142", "v143", "v144", "v145", "v146", "v147", "v148", "v149", "v150", "v151", "v152", "v153", "v154", "v155", "v156", "v157", "v158", "v159", "v160", "v161", "v162", "v163", "v164", "v165", "v166", "v167", "v168", "v169", "v170", "v171", "v172", "v173", "v174", "v175", "v176", "v177", "v178", "v179", "v180", "v181", "v182", "v183", "v184", "v185", "v186", "v187", "v188", "v189", "v190", "v191", "v192", "v193", "v194", "v195", "v196", "v197", "v198", "v199", "v200", "v201", "v202", "v203", "v204", "v205", "v206", "v207", "v208", "v209", "v210", "v211", "v212", "v213", "v214", "v215", "v216", "v217", "v218", "v219", "v220", "v221", "v222", "v223", "v224", "v225", "v226", "v227", "v228", "v229", "v230", "v231", "v232", "v233", "v234", "v235", "v236", "v237", "v238", "v239", "v240", "v241", "v242", "v243", "v244", "v245", "v246", "v247", "v248", "v249", "v250", "v251", "v252", "v253", "v254", "v255", "a0", "a1", "a2", "a3", "a4", "a5", "a6", "a7", "a8", "a9", "a10", "a11", "a12", "a13", "a14", "a15", "a16", "a17", "a18", "a19", "a20", "a21", "a22", "a23", "a24", "a25", "a26", "a27", "a28", "a29", "a30", "a31", "a32", "a33", "a34", "a35", "a36", "a37", "a38", "a39", "a40", "a41", "a42", "a43", "a44", "a45", "a46", "a47", "a48", "a49", "a50", "a51", "a52", "a53", "a54", "a55", "a56", "a57", "a58", "a59", "a60", "a61", "a62", "a63", "a64", "a65", "a66", "a67", "a68", "a69", "a70", "a71", "a72", "a73", "a74", "a75", "a76", "a77", "a78", "a79", "a80", "a81", "a82", "a83", "a84", "a85", "a86", "a87", "a88", "a89", "a90", "a91", "a92", "a93", "a94", "a95", "a96", "a97", "a98", "a99", "a100", "a101", "a102", "a103", "a104", "a105", "a106", "a107", "a108", "a109", "a110", "a111", "a112", "a113", "a114", "a115", "a116", "a117", "a118", "a119", "a120", "a121", "a122", "a123", "a124", "a125", "a126", "a127", "a128", "a129", "a130", "a131", "a132", "a133", "a134", "a135", "a136", "a137", "a138", "a139", "a140", "a141", "a142", "a143", "a144", "a145", "a146", "a147", "a148", "a149", "a150", "a151", "a152", "a153", "a154", "a155", "a156", "a157", "a158", "a159", "a160", "a161", "a162", "a163", "a164", "a165", "a166", "a167", "a168", "a169", "a170", "a171", "a172", "a173", "a174", "a175", "a176", "a177", "a178", "a179", "a180", "a181", "a182", "a183", "a184", "a185", "a186", "a187", "a188", "a189", "a190", "a191", "a192", "a193", "a194", "a195", "a196", "a197", "a198", "a199", "a200", "a201", "a202", "a203", "a204", "a205", "a206", "a207", "a208", "a209", "a210", "a211", "a212", "a213", "a214", "a215", "a216", "a217", "a218", "a219", "a220", "a221", "a222", "a223", "a224", "a225", "a226", "a227", "a228", "a229", "a230", "a231", "a232", "a233", "a234", "a235", "a236", "a237", "a238", "a239", "a240", "a241", "a242", "a243", "a244", "a245", "a246", "a247", "a248", "a249", "a250", "a251", "a252", "a253", "a254", "a255", "s8", "s9", "s10", "s11", "s12", "s13", "s14", "s15", "s16", "s17", "s18", "s19", "s20", "s21", "s22", "s23", "s24", "s25", "s26", "s27", "s28", "s29", "s30", "s31", "s33", "s34", "s35", "s36", "s37", "s38", "s39", "s40", "s41", "s42", "s43", "s44", "s45", "s46", "s47", "s48", "s49", "s50", "s51", "s52", "s53", "s54", "s55", "s56", "s57", "s58", "s59", "s60", "s61", "s62", "s63", "s64", "s65", "s66", "s67", "s68", "s69", "s70", "s71", "s72", "s73", "s74", "s75", "s76", "s77", "s78", "s79", "s80", "s81", "s82", "s83", "s84", "s85", "s86", "s87", "s88", "s89", "s90", "s91", "s92", "s93", "s94", "s95", "s96", "s97", "s98", "s99");
#endif
}

extern "C" void kernel_launch(void* const* d_in, const int* in_sizes, int n_in,
                              void* d_out, int out_size, void* d_ws, size_t ws_size,
                              hipStream_t stream) {
  (void)hipFuncSetAttribute((const void*)k_main, hipFuncAttributeMaxDynamicSharedMemorySize, 163840);
  k_main<<<256, 256, 163840, stream>>>(d_in[0], d_in[1], d_in[2], d_in[3], d_in[4], d_ws, d_in[11], d_in[7], d_in[5], d_in[8], d_in[9], d_in[10], d_out);
}
```

```cpp
#include <hip/hip_runtime.h>
#include <stdint.h>

extern "C" __global__ __launch_bounds__(256, 1)
void k_main(const void* x, const void* ckv, const void* wq, const void* wuk, const void* wuv, void* ws,
            const void* gate_w, const void* w_r, const void* wo, const void* wg, const void* wu, const void* wd, void* out) {
#if defined(__HIP_DEVICE_COMPILE__)
  asm volatile(
"s_mov_b64 s[8:9], %0\ns_mov_b32 s10, %1\nv_mov_b32 v0, %2\n.macro WL n\ns_waitcnt lgkmcnt(\\n)\n.endm\n.macro D0 x,r,s,o\n"
"buffer_load_dwordx4 v\\x, s[\\r:\\r+3], s\\s offen offset:\\o lds\n.endm\n.macro WV n\ns_waitcnt vmcnt(\\n)\n.endm\n.macro Ra d,x,o\nds_read_b128 a[\\d:\\d+3], v\\x offset:\\o\n"
".endm\n.macro Ta d,x,o\nds_read_b64_tr_b16 a[\\d:\\d+1], v\\x offset:\\o\n.endm\n.macro Zvaa d,x,y\nv_mfma_f32_16x16x32_bf16 v[\\d:\\d+3], a[\\x:\\x+3], a[\\y:\\y+3], 0\n.endm\n"
".macro Mvaa d,x,y\nv_mfma_f32_16x16x32_bf16 v[\\d:\\d+3], a[\\x:\\x+3], a[\\y:\\y+3], v[\\d:\\d+3]\n.endm\n.macro Zaaa d,x,y\n"
"v_mfma_f32_16x16x32_bf16 a[\\d:\\d+3], a[\\x:\\x+3], a[\\y:\\y+3], 0\n.endm\n.macro Maaa d,x,y\nv_mfma_f32_16x16x32_bf16 a[\\d:\\d+3], a[\\x:\\x+3], a[\\y:\\y+3], a[\\d:\\d+3]\n"
".endm\n.macro MU d,x,y\nv_mul_f32 v\\d, v\\x, v\\y\n.endm\n.macro CV d,x,y\nv_cvt_pk_bf16_f32 v\\d, v\\x, v\\y\n.endm\n.macro FM d,x,y\nv_fmac_f32 v\\d, v\\x, v\\y\n.endm\n"
".macro AD d,x,y\nv_add_f32 v\\d, v\\x, v\\y\n.endm\n.macro EX d,x\nv_exp_f32 v\\d, v\\x\n.endm\n.macro AR d,x\nv_accvgpr_read_b32 v\\d, a\\x\n.endm\n.macro Sv d,x,r,s,o\n"
"buffer_store_dwordx4 v[\\d:\\d+3], v\\x, s[\\r:\\r+3], s\\s offen offset:\\o sc0 sc1\n.endm\n.macro Rv d,x,o\nds_read_b128 v[\\d:\\d+3], v\\x offset:\\o\n.endm\n.macro D1 x,r,s,o\n"
"buffer_load_dwordx4 v\\x, s[\\r:\\r+3], s\\s offen offset:\\o sc1 lds\n.endm\n.macro Cvav d,x,y,c\nv_mfma_f32_16x16x32_bf16 v[\\d:\\d+3], a[\\x:\\x+3], v[\\y:\\y+3], v[\\c:\\c+3]\n"
".endm\n.macro Mvav d,x,y\nv_mfma_f32_16x16x32_bf16 v[\\d:\\d+3], a[\\x:\\x+3], v[\\y:\\y+3], v[\\d:\\d+3]\n.endm\n.macro Zaav d,x,y\n"
"v_mfma_f32_16x16x32_bf16 a[\\d:\\d+3], a[\\x:\\x+3], v[\\y:\\y+3], 0\n.endm\n.macro Maav d,x,y\nv_mfma_f32_16x16x32_bf16 a[\\d:\\d+3], a[\\x:\\x+3], v[\\y:\\y+3], a[\\d:\\d+3]\n"
".endm\n.macro SU d,x,y\nv_sub_f32 v\\d, v\\x, v\\y\n.endm\n.macro Mava d,x,y\nv_mfma_f32_16x16x32_bf16 a[\\d:\\d+3], v[\\x:\\x+3], a[\\y:\\y+3], a[\\d:\\d+3]\n.endm\n"
".macro Zvav d,x,y\nv_mfma_f32_16x16x32_bf16 v[\\d:\\d+3], a[\\x:\\x+3], v[\\y:\\y+3], 0\n.endm\n.macro Zvvv d,x,y\n"
"v_mfma_f32_16x16x32_bf16 v[\\d:\\d+3], v[\\x:\\x+3], v[\\y:\\y+3], 0\n.endm\n.macro Mvvv d,x,y\nv_mfma_f32_16x16x32_bf16 v[\\d:\\d+3], v[\\x:\\x+3], v[\\y:\\y+3], v[\\d:\\d+3]\n"
".endm\n.macro AW d,x\nv_accvgpr_write_b32 a\\d, v\\x\n.endm\n.macro Tv d,x,o\nds_read_b64_tr_b16 v[\\d:\\d+1], v\\x offset:\\o\n.endm\n.macro Sa d,x,r,s,o\n"
"buffer_store_dwordx4 a[\\d:\\d+3], v\\x, s[\\r:\\r+3], s\\s offen offset:\\o sc0 sc1\n.endm\n.macro Cvaa d,x,y,c\n"
"v_mfma_f32_16x16x32_bf16 v[\\d:\\d+3], a[\\x:\\x+3], a[\\y:\\y+3], v[\\c:\\c+3]\n.endm\n.Lcode0:\ns_mov_b32 s70, 0\ns_load_dwordx16 s[44:59], s[8:9], 0x0\n"
"s_load_dwordx8 s[60:67], s[8:9], 0x40\ns_load_dwordx2 s[68:69], s[8:9], 0x60\nv_lshrrev_b32 v2, 6, v0\nv_and_b32 v0, 63, v0\nv_readfirstlane_b32 s11, v2\nv_lshlrev_b32 v1, 4, v0\n"
"s_and_b32 s2, s10, 0x17\ns_bfe_u32 s3, s10, 0x10003\ns_lshl_b32 s3, s3, 7\ns_or_b32 s2, s2, s3\ns_bfe_u32 s3, s10, 0x10005\ns_lshl3_add_u32 s2, s3, s2\ns_lshr_b32 s3, s10, 6\n"
"s_lshl_b32 s3, s3, 5\ns_or_b32 s10, s2, s3\ns_and_b32 s73, s10, 7\ns_lshr_b32 s74, s10, 3\ns_and_b32 s2, s74, 1\ns_lshl1_add_u32 s12, s73, s2\ns_bfe_u32 s13, s74, 0x30001\n"
"s_lshr_b32 s14, s74, 4\ns_xor_b32 s14, s14, 1\nv_lshrrev_b32 v32, 5, v0\ns_and_b32 s3, s11, 1\ns_lshl_b32 s2, s3, 3\nv_lshl_or_b32 v33, v32, 1, s2\nv_and_b32 v34, 31, v0\n"
"v_xor_b32 v33, v33, v34\nv_lshlrev_b32 v33, 4, v33\nv_lshl_or_b32 v6, v32, 13, v33\nv_xor_b32 v33, 64, v6\nv_add_u32 v7, 15360, v33\nv_add_u32 v8, 30720, v6\n"
"v_add_u32 v9, 46080, v33\ns_lshl_b32 s2, s11, 16\ns_lshl_b32 s4, s12, 9\ns_add_u32 s5, s2, s4\ns_getpc_b64 s[40:41]\n.PC8:\ns_sub_u32 s40, s40, .PC8-.Lcode0\n"
"s_subb_u32 s41, s41, 0\ns_mov_b32 s42, .Lcode1-.Lcode0\ns_mov_b32 s43, 0x20000\ns_lshl2_add_u32 s6, s74, s11\ns_lshl_b32 s6, s6, 7\nv_lshlrev_b32 v54, 10, v1\n"
"buffer_load_dword v31, v54, s[40:43], s6 offen\nv_lshrrev_b32 v32, 4, v0\nv_and_b32 v33, 7, v0\nv_xor_b32 v33, v33, v32\nv_lshlrev_b32 v33, 4, v33\nv_lshrrev_b32 v32, 3, v0\n"
"v_lshl_or_b32 v2, v32, 11, v33\nv_xor_b32 v33, 64, v2\nv_add_u32 v3, 15360, v33\nv_add_u32 v4, 30720, v2\nv_add_u32 v5, 46080, v33\nv_lshrrev_b32 v144, 4, v0\n"
"v_and_b32 v145, 7, v0\nv_xor_b32 v145, v145, v144\nv_lshlrev_b32 v145, 4, v145\nv_lshrrev_b32 v144, 3, v0\nv_mul_u32_u24 v144, 896, v144\nv_add_u32 v136, v144, v145\n"
"v_xor_b32 v145, 64, v136\nv_add_u32 v137, 6144, v145\nv_add_u32 v138, 55296, v136\nv_add_u32 v139, 61440, v145\ns_lshr_b32 s88, s11, 1\ns_lshr_b32 s2, s13, 2\n"
"s_bfe_u32 s3, s13, 0x10001\ns_mul_i32 s3, s3, 17\ns_add_u32 s2, s2, s3\ns_lshl2_add_u32 s3, s14, s88\ns_lshl_b32 s3, s3, 2\ns_lshr_b32 s3, 0xe420ca86, s3\ns_and_b32 s3, s3, 15\n"
"s_add_u32 s2, s2, s3\ns_cmp_eq_u32 s2, 32\ns_cselect_b32 s86, 16, s2\ns_add_u32 s88, s88, 2\ns_lshr_b32 s2, s13, 2\ns_bfe_u32 s3, s13, 0x10001\ns_mul_i32 s3, s3, 17\n"
"s_add_u32 s2, s2, s3\ns_lshl2_add_u32 s3, s14, s88\ns_lshl_b32 s3, s3, 2\ns_lshr_b32 s3, 0xe420ca86, s3\ns_and_b32 s3, s3, 15\ns_add_u32 s2, s2, s3\ns_cmp_eq_u32 s2, 32\n"
"s_cselect_b32 s88, 16, s2\ns_and_b32 s89, s11, 1\ns_lshl1_add_u32 s86, s86, s89\ns_lshl1_add_u32 s88, s88, s89\ns_sub_u32 s2, 15, s13\ns_lshl3_add_u32 s2, s2, s11\n"
"s_mul_i32 s87, s2, 14336\ns_mov_b32 s18, 0xc0ae800\ns_mov_b32 s30, 0x1c0000\ns_mov_b32 s34, 0x380000\ns_mov_b32 s22, 0x400000\ns_mov_b32 s26, 0x800000\ns_mov_b32 s19, 0x20000\n"
"s_mov_b32 s31, 0x20000\ns_mov_b32 s35, 0x20000\ns_mov_b32 s23, 0x20000\ns_mov_b32 s27, 0x20000\nWL 0\ns_mov_b32 s28, s44\ns_and_b32 s29, s45, 0xffff\ns_mov_b32 s32, s48\n"
"s_and_b32 s33, s49, 0xffff\ns_mov_b32 s20, s46\ns_and_b32 s21, s47, 0xffff\ns_bitcmp1_b32 s13, 0\ns_cselect_b64 s[50:51], s[52:53], s[50:51]\ns_mov_b32 s24, s50\n"
"s_and_b32 s25, s51, 0xffff\ns_sub_u32 s44, s5, 0x40000\ns_mov_b32 s51, s5\ns_lshl_b32 s47, s11, 12\ns_add_u32 s47, s47, 0x10000\ns_lshl_b32 s45, s86, 16\ns_sub_u32 s45, s45, 128\n"
"s_lshl_b32 s48, s88, 16\ns_sub_u32 s48, s48, 128\ns_mov_b32 s52, s87\ns_cmp_eq_u32 s14, 1\ns_cbranch_scc1 .B6\ns_add_u32 m0, s47, 65536\ns_add_u32 s46, s51, 0\nD0 6,32,46,0\n"
"D0 7,32,46,1024\nD0 8,32,46,2048\nD0 9,32,46,3072\ns_add_u32 m0, s47, -65536\ns_add_u32 s46, s52, 0\nD0 136,28,46,0\nD0 137,28,46,1024\nD0 138,28,46,2048\nD0 139,28,46,3072\n"
"s_add_u32 m0, s47, 81920\ns_add_u32 s46, s51, 262144\nD0 6,32,46,0\nD0 7,32,46,1024\nD0 8,32,46,2048\nD0 9,32,46,3072\ns_branch .B7\n.B6:\ns_add_u32 m0, s47, 0\n"
"s_add_u32 s44, s44, 0x40000\nD0 6,24,44,0\nD0 7,24,44,1024\nD0 8,24,44,2048\nD0 9,24,44,3072\ns_add_u32 m0, s47, -65536\ns_add_u32 s45, s45, 128\nD0 2,20,45,0\nD0 3,20,45,1024\n"
"D0 4,20,45,2048\nD0 5,20,45,3072\ns_add_u32 m0, s47, 16384\ns_add_u32 s44, s44, 0x40000\nD0 6,24,44,0\nD0 7,24,44,1024\nD0 8,24,44,2048\nD0 9,24,44,3072\n.B7:\n"
"s_add_u32 s54, s54, 0x1fffff\ns_addc_u32 s55, s55, 0\ns_and_b32 s54, s54, 0xffe00000\ns_mov_b32 s16, s54\ns_and_b32 s17, s55, 0xffff\ns_mov_b32 s2, 0xc048000\n"
"s_load_dword s15, s[54:55], s2\nv_lshrrev_b32 v24, 3, v0\nv_lshlrev_b32 v24, 13, v24\nv_and_b32 v26, 7, v0\nv_lshl_or_b32 v24, v26, 7, v24\nv_lshrrev_b32 v26, 4, v0\n"
"v_lshl_or_b32 v24, v26, 28, v24\nv_lshrrev_b32 v26, 5, v0\nv_lshlrev_b32 v26, 30, v26\nv_lshl_or_b32 v26, v0, 7, v26\ns_and_b32 s2, s13, 6\ns_and_b32 s3, s12, 1\n"
"s_add_u32 s2, s2, s3\ns_lshl2_add_u32 s2, s2, s11\ns_lshl_b32 s2, s2, 14\ns_lshl_b32 s3, s73, 10\ns_add_u32 s2, s2, s3\ns_add_u32 s96, s2, 1048576\ns_and_b32 s2, s10, 127\n"
"s_lshl2_add_u32 s2, s2, s11\ns_lshl_b32 s97, s2, 12\nv_and_b32 v48, 15, v0\nv_lshrrev_b32 v49, 4, v0\nv_lshrrev_b32 v50, 1, v48\nv_xor_b32 v50, v50, v49\n"
"v_lshlrev_b32 v50, 4, v50\nv_lshl_or_b32 v50, v48, 7, v50\ns_lshr_b32 s2, s11, 1\ns_lshl_b32 s2, s2, 12\nv_add_u32 v10, s2, v50\nv_xor_b32 v11, 64, v10\nv_lshrrev_b32 v50, 2, v48\n"
"v_lshl_add_u32 v51, v49, 3, v50\nv_and_b32 v52, 1, v49\nv_lshl_or_b32 v50, v52, 2, v50\nv_lshlrev_b32 v50, 5, v50\nv_lshl_or_b32 v50, v51, 9, v50\nv_and_b32 v52, 3, v0\n"
"v_lshl_or_b32 v50, v52, 3, v50\ns_and_b32 s2, s11, 1\ns_lshl_b32 s2, s2, 8\ns_add_u32 s2, s2, 0x10000\nv_add_u32 v12, s2, v50\nv_xor_b32 v13, 32, v12\nv_xor_b32 v14, 64, v12\n"
"v_xor_b32 v15, 96, v12\nv_xor_b32 v16, 128, v12\nv_xor_b32 v17, 160, v12\nv_xor_b32 v18, 192, v12\nv_xor_b32 v19, 224, v12\ns_getpc_b64 s[0:1]\n.PC9:\ns_mov_b32 s2, .PA2-.PC9\n"
"s_mov_b32 s3, .PA4-.PC9\ns_bitcmp1_b32 s13, 0\ns_cselect_b32 s2, .PA3-.PC9, s2\ns_cselect_b32 s3, .PA5-.PC9, s3\ns_cmp_eq_u32 s14, 0\ns_cselect_b32 s2, s2, s3\n"
"s_add_u32 s0, s0, s2\ns_addc_u32 s1, s1, 0\ns_setpc_b64 s[0:1]\n.p2align 12\n.PA2:\nv_add_u32 v128, 0x10000, v12\nv_add_u32 v129, 0x10000, v13\nv_add_u32 v130, 0x10000, v14\n"
"v_add_u32 v131, 0x10000, v15\nv_add_u32 v132, 0x10000, v16\nv_add_u32 v133, 0x10000, v17\nv_add_u32 v134, 0x10000, v18\nv_add_u32 v135, 0x10000, v19\ns_add_u32 s2, s11, 1\n"
"s_lshr_b32 s2, s2, 1\ns_lshl_b32 s2, s2, 12\nv_add_u32 v140, s2, v10\nv_add_u32 v141, s2, v11\nv_xor_b32 v142, 0x1000, v140\nv_xor_b32 v143, 0x1000, v141\ns_add_u32 m0, s47, 0\n"
"s_add_u32 s46, s51, 524288\nD0 6,32,46,0\nD0 7,32,46,1024\nD0 8,32,46,2048\nD0 9,32,46,3072\ns_add_u32 m0, s47, -49152\ns_add_u32 s46, s52, 128\nD0 136,28,46,0\nD0 137,28,46,1024\n"
"D0 138,28,46,2048\nD0 139,28,46,3072\ns_add_u32 m0, s47, 16384\ns_add_u32 s46, s51, 786432\nD0 6,32,46,0\nD0 7,32,46,1024\nD0 8,32,46,2048\nD0 9,32,46,3072\n"
"v_mov_b32 v116, 0x40000000\nv_cmp_gt_u32 vcc, 56, v0\ns_lshl2_add_u32 s2, s10, s11\ns_mul_i32 s2, s2, 1792\nv_cndmask_b32 v117, v116, v1, vcc\ns_mov_b32 s36, s56\n"
"s_and_b32 s37, s57, 0xffff\ns_mov_b32 s38, 0x380\ns_mov_b32 s39, 0x20000\ns_mov_b32 s40, s58\ns_and_b32 s41, s59, 0xffff\ns_mov_b32 s42, 0x380\ns_mov_b32 s43, 0x20000\n"
"buffer_load_dwordx4 v[96:99], v117, s[28:31], s2 offen offset:0\nbuffer_load_dwordx4 v[100:103], v117, s[28:31], s2 offen offset:896\n"
"buffer_load_dwordx4 v[104:107], v117, s[36:39], 0 offen offset:0\nbuffer_load_dwordx4 v[108:111], v117, s[40:43], 0 offen offset:0\ns_add_u32 m0, s47, 32768\n"
"s_add_u32 s46, s51, 1048576\nD0 6,32,46,0\nD0 7,32,46,1024\nD0 8,32,46,2048\nD0 9,32,46,3072\ns_add_u32 m0, s47, -32768\ns_add_u32 s46, s52, 256\nD0 136,28,46,0\n"
"D0 137,28,46,1024\nD0 138,28,46,2048\nD0 139,28,46,3072\nWL 0\ns_mul_i32 s15, s15, 1664525\ns_add_u32 s15, s15, 1013904223\nv_mov_b32 v25, s15\ns_mov_b32 s57, 0x3db8aa3b\n"
"s_and_b32 s59, s11, 1\ns_lshl_b32 s59, s59, 13\ns_mul_i32 s2, s73, 25165824\ns_and_b32 s3, s12, 1\ns_lshl_b32 s3, s3, 21\ns_add_u32 s2, s2, s3\ns_lshl2_add_u32 s3, s13, s11\n"
"s_lshl_b32 s3, s3, 15\ns_add_u32 s2, s2, s3\ns_add_u32 s56, s2, 0x800000\ns_xor_b32 s53, s56, 0xc000\nWV 24\ns_barrier\nRa 192,140,0\nRa 196,141,0\nRa 200,140,2048\n"
"Ra 204,141,2048\nRa 208,142,0\nRa 212,143,0\nRa 216,142,2048\nRa 220,143,2048\nTa 160,128,0\nTa 162,128,2048\nTa 164,128,16384\nTa 166,128,18432\nTa 168,129,0\nTa 170,129,2048\n"
"Ta 172,129,16384\nTa 174,129,18432\nTa 176,130,0\nTa 178,130,2048\nTa 180,130,16384\nTa 182,130,18432\nTa 184,131,0\nTa 186,131,2048\nTa 188,131,16384\nTa 190,131,18432\nWL 0\n"
"Zvaa 32,160,192\nTa 224,132,0\ns_add_u32 m0, s47, 49152\ns_add_u32 s46, s51, 1310720\nMvaa 32,164,196\nTa 226,132,2048\nZaaa 96,164,204\nD0 6,32,46,0\nMaaa 96,160,200\n"
"Ta 228,132,16384\nZvaa 192,160,208\nTa 230,132,18432\nMvaa 192,164,212\nZvaa 224,164,220\nTa 232,133,0\nMvaa 224,160,216\nTa 234,133,2048\nD0 7,32,46,1024\nZvaa 228,168,216\n"
"Mvaa 228,172,220\nTa 236,133,16384\nZvaa 196,172,212\nTa 238,133,18432\nMvaa 196,168,208\nZaaa 100,168,200\nTa 240,134,0\nD0 8,32,46,2048\nMaaa 100,172,204\nTa 242,134,2048\n"
"Zvaa 36,172,196\nMvaa 36,168,192\nTa 244,134,16384\nZvaa 40,176,192\nTa 246,134,18432\nMvaa 40,180,196\nD0 9,32,46,3072\nZaaa 104,180,204\nTa 248,135,0\nMaaa 104,176,200\n"
"Ta 250,135,2048\nZvaa 200,176,208\ns_add_u32 m0, s47, -16384\ns_add_u32 s46, s52, 384\nMvaa 200,180,212\nTa 252,135,16384\nZvaa 232,180,220\nTa 254,135,18432\nD0 136,28,46,0\n"
"Mvaa 232,176,216\nZvaa 236,184,216\nMvaa 236,188,220\nZvaa 204,188,212\nMvaa 204,184,208\nD0 137,28,46,1024\nZaaa 108,184,200\nMaaa 108,188,204\nZvaa 44,188,196\nMvaa 44,184,192\n"
"s_waitcnt vmcnt(18) lgkmcnt(0)\ns_barrier\nZvaa 48,224,192\nRa 128,140,16384\nD0 138,28,46,2048\nMvaa 48,228,196\nRa 132,141,16384\nZaaa 112,228,204\nRa 136,140,18432\n"
"Maaa 112,224,200\nRa 140,141,18432\nZvaa 208,224,208\nRa 144,142,16384\nWV 15\nMvaa 208,228,212\nRa 148,143,16384\nD0 139,28,46,3072\nZvaa 240,228,220\nRa 152,142,18432\n"
"Mvaa 240,224,216\nRa 156,143,18432\nZvaa 244,232,216\nTa 160,12,0\ns_add_u32 m0, s47, 65536\ns_add_u32 s46, s51, 1572864\nMvaa 244,236,220\nTa 162,12,2048\nZvaa 212,236,212\n"
"Ta 164,12,16384\nD0 6,32,46,0\nMvaa 212,232,208\nTa 166,12,18432\nZaaa 116,232,200\nTa 168,13,0\nv_lshlrev_b32 v112, 16, v96\nMaaa 116,236,204\nTa 170,13,2048\nZvaa 52,236,196\n"
"Ta 172,13,16384\nMvaa 52,232,192\nTa 174,13,18432\nD0 7,32,46,1024\nZvaa 56,240,192\nTa 176,14,0\nv_lshlrev_b32 v113, 16, v100\nMvaa 56,244,196\nTa 178,14,2048\nZaaa 120,244,204\n"
"Ta 180,14,16384\nMaaa 120,240,200\nTa 182,14,18432\nZvaa 216,240,208\nTa 184,15,0\nD0 8,32,46,2048\nMvaa 216,244,212\nTa 186,15,2048\nZvaa 248,244,220\nTa 188,15,16384\n"
"Mvaa 248,240,216\nTa 190,15,18432\nZvaa 252,248,216\nv_lshlrev_b32 v114, 16, v104\nv_lshlrev_b32 v115, 16, v108\nMvaa 252,252,220\nD0 9,32,46,3072\nZvaa 220,252,212\n"
"MU 116,112,114\nMvaa 220,248,208\nMU 117,113,114\nZaaa 124,248,200\ns_add_u32 m0, s47, 81920\ns_add_u32 s46, s51, 1835008\nMaaa 124,252,204\nCV 116,116,117\nZvaa 60,252,196\n"
"D0 6,32,46,0\nMvaa 60,248,192\nWL 0\nMvaa 32,160,128\nTa 224,16,0\nv_lshlrev_b32 v118, 16, v116\nMvaa 32,164,132\nTa 226,16,2048\nMaaa 96,164,140\n"
"v_and_b32 v119, 0xffff0000, v116\nMaaa 96,160,136\nTa 228,16,16384\nD0 7,32,46,1024\nMvaa 192,160,144\nTa 230,16,18432\nMU 120,112,115\nMvaa 192,164,148\nMU 121,113,115\n"
"Mvaa 224,164,156\nTa 232,17,0\nMvaa 224,160,152\nTa 234,17,2048\nMvaa 228,168,152\nD0 8,32,46,2048\nv_and_b32 v112, 0xffff0000, v96\nMvaa 228,172,156\nTa 236,17,16384\n"
"Mvaa 196,172,148\nTa 238,17,18432\nMvaa 196,168,144\nv_and_b32 v113, 0xffff0000, v100\nMaaa 100,168,136\nTa 240,18,0\nv_and_b32 v114, 0xffff0000, v104\nMaaa 100,172,140\n"
"Ta 242,18,2048\nD0 9,32,46,3072\nMvaa 36,172,132\nv_and_b32 v115, 0xffff0000, v108\nMvaa 36,168,128\nTa 244,18,16384\nMvaa 40,176,128\nTa 246,18,18432\nMU 116,112,114\n"
"Mvaa 40,180,132\nMU 117,113,114\nMaaa 104,180,140\nTa 248,19,0\nMaaa 104,176,136\nTa 250,19,2048\nMvaa 200,176,144\nCV 116,116,117\nv_and_b32 v117, 0xffff0000, v116\n"
"Mvaa 200,180,148\nTa 252,19,16384\nMvaa 232,180,156\nTa 254,19,18432\nMvaa 232,176,152\nv_lshlrev_b32 v116, 16, v116\nMvaa 236,184,152\nFM 120,112,115\nFM 121,113,115\n"
"Mvaa 236,188,156\nAD 118,118,116\nMvaa 204,188,148\nAD 119,119,117\nMvaa 204,184,144\nv_lshlrev_b32 v112, 16, v97\nMaaa 108,184,136\nv_lshlrev_b32 v113, 16, v101\n"
"v_lshlrev_b32 v114, 16, v105\nMaaa 108,188,140\nv_lshlrev_b32 v115, 16, v109\nMvaa 44,188,132\nMU 116,112,114\nMvaa 44,184,128\ns_waitcnt vmcnt(12) lgkmcnt(0)\ns_barrier\n"
"Mvaa 48,224,128\nRa 192,140,32768\ns_add_u32 m0, s47, -49152\ns_add_u32 s46, s52, 512\nMvaa 48,228,132\nRa 196,141,32768\nMaaa 112,228,140\nRa 200,140,34816\nD0 136,28,46,0\n"
"Maaa 112,224,136\nRa 204,141,34816\nMvaa 208,224,144\nRa 208,142,32768\nMU 117,113,114\nMvaa 208,228,148\nRa 212,143,32768\nMvaa 240,228,156\nRa 216,142,34816\nMvaa 240,224,152\n"
"Ra 220,143,34816\nD0 137,28,46,1024\nMvaa 244,232,152\nTa 160,12,32768\nCV 116,116,117\nMvaa 244,236,156\nTa 162,12,34816\nMvaa 212,236,148\nTa 164,12,49152\nMvaa 212,232,144\n"
"Ta 166,12,51200\nMaaa 116,232,136\nTa 168,13,32768\nD0 138,28,46,2048\nMaaa 116,236,140\nTa 170,13,34816\nMvaa 52,236,132\nTa 172,13,49152\nMvaa 52,232,128\nTa 174,13,51200\n"
"Mvaa 56,240,128\nTa 176,14,32768\nv_and_b32 v117, 0xffff0000, v116\nMvaa 56,244,132\nTa 178,14,34816\nD0 139,28,46,3072\nMaaa 120,244,140\nTa 180,14,49152\nMaaa 120,240,136\n"
"Ta 182,14,51200\nMvaa 216,240,144\nTa 184,15,32768\ns_add_u32 m0, s47, 0\ns_add_u32 s46, s51, 2097152\nMvaa 216,244,148\nTa 186,15,34816\nMvaa 248,244,156\nTa 188,15,49152\n"
"D0 6,32,46,0\nMvaa 248,240,152\nTa 190,15,51200\nMvaa 252,248,152\nv_lshlrev_b32 v116, 16, v116\nFM 120,112,115\nMvaa 252,252,156\nFM 121,113,115\nMvaa 220,252,148\n"
"AD 118,118,116\nMvaa 220,248,144\nD0 7,32,46,1024\nMaaa 124,248,136\nAD 119,119,117\nv_and_b32 v112, 0xffff0000, v97\nMaaa 124,252,140\nv_and_b32 v113, 0xffff0000, v101\n"
"Mvaa 60,252,132\nv_and_b32 v114, 0xffff0000, v105\nMvaa 60,248,128\nWL 0\nMvaa 32,160,192\nTa 224,16,32768\nD0 8,32,46,2048\nMvaa 32,164,196\nTa 226,16,34816\nMaaa 96,164,204\n"
"v_and_b32 v115, 0xffff0000, v109\nMaaa 96,160,200\nTa 228,16,49152\nMvaa 192,160,208\nTa 230,16,51200\nMU 116,112,114\nMvaa 192,164,212\nD0 9,32,46,3072\nMvaa 224,164,220\n"
"Ta 232,17,32768\nMvaa 224,160,216\nTa 234,17,34816\nMvaa 228,168,216\ns_add_u32 m0, s47, 16384\ns_add_u32 s46, s51, 2359296\nMvaa 228,172,220\nTa 236,17,49152\nMvaa 196,172,212\n"
"Ta 238,17,51200\nD0 6,32,46,0\nMvaa 196,168,208\nMU 117,113,114\nMaaa 100,168,200\nTa 240,18,32768\nCV 116,116,117\nMaaa 100,172,204\nTa 242,18,34816\nMvaa 36,172,196\n"
"v_and_b32 v117, 0xffff0000, v116\nMvaa 36,168,192\nTa 244,18,49152\nD0 7,32,46,1024\nMvaa 40,176,192\nTa 246,18,51200\nv_lshlrev_b32 v116, 16, v116\nMvaa 40,180,196\n"
"FM 120,112,115\nMaaa 104,180,204\nTa 248,19,32768\nMaaa 104,176,200\nTa 250,19,34816\nMvaa 200,176,208\nD0 8,32,46,2048\nFM 121,113,115\nMvaa 200,180,212\nTa 252,19,49152\n"
"Mvaa 232,180,220\nTa 254,19,51200\nMvaa 232,176,216\nAD 118,118,116\nMvaa 236,184,216\nAD 119,119,117\nv_lshlrev_b32 v112, 16, v98\nMvaa 236,188,220\nD0 9,32,46,3072\n"
"Mvaa 204,188,212\nv_lshlrev_b32 v113, 16, v102\nMvaa 204,184,208\nv_lshlrev_b32 v114, 16, v106\nMaaa 108,184,200\nv_lshlrev_b32 v115, 16, v110\nMU 116,112,114\nMaaa 108,188,204\n"
"MU 117,113,114\nMvaa 44,188,196\nCV 116,116,117\nMvaa 44,184,192\ns_waitcnt vmcnt(12) lgkmcnt(0)\ns_barrier\nMvaa 48,224,192\nRa 128,140,49152\ns_add_u32 m0, s47, -32768\n"
"s_add_u32 s46, s52, 640\nMvaa 48,228,196\nRa 132,141,49152\nMaaa 112,228,204\nRa 136,140,51200\nD0 136,28,46,0\nMaaa 112,224,200\nRa 140,141,51200\nMvaa 208,224,208\n"
"Ra 144,142,49152\nv_and_b32 v117, 0xffff0000, v116\nMvaa 208,228,212\nRa 148,143,49152\nMvaa 240,228,220\nRa 152,142,51200\nMvaa 240,224,216\nRa 156,143,51200\nD0 137,28,46,1024\n"
"Mvaa 244,232,216\nTa 160,128,0\nv_lshlrev_b32 v116, 16, v116\nMvaa 244,236,220\nTa 162,128,2048\nMvaa 212,236,212\nTa 164,128,16384\nMvaa 212,232,208\nTa 166,128,18432\n"
"Maaa 116,232,200\nTa 168,129,0\nD0 138,28,46,2048\nMaaa 116,236,204\nTa 170,129,2048\nMvaa 52,236,196\nTa 172,129,16384\nMvaa 52,232,192\nTa 174,129,18432\nMvaa 56,240,192\n"
"Ta 176,130,0\nFM 120,112,115\nMvaa 56,244,196\nTa 178,130,2048\nD0 139,28,46,3072\nMaaa 120,244,204\nTa 180,130,16384\nMaaa 120,240,200\nTa 182,130,18432\nMvaa 216,240,208\n"
"Ta 184,131,0\ns_add_u32 m0, s47, 32768\ns_add_u32 s46, s51, 2621440\nMvaa 216,244,212\nTa 186,131,2048\nMvaa 248,244,220\nTa 188,131,16384\nD0 6,32,46,0\nMvaa 248,240,216\n"
"Ta 190,131,18432\nMvaa 252,248,216\nFM 121,113,115\nAD 118,118,116\nMvaa 252,252,220\nAD 119,119,117\nMvaa 220,252,212\nv_and_b32 v112, 0xffff0000, v98\nMvaa 220,248,208\n"
"D0 7,32,46,1024\nMaaa 124,248,200\nv_and_b32 v113, 0xffff0000, v102\nv_and_b32 v114, 0xffff0000, v106\nMaaa 124,252,204\nv_and_b32 v115, 0xffff0000, v110\nMvaa 60,252,196\n"
"MU 116,112,114\nMvaa 60,248,192\nWL 0\nMvaa 32,160,128\nTa 224,132,0\nD0 8,32,46,2048\nMvaa 32,164,132\nTa 226,132,2048\nMaaa 96,164,140\nMU 117,113,114\nMaaa 96,160,136\n"
"Ta 228,132,16384\nMvaa 192,160,144\nTa 230,132,18432\nCV 116,116,117\nMvaa 192,164,148\nD0 9,32,46,3072\nMvaa 224,164,156\nTa 232,133,0\nMvaa 224,160,152\nTa 234,133,2048\n"
"Mvaa 228,168,152\ns_add_u32 m0, s47, 49152\ns_add_u32 s46, s51, 2883584\nMvaa 228,172,156\nTa 236,133,16384\nMvaa 196,172,148\nTa 238,133,18432\nD0 6,32,46,0\nMvaa 196,168,144\n"
"v_and_b32 v117, 0xffff0000, v116\nMaaa 100,168,136\nTa 240,134,0\nv_lshlrev_b32 v116, 16, v116\nMaaa 100,172,140\nTa 242,134,2048\nMvaa 36,172,132\nFM 120,112,115\n"
"Mvaa 36,168,128\nTa 244,134,16384\nD0 7,32,46,1024\nMvaa 40,176,128\nTa 246,134,18432\nFM 121,113,115\nMvaa 40,180,132\nAD 118,118,116\nMaaa 104,180,140\nTa 248,135,0\n"
"Maaa 104,176,136\nTa 250,135,2048\nMvaa 200,176,144\nD0 8,32,46,2048\nAD 119,119,117\nMvaa 200,180,148\nTa 252,135,16384\nMvaa 232,180,156\nTa 254,135,18432\nMvaa 232,176,152\n"
"v_lshlrev_b32 v112, 16, v99\nMvaa 236,184,152\nv_lshlrev_b32 v113, 16, v103\nv_lshlrev_b32 v114, 16, v107\nMvaa 236,188,156\nD0 9,32,46,3072\nMvaa 204,188,148\n"
"v_lshlrev_b32 v115, 16, v111\nMvaa 204,184,144\nMU 116,112,114\nMaaa 108,184,136\nMU 117,113,114\nCV 116,116,117\ns_and_b32 s78, s74, 3\ns_lshl2_add_u32 s78, s73, s78\n"
"s_lshl_b32 s78, s78, 12\ns_bitcmp1_b32 s11, 0\ns_cselect_b32 s79, s48, s45\ns_add_u32 s79, s79, 512\ns_and_b32 s79, s79, 0xfffe0000\ns_add_u32 s78, s78, s79\n"
"buffer_load_dword v31, v26, s[20:23], s78 offen\nMaaa 108,188,140\nv_and_b32 v117, 0xffff0000, v116\nMvaa 44,188,132\nv_lshlrev_b32 v116, 16, v116\nMvaa 44,184,128\n"
"s_waitcnt vmcnt(13) lgkmcnt(0)\ns_barrier\nMvaa 48,224,128\nRa 192,140,16384\ns_add_u32 m0, s47, -16384\ns_add_u32 s46, s52, 768\nMvaa 48,228,132\nRa 196,141,16384\n"
"Maaa 112,228,140\nRa 200,140,18432\nD0 136,28,46,0\nMaaa 112,224,136\nRa 204,141,18432\nMvaa 208,224,144\nRa 208,142,16384\nFM 120,112,115\nMvaa 208,228,148\nRa 212,143,16384\n"
"Mvaa 240,228,156\nRa 216,142,18432\nMvaa 240,224,152\nRa 220,143,18432\nD0 137,28,46,1024\nMvaa 244,232,152\nTa 160,12,0\nFM 121,113,115\nMvaa 244,236,156\nTa 162,12,2048\n"
"Mvaa 212,236,148\nTa 164,12,16384\nMvaa 212,232,144\nTa 166,12,18432\nMaaa 116,232,136\nTa 168,13,0\nD0 138,28,46,2048\nMaaa 116,236,140\nTa 170,13,2048\nMvaa 52,236,132\n"
"Ta 172,13,16384\nMvaa 52,232,128\nTa 174,13,18432\nMvaa 56,240,128\nTa 176,14,0\nAD 118,118,116\nMvaa 56,244,132\nTa 178,14,2048\nD0 139,28,46,3072\nMaaa 120,244,140\n"
"Ta 180,14,16384\nMaaa 120,240,136\nTa 182,14,18432\nMvaa 216,240,144\nTa 184,15,0\ns_add_u32 m0, s47, 65536\ns_add_u32 s46, s51, 3145728\nMvaa 216,244,148\nTa 186,15,2048\n"
"Mvaa 248,244,156\nTa 188,15,16384\nD0 6,32,46,0\nMvaa 248,240,152\nTa 190,15,18432\nMvaa 252,248,152\nAD 119,119,117\nv_and_b32 v112, 0xffff0000, v99\nMvaa 252,252,156\n"
"v_and_b32 v113, 0xffff0000, v103\nMvaa 220,252,148\nv_and_b32 v114, 0xffff0000, v107\nMvaa 220,248,144\nD0 7,32,46,1024\nMaaa 124,248,136\nv_and_b32 v115, 0xffff0000, v111\n"
"MU 116,112,114\nMaaa 124,252,140\nMU 117,113,114\nMvaa 60,252,132\nCV 116,116,117\nMvaa 60,248,128\nWL 0\nMvaa 32,160,192\nTa 224,16,0\nD0 8,32,46,2048\nMvaa 32,164,196\n"
"Ta 226,16,2048\nMaaa 96,164,204\nv_and_b32 v117, 0xffff0000, v116\nMaaa 96,160,200\nTa 228,16,16384\nMvaa 192,160,208\nTa 230,16,18432\nv_lshlrev_b32 v116, 16, v116\n"
"Mvaa 192,164,212\nD0 9,32,46,3072\nMvaa 224,164,220\nTa 232,17,0\nMvaa 224,160,216\nTa 234,17,2048\nMvaa 228,168,216\ns_add_u32 m0, s47, 81920\ns_add_u32 s46, s51, 3407872\n"
"Mvaa 228,172,220\nTa 236,17,16384\nMvaa 196,172,212\nTa 238,17,18432\nD0 6,32,46,0\nMvaa 196,168,208\nFM 120,112,115\nMaaa 100,168,200\nTa 240,18,0\nFM 121,113,115\n"
"Maaa 100,172,204\nTa 242,18,2048\nMvaa 36,172,196\nAD 118,118,116\nMvaa 36,168,192\nTa 244,18,16384\nD0 7,32,46,1024\nMvaa 40,176,192\nTa 246,18,18432\nAD 119,119,117\n"
"Mvaa 40,180,196\ns_nop 0\nv_permlane32_swap_b32 v118, v119\nMaaa 104,180,204\nTa 248,19,0\nMaaa 104,176,200\nTa 250,19,2048\nMvaa 200,176,208\nD0 8,32,46,2048\n"
"v_permlane32_swap_b32 v120, v121\nMvaa 200,180,212\nTa 252,19,16384\nMvaa 232,180,220\nTa 254,19,18432\nMvaa 232,176,216\nAD 118,118,119\nMvaa 236,184,216\nAD 120,120,121\n"
"Mvaa 236,188,220\nD0 9,32,46,3072\nMvaa 204,188,212\nv_permlane16_swap_b32 v118, v120\nMvaa 204,184,208\nAD 118,118,120\nMaaa 108,184,200\nMaaa 108,188,204\n"
"v_add_f32_dpp v118, v118, v118 row_ror:8 row_mask:0xf bank_mask:0xf\nMvaa 44,188,196\nMvaa 44,184,192\ns_waitcnt vmcnt(13) lgkmcnt(0)\ns_barrier\nMvaa 48,224,192\n"
"Ra 128,140,32768\ns_add_u32 m0, s47, -65536\ns_add_u32 s45, s45, 128\nMvaa 48,228,196\nRa 132,141,32768\nMaaa 112,228,204\nRa 136,140,34816\nD0 2,20,45,0\nMaaa 112,224,200\n"
"Ra 140,141,34816\nMvaa 208,224,208\nRa 144,142,32768\nv_add_f32_dpp v118, v118, v118 row_ror:4 row_mask:0xf bank_mask:0xf\nMvaa 208,228,212\nRa 148,143,32768\nMvaa 240,228,220\n"
"Ra 152,142,34816\nMvaa 240,224,216\nRa 156,143,34816\nD0 3,20,45,1024\nMvaa 244,232,216\nTa 160,12,32768\nv_add_f32_dpp v118, v118, v118 row_ror:2 row_mask:0xf bank_mask:0xf\n"
"Mvaa 244,236,220\nTa 162,12,34816\nMvaa 212,236,212\nTa 164,12,49152\nMvaa 212,232,208\nTa 166,12,51200\nMaaa 116,232,200\nTa 168,13,32768\nD0 4,20,45,2048\nMaaa 116,236,204\n"
"Ta 170,13,34816\nMvaa 52,236,196\nTa 172,13,49152\nMvaa 52,232,192\nTa 174,13,51200\nMvaa 56,240,192\nTa 176,14,32768\n"
"v_add_f32_dpp v118, v118, v118 row_ror:1 row_mask:0xf bank_mask:0xf\nMvaa 56,244,196\nTa 178,14,34816\nD0 5,20,45,3072\nMaaa 120,244,204\nTa 180,14,49152\nMaaa 120,240,200\n"
"Ta 182,14,51200\nMvaa 216,240,208\nTa 184,15,32768\ns_add_u32 m0, s47, 0\ns_add_u32 s44, s44, 0x40000\nMvaa 216,244,212\nTa 186,15,34816\nMvaa 248,244,220\nTa 188,15,49152\n"
"D0 6,24,44,0\nMvaa 248,240,216\nTa 190,15,51200\nMvaa 252,248,216\nCV 118,118,118\nv_lshlrev_b32 v118, 16, v118\nMvaa 252,252,220\nv_mul_f32 v119, 0xbfb8aa3b, v118\nEX 119,119\n"
"Mvaa 220,252,212\nv_add_f32 v120, 0, v118\nMvaa 220,248,208\nD0 7,24,44,1024\nMaaa 124,248,200\nv_add_f32 v119, 1.0, v119\nMaaa 124,252,204\nv_rcp_f32 v119, v119\nMvaa 60,252,196\n"
"v_ashrrev_i32 v121, 31, v120\nMvaa 60,248,192\nWL 0\nMvaa 32,160,128\nTa 224,16,32768\nD0 8,24,44,2048\nMvaa 32,164,132\nTa 226,16,34816\nMaaa 96,164,140\nCV 119,119,119\n"
"Maaa 96,160,136\nTa 228,16,49152\nMvaa 192,160,144\nTa 230,16,51200\nv_lshlrev_b32 v119, 16, v119\nMvaa 192,164,148\nD0 9,24,44,3072\nMvaa 224,164,156\nTa 232,17,32768\n"
"Mvaa 224,160,152\nTa 234,17,34816\nMvaa 228,168,152\ns_add_u32 m0, s47, 16384\ns_add_u32 s44, s44, 0x40000\nMvaa 228,172,156\nTa 236,17,49152\nMvaa 196,172,148\nTa 238,17,51200\n"
"D0 6,24,44,0\nMvaa 196,168,144\nv_or_b32 v121, 0x80000000, v121\nMaaa 100,168,136\nTa 240,18,32768\nv_xor_b32 v120, v120, v121\nMaaa 100,172,140\nTa 242,18,34816\nMvaa 36,172,132\n"
"v_and_b32 v120, 0xffff0000, v120\nMvaa 36,168,128\nTa 244,18,49152\nD0 7,24,44,1024\nMvaa 40,176,128\nTa 246,18,51200\ns_lshl2_add_u32 s2, s10, s11\nMvaa 40,180,132\n"
"v_lshrrev_b32 v112, 5, v0\nMaaa 104,180,140\nTa 248,19,32768\nMaaa 104,176,136\nTa 250,19,34816\nMvaa 200,176,144\nD0 8,24,44,2048\nv_lshl_add_u32 v112, s2, 1, v112\n"
"Mvaa 200,180,148\nTa 252,19,49152\nMvaa 232,180,156\nTa 254,19,51200\nMvaa 232,176,152\nv_sub_u32 v113, 0xffff, v112\nMvaa 236,184,152\nv_or_b32 v120, v120, v113\n"
"v_mov_b32 v121, s15\nMvaa 236,188,156\nD0 9,24,44,3072\nMvaa 204,188,148\nv_and_b32 v113, 31, v0\nMvaa 204,184,144\nv_mov_b32 v116, 0x40000000\nv_cmp_eq_u32 vcc, 16, v113\n"
"v_lshlrev_b32 v114, 2, v112\nv_lshlrev_b32 v115, 10, v113\nv_cndmask_b32 v114, v116, v114, vcc\nv_cndmask_b32 v120, v119, v120, vcc\nMaaa 108,184,136\n"
"v_lshl_add_u32 v115, v112, 3, v115\nv_and_b32 v113, 15, v0\nMaaa 108,188,140\nv_cmp_eq_u32 vcc, 0, v113\ns_mov_b32 s2, 0xc040000\ns_mov_b32 s3, 0xc03e000\n"
"v_cndmask_b32 v115, v116, v115, vcc\nbuffer_store_dwordx2 v[120:121], v115, s[16:19], s2 offen sc0 sc1\nbuffer_store_dword v118, v114, s[16:19], s3 offen sc0 sc1\nMvaa 44,188,132\n"
"Mvaa 44,184,128\ns_waitcnt vmcnt(14) lgkmcnt(0)\ns_barrier\nMvaa 48,224,128\nRa 192,140,49152\ns_add_u32 m0, s47, -49152\ns_add_u32 s48, s48, 128\nMvaa 48,228,132\n"
"Ra 196,141,49152\nMaaa 112,228,140\nRa 200,140,51200\nD0 2,20,48,0\nMaaa 112,224,136\nRa 204,141,51200\nMvaa 208,224,144\nRa 208,142,49152\nMvaa 208,228,148\nRa 212,143,49152\n"
"Mvaa 240,228,156\nRa 216,142,51200\nMvaa 240,224,152\nRa 220,143,51200\nD0 3,20,48,1024\nMvaa 244,232,152\nTa 160,128,0\nMvaa 244,236,156\nTa 162,128,2048\nMvaa 212,236,148\n"
"Ta 164,128,16384\nMvaa 212,232,144\nTa 166,128,18432\nMaaa 116,232,136\nTa 168,129,0\nD0 4,20,48,2048\nMaaa 116,236,140\nTa 170,129,2048\nMvaa 52,236,132\nTa 172,129,16384\n"
"Mvaa 52,232,128\nTa 174,129,18432\nMvaa 56,240,128\nTa 176,130,0\nMvaa 56,244,132\nTa 178,130,2048\nD0 5,20,48,3072\nMaaa 120,244,140\nTa 180,130,16384\nMaaa 120,240,136\n"
"Ta 182,130,18432\nMvaa 216,240,144\nTa 184,131,0\ns_add_u32 m0, s47, -32768\ns_add_u32 s45, s45, 128\nMvaa 216,244,148\nTa 186,131,2048\nMvaa 248,244,156\nTa 188,131,16384\n"
"D0 2,20,45,0\nMvaa 248,240,152\nTa 190,131,18432\nMvaa 252,248,152\nMvaa 252,252,156\nMvaa 220,252,148\nMvaa 220,248,144\nD0 3,20,45,1024\nMaaa 124,248,136\nMaaa 124,252,140\n"
"Mvaa 60,252,132\nMvaa 60,248,128\nWL 0\nMvaa 32,160,192\nTa 224,132,0\nD0 4,20,45,2048\nMvaa 32,164,196\nTa 226,132,2048\nMvaa 36,172,196\nMvaa 36,168,192\nTa 228,132,16384\n"
"Mvaa 40,176,192\nTa 230,132,18432\nMvaa 40,180,196\nD0 5,20,45,3072\nMvaa 44,188,196\nTa 232,133,0\nMvaa 44,184,192\nTa 234,133,2048\nMaaa 108,184,200\ns_add_u32 m0, s47, 32768\n"
"s_add_u32 s44, s44, 0x40000\nMaaa 108,188,204\nTa 236,133,16384\nMaaa 104,180,204\nTa 238,133,18432\nD0 6,24,44,0\nMaaa 104,176,200\nv_mul_f32 v32, s57, v32\n"
"v_mul_f32 v33, s57, v33\nMaaa 100,168,200\nTa 240,134,0\nCV 32,32,33\nMaaa 100,172,204\nTa 242,134,2048\nv_mul_f32 v34, s57, v34\nMaaa 96,164,204\nv_mul_f32 v35, s57, v35\n"
"CV 33,34,35\nMaaa 96,160,200\nTa 244,134,16384\nD0 7,24,44,1024\nMvaa 192,160,208\nTa 246,134,18432\nv_mul_f32 v36, s57, v36\nMvaa 192,164,212\nv_mul_f32 v37, s57, v37\n"
"Mvaa 196,172,212\nTa 248,135,0\nCV 34,36,37\nMvaa 196,168,208\nTa 250,135,2048\nv_mul_f32 v38, s57, v38\nMvaa 200,176,208\nD0 8,24,44,2048\nv_mul_f32 v39, s57, v39\n"
"Mvaa 200,180,212\nTa 252,135,16384\nCV 35,38,39\nMvaa 204,188,212\nTa 254,135,18432\nv_mul_f32 v40, s57, v40\nMvaa 204,184,208\nv_mul_f32 v41, s57, v41\nCV 40,40,41\n"
"Mvaa 236,184,216\nv_mul_f32 v42, s57, v42\nv_mul_f32 v43, s57, v43\nMvaa 236,188,220\nD0 9,24,44,3072\nMvaa 232,180,220\nCV 41,42,43\nv_mul_f32 v44, s57, v44\nMvaa 232,176,216\n"
"v_mul_f32 v45, s57, v45\nCV 42,44,45\nMvaa 228,168,216\nv_mul_f32 v46, s57, v46\nv_mul_f32 v47, s57, v47\nMvaa 228,172,220\nCV 43,46,47\nAR 20,96\nMvaa 224,164,220\nAR 21,97\n"
"v_mul_f32 v20, s57, v20\nMvaa 224,160,216\ns_waitcnt vmcnt(14) lgkmcnt(0)\ns_barrier\nMvaa 48,224,192\ns_add_u32 m0, s47, 49152\ns_add_u32 s44, s44, 0x40000\nRa 128,10,0\n"
"Mvaa 48,228,196\nRa 132,11,0\nMvaa 52,236,196\nD0 6,24,44,0\nRa 136,10,2048\nMvaa 52,232,192\nRa 140,11,2048\nv_mul_f32 v21, s57, v21\nMvaa 56,240,192\nRa 144,10,8192\n"
"CV 36,20,21\nMvaa 56,244,196\nRa 148,11,8192\nAR 20,98\nMvaa 60,252,196\nD0 7,24,44,1024\nRa 152,10,10240\nMvaa 60,248,192\nRa 156,11,10240\nAR 21,99\nMaaa 124,248,200\n"
"Ta 160,12,0\nv_mul_f32 v20, s57, v20\nMaaa 124,252,204\nTa 162,12,2048\nMaaa 120,244,204\nD0 8,24,44,2048\nTa 164,12,16384\nMaaa 120,240,200\nTa 166,12,18432\n"
"v_mul_f32 v21, s57, v21\nMaaa 116,232,200\nTa 168,13,0\nCV 37,20,21\nMaaa 116,236,204\nTa 170,13,2048\nAR 20,100\nMaaa 112,228,204\nD0 9,24,44,3072\nTa 172,13,16384\n"
"Maaa 112,224,200\nTa 174,13,18432\nAR 21,101\nMvaa 208,224,208\ns_add_u32 m0, s47, -16384\ns_add_u32 s48, s48, 128\nTa 176,14,0\nMvaa 208,228,212\nTa 178,14,2048\n"
"Mvaa 212,236,212\nD0 2,20,48,0\nTa 180,14,16384\nMvaa 212,232,208\nTa 182,14,18432\nv_mul_f32 v20, s57, v20\nMvaa 216,240,208\nTa 184,15,0\nv_mul_f32 v21, s57, v21\n"
"Mvaa 216,244,212\nTa 186,15,2048\nCV 38,20,21\nMvaa 220,252,212\nD0 3,20,48,1024\nTa 188,15,16384\nMvaa 220,248,208\nTa 190,15,18432\nAR 20,102\nMvaa 252,248,216\nAR 21,103\n"
"v_mul_f32 v20, s57, v20\nMvaa 252,252,220\nv_mul_f32 v21, s57, v21\nMvaa 248,244,220\nD0 4,20,48,2048\nCV 39,20,21\nMvaa 248,240,216\nAR 20,104\nAR 21,105\nMvaa 244,232,216\n"
"v_mul_f32 v20, s57, v20\nv_mul_f32 v21, s57, v21\nMvaa 244,236,220\nCV 44,20,21\nAR 20,106\nMvaa 240,228,220\nD0 5,20,48,3072\nAR 21,107\nMvaa 240,224,216\nWL 0\ns_barrier\n"
"Zvaa 64,160,128\nTa 224,16,0\nv_mul_f32 v20, s57, v20\nMvaa 64,164,132\nTa 226,16,2048\nZvaa 96,164,140\ns_add_u32 m0, s47, -65536\ns_add_u32 s45, s45, 128\nMvaa 96,160,136\n"
"Ta 228,16,16384\nv_mul_f32 v21, s57, v21\nZvaa 128,160,144\nTa 230,16,18432\nCV 45,20,21\nMvaa 128,164,148\nD0 2,20,45,0\nAR 20,108\nZvaa 160,164,156\nTa 232,17,0\nAR 21,109\n"
"Mvaa 160,160,152\nTa 234,17,2048\nv_mul_f32 v20, s57, v20\nZvaa 164,168,152\nv_mul_f32 v21, s57, v21\nCV 46,20,21\nMvaa 164,172,156\nTa 236,17,16384\ns_add_u32 s6, s56, 0\n"
"Sv 32,1,16,6,0\nZvaa 132,172,148\nTa 238,17,18432\nAR 20,110\nMvaa 132,168,144\nAR 21,111\nv_mul_f32 v20, s57, v20\nZvaa 100,168,136\nTa 240,18,0\nv_mul_f32 v21, s57, v21\n"
"Mvaa 100,172,140\nTa 242,18,2048\nCV 47,20,21\nZvaa 68,172,132\nD0 3,20,45,1024\nv_mul_f32 v48, s57, v48\nMvaa 68,168,128\nTa 244,18,16384\nv_mul_f32 v49, s57, v49\n"
"Zvaa 72,176,128\nTa 246,18,18432\nCV 48,48,49\nMvaa 72,180,132\nv_mul_f32 v50, s57, v50\nZvaa 104,180,140\nTa 248,19,0\nv_mul_f32 v51, s57, v51\nMvaa 104,176,136\nTa 250,19,2048\n"
"CV 49,50,51\nZvaa 136,176,144\nv_mul_f32 v52, s57, v52\nv_mul_f32 v53, s57, v53\nMvaa 136,180,148\nTa 252,19,16384\nCV 50,52,53\nZvaa 168,180,156\nTa 254,19,18432\n"
"v_mul_f32 v54, s57, v54\nMvaa 168,176,152\nD0 4,20,45,2048\nv_mul_f32 v55, s57, v55\nZvaa 172,184,152\nCV 51,54,55\nv_mul_f32 v56, s57, v56\nMvaa 172,188,156\n"
"v_mul_f32 v57, s57, v57\ns_add_u32 s6, s56, 0\nSv 40,1,16,6,2048\nZvaa 140,188,148\nCV 56,56,57\nv_mul_f32 v58, s57, v58\nMvaa 140,184,144\nv_mul_f32 v59, s57, v59\nCV 57,58,59\n"
"Zvaa 108,184,136\nv_mul_f32 v60, s57, v60\nv_mul_f32 v61, s57, v61\nMvaa 108,188,140\nD0 5,20,45,3072\nCV 58,60,61\nZvaa 76,188,132\nv_mul_f32 v62, s57, v62\n"
"v_mul_f32 v63, s57, v63\nMvaa 76,184,128\ns_waitcnt vmcnt(18) lgkmcnt(0)\ns_barrier\nZvaa 80,224,128\nRa 192,10,16384\nCV 59,62,63\nMvaa 80,228,132\ns_add_u32 m0, s47, 0\n"
"s_add_u32 s44, s44, 0x40000\nZvaa 112,228,140\nRa 196,11,16384\nAR 20,112\nMvaa 112,224,136\nAR 21,113\nv_mul_f32 v20, s57, v20\nZvaa 144,224,144\nRa 200,10,18432\n"
"v_mul_f32 v21, s57, v21\nMvaa 144,228,148\nD0 6,24,44,0\nCV 52,20,21\nZvaa 176,228,156\nRa 204,11,18432\nAR 20,114\nMvaa 176,224,152\nAR 21,115\nv_mul_f32 v20, s57, v20\n"
"Zvaa 180,232,152\nRa 208,10,24576\nv_mul_f32 v21, s57, v21\nMvaa 180,236,156\nCV 53,20,21\ns_add_u32 s6, s56, 0\nSv 36,1,16,6,1024\nZvaa 148,236,148\nRa 212,11,24576\nAR 20,116\n"
"Mvaa 148,232,144\nAR 21,117\nv_mul_f32 v20, s57, v20\nZvaa 116,232,136\nRa 216,10,26624\nv_mul_f32 v21, s57, v21\nMvaa 116,236,140\nD0 7,24,44,1024\nCV 54,20,21\nZvaa 84,236,132\n"
"Ra 220,11,26624\nAR 20,118\nMvaa 84,232,128\nAR 21,119\nv_mul_f32 v20, s57, v20\nZvaa 88,240,128\nv_mul_f32 v21, s57, v21\nCV 55,20,21\nMvaa 88,244,132\nAR 20,120\n"
"Zvaa 120,244,140\nAR 21,121\nv_mul_f32 v20, s57, v20\nMvaa 120,240,136\nv_mul_f32 v21, s57, v21\nCV 60,20,21\nZvaa 152,240,144\nAR 20,122\nAR 21,123\nMvaa 152,244,148\n"
"D0 8,24,44,2048\nv_mul_f32 v20, s57, v20\nZvaa 184,244,156\nv_mul_f32 v21, s57, v21\nCV 61,20,21\nMvaa 184,240,152\nAR 20,124\nAR 21,125\nZvaa 188,248,152\n"
"v_mul_f32 v20, s57, v20\nv_mul_f32 v21, s57, v21\nMvaa 188,252,156\nCV 62,20,21\ns_add_u32 s6, s56, 0\nSv 44,1,16,6,3072\nZvaa 156,252,148\nAR 20,126\nAR 21,127\nMvaa 156,248,144\n"
"v_mul_f32 v20, s57, v20\nv_mul_f32 v21, s57, v21\nZvaa 124,248,136\nCV 63,20,21\nv_mul_f32 v192, s57, v192\nMvaa 124,252,140\nD0 9,24,44,3072\nv_mul_f32 v193, s57, v193\n"
"Zvaa 92,252,132\nCV 192,192,193\nv_mul_f32 v194, s57, v194\nMvaa 92,248,128\nWL 0\nZaaa 16,224,192\nRa 128,10,32768\nv_mul_f32 v195, s57, v195\nMaaa 16,228,196\n"
"s_add_u32 m0, s47, 16384\ns_add_u32 s44, s44, 0x40000\nZaaa 48,228,204\nRa 132,11,32768\nCV 193,194,195\nMaaa 48,224,200\nv_mul_f32 v196, s57, v196\nv_mul_f32 v197, s57, v197\n"
"Zaaa 80,224,208\nRa 136,10,34816\nCV 194,196,197\nMaaa 80,228,212\nD0 6,24,44,0\nv_mul_f32 v198, s57, v198\nZaaa 112,228,220\nRa 140,11,34816\nv_mul_f32 v199, s57, v199\n"
"Maaa 112,224,216\nCV 195,198,199\nv_mul_f32 v200, s57, v200\nZaaa 116,232,216\nRa 144,10,40960\nv_mul_f32 v201, s57, v201\nMaaa 116,236,220\nCV 200,200,201\n"
"v_mul_f32 v202, s57, v202\ns_add_u32 s6, s56, 4096\nSv 48,1,16,6,0\nZaaa 84,236,212\nRa 148,11,40960\nv_mul_f32 v203, s57, v203\nMaaa 84,232,208\nCV 201,202,203\n"
"v_mul_f32 v204, s57, v204\nZaaa 52,232,200\nRa 152,10,43008\nv_mul_f32 v205, s57, v205\nMaaa 52,236,204\nD0 7,24,44,1024\nCV 202,204,205\nZaaa 20,236,196\nRa 156,11,43008\n"
"v_mul_f32 v206, s57, v206\nMaaa 20,232,192\nv_mul_f32 v207, s57, v207\nCV 203,206,207\nZaaa 24,240,192\nv_mul_f32 v208, s57, v208\nv_mul_f32 v209, s57, v209\nMaaa 24,244,196\n"
"CV 208,208,209\nv_mul_f32 v210, s57, v210\nZaaa 56,244,204\nv_mul_f32 v211, s57, v211\nCV 209,210,211\nMaaa 56,240,200\nv_mul_f32 v212, s57, v212\nv_mul_f32 v213, s57, v213\n"
"Zaaa 88,240,208\nCV 210,212,213\nv_mul_f32 v214, s57, v214\nMaaa 88,244,212\nD0 8,24,44,2048\nv_mul_f32 v215, s57, v215\nZaaa 120,244,220\nCV 211,214,215\n"
"v_mul_f32 v216, s57, v216\nMaaa 120,240,216\nv_mul_f32 v217, s57, v217\nCV 216,216,217\nZaaa 124,248,216\nv_mul_f32 v218, s57, v218\nv_mul_f32 v219, s57, v219\nMaaa 124,252,220\n"
"CV 217,218,219\nv_mul_f32 v220, s57, v220\ns_add_u32 s6, s56, 4096\nSv 56,1,16,6,2048\nZaaa 92,252,212\nv_mul_f32 v221, s57, v221\nCV 218,220,221\nMaaa 92,248,208\n"
"v_mul_f32 v222, s57, v222\nv_mul_f32 v223, s57, v223\nZaaa 60,248,200\nCV 219,222,223\nv_mul_f32 v224, s57, v224\nMaaa 60,252,204\nD0 9,24,44,3072\nv_mul_f32 v225, s57, v225\n"
"Zaaa 28,252,196\nCV 224,224,225\nv_mul_f32 v226, s57, v226\nMaaa 28,248,192\ns_waitcnt vmcnt(22) lgkmcnt(0)\ns_barrier\nZaaa 0,160,192\nTa 224,16,32768\nv_mul_f32 v227, s57, v227\n"
"Maaa 0,164,196\nTa 226,16,34816\nCV 225,226,227\nZaaa 32,164,204\ns_add_u32 m0, s47, -49152\ns_add_u32 s48, s48, 128\nMaaa 32,160,200\nTa 228,16,49152\nv_mul_f32 v228, s57, v228\n"
"Zaaa 64,160,208\nTa 230,16,51200\nv_mul_f32 v229, s57, v229\nMaaa 64,164,212\nD0 2,20,48,0\nCV 226,228,229\nZaaa 96,164,220\nTa 232,17,32768\nv_mul_f32 v230, s57, v230\n"
"Maaa 96,160,216\nTa 234,17,34816\nv_mul_f32 v231, s57, v231\nZaaa 100,168,216\nCV 227,230,231\nv_mul_f32 v232, s57, v232\nMaaa 100,172,220\nTa 236,17,49152\n"
"v_mul_f32 v233, s57, v233\ns_add_u32 s6, s56, 4096\nSv 52,1,16,6,1024\nZaaa 68,172,212\nTa 238,17,51200\nCV 232,232,233\nMaaa 68,168,208\nv_mul_f32 v234, s57, v234\n"
"v_mul_f32 v235, s57, v235\nZaaa 36,168,200\nTa 240,18,32768\nCV 233,234,235\nMaaa 36,172,204\nTa 242,18,34816\nv_mul_f32 v236, s57, v236\nZaaa 4,172,196\nD0 3,20,48,1024\n"
"v_mul_f32 v237, s57, v237\nMaaa 4,168,192\nTa 244,18,49152\nCV 234,236,237\nZaaa 8,176,192\nTa 246,18,51200\nv_mul_f32 v238, s57, v238\nMaaa 8,180,196\nv_mul_f32 v239, s57, v239\n"
"CV 235,238,239\nZaaa 40,180,204\nTa 248,19,32768\nv_mul_f32 v240, s57, v240\nMaaa 40,176,200\nTa 250,19,34816\nv_mul_f32 v241, s57, v241\nZaaa 72,176,208\nCV 240,240,241\n"
"v_mul_f32 v242, s57, v242\nMaaa 72,180,212\nTa 252,19,49152\nv_mul_f32 v243, s57, v243\nZaaa 104,180,220\nTa 254,19,51200\nCV 241,242,243\nMaaa 104,176,216\nD0 4,20,48,2048\n"
"v_mul_f32 v244, s57, v244\nZaaa 108,184,216\nv_mul_f32 v245, s57, v245\nCV 242,244,245\nMaaa 108,188,220\nv_mul_f32 v246, s57, v246\nv_mul_f32 v247, s57, v247\n"
"s_add_u32 s6, s56, 4096\nSv 60,1,16,6,3072\nZaaa 76,188,212\nCV 243,246,247\nv_mul_f32 v248, s57, v248\nMaaa 76,184,208\nv_mul_f32 v249, s57, v249\nCV 248,248,249\n"
"Zaaa 44,184,200\nv_mul_f32 v250, s57, v250\nv_mul_f32 v251, s57, v251\nMaaa 44,188,204\nD0 5,20,48,3072\nCV 249,250,251\nZaaa 12,188,196\nv_mul_f32 v252, s57, v252\n"
"v_mul_f32 v253, s57, v253\nMaaa 12,184,192\nWL 0\nMvaa 80,224,128\nTa 160,12,32768\nCV 250,252,253\nMvaa 80,228,132\nTa 162,12,34816\nv_mul_f32 v254, s57, v254\nMvaa 112,228,140\n"
"s_add_u32 m0, s47, -32768\ns_add_u32 s45, s45, 128\nMvaa 112,224,136\nTa 164,12,49152\nv_mul_f32 v255, s57, v255\nMvaa 144,224,144\nTa 166,12,51200\nCV 251,254,255\n"
"Mvaa 144,228,148\nD0 2,20,45,0\nMvaa 176,228,156\nTa 168,13,32768\nMvaa 176,224,152\nTa 170,13,34816\nMvaa 180,232,152\nMvaa 180,236,156\nTa 172,13,49152\ns_add_u32 s6, s53, 0\n"
"Sv 192,1,16,6,0\nMvaa 148,236,148\nTa 174,13,51200\nMvaa 148,232,144\nMvaa 116,232,136\nTa 176,14,32768\nMvaa 116,236,140\nTa 178,14,34816\nMvaa 84,236,132\nD0 3,20,45,1024\n"
"Mvaa 84,232,128\nTa 180,14,49152\nMvaa 88,240,128\nTa 182,14,51200\nMvaa 88,244,132\nMvaa 120,244,140\nTa 184,15,32768\nMvaa 120,240,136\nTa 186,15,34816\nMvaa 152,240,144\n"
"Mvaa 152,244,148\nTa 188,15,49152\nMvaa 184,244,156\nTa 190,15,51200\nMvaa 184,240,152\nD0 4,20,45,2048\nMvaa 188,248,152\nMvaa 188,252,156\ns_add_u32 s6, s53, 0\n"
"Sv 200,1,16,6,2048\nMvaa 156,252,148\nMvaa 156,248,144\nMvaa 124,248,136\nMvaa 124,252,140\nD0 5,20,45,3072\nMvaa 92,252,132\nMvaa 92,248,128\ns_waitcnt vmcnt(24) lgkmcnt(0)\n"
"s_barrier\nMvaa 64,160,128\nRa 192,10,49152\nMvaa 64,164,132\ns_add_u32 m0, s47, 32768\ns_add_u32 s44, s44, 0x40000\nMvaa 96,164,140\nRa 196,11,49152\nMvaa 96,160,136\n"
"Mvaa 128,160,144\nRa 200,10,51200\nMvaa 128,164,148\nD0 6,24,44,0\nMvaa 160,164,156\nRa 204,11,51200\nMvaa 160,160,152\nMvaa 164,168,152\nRa 208,10,57344\nMvaa 164,172,156\n"
"s_add_u32 s6, s53, 4096\nSv 208,1,16,6,0\nMvaa 132,172,148\nRa 212,11,57344\nMvaa 132,168,144\nMvaa 100,168,136\nRa 216,10,59392\nMvaa 100,172,140\nD0 7,24,44,1024\n"
"Mvaa 68,172,132\nRa 220,11,59392\nMvaa 68,168,128\nMvaa 72,176,128\nMvaa 72,180,132\nMvaa 104,180,140\nMvaa 104,176,136\nMvaa 136,176,144\nMvaa 136,180,148\nD0 8,24,44,2048\n"
"Mvaa 168,180,156\nMvaa 168,176,152\nMvaa 172,184,152\nMvaa 172,188,156\ns_add_u32 s6, s53, 4096\nSv 216,1,16,6,2048\nMvaa 140,188,148\nMvaa 140,184,144\nMvaa 108,184,136\n"
"Mvaa 108,188,140\nD0 9,24,44,3072\nMvaa 76,188,132\nMvaa 76,184,128\nWL 0\nMaaa 0,160,192\nRa 128,10,0\nMaaa 0,164,196\ns_add_u32 m0, s47, 49152\ns_add_u32 s44, s44, 0x40000\n"
"Maaa 32,164,204\nRa 132,11,0\nMaaa 32,160,200\nMaaa 64,160,208\nRa 136,10,2048\nMaaa 64,164,212\nD0 6,24,44,0\nMaaa 96,164,220\nRa 140,11,2048\nMaaa 96,160,216\nMaaa 100,168,216\n"
"Ra 144,10,8192\nMaaa 100,172,220\ns_add_u32 s6, s53, 0\nSv 224,1,16,6,1024\nMaaa 68,172,212\nRa 148,11,8192\nMaaa 68,168,208\nMaaa 36,168,200\nRa 152,10,10240\nMaaa 36,172,204\n"
"D0 7,24,44,1024\nMaaa 4,172,196\nRa 156,11,10240\nMaaa 4,168,192\nMaaa 8,176,192\nMaaa 8,180,196\nMaaa 40,180,204\nMaaa 40,176,200\nMaaa 72,176,208\nMaaa 72,180,212\n"
"D0 8,24,44,2048\nMaaa 104,180,220\nMaaa 104,176,216\nMaaa 108,184,216\nMaaa 108,188,220\ns_add_u32 s6, s53, 0\nSv 232,1,16,6,3072\nMaaa 76,188,212\nMaaa 76,184,208\n"
"Maaa 44,184,200\nMaaa 44,188,204\nD0 9,24,44,3072\nMaaa 12,188,196\nMaaa 12,184,192\ns_waitcnt vmcnt(24) lgkmcnt(0)\ns_barrier\nMaaa 16,224,192\nTa 160,12,0\nMaaa 16,228,196\n"
"Ta 162,12,2048\nMaaa 48,228,204\ns_add_u32 m0, s47, -16384\ns_add_u32 s48, s48, 128\nMaaa 48,224,200\nTa 164,12,16384\nMaaa 80,224,208\nTa 166,12,18432\nMaaa 80,228,212\n"
"D0 2,20,48,0\nMaaa 112,228,220\nTa 168,13,0\nMaaa 112,224,216\nTa 170,13,2048\nMaaa 116,232,216\nMaaa 116,236,220\nTa 172,13,16384\ns_add_u32 s6, s53, 4096\nSv 240,1,16,6,1024\n"
"Maaa 84,236,212\nTa 174,13,18432\nMaaa 84,232,208\nMaaa 52,232,200\nTa 176,14,0\nMaaa 52,236,204\nTa 178,14,2048\nMaaa 20,236,196\nD0 3,20,48,1024\nMaaa 20,232,192\n"
"Ta 180,14,16384\nMaaa 24,240,192\nTa 182,14,18432\nMaaa 24,244,196\ns_add_u32 s6, s53, 4096\nSv 248,1,16,6,3072\nMaaa 56,244,204\nTa 184,15,0\nMaaa 56,240,200\nTa 186,15,2048\n"
"Maaa 88,240,208\nMaaa 88,244,212\nTa 188,15,16384\nMaaa 120,244,220\nTa 190,15,18432\nMaaa 120,240,216\nD0 4,20,48,2048\nMaaa 124,248,216\nMaaa 124,252,220\nMaaa 92,252,212\n"
"Maaa 92,248,208\ns_lshl3_add_u32 s90, s12, s13\ns_lshl2_add_u32 s90, s90, s11\ns_lshl2_add_u32 s90, s90, 0xc04a400\nMaaa 60,248,200\nMaaa 60,252,204\nD0 5,20,48,3072\n"
"Maaa 28,252,196\nMaaa 28,248,192\nWL 0\ns_mov_b64 s[76:77], s[62:63]\ns_mov_b64 s[78:79], s[64:65]\ns_mov_b32 s42, 1605632\ns_mov_b32 s50, 1\n.GL20:\nMvaa 64,160,128\nTa 224,16,0\n"
"Mvaa 64,164,132\nTa 226,16,2048\nMvaa 96,164,140\ns_add_u32 m0, s47, -65536\ns_add_u32 s45, s45, 128\nMvaa 96,160,136\nTa 228,16,16384\nMvaa 128,160,144\nTa 230,16,18432\n"
"Mvaa 128,164,148\nD0 2,20,45,0\nMvaa 160,164,156\nTa 232,17,0\nMvaa 160,160,152\nTa 234,17,2048\nMvaa 164,168,152\nMvaa 164,172,156\nTa 236,17,16384\nMvaa 132,172,148\n"
"Ta 238,17,18432\nMvaa 132,168,144\nMvaa 100,168,136\nTa 240,18,0\nMvaa 100,172,140\nTa 242,18,2048\nMvaa 68,172,132\nD0 3,20,45,1024\nMvaa 68,168,128\nTa 244,18,16384\n"
"Mvaa 72,176,128\nTa 246,18,18432\nMvaa 72,180,132\nMvaa 104,180,140\nTa 248,19,0\nMvaa 104,176,136\nTa 250,19,2048\nMvaa 136,176,144\nMvaa 136,180,148\nTa 252,19,16384\n"
"Mvaa 168,180,156\nTa 254,19,18432\nMvaa 168,176,152\nD0 4,20,45,2048\nMvaa 172,184,152\nMvaa 172,188,156\nMvaa 140,188,148\nMvaa 140,184,144\ns_mov_b32 s40, s76\n"
"s_and_b32 s41, s77, 0xffff\nMvaa 108,184,136\nMvaa 108,188,140\nD0 5,20,45,3072\nMvaa 76,188,132\nMvaa 76,184,128\ns_waitcnt vmcnt(17) lgkmcnt(0)\ns_barrier\nMvaa 80,224,128\n"
"Ra 192,10,16384\nMvaa 80,228,132\ns_add_u32 m0, s47, 0\ns_add_u32 s44, s44, 0x40000\nbuffer_load_dword v31, v26, s[40:43], s97 offen\ns_mov_b64 s[76:77], s[66:67]\n"
"Mvaa 112,228,140\nRa 196,11,16384\nMvaa 112,224,136\nMvaa 144,224,144\nRa 200,10,18432\nMvaa 144,228,148\nD0 6,24,44,0\nMvaa 176,228,156\nRa 204,11,18432\nMvaa 176,224,152\n"
"Mvaa 180,232,152\nRa 208,10,24576\nMvaa 180,236,156\nMvaa 148,236,148\nRa 212,11,24576\nMvaa 148,232,144\nMvaa 116,232,136\nRa 216,10,26624\nMvaa 116,236,140\nD0 7,24,44,1024\n"
"Mvaa 84,236,132\nRa 220,11,26624\nMvaa 84,232,128\nMvaa 88,240,128\nMvaa 88,244,132\nMvaa 120,244,140\nMvaa 120,240,136\nMvaa 152,240,144\nMvaa 152,244,148\nD0 8,24,44,2048\n"
"Mvaa 184,244,156\nMvaa 184,240,152\nMvaa 188,248,152\nMvaa 188,252,156\nMvaa 156,252,148\nMvaa 156,248,144\nMvaa 124,248,136\nMvaa 124,252,140\nD0 9,24,44,3072\nMvaa 92,252,132\n"
"Mvaa 92,248,128\nWL 0\nMaaa 16,224,192\nRa 128,10,32768\nMaaa 16,228,196\ns_add_u32 m0, s47, 16384\ns_add_u32 s44, s44, 0x40000\nMaaa 48,228,204\nRa 132,11,32768\nMaaa 48,224,200\n"
"Maaa 80,224,208\nRa 136,10,34816\nMaaa 80,228,212\nD0 6,24,44,0\nMaaa 112,228,220\nRa 140,11,34816\nMaaa 112,224,216\nMaaa 116,232,216\nRa 144,10,40960\nMaaa 116,236,220\n"
"Maaa 84,236,212\nRa 148,11,40960\nMaaa 84,232,208\nMaaa 52,232,200\nRa 152,10,43008\nMaaa 52,236,204\nD0 7,24,44,1024\nMaaa 20,236,196\nRa 156,11,43008\nMaaa 20,232,192\n"
"Maaa 24,240,192\nMaaa 24,244,196\nMaaa 56,244,204\nMaaa 56,240,200\nMaaa 88,240,208\nMaaa 88,244,212\nD0 8,24,44,2048\nMaaa 120,244,220\nMaaa 120,240,216\nMaaa 124,248,216\n"
"Maaa 124,252,220\nMaaa 92,252,212\nMaaa 92,248,208\nMaaa 60,248,200\nMaaa 60,252,204\nD0 9,24,44,3072\nMaaa 28,252,196\nMaaa 28,248,192\ns_waitcnt vmcnt(17) lgkmcnt(0)\ns_barrier\n"
"Maaa 0,160,192\nTa 224,16,32768\nMaaa 0,164,196\nTa 226,16,34816\nMaaa 32,164,204\ns_add_u32 m0, s47, -49152\ns_add_u32 s48, s48, 128\nMaaa 32,160,200\nTa 228,16,49152\n"
"Maaa 64,160,208\nTa 230,16,51200\nMaaa 64,164,212\nD0 2,20,48,0\nMaaa 96,164,220\nTa 232,17,32768\nMaaa 96,160,216\nTa 234,17,34816\nMaaa 100,168,216\nMaaa 100,172,220\n"
"Ta 236,17,49152\nMaaa 68,172,212\nTa 238,17,51200\nMaaa 68,168,208\nMaaa 36,168,200\nTa 240,18,32768\nMaaa 36,172,204\nTa 242,18,34816\nMaaa 4,172,196\nD0 3,20,48,1024\n"
"Maaa 4,168,192\nTa 244,18,49152\nMaaa 8,176,192\nTa 246,18,51200\nMaaa 8,180,196\nMaaa 40,180,204\nTa 248,19,32768\nMaaa 40,176,200\nTa 250,19,34816\nMaaa 72,176,208\n"
"Maaa 72,180,212\nTa 252,19,49152\nMaaa 104,180,220\nTa 254,19,51200\nMaaa 104,176,216\nD0 4,20,48,2048\nMaaa 108,184,216\nMaaa 108,188,220\nMaaa 76,188,212\nMaaa 76,184,208\n"
"Maaa 44,184,200\nMaaa 44,188,204\nD0 5,20,48,3072\nMaaa 12,188,196\nMaaa 12,184,192\nWL 0\nMvaa 80,224,128\nTa 160,12,32768\nMvaa 80,228,132\nTa 162,12,34816\nMvaa 112,228,140\n"
"s_add_u32 m0, s47, -32768\ns_add_u32 s45, s45, 128\nMvaa 112,224,136\nTa 164,12,49152\nMvaa 144,224,144\nTa 166,12,51200\nMvaa 144,228,148\nD0 2,20,45,0\nMvaa 176,228,156\n"
"Ta 168,13,32768\nMvaa 176,224,152\nTa 170,13,34816\nMvaa 180,232,152\nMvaa 180,236,156\nTa 172,13,49152\nMvaa 148,236,148\nTa 174,13,51200\nMvaa 148,232,144\nMvaa 116,232,136\n"
"Ta 176,14,32768\nMvaa 116,236,140\nTa 178,14,34816\nMvaa 84,236,132\nD0 3,20,45,1024\nMvaa 84,232,128\nTa 180,14,49152\nMvaa 88,240,128\nTa 182,14,51200\nMvaa 88,244,132\n"
"Mvaa 120,244,140\nTa 184,15,32768\nMvaa 120,240,136\nTa 186,15,34816\nMvaa 152,240,144\nMvaa 152,244,148\nTa 188,15,49152\nMvaa 184,244,156\nTa 190,15,51200\nMvaa 184,240,152\n"
"D0 4,20,45,2048\nMvaa 188,248,152\nMvaa 188,252,156\nMvaa 156,252,148\nMvaa 156,248,144\ns_mov_b32 s40, s78\ns_and_b32 s41, s79, 0xffff\nMvaa 124,248,136\nMvaa 124,252,140\n"
"D0 5,20,45,3072\nMvaa 92,252,132\nMvaa 92,248,128\ns_waitcnt vmcnt(17) lgkmcnt(0)\ns_barrier\nMvaa 64,160,128\nRa 192,10,49152\nMvaa 64,164,132\ns_add_u32 m0, s47, 32768\n"
"s_add_u32 s44, s44, 0x40000\nbuffer_load_dword v31, v26, s[40:43], s97 offen\ns_mov_b64 s[78:79], s[66:67]\nMvaa 96,164,140\nRa 196,11,49152\nMvaa 96,160,136\nMvaa 128,160,144\n"
"Ra 200,10,51200\nMvaa 128,164,148\nD0 6,24,44,0\nMvaa 160,164,156\nRa 204,11,51200\nMvaa 160,160,152\nMvaa 164,168,152\nRa 208,10,57344\nMvaa 164,172,156\nMvaa 132,172,148\n"
"Ra 212,11,57344\nMvaa 132,168,144\nMvaa 100,168,136\nRa 216,10,59392\nMvaa 100,172,140\nD0 7,24,44,1024\nMvaa 68,172,132\nRa 220,11,59392\nMvaa 68,168,128\nMvaa 72,176,128\n"
"Mvaa 72,180,132\nMvaa 104,180,140\nMvaa 104,176,136\nMvaa 136,176,144\nMvaa 136,180,148\nD0 8,24,44,2048\nMvaa 168,180,156\nMvaa 168,176,152\nMvaa 172,184,152\nMvaa 172,188,156\n"
"Mvaa 140,188,148\nMvaa 140,184,144\nMvaa 108,184,136\nMvaa 108,188,140\nD0 9,24,44,3072\nMvaa 76,188,132\nMvaa 76,184,128\nWL 0\nMaaa 0,160,192\nRa 128,10,0\nMaaa 0,164,196\n"
"s_add_u32 m0, s47, 49152\ns_add_u32 s44, s44, 0x40000\nMaaa 32,164,204\nRa 132,11,0\nMaaa 32,160,200\nMaaa 64,160,208\nRa 136,10,2048\nMaaa 64,164,212\nD0 6,24,44,0\n"
"Maaa 96,164,220\nRa 140,11,2048\nMaaa 96,160,216\nMaaa 100,168,216\nRa 144,10,8192\nMaaa 100,172,220\nMaaa 68,172,212\nRa 148,11,8192\nMaaa 68,168,208\nMaaa 36,168,200\n"
"Ra 152,10,10240\nMaaa 36,172,204\nD0 7,24,44,1024\nMaaa 4,172,196\nRa 156,11,10240\nMaaa 4,168,192\nMaaa 8,176,192\nMaaa 8,180,196\nMaaa 40,180,204\nMaaa 40,176,200\n"
"Maaa 72,176,208\nMaaa 72,180,212\nD0 8,24,44,2048\nMaaa 104,180,220\nMaaa 104,176,216\nMaaa 108,184,216\nMaaa 108,188,220\nMaaa 76,188,212\nMaaa 76,184,208\nMaaa 44,184,200\n"
"Maaa 44,188,204\nD0 9,24,44,3072\nMaaa 12,188,196\nMaaa 12,184,192\ns_waitcnt vmcnt(17) lgkmcnt(0)\ns_barrier\nMaaa 16,224,192\nTa 160,12,0\nMaaa 16,228,196\nTa 162,12,2048\n"
"Maaa 48,228,204\ns_add_u32 m0, s47, -16384\ns_add_u32 s48, s48, 128\nMaaa 48,224,200\nTa 164,12,16384\nMaaa 80,224,208\nTa 166,12,18432\nMaaa 80,228,212\nD0 2,20,48,0\n"
"Maaa 112,228,220\nTa 168,13,0\nMaaa 112,224,216\nTa 170,13,2048\nMaaa 116,232,216\nMaaa 116,236,220\nTa 172,13,16384\nMaaa 84,236,212\nTa 174,13,18432\nMaaa 84,232,208\n"
"Maaa 52,232,200\nTa 176,14,0\nMaaa 52,236,204\nTa 178,14,2048\nMaaa 20,236,196\nD0 3,20,48,1024\nMaaa 20,232,192\nTa 180,14,16384\nMaaa 24,240,192\nTa 182,14,18432\n"
"Maaa 24,244,196\nMaaa 56,244,204\nTa 184,15,0\nMaaa 56,240,200\nTa 186,15,2048\nMaaa 88,240,208\nMaaa 88,244,212\nTa 188,15,16384\nMaaa 120,244,220\nTa 190,15,18432\n"
"Maaa 120,240,216\nD0 4,20,48,2048\nMaaa 124,248,216\nMaaa 124,252,220\nMaaa 92,252,212\nMaaa 92,248,208\nMaaa 60,248,200\nMaaa 60,252,204\nD0 5,20,48,3072\ns_sub_u32 s50, s50, 1\n"
"Maaa 28,252,196\nMaaa 28,248,192\nWL 0\ns_cbranch_scc0 .GL20\nMvaa 64,160,128\nTa 224,16,0\nMvaa 64,164,132\nTa 226,16,2048\nMvaa 96,164,140\ns_add_u32 m0, s47, -65536\n"
"s_add_u32 s45, s45, 128\nMvaa 96,160,136\nTa 228,16,16384\nMvaa 128,160,144\nTa 230,16,18432\nMvaa 128,164,148\nD0 2,20,45,0\nMvaa 160,164,156\nTa 232,17,0\nMvaa 160,160,152\n"
"Ta 234,17,2048\nMvaa 164,168,152\ns_and_b32 s2, s74, 15\nMvaa 164,172,156\nTa 236,17,16384\nbuffer_store_dword v25, off, s[16:19], s90 offset:0 sc0 sc1\nMvaa 132,172,148\n"
"Ta 238,17,18432\nMvaa 132,168,144\ns_sub_u32 s2, s2, 2\ns_cmp_lt_u32 s2, 14\ns_cselect_b32 s5, 0, 0x40000000\nMvaa 100,168,136\nTa 240,18,0\nMvaa 100,172,140\nTa 242,18,2048\n"
"Mvaa 68,172,132\nD0 3,20,45,1024\nMvaa 68,168,128\nTa 244,18,16384\nMvaa 72,176,128\nTa 246,18,18432\nMvaa 72,180,132\ns_and_b32 s2, s2, 15\nMvaa 104,180,140\nTa 248,19,0\n"
"Mvaa 104,176,136\nTa 250,19,2048\nMvaa 136,176,144\ns_mul_i32 s4, s73, 14\nMvaa 136,180,148\nTa 252,19,16384\nMvaa 168,180,156\nTa 254,19,18432\nMvaa 168,176,152\nD0 4,20,45,2048\n"
"Mvaa 172,184,152\ns_add_u32 s4, s4, s2\nMvaa 172,188,156\ns_mov_b32 s36, s62\nMvaa 140,188,148\ns_and_b32 s37, s63, 0xffff\nMvaa 140,184,144\ns_mov_b32 s38, 0x188000\n"
"Mvaa 108,184,136\ns_mov_b32 s39, 0x20000\nMvaa 108,188,140\nD0 5,20,45,3072\nMvaa 76,188,132\ns_mov_b32 s40, s64\nMvaa 76,184,128\ns_waitcnt vmcnt(18) lgkmcnt(0)\ns_barrier\n"
"Mvaa 80,224,128\nRa 192,10,16384\nMvaa 80,228,132\ns_add_u32 m0, s47, 0\ns_add_u32 s44, s44, 0x40000\nMvaa 112,228,140\nRa 196,11,16384\nMvaa 112,224,136\n"
"s_and_b32 s41, s65, 0xffff\nMvaa 144,224,144\nRa 200,10,18432\nMvaa 144,228,148\nD0 6,24,44,0\nMvaa 176,228,156\nRa 204,11,18432\nMvaa 176,224,152\ns_mov_b32 s42, 0x188000\n"
"Mvaa 180,232,152\nRa 208,10,24576\nMvaa 180,236,156\ns_mov_b32 s43, 0x20000\nMvaa 148,236,148\nRa 212,11,24576\nMvaa 148,232,144\ns_mov_b32 s92, s66\nMvaa 116,232,136\n"
"Ra 216,10,26624\nMvaa 116,236,140\nD0 7,24,44,1024\nMvaa 84,236,132\nRa 220,11,26624\nMvaa 84,232,128\ns_and_b32 s93, s67, 0xffff\nMvaa 88,240,128\ns_mov_b32 s94, 0x188000\n"
"Mvaa 88,244,132\ns_mov_b32 s95, 0x20000\nMvaa 120,244,140\nv_lshrrev_b32 v252, 1, v0\nMvaa 120,240,136\nv_mul_u32_u24 v253, 14336, v252\nMvaa 152,240,144\nv_and_b32 v254, 1, v0\n"
"Mvaa 152,244,148\nD0 8,24,44,2048\nMvaa 184,244,156\nv_lshl_add_u32 v253, v254, 4, v253\nMvaa 184,240,152\nv_mov_b32 v254, 0x40000000\nv_cmp_gt_u32 vcc, 56, v0\n"
"v_lshlrev_b32 v252, 3, v252\ns_mul_i32 s2, s11, 401408\nv_cndmask_b32 v21, v254, v253, vcc\nv_cndmask_b32 v255, v254, v252, vcc\nMvaa 188,248,152\ns_lshl_b32 s3, s4, 5\n"
"Mvaa 188,252,156\ns_add_u32 s2, s2, s3\nMvaa 156,252,148\ns_or_b32 s86, s2, s5\nMvaa 156,248,144\ns_add_u32 s87, s86, 7168\nMvaa 124,248,136\nv_lshrrev_b32 v251, 4, v0\n"
"Mvaa 124,252,140\nD0 9,24,44,3072\nMvaa 92,252,132\nv_mul_u32_u24 v252, 3584, v251\nMvaa 92,248,128\nWL 0\nMaaa 16,224,192\nRa 128,10,32768\nMaaa 16,228,196\n"
"s_add_u32 m0, s47, 16384\ns_add_u32 s44, s44, 0x40000\nMaaa 48,228,204\nRa 132,11,32768\nMaaa 48,224,200\nv_and_b32 v251, 15, v0\nMaaa 80,224,208\nRa 136,10,34816\n"
"Maaa 80,228,212\nD0 6,24,44,0\nMaaa 112,228,220\nRa 140,11,34816\nMaaa 112,224,216\nv_lshl_add_u32 v252, v251, 4, v252\nv_cmp_gt_u32 vcc, 14, v251\ns_mul_i32 s2, s4, 14336\n"
"s_mul_i32 s3, s11, 224\nv_cndmask_b32 v20, v254, v252, vcc\nMaaa 116,232,216\nRa 144,10,40960\nMaaa 116,236,220\ns_add_u32 s2, s2, s3\nMaaa 84,236,212\nRa 148,11,40960\n"
"Maaa 84,232,208\ns_or_b32 s88, s2, s5\nMaaa 52,232,200\nRa 152,10,43008\nMaaa 52,236,204\nD0 7,24,44,1024\nMaaa 20,236,196\nRa 156,11,43008\nMaaa 20,232,192\nMaaa 24,240,192\n"
"Maaa 24,244,196\nMaaa 56,244,204\nMaaa 56,240,200\nMaaa 88,240,208\nMaaa 88,244,212\nD0 8,24,44,2048\nMaaa 120,244,220\nMaaa 120,240,216\nMaaa 124,248,216\nMaaa 124,252,220\n"
"Maaa 92,252,212\nMaaa 92,248,208\nMaaa 60,248,200\nMaaa 60,252,204\nD0 9,24,44,3072\nMaaa 28,252,196\nMaaa 28,248,192\ns_waitcnt vmcnt(17) lgkmcnt(0)\ns_barrier\nMaaa 0,160,192\n"
"Ta 224,16,32768\nMaaa 0,164,196\nTa 226,16,34816\nbuffer_load_dwordx4 v[192:195], v21, s[36:39], s86 offen offset:0\nMaaa 32,164,204\ns_add_u32 m0, s47, -49152\n"
"s_add_u32 s48, s48, 128\nMaaa 32,160,200\nTa 228,16,49152\ns_lshl_b32 s6, s11, 12\ns_add_u32 s6, s6, 0xc044000\nMaaa 64,160,208\nTa 230,16,51200\nMaaa 64,164,212\nD0 2,20,48,0\n"
"Maaa 96,164,220\nTa 232,17,32768\nMaaa 96,160,216\nTa 234,17,34816\nMaaa 100,168,216\nbuffer_load_dwordx4 v[32:35], v1, s[16:19], s6 offen offset:0 sc0 sc1\nMaaa 100,172,220\n"
"Ta 236,17,49152\nMaaa 68,172,212\nTa 238,17,51200\nMaaa 68,168,208\nMaaa 36,168,200\nTa 240,18,32768\nMaaa 36,172,204\nTa 242,18,34816\nMaaa 4,172,196\nD0 3,20,48,1024\n"
"Maaa 4,168,192\nTa 244,18,49152\nMaaa 8,176,192\nTa 246,18,51200\nMaaa 8,180,196\nbuffer_load_dwordx4 v[36:39], v1, s[16:19], s6 offen offset:1024 sc0 sc1\nMaaa 40,180,204\n"
"Ta 248,19,32768\nMaaa 40,176,200\nTa 250,19,34816\nMaaa 72,176,208\nMaaa 72,180,212\nTa 252,19,49152\nMaaa 104,180,220\nTa 254,19,51200\nMaaa 104,176,216\nD0 4,20,48,2048\n"
"Maaa 108,184,216\nbuffer_load_dwordx4 v[40:43], v1, s[16:19], s6 offen offset:2048 sc0 sc1\nMaaa 108,188,220\nMaaa 76,188,212\nMaaa 76,184,208\nMaaa 44,184,200\n"
"buffer_load_dwordx4 v[44:47], v1, s[16:19], s6 offen offset:3072 sc0 sc1\nMaaa 44,188,204\nD0 5,20,48,3072\nMaaa 12,188,196\nMaaa 12,184,192\nWL 0\nMvaa 80,224,128\n"
"Ta 160,12,32768\nMvaa 80,228,132\nTa 162,12,34816\nMvaa 112,228,140\ns_add_u32 m0, s47, -32768\ns_add_u32 s45, s45, 128\nMvaa 112,224,136\nTa 164,12,49152\nMvaa 144,224,144\n"
"Ta 166,12,51200\nMvaa 144,228,148\nD0 2,20,45,0\nMvaa 176,228,156\nTa 168,13,32768\nMvaa 176,224,152\nTa 170,13,34816\nMvaa 180,232,152\nMvaa 180,236,156\nTa 172,13,49152\n"
"Mvaa 148,236,148\nTa 174,13,51200\nMvaa 148,232,144\nMvaa 116,232,136\nTa 176,14,32768\nMvaa 116,236,140\nTa 178,14,34816\nMvaa 84,236,132\nD0 3,20,45,1024\nMvaa 84,232,128\n"
"Ta 180,14,49152\nMvaa 88,240,128\nTa 182,14,51200\nMvaa 88,244,132\nMvaa 120,244,140\nTa 184,15,32768\nMvaa 120,240,136\nTa 186,15,34816\nMvaa 152,240,144\nMvaa 152,244,148\n"
"Ta 188,15,49152\nMvaa 184,244,156\nTa 190,15,51200\nMvaa 184,240,152\nD0 4,20,45,2048\nMvaa 188,248,152\nMvaa 188,252,156\nMvaa 156,252,148\nMvaa 156,248,144\nMvaa 124,248,136\n"
"Mvaa 124,252,140\nD0 5,20,45,3072\nMvaa 92,252,132\nMvaa 92,248,128\ns_waitcnt vmcnt(21) lgkmcnt(0)\ns_barrier\nMvaa 64,160,128\nRa 192,10,49152\nMvaa 64,164,132\n"
"s_add_u32 m0, s47, 32768\ns_add_u32 s44, s44, 0x40000\nbuffer_load_dwordx4 v[208:211], v21, s[40:43], s86 offen offset:0\nMvaa 96,164,140\nRa 196,11,49152\nMvaa 96,160,136\n"
"Mvaa 128,160,144\nRa 200,10,51200\nMvaa 128,164,148\nD0 6,24,44,0\nMvaa 160,164,156\nRa 204,11,51200\nMvaa 160,160,152\nMvaa 164,168,152\nRa 208,10,57344\nMvaa 164,172,156\n"
"Mvaa 132,172,148\nRa 212,11,57344\nMvaa 132,168,144\nMvaa 100,168,136\nRa 216,10,59392\nMvaa 100,172,140\nD0 7,24,44,1024\nMvaa 68,172,132\nRa 220,11,59392\nMvaa 68,168,128\n"
"Mvaa 72,176,128\nMvaa 72,180,132\nMvaa 104,180,140\nMvaa 104,176,136\nMvaa 136,176,144\nMvaa 136,180,148\nD0 8,24,44,2048\nMvaa 168,180,156\nMvaa 168,176,152\nMvaa 172,184,152\n"
"Mvaa 172,188,156\nMvaa 140,188,148\nMvaa 140,184,144\nMvaa 108,184,136\nMvaa 108,188,140\nD0 9,24,44,3072\nMvaa 76,188,132\nMvaa 76,184,128\nWL 0\nMaaa 0,160,192\nRa 128,10,0\n"
"Maaa 0,164,196\ns_add_u32 m0, s47, 49152\ns_add_u32 s44, s44, 0x40000\nMaaa 32,164,204\nRa 132,11,0\nMaaa 32,160,200\nMaaa 64,160,208\nRa 136,10,2048\nMaaa 64,164,212\n"
"D0 6,24,44,0\nMaaa 96,164,220\nRa 140,11,2048\nMaaa 96,160,216\nMaaa 100,168,216\nRa 144,10,8192\nMaaa 100,172,220\nMaaa 68,172,212\nRa 148,11,8192\nMaaa 68,168,208\n"
"Maaa 36,168,200\nRa 152,10,10240\nMaaa 36,172,204\nD0 7,24,44,1024\nMaaa 4,172,196\nRa 156,11,10240\nMaaa 4,168,192\nMaaa 8,176,192\nMaaa 8,180,196\nMaaa 40,180,204\n"
"Maaa 40,176,200\nMaaa 72,176,208\nMaaa 72,180,212\nD0 8,24,44,2048\nMaaa 104,180,220\nMaaa 104,176,216\nMaaa 108,184,216\nMaaa 108,188,220\nMaaa 76,188,212\nMaaa 76,184,208\n"
"Maaa 44,184,200\nMaaa 44,188,204\nD0 9,24,44,3072\nMaaa 12,188,196\nMaaa 12,184,192\ns_waitcnt vmcnt(14) lgkmcnt(0)\ns_barrier\nMaaa 16,224,192\nTa 160,12,0\n"
"v_xor_b32 v48, s15, v33\nMaaa 16,228,196\nTa 162,12,2048\nbuffer_load_dwordx4 v[196:199], v21, s[36:39], s86 offen offset:3584\nMaaa 48,228,204\ns_add_u32 m0, s47, -16384\n"
"s_add_u32 s48, s48, 128\nMaaa 48,224,200\nTa 164,12,16384\nMaaa 80,224,208\nTa 166,12,18432\nMaaa 80,228,212\nD0 2,20,48,0\nMaaa 112,228,220\nTa 168,13,0\nMaaa 112,224,216\n"
"Ta 170,13,2048\nMaaa 116,232,216\nv_xor_b32 v49, s15, v35\nMaaa 116,236,220\nTa 172,13,16384\nMaaa 84,236,212\nTa 174,13,18432\nMaaa 84,232,208\nv_xor_b32 v50, s15, v37\n"
"Maaa 52,232,200\nTa 176,14,0\nMaaa 52,236,204\nTa 178,14,2048\nMaaa 20,236,196\nD0 3,20,48,1024\nMaaa 20,232,192\nTa 180,14,16384\nMaaa 24,240,192\nTa 182,14,18432\n"
"v_xor_b32 v51, s15, v39\nMaaa 24,244,196\nv_xor_b32 v52, s15, v41\nMaaa 56,244,204\nTa 184,15,0\nMaaa 56,240,200\nTa 186,15,2048\nMaaa 88,240,208\nv_xor_b32 v53, s15, v43\n"
"Maaa 88,244,212\nTa 188,15,16384\nMaaa 120,244,220\nTa 190,15,18432\nMaaa 120,240,216\nD0 4,20,48,2048\nMaaa 124,248,216\nv_xor_b32 v54, s15, v45\nMaaa 124,252,220\n"
"v_xor_b32 v55, s15, v47\nMaaa 92,252,212\nv_or3_b32 v48, v48, v49, v50\nMaaa 92,248,208\nv_or3_b32 v51, v51, v52, v53\nMaaa 60,248,200\nv_or3_b32 v48, v48, v54, v55\n"
"Maaa 60,252,204\nD0 5,20,48,3072\nMaaa 28,252,196\nv_or_b32 v48, v48, v51\nMaaa 28,248,192\nWL 0\nMvaa 64,160,128\nTa 224,16,0\nv_cmp_ne_u32 vcc, 0, v48\nMvaa 64,164,132\n"
"Ta 226,16,2048\nMvaa 96,164,140\ns_add_u32 m0, s47, -65536\ns_add_u32 s45, s45, 128\nMvaa 96,160,136\nTa 228,16,16384\nMvaa 128,160,144\nTa 230,16,18432\nMvaa 128,164,148\n"
"D0 2,20,45,0\nMvaa 160,164,156\nTa 232,17,0\nMvaa 160,160,152\nTa 234,17,2048\nMvaa 164,168,152\ns_cbranch_vccnz .S14\n.S15:\nMvaa 164,172,156\nTa 236,17,16384\nMvaa 132,172,148\n"
"Ta 238,17,18432\nMvaa 132,168,144\nv_max_u32 v48, v32, v34\nMvaa 100,168,136\nTa 240,18,0\nMvaa 100,172,140\nTa 242,18,2048\nMvaa 68,172,132\nD0 3,20,45,1024\nMvaa 68,168,128\n"
"Ta 244,18,16384\nMvaa 72,176,128\nTa 246,18,18432\nv_min_u32 v49, v32, v34\nMvaa 72,180,132\nv_med3_u32 v49, v48, v49, v36\nMvaa 104,180,140\nTa 248,19,0\nMvaa 104,176,136\n"
"Ta 250,19,2048\nMvaa 136,176,144\nv_max_u32 v48, v48, v36\nMvaa 136,180,148\nTa 252,19,16384\nMvaa 168,180,156\nTa 254,19,18432\nMvaa 168,176,152\nD0 4,20,45,2048\n"
"Mvaa 172,184,152\nv_med3_u32 v49, v48, v49, v38\nMvaa 172,188,156\nv_max_u32 v48, v48, v38\nMvaa 140,188,148\nv_med3_u32 v49, v48, v49, v40\nMvaa 140,184,144\n"
"v_max_u32 v48, v48, v40\nMvaa 108,184,136\nv_med3_u32 v49, v48, v49, v42\nMvaa 108,188,140\nD0 5,20,45,3072\nMvaa 76,188,132\nv_max_u32 v48, v48, v42\nMvaa 76,184,128\n"
"s_waitcnt vmcnt(18) lgkmcnt(0)\ns_barrier\nMvaa 80,224,128\nRa 192,10,16384\nv_med3_u32 v49, v48, v49, v44\nMvaa 80,228,132\ns_add_u32 m0, s47, 0\ns_add_u32 s44, s44, 0x40000\n"
"buffer_load_dwordx4 v[212:215], v21, s[40:43], s86 offen offset:3584\nMvaa 112,228,140\nRa 196,11,16384\nMvaa 112,224,136\nv_max_u32 v48, v48, v44\nMvaa 144,224,144\n"
"Ra 200,10,18432\nMvaa 144,228,148\nD0 6,24,44,0\nMvaa 176,228,156\nRa 204,11,18432\nMvaa 176,224,152\nv_med3_u32 v49, v48, v49, v46\nMvaa 180,232,152\nRa 208,10,24576\n"
"Mvaa 180,236,156\nv_max_u32 v48, v48, v46\nMvaa 148,236,148\nRa 212,11,24576\nMvaa 148,232,144\nv_mov_b32 v50, v48\nMvaa 116,232,136\nRa 216,10,26624\nMvaa 116,236,140\n"
"D0 7,24,44,1024\nMvaa 84,236,132\nRa 220,11,26624\nMvaa 84,232,128\nv_max_u32_dpp v50, v50, v50 row_ror:8 row_mask:0xf bank_mask:0xf\nMvaa 88,240,128\nMvaa 88,244,132\n"
"v_max_u32_dpp v50, v50, v50 row_ror:4 row_mask:0xf bank_mask:0xf\nMvaa 120,244,140\nMvaa 120,240,136\nv_max_u32_dpp v50, v50, v50 row_ror:2 row_mask:0xf bank_mask:0xf\n"
"Mvaa 152,240,144\nMvaa 152,244,148\nD0 8,24,44,2048\nMvaa 184,244,156\nv_max_u32_dpp v50, v50, v50 row_ror:1 row_mask:0xf bank_mask:0xf\nMvaa 184,240,152\nv_mov_b32 v52, v50\n"
"Mvaa 188,248,152\nMvaa 188,252,156\nv_permlane16_swap_b32 v50, v52\nMvaa 156,252,148\nv_max_u32 v50, v50, v52\nMvaa 156,248,144\nv_mov_b32 v52, v50\nMvaa 124,248,136\n"
"Mvaa 124,252,140\nD0 9,24,44,3072\nMvaa 92,252,132\nv_permlane32_swap_b32 v50, v52\nMvaa 92,248,128\nWL 0\nMaaa 16,224,192\nRa 128,10,32768\nv_max_u32 v50, v50, v52\n"
"Maaa 16,228,196\ns_add_u32 m0, s47, 16384\ns_add_u32 s44, s44, 0x40000\nMaaa 48,228,204\nRa 132,11,32768\nv_cmp_eq_u32 vcc, v48, v50\ns_lshl_b32 s2, s11, 3\n"
"s_add_u32 s2, s2, 131072\nv_cndmask_b32 v51, v48, v49, vcc\nMaaa 48,224,200\nMaaa 80,224,208\nRa 136,10,34816\nMaaa 80,228,212\nD0 6,24,44,0\nMaaa 112,228,220\nRa 140,11,34816\n"
"Maaa 112,224,216\nv_max_u32_dpp v51, v51, v51 row_ror:8 row_mask:0xf bank_mask:0xf\nMaaa 116,232,216\nRa 144,10,40960\nMaaa 116,236,220\n"
"v_max_u32_dpp v51, v51, v51 row_ror:4 row_mask:0xf bank_mask:0xf\nMaaa 84,236,212\nRa 148,11,40960\nMaaa 84,232,208\n"
"v_max_u32_dpp v51, v51, v51 row_ror:2 row_mask:0xf bank_mask:0xf\nMaaa 52,232,200\nRa 152,10,43008\nMaaa 52,236,204\nD0 7,24,44,1024\nMaaa 20,236,196\nRa 156,11,43008\n"
"Maaa 20,232,192\nv_max_u32_dpp v51, v51, v51 row_ror:1 row_mask:0xf bank_mask:0xf\nMaaa 24,240,192\nv_mov_b32 v52, v51\nMaaa 24,244,196\nMaaa 56,244,204\n"
"v_permlane16_swap_b32 v51, v52\nMaaa 56,240,200\nv_max_u32 v51, v51, v52\nMaaa 88,240,208\nv_mov_b32 v52, v51\nMaaa 88,244,212\nD0 8,24,44,2048\nMaaa 120,244,220\n"
"v_permlane32_swap_b32 v51, v52\nMaaa 120,240,216\nv_max_u32 v51, v51, v52\nMaaa 124,248,216\nv_mov_b32 v52, s2\nMaaa 124,252,220\nds_write_b64 v52, v[50:51]\nMaaa 92,252,212\n"
"v_mov_b32 v52, 131072\nMaaa 92,248,208\nMaaa 60,248,200\nMaaa 60,252,204\nD0 9,24,44,3072\nMaaa 28,252,196\nMaaa 28,248,192\ns_waitcnt vmcnt(18) lgkmcnt(0)\ns_barrier\n"
"Maaa 0,160,192\nTa 224,16,32768\nMaaa 0,164,196\nTa 226,16,34816\nbuffer_load_dwordx4 v[200:203], v21, s[36:39], s87 offen offset:0\nMaaa 32,164,204\ns_add_u32 m0, s47, -49152\n"
"s_add_u32 s48, s48, 128\nMaaa 32,160,200\nTa 228,16,49152\nMaaa 64,160,208\nTa 230,16,51200\nMaaa 64,164,212\nD0 2,20,48,0\nMaaa 96,164,220\nTa 232,17,32768\nMaaa 96,160,216\n"
"Ta 234,17,34816\nMaaa 100,168,216\nRv 32,52,0\nMaaa 100,172,220\nTa 236,17,49152\nMaaa 68,172,212\nTa 238,17,51200\nMaaa 68,168,208\nRv 36,52,16\nMaaa 36,168,200\nTa 240,18,32768\n"
"Maaa 36,172,204\nTa 242,18,34816\nMaaa 4,172,196\nD0 3,20,48,1024\nMaaa 4,168,192\nTa 244,18,49152\nMaaa 8,176,192\nTa 246,18,51200\nMaaa 8,180,196\nMaaa 40,180,204\n"
"Ta 248,19,32768\nMaaa 40,176,200\nTa 250,19,34816\nMaaa 72,176,208\nMaaa 72,180,212\nTa 252,19,49152\nMaaa 104,180,220\nTa 254,19,51200\nMaaa 104,176,216\nD0 4,20,48,2048\n"
"Maaa 108,184,216\nWL 11\nMaaa 108,188,220\nv_max_u32 v48, v32, v33\nMaaa 76,188,212\nv_min_u32 v49, v32, v33\nMaaa 76,184,208\nv_med3_u32 v49, v48, v49, v34\nMaaa 44,184,200\n"
"v_max_u32 v48, v48, v34\nMaaa 44,188,204\nD0 5,20,48,3072\nMaaa 12,188,196\nv_med3_u32 v49, v48, v49, v35\nMaaa 12,184,192\nWL 0\nMvaa 80,224,128\nTa 160,12,32768\n"
"Mvaa 80,228,132\nTa 162,12,34816\nMvaa 112,228,140\ns_add_u32 m0, s47, -32768\ns_add_u32 s45, s45, 128\nMvaa 112,224,136\nTa 164,12,49152\nMvaa 144,224,144\nTa 166,12,51200\n"
"Mvaa 144,228,148\nD0 2,20,45,0\nMvaa 176,228,156\nTa 168,13,32768\nMvaa 176,224,152\nTa 170,13,34816\nMvaa 180,232,152\nv_max_u32 v48, v48, v35\nMvaa 180,236,156\nTa 172,13,49152\n"
"Mvaa 148,236,148\nTa 174,13,51200\nMvaa 148,232,144\nv_med3_u32 v49, v48, v49, v36\nMvaa 116,232,136\nTa 176,14,32768\nMvaa 116,236,140\nTa 178,14,34816\nMvaa 84,236,132\n"
"D0 3,20,45,1024\nMvaa 84,232,128\nTa 180,14,49152\nMvaa 88,240,128\nTa 182,14,51200\nMvaa 88,244,132\nv_max_u32 v48, v48, v36\nMvaa 120,244,140\nTa 184,15,32768\nMvaa 120,240,136\n"
"Ta 186,15,34816\nMvaa 152,240,144\nv_med3_u32 v49, v48, v49, v37\nMvaa 152,244,148\nTa 188,15,49152\nMvaa 184,244,156\nTa 190,15,51200\nMvaa 184,240,152\nD0 4,20,45,2048\n"
"Mvaa 188,248,152\nv_max_u32 v48, v48, v37\nMvaa 188,252,156\nv_med3_u32 v49, v48, v49, v38\nMvaa 156,252,148\nv_max_u32 v48, v48, v38\nMvaa 156,248,144\n"
"v_med3_u32 v49, v48, v49, v39\nMvaa 124,248,136\nv_max_u32 v48, v48, v39\nMvaa 124,252,140\nD0 5,20,45,3072\nMvaa 92,252,132\nv_readfirstlane_b32 s2, v48\nMvaa 92,248,128\n"
"s_waitcnt vmcnt(18) lgkmcnt(0)\ns_barrier\nMvaa 64,160,128\nRa 192,10,49152\nMvaa 64,164,132\ns_add_u32 m0, s47, 32768\ns_add_u32 s44, s44, 0x40000\n"
"buffer_load_dwordx4 v[216:219], v21, s[40:43], s87 offen offset:0\nMvaa 96,164,140\nRa 196,11,49152\nMvaa 96,160,136\nv_readfirstlane_b32 s3, v49\nMvaa 128,160,144\n"
"Ra 200,10,51200\nMvaa 128,164,148\nD0 6,24,44,0\nMvaa 160,164,156\nRa 204,11,51200\nMvaa 160,160,152\ns_andn2_b32 s2, 0x7ff, s2\nMvaa 164,168,152\nRa 208,10,57344\n"
"Mvaa 164,172,156\ns_andn2_b32 s3, 0x7ff, s3\nMvaa 132,172,148\nRa 212,11,57344\nMvaa 132,168,144\ns_lshl_b32 s71, s3, 16\nMvaa 100,168,136\nRa 216,10,59392\nMvaa 100,172,140\n"
"D0 7,24,44,1024\nMvaa 68,172,132\nRa 220,11,59392\nMvaa 68,168,128\ns_or_b32 s71, s71, s2\nMvaa 72,176,128\ns_mul_i32 s2, s2, 896\nMvaa 72,180,132\ns_mul_i32 s3, s3, 896\n"
"Mvaa 104,180,140\ns_mul_i32 s6, s11, 224\nMvaa 104,176,136\ns_add_u32 s2, s2, s6\nMvaa 136,176,144\ns_add_u32 s3, s3, s6\nMvaa 136,180,148\nD0 8,24,44,2048\nMvaa 168,180,156\n"
"buffer_load_dwordx2 v[240:241], v255, s[28:31], s2 offen\nMvaa 168,176,152\nMvaa 172,184,152\nMvaa 172,188,156\nMvaa 140,188,148\n"
"buffer_load_dwordx2 v[242:243], v255, s[28:31], s3 offen\ns_lshl2_add_u32 s2, s10, s11\ns_cmp_eq_u32 s2, 512\ns_cselect_b32 s2, 0, 0x40000000\ns_add_u32 s2, s2, 0xc048000\n"
"Mvaa 140,184,144\nMvaa 108,184,136\nMvaa 108,188,140\nD0 9,24,44,3072\nMvaa 76,188,132\nbuffer_store_dword v25, off, s[16:19], s2 offset:0 sc0 sc1\nMvaa 76,184,128\nWL 0\n"
"Maaa 0,160,192\nRa 128,10,0\nMaaa 0,164,196\ns_add_u32 m0, s47, 49152\ns_add_u32 s44, s44, 0x40000\nMaaa 32,164,204\nRa 132,11,0\nMaaa 32,160,200\nMaaa 64,160,208\nRa 136,10,2048\n"
"Maaa 64,164,212\nD0 6,24,44,0\nMaaa 96,164,220\nRa 140,11,2048\nMaaa 96,160,216\nMaaa 100,168,216\nRa 144,10,8192\nMaaa 100,172,220\nMaaa 68,172,212\nRa 148,11,8192\n"
"Maaa 68,168,208\nMaaa 36,168,200\nRa 152,10,10240\nMaaa 36,172,204\nD0 7,24,44,1024\nMaaa 4,172,196\nRa 156,11,10240\nMaaa 4,168,192\nMaaa 8,176,192\nMaaa 8,180,196\n"
"Maaa 40,180,204\nMaaa 40,176,200\nMaaa 72,176,208\nMaaa 72,180,212\nD0 8,24,44,2048\nMaaa 104,180,220\nMaaa 104,176,216\nMaaa 108,184,216\nMaaa 108,188,220\nMaaa 76,188,212\n"
"Maaa 76,184,208\nMaaa 44,184,200\nMaaa 44,188,204\nD0 9,24,44,3072\nMaaa 12,188,196\nMaaa 12,184,192\ns_waitcnt vmcnt(21) lgkmcnt(0)\ns_barrier\nMaaa 16,224,192\nTa 160,12,0\n"
"Maaa 16,228,196\nTa 162,12,2048\nbuffer_load_dwordx4 v[204:207], v21, s[36:39], s87 offen offset:3584\nMaaa 48,228,204\ns_add_u32 m0, s47, -16384\ns_add_u32 s48, s48, 128\n"
"s_lshl_b32 s5, s73, 8\ns_add_u32 s5, s5, 0xc04ac00\nv_lshrrev_b32 v27, 2, v1\nbuffer_load_dword v27, v27, s[16:19], s5 offen sc0 sc1\nMaaa 48,224,200\nTa 164,12,16384\n"
"Maaa 80,224,208\nTa 166,12,18432\nMaaa 80,228,212\nD0 2,20,48,0\nMaaa 112,228,220\nTa 168,13,0\nMaaa 112,224,216\nTa 170,13,2048\nMaaa 116,232,216\nMaaa 116,236,220\n"
"Ta 172,13,16384\nMaaa 84,236,212\nTa 174,13,18432\nMaaa 84,232,208\nMaaa 52,232,200\nTa 176,14,0\nMaaa 52,236,204\nTa 178,14,2048\nMaaa 20,236,196\nD0 3,20,48,1024\n"
"Maaa 20,232,192\nTa 180,14,16384\nMaaa 24,240,192\nTa 182,14,18432\nMaaa 24,244,196\nMaaa 56,244,204\nTa 184,15,0\nMaaa 56,240,200\nTa 186,15,2048\nMaaa 88,240,208\n"
"Maaa 88,244,212\nTa 188,15,16384\nMaaa 120,244,220\nTa 190,15,18432\nMaaa 120,240,216\nD0 4,20,48,2048\nMaaa 124,248,216\nMaaa 124,252,220\nMaaa 92,252,212\nMaaa 92,248,208\n"
"Maaa 60,248,200\nMaaa 60,252,204\nD0 5,20,48,3072\nMaaa 28,252,196\nMaaa 28,248,192\nWL 0\nMvaa 64,160,128\nTa 224,16,0\nMvaa 64,164,132\nTa 226,16,2048\nMvaa 96,164,140\n"
"s_add_u32 m0, s47, -65536\ns_add_u32 s45, s45, 128\nMvaa 96,160,136\nTa 228,16,16384\nMvaa 128,160,144\nTa 230,16,18432\nMvaa 128,164,148\nD0 2,20,45,0\nMvaa 160,164,156\n"
"Ta 232,17,0\nMvaa 160,160,152\nTa 234,17,2048\nMvaa 164,168,152\nMvaa 164,172,156\nTa 236,17,16384\nMvaa 132,172,148\nTa 238,17,18432\nMvaa 132,168,144\nMvaa 100,168,136\n"
"Ta 240,18,0\nMvaa 100,172,140\nTa 242,18,2048\nMvaa 68,172,132\nD0 3,20,45,1024\nMvaa 68,168,128\nTa 244,18,16384\nMvaa 72,176,128\nTa 246,18,18432\nMvaa 72,180,132\n"
"Mvaa 104,180,140\nTa 248,19,0\nMvaa 104,176,136\nTa 250,19,2048\nMvaa 136,176,144\nMvaa 136,180,148\nTa 252,19,16384\nMvaa 168,180,156\nTa 254,19,18432\nMvaa 168,176,152\n"
"D0 4,20,45,2048\nMvaa 172,184,152\nMvaa 172,188,156\nMvaa 140,188,148\nMvaa 140,184,144\nMvaa 108,184,136\nMvaa 108,188,140\nD0 5,20,45,3072\nMvaa 76,188,132\nMvaa 76,184,128\n"
"s_waitcnt vmcnt(22) lgkmcnt(0)\ns_barrier\nMvaa 80,224,128\nRa 192,10,16384\nWV 16\nMvaa 80,228,132\ns_add_u32 m0, s47, 0\ns_add_u32 s44, s44, 0x40000\n"
"buffer_load_dwordx4 v[220:223], v21, s[40:43], s87 offen offset:3584\nMvaa 112,228,140\nRa 196,11,16384\nv_lshlrev_b32 v244, 16, v240\nMvaa 112,224,136\n"
"v_and_b32 v245, 0xffff0000, v240\nv_lshlrev_b32 v246, 16, v241\nMvaa 144,224,144\nRa 200,10,18432\nv_and_b32 v247, 0xffff0000, v241\nMvaa 144,228,148\nD0 6,24,44,0\n"
"v_lshlrev_b32 v248, 16, v242\nMvaa 176,228,156\nRa 204,11,18432\nMvaa 176,224,152\nv_and_b32 v249, 0xffff0000, v242\nv_lshlrev_b32 v250, 16, v243\nMvaa 180,232,152\n"
"Ra 208,10,24576\nv_and_b32 v251, 0xffff0000, v243\nMvaa 180,236,156\nv_lshlrev_b32 v252, 16, v192\nv_and_b32 v253, 0xffff0000, v192\nMvaa 148,236,148\nRa 212,11,24576\n"
"MU 32,244,252\nMvaa 148,232,144\nMU 33,244,253\nMvaa 116,232,136\nRa 216,10,26624\nMU 40,248,252\nMvaa 116,236,140\nD0 7,24,44,1024\nMU 41,248,253\nMvaa 84,236,132\n"
"Ra 220,11,26624\nv_lshlrev_b32 v252, 16, v193\nMvaa 84,232,128\nv_and_b32 v253, 0xffff0000, v193\nMU 34,244,252\nMvaa 88,240,128\nMU 35,244,253\nMU 42,248,252\nMvaa 88,244,132\n"
"MU 43,248,253\nMvaa 120,244,140\nv_lshlrev_b32 v252, 16, v194\nv_and_b32 v253, 0xffff0000, v194\nMvaa 120,240,136\nMU 36,244,252\nMU 37,244,253\nMvaa 152,240,144\nMU 44,248,252\n"
"MU 45,248,253\nMvaa 152,244,148\nD0 8,24,44,2048\nv_lshlrev_b32 v252, 16, v195\nMvaa 184,244,156\nv_and_b32 v253, 0xffff0000, v195\nMvaa 184,240,152\nMU 38,244,252\nMU 39,244,253\n"
"Mvaa 188,248,152\nMU 46,248,252\nMU 47,248,253\nMvaa 188,252,156\nv_lshlrev_b32 v252, 16, v208\nv_and_b32 v253, 0xffff0000, v208\nMvaa 156,252,148\nMU 48,244,252\nMU 49,244,253\n"
"Mvaa 156,248,144\nMU 56,248,252\nMvaa 124,248,136\nMU 57,248,253\nv_lshlrev_b32 v252, 16, v209\nMvaa 124,252,140\nD0 9,24,44,3072\nv_and_b32 v253, 0xffff0000, v209\n"
"Mvaa 92,252,132\nMU 50,244,252\nMU 51,244,253\nMvaa 92,248,128\nWL 0\nMaaa 16,224,192\nRa 128,10,32768\nMU 58,248,252\nMaaa 16,228,196\ns_add_u32 m0, s47, 16384\n"
"s_add_u32 s44, s44, 0x40000\nMaaa 48,228,204\nRa 132,11,32768\nMU 59,248,253\nMaaa 48,224,200\nv_lshlrev_b32 v252, 16, v210\nv_and_b32 v253, 0xffff0000, v210\nMaaa 80,224,208\n"
"Ra 136,10,34816\nMU 52,244,252\nMaaa 80,228,212\nD0 6,24,44,0\nMU 53,244,253\nMaaa 112,228,220\nRa 140,11,34816\nMaaa 112,224,216\nMU 60,248,252\nMU 61,248,253\nMaaa 116,232,216\n"
"Ra 144,10,40960\nv_lshlrev_b32 v252, 16, v211\nMaaa 116,236,220\nv_and_b32 v253, 0xffff0000, v211\nMU 54,244,252\nMaaa 84,236,212\nRa 148,11,40960\nMU 55,244,253\nMaaa 84,232,208\n"
"MU 62,248,252\nMaaa 52,232,200\nRa 152,10,43008\nMU 63,248,253\nMaaa 52,236,204\nD0 7,24,44,1024\nv_lshlrev_b32 v252, 16, v196\nMaaa 20,236,196\nRa 156,11,43008\n"
"v_and_b32 v253, 0xffff0000, v196\nMaaa 20,232,192\nFM 32,245,252\nFM 33,245,253\nMaaa 24,240,192\nFM 40,249,252\nFM 41,249,253\nMaaa 24,244,196\nv_lshlrev_b32 v252, 16, v197\n"
"Maaa 56,244,204\nv_and_b32 v253, 0xffff0000, v197\nFM 34,245,252\nMaaa 56,240,200\nFM 35,245,253\nFM 42,249,252\nMaaa 88,240,208\nFM 43,249,253\nv_lshlrev_b32 v252, 16, v198\n"
"Maaa 88,244,212\nD0 8,24,44,2048\nv_and_b32 v253, 0xffff0000, v198\nMaaa 120,244,220\nFM 36,245,252\nMaaa 120,240,216\nFM 37,245,253\nFM 44,249,252\nMaaa 124,248,216\n"
"FM 45,249,253\nv_lshlrev_b32 v252, 16, v199\nMaaa 124,252,220\nv_and_b32 v253, 0xffff0000, v199\nFM 38,245,252\nMaaa 92,252,212\nFM 39,245,253\nFM 46,249,252\nMaaa 92,248,208\n"
"FM 47,249,253\nMaaa 60,248,200\nv_lshlrev_b32 v252, 16, v212\nv_and_b32 v253, 0xffff0000, v212\nMaaa 60,252,204\nD0 9,24,44,3072\nFM 48,245,252\nMaaa 28,252,196\nFM 49,245,253\n"
"FM 56,249,252\nMaaa 28,248,192\ns_waitcnt vmcnt(19) lgkmcnt(0)\ns_barrier\nMaaa 0,160,192\nTa 224,16,32768\nFM 57,249,253\nMaaa 0,164,196\nTa 226,16,34816\n"
"buffer_load_dwordx4 v[224:227], v20, s[92:95], s88 offen offset:0\nMaaa 32,164,204\ns_add_u32 m0, s47, -49152\ns_add_u32 s48, s48, 128\nMaaa 32,160,200\nTa 228,16,49152\n"
"v_lshlrev_b32 v252, 16, v213\nMaaa 64,160,208\nTa 230,16,51200\nv_and_b32 v253, 0xffff0000, v213\nMaaa 64,164,212\nD0 2,20,48,0\nFM 50,245,252\nMaaa 96,164,220\nTa 232,17,32768\n"
"Maaa 96,160,216\nTa 234,17,34816\nFM 51,245,253\nMaaa 100,168,216\nFM 58,249,252\nFM 59,249,253\nMaaa 100,172,220\nTa 236,17,49152\nv_lshlrev_b32 v252, 16, v214\nMaaa 68,172,212\n"
"Ta 238,17,51200\nv_and_b32 v253, 0xffff0000, v214\nMaaa 68,168,208\nFM 52,245,252\nMaaa 36,168,200\nTa 240,18,32768\nFM 53,245,253\nMaaa 36,172,204\nTa 242,18,34816\n"
"FM 60,249,252\nMaaa 4,172,196\nD0 3,20,48,1024\nFM 61,249,253\nMaaa 4,168,192\nTa 244,18,49152\nv_lshlrev_b32 v252, 16, v215\nMaaa 8,176,192\nTa 246,18,51200\n"
"v_and_b32 v253, 0xffff0000, v215\nMaaa 8,180,196\nFM 54,245,252\nMaaa 40,180,204\nTa 248,19,32768\nFM 55,245,253\nMaaa 40,176,200\nTa 250,19,34816\nFM 62,249,252\nMaaa 72,176,208\n"
"FM 63,249,253\nv_lshlrev_b32 v252, 16, v200\nMaaa 72,180,212\nTa 252,19,49152\nv_and_b32 v253, 0xffff0000, v200\nMaaa 104,180,220\nTa 254,19,51200\nMaaa 104,176,216\n"
"D0 4,20,48,2048\nFM 32,246,252\nMaaa 108,184,216\nFM 33,246,253\nFM 40,250,252\nMaaa 108,188,220\nFM 41,250,253\nv_lshlrev_b32 v252, 16, v201\nMaaa 76,188,212\n"
"v_and_b32 v253, 0xffff0000, v201\nFM 34,246,252\nMaaa 76,184,208\nFM 35,246,253\nMaaa 44,184,200\nFM 42,250,252\nFM 43,250,253\nMaaa 44,188,204\nD0 5,20,48,3072\n"
"v_lshlrev_b32 v252, 16, v202\nMaaa 12,188,196\nv_and_b32 v253, 0xffff0000, v202\nFM 36,246,252\nMaaa 12,184,192\nWL 0\nMvaa 80,224,128\nTa 160,12,32768\nFM 37,246,253\n"
"Mvaa 80,228,132\nTa 162,12,34816\nbuffer_load_dwordx4 v[228:231], v20, s[92:95], s88 offen offset:896\nMvaa 112,228,140\ns_add_u32 m0, s47, -32768\ns_add_u32 s45, s45, 128\n"
"Mvaa 112,224,136\nTa 164,12,49152\nFM 44,250,252\nMvaa 144,224,144\nTa 166,12,51200\nFM 45,250,253\nMvaa 144,228,148\nD0 2,20,45,0\nv_lshlrev_b32 v252, 16, v203\nMvaa 176,228,156\n"
"Ta 168,13,32768\nMvaa 176,224,152\nTa 170,13,34816\nv_and_b32 v253, 0xffff0000, v203\nMvaa 180,232,152\nFM 38,246,252\nFM 39,246,253\nMvaa 180,236,156\nTa 172,13,49152\n"
"FM 46,250,252\nMvaa 148,236,148\nTa 174,13,51200\nFM 47,250,253\nMvaa 148,232,144\nv_lshlrev_b32 v252, 16, v216\nMvaa 116,232,136\nTa 176,14,32768\n"
"v_and_b32 v253, 0xffff0000, v216\nMvaa 116,236,140\nTa 178,14,34816\nFM 48,246,252\nMvaa 84,236,132\nD0 3,20,45,1024\nFM 49,246,253\nMvaa 84,232,128\nTa 180,14,49152\n"
"FM 56,250,252\nMvaa 88,240,128\nTa 182,14,51200\nFM 57,250,253\nMvaa 88,244,132\nv_lshlrev_b32 v252, 16, v217\nMvaa 120,244,140\nTa 184,15,32768\nv_and_b32 v253, 0xffff0000, v217\n"
"Mvaa 120,240,136\nTa 186,15,34816\nFM 50,246,252\nMvaa 152,240,144\nFM 51,246,253\nFM 58,250,252\nMvaa 152,244,148\nTa 188,15,49152\nFM 59,250,253\nMvaa 184,244,156\n"
"Ta 190,15,51200\nMvaa 184,240,152\nD0 4,20,45,2048\nv_lshlrev_b32 v252, 16, v218\nMvaa 188,248,152\nv_and_b32 v253, 0xffff0000, v218\nFM 52,246,252\nMvaa 188,252,156\n"
"FM 53,246,253\nFM 60,250,252\nMvaa 156,252,148\nFM 61,250,253\nv_lshlrev_b32 v252, 16, v219\nMvaa 156,248,144\nv_and_b32 v253, 0xffff0000, v219\nMvaa 124,248,136\nFM 54,246,252\n"
"WV 26\nv_xor_b32 v27, s15, v27\nv_and_b32 v255, 0xffffdfff, v27\nv_cmp_ne_u32 vcc, 0, v255\ns_nop 0\ns_cbranch_vccnz .S17\n.S18:\nv_cmp_ne_u32 vcc, 0, v27\ns_nop 3\n"
"s_bcnt1_i32_b64 s4, vcc\ns_cmp_gt_u32 s4, 32\ns_cselect_b32 s4, 1, 0\nMvaa 124,252,140\nD0 5,20,45,3072\ns_xor_b32 s4, s4, s12\nMvaa 92,252,132\ns_and_b32 s4, s4, 1\n"
"s_lshl_b32 s4, s4, 13\nMvaa 92,248,128\ns_waitcnt vmcnt(18) lgkmcnt(0)\ns_barrier\nMvaa 64,160,128\nRa 192,10,49152\ns_mul_i32 s72, s73, 25165824\nMvaa 64,164,132\n"
"s_add_u32 m0, s47, 32768\ns_add_u32 s44, s44, 0x40000\nbuffer_load_dwordx4 v[232:235], v20, s[92:95], s88 offen offset:1792\nMvaa 96,164,140\nRa 196,11,49152\n"
"s_add_u32 s72, s72, s4\nMvaa 96,160,136\ns_and_b32 s4, s12, 1\ns_lshl_b32 s4, s4, 21\nMvaa 128,160,144\nRa 200,10,51200\ns_add_u32 s72, s72, s4\nMvaa 128,164,148\nD0 6,24,44,0\n"
"s_lshl3_add_u32 s2, s13, s11\nMvaa 160,164,156\nRa 204,11,51200\nMvaa 160,160,152\ns_lshl_b32 s2, s2, 11\ns_lshl_b32 s3, s14, 22\nMvaa 164,168,152\nRa 208,10,57344\n"
"s_add_u32 s2, s2, s3\nMvaa 164,172,156\ns_add_u32 s2, s2, s72\ns_add_u32 s49, s2, 0xc00000\nMvaa 132,172,148\nRa 212,11,57344\nFM 55,246,253\nMvaa 132,168,144\nFM 62,250,252\n"
"Mvaa 100,168,136\nRa 216,10,59392\nFM 63,250,253\nMvaa 100,172,140\nD0 7,24,44,1024\nv_lshlrev_b32 v252, 16, v204\nMvaa 68,172,132\nRa 220,11,59392\n"
"v_and_b32 v253, 0xffff0000, v204\nMvaa 68,168,128\nFM 32,247,252\nFM 33,247,253\nMvaa 72,176,128\nFM 40,251,252\nFM 41,251,253\nMvaa 72,180,132\nv_lshlrev_b32 v252, 16, v205\n"
"Mvaa 104,180,140\nv_and_b32 v253, 0xffff0000, v205\nFM 34,247,252\nMvaa 104,176,136\nFM 35,247,253\nFM 42,251,252\nMvaa 136,176,144\nFM 43,251,253\nv_lshlrev_b32 v252, 16, v206\n"
"Mvaa 136,180,148\nD0 8,24,44,2048\nv_and_b32 v253, 0xffff0000, v206\nMvaa 168,180,156\nFM 36,247,252\nMvaa 168,176,152\nFM 37,247,253\nFM 44,251,252\nMvaa 172,184,152\n"
"FM 45,251,253\nv_lshlrev_b32 v252, 16, v207\nMvaa 172,188,156\nv_and_b32 v253, 0xffff0000, v207\nFM 38,247,252\nMvaa 140,188,148\nFM 39,247,253\nFM 46,251,252\nMvaa 140,184,144\n"
"FM 47,251,253\nMvaa 108,184,136\nv_lshlrev_b32 v252, 16, v220\nv_and_b32 v253, 0xffff0000, v220\nMvaa 108,188,140\nD0 9,24,44,3072\nFM 48,247,252\nMvaa 76,188,132\nFM 49,247,253\n"
"FM 56,251,252\nMvaa 76,184,128\nWL 0\nMaaa 0,160,192\nRa 128,10,0\nFM 57,251,253\nMaaa 0,164,196\ns_add_u32 m0, s47, 49152\ns_add_u32 s44, s44, 0x40000\n"
"buffer_load_dwordx4 v[236:239], v20, s[92:95], s88 offen offset:2688\nMaaa 32,164,204\nRa 132,11,0\nv_lshlrev_b32 v252, 16, v221\nMaaa 32,160,200\n"
"v_and_b32 v253, 0xffff0000, v221\nFM 50,247,252\nMaaa 64,160,208\nRa 136,10,2048\nFM 51,247,253\nMaaa 64,164,212\nD0 6,24,44,0\nFM 58,251,252\nMaaa 96,164,220\nRa 140,11,2048\n"
"Maaa 96,160,216\nFM 59,251,253\nv_lshlrev_b32 v252, 16, v222\nMaaa 100,168,216\nRa 144,10,8192\nv_and_b32 v253, 0xffff0000, v222\nMaaa 100,172,220\nFM 52,247,252\nFM 53,247,253\n"
"Maaa 68,172,212\nRa 148,11,8192\nFM 60,251,252\nMaaa 68,168,208\nFM 61,251,253\nMaaa 36,168,200\nRa 152,10,10240\nv_lshlrev_b32 v252, 16, v223\nMaaa 36,172,204\nD0 7,24,44,1024\n"
"v_and_b32 v253, 0xffff0000, v223\nMaaa 4,172,196\nRa 156,11,10240\nFM 54,247,252\nMaaa 4,168,192\nFM 55,247,253\nFM 62,251,252\nMaaa 8,176,192\nFM 63,251,253\nMaaa 8,180,196\n"
"v_permlane32_swap_b32 v32, v48\nMaaa 40,180,204\nv_permlane32_swap_b32 v33, v49\nv_permlane32_swap_b32 v34, v50\nMaaa 40,176,200\nv_permlane32_swap_b32 v35, v51\n"
"v_permlane32_swap_b32 v36, v52\nMaaa 72,176,208\nv_permlane32_swap_b32 v37, v53\nv_permlane32_swap_b32 v38, v54\nMaaa 72,180,212\nD0 8,24,44,2048\nv_permlane32_swap_b32 v39, v55\n"
"Maaa 104,180,220\nv_permlane32_swap_b32 v40, v56\nMaaa 104,176,216\nv_permlane32_swap_b32 v41, v57\nv_permlane32_swap_b32 v42, v58\nMaaa 108,184,216\n"
"v_permlane32_swap_b32 v43, v59\nv_permlane32_swap_b32 v44, v60\nMaaa 108,188,220\nv_permlane32_swap_b32 v45, v61\nv_permlane32_swap_b32 v46, v62\nMaaa 76,188,212\n"
"v_permlane32_swap_b32 v47, v63\nAD 32,32,48\nMaaa 76,184,208\nAD 33,33,49\nMaaa 44,184,200\nAD 34,34,50\nAD 35,35,51\nMaaa 44,188,204\nD0 9,24,44,3072\nAD 36,36,52\n"
"Maaa 12,188,196\nAD 37,37,53\nAD 38,38,54\nMaaa 12,184,192\ns_waitcnt vmcnt(20) lgkmcnt(0)\ns_barrier\nMaaa 16,224,192\nTa 160,12,0\nAD 39,39,55\nMaaa 16,228,196\nTa 162,12,2048\n"
"Maaa 48,228,204\ns_add_u32 m0, s47, -16384\ns_add_u32 s48, s48, 128\nMaaa 48,224,200\nTa 164,12,16384\nAD 40,40,56\nMaaa 80,224,208\nTa 166,12,18432\nAD 41,41,57\nMaaa 80,228,212\n"
"D0 2,20,48,0\nAD 42,42,58\nMaaa 112,228,220\nTa 168,13,0\nMaaa 112,224,216\nTa 170,13,2048\nAD 43,43,59\nMaaa 116,232,216\nAD 44,44,60\nAD 45,45,61\nMaaa 116,236,220\n"
"Ta 172,13,16384\nAD 46,46,62\nMaaa 84,236,212\nTa 174,13,18432\nAD 47,47,63\nMaaa 84,232,208\nv_permlane16_swap_b32 v32, v40\nMaaa 52,232,200\nTa 176,14,0\n"
"v_permlane16_swap_b32 v33, v41\nMaaa 52,236,204\nTa 178,14,2048\nv_permlane16_swap_b32 v34, v42\nMaaa 20,236,196\nD0 3,20,48,1024\nv_permlane16_swap_b32 v35, v43\nMaaa 20,232,192\n"
"Ta 180,14,16384\nv_permlane16_swap_b32 v36, v44\nMaaa 24,240,192\nTa 182,14,18432\nv_permlane16_swap_b32 v37, v45\nMaaa 24,244,196\nv_permlane16_swap_b32 v38, v46\n"
"Maaa 56,244,204\nTa 184,15,0\nv_permlane16_swap_b32 v39, v47\nMaaa 56,240,200\nTa 186,15,2048\nAD 32,32,40\nMaaa 88,240,208\nAD 33,33,41\nAD 34,34,42\nMaaa 88,244,212\n"
"Ta 188,15,16384\nAD 35,35,43\nMaaa 120,244,220\nTa 190,15,18432\nMaaa 120,240,216\nD0 4,20,48,2048\nAD 36,36,44\nMaaa 124,248,216\nAD 37,37,45\nAD 38,38,46\nMaaa 124,252,220\n"
"AD 39,39,47\nv_add_f32_dpp v32, v32, v32 row_ror:8 row_mask:0xf bank_mask:0xf\nMaaa 92,252,212\nv_add_f32_dpp v33, v33, v33 row_ror:8 row_mask:0xf bank_mask:0xf\n"
"v_add_f32_dpp v34, v34, v34 row_ror:8 row_mask:0xf bank_mask:0xf\nMaaa 92,248,208\nv_add_f32_dpp v35, v35, v35 row_ror:8 row_mask:0xf bank_mask:0xf\nMaaa 60,248,200\n"
"v_add_f32_dpp v36, v36, v36 row_ror:8 row_mask:0xf bank_mask:0xf\nv_add_f32_dpp v37, v37, v37 row_ror:8 row_mask:0xf bank_mask:0xf\nMaaa 60,252,204\nD0 5,20,48,3072\n"
"v_add_f32_dpp v38, v38, v38 row_ror:8 row_mask:0xf bank_mask:0xf\nMaaa 28,252,196\nv_add_f32_dpp v39, v39, v39 row_ror:8 row_mask:0xf bank_mask:0xf\n"
"v_add_f32_dpp v32, v32, v32 row_ror:4 row_mask:0xf bank_mask:0xf\nMaaa 28,248,192\nWL 0\nMvaa 64,160,128\nTa 224,16,0\n"
"v_add_f32_dpp v33, v33, v33 row_ror:4 row_mask:0xf bank_mask:0xf\nMvaa 64,164,132\nTa 226,16,2048\nMvaa 96,164,140\ns_add_u32 m0, s47, -65536\ns_add_u32 s45, s45, 128\n"
"Mvaa 96,160,136\nTa 228,16,16384\nv_add_f32_dpp v34, v34, v34 row_ror:4 row_mask:0xf bank_mask:0xf\nMvaa 128,160,144\nTa 230,16,18432\n"
"v_add_f32_dpp v35, v35, v35 row_ror:4 row_mask:0xf bank_mask:0xf\nMvaa 128,164,148\nD0 2,20,45,0\nv_add_f32_dpp v36, v36, v36 row_ror:4 row_mask:0xf bank_mask:0xf\n"
"Mvaa 160,164,156\nTa 232,17,0\nMvaa 160,160,152\nTa 234,17,2048\nv_add_f32_dpp v37, v37, v37 row_ror:4 row_mask:0xf bank_mask:0xf\nMvaa 164,168,152\n"
"v_add_f32_dpp v38, v38, v38 row_ror:4 row_mask:0xf bank_mask:0xf\nv_add_f32_dpp v39, v39, v39 row_ror:4 row_mask:0xf bank_mask:0xf\nMvaa 164,172,156\nTa 236,17,16384\n"
"v_add_f32_dpp v32, v32, v32 row_ror:2 row_mask:0xf bank_mask:0xf\nMvaa 132,172,148\nTa 238,17,18432\nv_add_f32_dpp v33, v33, v33 row_ror:2 row_mask:0xf bank_mask:0xf\n"
"Mvaa 132,168,144\nv_add_f32_dpp v34, v34, v34 row_ror:2 row_mask:0xf bank_mask:0xf\nMvaa 100,168,136\nTa 240,18,0\n"
"v_add_f32_dpp v35, v35, v35 row_ror:2 row_mask:0xf bank_mask:0xf\nMvaa 100,172,140\nTa 242,18,2048\nv_add_f32_dpp v36, v36, v36 row_ror:2 row_mask:0xf bank_mask:0xf\n"
"Mvaa 68,172,132\nD0 3,20,45,1024\nv_add_f32_dpp v37, v37, v37 row_ror:2 row_mask:0xf bank_mask:0xf\nMvaa 68,168,128\nTa 244,18,16384\n"
"v_add_f32_dpp v38, v38, v38 row_ror:2 row_mask:0xf bank_mask:0xf\nMvaa 72,176,128\nTa 246,18,18432\nv_add_f32_dpp v39, v39, v39 row_ror:2 row_mask:0xf bank_mask:0xf\n"
"Mvaa 72,180,132\nv_lshrrev_b32 v48, 4, v0\nMvaa 104,180,140\nTa 248,19,0\nv_and_b32 v49, 1, v0\nMvaa 104,176,136\nTa 250,19,2048\nv_lshlrev_b32 v49, 5, v49\nMvaa 136,176,144\n"
"v_lshl_add_u32 v49, v48, 6, v49\ns_lshl_b32 s2, s11, 8\nMvaa 136,180,148\nTa 252,19,16384\ns_add_u32 s2, s2, 132096\nMvaa 168,180,156\nTa 254,19,18432\nMvaa 168,176,152\n"
"D0 4,20,45,2048\nv_add_u32 v49, s2, v49\nMvaa 172,184,152\nds_write_b128 v49, v[32:35]\nds_write_b128 v49, v[36:39] offset:16\nMvaa 172,188,156\nv_and_b32 v50, 31, v0\n"
"v_lshlrev_b32 v50, 2, v50\nMvaa 140,188,148\ns_lshl_b32 s2, s11, 10\ns_add_u32 s2, s2, 133120\nMvaa 140,184,144\nv_add_u32 v51, s2, v50\nMvaa 108,184,136\n"
"v_add_u32 v50, 132096, v50\nv_lshl_add_u32 v52, v48, 4, s2\nMvaa 108,188,140\nD0 5,20,45,3072\nMvaa 76,188,132\nMvaa 76,184,128\ns_waitcnt vmcnt(18) lgkmcnt(0)\ns_barrier\n"
"Mvaa 80,224,128\nRa 192,10,16384\nds_read_b32 v32, v50 offset:0\nMvaa 80,228,132\ns_add_u32 m0, s47, 0\ns_add_u32 s44, s44, 0x40000\nMvaa 112,228,140\nRa 196,11,16384\n"
"Mvaa 112,224,136\nds_read_b32 v33, v50 offset:256\nds_read_b32 v34, v50 offset:512\nMvaa 144,224,144\nRa 200,10,18432\nMvaa 144,228,148\nD0 6,24,44,0\nMvaa 176,228,156\n"
"Ra 204,11,18432\nds_read_b32 v35, v50 offset:768\nMvaa 176,224,152\nds_read_b32 v36, v50 offset:128\nMvaa 180,232,152\nRa 208,10,24576\nds_read_b32 v37, v50 offset:384\n"
"Mvaa 180,236,156\nds_read_b32 v38, v50 offset:640\nMvaa 148,236,148\nRa 212,11,24576\nMvaa 148,232,144\nds_read_b32 v39, v50 offset:896\nMvaa 116,232,136\nRa 216,10,26624\n"
"Mvaa 116,236,140\nD0 7,24,44,1024\nMvaa 84,236,132\nRa 220,11,26624\nMvaa 84,232,128\nMvaa 88,240,128\nMvaa 88,244,132\nMvaa 120,244,140\nMvaa 120,240,136\nMvaa 152,240,144\n"
"Mvaa 152,244,148\nD0 8,24,44,2048\nMvaa 184,244,156\nWL 2\nAD 32,32,33\nMvaa 184,240,152\nAD 34,34,35\nMvaa 188,248,152\nAD 36,36,37\nAD 38,38,39\nMvaa 188,252,156\nAD 32,32,34\n"
"Mvaa 156,252,148\nAD 36,36,38\nMvaa 156,248,144\nCV 32,32,36\nv_and_b32 v36, 0xffff0000, v32\nMvaa 124,248,136\nv_lshlrev_b32 v32, 16, v32\nMvaa 124,252,140\nD0 9,24,44,3072\n"
"Mvaa 92,252,132\nv_mul_f32 v33, 0xbfb8aa3b, v32\nMvaa 92,248,128\nWL 0\nMaaa 16,224,192\nRa 128,10,32768\nMaaa 16,228,196\ns_add_u32 m0, s47, 16384\ns_add_u32 s44, s44, 0x40000\n"
"Maaa 48,228,204\nRa 132,11,32768\nEX 33,33\nMaaa 48,224,200\nv_add_f32 v33, 1.0, v33\nMaaa 80,224,208\nRa 136,10,34816\nv_rcp_f32 v33, v33\nMaaa 80,228,212\nD0 6,24,44,0\n"
"Maaa 112,228,220\nRa 140,11,34816\nMU 33,32,33\nMaaa 112,224,216\nCV 33,33,33\nMaaa 116,232,216\nRa 144,10,40960\nv_lshlrev_b32 v33, 16, v33\nMaaa 116,236,220\nMU 33,33,36\n"
"Maaa 84,236,212\nRa 148,11,40960\nMaaa 84,232,208\nCV 33,33,33\nv_lshlrev_b32 v33, 16, v33\nMaaa 52,232,200\nRa 152,10,43008\nMaaa 52,236,204\nD0 7,24,44,1024\nMaaa 20,236,196\n"
"Ra 156,11,43008\nds_write_b32 v51, v33\nMaaa 20,232,192\nRv 244,52,0\nMaaa 24,240,192\nRv 248,52,64\ns_and_b32 s2, s74, 15\nMaaa 24,244,196\ns_sub_u32 s2, s2, 2\n"
"s_cmp_lt_u32 s2, 14\ns_cselect_b32 s5, 0, 0x40000000\nMaaa 56,244,204\ns_and_b32 s2, s2, 15\nMaaa 56,240,200\ns_mul_i32 s4, s73, 14\ns_add_u32 s4, s4, s2\nMaaa 88,240,208\n"
"v_and_b32 v49, 15, v0\nMaaa 88,244,212\nD0 8,24,44,2048\nMaaa 120,244,220\nv_lshlrev_b32 v50, 3, v49\nv_and_b32 v51, 1, v48\nMaaa 120,240,216\nv_lshl_add_u32 v50, v51, 2, v50\n"
"Maaa 124,248,216\ns_mul_i32 s2, s11, 112\nv_add_u32 v50, s2, v50\nMaaa 124,252,220\nv_lshrrev_b32 v51, 4, v50\nMaaa 92,252,212\nv_lshrrev_b32 v48, 1, v48\nMaaa 92,248,208\n"
"v_mad_u32_u24 v51, v48, 28, v51\nv_mul_u32_u24 v51, 7168, v51\nMaaa 60,248,200\nv_and_b32 v50, 15, v50\nMaaa 60,252,204\nD0 9,24,44,3072\nMaaa 28,252,196\n"
"v_lshl_add_u32 v51, v50, 2, v51\nv_cmp_gt_u32 vcc, 14, v49\ns_lshl_b32 s4, s4, 6\ns_add_u32 s4, s4, 0xc04c800\nv_cndmask_b32 v51, v254, v51, vcc\nMaaa 28,248,192\n"
"s_waitcnt vmcnt(16) lgkmcnt(3)\ns_barrier\nMaaa 0,160,192\nTa 224,16,32768\ns_or_b32 s4, s4, s5\nMaaa 0,164,196\nTa 226,16,34816\nMaaa 32,164,204\ns_add_u32 m0, s47, -49152\n"
"s_add_u32 s48, s48, 128\nMaaa 32,160,200\nTa 228,16,49152\nWL 3\nMaaa 64,160,208\nTa 230,16,51200\nMaaa 64,164,212\nD0 2,20,48,0\nMaaa 96,164,220\nTa 232,17,32768\n"
"v_lshlrev_b32 v252, 16, v224\nMaaa 96,160,216\nTa 234,17,34816\nMaaa 100,168,216\nv_and_b32 v253, 0xffff0000, v224\nMU 32,244,252\nMaaa 100,172,220\nTa 236,17,49152\n"
"Maaa 68,172,212\nTa 238,17,51200\nMaaa 68,168,208\nMU 33,244,253\nMU 40,248,252\nMaaa 36,168,200\nTa 240,18,32768\nMaaa 36,172,204\nTa 242,18,34816\nMaaa 4,172,196\n"
"D0 3,20,48,1024\nMU 41,248,253\nMaaa 4,168,192\nTa 244,18,49152\nMaaa 8,176,192\nTa 246,18,51200\nv_lshlrev_b32 v252, 16, v225\nMaaa 8,180,196\nv_and_b32 v253, 0xffff0000, v225\n"
"Maaa 40,180,204\nTa 248,19,32768\nMaaa 40,176,200\nTa 250,19,34816\nMU 34,244,252\nMaaa 72,176,208\nMU 35,244,253\nMaaa 72,180,212\nTa 252,19,49152\nMaaa 104,180,220\n"
"Ta 254,19,51200\nMU 42,248,252\nMaaa 104,176,216\nD0 4,20,48,2048\nMaaa 108,184,216\nMU 43,248,253\nv_lshlrev_b32 v252, 16, v226\nMaaa 108,188,220\n"
"v_and_b32 v253, 0xffff0000, v226\nMaaa 76,188,212\nMU 36,244,252\nMaaa 76,184,208\nMU 37,244,253\nMU 44,248,252\nMaaa 44,184,200\nMU 45,248,253\nMaaa 44,188,204\nD0 5,20,48,3072\n"
"Maaa 12,188,196\nv_lshlrev_b32 v252, 16, v227\nv_and_b32 v253, 0xffff0000, v227\nMaaa 12,184,192\nWL 0\nMvaa 80,224,128\nTa 160,12,32768\nMU 38,244,252\nMvaa 80,228,132\n"
"Ta 162,12,34816\nMvaa 112,228,140\ns_add_u32 m0, s47, -32768\ns_add_u32 s45, s45, 128\nMvaa 112,224,136\nTa 164,12,49152\nMU 39,244,253\nMvaa 144,224,144\nTa 166,12,51200\n"
"Mvaa 144,228,148\nD0 2,20,45,0\nMvaa 176,228,156\nTa 168,13,32768\nMU 46,248,252\nMvaa 176,224,152\nTa 170,13,34816\nMvaa 180,232,152\nMU 47,248,253\nv_lshlrev_b32 v252, 16, v228\n"
"Mvaa 180,236,156\nTa 172,13,49152\nMvaa 148,236,148\nTa 174,13,51200\nMvaa 148,232,144\nv_and_b32 v253, 0xffff0000, v228\nFM 32,245,252\nMvaa 116,232,136\nTa 176,14,32768\n"
"Mvaa 116,236,140\nTa 178,14,34816\nMvaa 84,236,132\nD0 3,20,45,1024\nFM 33,245,253\nMvaa 84,232,128\nTa 180,14,49152\nMvaa 88,240,128\nTa 182,14,51200\nFM 40,249,252\n"
"Mvaa 88,244,132\nFM 41,249,253\nMvaa 120,244,140\nTa 184,15,32768\nMvaa 120,240,136\nTa 186,15,34816\nv_lshlrev_b32 v252, 16, v229\nMvaa 152,240,144\n"
"v_and_b32 v253, 0xffff0000, v229\nMvaa 152,244,148\nTa 188,15,49152\nMvaa 184,244,156\nTa 190,15,51200\nFM 34,245,252\nMvaa 184,240,152\nD0 4,20,45,2048\nMvaa 188,248,152\n"
"FM 35,245,253\nFM 42,249,252\nMvaa 188,252,156\nFM 43,249,253\nMvaa 156,252,148\nv_lshlrev_b32 v252, 16, v230\nMvaa 156,248,144\nv_and_b32 v253, 0xffff0000, v230\nFM 36,245,252\n"
"Mvaa 124,248,136\nFM 37,245,253\nMvaa 124,252,140\nD0 5,20,45,3072\nMvaa 92,252,132\nFM 44,249,252\nFM 45,249,253\nMvaa 92,248,128\ns_waitcnt vmcnt(16) lgkmcnt(0)\ns_barrier\n"
"Mvaa 64,160,128\nRa 192,10,49152\nv_lshlrev_b32 v252, 16, v231\nMvaa 64,164,132\ns_add_u32 m0, s47, 32768\ns_add_u32 s44, s44, 0x40000\nMvaa 96,164,140\nRa 196,11,49152\n"
"Mvaa 96,160,136\nv_and_b32 v253, 0xffff0000, v231\nFM 38,245,252\nMvaa 128,160,144\nRa 200,10,51200\nMvaa 128,164,148\nD0 6,24,44,0\nMvaa 160,164,156\nRa 204,11,51200\n"
"FM 39,245,253\nMvaa 160,160,152\nFM 46,249,252\nMvaa 164,168,152\nRa 208,10,57344\nFM 47,249,253\nMvaa 164,172,156\nv_lshlrev_b32 v252, 16, v232\nMvaa 132,172,148\n"
"Ra 212,11,57344\nMvaa 132,168,144\nv_and_b32 v253, 0xffff0000, v232\nFM 32,246,252\nMvaa 100,168,136\nRa 216,10,59392\nMvaa 100,172,140\nD0 7,24,44,1024\nMvaa 68,172,132\n"
"Ra 220,11,59392\nFM 33,246,253\nMvaa 68,168,128\nFM 40,250,252\nMvaa 72,176,128\nFM 41,250,253\nv_lshlrev_b32 v252, 16, v233\nMvaa 72,180,132\nv_and_b32 v253, 0xffff0000, v233\n"
"Mvaa 104,180,140\nFM 34,246,252\nMvaa 104,176,136\nFM 35,246,253\nFM 42,250,252\nMvaa 136,176,144\nFM 43,250,253\nMvaa 136,180,148\nD0 8,24,44,2048\nMvaa 168,180,156\n"
"v_lshlrev_b32 v252, 16, v234\nv_and_b32 v253, 0xffff0000, v234\nMvaa 168,176,152\nFM 36,246,252\nMvaa 172,184,152\nFM 37,246,253\nFM 44,250,252\nMvaa 172,188,156\nFM 45,250,253\n"
"Mvaa 140,188,148\nv_lshlrev_b32 v252, 16, v235\nMvaa 140,184,144\nv_and_b32 v253, 0xffff0000, v235\nFM 38,246,252\nMvaa 108,184,136\nFM 39,246,253\nMvaa 108,188,140\n"
"D0 9,24,44,3072\nMvaa 76,188,132\nFM 46,250,252\nFM 47,250,253\nMvaa 76,184,128\nWL 0\nMaaa 0,160,192\nRa 128,10,0\nv_lshlrev_b32 v252, 16, v236\nMaaa 0,164,196\n"
"s_add_u32 m0, s47, 49152\ns_add_u32 s44, s44, 0x40000\nMaaa 32,164,204\nRa 132,11,0\nMaaa 32,160,200\nv_and_b32 v253, 0xffff0000, v236\nFM 32,247,252\nMaaa 64,160,208\n"
"Ra 136,10,2048\nMaaa 64,164,212\nD0 6,24,44,0\nMaaa 96,164,220\nRa 140,11,2048\nFM 33,247,253\nMaaa 96,160,216\nFM 40,251,252\nMaaa 100,168,216\nRa 144,10,8192\nFM 41,251,253\n"
"Maaa 100,172,220\nv_lshlrev_b32 v252, 16, v237\nMaaa 68,172,212\nRa 148,11,8192\nMaaa 68,168,208\nv_and_b32 v253, 0xffff0000, v237\nFM 34,247,252\nMaaa 36,168,200\n"
"Ra 152,10,10240\nMaaa 36,172,204\nD0 7,24,44,1024\nMaaa 4,172,196\nRa 156,11,10240\nFM 35,247,253\nMaaa 4,168,192\nFM 42,251,252\nMaaa 8,176,192\nFM 43,251,253\n"
"v_lshlrev_b32 v252, 16, v238\nMaaa 8,180,196\nv_and_b32 v253, 0xffff0000, v238\nMaaa 40,180,204\nFM 36,247,252\nMaaa 40,176,200\nFM 37,247,253\nFM 44,251,252\nMaaa 72,176,208\n"
"FM 45,251,253\nMaaa 72,180,212\nD0 8,24,44,2048\nMaaa 104,180,220\nv_lshlrev_b32 v252, 16, v239\nv_and_b32 v253, 0xffff0000, v239\nMaaa 104,176,216\nFM 38,247,252\n"
"Maaa 108,184,216\nFM 39,247,253\nFM 46,251,252\nMaaa 108,188,220\nFM 47,251,253\nMaaa 76,188,212\nv_permlane32_swap_b32 v32, v40\nMaaa 76,184,208\nv_permlane32_swap_b32 v33, v41\n"
"v_permlane32_swap_b32 v34, v42\nMaaa 44,184,200\nv_permlane32_swap_b32 v35, v43\nMaaa 44,188,204\nD0 9,24,44,3072\nMaaa 12,188,196\nv_permlane32_swap_b32 v36, v44\n"
"v_permlane32_swap_b32 v37, v45\nMaaa 12,184,192\nWV 16\ns_barrier\nMaaa 16,224,192\nTa 160,12,0\nv_permlane32_swap_b32 v38, v46\nMaaa 16,228,196\nTa 162,12,2048\nMaaa 48,228,204\n"
"s_add_u32 m0, s47, -16384\ns_add_u32 s48, s48, 128\nMaaa 48,224,200\nTa 164,12,16384\nv_permlane32_swap_b32 v39, v47\nMaaa 80,224,208\nTa 166,12,18432\nMaaa 80,228,212\n"
"D0 2,20,48,0\nMaaa 112,228,220\nTa 168,13,0\nAD 32,32,40\nMaaa 112,224,216\nTa 170,13,2048\nMaaa 116,232,216\nAD 33,33,41\nAD 34,34,42\nMaaa 116,236,220\nTa 172,13,16384\n"
"Maaa 84,236,212\nTa 174,13,18432\nMaaa 84,232,208\nAD 35,35,43\nAD 36,36,44\nMaaa 52,232,200\nTa 176,14,0\nMaaa 52,236,204\nTa 178,14,2048\nMaaa 20,236,196\nD0 3,20,48,1024\n"
"AD 37,37,45\nMaaa 20,232,192\nTa 180,14,16384\nMaaa 24,240,192\nTa 182,14,18432\nAD 38,38,46\nMaaa 24,244,196\nAD 39,39,47\nMaaa 56,244,204\nTa 184,15,0\nMaaa 56,240,200\n"
"Ta 186,15,2048\nv_permlane16_swap_b32 v32, v36\nMaaa 88,240,208\nv_permlane16_swap_b32 v33, v37\nMaaa 88,244,212\nTa 188,15,16384\nMaaa 120,244,220\nTa 190,15,18432\n"
"v_permlane16_swap_b32 v34, v38\nMaaa 120,240,216\nD0 4,20,48,2048\nMaaa 124,248,216\nv_permlane16_swap_b32 v35, v39\nAD 32,32,36\nMaaa 124,252,220\nAD 33,33,37\nMaaa 92,252,212\n"
"AD 34,34,38\nMaaa 92,248,208\nAD 35,35,39\nSv 32,51,16,4,0\nMaaa 60,248,200\nMaaa 60,252,204\nD0 5,20,48,3072\nMaaa 28,252,196\nMaaa 28,248,192\nWL 0\nMvaa 64,160,128\n"
"Ta 224,16,0\ns_lshr_b32 s3, s11, 1\nMvaa 64,164,132\nTa 226,16,2048\ns_add_u32 s6, s56, 16384\nMvaa 96,164,140\ns_lshl_b32 s3, s3, 13\nMvaa 96,160,136\nTa 228,16,16384\n"
"buffer_load_dwordx4 v[224:227], v1, s[16:19], s6 offen offset:0 sc1\nMvaa 128,160,144\nTa 230,16,18432\nMvaa 128,164,148\ns_lshl_b32 s2, s11, 12\nMvaa 160,164,156\nTa 232,17,0\n"
"Mvaa 160,160,152\nTa 234,17,2048\nMvaa 164,168,152\ns_add_u32 s2, s2, s3\ns_add_u32 s2, s2, s72\nMvaa 164,172,156\nTa 236,17,16384\n"
"buffer_load_dwordx4 v[228:231], v1, s[16:19], s6 offen offset:1024 sc1\nMvaa 132,172,148\nTa 238,17,18432\nMvaa 132,168,144\ns_lshl_b32 s3, s11, 12\nMvaa 100,168,136\nTa 240,18,0\n"
"Mvaa 100,172,140\nTa 242,18,2048\nMvaa 68,172,132\ns_add_u32 s89, s3, 0x0\nMvaa 68,168,128\nTa 244,18,16384\n"
"buffer_load_dwordx4 v[232:235], v1, s[16:19], s6 offen offset:2048 sc1\nMvaa 72,176,128\nTa 246,18,18432\ns_mov_b32 s88, 0xffff8000\nMvaa 72,180,132\ns_lshl_b32 s4, s13, 1\n"
"Mvaa 104,180,140\nTa 248,19,0\nMvaa 104,176,136\nTa 250,19,2048\nMvaa 136,176,144\ns_sub_u32 s79, 15, s4\nMvaa 136,180,148\nTa 252,19,16384\n"
"buffer_load_dwordx4 v[236:239], v1, s[16:19], s6 offen offset:3072 sc1\nMvaa 168,180,156\nTa 254,19,18432\nMvaa 168,176,152\ns_add_u32 s96, s2, 0x1310000\nMvaa 172,184,152\n"
"s_mov_b32 s63, 0x53800000\ns_mov_b32 s64, 0x2b800000\nMvaa 172,188,156\ns_sub_u32 s98, s4, 1\ns_add_u32 s6, s56, 20480\nMvaa 140,188,148\ns_lshl_b32 s5, s13, 5\nMvaa 140,184,144\n"
"s_add_u32 s5, s5, s11\nbuffer_load_dwordx4 v[240:243], v1, s[16:19], s6 offen offset:0 sc1\nMvaa 108,184,136\ns_lshl_b32 s65, s5, 9\nMvaa 108,188,140\ns_add_u32 s65, s65, s72\n"
"Mvaa 76,188,132\ns_add_u32 s65, s65, 0x1580000\nMvaa 76,184,128\ns_waitcnt vmcnt(18) lgkmcnt(0)\ns_barrier\nMvaa 80,224,128\nRa 192,10,16384\ns_and_b32 s66, s72, 0x2000\n"
"Mvaa 80,228,132\ns_add_u32 s66, s66, s56\nbuffer_load_dwordx4 v[244:247], v1, s[16:19], s6 offen offset:1024 sc1\nMvaa 112,228,140\nRa 196,11,16384\nMvaa 112,224,136\n"
"s_add_u32 s66, s66, 0x100000\nMvaa 144,224,144\nRa 200,10,18432\nMvaa 144,228,148\ns_and_b32 s5, s12, 1\nMvaa 176,228,156\nRa 204,11,18432\nMvaa 176,224,152\n"
"s_mul_i32 s58, s5, 0x202000\nbuffer_load_dwordx4 v[248:251], v1, s[16:19], s6 offen offset:2048 sc1\nMvaa 180,232,152\nRa 208,10,24576\ns_xor_b32 s58, s58, s72\nMvaa 180,236,156\n"
"s_add_u32 s58, s58, 0x15c0000\nMvaa 148,236,148\nRa 212,11,24576\nMvaa 148,232,144\ns_lshl_b32 s5, s5, 8\nMvaa 116,232,136\nRa 216,10,26624\nMvaa 116,236,140\n"
"s_add_u32 s61, s58, s5\nbuffer_load_dwordx4 v[252:255], v1, s[16:19], s6 offen offset:3072 sc1\nMvaa 84,236,132\nRa 220,11,26624\nMvaa 84,232,128\ns_lshl4_add_u32 s60, s13, s61\n"
"Mvaa 88,240,128\ns_add_u32 s54, s60, 0\ns_xor_b32 s60, s54, 0x100\nMvaa 88,244,132\ns_cmp_eq_u32 s11, 0\ns_cselect_b32 s54, s54, 0x40000000\ns_add_u32 s6, s56, 0\n"
"Mvaa 120,244,140\ns_getpc_b64 s[8:9]\n.PC36:\ns_add_u32 s8, s8, .HD13-.PC36\ns_addc_u32 s9, s9, 0\nMvaa 120,240,136\ns_mov_b32 s97, 0xff800000\n"
"buffer_load_dwordx4 v[192:195], v1, s[16:19], s6 offen offset:0 sc1\nMvaa 152,240,144\ns_mov_b32 s90, 3\ns_cmp_lt_u32 s11, 2\ns_cselect_b32 s92, 2, 0\nMvaa 152,244,148\n"
"s_add_u32 s86, s2, 0x20000\nMvaa 184,244,156\ns_add_u32 s87, s2, 0x410000\ns_cmp_eq_u32 s13, 7\ns_cselect_b32 s92, s92, 0\nMvaa 184,240,152\ns_cmp_gt_u32 s13, 5\n"
"s_cselect_b32 s4, s12, 15\nMvaa 188,248,152\ns_cmp_lt_u32 s4, 14\ns_cselect_b32 s5, s11, 3\nMvaa 188,252,156\ns_cmp_lt_u32 s5, 2\ns_cselect_b32 s91, 1, 100\n"
"buffer_load_dwordx4 v[196:199], v1, s[16:19], s6 offen offset:1024 sc1\nMvaa 156,252,148\nv_and_b32 v27, 0x70, v1\n"
"buffer_load_dword v27, v27, s[16:19], s61 offen offset:128 sc0 sc1\nMvaa 156,248,144\ns_lshr_b32 s2, s13, 2\ns_bfe_u32 s3, s13, 0x10001\nMvaa 124,248,136\ns_mul_i32 s3, s3, 17\n"
"s_add_u32 s2, s2, s3\nMvaa 124,252,140\ns_lshl2_add_u32 s3, s14, 0\nMvaa 92,252,132\ns_lshl_b32 s3, s3, 2\ns_lshr_b32 s3, 0xe420ca86, s3\nMvaa 92,248,128\nWL 0\nMaaa 16,224,192\n"
"Ra 128,10,32768\ns_and_b32 s3, s3, 15\nbuffer_load_dwordx4 v[200:203], v1, s[16:19], s6 offen offset:2048 sc1\nMaaa 16,228,196\ns_add_u32 s2, s2, s3\ns_cmp_eq_u32 s2, 32\n"
"s_cselect_b32 s4, 16, s2\nMaaa 48,228,204\nRa 132,11,32768\ns_mul_i32 s75, s4, 65536\nMaaa 48,224,200\ns_lshr_b32 s2, s13, 2\ns_bfe_u32 s3, s13, 0x10001\nMaaa 80,224,208\n"
"Ra 136,10,34816\nMaaa 80,228,212\ns_mul_i32 s3, s3, 17\ns_add_u32 s2, s2, s3\nMaaa 112,228,220\nRa 140,11,34816\ns_lshl2_add_u32 s3, s14, 1\n"
"buffer_load_dwordx4 v[204:207], v1, s[16:19], s6 offen offset:3072 sc1\nMaaa 112,224,216\ns_lshl_b32 s3, s3, 2\nMaaa 116,232,216\nRa 144,10,40960\ns_lshr_b32 s3, 0xe420ca86, s3\n"
"Maaa 116,236,220\ns_and_b32 s3, s3, 15\ns_add_u32 s2, s2, s3\nMaaa 84,236,212\nRa 148,11,40960\ns_cmp_eq_u32 s2, 32\ns_cselect_b32 s4, 16, s2\ns_add_u32 s6, s56, 4096\n"
"Maaa 84,232,208\ns_mul_i32 s76, s4, 65536\ns_lshr_b32 s2, s13, 2\nMaaa 52,232,200\nRa 152,10,43008\ns_bfe_u32 s3, s13, 0x10001\n"
"buffer_load_dwordx4 v[208:211], v1, s[16:19], s6 offen offset:0 sc1\nMaaa 52,236,204\ns_mul_i32 s3, s3, 17\nMaaa 20,236,196\nRa 156,11,43008\ns_add_u32 s2, s2, s3\n"
"Maaa 20,232,192\ns_lshl2_add_u32 s3, s14, 2\ns_lshl_b32 s3, s3, 2\nMaaa 24,240,192\ns_lshr_b32 s3, 0xe420ca86, s3\ns_and_b32 s3, s3, 15\nMaaa 24,244,196\ns_add_u32 s2, s2, s3\n"
"s_cmp_eq_u32 s2, 32\ns_cselect_b32 s4, 16, s2\nMaaa 56,244,204\ns_mul_i32 s77, s4, 65536\ns_lshr_b32 s2, s13, 2\n"
"buffer_load_dwordx4 v[212:215], v1, s[16:19], s6 offen offset:1024 sc1\nMaaa 56,240,200\ns_bfe_u32 s3, s13, 0x10001\ns_mul_i32 s3, s3, 17\nMaaa 88,240,208\ns_add_u32 s2, s2, s3\n"
"Maaa 88,244,212\ns_lshl2_add_u32 s3, s14, 3\ns_lshl_b32 s3, s3, 2\nMaaa 120,244,220\ns_lshr_b32 s3, 0xe420ca86, s3\ns_and_b32 s3, s3, 15\nMaaa 120,240,216\ns_add_u32 s2, s2, s3\n"
"s_cmp_eq_u32 s2, 32\ns_cselect_b32 s4, 16, s2\nMaaa 124,248,216\ns_mul_i32 s78, s4, 65536\ns_lshr_b32 s3, s11, 1\n"
"buffer_load_dwordx4 v[216:219], v1, s[16:19], s6 offen offset:2048 sc1\nMaaa 124,252,220\ns_lshl_b32 s3, s3, 15\ns_add_u32 s2, s72, s3\nMaaa 92,252,212\ns_and_b32 s3, s11, 1\n"
"Maaa 92,248,208\ns_lshl_b32 s3, s3, 12\ns_add_u32 s2, s2, s3\nMaaa 60,248,200\ns_add_u32 s2, s2, 0x0\ns_mov_b32 s55, 16384\nMaaa 60,252,204\ns_add_u32 s75, s75, s2\n"
"Maaa 28,252,196\ns_add_u32 s76, s76, s2\ns_add_u32 s77, s77, s2\nbuffer_load_dwordx4 v[220:223], v1, s[16:19], s6 offen offset:3072 sc1\nMaaa 28,248,192\nWV 22\ns_barrier\n"
"Maaa 0,160,192\nTa 224,16,32768\ns_add_u32 s78, s78, s2\nMaaa 0,164,196\nTa 226,16,34816\nMaaa 32,164,204\nMaaa 32,160,200\nTa 228,16,49152\nMaaa 64,160,208\nTa 230,16,51200\n"
"Maaa 64,164,212\nMaaa 96,164,220\nTa 232,17,32768\nMaaa 96,160,216\nTa 234,17,34816\nMaaa 100,168,216\nWV 6\nv_cmp_ne_u32 vcc, s15, v27\ns_nop 0\ns_cbranch_vccnz .S37\n.S38:\n"
"s_sub_u32 s45, s86, 131072\nMaaa 100,172,220\nTa 236,17,49152\ns_add_u32 m0, s47, 0\ns_add_u32 s46, s45, 0x0\nMaaa 68,172,212\nTa 238,17,51200\nMaaa 68,168,208\nD1 1,16,46,0\n"
"s_mov_b32 s36, s68\nMaaa 36,168,200\nTa 240,18,32768\nMaaa 36,172,204\nTa 242,18,34816\nMaaa 4,172,196\ns_and_b32 s37, s69, 0xffff\nMaaa 4,168,192\nTa 244,18,49152\n"
"Maaa 8,176,192\nTa 246,18,51200\nD1 1,16,46,1024\nMaaa 8,180,196\ns_mov_b32 s38, 0x1c0000\nMaaa 40,180,204\nTa 248,19,32768\nMaaa 40,176,200\nTa 250,19,34816\nMaaa 72,176,208\n"
"s_mov_b32 s39, 0x20000\nMaaa 72,180,212\nTa 252,19,49152\nD1 1,16,46,2048\nMaaa 104,180,220\nTa 254,19,51200\ns_and_b32 s2, s74, 15\nMaaa 104,176,216\ns_max_u32 s2, s2, 2\n"
"Maaa 108,184,216\ns_sub_u32 s2, s2, 2\nMaaa 108,188,220\ns_mul_i32 s3, s73, 14\nMaaa 76,188,212\nD1 1,16,46,3072\nMaaa 76,184,208\ns_add_u32 s2, s2, s3\n"
"s_lshl2_add_u32 s2, s2, s11\nMaaa 44,184,200\ns_mul_i32 s3, s2, 4682\nMaaa 44,188,204\ns_lshr_b32 s3, s3, 16\nMaaa 12,188,196\ns_mul_i32 s22, s3, 14\nMaaa 12,184,192\nWL 0\n"
"Mvaa 80,224,128\nTa 160,12,32768\ns_add_u32 m0, s47, -65536\ns_add_u32 s46, s45, 0x10000\nD1 1,16,46,0\nMvaa 80,228,132\nTa 162,12,34816\nMvaa 84,236,132\ns_sub_u32 s22, s2, s22\n"
"Mvaa 84,232,128\nTa 164,12,49152\nMvaa 88,240,128\nTa 166,12,51200\nMvaa 88,244,132\nD1 1,16,46,1024\nMvaa 92,252,132\nTa 168,13,32768\ns_and_b32 s21, s3, 28\nMvaa 92,248,128\n"
"Ta 170,13,34816\nMvaa 124,248,136\ns_add_u32 s21, s21, s3\nMvaa 124,252,140\nTa 172,13,49152\nMvaa 120,244,140\nTa 174,13,51200\nD1 1,16,46,2048\nMvaa 120,240,136\n"
"s_lshl_b32 s21, s21, 4\ns_add_u32 s21, s21, 1024\nMvaa 116,232,136\nTa 176,14,32768\nCV 40,80,81\nMvaa 116,236,140\nTa 178,14,34816\nMvaa 112,228,140\ns_xor_b32 s3, s3, 28\n"
"CV 41,82,83\nMvaa 112,224,136\nTa 180,14,49152\nD1 1,16,46,3072\nMvaa 144,224,144\nTa 182,14,51200\ns_bfe_u32 s41, s3, 0x30002\nMvaa 144,228,148\ns_cmp_gt_u32 s22, 6\n"
"s_cselect_b32 s2, 1179648, 0\ns_cselect_b32 s1, 16, 0\nMvaa 148,236,148\nTa 184,15,32768\nMvaa 148,232,144\nTa 186,15,34816\nCV 42,84,85\nMvaa 152,240,144\ns_add_u32 s41, s41, s1\n"
"Mvaa 152,244,148\nTa 188,15,49152\nCV 43,86,87\nMvaa 156,252,148\nTa 190,15,51200\ns_lshl4_add_u32 s41, s41, 0xc048400\nMvaa 156,248,144\ns_and_b32 s1, s3, 28\nCV 44,88,89\n"
"Mvaa 188,248,152\ns_add_u32 s3, s3, s1\nMvaa 188,252,156\ns_lshl_b32 s3, s3, 11\nMvaa 184,244,156\ns_add_u32 s2, s2, s3\nCV 45,90,91\nMvaa 184,240,152\ns_lshl_b32 s3, s22, 17\n"
"s_add_u32 s2, s2, s3\nMvaa 180,232,152\ns_add_u32 s20, s2, 0xce0000\nCV 46,92,93\nMvaa 180,236,156\ns_lshl_b32 s2, s22, 7\nMvaa 176,228,156\ns_add_u32 s40, s2, 0xc04b800\n"
"CV 47,94,95\nMvaa 176,224,152\nWL 0\ns_barrier\nMvaa 64,160,128\nRa 192,10,49152\ns_add_u32 m0, s47, 32768\ns_add_u32 s46, s45, 0x8000\nMvaa 64,164,132\ns_mul_i32 s2, s21, 896\n"
"Mvaa 68,172,132\nRa 196,11,49152\nD1 1,16,46,0\nMvaa 68,168,128\ns_lshl_b32 s3, s22, 6\nCV 60,120,121\nMvaa 72,176,128\nRa 200,10,51200\nMvaa 72,180,132\ns_add_u32 s22, s2, s3\n"
"CV 61,122,123\nMvaa 76,188,132\nRa 204,11,51200\ns_add_u32 s23, s22, 57344\nMvaa 76,184,128\nD1 1,16,46,1024\nCV 62,124,125\nMvaa 108,184,136\nRa 208,10,57344\nMvaa 108,188,140\n"
"CV 63,126,127\nMvaa 104,180,140\nRa 212,11,57344\nCV 56,112,113\nMvaa 104,176,136\nCV 57,114,115\nMvaa 100,168,136\nRa 216,10,59392\nD1 1,16,46,2048\nMvaa 100,172,140\n"
"CV 58,116,117\nMvaa 96,164,140\nRa 220,11,59392\nCV 59,118,119\nMvaa 96,160,136\nCV 32,64,65\nMvaa 128,160,144\nCV 33,66,67\nCV 34,68,69\nMvaa 128,164,148\nD1 1,16,46,3072\n"
"Mvaa 132,172,148\nCV 35,70,71\nMvaa 132,168,144\nCV 36,72,73\nCV 37,74,75\nMvaa 136,176,144\ns_add_u32 m0, s47, 49152\ns_add_u32 s46, s45, 0x400000\nMvaa 136,180,148\nCV 38,76,77\n"
"CV 39,78,79\nMvaa 140,188,148\nD1 1,16,46,0\nMvaa 140,184,144\nCV 52,104,105\nCV 53,106,107\nMvaa 172,184,152\nCV 54,108,109\nMvaa 172,188,156\nCV 55,110,111\nMvaa 168,180,156\n"
"CV 48,96,97\nCV 49,98,99\nMvaa 168,176,152\nD1 1,16,46,1024\nMvaa 164,168,152\nCV 50,100,101\nCV 51,102,103\nMvaa 164,172,156\nCV 104,144,145\nMvaa 160,164,156\nCV 105,146,147\n"
"CV 106,148,149\nMvaa 160,160,152\nWL 0\nMaaa 0,160,192\nD1 1,16,46,2048\nSv 32,1,16,75,0\nMaaa 0,164,196\nCV 107,150,151\nMaaa 4,172,196\nCV 108,152,153\nCV 109,154,155\n"
"Maaa 4,168,192\nCV 110,156,157\nCV 111,158,159\nMaaa 8,176,192\nCV 124,184,185\nCV 125,186,187\nMaaa 8,180,196\nD1 1,16,46,3072\nMaaa 12,188,196\nCV 126,188,189\nCV 127,190,191\n"
"Maaa 12,184,192\nCV 120,176,177\nCV 121,178,179\nMaaa 44,184,200\ns_add_u32 m0, s47, -49152\ns_add_u32 s46, s45, 0x408000\nSv 36,1,16,75,1024\nMaaa 44,188,204\nCV 122,180,181\n"
"Maaa 40,180,204\nD1 1,16,46,0\nCV 123,182,183\nMaaa 40,176,200\nCV 96,128,129\nCV 97,130,131\nMaaa 36,168,200\nCV 98,132,133\nCV 99,134,135\nMaaa 36,172,204\nCV 100,136,137\n"
"Maaa 32,164,204\nCV 101,138,139\nCV 102,140,141\nMaaa 32,160,200\nD1 1,16,46,1024\nCV 103,142,143\nMaaa 64,160,208\nSv 40,1,16,75,2048\nCV 116,168,169\nMaaa 64,164,212\n"
"CV 117,170,171\nMaaa 68,172,212\nCV 118,172,173\nCV 119,174,175\nMaaa 68,168,208\nCV 112,160,161\nCV 113,162,163\nMaaa 72,176,208\nD1 1,16,46,2048\nCV 114,164,165\n"
"Maaa 72,180,212\nCV 115,166,167\nMaaa 76,188,212\nv_add_u32 v2, 0x10000, v1\ns_lshr_b32 s2, s59, 1\nMaaa 76,184,208\nv_add_u32 v24, s2, v2\nv_xor_b32 v26, 0x1000, v24\n"
"Maaa 108,184,216\nSv 44,1,16,75,3072\nv_lshrrev_b32 v20, 4, v0\nMaaa 108,188,220\nD1 1,16,46,3072\nMaaa 104,180,220\nv_lshlrev_b32 v20, 2, v20\nv_and_b32 v0, 15, v0\n"
"Maaa 104,176,216\nv_sub_u32 v0, v0, v20\ns_lshl_b32 s2, s11, 4\nMaaa 100,168,216\nv_add_u32 v3, s2, v0\nv_subrev_u32 v0, 32, v3\nMaaa 100,172,220\nv_mov_b32 v4, 0\n"
"Maaa 96,164,220\nv_mov_b32 v5, 0\nv_mov_b32 v6, 0\nMaaa 96,160,216\nWV 20\ns_barrier\nMaaa 16,224,192\ns_add_u32 s75, s75, s55\nSv 48,1,16,75,0\nMaaa 16,228,196\nv_mov_b32 v7, 0\n"
"Maaa 20,236,196\nv_mov_b32 v8, 0\nv_mov_b32 v9, 0\nMaaa 20,232,192\nv_mov_b32 v10, 0\nv_mov_b32 v11, 0\nMaaa 24,240,192\nv_mov_b32 v12, 0\nv_mov_b32 v13, 0\nMaaa 24,244,196\n"
"v_mov_b32 v14, 0\nMaaa 28,252,196\nv_mov_b32 v15, 0\nv_mov_b32 v16, 0\nMaaa 28,248,192\nv_mov_b32 v17, 0\nv_mov_b32 v18, 0\nMaaa 60,248,200\nSv 52,1,16,75,1024\nv_mov_b32 v19, 0\n"
"Maaa 60,252,204\nv_mov_b32 v86, 0\nMaaa 56,244,204\nv_mov_b32 v87, 0\nv_mov_b32 v88, 0\nMaaa 56,240,200\nv_mov_b32 v89, 0\nAR 92,0\nAR 93,1\nMaaa 52,232,200\nCV 128,92,93\n"
"AR 92,2\nAR 93,3\nMaaa 52,236,204\nCV 129,92,93\nAR 92,4\nAR 93,5\nMaaa 48,228,204\nRa 128,24,0\nCV 130,92,93\nAR 92,6\nMaaa 48,224,200\nAR 93,7\nCV 131,92,93\nAR 92,8\n"
"Maaa 80,224,208\nSv 56,1,16,75,2048\nRa 132,24,1024\nAR 93,9\nMaaa 80,228,212\nCV 132,92,93\nAR 92,10\nAR 93,11\nMaaa 84,236,212\nRa 136,24,2048\nCV 133,92,93\nAR 92,12\n"
"Maaa 84,232,208\nAR 93,13\nCV 134,92,93\nAR 92,14\nMaaa 88,240,208\nRa 140,24,3072\nAR 93,15\nCV 135,92,93\nMaaa 88,244,212\nAR 92,40\nAR 93,41\nCV 148,92,93\nMaaa 92,252,212\n"
"Ra 144,26,0\nAR 92,42\nAR 93,43\nMaaa 92,248,208\nCV 149,92,93\nAR 92,44\nAR 93,45\nMaaa 124,248,216\nSv 60,1,16,75,3072\nRa 148,26,1024\nCV 150,92,93\nMaaa 124,252,220\nAR 92,46\n"
"AR 93,47\nCV 151,92,93\nMaaa 120,244,220\nRa 152,26,2048\nAR 92,32\nAR 93,33\nMaaa 120,240,216\nCV 144,92,93\nAR 92,34\nAR 93,35\nMaaa 116,232,216\nRa 156,26,3072\nCV 145,92,93\n"
"AR 92,36\nMaaa 116,236,220\nAR 93,37\nCV 146,92,93\nAR 92,38\nMaaa 112,228,220\nAR 93,39\nCV 147,92,93\nAR 92,16\nMaaa 112,224,216\nWL 4\nCvav 32,128,192,4\nRa 160,24,8192\n"
"AR 93,17\nCV 136,92,93\nMvav 32,132,200\nAR 92,18\nAR 93,19\nCV 137,92,93\nMvav 32,136,208\nRa 164,24,9216\nAR 92,20\nAR 93,21\nMvav 32,140,216\nCV 138,92,93\nAR 92,22\nAR 93,23\n"
"Cvav 36,140,220,8\nRa 168,24,10240\nCV 139,92,93\nAR 92,24\nMvav 36,136,212\ns_add_u32 m0, s89, 32768\nAR 93,25\nCV 140,92,93\nMvav 36,132,204\nRa 172,24,11264\nWL 4\ns_barrier\n"
"Mvav 36,128,196\nAR 92,26\nAR 93,27\nCV 141,92,93\nMvav 36,144,228\nRa 176,26,8192\nAR 92,28\nAR 93,29\nSv 96,1,16,76,0\nMvav 36,148,236\ns_add_u32 s46, s86, s88\nCV 142,92,93\n"
"AR 92,30\nMvav 36,152,244\nRa 180,26,9216\nAR 93,31\nCV 143,92,93\nMvav 36,156,252\nAR 92,56\nAR 93,57\nCV 156,92,93\nMvav 32,156,248\nRa 184,26,10240\nAR 92,58\nAR 93,59\n"
"Mvav 32,152,240\nD1 1,16,46,0\nCV 157,92,93\nAR 92,60\nMvav 32,148,232\nRa 188,26,11264\nWL 4\nAR 93,61\nMvav 32,144,224\nCV 158,92,93\nAR 92,62\nAR 93,63\nCvav 40,160,192,12\n"
"CV 159,92,93\nAR 92,48\nAR 93,49\nMvav 40,164,200\nCV 152,92,93\nAR 92,50\nAR 93,51\nMvav 40,168,208\nCV 153,92,93\nAR 92,52\nAR 93,53\nv_and_b32 v27, 0x70, v1\n"
"buffer_load_dword v27, v27, s[16:19], s61 offen offset:136 sc0 sc1\nMvav 40,172,216\nCV 154,92,93\nAR 92,54\nAR 93,55\nCvav 44,172,220,16\nCV 155,92,93\nAR 92,64\nAR 93,65\n"
"Mvav 44,168,212\nD1 1,16,46,1024\nCV 160,92,93\nAR 92,66\nMvav 44,164,204\nWL 0\nAR 93,67\nCV 161,92,93\nMvav 44,160,196\nAR 92,68\nAR 93,69\nCV 162,92,93\nMvav 44,176,228\n"
"AR 92,70\nAR 93,71\nCV 163,92,93\nSv 100,1,16,76,1024\nMvav 44,180,236\ns_add_u32 s88, s88, 32768\nEX 80,32\nMvav 44,184,244\nAR 92,72\nAR 93,73\nCV 164,92,93\nMvav 44,188,252\n"
"EX 81,33\nMvav 40,188,248\nAR 92,74\nAR 93,75\nCV 165,92,93\nMvav 40,184,240\nD1 1,16,46,2048\nAR 92,76\nAR 93,77\nMvav 40,180,232\nCV 166,92,93\nAR 92,78\nAR 93,79\n"
"Mvav 40,176,224\nAD 84,80,81\nCV 64,80,81\nCV 167,92,93\nAR 92,80\nAR 93,81\nCV 168,92,93\nEX 82,34\nAR 92,82\nAR 93,83\nCV 169,92,93\nAR 92,84\nWV 17\nEX 83,35\nAD 84,84,82\n"
"AR 93,85\nCV 170,92,93\nD1 1,16,46,3072\ns_add_u32 m0, s89, 49152\nAR 92,86\ns_barrier\nAR 93,87\nCV 171,92,93\nAD 84,84,83\nCV 65,82,83\nAR 92,88\nRa 128,24,32768\nAR 93,89\n"
"CV 172,92,93\nSv 104,1,16,76,2048\ns_add_u32 s46, s87, s88\nEX 80,36\nRa 132,24,33792\nAR 92,90\nAR 93,91\nEX 81,37\nRa 136,24,34816\nCV 173,92,93\nAR 92,92\nD1 1,16,46,0\n"
"AR 93,93\nCV 174,92,93\nRa 140,24,35840\nAR 92,94\nAR 93,95\nAD 85,80,81\nCV 68,80,81\nCV 175,92,93\nRa 144,26,32768\nAR 92,104\nAR 93,105\nEX 82,38\nCV 180,92,93\n"
"Ra 148,26,33792\nAR 92,106\nAR 93,107\nEX 83,39\nRa 152,26,34816\nAD 85,85,82\nCV 181,92,93\nD1 1,16,46,1024\nAR 92,108\nAR 93,109\nRa 156,26,35840\nCV 182,92,93\nAR 92,110\n"
"AD 85,85,83\nCV 69,82,83\nAR 93,111\nRa 160,24,40960\nCV 183,92,93\nAR 92,96\nSv 108,1,16,76,3072\ns_xor_b32 s89, s89, 0x10000\nEX 80,40\nRa 164,24,41984\nAR 93,97\nCV 176,92,93\n"
"WL 6\nEX 81,41\nRa 168,24,43008\nAD 84,84,80\nAR 92,98\nD1 1,16,46,2048\nAR 93,99\nCV 177,92,93\nRa 172,24,44032\nAR 92,100\nAR 93,101\nAD 84,84,81\nCV 66,80,81\nCV 178,92,93\n"
"Cvav 48,128,192,4\nRa 176,26,40960\nAR 92,102\nAR 93,103\nMvav 48,132,200\nEX 82,42\nCV 179,92,93\nMvav 48,136,208\nRa 180,26,41984\nAR 92,120\nAR 93,121\nMvav 48,140,216\nWL 6\n"
"EX 83,43\nWV 9\nv_cmp_ne_u32 vcc, s15, v27\ns_nop 0\ns_cbranch_vccnz .S40\n.S41:\nCvav 52,140,220,8\nRa 184,26,43008\nAD 84,84,82\nCV 188,92,93\nMvav 52,136,212\nD1 1,16,46,3072\n"
"s_add_u32 m0, s89, 0\nAR 92,122\nMvav 52,132,204\nRa 188,26,44032\nAR 93,123\nCV 189,92,93\nMvav 52,128,196\nAD 84,84,83\nCV 67,82,83\nAR 92,124\nMvav 52,144,228\nRa 128,2,49152\n"
"AR 93,125\nCV 190,92,93\ns_add_u32 s76, s76, s55\nSv 112,1,16,76,0\nMvav 52,148,236\ns_add_u32 s46, s86, s88\nEX 80,44\nMvav 52,152,244\nRa 132,2,50176\n"
"v_xor_b32 v24, 0x10000, v24\nAR 92,126\nMvav 52,156,252\nWL 6\nEX 81,45\nMvav 48,156,248\nRa 136,2,51200\nAD 85,85,80\nAR 93,127\nMvav 48,152,240\nD1 1,16,46,0\nCV 191,92,93\n"
"AR 92,112\nMvav 48,148,232\nRa 140,2,52224\nAR 93,113\nCV 184,92,93\nMvav 48,144,224\nAD 85,85,81\nCV 70,80,81\nAR 92,114\nCvav 56,160,192,12\nRa 144,2,53248\nAR 93,115\n"
"CV 185,92,93\nMvav 56,164,200\nEX 82,46\nAR 92,116\nMvav 56,168,208\nRa 148,2,54272\nv_xor_b32 v26, 0x10000, v26\nAR 93,117\nMvav 56,172,216\nWL 6\nEX 83,47\nCvav 60,172,220,16\n"
"Ra 152,2,55296\nAD 85,85,82\nCV 186,92,93\nMvav 60,168,212\nD1 1,16,46,1024\nAR 92,118\nAR 93,119\nMvav 60,164,204\nRa 156,2,56320\nCV 187,92,93\nMvav 60,160,196\nAD 85,85,83\n"
"CV 71,82,83\nMvav 60,176,228\nRa 160,2,57344\nv_max_f32 v90, v84, v85\nSv 116,1,16,76,1024\nMvav 60,180,236\ns_add_u32 s88, s88, 32768\nEX 80,48\nMvav 60,184,244\nRa 164,2,58368\n"
"v_cmp_lt_f32 vcc, s63, v90\nMvav 60,188,252\nWL 8\nEX 81,49\nMvav 56,188,248\nRa 168,2,59392\ns_cbranch_vccnz .T22\n.T23:\nv_mov_b32 v20, v84\nv_mov_b32 v21, v85\n"
"v_mov_b32 v22, v20\nv_mov_b32 v23, v21\nMvav 56,184,240\nD1 1,16,46,2048\nv_permlane32_swap_b32 v20, v22\nv_permlane32_swap_b32 v21, v23\nAD 20,20,22\nAD 21,21,23\n"
"v_mov_b32 v22, v20\nv_mov_b32 v23, v21\nMvav 56,180,232\nRa 172,2,60416\nv_permlane16_swap_b32 v20, v22\nv_permlane16_swap_b32 v21, v23\nAD 20,20,22\nAD 21,21,23\n"
"v_min_f32 v90, v20, v21\nv_cmp_gt_f32 vcc, s64, v90\ns_nop 0\ns_cbranch_vccnz .T24\n.T25:\nAD 86,86,84\nMvav 56,176,224\nAD 84,80,81\nCV 72,80,81\nZaav 0,128,64\nRa 176,2,61440\n"
"Zaav 4,128,68\nEX 82,50\nZaav 12,132,68\nRa 180,2,62464\nZaav 8,132,64\nWL 8\nEX 83,51\nZaav 16,136,64\nRa 184,2,63488\nAD 84,84,82\nZaav 20,136,68\nD1 1,16,46,3072\n"
"s_add_u32 m0, s89, 16384\nZaav 28,140,68\nRa 188,2,64512\ns_barrier\nZaav 24,140,64\nAD 84,84,83\nCV 73,82,83\nZaav 32,144,64\nRa 128,24,0\nSv 120,1,16,76,2048\nZaav 36,144,68\n"
"s_add_u32 s46, s87, s88\nEX 80,52\nbuffer_store_dword v25, off, s[16:19], s54 offset:12 sc0 sc1\nZaav 44,148,68\nRa 132,24,1024\nv_xor_b32 v2, 0x10000, v2\nZaav 40,148,64\nWL 8\n"
"EX 81,53\nZaav 48,152,64\nRa 136,24,2048\nAD 87,87,85\nZaav 52,152,68\nD1 1,16,46,0\nZaav 60,156,68\nRa 140,24,3072\nZaav 56,156,64\nAD 85,80,81\nCV 76,80,81\nZaav 64,160,64\n"
"Ra 144,26,0\nZaav 68,160,68\nEX 82,54\nZaav 76,164,68\nRa 148,26,1024\nZaav 72,164,64\nWL 8\nEX 83,55\nZaav 80,168,64\nRa 152,26,2048\nAD 85,85,82\nZaav 84,168,68\n"
"D1 1,16,46,1024\nZaav 92,172,68\nRa 156,26,3072\nZaav 88,172,64\nAD 85,85,83\nCV 77,82,83\nZaav 96,176,64\nRa 160,24,8192\nSv 124,1,16,76,3072\nZaav 100,176,68\nEX 80,56\n"
"Zaav 108,180,68\nRa 164,24,9216\nZaav 104,180,64\nWL 6\nEX 81,57\nZaav 112,184,64\nRa 168,24,10240\nAD 84,84,80\nZaav 116,184,68\nD1 1,16,46,2048\nZaav 124,188,68\n"
"Ra 172,24,11264\nZaav 120,188,64\nAD 84,84,81\nCV 74,80,81\nCvav 32,128,192,4\nRa 176,26,8192\nMvav 32,132,200\nEX 82,58\nMvav 32,136,208\nRa 180,26,9216\nMvav 32,140,216\nWL 6\n"
"EX 83,59\nCvav 36,140,220,8\nRa 184,26,10240\nAD 84,84,82\nMvav 36,136,212\nD1 1,16,46,3072\ns_add_u32 m0, s89, 32768\nMvav 36,132,204\nRa 188,26,11264\nMvav 36,128,196\n"
"AD 84,84,83\nCV 75,82,83\nMvav 36,144,228\nRa 128,2,16384\nSv 128,1,16,77,0\nMvav 36,148,236\ns_add_u32 s46, s86, s88\nEX 80,60\nMvav 36,152,244\nRa 132,2,17408\nMvav 36,156,252\n"
"WL 6\nEX 81,61\nMvav 32,156,248\nRa 136,2,18432\nAD 85,85,80\nMvav 32,152,240\nD1 1,16,46,0\nMvav 32,148,232\nRa 140,2,19456\nMvav 32,144,224\nAD 85,85,81\nCV 78,80,81\n"
"Cvav 40,160,192,12\nRa 144,2,20480\nMvav 40,164,200\nEX 82,62\nMvav 40,168,208\nRa 148,2,21504\nMvav 40,172,216\nWL 6\nEX 83,63\nCvav 44,172,220,16\nRa 152,2,22528\nAD 85,85,82\n"
"Mvav 44,168,212\nD1 1,16,46,1024\nMvav 44,164,204\nRa 156,2,23552\nMvav 44,160,196\nAD 85,85,83\nCV 79,82,83\nMvav 44,176,228\nRa 160,2,24576\nv_max_f32 v90, v84, v85\n"
"Sv 132,1,16,77,1024\nMvav 44,180,236\ns_add_u32 s88, s88, 32768\nEX 80,32\nMvav 44,184,244\nRa 164,2,25600\nv_cmp_lt_f32 vcc, s63, v90\nMvav 44,188,252\nWL 8\nEX 81,33\n"
"Mvav 40,188,248\nRa 168,2,26624\ns_cbranch_vccnz .T27\n.T28:\nMvav 40,184,240\nD1 1,16,46,2048\nMvav 40,180,232\nRa 172,2,27648\nAD 86,86,84\nMvav 40,176,224\nAD 84,80,81\n"
"CV 64,80,81\nMaav 0,128,72\nRa 176,2,28672\nv_mov_b32 v112, -1\nMaav 4,128,76\nEX 82,34\nMaav 12,132,76\nRa 180,2,29696\nMaav 8,132,72\ns_waitcnt vmcnt(18) lgkmcnt(8)\nEX 83,35\n"
"Maav 16,136,72\nRa 184,2,30720\nAD 84,84,82\nMaav 20,136,76\nD1 1,16,46,3072\ns_add_u32 m0, s89, 49152\nMaav 28,140,76\nRa 188,2,31744\ns_barrier\nMaav 24,140,72\nAD 84,84,83\n"
"CV 65,82,83\nMaav 32,144,72\nRa 128,24,32768\nSv 136,1,16,77,2048\nMaav 36,144,76\ns_add_u32 s46, s87, s88\nEX 80,36\nMaav 44,148,76\nRa 132,24,33792\nMaav 40,148,72\nWL 8\n"
"EX 81,37\nMaav 48,152,72\nRa 136,24,34816\nAD 87,87,85\nMaav 52,152,76\nD1 1,16,46,0\nMaav 60,156,76\nRa 140,24,35840\nMaav 56,156,72\nAD 85,80,81\nCV 68,80,81\nMaav 64,160,72\n"
"Ra 144,26,32768\nv_mov_b32 v113, -1\nMaav 68,160,76\nEX 82,38\nMaav 76,164,76\nRa 148,26,33792\nMaav 72,164,72\nWL 8\nEX 83,39\nMaav 80,168,72\nRa 152,26,34816\nAD 85,85,82\n"
"Maav 84,168,76\nD1 1,16,46,1024\nMaav 92,172,76\nRa 156,26,35840\nMaav 88,172,72\nAD 85,85,83\nCV 69,82,83\nMaav 96,176,72\nRa 160,24,40960\nSv 140,1,16,77,3072\nMaav 100,176,76\n"
"s_xor_b32 s89, s89, 0x10000\nEX 80,40\nMaav 108,180,76\nRa 164,24,41984\nMaav 104,180,72\nWL 6\nEX 81,41\nMaav 112,184,72\nRa 168,24,43008\nAD 84,84,80\nMaav 116,184,76\n"
"D1 1,16,46,2048\nMaav 124,188,76\nRa 172,24,44032\nMaav 120,188,72\nAD 84,84,81\nCV 66,80,81\nCvav 48,128,192,4\nRa 176,26,40960\nv_mov_b32 v114, -1\nMvav 48,132,200\nEX 82,42\n"
"Mvav 48,136,208\nRa 180,26,41984\nMvav 48,140,216\nWL 6\nEX 83,43\nCvav 52,140,220,8\nRa 184,26,43008\nAD 84,84,82\nv_and_b32 v27, 0x70, v1\n"
"buffer_load_dword v27, v27, s[16:19], s61 offen offset:140 sc0 sc1\nMvav 52,136,212\nD1 1,16,46,3072\ns_add_u32 m0, s89, 0\nMvav 52,132,204\nRa 188,26,44032\nMvav 52,128,196\n"
"AD 84,84,83\nCV 67,82,83\nMvav 52,144,228\nRa 128,2,49152\ns_add_u32 s77, s77, s55\nSv 144,1,16,77,0\nMvav 52,148,236\ns_add_u32 s46, s86, s88\nEX 80,44\nMvav 52,152,244\n"
"Ra 132,2,50176\nv_xor_b32 v24, 0x10000, v24\nMvav 52,156,252\nWL 6\nEX 81,45\nMvav 48,156,248\nRa 136,2,51200\nAD 85,85,80\nMvav 48,152,240\nD1 1,16,46,0\nMvav 48,148,232\n"
"Ra 140,2,52224\nMvav 48,144,224\nAD 85,85,81\nCV 70,80,81\nCvav 56,160,192,12\nRa 144,2,53248\nv_mov_b32 v115, -1\nMvav 56,164,200\nEX 82,46\nMvav 56,168,208\nRa 148,2,54272\n"
"v_xor_b32 v26, 0x10000, v26\nMvav 56,172,216\nWL 6\nEX 83,47\nCvav 60,172,220,16\nRa 152,2,55296\nAD 85,85,82\nMvav 60,168,212\nD1 1,16,46,1024\nMvav 60,164,204\nRa 156,2,56320\n"
"Mvav 60,160,196\nAD 85,85,83\nCV 71,82,83\nMvav 60,176,228\nRa 160,2,57344\nv_max_f32 v90, v84, v85\nSv 148,1,16,77,1024\nMvav 60,180,236\ns_add_u32 s88, s88, 32768\nEX 80,48\n"
"Mvav 60,184,244\nRa 164,2,58368\nv_cmp_lt_f32 vcc, s63, v90\nMvav 60,188,252\nWL 8\nEX 81,49\nMvav 56,188,248\nRa 168,2,59392\ns_cbranch_vccnz .T29\n.T30:\nMvav 56,184,240\n"
"D1 1,16,46,2048\nMvav 56,180,232\nRa 172,2,60416\nAD 86,86,84\nMvav 56,176,224\nAD 84,80,81\nCV 72,80,81\nMaav 0,128,64\nRa 176,2,61440\nMaav 4,128,68\nEX 82,50\nMaav 12,132,68\n"
"Ra 180,2,62464\nMaav 8,132,64\ns_waitcnt vmcnt(18) lgkmcnt(8)\nEX 83,51\nMaav 16,136,64\nRa 184,2,63488\nAD 84,84,82\nMaav 20,136,68\nD1 1,16,46,3072\ns_add_u32 m0, s89, 16384\n"
"Maav 28,140,68\nRa 188,2,64512\ns_barrier\nMaav 24,140,64\nAD 84,84,83\nCV 73,82,83\nMaav 32,144,64\nRa 128,24,0\nSv 152,1,16,77,2048\nMaav 36,144,68\ns_add_u32 s46, s87, s88\n"
"EX 80,52\nMaav 44,148,68\nRa 132,24,1024\nv_xor_b32 v2, 0x10000, v2\nMaav 40,148,64\nWL 8\nEX 81,53\nMaav 48,152,64\nRa 136,24,2048\nAD 87,87,85\n"
"buffer_store_dword v25, off, s[16:19], s54 offset:8 sc0 sc1\nMaav 52,152,68\nD1 1,16,46,0\nMaav 60,156,68\nRa 140,24,3072\nMaav 56,156,64\nAD 85,80,81\nCV 76,80,81\n"
"Maav 64,160,64\nRa 144,26,0\nMaav 68,160,68\nEX 82,54\nMaav 76,164,68\nRa 148,26,1024\nMaav 72,164,64\nWL 8\nEX 83,55\nMaav 80,168,64\nRa 152,26,2048\nAD 85,85,82\nMaav 84,168,68\n"
"D1 1,16,46,1024\nMaav 92,172,68\nRa 156,26,3072\nMaav 88,172,64\nAD 85,85,83\nCV 77,82,83\nMaav 96,176,64\nRa 160,24,8192\nSv 156,1,16,77,3072\nMaav 100,176,68\nEX 80,56\n"
"Maav 108,180,68\nRa 164,24,9216\nMaav 104,180,64\nWL 6\nEX 81,57\nMaav 112,184,64\nRa 168,24,10240\nAD 84,84,80\nMaav 116,184,68\nD1 1,16,46,2048\nMaav 124,188,68\n"
"Ra 172,24,11264\nMaav 120,188,64\nAD 84,84,81\nCV 74,80,81\nCvav 32,128,192,4\nRa 176,26,8192\nMvav 32,132,200\nEX 82,58\nMvav 32,136,208\nRa 180,26,9216\nMvav 32,140,216\nWL 6\n"
"EX 83,59\nCvav 36,140,220,8\nRa 184,26,10240\nAD 84,84,82\nMvav 36,136,212\nD1 1,16,46,3072\ns_add_u32 m0, s89, 32768\nMvav 36,132,204\nRa 188,26,11264\nMvav 36,128,196\n"
"AD 84,84,83\nCV 75,82,83\nMvav 36,144,228\nRa 128,2,16384\nSv 160,1,16,78,0\nMvav 36,148,236\ns_add_u32 s46, s86, s88\nEX 80,60\nMvav 36,152,244\nRa 132,2,17408\nMvav 36,156,252\n"
"WL 6\nEX 81,61\nMvav 32,156,248\nRa 136,2,18432\nAD 85,85,80\nMvav 32,152,240\nD1 1,16,46,0\nMvav 32,148,232\nRa 140,2,19456\nSv 112,1,16,49,0\nMvav 32,144,224\nAD 85,85,81\n"
"CV 78,80,81\nCvav 40,160,192,12\nRa 144,2,20480\nMvav 40,164,200\nEX 82,62\nMvav 40,168,208\nRa 148,2,21504\nMvav 40,172,216\nWL 6\nEX 83,63\nCvav 44,172,220,16\nRa 152,2,22528\n"
"AD 85,85,82\nMvav 44,168,212\nD1 1,16,46,1024\nMvav 44,164,204\nRa 156,2,23552\nMvav 44,160,196\nAD 85,85,83\nCV 79,82,83\nMvav 44,176,228\nRa 160,2,24576\n"
"v_max_f32 v90, v84, v85\nSv 164,1,16,78,1024\nMvav 44,180,236\ns_add_u32 s88, s88, 32768\nEX 80,32\nMvav 44,184,244\nRa 164,2,25600\nv_cmp_lt_f32 vcc, s63, v90\nMvav 44,188,252\n"
"WL 8\nEX 81,33\nMvav 40,188,248\nRa 168,2,26624\ns_cbranch_vccnz .T32\n.T33:\nMvav 40,184,240\nD1 1,16,46,2048\nMvav 40,180,232\nRa 172,2,27648\nAD 86,86,84\nMvav 40,176,224\n"
"AD 84,80,81\nCV 64,80,81\nMaav 0,128,72\nRa 176,2,28672\nMaav 4,128,76\nEX 82,34\nMaav 12,132,76\nRa 180,2,29696\nMaav 8,132,72\ns_waitcnt vmcnt(19) lgkmcnt(8)\nEX 83,35\n"
"Maav 16,136,72\nRa 184,2,30720\nAD 84,84,82\nMaav 20,136,76\nD1 1,16,46,3072\ns_add_u32 m0, s89, 49152\nMaav 28,140,76\nRa 188,2,31744\ns_barrier\nMaav 24,140,72\nAD 84,84,83\n"
"CV 65,82,83\nMaav 32,144,72\nRa 128,24,32768\nSv 168,1,16,78,2048\nMaav 36,144,76\ns_add_u32 s46, s87, s88\nEX 80,36\nMaav 44,148,76\nRa 132,24,33792\nMaav 40,148,72\nWL 8\n"
"EX 81,37\nMaav 48,152,72\nRa 136,24,34816\nAD 87,87,85\nMaav 52,152,76\nD1 1,16,46,0\nMaav 60,156,76\nRa 140,24,35840\nMaav 56,156,72\nAD 85,80,81\nCV 68,80,81\nMaav 64,160,72\n"
"Ra 144,26,32768\nMaav 68,160,76\nEX 82,38\nMaav 76,164,76\nRa 148,26,33792\nMaav 72,164,72\nWL 8\nEX 83,39\nMaav 80,168,72\nRa 152,26,34816\nAD 85,85,82\nMaav 84,168,76\n"
"D1 1,16,46,1024\nMaav 92,172,76\nRa 156,26,35840\nMaav 88,172,72\nAD 85,85,83\nCV 69,82,83\nMaav 96,176,72\nRa 160,24,40960\nSv 172,1,16,78,3072\nMaav 100,176,76\n"
"s_xor_b32 s89, s89, 0x10000\nEX 80,40\nMaav 108,180,76\nRa 164,24,41984\nMaav 104,180,72\nWL 6\nEX 81,41\nMaav 112,184,72\nRa 168,24,43008\nAD 84,84,80\nMaav 116,184,76\n"
"D1 1,16,46,2048\nMaav 124,188,76\nRa 172,24,44032\nMaav 120,188,72\nAD 84,84,81\nCV 66,80,81\nCvav 48,128,192,4\nRa 176,26,40960\nMvav 48,132,200\nEX 82,42\nMvav 48,136,208\n"
"Ra 180,26,41984\nMvav 48,140,216\nWL 6\nEX 83,43\nv_cmp_ne_u32 vcc, s15, v27\ns_nop 0\ns_cbranch_vccnz .S43\n.S44:\nv_and_b32 v27, 0x70, v1\n"
"buffer_load_dword v27, v27, s[16:19], s61 offen offset:12 sc0 sc1\nCvav 52,140,220,8\nRa 184,26,43008\nAD 84,84,82\nMvav 52,136,212\nD1 1,16,46,3072\ns_add_u32 m0, s89, 0\n"
"Mvav 52,132,204\nRa 188,26,44032\nMvav 52,128,196\nAD 84,84,83\nCV 67,82,83\nMvav 52,144,228\nRa 128,2,49152\ns_add_u32 s78, s78, s55\nSv 176,1,16,78,0\nMvav 52,148,236\n"
"s_add_u32 s46, s86, s88\nEX 80,44\nMvav 52,152,244\nRa 132,2,50176\nv_xor_b32 v24, 0x10000, v24\nMvav 52,156,252\nWL 6\nEX 81,45\nMvav 48,156,248\nRa 136,2,51200\nAD 85,85,80\n"
"Mvav 48,152,240\nD1 1,16,46,0\nv_lshrrev_b32 v92, 8, v1\nMvav 48,148,232\nRa 140,2,52224\nSv 112,1,16,49,1024\nMvav 48,144,224\nAD 85,85,81\nCV 70,80,81\nCvav 56,160,192,12\n"
"Ra 144,2,53248\nv_bfe_u32 v93, v1, 6, 2\nMvav 56,164,200\ns_add_u32 s49, s49, 131072\nEX 82,46\nMvav 56,168,208\nRa 148,2,54272\nv_xor_b32 v26, 0x10000, v26\nMvav 56,172,216\n"
"WL 6\nEX 83,47\nCvav 60,172,220,16\nRa 152,2,55296\nAD 85,85,82\nMvav 60,168,212\nD1 1,16,46,1024\nv_cmp_eq_u32 vcc, v92, v93\nv_and_b32 v92, 16, v1\nv_mov_b32 v93, 0x3f80\n"
"v_cndmask_b32 v93, 0, v93, vcc\nMvav 60,164,204\nRa 156,2,56320\nMvav 60,160,196\nAD 85,85,83\nCV 71,82,83\nMvav 60,176,228\nRa 160,2,57344\nv_max_f32 v90, v84, v85\n"
"Sv 180,1,16,78,1024\nMvav 60,180,236\ns_add_u32 s88, s88, 32768\nEX 80,48\nMvav 60,184,244\nRa 164,2,58368\nv_cmp_lt_f32 vcc, s63, v90\nMvav 60,188,252\nWL 8\nEX 81,49\n"
"Mvav 56,188,248\nRa 168,2,59392\ns_cbranch_vccnz .T34\n.T35:\nMvav 56,184,240\nD1 1,16,46,2048\nv_lshlrev_b32 v93, v92, v93\nMvav 56,180,232\nRa 172,2,60416\nAD 86,86,84\n"
"Mvav 56,176,224\nAD 84,80,81\nCV 72,80,81\nMaav 0,128,64\nRa 176,2,61440\nv_and_b32 v92, 32, v1\nMaav 4,128,68\nEX 82,50\nMaav 12,132,68\nRa 180,2,62464\nv_cmp_eq_u32 vcc, 0, v92\n"
"v_mov_b32 v128, 0\nv_mov_b32 v129, 0\nv_cndmask_b32 v130, 0, v93, vcc\nMaav 8,132,64\ns_waitcnt vmcnt(20) lgkmcnt(8)\nEX 83,51\nMaav 16,136,64\nRa 184,2,63488\nAD 84,84,82\n"
"Maav 20,136,68\nD1 1,16,46,3072\ns_add_u32 m0, s89, 16384\nMaav 28,140,68\nRa 188,2,64512\ns_barrier\nMaav 24,140,64\nAD 84,84,83\nCV 73,82,83\nMaav 32,144,64\nRa 128,24,0\n"
"Sv 184,1,16,78,2048\nMaav 36,144,68\ns_add_u32 s46, s87, s88\nEX 80,52\nMaav 44,148,68\nRa 132,24,1024\nv_xor_b32 v2, 0x10000, v2\nMaav 40,148,64\nWL 8\nEX 81,53\nMaav 48,152,64\n"
"Ra 136,24,2048\nAD 87,87,85\nMaav 52,152,68\nD1 1,16,46,0\nv_sub_u32 v131, v93, v130\nMaav 60,156,68\nRa 140,24,3072\nMaav 56,156,64\nAD 85,80,81\nCV 76,80,81\nMaav 64,160,64\n"
"Ra 144,26,0\nv_mov_b32 v132, 0\nMaav 68,160,68\nEX 82,54\nMaav 76,164,68\nRa 148,26,1024\nMaav 72,164,64\nWL 8\nEX 83,55\nMaav 80,168,64\nRa 152,26,2048\nAD 85,85,82\n"
"Maav 84,168,68\nD1 1,16,46,1024\nMaav 92,172,68\nRa 156,26,3072\nMaav 88,172,64\nAD 85,85,83\nCV 77,82,83\nMaav 96,176,64\nRa 160,24,8192\nSv 188,1,16,78,3072\nMaav 100,176,68\n"
"EX 80,56\nMaav 108,180,68\nRa 164,24,9216\nv_mov_b32 v133, 0\nMaav 104,180,64\nWL 6\nEX 81,57\nMaav 112,184,64\nRa 168,24,10240\nAD 84,84,80\nMaav 116,184,68\nD1 1,16,46,2048\n"
"Maav 124,188,68\nRa 172,24,11264\nMaav 120,188,64\nAD 84,84,81\nCV 74,80,81\ns_mov_b32 s99, 11\ns_sub_u32 s90, s90, 3\n.p2align 6\n.A59:\nCvav 32,128,192,4\nRa 176,26,8192\n"
"Mvav 32,132,200\nEX 82,58\nMvav 32,136,208\nRa 180,26,9216\nMvav 32,140,216\nWL 6\nEX 83,59\nCvav 36,140,220,8\nRa 184,26,10240\nAD 84,84,82\nMvav 36,136,212\nD1 1,16,46,3072\n"
"s_add_u32 m0, s89, 32768\nMvav 36,132,204\nRa 188,26,11264\nMvav 36,128,196\nAD 84,84,83\nCV 75,82,83\nMvav 36,144,228\nRa 128,2,16384\nMvav 36,148,236\ns_add_u32 s46, s86, s88\n"
"EX 80,60\nMvav 36,152,244\nRa 132,2,17408\nMvav 36,156,252\nWL 6\nEX 81,61\nMvav 32,156,248\nRa 136,2,18432\nAD 85,85,80\nMvav 32,152,240\nD1 1,16,46,0\nMvav 32,148,232\n"
"Ra 140,2,19456\nSv 112,1,16,49,0\nMvav 32,144,224\nAD 85,85,81\nCV 78,80,81\nCvav 40,160,192,12\nRa 144,2,20480\nMvav 40,164,200\nEX 82,62\nMvav 40,168,208\nRa 148,2,21504\n"
"Mvav 40,172,216\nWL 6\nEX 83,63\nCvav 44,172,220,16\nRa 152,2,22528\nAD 85,85,82\nMvav 44,168,212\nD1 1,16,46,1024\nMvav 44,164,204\nRa 156,2,23552\nMvav 44,160,196\nAD 85,85,83\n"
"CV 79,82,83\nMvav 44,176,228\nRa 160,2,24576\nv_max_f32 v90, v84, v85\nMvav 44,180,236\ns_add_u32 s88, s88, 32768\nEX 80,32\nMvav 44,184,244\nRa 164,2,25600\n"
"v_cmp_lt_f32 vcc, s63, v90\nMvav 44,188,252\nWL 8\nEX 81,33\nMvav 40,188,248\nRa 168,2,26624\ns_cbranch_vccnz .T63\n.T64:\nMvav 40,184,240\nD1 1,16,46,2048\nMvav 40,180,232\n"
"Ra 172,2,27648\nAD 86,86,84\nMvav 40,176,224\nAD 84,80,81\nCV 64,80,81\nMaav 0,128,72\nRa 176,2,28672\nMaav 4,128,76\nEX 82,34\nMaav 12,132,76\nRa 180,2,29696\nMaav 8,132,72\n"
"s_waitcnt vmcnt(13) lgkmcnt(8)\nEX 83,35\nMaav 16,136,72\nRa 184,2,30720\nAD 84,84,82\nMaav 20,136,76\nD1 1,16,46,3072\ns_add_u32 m0, s89, 49152\nMaav 28,140,76\nRa 188,2,31744\n"
"s_barrier\nMaav 24,140,72\nAD 84,84,83\nCV 65,82,83\nMaav 32,144,72\nRa 128,24,32768\nMaav 36,144,76\ns_add_u32 s46, s87, s88\nEX 80,36\nMaav 44,148,76\nRa 132,24,33792\n"
"Maav 40,148,72\nWL 8\nEX 81,37\nMaav 48,152,72\nRa 136,24,34816\nAD 87,87,85\nMaav 52,152,76\nD1 1,16,46,0\nMaav 60,156,76\nRa 140,24,35840\nMaav 56,156,72\nAD 85,80,81\n"
"CV 68,80,81\nMaav 64,160,72\nRa 144,26,32768\nMaav 68,160,76\nEX 82,38\nMaav 76,164,76\nRa 148,26,33792\nMaav 72,164,72\nWL 8\nEX 83,39\nMaav 80,168,72\nRa 152,26,34816\n"
"AD 85,85,82\nMaav 84,168,76\nD1 1,16,46,1024\nMaav 92,172,76\nRa 156,26,35840\nMaav 88,172,72\nAD 85,85,83\nCV 69,82,83\nMaav 96,176,72\nRa 160,24,40960\nMaav 100,176,76\n"
"s_xor_b32 s89, s89, 0x10000\nEX 80,40\nMaav 108,180,76\nRa 164,24,41984\nMaav 104,180,72\nWL 6\nEX 81,41\nMaav 112,184,72\nRa 168,24,43008\nAD 84,84,80\nMaav 116,184,76\n"
"D1 1,16,46,2048\nMaav 124,188,76\nRa 172,24,44032\nMaav 120,188,72\nAD 84,84,81\nCV 66,80,81\nCvav 48,128,192,4\nRa 176,26,40960\nMvav 48,132,200\nEX 82,42\nMvav 48,136,208\n"
"Ra 180,26,41984\nMvav 48,140,216\nWL 6\nEX 83,43\nCvav 52,140,220,8\nRa 184,26,43008\nAD 84,84,82\nMvav 52,136,212\nD1 1,16,46,3072\ns_add_u32 m0, s89, 0\nMvav 52,132,204\n"
"Ra 188,26,44032\nMvav 52,128,196\nAD 84,84,83\nCV 67,82,83\nMvav 52,144,228\nRa 128,2,49152\nMvav 52,148,236\ns_add_u32 s46, s86, s88\nEX 80,44\nMvav 52,152,244\nRa 132,2,50176\n"
"v_xor_b32 v24, 0x10000, v24\nMvav 52,156,252\nWL 6\nEX 81,45\nMvav 48,156,248\nRa 136,2,51200\nAD 85,85,80\nMvav 48,152,240\nD1 1,16,46,0\nMvav 48,148,232\nRa 140,2,52224\n"
"Sv 112,1,16,49,1024\nMvav 48,144,224\nAD 85,85,81\nCV 70,80,81\nCvav 56,160,192,12\nRa 144,2,53248\nMvav 56,164,200\ns_add_u32 s49, s49, 131072\nEX 82,46\nMvav 56,168,208\n"
"Ra 148,2,54272\nv_xor_b32 v26, 0x10000, v26\nMvav 56,172,216\nWL 6\nEX 83,47\nCvav 60,172,220,16\nRa 152,2,55296\nAD 85,85,82\nMvav 60,168,212\nD1 1,16,46,1024\nMvav 60,164,204\n"
"Ra 156,2,56320\nMvav 60,160,196\nAD 85,85,83\nCV 71,82,83\nMvav 60,176,228\nRa 160,2,57344\nv_max_f32 v90, v84, v85\nMvav 60,180,236\ns_add_u32 s88, s88, 32768\nEX 80,48\n"
"Mvav 60,184,244\nRa 164,2,58368\nv_cmp_lt_f32 vcc, s63, v90\nMvav 60,188,252\nWL 8\nEX 81,49\nMvav 56,188,248\nRa 168,2,59392\ns_cbranch_vccnz .T65\n.T66:\nMvav 56,184,240\n"
"D1 1,16,46,2048\nMvav 56,180,232\nRa 172,2,60416\nAD 86,86,84\nMvav 56,176,224\nAD 84,80,81\nCV 72,80,81\nMaav 0,128,64\nRa 176,2,61440\nMaav 4,128,68\nEX 82,50\nMaav 12,132,68\n"
"Ra 180,2,62464\nMaav 8,132,64\ns_waitcnt vmcnt(13) lgkmcnt(8)\nEX 83,51\nMaav 16,136,64\nRa 184,2,63488\nAD 84,84,82\nMaav 20,136,68\nD1 1,16,46,3072\ns_add_u32 m0, s89, 16384\n"
"Maav 28,140,68\nRa 188,2,64512\ns_barrier\nMaav 24,140,64\nAD 84,84,83\nCV 73,82,83\nMaav 32,144,64\nRa 128,24,0\nMaav 36,144,68\ns_add_u32 s46, s87, s88\nEX 80,52\n"
"Maav 44,148,68\nRa 132,24,1024\nv_xor_b32 v2, 0x10000, v2\nMaav 40,148,64\nWL 8\nEX 81,53\nMaav 48,152,64\nRa 136,24,2048\nAD 87,87,85\nMaav 52,152,68\nD1 1,16,46,0\n"
"Maav 60,156,68\nRa 140,24,3072\nMaav 56,156,64\nAD 85,80,81\nCV 76,80,81\nMaav 64,160,64\nRa 144,26,0\nMaav 68,160,68\nEX 82,54\nMaav 76,164,68\nRa 148,26,1024\nMaav 72,164,64\n"
"WL 8\nEX 83,55\nMaav 80,168,64\nRa 152,26,2048\nAD 85,85,82\nMaav 84,168,68\nD1 1,16,46,1024\nMaav 92,172,68\nRa 156,26,3072\nMaav 88,172,64\nAD 85,85,83\nCV 77,82,83\n"
"Maav 96,176,64\nRa 160,24,8192\nMaav 100,176,68\ns_sub_u32 s90, s90, 1\nEX 80,56\nMaav 108,180,68\nRa 164,24,9216\ns_cbranch_scc1 .E61\n.E62:\nMaav 104,180,64\nWL 6\nEX 81,57\n"
"Maav 112,184,64\nRa 168,24,10240\nAD 84,84,80\nMaav 116,184,68\nD1 1,16,46,2048\ns_sub_u32 s99, s99, 1\nMaav 124,188,68\nRa 172,24,11264\nMaav 120,188,64\nAD 84,84,81\n"
"CV 74,80,81\ns_cbranch_scc0 .A59\ns_branch .A67\n.E61:\ns_swappc_b64 s[80:81], s[8:9]\ns_branch .E62\n.HD13:\nbuffer_store_dword v25, off, s[16:19], s54 offset:4 sc0 sc1\n"
"v_cmp_ne_u32 vcc, s15, v27\ns_nop 0\ns_cbranch_vccnz .S46\n.S47:\nv_and_b32 v27, 0x70, v1\nbuffer_load_dword v27, v27, s[16:19], s61 offen offset:8 sc0 sc1\ns_mov_b32 s90, 1\n"
"s_swappc_b64 s[8:9], s[80:81]\nv_cmp_ne_u32 vcc, s15, v27\ns_nop 0\ns_cbranch_vccnz .S49\n.S50:\nv_and_b32 v27, 0xf0, v1\n"
"buffer_load_dword v27, v27, s[16:19], s61 offen offset:4 sc0 sc1\ns_mov_b32 s90, 1\ns_swappc_b64 s[8:9], s[80:81]\nv_cmp_ne_u32 vcc, s15, v27\ns_nop 0\ns_cbranch_vccnz .S52\n"
".S53:\ns_mov_b32 s90, s91\ns_swappc_b64 s[8:9], s[80:81]\ns_branch .FF55\n.A67:\nCvav 32,128,192,4\nRa 176,26,8192\nMvav 32,132,200\nEX 82,58\nMvav 32,136,208\nRa 180,26,9216\n"
"Mvav 32,140,216\nWL 6\nEX 83,59\nCvav 36,140,220,8\nRa 184,26,10240\nAD 84,84,82\nMvav 36,136,212\nD1 1,16,46,3072\ns_add_u32 m0, s89, 32768\nMvav 36,132,204\nRa 188,26,11264\n"
"Mvav 36,128,196\nAD 84,84,83\nCV 75,82,83\ns_mov_b32 s94, s96\ns_add_u32 s95, s96, 65536\nv_and_b32 v108, 0xf0, v1\nMvav 36,144,228\nRa 128,2,16384\nMvav 36,148,236\n"
"s_add_u32 s46, s86, s88\nEX 80,60\nMvav 36,152,244\nRa 132,2,17408\nMvav 36,156,252\nWL 6\nEX 81,61\nMvav 32,156,248\nRa 136,2,18432\nAD 85,85,80\nMvav 32,152,240\nD1 1,16,46,0\n"
"Mvav 32,148,232\nRa 140,2,19456\nSv 112,1,16,49,0\nMvav 32,144,224\nAD 85,85,81\nCV 78,80,81\nbuffer_load_dwordx4 v[100:103], v108, s[16:19], s65 offen offset:0 sc0 sc1\n"
"buffer_load_dwordx4 v[104:107], v108, s[16:19], s65 offen offset:256 sc0 sc1\nCvav 40,160,192,12\nRa 144,2,20480\nMvav 40,164,200\nEX 82,62\nMvav 40,168,208\nRa 148,2,21504\n"
"Mvav 40,172,216\nWL 6\nEX 83,63\nCvav 44,172,220,16\nRa 152,2,22528\nAD 85,85,82\nMvav 44,168,212\nD1 1,16,46,1024\nMvav 44,164,204\nRa 156,2,23552\nMvav 44,160,196\nAD 85,85,83\n"
"CV 79,82,83\nMvav 44,176,228\nRa 160,2,24576\nv_max_f32 v90, v84, v85\nMvav 44,180,236\ns_add_u32 s88, s88, 32768\nEX 80,32\nMvav 44,184,244\nRa 164,2,25600\n"
"v_cmp_lt_f32 vcc, s63, v90\nMvav 44,188,252\nWL 8\nEX 81,33\nMvav 40,188,248\nRa 168,2,26624\ns_cbranch_vccnz .T69\n.T70:\nMvav 40,184,240\nD1 1,16,46,2048\nMvav 40,180,232\n"
"Ra 172,2,27648\nAD 86,86,84\nMvav 40,176,224\nAD 84,80,81\nCV 64,80,81\nMaav 0,128,72\nRa 176,2,28672\nMaav 4,128,76\nEX 82,34\nMaav 12,132,76\nRa 180,2,29696\nMaav 8,132,72\n"
"s_waitcnt vmcnt(15) lgkmcnt(8)\nEX 83,35\nMaav 16,136,72\nRa 184,2,30720\nAD 84,84,82\nMaav 20,136,76\nD1 1,16,46,3072\ns_add_u32 m0, s89, 49152\nMaav 28,140,76\nRa 188,2,31744\n"
"s_barrier\nMaav 24,140,72\nAD 84,84,83\nCV 65,82,83\nMaav 32,144,72\nRa 128,24,32768\nMaav 36,144,76\ns_add_u32 s46, s87, s88\nEX 80,36\nMaav 44,148,76\nRa 132,24,33792\n"
"Maav 40,148,72\nWL 8\nEX 81,37\nMaav 48,152,72\nRa 136,24,34816\nAD 87,87,85\nMaav 52,152,76\nD1 1,16,46,0\nMaav 60,156,76\nRa 140,24,35840\nMaav 56,156,72\nAD 85,80,81\n"
"CV 68,80,81\nMaav 64,160,72\nRa 144,26,32768\nMaav 68,160,76\nEX 82,38\nMaav 76,164,76\nRa 148,26,33792\nMaav 72,164,72\nWL 8\nEX 83,39\nMaav 80,168,72\nRa 152,26,34816\n"
"AD 85,85,82\nMaav 84,168,76\nD1 1,16,46,1024\nMaav 92,172,76\nRa 156,26,35840\nMaav 88,172,72\nAD 85,85,83\nCV 69,82,83\nMaav 96,176,72\nRa 160,24,40960\nMaav 100,176,76\n"
"s_xor_b32 s89, s89, 0x10000\nEX 80,40\nMaav 108,180,76\nRa 164,24,41984\nMaav 104,180,72\nWL 6\nEX 81,41\nMaav 112,184,72\nRa 168,24,43008\nAD 84,84,80\nMaav 116,184,76\n"
"D1 1,16,46,2048\nMaav 124,188,76\nRa 172,24,44032\nMaav 120,188,72\nAD 84,84,81\nCV 66,80,81\nCvav 48,128,192,4\nRa 176,26,40960\nMvav 48,132,200\ns_mov_b32 s86, s94\nEX 82,42\n"
"Mvav 48,136,208\nRa 180,26,41984\nMvav 48,140,216\nWL 6\nEX 83,43\nCvav 52,140,220,8\nRa 184,26,43008\nAD 84,84,82\nMvav 52,136,212\nD1 1,16,46,3072\ns_add_u32 m0, s89, 0\n"
"Mvav 52,132,204\nRa 188,26,44032\nMvav 52,128,196\nAD 84,84,83\nCV 67,82,83\nMvav 52,144,228\nRa 128,2,49152\nMvav 52,148,236\ns_add_u32 s46, s86, s88\nEX 80,44\nMvav 52,152,244\n"
"Ra 132,2,50176\nv_xor_b32 v24, 0x10000, v24\nMvav 52,156,252\nWL 6\nEX 81,45\nMvav 48,156,248\nRa 136,2,51200\nAD 85,85,80\nMvav 48,152,240\nD1 1,16,46,0\nMvav 48,148,232\n"
"Ra 140,2,52224\nSv 112,1,16,49,1024\nMvav 48,144,224\nAD 85,85,81\nCV 70,80,81\nWV 9\nv_xor_b32 v109, s15, v102\nv_xor_b32 v110, s15, v106\nv_or_b32 v109, v109, v110\n"
"v_cmp_ne_u32 vcc, 0, v109\ns_nop 3\ns_cmp_lg_u64 vcc, 0\ns_cselect_b32 s67, 1, 0\ns_cselect_b32 s6, 0x40000000, s66\nCvav 56,160,192,12\nRa 144,2,53248\ns_add_u32 s5, s6, 0\n"
"Mvav 56,164,200\ns_add_u32 s49, s49, 131072\nEX 82,46\nMvav 56,168,208\nRa 148,2,54272\nv_xor_b32 v26, 0x10000, v26\n"
"buffer_load_dwordx4 a[192:195], v1, s[16:19], s5 offen offset:0 sc1\nMvav 56,172,216\nWL 6\nEX 83,47\nCvav 60,172,220,16\nRa 152,2,55296\nAD 85,85,82\nMvav 60,168,212\n"
"D1 1,16,46,1024\nMvav 60,164,204\nRa 156,2,56320\nMvav 60,160,196\nAD 85,85,83\nCV 71,82,83\nMvav 60,176,228\nRa 160,2,57344\nv_max_f32 v90, v84, v85\nMvav 60,180,236\n"
"s_add_u32 s88, s88, 32768\nEX 80,48\nMvav 60,184,244\nRa 164,2,58368\nv_cmp_lt_f32 vcc, s63, v90\nMvav 60,188,252\nWL 8\nEX 81,49\nMvav 56,188,248\nRa 168,2,59392\n"
"s_cbranch_vccnz .T71\n.T72:\nbuffer_load_dwordx4 a[196:199], v1, s[16:19], s5 offen offset:1024 sc1\nMvav 56,184,240\nD1 1,16,46,2048\nMvav 56,180,232\nRa 172,2,60416\n"
"AD 86,86,84\nMvav 56,176,224\nAD 84,80,81\nCV 72,80,81\nMaav 0,128,64\nRa 176,2,61440\nMaav 4,128,68\nEX 82,50\nMaav 12,132,68\nRa 180,2,62464\nMaav 8,132,64\nWL 8\nEX 83,51\n"
"Maav 16,136,64\nRa 184,2,63488\nAD 84,84,82\nbuffer_load_dwordx4 a[200:203], v1, s[16:19], s5 offen offset:2048 sc1\nMaav 20,136,68\nD1 1,16,46,3072\ns_add_u32 m0, s89, 16384\n"
"Maav 28,140,68\nRa 188,2,64512\ns_barrier\nMaav 24,140,64\nAD 84,84,83\nCV 73,82,83\nMaav 32,144,64\nRa 128,24,0\nMaav 36,144,68\ns_add_u32 s46, s87, s88\nEX 80,52\n"
"Maav 44,148,68\nRa 132,24,1024\nv_xor_b32 v2, 0x10000, v2\nMaav 40,148,64\nWL 8\nEX 81,53\nMaav 48,152,64\nRa 136,24,2048\nAD 87,87,85\n"
"buffer_load_dwordx4 a[204:207], v1, s[16:19], s5 offen offset:3072 sc1\nMaav 52,152,68\nD1 1,16,46,0\nMaav 60,156,68\nRa 140,24,3072\nMaav 56,156,64\nAD 85,80,81\nCV 76,80,81\n"
"Maav 64,160,64\nRa 144,26,0\nMaav 68,160,68\nEX 82,54\nMaav 76,164,68\nRa 148,26,1024\ns_add_u32 s5, s6, 4096\nMaav 72,164,64\nWL 8\nEX 83,55\nMaav 80,168,64\nRa 152,26,2048\n"
"AD 85,85,82\nbuffer_load_dwordx4 a[208:211], v1, s[16:19], s5 offen offset:0 sc1\nMaav 84,168,68\nD1 1,16,46,1024\nMaav 92,172,68\nRa 156,26,3072\nMaav 88,172,64\nAD 85,85,83\n"
"CV 77,82,83\nMaav 96,176,64\nRa 160,24,8192\nMaav 100,176,68\nEX 80,56\nMaav 108,180,68\nRa 164,24,9216\nMaav 104,180,64\nWL 6\nEX 81,57\nMaav 112,184,64\nRa 168,24,10240\n"
"AD 84,84,80\nbuffer_load_dwordx4 a[212:215], v1, s[16:19], s5 offen offset:1024 sc1\nMaav 116,184,68\nD1 1,16,46,2048\nMaav 124,188,68\nRa 172,24,11264\nMaav 120,188,64\n"
"AD 84,84,81\nCV 74,80,81\nCvav 32,128,192,4\nRa 176,26,8192\nMvav 32,132,200\nEX 82,58\nMvav 32,136,208\nRa 180,26,9216\nMvav 32,140,216\nWL 6\nEX 83,59\nCvav 36,140,220,8\n"
"Ra 184,26,10240\nAD 84,84,82\nbuffer_load_dwordx4 a[216:219], v1, s[16:19], s5 offen offset:2048 sc1\nMvav 36,136,212\nD1 1,16,46,3072\ns_add_u32 m0, s89, 32768\nMvav 36,132,204\n"
"Ra 188,26,11264\nMvav 36,128,196\nAD 84,84,83\nCV 75,82,83\nMvav 36,144,228\nRa 128,2,16384\nMvav 36,148,236\ns_add_u32 s46, s86, s88\nEX 80,60\nMvav 36,152,244\nRa 132,2,17408\n"
"Mvav 36,156,252\nWL 6\nEX 81,61\nMvav 32,156,248\nRa 136,2,18432\nAD 85,85,80\nbuffer_load_dwordx4 a[220:223], v1, s[16:19], s5 offen offset:3072 sc1\nMvav 32,152,240\n"
"D1 1,16,46,0\nMvav 32,148,232\nRa 140,2,19456\nMvav 32,144,224\nAD 85,85,81\nCV 78,80,81\nCvav 40,160,192,12\nRa 144,2,20480\nMvav 40,164,200\ns_mov_b32 s87, s95\nEX 82,62\n"
"Mvav 40,168,208\nRa 148,2,21504\ns_add_u32 s5, s6, 16384\nMvav 40,172,216\nWL 6\nEX 83,63\nCvav 44,172,220,16\nRa 152,2,22528\nAD 85,85,82\n"
"buffer_load_dwordx4 a[224:227], v1, s[16:19], s5 offen offset:0 sc1\nMvav 44,168,212\nD1 1,16,46,1024\nMvav 44,164,204\nRa 156,2,23552\nMvav 44,160,196\nAD 85,85,83\nCV 79,82,83\n"
"Mvav 44,176,228\nRa 160,2,24576\nv_max_f32 v90, v84, v85\nMvav 44,180,236\ns_add_u32 s88, s88, 32768\nEX 80,32\nMvav 44,184,244\nRa 164,2,25600\nv_cmp_lt_f32 vcc, s63, v90\n"
"Mvav 44,188,252\nWL 8\nEX 81,33\nMvav 40,188,248\nRa 168,2,26624\ns_cbranch_vccnz .T74\n.T75:\nbuffer_load_dwordx4 a[228:231], v1, s[16:19], s5 offen offset:1024 sc1\n"
"Mvav 40,184,240\nD1 1,16,46,2048\nMvav 40,180,232\nRa 172,2,27648\nAD 86,86,84\nMvav 40,176,224\nAD 84,80,81\nCV 64,80,81\nMaav 0,128,72\nRa 176,2,28672\nMaav 4,128,76\nEX 82,34\n"
"Maav 12,132,76\nRa 180,2,29696\nMaav 8,132,72\ns_waitcnt vmcnt(22) lgkmcnt(8)\nEX 83,35\nMaav 16,136,72\nRa 184,2,30720\nAD 84,84,82\n"
"buffer_load_dwordx4 a[232:235], v1, s[16:19], s5 offen offset:2048 sc1\nMaav 20,136,76\nD1 1,16,46,3072\ns_add_u32 m0, s89, 49152\nMaav 28,140,76\nRa 188,2,31744\ns_barrier\n"
"Maav 24,140,72\nAD 84,84,83\nCV 65,82,83\nMaav 32,144,72\nRa 128,24,32768\nMaav 36,144,76\ns_add_u32 s46, s87, s88\nEX 80,36\nMaav 44,148,76\nRa 132,24,33792\nMaav 40,148,72\n"
"WL 8\nEX 81,37\nMaav 48,152,72\nRa 136,24,34816\nAD 87,87,85\nbuffer_load_dwordx4 a[236:239], v1, s[16:19], s5 offen offset:3072 sc1\nMaav 52,152,76\nD1 1,16,46,0\nMaav 60,156,76\n"
"Ra 140,24,35840\nMaav 56,156,72\nAD 85,80,81\nCV 68,80,81\nMaav 64,160,72\nRa 144,26,32768\nMaav 68,160,76\nEX 82,38\nMaav 76,164,76\nRa 148,26,33792\ns_add_u32 s5, s6, 20480\n"
"Maav 72,164,72\nWL 8\nEX 83,39\nMaav 80,168,72\nRa 152,26,34816\nAD 85,85,82\nbuffer_load_dwordx4 a[240:243], v1, s[16:19], s5 offen offset:0 sc1\nMaav 84,168,76\nD1 1,16,46,1024\n"
"Maav 92,172,76\nRa 156,26,35840\nMaav 88,172,72\nAD 85,85,83\nCV 69,82,83\nMaav 96,176,72\nRa 160,24,40960\nMaav 100,176,76\ns_xor_b32 s89, s89, 0x10000\nEX 80,40\n"
"Maav 108,180,76\nRa 164,24,41984\nMaav 104,180,72\nWL 6\nEX 81,41\nMaav 112,184,72\nRa 168,24,43008\nAD 84,84,80\n"
"buffer_load_dwordx4 a[244:247], v1, s[16:19], s5 offen offset:1024 sc1\nMaav 116,184,76\nD1 1,16,46,2048\nMaav 124,188,76\nRa 172,24,44032\nMaav 120,188,72\nAD 84,84,81\n"
"CV 66,80,81\nCvav 48,128,192,4\nRa 176,26,40960\nMvav 48,132,200\nEX 82,42\nMvav 48,136,208\nRa 180,26,41984\nMvav 48,140,216\nWL 6\nEX 83,43\nCvav 52,140,220,8\nRa 184,26,43008\n"
"AD 84,84,82\nbuffer_load_dwordx4 a[248:251], v1, s[16:19], s5 offen offset:2048 sc1\nMvav 52,136,212\nD1 1,16,46,3072\ns_add_u32 m0, s89, 0\nMvav 52,132,204\nRa 188,26,44032\n"
"Mvav 52,128,196\nAD 84,84,83\nCV 67,82,83\nMvav 52,144,228\nRa 128,2,49152\nMvav 52,148,236\ns_add_u32 s46, s86, s88\nEX 80,44\nMvav 52,152,244\nRa 132,2,50176\n"
"v_xor_b32 v24, 0x10000, v24\nMvav 52,156,252\nWL 6\nEX 81,45\nMvav 48,156,248\nRa 136,2,51200\nAD 85,85,80\nbuffer_load_dwordx4 a[252:255], v1, s[16:19], s5 offen offset:3072 sc1\n"
"Mvav 48,152,240\nD1 1,16,46,0\nMvav 48,148,232\nRa 140,2,52224\nMvav 48,144,224\nAD 85,85,81\nCV 70,80,81\nCvav 56,160,192,12\nRa 144,2,53248\nMvav 56,164,200\nEX 82,46\n"
"Mvav 56,168,208\nRa 148,2,54272\nv_xor_b32 v26, 0x10000, v26\nMvav 56,172,216\nWL 6\nEX 83,47\nCvav 60,172,220,16\nRa 152,2,55296\nAD 85,85,82\nMvav 60,168,212\nD1 1,16,46,1024\n"
"Mvav 60,164,204\nRa 156,2,56320\nMvav 60,160,196\nAD 85,85,83\nCV 71,82,83\nMvav 60,176,228\nRa 160,2,57344\nv_max_f32 v90, v84, v85\nMvav 60,180,236\ns_add_u32 s88, s88, 32768\n"
"EX 80,48\nMvav 60,184,244\nRa 164,2,58368\nv_cmp_lt_f32 vcc, s63, v90\nMvav 60,188,252\nWL 8\nEX 81,49\nMvav 56,188,248\nRa 168,2,59392\ns_cbranch_vccnz .T76\n.T77:\n"
"Mvav 56,184,240\nD1 1,16,46,2048\nMvav 56,180,232\nRa 172,2,60416\nAD 86,86,84\nMvav 56,176,224\nAD 84,80,81\nCV 72,80,81\nMaav 0,128,64\nRa 176,2,61440\n"
"buffer_load_dwordx4 v[100:103], v108, s[16:19], s65 offen offset:0 sc0 sc1\nbuffer_load_dwordx4 v[104:107], v108, s[16:19], s65 offen offset:256 sc0 sc1\nMaav 4,128,68\nEX 82,50\n"
"Maav 12,132,68\nRa 180,2,62464\nMaav 8,132,64\ns_waitcnt vmcnt(22) lgkmcnt(8)\nEX 83,51\nMaav 16,136,64\nRa 184,2,63488\nAD 84,84,82\nMaav 20,136,68\nD1 1,16,46,3072\n"
"s_add_u32 m0, s89, 16384\nMaav 28,140,68\nRa 188,2,64512\ns_barrier\nMaav 24,140,64\nAD 84,84,83\nCV 73,82,83\nMaav 32,144,64\nRa 128,24,0\nMaav 36,144,68\n"
"s_add_u32 s46, s87, s88\nEX 80,52\nMaav 44,148,68\nRa 132,24,1024\nv_xor_b32 v2, 0x10000, v2\nMaav 40,148,64\nWL 8\nEX 81,53\nMaav 48,152,64\nRa 136,24,2048\nAD 87,87,85\n"
"Maav 52,152,68\nD1 1,16,46,0\nMaav 60,156,68\nRa 140,24,3072\nMaav 56,156,64\nAD 85,80,81\nCV 76,80,81\nMaav 64,160,64\nRa 144,26,0\nMaav 68,160,68\nEX 82,54\nMaav 76,164,68\n"
"Ra 148,26,1024\nMaav 72,164,64\nWL 8\nEX 83,55\nMaav 80,168,64\nRa 152,26,2048\nAD 85,85,82\nMaav 84,168,68\nD1 1,16,46,1024\nMaav 92,172,68\nRa 156,26,3072\nMaav 88,172,64\n"
"AD 85,85,83\nCV 77,82,83\nMaav 96,176,64\nRa 160,24,8192\nMaav 100,176,68\nEX 80,56\nMaav 108,180,68\nRa 164,24,9216\nMaav 104,180,64\nWL 10\nEX 81,57\nMaav 112,184,64\n"
"Ra 168,24,10240\nAD 84,84,80\nMaav 116,184,68\nD1 1,16,46,2048\nMaav 124,188,68\nRa 172,24,11264\nMaav 120,188,64\nAD 84,84,81\nCV 74,80,81\nWV 4\ns_cmp_lg_u32 s67, 0\n"
"s_cbranch_scc0 .E85\nv_xor_b32 v109, s15, v102\nv_xor_b32 v110, s15, v106\nv_or_b32 v109, v109, v110\nv_cmp_ne_u32 vcc, 0, v109\ns_nop 0\ns_cbranch_vccnz .S82\n.S83:\n"
"s_add_u32 s5, s66, 0\nbuffer_load_dwordx4 a[192:195], v1, s[16:19], s5 offen offset:0 sc1\nbuffer_load_dwordx4 a[196:199], v1, s[16:19], s5 offen offset:1024 sc1\n"
"buffer_load_dwordx4 a[200:203], v1, s[16:19], s5 offen offset:2048 sc1\nbuffer_load_dwordx4 a[204:207], v1, s[16:19], s5 offen offset:3072 sc1\ns_add_u32 s5, s66, 4096\n"
"buffer_load_dwordx4 a[208:211], v1, s[16:19], s5 offen offset:0 sc1\nbuffer_load_dwordx4 a[212:215], v1, s[16:19], s5 offen offset:1024 sc1\n"
"buffer_load_dwordx4 a[216:219], v1, s[16:19], s5 offen offset:2048 sc1\nbuffer_load_dwordx4 a[220:223], v1, s[16:19], s5 offen offset:3072 sc1\ns_add_u32 s5, s66, 16384\n"
"buffer_load_dwordx4 a[224:227], v1, s[16:19], s5 offen offset:0 sc1\nbuffer_load_dwordx4 a[228:231], v1, s[16:19], s5 offen offset:1024 sc1\n"
"buffer_load_dwordx4 a[232:235], v1, s[16:19], s5 offen offset:2048 sc1\nbuffer_load_dwordx4 a[236:239], v1, s[16:19], s5 offen offset:3072 sc1\ns_add_u32 s5, s66, 20480\n"
"buffer_load_dwordx4 a[240:243], v1, s[16:19], s5 offen offset:0 sc1\nbuffer_load_dwordx4 a[244:247], v1, s[16:19], s5 offen offset:1024 sc1\n"
"buffer_load_dwordx4 a[248:251], v1, s[16:19], s5 offen offset:2048 sc1\nbuffer_load_dwordx4 a[252:255], v1, s[16:19], s5 offen offset:3072 sc1\nWV 0\ns_mov_b32 s67, 0\n.E85:\n"
"v_cmp_lt_f32 vcc, 0, v101\nSU 20,100,88\nv_mul_f32 v101, 0x3e800000, v101\nv_cndmask_b32 v20, 0, v20, vcc\nv_cmp_lt_f32 vcc, 0, v105\nSU 21,104,89\n"
"v_mul_f32 v105, 0x3e800000, v105\nv_cndmask_b32 v21, 0, v21, vcc\nv_or_b32 v91, v20, v21\nv_cmp_ne_u32 vcc, 0, v91\ns_nop 0\ns_cbranch_vccnz .D80\nAD 86,86,101\nAD 87,87,105\n"
".D81:\nMava 0,130,192\nMava 8,128,192\nEX 82,58\nMava 12,128,196\nMava 4,130,196\nEX 83,59\nMava 16,130,200\nAD 84,84,82\nMava 24,128,200\nD1 1,16,46,3072\nMava 28,128,204\n"
"s_add_u32 m0, s89, 32768\ns_add_u32 s46, s86, s88\nMava 20,130,204\nAD 84,84,83\nCV 75,82,83\nMava 32,130,208\nRa 128,2,16384\nv_xor_b32 v3, 0x10000, v2\n"
"v_add_u32 v0, 0x20000, v1\nMava 40,128,208\nEX 80,60\nMava 44,128,212\nRa 132,2,17408\ns_sub_u32 s94, s49, 1835008\nMava 36,130,212\nEX 81,61\nMava 48,130,216\nRa 136,2,18432\n"
"AD 85,85,80\ns_xor_b32 s92, s94, 0x202000\nMava 56,128,216\nD1 1,16,46,0\ns_lshl_b32 s89, s11, 12\nMava 60,128,220\nRa 140,2,19456\ns_lshl1_add_u32 s2, s12, 0\nMava 52,130,220\n"
"AD 85,85,81\nCV 78,80,81\nMava 64,130,224\nRa 144,2,20480\ns_lshl3_add_u32 s2, s2, s13\ns_lshl4_add_u32 s2, s2, 0xc048200\nMava 72,128,224\nEX 82,62\nMava 76,128,228\n"
"Ra 148,2,21504\ns_and_b32 s3, s72, 0x2000\nMava 68,130,228\nEX 83,63\nMava 80,130,232\nRa 152,2,22528\nAD 85,85,82\nMava 88,128,232\nD1 1,16,46,1024\ns_cmp_eq_u32 s11, 1\n"
"s_cselect_b32 s54, s2, s54\ns_cselect_b32 s3, s3, 0\nMava 92,128,236\nRa 156,2,23552\nv_xor_b32 v25, s3, v25\nMava 84,130,236\nAD 85,85,83\nCV 79,82,83\nMava 96,130,240\n"
"Ra 160,2,24576\nv_max_f32 v90, v84, v85\nMava 104,128,240\nv_cmp_lt_f32 vcc, s63, v90\nMava 108,128,244\nRa 164,2,25600\nMava 100,130,244\nWL 8\nMava 112,130,248\nRa 168,2,26624\n"
"Mava 120,128,248\nD1 1,16,46,2048\nMava 124,128,252\nRa 172,2,27648\nMava 116,130,252\ns_cbranch_vccnz .T86\n.T87:\nAD 86,86,84\nAD 87,87,85\nMaav 0,128,72\nRa 176,2,28672\n"
"v_mov_b32 v84, v86\nv_mov_b32 v85, v87\nMaav 4,128,76\nv_permlane32_swap_b32 v86, v84\nv_permlane32_swap_b32 v87, v85\nMaav 12,132,76\nRa 180,2,29696\nAD 86,86,84\nMaav 8,132,72\n"
"WL 8\nAD 87,87,85\nMaav 16,136,72\nRa 184,2,30720\nv_mov_b32 v84, v86\nv_mov_b32 v85, v87\nMaav 20,136,76\nD1 1,16,46,3072\nv_permlane16_swap_b32 v86, v84\nMaav 28,140,76\n"
"Ra 188,2,31744\nv_permlane16_swap_b32 v87, v85\nMaav 24,140,72\nAD 86,86,84\nAD 87,87,85\nMaav 32,144,72\nRa 128,2,0\nv_rcp_f32 v86, v86\nMaav 36,144,76\nv_rcp_f32 v87, v87\n"
"Maav 44,148,76\nRa 132,2,1024\nAR 82,0\nAR 83,1\nMU 82,82,86\nMU 83,83,86\nMaav 40,148,72\nWL 8\ns_add_u32 m0, s89, 131072\ns_add_u32 s46, s96, 1212416\nCV 96,82,83\nAR 82,2\n"
"Maav 48,152,72\nRa 136,2,2048\nAR 83,3\nMU 82,82,86\nMU 83,83,86\nCV 97,82,83\nMaav 52,152,76\nD1 1,16,46,0\nAR 82,4\nAR 83,5\nMU 82,82,87\nMaav 60,156,76\nRa 140,2,3072\n"
"MU 83,83,87\nCV 100,82,83\nAR 82,6\nAR 83,7\nMaav 56,156,72\nMU 82,82,87\nMU 83,83,87\nCV 101,82,83\nAR 82,8\nAR 83,9\nMaav 64,160,72\nRa 144,2,4096\nMU 82,82,86\nMU 83,83,86\n"
"CV 98,82,83\nAR 82,10\nMaav 68,160,76\nAR 83,11\nMU 82,82,86\nMU 83,83,86\nCV 99,82,83\nMaav 76,164,76\nRa 148,2,5120\nAR 82,12\nAR 83,13\nMU 82,82,87\nMU 83,83,87\n"
"Maav 72,164,72\nWL 8\nCV 102,82,83\nAR 82,14\nAR 83,15\nMU 82,82,87\nMaav 80,168,72\nRa 152,2,6144\nMU 83,83,87\nCV 103,82,83\nAR 82,16\nAR 83,17\nMaav 84,168,76\nD1 1,16,46,1024\n"
"MU 82,82,86\nMU 83,83,86\nCV 104,82,83\nMaav 92,172,76\nRa 156,2,7168\nAR 82,18\nAR 83,19\nMU 82,82,86\nMU 83,83,86\nMaav 88,172,72\nCV 105,82,83\nAR 82,20\nAR 83,21\nMU 82,82,87\n"
"MU 83,83,87\nMaav 96,176,72\nRa 160,2,8192\nCV 108,82,83\nAR 82,22\nAR 83,23\nMU 82,82,87\nMaav 100,176,76\nMU 83,83,87\nCV 109,82,83\nAR 82,24\nAR 83,25\nMaav 108,180,76\n"
"Ra 164,2,9216\nMU 82,82,86\nMU 83,83,86\nCV 106,82,83\nAR 82,26\nMaav 104,180,72\nWL 8\nAR 83,27\nMU 82,82,86\nMU 83,83,86\nCV 107,82,83\nMaav 112,184,72\nRa 168,2,10240\n"
"AR 82,28\nAR 83,29\nMU 82,82,87\nMU 83,83,87\nMaav 116,184,76\nD1 1,16,46,2048\nCV 110,82,83\nAR 82,30\nAR 83,31\nMaav 124,188,76\nRa 172,2,11264\nMU 82,82,87\nMU 83,83,87\n"
"CV 111,82,83\nMaav 120,188,72\nAR 82,32\nAR 83,33\nMU 82,82,86\nMU 83,83,86\nZvav 128,128,96\nRa 176,2,12288\nCV 112,82,83\nAR 82,34\nAR 83,35\nMU 82,82,86\nMvav 128,132,104\n"
"MU 83,83,86\nCV 113,82,83\nAR 82,36\nAR 83,37\nZvav 132,132,108\nRa 180,2,13312\nMU 82,82,87\nMU 83,83,87\nCV 116,82,83\nMvav 132,128,100\nWL 8\nAR 82,38\nAR 83,39\nMU 82,82,87\n"
"Zvav 140,136,100\nRa 184,2,14336\nMU 83,83,87\nCV 117,82,83\nAR 82,40\nMvav 140,140,108\nD1 1,16,46,3072\nAR 83,41\nMU 82,82,86\nMU 83,83,86\nZvav 136,140,104\nRa 188,2,15360\n"
"s_barrier\nCV 114,82,83\nAR 82,42\nAR 83,43\nMvav 136,136,96\nMU 82,82,86\nMU 83,83,86\nCV 115,82,83\nAR 82,44\nZvav 144,144,96\nRa 128,2,32768\nAR 83,45\nMU 82,82,87\n"
"MU 83,83,87\nMvav 144,148,104\nCV 118,82,83\nAR 82,46\nAR 83,47\nMU 82,82,87\nbuffer_store_dword v25, off, s[16:19], s54 offset:512 sc0 sc1\nZvav 148,148,108\nRa 132,2,33792\n"
"MU 83,83,87\nCV 119,82,83\nAR 82,48\nMvav 148,144,100\nWL 8\ns_add_u32 m0, s89, 114688\ns_add_u32 s46, s96, 1179648\nAR 83,49\nMU 82,82,86\nZvav 156,152,100\nRa 136,2,34816\n"
"MU 83,83,86\nCV 120,82,83\nAR 82,50\nMvav 156,156,108\nD1 1,16,46,0\nAR 83,51\nMU 82,82,86\nMU 83,83,86\nZvav 152,156,104\nRa 140,2,35840\nCV 121,82,83\nAR 82,52\nAR 83,53\n"
"Mvav 152,152,96\nMU 82,82,87\nMU 83,83,87\nCV 124,82,83\nAR 82,54\nZvav 64,160,96\nRa 144,2,36864\nAR 83,55\nMU 82,82,87\nMU 83,83,87\nCV 125,82,83\nMvav 64,164,104\nAR 82,56\n"
"AR 83,57\nMU 82,82,86\nMU 83,83,86\nZvav 68,164,108\nRa 148,2,37888\nCV 122,82,83\nAR 82,58\nAR 83,59\nMvav 68,160,100\nWL 8\nMU 82,82,86\nMU 83,83,86\nCV 123,82,83\n"
"Zvav 76,168,100\nRa 152,2,38912\nAR 82,60\nAR 83,61\nMU 82,82,87\nMvav 76,172,108\nD1 1,16,46,1024\nMU 83,83,87\nCV 126,82,83\nAR 82,62\nZvav 72,172,104\nRa 156,2,39936\nAR 83,63\n"
"MU 82,82,87\nMU 83,83,87\nCV 127,82,83\nMvav 72,168,96\nAR 82,64\nAR 83,65\nMU 82,82,86\nMU 83,83,86\nMvav 128,176,112\nRa 160,2,40960\nCV 32,82,83\nAR 82,66\nAR 83,67\n"
"Mvav 128,180,120\nMU 82,82,86\nMU 83,83,86\nCV 33,82,83\nAR 82,68\nMvav 132,180,124\nRa 164,2,41984\nAR 83,69\nMU 82,82,87\nMU 83,83,87\nMvav 132,176,116\nWL 8\nCV 36,82,83\n"
"AR 82,70\nAR 83,71\nMU 82,82,87\nMvav 140,184,116\nRa 168,2,43008\nMU 83,83,87\nCV 37,82,83\nAR 82,72\nMvav 140,188,124\nD1 1,16,46,2048\nAR 83,73\nMU 82,82,86\nMU 83,83,86\n"
"Mvav 136,188,120\nRa 172,2,44032\nCV 34,82,83\nAR 82,74\nAR 83,75\nMvav 136,184,112\nMU 82,82,86\nMU 83,83,86\nCV 35,82,83\nAR 82,76\nMvav 144,128,112\nRa 176,2,45056\nAR 83,77\n"
"MU 82,82,87\nMU 83,83,87\nCV 38,82,83\nMvav 144,132,120\nAR 82,78\nAR 83,79\nMU 82,82,87\nMU 83,83,87\nMvav 148,132,124\nRa 180,2,46080\nCV 39,82,83\nAR 82,80\nAR 83,81\n"
"Mvav 148,128,116\nWL 8\nMU 82,82,86\nMU 83,83,86\nCV 40,82,83\nMvav 156,136,116\nRa 184,2,47104\nAR 82,82\nAR 83,83\nMU 82,82,86\nMvav 156,140,124\nD1 1,16,46,3072\nMU 83,83,86\n"
"CV 41,82,83\nAR 82,84\nMvav 152,140,120\nRa 188,2,48128\nAR 83,85\nMU 82,82,87\nMU 83,83,87\nCV 44,82,83\nMvav 152,136,112\nAR 82,86\nAR 83,87\nMU 82,82,87\nMU 83,83,87\n"
"Mvav 64,144,112\nRa 128,2,49152\nCV 45,82,83\nAR 82,88\nAR 83,89\nMvav 64,148,120\nMU 82,82,86\nMU 83,83,86\nCV 42,82,83\nAR 82,90\nMvav 68,148,124\nRa 132,2,50176\nAR 83,91\n"
"MU 82,82,86\nMU 83,83,86\nMvav 68,144,116\nWL 8\ns_add_u32 m0, s89, 147456\ns_add_u32 s46, s96, 1245184\nCV 43,82,83\nAR 82,92\nMvav 76,152,116\nRa 136,2,51200\nAR 83,93\n"
"MU 82,82,87\nMU 83,83,87\nMvav 76,156,124\nD1 1,16,46,0\nCV 46,82,83\nAR 82,94\nAR 83,95\nMvav 72,156,120\nRa 140,2,52224\nMU 82,82,87\nMU 83,83,87\nCV 47,82,83\nMvav 72,152,112\n"
"AR 82,96\nAR 83,97\nMU 82,82,86\nMU 83,83,86\nMvav 128,160,32\nRa 144,2,53248\nCV 48,82,83\nAR 82,98\nAR 83,99\nMU 82,82,86\nMvav 128,164,40\nMU 83,83,86\nCV 49,82,83\nAR 82,100\n"
"AR 83,101\nMvav 132,164,44\nRa 148,2,54272\nMU 82,82,87\nMU 83,83,87\nCV 52,82,83\nMvav 132,160,36\nWL 8\nAR 82,102\nAR 83,103\nMU 82,82,87\nMvav 140,168,36\nRa 152,2,55296\n"
"MU 83,83,87\nCV 53,82,83\nAR 82,104\nMvav 140,172,44\nD1 1,16,46,1024\nAR 83,105\nMU 82,82,86\nMU 83,83,86\nMvav 136,172,40\nRa 156,2,56320\nCV 50,82,83\nAR 82,106\nAR 83,107\n"
"Mvav 136,168,32\nMU 82,82,86\nMU 83,83,86\nCV 51,82,83\nAR 82,108\nMvav 144,176,32\nRa 160,2,57344\nAR 83,109\nMU 82,82,87\nMU 83,83,87\nCV 54,82,83\nMvav 144,180,40\nAR 82,110\n"
"AR 83,111\nMU 82,82,87\nMU 83,83,87\nMvav 148,180,44\nRa 164,2,58368\nCV 55,82,83\nAR 82,112\nAR 83,113\nMvav 148,176,36\nWL 8\nMU 82,82,86\nMU 83,83,86\nCV 56,82,83\n"
"Mvav 156,184,36\nRa 168,2,59392\nAR 82,114\nAR 83,115\nMU 82,82,86\nMvav 156,188,44\nD1 1,16,46,2048\nMU 83,83,86\nCV 57,82,83\nAR 82,116\nMvav 152,188,40\nRa 172,2,60416\n"
"AR 83,117\nMU 82,82,87\nMU 83,83,87\nMvav 152,184,32\nCV 60,82,83\nAR 82,118\nAR 83,119\nMU 82,82,87\nMvav 64,128,32\nRa 176,2,61440\nMU 83,83,87\nCV 61,82,83\nAR 82,120\n"
"AR 83,121\nMvav 64,132,40\nMU 82,82,86\nMU 83,83,86\nCV 58,82,83\nAR 82,122\nMvav 68,132,44\nRa 180,2,62464\nAR 83,123\nMU 82,82,86\nMU 83,83,86\nMvav 68,128,36\n"
"s_waitcnt vmcnt(16) lgkmcnt(8)\nCV 59,82,83\nAR 82,124\nAR 83,125\nMvav 76,136,36\nRa 184,2,63488\nMU 82,82,87\nMU 83,83,87\nCV 62,82,83\nMvav 76,140,44\nD1 1,16,46,3072\n"
"AR 82,126\nAR 83,127\nMU 82,82,87\nMvav 72,140,40\nRa 188,2,64512\ns_barrier\nMU 83,83,87\nCV 63,82,83\nMvav 72,136,32\nMvav 128,144,48\nRa 128,3,0\nMvav 128,148,56\n"
"Mvav 132,148,60\nRa 132,3,1024\nMvav 132,144,52\nWL 8\nMvav 140,152,52\nRa 136,3,2048\nMvav 140,156,60\nMvav 136,156,56\nRa 140,3,3072\nMvav 136,152,48\nMvav 144,160,48\n"
"Ra 144,3,4096\nMvav 144,164,56\nMvav 148,164,60\nRa 148,3,5120\nCV 132,132,133\nMvav 148,160,52\nWL 8\nCV 133,134,135\nMvav 156,168,52\nRa 152,3,6144\nCV 134,140,141\n"
"Mvav 156,172,60\nCV 135,142,143\ns_add_u32 s5, s94, 0\nMvav 152,172,56\nRa 156,3,7168\nSv 132,1,16,5,1024\nMvav 152,168,48\nCV 128,128,129\nCV 129,130,131\nMvav 64,176,48\n"
"Ra 160,3,8192\nCV 130,136,137\nMvav 64,180,56\nbuffer_load_dword v27, off, s[16:19], s60 offset:512 sc0 sc1\nCV 131,138,139\nMvav 68,180,60\nRa 164,3,9216\nSv 128,1,16,5,0\n"
"Mvav 68,176,52\nWL 6\nCV 148,148,149\nMvav 76,184,52\nRa 168,3,10240\nCV 149,150,151\nMvav 76,188,60\nCV 150,156,157\nCV 151,158,159\nMvav 72,188,56\nRa 172,3,11264\n"
"s_add_u32 s5, s94, 131072\nMvav 72,184,48\nSv 148,1,16,5,1024\nCV 144,144,145\nZvav 160,128,96\nRa 176,3,12288\nCV 145,146,147\nMvav 160,132,104\nCV 146,152,153\nCV 147,154,155\n"
"Mvav 160,136,112\nRa 180,3,13312\nSv 144,1,16,5,0\nMvav 160,140,120\nWL 6\nCV 68,68,69\nZvav 164,140,124\nRa 184,3,14336\nCV 69,70,71\nMvav 164,136,116\nCV 70,76,77\nCV 71,78,79\n"
"Mvav 164,132,108\nRa 188,3,15360\ns_add_u32 s5, s94, 262144\nMvav 164,128,100\nSv 68,1,16,5,1024\nCV 64,64,65\nMvav 164,144,36\nRa 128,3,16384\nCV 65,66,67\nMvav 164,148,44\n"
"CV 66,72,73\nCV 67,74,75\nMvav 164,152,52\nRa 132,3,17408\nSv 64,1,16,5,0\nMvav 164,156,60\nWL 6\nMvav 160,156,56\nRa 136,3,18432\nMvav 160,152,48\nMvav 160,148,40\n"
"Ra 140,3,19456\nMvav 160,144,32\nZvav 168,160,96\nRa 144,3,20480\nMvav 168,164,104\nMvav 168,168,112\nRa 148,3,21504\nMvav 168,172,120\nWL 6\nZvav 172,172,124\nRa 152,3,22528\n"
"Mvav 172,168,116\nMvav 172,164,108\nRa 156,3,23552\nMvav 172,160,100\nMvav 172,176,36\nRa 160,3,24576\nMvav 172,180,44\nMvav 172,184,52\nRa 164,3,25600\nMvav 172,188,60\nWL 6\n"
"Mvav 168,188,56\nRa 168,3,26624\nMvav 168,184,48\nMvav 168,180,40\nRa 172,3,27648\nMvav 168,176,32\nZvav 176,128,96\nRa 176,3,28672\nCV 164,164,165\nMvav 176,132,104\n"
"CV 165,166,167\nCV 166,172,173\nMvav 176,136,112\nRa 180,3,29696\nCV 167,174,175\nMvav 176,140,120\ns_waitcnt vmcnt(11) lgkmcnt(6)\ns_add_u32 s5, s94, 393216\nZvav 180,140,124\n"
"Ra 184,3,30720\nSv 164,1,16,5,1024\nMvav 180,136,116\nCV 160,160,161\nCV 161,162,163\nMvav 180,132,108\nRa 188,3,31744\ns_barrier\nMvav 180,128,100\nCV 162,168,169\n"
"CV 163,170,171\nMvav 180,144,36\nRa 128,3,32768\nSv 160,1,16,5,0\nMvav 180,148,44\nMvav 180,152,52\nRa 132,3,33792\nMvav 180,156,60\nWL 6\ns_add_u32 m0, s89, 16384\n"
"s_add_u32 s46, s96, 1277952\nMvav 176,156,56\nRa 136,3,34816\nMvav 176,152,48\nD1 1,16,46,0\nMvav 176,148,40\nRa 140,3,35840\nMvav 176,144,32\nZvav 184,160,96\nRa 144,3,36864\n"
"Mvav 184,164,104\nMvav 184,168,112\nRa 148,3,37888\nMvav 184,172,120\nWL 6\nZvav 188,172,124\nRa 152,3,38912\nMvav 188,168,116\nD1 1,16,46,1024\nMvav 188,164,108\nRa 156,3,39936\n"
"Mvav 188,160,100\nMvav 188,176,36\nRa 160,3,40960\nWV 9\nv_cmp_ne_u32 vcc, s15, v27\ns_nop 0\ns_cbranch_vccnz .S113\n.S114:\nMvav 188,180,44\ns_add_u32 s4, s92, 0\n"
"Mvav 188,184,52\nRa 164,3,41984\nMvav 188,188,60\nWL 6\nbuffer_load_dwordx4 v[208:211], v1, s[16:19], s4 offen offset:0 sc0 sc1\nMvav 184,188,56\nRa 168,3,43008\nMvav 184,184,48\n"
"D1 1,16,46,2048\nMvav 184,180,40\nRa 172,3,44032\nMvav 184,176,32\nbuffer_load_dwordx4 v[212:215], v1, s[16:19], s4 offen offset:1024 sc0 sc1\nZvav 192,128,96\nRa 176,3,45056\n"
"v_lshrrev_b32 v88, 8, v1\nMvav 192,132,104\nv_lshlrev_b32 v89, 5, v88\nCV 180,180,181\nMvav 192,136,112\nRa 180,3,46080\nCV 181,182,183\nMvav 192,140,120\nWL 6\nCV 182,188,189\n"
"Zvav 196,140,124\nRa 184,3,47104\nv_bfe_u32 v90, v1, 4, 4\nMvav 196,136,116\nD1 1,16,46,3072\nCV 183,190,191\nMvav 196,132,108\nRa 188,3,48128\ns_add_u32 s5, s94, 524288\n"
"Mvav 196,128,100\nv_mul_u32_u24 v91, 896, v90\nSv 180,1,16,5,1024\nMvav 196,144,36\nRa 128,3,49152\nv_lshl_add_u32 v91, v88, 4, v91\nMvav 196,148,44\nv_add_u32 v90, s21, v90\n"
"CV 176,176,177\nMvav 196,152,52\nRa 132,3,50176\nCV 177,178,179\nMvav 196,156,60\nWL 6\ns_add_u32 m0, s89, 0\ns_add_u32 s46, s96, 1310720\nMvav 192,156,56\nRa 136,3,51200\n"
"v_lshlrev_b32 v88, 3, v90\nMvav 192,152,48\nD1 1,16,46,0\nCV 178,184,185\nMvav 192,148,40\nRa 140,3,52224\nCV 179,186,187\nMvav 192,144,32\nv_add_u32 v94, 64, v90\n"
"Sv 176,1,16,5,0\nZvav 200,160,96\nRa 144,3,53248\ns_mov_b32 s63, 0xc040000\nMvav 200,164,104\nv_lshrrev_b32 v92, 8, v1\nMvav 200,168,112\nRa 148,3,54272\nMvav 200,172,120\nWL 6\n"
"Zvav 204,172,124\nRa 152,3,55296\nv_bfe_u32 v93, v1, 6, 2\nMvav 204,168,116\nD1 1,16,46,1024\nv_cmp_eq_u32 vcc, v92, v93\nv_and_b32 v92, 16, v1\nv_mov_b32 v93, 0x3f80\n"
"v_cndmask_b32 v93, 0, v93, vcc\nMvav 204,164,108\nRa 156,3,56320\nMvav 204,160,100\nv_lshlrev_b32 v93, v92, v93\nMvav 204,176,36\nRa 160,3,57344\nv_and_b32 v92, 32, v1\n"
"Mvav 204,180,44\ns_add_u32 s4, s92, 131072\nv_cmp_eq_u32 vcc, 0, v92\nv_mov_b32 v4, 0\nv_mov_b32 v5, 0\nv_cndmask_b32 v6, 0, v93, vcc\nMvav 204,184,52\nRa 164,3,58368\n"
"Mvav 204,188,60\nWL 6\nbuffer_load_dwordx4 v[216:219], v1, s[16:19], s4 offen offset:0 sc0 sc1\nMvav 200,188,56\nRa 168,3,59392\nv_sub_u32 v7, v93, v6\nMvav 200,184,48\n"
"D1 1,16,46,2048\nMvav 200,180,40\nRa 172,3,60416\nMvav 200,176,32\nbuffer_load_dwordx4 v[220:223], v1, s[16:19], s4 offen offset:1024 sc0 sc1\nZvav 160,128,96\nRa 176,3,61440\n"
"v_mov_b32 v8, 0\nMvav 160,132,104\nv_mov_b32 v9, 0\nCV 196,196,197\nMvav 160,136,112\nRa 180,3,62464\nCV 197,198,199\nMvav 160,140,120\nWL 6\nCV 198,204,205\nZvav 164,140,124\n"
"Ra 184,3,63488\nCV 199,206,207\nMvav 164,136,116\nD1 1,16,46,3072\ns_add_u32 s5, s94, 655360\nMvav 164,132,108\nRa 188,3,64512\ns_barrier\nMvav 164,128,100\nSv 196,1,16,5,1024\n"
"CV 192,192,193\nMvav 164,144,36\nRa 128,0,0\nCV 193,194,195\nMvav 164,148,44\nCV 194,200,201\nCV 195,202,203\nMvav 164,152,52\nRa 132,0,1024\nSv 192,1,16,5,0\nMvav 164,156,60\n"
"WL 6\ns_add_u32 m0, s89, 32768\ns_add_u32 s46, s96, 1343488\nMvav 160,156,56\nRa 136,0,2048\nMvav 160,152,48\nD1 1,16,46,0\nMvav 160,148,40\nRa 140,0,3072\nMvav 160,144,32\n"
"Zvav 168,160,96\nRa 144,0,4096\nWV 12\nv_max_u32 v21, v211, v215\nv_cmp_eq_u32 vcc, -1, v21\ns_nop 0\ns_cbranch_vccnz .S89\n.S90:\nMvav 168,164,104\nv_lshlrev_b32 v176, 16, v208\n"
"Mvav 168,168,112\nRa 148,0,5120\nMvav 168,172,120\nWL 6\nv_and_b32 v177, 0xffff0000, v208\nZvav 172,172,124\nRa 152,0,6144\nMvav 172,168,116\nD1 1,16,46,1024\nMvav 172,164,108\n"
"Ra 156,0,7168\nv_lshlrev_b32 v178, 16, v209\nMvav 172,160,100\nv_and_b32 v179, 0xffff0000, v209\nMvav 172,176,36\nRa 160,0,8192\nv_lshlrev_b32 v180, 16, v212\nMvav 172,180,44\n"
"v_and_b32 v181, 0xffff0000, v212\ns_add_u32 s4, s92, 262144\nMvav 172,184,52\nRa 164,0,9216\nMvav 172,188,60\nWL 6\nv_lshlrev_b32 v182, 16, v213\n"
"buffer_load_dwordx4 v[224:227], v1, s[16:19], s4 offen offset:0 sc0 sc1\nMvav 168,188,56\nRa 168,0,10240\nMvav 168,184,48\nD1 1,16,46,2048\nMvav 168,180,40\nRa 172,0,11264\n"
"v_and_b32 v183, 0xffff0000, v213\nMvav 168,176,32\nv_lshlrev_b32 v184, 16, v210\nbuffer_load_dwordx4 v[228:231], v1, s[16:19], s4 offen offset:1024 sc0 sc1\nMvav 176,128,96\n"
"Ra 176,0,12288\nv_and_b32 v185, 0xffff0000, v210\nMvav 176,132,104\nv_lshlrev_b32 v186, 16, v211\nCV 164,164,165\nMvav 176,136,112\nRa 180,0,13312\nCV 165,166,167\n"
"Mvav 176,140,120\nWL 6\nCV 166,172,173\nMvav 180,140,124\nRa 184,0,14336\nv_and_b32 v187, 0xffff0000, v211\nMvav 180,136,116\nD1 1,16,46,3072\nCV 167,174,175\nMvav 180,132,108\n"
"Ra 188,0,15360\ns_add_u32 s5, s94, 786432\nMvav 180,128,100\nv_lshlrev_b32 v188, 16, v214\nSv 164,1,16,5,1024\nMvav 180,144,36\nRa 128,0,16384\nv_and_b32 v189, 0xffff0000, v214\n"
"Mvav 180,148,44\nv_lshlrev_b32 v190, 16, v215\nCV 160,160,161\nMvav 180,152,52\nRa 132,0,17408\nCV 161,162,163\nMvav 180,156,60\nWL 6\ns_add_u32 m0, s89, 49152\n"
"s_add_u32 s46, s96, 1376256\nMvav 176,156,56\nRa 136,0,18432\nv_and_b32 v191, 0xffff0000, v215\nMvav 176,152,48\nD1 1,16,46,0\nCV 162,168,169\nMvav 176,148,40\nRa 140,0,19456\n"
"CV 163,170,171\nMvav 176,144,32\nSv 160,1,16,5,0\nMvav 184,160,96\nRa 144,0,20480\nWV 12\nv_max_u32 v21, v219, v223\nv_cmp_eq_u32 vcc, -1, v21\ns_nop 0\ns_cbranch_vccnz .S92\n"
".S93:\nMvav 184,164,104\nv_lshlrev_b32 v192, 16, v216\nMvav 184,168,112\nRa 148,0,21504\nMvav 184,172,120\nWL 6\nv_and_b32 v193, 0xffff0000, v216\nMvav 188,172,124\n"
"Ra 152,0,22528\nMvav 188,168,116\nD1 1,16,46,1024\nMvav 188,164,108\nRa 156,0,23552\nv_lshlrev_b32 v194, 16, v217\nMvav 188,160,100\nv_and_b32 v195, 0xffff0000, v217\n"
"Mvav 188,176,36\nRa 160,0,24576\nv_lshlrev_b32 v196, 16, v220\nMvav 188,180,44\nv_and_b32 v197, 0xffff0000, v220\ns_add_u32 s4, s92, 393216\nMvav 188,184,52\nRa 164,0,25600\n"
"Mvav 188,188,60\nWL 6\nv_lshlrev_b32 v198, 16, v221\nbuffer_load_dwordx4 v[208:211], v1, s[16:19], s4 offen offset:0 sc0 sc1\nMvav 184,188,56\nRa 168,0,26624\nMvav 184,184,48\n"
"D1 1,16,46,2048\nMvav 184,180,40\nRa 172,0,27648\nv_and_b32 v199, 0xffff0000, v221\nMvav 184,176,32\nv_lshlrev_b32 v200, 16, v218\n"
"buffer_load_dwordx4 v[212:215], v1, s[16:19], s4 offen offset:1024 sc0 sc1\nMvav 192,128,96\nRa 176,0,28672\nv_and_b32 v201, 0xffff0000, v218\nMvav 192,132,104\n"
"v_lshlrev_b32 v202, 16, v219\nCV 180,180,181\nMvav 192,136,112\nRa 180,0,29696\nCV 181,182,183\nMvav 192,140,120\ns_waitcnt vmcnt(15) lgkmcnt(6)\nCV 182,188,189\nMvav 196,140,124\n"
"Ra 184,0,30720\nv_and_b32 v203, 0xffff0000, v219\nMvav 196,136,116\nD1 1,16,46,3072\nCV 183,190,191\nMvav 196,132,108\nRa 188,0,31744\ns_barrier\nMvav 196,128,100\n"
"v_lshlrev_b32 v204, 16, v222\ns_add_u32 s5, s94, 917504\nMvav 196,144,36\nRa 128,2,16384\nv_and_b32 v205, 0xffff0000, v222\nMvav 196,148,44\nv_lshlrev_b32 v206, 16, v223\n"
"Sv 180,1,16,5,1024\nMvav 196,152,52\nRa 132,2,17408\nCV 176,176,177\nMvav 196,156,60\nWL 6\ns_add_u32 m0, s89, 65536\ns_add_u32 s46, s96, 1409024\nMvav 192,156,56\nRa 136,2,18432\n"
"v_and_b32 v207, 0xffff0000, v223\nMvav 192,152,48\nD1 1,16,46,0\nCV 177,178,179\nMvav 192,148,40\nRa 140,2,19456\nCV 178,184,185\nMvav 192,144,32\nCV 179,186,187\nSv 176,1,16,5,0\n"
"Mvav 200,160,96\nRa 144,2,20480\nWV 12\nv_max_u32 v21, v227, v231\nv_cmp_eq_u32 vcc, -1, v21\ns_nop 0\ns_cbranch_vccnz .S95\n.S96:\nMvav 200,164,104\nv_lshlrev_b32 v160, 16, v224\n"
"Mvav 200,168,112\nRa 148,2,21504\nMvav 200,172,120\nWL 6\nv_and_b32 v161, 0xffff0000, v224\nMvav 204,172,124\nRa 152,2,22528\nMvav 204,168,116\nD1 1,16,46,1024\nMvav 204,164,108\n"
"Ra 156,2,23552\nv_lshlrev_b32 v162, 16, v225\nMvav 204,160,100\nv_and_b32 v163, 0xffff0000, v225\nMvav 204,176,36\nRa 160,2,24576\nv_lshlrev_b32 v164, 16, v228\nMvav 204,180,44\n"
"v_and_b32 v165, 0xffff0000, v228\ns_add_u32 s4, s92, 524288\nMvav 204,184,52\nRa 164,2,25600\nMvav 204,188,60\nWL 6\nv_lshlrev_b32 v166, 16, v229\n"
"buffer_load_dwordx4 v[216:219], v1, s[16:19], s4 offen offset:0 sc0 sc1\nMvav 200,188,56\nRa 168,2,26624\nMvav 200,184,48\nD1 1,16,46,2048\nMvav 200,180,40\nRa 172,2,27648\n"
"v_and_b32 v167, 0xffff0000, v229\nMvav 200,176,32\nv_lshlrev_b32 v168, 16, v226\nbuffer_load_dwordx4 v[220:223], v1, s[16:19], s4 offen offset:1024 sc0 sc1\nMvav 160,128,96\n"
"Ra 176,2,28672\nv_and_b32 v169, 0xffff0000, v226\nMvav 160,132,104\nv_lshlrev_b32 v170, 16, v227\nCV 196,196,197\nMvav 160,136,112\nRa 180,2,29696\nCV 197,198,199\n"
"Mvav 160,140,120\nWL 6\nCV 198,204,205\nMvav 164,140,124\nRa 184,2,30720\nv_and_b32 v24, 0x70, v1\nMvav 164,136,116\nD1 1,16,46,3072\nCV 199,206,207\nMvav 164,132,108\n"
"Ra 188,2,31744\nv_and_b32 v171, 0xffff0000, v227\nMvav 164,128,100\nv_lshlrev_b32 v172, 16, v230\ns_add_u32 s5, s94, 1048576\nMvav 164,144,36\nRa 128,2,0\n"
"v_lshlrev_b32 v24, 5, v24\nMvav 164,148,44\nv_and_b32 v173, 0xffff0000, v230\nSv 196,1,16,5,1024\nMvav 164,152,52\nRa 132,2,1024\nCV 192,192,193\nMvav 164,156,60\nWL 6\n"
"v_lshlrev_b32 v174, 16, v231\nMvav 160,156,56\nRa 136,2,2048\nbuffer_load_dword v27, v24, s[16:19], s41 offen sc0 sc1\nMvav 160,152,48\nv_and_b32 v175, 0xffff0000, v231\n"
"CV 193,194,195\nMvav 160,148,40\nRa 140,2,3072\nCV 194,200,201\nMvav 160,144,32\nCV 195,202,203\nSv 192,1,16,5,0\nMvav 168,160,96\nRa 144,2,4096\nWV 12\nv_max_u32 v21, v211, v215\n"
"v_cmp_eq_u32 vcc, -1, v21\ns_nop 0\ns_cbranch_vccnz .S98\n.S99:\nMvav 168,164,104\nv_lshlrev_b32 v176, 16, v208\nMvav 168,168,112\nRa 148,2,5120\nMvav 168,172,120\nWL 6\n"
"Mvav 172,172,124\nRa 152,2,6144\nv_and_b32 v177, 0xffff0000, v208\nMvav 172,168,116\nv_lshlrev_b32 v178, 16, v209\nMvav 172,164,108\nRa 156,2,7168\nMvav 172,160,100\n"
"v_and_b32 v179, 0xffff0000, v209\nMvav 172,176,36\nRa 160,2,8192\nv_lshlrev_b32 v180, 16, v212\nMvav 172,180,44\nv_and_b32 v181, 0xffff0000, v212\ns_add_u32 s4, s92, 655360\n"
"Mvav 172,184,52\nRa 164,2,9216\nMvav 172,188,60\nWL 6\nbuffer_load_dwordx4 v[224:227], v1, s[16:19], s4 offen offset:0 sc0 sc1\nMvav 168,188,56\nRa 168,2,10240\n"
"v_lshlrev_b32 v182, 16, v213\nMvav 168,184,48\nv_and_b32 v183, 0xffff0000, v213\nMvav 168,180,40\nRa 172,2,11264\nMvav 168,176,32\nv_lshlrev_b32 v184, 16, v210\n"
"buffer_load_dwordx4 v[228:231], v1, s[16:19], s4 offen offset:1024 sc0 sc1\nMvav 176,128,96\nRa 176,2,12288\nv_and_b32 v185, 0xffff0000, v210\nMvav 176,132,104\n"
"v_lshlrev_b32 v186, 16, v211\nCV 164,164,165\nMvav 176,136,112\nRa 180,2,13312\nCV 165,166,167\nMvav 176,140,120\ns_waitcnt vmcnt(13) lgkmcnt(6)\nCV 166,172,173\nMvav 180,140,124\n"
"Ra 184,2,14336\nCV 167,174,175\nMvav 180,136,116\nv_and_b32 v187, 0xffff0000, v211\ns_add_u32 s5, s94, 1179648\nMvav 180,132,108\nRa 188,2,15360\ns_barrier\nMvav 180,128,100\n"
"v_lshlrev_b32 v188, 16, v214\nSv 164,1,16,5,1024\nMvav 180,144,36\nRa 128,2,32768\nv_and_b32 v189, 0xffff0000, v214\nMvav 180,148,44\nv_lshlrev_b32 v190, 16, v215\nCV 160,160,161\n"
"Mvav 180,152,52\nRa 132,2,33792\nCV 161,162,163\nMvav 180,156,60\nWL 6\nCV 162,168,169\nMvav 176,156,56\nRa 136,2,34816\nCV 163,170,171\nMvav 176,152,48\n"
"v_and_b32 v191, 0xffff0000, v215\nSv 160,1,16,5,0\nMvav 176,148,40\nRa 140,2,35840\nMvav 176,144,32\nMvav 184,160,96\nRa 144,2,36864\nWV 8\nv_max_u32 v21, v219, v223\n"
"v_cmp_eq_u32 vcc, -1, v21\ns_nop 0\ns_cbranch_vccnz .S101\n.S102:\nMvav 184,164,104\nv_lshlrev_b32 v192, 16, v216\nMvav 184,168,112\nRa 148,2,37888\nMvav 184,172,120\nWL 6\n"
"Mvav 188,172,124\nRa 152,2,38912\nv_and_b32 v193, 0xffff0000, v216\nMvav 188,168,116\nv_lshlrev_b32 v194, 16, v217\nMvav 188,164,108\nRa 156,2,39936\nMvav 188,160,100\n"
"v_and_b32 v195, 0xffff0000, v217\nMvav 188,176,36\nRa 160,2,40960\nv_lshlrev_b32 v196, 16, v220\nMvav 188,180,44\nv_and_b32 v197, 0xffff0000, v220\ns_add_u32 s4, s92, 786432\n"
"Mvav 188,184,52\nRa 164,2,41984\nMvav 188,188,60\nWL 6\nbuffer_load_dwordx4 v[208:211], v1, s[16:19], s4 offen offset:0 sc0 sc1\nMvav 184,188,56\nRa 168,2,43008\n"
"v_lshlrev_b32 v198, 16, v221\nMvav 184,184,48\nv_and_b32 v199, 0xffff0000, v221\nMvav 184,180,40\nRa 172,2,44032\nMvav 184,176,32\nv_lshlrev_b32 v200, 16, v218\n"
"buffer_load_dwordx4 v[212:215], v1, s[16:19], s4 offen offset:1024 sc0 sc1\nMvav 192,128,96\nRa 176,2,45056\nv_and_b32 v201, 0xffff0000, v218\nMvav 192,132,104\n"
"v_lshlrev_b32 v202, 16, v219\nCV 180,180,181\nMvav 192,136,112\nRa 180,2,46080\nCV 181,182,183\nMvav 192,140,120\nWL 6\nCV 182,188,189\nMvav 196,140,124\nRa 184,2,47104\n"
"CV 183,190,191\nMvav 196,136,116\nv_and_b32 v203, 0xffff0000, v219\ns_add_u32 s5, s94, 1310720\nMvav 196,132,108\nRa 188,2,48128\nSv 180,1,16,5,1024\nMvav 196,128,100\n"
"v_lshlrev_b32 v204, 16, v222\nCV 176,176,177\nMvav 196,144,36\nRa 128,2,49152\nv_and_b32 v205, 0xffff0000, v222\nMvav 196,148,44\nv_lshlrev_b32 v206, 16, v223\nCV 177,178,179\n"
"Mvav 196,152,52\nRa 132,2,50176\nCV 178,184,185\nMvav 196,156,60\nWL 6\nCV 179,186,187\nMvav 192,156,56\nRa 136,2,51200\nSv 176,1,16,5,0\nMvav 192,152,48\n"
"v_and_b32 v207, 0xffff0000, v223\nMvav 192,148,40\nRa 140,2,52224\nMvav 192,144,32\nMvav 200,160,96\nRa 144,2,53248\nWV 6\nv_max_u32 v21, v227, v231\nv_cmp_eq_u32 vcc, -1, v21\n"
"s_nop 0\ns_cbranch_vccnz .S104\n.S105:\nMvav 200,164,104\nv_lshlrev_b32 v160, 16, v224\nv_xor_b32 v27, s15, v27\nv_and_b32 v26, 0xffffdfff, v27\nv_cmp_ne_u32 s[64:65], 0, v27\n"
"v_cmp_ne_u32 vcc, 0, v26\ns_nop 0\ns_cbranch_vccnz .S116\n.S117:\nMvav 200,168,112\nRa 148,2,54272\ns_bitcmp1_b32 s64, 0\ns_cselect_b32 s2, 0x2000, 0\nMvav 200,172,120\nWL 6\n"
"Mvav 204,172,124\nRa 152,2,55296\nv_and_b32 v161, 0xffff0000, v224\nMvav 204,168,116\nv_lshlrev_b32 v162, 16, v225\nMvav 204,164,108\nRa 156,2,56320\ns_xor_b32 s2, s2, s20\n"
"Mvav 204,160,100\nv_and_b32 v163, 0xffff0000, v225\nMvav 204,176,36\nRa 160,2,57344\nv_lshlrev_b32 v164, 16, v228\nMvav 204,180,44\nv_and_b32 v165, 0xffff0000, v228\n"
"Mvav 204,184,52\nRa 164,2,58368\nMvav 204,188,60\nWL 6\ns_add_u32 s24, s2, 0x0\nMvav 200,188,56\nRa 168,2,59392\nv_lshlrev_b32 v166, 16, v229\nMvav 200,184,48\n"
"v_and_b32 v167, 0xffff0000, v229\ns_bitcmp1_b32 s64, 1\ns_cselect_b32 s2, 0x2000, 0\nMvav 200,180,40\nRa 172,2,60416\nMvav 200,176,32\nv_lshlrev_b32 v168, 16, v226\n"
"Mvav 160,128,96\nRa 176,2,61440\nv_and_b32 v169, 0xffff0000, v226\nMvav 160,132,104\nv_lshlrev_b32 v170, 16, v227\nCV 196,196,197\nMvav 160,136,112\nRa 180,2,62464\n"
"CV 197,198,199\nMvav 160,140,120\nWL 6\nCV 198,204,205\nMvav 164,140,124\nRa 184,2,63488\nCV 199,206,207\nMvav 164,136,116\nv_and_b32 v171, 0xffff0000, v227\n"
"s_add_u32 s5, s94, 1441792\nMvav 164,132,108\nRa 188,2,64512\ns_barrier\nMvav 164,128,100\nv_lshlrev_b32 v172, 16, v230\nSv 196,1,16,5,1024\nMvav 164,144,36\nRa 128,3,0\n"
"v_and_b32 v173, 0xffff0000, v230\nMvav 164,148,44\nv_lshlrev_b32 v174, 16, v231\nCV 192,192,193\nMvav 164,152,52\nRa 132,3,1024\nCV 193,194,195\nMvav 164,156,60\nWL 6\n"
"s_xor_b32 s2, s2, s20\nMvav 160,156,56\nRa 136,3,2048\nCV 194,200,201\nMvav 160,152,48\nv_and_b32 v175, 0xffff0000, v231\nCV 195,202,203\nMvav 160,148,40\nRa 140,3,3072\n"
"Sv 192,1,16,5,0\nMvav 160,144,32\ns_add_u32 s25, s2, 0x1800000\ns_bitcmp1_b32 s64, 2\ns_cselect_b32 s2, 0x2000, 0\nMvav 168,160,96\nRa 144,3,4096\nWV 4\nv_max_u32 v21, v211, v215\n"
"v_cmp_eq_u32 vcc, -1, v21\ns_nop 0\ns_cbranch_vccnz .S107\n.S108:\nMvav 168,164,104\nv_lshlrev_b32 v176, 16, v208\nMvav 168,168,112\nRa 148,3,5120\nMvav 168,172,120\nWL 6\n"
"Mvav 172,172,124\nRa 152,3,6144\nv_and_b32 v177, 0xffff0000, v208\nMvav 172,168,116\nv_lshlrev_b32 v178, 16, v209\nMvav 172,164,108\nRa 156,3,7168\ns_xor_b32 s2, s2, s20\n"
"Mvav 172,160,100\nv_and_b32 v179, 0xffff0000, v209\nMvav 172,176,36\nRa 160,3,8192\nv_lshlrev_b32 v180, 16, v212\nMvav 172,180,44\nv_and_b32 v181, 0xffff0000, v212\n"
"Mvav 172,184,52\nRa 164,3,9216\nMvav 172,188,60\nWL 6\ns_add_u32 s26, s2, 0x3000000\nMvav 168,188,56\nRa 168,3,10240\nv_lshlrev_b32 v182, 16, v213\nMvav 168,184,48\n"
"v_and_b32 v183, 0xffff0000, v213\ns_bitcmp1_b32 s64, 3\ns_cselect_b32 s2, 0x2000, 0\nMvav 168,180,40\nRa 172,3,11264\nMvav 168,176,32\nv_lshlrev_b32 v184, 16, v210\n"
"Mvav 176,128,96\nRa 176,3,12288\ns_xor_b32 s2, s2, s20\nMvav 176,132,104\nv_and_b32 v185, 0xffff0000, v210\nCV 164,164,165\nMvav 176,136,112\nRa 180,3,13312\nCV 165,166,167\n"
"Mvav 176,140,120\nWL 6\nCV 166,172,173\nMvav 180,140,124\nRa 184,3,14336\nCV 167,174,175\nMvav 180,136,116\nv_lshlrev_b32 v186, 16, v211\ns_add_u32 s5, s94, 1572864\n"
"Mvav 180,132,108\nRa 188,3,15360\ns_add_u32 s27, s2, 0x4800000\nMvav 180,128,100\nv_and_b32 v187, 0xffff0000, v211\ns_bitcmp1_b32 s64, 4\ns_cselect_b32 s2, 0x2000, 0\n"
"Mvav 180,144,36\nv_lshlrev_b32 v188, 16, v214\nSv 164,1,16,5,1024\nMvav 180,148,44\nv_and_b32 v189, 0xffff0000, v214\nCV 160,160,161\nMvav 180,152,52\n"
"v_lshlrev_b32 v190, 16, v215\nCV 161,162,163\nMvav 180,156,60\nWL 4\ns_xor_b32 s2, s2, s20\nMvav 176,156,56\nv_and_b32 v191, 0xffff0000, v215\nCV 162,168,169\nMvav 176,152,48\n"
"s_add_u32 s32, s2, 0x6000000\ns_bitcmp1_b32 s64, 5\ns_cselect_b32 s2, 0x2000, 0\nMvav 176,148,40\ns_xor_b32 s2, s2, s20\nCV 163,170,171\nMvav 176,144,32\n"
"s_add_u32 s33, s2, 0x7800000\ns_bitcmp1_b32 s64, 6\ns_cselect_b32 s2, 0x2000, 0\nMvav 184,160,96\ns_xor_b32 s2, s2, s20\ns_add_u32 s34, s2, 0x9000000\nMvav 184,164,104\n"
"s_bitcmp1_b32 s64, 7\ns_cselect_b32 s2, 0x2000, 0\nMvav 184,168,112\ns_xor_b32 s2, s2, s20\nSv 160,1,16,5,0\nMvav 184,172,120\nWL 0\nMvav 188,172,124\n"
"s_add_u32 s35, s2, 0xa800000\nMvav 188,168,116\nMvav 188,164,108\nMvav 188,160,100\nMvav 188,176,36\nMvav 188,180,44\nMvav 188,184,52\nMvav 188,188,60\nMvav 184,188,56\n"
"Mvav 184,184,48\nMvav 184,180,40\nMvav 184,176,32\nCV 180,180,181\nCV 181,182,183\ns_nop 1\nCV 182,188,189\nCV 183,190,191\ns_add_u32 s5, s94, 1703936\nSv 180,1,16,5,1024\n"
"CV 176,176,177\nCV 177,178,179\nCV 178,184,185\nCV 179,186,187\nSv 176,1,16,5,0\ns_cmp_eq_u32 s13, 0\ns_cbranch_scc1 .Z119\n"
"buffer_load_dwordx4 v[12:15], v91, s[28:31], s22 offen offset:0\nbuffer_load_dwordx4 v[16:19], v91, s[28:31], s23 offen offset:0\n"
"buffer_load_dwordx4 v[64:67], v89, s[16:19], s40 offen offset:0 sc0 sc1\nbuffer_load_dwordx4 v[68:71], v89, s[16:19], s40 offen offset:16 sc0 sc1\n"
"buffer_load_dwordx4 v[72:75], v89, s[16:19], s40 offen offset:1792 sc0 sc1\nbuffer_load_dwordx4 v[76:79], v89, s[16:19], s40 offen offset:1808 sc0 sc1\n"
"buffer_load_dwordx2 v[80:81], v88, s[16:19], s63 offen offset:0 sc0 sc1\nbuffer_load_dwordx2 v[82:83], v88, s[16:19], s63 offen offset:512 sc0 sc1\n"
"buffer_load_dwordx4 v[96:99], v1, s[16:19], s24 offen offset:0 sc0 sc1\nbuffer_load_dwordx4 v[100:103], v1, s[16:19], s24 offen offset:1024 sc0 sc1\n"
"buffer_load_dwordx4 v[104:107], v1, s[16:19], s25 offen offset:0 sc0 sc1\nbuffer_load_dwordx4 v[108:111], v1, s[16:19], s25 offen offset:1024 sc0 sc1\n"
"buffer_load_dwordx4 v[112:115], v1, s[16:19], s26 offen offset:0 sc0 sc1\nbuffer_load_dwordx4 v[116:119], v1, s[16:19], s26 offen offset:1024 sc0 sc1\n"
"buffer_load_dwordx4 v[120:123], v1, s[16:19], s27 offen offset:0 sc0 sc1\nbuffer_load_dwordx4 v[124:127], v1, s[16:19], s27 offen offset:1024 sc0 sc1\n"
"buffer_load_dwordx4 v[32:35], v1, s[16:19], s32 offen offset:0 sc0 sc1\nbuffer_load_dwordx4 v[36:39], v1, s[16:19], s32 offen offset:1024 sc0 sc1\n"
"buffer_load_dwordx4 v[40:43], v1, s[16:19], s33 offen offset:0 sc0 sc1\nbuffer_load_dwordx4 v[44:47], v1, s[16:19], s33 offen offset:1024 sc0 sc1\n"
"buffer_load_dwordx4 v[48:51], v1, s[16:19], s34 offen offset:0 sc0 sc1\nbuffer_load_dwordx4 v[52:55], v1, s[16:19], s34 offen offset:1024 sc0 sc1\n"
"buffer_load_dwordx4 v[56:59], v1, s[16:19], s35 offen offset:0 sc0 sc1\nbuffer_load_dwordx4 v[60:63], v1, s[16:19], s35 offen offset:1024 sc0 sc1\nWV 16\nv_xor_b32 v66, s15, v66\n"
"v_xor_b32 v70, s15, v70\nv_xor_b32 v74, s15, v74\nv_xor_b32 v78, s15, v78\nv_xor_b32 v81, s15, v81\nv_xor_b32 v83, s15, v83\nv_or3_b32 v92, v66, v70, v74\n"
"v_or3_b32 v92, v92, v78, v81\nv_or_b32 v92, v92, v83\nv_cmp_ne_u32 vcc, 0, v92\ns_nop 0\ns_cbranch_vccnz .S110\n.S111:\nv_and_b32 v92, 0x7ff, v67\nv_lshrrev_b32 v93, 16, v67\n"
"v_cmp_eq_u32 s[44:45], v92, v90\nv_cmp_eq_u32 s[46:47], v93, v90\nv_cmp_eq_u32 s[48:49], v92, v94\nv_cmp_eq_u32 s[50:51], v93, v94\nv_cndmask_b32 v12, v12, v64, s[44:45]\n"
"v_cndmask_b32 v13, v13, v65, s[44:45]\nv_cndmask_b32 v14, v14, v68, s[44:45]\nv_cndmask_b32 v15, v15, v69, s[44:45]\nv_cndmask_b32 v12, v12, v72, s[46:47]\n"
"v_cndmask_b32 v13, v13, v73, s[46:47]\nv_cndmask_b32 v14, v14, v76, s[46:47]\nv_cndmask_b32 v15, v15, v77, s[46:47]\nv_cndmask_b32 v16, v16, v64, s[48:49]\n"
"v_cndmask_b32 v17, v17, v65, s[48:49]\nv_cndmask_b32 v18, v18, v68, s[48:49]\nv_cndmask_b32 v19, v19, v69, s[48:49]\nv_cndmask_b32 v16, v16, v72, s[50:51]\n"
"v_cndmask_b32 v17, v17, v73, s[50:51]\nv_cndmask_b32 v18, v18, v76, s[50:51]\nv_cndmask_b32 v19, v19, v77, s[50:51]\nv_sub_f32 v92, 1.0, v80\nv_sub_f32 v93, 1.0, v82\nCV 92,92,93\n"
"v_and_b32 v93, 0xffff0000, v92\nv_lshlrev_b32 v92, 16, v92\nv_lshlrev_b32 v64, 16, v12\nv_and_b32 v65, 0xffff0000, v12\nMU 64,64,92\nMU 65,65,92\nv_lshlrev_b32 v66, 16, v13\n"
"v_and_b32 v67, 0xffff0000, v13\nMU 66,66,92\nMU 67,67,92\nv_lshlrev_b32 v68, 16, v14\nv_and_b32 v69, 0xffff0000, v14\nMU 68,68,92\nMU 69,69,92\nv_lshlrev_b32 v70, 16, v15\n"
"v_and_b32 v71, 0xffff0000, v15\nMU 70,70,92\nMU 71,71,92\nv_lshlrev_b32 v72, 16, v16\nv_and_b32 v73, 0xffff0000, v16\nMU 72,72,93\nMU 73,73,93\nv_lshlrev_b32 v74, 16, v17\n"
"v_and_b32 v75, 0xffff0000, v17\nMU 74,74,93\nMU 75,75,93\nv_lshlrev_b32 v76, 16, v18\nv_and_b32 v77, 0xffff0000, v18\nMU 76,76,93\nMU 77,77,93\nv_lshlrev_b32 v78, 16, v19\n"
"v_and_b32 v79, 0xffff0000, v19\nMU 78,78,93\nMU 79,79,93\ns_mov_b32 s7, 262144\nWV 0\n.Z122:\nv_max_u32 v84, v99, v103\nv_cmp_eq_u32 s[44:45], -1, v84\nv_max_u32 v84, v107, v111\n"
"v_cmp_eq_u32 s[46:47], -1, v84\nv_max_u32 v84, v115, v119\nv_cmp_eq_u32 s[48:49], -1, v84\nv_max_u32 v84, v123, v127\nv_cmp_eq_u32 s[50:51], -1, v84\nv_max_u32 v84, v35, v39\n"
"v_cmp_eq_u32 s[52:53], -1, v84\nv_max_u32 v84, v43, v47\nv_cmp_eq_u32 s[54:55], -1, v84\nv_max_u32 v84, v51, v55\nv_cmp_eq_u32 s[56:57], -1, v84\nv_max_u32 v84, v59, v63\n"
"v_cmp_eq_u32 s[58:59], -1, v84\ns_or_b64 s[0:1], s[44:45], s[46:47]\ns_or_b64 s[2:3], s[48:49], s[50:51]\ns_or_b64 s[0:1], s[0:1], s[52:53]\ns_or_b64 s[2:3], s[2:3], s[54:55]\n"
"s_or_b64 s[0:1], s[0:1], s[56:57]\ns_or_b64 s[2:3], s[2:3], s[58:59]\ns_or_b64 s[0:1], s[0:1], s[2:3]\ns_cbranch_scc1 .Z120\n.Z121:\nZvvv 136,6,96\nMvvv 136,6,104\nMvvv 136,6,112\n"
"Mvvv 136,6,120\nMvvv 136,6,32\nMvvv 136,6,40\nMvvv 136,6,48\nMvvv 136,6,56\nZvvv 140,4,96\nMvvv 140,4,104\nMvvv 140,4,112\nMvvv 140,4,120\nMvvv 140,4,32\nMvvv 140,4,40\n"
"Mvvv 140,4,48\nMvvv 140,4,56\nZvvv 144,6,100\nMvvv 144,6,108\nMvvv 144,6,116\nMvvv 144,6,124\nMvvv 144,6,36\nMvvv 144,6,44\nMvvv 144,6,52\nMvvv 144,6,60\nZvvv 148,4,100\n"
"Mvvv 148,4,108\nMvvv 148,4,116\nMvvv 148,4,124\nMvvv 148,4,36\nMvvv 148,4,44\nMvvv 148,4,52\nMvvv 148,4,60\nFM 64,80,136\nFM 65,80,137\nFM 66,80,138\nFM 67,80,139\nFM 68,80,140\n"
"FM 69,80,141\nFM 70,80,142\nFM 71,80,143\nCV 12,64,65\nCV 13,66,67\nCV 14,68,69\nCV 15,70,71\nSv 12,91,36,22,0\nFM 72,82,144\nFM 73,82,145\nFM 74,82,146\nFM 75,82,147\n"
"FM 76,82,148\nFM 77,82,149\nFM 78,82,150\nFM 79,82,151\nCV 16,72,73\nCV 17,74,75\nCV 18,76,77\nCV 19,78,79\nSv 16,91,36,23,0\n.Z119:\ns_endpgm\n.S14:\ns_mov_b32 s7, 262144\n.S16:\n"
"buffer_load_dwordx4 v[32:35], v1, s[16:19], s6 offen offset:0 sc0 sc1\nbuffer_load_dwordx4 v[36:39], v1, s[16:19], s6 offen offset:1024 sc0 sc1\n"
"buffer_load_dwordx4 v[40:43], v1, s[16:19], s6 offen offset:2048 sc0 sc1\nbuffer_load_dwordx4 v[44:47], v1, s[16:19], s6 offen offset:3072 sc0 sc1\nWV 0\nv_xor_b32 v48, s15, v33\n"
"v_xor_b32 v49, s15, v35\nv_xor_b32 v50, s15, v37\nv_xor_b32 v51, s15, v39\nv_xor_b32 v52, s15, v41\nv_xor_b32 v53, s15, v43\nv_xor_b32 v54, s15, v45\nv_xor_b32 v55, s15, v47\n"
"v_or3_b32 v48, v48, v49, v50\nv_or3_b32 v51, v51, v52, v53\nv_or3_b32 v48, v48, v54, v55\nv_or_b32 v48, v48, v51\nv_cmp_ne_u32 vcc, 0, v48\ns_nop 0\ns_cbranch_vccz .S15\n"
"s_sleep 6\ns_sub_u32 s7, s7, 1\ns_cbranch_scc0 .S16\ns_branch .S15\n.S17:\ns_mov_b32 s7, 262144\n.S19:\ns_lshl_b32 s5, s73, 8\ns_add_u32 s5, s5, 0xc04ac00\n"
"v_lshrrev_b32 v27, 2, v1\nbuffer_load_dword v27, v27, s[16:19], s5 offen sc0 sc1\nWV 0\nv_xor_b32 v27, s15, v27\nv_and_b32 v255, 0xffffdfff, v27\nv_cmp_ne_u32 vcc, 0, v255\n"
"s_nop 0\ns_cbranch_vccz .S18\ns_sleep 6\ns_sub_u32 s7, s7, 1\ns_cbranch_scc0 .S19\ns_branch .S18\n.T22:\ns_mov_b32 s62, 1\ns_call_b64 s[80:81], .RA10\ns_branch .T23\n.T24:\n"
"s_mov_b32 s62, 1\ns_call_b64 s[80:81], .RA10\ns_branch .T25\n.T27:\ns_mov_b32 s62, 0\ns_call_b64 s[80:81], .RB11\ns_branch .T28\n.T29:\ns_mov_b32 s62, 0\n"
"s_call_b64 s[80:81], .RA10\ns_branch .T30\n.T32:\ns_mov_b32 s62, 0\ns_call_b64 s[80:81], .RB11\ns_branch .T33\n.T34:\ns_mov_b32 s62, 0\ns_call_b64 s[80:81], .RA10\ns_branch .T35\n"
".S37:\ns_mov_b32 s7, 262144\n.S39:\nv_and_b32 v27, 0x70, v1\nbuffer_load_dword v27, v27, s[16:19], s61 offen offset:128 sc0 sc1\nWV 0\nv_cmp_ne_u32 vcc, s15, v27\ns_nop 0\n"
"s_cbranch_vccz .S38\ns_sleep 6\ns_sub_u32 s7, s7, 1\ns_cbranch_scc0 .S39\ns_branch .S38\n.S40:\ns_mov_b32 s7, 262144\n.S42:\nv_and_b32 v27, 0x70, v1\n"
"buffer_load_dword v27, v27, s[16:19], s61 offen offset:136 sc0 sc1\nWV 0\nv_cmp_ne_u32 vcc, s15, v27\ns_nop 0\ns_cbranch_vccz .S41\ns_sleep 6\ns_sub_u32 s7, s7, 1\n"
"s_cbranch_scc0 .S42\ns_branch .S41\n.S43:\ns_mov_b32 s7, 262144\n.S45:\nv_and_b32 v27, 0x70, v1\nbuffer_load_dword v27, v27, s[16:19], s61 offen offset:140 sc0 sc1\nWV 0\n"
"v_cmp_ne_u32 vcc, s15, v27\ns_nop 0\ns_cbranch_vccz .S44\ns_sleep 6\ns_sub_u32 s7, s7, 1\ns_cbranch_scc0 .S45\ns_branch .S44\n.S46:\ns_mov_b32 s7, 262144\n.S48:\n"
"v_and_b32 v27, 0x70, v1\nbuffer_load_dword v27, v27, s[16:19], s61 offen offset:12 sc0 sc1\nWV 0\nv_cmp_ne_u32 vcc, s15, v27\ns_nop 0\ns_cbranch_vccz .S47\ns_sleep 6\n"
"s_sub_u32 s7, s7, 1\ns_cbranch_scc0 .S48\ns_branch .S47\n.S49:\ns_mov_b32 s7, 262144\n.S51:\nv_and_b32 v27, 0x70, v1\n"
"buffer_load_dword v27, v27, s[16:19], s61 offen offset:8 sc0 sc1\nWV 0\nv_cmp_ne_u32 vcc, s15, v27\ns_nop 0\ns_cbranch_vccz .S50\ns_sleep 6\ns_sub_u32 s7, s7, 1\n"
"s_cbranch_scc0 .S51\ns_branch .S50\n.S52:\ns_mov_b32 s7, 262144\n.S54:\nv_and_b32 v27, 0xf0, v1\nbuffer_load_dword v27, v27, s[16:19], s61 offen offset:4 sc0 sc1\nWV 0\n"
"v_cmp_ne_u32 vcc, s15, v27\ns_nop 0\ns_cbranch_vccz .S53\ns_sleep 6\ns_sub_u32 s7, s7, 1\ns_cbranch_scc0 .S54\ns_branch .S53\n.S56:\ns_mov_b32 s7, 262144\n.S58:\n"
"v_lshrrev_b32 v144, 9, v1\nv_mul_u32_u24 v144, 0x18000, v144\nv_lshlrev_b32 v144, 8, v144\nv_and_b32 v145, 0x1f0, v1\nv_add_u32 v144, v144, v145\ns_mov_b32 s6, 0x15c0000\n"
"buffer_load_dword v136, v144, s[16:19], s6 offen offset:4 sc0 sc1\ns_mov_b32 s6, 0x15c2000\nbuffer_load_dword v137, v144, s[16:19], s6 offen offset:4 sc0 sc1\n"
"s_mov_b32 s6, 0x45c0000\nbuffer_load_dword v138, v144, s[16:19], s6 offen offset:4 sc0 sc1\ns_mov_b32 s6, 0x45c2000\n"
"buffer_load_dword v139, v144, s[16:19], s6 offen offset:4 sc0 sc1\ns_mov_b32 s6, 0x75c0000\nbuffer_load_dword v140, v144, s[16:19], s6 offen offset:4 sc0 sc1\n"
"s_mov_b32 s6, 0x75c2000\nbuffer_load_dword v141, v144, s[16:19], s6 offen offset:4 sc0 sc1\ns_mov_b32 s6, 0xa5c0000\n"
"buffer_load_dword v142, v144, s[16:19], s6 offen offset:4 sc0 sc1\ns_mov_b32 s6, 0xa5c2000\nbuffer_load_dword v143, v144, s[16:19], s6 offen offset:4 sc0 sc1\nWV 0\n"
"v_xor_b32 v136, s15, v136\nv_xor_b32 v137, s15, v137\nv_xor_b32 v138, s15, v138\nv_xor_b32 v139, s15, v139\nv_xor_b32 v140, s15, v140\nv_xor_b32 v141, s15, v141\n"
"v_xor_b32 v142, s15, v142\nv_xor_b32 v143, s15, v143\nv_min_u32 v136, v136, v137\nv_min_u32 v138, v138, v139\nv_min_u32 v140, v140, v141\nv_min_u32 v142, v142, v143\n"
"v_or3_b32 v136, v136, v138, v140\nv_or_b32 v136, v136, v142\nv_cmp_ne_u32 vcc, 0, v136\ns_nop 0\ns_cbranch_vccz .S57\ns_sleep 6\ns_sub_u32 s7, s7, 1\ns_cbranch_scc0 .S58\n"
"s_branch .S57\n.FF55:\nv_lshrrev_b32 v144, 9, v1\nv_mul_u32_u24 v144, 0x18000, v144\nv_lshlrev_b32 v144, 8, v144\nv_and_b32 v145, 0x1f0, v1\nv_add_u32 v144, v144, v145\n"
"s_mov_b32 s6, 0x15c0000\nbuffer_load_dword v136, v144, s[16:19], s6 offen offset:4 sc0 sc1\ns_mov_b32 s6, 0x15c2000\n"
"buffer_load_dword v137, v144, s[16:19], s6 offen offset:4 sc0 sc1\ns_mov_b32 s6, 0x45c0000\nbuffer_load_dword v138, v144, s[16:19], s6 offen offset:4 sc0 sc1\n"
"s_mov_b32 s6, 0x45c2000\nbuffer_load_dword v139, v144, s[16:19], s6 offen offset:4 sc0 sc1\ns_mov_b32 s6, 0x75c0000\n"
"buffer_load_dword v140, v144, s[16:19], s6 offen offset:4 sc0 sc1\ns_mov_b32 s6, 0x75c2000\nbuffer_load_dword v141, v144, s[16:19], s6 offen offset:4 sc0 sc1\n"
"s_mov_b32 s6, 0xa5c0000\nbuffer_load_dword v142, v144, s[16:19], s6 offen offset:4 sc0 sc1\ns_mov_b32 s6, 0xa5c2000\n"
"buffer_load_dword v143, v144, s[16:19], s6 offen offset:4 sc0 sc1\ns_mov_b32 s90, 0\ns_swappc_b64 s[8:9], s[80:81]\nv_xor_b32 v136, s15, v136\nv_xor_b32 v137, s15, v137\n"
"v_xor_b32 v138, s15, v138\nv_xor_b32 v139, s15, v139\nv_xor_b32 v140, s15, v140\nv_xor_b32 v141, s15, v141\nv_xor_b32 v142, s15, v142\nv_xor_b32 v143, s15, v143\n"
"v_min_u32 v136, v136, v137\nv_min_u32 v138, v138, v139\nv_min_u32 v140, v140, v141\nv_min_u32 v142, v142, v143\nv_or3_b32 v136, v136, v138, v140\nv_or_b32 v136, v136, v142\n"
"v_cmp_ne_u32 vcc, 0, v136\ns_nop 0\ns_cbranch_vccnz .S56\n.S57:\ns_and_b32 s4, s13, 1\ns_lshl1_add_u32 s4, s4, s11\ns_lshl2_add_u32 s4, s12, s4\ns_lshr_b32 s5, s71, 16\n"
"s_cmp_lt_u32 s4, 28\ns_cselect_b32 s5, s71, s5\ns_cselect_b32 s3, 0, 28\ns_and_b32 s5, s5, 0x7ff\ns_sub_u32 s3, s4, s3\ns_mul_i32 s2, s4, 7168\ns_add_u32 s2, s2, 0xc04c800\n"
"s_add_u32 s6, s2, 4096\nbuffer_load_dwordx4 v[136:139], v1, s[16:19], s2 offen offset:0 sc0 sc1\nbuffer_load_dwordx4 v[140:143], v1, s[16:19], s2 offen offset:1024 sc0 sc1\n"
"buffer_load_dwordx4 v[144:147], v1, s[16:19], s2 offen offset:2048 sc0 sc1\nbuffer_load_dwordx4 v[148:151], v1, s[16:19], s2 offen offset:3072 sc0 sc1\n"
"buffer_load_dwordx4 v[152:155], v1, s[16:19], s6 offen offset:0 sc0 sc1\nbuffer_load_dwordx4 v[156:159], v1, s[16:19], s6 offen offset:1024 sc0 sc1\n"
"buffer_load_dwordx4 v[160:163], v1, s[16:19], s6 offen offset:2048 sc0 sc1\nv_lshrrev_b32 v167, 1, v1\nv_and_b32 v167, 24, v167\ns_mul_i32 s2, s5, 896\ns_lshl_b32 s3, s3, 5\n"
"s_add_u32 s2, s2, s3\ns_lshl2_add_u32 s5, s5, 0xc03e000\nbuffer_load_dwordx2 v[164:165], v167, s[28:31], s2 offen\nbuffer_load_dword v166, off, s[16:19], s5 sc0 sc1\n"
"s_mov_b32 s90, 0\ns_swappc_b64 s[8:9], s[80:81]\nAD 136,136,140\nAD 137,137,141\nAD 138,138,142\nAD 139,139,143\nAD 144,144,148\nAD 145,145,149\nAD 146,146,150\nAD 147,147,151\n"
"AD 152,152,156\nAD 153,153,157\nAD 154,154,158\nAD 155,155,159\nAD 136,136,144\nAD 137,137,145\nAD 138,138,146\nAD 139,139,147\nAD 152,152,160\nAD 153,153,161\nAD 154,154,162\n"
"AD 155,155,163\nAD 136,136,152\nAD 137,137,153\nAD 138,138,154\nAD 139,139,155\nv_mov_b32 v140, v136\nv_mov_b32 v141, v137\nv_mov_b32 v142, v138\nv_mov_b32 v143, v139\n"
"v_permlane32_swap_b32 v136, v140\nv_permlane32_swap_b32 v137, v141\nv_permlane32_swap_b32 v138, v142\nv_permlane32_swap_b32 v139, v143\nAD 136,136,140\nAD 137,137,141\n"
"AD 138,138,142\nAD 139,139,143\nv_mov_b32 v140, v136\nv_mov_b32 v141, v137\nv_mov_b32 v142, v138\nv_mov_b32 v143, v139\nv_permlane16_swap_b32 v136, v140\n"
"v_permlane16_swap_b32 v137, v141\nv_permlane16_swap_b32 v138, v142\nv_permlane16_swap_b32 v139, v143\nAD 136,136,140\nAD 137,137,141\nAD 138,138,142\nAD 139,139,143\n"
"v_add_f32_dpp v136, v136, v136 row_ror:8 row_mask:0xf bank_mask:0xf\nv_add_f32_dpp v137, v137, v137 row_ror:8 row_mask:0xf bank_mask:0xf\n"
"v_add_f32_dpp v138, v138, v138 row_ror:8 row_mask:0xf bank_mask:0xf\nv_add_f32_dpp v139, v139, v139 row_ror:8 row_mask:0xf bank_mask:0xf\n"
"v_add_f32_dpp v136, v136, v136 row_ror:4 row_mask:0xf bank_mask:0xf\nv_add_f32_dpp v137, v137, v137 row_ror:4 row_mask:0xf bank_mask:0xf\n"
"v_add_f32_dpp v138, v138, v138 row_ror:4 row_mask:0xf bank_mask:0xf\nv_add_f32_dpp v139, v139, v139 row_ror:4 row_mask:0xf bank_mask:0xf\nv_mul_f32 v166, 0xbfb8aa3b, v166\n"
"EX 166,166\nCV 136,136,137\nv_add_f32 v166, 1.0, v166\nv_rcp_f32 v166, v166\nCV 138,138,139\nCV 166,166,166\nv_lshlrev_b32 v166, 16, v166\nv_and_b32 v137, 0xffff0000, v136\n"
"v_lshlrev_b32 v136, 16, v136\nv_and_b32 v139, 0xffff0000, v138\nv_lshlrev_b32 v138, 16, v138\nMU 136,136,166\nMU 137,137,166\nMU 138,138,166\nMU 139,139,166\nCV 136,136,137\n"
"CV 138,138,139\nv_and_b32 v137, 0xffff0000, v136\nv_lshlrev_b32 v136, 16, v136\nv_and_b32 v139, 0xffff0000, v138\nv_lshlrev_b32 v138, 16, v138\nv_lshlrev_b32 v140, 16, v164\n"
"v_and_b32 v141, 0xffff0000, v164\nv_lshlrev_b32 v142, 16, v165\nv_and_b32 v143, 0xffff0000, v165\nAD 136,136,140\nAD 137,137,141\nAD 138,138,142\nAD 139,139,143\nCV 136,136,137\n"
"CV 137,138,139\nv_mov_b32 v138, s15\nv_mov_b32 v139, s71\ns_and_b32 s4, s13, 1\ns_lshl1_add_u32 s4, s4, s11\ns_lshl2_add_u32 s4, s12, s4\ns_lshr_b32 s5, s71, 16\n"
"s_cmp_lt_u32 s4, 28\ns_cselect_b32 s5, s71, s5\ns_cselect_b32 s3, 0, 28\ns_and_b32 s5, s5, 0x7ff\ns_sub_u32 s3, s4, s3\nv_mov_b32 v140, 0x40000000\nv_cmp_gt_u32 vcc, 64, v1\n"
"s_lshl_b32 s4, s4, 6\ns_add_u32 s4, s4, 0xc04b800\nv_cndmask_b32 v140, v140, v1, vcc\nSv 136,140,16,4,0\ns_mov_b32 s90, 100\ns_swappc_b64 s[8:9], s[80:81]\n.T63:\n"
"s_mov_b32 s62, 0\ns_call_b64 s[80:81], .RB11\ns_branch .T64\n.T65:\ns_mov_b32 s62, 0\ns_call_b64 s[80:81], .RA10\ns_branch .T66\n.T69:\ns_mov_b32 s62, 0\n"
"s_call_b64 s[80:81], .RB11\ns_branch .T70\n.T71:\ns_mov_b32 s62, 0\ns_call_b64 s[80:81], .RA10\ns_branch .T72\n.T74:\ns_mov_b32 s62, 0\ns_call_b64 s[80:81], .RB11\ns_branch .T75\n"
".T76:\ns_mov_b32 s62, 0\ns_call_b64 s[80:81], .RA10\ns_branch .T77\n.S82:\ns_mov_b32 s7, 262144\n.S84:\nbuffer_load_dwordx4 v[100:103], v108, s[16:19], s65 offen offset:0 sc0 sc1\n"
"buffer_load_dwordx4 v[104:107], v108, s[16:19], s65 offen offset:256 sc0 sc1\nWV 0\nv_xor_b32 v109, s15, v102\nv_xor_b32 v110, s15, v106\nv_or_b32 v109, v109, v110\n"
"v_cmp_ne_u32 vcc, 0, v109\ns_nop 0\ns_cbranch_vccz .S83\ns_sleep 6\ns_sub_u32 s7, s7, 1\ns_cbranch_scc0 .S84\ns_branch .S83\n.D80:\nv_mov_b32 v130, 0\nv_mov_b32 v131, 0\n"
"s_mov_b32 s63, 0xff800000\ns_mov_b32 s67, 1\ns_branch .D81\n.T86:\ns_lshl3_add_u32 s62, s67, 2\ns_call_b64 s[80:81], .RB11\ns_cmp_eq_u32 s67, 0\ns_cbranch_scc1 .T87\nSU 20,100,88\n"
"v_min_f32 v20, 0, v20\nEX 20,20\ns_nop 0\nFM 86,101,20\nSU 21,104,89\nv_min_f32 v21, 0, v21\nEX 21,21\ns_nop 0\nFM 87,105,21\nAR 22,192\nv_and_b32 v23, 0xffff0000, v22\n"
"v_lshlrev_b32 v22, 16, v22\nMU 22,22,20\nMU 23,23,20\nCV 22,22,23\nAW 192,22\nAR 22,193\nv_and_b32 v23, 0xffff0000, v22\nv_lshlrev_b32 v22, 16, v22\nMU 22,22,20\nMU 23,23,20\n"
"CV 22,22,23\nAW 193,22\nAR 22,194\nv_and_b32 v23, 0xffff0000, v22\nv_lshlrev_b32 v22, 16, v22\nMU 22,22,20\nMU 23,23,20\nCV 22,22,23\nAW 194,22\nAR 22,195\n"
"v_and_b32 v23, 0xffff0000, v22\nv_lshlrev_b32 v22, 16, v22\nMU 22,22,20\nMU 23,23,20\nCV 22,22,23\nAW 195,22\nAR 22,196\nv_and_b32 v23, 0xffff0000, v22\n"
"v_lshlrev_b32 v22, 16, v22\nMU 22,22,21\nMU 23,23,21\nCV 22,22,23\nAW 196,22\nAR 22,197\nv_and_b32 v23, 0xffff0000, v22\nv_lshlrev_b32 v22, 16, v22\nMU 22,22,21\nMU 23,23,21\n"
"CV 22,22,23\nAW 197,22\nAR 22,198\nv_and_b32 v23, 0xffff0000, v22\nv_lshlrev_b32 v22, 16, v22\nMU 22,22,21\nMU 23,23,21\nCV 22,22,23\nAW 198,22\nAR 22,199\n"
"v_and_b32 v23, 0xffff0000, v22\nv_lshlrev_b32 v22, 16, v22\nMU 22,22,21\nMU 23,23,21\nCV 22,22,23\nAW 199,22\nAR 22,200\nv_and_b32 v23, 0xffff0000, v22\n"
"v_lshlrev_b32 v22, 16, v22\nMU 22,22,20\nMU 23,23,20\nCV 22,22,23\nAW 200,22\nAR 22,201\nv_and_b32 v23, 0xffff0000, v22\nv_lshlrev_b32 v22, 16, v22\nMU 22,22,20\nMU 23,23,20\n"
"CV 22,22,23\nAW 201,22\nAR 22,202\nv_and_b32 v23, 0xffff0000, v22\nv_lshlrev_b32 v22, 16, v22\nMU 22,22,20\nMU 23,23,20\nCV 22,22,23\nAW 202,22\nAR 22,203\n"
"v_and_b32 v23, 0xffff0000, v22\nv_lshlrev_b32 v22, 16, v22\nMU 22,22,20\nMU 23,23,20\nCV 22,22,23\nAW 203,22\nAR 22,204\nv_and_b32 v23, 0xffff0000, v22\n"
"v_lshlrev_b32 v22, 16, v22\nMU 22,22,21\nMU 23,23,21\nCV 22,22,23\nAW 204,22\nAR 22,205\nv_and_b32 v23, 0xffff0000, v22\nv_lshlrev_b32 v22, 16, v22\nMU 22,22,21\nMU 23,23,21\n"
"CV 22,22,23\nAW 205,22\nAR 22,206\nv_and_b32 v23, 0xffff0000, v22\nv_lshlrev_b32 v22, 16, v22\nMU 22,22,21\nMU 23,23,21\nCV 22,22,23\nAW 206,22\nAR 22,207\n"
"v_and_b32 v23, 0xffff0000, v22\nv_lshlrev_b32 v22, 16, v22\nMU 22,22,21\nMU 23,23,21\nCV 22,22,23\nAW 207,22\nAR 22,208\nv_and_b32 v23, 0xffff0000, v22\n"
"v_lshlrev_b32 v22, 16, v22\nMU 22,22,20\nMU 23,23,20\nCV 22,22,23\nAW 208,22\nAR 22,209\nv_and_b32 v23, 0xffff0000, v22\nv_lshlrev_b32 v22, 16, v22\nMU 22,22,20\nMU 23,23,20\n"
"CV 22,22,23\nAW 209,22\nAR 22,210\nv_and_b32 v23, 0xffff0000, v22\nv_lshlrev_b32 v22, 16, v22\nMU 22,22,20\nMU 23,23,20\nCV 22,22,23\nAW 210,22\nAR 22,211\n"
"v_and_b32 v23, 0xffff0000, v22\nv_lshlrev_b32 v22, 16, v22\nMU 22,22,20\nMU 23,23,20\nCV 22,22,23\nAW 211,22\nAR 22,212\nv_and_b32 v23, 0xffff0000, v22\n"
"v_lshlrev_b32 v22, 16, v22\nMU 22,22,21\nMU 23,23,21\nCV 22,22,23\nAW 212,22\nAR 22,213\nv_and_b32 v23, 0xffff0000, v22\nv_lshlrev_b32 v22, 16, v22\nMU 22,22,21\nMU 23,23,21\n"
"CV 22,22,23\nAW 213,22\nAR 22,214\nv_and_b32 v23, 0xffff0000, v22\nv_lshlrev_b32 v22, 16, v22\nMU 22,22,21\nMU 23,23,21\nCV 22,22,23\nAW 214,22\nAR 22,215\n"
"v_and_b32 v23, 0xffff0000, v22\nv_lshlrev_b32 v22, 16, v22\nMU 22,22,21\nMU 23,23,21\nCV 22,22,23\nAW 215,22\nAR 22,216\nv_and_b32 v23, 0xffff0000, v22\n"
"v_lshlrev_b32 v22, 16, v22\nMU 22,22,20\nMU 23,23,20\nCV 22,22,23\nAW 216,22\nAR 22,217\nv_and_b32 v23, 0xffff0000, v22\nv_lshlrev_b32 v22, 16, v22\nMU 22,22,20\nMU 23,23,20\n"
"CV 22,22,23\nAW 217,22\nAR 22,218\nv_and_b32 v23, 0xffff0000, v22\nv_lshlrev_b32 v22, 16, v22\nMU 22,22,20\nMU 23,23,20\nCV 22,22,23\nAW 218,22\nAR 22,219\n"
"v_and_b32 v23, 0xffff0000, v22\nv_lshlrev_b32 v22, 16, v22\nMU 22,22,20\nMU 23,23,20\nCV 22,22,23\nAW 219,22\nAR 22,220\nv_and_b32 v23, 0xffff0000, v22\n"
"v_lshlrev_b32 v22, 16, v22\nMU 22,22,21\nMU 23,23,21\nCV 22,22,23\nAW 220,22\nAR 22,221\nv_and_b32 v23, 0xffff0000, v22\nv_lshlrev_b32 v22, 16, v22\nMU 22,22,21\nMU 23,23,21\n"
"CV 22,22,23\nAW 221,22\nAR 22,222\nv_and_b32 v23, 0xffff0000, v22\nv_lshlrev_b32 v22, 16, v22\nMU 22,22,21\nMU 23,23,21\nCV 22,22,23\nAW 222,22\nAR 22,223\n"
"v_and_b32 v23, 0xffff0000, v22\nv_lshlrev_b32 v22, 16, v22\nMU 22,22,21\nMU 23,23,21\nCV 22,22,23\nAW 223,22\nAR 22,224\nv_and_b32 v23, 0xffff0000, v22\n"
"v_lshlrev_b32 v22, 16, v22\nMU 22,22,20\nMU 23,23,20\nCV 22,22,23\nAW 224,22\nAR 22,225\nv_and_b32 v23, 0xffff0000, v22\nv_lshlrev_b32 v22, 16, v22\nMU 22,22,20\nMU 23,23,20\n"
"CV 22,22,23\nAW 225,22\nAR 22,226\nv_and_b32 v23, 0xffff0000, v22\nv_lshlrev_b32 v22, 16, v22\nMU 22,22,20\nMU 23,23,20\nCV 22,22,23\nAW 226,22\nAR 22,227\n"
"v_and_b32 v23, 0xffff0000, v22\nv_lshlrev_b32 v22, 16, v22\nMU 22,22,20\nMU 23,23,20\nCV 22,22,23\nAW 227,22\nAR 22,228\nv_and_b32 v23, 0xffff0000, v22\n"
"v_lshlrev_b32 v22, 16, v22\nMU 22,22,21\nMU 23,23,21\nCV 22,22,23\nAW 228,22\nAR 22,229\nv_and_b32 v23, 0xffff0000, v22\nv_lshlrev_b32 v22, 16, v22\nMU 22,22,21\nMU 23,23,21\n"
"CV 22,22,23\nAW 229,22\nAR 22,230\nv_and_b32 v23, 0xffff0000, v22\nv_lshlrev_b32 v22, 16, v22\nMU 22,22,21\nMU 23,23,21\nCV 22,22,23\nAW 230,22\nAR 22,231\n"
"v_and_b32 v23, 0xffff0000, v22\nv_lshlrev_b32 v22, 16, v22\nMU 22,22,21\nMU 23,23,21\nCV 22,22,23\nAW 231,22\nAR 22,232\nv_and_b32 v23, 0xffff0000, v22\n"
"v_lshlrev_b32 v22, 16, v22\nMU 22,22,20\nMU 23,23,20\nCV 22,22,23\nAW 232,22\nAR 22,233\nv_and_b32 v23, 0xffff0000, v22\nv_lshlrev_b32 v22, 16, v22\nMU 22,22,20\nMU 23,23,20\n"
"CV 22,22,23\nAW 233,22\nAR 22,234\nv_and_b32 v23, 0xffff0000, v22\nv_lshlrev_b32 v22, 16, v22\nMU 22,22,20\nMU 23,23,20\nCV 22,22,23\nAW 234,22\nAR 22,235\n"
"v_and_b32 v23, 0xffff0000, v22\nv_lshlrev_b32 v22, 16, v22\nMU 22,22,20\nMU 23,23,20\nCV 22,22,23\nAW 235,22\nAR 22,236\nv_and_b32 v23, 0xffff0000, v22\n"
"v_lshlrev_b32 v22, 16, v22\nMU 22,22,21\nMU 23,23,21\nCV 22,22,23\nAW 236,22\nAR 22,237\nv_and_b32 v23, 0xffff0000, v22\nv_lshlrev_b32 v22, 16, v22\nMU 22,22,21\nMU 23,23,21\n"
"CV 22,22,23\nAW 237,22\nAR 22,238\nv_and_b32 v23, 0xffff0000, v22\nv_lshlrev_b32 v22, 16, v22\nMU 22,22,21\nMU 23,23,21\nCV 22,22,23\nAW 238,22\nAR 22,239\n"
"v_and_b32 v23, 0xffff0000, v22\nv_lshlrev_b32 v22, 16, v22\nMU 22,22,21\nMU 23,23,21\nCV 22,22,23\nAW 239,22\nAR 22,240\nv_and_b32 v23, 0xffff0000, v22\n"
"v_lshlrev_b32 v22, 16, v22\nMU 22,22,20\nMU 23,23,20\nCV 22,22,23\nAW 240,22\nAR 22,241\nv_and_b32 v23, 0xffff0000, v22\nv_lshlrev_b32 v22, 16, v22\nMU 22,22,20\nMU 23,23,20\n"
"CV 22,22,23\nAW 241,22\nAR 22,242\nv_and_b32 v23, 0xffff0000, v22\nv_lshlrev_b32 v22, 16, v22\nMU 22,22,20\nMU 23,23,20\nCV 22,22,23\nAW 242,22\nAR 22,243\n"
"v_and_b32 v23, 0xffff0000, v22\nv_lshlrev_b32 v22, 16, v22\nMU 22,22,20\nMU 23,23,20\nCV 22,22,23\nAW 243,22\nAR 22,244\nv_and_b32 v23, 0xffff0000, v22\n"
"v_lshlrev_b32 v22, 16, v22\nMU 22,22,21\nMU 23,23,21\nCV 22,22,23\nAW 244,22\nAR 22,245\nv_and_b32 v23, 0xffff0000, v22\nv_lshlrev_b32 v22, 16, v22\nMU 22,22,21\nMU 23,23,21\n"
"CV 22,22,23\nAW 245,22\nAR 22,246\nv_and_b32 v23, 0xffff0000, v22\nv_lshlrev_b32 v22, 16, v22\nMU 22,22,21\nMU 23,23,21\nCV 22,22,23\nAW 246,22\nAR 22,247\n"
"v_and_b32 v23, 0xffff0000, v22\nv_lshlrev_b32 v22, 16, v22\nMU 22,22,21\nMU 23,23,21\nCV 22,22,23\nAW 247,22\nAR 22,248\nv_and_b32 v23, 0xffff0000, v22\n"
"v_lshlrev_b32 v22, 16, v22\nMU 22,22,20\nMU 23,23,20\nCV 22,22,23\nAW 248,22\nAR 22,249\nv_and_b32 v23, 0xffff0000, v22\nv_lshlrev_b32 v22, 16, v22\nMU 22,22,20\nMU 23,23,20\n"
"CV 22,22,23\nAW 249,22\nAR 22,250\nv_and_b32 v23, 0xffff0000, v22\nv_lshlrev_b32 v22, 16, v22\nMU 22,22,20\nMU 23,23,20\nCV 22,22,23\nAW 250,22\nAR 22,251\n"
"v_and_b32 v23, 0xffff0000, v22\nv_lshlrev_b32 v22, 16, v22\nMU 22,22,20\nMU 23,23,20\nCV 22,22,23\nAW 251,22\nAR 22,252\nv_and_b32 v23, 0xffff0000, v22\n"
"v_lshlrev_b32 v22, 16, v22\nMU 22,22,21\nMU 23,23,21\nCV 22,22,23\nAW 252,22\nAR 22,253\nv_and_b32 v23, 0xffff0000, v22\nv_lshlrev_b32 v22, 16, v22\nMU 22,22,21\nMU 23,23,21\n"
"CV 22,22,23\nAW 253,22\nAR 22,254\nv_and_b32 v23, 0xffff0000, v22\nv_lshlrev_b32 v22, 16, v22\nMU 22,22,21\nMU 23,23,21\nCV 22,22,23\nAW 254,22\nAR 22,255\n"
"v_and_b32 v23, 0xffff0000, v22\nv_lshlrev_b32 v22, 16, v22\nMU 22,22,21\nMU 23,23,21\nCV 22,22,23\nAW 255,22\nv_lshrrev_b32 v22, 8, v1\nv_bfe_u32 v23, v1, 6, 2\n"
"v_cmp_eq_u32 vcc, v22, v23\nv_and_b32 v22, 16, v1\nv_mov_b32 v23, 0x3f80\nv_cndmask_b32 v23, 0, v23, vcc\nv_lshlrev_b32 v23, v22, v23\nv_and_b32 v22, 32, v1\n"
"v_cmp_eq_u32 vcc, 0, v22\nv_mov_b32 v128, 0\nv_mov_b32 v129, 0\nv_cndmask_b32 v130, 0, v23, vcc\nv_sub_u32 v131, v23, v130\nv_mov_b32 v132, 0\nv_mov_b32 v133, 0\ns_nop 1\n"
"Mava 0,130,192\nMava 8,128,192\nMava 12,128,196\nMava 4,130,196\nMava 16,130,200\nMava 24,128,200\nMava 28,128,204\nMava 20,130,204\nMava 32,130,208\nMava 40,128,208\n"
"Mava 44,128,212\nMava 36,130,212\nMava 48,130,216\nMava 56,128,216\nMava 60,128,220\nMava 52,130,220\nMava 64,130,224\nMava 72,128,224\nMava 76,128,228\nMava 68,130,228\n"
"Mava 80,130,232\nMava 88,128,232\nMava 92,128,236\nMava 84,130,236\nMava 96,130,240\nMava 104,128,240\nMava 108,128,244\nMava 100,130,244\nMava 112,130,248\nMava 120,128,248\n"
"Mava 124,128,252\nMava 116,130,252\ns_nop 7\ns_branch .T87\n.S89:\ns_mov_b32 s7, 262144\n.S91:\ns_add_u32 s0, s92, 0\n"
"buffer_load_dwordx4 v[208:211], v1, s[16:19], s0 offen offset:0 sc0 sc1\nbuffer_load_dwordx4 v[212:215], v1, s[16:19], s0 offen offset:1024 sc0 sc1\nWV 0\n"
"v_max_u32 v21, v211, v215\nv_cmp_eq_u32 vcc, -1, v21\ns_nop 0\ns_cbranch_vccz .S90\ns_sleep 6\ns_sub_u32 s7, s7, 1\ns_cbranch_scc0 .S91\ns_branch .S90\n.S92:\n"
"s_mov_b32 s7, 262144\n.S94:\ns_add_u32 s0, s92, 131072\nbuffer_load_dwordx4 v[216:219], v1, s[16:19], s0 offen offset:0 sc0 sc1\n"
"buffer_load_dwordx4 v[220:223], v1, s[16:19], s0 offen offset:1024 sc0 sc1\nWV 0\nv_max_u32 v21, v219, v223\nv_cmp_eq_u32 vcc, -1, v21\ns_nop 0\ns_cbranch_vccz .S93\ns_sleep 6\n"
"s_sub_u32 s7, s7, 1\ns_cbranch_scc0 .S94\ns_branch .S93\n.S95:\ns_mov_b32 s7, 262144\n.S97:\ns_add_u32 s0, s92, 262144\n"
"buffer_load_dwordx4 v[224:227], v1, s[16:19], s0 offen offset:0 sc0 sc1\nbuffer_load_dwordx4 v[228:231], v1, s[16:19], s0 offen offset:1024 sc0 sc1\nWV 0\n"
"v_max_u32 v21, v227, v231\nv_cmp_eq_u32 vcc, -1, v21\ns_nop 0\ns_cbranch_vccz .S96\ns_sleep 6\ns_sub_u32 s7, s7, 1\ns_cbranch_scc0 .S97\ns_branch .S96\n.S98:\n"
"s_mov_b32 s7, 262144\n.S100:\ns_add_u32 s0, s92, 393216\nbuffer_load_dwordx4 v[208:211], v1, s[16:19], s0 offen offset:0 sc0 sc1\n"
"buffer_load_dwordx4 v[212:215], v1, s[16:19], s0 offen offset:1024 sc0 sc1\nWV 0\nv_max_u32 v21, v211, v215\nv_cmp_eq_u32 vcc, -1, v21\ns_nop 0\ns_cbranch_vccz .S99\ns_sleep 6\n"
"s_sub_u32 s7, s7, 1\ns_cbranch_scc0 .S100\ns_branch .S99\n.S101:\ns_mov_b32 s7, 262144\n.S103:\ns_add_u32 s0, s92, 524288\n"
"buffer_load_dwordx4 v[216:219], v1, s[16:19], s0 offen offset:0 sc0 sc1\nbuffer_load_dwordx4 v[220:223], v1, s[16:19], s0 offen offset:1024 sc0 sc1\nWV 0\n"
"v_max_u32 v21, v219, v223\nv_cmp_eq_u32 vcc, -1, v21\ns_nop 0\ns_cbranch_vccz .S102\ns_sleep 6\ns_sub_u32 s7, s7, 1\ns_cbranch_scc0 .S103\ns_branch .S102\n.S104:\n"
"s_mov_b32 s7, 262144\n.S106:\ns_add_u32 s0, s92, 655360\nbuffer_load_dwordx4 v[224:227], v1, s[16:19], s0 offen offset:0 sc0 sc1\n"
"buffer_load_dwordx4 v[228:231], v1, s[16:19], s0 offen offset:1024 sc0 sc1\nWV 0\nv_max_u32 v21, v227, v231\nv_cmp_eq_u32 vcc, -1, v21\ns_nop 0\ns_cbranch_vccz .S105\ns_sleep 6\n"
"s_sub_u32 s7, s7, 1\ns_cbranch_scc0 .S106\ns_branch .S105\n.S107:\ns_mov_b32 s7, 262144\n.S109:\ns_add_u32 s0, s92, 786432\n"
"buffer_load_dwordx4 v[208:211], v1, s[16:19], s0 offen offset:0 sc0 sc1\nbuffer_load_dwordx4 v[212:215], v1, s[16:19], s0 offen offset:1024 sc0 sc1\nWV 0\n"
"v_max_u32 v21, v211, v215\nv_cmp_eq_u32 vcc, -1, v21\ns_nop 0\ns_cbranch_vccz .S108\ns_sleep 6\ns_sub_u32 s7, s7, 1\ns_cbranch_scc0 .S109\ns_branch .S108\n.S110:\n"
"s_mov_b32 s7, 262144\n.S112:\nbuffer_load_dwordx4 v[64:67], v89, s[16:19], s40 offen offset:0 sc0 sc1\nbuffer_load_dwordx4 v[68:71], v89, s[16:19], s40 offen offset:16 sc0 sc1\n"
"buffer_load_dwordx4 v[72:75], v89, s[16:19], s40 offen offset:1792 sc0 sc1\nbuffer_load_dwordx4 v[76:79], v89, s[16:19], s40 offen offset:1808 sc0 sc1\n"
"buffer_load_dwordx2 v[80:81], v88, s[16:19], s63 offen offset:0 sc0 sc1\nbuffer_load_dwordx2 v[82:83], v88, s[16:19], s63 offen offset:512 sc0 sc1\nWV 0\nv_xor_b32 v66, s15, v66\n"
"v_xor_b32 v70, s15, v70\nv_xor_b32 v74, s15, v74\nv_xor_b32 v78, s15, v78\nv_xor_b32 v81, s15, v81\nv_xor_b32 v83, s15, v83\nv_or3_b32 v92, v66, v70, v74\n"
"v_or3_b32 v92, v92, v78, v81\nv_or_b32 v92, v92, v83\nv_cmp_ne_u32 vcc, 0, v92\ns_nop 0\ns_cbranch_vccz .S111\ns_sleep 6\ns_sub_u32 s7, s7, 1\ns_cbranch_scc0 .S112\n"
"s_branch .S111\n.S113:\ns_mov_b32 s7, 262144\n.S115:\nbuffer_load_dword v27, off, s[16:19], s60 offset:512 sc0 sc1\nWV 0\nv_cmp_ne_u32 vcc, s15, v27\ns_nop 0\n"
"s_cbranch_vccz .S114\ns_sleep 6\ns_sub_u32 s7, s7, 1\ns_cbranch_scc0 .S115\ns_branch .S114\n.S116:\ns_mov_b32 s7, 262144\n.S118:\n"
"buffer_load_dword v27, v24, s[16:19], s41 offen sc0 sc1\nWV 0\nv_xor_b32 v27, s15, v27\nv_and_b32 v26, 0xffffdfff, v27\nv_cmp_ne_u32 s[64:65], 0, v27\nv_cmp_ne_u32 vcc, 0, v26\n"
"s_nop 0\ns_cbranch_vccz .S117\ns_sleep 6\ns_sub_u32 s7, s7, 1\ns_cbranch_scc0 .S118\ns_branch .S117\n.Z120:\ns_sub_u32 s7, s7, 1\ns_cbranch_scc1 .Z121\ns_cmp_lg_u64 s[44:45], 0\n"
"s_cbranch_scc0 .Z123\nbuffer_load_dwordx4 v[96:99], v1, s[16:19], s24 offen offset:0 sc0 sc1\nbuffer_load_dwordx4 v[100:103], v1, s[16:19], s24 offen offset:1024 sc0 sc1\n.Z123:\n"
"s_cmp_lg_u64 s[46:47], 0\ns_cbranch_scc0 .Z124\nbuffer_load_dwordx4 v[104:107], v1, s[16:19], s25 offen offset:0 sc0 sc1\n"
"buffer_load_dwordx4 v[108:111], v1, s[16:19], s25 offen offset:1024 sc0 sc1\n.Z124:\ns_cmp_lg_u64 s[48:49], 0\ns_cbranch_scc0 .Z125\n"
"buffer_load_dwordx4 v[112:115], v1, s[16:19], s26 offen offset:0 sc0 sc1\nbuffer_load_dwordx4 v[116:119], v1, s[16:19], s26 offen offset:1024 sc0 sc1\n.Z125:\n"
"s_cmp_lg_u64 s[50:51], 0\ns_cbranch_scc0 .Z126\nbuffer_load_dwordx4 v[120:123], v1, s[16:19], s27 offen offset:0 sc0 sc1\n"
"buffer_load_dwordx4 v[124:127], v1, s[16:19], s27 offen offset:1024 sc0 sc1\n.Z126:\ns_cmp_lg_u64 s[52:53], 0\ns_cbranch_scc0 .Z127\n"
"buffer_load_dwordx4 v[32:35], v1, s[16:19], s32 offen offset:0 sc0 sc1\nbuffer_load_dwordx4 v[36:39], v1, s[16:19], s32 offen offset:1024 sc0 sc1\n.Z127:\n"
"s_cmp_lg_u64 s[54:55], 0\ns_cbranch_scc0 .Z128\nbuffer_load_dwordx4 v[40:43], v1, s[16:19], s33 offen offset:0 sc0 sc1\n"
"buffer_load_dwordx4 v[44:47], v1, s[16:19], s33 offen offset:1024 sc0 sc1\n.Z128:\ns_cmp_lg_u64 s[56:57], 0\ns_cbranch_scc0 .Z129\n"
"buffer_load_dwordx4 v[48:51], v1, s[16:19], s34 offen offset:0 sc0 sc1\nbuffer_load_dwordx4 v[52:55], v1, s[16:19], s34 offen offset:1024 sc0 sc1\n.Z129:\n"
"s_cmp_lg_u64 s[58:59], 0\ns_cbranch_scc0 .Z130\nbuffer_load_dwordx4 v[56:59], v1, s[16:19], s35 offen offset:0 sc0 sc1\n"
"buffer_load_dwordx4 v[60:63], v1, s[16:19], s35 offen offset:1024 sc0 sc1\n.Z130:\ns_sleep 8\nWV 0\ns_branch .Z122\n.RA10:\ns_nop 7\ns_nop 3\nv_max3_f32 v22, v32, v33, v34\n"
"v_max3_f32 v22, v22, v35, v40\nv_max3_f32 v22, v22, v41, v42\nv_max_f32 v22, v22, v43\nv_mov_b32 v91, v22\ns_nop 1\nv_permlane32_swap_b32 v22, v91\nv_max_f32 v22, v22, v91\n"
"v_mov_b32 v91, v22\ns_nop 1\nv_permlane16_swap_b32 v22, v91\nv_max_f32 v22, v22, v91\nv_ceil_f32 v22, v22\nv_max3_f32 v23, v36, v37, v38\nv_max3_f32 v23, v23, v39, v44\n"
"v_max3_f32 v23, v23, v45, v46\nv_max_f32 v23, v23, v47\nv_mov_b32 v91, v23\ns_nop 1\nv_permlane32_swap_b32 v23, v91\nv_max_f32 v23, v23, v91\nv_mov_b32 v91, v23\ns_nop 1\n"
"v_permlane16_swap_b32 v23, v91\nv_max_f32 v23, v23, v91\nv_ceil_f32 v23, v23\ns_bitcmp1_b32 s62, 0\ns_cbranch_scc0 .R131\nv_cmp_lt_f32 vcc, 0xff000000, v22\ns_nop 1\n"
"v_cndmask_b32 v22, 0, v22, vcc\nv_cmp_lt_f32 vcc, 0xff000000, v23\ns_nop 1\nv_cndmask_b32 v23, 0, v23, vcc\ns_branch .R132\n.R131:\nv_max_f32 v22, 0, v22\nv_max_f32 v23, 0, v23\n"
"s_bitcmp1_b32 s62, 3\ns_cbranch_scc0 .R132\nv_max_f32 v22, v20, v22\nv_max_f32 v23, v21, v23\n.R132:\nAD 88,88,22\nAD 89,89,23\nSU 32,32,22\nSU 33,33,22\nSU 34,34,22\nSU 35,35,22\n"
"SU 36,36,23\nSU 37,37,23\nSU 38,38,23\nSU 39,39,23\nSU 40,40,22\nSU 41,41,22\nSU 42,42,22\nSU 43,43,22\nSU 44,44,23\nSU 45,45,23\nSU 46,46,23\nSU 47,47,23\nv_mov_b32 v90, v80\n"
"v_mov_b32 v91, v81\nEX 80,32\nEX 81,33\ns_nop 0\nAD 84,80,81\nCV 64,80,81\nEX 82,34\nEX 83,35\nAD 84,84,82\nAD 84,84,83\nCV 65,82,83\nEX 80,36\nEX 81,37\ns_nop 0\nAD 85,80,81\n"
"CV 68,80,81\nEX 82,38\nEX 83,39\nAD 85,85,82\nAD 85,85,83\nCV 69,82,83\nEX 80,40\nEX 81,41\nAD 84,84,80\nAD 84,84,81\nCV 66,80,81\nEX 82,42\nEX 83,43\nAD 84,84,82\nAD 84,84,83\n"
"CV 67,82,83\nEX 80,44\nEX 81,45\nAD 85,85,80\nAD 85,85,81\nCV 70,80,81\nEX 82,46\nEX 83,47\nAD 85,85,82\nAD 85,85,83\nCV 71,82,83\nv_mov_b32 v80, v90\nv_mov_b32 v81, v91\n"
"s_bitcmp1_b32 s62, 1\ns_cbranch_scc1 .R133\nSU 48,48,22\nSU 49,49,22\nSU 50,50,22\nSU 51,51,22\nSU 52,52,23\nSU 53,53,23\nSU 54,54,23\nSU 55,55,23\nSU 56,56,22\nSU 57,57,22\n"
"SU 58,58,22\nSU 59,59,22\nSU 60,60,23\nSU 61,61,23\nSU 62,62,23\nSU 63,63,23\nSU 4,4,22\nSU 5,5,22\nSU 6,6,22\nSU 7,7,22\nSU 8,8,23\nSU 9,9,23\nSU 10,10,23\nSU 11,11,23\n"
"SU 12,12,22\nSU 13,13,22\nSU 14,14,22\nSU 15,15,22\nSU 16,16,23\nSU 17,17,23\nSU 18,18,23\nSU 19,19,23\nEX 80,48\nEX 81,49\n.R133:\ns_bitcmp1_b32 s62, 0\ns_cbranch_scc0 .RS12\n"
"s_nop 1\ns_setpc_b64 s[80:81]\n.RB11:\ns_nop 7\ns_nop 3\nv_max3_f32 v22, v48, v49, v50\nv_max3_f32 v22, v22, v51, v56\nv_max3_f32 v22, v22, v57, v58\nv_max_f32 v22, v22, v59\n"
"v_mov_b32 v91, v22\ns_nop 1\nv_permlane32_swap_b32 v22, v91\nv_max_f32 v22, v22, v91\nv_mov_b32 v91, v22\ns_nop 1\nv_permlane16_swap_b32 v22, v91\nv_max_f32 v22, v22, v91\n"
"v_ceil_f32 v22, v22\nv_max3_f32 v23, v52, v53, v54\nv_max3_f32 v23, v23, v55, v60\nv_max3_f32 v23, v23, v61, v62\nv_max_f32 v23, v23, v63\nv_mov_b32 v91, v23\ns_nop 1\n"
"v_permlane32_swap_b32 v23, v91\nv_max_f32 v23, v23, v91\nv_mov_b32 v91, v23\ns_nop 1\nv_permlane16_swap_b32 v23, v91\nv_max_f32 v23, v23, v91\nv_ceil_f32 v23, v23\n"
"s_bitcmp1_b32 s62, 0\ns_cbranch_scc0 .R136\nv_cmp_lt_f32 vcc, 0xff000000, v22\nv_cndmask_b32 v22, 0, v22, vcc\nv_cmp_lt_f32 vcc, 0xff000000, v23\nv_cndmask_b32 v23, 0, v23, vcc\n"
"s_branch .R137\n.R136:\nv_max_f32 v22, 0, v22\nv_max_f32 v23, 0, v23\ns_bitcmp1_b32 s62, 3\ns_cbranch_scc0 .R137\nv_max_f32 v22, v20, v22\nv_max_f32 v23, v21, v23\n.R137:\n"
"AD 88,88,22\nAD 89,89,23\nSU 48,48,22\nSU 49,49,22\nSU 50,50,22\nSU 51,51,22\nSU 52,52,23\nSU 53,53,23\nSU 54,54,23\nSU 55,55,23\nSU 56,56,22\nSU 57,57,22\nSU 58,58,22\n"
"SU 59,59,22\nSU 60,60,23\nSU 61,61,23\nSU 62,62,23\nSU 63,63,23\nv_mov_b32 v90, v80\nv_mov_b32 v91, v81\nEX 80,48\nEX 81,49\ns_nop 0\nAD 84,80,81\nCV 72,80,81\nEX 82,50\nEX 83,51\n"
"AD 84,84,82\nAD 84,84,83\nCV 73,82,83\nEX 80,52\nEX 81,53\ns_nop 0\nAD 85,80,81\nCV 76,80,81\nEX 82,54\nEX 83,55\nAD 85,85,82\nAD 85,85,83\nCV 77,82,83\nEX 80,56\nEX 81,57\n"
"AD 84,84,80\nAD 84,84,81\nCV 74,80,81\nEX 82,58\nEX 83,59\nAD 84,84,82\nAD 84,84,83\nCV 75,82,83\nEX 80,60\nEX 81,61\nAD 85,85,80\nAD 85,85,81\nCV 78,80,81\nEX 82,62\nEX 83,63\n"
"AD 85,85,82\nAD 85,85,83\nCV 79,82,83\nv_mov_b32 v80, v90\nv_mov_b32 v81, v91\ns_bitcmp1_b32 s62, 1\ns_cbranch_scc1 .R138\nSU 32,32,22\nSU 33,33,22\nSU 34,34,22\nSU 35,35,22\n"
"SU 36,36,23\nSU 37,37,23\nSU 38,38,23\nSU 39,39,23\nSU 40,40,22\nSU 41,41,22\nSU 42,42,22\nSU 43,43,22\nSU 44,44,23\nSU 45,45,23\nSU 46,46,23\nSU 47,47,23\nSU 4,4,22\nSU 5,5,22\n"
"SU 6,6,22\nSU 7,7,22\nSU 8,8,23\nSU 9,9,23\nSU 10,10,23\nSU 11,11,23\nSU 12,12,22\nSU 13,13,22\nSU 14,14,22\nSU 15,15,22\nSU 16,16,23\nSU 17,17,23\nSU 18,18,23\nSU 19,19,23\n"
"EX 80,32\nEX 81,33\n.R138:\ns_bitcmp1_b32 s62, 0\ns_cbranch_scc0 .RS12\ns_nop 1\ns_setpc_b64 s[80:81]\n.RS12:\nv_exp_f32 v22, -v22\nv_exp_f32 v23, -v23\nMU 86,86,22\nMU 87,87,23\n"
"AR 91,0\nMU 91,91,22\nAW 0,91\nAR 91,1\nMU 91,91,22\nAW 1,91\nAR 91,2\nMU 91,91,22\nAW 2,91\nAR 91,3\nMU 91,91,22\nAW 3,91\nAR 91,4\nMU 91,91,23\nAW 4,91\nAR 91,5\nMU 91,91,23\n"
"AW 5,91\nAR 91,6\nMU 91,91,23\nAW 6,91\nAR 91,7\nMU 91,91,23\nAW 7,91\nAR 91,8\nMU 91,91,22\nAW 8,91\nAR 91,9\nMU 91,91,22\nAW 9,91\nAR 91,10\nMU 91,91,22\nAW 10,91\nAR 91,11\n"
"MU 91,91,22\nAW 11,91\nAR 91,12\nMU 91,91,23\nAW 12,91\nAR 91,13\nMU 91,91,23\nAW 13,91\nAR 91,14\nMU 91,91,23\nAW 14,91\nAR 91,15\nMU 91,91,23\nAW 15,91\nAR 91,16\nMU 91,91,22\n"
"AW 16,91\nAR 91,17\nMU 91,91,22\nAW 17,91\nAR 91,18\nMU 91,91,22\nAW 18,91\nAR 91,19\nMU 91,91,22\nAW 19,91\nAR 91,20\nMU 91,91,23\nAW 20,91\nAR 91,21\nMU 91,91,23\nAW 21,91\n"
"AR 91,22\nMU 91,91,23\nAW 22,91\nAR 91,23\nMU 91,91,23\nAW 23,91\nAR 91,24\nMU 91,91,22\nAW 24,91\nAR 91,25\nMU 91,91,22\nAW 25,91\nAR 91,26\nMU 91,91,22\nAW 26,91\nAR 91,27\n"
"MU 91,91,22\nAW 27,91\nAR 91,28\nMU 91,91,23\nAW 28,91\nAR 91,29\nMU 91,91,23\nAW 29,91\nAR 91,30\nMU 91,91,23\nAW 30,91\nAR 91,31\nMU 91,91,23\nAW 31,91\nAR 91,32\nMU 91,91,22\n"
"AW 32,91\nAR 91,33\nMU 91,91,22\nAW 33,91\nAR 91,34\nMU 91,91,22\nAW 34,91\nAR 91,35\nMU 91,91,22\nAW 35,91\nAR 91,36\nMU 91,91,23\nAW 36,91\nAR 91,37\nMU 91,91,23\nAW 37,91\n"
"AR 91,38\nMU 91,91,23\nAW 38,91\nAR 91,39\nMU 91,91,23\nAW 39,91\nAR 91,40\nMU 91,91,22\nAW 40,91\nAR 91,41\nMU 91,91,22\nAW 41,91\nAR 91,42\nMU 91,91,22\nAW 42,91\nAR 91,43\n"
"MU 91,91,22\nAW 43,91\nAR 91,44\nMU 91,91,23\nAW 44,91\nAR 91,45\nMU 91,91,23\nAW 45,91\nAR 91,46\nMU 91,91,23\nAW 46,91\nAR 91,47\nMU 91,91,23\nAW 47,91\nAR 91,48\nMU 91,91,22\n"
"AW 48,91\nAR 91,49\nMU 91,91,22\nAW 49,91\nAR 91,50\nMU 91,91,22\nAW 50,91\nAR 91,51\nMU 91,91,22\nAW 51,91\nAR 91,52\nMU 91,91,23\nAW 52,91\nAR 91,53\nMU 91,91,23\nAW 53,91\n"
"AR 91,54\nMU 91,91,23\nAW 54,91\nAR 91,55\nMU 91,91,23\nAW 55,91\nAR 91,56\nMU 91,91,22\nAW 56,91\nAR 91,57\nMU 91,91,22\nAW 57,91\nAR 91,58\nMU 91,91,22\nAW 58,91\nAR 91,59\n"
"MU 91,91,22\nAW 59,91\nAR 91,60\nMU 91,91,23\nAW 60,91\nAR 91,61\nMU 91,91,23\nAW 61,91\nAR 91,62\nMU 91,91,23\nAW 62,91\nAR 91,63\nMU 91,91,23\nAW 63,91\nAR 91,64\nMU 91,91,22\n"
"AW 64,91\nAR 91,65\nMU 91,91,22\nAW 65,91\nAR 91,66\nMU 91,91,22\nAW 66,91\nAR 91,67\nMU 91,91,22\nAW 67,91\nAR 91,68\nMU 91,91,23\nAW 68,91\nAR 91,69\nMU 91,91,23\nAW 69,91\n"
"AR 91,70\nMU 91,91,23\nAW 70,91\nAR 91,71\nMU 91,91,23\nAW 71,91\nAR 91,72\nMU 91,91,22\nAW 72,91\nAR 91,73\nMU 91,91,22\nAW 73,91\nAR 91,74\nMU 91,91,22\nAW 74,91\nAR 91,75\n"
"MU 91,91,22\nAW 75,91\nAR 91,76\nMU 91,91,23\nAW 76,91\nAR 91,77\nMU 91,91,23\nAW 77,91\nAR 91,78\nMU 91,91,23\nAW 78,91\nAR 91,79\nMU 91,91,23\nAW 79,91\nAR 91,80\nMU 91,91,22\n"
"AW 80,91\nAR 91,81\nMU 91,91,22\nAW 81,91\nAR 91,82\nMU 91,91,22\nAW 82,91\nAR 91,83\nMU 91,91,22\nAW 83,91\nAR 91,84\nMU 91,91,23\nAW 84,91\nAR 91,85\nMU 91,91,23\nAW 85,91\n"
"AR 91,86\nMU 91,91,23\nAW 86,91\nAR 91,87\nMU 91,91,23\nAW 87,91\nAR 91,88\nMU 91,91,22\nAW 88,91\nAR 91,89\nMU 91,91,22\nAW 89,91\nAR 91,90\nMU 91,91,22\nAW 90,91\nAR 91,91\n"
"MU 91,91,22\nAW 91,91\nAR 91,92\nMU 91,91,23\nAW 92,91\nAR 91,93\nMU 91,91,23\nAW 93,91\nAR 91,94\nMU 91,91,23\nAW 94,91\nAR 91,95\nMU 91,91,23\nAW 95,91\nAR 91,96\nMU 91,91,22\n"
"AW 96,91\nAR 91,97\nMU 91,91,22\nAW 97,91\nAR 91,98\nMU 91,91,22\nAW 98,91\nAR 91,99\nMU 91,91,22\nAW 99,91\nAR 91,100\nMU 91,91,23\nAW 100,91\nAR 91,101\nMU 91,91,23\nAW 101,91\n"
"AR 91,102\nMU 91,91,23\nAW 102,91\nAR 91,103\nMU 91,91,23\nAW 103,91\nAR 91,104\nMU 91,91,22\nAW 104,91\nAR 91,105\nMU 91,91,22\nAW 105,91\nAR 91,106\nMU 91,91,22\nAW 106,91\n"
"AR 91,107\nMU 91,91,22\nAW 107,91\nAR 91,108\nMU 91,91,23\nAW 108,91\nAR 91,109\nMU 91,91,23\nAW 109,91\nAR 91,110\nMU 91,91,23\nAW 110,91\nAR 91,111\nMU 91,91,23\nAW 111,91\n"
"AR 91,112\nMU 91,91,22\nAW 112,91\nAR 91,113\nMU 91,91,22\nAW 113,91\nAR 91,114\nMU 91,91,22\nAW 114,91\nAR 91,115\nMU 91,91,22\nAW 115,91\nAR 91,116\nMU 91,91,23\nAW 116,91\n"
"AR 91,117\nMU 91,91,23\nAW 117,91\nAR 91,118\nMU 91,91,23\nAW 118,91\nAR 91,119\nMU 91,91,23\nAW 119,91\nAR 91,120\nMU 91,91,22\nAW 120,91\nAR 91,121\nMU 91,91,22\nAW 121,91\n"
"AR 91,122\nMU 91,91,22\nAW 122,91\nAR 91,123\nMU 91,91,22\nAW 123,91\nAR 91,124\nMU 91,91,23\nAW 124,91\nAR 91,125\nMU 91,91,23\nAW 125,91\nAR 91,126\nMU 91,91,23\nAW 126,91\n"
"AR 91,127\nMU 91,91,23\nAW 127,91\ns_nop 1\ns_setpc_b64 s[80:81]\n.p2align 12\n.PA3:\nv_add_u32 v128, 0x10000, v12\nv_add_u32 v129, 0x10000, v13\nv_add_u32 v130, 0x10000, v14\n"
"v_add_u32 v131, 0x10000, v15\nv_add_u32 v132, 0x10000, v16\nv_add_u32 v133, 0x10000, v17\nv_add_u32 v134, 0x10000, v18\nv_add_u32 v135, 0x10000, v19\ns_add_u32 s2, s11, 1\n"
"s_lshr_b32 s2, s2, 1\ns_lshl_b32 s2, s2, 12\nv_add_u32 v140, s2, v10\nv_add_u32 v141, s2, v11\nv_xor_b32 v142, 0x1000, v140\nv_xor_b32 v143, 0x1000, v141\ns_add_u32 m0, s47, 0\n"
"s_add_u32 s46, s51, 524288\nD0 6,32,46,0\nD0 7,32,46,1024\nD0 8,32,46,2048\nD0 9,32,46,3072\ns_add_u32 m0, s47, -49152\ns_add_u32 s46, s52, 128\nD0 136,28,46,0\nD0 137,28,46,1024\n"
"D0 138,28,46,2048\nD0 139,28,46,3072\ns_add_u32 m0, s47, 16384\ns_add_u32 s46, s51, 786432\nD0 6,32,46,0\nD0 7,32,46,1024\nD0 8,32,46,2048\nD0 9,32,46,3072\n"
"v_mov_b32 v116, 0x40000000\nv_cmp_gt_u32 vcc, 56, v0\ns_lshl2_add_u32 s2, s10, s11\ns_mul_i32 s2, s2, 1792\nv_cndmask_b32 v117, v116, v1, vcc\ns_mov_b32 s36, s56\n"
"s_and_b32 s37, s57, 0xffff\ns_mov_b32 s38, 0x380\ns_mov_b32 s39, 0x20000\ns_mov_b32 s40, s58\ns_and_b32 s41, s59, 0xffff\ns_mov_b32 s42, 0x380\ns_mov_b32 s43, 0x20000\n"
"buffer_load_dwordx4 v[96:99], v117, s[28:31], s2 offen offset:0\nbuffer_load_dwordx4 v[100:103], v117, s[28:31], s2 offen offset:896\n"
"buffer_load_dwordx4 v[104:107], v117, s[36:39], 0 offen offset:0\nbuffer_load_dwordx4 v[108:111], v117, s[40:43], 0 offen offset:0\ns_add_u32 m0, s47, 32768\n"
"s_add_u32 s46, s51, 1048576\nD0 6,32,46,0\nD0 7,32,46,1024\nD0 8,32,46,2048\nD0 9,32,46,3072\ns_add_u32 m0, s47, -32768\ns_add_u32 s46, s52, 256\nD0 136,28,46,0\n"
"D0 137,28,46,1024\nD0 138,28,46,2048\nD0 139,28,46,3072\nWL 0\ns_mul_i32 s15, s15, 1664525\ns_add_u32 s15, s15, 1013904223\nv_mov_b32 v25, s15\ns_mov_b32 s57, 0x3db8aa3b\n"
"s_and_b32 s59, s11, 1\ns_lshl_b32 s59, s59, 13\ns_mul_i32 s2, s73, 25165824\ns_and_b32 s3, s12, 1\ns_lshl_b32 s3, s3, 21\ns_add_u32 s2, s2, s3\ns_lshl2_add_u32 s3, s13, s11\n"
"s_lshl_b32 s3, s3, 15\ns_add_u32 s2, s2, s3\ns_add_u32 s56, s2, 0x800000\ns_xor_b32 s53, s56, 0xc000\nWV 24\ns_barrier\nRa 192,140,0\nRa 196,141,0\nRa 200,140,2048\n"
"Ra 204,141,2048\nRa 208,142,0\nRa 212,143,0\nRa 216,142,2048\nRa 220,143,2048\nTa 160,128,0\nTa 162,128,2048\nTa 164,128,16384\nTa 166,128,18432\nTa 168,129,0\nTa 170,129,2048\n"
"Ta 172,129,16384\nTa 174,129,18432\nTa 176,130,0\nTa 178,130,2048\nTa 180,130,16384\nTa 182,130,18432\nTa 184,131,0\nTa 186,131,2048\nTa 188,131,16384\nTa 190,131,18432\nWL 0\n"
"Zvaa 32,160,192\nTa 224,132,0\ns_add_u32 m0, s47, 49152\ns_add_u32 s46, s51, 1310720\nMvaa 32,164,196\nTa 226,132,2048\nZaaa 96,164,204\nD0 6,32,46,0\nMaaa 96,160,200\n"
"Ta 228,132,16384\nZvaa 192,160,208\nTa 230,132,18432\nMvaa 192,164,212\nZvaa 224,164,220\nTa 232,133,0\nMvaa 224,160,216\nTa 234,133,2048\nD0 7,32,46,1024\nZvaa 228,168,216\n"
"Mvaa 228,172,220\nTa 236,133,16384\nZvaa 196,172,212\nTa 238,133,18432\nMvaa 196,168,208\nZaaa 100,168,200\nTa 240,134,0\nD0 8,32,46,2048\nMaaa 100,172,204\nTa 242,134,2048\n"
"Zvaa 36,172,196\nMvaa 36,168,192\nTa 244,134,16384\nZvaa 40,176,192\nTa 246,134,18432\nMvaa 40,180,196\nD0 9,32,46,3072\nZaaa 104,180,204\nTa 248,135,0\nMaaa 104,176,200\n"
"Ta 250,135,2048\nZvaa 200,176,208\ns_add_u32 m0, s47, -16384\ns_add_u32 s46, s52, 384\nMvaa 200,180,212\nTa 252,135,16384\nZvaa 232,180,220\nTa 254,135,18432\nD0 136,28,46,0\n"
"Mvaa 232,176,216\nZvaa 236,184,216\nMvaa 236,188,220\nZvaa 204,188,212\nMvaa 204,184,208\nD0 137,28,46,1024\nZaaa 108,184,200\nMaaa 108,188,204\nZvaa 44,188,196\nMvaa 44,184,192\n"
"s_waitcnt vmcnt(18) lgkmcnt(0)\ns_barrier\nZvaa 48,224,192\nRa 128,140,16384\nD0 138,28,46,2048\nMvaa 48,228,196\nRa 132,141,16384\nZaaa 112,228,204\nRa 136,140,18432\n"
"Maaa 112,224,200\nRa 140,141,18432\nZvaa 208,224,208\nRa 144,142,16384\nWV 15\nMvaa 208,228,212\nRa 148,143,16384\nD0 139,28,46,3072\nZvaa 240,228,220\nRa 152,142,18432\n"
"Mvaa 240,224,216\nRa 156,143,18432\nZvaa 244,232,216\nTa 160,12,0\ns_add_u32 m0, s47, 65536\ns_add_u32 s46, s51, 1572864\nMvaa 244,236,220\nTa 162,12,2048\nZvaa 212,236,212\n"
"Ta 164,12,16384\nD0 6,32,46,0\nMvaa 212,232,208\nTa 166,12,18432\nZaaa 116,232,200\nTa 168,13,0\nv_lshlrev_b32 v112, 16, v96\nMaaa 116,236,204\nTa 170,13,2048\nZvaa 52,236,196\n"
"Ta 172,13,16384\nMvaa 52,232,192\nTa 174,13,18432\nD0 7,32,46,1024\nZvaa 56,240,192\nTa 176,14,0\nv_lshlrev_b32 v113, 16, v100\nMvaa 56,244,196\nTa 178,14,2048\nZaaa 120,244,204\n"
"Ta 180,14,16384\nMaaa 120,240,200\nTa 182,14,18432\nZvaa 216,240,208\nTa 184,15,0\nD0 8,32,46,2048\nMvaa 216,244,212\nTa 186,15,2048\nZvaa 248,244,220\nTa 188,15,16384\n"
"Mvaa 248,240,216\nTa 190,15,18432\nZvaa 252,248,216\nv_lshlrev_b32 v114, 16, v104\nv_lshlrev_b32 v115, 16, v108\nMvaa 252,252,220\nD0 9,32,46,3072\nZvaa 220,252,212\n"
"MU 116,112,114\nMvaa 220,248,208\nMU 117,113,114\nZaaa 124,248,200\ns_add_u32 m0, s47, 81920\ns_add_u32 s46, s51, 1835008\nMaaa 124,252,204\nCV 116,116,117\nZvaa 60,252,196\n"
"D0 6,32,46,0\nMvaa 60,248,192\nWL 0\nMvaa 32,160,128\nTa 224,16,0\nv_lshlrev_b32 v118, 16, v116\nMvaa 32,164,132\nTa 226,16,2048\nMaaa 96,164,140\n"
"v_and_b32 v119, 0xffff0000, v116\nMaaa 96,160,136\nTa 228,16,16384\nD0 7,32,46,1024\nMvaa 192,160,144\nTa 230,16,18432\nMU 120,112,115\nMvaa 192,164,148\nMU 121,113,115\n"
"Mvaa 224,164,156\nTa 232,17,0\nMvaa 224,160,152\nTa 234,17,2048\nMvaa 228,168,152\nD0 8,32,46,2048\nv_and_b32 v112, 0xffff0000, v96\nMvaa 228,172,156\nTa 236,17,16384\n"
"Mvaa 196,172,148\nTa 238,17,18432\nMvaa 196,168,144\nv_and_b32 v113, 0xffff0000, v100\nMaaa 100,168,136\nTa 240,18,0\nv_and_b32 v114, 0xffff0000, v104\nMaaa 100,172,140\n"
"Ta 242,18,2048\nD0 9,32,46,3072\nMvaa 36,172,132\nv_and_b32 v115, 0xffff0000, v108\nMvaa 36,168,128\nTa 244,18,16384\nMvaa 40,176,128\nTa 246,18,18432\nMU 116,112,114\n"
"Mvaa 40,180,132\nMU 117,113,114\nMaaa 104,180,140\nTa 248,19,0\nMaaa 104,176,136\nTa 250,19,2048\nMvaa 200,176,144\nCV 116,116,117\nv_and_b32 v117, 0xffff0000, v116\n"
"Mvaa 200,180,148\nTa 252,19,16384\nMvaa 232,180,156\nTa 254,19,18432\nMvaa 232,176,152\nv_lshlrev_b32 v116, 16, v116\nMvaa 236,184,152\nFM 120,112,115\nFM 121,113,115\n"
"Mvaa 236,188,156\nAD 118,118,116\nMvaa 204,188,148\nAD 119,119,117\nMvaa 204,184,144\nv_lshlrev_b32 v112, 16, v97\nMaaa 108,184,136\nv_lshlrev_b32 v113, 16, v101\n"
"v_lshlrev_b32 v114, 16, v105\nMaaa 108,188,140\nv_lshlrev_b32 v115, 16, v109\nMvaa 44,188,132\nMU 116,112,114\nMvaa 44,184,128\ns_waitcnt vmcnt(12) lgkmcnt(0)\ns_barrier\n"
"Mvaa 48,224,128\nRa 192,140,32768\ns_add_u32 m0, s47, -49152\ns_add_u32 s46, s52, 512\nMvaa 48,228,132\nRa 196,141,32768\nMaaa 112,228,140\nRa 200,140,34816\nD0 136,28,46,0\n"
"Maaa 112,224,136\nRa 204,141,34816\nMvaa 208,224,144\nRa 208,142,32768\nMU 117,113,114\nMvaa 208,228,148\nRa 212,143,32768\nMvaa 240,228,156\nRa 216,142,34816\nMvaa 240,224,152\n"
"Ra 220,143,34816\nD0 137,28,46,1024\nMvaa 244,232,152\nTa 160,12,32768\nCV 116,116,117\nMvaa 244,236,156\nTa 162,12,34816\nMvaa 212,236,148\nTa 164,12,49152\nMvaa 212,232,144\n"
"Ta 166,12,51200\nMaaa 116,232,136\nTa 168,13,32768\nD0 138,28,46,2048\nMaaa 116,236,140\nTa 170,13,34816\nMvaa 52,236,132\nTa 172,13,49152\nMvaa 52,232,128\nTa 174,13,51200\n"
"Mvaa 56,240,128\nTa 176,14,32768\nv_and_b32 v117, 0xffff0000, v116\nMvaa 56,244,132\nTa 178,14,34816\nD0 139,28,46,3072\nMaaa 120,244,140\nTa 180,14,49152\nMaaa 120,240,136\n"
"Ta 182,14,51200\nMvaa 216,240,144\nTa 184,15,32768\ns_add_u32 m0, s47, 0\ns_add_u32 s46, s51, 2097152\nMvaa 216,244,148\nTa 186,15,34816\nMvaa 248,244,156\nTa 188,15,49152\n"
"D0 6,32,46,0\nMvaa 248,240,152\nTa 190,15,51200\nMvaa 252,248,152\nv_lshlrev_b32 v116, 16, v116\nFM 120,112,115\nMvaa 252,252,156\nFM 121,113,115\nMvaa 220,252,148\n"
"AD 118,118,116\nMvaa 220,248,144\nD0 7,32,46,1024\nMaaa 124,248,136\nAD 119,119,117\nv_and_b32 v112, 0xffff0000, v97\nMaaa 124,252,140\nv_and_b32 v113, 0xffff0000, v101\n"
"Mvaa 60,252,132\nv_and_b32 v114, 0xffff0000, v105\nMvaa 60,248,128\nWL 0\nMvaa 32,160,192\nTa 224,16,32768\nD0 8,32,46,2048\nMvaa 32,164,196\nTa 226,16,34816\nMaaa 96,164,204\n"
"v_and_b32 v115, 0xffff0000, v109\nMaaa 96,160,200\nTa 228,16,49152\nMvaa 192,160,208\nTa 230,16,51200\nMU 116,112,114\nMvaa 192,164,212\nD0 9,32,46,3072\nMvaa 224,164,220\n"
"Ta 232,17,32768\nMvaa 224,160,216\nTa 234,17,34816\nMvaa 228,168,216\ns_add_u32 m0, s47, 16384\ns_add_u32 s46, s51, 2359296\nMvaa 228,172,220\nTa 236,17,49152\nMvaa 196,172,212\n"
"Ta 238,17,51200\nD0 6,32,46,0\nMvaa 196,168,208\nMU 117,113,114\nMaaa 100,168,200\nTa 240,18,32768\nCV 116,116,117\nMaaa 100,172,204\nTa 242,18,34816\nMvaa 36,172,196\n"
"v_and_b32 v117, 0xffff0000, v116\nMvaa 36,168,192\nTa 244,18,49152\nD0 7,32,46,1024\nMvaa 40,176,192\nTa 246,18,51200\nv_lshlrev_b32 v116, 16, v116\nMvaa 40,180,196\n"
"FM 120,112,115\nMaaa 104,180,204\nTa 248,19,32768\nMaaa 104,176,200\nTa 250,19,34816\nMvaa 200,176,208\nD0 8,32,46,2048\nFM 121,113,115\nMvaa 200,180,212\nTa 252,19,49152\n"
"Mvaa 232,180,220\nTa 254,19,51200\nMvaa 232,176,216\nAD 118,118,116\nMvaa 236,184,216\nAD 119,119,117\nv_lshlrev_b32 v112, 16, v98\nMvaa 236,188,220\nD0 9,32,46,3072\n"
"Mvaa 204,188,212\nv_lshlrev_b32 v113, 16, v102\nMvaa 204,184,208\nv_lshlrev_b32 v114, 16, v106\nMaaa 108,184,200\nv_lshlrev_b32 v115, 16, v110\nMU 116,112,114\nMaaa 108,188,204\n"
"MU 117,113,114\nMvaa 44,188,196\nCV 116,116,117\nMvaa 44,184,192\ns_waitcnt vmcnt(12) lgkmcnt(0)\ns_barrier\nMvaa 48,224,192\nRa 128,140,49152\ns_add_u32 m0, s47, -32768\n"
"s_add_u32 s46, s52, 640\nMvaa 48,228,196\nRa 132,141,49152\nMaaa 112,228,204\nRa 136,140,51200\nD0 136,28,46,0\nMaaa 112,224,200\nRa 140,141,51200\nMvaa 208,224,208\n"
"Ra 144,142,49152\nv_and_b32 v117, 0xffff0000, v116\nMvaa 208,228,212\nRa 148,143,49152\nMvaa 240,228,220\nRa 152,142,51200\nMvaa 240,224,216\nRa 156,143,51200\nD0 137,28,46,1024\n"
"Mvaa 244,232,216\nTa 160,128,0\nv_lshlrev_b32 v116, 16, v116\nMvaa 244,236,220\nTa 162,128,2048\nMvaa 212,236,212\nTa 164,128,16384\nMvaa 212,232,208\nTa 166,128,18432\n"
"Maaa 116,232,200\nTa 168,129,0\nD0 138,28,46,2048\nMaaa 116,236,204\nTa 170,129,2048\nMvaa 52,236,196\nTa 172,129,16384\nMvaa 52,232,192\nTa 174,129,18432\nMvaa 56,240,192\n"
"Ta 176,130,0\nFM 120,112,115\nMvaa 56,244,196\nTa 178,130,2048\nD0 139,28,46,3072\nMaaa 120,244,204\nTa 180,130,16384\nMaaa 120,240,200\nTa 182,130,18432\nMvaa 216,240,208\n"
"Ta 184,131,0\ns_add_u32 m0, s47, 32768\ns_add_u32 s46, s51, 2621440\nMvaa 216,244,212\nTa 186,131,2048\nMvaa 248,244,220\nTa 188,131,16384\nD0 6,32,46,0\nMvaa 248,240,216\n"
"Ta 190,131,18432\nMvaa 252,248,216\nFM 121,113,115\nAD 118,118,116\nMvaa 252,252,220\nAD 119,119,117\nMvaa 220,252,212\nv_and_b32 v112, 0xffff0000, v98\nMvaa 220,248,208\n"
"D0 7,32,46,1024\nMaaa 124,248,200\nv_and_b32 v113, 0xffff0000, v102\nv_and_b32 v114, 0xffff0000, v106\nMaaa 124,252,204\nv_and_b32 v115, 0xffff0000, v110\nMvaa 60,252,196\n"
"MU 116,112,114\nMvaa 60,248,192\nWL 0\nMvaa 32,160,128\nTa 224,132,0\nD0 8,32,46,2048\nMvaa 32,164,132\nTa 226,132,2048\nMaaa 96,164,140\nMU 117,113,114\nMaaa 96,160,136\n"
"Ta 228,132,16384\nMvaa 192,160,144\nTa 230,132,18432\nCV 116,116,117\nMvaa 192,164,148\nD0 9,32,46,3072\nMvaa 224,164,156\nTa 232,133,0\nMvaa 224,160,152\nTa 234,133,2048\n"
"Mvaa 228,168,152\ns_add_u32 m0, s47, 49152\ns_add_u32 s46, s51, 2883584\nMvaa 228,172,156\nTa 236,133,16384\nMvaa 196,172,148\nTa 238,133,18432\nD0 6,32,46,0\nMvaa 196,168,144\n"
"v_and_b32 v117, 0xffff0000, v116\nMaaa 100,168,136\nTa 240,134,0\nv_lshlrev_b32 v116, 16, v116\nMaaa 100,172,140\nTa 242,134,2048\nMvaa 36,172,132\nFM 120,112,115\n"
"Mvaa 36,168,128\nTa 244,134,16384\nD0 7,32,46,1024\nMvaa 40,176,128\nTa 246,134,18432\nFM 121,113,115\nMvaa 40,180,132\nAD 118,118,116\nMaaa 104,180,140\nTa 248,135,0\n"
"Maaa 104,176,136\nTa 250,135,2048\nMvaa 200,176,144\nD0 8,32,46,2048\nAD 119,119,117\nMvaa 200,180,148\nTa 252,135,16384\nMvaa 232,180,156\nTa 254,135,18432\nMvaa 232,176,152\n"
"v_lshlrev_b32 v112, 16, v99\nMvaa 236,184,152\nv_lshlrev_b32 v113, 16, v103\nv_lshlrev_b32 v114, 16, v107\nMvaa 236,188,156\nD0 9,32,46,3072\nMvaa 204,188,148\n"
"v_lshlrev_b32 v115, 16, v111\nMvaa 204,184,144\nMU 116,112,114\nMaaa 108,184,136\nMU 117,113,114\nCV 116,116,117\ns_and_b32 s78, s74, 3\ns_lshl2_add_u32 s78, s73, s78\n"
"s_lshl_b32 s78, s78, 12\ns_bitcmp1_b32 s11, 0\ns_cselect_b32 s79, s48, s45\ns_add_u32 s79, s79, 512\ns_and_b32 s79, s79, 0xfffe0000\ns_add_u32 s78, s78, s79\n"
"buffer_load_dword v31, v26, s[20:23], s78 offen\nMaaa 108,188,140\nv_and_b32 v117, 0xffff0000, v116\nMvaa 44,188,132\nv_lshlrev_b32 v116, 16, v116\nMvaa 44,184,128\n"
"s_waitcnt vmcnt(13) lgkmcnt(0)\ns_barrier\nMvaa 48,224,128\nRa 192,140,16384\ns_add_u32 m0, s47, -16384\ns_add_u32 s46, s52, 768\nMvaa 48,228,132\nRa 196,141,16384\n"
"Maaa 112,228,140\nRa 200,140,18432\nD0 136,28,46,0\nMaaa 112,224,136\nRa 204,141,18432\nMvaa 208,224,144\nRa 208,142,16384\nFM 120,112,115\nMvaa 208,228,148\nRa 212,143,16384\n"
"Mvaa 240,228,156\nRa 216,142,18432\nMvaa 240,224,152\nRa 220,143,18432\nD0 137,28,46,1024\nMvaa 244,232,152\nTa 160,12,0\nFM 121,113,115\nMvaa 244,236,156\nTa 162,12,2048\n"
"Mvaa 212,236,148\nTa 164,12,16384\nMvaa 212,232,144\nTa 166,12,18432\nMaaa 116,232,136\nTa 168,13,0\nD0 138,28,46,2048\nMaaa 116,236,140\nTa 170,13,2048\nMvaa 52,236,132\n"
"Ta 172,13,16384\nMvaa 52,232,128\nTa 174,13,18432\nMvaa 56,240,128\nTa 176,14,0\nAD 118,118,116\nMvaa 56,244,132\nTa 178,14,2048\nD0 139,28,46,3072\nMaaa 120,244,140\n"
"Ta 180,14,16384\nMaaa 120,240,136\nTa 182,14,18432\nMvaa 216,240,144\nTa 184,15,0\ns_add_u32 m0, s47, 65536\ns_add_u32 s46, s51, 3145728\nMvaa 216,244,148\nTa 186,15,2048\n"
"Mvaa 248,244,156\nTa 188,15,16384\nD0 6,32,46,0\nMvaa 248,240,152\nTa 190,15,18432\nMvaa 252,248,152\nAD 119,119,117\nv_and_b32 v112, 0xffff0000, v99\nMvaa 252,252,156\n"
"v_and_b32 v113, 0xffff0000, v103\nMvaa 220,252,148\nv_and_b32 v114, 0xffff0000, v107\nMvaa 220,248,144\nD0 7,32,46,1024\nMaaa 124,248,136\nv_and_b32 v115, 0xffff0000, v111\n"
"MU 116,112,114\nMaaa 124,252,140\nMU 117,113,114\nMvaa 60,252,132\nCV 116,116,117\nMvaa 60,248,128\nWL 0\nMvaa 32,160,192\nTa 224,16,0\nD0 8,32,46,2048\nMvaa 32,164,196\n"
"Ta 226,16,2048\nMaaa 96,164,204\nv_and_b32 v117, 0xffff0000, v116\nMaaa 96,160,200\nTa 228,16,16384\nMvaa 192,160,208\nTa 230,16,18432\nv_lshlrev_b32 v116, 16, v116\n"
"Mvaa 192,164,212\nD0 9,32,46,3072\nMvaa 224,164,220\nTa 232,17,0\nMvaa 224,160,216\nTa 234,17,2048\nMvaa 228,168,216\ns_add_u32 m0, s47, 81920\ns_add_u32 s46, s51, 3407872\n"
"Mvaa 228,172,220\nTa 236,17,16384\nMvaa 196,172,212\nTa 238,17,18432\nD0 6,32,46,0\nMvaa 196,168,208\nFM 120,112,115\nMaaa 100,168,200\nTa 240,18,0\nFM 121,113,115\n"
"Maaa 100,172,204\nTa 242,18,2048\nMvaa 36,172,196\nAD 118,118,116\nMvaa 36,168,192\nTa 244,18,16384\nD0 7,32,46,1024\nMvaa 40,176,192\nTa 246,18,18432\nAD 119,119,117\n"
"Mvaa 40,180,196\ns_nop 0\nv_permlane32_swap_b32 v118, v119\nMaaa 104,180,204\nTa 248,19,0\nMaaa 104,176,200\nTa 250,19,2048\nMvaa 200,176,208\nD0 8,32,46,2048\n"
"v_permlane32_swap_b32 v120, v121\nMvaa 200,180,212\nTa 252,19,16384\nMvaa 232,180,220\nTa 254,19,18432\nMvaa 232,176,216\nAD 118,118,119\nMvaa 236,184,216\nAD 120,120,121\n"
"Mvaa 236,188,220\nD0 9,32,46,3072\nMvaa 204,188,212\nv_permlane16_swap_b32 v118, v120\nMvaa 204,184,208\nAD 118,118,120\nMaaa 108,184,200\nMaaa 108,188,204\n"
"v_add_f32_dpp v118, v118, v118 row_ror:8 row_mask:0xf bank_mask:0xf\nMvaa 44,188,196\nMvaa 44,184,192\ns_waitcnt vmcnt(13) lgkmcnt(0)\ns_barrier\nMvaa 48,224,192\n"
"Ra 128,140,32768\ns_add_u32 m0, s47, -65536\ns_add_u32 s45, s45, 128\nMvaa 48,228,196\nRa 132,141,32768\nMaaa 112,228,204\nRa 136,140,34816\nD0 2,20,45,0\nMaaa 112,224,200\n"
"Ra 140,141,34816\nMvaa 208,224,208\nRa 144,142,32768\nv_add_f32_dpp v118, v118, v118 row_ror:4 row_mask:0xf bank_mask:0xf\nMvaa 208,228,212\nRa 148,143,32768\nMvaa 240,228,220\n"
"Ra 152,142,34816\nMvaa 240,224,216\nRa 156,143,34816\nD0 3,20,45,1024\nMvaa 244,232,216\nTa 160,12,32768\nv_add_f32_dpp v118, v118, v118 row_ror:2 row_mask:0xf bank_mask:0xf\n"
"Mvaa 244,236,220\nTa 162,12,34816\nMvaa 212,236,212\nTa 164,12,49152\nMvaa 212,232,208\nTa 166,12,51200\nMaaa 116,232,200\nTa 168,13,32768\nD0 4,20,45,2048\nMaaa 116,236,204\n"
"Ta 170,13,34816\nMvaa 52,236,196\nTa 172,13,49152\nMvaa 52,232,192\nTa 174,13,51200\nMvaa 56,240,192\nTa 176,14,32768\n"
"v_add_f32_dpp v118, v118, v118 row_ror:1 row_mask:0xf bank_mask:0xf\nMvaa 56,244,196\nTa 178,14,34816\nD0 5,20,45,3072\nMaaa 120,244,204\nTa 180,14,49152\nMaaa 120,240,200\n"
"Ta 182,14,51200\nMvaa 216,240,208\nTa 184,15,32768\ns_add_u32 m0, s47, 0\ns_add_u32 s44, s44, 0x40000\nMvaa 216,244,212\nTa 186,15,34816\nMvaa 248,244,220\nTa 188,15,49152\n"
"D0 6,24,44,0\nMvaa 248,240,216\nTa 190,15,51200\nMvaa 252,248,216\nCV 118,118,118\nv_lshlrev_b32 v118, 16, v118\nMvaa 252,252,220\nv_mul_f32 v119, 0xbfb8aa3b, v118\nEX 119,119\n"
"Mvaa 220,252,212\nv_add_f32 v120, 0, v118\nMvaa 220,248,208\nD0 7,24,44,1024\nMaaa 124,248,200\nv_add_f32 v119, 1.0, v119\nMaaa 124,252,204\nv_rcp_f32 v119, v119\nMvaa 60,252,196\n"
"v_ashrrev_i32 v121, 31, v120\nMvaa 60,248,192\nWL 0\nMvaa 32,160,128\nTa 224,16,32768\nD0 8,24,44,2048\nMvaa 32,164,132\nTa 226,16,34816\nMaaa 96,164,140\nCV 119,119,119\n"
"Maaa 96,160,136\nTa 228,16,49152\nMvaa 192,160,144\nTa 230,16,51200\nv_lshlrev_b32 v119, 16, v119\nMvaa 192,164,148\nD0 9,24,44,3072\nMvaa 224,164,156\nTa 232,17,32768\n"
"Mvaa 224,160,152\nTa 234,17,34816\nMvaa 228,168,152\ns_add_u32 m0, s47, 16384\ns_add_u32 s44, s44, 0x40000\nMvaa 228,172,156\nTa 236,17,49152\nMvaa 196,172,148\nTa 238,17,51200\n"
"D0 6,24,44,0\nMvaa 196,168,144\nv_or_b32 v121, 0x80000000, v121\nMaaa 100,168,136\nTa 240,18,32768\nv_xor_b32 v120, v120, v121\nMaaa 100,172,140\nTa 242,18,34816\nMvaa 36,172,132\n"
"v_and_b32 v120, 0xffff0000, v120\nMvaa 36,168,128\nTa 244,18,49152\nD0 7,24,44,1024\nMvaa 40,176,128\nTa 246,18,51200\ns_lshl2_add_u32 s2, s10, s11\nMvaa 40,180,132\n"
"v_lshrrev_b32 v112, 5, v0\nMaaa 104,180,140\nTa 248,19,32768\nMaaa 104,176,136\nTa 250,19,34816\nMvaa 200,176,144\nD0 8,24,44,2048\nv_lshl_add_u32 v112, s2, 1, v112\n"
"Mvaa 200,180,148\nTa 252,19,49152\nMvaa 232,180,156\nTa 254,19,51200\nMvaa 232,176,152\nv_sub_u32 v113, 0xffff, v112\nMvaa 236,184,152\nv_or_b32 v120, v120, v113\n"
"v_mov_b32 v121, s15\nMvaa 236,188,156\nD0 9,24,44,3072\nMvaa 204,188,148\nv_and_b32 v113, 31, v0\nMvaa 204,184,144\nv_mov_b32 v116, 0x40000000\nv_cmp_eq_u32 vcc, 16, v113\n"
"v_lshlrev_b32 v114, 2, v112\nv_lshlrev_b32 v115, 10, v113\nv_cndmask_b32 v114, v116, v114, vcc\nv_cndmask_b32 v120, v119, v120, vcc\nMaaa 108,184,136\n"
"v_lshl_add_u32 v115, v112, 3, v115\nv_and_b32 v113, 15, v0\nMaaa 108,188,140\nv_cmp_eq_u32 vcc, 0, v113\ns_mov_b32 s2, 0xc040000\ns_mov_b32 s3, 0xc03e000\n"
"v_cndmask_b32 v115, v116, v115, vcc\nbuffer_store_dwordx2 v[120:121], v115, s[16:19], s2 offen sc0 sc1\nbuffer_store_dword v118, v114, s[16:19], s3 offen sc0 sc1\nMvaa 44,188,132\n"
"Mvaa 44,184,128\ns_waitcnt vmcnt(14) lgkmcnt(0)\ns_barrier\nMvaa 48,224,128\nRa 192,140,49152\ns_add_u32 m0, s47, -49152\ns_add_u32 s48, s48, 128\nMvaa 48,228,132\n"
"Ra 196,141,49152\nMaaa 112,228,140\nRa 200,140,51200\nD0 2,20,48,0\nMaaa 112,224,136\nRa 204,141,51200\nMvaa 208,224,144\nRa 208,142,49152\nMvaa 208,228,148\nRa 212,143,49152\n"
"Mvaa 240,228,156\nRa 216,142,51200\nMvaa 240,224,152\nRa 220,143,51200\nD0 3,20,48,1024\nMvaa 244,232,152\nTa 160,128,0\nMvaa 244,236,156\nTa 162,128,2048\nMvaa 212,236,148\n"
"Ta 164,128,16384\nMvaa 212,232,144\nTa 166,128,18432\nMaaa 116,232,136\nTa 168,129,0\nD0 4,20,48,2048\nMaaa 116,236,140\nTa 170,129,2048\nMvaa 52,236,132\nTa 172,129,16384\n"
"Mvaa 52,232,128\nTa 174,129,18432\nMvaa 56,240,128\nTa 176,130,0\nMvaa 56,244,132\nTa 178,130,2048\nD0 5,20,48,3072\nMaaa 120,244,140\nTa 180,130,16384\nMaaa 120,240,136\n"
"Ta 182,130,18432\nMvaa 216,240,144\nTa 184,131,0\ns_add_u32 m0, s47, -32768\ns_add_u32 s45, s45, 128\nMvaa 216,244,148\nTa 186,131,2048\nMvaa 248,244,156\nTa 188,131,16384\n"
"D0 2,20,45,0\nMvaa 248,240,152\nTa 190,131,18432\nMvaa 252,248,152\nMvaa 252,252,156\nMvaa 220,252,148\nMvaa 220,248,144\nD0 3,20,45,1024\nMaaa 124,248,136\nMaaa 124,252,140\n"
"Mvaa 60,252,132\nMvaa 60,248,128\nWL 0\nMvaa 32,160,192\nTa 224,132,0\nD0 4,20,45,2048\nMvaa 32,164,196\nTa 226,132,2048\nMvaa 36,172,196\nMvaa 36,168,192\nTa 228,132,16384\n"
"Mvaa 40,176,192\nTa 230,132,18432\nMvaa 40,180,196\nD0 5,20,45,3072\nMvaa 44,188,196\nTa 232,133,0\nMvaa 44,184,192\nTa 234,133,2048\nMaaa 108,184,200\ns_add_u32 m0, s47, 32768\n"
"s_add_u32 s44, s44, 0x40000\nMaaa 108,188,204\nTa 236,133,16384\nMaaa 104,180,204\nTa 238,133,18432\nD0 6,24,44,0\nMaaa 104,176,200\nv_mul_f32 v32, s57, v32\n"
"v_mul_f32 v33, s57, v33\nMaaa 100,168,200\nTa 240,134,0\nCV 32,32,33\nMaaa 100,172,204\nTa 242,134,2048\nv_mul_f32 v34, s57, v34\nMaaa 96,164,204\nv_mul_f32 v35, s57, v35\n"
"CV 33,34,35\nMaaa 96,160,200\nTa 244,134,16384\nD0 7,24,44,1024\nMvaa 192,160,208\nTa 246,134,18432\nv_mul_f32 v36, s57, v36\nMvaa 192,164,212\nv_mul_f32 v37, s57, v37\n"
"Mvaa 196,172,212\nTa 248,135,0\nCV 34,36,37\nMvaa 196,168,208\nTa 250,135,2048\nv_mul_f32 v38, s57, v38\nMvaa 200,176,208\nD0 8,24,44,2048\nv_mul_f32 v39, s57, v39\n"
"Mvaa 200,180,212\nTa 252,135,16384\nCV 35,38,39\nMvaa 204,188,212\nTa 254,135,18432\nv_mul_f32 v40, s57, v40\nMvaa 204,184,208\nv_mul_f32 v41, s57, v41\nCV 40,40,41\n"
"Mvaa 236,184,216\nv_mul_f32 v42, s57, v42\nv_mul_f32 v43, s57, v43\nMvaa 236,188,220\nD0 9,24,44,3072\nMvaa 232,180,220\nCV 41,42,43\nv_mul_f32 v44, s57, v44\nMvaa 232,176,216\n"
"v_mul_f32 v45, s57, v45\nCV 42,44,45\nMvaa 228,168,216\nv_mul_f32 v46, s57, v46\nv_mul_f32 v47, s57, v47\nMvaa 228,172,220\nCV 43,46,47\nAR 20,96\nMvaa 224,164,220\nAR 21,97\n"
"v_mul_f32 v20, s57, v20\nMvaa 224,160,216\ns_waitcnt vmcnt(14) lgkmcnt(0)\ns_barrier\nMvaa 48,224,192\ns_add_u32 m0, s47, 49152\ns_add_u32 s44, s44, 0x40000\nRa 128,10,0\n"
"Mvaa 48,228,196\nRa 132,11,0\nMvaa 52,236,196\nD0 6,24,44,0\nRa 136,10,2048\nMvaa 52,232,192\nRa 140,11,2048\nv_mul_f32 v21, s57, v21\nMvaa 56,240,192\nRa 144,10,8192\n"
"CV 36,20,21\nMvaa 56,244,196\nRa 148,11,8192\nAR 20,98\nMvaa 60,252,196\nD0 7,24,44,1024\nRa 152,10,10240\nMvaa 60,248,192\nRa 156,11,10240\nAR 21,99\nMaaa 124,248,200\n"
"Ta 160,12,0\nv_mul_f32 v20, s57, v20\nMaaa 124,252,204\nTa 162,12,2048\nMaaa 120,244,204\nD0 8,24,44,2048\nTa 164,12,16384\nMaaa 120,240,200\nTa 166,12,18432\n"
"v_mul_f32 v21, s57, v21\nMaaa 116,232,200\nTa 168,13,0\nCV 37,20,21\nMaaa 116,236,204\nTa 170,13,2048\nAR 20,100\nMaaa 112,228,204\nD0 9,24,44,3072\nTa 172,13,16384\n"
"Maaa 112,224,200\nTa 174,13,18432\nAR 21,101\nMvaa 208,224,208\ns_add_u32 m0, s47, -16384\ns_add_u32 s48, s48, 128\nTa 176,14,0\nMvaa 208,228,212\nTa 178,14,2048\n"
"Mvaa 212,236,212\nD0 2,20,48,0\nTa 180,14,16384\nMvaa 212,232,208\nTa 182,14,18432\nv_mul_f32 v20, s57, v20\nMvaa 216,240,208\nTa 184,15,0\nv_mul_f32 v21, s57, v21\n"
"Mvaa 216,244,212\nTa 186,15,2048\nCV 38,20,21\nMvaa 220,252,212\nD0 3,20,48,1024\nTa 188,15,16384\nMvaa 220,248,208\nTa 190,15,18432\nAR 20,102\nMvaa 252,248,216\nAR 21,103\n"
"v_mul_f32 v20, s57, v20\nMvaa 252,252,220\nv_mul_f32 v21, s57, v21\nMvaa 248,244,220\nD0 4,20,48,2048\nCV 39,20,21\nMvaa 248,240,216\nAR 20,104\nAR 21,105\nMvaa 244,232,216\n"
"v_mul_f32 v20, s57, v20\nv_mul_f32 v21, s57, v21\nMvaa 244,236,220\nCV 44,20,21\nAR 20,106\nMvaa 240,228,220\nD0 5,20,48,3072\nAR 21,107\nMvaa 240,224,216\nWL 0\ns_barrier\n"
"Zvaa 64,128,160\nTa 224,16,0\nv_mul_f32 v20, s57, v20\nMvaa 64,132,164\nTa 226,16,2048\nZvaa 68,140,164\ns_add_u32 m0, s47, -65536\ns_add_u32 s45, s45, 128\nMvaa 68,136,160\n"
"Ta 228,16,16384\nv_mul_f32 v21, s57, v21\nZvaa 128,144,160\nTa 230,16,18432\nCV 45,20,21\nMvaa 128,148,164\nD0 2,20,45,0\nAR 20,108\nZvaa 132,156,164\nTa 232,17,0\nAR 21,109\n"
"Mvaa 132,152,160\nTa 234,17,2048\nv_mul_f32 v20, s57, v20\nZvaa 140,152,168\nv_mul_f32 v21, s57, v21\nCV 46,20,21\nMvaa 140,156,172\nTa 236,17,16384\ns_add_u32 s6, s56, 0\n"
"Sv 32,1,16,6,0\nZvaa 136,148,172\nTa 238,17,18432\nAR 20,110\nMvaa 136,144,168\nAR 21,111\nv_mul_f32 v20, s57, v20\nZvaa 76,136,168\nTa 240,18,0\nv_mul_f32 v21, s57, v21\n"
"Mvaa 76,140,172\nTa 242,18,2048\nCV 47,20,21\nZvaa 72,132,172\nD0 3,20,45,1024\nv_mul_f32 v48, s57, v48\nMvaa 72,128,168\nTa 244,18,16384\nv_mul_f32 v49, s57, v49\n"
"Zvaa 80,128,176\nTa 246,18,18432\nCV 48,48,49\nMvaa 80,132,180\nv_mul_f32 v50, s57, v50\nZvaa 84,140,180\nTa 248,19,0\nv_mul_f32 v51, s57, v51\nMvaa 84,136,176\nTa 250,19,2048\n"
"CV 49,50,51\nZvaa 144,144,176\nv_mul_f32 v52, s57, v52\nv_mul_f32 v53, s57, v53\nMvaa 144,148,180\nTa 252,19,16384\nCV 50,52,53\nZvaa 148,156,180\nTa 254,19,18432\n"
"v_mul_f32 v54, s57, v54\nMvaa 148,152,176\nD0 4,20,45,2048\nv_mul_f32 v55, s57, v55\nZvaa 156,152,184\nCV 51,54,55\nv_mul_f32 v56, s57, v56\nMvaa 156,156,188\n"
"v_mul_f32 v57, s57, v57\ns_add_u32 s6, s56, 0\nSv 40,1,16,6,2048\nZvaa 152,148,188\nCV 56,56,57\nv_mul_f32 v58, s57, v58\nMvaa 152,144,184\nv_mul_f32 v59, s57, v59\nCV 57,58,59\n"
"Zvaa 92,136,184\nv_mul_f32 v60, s57, v60\nv_mul_f32 v61, s57, v61\nMvaa 92,140,188\nD0 5,20,45,3072\nCV 58,60,61\nZvaa 88,132,188\nv_mul_f32 v62, s57, v62\n"
"v_mul_f32 v63, s57, v63\nMvaa 88,128,184\ns_waitcnt vmcnt(18) lgkmcnt(0)\ns_barrier\nZvaa 96,128,224\nRa 192,10,16384\nCV 59,62,63\nMvaa 96,132,228\ns_add_u32 m0, s47, 0\n"
"s_add_u32 s44, s44, 0x40000\nZvaa 100,140,228\nRa 196,11,16384\nAR 20,112\nMvaa 100,136,224\nAR 21,113\nv_mul_f32 v20, s57, v20\nZvaa 160,144,224\nRa 200,10,18432\n"
"v_mul_f32 v21, s57, v21\nMvaa 160,148,228\nD0 6,24,44,0\nCV 52,20,21\nZvaa 164,156,228\nRa 204,11,18432\nAR 20,114\nMvaa 164,152,224\nAR 21,115\nv_mul_f32 v20, s57, v20\n"
"Zvaa 172,152,232\nRa 208,10,24576\nv_mul_f32 v21, s57, v21\nMvaa 172,156,236\nCV 53,20,21\ns_add_u32 s6, s56, 0\nSv 36,1,16,6,1024\nZvaa 168,148,236\nRa 212,11,24576\nAR 20,116\n"
"Mvaa 168,144,232\nAR 21,117\nv_mul_f32 v20, s57, v20\nZvaa 108,136,232\nRa 216,10,26624\nv_mul_f32 v21, s57, v21\nMvaa 108,140,236\nD0 7,24,44,1024\nCV 54,20,21\nZvaa 104,132,236\n"
"Ra 220,11,26624\nAR 20,118\nMvaa 104,128,232\nAR 21,119\nv_mul_f32 v20, s57, v20\nZvaa 112,128,240\nv_mul_f32 v21, s57, v21\nCV 55,20,21\nMvaa 112,132,244\nAR 20,120\n"
"Zvaa 116,140,244\nAR 21,121\nv_mul_f32 v20, s57, v20\nMvaa 116,136,240\nv_mul_f32 v21, s57, v21\nCV 60,20,21\nZvaa 176,144,240\nAR 20,122\nAR 21,123\nMvaa 176,148,244\n"
"D0 8,24,44,2048\nv_mul_f32 v20, s57, v20\nZvaa 180,156,244\nv_mul_f32 v21, s57, v21\nCV 61,20,21\nMvaa 180,152,240\nAR 20,124\nAR 21,125\nZvaa 188,152,248\n"
"v_mul_f32 v20, s57, v20\nv_mul_f32 v21, s57, v21\nMvaa 188,156,252\nCV 62,20,21\ns_add_u32 s6, s56, 0\nSv 44,1,16,6,3072\nZvaa 184,148,252\nAR 20,126\nAR 21,127\nMvaa 184,144,248\n"
"v_mul_f32 v20, s57, v20\nv_mul_f32 v21, s57, v21\nZvaa 124,136,248\nCV 63,20,21\nv_mul_f32 v192, s57, v192\nMvaa 124,140,252\nD0 9,24,44,3072\nv_mul_f32 v193, s57, v193\n"
"Zvaa 120,132,252\nCV 192,192,193\nv_mul_f32 v194, s57, v194\nMvaa 120,128,248\nWL 0\nZaaa 32,192,224\nRa 128,10,32768\nv_mul_f32 v195, s57, v195\nMaaa 32,196,228\n"
"s_add_u32 m0, s47, 16384\ns_add_u32 s44, s44, 0x40000\nZaaa 36,204,228\nRa 132,11,32768\nCV 193,194,195\nMaaa 36,200,224\nv_mul_f32 v196, s57, v196\nv_mul_f32 v197, s57, v197\n"
"Zaaa 96,208,224\nRa 136,10,34816\nCV 194,196,197\nMaaa 96,212,228\nD0 6,24,44,0\nv_mul_f32 v198, s57, v198\nZaaa 100,220,228\nRa 140,11,34816\nv_mul_f32 v199, s57, v199\n"
"Maaa 100,216,224\nCV 195,198,199\nv_mul_f32 v200, s57, v200\nZaaa 108,216,232\nRa 144,10,40960\nv_mul_f32 v201, s57, v201\nMaaa 108,220,236\nCV 200,200,201\n"
"v_mul_f32 v202, s57, v202\ns_add_u32 s6, s56, 4096\nSv 48,1,16,6,0\nZaaa 104,212,236\nRa 148,11,40960\nv_mul_f32 v203, s57, v203\nMaaa 104,208,232\nCV 201,202,203\n"
"v_mul_f32 v204, s57, v204\nZaaa 44,200,232\nRa 152,10,43008\nv_mul_f32 v205, s57, v205\nMaaa 44,204,236\nD0 7,24,44,1024\nCV 202,204,205\nZaaa 40,196,236\nRa 156,11,43008\n"
"v_mul_f32 v206, s57, v206\nMaaa 40,192,232\nv_mul_f32 v207, s57, v207\nCV 203,206,207\nZaaa 48,192,240\nv_mul_f32 v208, s57, v208\nv_mul_f32 v209, s57, v209\nMaaa 48,196,244\n"
"CV 208,208,209\nv_mul_f32 v210, s57, v210\nZaaa 52,204,244\nv_mul_f32 v211, s57, v211\nCV 209,210,211\nMaaa 52,200,240\nv_mul_f32 v212, s57, v212\nv_mul_f32 v213, s57, v213\n"
"Zaaa 112,208,240\nCV 210,212,213\nv_mul_f32 v214, s57, v214\nMaaa 112,212,244\nD0 8,24,44,2048\nv_mul_f32 v215, s57, v215\nZaaa 116,220,244\nCV 211,214,215\n"
"v_mul_f32 v216, s57, v216\nMaaa 116,216,240\nv_mul_f32 v217, s57, v217\nCV 216,216,217\nZaaa 124,216,248\nv_mul_f32 v218, s57, v218\nv_mul_f32 v219, s57, v219\nMaaa 124,220,252\n"
"CV 217,218,219\nv_mul_f32 v220, s57, v220\ns_add_u32 s6, s56, 4096\nSv 56,1,16,6,2048\nZaaa 120,212,252\nv_mul_f32 v221, s57, v221\nCV 218,220,221\nMaaa 120,208,248\n"
"v_mul_f32 v222, s57, v222\nv_mul_f32 v223, s57, v223\nZaaa 60,200,248\nCV 219,222,223\nv_mul_f32 v224, s57, v224\nMaaa 60,204,252\nD0 9,24,44,3072\nv_mul_f32 v225, s57, v225\n"
"Zaaa 56,196,252\nCV 224,224,225\nv_mul_f32 v226, s57, v226\nMaaa 56,192,248\ns_waitcnt vmcnt(22) lgkmcnt(0)\ns_barrier\nZaaa 0,192,160\nTa 224,16,32768\nv_mul_f32 v227, s57, v227\n"
"Maaa 0,196,164\nTa 226,16,34816\nCV 225,226,227\nZaaa 4,204,164\ns_add_u32 m0, s47, -49152\ns_add_u32 s48, s48, 128\nMaaa 4,200,160\nTa 228,16,49152\nv_mul_f32 v228, s57, v228\n"
"Zaaa 64,208,160\nTa 230,16,51200\nv_mul_f32 v229, s57, v229\nMaaa 64,212,164\nD0 2,20,48,0\nCV 226,228,229\nZaaa 68,220,164\nTa 232,17,32768\nv_mul_f32 v230, s57, v230\n"
"Maaa 68,216,160\nTa 234,17,34816\nv_mul_f32 v231, s57, v231\nZaaa 76,216,168\nCV 227,230,231\nv_mul_f32 v232, s57, v232\nMaaa 76,220,172\nTa 236,17,49152\n"
"v_mul_f32 v233, s57, v233\ns_add_u32 s6, s56, 4096\nSv 52,1,16,6,1024\nZaaa 72,212,172\nTa 238,17,51200\nCV 232,232,233\nMaaa 72,208,168\nv_mul_f32 v234, s57, v234\n"
"v_mul_f32 v235, s57, v235\nZaaa 12,200,168\nTa 240,18,32768\nCV 233,234,235\nMaaa 12,204,172\nTa 242,18,34816\nv_mul_f32 v236, s57, v236\nZaaa 8,196,172\nD0 3,20,48,1024\n"
"v_mul_f32 v237, s57, v237\nMaaa 8,192,168\nTa 244,18,49152\nCV 234,236,237\nZaaa 16,192,176\nTa 246,18,51200\nv_mul_f32 v238, s57, v238\nMaaa 16,196,180\n"
"v_mul_f32 v239, s57, v239\nCV 235,238,239\nZaaa 20,204,180\nTa 248,19,32768\nv_mul_f32 v240, s57, v240\nMaaa 20,200,176\nTa 250,19,34816\nv_mul_f32 v241, s57, v241\n"
"Zaaa 80,208,176\nCV 240,240,241\nv_mul_f32 v242, s57, v242\nMaaa 80,212,180\nTa 252,19,49152\nv_mul_f32 v243, s57, v243\nZaaa 84,220,180\nTa 254,19,51200\nCV 241,242,243\n"
"Maaa 84,216,176\nD0 4,20,48,2048\nv_mul_f32 v244, s57, v244\nZaaa 92,216,184\nv_mul_f32 v245, s57, v245\nCV 242,244,245\nMaaa 92,220,188\nv_mul_f32 v246, s57, v246\n"
"v_mul_f32 v247, s57, v247\ns_add_u32 s6, s56, 4096\nSv 60,1,16,6,3072\nZaaa 88,212,188\nCV 243,246,247\nv_mul_f32 v248, s57, v248\nMaaa 88,208,184\nv_mul_f32 v249, s57, v249\n"
"CV 248,248,249\nZaaa 28,200,184\nv_mul_f32 v250, s57, v250\nv_mul_f32 v251, s57, v251\nMaaa 28,204,188\nD0 5,20,48,3072\nCV 249,250,251\nZaaa 24,196,188\n"
"v_mul_f32 v252, s57, v252\nv_mul_f32 v253, s57, v253\nMaaa 24,192,184\nWL 0\nMvaa 96,128,224\nTa 160,12,32768\nCV 250,252,253\nMvaa 96,132,228\nTa 162,12,34816\n"
"v_mul_f32 v254, s57, v254\nMvaa 100,140,228\ns_add_u32 m0, s47, -32768\ns_add_u32 s45, s45, 128\nMvaa 100,136,224\nTa 164,12,49152\nv_mul_f32 v255, s57, v255\nMvaa 160,144,224\n"
"Ta 166,12,51200\nCV 251,254,255\nMvaa 160,148,228\nD0 2,20,45,0\nMvaa 164,156,228\nTa 168,13,32768\nMvaa 164,152,224\nTa 170,13,34816\nMvaa 172,152,232\nMvaa 172,156,236\n"
"Ta 172,13,49152\ns_add_u32 s6, s53, 0\nSv 192,1,16,6,0\nMvaa 168,148,236\nTa 174,13,51200\nMvaa 168,144,232\nMvaa 108,136,232\nTa 176,14,32768\nMvaa 108,140,236\nTa 178,14,34816\n"
"Mvaa 104,132,236\nD0 3,20,45,1024\nMvaa 104,128,232\nTa 180,14,49152\nMvaa 112,128,240\nTa 182,14,51200\nMvaa 112,132,244\nMvaa 116,140,244\nTa 184,15,32768\nMvaa 116,136,240\n"
"Ta 186,15,34816\nMvaa 176,144,240\nMvaa 176,148,244\nTa 188,15,49152\nMvaa 180,156,244\nTa 190,15,51200\nMvaa 180,152,240\nD0 4,20,45,2048\nMvaa 188,152,248\nMvaa 188,156,252\n"
"s_add_u32 s6, s53, 0\nSv 200,1,16,6,2048\nMvaa 184,148,252\nMvaa 184,144,248\nMvaa 124,136,248\nMvaa 124,140,252\nD0 5,20,45,3072\nMvaa 120,132,252\nMvaa 120,128,248\n"
"s_waitcnt vmcnt(24) lgkmcnt(0)\ns_barrier\nMvaa 64,128,160\nRa 192,10,49152\nMvaa 64,132,164\ns_add_u32 m0, s47, 32768\ns_add_u32 s44, s44, 0x40000\nMvaa 68,140,164\n"
"Ra 196,11,49152\nMvaa 68,136,160\nMvaa 128,144,160\nRa 200,10,51200\nMvaa 128,148,164\nD0 6,24,44,0\nMvaa 132,156,164\nRa 204,11,51200\nMvaa 132,152,160\nMvaa 140,152,168\n"
"Ra 208,10,57344\nMvaa 140,156,172\ns_add_u32 s6, s53, 4096\nSv 208,1,16,6,0\nMvaa 136,148,172\nRa 212,11,57344\nMvaa 136,144,168\nMvaa 76,136,168\nRa 216,10,59392\n"
"Mvaa 76,140,172\nD0 7,24,44,1024\nMvaa 72,132,172\nRa 220,11,59392\nMvaa 72,128,168\nMvaa 80,128,176\nMvaa 80,132,180\nMvaa 84,140,180\nMvaa 84,136,176\nMvaa 144,144,176\n"
"Mvaa 144,148,180\nD0 8,24,44,2048\nMvaa 148,156,180\nMvaa 148,152,176\nMvaa 156,152,184\nMvaa 156,156,188\ns_add_u32 s6, s53, 4096\nSv 216,1,16,6,2048\nMvaa 152,148,188\n"
"Mvaa 152,144,184\nMvaa 92,136,184\nMvaa 92,140,188\nD0 9,24,44,3072\nMvaa 88,132,188\nMvaa 88,128,184\nWL 0\nMaaa 0,192,160\nRa 128,10,0\nMaaa 0,196,164\ns_add_u32 m0, s47, 49152\n"
"s_add_u32 s44, s44, 0x40000\nMaaa 4,204,164\nRa 132,11,0\nMaaa 4,200,160\nMaaa 64,208,160\nRa 136,10,2048\nMaaa 64,212,164\nD0 6,24,44,0\nMaaa 68,220,164\nRa 140,11,2048\n"
"Maaa 68,216,160\nMaaa 76,216,168\nRa 144,10,8192\nMaaa 76,220,172\ns_add_u32 s6, s53, 0\nSv 224,1,16,6,1024\nMaaa 72,212,172\nRa 148,11,8192\nMaaa 72,208,168\nMaaa 12,200,168\n"
"Ra 152,10,10240\nMaaa 12,204,172\nD0 7,24,44,1024\nMaaa 8,196,172\nRa 156,11,10240\nMaaa 8,192,168\nMaaa 16,192,176\nMaaa 16,196,180\nMaaa 20,204,180\nMaaa 20,200,176\n"
"Maaa 80,208,176\nMaaa 80,212,180\nD0 8,24,44,2048\nMaaa 84,220,180\nMaaa 84,216,176\nMaaa 92,216,184\nMaaa 92,220,188\ns_add_u32 s6, s53, 0\nSv 232,1,16,6,3072\nMaaa 88,212,188\n"
"Maaa 88,208,184\nMaaa 28,200,184\nMaaa 28,204,188\nD0 9,24,44,3072\nMaaa 24,196,188\nMaaa 24,192,184\ns_waitcnt vmcnt(24) lgkmcnt(0)\ns_barrier\nMaaa 32,192,224\nTa 160,12,0\n"
"Maaa 32,196,228\nTa 162,12,2048\nMaaa 36,204,228\ns_add_u32 m0, s47, -16384\ns_add_u32 s48, s48, 128\nMaaa 36,200,224\nTa 164,12,16384\nMaaa 96,208,224\nTa 166,12,18432\n"
"Maaa 96,212,228\nD0 2,20,48,0\nMaaa 100,220,228\nTa 168,13,0\nMaaa 100,216,224\nTa 170,13,2048\nMaaa 108,216,232\nMaaa 108,220,236\nTa 172,13,16384\ns_add_u32 s6, s53, 4096\n"
"Sv 240,1,16,6,1024\nMaaa 104,212,236\nTa 174,13,18432\nMaaa 104,208,232\nMaaa 44,200,232\nTa 176,14,0\nMaaa 44,204,236\nTa 178,14,2048\nMaaa 40,196,236\nD0 3,20,48,1024\n"
"Maaa 40,192,232\nTa 180,14,16384\nMaaa 48,192,240\nTa 182,14,18432\nMaaa 48,196,244\ns_add_u32 s6, s53, 4096\nSv 248,1,16,6,3072\nMaaa 52,204,244\nTa 184,15,0\nMaaa 52,200,240\n"
"Ta 186,15,2048\nMaaa 112,208,240\nMaaa 112,212,244\nTa 188,15,16384\nMaaa 116,220,244\nTa 190,15,18432\nMaaa 116,216,240\nD0 4,20,48,2048\nMaaa 124,216,248\nMaaa 124,220,252\n"
"Maaa 120,212,252\nMaaa 120,208,248\ns_lshl3_add_u32 s90, s12, s13\ns_lshl2_add_u32 s90, s90, s11\ns_lshl2_add_u32 s90, s90, 0xc04a400\nMaaa 60,200,248\nMaaa 60,204,252\n"
"D0 5,20,48,3072\nMaaa 56,196,252\nMaaa 56,192,248\nWL 0\ns_mov_b64 s[76:77], s[62:63]\ns_mov_b64 s[78:79], s[64:65]\ns_mov_b32 s42, 1605632\ns_mov_b32 s50, 1\n.GL149:\n"
"Mvaa 64,128,160\nTa 224,16,0\nMvaa 64,132,164\nTa 226,16,2048\nMvaa 68,140,164\ns_add_u32 m0, s47, -65536\ns_add_u32 s45, s45, 128\nMvaa 68,136,160\nTa 228,16,16384\n"
"Mvaa 128,144,160\nTa 230,16,18432\nMvaa 128,148,164\nD0 2,20,45,0\nMvaa 132,156,164\nTa 232,17,0\nMvaa 132,152,160\nTa 234,17,2048\nMvaa 140,152,168\nMvaa 140,156,172\n"
"Ta 236,17,16384\nMvaa 136,148,172\nTa 238,17,18432\nMvaa 136,144,168\nMvaa 76,136,168\nTa 240,18,0\nMvaa 76,140,172\nTa 242,18,2048\nMvaa 72,132,172\nD0 3,20,45,1024\n"
"Mvaa 72,128,168\nTa 244,18,16384\nMvaa 80,128,176\nTa 246,18,18432\nMvaa 80,132,180\nMvaa 84,140,180\nTa 248,19,0\nMvaa 84,136,176\nTa 250,19,2048\nMvaa 144,144,176\n"
"Mvaa 144,148,180\nTa 252,19,16384\nMvaa 148,156,180\nTa 254,19,18432\nMvaa 148,152,176\nD0 4,20,45,2048\nMvaa 156,152,184\nMvaa 156,156,188\nMvaa 152,148,188\nMvaa 152,144,184\n"
"s_mov_b32 s40, s76\ns_and_b32 s41, s77, 0xffff\nMvaa 92,136,184\nMvaa 92,140,188\nD0 5,20,45,3072\nMvaa 88,132,188\nMvaa 88,128,184\ns_waitcnt vmcnt(17) lgkmcnt(0)\ns_barrier\n"
"Mvaa 96,128,224\nRa 192,10,16384\nMvaa 96,132,228\ns_add_u32 m0, s47, 0\ns_add_u32 s44, s44, 0x40000\nbuffer_load_dword v31, v26, s[40:43], s97 offen\n"
"s_mov_b64 s[76:77], s[66:67]\nMvaa 100,140,228\nRa 196,11,16384\nMvaa 100,136,224\nMvaa 160,144,224\nRa 200,10,18432\nMvaa 160,148,228\nD0 6,24,44,0\nMvaa 164,156,228\n"
"Ra 204,11,18432\nMvaa 164,152,224\nMvaa 172,152,232\nRa 208,10,24576\nMvaa 172,156,236\nMvaa 168,148,236\nRa 212,11,24576\nMvaa 168,144,232\nMvaa 108,136,232\nRa 216,10,26624\n"
"Mvaa 108,140,236\nD0 7,24,44,1024\nMvaa 104,132,236\nRa 220,11,26624\nMvaa 104,128,232\nMvaa 112,128,240\nMvaa 112,132,244\nMvaa 116,140,244\nMvaa 116,136,240\nMvaa 176,144,240\n"
"Mvaa 176,148,244\nD0 8,24,44,2048\nMvaa 180,156,244\nMvaa 180,152,240\nMvaa 188,152,248\nMvaa 188,156,252\nMvaa 184,148,252\nMvaa 184,144,248\nMvaa 124,136,248\nMvaa 124,140,252\n"
"D0 9,24,44,3072\nMvaa 120,132,252\nMvaa 120,128,248\nWL 0\nMaaa 32,192,224\nRa 128,10,32768\nMaaa 32,196,228\ns_add_u32 m0, s47, 16384\ns_add_u32 s44, s44, 0x40000\n"
"Maaa 36,204,228\nRa 132,11,32768\nMaaa 36,200,224\nMaaa 96,208,224\nRa 136,10,34816\nMaaa 96,212,228\nD0 6,24,44,0\nMaaa 100,220,228\nRa 140,11,34816\nMaaa 100,216,224\n"
"Maaa 108,216,232\nRa 144,10,40960\nMaaa 108,220,236\nMaaa 104,212,236\nRa 148,11,40960\nMaaa 104,208,232\nMaaa 44,200,232\nRa 152,10,43008\nMaaa 44,204,236\nD0 7,24,44,1024\n"
"Maaa 40,196,236\nRa 156,11,43008\nMaaa 40,192,232\nMaaa 48,192,240\nMaaa 48,196,244\nMaaa 52,204,244\nMaaa 52,200,240\nMaaa 112,208,240\nMaaa 112,212,244\nD0 8,24,44,2048\n"
"Maaa 116,220,244\nMaaa 116,216,240\nMaaa 124,216,248\nMaaa 124,220,252\nMaaa 120,212,252\nMaaa 120,208,248\nMaaa 60,200,248\nMaaa 60,204,252\nD0 9,24,44,3072\nMaaa 56,196,252\n"
"Maaa 56,192,248\ns_waitcnt vmcnt(17) lgkmcnt(0)\ns_barrier\nMaaa 0,192,160\nTa 224,16,32768\nMaaa 0,196,164\nTa 226,16,34816\nMaaa 4,204,164\ns_add_u32 m0, s47, -49152\n"
"s_add_u32 s48, s48, 128\nMaaa 4,200,160\nTa 228,16,49152\nMaaa 64,208,160\nTa 230,16,51200\nMaaa 64,212,164\nD0 2,20,48,0\nMaaa 68,220,164\nTa 232,17,32768\nMaaa 68,216,160\n"
"Ta 234,17,34816\nMaaa 76,216,168\nMaaa 76,220,172\nTa 236,17,49152\nMaaa 72,212,172\nTa 238,17,51200\nMaaa 72,208,168\nMaaa 12,200,168\nTa 240,18,32768\nMaaa 12,204,172\n"
"Ta 242,18,34816\nMaaa 8,196,172\nD0 3,20,48,1024\nMaaa 8,192,168\nTa 244,18,49152\nMaaa 16,192,176\nTa 246,18,51200\nMaaa 16,196,180\nMaaa 20,204,180\nTa 248,19,32768\n"
"Maaa 20,200,176\nTa 250,19,34816\nMaaa 80,208,176\nMaaa 80,212,180\nTa 252,19,49152\nMaaa 84,220,180\nTa 254,19,51200\nMaaa 84,216,176\nD0 4,20,48,2048\nMaaa 92,216,184\n"
"Maaa 92,220,188\nMaaa 88,212,188\nMaaa 88,208,184\nMaaa 28,200,184\nMaaa 28,204,188\nD0 5,20,48,3072\nMaaa 24,196,188\nMaaa 24,192,184\nWL 0\nMvaa 96,128,224\nTa 160,12,32768\n"
"Mvaa 96,132,228\nTa 162,12,34816\nMvaa 100,140,228\ns_add_u32 m0, s47, -32768\ns_add_u32 s45, s45, 128\nMvaa 100,136,224\nTa 164,12,49152\nMvaa 160,144,224\nTa 166,12,51200\n"
"Mvaa 160,148,228\nD0 2,20,45,0\nMvaa 164,156,228\nTa 168,13,32768\nMvaa 164,152,224\nTa 170,13,34816\nMvaa 172,152,232\nMvaa 172,156,236\nTa 172,13,49152\nMvaa 168,148,236\n"
"Ta 174,13,51200\nMvaa 168,144,232\nMvaa 108,136,232\nTa 176,14,32768\nMvaa 108,140,236\nTa 178,14,34816\nMvaa 104,132,236\nD0 3,20,45,1024\nMvaa 104,128,232\nTa 180,14,49152\n"
"Mvaa 112,128,240\nTa 182,14,51200\nMvaa 112,132,244\nMvaa 116,140,244\nTa 184,15,32768\nMvaa 116,136,240\nTa 186,15,34816\nMvaa 176,144,240\nMvaa 176,148,244\nTa 188,15,49152\n"
"Mvaa 180,156,244\nTa 190,15,51200\nMvaa 180,152,240\nD0 4,20,45,2048\nMvaa 188,152,248\nMvaa 188,156,252\nMvaa 184,148,252\nMvaa 184,144,248\ns_mov_b32 s40, s78\n"
"s_and_b32 s41, s79, 0xffff\nMvaa 124,136,248\nMvaa 124,140,252\nD0 5,20,45,3072\nMvaa 120,132,252\nMvaa 120,128,248\ns_waitcnt vmcnt(17) lgkmcnt(0)\ns_barrier\nMvaa 64,128,160\n"
"Ra 192,10,49152\nMvaa 64,132,164\ns_add_u32 m0, s47, 32768\ns_add_u32 s44, s44, 0x40000\nbuffer_load_dword v31, v26, s[40:43], s97 offen\ns_mov_b64 s[78:79], s[66:67]\n"
"Mvaa 68,140,164\nRa 196,11,49152\nMvaa 68,136,160\nMvaa 128,144,160\nRa 200,10,51200\nMvaa 128,148,164\nD0 6,24,44,0\nMvaa 132,156,164\nRa 204,11,51200\nMvaa 132,152,160\n"
"Mvaa 140,152,168\nRa 208,10,57344\nMvaa 140,156,172\nMvaa 136,148,172\nRa 212,11,57344\nMvaa 136,144,168\nMvaa 76,136,168\nRa 216,10,59392\nMvaa 76,140,172\nD0 7,24,44,1024\n"
"Mvaa 72,132,172\nRa 220,11,59392\nMvaa 72,128,168\nMvaa 80,128,176\nMvaa 80,132,180\nMvaa 84,140,180\nMvaa 84,136,176\nMvaa 144,144,176\nMvaa 144,148,180\nD0 8,24,44,2048\n"
"Mvaa 148,156,180\nMvaa 148,152,176\nMvaa 156,152,184\nMvaa 156,156,188\nMvaa 152,148,188\nMvaa 152,144,184\nMvaa 92,136,184\nMvaa 92,140,188\nD0 9,24,44,3072\nMvaa 88,132,188\n"
"Mvaa 88,128,184\nWL 0\nMaaa 0,192,160\nRa 128,10,0\nMaaa 0,196,164\ns_add_u32 m0, s47, 49152\ns_add_u32 s44, s44, 0x40000\nMaaa 4,204,164\nRa 132,11,0\nMaaa 4,200,160\n"
"Maaa 64,208,160\nRa 136,10,2048\nMaaa 64,212,164\nD0 6,24,44,0\nMaaa 68,220,164\nRa 140,11,2048\nMaaa 68,216,160\nMaaa 76,216,168\nRa 144,10,8192\nMaaa 76,220,172\n"
"Maaa 72,212,172\nRa 148,11,8192\nMaaa 72,208,168\nMaaa 12,200,168\nRa 152,10,10240\nMaaa 12,204,172\nD0 7,24,44,1024\nMaaa 8,196,172\nRa 156,11,10240\nMaaa 8,192,168\n"
"Maaa 16,192,176\nMaaa 16,196,180\nMaaa 20,204,180\nMaaa 20,200,176\nMaaa 80,208,176\nMaaa 80,212,180\nD0 8,24,44,2048\nMaaa 84,220,180\nMaaa 84,216,176\nMaaa 92,216,184\n"
"Maaa 92,220,188\nMaaa 88,212,188\nMaaa 88,208,184\nMaaa 28,200,184\nMaaa 28,204,188\nD0 9,24,44,3072\nMaaa 24,196,188\nMaaa 24,192,184\ns_waitcnt vmcnt(17) lgkmcnt(0)\ns_barrier\n"
"Maaa 32,192,224\nTa 160,12,0\nMaaa 32,196,228\nTa 162,12,2048\nMaaa 36,204,228\ns_add_u32 m0, s47, -16384\ns_add_u32 s48, s48, 128\nMaaa 36,200,224\nTa 164,12,16384\n"
"Maaa 96,208,224\nTa 166,12,18432\nMaaa 96,212,228\nD0 2,20,48,0\nMaaa 100,220,228\nTa 168,13,0\nMaaa 100,216,224\nTa 170,13,2048\nMaaa 108,216,232\nMaaa 108,220,236\n"
"Ta 172,13,16384\nMaaa 104,212,236\nTa 174,13,18432\nMaaa 104,208,232\nMaaa 44,200,232\nTa 176,14,0\nMaaa 44,204,236\nTa 178,14,2048\nMaaa 40,196,236\nD0 3,20,48,1024\n"
"Maaa 40,192,232\nTa 180,14,16384\nMaaa 48,192,240\nTa 182,14,18432\nMaaa 48,196,244\nMaaa 52,204,244\nTa 184,15,0\nMaaa 52,200,240\nTa 186,15,2048\nMaaa 112,208,240\n"
"Maaa 112,212,244\nTa 188,15,16384\nMaaa 116,220,244\nTa 190,15,18432\nMaaa 116,216,240\nD0 4,20,48,2048\nMaaa 124,216,248\nMaaa 124,220,252\nMaaa 120,212,252\nMaaa 120,208,248\n"
"Maaa 60,200,248\nMaaa 60,204,252\nD0 5,20,48,3072\ns_sub_u32 s50, s50, 1\nMaaa 56,196,252\nMaaa 56,192,248\nWL 0\ns_cbranch_scc0 .GL149\nMvaa 64,128,160\nTa 224,16,0\n"
"Mvaa 64,132,164\nTa 226,16,2048\nMvaa 68,140,164\ns_add_u32 m0, s47, -65536\ns_add_u32 s45, s45, 128\nMvaa 68,136,160\nTa 228,16,16384\nMvaa 128,144,160\nTa 230,16,18432\n"
"Mvaa 128,148,164\nD0 2,20,45,0\nMvaa 132,156,164\nTa 232,17,0\nMvaa 132,152,160\nTa 234,17,2048\nMvaa 140,152,168\ns_and_b32 s2, s74, 15\nMvaa 140,156,172\nTa 236,17,16384\n"
"buffer_store_dword v25, off, s[16:19], s90 offset:0 sc0 sc1\nMvaa 136,148,172\nTa 238,17,18432\nMvaa 136,144,168\ns_sub_u32 s2, s2, 2\ns_cmp_lt_u32 s2, 14\n"
"s_cselect_b32 s5, 0, 0x40000000\nMvaa 76,136,168\nTa 240,18,0\nMvaa 76,140,172\nTa 242,18,2048\nMvaa 72,132,172\nD0 3,20,45,1024\nMvaa 72,128,168\nTa 244,18,16384\n"
"Mvaa 80,128,176\nTa 246,18,18432\nMvaa 80,132,180\ns_and_b32 s2, s2, 15\nMvaa 84,140,180\nTa 248,19,0\nMvaa 84,136,176\nTa 250,19,2048\nMvaa 144,144,176\ns_mul_i32 s4, s73, 14\n"
"Mvaa 144,148,180\nTa 252,19,16384\nMvaa 148,156,180\nTa 254,19,18432\nMvaa 148,152,176\nD0 4,20,45,2048\nMvaa 156,152,184\ns_add_u32 s4, s4, s2\nMvaa 156,156,188\n"
"s_mov_b32 s36, s62\nMvaa 152,148,188\ns_and_b32 s37, s63, 0xffff\nMvaa 152,144,184\ns_mov_b32 s38, 0x188000\nMvaa 92,136,184\ns_mov_b32 s39, 0x20000\nMvaa 92,140,188\n"
"D0 5,20,45,3072\nMvaa 88,132,188\ns_mov_b32 s40, s64\nMvaa 88,128,184\ns_waitcnt vmcnt(18) lgkmcnt(0)\ns_barrier\nMvaa 96,128,224\nRa 192,10,16384\nMvaa 96,132,228\n"
"s_add_u32 m0, s47, 0\ns_add_u32 s44, s44, 0x40000\nMvaa 100,140,228\nRa 196,11,16384\nMvaa 100,136,224\ns_and_b32 s41, s65, 0xffff\nMvaa 160,144,224\nRa 200,10,18432\n"
"Mvaa 160,148,228\nD0 6,24,44,0\nMvaa 164,156,228\nRa 204,11,18432\nMvaa 164,152,224\ns_mov_b32 s42, 0x188000\nMvaa 172,152,232\nRa 208,10,24576\nMvaa 172,156,236\n"
"s_mov_b32 s43, 0x20000\nMvaa 168,148,236\nRa 212,11,24576\nMvaa 168,144,232\ns_mov_b32 s92, s66\nMvaa 108,136,232\nRa 216,10,26624\nMvaa 108,140,236\nD0 7,24,44,1024\n"
"Mvaa 104,132,236\nRa 220,11,26624\nMvaa 104,128,232\ns_and_b32 s93, s67, 0xffff\nMvaa 112,128,240\ns_mov_b32 s94, 0x188000\nMvaa 112,132,244\ns_mov_b32 s95, 0x20000\n"
"Mvaa 116,140,244\nv_lshrrev_b32 v252, 1, v0\nMvaa 116,136,240\nv_mul_u32_u24 v253, 14336, v252\nMvaa 176,144,240\nv_and_b32 v254, 1, v0\nMvaa 176,148,244\nD0 8,24,44,2048\n"
"Mvaa 180,156,244\nv_lshl_add_u32 v253, v254, 4, v253\nMvaa 180,152,240\nv_mov_b32 v254, 0x40000000\nv_cmp_gt_u32 vcc, 56, v0\nv_lshlrev_b32 v252, 3, v252\n"
"s_mul_i32 s2, s11, 401408\nv_cndmask_b32 v21, v254, v253, vcc\nv_cndmask_b32 v255, v254, v252, vcc\nMvaa 188,152,248\ns_lshl_b32 s3, s4, 5\nMvaa 188,156,252\ns_add_u32 s2, s2, s3\n"
"Mvaa 184,148,252\ns_or_b32 s86, s2, s5\nMvaa 184,144,248\ns_add_u32 s87, s86, 7168\nMvaa 124,136,248\nv_lshrrev_b32 v251, 4, v0\nMvaa 124,140,252\nD0 9,24,44,3072\n"
"Mvaa 120,132,252\nv_mul_u32_u24 v252, 3584, v251\nMvaa 120,128,248\nWL 0\nMaaa 32,192,224\nRa 128,10,32768\nMaaa 32,196,228\ns_add_u32 m0, s47, 16384\ns_add_u32 s44, s44, 0x40000\n"
"Maaa 36,204,228\nRa 132,11,32768\nMaaa 36,200,224\nv_and_b32 v251, 15, v0\nMaaa 96,208,224\nRa 136,10,34816\nMaaa 96,212,228\nD0 6,24,44,0\nMaaa 100,220,228\nRa 140,11,34816\n"
"Maaa 100,216,224\nv_lshl_add_u32 v252, v251, 4, v252\nv_cmp_gt_u32 vcc, 14, v251\ns_mul_i32 s2, s4, 14336\ns_mul_i32 s3, s11, 224\nv_cndmask_b32 v20, v254, v252, vcc\n"
"Maaa 108,216,232\nRa 144,10,40960\nMaaa 108,220,236\ns_add_u32 s2, s2, s3\nMaaa 104,212,236\nRa 148,11,40960\nMaaa 104,208,232\ns_or_b32 s88, s2, s5\nMaaa 44,200,232\n"
"Ra 152,10,43008\nMaaa 44,204,236\nD0 7,24,44,1024\nMaaa 40,196,236\nRa 156,11,43008\nMaaa 40,192,232\nMaaa 48,192,240\nMaaa 48,196,244\nMaaa 52,204,244\nMaaa 52,200,240\n"
"Maaa 112,208,240\nMaaa 112,212,244\nD0 8,24,44,2048\nMaaa 116,220,244\nMaaa 116,216,240\nMaaa 124,216,248\nMaaa 124,220,252\nMaaa 120,212,252\nMaaa 120,208,248\nMaaa 60,200,248\n"
"Maaa 60,204,252\nD0 9,24,44,3072\nMaaa 56,196,252\nMaaa 56,192,248\ns_waitcnt vmcnt(17) lgkmcnt(0)\ns_barrier\nMaaa 0,192,160\nTa 224,16,32768\nMaaa 0,196,164\nTa 226,16,34816\n"
"buffer_load_dwordx4 v[192:195], v21, s[36:39], s86 offen offset:0\nMaaa 4,204,164\ns_add_u32 m0, s47, -49152\ns_add_u32 s48, s48, 128\nMaaa 4,200,160\nTa 228,16,49152\n"
"s_lshl_b32 s6, s11, 12\ns_add_u32 s6, s6, 0xc044000\nMaaa 64,208,160\nTa 230,16,51200\nMaaa 64,212,164\nD0 2,20,48,0\nMaaa 68,220,164\nTa 232,17,32768\nMaaa 68,216,160\n"
"Ta 234,17,34816\nMaaa 76,216,168\nbuffer_load_dwordx4 v[32:35], v1, s[16:19], s6 offen offset:0 sc0 sc1\nMaaa 76,220,172\nTa 236,17,49152\nMaaa 72,212,172\nTa 238,17,51200\n"
"Maaa 72,208,168\nMaaa 12,200,168\nTa 240,18,32768\nMaaa 12,204,172\nTa 242,18,34816\nMaaa 8,196,172\nD0 3,20,48,1024\nMaaa 8,192,168\nTa 244,18,49152\nMaaa 16,192,176\n"
"Ta 246,18,51200\nMaaa 16,196,180\nbuffer_load_dwordx4 v[36:39], v1, s[16:19], s6 offen offset:1024 sc0 sc1\nMaaa 20,204,180\nTa 248,19,32768\nMaaa 20,200,176\nTa 250,19,34816\n"
"Maaa 80,208,176\nMaaa 80,212,180\nTa 252,19,49152\nMaaa 84,220,180\nTa 254,19,51200\nMaaa 84,216,176\nD0 4,20,48,2048\nMaaa 92,216,184\n"
"buffer_load_dwordx4 v[40:43], v1, s[16:19], s6 offen offset:2048 sc0 sc1\nMaaa 92,220,188\nMaaa 88,212,188\nMaaa 88,208,184\nMaaa 28,200,184\n"
"buffer_load_dwordx4 v[44:47], v1, s[16:19], s6 offen offset:3072 sc0 sc1\nMaaa 28,204,188\nD0 5,20,48,3072\nMaaa 24,196,188\nMaaa 24,192,184\nWL 0\nMvaa 96,128,224\n"
"Ta 160,12,32768\nMvaa 96,132,228\nTa 162,12,34816\nMvaa 100,140,228\ns_add_u32 m0, s47, -32768\ns_add_u32 s45, s45, 128\nMvaa 100,136,224\nTa 164,12,49152\nMvaa 160,144,224\n"
"Ta 166,12,51200\nMvaa 160,148,228\nD0 2,20,45,0\nMvaa 164,156,228\nTa 168,13,32768\nMvaa 164,152,224\nTa 170,13,34816\nMvaa 172,152,232\nMvaa 172,156,236\nTa 172,13,49152\n"
"Mvaa 168,148,236\nTa 174,13,51200\nMvaa 168,144,232\nMvaa 108,136,232\nTa 176,14,32768\nMvaa 108,140,236\nTa 178,14,34816\nMvaa 104,132,236\nD0 3,20,45,1024\nMvaa 104,128,232\n"
"Ta 180,14,49152\nMvaa 112,128,240\nTa 182,14,51200\nMvaa 112,132,244\nMvaa 116,140,244\nTa 184,15,32768\nMvaa 116,136,240\nTa 186,15,34816\nMvaa 176,144,240\nMvaa 176,148,244\n"
"Ta 188,15,49152\nMvaa 180,156,244\nTa 190,15,51200\nMvaa 180,152,240\nD0 4,20,45,2048\nMvaa 188,152,248\nMvaa 188,156,252\nMvaa 184,148,252\nMvaa 184,144,248\nMvaa 124,136,248\n"
"Mvaa 124,140,252\nD0 5,20,45,3072\nMvaa 120,132,252\nMvaa 120,128,248\ns_waitcnt vmcnt(21) lgkmcnt(0)\ns_barrier\nMvaa 64,128,160\nRa 192,10,49152\nMvaa 64,132,164\n"
"s_add_u32 m0, s47, 32768\ns_add_u32 s44, s44, 0x40000\nbuffer_load_dwordx4 v[208:211], v21, s[40:43], s86 offen offset:0\nMvaa 68,140,164\nRa 196,11,49152\nMvaa 68,136,160\n"
"Mvaa 128,144,160\nRa 200,10,51200\nMvaa 128,148,164\nD0 6,24,44,0\nMvaa 132,156,164\nRa 204,11,51200\nMvaa 132,152,160\nMvaa 140,152,168\nRa 208,10,57344\nMvaa 140,156,172\n"
"Mvaa 136,148,172\nRa 212,11,57344\nMvaa 136,144,168\nMvaa 76,136,168\nRa 216,10,59392\nMvaa 76,140,172\nD0 7,24,44,1024\nMvaa 72,132,172\nRa 220,11,59392\nMvaa 72,128,168\n"
"Mvaa 80,128,176\nMvaa 80,132,180\nMvaa 84,140,180\nMvaa 84,136,176\nMvaa 144,144,176\nMvaa 144,148,180\nD0 8,24,44,2048\nMvaa 148,156,180\nMvaa 148,152,176\nMvaa 156,152,184\n"
"Mvaa 156,156,188\nMvaa 152,148,188\nMvaa 152,144,184\nMvaa 92,136,184\nMvaa 92,140,188\nD0 9,24,44,3072\nMvaa 88,132,188\nMvaa 88,128,184\nWL 0\nMaaa 0,192,160\nRa 128,10,0\n"
"Maaa 0,196,164\ns_add_u32 m0, s47, 49152\ns_add_u32 s44, s44, 0x40000\nMaaa 4,204,164\nRa 132,11,0\nMaaa 4,200,160\nMaaa 64,208,160\nRa 136,10,2048\nMaaa 64,212,164\nD0 6,24,44,0\n"
"Maaa 68,220,164\nRa 140,11,2048\nMaaa 68,216,160\nMaaa 76,216,168\nRa 144,10,8192\nMaaa 76,220,172\nMaaa 72,212,172\nRa 148,11,8192\nMaaa 72,208,168\nMaaa 12,200,168\n"
"Ra 152,10,10240\nMaaa 12,204,172\nD0 7,24,44,1024\nMaaa 8,196,172\nRa 156,11,10240\nMaaa 8,192,168\nMaaa 16,192,176\nMaaa 16,196,180\nMaaa 20,204,180\nMaaa 20,200,176\n"
"Maaa 80,208,176\nMaaa 80,212,180\nD0 8,24,44,2048\nMaaa 84,220,180\nMaaa 84,216,176\nMaaa 92,216,184\nMaaa 92,220,188\nMaaa 88,212,188\nMaaa 88,208,184\nMaaa 28,200,184\n"
"Maaa 28,204,188\nD0 9,24,44,3072\nMaaa 24,196,188\nMaaa 24,192,184\ns_waitcnt vmcnt(14) lgkmcnt(0)\ns_barrier\nMaaa 32,192,224\nTa 160,12,0\nv_xor_b32 v48, s15, v33\n"
"Maaa 32,196,228\nTa 162,12,2048\nbuffer_load_dwordx4 v[196:199], v21, s[36:39], s86 offen offset:3584\nMaaa 36,204,228\ns_add_u32 m0, s47, -16384\ns_add_u32 s48, s48, 128\n"
"Maaa 36,200,224\nTa 164,12,16384\nMaaa 96,208,224\nTa 166,12,18432\nMaaa 96,212,228\nD0 2,20,48,0\nMaaa 100,220,228\nTa 168,13,0\nMaaa 100,216,224\nTa 170,13,2048\n"
"Maaa 108,216,232\nv_xor_b32 v49, s15, v35\nMaaa 108,220,236\nTa 172,13,16384\nMaaa 104,212,236\nTa 174,13,18432\nMaaa 104,208,232\nv_xor_b32 v50, s15, v37\nMaaa 44,200,232\n"
"Ta 176,14,0\nMaaa 44,204,236\nTa 178,14,2048\nMaaa 40,196,236\nD0 3,20,48,1024\nMaaa 40,192,232\nTa 180,14,16384\nMaaa 48,192,240\nTa 182,14,18432\nv_xor_b32 v51, s15, v39\n"
"Maaa 48,196,244\nv_xor_b32 v52, s15, v41\nMaaa 52,204,244\nTa 184,15,0\nMaaa 52,200,240\nTa 186,15,2048\nMaaa 112,208,240\nv_xor_b32 v53, s15, v43\nMaaa 112,212,244\n"
"Ta 188,15,16384\nMaaa 116,220,244\nTa 190,15,18432\nMaaa 116,216,240\nD0 4,20,48,2048\nMaaa 124,216,248\nv_xor_b32 v54, s15, v45\nMaaa 124,220,252\nv_xor_b32 v55, s15, v47\n"
"Maaa 120,212,252\nv_or3_b32 v48, v48, v49, v50\nMaaa 120,208,248\nv_or3_b32 v51, v51, v52, v53\nMaaa 60,200,248\nv_or3_b32 v48, v48, v54, v55\nMaaa 60,204,252\nD0 5,20,48,3072\n"
"Maaa 56,196,252\nv_or_b32 v48, v48, v51\nMaaa 56,192,248\nWL 0\nMvaa 64,128,160\nTa 224,16,0\nv_cmp_ne_u32 vcc, 0, v48\nMvaa 64,132,164\nTa 226,16,2048\nMvaa 68,140,164\n"
"s_add_u32 m0, s47, -65536\ns_add_u32 s45, s45, 128\nMvaa 68,136,160\nTa 228,16,16384\nMvaa 128,144,160\nTa 230,16,18432\nMvaa 128,148,164\nD0 2,20,45,0\nMvaa 132,156,164\n"
"Ta 232,17,0\nMvaa 132,152,160\nTa 234,17,2048\nMvaa 140,152,168\ns_cbranch_vccnz .S143\n.S144:\nMvaa 140,156,172\nTa 236,17,16384\nMvaa 136,148,172\nTa 238,17,18432\n"
"Mvaa 136,144,168\nv_max_u32 v48, v32, v34\nMvaa 76,136,168\nTa 240,18,0\nMvaa 76,140,172\nTa 242,18,2048\nMvaa 72,132,172\nD0 3,20,45,1024\nMvaa 72,128,168\nTa 244,18,16384\n"
"Mvaa 80,128,176\nTa 246,18,18432\nv_min_u32 v49, v32, v34\nMvaa 80,132,180\nv_med3_u32 v49, v48, v49, v36\nMvaa 84,140,180\nTa 248,19,0\nMvaa 84,136,176\nTa 250,19,2048\n"
"Mvaa 144,144,176\nv_max_u32 v48, v48, v36\nMvaa 144,148,180\nTa 252,19,16384\nMvaa 148,156,180\nTa 254,19,18432\nMvaa 148,152,176\nD0 4,20,45,2048\nMvaa 156,152,184\n"
"v_med3_u32 v49, v48, v49, v38\nMvaa 156,156,188\nv_max_u32 v48, v48, v38\nMvaa 152,148,188\nv_med3_u32 v49, v48, v49, v40\nMvaa 152,144,184\nv_max_u32 v48, v48, v40\n"
"Mvaa 92,136,184\nv_med3_u32 v49, v48, v49, v42\nMvaa 92,140,188\nD0 5,20,45,3072\nMvaa 88,132,188\nv_max_u32 v48, v48, v42\nMvaa 88,128,184\ns_waitcnt vmcnt(18) lgkmcnt(0)\n"
"s_barrier\nMvaa 96,128,224\nRa 192,10,16384\nv_med3_u32 v49, v48, v49, v44\nMvaa 96,132,228\ns_add_u32 m0, s47, 0\ns_add_u32 s44, s44, 0x40000\n"
"buffer_load_dwordx4 v[212:215], v21, s[40:43], s86 offen offset:3584\nMvaa 100,140,228\nRa 196,11,16384\nMvaa 100,136,224\nv_max_u32 v48, v48, v44\nMvaa 160,144,224\n"
"Ra 200,10,18432\nMvaa 160,148,228\nD0 6,24,44,0\nMvaa 164,156,228\nRa 204,11,18432\nMvaa 164,152,224\nv_med3_u32 v49, v48, v49, v46\nMvaa 172,152,232\nRa 208,10,24576\n"
"Mvaa 172,156,236\nv_max_u32 v48, v48, v46\nMvaa 168,148,236\nRa 212,11,24576\nMvaa 168,144,232\nv_mov_b32 v50, v48\nMvaa 108,136,232\nRa 216,10,26624\nMvaa 108,140,236\n"
"D0 7,24,44,1024\nMvaa 104,132,236\nRa 220,11,26624\nMvaa 104,128,232\nv_max_u32_dpp v50, v50, v50 row_ror:8 row_mask:0xf bank_mask:0xf\nMvaa 112,128,240\nMvaa 112,132,244\n"
"v_max_u32_dpp v50, v50, v50 row_ror:4 row_mask:0xf bank_mask:0xf\nMvaa 116,140,244\nMvaa 116,136,240\nv_max_u32_dpp v50, v50, v50 row_ror:2 row_mask:0xf bank_mask:0xf\n"
"Mvaa 176,144,240\nMvaa 176,148,244\nD0 8,24,44,2048\nMvaa 180,156,244\nv_max_u32_dpp v50, v50, v50 row_ror:1 row_mask:0xf bank_mask:0xf\nMvaa 180,152,240\nv_mov_b32 v52, v50\n"
"Mvaa 188,152,248\nMvaa 188,156,252\nv_permlane16_swap_b32 v50, v52\nMvaa 184,148,252\nv_max_u32 v50, v50, v52\nMvaa 184,144,248\nv_mov_b32 v52, v50\nMvaa 124,136,248\n"
"Mvaa 124,140,252\nD0 9,24,44,3072\nMvaa 120,132,252\nv_permlane32_swap_b32 v50, v52\nMvaa 120,128,248\nWL 0\nMaaa 32,192,224\nRa 128,10,32768\nv_max_u32 v50, v50, v52\n"
"Maaa 32,196,228\ns_add_u32 m0, s47, 16384\ns_add_u32 s44, s44, 0x40000\nMaaa 36,204,228\nRa 132,11,32768\nv_cmp_eq_u32 vcc, v48, v50\ns_lshl_b32 s2, s11, 3\n"
"s_add_u32 s2, s2, 131072\nv_cndmask_b32 v51, v48, v49, vcc\nMaaa 36,200,224\nMaaa 96,208,224\nRa 136,10,34816\nMaaa 96,212,228\nD0 6,24,44,0\nMaaa 100,220,228\nRa 140,11,34816\n"
"Maaa 100,216,224\nv_max_u32_dpp v51, v51, v51 row_ror:8 row_mask:0xf bank_mask:0xf\nMaaa 108,216,232\nRa 144,10,40960\nMaaa 108,220,236\n"
"v_max_u32_dpp v51, v51, v51 row_ror:4 row_mask:0xf bank_mask:0xf\nMaaa 104,212,236\nRa 148,11,40960\nMaaa 104,208,232\n"
"v_max_u32_dpp v51, v51, v51 row_ror:2 row_mask:0xf bank_mask:0xf\nMaaa 44,200,232\nRa 152,10,43008\nMaaa 44,204,236\nD0 7,24,44,1024\nMaaa 40,196,236\nRa 156,11,43008\n"
"Maaa 40,192,232\nv_max_u32_dpp v51, v51, v51 row_ror:1 row_mask:0xf bank_mask:0xf\nMaaa 48,192,240\nv_mov_b32 v52, v51\nMaaa 48,196,244\nMaaa 52,204,244\n"
"v_permlane16_swap_b32 v51, v52\nMaaa 52,200,240\nv_max_u32 v51, v51, v52\nMaaa 112,208,240\nv_mov_b32 v52, v51\nMaaa 112,212,244\nD0 8,24,44,2048\nMaaa 116,220,244\n"
"v_permlane32_swap_b32 v51, v52\nMaaa 116,216,240\nv_max_u32 v51, v51, v52\nMaaa 124,216,248\nv_mov_b32 v52, s2\nMaaa 124,220,252\nds_write_b64 v52, v[50:51]\nMaaa 120,212,252\n"
"v_mov_b32 v52, 131072\nMaaa 120,208,248\nMaaa 60,200,248\nMaaa 60,204,252\nD0 9,24,44,3072\nMaaa 56,196,252\nMaaa 56,192,248\ns_waitcnt vmcnt(18) lgkmcnt(0)\ns_barrier\n"
"Maaa 0,192,160\nTa 224,16,32768\nMaaa 0,196,164\nTa 226,16,34816\nbuffer_load_dwordx4 v[200:203], v21, s[36:39], s87 offen offset:0\nMaaa 4,204,164\ns_add_u32 m0, s47, -49152\n"
"s_add_u32 s48, s48, 128\nMaaa 4,200,160\nTa 228,16,49152\nMaaa 64,208,160\nTa 230,16,51200\nMaaa 64,212,164\nD0 2,20,48,0\nMaaa 68,220,164\nTa 232,17,32768\nMaaa 68,216,160\n"
"Ta 234,17,34816\nMaaa 76,216,168\nRv 32,52,0\nMaaa 76,220,172\nTa 236,17,49152\nMaaa 72,212,172\nTa 238,17,51200\nMaaa 72,208,168\nRv 36,52,16\nMaaa 12,200,168\nTa 240,18,32768\n"
"Maaa 12,204,172\nTa 242,18,34816\nMaaa 8,196,172\nD0 3,20,48,1024\nMaaa 8,192,168\nTa 244,18,49152\nMaaa 16,192,176\nTa 246,18,51200\nMaaa 16,196,180\nMaaa 20,204,180\n"
"Ta 248,19,32768\nMaaa 20,200,176\nTa 250,19,34816\nMaaa 80,208,176\nMaaa 80,212,180\nTa 252,19,49152\nMaaa 84,220,180\nTa 254,19,51200\nMaaa 84,216,176\nD0 4,20,48,2048\n"
"Maaa 92,216,184\nWL 11\nMaaa 92,220,188\nv_max_u32 v48, v32, v33\nMaaa 88,212,188\nv_min_u32 v49, v32, v33\nMaaa 88,208,184\nv_med3_u32 v49, v48, v49, v34\nMaaa 28,200,184\n"
"v_max_u32 v48, v48, v34\nMaaa 28,204,188\nD0 5,20,48,3072\nMaaa 24,196,188\nv_med3_u32 v49, v48, v49, v35\nMaaa 24,192,184\nWL 0\nMvaa 96,128,224\nTa 160,12,32768\n"
"Mvaa 96,132,228\nTa 162,12,34816\nMvaa 100,140,228\ns_add_u32 m0, s47, -32768\ns_add_u32 s45, s45, 128\nMvaa 100,136,224\nTa 164,12,49152\nMvaa 160,144,224\nTa 166,12,51200\n"
"Mvaa 160,148,228\nD0 2,20,45,0\nMvaa 164,156,228\nTa 168,13,32768\nMvaa 164,152,224\nTa 170,13,34816\nMvaa 172,152,232\nv_max_u32 v48, v48, v35\nMvaa 172,156,236\nTa 172,13,49152\n"
"Mvaa 168,148,236\nTa 174,13,51200\nMvaa 168,144,232\nv_med3_u32 v49, v48, v49, v36\nMvaa 108,136,232\nTa 176,14,32768\nMvaa 108,140,236\nTa 178,14,34816\nMvaa 104,132,236\n"
"D0 3,20,45,1024\nMvaa 104,128,232\nTa 180,14,49152\nMvaa 112,128,240\nTa 182,14,51200\nMvaa 112,132,244\nv_max_u32 v48, v48, v36\nMvaa 116,140,244\nTa 184,15,32768\n"
"Mvaa 116,136,240\nTa 186,15,34816\nMvaa 176,144,240\nv_med3_u32 v49, v48, v49, v37\nMvaa 176,148,244\nTa 188,15,49152\nMvaa 180,156,244\nTa 190,15,51200\nMvaa 180,152,240\n"
"D0 4,20,45,2048\nMvaa 188,152,248\nv_max_u32 v48, v48, v37\nMvaa 188,156,252\nv_med3_u32 v49, v48, v49, v38\nMvaa 184,148,252\nv_max_u32 v48, v48, v38\nMvaa 184,144,248\n"
"v_med3_u32 v49, v48, v49, v39\nMvaa 124,136,248\nv_max_u32 v48, v48, v39\nMvaa 124,140,252\nD0 5,20,45,3072\nMvaa 120,132,252\nv_readfirstlane_b32 s2, v48\nMvaa 120,128,248\n"
"s_waitcnt vmcnt(18) lgkmcnt(0)\ns_barrier\nMvaa 64,128,160\nRa 192,10,49152\nMvaa 64,132,164\ns_add_u32 m0, s47, 32768\ns_add_u32 s44, s44, 0x40000\n"
"buffer_load_dwordx4 v[216:219], v21, s[40:43], s87 offen offset:0\nMvaa 68,140,164\nRa 196,11,49152\nMvaa 68,136,160\nv_readfirstlane_b32 s3, v49\nMvaa 128,144,160\n"
"Ra 200,10,51200\nMvaa 128,148,164\nD0 6,24,44,0\nMvaa 132,156,164\nRa 204,11,51200\nMvaa 132,152,160\ns_andn2_b32 s2, 0x7ff, s2\nMvaa 140,152,168\nRa 208,10,57344\n"
"Mvaa 140,156,172\ns_andn2_b32 s3, 0x7ff, s3\nMvaa 136,148,172\nRa 212,11,57344\nMvaa 136,144,168\ns_lshl_b32 s71, s3, 16\nMvaa 76,136,168\nRa 216,10,59392\nMvaa 76,140,172\n"
"D0 7,24,44,1024\nMvaa 72,132,172\nRa 220,11,59392\nMvaa 72,128,168\ns_or_b32 s71, s71, s2\nMvaa 80,128,176\ns_mul_i32 s2, s2, 896\nMvaa 80,132,180\ns_mul_i32 s3, s3, 896\n"
"Mvaa 84,140,180\ns_mul_i32 s6, s11, 224\nMvaa 84,136,176\ns_add_u32 s2, s2, s6\nMvaa 144,144,176\ns_add_u32 s3, s3, s6\nMvaa 144,148,180\nD0 8,24,44,2048\nMvaa 148,156,180\n"
"buffer_load_dwordx2 v[240:241], v255, s[28:31], s2 offen\nMvaa 148,152,176\nMvaa 156,152,184\nMvaa 156,156,188\nMvaa 152,148,188\n"
"buffer_load_dwordx2 v[242:243], v255, s[28:31], s3 offen\ns_lshl2_add_u32 s2, s10, s11\ns_cmp_eq_u32 s2, 512\ns_cselect_b32 s2, 0, 0x40000000\ns_add_u32 s2, s2, 0xc048000\n"
"Mvaa 152,144,184\nMvaa 92,136,184\nMvaa 92,140,188\nD0 9,24,44,3072\nMvaa 88,132,188\nbuffer_store_dword v25, off, s[16:19], s2 offset:0 sc0 sc1\nMvaa 88,128,184\nWL 0\n"
"Maaa 0,192,160\nRa 128,10,0\nMaaa 0,196,164\ns_add_u32 m0, s47, 49152\ns_add_u32 s44, s44, 0x40000\nMaaa 4,204,164\nRa 132,11,0\nMaaa 4,200,160\nMaaa 64,208,160\nRa 136,10,2048\n"
"Maaa 64,212,164\nD0 6,24,44,0\nMaaa 68,220,164\nRa 140,11,2048\nMaaa 68,216,160\nMaaa 76,216,168\nRa 144,10,8192\nMaaa 76,220,172\nMaaa 72,212,172\nRa 148,11,8192\n"
"Maaa 72,208,168\nMaaa 12,200,168\nRa 152,10,10240\nMaaa 12,204,172\nD0 7,24,44,1024\nMaaa 8,196,172\nRa 156,11,10240\nMaaa 8,192,168\nMaaa 16,192,176\nMaaa 16,196,180\n"
"Maaa 20,204,180\nMaaa 20,200,176\nMaaa 80,208,176\nMaaa 80,212,180\nD0 8,24,44,2048\nMaaa 84,220,180\nMaaa 84,216,176\nMaaa 92,216,184\nMaaa 92,220,188\nMaaa 88,212,188\n"
"Maaa 88,208,184\nMaaa 28,200,184\nMaaa 28,204,188\nD0 9,24,44,3072\nMaaa 24,196,188\nMaaa 24,192,184\ns_waitcnt vmcnt(21) lgkmcnt(0)\ns_barrier\nMaaa 32,192,224\nTa 160,12,0\n"
"Maaa 32,196,228\nTa 162,12,2048\nbuffer_load_dwordx4 v[204:207], v21, s[36:39], s87 offen offset:3584\nMaaa 36,204,228\ns_add_u32 m0, s47, -16384\ns_add_u32 s48, s48, 128\n"
"s_lshl_b32 s5, s73, 8\ns_add_u32 s5, s5, 0xc04ac00\nv_lshrrev_b32 v27, 2, v1\nbuffer_load_dword v27, v27, s[16:19], s5 offen sc0 sc1\nMaaa 36,200,224\nTa 164,12,16384\n"
"Maaa 96,208,224\nTa 166,12,18432\nMaaa 96,212,228\nD0 2,20,48,0\nMaaa 100,220,228\nTa 168,13,0\nMaaa 100,216,224\nTa 170,13,2048\nMaaa 108,216,232\nMaaa 108,220,236\n"
"Ta 172,13,16384\nMaaa 104,212,236\nTa 174,13,18432\nMaaa 104,208,232\nMaaa 44,200,232\nTa 176,14,0\nMaaa 44,204,236\nTa 178,14,2048\nMaaa 40,196,236\nD0 3,20,48,1024\n"
"Maaa 40,192,232\nTa 180,14,16384\nMaaa 48,192,240\nTa 182,14,18432\nMaaa 48,196,244\nMaaa 52,204,244\nTa 184,15,0\nMaaa 52,200,240\nTa 186,15,2048\nMaaa 112,208,240\n"
"Maaa 112,212,244\nTa 188,15,16384\nMaaa 116,220,244\nTa 190,15,18432\nMaaa 116,216,240\nD0 4,20,48,2048\nMaaa 124,216,248\nMaaa 124,220,252\nMaaa 120,212,252\nMaaa 120,208,248\n"
"Maaa 60,200,248\nMaaa 60,204,252\nD0 5,20,48,3072\nMaaa 56,196,252\nMaaa 56,192,248\nWL 0\nMvaa 64,128,160\nTa 224,16,0\nMvaa 64,132,164\nTa 226,16,2048\nMvaa 68,140,164\n"
"s_add_u32 m0, s47, -65536\ns_add_u32 s45, s45, 128\nMvaa 68,136,160\nTa 228,16,16384\nMvaa 128,144,160\nTa 230,16,18432\nMvaa 128,148,164\nD0 2,20,45,0\nMvaa 132,156,164\n"
"Ta 232,17,0\nMvaa 132,152,160\nTa 234,17,2048\nMvaa 140,152,168\nMvaa 140,156,172\nTa 236,17,16384\nMvaa 136,148,172\nTa 238,17,18432\nMvaa 136,144,168\nMvaa 76,136,168\n"
"Ta 240,18,0\nMvaa 76,140,172\nTa 242,18,2048\nMvaa 72,132,172\nD0 3,20,45,1024\nMvaa 72,128,168\nTa 244,18,16384\nMvaa 80,128,176\nTa 246,18,18432\nMvaa 80,132,180\n"
"Mvaa 84,140,180\nTa 248,19,0\nMvaa 84,136,176\nTa 250,19,2048\nMvaa 144,144,176\nMvaa 144,148,180\nTa 252,19,16384\nMvaa 148,156,180\nTa 254,19,18432\nMvaa 148,152,176\n"
"D0 4,20,45,2048\nMvaa 156,152,184\nMvaa 156,156,188\nMvaa 152,148,188\nMvaa 152,144,184\nMvaa 92,136,184\nMvaa 92,140,188\nD0 5,20,45,3072\nMvaa 88,132,188\nMvaa 88,128,184\n"
"s_waitcnt vmcnt(22) lgkmcnt(0)\ns_barrier\nMvaa 96,128,224\nRa 192,10,16384\nWV 16\nMvaa 96,132,228\ns_add_u32 m0, s47, 0\ns_add_u32 s44, s44, 0x40000\n"
"buffer_load_dwordx4 v[220:223], v21, s[40:43], s87 offen offset:3584\nMvaa 100,140,228\nRa 196,11,16384\nv_lshlrev_b32 v244, 16, v240\nMvaa 100,136,224\n"
"v_and_b32 v245, 0xffff0000, v240\nv_lshlrev_b32 v246, 16, v241\nMvaa 160,144,224\nRa 200,10,18432\nv_and_b32 v247, 0xffff0000, v241\nMvaa 160,148,228\nD0 6,24,44,0\n"
"v_lshlrev_b32 v248, 16, v242\nMvaa 164,156,228\nRa 204,11,18432\nMvaa 164,152,224\nv_and_b32 v249, 0xffff0000, v242\nv_lshlrev_b32 v250, 16, v243\nMvaa 172,152,232\n"
"Ra 208,10,24576\nv_and_b32 v251, 0xffff0000, v243\nMvaa 172,156,236\nv_lshlrev_b32 v252, 16, v192\nv_and_b32 v253, 0xffff0000, v192\nMvaa 168,148,236\nRa 212,11,24576\n"
"MU 32,244,252\nMvaa 168,144,232\nMU 33,244,253\nMvaa 108,136,232\nRa 216,10,26624\nMU 40,248,252\nMvaa 108,140,236\nD0 7,24,44,1024\nMU 41,248,253\nMvaa 104,132,236\n"
"Ra 220,11,26624\nv_lshlrev_b32 v252, 16, v193\nMvaa 104,128,232\nv_and_b32 v253, 0xffff0000, v193\nMU 34,244,252\nMvaa 112,128,240\nMU 35,244,253\nMU 42,248,252\nMvaa 112,132,244\n"
"MU 43,248,253\nMvaa 116,140,244\nv_lshlrev_b32 v252, 16, v194\nv_and_b32 v253, 0xffff0000, v194\nMvaa 116,136,240\nMU 36,244,252\nMU 37,244,253\nMvaa 176,144,240\nMU 44,248,252\n"
"MU 45,248,253\nMvaa 176,148,244\nD0 8,24,44,2048\nv_lshlrev_b32 v252, 16, v195\nMvaa 180,156,244\nv_and_b32 v253, 0xffff0000, v195\nMvaa 180,152,240\nMU 38,244,252\nMU 39,244,253\n"
"Mvaa 188,152,248\nMU 46,248,252\nMU 47,248,253\nMvaa 188,156,252\nv_lshlrev_b32 v252, 16, v208\nv_and_b32 v253, 0xffff0000, v208\nMvaa 184,148,252\nMU 48,244,252\nMU 49,244,253\n"
"Mvaa 184,144,248\nMU 56,248,252\nMvaa 124,136,248\nMU 57,248,253\nv_lshlrev_b32 v252, 16, v209\nMvaa 124,140,252\nD0 9,24,44,3072\nv_and_b32 v253, 0xffff0000, v209\n"
"Mvaa 120,132,252\nMU 50,244,252\nMU 51,244,253\nMvaa 120,128,248\nWL 0\nMaaa 32,192,224\nRa 128,10,32768\nMU 58,248,252\nMaaa 32,196,228\ns_add_u32 m0, s47, 16384\n"
"s_add_u32 s44, s44, 0x40000\nMaaa 36,204,228\nRa 132,11,32768\nMU 59,248,253\nMaaa 36,200,224\nv_lshlrev_b32 v252, 16, v210\nv_and_b32 v253, 0xffff0000, v210\nMaaa 96,208,224\n"
"Ra 136,10,34816\nMU 52,244,252\nMaaa 96,212,228\nD0 6,24,44,0\nMU 53,244,253\nMaaa 100,220,228\nRa 140,11,34816\nMaaa 100,216,224\nMU 60,248,252\nMU 61,248,253\nMaaa 108,216,232\n"
"Ra 144,10,40960\nv_lshlrev_b32 v252, 16, v211\nMaaa 108,220,236\nv_and_b32 v253, 0xffff0000, v211\nMU 54,244,252\nMaaa 104,212,236\nRa 148,11,40960\nMU 55,244,253\n"
"Maaa 104,208,232\nMU 62,248,252\nMaaa 44,200,232\nRa 152,10,43008\nMU 63,248,253\nMaaa 44,204,236\nD0 7,24,44,1024\nv_lshlrev_b32 v252, 16, v196\nMaaa 40,196,236\nRa 156,11,43008\n"
"v_and_b32 v253, 0xffff0000, v196\nMaaa 40,192,232\nFM 32,245,252\nFM 33,245,253\nMaaa 48,192,240\nFM 40,249,252\nFM 41,249,253\nMaaa 48,196,244\nv_lshlrev_b32 v252, 16, v197\n"
"Maaa 52,204,244\nv_and_b32 v253, 0xffff0000, v197\nFM 34,245,252\nMaaa 52,200,240\nFM 35,245,253\nFM 42,249,252\nMaaa 112,208,240\nFM 43,249,253\nv_lshlrev_b32 v252, 16, v198\n"
"Maaa 112,212,244\nD0 8,24,44,2048\nv_and_b32 v253, 0xffff0000, v198\nMaaa 116,220,244\nFM 36,245,252\nMaaa 116,216,240\nFM 37,245,253\nFM 44,249,252\nMaaa 124,216,248\n"
"FM 45,249,253\nv_lshlrev_b32 v252, 16, v199\nMaaa 124,220,252\nv_and_b32 v253, 0xffff0000, v199\nFM 38,245,252\nMaaa 120,212,252\nFM 39,245,253\nFM 46,249,252\nMaaa 120,208,248\n"
"FM 47,249,253\nMaaa 60,200,248\nv_lshlrev_b32 v252, 16, v212\nv_and_b32 v253, 0xffff0000, v212\nMaaa 60,204,252\nD0 9,24,44,3072\nFM 48,245,252\nMaaa 56,196,252\nFM 49,245,253\n"
"FM 56,249,252\nMaaa 56,192,248\ns_waitcnt vmcnt(19) lgkmcnt(0)\ns_barrier\nMaaa 0,192,160\nTa 224,16,32768\nFM 57,249,253\nMaaa 0,196,164\nTa 226,16,34816\n"
"buffer_load_dwordx4 v[224:227], v20, s[92:95], s88 offen offset:0\nMaaa 4,204,164\ns_add_u32 m0, s47, -49152\ns_add_u32 s48, s48, 128\nMaaa 4,200,160\nTa 228,16,49152\n"
"v_lshlrev_b32 v252, 16, v213\nMaaa 64,208,160\nTa 230,16,51200\nv_and_b32 v253, 0xffff0000, v213\nMaaa 64,212,164\nD0 2,20,48,0\nFM 50,245,252\nMaaa 68,220,164\nTa 232,17,32768\n"
"Maaa 68,216,160\nTa 234,17,34816\nFM 51,245,253\nMaaa 76,216,168\nFM 58,249,252\nFM 59,249,253\nMaaa 76,220,172\nTa 236,17,49152\nv_lshlrev_b32 v252, 16, v214\nMaaa 72,212,172\n"
"Ta 238,17,51200\nv_and_b32 v253, 0xffff0000, v214\nMaaa 72,208,168\nFM 52,245,252\nMaaa 12,200,168\nTa 240,18,32768\nFM 53,245,253\nMaaa 12,204,172\nTa 242,18,34816\n"
"FM 60,249,252\nMaaa 8,196,172\nD0 3,20,48,1024\nFM 61,249,253\nMaaa 8,192,168\nTa 244,18,49152\nv_lshlrev_b32 v252, 16, v215\nMaaa 16,192,176\nTa 246,18,51200\n"
"v_and_b32 v253, 0xffff0000, v215\nMaaa 16,196,180\nFM 54,245,252\nMaaa 20,204,180\nTa 248,19,32768\nFM 55,245,253\nMaaa 20,200,176\nTa 250,19,34816\nFM 62,249,252\n"
"Maaa 80,208,176\nFM 63,249,253\nv_lshlrev_b32 v252, 16, v200\nMaaa 80,212,180\nTa 252,19,49152\nv_and_b32 v253, 0xffff0000, v200\nMaaa 84,220,180\nTa 254,19,51200\n"
"Maaa 84,216,176\nD0 4,20,48,2048\nFM 32,246,252\nMaaa 92,216,184\nFM 33,246,253\nFM 40,250,252\nMaaa 92,220,188\nFM 41,250,253\nv_lshlrev_b32 v252, 16, v201\nMaaa 88,212,188\n"
"v_and_b32 v253, 0xffff0000, v201\nFM 34,246,252\nMaaa 88,208,184\nFM 35,246,253\nMaaa 28,200,184\nFM 42,250,252\nFM 43,250,253\nMaaa 28,204,188\nD0 5,20,48,3072\n"
"v_lshlrev_b32 v252, 16, v202\nMaaa 24,196,188\nv_and_b32 v253, 0xffff0000, v202\nFM 36,246,252\nMaaa 24,192,184\nWL 0\nMvaa 96,128,224\nTa 160,12,32768\nFM 37,246,253\n"
"Mvaa 96,132,228\nTa 162,12,34816\nbuffer_load_dwordx4 v[228:231], v20, s[92:95], s88 offen offset:896\nMvaa 100,140,228\ns_add_u32 m0, s47, -32768\ns_add_u32 s45, s45, 128\n"
"Mvaa 100,136,224\nTa 164,12,49152\nFM 44,250,252\nMvaa 160,144,224\nTa 166,12,51200\nFM 45,250,253\nMvaa 160,148,228\nD0 2,20,45,0\nv_lshlrev_b32 v252, 16, v203\nMvaa 164,156,228\n"
"Ta 168,13,32768\nMvaa 164,152,224\nTa 170,13,34816\nv_and_b32 v253, 0xffff0000, v203\nMvaa 172,152,232\nFM 38,246,252\nFM 39,246,253\nMvaa 172,156,236\nTa 172,13,49152\n"
"FM 46,250,252\nMvaa 168,148,236\nTa 174,13,51200\nFM 47,250,253\nMvaa 168,144,232\nv_lshlrev_b32 v252, 16, v216\nMvaa 108,136,232\nTa 176,14,32768\n"
"v_and_b32 v253, 0xffff0000, v216\nMvaa 108,140,236\nTa 178,14,34816\nFM 48,246,252\nMvaa 104,132,236\nD0 3,20,45,1024\nFM 49,246,253\nMvaa 104,128,232\nTa 180,14,49152\n"
"FM 56,250,252\nMvaa 112,128,240\nTa 182,14,51200\nFM 57,250,253\nMvaa 112,132,244\nv_lshlrev_b32 v252, 16, v217\nMvaa 116,140,244\nTa 184,15,32768\n"
"v_and_b32 v253, 0xffff0000, v217\nMvaa 116,136,240\nTa 186,15,34816\nFM 50,246,252\nMvaa 176,144,240\nFM 51,246,253\nFM 58,250,252\nMvaa 176,148,244\nTa 188,15,49152\n"
"FM 59,250,253\nMvaa 180,156,244\nTa 190,15,51200\nMvaa 180,152,240\nD0 4,20,45,2048\nv_lshlrev_b32 v252, 16, v218\nMvaa 188,152,248\nv_and_b32 v253, 0xffff0000, v218\n"
"FM 52,246,252\nMvaa 188,156,252\nFM 53,246,253\nFM 60,250,252\nMvaa 184,148,252\nFM 61,250,253\nv_lshlrev_b32 v252, 16, v219\nMvaa 184,144,248\nv_and_b32 v253, 0xffff0000, v219\n"
"Mvaa 124,136,248\nFM 54,246,252\nWV 26\nv_xor_b32 v27, s15, v27\nv_and_b32 v255, 0xffffdfff, v27\nv_cmp_ne_u32 vcc, 0, v255\ns_nop 0\ns_cbranch_vccnz .S146\n.S147:\n"
"v_cmp_ne_u32 vcc, 0, v27\ns_nop 3\ns_bcnt1_i32_b64 s4, vcc\ns_cmp_gt_u32 s4, 32\ns_cselect_b32 s4, 1, 0\nMvaa 124,140,252\nD0 5,20,45,3072\ns_xor_b32 s4, s4, s12\n"
"Mvaa 120,132,252\ns_and_b32 s4, s4, 1\ns_lshl_b32 s4, s4, 13\nMvaa 120,128,248\ns_waitcnt vmcnt(18) lgkmcnt(0)\ns_barrier\nMvaa 64,128,160\nRa 192,10,49152\n"
"s_mul_i32 s72, s73, 25165824\nMvaa 64,132,164\ns_add_u32 m0, s47, 32768\ns_add_u32 s44, s44, 0x40000\nbuffer_load_dwordx4 v[232:235], v20, s[92:95], s88 offen offset:1792\n"
"Mvaa 68,140,164\nRa 196,11,49152\ns_add_u32 s72, s72, s4\nMvaa 68,136,160\ns_and_b32 s4, s12, 1\ns_lshl_b32 s4, s4, 21\nMvaa 128,144,160\nRa 200,10,51200\ns_add_u32 s72, s72, s4\n"
"Mvaa 128,148,164\nD0 6,24,44,0\ns_lshl3_add_u32 s2, s13, s11\nMvaa 132,156,164\nRa 204,11,51200\nMvaa 132,152,160\ns_lshl_b32 s2, s2, 11\ns_lshl_b32 s3, s14, 22\nMvaa 140,152,168\n"
"Ra 208,10,57344\ns_add_u32 s2, s2, s3\nMvaa 140,156,172\ns_add_u32 s2, s2, s72\ns_add_u32 s49, s2, 0xc00000\nMvaa 136,148,172\nRa 212,11,57344\nFM 55,246,253\nMvaa 136,144,168\n"
"FM 62,250,252\nMvaa 76,136,168\nRa 216,10,59392\nFM 63,250,253\nMvaa 76,140,172\nD0 7,24,44,1024\nv_lshlrev_b32 v252, 16, v204\nMvaa 72,132,172\nRa 220,11,59392\n"
"v_and_b32 v253, 0xffff0000, v204\nMvaa 72,128,168\nFM 32,247,252\nFM 33,247,253\nMvaa 80,128,176\nFM 40,251,252\nFM 41,251,253\nMvaa 80,132,180\nv_lshlrev_b32 v252, 16, v205\n"
"Mvaa 84,140,180\nv_and_b32 v253, 0xffff0000, v205\nFM 34,247,252\nMvaa 84,136,176\nFM 35,247,253\nFM 42,251,252\nMvaa 144,144,176\nFM 43,251,253\nv_lshlrev_b32 v252, 16, v206\n"
"Mvaa 144,148,180\nD0 8,24,44,2048\nv_and_b32 v253, 0xffff0000, v206\nMvaa 148,156,180\nFM 36,247,252\nMvaa 148,152,176\nFM 37,247,253\nFM 44,251,252\nMvaa 156,152,184\n"
"FM 45,251,253\nv_lshlrev_b32 v252, 16, v207\nMvaa 156,156,188\nv_and_b32 v253, 0xffff0000, v207\nFM 38,247,252\nMvaa 152,148,188\nFM 39,247,253\nFM 46,251,252\nMvaa 152,144,184\n"
"FM 47,251,253\nMvaa 92,136,184\nv_lshlrev_b32 v252, 16, v220\nv_and_b32 v253, 0xffff0000, v220\nMvaa 92,140,188\nD0 9,24,44,3072\nFM 48,247,252\nMvaa 88,132,188\nFM 49,247,253\n"
"FM 56,251,252\nMvaa 88,128,184\nWL 0\nMaaa 0,192,160\nRa 128,10,0\nFM 57,251,253\nMaaa 0,196,164\ns_add_u32 m0, s47, 49152\ns_add_u32 s44, s44, 0x40000\n"
"buffer_load_dwordx4 v[236:239], v20, s[92:95], s88 offen offset:2688\nMaaa 4,204,164\nRa 132,11,0\nv_lshlrev_b32 v252, 16, v221\nMaaa 4,200,160\nv_and_b32 v253, 0xffff0000, v221\n"
"FM 50,247,252\nMaaa 64,208,160\nRa 136,10,2048\nFM 51,247,253\nMaaa 64,212,164\nD0 6,24,44,0\nFM 58,251,252\nMaaa 68,220,164\nRa 140,11,2048\nMaaa 68,216,160\nFM 59,251,253\n"
"v_lshlrev_b32 v252, 16, v222\nMaaa 76,216,168\nRa 144,10,8192\nv_and_b32 v253, 0xffff0000, v222\nMaaa 76,220,172\nFM 52,247,252\nFM 53,247,253\nMaaa 72,212,172\nRa 148,11,8192\n"
"FM 60,251,252\nMaaa 72,208,168\nFM 61,251,253\nMaaa 12,200,168\nRa 152,10,10240\nv_lshlrev_b32 v252, 16, v223\nMaaa 12,204,172\nD0 7,24,44,1024\nv_and_b32 v253, 0xffff0000, v223\n"
"Maaa 8,196,172\nRa 156,11,10240\nFM 54,247,252\nMaaa 8,192,168\nFM 55,247,253\nFM 62,251,252\nMaaa 16,192,176\nFM 63,251,253\nMaaa 16,196,180\nv_permlane32_swap_b32 v32, v48\n"
"Maaa 20,204,180\nv_permlane32_swap_b32 v33, v49\nv_permlane32_swap_b32 v34, v50\nMaaa 20,200,176\nv_permlane32_swap_b32 v35, v51\nv_permlane32_swap_b32 v36, v52\nMaaa 80,208,176\n"
"v_permlane32_swap_b32 v37, v53\nv_permlane32_swap_b32 v38, v54\nMaaa 80,212,180\nD0 8,24,44,2048\nv_permlane32_swap_b32 v39, v55\nMaaa 84,220,180\nv_permlane32_swap_b32 v40, v56\n"
"Maaa 84,216,176\nv_permlane32_swap_b32 v41, v57\nv_permlane32_swap_b32 v42, v58\nMaaa 92,216,184\nv_permlane32_swap_b32 v43, v59\nv_permlane32_swap_b32 v44, v60\nMaaa 92,220,188\n"
"v_permlane32_swap_b32 v45, v61\nv_permlane32_swap_b32 v46, v62\nMaaa 88,212,188\nv_permlane32_swap_b32 v47, v63\nAD 32,32,48\nMaaa 88,208,184\nAD 33,33,49\nMaaa 28,200,184\n"
"AD 34,34,50\nAD 35,35,51\nMaaa 28,204,188\nD0 9,24,44,3072\nAD 36,36,52\nMaaa 24,196,188\nAD 37,37,53\nAD 38,38,54\nMaaa 24,192,184\ns_waitcnt vmcnt(20) lgkmcnt(0)\ns_barrier\n"
"Maaa 32,192,224\nTa 160,12,0\nAD 39,39,55\nMaaa 32,196,228\nTa 162,12,2048\nMaaa 36,204,228\ns_add_u32 m0, s47, -16384\ns_add_u32 s48, s48, 128\nMaaa 36,200,224\nTa 164,12,16384\n"
"AD 40,40,56\nMaaa 96,208,224\nTa 166,12,18432\nAD 41,41,57\nMaaa 96,212,228\nD0 2,20,48,0\nAD 42,42,58\nMaaa 100,220,228\nTa 168,13,0\nMaaa 100,216,224\nTa 170,13,2048\n"
"AD 43,43,59\nMaaa 108,216,232\nAD 44,44,60\nAD 45,45,61\nMaaa 108,220,236\nTa 172,13,16384\nAD 46,46,62\nMaaa 104,212,236\nTa 174,13,18432\nAD 47,47,63\nMaaa 104,208,232\n"
"v_permlane16_swap_b32 v32, v40\nMaaa 44,200,232\nTa 176,14,0\nv_permlane16_swap_b32 v33, v41\nMaaa 44,204,236\nTa 178,14,2048\nv_permlane16_swap_b32 v34, v42\nMaaa 40,196,236\n"
"D0 3,20,48,1024\nv_permlane16_swap_b32 v35, v43\nMaaa 40,192,232\nTa 180,14,16384\nv_permlane16_swap_b32 v36, v44\nMaaa 48,192,240\nTa 182,14,18432\n"
"v_permlane16_swap_b32 v37, v45\nMaaa 48,196,244\nv_permlane16_swap_b32 v38, v46\nMaaa 52,204,244\nTa 184,15,0\nv_permlane16_swap_b32 v39, v47\nMaaa 52,200,240\nTa 186,15,2048\n"
"AD 32,32,40\nMaaa 112,208,240\nAD 33,33,41\nAD 34,34,42\nMaaa 112,212,244\nTa 188,15,16384\nAD 35,35,43\nMaaa 116,220,244\nTa 190,15,18432\nMaaa 116,216,240\nD0 4,20,48,2048\n"
"AD 36,36,44\nMaaa 124,216,248\nAD 37,37,45\nAD 38,38,46\nMaaa 124,220,252\nAD 39,39,47\nv_add_f32_dpp v32, v32, v32 row_ror:8 row_mask:0xf bank_mask:0xf\nMaaa 120,212,252\n"
"v_add_f32_dpp v33, v33, v33 row_ror:8 row_mask:0xf bank_mask:0xf\nv_add_f32_dpp v34, v34, v34 row_ror:8 row_mask:0xf bank_mask:0xf\nMaaa 120,208,248\n"
"v_add_f32_dpp v35, v35, v35 row_ror:8 row_mask:0xf bank_mask:0xf\nMaaa 60,200,248\nv_add_f32_dpp v36, v36, v36 row_ror:8 row_mask:0xf bank_mask:0xf\n"
"v_add_f32_dpp v37, v37, v37 row_ror:8 row_mask:0xf bank_mask:0xf\nMaaa 60,204,252\nD0 5,20,48,3072\nv_add_f32_dpp v38, v38, v38 row_ror:8 row_mask:0xf bank_mask:0xf\n"
"Maaa 56,196,252\nv_add_f32_dpp v39, v39, v39 row_ror:8 row_mask:0xf bank_mask:0xf\nv_add_f32_dpp v32, v32, v32 row_ror:4 row_mask:0xf bank_mask:0xf\nMaaa 56,192,248\nWL 0\n"
"Mvaa 64,128,160\nTa 224,16,0\nv_add_f32_dpp v33, v33, v33 row_ror:4 row_mask:0xf bank_mask:0xf\nMvaa 64,132,164\nTa 226,16,2048\nMvaa 68,140,164\ns_add_u32 m0, s47, -65536\n"
"s_add_u32 s45, s45, 128\nMvaa 68,136,160\nTa 228,16,16384\nv_add_f32_dpp v34, v34, v34 row_ror:4 row_mask:0xf bank_mask:0xf\nMvaa 128,144,160\nTa 230,16,18432\n"
"v_add_f32_dpp v35, v35, v35 row_ror:4 row_mask:0xf bank_mask:0xf\nMvaa 128,148,164\nD0 2,20,45,0\nv_add_f32_dpp v36, v36, v36 row_ror:4 row_mask:0xf bank_mask:0xf\n"
"Mvaa 132,156,164\nTa 232,17,0\nMvaa 132,152,160\nTa 234,17,2048\nv_add_f32_dpp v37, v37, v37 row_ror:4 row_mask:0xf bank_mask:0xf\nMvaa 140,152,168\n"
"v_add_f32_dpp v38, v38, v38 row_ror:4 row_mask:0xf bank_mask:0xf\nv_add_f32_dpp v39, v39, v39 row_ror:4 row_mask:0xf bank_mask:0xf\nMvaa 140,156,172\nTa 236,17,16384\n"
"v_add_f32_dpp v32, v32, v32 row_ror:2 row_mask:0xf bank_mask:0xf\nMvaa 136,148,172\nTa 238,17,18432\nv_add_f32_dpp v33, v33, v33 row_ror:2 row_mask:0xf bank_mask:0xf\n"
"Mvaa 136,144,168\nv_add_f32_dpp v34, v34, v34 row_ror:2 row_mask:0xf bank_mask:0xf\nMvaa 76,136,168\nTa 240,18,0\nv_add_f32_dpp v35, v35, v35 row_ror:2 row_mask:0xf bank_mask:0xf\n"
"Mvaa 76,140,172\nTa 242,18,2048\nv_add_f32_dpp v36, v36, v36 row_ror:2 row_mask:0xf bank_mask:0xf\nMvaa 72,132,172\nD0 3,20,45,1024\n"
"v_add_f32_dpp v37, v37, v37 row_ror:2 row_mask:0xf bank_mask:0xf\nMvaa 72,128,168\nTa 244,18,16384\nv_add_f32_dpp v38, v38, v38 row_ror:2 row_mask:0xf bank_mask:0xf\n"
"Mvaa 80,128,176\nTa 246,18,18432\nv_add_f32_dpp v39, v39, v39 row_ror:2 row_mask:0xf bank_mask:0xf\nMvaa 80,132,180\nv_lshrrev_b32 v48, 4, v0\nMvaa 84,140,180\nTa 248,19,0\n"
"v_and_b32 v49, 1, v0\nMvaa 84,136,176\nTa 250,19,2048\nv_lshlrev_b32 v49, 5, v49\nMvaa 144,144,176\nv_lshl_add_u32 v49, v48, 6, v49\ns_lshl_b32 s2, s11, 8\nMvaa 144,148,180\n"
"Ta 252,19,16384\ns_add_u32 s2, s2, 132096\nMvaa 148,156,180\nTa 254,19,18432\nMvaa 148,152,176\nD0 4,20,45,2048\nv_add_u32 v49, s2, v49\nMvaa 156,152,184\n"
"ds_write_b128 v49, v[32:35]\nds_write_b128 v49, v[36:39] offset:16\nMvaa 156,156,188\nv_and_b32 v50, 31, v0\nv_lshlrev_b32 v50, 2, v50\nMvaa 152,148,188\ns_lshl_b32 s2, s11, 10\n"
"s_add_u32 s2, s2, 133120\nMvaa 152,144,184\nv_add_u32 v51, s2, v50\nMvaa 92,136,184\nv_add_u32 v50, 132096, v50\nv_lshl_add_u32 v52, v48, 4, s2\nMvaa 92,140,188\nD0 5,20,45,3072\n"
"Mvaa 88,132,188\nMvaa 88,128,184\ns_waitcnt vmcnt(18) lgkmcnt(0)\ns_barrier\nMvaa 96,128,224\nRa 192,10,16384\nds_read_b32 v32, v50 offset:0\nMvaa 96,132,228\n"
"s_add_u32 m0, s47, 0\ns_add_u32 s44, s44, 0x40000\nMvaa 100,140,228\nRa 196,11,16384\nMvaa 100,136,224\nds_read_b32 v33, v50 offset:256\nds_read_b32 v34, v50 offset:512\n"
"Mvaa 160,144,224\nRa 200,10,18432\nMvaa 160,148,228\nD0 6,24,44,0\nMvaa 164,156,228\nRa 204,11,18432\nds_read_b32 v35, v50 offset:768\nMvaa 164,152,224\n"
"ds_read_b32 v36, v50 offset:128\nMvaa 172,152,232\nRa 208,10,24576\nds_read_b32 v37, v50 offset:384\nMvaa 172,156,236\nds_read_b32 v38, v50 offset:640\nMvaa 168,148,236\n"
"Ra 212,11,24576\nMvaa 168,144,232\nds_read_b32 v39, v50 offset:896\nMvaa 108,136,232\nRa 216,10,26624\nMvaa 108,140,236\nD0 7,24,44,1024\nMvaa 104,132,236\nRa 220,11,26624\n"
"Mvaa 104,128,232\nMvaa 112,128,240\nMvaa 112,132,244\nMvaa 116,140,244\nMvaa 116,136,240\nMvaa 176,144,240\nMvaa 176,148,244\nD0 8,24,44,2048\nMvaa 180,156,244\nWL 2\nAD 32,32,33\n"
"Mvaa 180,152,240\nAD 34,34,35\nMvaa 188,152,248\nAD 36,36,37\nAD 38,38,39\nMvaa 188,156,252\nAD 32,32,34\nMvaa 184,148,252\nAD 36,36,38\nMvaa 184,144,248\nCV 32,32,36\n"
"v_and_b32 v36, 0xffff0000, v32\nMvaa 124,136,248\nv_lshlrev_b32 v32, 16, v32\nMvaa 124,140,252\nD0 9,24,44,3072\nMvaa 120,132,252\nv_mul_f32 v33, 0xbfb8aa3b, v32\n"
"Mvaa 120,128,248\nWL 0\nMaaa 32,192,224\nRa 128,10,32768\nMaaa 32,196,228\ns_add_u32 m0, s47, 16384\ns_add_u32 s44, s44, 0x40000\nMaaa 36,204,228\nRa 132,11,32768\nEX 33,33\n"
"Maaa 36,200,224\nv_add_f32 v33, 1.0, v33\nMaaa 96,208,224\nRa 136,10,34816\nv_rcp_f32 v33, v33\nMaaa 96,212,228\nD0 6,24,44,0\nMaaa 100,220,228\nRa 140,11,34816\nMU 33,32,33\n"
"Maaa 100,216,224\nCV 33,33,33\nMaaa 108,216,232\nRa 144,10,40960\nv_lshlrev_b32 v33, 16, v33\nMaaa 108,220,236\nMU 33,33,36\nMaaa 104,212,236\nRa 148,11,40960\nMaaa 104,208,232\n"
"CV 33,33,33\nv_lshlrev_b32 v33, 16, v33\nMaaa 44,200,232\nRa 152,10,43008\nMaaa 44,204,236\nD0 7,24,44,1024\nMaaa 40,196,236\nRa 156,11,43008\nds_write_b32 v51, v33\n"
"Maaa 40,192,232\nRv 244,52,0\nMaaa 48,192,240\nRv 248,52,64\ns_and_b32 s2, s74, 15\nMaaa 48,196,244\ns_sub_u32 s2, s2, 2\ns_cmp_lt_u32 s2, 14\ns_cselect_b32 s5, 0, 0x40000000\n"
"Maaa 52,204,244\ns_and_b32 s2, s2, 15\nMaaa 52,200,240\ns_mul_i32 s4, s73, 14\ns_add_u32 s4, s4, s2\nMaaa 112,208,240\nv_and_b32 v49, 15, v0\nMaaa 112,212,244\nD0 8,24,44,2048\n"
"Maaa 116,220,244\nv_lshlrev_b32 v50, 3, v49\nv_and_b32 v51, 1, v48\nMaaa 116,216,240\nv_lshl_add_u32 v50, v51, 2, v50\nMaaa 124,216,248\ns_mul_i32 s2, s11, 112\n"
"v_add_u32 v50, s2, v50\nMaaa 124,220,252\nv_lshrrev_b32 v51, 4, v50\nMaaa 120,212,252\nv_lshrrev_b32 v48, 1, v48\nMaaa 120,208,248\nv_mad_u32_u24 v51, v48, 28, v51\n"
"v_mul_u32_u24 v51, 7168, v51\nMaaa 60,200,248\nv_and_b32 v50, 15, v50\nMaaa 60,204,252\nD0 9,24,44,3072\nMaaa 56,196,252\nv_lshl_add_u32 v51, v50, 2, v51\n"
"v_cmp_gt_u32 vcc, 14, v49\ns_lshl_b32 s4, s4, 6\ns_add_u32 s4, s4, 0xc04c800\nv_cndmask_b32 v51, v254, v51, vcc\nMaaa 56,192,248\ns_waitcnt vmcnt(16) lgkmcnt(3)\ns_barrier\n"
"Maaa 0,192,160\nTa 224,16,32768\ns_or_b32 s4, s4, s5\nMaaa 0,196,164\nTa 226,16,34816\nMaaa 4,204,164\ns_add_u32 m0, s47, -49152\ns_add_u32 s48, s48, 128\nMaaa 4,200,160\n"
"Ta 228,16,49152\nWL 3\nMaaa 64,208,160\nTa 230,16,51200\nMaaa 64,212,164\nD0 2,20,48,0\nMaaa 68,220,164\nTa 232,17,32768\nv_lshlrev_b32 v252, 16, v224\nMaaa 68,216,160\n"
"Ta 234,17,34816\nMaaa 76,216,168\nv_and_b32 v253, 0xffff0000, v224\nMU 32,244,252\nMaaa 76,220,172\nTa 236,17,49152\nMaaa 72,212,172\nTa 238,17,51200\nMaaa 72,208,168\n"
"MU 33,244,253\nMU 40,248,252\nMaaa 12,200,168\nTa 240,18,32768\nMaaa 12,204,172\nTa 242,18,34816\nMaaa 8,196,172\nD0 3,20,48,1024\nMU 41,248,253\nMaaa 8,192,168\nTa 244,18,49152\n"
"Maaa 16,192,176\nTa 246,18,51200\nv_lshlrev_b32 v252, 16, v225\nMaaa 16,196,180\nv_and_b32 v253, 0xffff0000, v225\nMaaa 20,204,180\nTa 248,19,32768\nMaaa 20,200,176\n"
"Ta 250,19,34816\nMU 34,244,252\nMaaa 80,208,176\nMU 35,244,253\nMaaa 80,212,180\nTa 252,19,49152\nMaaa 84,220,180\nTa 254,19,51200\nMU 42,248,252\nMaaa 84,216,176\n"
"D0 4,20,48,2048\nMaaa 92,216,184\nMU 43,248,253\nv_lshlrev_b32 v252, 16, v226\nMaaa 92,220,188\nv_and_b32 v253, 0xffff0000, v226\nMaaa 88,212,188\nMU 36,244,252\nMaaa 88,208,184\n"
"MU 37,244,253\nMU 44,248,252\nMaaa 28,200,184\nMU 45,248,253\nMaaa 28,204,188\nD0 5,20,48,3072\nMaaa 24,196,188\nv_lshlrev_b32 v252, 16, v227\nv_and_b32 v253, 0xffff0000, v227\n"
"Maaa 24,192,184\nWL 0\nMvaa 96,128,224\nTa 160,12,32768\nMU 38,244,252\nMvaa 96,132,228\nTa 162,12,34816\nMvaa 100,140,228\ns_add_u32 m0, s47, -32768\ns_add_u32 s45, s45, 128\n"
"Mvaa 100,136,224\nTa 164,12,49152\nMU 39,244,253\nMvaa 160,144,224\nTa 166,12,51200\nMvaa 160,148,228\nD0 2,20,45,0\nMvaa 164,156,228\nTa 168,13,32768\nMU 46,248,252\n"
"Mvaa 164,152,224\nTa 170,13,34816\nMvaa 172,152,232\nMU 47,248,253\nv_lshlrev_b32 v252, 16, v228\nMvaa 172,156,236\nTa 172,13,49152\nMvaa 168,148,236\nTa 174,13,51200\n"
"Mvaa 168,144,232\nv_and_b32 v253, 0xffff0000, v228\nFM 32,245,252\nMvaa 108,136,232\nTa 176,14,32768\nMvaa 108,140,236\nTa 178,14,34816\nMvaa 104,132,236\nD0 3,20,45,1024\n"
"FM 33,245,253\nMvaa 104,128,232\nTa 180,14,49152\nMvaa 112,128,240\nTa 182,14,51200\nFM 40,249,252\nMvaa 112,132,244\nFM 41,249,253\nMvaa 116,140,244\nTa 184,15,32768\n"
"Mvaa 116,136,240\nTa 186,15,34816\nv_lshlrev_b32 v252, 16, v229\nMvaa 176,144,240\nv_and_b32 v253, 0xffff0000, v229\nMvaa 176,148,244\nTa 188,15,49152\nMvaa 180,156,244\n"
"Ta 190,15,51200\nFM 34,245,252\nMvaa 180,152,240\nD0 4,20,45,2048\nMvaa 188,152,248\nFM 35,245,253\nFM 42,249,252\nMvaa 188,156,252\nFM 43,249,253\nMvaa 184,148,252\n"
"v_lshlrev_b32 v252, 16, v230\nMvaa 184,144,248\nv_and_b32 v253, 0xffff0000, v230\nFM 36,245,252\nMvaa 124,136,248\nFM 37,245,253\nMvaa 124,140,252\nD0 5,20,45,3072\n"
"Mvaa 120,132,252\nFM 44,249,252\nFM 45,249,253\nMvaa 120,128,248\ns_waitcnt vmcnt(16) lgkmcnt(0)\ns_barrier\nMvaa 64,128,160\nRa 192,10,49152\nv_lshlrev_b32 v252, 16, v231\n"
"Mvaa 64,132,164\ns_add_u32 m0, s47, 32768\ns_add_u32 s44, s44, 0x40000\nMvaa 68,140,164\nRa 196,11,49152\nMvaa 68,136,160\nv_and_b32 v253, 0xffff0000, v231\nFM 38,245,252\n"
"Mvaa 128,144,160\nRa 200,10,51200\nMvaa 128,148,164\nD0 6,24,44,0\nMvaa 132,156,164\nRa 204,11,51200\nFM 39,245,253\nMvaa 132,152,160\nFM 46,249,252\nMvaa 140,152,168\n"
"Ra 208,10,57344\nFM 47,249,253\nMvaa 140,156,172\nv_lshlrev_b32 v252, 16, v232\nMvaa 136,148,172\nRa 212,11,57344\nMvaa 136,144,168\nv_and_b32 v253, 0xffff0000, v232\n"
"FM 32,246,252\nMvaa 76,136,168\nRa 216,10,59392\nMvaa 76,140,172\nD0 7,24,44,1024\nMvaa 72,132,172\nRa 220,11,59392\nFM 33,246,253\nMvaa 72,128,168\nFM 40,250,252\n"
"Mvaa 80,128,176\nFM 41,250,253\nv_lshlrev_b32 v252, 16, v233\nMvaa 80,132,180\nv_and_b32 v253, 0xffff0000, v233\nMvaa 84,140,180\nFM 34,246,252\nMvaa 84,136,176\nFM 35,246,253\n"
"FM 42,250,252\nMvaa 144,144,176\nFM 43,250,253\nMvaa 144,148,180\nD0 8,24,44,2048\nMvaa 148,156,180\nv_lshlrev_b32 v252, 16, v234\nv_and_b32 v253, 0xffff0000, v234\n"
"Mvaa 148,152,176\nFM 36,246,252\nMvaa 156,152,184\nFM 37,246,253\nFM 44,250,252\nMvaa 156,156,188\nFM 45,250,253\nMvaa 152,148,188\nv_lshlrev_b32 v252, 16, v235\nMvaa 152,144,184\n"
"v_and_b32 v253, 0xffff0000, v235\nFM 38,246,252\nMvaa 92,136,184\nFM 39,246,253\nMvaa 92,140,188\nD0 9,24,44,3072\nMvaa 88,132,188\nFM 46,250,252\nFM 47,250,253\nMvaa 88,128,184\n"
"WL 0\nMaaa 0,192,160\nRa 128,10,0\nv_lshlrev_b32 v252, 16, v236\nMaaa 0,196,164\ns_add_u32 m0, s47, 49152\ns_add_u32 s44, s44, 0x40000\nMaaa 4,204,164\nRa 132,11,0\n"
"Maaa 4,200,160\nv_and_b32 v253, 0xffff0000, v236\nFM 32,247,252\nMaaa 64,208,160\nRa 136,10,2048\nMaaa 64,212,164\nD0 6,24,44,0\nMaaa 68,220,164\nRa 140,11,2048\nFM 33,247,253\n"
"Maaa 68,216,160\nFM 40,251,252\nMaaa 76,216,168\nRa 144,10,8192\nFM 41,251,253\nMaaa 76,220,172\nv_lshlrev_b32 v252, 16, v237\nMaaa 72,212,172\nRa 148,11,8192\nMaaa 72,208,168\n"
"v_and_b32 v253, 0xffff0000, v237\nFM 34,247,252\nMaaa 12,200,168\nRa 152,10,10240\nMaaa 12,204,172\nD0 7,24,44,1024\nMaaa 8,196,172\nRa 156,11,10240\nFM 35,247,253\n"
"Maaa 8,192,168\nFM 42,251,252\nMaaa 16,192,176\nFM 43,251,253\nv_lshlrev_b32 v252, 16, v238\nMaaa 16,196,180\nv_and_b32 v253, 0xffff0000, v238\nMaaa 20,204,180\nFM 36,247,252\n"
"Maaa 20,200,176\nFM 37,247,253\nFM 44,251,252\nMaaa 80,208,176\nFM 45,251,253\nMaaa 80,212,180\nD0 8,24,44,2048\nMaaa 84,220,180\nv_lshlrev_b32 v252, 16, v239\n"
"v_and_b32 v253, 0xffff0000, v239\nMaaa 84,216,176\nFM 38,247,252\nMaaa 92,216,184\nFM 39,247,253\nFM 46,251,252\nMaaa 92,220,188\nFM 47,251,253\nMaaa 88,212,188\n"
"v_permlane32_swap_b32 v32, v40\nMaaa 88,208,184\nv_permlane32_swap_b32 v33, v41\nv_permlane32_swap_b32 v34, v42\nMaaa 28,200,184\nv_permlane32_swap_b32 v35, v43\nMaaa 28,204,188\n"
"D0 9,24,44,3072\nMaaa 24,196,188\nv_permlane32_swap_b32 v36, v44\nv_permlane32_swap_b32 v37, v45\nMaaa 24,192,184\nWV 16\ns_barrier\nMaaa 32,192,224\nTa 160,12,0\n"
"v_permlane32_swap_b32 v38, v46\nMaaa 32,196,228\nTa 162,12,2048\nMaaa 36,204,228\ns_add_u32 m0, s47, -16384\ns_add_u32 s48, s48, 128\nMaaa 36,200,224\nTa 164,12,16384\n"
"v_permlane32_swap_b32 v39, v47\nMaaa 96,208,224\nTa 166,12,18432\nMaaa 96,212,228\nD0 2,20,48,0\nMaaa 100,220,228\nTa 168,13,0\nAD 32,32,40\nMaaa 100,216,224\nTa 170,13,2048\n"
"Maaa 108,216,232\nAD 33,33,41\nAD 34,34,42\nMaaa 108,220,236\nTa 172,13,16384\nMaaa 104,212,236\nTa 174,13,18432\nMaaa 104,208,232\nAD 35,35,43\nAD 36,36,44\nMaaa 44,200,232\n"
"Ta 176,14,0\nMaaa 44,204,236\nTa 178,14,2048\nMaaa 40,196,236\nD0 3,20,48,1024\nAD 37,37,45\nMaaa 40,192,232\nTa 180,14,16384\nMaaa 48,192,240\nTa 182,14,18432\nAD 38,38,46\n"
"Maaa 48,196,244\nAD 39,39,47\nMaaa 52,204,244\nTa 184,15,0\nMaaa 52,200,240\nTa 186,15,2048\nv_permlane16_swap_b32 v32, v36\nMaaa 112,208,240\nv_permlane16_swap_b32 v33, v37\n"
"Maaa 112,212,244\nTa 188,15,16384\nMaaa 116,220,244\nTa 190,15,18432\nv_permlane16_swap_b32 v34, v38\nMaaa 116,216,240\nD0 4,20,48,2048\nMaaa 124,216,248\n"
"v_permlane16_swap_b32 v35, v39\nAD 32,32,36\nMaaa 124,220,252\nAD 33,33,37\nMaaa 120,212,252\nAD 34,34,38\nMaaa 120,208,248\nAD 35,35,39\nSv 32,51,16,4,0\nMaaa 60,200,248\n"
"Maaa 60,204,252\nD0 5,20,48,3072\nMaaa 56,196,252\nMaaa 56,192,248\nWL 0\nMvaa 64,128,160\nTa 224,16,0\ns_lshr_b32 s3, s11, 1\nMvaa 64,132,164\nTa 226,16,2048\n"
"s_add_u32 s6, s56, 16384\nMvaa 68,140,164\ns_lshl_b32 s3, s3, 13\nMvaa 68,136,160\nTa 228,16,16384\nbuffer_load_dwordx4 v[224:227], v1, s[16:19], s6 offen offset:0 sc1\n"
"Mvaa 128,144,160\nTa 230,16,18432\nMvaa 128,148,164\ns_lshl_b32 s2, s11, 12\nMvaa 132,156,164\nTa 232,17,0\nMvaa 132,152,160\nTa 234,17,2048\nMvaa 140,152,168\n"
"s_add_u32 s2, s2, s3\ns_add_u32 s2, s2, s72\nMvaa 140,156,172\nTa 236,17,16384\nbuffer_load_dwordx4 v[228:231], v1, s[16:19], s6 offen offset:1024 sc1\nMvaa 136,148,172\n"
"Ta 238,17,18432\nMvaa 136,144,168\ns_lshl_b32 s3, s11, 12\nMvaa 76,136,168\nTa 240,18,0\nMvaa 76,140,172\nTa 242,18,2048\nMvaa 72,132,172\ns_add_u32 s89, s3, 0x0\nMvaa 72,128,168\n"
"Ta 244,18,16384\nbuffer_load_dwordx4 v[232:235], v1, s[16:19], s6 offen offset:2048 sc1\nMvaa 80,128,176\nTa 246,18,18432\ns_mov_b32 s88, 0xffff8000\nMvaa 80,132,180\n"
"s_lshl_b32 s4, s13, 1\nMvaa 84,140,180\nTa 248,19,0\nMvaa 84,136,176\nTa 250,19,2048\nMvaa 144,144,176\ns_sub_u32 s79, 15, s4\nMvaa 144,148,180\nTa 252,19,16384\n"
"buffer_load_dwordx4 v[236:239], v1, s[16:19], s6 offen offset:3072 sc1\nMvaa 148,156,180\nTa 254,19,18432\nMvaa 148,152,176\ns_add_u32 s96, s2, 0x1310000\nMvaa 156,152,184\n"
"s_mov_b32 s63, 0x53800000\ns_mov_b32 s64, 0x2b800000\nMvaa 156,156,188\ns_sub_u32 s98, s4, 1\ns_add_u32 s6, s56, 20480\nMvaa 152,148,188\ns_lshl_b32 s5, s13, 5\nMvaa 152,144,184\n"
"s_add_u32 s5, s5, s11\nbuffer_load_dwordx4 v[240:243], v1, s[16:19], s6 offen offset:0 sc1\nMvaa 92,136,184\ns_lshl_b32 s65, s5, 9\nMvaa 92,140,188\ns_add_u32 s65, s65, s72\n"
"Mvaa 88,132,188\ns_add_u32 s65, s65, 0x1580000\nMvaa 88,128,184\ns_waitcnt vmcnt(18) lgkmcnt(0)\ns_barrier\nMvaa 96,128,224\nRa 192,10,16384\ns_and_b32 s66, s72, 0x2000\n"
"Mvaa 96,132,228\ns_add_u32 s66, s66, s56\nbuffer_load_dwordx4 v[244:247], v1, s[16:19], s6 offen offset:1024 sc1\nMvaa 100,140,228\nRa 196,11,16384\nMvaa 100,136,224\n"
"s_add_u32 s66, s66, 0x100000\nMvaa 160,144,224\nRa 200,10,18432\nMvaa 160,148,228\ns_and_b32 s5, s12, 1\nMvaa 164,156,228\nRa 204,11,18432\nMvaa 164,152,224\n"
"s_mul_i32 s58, s5, 0x202000\nbuffer_load_dwordx4 v[248:251], v1, s[16:19], s6 offen offset:2048 sc1\nMvaa 172,152,232\nRa 208,10,24576\ns_xor_b32 s58, s58, s72\nMvaa 172,156,236\n"
"s_add_u32 s58, s58, 0x15c0000\nMvaa 168,148,236\nRa 212,11,24576\nMvaa 168,144,232\ns_lshl_b32 s5, s5, 8\nMvaa 108,136,232\nRa 216,10,26624\nMvaa 108,140,236\n"
"s_add_u32 s61, s58, s5\nbuffer_load_dwordx4 v[252:255], v1, s[16:19], s6 offen offset:3072 sc1\nMvaa 104,132,236\nRa 220,11,26624\nMvaa 104,128,232\ns_lshl4_add_u32 s60, s13, s61\n"
"Mvaa 112,128,240\ns_add_u32 s54, s60, 0\ns_xor_b32 s60, s54, 0x100\nMvaa 112,132,244\ns_cmp_eq_u32 s11, 0\ns_cselect_b32 s54, s54, 0x40000000\ns_add_u32 s6, s56, 0\n"
"Mvaa 116,140,244\ns_getpc_b64 s[8:9]\n.PC165:\ns_add_u32 s8, s8, .HD142-.PC165\ns_addc_u32 s9, s9, 0\nMvaa 116,136,240\ns_mov_b32 s97, 0xff800000\n"
"buffer_load_dwordx4 v[192:195], v1, s[16:19], s6 offen offset:0 sc1\nMvaa 176,144,240\ns_mov_b32 s90, 3\ns_cmp_lt_u32 s11, 2\ns_cselect_b32 s92, 2, 0\nMvaa 176,148,244\n"
"s_add_u32 s86, s2, 0x20000\nMvaa 180,156,244\ns_add_u32 s87, s2, 0x410000\ns_cmp_eq_u32 s13, 7\ns_cselect_b32 s92, s92, 0\nMvaa 180,152,240\ns_cmp_gt_u32 s13, 5\n"
"s_cselect_b32 s4, s12, 15\nMvaa 188,152,248\ns_cmp_lt_u32 s4, 14\ns_cselect_b32 s5, s11, 3\nMvaa 188,156,252\ns_cmp_lt_u32 s5, 2\ns_cselect_b32 s91, 1, 100\n"
"buffer_load_dwordx4 v[196:199], v1, s[16:19], s6 offen offset:1024 sc1\nMvaa 184,148,252\nv_and_b32 v27, 0x70, v1\n"
"buffer_load_dword v27, v27, s[16:19], s61 offen offset:128 sc0 sc1\nMvaa 184,144,248\ns_lshr_b32 s2, s13, 2\ns_bfe_u32 s3, s13, 0x10001\nMvaa 124,136,248\ns_mul_i32 s3, s3, 17\n"
"s_add_u32 s2, s2, s3\nMvaa 124,140,252\ns_lshl2_add_u32 s3, s14, 0\nMvaa 120,132,252\ns_lshl_b32 s3, s3, 2\ns_lshr_b32 s3, 0xe420ca86, s3\nMvaa 120,128,248\nWL 0\nMaaa 32,192,224\n"
"Ra 128,10,32768\ns_and_b32 s3, s3, 15\nbuffer_load_dwordx4 v[200:203], v1, s[16:19], s6 offen offset:2048 sc1\nMaaa 32,196,228\ns_add_u32 s2, s2, s3\ns_cmp_eq_u32 s2, 32\n"
"s_cselect_b32 s4, 16, s2\nMaaa 36,204,228\nRa 132,11,32768\ns_mul_i32 s75, s4, 65536\nMaaa 36,200,224\ns_lshr_b32 s2, s13, 2\ns_bfe_u32 s3, s13, 0x10001\nMaaa 96,208,224\n"
"Ra 136,10,34816\nMaaa 96,212,228\ns_mul_i32 s3, s3, 17\ns_add_u32 s2, s2, s3\nMaaa 100,220,228\nRa 140,11,34816\ns_lshl2_add_u32 s3, s14, 1\n"
"buffer_load_dwordx4 v[204:207], v1, s[16:19], s6 offen offset:3072 sc1\nMaaa 100,216,224\ns_lshl_b32 s3, s3, 2\nMaaa 108,216,232\nRa 144,10,40960\ns_lshr_b32 s3, 0xe420ca86, s3\n"
"Maaa 108,220,236\ns_and_b32 s3, s3, 15\ns_add_u32 s2, s2, s3\nMaaa 104,212,236\nRa 148,11,40960\ns_cmp_eq_u32 s2, 32\ns_cselect_b32 s4, 16, s2\ns_add_u32 s6, s56, 4096\n"
"Maaa 104,208,232\ns_mul_i32 s76, s4, 65536\ns_lshr_b32 s2, s13, 2\nMaaa 44,200,232\nRa 152,10,43008\ns_bfe_u32 s3, s13, 0x10001\n"
"buffer_load_dwordx4 v[208:211], v1, s[16:19], s6 offen offset:0 sc1\nMaaa 44,204,236\ns_mul_i32 s3, s3, 17\nMaaa 40,196,236\nRa 156,11,43008\ns_add_u32 s2, s2, s3\n"
"Maaa 40,192,232\ns_lshl2_add_u32 s3, s14, 2\ns_lshl_b32 s3, s3, 2\nMaaa 48,192,240\ns_lshr_b32 s3, 0xe420ca86, s3\ns_and_b32 s3, s3, 15\nMaaa 48,196,244\ns_add_u32 s2, s2, s3\n"
"s_cmp_eq_u32 s2, 32\ns_cselect_b32 s4, 16, s2\nMaaa 52,204,244\ns_mul_i32 s77, s4, 65536\ns_lshr_b32 s2, s13, 2\n"
"buffer_load_dwordx4 v[212:215], v1, s[16:19], s6 offen offset:1024 sc1\nMaaa 52,200,240\ns_bfe_u32 s3, s13, 0x10001\ns_mul_i32 s3, s3, 17\nMaaa 112,208,240\ns_add_u32 s2, s2, s3\n"
"Maaa 112,212,244\ns_lshl2_add_u32 s3, s14, 3\ns_lshl_b32 s3, s3, 2\nMaaa 116,220,244\ns_lshr_b32 s3, 0xe420ca86, s3\ns_and_b32 s3, s3, 15\nMaaa 116,216,240\ns_add_u32 s2, s2, s3\n"
"s_cmp_eq_u32 s2, 32\ns_cselect_b32 s4, 16, s2\nMaaa 124,216,248\ns_mul_i32 s78, s4, 65536\ns_lshr_b32 s3, s11, 1\n"
"buffer_load_dwordx4 v[216:219], v1, s[16:19], s6 offen offset:2048 sc1\nMaaa 124,220,252\ns_lshl_b32 s3, s3, 15\ns_add_u32 s2, s72, s3\nMaaa 120,212,252\ns_and_b32 s3, s11, 1\n"
"Maaa 120,208,248\ns_lshl_b32 s3, s3, 14\ns_add_u32 s2, s2, s3\nMaaa 60,200,248\ns_add_u32 s2, s2, 0x400000\ns_mov_b32 s55, 4096\nMaaa 60,204,252\ns_add_u32 s75, s75, s2\n"
"Maaa 56,196,252\ns_add_u32 s76, s76, s2\ns_add_u32 s77, s77, s2\nbuffer_load_dwordx4 v[220:223], v1, s[16:19], s6 offen offset:3072 sc1\nMaaa 56,192,248\nWV 22\ns_barrier\n"
"Maaa 0,192,160\nTa 224,16,32768\ns_add_u32 s78, s78, s2\nMaaa 0,196,164\nTa 226,16,34816\nMaaa 4,204,164\nMaaa 4,200,160\nTa 228,16,49152\nMaaa 64,208,160\nTa 230,16,51200\n"
"Maaa 64,212,164\nMaaa 68,220,164\nTa 232,17,32768\nMaaa 68,216,160\nTa 234,17,34816\nMaaa 76,216,168\nWV 6\nv_cmp_ne_u32 vcc, s15, v27\ns_nop 0\ns_cbranch_vccnz .S166\n.S167:\n"
"s_sub_u32 s45, s86, 131072\nMaaa 76,220,172\nTa 236,17,49152\ns_add_u32 m0, s47, 0\ns_add_u32 s46, s45, 0x0\nMaaa 72,212,172\nTa 238,17,51200\nMaaa 72,208,168\nD1 1,16,46,0\n"
"s_mov_b32 s36, s68\nMaaa 12,200,168\nTa 240,18,32768\nMaaa 12,204,172\nTa 242,18,34816\nMaaa 8,196,172\ns_and_b32 s37, s69, 0xffff\nMaaa 8,192,168\nTa 244,18,49152\n"
"Maaa 16,192,176\nTa 246,18,51200\nD1 1,16,46,1024\nMaaa 16,196,180\ns_mov_b32 s38, 0x1c0000\nMaaa 20,204,180\nTa 248,19,32768\nMaaa 20,200,176\nTa 250,19,34816\nMaaa 80,208,176\n"
"s_mov_b32 s39, 0x20000\nMaaa 80,212,180\nTa 252,19,49152\nD1 1,16,46,2048\nMaaa 84,220,180\nTa 254,19,51200\ns_and_b32 s2, s74, 15\nMaaa 84,216,176\ns_max_u32 s2, s2, 2\n"
"Maaa 92,216,184\ns_sub_u32 s2, s2, 2\nMaaa 92,220,188\ns_mul_i32 s3, s73, 14\nMaaa 88,212,188\nD1 1,16,46,3072\nMaaa 88,208,184\ns_add_u32 s2, s2, s3\ns_lshl2_add_u32 s2, s2, s11\n"
"Maaa 28,200,184\ns_mul_i32 s3, s2, 4682\nMaaa 28,204,188\ns_lshr_b32 s3, s3, 16\nMaaa 24,196,188\ns_mul_i32 s22, s3, 14\nMaaa 24,192,184\nWL 0\nMvaa 96,128,224\nTa 160,12,32768\n"
"s_add_u32 m0, s47, -65536\ns_add_u32 s46, s45, 0x10000\nD1 1,16,46,0\nMvaa 96,132,228\nTa 162,12,34816\nMvaa 100,140,228\ns_sub_u32 s22, s2, s22\nMvaa 100,136,224\n"
"Ta 164,12,49152\nMvaa 108,136,232\nTa 166,12,51200\nMvaa 108,140,236\nD1 1,16,46,1024\nMvaa 104,132,236\nTa 168,13,32768\ns_and_b32 s21, s3, 28\nMvaa 104,128,232\nTa 170,13,34816\n"
"Mvaa 112,128,240\ns_add_u32 s21, s21, s3\nMvaa 112,132,244\nTa 172,13,49152\nMvaa 116,140,244\nTa 174,13,51200\nD1 1,16,46,2048\nMvaa 116,136,240\ns_lshl_b32 s21, s21, 4\n"
"s_add_u32 s21, s21, 1024\nMvaa 124,136,248\nTa 176,14,32768\nCV 48,96,97\nMvaa 124,140,252\nTa 178,14,34816\nMvaa 120,132,252\ns_xor_b32 s3, s3, 28\nCV 49,98,99\nMvaa 120,128,248\n"
"Ta 180,14,49152\nD1 1,16,46,3072\nMvaa 160,144,224\nTa 182,14,51200\ns_bfe_u32 s41, s3, 0x30002\nMvaa 160,148,228\ns_cmp_gt_u32 s22, 6\ns_cselect_b32 s2, 1179648, 0\n"
"s_cselect_b32 s1, 16, 0\nMvaa 164,156,228\nTa 184,15,32768\nMvaa 164,152,224\nTa 186,15,34816\nCV 50,100,101\nMvaa 172,152,232\ns_add_u32 s41, s41, s1\nMvaa 172,156,236\n"
"Ta 188,15,49152\nCV 51,102,103\nMvaa 168,148,236\nTa 190,15,51200\ns_lshl4_add_u32 s41, s41, 0xc048400\nMvaa 168,144,232\ns_and_b32 s1, s3, 28\nCV 52,104,105\nMvaa 176,144,240\n"
"s_add_u32 s3, s3, s1\nMvaa 176,148,244\ns_lshl_b32 s3, s3, 11\nMvaa 180,156,244\ns_add_u32 s2, s2, s3\nCV 53,106,107\nMvaa 180,152,240\ns_lshl_b32 s3, s22, 17\n"
"s_add_u32 s2, s2, s3\nMvaa 188,152,248\ns_add_u32 s20, s2, 0xce0000\nCV 54,108,109\nMvaa 188,156,252\ns_lshl_b32 s2, s22, 7\nMvaa 184,148,252\ns_add_u32 s40, s2, 0xc04b800\n"
"CV 55,110,111\nMvaa 184,144,248\nWL 0\ns_barrier\nMvaa 64,128,160\nRa 192,10,49152\ns_add_u32 m0, s47, 32768\ns_add_u32 s46, s45, 0x8000\nMvaa 64,132,164\ns_mul_i32 s2, s21, 896\n"
"Mvaa 68,140,164\nRa 196,11,49152\nD1 1,16,46,0\nMvaa 68,136,160\ns_lshl_b32 s3, s22, 6\nCV 56,112,113\nMvaa 76,136,168\nRa 200,10,51200\nMvaa 76,140,172\ns_add_u32 s22, s2, s3\n"
"CV 57,114,115\nMvaa 72,132,172\nRa 204,11,51200\ns_add_u32 s23, s22, 57344\nMvaa 72,128,168\nD1 1,16,46,1024\nCV 58,116,117\nMvaa 80,128,176\nRa 208,10,57344\nMvaa 80,132,180\n"
"CV 59,118,119\nMvaa 84,140,180\nRa 212,11,57344\nCV 60,120,121\nMvaa 84,136,176\nCV 61,122,123\nMvaa 92,136,184\nRa 216,10,59392\nD1 1,16,46,2048\nMvaa 92,140,188\nCV 62,124,125\n"
"Mvaa 88,132,188\nRa 220,11,59392\nCV 63,126,127\nMvaa 88,128,184\nCV 32,64,65\nMvaa 128,144,160\nCV 33,66,67\nCV 34,68,69\nMvaa 128,148,164\nD1 1,16,46,3072\nMvaa 132,156,164\n"
"CV 35,70,71\nMvaa 132,152,160\nCV 36,72,73\nCV 37,74,75\nMvaa 140,152,168\ns_add_u32 m0, s47, 49152\ns_add_u32 s46, s45, 0x400000\nMvaa 140,156,172\nCV 38,76,77\nCV 39,78,79\n"
"Mvaa 136,148,172\nD1 1,16,46,0\nMvaa 136,144,168\nCV 40,80,81\nCV 41,82,83\nMvaa 144,144,176\nCV 42,84,85\nMvaa 144,148,180\nCV 43,86,87\nMvaa 148,156,180\nCV 44,88,89\n"
"CV 45,90,91\nMvaa 148,152,176\nD1 1,16,46,1024\nMvaa 156,152,184\nCV 46,92,93\nCV 47,94,95\nMvaa 156,156,188\nCV 112,160,161\nMvaa 152,148,188\nCV 113,162,163\nCV 114,164,165\n"
"Mvaa 152,144,184\nWL 0\nMaaa 0,192,160\nD1 1,16,46,2048\nSv 32,1,16,75,0\nMaaa 0,196,164\nCV 115,166,167\nMaaa 4,204,164\nCV 116,168,169\nCV 117,170,171\nMaaa 4,200,160\n"
"CV 118,172,173\nCV 119,174,175\nMaaa 12,200,168\nCV 120,176,177\nCV 121,178,179\nMaaa 12,204,172\nD1 1,16,46,3072\nMaaa 8,196,172\nCV 122,180,181\nCV 123,182,183\nMaaa 8,192,168\n"
"CV 124,184,185\nCV 125,186,187\nMaaa 16,192,176\ns_add_u32 m0, s47, -49152\ns_add_u32 s46, s45, 0x408000\nSv 36,1,16,75,1024\nMaaa 16,196,180\nCV 126,188,189\nMaaa 20,204,180\n"
"D1 1,16,46,0\nCV 127,190,191\nMaaa 20,200,176\nCV 96,128,129\nCV 97,130,131\nMaaa 28,200,184\nCV 98,132,133\nCV 99,134,135\nMaaa 28,204,188\nCV 100,136,137\nMaaa 24,196,188\n"
"CV 101,138,139\nCV 102,140,141\nMaaa 24,192,184\nD1 1,16,46,1024\nCV 103,142,143\nMaaa 64,208,160\nSv 40,1,16,75,2048\nCV 104,144,145\nMaaa 64,212,164\nCV 105,146,147\n"
"Maaa 68,220,164\nCV 106,148,149\nCV 107,150,151\nMaaa 68,216,160\nCV 108,152,153\nCV 109,154,155\nMaaa 76,216,168\nD1 1,16,46,2048\nCV 110,156,157\nMaaa 76,220,172\n"
"CV 111,158,159\nMaaa 72,212,172\nv_add_u32 v2, 0x10000, v1\ns_lshr_b32 s2, s59, 1\nMaaa 72,208,168\nv_add_u32 v24, s2, v2\nv_xor_b32 v26, 0x1000, v24\nMaaa 80,208,176\n"
"Sv 44,1,16,75,3072\nv_lshrrev_b32 v20, 4, v0\nMaaa 80,212,180\nD1 1,16,46,3072\nMaaa 84,220,180\nv_lshlrev_b32 v20, 2, v20\nv_and_b32 v0, 15, v0\nMaaa 84,216,176\n"
"v_sub_u32 v0, v0, v20\ns_lshl_b32 s2, s11, 4\nMaaa 92,216,184\nv_add_u32 v3, s2, v0\nv_subrev_u32 v0, 32, v3\nMaaa 92,220,188\nv_mov_b32 v4, 0\nMaaa 88,212,188\nv_mov_b32 v5, 0\n"
"v_mov_b32 v6, 0\nMaaa 88,208,184\nWV 20\ns_barrier\nMaaa 32,192,224\ns_add_u32 s75, s75, s55\nSv 48,1,16,75,0\nMaaa 32,196,228\nv_mov_b32 v7, 0\nMaaa 36,204,228\nv_mov_b32 v8, 0\n"
"v_mov_b32 v9, 0\nMaaa 36,200,224\nv_mov_b32 v10, 0\nv_mov_b32 v11, 0\nMaaa 44,200,232\nv_mov_b32 v12, 0\nv_mov_b32 v13, 0\nMaaa 44,204,236\nv_mov_b32 v14, 0\nMaaa 40,196,236\n"
"v_mov_b32 v15, 0\nv_mov_b32 v16, 0\nMaaa 40,192,232\nv_mov_b32 v17, 0\nv_mov_b32 v18, 0\nMaaa 48,192,240\nSv 52,1,16,75,1024\nv_mov_b32 v19, 0\nMaaa 48,196,244\nv_mov_b32 v86, 0\n"
"Maaa 52,204,244\nv_mov_b32 v87, 0\nv_mov_b32 v88, 0\nMaaa 52,200,240\nv_mov_b32 v89, 0\nAR 92,0\nAR 93,1\nMaaa 60,200,248\nCV 128,92,93\nAR 92,2\nAR 93,3\nMaaa 60,204,252\n"
"CV 129,92,93\nAR 92,4\nAR 93,5\nMaaa 56,196,252\nRa 128,24,0\nCV 130,92,93\nAR 92,6\nMaaa 56,192,248\nAR 93,7\nCV 131,92,93\nAR 92,8\nMaaa 96,208,224\nSv 56,1,16,75,2048\n"
"Ra 132,24,1024\nAR 93,9\nMaaa 96,212,228\nCV 132,92,93\nAR 92,10\nAR 93,11\nMaaa 100,220,228\nRa 136,24,2048\nCV 133,92,93\nAR 92,12\nMaaa 100,216,224\nAR 93,13\nCV 134,92,93\n"
"AR 92,14\nMaaa 108,216,232\nRa 140,24,3072\nAR 93,15\nCV 135,92,93\nMaaa 108,220,236\nAR 92,16\nAR 93,17\nCV 136,92,93\nMaaa 104,212,236\nRa 144,26,0\nAR 92,18\nAR 93,19\n"
"Maaa 104,208,232\nCV 137,92,93\nAR 92,20\nAR 93,21\nMaaa 112,208,240\nSv 60,1,16,75,3072\nRa 148,26,1024\nCV 138,92,93\nMaaa 112,212,244\nAR 92,22\nAR 93,23\nCV 139,92,93\n"
"Maaa 116,220,244\nRa 152,26,2048\nAR 92,24\nAR 93,25\nMaaa 116,216,240\nCV 140,92,93\nAR 92,26\nAR 93,27\nMaaa 124,216,248\nRa 156,26,3072\nCV 141,92,93\nAR 92,28\n"
"Maaa 124,220,252\nAR 93,29\nCV 142,92,93\nAR 92,30\nMaaa 120,212,252\nAR 93,31\nCV 143,92,93\nAR 92,32\nMaaa 120,208,248\nWL 4\nCvav 32,128,192,4\nRa 160,24,8192\nAR 93,33\n"
"CV 144,92,93\nMvav 32,132,200\nAR 92,34\nAR 93,35\nCV 145,92,93\nMvav 32,136,208\nRa 164,24,9216\nAR 92,36\nAR 93,37\nMvav 32,140,216\nCV 146,92,93\nAR 92,38\nAR 93,39\n"
"Cvav 36,140,220,8\nRa 168,24,10240\nCV 147,92,93\nAR 92,40\nMvav 36,136,212\ns_add_u32 m0, s89, 32768\nAR 93,41\nCV 148,92,93\nMvav 36,132,204\nRa 172,24,11264\nWL 4\ns_barrier\n"
"Mvav 36,128,196\nAR 92,42\nAR 93,43\nCV 149,92,93\nMvav 36,144,228\nRa 176,26,8192\nAR 92,44\nAR 93,45\nSv 96,1,16,76,0\nMvav 36,148,236\ns_add_u32 s46, s86, s88\nCV 150,92,93\n"
"AR 92,46\nMvav 36,152,244\nRa 180,26,9216\nAR 93,47\nCV 151,92,93\nMvav 36,156,252\nAR 92,48\nAR 93,49\nCV 152,92,93\nMvav 32,156,248\nRa 184,26,10240\nAR 92,50\nAR 93,51\n"
"Mvav 32,152,240\nD1 1,16,46,0\nCV 153,92,93\nAR 92,52\nMvav 32,148,232\nRa 188,26,11264\nWL 4\nAR 93,53\nMvav 32,144,224\nCV 154,92,93\nAR 92,54\nAR 93,55\nCvav 40,160,192,12\n"
"CV 155,92,93\nAR 92,56\nAR 93,57\nMvav 40,164,200\nCV 156,92,93\nAR 92,58\nAR 93,59\nMvav 40,168,208\nCV 157,92,93\nAR 92,60\nAR 93,61\nv_and_b32 v27, 0x70, v1\n"
"buffer_load_dword v27, v27, s[16:19], s61 offen offset:136 sc0 sc1\nMvav 40,172,216\nCV 158,92,93\nAR 92,62\nAR 93,63\nCvav 44,172,220,16\nCV 159,92,93\nAR 92,64\nAR 93,65\n"
"Mvav 44,168,212\nD1 1,16,46,1024\nCV 160,92,93\nAR 92,66\nMvav 44,164,204\nWL 0\nAR 93,67\nCV 161,92,93\nMvav 44,160,196\nAR 92,68\nAR 93,69\nCV 162,92,93\nMvav 44,176,228\n"
"AR 92,70\nAR 93,71\nCV 163,92,93\nSv 100,1,16,76,1024\nMvav 44,180,236\ns_add_u32 s88, s88, 32768\nEX 80,32\nMvav 44,184,244\nAR 92,72\nAR 93,73\nCV 164,92,93\nMvav 44,188,252\n"
"EX 81,33\nMvav 40,188,248\nAR 92,74\nAR 93,75\nCV 165,92,93\nMvav 40,184,240\nD1 1,16,46,2048\nAR 92,76\nAR 93,77\nMvav 40,180,232\nCV 166,92,93\nAR 92,78\nAR 93,79\n"
"Mvav 40,176,224\nAD 84,80,81\nCV 64,80,81\nCV 167,92,93\nAR 92,80\nAR 93,81\nCV 168,92,93\nEX 82,34\nAR 92,82\nAR 93,83\nCV 169,92,93\nAR 92,84\nWV 17\nEX 83,35\nAD 84,84,82\n"
"AR 93,85\nCV 170,92,93\nD1 1,16,46,3072\ns_add_u32 m0, s89, 49152\nAR 92,86\ns_barrier\nAR 93,87\nCV 171,92,93\nAD 84,84,83\nCV 65,82,83\nAR 92,88\nRa 128,24,32768\nAR 93,89\n"
"CV 172,92,93\nSv 104,1,16,76,2048\ns_add_u32 s46, s87, s88\nEX 80,36\nRa 132,24,33792\nAR 92,90\nAR 93,91\nEX 81,37\nRa 136,24,34816\nCV 173,92,93\nAR 92,92\nD1 1,16,46,0\n"
"AR 93,93\nCV 174,92,93\nRa 140,24,35840\nAR 92,94\nAR 93,95\nAD 85,80,81\nCV 68,80,81\nCV 175,92,93\nRa 144,26,32768\nAR 92,96\nAR 93,97\nEX 82,38\nCV 176,92,93\nRa 148,26,33792\n"
"AR 92,98\nAR 93,99\nEX 83,39\nRa 152,26,34816\nAD 85,85,82\nCV 177,92,93\nD1 1,16,46,1024\nAR 92,100\nAR 93,101\nRa 156,26,35840\nCV 178,92,93\nAR 92,102\nAD 85,85,83\n"
"CV 69,82,83\nAR 93,103\nRa 160,24,40960\nCV 179,92,93\nAR 92,104\nSv 108,1,16,76,3072\ns_xor_b32 s89, s89, 0x10000\nEX 80,40\nRa 164,24,41984\nAR 93,105\nCV 180,92,93\nWL 6\n"
"EX 81,41\nRa 168,24,43008\nAD 84,84,80\nAR 92,106\nD1 1,16,46,2048\nAR 93,107\nCV 181,92,93\nRa 172,24,44032\nAR 92,108\nAR 93,109\nAD 84,84,81\nCV 66,80,81\nCV 182,92,93\n"
"Cvav 48,128,192,4\nRa 176,26,40960\nAR 92,110\nAR 93,111\nMvav 48,132,200\nEX 82,42\nCV 183,92,93\nMvav 48,136,208\nRa 180,26,41984\nAR 92,112\nAR 93,113\nMvav 48,140,216\nWL 6\n"
"EX 83,43\nWV 9\nv_cmp_ne_u32 vcc, s15, v27\ns_nop 0\ns_cbranch_vccnz .S169\n.S170:\nCvav 52,140,220,8\nRa 184,26,43008\nAD 84,84,82\nCV 184,92,93\nMvav 52,136,212\n"
"D1 1,16,46,3072\ns_add_u32 m0, s89, 0\nAR 92,114\nMvav 52,132,204\nRa 188,26,44032\nAR 93,115\nCV 185,92,93\nMvav 52,128,196\nAD 84,84,83\nCV 67,82,83\nAR 92,116\nMvav 52,144,228\n"
"Ra 128,2,49152\nAR 93,117\nCV 186,92,93\ns_add_u32 s76, s76, s55\nSv 112,1,16,76,0\nMvav 52,148,236\ns_add_u32 s46, s86, s88\nEX 80,44\nMvav 52,152,244\nRa 132,2,50176\n"
"v_xor_b32 v24, 0x10000, v24\nAR 92,118\nMvav 52,156,252\nWL 6\nEX 81,45\nMvav 48,156,248\nRa 136,2,51200\nAD 85,85,80\nAR 93,119\nMvav 48,152,240\nD1 1,16,46,0\nCV 187,92,93\n"
"AR 92,120\nMvav 48,148,232\nRa 140,2,52224\nAR 93,121\nCV 188,92,93\nMvav 48,144,224\nAD 85,85,81\nCV 70,80,81\nAR 92,122\nCvav 56,160,192,12\nRa 144,2,53248\nAR 93,123\n"
"CV 189,92,93\nMvav 56,164,200\nEX 82,46\nAR 92,124\nMvav 56,168,208\nRa 148,2,54272\nv_xor_b32 v26, 0x10000, v26\nAR 93,125\nMvav 56,172,216\nWL 6\nEX 83,47\nCvav 60,172,220,16\n"
"Ra 152,2,55296\nAD 85,85,82\nCV 190,92,93\nMvav 60,168,212\nD1 1,16,46,1024\nAR 92,126\nAR 93,127\nMvav 60,164,204\nRa 156,2,56320\nCV 191,92,93\nMvav 60,160,196\nAD 85,85,83\n"
"CV 71,82,83\nMvav 60,176,228\nRa 160,2,57344\nv_max_f32 v90, v84, v85\nSv 116,1,16,76,1024\nMvav 60,180,236\ns_add_u32 s88, s88, 32768\nEX 80,48\nMvav 60,184,244\nRa 164,2,58368\n"
"v_cmp_lt_f32 vcc, s63, v90\nMvav 60,188,252\nWL 8\nEX 81,49\nMvav 56,188,248\nRa 168,2,59392\ns_cbranch_vccnz .T151\n.T152:\nv_mov_b32 v20, v84\nv_mov_b32 v21, v85\n"
"v_mov_b32 v22, v20\nv_mov_b32 v23, v21\nMvav 56,184,240\nD1 1,16,46,2048\nv_permlane32_swap_b32 v20, v22\nv_permlane32_swap_b32 v21, v23\nAD 20,20,22\nAD 21,21,23\n"
"v_mov_b32 v22, v20\nv_mov_b32 v23, v21\nMvav 56,180,232\nRa 172,2,60416\nv_permlane16_swap_b32 v20, v22\nv_permlane16_swap_b32 v21, v23\nAD 20,20,22\nAD 21,21,23\n"
"v_min_f32 v90, v20, v21\nv_cmp_gt_f32 vcc, s64, v90\ns_nop 0\ns_cbranch_vccnz .T153\n.T154:\nAD 86,86,84\nMvav 56,176,224\nAD 84,80,81\nCV 72,80,81\nZaav 0,128,64\nRa 176,2,61440\n"
"Zaav 4,128,68\nEX 82,50\nZaav 12,132,68\nRa 180,2,62464\nZaav 8,132,64\nWL 8\nEX 83,51\nZaav 16,136,64\nRa 184,2,63488\nAD 84,84,82\nZaav 20,136,68\nD1 1,16,46,3072\n"
"s_add_u32 m0, s89, 16384\nZaav 28,140,68\nRa 188,2,64512\ns_barrier\nZaav 24,140,64\nAD 84,84,83\nCV 73,82,83\nZaav 32,144,64\nRa 128,24,0\nSv 120,1,16,76,2048\nZaav 36,144,68\n"
"s_add_u32 s46, s87, s88\nEX 80,52\nbuffer_store_dword v25, off, s[16:19], s54 offset:12 sc0 sc1\nZaav 44,148,68\nRa 132,24,1024\nv_xor_b32 v2, 0x10000, v2\nZaav 40,148,64\nWL 8\n"
"EX 81,53\nZaav 48,152,64\nRa 136,24,2048\nAD 87,87,85\nZaav 52,152,68\nD1 1,16,46,0\nZaav 60,156,68\nRa 140,24,3072\nZaav 56,156,64\nAD 85,80,81\nCV 76,80,81\nZaav 64,160,64\n"
"Ra 144,26,0\nZaav 68,160,68\nEX 82,54\nZaav 76,164,68\nRa 148,26,1024\nZaav 72,164,64\nWL 8\nEX 83,55\nZaav 80,168,64\nRa 152,26,2048\nAD 85,85,82\nZaav 84,168,68\n"
"D1 1,16,46,1024\nZaav 92,172,68\nRa 156,26,3072\nZaav 88,172,64\nAD 85,85,83\nCV 77,82,83\nZaav 96,176,64\nRa 160,24,8192\nSv 124,1,16,76,3072\nZaav 100,176,68\nEX 80,56\n"
"Zaav 108,180,68\nRa 164,24,9216\nZaav 104,180,64\nWL 6\nEX 81,57\nZaav 112,184,64\nRa 168,24,10240\nAD 84,84,80\nZaav 116,184,68\nD1 1,16,46,2048\nZaav 124,188,68\n"
"Ra 172,24,11264\nZaav 120,188,64\nAD 84,84,81\nCV 74,80,81\nCvav 32,128,192,4\nRa 176,26,8192\nMvav 32,132,200\nEX 82,58\nMvav 32,136,208\nRa 180,26,9216\nMvav 32,140,216\nWL 6\n"
"EX 83,59\nCvav 36,140,220,8\nRa 184,26,10240\nAD 84,84,82\nMvav 36,136,212\nD1 1,16,46,3072\ns_add_u32 m0, s89, 32768\nMvav 36,132,204\nRa 188,26,11264\nMvav 36,128,196\n"
"AD 84,84,83\nCV 75,82,83\nMvav 36,144,228\nRa 128,2,16384\nSv 128,1,16,77,0\nMvav 36,148,236\ns_add_u32 s46, s86, s88\nEX 80,60\nMvav 36,152,244\nRa 132,2,17408\nMvav 36,156,252\n"
"WL 6\nEX 81,61\nMvav 32,156,248\nRa 136,2,18432\nAD 85,85,80\nMvav 32,152,240\nD1 1,16,46,0\nMvav 32,148,232\nRa 140,2,19456\nMvav 32,144,224\nAD 85,85,81\nCV 78,80,81\n"
"Cvav 40,160,192,12\nRa 144,2,20480\nMvav 40,164,200\nEX 82,62\nMvav 40,168,208\nRa 148,2,21504\nMvav 40,172,216\nWL 6\nEX 83,63\nCvav 44,172,220,16\nRa 152,2,22528\nAD 85,85,82\n"
"Mvav 44,168,212\nD1 1,16,46,1024\nMvav 44,164,204\nRa 156,2,23552\nMvav 44,160,196\nAD 85,85,83\nCV 79,82,83\nMvav 44,176,228\nRa 160,2,24576\nv_max_f32 v90, v84, v85\n"
"Sv 132,1,16,77,1024\nMvav 44,180,236\ns_add_u32 s88, s88, 32768\nEX 80,32\nMvav 44,184,244\nRa 164,2,25600\nv_cmp_lt_f32 vcc, s63, v90\nMvav 44,188,252\nWL 8\nEX 81,33\n"
"Mvav 40,188,248\nRa 168,2,26624\ns_cbranch_vccnz .T156\n.T157:\nMvav 40,184,240\nD1 1,16,46,2048\nMvav 40,180,232\nRa 172,2,27648\nAD 86,86,84\nMvav 40,176,224\nAD 84,80,81\n"
"CV 64,80,81\nMaav 0,128,72\nRa 176,2,28672\nv_mov_b32 v112, -1\nMaav 4,128,76\nEX 82,34\nMaav 12,132,76\nRa 180,2,29696\nMaav 8,132,72\ns_waitcnt vmcnt(18) lgkmcnt(8)\nEX 83,35\n"
"Maav 16,136,72\nRa 184,2,30720\nAD 84,84,82\nMaav 20,136,76\nD1 1,16,46,3072\ns_add_u32 m0, s89, 49152\nMaav 28,140,76\nRa 188,2,31744\ns_barrier\nMaav 24,140,72\nAD 84,84,83\n"
"CV 65,82,83\nMaav 32,144,72\nRa 128,24,32768\nSv 136,1,16,77,2048\nMaav 36,144,76\ns_add_u32 s46, s87, s88\nEX 80,36\nMaav 44,148,76\nRa 132,24,33792\nMaav 40,148,72\nWL 8\n"
"EX 81,37\nMaav 48,152,72\nRa 136,24,34816\nAD 87,87,85\nMaav 52,152,76\nD1 1,16,46,0\nMaav 60,156,76\nRa 140,24,35840\nMaav 56,156,72\nAD 85,80,81\nCV 68,80,81\nMaav 64,160,72\n"
"Ra 144,26,32768\nv_mov_b32 v113, -1\nMaav 68,160,76\nEX 82,38\nMaav 76,164,76\nRa 148,26,33792\nMaav 72,164,72\nWL 8\nEX 83,39\nMaav 80,168,72\nRa 152,26,34816\nAD 85,85,82\n"
"Maav 84,168,76\nD1 1,16,46,1024\nMaav 92,172,76\nRa 156,26,35840\nMaav 88,172,72\nAD 85,85,83\nCV 69,82,83\nMaav 96,176,72\nRa 160,24,40960\nSv 140,1,16,77,3072\nMaav 100,176,76\n"
"s_xor_b32 s89, s89, 0x10000\nEX 80,40\nMaav 108,180,76\nRa 164,24,41984\nMaav 104,180,72\nWL 6\nEX 81,41\nMaav 112,184,72\nRa 168,24,43008\nAD 84,84,80\nMaav 116,184,76\n"
"D1 1,16,46,2048\nMaav 124,188,76\nRa 172,24,44032\nMaav 120,188,72\nAD 84,84,81\nCV 66,80,81\nCvav 48,128,192,4\nRa 176,26,40960\nv_mov_b32 v114, -1\nMvav 48,132,200\nEX 82,42\n"
"Mvav 48,136,208\nRa 180,26,41984\nMvav 48,140,216\nWL 6\nEX 83,43\nCvav 52,140,220,8\nRa 184,26,43008\nAD 84,84,82\nv_and_b32 v27, 0x70, v1\n"
"buffer_load_dword v27, v27, s[16:19], s61 offen offset:140 sc0 sc1\nMvav 52,136,212\nD1 1,16,46,3072\ns_add_u32 m0, s89, 0\nMvav 52,132,204\nRa 188,26,44032\nMvav 52,128,196\n"
"AD 84,84,83\nCV 67,82,83\nMvav 52,144,228\nRa 128,2,49152\ns_add_u32 s77, s77, s55\nSv 144,1,16,77,0\nMvav 52,148,236\ns_add_u32 s46, s86, s88\nEX 80,44\nMvav 52,152,244\n"
"Ra 132,2,50176\nv_xor_b32 v24, 0x10000, v24\nMvav 52,156,252\nWL 6\nEX 81,45\nMvav 48,156,248\nRa 136,2,51200\nAD 85,85,80\nMvav 48,152,240\nD1 1,16,46,0\nMvav 48,148,232\n"
"Ra 140,2,52224\nMvav 48,144,224\nAD 85,85,81\nCV 70,80,81\nCvav 56,160,192,12\nRa 144,2,53248\nv_mov_b32 v115, -1\nMvav 56,164,200\nEX 82,46\nMvav 56,168,208\nRa 148,2,54272\n"
"v_xor_b32 v26, 0x10000, v26\nMvav 56,172,216\nWL 6\nEX 83,47\nCvav 60,172,220,16\nRa 152,2,55296\nAD 85,85,82\nMvav 60,168,212\nD1 1,16,46,1024\nMvav 60,164,204\nRa 156,2,56320\n"
"Mvav 60,160,196\nAD 85,85,83\nCV 71,82,83\nMvav 60,176,228\nRa 160,2,57344\nv_max_f32 v90, v84, v85\nSv 148,1,16,77,1024\nMvav 60,180,236\ns_add_u32 s88, s88, 32768\nEX 80,48\n"
"Mvav 60,184,244\nRa 164,2,58368\nv_cmp_lt_f32 vcc, s63, v90\nMvav 60,188,252\nWL 8\nEX 81,49\nMvav 56,188,248\nRa 168,2,59392\ns_cbranch_vccnz .T158\n.T159:\nMvav 56,184,240\n"
"D1 1,16,46,2048\nMvav 56,180,232\nRa 172,2,60416\nAD 86,86,84\nMvav 56,176,224\nAD 84,80,81\nCV 72,80,81\nMaav 0,128,64\nRa 176,2,61440\nMaav 4,128,68\nEX 82,50\nMaav 12,132,68\n"
"Ra 180,2,62464\nMaav 8,132,64\ns_waitcnt vmcnt(18) lgkmcnt(8)\nEX 83,51\nMaav 16,136,64\nRa 184,2,63488\nAD 84,84,82\nMaav 20,136,68\nD1 1,16,46,3072\ns_add_u32 m0, s89, 16384\n"
"Maav 28,140,68\nRa 188,2,64512\ns_barrier\nMaav 24,140,64\nAD 84,84,83\nCV 73,82,83\nMaav 32,144,64\nRa 128,24,0\nSv 152,1,16,77,2048\nMaav 36,144,68\ns_add_u32 s46, s87, s88\n"
"EX 80,52\nMaav 44,148,68\nRa 132,24,1024\nv_xor_b32 v2, 0x10000, v2\nMaav 40,148,64\nWL 8\nEX 81,53\nMaav 48,152,64\nRa 136,24,2048\nAD 87,87,85\n"
"buffer_store_dword v25, off, s[16:19], s54 offset:8 sc0 sc1\nMaav 52,152,68\nD1 1,16,46,0\nMaav 60,156,68\nRa 140,24,3072\nMaav 56,156,64\nAD 85,80,81\nCV 76,80,81\n"
"Maav 64,160,64\nRa 144,26,0\nMaav 68,160,68\nEX 82,54\nMaav 76,164,68\nRa 148,26,1024\nMaav 72,164,64\nWL 8\nEX 83,55\nMaav 80,168,64\nRa 152,26,2048\nAD 85,85,82\nMaav 84,168,68\n"
"D1 1,16,46,1024\nMaav 92,172,68\nRa 156,26,3072\nMaav 88,172,64\nAD 85,85,83\nCV 77,82,83\nMaav 96,176,64\nRa 160,24,8192\nSv 156,1,16,77,3072\nMaav 100,176,68\nEX 80,56\n"
"Maav 108,180,68\nRa 164,24,9216\nMaav 104,180,64\nWL 6\nEX 81,57\nMaav 112,184,64\nRa 168,24,10240\nAD 84,84,80\nMaav 116,184,68\nD1 1,16,46,2048\nMaav 124,188,68\n"
"Ra 172,24,11264\nMaav 120,188,64\nAD 84,84,81\nCV 74,80,81\nCvav 32,128,192,4\nRa 176,26,8192\nMvav 32,132,200\nEX 82,58\nMvav 32,136,208\nRa 180,26,9216\nMvav 32,140,216\nWL 6\n"
"EX 83,59\nCvav 36,140,220,8\nRa 184,26,10240\nAD 84,84,82\nMvav 36,136,212\nD1 1,16,46,3072\ns_add_u32 m0, s89, 32768\nMvav 36,132,204\nRa 188,26,11264\nMvav 36,128,196\n"
"AD 84,84,83\nCV 75,82,83\nMvav 36,144,228\nRa 128,2,16384\nSv 160,1,16,78,0\nMvav 36,148,236\ns_add_u32 s46, s86, s88\nEX 80,60\nMvav 36,152,244\nRa 132,2,17408\nMvav 36,156,252\n"
"WL 6\nEX 81,61\nMvav 32,156,248\nRa 136,2,18432\nAD 85,85,80\nMvav 32,152,240\nD1 1,16,46,0\nMvav 32,148,232\nRa 140,2,19456\nSv 112,1,16,49,0\nMvav 32,144,224\nAD 85,85,81\n"
"CV 78,80,81\nCvav 40,160,192,12\nRa 144,2,20480\nMvav 40,164,200\nEX 82,62\nMvav 40,168,208\nRa 148,2,21504\nMvav 40,172,216\nWL 6\nEX 83,63\nCvav 44,172,220,16\nRa 152,2,22528\n"
"AD 85,85,82\nMvav 44,168,212\nD1 1,16,46,1024\nMvav 44,164,204\nRa 156,2,23552\nMvav 44,160,196\nAD 85,85,83\nCV 79,82,83\nMvav 44,176,228\nRa 160,2,24576\n"
"v_max_f32 v90, v84, v85\nSv 164,1,16,78,1024\nMvav 44,180,236\ns_add_u32 s88, s88, 32768\nEX 80,32\nMvav 44,184,244\nRa 164,2,25600\nv_cmp_lt_f32 vcc, s63, v90\nMvav 44,188,252\n"
"WL 8\nEX 81,33\nMvav 40,188,248\nRa 168,2,26624\ns_cbranch_vccnz .T161\n.T162:\nMvav 40,184,240\nD1 1,16,46,2048\nMvav 40,180,232\nRa 172,2,27648\nAD 86,86,84\nMvav 40,176,224\n"
"AD 84,80,81\nCV 64,80,81\nMaav 0,128,72\nRa 176,2,28672\nMaav 4,128,76\nEX 82,34\nMaav 12,132,76\nRa 180,2,29696\nMaav 8,132,72\ns_waitcnt vmcnt(19) lgkmcnt(8)\nEX 83,35\n"
"Maav 16,136,72\nRa 184,2,30720\nAD 84,84,82\nMaav 20,136,76\nD1 1,16,46,3072\ns_add_u32 m0, s89, 49152\nMaav 28,140,76\nRa 188,2,31744\ns_barrier\nMaav 24,140,72\nAD 84,84,83\n"
"CV 65,82,83\nMaav 32,144,72\nRa 128,24,32768\nSv 168,1,16,78,2048\nMaav 36,144,76\ns_add_u32 s46, s87, s88\nEX 80,36\nMaav 44,148,76\nRa 132,24,33792\nMaav 40,148,72\nWL 8\n"
"EX 81,37\nMaav 48,152,72\nRa 136,24,34816\nAD 87,87,85\nMaav 52,152,76\nD1 1,16,46,0\nMaav 60,156,76\nRa 140,24,35840\nMaav 56,156,72\nAD 85,80,81\nCV 68,80,81\nMaav 64,160,72\n"
"Ra 144,26,32768\nMaav 68,160,76\nEX 82,38\nMaav 76,164,76\nRa 148,26,33792\nMaav 72,164,72\nWL 8\nEX 83,39\nMaav 80,168,72\nRa 152,26,34816\nAD 85,85,82\nMaav 84,168,76\n"
"D1 1,16,46,1024\nMaav 92,172,76\nRa 156,26,35840\nMaav 88,172,72\nAD 85,85,83\nCV 69,82,83\nMaav 96,176,72\nRa 160,24,40960\nSv 172,1,16,78,3072\nMaav 100,176,76\n"
"s_xor_b32 s89, s89, 0x10000\nEX 80,40\nMaav 108,180,76\nRa 164,24,41984\nMaav 104,180,72\nWL 6\nEX 81,41\nMaav 112,184,72\nRa 168,24,43008\nAD 84,84,80\nMaav 116,184,76\n"
"D1 1,16,46,2048\nMaav 124,188,76\nRa 172,24,44032\nMaav 120,188,72\nAD 84,84,81\nCV 66,80,81\nCvav 48,128,192,4\nRa 176,26,40960\nMvav 48,132,200\nEX 82,42\nMvav 48,136,208\n"
"Ra 180,26,41984\nMvav 48,140,216\nWL 6\nEX 83,43\nv_cmp_ne_u32 vcc, s15, v27\ns_nop 0\ns_cbranch_vccnz .S172\n.S173:\nv_and_b32 v27, 0x70, v1\n"
"buffer_load_dword v27, v27, s[16:19], s61 offen offset:12 sc0 sc1\nCvav 52,140,220,8\nRa 184,26,43008\nAD 84,84,82\nMvav 52,136,212\nD1 1,16,46,3072\ns_add_u32 m0, s89, 0\n"
"Mvav 52,132,204\nRa 188,26,44032\nMvav 52,128,196\nAD 84,84,83\nCV 67,82,83\nMvav 52,144,228\nRa 128,2,49152\ns_add_u32 s78, s78, s55\nSv 176,1,16,78,0\nMvav 52,148,236\n"
"s_add_u32 s46, s86, s88\nEX 80,44\nMvav 52,152,244\nRa 132,2,50176\nv_xor_b32 v24, 0x10000, v24\nMvav 52,156,252\nWL 6\nEX 81,45\nMvav 48,156,248\nRa 136,2,51200\nAD 85,85,80\n"
"Mvav 48,152,240\nD1 1,16,46,0\nv_lshrrev_b32 v92, 8, v1\nMvav 48,148,232\nRa 140,2,52224\nSv 112,1,16,49,1024\nMvav 48,144,224\nAD 85,85,81\nCV 70,80,81\nCvav 56,160,192,12\n"
"Ra 144,2,53248\nv_bfe_u32 v93, v1, 6, 2\nMvav 56,164,200\ns_add_u32 s49, s49, 131072\nEX 82,46\nMvav 56,168,208\nRa 148,2,54272\nv_xor_b32 v26, 0x10000, v26\nMvav 56,172,216\n"
"WL 6\nEX 83,47\nCvav 60,172,220,16\nRa 152,2,55296\nAD 85,85,82\nMvav 60,168,212\nD1 1,16,46,1024\nv_cmp_eq_u32 vcc, v92, v93\nv_and_b32 v92, 16, v1\nv_mov_b32 v93, 0x3f80\n"
"v_cndmask_b32 v93, 0, v93, vcc\nMvav 60,164,204\nRa 156,2,56320\nMvav 60,160,196\nAD 85,85,83\nCV 71,82,83\nMvav 60,176,228\nRa 160,2,57344\nv_max_f32 v90, v84, v85\n"
"Sv 180,1,16,78,1024\nMvav 60,180,236\ns_add_u32 s88, s88, 32768\nEX 80,48\nMvav 60,184,244\nRa 164,2,58368\nv_cmp_lt_f32 vcc, s63, v90\nMvav 60,188,252\nWL 8\nEX 81,49\n"
"Mvav 56,188,248\nRa 168,2,59392\ns_cbranch_vccnz .T163\n.T164:\nMvav 56,184,240\nD1 1,16,46,2048\nv_lshlrev_b32 v93, v92, v93\nMvav 56,180,232\nRa 172,2,60416\nAD 86,86,84\n"
"Mvav 56,176,224\nAD 84,80,81\nCV 72,80,81\nMaav 0,128,64\nRa 176,2,61440\nv_and_b32 v92, 32, v1\nMaav 4,128,68\nEX 82,50\nMaav 12,132,68\nRa 180,2,62464\nv_cmp_eq_u32 vcc, 0, v92\n"
"v_mov_b32 v128, 0\nv_mov_b32 v129, 0\nv_cndmask_b32 v130, 0, v93, vcc\nMaav 8,132,64\ns_waitcnt vmcnt(20) lgkmcnt(8)\nEX 83,51\nMaav 16,136,64\nRa 184,2,63488\nAD 84,84,82\n"
"Maav 20,136,68\nD1 1,16,46,3072\ns_add_u32 m0, s89, 16384\nMaav 28,140,68\nRa 188,2,64512\ns_barrier\nMaav 24,140,64\nAD 84,84,83\nCV 73,82,83\nMaav 32,144,64\nRa 128,24,0\n"
"Sv 184,1,16,78,2048\nMaav 36,144,68\ns_add_u32 s46, s87, s88\nEX 80,52\nMaav 44,148,68\nRa 132,24,1024\nv_xor_b32 v2, 0x10000, v2\nMaav 40,148,64\nWL 8\nEX 81,53\nMaav 48,152,64\n"
"Ra 136,24,2048\nAD 87,87,85\nMaav 52,152,68\nD1 1,16,46,0\nv_sub_u32 v131, v93, v130\nMaav 60,156,68\nRa 140,24,3072\nMaav 56,156,64\nAD 85,80,81\nCV 76,80,81\nMaav 64,160,64\n"
"Ra 144,26,0\nv_mov_b32 v132, 0\nMaav 68,160,68\nEX 82,54\nMaav 76,164,68\nRa 148,26,1024\nMaav 72,164,64\nWL 8\nEX 83,55\nMaav 80,168,64\nRa 152,26,2048\nAD 85,85,82\n"
"Maav 84,168,68\nD1 1,16,46,1024\nMaav 92,172,68\nRa 156,26,3072\nMaav 88,172,64\nAD 85,85,83\nCV 77,82,83\nMaav 96,176,64\nRa 160,24,8192\nSv 188,1,16,78,3072\nMaav 100,176,68\n"
"EX 80,56\nMaav 108,180,68\nRa 164,24,9216\nv_mov_b32 v133, 0\nMaav 104,180,64\nWL 6\nEX 81,57\nMaav 112,184,64\nRa 168,24,10240\nAD 84,84,80\nMaav 116,184,68\nD1 1,16,46,2048\n"
"Maav 124,188,68\nRa 172,24,11264\nMaav 120,188,64\nAD 84,84,81\nCV 74,80,81\ns_mov_b32 s99, 11\ns_sub_u32 s90, s90, 3\n.p2align 6\n.A188:\nCvav 32,128,192,4\nRa 176,26,8192\n"
"Mvav 32,132,200\nEX 82,58\nMvav 32,136,208\nRa 180,26,9216\nMvav 32,140,216\nWL 6\nEX 83,59\nCvav 36,140,220,8\nRa 184,26,10240\nAD 84,84,82\nMvav 36,136,212\nD1 1,16,46,3072\n"
"s_add_u32 m0, s89, 32768\nMvav 36,132,204\nRa 188,26,11264\nMvav 36,128,196\nAD 84,84,83\nCV 75,82,83\nMvav 36,144,228\nRa 128,2,16384\nMvav 36,148,236\ns_add_u32 s46, s86, s88\n"
"EX 80,60\nMvav 36,152,244\nRa 132,2,17408\nMvav 36,156,252\nWL 6\nEX 81,61\nMvav 32,156,248\nRa 136,2,18432\nAD 85,85,80\nMvav 32,152,240\nD1 1,16,46,0\nMvav 32,148,232\n"
"Ra 140,2,19456\nSv 112,1,16,49,0\nMvav 32,144,224\nAD 85,85,81\nCV 78,80,81\nCvav 40,160,192,12\nRa 144,2,20480\nMvav 40,164,200\nEX 82,62\nMvav 40,168,208\nRa 148,2,21504\n"
"Mvav 40,172,216\nWL 6\nEX 83,63\nCvav 44,172,220,16\nRa 152,2,22528\nAD 85,85,82\nMvav 44,168,212\nD1 1,16,46,1024\nMvav 44,164,204\nRa 156,2,23552\nMvav 44,160,196\nAD 85,85,83\n"
"CV 79,82,83\nMvav 44,176,228\nRa 160,2,24576\nv_max_f32 v90, v84, v85\nMvav 44,180,236\ns_add_u32 s88, s88, 32768\nEX 80,32\nMvav 44,184,244\nRa 164,2,25600\n"
"v_cmp_lt_f32 vcc, s63, v90\nMvav 44,188,252\nWL 8\nEX 81,33\nMvav 40,188,248\nRa 168,2,26624\ns_cbranch_vccnz .T192\n.T193:\nMvav 40,184,240\nD1 1,16,46,2048\nMvav 40,180,232\n"
"Ra 172,2,27648\nAD 86,86,84\nMvav 40,176,224\nAD 84,80,81\nCV 64,80,81\nMaav 0,128,72\nRa 176,2,28672\nMaav 4,128,76\nEX 82,34\nMaav 12,132,76\nRa 180,2,29696\nMaav 8,132,72\n"
"s_waitcnt vmcnt(13) lgkmcnt(8)\nEX 83,35\nMaav 16,136,72\nRa 184,2,30720\nAD 84,84,82\nMaav 20,136,76\nD1 1,16,46,3072\ns_add_u32 m0, s89, 49152\nMaav 28,140,76\nRa 188,2,31744\n"
"s_barrier\nMaav 24,140,72\nAD 84,84,83\nCV 65,82,83\nMaav 32,144,72\nRa 128,24,32768\nMaav 36,144,76\ns_add_u32 s46, s87, s88\nEX 80,36\nMaav 44,148,76\nRa 132,24,33792\n"
"Maav 40,148,72\nWL 8\nEX 81,37\nMaav 48,152,72\nRa 136,24,34816\nAD 87,87,85\nMaav 52,152,76\nD1 1,16,46,0\nMaav 60,156,76\nRa 140,24,35840\nMaav 56,156,72\nAD 85,80,81\n"
"CV 68,80,81\nMaav 64,160,72\nRa 144,26,32768\nMaav 68,160,76\nEX 82,38\nMaav 76,164,76\nRa 148,26,33792\nMaav 72,164,72\nWL 8\nEX 83,39\nMaav 80,168,72\nRa 152,26,34816\n"
"AD 85,85,82\nMaav 84,168,76\nD1 1,16,46,1024\nMaav 92,172,76\nRa 156,26,35840\nMaav 88,172,72\nAD 85,85,83\nCV 69,82,83\nMaav 96,176,72\nRa 160,24,40960\nMaav 100,176,76\n"
"s_xor_b32 s89, s89, 0x10000\nEX 80,40\nMaav 108,180,76\nRa 164,24,41984\nMaav 104,180,72\nWL 6\nEX 81,41\nMaav 112,184,72\nRa 168,24,43008\nAD 84,84,80\nMaav 116,184,76\n"
"D1 1,16,46,2048\nMaav 124,188,76\nRa 172,24,44032\nMaav 120,188,72\nAD 84,84,81\nCV 66,80,81\nCvav 48,128,192,4\nRa 176,26,40960\nMvav 48,132,200\nEX 82,42\nMvav 48,136,208\n"
"Ra 180,26,41984\nMvav 48,140,216\nWL 6\nEX 83,43\nCvav 52,140,220,8\nRa 184,26,43008\nAD 84,84,82\nMvav 52,136,212\nD1 1,16,46,3072\ns_add_u32 m0, s89, 0\nMvav 52,132,204\n"
"Ra 188,26,44032\nMvav 52,128,196\nAD 84,84,83\nCV 67,82,83\nMvav 52,144,228\nRa 128,2,49152\nMvav 52,148,236\ns_add_u32 s46, s86, s88\nEX 80,44\nMvav 52,152,244\nRa 132,2,50176\n"
"v_xor_b32 v24, 0x10000, v24\nMvav 52,156,252\nWL 6\nEX 81,45\nMvav 48,156,248\nRa 136,2,51200\nAD 85,85,80\nMvav 48,152,240\nD1 1,16,46,0\nMvav 48,148,232\nRa 140,2,52224\n"
"Sv 112,1,16,49,1024\nMvav 48,144,224\nAD 85,85,81\nCV 70,80,81\nCvav 56,160,192,12\nRa 144,2,53248\nMvav 56,164,200\ns_add_u32 s49, s49, 131072\nEX 82,46\nMvav 56,168,208\n"
"Ra 148,2,54272\nv_xor_b32 v26, 0x10000, v26\nMvav 56,172,216\nWL 6\nEX 83,47\nCvav 60,172,220,16\nRa 152,2,55296\nAD 85,85,82\nMvav 60,168,212\nD1 1,16,46,1024\nMvav 60,164,204\n"
"Ra 156,2,56320\nMvav 60,160,196\nAD 85,85,83\nCV 71,82,83\nMvav 60,176,228\nRa 160,2,57344\nv_max_f32 v90, v84, v85\nMvav 60,180,236\ns_add_u32 s88, s88, 32768\nEX 80,48\n"
"Mvav 60,184,244\nRa 164,2,58368\nv_cmp_lt_f32 vcc, s63, v90\nMvav 60,188,252\nWL 8\nEX 81,49\nMvav 56,188,248\nRa 168,2,59392\ns_cbranch_vccnz .T194\n.T195:\nMvav 56,184,240\n"
"D1 1,16,46,2048\nMvav 56,180,232\nRa 172,2,60416\nAD 86,86,84\nMvav 56,176,224\nAD 84,80,81\nCV 72,80,81\nMaav 0,128,64\nRa 176,2,61440\nMaav 4,128,68\nEX 82,50\nMaav 12,132,68\n"
"Ra 180,2,62464\nMaav 8,132,64\ns_waitcnt vmcnt(13) lgkmcnt(8)\nEX 83,51\nMaav 16,136,64\nRa 184,2,63488\nAD 84,84,82\nMaav 20,136,68\nD1 1,16,46,3072\ns_add_u32 m0, s89, 16384\n"
"Maav 28,140,68\nRa 188,2,64512\ns_barrier\nMaav 24,140,64\nAD 84,84,83\nCV 73,82,83\nMaav 32,144,64\nRa 128,24,0\nMaav 36,144,68\ns_add_u32 s46, s87, s88\nEX 80,52\n"
"Maav 44,148,68\nRa 132,24,1024\nv_xor_b32 v2, 0x10000, v2\nMaav 40,148,64\nWL 8\nEX 81,53\nMaav 48,152,64\nRa 136,24,2048\nAD 87,87,85\nMaav 52,152,68\nD1 1,16,46,0\n"
"Maav 60,156,68\nRa 140,24,3072\nMaav 56,156,64\nAD 85,80,81\nCV 76,80,81\nMaav 64,160,64\nRa 144,26,0\nMaav 68,160,68\nEX 82,54\nMaav 76,164,68\nRa 148,26,1024\nMaav 72,164,64\n"
"WL 8\nEX 83,55\nMaav 80,168,64\nRa 152,26,2048\nAD 85,85,82\nMaav 84,168,68\nD1 1,16,46,1024\nMaav 92,172,68\nRa 156,26,3072\nMaav 88,172,64\nAD 85,85,83\nCV 77,82,83\n"
"Maav 96,176,64\nRa 160,24,8192\nMaav 100,176,68\ns_sub_u32 s90, s90, 1\nEX 80,56\nMaav 108,180,68\nRa 164,24,9216\ns_cbranch_scc1 .E190\n.E191:\nMaav 104,180,64\nWL 6\nEX 81,57\n"
"Maav 112,184,64\nRa 168,24,10240\nAD 84,84,80\nMaav 116,184,68\nD1 1,16,46,2048\ns_sub_u32 s99, s99, 1\nMaav 124,188,68\nRa 172,24,11264\nMaav 120,188,64\nAD 84,84,81\n"
"CV 74,80,81\ns_cbranch_scc0 .A188\ns_branch .A196\n.E190:\ns_swappc_b64 s[80:81], s[8:9]\ns_branch .E191\n.HD142:\nbuffer_store_dword v25, off, s[16:19], s54 offset:4 sc0 sc1\n"
"v_cmp_ne_u32 vcc, s15, v27\ns_nop 0\ns_cbranch_vccnz .S175\n.S176:\nv_and_b32 v27, 0x70, v1\nbuffer_load_dword v27, v27, s[16:19], s61 offen offset:8 sc0 sc1\ns_mov_b32 s90, 1\n"
"s_swappc_b64 s[8:9], s[80:81]\nv_cmp_ne_u32 vcc, s15, v27\ns_nop 0\ns_cbranch_vccnz .S178\n.S179:\nv_and_b32 v27, 0xf0, v1\n"
"buffer_load_dword v27, v27, s[16:19], s61 offen offset:4 sc0 sc1\ns_mov_b32 s90, 1\ns_swappc_b64 s[8:9], s[80:81]\nv_cmp_ne_u32 vcc, s15, v27\ns_nop 0\ns_cbranch_vccnz .S181\n"
".S182:\ns_mov_b32 s90, s91\ns_swappc_b64 s[8:9], s[80:81]\ns_branch .FF184\n.A196:\nCvav 32,128,192,4\nRa 176,26,8192\nMvav 32,132,200\nEX 82,58\nMvav 32,136,208\nRa 180,26,9216\n"
"Mvav 32,140,216\nWL 6\nEX 83,59\nCvav 36,140,220,8\nRa 184,26,10240\nAD 84,84,82\nMvav 36,136,212\nD1 1,16,46,3072\ns_add_u32 m0, s89, 32768\nMvav 36,132,204\nRa 188,26,11264\n"
"Mvav 36,128,196\nAD 84,84,83\nCV 75,82,83\ns_mov_b32 s94, s96\ns_add_u32 s95, s96, 65536\nv_and_b32 v108, 0xf0, v1\nMvav 36,144,228\nRa 128,2,16384\nMvav 36,148,236\n"
"s_add_u32 s46, s86, s88\nEX 80,60\nMvav 36,152,244\nRa 132,2,17408\nMvav 36,156,252\nWL 6\nEX 81,61\nMvav 32,156,248\nRa 136,2,18432\nAD 85,85,80\nMvav 32,152,240\nD1 1,16,46,0\n"
"Mvav 32,148,232\nRa 140,2,19456\nSv 112,1,16,49,0\nMvav 32,144,224\nAD 85,85,81\nCV 78,80,81\nbuffer_load_dwordx4 v[100:103], v108, s[16:19], s65 offen offset:0 sc0 sc1\n"
"buffer_load_dwordx4 v[104:107], v108, s[16:19], s65 offen offset:256 sc0 sc1\nCvav 40,160,192,12\nRa 144,2,20480\nMvav 40,164,200\nEX 82,62\nMvav 40,168,208\nRa 148,2,21504\n"
"Mvav 40,172,216\nWL 6\nEX 83,63\nCvav 44,172,220,16\nRa 152,2,22528\nAD 85,85,82\nMvav 44,168,212\nD1 1,16,46,1024\nMvav 44,164,204\nRa 156,2,23552\nMvav 44,160,196\nAD 85,85,83\n"
"CV 79,82,83\nMvav 44,176,228\nRa 160,2,24576\nv_max_f32 v90, v84, v85\nMvav 44,180,236\ns_add_u32 s88, s88, 32768\nEX 80,32\nMvav 44,184,244\nRa 164,2,25600\n"
"v_cmp_lt_f32 vcc, s63, v90\nMvav 44,188,252\nWL 8\nEX 81,33\nMvav 40,188,248\nRa 168,2,26624\ns_cbranch_vccnz .T199\n.T200:\nMvav 40,184,240\nD1 1,16,46,2048\nMvav 40,180,232\n"
"Ra 172,2,27648\nAD 86,86,84\nMvav 40,176,224\nAD 84,80,81\nCV 64,80,81\nMaav 0,128,72\nRa 176,2,28672\nMaav 4,128,76\nEX 82,34\nMaav 12,132,76\nRa 180,2,29696\nMaav 8,132,72\n"
"s_waitcnt vmcnt(15) lgkmcnt(8)\nEX 83,35\nMaav 16,136,72\nRa 184,2,30720\nAD 84,84,82\nMaav 20,136,76\nD1 1,16,46,3072\ns_add_u32 m0, s89, 49152\nMaav 28,140,76\nRa 188,2,31744\n"
"s_barrier\nMaav 24,140,72\nAD 84,84,83\nCV 65,82,83\nMaav 32,144,72\nRa 128,24,32768\nMaav 36,144,76\ns_add_u32 s46, s87, s88\nEX 80,36\nMaav 44,148,76\nRa 132,24,33792\n"
"Maav 40,148,72\nWL 8\nEX 81,37\nMaav 48,152,72\nRa 136,24,34816\nAD 87,87,85\nMaav 52,152,76\nD1 1,16,46,0\nMaav 60,156,76\nRa 140,24,35840\nMaav 56,156,72\nAD 85,80,81\n"
"CV 68,80,81\nMaav 64,160,72\nRa 144,26,32768\nMaav 68,160,76\nEX 82,38\nMaav 76,164,76\nRa 148,26,33792\nMaav 72,164,72\nWL 8\nEX 83,39\nMaav 80,168,72\nRa 152,26,34816\n"
"AD 85,85,82\nMaav 84,168,76\nD1 1,16,46,1024\nMaav 92,172,76\nRa 156,26,35840\nMaav 88,172,72\nAD 85,85,83\nCV 69,82,83\nMaav 96,176,72\nRa 160,24,40960\nMaav 100,176,76\n"
"s_xor_b32 s89, s89, 0x10000\nEX 80,40\nMaav 108,180,76\nRa 164,24,41984\nMaav 104,180,72\nWL 6\nEX 81,41\nMaav 112,184,72\nRa 168,24,43008\nAD 84,84,80\nMaav 116,184,76\n"
"D1 1,16,46,2048\nMaav 124,188,76\nRa 172,24,44032\nMaav 120,188,72\nAD 84,84,81\nCV 66,80,81\nCvav 48,128,192,4\nRa 176,26,40960\nMvav 48,132,200\ns_mov_b32 s86, s94\nEX 82,42\n"
"Mvav 48,136,208\nRa 180,26,41984\nMvav 48,140,216\nWL 6\nEX 83,43\nCvav 52,140,220,8\nRa 184,26,43008\nAD 84,84,82\nMvav 52,136,212\nD1 1,16,46,3072\ns_add_u32 m0, s89, 0\n"
"Mvav 52,132,204\nRa 188,26,44032\nMvav 52,128,196\nAD 84,84,83\nCV 67,82,83\nMvav 52,144,228\nRa 128,2,49152\nMvav 52,148,236\ns_add_u32 s46, s86, s88\nEX 80,44\nMvav 52,152,244\n"
"Ra 132,2,50176\nv_xor_b32 v24, 0x10000, v24\nMvav 52,156,252\nWL 6\nEX 81,45\nMvav 48,156,248\nRa 136,2,51200\nAD 85,85,80\nMvav 48,152,240\nD1 1,16,46,0\nMvav 48,148,232\n"
"Ra 140,2,52224\nSv 112,1,16,49,1024\nMvav 48,144,224\nAD 85,85,81\nCV 70,80,81\nWV 9\nv_xor_b32 v109, s15, v102\nv_xor_b32 v110, s15, v106\nv_or_b32 v109, v109, v110\n"
"v_cmp_ne_u32 vcc, 0, v109\ns_nop 3\ns_cmp_lg_u64 vcc, 0\ns_cselect_b32 s67, 1, 0\ns_cselect_b32 s6, 0x40000000, s66\nCvav 56,160,192,12\nRa 144,2,53248\ns_add_u32 s5, s6, 0\n"
"Mvav 56,164,200\ns_add_u32 s49, s49, 131072\nEX 82,46\nMvav 56,168,208\nRa 148,2,54272\nv_xor_b32 v26, 0x10000, v26\n"
"buffer_load_dwordx4 a[192:195], v1, s[16:19], s5 offen offset:0 sc1\nMvav 56,172,216\nWL 6\nEX 83,47\nCvav 60,172,220,16\nRa 152,2,55296\nAD 85,85,82\nMvav 60,168,212\n"
"D1 1,16,46,1024\nMvav 60,164,204\nRa 156,2,56320\nMvav 60,160,196\nAD 85,85,83\nCV 71,82,83\nMvav 60,176,228\nRa 160,2,57344\nv_max_f32 v90, v84, v85\nMvav 60,180,236\n"
"s_add_u32 s88, s88, 32768\nEX 80,48\nMvav 60,184,244\nRa 164,2,58368\nv_cmp_lt_f32 vcc, s63, v90\nMvav 60,188,252\nWL 8\nEX 81,49\nMvav 56,188,248\nRa 168,2,59392\n"
"s_cbranch_vccnz .T201\n.T202:\nbuffer_load_dwordx4 a[196:199], v1, s[16:19], s5 offen offset:1024 sc1\nMvav 56,184,240\nD1 1,16,46,2048\nMvav 56,180,232\nRa 172,2,60416\n"
"AD 86,86,84\nMvav 56,176,224\nAD 84,80,81\nCV 72,80,81\nMaav 0,128,64\nRa 176,2,61440\nMaav 4,128,68\nEX 82,50\nMaav 12,132,68\nRa 180,2,62464\nMaav 8,132,64\nWL 8\nEX 83,51\n"
"Maav 16,136,64\nRa 184,2,63488\nAD 84,84,82\nbuffer_load_dwordx4 a[200:203], v1, s[16:19], s5 offen offset:2048 sc1\nMaav 20,136,68\nD1 1,16,46,3072\ns_add_u32 m0, s89, 16384\n"
"Maav 28,140,68\nRa 188,2,64512\ns_barrier\nMaav 24,140,64\nAD 84,84,83\nCV 73,82,83\nMaav 32,144,64\nRa 128,24,0\nMaav 36,144,68\ns_add_u32 s46, s87, s88\nEX 80,52\n"
"Maav 44,148,68\nRa 132,24,1024\nv_xor_b32 v2, 0x10000, v2\nMaav 40,148,64\nWL 8\nEX 81,53\nMaav 48,152,64\nRa 136,24,2048\nAD 87,87,85\n"
"buffer_load_dwordx4 a[204:207], v1, s[16:19], s5 offen offset:3072 sc1\nMaav 52,152,68\nD1 1,16,46,0\nMaav 60,156,68\nRa 140,24,3072\nMaav 56,156,64\nAD 85,80,81\nCV 76,80,81\n"
"Maav 64,160,64\nRa 144,26,0\nMaav 68,160,68\ns_cmp_lg_u32 s92, 0\nEX 82,54\nMaav 76,164,68\nRa 148,26,1024\ns_cbranch_scc0 .E197\nv_cmpx_gt_i32 vcc, 19, v3\nv_mov_b32 v15, s97\n"
"v_cmpx_gt_i32 vcc, 18, v3\nv_mov_b32 v14, s97\nv_cmpx_gt_i32 vcc, 17, v3\nv_mov_b32 v13, s97\nv_cmpx_gt_i32 vcc, 16, v3\nv_mov_b32 v12, s97\nv_cmpx_gt_i32 vcc, 3, v3\n"
"v_mov_b32 v7, s97\nv_cmpx_gt_i32 vcc, 2, v3\nv_mov_b32 v6, s97\nv_cmpx_gt_i32 vcc, 1, v3\nv_mov_b32 v5, s97\nv_cmpx_gt_i32 vcc, 0, v3\nv_mov_b32 v4, s97\ns_mov_b64 exec, -1\n"
"s_nop 2\n.E197:\ns_add_u32 s5, s6, 4096\nMaav 72,164,64\nWL 8\nEX 83,55\nMaav 80,168,64\nRa 152,26,2048\nAD 85,85,82\n"
"buffer_load_dwordx4 a[208:211], v1, s[16:19], s5 offen offset:0 sc1\nMaav 84,168,68\nD1 1,16,46,1024\nMaav 92,172,68\nRa 156,26,3072\nMaav 88,172,64\nAD 85,85,83\nCV 77,82,83\n"
"Maav 96,176,64\nRa 160,24,8192\nMaav 100,176,68\nEX 80,56\nMaav 108,180,68\nRa 164,24,9216\nMaav 104,180,64\nWL 6\nEX 81,57\nMaav 112,184,64\nRa 168,24,10240\nAD 84,84,80\n"
"buffer_load_dwordx4 a[212:215], v1, s[16:19], s5 offen offset:1024 sc1\nMaav 116,184,68\nD1 1,16,46,2048\nMaav 124,188,68\nRa 172,24,11264\nMaav 120,188,64\nAD 84,84,81\n"
"CV 74,80,81\nCvav 32,128,192,4\nRa 176,26,8192\nMvav 32,132,200\nEX 82,58\nMvav 32,136,208\nRa 180,26,9216\nMvav 32,140,216\nWL 6\nEX 83,59\nCvav 36,140,220,8\nRa 184,26,10240\n"
"AD 84,84,82\nbuffer_load_dwordx4 a[216:219], v1, s[16:19], s5 offen offset:2048 sc1\nMvav 36,136,212\nD1 1,16,46,3072\ns_add_u32 m0, s89, 32768\nMvav 36,132,204\nRa 188,26,11264\n"
"Mvav 36,128,196\nAD 84,84,83\nCV 75,82,83\nMvav 36,144,228\nRa 128,2,16384\nMvav 36,148,236\ns_add_u32 s46, s86, s88\nEX 80,60\nMvav 36,152,244\nRa 132,2,17408\nMvav 36,156,252\n"
"WL 6\nEX 81,61\nMvav 32,156,248\nRa 136,2,18432\nAD 85,85,80\nbuffer_load_dwordx4 a[220:223], v1, s[16:19], s5 offen offset:3072 sc1\nMvav 32,152,240\nD1 1,16,46,0\n"
"Mvav 32,148,232\nRa 140,2,19456\nMvav 32,144,224\nAD 85,85,81\nCV 78,80,81\nCvav 40,160,192,12\nRa 144,2,20480\nMvav 40,164,200\ns_mov_b32 s87, s95\nEX 82,62\nMvav 40,168,208\n"
"Ra 148,2,21504\ns_add_u32 s5, s6, 16384\nMvav 40,172,216\nWL 6\nEX 83,63\nCvav 44,172,220,16\nRa 152,2,22528\nAD 85,85,82\n"
"buffer_load_dwordx4 a[224:227], v1, s[16:19], s5 offen offset:0 sc1\nMvav 44,168,212\nD1 1,16,46,1024\nMvav 44,164,204\nRa 156,2,23552\nMvav 44,160,196\nAD 85,85,83\nCV 79,82,83\n"
"Mvav 44,176,228\nRa 160,2,24576\nv_max_f32 v90, v84, v85\nMvav 44,180,236\ns_add_u32 s88, s88, 32768\nEX 80,32\nMvav 44,184,244\nRa 164,2,25600\nv_cmp_lt_f32 vcc, s63, v90\n"
"Mvav 44,188,252\nWL 8\nEX 81,33\nMvav 40,188,248\nRa 168,2,26624\ns_cbranch_vccnz .T205\n.T206:\nbuffer_load_dwordx4 a[228:231], v1, s[16:19], s5 offen offset:1024 sc1\n"
"Mvav 40,184,240\nD1 1,16,46,2048\nMvav 40,180,232\nRa 172,2,27648\nAD 86,86,84\nMvav 40,176,224\nAD 84,80,81\nCV 64,80,81\nMaav 0,128,72\nRa 176,2,28672\nMaav 4,128,76\nEX 82,34\n"
"Maav 12,132,76\nRa 180,2,29696\nMaav 8,132,72\ns_waitcnt vmcnt(22) lgkmcnt(8)\nEX 83,35\nMaav 16,136,72\nRa 184,2,30720\nAD 84,84,82\n"
"buffer_load_dwordx4 a[232:235], v1, s[16:19], s5 offen offset:2048 sc1\nMaav 20,136,76\nD1 1,16,46,3072\ns_add_u32 m0, s89, 49152\nMaav 28,140,76\nRa 188,2,31744\ns_barrier\n"
"Maav 24,140,72\nAD 84,84,83\nCV 65,82,83\nMaav 32,144,72\nRa 128,24,32768\nMaav 36,144,76\ns_add_u32 s46, s87, s88\nEX 80,36\nMaav 44,148,76\nRa 132,24,33792\nMaav 40,148,72\n"
"WL 8\nEX 81,37\nMaav 48,152,72\nRa 136,24,34816\nAD 87,87,85\nbuffer_load_dwordx4 a[236:239], v1, s[16:19], s5 offen offset:3072 sc1\nMaav 52,152,76\nD1 1,16,46,0\nMaav 60,156,76\n"
"Ra 140,24,35840\nMaav 56,156,72\nAD 85,80,81\nCV 68,80,81\nMaav 64,160,72\nRa 144,26,32768\nMaav 68,160,76\ns_cmp_eq_u32 s13, 7\nEX 82,38\nMaav 76,164,76\nRa 148,26,33792\n"
"s_cbranch_scc0 .E203\nv_cmpx_gt_i32 vcc, 19, v0\nv_mov_b32 v15, s97\nv_cmpx_gt_i32 vcc, 18, v0\nv_mov_b32 v14, s97\nv_cmpx_gt_i32 vcc, 17, v0\nv_mov_b32 v13, s97\n"
"v_cmpx_gt_i32 vcc, 16, v0\nv_mov_b32 v12, s97\nv_cmpx_gt_i32 vcc, 3, v0\nv_mov_b32 v7, s97\nv_cmpx_gt_i32 vcc, 2, v0\nv_mov_b32 v6, s97\nv_cmpx_gt_i32 vcc, 1, v0\n"
"v_mov_b32 v5, s97\nv_cmpx_gt_i32 vcc, 0, v0\nv_mov_b32 v4, s97\ns_mov_b64 exec, -1\ns_nop 2\n.E203:\ns_add_u32 s5, s6, 20480\nMaav 72,164,72\nWL 8\nEX 83,39\nMaav 80,168,72\n"
"Ra 152,26,34816\nAD 85,85,82\nbuffer_load_dwordx4 a[240:243], v1, s[16:19], s5 offen offset:0 sc1\nMaav 84,168,76\nD1 1,16,46,1024\nMaav 92,172,76\nRa 156,26,35840\n"
"Maav 88,172,72\nAD 85,85,83\nCV 69,82,83\nMaav 96,176,72\nRa 160,24,40960\nMaav 100,176,76\ns_xor_b32 s89, s89, 0x10000\nEX 80,40\nMaav 108,180,76\nRa 164,24,41984\n"
"Maav 104,180,72\nWL 6\nEX 81,41\nMaav 112,184,72\nRa 168,24,43008\nAD 84,84,80\nbuffer_load_dwordx4 a[244:247], v1, s[16:19], s5 offen offset:1024 sc1\nMaav 116,184,76\n"
"D1 1,16,46,2048\nMaav 124,188,76\nRa 172,24,44032\nMaav 120,188,72\nAD 84,84,81\nCV 66,80,81\nCvav 48,128,192,4\nRa 176,26,40960\nMvav 48,132,200\nEX 82,42\nMvav 48,136,208\n"
"Ra 180,26,41984\nMvav 48,140,216\nWL 6\nEX 83,43\nCvav 52,140,220,8\nRa 184,26,43008\nAD 84,84,82\nbuffer_load_dwordx4 a[248:251], v1, s[16:19], s5 offen offset:2048 sc1\n"
"Mvav 52,136,212\nD1 1,16,46,3072\ns_add_u32 m0, s89, 0\nMvav 52,132,204\nRa 188,26,44032\nMvav 52,128,196\nAD 84,84,83\nCV 67,82,83\nMvav 52,144,228\nRa 128,2,49152\n"
"Mvav 52,148,236\ns_add_u32 s46, s86, s88\nEX 80,44\nMvav 52,152,244\nRa 132,2,50176\nv_xor_b32 v24, 0x10000, v24\nMvav 52,156,252\nWL 6\nEX 81,45\nMvav 48,156,248\nRa 136,2,51200\n"
"AD 85,85,80\nbuffer_load_dwordx4 a[252:255], v1, s[16:19], s5 offen offset:3072 sc1\nMvav 48,152,240\nD1 1,16,46,0\nMvav 48,148,232\nRa 140,2,52224\nMvav 48,144,224\nAD 85,85,81\n"
"CV 70,80,81\nCvav 56,160,192,12\nRa 144,2,53248\nMvav 56,164,200\nEX 82,46\nMvav 56,168,208\nRa 148,2,54272\nv_xor_b32 v26, 0x10000, v26\nMvav 56,172,216\nWL 6\nEX 83,47\n"
"Cvav 60,172,220,16\nRa 152,2,55296\nAD 85,85,82\nMvav 60,168,212\nD1 1,16,46,1024\nMvav 60,164,204\nRa 156,2,56320\nMvav 60,160,196\nAD 85,85,83\nCV 71,82,83\nMvav 60,176,228\n"
"Ra 160,2,57344\nv_max_f32 v90, v84, v85\nMvav 60,180,236\ns_add_u32 s88, s88, 32768\nEX 80,48\nMvav 60,184,244\nRa 164,2,58368\nv_cmp_lt_f32 vcc, s63, v90\nMvav 60,188,252\nWL 8\n"
"EX 81,49\nMvav 56,188,248\nRa 168,2,59392\ns_cbranch_vccnz .T207\n.T208:\nMvav 56,184,240\nD1 1,16,46,2048\nMvav 56,180,232\nRa 172,2,60416\nAD 86,86,84\nMvav 56,176,224\n"
"AD 84,80,81\nCV 72,80,81\nMaav 0,128,64\nRa 176,2,61440\nbuffer_load_dwordx4 v[100:103], v108, s[16:19], s65 offen offset:0 sc0 sc1\n"
"buffer_load_dwordx4 v[104:107], v108, s[16:19], s65 offen offset:256 sc0 sc1\nMaav 4,128,68\nEX 82,50\nMaav 12,132,68\nRa 180,2,62464\nMaav 8,132,64\n"
"s_waitcnt vmcnt(22) lgkmcnt(8)\nEX 83,51\nMaav 16,136,64\nRa 184,2,63488\nAD 84,84,82\nMaav 20,136,68\nD1 1,16,46,3072\ns_add_u32 m0, s89, 16384\nMaav 28,140,68\nRa 188,2,64512\n"
"s_barrier\nMaav 24,140,64\nAD 84,84,83\nCV 73,82,83\nMaav 32,144,64\nRa 128,24,0\nMaav 36,144,68\ns_add_u32 s46, s87, s88\nEX 80,52\nMaav 44,148,68\nRa 132,24,1024\n"
"v_xor_b32 v2, 0x10000, v2\nMaav 40,148,64\nWL 8\nEX 81,53\nMaav 48,152,64\nRa 136,24,2048\nAD 87,87,85\nMaav 52,152,68\nD1 1,16,46,0\nMaav 60,156,68\nRa 140,24,3072\n"
"Maav 56,156,64\nAD 85,80,81\nCV 76,80,81\nMaav 64,160,64\nRa 144,26,0\nMaav 68,160,68\nEX 82,54\nMaav 76,164,68\nRa 148,26,1024\nMaav 72,164,64\nWL 8\nEX 83,55\nMaav 80,168,64\n"
"Ra 152,26,2048\nAD 85,85,82\nMaav 84,168,68\nD1 1,16,46,1024\nMaav 92,172,68\nRa 156,26,3072\nMaav 88,172,64\nAD 85,85,83\nCV 77,82,83\nMaav 96,176,64\nRa 160,24,8192\n"
"Maav 100,176,68\nEX 80,56\nMaav 108,180,68\nRa 164,24,9216\nMaav 104,180,64\nWL 10\nEX 81,57\nMaav 112,184,64\nRa 168,24,10240\nAD 84,84,80\nMaav 116,184,68\nD1 1,16,46,2048\n"
"Maav 124,188,68\nRa 172,24,11264\nMaav 120,188,64\nAD 84,84,81\nCV 74,80,81\nWV 4\ns_cmp_lg_u32 s67, 0\ns_cbranch_scc0 .E216\nv_xor_b32 v109, s15, v102\nv_xor_b32 v110, s15, v106\n"
"v_or_b32 v109, v109, v110\nv_cmp_ne_u32 vcc, 0, v109\ns_nop 0\ns_cbranch_vccnz .S213\n.S214:\ns_add_u32 s5, s66, 0\n"
"buffer_load_dwordx4 a[192:195], v1, s[16:19], s5 offen offset:0 sc1\nbuffer_load_dwordx4 a[196:199], v1, s[16:19], s5 offen offset:1024 sc1\n"
"buffer_load_dwordx4 a[200:203], v1, s[16:19], s5 offen offset:2048 sc1\nbuffer_load_dwordx4 a[204:207], v1, s[16:19], s5 offen offset:3072 sc1\ns_add_u32 s5, s66, 4096\n"
"buffer_load_dwordx4 a[208:211], v1, s[16:19], s5 offen offset:0 sc1\nbuffer_load_dwordx4 a[212:215], v1, s[16:19], s5 offen offset:1024 sc1\n"
"buffer_load_dwordx4 a[216:219], v1, s[16:19], s5 offen offset:2048 sc1\nbuffer_load_dwordx4 a[220:223], v1, s[16:19], s5 offen offset:3072 sc1\ns_add_u32 s5, s66, 16384\n"
"buffer_load_dwordx4 a[224:227], v1, s[16:19], s5 offen offset:0 sc1\nbuffer_load_dwordx4 a[228:231], v1, s[16:19], s5 offen offset:1024 sc1\n"
"buffer_load_dwordx4 a[232:235], v1, s[16:19], s5 offen offset:2048 sc1\nbuffer_load_dwordx4 a[236:239], v1, s[16:19], s5 offen offset:3072 sc1\ns_add_u32 s5, s66, 20480\n"
"buffer_load_dwordx4 a[240:243], v1, s[16:19], s5 offen offset:0 sc1\nbuffer_load_dwordx4 a[244:247], v1, s[16:19], s5 offen offset:1024 sc1\n"
"buffer_load_dwordx4 a[248:251], v1, s[16:19], s5 offen offset:2048 sc1\nbuffer_load_dwordx4 a[252:255], v1, s[16:19], s5 offen offset:3072 sc1\nWV 0\ns_mov_b32 s67, 0\n.E216:\n"
"v_cmp_lt_f32 vcc, 0, v101\nSU 20,100,88\nv_mul_f32 v101, 0x3e800000, v101\nv_cndmask_b32 v20, 0, v20, vcc\nv_cmp_lt_f32 vcc, 0, v105\nSU 21,104,89\n"
"v_mul_f32 v105, 0x3e800000, v105\nv_cndmask_b32 v21, 0, v21, vcc\nv_or_b32 v91, v20, v21\nv_cmp_ne_u32 vcc, 0, v91\ns_nop 0\ns_cbranch_vccnz .D211\nAD 86,86,101\nAD 87,87,105\n"
".D212:\nMava 0,130,192\nMava 8,128,192\nEX 82,58\nMava 12,128,196\nMava 4,130,196\nEX 83,59\nMava 16,130,200\nAD 84,84,82\nMava 24,128,200\nD1 1,16,46,3072\nMava 28,128,204\n"
"s_add_u32 m0, s89, 32768\ns_add_u32 s46, s86, s88\nMava 20,130,204\nAD 84,84,83\nCV 75,82,83\nMava 32,130,208\nRa 128,2,16384\nv_xor_b32 v3, 0x10000, v2\n"
"v_add_u32 v0, 0x20000, v1\nMava 40,128,208\nEX 80,60\nMava 44,128,212\nRa 132,2,17408\ns_sub_u32 s94, s49, 1835008\nMava 36,130,212\nEX 81,61\nMava 48,130,216\nRa 136,2,18432\n"
"AD 85,85,80\ns_xor_b32 s92, s94, 0x202000\nMava 56,128,216\nD1 1,16,46,0\ns_lshl_b32 s89, s11, 12\nMava 60,128,220\nRa 140,2,19456\ns_lshl1_add_u32 s2, s12, 0\nMava 52,130,220\n"
"AD 85,85,81\nCV 78,80,81\nMava 64,130,224\nRa 144,2,20480\ns_lshl3_add_u32 s2, s2, s13\ns_lshl4_add_u32 s2, s2, 0xc048200\nMava 72,128,224\nEX 82,62\nMava 76,128,228\n"
"Ra 148,2,21504\ns_and_b32 s3, s72, 0x2000\nMava 68,130,228\nEX 83,63\nMava 80,130,232\nRa 152,2,22528\nAD 85,85,82\nMava 88,128,232\nD1 1,16,46,1024\ns_cmp_eq_u32 s11, 1\n"
"s_cselect_b32 s54, s2, s54\ns_cselect_b32 s3, s3, 0\nMava 92,128,236\nRa 156,2,23552\nv_xor_b32 v25, s3, v25\nMava 84,130,236\nAD 85,85,83\nCV 79,82,83\nMava 96,130,240\n"
"Ra 160,2,24576\nv_max_f32 v90, v84, v85\nMava 104,128,240\nv_cmp_lt_f32 vcc, s63, v90\nMava 108,128,244\nRa 164,2,25600\nMava 100,130,244\nWL 8\nMava 112,130,248\nRa 168,2,26624\n"
"Mava 120,128,248\nD1 1,16,46,2048\nMava 124,128,252\nRa 172,2,27648\nMava 116,130,252\ns_cbranch_vccnz .T217\n.T218:\nAD 86,86,84\nAD 87,87,85\nMaav 0,128,72\nRa 176,2,28672\n"
"v_mov_b32 v84, v86\nv_mov_b32 v85, v87\nMaav 4,128,76\nv_permlane32_swap_b32 v86, v84\nv_permlane32_swap_b32 v87, v85\nMaav 12,132,76\nRa 180,2,29696\nAD 86,86,84\nMaav 8,132,72\n"
"WL 8\nAD 87,87,85\nMaav 16,136,72\nRa 184,2,30720\nv_mov_b32 v84, v86\nv_mov_b32 v85, v87\nMaav 20,136,76\nD1 1,16,46,3072\nv_permlane16_swap_b32 v86, v84\nMaav 28,140,76\n"
"Ra 188,2,31744\nv_permlane16_swap_b32 v87, v85\nMaav 24,140,72\nAD 86,86,84\nAD 87,87,85\nMaav 32,144,72\nRa 128,2,0\nv_rcp_f32 v86, v86\nMaav 36,144,76\nv_rcp_f32 v87, v87\n"
"Maav 44,148,76\nRa 132,2,1024\nAR 82,0\nAR 83,1\nMU 82,82,86\nMU 83,83,86\nMaav 40,148,72\nWL 8\ns_add_u32 m0, s89, 131072\ns_add_u32 s46, s96, 1212416\nCV 96,82,83\nAR 82,2\n"
"Maav 48,152,72\nRa 136,2,2048\nAR 83,3\nMU 82,82,86\nMU 83,83,86\nCV 97,82,83\nMaav 52,152,76\nD1 1,16,46,0\nAR 82,4\nAR 83,5\nMU 82,82,87\nMaav 60,156,76\nRa 140,2,3072\n"
"MU 83,83,87\nCV 100,82,83\nAR 82,6\nAR 83,7\nMaav 56,156,72\nMU 82,82,87\nMU 83,83,87\nCV 101,82,83\nAR 82,8\nAR 83,9\nMaav 64,160,72\nRa 144,2,4096\nMU 82,82,86\nMU 83,83,86\n"
"CV 98,82,83\nAR 82,10\nMaav 68,160,76\nAR 83,11\nMU 82,82,86\nMU 83,83,86\nCV 99,82,83\nMaav 76,164,76\nRa 148,2,5120\nAR 82,12\nAR 83,13\nMU 82,82,87\nMU 83,83,87\n"
"Maav 72,164,72\nWL 8\nCV 102,82,83\nAR 82,14\nAR 83,15\nMU 82,82,87\nMaav 80,168,72\nRa 152,2,6144\nMU 83,83,87\nCV 103,82,83\nAR 82,16\nAR 83,17\nMaav 84,168,76\nD1 1,16,46,1024\n"
"MU 82,82,86\nMU 83,83,86\nCV 104,82,83\nMaav 92,172,76\nRa 156,2,7168\nAR 82,18\nAR 83,19\nMU 82,82,86\nMU 83,83,86\nMaav 88,172,72\nCV 105,82,83\nAR 82,20\nAR 83,21\nMU 82,82,87\n"
"MU 83,83,87\nMaav 96,176,72\nRa 160,2,8192\nCV 108,82,83\nAR 82,22\nAR 83,23\nMU 82,82,87\nMaav 100,176,76\nMU 83,83,87\nCV 109,82,83\nAR 82,24\nAR 83,25\nMaav 108,180,76\n"
"Ra 164,2,9216\nMU 82,82,86\nMU 83,83,86\nCV 106,82,83\nAR 82,26\nMaav 104,180,72\nWL 8\nAR 83,27\nMU 82,82,86\nMU 83,83,86\nCV 107,82,83\nMaav 112,184,72\nRa 168,2,10240\n"
"AR 82,28\nAR 83,29\nMU 82,82,87\nMU 83,83,87\nMaav 116,184,76\nD1 1,16,46,2048\nCV 110,82,83\nAR 82,30\nAR 83,31\nMaav 124,188,76\nRa 172,2,11264\nMU 82,82,87\nMU 83,83,87\n"
"CV 111,82,83\nMaav 120,188,72\nAR 82,32\nAR 83,33\nMU 82,82,86\nMU 83,83,86\nZvav 128,128,96\nRa 176,2,12288\nCV 112,82,83\nAR 82,34\nAR 83,35\nMU 82,82,86\nMvav 128,132,104\n"
"MU 83,83,86\nCV 113,82,83\nAR 82,36\nAR 83,37\nZvav 132,132,108\nRa 180,2,13312\nMU 82,82,87\nMU 83,83,87\nCV 116,82,83\nMvav 132,128,100\nWL 8\nAR 82,38\nAR 83,39\nMU 82,82,87\n"
"Zvav 140,136,100\nRa 184,2,14336\nMU 83,83,87\nCV 117,82,83\nAR 82,40\nMvav 140,140,108\nD1 1,16,46,3072\nAR 83,41\nMU 82,82,86\nMU 83,83,86\nZvav 136,140,104\nRa 188,2,15360\n"
"s_barrier\nCV 114,82,83\nAR 82,42\nAR 83,43\nMvav 136,136,96\nMU 82,82,86\nMU 83,83,86\nCV 115,82,83\nAR 82,44\nZvav 144,144,96\nRa 128,2,32768\nAR 83,45\nMU 82,82,87\n"
"MU 83,83,87\nMvav 144,148,104\nCV 118,82,83\nAR 82,46\nAR 83,47\nMU 82,82,87\nbuffer_store_dword v25, off, s[16:19], s54 offset:512 sc0 sc1\nZvav 148,148,108\nRa 132,2,33792\n"
"MU 83,83,87\nCV 119,82,83\nAR 82,48\nMvav 148,144,100\nWL 8\ns_add_u32 m0, s89, 114688\ns_add_u32 s46, s96, 1179648\nAR 83,49\nMU 82,82,86\nZvav 156,152,100\nRa 136,2,34816\n"
"MU 83,83,86\nCV 120,82,83\nAR 82,50\nMvav 156,156,108\nD1 1,16,46,0\nAR 83,51\nMU 82,82,86\nMU 83,83,86\nZvav 152,156,104\nRa 140,2,35840\nCV 121,82,83\nAR 82,52\nAR 83,53\n"
"Mvav 152,152,96\nMU 82,82,87\nMU 83,83,87\nCV 124,82,83\nAR 82,54\nZvav 64,160,96\nRa 144,2,36864\nAR 83,55\nMU 82,82,87\nMU 83,83,87\nCV 125,82,83\nMvav 64,164,104\nAR 82,56\n"
"AR 83,57\nMU 82,82,86\nMU 83,83,86\nZvav 68,164,108\nRa 148,2,37888\nCV 122,82,83\nAR 82,58\nAR 83,59\nMvav 68,160,100\nWL 8\nMU 82,82,86\nMU 83,83,86\nCV 123,82,83\n"
"Zvav 76,168,100\nRa 152,2,38912\nAR 82,60\nAR 83,61\nMU 82,82,87\nMvav 76,172,108\nD1 1,16,46,1024\nMU 83,83,87\nCV 126,82,83\nAR 82,62\nZvav 72,172,104\nRa 156,2,39936\nAR 83,63\n"
"MU 82,82,87\nMU 83,83,87\nCV 127,82,83\nMvav 72,168,96\nAR 82,64\nAR 83,65\nMU 82,82,86\nMU 83,83,86\nMvav 128,176,112\nRa 160,2,40960\nCV 32,82,83\nAR 82,66\nAR 83,67\n"
"Mvav 128,180,120\nMU 82,82,86\nMU 83,83,86\nCV 33,82,83\nAR 82,68\nMvav 132,180,124\nRa 164,2,41984\nAR 83,69\nMU 82,82,87\nMU 83,83,87\nMvav 132,176,116\nWL 8\nCV 36,82,83\n"
"AR 82,70\nAR 83,71\nMU 82,82,87\nMvav 140,184,116\nRa 168,2,43008\nMU 83,83,87\nCV 37,82,83\nAR 82,72\nMvav 140,188,124\nD1 1,16,46,2048\nAR 83,73\nMU 82,82,86\nMU 83,83,86\n"
"Mvav 136,188,120\nRa 172,2,44032\nCV 34,82,83\nAR 82,74\nAR 83,75\nMvav 136,184,112\nMU 82,82,86\nMU 83,83,86\nCV 35,82,83\nAR 82,76\nMvav 144,128,112\nRa 176,2,45056\nAR 83,77\n"
"MU 82,82,87\nMU 83,83,87\nCV 38,82,83\nMvav 144,132,120\nAR 82,78\nAR 83,79\nMU 82,82,87\nMU 83,83,87\nMvav 148,132,124\nRa 180,2,46080\nCV 39,82,83\nAR 82,80\nAR 83,81\n"
"Mvav 148,128,116\nWL 8\nMU 82,82,86\nMU 83,83,86\nCV 40,82,83\nMvav 156,136,116\nRa 184,2,47104\nAR 82,82\nAR 83,83\nMU 82,82,86\nMvav 156,140,124\nD1 1,16,46,3072\nMU 83,83,86\n"
"CV 41,82,83\nAR 82,84\nMvav 152,140,120\nRa 188,2,48128\nAR 83,85\nMU 82,82,87\nMU 83,83,87\nCV 44,82,83\nMvav 152,136,112\nAR 82,86\nAR 83,87\nMU 82,82,87\nMU 83,83,87\n"
"Mvav 64,144,112\nRa 128,2,49152\nCV 45,82,83\nAR 82,88\nAR 83,89\nMvav 64,148,120\nMU 82,82,86\nMU 83,83,86\nCV 42,82,83\nAR 82,90\nMvav 68,148,124\nRa 132,2,50176\nAR 83,91\n"
"MU 82,82,86\nMU 83,83,86\nMvav 68,144,116\nWL 8\ns_add_u32 m0, s89, 147456\ns_add_u32 s46, s96, 1245184\nCV 43,82,83\nAR 82,92\nMvav 76,152,116\nRa 136,2,51200\nAR 83,93\n"
"MU 82,82,87\nMU 83,83,87\nMvav 76,156,124\nD1 1,16,46,0\nCV 46,82,83\nAR 82,94\nAR 83,95\nMvav 72,156,120\nRa 140,2,52224\nMU 82,82,87\nMU 83,83,87\nCV 47,82,83\nMvav 72,152,112\n"
"AR 82,96\nAR 83,97\nMU 82,82,86\nMU 83,83,86\nMvav 128,160,32\nRa 144,2,53248\nCV 48,82,83\nAR 82,98\nAR 83,99\nMU 82,82,86\nMvav 128,164,40\nMU 83,83,86\nCV 49,82,83\nAR 82,100\n"
"AR 83,101\nMvav 132,164,44\nRa 148,2,54272\nMU 82,82,87\nMU 83,83,87\nCV 52,82,83\nMvav 132,160,36\nWL 8\nAR 82,102\nAR 83,103\nMU 82,82,87\nMvav 140,168,36\nRa 152,2,55296\n"
"MU 83,83,87\nCV 53,82,83\nAR 82,104\nMvav 140,172,44\nD1 1,16,46,1024\nAR 83,105\nMU 82,82,86\nMU 83,83,86\nMvav 136,172,40\nRa 156,2,56320\nCV 50,82,83\nAR 82,106\nAR 83,107\n"
"Mvav 136,168,32\nMU 82,82,86\nMU 83,83,86\nCV 51,82,83\nAR 82,108\nMvav 144,176,32\nRa 160,2,57344\nAR 83,109\nMU 82,82,87\nMU 83,83,87\nCV 54,82,83\nMvav 144,180,40\nAR 82,110\n"
"AR 83,111\nMU 82,82,87\nMU 83,83,87\nMvav 148,180,44\nRa 164,2,58368\nCV 55,82,83\nAR 82,112\nAR 83,113\nMvav 148,176,36\nWL 8\nMU 82,82,86\nMU 83,83,86\nCV 56,82,83\n"
"Mvav 156,184,36\nRa 168,2,59392\nAR 82,114\nAR 83,115\nMU 82,82,86\nMvav 156,188,44\nD1 1,16,46,2048\nMU 83,83,86\nCV 57,82,83\nAR 82,116\nMvav 152,188,40\nRa 172,2,60416\n"
"AR 83,117\nMU 82,82,87\nMU 83,83,87\nMvav 152,184,32\nCV 60,82,83\nAR 82,118\nAR 83,119\nMU 82,82,87\nMvav 64,128,32\nRa 176,2,61440\nMU 83,83,87\nCV 61,82,83\nAR 82,120\n"
"AR 83,121\nMvav 64,132,40\nMU 82,82,86\nMU 83,83,86\nCV 58,82,83\nAR 82,122\nMvav 68,132,44\nRa 180,2,62464\nAR 83,123\nMU 82,82,86\nMU 83,83,86\nMvav 68,128,36\n"
"s_waitcnt vmcnt(16) lgkmcnt(8)\nCV 59,82,83\nAR 82,124\nAR 83,125\nMvav 76,136,36\nRa 184,2,63488\nMU 82,82,87\nMU 83,83,87\nCV 62,82,83\nMvav 76,140,44\nD1 1,16,46,3072\n"
"AR 82,126\nAR 83,127\nMU 82,82,87\nMvav 72,140,40\nRa 188,2,64512\ns_barrier\nMU 83,83,87\nCV 63,82,83\nMvav 72,136,32\nMvav 128,144,48\nRa 128,3,0\nMvav 128,148,56\n"
"Mvav 132,148,60\nRa 132,3,1024\nMvav 132,144,52\nWL 8\nMvav 140,152,52\nRa 136,3,2048\nMvav 140,156,60\nMvav 136,156,56\nRa 140,3,3072\nMvav 136,152,48\nMvav 144,160,48\n"
"Ra 144,3,4096\nMvav 144,164,56\nMvav 148,164,60\nRa 148,3,5120\nCV 132,132,133\nMvav 148,160,52\nWL 8\nCV 133,134,135\nMvav 156,168,52\nRa 152,3,6144\nCV 134,140,141\n"
"Mvav 156,172,60\nCV 135,142,143\ns_add_u32 s5, s94, 0\nMvav 152,172,56\nRa 156,3,7168\nSv 132,1,16,5,1024\nMvav 152,168,48\nCV 128,128,129\nCV 129,130,131\nMvav 64,176,48\n"
"Ra 160,3,8192\nCV 130,136,137\nMvav 64,180,56\nbuffer_load_dword v27, off, s[16:19], s60 offset:512 sc0 sc1\nCV 131,138,139\nMvav 68,180,60\nRa 164,3,9216\nSv 128,1,16,5,0\n"
"Mvav 68,176,52\nWL 6\nCV 148,148,149\nMvav 76,184,52\nRa 168,3,10240\nCV 149,150,151\nMvav 76,188,60\nCV 150,156,157\nCV 151,158,159\nMvav 72,188,56\nRa 172,3,11264\n"
"s_add_u32 s5, s94, 131072\nMvav 72,184,48\nSv 148,1,16,5,1024\nCV 144,144,145\nZvav 160,128,96\nRa 176,3,12288\nCV 145,146,147\nMvav 160,132,104\nCV 146,152,153\nCV 147,154,155\n"
"Mvav 160,136,112\nRa 180,3,13312\nSv 144,1,16,5,0\nMvav 160,140,120\nWL 6\nCV 68,68,69\nZvav 164,140,124\nRa 184,3,14336\nCV 69,70,71\nMvav 164,136,116\nCV 70,76,77\nCV 71,78,79\n"
"Mvav 164,132,108\nRa 188,3,15360\ns_add_u32 s5, s94, 262144\nMvav 164,128,100\nSv 68,1,16,5,1024\nCV 64,64,65\nMvav 164,144,36\nRa 128,3,16384\nCV 65,66,67\nMvav 164,148,44\n"
"CV 66,72,73\nCV 67,74,75\nMvav 164,152,52\nRa 132,3,17408\nSv 64,1,16,5,0\nMvav 164,156,60\nWL 6\nMvav 160,156,56\nRa 136,3,18432\nMvav 160,152,48\nMvav 160,148,40\n"
"Ra 140,3,19456\nMvav 160,144,32\nZvav 168,160,96\nRa 144,3,20480\nMvav 168,164,104\nMvav 168,168,112\nRa 148,3,21504\nMvav 168,172,120\nWL 6\nZvav 172,172,124\nRa 152,3,22528\n"
"Mvav 172,168,116\nMvav 172,164,108\nRa 156,3,23552\nMvav 172,160,100\nMvav 172,176,36\nRa 160,3,24576\nMvav 172,180,44\nMvav 172,184,52\nRa 164,3,25600\nMvav 172,188,60\nWL 6\n"
"Mvav 168,188,56\nRa 168,3,26624\nMvav 168,184,48\nMvav 168,180,40\nRa 172,3,27648\nMvav 168,176,32\nZvav 176,128,96\nRa 176,3,28672\nCV 164,164,165\nMvav 176,132,104\n"
"CV 165,166,167\nCV 166,172,173\nMvav 176,136,112\nRa 180,3,29696\nCV 167,174,175\nMvav 176,140,120\ns_waitcnt vmcnt(11) lgkmcnt(6)\ns_add_u32 s5, s94, 393216\nZvav 180,140,124\n"
"Ra 184,3,30720\nSv 164,1,16,5,1024\nMvav 180,136,116\nCV 160,160,161\nCV 161,162,163\nMvav 180,132,108\nRa 188,3,31744\ns_barrier\nMvav 180,128,100\nCV 162,168,169\n"
"CV 163,170,171\nMvav 180,144,36\nRa 128,3,32768\nSv 160,1,16,5,0\nMvav 180,148,44\nMvav 180,152,52\nRa 132,3,33792\nMvav 180,156,60\nWL 6\ns_add_u32 m0, s89, 16384\n"
"s_add_u32 s46, s96, 1277952\nMvav 176,156,56\nRa 136,3,34816\nMvav 176,152,48\nD1 1,16,46,0\nMvav 176,148,40\nRa 140,3,35840\nMvav 176,144,32\nZvav 184,160,96\nRa 144,3,36864\n"
"Mvav 184,164,104\nMvav 184,168,112\nRa 148,3,37888\nMvav 184,172,120\nWL 6\nZvav 188,172,124\nRa 152,3,38912\nMvav 188,168,116\nD1 1,16,46,1024\nMvav 188,164,108\nRa 156,3,39936\n"
"Mvav 188,160,100\nMvav 188,176,36\nRa 160,3,40960\nWV 9\nv_cmp_ne_u32 vcc, s15, v27\ns_nop 0\ns_cbranch_vccnz .S244\n.S245:\nMvav 188,180,44\ns_add_u32 s4, s92, 0\n"
"Mvav 188,184,52\nRa 164,3,41984\nMvav 188,188,60\nWL 6\nbuffer_load_dwordx4 v[208:211], v1, s[16:19], s4 offen offset:0 sc0 sc1\nMvav 184,188,56\nRa 168,3,43008\nMvav 184,184,48\n"
"D1 1,16,46,2048\nMvav 184,180,40\nRa 172,3,44032\nMvav 184,176,32\nbuffer_load_dwordx4 v[212:215], v1, s[16:19], s4 offen offset:1024 sc0 sc1\nZvav 192,128,96\nRa 176,3,45056\n"
"v_lshrrev_b32 v88, 8, v1\nMvav 192,132,104\nv_lshlrev_b32 v89, 5, v88\nCV 180,180,181\nMvav 192,136,112\nRa 180,3,46080\nCV 181,182,183\nMvav 192,140,120\nWL 6\nCV 182,188,189\n"
"Zvav 196,140,124\nRa 184,3,47104\nv_bfe_u32 v90, v1, 4, 4\nMvav 196,136,116\nD1 1,16,46,3072\nCV 183,190,191\nMvav 196,132,108\nRa 188,3,48128\ns_add_u32 s5, s94, 524288\n"
"Mvav 196,128,100\nv_mul_u32_u24 v91, 896, v90\nSv 180,1,16,5,1024\nMvav 196,144,36\nRa 128,3,49152\nv_lshl_add_u32 v91, v88, 4, v91\nMvav 196,148,44\nv_add_u32 v90, s21, v90\n"
"CV 176,176,177\nMvav 196,152,52\nRa 132,3,50176\nCV 177,178,179\nMvav 196,156,60\nWL 6\ns_add_u32 m0, s89, 0\ns_add_u32 s46, s96, 1310720\nMvav 192,156,56\nRa 136,3,51200\n"
"v_lshlrev_b32 v88, 3, v90\nMvav 192,152,48\nD1 1,16,46,0\nCV 178,184,185\nMvav 192,148,40\nRa 140,3,52224\nCV 179,186,187\nMvav 192,144,32\nv_add_u32 v94, 64, v90\n"
"Sv 176,1,16,5,0\nZvav 200,160,96\nRa 144,3,53248\ns_mov_b32 s63, 0xc040000\nMvav 200,164,104\nv_lshrrev_b32 v92, 8, v1\nMvav 200,168,112\nRa 148,3,54272\nMvav 200,172,120\nWL 6\n"
"Zvav 204,172,124\nRa 152,3,55296\nv_bfe_u32 v93, v1, 6, 2\nMvav 204,168,116\nD1 1,16,46,1024\nv_cmp_eq_u32 vcc, v92, v93\nv_and_b32 v92, 16, v1\nv_mov_b32 v93, 0x3f80\n"
"v_cndmask_b32 v93, 0, v93, vcc\nMvav 204,164,108\nRa 156,3,56320\nMvav 204,160,100\nv_lshlrev_b32 v93, v92, v93\nMvav 204,176,36\nRa 160,3,57344\nv_and_b32 v92, 32, v1\n"
"Mvav 204,180,44\ns_add_u32 s4, s92, 131072\nv_cmp_eq_u32 vcc, 0, v92\nv_mov_b32 v4, 0\nv_mov_b32 v5, 0\nv_cndmask_b32 v6, 0, v93, vcc\nMvav 204,184,52\nRa 164,3,58368\n"
"Mvav 204,188,60\nWL 6\nbuffer_load_dwordx4 v[216:219], v1, s[16:19], s4 offen offset:0 sc0 sc1\nMvav 200,188,56\nRa 168,3,59392\nv_sub_u32 v7, v93, v6\nMvav 200,184,48\n"
"D1 1,16,46,2048\nMvav 200,180,40\nRa 172,3,60416\nMvav 200,176,32\nbuffer_load_dwordx4 v[220:223], v1, s[16:19], s4 offen offset:1024 sc0 sc1\nZvav 160,128,96\nRa 176,3,61440\n"
"v_mov_b32 v8, 0\nMvav 160,132,104\nv_mov_b32 v9, 0\nCV 196,196,197\nMvav 160,136,112\nRa 180,3,62464\nCV 197,198,199\nMvav 160,140,120\nWL 6\nCV 198,204,205\nZvav 164,140,124\n"
"Ra 184,3,63488\nCV 199,206,207\nMvav 164,136,116\nD1 1,16,46,3072\ns_add_u32 s5, s94, 655360\nMvav 164,132,108\nRa 188,3,64512\ns_barrier\nMvav 164,128,100\nSv 196,1,16,5,1024\n"
"CV 192,192,193\nMvav 164,144,36\nRa 128,0,0\nCV 193,194,195\nMvav 164,148,44\nCV 194,200,201\nCV 195,202,203\nMvav 164,152,52\nRa 132,0,1024\nSv 192,1,16,5,0\nMvav 164,156,60\n"
"WL 6\ns_add_u32 m0, s89, 32768\ns_add_u32 s46, s96, 1343488\nMvav 160,156,56\nRa 136,0,2048\nMvav 160,152,48\nD1 1,16,46,0\nMvav 160,148,40\nRa 140,0,3072\nMvav 160,144,32\n"
"Zvav 168,160,96\nRa 144,0,4096\nWV 12\nv_max_u32 v21, v211, v215\nv_cmp_eq_u32 vcc, -1, v21\ns_nop 0\ns_cbranch_vccnz .S220\n.S221:\nMvav 168,164,104\n"
"v_lshlrev_b32 v176, 16, v208\nMvav 168,168,112\nRa 148,0,5120\nMvav 168,172,120\nWL 6\nv_and_b32 v177, 0xffff0000, v208\nZvav 172,172,124\nRa 152,0,6144\nMvav 172,168,116\n"
"D1 1,16,46,1024\nMvav 172,164,108\nRa 156,0,7168\nv_lshlrev_b32 v178, 16, v209\nMvav 172,160,100\nv_and_b32 v179, 0xffff0000, v209\nMvav 172,176,36\nRa 160,0,8192\n"
"v_lshlrev_b32 v180, 16, v212\nMvav 172,180,44\nv_and_b32 v181, 0xffff0000, v212\ns_add_u32 s4, s92, 262144\nMvav 172,184,52\nRa 164,0,9216\nMvav 172,188,60\nWL 6\n"
"v_lshlrev_b32 v182, 16, v213\nbuffer_load_dwordx4 v[224:227], v1, s[16:19], s4 offen offset:0 sc0 sc1\nMvav 168,188,56\nRa 168,0,10240\nMvav 168,184,48\nD1 1,16,46,2048\n"
"Mvav 168,180,40\nRa 172,0,11264\nv_and_b32 v183, 0xffff0000, v213\nMvav 168,176,32\nv_lshlrev_b32 v184, 16, v210\n"
"buffer_load_dwordx4 v[228:231], v1, s[16:19], s4 offen offset:1024 sc0 sc1\nMvav 176,128,96\nRa 176,0,12288\nv_and_b32 v185, 0xffff0000, v210\nMvav 176,132,104\n"
"v_lshlrev_b32 v186, 16, v211\nCV 164,164,165\nMvav 176,136,112\nRa 180,0,13312\nCV 165,166,167\nMvav 176,140,120\nWL 6\nCV 166,172,173\nMvav 180,140,124\nRa 184,0,14336\n"
"v_and_b32 v187, 0xffff0000, v211\nMvav 180,136,116\nD1 1,16,46,3072\nCV 167,174,175\nMvav 180,132,108\nRa 188,0,15360\ns_add_u32 s5, s94, 786432\nMvav 180,128,100\n"
"v_lshlrev_b32 v188, 16, v214\nSv 164,1,16,5,1024\nMvav 180,144,36\nRa 128,0,16384\nv_and_b32 v189, 0xffff0000, v214\nMvav 180,148,44\nv_lshlrev_b32 v190, 16, v215\nCV 160,160,161\n"
"Mvav 180,152,52\nRa 132,0,17408\nCV 161,162,163\nMvav 180,156,60\nWL 6\ns_add_u32 m0, s89, 49152\ns_add_u32 s46, s96, 1376256\nMvav 176,156,56\nRa 136,0,18432\n"
"v_and_b32 v191, 0xffff0000, v215\nMvav 176,152,48\nD1 1,16,46,0\nCV 162,168,169\nMvav 176,148,40\nRa 140,0,19456\nCV 163,170,171\nMvav 176,144,32\nSv 160,1,16,5,0\n"
"Mvav 184,160,96\nRa 144,0,20480\nWV 12\nv_max_u32 v21, v219, v223\nv_cmp_eq_u32 vcc, -1, v21\ns_nop 0\ns_cbranch_vccnz .S223\n.S224:\nMvav 184,164,104\n"
"v_lshlrev_b32 v192, 16, v216\nMvav 184,168,112\nRa 148,0,21504\nMvav 184,172,120\nWL 6\nv_and_b32 v193, 0xffff0000, v216\nMvav 188,172,124\nRa 152,0,22528\nMvav 188,168,116\n"
"D1 1,16,46,1024\nMvav 188,164,108\nRa 156,0,23552\nv_lshlrev_b32 v194, 16, v217\nMvav 188,160,100\nv_and_b32 v195, 0xffff0000, v217\nMvav 188,176,36\nRa 160,0,24576\n"
"v_lshlrev_b32 v196, 16, v220\nMvav 188,180,44\nv_and_b32 v197, 0xffff0000, v220\ns_add_u32 s4, s92, 393216\nMvav 188,184,52\nRa 164,0,25600\nMvav 188,188,60\nWL 6\n"
"v_lshlrev_b32 v198, 16, v221\nbuffer_load_dwordx4 v[208:211], v1, s[16:19], s4 offen offset:0 sc0 sc1\nMvav 184,188,56\nRa 168,0,26624\nMvav 184,184,48\nD1 1,16,46,2048\n"
"Mvav 184,180,40\nRa 172,0,27648\nv_and_b32 v199, 0xffff0000, v221\nMvav 184,176,32\nv_lshlrev_b32 v200, 16, v218\n"
"buffer_load_dwordx4 v[212:215], v1, s[16:19], s4 offen offset:1024 sc0 sc1\nMvav 192,128,96\nRa 176,0,28672\nv_and_b32 v201, 0xffff0000, v218\nMvav 192,132,104\n"
"v_lshlrev_b32 v202, 16, v219\nCV 180,180,181\nMvav 192,136,112\nRa 180,0,29696\nCV 181,182,183\nMvav 192,140,120\ns_waitcnt vmcnt(15) lgkmcnt(6)\nCV 182,188,189\nMvav 196,140,124\n"
"Ra 184,0,30720\nv_and_b32 v203, 0xffff0000, v219\nMvav 196,136,116\nD1 1,16,46,3072\nCV 183,190,191\nMvav 196,132,108\nRa 188,0,31744\ns_barrier\nMvav 196,128,100\n"
"v_lshlrev_b32 v204, 16, v222\ns_add_u32 s5, s94, 917504\nMvav 196,144,36\nRa 128,2,16384\nv_and_b32 v205, 0xffff0000, v222\nMvav 196,148,44\nv_lshlrev_b32 v206, 16, v223\n"
"Sv 180,1,16,5,1024\nMvav 196,152,52\nRa 132,2,17408\nCV 176,176,177\nMvav 196,156,60\nWL 6\ns_add_u32 m0, s89, 65536\ns_add_u32 s46, s96, 1409024\nMvav 192,156,56\nRa 136,2,18432\n"
"v_and_b32 v207, 0xffff0000, v223\nMvav 192,152,48\nD1 1,16,46,0\nCV 177,178,179\nMvav 192,148,40\nRa 140,2,19456\nCV 178,184,185\nMvav 192,144,32\nCV 179,186,187\nSv 176,1,16,5,0\n"
"Mvav 200,160,96\nRa 144,2,20480\nWV 12\nv_max_u32 v21, v227, v231\nv_cmp_eq_u32 vcc, -1, v21\ns_nop 0\ns_cbranch_vccnz .S226\n.S227:\nMvav 200,164,104\n"
"v_lshlrev_b32 v160, 16, v224\nMvav 200,168,112\nRa 148,2,21504\nMvav 200,172,120\nWL 6\nv_and_b32 v161, 0xffff0000, v224\nMvav 204,172,124\nRa 152,2,22528\nMvav 204,168,116\n"
"D1 1,16,46,1024\nMvav 204,164,108\nRa 156,2,23552\nv_lshlrev_b32 v162, 16, v225\nMvav 204,160,100\nv_and_b32 v163, 0xffff0000, v225\nMvav 204,176,36\nRa 160,2,24576\n"
"v_lshlrev_b32 v164, 16, v228\nMvav 204,180,44\nv_and_b32 v165, 0xffff0000, v228\ns_add_u32 s4, s92, 524288\nMvav 204,184,52\nRa 164,2,25600\nMvav 204,188,60\nWL 6\n"
"v_lshlrev_b32 v166, 16, v229\nbuffer_load_dwordx4 v[216:219], v1, s[16:19], s4 offen offset:0 sc0 sc1\nMvav 200,188,56\nRa 168,2,26624\nMvav 200,184,48\nD1 1,16,46,2048\n"
"Mvav 200,180,40\nRa 172,2,27648\nv_and_b32 v167, 0xffff0000, v229\nMvav 200,176,32\nv_lshlrev_b32 v168, 16, v226\n"
"buffer_load_dwordx4 v[220:223], v1, s[16:19], s4 offen offset:1024 sc0 sc1\nMvav 160,128,96\nRa 176,2,28672\nv_and_b32 v169, 0xffff0000, v226\nMvav 160,132,104\n"
"v_lshlrev_b32 v170, 16, v227\nCV 196,196,197\nMvav 160,136,112\nRa 180,2,29696\nCV 197,198,199\nMvav 160,140,120\nWL 6\nCV 198,204,205\nMvav 164,140,124\nRa 184,2,30720\n"
"v_and_b32 v24, 0x70, v1\nMvav 164,136,116\nD1 1,16,46,3072\nCV 199,206,207\nMvav 164,132,108\nRa 188,2,31744\nv_and_b32 v171, 0xffff0000, v227\nMvav 164,128,100\n"
"v_lshlrev_b32 v172, 16, v230\ns_add_u32 s5, s94, 1048576\nMvav 164,144,36\nRa 128,2,0\nv_lshlrev_b32 v24, 5, v24\nMvav 164,148,44\nv_and_b32 v173, 0xffff0000, v230\n"
"Sv 196,1,16,5,1024\nMvav 164,152,52\nRa 132,2,1024\nCV 192,192,193\nMvav 164,156,60\nWL 6\nv_lshlrev_b32 v174, 16, v231\nMvav 160,156,56\nRa 136,2,2048\n"
"buffer_load_dword v27, v24, s[16:19], s41 offen sc0 sc1\nMvav 160,152,48\nv_and_b32 v175, 0xffff0000, v231\nCV 193,194,195\nMvav 160,148,40\nRa 140,2,3072\nCV 194,200,201\n"
"Mvav 160,144,32\nCV 195,202,203\nSv 192,1,16,5,0\nMvav 168,160,96\nRa 144,2,4096\nWV 12\nv_max_u32 v21, v211, v215\nv_cmp_eq_u32 vcc, -1, v21\ns_nop 0\ns_cbranch_vccnz .S229\n"
".S230:\nMvav 168,164,104\nv_lshlrev_b32 v176, 16, v208\nMvav 168,168,112\nRa 148,2,5120\nMvav 168,172,120\nWL 6\nMvav 172,172,124\nRa 152,2,6144\nv_and_b32 v177, 0xffff0000, v208\n"
"Mvav 172,168,116\nv_lshlrev_b32 v178, 16, v209\nMvav 172,164,108\nRa 156,2,7168\nMvav 172,160,100\nv_and_b32 v179, 0xffff0000, v209\nMvav 172,176,36\nRa 160,2,8192\n"
"v_lshlrev_b32 v180, 16, v212\nMvav 172,180,44\nv_and_b32 v181, 0xffff0000, v212\ns_add_u32 s4, s92, 655360\nMvav 172,184,52\nRa 164,2,9216\nMvav 172,188,60\nWL 6\n"
"buffer_load_dwordx4 v[224:227], v1, s[16:19], s4 offen offset:0 sc0 sc1\nMvav 168,188,56\nRa 168,2,10240\nv_lshlrev_b32 v182, 16, v213\nMvav 168,184,48\n"
"v_and_b32 v183, 0xffff0000, v213\nMvav 168,180,40\nRa 172,2,11264\nMvav 168,176,32\nv_lshlrev_b32 v184, 16, v210\n"
"buffer_load_dwordx4 v[228:231], v1, s[16:19], s4 offen offset:1024 sc0 sc1\nMvav 176,128,96\nRa 176,2,12288\nv_and_b32 v185, 0xffff0000, v210\nMvav 176,132,104\n"
"v_lshlrev_b32 v186, 16, v211\nCV 164,164,165\nMvav 176,136,112\nRa 180,2,13312\nCV 165,166,167\nMvav 176,140,120\ns_waitcnt vmcnt(13) lgkmcnt(6)\nCV 166,172,173\nMvav 180,140,124\n"
"Ra 184,2,14336\nCV 167,174,175\nMvav 180,136,116\nv_and_b32 v187, 0xffff0000, v211\ns_add_u32 s5, s94, 1179648\nMvav 180,132,108\nRa 188,2,15360\ns_barrier\nMvav 180,128,100\n"
"v_lshlrev_b32 v188, 16, v214\nSv 164,1,16,5,1024\nMvav 180,144,36\nRa 128,2,32768\nv_and_b32 v189, 0xffff0000, v214\nMvav 180,148,44\nv_lshlrev_b32 v190, 16, v215\nCV 160,160,161\n"
"Mvav 180,152,52\nRa 132,2,33792\nCV 161,162,163\nMvav 180,156,60\nWL 6\nCV 162,168,169\nMvav 176,156,56\nRa 136,2,34816\nCV 163,170,171\nMvav 176,152,48\n"
"v_and_b32 v191, 0xffff0000, v215\nSv 160,1,16,5,0\nMvav 176,148,40\nRa 140,2,35840\nMvav 176,144,32\nMvav 184,160,96\nRa 144,2,36864\nWV 8\nv_max_u32 v21, v219, v223\n"
"v_cmp_eq_u32 vcc, -1, v21\ns_nop 0\ns_cbranch_vccnz .S232\n.S233:\nMvav 184,164,104\nv_lshlrev_b32 v192, 16, v216\nMvav 184,168,112\nRa 148,2,37888\nMvav 184,172,120\nWL 6\n"
"Mvav 188,172,124\nRa 152,2,38912\nv_and_b32 v193, 0xffff0000, v216\nMvav 188,168,116\nv_lshlrev_b32 v194, 16, v217\nMvav 188,164,108\nRa 156,2,39936\nMvav 188,160,100\n"
"v_and_b32 v195, 0xffff0000, v217\nMvav 188,176,36\nRa 160,2,40960\nv_lshlrev_b32 v196, 16, v220\nMvav 188,180,44\nv_and_b32 v197, 0xffff0000, v220\ns_add_u32 s4, s92, 786432\n"
"Mvav 188,184,52\nRa 164,2,41984\nMvav 188,188,60\nWL 6\nbuffer_load_dwordx4 v[208:211], v1, s[16:19], s4 offen offset:0 sc0 sc1\nMvav 184,188,56\nRa 168,2,43008\n"
"v_lshlrev_b32 v198, 16, v221\nMvav 184,184,48\nv_and_b32 v199, 0xffff0000, v221\nMvav 184,180,40\nRa 172,2,44032\nMvav 184,176,32\nv_lshlrev_b32 v200, 16, v218\n"
"buffer_load_dwordx4 v[212:215], v1, s[16:19], s4 offen offset:1024 sc0 sc1\nMvav 192,128,96\nRa 176,2,45056\nv_and_b32 v201, 0xffff0000, v218\nMvav 192,132,104\n"
"v_lshlrev_b32 v202, 16, v219\nCV 180,180,181\nMvav 192,136,112\nRa 180,2,46080\nCV 181,182,183\nMvav 192,140,120\nWL 6\nCV 182,188,189\nMvav 196,140,124\nRa 184,2,47104\n"
"CV 183,190,191\nMvav 196,136,116\nv_and_b32 v203, 0xffff0000, v219\ns_add_u32 s5, s94, 1310720\nMvav 196,132,108\nRa 188,2,48128\nSv 180,1,16,5,1024\nMvav 196,128,100\n"
"v_lshlrev_b32 v204, 16, v222\nCV 176,176,177\nMvav 196,144,36\nRa 128,2,49152\nv_and_b32 v205, 0xffff0000, v222\nMvav 196,148,44\nv_lshlrev_b32 v206, 16, v223\nCV 177,178,179\n"
"Mvav 196,152,52\nRa 132,2,50176\nCV 178,184,185\nMvav 196,156,60\nWL 6\nCV 179,186,187\nMvav 192,156,56\nRa 136,2,51200\nSv 176,1,16,5,0\nMvav 192,152,48\n"
"v_and_b32 v207, 0xffff0000, v223\nMvav 192,148,40\nRa 140,2,52224\nMvav 192,144,32\nMvav 200,160,96\nRa 144,2,53248\nWV 6\nv_max_u32 v21, v227, v231\nv_cmp_eq_u32 vcc, -1, v21\n"
"s_nop 0\ns_cbranch_vccnz .S235\n.S236:\nMvav 200,164,104\nv_lshlrev_b32 v160, 16, v224\nv_xor_b32 v27, s15, v27\nv_and_b32 v26, 0xffffdfff, v27\nv_cmp_ne_u32 s[64:65], 0, v27\n"
"v_cmp_ne_u32 vcc, 0, v26\ns_nop 0\ns_cbranch_vccnz .S247\n.S248:\nMvav 200,168,112\nRa 148,2,54272\ns_bitcmp1_b32 s64, 0\ns_cselect_b32 s2, 0x2000, 0\nMvav 200,172,120\nWL 6\n"
"Mvav 204,172,124\nRa 152,2,55296\nv_and_b32 v161, 0xffff0000, v224\nMvav 204,168,116\nv_lshlrev_b32 v162, 16, v225\nMvav 204,164,108\nRa 156,2,56320\ns_xor_b32 s2, s2, s20\n"
"Mvav 204,160,100\nv_and_b32 v163, 0xffff0000, v225\nMvav 204,176,36\nRa 160,2,57344\nv_lshlrev_b32 v164, 16, v228\nMvav 204,180,44\nv_and_b32 v165, 0xffff0000, v228\n"
"Mvav 204,184,52\nRa 164,2,58368\nMvav 204,188,60\nWL 6\ns_add_u32 s24, s2, 0x0\nMvav 200,188,56\nRa 168,2,59392\nv_lshlrev_b32 v166, 16, v229\nMvav 200,184,48\n"
"v_and_b32 v167, 0xffff0000, v229\ns_bitcmp1_b32 s64, 1\ns_cselect_b32 s2, 0x2000, 0\nMvav 200,180,40\nRa 172,2,60416\nMvav 200,176,32\nv_lshlrev_b32 v168, 16, v226\n"
"Mvav 160,128,96\nRa 176,2,61440\nv_and_b32 v169, 0xffff0000, v226\nMvav 160,132,104\nv_lshlrev_b32 v170, 16, v227\nCV 196,196,197\nMvav 160,136,112\nRa 180,2,62464\n"
"CV 197,198,199\nMvav 160,140,120\nWL 6\nCV 198,204,205\nMvav 164,140,124\nRa 184,2,63488\nCV 199,206,207\nMvav 164,136,116\nv_and_b32 v171, 0xffff0000, v227\n"
"s_add_u32 s5, s94, 1441792\nMvav 164,132,108\nRa 188,2,64512\ns_barrier\nMvav 164,128,100\nv_lshlrev_b32 v172, 16, v230\nSv 196,1,16,5,1024\nMvav 164,144,36\nRa 128,3,0\n"
"v_and_b32 v173, 0xffff0000, v230\nMvav 164,148,44\nv_lshlrev_b32 v174, 16, v231\nCV 192,192,193\nMvav 164,152,52\nRa 132,3,1024\nCV 193,194,195\nMvav 164,156,60\nWL 6\n"
"s_xor_b32 s2, s2, s20\nMvav 160,156,56\nRa 136,3,2048\nCV 194,200,201\nMvav 160,152,48\nv_and_b32 v175, 0xffff0000, v231\nCV 195,202,203\nMvav 160,148,40\nRa 140,3,3072\n"
"Sv 192,1,16,5,0\nMvav 160,144,32\ns_add_u32 s25, s2, 0x1800000\ns_bitcmp1_b32 s64, 2\ns_cselect_b32 s2, 0x2000, 0\nMvav 168,160,96\nRa 144,3,4096\nWV 4\nv_max_u32 v21, v211, v215\n"
"v_cmp_eq_u32 vcc, -1, v21\ns_nop 0\ns_cbranch_vccnz .S238\n.S239:\nMvav 168,164,104\nv_lshlrev_b32 v176, 16, v208\nMvav 168,168,112\nRa 148,3,5120\nMvav 168,172,120\nWL 6\n"
"Mvav 172,172,124\nRa 152,3,6144\nv_and_b32 v177, 0xffff0000, v208\nMvav 172,168,116\nv_lshlrev_b32 v178, 16, v209\nMvav 172,164,108\nRa 156,3,7168\ns_xor_b32 s2, s2, s20\n"
"Mvav 172,160,100\nv_and_b32 v179, 0xffff0000, v209\nMvav 172,176,36\nRa 160,3,8192\nv_lshlrev_b32 v180, 16, v212\nMvav 172,180,44\nv_and_b32 v181, 0xffff0000, v212\n"
"Mvav 172,184,52\nRa 164,3,9216\nMvav 172,188,60\nWL 6\ns_add_u32 s26, s2, 0x3000000\nMvav 168,188,56\nRa 168,3,10240\nv_lshlrev_b32 v182, 16, v213\nMvav 168,184,48\n"
"v_and_b32 v183, 0xffff0000, v213\ns_bitcmp1_b32 s64, 3\ns_cselect_b32 s2, 0x2000, 0\nMvav 168,180,40\nRa 172,3,11264\nMvav 168,176,32\nv_lshlrev_b32 v184, 16, v210\n"
"Mvav 176,128,96\nRa 176,3,12288\ns_xor_b32 s2, s2, s20\nMvav 176,132,104\nv_and_b32 v185, 0xffff0000, v210\nCV 164,164,165\nMvav 176,136,112\nRa 180,3,13312\nCV 165,166,167\n"
"Mvav 176,140,120\nWL 6\nCV 166,172,173\nMvav 180,140,124\nRa 184,3,14336\nCV 167,174,175\nMvav 180,136,116\nv_lshlrev_b32 v186, 16, v211\ns_add_u32 s5, s94, 1572864\n"
"Mvav 180,132,108\nRa 188,3,15360\ns_add_u32 s27, s2, 0x4800000\nMvav 180,128,100\nv_and_b32 v187, 0xffff0000, v211\ns_bitcmp1_b32 s64, 4\ns_cselect_b32 s2, 0x2000, 0\n"
"Mvav 180,144,36\nv_lshlrev_b32 v188, 16, v214\nSv 164,1,16,5,1024\nMvav 180,148,44\nv_and_b32 v189, 0xffff0000, v214\nCV 160,160,161\nMvav 180,152,52\n"
"v_lshlrev_b32 v190, 16, v215\nCV 161,162,163\nMvav 180,156,60\nWL 4\ns_xor_b32 s2, s2, s20\nMvav 176,156,56\nv_and_b32 v191, 0xffff0000, v215\nCV 162,168,169\nMvav 176,152,48\n"
"s_add_u32 s32, s2, 0x6000000\ns_bitcmp1_b32 s64, 5\ns_cselect_b32 s2, 0x2000, 0\nMvav 176,148,40\ns_xor_b32 s2, s2, s20\nCV 163,170,171\nMvav 176,144,32\n"
"s_add_u32 s33, s2, 0x7800000\ns_bitcmp1_b32 s64, 6\ns_cselect_b32 s2, 0x2000, 0\nMvav 184,160,96\ns_xor_b32 s2, s2, s20\ns_add_u32 s34, s2, 0x9000000\nMvav 184,164,104\n"
"s_bitcmp1_b32 s64, 7\ns_cselect_b32 s2, 0x2000, 0\nMvav 184,168,112\ns_xor_b32 s2, s2, s20\nSv 160,1,16,5,0\nMvav 184,172,120\nWL 0\nMvav 188,172,124\n"
"s_add_u32 s35, s2, 0xa800000\nMvav 188,168,116\nMvav 188,164,108\nMvav 188,160,100\nMvav 188,176,36\nMvav 188,180,44\nMvav 188,184,52\nMvav 188,188,60\nMvav 184,188,56\n"
"Mvav 184,184,48\nMvav 184,180,40\nMvav 184,176,32\nCV 180,180,181\nCV 181,182,183\ns_nop 1\nCV 182,188,189\nCV 183,190,191\ns_add_u32 s5, s94, 1703936\nSv 180,1,16,5,1024\n"
"CV 176,176,177\nCV 177,178,179\nCV 178,184,185\nCV 179,186,187\nSv 176,1,16,5,0\ns_cmp_eq_u32 s13, 0\ns_cbranch_scc1 .Z250\n"
"buffer_load_dwordx4 v[12:15], v91, s[28:31], s22 offen offset:0\nbuffer_load_dwordx4 v[16:19], v91, s[28:31], s23 offen offset:0\n"
"buffer_load_dwordx4 v[64:67], v89, s[16:19], s40 offen offset:0 sc0 sc1\nbuffer_load_dwordx4 v[68:71], v89, s[16:19], s40 offen offset:16 sc0 sc1\n"
"buffer_load_dwordx4 v[72:75], v89, s[16:19], s40 offen offset:1792 sc0 sc1\nbuffer_load_dwordx4 v[76:79], v89, s[16:19], s40 offen offset:1808 sc0 sc1\n"
"buffer_load_dwordx2 v[80:81], v88, s[16:19], s63 offen offset:0 sc0 sc1\nbuffer_load_dwordx2 v[82:83], v88, s[16:19], s63 offen offset:512 sc0 sc1\n"
"buffer_load_dwordx4 v[96:99], v1, s[16:19], s24 offen offset:0 sc0 sc1\nbuffer_load_dwordx4 v[100:103], v1, s[16:19], s24 offen offset:1024 sc0 sc1\n"
"buffer_load_dwordx4 v[104:107], v1, s[16:19], s25 offen offset:0 sc0 sc1\nbuffer_load_dwordx4 v[108:111], v1, s[16:19], s25 offen offset:1024 sc0 sc1\n"
"buffer_load_dwordx4 v[112:115], v1, s[16:19], s26 offen offset:0 sc0 sc1\nbuffer_load_dwordx4 v[116:119], v1, s[16:19], s26 offen offset:1024 sc0 sc1\n"
"buffer_load_dwordx4 v[120:123], v1, s[16:19], s27 offen offset:0 sc0 sc1\nbuffer_load_dwordx4 v[124:127], v1, s[16:19], s27 offen offset:1024 sc0 sc1\n"
"buffer_load_dwordx4 v[32:35], v1, s[16:19], s32 offen offset:0 sc0 sc1\nbuffer_load_dwordx4 v[36:39], v1, s[16:19], s32 offen offset:1024 sc0 sc1\n"
"buffer_load_dwordx4 v[40:43], v1, s[16:19], s33 offen offset:0 sc0 sc1\nbuffer_load_dwordx4 v[44:47], v1, s[16:19], s33 offen offset:1024 sc0 sc1\n"
"buffer_load_dwordx4 v[48:51], v1, s[16:19], s34 offen offset:0 sc0 sc1\nbuffer_load_dwordx4 v[52:55], v1, s[16:19], s34 offen offset:1024 sc0 sc1\n"
"buffer_load_dwordx4 v[56:59], v1, s[16:19], s35 offen offset:0 sc0 sc1\nbuffer_load_dwordx4 v[60:63], v1, s[16:19], s35 offen offset:1024 sc0 sc1\nWV 16\nv_xor_b32 v66, s15, v66\n"
"v_xor_b32 v70, s15, v70\nv_xor_b32 v74, s15, v74\nv_xor_b32 v78, s15, v78\nv_xor_b32 v81, s15, v81\nv_xor_b32 v83, s15, v83\nv_or3_b32 v92, v66, v70, v74\n"
"v_or3_b32 v92, v92, v78, v81\nv_or_b32 v92, v92, v83\nv_cmp_ne_u32 vcc, 0, v92\ns_nop 0\ns_cbranch_vccnz .S241\n.S242:\nv_and_b32 v92, 0x7ff, v67\nv_lshrrev_b32 v93, 16, v67\n"
"v_cmp_eq_u32 s[44:45], v92, v90\nv_cmp_eq_u32 s[46:47], v93, v90\nv_cmp_eq_u32 s[48:49], v92, v94\nv_cmp_eq_u32 s[50:51], v93, v94\nv_cndmask_b32 v12, v12, v64, s[44:45]\n"
"v_cndmask_b32 v13, v13, v65, s[44:45]\nv_cndmask_b32 v14, v14, v68, s[44:45]\nv_cndmask_b32 v15, v15, v69, s[44:45]\nv_cndmask_b32 v12, v12, v72, s[46:47]\n"
"v_cndmask_b32 v13, v13, v73, s[46:47]\nv_cndmask_b32 v14, v14, v76, s[46:47]\nv_cndmask_b32 v15, v15, v77, s[46:47]\nv_cndmask_b32 v16, v16, v64, s[48:49]\n"
"v_cndmask_b32 v17, v17, v65, s[48:49]\nv_cndmask_b32 v18, v18, v68, s[48:49]\nv_cndmask_b32 v19, v19, v69, s[48:49]\nv_cndmask_b32 v16, v16, v72, s[50:51]\n"
"v_cndmask_b32 v17, v17, v73, s[50:51]\nv_cndmask_b32 v18, v18, v76, s[50:51]\nv_cndmask_b32 v19, v19, v77, s[50:51]\nv_sub_f32 v92, 1.0, v80\nv_sub_f32 v93, 1.0, v82\nCV 92,92,93\n"
"v_and_b32 v93, 0xffff0000, v92\nv_lshlrev_b32 v92, 16, v92\nv_lshlrev_b32 v64, 16, v12\nv_and_b32 v65, 0xffff0000, v12\nMU 64,64,92\nMU 65,65,92\nv_lshlrev_b32 v66, 16, v13\n"
"v_and_b32 v67, 0xffff0000, v13\nMU 66,66,92\nMU 67,67,92\nv_lshlrev_b32 v68, 16, v14\nv_and_b32 v69, 0xffff0000, v14\nMU 68,68,92\nMU 69,69,92\nv_lshlrev_b32 v70, 16, v15\n"
"v_and_b32 v71, 0xffff0000, v15\nMU 70,70,92\nMU 71,71,92\nv_lshlrev_b32 v72, 16, v16\nv_and_b32 v73, 0xffff0000, v16\nMU 72,72,93\nMU 73,73,93\nv_lshlrev_b32 v74, 16, v17\n"
"v_and_b32 v75, 0xffff0000, v17\nMU 74,74,93\nMU 75,75,93\nv_lshlrev_b32 v76, 16, v18\nv_and_b32 v77, 0xffff0000, v18\nMU 76,76,93\nMU 77,77,93\nv_lshlrev_b32 v78, 16, v19\n"
"v_and_b32 v79, 0xffff0000, v19\nMU 78,78,93\nMU 79,79,93\ns_mov_b32 s7, 262144\nWV 0\n.Z253:\nv_max_u32 v84, v99, v103\nv_cmp_eq_u32 s[44:45], -1, v84\nv_max_u32 v84, v107, v111\n"
"v_cmp_eq_u32 s[46:47], -1, v84\nv_max_u32 v84, v115, v119\nv_cmp_eq_u32 s[48:49], -1, v84\nv_max_u32 v84, v123, v127\nv_cmp_eq_u32 s[50:51], -1, v84\nv_max_u32 v84, v35, v39\n"
"v_cmp_eq_u32 s[52:53], -1, v84\nv_max_u32 v84, v43, v47\nv_cmp_eq_u32 s[54:55], -1, v84\nv_max_u32 v84, v51, v55\nv_cmp_eq_u32 s[56:57], -1, v84\nv_max_u32 v84, v59, v63\n"
"v_cmp_eq_u32 s[58:59], -1, v84\ns_or_b64 s[0:1], s[44:45], s[46:47]\ns_or_b64 s[2:3], s[48:49], s[50:51]\ns_or_b64 s[0:1], s[0:1], s[52:53]\ns_or_b64 s[2:3], s[2:3], s[54:55]\n"
"s_or_b64 s[0:1], s[0:1], s[56:57]\ns_or_b64 s[2:3], s[2:3], s[58:59]\ns_or_b64 s[0:1], s[0:1], s[2:3]\ns_cbranch_scc1 .Z251\n.Z252:\nZvvv 136,6,96\nMvvv 136,6,104\nMvvv 136,6,112\n"
"Mvvv 136,6,120\nMvvv 136,6,32\nMvvv 136,6,40\nMvvv 136,6,48\nMvvv 136,6,56\nZvvv 140,4,96\nMvvv 140,4,104\nMvvv 140,4,112\nMvvv 140,4,120\nMvvv 140,4,32\nMvvv 140,4,40\n"
"Mvvv 140,4,48\nMvvv 140,4,56\nZvvv 144,6,100\nMvvv 144,6,108\nMvvv 144,6,116\nMvvv 144,6,124\nMvvv 144,6,36\nMvvv 144,6,44\nMvvv 144,6,52\nMvvv 144,6,60\nZvvv 148,4,100\n"
"Mvvv 148,4,108\nMvvv 148,4,116\nMvvv 148,4,124\nMvvv 148,4,36\nMvvv 148,4,44\nMvvv 148,4,52\nMvvv 148,4,60\nFM 64,80,136\nFM 65,80,137\nFM 66,80,138\nFM 67,80,139\nFM 68,80,140\n"
"FM 69,80,141\nFM 70,80,142\nFM 71,80,143\nCV 12,64,65\nCV 13,66,67\nCV 14,68,69\nCV 15,70,71\nSv 12,91,36,22,0\nFM 72,82,144\nFM 73,82,145\nFM 74,82,146\nFM 75,82,147\n"
"FM 76,82,148\nFM 77,82,149\nFM 78,82,150\nFM 79,82,151\nCV 16,72,73\nCV 17,74,75\nCV 18,76,77\nCV 19,78,79\nSv 16,91,36,23,0\n.Z250:\ns_endpgm\n.S143:\ns_mov_b32 s7, 262144\n"
".S145:\nbuffer_load_dwordx4 v[32:35], v1, s[16:19], s6 offen offset:0 sc0 sc1\nbuffer_load_dwordx4 v[36:39], v1, s[16:19], s6 offen offset:1024 sc0 sc1\n"
"buffer_load_dwordx4 v[40:43], v1, s[16:19], s6 offen offset:2048 sc0 sc1\nbuffer_load_dwordx4 v[44:47], v1, s[16:19], s6 offen offset:3072 sc0 sc1\nWV 0\nv_xor_b32 v48, s15, v33\n"
"v_xor_b32 v49, s15, v35\nv_xor_b32 v50, s15, v37\nv_xor_b32 v51, s15, v39\nv_xor_b32 v52, s15, v41\nv_xor_b32 v53, s15, v43\nv_xor_b32 v54, s15, v45\nv_xor_b32 v55, s15, v47\n"
"v_or3_b32 v48, v48, v49, v50\nv_or3_b32 v51, v51, v52, v53\nv_or3_b32 v48, v48, v54, v55\nv_or_b32 v48, v48, v51\nv_cmp_ne_u32 vcc, 0, v48\ns_nop 0\ns_cbranch_vccz .S144\n"
"s_sleep 6\ns_sub_u32 s7, s7, 1\ns_cbranch_scc0 .S145\ns_branch .S144\n.S146:\ns_mov_b32 s7, 262144\n.S148:\ns_lshl_b32 s5, s73, 8\ns_add_u32 s5, s5, 0xc04ac00\n"
"v_lshrrev_b32 v27, 2, v1\nbuffer_load_dword v27, v27, s[16:19], s5 offen sc0 sc1\nWV 0\nv_xor_b32 v27, s15, v27\nv_and_b32 v255, 0xffffdfff, v27\nv_cmp_ne_u32 vcc, 0, v255\n"
"s_nop 0\ns_cbranch_vccz .S147\ns_sleep 6\ns_sub_u32 s7, s7, 1\ns_cbranch_scc0 .S148\ns_branch .S147\n.T151:\ns_mov_b32 s62, 1\ns_call_b64 s[80:81], .RA10\ns_branch .T152\n.T153:\n"
"s_mov_b32 s62, 1\ns_call_b64 s[80:81], .RA10\ns_branch .T154\n.T156:\ns_mov_b32 s62, 0\ns_call_b64 s[80:81], .RB11\ns_branch .T157\n.T158:\ns_mov_b32 s62, 0\n"
"s_call_b64 s[80:81], .RA10\ns_branch .T159\n.T161:\ns_mov_b32 s62, 0\ns_call_b64 s[80:81], .RB11\ns_branch .T162\n.T163:\ns_mov_b32 s62, 0\ns_call_b64 s[80:81], .RA10\n"
"s_branch .T164\n.S166:\ns_mov_b32 s7, 262144\n.S168:\nv_and_b32 v27, 0x70, v1\nbuffer_load_dword v27, v27, s[16:19], s61 offen offset:128 sc0 sc1\nWV 0\n"
"v_cmp_ne_u32 vcc, s15, v27\ns_nop 0\ns_cbranch_vccz .S167\ns_sleep 6\ns_sub_u32 s7, s7, 1\ns_cbranch_scc0 .S168\ns_branch .S167\n.S169:\ns_mov_b32 s7, 262144\n.S171:\n"
"v_and_b32 v27, 0x70, v1\nbuffer_load_dword v27, v27, s[16:19], s61 offen offset:136 sc0 sc1\nWV 0\nv_cmp_ne_u32 vcc, s15, v27\ns_nop 0\ns_cbranch_vccz .S170\ns_sleep 6\n"
"s_sub_u32 s7, s7, 1\ns_cbranch_scc0 .S171\ns_branch .S170\n.S172:\ns_mov_b32 s7, 262144\n.S174:\nv_and_b32 v27, 0x70, v1\n"
"buffer_load_dword v27, v27, s[16:19], s61 offen offset:140 sc0 sc1\nWV 0\nv_cmp_ne_u32 vcc, s15, v27\ns_nop 0\ns_cbranch_vccz .S173\ns_sleep 6\ns_sub_u32 s7, s7, 1\n"
"s_cbranch_scc0 .S174\ns_branch .S173\n.S175:\ns_mov_b32 s7, 262144\n.S177:\nv_and_b32 v27, 0x70, v1\nbuffer_load_dword v27, v27, s[16:19], s61 offen offset:12 sc0 sc1\nWV 0\n"
"v_cmp_ne_u32 vcc, s15, v27\ns_nop 0\ns_cbranch_vccz .S176\ns_sleep 6\ns_sub_u32 s7, s7, 1\ns_cbranch_scc0 .S177\ns_branch .S176\n.S178:\ns_mov_b32 s7, 262144\n.S180:\n"
"v_and_b32 v27, 0x70, v1\nbuffer_load_dword v27, v27, s[16:19], s61 offen offset:8 sc0 sc1\nWV 0\nv_cmp_ne_u32 vcc, s15, v27\ns_nop 0\ns_cbranch_vccz .S179\ns_sleep 6\n"
"s_sub_u32 s7, s7, 1\ns_cbranch_scc0 .S180\ns_branch .S179\n.S181:\ns_mov_b32 s7, 262144\n.S183:\nv_and_b32 v27, 0xf0, v1\n"
"buffer_load_dword v27, v27, s[16:19], s61 offen offset:4 sc0 sc1\nWV 0\nv_cmp_ne_u32 vcc, s15, v27\ns_nop 0\ns_cbranch_vccz .S182\ns_sleep 6\ns_sub_u32 s7, s7, 1\n"
"s_cbranch_scc0 .S183\ns_branch .S182\n.S185:\ns_mov_b32 s7, 262144\n.S187:\nv_lshrrev_b32 v144, 9, v1\nv_mul_u32_u24 v144, 0x18000, v144\nv_lshlrev_b32 v144, 8, v144\n"
"v_and_b32 v145, 0x1f0, v1\nv_add_u32 v144, v144, v145\ns_mov_b32 s6, 0x15c0000\nbuffer_load_dword v136, v144, s[16:19], s6 offen offset:4 sc0 sc1\ns_mov_b32 s6, 0x15c2000\n"
"buffer_load_dword v137, v144, s[16:19], s6 offen offset:4 sc0 sc1\ns_mov_b32 s6, 0x45c0000\nbuffer_load_dword v138, v144, s[16:19], s6 offen offset:4 sc0 sc1\n"
"s_mov_b32 s6, 0x45c2000\nbuffer_load_dword v139, v144, s[16:19], s6 offen offset:4 sc0 sc1\ns_mov_b32 s6, 0x75c0000\n"
"buffer_load_dword v140, v144, s[16:19], s6 offen offset:4 sc0 sc1\ns_mov_b32 s6, 0x75c2000\nbuffer_load_dword v141, v144, s[16:19], s6 offen offset:4 sc0 sc1\n"
"s_mov_b32 s6, 0xa5c0000\nbuffer_load_dword v142, v144, s[16:19], s6 offen offset:4 sc0 sc1\ns_mov_b32 s6, 0xa5c2000\n"
"buffer_load_dword v143, v144, s[16:19], s6 offen offset:4 sc0 sc1\nWV 0\nv_xor_b32 v136, s15, v136\nv_xor_b32 v137, s15, v137\nv_xor_b32 v138, s15, v138\n"
"v_xor_b32 v139, s15, v139\nv_xor_b32 v140, s15, v140\nv_xor_b32 v141, s15, v141\nv_xor_b32 v142, s15, v142\nv_xor_b32 v143, s15, v143\nv_min_u32 v136, v136, v137\n"
"v_min_u32 v138, v138, v139\nv_min_u32 v140, v140, v141\nv_min_u32 v142, v142, v143\nv_or3_b32 v136, v136, v138, v140\nv_or_b32 v136, v136, v142\nv_cmp_ne_u32 vcc, 0, v136\n"
"s_nop 0\ns_cbranch_vccz .S186\ns_sleep 6\ns_sub_u32 s7, s7, 1\ns_cbranch_scc0 .S187\ns_branch .S186\n.FF184:\nv_lshrrev_b32 v144, 9, v1\nv_mul_u32_u24 v144, 0x18000, v144\n"
"v_lshlrev_b32 v144, 8, v144\nv_and_b32 v145, 0x1f0, v1\nv_add_u32 v144, v144, v145\ns_mov_b32 s6, 0x15c0000\nbuffer_load_dword v136, v144, s[16:19], s6 offen offset:4 sc0 sc1\n"
"s_mov_b32 s6, 0x15c2000\nbuffer_load_dword v137, v144, s[16:19], s6 offen offset:4 sc0 sc1\ns_mov_b32 s6, 0x45c0000\n"
"buffer_load_dword v138, v144, s[16:19], s6 offen offset:4 sc0 sc1\ns_mov_b32 s6, 0x45c2000\nbuffer_load_dword v139, v144, s[16:19], s6 offen offset:4 sc0 sc1\n"
"s_mov_b32 s6, 0x75c0000\nbuffer_load_dword v140, v144, s[16:19], s6 offen offset:4 sc0 sc1\ns_mov_b32 s6, 0x75c2000\n"
"buffer_load_dword v141, v144, s[16:19], s6 offen offset:4 sc0 sc1\ns_mov_b32 s6, 0xa5c0000\nbuffer_load_dword v142, v144, s[16:19], s6 offen offset:4 sc0 sc1\n"
"s_mov_b32 s6, 0xa5c2000\nbuffer_load_dword v143, v144, s[16:19], s6 offen offset:4 sc0 sc1\ns_mov_b32 s90, 0\ns_swappc_b64 s[8:9], s[80:81]\nv_xor_b32 v136, s15, v136\n"
"v_xor_b32 v137, s15, v137\nv_xor_b32 v138, s15, v138\nv_xor_b32 v139, s15, v139\nv_xor_b32 v140, s15, v140\nv_xor_b32 v141, s15, v141\nv_xor_b32 v142, s15, v142\n"
"v_xor_b32 v143, s15, v143\nv_min_u32 v136, v136, v137\nv_min_u32 v138, v138, v139\nv_min_u32 v140, v140, v141\nv_min_u32 v142, v142, v143\nv_or3_b32 v136, v136, v138, v140\n"
"v_or_b32 v136, v136, v142\nv_cmp_ne_u32 vcc, 0, v136\ns_nop 0\ns_cbranch_vccnz .S185\n.S186:\ns_and_b32 s4, s13, 1\ns_lshl1_add_u32 s4, s4, s11\ns_lshl2_add_u32 s4, s12, s4\n"
"s_lshr_b32 s5, s71, 16\ns_cmp_lt_u32 s4, 28\ns_cselect_b32 s5, s71, s5\ns_cselect_b32 s3, 0, 28\ns_and_b32 s5, s5, 0x7ff\ns_sub_u32 s3, s4, s3\ns_mul_i32 s2, s4, 7168\n"
"s_add_u32 s2, s2, 0xc04c800\ns_add_u32 s6, s2, 4096\nbuffer_load_dwordx4 v[136:139], v1, s[16:19], s2 offen offset:0 sc0 sc1\n"
"buffer_load_dwordx4 v[140:143], v1, s[16:19], s2 offen offset:1024 sc0 sc1\nbuffer_load_dwordx4 v[144:147], v1, s[16:19], s2 offen offset:2048 sc0 sc1\n"
"buffer_load_dwordx4 v[148:151], v1, s[16:19], s2 offen offset:3072 sc0 sc1\nbuffer_load_dwordx4 v[152:155], v1, s[16:19], s6 offen offset:0 sc0 sc1\n"
"buffer_load_dwordx4 v[156:159], v1, s[16:19], s6 offen offset:1024 sc0 sc1\nbuffer_load_dwordx4 v[160:163], v1, s[16:19], s6 offen offset:2048 sc0 sc1\nv_lshrrev_b32 v167, 1, v1\n"
"v_and_b32 v167, 24, v167\ns_mul_i32 s2, s5, 896\ns_lshl_b32 s3, s3, 5\ns_add_u32 s2, s2, s3\ns_lshl2_add_u32 s5, s5, 0xc03e000\n"
"buffer_load_dwordx2 v[164:165], v167, s[28:31], s2 offen\nbuffer_load_dword v166, off, s[16:19], s5 sc0 sc1\ns_mov_b32 s90, 0\ns_swappc_b64 s[8:9], s[80:81]\nAD 136,136,140\n"
"AD 137,137,141\nAD 138,138,142\nAD 139,139,143\nAD 144,144,148\nAD 145,145,149\nAD 146,146,150\nAD 147,147,151\nAD 152,152,156\nAD 153,153,157\nAD 154,154,158\nAD 155,155,159\n"
"AD 136,136,144\nAD 137,137,145\nAD 138,138,146\nAD 139,139,147\nAD 152,152,160\nAD 153,153,161\nAD 154,154,162\nAD 155,155,163\nAD 136,136,152\nAD 137,137,153\nAD 138,138,154\n"
"AD 139,139,155\nv_mov_b32 v140, v136\nv_mov_b32 v141, v137\nv_mov_b32 v142, v138\nv_mov_b32 v143, v139\nv_permlane32_swap_b32 v136, v140\nv_permlane32_swap_b32 v137, v141\n"
"v_permlane32_swap_b32 v138, v142\nv_permlane32_swap_b32 v139, v143\nAD 136,136,140\nAD 137,137,141\nAD 138,138,142\nAD 139,139,143\nv_mov_b32 v140, v136\nv_mov_b32 v141, v137\n"
"v_mov_b32 v142, v138\nv_mov_b32 v143, v139\nv_permlane16_swap_b32 v136, v140\nv_permlane16_swap_b32 v137, v141\nv_permlane16_swap_b32 v138, v142\nv_permlane16_swap_b32 v139, v143\n"
"AD 136,136,140\nAD 137,137,141\nAD 138,138,142\nAD 139,139,143\nv_add_f32_dpp v136, v136, v136 row_ror:8 row_mask:0xf bank_mask:0xf\n"
"v_add_f32_dpp v137, v137, v137 row_ror:8 row_mask:0xf bank_mask:0xf\nv_add_f32_dpp v138, v138, v138 row_ror:8 row_mask:0xf bank_mask:0xf\n"
"v_add_f32_dpp v139, v139, v139 row_ror:8 row_mask:0xf bank_mask:0xf\nv_add_f32_dpp v136, v136, v136 row_ror:4 row_mask:0xf bank_mask:0xf\n"
"v_add_f32_dpp v137, v137, v137 row_ror:4 row_mask:0xf bank_mask:0xf\nv_add_f32_dpp v138, v138, v138 row_ror:4 row_mask:0xf bank_mask:0xf\n"
"v_add_f32_dpp v139, v139, v139 row_ror:4 row_mask:0xf bank_mask:0xf\nv_mul_f32 v166, 0xbfb8aa3b, v166\nEX 166,166\nCV 136,136,137\nv_add_f32 v166, 1.0, v166\nv_rcp_f32 v166, v166\n"
"CV 138,138,139\nCV 166,166,166\nv_lshlrev_b32 v166, 16, v166\nv_and_b32 v137, 0xffff0000, v136\nv_lshlrev_b32 v136, 16, v136\nv_and_b32 v139, 0xffff0000, v138\n"
"v_lshlrev_b32 v138, 16, v138\nMU 136,136,166\nMU 137,137,166\nMU 138,138,166\nMU 139,139,166\nCV 136,136,137\nCV 138,138,139\nv_and_b32 v137, 0xffff0000, v136\n"
"v_lshlrev_b32 v136, 16, v136\nv_and_b32 v139, 0xffff0000, v138\nv_lshlrev_b32 v138, 16, v138\nv_lshlrev_b32 v140, 16, v164\nv_and_b32 v141, 0xffff0000, v164\n"
"v_lshlrev_b32 v142, 16, v165\nv_and_b32 v143, 0xffff0000, v165\nAD 136,136,140\nAD 137,137,141\nAD 138,138,142\nAD 139,139,143\nCV 136,136,137\nCV 137,138,139\n"
"v_mov_b32 v138, s15\nv_mov_b32 v139, s71\ns_and_b32 s4, s13, 1\ns_lshl1_add_u32 s4, s4, s11\ns_lshl2_add_u32 s4, s12, s4\ns_lshr_b32 s5, s71, 16\ns_cmp_lt_u32 s4, 28\n"
"s_cselect_b32 s5, s71, s5\ns_cselect_b32 s3, 0, 28\ns_and_b32 s5, s5, 0x7ff\ns_sub_u32 s3, s4, s3\nv_mov_b32 v140, 0x40000000\nv_cmp_gt_u32 vcc, 64, v1\ns_lshl_b32 s4, s4, 6\n"
"s_add_u32 s4, s4, 0xc04b800\nv_cndmask_b32 v140, v140, v1, vcc\nSv 136,140,16,4,0\ns_mov_b32 s90, 100\ns_swappc_b64 s[8:9], s[80:81]\n.T192:\ns_mov_b32 s62, 0\n"
"s_call_b64 s[80:81], .RB11\ns_branch .T193\n.T194:\ns_mov_b32 s62, 0\ns_call_b64 s[80:81], .RA10\ns_branch .T195\n.T199:\ns_mov_b32 s62, 0\ns_call_b64 s[80:81], .RB11\n"
"s_branch .T200\n.T201:\ns_mov_b32 s62, 0\ns_call_b64 s[80:81], .RA10\ns_branch .T202\n.T205:\ns_mov_b32 s62, 0\ns_call_b64 s[80:81], .RB11\ns_branch .T206\n.T207:\n"
"s_mov_b32 s62, 0\ns_call_b64 s[80:81], .RA10\ns_branch .T208\n.S213:\ns_mov_b32 s7, 262144\n.S215:\nbuffer_load_dwordx4 v[100:103], v108, s[16:19], s65 offen offset:0 sc0 sc1\n"
"buffer_load_dwordx4 v[104:107], v108, s[16:19], s65 offen offset:256 sc0 sc1\nWV 0\nv_xor_b32 v109, s15, v102\nv_xor_b32 v110, s15, v106\nv_or_b32 v109, v109, v110\n"
"v_cmp_ne_u32 vcc, 0, v109\ns_nop 0\ns_cbranch_vccz .S214\ns_sleep 6\ns_sub_u32 s7, s7, 1\ns_cbranch_scc0 .S215\ns_branch .S214\n.D211:\nv_mov_b32 v130, 0\nv_mov_b32 v131, 0\n"
"s_mov_b32 s63, 0xff800000\ns_mov_b32 s67, 1\ns_branch .D212\n.T217:\ns_lshl3_add_u32 s62, s67, 2\ns_call_b64 s[80:81], .RB11\ns_cmp_eq_u32 s67, 0\ns_cbranch_scc1 .T218\n"
"SU 20,100,88\nv_min_f32 v20, 0, v20\nEX 20,20\ns_nop 0\nFM 86,101,20\nSU 21,104,89\nv_min_f32 v21, 0, v21\nEX 21,21\ns_nop 0\nFM 87,105,21\nAR 22,192\n"
"v_and_b32 v23, 0xffff0000, v22\nv_lshlrev_b32 v22, 16, v22\nMU 22,22,20\nMU 23,23,20\nCV 22,22,23\nAW 192,22\nAR 22,193\nv_and_b32 v23, 0xffff0000, v22\n"
"v_lshlrev_b32 v22, 16, v22\nMU 22,22,20\nMU 23,23,20\nCV 22,22,23\nAW 193,22\nAR 22,194\nv_and_b32 v23, 0xffff0000, v22\nv_lshlrev_b32 v22, 16, v22\nMU 22,22,20\nMU 23,23,20\n"
"CV 22,22,23\nAW 194,22\nAR 22,195\nv_and_b32 v23, 0xffff0000, v22\nv_lshlrev_b32 v22, 16, v22\nMU 22,22,20\nMU 23,23,20\nCV 22,22,23\nAW 195,22\nAR 22,196\n"
"v_and_b32 v23, 0xffff0000, v22\nv_lshlrev_b32 v22, 16, v22\nMU 22,22,21\nMU 23,23,21\nCV 22,22,23\nAW 196,22\nAR 22,197\nv_and_b32 v23, 0xffff0000, v22\n"
"v_lshlrev_b32 v22, 16, v22\nMU 22,22,21\nMU 23,23,21\nCV 22,22,23\nAW 197,22\nAR 22,198\nv_and_b32 v23, 0xffff0000, v22\nv_lshlrev_b32 v22, 16, v22\nMU 22,22,21\nMU 23,23,21\n"
"CV 22,22,23\nAW 198,22\nAR 22,199\nv_and_b32 v23, 0xffff0000, v22\nv_lshlrev_b32 v22, 16, v22\nMU 22,22,21\nMU 23,23,21\nCV 22,22,23\nAW 199,22\nAR 22,200\n"
"v_and_b32 v23, 0xffff0000, v22\nv_lshlrev_b32 v22, 16, v22\nMU 22,22,20\nMU 23,23,20\nCV 22,22,23\nAW 200,22\nAR 22,201\nv_and_b32 v23, 0xffff0000, v22\n"
"v_lshlrev_b32 v22, 16, v22\nMU 22,22,20\nMU 23,23,20\nCV 22,22,23\nAW 201,22\nAR 22,202\nv_and_b32 v23, 0xffff0000, v22\nv_lshlrev_b32 v22, 16, v22\nMU 22,22,20\nMU 23,23,20\n"
"CV 22,22,23\nAW 202,22\nAR 22,203\nv_and_b32 v23, 0xffff0000, v22\nv_lshlrev_b32 v22, 16, v22\nMU 22,22,20\nMU 23,23,20\nCV 22,22,23\nAW 203,22\nAR 22,204\n"
"v_and_b32 v23, 0xffff0000, v22\nv_lshlrev_b32 v22, 16, v22\nMU 22,22,21\nMU 23,23,21\nCV 22,22,23\nAW 204,22\nAR 22,205\nv_and_b32 v23, 0xffff0000, v22\n"
"v_lshlrev_b32 v22, 16, v22\nMU 22,22,21\nMU 23,23,21\nCV 22,22,23\nAW 205,22\nAR 22,206\nv_and_b32 v23, 0xffff0000, v22\nv_lshlrev_b32 v22, 16, v22\nMU 22,22,21\nMU 23,23,21\n"
"CV 22,22,23\nAW 206,22\nAR 22,207\nv_and_b32 v23, 0xffff0000, v22\nv_lshlrev_b32 v22, 16, v22\nMU 22,22,21\nMU 23,23,21\nCV 22,22,23\nAW 207,22\nAR 22,208\n"
"v_and_b32 v23, 0xffff0000, v22\nv_lshlrev_b32 v22, 16, v22\nMU 22,22,20\nMU 23,23,20\nCV 22,22,23\nAW 208,22\nAR 22,209\nv_and_b32 v23, 0xffff0000, v22\n"
"v_lshlrev_b32 v22, 16, v22\nMU 22,22,20\nMU 23,23,20\nCV 22,22,23\nAW 209,22\nAR 22,210\nv_and_b32 v23, 0xffff0000, v22\nv_lshlrev_b32 v22, 16, v22\nMU 22,22,20\nMU 23,23,20\n"
"CV 22,22,23\nAW 210,22\nAR 22,211\nv_and_b32 v23, 0xffff0000, v22\nv_lshlrev_b32 v22, 16, v22\nMU 22,22,20\nMU 23,23,20\nCV 22,22,23\nAW 211,22\nAR 22,212\n"
"v_and_b32 v23, 0xffff0000, v22\nv_lshlrev_b32 v22, 16, v22\nMU 22,22,21\nMU 23,23,21\nCV 22,22,23\nAW 212,22\nAR 22,213\nv_and_b32 v23, 0xffff0000, v22\n"
"v_lshlrev_b32 v22, 16, v22\nMU 22,22,21\nMU 23,23,21\nCV 22,22,23\nAW 213,22\nAR 22,214\nv_and_b32 v23, 0xffff0000, v22\nv_lshlrev_b32 v22, 16, v22\nMU 22,22,21\nMU 23,23,21\n"
"CV 22,22,23\nAW 214,22\nAR 22,215\nv_and_b32 v23, 0xffff0000, v22\nv_lshlrev_b32 v22, 16, v22\nMU 22,22,21\nMU 23,23,21\nCV 22,22,23\nAW 215,22\nAR 22,216\n"
"v_and_b32 v23, 0xffff0000, v22\nv_lshlrev_b32 v22, 16, v22\nMU 22,22,20\nMU 23,23,20\nCV 22,22,23\nAW 216,22\nAR 22,217\nv_and_b32 v23, 0xffff0000, v22\n"
"v_lshlrev_b32 v22, 16, v22\nMU 22,22,20\nMU 23,23,20\nCV 22,22,23\nAW 217,22\nAR 22,218\nv_and_b32 v23, 0xffff0000, v22\nv_lshlrev_b32 v22, 16, v22\nMU 22,22,20\nMU 23,23,20\n"
"CV 22,22,23\nAW 218,22\nAR 22,219\nv_and_b32 v23, 0xffff0000, v22\nv_lshlrev_b32 v22, 16, v22\nMU 22,22,20\nMU 23,23,20\nCV 22,22,23\nAW 219,22\nAR 22,220\n"
"v_and_b32 v23, 0xffff0000, v22\nv_lshlrev_b32 v22, 16, v22\nMU 22,22,21\nMU 23,23,21\nCV 22,22,23\nAW 220,22\nAR 22,221\nv_and_b32 v23, 0xffff0000, v22\n"
"v_lshlrev_b32 v22, 16, v22\nMU 22,22,21\nMU 23,23,21\nCV 22,22,23\nAW 221,22\nAR 22,222\nv_and_b32 v23, 0xffff0000, v22\nv_lshlrev_b32 v22, 16, v22\nMU 22,22,21\nMU 23,23,21\n"
"CV 22,22,23\nAW 222,22\nAR 22,223\nv_and_b32 v23, 0xffff0000, v22\nv_lshlrev_b32 v22, 16, v22\nMU 22,22,21\nMU 23,23,21\nCV 22,22,23\nAW 223,22\nAR 22,224\n"
"v_and_b32 v23, 0xffff0000, v22\nv_lshlrev_b32 v22, 16, v22\nMU 22,22,20\nMU 23,23,20\nCV 22,22,23\nAW 224,22\nAR 22,225\nv_and_b32 v23, 0xffff0000, v22\n"
"v_lshlrev_b32 v22, 16, v22\nMU 22,22,20\nMU 23,23,20\nCV 22,22,23\nAW 225,22\nAR 22,226\nv_and_b32 v23, 0xffff0000, v22\nv_lshlrev_b32 v22, 16, v22\nMU 22,22,20\nMU 23,23,20\n"
"CV 22,22,23\nAW 226,22\nAR 22,227\nv_and_b32 v23, 0xffff0000, v22\nv_lshlrev_b32 v22, 16, v22\nMU 22,22,20\nMU 23,23,20\nCV 22,22,23\nAW 227,22\nAR 22,228\n"
"v_and_b32 v23, 0xffff0000, v22\nv_lshlrev_b32 v22, 16, v22\nMU 22,22,21\nMU 23,23,21\nCV 22,22,23\nAW 228,22\nAR 22,229\nv_and_b32 v23, 0xffff0000, v22\n"
"v_lshlrev_b32 v22, 16, v22\nMU 22,22,21\nMU 23,23,21\nCV 22,22,23\nAW 229,22\nAR 22,230\nv_and_b32 v23, 0xffff0000, v22\nv_lshlrev_b32 v22, 16, v22\nMU 22,22,21\nMU 23,23,21\n"
"CV 22,22,23\nAW 230,22\nAR 22,231\nv_and_b32 v23, 0xffff0000, v22\nv_lshlrev_b32 v22, 16, v22\nMU 22,22,21\nMU 23,23,21\nCV 22,22,23\nAW 231,22\nAR 22,232\n"
"v_and_b32 v23, 0xffff0000, v22\nv_lshlrev_b32 v22, 16, v22\nMU 22,22,20\nMU 23,23,20\nCV 22,22,23\nAW 232,22\nAR 22,233\nv_and_b32 v23, 0xffff0000, v22\n"
"v_lshlrev_b32 v22, 16, v22\nMU 22,22,20\nMU 23,23,20\nCV 22,22,23\nAW 233,22\nAR 22,234\nv_and_b32 v23, 0xffff0000, v22\nv_lshlrev_b32 v22, 16, v22\nMU 22,22,20\nMU 23,23,20\n"
"CV 22,22,23\nAW 234,22\nAR 22,235\nv_and_b32 v23, 0xffff0000, v22\nv_lshlrev_b32 v22, 16, v22\nMU 22,22,20\nMU 23,23,20\nCV 22,22,23\nAW 235,22\nAR 22,236\n"
"v_and_b32 v23, 0xffff0000, v22\nv_lshlrev_b32 v22, 16, v22\nMU 22,22,21\nMU 23,23,21\nCV 22,22,23\nAW 236,22\nAR 22,237\nv_and_b32 v23, 0xffff0000, v22\n"
"v_lshlrev_b32 v22, 16, v22\nMU 22,22,21\nMU 23,23,21\nCV 22,22,23\nAW 237,22\nAR 22,238\nv_and_b32 v23, 0xffff0000, v22\nv_lshlrev_b32 v22, 16, v22\nMU 22,22,21\nMU 23,23,21\n"
"CV 22,22,23\nAW 238,22\nAR 22,239\nv_and_b32 v23, 0xffff0000, v22\nv_lshlrev_b32 v22, 16, v22\nMU 22,22,21\nMU 23,23,21\nCV 22,22,23\nAW 239,22\nAR 22,240\n"
"v_and_b32 v23, 0xffff0000, v22\nv_lshlrev_b32 v22, 16, v22\nMU 22,22,20\nMU 23,23,20\nCV 22,22,23\nAW 240,22\nAR 22,241\nv_and_b32 v23, 0xffff0000, v22\n"
"v_lshlrev_b32 v22, 16, v22\nMU 22,22,20\nMU 23,23,20\nCV 22,22,23\nAW 241,22\nAR 22,242\nv_and_b32 v23, 0xffff0000, v22\nv_lshlrev_b32 v22, 16, v22\nMU 22,22,20\nMU 23,23,20\n"
"CV 22,22,23\nAW 242,22\nAR 22,243\nv_and_b32 v23, 0xffff0000, v22\nv_lshlrev_b32 v22, 16, v22\nMU 22,22,20\nMU 23,23,20\nCV 22,22,23\nAW 243,22\nAR 22,244\n"
"v_and_b32 v23, 0xffff0000, v22\nv_lshlrev_b32 v22, 16, v22\nMU 22,22,21\nMU 23,23,21\nCV 22,22,23\nAW 244,22\nAR 22,245\nv_and_b32 v23, 0xffff0000, v22\n"
"v_lshlrev_b32 v22, 16, v22\nMU 22,22,21\nMU 23,23,21\nCV 22,22,23\nAW 245,22\nAR 22,246\nv_and_b32 v23, 0xffff0000, v22\nv_lshlrev_b32 v22, 16, v22\nMU 22,22,21\nMU 23,23,21\n"
"CV 22,22,23\nAW 246,22\nAR 22,247\nv_and_b32 v23, 0xffff0000, v22\nv_lshlrev_b32 v22, 16, v22\nMU 22,22,21\nMU 23,23,21\nCV 22,22,23\nAW 247,22\nAR 22,248\n"
"v_and_b32 v23, 0xffff0000, v22\nv_lshlrev_b32 v22, 16, v22\nMU 22,22,20\nMU 23,23,20\nCV 22,22,23\nAW 248,22\nAR 22,249\nv_and_b32 v23, 0xffff0000, v22\n"
"v_lshlrev_b32 v22, 16, v22\nMU 22,22,20\nMU 23,23,20\nCV 22,22,23\nAW 249,22\nAR 22,250\nv_and_b32 v23, 0xffff0000, v22\nv_lshlrev_b32 v22, 16, v22\nMU 22,22,20\nMU 23,23,20\n"
"CV 22,22,23\nAW 250,22\nAR 22,251\nv_and_b32 v23, 0xffff0000, v22\nv_lshlrev_b32 v22, 16, v22\nMU 22,22,20\nMU 23,23,20\nCV 22,22,23\nAW 251,22\nAR 22,252\n"
"v_and_b32 v23, 0xffff0000, v22\nv_lshlrev_b32 v22, 16, v22\nMU 22,22,21\nMU 23,23,21\nCV 22,22,23\nAW 252,22\nAR 22,253\nv_and_b32 v23, 0xffff0000, v22\n"
"v_lshlrev_b32 v22, 16, v22\nMU 22,22,21\nMU 23,23,21\nCV 22,22,23\nAW 253,22\nAR 22,254\nv_and_b32 v23, 0xffff0000, v22\nv_lshlrev_b32 v22, 16, v22\nMU 22,22,21\nMU 23,23,21\n"
"CV 22,22,23\nAW 254,22\nAR 22,255\nv_and_b32 v23, 0xffff0000, v22\nv_lshlrev_b32 v22, 16, v22\nMU 22,22,21\nMU 23,23,21\nCV 22,22,23\nAW 255,22\nv_lshrrev_b32 v22, 8, v1\n"
"v_bfe_u32 v23, v1, 6, 2\nv_cmp_eq_u32 vcc, v22, v23\nv_and_b32 v22, 16, v1\nv_mov_b32 v23, 0x3f80\nv_cndmask_b32 v23, 0, v23, vcc\nv_lshlrev_b32 v23, v22, v23\n"
"v_and_b32 v22, 32, v1\nv_cmp_eq_u32 vcc, 0, v22\nv_mov_b32 v128, 0\nv_mov_b32 v129, 0\nv_cndmask_b32 v130, 0, v23, vcc\nv_sub_u32 v131, v23, v130\nv_mov_b32 v132, 0\n"
"v_mov_b32 v133, 0\ns_nop 1\nMava 0,130,192\nMava 8,128,192\nMava 12,128,196\nMava 4,130,196\nMava 16,130,200\nMava 24,128,200\nMava 28,128,204\nMava 20,130,204\nMava 32,130,208\n"
"Mava 40,128,208\nMava 44,128,212\nMava 36,130,212\nMava 48,130,216\nMava 56,128,216\nMava 60,128,220\nMava 52,130,220\nMava 64,130,224\nMava 72,128,224\nMava 76,128,228\n"
"Mava 68,130,228\nMava 80,130,232\nMava 88,128,232\nMava 92,128,236\nMava 84,130,236\nMava 96,130,240\nMava 104,128,240\nMava 108,128,244\nMava 100,130,244\nMava 112,130,248\n"
"Mava 120,128,248\nMava 124,128,252\nMava 116,130,252\ns_nop 7\ns_branch .T218\n.S220:\ns_mov_b32 s7, 262144\n.S222:\ns_add_u32 s0, s92, 0\n"
"buffer_load_dwordx4 v[208:211], v1, s[16:19], s0 offen offset:0 sc0 sc1\nbuffer_load_dwordx4 v[212:215], v1, s[16:19], s0 offen offset:1024 sc0 sc1\nWV 0\n"
"v_max_u32 v21, v211, v215\nv_cmp_eq_u32 vcc, -1, v21\ns_nop 0\ns_cbranch_vccz .S221\ns_sleep 6\ns_sub_u32 s7, s7, 1\ns_cbranch_scc0 .S222\ns_branch .S221\n.S223:\n"
"s_mov_b32 s7, 262144\n.S225:\ns_add_u32 s0, s92, 131072\nbuffer_load_dwordx4 v[216:219], v1, s[16:19], s0 offen offset:0 sc0 sc1\n"
"buffer_load_dwordx4 v[220:223], v1, s[16:19], s0 offen offset:1024 sc0 sc1\nWV 0\nv_max_u32 v21, v219, v223\nv_cmp_eq_u32 vcc, -1, v21\ns_nop 0\ns_cbranch_vccz .S224\ns_sleep 6\n"
"s_sub_u32 s7, s7, 1\ns_cbranch_scc0 .S225\ns_branch .S224\n.S226:\ns_mov_b32 s7, 262144\n.S228:\ns_add_u32 s0, s92, 262144\n"
"buffer_load_dwordx4 v[224:227], v1, s[16:19], s0 offen offset:0 sc0 sc1\nbuffer_load_dwordx4 v[228:231], v1, s[16:19], s0 offen offset:1024 sc0 sc1\nWV 0\n"
"v_max_u32 v21, v227, v231\nv_cmp_eq_u32 vcc, -1, v21\ns_nop 0\ns_cbranch_vccz .S227\ns_sleep 6\ns_sub_u32 s7, s7, 1\ns_cbranch_scc0 .S228\ns_branch .S227\n.S229:\n"
"s_mov_b32 s7, 262144\n.S231:\ns_add_u32 s0, s92, 393216\nbuffer_load_dwordx4 v[208:211], v1, s[16:19], s0 offen offset:0 sc0 sc1\n"
"buffer_load_dwordx4 v[212:215], v1, s[16:19], s0 offen offset:1024 sc0 sc1\nWV 0\nv_max_u32 v21, v211, v215\nv_cmp_eq_u32 vcc, -1, v21\ns_nop 0\ns_cbranch_vccz .S230\ns_sleep 6\n"
"s_sub_u32 s7, s7, 1\ns_cbranch_scc0 .S231\ns_branch .S230\n.S232:\ns_mov_b32 s7, 262144\n.S234:\ns_add_u32 s0, s92, 524288\n"
"buffer_load_dwordx4 v[216:219], v1, s[16:19], s0 offen offset:0 sc0 sc1\nbuffer_load_dwordx4 v[220:223], v1, s[16:19], s0 offen offset:1024 sc0 sc1\nWV 0\n"
"v_max_u32 v21, v219, v223\nv_cmp_eq_u32 vcc, -1, v21\ns_nop 0\ns_cbranch_vccz .S233\ns_sleep 6\ns_sub_u32 s7, s7, 1\ns_cbranch_scc0 .S234\ns_branch .S233\n.S235:\n"
"s_mov_b32 s7, 262144\n.S237:\ns_add_u32 s0, s92, 655360\nbuffer_load_dwordx4 v[224:227], v1, s[16:19], s0 offen offset:0 sc0 sc1\n"
"buffer_load_dwordx4 v[228:231], v1, s[16:19], s0 offen offset:1024 sc0 sc1\nWV 0\nv_max_u32 v21, v227, v231\nv_cmp_eq_u32 vcc, -1, v21\ns_nop 0\ns_cbranch_vccz .S236\ns_sleep 6\n"
"s_sub_u32 s7, s7, 1\ns_cbranch_scc0 .S237\ns_branch .S236\n.S238:\ns_mov_b32 s7, 262144\n.S240:\ns_add_u32 s0, s92, 786432\n"
"buffer_load_dwordx4 v[208:211], v1, s[16:19], s0 offen offset:0 sc0 sc1\nbuffer_load_dwordx4 v[212:215], v1, s[16:19], s0 offen offset:1024 sc0 sc1\nWV 0\n"
"v_max_u32 v21, v211, v215\nv_cmp_eq_u32 vcc, -1, v21\ns_nop 0\ns_cbranch_vccz .S239\ns_sleep 6\ns_sub_u32 s7, s7, 1\ns_cbranch_scc0 .S240\ns_branch .S239\n.S241:\n"
"s_mov_b32 s7, 262144\n.S243:\nbuffer_load_dwordx4 v[64:67], v89, s[16:19], s40 offen offset:0 sc0 sc1\nbuffer_load_dwordx4 v[68:71], v89, s[16:19], s40 offen offset:16 sc0 sc1\n"
"buffer_load_dwordx4 v[72:75], v89, s[16:19], s40 offen offset:1792 sc0 sc1\nbuffer_load_dwordx4 v[76:79], v89, s[16:19], s40 offen offset:1808 sc0 sc1\n"
"buffer_load_dwordx2 v[80:81], v88, s[16:19], s63 offen offset:0 sc0 sc1\nbuffer_load_dwordx2 v[82:83], v88, s[16:19], s63 offen offset:512 sc0 sc1\nWV 0\nv_xor_b32 v66, s15, v66\n"
"v_xor_b32 v70, s15, v70\nv_xor_b32 v74, s15, v74\nv_xor_b32 v78, s15, v78\nv_xor_b32 v81, s15, v81\nv_xor_b32 v83, s15, v83\nv_or3_b32 v92, v66, v70, v74\n"
"v_or3_b32 v92, v92, v78, v81\nv_or_b32 v92, v92, v83\nv_cmp_ne_u32 vcc, 0, v92\ns_nop 0\ns_cbranch_vccz .S242\ns_sleep 6\ns_sub_u32 s7, s7, 1\ns_cbranch_scc0 .S243\n"
"s_branch .S242\n.S244:\ns_mov_b32 s7, 262144\n.S246:\nbuffer_load_dword v27, off, s[16:19], s60 offset:512 sc0 sc1\nWV 0\nv_cmp_ne_u32 vcc, s15, v27\ns_nop 0\n"
"s_cbranch_vccz .S245\ns_sleep 6\ns_sub_u32 s7, s7, 1\ns_cbranch_scc0 .S246\ns_branch .S245\n.S247:\ns_mov_b32 s7, 262144\n.S249:\n"
"buffer_load_dword v27, v24, s[16:19], s41 offen sc0 sc1\nWV 0\nv_xor_b32 v27, s15, v27\nv_and_b32 v26, 0xffffdfff, v27\nv_cmp_ne_u32 s[64:65], 0, v27\nv_cmp_ne_u32 vcc, 0, v26\n"
"s_nop 0\ns_cbranch_vccz .S248\ns_sleep 6\ns_sub_u32 s7, s7, 1\ns_cbranch_scc0 .S249\ns_branch .S248\n.Z251:\ns_sub_u32 s7, s7, 1\ns_cbranch_scc1 .Z252\ns_cmp_lg_u64 s[44:45], 0\n"
"s_cbranch_scc0 .Z254\nbuffer_load_dwordx4 v[96:99], v1, s[16:19], s24 offen offset:0 sc0 sc1\nbuffer_load_dwordx4 v[100:103], v1, s[16:19], s24 offen offset:1024 sc0 sc1\n.Z254:\n"
"s_cmp_lg_u64 s[46:47], 0\ns_cbranch_scc0 .Z255\nbuffer_load_dwordx4 v[104:107], v1, s[16:19], s25 offen offset:0 sc0 sc1\n"
"buffer_load_dwordx4 v[108:111], v1, s[16:19], s25 offen offset:1024 sc0 sc1\n.Z255:\ns_cmp_lg_u64 s[48:49], 0\ns_cbranch_scc0 .Z256\n"
"buffer_load_dwordx4 v[112:115], v1, s[16:19], s26 offen offset:0 sc0 sc1\nbuffer_load_dwordx4 v[116:119], v1, s[16:19], s26 offen offset:1024 sc0 sc1\n.Z256:\n"
"s_cmp_lg_u64 s[50:51], 0\ns_cbranch_scc0 .Z257\nbuffer_load_dwordx4 v[120:123], v1, s[16:19], s27 offen offset:0 sc0 sc1\n"
"buffer_load_dwordx4 v[124:127], v1, s[16:19], s27 offen offset:1024 sc0 sc1\n.Z257:\ns_cmp_lg_u64 s[52:53], 0\ns_cbranch_scc0 .Z258\n"
"buffer_load_dwordx4 v[32:35], v1, s[16:19], s32 offen offset:0 sc0 sc1\nbuffer_load_dwordx4 v[36:39], v1, s[16:19], s32 offen offset:1024 sc0 sc1\n.Z258:\n"
"s_cmp_lg_u64 s[54:55], 0\ns_cbranch_scc0 .Z259\nbuffer_load_dwordx4 v[40:43], v1, s[16:19], s33 offen offset:0 sc0 sc1\n"
"buffer_load_dwordx4 v[44:47], v1, s[16:19], s33 offen offset:1024 sc0 sc1\n.Z259:\ns_cmp_lg_u64 s[56:57], 0\ns_cbranch_scc0 .Z260\n"
"buffer_load_dwordx4 v[48:51], v1, s[16:19], s34 offen offset:0 sc0 sc1\nbuffer_load_dwordx4 v[52:55], v1, s[16:19], s34 offen offset:1024 sc0 sc1\n.Z260:\n"
"s_cmp_lg_u64 s[58:59], 0\ns_cbranch_scc0 .Z261\nbuffer_load_dwordx4 v[56:59], v1, s[16:19], s35 offen offset:0 sc0 sc1\n"
"buffer_load_dwordx4 v[60:63], v1, s[16:19], s35 offen offset:1024 sc0 sc1\n.Z261:\ns_sleep 8\nWV 0\ns_branch .Z253\n.p2align 12\n.PA4:\ns_add_u32 m0, s47, -49152\n"
"s_add_u32 s48, s48, 128\nD0 2,20,48,0\nD0 3,20,48,1024\nD0 4,20,48,2048\nD0 5,20,48,3072\ns_and_b32 s86, s74, 15\ns_lshl2_add_u32 s86, s86, s11\ns_mov_b32 s88, 0\n"
"s_lshl_b32 s93, s73, 8\ns_lshl2_add_u32 s93, s86, s93\ns_add_u32 s93, s93, 0xc04ac00\ns_lshl_b32 s86, s86, 14\ns_mul_i32 s91, s73, 25165824\ns_add_u32 s86, s86, s91\n"
"s_and_b32 s92, s13, 1\ns_lshl_b32 s92, s92, 13\ns_add_u32 s86, s86, s92\ns_add_u32 s94, s86, 0x1480000\ns_xor_b32 s95, s94, 0x2000\ns_add_u32 s86, s94, 512\n"
"s_load_dword s87, s[54:55], s86\ns_add_u32 m0, s47, -32768\ns_add_u32 s45, s45, 128\nD0 2,20,45,0\nD0 3,20,45,1024\nD0 4,20,45,2048\nD0 5,20,45,3072\ns_add_u32 m0, s47, 32768\n"
"s_add_u32 s44, s44, 0x40000\nD0 6,24,44,0\nD0 7,24,44,1024\nD0 8,24,44,2048\nD0 9,24,44,3072\ns_add_u32 m0, s47, 49152\ns_add_u32 s44, s44, 0x40000\nD0 6,24,44,0\nD0 7,24,44,1024\n"
"D0 8,24,44,2048\nD0 9,24,44,3072\ns_add_u32 m0, s47, -16384\ns_add_u32 s48, s48, 128\nD0 2,20,48,0\nD0 3,20,48,1024\nD0 4,20,48,2048\nD0 5,20,48,3072\nv_mov_b32 v52, 0x40000000\n"
"v_cmp_gt_u32 vcc, 56, v0\ns_lshl2_add_u32 s2, s10, s11\ns_mul_i32 s2, s2, 1792\nv_cndmask_b32 v53, v52, v1, vcc\ns_mov_b32 s36, s56\ns_and_b32 s37, s57, 0xffff\n"
"s_mov_b32 s38, 0x380\ns_mov_b32 s39, 0x20000\ns_mov_b32 s40, s58\ns_and_b32 s41, s59, 0xffff\ns_mov_b32 s42, 0x380\ns_mov_b32 s43, 0x20000\n"
"buffer_load_dwordx4 v[32:35], v53, s[28:31], s2 offen offset:0\nbuffer_load_dwordx4 v[36:39], v53, s[28:31], s2 offen offset:896\n"
"buffer_load_dwordx4 v[40:43], v53, s[36:39], 0 offen offset:0\nbuffer_load_dwordx4 v[44:47], v53, s[40:43], 0 offen offset:0\nWL 0\ns_mul_i32 s15, s15, 1664525\n"
"s_add_u32 s15, s15, 1013904223\nv_mov_b32 v25, s15\ns_mov_b32 s57, 0x3db8aa3b\ns_and_b32 s59, s11, 1\ns_lshl_b32 s59, s59, 13\ns_mul_i32 s2, s73, 25165824\ns_and_b32 s3, s12, 1\n"
"s_lshl_b32 s3, s3, 21\ns_add_u32 s2, s2, s3\ns_lshl2_add_u32 s3, s13, s11\ns_lshl_b32 s3, s3, 15\ns_add_u32 s2, s2, s3\ns_add_u32 s56, s2, 0x800000\ns_xor_b32 s53, s56, 0xc000\n"
"buffer_load_dword v31, v24, s[24:27], s96 offen\ns_add_u32 s96, s96, 0x80000\nbuffer_load_dword v31, v24, s[24:27], s96 offen\ns_add_u32 s96, s96, 0x80000\n"
"v_lshrrev_b32 v60, 3, v0\nv_and_b32 v61, 0x70, v1\nv_lshl_or_b32 v60, v60, 10, v61\nv_mov_b32 v20, 0\nv_mov_b32 v21, 0\nv_mov_b32 v22, 0\nv_mov_b32 v23, 0\nSv 20,60,16,94,0\n"
"Sv 20,60,16,95,0\nWV 28\ns_barrier\nRa 128,10,0\nRa 132,11,0\nRa 136,10,2048\nRa 140,11,2048\nRa 144,10,8192\nRa 148,11,8192\nRa 152,10,10240\nRa 156,11,10240\nTa 160,12,0\n"
"Ta 162,12,2048\nTa 164,12,16384\nTa 166,12,18432\nTa 168,13,0\nTa 170,13,2048\nTa 172,13,16384\nTa 174,13,18432\nTa 176,14,0\nTa 178,14,2048\nTa 180,14,16384\nTa 182,14,18432\n"
"Ta 184,15,0\nTa 186,15,2048\nTa 188,15,16384\nTa 190,15,18432\nWL 0\ns_barrier\nZvaa 64,160,128\nTa 224,16,0\nMvaa 64,164,132\nTa 226,16,2048\nZvaa 96,164,140\n"
"s_add_u32 m0, s47, -65536\ns_add_u32 s45, s45, 128\nMvaa 96,160,136\nTa 228,16,16384\nZvaa 128,160,144\nTa 230,16,18432\nMvaa 128,164,148\nD0 2,20,45,0\nZvaa 160,164,156\n"
"Ta 232,17,0\nMvaa 160,160,152\nTa 234,17,2048\nZvaa 164,168,152\ns_mov_b32 s36, s60\nMvaa 164,172,156\nTa 236,17,16384\nZvaa 132,172,148\nTa 238,17,18432\nMvaa 132,168,144\n"
"s_and_b32 s37, s61, 0xffff\nZvaa 100,168,136\nTa 240,18,0\nMvaa 100,172,140\nTa 242,18,2048\nZvaa 68,172,132\nD0 3,20,45,1024\nMvaa 68,168,128\nTa 244,18,16384\nZvaa 72,176,128\n"
"Ta 246,18,18432\nMvaa 72,180,132\ns_mov_b32 s38, 0x380000\nZvaa 104,180,140\nTa 248,19,0\nMvaa 104,176,136\nTa 250,19,2048\nZvaa 136,176,144\ns_mov_b32 s39, 0x20000\n"
"Mvaa 136,180,148\nTa 252,19,16384\nZvaa 168,180,156\nTa 254,19,18432\nMvaa 168,176,152\nD0 4,20,45,2048\nZvaa 172,184,152\ns_lshl3_add_u32 s75, s12, s13\nMvaa 172,188,156\n"
"s_lshl2_add_u32 s75, s75, s11\nZvaa 140,188,148\ns_mul_i32 s75, s75, 7168\nMvaa 140,184,144\ns_mul_i32 s76, s11, 7168\nZvaa 108,184,136\ns_add_u32 s76, s76, 131072\n"
"Mvaa 108,188,140\nD0 5,20,45,3072\nZvaa 76,188,132\ns_mov_b32 s42, 1605632\nMvaa 76,184,128\ns_waitcnt vmcnt(24) lgkmcnt(0)\ns_barrier\nZvaa 80,224,128\nRa 192,10,16384\n"
"Mvaa 80,228,132\ns_add_u32 m0, s47, 0\ns_add_u32 s44, s44, 0x40000\nZvaa 112,228,140\nRa 196,11,16384\nMvaa 112,224,136\nZvaa 144,224,144\nRa 200,10,18432\nMvaa 144,228,148\n"
"D0 6,24,44,0\nZvaa 176,228,156\nRa 204,11,18432\nMvaa 176,224,152\nZvaa 180,232,152\nRa 208,10,24576\nMvaa 180,236,156\ns_and_b32 s78, s74, 3\ns_lshl2_add_u32 s78, s73, s78\n"
"s_lshl_b32 s78, s78, 12\ns_bitcmp1_b32 s11, 0\ns_cselect_b32 s79, s48, s45\ns_add_u32 s79, s79, 512\ns_and_b32 s79, s79, 0xfffe0000\ns_add_u32 s78, s78, s79\n"
"buffer_load_dword v31, v26, s[20:23], s78 offen\nZvaa 148,236,148\nRa 212,11,24576\nMvaa 148,232,144\nZvaa 116,232,136\nRa 216,10,26624\nMvaa 116,236,140\nD0 7,24,44,1024\n"
"Zvaa 84,236,132\nRa 220,11,26624\nMvaa 84,232,128\nZvaa 88,240,128\nMvaa 88,244,132\nZvaa 120,244,140\nMvaa 120,240,136\nZvaa 152,240,144\nMvaa 152,244,148\nD0 8,24,44,2048\n"
"Zvaa 184,244,156\nMvaa 184,240,152\nZvaa 188,248,152\nMvaa 188,252,156\nZvaa 156,252,148\nMvaa 156,248,144\nZvaa 124,248,136\nMvaa 124,252,140\nD0 9,24,44,3072\nZvaa 92,252,132\n"
"Mvaa 92,248,128\nWL 0\nZaaa 16,224,192\nRa 128,10,32768\nMaaa 16,228,196\ns_add_u32 m0, s47, 16384\ns_add_u32 s44, s44, 0x40000\nZaaa 48,228,204\nRa 132,11,32768\nMaaa 48,224,200\n"
"Zaaa 80,224,208\nRa 136,10,34816\nMaaa 80,228,212\nD0 6,24,44,0\nZaaa 112,228,220\nRa 140,11,34816\nMaaa 112,224,216\nZaaa 116,232,216\nRa 144,10,40960\nMaaa 116,236,220\n"
"Zaaa 84,236,212\nRa 148,11,40960\nMaaa 84,232,208\nZaaa 52,232,200\nRa 152,10,43008\nMaaa 52,236,204\nD0 7,24,44,1024\nZaaa 20,236,196\nRa 156,11,43008\nMaaa 20,232,192\n"
"Zaaa 24,240,192\nMaaa 24,244,196\nZaaa 56,244,204\nMaaa 56,240,200\nZaaa 88,240,208\nMaaa 88,244,212\nD0 8,24,44,2048\nZaaa 120,244,220\nMaaa 120,240,216\nZaaa 124,248,216\n"
"Maaa 124,252,220\nZaaa 92,252,212\nMaaa 92,248,208\nZaaa 60,248,200\nMaaa 60,252,204\nD0 9,24,44,3072\nZaaa 28,252,196\nMaaa 28,248,192\ns_waitcnt vmcnt(25) lgkmcnt(0)\ns_barrier\n"
"Zaaa 0,160,192\nTa 224,16,32768\nWV 17\nMaaa 0,164,196\nTa 226,16,34816\nZaaa 32,164,204\ns_add_u32 m0, s47, -49152\ns_add_u32 s48, s48, 128\nMaaa 32,160,200\nTa 228,16,49152\n"
"Zaaa 64,160,208\nTa 230,16,51200\nbuffer_load_dword v31, v24, s[24:27], s96 offen\ns_add_u32 s96, s96, 0x80000\nMaaa 64,164,212\nD0 2,20,48,0\nv_lshlrev_b32 v48, 16, v32\n"
"Zaaa 96,164,220\nTa 232,17,32768\nMaaa 96,160,216\nTa 234,17,34816\nv_lshlrev_b32 v49, 16, v36\nZaaa 100,168,216\nv_lshlrev_b32 v50, 16, v40\nv_lshlrev_b32 v51, 16, v44\n"
"Maaa 100,172,220\nTa 236,17,49152\nZaaa 68,172,212\nTa 238,17,51200\nMU 52,48,50\nMaaa 68,168,208\nMU 53,49,50\nZaaa 36,168,200\nTa 240,18,32768\nCV 52,52,53\nMaaa 36,172,204\n"
"Ta 242,18,34816\nv_lshlrev_b32 v54, 16, v52\nZaaa 4,172,196\nD0 3,20,48,1024\nMaaa 4,168,192\nTa 244,18,49152\nv_and_b32 v55, 0xffff0000, v52\nZaaa 8,176,192\nTa 246,18,51200\n"
"MU 56,48,51\nMaaa 8,180,196\nMU 57,49,51\nZaaa 40,180,204\nTa 248,19,32768\nv_and_b32 v48, 0xffff0000, v32\nMaaa 40,176,200\nTa 250,19,34816\nZaaa 72,176,208\n"
"v_and_b32 v49, 0xffff0000, v36\nv_and_b32 v50, 0xffff0000, v40\nMaaa 72,180,212\nTa 252,19,49152\nv_and_b32 v51, 0xffff0000, v44\nZaaa 104,180,220\nTa 254,19,51200\n"
"Maaa 104,176,216\nD0 4,20,48,2048\nMU 52,48,50\nZaaa 108,184,216\nMU 53,49,50\nCV 52,52,53\nMaaa 108,188,220\nv_and_b32 v53, 0xffff0000, v52\nZaaa 76,188,212\n"
"v_lshlrev_b32 v52, 16, v52\nFM 56,48,51\nMaaa 76,184,208\nFM 57,49,51\nZaaa 44,184,200\nAD 54,54,52\nAD 55,55,53\nMaaa 44,188,204\nD0 5,20,48,3072\nv_lshlrev_b32 v48, 16, v33\n"
"Zaaa 12,188,196\nv_lshlrev_b32 v49, 16, v37\nMaaa 12,184,192\nWL 0\nMvaa 80,224,128\nTa 160,12,32768\nv_lshlrev_b32 v50, 16, v41\nMvaa 80,228,132\nTa 162,12,34816\n"
"Mvaa 112,228,140\ns_add_u32 m0, s47, -32768\ns_add_u32 s45, s45, 128\nMvaa 112,224,136\nTa 164,12,49152\nMvaa 144,224,144\nTa 166,12,51200\nv_lshlrev_b32 v51, 16, v45\n"
"Mvaa 144,228,148\nD0 2,20,45,0\nMU 52,48,50\nMvaa 176,228,156\nTa 168,13,32768\nMvaa 176,224,152\nTa 170,13,34816\nMU 53,49,50\nMvaa 180,232,152\nCV 52,52,53\n"
"v_and_b32 v53, 0xffff0000, v52\nMvaa 180,236,156\nTa 172,13,49152\nMvaa 148,236,148\nTa 174,13,51200\nv_lshlrev_b32 v52, 16, v52\nMvaa 148,232,144\nFM 56,48,51\nMvaa 116,232,136\n"
"Ta 176,14,32768\nFM 57,49,51\nMvaa 116,236,140\nTa 178,14,34816\nAD 54,54,52\nMvaa 84,236,132\nD0 3,20,45,1024\nMvaa 84,232,128\nTa 180,14,49152\nAD 55,55,53\nMvaa 88,240,128\n"
"Ta 182,14,51200\nv_and_b32 v48, 0xffff0000, v33\nMvaa 88,244,132\nv_and_b32 v49, 0xffff0000, v37\nMvaa 120,244,140\nTa 184,15,32768\nv_and_b32 v50, 0xffff0000, v41\n"
"Mvaa 120,240,136\nTa 186,15,34816\nMvaa 152,240,144\nv_and_b32 v51, 0xffff0000, v45\nMU 52,48,50\nMvaa 152,244,148\nTa 188,15,49152\nMU 53,49,50\nMvaa 184,244,156\n"
"Ta 190,15,51200\nMvaa 184,240,152\nD0 4,20,45,2048\nCV 52,52,53\nMvaa 188,248,152\nv_and_b32 v53, 0xffff0000, v52\nv_lshlrev_b32 v52, 16, v52\nMvaa 188,252,156\nFM 56,48,51\n"
"Mvaa 156,252,148\nFM 57,49,51\nAD 54,54,52\nMvaa 156,248,144\nAD 55,55,53\nMvaa 124,248,136\nv_lshlrev_b32 v48, 16, v34\nv_lshlrev_b32 v49, 16, v38\nMvaa 124,252,140\n"
"D0 5,20,45,3072\nv_lshlrev_b32 v50, 16, v42\nMvaa 92,252,132\nv_lshlrev_b32 v51, 16, v46\nMvaa 92,248,128\ns_waitcnt vmcnt(18) lgkmcnt(0)\ns_barrier\nMvaa 64,160,128\n"
"Ra 192,10,49152\nMU 52,48,50\nMvaa 64,164,132\ns_add_u32 m0, s47, 32768\ns_add_u32 s44, s44, 0x40000\nMvaa 96,164,140\nRa 196,11,49152\nMU 53,49,50\nMvaa 96,160,136\nCV 52,52,53\n"
"Mvaa 128,160,144\nRa 200,10,51200\nv_and_b32 v53, 0xffff0000, v52\nMvaa 128,164,148\nD0 6,24,44,0\nv_lshlrev_b32 v52, 16, v52\nMvaa 160,164,156\nRa 204,11,51200\nMvaa 160,160,152\n"
"FM 56,48,51\nFM 57,49,51\nMvaa 164,168,152\nRa 208,10,57344\nAD 54,54,52\nMvaa 164,172,156\nAD 55,55,53\nMvaa 132,172,148\nRa 212,11,57344\nv_and_b32 v48, 0xffff0000, v34\n"
"Mvaa 132,168,144\nv_and_b32 v49, 0xffff0000, v38\nMvaa 100,168,136\nRa 216,10,59392\nv_and_b32 v50, 0xffff0000, v42\nMvaa 100,172,140\nD0 7,24,44,1024\n"
"v_and_b32 v51, 0xffff0000, v46\nMvaa 68,172,132\nRa 220,11,59392\nMvaa 68,168,128\nMU 52,48,50\nMU 53,49,50\nMvaa 72,176,128\nCV 52,52,53\nv_and_b32 v53, 0xffff0000, v52\n"
"Mvaa 72,180,132\nv_lshlrev_b32 v52, 16, v52\nMvaa 104,180,140\nFM 56,48,51\nFM 57,49,51\nMvaa 104,176,136\nAD 54,54,52\nMvaa 136,176,144\nAD 55,55,53\nv_lshlrev_b32 v48, 16, v35\n"
"Mvaa 136,180,148\nD0 8,24,44,2048\nv_lshlrev_b32 v49, 16, v39\nMvaa 168,180,156\nv_lshlrev_b32 v50, 16, v43\nMvaa 168,176,152\nv_lshlrev_b32 v51, 16, v47\nMU 52,48,50\n"
"Mvaa 172,184,152\nMU 53,49,50\nCV 52,52,53\nMvaa 172,188,156\nv_and_b32 v53, 0xffff0000, v52\nMvaa 140,188,148\nv_lshlrev_b32 v52, 16, v52\nFM 56,48,51\nMvaa 140,184,144\n"
"FM 57,49,51\nMvaa 108,184,136\nAD 54,54,52\nAD 55,55,53\nMvaa 108,188,140\nD0 9,24,44,3072\nv_and_b32 v48, 0xffff0000, v35\nMvaa 76,188,132\nv_and_b32 v49, 0xffff0000, v39\n"
"Mvaa 76,184,128\nWL 0\nMaaa 0,160,192\nRa 128,10,0\nv_and_b32 v50, 0xffff0000, v43\nMaaa 0,164,196\ns_add_u32 m0, s47, 49152\ns_add_u32 s44, s44, 0x40000\nMaaa 32,164,204\n"
"Ra 132,11,0\nv_and_b32 v51, 0xffff0000, v47\nMaaa 32,160,200\nMU 52,48,50\nMaaa 64,160,208\nRa 136,10,2048\nMU 53,49,50\nMaaa 64,164,212\nD0 6,24,44,0\nCV 52,52,53\n"
"Maaa 96,164,220\nRa 140,11,2048\nMaaa 96,160,216\nv_and_b32 v53, 0xffff0000, v52\nv_lshlrev_b32 v52, 16, v52\nMaaa 100,168,216\nRa 144,10,8192\nFM 56,48,51\nMaaa 100,172,220\n"
"FM 57,49,51\nMaaa 68,172,212\nRa 148,11,8192\nAD 54,54,52\nMaaa 68,168,208\nAD 55,55,53\nMaaa 36,168,200\nRa 152,10,10240\nv_permlane32_swap_b32 v54, v55\nMaaa 36,172,204\n"
"D0 7,24,44,1024\nv_permlane32_swap_b32 v56, v57\nMaaa 4,172,196\nRa 156,11,10240\nMaaa 4,168,192\nAD 54,54,55\nAD 56,56,57\nMaaa 8,176,192\nMaaa 8,180,196\n"
"v_permlane16_swap_b32 v54, v56\nMaaa 40,180,204\nAD 54,54,56\nMaaa 40,176,200\nMaaa 72,176,208\nv_add_f32_dpp v54, v54, v54 row_ror:8 row_mask:0xf bank_mask:0xf\nMaaa 72,180,212\n"
"D0 8,24,44,2048\nMaaa 104,180,220\nv_add_f32_dpp v54, v54, v54 row_ror:4 row_mask:0xf bank_mask:0xf\nMaaa 104,176,216\nMaaa 108,184,216\n"
"v_add_f32_dpp v54, v54, v54 row_ror:2 row_mask:0xf bank_mask:0xf\nMaaa 108,188,220\nMaaa 76,188,212\nv_add_f32_dpp v54, v54, v54 row_ror:1 row_mask:0xf bank_mask:0xf\nCV 54,54,54\n"
"Maaa 76,184,208\nv_lshlrev_b32 v54, 16, v54\nMaaa 44,184,200\nv_mul_f32 v55, 0xbfb8aa3b, v54\nMaaa 44,188,204\nD0 9,24,44,3072\nMaaa 12,188,196\nEX 55,55\nMaaa 12,184,192\n"
"s_waitcnt vmcnt(12) lgkmcnt(0)\ns_barrier\nMaaa 16,224,192\nTa 160,12,0\nv_add_f32 v56, 0, v54\nMaaa 16,228,196\nTa 162,12,2048\nMaaa 48,228,204\ns_add_u32 m0, s47, -16384\n"
"s_add_u32 s48, s48, 128\nMaaa 48,224,200\nTa 164,12,16384\nMaaa 80,224,208\nTa 166,12,18432\nbuffer_load_dword v31, v24, s[24:27], s96 offen\ns_add_u32 s96, s96, 0x80000\n"
"Maaa 80,228,212\nD0 2,20,48,0\nv_add_f32 v55, 1.0, v55\nMaaa 112,228,220\nTa 168,13,0\nv_rcp_f32 v55, v55\nMaaa 112,224,216\nTa 170,13,2048\nv_ashrrev_i32 v57, 31, v56\n"
"Maaa 116,232,216\nCV 55,55,55\nv_lshlrev_b32 v55, 16, v55\nMaaa 116,236,220\nTa 172,13,16384\nMaaa 84,236,212\nTa 174,13,18432\nv_or_b32 v57, 0x80000000, v57\nMaaa 84,232,208\n"
"v_xor_b32 v56, v56, v57\nMaaa 52,232,200\nTa 176,14,0\nv_and_b32 v56, 0xffff0000, v56\nMaaa 52,236,204\nTa 178,14,2048\ns_lshl2_add_u32 s2, s10, s11\nMaaa 20,236,196\n"
"D0 3,20,48,1024\nMaaa 20,232,192\nTa 180,14,16384\nv_lshrrev_b32 v48, 5, v0\nMaaa 24,240,192\nTa 182,14,18432\nv_lshl_add_u32 v48, s2, 1, v48\nMaaa 24,244,196\n"
"v_sub_u32 v49, 0xffff, v48\nMaaa 56,244,204\nTa 184,15,0\nv_or_b32 v56, v56, v49\nMaaa 56,240,200\nTa 186,15,2048\nMaaa 88,240,208\nv_mov_b32 v57, s15\nv_and_b32 v49, 31, v0\n"
"Maaa 88,244,212\nTa 188,15,16384\nv_mov_b32 v52, 0x40000000\nMaaa 120,244,220\nTa 190,15,18432\nv_cmp_eq_u32 vcc, 16, v49\nv_lshlrev_b32 v50, 2, v48\nv_lshlrev_b32 v51, 10, v49\n"
"v_cndmask_b32 v50, v52, v50, vcc\nv_cndmask_b32 v56, v55, v56, vcc\nMaaa 120,240,216\nD0 4,20,48,2048\nv_lshl_add_u32 v51, v48, 3, v51\nMaaa 124,248,216\nv_and_b32 v49, 15, v0\n"
"v_cmp_eq_u32 vcc, 0, v49\ns_mov_b32 s2, 0xc040000\ns_mov_b32 s3, 0xc03e000\nv_cndmask_b32 v51, v52, v51, vcc\nbuffer_store_dwordx2 v[56:57], v51, s[16:19], s2 offen sc0 sc1\n"
"buffer_store_dword v54, v50, s[16:19], s3 offen sc0 sc1\nMaaa 124,252,220\nMaaa 92,252,212\nMaaa 92,248,208\nMaaa 60,248,200\nMaaa 60,252,204\nD0 5,20,48,3072\nMaaa 28,252,196\n"
"Maaa 28,248,192\nWL 0\ns_mov_b32 s50, 3\n.GL269:\nMvaa 64,160,128\nTa 224,16,0\nMvaa 64,164,132\nTa 226,16,2048\nMvaa 96,164,140\nTa 228,16,16384\ns_add_u32 m0, s47, -65536\n"
"s_add_u32 s45, s45, 128\nMvaa 96,160,136\nTa 230,16,18432\nMvaa 128,160,144\nTa 232,17,0\nMvaa 128,164,148\nTa 234,17,2048\nD0 2,20,45,0\nMvaa 160,164,156\nTa 236,17,16384\n"
"Mvaa 160,160,152\nTa 238,17,18432\nMvaa 164,168,152\nTa 240,18,0\nMvaa 164,172,156\nTa 242,18,2048\nMvaa 132,172,148\nTa 244,18,16384\nMvaa 132,168,144\nTa 246,18,18432\n"
"Mvaa 100,168,136\nTa 248,19,0\nMvaa 100,172,140\nTa 250,19,2048\nMvaa 68,172,132\nTa 252,19,16384\nD0 3,20,45,1024\nMvaa 68,168,128\nTa 254,19,18432\nMvaa 72,176,128\n"
"Ra 192,10,16384\nMvaa 72,180,132\nRa 196,11,16384\nMvaa 104,180,140\nRa 200,10,18432\nMvaa 104,176,136\nRa 204,11,18432\nMvaa 136,176,144\nRa 208,10,24576\nMvaa 136,180,148\n"
"Ra 212,11,24576\nMvaa 168,180,156\nRa 216,10,26624\nMvaa 168,176,152\nRa 220,11,26624\nD0 4,20,45,2048\nMvaa 172,184,152\nMvaa 172,188,156\nMvaa 140,188,148\nMvaa 140,184,144\n"
"Mvaa 108,184,136\nMvaa 108,188,140\nD0 5,20,45,3072\nMvaa 76,188,132\nMvaa 76,184,128\ns_waitcnt vmcnt(18) lgkmcnt(8)\ns_barrier\nMvaa 80,224,128\nMvaa 80,228,132\n"
"s_add_u32 m0, s47, 0\ns_add_u32 s44, s44, 0x40000\ns_mov_b32 s86, 1\ns_load_dword s86, s[54:55], s94\nMvaa 112,228,140\ns_add_u32 s94, s94, 1024\nMvaa 112,224,136\n"
"Mvaa 144,224,144\nMvaa 144,228,148\nD0 6,24,44,0\nMvaa 176,228,156\nMvaa 176,224,152\nMvaa 180,232,152\nMvaa 180,236,156\nMvaa 148,236,148\nMvaa 148,232,144\n"
"s_add_u32 s88, s88, s86\nMvaa 116,232,136\nMvaa 116,236,140\nD0 7,24,44,1024\ns_add_u32 s88, s88, s86\nMvaa 84,236,132\nMvaa 84,232,128\ns_add_u32 s88, s88, s86\nMvaa 88,240,128\n"
"Mvaa 88,244,132\ns_add_u32 s88, s88, s86\nMvaa 120,244,140\nMvaa 120,240,136\ns_add_u32 s88, s88, s86\nMvaa 152,240,144\nMvaa 152,244,148\nD0 8,24,44,2048\n"
"s_add_u32 s88, s88, s86\nMvaa 184,244,156\nMvaa 184,240,152\ns_add_u32 s88, s88, s86\nMvaa 188,248,152\nMvaa 188,252,156\ns_add_u32 s88, s88, s86\nMvaa 156,252,148\n"
"Mvaa 156,248,144\ns_add_u32 s88, s88, s86\nMvaa 124,248,136\nMvaa 124,252,140\nD0 9,24,44,3072\ns_add_u32 s88, s88, s86\nMvaa 92,252,132\nMvaa 92,248,128\nWL 0\n"
"s_add_u32 s88, s88, s86\nMaaa 16,224,192\nRa 128,10,32768\nMaaa 16,228,196\ns_add_u32 m0, s47, 16384\ns_add_u32 s44, s44, 0x40000\ns_add_u32 s88, s88, s86\nMaaa 48,228,204\n"
"Ra 132,11,32768\nMaaa 48,224,200\ns_add_u32 s88, s88, s86\nMaaa 80,224,208\nRa 136,10,34816\nMaaa 80,228,212\nD0 6,24,44,0\ns_add_u32 s88, s88, s86\nMaaa 112,228,220\n"
"Ra 140,11,34816\nMaaa 112,224,216\ns_add_u32 s88, s88, s86\nMaaa 116,232,216\nRa 144,10,40960\nMaaa 116,236,220\ns_add_u32 s88, s88, s86\nMaaa 84,236,212\nRa 148,11,40960\n"
"Maaa 84,232,208\nMaaa 52,232,200\nRa 152,10,43008\nMaaa 52,236,204\nD0 7,24,44,1024\nMaaa 20,236,196\nRa 156,11,43008\nMaaa 20,232,192\nMaaa 24,240,192\nMaaa 24,244,196\n"
"Maaa 56,244,204\nMaaa 56,240,200\nMaaa 88,240,208\nMaaa 88,244,212\nD0 8,24,44,2048\nMaaa 120,244,220\nMaaa 120,240,216\nMaaa 124,248,216\nMaaa 124,252,220\nMaaa 92,252,212\n"
"Maaa 92,248,208\nMaaa 60,248,200\nMaaa 60,252,204\nD0 9,24,44,3072\nMaaa 28,252,196\nMaaa 28,248,192\ns_waitcnt vmcnt(12) lgkmcnt(0)\ns_barrier\nMaaa 0,160,192\nTa 224,16,32768\n"
"s_mov_b32 m0, s76\ns_add_u32 s76, s76, 1024\nMaaa 0,164,196\nTa 226,16,34816\nD0 1,36,75,0\ns_add_u32 s75, s75, 1024\nMaaa 32,164,204\ns_add_u32 m0, s47, -49152\n"
"s_add_u32 s48, s48, 128\nMaaa 32,160,200\nTa 228,16,49152\nMaaa 64,160,208\nTa 230,16,51200\nbuffer_load_dword v31, v24, s[24:27], s96 offen\ns_add_u32 s96, s96, 0x80000\n"
"Maaa 64,164,212\nD0 2,20,48,0\nMaaa 96,164,220\nTa 232,17,32768\nMaaa 96,160,216\nTa 234,17,34816\nMaaa 100,168,216\nMaaa 100,172,220\nTa 236,17,49152\nMaaa 68,172,212\n"
"Ta 238,17,51200\nMaaa 68,168,208\nMaaa 36,168,200\nTa 240,18,32768\nMaaa 36,172,204\nTa 242,18,34816\nMaaa 4,172,196\nD0 3,20,48,1024\nMaaa 4,168,192\nTa 244,18,49152\n"
"Maaa 8,176,192\nTa 246,18,51200\nMaaa 8,180,196\nMaaa 40,180,204\nTa 248,19,32768\nMaaa 40,176,200\nTa 250,19,34816\nMaaa 72,176,208\nMaaa 72,180,212\nTa 252,19,49152\n"
"Maaa 104,180,220\nTa 254,19,51200\nMaaa 104,176,216\nD0 4,20,48,2048\nMaaa 108,184,216\nMaaa 108,188,220\nMaaa 76,188,212\nMaaa 76,184,208\nMaaa 44,184,200\nMaaa 44,188,204\n"
"D0 5,20,48,3072\nMaaa 12,188,196\nMaaa 12,184,192\nWL 0\nMvaa 80,224,128\nTa 160,12,32768\nMvaa 80,228,132\nTa 162,12,34816\nMvaa 112,228,140\nTa 164,12,49152\n"
"s_add_u32 m0, s47, -32768\ns_add_u32 s45, s45, 128\nMvaa 112,224,136\nTa 166,12,51200\nMvaa 144,224,144\nTa 168,13,32768\nMvaa 144,228,148\nTa 170,13,34816\nD0 2,20,45,0\n"
"Mvaa 176,228,156\nTa 172,13,49152\nMvaa 176,224,152\nTa 174,13,51200\nMvaa 180,232,152\nTa 176,14,32768\nMvaa 180,236,156\nTa 178,14,34816\nMvaa 148,236,148\nTa 180,14,49152\n"
"Mvaa 148,232,144\nTa 182,14,51200\nMvaa 116,232,136\nTa 184,15,32768\nMvaa 116,236,140\nTa 186,15,34816\nMvaa 84,236,132\nTa 188,15,49152\nD0 3,20,45,1024\nMvaa 84,232,128\n"
"Ta 190,15,51200\nMvaa 88,240,128\nRa 192,10,49152\nMvaa 88,244,132\nRa 196,11,49152\nMvaa 120,244,140\nRa 200,10,51200\nMvaa 120,240,136\nRa 204,11,51200\nMvaa 152,240,144\n"
"Ra 208,10,57344\nMvaa 152,244,148\nRa 212,11,57344\nMvaa 184,244,156\nRa 216,10,59392\nMvaa 184,240,152\nRa 220,11,59392\nD0 4,20,45,2048\nMvaa 188,248,152\nMvaa 188,252,156\n"
"Mvaa 156,252,148\nMvaa 156,248,144\nMvaa 124,248,136\nMvaa 124,252,140\nD0 5,20,45,3072\nMvaa 92,252,132\nMvaa 92,248,128\ns_waitcnt vmcnt(18) lgkmcnt(8)\ns_barrier\n"
"Mvaa 64,160,128\nMvaa 64,164,132\ns_add_u32 m0, s47, 32768\ns_add_u32 s44, s44, 0x40000\ns_mov_b32 s86, 1\ns_load_dword s86, s[54:55], s95\nMvaa 96,164,140\n"
"s_add_u32 s95, s95, 1024\nMvaa 96,160,136\nMvaa 128,160,144\nMvaa 128,164,148\nD0 6,24,44,0\nMvaa 160,164,156\nMvaa 160,160,152\nMvaa 164,168,152\nMvaa 164,172,156\n"
"Mvaa 132,172,148\nMvaa 132,168,144\ns_sub_u32 s88, s88, s86\nMvaa 100,168,136\nMvaa 100,172,140\nD0 7,24,44,1024\ns_sub_u32 s88, s88, s86\nMvaa 68,172,132\nMvaa 68,168,128\n"
"s_sub_u32 s88, s88, s86\nMvaa 72,176,128\nMvaa 72,180,132\ns_sub_u32 s88, s88, s86\nMvaa 104,180,140\nMvaa 104,176,136\ns_sub_u32 s88, s88, s86\nMvaa 136,176,144\n"
"Mvaa 136,180,148\nD0 8,24,44,2048\ns_sub_u32 s88, s88, s86\nMvaa 168,180,156\nMvaa 168,176,152\ns_sub_u32 s88, s88, s86\nMvaa 172,184,152\nMvaa 172,188,156\n"
"s_sub_u32 s88, s88, s86\nMvaa 140,188,148\nMvaa 140,184,144\ns_sub_u32 s88, s88, s86\nMvaa 108,184,136\nMvaa 108,188,140\nD0 9,24,44,3072\ns_sub_u32 s88, s88, s86\n"
"Mvaa 76,188,132\nMvaa 76,184,128\nWL 0\ns_sub_u32 s88, s88, s86\nMaaa 0,160,192\nRa 128,10,0\nMaaa 0,164,196\ns_add_u32 m0, s47, 49152\ns_add_u32 s44, s44, 0x40000\n"
"s_sub_u32 s88, s88, s86\nMaaa 32,164,204\nRa 132,11,0\nMaaa 32,160,200\ns_sub_u32 s88, s88, s86\nMaaa 64,160,208\nRa 136,10,2048\nMaaa 64,164,212\nD0 6,24,44,0\n"
"s_sub_u32 s88, s88, s86\nMaaa 96,164,220\nRa 140,11,2048\nMaaa 96,160,216\ns_sub_u32 s88, s88, s86\nMaaa 100,168,216\nRa 144,10,8192\nMaaa 100,172,220\ns_sub_u32 s88, s88, s86\n"
"Maaa 68,172,212\nRa 148,11,8192\nMaaa 68,168,208\nMaaa 36,168,200\nRa 152,10,10240\nMaaa 36,172,204\nD0 7,24,44,1024\nMaaa 4,172,196\nRa 156,11,10240\nMaaa 4,168,192\n"
"Maaa 8,176,192\nMaaa 8,180,196\nMaaa 40,180,204\nMaaa 40,176,200\nMaaa 72,176,208\nMaaa 72,180,212\nD0 8,24,44,2048\nMaaa 104,180,220\nMaaa 104,176,216\nMaaa 108,184,216\n"
"Maaa 108,188,220\nMaaa 76,188,212\nMaaa 76,184,208\nMaaa 44,184,200\nMaaa 44,188,204\nD0 9,24,44,3072\nMaaa 12,188,196\nMaaa 12,184,192\ns_waitcnt vmcnt(12) lgkmcnt(0)\ns_barrier\n"
"Maaa 16,224,192\nTa 160,12,0\ns_mov_b32 m0, s76\ns_add_u32 s76, s76, 1024\nMaaa 16,228,196\nTa 162,12,2048\nD0 1,36,75,0\ns_add_u32 s75, s75, 1024\nMaaa 48,228,204\n"
"s_add_u32 m0, s47, -16384\ns_add_u32 s48, s48, 128\nMaaa 48,224,200\nTa 164,12,16384\nMaaa 80,224,208\nTa 166,12,18432\nbuffer_load_dword v31, v24, s[24:27], s96 offen\n"
"s_add_u32 s96, s96, 0x80000\nMaaa 80,228,212\nD0 2,20,48,0\nMaaa 112,228,220\nTa 168,13,0\nMaaa 112,224,216\nTa 170,13,2048\nMaaa 116,232,216\nMaaa 116,236,220\nTa 172,13,16384\n"
"Maaa 84,236,212\nTa 174,13,18432\nMaaa 84,232,208\nMaaa 52,232,200\nTa 176,14,0\nMaaa 52,236,204\nTa 178,14,2048\nMaaa 20,236,196\nD0 3,20,48,1024\nMaaa 20,232,192\n"
"Ta 180,14,16384\nMaaa 24,240,192\nTa 182,14,18432\nMaaa 24,244,196\nMaaa 56,244,204\nTa 184,15,0\nMaaa 56,240,200\nTa 186,15,2048\nMaaa 88,240,208\nMaaa 88,244,212\n"
"Ta 188,15,16384\nMaaa 120,244,220\nTa 190,15,18432\nMaaa 120,240,216\nD0 4,20,48,2048\nMaaa 124,248,216\nMaaa 124,252,220\nMaaa 92,252,212\nMaaa 92,248,208\nMaaa 60,248,200\n"
"s_xor_b32 s94, s94, 0x2000\ns_xor_b32 s95, s95, 0x2000\ns_sub_u32 s88, 0, s88\nMaaa 60,252,204\nD0 5,20,48,3072\ns_sub_u32 s50, s50, 1\nMaaa 28,252,196\nMaaa 28,248,192\nWL 0\n"
"s_cbranch_scc0 .GL269\nMvaa 64,160,128\nTa 224,16,0\nMvaa 64,164,132\nTa 226,16,2048\ns_cmp_gt_i32 s88, 0\ns_cselect_b32 s88, 0x2000, 0\nMvaa 96,164,140\n"
"s_add_u32 m0, s47, -65536\ns_add_u32 s45, s45, 128\nMvaa 96,160,136\nTa 228,16,16384\nMvaa 128,160,144\nTa 230,16,18432\nMvaa 128,164,148\nD0 2,20,45,0\nMvaa 160,164,156\n"
"Ta 232,17,0\nMvaa 160,160,152\nTa 234,17,2048\nMvaa 164,168,152\ns_xor_b32 s88, s88, s92\nMvaa 164,172,156\nTa 236,17,16384\nMvaa 132,172,148\nTa 238,17,18432\nMvaa 132,168,144\n"
"v_xor_b32 v27, s88, v25\nMvaa 100,168,136\nTa 240,18,0\nMvaa 100,172,140\nTa 242,18,2048\nMvaa 68,172,132\nD0 3,20,45,1024\nMvaa 68,168,128\nTa 244,18,16384\nMvaa 72,176,128\n"
"Ta 246,18,18432\nMvaa 72,180,132\nbuffer_store_dword v27, off, s[16:19], s93 sc0 sc1\nMvaa 104,180,140\nTa 248,19,0\nMvaa 104,176,136\nTa 250,19,2048\nMvaa 136,176,144\n"
"Mvaa 136,180,148\nTa 252,19,16384\nMvaa 168,180,156\nTa 254,19,18432\nMvaa 168,176,152\nD0 4,20,45,2048\nMvaa 172,184,152\nMvaa 172,188,156\nMvaa 140,188,148\nMvaa 140,184,144\n"
"Mvaa 108,184,136\nMvaa 108,188,140\nD0 5,20,45,3072\nMvaa 76,188,132\nMvaa 76,184,128\ns_waitcnt vmcnt(19) lgkmcnt(0)\ns_barrier\nMvaa 80,224,128\nRa 192,10,16384\n"
"Mvaa 80,228,132\ns_add_u32 m0, s47, 0\ns_add_u32 s44, s44, 0x40000\nMvaa 112,228,140\nRa 196,11,16384\nMvaa 112,224,136\nMvaa 144,224,144\nRa 200,10,18432\nMvaa 144,228,148\n"
"D0 6,24,44,0\nMvaa 176,228,156\nRa 204,11,18432\nMvaa 176,224,152\nMvaa 180,232,152\nRa 208,10,24576\nMvaa 180,236,156\nMvaa 148,236,148\nRa 212,11,24576\nMvaa 148,232,144\n"
"Mvaa 116,232,136\nRa 216,10,26624\nMvaa 116,236,140\nD0 7,24,44,1024\nMvaa 84,236,132\nRa 220,11,26624\nMvaa 84,232,128\nMvaa 88,240,128\nMvaa 88,244,132\nMvaa 120,244,140\n"
"Mvaa 120,240,136\nMvaa 152,240,144\nMvaa 152,244,148\nD0 8,24,44,2048\nMvaa 184,244,156\nMvaa 184,240,152\nMvaa 188,248,152\nMvaa 188,252,156\nMvaa 156,252,148\nMvaa 156,248,144\n"
"Mvaa 124,248,136\nMvaa 124,252,140\nD0 9,24,44,3072\nMvaa 92,252,132\nMvaa 92,248,128\nWL 0\nMaaa 16,224,192\nRa 128,10,32768\nMaaa 16,228,196\ns_add_u32 m0, s47, 16384\n"
"s_add_u32 s44, s44, 0x40000\nMaaa 48,228,204\nRa 132,11,32768\nMaaa 48,224,200\nMaaa 80,224,208\nRa 136,10,34816\nMaaa 80,228,212\nD0 6,24,44,0\nMaaa 112,228,220\nRa 140,11,34816\n"
"Maaa 112,224,216\nMaaa 116,232,216\nRa 144,10,40960\nMaaa 116,236,220\nMaaa 84,236,212\nRa 148,11,40960\nMaaa 84,232,208\nMaaa 52,232,200\nRa 152,10,43008\nMaaa 52,236,204\n"
"D0 7,24,44,1024\nMaaa 20,236,196\nRa 156,11,43008\nMaaa 20,232,192\nMaaa 24,240,192\nMaaa 24,244,196\nMaaa 56,244,204\nMaaa 56,240,200\nMaaa 88,240,208\nMaaa 88,244,212\n"
"D0 8,24,44,2048\nMaaa 120,244,220\nMaaa 120,240,216\nMaaa 124,248,216\nMaaa 124,252,220\nMaaa 92,252,212\nMaaa 92,248,208\nMaaa 60,248,200\nMaaa 60,252,204\nD0 9,24,44,3072\n"
"Maaa 28,252,196\nMaaa 28,248,192\ns_waitcnt vmcnt(19) lgkmcnt(0)\ns_barrier\nMaaa 0,160,192\nTa 224,16,32768\nMaaa 0,164,196\nTa 226,16,34816\nMaaa 32,164,204\n"
"s_add_u32 m0, s47, -49152\ns_add_u32 s48, s48, 128\nMaaa 32,160,200\nTa 228,16,49152\nMaaa 64,160,208\nTa 230,16,51200\nbuffer_load_dword v31, v24, s[24:27], s96 offen\n"
"s_add_u32 s96, s96, 0x80000\nMaaa 64,164,212\nD0 2,20,48,0\nMaaa 96,164,220\nTa 232,17,32768\nMaaa 96,160,216\nTa 234,17,34816\nMaaa 100,168,216\nMaaa 100,172,220\n"
"Ta 236,17,49152\nMaaa 68,172,212\nTa 238,17,51200\nMaaa 68,168,208\nMaaa 36,168,200\nTa 240,18,32768\nMaaa 36,172,204\nTa 242,18,34816\nMaaa 4,172,196\nD0 3,20,48,1024\n"
"Maaa 4,168,192\nTa 244,18,49152\nMaaa 8,176,192\nTa 246,18,51200\nMaaa 8,180,196\nMaaa 40,180,204\nTa 248,19,32768\nMaaa 40,176,200\nTa 250,19,34816\nMaaa 72,176,208\n"
"Maaa 72,180,212\nTa 252,19,49152\nMaaa 104,180,220\nTa 254,19,51200\nMaaa 104,176,216\nD0 4,20,48,2048\nMaaa 108,184,216\nMaaa 108,188,220\nMaaa 76,188,212\nMaaa 76,184,208\n"
"Maaa 44,184,200\nMaaa 44,188,204\nD0 5,20,48,3072\nMaaa 12,188,196\nMaaa 12,184,192\nWL 0\nMvaa 80,224,128\nTa 160,12,32768\nMvaa 80,228,132\nTa 162,12,34816\nMvaa 112,228,140\n"
"s_add_u32 m0, s47, -32768\ns_add_u32 s45, s45, 128\nMvaa 112,224,136\nTa 164,12,49152\nMvaa 144,224,144\nTa 166,12,51200\nMvaa 144,228,148\nD0 2,20,45,0\nMvaa 176,228,156\n"
"Ta 168,13,32768\nMvaa 176,224,152\nTa 170,13,34816\nMvaa 180,232,152\nMvaa 180,236,156\nTa 172,13,49152\nMvaa 148,236,148\nTa 174,13,51200\nMvaa 148,232,144\nMvaa 116,232,136\n"
"Ta 176,14,32768\nMvaa 116,236,140\nTa 178,14,34816\nMvaa 84,236,132\nD0 3,20,45,1024\nMvaa 84,232,128\nTa 180,14,49152\nMvaa 88,240,128\nTa 182,14,51200\nMvaa 88,244,132\n"
"Mvaa 120,244,140\nTa 184,15,32768\nMvaa 120,240,136\nTa 186,15,34816\nMvaa 152,240,144\nMvaa 152,244,148\nTa 188,15,49152\nMvaa 184,244,156\nTa 190,15,51200\nMvaa 184,240,152\n"
"D0 4,20,45,2048\nMvaa 188,248,152\nMvaa 188,252,156\nMvaa 156,252,148\nMvaa 156,248,144\nMvaa 124,248,136\nMvaa 124,252,140\nD0 5,20,45,3072\nMvaa 92,252,132\nMvaa 92,248,128\n"
"s_waitcnt vmcnt(17) lgkmcnt(0)\ns_barrier\nMvaa 64,160,128\nRa 192,10,49152\nMvaa 64,164,132\ns_add_u32 m0, s47, 32768\ns_add_u32 s44, s44, 0x40000\nMvaa 96,164,140\n"
"Ra 196,11,49152\nMvaa 96,160,136\nMvaa 128,160,144\nRa 200,10,51200\nMvaa 128,164,148\nD0 6,24,44,0\nMvaa 160,164,156\nRa 204,11,51200\nMvaa 160,160,152\nMvaa 164,168,152\n"
"Ra 208,10,57344\nMvaa 164,172,156\ns_lshl_b32 s5, s73, 8\ns_add_u32 s5, s5, 0xc04ac00\nv_lshrrev_b32 v27, 2, v1\nbuffer_load_dword v27, v27, s[16:19], s5 offen sc0 sc1\n"
"Mvaa 132,172,148\nRa 212,11,57344\nMvaa 132,168,144\nMvaa 100,168,136\nRa 216,10,59392\nMvaa 100,172,140\nD0 7,24,44,1024\nMvaa 68,172,132\nRa 220,11,59392\nMvaa 68,168,128\n"
"Mvaa 72,176,128\nMvaa 72,180,132\nMvaa 104,180,140\nMvaa 104,176,136\nMvaa 136,176,144\nMvaa 136,180,148\nD0 8,24,44,2048\nMvaa 168,180,156\nMvaa 168,176,152\nMvaa 172,184,152\n"
"Mvaa 172,188,156\nMvaa 140,188,148\nMvaa 140,184,144\nMvaa 108,184,136\nMvaa 108,188,140\nD0 9,24,44,3072\nMvaa 76,188,132\nMvaa 76,184,128\nWL 0\nMaaa 0,160,192\nRa 128,10,0\n"
"Maaa 0,164,196\ns_add_u32 m0, s47, 49152\ns_add_u32 s44, s44, 0x40000\nMaaa 32,164,204\nRa 132,11,0\nMaaa 32,160,200\nMaaa 64,160,208\nRa 136,10,2048\nMaaa 64,164,212\n"
"D0 6,24,44,0\nMaaa 96,164,220\nRa 140,11,2048\nMaaa 96,160,216\nMaaa 100,168,216\nRa 144,10,8192\nMaaa 100,172,220\nMaaa 68,172,212\nRa 148,11,8192\nMaaa 68,168,208\n"
"Maaa 36,168,200\nRa 152,10,10240\nMaaa 36,172,204\nD0 7,24,44,1024\nMaaa 4,172,196\nRa 156,11,10240\nMaaa 4,168,192\nMaaa 8,176,192\nMaaa 8,180,196\nMaaa 40,180,204\n"
"Maaa 40,176,200\nMaaa 72,176,208\nMaaa 72,180,212\nD0 8,24,44,2048\nMaaa 104,180,220\nMaaa 104,176,216\nMaaa 108,184,216\nMaaa 108,188,220\nMaaa 76,188,212\nMaaa 76,184,208\n"
"Maaa 44,184,200\nMaaa 44,188,204\nD0 9,24,44,3072\nMaaa 12,188,196\nMaaa 12,184,192\ns_waitcnt vmcnt(18) lgkmcnt(0)\ns_barrier\nMaaa 16,224,192\nTa 160,12,0\nMaaa 16,228,196\n"
"Ta 162,12,2048\nMaaa 48,228,204\ns_add_u32 m0, s47, -16384\ns_add_u32 s48, s48, 128\nMaaa 48,224,200\nTa 164,12,16384\nMaaa 80,224,208\nTa 166,12,18432\n"
"buffer_load_dword v31, v24, s[24:27], s96 offen\ns_add_u32 s96, s96, 0x80000\nMaaa 80,228,212\nD0 2,20,48,0\nMaaa 112,228,220\nTa 168,13,0\nMaaa 112,224,216\nTa 170,13,2048\n"
"Maaa 116,232,216\nMaaa 116,236,220\nTa 172,13,16384\nMaaa 84,236,212\nTa 174,13,18432\nMaaa 84,232,208\nMaaa 52,232,200\nTa 176,14,0\nMaaa 52,236,204\nTa 178,14,2048\n"
"Maaa 20,236,196\nD0 3,20,48,1024\nMaaa 20,232,192\nTa 180,14,16384\nMaaa 24,240,192\nTa 182,14,18432\nMaaa 24,244,196\nMaaa 56,244,204\nTa 184,15,0\nMaaa 56,240,200\n"
"Ta 186,15,2048\nMaaa 88,240,208\nMaaa 88,244,212\nTa 188,15,16384\nMaaa 120,244,220\nTa 190,15,18432\nMaaa 120,240,216\nD0 4,20,48,2048\nMaaa 124,248,216\nMaaa 124,252,220\n"
"Maaa 92,252,212\nMaaa 92,248,208\nMaaa 60,248,200\nMaaa 60,252,204\nD0 5,20,48,3072\nMaaa 28,252,196\nMaaa 28,248,192\nWL 0\nMvaa 64,160,128\nTa 224,16,0\nWV 12\n"
"v_xor_b32 v27, s15, v27\nv_and_b32 v32, 0xffffdfff, v27\nv_cmp_ne_u32 vcc, 0, v32\ns_nop 0\ns_cbranch_vccnz .S266\n.S267:\nv_cmp_ne_u32 vcc, 0, v27\ns_nop 3\n"
"s_bcnt1_i32_b64 s4, vcc\ns_cmp_gt_u32 s4, 32\ns_cselect_b32 s4, 1, 0\nMvaa 64,164,132\nTa 226,16,2048\nMvaa 96,164,140\ns_add_u32 m0, s47, -65536\ns_add_u32 s45, s45, 128\n"
"Mvaa 96,160,136\nTa 228,16,16384\nMvaa 128,160,144\nTa 230,16,18432\nMvaa 128,164,148\nD0 2,20,45,0\nMvaa 160,164,156\nTa 232,17,0\nMvaa 160,160,152\nTa 234,17,2048\n"
"Mvaa 164,168,152\ns_xor_b32 s4, s4, s12\nMvaa 164,172,156\nTa 236,17,16384\nMvaa 132,172,148\nTa 238,17,18432\nMvaa 132,168,144\ns_and_b32 s4, s4, 1\nMvaa 100,168,136\n"
"Ta 240,18,0\nMvaa 100,172,140\nTa 242,18,2048\nMvaa 68,172,132\nD0 3,20,45,1024\nMvaa 68,168,128\nTa 244,18,16384\nMvaa 72,176,128\nTa 246,18,18432\nMvaa 72,180,132\n"
"s_lshl_b32 s4, s4, 13\nMvaa 104,180,140\nTa 248,19,0\nMvaa 104,176,136\nTa 250,19,2048\nMvaa 136,176,144\ns_mul_i32 s72, s73, 25165824\nMvaa 136,180,148\nTa 252,19,16384\n"
"Mvaa 168,180,156\nTa 254,19,18432\nMvaa 168,176,152\nD0 4,20,45,2048\nMvaa 172,184,152\ns_add_u32 s72, s72, s4\nMvaa 172,188,156\ns_and_b32 s4, s12, 1\nMvaa 140,188,148\n"
"s_lshl_b32 s4, s4, 21\nMvaa 140,184,144\ns_add_u32 s72, s72, s4\nMvaa 108,184,136\ns_lshl3_add_u32 s2, s13, s11\nMvaa 108,188,140\nD0 5,20,45,3072\nMvaa 76,188,132\n"
"s_lshl_b32 s2, s2, 11\nMvaa 76,184,128\nWL 0\ns_barrier\nMvaa 80,224,128\nRa 192,10,16384\nMvaa 80,228,132\ns_add_u32 m0, s47, 0\ns_add_u32 s44, s44, 0x40000\nMvaa 112,228,140\n"
"Ra 196,11,16384\nMvaa 112,224,136\ns_lshl_b32 s3, s14, 22\nMvaa 144,224,144\nRa 200,10,18432\nMvaa 144,228,148\nD0 6,24,44,0\nMvaa 176,228,156\nRa 204,11,18432\nMvaa 176,224,152\n"
"s_add_u32 s2, s2, s3\nMvaa 180,232,152\nRa 208,10,24576\nMvaa 180,236,156\ns_add_u32 s2, s2, s72\nMvaa 148,236,148\nRa 212,11,24576\nMvaa 148,232,144\ns_add_u32 s49, s2, 0xc00000\n"
"Mvaa 116,232,136\nRa 216,10,26624\nMvaa 116,236,140\nD0 7,24,44,1024\nMvaa 84,236,132\nRa 220,11,26624\nMvaa 84,232,128\nMvaa 88,240,128\nMvaa 88,244,132\nMvaa 120,244,140\n"
"Mvaa 120,240,136\nMvaa 152,240,144\nMvaa 152,244,148\nD0 8,24,44,2048\nMvaa 184,244,156\nMvaa 184,240,152\nMvaa 188,248,152\nMvaa 188,252,156\nMvaa 156,252,148\nMvaa 156,248,144\n"
"Mvaa 124,248,136\nMvaa 124,252,140\nD0 9,24,44,3072\nMvaa 92,252,132\nMvaa 92,248,128\nWL 0\nMaaa 16,224,192\nRa 128,10,32768\ns_mov_b32 s4, s13\nMaaa 16,228,196\n"
"s_add_u32 m0, s47, 16384\ns_add_u32 s44, s44, 0x40000\nMaaa 48,228,204\nRa 132,11,32768\nv_lshrrev_b32 v32, 2, v0\nMaaa 48,224,200\nv_mul_u32_u24 v32, 896, v32\nMaaa 80,224,208\n"
"Ra 136,10,34816\nv_and_b32 v33, 3, v0\nMaaa 80,228,212\nD0 6,24,44,0\nMaaa 112,228,220\nRa 140,11,34816\nv_lshl_add_u32 v32, v33, 4, v32\nMaaa 112,224,216\ns_lshr_b32 s2, s11, 1\n"
"Maaa 116,232,216\nRa 144,10,40960\ns_mul_i32 s3, s2, 448\nMaaa 116,236,220\ns_and_b32 s5, s11, 1\nMaaa 84,236,212\nRa 148,11,40960\ns_lshl3_add_u32 s3, s5, s3\nMaaa 84,232,208\n"
"s_add_u32 s3, s3, 131072\nMaaa 52,232,200\nRa 152,10,43008\nv_add_u32 v32, s3, v32\nMaaa 52,236,204\nD0 7,24,44,1024\nMaaa 20,236,196\nRa 156,11,43008\nTv 36,32,0\n"
"Maaa 20,232,192\nTv 38,32,14336\nMaaa 24,240,192\nTv 40,32,64\nTv 42,32,14400\nMaaa 24,244,196\nTv 44,32,128\nMaaa 56,244,204\nTv 46,32,14464\nTv 48,32,192\nMaaa 56,240,200\n"
"Tv 50,32,14528\nMaaa 88,240,208\nTv 52,32,256\nTv 54,32,14592\nMaaa 88,244,212\nD0 8,24,44,2048\nMaaa 120,244,220\nTv 56,32,320\nTv 58,32,14656\nMaaa 120,240,216\nTv 60,32,384\n"
"Maaa 124,248,216\nTv 62,32,14720\ns_lshr_b32 s0, s4, 1\nMaaa 124,252,220\ns_mul_i32 s0, s0, 12\nMaaa 92,252,212\ns_lshl1_add_u32 s0, s5, s0\ns_and_b32 s6, s4, 1\nMaaa 92,248,208\n"
"s_add_u32 s0, s0, s6\nMaaa 60,248,200\ns_lshl3_add_u32 s1, s5, s4\ns_add_u32 s77, s1, 48\nMaaa 60,252,204\nD0 9,24,44,3072\nMaaa 28,252,196\ns_and_b32 s4, s12, 1\n"
"s_mov_b32 s7, 0x6543210\nMaaa 28,248,192\ns_waitcnt vmcnt(17) lgkmcnt(14)\ns_barrier\nMaaa 0,160,192\nTa 224,16,32768\ns_cmp_lg_u32 s4, s2\ns_cselect_b32 s6, s0, s1\n"
"s_cselect_b32 s5, 4, 16\ns_cselect_b32 s7, 0x6352410, s7\ns_cselect_b32 s3, 0, 229376\nMaaa 0,164,196\nTa 226,16,34816\nMaaa 32,164,204\ns_add_u32 m0, s47, -49152\n"
"s_add_u32 s48, s48, 128\nMaaa 32,160,200\nTa 228,16,49152\nMaaa 64,160,208\nTa 230,16,51200\ns_add_u32 s3, s3, s72\nMaaa 64,164,212\nD0 2,20,48,0\nMaaa 96,164,220\n"
"Ta 232,17,32768\ns_add_u32 s3, s3, 0x1400000\nMaaa 96,160,216\nTa 234,17,34816\nMaaa 100,168,216\nWL 14\ns_lshr_b32 s0, s6, 4\nMaaa 100,172,220\nTa 236,17,49152\nMaaa 68,172,212\n"
"Ta 238,17,51200\ns_lshl_b32 s0, s0, 2\nMaaa 68,168,208\ns_lshr_b32 s0, s7, s0\nMaaa 36,168,200\nTa 240,18,32768\ns_and_b32 s0, s0, 7\nMaaa 36,172,204\nTa 242,18,34816\n"
"Maaa 4,172,196\nD0 3,20,48,1024\ns_lshl_b32 s0, s0, 5\nMaaa 4,168,192\nTa 244,18,49152\nMaaa 8,176,192\nTa 246,18,51200\ns_and_b32 s1, s6, 15\nMaaa 8,180,196\n"
"s_add_u32 s0, s0, s1\nMaaa 40,180,204\nTa 248,19,32768\ns_and_b32 s1, s6, 8\nMaaa 40,176,200\nTa 250,19,34816\nMaaa 72,176,208\ns_add_u32 s0, s0, s1\ns_lshl_b32 s0, s0, 10\n"
"Maaa 72,180,212\nTa 252,19,49152\nMaaa 104,180,220\nTa 254,19,51200\ns_add_u32 s0, s0, s3\nMaaa 104,176,216\nD0 4,20,48,2048\nMaaa 108,184,216\nSv 36,1,16,0,0\n"
"s_add_u32 s6, s6, s5\nMaaa 108,188,220\nMaaa 76,188,212\nMaaa 76,184,208\nMaaa 44,184,200\nMaaa 44,188,204\nD0 5,20,48,3072\nMaaa 12,188,196\nMaaa 12,184,192\nWL 0\n"
"Mvaa 80,224,128\nTa 160,12,32768\ns_lshr_b32 s0, s6, 4\nMvaa 80,228,132\nTa 162,12,34816\nMvaa 112,228,140\ns_add_u32 m0, s47, -32768\ns_add_u32 s45, s45, 128\nMvaa 112,224,136\n"
"Ta 164,12,49152\nMvaa 144,224,144\nTa 166,12,51200\ns_lshl_b32 s0, s0, 2\nMvaa 144,228,148\nD0 2,20,45,0\nMvaa 176,228,156\nTa 168,13,32768\ns_lshr_b32 s0, s7, s0\n"
"Mvaa 176,224,152\nTa 170,13,34816\nMvaa 180,232,152\ns_and_b32 s0, s0, 7\ns_lshl_b32 s0, s0, 5\nMvaa 180,236,156\nTa 172,13,49152\nMvaa 148,236,148\nTa 174,13,51200\n"
"s_and_b32 s1, s6, 15\nMvaa 148,232,144\ns_add_u32 s0, s0, s1\nMvaa 116,232,136\nTa 176,14,32768\ns_and_b32 s1, s6, 8\nMvaa 116,236,140\nTa 178,14,34816\nMvaa 84,236,132\n"
"D0 3,20,45,1024\ns_add_u32 s0, s0, s1\nMvaa 84,232,128\nTa 180,14,49152\nMvaa 88,240,128\nTa 182,14,51200\ns_lshl_b32 s0, s0, 10\nMvaa 88,244,132\ns_add_u32 s0, s0, s3\n"
"Mvaa 120,244,140\nTa 184,15,32768\nSv 40,1,16,0,0\nMvaa 120,240,136\nTa 186,15,34816\nMvaa 152,240,144\ns_add_u32 s6, s6, s5\nMvaa 152,244,148\nTa 188,15,49152\nMvaa 184,244,156\n"
"Ta 190,15,51200\nMvaa 184,240,152\nD0 4,20,45,2048\nMvaa 188,248,152\nMvaa 188,252,156\nMvaa 156,252,148\nMvaa 156,248,144\nMvaa 124,248,136\ns_lshr_b32 s0, s6, 4\n"
"s_lshl_b32 s0, s0, 2\nMvaa 124,252,140\nD0 5,20,45,3072\nMvaa 92,252,132\ns_lshr_b32 s0, s7, s0\ns_and_b32 s0, s0, 7\nMvaa 92,248,128\ns_waitcnt vmcnt(18) lgkmcnt(0)\ns_barrier\n"
"Mvaa 64,160,128\nRa 192,10,49152\ns_lshl_b32 s0, s0, 5\nMvaa 64,164,132\ns_add_u32 m0, s47, 32768\ns_add_u32 s44, s44, 0x40000\nMvaa 96,164,140\nRa 196,11,49152\n"
"s_and_b32 s1, s6, 15\nMvaa 96,160,136\ns_add_u32 s0, s0, s1\nMvaa 128,160,144\nRa 200,10,51200\ns_and_b32 s1, s6, 8\nMvaa 128,164,148\nD0 6,24,44,0\nMvaa 160,164,156\n"
"Ra 204,11,51200\ns_add_u32 s0, s0, s1\nMvaa 160,160,152\ns_lshl_b32 s0, s0, 10\nMvaa 164,168,152\nRa 208,10,57344\ns_add_u32 s0, s0, s3\nMvaa 164,172,156\nSv 44,1,16,0,0\n"
"Mvaa 132,172,148\nRa 212,11,57344\ns_add_u32 s6, s6, s5\nMvaa 132,168,144\nMvaa 100,168,136\nRa 216,10,59392\nMvaa 100,172,140\nD0 7,24,44,1024\nMvaa 68,172,132\nRa 220,11,59392\n"
"Mvaa 68,168,128\nMvaa 72,176,128\nMvaa 72,180,132\nMvaa 104,180,140\ns_mov_b32 s6, s77\ns_mov_b32 s5, 16\nMvaa 104,176,136\ns_lshr_b32 s0, s6, 4\nMvaa 136,176,144\n"
"s_lshl_b32 s0, s0, 2\ns_lshr_b32 s0, s7, s0\nMvaa 136,180,148\nD0 8,24,44,2048\nMvaa 168,180,156\ns_and_b32 s0, s0, 7\ns_lshl_b32 s0, s0, 5\nMvaa 168,176,152\n"
"s_and_b32 s1, s6, 15\nMvaa 172,184,152\ns_add_u32 s0, s0, s1\ns_and_b32 s1, s6, 8\nMvaa 172,188,156\ns_add_u32 s0, s0, s1\nMvaa 140,188,148\ns_lshl_b32 s0, s0, 10\n"
"s_add_u32 s0, s0, s3\nMvaa 140,184,144\nSv 48,1,16,0,0\nMvaa 108,184,136\ns_add_u32 s6, s6, s5\nMvaa 108,188,140\nD0 9,24,44,3072\nMvaa 76,188,132\nMvaa 76,184,128\nWL 0\n"
"Maaa 0,160,192\nRa 128,10,0\nMaaa 0,164,196\ns_add_u32 m0, s47, 49152\ns_add_u32 s44, s44, 0x40000\nMaaa 32,164,204\nRa 132,11,0\nMaaa 32,160,200\nMaaa 64,160,208\nRa 136,10,2048\n"
"s_lshr_b32 s0, s6, 4\nMaaa 64,164,212\nD0 6,24,44,0\nMaaa 96,164,220\nRa 140,11,2048\ns_lshl_b32 s0, s0, 2\nMaaa 96,160,216\ns_lshr_b32 s0, s7, s0\nMaaa 100,168,216\n"
"Ra 144,10,8192\ns_and_b32 s0, s0, 7\nMaaa 100,172,220\ns_lshl_b32 s0, s0, 5\nMaaa 68,172,212\nRa 148,11,8192\ns_and_b32 s1, s6, 15\nMaaa 68,168,208\ns_add_u32 s0, s0, s1\n"
"Maaa 36,168,200\nRa 152,10,10240\ns_and_b32 s1, s6, 8\nMaaa 36,172,204\nD0 7,24,44,1024\nMaaa 4,172,196\nRa 156,11,10240\ns_add_u32 s0, s0, s1\nMaaa 4,168,192\n"
"s_lshl_b32 s0, s0, 10\nMaaa 8,176,192\ns_add_u32 s0, s0, s3\nSv 52,1,16,0,0\nMaaa 8,180,196\ns_add_u32 s6, s6, s5\nMaaa 40,180,204\nMaaa 40,176,200\nMaaa 72,176,208\n"
"Maaa 72,180,212\nD0 8,24,44,2048\nMaaa 104,180,220\nMaaa 104,176,216\nMaaa 108,184,216\nMaaa 108,188,220\ns_lshr_b32 s0, s6, 4\nMaaa 76,188,212\ns_lshl_b32 s0, s0, 2\n"
"s_lshr_b32 s0, s7, s0\nMaaa 76,184,208\ns_and_b32 s0, s0, 7\nMaaa 44,184,200\ns_lshl_b32 s0, s0, 5\ns_and_b32 s1, s6, 15\nMaaa 44,188,204\nD0 9,24,44,3072\nMaaa 12,188,196\n"
"s_add_u32 s0, s0, s1\ns_and_b32 s1, s6, 8\nMaaa 12,184,192\nWV 21\ns_barrier\nMaaa 16,224,192\nTa 160,12,0\ns_add_u32 s0, s0, s1\nMaaa 16,228,196\nTa 162,12,2048\nMaaa 48,228,204\n"
"s_add_u32 m0, s47, -16384\ns_add_u32 s48, s48, 128\nMaaa 48,224,200\nTa 164,12,16384\nMaaa 80,224,208\nTa 166,12,18432\ns_lshl_b32 s0, s0, 10\nMaaa 80,228,212\nD0 2,20,48,0\n"
"Maaa 112,228,220\nTa 168,13,0\ns_add_u32 s0, s0, s3\nMaaa 112,224,216\nTa 170,13,2048\nMaaa 116,232,216\nSv 56,1,16,0,0\ns_add_u32 s6, s6, s5\nMaaa 116,236,220\nTa 172,13,16384\n"
"Maaa 84,236,212\nTa 174,13,18432\nMaaa 84,232,208\nMaaa 52,232,200\nTa 176,14,0\nMaaa 52,236,204\nTa 178,14,2048\nMaaa 20,236,196\nD0 3,20,48,1024\nMaaa 20,232,192\n"
"Ta 180,14,16384\nMaaa 24,240,192\nTa 182,14,18432\ns_lshr_b32 s0, s6, 4\nMaaa 24,244,196\ns_lshl_b32 s0, s0, 2\nMaaa 56,244,204\nTa 184,15,0\ns_lshr_b32 s0, s7, s0\n"
"Maaa 56,240,200\nTa 186,15,2048\nMaaa 88,240,208\ns_and_b32 s0, s0, 7\ns_lshl_b32 s0, s0, 5\nMaaa 88,244,212\nTa 188,15,16384\nMaaa 120,244,220\nTa 190,15,18432\n"
"s_and_b32 s1, s6, 15\nMaaa 120,240,216\nD0 4,20,48,2048\nMaaa 124,248,216\ns_add_u32 s0, s0, s1\ns_and_b32 s1, s6, 8\nMaaa 124,252,220\ns_add_u32 s0, s0, s1\nMaaa 92,252,212\n"
"s_lshl_b32 s0, s0, 10\ns_add_u32 s0, s0, s3\nMaaa 92,248,208\nSv 60,1,16,0,0\nMaaa 60,248,200\ns_add_u32 s6, s6, s5\nMaaa 60,252,204\nD0 5,20,48,3072\nMaaa 28,252,196\n"
"Maaa 28,248,192\nWL 0\nMvaa 64,160,128\nTa 224,16,0\nMvaa 64,164,132\nTa 226,16,2048\ns_add_u32 m0, s47, 65536\ns_add_u32 s46, s51, 0\nMvaa 96,164,140\nv_lshrrev_b32 v48, 4, v0\n"
"Mvaa 96,160,136\nTa 228,16,16384\nD0 6,32,46,0\nMvaa 128,160,144\nTa 230,16,18432\nMvaa 128,164,148\nv_and_b32 v49, 7, v0\nMvaa 160,164,156\nTa 232,17,0\nMvaa 160,160,152\n"
"Ta 234,17,2048\nMvaa 164,168,152\nv_xor_b32 v49, v49, v48\nD0 7,32,46,1024\nMvaa 164,172,156\nTa 236,17,16384\nMvaa 132,172,148\nTa 238,17,18432\nMvaa 132,168,144\n"
"v_lshlrev_b32 v49, 4, v49\nMvaa 100,168,136\nTa 240,18,0\nMvaa 100,172,140\nTa 242,18,2048\nD0 8,32,46,2048\nMvaa 68,172,132\nv_lshrrev_b32 v48, 3, v0\nMvaa 68,168,128\n"
"Ta 244,18,16384\nMvaa 72,176,128\nTa 246,18,18432\nMvaa 72,180,132\nv_mul_u32_u24 v48, 896, v48\nMvaa 104,180,140\nTa 248,19,0\nD0 9,32,46,3072\nMvaa 104,176,136\nTa 250,19,2048\n"
"Mvaa 136,176,144\nv_add_u32 v40, v48, v49\nMvaa 136,180,148\nTa 252,19,16384\ns_add_u32 m0, s47, 81920\ns_add_u32 s46, s51, 262144\nMvaa 168,180,156\nTa 254,19,18432\n"
"Mvaa 168,176,152\nv_xor_b32 v49, 64, v40\nD0 6,32,46,0\nMvaa 172,184,152\nv_add_u32 v41, 6144, v49\nMvaa 172,188,156\nv_add_u32 v42, 55296, v40\nMvaa 140,188,148\n"
"v_add_u32 v43, 61440, v49\nMvaa 140,184,144\ns_mov_b32 s2, s13\nMvaa 108,184,136\ns_lshl3_add_u32 s2, s2, s11\nD0 7,32,46,1024\nMvaa 108,188,140\ns_mul_i32 s52, s2, 14336\n"
"Mvaa 76,188,132\ns_lshr_b32 s2, s13, 2\ns_bfe_u32 s3, s13, 0x10001\nMvaa 76,184,128\ns_waitcnt vmcnt(23) lgkmcnt(0)\ns_barrier\nMvaa 80,224,128\nRa 192,10,16384\n"
"s_mul_i32 s3, s3, 17\nMvaa 80,228,132\ns_add_u32 s2, s2, s3\nD0 8,32,46,2048\nMvaa 112,228,140\nRa 196,11,16384\ns_lshl2_add_u32 s3, s14, 0\nMvaa 112,224,136\n"
"s_lshl_b32 s3, s3, 2\nMvaa 144,224,144\nRa 200,10,18432\ns_lshr_b32 s3, 0xe420ca86, s3\nMvaa 144,228,148\ns_and_b32 s3, s3, 15\nMvaa 176,228,156\nRa 204,11,18432\n"
"s_add_u32 s2, s2, s3\nD0 9,32,46,3072\nMvaa 176,224,152\ns_cmp_eq_u32 s2, 32\ns_cselect_b32 s4, 16, s2\nMvaa 180,232,152\nRa 208,10,24576\ns_mul_i32 s75, s4, 65536\n"
"Mvaa 180,236,156\ns_lshr_b32 s2, s13, 2\ns_add_u32 m0, s47, -65536\ns_add_u32 s46, s52, 0\nMvaa 148,236,148\nRa 212,11,24576\ns_bfe_u32 s3, s13, 0x10001\nMvaa 148,232,144\n"
"s_mul_i32 s3, s3, 17\nD0 40,28,46,0\nMvaa 116,232,136\nRa 216,10,26624\ns_add_u32 s2, s2, s3\nMvaa 116,236,140\ns_lshl2_add_u32 s3, s14, 1\nMvaa 84,236,132\nRa 220,11,26624\n"
"s_lshl_b32 s3, s3, 2\nMvaa 84,232,128\ns_lshr_b32 s3, 0xe420ca86, s3\nMvaa 88,240,128\ns_and_b32 s3, s3, 15\ns_add_u32 s2, s2, s3\nD0 41,28,46,1024\nMvaa 88,244,132\n"
"s_cmp_eq_u32 s2, 32\ns_cselect_b32 s4, 16, s2\nMvaa 120,244,140\ns_mul_i32 s76, s4, 65536\ns_lshr_b32 s2, s13, 2\nMvaa 120,240,136\ns_bfe_u32 s3, s13, 0x10001\nMvaa 152,240,144\n"
"s_mul_i32 s3, s3, 17\ns_add_u32 s2, s2, s3\nMvaa 152,244,148\ns_lshl2_add_u32 s3, s14, 2\nD0 42,28,46,2048\nMvaa 184,244,156\ns_lshl_b32 s3, s3, 2\ns_lshr_b32 s3, 0xe420ca86, s3\n"
"Mvaa 184,240,152\ns_and_b32 s3, s3, 15\nMvaa 188,248,152\ns_add_u32 s2, s2, s3\nMvaa 188,252,156\ns_cmp_eq_u32 s2, 32\ns_cselect_b32 s4, 16, s2\nMvaa 156,252,148\n"
"s_mul_i32 s77, s4, 65536\ns_lshr_b32 s2, s13, 2\nD0 43,28,46,3072\nMvaa 156,248,144\ns_bfe_u32 s3, s13, 0x10001\nMvaa 124,248,136\ns_mul_i32 s3, s3, 17\ns_add_u32 s2, s2, s3\n"
"Mvaa 124,252,140\ns_lshl2_add_u32 s3, s14, 3\nMvaa 92,252,132\ns_lshl_b32 s3, s3, 2\ns_lshr_b32 s3, 0xe420ca86, s3\nMvaa 92,248,128\nWL 0\nMaaa 16,224,192\nRa 128,10,32768\n"
"s_and_b32 s3, s3, 15\ns_add_u32 m0, s47, 0\ns_add_u32 s46, s51, 524288\nD0 6,32,46,0\nMaaa 16,228,196\ns_add_u32 s2, s2, s3\ns_cmp_eq_u32 s2, 32\ns_cselect_b32 s4, 16, s2\n"
"Maaa 48,228,204\nRa 132,11,32768\ns_mul_i32 s78, s4, 65536\nMaaa 48,224,200\ns_lshr_b32 s3, s11, 1\nMaaa 80,224,208\nRa 136,10,34816\ns_lshl_b32 s3, s3, 15\nMaaa 80,228,212\n"
"s_add_u32 s2, s72, s3\nD0 7,32,46,1024\nMaaa 112,228,220\nRa 140,11,34816\ns_and_b32 s3, s11, 1\nMaaa 112,224,216\ns_lshl_b32 s3, s3, 12\nMaaa 116,232,216\nRa 144,10,40960\n"
"s_add_u32 s2, s2, s3\nMaaa 116,236,220\ns_add_u32 s2, s2, 0x0\nMaaa 84,236,212\nRa 148,11,40960\ns_mov_b32 s55, 16384\nD0 8,32,46,2048\nMaaa 84,232,208\ns_add_u32 s75, s75, s2\n"
"Maaa 52,232,200\nRa 152,10,43008\ns_add_u32 s76, s76, s2\nMaaa 52,236,204\ns_add_u32 s77, s77, s2\nMaaa 20,236,196\nRa 156,11,43008\ns_add_u32 s78, s78, s2\nMaaa 20,232,192\n"
"v_add_u32 v32, 0x10000, v12\nD0 9,32,46,3072\nMaaa 24,240,192\nv_add_u32 v33, 0x10000, v13\nv_add_u32 v34, 0x10000, v14\nMaaa 24,244,196\nv_add_u32 v35, 0x10000, v15\n"
"Maaa 56,244,204\nv_add_u32 v36, 0x10000, v16\nv_add_u32 v37, 0x10000, v17\ns_add_u32 m0, s47, 16384\ns_add_u32 s46, s51, 786432\nMaaa 56,240,200\nv_add_u32 v38, 0x10000, v18\n"
"Maaa 88,240,208\nv_add_u32 v39, 0x10000, v19\ns_add_u32 s2, s11, 1\nD0 6,32,46,0\nMaaa 88,244,212\ns_lshr_b32 s2, s2, 1\nMaaa 120,244,220\ns_lshl_b32 s2, s2, 12\n"
"v_add_u32 v44, s2, v10\nMaaa 120,240,216\nv_add_u32 v45, s2, v11\nMaaa 124,248,216\nv_xor_b32 v46, 0x1000, v44\nv_xor_b32 v47, 0x1000, v45\nMaaa 124,252,220\n"
"s_lshr_b32 s3, s11, 1\nD0 7,32,46,1024\nMaaa 92,252,212\ns_lshl_b32 s3, s3, 13\ns_lshl_b32 s2, s11, 12\nMaaa 92,248,208\ns_add_u32 s2, s2, s3\nMaaa 60,248,200\n"
"s_add_u32 s2, s2, s72\ns_lshl_b32 s3, s11, 12\nMaaa 60,252,204\ns_add_u32 s89, s3, 0x10000\nMaaa 28,252,196\ns_mov_b32 s88, 0xffff8000\ns_lshl_b32 s4, s13, 1\nD0 8,32,46,2048\n"
"Maaa 28,248,192\ns_waitcnt vmcnt(25) lgkmcnt(0)\ns_barrier\nMaaa 0,160,192\nTa 224,16,32768\ns_sub_u32 s79, 15, s4\nMaaa 0,164,196\nTa 226,16,34816\nMaaa 32,164,204\n"
"s_add_u32 s96, s2, 0x1310000\ns_mov_b32 s63, 0x53800000\nMaaa 32,160,200\nTa 228,16,49152\nD0 9,32,46,3072\nMaaa 64,160,208\nTa 230,16,51200\ns_mov_b32 s64, 0x2b800000\n"
"Maaa 64,164,212\ns_sub_u32 s98, s4, 1\nMaaa 96,164,220\nTa 232,17,32768\ns_lshl_b32 s5, s13, 5\ns_add_u32 m0, s47, -32768\ns_add_u32 s46, s52, 128\nMaaa 96,160,216\n"
"Ta 234,17,34816\nMaaa 100,168,216\ns_add_u32 s5, s5, s11\ns_lshl_b32 s65, s5, 9\nD0 40,28,46,0\nMaaa 100,172,220\nTa 236,17,49152\nMaaa 68,172,212\nTa 238,17,51200\n"
"s_add_u32 s65, s65, s72\nMaaa 68,168,208\ns_add_u32 s65, s65, 0x1580000\nMaaa 36,168,200\nTa 240,18,32768\ns_and_b32 s66, s72, 0x2000\nMaaa 36,172,204\nTa 242,18,34816\n"
"D0 41,28,46,1024\nMaaa 4,172,196\ns_add_u32 s66, s66, s56\ns_add_u32 s66, s66, 0x100000\nMaaa 4,168,192\nTa 244,18,49152\nMaaa 8,176,192\nTa 246,18,51200\ns_and_b32 s5, s12, 1\n"
"Maaa 8,180,196\ns_mul_i32 s58, s5, 0x202000\nMaaa 40,180,204\nTa 248,19,32768\ns_xor_b32 s58, s58, s72\nD0 42,28,46,2048\nMaaa 40,176,200\nTa 250,19,34816\nMaaa 72,176,208\n"
"s_add_u32 s58, s58, 0x15c0000\ns_lshl_b32 s5, s5, 8\nMaaa 72,180,212\nTa 252,19,49152\nMaaa 104,180,220\nTa 254,19,51200\ns_add_u32 s61, s58, s5\nMaaa 104,176,216\n"
"s_lshl4_add_u32 s60, s13, s61\nD0 43,28,46,3072\nMaaa 108,184,216\ns_add_u32 s54, s60, 128\ns_xor_b32 s60, s54, 0x100\nMaaa 108,188,220\ns_cmp_eq_u32 s11, 0\n"
"s_cselect_b32 s54, s54, 0x40000000\nMaaa 76,188,212\ns_getpc_b64 s[8:9]\n.PC278:\ns_add_u32 s8, s8, .HD265-.PC278\ns_addc_u32 s9, s9, 0\ns_add_u32 m0, s47, -49152\n"
"s_add_u32 s46, s52, 256\nMaaa 76,184,208\ns_mov_b32 s97, 0xff800000\nMaaa 44,184,200\ns_mov_b32 s90, 0\nD0 40,28,46,0\nMaaa 44,188,204\ns_cmp_lt_u32 s11, 2\n"
"s_cselect_b32 s92, 2, 0\nMaaa 12,188,196\ns_mul_i32 s4, s79, 65536\ns_sub_u32 s4, s2, s4\nMaaa 12,184,192\nWL 0\nMvaa 80,224,128\nTa 160,12,32768\ns_add_u32 s94, s4, 0x20000\n"
"Mvaa 80,228,132\nTa 162,12,34816\nD0 41,28,46,1024\nMvaa 84,236,132\ns_add_u32 s95, s4, 0x410000\ns_add_u32 s86, s2, 0x130000\nMvaa 84,232,128\nTa 164,12,49152\nMvaa 88,240,128\n"
"Ta 166,12,51200\ns_add_u32 s87, s2, 0x520000\nMvaa 88,244,132\ns_sub_u32 s99, s79, 2\ns_cmp_eq_u32 s13, 7\ns_cselect_b32 s93, 1.0, 0\nMvaa 92,252,132\nTa 168,13,32768\n"
"s_mov_b32 s36, s68\nD0 42,28,46,2048\nMvaa 92,248,128\nTa 170,13,34816\nMvaa 124,248,136\ns_and_b32 s37, s69, 0xffff\ns_mov_b32 s38, 0x1c0000\nMvaa 124,252,140\nTa 172,13,49152\n"
"CV 200,80,81\nMvaa 120,244,140\nTa 174,13,51200\ns_mov_b32 s39, 0x20000\nMvaa 120,240,136\ns_and_b32 s2, s74, 15\nD0 43,28,46,3072\nMvaa 116,232,136\nTa 176,14,32768\n"
"s_min_u32 s2, s2, 13\nMvaa 116,236,140\nTa 178,14,34816\nMvaa 112,228,140\ns_sub_u32 s2, s2, 0\ns_mul_i32 s3, s73, 14\nMvaa 112,224,136\nTa 180,14,49152\nCV 201,82,83\n"
"Mvaa 144,224,144\nTa 182,14,51200\ns_add_u32 s2, s2, s3\nMvaa 144,228,148\ns_lshl2_add_u32 s2, s2, s11\nMvaa 148,236,148\nTa 184,15,32768\ns_mul_i32 s3, s2, 4682\n"
"Mvaa 148,232,144\nTa 186,15,34816\nCV 202,84,85\nMvaa 152,240,144\ns_lshr_b32 s3, s3, 16\ns_mul_i32 s22, s3, 14\nMvaa 152,244,148\nTa 188,15,49152\nCV 203,86,87\nMvaa 156,252,148\n"
"Ta 190,15,51200\ns_sub_u32 s22, s2, s22\nMvaa 156,248,144\ns_and_b32 s21, s3, 28\nMvaa 188,248,152\ns_add_u32 s21, s21, s3\ns_lshl_b32 s21, s21, 4\nMvaa 188,252,156\n"
"s_bfe_u32 s41, s3, 0x30002\ns_cmp_gt_u32 s22, 6\ns_cselect_b32 s2, 1179648, 0\ns_cselect_b32 s1, 16, 0\nMvaa 184,244,156\ns_add_u32 s41, s41, s1\n"
"s_lshl4_add_u32 s41, s41, 0xc048480\nMvaa 184,240,152\ns_and_b32 s1, s3, 28\nCV 204,88,89\nMvaa 180,232,152\ns_add_u32 s3, s3, s1\ns_lshl_b32 s3, s3, 11\nMvaa 180,236,156\n"
"s_add_u32 s2, s2, s3\nMvaa 176,228,156\ns_lshl_b32 s3, s22, 17\ns_add_u32 s2, s2, s3\nMvaa 176,224,152\ns_waitcnt vmcnt(16) lgkmcnt(0)\ns_barrier\nMvaa 64,160,128\n"
"Ra 192,10,49152\ns_add_u32 s20, s2, 0x10e0000\ns_add_u32 m0, s47, 32768\ns_add_u32 s46, s51, 1048576\nMvaa 64,164,132\ns_lshl_b32 s2, s22, 7\nMvaa 68,172,132\nRa 196,11,49152\n"
"s_add_u32 s40, s2, 0xc04b800\nD0 6,32,46,0\nMvaa 68,168,128\ns_mul_i32 s2, s21, 896\nCV 205,90,91\nMvaa 72,176,128\nRa 200,10,51200\ns_lshl_b32 s3, s22, 6\nMvaa 72,180,132\n"
"s_add_u32 s22, s2, s3\nCV 206,92,93\nMvaa 76,188,132\nRa 204,11,51200\ns_add_u32 s23, s22, 57344\nMvaa 76,184,128\nD0 7,32,46,1024\nMvaa 108,184,136\nRa 208,10,57344\n"
"CV 207,94,95\nMvaa 108,188,140\nCV 220,120,121\nCV 221,122,123\nMvaa 104,180,140\nRa 212,11,57344\nMvaa 104,176,136\nCV 222,124,125\nCV 223,126,127\nMvaa 100,168,136\n"
"Ra 216,10,59392\nD0 8,32,46,2048\ns_lshl3_add_u32 s5, s12, s13\ns_lshl2_add_u32 s5, s5, s11\ns_lshl2_add_u32 s5, s5, 0xc04a400\nbuffer_load_dword v27, off, s[16:19], s5 sc0 sc1\n"
"Mvaa 100,172,140\nCV 216,112,113\nMvaa 96,164,140\nRa 220,11,59392\nCV 217,114,115\nMvaa 96,160,136\nCV 218,116,117\nCV 219,118,119\nMvaa 128,160,144\nCV 192,64,65\nCV 193,66,67\n"
"Mvaa 128,164,148\nD0 9,32,46,3072\nMvaa 132,172,148\nCV 194,68,69\nCV 195,70,71\nMvaa 132,168,144\nCV 196,72,73\nCV 197,74,75\nMvaa 136,176,144\ns_add_u32 m0, s47, 49152\n"
"s_add_u32 s46, s51, 1310720\nMvaa 136,180,148\nCV 198,76,77\nCV 199,78,79\nMvaa 140,188,148\nD0 6,32,46,0\nCV 212,104,105\nMvaa 140,184,144\nCV 213,106,107\nMvaa 172,184,152\n"
"CV 214,108,109\nCV 215,110,111\nMvaa 172,188,156\nCV 208,96,97\nCV 209,98,99\nMvaa 168,180,156\nCV 210,100,101\nMvaa 168,176,152\nD0 7,32,46,1024\nCV 211,102,103\n"
"Mvaa 164,168,152\nCV 232,144,145\nMvaa 164,172,156\nCV 233,146,147\nSv 192,1,16,75,0\nMvaa 160,164,156\nCV 234,148,149\nMvaa 160,160,152\nWL 0\nMaaa 0,160,192\nD0 8,32,46,2048\n"
"Ra 128,44,0\nMaaa 0,164,196\nCV 235,150,151\nMaaa 4,172,196\nRa 132,45,0\nMaaa 4,168,192\nCV 236,152,153\nMaaa 8,176,192\nRa 136,44,2048\nMaaa 8,180,196\nD0 9,32,46,3072\n"
"Maaa 12,188,196\nRa 140,45,2048\nSv 196,1,16,75,1024\nMaaa 12,184,192\nCV 237,154,155\nMaaa 44,184,200\nRa 144,46,0\nMaaa 44,188,204\nCV 238,156,157\nMaaa 40,180,204\nRa 148,47,0\n"
"Maaa 40,176,200\nCV 239,158,159\nMaaa 36,168,200\nRa 152,46,2048\nMaaa 36,172,204\nCV 252,184,185\nMaaa 32,164,204\nRa 156,47,2048\nMaaa 32,160,200\nCV 253,186,187\n"
"Sv 200,1,16,75,2048\nMaaa 64,160,208\nCV 254,188,189\nCV 255,190,191\nMaaa 64,164,212\nCV 248,176,177\nMaaa 68,172,212\nCV 249,178,179\nMaaa 68,168,208\nCV 250,180,181\n"
"Maaa 72,176,208\nCV 251,182,183\nMaaa 72,180,212\nCV 224,128,129\nMaaa 76,188,212\nCV 225,130,131\nMaaa 76,184,208\nCV 226,132,133\nMaaa 108,184,216\nCV 227,134,135\n"
"Sv 204,1,16,75,3072\nMaaa 108,188,220\nCV 228,136,137\nMaaa 104,180,220\nCV 229,138,139\nMaaa 104,176,216\nCV 230,140,141\nMaaa 100,168,216\nCV 231,142,143\nMaaa 100,172,220\n"
"CV 244,168,169\nMaaa 96,164,220\nCV 245,170,171\nMaaa 96,160,216\ns_barrier\nMaaa 16,224,192\ns_add_u32 m0, s47, -16384\ns_add_u32 s46, s52, 384\nTa 160,32,0\nMaaa 16,228,196\n"
"Ta 162,32,2048\nMaaa 20,236,196\nD0 40,28,46,0\ns_add_u32 s75, s75, s55\nSv 208,1,16,75,0\nMaaa 20,232,192\nTa 164,32,16384\nMaaa 24,240,192\nTa 166,32,18432\nMaaa 24,244,196\n"
"CV 246,172,173\nMaaa 28,252,196\nTa 168,33,0\nMaaa 28,248,192\nD0 41,28,46,1024\nTa 170,33,2048\nMaaa 60,248,200\nCV 247,174,175\nMaaa 60,252,204\nTa 172,33,16384\n"
"Maaa 56,244,204\nTa 174,33,18432\nMaaa 56,240,200\nCV 240,160,161\nSv 212,1,16,75,1024\nMaaa 52,232,200\nD0 42,28,46,2048\nTa 176,34,0\nMaaa 52,236,204\nTa 178,34,2048\n"
"Maaa 48,228,204\nCV 241,162,163\nMaaa 48,224,200\nTa 180,34,16384\nMaaa 80,224,208\nTa 182,34,18432\nCV 242,164,165\nMaaa 80,228,212\nD0 43,28,46,3072\nMaaa 84,236,212\n"
"Ta 184,35,0\nMaaa 84,232,208\nTa 186,35,2048\nMaaa 88,240,208\nCV 243,166,167\nSv 216,1,16,75,2048\nMaaa 88,244,212\nTa 188,35,16384\nMaaa 92,252,212\nTa 190,35,18432\n"
"Maaa 92,248,208\nMaaa 124,248,216\nMaaa 124,252,220\nMaaa 120,244,220\nMaaa 120,240,216\nMaaa 116,232,216\nMaaa 116,236,220\nSv 220,1,16,75,3072\nMaaa 112,228,220\n"
"Maaa 112,224,216\nWL 0\nZvaa 64,160,128\nTa 224,36,0\nAR 60,0\nMvaa 64,164,132\nTa 226,36,2048\nZvaa 96,164,140\nAR 61,1\nMvaa 96,160,136\nTa 228,36,16384\nCV 192,60,61\n"
"Zvaa 128,160,144\nTa 230,36,18432\nMvaa 128,164,148\nAR 60,2\nAR 61,3\nZvaa 160,164,156\nTa 232,37,0\nSv 224,1,16,76,0\nMvaa 160,160,152\nTa 234,37,2048\nCV 193,60,61\n"
"Zvaa 164,168,152\nAR 60,4\nMvaa 164,172,156\nTa 236,37,16384\nZvaa 132,172,148\nTa 238,37,18432\nAR 61,5\nMvaa 132,168,144\nCV 194,60,61\nZvaa 100,168,136\nTa 240,38,0\nAR 60,6\n"
"Mvaa 100,172,140\nTa 242,38,2048\nZvaa 68,172,132\nSv 228,1,16,76,1024\nAR 61,7\nMvaa 68,168,128\nTa 244,38,16384\nZvaa 72,176,128\nTa 246,38,18432\nCV 195,60,61\nMvaa 72,180,132\n"
"AR 60,8\nZvaa 104,180,140\nTa 248,39,0\nMvaa 104,176,136\nTa 250,39,2048\nAR 61,9\nZvaa 136,176,144\nCV 196,60,61\nMvaa 136,180,148\nTa 252,39,16384\nAR 60,10\nZvaa 168,180,156\n"
"Ta 254,39,18432\nSv 232,1,16,76,2048\nMvaa 168,176,152\nAR 61,11\nCV 197,60,61\nZvaa 172,184,152\nAR 60,12\nMvaa 172,188,156\nAR 61,13\nZvaa 140,188,148\nCV 198,60,61\nAR 60,14\n"
"Mvaa 140,184,144\nAR 61,15\nZvaa 108,184,136\nCV 199,60,61\nAR 60,16\nMvaa 108,188,140\nSv 236,1,16,76,3072\nZvaa 76,188,132\nAR 61,17\nCV 200,60,61\nMvaa 76,184,128\n"
"s_waitcnt vmcnt(29) lgkmcnt(0)\ns_barrier\nZvaa 80,224,128\nRa 192,44,32768\nAR 60,18\nMvaa 80,228,132\nRa 196,45,32768\ns_add_u32 m0, s47, 65536\ns_add_u32 s46, s51, 1572864\n"
"D0 6,32,46,0\nZvaa 112,228,140\nRa 200,44,34816\nMvaa 112,224,136\nRa 204,45,34816\nAR 61,19\nZvaa 144,224,144\nRa 208,46,32768\nMvaa 144,228,148\nRa 212,47,32768\nCV 201,60,61\n"
"D0 7,32,46,1024\nZvaa 176,228,156\nRa 216,46,34816\nMvaa 176,224,152\nRa 220,47,34816\nAR 60,20\nZvaa 180,232,152\nTa 160,12,0\nMvaa 180,236,156\nTa 162,12,2048\nD0 8,32,46,2048\n"
"Zvaa 148,236,148\nTa 164,12,16384\nAR 61,21\nMvaa 148,232,144\nTa 166,12,18432\nZvaa 116,232,136\nTa 168,13,0\nCV 202,60,61\nMvaa 116,236,140\nTa 170,13,2048\nD0 9,32,46,3072\n"
"Zvaa 84,236,132\nTa 172,13,16384\nAR 60,22\nMvaa 84,232,128\nTa 174,13,18432\ns_add_u32 m0, s47, 81920\ns_add_u32 s46, s51, 1835008\nZvaa 88,240,128\nTa 176,14,0\nAR 61,23\n"
"Mvaa 88,244,132\nTa 178,14,2048\nD0 6,32,46,0\nZvaa 120,244,140\nTa 180,14,16384\nMvaa 120,240,136\nTa 182,14,18432\nCV 203,60,61\nZvaa 152,240,144\nTa 184,15,0\nMvaa 152,244,148\n"
"Ta 186,15,2048\nAR 60,24\nD0 7,32,46,1024\nZvaa 184,244,156\nTa 188,15,16384\nMvaa 184,240,152\nTa 190,15,18432\nAR 61,25\nZvaa 188,248,152\nCV 204,60,61\nMvaa 188,252,156\n"
"AR 60,26\nD0 8,32,46,2048\nZvaa 156,252,148\nAR 61,27\nCV 205,60,61\nMvaa 156,248,144\nAR 60,28\nZvaa 124,248,136\nAR 61,29\nCV 206,60,61\nMvaa 124,252,140\nAR 60,30\n"
"D0 9,32,46,3072\nZvaa 92,252,132\nAR 61,31\nCV 207,60,61\nMvaa 92,248,128\nWL 0\nMvaa 64,160,192\nTa 224,16,0\nAR 60,32\nMvaa 64,164,196\nTa 226,16,2048\n"
"s_add_u32 m0, s47, -65536\ns_add_u32 s46, s52, 512\nD0 40,28,46,0\nMvaa 96,164,204\nAR 61,33\nMvaa 96,160,200\nTa 228,16,16384\nCV 208,60,61\nMvaa 128,160,208\nTa 230,16,18432\n"
"Mvaa 128,164,212\nAR 60,34\nAR 61,35\nD0 41,28,46,1024\nMvaa 160,164,220\nTa 232,17,0\nMvaa 160,160,216\nTa 234,17,2048\nCV 209,60,61\nMvaa 164,168,216\nAR 60,36\n"
"Mvaa 164,172,220\nTa 236,17,16384\nD0 42,28,46,2048\nMvaa 132,172,212\nTa 238,17,18432\nAR 61,37\nMvaa 132,168,208\nCV 210,60,61\nMvaa 100,168,200\nTa 240,18,0\nAR 60,38\n"
"Mvaa 100,172,204\nTa 242,18,2048\nD0 43,28,46,3072\nMvaa 68,172,196\ns_add_u32 s76, s76, s55\nSv 240,1,16,76,0\nMvaa 68,168,192\nTa 244,18,16384\nMvaa 72,176,192\nTa 246,18,18432\n"
"AR 61,39\nMvaa 72,180,196\nCV 211,60,61\nMvaa 104,180,204\nTa 248,19,0\nMvaa 104,176,200\nTa 250,19,2048\nSv 244,1,16,76,1024\nMvaa 136,176,208\nAR 60,40\nMvaa 136,180,212\n"
"Ta 252,19,16384\nAR 61,41\nMvaa 168,180,220\nTa 254,19,18432\nMvaa 168,176,216\nCV 212,60,61\nAR 60,42\nMvaa 172,184,216\nSv 248,1,16,76,2048\nMvaa 172,188,220\nAR 61,43\n"
"Mvaa 140,188,212\nCV 213,60,61\nAR 60,44\nMvaa 140,184,208\nAR 61,45\nMvaa 108,184,200\nCV 214,60,61\nAR 60,46\nMvaa 108,188,204\nSv 252,1,16,76,3072\nMvaa 76,188,196\nAR 61,47\n"
"CV 215,60,61\nMvaa 76,184,192\ns_waitcnt vmcnt(20) lgkmcnt(0)\ns_barrier\nMvaa 80,224,192\nRa 128,44,16384\nAR 60,48\nv_cmp_ne_u32 vcc, s15, v27\ns_nop 0\ns_cbranch_vccnz .S279\n"
".S280:\nMvaa 80,228,196\nRa 132,45,16384\nbuffer_store_dword v25, off, s[16:19], s54 offset:0 sc0 sc1\ns_add_u32 m0, s47, 0\ns_add_u32 s46, s51, 2097152\nD0 6,32,46,0\n"
"Mvaa 112,228,204\nRa 136,44,18432\nMvaa 112,224,200\nRa 140,45,18432\nAR 61,49\nMvaa 144,224,208\nRa 144,46,16384\nMvaa 144,228,212\nRa 148,47,16384\nCV 216,60,61\n"
"D0 7,32,46,1024\nMvaa 176,228,220\nRa 152,46,18432\nMvaa 176,224,216\nRa 156,47,18432\nAR 60,50\nMvaa 180,232,216\nTa 160,12,32768\nMvaa 180,236,220\nTa 162,12,34816\n"
"D0 8,32,46,2048\nMvaa 148,236,212\nTa 164,12,49152\nAR 61,51\nMvaa 148,232,208\nTa 166,12,51200\nMvaa 116,232,200\nTa 168,13,32768\nCV 217,60,61\nMvaa 116,236,204\n"
"Ta 170,13,34816\nD0 9,32,46,3072\nMvaa 84,236,196\nTa 172,13,49152\nAR 60,52\nMvaa 84,232,192\nTa 174,13,51200\ns_add_u32 m0, s47, 16384\ns_add_u32 s46, s51, 2359296\n"
"Mvaa 88,240,192\nTa 176,14,32768\nAR 61,53\nMvaa 88,244,196\nTa 178,14,34816\nD0 6,32,46,0\nMvaa 120,244,204\nTa 180,14,49152\nMvaa 120,240,200\nTa 182,14,51200\nCV 218,60,61\n"
"Mvaa 152,240,208\nTa 184,15,32768\nMvaa 152,244,212\nTa 186,15,34816\nAR 60,54\nD0 7,32,46,1024\nMvaa 184,244,220\nTa 188,15,49152\nMvaa 184,240,216\nTa 190,15,51200\nAR 61,55\n"
"Mvaa 188,248,216\nCV 219,60,61\nMvaa 188,252,220\nAR 60,56\nD0 8,32,46,2048\nMvaa 156,252,212\nAR 61,57\nCV 220,60,61\nMvaa 156,248,208\nAR 60,58\nMvaa 124,248,200\nAR 61,59\n"
"CV 221,60,61\nMvaa 124,252,204\nAR 60,60\nD0 9,32,46,3072\nMvaa 92,252,196\nAR 61,61\nCV 222,60,61\nMvaa 92,248,192\nWL 0\nMvaa 64,160,128\nTa 224,16,32768\nAR 60,62\n"
"Mvaa 64,164,132\nTa 226,16,34816\ns_add_u32 m0, s47, -32768\ns_add_u32 s46, s52, 640\nD0 40,28,46,0\nMvaa 96,164,140\nAR 61,63\nMvaa 96,160,136\nTa 228,16,49152\nCV 223,60,61\n"
"Mvaa 128,160,144\nTa 230,16,51200\nMvaa 128,164,148\nD0 41,28,46,1024\nMvaa 160,164,156\nTa 232,17,32768\nAR 60,126\nMvaa 160,160,152\nTa 234,17,34816\nMvaa 164,168,152\n"
"AR 61,127\nCV 60,60,61\nMvaa 164,172,156\nTa 236,17,49152\nD0 42,28,46,2048\nMvaa 132,172,148\nTa 238,17,51200\nMvaa 132,168,144\nAW 127,60\nAR 60,124\nMvaa 100,168,136\n"
"Ta 240,18,32768\nMvaa 100,172,140\nTa 242,18,34816\nD0 43,28,46,3072\nMvaa 68,172,132\nSv 192,1,16,77,0\nAR 61,125\nMvaa 68,168,128\nTa 244,18,49152\nMvaa 72,176,128\n"
"Ta 246,18,51200\nCV 60,60,61\nMvaa 72,180,132\nAW 126,60\nMvaa 104,180,140\nTa 248,19,32768\nMvaa 104,176,136\nTa 250,19,34816\nSv 196,1,16,77,1024\nMvaa 136,176,144\nAR 60,122\n"
"Mvaa 136,180,148\nTa 252,19,49152\nMvaa 168,180,156\nTa 254,19,51200\nAR 61,123\nMvaa 168,176,152\nCV 60,60,61\nMvaa 172,184,152\nSv 200,1,16,77,2048\nAW 125,60\nMvaa 172,188,156\n"
"AR 60,120\nMvaa 140,188,148\nAR 61,121\nMvaa 140,184,144\nCV 60,60,61\nAW 124,60\nMvaa 108,184,136\nAR 60,118\nMvaa 108,188,140\nSv 204,1,16,77,3072\nMvaa 76,188,132\nAR 61,119\n"
"CV 60,60,61\nMvaa 76,184,128\ns_waitcnt vmcnt(17) lgkmcnt(0)\ns_barrier\nMvaa 80,224,128\nRa 192,44,49152\nAW 123,60\nMvaa 80,228,132\nRa 196,45,49152\n"
"buffer_store_dword v25, off, s[16:19], s54 offset:8 sc0 sc1\ns_add_u32 m0, s47, 32768\ns_add_u32 s46, s51, 2621440\nD0 6,32,46,0\nMvaa 112,228,140\nRa 200,44,51200\n"
"Mvaa 112,224,136\nRa 204,45,51200\nAR 60,116\nMvaa 144,224,144\nRa 208,46,49152\nMvaa 144,228,148\nRa 212,47,49152\nD0 7,32,46,1024\nMvaa 176,228,156\nRa 216,46,51200\nAR 61,117\n"
"Mvaa 176,224,152\nRa 220,47,51200\nMvaa 180,232,152\nTa 160,32,0\nCV 60,60,61\nMvaa 180,236,156\nTa 162,32,2048\nD0 8,32,46,2048\nMvaa 148,236,148\nTa 164,32,16384\n"
"Mvaa 148,232,144\nTa 166,32,18432\nAW 122,60\nMvaa 116,232,136\nTa 168,33,0\nMvaa 116,236,140\nTa 170,33,2048\nD0 9,32,46,3072\nMvaa 84,236,132\nTa 172,33,16384\nAR 60,114\n"
"Mvaa 84,232,128\nTa 174,33,18432\ns_add_u32 m0, s47, 49152\ns_add_u32 s46, s51, 2883584\nMvaa 88,240,128\nTa 176,34,0\nAR 61,115\nMvaa 88,244,132\nTa 178,34,2048\nD0 6,32,46,0\n"
"Mvaa 120,244,140\nTa 180,34,16384\nMvaa 120,240,136\nTa 182,34,18432\nCV 60,60,61\nMvaa 152,240,144\nTa 184,35,0\nMvaa 152,244,148\nTa 186,35,2048\nD0 7,32,46,1024\n"
"Mvaa 184,244,156\nTa 188,35,16384\nAW 121,60\nMvaa 184,240,152\nTa 190,35,18432\nMvaa 188,248,152\nAR 60,112\nAR 61,113\nMvaa 188,252,156\nD0 8,32,46,2048\nMvaa 156,252,148\n"
"CV 60,60,61\nMvaa 156,248,144\nAW 120,60\nAR 60,110\nMvaa 124,248,136\nAR 61,111\nMvaa 124,252,140\nD0 9,32,46,3072\nMvaa 92,252,132\nCV 60,60,61\nAW 119,60\nMvaa 92,248,128\n"
"WL 0\nMvaa 64,160,192\nTa 224,36,0\nAR 60,108\nMvaa 64,164,196\nTa 226,36,2048\ns_add_u32 m0, s47, -49152\ns_add_u32 s46, s52, 768\nD0 40,28,46,0\nMvaa 96,164,204\nAR 61,109\n"
"Mvaa 96,160,200\nTa 228,36,16384\nCV 60,60,61\nMvaa 128,160,208\nTa 230,36,18432\nMvaa 128,164,212\nD0 41,28,46,1024\nMvaa 160,164,220\nTa 232,37,0\nAW 118,60\nMvaa 160,160,216\n"
"Ta 234,37,2048\nMvaa 164,168,216\nAR 60,106\nAR 61,107\nMvaa 164,172,220\nTa 236,37,16384\nD0 42,28,46,2048\nMvaa 132,172,212\nTa 238,37,18432\nMvaa 132,168,208\nCV 60,60,61\n"
"AW 117,60\nMvaa 100,168,200\nTa 240,38,0\nMvaa 100,172,204\nTa 242,38,2048\nD0 43,28,46,3072\nMvaa 68,172,196\ns_add_u32 s77, s77, s55\nSv 208,1,16,77,0\nMvaa 68,168,192\n"
"Ta 244,38,16384\nMvaa 72,176,192\nTa 246,38,18432\nAR 60,104\nMvaa 72,180,196\nAR 61,105\nMvaa 104,180,204\nTa 248,39,0\nMvaa 104,176,200\nTa 250,39,2048\nSv 212,1,16,77,1024\n"
"Mvaa 136,176,208\nCV 60,60,61\nMvaa 136,180,212\nTa 252,39,16384\nMvaa 168,180,220\nTa 254,39,18432\nAW 116,60\nMvaa 168,176,216\nAR 60,102\nMvaa 172,184,216\nSv 216,1,16,77,2048\n"
"AR 61,103\nMvaa 172,188,220\nCV 60,60,61\nMvaa 140,188,212\nAW 115,60\nMvaa 140,184,208\nAR 60,100\nAR 61,101\nMvaa 108,184,200\nCV 60,60,61\nMvaa 108,188,204\n"
"Sv 220,1,16,77,3072\nMvaa 76,188,196\nAW 114,60\nAR 60,98\nMvaa 76,184,192\ns_waitcnt vmcnt(25) lgkmcnt(0)\ns_barrier\nMvaa 80,224,192\nRa 128,44,0\nv_and_b32 v27, 0x70, v1\n"
"buffer_load_dword v27, v27, s[16:19], s61 offen offset:128 sc0 sc1\nMvaa 80,228,196\nRa 132,45,0\ns_add_u32 m0, s47, 65536\ns_add_u32 s46, s51, 3145728\nD0 6,32,46,0\n"
"Mvaa 112,228,204\nRa 136,44,2048\nMvaa 112,224,200\nRa 140,45,2048\nAR 61,99\nMvaa 144,224,208\nRa 144,46,0\nMvaa 144,228,212\nRa 148,47,0\nD0 7,32,46,1024\nMvaa 176,228,220\n"
"Ra 152,46,2048\nCV 60,60,61\nMvaa 176,224,216\nRa 156,47,2048\nMvaa 180,232,216\nTa 160,12,0\nAW 113,60\nMvaa 180,236,220\nTa 162,12,2048\nD0 8,32,46,2048\nMvaa 148,236,212\n"
"Ta 164,12,16384\nMvaa 148,232,208\nTa 166,12,18432\nAR 60,96\nMvaa 116,232,200\nTa 168,13,0\nMvaa 116,236,204\nTa 170,13,2048\nD0 9,32,46,3072\nMvaa 84,236,196\nTa 172,13,16384\n"
"AR 61,97\nMvaa 84,232,192\nTa 174,13,18432\ns_add_u32 m0, s47, 81920\ns_add_u32 s46, s51, 3407872\nMvaa 88,240,192\nTa 176,14,0\nCV 60,60,61\nMvaa 88,244,196\nTa 178,14,2048\n"
"D0 6,32,46,0\nMvaa 120,244,204\nTa 180,14,16384\nMvaa 120,240,200\nTa 182,14,18432\nAW 112,60\nMvaa 152,240,208\nTa 184,15,0\nMvaa 152,244,212\nTa 186,15,2048\nD0 7,32,46,1024\n"
"Mvaa 184,244,220\nTa 188,15,16384\nAR 60,94\nMvaa 184,240,216\nTa 190,15,18432\nMvaa 188,248,216\nAR 61,95\nCV 60,60,61\nMvaa 188,252,220\nD0 8,32,46,2048\nMvaa 156,252,212\n"
"AW 111,60\nMvaa 156,248,208\nAR 60,92\nAR 61,93\nMvaa 124,248,200\nCV 60,60,61\nMvaa 124,252,204\nD0 9,32,46,3072\nMvaa 92,252,196\nAW 110,60\nAR 60,90\nMvaa 92,248,192\nWL 0\n"
"Mvaa 64,160,128\nTa 224,16,0\nAR 61,91\nMvaa 64,164,132\nTa 226,16,2048\ns_add_u32 s6, s56, 16384\nbuffer_load_dwordx4 v[224:227], v1, s[16:19], s6 offen offset:0 sc1\n"
"Mvaa 96,164,140\nCV 60,60,61\nMvaa 96,160,136\nTa 228,16,16384\nAW 109,60\nMvaa 128,160,144\nTa 230,16,18432\nMvaa 128,164,148\n"
"buffer_load_dwordx4 v[228:231], v1, s[16:19], s6 offen offset:1024 sc1\nMvaa 160,164,156\nTa 232,17,0\nAR 60,88\nMvaa 160,160,152\nTa 234,17,2048\nMvaa 164,168,152\nAR 61,89\n"
"CV 60,60,61\nMvaa 164,172,156\nTa 236,17,16384\nbuffer_load_dwordx4 v[232:235], v1, s[16:19], s6 offen offset:2048 sc1\nMvaa 132,172,148\nTa 238,17,18432\nMvaa 132,168,144\n"
"AW 108,60\nAR 60,86\nMvaa 100,168,136\nTa 240,18,0\nMvaa 100,172,140\nTa 242,18,2048\nbuffer_load_dwordx4 v[236:239], v1, s[16:19], s6 offen offset:3072 sc1\nMvaa 68,172,132\n"
"AR 61,87\nCV 60,60,61\nMvaa 68,168,128\nTa 244,18,16384\nMvaa 72,176,128\nTa 246,18,18432\nAW 107,60\nMvaa 72,180,132\nAR 60,84\nMvaa 104,180,140\nTa 248,19,0\nMvaa 104,176,136\n"
"Ta 250,19,2048\nAR 61,85\nMvaa 136,176,144\nCV 60,60,61\nMvaa 136,180,148\nTa 252,19,16384\nMvaa 168,180,156\nTa 254,19,18432\nAW 106,60\nMvaa 168,176,152\nAR 60,82\n"
"Mvaa 172,184,152\nAR 61,83\nCV 60,60,61\nMvaa 172,188,156\nAW 105,60\nMvaa 140,188,148\nAR 60,80\nMvaa 140,184,144\nAR 61,81\nCV 60,60,61\nMvaa 108,184,136\nAW 104,60\n"
"Mvaa 108,188,140\nAR 60,78\nMvaa 76,188,132\nAR 61,79\nCV 60,60,61\nMvaa 76,184,128\ns_waitcnt vmcnt(12) lgkmcnt(0)\ns_barrier\nMvaa 80,224,128\nRa 192,44,32768\n"
"v_cmp_ne_u32 vcc, s15, v27\ns_nop 0\ns_cbranch_vccnz .S282\n.S283:\ns_sub_u32 s45, s86, 131072\ns_cmp_eq_u32 s79, 1\ns_cselect_b32 s86, s94, s86\ns_add_u32 m0, s47, -65536\n"
"s_add_u32 s46, s45, 0x0\nMvaa 80,228,132\nRa 196,45,32768\nMvaa 112,228,140\nRa 200,44,34816\nD1 1,16,46,0\nMvaa 112,224,136\nRa 204,45,34816\nAW 103,60\nMvaa 144,224,144\n"
"Ra 208,46,32768\nMvaa 144,228,148\nRa 212,47,32768\nMvaa 176,228,156\nRa 216,46,34816\nD1 1,16,46,1024\nMvaa 176,224,152\nRa 220,47,34816\nMvaa 180,232,152\nTa 160,12,32768\n"
"AR 60,76\nMvaa 180,236,156\nTa 162,12,34816\nMvaa 148,236,148\nTa 164,12,49152\nD1 1,16,46,2048\nMvaa 148,232,144\nTa 166,12,51200\nAR 61,77\nMvaa 116,232,136\nTa 168,13,32768\n"
"Mvaa 116,236,140\nTa 170,13,34816\nMvaa 84,236,132\nTa 172,13,49152\nD1 1,16,46,3072\nMvaa 84,232,128\nTa 174,13,51200\nMvaa 88,240,128\nTa 176,14,32768\ns_add_u32 s6, s56, 20480\n"
"Mvaa 88,244,132\nTa 178,14,34816\nMvaa 120,244,140\nTa 180,14,49152\nbuffer_load_dwordx4 v[240:243], v1, s[16:19], s6 offen offset:0 sc1\nMvaa 120,240,136\nTa 182,14,51200\n"
"CV 60,60,61\nMvaa 152,240,144\nTa 184,15,32768\nMvaa 152,244,148\nTa 186,15,34816\nMvaa 184,244,156\nTa 188,15,49152\n"
"buffer_load_dwordx4 v[244:247], v1, s[16:19], s6 offen offset:1024 sc1\nMvaa 184,240,152\nTa 190,15,51200\nMvaa 188,248,152\nAW 102,60\nAR 60,74\nMvaa 188,252,156\nAR 61,75\n"
"Mvaa 156,252,148\nbuffer_load_dwordx4 v[248:251], v1, s[16:19], s6 offen offset:2048 sc1\nMvaa 156,248,144\nCV 60,60,61\nAW 101,60\nMvaa 124,248,136\nAR 60,72\nMvaa 124,252,140\n"
"AR 61,73\nMvaa 92,252,132\nbuffer_load_dwordx4 v[252:255], v1, s[16:19], s6 offen offset:3072 sc1\nCV 60,60,61\nMvaa 92,248,128\nWL 0\nMvaa 64,160,192\nTa 224,16,32768\n"
"s_add_u32 s6, s56, 0\nMvaa 64,164,196\nTa 226,16,34816\nMvaa 96,164,204\nbuffer_load_dwordx4 v[192:195], v1, s[16:19], s6 offen offset:0 sc1\nMvaa 96,160,200\nTa 228,16,49152\n"
"AW 100,60\nMvaa 128,160,208\nTa 230,16,51200\nMvaa 128,164,212\nAR 60,70\nMvaa 160,164,220\nTa 232,17,32768\n"
"buffer_load_dwordx4 v[196:199], v1, s[16:19], s6 offen offset:1024 sc1\nMvaa 160,160,216\nTa 234,17,34816\nMvaa 164,168,216\nAR 61,71\nCV 60,60,61\nMvaa 164,172,220\n"
"Ta 236,17,49152\nMvaa 132,172,212\nTa 238,17,51200\nbuffer_load_dwordx4 v[200:203], v1, s[16:19], s6 offen offset:2048 sc1\nMvaa 132,168,208\nAW 99,60\nAR 60,68\nMvaa 100,168,200\n"
"Ta 240,18,32768\nMvaa 100,172,204\nTa 242,18,34816\nMvaa 68,172,196\nbuffer_load_dwordx4 v[204:207], v1, s[16:19], s6 offen offset:3072 sc1\nAR 61,69\nMvaa 68,168,192\n"
"Ta 244,18,49152\nMvaa 72,176,192\nTa 246,18,51200\nCV 60,60,61\nMvaa 72,180,196\nAW 98,60\nMvaa 104,180,204\nTa 248,19,32768\nMvaa 104,176,200\nTa 250,19,34816\nAR 60,66\n"
"Mvaa 136,176,208\nAR 61,67\nMvaa 136,180,212\nTa 252,19,49152\nMvaa 168,180,220\nTa 254,19,51200\nCV 60,60,61\nMvaa 168,176,216\nAW 97,60\nMvaa 172,184,216\nAR 60,64\nAR 61,65\n"
"Mvaa 172,188,220\nCV 60,60,61\nMvaa 140,188,212\nAW 96,60\nv_add_u32 v2, 0x0, v1\nMvaa 140,184,208\ns_lshr_b32 s2, s59, 1\nMvaa 108,184,200\nv_add_u32 v24, s2, v2\n"
"v_xor_b32 v26, 0x1000, v24\nMvaa 108,188,204\nv_lshrrev_b32 v20, 4, v0\nMvaa 76,188,196\nv_lshlrev_b32 v20, 2, v20\nv_and_b32 v0, 15, v0\nMvaa 76,184,192\n"
"s_waitcnt vmcnt(16) lgkmcnt(0)\ns_barrier\nMvaa 80,224,192\nRa 128,44,16384\nv_sub_u32 v0, v0, v20\nMvaa 80,228,196\nRa 132,45,16384\n"
"buffer_store_dword v25, off, s[16:19], s54 offset:12 sc0 sc1\ns_add_u32 s6, s56, 4096\nbuffer_load_dwordx4 v[208:211], v1, s[16:19], s6 offen offset:0 sc1\nMvaa 112,228,204\n"
"Ra 136,44,18432\ns_lshl_b32 s2, s11, 4\nMvaa 112,224,200\nRa 140,45,18432\nMvaa 144,224,208\nRa 144,46,16384\nv_add_u32 v3, s2, v0\nMvaa 144,228,212\nRa 148,47,16384\n"
"buffer_load_dwordx4 v[212:215], v1, s[16:19], s6 offen offset:1024 sc1\nMvaa 176,228,220\nRa 152,46,18432\nv_subrev_u32 v0, 32, v3\nMvaa 176,224,216\nRa 156,47,18432\n"
"Mvaa 180,232,216\nTa 160,32,0\ns_lshl_b32 s2, s11, 13\nMvaa 180,236,220\nTa 162,32,2048\nbuffer_load_dwordx4 v[216:219], v1, s[16:19], s6 offen offset:2048 sc1\nMvaa 148,236,212\n"
"Ta 164,32,16384\ns_add_u32 s2, s2, 0x20000\nMvaa 148,232,208\nTa 166,32,18432\nMvaa 116,232,200\nTa 168,33,0\nv_add_u32 v22, s2, v1\nMvaa 116,236,204\nTa 170,33,2048\n"
"buffer_load_dwordx4 v[220:223], v1, s[16:19], s6 offen offset:3072 sc1\nMvaa 84,236,196\nTa 172,33,16384\nv_xor_b32 v23, 0x2000, v22\nMvaa 84,232,192\nTa 174,33,18432\n"
"s_add_u32 m0, s47, -32768\ns_add_u32 s46, s45, 0x8000\nMvaa 88,240,192\nTa 176,34,0\nv_mov_b32 v4, 0\nMvaa 88,244,196\nTa 178,34,2048\nD1 1,16,46,0\nMvaa 120,244,204\n"
"Ta 180,34,16384\nv_mov_b32 v5, 0\nMvaa 120,240,200\nTa 182,34,18432\nMvaa 152,240,208\nTa 184,35,0\nv_mov_b32 v6, 0\nMvaa 152,244,212\nTa 186,35,2048\nD1 1,16,46,1024\n"
"Mvaa 184,244,220\nTa 188,35,16384\nv_mov_b32 v7, 0\nMvaa 184,240,216\nTa 190,35,18432\nMvaa 188,248,216\nv_mov_b32 v8, 0\nv_mov_b32 v9, 0\nMvaa 188,252,220\nD1 1,16,46,2048\n"
"Mvaa 156,252,212\nv_mov_b32 v10, 0\nv_mov_b32 v11, 0\nMvaa 156,248,208\nv_mov_b32 v12, 0\nMvaa 124,248,200\nv_mov_b32 v13, 0\nv_mov_b32 v14, 0\nMvaa 124,252,204\nD1 1,16,46,3072\n"
"Mvaa 92,252,196\nv_mov_b32 v15, 0\nv_mov_b32 v16, 0\nMvaa 92,248,192\nWL 0\nMvaa 64,160,128\nTa 224,36,0\nv_mov_b32 v17, 0\nMvaa 64,164,132\nTa 226,36,2048\n"
"s_add_u32 m0, s47, -16384\ns_add_u32 s46, s45, 0x400000\nD1 1,16,46,0\nMvaa 68,172,132\nv_mov_b32 v18, 0\nv_mov_b32 v19, 0\nMvaa 68,168,128\nTa 228,36,16384\nMvaa 72,176,128\n"
"Ta 230,36,18432\nMvaa 72,180,132\nD1 1,16,46,1024\nMvaa 76,188,132\nTa 232,37,0\nMvaa 76,184,128\nTa 234,37,2048\nMvaa 108,184,136\nv_mul_f32 v64, s57, v64\n"
"v_mul_f32 v65, s57, v65\nMvaa 108,188,140\nTa 236,37,16384\nD1 1,16,46,2048\nMvaa 104,180,140\nTa 238,37,18432\nCV 64,64,65\nMvaa 104,176,136\nv_mul_f32 v66, s57, v66\n"
"v_mul_f32 v67, s57, v67\nMvaa 100,168,136\nTa 240,38,0\nCV 65,66,67\nMvaa 100,172,140\nTa 242,38,2048\nD1 1,16,46,3072\nMvaa 96,164,140\nv_mul_f32 v68, s57, v68\n"
"v_mul_f32 v69, s57, v69\nMvaa 96,160,136\nTa 244,38,16384\nCV 66,68,69\nMvaa 128,160,144\nTa 246,38,18432\nv_mul_f32 v70, s57, v70\nMvaa 128,164,148\nv_mul_f32 v71, s57, v71\n"
"Mvaa 132,172,148\nTa 248,39,0\nCV 67,70,71\nMvaa 132,168,144\nTa 250,39,2048\nAW 192,64\nMvaa 136,176,144\nAW 193,65\nAW 194,66\nMvaa 136,180,148\nTa 252,39,16384\nAW 195,67\n"
"Mvaa 140,188,148\nTa 254,39,18432\nv_mul_f32 v72, s57, v72\nMvaa 140,184,144\nv_mul_f32 v73, s57, v73\nCV 72,72,73\nMvaa 172,184,152\nv_mul_f32 v74, s57, v74\n"
"v_mul_f32 v75, s57, v75\nMvaa 172,188,156\nCV 73,74,75\nv_mul_f32 v76, s57, v76\nMvaa 168,180,156\nv_mul_f32 v77, s57, v77\nCV 74,76,77\nMvaa 168,176,152\nv_mul_f32 v78, s57, v78\n"
"v_mul_f32 v79, s57, v79\nMvaa 164,168,152\nCV 75,78,79\nAW 200,72\nMvaa 164,172,156\nAW 201,73\nAW 202,74\nMvaa 160,164,156\nAW 203,75\nv_mul_f32 v96, s57, v96\nMvaa 160,160,152\n"
"s_waitcnt vmcnt(21) lgkmcnt(0)\ns_barrier\nMvaa 80,224,128\nv_mul_f32 v97, s57, v97\nCV 96,96,97\nMvaa 80,228,132\ns_add_u32 m0, s47, 0\ns_add_u32 s46, s86, 0xffff0000\n"
"D1 1,16,46,0\nMvaa 84,236,132\nv_mul_f32 v98, s57, v98\nv_mul_f32 v99, s57, v99\nMvaa 84,232,128\nCV 97,98,99\nv_mul_f32 v100, s57, v100\nMvaa 88,240,128\n"
"v_mul_f32 v101, s57, v101\nCV 98,100,101\nMvaa 88,244,132\nD1 1,16,46,1024\nv_mul_f32 v102, s57, v102\nMvaa 92,252,132\nv_mul_f32 v103, s57, v103\nCV 99,102,103\nMvaa 92,248,128\n"
"AW 196,96\nAW 197,97\nMvaa 124,248,136\nAW 198,98\nAW 199,99\nMvaa 124,252,140\nD1 1,16,46,2048\nv_mul_f32 v128, s57, v128\nMvaa 120,244,140\nv_mul_f32 v129, s57, v129\n"
"CV 128,128,129\nMvaa 120,240,136\nv_mul_f32 v130, s57, v130\nv_mul_f32 v131, s57, v131\nMvaa 116,232,136\nRa 160,24,0\nCV 129,130,131\nMvaa 116,236,140\nD1 1,16,46,3072\n"
"v_mul_f32 v132, s57, v132\nMvaa 112,228,140\nRa 164,24,1024\nv_mul_f32 v133, s57, v133\nMvaa 112,224,136\ns_add_u32 m0, s47, 16384\ns_add_u32 s46, s45, 0x408000\nMvaa 144,224,144\n"
"Ra 168,24,2048\nCV 130,132,133\nMvaa 144,228,148\nD1 1,16,46,0\nMvaa 148,236,148\nRa 172,24,3072\nv_mul_f32 v134, s57, v134\nMvaa 148,232,144\nv_mul_f32 v135, s57, v135\n"
"CV 131,134,135\nMvaa 152,240,144\nRa 176,26,0\nds_write_b128 v23, v[128:131] offset:0\nMvaa 152,244,148\nD1 1,16,46,1024\nv_mul_f32 v80, s57, v80\nMvaa 156,252,148\n"
"Ra 180,26,1024\nv_mul_f32 v81, s57, v81\nMvaa 156,248,144\nCV 80,80,81\nv_mul_f32 v82, s57, v82\nMvaa 188,248,152\nRa 184,26,2048\nv_mul_f32 v83, s57, v83\nMvaa 188,252,156\n"
"D1 1,16,46,2048\nCV 81,82,83\nMvaa 184,244,156\nRa 188,26,3072\nv_mul_f32 v84, s57, v84\nMvaa 184,240,152\nv_mul_f32 v85, s57, v85\nCV 82,84,85\nMvaa 180,232,152\n"
"v_mul_f32 v86, s57, v86\nv_mul_f32 v87, s57, v87\nMvaa 180,236,156\nD1 1,16,46,3072\nCV 83,86,87\nMvaa 176,228,156\nAW 208,80\nAW 209,81\nMvaa 176,224,152\n"
"s_waitcnt vmcnt(16) lgkmcnt(5)\nCvav 32,160,192,4\nRa 128,24,8192\nAW 210,82\nMvav 32,164,200\nAW 211,83\nMvav 32,168,208\nRa 132,24,9216\nv_mul_f32 v88, s57, v88\n"
"Mvav 32,172,216\nv_mul_f32 v89, s57, v89\nCV 88,88,89\nCvav 36,172,220,8\nRa 136,24,10240\nv_mul_f32 v90, s57, v90\nMvav 36,168,212\ns_add_u32 m0, s89, 32768\n"
"v_mul_f32 v91, s57, v91\nMvav 36,164,204\nRa 140,24,11264\nWL 4\ns_barrier\nMvav 36,160,196\nCV 89,90,91\nv_mul_f32 v92, s57, v92\nMvav 36,176,228\nRa 144,26,8192\n"
"Sa 96,1,16,78,0\nMvav 36,180,236\ns_add_u32 s46, s86, s88\nv_mul_f32 v93, s57, v93\nMvav 36,184,244\nRa 148,26,9216\nCV 90,92,93\nMvav 36,188,252\nv_mul_f32 v94, s57, v94\n"
"v_mul_f32 v95, s57, v95\nMvav 32,188,248\nRa 152,26,10240\nCV 91,94,95\nMvav 32,184,240\nD1 1,16,46,0\nAW 216,88\nMvav 32,180,232\nRa 156,26,11264\nWL 4\nMvav 32,176,224\n"
"AW 217,89\nAW 218,90\nCvav 40,128,192,12\nSa 100,1,16,78,1024\nAW 219,91\nMvav 40,132,200\nv_mov_b32 v86, 0\nMvav 40,136,208\nv_mov_b32 v87, 0\nv_mov_b32 v88, 0\nMvav 40,140,216\n"
"v_mov_b32 v89, 0\nv_mul_f32 v136, s57, v136\nv_mul_f32 v137, s57, v137\nCV 136,136,137\nv_mul_f32 v138, s57, v138\nv_mul_f32 v139, s57, v139\nCV 137,138,139\nCvav 44,140,220,16\n"
"v_and_b32 v27, 0x70, v1\nbuffer_load_dword v27, v27, s[16:19], s61 offen offset:136 sc0 sc1\nv_mul_f32 v140, s57, v140\nv_mul_f32 v141, s57, v141\nCV 138,140,141\n"
"v_mul_f32 v142, s57, v142\nv_mul_f32 v143, s57, v143\nMvav 44,136,212\nD1 1,16,46,1024\nCV 139,142,143\nds_write_b128 v23, v[136:139] offset:2048\nv_mul_f32 v144, s57, v144\n"
"v_mul_f32 v145, s57, v145\nCV 144,144,145\nv_mul_f32 v146, s57, v146\nMvav 44,132,204\nWL 1\nv_mul_f32 v147, s57, v147\nCV 145,146,147\nv_mul_f32 v148, s57, v148\n"
"v_mul_f32 v149, s57, v149\nCV 146,148,149\nMvav 44,128,196\nv_mul_f32 v150, s57, v150\nv_mul_f32 v151, s57, v151\nCV 147,150,151\nds_write_b128 v23, v[144:147] offset:4096\n"
"v_mul_f32 v152, s57, v152\nv_mul_f32 v153, s57, v153\nCV 152,152,153\nMvav 44,144,228\nSa 104,1,16,78,2048\nv_mul_f32 v154, s57, v154\nv_mul_f32 v155, s57, v155\nCV 153,154,155\n"
"v_mul_f32 v156, s57, v156\nv_mul_f32 v157, s57, v157\nCV 154,156,157\nMvav 44,148,236\ns_add_u32 s88, s88, 32768\nEX 80,32\nv_mul_f32 v158, s57, v158\nv_mul_f32 v159, s57, v159\n"
"CV 155,158,159\nds_write_b128 v23, v[152:155] offset:6144\nMvav 44,152,244\nv_mul_f32 v160, s57, v160\nv_mul_f32 v161, s57, v161\nCV 160,160,161\nv_mul_f32 v162, s57, v162\n"
"v_mul_f32 v163, s57, v163\nCV 161,162,163\nv_mul_f32 v164, s57, v164\nMvav 44,156,252\nEX 81,33\nv_mul_f32 v165, s57, v165\nCV 162,164,165\nv_mul_f32 v166, s57, v166\n"
"Mvav 40,156,248\nv_mul_f32 v167, s57, v167\nCV 163,166,167\nds_write_b128 v23, v[160:163] offset:1024\nv_mul_f32 v168, s57, v168\nv_mul_f32 v169, s57, v169\nCV 168,168,169\n"
"v_mul_f32 v170, s57, v170\nMvav 40,152,240\nD1 1,16,46,2048\nv_mul_f32 v171, s57, v171\nCV 169,170,171\nv_mul_f32 v172, s57, v172\nv_mul_f32 v173, s57, v173\nCV 170,172,173\n"
"v_mul_f32 v174, s57, v174\nMvav 40,148,232\nv_mul_f32 v175, s57, v175\nCV 171,174,175\nds_write_b128 v23, v[168:171] offset:3072\nv_mul_f32 v176, s57, v176\n"
"v_mul_f32 v177, s57, v177\nCV 176,176,177\nv_mul_f32 v178, s57, v178\nMvav 40,144,224\nAD 84,80,81\nCV 64,80,81\nv_mul_f32 v179, s57, v179\nCV 177,178,179\n"
"v_mul_f32 v180, s57, v180\nv_mul_f32 v181, s57, v181\nCV 178,180,181\nSa 108,1,16,78,3072\nv_mul_f32 v182, s57, v182\nv_mul_f32 v183, s57, v183\nCV 179,182,183\n"
"ds_write_b128 v23, v[176:179] offset:5120\nv_mul_f32 v184, s57, v184\nv_mul_f32 v185, s57, v185\nEX 82,34\nCV 184,184,185\nv_mul_f32 v186, s57, v186\nv_mul_f32 v187, s57, v187\n"
"CV 185,186,187\nv_mul_f32 v188, s57, v188\nv_mul_f32 v189, s57, v189\nCV 186,188,189\nv_mul_f32 v190, s57, v190\nv_mul_f32 v191, s57, v191\nCV 187,190,191\n"
"ds_write_b128 v23, v[184:187] offset:7168\ns_waitcnt vmcnt(16) lgkmcnt(0)\nEX 83,35\nAD 84,84,82\nD1 1,16,46,3072\ns_add_u32 m0, s89, 49152\ns_barrier\nAD 84,84,83\nCV 65,82,83\n"
"Ra 128,24,32768\nRa 224,22,0\nRa 228,22,1024\ns_add_u32 s46, s87, s88\nEX 80,36\nRa 132,24,33792\nRa 232,22,2048\nRa 236,22,3072\nRa 240,22,4096\nEX 81,37\nRa 136,24,34816\n"
"Ra 244,22,5120\nRa 248,22,6144\nD1 1,16,46,0\nRa 252,22,7168\nv_mul_f32 v104, s57, v104\nv_mul_f32 v105, s57, v105\nRa 140,24,35840\nCV 104,104,105\nv_mul_f32 v106, s57, v106\n"
"AD 85,80,81\nCV 68,80,81\nv_mul_f32 v107, s57, v107\nRa 144,26,32768\nCV 105,106,107\nv_mul_f32 v108, s57, v108\nv_mul_f32 v109, s57, v109\nEX 82,38\nCV 106,108,109\n"
"Ra 148,26,33792\nv_mul_f32 v110, s57, v110\nv_mul_f32 v111, s57, v111\nEX 83,39\nRa 152,26,34816\nAD 85,85,82\nCV 107,110,111\nAW 204,104\nD1 1,16,46,1024\nAW 205,105\nAW 206,106\n"
"Ra 156,26,35840\nAW 207,107\nv_mul_f32 v112, s57, v112\nAD 85,85,83\nCV 69,82,83\nv_mul_f32 v113, s57, v113\nCV 112,112,113\nRa 160,24,40960\nv_mul_f32 v114, s57, v114\n"
"v_mul_f32 v115, s57, v115\ns_xor_b32 s89, s89, 0x10000\nEX 80,40\nRa 164,24,41984\nCV 113,114,115\nv_mul_f32 v116, s57, v116\nv_mul_f32 v117, s57, v117\nWL 6\nEX 81,41\n"
"Ra 168,24,43008\nAD 84,84,80\nCV 114,116,117\nD1 1,16,46,2048\nv_mul_f32 v118, s57, v118\nv_mul_f32 v119, s57, v119\nCV 115,118,119\nRa 172,24,44032\nAW 212,112\nAW 213,113\n"
"AD 84,84,81\nCV 66,80,81\nAW 214,114\nCvav 48,128,192,4\nRa 176,26,40960\nAW 215,115\nv_mul_f32 v120, s57, v120\nv_mul_f32 v121, s57, v121\nMvav 48,132,200\nEX 82,42\n"
"CV 120,120,121\nMvav 48,136,208\nRa 180,26,41984\nv_mul_f32 v122, s57, v122\nv_mul_f32 v123, s57, v123\nMvav 48,140,216\nWL 6\nEX 83,43\nWV 8\nv_cmp_ne_u32 vcc, s15, v27\ns_nop 0\n"
"s_cbranch_vccnz .S285\n.S286:\nCvav 52,140,220,8\nRa 184,26,43008\nAD 84,84,82\nCV 121,122,123\nv_mul_f32 v124, s57, v124\nMvav 52,136,212\nD1 1,16,46,3072\ns_add_u32 m0, s89, 0\n"
"v_mul_f32 v125, s57, v125\nMvav 52,132,204\nRa 188,26,44032\nCV 122,124,125\nv_mul_f32 v126, s57, v126\nMvav 52,128,196\nAD 84,84,83\nCV 67,82,83\nv_mul_f32 v127, s57, v127\n"
"CV 123,126,127\nMvav 52,144,228\nRa 128,2,49152\ns_add_u32 s78, s78, s55\nSa 112,1,16,78,0\nMvav 52,148,236\ns_add_u32 s46, s86, s88\nEX 80,44\nMvav 52,152,244\nRa 132,2,50176\n"
"v_xor_b32 v24, 0x10000, v24\nAW 220,120\nAW 221,121\nMvav 52,156,252\nWL 6\nEX 81,45\nMvav 48,156,248\nRa 136,2,51200\nAD 85,85,80\nAW 222,122\nMvav 48,152,240\nD1 1,16,46,0\n"
"AW 223,123\nv_mov_b32 v112, -1\nv_mov_b32 v113, -1\nMvav 48,148,232\nRa 140,2,52224\nv_mov_b32 v114, -1\nv_mov_b32 v115, -1\nMvav 48,144,224\nAD 85,85,81\nCV 70,80,81\n"
"Cvav 56,160,192,12\nRa 144,2,53248\nSa 116,1,16,78,1024\nMvav 56,164,200\nEX 82,46\nMvav 56,168,208\nRa 148,2,54272\nv_xor_b32 v26, 0x10000, v26\nMvav 56,172,216\nWL 6\nEX 83,47\n"
"Cvav 60,172,220,16\nRa 152,2,55296\nAD 85,85,82\nMvav 60,168,212\nD1 1,16,46,1024\nMvav 60,164,204\nRa 156,2,56320\nMvav 60,160,196\nAD 85,85,83\nCV 71,82,83\nMvav 60,176,228\n"
"Ra 160,2,57344\nv_max_f32 v90, v84, v85\nMvav 60,180,236\ns_add_u32 s88, s88, 32768\nEX 80,48\nMvav 60,184,244\nRa 164,2,58368\nv_cmp_lt_f32 vcc, s63, v90\nMvav 60,188,252\nWL 8\n"
"EX 81,49\nMvav 56,188,248\nRa 168,2,59392\ns_cbranch_vccnz .T274\n.T275:\nv_add_f32 v20, s93, v84\nv_mov_b32 v21, v85\nv_mov_b32 v22, v20\nv_mov_b32 v23, v21\nMvav 56,184,240\n"
"D1 1,16,46,2048\nv_permlane32_swap_b32 v20, v22\nv_permlane32_swap_b32 v21, v23\nAD 20,20,22\nAD 21,21,23\nv_mov_b32 v22, v20\nv_mov_b32 v23, v21\nMvav 56,180,232\nRa 172,2,60416\n"
"v_permlane16_swap_b32 v20, v22\nv_permlane16_swap_b32 v21, v23\nAD 20,20,22\nAD 21,21,23\nv_min_f32 v90, v20, v21\nv_cmp_gt_f32 vcc, s64, v90\ns_nop 0\ns_cbranch_vccnz .T276\n"
".T277:\nAD 86,86,84\nMvav 56,176,224\nAD 84,80,81\nCV 72,80,81\nZaav 0,128,64\nRa 176,2,61440\nSa 120,1,16,78,2048\nZaav 4,128,68\nEX 82,50\nZaav 12,132,68\nRa 180,2,62464\n"
"Zaav 8,132,64\nWL 8\nEX 83,51\nZaav 16,136,64\nRa 184,2,63488\nAD 84,84,82\nZaav 20,136,68\nD1 1,16,46,3072\ns_add_u32 m0, s89, 16384\nZaav 28,140,68\nRa 188,2,64512\ns_barrier\n"
"Zaav 24,140,64\nAD 84,84,83\nCV 73,82,83\nZaav 32,144,64\nRa 128,24,0\nSa 124,1,16,78,3072\nZaav 36,144,68\ns_add_u32 s46, s87, s88\nEX 80,52\nZaav 44,148,68\nRa 132,24,1024\n"
"v_xor_b32 v2, 0x10000, v2\nZaav 40,148,64\nWL 8\nEX 81,53\nZaav 48,152,64\nRa 136,24,2048\nAD 87,87,85\nZaav 52,152,68\nD1 1,16,46,0\nZaav 60,156,68\nRa 140,24,3072\n"
"Zaav 56,156,64\nAD 85,80,81\nCV 76,80,81\nZaav 64,160,64\nRa 144,26,0\nZaav 68,160,68\ns_cmp_lt_u32 s99, s92\nEX 82,54\nZaav 76,164,68\nRa 148,26,1024\ns_cbranch_scc0 .E272\n"
"v_cmpx_gt_i32 vcc, 19, v3\nv_mov_b32 v15, s97\nv_cmpx_gt_i32 vcc, 18, v3\nv_mov_b32 v14, s97\nv_cmpx_gt_i32 vcc, 17, v3\nv_mov_b32 v13, s97\nv_cmpx_gt_i32 vcc, 16, v3\n"
"v_mov_b32 v12, s97\nv_cmpx_gt_i32 vcc, 3, v3\nv_mov_b32 v7, s97\nv_cmpx_gt_i32 vcc, 2, v3\nv_mov_b32 v6, s97\nv_cmpx_gt_i32 vcc, 1, v3\nv_mov_b32 v5, s97\n"
"v_cmpx_gt_i32 vcc, 0, v3\nv_mov_b32 v4, s97\ns_mov_b64 exec, -1\ns_nop 2\n.E272:\ns_nop 0\nZaav 72,164,64\nWL 8\nEX 83,55\nZaav 80,168,64\nRa 152,26,2048\nAD 85,85,82\n"
"Zaav 84,168,68\nD1 1,16,46,1024\nZaav 92,172,68\nRa 156,26,3072\nZaav 88,172,64\nAD 85,85,83\nCV 77,82,83\nZaav 96,176,64\nRa 160,24,8192\nZaav 100,176,68\nEX 80,56\n"
"Zaav 108,180,68\nRa 164,24,9216\nZaav 104,180,64\nWL 6\nEX 81,57\nZaav 112,184,64\nRa 168,24,10240\nAD 84,84,80\nv_and_b32 v27, 0x70, v1\n"
"buffer_load_dword v27, v27, s[16:19], s61 offen offset:140 sc0 sc1\nZaav 116,184,68\nD1 1,16,46,2048\nZaav 124,188,68\nRa 172,24,11264\nZaav 120,188,64\nAD 84,84,81\nCV 74,80,81\n"
"s_sub_u32 s99, s99, 1\n.p2align 6\n.A300:\nCvav 32,128,192,4\nRa 176,26,8192\nMvav 32,132,200\nEX 82,58\nMvav 32,136,208\nRa 180,26,9216\nMvav 32,140,216\nWL 6\nEX 83,59\n"
"Cvav 36,140,220,8\nRa 184,26,10240\nAD 84,84,82\nMvav 36,136,212\nD1 1,16,46,3072\ns_add_u32 m0, s89, 32768\nMvav 36,132,204\nRa 188,26,11264\nMvav 36,128,196\nAD 84,84,83\n"
"CV 75,82,83\nMvav 36,144,228\nRa 128,2,16384\nMvav 36,148,236\ns_add_u32 s46, s86, s88\nEX 80,60\nMvav 36,152,244\nRa 132,2,17408\nMvav 36,156,252\nWL 6\nEX 81,61\n"
"Mvav 32,156,248\nRa 136,2,18432\nAD 85,85,80\nMvav 32,152,240\nD1 1,16,46,0\nMvav 32,148,232\nRa 140,2,19456\nSv 112,1,16,49,0\nMvav 32,144,224\nAD 85,85,81\nCV 78,80,81\n"
"Cvav 40,160,192,12\nRa 144,2,20480\nMvav 40,164,200\nEX 82,62\nMvav 40,168,208\nRa 148,2,21504\nMvav 40,172,216\nWL 6\nEX 83,63\nCvav 44,172,220,16\nRa 152,2,22528\nAD 85,85,82\n"
"Mvav 44,168,212\nD1 1,16,46,1024\nMvav 44,164,204\nRa 156,2,23552\nMvav 44,160,196\nAD 85,85,83\nCV 79,82,83\nMvav 44,176,228\nRa 160,2,24576\nv_max_f32 v90, v84, v85\n"
"Mvav 44,180,236\ns_add_u32 s88, s88, 32768\nEX 80,32\nMvav 44,184,244\nRa 164,2,25600\nv_cmp_lt_f32 vcc, s63, v90\nMvav 44,188,252\nWL 8\nEX 81,33\nMvav 40,188,248\n"
"Ra 168,2,26624\ns_cbranch_vccnz .T308\n.T309:\nMvav 40,184,240\nD1 1,16,46,2048\nMvav 40,180,232\nRa 172,2,27648\nAD 86,86,84\nMvav 40,176,224\nAD 84,80,81\nCV 64,80,81\n"
"Maav 0,128,72\nRa 176,2,28672\nMaav 4,128,76\nEX 82,34\nMaav 12,132,76\nRa 180,2,29696\nMaav 8,132,72\ns_waitcnt vmcnt(13) lgkmcnt(8)\nEX 83,35\nMaav 16,136,72\nRa 184,2,30720\n"
"AD 84,84,82\nMaav 20,136,76\nD1 1,16,46,3072\ns_add_u32 m0, s89, 49152\nMaav 28,140,76\nRa 188,2,31744\ns_barrier\nMaav 24,140,72\nAD 84,84,83\nCV 65,82,83\nMaav 32,144,72\n"
"Ra 128,24,32768\nMaav 36,144,76\ns_add_u32 s46, s87, s88\nEX 80,36\nMaav 44,148,76\nRa 132,24,33792\nMaav 40,148,72\nWL 8\nEX 81,37\nMaav 48,152,72\nRa 136,24,34816\nAD 87,87,85\n"
"Maav 52,152,76\nD1 1,16,46,0\nMaav 60,156,76\nRa 140,24,35840\nMaav 56,156,72\nAD 85,80,81\nCV 68,80,81\nMaav 64,160,72\nRa 144,26,32768\nMaav 68,160,76\ns_cmp_eq_u32 s99, 0\n"
"EX 82,38\nMaav 76,164,76\nRa 148,26,33792\ns_cbranch_scc1 .E302\n.E303:\nMaav 72,164,72\nWL 8\nEX 83,39\nMaav 80,168,72\nRa 152,26,34816\nAD 85,85,82\nMaav 84,168,76\n"
"D1 1,16,46,1024\nMaav 92,172,76\nRa 156,26,35840\nMaav 88,172,72\nAD 85,85,83\nCV 69,82,83\nMaav 96,176,72\nRa 160,24,40960\nMaav 100,176,76\ns_xor_b32 s89, s89, 0x10000\n"
"EX 80,40\nMaav 108,180,76\nRa 164,24,41984\nMaav 104,180,72\nWL 6\nEX 81,41\nMaav 112,184,72\nRa 168,24,43008\nAD 84,84,80\nMaav 116,184,76\nD1 1,16,46,2048\nMaav 124,188,76\n"
"Ra 172,24,44032\nMaav 120,188,72\nAD 84,84,81\nCV 66,80,81\nCvav 48,128,192,4\nRa 176,26,40960\nMvav 48,132,200\ns_cmp_eq_u32 s99, 0\nEX 82,42\nMvav 48,136,208\nRa 180,26,41984\n"
"s_cselect_b32 s86, s94, s86\nMvav 48,140,216\nWL 6\nEX 83,43\nCvav 52,140,220,8\nRa 184,26,43008\nAD 84,84,82\nMvav 52,136,212\nD1 1,16,46,3072\ns_add_u32 m0, s89, 0\n"
"Mvav 52,132,204\nRa 188,26,44032\nMvav 52,128,196\nAD 84,84,83\nCV 67,82,83\nMvav 52,144,228\nRa 128,2,49152\nMvav 52,148,236\ns_add_u32 s46, s86, s88\nEX 80,44\nMvav 52,152,244\n"
"Ra 132,2,50176\nv_xor_b32 v24, 0x10000, v24\nMvav 52,156,252\nWL 6\nEX 81,45\nMvav 48,156,248\nRa 136,2,51200\nAD 85,85,80\nMvav 48,152,240\nD1 1,16,46,0\nMvav 48,148,232\n"
"Ra 140,2,52224\nSv 112,1,16,49,1024\nMvav 48,144,224\nAD 85,85,81\nCV 70,80,81\nCvav 56,160,192,12\nRa 144,2,53248\nMvav 56,164,200\ns_add_u32 s49, s49, 131072\nEX 82,46\n"
"Mvav 56,168,208\nRa 148,2,54272\nv_xor_b32 v26, 0x10000, v26\nMvav 56,172,216\nWL 6\nEX 83,47\nCvav 60,172,220,16\nRa 152,2,55296\nAD 85,85,82\nMvav 60,168,212\nD1 1,16,46,1024\n"
"Mvav 60,164,204\nRa 156,2,56320\nMvav 60,160,196\nAD 85,85,83\nCV 71,82,83\nMvav 60,176,228\nRa 160,2,57344\nv_max_f32 v90, v84, v85\nMvav 60,180,236\ns_add_u32 s88, s88, 32768\n"
"EX 80,48\nMvav 60,184,244\nRa 164,2,58368\nv_cmp_lt_f32 vcc, s63, v90\nMvav 60,188,252\nWL 8\nEX 81,49\nMvav 56,188,248\nRa 168,2,59392\ns_cbranch_vccnz .T310\n.T311:\n"
"Mvav 56,184,240\nD1 1,16,46,2048\nMvav 56,180,232\nRa 172,2,60416\nAD 86,86,84\nMvav 56,176,224\nAD 84,80,81\nCV 72,80,81\nMaav 0,128,64\nRa 176,2,61440\nMaav 4,128,68\nEX 82,50\n"
"Maav 12,132,68\nRa 180,2,62464\nMaav 8,132,64\ns_waitcnt vmcnt(13) lgkmcnt(8)\nEX 83,51\nMaav 16,136,64\nRa 184,2,63488\nAD 84,84,82\nMaav 20,136,68\nD1 1,16,46,3072\n"
"s_add_u32 m0, s89, 16384\nMaav 28,140,68\nRa 188,2,64512\ns_barrier\nMaav 24,140,64\nAD 84,84,83\nCV 73,82,83\nMaav 32,144,64\nRa 128,24,0\nMaav 36,144,68\n"
"s_add_u32 s46, s87, s88\nEX 80,52\nMaav 44,148,68\nRa 132,24,1024\nv_xor_b32 v2, 0x10000, v2\nMaav 40,148,64\nWL 8\nEX 81,53\nMaav 48,152,64\nRa 136,24,2048\nAD 87,87,85\n"
"Maav 52,152,68\nD1 1,16,46,0\nMaav 60,156,68\nRa 140,24,3072\nMaav 56,156,64\nAD 85,80,81\nCV 76,80,81\nMaav 64,160,64\nRa 144,26,0\nMaav 68,160,68\ns_cmp_lt_u32 s99, s92\n"
"EX 82,54\nMaav 76,164,68\nRa 148,26,1024\ns_cbranch_scc1 .E304\n.E305:\nMaav 72,164,64\nWL 8\nEX 83,55\nMaav 80,168,64\nRa 152,26,2048\nAD 85,85,82\nMaav 84,168,68\n"
"D1 1,16,46,1024\nMaav 92,172,68\nRa 156,26,3072\nMaav 88,172,64\nAD 85,85,83\nCV 77,82,83\nMaav 96,176,64\nRa 160,24,8192\nMaav 100,176,68\ns_sub_u32 s90, s90, 1\nEX 80,56\n"
"Maav 108,180,68\nRa 164,24,9216\ns_cbranch_scc1 .E306\n.E307:\nMaav 104,180,64\nWL 6\nEX 81,57\nMaav 112,184,64\nRa 168,24,10240\nAD 84,84,80\nMaav 116,184,68\nD1 1,16,46,2048\n"
"s_sub_u32 s99, s99, 1\nMaav 124,188,68\nRa 172,24,11264\nMaav 120,188,64\nAD 84,84,81\nCV 74,80,81\ns_cbranch_scc0 .A300\ns_branch .A312\n.E302:\nv_cmpx_gt_i32 vcc, 19, v0\n"
"v_mov_b32 v15, s97\nv_cmpx_gt_i32 vcc, 18, v0\nv_mov_b32 v14, s97\nv_cmpx_gt_i32 vcc, 17, v0\nv_mov_b32 v13, s97\nv_cmpx_gt_i32 vcc, 16, v0\nv_mov_b32 v12, s97\n"
"v_cmpx_gt_i32 vcc, 3, v0\nv_mov_b32 v7, s97\nv_cmpx_gt_i32 vcc, 2, v0\nv_mov_b32 v6, s97\nv_cmpx_gt_i32 vcc, 1, v0\nv_mov_b32 v5, s97\nv_cmpx_gt_i32 vcc, 0, v0\nv_mov_b32 v4, s97\n"
"s_mov_b64 exec, -1\ns_nop 2\ns_branch .E303\n.E304:\ns_cmp_eq_u32 s99, 0\ns_cbranch_scc1 .K301\nv_cmpx_gt_i32 vcc, 19, v3\nv_mov_b32 v15, s97\nv_cmpx_gt_i32 vcc, 18, v3\n"
"v_mov_b32 v14, s97\nv_cmpx_gt_i32 vcc, 17, v3\nv_mov_b32 v13, s97\nv_cmpx_gt_i32 vcc, 16, v3\nv_mov_b32 v12, s97\nv_cmpx_gt_i32 vcc, 3, v3\nv_mov_b32 v7, s97\n"
"v_cmpx_gt_i32 vcc, 2, v3\nv_mov_b32 v6, s97\nv_cmpx_gt_i32 vcc, 1, v3\nv_mov_b32 v5, s97\nv_cmpx_gt_i32 vcc, 0, v3\nv_mov_b32 v4, s97\ns_mov_b64 exec, -1\ns_nop 2\ns_branch .E305\n"
".K301:\nv_cmpx_gt_i32 vcc, 19, v3\nv_mov_b32 v19, s97\nv_cmpx_gt_i32 vcc, 18, v3\nv_mov_b32 v18, s97\nv_cmpx_gt_i32 vcc, 17, v3\nv_mov_b32 v17, s97\nv_cmpx_gt_i32 vcc, 16, v3\n"
"v_mov_b32 v16, s97\nv_cmpx_gt_i32 vcc, 3, v3\nv_mov_b32 v11, s97\nv_cmpx_gt_i32 vcc, 2, v3\nv_mov_b32 v10, s97\nv_cmpx_gt_i32 vcc, 1, v3\nv_mov_b32 v9, s97\n"
"v_cmpx_gt_i32 vcc, 0, v3\nv_mov_b32 v8, s97\ns_mov_b64 exec, -1\ns_nop 2\ns_branch .E305\n.E306:\ns_swappc_b64 s[80:81], s[8:9]\ns_branch .E307\n.HD265:\n"
"v_cmp_ne_u32 vcc, s15, v27\ns_nop 0\ns_cbranch_vccnz .S288\n.S289:\nbuffer_store_dword v25, off, s[16:19], s54 offset:4 sc0 sc1\nv_and_b32 v27, 0x70, v1\n"
"buffer_load_dword v27, v27, s[16:19], s61 offen offset:12 sc0 sc1\ns_mov_b32 s90, 1\ns_swappc_b64 s[8:9], s[80:81]\nv_cmp_ne_u32 vcc, s15, v27\ns_nop 0\ns_cbranch_vccnz .S291\n"
".S292:\nv_and_b32 v27, 0x70, v1\nbuffer_load_dword v27, v27, s[16:19], s61 offen offset:8 sc0 sc1\ns_mov_b32 s90, 1\ns_swappc_b64 s[8:9], s[80:81]\nv_cmp_ne_u32 vcc, s15, v27\n"
"s_nop 0\ns_cbranch_vccnz .S294\n.S295:\nv_and_b32 v27, 0xf0, v1\nbuffer_load_dword v27, v27, s[16:19], s61 offen offset:4 sc0 sc1\ns_mov_b32 s90, 1\ns_swappc_b64 s[8:9], s[80:81]\n"
"v_cmp_ne_u32 vcc, s15, v27\ns_nop 0\ns_cbranch_vccnz .S297\n.S298:\ns_mov_b32 s90, 100\ns_swappc_b64 s[8:9], s[80:81]\n.A312:\nRa 176,26,8192\nEX 82,58\nRa 180,26,9216\nWL 6\n"
"EX 83,59\nCvav 36,140,220,8\nRa 184,26,10240\nAD 84,84,82\nMvav 36,136,212\nD1 1,16,46,3072\ns_add_u32 m0, s89, 32768\nMvav 36,132,204\nRa 188,26,11264\nMvav 36,128,196\n"
"AD 84,84,83\nCV 75,82,83\nMvav 36,144,228\nRa 128,2,16384\nMvav 36,148,236\ns_add_u32 s46, s86, s88\nEX 80,60\nMvav 36,152,244\nRa 132,2,17408\nMvav 36,156,252\nEX 81,61\n"
"Ra 136,2,18432\nAD 85,85,80\nD1 1,16,46,0\nRa 140,2,19456\nSv 112,1,16,49,0\nAD 85,85,81\nCV 78,80,81\nRa 144,2,20480\ns_mov_b32 s87, s95\nEX 82,62\nRa 148,2,21504\nWL 6\n"
"EX 83,63\nCvav 44,172,220,16\nRa 152,2,22528\nAD 85,85,82\nMvav 44,168,212\nD1 1,16,46,1024\nMvav 44,164,204\nRa 156,2,23552\nMvav 44,160,196\nAD 85,85,83\nCV 79,82,83\n"
"Mvav 44,176,228\nRa 160,2,24576\nv_max_f32 v90, v84, v85\nMvav 44,180,236\ns_add_u32 s88, s88, 32768\nMvav 44,184,244\nRa 164,2,25600\nv_cmp_lt_f32 vcc, s63, v90\nMvav 44,188,252\n"
"WL 8\nRa 168,2,26624\ns_cbranch_vccnz .T314\n.T315:\nD1 1,16,46,2048\nRa 172,2,27648\nAD 86,86,84\nMaav 0,128,72\nRa 176,2,28672\nMaav 4,128,76\nMaav 12,132,76\nRa 180,2,29696\n"
"Maav 8,132,72\ns_waitcnt vmcnt(13) lgkmcnt(8)\nMaav 16,136,72\nRa 184,2,30720\nMaav 20,136,76\nD1 1,16,46,3072\ns_add_u32 m0, s89, 49152\nMaav 28,140,76\nRa 188,2,31744\n"
"s_barrier\nMaav 24,140,72\nMaav 32,144,72\nRa 128,24,32768\nMaav 36,144,76\ns_add_u32 s46, s87, s88\nEX 80,36\nMaav 44,148,76\nRa 132,24,33792\nMaav 40,148,72\nWL 8\nEX 81,37\n"
"Maav 48,152,72\nRa 136,24,34816\nAD 87,87,85\nMaav 52,152,76\nD1 1,16,46,0\nMaav 60,156,76\nRa 140,24,35840\nMaav 56,156,72\nAD 85,80,81\nCV 68,80,81\nMaav 64,160,72\n"
"Ra 144,26,32768\nMaav 68,160,76\ns_nop 0\nEX 82,38\nMaav 76,164,76\nRa 148,26,33792\nv_cmpx_gt_i32 vcc, 19, v0\nv_mov_b32 v19, s97\nv_cmpx_gt_i32 vcc, 18, v0\nv_mov_b32 v18, s97\n"
"v_cmpx_gt_i32 vcc, 17, v0\nv_mov_b32 v17, s97\nv_cmpx_gt_i32 vcc, 16, v0\nv_mov_b32 v16, s97\nv_cmpx_gt_i32 vcc, 3, v0\nv_mov_b32 v11, s97\nv_cmpx_gt_i32 vcc, 2, v0\n"
"v_mov_b32 v10, s97\nv_cmpx_gt_i32 vcc, 1, v0\nv_mov_b32 v9, s97\nv_cmpx_gt_i32 vcc, 0, v0\nv_mov_b32 v8, s97\ns_mov_b64 exec, -1\ns_nop 2\ns_nop 0\nMaav 72,164,72\nWL 8\nEX 83,39\n"
"Maav 80,168,72\nRa 152,26,34816\nAD 85,85,82\nMaav 84,168,76\nD1 1,16,46,1024\nMaav 92,172,76\nRa 156,26,35840\nMaav 88,172,72\nAD 85,85,83\nCV 69,82,83\nMaav 96,176,72\n"
"Ra 160,24,40960\nMaav 100,176,76\ns_xor_b32 s89, s89, 0x10000\nMaav 108,180,76\nRa 164,24,41984\nMaav 104,180,72\nWL 10\nMaav 112,184,72\nRa 168,24,43008\nMaav 116,184,76\n"
"D1 1,16,46,2048\nMaav 124,188,76\nRa 172,24,44032\nMaav 120,188,72\nRa 176,26,40960\nRa 180,26,41984\nWL 6\nCvav 52,140,220,8\nRa 184,26,43008\nMvav 52,136,212\nD1 1,16,46,3072\n"
"s_add_u32 m0, s89, 0\nMvav 52,132,204\nRa 188,26,44032\nMvav 52,128,196\nMvav 52,144,228\nRa 128,2,49152\nMvav 52,148,236\ns_add_u32 s46, s86, s88\nEX 80,44\nMvav 52,152,244\n"
"Ra 132,2,50176\nv_xor_b32 v24, 0x10000, v24\nMvav 52,156,252\nEX 81,45\nRa 136,2,51200\nAD 85,85,80\nD1 1,16,46,0\nRa 140,2,52224\nSv 112,1,16,49,1024\nAD 85,85,81\nCV 70,80,81\n"
"Ra 144,2,53248\ns_add_u32 s49, s49, 131072\nEX 82,46\nRa 148,2,54272\nv_xor_b32 v26, 0x10000, v26\nWL 6\nEX 83,47\nCvav 60,172,220,16\nRa 152,2,55296\nAD 85,85,82\n"
"Mvav 60,168,212\nD1 1,16,46,1024\nMvav 60,164,204\nRa 156,2,56320\nMvav 60,160,196\nAD 85,85,83\nCV 71,82,83\nMvav 60,176,228\nRa 160,2,57344\nMvav 60,180,236\n"
"s_add_u32 s88, s88, 32768\nMvav 60,184,244\nRa 164,2,58368\nv_cmp_lt_f32 vcc, s63, v85\nMvav 60,188,252\nWL 8\nRa 168,2,59392\ns_cbranch_vccnz .T316\n.T317:\nD1 1,16,46,2048\n"
"Ra 172,2,60416\nRa 176,2,61440\nMaav 4,128,68\nMaav 12,132,68\nRa 180,2,62464\ns_waitcnt vmcnt(13) lgkmcnt(8)\nRa 184,2,63488\nMaav 20,136,68\nD1 1,16,46,3072\n"
"s_add_u32 m0, s89, 16384\nMaav 28,140,68\nRa 188,2,64512\ns_barrier\nRa 128,24,0\nMaav 36,144,68\ns_add_u32 s46, s87, s88\nEX 80,52\nMaav 44,148,68\nRa 132,24,1024\n"
"v_xor_b32 v2, 0x10000, v2\nWL 8\nEX 81,53\nRa 136,24,2048\nAD 87,87,85\nMaav 52,152,68\nD1 1,16,46,0\nMaav 60,156,68\nRa 140,24,3072\nAD 85,80,81\nCV 76,80,81\nRa 144,26,0\n"
"Maav 68,160,68\nEX 82,54\nMaav 76,164,68\nRa 148,26,1024\nWL 8\nEX 83,55\nRa 152,26,2048\nAD 85,85,82\nMaav 84,168,68\nD1 1,16,46,1024\nMaav 92,172,68\nRa 156,26,3072\n"
"AD 85,85,83\nCV 77,82,83\nRa 160,24,8192\nMaav 100,176,68\ns_sub_u32 s90, s90, 1\nMaav 108,180,68\nRa 164,24,9216\nWL 6\nRa 168,24,10240\nMaav 116,184,68\nD1 1,16,46,2048\n"
"Maav 124,188,68\nRa 172,24,11264\n.SW270:\nv_mov_b32 v4, 0\nv_mov_b32 v5, 0\nv_mov_b32 v6, 0\nv_mov_b32 v7, 0\nv_mov_b32 v8, 0\nv_mov_b32 v9, 0\nv_mov_b32 v10, 0\n"
"v_mov_b32 v11, 0\nv_mov_b32 v12, 0\nv_mov_b32 v13, 0\nv_mov_b32 v14, 0\nv_mov_b32 v15, 0\nv_mov_b32 v16, 0\nv_mov_b32 v17, 0\nv_mov_b32 v18, 0\nv_mov_b32 v19, 0\n"
"s_add_u32 s99, s98, 1\ns_mov_b32 s94, s96\ns_add_u32 s95, s96, 65536\ns_cmp_lt_u32 s99, s92\ns_cbranch_scc0 .E318\nv_cmpx_gt_i32 vcc, 19, v3\nv_mov_b32 v15, s97\n"
"v_cmpx_gt_i32 vcc, 18, v3\nv_mov_b32 v14, s97\nv_cmpx_gt_i32 vcc, 17, v3\nv_mov_b32 v13, s97\nv_cmpx_gt_i32 vcc, 16, v3\nv_mov_b32 v12, s97\nv_cmpx_gt_i32 vcc, 3, v3\n"
"v_mov_b32 v7, s97\nv_cmpx_gt_i32 vcc, 2, v3\nv_mov_b32 v6, s97\nv_cmpx_gt_i32 vcc, 1, v3\nv_mov_b32 v5, s97\nv_cmpx_gt_i32 vcc, 0, v3\nv_mov_b32 v4, s97\ns_mov_b64 exec, -1\n"
"s_nop 2\n.E318:\ns_nop 0\nCvaa 32,128,192,4\nRa 176,26,8192\nMvaa 32,132,200\nMvaa 32,136,208\nRa 180,26,9216\nMvaa 32,140,216\nWL 6\nCvaa 36,140,220,8\nRa 184,26,10240\n"
"Mvaa 36,136,212\nD1 1,16,46,3072\ns_add_u32 m0, s89, 32768\nMvaa 36,132,204\nRa 188,26,11264\nMvaa 36,128,196\nMvaa 36,144,228\nRa 128,2,16384\nMvaa 36,148,236\n"
"s_add_u32 s46, s86, s88\nEX 80,60\nMvaa 36,152,244\nRa 132,2,17408\nMvaa 36,156,252\nWL 6\nEX 81,61\nMvaa 32,156,248\nRa 136,2,18432\nAD 85,85,80\nMvaa 32,152,240\nD1 1,16,46,0\n"
"Mvaa 32,148,232\nRa 140,2,19456\nMvaa 32,144,224\nAD 85,85,81\nCV 78,80,81\nCvaa 40,160,192,12\nRa 144,2,20480\nMvaa 40,164,200\nEX 82,62\nMvaa 40,168,208\nRa 148,2,21504\n"
"Mvaa 40,172,216\nWL 6\nEX 83,63\nCvaa 44,172,220,16\nRa 152,2,22528\nAD 85,85,82\nMvaa 44,168,212\nD1 1,16,46,1024\nMvaa 44,164,204\nRa 156,2,23552\nMvaa 44,160,196\nAD 85,85,83\n"
"CV 79,82,83\nMvaa 44,176,228\nRa 160,2,24576\nMvaa 44,180,236\ns_add_u32 s88, s88, 32768\nEX 80,32\nMvaa 44,184,244\nRa 164,2,25600\nv_cmp_lt_f32 vcc, s63, v85\nMvaa 44,188,252\n"
"WL 8\nEX 81,33\nMvaa 40,188,248\nRa 168,2,26624\ns_cbranch_vccnz .T323\n.T324:\nMvaa 40,184,240\nD1 1,16,46,2048\nMvaa 40,180,232\nRa 172,2,27648\nMvaa 40,176,224\nAD 84,80,81\n"
"CV 64,80,81\nRa 176,2,28672\nMaav 4,128,76\nEX 82,34\nMaav 12,132,76\nRa 180,2,29696\ns_waitcnt vmcnt(12) lgkmcnt(8)\nEX 83,35\nRa 184,2,30720\nAD 84,84,82\nMaav 20,136,76\n"
"D1 1,16,46,3072\ns_add_u32 m0, s89, 49152\nMaav 28,140,76\nRa 188,2,31744\ns_barrier\nAR 98,0\nAR 99,1\nCV 100,98,99\nAD 84,84,83\nCV 65,82,83\nAR 98,2\nAR 99,3\nCV 101,98,99\n"
"Ra 128,24,32768\nAR 98,8\nAR 99,9\nCV 102,98,99\nAR 98,10\nMaav 36,144,76\ns_add_u32 s46, s87, s88\nEX 80,36\nAR 99,11\nCV 103,98,99\nMaav 44,148,76\nRa 132,24,33792\n"
"s_add_u32 s5, s66, 0\nSv 100,1,16,5,0\nAR 98,4\nAR 99,5\nWL 8\nEX 81,37\nCV 104,98,99\nAR 98,6\nRa 136,24,34816\nAD 87,87,85\nAR 99,7\nCV 105,98,99\nAR 98,12\nMaav 52,152,76\n"
"D1 1,16,46,0\nv_mov_b32 v96, v86\nv_mov_b32 v97, v87\nAR 99,13\nCV 106,98,99\nMaav 60,156,76\nRa 140,24,35840\nv_permlane32_swap_b32 v86, v96\nAR 98,14\nAR 99,15\nCV 107,98,99\n"
"AD 85,80,81\nCV 68,80,81\ns_add_u32 s5, s66, 0\nSv 104,1,16,5,1024\nAR 98,16\nRa 144,26,32768\nv_permlane32_swap_b32 v87, v97\nAD 86,86,96\nAR 99,17\nCV 108,98,99\nMaav 68,160,76\n"
"s_cmp_eq_u32 s99, 0\nEX 82,38\nMaav 76,164,76\nRa 148,26,33792\ns_cbranch_scc0 .E320\nv_cmpx_gt_i32 vcc, 19, v0\nv_mov_b32 v15, s97\nv_cmpx_gt_i32 vcc, 18, v0\nv_mov_b32 v14, s97\n"
"v_cmpx_gt_i32 vcc, 17, v0\nv_mov_b32 v13, s97\nv_cmpx_gt_i32 vcc, 16, v0\nv_mov_b32 v12, s97\nv_cmpx_gt_i32 vcc, 3, v0\nv_mov_b32 v7, s97\nv_cmpx_gt_i32 vcc, 2, v0\n"
"v_mov_b32 v6, s97\nv_cmpx_gt_i32 vcc, 1, v0\nv_mov_b32 v5, s97\nv_cmpx_gt_i32 vcc, 0, v0\nv_mov_b32 v4, s97\ns_mov_b64 exec, -1\ns_nop 2\n.E320:\nWL 8\nEX 83,39\nAR 98,18\n"
"AR 99,19\nRa 152,26,34816\nAD 85,85,82\nAD 87,87,97\nCV 109,98,99\nAR 98,24\nMaav 84,168,76\nD1 1,16,46,1024\nv_mov_b32 v96, v86\nAR 99,25\nCV 110,98,99\nAR 98,26\nMaav 92,172,76\n"
"Ra 156,26,35840\nv_mov_b32 v97, v87\nAR 99,27\nCV 111,98,99\nAD 85,85,83\nCV 69,82,83\nv_permlane16_swap_b32 v86, v96\ns_add_u32 s5, s66, 0\nSv 108,1,16,5,2048\nRa 160,24,40960\n"
"v_permlane16_swap_b32 v87, v97\nAR 98,20\nAR 99,21\nCV 100,98,99\nMaav 100,176,76\ns_xor_b32 s89, s89, 0x10000\nEX 80,40\nAR 98,22\nAR 99,23\nMaav 108,180,76\nRa 164,24,41984\n"
"AD 86,86,96\nAD 87,87,97\nCV 101,98,99\nAR 98,28\nWL 6\nEX 81,41\nAR 99,29\nCV 102,98,99\nRa 168,24,43008\nAD 84,84,80\nAR 98,30\nAR 99,31\nCV 103,98,99\nMaav 116,184,76\n"
"D1 1,16,46,2048\nv_cmp_gt_u32 vcc, 0x100, v1\nv_mov_b32 v94, s15\nv_mov_b32 v95, s15\nv_cndmask_b32 v92, v89, v88, vcc\nv_cndmask_b32 v93, v87, v86, vcc\nMaav 124,188,76\n"
"Ra 172,24,44032\nv_mov_b32 v86, 0\ns_add_u32 s5, s66, 0\nSv 100,1,16,5,3072\nAR 98,32\nAD 84,84,81\nCV 66,80,81\nAR 99,33\nCV 104,98,99\nAR 98,34\nCvaa 48,128,192,4\n"
"Ra 176,26,40960\nv_mov_b32 v87, 0\nv_mov_b32 v88, 0\nAR 99,35\nCV 105,98,99\nMvaa 48,132,200\ns_cmp_eq_u32 s99, 0\nEX 82,42\nMvaa 48,136,208\nRa 180,26,41984\n"
"s_cselect_b32 s86, s94, s86\nAR 98,40\nAR 99,41\nCV 106,98,99\nMvaa 48,140,216\nWL 6\nEX 83,43\nAR 98,42\nAR 99,43\nCvaa 52,140,220,8\nRa 184,26,43008\nAD 84,84,82\n"
"v_mov_b32 v89, 0\nCV 107,98,99\nMvaa 52,136,212\nD1 1,16,46,3072\ns_add_u32 m0, s89, 0\ns_add_u32 s5, s66, 4096\nSv 104,1,16,5,0\nAR 98,36\nMvaa 52,132,204\nRa 188,26,44032\n"
"AR 99,37\nCV 108,98,99\nAR 98,38\nAR 99,39\nMvaa 52,128,196\nAD 84,84,83\nCV 67,82,83\nCV 109,98,99\nAR 98,44\nAR 99,45\nMvaa 52,144,228\nRa 128,2,49152\nCV 110,98,99\nAR 98,46\n"
"AR 99,47\nCV 111,98,99\nMvaa 52,148,236\ns_add_u32 s46, s86, s88\nEX 80,44\ns_add_u32 s5, s66, 4096\nSv 108,1,16,5,1024\nMvaa 52,152,244\nRa 132,2,50176\n"
"v_xor_b32 v24, 0x10000, v24\nAR 98,48\nAR 99,49\nCV 100,98,99\nMvaa 52,156,252\nWL 6\nEX 81,45\nAR 98,50\nAR 99,51\nMvaa 48,156,248\nRa 136,2,51200\nAD 85,85,80\nCV 101,98,99\n"
"AR 98,56\nAR 99,57\nMvaa 48,152,240\nD1 1,16,46,0\nCV 102,98,99\nAR 98,58\nAR 99,59\nCV 103,98,99\nMvaa 48,148,232\nRa 140,2,52224\ns_add_u32 s5, s66, 4096\nSv 100,1,16,5,2048\n"
"AR 98,52\nAR 99,53\nMvaa 48,144,224\nAD 85,85,81\nCV 70,80,81\nCV 104,98,99\nAR 98,54\nAR 99,55\nCvaa 56,160,192,12\nRa 144,2,53248\nCV 105,98,99\nAR 98,60\nAR 99,61\n"
"CV 106,98,99\nMvaa 56,164,200\nEX 82,46\nAR 98,62\nAR 99,63\nCV 107,98,99\nMvaa 56,168,208\nRa 148,2,54272\nv_xor_b32 v26, 0x10000, v26\ns_add_u32 s5, s66, 4096\n"
"Sv 104,1,16,5,3072\nAR 98,64\nMvaa 56,172,216\nWL 6\nEX 83,47\nAR 99,65\nCV 108,98,99\nCvaa 60,172,220,16\nRa 152,2,55296\nAD 85,85,82\nAR 98,66\nAR 99,67\nCV 109,98,99\n"
"Mvaa 60,168,212\nD1 1,16,46,1024\nAR 98,72\nAR 99,73\nCV 110,98,99\nAR 98,74\nMvaa 60,164,204\nRa 156,2,56320\nAR 99,75\nCV 111,98,99\ns_add_u32 s5, s66, 16384\nSv 108,1,16,5,0\n"
"Mvaa 60,160,196\nAD 85,85,83\nCV 71,82,83\nAR 98,68\nAR 99,69\nCV 100,98,99\nMvaa 60,176,228\nRa 160,2,57344\nv_max_f32 v90, v84, v85\nAR 98,70\nAR 99,71\nCV 101,98,99\n"
"Mvaa 60,180,236\ns_add_u32 s88, s88, 32768\nEX 80,48\nAR 98,76\nAR 99,77\nMvaa 60,184,244\nRa 164,2,58368\nv_cmp_lt_f32 vcc, s63, v90\nMvaa 60,188,252\nWL 8\nEX 81,49\n"
"Mvaa 56,188,248\nRa 168,2,59392\ns_cbranch_vccnz .T325\n.T326:\nv_mov_b32 v20, v84\nv_mov_b32 v21, v85\nv_mov_b32 v22, v20\nv_mov_b32 v23, v21\nMvaa 56,184,240\nD1 1,16,46,2048\n"
"v_permlane32_swap_b32 v20, v22\nv_permlane32_swap_b32 v21, v23\nAD 20,20,22\nAD 21,21,23\nv_mov_b32 v22, v20\nv_mov_b32 v23, v21\nMvaa 56,180,232\nRa 172,2,60416\n"
"v_permlane16_swap_b32 v20, v22\nv_permlane16_swap_b32 v21, v23\nAD 20,20,22\nAD 21,21,23\nv_min_f32 v90, v20, v21\nv_cmp_gt_f32 vcc, s64, v90\ns_nop 0\ns_cbranch_vccnz .T327\n"
".T328:\nAD 86,86,84\nMvaa 56,176,224\nAD 84,80,81\nCV 72,80,81\nCV 102,98,99\nAR 98,78\nAR 99,79\nZvav 128,128,64\nRa 176,2,61440\nCV 103,98,99\ns_add_u32 s5, s66, 16384\n"
"Sv 100,1,16,5,1024\nAR 98,80\nZvav 132,128,68\nEX 82,50\nAR 99,81\nCV 104,98,99\nAR 98,82\nZvav 140,132,68\nRa 180,2,62464\nAR 99,83\nCV 105,98,99\nAR 98,88\nAR 99,89\n"
"Zvav 136,132,64\ns_waitcnt vmcnt(21) lgkmcnt(8)\nEX 83,51\nCV 106,98,99\nAR 98,90\nZvav 144,136,64\nRa 184,2,63488\nAD 84,84,82\nAR 99,91\nCV 107,98,99\nZvav 148,136,68\n"
"D1 1,16,46,3072\ns_add_u32 m0, s89, 16384\ns_add_u32 s5, s66, 16384\nSv 104,1,16,5,2048\nAR 98,84\nZvav 156,140,68\nRa 188,2,64512\ns_barrier\nAR 99,85\nCV 108,98,99\nAR 98,86\n"
"Zvav 152,140,64\nAD 84,84,83\nCV 73,82,83\nAR 99,87\nCV 109,98,99\nAR 98,92\nZvav 160,144,64\nRa 128,24,0\nAR 99,93\nCV 110,98,99\nAR 98,94\nAR 99,95\nZvav 164,144,68\n"
"s_add_u32 s46, s87, s88\nEX 80,52\nCV 111,98,99\nZvav 172,148,68\nRa 132,24,1024\nv_xor_b32 v2, 0x10000, v2\ns_add_u32 s5, s66, 16384\nSv 108,1,16,5,3072\nAR 98,96\n"
"Zvav 168,148,64\nWL 8\nEX 81,53\nAR 99,97\nCV 100,98,99\nZvav 176,152,64\nRa 136,24,2048\nAD 87,87,85\nAR 98,98\nAR 99,99\nCV 101,98,99\nZvav 180,152,68\nD1 1,16,46,0\nAR 98,104\n"
"AR 99,105\nCV 102,98,99\nAR 98,106\nZvav 188,156,68\nRa 140,24,3072\nAR 99,107\nCV 103,98,99\ns_add_u32 s5, s66, 20480\nSv 100,1,16,5,0\nZvav 184,156,64\nAD 85,80,81\nCV 76,80,81\n"
"AR 98,100\nAR 99,101\nCV 104,98,99\nZvav 192,160,64\nRa 144,26,0\nAR 98,102\nAR 99,103\nCV 105,98,99\nAR 98,108\nZvav 196,160,68\ns_cmp_lt_u32 s99, s92\nEX 82,54\nZvav 204,164,68\n"
"Ra 148,26,1024\ns_cbranch_scc0 .E321\nv_cmpx_gt_i32 vcc, 19, v3\nv_mov_b32 v19, s97\nv_cmpx_gt_i32 vcc, 18, v3\nv_mov_b32 v18, s97\nv_cmpx_gt_i32 vcc, 17, v3\nv_mov_b32 v17, s97\n"
"v_cmpx_gt_i32 vcc, 16, v3\nv_mov_b32 v16, s97\nv_cmpx_gt_i32 vcc, 3, v3\nv_mov_b32 v11, s97\nv_cmpx_gt_i32 vcc, 2, v3\nv_mov_b32 v10, s97\nv_cmpx_gt_i32 vcc, 1, v3\n"
"v_mov_b32 v9, s97\nv_cmpx_gt_i32 vcc, 0, v3\nv_mov_b32 v8, s97\ns_mov_b64 exec, -1\ns_nop 2\n.E321:\ns_nop 0\nZvav 200,164,64\nWL 8\nEX 83,55\nAR 99,109\nCV 106,98,99\n"
"Zvav 208,168,64\nRa 152,26,2048\nAD 85,85,82\nAR 98,110\nAR 99,111\nCV 107,98,99\nZvav 212,168,68\nD1 1,16,46,1024\ns_add_u32 s5, s66, 20480\nSv 104,1,16,5,1024\nAR 98,112\n"
"AR 99,113\nZvav 220,172,68\nRa 156,26,3072\nCV 108,98,99\nAR 98,114\nAR 99,115\nCV 109,98,99\nZvav 216,172,64\nAD 85,85,83\nCV 77,82,83\nAR 98,120\nAR 99,121\nCV 110,98,99\n"
"Zvav 224,176,64\nRa 160,24,8192\nAR 98,122\nAR 99,123\nCV 111,98,99\nZvav 228,176,68\ns_sub_u32 s90, s90, 1\nEX 80,56\nZvav 236,180,68\nRa 164,24,9216\ns_cbranch_scc0 .E319\n"
"s_swappc_b64 s[80:81], s[8:9]\n.E319:\ns_add_u32 s5, s66, 20480\nSv 108,1,16,5,2048\nZvav 232,180,64\nWL 6\nEX 81,57\nAR 98,116\nAR 99,117\nZvav 240,184,64\nRa 168,24,10240\n"
"AD 84,84,80\nCV 100,98,99\nAR 98,118\nAR 99,119\nZvav 244,184,68\nD1 1,16,46,2048\nCV 101,98,99\nAR 98,124\nAR 99,125\nCV 102,98,99\nZvav 252,188,68\nRa 172,24,11264\nAR 98,126\n"
"AR 99,127\nCV 103,98,99\nZvav 248,188,64\nAD 84,84,81\nCV 74,80,81\ns_add_u32 s5, s66, 20480\nSv 100,1,16,5,3072\ns_sub_u32 s99, s99, 1\ns_cbranch_scc1 .LE271\n.p2align 6\n.A329:\n"
"Cvaa 32,128,192,4\nRa 176,26,8192\nMvaa 32,132,200\nEX 82,58\nMvaa 32,136,208\nRa 180,26,9216\nMvaa 32,140,216\nWL 6\nEX 83,59\nCvaa 36,140,220,8\nRa 184,26,10240\nAD 84,84,82\n"
"Mvaa 36,136,212\nD1 1,16,46,3072\ns_add_u32 m0, s89, 32768\nMvaa 36,132,204\nRa 188,26,11264\nMvaa 36,128,196\nAD 84,84,83\nCV 75,82,83\nMvaa 36,144,228\nRa 128,2,16384\n"
"Mvaa 36,148,236\ns_add_u32 s46, s86, s88\nEX 80,60\nMvaa 36,152,244\nRa 132,2,17408\nMvaa 36,156,252\nWL 6\nEX 81,61\nMvaa 32,156,248\nRa 136,2,18432\nAD 85,85,80\n"
"Mvaa 32,152,240\nD1 1,16,46,0\nMvaa 32,148,232\nRa 140,2,19456\nSv 112,1,16,49,0\nMvaa 32,144,224\nAD 85,85,81\nCV 78,80,81\nCvaa 40,160,192,12\nRa 144,2,20480\nMvaa 40,164,200\n"
"EX 82,62\nMvaa 40,168,208\nRa 148,2,21504\nMvaa 40,172,216\nWL 6\nEX 83,63\nCvaa 44,172,220,16\nRa 152,2,22528\nAD 85,85,82\nMvaa 44,168,212\nD1 1,16,46,1024\nMvaa 44,164,204\n"
"Ra 156,2,23552\nMvaa 44,160,196\nAD 85,85,83\nCV 79,82,83\nMvaa 44,176,228\nRa 160,2,24576\nv_max_f32 v90, v84, v85\nMvaa 44,180,236\ns_add_u32 s88, s88, 32768\nEX 80,32\n"
"Mvaa 44,184,244\nRa 164,2,25600\nv_cmp_lt_f32 vcc, s63, v90\nMvaa 44,188,252\nWL 8\nEX 81,33\nMvaa 40,188,248\nRa 168,2,26624\ns_cbranch_vccnz .T339\n.T340:\nMvaa 40,184,240\n"
"D1 1,16,46,2048\nMvaa 40,180,232\nRa 172,2,27648\nAD 86,86,84\nMvaa 40,176,224\nAD 84,80,81\nCV 64,80,81\nMvav 128,128,72\nRa 176,2,28672\nMvav 132,128,76\ns_cmp_eq_u32 s99, s98\n"
"EX 82,34\nMvav 140,132,76\nRa 180,2,29696\ns_cbranch_scc1 .E337\n.E338:\nMvav 136,132,72\ns_waitcnt vmcnt(13) lgkmcnt(8)\nEX 83,35\nMvav 144,136,72\nRa 184,2,30720\nAD 84,84,82\n"
"Mvav 148,136,76\nD1 1,16,46,3072\ns_add_u32 m0, s89, 49152\nMvav 156,140,76\nRa 188,2,31744\ns_barrier\nMvav 152,140,72\nAD 84,84,83\nCV 65,82,83\nMvav 160,144,72\n"
"Ra 128,24,32768\nMvav 164,144,76\ns_add_u32 s46, s87, s88\nEX 80,36\nMvav 172,148,76\nRa 132,24,33792\nMvav 168,148,72\nWL 8\nEX 81,37\nMvav 176,152,72\nRa 136,24,34816\n"
"AD 87,87,85\nMvav 180,152,76\nD1 1,16,46,0\nMvav 188,156,76\nRa 140,24,35840\nMvav 184,156,72\nAD 85,80,81\nCV 68,80,81\nMvav 192,160,72\nRa 144,26,32768\nMvav 196,160,76\n"
"s_cmp_eq_u32 s99, 0\nEX 82,38\nMvav 204,164,76\nRa 148,26,33792\ns_cbranch_scc1 .E331\n.E332:\nMvav 200,164,72\nWL 8\nEX 83,39\nMvav 208,168,72\nRa 152,26,34816\nAD 85,85,82\n"
"Mvav 212,168,76\nD1 1,16,46,1024\nMvav 220,172,76\nRa 156,26,35840\nMvav 216,172,72\nAD 85,85,83\nCV 69,82,83\nMvav 224,176,72\nRa 160,24,40960\nMvav 228,176,76\n"
"s_xor_b32 s89, s89, 0x10000\nEX 80,40\nMvav 236,180,76\nRa 164,24,41984\nMvav 232,180,72\nWL 6\nEX 81,41\nMvav 240,184,72\nRa 168,24,43008\nAD 84,84,80\nMvav 244,184,76\n"
"D1 1,16,46,2048\nMvav 252,188,76\nRa 172,24,44032\nMvav 248,188,72\nAD 84,84,81\nCV 66,80,81\nCvaa 48,128,192,4\nRa 176,26,40960\nMvaa 48,132,200\ns_cmp_eq_u32 s99, 0\nEX 82,42\n"
"Mvaa 48,136,208\nRa 180,26,41984\ns_cselect_b32 s86, s94, s86\nMvaa 48,140,216\nWL 6\nEX 83,43\nCvaa 52,140,220,8\nRa 184,26,43008\nAD 84,84,82\nMvaa 52,136,212\nD1 1,16,46,3072\n"
"s_add_u32 m0, s89, 0\nMvaa 52,132,204\nRa 188,26,44032\nMvaa 52,128,196\nAD 84,84,83\nCV 67,82,83\nMvaa 52,144,228\nRa 128,2,49152\nMvaa 52,148,236\ns_add_u32 s46, s86, s88\n"
"EX 80,44\nMvaa 52,152,244\nRa 132,2,50176\nv_xor_b32 v24, 0x10000, v24\nMvaa 52,156,252\nWL 6\nEX 81,45\nMvaa 48,156,248\nRa 136,2,51200\nAD 85,85,80\nMvaa 48,152,240\n"
"D1 1,16,46,0\nMvaa 48,148,232\nRa 140,2,52224\nSv 112,1,16,49,1024\nMvaa 48,144,224\nAD 85,85,81\nCV 70,80,81\nCvaa 56,160,192,12\nRa 144,2,53248\nMvaa 56,164,200\n"
"s_add_u32 s49, s49, 131072\nEX 82,46\nMvaa 56,168,208\nRa 148,2,54272\nv_xor_b32 v26, 0x10000, v26\nMvaa 56,172,216\nWL 6\nEX 83,47\nCvaa 60,172,220,16\nRa 152,2,55296\n"
"AD 85,85,82\nMvaa 60,168,212\nD1 1,16,46,1024\nMvaa 60,164,204\nRa 156,2,56320\nMvaa 60,160,196\nAD 85,85,83\nCV 71,82,83\nMvaa 60,176,228\nRa 160,2,57344\n"
"v_max_f32 v90, v84, v85\nMvaa 60,180,236\ns_add_u32 s88, s88, 32768\nEX 80,48\nMvaa 60,184,244\nRa 164,2,58368\nv_cmp_lt_f32 vcc, s63, v90\nMvaa 60,188,252\nWL 8\nEX 81,49\n"
"Mvaa 56,188,248\nRa 168,2,59392\ns_cbranch_vccnz .T341\n.T342:\nMvaa 56,184,240\nD1 1,16,46,2048\nMvaa 56,180,232\nRa 172,2,60416\nAD 86,86,84\nMvaa 56,176,224\nAD 84,80,81\n"
"CV 72,80,81\nMvav 128,128,64\nRa 176,2,61440\nMvav 132,128,68\nEX 82,50\nMvav 140,132,68\nRa 180,2,62464\nMvav 136,132,64\ns_waitcnt vmcnt(13) lgkmcnt(8)\nEX 83,51\n"
"Mvav 144,136,64\nRa 184,2,63488\nAD 84,84,82\nMvav 148,136,68\nD1 1,16,46,3072\ns_add_u32 m0, s89, 16384\nMvav 156,140,68\nRa 188,2,64512\ns_barrier\nMvav 152,140,64\nAD 84,84,83\n"
"CV 73,82,83\nMvav 160,144,64\nRa 128,24,0\nMvav 164,144,68\ns_add_u32 s46, s87, s88\nEX 80,52\nMvav 172,148,68\nRa 132,24,1024\nv_xor_b32 v2, 0x10000, v2\nMvav 168,148,64\nWL 8\n"
"EX 81,53\nMvav 176,152,64\nRa 136,24,2048\nAD 87,87,85\nMvav 180,152,68\nD1 1,16,46,0\nMvav 188,156,68\nRa 140,24,3072\nMvav 184,156,64\nAD 85,80,81\nCV 76,80,81\nMvav 192,160,64\n"
"Ra 144,26,0\nMvav 196,160,68\ns_cmp_lt_u32 s99, s92\nEX 82,54\nMvav 204,164,68\nRa 148,26,1024\ns_cbranch_scc1 .E333\n.E334:\nMvav 200,164,64\nWL 8\nEX 83,55\nMvav 208,168,64\n"
"Ra 152,26,2048\nAD 85,85,82\nMvav 212,168,68\nD1 1,16,46,1024\nMvav 220,172,68\nRa 156,26,3072\nMvav 216,172,64\nAD 85,85,83\nCV 77,82,83\nMvav 224,176,64\nRa 160,24,8192\n"
"Mvav 228,176,68\ns_sub_u32 s90, s90, 1\nEX 80,56\nMvav 236,180,68\nRa 164,24,9216\ns_cbranch_scc1 .E335\n.E336:\nMvav 232,180,64\nWL 6\nEX 81,57\nMvav 240,184,64\nRa 168,24,10240\n"
"AD 84,84,80\nMvav 244,184,68\nD1 1,16,46,2048\ns_sub_u32 s99, s99, 1\nMvav 252,188,68\nRa 172,24,11264\nMvav 248,188,64\nAD 84,84,81\nCV 74,80,81\ns_cbranch_scc0 .A329\n"
"s_branch .A343\n.E331:\nv_cmpx_gt_i32 vcc, 19, v0\nv_mov_b32 v15, s97\nv_cmpx_gt_i32 vcc, 18, v0\nv_mov_b32 v14, s97\nv_cmpx_gt_i32 vcc, 17, v0\nv_mov_b32 v13, s97\n"
"v_cmpx_gt_i32 vcc, 16, v0\nv_mov_b32 v12, s97\nv_cmpx_gt_i32 vcc, 3, v0\nv_mov_b32 v7, s97\nv_cmpx_gt_i32 vcc, 2, v0\nv_mov_b32 v6, s97\nv_cmpx_gt_i32 vcc, 1, v0\n"
"v_mov_b32 v5, s97\nv_cmpx_gt_i32 vcc, 0, v0\nv_mov_b32 v4, s97\ns_mov_b64 exec, -1\ns_nop 2\ns_branch .E332\n.E333:\ns_cmp_eq_u32 s99, 0\ns_cbranch_scc1 .K330\n"
"v_cmpx_gt_i32 vcc, 19, v3\nv_mov_b32 v15, s97\nv_cmpx_gt_i32 vcc, 18, v3\nv_mov_b32 v14, s97\nv_cmpx_gt_i32 vcc, 17, v3\nv_mov_b32 v13, s97\nv_cmpx_gt_i32 vcc, 16, v3\n"
"v_mov_b32 v12, s97\nv_cmpx_gt_i32 vcc, 3, v3\nv_mov_b32 v7, s97\nv_cmpx_gt_i32 vcc, 2, v3\nv_mov_b32 v6, s97\nv_cmpx_gt_i32 vcc, 1, v3\nv_mov_b32 v5, s97\n"
"v_cmpx_gt_i32 vcc, 0, v3\nv_mov_b32 v4, s97\ns_mov_b64 exec, -1\ns_nop 2\ns_branch .E334\n.K330:\nv_cmpx_gt_i32 vcc, 19, v3\nv_mov_b32 v19, s97\nv_cmpx_gt_i32 vcc, 18, v3\n"
"v_mov_b32 v18, s97\nv_cmpx_gt_i32 vcc, 17, v3\nv_mov_b32 v17, s97\nv_cmpx_gt_i32 vcc, 16, v3\nv_mov_b32 v16, s97\nv_cmpx_gt_i32 vcc, 3, v3\nv_mov_b32 v11, s97\n"
"v_cmpx_gt_i32 vcc, 2, v3\nv_mov_b32 v10, s97\nv_cmpx_gt_i32 vcc, 1, v3\nv_mov_b32 v9, s97\nv_cmpx_gt_i32 vcc, 0, v3\nv_mov_b32 v8, s97\ns_mov_b64 exec, -1\ns_nop 2\n"
"s_branch .E334\n.E335:\ns_swappc_b64 s[80:81], s[8:9]\ns_branch .E336\n.E337:\nv_mov_b32 v91, 0x40000000\nv_cmp_gt_u32 vcc, 0x200, v1\nWV 5\ns_nop 0\n"
"v_cndmask_b32 v91, v91, v1, vcc\nSv 92,91,16,65,0\ns_branch .E338\n.A343:\n.LE271:\nRa 176,26,8192\nEX 82,58\nRa 180,26,9216\nWL 6\nEX 83,59\nCvaa 36,140,220,8\nRa 184,26,10240\n"
"AD 84,84,82\nMvaa 36,136,212\nD1 1,16,46,3072\ns_add_u32 m0, s89, 32768\nMvaa 36,132,204\nRa 188,26,11264\nMvaa 36,128,196\nAD 84,84,83\nCV 75,82,83\nMvaa 36,144,228\n"
"Ra 128,2,16384\nMvaa 36,148,236\ns_add_u32 s46, s86, s88\nEX 80,60\nMvaa 36,152,244\nRa 132,2,17408\nMvaa 36,156,252\nEX 81,61\nRa 136,2,18432\nAD 85,85,80\nD1 1,16,46,0\n"
"Ra 140,2,19456\nAD 85,85,81\nCV 78,80,81\nRa 144,2,20480\ns_mov_b32 s87, s95\nEX 82,62\nRa 148,2,21504\nWL 6\nEX 83,63\nCvaa 44,172,220,16\nRa 152,2,22528\nAD 85,85,82\n"
"Mvaa 44,168,212\nD1 1,16,46,1024\nMvaa 44,164,204\nRa 156,2,23552\nMvaa 44,160,196\nAD 85,85,83\nCV 79,82,83\nMvaa 44,176,228\nRa 160,2,24576\nv_max_f32 v90, v84, v85\n"
"Mvaa 44,180,236\ns_add_u32 s88, s88, 32768\nMvaa 44,184,244\nRa 164,2,25600\nv_cmp_lt_f32 vcc, s63, v90\nMvaa 44,188,252\nWL 8\nRa 168,2,26624\ns_cbranch_vccnz .T346\n.T347:\n"
"D1 1,16,46,2048\nRa 172,2,27648\nAD 86,86,84\nMvav 128,128,72\nRa 176,2,28672\nMvav 132,128,76\ns_cmp_eq_u32 s13, 0\nMvav 140,132,76\nRa 180,2,29696\ns_cbranch_scc0 .E344\n"
"v_mov_b32 v91, 0x40000000\nv_cmp_gt_u32 vcc, 0x200, v1\nWV 4\ns_nop 0\nv_cndmask_b32 v91, v91, v1, vcc\nSv 92,91,16,65,0\n.E344:\nMvav 136,132,72\ns_waitcnt vmcnt(12) lgkmcnt(8)\n"
"Mvav 144,136,72\nRa 184,2,30720\nMvav 148,136,76\nD1 1,16,46,3072\ns_add_u32 m0, s89, 49152\nMvav 156,140,76\nRa 188,2,31744\ns_barrier\nMvav 152,140,72\nMvav 160,144,72\n"
"Ra 128,24,32768\nMvav 164,144,76\ns_add_u32 s46, s87, s88\nEX 80,36\nMvav 172,148,76\nRa 132,24,33792\nMvav 168,148,72\nWL 8\nEX 81,37\nMvav 176,152,72\nRa 136,24,34816\n"
"AD 87,87,85\nMvav 180,152,76\nD1 1,16,46,0\nMvav 188,156,76\nRa 140,24,35840\nMvav 184,156,72\nAD 85,80,81\nCV 68,80,81\nMvav 192,160,72\nRa 144,26,32768\nMvav 196,160,76\n"
"s_nop 0\nEX 82,38\nMvav 204,164,76\nRa 148,26,33792\nv_cmpx_gt_i32 vcc, 19, v0\nv_mov_b32 v19, s97\nv_cmpx_gt_i32 vcc, 18, v0\nv_mov_b32 v18, s97\nv_cmpx_gt_i32 vcc, 17, v0\n"
"v_mov_b32 v17, s97\nv_cmpx_gt_i32 vcc, 16, v0\nv_mov_b32 v16, s97\nv_cmpx_gt_i32 vcc, 3, v0\nv_mov_b32 v11, s97\nv_cmpx_gt_i32 vcc, 2, v0\nv_mov_b32 v10, s97\n"
"v_cmpx_gt_i32 vcc, 1, v0\nv_mov_b32 v9, s97\nv_cmpx_gt_i32 vcc, 0, v0\nv_mov_b32 v8, s97\ns_mov_b64 exec, -1\ns_nop 2\ns_nop 0\nMvav 200,164,72\nWL 8\nEX 83,39\nMvav 208,168,72\n"
"Ra 152,26,34816\nAD 85,85,82\nMvav 212,168,76\nD1 1,16,46,1024\nMvav 220,172,76\nRa 156,26,35840\nMvav 216,172,72\nAD 85,85,83\nCV 69,82,83\nMvav 224,176,72\nRa 160,24,40960\n"
"Mvav 228,176,76\ns_xor_b32 s89, s89, 0x10000\nMvav 236,180,76\nRa 164,24,41984\nMvav 232,180,72\nWL 10\nMvav 240,184,72\nRa 168,24,43008\nMvav 244,184,76\nD1 1,16,46,2048\n"
"Mvav 252,188,76\nRa 172,24,44032\nMvav 248,188,72\nRa 176,26,40960\nRa 180,26,41984\nWL 6\nCvaa 52,140,220,8\nRa 184,26,43008\nMvaa 52,136,212\nD1 1,16,46,3072\n"
"s_add_u32 m0, s89, 0\nMvaa 52,132,204\nRa 188,26,44032\nMvaa 52,128,196\nMvaa 52,144,228\nRa 128,2,49152\nMvaa 52,148,236\ns_add_u32 s46, s86, s88\nEX 80,44\nMvaa 52,152,244\n"
"Ra 132,2,50176\nv_xor_b32 v24, 0x10000, v24\nMvaa 52,156,252\nEX 81,45\nRa 136,2,51200\nAD 85,85,80\nD1 1,16,46,0\nRa 140,2,52224\nAD 85,85,81\nCV 70,80,81\nRa 144,2,53248\n"
"EX 82,46\nRa 148,2,54272\nv_xor_b32 v26, 0x10000, v26\nWL 6\nEX 83,47\nCvaa 60,172,220,16\nRa 152,2,55296\nAD 85,85,82\nMvaa 60,168,212\nD1 1,16,46,1024\nMvaa 60,164,204\n"
"Ra 156,2,56320\nMvaa 60,160,196\nAD 85,85,83\nCV 71,82,83\nMvaa 60,176,228\nRa 160,2,57344\nMvaa 60,180,236\ns_add_u32 s88, s88, 32768\nMvaa 60,184,244\nRa 164,2,58368\n"
"v_cmp_lt_f32 vcc, s63, v85\nMvaa 60,188,252\nWL 8\nRa 168,2,59392\ns_cbranch_vccnz .T348\n.T349:\nD1 1,16,46,2048\nRa 172,2,60416\nRa 176,2,61440\nMvav 132,128,68\n"
"Mvav 140,132,68\nRa 180,2,62464\ns_waitcnt vmcnt(11) lgkmcnt(8)\nRa 184,2,63488\nMvav 148,136,68\nD1 1,16,46,3072\ns_add_u32 m0, s89, 16384\nMvav 156,140,68\nRa 188,2,64512\n"
"s_barrier\nRa 128,24,0\nMvav 164,144,68\ns_add_u32 s46, s87, s88\nEX 80,52\nMvav 172,148,68\nRa 132,24,1024\nv_xor_b32 v2, 0x10000, v2\nWL 8\nEX 81,53\nRa 136,24,2048\n"
"AD 87,87,85\nMvav 180,152,68\nD1 1,16,46,0\nMvav 188,156,68\nRa 140,24,3072\nAD 85,80,81\nCV 76,80,81\nRa 144,26,0\nMvav 196,160,68\nEX 82,54\nMvav 204,164,68\nRa 148,26,1024\n"
"WL 8\nEX 83,55\nRa 152,26,2048\nAD 85,85,82\nMvav 212,168,68\nD1 1,16,46,1024\nMvav 220,172,68\nRa 156,26,3072\nAD 85,85,83\nCV 77,82,83\nRa 160,24,8192\nMvav 228,176,68\n"
"Mvav 236,180,68\nRa 164,24,9216\nWL 10\nRa 168,24,10240\nMvav 244,184,68\nD1 1,16,46,2048\nMvav 252,188,68\nRa 172,24,11264\nD1 1,16,46,3072\ns_add_u32 m0, s89, 32768\n"
"s_add_u32 s46, s86, s88\nRa 128,2,16384\nv_xor_b32 v3, 0x10000, v2\nv_add_u32 v0, 0x20000, v1\nEX 80,60\nRa 132,2,17408\ns_sub_u32 s94, s49, 1835008\nEX 81,61\nRa 136,2,18432\n"
"AD 85,85,80\ns_xor_b32 s92, s94, 0x202000\nD1 1,16,46,0\ns_lshl_b32 s89, s11, 12\nRa 140,2,19456\ns_lshl1_add_u32 s2, s12, 1\nAD 85,85,81\nCV 78,80,81\nRa 144,2,20480\n"
"s_lshl3_add_u32 s2, s2, s13\nEX 82,62\nRa 148,2,21504\ns_lshl4_add_u32 s2, s2, 0xc048200\nEX 83,63\nRa 152,2,22528\nAD 85,85,82\nD1 1,16,46,1024\nRa 156,2,23552\n"
"s_and_b32 s3, s72, 0x2000\nAD 85,85,83\nCV 79,82,83\nRa 160,2,24576\ns_cmp_eq_u32 s11, 1\ns_cselect_b32 s54, s2, s54\ns_cselect_b32 s3, s3, 0\nv_cmp_lt_f32 vcc, s63, v85\n"
"Ra 164,2,25600\ns_cbranch_vccnz .T350\n.T351:\nAD 87,87,85\nWL 8\nv_mov_b32 v84, v86\nv_mov_b32 v85, v87\nRa 168,2,26624\nv_permlane32_swap_b32 v86, v84\n"
"v_permlane32_swap_b32 v87, v85\nD1 1,16,46,2048\nAD 86,86,84\nRa 172,2,27648\nAD 87,87,85\nv_mov_b32 v84, v86\nv_mov_b32 v85, v87\nRa 176,2,28672\nv_permlane16_swap_b32 v86, v84\n"
"v_permlane16_swap_b32 v87, v85\nMvav 132,128,76\nAD 86,86,84\nAD 87,87,85\nMvav 140,132,76\nRa 180,2,29696\nv_rcp_f32 v86, v86\nWL 8\nv_rcp_f32 v87, v87\nRa 184,2,30720\n"
"v_xor_b32 v25, s3, v25\nMvav 148,136,76\nD1 1,16,46,3072\nMvav 156,140,76\nRa 188,2,31744\nMU 128,128,86\nMU 129,129,86\nCV 96,128,129\nMU 130,130,86\nMU 131,131,86\nRa 128,2,0\n"
"CV 97,130,131\nMU 132,132,87\nMvav 164,144,76\nMU 133,133,87\nCV 100,132,133\nMvav 172,148,76\nRa 132,2,1024\nMU 134,134,87\nMU 135,135,87\nWL 8\ns_add_u32 m0, s89, 131072\n"
"s_add_u32 s46, s96, 1212416\nRa 136,2,2048\nCV 101,134,135\nMU 136,136,86\nMvav 180,152,76\nD1 1,16,46,0\nMU 137,137,86\nMvav 188,156,76\nRa 140,2,3072\nCV 98,136,137\n"
"MU 138,138,86\nMU 139,139,86\nCV 99,138,139\nMU 140,140,87\nRa 144,2,4096\nMU 141,141,87\nCV 102,140,141\nMvav 196,160,76\nMU 142,142,87\nMU 143,143,87\nMvav 204,164,76\n"
"Ra 148,2,5120\nCV 103,142,143\nMU 144,144,86\nWL 8\nMU 145,145,86\nCV 104,144,145\nRa 152,2,6144\nMU 146,146,86\nMU 147,147,86\nMvav 212,168,76\nD1 1,16,46,1024\nCV 105,146,147\n"
"Mvav 220,172,76\nRa 156,2,7168\nMU 148,148,87\nMU 149,149,87\nCV 108,148,149\nMU 150,150,87\nMU 151,151,87\nRa 160,2,8192\nCV 109,150,151\nMU 152,152,86\nMvav 228,176,76\n"
"MU 153,153,86\nCV 106,152,153\nMvav 236,180,76\nRa 164,2,9216\nMU 154,154,86\nMU 155,155,86\nWL 8\nCV 107,154,155\nMU 156,156,87\nRa 168,2,10240\nMU 157,157,87\nCV 110,156,157\n"
"Mvav 244,184,76\nD1 1,16,46,2048\nMU 158,158,87\nMvav 252,188,76\nRa 172,2,11264\nMU 159,159,87\nCV 111,158,159\nMU 160,160,86\nMU 161,161,86\nCV 112,160,161\nZvav 128,128,96\n"
"Ra 176,2,12288\nMU 162,162,86\nMU 163,163,86\nMvav 128,132,104\nCV 113,162,163\nMU 164,164,87\nZvav 132,132,108\nRa 180,2,13312\nMU 165,165,87\nCV 116,164,165\nMvav 132,128,100\n"
"s_waitcnt vmcnt(15) lgkmcnt(8)\nMU 166,166,87\nMU 167,167,87\nZvav 140,136,100\nRa 184,2,14336\nCV 117,166,167\nMU 168,168,86\nMvav 140,140,108\nD1 1,16,46,3072\nMU 169,169,86\n"
"CV 114,168,169\nZvav 136,140,104\nRa 188,2,15360\ns_barrier\nMU 170,170,86\nMvav 136,136,96\nMU 171,171,86\nCV 115,170,171\nMU 172,172,87\nZvav 144,144,96\nRa 128,2,32768\n"
"MU 173,173,87\nCV 118,172,173\nMvav 144,148,104\nMU 174,174,87\nMU 175,175,87\nbuffer_store_dword v25, off, s[16:19], s54 offset:512 sc0 sc1\nZvav 148,148,108\nRa 132,2,33792\n"
"CV 119,174,175\nMU 176,176,86\nMvav 148,144,100\nWL 8\ns_add_u32 m0, s89, 49152\ns_add_u32 s46, s96, 1179648\nZvav 156,152,100\nRa 136,2,34816\nMU 177,177,86\nCV 120,176,177\n"
"Mvav 156,156,108\nD1 1,16,46,0\nMU 178,178,86\nMU 179,179,86\nZvav 152,156,104\nRa 140,2,35840\nCV 121,178,179\nMU 180,180,87\nMvav 152,152,96\nMU 181,181,87\nCV 124,180,181\n"
"MU 182,182,87\nZvav 64,160,96\nRa 144,2,36864\nMU 183,183,87\nCV 125,182,183\nMvav 64,164,104\nMU 184,184,86\nMU 185,185,86\nZvav 68,164,108\nRa 148,2,37888\nCV 122,184,185\n"
"MU 186,186,86\nMvav 68,160,100\nWL 8\nMU 187,187,86\nCV 123,186,187\nZvav 76,168,100\nRa 152,2,38912\nMU 188,188,87\nMU 189,189,87\nMvav 76,172,108\nD1 1,16,46,1024\n"
"CV 126,188,189\nMU 190,190,87\nZvav 72,172,104\nRa 156,2,39936\nMU 191,191,87\nCV 127,190,191\nMvav 72,168,96\nMU 192,192,86\nMU 193,193,86\nCV 32,192,193\nMvav 128,176,112\n"
"Ra 160,2,40960\nMU 194,194,86\nMU 195,195,86\nMvav 128,180,120\nCV 33,194,195\nMU 196,196,87\nMU 197,197,87\nMvav 132,180,124\nRa 164,2,41984\nCV 36,196,197\nMU 198,198,87\n"
"Mvav 132,176,116\nWL 8\nMU 199,199,87\nMvav 140,184,116\nRa 168,2,43008\nCV 37,198,199\nMU 200,200,86\nMvav 140,188,124\nD1 1,16,46,2048\nMU 201,201,86\nCV 34,200,201\n"
"Mvav 136,188,120\nRa 172,2,44032\nMU 202,202,86\nMU 203,203,86\nMvav 136,184,112\nCV 35,202,203\nMU 204,204,87\nMU 205,205,87\nMvav 144,128,112\nRa 176,2,45056\nCV 38,204,205\n"
"MU 206,206,87\nMvav 144,132,120\nMU 207,207,87\nCV 39,206,207\nMvav 148,132,124\nRa 180,2,46080\nMU 208,208,86\nMU 209,209,86\nMvav 148,128,116\nWL 8\nCV 40,208,209\n"
"MU 210,210,86\nMvav 156,136,116\nRa 184,2,47104\nMU 211,211,86\nCV 41,210,211\nMvav 156,140,124\nD1 1,16,46,3072\nMU 212,212,87\nMU 213,213,87\nMvav 152,140,120\nRa 188,2,48128\n"
"CV 44,212,213\nMvav 152,136,112\nMU 214,214,87\nMU 215,215,87\nCV 45,214,215\nMvav 64,144,112\nRa 128,2,49152\nMU 216,216,86\nMU 217,217,86\nMvav 64,148,120\nCV 42,216,217\n"
"MU 218,218,86\nMU 219,219,86\nMvav 68,148,124\nRa 132,2,50176\nCV 43,218,219\nMU 220,220,87\nMvav 68,144,116\nWL 8\ns_add_u32 m0, s89, 147456\ns_add_u32 s46, s96, 1245184\n"
"Mvav 76,152,116\nRa 136,2,51200\nMU 221,221,87\nCV 46,220,221\nMvav 76,156,124\nD1 1,16,46,0\nMU 222,222,87\nMU 223,223,87\nMvav 72,156,120\nRa 140,2,52224\nCV 47,222,223\n"
"Mvav 72,152,112\nMU 224,224,86\nMU 225,225,86\nCV 48,224,225\nMvav 128,160,32\nRa 144,2,53248\nMU 226,226,86\nMU 227,227,86\nMvav 128,164,40\nCV 49,226,227\nMU 228,228,87\n"
"Mvav 132,164,44\nRa 148,2,54272\nMU 229,229,87\nCV 52,228,229\nMvav 132,160,36\nWL 8\nMU 230,230,87\nMU 231,231,87\nMvav 140,168,36\nRa 152,2,55296\nCV 53,230,231\nMU 232,232,86\n"
"Mvav 140,172,44\nD1 1,16,46,1024\nMU 233,233,86\nCV 50,232,233\nMvav 136,172,40\nRa 156,2,56320\nMU 234,234,86\nMvav 136,168,32\nMU 235,235,86\nCV 51,234,235\nMU 236,236,87\n"
"Mvav 144,176,32\nRa 160,2,57344\nMU 237,237,87\nCV 54,236,237\nMvav 144,180,40\nMU 238,238,87\nMU 239,239,87\nCV 55,238,239\nMvav 148,180,44\nRa 164,2,58368\nMU 240,240,86\n"
"MU 241,241,86\nMvav 148,176,36\nWL 8\nCV 56,240,241\nMvav 156,184,36\nRa 168,2,59392\nMU 242,242,86\nMU 243,243,86\nMvav 156,188,44\nD1 1,16,46,2048\nCV 57,242,243\nMU 244,244,87\n"
"Mvav 152,188,40\nRa 172,2,60416\nMU 245,245,87\nCV 60,244,245\nMvav 152,184,32\nMU 246,246,87\nMU 247,247,87\nCV 61,246,247\nMvav 64,128,32\nRa 176,2,61440\nMU 248,248,86\n"
"MU 249,249,86\nMvav 64,132,40\nCV 58,248,249\nMU 250,250,86\nMvav 68,132,44\nRa 180,2,62464\nMU 251,251,86\nCV 59,250,251\nMvav 68,128,36\ns_waitcnt vmcnt(16) lgkmcnt(8)\n"
"MU 252,252,87\nMU 253,253,87\nMvav 76,136,36\nRa 184,2,63488\nCV 62,252,253\nMU 254,254,87\nMvav 76,140,44\nD1 1,16,46,3072\nMU 255,255,87\nCV 63,254,255\nMvav 72,140,40\n"
"Ra 188,2,64512\ns_barrier\nMvav 72,136,32\nMvav 128,144,48\nRa 128,3,0\nMvav 128,148,56\nMvav 132,148,60\nRa 132,3,1024\nMvav 132,144,52\nWL 8\nMvav 140,152,52\nRa 136,3,2048\n"
"Mvav 140,156,60\nMvav 136,156,56\nRa 140,3,3072\nMvav 136,152,48\nMvav 144,160,48\nRa 144,3,4096\nMvav 144,164,56\nMvav 148,164,60\nRa 148,3,5120\nCV 132,132,133\nMvav 148,160,52\n"
"WL 8\nCV 133,134,135\nMvav 156,168,52\nRa 152,3,6144\nCV 134,140,141\nMvav 156,172,60\nCV 135,142,143\ns_add_u32 s5, s94, 0\nMvav 152,172,56\nRa 156,3,7168\nSv 132,1,16,5,1024\n"
"Mvav 152,168,48\nCV 128,128,129\nCV 129,130,131\nMvav 64,176,48\nRa 160,3,8192\nCV 130,136,137\nMvav 64,180,56\nbuffer_load_dword v27, off, s[16:19], s60 offset:512 sc0 sc1\n"
"CV 131,138,139\nMvav 68,180,60\nRa 164,3,9216\nSv 128,1,16,5,0\nMvav 68,176,52\nWL 6\nCV 148,148,149\nMvav 76,184,52\nRa 168,3,10240\nCV 149,150,151\nMvav 76,188,60\n"
"CV 150,156,157\nCV 151,158,159\nMvav 72,188,56\nRa 172,3,11264\ns_add_u32 s5, s94, 131072\nMvav 72,184,48\nSv 148,1,16,5,1024\nCV 144,144,145\nZvav 160,128,96\nRa 176,3,12288\n"
"CV 145,146,147\nMvav 160,132,104\nCV 146,152,153\nCV 147,154,155\nMvav 160,136,112\nRa 180,3,13312\nSv 144,1,16,5,0\nMvav 160,140,120\nWL 6\nCV 68,68,69\nZvav 164,140,124\n"
"Ra 184,3,14336\nCV 69,70,71\nMvav 164,136,116\nCV 70,76,77\nCV 71,78,79\nMvav 164,132,108\nRa 188,3,15360\ns_add_u32 s5, s94, 262144\nMvav 164,128,100\nSv 68,1,16,5,1024\n"
"CV 64,64,65\nMvav 164,144,36\nRa 128,3,16384\nCV 65,66,67\nMvav 164,148,44\nCV 66,72,73\nCV 67,74,75\nMvav 164,152,52\nRa 132,3,17408\nSv 64,1,16,5,0\nMvav 164,156,60\nWL 6\n"
"Mvav 160,156,56\nRa 136,3,18432\nMvav 160,152,48\nMvav 160,148,40\nRa 140,3,19456\nMvav 160,144,32\nZvav 168,160,96\nRa 144,3,20480\nMvav 168,164,104\nMvav 168,168,112\n"
"Ra 148,3,21504\nMvav 168,172,120\nWL 6\nZvav 172,172,124\nRa 152,3,22528\nMvav 172,168,116\nMvav 172,164,108\nRa 156,3,23552\nMvav 172,160,100\nMvav 172,176,36\nRa 160,3,24576\n"
"Mvav 172,180,44\nMvav 172,184,52\nRa 164,3,25600\nMvav 172,188,60\nWL 6\nMvav 168,188,56\nRa 168,3,26624\nMvav 168,184,48\nMvav 168,180,40\nRa 172,3,27648\nMvav 168,176,32\n"
"Zvav 176,128,96\nRa 176,3,28672\nCV 164,164,165\nMvav 176,132,104\nCV 165,166,167\nCV 166,172,173\nMvav 176,136,112\nRa 180,3,29696\nCV 167,174,175\nMvav 176,140,120\n"
"s_waitcnt vmcnt(11) lgkmcnt(6)\ns_add_u32 s5, s94, 393216\nZvav 180,140,124\nRa 184,3,30720\nSv 164,1,16,5,1024\nMvav 180,136,116\nCV 160,160,161\nCV 161,162,163\n"
"Mvav 180,132,108\nRa 188,3,31744\ns_barrier\nMvav 180,128,100\nCV 162,168,169\nCV 163,170,171\nMvav 180,144,36\nRa 128,3,32768\nSv 160,1,16,5,0\nMvav 180,148,44\nMvav 180,152,52\n"
"Ra 132,3,33792\nMvav 180,156,60\nWL 6\ns_add_u32 m0, s89, 81920\ns_add_u32 s46, s96, 1277952\nMvav 176,156,56\nRa 136,3,34816\nMvav 176,152,48\nD1 1,16,46,0\nMvav 176,148,40\n"
"Ra 140,3,35840\nMvav 176,144,32\nZvav 184,160,96\nRa 144,3,36864\nMvav 184,164,104\nMvav 184,168,112\nRa 148,3,37888\nMvav 184,172,120\nWL 6\nZvav 188,172,124\nRa 152,3,38912\n"
"Mvav 188,168,116\nD1 1,16,46,1024\nMvav 188,164,108\nRa 156,3,39936\nMvav 188,160,100\nMvav 188,176,36\nRa 160,3,40960\nWV 9\nv_cmp_ne_u32 vcc, s15, v27\ns_nop 0\n"
"s_cbranch_vccnz .S376\n.S377:\nMvav 188,180,44\ns_add_u32 s4, s92, 0\nMvav 188,184,52\nRa 164,3,41984\nMvav 188,188,60\nWL 6\n"
"buffer_load_dwordx4 v[208:211], v1, s[16:19], s4 offen offset:0 sc0 sc1\nMvav 184,188,56\nRa 168,3,43008\nMvav 184,184,48\nD1 1,16,46,2048\nMvav 184,180,40\nRa 172,3,44032\n"
"Mvav 184,176,32\nbuffer_load_dwordx4 v[212:215], v1, s[16:19], s4 offen offset:1024 sc0 sc1\nZvav 192,128,96\nRa 176,3,45056\nv_lshrrev_b32 v88, 8, v1\nMvav 192,132,104\n"
"v_lshlrev_b32 v89, 5, v88\nCV 180,180,181\nMvav 192,136,112\nRa 180,3,46080\nCV 181,182,183\nMvav 192,140,120\nWL 6\nCV 182,188,189\nZvav 196,140,124\nRa 184,3,47104\n"
"v_bfe_u32 v90, v1, 4, 4\nMvav 196,136,116\nD1 1,16,46,3072\nCV 183,190,191\nMvav 196,132,108\nRa 188,3,48128\ns_add_u32 s5, s94, 524288\nMvav 196,128,100\n"
"v_mul_u32_u24 v91, 896, v90\nSv 180,1,16,5,1024\nMvav 196,144,36\nRa 128,3,49152\nv_lshl_add_u32 v91, v88, 4, v91\nMvav 196,148,44\nv_add_u32 v90, s21, v90\nCV 176,176,177\n"
"Mvav 196,152,52\nRa 132,3,50176\nCV 177,178,179\nMvav 196,156,60\nWL 6\ns_add_u32 m0, s89, 65536\ns_add_u32 s46, s96, 1310720\nMvav 192,156,56\nRa 136,3,51200\n"
"v_lshlrev_b32 v88, 3, v90\nMvav 192,152,48\nD1 1,16,46,0\nCV 178,184,185\nMvav 192,148,40\nRa 140,3,52224\nCV 179,186,187\nMvav 192,144,32\nv_add_u32 v94, 64, v90\n"
"Sv 176,1,16,5,0\nZvav 200,160,96\nRa 144,3,53248\ns_mov_b32 s63, 0xc040000\nMvav 200,164,104\nv_lshrrev_b32 v92, 8, v1\nMvav 200,168,112\nRa 148,3,54272\nMvav 200,172,120\nWL 6\n"
"Zvav 204,172,124\nRa 152,3,55296\nv_bfe_u32 v93, v1, 6, 2\nMvav 204,168,116\nD1 1,16,46,1024\nv_cmp_eq_u32 vcc, v92, v93\nv_and_b32 v92, 16, v1\nv_mov_b32 v93, 0x3f80\n"
"v_cndmask_b32 v93, 0, v93, vcc\nMvav 204,164,108\nRa 156,3,56320\nMvav 204,160,100\nv_lshlrev_b32 v93, v92, v93\nMvav 204,176,36\nRa 160,3,57344\nv_and_b32 v92, 32, v1\n"
"Mvav 204,180,44\ns_add_u32 s4, s92, 131072\nv_cmp_eq_u32 vcc, 0, v92\nv_mov_b32 v4, 0\nv_mov_b32 v5, 0\nv_cndmask_b32 v6, 0, v93, vcc\nMvav 204,184,52\nRa 164,3,58368\n"
"Mvav 204,188,60\nWL 6\nbuffer_load_dwordx4 v[216:219], v1, s[16:19], s4 offen offset:0 sc0 sc1\nMvav 200,188,56\nRa 168,3,59392\nv_sub_u32 v7, v93, v6\nMvav 200,184,48\n"
"D1 1,16,46,2048\nMvav 200,180,40\nRa 172,3,60416\nMvav 200,176,32\nbuffer_load_dwordx4 v[220:223], v1, s[16:19], s4 offen offset:1024 sc0 sc1\nZvav 160,128,96\nRa 176,3,61440\n"
"v_mov_b32 v8, 0\nMvav 160,132,104\nv_mov_b32 v9, 0\nCV 196,196,197\nMvav 160,136,112\nRa 180,3,62464\nCV 197,198,199\nMvav 160,140,120\nWL 6\nCV 198,204,205\nZvav 164,140,124\n"
"Ra 184,3,63488\nCV 199,206,207\nMvav 164,136,116\nD1 1,16,46,3072\ns_add_u32 s5, s94, 655360\nMvav 164,132,108\nRa 188,3,64512\ns_barrier\nMvav 164,128,100\nSv 196,1,16,5,1024\n"
"CV 192,192,193\nMvav 164,144,36\nRa 128,0,0\nCV 193,194,195\nMvav 164,148,44\nCV 194,200,201\nCV 195,202,203\nMvav 164,152,52\nRa 132,0,1024\nSv 192,1,16,5,0\nMvav 164,156,60\n"
"WL 6\ns_add_u32 m0, s89, 98304\ns_add_u32 s46, s96, 1343488\nMvav 160,156,56\nRa 136,0,2048\nMvav 160,152,48\nD1 1,16,46,0\nMvav 160,148,40\nRa 140,0,3072\nMvav 160,144,32\n"
"Zvav 168,160,96\nRa 144,0,4096\nWV 12\nv_max_u32 v21, v211, v215\nv_cmp_eq_u32 vcc, -1, v21\ns_nop 0\ns_cbranch_vccnz .S352\n.S353:\nMvav 168,164,104\n"
"v_lshlrev_b32 v176, 16, v208\nMvav 168,168,112\nRa 148,0,5120\nMvav 168,172,120\nWL 6\nv_and_b32 v177, 0xffff0000, v208\nZvav 172,172,124\nRa 152,0,6144\nMvav 172,168,116\n"
"D1 1,16,46,1024\nMvav 172,164,108\nRa 156,0,7168\nv_lshlrev_b32 v178, 16, v209\nMvav 172,160,100\nv_and_b32 v179, 0xffff0000, v209\nMvav 172,176,36\nRa 160,0,8192\n"
"v_lshlrev_b32 v180, 16, v212\nMvav 172,180,44\nv_and_b32 v181, 0xffff0000, v212\ns_add_u32 s4, s92, 262144\nMvav 172,184,52\nRa 164,0,9216\nMvav 172,188,60\nWL 6\n"
"v_lshlrev_b32 v182, 16, v213\nbuffer_load_dwordx4 v[224:227], v1, s[16:19], s4 offen offset:0 sc0 sc1\nMvav 168,188,56\nRa 168,0,10240\nMvav 168,184,48\nD1 1,16,46,2048\n"
"Mvav 168,180,40\nRa 172,0,11264\nv_and_b32 v183, 0xffff0000, v213\nMvav 168,176,32\nv_lshlrev_b32 v184, 16, v210\n"
"buffer_load_dwordx4 v[228:231], v1, s[16:19], s4 offen offset:1024 sc0 sc1\nMvav 176,128,96\nRa 176,0,12288\nv_and_b32 v185, 0xffff0000, v210\nMvav 176,132,104\n"
"v_lshlrev_b32 v186, 16, v211\nCV 164,164,165\nMvav 176,136,112\nRa 180,0,13312\nCV 165,166,167\nMvav 176,140,120\nWL 6\nCV 166,172,173\nMvav 180,140,124\nRa 184,0,14336\n"
"v_and_b32 v187, 0xffff0000, v211\nMvav 180,136,116\nD1 1,16,46,3072\nCV 167,174,175\nMvav 180,132,108\nRa 188,0,15360\ns_add_u32 s5, s94, 786432\nMvav 180,128,100\n"
"v_lshlrev_b32 v188, 16, v214\nSv 164,1,16,5,1024\nMvav 180,144,36\nRa 128,0,16384\nv_and_b32 v189, 0xffff0000, v214\nMvav 180,148,44\nv_lshlrev_b32 v190, 16, v215\nCV 160,160,161\n"
"Mvav 180,152,52\nRa 132,0,17408\nCV 161,162,163\nMvav 180,156,60\nWL 6\ns_add_u32 m0, s89, 114688\ns_add_u32 s46, s96, 1376256\nMvav 176,156,56\nRa 136,0,18432\n"
"v_and_b32 v191, 0xffff0000, v215\nMvav 176,152,48\nD1 1,16,46,0\nCV 162,168,169\nMvav 176,148,40\nRa 140,0,19456\nCV 163,170,171\nMvav 176,144,32\nSv 160,1,16,5,0\n"
"Mvav 184,160,96\nRa 144,0,20480\nWV 12\nv_max_u32 v21, v219, v223\nv_cmp_eq_u32 vcc, -1, v21\ns_nop 0\ns_cbranch_vccnz .S355\n.S356:\nMvav 184,164,104\n"
"v_lshlrev_b32 v192, 16, v216\nMvav 184,168,112\nRa 148,0,21504\nMvav 184,172,120\nWL 6\nv_and_b32 v193, 0xffff0000, v216\nMvav 188,172,124\nRa 152,0,22528\nMvav 188,168,116\n"
"D1 1,16,46,1024\nMvav 188,164,108\nRa 156,0,23552\nv_lshlrev_b32 v194, 16, v217\nMvav 188,160,100\nv_and_b32 v195, 0xffff0000, v217\nMvav 188,176,36\nRa 160,0,24576\n"
"v_lshlrev_b32 v196, 16, v220\nMvav 188,180,44\nv_and_b32 v197, 0xffff0000, v220\ns_add_u32 s4, s92, 393216\nMvav 188,184,52\nRa 164,0,25600\nMvav 188,188,60\nWL 6\n"
"v_lshlrev_b32 v198, 16, v221\nbuffer_load_dwordx4 v[208:211], v1, s[16:19], s4 offen offset:0 sc0 sc1\nMvav 184,188,56\nRa 168,0,26624\nMvav 184,184,48\nD1 1,16,46,2048\n"
"Mvav 184,180,40\nRa 172,0,27648\nv_and_b32 v199, 0xffff0000, v221\nMvav 184,176,32\nv_lshlrev_b32 v200, 16, v218\n"
"buffer_load_dwordx4 v[212:215], v1, s[16:19], s4 offen offset:1024 sc0 sc1\nMvav 192,128,96\nRa 176,0,28672\nv_and_b32 v201, 0xffff0000, v218\nMvav 192,132,104\n"
"v_lshlrev_b32 v202, 16, v219\nCV 180,180,181\nMvav 192,136,112\nRa 180,0,29696\nCV 181,182,183\nMvav 192,140,120\ns_waitcnt vmcnt(15) lgkmcnt(6)\nCV 182,188,189\nMvav 196,140,124\n"
"Ra 184,0,30720\nv_and_b32 v203, 0xffff0000, v219\nMvav 196,136,116\nD1 1,16,46,3072\nCV 183,190,191\nMvav 196,132,108\nRa 188,0,31744\ns_barrier\nMvav 196,128,100\n"
"v_lshlrev_b32 v204, 16, v222\ns_add_u32 s5, s94, 917504\nMvav 196,144,36\nRa 128,2,16384\nv_and_b32 v205, 0xffff0000, v222\nMvav 196,148,44\nv_lshlrev_b32 v206, 16, v223\n"
"Sv 180,1,16,5,1024\nMvav 196,152,52\nRa 132,2,17408\nCV 176,176,177\nMvav 196,156,60\nWL 6\ns_add_u32 m0, s89, 0\ns_add_u32 s46, s96, 1409024\nMvav 192,156,56\nRa 136,2,18432\n"
"v_and_b32 v207, 0xffff0000, v223\nMvav 192,152,48\nD1 1,16,46,0\nCV 177,178,179\nMvav 192,148,40\nRa 140,2,19456\nCV 178,184,185\nMvav 192,144,32\nCV 179,186,187\nSv 176,1,16,5,0\n"
"Mvav 200,160,96\nRa 144,2,20480\nWV 12\nv_max_u32 v21, v227, v231\nv_cmp_eq_u32 vcc, -1, v21\ns_nop 0\ns_cbranch_vccnz .S358\n.S359:\nMvav 200,164,104\n"
"v_lshlrev_b32 v160, 16, v224\nMvav 200,168,112\nRa 148,2,21504\nMvav 200,172,120\nWL 6\nv_and_b32 v161, 0xffff0000, v224\nMvav 204,172,124\nRa 152,2,22528\nMvav 204,168,116\n"
"D1 1,16,46,1024\nMvav 204,164,108\nRa 156,2,23552\nv_lshlrev_b32 v162, 16, v225\nMvav 204,160,100\nv_and_b32 v163, 0xffff0000, v225\nMvav 204,176,36\nRa 160,2,24576\n"
"v_lshlrev_b32 v164, 16, v228\nMvav 204,180,44\nv_and_b32 v165, 0xffff0000, v228\ns_add_u32 s4, s92, 524288\nMvav 204,184,52\nRa 164,2,25600\nMvav 204,188,60\nWL 6\n"
"v_lshlrev_b32 v166, 16, v229\nbuffer_load_dwordx4 v[216:219], v1, s[16:19], s4 offen offset:0 sc0 sc1\nMvav 200,188,56\nRa 168,2,26624\nMvav 200,184,48\nD1 1,16,46,2048\n"
"Mvav 200,180,40\nRa 172,2,27648\nv_and_b32 v167, 0xffff0000, v229\nMvav 200,176,32\nv_lshlrev_b32 v168, 16, v226\n"
"buffer_load_dwordx4 v[220:223], v1, s[16:19], s4 offen offset:1024 sc0 sc1\nMvav 160,128,96\nRa 176,2,28672\nv_and_b32 v169, 0xffff0000, v226\nMvav 160,132,104\n"
"v_lshlrev_b32 v170, 16, v227\nCV 196,196,197\nMvav 160,136,112\nRa 180,2,29696\nCV 197,198,199\nMvav 160,140,120\nWL 6\nCV 198,204,205\nMvav 164,140,124\nRa 184,2,30720\n"
"v_and_b32 v24, 0x70, v1\nMvav 164,136,116\nD1 1,16,46,3072\nCV 199,206,207\nMvav 164,132,108\nRa 188,2,31744\nv_and_b32 v171, 0xffff0000, v227\nMvav 164,128,100\n"
"v_lshlrev_b32 v172, 16, v230\ns_add_u32 s5, s94, 1048576\nMvav 164,144,36\nRa 128,2,0\nv_lshlrev_b32 v24, 5, v24\nMvav 164,148,44\nv_and_b32 v173, 0xffff0000, v230\n"
"Sv 196,1,16,5,1024\nMvav 164,152,52\nRa 132,2,1024\nCV 192,192,193\nMvav 164,156,60\nWL 6\nv_lshlrev_b32 v174, 16, v231\nMvav 160,156,56\nRa 136,2,2048\n"
"buffer_load_dword v27, v24, s[16:19], s41 offen sc0 sc1\nMvav 160,152,48\nv_and_b32 v175, 0xffff0000, v231\nCV 193,194,195\nMvav 160,148,40\nRa 140,2,3072\nCV 194,200,201\n"
"Mvav 160,144,32\nCV 195,202,203\nSv 192,1,16,5,0\nMvav 168,160,96\nRa 144,2,4096\nWV 12\nv_max_u32 v21, v211, v215\nv_cmp_eq_u32 vcc, -1, v21\ns_nop 0\ns_cbranch_vccnz .S361\n"
".S362:\nMvav 168,164,104\nv_lshlrev_b32 v176, 16, v208\nMvav 168,168,112\nRa 148,2,5120\nMvav 168,172,120\nWL 6\nMvav 172,172,124\nRa 152,2,6144\nv_and_b32 v177, 0xffff0000, v208\n"
"Mvav 172,168,116\nv_lshlrev_b32 v178, 16, v209\nMvav 172,164,108\nRa 156,2,7168\nMvav 172,160,100\nv_and_b32 v179, 0xffff0000, v209\nMvav 172,176,36\nRa 160,2,8192\n"
"v_lshlrev_b32 v180, 16, v212\nMvav 172,180,44\nv_and_b32 v181, 0xffff0000, v212\ns_add_u32 s4, s92, 655360\nMvav 172,184,52\nRa 164,2,9216\nMvav 172,188,60\nWL 6\n"
"buffer_load_dwordx4 v[224:227], v1, s[16:19], s4 offen offset:0 sc0 sc1\nMvav 168,188,56\nRa 168,2,10240\nv_lshlrev_b32 v182, 16, v213\nMvav 168,184,48\n"
"v_and_b32 v183, 0xffff0000, v213\nMvav 168,180,40\nRa 172,2,11264\nMvav 168,176,32\nv_lshlrev_b32 v184, 16, v210\n"
"buffer_load_dwordx4 v[228:231], v1, s[16:19], s4 offen offset:1024 sc0 sc1\nMvav 176,128,96\nRa 176,2,12288\nv_and_b32 v185, 0xffff0000, v210\nMvav 176,132,104\n"
"v_lshlrev_b32 v186, 16, v211\nCV 164,164,165\nMvav 176,136,112\nRa 180,2,13312\nCV 165,166,167\nMvav 176,140,120\ns_waitcnt vmcnt(13) lgkmcnt(6)\nCV 166,172,173\nMvav 180,140,124\n"
"Ra 184,2,14336\nCV 167,174,175\nMvav 180,136,116\nv_and_b32 v187, 0xffff0000, v211\ns_add_u32 s5, s94, 1179648\nMvav 180,132,108\nRa 188,2,15360\ns_barrier\nMvav 180,128,100\n"
"v_lshlrev_b32 v188, 16, v214\nSv 164,1,16,5,1024\nMvav 180,144,36\nRa 128,2,32768\nv_and_b32 v189, 0xffff0000, v214\nMvav 180,148,44\nv_lshlrev_b32 v190, 16, v215\nCV 160,160,161\n"
"Mvav 180,152,52\nRa 132,2,33792\nCV 161,162,163\nMvav 180,156,60\nWL 6\nCV 162,168,169\nMvav 176,156,56\nRa 136,2,34816\nCV 163,170,171\nMvav 176,152,48\n"
"v_and_b32 v191, 0xffff0000, v215\nSv 160,1,16,5,0\nMvav 176,148,40\nRa 140,2,35840\nMvav 176,144,32\nMvav 184,160,96\nRa 144,2,36864\nWV 8\nv_max_u32 v21, v219, v223\n"
"v_cmp_eq_u32 vcc, -1, v21\ns_nop 0\ns_cbranch_vccnz .S364\n.S365:\nMvav 184,164,104\nv_lshlrev_b32 v192, 16, v216\nMvav 184,168,112\nRa 148,2,37888\nMvav 184,172,120\nWL 6\n"
"Mvav 188,172,124\nRa 152,2,38912\nv_and_b32 v193, 0xffff0000, v216\nMvav 188,168,116\nv_lshlrev_b32 v194, 16, v217\nMvav 188,164,108\nRa 156,2,39936\nMvav 188,160,100\n"
"v_and_b32 v195, 0xffff0000, v217\nMvav 188,176,36\nRa 160,2,40960\nv_lshlrev_b32 v196, 16, v220\nMvav 188,180,44\nv_and_b32 v197, 0xffff0000, v220\ns_add_u32 s4, s92, 786432\n"
"Mvav 188,184,52\nRa 164,2,41984\nMvav 188,188,60\nWL 6\nbuffer_load_dwordx4 v[208:211], v1, s[16:19], s4 offen offset:0 sc0 sc1\nMvav 184,188,56\nRa 168,2,43008\n"
"v_lshlrev_b32 v198, 16, v221\nMvav 184,184,48\nv_and_b32 v199, 0xffff0000, v221\nMvav 184,180,40\nRa 172,2,44032\nMvav 184,176,32\nv_lshlrev_b32 v200, 16, v218\n"
"buffer_load_dwordx4 v[212:215], v1, s[16:19], s4 offen offset:1024 sc0 sc1\nMvav 192,128,96\nRa 176,2,45056\nv_and_b32 v201, 0xffff0000, v218\nMvav 192,132,104\n"
"v_lshlrev_b32 v202, 16, v219\nCV 180,180,181\nMvav 192,136,112\nRa 180,2,46080\nCV 181,182,183\nMvav 192,140,120\nWL 6\nCV 182,188,189\nMvav 196,140,124\nRa 184,2,47104\n"
"CV 183,190,191\nMvav 196,136,116\nv_and_b32 v203, 0xffff0000, v219\ns_add_u32 s5, s94, 1310720\nMvav 196,132,108\nRa 188,2,48128\nSv 180,1,16,5,1024\nMvav 196,128,100\n"
"v_lshlrev_b32 v204, 16, v222\nCV 176,176,177\nMvav 196,144,36\nRa 128,2,49152\nv_and_b32 v205, 0xffff0000, v222\nMvav 196,148,44\nv_lshlrev_b32 v206, 16, v223\nCV 177,178,179\n"
"Mvav 196,152,52\nRa 132,2,50176\nCV 178,184,185\nMvav 196,156,60\nWL 6\nCV 179,186,187\nMvav 192,156,56\nRa 136,2,51200\nSv 176,1,16,5,0\nMvav 192,152,48\n"
"v_and_b32 v207, 0xffff0000, v223\nMvav 192,148,40\nRa 140,2,52224\nMvav 192,144,32\nMvav 200,160,96\nRa 144,2,53248\nWV 6\nv_max_u32 v21, v227, v231\nv_cmp_eq_u32 vcc, -1, v21\n"
"s_nop 0\ns_cbranch_vccnz .S367\n.S368:\nMvav 200,164,104\nv_lshlrev_b32 v160, 16, v224\nv_xor_b32 v27, s15, v27\nv_and_b32 v26, 0xffffdfff, v27\nv_cmp_ne_u32 s[64:65], 0, v27\n"
"v_cmp_ne_u32 vcc, 0, v26\ns_nop 0\ns_cbranch_vccnz .S379\n.S380:\nMvav 200,168,112\nRa 148,2,54272\ns_bitcmp1_b32 s64, 0\ns_cselect_b32 s2, 0x2000, 0\nMvav 200,172,120\nWL 6\n"
"Mvav 204,172,124\nRa 152,2,55296\nv_and_b32 v161, 0xffff0000, v224\nMvav 204,168,116\nv_lshlrev_b32 v162, 16, v225\nMvav 204,164,108\nRa 156,2,56320\ns_xor_b32 s2, s2, s20\n"
"Mvav 204,160,100\nv_and_b32 v163, 0xffff0000, v225\nMvav 204,176,36\nRa 160,2,57344\nv_lshlrev_b32 v164, 16, v228\nMvav 204,180,44\nv_and_b32 v165, 0xffff0000, v228\n"
"Mvav 204,184,52\nRa 164,2,58368\nMvav 204,188,60\nWL 6\ns_add_u32 s24, s2, 0x0\nMvav 200,188,56\nRa 168,2,59392\nv_lshlrev_b32 v166, 16, v229\nMvav 200,184,48\n"
"v_and_b32 v167, 0xffff0000, v229\ns_bitcmp1_b32 s64, 1\ns_cselect_b32 s2, 0x2000, 0\nMvav 200,180,40\nRa 172,2,60416\nMvav 200,176,32\nv_lshlrev_b32 v168, 16, v226\n"
"Mvav 160,128,96\nRa 176,2,61440\nv_and_b32 v169, 0xffff0000, v226\nMvav 160,132,104\nv_lshlrev_b32 v170, 16, v227\nCV 196,196,197\nMvav 160,136,112\nRa 180,2,62464\n"
"CV 197,198,199\nMvav 160,140,120\nWL 6\nCV 198,204,205\nMvav 164,140,124\nRa 184,2,63488\nCV 199,206,207\nMvav 164,136,116\nv_and_b32 v171, 0xffff0000, v227\n"
"s_add_u32 s5, s94, 1441792\nMvav 164,132,108\nRa 188,2,64512\ns_barrier\nMvav 164,128,100\nv_lshlrev_b32 v172, 16, v230\nSv 196,1,16,5,1024\nMvav 164,144,36\nRa 128,3,0\n"
"v_and_b32 v173, 0xffff0000, v230\nMvav 164,148,44\nv_lshlrev_b32 v174, 16, v231\nCV 192,192,193\nMvav 164,152,52\nRa 132,3,1024\nCV 193,194,195\nMvav 164,156,60\nWL 6\n"
"s_xor_b32 s2, s2, s20\nMvav 160,156,56\nRa 136,3,2048\nCV 194,200,201\nMvav 160,152,48\nv_and_b32 v175, 0xffff0000, v231\nCV 195,202,203\nMvav 160,148,40\nRa 140,3,3072\n"
"Sv 192,1,16,5,0\nMvav 160,144,32\ns_add_u32 s25, s2, 0x1800000\ns_bitcmp1_b32 s64, 2\ns_cselect_b32 s2, 0x2000, 0\nMvav 168,160,96\nRa 144,3,4096\nWV 4\nv_max_u32 v21, v211, v215\n"
"v_cmp_eq_u32 vcc, -1, v21\ns_nop 0\ns_cbranch_vccnz .S370\n.S371:\nMvav 168,164,104\nv_lshlrev_b32 v176, 16, v208\nMvav 168,168,112\nRa 148,3,5120\nMvav 168,172,120\nWL 6\n"
"Mvav 172,172,124\nRa 152,3,6144\nv_and_b32 v177, 0xffff0000, v208\nMvav 172,168,116\nv_lshlrev_b32 v178, 16, v209\nMvav 172,164,108\nRa 156,3,7168\ns_xor_b32 s2, s2, s20\n"
"Mvav 172,160,100\nv_and_b32 v179, 0xffff0000, v209\nMvav 172,176,36\nRa 160,3,8192\nv_lshlrev_b32 v180, 16, v212\nMvav 172,180,44\nv_and_b32 v181, 0xffff0000, v212\n"
"Mvav 172,184,52\nRa 164,3,9216\nMvav 172,188,60\nWL 6\ns_add_u32 s26, s2, 0x3000000\nMvav 168,188,56\nRa 168,3,10240\nv_lshlrev_b32 v182, 16, v213\nMvav 168,184,48\n"
"v_and_b32 v183, 0xffff0000, v213\ns_bitcmp1_b32 s64, 3\ns_cselect_b32 s2, 0x2000, 0\nMvav 168,180,40\nRa 172,3,11264\nMvav 168,176,32\nv_lshlrev_b32 v184, 16, v210\n"
"Mvav 176,128,96\nRa 176,3,12288\ns_xor_b32 s2, s2, s20\nMvav 176,132,104\nv_and_b32 v185, 0xffff0000, v210\nCV 164,164,165\nMvav 176,136,112\nRa 180,3,13312\nCV 165,166,167\n"
"Mvav 176,140,120\nWL 6\nCV 166,172,173\nMvav 180,140,124\nRa 184,3,14336\nCV 167,174,175\nMvav 180,136,116\nv_lshlrev_b32 v186, 16, v211\ns_add_u32 s5, s94, 1572864\n"
"Mvav 180,132,108\nRa 188,3,15360\ns_add_u32 s27, s2, 0x4800000\nMvav 180,128,100\nv_and_b32 v187, 0xffff0000, v211\ns_bitcmp1_b32 s64, 4\ns_cselect_b32 s2, 0x2000, 0\n"
"Mvav 180,144,36\nv_lshlrev_b32 v188, 16, v214\nSv 164,1,16,5,1024\nMvav 180,148,44\nv_and_b32 v189, 0xffff0000, v214\nCV 160,160,161\nMvav 180,152,52\n"
"v_lshlrev_b32 v190, 16, v215\nCV 161,162,163\nMvav 180,156,60\nWL 4\ns_xor_b32 s2, s2, s20\nMvav 176,156,56\nv_and_b32 v191, 0xffff0000, v215\nCV 162,168,169\nMvav 176,152,48\n"
"s_add_u32 s32, s2, 0x6000000\ns_bitcmp1_b32 s64, 5\ns_cselect_b32 s2, 0x2000, 0\nMvav 176,148,40\ns_xor_b32 s2, s2, s20\nCV 163,170,171\nMvav 176,144,32\n"
"s_add_u32 s33, s2, 0x7800000\ns_bitcmp1_b32 s64, 6\ns_cselect_b32 s2, 0x2000, 0\nMvav 184,160,96\ns_xor_b32 s2, s2, s20\ns_add_u32 s34, s2, 0x9000000\nMvav 184,164,104\n"
"s_bitcmp1_b32 s64, 7\ns_cselect_b32 s2, 0x2000, 0\nMvav 184,168,112\ns_xor_b32 s2, s2, s20\nSv 160,1,16,5,0\nMvav 184,172,120\nWL 0\nMvav 188,172,124\n"
"s_add_u32 s35, s2, 0xa800000\nMvav 188,168,116\nMvav 188,164,108\nMvav 188,160,100\nMvav 188,176,36\nMvav 188,180,44\nMvav 188,184,52\nMvav 188,188,60\nMvav 184,188,56\n"
"Mvav 184,184,48\nMvav 184,180,40\nMvav 184,176,32\nCV 180,180,181\nCV 181,182,183\ns_nop 1\nCV 182,188,189\nCV 183,190,191\ns_add_u32 s5, s94, 1703936\nSv 180,1,16,5,1024\n"
"CV 176,176,177\nCV 177,178,179\nCV 178,184,185\nCV 179,186,187\nSv 176,1,16,5,0\ns_cmp_eq_u32 s13, 7\ns_cbranch_scc1 .Z382\n"
"buffer_load_dwordx4 v[12:15], v91, s[28:31], s22 offen offset:0\nbuffer_load_dwordx4 v[16:19], v91, s[28:31], s23 offen offset:0\n"
"buffer_load_dwordx4 v[64:67], v89, s[16:19], s40 offen offset:0 sc0 sc1\nbuffer_load_dwordx4 v[68:71], v89, s[16:19], s40 offen offset:16 sc0 sc1\n"
"buffer_load_dwordx4 v[72:75], v89, s[16:19], s40 offen offset:1792 sc0 sc1\nbuffer_load_dwordx4 v[76:79], v89, s[16:19], s40 offen offset:1808 sc0 sc1\n"
"buffer_load_dwordx2 v[80:81], v88, s[16:19], s63 offen offset:0 sc0 sc1\nbuffer_load_dwordx2 v[82:83], v88, s[16:19], s63 offen offset:512 sc0 sc1\n"
"buffer_load_dwordx4 v[96:99], v1, s[16:19], s24 offen offset:0 sc0 sc1\nbuffer_load_dwordx4 v[100:103], v1, s[16:19], s24 offen offset:1024 sc0 sc1\n"
"buffer_load_dwordx4 v[104:107], v1, s[16:19], s25 offen offset:0 sc0 sc1\nbuffer_load_dwordx4 v[108:111], v1, s[16:19], s25 offen offset:1024 sc0 sc1\n"
"buffer_load_dwordx4 v[112:115], v1, s[16:19], s26 offen offset:0 sc0 sc1\nbuffer_load_dwordx4 v[116:119], v1, s[16:19], s26 offen offset:1024 sc0 sc1\n"
"buffer_load_dwordx4 v[120:123], v1, s[16:19], s27 offen offset:0 sc0 sc1\nbuffer_load_dwordx4 v[124:127], v1, s[16:19], s27 offen offset:1024 sc0 sc1\n"
"buffer_load_dwordx4 v[32:35], v1, s[16:19], s32 offen offset:0 sc0 sc1\nbuffer_load_dwordx4 v[36:39], v1, s[16:19], s32 offen offset:1024 sc0 sc1\n"
"buffer_load_dwordx4 v[40:43], v1, s[16:19], s33 offen offset:0 sc0 sc1\nbuffer_load_dwordx4 v[44:47], v1, s[16:19], s33 offen offset:1024 sc0 sc1\n"
"buffer_load_dwordx4 v[48:51], v1, s[16:19], s34 offen offset:0 sc0 sc1\nbuffer_load_dwordx4 v[52:55], v1, s[16:19], s34 offen offset:1024 sc0 sc1\n"
"buffer_load_dwordx4 v[56:59], v1, s[16:19], s35 offen offset:0 sc0 sc1\nbuffer_load_dwordx4 v[60:63], v1, s[16:19], s35 offen offset:1024 sc0 sc1\nWV 16\nv_xor_b32 v66, s15, v66\n"
"v_xor_b32 v70, s15, v70\nv_xor_b32 v74, s15, v74\nv_xor_b32 v78, s15, v78\nv_xor_b32 v81, s15, v81\nv_xor_b32 v83, s15, v83\nv_or3_b32 v92, v66, v70, v74\n"
"v_or3_b32 v92, v92, v78, v81\nv_or_b32 v92, v92, v83\nv_cmp_ne_u32 vcc, 0, v92\ns_nop 0\ns_cbranch_vccnz .S373\n.S374:\nv_and_b32 v92, 0x7ff, v67\nv_lshrrev_b32 v93, 16, v67\n"
"v_cmp_eq_u32 s[44:45], v92, v90\nv_cmp_eq_u32 s[46:47], v93, v90\nv_cmp_eq_u32 s[48:49], v92, v94\nv_cmp_eq_u32 s[50:51], v93, v94\nv_cndmask_b32 v12, v12, v64, s[44:45]\n"
"v_cndmask_b32 v13, v13, v65, s[44:45]\nv_cndmask_b32 v14, v14, v68, s[44:45]\nv_cndmask_b32 v15, v15, v69, s[44:45]\nv_cndmask_b32 v12, v12, v72, s[46:47]\n"
"v_cndmask_b32 v13, v13, v73, s[46:47]\nv_cndmask_b32 v14, v14, v76, s[46:47]\nv_cndmask_b32 v15, v15, v77, s[46:47]\nv_cndmask_b32 v16, v16, v64, s[48:49]\n"
"v_cndmask_b32 v17, v17, v65, s[48:49]\nv_cndmask_b32 v18, v18, v68, s[48:49]\nv_cndmask_b32 v19, v19, v69, s[48:49]\nv_cndmask_b32 v16, v16, v72, s[50:51]\n"
"v_cndmask_b32 v17, v17, v73, s[50:51]\nv_cndmask_b32 v18, v18, v76, s[50:51]\nv_cndmask_b32 v19, v19, v77, s[50:51]\nv_sub_f32 v92, 1.0, v80\nv_sub_f32 v93, 1.0, v82\nCV 92,92,93\n"
"v_and_b32 v93, 0xffff0000, v92\nv_lshlrev_b32 v92, 16, v92\nv_lshlrev_b32 v64, 16, v12\nv_and_b32 v65, 0xffff0000, v12\nMU 64,64,92\nMU 65,65,92\nv_lshlrev_b32 v66, 16, v13\n"
"v_and_b32 v67, 0xffff0000, v13\nMU 66,66,92\nMU 67,67,92\nv_lshlrev_b32 v68, 16, v14\nv_and_b32 v69, 0xffff0000, v14\nMU 68,68,92\nMU 69,69,92\nv_lshlrev_b32 v70, 16, v15\n"
"v_and_b32 v71, 0xffff0000, v15\nMU 70,70,92\nMU 71,71,92\nv_lshlrev_b32 v72, 16, v16\nv_and_b32 v73, 0xffff0000, v16\nMU 72,72,93\nMU 73,73,93\nv_lshlrev_b32 v74, 16, v17\n"
"v_and_b32 v75, 0xffff0000, v17\nMU 74,74,93\nMU 75,75,93\nv_lshlrev_b32 v76, 16, v18\nv_and_b32 v77, 0xffff0000, v18\nMU 76,76,93\nMU 77,77,93\nv_lshlrev_b32 v78, 16, v19\n"
"v_and_b32 v79, 0xffff0000, v19\nMU 78,78,93\nMU 79,79,93\ns_mov_b32 s7, 262144\nWV 0\n.Z385:\nv_max_u32 v84, v99, v103\nv_cmp_eq_u32 s[44:45], -1, v84\nv_max_u32 v84, v107, v111\n"
"v_cmp_eq_u32 s[46:47], -1, v84\nv_max_u32 v84, v115, v119\nv_cmp_eq_u32 s[48:49], -1, v84\nv_max_u32 v84, v123, v127\nv_cmp_eq_u32 s[50:51], -1, v84\nv_max_u32 v84, v35, v39\n"
"v_cmp_eq_u32 s[52:53], -1, v84\nv_max_u32 v84, v43, v47\nv_cmp_eq_u32 s[54:55], -1, v84\nv_max_u32 v84, v51, v55\nv_cmp_eq_u32 s[56:57], -1, v84\nv_max_u32 v84, v59, v63\n"
"v_cmp_eq_u32 s[58:59], -1, v84\ns_or_b64 s[0:1], s[44:45], s[46:47]\ns_or_b64 s[2:3], s[48:49], s[50:51]\ns_or_b64 s[0:1], s[0:1], s[52:53]\ns_or_b64 s[2:3], s[2:3], s[54:55]\n"
"s_or_b64 s[0:1], s[0:1], s[56:57]\ns_or_b64 s[2:3], s[2:3], s[58:59]\ns_or_b64 s[0:1], s[0:1], s[2:3]\ns_cbranch_scc1 .Z383\n.Z384:\nZvvv 136,6,96\nMvvv 136,6,104\nMvvv 136,6,112\n"
"Mvvv 136,6,120\nMvvv 136,6,32\nMvvv 136,6,40\nMvvv 136,6,48\nMvvv 136,6,56\nZvvv 140,4,96\nMvvv 140,4,104\nMvvv 140,4,112\nMvvv 140,4,120\nMvvv 140,4,32\nMvvv 140,4,40\n"
"Mvvv 140,4,48\nMvvv 140,4,56\nZvvv 144,6,100\nMvvv 144,6,108\nMvvv 144,6,116\nMvvv 144,6,124\nMvvv 144,6,36\nMvvv 144,6,44\nMvvv 144,6,52\nMvvv 144,6,60\nZvvv 148,4,100\n"
"Mvvv 148,4,108\nMvvv 148,4,116\nMvvv 148,4,124\nMvvv 148,4,36\nMvvv 148,4,44\nMvvv 148,4,52\nMvvv 148,4,60\nFM 64,80,136\nFM 65,80,137\nFM 66,80,138\nFM 67,80,139\nFM 68,80,140\n"
"FM 69,80,141\nFM 70,80,142\nFM 71,80,143\nCV 12,64,65\nCV 13,66,67\nCV 14,68,69\nCV 15,70,71\nSv 12,91,36,22,0\nFM 72,82,144\nFM 73,82,145\nFM 74,82,146\nFM 75,82,147\n"
"FM 76,82,148\nFM 77,82,149\nFM 78,82,150\nFM 79,82,151\nCV 16,72,73\nCV 17,74,75\nCV 18,76,77\nCV 19,78,79\nSv 16,91,36,23,0\n.Z382:\ns_endpgm\n.S266:\ns_mov_b32 s7, 262144\n"
".S268:\ns_lshl_b32 s5, s73, 8\ns_add_u32 s5, s5, 0xc04ac00\nv_lshrrev_b32 v27, 2, v1\nbuffer_load_dword v27, v27, s[16:19], s5 offen sc0 sc1\nWV 0\nv_xor_b32 v27, s15, v27\n"
"v_and_b32 v32, 0xffffdfff, v27\nv_cmp_ne_u32 vcc, 0, v32\ns_nop 0\ns_cbranch_vccz .S267\ns_sleep 6\ns_sub_u32 s7, s7, 1\ns_cbranch_scc0 .S268\ns_branch .S267\n.T274:\n"
"s_mov_b32 s62, 1\ns_call_b64 s[80:81], .RA262\ns_branch .T275\n.T276:\ns_mov_b32 s62, 1\ns_call_b64 s[80:81], .RA262\ns_branch .T277\n.S279:\ns_mov_b32 s7, 262144\n.S281:\n"
"buffer_load_dword v27, off, s[16:19], s5 sc0 sc1\nWV 0\nv_cmp_ne_u32 vcc, s15, v27\ns_nop 0\ns_cbranch_vccz .S280\ns_sleep 6\ns_sub_u32 s7, s7, 1\ns_cbranch_scc0 .S281\n"
"s_branch .S280\n.S282:\ns_mov_b32 s7, 262144\n.S284:\nv_and_b32 v27, 0x70, v1\nbuffer_load_dword v27, v27, s[16:19], s61 offen offset:128 sc0 sc1\nWV 0\n"
"v_cmp_ne_u32 vcc, s15, v27\ns_nop 0\ns_cbranch_vccz .S283\ns_sleep 6\ns_sub_u32 s7, s7, 1\ns_cbranch_scc0 .S284\ns_branch .S283\n.S285:\ns_mov_b32 s7, 262144\n.S287:\n"
"v_and_b32 v27, 0x70, v1\nbuffer_load_dword v27, v27, s[16:19], s61 offen offset:136 sc0 sc1\nWV 0\nv_cmp_ne_u32 vcc, s15, v27\ns_nop 0\ns_cbranch_vccz .S286\ns_sleep 6\n"
"s_sub_u32 s7, s7, 1\ns_cbranch_scc0 .S287\ns_branch .S286\n.S288:\ns_mov_b32 s7, 262144\n.S290:\nv_and_b32 v27, 0x70, v1\n"
"buffer_load_dword v27, v27, s[16:19], s61 offen offset:140 sc0 sc1\nWV 0\nv_cmp_ne_u32 vcc, s15, v27\ns_nop 0\ns_cbranch_vccz .S289\ns_sleep 6\ns_sub_u32 s7, s7, 1\n"
"s_cbranch_scc0 .S290\ns_branch .S289\n.S291:\ns_mov_b32 s7, 262144\n.S293:\nv_and_b32 v27, 0x70, v1\nbuffer_load_dword v27, v27, s[16:19], s61 offen offset:12 sc0 sc1\nWV 0\n"
"v_cmp_ne_u32 vcc, s15, v27\ns_nop 0\ns_cbranch_vccz .S292\ns_sleep 6\ns_sub_u32 s7, s7, 1\ns_cbranch_scc0 .S293\ns_branch .S292\n.S294:\ns_mov_b32 s7, 262144\n.S296:\n"
"v_and_b32 v27, 0x70, v1\nbuffer_load_dword v27, v27, s[16:19], s61 offen offset:8 sc0 sc1\nWV 0\nv_cmp_ne_u32 vcc, s15, v27\ns_nop 0\ns_cbranch_vccz .S295\ns_sleep 6\n"
"s_sub_u32 s7, s7, 1\ns_cbranch_scc0 .S296\ns_branch .S295\n.S297:\ns_mov_b32 s7, 262144\n.S299:\nv_and_b32 v27, 0xf0, v1\n"
"buffer_load_dword v27, v27, s[16:19], s61 offen offset:4 sc0 sc1\nWV 0\nv_cmp_ne_u32 vcc, s15, v27\ns_nop 0\ns_cbranch_vccz .S298\ns_sleep 6\ns_sub_u32 s7, s7, 1\n"
"s_cbranch_scc0 .S299\ns_branch .S298\n.T308:\ns_mov_b32 s62, 0\ns_call_b64 s[80:81], .RB263\ns_branch .T309\n.T310:\ns_mov_b32 s62, 0\ns_call_b64 s[80:81], .RA262\ns_branch .T311\n"
".T314:\ns_mov_b32 s62, 0\ns_call_b64 s[80:81], .RB263\ns_branch .T315\n.T316:\ns_mov_b32 s62, 0\ns_call_b64 s[80:81], .RA262\ns_branch .T317\n.T323:\ns_mov_b32 s62, 2\n"
"s_call_b64 s[80:81], .RB263\ns_branch .T324\n.T325:\ns_mov_b32 s62, 5\ns_call_b64 s[80:81], .RA262\ns_branch .T326\n.T327:\ns_mov_b32 s62, 5\ns_call_b64 s[80:81], .RA262\n"
"s_branch .T328\n.T339:\ns_mov_b32 s62, 4\ns_call_b64 s[80:81], .RB263\ns_branch .T340\n.T341:\ns_mov_b32 s62, 4\ns_call_b64 s[80:81], .RA262\ns_branch .T342\n.T346:\n"
"s_mov_b32 s62, 4\ns_call_b64 s[80:81], .RB263\ns_branch .T347\n.T348:\ns_mov_b32 s62, 4\ns_call_b64 s[80:81], .RA262\ns_branch .T349\n.T350:\ns_mov_b32 s62, 6\n"
"s_call_b64 s[80:81], .RB263\ns_branch .T351\n.S352:\ns_mov_b32 s7, 262144\n.S354:\ns_add_u32 s0, s92, 0\nbuffer_load_dwordx4 v[208:211], v1, s[16:19], s0 offen offset:0 sc0 sc1\n"
"buffer_load_dwordx4 v[212:215], v1, s[16:19], s0 offen offset:1024 sc0 sc1\nWV 0\nv_max_u32 v21, v211, v215\nv_cmp_eq_u32 vcc, -1, v21\ns_nop 0\ns_cbranch_vccz .S353\ns_sleep 6\n"
"s_sub_u32 s7, s7, 1\ns_cbranch_scc0 .S354\ns_branch .S353\n.S355:\ns_mov_b32 s7, 262144\n.S357:\ns_add_u32 s0, s92, 131072\n"
"buffer_load_dwordx4 v[216:219], v1, s[16:19], s0 offen offset:0 sc0 sc1\nbuffer_load_dwordx4 v[220:223], v1, s[16:19], s0 offen offset:1024 sc0 sc1\nWV 0\n"
"v_max_u32 v21, v219, v223\nv_cmp_eq_u32 vcc, -1, v21\ns_nop 0\ns_cbranch_vccz .S356\ns_sleep 6\ns_sub_u32 s7, s7, 1\ns_cbranch_scc0 .S357\ns_branch .S356\n.S358:\n"
"s_mov_b32 s7, 262144\n.S360:\ns_add_u32 s0, s92, 262144\nbuffer_load_dwordx4 v[224:227], v1, s[16:19], s0 offen offset:0 sc0 sc1\n"
"buffer_load_dwordx4 v[228:231], v1, s[16:19], s0 offen offset:1024 sc0 sc1\nWV 0\nv_max_u32 v21, v227, v231\nv_cmp_eq_u32 vcc, -1, v21\ns_nop 0\ns_cbranch_vccz .S359\ns_sleep 6\n"
"s_sub_u32 s7, s7, 1\ns_cbranch_scc0 .S360\ns_branch .S359\n.S361:\ns_mov_b32 s7, 262144\n.S363:\ns_add_u32 s0, s92, 393216\n"
"buffer_load_dwordx4 v[208:211], v1, s[16:19], s0 offen offset:0 sc0 sc1\nbuffer_load_dwordx4 v[212:215], v1, s[16:19], s0 offen offset:1024 sc0 sc1\nWV 0\n"
"v_max_u32 v21, v211, v215\nv_cmp_eq_u32 vcc, -1, v21\ns_nop 0\ns_cbranch_vccz .S362\ns_sleep 6\ns_sub_u32 s7, s7, 1\ns_cbranch_scc0 .S363\ns_branch .S362\n.S364:\n"
"s_mov_b32 s7, 262144\n.S366:\ns_add_u32 s0, s92, 524288\nbuffer_load_dwordx4 v[216:219], v1, s[16:19], s0 offen offset:0 sc0 sc1\n"
"buffer_load_dwordx4 v[220:223], v1, s[16:19], s0 offen offset:1024 sc0 sc1\nWV 0\nv_max_u32 v21, v219, v223\nv_cmp_eq_u32 vcc, -1, v21\ns_nop 0\ns_cbranch_vccz .S365\ns_sleep 6\n"
"s_sub_u32 s7, s7, 1\ns_cbranch_scc0 .S366\ns_branch .S365\n.S367:\ns_mov_b32 s7, 262144\n.S369:\ns_add_u32 s0, s92, 655360\n"
"buffer_load_dwordx4 v[224:227], v1, s[16:19], s0 offen offset:0 sc0 sc1\nbuffer_load_dwordx4 v[228:231], v1, s[16:19], s0 offen offset:1024 sc0 sc1\nWV 0\n"
"v_max_u32 v21, v227, v231\nv_cmp_eq_u32 vcc, -1, v21\ns_nop 0\ns_cbranch_vccz .S368\ns_sleep 6\ns_sub_u32 s7, s7, 1\ns_cbranch_scc0 .S369\ns_branch .S368\n.S370:\n"
"s_mov_b32 s7, 262144\n.S372:\ns_add_u32 s0, s92, 786432\nbuffer_load_dwordx4 v[208:211], v1, s[16:19], s0 offen offset:0 sc0 sc1\n"
"buffer_load_dwordx4 v[212:215], v1, s[16:19], s0 offen offset:1024 sc0 sc1\nWV 0\nv_max_u32 v21, v211, v215\nv_cmp_eq_u32 vcc, -1, v21\ns_nop 0\ns_cbranch_vccz .S371\ns_sleep 6\n"
"s_sub_u32 s7, s7, 1\ns_cbranch_scc0 .S372\ns_branch .S371\n.S373:\ns_mov_b32 s7, 262144\n.S375:\nbuffer_load_dwordx4 v[64:67], v89, s[16:19], s40 offen offset:0 sc0 sc1\n"
"buffer_load_dwordx4 v[68:71], v89, s[16:19], s40 offen offset:16 sc0 sc1\nbuffer_load_dwordx4 v[72:75], v89, s[16:19], s40 offen offset:1792 sc0 sc1\n"
"buffer_load_dwordx4 v[76:79], v89, s[16:19], s40 offen offset:1808 sc0 sc1\nbuffer_load_dwordx2 v[80:81], v88, s[16:19], s63 offen offset:0 sc0 sc1\n"
"buffer_load_dwordx2 v[82:83], v88, s[16:19], s63 offen offset:512 sc0 sc1\nWV 0\nv_xor_b32 v66, s15, v66\nv_xor_b32 v70, s15, v70\nv_xor_b32 v74, s15, v74\n"
"v_xor_b32 v78, s15, v78\nv_xor_b32 v81, s15, v81\nv_xor_b32 v83, s15, v83\nv_or3_b32 v92, v66, v70, v74\nv_or3_b32 v92, v92, v78, v81\nv_or_b32 v92, v92, v83\n"
"v_cmp_ne_u32 vcc, 0, v92\ns_nop 0\ns_cbranch_vccz .S374\ns_sleep 6\ns_sub_u32 s7, s7, 1\ns_cbranch_scc0 .S375\ns_branch .S374\n.S376:\ns_mov_b32 s7, 262144\n.S378:\n"
"buffer_load_dword v27, off, s[16:19], s60 offset:512 sc0 sc1\nWV 0\nv_cmp_ne_u32 vcc, s15, v27\ns_nop 0\ns_cbranch_vccz .S377\ns_sleep 6\ns_sub_u32 s7, s7, 1\n"
"s_cbranch_scc0 .S378\ns_branch .S377\n.S379:\ns_mov_b32 s7, 262144\n.S381:\nbuffer_load_dword v27, v24, s[16:19], s41 offen sc0 sc1\nWV 0\nv_xor_b32 v27, s15, v27\n"
"v_and_b32 v26, 0xffffdfff, v27\nv_cmp_ne_u32 s[64:65], 0, v27\nv_cmp_ne_u32 vcc, 0, v26\ns_nop 0\ns_cbranch_vccz .S380\ns_sleep 6\ns_sub_u32 s7, s7, 1\ns_cbranch_scc0 .S381\n"
"s_branch .S380\n.Z383:\ns_sub_u32 s7, s7, 1\ns_cbranch_scc1 .Z384\ns_cmp_lg_u64 s[44:45], 0\ns_cbranch_scc0 .Z386\n"
"buffer_load_dwordx4 v[96:99], v1, s[16:19], s24 offen offset:0 sc0 sc1\nbuffer_load_dwordx4 v[100:103], v1, s[16:19], s24 offen offset:1024 sc0 sc1\n.Z386:\n"
"s_cmp_lg_u64 s[46:47], 0\ns_cbranch_scc0 .Z387\nbuffer_load_dwordx4 v[104:107], v1, s[16:19], s25 offen offset:0 sc0 sc1\n"
"buffer_load_dwordx4 v[108:111], v1, s[16:19], s25 offen offset:1024 sc0 sc1\n.Z387:\ns_cmp_lg_u64 s[48:49], 0\ns_cbranch_scc0 .Z388\n"
"buffer_load_dwordx4 v[112:115], v1, s[16:19], s26 offen offset:0 sc0 sc1\nbuffer_load_dwordx4 v[116:119], v1, s[16:19], s26 offen offset:1024 sc0 sc1\n.Z388:\n"
"s_cmp_lg_u64 s[50:51], 0\ns_cbranch_scc0 .Z389\nbuffer_load_dwordx4 v[120:123], v1, s[16:19], s27 offen offset:0 sc0 sc1\n"
"buffer_load_dwordx4 v[124:127], v1, s[16:19], s27 offen offset:1024 sc0 sc1\n.Z389:\ns_cmp_lg_u64 s[52:53], 0\ns_cbranch_scc0 .Z390\n"
"buffer_load_dwordx4 v[32:35], v1, s[16:19], s32 offen offset:0 sc0 sc1\nbuffer_load_dwordx4 v[36:39], v1, s[16:19], s32 offen offset:1024 sc0 sc1\n.Z390:\n"
"s_cmp_lg_u64 s[54:55], 0\ns_cbranch_scc0 .Z391\nbuffer_load_dwordx4 v[40:43], v1, s[16:19], s33 offen offset:0 sc0 sc1\n"
"buffer_load_dwordx4 v[44:47], v1, s[16:19], s33 offen offset:1024 sc0 sc1\n.Z391:\ns_cmp_lg_u64 s[56:57], 0\ns_cbranch_scc0 .Z392\n"
"buffer_load_dwordx4 v[48:51], v1, s[16:19], s34 offen offset:0 sc0 sc1\nbuffer_load_dwordx4 v[52:55], v1, s[16:19], s34 offen offset:1024 sc0 sc1\n.Z392:\n"
"s_cmp_lg_u64 s[58:59], 0\ns_cbranch_scc0 .Z393\nbuffer_load_dwordx4 v[56:59], v1, s[16:19], s35 offen offset:0 sc0 sc1\n"
"buffer_load_dwordx4 v[60:63], v1, s[16:19], s35 offen offset:1024 sc0 sc1\n.Z393:\ns_sleep 8\nWV 0\ns_branch .Z385\n.RA262:\ns_nop 7\ns_nop 3\nv_max3_f32 v22, v32, v33, v34\n"
"v_max3_f32 v22, v22, v35, v40\nv_max3_f32 v22, v22, v41, v42\nv_max_f32 v22, v22, v43\nv_mov_b32 v91, v22\ns_nop 1\nv_permlane32_swap_b32 v22, v91\nv_max_f32 v22, v22, v91\n"
"v_mov_b32 v91, v22\ns_nop 1\nv_permlane16_swap_b32 v22, v91\nv_max_f32 v22, v22, v91\nv_ceil_f32 v22, v22\nv_max3_f32 v23, v36, v37, v38\nv_max3_f32 v23, v23, v39, v44\n"
"v_max3_f32 v23, v23, v45, v46\nv_max_f32 v23, v23, v47\nv_mov_b32 v91, v23\ns_nop 1\nv_permlane32_swap_b32 v23, v91\nv_max_f32 v23, v23, v91\nv_mov_b32 v91, v23\ns_nop 1\n"
"v_permlane16_swap_b32 v23, v91\nv_max_f32 v23, v23, v91\nv_ceil_f32 v23, v23\ns_bitcmp1_b32 s62, 0\ns_cbranch_scc0 .R394\nv_cmp_lt_f32 vcc, 0xff000000, v22\ns_nop 1\n"
"v_cndmask_b32 v22, 0, v22, vcc\nv_cmp_lt_f32 vcc, 0xff000000, v23\ns_nop 1\nv_cndmask_b32 v23, 0, v23, vcc\ns_branch .R395\n.R394:\nv_max_f32 v22, 0, v22\nv_max_f32 v23, 0, v23\n"
"s_bitcmp1_b32 s62, 3\ns_cbranch_scc0 .R395\nv_max_f32 v22, v20, v22\nv_max_f32 v23, v21, v23\n.R395:\nAD 88,88,22\nAD 89,89,23\nSU 32,32,22\nSU 33,33,22\nSU 34,34,22\nSU 35,35,22\n"
"SU 36,36,23\nSU 37,37,23\nSU 38,38,23\nSU 39,39,23\nSU 40,40,22\nSU 41,41,22\nSU 42,42,22\nSU 43,43,22\nSU 44,44,23\nSU 45,45,23\nSU 46,46,23\nSU 47,47,23\nv_mov_b32 v90, v80\n"
"v_mov_b32 v91, v81\nEX 80,32\nEX 81,33\ns_nop 0\nAD 84,80,81\nCV 64,80,81\nEX 82,34\nEX 83,35\nAD 84,84,82\nAD 84,84,83\nCV 65,82,83\nEX 80,36\nEX 81,37\ns_nop 0\nAD 85,80,81\n"
"CV 68,80,81\nEX 82,38\nEX 83,39\nAD 85,85,82\nAD 85,85,83\nCV 69,82,83\nEX 80,40\nEX 81,41\nAD 84,84,80\nAD 84,84,81\nCV 66,80,81\nEX 82,42\nEX 83,43\nAD 84,84,82\nAD 84,84,83\n"
"CV 67,82,83\nEX 80,44\nEX 81,45\nAD 85,85,80\nAD 85,85,81\nCV 70,80,81\nEX 82,46\nEX 83,47\nAD 85,85,82\nAD 85,85,83\nCV 71,82,83\nv_mov_b32 v80, v90\nv_mov_b32 v81, v91\n"
"s_bitcmp1_b32 s62, 1\ns_cbranch_scc1 .R396\nSU 48,48,22\nSU 49,49,22\nSU 50,50,22\nSU 51,51,22\nSU 52,52,23\nSU 53,53,23\nSU 54,54,23\nSU 55,55,23\nSU 56,56,22\nSU 57,57,22\n"
"SU 58,58,22\nSU 59,59,22\nSU 60,60,23\nSU 61,61,23\nSU 62,62,23\nSU 63,63,23\nSU 4,4,22\nSU 5,5,22\nSU 6,6,22\nSU 7,7,22\nSU 8,8,23\nSU 9,9,23\nSU 10,10,23\nSU 11,11,23\n"
"SU 12,12,22\nSU 13,13,22\nSU 14,14,22\nSU 15,15,22\nSU 16,16,23\nSU 17,17,23\nSU 18,18,23\nSU 19,19,23\nEX 80,48\nEX 81,49\n.R396:\ns_bitcmp1_b32 s62, 0\ns_cbranch_scc0 .RS264\n"
"s_nop 1\ns_setpc_b64 s[80:81]\n.RB263:\ns_nop 7\ns_nop 3\nv_max3_f32 v22, v48, v49, v50\nv_max3_f32 v22, v22, v51, v56\nv_max3_f32 v22, v22, v57, v58\nv_max_f32 v22, v22, v59\n"
"v_mov_b32 v91, v22\ns_nop 1\nv_permlane32_swap_b32 v22, v91\nv_max_f32 v22, v22, v91\nv_mov_b32 v91, v22\ns_nop 1\nv_permlane16_swap_b32 v22, v91\nv_max_f32 v22, v22, v91\n"
"v_ceil_f32 v22, v22\nv_max3_f32 v23, v52, v53, v54\nv_max3_f32 v23, v23, v55, v60\nv_max3_f32 v23, v23, v61, v62\nv_max_f32 v23, v23, v63\nv_mov_b32 v91, v23\ns_nop 1\n"
"v_permlane32_swap_b32 v23, v91\nv_max_f32 v23, v23, v91\nv_mov_b32 v91, v23\ns_nop 1\nv_permlane16_swap_b32 v23, v91\nv_max_f32 v23, v23, v91\nv_ceil_f32 v23, v23\n"
"s_bitcmp1_b32 s62, 0\ns_cbranch_scc0 .R399\nv_cmp_lt_f32 vcc, 0xff000000, v22\nv_cndmask_b32 v22, 0, v22, vcc\nv_cmp_lt_f32 vcc, 0xff000000, v23\nv_cndmask_b32 v23, 0, v23, vcc\n"
"s_branch .R400\n.R399:\nv_max_f32 v22, 0, v22\nv_max_f32 v23, 0, v23\ns_bitcmp1_b32 s62, 3\ns_cbranch_scc0 .R400\nv_max_f32 v22, v20, v22\nv_max_f32 v23, v21, v23\n.R400:\n"
"AD 88,88,22\nAD 89,89,23\nSU 48,48,22\nSU 49,49,22\nSU 50,50,22\nSU 51,51,22\nSU 52,52,23\nSU 53,53,23\nSU 54,54,23\nSU 55,55,23\nSU 56,56,22\nSU 57,57,22\nSU 58,58,22\n"
"SU 59,59,22\nSU 60,60,23\nSU 61,61,23\nSU 62,62,23\nSU 63,63,23\nv_mov_b32 v90, v80\nv_mov_b32 v91, v81\nEX 80,48\nEX 81,49\ns_nop 0\nAD 84,80,81\nCV 72,80,81\nEX 82,50\nEX 83,51\n"
"AD 84,84,82\nAD 84,84,83\nCV 73,82,83\nEX 80,52\nEX 81,53\ns_nop 0\nAD 85,80,81\nCV 76,80,81\nEX 82,54\nEX 83,55\nAD 85,85,82\nAD 85,85,83\nCV 77,82,83\nEX 80,56\nEX 81,57\n"
"AD 84,84,80\nAD 84,84,81\nCV 74,80,81\nEX 82,58\nEX 83,59\nAD 84,84,82\nAD 84,84,83\nCV 75,82,83\nEX 80,60\nEX 81,61\nAD 85,85,80\nAD 85,85,81\nCV 78,80,81\nEX 82,62\nEX 83,63\n"
"AD 85,85,82\nAD 85,85,83\nCV 79,82,83\nv_mov_b32 v80, v90\nv_mov_b32 v81, v91\ns_bitcmp1_b32 s62, 1\ns_cbranch_scc1 .R401\nSU 32,32,22\nSU 33,33,22\nSU 34,34,22\nSU 35,35,22\n"
"SU 36,36,23\nSU 37,37,23\nSU 38,38,23\nSU 39,39,23\nSU 40,40,22\nSU 41,41,22\nSU 42,42,22\nSU 43,43,22\nSU 44,44,23\nSU 45,45,23\nSU 46,46,23\nSU 47,47,23\nSU 4,4,22\nSU 5,5,22\n"
"SU 6,6,22\nSU 7,7,22\nSU 8,8,23\nSU 9,9,23\nSU 10,10,23\nSU 11,11,23\nSU 12,12,22\nSU 13,13,22\nSU 14,14,22\nSU 15,15,22\nSU 16,16,23\nSU 17,17,23\nSU 18,18,23\nSU 19,19,23\n"
"EX 80,32\nEX 81,33\n.R401:\ns_bitcmp1_b32 s62, 0\ns_cbranch_scc0 .RS264\ns_nop 1\ns_setpc_b64 s[80:81]\n.RS264:\nv_exp_f32 v22, -v22\nv_exp_f32 v23, -v23\nMU 86,86,22\n"
"MU 87,87,23\ns_bitcmp1_b32 s62, 2\ns_cbranch_scc1 .R404\nAR 91,0\nMU 91,91,22\nAW 0,91\nAR 91,1\nMU 91,91,22\nAW 1,91\nAR 91,2\nMU 91,91,22\nAW 2,91\nAR 91,3\nMU 91,91,22\n"
"AW 3,91\nAR 91,4\nMU 91,91,23\nAW 4,91\nAR 91,5\nMU 91,91,23\nAW 5,91\nAR 91,6\nMU 91,91,23\nAW 6,91\nAR 91,7\nMU 91,91,23\nAW 7,91\nAR 91,8\nMU 91,91,22\nAW 8,91\nAR 91,9\n"
"MU 91,91,22\nAW 9,91\nAR 91,10\nMU 91,91,22\nAW 10,91\nAR 91,11\nMU 91,91,22\nAW 11,91\nAR 91,12\nMU 91,91,23\nAW 12,91\nAR 91,13\nMU 91,91,23\nAW 13,91\nAR 91,14\nMU 91,91,23\n"
"AW 14,91\nAR 91,15\nMU 91,91,23\nAW 15,91\nAR 91,16\nMU 91,91,22\nAW 16,91\nAR 91,17\nMU 91,91,22\nAW 17,91\nAR 91,18\nMU 91,91,22\nAW 18,91\nAR 91,19\nMU 91,91,22\nAW 19,91\n"
"AR 91,20\nMU 91,91,23\nAW 20,91\nAR 91,21\nMU 91,91,23\nAW 21,91\nAR 91,22\nMU 91,91,23\nAW 22,91\nAR 91,23\nMU 91,91,23\nAW 23,91\nAR 91,24\nMU 91,91,22\nAW 24,91\nAR 91,25\n"
"MU 91,91,22\nAW 25,91\nAR 91,26\nMU 91,91,22\nAW 26,91\nAR 91,27\nMU 91,91,22\nAW 27,91\nAR 91,28\nMU 91,91,23\nAW 28,91\nAR 91,29\nMU 91,91,23\nAW 29,91\nAR 91,30\nMU 91,91,23\n"
"AW 30,91\nAR 91,31\nMU 91,91,23\nAW 31,91\nAR 91,32\nMU 91,91,22\nAW 32,91\nAR 91,33\nMU 91,91,22\nAW 33,91\nAR 91,34\nMU 91,91,22\nAW 34,91\nAR 91,35\nMU 91,91,22\nAW 35,91\n"
"AR 91,36\nMU 91,91,23\nAW 36,91\nAR 91,37\nMU 91,91,23\nAW 37,91\nAR 91,38\nMU 91,91,23\nAW 38,91\nAR 91,39\nMU 91,91,23\nAW 39,91\nAR 91,40\nMU 91,91,22\nAW 40,91\nAR 91,41\n"
"MU 91,91,22\nAW 41,91\nAR 91,42\nMU 91,91,22\nAW 42,91\nAR 91,43\nMU 91,91,22\nAW 43,91\nAR 91,44\nMU 91,91,23\nAW 44,91\nAR 91,45\nMU 91,91,23\nAW 45,91\nAR 91,46\nMU 91,91,23\n"
"AW 46,91\nAR 91,47\nMU 91,91,23\nAW 47,91\nAR 91,48\nMU 91,91,22\nAW 48,91\nAR 91,49\nMU 91,91,22\nAW 49,91\nAR 91,50\nMU 91,91,22\nAW 50,91\nAR 91,51\nMU 91,91,22\nAW 51,91\n"
"AR 91,52\nMU 91,91,23\nAW 52,91\nAR 91,53\nMU 91,91,23\nAW 53,91\nAR 91,54\nMU 91,91,23\nAW 54,91\nAR 91,55\nMU 91,91,23\nAW 55,91\nAR 91,56\nMU 91,91,22\nAW 56,91\nAR 91,57\n"
"MU 91,91,22\nAW 57,91\nAR 91,58\nMU 91,91,22\nAW 58,91\nAR 91,59\nMU 91,91,22\nAW 59,91\nAR 91,60\nMU 91,91,23\nAW 60,91\nAR 91,61\nMU 91,91,23\nAW 61,91\nAR 91,62\nMU 91,91,23\n"
"AW 62,91\nAR 91,63\nMU 91,91,23\nAW 63,91\nAR 91,64\nMU 91,91,22\nAW 64,91\nAR 91,65\nMU 91,91,22\nAW 65,91\nAR 91,66\nMU 91,91,22\nAW 66,91\nAR 91,67\nMU 91,91,22\nAW 67,91\n"
"AR 91,68\nMU 91,91,23\nAW 68,91\nAR 91,69\nMU 91,91,23\nAW 69,91\nAR 91,70\nMU 91,91,23\nAW 70,91\nAR 91,71\nMU 91,91,23\nAW 71,91\nAR 91,72\nMU 91,91,22\nAW 72,91\nAR 91,73\n"
"MU 91,91,22\nAW 73,91\nAR 91,74\nMU 91,91,22\nAW 74,91\nAR 91,75\nMU 91,91,22\nAW 75,91\nAR 91,76\nMU 91,91,23\nAW 76,91\nAR 91,77\nMU 91,91,23\nAW 77,91\nAR 91,78\nMU 91,91,23\n"
"AW 78,91\nAR 91,79\nMU 91,91,23\nAW 79,91\nAR 91,80\nMU 91,91,22\nAW 80,91\nAR 91,81\nMU 91,91,22\nAW 81,91\nAR 91,82\nMU 91,91,22\nAW 82,91\nAR 91,83\nMU 91,91,22\nAW 83,91\n"
"AR 91,84\nMU 91,91,23\nAW 84,91\nAR 91,85\nMU 91,91,23\nAW 85,91\nAR 91,86\nMU 91,91,23\nAW 86,91\nAR 91,87\nMU 91,91,23\nAW 87,91\nAR 91,88\nMU 91,91,22\nAW 88,91\nAR 91,89\n"
"MU 91,91,22\nAW 89,91\nAR 91,90\nMU 91,91,22\nAW 90,91\nAR 91,91\nMU 91,91,22\nAW 91,91\nAR 91,92\nMU 91,91,23\nAW 92,91\nAR 91,93\nMU 91,91,23\nAW 93,91\nAR 91,94\nMU 91,91,23\n"
"AW 94,91\nAR 91,95\nMU 91,91,23\nAW 95,91\nAR 91,96\nMU 91,91,22\nAW 96,91\nAR 91,97\nMU 91,91,22\nAW 97,91\nAR 91,98\nMU 91,91,22\nAW 98,91\nAR 91,99\nMU 91,91,22\nAW 99,91\n"
"AR 91,100\nMU 91,91,23\nAW 100,91\nAR 91,101\nMU 91,91,23\nAW 101,91\nAR 91,102\nMU 91,91,23\nAW 102,91\nAR 91,103\nMU 91,91,23\nAW 103,91\nAR 91,104\nMU 91,91,22\nAW 104,91\n"
"AR 91,105\nMU 91,91,22\nAW 105,91\nAR 91,106\nMU 91,91,22\nAW 106,91\nAR 91,107\nMU 91,91,22\nAW 107,91\nAR 91,108\nMU 91,91,23\nAW 108,91\nAR 91,109\nMU 91,91,23\nAW 109,91\n"
"AR 91,110\nMU 91,91,23\nAW 110,91\nAR 91,111\nMU 91,91,23\nAW 111,91\nAR 91,112\nMU 91,91,22\nAW 112,91\nAR 91,113\nMU 91,91,22\nAW 113,91\nAR 91,114\nMU 91,91,22\nAW 114,91\n"
"AR 91,115\nMU 91,91,22\nAW 115,91\nAR 91,116\nMU 91,91,23\nAW 116,91\nAR 91,117\nMU 91,91,23\nAW 117,91\nAR 91,118\nMU 91,91,23\nAW 118,91\nAR 91,119\nMU 91,91,23\nAW 119,91\n"
"AR 91,120\nMU 91,91,22\nAW 120,91\nAR 91,121\nMU 91,91,22\nAW 121,91\nAR 91,122\nMU 91,91,22\nAW 122,91\nAR 91,123\nMU 91,91,22\nAW 123,91\nAR 91,124\nMU 91,91,23\nAW 124,91\n"
"AR 91,125\nMU 91,91,23\nAW 125,91\nAR 91,126\nMU 91,91,23\nAW 126,91\nAR 91,127\nMU 91,91,23\nAW 127,91\ns_nop 1\ns_setpc_b64 s[80:81]\n.R404:\nMU 128,128,22\nMU 129,129,22\n"
"MU 130,130,22\nMU 131,131,22\nMU 132,132,23\nMU 133,133,23\nMU 134,134,23\nMU 135,135,23\nMU 136,136,22\nMU 137,137,22\nMU 138,138,22\nMU 139,139,22\nMU 140,140,23\nMU 141,141,23\n"
"MU 142,142,23\nMU 143,143,23\nMU 144,144,22\nMU 145,145,22\nMU 146,146,22\nMU 147,147,22\nMU 148,148,23\nMU 149,149,23\nMU 150,150,23\nMU 151,151,23\nMU 152,152,22\nMU 153,153,22\n"
"MU 154,154,22\nMU 155,155,22\nMU 156,156,23\nMU 157,157,23\nMU 158,158,23\nMU 159,159,23\nMU 160,160,22\nMU 161,161,22\nMU 162,162,22\nMU 163,163,22\nMU 164,164,23\nMU 165,165,23\n"
"MU 166,166,23\nMU 167,167,23\nMU 168,168,22\nMU 169,169,22\nMU 170,170,22\nMU 171,171,22\nMU 172,172,23\nMU 173,173,23\nMU 174,174,23\nMU 175,175,23\nMU 176,176,22\nMU 177,177,22\n"
"MU 178,178,22\nMU 179,179,22\nMU 180,180,23\nMU 181,181,23\nMU 182,182,23\nMU 183,183,23\nMU 184,184,22\nMU 185,185,22\nMU 186,186,22\nMU 187,187,22\nMU 188,188,23\nMU 189,189,23\n"
"MU 190,190,23\nMU 191,191,23\nMU 192,192,22\nMU 193,193,22\nMU 194,194,22\nMU 195,195,22\nMU 196,196,23\nMU 197,197,23\nMU 198,198,23\nMU 199,199,23\nMU 200,200,22\nMU 201,201,22\n"
"MU 202,202,22\nMU 203,203,22\nMU 204,204,23\nMU 205,205,23\nMU 206,206,23\nMU 207,207,23\nMU 208,208,22\nMU 209,209,22\nMU 210,210,22\nMU 211,211,22\nMU 212,212,23\nMU 213,213,23\n"
"MU 214,214,23\nMU 215,215,23\nMU 216,216,22\nMU 217,217,22\nMU 218,218,22\nMU 219,219,22\nMU 220,220,23\nMU 221,221,23\nMU 222,222,23\nMU 223,223,23\nMU 224,224,22\nMU 225,225,22\n"
"MU 226,226,22\nMU 227,227,22\nMU 228,228,23\nMU 229,229,23\nMU 230,230,23\nMU 231,231,23\nMU 232,232,22\nMU 233,233,22\nMU 234,234,22\nMU 235,235,22\nMU 236,236,23\nMU 237,237,23\n"
"MU 238,238,23\nMU 239,239,23\nMU 240,240,22\nMU 241,241,22\nMU 242,242,22\nMU 243,243,22\nMU 244,244,23\nMU 245,245,23\nMU 246,246,23\nMU 247,247,23\nMU 248,248,22\nMU 249,249,22\n"
"MU 250,250,22\nMU 251,251,22\nMU 252,252,23\nMU 253,253,23\nMU 254,254,23\nMU 255,255,23\ns_nop 1\ns_setpc_b64 s[80:81]\n.p2align 12\n.PA5:\ns_add_u32 m0, s47, -49152\n"
"s_add_u32 s48, s48, 128\nD0 2,20,48,0\nD0 3,20,48,1024\nD0 4,20,48,2048\nD0 5,20,48,3072\ns_and_b32 s86, s74, 15\ns_lshl2_add_u32 s86, s86, s11\ns_mov_b32 s88, 0\n"
"s_lshl_b32 s93, s73, 8\ns_lshl2_add_u32 s93, s86, s93\ns_add_u32 s93, s93, 0xc04ac00\ns_lshl_b32 s86, s86, 14\ns_mul_i32 s91, s73, 25165824\ns_add_u32 s86, s86, s91\n"
"s_and_b32 s92, s13, 1\ns_lshl_b32 s92, s92, 13\ns_add_u32 s86, s86, s92\ns_add_u32 s94, s86, 0x1480000\ns_xor_b32 s95, s94, 0x2000\ns_add_u32 s86, s94, 512\n"
"s_load_dword s87, s[54:55], s86\ns_add_u32 m0, s47, -32768\ns_add_u32 s45, s45, 128\nD0 2,20,45,0\nD0 3,20,45,1024\nD0 4,20,45,2048\nD0 5,20,45,3072\ns_add_u32 m0, s47, 32768\n"
"s_add_u32 s44, s44, 0x40000\nD0 6,24,44,0\nD0 7,24,44,1024\nD0 8,24,44,2048\nD0 9,24,44,3072\ns_add_u32 m0, s47, 49152\ns_add_u32 s44, s44, 0x40000\nD0 6,24,44,0\nD0 7,24,44,1024\n"
"D0 8,24,44,2048\nD0 9,24,44,3072\ns_add_u32 m0, s47, -16384\ns_add_u32 s48, s48, 128\nD0 2,20,48,0\nD0 3,20,48,1024\nD0 4,20,48,2048\nD0 5,20,48,3072\nv_mov_b32 v52, 0x40000000\n"
"v_cmp_gt_u32 vcc, 56, v0\ns_lshl2_add_u32 s2, s10, s11\ns_mul_i32 s2, s2, 1792\nv_cndmask_b32 v53, v52, v1, vcc\ns_mov_b32 s36, s56\ns_and_b32 s37, s57, 0xffff\n"
"s_mov_b32 s38, 0x380\ns_mov_b32 s39, 0x20000\ns_mov_b32 s40, s58\ns_and_b32 s41, s59, 0xffff\ns_mov_b32 s42, 0x380\ns_mov_b32 s43, 0x20000\n"
"buffer_load_dwordx4 v[32:35], v53, s[28:31], s2 offen offset:0\nbuffer_load_dwordx4 v[36:39], v53, s[28:31], s2 offen offset:896\n"
"buffer_load_dwordx4 v[40:43], v53, s[36:39], 0 offen offset:0\nbuffer_load_dwordx4 v[44:47], v53, s[40:43], 0 offen offset:0\nWL 0\ns_mul_i32 s15, s15, 1664525\n"
"s_add_u32 s15, s15, 1013904223\nv_mov_b32 v25, s15\ns_mov_b32 s57, 0x3db8aa3b\ns_and_b32 s59, s11, 1\ns_lshl_b32 s59, s59, 13\ns_mul_i32 s2, s73, 25165824\ns_and_b32 s3, s12, 1\n"
"s_lshl_b32 s3, s3, 21\ns_add_u32 s2, s2, s3\ns_lshl2_add_u32 s3, s13, s11\ns_lshl_b32 s3, s3, 15\ns_add_u32 s2, s2, s3\ns_add_u32 s56, s2, 0x800000\ns_xor_b32 s53, s56, 0xc000\n"
"buffer_load_dword v31, v24, s[24:27], s96 offen\ns_add_u32 s96, s96, 0x80000\nbuffer_load_dword v31, v24, s[24:27], s96 offen\ns_add_u32 s96, s96, 0x80000\n"
"v_lshrrev_b32 v60, 3, v0\nv_and_b32 v61, 0x70, v1\nv_lshl_or_b32 v60, v60, 10, v61\nv_mov_b32 v20, 0\nv_mov_b32 v21, 0\nv_mov_b32 v22, 0\nv_mov_b32 v23, 0\nSv 20,60,16,94,0\n"
"Sv 20,60,16,95,0\nWV 28\ns_barrier\nRa 128,10,0\nRa 132,11,0\nRa 136,10,2048\nRa 140,11,2048\nRa 144,10,8192\nRa 148,11,8192\nRa 152,10,10240\nRa 156,11,10240\nTa 160,12,0\n"
"Ta 162,12,2048\nTa 164,12,16384\nTa 166,12,18432\nTa 168,13,0\nTa 170,13,2048\nTa 172,13,16384\nTa 174,13,18432\nTa 176,14,0\nTa 178,14,2048\nTa 180,14,16384\nTa 182,14,18432\n"
"Ta 184,15,0\nTa 186,15,2048\nTa 188,15,16384\nTa 190,15,18432\nWL 0\ns_barrier\nZvaa 64,128,160\nTa 224,16,0\nMvaa 64,132,164\nTa 226,16,2048\nZvaa 68,140,164\n"
"s_add_u32 m0, s47, -65536\ns_add_u32 s45, s45, 128\nMvaa 68,136,160\nTa 228,16,16384\nZvaa 128,144,160\nTa 230,16,18432\nMvaa 128,148,164\nD0 2,20,45,0\nZvaa 132,156,164\n"
"Ta 232,17,0\nMvaa 132,152,160\nTa 234,17,2048\nZvaa 140,152,168\ns_mov_b32 s36, s60\nMvaa 140,156,172\nTa 236,17,16384\nZvaa 136,148,172\nTa 238,17,18432\nMvaa 136,144,168\n"
"s_and_b32 s37, s61, 0xffff\nZvaa 76,136,168\nTa 240,18,0\nMvaa 76,140,172\nTa 242,18,2048\nZvaa 72,132,172\nD0 3,20,45,1024\nMvaa 72,128,168\nTa 244,18,16384\nZvaa 80,128,176\n"
"Ta 246,18,18432\nMvaa 80,132,180\ns_mov_b32 s38, 0x380000\nZvaa 84,140,180\nTa 248,19,0\nMvaa 84,136,176\nTa 250,19,2048\nZvaa 144,144,176\ns_mov_b32 s39, 0x20000\n"
"Mvaa 144,148,180\nTa 252,19,16384\nZvaa 148,156,180\nTa 254,19,18432\nMvaa 148,152,176\nD0 4,20,45,2048\nZvaa 156,152,184\ns_lshl3_add_u32 s75, s12, s13\nMvaa 156,156,188\n"
"s_lshl2_add_u32 s75, s75, s11\nZvaa 152,148,188\ns_mul_i32 s75, s75, 7168\nMvaa 152,144,184\ns_mul_i32 s76, s11, 7168\nZvaa 92,136,184\ns_add_u32 s76, s76, 131072\n"
"Mvaa 92,140,188\nD0 5,20,45,3072\nZvaa 88,132,188\ns_mov_b32 s42, 1605632\nMvaa 88,128,184\ns_waitcnt vmcnt(24) lgkmcnt(0)\ns_barrier\nZvaa 96,128,224\nRa 192,10,16384\n"
"Mvaa 96,132,228\ns_add_u32 m0, s47, 0\ns_add_u32 s44, s44, 0x40000\nZvaa 100,140,228\nRa 196,11,16384\nMvaa 100,136,224\nZvaa 160,144,224\nRa 200,10,18432\nMvaa 160,148,228\n"
"D0 6,24,44,0\nZvaa 164,156,228\nRa 204,11,18432\nMvaa 164,152,224\nZvaa 172,152,232\nRa 208,10,24576\nMvaa 172,156,236\ns_and_b32 s78, s74, 3\ns_lshl2_add_u32 s78, s73, s78\n"
"s_lshl_b32 s78, s78, 12\ns_bitcmp1_b32 s11, 0\ns_cselect_b32 s79, s48, s45\ns_add_u32 s79, s79, 512\ns_and_b32 s79, s79, 0xfffe0000\ns_add_u32 s78, s78, s79\n"
"buffer_load_dword v31, v26, s[20:23], s78 offen\nZvaa 168,148,236\nRa 212,11,24576\nMvaa 168,144,232\nZvaa 108,136,232\nRa 216,10,26624\nMvaa 108,140,236\nD0 7,24,44,1024\n"
"Zvaa 104,132,236\nRa 220,11,26624\nMvaa 104,128,232\nZvaa 112,128,240\nMvaa 112,132,244\nZvaa 116,140,244\nMvaa 116,136,240\nZvaa 176,144,240\nMvaa 176,148,244\nD0 8,24,44,2048\n"
"Zvaa 180,156,244\nMvaa 180,152,240\nZvaa 188,152,248\nMvaa 188,156,252\nZvaa 184,148,252\nMvaa 184,144,248\nZvaa 124,136,248\nMvaa 124,140,252\nD0 9,24,44,3072\nZvaa 120,132,252\n"
"Mvaa 120,128,248\nWL 0\nZaaa 32,192,224\nRa 128,10,32768\nMaaa 32,196,228\ns_add_u32 m0, s47, 16384\ns_add_u32 s44, s44, 0x40000\nZaaa 36,204,228\nRa 132,11,32768\n"
"Maaa 36,200,224\nZaaa 96,208,224\nRa 136,10,34816\nMaaa 96,212,228\nD0 6,24,44,0\nZaaa 100,220,228\nRa 140,11,34816\nMaaa 100,216,224\nZaaa 108,216,232\nRa 144,10,40960\n"
"Maaa 108,220,236\nZaaa 104,212,236\nRa 148,11,40960\nMaaa 104,208,232\nZaaa 44,200,232\nRa 152,10,43008\nMaaa 44,204,236\nD0 7,24,44,1024\nZaaa 40,196,236\nRa 156,11,43008\n"
"Maaa 40,192,232\nZaaa 48,192,240\nMaaa 48,196,244\nZaaa 52,204,244\nMaaa 52,200,240\nZaaa 112,208,240\nMaaa 112,212,244\nD0 8,24,44,2048\nZaaa 116,220,244\nMaaa 116,216,240\n"
"Zaaa 124,216,248\nMaaa 124,220,252\nZaaa 120,212,252\nMaaa 120,208,248\nZaaa 60,200,248\nMaaa 60,204,252\nD0 9,24,44,3072\nZaaa 56,196,252\nMaaa 56,192,248\n"
"s_waitcnt vmcnt(25) lgkmcnt(0)\ns_barrier\nZaaa 0,192,160\nTa 224,16,32768\nWV 17\nMaaa 0,196,164\nTa 226,16,34816\nZaaa 4,204,164\ns_add_u32 m0, s47, -49152\n"
"s_add_u32 s48, s48, 128\nMaaa 4,200,160\nTa 228,16,49152\nZaaa 64,208,160\nTa 230,16,51200\nbuffer_load_dword v31, v24, s[24:27], s96 offen\ns_add_u32 s96, s96, 0x80000\n"
"Maaa 64,212,164\nD0 2,20,48,0\nv_lshlrev_b32 v48, 16, v32\nZaaa 68,220,164\nTa 232,17,32768\nMaaa 68,216,160\nTa 234,17,34816\nv_lshlrev_b32 v49, 16, v36\nZaaa 76,216,168\n"
"v_lshlrev_b32 v50, 16, v40\nv_lshlrev_b32 v51, 16, v44\nMaaa 76,220,172\nTa 236,17,49152\nZaaa 72,212,172\nTa 238,17,51200\nMU 52,48,50\nMaaa 72,208,168\nMU 53,49,50\n"
"Zaaa 12,200,168\nTa 240,18,32768\nCV 52,52,53\nMaaa 12,204,172\nTa 242,18,34816\nv_lshlrev_b32 v54, 16, v52\nZaaa 8,196,172\nD0 3,20,48,1024\nMaaa 8,192,168\nTa 244,18,49152\n"
"v_and_b32 v55, 0xffff0000, v52\nZaaa 16,192,176\nTa 246,18,51200\nMU 56,48,51\nMaaa 16,196,180\nMU 57,49,51\nZaaa 20,204,180\nTa 248,19,32768\nv_and_b32 v48, 0xffff0000, v32\n"
"Maaa 20,200,176\nTa 250,19,34816\nZaaa 80,208,176\nv_and_b32 v49, 0xffff0000, v36\nv_and_b32 v50, 0xffff0000, v40\nMaaa 80,212,180\nTa 252,19,49152\n"
"v_and_b32 v51, 0xffff0000, v44\nZaaa 84,220,180\nTa 254,19,51200\nMaaa 84,216,176\nD0 4,20,48,2048\nMU 52,48,50\nZaaa 92,216,184\nMU 53,49,50\nCV 52,52,53\nMaaa 92,220,188\n"
"v_and_b32 v53, 0xffff0000, v52\nZaaa 88,212,188\nv_lshlrev_b32 v52, 16, v52\nFM 56,48,51\nMaaa 88,208,184\nFM 57,49,51\nZaaa 28,200,184\nAD 54,54,52\nAD 55,55,53\nMaaa 28,204,188\n"
"D0 5,20,48,3072\nv_lshlrev_b32 v48, 16, v33\nZaaa 24,196,188\nv_lshlrev_b32 v49, 16, v37\nMaaa 24,192,184\nWL 0\nMvaa 96,128,224\nTa 160,12,32768\nv_lshlrev_b32 v50, 16, v41\n"
"Mvaa 96,132,228\nTa 162,12,34816\nMvaa 100,140,228\ns_add_u32 m0, s47, -32768\ns_add_u32 s45, s45, 128\nMvaa 100,136,224\nTa 164,12,49152\nMvaa 160,144,224\nTa 166,12,51200\n"
"v_lshlrev_b32 v51, 16, v45\nMvaa 160,148,228\nD0 2,20,45,0\nMU 52,48,50\nMvaa 164,156,228\nTa 168,13,32768\nMvaa 164,152,224\nTa 170,13,34816\nMU 53,49,50\nMvaa 172,152,232\n"
"CV 52,52,53\nv_and_b32 v53, 0xffff0000, v52\nMvaa 172,156,236\nTa 172,13,49152\nMvaa 168,148,236\nTa 174,13,51200\nv_lshlrev_b32 v52, 16, v52\nMvaa 168,144,232\nFM 56,48,51\n"
"Mvaa 108,136,232\nTa 176,14,32768\nFM 57,49,51\nMvaa 108,140,236\nTa 178,14,34816\nAD 54,54,52\nMvaa 104,132,236\nD0 3,20,45,1024\nMvaa 104,128,232\nTa 180,14,49152\nAD 55,55,53\n"
"Mvaa 112,128,240\nTa 182,14,51200\nv_and_b32 v48, 0xffff0000, v33\nMvaa 112,132,244\nv_and_b32 v49, 0xffff0000, v37\nMvaa 116,140,244\nTa 184,15,32768\n"
"v_and_b32 v50, 0xffff0000, v41\nMvaa 116,136,240\nTa 186,15,34816\nMvaa 176,144,240\nv_and_b32 v51, 0xffff0000, v45\nMU 52,48,50\nMvaa 176,148,244\nTa 188,15,49152\nMU 53,49,50\n"
"Mvaa 180,156,244\nTa 190,15,51200\nMvaa 180,152,240\nD0 4,20,45,2048\nCV 52,52,53\nMvaa 188,152,248\nv_and_b32 v53, 0xffff0000, v52\nv_lshlrev_b32 v52, 16, v52\nMvaa 188,156,252\n"
"FM 56,48,51\nMvaa 184,148,252\nFM 57,49,51\nAD 54,54,52\nMvaa 184,144,248\nAD 55,55,53\nMvaa 124,136,248\nv_lshlrev_b32 v48, 16, v34\nv_lshlrev_b32 v49, 16, v38\nMvaa 124,140,252\n"
"D0 5,20,45,3072\nv_lshlrev_b32 v50, 16, v42\nMvaa 120,132,252\nv_lshlrev_b32 v51, 16, v46\nMvaa 120,128,248\ns_waitcnt vmcnt(18) lgkmcnt(0)\ns_barrier\nMvaa 64,128,160\n"
"Ra 192,10,49152\nMU 52,48,50\nMvaa 64,132,164\ns_add_u32 m0, s47, 32768\ns_add_u32 s44, s44, 0x40000\nMvaa 68,140,164\nRa 196,11,49152\nMU 53,49,50\nMvaa 68,136,160\nCV 52,52,53\n"
"Mvaa 128,144,160\nRa 200,10,51200\nv_and_b32 v53, 0xffff0000, v52\nMvaa 128,148,164\nD0 6,24,44,0\nv_lshlrev_b32 v52, 16, v52\nMvaa 132,156,164\nRa 204,11,51200\nMvaa 132,152,160\n"
"FM 56,48,51\nFM 57,49,51\nMvaa 140,152,168\nRa 208,10,57344\nAD 54,54,52\nMvaa 140,156,172\nAD 55,55,53\nMvaa 136,148,172\nRa 212,11,57344\nv_and_b32 v48, 0xffff0000, v34\n"
"Mvaa 136,144,168\nv_and_b32 v49, 0xffff0000, v38\nMvaa 76,136,168\nRa 216,10,59392\nv_and_b32 v50, 0xffff0000, v42\nMvaa 76,140,172\nD0 7,24,44,1024\n"
"v_and_b32 v51, 0xffff0000, v46\nMvaa 72,132,172\nRa 220,11,59392\nMvaa 72,128,168\nMU 52,48,50\nMU 53,49,50\nMvaa 80,128,176\nCV 52,52,53\nv_and_b32 v53, 0xffff0000, v52\n"
"Mvaa 80,132,180\nv_lshlrev_b32 v52, 16, v52\nMvaa 84,140,180\nFM 56,48,51\nFM 57,49,51\nMvaa 84,136,176\nAD 54,54,52\nMvaa 144,144,176\nAD 55,55,53\nv_lshlrev_b32 v48, 16, v35\n"
"Mvaa 144,148,180\nD0 8,24,44,2048\nv_lshlrev_b32 v49, 16, v39\nMvaa 148,156,180\nv_lshlrev_b32 v50, 16, v43\nMvaa 148,152,176\nv_lshlrev_b32 v51, 16, v47\nMU 52,48,50\n"
"Mvaa 156,152,184\nMU 53,49,50\nCV 52,52,53\nMvaa 156,156,188\nv_and_b32 v53, 0xffff0000, v52\nMvaa 152,148,188\nv_lshlrev_b32 v52, 16, v52\nFM 56,48,51\nMvaa 152,144,184\n"
"FM 57,49,51\nMvaa 92,136,184\nAD 54,54,52\nAD 55,55,53\nMvaa 92,140,188\nD0 9,24,44,3072\nv_and_b32 v48, 0xffff0000, v35\nMvaa 88,132,188\nv_and_b32 v49, 0xffff0000, v39\n"
"Mvaa 88,128,184\nWL 0\nMaaa 0,192,160\nRa 128,10,0\nv_and_b32 v50, 0xffff0000, v43\nMaaa 0,196,164\ns_add_u32 m0, s47, 49152\ns_add_u32 s44, s44, 0x40000\nMaaa 4,204,164\n"
"Ra 132,11,0\nv_and_b32 v51, 0xffff0000, v47\nMaaa 4,200,160\nMU 52,48,50\nMaaa 64,208,160\nRa 136,10,2048\nMU 53,49,50\nMaaa 64,212,164\nD0 6,24,44,0\nCV 52,52,53\n"
"Maaa 68,220,164\nRa 140,11,2048\nMaaa 68,216,160\nv_and_b32 v53, 0xffff0000, v52\nv_lshlrev_b32 v52, 16, v52\nMaaa 76,216,168\nRa 144,10,8192\nFM 56,48,51\nMaaa 76,220,172\n"
"FM 57,49,51\nMaaa 72,212,172\nRa 148,11,8192\nAD 54,54,52\nMaaa 72,208,168\nAD 55,55,53\nMaaa 12,200,168\nRa 152,10,10240\nv_permlane32_swap_b32 v54, v55\nMaaa 12,204,172\n"
"D0 7,24,44,1024\nv_permlane32_swap_b32 v56, v57\nMaaa 8,196,172\nRa 156,11,10240\nMaaa 8,192,168\nAD 54,54,55\nAD 56,56,57\nMaaa 16,192,176\nMaaa 16,196,180\n"
"v_permlane16_swap_b32 v54, v56\nMaaa 20,204,180\nAD 54,54,56\nMaaa 20,200,176\nMaaa 80,208,176\nv_add_f32_dpp v54, v54, v54 row_ror:8 row_mask:0xf bank_mask:0xf\nMaaa 80,212,180\n"
"D0 8,24,44,2048\nMaaa 84,220,180\nv_add_f32_dpp v54, v54, v54 row_ror:4 row_mask:0xf bank_mask:0xf\nMaaa 84,216,176\nMaaa 92,216,184\n"
"v_add_f32_dpp v54, v54, v54 row_ror:2 row_mask:0xf bank_mask:0xf\nMaaa 92,220,188\nMaaa 88,212,188\nv_add_f32_dpp v54, v54, v54 row_ror:1 row_mask:0xf bank_mask:0xf\nCV 54,54,54\n"
"Maaa 88,208,184\nv_lshlrev_b32 v54, 16, v54\nMaaa 28,200,184\nv_mul_f32 v55, 0xbfb8aa3b, v54\nMaaa 28,204,188\nD0 9,24,44,3072\nMaaa 24,196,188\nEX 55,55\nMaaa 24,192,184\n"
"s_waitcnt vmcnt(12) lgkmcnt(0)\ns_barrier\nMaaa 32,192,224\nTa 160,12,0\nv_add_f32 v56, 0, v54\nMaaa 32,196,228\nTa 162,12,2048\nMaaa 36,204,228\ns_add_u32 m0, s47, -16384\n"
"s_add_u32 s48, s48, 128\nMaaa 36,200,224\nTa 164,12,16384\nMaaa 96,208,224\nTa 166,12,18432\nbuffer_load_dword v31, v24, s[24:27], s96 offen\ns_add_u32 s96, s96, 0x80000\n"
"Maaa 96,212,228\nD0 2,20,48,0\nv_add_f32 v55, 1.0, v55\nMaaa 100,220,228\nTa 168,13,0\nv_rcp_f32 v55, v55\nMaaa 100,216,224\nTa 170,13,2048\nv_ashrrev_i32 v57, 31, v56\n"
"Maaa 108,216,232\nCV 55,55,55\nv_lshlrev_b32 v55, 16, v55\nMaaa 108,220,236\nTa 172,13,16384\nMaaa 104,212,236\nTa 174,13,18432\nv_or_b32 v57, 0x80000000, v57\nMaaa 104,208,232\n"
"v_xor_b32 v56, v56, v57\nMaaa 44,200,232\nTa 176,14,0\nv_and_b32 v56, 0xffff0000, v56\nMaaa 44,204,236\nTa 178,14,2048\ns_lshl2_add_u32 s2, s10, s11\nMaaa 40,196,236\n"
"D0 3,20,48,1024\nMaaa 40,192,232\nTa 180,14,16384\nv_lshrrev_b32 v48, 5, v0\nMaaa 48,192,240\nTa 182,14,18432\nv_lshl_add_u32 v48, s2, 1, v48\nMaaa 48,196,244\n"
"v_sub_u32 v49, 0xffff, v48\nMaaa 52,204,244\nTa 184,15,0\nv_or_b32 v56, v56, v49\nMaaa 52,200,240\nTa 186,15,2048\nMaaa 112,208,240\nv_mov_b32 v57, s15\nv_and_b32 v49, 31, v0\n"
"Maaa 112,212,244\nTa 188,15,16384\nv_mov_b32 v52, 0x40000000\nMaaa 116,220,244\nTa 190,15,18432\nv_cmp_eq_u32 vcc, 16, v49\nv_lshlrev_b32 v50, 2, v48\nv_lshlrev_b32 v51, 10, v49\n"
"v_cndmask_b32 v50, v52, v50, vcc\nv_cndmask_b32 v56, v55, v56, vcc\nMaaa 116,216,240\nD0 4,20,48,2048\nv_lshl_add_u32 v51, v48, 3, v51\nMaaa 124,216,248\nv_and_b32 v49, 15, v0\n"
"v_cmp_eq_u32 vcc, 0, v49\ns_mov_b32 s2, 0xc040000\ns_mov_b32 s3, 0xc03e000\nv_cndmask_b32 v51, v52, v51, vcc\nbuffer_store_dwordx2 v[56:57], v51, s[16:19], s2 offen sc0 sc1\n"
"buffer_store_dword v54, v50, s[16:19], s3 offen sc0 sc1\nMaaa 124,220,252\nMaaa 120,212,252\nMaaa 120,208,248\nMaaa 60,200,248\nMaaa 60,204,252\nD0 5,20,48,3072\nMaaa 56,196,252\n"
"Maaa 56,192,248\nWL 0\ns_mov_b32 s50, 3\n.GL409:\nMvaa 64,128,160\nTa 224,16,0\nMvaa 64,132,164\nTa 226,16,2048\nMvaa 68,140,164\nTa 228,16,16384\ns_add_u32 m0, s47, -65536\n"
"s_add_u32 s45, s45, 128\nMvaa 68,136,160\nTa 230,16,18432\nMvaa 128,144,160\nTa 232,17,0\nMvaa 128,148,164\nTa 234,17,2048\nD0 2,20,45,0\nMvaa 132,156,164\nTa 236,17,16384\n"
"Mvaa 132,152,160\nTa 238,17,18432\nMvaa 140,152,168\nTa 240,18,0\nMvaa 140,156,172\nTa 242,18,2048\nMvaa 136,148,172\nTa 244,18,16384\nMvaa 136,144,168\nTa 246,18,18432\n"
"Mvaa 76,136,168\nTa 248,19,0\nMvaa 76,140,172\nTa 250,19,2048\nMvaa 72,132,172\nTa 252,19,16384\nD0 3,20,45,1024\nMvaa 72,128,168\nTa 254,19,18432\nMvaa 80,128,176\n"
"Ra 192,10,16384\nMvaa 80,132,180\nRa 196,11,16384\nMvaa 84,140,180\nRa 200,10,18432\nMvaa 84,136,176\nRa 204,11,18432\nMvaa 144,144,176\nRa 208,10,24576\nMvaa 144,148,180\n"
"Ra 212,11,24576\nMvaa 148,156,180\nRa 216,10,26624\nMvaa 148,152,176\nRa 220,11,26624\nD0 4,20,45,2048\nMvaa 156,152,184\nMvaa 156,156,188\nMvaa 152,148,188\nMvaa 152,144,184\n"
"Mvaa 92,136,184\nMvaa 92,140,188\nD0 5,20,45,3072\nMvaa 88,132,188\nMvaa 88,128,184\ns_waitcnt vmcnt(18) lgkmcnt(8)\ns_barrier\nMvaa 96,128,224\nMvaa 96,132,228\n"
"s_add_u32 m0, s47, 0\ns_add_u32 s44, s44, 0x40000\ns_mov_b32 s86, 1\ns_load_dword s86, s[54:55], s94\nMvaa 100,140,228\ns_add_u32 s94, s94, 1024\nMvaa 100,136,224\n"
"Mvaa 160,144,224\nMvaa 160,148,228\nD0 6,24,44,0\nMvaa 164,156,228\nMvaa 164,152,224\nMvaa 172,152,232\nMvaa 172,156,236\nMvaa 168,148,236\nMvaa 168,144,232\n"
"s_add_u32 s88, s88, s86\nMvaa 108,136,232\nMvaa 108,140,236\nD0 7,24,44,1024\ns_add_u32 s88, s88, s86\nMvaa 104,132,236\nMvaa 104,128,232\ns_add_u32 s88, s88, s86\n"
"Mvaa 112,128,240\nMvaa 112,132,244\ns_add_u32 s88, s88, s86\nMvaa 116,140,244\nMvaa 116,136,240\ns_add_u32 s88, s88, s86\nMvaa 176,144,240\nMvaa 176,148,244\nD0 8,24,44,2048\n"
"s_add_u32 s88, s88, s86\nMvaa 180,156,244\nMvaa 180,152,240\ns_add_u32 s88, s88, s86\nMvaa 188,152,248\nMvaa 188,156,252\ns_add_u32 s88, s88, s86\nMvaa 184,148,252\n"
"Mvaa 184,144,248\ns_add_u32 s88, s88, s86\nMvaa 124,136,248\nMvaa 124,140,252\nD0 9,24,44,3072\ns_add_u32 s88, s88, s86\nMvaa 120,132,252\nMvaa 120,128,248\nWL 0\n"
"s_add_u32 s88, s88, s86\nMaaa 32,192,224\nRa 128,10,32768\nMaaa 32,196,228\ns_add_u32 m0, s47, 16384\ns_add_u32 s44, s44, 0x40000\ns_add_u32 s88, s88, s86\nMaaa 36,204,228\n"
"Ra 132,11,32768\nMaaa 36,200,224\ns_add_u32 s88, s88, s86\nMaaa 96,208,224\nRa 136,10,34816\nMaaa 96,212,228\nD0 6,24,44,0\ns_add_u32 s88, s88, s86\nMaaa 100,220,228\n"
"Ra 140,11,34816\nMaaa 100,216,224\ns_add_u32 s88, s88, s86\nMaaa 108,216,232\nRa 144,10,40960\nMaaa 108,220,236\ns_add_u32 s88, s88, s86\nMaaa 104,212,236\nRa 148,11,40960\n"
"Maaa 104,208,232\nMaaa 44,200,232\nRa 152,10,43008\nMaaa 44,204,236\nD0 7,24,44,1024\nMaaa 40,196,236\nRa 156,11,43008\nMaaa 40,192,232\nMaaa 48,192,240\nMaaa 48,196,244\n"
"Maaa 52,204,244\nMaaa 52,200,240\nMaaa 112,208,240\nMaaa 112,212,244\nD0 8,24,44,2048\nMaaa 116,220,244\nMaaa 116,216,240\nMaaa 124,216,248\nMaaa 124,220,252\nMaaa 120,212,252\n"
"Maaa 120,208,248\nMaaa 60,200,248\nMaaa 60,204,252\nD0 9,24,44,3072\nMaaa 56,196,252\nMaaa 56,192,248\ns_waitcnt vmcnt(12) lgkmcnt(0)\ns_barrier\nMaaa 0,192,160\nTa 224,16,32768\n"
"s_mov_b32 m0, s76\ns_add_u32 s76, s76, 1024\nMaaa 0,196,164\nTa 226,16,34816\nD0 1,36,75,0\ns_add_u32 s75, s75, 1024\nMaaa 4,204,164\ns_add_u32 m0, s47, -49152\n"
"s_add_u32 s48, s48, 128\nMaaa 4,200,160\nTa 228,16,49152\nMaaa 64,208,160\nTa 230,16,51200\nbuffer_load_dword v31, v24, s[24:27], s96 offen\ns_add_u32 s96, s96, 0x80000\n"
"Maaa 64,212,164\nD0 2,20,48,0\nMaaa 68,220,164\nTa 232,17,32768\nMaaa 68,216,160\nTa 234,17,34816\nMaaa 76,216,168\nMaaa 76,220,172\nTa 236,17,49152\nMaaa 72,212,172\n"
"Ta 238,17,51200\nMaaa 72,208,168\nMaaa 12,200,168\nTa 240,18,32768\nMaaa 12,204,172\nTa 242,18,34816\nMaaa 8,196,172\nD0 3,20,48,1024\nMaaa 8,192,168\nTa 244,18,49152\n"
"Maaa 16,192,176\nTa 246,18,51200\nMaaa 16,196,180\nMaaa 20,204,180\nTa 248,19,32768\nMaaa 20,200,176\nTa 250,19,34816\nMaaa 80,208,176\nMaaa 80,212,180\nTa 252,19,49152\n"
"Maaa 84,220,180\nTa 254,19,51200\nMaaa 84,216,176\nD0 4,20,48,2048\nMaaa 92,216,184\nMaaa 92,220,188\nMaaa 88,212,188\nMaaa 88,208,184\nMaaa 28,200,184\nMaaa 28,204,188\n"
"D0 5,20,48,3072\nMaaa 24,196,188\nMaaa 24,192,184\nWL 0\nMvaa 96,128,224\nTa 160,12,32768\nMvaa 96,132,228\nTa 162,12,34816\nMvaa 100,140,228\nTa 164,12,49152\n"
"s_add_u32 m0, s47, -32768\ns_add_u32 s45, s45, 128\nMvaa 100,136,224\nTa 166,12,51200\nMvaa 160,144,224\nTa 168,13,32768\nMvaa 160,148,228\nTa 170,13,34816\nD0 2,20,45,0\n"
"Mvaa 164,156,228\nTa 172,13,49152\nMvaa 164,152,224\nTa 174,13,51200\nMvaa 172,152,232\nTa 176,14,32768\nMvaa 172,156,236\nTa 178,14,34816\nMvaa 168,148,236\nTa 180,14,49152\n"
"Mvaa 168,144,232\nTa 182,14,51200\nMvaa 108,136,232\nTa 184,15,32768\nMvaa 108,140,236\nTa 186,15,34816\nMvaa 104,132,236\nTa 188,15,49152\nD0 3,20,45,1024\nMvaa 104,128,232\n"
"Ta 190,15,51200\nMvaa 112,128,240\nRa 192,10,49152\nMvaa 112,132,244\nRa 196,11,49152\nMvaa 116,140,244\nRa 200,10,51200\nMvaa 116,136,240\nRa 204,11,51200\nMvaa 176,144,240\n"
"Ra 208,10,57344\nMvaa 176,148,244\nRa 212,11,57344\nMvaa 180,156,244\nRa 216,10,59392\nMvaa 180,152,240\nRa 220,11,59392\nD0 4,20,45,2048\nMvaa 188,152,248\nMvaa 188,156,252\n"
"Mvaa 184,148,252\nMvaa 184,144,248\nMvaa 124,136,248\nMvaa 124,140,252\nD0 5,20,45,3072\nMvaa 120,132,252\nMvaa 120,128,248\ns_waitcnt vmcnt(18) lgkmcnt(8)\ns_barrier\n"
"Mvaa 64,128,160\nMvaa 64,132,164\ns_add_u32 m0, s47, 32768\ns_add_u32 s44, s44, 0x40000\ns_mov_b32 s86, 1\ns_load_dword s86, s[54:55], s95\nMvaa 68,140,164\n"
"s_add_u32 s95, s95, 1024\nMvaa 68,136,160\nMvaa 128,144,160\nMvaa 128,148,164\nD0 6,24,44,0\nMvaa 132,156,164\nMvaa 132,152,160\nMvaa 140,152,168\nMvaa 140,156,172\n"
"Mvaa 136,148,172\nMvaa 136,144,168\ns_sub_u32 s88, s88, s86\nMvaa 76,136,168\nMvaa 76,140,172\nD0 7,24,44,1024\ns_sub_u32 s88, s88, s86\nMvaa 72,132,172\nMvaa 72,128,168\n"
"s_sub_u32 s88, s88, s86\nMvaa 80,128,176\nMvaa 80,132,180\ns_sub_u32 s88, s88, s86\nMvaa 84,140,180\nMvaa 84,136,176\ns_sub_u32 s88, s88, s86\nMvaa 144,144,176\nMvaa 144,148,180\n"
"D0 8,24,44,2048\ns_sub_u32 s88, s88, s86\nMvaa 148,156,180\nMvaa 148,152,176\ns_sub_u32 s88, s88, s86\nMvaa 156,152,184\nMvaa 156,156,188\ns_sub_u32 s88, s88, s86\n"
"Mvaa 152,148,188\nMvaa 152,144,184\ns_sub_u32 s88, s88, s86\nMvaa 92,136,184\nMvaa 92,140,188\nD0 9,24,44,3072\ns_sub_u32 s88, s88, s86\nMvaa 88,132,188\nMvaa 88,128,184\nWL 0\n"
"s_sub_u32 s88, s88, s86\nMaaa 0,192,160\nRa 128,10,0\nMaaa 0,196,164\ns_add_u32 m0, s47, 49152\ns_add_u32 s44, s44, 0x40000\ns_sub_u32 s88, s88, s86\nMaaa 4,204,164\nRa 132,11,0\n"
"Maaa 4,200,160\ns_sub_u32 s88, s88, s86\nMaaa 64,208,160\nRa 136,10,2048\nMaaa 64,212,164\nD0 6,24,44,0\ns_sub_u32 s88, s88, s86\nMaaa 68,220,164\nRa 140,11,2048\nMaaa 68,216,160\n"
"s_sub_u32 s88, s88, s86\nMaaa 76,216,168\nRa 144,10,8192\nMaaa 76,220,172\ns_sub_u32 s88, s88, s86\nMaaa 72,212,172\nRa 148,11,8192\nMaaa 72,208,168\nMaaa 12,200,168\n"
"Ra 152,10,10240\nMaaa 12,204,172\nD0 7,24,44,1024\nMaaa 8,196,172\nRa 156,11,10240\nMaaa 8,192,168\nMaaa 16,192,176\nMaaa 16,196,180\nMaaa 20,204,180\nMaaa 20,200,176\n"
"Maaa 80,208,176\nMaaa 80,212,180\nD0 8,24,44,2048\nMaaa 84,220,180\nMaaa 84,216,176\nMaaa 92,216,184\nMaaa 92,220,188\nMaaa 88,212,188\nMaaa 88,208,184\nMaaa 28,200,184\n"
"Maaa 28,204,188\nD0 9,24,44,3072\nMaaa 24,196,188\nMaaa 24,192,184\ns_waitcnt vmcnt(12) lgkmcnt(0)\ns_barrier\nMaaa 32,192,224\nTa 160,12,0\ns_mov_b32 m0, s76\n"
"s_add_u32 s76, s76, 1024\nMaaa 32,196,228\nTa 162,12,2048\nD0 1,36,75,0\ns_add_u32 s75, s75, 1024\nMaaa 36,204,228\ns_add_u32 m0, s47, -16384\ns_add_u32 s48, s48, 128\n"
"Maaa 36,200,224\nTa 164,12,16384\nMaaa 96,208,224\nTa 166,12,18432\nbuffer_load_dword v31, v24, s[24:27], s96 offen\ns_add_u32 s96, s96, 0x80000\nMaaa 96,212,228\nD0 2,20,48,0\n"
"Maaa 100,220,228\nTa 168,13,0\nMaaa 100,216,224\nTa 170,13,2048\nMaaa 108,216,232\nMaaa 108,220,236\nTa 172,13,16384\nMaaa 104,212,236\nTa 174,13,18432\nMaaa 104,208,232\n"
"Maaa 44,200,232\nTa 176,14,0\nMaaa 44,204,236\nTa 178,14,2048\nMaaa 40,196,236\nD0 3,20,48,1024\nMaaa 40,192,232\nTa 180,14,16384\nMaaa 48,192,240\nTa 182,14,18432\n"
"Maaa 48,196,244\nMaaa 52,204,244\nTa 184,15,0\nMaaa 52,200,240\nTa 186,15,2048\nMaaa 112,208,240\nMaaa 112,212,244\nTa 188,15,16384\nMaaa 116,220,244\nTa 190,15,18432\n"
"Maaa 116,216,240\nD0 4,20,48,2048\nMaaa 124,216,248\nMaaa 124,220,252\nMaaa 120,212,252\nMaaa 120,208,248\nMaaa 60,200,248\ns_xor_b32 s94, s94, 0x2000\ns_xor_b32 s95, s95, 0x2000\n"
"s_sub_u32 s88, 0, s88\nMaaa 60,204,252\nD0 5,20,48,3072\ns_sub_u32 s50, s50, 1\nMaaa 56,196,252\nMaaa 56,192,248\nWL 0\ns_cbranch_scc0 .GL409\nMvaa 64,128,160\nTa 224,16,0\n"
"Mvaa 64,132,164\nTa 226,16,2048\ns_cmp_gt_i32 s88, 0\ns_cselect_b32 s88, 0x2000, 0\nMvaa 68,140,164\ns_add_u32 m0, s47, -65536\ns_add_u32 s45, s45, 128\nMvaa 68,136,160\n"
"Ta 228,16,16384\nMvaa 128,144,160\nTa 230,16,18432\nMvaa 128,148,164\nD0 2,20,45,0\nMvaa 132,156,164\nTa 232,17,0\nMvaa 132,152,160\nTa 234,17,2048\nMvaa 140,152,168\n"
"s_xor_b32 s88, s88, s92\nMvaa 140,156,172\nTa 236,17,16384\nMvaa 136,148,172\nTa 238,17,18432\nMvaa 136,144,168\nv_xor_b32 v27, s88, v25\nMvaa 76,136,168\nTa 240,18,0\n"
"Mvaa 76,140,172\nTa 242,18,2048\nMvaa 72,132,172\nD0 3,20,45,1024\nMvaa 72,128,168\nTa 244,18,16384\nMvaa 80,128,176\nTa 246,18,18432\nMvaa 80,132,180\n"
"buffer_store_dword v27, off, s[16:19], s93 sc0 sc1\nMvaa 84,140,180\nTa 248,19,0\nMvaa 84,136,176\nTa 250,19,2048\nMvaa 144,144,176\nMvaa 144,148,180\nTa 252,19,16384\n"
"Mvaa 148,156,180\nTa 254,19,18432\nMvaa 148,152,176\nD0 4,20,45,2048\nMvaa 156,152,184\nMvaa 156,156,188\nMvaa 152,148,188\nMvaa 152,144,184\nMvaa 92,136,184\nMvaa 92,140,188\n"
"D0 5,20,45,3072\nMvaa 88,132,188\nMvaa 88,128,184\ns_waitcnt vmcnt(19) lgkmcnt(0)\ns_barrier\nMvaa 96,128,224\nRa 192,10,16384\nMvaa 96,132,228\ns_add_u32 m0, s47, 0\n"
"s_add_u32 s44, s44, 0x40000\nMvaa 100,140,228\nRa 196,11,16384\nMvaa 100,136,224\nMvaa 160,144,224\nRa 200,10,18432\nMvaa 160,148,228\nD0 6,24,44,0\nMvaa 164,156,228\n"
"Ra 204,11,18432\nMvaa 164,152,224\nMvaa 172,152,232\nRa 208,10,24576\nMvaa 172,156,236\nMvaa 168,148,236\nRa 212,11,24576\nMvaa 168,144,232\nMvaa 108,136,232\nRa 216,10,26624\n"
"Mvaa 108,140,236\nD0 7,24,44,1024\nMvaa 104,132,236\nRa 220,11,26624\nMvaa 104,128,232\nMvaa 112,128,240\nMvaa 112,132,244\nMvaa 116,140,244\nMvaa 116,136,240\nMvaa 176,144,240\n"
"Mvaa 176,148,244\nD0 8,24,44,2048\nMvaa 180,156,244\nMvaa 180,152,240\nMvaa 188,152,248\nMvaa 188,156,252\nMvaa 184,148,252\nMvaa 184,144,248\nMvaa 124,136,248\nMvaa 124,140,252\n"
"D0 9,24,44,3072\nMvaa 120,132,252\nMvaa 120,128,248\nWL 0\nMaaa 32,192,224\nRa 128,10,32768\nMaaa 32,196,228\ns_add_u32 m0, s47, 16384\ns_add_u32 s44, s44, 0x40000\n"
"Maaa 36,204,228\nRa 132,11,32768\nMaaa 36,200,224\nMaaa 96,208,224\nRa 136,10,34816\nMaaa 96,212,228\nD0 6,24,44,0\nMaaa 100,220,228\nRa 140,11,34816\nMaaa 100,216,224\n"
"Maaa 108,216,232\nRa 144,10,40960\nMaaa 108,220,236\nMaaa 104,212,236\nRa 148,11,40960\nMaaa 104,208,232\nMaaa 44,200,232\nRa 152,10,43008\nMaaa 44,204,236\nD0 7,24,44,1024\n"
"Maaa 40,196,236\nRa 156,11,43008\nMaaa 40,192,232\nMaaa 48,192,240\nMaaa 48,196,244\nMaaa 52,204,244\nMaaa 52,200,240\nMaaa 112,208,240\nMaaa 112,212,244\nD0 8,24,44,2048\n"
"Maaa 116,220,244\nMaaa 116,216,240\nMaaa 124,216,248\nMaaa 124,220,252\nMaaa 120,212,252\nMaaa 120,208,248\nMaaa 60,200,248\nMaaa 60,204,252\nD0 9,24,44,3072\nMaaa 56,196,252\n"
"Maaa 56,192,248\ns_waitcnt vmcnt(19) lgkmcnt(0)\ns_barrier\nMaaa 0,192,160\nTa 224,16,32768\nMaaa 0,196,164\nTa 226,16,34816\nMaaa 4,204,164\ns_add_u32 m0, s47, -49152\n"
"s_add_u32 s48, s48, 128\nMaaa 4,200,160\nTa 228,16,49152\nMaaa 64,208,160\nTa 230,16,51200\nbuffer_load_dword v31, v24, s[24:27], s96 offen\ns_add_u32 s96, s96, 0x80000\n"
"Maaa 64,212,164\nD0 2,20,48,0\nMaaa 68,220,164\nTa 232,17,32768\nMaaa 68,216,160\nTa 234,17,34816\nMaaa 76,216,168\nMaaa 76,220,172\nTa 236,17,49152\nMaaa 72,212,172\n"
"Ta 238,17,51200\nMaaa 72,208,168\nMaaa 12,200,168\nTa 240,18,32768\nMaaa 12,204,172\nTa 242,18,34816\nMaaa 8,196,172\nD0 3,20,48,1024\nMaaa 8,192,168\nTa 244,18,49152\n"
"Maaa 16,192,176\nTa 246,18,51200\nMaaa 16,196,180\nMaaa 20,204,180\nTa 248,19,32768\nMaaa 20,200,176\nTa 250,19,34816\nMaaa 80,208,176\nMaaa 80,212,180\nTa 252,19,49152\n"
"Maaa 84,220,180\nTa 254,19,51200\nMaaa 84,216,176\nD0 4,20,48,2048\nMaaa 92,216,184\nMaaa 92,220,188\nMaaa 88,212,188\nMaaa 88,208,184\nMaaa 28,200,184\nMaaa 28,204,188\n"
"D0 5,20,48,3072\nMaaa 24,196,188\nMaaa 24,192,184\nWL 0\nMvaa 96,128,224\nTa 160,12,32768\nMvaa 96,132,228\nTa 162,12,34816\nMvaa 100,140,228\ns_add_u32 m0, s47, -32768\n"
"s_add_u32 s45, s45, 128\nMvaa 100,136,224\nTa 164,12,49152\nMvaa 160,144,224\nTa 166,12,51200\nMvaa 160,148,228\nD0 2,20,45,0\nMvaa 164,156,228\nTa 168,13,32768\nMvaa 164,152,224\n"
"Ta 170,13,34816\nMvaa 172,152,232\nMvaa 172,156,236\nTa 172,13,49152\nMvaa 168,148,236\nTa 174,13,51200\nMvaa 168,144,232\nMvaa 108,136,232\nTa 176,14,32768\nMvaa 108,140,236\n"
"Ta 178,14,34816\nMvaa 104,132,236\nD0 3,20,45,1024\nMvaa 104,128,232\nTa 180,14,49152\nMvaa 112,128,240\nTa 182,14,51200\nMvaa 112,132,244\nMvaa 116,140,244\nTa 184,15,32768\n"
"Mvaa 116,136,240\nTa 186,15,34816\nMvaa 176,144,240\nMvaa 176,148,244\nTa 188,15,49152\nMvaa 180,156,244\nTa 190,15,51200\nMvaa 180,152,240\nD0 4,20,45,2048\nMvaa 188,152,248\n"
"Mvaa 188,156,252\nMvaa 184,148,252\nMvaa 184,144,248\nMvaa 124,136,248\nMvaa 124,140,252\nD0 5,20,45,3072\nMvaa 120,132,252\nMvaa 120,128,248\ns_waitcnt vmcnt(17) lgkmcnt(0)\n"
"s_barrier\nMvaa 64,128,160\nRa 192,10,49152\nMvaa 64,132,164\ns_add_u32 m0, s47, 32768\ns_add_u32 s44, s44, 0x40000\nMvaa 68,140,164\nRa 196,11,49152\nMvaa 68,136,160\n"
"Mvaa 128,144,160\nRa 200,10,51200\nMvaa 128,148,164\nD0 6,24,44,0\nMvaa 132,156,164\nRa 204,11,51200\nMvaa 132,152,160\nMvaa 140,152,168\nRa 208,10,57344\nMvaa 140,156,172\n"
"s_lshl_b32 s5, s73, 8\ns_add_u32 s5, s5, 0xc04ac00\nv_lshrrev_b32 v27, 2, v1\nbuffer_load_dword v27, v27, s[16:19], s5 offen sc0 sc1\nMvaa 136,148,172\nRa 212,11,57344\n"
"Mvaa 136,144,168\nMvaa 76,136,168\nRa 216,10,59392\nMvaa 76,140,172\nD0 7,24,44,1024\nMvaa 72,132,172\nRa 220,11,59392\nMvaa 72,128,168\nMvaa 80,128,176\nMvaa 80,132,180\n"
"Mvaa 84,140,180\nMvaa 84,136,176\nMvaa 144,144,176\nMvaa 144,148,180\nD0 8,24,44,2048\nMvaa 148,156,180\nMvaa 148,152,176\nMvaa 156,152,184\nMvaa 156,156,188\nMvaa 152,148,188\n"
"Mvaa 152,144,184\nMvaa 92,136,184\nMvaa 92,140,188\nD0 9,24,44,3072\nMvaa 88,132,188\nMvaa 88,128,184\nWL 0\nMaaa 0,192,160\nRa 128,10,0\nMaaa 0,196,164\ns_add_u32 m0, s47, 49152\n"
"s_add_u32 s44, s44, 0x40000\nMaaa 4,204,164\nRa 132,11,0\nMaaa 4,200,160\nMaaa 64,208,160\nRa 136,10,2048\nMaaa 64,212,164\nD0 6,24,44,0\nMaaa 68,220,164\nRa 140,11,2048\n"
"Maaa 68,216,160\nMaaa 76,216,168\nRa 144,10,8192\nMaaa 76,220,172\nMaaa 72,212,172\nRa 148,11,8192\nMaaa 72,208,168\nMaaa 12,200,168\nRa 152,10,10240\nMaaa 12,204,172\n"
"D0 7,24,44,1024\nMaaa 8,196,172\nRa 156,11,10240\nMaaa 8,192,168\nMaaa 16,192,176\nMaaa 16,196,180\nMaaa 20,204,180\nMaaa 20,200,176\nMaaa 80,208,176\nMaaa 80,212,180\n"
"D0 8,24,44,2048\nMaaa 84,220,180\nMaaa 84,216,176\nMaaa 92,216,184\nMaaa 92,220,188\nMaaa 88,212,188\nMaaa 88,208,184\nMaaa 28,200,184\nMaaa 28,204,188\nD0 9,24,44,3072\n"
"Maaa 24,196,188\nMaaa 24,192,184\ns_waitcnt vmcnt(18) lgkmcnt(0)\ns_barrier\nMaaa 32,192,224\nTa 160,12,0\nMaaa 32,196,228\nTa 162,12,2048\nMaaa 36,204,228\n"
"s_add_u32 m0, s47, -16384\ns_add_u32 s48, s48, 128\nMaaa 36,200,224\nTa 164,12,16384\nMaaa 96,208,224\nTa 166,12,18432\nbuffer_load_dword v31, v24, s[24:27], s96 offen\n"
"s_add_u32 s96, s96, 0x80000\nMaaa 96,212,228\nD0 2,20,48,0\nMaaa 100,220,228\nTa 168,13,0\nMaaa 100,216,224\nTa 170,13,2048\nMaaa 108,216,232\nMaaa 108,220,236\nTa 172,13,16384\n"
"Maaa 104,212,236\nTa 174,13,18432\nMaaa 104,208,232\nMaaa 44,200,232\nTa 176,14,0\nMaaa 44,204,236\nTa 178,14,2048\nMaaa 40,196,236\nD0 3,20,48,1024\nMaaa 40,192,232\n"
"Ta 180,14,16384\nMaaa 48,192,240\nTa 182,14,18432\nMaaa 48,196,244\nMaaa 52,204,244\nTa 184,15,0\nMaaa 52,200,240\nTa 186,15,2048\nMaaa 112,208,240\nMaaa 112,212,244\n"
"Ta 188,15,16384\nMaaa 116,220,244\nTa 190,15,18432\nMaaa 116,216,240\nD0 4,20,48,2048\nMaaa 124,216,248\nMaaa 124,220,252\nMaaa 120,212,252\nMaaa 120,208,248\nMaaa 60,200,248\n"
"Maaa 60,204,252\nD0 5,20,48,3072\nMaaa 56,196,252\nMaaa 56,192,248\nWL 0\nMvaa 64,128,160\nTa 224,16,0\nWV 12\nv_xor_b32 v27, s15, v27\nv_and_b32 v32, 0xffffdfff, v27\n"
"v_cmp_ne_u32 vcc, 0, v32\ns_nop 0\ns_cbranch_vccnz .S406\n.S407:\nv_cmp_ne_u32 vcc, 0, v27\ns_nop 3\ns_bcnt1_i32_b64 s4, vcc\ns_cmp_gt_u32 s4, 32\ns_cselect_b32 s4, 1, 0\n"
"Mvaa 64,132,164\nTa 226,16,2048\nMvaa 68,140,164\ns_add_u32 m0, s47, -65536\ns_add_u32 s45, s45, 128\nMvaa 68,136,160\nTa 228,16,16384\nMvaa 128,144,160\nTa 230,16,18432\n"
"Mvaa 128,148,164\nD0 2,20,45,0\nMvaa 132,156,164\nTa 232,17,0\nMvaa 132,152,160\nTa 234,17,2048\nMvaa 140,152,168\ns_xor_b32 s4, s4, s12\nMvaa 140,156,172\nTa 236,17,16384\n"
"Mvaa 136,148,172\nTa 238,17,18432\nMvaa 136,144,168\ns_and_b32 s4, s4, 1\nMvaa 76,136,168\nTa 240,18,0\nMvaa 76,140,172\nTa 242,18,2048\nMvaa 72,132,172\nD0 3,20,45,1024\n"
"Mvaa 72,128,168\nTa 244,18,16384\nMvaa 80,128,176\nTa 246,18,18432\nMvaa 80,132,180\ns_lshl_b32 s4, s4, 13\nMvaa 84,140,180\nTa 248,19,0\nMvaa 84,136,176\nTa 250,19,2048\n"
"Mvaa 144,144,176\ns_mul_i32 s72, s73, 25165824\nMvaa 144,148,180\nTa 252,19,16384\nMvaa 148,156,180\nTa 254,19,18432\nMvaa 148,152,176\nD0 4,20,45,2048\nMvaa 156,152,184\n"
"s_add_u32 s72, s72, s4\nMvaa 156,156,188\ns_and_b32 s4, s12, 1\nMvaa 152,148,188\ns_lshl_b32 s4, s4, 21\nMvaa 152,144,184\ns_add_u32 s72, s72, s4\nMvaa 92,136,184\n"
"s_lshl3_add_u32 s2, s13, s11\nMvaa 92,140,188\nD0 5,20,45,3072\nMvaa 88,132,188\ns_lshl_b32 s2, s2, 11\nMvaa 88,128,184\nWL 0\ns_barrier\nMvaa 96,128,224\nRa 192,10,16384\n"
"Mvaa 96,132,228\ns_add_u32 m0, s47, 0\ns_add_u32 s44, s44, 0x40000\nMvaa 100,140,228\nRa 196,11,16384\nMvaa 100,136,224\ns_lshl_b32 s3, s14, 22\nMvaa 160,144,224\nRa 200,10,18432\n"
"Mvaa 160,148,228\nD0 6,24,44,0\nMvaa 164,156,228\nRa 204,11,18432\nMvaa 164,152,224\ns_add_u32 s2, s2, s3\nMvaa 172,152,232\nRa 208,10,24576\nMvaa 172,156,236\n"
"s_add_u32 s2, s2, s72\nMvaa 168,148,236\nRa 212,11,24576\nMvaa 168,144,232\ns_add_u32 s49, s2, 0xc00000\nMvaa 108,136,232\nRa 216,10,26624\nMvaa 108,140,236\nD0 7,24,44,1024\n"
"Mvaa 104,132,236\nRa 220,11,26624\nMvaa 104,128,232\nMvaa 112,128,240\nMvaa 112,132,244\nMvaa 116,140,244\nMvaa 116,136,240\nMvaa 176,144,240\nMvaa 176,148,244\nD0 8,24,44,2048\n"
"Mvaa 180,156,244\nMvaa 180,152,240\nMvaa 188,152,248\nMvaa 188,156,252\nMvaa 184,148,252\nMvaa 184,144,248\nMvaa 124,136,248\nMvaa 124,140,252\nD0 9,24,44,3072\nMvaa 120,132,252\n"
"Mvaa 120,128,248\nWL 0\nMaaa 32,192,224\nRa 128,10,32768\ns_mov_b32 s4, s13\nMaaa 32,196,228\ns_add_u32 m0, s47, 16384\ns_add_u32 s44, s44, 0x40000\nMaaa 36,204,228\n"
"Ra 132,11,32768\nv_lshrrev_b32 v32, 2, v0\nMaaa 36,200,224\nv_mul_u32_u24 v32, 896, v32\nMaaa 96,208,224\nRa 136,10,34816\nv_and_b32 v33, 3, v0\nMaaa 96,212,228\nD0 6,24,44,0\n"
"Maaa 100,220,228\nRa 140,11,34816\nv_lshl_add_u32 v32, v33, 4, v32\nMaaa 100,216,224\ns_lshr_b32 s2, s11, 1\nMaaa 108,216,232\nRa 144,10,40960\ns_mul_i32 s3, s2, 448\n"
"Maaa 108,220,236\ns_and_b32 s5, s11, 1\nMaaa 104,212,236\nRa 148,11,40960\ns_lshl3_add_u32 s3, s5, s3\nMaaa 104,208,232\ns_add_u32 s3, s3, 131072\nMaaa 44,200,232\n"
"Ra 152,10,43008\nv_add_u32 v32, s3, v32\nMaaa 44,204,236\nD0 7,24,44,1024\nMaaa 40,196,236\nRa 156,11,43008\nTv 36,32,0\nMaaa 40,192,232\nTv 38,32,14336\nMaaa 48,192,240\n"
"Tv 40,32,64\nTv 42,32,14400\nMaaa 48,196,244\nTv 44,32,128\nMaaa 52,204,244\nTv 46,32,14464\nTv 48,32,192\nMaaa 52,200,240\nTv 50,32,14528\nMaaa 112,208,240\nTv 52,32,256\n"
"Tv 54,32,14592\nMaaa 112,212,244\nD0 8,24,44,2048\nMaaa 116,220,244\nTv 56,32,320\nTv 58,32,14656\nMaaa 116,216,240\nTv 60,32,384\nMaaa 124,216,248\nTv 62,32,14720\n"
"s_lshr_b32 s0, s4, 1\nMaaa 124,220,252\ns_mul_i32 s0, s0, 12\nMaaa 120,212,252\ns_lshl1_add_u32 s0, s5, s0\ns_and_b32 s6, s4, 1\nMaaa 120,208,248\ns_add_u32 s0, s0, s6\n"
"Maaa 60,200,248\ns_lshl3_add_u32 s1, s5, s4\ns_add_u32 s77, s1, 48\nMaaa 60,204,252\nD0 9,24,44,3072\nMaaa 56,196,252\ns_and_b32 s4, s12, 1\ns_mov_b32 s7, 0x6543210\n"
"Maaa 56,192,248\ns_waitcnt vmcnt(17) lgkmcnt(14)\ns_barrier\nMaaa 0,192,160\nTa 224,16,32768\ns_cmp_lg_u32 s4, s2\ns_cselect_b32 s6, s0, s1\ns_cselect_b32 s5, 4, 16\n"
"s_cselect_b32 s7, 0x6352410, s7\ns_cselect_b32 s3, 0, 229376\nMaaa 0,196,164\nTa 226,16,34816\nMaaa 4,204,164\ns_add_u32 m0, s47, -49152\ns_add_u32 s48, s48, 128\nMaaa 4,200,160\n"
"Ta 228,16,49152\nMaaa 64,208,160\nTa 230,16,51200\ns_add_u32 s3, s3, s72\nMaaa 64,212,164\nD0 2,20,48,0\nMaaa 68,220,164\nTa 232,17,32768\ns_add_u32 s3, s3, 0x1400000\n"
"Maaa 68,216,160\nTa 234,17,34816\nMaaa 76,216,168\nWL 14\ns_lshr_b32 s0, s6, 4\nMaaa 76,220,172\nTa 236,17,49152\nMaaa 72,212,172\nTa 238,17,51200\ns_lshl_b32 s0, s0, 2\n"
"Maaa 72,208,168\ns_lshr_b32 s0, s7, s0\nMaaa 12,200,168\nTa 240,18,32768\ns_and_b32 s0, s0, 7\nMaaa 12,204,172\nTa 242,18,34816\nMaaa 8,196,172\nD0 3,20,48,1024\n"
"s_lshl_b32 s0, s0, 5\nMaaa 8,192,168\nTa 244,18,49152\nMaaa 16,192,176\nTa 246,18,51200\ns_and_b32 s1, s6, 15\nMaaa 16,196,180\ns_add_u32 s0, s0, s1\nMaaa 20,204,180\n"
"Ta 248,19,32768\ns_and_b32 s1, s6, 8\nMaaa 20,200,176\nTa 250,19,34816\nMaaa 80,208,176\ns_add_u32 s0, s0, s1\ns_lshl_b32 s0, s0, 10\nMaaa 80,212,180\nTa 252,19,49152\n"
"Maaa 84,220,180\nTa 254,19,51200\ns_add_u32 s0, s0, s3\nMaaa 84,216,176\nD0 4,20,48,2048\nMaaa 92,216,184\nSv 36,1,16,0,0\ns_add_u32 s6, s6, s5\nMaaa 92,220,188\nMaaa 88,212,188\n"
"Maaa 88,208,184\nMaaa 28,200,184\nMaaa 28,204,188\nD0 5,20,48,3072\nMaaa 24,196,188\nMaaa 24,192,184\nWL 0\nMvaa 96,128,224\nTa 160,12,32768\ns_lshr_b32 s0, s6, 4\n"
"Mvaa 96,132,228\nTa 162,12,34816\nMvaa 100,140,228\ns_add_u32 m0, s47, -32768\ns_add_u32 s45, s45, 128\nMvaa 100,136,224\nTa 164,12,49152\nMvaa 160,144,224\nTa 166,12,51200\n"
"s_lshl_b32 s0, s0, 2\nMvaa 160,148,228\nD0 2,20,45,0\nMvaa 164,156,228\nTa 168,13,32768\ns_lshr_b32 s0, s7, s0\nMvaa 164,152,224\nTa 170,13,34816\nMvaa 172,152,232\n"
"s_and_b32 s0, s0, 7\ns_lshl_b32 s0, s0, 5\nMvaa 172,156,236\nTa 172,13,49152\nMvaa 168,148,236\nTa 174,13,51200\ns_and_b32 s1, s6, 15\nMvaa 168,144,232\ns_add_u32 s0, s0, s1\n"
"Mvaa 108,136,232\nTa 176,14,32768\ns_and_b32 s1, s6, 8\nMvaa 108,140,236\nTa 178,14,34816\nMvaa 104,132,236\nD0 3,20,45,1024\ns_add_u32 s0, s0, s1\nMvaa 104,128,232\n"
"Ta 180,14,49152\nMvaa 112,128,240\nTa 182,14,51200\ns_lshl_b32 s0, s0, 10\nMvaa 112,132,244\ns_add_u32 s0, s0, s3\nMvaa 116,140,244\nTa 184,15,32768\nSv 40,1,16,0,0\n"
"Mvaa 116,136,240\nTa 186,15,34816\nMvaa 176,144,240\ns_add_u32 s6, s6, s5\nMvaa 176,148,244\nTa 188,15,49152\nMvaa 180,156,244\nTa 190,15,51200\nMvaa 180,152,240\nD0 4,20,45,2048\n"
"Mvaa 188,152,248\nMvaa 188,156,252\nMvaa 184,148,252\nMvaa 184,144,248\nMvaa 124,136,248\ns_lshr_b32 s0, s6, 4\ns_lshl_b32 s0, s0, 2\nMvaa 124,140,252\nD0 5,20,45,3072\n"
"Mvaa 120,132,252\ns_lshr_b32 s0, s7, s0\ns_and_b32 s0, s0, 7\nMvaa 120,128,248\ns_waitcnt vmcnt(18) lgkmcnt(0)\ns_barrier\nMvaa 64,128,160\nRa 192,10,49152\ns_lshl_b32 s0, s0, 5\n"
"Mvaa 64,132,164\ns_add_u32 m0, s47, 32768\ns_add_u32 s44, s44, 0x40000\nMvaa 68,140,164\nRa 196,11,49152\ns_and_b32 s1, s6, 15\nMvaa 68,136,160\ns_add_u32 s0, s0, s1\n"
"Mvaa 128,144,160\nRa 200,10,51200\ns_and_b32 s1, s6, 8\nMvaa 128,148,164\nD0 6,24,44,0\nMvaa 132,156,164\nRa 204,11,51200\ns_add_u32 s0, s0, s1\nMvaa 132,152,160\n"
"s_lshl_b32 s0, s0, 10\nMvaa 140,152,168\nRa 208,10,57344\ns_add_u32 s0, s0, s3\nMvaa 140,156,172\nSv 44,1,16,0,0\nMvaa 136,148,172\nRa 212,11,57344\ns_add_u32 s6, s6, s5\n"
"Mvaa 136,144,168\nMvaa 76,136,168\nRa 216,10,59392\nMvaa 76,140,172\nD0 7,24,44,1024\nMvaa 72,132,172\nRa 220,11,59392\nMvaa 72,128,168\nMvaa 80,128,176\nMvaa 80,132,180\n"
"Mvaa 84,140,180\ns_mov_b32 s6, s77\ns_mov_b32 s5, 16\nMvaa 84,136,176\ns_lshr_b32 s0, s6, 4\nMvaa 144,144,176\ns_lshl_b32 s0, s0, 2\ns_lshr_b32 s0, s7, s0\nMvaa 144,148,180\n"
"D0 8,24,44,2048\nMvaa 148,156,180\ns_and_b32 s0, s0, 7\ns_lshl_b32 s0, s0, 5\nMvaa 148,152,176\ns_and_b32 s1, s6, 15\nMvaa 156,152,184\ns_add_u32 s0, s0, s1\ns_and_b32 s1, s6, 8\n"
"Mvaa 156,156,188\ns_add_u32 s0, s0, s1\nMvaa 152,148,188\ns_lshl_b32 s0, s0, 10\ns_add_u32 s0, s0, s3\nMvaa 152,144,184\nSv 48,1,16,0,0\nMvaa 92,136,184\ns_add_u32 s6, s6, s5\n"
"Mvaa 92,140,188\nD0 9,24,44,3072\nMvaa 88,132,188\nMvaa 88,128,184\nWL 0\nMaaa 0,192,160\nRa 128,10,0\nMaaa 0,196,164\ns_add_u32 m0, s47, 49152\ns_add_u32 s44, s44, 0x40000\n"
"Maaa 4,204,164\nRa 132,11,0\nMaaa 4,200,160\nMaaa 64,208,160\nRa 136,10,2048\ns_lshr_b32 s0, s6, 4\nMaaa 64,212,164\nD0 6,24,44,0\nMaaa 68,220,164\nRa 140,11,2048\n"
"s_lshl_b32 s0, s0, 2\nMaaa 68,216,160\ns_lshr_b32 s0, s7, s0\nMaaa 76,216,168\nRa 144,10,8192\ns_and_b32 s0, s0, 7\nMaaa 76,220,172\ns_lshl_b32 s0, s0, 5\nMaaa 72,212,172\n"
"Ra 148,11,8192\ns_and_b32 s1, s6, 15\nMaaa 72,208,168\ns_add_u32 s0, s0, s1\nMaaa 12,200,168\nRa 152,10,10240\ns_and_b32 s1, s6, 8\nMaaa 12,204,172\nD0 7,24,44,1024\n"
"Maaa 8,196,172\nRa 156,11,10240\ns_add_u32 s0, s0, s1\nMaaa 8,192,168\ns_lshl_b32 s0, s0, 10\nMaaa 16,192,176\ns_add_u32 s0, s0, s3\nSv 52,1,16,0,0\nMaaa 16,196,180\n"
"s_add_u32 s6, s6, s5\nMaaa 20,204,180\nMaaa 20,200,176\nMaaa 80,208,176\nMaaa 80,212,180\nD0 8,24,44,2048\nMaaa 84,220,180\nMaaa 84,216,176\nMaaa 92,216,184\nMaaa 92,220,188\n"
"s_lshr_b32 s0, s6, 4\nMaaa 88,212,188\ns_lshl_b32 s0, s0, 2\ns_lshr_b32 s0, s7, s0\nMaaa 88,208,184\ns_and_b32 s0, s0, 7\nMaaa 28,200,184\ns_lshl_b32 s0, s0, 5\n"
"s_and_b32 s1, s6, 15\nMaaa 28,204,188\nD0 9,24,44,3072\nMaaa 24,196,188\ns_add_u32 s0, s0, s1\ns_and_b32 s1, s6, 8\nMaaa 24,192,184\nWV 21\ns_barrier\nMaaa 32,192,224\n"
"Ta 160,12,0\ns_add_u32 s0, s0, s1\nMaaa 32,196,228\nTa 162,12,2048\nMaaa 36,204,228\ns_add_u32 m0, s47, -16384\ns_add_u32 s48, s48, 128\nMaaa 36,200,224\nTa 164,12,16384\n"
"Maaa 96,208,224\nTa 166,12,18432\ns_lshl_b32 s0, s0, 10\nMaaa 96,212,228\nD0 2,20,48,0\nMaaa 100,220,228\nTa 168,13,0\ns_add_u32 s0, s0, s3\nMaaa 100,216,224\nTa 170,13,2048\n"
"Maaa 108,216,232\nSv 56,1,16,0,0\ns_add_u32 s6, s6, s5\nMaaa 108,220,236\nTa 172,13,16384\nMaaa 104,212,236\nTa 174,13,18432\nMaaa 104,208,232\nMaaa 44,200,232\nTa 176,14,0\n"
"Maaa 44,204,236\nTa 178,14,2048\nMaaa 40,196,236\nD0 3,20,48,1024\nMaaa 40,192,232\nTa 180,14,16384\nMaaa 48,192,240\nTa 182,14,18432\ns_lshr_b32 s0, s6, 4\nMaaa 48,196,244\n"
"s_lshl_b32 s0, s0, 2\nMaaa 52,204,244\nTa 184,15,0\ns_lshr_b32 s0, s7, s0\nMaaa 52,200,240\nTa 186,15,2048\nMaaa 112,208,240\ns_and_b32 s0, s0, 7\ns_lshl_b32 s0, s0, 5\n"
"Maaa 112,212,244\nTa 188,15,16384\nMaaa 116,220,244\nTa 190,15,18432\ns_and_b32 s1, s6, 15\nMaaa 116,216,240\nD0 4,20,48,2048\nMaaa 124,216,248\ns_add_u32 s0, s0, s1\n"
"s_and_b32 s1, s6, 8\nMaaa 124,220,252\ns_add_u32 s0, s0, s1\nMaaa 120,212,252\ns_lshl_b32 s0, s0, 10\ns_add_u32 s0, s0, s3\nMaaa 120,208,248\nSv 60,1,16,0,0\nMaaa 60,200,248\n"
"s_add_u32 s6, s6, s5\nMaaa 60,204,252\nD0 5,20,48,3072\nMaaa 56,196,252\nMaaa 56,192,248\nWL 0\nMvaa 64,128,160\nTa 224,16,0\nMvaa 64,132,164\nTa 226,16,2048\n"
"s_add_u32 m0, s47, 65536\ns_add_u32 s46, s51, 0\nMvaa 68,140,164\nv_lshrrev_b32 v48, 4, v0\nMvaa 68,136,160\nTa 228,16,16384\nD0 6,32,46,0\nMvaa 128,144,160\nTa 230,16,18432\n"
"Mvaa 128,148,164\nv_and_b32 v49, 7, v0\nMvaa 132,156,164\nTa 232,17,0\nMvaa 132,152,160\nTa 234,17,2048\nMvaa 140,152,168\nv_xor_b32 v49, v49, v48\nD0 7,32,46,1024\n"
"Mvaa 140,156,172\nTa 236,17,16384\nMvaa 136,148,172\nTa 238,17,18432\nMvaa 136,144,168\nv_lshlrev_b32 v49, 4, v49\nMvaa 76,136,168\nTa 240,18,0\nMvaa 76,140,172\nTa 242,18,2048\n"
"D0 8,32,46,2048\nMvaa 72,132,172\nv_lshrrev_b32 v48, 3, v0\nMvaa 72,128,168\nTa 244,18,16384\nMvaa 80,128,176\nTa 246,18,18432\nMvaa 80,132,180\nv_mul_u32_u24 v48, 896, v48\n"
"Mvaa 84,140,180\nTa 248,19,0\nD0 9,32,46,3072\nMvaa 84,136,176\nTa 250,19,2048\nMvaa 144,144,176\nv_add_u32 v40, v48, v49\nMvaa 144,148,180\nTa 252,19,16384\n"
"s_add_u32 m0, s47, 81920\ns_add_u32 s46, s51, 262144\nMvaa 148,156,180\nTa 254,19,18432\nMvaa 148,152,176\nv_xor_b32 v49, 64, v40\nD0 6,32,46,0\nMvaa 156,152,184\n"
"v_add_u32 v41, 6144, v49\nMvaa 156,156,188\nv_add_u32 v42, 55296, v40\nMvaa 152,148,188\nv_add_u32 v43, 61440, v49\nMvaa 152,144,184\ns_mov_b32 s2, s13\nMvaa 92,136,184\n"
"s_lshl3_add_u32 s2, s2, s11\nD0 7,32,46,1024\nMvaa 92,140,188\ns_mul_i32 s52, s2, 14336\nMvaa 88,132,188\ns_lshr_b32 s2, s13, 2\ns_bfe_u32 s3, s13, 0x10001\nMvaa 88,128,184\n"
"s_waitcnt vmcnt(23) lgkmcnt(0)\ns_barrier\nMvaa 96,128,224\nRa 192,10,16384\ns_mul_i32 s3, s3, 17\nMvaa 96,132,228\ns_add_u32 s2, s2, s3\nD0 8,32,46,2048\nMvaa 100,140,228\n"
"Ra 196,11,16384\ns_lshl2_add_u32 s3, s14, 0\nMvaa 100,136,224\ns_lshl_b32 s3, s3, 2\nMvaa 160,144,224\nRa 200,10,18432\ns_lshr_b32 s3, 0xe420ca86, s3\nMvaa 160,148,228\n"
"s_and_b32 s3, s3, 15\nMvaa 164,156,228\nRa 204,11,18432\ns_add_u32 s2, s2, s3\nD0 9,32,46,3072\nMvaa 164,152,224\ns_cmp_eq_u32 s2, 32\ns_cselect_b32 s4, 16, s2\nMvaa 172,152,232\n"
"Ra 208,10,24576\ns_mul_i32 s75, s4, 65536\nMvaa 172,156,236\ns_lshr_b32 s2, s13, 2\ns_add_u32 m0, s47, -65536\ns_add_u32 s46, s52, 0\nMvaa 168,148,236\nRa 212,11,24576\n"
"s_bfe_u32 s3, s13, 0x10001\nMvaa 168,144,232\ns_mul_i32 s3, s3, 17\nD0 40,28,46,0\nMvaa 108,136,232\nRa 216,10,26624\ns_add_u32 s2, s2, s3\nMvaa 108,140,236\n"
"s_lshl2_add_u32 s3, s14, 1\nMvaa 104,132,236\nRa 220,11,26624\ns_lshl_b32 s3, s3, 2\nMvaa 104,128,232\ns_lshr_b32 s3, 0xe420ca86, s3\nMvaa 112,128,240\ns_and_b32 s3, s3, 15\n"
"s_add_u32 s2, s2, s3\nD0 41,28,46,1024\nMvaa 112,132,244\ns_cmp_eq_u32 s2, 32\ns_cselect_b32 s4, 16, s2\nMvaa 116,140,244\ns_mul_i32 s76, s4, 65536\ns_lshr_b32 s2, s13, 2\n"
"Mvaa 116,136,240\ns_bfe_u32 s3, s13, 0x10001\nMvaa 176,144,240\ns_mul_i32 s3, s3, 17\ns_add_u32 s2, s2, s3\nMvaa 176,148,244\ns_lshl2_add_u32 s3, s14, 2\nD0 42,28,46,2048\n"
"Mvaa 180,156,244\ns_lshl_b32 s3, s3, 2\ns_lshr_b32 s3, 0xe420ca86, s3\nMvaa 180,152,240\ns_and_b32 s3, s3, 15\nMvaa 188,152,248\ns_add_u32 s2, s2, s3\nMvaa 188,156,252\n"
"s_cmp_eq_u32 s2, 32\ns_cselect_b32 s4, 16, s2\nMvaa 184,148,252\ns_mul_i32 s77, s4, 65536\ns_lshr_b32 s2, s13, 2\nD0 43,28,46,3072\nMvaa 184,144,248\ns_bfe_u32 s3, s13, 0x10001\n"
"Mvaa 124,136,248\ns_mul_i32 s3, s3, 17\ns_add_u32 s2, s2, s3\nMvaa 124,140,252\ns_lshl2_add_u32 s3, s14, 3\nMvaa 120,132,252\ns_lshl_b32 s3, s3, 2\ns_lshr_b32 s3, 0xe420ca86, s3\n"
"Mvaa 120,128,248\nWL 0\nMaaa 32,192,224\nRa 128,10,32768\ns_and_b32 s3, s3, 15\ns_add_u32 m0, s47, 0\ns_add_u32 s46, s51, 524288\nD0 6,32,46,0\nMaaa 32,196,228\n"
"s_add_u32 s2, s2, s3\ns_cmp_eq_u32 s2, 32\ns_cselect_b32 s4, 16, s2\nMaaa 36,204,228\nRa 132,11,32768\ns_mul_i32 s78, s4, 65536\nMaaa 36,200,224\ns_lshr_b32 s3, s11, 1\n"
"Maaa 96,208,224\nRa 136,10,34816\ns_lshl_b32 s3, s3, 15\nMaaa 96,212,228\ns_add_u32 s2, s72, s3\nD0 7,32,46,1024\nMaaa 100,220,228\nRa 140,11,34816\ns_and_b32 s3, s11, 1\n"
"Maaa 100,216,224\ns_lshl_b32 s3, s3, 14\nMaaa 108,216,232\nRa 144,10,40960\ns_add_u32 s2, s2, s3\nMaaa 108,220,236\ns_add_u32 s2, s2, 0x400000\nMaaa 104,212,236\nRa 148,11,40960\n"
"s_mov_b32 s55, 4096\nD0 8,32,46,2048\nMaaa 104,208,232\ns_add_u32 s75, s75, s2\nMaaa 44,200,232\nRa 152,10,43008\ns_add_u32 s76, s76, s2\nMaaa 44,204,236\ns_add_u32 s77, s77, s2\n"
"Maaa 40,196,236\nRa 156,11,43008\ns_add_u32 s78, s78, s2\nMaaa 40,192,232\nv_add_u32 v32, 0x10000, v12\nD0 9,32,46,3072\nMaaa 48,192,240\nv_add_u32 v33, 0x10000, v13\n"
"v_add_u32 v34, 0x10000, v14\nMaaa 48,196,244\nv_add_u32 v35, 0x10000, v15\nMaaa 52,204,244\nv_add_u32 v36, 0x10000, v16\nv_add_u32 v37, 0x10000, v17\ns_add_u32 m0, s47, 16384\n"
"s_add_u32 s46, s51, 786432\nMaaa 52,200,240\nv_add_u32 v38, 0x10000, v18\nMaaa 112,208,240\nv_add_u32 v39, 0x10000, v19\ns_add_u32 s2, s11, 1\nD0 6,32,46,0\nMaaa 112,212,244\n"
"s_lshr_b32 s2, s2, 1\nMaaa 116,220,244\ns_lshl_b32 s2, s2, 12\nv_add_u32 v44, s2, v10\nMaaa 116,216,240\nv_add_u32 v45, s2, v11\nMaaa 124,216,248\nv_xor_b32 v46, 0x1000, v44\n"
"v_xor_b32 v47, 0x1000, v45\nMaaa 124,220,252\ns_lshr_b32 s3, s11, 1\nD0 7,32,46,1024\nMaaa 120,212,252\ns_lshl_b32 s3, s3, 13\ns_lshl_b32 s2, s11, 12\nMaaa 120,208,248\n"
"s_add_u32 s2, s2, s3\nMaaa 60,200,248\ns_add_u32 s2, s2, s72\ns_lshl_b32 s3, s11, 12\nMaaa 60,204,252\ns_add_u32 s89, s3, 0x10000\nMaaa 56,196,252\ns_mov_b32 s88, 0xffff8000\n"
"s_lshl_b32 s4, s13, 1\nD0 8,32,46,2048\nMaaa 56,192,248\ns_waitcnt vmcnt(25) lgkmcnt(0)\ns_barrier\nMaaa 0,192,160\nTa 224,16,32768\ns_sub_u32 s79, 15, s4\nMaaa 0,196,164\n"
"Ta 226,16,34816\nMaaa 4,204,164\ns_add_u32 s96, s2, 0x1310000\ns_mov_b32 s63, 0x53800000\nMaaa 4,200,160\nTa 228,16,49152\nD0 9,32,46,3072\nMaaa 64,208,160\nTa 230,16,51200\n"
"s_mov_b32 s64, 0x2b800000\nMaaa 64,212,164\ns_sub_u32 s98, s4, 1\nMaaa 68,220,164\nTa 232,17,32768\ns_lshl_b32 s5, s13, 5\ns_add_u32 m0, s47, -32768\ns_add_u32 s46, s52, 128\n"
"Maaa 68,216,160\nTa 234,17,34816\nMaaa 76,216,168\ns_add_u32 s5, s5, s11\ns_lshl_b32 s65, s5, 9\nD0 40,28,46,0\nMaaa 76,220,172\nTa 236,17,49152\nMaaa 72,212,172\nTa 238,17,51200\n"
"s_add_u32 s65, s65, s72\nMaaa 72,208,168\ns_add_u32 s65, s65, 0x1580000\nMaaa 12,200,168\nTa 240,18,32768\ns_and_b32 s66, s72, 0x2000\nMaaa 12,204,172\nTa 242,18,34816\n"
"D0 41,28,46,1024\nMaaa 8,196,172\ns_add_u32 s66, s66, s56\ns_add_u32 s66, s66, 0x100000\nMaaa 8,192,168\nTa 244,18,49152\nMaaa 16,192,176\nTa 246,18,51200\ns_and_b32 s5, s12, 1\n"
"Maaa 16,196,180\ns_mul_i32 s58, s5, 0x202000\nMaaa 20,204,180\nTa 248,19,32768\ns_xor_b32 s58, s58, s72\nD0 42,28,46,2048\nMaaa 20,200,176\nTa 250,19,34816\nMaaa 80,208,176\n"
"s_add_u32 s58, s58, 0x15c0000\ns_lshl_b32 s5, s5, 8\nMaaa 80,212,180\nTa 252,19,49152\nMaaa 84,220,180\nTa 254,19,51200\ns_add_u32 s61, s58, s5\nMaaa 84,216,176\n"
"s_lshl4_add_u32 s60, s13, s61\nD0 43,28,46,3072\nMaaa 92,216,184\ns_add_u32 s54, s60, 128\ns_xor_b32 s60, s54, 0x100\nMaaa 92,220,188\ns_cmp_eq_u32 s11, 0\n"
"s_cselect_b32 s54, s54, 0x40000000\nMaaa 88,212,188\ns_getpc_b64 s[8:9]\n.PC418:\ns_add_u32 s8, s8, .HD405-.PC418\ns_addc_u32 s9, s9, 0\ns_add_u32 m0, s47, -49152\n"
"s_add_u32 s46, s52, 256\nMaaa 88,208,184\ns_mov_b32 s97, 0xff800000\nMaaa 28,200,184\ns_mov_b32 s90, 0\nD0 40,28,46,0\nMaaa 28,204,188\ns_cmp_lt_u32 s11, 2\n"
"s_cselect_b32 s92, 2, 0\nMaaa 24,196,188\ns_mul_i32 s4, s79, 65536\ns_sub_u32 s4, s2, s4\nMaaa 24,192,184\nWL 0\nMvaa 96,128,224\nTa 160,12,32768\ns_add_u32 s94, s4, 0x20000\n"
"Mvaa 96,132,228\nTa 162,12,34816\nD0 41,28,46,1024\nMvaa 100,140,228\ns_add_u32 s95, s4, 0x410000\ns_add_u32 s86, s2, 0x130000\nMvaa 100,136,224\nTa 164,12,49152\n"
"Mvaa 108,136,232\nTa 166,12,51200\ns_add_u32 s87, s2, 0x520000\nMvaa 108,140,236\ns_sub_u32 s99, s79, 2\ns_cmp_eq_u32 s13, 7\ns_cselect_b32 s93, 1.0, 0\nMvaa 104,132,236\n"
"Ta 168,13,32768\ns_mov_b32 s36, s68\nD0 42,28,46,2048\nMvaa 104,128,232\nTa 170,13,34816\nMvaa 112,128,240\ns_and_b32 s37, s69, 0xffff\ns_mov_b32 s38, 0x1c0000\nMvaa 112,132,244\n"
"Ta 172,13,49152\nCV 208,96,97\nMvaa 116,140,244\nTa 174,13,51200\ns_mov_b32 s39, 0x20000\nMvaa 116,136,240\ns_and_b32 s2, s74, 15\nD0 43,28,46,3072\nMvaa 124,136,248\n"
"Ta 176,14,32768\ns_min_u32 s2, s2, 13\nMvaa 124,140,252\nTa 178,14,34816\nMvaa 120,132,252\ns_sub_u32 s2, s2, 0\ns_mul_i32 s3, s73, 14\nMvaa 120,128,248\nTa 180,14,49152\n"
"CV 209,98,99\nMvaa 160,144,224\nTa 182,14,51200\ns_add_u32 s2, s2, s3\nMvaa 160,148,228\ns_lshl2_add_u32 s2, s2, s11\nMvaa 164,156,228\nTa 184,15,32768\ns_mul_i32 s3, s2, 4682\n"
"Mvaa 164,152,224\nTa 186,15,34816\nCV 210,100,101\nMvaa 172,152,232\ns_lshr_b32 s3, s3, 16\ns_mul_i32 s22, s3, 14\nMvaa 172,156,236\nTa 188,15,49152\nCV 211,102,103\n"
"Mvaa 168,148,236\nTa 190,15,51200\ns_sub_u32 s22, s2, s22\nMvaa 168,144,232\ns_and_b32 s21, s3, 28\nMvaa 176,144,240\ns_add_u32 s21, s21, s3\ns_lshl_b32 s21, s21, 4\n"
"Mvaa 176,148,244\ns_bfe_u32 s41, s3, 0x30002\ns_cmp_gt_u32 s22, 6\ns_cselect_b32 s2, 1179648, 0\ns_cselect_b32 s1, 16, 0\nMvaa 180,156,244\ns_add_u32 s41, s41, s1\n"
"s_lshl4_add_u32 s41, s41, 0xc048480\nMvaa 180,152,240\ns_and_b32 s1, s3, 28\nCV 212,104,105\nMvaa 188,152,248\ns_add_u32 s3, s3, s1\ns_lshl_b32 s3, s3, 11\nMvaa 188,156,252\n"
"s_add_u32 s2, s2, s3\nMvaa 184,148,252\ns_lshl_b32 s3, s22, 17\ns_add_u32 s2, s2, s3\nMvaa 184,144,248\ns_waitcnt vmcnt(16) lgkmcnt(0)\ns_barrier\nMvaa 64,128,160\n"
"Ra 192,10,49152\ns_add_u32 s20, s2, 0x10e0000\ns_add_u32 m0, s47, 32768\ns_add_u32 s46, s51, 1048576\nMvaa 64,132,164\ns_lshl_b32 s2, s22, 7\nMvaa 68,140,164\nRa 196,11,49152\n"
"s_add_u32 s40, s2, 0xc04b800\nD0 6,32,46,0\nMvaa 68,136,160\ns_mul_i32 s2, s21, 896\nCV 213,106,107\nMvaa 76,136,168\nRa 200,10,51200\ns_lshl_b32 s3, s22, 6\nMvaa 76,140,172\n"
"s_add_u32 s22, s2, s3\nCV 214,108,109\nMvaa 72,132,172\nRa 204,11,51200\ns_add_u32 s23, s22, 57344\nMvaa 72,128,168\nD0 7,32,46,1024\nMvaa 80,128,176\nRa 208,10,57344\n"
"CV 215,110,111\nMvaa 80,132,180\nCV 216,112,113\nCV 217,114,115\nMvaa 84,140,180\nRa 212,11,57344\nMvaa 84,136,176\nCV 218,116,117\nCV 219,118,119\nMvaa 92,136,184\n"
"Ra 216,10,59392\nD0 8,32,46,2048\ns_lshl3_add_u32 s5, s12, s13\ns_lshl2_add_u32 s5, s5, s11\ns_lshl2_add_u32 s5, s5, 0xc04a400\nbuffer_load_dword v27, off, s[16:19], s5 sc0 sc1\n"
"Mvaa 92,140,188\nCV 220,120,121\nMvaa 88,132,188\nRa 220,11,59392\nCV 221,122,123\nMvaa 88,128,184\nCV 222,124,125\nCV 223,126,127\nMvaa 128,144,160\nCV 192,64,65\nCV 193,66,67\n"
"Mvaa 128,148,164\nD0 9,32,46,3072\nMvaa 132,156,164\nCV 194,68,69\nCV 195,70,71\nMvaa 132,152,160\nCV 196,72,73\nCV 197,74,75\nMvaa 140,152,168\ns_add_u32 m0, s47, 49152\n"
"s_add_u32 s46, s51, 1310720\nMvaa 140,156,172\nCV 198,76,77\nCV 199,78,79\nMvaa 136,148,172\nD0 6,32,46,0\nCV 200,80,81\nMvaa 136,144,168\nCV 201,82,83\nMvaa 144,144,176\n"
"CV 202,84,85\nCV 203,86,87\nMvaa 144,148,180\nCV 204,88,89\nCV 205,90,91\nMvaa 148,156,180\nCV 206,92,93\nMvaa 148,152,176\nD0 7,32,46,1024\nCV 207,94,95\nMvaa 156,152,184\n"
"CV 240,160,161\nMvaa 156,156,188\nCV 241,162,163\nSv 192,1,16,75,0\nMvaa 152,148,188\nCV 242,164,165\nMvaa 152,144,184\nWL 0\nMaaa 0,192,160\nD0 8,32,46,2048\nRa 128,44,0\n"
"Maaa 0,196,164\nCV 243,166,167\nMaaa 4,204,164\nRa 132,45,0\nMaaa 4,200,160\nCV 244,168,169\nMaaa 12,200,168\nRa 136,44,2048\nMaaa 12,204,172\nD0 9,32,46,3072\nMaaa 8,196,172\n"
"Ra 140,45,2048\nSv 196,1,16,75,1024\nMaaa 8,192,168\nCV 245,170,171\nMaaa 16,192,176\nRa 144,46,0\nMaaa 16,196,180\nCV 246,172,173\nMaaa 20,204,180\nRa 148,47,0\nMaaa 20,200,176\n"
"CV 247,174,175\nMaaa 28,200,184\nRa 152,46,2048\nMaaa 28,204,188\nCV 248,176,177\nMaaa 24,196,188\nRa 156,47,2048\nMaaa 24,192,184\nCV 249,178,179\nSv 200,1,16,75,2048\n"
"Maaa 64,208,160\nCV 250,180,181\nCV 251,182,183\nMaaa 64,212,164\nCV 252,184,185\nMaaa 68,220,164\nCV 253,186,187\nMaaa 68,216,160\nCV 254,188,189\nMaaa 76,216,168\n"
"CV 255,190,191\nMaaa 76,220,172\nCV 224,128,129\nMaaa 72,212,172\nCV 225,130,131\nMaaa 72,208,168\nCV 226,132,133\nMaaa 80,208,176\nCV 227,134,135\nSv 204,1,16,75,3072\n"
"Maaa 80,212,180\nCV 228,136,137\nMaaa 84,220,180\nCV 229,138,139\nMaaa 84,216,176\nCV 230,140,141\nMaaa 92,216,184\nCV 231,142,143\nMaaa 92,220,188\nCV 232,144,145\n"
"Maaa 88,212,188\nCV 233,146,147\nMaaa 88,208,184\ns_barrier\nMaaa 32,192,224\ns_add_u32 m0, s47, -16384\ns_add_u32 s46, s52, 384\nTa 160,32,0\nMaaa 32,196,228\nTa 162,32,2048\n"
"Maaa 36,204,228\nD0 40,28,46,0\ns_add_u32 s75, s75, s55\nSv 208,1,16,75,0\nMaaa 36,200,224\nTa 164,32,16384\nMaaa 44,200,232\nTa 166,32,18432\nMaaa 44,204,236\nCV 234,148,149\n"
"Maaa 40,196,236\nTa 168,33,0\nMaaa 40,192,232\nD0 41,28,46,1024\nTa 170,33,2048\nMaaa 48,192,240\nCV 235,150,151\nMaaa 48,196,244\nTa 172,33,16384\nMaaa 52,204,244\n"
"Ta 174,33,18432\nMaaa 52,200,240\nCV 236,152,153\nSv 212,1,16,75,1024\nMaaa 60,200,248\nD0 42,28,46,2048\nTa 176,34,0\nMaaa 60,204,252\nTa 178,34,2048\nMaaa 56,196,252\n"
"CV 237,154,155\nMaaa 56,192,248\nTa 180,34,16384\nMaaa 96,208,224\nTa 182,34,18432\nCV 238,156,157\nMaaa 96,212,228\nD0 43,28,46,3072\nMaaa 100,220,228\nTa 184,35,0\n"
"Maaa 100,216,224\nTa 186,35,2048\nMaaa 108,216,232\nCV 239,158,159\nSv 216,1,16,75,2048\nMaaa 108,220,236\nTa 188,35,16384\nMaaa 104,212,236\nTa 190,35,18432\nMaaa 104,208,232\n"
"Maaa 112,208,240\nMaaa 112,212,244\nMaaa 116,220,244\nMaaa 116,216,240\nMaaa 124,216,248\nMaaa 124,220,252\nSv 220,1,16,75,3072\nMaaa 120,212,252\nMaaa 120,208,248\nWL 0\n"
"Zvaa 64,160,128\nTa 224,36,0\nAR 60,0\nMvaa 64,164,132\nTa 226,36,2048\nZvaa 96,164,140\nAR 61,1\nMvaa 96,160,136\nTa 228,36,16384\nCV 192,60,61\nZvaa 128,160,144\n"
"Ta 230,36,18432\nMvaa 128,164,148\nAR 60,2\nAR 61,3\nZvaa 160,164,156\nTa 232,37,0\nSv 224,1,16,76,0\nMvaa 160,160,152\nTa 234,37,2048\nCV 193,60,61\nZvaa 164,168,152\nAR 60,4\n"
"Mvaa 164,172,156\nTa 236,37,16384\nZvaa 132,172,148\nTa 238,37,18432\nAR 61,5\nMvaa 132,168,144\nCV 194,60,61\nZvaa 100,168,136\nTa 240,38,0\nAR 60,6\nMvaa 100,172,140\n"
"Ta 242,38,2048\nZvaa 68,172,132\nSv 228,1,16,76,1024\nAR 61,7\nMvaa 68,168,128\nTa 244,38,16384\nZvaa 72,176,128\nTa 246,38,18432\nCV 195,60,61\nMvaa 72,180,132\nAR 60,8\n"
"Zvaa 104,180,140\nTa 248,39,0\nMvaa 104,176,136\nTa 250,39,2048\nAR 61,9\nZvaa 136,176,144\nCV 196,60,61\nMvaa 136,180,148\nTa 252,39,16384\nAR 60,10\nZvaa 168,180,156\n"
"Ta 254,39,18432\nSv 232,1,16,76,2048\nMvaa 168,176,152\nAR 61,11\nCV 197,60,61\nZvaa 172,184,152\nAR 60,12\nMvaa 172,188,156\nAR 61,13\nZvaa 140,188,148\nCV 198,60,61\nAR 60,14\n"
"Mvaa 140,184,144\nAR 61,15\nZvaa 108,184,136\nCV 199,60,61\nAR 60,16\nMvaa 108,188,140\nSv 236,1,16,76,3072\nZvaa 76,188,132\nAR 61,17\nCV 200,60,61\nMvaa 76,184,128\n"
"s_waitcnt vmcnt(29) lgkmcnt(0)\ns_barrier\nZvaa 80,224,128\nRa 192,44,32768\nAR 60,18\nMvaa 80,228,132\nRa 196,45,32768\ns_add_u32 m0, s47, 65536\ns_add_u32 s46, s51, 1572864\n"
"D0 6,32,46,0\nZvaa 112,228,140\nRa 200,44,34816\nMvaa 112,224,136\nRa 204,45,34816\nAR 61,19\nZvaa 144,224,144\nRa 208,46,32768\nMvaa 144,228,148\nRa 212,47,32768\nCV 201,60,61\n"
"D0 7,32,46,1024\nZvaa 176,228,156\nRa 216,46,34816\nMvaa 176,224,152\nRa 220,47,34816\nAR 60,20\nZvaa 180,232,152\nTa 160,12,0\nMvaa 180,236,156\nTa 162,12,2048\nD0 8,32,46,2048\n"
"Zvaa 148,236,148\nTa 164,12,16384\nAR 61,21\nMvaa 148,232,144\nTa 166,12,18432\nZvaa 116,232,136\nTa 168,13,0\nCV 202,60,61\nMvaa 116,236,140\nTa 170,13,2048\nD0 9,32,46,3072\n"
"Zvaa 84,236,132\nTa 172,13,16384\nAR 60,22\nMvaa 84,232,128\nTa 174,13,18432\ns_add_u32 m0, s47, 81920\ns_add_u32 s46, s51, 1835008\nZvaa 88,240,128\nTa 176,14,0\nAR 61,23\n"
"Mvaa 88,244,132\nTa 178,14,2048\nD0 6,32,46,0\nZvaa 120,244,140\nTa 180,14,16384\nMvaa 120,240,136\nTa 182,14,18432\nCV 203,60,61\nZvaa 152,240,144\nTa 184,15,0\nMvaa 152,244,148\n"
"Ta 186,15,2048\nAR 60,24\nD0 7,32,46,1024\nZvaa 184,244,156\nTa 188,15,16384\nMvaa 184,240,152\nTa 190,15,18432\nAR 61,25\nZvaa 188,248,152\nCV 204,60,61\nMvaa 188,252,156\n"
"AR 60,26\nD0 8,32,46,2048\nZvaa 156,252,148\nAR 61,27\nCV 205,60,61\nMvaa 156,248,144\nAR 60,28\nZvaa 124,248,136\nAR 61,29\nCV 206,60,61\nMvaa 124,252,140\nAR 60,30\n"
"D0 9,32,46,3072\nZvaa 92,252,132\nAR 61,31\nCV 207,60,61\nMvaa 92,248,128\nWL 0\nMvaa 64,160,192\nTa 224,16,0\nAR 60,32\nMvaa 64,164,196\nTa 226,16,2048\n"
"s_add_u32 m0, s47, -65536\ns_add_u32 s46, s52, 512\nD0 40,28,46,0\nMvaa 96,164,204\nAR 61,33\nMvaa 96,160,200\nTa 228,16,16384\nCV 208,60,61\nMvaa 128,160,208\nTa 230,16,18432\n"
"Mvaa 128,164,212\nAR 60,34\nAR 61,35\nD0 41,28,46,1024\nMvaa 160,164,220\nTa 232,17,0\nMvaa 160,160,216\nTa 234,17,2048\nCV 209,60,61\nMvaa 164,168,216\nAR 60,36\n"
"Mvaa 164,172,220\nTa 236,17,16384\nD0 42,28,46,2048\nMvaa 132,172,212\nTa 238,17,18432\nAR 61,37\nMvaa 132,168,208\nCV 210,60,61\nMvaa 100,168,200\nTa 240,18,0\nAR 60,38\n"
"Mvaa 100,172,204\nTa 242,18,2048\nD0 43,28,46,3072\nMvaa 68,172,196\ns_add_u32 s76, s76, s55\nSv 240,1,16,76,0\nMvaa 68,168,192\nTa 244,18,16384\nMvaa 72,176,192\nTa 246,18,18432\n"
"AR 61,39\nMvaa 72,180,196\nCV 211,60,61\nMvaa 104,180,204\nTa 248,19,0\nMvaa 104,176,200\nTa 250,19,2048\nSv 244,1,16,76,1024\nMvaa 136,176,208\nAR 60,40\nMvaa 136,180,212\n"
"Ta 252,19,16384\nAR 61,41\nMvaa 168,180,220\nTa 254,19,18432\nMvaa 168,176,216\nCV 212,60,61\nAR 60,42\nMvaa 172,184,216\nSv 248,1,16,76,2048\nMvaa 172,188,220\nAR 61,43\n"
"Mvaa 140,188,212\nCV 213,60,61\nAR 60,44\nMvaa 140,184,208\nAR 61,45\nMvaa 108,184,200\nCV 214,60,61\nAR 60,46\nMvaa 108,188,204\nSv 252,1,16,76,3072\nMvaa 76,188,196\nAR 61,47\n"
"CV 215,60,61\nMvaa 76,184,192\ns_waitcnt vmcnt(20) lgkmcnt(0)\ns_barrier\nMvaa 80,224,192\nRa 128,44,16384\nAR 60,48\nv_cmp_ne_u32 vcc, s15, v27\ns_nop 0\ns_cbranch_vccnz .S419\n"
".S420:\nMvaa 80,228,196\nRa 132,45,16384\nbuffer_store_dword v25, off, s[16:19], s54 offset:0 sc0 sc1\ns_add_u32 m0, s47, 0\ns_add_u32 s46, s51, 2097152\nD0 6,32,46,0\n"
"Mvaa 112,228,204\nRa 136,44,18432\nMvaa 112,224,200\nRa 140,45,18432\nAR 61,49\nMvaa 144,224,208\nRa 144,46,16384\nMvaa 144,228,212\nRa 148,47,16384\nCV 216,60,61\n"
"D0 7,32,46,1024\nMvaa 176,228,220\nRa 152,46,18432\nMvaa 176,224,216\nRa 156,47,18432\nAR 60,50\nMvaa 180,232,216\nTa 160,12,32768\nMvaa 180,236,220\nTa 162,12,34816\n"
"D0 8,32,46,2048\nMvaa 148,236,212\nTa 164,12,49152\nAR 61,51\nMvaa 148,232,208\nTa 166,12,51200\nMvaa 116,232,200\nTa 168,13,32768\nCV 217,60,61\nMvaa 116,236,204\n"
"Ta 170,13,34816\nD0 9,32,46,3072\nMvaa 84,236,196\nTa 172,13,49152\nAR 60,52\nMvaa 84,232,192\nTa 174,13,51200\ns_add_u32 m0, s47, 16384\ns_add_u32 s46, s51, 2359296\n"
"Mvaa 88,240,192\nTa 176,14,32768\nAR 61,53\nMvaa 88,244,196\nTa 178,14,34816\nD0 6,32,46,0\nMvaa 120,244,204\nTa 180,14,49152\nMvaa 120,240,200\nTa 182,14,51200\nCV 218,60,61\n"
"Mvaa 152,240,208\nTa 184,15,32768\nMvaa 152,244,212\nTa 186,15,34816\nAR 60,54\nD0 7,32,46,1024\nMvaa 184,244,220\nTa 188,15,49152\nMvaa 184,240,216\nTa 190,15,51200\nAR 61,55\n"
"Mvaa 188,248,216\nCV 219,60,61\nMvaa 188,252,220\nAR 60,56\nD0 8,32,46,2048\nMvaa 156,252,212\nAR 61,57\nCV 220,60,61\nMvaa 156,248,208\nAR 60,58\nMvaa 124,248,200\nAR 61,59\n"
"CV 221,60,61\nMvaa 124,252,204\nAR 60,60\nD0 9,32,46,3072\nMvaa 92,252,196\nAR 61,61\nCV 222,60,61\nMvaa 92,248,192\nWL 0\nMvaa 64,160,128\nTa 224,16,32768\nAR 60,62\n"
"Mvaa 64,164,132\nTa 226,16,34816\ns_add_u32 m0, s47, -32768\ns_add_u32 s46, s52, 640\nD0 40,28,46,0\nMvaa 96,164,140\nAR 61,63\nMvaa 96,160,136\nTa 228,16,49152\nCV 223,60,61\n"
"Mvaa 128,160,144\nTa 230,16,51200\nMvaa 128,164,148\nD0 41,28,46,1024\nMvaa 160,164,156\nTa 232,17,32768\nAR 60,126\nMvaa 160,160,152\nTa 234,17,34816\nMvaa 164,168,152\n"
"AR 61,127\nCV 60,60,61\nMvaa 164,172,156\nTa 236,17,49152\nD0 42,28,46,2048\nMvaa 132,172,148\nTa 238,17,51200\nMvaa 132,168,144\nAW 127,60\nAR 60,124\nMvaa 100,168,136\n"
"Ta 240,18,32768\nMvaa 100,172,140\nTa 242,18,34816\nD0 43,28,46,3072\nMvaa 68,172,132\nSv 192,1,16,77,0\nAR 61,125\nMvaa 68,168,128\nTa 244,18,49152\nMvaa 72,176,128\n"
"Ta 246,18,51200\nCV 60,60,61\nMvaa 72,180,132\nAW 126,60\nMvaa 104,180,140\nTa 248,19,32768\nMvaa 104,176,136\nTa 250,19,34816\nSv 196,1,16,77,1024\nMvaa 136,176,144\nAR 60,122\n"
"Mvaa 136,180,148\nTa 252,19,49152\nMvaa 168,180,156\nTa 254,19,51200\nAR 61,123\nMvaa 168,176,152\nCV 60,60,61\nMvaa 172,184,152\nSv 200,1,16,77,2048\nAW 125,60\nMvaa 172,188,156\n"
"AR 60,120\nMvaa 140,188,148\nAR 61,121\nMvaa 140,184,144\nCV 60,60,61\nAW 124,60\nMvaa 108,184,136\nAR 60,118\nMvaa 108,188,140\nSv 204,1,16,77,3072\nMvaa 76,188,132\nAR 61,119\n"
"CV 60,60,61\nMvaa 76,184,128\ns_waitcnt vmcnt(17) lgkmcnt(0)\ns_barrier\nMvaa 80,224,128\nRa 192,44,49152\nAW 123,60\nMvaa 80,228,132\nRa 196,45,49152\n"
"buffer_store_dword v25, off, s[16:19], s54 offset:8 sc0 sc1\ns_add_u32 m0, s47, 32768\ns_add_u32 s46, s51, 2621440\nD0 6,32,46,0\nMvaa 112,228,140\nRa 200,44,51200\n"
"Mvaa 112,224,136\nRa 204,45,51200\nAR 60,116\nMvaa 144,224,144\nRa 208,46,49152\nMvaa 144,228,148\nRa 212,47,49152\nD0 7,32,46,1024\nMvaa 176,228,156\nRa 216,46,51200\nAR 61,117\n"
"Mvaa 176,224,152\nRa 220,47,51200\nMvaa 180,232,152\nTa 160,32,0\nCV 60,60,61\nMvaa 180,236,156\nTa 162,32,2048\nD0 8,32,46,2048\nMvaa 148,236,148\nTa 164,32,16384\n"
"Mvaa 148,232,144\nTa 166,32,18432\nAW 122,60\nMvaa 116,232,136\nTa 168,33,0\nMvaa 116,236,140\nTa 170,33,2048\nD0 9,32,46,3072\nMvaa 84,236,132\nTa 172,33,16384\nAR 60,114\n"
"Mvaa 84,232,128\nTa 174,33,18432\ns_add_u32 m0, s47, 49152\ns_add_u32 s46, s51, 2883584\nMvaa 88,240,128\nTa 176,34,0\nAR 61,115\nMvaa 88,244,132\nTa 178,34,2048\nD0 6,32,46,0\n"
"Mvaa 120,244,140\nTa 180,34,16384\nMvaa 120,240,136\nTa 182,34,18432\nCV 60,60,61\nMvaa 152,240,144\nTa 184,35,0\nMvaa 152,244,148\nTa 186,35,2048\nD0 7,32,46,1024\n"
"Mvaa 184,244,156\nTa 188,35,16384\nAW 121,60\nMvaa 184,240,152\nTa 190,35,18432\nMvaa 188,248,152\nAR 60,112\nAR 61,113\nMvaa 188,252,156\nD0 8,32,46,2048\nMvaa 156,252,148\n"
"CV 60,60,61\nMvaa 156,248,144\nAW 120,60\nAR 60,110\nMvaa 124,248,136\nAR 61,111\nMvaa 124,252,140\nD0 9,32,46,3072\nMvaa 92,252,132\nCV 60,60,61\nAW 119,60\nMvaa 92,248,128\n"
"WL 0\nMvaa 64,160,192\nTa 224,36,0\nAR 60,108\nMvaa 64,164,196\nTa 226,36,2048\ns_add_u32 m0, s47, -49152\ns_add_u32 s46, s52, 768\nD0 40,28,46,0\nMvaa 96,164,204\nAR 61,109\n"
"Mvaa 96,160,200\nTa 228,36,16384\nCV 60,60,61\nMvaa 128,160,208\nTa 230,36,18432\nMvaa 128,164,212\nD0 41,28,46,1024\nMvaa 160,164,220\nTa 232,37,0\nAW 118,60\nMvaa 160,160,216\n"
"Ta 234,37,2048\nMvaa 164,168,216\nAR 60,106\nAR 61,107\nMvaa 164,172,220\nTa 236,37,16384\nD0 42,28,46,2048\nMvaa 132,172,212\nTa 238,37,18432\nMvaa 132,168,208\nCV 60,60,61\n"
"AW 117,60\nMvaa 100,168,200\nTa 240,38,0\nMvaa 100,172,204\nTa 242,38,2048\nD0 43,28,46,3072\nMvaa 68,172,196\ns_add_u32 s77, s77, s55\nSv 208,1,16,77,0\nMvaa 68,168,192\n"
"Ta 244,38,16384\nMvaa 72,176,192\nTa 246,38,18432\nAR 60,104\nMvaa 72,180,196\nAR 61,105\nMvaa 104,180,204\nTa 248,39,0\nMvaa 104,176,200\nTa 250,39,2048\nSv 212,1,16,77,1024\n"
"Mvaa 136,176,208\nCV 60,60,61\nMvaa 136,180,212\nTa 252,39,16384\nMvaa 168,180,220\nTa 254,39,18432\nAW 116,60\nMvaa 168,176,216\nAR 60,102\nMvaa 172,184,216\nSv 216,1,16,77,2048\n"
"AR 61,103\nMvaa 172,188,220\nCV 60,60,61\nMvaa 140,188,212\nAW 115,60\nMvaa 140,184,208\nAR 60,100\nAR 61,101\nMvaa 108,184,200\nCV 60,60,61\nMvaa 108,188,204\n"
"Sv 220,1,16,77,3072\nMvaa 76,188,196\nAW 114,60\nAR 60,98\nMvaa 76,184,192\ns_waitcnt vmcnt(25) lgkmcnt(0)\ns_barrier\nMvaa 80,224,192\nRa 128,44,0\nv_and_b32 v27, 0x70, v1\n"
"buffer_load_dword v27, v27, s[16:19], s61 offen offset:128 sc0 sc1\nMvaa 80,228,196\nRa 132,45,0\ns_add_u32 m0, s47, 65536\ns_add_u32 s46, s51, 3145728\nD0 6,32,46,0\n"
"Mvaa 112,228,204\nRa 136,44,2048\nMvaa 112,224,200\nRa 140,45,2048\nAR 61,99\nMvaa 144,224,208\nRa 144,46,0\nMvaa 144,228,212\nRa 148,47,0\nD0 7,32,46,1024\nMvaa 176,228,220\n"
"Ra 152,46,2048\nCV 60,60,61\nMvaa 176,224,216\nRa 156,47,2048\nMvaa 180,232,216\nTa 160,12,0\nAW 113,60\nMvaa 180,236,220\nTa 162,12,2048\nD0 8,32,46,2048\nMvaa 148,236,212\n"
"Ta 164,12,16384\nMvaa 148,232,208\nTa 166,12,18432\nAR 60,96\nMvaa 116,232,200\nTa 168,13,0\nMvaa 116,236,204\nTa 170,13,2048\nD0 9,32,46,3072\nMvaa 84,236,196\nTa 172,13,16384\n"
"AR 61,97\nMvaa 84,232,192\nTa 174,13,18432\ns_add_u32 m0, s47, 81920\ns_add_u32 s46, s51, 3407872\nMvaa 88,240,192\nTa 176,14,0\nCV 60,60,61\nMvaa 88,244,196\nTa 178,14,2048\n"
"D0 6,32,46,0\nMvaa 120,244,204\nTa 180,14,16384\nMvaa 120,240,200\nTa 182,14,18432\nAW 112,60\nMvaa 152,240,208\nTa 184,15,0\nMvaa 152,244,212\nTa 186,15,2048\nD0 7,32,46,1024\n"
"Mvaa 184,244,220\nTa 188,15,16384\nAR 60,94\nMvaa 184,240,216\nTa 190,15,18432\nMvaa 188,248,216\nAR 61,95\nCV 60,60,61\nMvaa 188,252,220\nD0 8,32,46,2048\nMvaa 156,252,212\n"
"AW 111,60\nMvaa 156,248,208\nAR 60,92\nAR 61,93\nMvaa 124,248,200\nCV 60,60,61\nMvaa 124,252,204\nD0 9,32,46,3072\nMvaa 92,252,196\nAW 110,60\nAR 60,90\nMvaa 92,248,192\nWL 0\n"
"Mvaa 64,160,128\nTa 224,16,0\nAR 61,91\nMvaa 64,164,132\nTa 226,16,2048\ns_add_u32 s6, s56, 16384\nbuffer_load_dwordx4 v[224:227], v1, s[16:19], s6 offen offset:0 sc1\n"
"Mvaa 96,164,140\nCV 60,60,61\nMvaa 96,160,136\nTa 228,16,16384\nAW 109,60\nMvaa 128,160,144\nTa 230,16,18432\nMvaa 128,164,148\n"
"buffer_load_dwordx4 v[228:231], v1, s[16:19], s6 offen offset:1024 sc1\nMvaa 160,164,156\nTa 232,17,0\nAR 60,88\nMvaa 160,160,152\nTa 234,17,2048\nMvaa 164,168,152\nAR 61,89\n"
"CV 60,60,61\nMvaa 164,172,156\nTa 236,17,16384\nbuffer_load_dwordx4 v[232:235], v1, s[16:19], s6 offen offset:2048 sc1\nMvaa 132,172,148\nTa 238,17,18432\nMvaa 132,168,144\n"
"AW 108,60\nAR 60,86\nMvaa 100,168,136\nTa 240,18,0\nMvaa 100,172,140\nTa 242,18,2048\nbuffer_load_dwordx4 v[236:239], v1, s[16:19], s6 offen offset:3072 sc1\nMvaa 68,172,132\n"
"AR 61,87\nCV 60,60,61\nMvaa 68,168,128\nTa 244,18,16384\nMvaa 72,176,128\nTa 246,18,18432\nAW 107,60\nMvaa 72,180,132\nAR 60,84\nMvaa 104,180,140\nTa 248,19,0\nMvaa 104,176,136\n"
"Ta 250,19,2048\nAR 61,85\nMvaa 136,176,144\nCV 60,60,61\nMvaa 136,180,148\nTa 252,19,16384\nMvaa 168,180,156\nTa 254,19,18432\nAW 106,60\nMvaa 168,176,152\nAR 60,82\n"
"Mvaa 172,184,152\nAR 61,83\nCV 60,60,61\nMvaa 172,188,156\nAW 105,60\nMvaa 140,188,148\nAR 60,80\nMvaa 140,184,144\nAR 61,81\nCV 60,60,61\nMvaa 108,184,136\nAW 104,60\n"
"Mvaa 108,188,140\nAR 60,78\nMvaa 76,188,132\nAR 61,79\nCV 60,60,61\nMvaa 76,184,128\ns_waitcnt vmcnt(12) lgkmcnt(0)\ns_barrier\nMvaa 80,224,128\nRa 192,44,32768\n"
"v_cmp_ne_u32 vcc, s15, v27\ns_nop 0\ns_cbranch_vccnz .S422\n.S423:\ns_sub_u32 s45, s86, 131072\ns_cmp_eq_u32 s79, 1\ns_cselect_b32 s86, s94, s86\ns_add_u32 m0, s47, -65536\n"
"s_add_u32 s46, s45, 0x0\nMvaa 80,228,132\nRa 196,45,32768\nMvaa 112,228,140\nRa 200,44,34816\nD1 1,16,46,0\nMvaa 112,224,136\nRa 204,45,34816\nAW 103,60\nMvaa 144,224,144\n"
"Ra 208,46,32768\nMvaa 144,228,148\nRa 212,47,32768\nMvaa 176,228,156\nRa 216,46,34816\nD1 1,16,46,1024\nMvaa 176,224,152\nRa 220,47,34816\nMvaa 180,232,152\nTa 160,12,32768\n"
"AR 60,76\nMvaa 180,236,156\nTa 162,12,34816\nMvaa 148,236,148\nTa 164,12,49152\nD1 1,16,46,2048\nMvaa 148,232,144\nTa 166,12,51200\nAR 61,77\nMvaa 116,232,136\nTa 168,13,32768\n"
"Mvaa 116,236,140\nTa 170,13,34816\nMvaa 84,236,132\nTa 172,13,49152\nD1 1,16,46,3072\nMvaa 84,232,128\nTa 174,13,51200\nMvaa 88,240,128\nTa 176,14,32768\ns_add_u32 s6, s56, 20480\n"
"Mvaa 88,244,132\nTa 178,14,34816\nMvaa 120,244,140\nTa 180,14,49152\nbuffer_load_dwordx4 v[240:243], v1, s[16:19], s6 offen offset:0 sc1\nMvaa 120,240,136\nTa 182,14,51200\n"
"CV 60,60,61\nMvaa 152,240,144\nTa 184,15,32768\nMvaa 152,244,148\nTa 186,15,34816\nMvaa 184,244,156\nTa 188,15,49152\n"
"buffer_load_dwordx4 v[244:247], v1, s[16:19], s6 offen offset:1024 sc1\nMvaa 184,240,152\nTa 190,15,51200\nMvaa 188,248,152\nAW 102,60\nAR 60,74\nMvaa 188,252,156\nAR 61,75\n"
"Mvaa 156,252,148\nbuffer_load_dwordx4 v[248:251], v1, s[16:19], s6 offen offset:2048 sc1\nMvaa 156,248,144\nCV 60,60,61\nAW 101,60\nMvaa 124,248,136\nAR 60,72\nMvaa 124,252,140\n"
"AR 61,73\nMvaa 92,252,132\nbuffer_load_dwordx4 v[252:255], v1, s[16:19], s6 offen offset:3072 sc1\nCV 60,60,61\nMvaa 92,248,128\nWL 0\nMvaa 64,160,192\nTa 224,16,32768\n"
"s_add_u32 s6, s56, 0\nMvaa 64,164,196\nTa 226,16,34816\nMvaa 96,164,204\nbuffer_load_dwordx4 v[192:195], v1, s[16:19], s6 offen offset:0 sc1\nMvaa 96,160,200\nTa 228,16,49152\n"
"AW 100,60\nMvaa 128,160,208\nTa 230,16,51200\nMvaa 128,164,212\nAR 60,70\nMvaa 160,164,220\nTa 232,17,32768\n"
"buffer_load_dwordx4 v[196:199], v1, s[16:19], s6 offen offset:1024 sc1\nMvaa 160,160,216\nTa 234,17,34816\nMvaa 164,168,216\nAR 61,71\nCV 60,60,61\nMvaa 164,172,220\n"
"Ta 236,17,49152\nMvaa 132,172,212\nTa 238,17,51200\nbuffer_load_dwordx4 v[200:203], v1, s[16:19], s6 offen offset:2048 sc1\nMvaa 132,168,208\nAW 99,60\nAR 60,68\nMvaa 100,168,200\n"
"Ta 240,18,32768\nMvaa 100,172,204\nTa 242,18,34816\nMvaa 68,172,196\nbuffer_load_dwordx4 v[204:207], v1, s[16:19], s6 offen offset:3072 sc1\nAR 61,69\nMvaa 68,168,192\n"
"Ta 244,18,49152\nMvaa 72,176,192\nTa 246,18,51200\nCV 60,60,61\nMvaa 72,180,196\nAW 98,60\nMvaa 104,180,204\nTa 248,19,32768\nMvaa 104,176,200\nTa 250,19,34816\nAR 60,66\n"
"Mvaa 136,176,208\nAR 61,67\nMvaa 136,180,212\nTa 252,19,49152\nMvaa 168,180,220\nTa 254,19,51200\nCV 60,60,61\nMvaa 168,176,216\nAW 97,60\nMvaa 172,184,216\nAR 60,64\nAR 61,65\n"
"Mvaa 172,188,220\nCV 60,60,61\nMvaa 140,188,212\nAW 96,60\nv_add_u32 v2, 0x0, v1\nMvaa 140,184,208\ns_lshr_b32 s2, s59, 1\nMvaa 108,184,200\nv_add_u32 v24, s2, v2\n"
"v_xor_b32 v26, 0x1000, v24\nMvaa 108,188,204\nv_lshrrev_b32 v20, 4, v0\nMvaa 76,188,196\nv_lshlrev_b32 v20, 2, v20\nv_and_b32 v0, 15, v0\nMvaa 76,184,192\n"
"s_waitcnt vmcnt(16) lgkmcnt(0)\ns_barrier\nMvaa 80,224,192\nRa 128,44,16384\nv_sub_u32 v0, v0, v20\nMvaa 80,228,196\nRa 132,45,16384\n"
"buffer_store_dword v25, off, s[16:19], s54 offset:12 sc0 sc1\ns_add_u32 s6, s56, 4096\nbuffer_load_dwordx4 v[208:211], v1, s[16:19], s6 offen offset:0 sc1\nMvaa 112,228,204\n"
"Ra 136,44,18432\ns_lshl_b32 s2, s11, 4\nMvaa 112,224,200\nRa 140,45,18432\nMvaa 144,224,208\nRa 144,46,16384\nv_add_u32 v3, s2, v0\nMvaa 144,228,212\nRa 148,47,16384\n"
"buffer_load_dwordx4 v[212:215], v1, s[16:19], s6 offen offset:1024 sc1\nMvaa 176,228,220\nRa 152,46,18432\nv_subrev_u32 v0, 32, v3\nMvaa 176,224,216\nRa 156,47,18432\n"
"Mvaa 180,232,216\nTa 160,32,0\ns_lshl_b32 s2, s11, 13\nMvaa 180,236,220\nTa 162,32,2048\nbuffer_load_dwordx4 v[216:219], v1, s[16:19], s6 offen offset:2048 sc1\nMvaa 148,236,212\n"
"Ta 164,32,16384\ns_add_u32 s2, s2, 0x20000\nMvaa 148,232,208\nTa 166,32,18432\nMvaa 116,232,200\nTa 168,33,0\nv_add_u32 v22, s2, v1\nMvaa 116,236,204\nTa 170,33,2048\n"
"buffer_load_dwordx4 v[220:223], v1, s[16:19], s6 offen offset:3072 sc1\nMvaa 84,236,196\nTa 172,33,16384\nv_xor_b32 v23, 0x2000, v22\nMvaa 84,232,192\nTa 174,33,18432\n"
"s_add_u32 m0, s47, -32768\ns_add_u32 s46, s45, 0x8000\nMvaa 88,240,192\nTa 176,34,0\nv_mov_b32 v4, 0\nMvaa 88,244,196\nTa 178,34,2048\nD1 1,16,46,0\nMvaa 120,244,204\n"
"Ta 180,34,16384\nv_mov_b32 v5, 0\nMvaa 120,240,200\nTa 182,34,18432\nMvaa 152,240,208\nTa 184,35,0\nv_mov_b32 v6, 0\nMvaa 152,244,212\nTa 186,35,2048\nD1 1,16,46,1024\n"
"Mvaa 184,244,220\nTa 188,35,16384\nv_mov_b32 v7, 0\nMvaa 184,240,216\nTa 190,35,18432\nMvaa 188,248,216\nv_mov_b32 v8, 0\nv_mov_b32 v9, 0\nMvaa 188,252,220\nD1 1,16,46,2048\n"
"Mvaa 156,252,212\nv_mov_b32 v10, 0\nv_mov_b32 v11, 0\nMvaa 156,248,208\nv_mov_b32 v12, 0\nMvaa 124,248,200\nv_mov_b32 v13, 0\nv_mov_b32 v14, 0\nMvaa 124,252,204\nD1 1,16,46,3072\n"
"Mvaa 92,252,196\nv_mov_b32 v15, 0\nv_mov_b32 v16, 0\nMvaa 92,248,192\nWL 0\nMvaa 64,160,128\nTa 224,36,0\nv_mov_b32 v17, 0\nMvaa 64,164,132\nTa 226,36,2048\n"
"s_add_u32 m0, s47, -16384\ns_add_u32 s46, s45, 0x400000\nD1 1,16,46,0\nMvaa 68,172,132\nv_mov_b32 v18, 0\nv_mov_b32 v19, 0\nMvaa 68,168,128\nTa 228,36,16384\nMvaa 72,176,128\n"
"Ta 230,36,18432\nMvaa 72,180,132\nD1 1,16,46,1024\nMvaa 76,188,132\nTa 232,37,0\nMvaa 76,184,128\nTa 234,37,2048\nMvaa 108,184,136\nv_mul_f32 v64, s57, v64\n"
"v_mul_f32 v65, s57, v65\nMvaa 108,188,140\nTa 236,37,16384\nD1 1,16,46,2048\nMvaa 104,180,140\nTa 238,37,18432\nCV 64,64,65\nMvaa 104,176,136\nv_mul_f32 v66, s57, v66\n"
"v_mul_f32 v67, s57, v67\nMvaa 100,168,136\nTa 240,38,0\nCV 65,66,67\nMvaa 100,172,140\nTa 242,38,2048\nD1 1,16,46,3072\nMvaa 96,164,140\nv_mul_f32 v68, s57, v68\n"
"v_mul_f32 v69, s57, v69\nMvaa 96,160,136\nTa 244,38,16384\nCV 66,68,69\nMvaa 128,160,144\nTa 246,38,18432\nv_mul_f32 v70, s57, v70\nMvaa 128,164,148\nv_mul_f32 v71, s57, v71\n"
"CV 67,70,71\nMvaa 132,172,148\nTa 248,39,0\nAW 192,64\nMvaa 132,168,144\nTa 250,39,2048\nAW 193,65\nMvaa 136,176,144\nAW 194,66\nAW 195,67\nMvaa 136,180,148\nTa 252,39,16384\n"
"v_mul_f32 v72, s57, v72\nMvaa 140,188,148\nTa 254,39,18432\nv_mul_f32 v73, s57, v73\nMvaa 140,184,144\nCV 72,72,73\nv_mul_f32 v74, s57, v74\nMvaa 172,184,152\n"
"v_mul_f32 v75, s57, v75\nCV 73,74,75\nMvaa 172,188,156\nv_mul_f32 v76, s57, v76\nv_mul_f32 v77, s57, v77\nMvaa 168,180,156\nCV 74,76,77\nv_mul_f32 v78, s57, v78\nMvaa 168,176,152\n"
"v_mul_f32 v79, s57, v79\nCV 75,78,79\nMvaa 164,168,152\nAW 200,72\nAW 201,73\nMvaa 164,172,156\nAW 202,74\nAW 203,75\nMvaa 160,164,156\nv_mul_f32 v96, s57, v96\n"
"v_mul_f32 v97, s57, v97\nMvaa 160,160,152\ns_waitcnt vmcnt(21) lgkmcnt(0)\ns_barrier\nMvaa 80,224,128\nCV 96,96,97\nv_mul_f32 v98, s57, v98\nMvaa 80,228,132\ns_add_u32 m0, s47, 0\n"
"s_add_u32 s46, s86, 0xffff0000\nD1 1,16,46,0\nMvaa 84,236,132\nv_mul_f32 v99, s57, v99\nCV 97,98,99\nMvaa 84,232,128\nv_mul_f32 v100, s57, v100\nv_mul_f32 v101, s57, v101\n"
"Mvaa 88,240,128\nCV 98,100,101\nv_mul_f32 v102, s57, v102\nMvaa 88,244,132\nD1 1,16,46,1024\nv_mul_f32 v103, s57, v103\nMvaa 92,252,132\nCV 99,102,103\nAW 196,96\nMvaa 92,248,128\n"
"AW 197,97\nAW 198,98\nMvaa 124,248,136\nAW 199,99\nv_mul_f32 v128, s57, v128\nMvaa 124,252,140\nD1 1,16,46,2048\nv_mul_f32 v129, s57, v129\nMvaa 120,244,140\nCV 128,128,129\n"
"v_mul_f32 v130, s57, v130\nMvaa 120,240,136\nv_mul_f32 v131, s57, v131\nCV 129,130,131\nMvaa 116,232,136\nRa 160,24,0\nv_mul_f32 v132, s57, v132\nMvaa 116,236,140\n"
"D1 1,16,46,3072\nv_mul_f32 v133, s57, v133\nMvaa 112,228,140\nRa 164,24,1024\nCV 130,132,133\nMvaa 112,224,136\ns_add_u32 m0, s47, 16384\ns_add_u32 s46, s45, 0x408000\n"
"Mvaa 144,224,144\nRa 168,24,2048\nv_mul_f32 v134, s57, v134\nMvaa 144,228,148\nD1 1,16,46,0\nv_mul_f32 v135, s57, v135\nMvaa 148,236,148\nRa 172,24,3072\nCV 131,134,135\n"
"Mvaa 148,232,144\nds_write_b128 v23, v[128:131] offset:0\nv_mul_f32 v80, s57, v80\nMvaa 152,240,144\nRa 176,26,0\nv_mul_f32 v81, s57, v81\nMvaa 152,244,148\nD1 1,16,46,1024\n"
"CV 80,80,81\nMvaa 156,252,148\nRa 180,26,1024\nv_mul_f32 v82, s57, v82\nMvaa 156,248,144\nv_mul_f32 v83, s57, v83\nCV 81,82,83\nMvaa 188,248,152\nRa 184,26,2048\n"
"v_mul_f32 v84, s57, v84\nMvaa 188,252,156\nD1 1,16,46,2048\nv_mul_f32 v85, s57, v85\nMvaa 184,244,156\nRa 188,26,3072\nCV 82,84,85\nMvaa 184,240,152\nv_mul_f32 v86, s57, v86\n"
"v_mul_f32 v87, s57, v87\nMvaa 180,232,152\nCV 83,86,87\nAW 208,80\nMvaa 180,236,156\nD1 1,16,46,3072\nAW 209,81\nMvaa 176,228,156\nAW 210,82\nAW 211,83\nMvaa 176,224,152\n"
"s_waitcnt vmcnt(16) lgkmcnt(5)\ns_cmp_eq_u32 s79, 1\ns_cbranch_scc0 .E425\nv_mov_b32 v4, s97\nv_mov_b32 v5, s97\nv_mov_b32 v6, s97\nv_mov_b32 v7, s97\nv_mov_b32 v12, s97\n"
"v_mov_b32 v13, s97\nv_mov_b32 v14, s97\nv_mov_b32 v15, s97\nv_cmpx_gt_i32 vcc, 19, v3\nv_mov_b32 v19, s97\nv_cmpx_gt_i32 vcc, 18, v3\nv_mov_b32 v18, s97\n"
"v_cmpx_gt_i32 vcc, 17, v3\nv_mov_b32 v17, s97\nv_cmpx_gt_i32 vcc, 16, v3\nv_mov_b32 v16, s97\nv_cmpx_gt_i32 vcc, 3, v3\nv_mov_b32 v11, s97\nv_cmpx_gt_i32 vcc, 2, v3\n"
"v_mov_b32 v10, s97\nv_cmpx_gt_i32 vcc, 1, v3\nv_mov_b32 v9, s97\nv_cmpx_gt_i32 vcc, 0, v3\nv_mov_b32 v8, s97\ns_mov_b64 exec, -1\ns_nop 2\n.E425:\ns_nop 0\nCvav 32,160,192,4\n"
"Ra 128,24,8192\nv_mul_f32 v88, s57, v88\nMvav 32,164,200\nv_mul_f32 v89, s57, v89\nCV 88,88,89\nMvav 32,168,208\nRa 132,24,9216\nv_mul_f32 v90, s57, v90\nMvav 32,172,216\n"
"v_mul_f32 v91, s57, v91\nCV 89,90,91\nCvav 36,172,220,8\nRa 136,24,10240\nv_mul_f32 v92, s57, v92\nMvav 36,168,212\ns_add_u32 m0, s89, 32768\nv_mul_f32 v93, s57, v93\n"
"Mvav 36,164,204\nRa 140,24,11264\nWL 4\ns_barrier\nMvav 36,160,196\nCV 90,92,93\nv_mul_f32 v94, s57, v94\nMvav 36,176,228\nRa 144,26,8192\nSa 96,1,16,78,0\nMvav 36,180,236\n"
"s_add_u32 s46, s86, s88\nv_mul_f32 v95, s57, v95\nMvav 36,184,244\nRa 148,26,9216\nCV 91,94,95\nMvav 36,188,252\nAW 216,88\nAW 217,89\nMvav 32,188,248\nRa 152,26,10240\nAW 218,90\n"
"Mvav 32,184,240\nD1 1,16,46,0\nAW 219,91\nMvav 32,180,232\nRa 156,26,11264\nWL 4\nMvav 32,176,224\nv_mov_b32 v86, 0\nv_mov_b32 v87, 0\nCvav 40,128,192,12\nSa 100,1,16,78,1024\n"
"v_mov_b32 v88, 0\nMvav 40,132,200\ns_cmp_eq_u32 s99, -1\nMvav 40,136,208\ns_cselect_b32 s87, s95, s87\nv_mov_b32 v89, 0\nv_mul_f32 v136, s57, v136\nv_mul_f32 v137, s57, v137\n"
"CV 136,136,137\nv_mul_f32 v138, s57, v138\nv_mul_f32 v139, s57, v139\nMvav 40,140,216\nCV 137,138,139\nv_mul_f32 v140, s57, v140\nv_mul_f32 v141, s57, v141\nCV 138,140,141\n"
"v_mul_f32 v142, s57, v142\nv_mul_f32 v143, s57, v143\nCvav 44,140,220,16\nv_and_b32 v27, 0x70, v1\nbuffer_load_dword v27, v27, s[16:19], s61 offen offset:136 sc0 sc1\n"
"CV 139,142,143\nds_write_b128 v23, v[136:139] offset:2048\nv_mul_f32 v144, s57, v144\nv_mul_f32 v145, s57, v145\nCV 144,144,145\nMvav 44,136,212\nD1 1,16,46,1024\n"
"v_mul_f32 v146, s57, v146\nv_mul_f32 v147, s57, v147\nCV 145,146,147\nv_mul_f32 v148, s57, v148\nv_mul_f32 v149, s57, v149\nMvav 44,132,204\nWL 1\nCV 146,148,149\n"
"v_mul_f32 v150, s57, v150\nv_mul_f32 v151, s57, v151\nCV 147,150,151\nds_write_b128 v23, v[144:147] offset:4096\nv_mul_f32 v152, s57, v152\nMvav 44,128,196\n"
"v_mul_f32 v153, s57, v153\nCV 152,152,153\nv_mul_f32 v154, s57, v154\nv_mul_f32 v155, s57, v155\nCV 153,154,155\nv_mul_f32 v156, s57, v156\nMvav 44,144,228\nSa 104,1,16,78,2048\n"
"v_mul_f32 v157, s57, v157\nCV 154,156,157\nv_mul_f32 v158, s57, v158\nv_mul_f32 v159, s57, v159\nCV 155,158,159\nds_write_b128 v23, v[152:155] offset:6144\nMvav 44,148,236\n"
"s_add_u32 s88, s88, 32768\nEX 80,32\nv_mul_f32 v160, s57, v160\nv_mul_f32 v161, s57, v161\nCV 160,160,161\nMvav 44,152,244\nv_mul_f32 v162, s57, v162\nv_mul_f32 v163, s57, v163\n"
"CV 161,162,163\nv_mul_f32 v164, s57, v164\nv_mul_f32 v165, s57, v165\nCV 162,164,165\nv_mul_f32 v166, s57, v166\nMvav 44,156,252\nEX 81,33\nv_mul_f32 v167, s57, v167\n"
"CV 163,166,167\nds_write_b128 v23, v[160:163] offset:1024\nMvav 40,156,248\nv_mul_f32 v168, s57, v168\nv_mul_f32 v169, s57, v169\nCV 168,168,169\nv_mul_f32 v170, s57, v170\n"
"v_mul_f32 v171, s57, v171\nCV 169,170,171\nv_mul_f32 v172, s57, v172\nMvav 40,152,240\nD1 1,16,46,2048\nv_mul_f32 v173, s57, v173\nCV 170,172,173\nv_mul_f32 v174, s57, v174\n"
"v_mul_f32 v175, s57, v175\nCV 171,174,175\nMvav 40,148,232\nds_write_b128 v23, v[168:171] offset:3072\nv_mul_f32 v176, s57, v176\nv_mul_f32 v177, s57, v177\nCV 176,176,177\n"
"v_mul_f32 v178, s57, v178\nv_mul_f32 v179, s57, v179\nCV 177,178,179\nMvav 40,144,224\nAD 84,80,81\nCV 64,80,81\nv_mul_f32 v180, s57, v180\nv_mul_f32 v181, s57, v181\n"
"CV 178,180,181\nv_mul_f32 v182, s57, v182\nSa 108,1,16,78,3072\nv_mul_f32 v183, s57, v183\nCV 179,182,183\nds_write_b128 v23, v[176:179] offset:5120\nv_mul_f32 v184, s57, v184\n"
"v_mul_f32 v185, s57, v185\nCV 184,184,185\nEX 82,34\nv_mul_f32 v186, s57, v186\nv_mul_f32 v187, s57, v187\nCV 185,186,187\nv_mul_f32 v188, s57, v188\nv_mul_f32 v189, s57, v189\n"
"CV 186,188,189\nv_mul_f32 v190, s57, v190\nv_mul_f32 v191, s57, v191\nCV 187,190,191\nds_write_b128 v23, v[184:187] offset:7168\ns_waitcnt vmcnt(16) lgkmcnt(0)\nEX 83,35\n"
"AD 84,84,82\nD1 1,16,46,3072\ns_add_u32 m0, s89, 49152\ns_barrier\nAD 84,84,83\nCV 65,82,83\nRa 128,24,32768\nRa 224,22,0\nRa 228,22,1024\nRa 232,22,2048\ns_add_u32 s46, s87, s88\n"
"EX 80,36\nRa 132,24,33792\nRa 236,22,3072\nRa 240,22,4096\nEX 81,37\nRa 244,22,5120\nRa 136,24,34816\nRa 248,22,6144\nRa 252,22,7168\nD1 1,16,46,0\nv_mul_f32 v104, s57, v104\n"
"v_mul_f32 v105, s57, v105\nRa 140,24,35840\nCV 104,104,105\nv_mul_f32 v106, s57, v106\nv_mul_f32 v107, s57, v107\nAD 85,80,81\nCV 68,80,81\nCV 105,106,107\nRa 144,26,32768\n"
"v_mul_f32 v108, s57, v108\nv_mul_f32 v109, s57, v109\nCV 106,108,109\ns_cmp_eq_u32 s13, 7\nEX 82,38\nRa 148,26,33792\ns_cbranch_scc0 .E412\nv_cmpx_gt_i32 vcc, 19, v0\n"
"v_mov_b32 v19, s97\nv_cmpx_gt_i32 vcc, 18, v0\nv_mov_b32 v18, s97\nv_cmpx_gt_i32 vcc, 17, v0\nv_mov_b32 v17, s97\nv_cmpx_gt_i32 vcc, 16, v0\nv_mov_b32 v16, s97\n"
"v_cmpx_gt_i32 vcc, 3, v0\nv_mov_b32 v11, s97\nv_cmpx_gt_i32 vcc, 2, v0\nv_mov_b32 v10, s97\nv_cmpx_gt_i32 vcc, 1, v0\nv_mov_b32 v9, s97\nv_cmpx_gt_i32 vcc, 0, v0\n"
"v_mov_b32 v8, s97\ns_mov_b64 exec, -1\ns_nop 2\n.E412:\nEX 83,39\nv_mul_f32 v110, s57, v110\nRa 152,26,34816\nAD 85,85,82\nv_mul_f32 v111, s57, v111\nD1 1,16,46,1024\n"
"CV 107,110,111\nAW 204,104\nRa 156,26,35840\nAW 205,105\nAW 206,106\nAW 207,107\nAD 85,85,83\nCV 69,82,83\nv_mul_f32 v112, s57, v112\nRa 160,24,40960\nv_mul_f32 v113, s57, v113\n"
"CV 112,112,113\nv_mul_f32 v114, s57, v114\ns_xor_b32 s89, s89, 0x10000\nEX 80,40\nRa 164,24,41984\nv_mul_f32 v115, s57, v115\nCV 113,114,115\nWL 6\nEX 81,41\n"
"v_mul_f32 v116, s57, v116\nRa 168,24,43008\nAD 84,84,80\nv_mul_f32 v117, s57, v117\nD1 1,16,46,2048\nCV 114,116,117\nv_mul_f32 v118, s57, v118\nRa 172,24,44032\n"
"v_mul_f32 v119, s57, v119\nCV 115,118,119\nAW 212,112\nAD 84,84,81\nCV 66,80,81\nAW 213,113\nCvav 48,128,192,4\nRa 176,26,40960\nAW 214,114\nAW 215,115\nv_mul_f32 v120, s57, v120\n"
"Mvav 48,132,200\nEX 82,42\nv_mul_f32 v121, s57, v121\nMvav 48,136,208\nRa 180,26,41984\nCV 120,120,121\nv_mul_f32 v122, s57, v122\nMvav 48,140,216\nWL 6\nEX 83,43\nWV 8\n"
"v_cmp_ne_u32 vcc, s15, v27\ns_nop 0\ns_cbranch_vccnz .S426\n.S427:\nCvav 52,140,220,8\nRa 184,26,43008\nAD 84,84,82\nv_mul_f32 v123, s57, v123\nMvav 52,136,212\nD1 1,16,46,3072\n"
"s_add_u32 m0, s89, 0\nCV 121,122,123\nMvav 52,132,204\nRa 188,26,44032\nv_mul_f32 v124, s57, v124\nv_mul_f32 v125, s57, v125\nCV 122,124,125\nMvav 52,128,196\nAD 84,84,83\n"
"CV 67,82,83\nv_mul_f32 v126, s57, v126\nMvav 52,144,228\nRa 128,2,49152\ns_add_u32 s78, s78, s55\nSa 112,1,16,78,0\nv_mul_f32 v127, s57, v127\nMvav 52,148,236\n"
"s_add_u32 s46, s86, s88\nEX 80,44\nMvav 52,152,244\nRa 132,2,50176\nv_xor_b32 v24, 0x10000, v24\nCV 123,126,127\nMvav 52,156,252\nWL 6\nEX 81,45\nAW 220,120\nMvav 48,156,248\n"
"Ra 136,2,51200\nAD 85,85,80\nAW 221,121\nMvav 48,152,240\nD1 1,16,46,0\nAW 222,122\nAW 223,123\nMvav 48,148,232\nRa 140,2,52224\nv_mov_b32 v112, -1\nv_mov_b32 v113, -1\n"
"v_mov_b32 v114, -1\nMvav 48,144,224\nAD 85,85,81\nCV 70,80,81\nv_mov_b32 v115, -1\nCvav 56,160,192,12\nRa 144,2,53248\nSa 116,1,16,78,1024\nMvav 56,164,200\nEX 82,46\n"
"Mvav 56,168,208\nRa 148,2,54272\nv_xor_b32 v26, 0x10000, v26\nMvav 56,172,216\nWL 6\nEX 83,47\nCvav 60,172,220,16\nRa 152,2,55296\nAD 85,85,82\nMvav 60,168,212\nD1 1,16,46,1024\n"
"Mvav 60,164,204\nRa 156,2,56320\nMvav 60,160,196\nAD 85,85,83\nCV 71,82,83\nMvav 60,176,228\nRa 160,2,57344\nv_max_f32 v90, v84, v85\nMvav 60,180,236\ns_add_u32 s88, s88, 32768\n"
"EX 80,48\nMvav 60,184,244\nRa 164,2,58368\nv_cmp_lt_f32 vcc, s63, v90\nMvav 60,188,252\nWL 8\nEX 81,49\nMvav 56,188,248\nRa 168,2,59392\ns_cbranch_vccnz .T414\n.T415:\n"
"v_add_f32 v20, s93, v84\nv_mov_b32 v21, v85\nv_mov_b32 v22, v20\nv_mov_b32 v23, v21\nMvav 56,184,240\nD1 1,16,46,2048\nv_permlane32_swap_b32 v20, v22\n"
"v_permlane32_swap_b32 v21, v23\nAD 20,20,22\nAD 21,21,23\nv_mov_b32 v22, v20\nv_mov_b32 v23, v21\nMvav 56,180,232\nRa 172,2,60416\nv_permlane16_swap_b32 v20, v22\n"
"v_permlane16_swap_b32 v21, v23\nAD 20,20,22\nAD 21,21,23\nv_min_f32 v90, v20, v21\nv_cmp_gt_f32 vcc, s64, v90\ns_nop 0\ns_cbranch_vccnz .T416\n.T417:\nAD 86,86,84\n"
"Mvav 56,176,224\nAD 84,80,81\nCV 72,80,81\nZaav 0,128,64\nRa 176,2,61440\nSa 120,1,16,78,2048\nZaav 4,128,68\nEX 82,50\nZaav 12,132,68\nRa 180,2,62464\nZaav 8,132,64\nWL 8\n"
"EX 83,51\nZaav 16,136,64\nRa 184,2,63488\nAD 84,84,82\nZaav 20,136,68\nD1 1,16,46,3072\ns_add_u32 m0, s89, 16384\nZaav 28,140,68\nRa 188,2,64512\ns_barrier\nZaav 24,140,64\n"
"AD 84,84,83\nCV 73,82,83\nZaav 32,144,64\nRa 128,24,0\nSa 124,1,16,78,3072\nZaav 36,144,68\ns_add_u32 s46, s87, s88\nEX 80,52\nZaav 44,148,68\nRa 132,24,1024\n"
"v_xor_b32 v2, 0x10000, v2\nZaav 40,148,64\nWL 8\nEX 81,53\nZaav 48,152,64\nRa 136,24,2048\nAD 87,87,85\nZaav 52,152,68\nD1 1,16,46,0\nZaav 60,156,68\nRa 140,24,3072\n"
"Zaav 56,156,64\nAD 85,80,81\nCV 76,80,81\nZaav 64,160,64\nRa 144,26,0\nZaav 68,160,68\nEX 82,54\nZaav 76,164,68\nRa 148,26,1024\nZaav 72,164,64\nWL 8\nEX 83,55\nZaav 80,168,64\n"
"Ra 152,26,2048\nAD 85,85,82\nZaav 84,168,68\nD1 1,16,46,1024\nZaav 92,172,68\nRa 156,26,3072\nZaav 88,172,64\nAD 85,85,83\nCV 77,82,83\nZaav 96,176,64\nRa 160,24,8192\n"
"Zaav 100,176,68\nEX 80,56\nZaav 108,180,68\nRa 164,24,9216\nZaav 104,180,64\nWL 6\nEX 81,57\nZaav 112,184,64\nRa 168,24,10240\nAD 84,84,80\nv_and_b32 v27, 0x70, v1\n"
"buffer_load_dword v27, v27, s[16:19], s61 offen offset:140 sc0 sc1\nZaav 116,184,68\nD1 1,16,46,2048\nZaav 124,188,68\nRa 172,24,11264\nZaav 120,188,64\nAD 84,84,81\nCV 74,80,81\n"
"s_sub_u32 s99, s99, 1\ns_cmp_eq_u32 s13, 7\ns_cbranch_scc1 .SW410\n.p2align 6\n.A441:\nCvav 32,128,192,4\nRa 176,26,8192\nMvav 32,132,200\nEX 82,58\nMvav 32,136,208\n"
"Ra 180,26,9216\nMvav 32,140,216\nWL 6\nEX 83,59\nCvav 36,140,220,8\nRa 184,26,10240\nAD 84,84,82\nMvav 36,136,212\nD1 1,16,46,3072\ns_add_u32 m0, s89, 32768\nMvav 36,132,204\n"
"Ra 188,26,11264\nMvav 36,128,196\nAD 84,84,83\nCV 75,82,83\nMvav 36,144,228\nRa 128,2,16384\nMvav 36,148,236\ns_add_u32 s46, s86, s88\nEX 80,60\nMvav 36,152,244\nRa 132,2,17408\n"
"Mvav 36,156,252\nWL 6\nEX 81,61\nMvav 32,156,248\nRa 136,2,18432\nAD 85,85,80\nMvav 32,152,240\nD1 1,16,46,0\nMvav 32,148,232\nRa 140,2,19456\nSv 112,1,16,49,0\nMvav 32,144,224\n"
"AD 85,85,81\nCV 78,80,81\nCvav 40,160,192,12\nRa 144,2,20480\nMvav 40,164,200\nEX 82,62\nMvav 40,168,208\nRa 148,2,21504\nMvav 40,172,216\nWL 6\nEX 83,63\nCvav 44,172,220,16\n"
"Ra 152,2,22528\nAD 85,85,82\nMvav 44,168,212\nD1 1,16,46,1024\nMvav 44,164,204\nRa 156,2,23552\nMvav 44,160,196\nAD 85,85,83\nCV 79,82,83\nMvav 44,176,228\nRa 160,2,24576\n"
"v_max_f32 v90, v84, v85\nMvav 44,180,236\ns_add_u32 s88, s88, 32768\nEX 80,32\nMvav 44,184,244\nRa 164,2,25600\nv_cmp_lt_f32 vcc, s63, v90\nMvav 44,188,252\nWL 8\nEX 81,33\n"
"Mvav 40,188,248\nRa 168,2,26624\ns_cbranch_vccnz .T449\n.T450:\nMvav 40,184,240\nD1 1,16,46,2048\nMvav 40,180,232\nRa 172,2,27648\nAD 86,86,84\nMvav 40,176,224\nAD 84,80,81\n"
"CV 64,80,81\nMaav 0,128,72\nRa 176,2,28672\nMaav 4,128,76\nEX 82,34\nMaav 12,132,76\nRa 180,2,29696\nMaav 8,132,72\ns_waitcnt vmcnt(13) lgkmcnt(8)\nEX 83,35\nMaav 16,136,72\n"
"Ra 184,2,30720\nAD 84,84,82\nMaav 20,136,76\nD1 1,16,46,3072\ns_add_u32 m0, s89, 49152\nMaav 28,140,76\nRa 188,2,31744\ns_barrier\nMaav 24,140,72\nAD 84,84,83\nCV 65,82,83\n"
"Maav 32,144,72\nRa 128,24,32768\nMaav 36,144,76\ns_add_u32 s46, s87, s88\nEX 80,36\nMaav 44,148,76\nRa 132,24,33792\nMaav 40,148,72\nWL 8\nEX 81,37\nMaav 48,152,72\n"
"Ra 136,24,34816\nAD 87,87,85\nMaav 52,152,76\nD1 1,16,46,0\nMaav 60,156,76\nRa 140,24,35840\nMaav 56,156,72\nAD 85,80,81\nCV 68,80,81\nMaav 64,160,72\nRa 144,26,32768\n"
"Maav 68,160,76\ns_cmp_eq_u32 s99, 0\nEX 82,38\nMaav 76,164,76\nRa 148,26,33792\ns_cbranch_scc1 .E443\n.E444:\nMaav 72,164,72\nWL 8\nEX 83,39\nMaav 80,168,72\nRa 152,26,34816\n"
"AD 85,85,82\nMaav 84,168,76\nD1 1,16,46,1024\nMaav 92,172,76\nRa 156,26,35840\nMaav 88,172,72\nAD 85,85,83\nCV 69,82,83\nMaav 96,176,72\nRa 160,24,40960\nMaav 100,176,76\n"
"s_xor_b32 s89, s89, 0x10000\nEX 80,40\nMaav 108,180,76\nRa 164,24,41984\nMaav 104,180,72\nWL 6\nEX 81,41\nMaav 112,184,72\nRa 168,24,43008\nAD 84,84,80\nMaav 116,184,76\n"
"D1 1,16,46,2048\nMaav 124,188,76\nRa 172,24,44032\nMaav 120,188,72\nAD 84,84,81\nCV 66,80,81\nCvav 48,128,192,4\nRa 176,26,40960\nMvav 48,132,200\ns_cmp_eq_u32 s99, 0\nEX 82,42\n"
"Mvav 48,136,208\nRa 180,26,41984\ns_cselect_b32 s86, s94, s86\nMvav 48,140,216\nWL 6\nEX 83,43\nCvav 52,140,220,8\nRa 184,26,43008\nAD 84,84,82\nMvav 52,136,212\nD1 1,16,46,3072\n"
"s_add_u32 m0, s89, 0\nMvav 52,132,204\nRa 188,26,44032\nMvav 52,128,196\nAD 84,84,83\nCV 67,82,83\nMvav 52,144,228\nRa 128,2,49152\nMvav 52,148,236\ns_add_u32 s46, s86, s88\n"
"EX 80,44\nMvav 52,152,244\nRa 132,2,50176\nv_xor_b32 v24, 0x10000, v24\nMvav 52,156,252\nWL 6\nEX 81,45\nMvav 48,156,248\nRa 136,2,51200\nAD 85,85,80\nMvav 48,152,240\n"
"D1 1,16,46,0\nMvav 48,148,232\nRa 140,2,52224\nSv 112,1,16,49,1024\nMvav 48,144,224\nAD 85,85,81\nCV 70,80,81\nCvav 56,160,192,12\nRa 144,2,53248\nMvav 56,164,200\n"
"s_add_u32 s49, s49, 131072\nEX 82,46\nMvav 56,168,208\nRa 148,2,54272\nv_xor_b32 v26, 0x10000, v26\nMvav 56,172,216\nWL 6\nEX 83,47\nCvav 60,172,220,16\nRa 152,2,55296\n"
"AD 85,85,82\nMvav 60,168,212\nD1 1,16,46,1024\nMvav 60,164,204\nRa 156,2,56320\nMvav 60,160,196\nAD 85,85,83\nCV 71,82,83\nMvav 60,176,228\nRa 160,2,57344\n"
"v_max_f32 v90, v84, v85\nMvav 60,180,236\ns_add_u32 s88, s88, 32768\nEX 80,48\nMvav 60,184,244\nRa 164,2,58368\nv_cmp_lt_f32 vcc, s63, v90\nMvav 60,188,252\nWL 8\nEX 81,49\n"
"Mvav 56,188,248\nRa 168,2,59392\ns_cbranch_vccnz .T451\n.T452:\nMvav 56,184,240\nD1 1,16,46,2048\nMvav 56,180,232\nRa 172,2,60416\nAD 86,86,84\nMvav 56,176,224\nAD 84,80,81\n"
"CV 72,80,81\nMaav 0,128,64\nRa 176,2,61440\nMaav 4,128,68\nEX 82,50\nMaav 12,132,68\nRa 180,2,62464\nMaav 8,132,64\ns_waitcnt vmcnt(13) lgkmcnt(8)\nEX 83,51\nMaav 16,136,64\n"
"Ra 184,2,63488\nAD 84,84,82\nMaav 20,136,68\nD1 1,16,46,3072\ns_add_u32 m0, s89, 16384\nMaav 28,140,68\nRa 188,2,64512\ns_barrier\nMaav 24,140,64\nAD 84,84,83\nCV 73,82,83\n"
"Maav 32,144,64\nRa 128,24,0\nMaav 36,144,68\ns_add_u32 s46, s87, s88\nEX 80,52\nMaav 44,148,68\nRa 132,24,1024\nv_xor_b32 v2, 0x10000, v2\nMaav 40,148,64\nWL 8\nEX 81,53\n"
"Maav 48,152,64\nRa 136,24,2048\nAD 87,87,85\nMaav 52,152,68\nD1 1,16,46,0\nMaav 60,156,68\nRa 140,24,3072\nMaav 56,156,64\nAD 85,80,81\nCV 76,80,81\nMaav 64,160,64\nRa 144,26,0\n"
"Maav 68,160,68\ns_cmp_lt_u32 s99, s92\nEX 82,54\nMaav 76,164,68\nRa 148,26,1024\ns_cbranch_scc1 .E445\n.E446:\nMaav 72,164,64\nWL 8\nEX 83,55\nMaav 80,168,64\nRa 152,26,2048\n"
"AD 85,85,82\nMaav 84,168,68\nD1 1,16,46,1024\nMaav 92,172,68\nRa 156,26,3072\nMaav 88,172,64\nAD 85,85,83\nCV 77,82,83\nMaav 96,176,64\nRa 160,24,8192\nMaav 100,176,68\n"
"s_sub_u32 s90, s90, 1\nEX 80,56\nMaav 108,180,68\nRa 164,24,9216\ns_cbranch_scc1 .E447\n.E448:\nMaav 104,180,64\nWL 6\nEX 81,57\nMaav 112,184,64\nRa 168,24,10240\nAD 84,84,80\n"
"Maav 116,184,68\nD1 1,16,46,2048\ns_sub_u32 s99, s99, 1\nMaav 124,188,68\nRa 172,24,11264\nMaav 120,188,64\nAD 84,84,81\nCV 74,80,81\ns_cbranch_scc0 .A441\ns_branch .A453\n.E443:\n"
"v_cmpx_gt_i32 vcc, 19, v0\nv_mov_b32 v15, s97\nv_cmpx_gt_i32 vcc, 18, v0\nv_mov_b32 v14, s97\nv_cmpx_gt_i32 vcc, 17, v0\nv_mov_b32 v13, s97\nv_cmpx_gt_i32 vcc, 16, v0\n"
"v_mov_b32 v12, s97\nv_cmpx_gt_i32 vcc, 3, v0\nv_mov_b32 v7, s97\nv_cmpx_gt_i32 vcc, 2, v0\nv_mov_b32 v6, s97\nv_cmpx_gt_i32 vcc, 1, v0\nv_mov_b32 v5, s97\n"
"v_cmpx_gt_i32 vcc, 0, v0\nv_mov_b32 v4, s97\ns_mov_b64 exec, -1\ns_nop 2\ns_branch .E444\n.E445:\ns_cmp_eq_u32 s99, 0\ns_cbranch_scc1 .K442\nv_cmpx_gt_i32 vcc, 19, v3\n"
"v_mov_b32 v15, s97\nv_cmpx_gt_i32 vcc, 18, v3\nv_mov_b32 v14, s97\nv_cmpx_gt_i32 vcc, 17, v3\nv_mov_b32 v13, s97\nv_cmpx_gt_i32 vcc, 16, v3\nv_mov_b32 v12, s97\n"
"v_cmpx_gt_i32 vcc, 3, v3\nv_mov_b32 v7, s97\nv_cmpx_gt_i32 vcc, 2, v3\nv_mov_b32 v6, s97\nv_cmpx_gt_i32 vcc, 1, v3\nv_mov_b32 v5, s97\nv_cmpx_gt_i32 vcc, 0, v3\nv_mov_b32 v4, s97\n"
"s_mov_b64 exec, -1\ns_nop 2\ns_branch .E446\n.K442:\nv_cmpx_gt_i32 vcc, 19, v3\nv_mov_b32 v19, s97\nv_cmpx_gt_i32 vcc, 18, v3\nv_mov_b32 v18, s97\nv_cmpx_gt_i32 vcc, 17, v3\n"
"v_mov_b32 v17, s97\nv_cmpx_gt_i32 vcc, 16, v3\nv_mov_b32 v16, s97\nv_cmpx_gt_i32 vcc, 3, v3\nv_mov_b32 v11, s97\nv_cmpx_gt_i32 vcc, 2, v3\nv_mov_b32 v10, s97\n"
"v_cmpx_gt_i32 vcc, 1, v3\nv_mov_b32 v9, s97\nv_cmpx_gt_i32 vcc, 0, v3\nv_mov_b32 v8, s97\ns_mov_b64 exec, -1\ns_nop 2\ns_branch .E446\n.E447:\ns_swappc_b64 s[80:81], s[8:9]\n"
"s_branch .E448\n.HD405:\nv_cmp_ne_u32 vcc, s15, v27\ns_nop 0\ns_cbranch_vccnz .S429\n.S430:\nbuffer_store_dword v25, off, s[16:19], s54 offset:4 sc0 sc1\nv_and_b32 v27, 0x70, v1\n"
"buffer_load_dword v27, v27, s[16:19], s61 offen offset:12 sc0 sc1\ns_mov_b32 s90, 1\ns_swappc_b64 s[8:9], s[80:81]\nv_cmp_ne_u32 vcc, s15, v27\ns_nop 0\ns_cbranch_vccnz .S432\n"
".S433:\nv_and_b32 v27, 0x70, v1\nbuffer_load_dword v27, v27, s[16:19], s61 offen offset:8 sc0 sc1\ns_mov_b32 s90, 1\ns_swappc_b64 s[8:9], s[80:81]\nv_cmp_ne_u32 vcc, s15, v27\n"
"s_nop 0\ns_cbranch_vccnz .S435\n.S436:\nv_and_b32 v27, 0xf0, v1\nbuffer_load_dword v27, v27, s[16:19], s61 offen offset:4 sc0 sc1\ns_mov_b32 s90, 1\ns_swappc_b64 s[8:9], s[80:81]\n"
"v_cmp_ne_u32 vcc, s15, v27\ns_nop 0\ns_cbranch_vccnz .S438\n.S439:\ns_mov_b32 s90, 100\ns_swappc_b64 s[8:9], s[80:81]\n.A453:\nRa 176,26,8192\nEX 82,58\nRa 180,26,9216\nWL 6\n"
"EX 83,59\nCvav 36,140,220,8\nRa 184,26,10240\nAD 84,84,82\nMvav 36,136,212\nD1 1,16,46,3072\ns_add_u32 m0, s89, 32768\nMvav 36,132,204\nRa 188,26,11264\nMvav 36,128,196\n"
"AD 84,84,83\nCV 75,82,83\nMvav 36,144,228\nRa 128,2,16384\nMvav 36,148,236\ns_add_u32 s46, s86, s88\nEX 80,60\nMvav 36,152,244\nRa 132,2,17408\nMvav 36,156,252\nEX 81,61\n"
"Ra 136,2,18432\nAD 85,85,80\nD1 1,16,46,0\nRa 140,2,19456\nSv 112,1,16,49,0\nAD 85,85,81\nCV 78,80,81\nRa 144,2,20480\ns_mov_b32 s87, s95\nEX 82,62\nRa 148,2,21504\nWL 6\n"
"EX 83,63\nCvav 44,172,220,16\nRa 152,2,22528\nAD 85,85,82\nMvav 44,168,212\nD1 1,16,46,1024\nMvav 44,164,204\nRa 156,2,23552\nMvav 44,160,196\nAD 85,85,83\nCV 79,82,83\n"
"Mvav 44,176,228\nRa 160,2,24576\nv_max_f32 v90, v84, v85\nMvav 44,180,236\ns_add_u32 s88, s88, 32768\nMvav 44,184,244\nRa 164,2,25600\nv_cmp_lt_f32 vcc, s63, v90\nMvav 44,188,252\n"
"WL 8\nRa 168,2,26624\ns_cbranch_vccnz .T455\n.T456:\nD1 1,16,46,2048\nRa 172,2,27648\nAD 86,86,84\nMaav 0,128,72\nRa 176,2,28672\nMaav 4,128,76\nMaav 12,132,76\nRa 180,2,29696\n"
"Maav 8,132,72\ns_waitcnt vmcnt(13) lgkmcnt(8)\nMaav 16,136,72\nRa 184,2,30720\nMaav 20,136,76\nD1 1,16,46,3072\ns_add_u32 m0, s89, 49152\nMaav 28,140,76\nRa 188,2,31744\n"
"s_barrier\nMaav 24,140,72\nMaav 32,144,72\nRa 128,24,32768\nMaav 36,144,76\ns_add_u32 s46, s87, s88\nEX 80,36\nMaav 44,148,76\nRa 132,24,33792\nMaav 40,148,72\nWL 8\nEX 81,37\n"
"Maav 48,152,72\nRa 136,24,34816\nAD 87,87,85\nMaav 52,152,76\nD1 1,16,46,0\nMaav 60,156,76\nRa 140,24,35840\nMaav 56,156,72\nAD 85,80,81\nCV 68,80,81\nMaav 64,160,72\n"
"Ra 144,26,32768\nMaav 68,160,76\ns_nop 0\nEX 82,38\nMaav 76,164,76\nRa 148,26,33792\nv_cmpx_gt_i32 vcc, 19, v0\nv_mov_b32 v19, s97\nv_cmpx_gt_i32 vcc, 18, v0\nv_mov_b32 v18, s97\n"
"v_cmpx_gt_i32 vcc, 17, v0\nv_mov_b32 v17, s97\nv_cmpx_gt_i32 vcc, 16, v0\nv_mov_b32 v16, s97\nv_cmpx_gt_i32 vcc, 3, v0\nv_mov_b32 v11, s97\nv_cmpx_gt_i32 vcc, 2, v0\n"
"v_mov_b32 v10, s97\nv_cmpx_gt_i32 vcc, 1, v0\nv_mov_b32 v9, s97\nv_cmpx_gt_i32 vcc, 0, v0\nv_mov_b32 v8, s97\ns_mov_b64 exec, -1\ns_nop 2\ns_nop 0\nMaav 72,164,72\nWL 8\nEX 83,39\n"
"Maav 80,168,72\nRa 152,26,34816\nAD 85,85,82\nMaav 84,168,76\nD1 1,16,46,1024\nMaav 92,172,76\nRa 156,26,35840\nMaav 88,172,72\nAD 85,85,83\nCV 69,82,83\nMaav 96,176,72\n"
"Ra 160,24,40960\nMaav 100,176,76\ns_xor_b32 s89, s89, 0x10000\nMaav 108,180,76\nRa 164,24,41984\nMaav 104,180,72\nWL 10\nMaav 112,184,72\nRa 168,24,43008\nMaav 116,184,76\n"
"D1 1,16,46,2048\nMaav 124,188,76\nRa 172,24,44032\nMaav 120,188,72\nRa 176,26,40960\nRa 180,26,41984\nWL 6\nCvav 52,140,220,8\nRa 184,26,43008\nMvav 52,136,212\nD1 1,16,46,3072\n"
"s_add_u32 m0, s89, 0\nMvav 52,132,204\nRa 188,26,44032\nMvav 52,128,196\nMvav 52,144,228\nRa 128,2,49152\nMvav 52,148,236\ns_add_u32 s46, s86, s88\nEX 80,44\nMvav 52,152,244\n"
"Ra 132,2,50176\nv_xor_b32 v24, 0x10000, v24\nMvav 52,156,252\nEX 81,45\nRa 136,2,51200\nAD 85,85,80\nD1 1,16,46,0\nRa 140,2,52224\nSv 112,1,16,49,1024\nAD 85,85,81\nCV 70,80,81\n"
"Ra 144,2,53248\ns_add_u32 s49, s49, 131072\nEX 82,46\nRa 148,2,54272\nv_xor_b32 v26, 0x10000, v26\nWL 6\nEX 83,47\nCvav 60,172,220,16\nRa 152,2,55296\nAD 85,85,82\n"
"Mvav 60,168,212\nD1 1,16,46,1024\nMvav 60,164,204\nRa 156,2,56320\nMvav 60,160,196\nAD 85,85,83\nCV 71,82,83\nMvav 60,176,228\nRa 160,2,57344\nMvav 60,180,236\n"
"s_add_u32 s88, s88, 32768\nMvav 60,184,244\nRa 164,2,58368\nv_cmp_lt_f32 vcc, s63, v85\nMvav 60,188,252\nWL 8\nRa 168,2,59392\ns_cbranch_vccnz .T457\n.T458:\nD1 1,16,46,2048\n"
"Ra 172,2,60416\nRa 176,2,61440\nMaav 4,128,68\nMaav 12,132,68\nRa 180,2,62464\ns_waitcnt vmcnt(13) lgkmcnt(8)\nRa 184,2,63488\nMaav 20,136,68\nD1 1,16,46,3072\n"
"s_add_u32 m0, s89, 16384\nMaav 28,140,68\nRa 188,2,64512\ns_barrier\nRa 128,24,0\nMaav 36,144,68\ns_add_u32 s46, s87, s88\nEX 80,52\nMaav 44,148,68\nRa 132,24,1024\n"
"v_xor_b32 v2, 0x10000, v2\nWL 8\nEX 81,53\nRa 136,24,2048\nAD 87,87,85\nMaav 52,152,68\nD1 1,16,46,0\nMaav 60,156,68\nRa 140,24,3072\nAD 85,80,81\nCV 76,80,81\nRa 144,26,0\n"
"Maav 68,160,68\nEX 82,54\nMaav 76,164,68\nRa 148,26,1024\nWL 8\nEX 83,55\nRa 152,26,2048\nAD 85,85,82\nMaav 84,168,68\nD1 1,16,46,1024\nMaav 92,172,68\nRa 156,26,3072\n"
"AD 85,85,83\nCV 77,82,83\nRa 160,24,8192\nMaav 100,176,68\ns_sub_u32 s90, s90, 1\nMaav 108,180,68\nRa 164,24,9216\nWL 6\nRa 168,24,10240\nMaav 116,184,68\nD1 1,16,46,2048\n"
"Maav 124,188,68\nRa 172,24,11264\n.SW410:\nv_mov_b32 v4, 0\nv_mov_b32 v5, 0\nv_mov_b32 v6, 0\nv_mov_b32 v7, 0\nv_mov_b32 v8, 0\nv_mov_b32 v9, 0\nv_mov_b32 v10, 0\n"
"v_mov_b32 v11, 0\nv_mov_b32 v12, 0\nv_mov_b32 v13, 0\nv_mov_b32 v14, 0\nv_mov_b32 v15, 0\nv_mov_b32 v16, 0\nv_mov_b32 v17, 0\nv_mov_b32 v18, 0\nv_mov_b32 v19, 0\n"
"s_add_u32 s99, s98, 1\ns_mov_b32 s94, s96\ns_add_u32 s95, s96, 65536\nCvaa 32,128,192,4\nRa 176,26,8192\nMvaa 32,132,200\nMvaa 32,136,208\nRa 180,26,9216\nMvaa 32,140,216\nWL 6\n"
"Cvaa 36,140,220,8\nRa 184,26,10240\nMvaa 36,136,212\nD1 1,16,46,3072\ns_add_u32 m0, s89, 32768\nMvaa 36,132,204\nRa 188,26,11264\nMvaa 36,128,196\nMvaa 36,144,228\nRa 128,2,16384\n"
"Mvaa 36,148,236\ns_add_u32 s46, s86, s88\nEX 80,60\nMvaa 36,152,244\nRa 132,2,17408\nMvaa 36,156,252\nWL 6\nEX 81,61\nMvaa 32,156,248\nRa 136,2,18432\nAD 85,85,80\n"
"Mvaa 32,152,240\nD1 1,16,46,0\nMvaa 32,148,232\nRa 140,2,19456\nMvaa 32,144,224\nAD 85,85,81\nCV 78,80,81\nCvaa 40,160,192,12\nRa 144,2,20480\nMvaa 40,164,200\nEX 82,62\n"
"Mvaa 40,168,208\nRa 148,2,21504\nMvaa 40,172,216\nWL 6\nEX 83,63\nCvaa 44,172,220,16\nRa 152,2,22528\nAD 85,85,82\nMvaa 44,168,212\nD1 1,16,46,1024\nMvaa 44,164,204\n"
"Ra 156,2,23552\nMvaa 44,160,196\nAD 85,85,83\nCV 79,82,83\nMvaa 44,176,228\nRa 160,2,24576\nMvaa 44,180,236\ns_add_u32 s88, s88, 32768\nEX 80,32\nMvaa 44,184,244\nRa 164,2,25600\n"
"v_cmp_lt_f32 vcc, s63, v85\nMvaa 44,188,252\nWL 8\nEX 81,33\nMvaa 40,188,248\nRa 168,2,26624\ns_cbranch_vccnz .T461\n.T462:\nMvaa 40,184,240\nD1 1,16,46,2048\nMvaa 40,180,232\n"
"Ra 172,2,27648\nMvaa 40,176,224\nAD 84,80,81\nCV 64,80,81\nRa 176,2,28672\nMaav 4,128,76\nEX 82,34\nMaav 12,132,76\nRa 180,2,29696\ns_waitcnt vmcnt(12) lgkmcnt(8)\nEX 83,35\n"
"Ra 184,2,30720\nAD 84,84,82\nMaav 20,136,76\nD1 1,16,46,3072\ns_add_u32 m0, s89, 49152\nMaav 28,140,76\nRa 188,2,31744\ns_barrier\nAR 98,0\nAR 99,1\nCV 100,98,99\nAD 84,84,83\n"
"CV 65,82,83\nAR 98,2\nAR 99,3\nCV 101,98,99\nRa 128,24,32768\nAR 98,8\nAR 99,9\nCV 102,98,99\nAR 98,10\nMaav 36,144,76\ns_add_u32 s46, s87, s88\nEX 80,36\nAR 99,11\n"
"Maav 44,148,76\nRa 132,24,33792\nCV 103,98,99\ns_add_u32 s5, s66, 0\nSv 100,1,16,5,0\nAR 98,4\nWL 8\nEX 81,37\nAR 99,5\nCV 104,98,99\nRa 136,24,34816\nAD 87,87,85\nAR 98,6\n"
"AR 99,7\nCV 105,98,99\nMaav 52,152,76\nD1 1,16,46,0\nv_mov_b32 v96, v86\nAR 98,12\nAR 99,13\nMaav 60,156,76\nRa 140,24,35840\nv_mov_b32 v97, v87\nCV 106,98,99\nAR 98,14\nAR 99,15\n"
"AD 85,80,81\nCV 68,80,81\nCV 107,98,99\ns_add_u32 s5, s66, 0\nSv 104,1,16,5,1024\nRa 144,26,32768\nv_permlane32_swap_b32 v86, v96\nv_permlane32_swap_b32 v87, v97\nAR 98,16\n"
"AR 99,17\nMaav 68,160,76\nEX 82,38\nCV 108,98,99\nAR 98,18\nMaav 76,164,76\nRa 148,26,33792\nAD 86,86,96\nAR 99,19\nCV 109,98,99\nAR 98,24\nWL 8\nEX 83,39\nAR 99,25\nCV 110,98,99\n"
"Ra 152,26,34816\nAD 85,85,82\nAD 87,87,97\nAR 98,26\nAR 99,27\nMaav 84,168,76\nD1 1,16,46,1024\nv_mov_b32 v96, v86\nCV 111,98,99\nMaav 92,172,76\nRa 156,26,35840\n"
"v_mov_b32 v97, v87\ns_add_u32 s5, s66, 0\nSv 108,1,16,5,2048\nAR 98,20\nAD 85,85,83\nCV 69,82,83\nAR 99,21\nCV 100,98,99\nAR 98,22\nRa 160,24,40960\n"
"v_permlane16_swap_b32 v86, v96\nv_permlane16_swap_b32 v87, v97\nAR 99,23\nCV 101,98,99\nMaav 100,176,76\ns_xor_b32 s89, s89, 0x10000\nEX 80,40\nAR 98,28\nMaav 108,180,76\n"
"Ra 164,24,41984\nAD 86,86,96\nAR 99,29\nCV 102,98,99\nAR 98,30\nWL 6\nEX 81,41\nAR 99,31\nCV 103,98,99\nRa 168,24,43008\nAD 84,84,80\nAD 87,87,97\ns_add_u32 s5, s66, 0\n"
"Sv 100,1,16,5,3072\nMaav 116,184,76\nD1 1,16,46,2048\nv_cmp_gt_u32 vcc, 0x100, v1\nv_mov_b32 v94, s15\nv_mov_b32 v95, s15\nv_cndmask_b32 v92, v89, v88, vcc\n"
"v_cndmask_b32 v93, v87, v86, vcc\nMaav 124,188,76\nRa 172,24,44032\nv_mov_b32 v86, 0\nAR 98,32\nAR 99,33\nCV 104,98,99\nAD 84,84,81\nCV 66,80,81\nAR 98,34\nAR 99,35\nCV 105,98,99\n"
"Cvaa 48,128,192,4\nRa 176,26,40960\nv_mov_b32 v87, 0\nv_mov_b32 v88, 0\nAR 98,40\nAR 99,41\nMvaa 48,132,200\nEX 82,42\nCV 106,98,99\nAR 98,42\nMvaa 48,136,208\nRa 180,26,41984\n"
"v_mov_b32 v89, 0\nAR 99,43\nCV 107,98,99\nMvaa 48,140,216\nWL 6\nEX 83,43\ns_add_u32 s5, s66, 4096\nSv 104,1,16,5,0\nCvaa 52,140,220,8\nRa 184,26,43008\nAD 84,84,82\nAR 98,36\n"
"AR 99,37\nCV 108,98,99\nMvaa 52,136,212\nD1 1,16,46,3072\ns_add_u32 m0, s89, 0\nAR 98,38\nAR 99,39\nMvaa 52,132,204\nRa 188,26,44032\nCV 109,98,99\nAR 98,44\nAR 99,45\n"
"CV 110,98,99\nMvaa 52,128,196\nAD 84,84,83\nCV 67,82,83\nAR 98,46\nAR 99,47\nCV 111,98,99\nMvaa 52,144,228\nRa 128,2,49152\ns_add_u32 s5, s66, 4096\nSv 108,1,16,5,1024\nAR 98,48\n"
"AR 99,49\nMvaa 52,148,236\ns_add_u32 s46, s86, s88\nEX 80,44\nCV 100,98,99\nMvaa 52,152,244\nRa 132,2,50176\nv_xor_b32 v24, 0x10000, v24\nAR 98,50\nAR 99,51\nCV 101,98,99\n"
"Mvaa 52,156,252\nWL 6\nEX 81,45\nAR 98,56\nAR 99,57\nMvaa 48,156,248\nRa 136,2,51200\nAD 85,85,80\nCV 102,98,99\nAR 98,58\nAR 99,59\nMvaa 48,152,240\nD1 1,16,46,0\nCV 103,98,99\n"
"s_add_u32 s5, s66, 4096\nSv 100,1,16,5,2048\nMvaa 48,148,232\nRa 140,2,52224\nAR 98,52\nAR 99,53\nCV 104,98,99\nAR 98,54\nMvaa 48,144,224\nAD 85,85,81\nCV 70,80,81\nAR 99,55\n"
"CV 105,98,99\nAR 98,60\nCvaa 56,160,192,12\nRa 144,2,53248\nAR 99,61\nCV 106,98,99\nAR 98,62\nAR 99,63\nMvaa 56,164,200\nEX 82,46\nCV 107,98,99\nMvaa 56,168,208\nRa 148,2,54272\n"
"v_xor_b32 v26, 0x10000, v26\ns_add_u32 s5, s66, 4096\nSv 104,1,16,5,3072\nAR 98,64\nMvaa 56,172,216\nWL 6\nEX 83,47\nAR 99,65\nCV 108,98,99\nCvaa 60,172,220,16\nRa 152,2,55296\n"
"AD 85,85,82\nAR 98,66\nAR 99,67\nCV 109,98,99\nMvaa 60,168,212\nD1 1,16,46,1024\nAR 98,72\nAR 99,73\nCV 110,98,99\nMvaa 60,164,204\nRa 156,2,56320\nAR 98,74\nAR 99,75\n"
"CV 111,98,99\nMvaa 60,160,196\nAD 85,85,83\nCV 71,82,83\ns_add_u32 s5, s66, 16384\nSv 108,1,16,5,0\nAR 98,68\nMvaa 60,176,228\nRa 160,2,57344\nv_max_f32 v90, v84, v85\nAR 99,69\n"
"CV 100,98,99\nAR 98,70\nMvaa 60,180,236\ns_add_u32 s88, s88, 32768\nEX 80,48\nAR 99,71\nMvaa 60,184,244\nRa 164,2,58368\nv_cmp_lt_f32 vcc, s63, v90\nMvaa 60,188,252\nWL 8\n"
"EX 81,49\nMvaa 56,188,248\nRa 168,2,59392\ns_cbranch_vccnz .T463\n.T464:\nv_mov_b32 v20, v84\nv_mov_b32 v21, v85\nv_mov_b32 v22, v20\nv_mov_b32 v23, v21\nMvaa 56,184,240\n"
"D1 1,16,46,2048\nv_permlane32_swap_b32 v20, v22\nv_permlane32_swap_b32 v21, v23\nAD 20,20,22\nAD 21,21,23\nv_mov_b32 v22, v20\nv_mov_b32 v23, v21\nMvaa 56,180,232\nRa 172,2,60416\n"
"v_permlane16_swap_b32 v20, v22\nv_permlane16_swap_b32 v21, v23\nAD 20,20,22\nAD 21,21,23\nv_min_f32 v90, v20, v21\nv_cmp_gt_f32 vcc, s64, v90\ns_nop 0\ns_cbranch_vccnz .T465\n"
".T466:\nAD 86,86,84\nMvaa 56,176,224\nAD 84,80,81\nCV 72,80,81\nCV 101,98,99\nAR 98,76\nAR 99,77\nZvav 128,128,64\nRa 176,2,61440\nCV 102,98,99\nAR 98,78\nAR 99,79\nCV 103,98,99\n"
"Zvav 132,128,68\nEX 82,50\ns_add_u32 s5, s66, 16384\nSv 100,1,16,5,1024\nZvav 140,132,68\nRa 180,2,62464\nAR 98,80\nAR 99,81\nCV 104,98,99\nAR 98,82\nZvav 136,132,64\n"
"s_waitcnt vmcnt(21) lgkmcnt(8)\nEX 83,51\nAR 99,83\nCV 105,98,99\nZvav 144,136,64\nRa 184,2,63488\nAD 84,84,82\nAR 98,88\nAR 99,89\nCV 106,98,99\nZvav 148,136,68\nD1 1,16,46,3072\n"
"s_add_u32 m0, s89, 16384\nAR 98,90\nAR 99,91\nZvav 156,140,68\nRa 188,2,64512\ns_barrier\nCV 107,98,99\ns_add_u32 s5, s66, 16384\nSv 104,1,16,5,2048\nZvav 152,140,64\nAD 84,84,83\n"
"CV 73,82,83\nAR 98,84\nAR 99,85\nCV 108,98,99\nZvav 160,144,64\nRa 128,24,0\nAR 98,86\nAR 99,87\nCV 109,98,99\nAR 98,92\nZvav 164,144,68\ns_add_u32 s46, s87, s88\nEX 80,52\n"
"AR 99,93\nZvav 172,148,68\nRa 132,24,1024\nv_xor_b32 v2, 0x10000, v2\nCV 110,98,99\nAR 98,94\nAR 99,95\nZvav 168,148,64\nWL 8\nEX 81,53\nCV 111,98,99\nZvav 176,152,64\n"
"Ra 136,24,2048\nAD 87,87,85\ns_add_u32 s5, s66, 16384\nSv 108,1,16,5,3072\nAR 98,96\nZvav 180,152,68\nD1 1,16,46,0\nAR 99,97\nCV 100,98,99\nAR 98,98\nZvav 188,156,68\n"
"Ra 140,24,3072\nAR 99,99\nCV 101,98,99\nAR 98,104\nAR 99,105\nZvav 184,156,64\nAD 85,80,81\nCV 76,80,81\nCV 102,98,99\nAR 98,106\nAR 99,107\nZvav 192,160,64\nRa 144,26,0\n"
"CV 103,98,99\ns_add_u32 s5, s66, 20480\nSv 100,1,16,5,0\nAR 98,100\nZvav 196,160,68\nEX 82,54\nAR 99,101\nCV 104,98,99\nZvav 204,164,68\nRa 148,26,1024\nAR 98,102\nAR 99,103\n"
"CV 105,98,99\nAR 98,108\nZvav 200,164,64\nWL 8\nEX 83,55\nAR 99,109\nCV 106,98,99\nZvav 208,168,64\nRa 152,26,2048\nAD 85,85,82\nAR 98,110\nAR 99,111\nCV 107,98,99\n"
"Zvav 212,168,68\nD1 1,16,46,1024\ns_add_u32 s5, s66, 20480\nSv 104,1,16,5,1024\nAR 98,112\nZvav 220,172,68\nRa 156,26,3072\nAR 99,113\nCV 108,98,99\nAR 98,114\nAR 99,115\n"
"Zvav 216,172,64\nAD 85,85,83\nCV 77,82,83\nCV 109,98,99\nAR 98,120\nAR 99,121\nZvav 224,176,64\nRa 160,24,8192\nCV 110,98,99\nAR 98,122\nAR 99,123\nCV 111,98,99\nZvav 228,176,68\n"
"s_sub_u32 s90, s90, 1\nEX 80,56\nZvav 236,180,68\nRa 164,24,9216\ns_cbranch_scc0 .E459\ns_swappc_b64 s[80:81], s[8:9]\n.E459:\ns_add_u32 s5, s66, 20480\nSv 108,1,16,5,2048\n"
"Zvav 232,180,64\nWL 6\nEX 81,57\nAR 98,116\nAR 99,117\nZvav 240,184,64\nRa 168,24,10240\nAD 84,84,80\nCV 100,98,99\nAR 98,118\nAR 99,119\nZvav 244,184,68\nD1 1,16,46,2048\n"
"CV 101,98,99\nAR 98,124\nAR 99,125\nZvav 252,188,68\nRa 172,24,11264\nCV 102,98,99\nAR 98,126\nAR 99,127\nCV 103,98,99\nZvav 248,188,64\nAD 84,84,81\nCV 74,80,81\n"
"s_add_u32 s5, s66, 20480\nSv 100,1,16,5,3072\ns_sub_u32 s99, s99, 1\n.p2align 6\n.A467:\nCvaa 32,128,192,4\nRa 176,26,8192\nMvaa 32,132,200\nEX 82,58\nMvaa 32,136,208\n"
"Ra 180,26,9216\nMvaa 32,140,216\nWL 6\nEX 83,59\nCvaa 36,140,220,8\nRa 184,26,10240\nAD 84,84,82\nMvaa 36,136,212\nD1 1,16,46,3072\ns_add_u32 m0, s89, 32768\nMvaa 36,132,204\n"
"Ra 188,26,11264\nMvaa 36,128,196\nAD 84,84,83\nCV 75,82,83\nMvaa 36,144,228\nRa 128,2,16384\nMvaa 36,148,236\ns_add_u32 s46, s86, s88\nEX 80,60\nMvaa 36,152,244\nRa 132,2,17408\n"
"Mvaa 36,156,252\nWL 6\nEX 81,61\nMvaa 32,156,248\nRa 136,2,18432\nAD 85,85,80\nMvaa 32,152,240\nD1 1,16,46,0\nMvaa 32,148,232\nRa 140,2,19456\nSv 112,1,16,49,0\nMvaa 32,144,224\n"
"AD 85,85,81\nCV 78,80,81\nCvaa 40,160,192,12\nRa 144,2,20480\nMvaa 40,164,200\nEX 82,62\nMvaa 40,168,208\nRa 148,2,21504\nMvaa 40,172,216\nWL 6\nEX 83,63\nCvaa 44,172,220,16\n"
"Ra 152,2,22528\nAD 85,85,82\nMvaa 44,168,212\nD1 1,16,46,1024\nMvaa 44,164,204\nRa 156,2,23552\nMvaa 44,160,196\nAD 85,85,83\nCV 79,82,83\nMvaa 44,176,228\nRa 160,2,24576\n"
"v_max_f32 v90, v84, v85\nMvaa 44,180,236\ns_add_u32 s88, s88, 32768\nEX 80,32\nMvaa 44,184,244\nRa 164,2,25600\nv_cmp_lt_f32 vcc, s63, v90\nMvaa 44,188,252\nWL 8\nEX 81,33\n"
"Mvaa 40,188,248\nRa 168,2,26624\ns_cbranch_vccnz .T477\n.T478:\nMvaa 40,184,240\nD1 1,16,46,2048\nMvaa 40,180,232\nRa 172,2,27648\nAD 86,86,84\nMvaa 40,176,224\nAD 84,80,81\n"
"CV 64,80,81\nMvav 128,128,72\nRa 176,2,28672\nMvav 132,128,76\ns_cmp_eq_u32 s99, s98\nEX 82,34\nMvav 140,132,76\nRa 180,2,29696\ns_cbranch_scc1 .E475\n.E476:\nMvav 136,132,72\n"
"s_waitcnt vmcnt(13) lgkmcnt(8)\nEX 83,35\nMvav 144,136,72\nRa 184,2,30720\nAD 84,84,82\nMvav 148,136,76\nD1 1,16,46,3072\ns_add_u32 m0, s89, 49152\nMvav 156,140,76\n"
"Ra 188,2,31744\ns_barrier\nMvav 152,140,72\nAD 84,84,83\nCV 65,82,83\nMvav 160,144,72\nRa 128,24,32768\nMvav 164,144,76\ns_add_u32 s46, s87, s88\nEX 80,36\nMvav 172,148,76\n"
"Ra 132,24,33792\nMvav 168,148,72\nWL 8\nEX 81,37\nMvav 176,152,72\nRa 136,24,34816\nAD 87,87,85\nMvav 180,152,76\nD1 1,16,46,0\nMvav 188,156,76\nRa 140,24,35840\nMvav 184,156,72\n"
"AD 85,80,81\nCV 68,80,81\nMvav 192,160,72\nRa 144,26,32768\nMvav 196,160,76\ns_cmp_eq_u32 s99, 0\nEX 82,38\nMvav 204,164,76\nRa 148,26,33792\ns_cbranch_scc1 .E469\n.E470:\n"
"Mvav 200,164,72\nWL 8\nEX 83,39\nMvav 208,168,72\nRa 152,26,34816\nAD 85,85,82\nMvav 212,168,76\nD1 1,16,46,1024\nMvav 220,172,76\nRa 156,26,35840\nMvav 216,172,72\nAD 85,85,83\n"
"CV 69,82,83\nMvav 224,176,72\nRa 160,24,40960\nMvav 228,176,76\ns_xor_b32 s89, s89, 0x10000\nEX 80,40\nMvav 236,180,76\nRa 164,24,41984\nMvav 232,180,72\nWL 6\nEX 81,41\n"
"Mvav 240,184,72\nRa 168,24,43008\nAD 84,84,80\nMvav 244,184,76\nD1 1,16,46,2048\nMvav 252,188,76\nRa 172,24,44032\nMvav 248,188,72\nAD 84,84,81\nCV 66,80,81\nCvaa 48,128,192,4\n"
"Ra 176,26,40960\nMvaa 48,132,200\ns_cmp_eq_u32 s99, 0\nEX 82,42\nMvaa 48,136,208\nRa 180,26,41984\ns_cselect_b32 s86, s94, s86\nMvaa 48,140,216\nWL 6\nEX 83,43\nCvaa 52,140,220,8\n"
"Ra 184,26,43008\nAD 84,84,82\nMvaa 52,136,212\nD1 1,16,46,3072\ns_add_u32 m0, s89, 0\nMvaa 52,132,204\nRa 188,26,44032\nMvaa 52,128,196\nAD 84,84,83\nCV 67,82,83\nMvaa 52,144,228\n"
"Ra 128,2,49152\nMvaa 52,148,236\ns_add_u32 s46, s86, s88\nEX 80,44\nMvaa 52,152,244\nRa 132,2,50176\nv_xor_b32 v24, 0x10000, v24\nMvaa 52,156,252\nWL 6\nEX 81,45\nMvaa 48,156,248\n"
"Ra 136,2,51200\nAD 85,85,80\nMvaa 48,152,240\nD1 1,16,46,0\nMvaa 48,148,232\nRa 140,2,52224\nSv 112,1,16,49,1024\nMvaa 48,144,224\nAD 85,85,81\nCV 70,80,81\nCvaa 56,160,192,12\n"
"Ra 144,2,53248\nMvaa 56,164,200\ns_add_u32 s49, s49, 131072\nEX 82,46\nMvaa 56,168,208\nRa 148,2,54272\nv_xor_b32 v26, 0x10000, v26\nMvaa 56,172,216\nWL 6\nEX 83,47\n"
"Cvaa 60,172,220,16\nRa 152,2,55296\nAD 85,85,82\nMvaa 60,168,212\nD1 1,16,46,1024\nMvaa 60,164,204\nRa 156,2,56320\nMvaa 60,160,196\nAD 85,85,83\nCV 71,82,83\nMvaa 60,176,228\n"
"Ra 160,2,57344\nv_max_f32 v90, v84, v85\nMvaa 60,180,236\ns_add_u32 s88, s88, 32768\nEX 80,48\nMvaa 60,184,244\nRa 164,2,58368\nv_cmp_lt_f32 vcc, s63, v90\nMvaa 60,188,252\nWL 8\n"
"EX 81,49\nMvaa 56,188,248\nRa 168,2,59392\ns_cbranch_vccnz .T479\n.T480:\nMvaa 56,184,240\nD1 1,16,46,2048\nMvaa 56,180,232\nRa 172,2,60416\nAD 86,86,84\nMvaa 56,176,224\n"
"AD 84,80,81\nCV 72,80,81\nMvav 128,128,64\nRa 176,2,61440\nMvav 132,128,68\nEX 82,50\nMvav 140,132,68\nRa 180,2,62464\nMvav 136,132,64\ns_waitcnt vmcnt(13) lgkmcnt(8)\nEX 83,51\n"
"Mvav 144,136,64\nRa 184,2,63488\nAD 84,84,82\nMvav 148,136,68\nD1 1,16,46,3072\ns_add_u32 m0, s89, 16384\nMvav 156,140,68\nRa 188,2,64512\ns_barrier\nMvav 152,140,64\nAD 84,84,83\n"
"CV 73,82,83\nMvav 160,144,64\nRa 128,24,0\nMvav 164,144,68\ns_add_u32 s46, s87, s88\nEX 80,52\nMvav 172,148,68\nRa 132,24,1024\nv_xor_b32 v2, 0x10000, v2\nMvav 168,148,64\nWL 8\n"
"EX 81,53\nMvav 176,152,64\nRa 136,24,2048\nAD 87,87,85\nMvav 180,152,68\nD1 1,16,46,0\nMvav 188,156,68\nRa 140,24,3072\nMvav 184,156,64\nAD 85,80,81\nCV 76,80,81\nMvav 192,160,64\n"
"Ra 144,26,0\nMvav 196,160,68\ns_cmp_lt_u32 s99, s92\nEX 82,54\nMvav 204,164,68\nRa 148,26,1024\ns_cbranch_scc1 .E471\n.E472:\nMvav 200,164,64\nWL 8\nEX 83,55\nMvav 208,168,64\n"
"Ra 152,26,2048\nAD 85,85,82\nMvav 212,168,68\nD1 1,16,46,1024\nMvav 220,172,68\nRa 156,26,3072\nMvav 216,172,64\nAD 85,85,83\nCV 77,82,83\nMvav 224,176,64\nRa 160,24,8192\n"
"Mvav 228,176,68\ns_sub_u32 s90, s90, 1\nEX 80,56\nMvav 236,180,68\nRa 164,24,9216\ns_cbranch_scc1 .E473\n.E474:\nMvav 232,180,64\nWL 6\nEX 81,57\nMvav 240,184,64\nRa 168,24,10240\n"
"AD 84,84,80\nMvav 244,184,68\nD1 1,16,46,2048\ns_sub_u32 s99, s99, 1\nMvav 252,188,68\nRa 172,24,11264\nMvav 248,188,64\nAD 84,84,81\nCV 74,80,81\ns_cbranch_scc0 .A467\n"
"s_branch .A481\n.E469:\nv_cmpx_gt_i32 vcc, 19, v0\nv_mov_b32 v15, s97\nv_cmpx_gt_i32 vcc, 18, v0\nv_mov_b32 v14, s97\nv_cmpx_gt_i32 vcc, 17, v0\nv_mov_b32 v13, s97\n"
"v_cmpx_gt_i32 vcc, 16, v0\nv_mov_b32 v12, s97\nv_cmpx_gt_i32 vcc, 3, v0\nv_mov_b32 v7, s97\nv_cmpx_gt_i32 vcc, 2, v0\nv_mov_b32 v6, s97\nv_cmpx_gt_i32 vcc, 1, v0\n"
"v_mov_b32 v5, s97\nv_cmpx_gt_i32 vcc, 0, v0\nv_mov_b32 v4, s97\ns_mov_b64 exec, -1\ns_nop 2\ns_branch .E470\n.E471:\ns_cmp_eq_u32 s99, 0\ns_cbranch_scc1 .K468\n"
"v_cmpx_gt_i32 vcc, 19, v3\nv_mov_b32 v15, s97\nv_cmpx_gt_i32 vcc, 18, v3\nv_mov_b32 v14, s97\nv_cmpx_gt_i32 vcc, 17, v3\nv_mov_b32 v13, s97\nv_cmpx_gt_i32 vcc, 16, v3\n"
"v_mov_b32 v12, s97\nv_cmpx_gt_i32 vcc, 3, v3\nv_mov_b32 v7, s97\nv_cmpx_gt_i32 vcc, 2, v3\nv_mov_b32 v6, s97\nv_cmpx_gt_i32 vcc, 1, v3\nv_mov_b32 v5, s97\n"
"v_cmpx_gt_i32 vcc, 0, v3\nv_mov_b32 v4, s97\ns_mov_b64 exec, -1\ns_nop 2\ns_branch .E472\n.K468:\nv_cmpx_gt_i32 vcc, 19, v3\nv_mov_b32 v19, s97\nv_cmpx_gt_i32 vcc, 18, v3\n"
"v_mov_b32 v18, s97\nv_cmpx_gt_i32 vcc, 17, v3\nv_mov_b32 v17, s97\nv_cmpx_gt_i32 vcc, 16, v3\nv_mov_b32 v16, s97\nv_cmpx_gt_i32 vcc, 3, v3\nv_mov_b32 v11, s97\n"
"v_cmpx_gt_i32 vcc, 2, v3\nv_mov_b32 v10, s97\nv_cmpx_gt_i32 vcc, 1, v3\nv_mov_b32 v9, s97\nv_cmpx_gt_i32 vcc, 0, v3\nv_mov_b32 v8, s97\ns_mov_b64 exec, -1\ns_nop 2\n"
"s_branch .E472\n.E473:\ns_swappc_b64 s[80:81], s[8:9]\ns_branch .E474\n.E475:\nv_mov_b32 v91, 0x40000000\nv_cmp_gt_u32 vcc, 0x200, v1\nWV 5\ns_nop 0\n"
"v_cndmask_b32 v91, v91, v1, vcc\nSv 92,91,16,65,0\ns_branch .E476\n.A481:\n.LE411:\nRa 176,26,8192\nEX 82,58\nRa 180,26,9216\nWL 6\nEX 83,59\nCvaa 36,140,220,8\nRa 184,26,10240\n"
"AD 84,84,82\nMvaa 36,136,212\nD1 1,16,46,3072\ns_add_u32 m0, s89, 32768\nMvaa 36,132,204\nRa 188,26,11264\nMvaa 36,128,196\nAD 84,84,83\nCV 75,82,83\nMvaa 36,144,228\n"
"Ra 128,2,16384\nMvaa 36,148,236\ns_add_u32 s46, s86, s88\nEX 80,60\nMvaa 36,152,244\nRa 132,2,17408\nMvaa 36,156,252\nEX 81,61\nRa 136,2,18432\nAD 85,85,80\nD1 1,16,46,0\n"
"Ra 140,2,19456\nAD 85,85,81\nCV 78,80,81\nRa 144,2,20480\ns_mov_b32 s87, s95\nEX 82,62\nRa 148,2,21504\nWL 6\nEX 83,63\nCvaa 44,172,220,16\nRa 152,2,22528\nAD 85,85,82\n"
"Mvaa 44,168,212\nD1 1,16,46,1024\nMvaa 44,164,204\nRa 156,2,23552\nMvaa 44,160,196\nAD 85,85,83\nCV 79,82,83\nMvaa 44,176,228\nRa 160,2,24576\nv_max_f32 v90, v84, v85\n"
"Mvaa 44,180,236\ns_add_u32 s88, s88, 32768\nMvaa 44,184,244\nRa 164,2,25600\nv_cmp_lt_f32 vcc, s63, v90\nMvaa 44,188,252\nWL 8\nRa 168,2,26624\ns_cbranch_vccnz .T483\n.T484:\n"
"D1 1,16,46,2048\nRa 172,2,27648\nAD 86,86,84\nMvav 128,128,72\nRa 176,2,28672\nMvav 132,128,76\nMvav 140,132,76\nRa 180,2,29696\nMvav 136,132,72\ns_waitcnt vmcnt(12) lgkmcnt(8)\n"
"Mvav 144,136,72\nRa 184,2,30720\nMvav 148,136,76\nD1 1,16,46,3072\ns_add_u32 m0, s89, 49152\nMvav 156,140,76\nRa 188,2,31744\ns_barrier\nMvav 152,140,72\nMvav 160,144,72\n"
"Ra 128,24,32768\nMvav 164,144,76\ns_add_u32 s46, s87, s88\nEX 80,36\nMvav 172,148,76\nRa 132,24,33792\nMvav 168,148,72\nWL 8\nEX 81,37\nMvav 176,152,72\nRa 136,24,34816\n"
"AD 87,87,85\nMvav 180,152,76\nD1 1,16,46,0\nMvav 188,156,76\nRa 140,24,35840\nMvav 184,156,72\nAD 85,80,81\nCV 68,80,81\nMvav 192,160,72\nRa 144,26,32768\nMvav 196,160,76\n"
"s_nop 0\nEX 82,38\nMvav 204,164,76\nRa 148,26,33792\nv_cmpx_gt_i32 vcc, 19, v0\nv_mov_b32 v19, s97\nv_cmpx_gt_i32 vcc, 18, v0\nv_mov_b32 v18, s97\nv_cmpx_gt_i32 vcc, 17, v0\n"
"v_mov_b32 v17, s97\nv_cmpx_gt_i32 vcc, 16, v0\nv_mov_b32 v16, s97\nv_cmpx_gt_i32 vcc, 3, v0\nv_mov_b32 v11, s97\nv_cmpx_gt_i32 vcc, 2, v0\nv_mov_b32 v10, s97\n"
"v_cmpx_gt_i32 vcc, 1, v0\nv_mov_b32 v9, s97\nv_cmpx_gt_i32 vcc, 0, v0\nv_mov_b32 v8, s97\ns_mov_b64 exec, -1\ns_nop 2\ns_nop 0\nMvav 200,164,72\nWL 8\nEX 83,39\nMvav 208,168,72\n"
"Ra 152,26,34816\nAD 85,85,82\nMvav 212,168,76\nD1 1,16,46,1024\nMvav 220,172,76\nRa 156,26,35840\nMvav 216,172,72\nAD 85,85,83\nCV 69,82,83\nMvav 224,176,72\nRa 160,24,40960\n"
"Mvav 228,176,76\ns_xor_b32 s89, s89, 0x10000\nMvav 236,180,76\nRa 164,24,41984\nMvav 232,180,72\nWL 10\nMvav 240,184,72\nRa 168,24,43008\nMvav 244,184,76\nD1 1,16,46,2048\n"
"Mvav 252,188,76\nRa 172,24,44032\nMvav 248,188,72\nRa 176,26,40960\nRa 180,26,41984\nWL 6\nCvaa 52,140,220,8\nRa 184,26,43008\nMvaa 52,136,212\nD1 1,16,46,3072\n"
"s_add_u32 m0, s89, 0\nMvaa 52,132,204\nRa 188,26,44032\nMvaa 52,128,196\nMvaa 52,144,228\nRa 128,2,49152\nMvaa 52,148,236\ns_add_u32 s46, s86, s88\nEX 80,44\nMvaa 52,152,244\n"
"Ra 132,2,50176\nv_xor_b32 v24, 0x10000, v24\nMvaa 52,156,252\nEX 81,45\nRa 136,2,51200\nAD 85,85,80\nD1 1,16,46,0\nRa 140,2,52224\nAD 85,85,81\nCV 70,80,81\nRa 144,2,53248\n"
"EX 82,46\nRa 148,2,54272\nv_xor_b32 v26, 0x10000, v26\nWL 6\nEX 83,47\nCvaa 60,172,220,16\nRa 152,2,55296\nAD 85,85,82\nMvaa 60,168,212\nD1 1,16,46,1024\nMvaa 60,164,204\n"
"Ra 156,2,56320\nMvaa 60,160,196\nAD 85,85,83\nCV 71,82,83\nMvaa 60,176,228\nRa 160,2,57344\nMvaa 60,180,236\ns_add_u32 s88, s88, 32768\nMvaa 60,184,244\nRa 164,2,58368\n"
"v_cmp_lt_f32 vcc, s63, v85\nMvaa 60,188,252\nWL 8\nRa 168,2,59392\ns_cbranch_vccnz .T485\n.T486:\nD1 1,16,46,2048\nRa 172,2,60416\nRa 176,2,61440\nMvav 132,128,68\n"
"Mvav 140,132,68\nRa 180,2,62464\ns_waitcnt vmcnt(11) lgkmcnt(8)\nRa 184,2,63488\nMvav 148,136,68\nD1 1,16,46,3072\ns_add_u32 m0, s89, 16384\nMvav 156,140,68\nRa 188,2,64512\n"
"s_barrier\nRa 128,24,0\nMvav 164,144,68\ns_add_u32 s46, s87, s88\nEX 80,52\nMvav 172,148,68\nRa 132,24,1024\nv_xor_b32 v2, 0x10000, v2\nWL 8\nEX 81,53\nRa 136,24,2048\n"
"AD 87,87,85\nMvav 180,152,68\nD1 1,16,46,0\nMvav 188,156,68\nRa 140,24,3072\nAD 85,80,81\nCV 76,80,81\nRa 144,26,0\nMvav 196,160,68\nEX 82,54\nMvav 204,164,68\nRa 148,26,1024\n"
"WL 8\nEX 83,55\nRa 152,26,2048\nAD 85,85,82\nMvav 212,168,68\nD1 1,16,46,1024\nMvav 220,172,68\nRa 156,26,3072\nAD 85,85,83\nCV 77,82,83\nRa 160,24,8192\nMvav 228,176,68\n"
"Mvav 236,180,68\nRa 164,24,9216\nWL 10\nRa 168,24,10240\nMvav 244,184,68\nD1 1,16,46,2048\nMvav 252,188,68\nRa 172,24,11264\nD1 1,16,46,3072\ns_add_u32 m0, s89, 32768\n"
"s_add_u32 s46, s86, s88\nRa 128,2,16384\nv_xor_b32 v3, 0x10000, v2\nv_add_u32 v0, 0x20000, v1\nEX 80,60\nRa 132,2,17408\ns_sub_u32 s94, s49, 1835008\nEX 81,61\nRa 136,2,18432\n"
"AD 85,85,80\ns_xor_b32 s92, s94, 0x202000\nD1 1,16,46,0\ns_lshl_b32 s89, s11, 12\nRa 140,2,19456\ns_lshl1_add_u32 s2, s12, 1\nAD 85,85,81\nCV 78,80,81\nRa 144,2,20480\n"
"s_lshl3_add_u32 s2, s2, s13\nEX 82,62\nRa 148,2,21504\ns_lshl4_add_u32 s2, s2, 0xc048200\nEX 83,63\nRa 152,2,22528\nAD 85,85,82\nD1 1,16,46,1024\nRa 156,2,23552\n"
"s_and_b32 s3, s72, 0x2000\nAD 85,85,83\nCV 79,82,83\nRa 160,2,24576\ns_cmp_eq_u32 s11, 1\ns_cselect_b32 s54, s2, s54\ns_cselect_b32 s3, s3, 0\nv_cmp_lt_f32 vcc, s63, v85\n"
"Ra 164,2,25600\ns_cbranch_vccnz .T487\n.T488:\nAD 87,87,85\nWL 8\nv_mov_b32 v84, v86\nv_mov_b32 v85, v87\nRa 168,2,26624\nv_permlane32_swap_b32 v86, v84\n"
"v_permlane32_swap_b32 v87, v85\nD1 1,16,46,2048\nAD 86,86,84\nRa 172,2,27648\nAD 87,87,85\nv_mov_b32 v84, v86\nv_mov_b32 v85, v87\nRa 176,2,28672\nv_permlane16_swap_b32 v86, v84\n"
"v_permlane16_swap_b32 v87, v85\nMvav 132,128,76\nAD 86,86,84\nAD 87,87,85\nMvav 140,132,76\nRa 180,2,29696\nv_rcp_f32 v86, v86\nWL 8\nv_rcp_f32 v87, v87\nRa 184,2,30720\n"
"v_xor_b32 v25, s3, v25\nMvav 148,136,76\nD1 1,16,46,3072\nMvav 156,140,76\nRa 188,2,31744\nMU 128,128,86\nMU 129,129,86\nCV 96,128,129\nMU 130,130,86\nMU 131,131,86\nRa 128,2,0\n"
"CV 97,130,131\nMU 132,132,87\nMvav 164,144,76\nMU 133,133,87\nCV 100,132,133\nMvav 172,148,76\nRa 132,2,1024\nMU 134,134,87\nMU 135,135,87\nWL 8\ns_add_u32 m0, s89, 131072\n"
"s_add_u32 s46, s96, 1212416\nRa 136,2,2048\nCV 101,134,135\nMU 136,136,86\nMvav 180,152,76\nD1 1,16,46,0\nMU 137,137,86\nMvav 188,156,76\nRa 140,2,3072\nCV 98,136,137\n"
"MU 138,138,86\nMU 139,139,86\nCV 99,138,139\nMU 140,140,87\nRa 144,2,4096\nMU 141,141,87\nCV 102,140,141\nMvav 196,160,76\nMU 142,142,87\nMU 143,143,87\nMvav 204,164,76\n"
"Ra 148,2,5120\nCV 103,142,143\nMU 144,144,86\nWL 8\nMU 145,145,86\nCV 104,144,145\nRa 152,2,6144\nMU 146,146,86\nMU 147,147,86\nMvav 212,168,76\nD1 1,16,46,1024\nCV 105,146,147\n"
"Mvav 220,172,76\nRa 156,2,7168\nMU 148,148,87\nMU 149,149,87\nCV 108,148,149\nMU 150,150,87\nMU 151,151,87\nRa 160,2,8192\nCV 109,150,151\nMU 152,152,86\nMvav 228,176,76\n"
"MU 153,153,86\nCV 106,152,153\nMvav 236,180,76\nRa 164,2,9216\nMU 154,154,86\nMU 155,155,86\nWL 8\nCV 107,154,155\nMU 156,156,87\nRa 168,2,10240\nMU 157,157,87\nCV 110,156,157\n"
"Mvav 244,184,76\nD1 1,16,46,2048\nMU 158,158,87\nMvav 252,188,76\nRa 172,2,11264\nMU 159,159,87\nCV 111,158,159\nMU 160,160,86\nMU 161,161,86\nCV 112,160,161\nZvav 128,128,96\n"
"Ra 176,2,12288\nMU 162,162,86\nMU 163,163,86\nMvav 128,132,104\nCV 113,162,163\nMU 164,164,87\nZvav 132,132,108\nRa 180,2,13312\nMU 165,165,87\nCV 116,164,165\nMvav 132,128,100\n"
"s_waitcnt vmcnt(15) lgkmcnt(8)\nMU 166,166,87\nMU 167,167,87\nZvav 140,136,100\nRa 184,2,14336\nCV 117,166,167\nMU 168,168,86\nMvav 140,140,108\nD1 1,16,46,3072\nMU 169,169,86\n"
"CV 114,168,169\nZvav 136,140,104\nRa 188,2,15360\ns_barrier\nMU 170,170,86\nMvav 136,136,96\nMU 171,171,86\nCV 115,170,171\nMU 172,172,87\nZvav 144,144,96\nRa 128,2,32768\n"
"MU 173,173,87\nCV 118,172,173\nMvav 144,148,104\nMU 174,174,87\nMU 175,175,87\nbuffer_store_dword v25, off, s[16:19], s54 offset:512 sc0 sc1\nZvav 148,148,108\nRa 132,2,33792\n"
"CV 119,174,175\nMU 176,176,86\nMvav 148,144,100\nWL 8\ns_add_u32 m0, s89, 49152\ns_add_u32 s46, s96, 1179648\nZvav 156,152,100\nRa 136,2,34816\nMU 177,177,86\nCV 120,176,177\n"
"Mvav 156,156,108\nD1 1,16,46,0\nMU 178,178,86\nMU 179,179,86\nZvav 152,156,104\nRa 140,2,35840\nCV 121,178,179\nMU 180,180,87\nMvav 152,152,96\nMU 181,181,87\nCV 124,180,181\n"
"MU 182,182,87\nZvav 64,160,96\nRa 144,2,36864\nMU 183,183,87\nCV 125,182,183\nMvav 64,164,104\nMU 184,184,86\nMU 185,185,86\nZvav 68,164,108\nRa 148,2,37888\nCV 122,184,185\n"
"MU 186,186,86\nMvav 68,160,100\nWL 8\nMU 187,187,86\nCV 123,186,187\nZvav 76,168,100\nRa 152,2,38912\nMU 188,188,87\nMU 189,189,87\nMvav 76,172,108\nD1 1,16,46,1024\n"
"CV 126,188,189\nMU 190,190,87\nZvav 72,172,104\nRa 156,2,39936\nMU 191,191,87\nCV 127,190,191\nMvav 72,168,96\nMU 192,192,86\nMU 193,193,86\nCV 32,192,193\nMvav 128,176,112\n"
"Ra 160,2,40960\nMU 194,194,86\nMU 195,195,86\nMvav 128,180,120\nCV 33,194,195\nMU 196,196,87\nMU 197,197,87\nMvav 132,180,124\nRa 164,2,41984\nCV 36,196,197\nMU 198,198,87\n"
"Mvav 132,176,116\nWL 8\nMU 199,199,87\nMvav 140,184,116\nRa 168,2,43008\nCV 37,198,199\nMU 200,200,86\nMvav 140,188,124\nD1 1,16,46,2048\nMU 201,201,86\nCV 34,200,201\n"
"Mvav 136,188,120\nRa 172,2,44032\nMU 202,202,86\nMU 203,203,86\nMvav 136,184,112\nCV 35,202,203\nMU 204,204,87\nMU 205,205,87\nMvav 144,128,112\nRa 176,2,45056\nCV 38,204,205\n"
"MU 206,206,87\nMvav 144,132,120\nMU 207,207,87\nCV 39,206,207\nMvav 148,132,124\nRa 180,2,46080\nMU 208,208,86\nMU 209,209,86\nMvav 148,128,116\nWL 8\nCV 40,208,209\n"
"MU 210,210,86\nMvav 156,136,116\nRa 184,2,47104\nMU 211,211,86\nCV 41,210,211\nMvav 156,140,124\nD1 1,16,46,3072\nMU 212,212,87\nMU 213,213,87\nMvav 152,140,120\nRa 188,2,48128\n"
"CV 44,212,213\nMvav 152,136,112\nMU 214,214,87\nMU 215,215,87\nCV 45,214,215\nMvav 64,144,112\nRa 128,2,49152\nMU 216,216,86\nMU 217,217,86\nMvav 64,148,120\nCV 42,216,217\n"
"MU 218,218,86\nMU 219,219,86\nMvav 68,148,124\nRa 132,2,50176\nCV 43,218,219\nMU 220,220,87\nMvav 68,144,116\nWL 8\ns_add_u32 m0, s89, 147456\ns_add_u32 s46, s96, 1245184\n"
"Mvav 76,152,116\nRa 136,2,51200\nMU 221,221,87\nCV 46,220,221\nMvav 76,156,124\nD1 1,16,46,0\nMU 222,222,87\nMU 223,223,87\nMvav 72,156,120\nRa 140,2,52224\nCV 47,222,223\n"
"Mvav 72,152,112\nMU 224,224,86\nMU 225,225,86\nCV 48,224,225\nMvav 128,160,32\nRa 144,2,53248\nMU 226,226,86\nMU 227,227,86\nMvav 128,164,40\nCV 49,226,227\nMU 228,228,87\n"
"Mvav 132,164,44\nRa 148,2,54272\nMU 229,229,87\nCV 52,228,229\nMvav 132,160,36\nWL 8\nMU 230,230,87\nMU 231,231,87\nMvav 140,168,36\nRa 152,2,55296\nCV 53,230,231\nMU 232,232,86\n"
"Mvav 140,172,44\nD1 1,16,46,1024\nMU 233,233,86\nCV 50,232,233\nMvav 136,172,40\nRa 156,2,56320\nMU 234,234,86\nMvav 136,168,32\nMU 235,235,86\nCV 51,234,235\nMU 236,236,87\n"
"Mvav 144,176,32\nRa 160,2,57344\nMU 237,237,87\nCV 54,236,237\nMvav 144,180,40\nMU 238,238,87\nMU 239,239,87\nCV 55,238,239\nMvav 148,180,44\nRa 164,2,58368\nMU 240,240,86\n"
"MU 241,241,86\nMvav 148,176,36\nWL 8\nCV 56,240,241\nMvav 156,184,36\nRa 168,2,59392\nMU 242,242,86\nMU 243,243,86\nMvav 156,188,44\nD1 1,16,46,2048\nCV 57,242,243\nMU 244,244,87\n"
"Mvav 152,188,40\nRa 172,2,60416\nMU 245,245,87\nCV 60,244,245\nMvav 152,184,32\nMU 246,246,87\nMU 247,247,87\nCV 61,246,247\nMvav 64,128,32\nRa 176,2,61440\nMU 248,248,86\n"
"MU 249,249,86\nMvav 64,132,40\nCV 58,248,249\nMU 250,250,86\nMvav 68,132,44\nRa 180,2,62464\nMU 251,251,86\nCV 59,250,251\nMvav 68,128,36\ns_waitcnt vmcnt(16) lgkmcnt(8)\n"
"MU 252,252,87\nMU 253,253,87\nMvav 76,136,36\nRa 184,2,63488\nCV 62,252,253\nMU 254,254,87\nMvav 76,140,44\nD1 1,16,46,3072\nMU 255,255,87\nCV 63,254,255\nMvav 72,140,40\n"
"Ra 188,2,64512\ns_barrier\nMvav 72,136,32\nMvav 128,144,48\nRa 128,3,0\nMvav 128,148,56\nMvav 132,148,60\nRa 132,3,1024\nMvav 132,144,52\nWL 8\nMvav 140,152,52\nRa 136,3,2048\n"
"Mvav 140,156,60\nMvav 136,156,56\nRa 140,3,3072\nMvav 136,152,48\nMvav 144,160,48\nRa 144,3,4096\nMvav 144,164,56\nMvav 148,164,60\nRa 148,3,5120\nCV 132,132,133\nMvav 148,160,52\n"
"WL 8\nCV 133,134,135\nMvav 156,168,52\nRa 152,3,6144\nCV 134,140,141\nMvav 156,172,60\nCV 135,142,143\ns_add_u32 s5, s94, 0\nMvav 152,172,56\nRa 156,3,7168\nSv 132,1,16,5,1024\n"
"Mvav 152,168,48\nCV 128,128,129\nCV 129,130,131\nMvav 64,176,48\nRa 160,3,8192\nCV 130,136,137\nMvav 64,180,56\nbuffer_load_dword v27, off, s[16:19], s60 offset:512 sc0 sc1\n"
"CV 131,138,139\nMvav 68,180,60\nRa 164,3,9216\nSv 128,1,16,5,0\nMvav 68,176,52\nWL 6\nCV 148,148,149\nMvav 76,184,52\nRa 168,3,10240\nCV 149,150,151\nMvav 76,188,60\n"
"CV 150,156,157\nCV 151,158,159\nMvav 72,188,56\nRa 172,3,11264\ns_add_u32 s5, s94, 131072\nMvav 72,184,48\nSv 148,1,16,5,1024\nCV 144,144,145\nZvav 160,128,96\nRa 176,3,12288\n"
"CV 145,146,147\nMvav 160,132,104\nCV 146,152,153\nCV 147,154,155\nMvav 160,136,112\nRa 180,3,13312\nSv 144,1,16,5,0\nMvav 160,140,120\nWL 6\nCV 68,68,69\nZvav 164,140,124\n"
"Ra 184,3,14336\nCV 69,70,71\nMvav 164,136,116\nCV 70,76,77\nCV 71,78,79\nMvav 164,132,108\nRa 188,3,15360\ns_add_u32 s5, s94, 262144\nMvav 164,128,100\nSv 68,1,16,5,1024\n"
"CV 64,64,65\nMvav 164,144,36\nRa 128,3,16384\nCV 65,66,67\nMvav 164,148,44\nCV 66,72,73\nCV 67,74,75\nMvav 164,152,52\nRa 132,3,17408\nSv 64,1,16,5,0\nMvav 164,156,60\nWL 6\n"
"Mvav 160,156,56\nRa 136,3,18432\nMvav 160,152,48\nMvav 160,148,40\nRa 140,3,19456\nMvav 160,144,32\nZvav 168,160,96\nRa 144,3,20480\nMvav 168,164,104\nMvav 168,168,112\n"
"Ra 148,3,21504\nMvav 168,172,120\nWL 6\nZvav 172,172,124\nRa 152,3,22528\nMvav 172,168,116\nMvav 172,164,108\nRa 156,3,23552\nMvav 172,160,100\nMvav 172,176,36\nRa 160,3,24576\n"
"Mvav 172,180,44\nMvav 172,184,52\nRa 164,3,25600\nMvav 172,188,60\nWL 6\nMvav 168,188,56\nRa 168,3,26624\nMvav 168,184,48\nMvav 168,180,40\nRa 172,3,27648\nMvav 168,176,32\n"
"Zvav 176,128,96\nRa 176,3,28672\nCV 164,164,165\nMvav 176,132,104\nCV 165,166,167\nCV 166,172,173\nMvav 176,136,112\nRa 180,3,29696\nCV 167,174,175\nMvav 176,140,120\n"
"s_waitcnt vmcnt(11) lgkmcnt(6)\ns_add_u32 s5, s94, 393216\nZvav 180,140,124\nRa 184,3,30720\nSv 164,1,16,5,1024\nMvav 180,136,116\nCV 160,160,161\nCV 161,162,163\n"
"Mvav 180,132,108\nRa 188,3,31744\ns_barrier\nMvav 180,128,100\nCV 162,168,169\nCV 163,170,171\nMvav 180,144,36\nRa 128,3,32768\nSv 160,1,16,5,0\nMvav 180,148,44\nMvav 180,152,52\n"
"Ra 132,3,33792\nMvav 180,156,60\nWL 6\ns_add_u32 m0, s89, 81920\ns_add_u32 s46, s96, 1277952\nMvav 176,156,56\nRa 136,3,34816\nMvav 176,152,48\nD1 1,16,46,0\nMvav 176,148,40\n"
"Ra 140,3,35840\nMvav 176,144,32\nZvav 184,160,96\nRa 144,3,36864\nMvav 184,164,104\nMvav 184,168,112\nRa 148,3,37888\nMvav 184,172,120\nWL 6\nZvav 188,172,124\nRa 152,3,38912\n"
"Mvav 188,168,116\nD1 1,16,46,1024\nMvav 188,164,108\nRa 156,3,39936\nMvav 188,160,100\nMvav 188,176,36\nRa 160,3,40960\nWV 9\nv_cmp_ne_u32 vcc, s15, v27\ns_nop 0\n"
"s_cbranch_vccnz .S513\n.S514:\nMvav 188,180,44\ns_add_u32 s4, s92, 0\nMvav 188,184,52\nRa 164,3,41984\nMvav 188,188,60\nWL 6\n"
"buffer_load_dwordx4 v[208:211], v1, s[16:19], s4 offen offset:0 sc0 sc1\nMvav 184,188,56\nRa 168,3,43008\nMvav 184,184,48\nD1 1,16,46,2048\nMvav 184,180,40\nRa 172,3,44032\n"
"Mvav 184,176,32\nbuffer_load_dwordx4 v[212:215], v1, s[16:19], s4 offen offset:1024 sc0 sc1\nZvav 192,128,96\nRa 176,3,45056\nv_lshrrev_b32 v88, 8, v1\nMvav 192,132,104\n"
"v_lshlrev_b32 v89, 5, v88\nCV 180,180,181\nMvav 192,136,112\nRa 180,3,46080\nCV 181,182,183\nMvav 192,140,120\nWL 6\nCV 182,188,189\nZvav 196,140,124\nRa 184,3,47104\n"
"v_bfe_u32 v90, v1, 4, 4\nMvav 196,136,116\nD1 1,16,46,3072\nCV 183,190,191\nMvav 196,132,108\nRa 188,3,48128\ns_add_u32 s5, s94, 524288\nMvav 196,128,100\n"
"v_mul_u32_u24 v91, 896, v90\nSv 180,1,16,5,1024\nMvav 196,144,36\nRa 128,3,49152\nv_lshl_add_u32 v91, v88, 4, v91\nMvav 196,148,44\nv_add_u32 v90, s21, v90\nCV 176,176,177\n"
"Mvav 196,152,52\nRa 132,3,50176\nCV 177,178,179\nMvav 196,156,60\nWL 6\ns_add_u32 m0, s89, 65536\ns_add_u32 s46, s96, 1310720\nMvav 192,156,56\nRa 136,3,51200\n"
"v_lshlrev_b32 v88, 3, v90\nMvav 192,152,48\nD1 1,16,46,0\nCV 178,184,185\nMvav 192,148,40\nRa 140,3,52224\nCV 179,186,187\nMvav 192,144,32\nv_add_u32 v94, 64, v90\n"
"Sv 176,1,16,5,0\nZvav 200,160,96\nRa 144,3,53248\ns_mov_b32 s63, 0xc040000\nMvav 200,164,104\nv_lshrrev_b32 v92, 8, v1\nMvav 200,168,112\nRa 148,3,54272\nMvav 200,172,120\nWL 6\n"
"Zvav 204,172,124\nRa 152,3,55296\nv_bfe_u32 v93, v1, 6, 2\nMvav 204,168,116\nD1 1,16,46,1024\nv_cmp_eq_u32 vcc, v92, v93\nv_and_b32 v92, 16, v1\nv_mov_b32 v93, 0x3f80\n"
"v_cndmask_b32 v93, 0, v93, vcc\nMvav 204,164,108\nRa 156,3,56320\nMvav 204,160,100\nv_lshlrev_b32 v93, v92, v93\nMvav 204,176,36\nRa 160,3,57344\nv_and_b32 v92, 32, v1\n"
"Mvav 204,180,44\ns_add_u32 s4, s92, 131072\nv_cmp_eq_u32 vcc, 0, v92\nv_mov_b32 v4, 0\nv_mov_b32 v5, 0\nv_cndmask_b32 v6, 0, v93, vcc\nMvav 204,184,52\nRa 164,3,58368\n"
"Mvav 204,188,60\nWL 6\nbuffer_load_dwordx4 v[216:219], v1, s[16:19], s4 offen offset:0 sc0 sc1\nMvav 200,188,56\nRa 168,3,59392\nv_sub_u32 v7, v93, v6\nMvav 200,184,48\n"
"D1 1,16,46,2048\nMvav 200,180,40\nRa 172,3,60416\nMvav 200,176,32\nbuffer_load_dwordx4 v[220:223], v1, s[16:19], s4 offen offset:1024 sc0 sc1\nZvav 160,128,96\nRa 176,3,61440\n"
"v_mov_b32 v8, 0\nMvav 160,132,104\nv_mov_b32 v9, 0\nCV 196,196,197\nMvav 160,136,112\nRa 180,3,62464\nCV 197,198,199\nMvav 160,140,120\nWL 6\nCV 198,204,205\nZvav 164,140,124\n"
"Ra 184,3,63488\nCV 199,206,207\nMvav 164,136,116\nD1 1,16,46,3072\ns_add_u32 s5, s94, 655360\nMvav 164,132,108\nRa 188,3,64512\ns_barrier\nMvav 164,128,100\nSv 196,1,16,5,1024\n"
"CV 192,192,193\nMvav 164,144,36\nRa 128,0,0\nCV 193,194,195\nMvav 164,148,44\nCV 194,200,201\nCV 195,202,203\nMvav 164,152,52\nRa 132,0,1024\nSv 192,1,16,5,0\nMvav 164,156,60\n"
"WL 6\ns_add_u32 m0, s89, 98304\ns_add_u32 s46, s96, 1343488\nMvav 160,156,56\nRa 136,0,2048\nMvav 160,152,48\nD1 1,16,46,0\nMvav 160,148,40\nRa 140,0,3072\nMvav 160,144,32\n"
"Zvav 168,160,96\nRa 144,0,4096\nWV 12\nv_max_u32 v21, v211, v215\nv_cmp_eq_u32 vcc, -1, v21\ns_nop 0\ns_cbranch_vccnz .S489\n.S490:\nMvav 168,164,104\n"
"v_lshlrev_b32 v176, 16, v208\nMvav 168,168,112\nRa 148,0,5120\nMvav 168,172,120\nWL 6\nv_and_b32 v177, 0xffff0000, v208\nZvav 172,172,124\nRa 152,0,6144\nMvav 172,168,116\n"
"D1 1,16,46,1024\nMvav 172,164,108\nRa 156,0,7168\nv_lshlrev_b32 v178, 16, v209\nMvav 172,160,100\nv_and_b32 v179, 0xffff0000, v209\nMvav 172,176,36\nRa 160,0,8192\n"
"v_lshlrev_b32 v180, 16, v212\nMvav 172,180,44\nv_and_b32 v181, 0xffff0000, v212\ns_add_u32 s4, s92, 262144\nMvav 172,184,52\nRa 164,0,9216\nMvav 172,188,60\nWL 6\n"
"v_lshlrev_b32 v182, 16, v213\nbuffer_load_dwordx4 v[224:227], v1, s[16:19], s4 offen offset:0 sc0 sc1\nMvav 168,188,56\nRa 168,0,10240\nMvav 168,184,48\nD1 1,16,46,2048\n"
"Mvav 168,180,40\nRa 172,0,11264\nv_and_b32 v183, 0xffff0000, v213\nMvav 168,176,32\nv_lshlrev_b32 v184, 16, v210\n"
"buffer_load_dwordx4 v[228:231], v1, s[16:19], s4 offen offset:1024 sc0 sc1\nMvav 176,128,96\nRa 176,0,12288\nv_and_b32 v185, 0xffff0000, v210\nMvav 176,132,104\n"
"v_lshlrev_b32 v186, 16, v211\nCV 164,164,165\nMvav 176,136,112\nRa 180,0,13312\nCV 165,166,167\nMvav 176,140,120\nWL 6\nCV 166,172,173\nMvav 180,140,124\nRa 184,0,14336\n"
"v_and_b32 v187, 0xffff0000, v211\nMvav 180,136,116\nD1 1,16,46,3072\nCV 167,174,175\nMvav 180,132,108\nRa 188,0,15360\ns_add_u32 s5, s94, 786432\nMvav 180,128,100\n"
"v_lshlrev_b32 v188, 16, v214\nSv 164,1,16,5,1024\nMvav 180,144,36\nRa 128,0,16384\nv_and_b32 v189, 0xffff0000, v214\nMvav 180,148,44\nv_lshlrev_b32 v190, 16, v215\nCV 160,160,161\n"
"Mvav 180,152,52\nRa 132,0,17408\nCV 161,162,163\nMvav 180,156,60\nWL 6\ns_add_u32 m0, s89, 114688\ns_add_u32 s46, s96, 1376256\nMvav 176,156,56\nRa 136,0,18432\n"
"v_and_b32 v191, 0xffff0000, v215\nMvav 176,152,48\nD1 1,16,46,0\nCV 162,168,169\nMvav 176,148,40\nRa 140,0,19456\nCV 163,170,171\nMvav 176,144,32\nSv 160,1,16,5,0\n"
"Mvav 184,160,96\nRa 144,0,20480\nWV 12\nv_max_u32 v21, v219, v223\nv_cmp_eq_u32 vcc, -1, v21\ns_nop 0\ns_cbranch_vccnz .S492\n.S493:\nMvav 184,164,104\n"
"v_lshlrev_b32 v192, 16, v216\nMvav 184,168,112\nRa 148,0,21504\nMvav 184,172,120\nWL 6\nv_and_b32 v193, 0xffff0000, v216\nMvav 188,172,124\nRa 152,0,22528\nMvav 188,168,116\n"
"D1 1,16,46,1024\nMvav 188,164,108\nRa 156,0,23552\nv_lshlrev_b32 v194, 16, v217\nMvav 188,160,100\nv_and_b32 v195, 0xffff0000, v217\nMvav 188,176,36\nRa 160,0,24576\n"
"v_lshlrev_b32 v196, 16, v220\nMvav 188,180,44\nv_and_b32 v197, 0xffff0000, v220\ns_add_u32 s4, s92, 393216\nMvav 188,184,52\nRa 164,0,25600\nMvav 188,188,60\nWL 6\n"
"v_lshlrev_b32 v198, 16, v221\nbuffer_load_dwordx4 v[208:211], v1, s[16:19], s4 offen offset:0 sc0 sc1\nMvav 184,188,56\nRa 168,0,26624\nMvav 184,184,48\nD1 1,16,46,2048\n"
"Mvav 184,180,40\nRa 172,0,27648\nv_and_b32 v199, 0xffff0000, v221\nMvav 184,176,32\nv_lshlrev_b32 v200, 16, v218\n"
"buffer_load_dwordx4 v[212:215], v1, s[16:19], s4 offen offset:1024 sc0 sc1\nMvav 192,128,96\nRa 176,0,28672\nv_and_b32 v201, 0xffff0000, v218\nMvav 192,132,104\n"
"v_lshlrev_b32 v202, 16, v219\nCV 180,180,181\nMvav 192,136,112\nRa 180,0,29696\nCV 181,182,183\nMvav 192,140,120\ns_waitcnt vmcnt(15) lgkmcnt(6)\nCV 182,188,189\nMvav 196,140,124\n"
"Ra 184,0,30720\nv_and_b32 v203, 0xffff0000, v219\nMvav 196,136,116\nD1 1,16,46,3072\nCV 183,190,191\nMvav 196,132,108\nRa 188,0,31744\ns_barrier\nMvav 196,128,100\n"
"v_lshlrev_b32 v204, 16, v222\ns_add_u32 s5, s94, 917504\nMvav 196,144,36\nRa 128,2,16384\nv_and_b32 v205, 0xffff0000, v222\nMvav 196,148,44\nv_lshlrev_b32 v206, 16, v223\n"
"Sv 180,1,16,5,1024\nMvav 196,152,52\nRa 132,2,17408\nCV 176,176,177\nMvav 196,156,60\nWL 6\ns_add_u32 m0, s89, 0\ns_add_u32 s46, s96, 1409024\nMvav 192,156,56\nRa 136,2,18432\n"
"v_and_b32 v207, 0xffff0000, v223\nMvav 192,152,48\nD1 1,16,46,0\nCV 177,178,179\nMvav 192,148,40\nRa 140,2,19456\nCV 178,184,185\nMvav 192,144,32\nCV 179,186,187\nSv 176,1,16,5,0\n"
"Mvav 200,160,96\nRa 144,2,20480\nWV 12\nv_max_u32 v21, v227, v231\nv_cmp_eq_u32 vcc, -1, v21\ns_nop 0\ns_cbranch_vccnz .S495\n.S496:\nMvav 200,164,104\n"
"v_lshlrev_b32 v160, 16, v224\nMvav 200,168,112\nRa 148,2,21504\nMvav 200,172,120\nWL 6\nv_and_b32 v161, 0xffff0000, v224\nMvav 204,172,124\nRa 152,2,22528\nMvav 204,168,116\n"
"D1 1,16,46,1024\nMvav 204,164,108\nRa 156,2,23552\nv_lshlrev_b32 v162, 16, v225\nMvav 204,160,100\nv_and_b32 v163, 0xffff0000, v225\nMvav 204,176,36\nRa 160,2,24576\n"
"v_lshlrev_b32 v164, 16, v228\nMvav 204,180,44\nv_and_b32 v165, 0xffff0000, v228\ns_add_u32 s4, s92, 524288\nMvav 204,184,52\nRa 164,2,25600\nMvav 204,188,60\nWL 6\n"
"v_lshlrev_b32 v166, 16, v229\nbuffer_load_dwordx4 v[216:219], v1, s[16:19], s4 offen offset:0 sc0 sc1\nMvav 200,188,56\nRa 168,2,26624\nMvav 200,184,48\nD1 1,16,46,2048\n"
"Mvav 200,180,40\nRa 172,2,27648\nv_and_b32 v167, 0xffff0000, v229\nMvav 200,176,32\nv_lshlrev_b32 v168, 16, v226\n"
"buffer_load_dwordx4 v[220:223], v1, s[16:19], s4 offen offset:1024 sc0 sc1\nMvav 160,128,96\nRa 176,2,28672\nv_and_b32 v169, 0xffff0000, v226\nMvav 160,132,104\n"
"v_lshlrev_b32 v170, 16, v227\nCV 196,196,197\nMvav 160,136,112\nRa 180,2,29696\nCV 197,198,199\nMvav 160,140,120\nWL 6\nCV 198,204,205\nMvav 164,140,124\nRa 184,2,30720\n"
"v_and_b32 v24, 0x70, v1\nMvav 164,136,116\nD1 1,16,46,3072\nCV 199,206,207\nMvav 164,132,108\nRa 188,2,31744\nv_and_b32 v171, 0xffff0000, v227\nMvav 164,128,100\n"
"v_lshlrev_b32 v172, 16, v230\ns_add_u32 s5, s94, 1048576\nMvav 164,144,36\nRa 128,2,0\nv_lshlrev_b32 v24, 5, v24\nMvav 164,148,44\nv_and_b32 v173, 0xffff0000, v230\n"
"Sv 196,1,16,5,1024\nMvav 164,152,52\nRa 132,2,1024\nCV 192,192,193\nMvav 164,156,60\nWL 6\nv_lshlrev_b32 v174, 16, v231\nMvav 160,156,56\nRa 136,2,2048\n"
"buffer_load_dword v27, v24, s[16:19], s41 offen sc0 sc1\nMvav 160,152,48\nv_and_b32 v175, 0xffff0000, v231\nCV 193,194,195\nMvav 160,148,40\nRa 140,2,3072\nCV 194,200,201\n"
"Mvav 160,144,32\nCV 195,202,203\nSv 192,1,16,5,0\nMvav 168,160,96\nRa 144,2,4096\nWV 12\nv_max_u32 v21, v211, v215\nv_cmp_eq_u32 vcc, -1, v21\ns_nop 0\ns_cbranch_vccnz .S498\n"
".S499:\nMvav 168,164,104\nv_lshlrev_b32 v176, 16, v208\nMvav 168,168,112\nRa 148,2,5120\nMvav 168,172,120\nWL 6\nMvav 172,172,124\nRa 152,2,6144\nv_and_b32 v177, 0xffff0000, v208\n"
"Mvav 172,168,116\nv_lshlrev_b32 v178, 16, v209\nMvav 172,164,108\nRa 156,2,7168\nMvav 172,160,100\nv_and_b32 v179, 0xffff0000, v209\nMvav 172,176,36\nRa 160,2,8192\n"
"v_lshlrev_b32 v180, 16, v212\nMvav 172,180,44\nv_and_b32 v181, 0xffff0000, v212\ns_add_u32 s4, s92, 655360\nMvav 172,184,52\nRa 164,2,9216\nMvav 172,188,60\nWL 6\n"
"buffer_load_dwordx4 v[224:227], v1, s[16:19], s4 offen offset:0 sc0 sc1\nMvav 168,188,56\nRa 168,2,10240\nv_lshlrev_b32 v182, 16, v213\nMvav 168,184,48\n"
"v_and_b32 v183, 0xffff0000, v213\nMvav 168,180,40\nRa 172,2,11264\nMvav 168,176,32\nv_lshlrev_b32 v184, 16, v210\n"
"buffer_load_dwordx4 v[228:231], v1, s[16:19], s4 offen offset:1024 sc0 sc1\nMvav 176,128,96\nRa 176,2,12288\nv_and_b32 v185, 0xffff0000, v210\nMvav 176,132,104\n"
"v_lshlrev_b32 v186, 16, v211\nCV 164,164,165\nMvav 176,136,112\nRa 180,2,13312\nCV 165,166,167\nMvav 176,140,120\ns_waitcnt vmcnt(13) lgkmcnt(6)\nCV 166,172,173\nMvav 180,140,124\n"
"Ra 184,2,14336\nCV 167,174,175\nMvav 180,136,116\nv_and_b32 v187, 0xffff0000, v211\ns_add_u32 s5, s94, 1179648\nMvav 180,132,108\nRa 188,2,15360\ns_barrier\nMvav 180,128,100\n"
"v_lshlrev_b32 v188, 16, v214\nSv 164,1,16,5,1024\nMvav 180,144,36\nRa 128,2,32768\nv_and_b32 v189, 0xffff0000, v214\nMvav 180,148,44\nv_lshlrev_b32 v190, 16, v215\nCV 160,160,161\n"
"Mvav 180,152,52\nRa 132,2,33792\nCV 161,162,163\nMvav 180,156,60\nWL 6\nCV 162,168,169\nMvav 176,156,56\nRa 136,2,34816\nCV 163,170,171\nMvav 176,152,48\n"
"v_and_b32 v191, 0xffff0000, v215\nSv 160,1,16,5,0\nMvav 176,148,40\nRa 140,2,35840\nMvav 176,144,32\nMvav 184,160,96\nRa 144,2,36864\nWV 8\nv_max_u32 v21, v219, v223\n"
"v_cmp_eq_u32 vcc, -1, v21\ns_nop 0\ns_cbranch_vccnz .S501\n.S502:\nMvav 184,164,104\nv_lshlrev_b32 v192, 16, v216\nMvav 184,168,112\nRa 148,2,37888\nMvav 184,172,120\nWL 6\n"
"Mvav 188,172,124\nRa 152,2,38912\nv_and_b32 v193, 0xffff0000, v216\nMvav 188,168,116\nv_lshlrev_b32 v194, 16, v217\nMvav 188,164,108\nRa 156,2,39936\nMvav 188,160,100\n"
"v_and_b32 v195, 0xffff0000, v217\nMvav 188,176,36\nRa 160,2,40960\nv_lshlrev_b32 v196, 16, v220\nMvav 188,180,44\nv_and_b32 v197, 0xffff0000, v220\ns_add_u32 s4, s92, 786432\n"
"Mvav 188,184,52\nRa 164,2,41984\nMvav 188,188,60\nWL 6\nbuffer_load_dwordx4 v[208:211], v1, s[16:19], s4 offen offset:0 sc0 sc1\nMvav 184,188,56\nRa 168,2,43008\n"
"v_lshlrev_b32 v198, 16, v221\nMvav 184,184,48\nv_and_b32 v199, 0xffff0000, v221\nMvav 184,180,40\nRa 172,2,44032\nMvav 184,176,32\nv_lshlrev_b32 v200, 16, v218\n"
"buffer_load_dwordx4 v[212:215], v1, s[16:19], s4 offen offset:1024 sc0 sc1\nMvav 192,128,96\nRa 176,2,45056\nv_and_b32 v201, 0xffff0000, v218\nMvav 192,132,104\n"
"v_lshlrev_b32 v202, 16, v219\nCV 180,180,181\nMvav 192,136,112\nRa 180,2,46080\nCV 181,182,183\nMvav 192,140,120\nWL 6\nCV 182,188,189\nMvav 196,140,124\nRa 184,2,47104\n"
"CV 183,190,191\nMvav 196,136,116\nv_and_b32 v203, 0xffff0000, v219\ns_add_u32 s5, s94, 1310720\nMvav 196,132,108\nRa 188,2,48128\nSv 180,1,16,5,1024\nMvav 196,128,100\n"
"v_lshlrev_b32 v204, 16, v222\nCV 176,176,177\nMvav 196,144,36\nRa 128,2,49152\nv_and_b32 v205, 0xffff0000, v222\nMvav 196,148,44\nv_lshlrev_b32 v206, 16, v223\nCV 177,178,179\n"
"Mvav 196,152,52\nRa 132,2,50176\nCV 178,184,185\nMvav 196,156,60\nWL 6\nCV 179,186,187\nMvav 192,156,56\nRa 136,2,51200\nSv 176,1,16,5,0\nMvav 192,152,48\n"
"v_and_b32 v207, 0xffff0000, v223\nMvav 192,148,40\nRa 140,2,52224\nMvav 192,144,32\nMvav 200,160,96\nRa 144,2,53248\nWV 6\nv_max_u32 v21, v227, v231\nv_cmp_eq_u32 vcc, -1, v21\n"
"s_nop 0\ns_cbranch_vccnz .S504\n.S505:\nMvav 200,164,104\nv_lshlrev_b32 v160, 16, v224\nv_xor_b32 v27, s15, v27\nv_and_b32 v26, 0xffffdfff, v27\nv_cmp_ne_u32 s[64:65], 0, v27\n"
"v_cmp_ne_u32 vcc, 0, v26\ns_nop 0\ns_cbranch_vccnz .S516\n.S517:\nMvav 200,168,112\nRa 148,2,54272\ns_bitcmp1_b32 s64, 0\ns_cselect_b32 s2, 0x2000, 0\nMvav 200,172,120\nWL 6\n"
"Mvav 204,172,124\nRa 152,2,55296\nv_and_b32 v161, 0xffff0000, v224\nMvav 204,168,116\nv_lshlrev_b32 v162, 16, v225\nMvav 204,164,108\nRa 156,2,56320\ns_xor_b32 s2, s2, s20\n"
"Mvav 204,160,100\nv_and_b32 v163, 0xffff0000, v225\nMvav 204,176,36\nRa 160,2,57344\nv_lshlrev_b32 v164, 16, v228\nMvav 204,180,44\nv_and_b32 v165, 0xffff0000, v228\n"
"Mvav 204,184,52\nRa 164,2,58368\nMvav 204,188,60\nWL 6\ns_add_u32 s24, s2, 0x0\nMvav 200,188,56\nRa 168,2,59392\nv_lshlrev_b32 v166, 16, v229\nMvav 200,184,48\n"
"v_and_b32 v167, 0xffff0000, v229\ns_bitcmp1_b32 s64, 1\ns_cselect_b32 s2, 0x2000, 0\nMvav 200,180,40\nRa 172,2,60416\nMvav 200,176,32\nv_lshlrev_b32 v168, 16, v226\n"
"Mvav 160,128,96\nRa 176,2,61440\nv_and_b32 v169, 0xffff0000, v226\nMvav 160,132,104\nv_lshlrev_b32 v170, 16, v227\nCV 196,196,197\nMvav 160,136,112\nRa 180,2,62464\n"
"CV 197,198,199\nMvav 160,140,120\nWL 6\nCV 198,204,205\nMvav 164,140,124\nRa 184,2,63488\nCV 199,206,207\nMvav 164,136,116\nv_and_b32 v171, 0xffff0000, v227\n"
"s_add_u32 s5, s94, 1441792\nMvav 164,132,108\nRa 188,2,64512\ns_barrier\nMvav 164,128,100\nv_lshlrev_b32 v172, 16, v230\nSv 196,1,16,5,1024\nMvav 164,144,36\nRa 128,3,0\n"
"v_and_b32 v173, 0xffff0000, v230\nMvav 164,148,44\nv_lshlrev_b32 v174, 16, v231\nCV 192,192,193\nMvav 164,152,52\nRa 132,3,1024\nCV 193,194,195\nMvav 164,156,60\nWL 6\n"
"s_xor_b32 s2, s2, s20\nMvav 160,156,56\nRa 136,3,2048\nCV 194,200,201\nMvav 160,152,48\nv_and_b32 v175, 0xffff0000, v231\nCV 195,202,203\nMvav 160,148,40\nRa 140,3,3072\n"
"Sv 192,1,16,5,0\nMvav 160,144,32\ns_add_u32 s25, s2, 0x1800000\ns_bitcmp1_b32 s64, 2\ns_cselect_b32 s2, 0x2000, 0\nMvav 168,160,96\nRa 144,3,4096\nWV 4\nv_max_u32 v21, v211, v215\n"
"v_cmp_eq_u32 vcc, -1, v21\ns_nop 0\ns_cbranch_vccnz .S507\n.S508:\nMvav 168,164,104\nv_lshlrev_b32 v176, 16, v208\nMvav 168,168,112\nRa 148,3,5120\nMvav 168,172,120\nWL 6\n"
"Mvav 172,172,124\nRa 152,3,6144\nv_and_b32 v177, 0xffff0000, v208\nMvav 172,168,116\nv_lshlrev_b32 v178, 16, v209\nMvav 172,164,108\nRa 156,3,7168\ns_xor_b32 s2, s2, s20\n"
"Mvav 172,160,100\nv_and_b32 v179, 0xffff0000, v209\nMvav 172,176,36\nRa 160,3,8192\nv_lshlrev_b32 v180, 16, v212\nMvav 172,180,44\nv_and_b32 v181, 0xffff0000, v212\n"
"Mvav 172,184,52\nRa 164,3,9216\nMvav 172,188,60\nWL 6\ns_add_u32 s26, s2, 0x3000000\nMvav 168,188,56\nRa 168,3,10240\nv_lshlrev_b32 v182, 16, v213\nMvav 168,184,48\n"
"v_and_b32 v183, 0xffff0000, v213\ns_bitcmp1_b32 s64, 3\ns_cselect_b32 s2, 0x2000, 0\nMvav 168,180,40\nRa 172,3,11264\nMvav 168,176,32\nv_lshlrev_b32 v184, 16, v210\n"
"Mvav 176,128,96\nRa 176,3,12288\ns_xor_b32 s2, s2, s20\nMvav 176,132,104\nv_and_b32 v185, 0xffff0000, v210\nCV 164,164,165\nMvav 176,136,112\nRa 180,3,13312\nCV 165,166,167\n"
"Mvav 176,140,120\nWL 6\nCV 166,172,173\nMvav 180,140,124\nRa 184,3,14336\nCV 167,174,175\nMvav 180,136,116\nv_lshlrev_b32 v186, 16, v211\ns_add_u32 s5, s94, 1572864\n"
"Mvav 180,132,108\nRa 188,3,15360\ns_add_u32 s27, s2, 0x4800000\nMvav 180,128,100\nv_and_b32 v187, 0xffff0000, v211\ns_bitcmp1_b32 s64, 4\ns_cselect_b32 s2, 0x2000, 0\n"
"Mvav 180,144,36\nv_lshlrev_b32 v188, 16, v214\nSv 164,1,16,5,1024\nMvav 180,148,44\nv_and_b32 v189, 0xffff0000, v214\nCV 160,160,161\nMvav 180,152,52\n"
"v_lshlrev_b32 v190, 16, v215\nCV 161,162,163\nMvav 180,156,60\nWL 4\ns_xor_b32 s2, s2, s20\nMvav 176,156,56\nv_and_b32 v191, 0xffff0000, v215\nCV 162,168,169\nMvav 176,152,48\n"
"s_add_u32 s32, s2, 0x6000000\ns_bitcmp1_b32 s64, 5\ns_cselect_b32 s2, 0x2000, 0\nMvav 176,148,40\ns_xor_b32 s2, s2, s20\nCV 163,170,171\nMvav 176,144,32\n"
"s_add_u32 s33, s2, 0x7800000\ns_bitcmp1_b32 s64, 6\ns_cselect_b32 s2, 0x2000, 0\nMvav 184,160,96\ns_xor_b32 s2, s2, s20\ns_add_u32 s34, s2, 0x9000000\nMvav 184,164,104\n"
"s_bitcmp1_b32 s64, 7\ns_cselect_b32 s2, 0x2000, 0\nMvav 184,168,112\ns_xor_b32 s2, s2, s20\nSv 160,1,16,5,0\nMvav 184,172,120\nWL 0\nMvav 188,172,124\n"
"s_add_u32 s35, s2, 0xa800000\nMvav 188,168,116\nMvav 188,164,108\nMvav 188,160,100\nMvav 188,176,36\nMvav 188,180,44\nMvav 188,184,52\nMvav 188,188,60\nMvav 184,188,56\n"
"Mvav 184,184,48\nMvav 184,180,40\nMvav 184,176,32\nCV 180,180,181\nCV 181,182,183\ns_nop 1\nCV 182,188,189\nCV 183,190,191\ns_add_u32 s5, s94, 1703936\nSv 180,1,16,5,1024\n"
"CV 176,176,177\nCV 177,178,179\nCV 178,184,185\nCV 179,186,187\nSv 176,1,16,5,0\ns_cmp_eq_u32 s13, 7\ns_cbranch_scc1 .Z519\n"
"buffer_load_dwordx4 v[12:15], v91, s[28:31], s22 offen offset:0\nbuffer_load_dwordx4 v[16:19], v91, s[28:31], s23 offen offset:0\n"
"buffer_load_dwordx4 v[64:67], v89, s[16:19], s40 offen offset:0 sc0 sc1\nbuffer_load_dwordx4 v[68:71], v89, s[16:19], s40 offen offset:16 sc0 sc1\n"
"buffer_load_dwordx4 v[72:75], v89, s[16:19], s40 offen offset:1792 sc0 sc1\nbuffer_load_dwordx4 v[76:79], v89, s[16:19], s40 offen offset:1808 sc0 sc1\n"
"buffer_load_dwordx2 v[80:81], v88, s[16:19], s63 offen offset:0 sc0 sc1\nbuffer_load_dwordx2 v[82:83], v88, s[16:19], s63 offen offset:512 sc0 sc1\n"
"buffer_load_dwordx4 v[96:99], v1, s[16:19], s24 offen offset:0 sc0 sc1\nbuffer_load_dwordx4 v[100:103], v1, s[16:19], s24 offen offset:1024 sc0 sc1\n"
"buffer_load_dwordx4 v[104:107], v1, s[16:19], s25 offen offset:0 sc0 sc1\nbuffer_load_dwordx4 v[108:111], v1, s[16:19], s25 offen offset:1024 sc0 sc1\n"
"buffer_load_dwordx4 v[112:115], v1, s[16:19], s26 offen offset:0 sc0 sc1\nbuffer_load_dwordx4 v[116:119], v1, s[16:19], s26 offen offset:1024 sc0 sc1\n"
"buffer_load_dwordx4 v[120:123], v1, s[16:19], s27 offen offset:0 sc0 sc1\nbuffer_load_dwordx4 v[124:127], v1, s[16:19], s27 offen offset:1024 sc0 sc1\n"
"buffer_load_dwordx4 v[32:35], v1, s[16:19], s32 offen offset:0 sc0 sc1\nbuffer_load_dwordx4 v[36:39], v1, s[16:19], s32 offen offset:1024 sc0 sc1\n"
"buffer_load_dwordx4 v[40:43], v1, s[16:19], s33 offen offset:0 sc0 sc1\nbuffer_load_dwordx4 v[44:47], v1, s[16:19], s33 offen offset:1024 sc0 sc1\n"
"buffer_load_dwordx4 v[48:51], v1, s[16:19], s34 offen offset:0 sc0 sc1\nbuffer_load_dwordx4 v[52:55], v1, s[16:19], s34 offen offset:1024 sc0 sc1\n"
"buffer_load_dwordx4 v[56:59], v1, s[16:19], s35 offen offset:0 sc0 sc1\nbuffer_load_dwordx4 v[60:63], v1, s[16:19], s35 offen offset:1024 sc0 sc1\nWV 16\nv_xor_b32 v66, s15, v66\n"
"v_xor_b32 v70, s15, v70\nv_xor_b32 v74, s15, v74\nv_xor_b32 v78, s15, v78\nv_xor_b32 v81, s15, v81\nv_xor_b32 v83, s15, v83\nv_or3_b32 v92, v66, v70, v74\n"
"v_or3_b32 v92, v92, v78, v81\nv_or_b32 v92, v92, v83\nv_cmp_ne_u32 vcc, 0, v92\ns_nop 0\ns_cbranch_vccnz .S510\n.S511:\nv_and_b32 v92, 0x7ff, v67\nv_lshrrev_b32 v93, 16, v67\n"
"v_cmp_eq_u32 s[44:45], v92, v90\nv_cmp_eq_u32 s[46:47], v93, v90\nv_cmp_eq_u32 s[48:49], v92, v94\nv_cmp_eq_u32 s[50:51], v93, v94\nv_cndmask_b32 v12, v12, v64, s[44:45]\n"
"v_cndmask_b32 v13, v13, v65, s[44:45]\nv_cndmask_b32 v14, v14, v68, s[44:45]\nv_cndmask_b32 v15, v15, v69, s[44:45]\nv_cndmask_b32 v12, v12, v72, s[46:47]\n"
"v_cndmask_b32 v13, v13, v73, s[46:47]\nv_cndmask_b32 v14, v14, v76, s[46:47]\nv_cndmask_b32 v15, v15, v77, s[46:47]\nv_cndmask_b32 v16, v16, v64, s[48:49]\n"
"v_cndmask_b32 v17, v17, v65, s[48:49]\nv_cndmask_b32 v18, v18, v68, s[48:49]\nv_cndmask_b32 v19, v19, v69, s[48:49]\nv_cndmask_b32 v16, v16, v72, s[50:51]\n"
"v_cndmask_b32 v17, v17, v73, s[50:51]\nv_cndmask_b32 v18, v18, v76, s[50:51]\nv_cndmask_b32 v19, v19, v77, s[50:51]\nv_sub_f32 v92, 1.0, v80\nv_sub_f32 v93, 1.0, v82\nCV 92,92,93\n"
"v_and_b32 v93, 0xffff0000, v92\nv_lshlrev_b32 v92, 16, v92\nv_lshlrev_b32 v64, 16, v12\nv_and_b32 v65, 0xffff0000, v12\nMU 64,64,92\nMU 65,65,92\nv_lshlrev_b32 v66, 16, v13\n"
"v_and_b32 v67, 0xffff0000, v13\nMU 66,66,92\nMU 67,67,92\nv_lshlrev_b32 v68, 16, v14\nv_and_b32 v69, 0xffff0000, v14\nMU 68,68,92\nMU 69,69,92\nv_lshlrev_b32 v70, 16, v15\n"
"v_and_b32 v71, 0xffff0000, v15\nMU 70,70,92\nMU 71,71,92\nv_lshlrev_b32 v72, 16, v16\nv_and_b32 v73, 0xffff0000, v16\nMU 72,72,93\nMU 73,73,93\nv_lshlrev_b32 v74, 16, v17\n"
"v_and_b32 v75, 0xffff0000, v17\nMU 74,74,93\nMU 75,75,93\nv_lshlrev_b32 v76, 16, v18\nv_and_b32 v77, 0xffff0000, v18\nMU 76,76,93\nMU 77,77,93\nv_lshlrev_b32 v78, 16, v19\n"
"v_and_b32 v79, 0xffff0000, v19\nMU 78,78,93\nMU 79,79,93\ns_mov_b32 s7, 262144\nWV 0\n.Z522:\nv_max_u32 v84, v99, v103\nv_cmp_eq_u32 s[44:45], -1, v84\nv_max_u32 v84, v107, v111\n"
"v_cmp_eq_u32 s[46:47], -1, v84\nv_max_u32 v84, v115, v119\nv_cmp_eq_u32 s[48:49], -1, v84\nv_max_u32 v84, v123, v127\nv_cmp_eq_u32 s[50:51], -1, v84\nv_max_u32 v84, v35, v39\n"
"v_cmp_eq_u32 s[52:53], -1, v84\nv_max_u32 v84, v43, v47\nv_cmp_eq_u32 s[54:55], -1, v84\nv_max_u32 v84, v51, v55\nv_cmp_eq_u32 s[56:57], -1, v84\nv_max_u32 v84, v59, v63\n"
"v_cmp_eq_u32 s[58:59], -1, v84\ns_or_b64 s[0:1], s[44:45], s[46:47]\ns_or_b64 s[2:3], s[48:49], s[50:51]\ns_or_b64 s[0:1], s[0:1], s[52:53]\ns_or_b64 s[2:3], s[2:3], s[54:55]\n"
"s_or_b64 s[0:1], s[0:1], s[56:57]\ns_or_b64 s[2:3], s[2:3], s[58:59]\ns_or_b64 s[0:1], s[0:1], s[2:3]\ns_cbranch_scc1 .Z520\n.Z521:\nZvvv 136,6,96\nMvvv 136,6,104\nMvvv 136,6,112\n"
"Mvvv 136,6,120\nMvvv 136,6,32\nMvvv 136,6,40\nMvvv 136,6,48\nMvvv 136,6,56\nZvvv 140,4,96\nMvvv 140,4,104\nMvvv 140,4,112\nMvvv 140,4,120\nMvvv 140,4,32\nMvvv 140,4,40\n"
"Mvvv 140,4,48\nMvvv 140,4,56\nZvvv 144,6,100\nMvvv 144,6,108\nMvvv 144,6,116\nMvvv 144,6,124\nMvvv 144,6,36\nMvvv 144,6,44\nMvvv 144,6,52\nMvvv 144,6,60\nZvvv 148,4,100\n"
"Mvvv 148,4,108\nMvvv 148,4,116\nMvvv 148,4,124\nMvvv 148,4,36\nMvvv 148,4,44\nMvvv 148,4,52\nMvvv 148,4,60\nFM 64,80,136\nFM 65,80,137\nFM 66,80,138\nFM 67,80,139\nFM 68,80,140\n"
"FM 69,80,141\nFM 70,80,142\nFM 71,80,143\nCV 12,64,65\nCV 13,66,67\nCV 14,68,69\nCV 15,70,71\nSv 12,91,36,22,0\nFM 72,82,144\nFM 73,82,145\nFM 74,82,146\nFM 75,82,147\n"
"FM 76,82,148\nFM 77,82,149\nFM 78,82,150\nFM 79,82,151\nCV 16,72,73\nCV 17,74,75\nCV 18,76,77\nCV 19,78,79\nSv 16,91,36,23,0\n.Z519:\ns_endpgm\n.S406:\ns_mov_b32 s7, 262144\n"
".S408:\ns_lshl_b32 s5, s73, 8\ns_add_u32 s5, s5, 0xc04ac00\nv_lshrrev_b32 v27, 2, v1\nbuffer_load_dword v27, v27, s[16:19], s5 offen sc0 sc1\nWV 0\nv_xor_b32 v27, s15, v27\n"
"v_and_b32 v32, 0xffffdfff, v27\nv_cmp_ne_u32 vcc, 0, v32\ns_nop 0\ns_cbranch_vccz .S407\ns_sleep 6\ns_sub_u32 s7, s7, 1\ns_cbranch_scc0 .S408\ns_branch .S407\n.T414:\n"
"s_mov_b32 s62, 1\ns_call_b64 s[80:81], .RA262\ns_branch .T415\n.T416:\ns_mov_b32 s62, 1\ns_call_b64 s[80:81], .RA262\ns_branch .T417\n.S419:\ns_mov_b32 s7, 262144\n.S421:\n"
"buffer_load_dword v27, off, s[16:19], s5 sc0 sc1\nWV 0\nv_cmp_ne_u32 vcc, s15, v27\ns_nop 0\ns_cbranch_vccz .S420\ns_sleep 6\ns_sub_u32 s7, s7, 1\ns_cbranch_scc0 .S421\n"
"s_branch .S420\n.S422:\ns_mov_b32 s7, 262144\n.S424:\nv_and_b32 v27, 0x70, v1\nbuffer_load_dword v27, v27, s[16:19], s61 offen offset:128 sc0 sc1\nWV 0\n"
"v_cmp_ne_u32 vcc, s15, v27\ns_nop 0\ns_cbranch_vccz .S423\ns_sleep 6\ns_sub_u32 s7, s7, 1\ns_cbranch_scc0 .S424\ns_branch .S423\n.S426:\ns_mov_b32 s7, 262144\n.S428:\n"
"v_and_b32 v27, 0x70, v1\nbuffer_load_dword v27, v27, s[16:19], s61 offen offset:136 sc0 sc1\nWV 0\nv_cmp_ne_u32 vcc, s15, v27\ns_nop 0\ns_cbranch_vccz .S427\ns_sleep 6\n"
"s_sub_u32 s7, s7, 1\ns_cbranch_scc0 .S428\ns_branch .S427\n.S429:\ns_mov_b32 s7, 262144\n.S431:\nv_and_b32 v27, 0x70, v1\n"
"buffer_load_dword v27, v27, s[16:19], s61 offen offset:140 sc0 sc1\nWV 0\nv_cmp_ne_u32 vcc, s15, v27\ns_nop 0\ns_cbranch_vccz .S430\ns_sleep 6\ns_sub_u32 s7, s7, 1\n"
"s_cbranch_scc0 .S431\ns_branch .S430\n.S432:\ns_mov_b32 s7, 262144\n.S434:\nv_and_b32 v27, 0x70, v1\nbuffer_load_dword v27, v27, s[16:19], s61 offen offset:12 sc0 sc1\nWV 0\n"
"v_cmp_ne_u32 vcc, s15, v27\ns_nop 0\ns_cbranch_vccz .S433\ns_sleep 6\ns_sub_u32 s7, s7, 1\ns_cbranch_scc0 .S434\ns_branch .S433\n.S435:\ns_mov_b32 s7, 262144\n.S437:\n"
"v_and_b32 v27, 0x70, v1\nbuffer_load_dword v27, v27, s[16:19], s61 offen offset:8 sc0 sc1\nWV 0\nv_cmp_ne_u32 vcc, s15, v27\ns_nop 0\ns_cbranch_vccz .S436\ns_sleep 6\n"
"s_sub_u32 s7, s7, 1\ns_cbranch_scc0 .S437\ns_branch .S436\n.S438:\ns_mov_b32 s7, 262144\n.S440:\nv_and_b32 v27, 0xf0, v1\n"
"buffer_load_dword v27, v27, s[16:19], s61 offen offset:4 sc0 sc1\nWV 0\nv_cmp_ne_u32 vcc, s15, v27\ns_nop 0\ns_cbranch_vccz .S439\ns_sleep 6\ns_sub_u32 s7, s7, 1\n"
"s_cbranch_scc0 .S440\ns_branch .S439\n.T449:\ns_mov_b32 s62, 0\ns_call_b64 s[80:81], .RB263\ns_branch .T450\n.T451:\ns_mov_b32 s62, 0\ns_call_b64 s[80:81], .RA262\ns_branch .T452\n"
".T455:\ns_mov_b32 s62, 0\ns_call_b64 s[80:81], .RB263\ns_branch .T456\n.T457:\ns_mov_b32 s62, 0\ns_call_b64 s[80:81], .RA262\ns_branch .T458\n.T461:\ns_mov_b32 s62, 2\n"
"s_call_b64 s[80:81], .RB263\ns_branch .T462\n.T463:\ns_mov_b32 s62, 5\ns_call_b64 s[80:81], .RA262\ns_branch .T464\n.T465:\ns_mov_b32 s62, 5\ns_call_b64 s[80:81], .RA262\n"
"s_branch .T466\n.T477:\ns_mov_b32 s62, 4\ns_call_b64 s[80:81], .RB263\ns_branch .T478\n.T479:\ns_mov_b32 s62, 4\ns_call_b64 s[80:81], .RA262\ns_branch .T480\n.T483:\n"
"s_mov_b32 s62, 4\ns_call_b64 s[80:81], .RB263\ns_branch .T484\n.T485:\ns_mov_b32 s62, 4\ns_call_b64 s[80:81], .RA262\ns_branch .T486\n.T487:\ns_mov_b32 s62, 6\n"
"s_call_b64 s[80:81], .RB263\ns_branch .T488\n.S489:\ns_mov_b32 s7, 262144\n.S491:\ns_add_u32 s0, s92, 0\nbuffer_load_dwordx4 v[208:211], v1, s[16:19], s0 offen offset:0 sc0 sc1\n"
"buffer_load_dwordx4 v[212:215], v1, s[16:19], s0 offen offset:1024 sc0 sc1\nWV 0\nv_max_u32 v21, v211, v215\nv_cmp_eq_u32 vcc, -1, v21\ns_nop 0\ns_cbranch_vccz .S490\ns_sleep 6\n"
"s_sub_u32 s7, s7, 1\ns_cbranch_scc0 .S491\ns_branch .S490\n.S492:\ns_mov_b32 s7, 262144\n.S494:\ns_add_u32 s0, s92, 131072\n"
"buffer_load_dwordx4 v[216:219], v1, s[16:19], s0 offen offset:0 sc0 sc1\nbuffer_load_dwordx4 v[220:223], v1, s[16:19], s0 offen offset:1024 sc0 sc1\nWV 0\n"
"v_max_u32 v21, v219, v223\nv_cmp_eq_u32 vcc, -1, v21\ns_nop 0\ns_cbranch_vccz .S493\ns_sleep 6\ns_sub_u32 s7, s7, 1\ns_cbranch_scc0 .S494\ns_branch .S493\n.S495:\n"
"s_mov_b32 s7, 262144\n.S497:\ns_add_u32 s0, s92, 262144\nbuffer_load_dwordx4 v[224:227], v1, s[16:19], s0 offen offset:0 sc0 sc1\n"
"buffer_load_dwordx4 v[228:231], v1, s[16:19], s0 offen offset:1024 sc0 sc1\nWV 0\nv_max_u32 v21, v227, v231\nv_cmp_eq_u32 vcc, -1, v21\ns_nop 0\ns_cbranch_vccz .S496\ns_sleep 6\n"
"s_sub_u32 s7, s7, 1\ns_cbranch_scc0 .S497\ns_branch .S496\n.S498:\ns_mov_b32 s7, 262144\n.S500:\ns_add_u32 s0, s92, 393216\n"
"buffer_load_dwordx4 v[208:211], v1, s[16:19], s0 offen offset:0 sc0 sc1\nbuffer_load_dwordx4 v[212:215], v1, s[16:19], s0 offen offset:1024 sc0 sc1\nWV 0\n"
"v_max_u32 v21, v211, v215\nv_cmp_eq_u32 vcc, -1, v21\ns_nop 0\ns_cbranch_vccz .S499\ns_sleep 6\ns_sub_u32 s7, s7, 1\ns_cbranch_scc0 .S500\ns_branch .S499\n.S501:\n"
"s_mov_b32 s7, 262144\n.S503:\ns_add_u32 s0, s92, 524288\nbuffer_load_dwordx4 v[216:219], v1, s[16:19], s0 offen offset:0 sc0 sc1\n"
"buffer_load_dwordx4 v[220:223], v1, s[16:19], s0 offen offset:1024 sc0 sc1\nWV 0\nv_max_u32 v21, v219, v223\nv_cmp_eq_u32 vcc, -1, v21\ns_nop 0\ns_cbranch_vccz .S502\ns_sleep 6\n"
"s_sub_u32 s7, s7, 1\ns_cbranch_scc0 .S503\ns_branch .S502\n.S504:\ns_mov_b32 s7, 262144\n.S506:\ns_add_u32 s0, s92, 655360\n"
"buffer_load_dwordx4 v[224:227], v1, s[16:19], s0 offen offset:0 sc0 sc1\nbuffer_load_dwordx4 v[228:231], v1, s[16:19], s0 offen offset:1024 sc0 sc1\nWV 0\n"
"v_max_u32 v21, v227, v231\nv_cmp_eq_u32 vcc, -1, v21\ns_nop 0\ns_cbranch_vccz .S505\ns_sleep 6\ns_sub_u32 s7, s7, 1\ns_cbranch_scc0 .S506\ns_branch .S505\n.S507:\n"
"s_mov_b32 s7, 262144\n.S509:\ns_add_u32 s0, s92, 786432\nbuffer_load_dwordx4 v[208:211], v1, s[16:19], s0 offen offset:0 sc0 sc1\n"
"buffer_load_dwordx4 v[212:215], v1, s[16:19], s0 offen offset:1024 sc0 sc1\nWV 0\nv_max_u32 v21, v211, v215\nv_cmp_eq_u32 vcc, -1, v21\ns_nop 0\ns_cbranch_vccz .S508\ns_sleep 6\n"
"s_sub_u32 s7, s7, 1\ns_cbranch_scc0 .S509\ns_branch .S508\n.S510:\ns_mov_b32 s7, 262144\n.S512:\nbuffer_load_dwordx4 v[64:67], v89, s[16:19], s40 offen offset:0 sc0 sc1\n"
"buffer_load_dwordx4 v[68:71], v89, s[16:19], s40 offen offset:16 sc0 sc1\nbuffer_load_dwordx4 v[72:75], v89, s[16:19], s40 offen offset:1792 sc0 sc1\n"
"buffer_load_dwordx4 v[76:79], v89, s[16:19], s40 offen offset:1808 sc0 sc1\nbuffer_load_dwordx2 v[80:81], v88, s[16:19], s63 offen offset:0 sc0 sc1\n"
"buffer_load_dwordx2 v[82:83], v88, s[16:19], s63 offen offset:512 sc0 sc1\nWV 0\nv_xor_b32 v66, s15, v66\nv_xor_b32 v70, s15, v70\nv_xor_b32 v74, s15, v74\n"
"v_xor_b32 v78, s15, v78\nv_xor_b32 v81, s15, v81\nv_xor_b32 v83, s15, v83\nv_or3_b32 v92, v66, v70, v74\nv_or3_b32 v92, v92, v78, v81\nv_or_b32 v92, v92, v83\n"
"v_cmp_ne_u32 vcc, 0, v92\ns_nop 0\ns_cbranch_vccz .S511\ns_sleep 6\ns_sub_u32 s7, s7, 1\ns_cbranch_scc0 .S512\ns_branch .S511\n.S513:\ns_mov_b32 s7, 262144\n.S515:\n"
"buffer_load_dword v27, off, s[16:19], s60 offset:512 sc0 sc1\nWV 0\nv_cmp_ne_u32 vcc, s15, v27\ns_nop 0\ns_cbranch_vccz .S514\ns_sleep 6\ns_sub_u32 s7, s7, 1\n"
"s_cbranch_scc0 .S515\ns_branch .S514\n.S516:\ns_mov_b32 s7, 262144\n.S518:\nbuffer_load_dword v27, v24, s[16:19], s41 offen sc0 sc1\nWV 0\nv_xor_b32 v27, s15, v27\n"
"v_and_b32 v26, 0xffffdfff, v27\nv_cmp_ne_u32 s[64:65], 0, v27\nv_cmp_ne_u32 vcc, 0, v26\ns_nop 0\ns_cbranch_vccz .S517\ns_sleep 6\ns_sub_u32 s7, s7, 1\ns_cbranch_scc0 .S518\n"
"s_branch .S517\n.Z520:\ns_sub_u32 s7, s7, 1\ns_cbranch_scc1 .Z521\ns_cmp_lg_u64 s[44:45], 0\ns_cbranch_scc0 .Z523\n"
"buffer_load_dwordx4 v[96:99], v1, s[16:19], s24 offen offset:0 sc0 sc1\nbuffer_load_dwordx4 v[100:103], v1, s[16:19], s24 offen offset:1024 sc0 sc1\n.Z523:\n"
"s_cmp_lg_u64 s[46:47], 0\ns_cbranch_scc0 .Z524\nbuffer_load_dwordx4 v[104:107], v1, s[16:19], s25 offen offset:0 sc0 sc1\n"
"buffer_load_dwordx4 v[108:111], v1, s[16:19], s25 offen offset:1024 sc0 sc1\n.Z524:\ns_cmp_lg_u64 s[48:49], 0\ns_cbranch_scc0 .Z525\n"
"buffer_load_dwordx4 v[112:115], v1, s[16:19], s26 offen offset:0 sc0 sc1\nbuffer_load_dwordx4 v[116:119], v1, s[16:19], s26 offen offset:1024 sc0 sc1\n.Z525:\n"
"s_cmp_lg_u64 s[50:51], 0\ns_cbranch_scc0 .Z526\nbuffer_load_dwordx4 v[120:123], v1, s[16:19], s27 offen offset:0 sc0 sc1\n"
"buffer_load_dwordx4 v[124:127], v1, s[16:19], s27 offen offset:1024 sc0 sc1\n.Z526:\ns_cmp_lg_u64 s[52:53], 0\ns_cbranch_scc0 .Z527\n"
"buffer_load_dwordx4 v[32:35], v1, s[16:19], s32 offen offset:0 sc0 sc1\nbuffer_load_dwordx4 v[36:39], v1, s[16:19], s32 offen offset:1024 sc0 sc1\n.Z527:\n"
"s_cmp_lg_u64 s[54:55], 0\ns_cbranch_scc0 .Z528\nbuffer_load_dwordx4 v[40:43], v1, s[16:19], s33 offen offset:0 sc0 sc1\n"
"buffer_load_dwordx4 v[44:47], v1, s[16:19], s33 offen offset:1024 sc0 sc1\n.Z528:\ns_cmp_lg_u64 s[56:57], 0\ns_cbranch_scc0 .Z529\n"
"buffer_load_dwordx4 v[48:51], v1, s[16:19], s34 offen offset:0 sc0 sc1\nbuffer_load_dwordx4 v[52:55], v1, s[16:19], s34 offen offset:1024 sc0 sc1\n.Z529:\n"
"s_cmp_lg_u64 s[58:59], 0\ns_cbranch_scc0 .Z530\nbuffer_load_dwordx4 v[56:59], v1, s[16:19], s35 offen offset:0 sc0 sc1\n"
"buffer_load_dwordx4 v[60:63], v1, s[16:19], s35 offen offset:1024 sc0 sc1\n.Z530:\ns_sleep 8\nWV 0\ns_branch .Z522\n.Lcode1:\n.fill 2304, 4, 0xbf810000\n"
  :: "s"(__builtin_amdgcn_kernarg_segment_ptr()), "s"(blockIdx.x), "v"(threadIdx.x)
  : "memory", "scc", "vcc", "v1", "v2", "v3", "v4", "v5", "v6", "v7", "v8", "v9", "v10", "v11", "v12", "v13", "v14", "v15", "v16", "v17", "v18", "v19", "v20", "v21", "v22", "v23", "v24", "v25", "v26", "v27", "v28", "v29", "v30", "v31", "v32", "v33", "v34", "v35", "v36", "v37", "v38", "v39", "v40", "v41", "v42", "v43", "v44", "v45", "v46", "v47", "v48", "v49", "v50", "v51", "v52", "v53", "v54", "v55", "v56", "v57", "v58", "v59", "v60", "v61", "v62", "v63", "v64", "v65", "v66", "v67", "v68", "v69", "v70", "v71", "v72", "v73", "v74", "v75", "v76", "v77", "v78", "v79", "v80", "v81", "v82", "v83", "v84", "v85", "v86", "v87", "v88", "v89", "v90", "v91", "v92", "v93", "v94", "v95", "v96", "v97", "v98", "v99", "v100", "v101", "v102", "v103", "v104", "v105", "v106", "v107", "v108", "v109", "v110", "v111", "v112", "v113", "v114", "v115", "v116", "v117", "v118", "v119", "v120", "v121", "v122", "v123", "v124", "v125", "v126", "v127", "v128", "v129", "v130", "v131", "v132", "v133", "v134", "v135", "v136", "v137", "v138", "v139", "v140", "v141", "v142", "v143", "v144", "v145", "v146", "v147", "v148", "v149", "v150", "v151", "v152", "v153", "v154", "v155", "v156", "v157", "v158", "v159", "v160", "v161", "v162", "v163", "v164", "v165", "v166", "v167", "v168", "v169", "v170", "v171", "v172", "v173", "v174", "v175", "v176", "v177", "v178", "v179", "v180", "v181", "v182", "v183", "v184", "v185", "v186", "v187", "v188", "v189", "v190", "v191", "v192", "v193", "v194", "v195", "v196", "v197", "v198", "v199", "v200", "v201", "v202", "v203", "v204", "v205", "v206", "v207", "v208", "v209", "v210", "v211", "v212", "v213", "v214", "v215", "v216", "v217", "v218", "v219", "v220", "v221", "v222", "v223", "v224", "v225", "v226", "v227", "v228", "v229", "v230", "v231", "v232", "v233", "v234", "v235", "v236", "v237", "v238", "v239", "v240", "v241", "v242", "v243", "v244", "v245", "v246", "v247", "v248", "v249", "v250", "v251", "v252", "v253", "v254", "v255", "a0", "a1", "a2", "a3", "a4", "a5", "a6", "a7", "a8", "a9", "a10", "a11", "a12", "a13", "a14", "a15", "a16", "a17", "a18", "a19", "a20", "a21", "a22", "a23", "a24", "a25", "a26", "a27", "a28", "a29", "a30", "a31", "a32", "a33", "a34", "a35", "a36", "a37", "a38", "a39", "a40", "a41", "a42", "a43", "a44", "a45", "a46", "a47", "a48", "a49", "a50", "a51", "a52", "a53", "a54", "a55", "a56", "a57", "a58", "a59", "a60", "a61", "a62", "a63", "a64", "a65", "a66", "a67", "a68", "a69", "a70", "a71", "a72", "a73", "a74", "a75", "a76", "a77", "a78", "a79", "a80", "a81", "a82", "a83", "a84", "a85", "a86", "a87", "a88", "a89", "a90", "a91", "a92", "a93", "a94", "a95", "a96", "a97", "a98", "a99", "a100", "a101", "a102", "a103", "a104", "a105", "a106", "a107", "a108", "a109", "a110", "a111", "a112", "a113", "a114", "a115", "a116", "a117", "a118", "a119", "a120", "a121", "a122", "a123", "a124", "a125", "a126", "a127", "a128", "a129", "a130", "a131", "a132", "a133", "a134", "a135", "a136", "a137", "a138", "a139", "a140", "a141", "a142", "a143", "a144", "a145", "a146", "a147", "a148", "a149", "a150", "a151", "a152", "a153", "a154", "a155", "a156", "a157", "a158", "a159", "a160", "a161", "a162", "a163", "a164", "a165", "a166", "a167", "a168", "a169", "a170", "a171", "a172", "a173", "a174", "a175", "a176", "a177", "a178", "a179", "a180", "a181", "a182", "a183", "a184", "a185", "a186", "a187", "a188", "a189", "a190", "a191", "a192", "a193", "a194", "a195", "a196", "a197", "a198", "a199", "a200", "a201", "a202", "a203", "a204", "a205", "a206", "a207", "a208", "a209", "a210", "a211", "a212", "a213", "a214", "a215", "a216", "a217", "a218", "a219", "a220", "a221", "a222", "a223", "a224", "a225", "a226", "a227", "a228", "a229", "a230", "a231", "a232", "a233", "a234", "a235", "a236", "a237", "a238", "a239", "a240", "a241", "a242", "a243", "a244", "a245", "a246", "a247", "a248", "a249", "a250", "a251", "a252", "a253", "a254", "a255", "s8", "s9", "s10", "s11", "s12", "s13", "s14", "s15", "s16", "s17", "s18", "s19", "s20", "s21", "s22", "s23", "s24", "s25", "s26", "s27", "s28", "s29", "s30", "s31", "s33", "s34", "s35", "s36", "s37", "s38", "s39", "s40", "s41", "s42", "s43", "s44", "s45", "s46", "s47", "s48", "s49", "s50", "s51", "s52", "s53", "s54", "s55", "s56", "s57", "s58", "s59", "s60", "s61", "s62", "s63", "s64", "s65", "s66", "s67", "s68", "s69", "s70", "s71", "s72", "s73", "s74", "s75", "s76", "s77", "s78", "s79", "s80", "s81", "s82", "s83", "s84", "s85", "s86", "s87", "s88", "s89", "s90", "s91", "s92", "s93", "s94", "s95", "s96", "s97", "s98", "s99");
#endif
}

extern "C" void kernel_launch(void* const* d_in, const int* in_sizes, int n_in,
                              void* d_out, int out_size, void* d_ws, size_t ws_size,
                              hipStream_t stream) {
  (void)hipFuncSetAttribute((const void*)k_main, hipFuncAttributeMaxDynamicSharedMemorySize, 163840);
  k_main<<<256, 256, 163840, stream>>>(d_in[0], d_in[1], d_in[2], d_in[3], d_in[4], d_ws, d_in[11], d_in[7], d_in[5], d_in[8], d_in[9], d_in[10], d_out);
}
```

```cpp
#include <hip/hip_runtime.h>
#include <stdint.h>

extern "C" __global__ __launch_bounds__(256, 1)
void k_main(const void* x, const void* ckv, const void* wq, const void* wuk, const void* wuv, void* ws,
            const void* gate_w, const void* w_r, const void* wo, const void* wg, const void* wu, const void* wd, void* out) {
#if defined(__HIP_DEVICE_COMPILE__)
  asm volatile(
"s_mov_b64 s[8:9], %0\ns_mov_b32 s10, %1\nv_mov_b32 v0, %2\n.macro WL n\ns_waitcnt lgkmcnt(\\n)\n.endm\n.macro D0 x,r,s,o\n"
"buffer_load_dwordx4 v\\x, s[\\r:\\r+3], s\\s offen offset:\\o lds\n.endm\n.macro WV n\ns_waitcnt vmcnt(\\n)\n.endm\n.macro Ra d,x,o\nds_read_b128 a[\\d:\\d+3], v\\x offset:\\o\n"
".endm\n.macro Ta d,x,o\nds_read_b64_tr_b16 a[\\d:\\d+1], v\\x offset:\\o\n.endm\n.macro Zvaa d,x,y\nv_mfma_f32_16x16x32_bf16 v[\\d:\\d+3], a[\\x:\\x+3], a[\\y:\\y+3], 0\n.endm\n"
".macro Mvaa d,x,y\nv_mfma_f32_16x16x32_bf16 v[\\d:\\d+3], a[\\x:\\x+3], a[\\y:\\y+3], v[\\d:\\d+3]\n.endm\n.macro Zaaa d,x,y\n"
"v_mfma_f32_16x16x32_bf16 a[\\d:\\d+3], a[\\x:\\x+3], a[\\y:\\y+3], 0\n.endm\n.macro Maaa d,x,y\nv_mfma_f32_16x16x32_bf16 a[\\d:\\d+3], a[\\x:\\x+3], a[\\y:\\y+3], a[\\d:\\d+3]\n"
".endm\n.macro MU d,x,y\nv_mul_f32 v\\d, v\\x, v\\y\n.endm\n.macro CV d,x,y\nv_cvt_pk_bf16_f32 v\\d, v\\x, v\\y\n.endm\n.macro FM d,x,y\nv_fmac_f32 v\\d, v\\x, v\\y\n.endm\n"
".macro AD d,x,y\nv_add_f32 v\\d, v\\x, v\\y\n.endm\n.macro EX d,x\nv_exp_f32 v\\d, v\\x\n.endm\n.macro AR d,x\nv_accvgpr_read_b32 v\\d, a\\x\n.endm\n.macro Sv d,x,r,s,o\n"
"buffer_store_dwordx4 v[\\d:\\d+3], v\\x, s[\\r:\\r+3], s\\s offen offset:\\o sc0 sc1\n.endm\n.macro Rv d,x,o\nds_read_b128 v[\\d:\\d+3], v\\x offset:\\o\n.endm\n.macro D1 x,r,s,o\n"
"buffer_load_dwordx4 v\\x, s[\\r:\\r+3], s\\s offen offset:\\o sc1 lds\n.endm\n.macro Cvav d,x,y,c\nv_mfma_f32_16x16x32_bf16 v[\\d:\\d+3], a[\\x:\\x+3], v[\\y:\\y+3], v[\\c:\\c+3]\n"
".endm\n.macro Mvav d,x,y\nv_mfma_f32_16x16x32_bf16 v[\\d:\\d+3], a[\\x:\\x+3], v[\\y:\\y+3], v[\\d:\\d+3]\n.endm\n.macro Zaav d,x,y\n"
"v_mfma_f32_16x16x32_bf16 a[\\d:\\d+3], a[\\x:\\x+3], v[\\y:\\y+3], 0\n.endm\n.macro Maav d,x,y\nv_mfma_f32_16x16x32_bf16 a[\\d:\\d+3], a[\\x:\\x+3], v[\\y:\\y+3], a[\\d:\\d+3]\n"
".endm\n.macro SU d,x,y\nv_sub_f32 v\\d, v\\x, v\\y\n.endm\n.macro Mava d,x,y\nv_mfma_f32_16x16x32_bf16 a[\\d:\\d+3], v[\\x:\\x+3], a[\\y:\\y+3], a[\\d:\\d+3]\n.endm\n"
".macro Zvav d,x,y\nv_mfma_f32_16x16x32_bf16 v[\\d:\\d+3], a[\\x:\\x+3], v[\\y:\\y+3], 0\n.endm\n.macro Zvvv d,x,y\n"
"v_mfma_f32_16x16x32_bf16 v[\\d:\\d+3], v[\\x:\\x+3], v[\\y:\\y+3], 0\n.endm\n.macro Mvvv d,x,y\nv_mfma_f32_16x16x32_bf16 v[\\d:\\d+3], v[\\x:\\x+3], v[\\y:\\y+3], v[\\d:\\d+3]\n"
".endm\n.macro AW d,x\nv_accvgpr_write_b32 a\\d, v\\x\n.endm\n.macro Tv d,x,o\nds_read_b64_tr_b16 v[\\d:\\d+1], v\\x offset:\\o\n.endm\n.macro Sa d,x,r,s,o\n"
"buffer_store_dwordx4 a[\\d:\\d+3], v\\x, s[\\r:\\r+3], s\\s offen offset:\\o sc0 sc1\n.endm\n.macro Cvaa d,x,y,c\n"
"v_mfma_f32_16x16x32_bf16 v[\\d:\\d+3], a[\\x:\\x+3], a[\\y:\\y+3], v[\\c:\\c+3]\n.endm\n.Lcode0:\ns_mov_b32 s70, 0\ns_load_dwordx16 s[44:59], s[8:9], 0x0\n"
"s_load_dwordx8 s[60:67], s[8:9], 0x40\ns_load_dwordx2 s[68:69], s[8:9], 0x60\nv_lshrrev_b32 v2, 6, v0\nv_and_b32 v0, 63, v0\nv_readfirstlane_b32 s11, v2\nv_lshlrev_b32 v1, 4, v0\n"
"s_and_b32 s2, s10, 0x17\ns_bfe_u32 s3, s10, 0x10003\ns_lshl_b32 s3, s3, 7\ns_or_b32 s2, s2, s3\ns_bfe_u32 s3, s10, 0x10005\ns_lshl3_add_u32 s2, s3, s2\ns_lshr_b32 s3, s10, 6\n"
"s_lshl_b32 s3, s3, 5\ns_or_b32 s10, s2, s3\ns_and_b32 s73, s10, 7\ns_lshr_b32 s74, s10, 3\ns_and_b32 s2, s74, 1\ns_lshl1_add_u32 s12, s73, s2\ns_bfe_u32 s13, s74, 0x30001\n"
"s_lshr_b32 s14, s74, 4\ns_xor_b32 s14, s14, 1\nv_lshrrev_b32 v32, 5, v0\ns_and_b32 s3, s11, 1\ns_lshl_b32 s2, s3, 3\nv_lshl_or_b32 v33, v32, 1, s2\nv_and_b32 v34, 31, v0\n"
"v_xor_b32 v33, v33, v34\nv_lshlrev_b32 v33, 4, v33\nv_lshl_or_b32 v6, v32, 13, v33\nv_xor_b32 v33, 64, v6\nv_add_u32 v7, 15360, v33\nv_add_u32 v8, 30720, v6\n"
"v_add_u32 v9, 46080, v33\ns_lshl_b32 s2, s11, 16\ns_lshl_b32 s4, s12, 9\ns_add_u32 s5, s2, s4\ns_getpc_b64 s[40:41]\n.PC8:\ns_sub_u32 s40, s40, .PC8-.Lcode0\n"
"s_subb_u32 s41, s41, 0\ns_mov_b32 s42, .Lcode1-.Lcode0\ns_mov_b32 s43, 0x20000\ns_lshl2_add_u32 s6, s74, s11\ns_lshl_b32 s6, s6, 7\nv_lshlrev_b32 v54, 10, v1\n"
"buffer_load_dword v31, v54, s[40:43], s6 offen\nv_lshrrev_b32 v32, 4, v0\nv_and_b32 v33, 7, v0\nv_xor_b32 v33, v33, v32\nv_lshlrev_b32 v33, 4, v33\nv_lshrrev_b32 v32, 3, v0\n"
"v_lshl_or_b32 v2, v32, 11, v33\nv_xor_b32 v33, 64, v2\nv_add_u32 v3, 15360, v33\nv_add_u32 v4, 30720, v2\nv_add_u32 v5, 46080, v33\nv_lshrrev_b32 v144, 4, v0\n"
"v_and_b32 v145, 7, v0\nv_xor_b32 v145, v145, v144\nv_lshlrev_b32 v145, 4, v145\nv_lshrrev_b32 v144, 3, v0\nv_mul_u32_u24 v144, 896, v144\nv_add_u32 v136, v144, v145\n"
"v_xor_b32 v145, 64, v136\nv_add_u32 v137, 6144, v145\nv_add_u32 v138, 55296, v136\nv_add_u32 v139, 61440, v145\ns_lshr_b32 s88, s11, 1\ns_lshr_b32 s2, s13, 2\n"
"s_bfe_u32 s3, s13, 0x10001\ns_mul_i32 s3, s3, 17\ns_add_u32 s2, s2, s3\ns_lshl2_add_u32 s3, s14, s88\ns_lshl_b32 s3, s3, 2\ns_lshr_b32 s3, 0xe420ca86, s3\ns_and_b32 s3, s3, 15\n"
"s_add_u32 s2, s2, s3\ns_cmp_eq_u32 s2, 32\ns_cselect_b32 s86, 16, s2\ns_add_u32 s88, s88, 2\ns_lshr_b32 s2, s13, 2\ns_bfe_u32 s3, s13, 0x10001\ns_mul_i32 s3, s3, 17\n"
"s_add_u32 s2, s2, s3\ns_lshl2_add_u32 s3, s14, s88\ns_lshl_b32 s3, s3, 2\ns_lshr_b32 s3, 0xe420ca86, s3\ns_and_b32 s3, s3, 15\ns_add_u32 s2, s2, s3\ns_cmp_eq_u32 s2, 32\n"
"s_cselect_b32 s88, 16, s2\ns_and_b32 s89, s11, 1\ns_lshl1_add_u32 s86, s86, s89\ns_lshl1_add_u32 s88, s88, s89\ns_sub_u32 s2, 15, s13\ns_lshl3_add_u32 s2, s2, s11\n"
"s_mul_i32 s87, s2, 14336\ns_mov_b32 s18, 0xc0ae800\ns_mov_b32 s30, 0x1c0000\ns_mov_b32 s34, 0x380000\ns_mov_b32 s22, 0x400000\ns_mov_b32 s26, 0x800000\ns_mov_b32 s19, 0x20000\n"
"s_mov_b32 s31, 0x20000\ns_mov_b32 s35, 0x20000\ns_mov_b32 s23, 0x20000\ns_mov_b32 s27, 0x20000\nWL 0\ns_mov_b32 s28, s44\ns_and_b32 s29, s45, 0xffff\ns_mov_b32 s32, s48\n"
"s_and_b32 s33, s49, 0xffff\ns_mov_b32 s20, s46\ns_and_b32 s21, s47, 0xffff\ns_bitcmp1_b32 s13, 0\ns_cselect_b64 s[50:51], s[52:53], s[50:51]\ns_mov_b32 s24, s50\n"
"s_and_b32 s25, s51, 0xffff\ns_sub_u32 s44, s5, 0x40000\ns_mov_b32 s51, s5\ns_lshl_b32 s47, s11, 12\ns_add_u32 s47, s47, 0x10000\ns_lshl_b32 s45, s86, 16\ns_sub_u32 s45, s45, 128\n"
"s_lshl_b32 s48, s88, 16\ns_sub_u32 s48, s48, 128\ns_mov_b32 s52, s87\ns_cmp_eq_u32 s14, 1\ns_cbranch_scc1 .B6\ns_add_u32 m0, s47, 65536\ns_add_u32 s46, s51, 0\nD0 6,32,46,0\n"
"D0 7,32,46,1024\nD0 8,32,46,2048\nD0 9,32,46,3072\ns_add_u32 m0, s47, -65536\ns_add_u32 s46, s52, 0\nD0 136,28,46,0\nD0 137,28,46,1024\nD0 138,28,46,2048\nD0 139,28,46,3072\n"
"s_add_u32 m0, s47, 81920\ns_add_u32 s46, s51, 262144\nD0 6,32,46,0\nD0 7,32,46,1024\nD0 8,32,46,2048\nD0 9,32,46,3072\ns_branch .B7\n.B6:\ns_add_u32 m0, s47, 0\n"
"s_add_u32 s44, s44, 0x40000\nD0 6,24,44,0\nD0 7,24,44,1024\nD0 8,24,44,2048\nD0 9,24,44,3072\ns_add_u32 m0, s47, -65536\ns_add_u32 s45, s45, 128\nD0 2,20,45,0\nD0 3,20,45,1024\n"
"D0 4,20,45,2048\nD0 5,20,45,3072\ns_add_u32 m0, s47, 16384\ns_add_u32 s44, s44, 0x40000\nD0 6,24,44,0\nD0 7,24,44,1024\nD0 8,24,44,2048\nD0 9,24,44,3072\n.B7:\n"
"s_add_u32 s54, s54, 0x1fffff\ns_addc_u32 s55, s55, 0\ns_and_b32 s54, s54, 0xffe00000\ns_mov_b32 s16, s54\ns_and_b32 s17, s55, 0xffff\ns_mov_b32 s2, 0xc048000\n"
"s_load_dword s15, s[54:55], s2\nv_lshrrev_b32 v24, 3, v0\nv_lshlrev_b32 v24, 13, v24\nv_and_b32 v26, 7, v0\nv_lshl_or_b32 v24, v26, 7, v24\nv_lshrrev_b32 v26, 4, v0\n"
"v_lshl_or_b32 v24, v26, 28, v24\nv_lshrrev_b32 v26, 5, v0\nv_lshlrev_b32 v26, 30, v26\nv_lshl_or_b32 v26, v0, 7, v26\ns_and_b32 s2, s13, 6\ns_and_b32 s3, s12, 1\n"
"s_add_u32 s2, s2, s3\ns_lshl2_add_u32 s2, s2, s11\ns_lshl_b32 s2, s2, 14\ns_lshl_b32 s3, s73, 10\ns_add_u32 s2, s2, s3\ns_add_u32 s96, s2, 1048576\ns_and_b32 s2, s10, 127\n"
"s_lshl2_add_u32 s2, s2, s11\ns_lshl_b32 s97, s2, 12\nv_and_b32 v48, 15, v0\nv_lshrrev_b32 v49, 4, v0\nv_lshrrev_b32 v50, 1, v48\nv_xor_b32 v50, v50, v49\n"
"v_lshlrev_b32 v50, 4, v50\nv_lshl_or_b32 v50, v48, 7, v50\ns_lshr_b32 s2, s11, 1\ns_lshl_b32 s2, s2, 12\nv_add_u32 v10, s2, v50\nv_xor_b32 v11, 64, v10\nv_lshrrev_b32 v50, 2, v48\n"
"v_lshl_add_u32 v51, v49, 3, v50\nv_and_b32 v52, 1, v49\nv_lshl_or_b32 v50, v52, 2, v50\nv_lshlrev_b32 v50, 5, v50\nv_lshl_or_b32 v50, v51, 9, v50\nv_and_b32 v52, 3, v0\n"
"v_lshl_or_b32 v50, v52, 3, v50\ns_and_b32 s2, s11, 1\ns_lshl_b32 s2, s2, 8\ns_add_u32 s2, s2, 0x10000\nv_add_u32 v12, s2, v50\nv_xor_b32 v13, 32, v12\nv_xor_b32 v14, 64, v12\n"
"v_xor_b32 v15, 96, v12\nv_xor_b32 v16, 128, v12\nv_xor_b32 v17, 160, v12\nv_xor_b32 v18, 192, v12\nv_xor_b32 v19, 224, v12\ns_getpc_b64 s[0:1]\n.PC9:\ns_mov_b32 s2, .PA2-.PC9\n"
"s_mov_b32 s3, .PA4-.PC9\ns_bitcmp1_b32 s13, 0\ns_cselect_b32 s2, .PA3-.PC9, s2\ns_cselect_b32 s3, .PA5-.PC9, s3\ns_cmp_eq_u32 s14, 0\ns_cselect_b32 s2, s2, s3\n"
"s_add_u32 s0, s0, s2\ns_addc_u32 s1, s1, 0\ns_setpc_b64 s[0:1]\n.p2align 12\n.PA2:\ns_mov_b64 exec, 0\ns_cmp_eq_u32 s11, 1\ns_cbranch_scc1 .W540\ns_cmp_eq_u32 s11, 2\n"
"s_cbranch_scc1 .W541\ns_cmp_eq_u32 s11, 3\ns_cbranch_scc1 .W542\ns_branch .W539\n.WB21:\ns_mov_b64 exec, -1\nv_add_u32 v128, 0x10000, v12\nv_add_u32 v129, 0x10000, v13\n"
"v_add_u32 v130, 0x10000, v14\nv_add_u32 v131, 0x10000, v15\nv_add_u32 v132, 0x10000, v16\nv_add_u32 v133, 0x10000, v17\nv_add_u32 v134, 0x10000, v18\nv_add_u32 v135, 0x10000, v19\n"
"s_add_u32 s2, s11, 1\ns_lshr_b32 s2, s2, 1\ns_lshl_b32 s2, s2, 12\nv_add_u32 v140, s2, v10\nv_add_u32 v141, s2, v11\nv_xor_b32 v142, 0x1000, v140\nv_xor_b32 v143, 0x1000, v141\n"
"s_add_u32 m0, s47, 0\ns_add_u32 s46, s51, 524288\nD0 6,32,46,0\nD0 7,32,46,1024\nD0 8,32,46,2048\nD0 9,32,46,3072\ns_add_u32 m0, s47, -49152\ns_add_u32 s46, s52, 128\n"
"D0 136,28,46,0\nD0 137,28,46,1024\nD0 138,28,46,2048\nD0 139,28,46,3072\ns_add_u32 m0, s47, 16384\ns_add_u32 s46, s51, 786432\nD0 6,32,46,0\nD0 7,32,46,1024\nD0 8,32,46,2048\n"
"D0 9,32,46,3072\nv_mov_b32 v116, 0x40000000\nv_cmp_gt_u32 vcc, 56, v0\ns_lshl2_add_u32 s2, s10, s11\ns_mul_i32 s2, s2, 1792\nv_cndmask_b32 v117, v116, v1, vcc\ns_mov_b32 s36, s56\n"
"s_and_b32 s37, s57, 0xffff\ns_mov_b32 s38, 0x380\ns_mov_b32 s39, 0x20000\ns_mov_b32 s40, s58\ns_and_b32 s41, s59, 0xffff\ns_mov_b32 s42, 0x380\ns_mov_b32 s43, 0x20000\n"
"buffer_load_dwordx4 v[96:99], v117, s[28:31], s2 offen offset:0\nbuffer_load_dwordx4 v[100:103], v117, s[28:31], s2 offen offset:896\n"
"buffer_load_dwordx4 v[104:107], v117, s[36:39], 0 offen offset:0\nbuffer_load_dwordx4 v[108:111], v117, s[40:43], 0 offen offset:0\ns_add_u32 m0, s47, 32768\n"
"s_add_u32 s46, s51, 1048576\nD0 6,32,46,0\nD0 7,32,46,1024\nD0 8,32,46,2048\nD0 9,32,46,3072\ns_add_u32 m0, s47, -32768\ns_add_u32 s46, s52, 256\nD0 136,28,46,0\n"
"D0 137,28,46,1024\nD0 138,28,46,2048\nD0 139,28,46,3072\nWL 0\ns_mul_i32 s15, s15, 1664525\ns_add_u32 s15, s15, 1013904223\nv_mov_b32 v25, s15\ns_mov_b32 s57, 0x3db8aa3b\n"
"s_and_b32 s59, s11, 1\ns_lshl_b32 s59, s59, 13\ns_mul_i32 s2, s73, 25165824\ns_and_b32 s3, s12, 1\ns_lshl_b32 s3, s3, 21\ns_add_u32 s2, s2, s3\ns_lshl2_add_u32 s3, s13, s11\n"
"s_lshl_b32 s3, s3, 15\ns_add_u32 s2, s2, s3\ns_add_u32 s56, s2, 0x800000\ns_xor_b32 s53, s56, 0xc000\nWV 24\ns_barrier\nRa 192,140,0\nRa 196,141,0\nRa 200,140,2048\n"
"Ra 204,141,2048\nRa 208,142,0\nRa 212,143,0\nRa 216,142,2048\nRa 220,143,2048\nTa 160,128,0\nTa 162,128,2048\nTa 164,128,16384\nTa 166,128,18432\nTa 168,129,0\nTa 170,129,2048\n"
"Ta 172,129,16384\nTa 174,129,18432\nTa 176,130,0\nTa 178,130,2048\nTa 180,130,16384\nTa 182,130,18432\nTa 184,131,0\nTa 186,131,2048\nTa 188,131,16384\nTa 190,131,18432\nWL 0\n"
"Zvaa 32,160,192\nTa 224,132,0\ns_add_u32 m0, s47, 49152\ns_add_u32 s46, s51, 1310720\nMvaa 32,164,196\nTa 226,132,2048\nZaaa 96,164,204\nD0 6,32,46,0\nMaaa 96,160,200\n"
"Ta 228,132,16384\nZvaa 192,160,208\nTa 230,132,18432\nMvaa 192,164,212\nZvaa 224,164,220\nTa 232,133,0\nMvaa 224,160,216\nTa 234,133,2048\nD0 7,32,46,1024\nZvaa 228,168,216\n"
"Mvaa 228,172,220\nTa 236,133,16384\nZvaa 196,172,212\nTa 238,133,18432\nMvaa 196,168,208\nZaaa 100,168,200\nTa 240,134,0\nD0 8,32,46,2048\nMaaa 100,172,204\nTa 242,134,2048\n"
"Zvaa 36,172,196\nMvaa 36,168,192\nTa 244,134,16384\nZvaa 40,176,192\nTa 246,134,18432\nMvaa 40,180,196\nD0 9,32,46,3072\nZaaa 104,180,204\nTa 248,135,0\nMaaa 104,176,200\n"
"Ta 250,135,2048\nZvaa 200,176,208\ns_add_u32 m0, s47, -16384\ns_add_u32 s46, s52, 384\nMvaa 200,180,212\nTa 252,135,16384\nZvaa 232,180,220\nTa 254,135,18432\nD0 136,28,46,0\n"
"Mvaa 232,176,216\nZvaa 236,184,216\nMvaa 236,188,220\nZvaa 204,188,212\nMvaa 204,184,208\nD0 137,28,46,1024\nZaaa 108,184,200\nMaaa 108,188,204\nZvaa 44,188,196\nMvaa 44,184,192\n"
"s_waitcnt vmcnt(18) lgkmcnt(0)\ns_barrier\nZvaa 48,224,192\nRa 128,140,16384\nD0 138,28,46,2048\nMvaa 48,228,196\nRa 132,141,16384\nZaaa 112,228,204\nRa 136,140,18432\n"
"Maaa 112,224,200\nRa 140,141,18432\nZvaa 208,224,208\nRa 144,142,16384\nWV 15\nMvaa 208,228,212\nRa 148,143,16384\nD0 139,28,46,3072\nZvaa 240,228,220\nRa 152,142,18432\n"
"Mvaa 240,224,216\nRa 156,143,18432\nZvaa 244,232,216\nTa 160,12,0\ns_add_u32 m0, s47, 65536\ns_add_u32 s46, s51, 1572864\nMvaa 244,236,220\nTa 162,12,2048\nZvaa 212,236,212\n"
"Ta 164,12,16384\nD0 6,32,46,0\nMvaa 212,232,208\nTa 166,12,18432\nZaaa 116,232,200\nTa 168,13,0\nv_lshlrev_b32 v112, 16, v96\nMaaa 116,236,204\nTa 170,13,2048\nZvaa 52,236,196\n"
"Ta 172,13,16384\nMvaa 52,232,192\nTa 174,13,18432\nD0 7,32,46,1024\nZvaa 56,240,192\nTa 176,14,0\nv_lshlrev_b32 v113, 16, v100\nMvaa 56,244,196\nTa 178,14,2048\nZaaa 120,244,204\n"
"Ta 180,14,16384\nMaaa 120,240,200\nTa 182,14,18432\nZvaa 216,240,208\nTa 184,15,0\nD0 8,32,46,2048\nMvaa 216,244,212\nTa 186,15,2048\nZvaa 248,244,220\nTa 188,15,16384\n"
"Mvaa 248,240,216\nTa 190,15,18432\nZvaa 252,248,216\nv_lshlrev_b32 v114, 16, v104\nv_lshlrev_b32 v115, 16, v108\nMvaa 252,252,220\nD0 9,32,46,3072\nZvaa 220,252,212\n"
"MU 116,112,114\nMvaa 220,248,208\nMU 117,113,114\nZaaa 124,248,200\ns_add_u32 m0, s47, 81920\ns_add_u32 s46, s51, 1835008\nMaaa 124,252,204\nCV 116,116,117\nZvaa 60,252,196\n"
"D0 6,32,46,0\nMvaa 60,248,192\nWL 0\nMvaa 32,160,128\nTa 224,16,0\nv_lshlrev_b32 v118, 16, v116\nMvaa 32,164,132\nTa 226,16,2048\nMaaa 96,164,140\n"
"v_and_b32 v119, 0xffff0000, v116\nMaaa 96,160,136\nTa 228,16,16384\nD0 7,32,46,1024\nMvaa 192,160,144\nTa 230,16,18432\nMU 120,112,115\nMvaa 192,164,148\nMU 121,113,115\n"
"Mvaa 224,164,156\nTa 232,17,0\nMvaa 224,160,152\nTa 234,17,2048\nMvaa 228,168,152\nD0 8,32,46,2048\nv_and_b32 v112, 0xffff0000, v96\nMvaa 228,172,156\nTa 236,17,16384\n"
"Mvaa 196,172,148\nTa 238,17,18432\nMvaa 196,168,144\nv_and_b32 v113, 0xffff0000, v100\nMaaa 100,168,136\nTa 240,18,0\nv_and_b32 v114, 0xffff0000, v104\nMaaa 100,172,140\n"
"Ta 242,18,2048\nD0 9,32,46,3072\nMvaa 36,172,132\nv_and_b32 v115, 0xffff0000, v108\nMvaa 36,168,128\nTa 244,18,16384\nMvaa 40,176,128\nTa 246,18,18432\nMU 116,112,114\n"
"Mvaa 40,180,132\nMU 117,113,114\nMaaa 104,180,140\nTa 248,19,0\nMaaa 104,176,136\nTa 250,19,2048\nMvaa 200,176,144\nCV 116,116,117\nv_and_b32 v117, 0xffff0000, v116\n"
"Mvaa 200,180,148\nTa 252,19,16384\nMvaa 232,180,156\nTa 254,19,18432\nMvaa 232,176,152\nv_lshlrev_b32 v116, 16, v116\nMvaa 236,184,152\nFM 120,112,115\nFM 121,113,115\n"
"Mvaa 236,188,156\nAD 118,118,116\nMvaa 204,188,148\nAD 119,119,117\nMvaa 204,184,144\nv_lshlrev_b32 v112, 16, v97\nMaaa 108,184,136\nv_lshlrev_b32 v113, 16, v101\n"
"v_lshlrev_b32 v114, 16, v105\nMaaa 108,188,140\nv_lshlrev_b32 v115, 16, v109\nMvaa 44,188,132\nMU 116,112,114\nMvaa 44,184,128\ns_waitcnt vmcnt(12) lgkmcnt(0)\ns_barrier\n"
"Mvaa 48,224,128\nRa 192,140,32768\ns_add_u32 m0, s47, -49152\ns_add_u32 s46, s52, 512\nMvaa 48,228,132\nRa 196,141,32768\nMaaa 112,228,140\nRa 200,140,34816\nD0 136,28,46,0\n"
"Maaa 112,224,136\nRa 204,141,34816\nMvaa 208,224,144\nRa 208,142,32768\nMU 117,113,114\nMvaa 208,228,148\nRa 212,143,32768\nMvaa 240,228,156\nRa 216,142,34816\nMvaa 240,224,152\n"
"Ra 220,143,34816\nD0 137,28,46,1024\nMvaa 244,232,152\nTa 160,12,32768\nCV 116,116,117\nMvaa 244,236,156\nTa 162,12,34816\nMvaa 212,236,148\nTa 164,12,49152\nMvaa 212,232,144\n"
"Ta 166,12,51200\nMaaa 116,232,136\nTa 168,13,32768\nD0 138,28,46,2048\nMaaa 116,236,140\nTa 170,13,34816\nMvaa 52,236,132\nTa 172,13,49152\nMvaa 52,232,128\nTa 174,13,51200\n"
"Mvaa 56,240,128\nTa 176,14,32768\nv_and_b32 v117, 0xffff0000, v116\nMvaa 56,244,132\nTa 178,14,34816\nD0 139,28,46,3072\nMaaa 120,244,140\nTa 180,14,49152\nMaaa 120,240,136\n"
"Ta 182,14,51200\nMvaa 216,240,144\nTa 184,15,32768\ns_add_u32 m0, s47, 0\ns_add_u32 s46, s51, 2097152\nMvaa 216,244,148\nTa 186,15,34816\nMvaa 248,244,156\nTa 188,15,49152\n"
"D0 6,32,46,0\nMvaa 248,240,152\nTa 190,15,51200\nMvaa 252,248,152\nv_lshlrev_b32 v116, 16, v116\nFM 120,112,115\nMvaa 252,252,156\nFM 121,113,115\nMvaa 220,252,148\n"
"AD 118,118,116\nMvaa 220,248,144\nD0 7,32,46,1024\nMaaa 124,248,136\nAD 119,119,117\nv_and_b32 v112, 0xffff0000, v97\nMaaa 124,252,140\nv_and_b32 v113, 0xffff0000, v101\n"
"Mvaa 60,252,132\nv_and_b32 v114, 0xffff0000, v105\nMvaa 60,248,128\nWL 0\nMvaa 32,160,192\nTa 224,16,32768\nD0 8,32,46,2048\nMvaa 32,164,196\nTa 226,16,34816\nMaaa 96,164,204\n"
"v_and_b32 v115, 0xffff0000, v109\nMaaa 96,160,200\nTa 228,16,49152\nMvaa 192,160,208\nTa 230,16,51200\nMU 116,112,114\nMvaa 192,164,212\nD0 9,32,46,3072\nMvaa 224,164,220\n"
"Ta 232,17,32768\nMvaa 224,160,216\nTa 234,17,34816\nMvaa 228,168,216\ns_add_u32 m0, s47, 16384\ns_add_u32 s46, s51, 2359296\nMvaa 228,172,220\nTa 236,17,49152\nMvaa 196,172,212\n"
"Ta 238,17,51200\nD0 6,32,46,0\nMvaa 196,168,208\nMU 117,113,114\nMaaa 100,168,200\nTa 240,18,32768\nCV 116,116,117\nMaaa 100,172,204\nTa 242,18,34816\nMvaa 36,172,196\n"
"v_and_b32 v117, 0xffff0000, v116\nMvaa 36,168,192\nTa 244,18,49152\nD0 7,32,46,1024\nMvaa 40,176,192\nTa 246,18,51200\nv_lshlrev_b32 v116, 16, v116\nMvaa 40,180,196\n"
"FM 120,112,115\nMaaa 104,180,204\nTa 248,19,32768\nMaaa 104,176,200\nTa 250,19,34816\nMvaa 200,176,208\nD0 8,32,46,2048\nFM 121,113,115\nMvaa 200,180,212\nTa 252,19,49152\n"
"Mvaa 232,180,220\nTa 254,19,51200\nMvaa 232,176,216\nAD 118,118,116\nMvaa 236,184,216\nAD 119,119,117\nv_lshlrev_b32 v112, 16, v98\nMvaa 236,188,220\nD0 9,32,46,3072\n"
"Mvaa 204,188,212\nv_lshlrev_b32 v113, 16, v102\nMvaa 204,184,208\nv_lshlrev_b32 v114, 16, v106\nMaaa 108,184,200\nv_lshlrev_b32 v115, 16, v110\nMU 116,112,114\nMaaa 108,188,204\n"
"MU 117,113,114\nMvaa 44,188,196\nCV 116,116,117\nMvaa 44,184,192\ns_waitcnt vmcnt(12) lgkmcnt(0)\ns_barrier\nMvaa 48,224,192\nRa 128,140,49152\ns_add_u32 m0, s47, -32768\n"
"s_add_u32 s46, s52, 640\nMvaa 48,228,196\nRa 132,141,49152\nMaaa 112,228,204\nRa 136,140,51200\nD0 136,28,46,0\nMaaa 112,224,200\nRa 140,141,51200\nMvaa 208,224,208\n"
"Ra 144,142,49152\nv_and_b32 v117, 0xffff0000, v116\nMvaa 208,228,212\nRa 148,143,49152\nMvaa 240,228,220\nRa 152,142,51200\nMvaa 240,224,216\nRa 156,143,51200\nD0 137,28,46,1024\n"
"Mvaa 244,232,216\nTa 160,128,0\nv_lshlrev_b32 v116, 16, v116\nMvaa 244,236,220\nTa 162,128,2048\nMvaa 212,236,212\nTa 164,128,16384\nMvaa 212,232,208\nTa 166,128,18432\n"
"Maaa 116,232,200\nTa 168,129,0\nD0 138,28,46,2048\nMaaa 116,236,204\nTa 170,129,2048\nMvaa 52,236,196\nTa 172,129,16384\nMvaa 52,232,192\nTa 174,129,18432\nMvaa 56,240,192\n"
"Ta 176,130,0\nFM 120,112,115\nMvaa 56,244,196\nTa 178,130,2048\nD0 139,28,46,3072\nMaaa 120,244,204\nTa 180,130,16384\nMaaa 120,240,200\nTa 182,130,18432\nMvaa 216,240,208\n"
"Ta 184,131,0\ns_add_u32 m0, s47, 32768\ns_add_u32 s46, s51, 2621440\nMvaa 216,244,212\nTa 186,131,2048\nMvaa 248,244,220\nTa 188,131,16384\nD0 6,32,46,0\nMvaa 248,240,216\n"
"Ta 190,131,18432\nMvaa 252,248,216\nFM 121,113,115\nAD 118,118,116\nMvaa 252,252,220\nAD 119,119,117\nMvaa 220,252,212\nv_and_b32 v112, 0xffff0000, v98\nMvaa 220,248,208\n"
"D0 7,32,46,1024\nMaaa 124,248,200\nv_and_b32 v113, 0xffff0000, v102\nv_and_b32 v114, 0xffff0000, v106\nMaaa 124,252,204\nv_and_b32 v115, 0xffff0000, v110\nMvaa 60,252,196\n"
"MU 116,112,114\nMvaa 60,248,192\nWL 0\nMvaa 32,160,128\nTa 224,132,0\n.W539:\ns_cbranch_execz .W543\nD0 8,32,46,2048\nMvaa 32,164,132\nTa 226,132,2048\nMaaa 96,164,140\n"
"MU 117,113,114\nMaaa 96,160,136\nTa 228,132,16384\nMvaa 192,160,144\nTa 230,132,18432\nCV 116,116,117\nMvaa 192,164,148\nD0 9,32,46,3072\nMvaa 224,164,156\nTa 232,133,0\n"
"Mvaa 224,160,152\nTa 234,133,2048\nMvaa 228,168,152\ns_add_u32 m0, s47, 49152\ns_add_u32 s46, s51, 2883584\nMvaa 228,172,156\nTa 236,133,16384\nMvaa 196,172,148\nTa 238,133,18432\n"
"D0 6,32,46,0\nMvaa 196,168,144\nv_and_b32 v117, 0xffff0000, v116\nMaaa 100,168,136\nTa 240,134,0\nv_lshlrev_b32 v116, 16, v116\nMaaa 100,172,140\nTa 242,134,2048\nMvaa 36,172,132\n"
"FM 120,112,115\nMvaa 36,168,128\nTa 244,134,16384\nD0 7,32,46,1024\nMvaa 40,176,128\nTa 246,134,18432\nFM 121,113,115\nMvaa 40,180,132\nAD 118,118,116\nMaaa 104,180,140\n"
"Ta 248,135,0\nMaaa 104,176,136\nTa 250,135,2048\nMvaa 200,176,144\nD0 8,32,46,2048\nAD 119,119,117\nMvaa 200,180,148\nTa 252,135,16384\nMvaa 232,180,156\nTa 254,135,18432\n"
"Mvaa 232,176,152\nv_lshlrev_b32 v112, 16, v99\nMvaa 236,184,152\nv_lshlrev_b32 v113, 16, v103\nv_lshlrev_b32 v114, 16, v107\nMvaa 236,188,156\nD0 9,32,46,3072\nMvaa 204,188,148\n"
"v_lshlrev_b32 v115, 16, v111\nMvaa 204,184,144\nMU 116,112,114\nMaaa 108,184,136\nMU 117,113,114\nCV 116,116,117\ns_and_b32 s78, s74, 3\ns_lshl2_add_u32 s78, s73, s78\n"
"s_lshl_b32 s78, s78, 12\ns_bitcmp1_b32 s11, 0\ns_cselect_b32 s79, s48, s45\ns_add_u32 s79, s79, 512\ns_and_b32 s79, s79, 0xfffe0000\ns_add_u32 s78, s78, s79\n"
"buffer_load_dword v31, v26, s[20:23], s78 offen\nMaaa 108,188,140\nv_and_b32 v117, 0xffff0000, v116\nMvaa 44,188,132\nv_lshlrev_b32 v116, 16, v116\nMvaa 44,184,128\n"
"s_waitcnt vmcnt(13) lgkmcnt(0)\ns_barrier\nMvaa 48,224,128\nRa 192,140,16384\ns_add_u32 m0, s47, -16384\ns_add_u32 s46, s52, 768\nMvaa 48,228,132\nRa 196,141,16384\n"
"Maaa 112,228,140\nRa 200,140,18432\nD0 136,28,46,0\nMaaa 112,224,136\nRa 204,141,18432\nMvaa 208,224,144\nRa 208,142,16384\nFM 120,112,115\nMvaa 208,228,148\nRa 212,143,16384\n"
"Mvaa 240,228,156\nRa 216,142,18432\nMvaa 240,224,152\nRa 220,143,18432\nD0 137,28,46,1024\nMvaa 244,232,152\nTa 160,12,0\nFM 121,113,115\nMvaa 244,236,156\nTa 162,12,2048\n"
"Mvaa 212,236,148\nTa 164,12,16384\nMvaa 212,232,144\nTa 166,12,18432\nMaaa 116,232,136\nTa 168,13,0\nD0 138,28,46,2048\nMaaa 116,236,140\nTa 170,13,2048\nMvaa 52,236,132\n"
"Ta 172,13,16384\nMvaa 52,232,128\nTa 174,13,18432\nMvaa 56,240,128\nTa 176,14,0\nAD 118,118,116\nMvaa 56,244,132\nTa 178,14,2048\nD0 139,28,46,3072\nMaaa 120,244,140\n"
"Ta 180,14,16384\nMaaa 120,240,136\nTa 182,14,18432\nMvaa 216,240,144\nTa 184,15,0\ns_add_u32 m0, s47, 65536\ns_add_u32 s46, s51, 3145728\nMvaa 216,244,148\nTa 186,15,2048\n"
"Mvaa 248,244,156\nTa 188,15,16384\nD0 6,32,46,0\nMvaa 248,240,152\nTa 190,15,18432\nMvaa 252,248,152\nAD 119,119,117\nv_and_b32 v112, 0xffff0000, v99\nMvaa 252,252,156\n"
"v_and_b32 v113, 0xffff0000, v103\nMvaa 220,252,148\nv_and_b32 v114, 0xffff0000, v107\nMvaa 220,248,144\nD0 7,32,46,1024\nMaaa 124,248,136\nv_and_b32 v115, 0xffff0000, v111\n"
"MU 116,112,114\nMaaa 124,252,140\nMU 117,113,114\nMvaa 60,252,132\nCV 116,116,117\nMvaa 60,248,128\nWL 0\nMvaa 32,160,192\nTa 224,16,0\nD0 8,32,46,2048\nMvaa 32,164,196\n"
"Ta 226,16,2048\nMaaa 96,164,204\nv_and_b32 v117, 0xffff0000, v116\nMaaa 96,160,200\nTa 228,16,16384\nMvaa 192,160,208\nTa 230,16,18432\nv_lshlrev_b32 v116, 16, v116\n"
"Mvaa 192,164,212\nD0 9,32,46,3072\nMvaa 224,164,220\nTa 232,17,0\nMvaa 224,160,216\nTa 234,17,2048\nMvaa 228,168,216\ns_add_u32 m0, s47, 81920\ns_add_u32 s46, s51, 3407872\n"
"Mvaa 228,172,220\nTa 236,17,16384\nMvaa 196,172,212\nTa 238,17,18432\nD0 6,32,46,0\nMvaa 196,168,208\nFM 120,112,115\nMaaa 100,168,200\nTa 240,18,0\nFM 121,113,115\n"
"Maaa 100,172,204\nTa 242,18,2048\nMvaa 36,172,196\nAD 118,118,116\nMvaa 36,168,192\nTa 244,18,16384\nD0 7,32,46,1024\nMvaa 40,176,192\nTa 246,18,18432\nAD 119,119,117\n"
"Mvaa 40,180,196\ns_nop 0\nv_permlane32_swap_b32 v118, v119\nMaaa 104,180,204\nTa 248,19,0\nMaaa 104,176,200\nTa 250,19,2048\nMvaa 200,176,208\nD0 8,32,46,2048\n"
"v_permlane32_swap_b32 v120, v121\nMvaa 200,180,212\nTa 252,19,16384\nMvaa 232,180,220\nTa 254,19,18432\nMvaa 232,176,216\nAD 118,118,119\nMvaa 236,184,216\nAD 120,120,121\n"
"Mvaa 236,188,220\nD0 9,32,46,3072\nMvaa 204,188,212\nv_permlane16_swap_b32 v118, v120\nMvaa 204,184,208\nAD 118,118,120\nMaaa 108,184,200\nMaaa 108,188,204\n"
"v_add_f32_dpp v118, v118, v118 row_ror:8 row_mask:0xf bank_mask:0xf\nMvaa 44,188,196\nMvaa 44,184,192\ns_waitcnt vmcnt(13) lgkmcnt(0)\ns_barrier\nMvaa 48,224,192\n"
"Ra 128,140,32768\ns_add_u32 m0, s47, -65536\ns_add_u32 s45, s45, 128\nMvaa 48,228,196\nRa 132,141,32768\nMaaa 112,228,204\nRa 136,140,34816\nD0 2,20,45,0\nMaaa 112,224,200\n"
"Ra 140,141,34816\nMvaa 208,224,208\nRa 144,142,32768\nv_add_f32_dpp v118, v118, v118 row_ror:4 row_mask:0xf bank_mask:0xf\nMvaa 208,228,212\nRa 148,143,32768\nMvaa 240,228,220\n"
"Ra 152,142,34816\nMvaa 240,224,216\nRa 156,143,34816\nD0 3,20,45,1024\nMvaa 244,232,216\nTa 160,12,32768\nv_add_f32_dpp v118, v118, v118 row_ror:2 row_mask:0xf bank_mask:0xf\n"
"Mvaa 244,236,220\nTa 162,12,34816\nMvaa 212,236,212\nTa 164,12,49152\nMvaa 212,232,208\nTa 166,12,51200\nMaaa 116,232,200\nTa 168,13,32768\nD0 4,20,45,2048\nMaaa 116,236,204\n"
"Ta 170,13,34816\nMvaa 52,236,196\nTa 172,13,49152\nMvaa 52,232,192\nTa 174,13,51200\nMvaa 56,240,192\nTa 176,14,32768\n"
"v_add_f32_dpp v118, v118, v118 row_ror:1 row_mask:0xf bank_mask:0xf\nMvaa 56,244,196\nTa 178,14,34816\nD0 5,20,45,3072\nMaaa 120,244,204\nTa 180,14,49152\nMaaa 120,240,200\n"
"Ta 182,14,51200\nMvaa 216,240,208\nTa 184,15,32768\ns_add_u32 m0, s47, 0\ns_add_u32 s44, s44, 0x40000\nMvaa 216,244,212\nTa 186,15,34816\nMvaa 248,244,220\nTa 188,15,49152\n"
"D0 6,24,44,0\nMvaa 248,240,216\nTa 190,15,51200\nMvaa 252,248,216\nCV 118,118,118\nv_lshlrev_b32 v118, 16, v118\nMvaa 252,252,220\nv_mul_f32 v119, 0xbfb8aa3b, v118\nEX 119,119\n"
"Mvaa 220,252,212\nv_add_f32 v120, 0, v118\nMvaa 220,248,208\nD0 7,24,44,1024\nMaaa 124,248,200\nv_add_f32 v119, 1.0, v119\nMaaa 124,252,204\nv_rcp_f32 v119, v119\nMvaa 60,252,196\n"
"v_ashrrev_i32 v121, 31, v120\nMvaa 60,248,192\nWL 0\nMvaa 32,160,128\nTa 224,16,32768\nD0 8,24,44,2048\nMvaa 32,164,132\nTa 226,16,34816\nMaaa 96,164,140\nCV 119,119,119\n"
"Maaa 96,160,136\nTa 228,16,49152\nMvaa 192,160,144\nTa 230,16,51200\nv_lshlrev_b32 v119, 16, v119\nMvaa 192,164,148\nD0 9,24,44,3072\nMvaa 224,164,156\nTa 232,17,32768\n"
"Mvaa 224,160,152\nTa 234,17,34816\nMvaa 228,168,152\ns_add_u32 m0, s47, 16384\ns_add_u32 s44, s44, 0x40000\nMvaa 228,172,156\nTa 236,17,49152\nMvaa 196,172,148\nTa 238,17,51200\n"
"D0 6,24,44,0\nMvaa 196,168,144\nv_or_b32 v121, 0x80000000, v121\nMaaa 100,168,136\nTa 240,18,32768\nv_xor_b32 v120, v120, v121\nMaaa 100,172,140\nTa 242,18,34816\nMvaa 36,172,132\n"
"v_and_b32 v120, 0xffff0000, v120\nMvaa 36,168,128\nTa 244,18,49152\nD0 7,24,44,1024\nMvaa 40,176,128\nTa 246,18,51200\ns_lshl2_add_u32 s2, s10, s11\nMvaa 40,180,132\n"
"v_lshrrev_b32 v112, 5, v0\nMaaa 104,180,140\nTa 248,19,32768\nMaaa 104,176,136\nTa 250,19,34816\nMvaa 200,176,144\nD0 8,24,44,2048\nv_lshl_add_u32 v112, s2, 1, v112\n"
"Mvaa 200,180,148\nTa 252,19,49152\nMvaa 232,180,156\nTa 254,19,51200\nMvaa 232,176,152\nv_sub_u32 v113, 0xffff, v112\nMvaa 236,184,152\nv_or_b32 v120, v120, v113\n"
"v_mov_b32 v121, s15\nMvaa 236,188,156\nD0 9,24,44,3072\nMvaa 204,188,148\nv_and_b32 v113, 31, v0\nMvaa 204,184,144\nv_mov_b32 v116, 0x40000000\nv_cmp_eq_u32 vcc, 16, v113\n"
"v_lshlrev_b32 v114, 2, v112\nv_lshlrev_b32 v115, 10, v113\nv_cndmask_b32 v114, v116, v114, vcc\nv_cndmask_b32 v120, v119, v120, vcc\nMaaa 108,184,136\n"
"v_lshl_add_u32 v115, v112, 3, v115\nv_and_b32 v113, 15, v0\nMaaa 108,188,140\nv_cmp_eq_u32 vcc, 0, v113\ns_mov_b32 s2, 0xc040000\ns_mov_b32 s3, 0xc03e000\n"
"v_cndmask_b32 v115, v116, v115, vcc\nbuffer_store_dwordx2 v[120:121], v115, s[16:19], s2 offen sc0 sc1\nbuffer_store_dword v118, v114, s[16:19], s3 offen sc0 sc1\nMvaa 44,188,132\n"
"Mvaa 44,184,128\ns_waitcnt vmcnt(14) lgkmcnt(0)\ns_barrier\nMvaa 48,224,128\nRa 192,140,49152\ns_add_u32 m0, s47, -49152\ns_add_u32 s48, s48, 128\nMvaa 48,228,132\n"
"Ra 196,141,49152\nMaaa 112,228,140\nRa 200,140,51200\nD0 2,20,48,0\nMaaa 112,224,136\nRa 204,141,51200\nMvaa 208,224,144\nRa 208,142,49152\nMvaa 208,228,148\nRa 212,143,49152\n"
"Mvaa 240,228,156\nRa 216,142,51200\nMvaa 240,224,152\nRa 220,143,51200\nD0 3,20,48,1024\nMvaa 244,232,152\nTa 160,128,0\nMvaa 244,236,156\nTa 162,128,2048\nMvaa 212,236,148\n"
"Ta 164,128,16384\nMvaa 212,232,144\nTa 166,128,18432\nMaaa 116,232,136\nTa 168,129,0\nD0 4,20,48,2048\nMaaa 116,236,140\nTa 170,129,2048\nMvaa 52,236,132\nTa 172,129,16384\n"
"Mvaa 52,232,128\nTa 174,129,18432\nMvaa 56,240,128\nTa 176,130,0\nMvaa 56,244,132\nTa 178,130,2048\nD0 5,20,48,3072\nMaaa 120,244,140\nTa 180,130,16384\nMaaa 120,240,136\n"
"Ta 182,130,18432\nMvaa 216,240,144\nTa 184,131,0\ns_add_u32 m0, s47, -32768\ns_add_u32 s45, s45, 128\nMvaa 216,244,148\nTa 186,131,2048\nMvaa 248,244,156\nTa 188,131,16384\n"
"D0 2,20,45,0\nMvaa 248,240,152\nTa 190,131,18432\nMvaa 252,248,152\nMvaa 252,252,156\nMvaa 220,252,148\nMvaa 220,248,144\nD0 3,20,45,1024\nMaaa 124,248,136\nMaaa 124,252,140\n"
"Mvaa 60,252,132\nMvaa 60,248,128\nWL 0\nMvaa 32,160,192\nTa 224,132,0\nD0 4,20,45,2048\nMvaa 32,164,196\nTa 226,132,2048\nMvaa 36,172,196\nMvaa 36,168,192\nTa 228,132,16384\n"
"Mvaa 40,176,192\nTa 230,132,18432\nMvaa 40,180,196\nD0 5,20,45,3072\nMvaa 44,188,196\nTa 232,133,0\nMvaa 44,184,192\nTa 234,133,2048\nMaaa 108,184,200\ns_add_u32 m0, s47, 32768\n"
"s_add_u32 s44, s44, 0x40000\nMaaa 108,188,204\nTa 236,133,16384\nMaaa 104,180,204\nTa 238,133,18432\nD0 6,24,44,0\nMaaa 104,176,200\nv_mul_f32 v32, s57, v32\n"
"v_mul_f32 v33, s57, v33\nMaaa 100,168,200\nTa 240,134,0\nCV 32,32,33\nMaaa 100,172,204\nTa 242,134,2048\nv_mul_f32 v34, s57, v34\nMaaa 96,164,204\nv_mul_f32 v35, s57, v35\n"
"CV 33,34,35\nMaaa 96,160,200\nTa 244,134,16384\nD0 7,24,44,1024\nMvaa 192,160,208\nTa 246,134,18432\nv_mul_f32 v36, s57, v36\nMvaa 192,164,212\nv_mul_f32 v37, s57, v37\n"
"Mvaa 196,172,212\nTa 248,135,0\nCV 34,36,37\nMvaa 196,168,208\nTa 250,135,2048\nv_mul_f32 v38, s57, v38\nMvaa 200,176,208\nD0 8,24,44,2048\nv_mul_f32 v39, s57, v39\n"
"Mvaa 200,180,212\nTa 252,135,16384\nCV 35,38,39\nMvaa 204,188,212\nTa 254,135,18432\nv_mul_f32 v40, s57, v40\nMvaa 204,184,208\nv_mul_f32 v41, s57, v41\nCV 40,40,41\n"
"Mvaa 236,184,216\nv_mul_f32 v42, s57, v42\nv_mul_f32 v43, s57, v43\nMvaa 236,188,220\nD0 9,24,44,3072\nMvaa 232,180,220\nCV 41,42,43\nv_mul_f32 v44, s57, v44\nMvaa 232,176,216\n"
"v_mul_f32 v45, s57, v45\nCV 42,44,45\nMvaa 228,168,216\nv_mul_f32 v46, s57, v46\nv_mul_f32 v47, s57, v47\nMvaa 228,172,220\nCV 43,46,47\nAR 20,96\nMvaa 224,164,220\nAR 21,97\n"
"v_mul_f32 v20, s57, v20\nMvaa 224,160,216\ns_waitcnt vmcnt(14) lgkmcnt(0)\ns_barrier\nMvaa 48,224,192\ns_add_u32 m0, s47, 49152\ns_add_u32 s44, s44, 0x40000\nRa 128,10,0\n"
"Mvaa 48,228,196\nRa 132,11,0\nMvaa 52,236,196\nD0 6,24,44,0\nRa 136,10,2048\n.W540:\ns_cbranch_execz .W544\nMvaa 52,232,192\nRa 140,11,2048\nv_mul_f32 v21, s57, v21\n"
"Mvaa 56,240,192\nRa 144,10,8192\nCV 36,20,21\nMvaa 56,244,196\nRa 148,11,8192\nAR 20,98\nMvaa 60,252,196\nD0 7,24,44,1024\nRa 152,10,10240\nMvaa 60,248,192\nRa 156,11,10240\n"
"AR 21,99\nMaaa 124,248,200\nTa 160,12,0\nv_mul_f32 v20, s57, v20\nMaaa 124,252,204\nTa 162,12,2048\nMaaa 120,244,204\nD0 8,24,44,2048\nTa 164,12,16384\nMaaa 120,240,200\n"
"Ta 166,12,18432\nv_mul_f32 v21, s57, v21\nMaaa 116,232,200\nTa 168,13,0\nCV 37,20,21\nMaaa 116,236,204\nTa 170,13,2048\nAR 20,100\nMaaa 112,228,204\nD0 9,24,44,3072\n"
"Ta 172,13,16384\nMaaa 112,224,200\nTa 174,13,18432\nAR 21,101\nMvaa 208,224,208\ns_add_u32 m0, s47, -16384\ns_add_u32 s48, s48, 128\nTa 176,14,0\nMvaa 208,228,212\nTa 178,14,2048\n"
"Mvaa 212,236,212\nD0 2,20,48,0\nTa 180,14,16384\nMvaa 212,232,208\nTa 182,14,18432\nv_mul_f32 v20, s57, v20\nMvaa 216,240,208\nTa 184,15,0\nv_mul_f32 v21, s57, v21\n"
"Mvaa 216,244,212\nTa 186,15,2048\nCV 38,20,21\nMvaa 220,252,212\nD0 3,20,48,1024\nTa 188,15,16384\nMvaa 220,248,208\nTa 190,15,18432\nAR 20,102\nMvaa 252,248,216\nAR 21,103\n"
"v_mul_f32 v20, s57, v20\nMvaa 252,252,220\nv_mul_f32 v21, s57, v21\nMvaa 248,244,220\nD0 4,20,48,2048\nCV 39,20,21\nMvaa 248,240,216\nAR 20,104\nAR 21,105\nMvaa 244,232,216\n"
"v_mul_f32 v20, s57, v20\nv_mul_f32 v21, s57, v21\nMvaa 244,236,220\nCV 44,20,21\nAR 20,106\nMvaa 240,228,220\nD0 5,20,48,3072\nAR 21,107\nMvaa 240,224,216\nWL 0\ns_barrier\n"
"Zvaa 64,160,128\nTa 224,16,0\nv_mul_f32 v20, s57, v20\nMvaa 64,164,132\nTa 226,16,2048\nZvaa 96,164,140\ns_add_u32 m0, s47, -65536\ns_add_u32 s45, s45, 128\nMvaa 96,160,136\n"
"Ta 228,16,16384\nv_mul_f32 v21, s57, v21\nZvaa 128,160,144\nTa 230,16,18432\nCV 45,20,21\nMvaa 128,164,148\nD0 2,20,45,0\nAR 20,108\nZvaa 160,164,156\nTa 232,17,0\nAR 21,109\n"
"Mvaa 160,160,152\nTa 234,17,2048\nv_mul_f32 v20, s57, v20\nZvaa 164,168,152\nv_mul_f32 v21, s57, v21\nCV 46,20,21\nMvaa 164,172,156\nTa 236,17,16384\ns_add_u32 s6, s56, 0\n"
"Sv 32,1,16,6,0\nZvaa 132,172,148\nTa 238,17,18432\nAR 20,110\nMvaa 132,168,144\nAR 21,111\nv_mul_f32 v20, s57, v20\nZvaa 100,168,136\nTa 240,18,0\nv_mul_f32 v21, s57, v21\n"
"Mvaa 100,172,140\nTa 242,18,2048\nCV 47,20,21\nZvaa 68,172,132\nD0 3,20,45,1024\nv_mul_f32 v48, s57, v48\nMvaa 68,168,128\nTa 244,18,16384\nv_mul_f32 v49, s57, v49\n"
"Zvaa 72,176,128\nTa 246,18,18432\nCV 48,48,49\nMvaa 72,180,132\nv_mul_f32 v50, s57, v50\nZvaa 104,180,140\nTa 248,19,0\nv_mul_f32 v51, s57, v51\nMvaa 104,176,136\nTa 250,19,2048\n"
"CV 49,50,51\nZvaa 136,176,144\nv_mul_f32 v52, s57, v52\nv_mul_f32 v53, s57, v53\nMvaa 136,180,148\nTa 252,19,16384\nCV 50,52,53\nZvaa 168,180,156\nTa 254,19,18432\n"
"v_mul_f32 v54, s57, v54\nMvaa 168,176,152\nD0 4,20,45,2048\nv_mul_f32 v55, s57, v55\nZvaa 172,184,152\nCV 51,54,55\nv_mul_f32 v56, s57, v56\nMvaa 172,188,156\n"
"v_mul_f32 v57, s57, v57\ns_add_u32 s6, s56, 0\nSv 40,1,16,6,2048\nZvaa 140,188,148\nCV 56,56,57\nv_mul_f32 v58, s57, v58\nMvaa 140,184,144\nv_mul_f32 v59, s57, v59\nCV 57,58,59\n"
"Zvaa 108,184,136\nv_mul_f32 v60, s57, v60\nv_mul_f32 v61, s57, v61\nMvaa 108,188,140\nD0 5,20,45,3072\nCV 58,60,61\nZvaa 76,188,132\nv_mul_f32 v62, s57, v62\n"
"v_mul_f32 v63, s57, v63\nMvaa 76,184,128\ns_waitcnt vmcnt(18) lgkmcnt(0)\ns_barrier\nZvaa 80,224,128\nRa 192,10,16384\nCV 59,62,63\nMvaa 80,228,132\ns_add_u32 m0, s47, 0\n"
"s_add_u32 s44, s44, 0x40000\nZvaa 112,228,140\nRa 196,11,16384\nAR 20,112\nMvaa 112,224,136\nAR 21,113\nv_mul_f32 v20, s57, v20\nZvaa 144,224,144\nRa 200,10,18432\n"
"v_mul_f32 v21, s57, v21\nMvaa 144,228,148\nD0 6,24,44,0\nCV 52,20,21\nZvaa 176,228,156\nRa 204,11,18432\nAR 20,114\nMvaa 176,224,152\nAR 21,115\nv_mul_f32 v20, s57, v20\n"
"Zvaa 180,232,152\nRa 208,10,24576\nv_mul_f32 v21, s57, v21\nMvaa 180,236,156\nCV 53,20,21\ns_add_u32 s6, s56, 0\nSv 36,1,16,6,1024\nZvaa 148,236,148\nRa 212,11,24576\nAR 20,116\n"
"Mvaa 148,232,144\nAR 21,117\nv_mul_f32 v20, s57, v20\nZvaa 116,232,136\nRa 216,10,26624\nv_mul_f32 v21, s57, v21\nMvaa 116,236,140\nD0 7,24,44,1024\nCV 54,20,21\nZvaa 84,236,132\n"
"Ra 220,11,26624\nAR 20,118\nMvaa 84,232,128\nAR 21,119\nv_mul_f32 v20, s57, v20\nZvaa 88,240,128\nv_mul_f32 v21, s57, v21\nCV 55,20,21\nMvaa 88,244,132\nAR 20,120\n"
"Zvaa 120,244,140\nAR 21,121\nv_mul_f32 v20, s57, v20\nMvaa 120,240,136\nv_mul_f32 v21, s57, v21\nCV 60,20,21\nZvaa 152,240,144\nAR 20,122\nAR 21,123\nMvaa 152,244,148\n"
"D0 8,24,44,2048\nv_mul_f32 v20, s57, v20\nZvaa 184,244,156\nv_mul_f32 v21, s57, v21\nCV 61,20,21\nMvaa 184,240,152\nAR 20,124\nAR 21,125\nZvaa 188,248,152\n"
"v_mul_f32 v20, s57, v20\nv_mul_f32 v21, s57, v21\nMvaa 188,252,156\nCV 62,20,21\ns_add_u32 s6, s56, 0\nSv 44,1,16,6,3072\nZvaa 156,252,148\nAR 20,126\nAR 21,127\nMvaa 156,248,144\n"
"v_mul_f32 v20, s57, v20\nv_mul_f32 v21, s57, v21\nZvaa 124,248,136\nCV 63,20,21\nv_mul_f32 v192, s57, v192\nMvaa 124,252,140\nD0 9,24,44,3072\nv_mul_f32 v193, s57, v193\n"
"Zvaa 92,252,132\nCV 192,192,193\nv_mul_f32 v194, s57, v194\nMvaa 92,248,128\nWL 0\nZaaa 16,224,192\nRa 128,10,32768\nv_mul_f32 v195, s57, v195\nMaaa 16,228,196\n"
"s_add_u32 m0, s47, 16384\ns_add_u32 s44, s44, 0x40000\nZaaa 48,228,204\nRa 132,11,32768\nCV 193,194,195\nMaaa 48,224,200\nv_mul_f32 v196, s57, v196\nv_mul_f32 v197, s57, v197\n"
"Zaaa 80,224,208\nRa 136,10,34816\nCV 194,196,197\nMaaa 80,228,212\nD0 6,24,44,0\nv_mul_f32 v198, s57, v198\nZaaa 112,228,220\nRa 140,11,34816\nv_mul_f32 v199, s57, v199\n"
"Maaa 112,224,216\nCV 195,198,199\nv_mul_f32 v200, s57, v200\nZaaa 116,232,216\nRa 144,10,40960\nv_mul_f32 v201, s57, v201\nMaaa 116,236,220\nCV 200,200,201\n"
"v_mul_f32 v202, s57, v202\ns_add_u32 s6, s56, 4096\nSv 48,1,16,6,0\nZaaa 84,236,212\nRa 148,11,40960\nv_mul_f32 v203, s57, v203\nMaaa 84,232,208\nCV 201,202,203\n"
"v_mul_f32 v204, s57, v204\nZaaa 52,232,200\nRa 152,10,43008\nv_mul_f32 v205, s57, v205\nMaaa 52,236,204\nD0 7,24,44,1024\nCV 202,204,205\nZaaa 20,236,196\nRa 156,11,43008\n"
"v_mul_f32 v206, s57, v206\nMaaa 20,232,192\nv_mul_f32 v207, s57, v207\nCV 203,206,207\nZaaa 24,240,192\nv_mul_f32 v208, s57, v208\nv_mul_f32 v209, s57, v209\nMaaa 24,244,196\n"
"CV 208,208,209\nv_mul_f32 v210, s57, v210\nZaaa 56,244,204\nv_mul_f32 v211, s57, v211\nCV 209,210,211\nMaaa 56,240,200\nv_mul_f32 v212, s57, v212\nv_mul_f32 v213, s57, v213\n"
"Zaaa 88,240,208\nCV 210,212,213\nv_mul_f32 v214, s57, v214\nMaaa 88,244,212\nD0 8,24,44,2048\nv_mul_f32 v215, s57, v215\nZaaa 120,244,220\nCV 211,214,215\n"
"v_mul_f32 v216, s57, v216\nMaaa 120,240,216\nv_mul_f32 v217, s57, v217\nCV 216,216,217\nZaaa 124,248,216\nv_mul_f32 v218, s57, v218\nv_mul_f32 v219, s57, v219\nMaaa 124,252,220\n"
"CV 217,218,219\nv_mul_f32 v220, s57, v220\ns_add_u32 s6, s56, 4096\nSv 56,1,16,6,2048\nZaaa 92,252,212\nv_mul_f32 v221, s57, v221\nCV 218,220,221\nMaaa 92,248,208\n"
"v_mul_f32 v222, s57, v222\nv_mul_f32 v223, s57, v223\nZaaa 60,248,200\nCV 219,222,223\nv_mul_f32 v224, s57, v224\nMaaa 60,252,204\nD0 9,24,44,3072\nv_mul_f32 v225, s57, v225\n"
"Zaaa 28,252,196\nCV 224,224,225\nv_mul_f32 v226, s57, v226\nMaaa 28,248,192\ns_waitcnt vmcnt(22) lgkmcnt(0)\ns_barrier\nZaaa 0,160,192\nTa 224,16,32768\nv_mul_f32 v227, s57, v227\n"
"Maaa 0,164,196\nTa 226,16,34816\nCV 225,226,227\nZaaa 32,164,204\ns_add_u32 m0, s47, -49152\ns_add_u32 s48, s48, 128\nMaaa 32,160,200\nTa 228,16,49152\nv_mul_f32 v228, s57, v228\n"
"Zaaa 64,160,208\nTa 230,16,51200\nv_mul_f32 v229, s57, v229\nMaaa 64,164,212\nD0 2,20,48,0\nCV 226,228,229\nZaaa 96,164,220\nTa 232,17,32768\nv_mul_f32 v230, s57, v230\n"
"Maaa 96,160,216\nTa 234,17,34816\nv_mul_f32 v231, s57, v231\nZaaa 100,168,216\nCV 227,230,231\nv_mul_f32 v232, s57, v232\nMaaa 100,172,220\nTa 236,17,49152\n"
"v_mul_f32 v233, s57, v233\ns_add_u32 s6, s56, 4096\nSv 52,1,16,6,1024\nZaaa 68,172,212\nTa 238,17,51200\nCV 232,232,233\nMaaa 68,168,208\nv_mul_f32 v234, s57, v234\n"
"v_mul_f32 v235, s57, v235\nZaaa 36,168,200\nTa 240,18,32768\nCV 233,234,235\nMaaa 36,172,204\nTa 242,18,34816\nv_mul_f32 v236, s57, v236\nZaaa 4,172,196\nD0 3,20,48,1024\n"
"v_mul_f32 v237, s57, v237\nMaaa 4,168,192\nTa 244,18,49152\nCV 234,236,237\nZaaa 8,176,192\nTa 246,18,51200\nv_mul_f32 v238, s57, v238\nMaaa 8,180,196\nv_mul_f32 v239, s57, v239\n"
"CV 235,238,239\nZaaa 40,180,204\nTa 248,19,32768\nv_mul_f32 v240, s57, v240\nMaaa 40,176,200\nTa 250,19,34816\nv_mul_f32 v241, s57, v241\nZaaa 72,176,208\nCV 240,240,241\n"
"v_mul_f32 v242, s57, v242\nMaaa 72,180,212\nTa 252,19,49152\nv_mul_f32 v243, s57, v243\nZaaa 104,180,220\nTa 254,19,51200\nCV 241,242,243\nMaaa 104,176,216\nD0 4,20,48,2048\n"
"v_mul_f32 v244, s57, v244\nZaaa 108,184,216\nv_mul_f32 v245, s57, v245\nCV 242,244,245\nMaaa 108,188,220\nv_mul_f32 v246, s57, v246\nv_mul_f32 v247, s57, v247\n"
"s_add_u32 s6, s56, 4096\nSv 60,1,16,6,3072\nZaaa 76,188,212\nCV 243,246,247\nv_mul_f32 v248, s57, v248\nMaaa 76,184,208\nv_mul_f32 v249, s57, v249\nCV 248,248,249\n"
"Zaaa 44,184,200\nv_mul_f32 v250, s57, v250\nv_mul_f32 v251, s57, v251\nMaaa 44,188,204\nD0 5,20,48,3072\nCV 249,250,251\nZaaa 12,188,196\nv_mul_f32 v252, s57, v252\n"
"v_mul_f32 v253, s57, v253\nMaaa 12,184,192\nWL 0\nMvaa 80,224,128\nTa 160,12,32768\nCV 250,252,253\nMvaa 80,228,132\nTa 162,12,34816\nv_mul_f32 v254, s57, v254\nMvaa 112,228,140\n"
"s_add_u32 m0, s47, -32768\ns_add_u32 s45, s45, 128\nMvaa 112,224,136\nTa 164,12,49152\nv_mul_f32 v255, s57, v255\nMvaa 144,224,144\nTa 166,12,51200\nCV 251,254,255\n"
"Mvaa 144,228,148\nD0 2,20,45,0\nMvaa 176,228,156\nTa 168,13,32768\nMvaa 176,224,152\nTa 170,13,34816\nMvaa 180,232,152\nMvaa 180,236,156\nTa 172,13,49152\ns_add_u32 s6, s53, 0\n"
"Sv 192,1,16,6,0\nMvaa 148,236,148\nTa 174,13,51200\nMvaa 148,232,144\nMvaa 116,232,136\nTa 176,14,32768\nMvaa 116,236,140\nTa 178,14,34816\nMvaa 84,236,132\nD0 3,20,45,1024\n"
"Mvaa 84,232,128\nTa 180,14,49152\nMvaa 88,240,128\nTa 182,14,51200\nMvaa 88,244,132\nMvaa 120,244,140\nTa 184,15,32768\nMvaa 120,240,136\nTa 186,15,34816\nMvaa 152,240,144\n"
"Mvaa 152,244,148\nTa 188,15,49152\nMvaa 184,244,156\nTa 190,15,51200\nMvaa 184,240,152\nD0 4,20,45,2048\nMvaa 188,248,152\nMvaa 188,252,156\ns_add_u32 s6, s53, 0\n"
"Sv 200,1,16,6,2048\nMvaa 156,252,148\nMvaa 156,248,144\nMvaa 124,248,136\nMvaa 124,252,140\nD0 5,20,45,3072\nMvaa 92,252,132\nMvaa 92,248,128\ns_waitcnt vmcnt(24) lgkmcnt(0)\n"
"s_barrier\nMvaa 64,160,128\nRa 192,10,49152\nMvaa 64,164,132\ns_add_u32 m0, s47, 32768\ns_add_u32 s44, s44, 0x40000\nMvaa 96,164,140\nRa 196,11,49152\nMvaa 96,160,136\n"
"Mvaa 128,160,144\nRa 200,10,51200\nMvaa 128,164,148\nD0 6,24,44,0\nMvaa 160,164,156\nRa 204,11,51200\nMvaa 160,160,152\nMvaa 164,168,152\nRa 208,10,57344\nMvaa 164,172,156\n"
"s_add_u32 s6, s53, 4096\nSv 208,1,16,6,0\nMvaa 132,172,148\nRa 212,11,57344\nMvaa 132,168,144\nMvaa 100,168,136\nRa 216,10,59392\nMvaa 100,172,140\nD0 7,24,44,1024\n"
"Mvaa 68,172,132\nRa 220,11,59392\nMvaa 68,168,128\nMvaa 72,176,128\nMvaa 72,180,132\nMvaa 104,180,140\nMvaa 104,176,136\nMvaa 136,176,144\nMvaa 136,180,148\n.W541:\n"
"s_cbranch_execz .W545\nD0 8,24,44,2048\nMvaa 168,180,156\nMvaa 168,176,152\nMvaa 172,184,152\nMvaa 172,188,156\ns_add_u32 s6, s53, 4096\nSv 216,1,16,6,2048\nMvaa 140,188,148\n"
"Mvaa 140,184,144\nMvaa 108,184,136\nMvaa 108,188,140\nD0 9,24,44,3072\nMvaa 76,188,132\nMvaa 76,184,128\nWL 0\nMaaa 0,160,192\nRa 128,10,0\nMaaa 0,164,196\n"
"s_add_u32 m0, s47, 49152\ns_add_u32 s44, s44, 0x40000\nMaaa 32,164,204\nRa 132,11,0\nMaaa 32,160,200\nMaaa 64,160,208\nRa 136,10,2048\nMaaa 64,164,212\nD0 6,24,44,0\n"
"Maaa 96,164,220\nRa 140,11,2048\nMaaa 96,160,216\nMaaa 100,168,216\nRa 144,10,8192\nMaaa 100,172,220\ns_add_u32 s6, s53, 0\nSv 224,1,16,6,1024\nMaaa 68,172,212\nRa 148,11,8192\n"
"Maaa 68,168,208\nMaaa 36,168,200\nRa 152,10,10240\nMaaa 36,172,204\nD0 7,24,44,1024\nMaaa 4,172,196\nRa 156,11,10240\nMaaa 4,168,192\nMaaa 8,176,192\nMaaa 8,180,196\n"
"Maaa 40,180,204\nMaaa 40,176,200\nMaaa 72,176,208\nMaaa 72,180,212\nD0 8,24,44,2048\nMaaa 104,180,220\nMaaa 104,176,216\nMaaa 108,184,216\nMaaa 108,188,220\ns_add_u32 s6, s53, 0\n"
"Sv 232,1,16,6,3072\nMaaa 76,188,212\nMaaa 76,184,208\nMaaa 44,184,200\nMaaa 44,188,204\nD0 9,24,44,3072\nMaaa 12,188,196\nMaaa 12,184,192\ns_waitcnt vmcnt(24) lgkmcnt(0)\n"
"s_barrier\nMaaa 16,224,192\nTa 160,12,0\nMaaa 16,228,196\nTa 162,12,2048\nMaaa 48,228,204\ns_add_u32 m0, s47, -16384\ns_add_u32 s48, s48, 128\nMaaa 48,224,200\nTa 164,12,16384\n"
"Maaa 80,224,208\nTa 166,12,18432\nMaaa 80,228,212\nD0 2,20,48,0\nMaaa 112,228,220\nTa 168,13,0\nMaaa 112,224,216\nTa 170,13,2048\nMaaa 116,232,216\nMaaa 116,236,220\n"
"Ta 172,13,16384\ns_add_u32 s6, s53, 4096\nSv 240,1,16,6,1024\nMaaa 84,236,212\nTa 174,13,18432\nMaaa 84,232,208\nMaaa 52,232,200\nTa 176,14,0\nMaaa 52,236,204\nTa 178,14,2048\n"
"Maaa 20,236,196\nD0 3,20,48,1024\nMaaa 20,232,192\nTa 180,14,16384\nMaaa 24,240,192\nTa 182,14,18432\nMaaa 24,244,196\ns_add_u32 s6, s53, 4096\nSv 248,1,16,6,3072\n"
"Maaa 56,244,204\nTa 184,15,0\nMaaa 56,240,200\nTa 186,15,2048\nMaaa 88,240,208\nMaaa 88,244,212\nTa 188,15,16384\nMaaa 120,244,220\nTa 190,15,18432\nMaaa 120,240,216\n"
"D0 4,20,48,2048\nMaaa 124,248,216\nMaaa 124,252,220\nMaaa 92,252,212\nMaaa 92,248,208\ns_lshl3_add_u32 s90, s12, s13\ns_lshl2_add_u32 s90, s90, s11\n"
"s_lshl2_add_u32 s90, s90, 0xc04a400\nMaaa 60,248,200\nMaaa 60,252,204\nD0 5,20,48,3072\nMaaa 28,252,196\nMaaa 28,248,192\nWL 0\ns_mov_b64 s[76:77], s[62:63]\n"
"s_mov_b64 s[78:79], s[64:65]\ns_mov_b32 s42, 1605632\ns_mov_b32 s50, 1\n.GL20:\nMvaa 64,160,128\nTa 224,16,0\nMvaa 64,164,132\nTa 226,16,2048\nMvaa 96,164,140\n"
"s_add_u32 m0, s47, -65536\ns_add_u32 s45, s45, 128\nMvaa 96,160,136\nTa 228,16,16384\nMvaa 128,160,144\nTa 230,16,18432\nMvaa 128,164,148\nD0 2,20,45,0\nMvaa 160,164,156\n"
"Ta 232,17,0\nMvaa 160,160,152\nTa 234,17,2048\nMvaa 164,168,152\nMvaa 164,172,156\nTa 236,17,16384\nMvaa 132,172,148\nTa 238,17,18432\nMvaa 132,168,144\nMvaa 100,168,136\n"
"Ta 240,18,0\nMvaa 100,172,140\nTa 242,18,2048\nMvaa 68,172,132\nD0 3,20,45,1024\nMvaa 68,168,128\nTa 244,18,16384\nMvaa 72,176,128\nTa 246,18,18432\nMvaa 72,180,132\n"
"Mvaa 104,180,140\nTa 248,19,0\nMvaa 104,176,136\nTa 250,19,2048\nMvaa 136,176,144\nMvaa 136,180,148\nTa 252,19,16384\nMvaa 168,180,156\nTa 254,19,18432\nMvaa 168,176,152\n"
"D0 4,20,45,2048\nMvaa 172,184,152\nMvaa 172,188,156\nMvaa 140,188,148\nMvaa 140,184,144\ns_mov_b32 s40, s76\ns_and_b32 s41, s77, 0xffff\nMvaa 108,184,136\nMvaa 108,188,140\n"
"D0 5,20,45,3072\nMvaa 76,188,132\nMvaa 76,184,128\ns_waitcnt vmcnt(17) lgkmcnt(0)\ns_barrier\nMvaa 80,224,128\nRa 192,10,16384\nMvaa 80,228,132\ns_add_u32 m0, s47, 0\n"
"s_add_u32 s44, s44, 0x40000\nbuffer_load_dword v31, v26, s[40:43], s97 offen\ns_mov_b64 s[76:77], s[66:67]\nMvaa 112,228,140\nRa 196,11,16384\nMvaa 112,224,136\nMvaa 144,224,144\n"
"Ra 200,10,18432\nMvaa 144,228,148\nD0 6,24,44,0\nMvaa 176,228,156\nRa 204,11,18432\nMvaa 176,224,152\nMvaa 180,232,152\nRa 208,10,24576\nMvaa 180,236,156\nMvaa 148,236,148\n"
"Ra 212,11,24576\nMvaa 148,232,144\nMvaa 116,232,136\nRa 216,10,26624\nMvaa 116,236,140\nD0 7,24,44,1024\nMvaa 84,236,132\nRa 220,11,26624\nMvaa 84,232,128\nMvaa 88,240,128\n"
"Mvaa 88,244,132\nMvaa 120,244,140\nMvaa 120,240,136\nMvaa 152,240,144\nMvaa 152,244,148\nD0 8,24,44,2048\nMvaa 184,244,156\nMvaa 184,240,152\nMvaa 188,248,152\nMvaa 188,252,156\n"
"Mvaa 156,252,148\nMvaa 156,248,144\nMvaa 124,248,136\nMvaa 124,252,140\nD0 9,24,44,3072\nMvaa 92,252,132\nMvaa 92,248,128\nWL 0\nMaaa 16,224,192\nRa 128,10,32768\nMaaa 16,228,196\n"
"s_add_u32 m0, s47, 16384\ns_add_u32 s44, s44, 0x40000\nMaaa 48,228,204\nRa 132,11,32768\nMaaa 48,224,200\nMaaa 80,224,208\nRa 136,10,34816\nMaaa 80,228,212\nD0 6,24,44,0\n"
"Maaa 112,228,220\nRa 140,11,34816\nMaaa 112,224,216\nMaaa 116,232,216\nRa 144,10,40960\nMaaa 116,236,220\nMaaa 84,236,212\nRa 148,11,40960\nMaaa 84,232,208\nMaaa 52,232,200\n"
"Ra 152,10,43008\nMaaa 52,236,204\nD0 7,24,44,1024\nMaaa 20,236,196\nRa 156,11,43008\nMaaa 20,232,192\nMaaa 24,240,192\nMaaa 24,244,196\nMaaa 56,244,204\nMaaa 56,240,200\n"
"Maaa 88,240,208\nMaaa 88,244,212\nD0 8,24,44,2048\nMaaa 120,244,220\nMaaa 120,240,216\nMaaa 124,248,216\nMaaa 124,252,220\nMaaa 92,252,212\nMaaa 92,248,208\nMaaa 60,248,200\n"
"Maaa 60,252,204\nD0 9,24,44,3072\nMaaa 28,252,196\nMaaa 28,248,192\ns_waitcnt vmcnt(17) lgkmcnt(0)\ns_barrier\nMaaa 0,160,192\nTa 224,16,32768\nMaaa 0,164,196\nTa 226,16,34816\n"
"Maaa 32,164,204\ns_add_u32 m0, s47, -49152\ns_add_u32 s48, s48, 128\nMaaa 32,160,200\nTa 228,16,49152\nMaaa 64,160,208\nTa 230,16,51200\nMaaa 64,164,212\nD0 2,20,48,0\n"
"Maaa 96,164,220\nTa 232,17,32768\nMaaa 96,160,216\nTa 234,17,34816\nMaaa 100,168,216\nMaaa 100,172,220\nTa 236,17,49152\nMaaa 68,172,212\nTa 238,17,51200\nMaaa 68,168,208\n"
"Maaa 36,168,200\nTa 240,18,32768\nMaaa 36,172,204\nTa 242,18,34816\nMaaa 4,172,196\nD0 3,20,48,1024\nMaaa 4,168,192\nTa 244,18,49152\nMaaa 8,176,192\nTa 246,18,51200\n"
"Maaa 8,180,196\nMaaa 40,180,204\nTa 248,19,32768\nMaaa 40,176,200\nTa 250,19,34816\nMaaa 72,176,208\nMaaa 72,180,212\nTa 252,19,49152\nMaaa 104,180,220\nTa 254,19,51200\n"
"Maaa 104,176,216\nD0 4,20,48,2048\nMaaa 108,184,216\nMaaa 108,188,220\nMaaa 76,188,212\nMaaa 76,184,208\nMaaa 44,184,200\nMaaa 44,188,204\nD0 5,20,48,3072\nMaaa 12,188,196\n"
"Maaa 12,184,192\nWL 0\nMvaa 80,224,128\nTa 160,12,32768\nMvaa 80,228,132\nTa 162,12,34816\nMvaa 112,228,140\ns_add_u32 m0, s47, -32768\ns_add_u32 s45, s45, 128\nMvaa 112,224,136\n"
"Ta 164,12,49152\nMvaa 144,224,144\nTa 166,12,51200\nMvaa 144,228,148\nD0 2,20,45,0\nMvaa 176,228,156\nTa 168,13,32768\nMvaa 176,224,152\nTa 170,13,34816\nMvaa 180,232,152\n"
"Mvaa 180,236,156\nTa 172,13,49152\nMvaa 148,236,148\nTa 174,13,51200\nMvaa 148,232,144\nMvaa 116,232,136\nTa 176,14,32768\nMvaa 116,236,140\nTa 178,14,34816\nMvaa 84,236,132\n"
"D0 3,20,45,1024\nMvaa 84,232,128\nTa 180,14,49152\nMvaa 88,240,128\nTa 182,14,51200\nMvaa 88,244,132\nMvaa 120,244,140\nTa 184,15,32768\nMvaa 120,240,136\nTa 186,15,34816\n"
"Mvaa 152,240,144\nMvaa 152,244,148\nTa 188,15,49152\nMvaa 184,244,156\nTa 190,15,51200\nMvaa 184,240,152\nD0 4,20,45,2048\nMvaa 188,248,152\nMvaa 188,252,156\nMvaa 156,252,148\n"
"Mvaa 156,248,144\ns_mov_b32 s40, s78\ns_and_b32 s41, s79, 0xffff\nMvaa 124,248,136\nMvaa 124,252,140\nD0 5,20,45,3072\nMvaa 92,252,132\nMvaa 92,248,128\n"
"s_waitcnt vmcnt(17) lgkmcnt(0)\ns_barrier\nMvaa 64,160,128\nRa 192,10,49152\nMvaa 64,164,132\ns_add_u32 m0, s47, 32768\ns_add_u32 s44, s44, 0x40000\n"
"buffer_load_dword v31, v26, s[40:43], s97 offen\ns_mov_b64 s[78:79], s[66:67]\nMvaa 96,164,140\nRa 196,11,49152\nMvaa 96,160,136\nMvaa 128,160,144\nRa 200,10,51200\n"
"Mvaa 128,164,148\nD0 6,24,44,0\nMvaa 160,164,156\nRa 204,11,51200\nMvaa 160,160,152\nMvaa 164,168,152\nRa 208,10,57344\nMvaa 164,172,156\nMvaa 132,172,148\nRa 212,11,57344\n"
"Mvaa 132,168,144\nMvaa 100,168,136\nRa 216,10,59392\nMvaa 100,172,140\nD0 7,24,44,1024\nMvaa 68,172,132\nRa 220,11,59392\nMvaa 68,168,128\nMvaa 72,176,128\nMvaa 72,180,132\n"
"Mvaa 104,180,140\nMvaa 104,176,136\nMvaa 136,176,144\nMvaa 136,180,148\nD0 8,24,44,2048\nMvaa 168,180,156\nMvaa 168,176,152\nMvaa 172,184,152\nMvaa 172,188,156\nMvaa 140,188,148\n"
"Mvaa 140,184,144\nMvaa 108,184,136\nMvaa 108,188,140\nD0 9,24,44,3072\nMvaa 76,188,132\nMvaa 76,184,128\nWL 0\nMaaa 0,160,192\nRa 128,10,0\nMaaa 0,164,196\n"
"s_add_u32 m0, s47, 49152\ns_add_u32 s44, s44, 0x40000\nMaaa 32,164,204\nRa 132,11,0\nMaaa 32,160,200\nMaaa 64,160,208\nRa 136,10,2048\nMaaa 64,164,212\nD0 6,24,44,0\n"
"Maaa 96,164,220\nRa 140,11,2048\nMaaa 96,160,216\nMaaa 100,168,216\nRa 144,10,8192\nMaaa 100,172,220\nMaaa 68,172,212\nRa 148,11,8192\nMaaa 68,168,208\nMaaa 36,168,200\n"
"Ra 152,10,10240\nMaaa 36,172,204\nD0 7,24,44,1024\nMaaa 4,172,196\nRa 156,11,10240\nMaaa 4,168,192\nMaaa 8,176,192\nMaaa 8,180,196\nMaaa 40,180,204\nMaaa 40,176,200\n"
"Maaa 72,176,208\nMaaa 72,180,212\nD0 8,24,44,2048\nMaaa 104,180,220\nMaaa 104,176,216\nMaaa 108,184,216\nMaaa 108,188,220\nMaaa 76,188,212\nMaaa 76,184,208\nMaaa 44,184,200\n"
"Maaa 44,188,204\nD0 9,24,44,3072\nMaaa 12,188,196\nMaaa 12,184,192\ns_waitcnt vmcnt(17) lgkmcnt(0)\ns_barrier\nMaaa 16,224,192\nTa 160,12,0\nMaaa 16,228,196\nTa 162,12,2048\n"
"Maaa 48,228,204\ns_add_u32 m0, s47, -16384\ns_add_u32 s48, s48, 128\nMaaa 48,224,200\nTa 164,12,16384\nMaaa 80,224,208\nTa 166,12,18432\nMaaa 80,228,212\nD0 2,20,48,0\n"
"Maaa 112,228,220\nTa 168,13,0\nMaaa 112,224,216\nTa 170,13,2048\nMaaa 116,232,216\nMaaa 116,236,220\nTa 172,13,16384\nMaaa 84,236,212\nTa 174,13,18432\nMaaa 84,232,208\n"
"Maaa 52,232,200\nTa 176,14,0\nMaaa 52,236,204\nTa 178,14,2048\n.W542:\ns_cbranch_execz .W546\nMaaa 20,236,196\nD0 3,20,48,1024\nMaaa 20,232,192\nTa 180,14,16384\nMaaa 24,240,192\n"
"Ta 182,14,18432\nMaaa 24,244,196\nMaaa 56,244,204\nTa 184,15,0\nMaaa 56,240,200\nTa 186,15,2048\nMaaa 88,240,208\nMaaa 88,244,212\nTa 188,15,16384\nMaaa 120,244,220\n"
"Ta 190,15,18432\nMaaa 120,240,216\nD0 4,20,48,2048\nMaaa 124,248,216\nMaaa 124,252,220\nMaaa 92,252,212\nMaaa 92,248,208\nMaaa 60,248,200\nMaaa 60,252,204\nD0 5,20,48,3072\n"
"s_sub_u32 s50, s50, 1\nMaaa 28,252,196\nMaaa 28,248,192\nWL 0\ns_cbranch_scc0 .GL20\nMvaa 64,160,128\nTa 224,16,0\nMvaa 64,164,132\nTa 226,16,2048\nMvaa 96,164,140\n"
"s_add_u32 m0, s47, -65536\ns_add_u32 s45, s45, 128\nMvaa 96,160,136\nTa 228,16,16384\nMvaa 128,160,144\nTa 230,16,18432\nMvaa 128,164,148\nD0 2,20,45,0\nMvaa 160,164,156\n"
"Ta 232,17,0\nMvaa 160,160,152\nTa 234,17,2048\nMvaa 164,168,152\ns_and_b32 s2, s74, 15\nMvaa 164,172,156\nTa 236,17,16384\n"
"buffer_store_dword v25, off, s[16:19], s90 offset:0 sc0 sc1\nMvaa 132,172,148\nTa 238,17,18432\nMvaa 132,168,144\ns_sub_u32 s2, s2, 2\ns_cmp_lt_u32 s2, 14\n"
"s_cselect_b32 s5, 0, 0x40000000\nMvaa 100,168,136\nTa 240,18,0\nMvaa 100,172,140\nTa 242,18,2048\nMvaa 68,172,132\nD0 3,20,45,1024\nMvaa 68,168,128\nTa 244,18,16384\n"
"Mvaa 72,176,128\nTa 246,18,18432\nMvaa 72,180,132\ns_and_b32 s2, s2, 15\nMvaa 104,180,140\nTa 248,19,0\nMvaa 104,176,136\nTa 250,19,2048\nMvaa 136,176,144\ns_mul_i32 s4, s73, 14\n"
"Mvaa 136,180,148\nTa 252,19,16384\nMvaa 168,180,156\nTa 254,19,18432\nMvaa 168,176,152\nD0 4,20,45,2048\nMvaa 172,184,152\ns_add_u32 s4, s4, s2\nMvaa 172,188,156\n"
"s_mov_b32 s36, s62\nMvaa 140,188,148\ns_and_b32 s37, s63, 0xffff\nMvaa 140,184,144\ns_mov_b32 s38, 0x188000\nMvaa 108,184,136\ns_mov_b32 s39, 0x20000\nMvaa 108,188,140\n"
"D0 5,20,45,3072\nMvaa 76,188,132\ns_mov_b32 s40, s64\nMvaa 76,184,128\ns_waitcnt vmcnt(18) lgkmcnt(0)\ns_barrier\nMvaa 80,224,128\nRa 192,10,16384\nMvaa 80,228,132\n"
"s_add_u32 m0, s47, 0\ns_add_u32 s44, s44, 0x40000\nMvaa 112,228,140\nRa 196,11,16384\nMvaa 112,224,136\ns_and_b32 s41, s65, 0xffff\nMvaa 144,224,144\nRa 200,10,18432\n"
"Mvaa 144,228,148\nD0 6,24,44,0\nMvaa 176,228,156\nRa 204,11,18432\nMvaa 176,224,152\ns_mov_b32 s42, 0x188000\nMvaa 180,232,152\nRa 208,10,24576\nMvaa 180,236,156\n"
"s_mov_b32 s43, 0x20000\nMvaa 148,236,148\nRa 212,11,24576\nMvaa 148,232,144\ns_mov_b32 s92, s66\nMvaa 116,232,136\nRa 216,10,26624\nMvaa 116,236,140\nD0 7,24,44,1024\n"
"Mvaa 84,236,132\nRa 220,11,26624\nMvaa 84,232,128\ns_and_b32 s93, s67, 0xffff\nMvaa 88,240,128\ns_mov_b32 s94, 0x188000\nMvaa 88,244,132\ns_mov_b32 s95, 0x20000\nMvaa 120,244,140\n"
"v_lshrrev_b32 v252, 1, v0\nMvaa 120,240,136\nv_mul_u32_u24 v253, 14336, v252\nMvaa 152,240,144\nv_and_b32 v254, 1, v0\nMvaa 152,244,148\nD0 8,24,44,2048\nMvaa 184,244,156\n"
"v_lshl_add_u32 v253, v254, 4, v253\nMvaa 184,240,152\nv_mov_b32 v254, 0x40000000\nv_cmp_gt_u32 vcc, 56, v0\nv_lshlrev_b32 v252, 3, v252\ns_mul_i32 s2, s11, 401408\n"
"v_cndmask_b32 v21, v254, v253, vcc\nv_cndmask_b32 v255, v254, v252, vcc\nMvaa 188,248,152\ns_lshl_b32 s3, s4, 5\nMvaa 188,252,156\ns_add_u32 s2, s2, s3\nMvaa 156,252,148\n"
"s_or_b32 s86, s2, s5\nMvaa 156,248,144\ns_add_u32 s87, s86, 7168\nMvaa 124,248,136\nv_lshrrev_b32 v251, 4, v0\nMvaa 124,252,140\nD0 9,24,44,3072\nMvaa 92,252,132\n"
"v_mul_u32_u24 v252, 3584, v251\nMvaa 92,248,128\nWL 0\nMaaa 16,224,192\nRa 128,10,32768\nMaaa 16,228,196\ns_add_u32 m0, s47, 16384\ns_add_u32 s44, s44, 0x40000\nMaaa 48,228,204\n"
"Ra 132,11,32768\nMaaa 48,224,200\nv_and_b32 v251, 15, v0\nMaaa 80,224,208\nRa 136,10,34816\nMaaa 80,228,212\nD0 6,24,44,0\nMaaa 112,228,220\nRa 140,11,34816\nMaaa 112,224,216\n"
"v_lshl_add_u32 v252, v251, 4, v252\nv_cmp_gt_u32 vcc, 14, v251\ns_mul_i32 s2, s4, 14336\ns_mul_i32 s3, s11, 224\nv_cndmask_b32 v20, v254, v252, vcc\nMaaa 116,232,216\n"
"Ra 144,10,40960\nMaaa 116,236,220\ns_add_u32 s2, s2, s3\nMaaa 84,236,212\nRa 148,11,40960\nMaaa 84,232,208\ns_or_b32 s88, s2, s5\nMaaa 52,232,200\nRa 152,10,43008\n"
"Maaa 52,236,204\nD0 7,24,44,1024\nMaaa 20,236,196\nRa 156,11,43008\nMaaa 20,232,192\nMaaa 24,240,192\nMaaa 24,244,196\nMaaa 56,244,204\nMaaa 56,240,200\nMaaa 88,240,208\n"
"Maaa 88,244,212\nD0 8,24,44,2048\nMaaa 120,244,220\nMaaa 120,240,216\nMaaa 124,248,216\nMaaa 124,252,220\nMaaa 92,252,212\nMaaa 92,248,208\nMaaa 60,248,200\nMaaa 60,252,204\n"
"D0 9,24,44,3072\nMaaa 28,252,196\nMaaa 28,248,192\ns_waitcnt vmcnt(17) lgkmcnt(0)\ns_barrier\nMaaa 0,160,192\nTa 224,16,32768\nMaaa 0,164,196\nTa 226,16,34816\n"
"buffer_load_dwordx4 v[192:195], v21, s[36:39], s86 offen offset:0\nMaaa 32,164,204\ns_add_u32 m0, s47, -49152\ns_add_u32 s48, s48, 128\nMaaa 32,160,200\nTa 228,16,49152\n"
"s_lshl_b32 s6, s11, 12\ns_add_u32 s6, s6, 0xc044000\nMaaa 64,160,208\nTa 230,16,51200\nMaaa 64,164,212\nD0 2,20,48,0\nMaaa 96,164,220\nTa 232,17,32768\nMaaa 96,160,216\n"
"Ta 234,17,34816\nMaaa 100,168,216\nbuffer_load_dwordx4 v[32:35], v1, s[16:19], s6 offen offset:0 sc0 sc1\nMaaa 100,172,220\nTa 236,17,49152\nMaaa 68,172,212\nTa 238,17,51200\n"
"Maaa 68,168,208\nMaaa 36,168,200\nTa 240,18,32768\nMaaa 36,172,204\nTa 242,18,34816\nMaaa 4,172,196\nD0 3,20,48,1024\nMaaa 4,168,192\nTa 244,18,49152\nMaaa 8,176,192\n"
"Ta 246,18,51200\nMaaa 8,180,196\nbuffer_load_dwordx4 v[36:39], v1, s[16:19], s6 offen offset:1024 sc0 sc1\nMaaa 40,180,204\nTa 248,19,32768\nMaaa 40,176,200\nTa 250,19,34816\n"
"Maaa 72,176,208\nMaaa 72,180,212\nTa 252,19,49152\nMaaa 104,180,220\nTa 254,19,51200\nMaaa 104,176,216\nD0 4,20,48,2048\nMaaa 108,184,216\n"
"buffer_load_dwordx4 v[40:43], v1, s[16:19], s6 offen offset:2048 sc0 sc1\nMaaa 108,188,220\nMaaa 76,188,212\nMaaa 76,184,208\nMaaa 44,184,200\n"
"buffer_load_dwordx4 v[44:47], v1, s[16:19], s6 offen offset:3072 sc0 sc1\nMaaa 44,188,204\nD0 5,20,48,3072\nMaaa 12,188,196\nMaaa 12,184,192\nWL 0\nMvaa 80,224,128\n"
"Ta 160,12,32768\nMvaa 80,228,132\nTa 162,12,34816\nMvaa 112,228,140\ns_add_u32 m0, s47, -32768\ns_add_u32 s45, s45, 128\nMvaa 112,224,136\nTa 164,12,49152\nMvaa 144,224,144\n"
"Ta 166,12,51200\nMvaa 144,228,148\nD0 2,20,45,0\nMvaa 176,228,156\nTa 168,13,32768\nMvaa 176,224,152\nTa 170,13,34816\nMvaa 180,232,152\nMvaa 180,236,156\nTa 172,13,49152\n"
"Mvaa 148,236,148\nTa 174,13,51200\nMvaa 148,232,144\nMvaa 116,232,136\nTa 176,14,32768\nMvaa 116,236,140\nTa 178,14,34816\nMvaa 84,236,132\nD0 3,20,45,1024\nMvaa 84,232,128\n"
"Ta 180,14,49152\nMvaa 88,240,128\nTa 182,14,51200\nMvaa 88,244,132\nMvaa 120,244,140\nTa 184,15,32768\nMvaa 120,240,136\nTa 186,15,34816\nMvaa 152,240,144\nMvaa 152,244,148\n"
"Ta 188,15,49152\nMvaa 184,244,156\nTa 190,15,51200\nMvaa 184,240,152\nD0 4,20,45,2048\nMvaa 188,248,152\nMvaa 188,252,156\nMvaa 156,252,148\nMvaa 156,248,144\nMvaa 124,248,136\n"
"Mvaa 124,252,140\nD0 5,20,45,3072\nMvaa 92,252,132\nMvaa 92,248,128\ns_waitcnt vmcnt(21) lgkmcnt(0)\ns_barrier\nMvaa 64,160,128\nRa 192,10,49152\nMvaa 64,164,132\n"
"s_add_u32 m0, s47, 32768\ns_add_u32 s44, s44, 0x40000\nbuffer_load_dwordx4 v[208:211], v21, s[40:43], s86 offen offset:0\nMvaa 96,164,140\nRa 196,11,49152\nMvaa 96,160,136\n"
"Mvaa 128,160,144\nRa 200,10,51200\nMvaa 128,164,148\nD0 6,24,44,0\nMvaa 160,164,156\nRa 204,11,51200\nMvaa 160,160,152\nMvaa 164,168,152\nRa 208,10,57344\nMvaa 164,172,156\n"
"Mvaa 132,172,148\nRa 212,11,57344\nMvaa 132,168,144\nMvaa 100,168,136\nRa 216,10,59392\nMvaa 100,172,140\nD0 7,24,44,1024\nMvaa 68,172,132\nRa 220,11,59392\nMvaa 68,168,128\n"
"Mvaa 72,176,128\nMvaa 72,180,132\nMvaa 104,180,140\nMvaa 104,176,136\nMvaa 136,176,144\nMvaa 136,180,148\nD0 8,24,44,2048\nMvaa 168,180,156\nMvaa 168,176,152\nMvaa 172,184,152\n"
"Mvaa 172,188,156\nMvaa 140,188,148\nMvaa 140,184,144\nMvaa 108,184,136\nMvaa 108,188,140\nD0 9,24,44,3072\nMvaa 76,188,132\nMvaa 76,184,128\nWL 0\nMaaa 0,160,192\nRa 128,10,0\n"
"Maaa 0,164,196\ns_add_u32 m0, s47, 49152\ns_add_u32 s44, s44, 0x40000\nMaaa 32,164,204\nRa 132,11,0\nMaaa 32,160,200\nMaaa 64,160,208\nRa 136,10,2048\nMaaa 64,164,212\n"
"D0 6,24,44,0\nMaaa 96,164,220\nRa 140,11,2048\nMaaa 96,160,216\nMaaa 100,168,216\nRa 144,10,8192\nMaaa 100,172,220\nMaaa 68,172,212\nRa 148,11,8192\nMaaa 68,168,208\n"
"Maaa 36,168,200\nRa 152,10,10240\nMaaa 36,172,204\nD0 7,24,44,1024\nMaaa 4,172,196\nRa 156,11,10240\nMaaa 4,168,192\nMaaa 8,176,192\nMaaa 8,180,196\nMaaa 40,180,204\n"
"Maaa 40,176,200\nMaaa 72,176,208\nMaaa 72,180,212\nD0 8,24,44,2048\nMaaa 104,180,220\nMaaa 104,176,216\nMaaa 108,184,216\nMaaa 108,188,220\nMaaa 76,188,212\nMaaa 76,184,208\n"
"Maaa 44,184,200\nMaaa 44,188,204\nD0 9,24,44,3072\nMaaa 12,188,196\nMaaa 12,184,192\ns_waitcnt vmcnt(14) lgkmcnt(0)\ns_barrier\nMaaa 16,224,192\nTa 160,12,0\n"
"v_xor_b32 v48, s15, v33\nMaaa 16,228,196\nTa 162,12,2048\nbuffer_load_dwordx4 v[196:199], v21, s[36:39], s86 offen offset:3584\nMaaa 48,228,204\ns_add_u32 m0, s47, -16384\n"
"s_add_u32 s48, s48, 128\nMaaa 48,224,200\nTa 164,12,16384\nMaaa 80,224,208\nTa 166,12,18432\nMaaa 80,228,212\nD0 2,20,48,0\nMaaa 112,228,220\nTa 168,13,0\nMaaa 112,224,216\n"
"Ta 170,13,2048\nMaaa 116,232,216\nv_xor_b32 v49, s15, v35\nMaaa 116,236,220\nTa 172,13,16384\nMaaa 84,236,212\nTa 174,13,18432\nMaaa 84,232,208\nv_xor_b32 v50, s15, v37\n"
"Maaa 52,232,200\nTa 176,14,0\nMaaa 52,236,204\nTa 178,14,2048\nMaaa 20,236,196\nD0 3,20,48,1024\nMaaa 20,232,192\nTa 180,14,16384\nMaaa 24,240,192\nTa 182,14,18432\n"
"v_xor_b32 v51, s15, v39\nMaaa 24,244,196\nv_xor_b32 v52, s15, v41\nMaaa 56,244,204\nTa 184,15,0\nMaaa 56,240,200\nTa 186,15,2048\nMaaa 88,240,208\nv_xor_b32 v53, s15, v43\n"
"Maaa 88,244,212\nTa 188,15,16384\nMaaa 120,244,220\nTa 190,15,18432\nMaaa 120,240,216\nD0 4,20,48,2048\nMaaa 124,248,216\nv_xor_b32 v54, s15, v45\nMaaa 124,252,220\n"
"v_xor_b32 v55, s15, v47\nMaaa 92,252,212\nv_or3_b32 v48, v48, v49, v50\nMaaa 92,248,208\nv_or3_b32 v51, v51, v52, v53\nMaaa 60,248,200\nv_or3_b32 v48, v48, v54, v55\n"
"Maaa 60,252,204\nD0 5,20,48,3072\nMaaa 28,252,196\nv_or_b32 v48, v48, v51\nMaaa 28,248,192\nWL 0\nMvaa 64,160,128\nTa 224,16,0\nv_cmp_ne_u32 vcc, 0, v48\nMvaa 64,164,132\n"
"Ta 226,16,2048\nMvaa 96,164,140\ns_add_u32 m0, s47, -65536\ns_add_u32 s45, s45, 128\nMvaa 96,160,136\nTa 228,16,16384\nMvaa 128,160,144\nTa 230,16,18432\nMvaa 128,164,148\n"
"D0 2,20,45,0\nMvaa 160,164,156\nTa 232,17,0\nMvaa 160,160,152\nTa 234,17,2048\nMvaa 164,168,152\ns_cbranch_vccnz .S14\n.S15:\nMvaa 164,172,156\nTa 236,17,16384\nMvaa 132,172,148\n"
"Ta 238,17,18432\nMvaa 132,168,144\nv_max_u32 v48, v32, v34\nMvaa 100,168,136\nTa 240,18,0\nMvaa 100,172,140\nTa 242,18,2048\nMvaa 68,172,132\nD0 3,20,45,1024\n.W543:\n"
"s_cbranch_execz .W547\nMvaa 68,168,128\nTa 244,18,16384\nMvaa 72,176,128\nTa 246,18,18432\nv_min_u32 v49, v32, v34\nMvaa 72,180,132\nv_med3_u32 v49, v48, v49, v36\n"
"Mvaa 104,180,140\nTa 248,19,0\nMvaa 104,176,136\nTa 250,19,2048\nMvaa 136,176,144\nv_max_u32 v48, v48, v36\nMvaa 136,180,148\nTa 252,19,16384\nMvaa 168,180,156\nTa 254,19,18432\n"
"Mvaa 168,176,152\nD0 4,20,45,2048\nMvaa 172,184,152\nv_med3_u32 v49, v48, v49, v38\nMvaa 172,188,156\nv_max_u32 v48, v48, v38\nMvaa 140,188,148\nv_med3_u32 v49, v48, v49, v40\n"
"Mvaa 140,184,144\nv_max_u32 v48, v48, v40\nMvaa 108,184,136\nv_med3_u32 v49, v48, v49, v42\nMvaa 108,188,140\nD0 5,20,45,3072\nMvaa 76,188,132\nv_max_u32 v48, v48, v42\n"
"Mvaa 76,184,128\ns_waitcnt vmcnt(18) lgkmcnt(0)\ns_barrier\nMvaa 80,224,128\nRa 192,10,16384\nv_med3_u32 v49, v48, v49, v44\nMvaa 80,228,132\ns_add_u32 m0, s47, 0\n"
"s_add_u32 s44, s44, 0x40000\nbuffer_load_dwordx4 v[212:215], v21, s[40:43], s86 offen offset:3584\nMvaa 112,228,140\nRa 196,11,16384\nMvaa 112,224,136\nv_max_u32 v48, v48, v44\n"
"Mvaa 144,224,144\nRa 200,10,18432\nMvaa 144,228,148\nD0 6,24,44,0\nMvaa 176,228,156\nRa 204,11,18432\nMvaa 176,224,152\nv_med3_u32 v49, v48, v49, v46\nMvaa 180,232,152\n"
"Ra 208,10,24576\nMvaa 180,236,156\nv_max_u32 v48, v48, v46\nMvaa 148,236,148\nRa 212,11,24576\nMvaa 148,232,144\nv_mov_b32 v50, v48\nMvaa 116,232,136\nRa 216,10,26624\n"
"Mvaa 116,236,140\nD0 7,24,44,1024\nMvaa 84,236,132\nRa 220,11,26624\nMvaa 84,232,128\nv_max_u32_dpp v50, v50, v50 row_ror:8 row_mask:0xf bank_mask:0xf\nMvaa 88,240,128\n"
"Mvaa 88,244,132\nv_max_u32_dpp v50, v50, v50 row_ror:4 row_mask:0xf bank_mask:0xf\nMvaa 120,244,140\nMvaa 120,240,136\n"
"v_max_u32_dpp v50, v50, v50 row_ror:2 row_mask:0xf bank_mask:0xf\nMvaa 152,240,144\nMvaa 152,244,148\nD0 8,24,44,2048\nMvaa 184,244,156\n"
"v_max_u32_dpp v50, v50, v50 row_ror:1 row_mask:0xf bank_mask:0xf\nMvaa 184,240,152\nv_mov_b32 v52, v50\nMvaa 188,248,152\nMvaa 188,252,156\nv_permlane16_swap_b32 v50, v52\n"
"Mvaa 156,252,148\nv_max_u32 v50, v50, v52\nMvaa 156,248,144\nv_mov_b32 v52, v50\nMvaa 124,248,136\nMvaa 124,252,140\nD0 9,24,44,3072\nMvaa 92,252,132\n"
"v_permlane32_swap_b32 v50, v52\nMvaa 92,248,128\nWL 0\nMaaa 16,224,192\nRa 128,10,32768\nv_max_u32 v50, v50, v52\nMaaa 16,228,196\ns_add_u32 m0, s47, 16384\n"
"s_add_u32 s44, s44, 0x40000\nMaaa 48,228,204\nRa 132,11,32768\nv_cmp_eq_u32 vcc, v48, v50\ns_lshl_b32 s2, s11, 3\ns_add_u32 s2, s2, 131072\nv_cndmask_b32 v51, v48, v49, vcc\n"
"Maaa 48,224,200\nMaaa 80,224,208\nRa 136,10,34816\nMaaa 80,228,212\nD0 6,24,44,0\nMaaa 112,228,220\nRa 140,11,34816\nMaaa 112,224,216\n"
"v_max_u32_dpp v51, v51, v51 row_ror:8 row_mask:0xf bank_mask:0xf\nMaaa 116,232,216\nRa 144,10,40960\nMaaa 116,236,220\n"
"v_max_u32_dpp v51, v51, v51 row_ror:4 row_mask:0xf bank_mask:0xf\nMaaa 84,236,212\nRa 148,11,40960\nMaaa 84,232,208\n"
"v_max_u32_dpp v51, v51, v51 row_ror:2 row_mask:0xf bank_mask:0xf\nMaaa 52,232,200\nRa 152,10,43008\nMaaa 52,236,204\nD0 7,24,44,1024\nMaaa 20,236,196\nRa 156,11,43008\n"
"Maaa 20,232,192\nv_max_u32_dpp v51, v51, v51 row_ror:1 row_mask:0xf bank_mask:0xf\nMaaa 24,240,192\nv_mov_b32 v52, v51\nMaaa 24,244,196\nMaaa 56,244,204\n"
"v_permlane16_swap_b32 v51, v52\nMaaa 56,240,200\nv_max_u32 v51, v51, v52\nMaaa 88,240,208\nv_mov_b32 v52, v51\nMaaa 88,244,212\nD0 8,24,44,2048\nMaaa 120,244,220\n"
"v_permlane32_swap_b32 v51, v52\nMaaa 120,240,216\nv_max_u32 v51, v51, v52\nMaaa 124,248,216\nv_mov_b32 v52, s2\nMaaa 124,252,220\nds_write_b64 v52, v[50:51]\nMaaa 92,252,212\n"
"v_mov_b32 v52, 131072\nMaaa 92,248,208\nMaaa 60,248,200\nMaaa 60,252,204\nD0 9,24,44,3072\nMaaa 28,252,196\nMaaa 28,248,192\ns_waitcnt vmcnt(18) lgkmcnt(0)\ns_barrier\n"
"Maaa 0,160,192\nTa 224,16,32768\nMaaa 0,164,196\nTa 226,16,34816\nbuffer_load_dwordx4 v[200:203], v21, s[36:39], s87 offen offset:0\nMaaa 32,164,204\ns_add_u32 m0, s47, -49152\n"
"s_add_u32 s48, s48, 128\nMaaa 32,160,200\nTa 228,16,49152\nMaaa 64,160,208\nTa 230,16,51200\nMaaa 64,164,212\nD0 2,20,48,0\nMaaa 96,164,220\nTa 232,17,32768\nMaaa 96,160,216\n"
"Ta 234,17,34816\nMaaa 100,168,216\nRv 32,52,0\nMaaa 100,172,220\nTa 236,17,49152\nMaaa 68,172,212\nTa 238,17,51200\nMaaa 68,168,208\nRv 36,52,16\nMaaa 36,168,200\nTa 240,18,32768\n"
"Maaa 36,172,204\nTa 242,18,34816\nMaaa 4,172,196\nD0 3,20,48,1024\nMaaa 4,168,192\nTa 244,18,49152\nMaaa 8,176,192\nTa 246,18,51200\nMaaa 8,180,196\nMaaa 40,180,204\n"
"Ta 248,19,32768\nMaaa 40,176,200\nTa 250,19,34816\nMaaa 72,176,208\nMaaa 72,180,212\nTa 252,19,49152\nMaaa 104,180,220\nTa 254,19,51200\nMaaa 104,176,216\nD0 4,20,48,2048\n"
"Maaa 108,184,216\nWL 11\nMaaa 108,188,220\nv_max_u32 v48, v32, v33\nMaaa 76,188,212\nv_min_u32 v49, v32, v33\nMaaa 76,184,208\nv_med3_u32 v49, v48, v49, v34\nMaaa 44,184,200\n"
"v_max_u32 v48, v48, v34\nMaaa 44,188,204\nD0 5,20,48,3072\nMaaa 12,188,196\nv_med3_u32 v49, v48, v49, v35\nMaaa 12,184,192\nWL 0\nMvaa 80,224,128\nTa 160,12,32768\n"
"Mvaa 80,228,132\nTa 162,12,34816\nMvaa 112,228,140\ns_add_u32 m0, s47, -32768\ns_add_u32 s45, s45, 128\nMvaa 112,224,136\nTa 164,12,49152\nMvaa 144,224,144\nTa 166,12,51200\n"
"Mvaa 144,228,148\nD0 2,20,45,0\nMvaa 176,228,156\nTa 168,13,32768\nMvaa 176,224,152\nTa 170,13,34816\nMvaa 180,232,152\nv_max_u32 v48, v48, v35\nMvaa 180,236,156\nTa 172,13,49152\n"
"Mvaa 148,236,148\nTa 174,13,51200\nMvaa 148,232,144\nv_med3_u32 v49, v48, v49, v36\nMvaa 116,232,136\nTa 176,14,32768\nMvaa 116,236,140\nTa 178,14,34816\nMvaa 84,236,132\n"
"D0 3,20,45,1024\nMvaa 84,232,128\nTa 180,14,49152\nMvaa 88,240,128\nTa 182,14,51200\nMvaa 88,244,132\nv_max_u32 v48, v48, v36\nMvaa 120,244,140\nTa 184,15,32768\nMvaa 120,240,136\n"
"Ta 186,15,34816\nMvaa 152,240,144\nv_med3_u32 v49, v48, v49, v37\nMvaa 152,244,148\nTa 188,15,49152\nMvaa 184,244,156\nTa 190,15,51200\nMvaa 184,240,152\nD0 4,20,45,2048\n"
"Mvaa 188,248,152\nv_max_u32 v48, v48, v37\nMvaa 188,252,156\nv_med3_u32 v49, v48, v49, v38\nMvaa 156,252,148\nv_max_u32 v48, v48, v38\nMvaa 156,248,144\n"
"v_med3_u32 v49, v48, v49, v39\nMvaa 124,248,136\nv_max_u32 v48, v48, v39\nMvaa 124,252,140\nD0 5,20,45,3072\nMvaa 92,252,132\nv_readfirstlane_b32 s2, v48\nMvaa 92,248,128\n"
"s_waitcnt vmcnt(18) lgkmcnt(0)\ns_barrier\nMvaa 64,160,128\nRa 192,10,49152\nMvaa 64,164,132\ns_add_u32 m0, s47, 32768\ns_add_u32 s44, s44, 0x40000\n"
"buffer_load_dwordx4 v[216:219], v21, s[40:43], s87 offen offset:0\nMvaa 96,164,140\nRa 196,11,49152\nMvaa 96,160,136\nv_readfirstlane_b32 s3, v49\nMvaa 128,160,144\n"
"Ra 200,10,51200\nMvaa 128,164,148\nD0 6,24,44,0\nMvaa 160,164,156\nRa 204,11,51200\nMvaa 160,160,152\ns_andn2_b32 s2, 0x7ff, s2\nMvaa 164,168,152\nRa 208,10,57344\n"
"Mvaa 164,172,156\ns_andn2_b32 s3, 0x7ff, s3\nMvaa 132,172,148\nRa 212,11,57344\nMvaa 132,168,144\ns_lshl_b32 s71, s3, 16\nMvaa 100,168,136\nRa 216,10,59392\nMvaa 100,172,140\n"
"D0 7,24,44,1024\nMvaa 68,172,132\nRa 220,11,59392\nMvaa 68,168,128\ns_or_b32 s71, s71, s2\nMvaa 72,176,128\ns_mul_i32 s2, s2, 896\nMvaa 72,180,132\ns_mul_i32 s3, s3, 896\n"
"Mvaa 104,180,140\ns_mul_i32 s6, s11, 224\nMvaa 104,176,136\ns_add_u32 s2, s2, s6\nMvaa 136,176,144\ns_add_u32 s3, s3, s6\nMvaa 136,180,148\nD0 8,24,44,2048\nMvaa 168,180,156\n"
"buffer_load_dwordx2 v[240:241], v255, s[28:31], s2 offen\nMvaa 168,176,152\nMvaa 172,184,152\nMvaa 172,188,156\nMvaa 140,188,148\n"
"buffer_load_dwordx2 v[242:243], v255, s[28:31], s3 offen\ns_lshl2_add_u32 s2, s10, s11\ns_cmp_eq_u32 s2, 512\ns_cselect_b32 s2, 0, 0x40000000\ns_add_u32 s2, s2, 0xc048000\n"
"Mvaa 140,184,144\nMvaa 108,184,136\nMvaa 108,188,140\nD0 9,24,44,3072\nMvaa 76,188,132\nbuffer_store_dword v25, off, s[16:19], s2 offset:0 sc0 sc1\nMvaa 76,184,128\nWL 0\n"
"Maaa 0,160,192\nRa 128,10,0\nMaaa 0,164,196\ns_add_u32 m0, s47, 49152\ns_add_u32 s44, s44, 0x40000\nMaaa 32,164,204\nRa 132,11,0\nMaaa 32,160,200\nMaaa 64,160,208\nRa 136,10,2048\n"
"Maaa 64,164,212\nD0 6,24,44,0\nMaaa 96,164,220\nRa 140,11,2048\nMaaa 96,160,216\nMaaa 100,168,216\nRa 144,10,8192\nMaaa 100,172,220\nMaaa 68,172,212\nRa 148,11,8192\n"
"Maaa 68,168,208\nMaaa 36,168,200\nRa 152,10,10240\nMaaa 36,172,204\nD0 7,24,44,1024\nMaaa 4,172,196\nRa 156,11,10240\nMaaa 4,168,192\nMaaa 8,176,192\nMaaa 8,180,196\n"
"Maaa 40,180,204\nMaaa 40,176,200\nMaaa 72,176,208\nMaaa 72,180,212\nD0 8,24,44,2048\nMaaa 104,180,220\nMaaa 104,176,216\nMaaa 108,184,216\nMaaa 108,188,220\nMaaa 76,188,212\n"
"Maaa 76,184,208\nMaaa 44,184,200\nMaaa 44,188,204\nD0 9,24,44,3072\nMaaa 12,188,196\nMaaa 12,184,192\ns_waitcnt vmcnt(21) lgkmcnt(0)\ns_barrier\nMaaa 16,224,192\nTa 160,12,0\n"
"Maaa 16,228,196\nTa 162,12,2048\nbuffer_load_dwordx4 v[204:207], v21, s[36:39], s87 offen offset:3584\nMaaa 48,228,204\ns_add_u32 m0, s47, -16384\ns_add_u32 s48, s48, 128\n"
"s_lshl_b32 s5, s73, 8\ns_add_u32 s5, s5, 0xc04ac00\nv_lshrrev_b32 v27, 2, v1\nbuffer_load_dword v27, v27, s[16:19], s5 offen sc0 sc1\nMaaa 48,224,200\nTa 164,12,16384\n"
"Maaa 80,224,208\nTa 166,12,18432\nMaaa 80,228,212\nD0 2,20,48,0\nMaaa 112,228,220\nTa 168,13,0\nMaaa 112,224,216\nTa 170,13,2048\nMaaa 116,232,216\nMaaa 116,236,220\n"
"Ta 172,13,16384\nMaaa 84,236,212\nTa 174,13,18432\nMaaa 84,232,208\nMaaa 52,232,200\nTa 176,14,0\nMaaa 52,236,204\nTa 178,14,2048\nMaaa 20,236,196\nD0 3,20,48,1024\n"
"Maaa 20,232,192\nTa 180,14,16384\nMaaa 24,240,192\nTa 182,14,18432\nMaaa 24,244,196\nMaaa 56,244,204\nTa 184,15,0\nMaaa 56,240,200\nTa 186,15,2048\nMaaa 88,240,208\n"
"Maaa 88,244,212\nTa 188,15,16384\nMaaa 120,244,220\nTa 190,15,18432\nMaaa 120,240,216\nD0 4,20,48,2048\nMaaa 124,248,216\nMaaa 124,252,220\nMaaa 92,252,212\nMaaa 92,248,208\n"
"Maaa 60,248,200\nMaaa 60,252,204\nD0 5,20,48,3072\nMaaa 28,252,196\nMaaa 28,248,192\nWL 0\nMvaa 64,160,128\nTa 224,16,0\nMvaa 64,164,132\nTa 226,16,2048\nMvaa 96,164,140\n"
"s_add_u32 m0, s47, -65536\ns_add_u32 s45, s45, 128\nMvaa 96,160,136\nTa 228,16,16384\nMvaa 128,160,144\nTa 230,16,18432\nMvaa 128,164,148\nD0 2,20,45,0\nMvaa 160,164,156\n"
"Ta 232,17,0\nMvaa 160,160,152\nTa 234,17,2048\nMvaa 164,168,152\nMvaa 164,172,156\nTa 236,17,16384\nMvaa 132,172,148\nTa 238,17,18432\nMvaa 132,168,144\nMvaa 100,168,136\n"
"Ta 240,18,0\nMvaa 100,172,140\nTa 242,18,2048\nMvaa 68,172,132\nD0 3,20,45,1024\nMvaa 68,168,128\nTa 244,18,16384\nMvaa 72,176,128\nTa 246,18,18432\nMvaa 72,180,132\n"
"Mvaa 104,180,140\nTa 248,19,0\nMvaa 104,176,136\nTa 250,19,2048\nMvaa 136,176,144\nMvaa 136,180,148\nTa 252,19,16384\nMvaa 168,180,156\nTa 254,19,18432\nMvaa 168,176,152\n"
"D0 4,20,45,2048\nMvaa 172,184,152\nMvaa 172,188,156\nMvaa 140,188,148\nMvaa 140,184,144\nMvaa 108,184,136\nMvaa 108,188,140\nD0 5,20,45,3072\nMvaa 76,188,132\nMvaa 76,184,128\n"
"s_waitcnt vmcnt(22) lgkmcnt(0)\ns_barrier\nMvaa 80,224,128\nRa 192,10,16384\nWV 16\nMvaa 80,228,132\ns_add_u32 m0, s47, 0\ns_add_u32 s44, s44, 0x40000\n"
"buffer_load_dwordx4 v[220:223], v21, s[40:43], s87 offen offset:3584\nMvaa 112,228,140\nRa 196,11,16384\nv_lshlrev_b32 v244, 16, v240\nMvaa 112,224,136\n"
"v_and_b32 v245, 0xffff0000, v240\nv_lshlrev_b32 v246, 16, v241\nMvaa 144,224,144\nRa 200,10,18432\nv_and_b32 v247, 0xffff0000, v241\nMvaa 144,228,148\nD0 6,24,44,0\n"
"v_lshlrev_b32 v248, 16, v242\nMvaa 176,228,156\n.W544:\ns_cbranch_execz .W548\nRa 204,11,18432\nMvaa 176,224,152\nv_and_b32 v249, 0xffff0000, v242\nv_lshlrev_b32 v250, 16, v243\n"
"Mvaa 180,232,152\nRa 208,10,24576\nv_and_b32 v251, 0xffff0000, v243\nMvaa 180,236,156\nv_lshlrev_b32 v252, 16, v192\nv_and_b32 v253, 0xffff0000, v192\nMvaa 148,236,148\n"
"Ra 212,11,24576\nMU 32,244,252\nMvaa 148,232,144\nMU 33,244,253\nMvaa 116,232,136\nRa 216,10,26624\nMU 40,248,252\nMvaa 116,236,140\nD0 7,24,44,1024\nMU 41,248,253\n"
"Mvaa 84,236,132\nRa 220,11,26624\nv_lshlrev_b32 v252, 16, v193\nMvaa 84,232,128\nv_and_b32 v253, 0xffff0000, v193\nMU 34,244,252\nMvaa 88,240,128\nMU 35,244,253\nMU 42,248,252\n"
"Mvaa 88,244,132\nMU 43,248,253\nMvaa 120,244,140\nv_lshlrev_b32 v252, 16, v194\nv_and_b32 v253, 0xffff0000, v194\nMvaa 120,240,136\nMU 36,244,252\nMU 37,244,253\nMvaa 152,240,144\n"
"MU 44,248,252\nMU 45,248,253\nMvaa 152,244,148\nD0 8,24,44,2048\nv_lshlrev_b32 v252, 16, v195\nMvaa 184,244,156\nv_and_b32 v253, 0xffff0000, v195\nMvaa 184,240,152\nMU 38,244,252\n"
"MU 39,244,253\nMvaa 188,248,152\nMU 46,248,252\nMU 47,248,253\nMvaa 188,252,156\nv_lshlrev_b32 v252, 16, v208\nv_and_b32 v253, 0xffff0000, v208\nMvaa 156,252,148\nMU 48,244,252\n"
"MU 49,244,253\nMvaa 156,248,144\nMU 56,248,252\nMvaa 124,248,136\nMU 57,248,253\nv_lshlrev_b32 v252, 16, v209\nMvaa 124,252,140\nD0 9,24,44,3072\nv_and_b32 v253, 0xffff0000, v209\n"
"Mvaa 92,252,132\nMU 50,244,252\nMU 51,244,253\nMvaa 92,248,128\nWL 0\nMaaa 16,224,192\nRa 128,10,32768\nMU 58,248,252\nMaaa 16,228,196\ns_add_u32 m0, s47, 16384\n"
"s_add_u32 s44, s44, 0x40000\nMaaa 48,228,204\nRa 132,11,32768\nMU 59,248,253\nMaaa 48,224,200\nv_lshlrev_b32 v252, 16, v210\nv_and_b32 v253, 0xffff0000, v210\nMaaa 80,224,208\n"
"Ra 136,10,34816\nMU 52,244,252\nMaaa 80,228,212\nD0 6,24,44,0\nMU 53,244,253\nMaaa 112,228,220\nRa 140,11,34816\nMaaa 112,224,216\nMU 60,248,252\nMU 61,248,253\nMaaa 116,232,216\n"
"Ra 144,10,40960\nv_lshlrev_b32 v252, 16, v211\nMaaa 116,236,220\nv_and_b32 v253, 0xffff0000, v211\nMU 54,244,252\nMaaa 84,236,212\nRa 148,11,40960\nMU 55,244,253\nMaaa 84,232,208\n"
"MU 62,248,252\nMaaa 52,232,200\nRa 152,10,43008\nMU 63,248,253\nMaaa 52,236,204\nD0 7,24,44,1024\nv_lshlrev_b32 v252, 16, v196\nMaaa 20,236,196\nRa 156,11,43008\n"
"v_and_b32 v253, 0xffff0000, v196\nMaaa 20,232,192\nFM 32,245,252\nFM 33,245,253\nMaaa 24,240,192\nFM 40,249,252\nFM 41,249,253\nMaaa 24,244,196\nv_lshlrev_b32 v252, 16, v197\n"
"Maaa 56,244,204\nv_and_b32 v253, 0xffff0000, v197\nFM 34,245,252\nMaaa 56,240,200\nFM 35,245,253\nFM 42,249,252\nMaaa 88,240,208\nFM 43,249,253\nv_lshlrev_b32 v252, 16, v198\n"
"Maaa 88,244,212\nD0 8,24,44,2048\nv_and_b32 v253, 0xffff0000, v198\nMaaa 120,244,220\nFM 36,245,252\nMaaa 120,240,216\nFM 37,245,253\nFM 44,249,252\nMaaa 124,248,216\n"
"FM 45,249,253\nv_lshlrev_b32 v252, 16, v199\nMaaa 124,252,220\nv_and_b32 v253, 0xffff0000, v199\nFM 38,245,252\nMaaa 92,252,212\nFM 39,245,253\nFM 46,249,252\nMaaa 92,248,208\n"
"FM 47,249,253\nMaaa 60,248,200\nv_lshlrev_b32 v252, 16, v212\nv_and_b32 v253, 0xffff0000, v212\nMaaa 60,252,204\nD0 9,24,44,3072\nFM 48,245,252\nMaaa 28,252,196\nFM 49,245,253\n"
"FM 56,249,252\nMaaa 28,248,192\ns_waitcnt vmcnt(19) lgkmcnt(0)\ns_barrier\nMaaa 0,160,192\nTa 224,16,32768\nFM 57,249,253\nMaaa 0,164,196\nTa 226,16,34816\n"
"buffer_load_dwordx4 v[224:227], v20, s[92:95], s88 offen offset:0\nMaaa 32,164,204\ns_add_u32 m0, s47, -49152\ns_add_u32 s48, s48, 128\nMaaa 32,160,200\nTa 228,16,49152\n"
"v_lshlrev_b32 v252, 16, v213\nMaaa 64,160,208\nTa 230,16,51200\nv_and_b32 v253, 0xffff0000, v213\nMaaa 64,164,212\nD0 2,20,48,0\nFM 50,245,252\nMaaa 96,164,220\nTa 232,17,32768\n"
"Maaa 96,160,216\nTa 234,17,34816\nFM 51,245,253\nMaaa 100,168,216\nFM 58,249,252\nFM 59,249,253\nMaaa 100,172,220\nTa 236,17,49152\nv_lshlrev_b32 v252, 16, v214\nMaaa 68,172,212\n"
"Ta 238,17,51200\nv_and_b32 v253, 0xffff0000, v214\nMaaa 68,168,208\nFM 52,245,252\nMaaa 36,168,200\nTa 240,18,32768\nFM 53,245,253\nMaaa 36,172,204\nTa 242,18,34816\n"
"FM 60,249,252\nMaaa 4,172,196\nD0 3,20,48,1024\nFM 61,249,253\nMaaa 4,168,192\nTa 244,18,49152\nv_lshlrev_b32 v252, 16, v215\nMaaa 8,176,192\nTa 246,18,51200\n"
"v_and_b32 v253, 0xffff0000, v215\nMaaa 8,180,196\nFM 54,245,252\nMaaa 40,180,204\nTa 248,19,32768\nFM 55,245,253\nMaaa 40,176,200\nTa 250,19,34816\nFM 62,249,252\nMaaa 72,176,208\n"
"FM 63,249,253\nv_lshlrev_b32 v252, 16, v200\nMaaa 72,180,212\nTa 252,19,49152\nv_and_b32 v253, 0xffff0000, v200\nMaaa 104,180,220\nTa 254,19,51200\nMaaa 104,176,216\n"
"D0 4,20,48,2048\nFM 32,246,252\nMaaa 108,184,216\nFM 33,246,253\nFM 40,250,252\nMaaa 108,188,220\nFM 41,250,253\nv_lshlrev_b32 v252, 16, v201\nMaaa 76,188,212\n"
"v_and_b32 v253, 0xffff0000, v201\nFM 34,246,252\nMaaa 76,184,208\nFM 35,246,253\nMaaa 44,184,200\nFM 42,250,252\nFM 43,250,253\nMaaa 44,188,204\nD0 5,20,48,3072\n"
"v_lshlrev_b32 v252, 16, v202\nMaaa 12,188,196\nv_and_b32 v253, 0xffff0000, v202\nFM 36,246,252\nMaaa 12,184,192\nWL 0\nMvaa 80,224,128\nTa 160,12,32768\nFM 37,246,253\n"
"Mvaa 80,228,132\nTa 162,12,34816\nbuffer_load_dwordx4 v[228:231], v20, s[92:95], s88 offen offset:896\nMvaa 112,228,140\ns_add_u32 m0, s47, -32768\ns_add_u32 s45, s45, 128\n"
"Mvaa 112,224,136\nTa 164,12,49152\nFM 44,250,252\nMvaa 144,224,144\nTa 166,12,51200\nFM 45,250,253\nMvaa 144,228,148\nD0 2,20,45,0\nv_lshlrev_b32 v252, 16, v203\nMvaa 176,228,156\n"
"Ta 168,13,32768\nMvaa 176,224,152\nTa 170,13,34816\nv_and_b32 v253, 0xffff0000, v203\nMvaa 180,232,152\nFM 38,246,252\nFM 39,246,253\nMvaa 180,236,156\nTa 172,13,49152\n"
"FM 46,250,252\nMvaa 148,236,148\nTa 174,13,51200\nFM 47,250,253\nMvaa 148,232,144\nv_lshlrev_b32 v252, 16, v216\nMvaa 116,232,136\nTa 176,14,32768\n"
"v_and_b32 v253, 0xffff0000, v216\nMvaa 116,236,140\nTa 178,14,34816\nFM 48,246,252\nMvaa 84,236,132\nD0 3,20,45,1024\nFM 49,246,253\nMvaa 84,232,128\nTa 180,14,49152\n"
"FM 56,250,252\nMvaa 88,240,128\nTa 182,14,51200\nFM 57,250,253\nMvaa 88,244,132\nv_lshlrev_b32 v252, 16, v217\nMvaa 120,244,140\nTa 184,15,32768\nv_and_b32 v253, 0xffff0000, v217\n"
"Mvaa 120,240,136\nTa 186,15,34816\nFM 50,246,252\nMvaa 152,240,144\nFM 51,246,253\nFM 58,250,252\nMvaa 152,244,148\nTa 188,15,49152\nFM 59,250,253\nMvaa 184,244,156\n"
"Ta 190,15,51200\nMvaa 184,240,152\nD0 4,20,45,2048\nv_lshlrev_b32 v252, 16, v218\nMvaa 188,248,152\nv_and_b32 v253, 0xffff0000, v218\nFM 52,246,252\nMvaa 188,252,156\n"
"FM 53,246,253\nFM 60,250,252\nMvaa 156,252,148\nFM 61,250,253\nv_lshlrev_b32 v252, 16, v219\nMvaa 156,248,144\nv_and_b32 v253, 0xffff0000, v219\nMvaa 124,248,136\nFM 54,246,252\n"
"WV 26\nv_xor_b32 v27, s15, v27\nv_and_b32 v255, 0xffffdfff, v27\nv_cmp_ne_u32 vcc, 0, v255\ns_nop 0\ns_cbranch_vccnz .S17\n.S18:\nv_cmp_ne_u32 vcc, 0, v27\ns_nop 3\n"
"s_bcnt1_i32_b64 s4, vcc\ns_cmp_gt_u32 s4, 32\ns_cselect_b32 s4, 1, 0\nMvaa 124,252,140\nD0 5,20,45,3072\ns_xor_b32 s4, s4, s12\nMvaa 92,252,132\ns_and_b32 s4, s4, 1\n"
"s_lshl_b32 s4, s4, 13\nMvaa 92,248,128\ns_waitcnt vmcnt(18) lgkmcnt(0)\ns_barrier\nMvaa 64,160,128\nRa 192,10,49152\ns_mul_i32 s72, s73, 25165824\nMvaa 64,164,132\n"
"s_add_u32 m0, s47, 32768\ns_add_u32 s44, s44, 0x40000\nbuffer_load_dwordx4 v[232:235], v20, s[92:95], s88 offen offset:1792\nMvaa 96,164,140\nRa 196,11,49152\n"
"s_add_u32 s72, s72, s4\nMvaa 96,160,136\ns_and_b32 s4, s12, 1\ns_lshl_b32 s4, s4, 21\nMvaa 128,160,144\nRa 200,10,51200\ns_add_u32 s72, s72, s4\nMvaa 128,164,148\nD0 6,24,44,0\n"
"s_lshl3_add_u32 s2, s13, s11\nMvaa 160,164,156\nRa 204,11,51200\nMvaa 160,160,152\ns_lshl_b32 s2, s2, 11\ns_lshl_b32 s3, s14, 22\nMvaa 164,168,152\nRa 208,10,57344\n"
"s_add_u32 s2, s2, s3\nMvaa 164,172,156\ns_add_u32 s2, s2, s72\ns_add_u32 s49, s2, 0xc00000\nMvaa 132,172,148\nRa 212,11,57344\nFM 55,246,253\nMvaa 132,168,144\nFM 62,250,252\n"
"Mvaa 100,168,136\nRa 216,10,59392\nFM 63,250,253\nMvaa 100,172,140\nD0 7,24,44,1024\nv_lshlrev_b32 v252, 16, v204\nMvaa 68,172,132\nRa 220,11,59392\n"
"v_and_b32 v253, 0xffff0000, v204\nMvaa 68,168,128\nFM 32,247,252\nFM 33,247,253\nMvaa 72,176,128\nFM 40,251,252\nFM 41,251,253\nMvaa 72,180,132\nv_lshlrev_b32 v252, 16, v205\n"
"Mvaa 104,180,140\nv_and_b32 v253, 0xffff0000, v205\nFM 34,247,252\nMvaa 104,176,136\nFM 35,247,253\nFM 42,251,252\nMvaa 136,176,144\nFM 43,251,253\nv_lshlrev_b32 v252, 16, v206\n"
"Mvaa 136,180,148\nD0 8,24,44,2048\nv_and_b32 v253, 0xffff0000, v206\nMvaa 168,180,156\nFM 36,247,252\nMvaa 168,176,152\nFM 37,247,253\nFM 44,251,252\nMvaa 172,184,152\n"
"FM 45,251,253\nv_lshlrev_b32 v252, 16, v207\nMvaa 172,188,156\nv_and_b32 v253, 0xffff0000, v207\nFM 38,247,252\nMvaa 140,188,148\nFM 39,247,253\nFM 46,251,252\nMvaa 140,184,144\n"
"FM 47,251,253\nMvaa 108,184,136\nv_lshlrev_b32 v252, 16, v220\nv_and_b32 v253, 0xffff0000, v220\nMvaa 108,188,140\nD0 9,24,44,3072\nFM 48,247,252\nMvaa 76,188,132\nFM 49,247,253\n"
"FM 56,251,252\nMvaa 76,184,128\nWL 0\nMaaa 0,160,192\nRa 128,10,0\nFM 57,251,253\nMaaa 0,164,196\ns_add_u32 m0, s47, 49152\ns_add_u32 s44, s44, 0x40000\n"
"buffer_load_dwordx4 v[236:239], v20, s[92:95], s88 offen offset:2688\nMaaa 32,164,204\nRa 132,11,0\nv_lshlrev_b32 v252, 16, v221\nMaaa 32,160,200\n"
"v_and_b32 v253, 0xffff0000, v221\nFM 50,247,252\nMaaa 64,160,208\nRa 136,10,2048\nFM 51,247,253\nMaaa 64,164,212\nD0 6,24,44,0\nFM 58,251,252\nMaaa 96,164,220\nRa 140,11,2048\n"
"Maaa 96,160,216\nFM 59,251,253\nv_lshlrev_b32 v252, 16, v222\nMaaa 100,168,216\nRa 144,10,8192\nv_and_b32 v253, 0xffff0000, v222\nMaaa 100,172,220\nFM 52,247,252\nFM 53,247,253\n"
"Maaa 68,172,212\nRa 148,11,8192\nFM 60,251,252\nMaaa 68,168,208\nFM 61,251,253\nMaaa 36,168,200\nRa 152,10,10240\nv_lshlrev_b32 v252, 16, v223\nMaaa 36,172,204\nD0 7,24,44,1024\n"
"v_and_b32 v253, 0xffff0000, v223\nMaaa 4,172,196\nRa 156,11,10240\nFM 54,247,252\nMaaa 4,168,192\nFM 55,247,253\nFM 62,251,252\nMaaa 8,176,192\nFM 63,251,253\nMaaa 8,180,196\n"
"v_permlane32_swap_b32 v32, v48\nMaaa 40,180,204\nv_permlane32_swap_b32 v33, v49\nv_permlane32_swap_b32 v34, v50\nMaaa 40,176,200\nv_permlane32_swap_b32 v35, v51\n"
"v_permlane32_swap_b32 v36, v52\nMaaa 72,176,208\nv_permlane32_swap_b32 v37, v53\nv_permlane32_swap_b32 v38, v54\nMaaa 72,180,212\nD0 8,24,44,2048\nv_permlane32_swap_b32 v39, v55\n"
"Maaa 104,180,220\nv_permlane32_swap_b32 v40, v56\nMaaa 104,176,216\nv_permlane32_swap_b32 v41, v57\nv_permlane32_swap_b32 v42, v58\nMaaa 108,184,216\n"
"v_permlane32_swap_b32 v43, v59\nv_permlane32_swap_b32 v44, v60\nMaaa 108,188,220\nv_permlane32_swap_b32 v45, v61\nv_permlane32_swap_b32 v46, v62\nMaaa 76,188,212\n"
"v_permlane32_swap_b32 v47, v63\nAD 32,32,48\nMaaa 76,184,208\nAD 33,33,49\nMaaa 44,184,200\nAD 34,34,50\nAD 35,35,51\nMaaa 44,188,204\nD0 9,24,44,3072\nAD 36,36,52\n"
"Maaa 12,188,196\nAD 37,37,53\nAD 38,38,54\nMaaa 12,184,192\ns_waitcnt vmcnt(20) lgkmcnt(0)\ns_barrier\nMaaa 16,224,192\nTa 160,12,0\nAD 39,39,55\nMaaa 16,228,196\nTa 162,12,2048\n"
"Maaa 48,228,204\ns_add_u32 m0, s47, -16384\ns_add_u32 s48, s48, 128\nMaaa 48,224,200\nTa 164,12,16384\nAD 40,40,56\nMaaa 80,224,208\nTa 166,12,18432\nAD 41,41,57\nMaaa 80,228,212\n"
"D0 2,20,48,0\nAD 42,42,58\nMaaa 112,228,220\nTa 168,13,0\nMaaa 112,224,216\nTa 170,13,2048\nAD 43,43,59\nMaaa 116,232,216\nAD 44,44,60\nAD 45,45,61\nMaaa 116,236,220\n"
"Ta 172,13,16384\nAD 46,46,62\nMaaa 84,236,212\nTa 174,13,18432\nAD 47,47,63\nMaaa 84,232,208\nv_permlane16_swap_b32 v32, v40\nMaaa 52,232,200\nTa 176,14,0\n"
"v_permlane16_swap_b32 v33, v41\nMaaa 52,236,204\nTa 178,14,2048\nv_permlane16_swap_b32 v34, v42\nMaaa 20,236,196\nD0 3,20,48,1024\nv_permlane16_swap_b32 v35, v43\nMaaa 20,232,192\n"
"Ta 180,14,16384\nv_permlane16_swap_b32 v36, v44\nMaaa 24,240,192\nTa 182,14,18432\nv_permlane16_swap_b32 v37, v45\nMaaa 24,244,196\nv_permlane16_swap_b32 v38, v46\n"
"Maaa 56,244,204\nTa 184,15,0\nv_permlane16_swap_b32 v39, v47\nMaaa 56,240,200\nTa 186,15,2048\nAD 32,32,40\nMaaa 88,240,208\nAD 33,33,41\nAD 34,34,42\nMaaa 88,244,212\n"
"Ta 188,15,16384\nAD 35,35,43\nMaaa 120,244,220\nTa 190,15,18432\nMaaa 120,240,216\nD0 4,20,48,2048\nAD 36,36,44\nMaaa 124,248,216\nAD 37,37,45\nAD 38,38,46\nMaaa 124,252,220\n"
"AD 39,39,47\nv_add_f32_dpp v32, v32, v32 row_ror:8 row_mask:0xf bank_mask:0xf\nMaaa 92,252,212\nv_add_f32_dpp v33, v33, v33 row_ror:8 row_mask:0xf bank_mask:0xf\n"
"v_add_f32_dpp v34, v34, v34 row_ror:8 row_mask:0xf bank_mask:0xf\nMaaa 92,248,208\nv_add_f32_dpp v35, v35, v35 row_ror:8 row_mask:0xf bank_mask:0xf\nMaaa 60,248,200\n"
"v_add_f32_dpp v36, v36, v36 row_ror:8 row_mask:0xf bank_mask:0xf\nv_add_f32_dpp v37, v37, v37 row_ror:8 row_mask:0xf bank_mask:0xf\nMaaa 60,252,204\nD0 5,20,48,3072\n"
"v_add_f32_dpp v38, v38, v38 row_ror:8 row_mask:0xf bank_mask:0xf\nMaaa 28,252,196\nv_add_f32_dpp v39, v39, v39 row_ror:8 row_mask:0xf bank_mask:0xf\n"
"v_add_f32_dpp v32, v32, v32 row_ror:4 row_mask:0xf bank_mask:0xf\nMaaa 28,248,192\nWL 0\nMvaa 64,160,128\nTa 224,16,0\n"
"v_add_f32_dpp v33, v33, v33 row_ror:4 row_mask:0xf bank_mask:0xf\nMvaa 64,164,132\nTa 226,16,2048\nMvaa 96,164,140\ns_add_u32 m0, s47, -65536\ns_add_u32 s45, s45, 128\n.W545:\n"
"s_cbranch_execz .W549\nMvaa 96,160,136\nTa 228,16,16384\nv_add_f32_dpp v34, v34, v34 row_ror:4 row_mask:0xf bank_mask:0xf\nMvaa 128,160,144\nTa 230,16,18432\n"
"v_add_f32_dpp v35, v35, v35 row_ror:4 row_mask:0xf bank_mask:0xf\nMvaa 128,164,148\nD0 2,20,45,0\nv_add_f32_dpp v36, v36, v36 row_ror:4 row_mask:0xf bank_mask:0xf\n"
"Mvaa 160,164,156\nTa 232,17,0\nMvaa 160,160,152\nTa 234,17,2048\nv_add_f32_dpp v37, v37, v37 row_ror:4 row_mask:0xf bank_mask:0xf\nMvaa 164,168,152\n"
"v_add_f32_dpp v38, v38, v38 row_ror:4 row_mask:0xf bank_mask:0xf\nv_add_f32_dpp v39, v39, v39 row_ror:4 row_mask:0xf bank_mask:0xf\nMvaa 164,172,156\nTa 236,17,16384\n"
"v_add_f32_dpp v32, v32, v32 row_ror:2 row_mask:0xf bank_mask:0xf\nMvaa 132,172,148\nTa 238,17,18432\nv_add_f32_dpp v33, v33, v33 row_ror:2 row_mask:0xf bank_mask:0xf\n"
"Mvaa 132,168,144\nv_add_f32_dpp v34, v34, v34 row_ror:2 row_mask:0xf bank_mask:0xf\nMvaa 100,168,136\nTa 240,18,0\n"
"v_add_f32_dpp v35, v35, v35 row_ror:2 row_mask:0xf bank_mask:0xf\nMvaa 100,172,140\nTa 242,18,2048\nv_add_f32_dpp v36, v36, v36 row_ror:2 row_mask:0xf bank_mask:0xf\n"
"Mvaa 68,172,132\nD0 3,20,45,1024\nv_add_f32_dpp v37, v37, v37 row_ror:2 row_mask:0xf bank_mask:0xf\nMvaa 68,168,128\nTa 244,18,16384\n"
"v_add_f32_dpp v38, v38, v38 row_ror:2 row_mask:0xf bank_mask:0xf\nMvaa 72,176,128\nTa 246,18,18432\nv_add_f32_dpp v39, v39, v39 row_ror:2 row_mask:0xf bank_mask:0xf\n"
"Mvaa 72,180,132\nv_lshrrev_b32 v48, 4, v0\nMvaa 104,180,140\nTa 248,19,0\nv_and_b32 v49, 1, v0\nMvaa 104,176,136\nTa 250,19,2048\nv_lshlrev_b32 v49, 5, v49\nMvaa 136,176,144\n"
"v_lshl_add_u32 v49, v48, 6, v49\ns_lshl_b32 s2, s11, 8\nMvaa 136,180,148\nTa 252,19,16384\ns_add_u32 s2, s2, 132096\nMvaa 168,180,156\nTa 254,19,18432\nMvaa 168,176,152\n"
"D0 4,20,45,2048\nv_add_u32 v49, s2, v49\nMvaa 172,184,152\nds_write_b128 v49, v[32:35]\nds_write_b128 v49, v[36:39] offset:16\nMvaa 172,188,156\nv_and_b32 v50, 31, v0\n"
"v_lshlrev_b32 v50, 2, v50\nMvaa 140,188,148\ns_lshl_b32 s2, s11, 10\ns_add_u32 s2, s2, 133120\nMvaa 140,184,144\nv_add_u32 v51, s2, v50\nMvaa 108,184,136\n"
"v_add_u32 v50, 132096, v50\nv_lshl_add_u32 v52, v48, 4, s2\nMvaa 108,188,140\nD0 5,20,45,3072\nMvaa 76,188,132\nMvaa 76,184,128\ns_waitcnt vmcnt(18) lgkmcnt(0)\ns_barrier\n"
"Mvaa 80,224,128\nRa 192,10,16384\nds_read_b32 v32, v50 offset:0\nMvaa 80,228,132\ns_add_u32 m0, s47, 0\ns_add_u32 s44, s44, 0x40000\nMvaa 112,228,140\nRa 196,11,16384\n"
"Mvaa 112,224,136\nds_read_b32 v33, v50 offset:256\nds_read_b32 v34, v50 offset:512\nMvaa 144,224,144\nRa 200,10,18432\nMvaa 144,228,148\nD0 6,24,44,0\nMvaa 176,228,156\n"
"Ra 204,11,18432\nds_read_b32 v35, v50 offset:768\nMvaa 176,224,152\nds_read_b32 v36, v50 offset:128\nMvaa 180,232,152\nRa 208,10,24576\nds_read_b32 v37, v50 offset:384\n"
"Mvaa 180,236,156\nds_read_b32 v38, v50 offset:640\nMvaa 148,236,148\nRa 212,11,24576\nMvaa 148,232,144\nds_read_b32 v39, v50 offset:896\nMvaa 116,232,136\nRa 216,10,26624\n"
"Mvaa 116,236,140\nD0 7,24,44,1024\nMvaa 84,236,132\nRa 220,11,26624\nMvaa 84,232,128\nMvaa 88,240,128\nMvaa 88,244,132\nMvaa 120,244,140\nMvaa 120,240,136\nMvaa 152,240,144\n"
"Mvaa 152,244,148\nD0 8,24,44,2048\nMvaa 184,244,156\nWL 2\nAD 32,32,33\nMvaa 184,240,152\nAD 34,34,35\nMvaa 188,248,152\nAD 36,36,37\nAD 38,38,39\nMvaa 188,252,156\nAD 32,32,34\n"
"Mvaa 156,252,148\nAD 36,36,38\nMvaa 156,248,144\nCV 32,32,36\nv_and_b32 v36, 0xffff0000, v32\nMvaa 124,248,136\nv_lshlrev_b32 v32, 16, v32\nMvaa 124,252,140\nD0 9,24,44,3072\n"
"Mvaa 92,252,132\nv_mul_f32 v33, 0xbfb8aa3b, v32\nMvaa 92,248,128\nWL 0\nMaaa 16,224,192\nRa 128,10,32768\nMaaa 16,228,196\ns_add_u32 m0, s47, 16384\ns_add_u32 s44, s44, 0x40000\n"
"Maaa 48,228,204\nRa 132,11,32768\nEX 33,33\nMaaa 48,224,200\nv_add_f32 v33, 1.0, v33\nMaaa 80,224,208\nRa 136,10,34816\nv_rcp_f32 v33, v33\nMaaa 80,228,212\nD0 6,24,44,0\n"
"Maaa 112,228,220\nRa 140,11,34816\nMU 33,32,33\nMaaa 112,224,216\nCV 33,33,33\nMaaa 116,232,216\nRa 144,10,40960\nv_lshlrev_b32 v33, 16, v33\nMaaa 116,236,220\nMU 33,33,36\n"
"Maaa 84,236,212\nRa 148,11,40960\nMaaa 84,232,208\nCV 33,33,33\nv_lshlrev_b32 v33, 16, v33\nMaaa 52,232,200\nRa 152,10,43008\nMaaa 52,236,204\nD0 7,24,44,1024\nMaaa 20,236,196\n"
"Ra 156,11,43008\nds_write_b32 v51, v33\nMaaa 20,232,192\nRv 244,52,0\nMaaa 24,240,192\nRv 248,52,64\ns_and_b32 s2, s74, 15\nMaaa 24,244,196\ns_sub_u32 s2, s2, 2\n"
"s_cmp_lt_u32 s2, 14\ns_cselect_b32 s5, 0, 0x40000000\nMaaa 56,244,204\ns_and_b32 s2, s2, 15\nMaaa 56,240,200\ns_mul_i32 s4, s73, 14\ns_add_u32 s4, s4, s2\nMaaa 88,240,208\n"
"v_and_b32 v49, 15, v0\nMaaa 88,244,212\nD0 8,24,44,2048\nMaaa 120,244,220\nv_lshlrev_b32 v50, 3, v49\nv_and_b32 v51, 1, v48\nMaaa 120,240,216\nv_lshl_add_u32 v50, v51, 2, v50\n"
"Maaa 124,248,216\ns_mul_i32 s2, s11, 112\nv_add_u32 v50, s2, v50\nMaaa 124,252,220\nv_lshrrev_b32 v51, 4, v50\nMaaa 92,252,212\nv_lshrrev_b32 v48, 1, v48\nMaaa 92,248,208\n"
"v_mad_u32_u24 v51, v48, 28, v51\nv_mul_u32_u24 v51, 7168, v51\nMaaa 60,248,200\nv_and_b32 v50, 15, v50\nMaaa 60,252,204\nD0 9,24,44,3072\nMaaa 28,252,196\n"
"v_lshl_add_u32 v51, v50, 2, v51\nv_cmp_gt_u32 vcc, 14, v49\ns_lshl_b32 s4, s4, 6\ns_add_u32 s4, s4, 0xc04c800\nv_cndmask_b32 v51, v254, v51, vcc\nMaaa 28,248,192\n"
"s_waitcnt vmcnt(16) lgkmcnt(3)\ns_barrier\nMaaa 0,160,192\nTa 224,16,32768\ns_or_b32 s4, s4, s5\nMaaa 0,164,196\nTa 226,16,34816\nMaaa 32,164,204\ns_add_u32 m0, s47, -49152\n"
"s_add_u32 s48, s48, 128\nMaaa 32,160,200\nTa 228,16,49152\nWL 3\nMaaa 64,160,208\nTa 230,16,51200\nMaaa 64,164,212\nD0 2,20,48,0\nMaaa 96,164,220\nTa 232,17,32768\n"
"v_lshlrev_b32 v252, 16, v224\nMaaa 96,160,216\nTa 234,17,34816\nMaaa 100,168,216\nv_and_b32 v253, 0xffff0000, v224\nMU 32,244,252\nMaaa 100,172,220\nTa 236,17,49152\n"
"Maaa 68,172,212\nTa 238,17,51200\nMaaa 68,168,208\nMU 33,244,253\nMU 40,248,252\nMaaa 36,168,200\nTa 240,18,32768\nMaaa 36,172,204\nTa 242,18,34816\nMaaa 4,172,196\n"
"D0 3,20,48,1024\nMU 41,248,253\nMaaa 4,168,192\nTa 244,18,49152\nMaaa 8,176,192\nTa 246,18,51200\nv_lshlrev_b32 v252, 16, v225\nMaaa 8,180,196\nv_and_b32 v253, 0xffff0000, v225\n"
"Maaa 40,180,204\nTa 248,19,32768\nMaaa 40,176,200\nTa 250,19,34816\nMU 34,244,252\nMaaa 72,176,208\nMU 35,244,253\nMaaa 72,180,212\nTa 252,19,49152\nMaaa 104,180,220\n"
"Ta 254,19,51200\nMU 42,248,252\nMaaa 104,176,216\nD0 4,20,48,2048\nMaaa 108,184,216\nMU 43,248,253\nv_lshlrev_b32 v252, 16, v226\nMaaa 108,188,220\n"
"v_and_b32 v253, 0xffff0000, v226\nMaaa 76,188,212\nMU 36,244,252\nMaaa 76,184,208\nMU 37,244,253\nMU 44,248,252\nMaaa 44,184,200\nMU 45,248,253\nMaaa 44,188,204\nD0 5,20,48,3072\n"
"Maaa 12,188,196\nv_lshlrev_b32 v252, 16, v227\nv_and_b32 v253, 0xffff0000, v227\nMaaa 12,184,192\nWL 0\nMvaa 80,224,128\nTa 160,12,32768\nMU 38,244,252\nMvaa 80,228,132\n"
"Ta 162,12,34816\nMvaa 112,228,140\ns_add_u32 m0, s47, -32768\ns_add_u32 s45, s45, 128\nMvaa 112,224,136\nTa 164,12,49152\nMU 39,244,253\nMvaa 144,224,144\nTa 166,12,51200\n"
"Mvaa 144,228,148\nD0 2,20,45,0\nMvaa 176,228,156\nTa 168,13,32768\nMU 46,248,252\nMvaa 176,224,152\nTa 170,13,34816\nMvaa 180,232,152\nMU 47,248,253\nv_lshlrev_b32 v252, 16, v228\n"
"Mvaa 180,236,156\nTa 172,13,49152\nMvaa 148,236,148\nTa 174,13,51200\nMvaa 148,232,144\nv_and_b32 v253, 0xffff0000, v228\nFM 32,245,252\nMvaa 116,232,136\nTa 176,14,32768\n"
"Mvaa 116,236,140\nTa 178,14,34816\nMvaa 84,236,132\nD0 3,20,45,1024\nFM 33,245,253\nMvaa 84,232,128\nTa 180,14,49152\nMvaa 88,240,128\nTa 182,14,51200\nFM 40,249,252\n"
"Mvaa 88,244,132\nFM 41,249,253\nMvaa 120,244,140\nTa 184,15,32768\nMvaa 120,240,136\nTa 186,15,34816\nv_lshlrev_b32 v252, 16, v229\nMvaa 152,240,144\n"
"v_and_b32 v253, 0xffff0000, v229\nMvaa 152,244,148\nTa 188,15,49152\nMvaa 184,244,156\nTa 190,15,51200\nFM 34,245,252\nMvaa 184,240,152\nD0 4,20,45,2048\nMvaa 188,248,152\n"
"FM 35,245,253\nFM 42,249,252\nMvaa 188,252,156\nFM 43,249,253\nMvaa 156,252,148\nv_lshlrev_b32 v252, 16, v230\nMvaa 156,248,144\nv_and_b32 v253, 0xffff0000, v230\nFM 36,245,252\n"
"Mvaa 124,248,136\nFM 37,245,253\nMvaa 124,252,140\nD0 5,20,45,3072\nMvaa 92,252,132\nFM 44,249,252\nFM 45,249,253\nMvaa 92,248,128\ns_waitcnt vmcnt(16) lgkmcnt(0)\ns_barrier\n"
"Mvaa 64,160,128\nRa 192,10,49152\nv_lshlrev_b32 v252, 16, v231\nMvaa 64,164,132\ns_add_u32 m0, s47, 32768\ns_add_u32 s44, s44, 0x40000\nMvaa 96,164,140\nRa 196,11,49152\n"
"Mvaa 96,160,136\nv_and_b32 v253, 0xffff0000, v231\nFM 38,245,252\nMvaa 128,160,144\nRa 200,10,51200\nMvaa 128,164,148\nD0 6,24,44,0\nMvaa 160,164,156\nRa 204,11,51200\n"
"FM 39,245,253\nMvaa 160,160,152\nFM 46,249,252\nMvaa 164,168,152\nRa 208,10,57344\nFM 47,249,253\nMvaa 164,172,156\nv_lshlrev_b32 v252, 16, v232\nMvaa 132,172,148\n"
"Ra 212,11,57344\nMvaa 132,168,144\nv_and_b32 v253, 0xffff0000, v232\nFM 32,246,252\nMvaa 100,168,136\nRa 216,10,59392\nMvaa 100,172,140\nD0 7,24,44,1024\nMvaa 68,172,132\n"
"Ra 220,11,59392\nFM 33,246,253\nMvaa 68,168,128\nFM 40,250,252\nMvaa 72,176,128\nFM 41,250,253\nv_lshlrev_b32 v252, 16, v233\nMvaa 72,180,132\nv_and_b32 v253, 0xffff0000, v233\n"
"Mvaa 104,180,140\nFM 34,246,252\nMvaa 104,176,136\nFM 35,246,253\nFM 42,250,252\nMvaa 136,176,144\nFM 43,250,253\nMvaa 136,180,148\nD0 8,24,44,2048\nMvaa 168,180,156\n"
"v_lshlrev_b32 v252, 16, v234\nv_and_b32 v253, 0xffff0000, v234\nMvaa 168,176,152\nFM 36,246,252\nMvaa 172,184,152\nFM 37,246,253\nFM 44,250,252\nMvaa 172,188,156\nFM 45,250,253\n"
"Mvaa 140,188,148\nv_lshlrev_b32 v252, 16, v235\nMvaa 140,184,144\nv_and_b32 v253, 0xffff0000, v235\nFM 38,246,252\nMvaa 108,184,136\nFM 39,246,253\nMvaa 108,188,140\n"
"D0 9,24,44,3072\nMvaa 76,188,132\nFM 46,250,252\nFM 47,250,253\nMvaa 76,184,128\nWL 0\nMaaa 0,160,192\nRa 128,10,0\nv_lshlrev_b32 v252, 16, v236\nMaaa 0,164,196\n"
"s_add_u32 m0, s47, 49152\ns_add_u32 s44, s44, 0x40000\nMaaa 32,164,204\nRa 132,11,0\nMaaa 32,160,200\nv_and_b32 v253, 0xffff0000, v236\nFM 32,247,252\nMaaa 64,160,208\n"
"Ra 136,10,2048\nMaaa 64,164,212\nD0 6,24,44,0\nMaaa 96,164,220\nRa 140,11,2048\nFM 33,247,253\nMaaa 96,160,216\nFM 40,251,252\nMaaa 100,168,216\nRa 144,10,8192\nFM 41,251,253\n"
"Maaa 100,172,220\nv_lshlrev_b32 v252, 16, v237\nMaaa 68,172,212\nRa 148,11,8192\nMaaa 68,168,208\nv_and_b32 v253, 0xffff0000, v237\nFM 34,247,252\nMaaa 36,168,200\n"
"Ra 152,10,10240\nMaaa 36,172,204\nD0 7,24,44,1024\nMaaa 4,172,196\nRa 156,11,10240\nFM 35,247,253\nMaaa 4,168,192\nFM 42,251,252\nMaaa 8,176,192\nFM 43,251,253\n"
"v_lshlrev_b32 v252, 16, v238\nMaaa 8,180,196\nv_and_b32 v253, 0xffff0000, v238\nMaaa 40,180,204\nFM 36,247,252\nMaaa 40,176,200\nFM 37,247,253\nFM 44,251,252\nMaaa 72,176,208\n"
"FM 45,251,253\nMaaa 72,180,212\nD0 8,24,44,2048\nMaaa 104,180,220\nv_lshlrev_b32 v252, 16, v239\nv_and_b32 v253, 0xffff0000, v239\nMaaa 104,176,216\nFM 38,247,252\n"
"Maaa 108,184,216\nFM 39,247,253\nFM 46,251,252\nMaaa 108,188,220\nFM 47,251,253\nMaaa 76,188,212\nv_permlane32_swap_b32 v32, v40\nMaaa 76,184,208\nv_permlane32_swap_b32 v33, v41\n"
"v_permlane32_swap_b32 v34, v42\nMaaa 44,184,200\nv_permlane32_swap_b32 v35, v43\nMaaa 44,188,204\nD0 9,24,44,3072\nMaaa 12,188,196\nv_permlane32_swap_b32 v36, v44\n"
"v_permlane32_swap_b32 v37, v45\nMaaa 12,184,192\nWV 16\ns_barrier\nMaaa 16,224,192\nTa 160,12,0\nv_permlane32_swap_b32 v38, v46\nMaaa 16,228,196\nTa 162,12,2048\nMaaa 48,228,204\n"
"s_add_u32 m0, s47, -16384\ns_add_u32 s48, s48, 128\nMaaa 48,224,200\nTa 164,12,16384\nv_permlane32_swap_b32 v39, v47\nMaaa 80,224,208\nTa 166,12,18432\nMaaa 80,228,212\n"
"D0 2,20,48,0\nMaaa 112,228,220\nTa 168,13,0\nAD 32,32,40\nMaaa 112,224,216\nTa 170,13,2048\nMaaa 116,232,216\nAD 33,33,41\nAD 34,34,42\nMaaa 116,236,220\nTa 172,13,16384\n"
"Maaa 84,236,212\nTa 174,13,18432\nMaaa 84,232,208\nAD 35,35,43\nAD 36,36,44\nMaaa 52,232,200\nTa 176,14,0\nMaaa 52,236,204\nTa 178,14,2048\nMaaa 20,236,196\nD0 3,20,48,1024\n"
"AD 37,37,45\nMaaa 20,232,192\nTa 180,14,16384\nMaaa 24,240,192\nTa 182,14,18432\nAD 38,38,46\nMaaa 24,244,196\nAD 39,39,47\nMaaa 56,244,204\nTa 184,15,0\nMaaa 56,240,200\n"
"Ta 186,15,2048\nv_permlane16_swap_b32 v32, v36\n.W546:\ns_cbranch_execz .W550\nMaaa 88,240,208\nv_permlane16_swap_b32 v33, v37\nMaaa 88,244,212\nTa 188,15,16384\nMaaa 120,244,220\n"
"Ta 190,15,18432\nv_permlane16_swap_b32 v34, v38\nMaaa 120,240,216\nD0 4,20,48,2048\nMaaa 124,248,216\nv_permlane16_swap_b32 v35, v39\nAD 32,32,36\nMaaa 124,252,220\nAD 33,33,37\n"
"Maaa 92,252,212\nAD 34,34,38\nMaaa 92,248,208\nAD 35,35,39\nSv 32,51,16,4,0\nMaaa 60,248,200\nMaaa 60,252,204\nD0 5,20,48,3072\nMaaa 28,252,196\nMaaa 28,248,192\nWL 0\n"
"Mvaa 64,160,128\nTa 224,16,0\ns_lshr_b32 s3, s11, 1\nMvaa 64,164,132\nTa 226,16,2048\ns_add_u32 s6, s56, 16384\nMvaa 96,164,140\ns_lshl_b32 s3, s3, 13\nMvaa 96,160,136\n"
"Ta 228,16,16384\nbuffer_load_dwordx4 v[224:227], v1, s[16:19], s6 offen offset:0 sc1\nMvaa 128,160,144\nTa 230,16,18432\nMvaa 128,164,148\ns_lshl_b32 s2, s11, 12\n"
"Mvaa 160,164,156\nTa 232,17,0\nMvaa 160,160,152\nTa 234,17,2048\nMvaa 164,168,152\ns_add_u32 s2, s2, s3\ns_add_u32 s2, s2, s72\nMvaa 164,172,156\nTa 236,17,16384\n"
"buffer_load_dwordx4 v[228:231], v1, s[16:19], s6 offen offset:1024 sc1\nMvaa 132,172,148\nTa 238,17,18432\nMvaa 132,168,144\ns_lshl_b32 s3, s11, 12\nMvaa 100,168,136\nTa 240,18,0\n"
"Mvaa 100,172,140\nTa 242,18,2048\nMvaa 68,172,132\ns_add_u32 s89, s3, 0x0\nMvaa 68,168,128\nTa 244,18,16384\n"
"buffer_load_dwordx4 v[232:235], v1, s[16:19], s6 offen offset:2048 sc1\nMvaa 72,176,128\nTa 246,18,18432\ns_mov_b32 s88, 0xffff8000\nMvaa 72,180,132\ns_lshl_b32 s4, s13, 1\n"
"Mvaa 104,180,140\nTa 248,19,0\nMvaa 104,176,136\nTa 250,19,2048\nMvaa 136,176,144\ns_sub_u32 s79, 15, s4\nMvaa 136,180,148\nTa 252,19,16384\n"
"buffer_load_dwordx4 v[236:239], v1, s[16:19], s6 offen offset:3072 sc1\nMvaa 168,180,156\nTa 254,19,18432\nMvaa 168,176,152\ns_add_u32 s96, s2, 0x1310000\nMvaa 172,184,152\n"
"s_mov_b32 s63, 0x53800000\ns_mov_b32 s64, 0x2b800000\nMvaa 172,188,156\ns_sub_u32 s98, s4, 1\ns_add_u32 s6, s56, 20480\nMvaa 140,188,148\ns_lshl_b32 s5, s13, 5\nMvaa 140,184,144\n"
"s_add_u32 s5, s5, s11\nbuffer_load_dwordx4 v[240:243], v1, s[16:19], s6 offen offset:0 sc1\nMvaa 108,184,136\ns_lshl_b32 s65, s5, 9\nMvaa 108,188,140\ns_add_u32 s65, s65, s72\n"
"Mvaa 76,188,132\ns_add_u32 s65, s65, 0x1580000\nMvaa 76,184,128\ns_waitcnt vmcnt(18) lgkmcnt(0)\ns_barrier\nMvaa 80,224,128\nRa 192,10,16384\ns_and_b32 s66, s72, 0x2000\n"
"Mvaa 80,228,132\ns_add_u32 s66, s66, s56\nbuffer_load_dwordx4 v[244:247], v1, s[16:19], s6 offen offset:1024 sc1\nMvaa 112,228,140\nRa 196,11,16384\nMvaa 112,224,136\n"
"s_add_u32 s66, s66, 0x100000\nMvaa 144,224,144\nRa 200,10,18432\nMvaa 144,228,148\ns_and_b32 s5, s12, 1\nMvaa 176,228,156\nRa 204,11,18432\nMvaa 176,224,152\n"
"s_mul_i32 s58, s5, 0x202000\nbuffer_load_dwordx4 v[248:251], v1, s[16:19], s6 offen offset:2048 sc1\nMvaa 180,232,152\nRa 208,10,24576\ns_xor_b32 s58, s58, s72\nMvaa 180,236,156\n"
"s_add_u32 s58, s58, 0x15c0000\nMvaa 148,236,148\nRa 212,11,24576\nMvaa 148,232,144\ns_lshl_b32 s5, s5, 8\nMvaa 116,232,136\nRa 216,10,26624\nMvaa 116,236,140\n"
"s_add_u32 s61, s58, s5\nbuffer_load_dwordx4 v[252:255], v1, s[16:19], s6 offen offset:3072 sc1\nMvaa 84,236,132\nRa 220,11,26624\nMvaa 84,232,128\ns_lshl4_add_u32 s60, s13, s61\n"
"Mvaa 88,240,128\ns_add_u32 s54, s60, 0\ns_xor_b32 s60, s54, 0x100\nMvaa 88,244,132\ns_cmp_eq_u32 s11, 0\ns_cselect_b32 s54, s54, 0x40000000\ns_add_u32 s6, s56, 0\n"
"Mvaa 120,244,140\ns_getpc_b64 s[8:9]\n.PC37:\ns_add_u32 s8, s8, .HD13-.PC37\ns_addc_u32 s9, s9, 0\nMvaa 120,240,136\ns_mov_b32 s97, 0xff800000\n"
"buffer_load_dwordx4 v[192:195], v1, s[16:19], s6 offen offset:0 sc1\nMvaa 152,240,144\ns_mov_b32 s90, 3\ns_cmp_lt_u32 s11, 2\ns_cselect_b32 s92, 2, 0\nMvaa 152,244,148\n"
"s_add_u32 s86, s2, 0x20000\nMvaa 184,244,156\ns_add_u32 s87, s2, 0x410000\ns_cmp_eq_u32 s13, 7\ns_cselect_b32 s92, s92, 0\nMvaa 184,240,152\ns_cmp_gt_u32 s13, 5\n"
"s_cselect_b32 s4, s12, 15\nMvaa 188,248,152\ns_cmp_lt_u32 s4, 14\ns_cselect_b32 s5, s11, 3\nMvaa 188,252,156\ns_cmp_lt_u32 s5, 2\ns_cselect_b32 s91, 1, 100\n"
"buffer_load_dwordx4 v[196:199], v1, s[16:19], s6 offen offset:1024 sc1\nMvaa 156,252,148\nv_and_b32 v27, 0x70, v1\n"
"buffer_load_dword v27, v27, s[16:19], s61 offen offset:128 sc0 sc1\nMvaa 156,248,144\ns_lshr_b32 s2, s13, 2\ns_bfe_u32 s3, s13, 0x10001\nMvaa 124,248,136\ns_mul_i32 s3, s3, 17\n"
"s_add_u32 s2, s2, s3\nMvaa 124,252,140\ns_lshl2_add_u32 s3, s14, 0\nMvaa 92,252,132\ns_lshl_b32 s3, s3, 2\ns_lshr_b32 s3, 0xe420ca86, s3\nMvaa 92,248,128\nWL 0\nMaaa 16,224,192\n"
"Ra 128,10,32768\ns_and_b32 s3, s3, 15\nbuffer_load_dwordx4 v[200:203], v1, s[16:19], s6 offen offset:2048 sc1\nMaaa 16,228,196\ns_add_u32 s2, s2, s3\ns_cmp_eq_u32 s2, 32\n"
"s_cselect_b32 s4, 16, s2\nMaaa 48,228,204\nRa 132,11,32768\ns_mul_i32 s75, s4, 65536\nMaaa 48,224,200\ns_lshr_b32 s2, s13, 2\ns_bfe_u32 s3, s13, 0x10001\nMaaa 80,224,208\n"
"Ra 136,10,34816\nMaaa 80,228,212\ns_mul_i32 s3, s3, 17\ns_add_u32 s2, s2, s3\nMaaa 112,228,220\nRa 140,11,34816\ns_lshl2_add_u32 s3, s14, 1\n"
"buffer_load_dwordx4 v[204:207], v1, s[16:19], s6 offen offset:3072 sc1\nMaaa 112,224,216\ns_lshl_b32 s3, s3, 2\nMaaa 116,232,216\nRa 144,10,40960\ns_lshr_b32 s3, 0xe420ca86, s3\n"
"Maaa 116,236,220\ns_and_b32 s3, s3, 15\ns_add_u32 s2, s2, s3\nMaaa 84,236,212\nRa 148,11,40960\ns_cmp_eq_u32 s2, 32\ns_cselect_b32 s4, 16, s2\ns_add_u32 s6, s56, 4096\n"
"Maaa 84,232,208\ns_mul_i32 s76, s4, 65536\ns_lshr_b32 s2, s13, 2\nMaaa 52,232,200\nRa 152,10,43008\ns_bfe_u32 s3, s13, 0x10001\n"
"buffer_load_dwordx4 v[208:211], v1, s[16:19], s6 offen offset:0 sc1\nMaaa 52,236,204\ns_mul_i32 s3, s3, 17\nMaaa 20,236,196\nRa 156,11,43008\ns_add_u32 s2, s2, s3\n"
"Maaa 20,232,192\ns_lshl2_add_u32 s3, s14, 2\ns_lshl_b32 s3, s3, 2\nMaaa 24,240,192\ns_lshr_b32 s3, 0xe420ca86, s3\ns_and_b32 s3, s3, 15\nMaaa 24,244,196\ns_add_u32 s2, s2, s3\n"
"s_cmp_eq_u32 s2, 32\ns_cselect_b32 s4, 16, s2\nMaaa 56,244,204\ns_mul_i32 s77, s4, 65536\ns_lshr_b32 s2, s13, 2\n"
"buffer_load_dwordx4 v[212:215], v1, s[16:19], s6 offen offset:1024 sc1\nMaaa 56,240,200\ns_bfe_u32 s3, s13, 0x10001\ns_mul_i32 s3, s3, 17\nMaaa 88,240,208\ns_add_u32 s2, s2, s3\n"
"Maaa 88,244,212\ns_lshl2_add_u32 s3, s14, 3\ns_lshl_b32 s3, s3, 2\nMaaa 120,244,220\ns_lshr_b32 s3, 0xe420ca86, s3\ns_and_b32 s3, s3, 15\nMaaa 120,240,216\ns_add_u32 s2, s2, s3\n"
"s_cmp_eq_u32 s2, 32\ns_cselect_b32 s4, 16, s2\nMaaa 124,248,216\ns_mul_i32 s78, s4, 65536\ns_lshr_b32 s3, s11, 1\n"
"buffer_load_dwordx4 v[216:219], v1, s[16:19], s6 offen offset:2048 sc1\nMaaa 124,252,220\ns_lshl_b32 s3, s3, 15\ns_add_u32 s2, s72, s3\nMaaa 92,252,212\ns_and_b32 s3, s11, 1\n"
"Maaa 92,248,208\ns_lshl_b32 s3, s3, 12\ns_add_u32 s2, s2, s3\nMaaa 60,248,200\ns_add_u32 s2, s2, 0x0\ns_mov_b32 s55, 16384\nMaaa 60,252,204\ns_add_u32 s75, s75, s2\n"
"Maaa 28,252,196\ns_add_u32 s76, s76, s2\ns_add_u32 s77, s77, s2\nbuffer_load_dwordx4 v[220:223], v1, s[16:19], s6 offen offset:3072 sc1\nMaaa 28,248,192\nWV 22\ns_barrier\n"
"Maaa 0,160,192\nTa 224,16,32768\ns_add_u32 s78, s78, s2\nMaaa 0,164,196\nTa 226,16,34816\nMaaa 32,164,204\nMaaa 32,160,200\nTa 228,16,49152\nMaaa 64,160,208\nTa 230,16,51200\n"
"Maaa 64,164,212\nMaaa 96,164,220\nTa 232,17,32768\nMaaa 96,160,216\nTa 234,17,34816\nMaaa 100,168,216\nWV 6\nv_cmp_ne_u32 vcc, s15, v27\ns_nop 0\ns_cbranch_vccnz .S38\n.S39:\n"
"s_sub_u32 s45, s86, 131072\nMaaa 100,172,220\nTa 236,17,49152\ns_add_u32 m0, s47, 0\ns_add_u32 s46, s45, 0x0\nMaaa 68,172,212\nTa 238,17,51200\nMaaa 68,168,208\nD1 1,16,46,0\n"
"s_mov_b32 s36, s68\nMaaa 36,168,200\nTa 240,18,32768\nMaaa 36,172,204\nTa 242,18,34816\nMaaa 4,172,196\ns_and_b32 s37, s69, 0xffff\nMaaa 4,168,192\nTa 244,18,49152\n"
"Maaa 8,176,192\nTa 246,18,51200\nD1 1,16,46,1024\nMaaa 8,180,196\ns_mov_b32 s38, 0x1c0000\nMaaa 40,180,204\nTa 248,19,32768\nMaaa 40,176,200\nTa 250,19,34816\nMaaa 72,176,208\n"
"s_mov_b32 s39, 0x20000\nMaaa 72,180,212\nTa 252,19,49152\nD1 1,16,46,2048\nMaaa 104,180,220\nTa 254,19,51200\ns_and_b32 s2, s74, 15\nMaaa 104,176,216\ns_max_u32 s2, s2, 2\n"
"Maaa 108,184,216\ns_sub_u32 s2, s2, 2\nMaaa 108,188,220\ns_mul_i32 s3, s73, 14\nMaaa 76,188,212\nD1 1,16,46,3072\nMaaa 76,184,208\ns_add_u32 s2, s2, s3\n"
"s_lshl2_add_u32 s2, s2, s11\nMaaa 44,184,200\ns_mul_i32 s3, s2, 4682\nMaaa 44,188,204\ns_lshr_b32 s3, s3, 16\nMaaa 12,188,196\ns_mul_i32 s22, s3, 14\nMaaa 12,184,192\nWL 0\n"
"Mvaa 80,224,128\nTa 160,12,32768\ns_add_u32 m0, s47, -65536\ns_add_u32 s46, s45, 0x10000\nD1 1,16,46,0\nMvaa 80,228,132\nTa 162,12,34816\nMvaa 84,236,132\ns_sub_u32 s22, s2, s22\n"
"Mvaa 84,232,128\nTa 164,12,49152\nMvaa 88,240,128\nTa 166,12,51200\nMvaa 88,244,132\nD1 1,16,46,1024\nMvaa 92,252,132\nTa 168,13,32768\ns_and_b32 s21, s3, 28\nMvaa 92,248,128\n"
"Ta 170,13,34816\nMvaa 124,248,136\ns_add_u32 s21, s21, s3\nMvaa 124,252,140\nTa 172,13,49152\nMvaa 120,244,140\nTa 174,13,51200\nD1 1,16,46,2048\nMvaa 120,240,136\n"
"s_lshl_b32 s21, s21, 4\ns_add_u32 s21, s21, 1024\nMvaa 116,232,136\nTa 176,14,32768\nCV 40,80,81\nMvaa 116,236,140\nTa 178,14,34816\nMvaa 112,228,140\ns_xor_b32 s3, s3, 28\n"
"CV 41,82,83\nMvaa 112,224,136\nTa 180,14,49152\nD1 1,16,46,3072\nMvaa 144,224,144\nTa 182,14,51200\ns_bfe_u32 s41, s3, 0x30002\nMvaa 144,228,148\ns_cmp_gt_u32 s22, 6\n"
"s_cselect_b32 s2, 1179648, 0\ns_cselect_b32 s1, 16, 0\nMvaa 148,236,148\nTa 184,15,32768\nMvaa 148,232,144\nTa 186,15,34816\nCV 42,84,85\nMvaa 152,240,144\ns_add_u32 s41, s41, s1\n"
"Mvaa 152,244,148\nTa 188,15,49152\nCV 43,86,87\nMvaa 156,252,148\nTa 190,15,51200\ns_lshl4_add_u32 s41, s41, 0xc048400\nMvaa 156,248,144\ns_and_b32 s1, s3, 28\nCV 44,88,89\n"
"Mvaa 188,248,152\ns_add_u32 s3, s3, s1\nMvaa 188,252,156\ns_lshl_b32 s3, s3, 11\nMvaa 184,244,156\ns_add_u32 s2, s2, s3\nCV 45,90,91\nMvaa 184,240,152\ns_lshl_b32 s3, s22, 17\n"
"s_add_u32 s2, s2, s3\nMvaa 180,232,152\ns_add_u32 s20, s2, 0xce0000\nCV 46,92,93\nMvaa 180,236,156\ns_lshl_b32 s2, s22, 7\nMvaa 176,228,156\ns_add_u32 s40, s2, 0xc04b800\n"
"CV 47,94,95\nMvaa 176,224,152\nWL 0\ns_barrier\nMvaa 64,160,128\nRa 192,10,49152\ns_add_u32 m0, s47, 32768\ns_add_u32 s46, s45, 0x8000\nMvaa 64,164,132\ns_mul_i32 s2, s21, 896\n"
"Mvaa 68,172,132\nRa 196,11,49152\nD1 1,16,46,0\nMvaa 68,168,128\ns_lshl_b32 s3, s22, 6\nCV 60,120,121\nMvaa 72,176,128\nRa 200,10,51200\nMvaa 72,180,132\ns_add_u32 s22, s2, s3\n"
"CV 61,122,123\nMvaa 76,188,132\nRa 204,11,51200\ns_add_u32 s23, s22, 57344\nMvaa 76,184,128\nD1 1,16,46,1024\nCV 62,124,125\nMvaa 108,184,136\nRa 208,10,57344\nMvaa 108,188,140\n"
"CV 63,126,127\nMvaa 104,180,140\nRa 212,11,57344\nCV 56,112,113\nMvaa 104,176,136\nCV 57,114,115\nMvaa 100,168,136\nRa 216,10,59392\nD1 1,16,46,2048\nMvaa 100,172,140\n"
"CV 58,116,117\nMvaa 96,164,140\nRa 220,11,59392\nCV 59,118,119\nMvaa 96,160,136\nCV 32,64,65\nMvaa 128,160,144\nCV 33,66,67\nCV 34,68,69\nMvaa 128,164,148\nD1 1,16,46,3072\n"
"Mvaa 132,172,148\nCV 35,70,71\nMvaa 132,168,144\nCV 36,72,73\nCV 37,74,75\nMvaa 136,176,144\ns_add_u32 m0, s47, 49152\ns_add_u32 s46, s45, 0x400000\nMvaa 136,180,148\nCV 38,76,77\n"
"CV 39,78,79\nMvaa 140,188,148\nD1 1,16,46,0\nMvaa 140,184,144\nCV 52,104,105\nCV 53,106,107\nMvaa 172,184,152\nCV 54,108,109\nMvaa 172,188,156\nCV 55,110,111\nMvaa 168,180,156\n"
"CV 48,96,97\nCV 49,98,99\nMvaa 168,176,152\nD1 1,16,46,1024\nMvaa 164,168,152\nCV 50,100,101\nCV 51,102,103\nMvaa 164,172,156\nCV 104,144,145\nMvaa 160,164,156\nCV 105,146,147\n"
"CV 106,148,149\nMvaa 160,160,152\nWL 0\nMaaa 0,160,192\nD1 1,16,46,2048\nSv 32,1,16,75,0\nMaaa 0,164,196\nCV 107,150,151\nMaaa 4,172,196\nCV 108,152,153\nCV 109,154,155\n"
"Maaa 4,168,192\nCV 110,156,157\nCV 111,158,159\nMaaa 8,176,192\nCV 124,184,185\nCV 125,186,187\nMaaa 8,180,196\nD1 1,16,46,3072\nMaaa 12,188,196\nCV 126,188,189\nCV 127,190,191\n"
"Maaa 12,184,192\nCV 120,176,177\nCV 121,178,179\nMaaa 44,184,200\ns_add_u32 m0, s47, -49152\ns_add_u32 s46, s45, 0x408000\nSv 36,1,16,75,1024\nMaaa 44,188,204\nCV 122,180,181\n"
"Maaa 40,180,204\nD1 1,16,46,0\nCV 123,182,183\nMaaa 40,176,200\nCV 96,128,129\nCV 97,130,131\nMaaa 36,168,200\nCV 98,132,133\nCV 99,134,135\nMaaa 36,172,204\nCV 100,136,137\n"
"Maaa 32,164,204\nCV 101,138,139\nCV 102,140,141\nMaaa 32,160,200\nD1 1,16,46,1024\nCV 103,142,143\nMaaa 64,160,208\nSv 40,1,16,75,2048\nCV 116,168,169\n.W547:\n"
"s_cbranch_execz .W551\nMaaa 64,164,212\nCV 117,170,171\nMaaa 68,172,212\nCV 118,172,173\nCV 119,174,175\nMaaa 68,168,208\nCV 112,160,161\nCV 113,162,163\nMaaa 72,176,208\n"
"D1 1,16,46,2048\nCV 114,164,165\nMaaa 72,180,212\nCV 115,166,167\nMaaa 76,188,212\nv_add_u32 v2, 0x10000, v1\ns_lshr_b32 s2, s59, 1\nMaaa 76,184,208\nv_add_u32 v24, s2, v2\n"
"v_xor_b32 v26, 0x1000, v24\nMaaa 108,184,216\nSv 44,1,16,75,3072\nv_lshrrev_b32 v20, 4, v0\nMaaa 108,188,220\nD1 1,16,46,3072\nMaaa 104,180,220\nv_lshlrev_b32 v20, 2, v20\n"
"v_and_b32 v0, 15, v0\nMaaa 104,176,216\nv_sub_u32 v0, v0, v20\ns_lshl_b32 s2, s11, 4\nMaaa 100,168,216\nv_add_u32 v3, s2, v0\nv_subrev_u32 v0, 32, v3\nMaaa 100,172,220\n"
"v_mov_b32 v4, 0\nMaaa 96,164,220\nv_mov_b32 v5, 0\nv_mov_b32 v6, 0\nMaaa 96,160,216\nWV 20\ns_barrier\nMaaa 16,224,192\ns_add_u32 s75, s75, s55\nSv 48,1,16,75,0\nMaaa 16,228,196\n"
"v_mov_b32 v7, 0\nMaaa 20,236,196\nv_mov_b32 v8, 0\nv_mov_b32 v9, 0\nMaaa 20,232,192\nv_mov_b32 v10, 0\nv_mov_b32 v11, 0\nMaaa 24,240,192\nv_mov_b32 v12, 0\nv_mov_b32 v13, 0\n"
"Maaa 24,244,196\nv_mov_b32 v14, 0\nMaaa 28,252,196\nv_mov_b32 v15, 0\nv_mov_b32 v16, 0\nMaaa 28,248,192\nv_mov_b32 v17, 0\nv_mov_b32 v18, 0\nMaaa 60,248,200\nSv 52,1,16,75,1024\n"
"v_mov_b32 v19, 0\nMaaa 60,252,204\nv_mov_b32 v86, 0\nMaaa 56,244,204\nv_mov_b32 v87, 0\nv_mov_b32 v88, 0\nMaaa 56,240,200\nv_mov_b32 v89, 0\nAR 92,0\nAR 93,1\nMaaa 52,232,200\n"
"CV 128,92,93\nAR 92,2\nAR 93,3\nMaaa 52,236,204\nCV 129,92,93\nAR 92,4\nAR 93,5\nMaaa 48,228,204\nRa 128,24,0\nCV 130,92,93\nAR 92,6\nMaaa 48,224,200\nAR 93,7\nCV 131,92,93\n"
"AR 92,8\nMaaa 80,224,208\nSv 56,1,16,75,2048\nRa 132,24,1024\nAR 93,9\nMaaa 80,228,212\nCV 132,92,93\nAR 92,10\nAR 93,11\nMaaa 84,236,212\nRa 136,24,2048\nCV 133,92,93\nAR 92,12\n"
"Maaa 84,232,208\nAR 93,13\nCV 134,92,93\nAR 92,14\nMaaa 88,240,208\nRa 140,24,3072\nAR 93,15\nCV 135,92,93\nMaaa 88,244,212\nAR 92,40\nAR 93,41\nCV 148,92,93\nMaaa 92,252,212\n"
"Ra 144,26,0\nAR 92,42\nAR 93,43\nMaaa 92,248,208\nCV 149,92,93\nAR 92,44\nAR 93,45\nMaaa 124,248,216\nSv 60,1,16,75,3072\nRa 148,26,1024\nCV 150,92,93\nMaaa 124,252,220\nAR 92,46\n"
"AR 93,47\nCV 151,92,93\nMaaa 120,244,220\nRa 152,26,2048\nAR 92,32\nAR 93,33\nMaaa 120,240,216\nCV 144,92,93\nAR 92,34\nAR 93,35\nMaaa 116,232,216\nRa 156,26,3072\nCV 145,92,93\n"
"AR 92,36\nMaaa 116,236,220\nAR 93,37\nCV 146,92,93\nAR 92,38\nMaaa 112,228,220\nAR 93,39\nCV 147,92,93\nAR 92,16\nMaaa 112,224,216\nWL 4\nCvav 32,128,192,4\nRa 160,24,8192\n"
"AR 93,17\nCV 136,92,93\nMvav 32,132,200\nAR 92,18\nAR 93,19\nCV 137,92,93\nMvav 32,136,208\nRa 164,24,9216\nAR 92,20\nAR 93,21\nMvav 32,140,216\nCV 138,92,93\nAR 92,22\nAR 93,23\n"
"Cvav 36,140,220,8\nRa 168,24,10240\nCV 139,92,93\nAR 92,24\nMvav 36,136,212\ns_add_u32 m0, s89, 32768\nAR 93,25\nCV 140,92,93\nMvav 36,132,204\nRa 172,24,11264\nWL 4\ns_barrier\n"
"Mvav 36,128,196\nAR 92,26\nAR 93,27\nCV 141,92,93\nMvav 36,144,228\nRa 176,26,8192\nAR 92,28\nAR 93,29\nSv 96,1,16,76,0\nMvav 36,148,236\ns_add_u32 s46, s86, s88\nCV 142,92,93\n"
"AR 92,30\nMvav 36,152,244\nRa 180,26,9216\nAR 93,31\nCV 143,92,93\nMvav 36,156,252\nAR 92,56\nAR 93,57\nCV 156,92,93\nMvav 32,156,248\nRa 184,26,10240\nAR 92,58\nAR 93,59\n"
"Mvav 32,152,240\nD1 1,16,46,0\nCV 157,92,93\nAR 92,60\nMvav 32,148,232\nRa 188,26,11264\nWL 4\nAR 93,61\nMvav 32,144,224\nCV 158,92,93\nAR 92,62\nAR 93,63\nCvav 40,160,192,12\n"
"CV 159,92,93\nAR 92,48\nAR 93,49\nMvav 40,164,200\nCV 152,92,93\nAR 92,50\nAR 93,51\nMvav 40,168,208\nCV 153,92,93\nAR 92,52\nAR 93,53\nv_and_b32 v27, 0x70, v1\n"
"buffer_load_dword v27, v27, s[16:19], s61 offen offset:136 sc0 sc1\nMvav 40,172,216\nCV 154,92,93\nAR 92,54\nAR 93,55\nCvav 44,172,220,16\nCV 155,92,93\nAR 92,64\nAR 93,65\n"
"Mvav 44,168,212\nD1 1,16,46,1024\nCV 160,92,93\nAR 92,66\nMvav 44,164,204\nWL 0\nAR 93,67\nCV 161,92,93\nMvav 44,160,196\nAR 92,68\nAR 93,69\nCV 162,92,93\nMvav 44,176,228\n"
"AR 92,70\nAR 93,71\nCV 163,92,93\nSv 100,1,16,76,1024\nMvav 44,180,236\ns_add_u32 s88, s88, 32768\nEX 80,32\nMvav 44,184,244\nAR 92,72\nAR 93,73\nCV 164,92,93\nMvav 44,188,252\n"
"EX 81,33\nMvav 40,188,248\nAR 92,74\nAR 93,75\nCV 165,92,93\nMvav 40,184,240\nD1 1,16,46,2048\nAR 92,76\nAR 93,77\nMvav 40,180,232\nCV 166,92,93\nAR 92,78\nAR 93,79\n"
"Mvav 40,176,224\nAD 84,80,81\nCV 64,80,81\nCV 167,92,93\nAR 92,80\nAR 93,81\nCV 168,92,93\nEX 82,34\nAR 92,82\nAR 93,83\nCV 169,92,93\nAR 92,84\nWV 17\nEX 83,35\nAD 84,84,82\n"
"AR 93,85\nCV 170,92,93\nD1 1,16,46,3072\ns_add_u32 m0, s89, 49152\nAR 92,86\ns_barrier\nAR 93,87\nCV 171,92,93\nAD 84,84,83\nCV 65,82,83\nAR 92,88\nRa 128,24,32768\nAR 93,89\n"
"CV 172,92,93\nSv 104,1,16,76,2048\ns_add_u32 s46, s87, s88\nEX 80,36\nRa 132,24,33792\nAR 92,90\nAR 93,91\nEX 81,37\nRa 136,24,34816\nCV 173,92,93\nAR 92,92\nD1 1,16,46,0\n"
"AR 93,93\nCV 174,92,93\nRa 140,24,35840\nAR 92,94\nAR 93,95\nAD 85,80,81\nCV 68,80,81\nCV 175,92,93\nRa 144,26,32768\nAR 92,104\nAR 93,105\nEX 82,38\nCV 180,92,93\n"
"Ra 148,26,33792\nAR 92,106\nAR 93,107\nEX 83,39\nRa 152,26,34816\nAD 85,85,82\nCV 181,92,93\nD1 1,16,46,1024\nAR 92,108\nAR 93,109\nRa 156,26,35840\nCV 182,92,93\nAR 92,110\n"
"AD 85,85,83\nCV 69,82,83\nAR 93,111\nRa 160,24,40960\nCV 183,92,93\nAR 92,96\nSv 108,1,16,76,3072\ns_xor_b32 s89, s89, 0x10000\nEX 80,40\nRa 164,24,41984\nAR 93,97\nCV 176,92,93\n"
"WL 6\nEX 81,41\nRa 168,24,43008\nAD 84,84,80\nAR 92,98\nD1 1,16,46,2048\nAR 93,99\nCV 177,92,93\nRa 172,24,44032\nAR 92,100\nAR 93,101\nAD 84,84,81\nCV 66,80,81\nCV 178,92,93\n"
"Cvav 48,128,192,4\nRa 176,26,40960\nAR 92,102\nAR 93,103\nMvav 48,132,200\nEX 82,42\nCV 179,92,93\nMvav 48,136,208\nRa 180,26,41984\nAR 92,120\nAR 93,121\nMvav 48,140,216\nWL 6\n"
"EX 83,43\nWV 9\nv_cmp_ne_u32 vcc, s15, v27\ns_nop 0\ns_cbranch_vccnz .S41\n.S42:\nCvav 52,140,220,8\nRa 184,26,43008\nAD 84,84,82\nCV 188,92,93\nMvav 52,136,212\nD1 1,16,46,3072\n"
"s_add_u32 m0, s89, 0\nAR 92,122\nMvav 52,132,204\nRa 188,26,44032\nAR 93,123\nCV 189,92,93\nMvav 52,128,196\nAD 84,84,83\nCV 67,82,83\nAR 92,124\nMvav 52,144,228\nRa 128,2,49152\n"
"AR 93,125\nCV 190,92,93\ns_add_u32 s76, s76, s55\nSv 112,1,16,76,0\nMvav 52,148,236\ns_add_u32 s46, s86, s88\nEX 80,44\nMvav 52,152,244\nRa 132,2,50176\n"
"v_xor_b32 v24, 0x10000, v24\nAR 92,126\nMvav 52,156,252\nWL 6\nEX 81,45\nMvav 48,156,248\nRa 136,2,51200\nAD 85,85,80\nAR 93,127\nMvav 48,152,240\nD1 1,16,46,0\nCV 191,92,93\n"
"AR 92,112\nMvav 48,148,232\nRa 140,2,52224\nAR 93,113\nCV 184,92,93\nMvav 48,144,224\nAD 85,85,81\nCV 70,80,81\nAR 92,114\nCvav 56,160,192,12\nRa 144,2,53248\nAR 93,115\n"
"CV 185,92,93\nMvav 56,164,200\nEX 82,46\nAR 92,116\nMvav 56,168,208\nRa 148,2,54272\nv_xor_b32 v26, 0x10000, v26\nAR 93,117\nMvav 56,172,216\nWL 6\nEX 83,47\nCvav 60,172,220,16\n"
"Ra 152,2,55296\nAD 85,85,82\nCV 186,92,93\nMvav 60,168,212\nD1 1,16,46,1024\nAR 92,118\nAR 93,119\nMvav 60,164,204\nRa 156,2,56320\nCV 187,92,93\nMvav 60,160,196\nAD 85,85,83\n"
"CV 71,82,83\nMvav 60,176,228\nRa 160,2,57344\nv_max_f32 v90, v84, v85\nSv 116,1,16,76,1024\nMvav 60,180,236\ns_add_u32 s88, s88, 32768\nEX 80,48\nMvav 60,184,244\nRa 164,2,58368\n"
"v_cmp_lt_f32 vcc, s63, v90\nMvav 60,188,252\nWL 8\nEX 81,49\nMvav 56,188,248\nRa 168,2,59392\ns_cbranch_vccnz .T23\n.T24:\nv_mov_b32 v20, v84\nv_mov_b32 v21, v85\n"
"v_mov_b32 v22, v20\nv_mov_b32 v23, v21\nMvav 56,184,240\nD1 1,16,46,2048\nv_permlane32_swap_b32 v20, v22\nv_permlane32_swap_b32 v21, v23\nAD 20,20,22\nAD 21,21,23\n"
"v_mov_b32 v22, v20\nv_mov_b32 v23, v21\nMvav 56,180,232\nRa 172,2,60416\nv_permlane16_swap_b32 v20, v22\nv_permlane16_swap_b32 v21, v23\nAD 20,20,22\nAD 21,21,23\n"
"v_min_f32 v90, v20, v21\nv_cmp_gt_f32 vcc, s64, v90\ns_nop 0\ns_cbranch_vccnz .T25\n.T26:\nAD 86,86,84\nMvav 56,176,224\nAD 84,80,81\nCV 72,80,81\nZaav 0,128,64\nRa 176,2,61440\n"
"Zaav 4,128,68\nEX 82,50\nZaav 12,132,68\nRa 180,2,62464\nZaav 8,132,64\nWL 8\nEX 83,51\nZaav 16,136,64\nRa 184,2,63488\nAD 84,84,82\nZaav 20,136,68\nD1 1,16,46,3072\n"
"s_add_u32 m0, s89, 16384\nZaav 28,140,68\nRa 188,2,64512\ns_barrier\nZaav 24,140,64\nAD 84,84,83\nCV 73,82,83\nZaav 32,144,64\nRa 128,24,0\nSv 120,1,16,76,2048\nZaav 36,144,68\n"
"s_add_u32 s46, s87, s88\nEX 80,52\nbuffer_store_dword v25, off, s[16:19], s54 offset:12 sc0 sc1\nZaav 44,148,68\nRa 132,24,1024\nv_xor_b32 v2, 0x10000, v2\nZaav 40,148,64\nWL 8\n"
"EX 81,53\nZaav 48,152,64\nRa 136,24,2048\nAD 87,87,85\nZaav 52,152,68\nD1 1,16,46,0\nZaav 60,156,68\nRa 140,24,3072\nZaav 56,156,64\nAD 85,80,81\nCV 76,80,81\nZaav 64,160,64\n"
"Ra 144,26,0\nZaav 68,160,68\nEX 82,54\nZaav 76,164,68\nRa 148,26,1024\nZaav 72,164,64\nWL 8\nEX 83,55\nZaav 80,168,64\nRa 152,26,2048\nAD 85,85,82\nZaav 84,168,68\n"
"D1 1,16,46,1024\nZaav 92,172,68\nRa 156,26,3072\nZaav 88,172,64\nAD 85,85,83\nCV 77,82,83\nZaav 96,176,64\nRa 160,24,8192\nSv 124,1,16,76,3072\nZaav 100,176,68\nEX 80,56\n.W548:\n"
"s_cbranch_execz .W552\nZaav 108,180,68\nRa 164,24,9216\nZaav 104,180,64\nWL 6\nEX 81,57\nZaav 112,184,64\nRa 168,24,10240\nAD 84,84,80\nZaav 116,184,68\nD1 1,16,46,2048\n"
"Zaav 124,188,68\nRa 172,24,11264\nZaav 120,188,64\nAD 84,84,81\nCV 74,80,81\nCvav 32,128,192,4\nRa 176,26,8192\nMvav 32,132,200\nEX 82,58\nMvav 32,136,208\nRa 180,26,9216\n"
"Mvav 32,140,216\nWL 6\nEX 83,59\nCvav 36,140,220,8\nRa 184,26,10240\nAD 84,84,82\nMvav 36,136,212\nD1 1,16,46,3072\ns_add_u32 m0, s89, 32768\nMvav 36,132,204\nRa 188,26,11264\n"
"Mvav 36,128,196\nAD 84,84,83\nCV 75,82,83\nMvav 36,144,228\nRa 128,2,16384\nSv 128,1,16,77,0\nMvav 36,148,236\ns_add_u32 s46, s86, s88\nEX 80,60\nMvav 36,152,244\nRa 132,2,17408\n"
"Mvav 36,156,252\nWL 6\nEX 81,61\nMvav 32,156,248\nRa 136,2,18432\nAD 85,85,80\nMvav 32,152,240\nD1 1,16,46,0\nMvav 32,148,232\nRa 140,2,19456\nMvav 32,144,224\nAD 85,85,81\n"
"CV 78,80,81\nCvav 40,160,192,12\nRa 144,2,20480\nMvav 40,164,200\nEX 82,62\nMvav 40,168,208\nRa 148,2,21504\nMvav 40,172,216\nWL 6\nEX 83,63\nCvav 44,172,220,16\nRa 152,2,22528\n"
"AD 85,85,82\nMvav 44,168,212\nD1 1,16,46,1024\nMvav 44,164,204\nRa 156,2,23552\nMvav 44,160,196\nAD 85,85,83\nCV 79,82,83\nMvav 44,176,228\nRa 160,2,24576\n"
"v_max_f32 v90, v84, v85\nSv 132,1,16,77,1024\nMvav 44,180,236\ns_add_u32 s88, s88, 32768\nEX 80,32\nMvav 44,184,244\nRa 164,2,25600\nv_cmp_lt_f32 vcc, s63, v90\nMvav 44,188,252\n"
"WL 8\nEX 81,33\nMvav 40,188,248\nRa 168,2,26624\ns_cbranch_vccnz .T28\n.T29:\nMvav 40,184,240\nD1 1,16,46,2048\nMvav 40,180,232\nRa 172,2,27648\nAD 86,86,84\nMvav 40,176,224\n"
"AD 84,80,81\nCV 64,80,81\nMaav 0,128,72\nRa 176,2,28672\nv_mov_b32 v112, -1\nMaav 4,128,76\nEX 82,34\nMaav 12,132,76\nRa 180,2,29696\nMaav 8,132,72\n"
"s_waitcnt vmcnt(18) lgkmcnt(8)\nEX 83,35\nMaav 16,136,72\nRa 184,2,30720\nAD 84,84,82\nMaav 20,136,76\nD1 1,16,46,3072\ns_add_u32 m0, s89, 49152\nMaav 28,140,76\nRa 188,2,31744\n"
"s_barrier\nMaav 24,140,72\nAD 84,84,83\nCV 65,82,83\nMaav 32,144,72\nRa 128,24,32768\nSv 136,1,16,77,2048\nMaav 36,144,76\ns_add_u32 s46, s87, s88\nEX 80,36\nMaav 44,148,76\n"
"Ra 132,24,33792\nMaav 40,148,72\nWL 8\nEX 81,37\nMaav 48,152,72\nRa 136,24,34816\nAD 87,87,85\nMaav 52,152,76\nD1 1,16,46,0\nMaav 60,156,76\nRa 140,24,35840\nMaav 56,156,72\n"
"AD 85,80,81\nCV 68,80,81\nMaav 64,160,72\nRa 144,26,32768\nv_mov_b32 v113, -1\nMaav 68,160,76\nEX 82,38\nMaav 76,164,76\nRa 148,26,33792\nMaav 72,164,72\nWL 8\nEX 83,39\n"
"Maav 80,168,72\nRa 152,26,34816\nAD 85,85,82\nMaav 84,168,76\nD1 1,16,46,1024\nMaav 92,172,76\nRa 156,26,35840\nMaav 88,172,72\nAD 85,85,83\nCV 69,82,83\nMaav 96,176,72\n"
"Ra 160,24,40960\nSv 140,1,16,77,3072\nMaav 100,176,76\ns_xor_b32 s89, s89, 0x10000\nEX 80,40\nMaav 108,180,76\nRa 164,24,41984\nMaav 104,180,72\nWL 6\nEX 81,41\nMaav 112,184,72\n"
"Ra 168,24,43008\nAD 84,84,80\nMaav 116,184,76\nD1 1,16,46,2048\nMaav 124,188,76\nRa 172,24,44032\nMaav 120,188,72\nAD 84,84,81\nCV 66,80,81\nCvav 48,128,192,4\nRa 176,26,40960\n"
"v_mov_b32 v114, -1\nMvav 48,132,200\nEX 82,42\nMvav 48,136,208\nRa 180,26,41984\nMvav 48,140,216\nWL 6\nEX 83,43\nCvav 52,140,220,8\nRa 184,26,43008\nAD 84,84,82\n"
"v_and_b32 v27, 0x70, v1\nbuffer_load_dword v27, v27, s[16:19], s61 offen offset:140 sc0 sc1\nMvav 52,136,212\nD1 1,16,46,3072\ns_add_u32 m0, s89, 0\nMvav 52,132,204\n"
"Ra 188,26,44032\nMvav 52,128,196\nAD 84,84,83\nCV 67,82,83\nMvav 52,144,228\nRa 128,2,49152\ns_add_u32 s77, s77, s55\nSv 144,1,16,77,0\nMvav 52,148,236\ns_add_u32 s46, s86, s88\n"
"EX 80,44\nMvav 52,152,244\nRa 132,2,50176\nv_xor_b32 v24, 0x10000, v24\nMvav 52,156,252\nWL 6\nEX 81,45\nMvav 48,156,248\nRa 136,2,51200\nAD 85,85,80\nMvav 48,152,240\n"
"D1 1,16,46,0\nMvav 48,148,232\nRa 140,2,52224\nMvav 48,144,224\nAD 85,85,81\nCV 70,80,81\nCvav 56,160,192,12\nRa 144,2,53248\nv_mov_b32 v115, -1\nMvav 56,164,200\nEX 82,46\n"
"Mvav 56,168,208\nRa 148,2,54272\nv_xor_b32 v26, 0x10000, v26\nMvav 56,172,216\nWL 6\nEX 83,47\nCvav 60,172,220,16\nRa 152,2,55296\nAD 85,85,82\nMvav 60,168,212\nD1 1,16,46,1024\n"
"Mvav 60,164,204\nRa 156,2,56320\nMvav 60,160,196\nAD 85,85,83\nCV 71,82,83\nMvav 60,176,228\nRa 160,2,57344\nv_max_f32 v90, v84, v85\nSv 148,1,16,77,1024\nMvav 60,180,236\n"
"s_add_u32 s88, s88, 32768\nEX 80,48\nMvav 60,184,244\nRa 164,2,58368\nv_cmp_lt_f32 vcc, s63, v90\nMvav 60,188,252\nWL 8\nEX 81,49\nMvav 56,188,248\nRa 168,2,59392\n"
"s_cbranch_vccnz .T30\n.T31:\nMvav 56,184,240\nD1 1,16,46,2048\nMvav 56,180,232\nRa 172,2,60416\nAD 86,86,84\nMvav 56,176,224\nAD 84,80,81\nCV 72,80,81\nMaav 0,128,64\n"
"Ra 176,2,61440\nMaav 4,128,68\nEX 82,50\nMaav 12,132,68\nRa 180,2,62464\nMaav 8,132,64\ns_waitcnt vmcnt(18) lgkmcnt(8)\nEX 83,51\nMaav 16,136,64\nRa 184,2,63488\nAD 84,84,82\n"
"Maav 20,136,68\nD1 1,16,46,3072\ns_add_u32 m0, s89, 16384\nMaav 28,140,68\nRa 188,2,64512\ns_barrier\nMaav 24,140,64\nAD 84,84,83\nCV 73,82,83\nMaav 32,144,64\nRa 128,24,0\n"
"Sv 152,1,16,77,2048\nMaav 36,144,68\ns_add_u32 s46, s87, s88\nEX 80,52\nMaav 44,148,68\nRa 132,24,1024\nv_xor_b32 v2, 0x10000, v2\nMaav 40,148,64\nWL 8\nEX 81,53\nMaav 48,152,64\n"
"Ra 136,24,2048\nAD 87,87,85\nbuffer_store_dword v25, off, s[16:19], s54 offset:8 sc0 sc1\nMaav 52,152,68\nD1 1,16,46,0\nMaav 60,156,68\nRa 140,24,3072\nMaav 56,156,64\n"
"AD 85,80,81\nCV 76,80,81\nMaav 64,160,64\nRa 144,26,0\nMaav 68,160,68\nEX 82,54\nMaav 76,164,68\nRa 148,26,1024\nMaav 72,164,64\nWL 8\nEX 83,55\nMaav 80,168,64\nRa 152,26,2048\n"
"AD 85,85,82\nMaav 84,168,68\nD1 1,16,46,1024\nMaav 92,172,68\nRa 156,26,3072\nMaav 88,172,64\nAD 85,85,83\nCV 77,82,83\nMaav 96,176,64\nRa 160,24,8192\nSv 156,1,16,77,3072\n"
"Maav 100,176,68\nEX 80,56\nMaav 108,180,68\nRa 164,24,9216\nMaav 104,180,64\nWL 6\nEX 81,57\nMaav 112,184,64\nRa 168,24,10240\nAD 84,84,80\nMaav 116,184,68\nD1 1,16,46,2048\n"
"Maav 124,188,68\nRa 172,24,11264\nMaav 120,188,64\nAD 84,84,81\nCV 74,80,81\nCvav 32,128,192,4\nRa 176,26,8192\nMvav 32,132,200\nEX 82,58\nMvav 32,136,208\nRa 180,26,9216\n"
"Mvav 32,140,216\nWL 6\nEX 83,59\nCvav 36,140,220,8\nRa 184,26,10240\nAD 84,84,82\nMvav 36,136,212\nD1 1,16,46,3072\ns_add_u32 m0, s89, 32768\nMvav 36,132,204\nRa 188,26,11264\n"
"Mvav 36,128,196\nAD 84,84,83\nCV 75,82,83\nMvav 36,144,228\nRa 128,2,16384\nSv 160,1,16,78,0\nMvav 36,148,236\ns_add_u32 s46, s86, s88\nEX 80,60\nMvav 36,152,244\nRa 132,2,17408\n"
"Mvav 36,156,252\nWL 6\nEX 81,61\nMvav 32,156,248\nRa 136,2,18432\nAD 85,85,80\nMvav 32,152,240\nD1 1,16,46,0\nMvav 32,148,232\nRa 140,2,19456\nSv 112,1,16,49,0\nMvav 32,144,224\n"
"AD 85,85,81\nCV 78,80,81\nCvav 40,160,192,12\nRa 144,2,20480\nMvav 40,164,200\nEX 82,62\nMvav 40,168,208\nRa 148,2,21504\nMvav 40,172,216\nWL 6\nEX 83,63\nCvav 44,172,220,16\n"
"Ra 152,2,22528\nAD 85,85,82\nMvav 44,168,212\nD1 1,16,46,1024\nMvav 44,164,204\nRa 156,2,23552\nMvav 44,160,196\nAD 85,85,83\nCV 79,82,83\nMvav 44,176,228\nRa 160,2,24576\n"
"v_max_f32 v90, v84, v85\nSv 164,1,16,78,1024\nMvav 44,180,236\ns_add_u32 s88, s88, 32768\nEX 80,32\nMvav 44,184,244\nRa 164,2,25600\nv_cmp_lt_f32 vcc, s63, v90\nMvav 44,188,252\n"
"WL 8\nEX 81,33\nMvav 40,188,248\nRa 168,2,26624\ns_cbranch_vccnz .T33\n.T34:\nMvav 40,184,240\nD1 1,16,46,2048\nMvav 40,180,232\nRa 172,2,27648\nAD 86,86,84\nMvav 40,176,224\n"
"AD 84,80,81\nCV 64,80,81\nMaav 0,128,72\nRa 176,2,28672\nMaav 4,128,76\nEX 82,34\nMaav 12,132,76\nRa 180,2,29696\nMaav 8,132,72\ns_waitcnt vmcnt(19) lgkmcnt(8)\nEX 83,35\n"
"Maav 16,136,72\nRa 184,2,30720\nAD 84,84,82\nMaav 20,136,76\nD1 1,16,46,3072\ns_add_u32 m0, s89, 49152\nMaav 28,140,76\nRa 188,2,31744\ns_barrier\nMaav 24,140,72\nAD 84,84,83\n"
"CV 65,82,83\nMaav 32,144,72\nRa 128,24,32768\nSv 168,1,16,78,2048\nMaav 36,144,76\ns_add_u32 s46, s87, s88\nEX 80,36\nMaav 44,148,76\nRa 132,24,33792\nMaav 40,148,72\nWL 8\n"
"EX 81,37\nMaav 48,152,72\nRa 136,24,34816\nAD 87,87,85\nMaav 52,152,76\nD1 1,16,46,0\nMaav 60,156,76\nRa 140,24,35840\nMaav 56,156,72\nAD 85,80,81\nCV 68,80,81\nMaav 64,160,72\n"
"Ra 144,26,32768\nMaav 68,160,76\nEX 82,38\nMaav 76,164,76\nRa 148,26,33792\nMaav 72,164,72\nWL 8\nEX 83,39\nMaav 80,168,72\nRa 152,26,34816\nAD 85,85,82\nMaav 84,168,76\n"
"D1 1,16,46,1024\nMaav 92,172,76\nRa 156,26,35840\nMaav 88,172,72\nAD 85,85,83\nCV 69,82,83\nMaav 96,176,72\nRa 160,24,40960\nSv 172,1,16,78,3072\nMaav 100,176,76\n"
"s_xor_b32 s89, s89, 0x10000\nEX 80,40\nMaav 108,180,76\nRa 164,24,41984\nMaav 104,180,72\nWL 6\nEX 81,41\nMaav 112,184,72\nRa 168,24,43008\nAD 84,84,80\nMaav 116,184,76\n"
"D1 1,16,46,2048\nMaav 124,188,76\nRa 172,24,44032\nMaav 120,188,72\nAD 84,84,81\nCV 66,80,81\nCvav 48,128,192,4\nRa 176,26,40960\nMvav 48,132,200\nEX 82,42\nMvav 48,136,208\n"
"Ra 180,26,41984\nMvav 48,140,216\nWL 6\nEX 83,43\nv_cmp_ne_u32 vcc, s15, v27\ns_nop 0\ns_cbranch_vccnz .S44\n.S45:\nv_and_b32 v27, 0x70, v1\n"
"buffer_load_dword v27, v27, s[16:19], s61 offen offset:12 sc0 sc1\nCvav 52,140,220,8\nRa 184,26,43008\nAD 84,84,82\nMvav 52,136,212\nD1 1,16,46,3072\ns_add_u32 m0, s89, 0\n"
"Mvav 52,132,204\nRa 188,26,44032\nMvav 52,128,196\nAD 84,84,83\nCV 67,82,83\nMvav 52,144,228\nRa 128,2,49152\ns_add_u32 s78, s78, s55\nSv 176,1,16,78,0\nMvav 52,148,236\n"
"s_add_u32 s46, s86, s88\nEX 80,44\nMvav 52,152,244\nRa 132,2,50176\nv_xor_b32 v24, 0x10000, v24\nMvav 52,156,252\nWL 6\nEX 81,45\nMvav 48,156,248\nRa 136,2,51200\nAD 85,85,80\n"
"Mvav 48,152,240\nD1 1,16,46,0\nv_lshrrev_b32 v92, 8, v1\nMvav 48,148,232\nRa 140,2,52224\nSv 112,1,16,49,1024\nMvav 48,144,224\nAD 85,85,81\nCV 70,80,81\nCvav 56,160,192,12\n"
"Ra 144,2,53248\nv_bfe_u32 v93, v1, 6, 2\nMvav 56,164,200\ns_add_u32 s49, s49, 131072\nEX 82,46\nMvav 56,168,208\nRa 148,2,54272\nv_xor_b32 v26, 0x10000, v26\nMvav 56,172,216\n"
"WL 6\nEX 83,47\nCvav 60,172,220,16\nRa 152,2,55296\nAD 85,85,82\nMvav 60,168,212\nD1 1,16,46,1024\nv_cmp_eq_u32 vcc, v92, v93\nv_and_b32 v92, 16, v1\nv_mov_b32 v93, 0x3f80\n"
"v_cndmask_b32 v93, 0, v93, vcc\nMvav 60,164,204\nRa 156,2,56320\nMvav 60,160,196\n.W549:\ns_cbranch_execz .W553\nAD 85,85,83\nCV 71,82,83\nMvav 60,176,228\nRa 160,2,57344\n"
"v_max_f32 v90, v84, v85\nSv 180,1,16,78,1024\nMvav 60,180,236\ns_add_u32 s88, s88, 32768\nEX 80,48\nMvav 60,184,244\nRa 164,2,58368\nv_cmp_lt_f32 vcc, s63, v90\nMvav 60,188,252\n"
"WL 8\nEX 81,49\nMvav 56,188,248\nRa 168,2,59392\ns_cbranch_vccnz .T35\n.T36:\nMvav 56,184,240\nD1 1,16,46,2048\nv_lshlrev_b32 v93, v92, v93\nMvav 56,180,232\nRa 172,2,60416\n"
"AD 86,86,84\nMvav 56,176,224\nAD 84,80,81\nCV 72,80,81\nMaav 0,128,64\nRa 176,2,61440\nv_and_b32 v92, 32, v1\nMaav 4,128,68\nEX 82,50\nMaav 12,132,68\nRa 180,2,62464\n"
"v_cmp_eq_u32 vcc, 0, v92\nv_mov_b32 v128, 0\nv_mov_b32 v129, 0\nv_cndmask_b32 v130, 0, v93, vcc\nMaav 8,132,64\ns_waitcnt vmcnt(20) lgkmcnt(8)\nEX 83,51\nMaav 16,136,64\n"
"Ra 184,2,63488\nAD 84,84,82\nMaav 20,136,68\nD1 1,16,46,3072\ns_add_u32 m0, s89, 16384\nMaav 28,140,68\nRa 188,2,64512\ns_barrier\nMaav 24,140,64\nAD 84,84,83\nCV 73,82,83\n"
"Maav 32,144,64\nRa 128,24,0\nSv 184,1,16,78,2048\nMaav 36,144,68\ns_add_u32 s46, s87, s88\nEX 80,52\nMaav 44,148,68\nRa 132,24,1024\nv_xor_b32 v2, 0x10000, v2\nMaav 40,148,64\n"
"WL 8\nEX 81,53\nMaav 48,152,64\nRa 136,24,2048\nAD 87,87,85\nMaav 52,152,68\nD1 1,16,46,0\nv_sub_u32 v131, v93, v130\nMaav 60,156,68\nRa 140,24,3072\nMaav 56,156,64\nAD 85,80,81\n"
"CV 76,80,81\nMaav 64,160,64\nRa 144,26,0\nv_mov_b32 v132, 0\nMaav 68,160,68\nEX 82,54\nMaav 76,164,68\nRa 148,26,1024\nMaav 72,164,64\nWL 8\nEX 83,55\nMaav 80,168,64\n"
"Ra 152,26,2048\nAD 85,85,82\nMaav 84,168,68\nD1 1,16,46,1024\nMaav 92,172,68\nRa 156,26,3072\nMaav 88,172,64\nAD 85,85,83\nCV 77,82,83\nMaav 96,176,64\nRa 160,24,8192\n"
"Sv 188,1,16,78,3072\nMaav 100,176,68\nEX 80,56\nMaav 108,180,68\nRa 164,24,9216\nv_mov_b32 v133, 0\nMaav 104,180,64\nWL 6\nEX 81,57\nMaav 112,184,64\nRa 168,24,10240\nAD 84,84,80\n"
"Maav 116,184,68\nD1 1,16,46,2048\nMaav 124,188,68\nRa 172,24,11264\nMaav 120,188,64\nAD 84,84,81\nCV 74,80,81\ns_mov_b32 s99, 11\ns_sub_u32 s90, s90, 3\n.p2align 6\n.A60:\n"
"Cvav 32,128,192,4\nRa 176,26,8192\nMvav 32,132,200\nEX 82,58\nMvav 32,136,208\nRa 180,26,9216\nMvav 32,140,216\nWL 6\nEX 83,59\nCvav 36,140,220,8\nRa 184,26,10240\nAD 84,84,82\n"
"Mvav 36,136,212\nD1 1,16,46,3072\ns_add_u32 m0, s89, 32768\nMvav 36,132,204\nRa 188,26,11264\nMvav 36,128,196\nAD 84,84,83\nCV 75,82,83\nMvav 36,144,228\nRa 128,2,16384\n"
"Mvav 36,148,236\ns_add_u32 s46, s86, s88\nEX 80,60\nMvav 36,152,244\nRa 132,2,17408\nMvav 36,156,252\nWL 6\nEX 81,61\nMvav 32,156,248\nRa 136,2,18432\nAD 85,85,80\n"
"Mvav 32,152,240\nD1 1,16,46,0\nMvav 32,148,232\nRa 140,2,19456\nSv 112,1,16,49,0\nMvav 32,144,224\nAD 85,85,81\nCV 78,80,81\nCvav 40,160,192,12\nRa 144,2,20480\nMvav 40,164,200\n"
"EX 82,62\nMvav 40,168,208\nRa 148,2,21504\nMvav 40,172,216\nWL 6\nEX 83,63\nCvav 44,172,220,16\nRa 152,2,22528\nAD 85,85,82\nMvav 44,168,212\nD1 1,16,46,1024\nMvav 44,164,204\n"
"Ra 156,2,23552\nMvav 44,160,196\nAD 85,85,83\nCV 79,82,83\nMvav 44,176,228\nRa 160,2,24576\nv_max_f32 v90, v84, v85\nMvav 44,180,236\ns_add_u32 s88, s88, 32768\nEX 80,32\n"
"Mvav 44,184,244\nRa 164,2,25600\nv_cmp_lt_f32 vcc, s63, v90\nMvav 44,188,252\nWL 8\nEX 81,33\nMvav 40,188,248\nRa 168,2,26624\ns_cbranch_vccnz .T64\n.T65:\nMvav 40,184,240\n"
"D1 1,16,46,2048\nMvav 40,180,232\nRa 172,2,27648\nAD 86,86,84\nMvav 40,176,224\nAD 84,80,81\nCV 64,80,81\nMaav 0,128,72\nRa 176,2,28672\nMaav 4,128,76\nEX 82,34\nMaav 12,132,76\n"
"Ra 180,2,29696\nMaav 8,132,72\ns_waitcnt vmcnt(13) lgkmcnt(8)\nEX 83,35\nMaav 16,136,72\nRa 184,2,30720\nAD 84,84,82\nMaav 20,136,76\nD1 1,16,46,3072\ns_add_u32 m0, s89, 49152\n"
"Maav 28,140,76\nRa 188,2,31744\ns_barrier\nMaav 24,140,72\nAD 84,84,83\nCV 65,82,83\nMaav 32,144,72\nRa 128,24,32768\nMaav 36,144,76\ns_add_u32 s46, s87, s88\nEX 80,36\n"
"Maav 44,148,76\nRa 132,24,33792\nMaav 40,148,72\nWL 8\nEX 81,37\nMaav 48,152,72\nRa 136,24,34816\nAD 87,87,85\nMaav 52,152,76\nD1 1,16,46,0\nMaav 60,156,76\nRa 140,24,35840\n"
"Maav 56,156,72\nAD 85,80,81\nCV 68,80,81\nMaav 64,160,72\nRa 144,26,32768\nMaav 68,160,76\nEX 82,38\nMaav 76,164,76\nRa 148,26,33792\nMaav 72,164,72\nWL 8\nEX 83,39\n"
"Maav 80,168,72\nRa 152,26,34816\nAD 85,85,82\nMaav 84,168,76\nD1 1,16,46,1024\nMaav 92,172,76\nRa 156,26,35840\nMaav 88,172,72\nAD 85,85,83\nCV 69,82,83\nMaav 96,176,72\n"
"Ra 160,24,40960\nMaav 100,176,76\ns_xor_b32 s89, s89, 0x10000\nEX 80,40\nMaav 108,180,76\nRa 164,24,41984\nMaav 104,180,72\nWL 6\nEX 81,41\nMaav 112,184,72\nRa 168,24,43008\n"
"AD 84,84,80\nMaav 116,184,76\nD1 1,16,46,2048\nMaav 124,188,76\nRa 172,24,44032\nMaav 120,188,72\nAD 84,84,81\nCV 66,80,81\nCvav 48,128,192,4\nRa 176,26,40960\nMvav 48,132,200\n"
"EX 82,42\nMvav 48,136,208\nRa 180,26,41984\nMvav 48,140,216\nWL 6\nEX 83,43\nCvav 52,140,220,8\nRa 184,26,43008\nAD 84,84,82\nMvav 52,136,212\nD1 1,16,46,3072\n"
"s_add_u32 m0, s89, 0\nMvav 52,132,204\nRa 188,26,44032\nMvav 52,128,196\nAD 84,84,83\nCV 67,82,83\nMvav 52,144,228\nRa 128,2,49152\nMvav 52,148,236\ns_add_u32 s46, s86, s88\n"
"EX 80,44\nMvav 52,152,244\nRa 132,2,50176\nv_xor_b32 v24, 0x10000, v24\nMvav 52,156,252\nWL 6\nEX 81,45\nMvav 48,156,248\nRa 136,2,51200\nAD 85,85,80\nMvav 48,152,240\n"
"D1 1,16,46,0\nMvav 48,148,232\nRa 140,2,52224\nSv 112,1,16,49,1024\nMvav 48,144,224\nAD 85,85,81\nCV 70,80,81\nCvav 56,160,192,12\nRa 144,2,53248\nMvav 56,164,200\n"
"s_add_u32 s49, s49, 131072\nEX 82,46\nMvav 56,168,208\nRa 148,2,54272\nv_xor_b32 v26, 0x10000, v26\nMvav 56,172,216\nWL 6\nEX 83,47\nCvav 60,172,220,16\nRa 152,2,55296\n"
"AD 85,85,82\nMvav 60,168,212\nD1 1,16,46,1024\nMvav 60,164,204\nRa 156,2,56320\nMvav 60,160,196\nAD 85,85,83\nCV 71,82,83\nMvav 60,176,228\nRa 160,2,57344\n"
"v_max_f32 v90, v84, v85\nMvav 60,180,236\ns_add_u32 s88, s88, 32768\nEX 80,48\nMvav 60,184,244\nRa 164,2,58368\nv_cmp_lt_f32 vcc, s63, v90\nMvav 60,188,252\nWL 8\nEX 81,49\n"
"Mvav 56,188,248\nRa 168,2,59392\ns_cbranch_vccnz .T66\n.T67:\nMvav 56,184,240\nD1 1,16,46,2048\nMvav 56,180,232\nRa 172,2,60416\nAD 86,86,84\nMvav 56,176,224\nAD 84,80,81\n"
"CV 72,80,81\nMaav 0,128,64\nRa 176,2,61440\nMaav 4,128,68\nEX 82,50\nMaav 12,132,68\nRa 180,2,62464\nMaav 8,132,64\ns_waitcnt vmcnt(13) lgkmcnt(8)\nEX 83,51\nMaav 16,136,64\n"
"Ra 184,2,63488\nAD 84,84,82\nMaav 20,136,68\nD1 1,16,46,3072\ns_add_u32 m0, s89, 16384\nMaav 28,140,68\nRa 188,2,64512\ns_barrier\nMaav 24,140,64\nAD 84,84,83\nCV 73,82,83\n"
"Maav 32,144,64\nRa 128,24,0\nMaav 36,144,68\ns_add_u32 s46, s87, s88\nEX 80,52\nMaav 44,148,68\nRa 132,24,1024\nv_xor_b32 v2, 0x10000, v2\nMaav 40,148,64\nWL 8\nEX 81,53\n"
"Maav 48,152,64\nRa 136,24,2048\nAD 87,87,85\nMaav 52,152,68\nD1 1,16,46,0\nMaav 60,156,68\nRa 140,24,3072\nMaav 56,156,64\nAD 85,80,81\nCV 76,80,81\nMaav 64,160,64\nRa 144,26,0\n"
"Maav 68,160,68\nEX 82,54\nMaav 76,164,68\nRa 148,26,1024\nMaav 72,164,64\nWL 8\nEX 83,55\nMaav 80,168,64\nRa 152,26,2048\nAD 85,85,82\nMaav 84,168,68\nD1 1,16,46,1024\n"
"Maav 92,172,68\nRa 156,26,3072\nMaav 88,172,64\nAD 85,85,83\nCV 77,82,83\nMaav 96,176,64\nRa 160,24,8192\nMaav 100,176,68\ns_sub_u32 s90, s90, 1\nEX 80,56\nMaav 108,180,68\n"
"Ra 164,24,9216\ns_cbranch_scc1 .E62\n.E63:\nMaav 104,180,64\nWL 6\nEX 81,57\nMaav 112,184,64\nRa 168,24,10240\nAD 84,84,80\nMaav 116,184,68\nD1 1,16,46,2048\n"
"s_sub_u32 s99, s99, 1\nMaav 124,188,68\nRa 172,24,11264\nMaav 120,188,64\nAD 84,84,81\nCV 74,80,81\ns_cbranch_scc0 .A60\ns_branch .A68\n.E62:\ns_swappc_b64 s[80:81], s[8:9]\n"
"s_branch .E63\n.HD13:\nbuffer_store_dword v25, off, s[16:19], s54 offset:4 sc0 sc1\nv_cmp_ne_u32 vcc, s15, v27\ns_nop 0\ns_cbranch_vccnz .S47\n.S48:\nv_and_b32 v27, 0x70, v1\n"
"buffer_load_dword v27, v27, s[16:19], s61 offen offset:8 sc0 sc1\ns_mov_b32 s90, 1\ns_swappc_b64 s[8:9], s[80:81]\nv_cmp_ne_u32 vcc, s15, v27\ns_nop 0\ns_cbranch_vccnz .S50\n"
".S51:\nv_and_b32 v27, 0xf0, v1\nbuffer_load_dword v27, v27, s[16:19], s61 offen offset:4 sc0 sc1\ns_mov_b32 s90, 1\ns_swappc_b64 s[8:9], s[80:81]\nv_cmp_ne_u32 vcc, s15, v27\n"
"s_nop 0\ns_cbranch_vccnz .S53\n.S54:\ns_mov_b32 s90, s91\ns_swappc_b64 s[8:9], s[80:81]\ns_branch .FF56\n.A68:\nCvav 32,128,192,4\nRa 176,26,8192\nMvav 32,132,200\nEX 82,58\n"
"Mvav 32,136,208\nRa 180,26,9216\nMvav 32,140,216\nWL 6\nEX 83,59\nCvav 36,140,220,8\nRa 184,26,10240\nAD 84,84,82\nMvav 36,136,212\nD1 1,16,46,3072\ns_add_u32 m0, s89, 32768\n"
"Mvav 36,132,204\nRa 188,26,11264\nMvav 36,128,196\nAD 84,84,83\nCV 75,82,83\ns_mov_b32 s94, s96\ns_add_u32 s95, s96, 65536\nv_and_b32 v108, 0xf0, v1\nMvav 36,144,228\n"
"Ra 128,2,16384\nMvav 36,148,236\ns_add_u32 s46, s86, s88\nEX 80,60\nMvav 36,152,244\nRa 132,2,17408\nMvav 36,156,252\nWL 6\nEX 81,61\nMvav 32,156,248\nRa 136,2,18432\nAD 85,85,80\n"
"Mvav 32,152,240\nD1 1,16,46,0\nMvav 32,148,232\nRa 140,2,19456\nSv 112,1,16,49,0\nMvav 32,144,224\nAD 85,85,81\nCV 78,80,81\n"
"buffer_load_dwordx4 v[100:103], v108, s[16:19], s65 offen offset:0 sc0 sc1\nbuffer_load_dwordx4 v[104:107], v108, s[16:19], s65 offen offset:256 sc0 sc1\nCvav 40,160,192,12\n"
"Ra 144,2,20480\nMvav 40,164,200\nEX 82,62\nMvav 40,168,208\nRa 148,2,21504\nMvav 40,172,216\nWL 6\nEX 83,63\nCvav 44,172,220,16\nRa 152,2,22528\nAD 85,85,82\nMvav 44,168,212\n"
"D1 1,16,46,1024\nMvav 44,164,204\nRa 156,2,23552\nMvav 44,160,196\nAD 85,85,83\nCV 79,82,83\nMvav 44,176,228\nRa 160,2,24576\nv_max_f32 v90, v84, v85\nMvav 44,180,236\n"
"s_add_u32 s88, s88, 32768\nEX 80,32\nMvav 44,184,244\nRa 164,2,25600\nv_cmp_lt_f32 vcc, s63, v90\nMvav 44,188,252\nWL 8\nEX 81,33\nMvav 40,188,248\nRa 168,2,26624\n"
"s_cbranch_vccnz .T70\n.T71:\nMvav 40,184,240\nD1 1,16,46,2048\nMvav 40,180,232\nRa 172,2,27648\nAD 86,86,84\nMvav 40,176,224\nAD 84,80,81\nCV 64,80,81\nMaav 0,128,72\n"
"Ra 176,2,28672\nMaav 4,128,76\nEX 82,34\nMaav 12,132,76\nRa 180,2,29696\nMaav 8,132,72\ns_waitcnt vmcnt(15) lgkmcnt(8)\nEX 83,35\nMaav 16,136,72\nRa 184,2,30720\nAD 84,84,82\n"
"Maav 20,136,76\nD1 1,16,46,3072\ns_add_u32 m0, s89, 49152\nMaav 28,140,76\nRa 188,2,31744\ns_barrier\nMaav 24,140,72\nAD 84,84,83\nCV 65,82,83\nMaav 32,144,72\nRa 128,24,32768\n"
"Maav 36,144,76\ns_add_u32 s46, s87, s88\nEX 80,36\nMaav 44,148,76\nRa 132,24,33792\nMaav 40,148,72\nWL 8\nEX 81,37\nMaav 48,152,72\nRa 136,24,34816\nAD 87,87,85\nMaav 52,152,76\n"
"D1 1,16,46,0\nMaav 60,156,76\nRa 140,24,35840\nMaav 56,156,72\n.W550:\ns_cbranch_execz .W554\nAD 85,80,81\nCV 68,80,81\nMaav 64,160,72\nRa 144,26,32768\nMaav 68,160,76\nEX 82,38\n"
"Maav 76,164,76\nRa 148,26,33792\nMaav 72,164,72\nWL 8\nEX 83,39\nMaav 80,168,72\nRa 152,26,34816\nAD 85,85,82\nMaav 84,168,76\nD1 1,16,46,1024\nMaav 92,172,76\nRa 156,26,35840\n"
"Maav 88,172,72\nAD 85,85,83\nCV 69,82,83\nMaav 96,176,72\nRa 160,24,40960\nMaav 100,176,76\ns_xor_b32 s89, s89, 0x10000\nEX 80,40\nMaav 108,180,76\nRa 164,24,41984\n"
"Maav 104,180,72\nWL 6\nEX 81,41\nMaav 112,184,72\nRa 168,24,43008\nAD 84,84,80\nMaav 116,184,76\nD1 1,16,46,2048\nMaav 124,188,76\nRa 172,24,44032\nMaav 120,188,72\nAD 84,84,81\n"
"CV 66,80,81\nCvav 48,128,192,4\nRa 176,26,40960\nMvav 48,132,200\ns_mov_b32 s86, s94\nEX 82,42\nMvav 48,136,208\nRa 180,26,41984\nMvav 48,140,216\nWL 6\nEX 83,43\n"
"Cvav 52,140,220,8\nRa 184,26,43008\nAD 84,84,82\nMvav 52,136,212\nD1 1,16,46,3072\ns_add_u32 m0, s89, 0\nMvav 52,132,204\nRa 188,26,44032\nMvav 52,128,196\nAD 84,84,83\n"
"CV 67,82,83\nMvav 52,144,228\nRa 128,2,49152\nMvav 52,148,236\ns_add_u32 s46, s86, s88\nEX 80,44\nMvav 52,152,244\nRa 132,2,50176\nv_xor_b32 v24, 0x10000, v24\nMvav 52,156,252\n"
"WL 6\nEX 81,45\nMvav 48,156,248\nRa 136,2,51200\nAD 85,85,80\nMvav 48,152,240\nD1 1,16,46,0\nMvav 48,148,232\nRa 140,2,52224\nSv 112,1,16,49,1024\nMvav 48,144,224\nAD 85,85,81\n"
"CV 70,80,81\nWV 9\nv_xor_b32 v109, s15, v102\nv_xor_b32 v110, s15, v106\nv_or_b32 v109, v109, v110\nv_cmp_ne_u32 vcc, 0, v109\ns_nop 3\ns_cmp_lg_u64 vcc, 0\n"
"s_cselect_b32 s67, 1, 0\ns_cselect_b32 s6, 0x40000000, s66\nCvav 56,160,192,12\nRa 144,2,53248\ns_add_u32 s5, s6, 0\nMvav 56,164,200\ns_add_u32 s49, s49, 131072\nEX 82,46\n"
"Mvav 56,168,208\nRa 148,2,54272\nv_xor_b32 v26, 0x10000, v26\nbuffer_load_dwordx4 a[192:195], v1, s[16:19], s5 offen offset:0 sc1\nMvav 56,172,216\nWL 6\nEX 83,47\n"
"Cvav 60,172,220,16\nRa 152,2,55296\nAD 85,85,82\nMvav 60,168,212\nD1 1,16,46,1024\nMvav 60,164,204\nRa 156,2,56320\nMvav 60,160,196\nAD 85,85,83\nCV 71,82,83\nMvav 60,176,228\n"
"Ra 160,2,57344\nv_max_f32 v90, v84, v85\nMvav 60,180,236\ns_add_u32 s88, s88, 32768\nEX 80,48\nMvav 60,184,244\nRa 164,2,58368\nv_cmp_lt_f32 vcc, s63, v90\nMvav 60,188,252\nWL 8\n"
"EX 81,49\nMvav 56,188,248\nRa 168,2,59392\ns_cbranch_vccnz .T72\n.T73:\nbuffer_load_dwordx4 a[196:199], v1, s[16:19], s5 offen offset:1024 sc1\nMvav 56,184,240\nD1 1,16,46,2048\n"
"Mvav 56,180,232\nRa 172,2,60416\nAD 86,86,84\nMvav 56,176,224\nAD 84,80,81\nCV 72,80,81\nMaav 0,128,64\nRa 176,2,61440\nMaav 4,128,68\nEX 82,50\nMaav 12,132,68\nRa 180,2,62464\n"
"Maav 8,132,64\nWL 8\nEX 83,51\nMaav 16,136,64\nRa 184,2,63488\nAD 84,84,82\nbuffer_load_dwordx4 a[200:203], v1, s[16:19], s5 offen offset:2048 sc1\nMaav 20,136,68\n"
"D1 1,16,46,3072\ns_add_u32 m0, s89, 16384\nMaav 28,140,68\nRa 188,2,64512\ns_barrier\nMaav 24,140,64\nAD 84,84,83\nCV 73,82,83\nMaav 32,144,64\nRa 128,24,0\nMaav 36,144,68\n"
"s_add_u32 s46, s87, s88\nEX 80,52\nMaav 44,148,68\nRa 132,24,1024\nv_xor_b32 v2, 0x10000, v2\nMaav 40,148,64\nWL 8\nEX 81,53\nMaav 48,152,64\nRa 136,24,2048\nAD 87,87,85\n"
"buffer_load_dwordx4 a[204:207], v1, s[16:19], s5 offen offset:3072 sc1\nMaav 52,152,68\nD1 1,16,46,0\nMaav 60,156,68\nRa 140,24,3072\nMaav 56,156,64\nAD 85,80,81\nCV 76,80,81\n"
"Maav 64,160,64\nRa 144,26,0\nMaav 68,160,68\nEX 82,54\nMaav 76,164,68\nRa 148,26,1024\ns_add_u32 s5, s6, 4096\nMaav 72,164,64\nWL 8\nEX 83,55\nMaav 80,168,64\nRa 152,26,2048\n"
"AD 85,85,82\nbuffer_load_dwordx4 a[208:211], v1, s[16:19], s5 offen offset:0 sc1\nMaav 84,168,68\nD1 1,16,46,1024\nMaav 92,172,68\nRa 156,26,3072\nMaav 88,172,64\nAD 85,85,83\n"
"CV 77,82,83\nMaav 96,176,64\nRa 160,24,8192\nMaav 100,176,68\nEX 80,56\nMaav 108,180,68\nRa 164,24,9216\nMaav 104,180,64\nWL 6\nEX 81,57\nMaav 112,184,64\nRa 168,24,10240\n"
"AD 84,84,80\nbuffer_load_dwordx4 a[212:215], v1, s[16:19], s5 offen offset:1024 sc1\nMaav 116,184,68\nD1 1,16,46,2048\nMaav 124,188,68\nRa 172,24,11264\nMaav 120,188,64\n"
"AD 84,84,81\nCV 74,80,81\nCvav 32,128,192,4\nRa 176,26,8192\nMvav 32,132,200\nEX 82,58\nMvav 32,136,208\nRa 180,26,9216\nMvav 32,140,216\nWL 6\nEX 83,59\nCvav 36,140,220,8\n"
"Ra 184,26,10240\nAD 84,84,82\nbuffer_load_dwordx4 a[216:219], v1, s[16:19], s5 offen offset:2048 sc1\nMvav 36,136,212\nD1 1,16,46,3072\ns_add_u32 m0, s89, 32768\nMvav 36,132,204\n"
"Ra 188,26,11264\nMvav 36,128,196\nAD 84,84,83\nCV 75,82,83\nMvav 36,144,228\nRa 128,2,16384\nMvav 36,148,236\ns_add_u32 s46, s86, s88\nEX 80,60\nMvav 36,152,244\nRa 132,2,17408\n"
"Mvav 36,156,252\nWL 6\nEX 81,61\nMvav 32,156,248\nRa 136,2,18432\nAD 85,85,80\nbuffer_load_dwordx4 a[220:223], v1, s[16:19], s5 offen offset:3072 sc1\nMvav 32,152,240\n"
"D1 1,16,46,0\nMvav 32,148,232\nRa 140,2,19456\nMvav 32,144,224\nAD 85,85,81\nCV 78,80,81\nCvav 40,160,192,12\nRa 144,2,20480\nMvav 40,164,200\ns_mov_b32 s87, s95\nEX 82,62\n"
"Mvav 40,168,208\nRa 148,2,21504\ns_add_u32 s5, s6, 16384\nMvav 40,172,216\nWL 6\nEX 83,63\nCvav 44,172,220,16\nRa 152,2,22528\nAD 85,85,82\n"
"buffer_load_dwordx4 a[224:227], v1, s[16:19], s5 offen offset:0 sc1\nMvav 44,168,212\nD1 1,16,46,1024\nMvav 44,164,204\nRa 156,2,23552\nMvav 44,160,196\nAD 85,85,83\nCV 79,82,83\n"
"Mvav 44,176,228\nRa 160,2,24576\nv_max_f32 v90, v84, v85\nMvav 44,180,236\ns_add_u32 s88, s88, 32768\nEX 80,32\nMvav 44,184,244\nRa 164,2,25600\nv_cmp_lt_f32 vcc, s63, v90\n"
"Mvav 44,188,252\nWL 8\nEX 81,33\nMvav 40,188,248\nRa 168,2,26624\ns_cbranch_vccnz .T75\n.T76:\nbuffer_load_dwordx4 a[228:231], v1, s[16:19], s5 offen offset:1024 sc1\n"
"Mvav 40,184,240\nD1 1,16,46,2048\nMvav 40,180,232\nRa 172,2,27648\nAD 86,86,84\nMvav 40,176,224\nAD 84,80,81\nCV 64,80,81\nMaav 0,128,72\nRa 176,2,28672\nMaav 4,128,76\nEX 82,34\n"
"Maav 12,132,76\nRa 180,2,29696\nMaav 8,132,72\ns_waitcnt vmcnt(22) lgkmcnt(8)\nEX 83,35\nMaav 16,136,72\nRa 184,2,30720\nAD 84,84,82\n"
"buffer_load_dwordx4 a[232:235], v1, s[16:19], s5 offen offset:2048 sc1\nMaav 20,136,76\nD1 1,16,46,3072\ns_add_u32 m0, s89, 49152\nMaav 28,140,76\nRa 188,2,31744\ns_barrier\n"
"Maav 24,140,72\nAD 84,84,83\nCV 65,82,83\nMaav 32,144,72\nRa 128,24,32768\nMaav 36,144,76\ns_add_u32 s46, s87, s88\nEX 80,36\nMaav 44,148,76\nRa 132,24,33792\nMaav 40,148,72\n"
"WL 8\nEX 81,37\nMaav 48,152,72\nRa 136,24,34816\nAD 87,87,85\nbuffer_load_dwordx4 a[236:239], v1, s[16:19], s5 offen offset:3072 sc1\nMaav 52,152,76\nD1 1,16,46,0\nMaav 60,156,76\n"
"Ra 140,24,35840\nMaav 56,156,72\nAD 85,80,81\nCV 68,80,81\nMaav 64,160,72\nRa 144,26,32768\nMaav 68,160,76\nEX 82,38\nMaav 76,164,76\nRa 148,26,33792\ns_add_u32 s5, s6, 20480\n"
"Maav 72,164,72\nWL 8\nEX 83,39\nMaav 80,168,72\nRa 152,26,34816\nAD 85,85,82\nbuffer_load_dwordx4 a[240:243], v1, s[16:19], s5 offen offset:0 sc1\nMaav 84,168,76\nD1 1,16,46,1024\n"
"Maav 92,172,76\nRa 156,26,35840\nMaav 88,172,72\nAD 85,85,83\nCV 69,82,83\nMaav 96,176,72\nRa 160,24,40960\nMaav 100,176,76\ns_xor_b32 s89, s89, 0x10000\nEX 80,40\n"
"Maav 108,180,76\nRa 164,24,41984\nMaav 104,180,72\nWL 6\nEX 81,41\nMaav 112,184,72\nRa 168,24,43008\nAD 84,84,80\n"
"buffer_load_dwordx4 a[244:247], v1, s[16:19], s5 offen offset:1024 sc1\nMaav 116,184,76\nD1 1,16,46,2048\nMaav 124,188,76\nRa 172,24,44032\nMaav 120,188,72\nAD 84,84,81\n"
"CV 66,80,81\nCvav 48,128,192,4\nRa 176,26,40960\nMvav 48,132,200\nEX 82,42\nMvav 48,136,208\nRa 180,26,41984\nMvav 48,140,216\nWL 6\nEX 83,43\nCvav 52,140,220,8\nRa 184,26,43008\n"
"AD 84,84,82\nbuffer_load_dwordx4 a[248:251], v1, s[16:19], s5 offen offset:2048 sc1\nMvav 52,136,212\nD1 1,16,46,3072\ns_add_u32 m0, s89, 0\nMvav 52,132,204\nRa 188,26,44032\n"
"Mvav 52,128,196\nAD 84,84,83\nCV 67,82,83\nMvav 52,144,228\nRa 128,2,49152\nMvav 52,148,236\ns_add_u32 s46, s86, s88\nEX 80,44\nMvav 52,152,244\nRa 132,2,50176\n"
"v_xor_b32 v24, 0x10000, v24\nMvav 52,156,252\nWL 6\nEX 81,45\nMvav 48,156,248\nRa 136,2,51200\nAD 85,85,80\nbuffer_load_dwordx4 a[252:255], v1, s[16:19], s5 offen offset:3072 sc1\n"
"Mvav 48,152,240\nD1 1,16,46,0\nMvav 48,148,232\nRa 140,2,52224\nMvav 48,144,224\nAD 85,85,81\nCV 70,80,81\nCvav 56,160,192,12\nRa 144,2,53248\nMvav 56,164,200\nEX 82,46\n"
"Mvav 56,168,208\nRa 148,2,54272\nv_xor_b32 v26, 0x10000, v26\nMvav 56,172,216\nWL 6\nEX 83,47\nCvav 60,172,220,16\nRa 152,2,55296\nAD 85,85,82\nMvav 60,168,212\nD1 1,16,46,1024\n"
"Mvav 60,164,204\nRa 156,2,56320\nMvav 60,160,196\nAD 85,85,83\nCV 71,82,83\nMvav 60,176,228\nRa 160,2,57344\nv_max_f32 v90, v84, v85\nMvav 60,180,236\ns_add_u32 s88, s88, 32768\n"
"EX 80,48\nMvav 60,184,244\nRa 164,2,58368\nv_cmp_lt_f32 vcc, s63, v90\nMvav 60,188,252\nWL 8\nEX 81,49\nMvav 56,188,248\nRa 168,2,59392\ns_cbranch_vccnz .T77\n.T78:\n"
"Mvav 56,184,240\nD1 1,16,46,2048\nMvav 56,180,232\nRa 172,2,60416\nAD 86,86,84\nMvav 56,176,224\nAD 84,80,81\nCV 72,80,81\nMaav 0,128,64\nRa 176,2,61440\n"
"buffer_load_dwordx4 v[100:103], v108, s[16:19], s65 offen offset:0 sc0 sc1\nbuffer_load_dwordx4 v[104:107], v108, s[16:19], s65 offen offset:256 sc0 sc1\nMaav 4,128,68\nEX 82,50\n"
"Maav 12,132,68\nRa 180,2,62464\nMaav 8,132,64\ns_waitcnt vmcnt(22) lgkmcnt(8)\nEX 83,51\nMaav 16,136,64\nRa 184,2,63488\nAD 84,84,82\nMaav 20,136,68\nD1 1,16,46,3072\n"
"s_add_u32 m0, s89, 16384\nMaav 28,140,68\nRa 188,2,64512\ns_barrier\nMaav 24,140,64\nAD 84,84,83\nCV 73,82,83\nMaav 32,144,64\nRa 128,24,0\nMaav 36,144,68\n"
"s_add_u32 s46, s87, s88\nEX 80,52\nMaav 44,148,68\nRa 132,24,1024\nv_xor_b32 v2, 0x10000, v2\nMaav 40,148,64\nWL 8\nEX 81,53\nMaav 48,152,64\nRa 136,24,2048\nAD 87,87,85\n"
"Maav 52,152,68\nD1 1,16,46,0\nMaav 60,156,68\nRa 140,24,3072\nMaav 56,156,64\nAD 85,80,81\nCV 76,80,81\nMaav 64,160,64\nRa 144,26,0\nMaav 68,160,68\nEX 82,54\nMaav 76,164,68\n"
"Ra 148,26,1024\nMaav 72,164,64\nWL 8\nEX 83,55\nMaav 80,168,64\nRa 152,26,2048\nAD 85,85,82\nMaav 84,168,68\nD1 1,16,46,1024\nMaav 92,172,68\nRa 156,26,3072\nMaav 88,172,64\n"
"AD 85,85,83\nCV 77,82,83\nMaav 96,176,64\nRa 160,24,8192\nMaav 100,176,68\nEX 80,56\nMaav 108,180,68\nRa 164,24,9216\nMaav 104,180,64\nWL 10\nEX 81,57\nMaav 112,184,64\n"
"Ra 168,24,10240\nAD 84,84,80\nMaav 116,184,68\nD1 1,16,46,2048\nMaav 124,188,68\nRa 172,24,11264\nMaav 120,188,64\nAD 84,84,81\nCV 74,80,81\nWV 4\ns_cmp_lg_u32 s67, 0\n"
"s_cbranch_scc0 .E86\nv_xor_b32 v109, s15, v102\nv_xor_b32 v110, s15, v106\nv_or_b32 v109, v109, v110\nv_cmp_ne_u32 vcc, 0, v109\ns_nop 0\ns_cbranch_vccnz .S83\n.S84:\n"
"s_add_u32 s5, s66, 0\nbuffer_load_dwordx4 a[192:195], v1, s[16:19], s5 offen offset:0 sc1\nbuffer_load_dwordx4 a[196:199], v1, s[16:19], s5 offen offset:1024 sc1\n"
"buffer_load_dwordx4 a[200:203], v1, s[16:19], s5 offen offset:2048 sc1\nbuffer_load_dwordx4 a[204:207], v1, s[16:19], s5 offen offset:3072 sc1\ns_add_u32 s5, s66, 4096\n"
"buffer_load_dwordx4 a[208:211], v1, s[16:19], s5 offen offset:0 sc1\nbuffer_load_dwordx4 a[212:215], v1, s[16:19], s5 offen offset:1024 sc1\n"
"buffer_load_dwordx4 a[216:219], v1, s[16:19], s5 offen offset:2048 sc1\nbuffer_load_dwordx4 a[220:223], v1, s[16:19], s5 offen offset:3072 sc1\ns_add_u32 s5, s66, 16384\n"
"buffer_load_dwordx4 a[224:227], v1, s[16:19], s5 offen offset:0 sc1\nbuffer_load_dwordx4 a[228:231], v1, s[16:19], s5 offen offset:1024 sc1\n"
"buffer_load_dwordx4 a[232:235], v1, s[16:19], s5 offen offset:2048 sc1\nbuffer_load_dwordx4 a[236:239], v1, s[16:19], s5 offen offset:3072 sc1\ns_add_u32 s5, s66, 20480\n"
"buffer_load_dwordx4 a[240:243], v1, s[16:19], s5 offen offset:0 sc1\nbuffer_load_dwordx4 a[244:247], v1, s[16:19], s5 offen offset:1024 sc1\n"
"buffer_load_dwordx4 a[248:251], v1, s[16:19], s5 offen offset:2048 sc1\nbuffer_load_dwordx4 a[252:255], v1, s[16:19], s5 offen offset:3072 sc1\nWV 0\ns_mov_b32 s67, 0\n.E86:\n"
"v_cmp_lt_f32 vcc, 0, v101\nSU 20,100,88\nv_mul_f32 v101, 0x3e800000, v101\n.W551:\ns_cbranch_execz .WB21\nv_cndmask_b32 v20, 0, v20, vcc\nv_cmp_lt_f32 vcc, 0, v105\nSU 21,104,89\n"
"v_mul_f32 v105, 0x3e800000, v105\nv_cndmask_b32 v21, 0, v21, vcc\nv_or_b32 v91, v20, v21\nv_cmp_ne_u32 vcc, 0, v91\ns_nop 0\ns_cbranch_vccnz .D81\nAD 86,86,101\nAD 87,87,105\n"
".D82:\nMava 0,130,192\nMava 8,128,192\nEX 82,58\nMava 12,128,196\nMava 4,130,196\nEX 83,59\nMava 16,130,200\nAD 84,84,82\nMava 24,128,200\nD1 1,16,46,3072\nMava 28,128,204\n"
"s_add_u32 m0, s89, 32768\ns_add_u32 s46, s86, s88\nMava 20,130,204\nAD 84,84,83\nCV 75,82,83\nMava 32,130,208\nRa 128,2,16384\nv_xor_b32 v3, 0x10000, v2\n"
"v_add_u32 v0, 0x20000, v1\nMava 40,128,208\nEX 80,60\nMava 44,128,212\nRa 132,2,17408\ns_sub_u32 s94, s49, 1835008\nMava 36,130,212\nEX 81,61\nMava 48,130,216\nRa 136,2,18432\n"
"AD 85,85,80\ns_xor_b32 s92, s94, 0x202000\nMava 56,128,216\nD1 1,16,46,0\ns_lshl_b32 s89, s11, 12\nMava 60,128,220\nRa 140,2,19456\ns_lshl1_add_u32 s2, s12, 0\nMava 52,130,220\n"
"AD 85,85,81\nCV 78,80,81\nMava 64,130,224\nRa 144,2,20480\ns_lshl3_add_u32 s2, s2, s13\ns_lshl4_add_u32 s2, s2, 0xc048200\nMava 72,128,224\nEX 82,62\nMava 76,128,228\n"
"Ra 148,2,21504\ns_and_b32 s3, s72, 0x2000\nMava 68,130,228\nEX 83,63\nMava 80,130,232\nRa 152,2,22528\nAD 85,85,82\nMava 88,128,232\nD1 1,16,46,1024\ns_cmp_eq_u32 s11, 1\n"
"s_cselect_b32 s54, s2, s54\ns_cselect_b32 s3, s3, 0\nMava 92,128,236\nRa 156,2,23552\nv_xor_b32 v25, s3, v25\nMava 84,130,236\nAD 85,85,83\nCV 79,82,83\nMava 96,130,240\n"
"Ra 160,2,24576\nv_max_f32 v90, v84, v85\nMava 104,128,240\nv_cmp_lt_f32 vcc, s63, v90\nMava 108,128,244\nRa 164,2,25600\nMava 100,130,244\nWL 8\nMava 112,130,248\nRa 168,2,26624\n"
"Mava 120,128,248\nD1 1,16,46,2048\nMava 124,128,252\nRa 172,2,27648\nMava 116,130,252\ns_cbranch_vccnz .T87\n.T88:\nAD 86,86,84\nAD 87,87,85\nMaav 0,128,72\nRa 176,2,28672\n"
"v_mov_b32 v84, v86\nv_mov_b32 v85, v87\nMaav 4,128,76\nv_permlane32_swap_b32 v86, v84\nv_permlane32_swap_b32 v87, v85\nMaav 12,132,76\nRa 180,2,29696\nAD 86,86,84\nMaav 8,132,72\n"
"WL 8\nAD 87,87,85\nMaav 16,136,72\nRa 184,2,30720\nv_mov_b32 v84, v86\nv_mov_b32 v85, v87\nMaav 20,136,76\nD1 1,16,46,3072\nv_permlane16_swap_b32 v86, v84\nMaav 28,140,76\n"
"Ra 188,2,31744\nv_permlane16_swap_b32 v87, v85\nMaav 24,140,72\nAD 86,86,84\nAD 87,87,85\nMaav 32,144,72\nRa 128,2,0\nv_rcp_f32 v86, v86\nMaav 36,144,76\nv_rcp_f32 v87, v87\n"
"Maav 44,148,76\nRa 132,2,1024\nAR 82,0\nAR 83,1\nMU 82,82,86\nMU 83,83,86\nMaav 40,148,72\nWL 8\ns_add_u32 m0, s89, 131072\ns_add_u32 s46, s96, 1212416\nCV 96,82,83\nAR 82,2\n"
"Maav 48,152,72\nRa 136,2,2048\nAR 83,3\nMU 82,82,86\nMU 83,83,86\nCV 97,82,83\nMaav 52,152,76\nD1 1,16,46,0\nAR 82,4\nAR 83,5\nMU 82,82,87\nMaav 60,156,76\nRa 140,2,3072\n"
"MU 83,83,87\nCV 100,82,83\nAR 82,6\nAR 83,7\nMaav 56,156,72\nMU 82,82,87\nMU 83,83,87\nCV 101,82,83\nAR 82,8\nAR 83,9\nMaav 64,160,72\nRa 144,2,4096\nMU 82,82,86\nMU 83,83,86\n"
"CV 98,82,83\nAR 82,10\nMaav 68,160,76\nAR 83,11\nMU 82,82,86\nMU 83,83,86\nCV 99,82,83\nMaav 76,164,76\nRa 148,2,5120\nAR 82,12\nAR 83,13\nMU 82,82,87\nMU 83,83,87\n"
"Maav 72,164,72\nWL 8\nCV 102,82,83\nAR 82,14\nAR 83,15\nMU 82,82,87\nMaav 80,168,72\nRa 152,2,6144\nMU 83,83,87\nCV 103,82,83\nAR 82,16\nAR 83,17\nMaav 84,168,76\nD1 1,16,46,1024\n"
"MU 82,82,86\nMU 83,83,86\nCV 104,82,83\nMaav 92,172,76\nRa 156,2,7168\nAR 82,18\nAR 83,19\nMU 82,82,86\nMU 83,83,86\nMaav 88,172,72\nCV 105,82,83\nAR 82,20\nAR 83,21\nMU 82,82,87\n"
"MU 83,83,87\nMaav 96,176,72\nRa 160,2,8192\nCV 108,82,83\nAR 82,22\nAR 83,23\nMU 82,82,87\nMaav 100,176,76\nMU 83,83,87\nCV 109,82,83\nAR 82,24\nAR 83,25\nMaav 108,180,76\n"
"Ra 164,2,9216\nMU 82,82,86\nMU 83,83,86\nCV 106,82,83\nAR 82,26\nMaav 104,180,72\nWL 8\nAR 83,27\nMU 82,82,86\nMU 83,83,86\nCV 107,82,83\nMaav 112,184,72\nRa 168,2,10240\n"
"AR 82,28\nAR 83,29\nMU 82,82,87\nMU 83,83,87\nMaav 116,184,76\nD1 1,16,46,2048\nCV 110,82,83\nAR 82,30\nAR 83,31\nMaav 124,188,76\nRa 172,2,11264\nMU 82,82,87\nMU 83,83,87\n"
"CV 111,82,83\nMaav 120,188,72\nAR 82,32\nAR 83,33\nMU 82,82,86\nMU 83,83,86\nZvav 128,128,96\nRa 176,2,12288\nCV 112,82,83\nAR 82,34\nAR 83,35\nMU 82,82,86\nMvav 128,132,104\n"
"MU 83,83,86\nCV 113,82,83\nAR 82,36\nAR 83,37\nZvav 132,132,108\nRa 180,2,13312\nMU 82,82,87\nMU 83,83,87\nCV 116,82,83\nMvav 132,128,100\nWL 8\nAR 82,38\nAR 83,39\nMU 82,82,87\n"
"Zvav 140,136,100\nRa 184,2,14336\nMU 83,83,87\nCV 117,82,83\nAR 82,40\nMvav 140,140,108\nD1 1,16,46,3072\nAR 83,41\nMU 82,82,86\nMU 83,83,86\nZvav 136,140,104\nRa 188,2,15360\n"
"s_barrier\nCV 114,82,83\nAR 82,42\nAR 83,43\nMvav 136,136,96\nMU 82,82,86\nMU 83,83,86\nCV 115,82,83\nAR 82,44\nZvav 144,144,96\nRa 128,2,32768\nAR 83,45\nMU 82,82,87\n"
"MU 83,83,87\nMvav 144,148,104\nCV 118,82,83\nAR 82,46\nAR 83,47\nMU 82,82,87\nbuffer_store_dword v25, off, s[16:19], s54 offset:512 sc0 sc1\nZvav 148,148,108\nRa 132,2,33792\n"
"MU 83,83,87\nCV 119,82,83\nAR 82,48\nMvav 148,144,100\nWL 8\ns_add_u32 m0, s89, 114688\ns_add_u32 s46, s96, 1179648\nAR 83,49\nMU 82,82,86\nZvav 156,152,100\nRa 136,2,34816\n"
"MU 83,83,86\nCV 120,82,83\nAR 82,50\nMvav 156,156,108\nD1 1,16,46,0\nAR 83,51\nMU 82,82,86\nMU 83,83,86\nZvav 152,156,104\nRa 140,2,35840\nCV 121,82,83\nAR 82,52\nAR 83,53\n"
"Mvav 152,152,96\nMU 82,82,87\nMU 83,83,87\nCV 124,82,83\nAR 82,54\nZvav 64,160,96\nRa 144,2,36864\nAR 83,55\nMU 82,82,87\nMU 83,83,87\nCV 125,82,83\nMvav 64,164,104\nAR 82,56\n"
"AR 83,57\nMU 82,82,86\nMU 83,83,86\nZvav 68,164,108\nRa 148,2,37888\nCV 122,82,83\nAR 82,58\nAR 83,59\nMvav 68,160,100\nWL 8\nMU 82,82,86\nMU 83,83,86\nCV 123,82,83\n"
"Zvav 76,168,100\nRa 152,2,38912\nAR 82,60\nAR 83,61\nMU 82,82,87\nMvav 76,172,108\nD1 1,16,46,1024\nMU 83,83,87\nCV 126,82,83\nAR 82,62\nZvav 72,172,104\nRa 156,2,39936\nAR 83,63\n"
"MU 82,82,87\nMU 83,83,87\nCV 127,82,83\nMvav 72,168,96\nAR 82,64\nAR 83,65\nMU 82,82,86\nMU 83,83,86\nMvav 128,176,112\nRa 160,2,40960\nCV 32,82,83\nAR 82,66\nAR 83,67\n"
"Mvav 128,180,120\nMU 82,82,86\nMU 83,83,86\nCV 33,82,83\nAR 82,68\nMvav 132,180,124\nRa 164,2,41984\nAR 83,69\nMU 82,82,87\nMU 83,83,87\nMvav 132,176,116\nWL 8\nCV 36,82,83\n"
"AR 82,70\nAR 83,71\nMU 82,82,87\nMvav 140,184,116\nRa 168,2,43008\nMU 83,83,87\nCV 37,82,83\nAR 82,72\nMvav 140,188,124\nD1 1,16,46,2048\nAR 83,73\nMU 82,82,86\nMU 83,83,86\n"
"Mvav 136,188,120\nRa 172,2,44032\nCV 34,82,83\nAR 82,74\nAR 83,75\nMvav 136,184,112\nMU 82,82,86\nMU 83,83,86\nCV 35,82,83\nAR 82,76\nMvav 144,128,112\nRa 176,2,45056\nAR 83,77\n"
"MU 82,82,87\nMU 83,83,87\nCV 38,82,83\nMvav 144,132,120\nAR 82,78\nAR 83,79\nMU 82,82,87\nMU 83,83,87\nMvav 148,132,124\nRa 180,2,46080\nCV 39,82,83\nAR 82,80\nAR 83,81\n"
"Mvav 148,128,116\nWL 8\nMU 82,82,86\nMU 83,83,86\nCV 40,82,83\nMvav 156,136,116\nRa 184,2,47104\nAR 82,82\nAR 83,83\nMU 82,82,86\nMvav 156,140,124\nD1 1,16,46,3072\nMU 83,83,86\n"
"CV 41,82,83\nAR 82,84\nMvav 152,140,120\nRa 188,2,48128\nAR 83,85\nMU 82,82,87\nMU 83,83,87\nCV 44,82,83\nMvav 152,136,112\nAR 82,86\nAR 83,87\nMU 82,82,87\nMU 83,83,87\n"
"Mvav 64,144,112\nRa 128,2,49152\nCV 45,82,83\nAR 82,88\nAR 83,89\nMvav 64,148,120\nMU 82,82,86\nMU 83,83,86\nCV 42,82,83\nAR 82,90\nMvav 68,148,124\nRa 132,2,50176\nAR 83,91\n"
"MU 82,82,86\nMU 83,83,86\nMvav 68,144,116\nWL 8\ns_add_u32 m0, s89, 147456\ns_add_u32 s46, s96, 1245184\nCV 43,82,83\nAR 82,92\nMvav 76,152,116\nRa 136,2,51200\nAR 83,93\n"
"MU 82,82,87\nMU 83,83,87\nMvav 76,156,124\nD1 1,16,46,0\nCV 46,82,83\nAR 82,94\nAR 83,95\nMvav 72,156,120\nRa 140,2,52224\nMU 82,82,87\nMU 83,83,87\nCV 47,82,83\nMvav 72,152,112\n"
"AR 82,96\nAR 83,97\nMU 82,82,86\nMU 83,83,86\nMvav 128,160,32\nRa 144,2,53248\nCV 48,82,83\nAR 82,98\nAR 83,99\nMU 82,82,86\nMvav 128,164,40\nMU 83,83,86\nCV 49,82,83\nAR 82,100\n"
"AR 83,101\nMvav 132,164,44\nRa 148,2,54272\nMU 82,82,87\nMU 83,83,87\nCV 52,82,83\nMvav 132,160,36\nWL 8\nAR 82,102\nAR 83,103\nMU 82,82,87\nMvav 140,168,36\nRa 152,2,55296\n"
"MU 83,83,87\nCV 53,82,83\nAR 82,104\nMvav 140,172,44\nD1 1,16,46,1024\nAR 83,105\nMU 82,82,86\nMU 83,83,86\nMvav 136,172,40\nRa 156,2,56320\nCV 50,82,83\nAR 82,106\nAR 83,107\n"
"Mvav 136,168,32\nMU 82,82,86\nMU 83,83,86\nCV 51,82,83\nAR 82,108\nMvav 144,176,32\nRa 160,2,57344\nAR 83,109\nMU 82,82,87\nMU 83,83,87\nCV 54,82,83\nMvav 144,180,40\nAR 82,110\n"
"AR 83,111\nMU 82,82,87\nMU 83,83,87\nMvav 148,180,44\nRa 164,2,58368\nCV 55,82,83\nAR 82,112\nAR 83,113\nMvav 148,176,36\nWL 8\nMU 82,82,86\nMU 83,83,86\nCV 56,82,83\n"
"Mvav 156,184,36\nRa 168,2,59392\nAR 82,114\nAR 83,115\nMU 82,82,86\nMvav 156,188,44\nD1 1,16,46,2048\nMU 83,83,86\nCV 57,82,83\nAR 82,116\nMvav 152,188,40\nRa 172,2,60416\n"
"AR 83,117\nMU 82,82,87\nMU 83,83,87\nMvav 152,184,32\nCV 60,82,83\nAR 82,118\nAR 83,119\nMU 82,82,87\nMvav 64,128,32\nRa 176,2,61440\nMU 83,83,87\nCV 61,82,83\nAR 82,120\n"
"AR 83,121\nMvav 64,132,40\nMU 82,82,86\nMU 83,83,86\nCV 58,82,83\nAR 82,122\nMvav 68,132,44\nRa 180,2,62464\nAR 83,123\nMU 82,82,86\nMU 83,83,86\nMvav 68,128,36\n"
"s_waitcnt vmcnt(16) lgkmcnt(8)\nCV 59,82,83\nAR 82,124\nAR 83,125\nMvav 76,136,36\nRa 184,2,63488\n.W552:\ns_cbranch_execz .WB21\nMU 82,82,87\nMU 83,83,87\nCV 62,82,83\n"
"Mvav 76,140,44\nD1 1,16,46,3072\nAR 82,126\nAR 83,127\nMU 82,82,87\nMvav 72,140,40\nRa 188,2,64512\ns_barrier\nMU 83,83,87\nCV 63,82,83\nMvav 72,136,32\nMvav 128,144,48\n"
"Ra 128,3,0\nMvav 128,148,56\nMvav 132,148,60\nRa 132,3,1024\nMvav 132,144,52\nWL 8\nMvav 140,152,52\nRa 136,3,2048\nMvav 140,156,60\nMvav 136,156,56\nRa 140,3,3072\n"
"Mvav 136,152,48\nMvav 144,160,48\nRa 144,3,4096\nMvav 144,164,56\nMvav 148,164,60\nRa 148,3,5120\nCV 132,132,133\nMvav 148,160,52\nWL 8\nCV 133,134,135\nMvav 156,168,52\n"
"Ra 152,3,6144\nCV 134,140,141\nMvav 156,172,60\nCV 135,142,143\ns_add_u32 s5, s94, 0\nMvav 152,172,56\nRa 156,3,7168\nSv 132,1,16,5,1024\nMvav 152,168,48\nCV 128,128,129\n"
"CV 129,130,131\nMvav 64,176,48\nRa 160,3,8192\nCV 130,136,137\nMvav 64,180,56\nbuffer_load_dword v27, off, s[16:19], s60 offset:512 sc0 sc1\nCV 131,138,139\nMvav 68,180,60\n"
"Ra 164,3,9216\nSv 128,1,16,5,0\nMvav 68,176,52\nWL 6\nCV 148,148,149\nMvav 76,184,52\nRa 168,3,10240\nCV 149,150,151\nMvav 76,188,60\nCV 150,156,157\nCV 151,158,159\n"
"Mvav 72,188,56\nRa 172,3,11264\ns_add_u32 s5, s94, 131072\nMvav 72,184,48\nSv 148,1,16,5,1024\nCV 144,144,145\nZvav 160,128,96\nRa 176,3,12288\nCV 145,146,147\nMvav 160,132,104\n"
"CV 146,152,153\nCV 147,154,155\nMvav 160,136,112\nRa 180,3,13312\nSv 144,1,16,5,0\nMvav 160,140,120\nWL 6\nCV 68,68,69\nZvav 164,140,124\nRa 184,3,14336\nCV 69,70,71\n"
"Mvav 164,136,116\nCV 70,76,77\nCV 71,78,79\nMvav 164,132,108\nRa 188,3,15360\ns_add_u32 s5, s94, 262144\nMvav 164,128,100\nSv 68,1,16,5,1024\nCV 64,64,65\nMvav 164,144,36\n"
"Ra 128,3,16384\nCV 65,66,67\nMvav 164,148,44\nCV 66,72,73\nCV 67,74,75\nMvav 164,152,52\nRa 132,3,17408\nSv 64,1,16,5,0\nMvav 164,156,60\nWL 6\nMvav 160,156,56\nRa 136,3,18432\n"
"Mvav 160,152,48\nMvav 160,148,40\nRa 140,3,19456\nMvav 160,144,32\nZvav 168,160,96\nRa 144,3,20480\nMvav 168,164,104\nMvav 168,168,112\nRa 148,3,21504\nMvav 168,172,120\nWL 6\n"
"Zvav 172,172,124\nRa 152,3,22528\nMvav 172,168,116\nMvav 172,164,108\nRa 156,3,23552\nMvav 172,160,100\nMvav 172,176,36\nRa 160,3,24576\nMvav 172,180,44\nMvav 172,184,52\n"
"Ra 164,3,25600\nMvav 172,188,60\nWL 6\nMvav 168,188,56\nRa 168,3,26624\nMvav 168,184,48\nMvav 168,180,40\nRa 172,3,27648\nMvav 168,176,32\nZvav 176,128,96\nRa 176,3,28672\n"
"CV 164,164,165\nMvav 176,132,104\nCV 165,166,167\nCV 166,172,173\nMvav 176,136,112\nRa 180,3,29696\nCV 167,174,175\nMvav 176,140,120\ns_waitcnt vmcnt(11) lgkmcnt(6)\n"
"s_add_u32 s5, s94, 393216\nZvav 180,140,124\nRa 184,3,30720\nSv 164,1,16,5,1024\nMvav 180,136,116\nCV 160,160,161\nCV 161,162,163\nMvav 180,132,108\nRa 188,3,31744\ns_barrier\n"
"Mvav 180,128,100\nCV 162,168,169\nCV 163,170,171\nMvav 180,144,36\nRa 128,3,32768\nSv 160,1,16,5,0\nMvav 180,148,44\nMvav 180,152,52\nRa 132,3,33792\nMvav 180,156,60\nWL 6\n"
"s_add_u32 m0, s89, 16384\ns_add_u32 s46, s96, 1277952\nMvav 176,156,56\nRa 136,3,34816\nMvav 176,152,48\nD1 1,16,46,0\nMvav 176,148,40\nRa 140,3,35840\nMvav 176,144,32\n"
"Zvav 184,160,96\nRa 144,3,36864\nMvav 184,164,104\nMvav 184,168,112\nRa 148,3,37888\nMvav 184,172,120\nWL 6\nZvav 188,172,124\nRa 152,3,38912\nMvav 188,168,116\nD1 1,16,46,1024\n"
"Mvav 188,164,108\nRa 156,3,39936\nMvav 188,160,100\nMvav 188,176,36\nRa 160,3,40960\nWV 9\nv_cmp_ne_u32 vcc, s15, v27\ns_nop 0\ns_cbranch_vccnz .S114\n.S115:\nMvav 188,180,44\n"
"s_add_u32 s4, s92, 0\nMvav 188,184,52\nRa 164,3,41984\nMvav 188,188,60\nWL 6\nbuffer_load_dwordx4 v[208:211], v1, s[16:19], s4 offen offset:0 sc0 sc1\nMvav 184,188,56\n"
"Ra 168,3,43008\nMvav 184,184,48\nD1 1,16,46,2048\nMvav 184,180,40\nRa 172,3,44032\nMvav 184,176,32\nbuffer_load_dwordx4 v[212:215], v1, s[16:19], s4 offen offset:1024 sc0 sc1\n"
"Zvav 192,128,96\nRa 176,3,45056\nv_lshrrev_b32 v88, 8, v1\nMvav 192,132,104\nv_lshlrev_b32 v89, 5, v88\nCV 180,180,181\nMvav 192,136,112\nRa 180,3,46080\nCV 181,182,183\n"
"Mvav 192,140,120\nWL 6\nCV 182,188,189\nZvav 196,140,124\nRa 184,3,47104\nv_bfe_u32 v90, v1, 4, 4\nMvav 196,136,116\nD1 1,16,46,3072\nCV 183,190,191\nMvav 196,132,108\n"
"Ra 188,3,48128\ns_add_u32 s5, s94, 524288\nMvav 196,128,100\nv_mul_u32_u24 v91, 896, v90\nSv 180,1,16,5,1024\nMvav 196,144,36\nRa 128,3,49152\nv_lshl_add_u32 v91, v88, 4, v91\n"
"Mvav 196,148,44\nv_add_u32 v90, s21, v90\nCV 176,176,177\nMvav 196,152,52\nRa 132,3,50176\nCV 177,178,179\nMvav 196,156,60\nWL 6\ns_add_u32 m0, s89, 0\n"
"s_add_u32 s46, s96, 1310720\nMvav 192,156,56\nRa 136,3,51200\nv_lshlrev_b32 v88, 3, v90\nMvav 192,152,48\nD1 1,16,46,0\nCV 178,184,185\nMvav 192,148,40\nRa 140,3,52224\n"
"CV 179,186,187\nMvav 192,144,32\nv_add_u32 v94, 64, v90\nSv 176,1,16,5,0\nZvav 200,160,96\nRa 144,3,53248\ns_mov_b32 s63, 0xc040000\nMvav 200,164,104\nv_lshrrev_b32 v92, 8, v1\n"
"Mvav 200,168,112\nRa 148,3,54272\nMvav 200,172,120\nWL 6\nZvav 204,172,124\nRa 152,3,55296\nv_bfe_u32 v93, v1, 6, 2\nMvav 204,168,116\nD1 1,16,46,1024\nv_cmp_eq_u32 vcc, v92, v93\n"
"v_and_b32 v92, 16, v1\nv_mov_b32 v93, 0x3f80\nv_cndmask_b32 v93, 0, v93, vcc\nMvav 204,164,108\nRa 156,3,56320\nMvav 204,160,100\nv_lshlrev_b32 v93, v92, v93\nMvav 204,176,36\n"
"Ra 160,3,57344\nv_and_b32 v92, 32, v1\nMvav 204,180,44\ns_add_u32 s4, s92, 131072\nv_cmp_eq_u32 vcc, 0, v92\nv_mov_b32 v4, 0\nv_mov_b32 v5, 0\nv_cndmask_b32 v6, 0, v93, vcc\n"
"Mvav 204,184,52\nRa 164,3,58368\nMvav 204,188,60\nWL 6\nbuffer_load_dwordx4 v[216:219], v1, s[16:19], s4 offen offset:0 sc0 sc1\nMvav 200,188,56\nRa 168,3,59392\n"
"v_sub_u32 v7, v93, v6\nMvav 200,184,48\nD1 1,16,46,2048\nMvav 200,180,40\nRa 172,3,60416\nMvav 200,176,32\n"
"buffer_load_dwordx4 v[220:223], v1, s[16:19], s4 offen offset:1024 sc0 sc1\nZvav 160,128,96\nRa 176,3,61440\nv_mov_b32 v8, 0\nMvav 160,132,104\nv_mov_b32 v9, 0\nCV 196,196,197\n"
"Mvav 160,136,112\nRa 180,3,62464\nCV 197,198,199\nMvav 160,140,120\nWL 6\nCV 198,204,205\nZvav 164,140,124\nRa 184,3,63488\nCV 199,206,207\nMvav 164,136,116\nD1 1,16,46,3072\n"
"s_add_u32 s5, s94, 655360\nMvav 164,132,108\nRa 188,3,64512\ns_barrier\nMvav 164,128,100\nSv 196,1,16,5,1024\nCV 192,192,193\nMvav 164,144,36\nRa 128,0,0\nCV 193,194,195\n"
"Mvav 164,148,44\nCV 194,200,201\nCV 195,202,203\nMvav 164,152,52\nRa 132,0,1024\nSv 192,1,16,5,0\nMvav 164,156,60\nWL 6\ns_add_u32 m0, s89, 32768\ns_add_u32 s46, s96, 1343488\n"
"Mvav 160,156,56\nRa 136,0,2048\nMvav 160,152,48\nD1 1,16,46,0\nMvav 160,148,40\nRa 140,0,3072\nMvav 160,144,32\nZvav 168,160,96\nRa 144,0,4096\nWV 12\nv_max_u32 v21, v211, v215\n"
"v_cmp_eq_u32 vcc, -1, v21\ns_nop 0\ns_cbranch_vccnz .S90\n.S91:\nMvav 168,164,104\nv_lshlrev_b32 v176, 16, v208\nMvav 168,168,112\nRa 148,0,5120\nMvav 168,172,120\nWL 6\n"
"v_and_b32 v177, 0xffff0000, v208\nZvav 172,172,124\nRa 152,0,6144\nMvav 172,168,116\nD1 1,16,46,1024\nMvav 172,164,108\nRa 156,0,7168\nv_lshlrev_b32 v178, 16, v209\n"
"Mvav 172,160,100\nv_and_b32 v179, 0xffff0000, v209\nMvav 172,176,36\nRa 160,0,8192\nv_lshlrev_b32 v180, 16, v212\nMvav 172,180,44\nv_and_b32 v181, 0xffff0000, v212\n"
"s_add_u32 s4, s92, 262144\nMvav 172,184,52\nRa 164,0,9216\nMvav 172,188,60\nWL 6\nv_lshlrev_b32 v182, 16, v213\n"
"buffer_load_dwordx4 v[224:227], v1, s[16:19], s4 offen offset:0 sc0 sc1\nMvav 168,188,56\nRa 168,0,10240\nMvav 168,184,48\nD1 1,16,46,2048\nMvav 168,180,40\nRa 172,0,11264\n"
"v_and_b32 v183, 0xffff0000, v213\nMvav 168,176,32\nv_lshlrev_b32 v184, 16, v210\nbuffer_load_dwordx4 v[228:231], v1, s[16:19], s4 offen offset:1024 sc0 sc1\nMvav 176,128,96\n"
"Ra 176,0,12288\nv_and_b32 v185, 0xffff0000, v210\nMvav 176,132,104\nv_lshlrev_b32 v186, 16, v211\nCV 164,164,165\nMvav 176,136,112\nRa 180,0,13312\nCV 165,166,167\n"
"Mvav 176,140,120\nWL 6\nCV 166,172,173\nMvav 180,140,124\nRa 184,0,14336\nv_and_b32 v187, 0xffff0000, v211\nMvav 180,136,116\nD1 1,16,46,3072\nCV 167,174,175\nMvav 180,132,108\n"
"Ra 188,0,15360\ns_add_u32 s5, s94, 786432\nMvav 180,128,100\nv_lshlrev_b32 v188, 16, v214\nSv 164,1,16,5,1024\nMvav 180,144,36\nRa 128,0,16384\nv_and_b32 v189, 0xffff0000, v214\n"
"Mvav 180,148,44\nv_lshlrev_b32 v190, 16, v215\nCV 160,160,161\nMvav 180,152,52\nRa 132,0,17408\nCV 161,162,163\nMvav 180,156,60\nWL 6\ns_add_u32 m0, s89, 49152\n"
"s_add_u32 s46, s96, 1376256\nMvav 176,156,56\nRa 136,0,18432\nv_and_b32 v191, 0xffff0000, v215\nMvav 176,152,48\nD1 1,16,46,0\nCV 162,168,169\nMvav 176,148,40\nRa 140,0,19456\n"
"CV 163,170,171\nMvav 176,144,32\nSv 160,1,16,5,0\nMvav 184,160,96\nRa 144,0,20480\nWV 12\nv_max_u32 v21, v219, v223\nv_cmp_eq_u32 vcc, -1, v21\ns_nop 0\ns_cbranch_vccnz .S93\n"
".S94:\nMvav 184,164,104\nv_lshlrev_b32 v192, 16, v216\nMvav 184,168,112\nRa 148,0,21504\nMvav 184,172,120\nWL 6\nv_and_b32 v193, 0xffff0000, v216\nMvav 188,172,124\n"
"Ra 152,0,22528\nMvav 188,168,116\nD1 1,16,46,1024\nMvav 188,164,108\nRa 156,0,23552\nv_lshlrev_b32 v194, 16, v217\nMvav 188,160,100\nv_and_b32 v195, 0xffff0000, v217\n"
"Mvav 188,176,36\nRa 160,0,24576\nv_lshlrev_b32 v196, 16, v220\nMvav 188,180,44\nv_and_b32 v197, 0xffff0000, v220\ns_add_u32 s4, s92, 393216\nMvav 188,184,52\nRa 164,0,25600\n"
"Mvav 188,188,60\nWL 6\nv_lshlrev_b32 v198, 16, v221\nbuffer_load_dwordx4 v[208:211], v1, s[16:19], s4 offen offset:0 sc0 sc1\nMvav 184,188,56\nRa 168,0,26624\nMvav 184,184,48\n"
"D1 1,16,46,2048\nMvav 184,180,40\nRa 172,0,27648\nv_and_b32 v199, 0xffff0000, v221\nMvav 184,176,32\nv_lshlrev_b32 v200, 16, v218\n"
"buffer_load_dwordx4 v[212:215], v1, s[16:19], s4 offen offset:1024 sc0 sc1\nMvav 192,128,96\nRa 176,0,28672\nv_and_b32 v201, 0xffff0000, v218\nMvav 192,132,104\n"
"v_lshlrev_b32 v202, 16, v219\nCV 180,180,181\nMvav 192,136,112\nRa 180,0,29696\nCV 181,182,183\nMvav 192,140,120\ns_waitcnt vmcnt(15) lgkmcnt(6)\nCV 182,188,189\nMvav 196,140,124\n"
"Ra 184,0,30720\nv_and_b32 v203, 0xffff0000, v219\nMvav 196,136,116\nD1 1,16,46,3072\nCV 183,190,191\nMvav 196,132,108\nRa 188,0,31744\ns_barrier\nMvav 196,128,100\n"
"v_lshlrev_b32 v204, 16, v222\ns_add_u32 s5, s94, 917504\nMvav 196,144,36\nRa 128,2,16384\nv_and_b32 v205, 0xffff0000, v222\nMvav 196,148,44\nv_lshlrev_b32 v206, 16, v223\n"
"Sv 180,1,16,5,1024\nMvav 196,152,52\nRa 132,2,17408\nCV 176,176,177\nMvav 196,156,60\nWL 6\ns_add_u32 m0, s89, 65536\ns_add_u32 s46, s96, 1409024\nMvav 192,156,56\nRa 136,2,18432\n"
"v_and_b32 v207, 0xffff0000, v223\nMvav 192,152,48\nD1 1,16,46,0\nCV 177,178,179\nMvav 192,148,40\nRa 140,2,19456\nCV 178,184,185\nMvav 192,144,32\nCV 179,186,187\nSv 176,1,16,5,0\n"
"Mvav 200,160,96\nRa 144,2,20480\nWV 12\nv_max_u32 v21, v227, v231\nv_cmp_eq_u32 vcc, -1, v21\ns_nop 0\ns_cbranch_vccnz .S96\n.S97:\nMvav 200,164,104\nv_lshlrev_b32 v160, 16, v224\n"
"Mvav 200,168,112\nRa 148,2,21504\nMvav 200,172,120\nWL 6\nv_and_b32 v161, 0xffff0000, v224\nMvav 204,172,124\nRa 152,2,22528\nMvav 204,168,116\n.W553:\ns_cbranch_execz .WB21\n"
"D1 1,16,46,1024\nMvav 204,164,108\nRa 156,2,23552\nv_lshlrev_b32 v162, 16, v225\nMvav 204,160,100\nv_and_b32 v163, 0xffff0000, v225\nMvav 204,176,36\nRa 160,2,24576\n"
"v_lshlrev_b32 v164, 16, v228\nMvav 204,180,44\nv_and_b32 v165, 0xffff0000, v228\ns_add_u32 s4, s92, 524288\nMvav 204,184,52\nRa 164,2,25600\nMvav 204,188,60\nWL 6\n"
"v_lshlrev_b32 v166, 16, v229\nbuffer_load_dwordx4 v[216:219], v1, s[16:19], s4 offen offset:0 sc0 sc1\nMvav 200,188,56\nRa 168,2,26624\nMvav 200,184,48\nD1 1,16,46,2048\n"
"Mvav 200,180,40\nRa 172,2,27648\nv_and_b32 v167, 0xffff0000, v229\nMvav 200,176,32\nv_lshlrev_b32 v168, 16, v226\n"
"buffer_load_dwordx4 v[220:223], v1, s[16:19], s4 offen offset:1024 sc0 sc1\nMvav 160,128,96\nRa 176,2,28672\nv_and_b32 v169, 0xffff0000, v226\nMvav 160,132,104\n"
"v_lshlrev_b32 v170, 16, v227\nCV 196,196,197\nMvav 160,136,112\nRa 180,2,29696\nCV 197,198,199\nMvav 160,140,120\nWL 6\nCV 198,204,205\nMvav 164,140,124\nRa 184,2,30720\n"
"v_and_b32 v24, 0x70, v1\nMvav 164,136,116\nD1 1,16,46,3072\nCV 199,206,207\nMvav 164,132,108\nRa 188,2,31744\nv_and_b32 v171, 0xffff0000, v227\nMvav 164,128,100\n"
"v_lshlrev_b32 v172, 16, v230\ns_add_u32 s5, s94, 1048576\nMvav 164,144,36\nRa 128,2,0\nv_lshlrev_b32 v24, 5, v24\nMvav 164,148,44\nv_and_b32 v173, 0xffff0000, v230\n"
"Sv 196,1,16,5,1024\nMvav 164,152,52\nRa 132,2,1024\nCV 192,192,193\nMvav 164,156,60\nWL 6\nv_lshlrev_b32 v174, 16, v231\nMvav 160,156,56\nRa 136,2,2048\n"
"buffer_load_dword v27, v24, s[16:19], s41 offen sc0 sc1\nMvav 160,152,48\nv_and_b32 v175, 0xffff0000, v231\nCV 193,194,195\nMvav 160,148,40\nRa 140,2,3072\nCV 194,200,201\n"
"Mvav 160,144,32\nCV 195,202,203\nSv 192,1,16,5,0\nMvav 168,160,96\nRa 144,2,4096\nWV 12\nv_max_u32 v21, v211, v215\nv_cmp_eq_u32 vcc, -1, v21\ns_nop 0\ns_cbranch_vccnz .S99\n"
".S100:\nMvav 168,164,104\nv_lshlrev_b32 v176, 16, v208\nMvav 168,168,112\nRa 148,2,5120\nMvav 168,172,120\nWL 6\nMvav 172,172,124\nRa 152,2,6144\nv_and_b32 v177, 0xffff0000, v208\n"
"Mvav 172,168,116\nv_lshlrev_b32 v178, 16, v209\nMvav 172,164,108\nRa 156,2,7168\nMvav 172,160,100\nv_and_b32 v179, 0xffff0000, v209\nMvav 172,176,36\nRa 160,2,8192\n"
"v_lshlrev_b32 v180, 16, v212\nMvav 172,180,44\nv_and_b32 v181, 0xffff0000, v212\ns_add_u32 s4, s92, 655360\nMvav 172,184,52\nRa 164,2,9216\nMvav 172,188,60\nWL 6\n"
"buffer_load_dwordx4 v[224:227], v1, s[16:19], s4 offen offset:0 sc0 sc1\nMvav 168,188,56\nRa 168,2,10240\nv_lshlrev_b32 v182, 16, v213\nMvav 168,184,48\n"
"v_and_b32 v183, 0xffff0000, v213\nMvav 168,180,40\nRa 172,2,11264\nMvav 168,176,32\nv_lshlrev_b32 v184, 16, v210\n"
"buffer_load_dwordx4 v[228:231], v1, s[16:19], s4 offen offset:1024 sc0 sc1\nMvav 176,128,96\nRa 176,2,12288\nv_and_b32 v185, 0xffff0000, v210\nMvav 176,132,104\n"
"v_lshlrev_b32 v186, 16, v211\nCV 164,164,165\nMvav 176,136,112\nRa 180,2,13312\nCV 165,166,167\nMvav 176,140,120\ns_waitcnt vmcnt(13) lgkmcnt(6)\nCV 166,172,173\nMvav 180,140,124\n"
"Ra 184,2,14336\nCV 167,174,175\nMvav 180,136,116\nv_and_b32 v187, 0xffff0000, v211\ns_add_u32 s5, s94, 1179648\nMvav 180,132,108\nRa 188,2,15360\ns_barrier\nMvav 180,128,100\n"
"v_lshlrev_b32 v188, 16, v214\nSv 164,1,16,5,1024\nMvav 180,144,36\nRa 128,2,32768\nv_and_b32 v189, 0xffff0000, v214\nMvav 180,148,44\nv_lshlrev_b32 v190, 16, v215\nCV 160,160,161\n"
"Mvav 180,152,52\nRa 132,2,33792\nCV 161,162,163\nMvav 180,156,60\nWL 6\nCV 162,168,169\nMvav 176,156,56\nRa 136,2,34816\nCV 163,170,171\nMvav 176,152,48\n"
"v_and_b32 v191, 0xffff0000, v215\nSv 160,1,16,5,0\nMvav 176,148,40\nRa 140,2,35840\nMvav 176,144,32\nMvav 184,160,96\nRa 144,2,36864\nWV 8\nv_max_u32 v21, v219, v223\n"
"v_cmp_eq_u32 vcc, -1, v21\ns_nop 0\ns_cbranch_vccnz .S102\n.S103:\nMvav 184,164,104\nv_lshlrev_b32 v192, 16, v216\nMvav 184,168,112\nRa 148,2,37888\nMvav 184,172,120\nWL 6\n"
"Mvav 188,172,124\nRa 152,2,38912\nv_and_b32 v193, 0xffff0000, v216\nMvav 188,168,116\nv_lshlrev_b32 v194, 16, v217\nMvav 188,164,108\nRa 156,2,39936\nMvav 188,160,100\n"
"v_and_b32 v195, 0xffff0000, v217\nMvav 188,176,36\nRa 160,2,40960\nv_lshlrev_b32 v196, 16, v220\nMvav 188,180,44\nv_and_b32 v197, 0xffff0000, v220\ns_add_u32 s4, s92, 786432\n"
"Mvav 188,184,52\nRa 164,2,41984\nMvav 188,188,60\nWL 6\nbuffer_load_dwordx4 v[208:211], v1, s[16:19], s4 offen offset:0 sc0 sc1\nMvav 184,188,56\nRa 168,2,43008\n"
"v_lshlrev_b32 v198, 16, v221\nMvav 184,184,48\nv_and_b32 v199, 0xffff0000, v221\nMvav 184,180,40\nRa 172,2,44032\nMvav 184,176,32\nv_lshlrev_b32 v200, 16, v218\n"
"buffer_load_dwordx4 v[212:215], v1, s[16:19], s4 offen offset:1024 sc0 sc1\nMvav 192,128,96\nRa 176,2,45056\nv_and_b32 v201, 0xffff0000, v218\nMvav 192,132,104\n"
"v_lshlrev_b32 v202, 16, v219\nCV 180,180,181\nMvav 192,136,112\nRa 180,2,46080\nCV 181,182,183\nMvav 192,140,120\nWL 6\nCV 182,188,189\nMvav 196,140,124\nRa 184,2,47104\n"
"CV 183,190,191\nMvav 196,136,116\nv_and_b32 v203, 0xffff0000, v219\ns_add_u32 s5, s94, 1310720\nMvav 196,132,108\nRa 188,2,48128\nSv 180,1,16,5,1024\nMvav 196,128,100\n"
"v_lshlrev_b32 v204, 16, v222\nCV 176,176,177\nMvav 196,144,36\nRa 128,2,49152\nv_and_b32 v205, 0xffff0000, v222\nMvav 196,148,44\nv_lshlrev_b32 v206, 16, v223\nCV 177,178,179\n"
"Mvav 196,152,52\nRa 132,2,50176\nCV 178,184,185\nMvav 196,156,60\nWL 6\nCV 179,186,187\nMvav 192,156,56\nRa 136,2,51200\nSv 176,1,16,5,0\nMvav 192,152,48\n"
"v_and_b32 v207, 0xffff0000, v223\nMvav 192,148,40\nRa 140,2,52224\nMvav 192,144,32\nMvav 200,160,96\nRa 144,2,53248\nWV 6\nv_max_u32 v21, v227, v231\nv_cmp_eq_u32 vcc, -1, v21\n"
"s_nop 0\ns_cbranch_vccnz .S105\n.S106:\nMvav 200,164,104\nv_lshlrev_b32 v160, 16, v224\nv_xor_b32 v27, s15, v27\nv_and_b32 v26, 0xffffdfff, v27\nv_cmp_ne_u32 s[64:65], 0, v27\n"
"v_cmp_ne_u32 vcc, 0, v26\ns_nop 0\ns_cbranch_vccnz .S117\n.S118:\nMvav 200,168,112\nRa 148,2,54272\ns_bitcmp1_b32 s64, 0\ns_cselect_b32 s2, 0x2000, 0\nMvav 200,172,120\nWL 6\n"
"Mvav 204,172,124\nRa 152,2,55296\nv_and_b32 v161, 0xffff0000, v224\nMvav 204,168,116\nv_lshlrev_b32 v162, 16, v225\nMvav 204,164,108\nRa 156,2,56320\ns_xor_b32 s2, s2, s20\n"
"Mvav 204,160,100\nv_and_b32 v163, 0xffff0000, v225\nMvav 204,176,36\nRa 160,2,57344\nv_lshlrev_b32 v164, 16, v228\nMvav 204,180,44\nv_and_b32 v165, 0xffff0000, v228\n"
"Mvav 204,184,52\nRa 164,2,58368\nMvav 204,188,60\nWL 6\ns_add_u32 s24, s2, 0x0\nMvav 200,188,56\nRa 168,2,59392\nv_lshlrev_b32 v166, 16, v229\nMvav 200,184,48\n"
"v_and_b32 v167, 0xffff0000, v229\ns_bitcmp1_b32 s64, 1\ns_cselect_b32 s2, 0x2000, 0\nMvav 200,180,40\nRa 172,2,60416\nMvav 200,176,32\nv_lshlrev_b32 v168, 16, v226\n"
"Mvav 160,128,96\nRa 176,2,61440\nv_and_b32 v169, 0xffff0000, v226\nMvav 160,132,104\nv_lshlrev_b32 v170, 16, v227\nCV 196,196,197\nMvav 160,136,112\nRa 180,2,62464\n"
"CV 197,198,199\nMvav 160,140,120\nWL 6\nCV 198,204,205\nMvav 164,140,124\nRa 184,2,63488\nCV 199,206,207\nMvav 164,136,116\nv_and_b32 v171, 0xffff0000, v227\n"
"s_add_u32 s5, s94, 1441792\nMvav 164,132,108\nRa 188,2,64512\ns_barrier\nMvav 164,128,100\nv_lshlrev_b32 v172, 16, v230\nSv 196,1,16,5,1024\nMvav 164,144,36\nRa 128,3,0\n"
"v_and_b32 v173, 0xffff0000, v230\nMvav 164,148,44\nv_lshlrev_b32 v174, 16, v231\nCV 192,192,193\nMvav 164,152,52\nRa 132,3,1024\nCV 193,194,195\nMvav 164,156,60\nWL 6\n"
"s_xor_b32 s2, s2, s20\nMvav 160,156,56\nRa 136,3,2048\nCV 194,200,201\nMvav 160,152,48\nv_and_b32 v175, 0xffff0000, v231\nCV 195,202,203\nMvav 160,148,40\nRa 140,3,3072\n"
"Sv 192,1,16,5,0\nMvav 160,144,32\ns_add_u32 s25, s2, 0x1800000\ns_bitcmp1_b32 s64, 2\ns_cselect_b32 s2, 0x2000, 0\nMvav 168,160,96\nRa 144,3,4096\nWV 4\nv_max_u32 v21, v211, v215\n"
"v_cmp_eq_u32 vcc, -1, v21\ns_nop 0\ns_cbranch_vccnz .S108\n.S109:\nMvav 168,164,104\nv_lshlrev_b32 v176, 16, v208\nMvav 168,168,112\nRa 148,3,5120\nMvav 168,172,120\nWL 6\n"
"Mvav 172,172,124\nRa 152,3,6144\nv_and_b32 v177, 0xffff0000, v208\nMvav 172,168,116\nv_lshlrev_b32 v178, 16, v209\nMvav 172,164,108\nRa 156,3,7168\ns_xor_b32 s2, s2, s20\n"
"Mvav 172,160,100\nv_and_b32 v179, 0xffff0000, v209\nMvav 172,176,36\nRa 160,3,8192\nv_lshlrev_b32 v180, 16, v212\nMvav 172,180,44\nv_and_b32 v181, 0xffff0000, v212\n"
"Mvav 172,184,52\nRa 164,3,9216\nMvav 172,188,60\nWL 6\ns_add_u32 s26, s2, 0x3000000\nMvav 168,188,56\nRa 168,3,10240\nv_lshlrev_b32 v182, 16, v213\nMvav 168,184,48\n"
"v_and_b32 v183, 0xffff0000, v213\ns_bitcmp1_b32 s64, 3\ns_cselect_b32 s2, 0x2000, 0\nMvav 168,180,40\nRa 172,3,11264\nMvav 168,176,32\nv_lshlrev_b32 v184, 16, v210\n"
"Mvav 176,128,96\nRa 176,3,12288\ns_xor_b32 s2, s2, s20\nMvav 176,132,104\nv_and_b32 v185, 0xffff0000, v210\nCV 164,164,165\nMvav 176,136,112\nRa 180,3,13312\nCV 165,166,167\n"
"Mvav 176,140,120\nWL 6\nCV 166,172,173\nMvav 180,140,124\nRa 184,3,14336\nCV 167,174,175\nMvav 180,136,116\nv_lshlrev_b32 v186, 16, v211\ns_add_u32 s5, s94, 1572864\n"
"Mvav 180,132,108\nRa 188,3,15360\ns_add_u32 s27, s2, 0x4800000\nMvav 180,128,100\nv_and_b32 v187, 0xffff0000, v211\ns_bitcmp1_b32 s64, 4\ns_cselect_b32 s2, 0x2000, 0\n"
"Mvav 180,144,36\nv_lshlrev_b32 v188, 16, v214\nSv 164,1,16,5,1024\nMvav 180,148,44\nv_and_b32 v189, 0xffff0000, v214\nCV 160,160,161\nMvav 180,152,52\n"
"v_lshlrev_b32 v190, 16, v215\nCV 161,162,163\nMvav 180,156,60\nWL 4\ns_xor_b32 s2, s2, s20\nMvav 176,156,56\nv_and_b32 v191, 0xffff0000, v215\nCV 162,168,169\nMvav 176,152,48\n"
"s_add_u32 s32, s2, 0x6000000\ns_bitcmp1_b32 s64, 5\ns_cselect_b32 s2, 0x2000, 0\nMvav 176,148,40\ns_xor_b32 s2, s2, s20\nCV 163,170,171\nMvav 176,144,32\n"
"s_add_u32 s33, s2, 0x7800000\ns_bitcmp1_b32 s64, 6\ns_cselect_b32 s2, 0x2000, 0\nMvav 184,160,96\ns_xor_b32 s2, s2, s20\ns_add_u32 s34, s2, 0x9000000\nMvav 184,164,104\n"
"s_bitcmp1_b32 s64, 7\ns_cselect_b32 s2, 0x2000, 0\nMvav 184,168,112\ns_xor_b32 s2, s2, s20\nSv 160,1,16,5,0\nMvav 184,172,120\nWL 0\nMvav 188,172,124\n"
"s_add_u32 s35, s2, 0xa800000\nMvav 188,168,116\nMvav 188,164,108\nMvav 188,160,100\nMvav 188,176,36\nMvav 188,180,44\nMvav 188,184,52\nMvav 188,188,60\nMvav 184,188,56\n"
"Mvav 184,184,48\nMvav 184,180,40\nMvav 184,176,32\nCV 180,180,181\nCV 181,182,183\ns_nop 1\nCV 182,188,189\nCV 183,190,191\ns_add_u32 s5, s94, 1703936\nSv 180,1,16,5,1024\n"
"CV 176,176,177\nCV 177,178,179\nCV 178,184,185\nCV 179,186,187\nSv 176,1,16,5,0\ns_cmp_eq_u32 s13, 0\ns_cbranch_scc1 .Z120\n"
"buffer_load_dwordx4 v[12:15], v91, s[28:31], s22 offen offset:0\nbuffer_load_dwordx4 v[16:19], v91, s[28:31], s23 offen offset:0\n"
"buffer_load_dwordx4 v[64:67], v89, s[16:19], s40 offen offset:0 sc0 sc1\nbuffer_load_dwordx4 v[68:71], v89, s[16:19], s40 offen offset:16 sc0 sc1\n"
"buffer_load_dwordx4 v[72:75], v89, s[16:19], s40 offen offset:1792 sc0 sc1\nbuffer_load_dwordx4 v[76:79], v89, s[16:19], s40 offen offset:1808 sc0 sc1\n"
"buffer_load_dwordx2 v[80:81], v88, s[16:19], s63 offen offset:0 sc0 sc1\nbuffer_load_dwordx2 v[82:83], v88, s[16:19], s63 offen offset:512 sc0 sc1\n"
"buffer_load_dwordx4 v[96:99], v1, s[16:19], s24 offen offset:0 sc0 sc1\nbuffer_load_dwordx4 v[100:103], v1, s[16:19], s24 offen offset:1024 sc0 sc1\n"
"buffer_load_dwordx4 v[104:107], v1, s[16:19], s25 offen offset:0 sc0 sc1\nbuffer_load_dwordx4 v[108:111], v1, s[16:19], s25 offen offset:1024 sc0 sc1\n"
"buffer_load_dwordx4 v[112:115], v1, s[16:19], s26 offen offset:0 sc0 sc1\nbuffer_load_dwordx4 v[116:119], v1, s[16:19], s26 offen offset:1024 sc0 sc1\n"
"buffer_load_dwordx4 v[120:123], v1, s[16:19], s27 offen offset:0 sc0 sc1\nbuffer_load_dwordx4 v[124:127], v1, s[16:19], s27 offen offset:1024 sc0 sc1\n"
"buffer_load_dwordx4 v[32:35], v1, s[16:19], s32 offen offset:0 sc0 sc1\nbuffer_load_dwordx4 v[36:39], v1, s[16:19], s32 offen offset:1024 sc0 sc1\n"
"buffer_load_dwordx4 v[40:43], v1, s[16:19], s33 offen offset:0 sc0 sc1\nbuffer_load_dwordx4 v[44:47], v1, s[16:19], s33 offen offset:1024 sc0 sc1\n"
"buffer_load_dwordx4 v[48:51], v1, s[16:19], s34 offen offset:0 sc0 sc1\nbuffer_load_dwordx4 v[52:55], v1, s[16:19], s34 offen offset:1024 sc0 sc1\n"
"buffer_load_dwordx4 v[56:59], v1, s[16:19], s35 offen offset:0 sc0 sc1\nbuffer_load_dwordx4 v[60:63], v1, s[16:19], s35 offen offset:1024 sc0 sc1\nWV 16\nv_xor_b32 v66, s15, v66\n"
"v_xor_b32 v70, s15, v70\nv_xor_b32 v74, s15, v74\nv_xor_b32 v78, s15, v78\nv_xor_b32 v81, s15, v81\nv_xor_b32 v83, s15, v83\nv_or3_b32 v92, v66, v70, v74\n"
"v_or3_b32 v92, v92, v78, v81\nv_or_b32 v92, v92, v83\nv_cmp_ne_u32 vcc, 0, v92\ns_nop 0\ns_cbranch_vccnz .S111\n.S112:\nv_and_b32 v92, 0x7ff, v67\nv_lshrrev_b32 v93, 16, v67\n"
"v_cmp_eq_u32 s[44:45], v92, v90\nv_cmp_eq_u32 s[46:47], v93, v90\nv_cmp_eq_u32 s[48:49], v92, v94\nv_cmp_eq_u32 s[50:51], v93, v94\nv_cndmask_b32 v12, v12, v64, s[44:45]\n"
"v_cndmask_b32 v13, v13, v65, s[44:45]\nv_cndmask_b32 v14, v14, v68, s[44:45]\nv_cndmask_b32 v15, v15, v69, s[44:45]\nv_cndmask_b32 v12, v12, v72, s[46:47]\n"
"v_cndmask_b32 v13, v13, v73, s[46:47]\nv_cndmask_b32 v14, v14, v76, s[46:47]\nv_cndmask_b32 v15, v15, v77, s[46:47]\nv_cndmask_b32 v16, v16, v64, s[48:49]\n"
"v_cndmask_b32 v17, v17, v65, s[48:49]\nv_cndmask_b32 v18, v18, v68, s[48:49]\nv_cndmask_b32 v19, v19, v69, s[48:49]\nv_cndmask_b32 v16, v16, v72, s[50:51]\n"
"v_cndmask_b32 v17, v17, v73, s[50:51]\nv_cndmask_b32 v18, v18, v76, s[50:51]\nv_cndmask_b32 v19, v19, v77, s[50:51]\nv_sub_f32 v92, 1.0, v80\nv_sub_f32 v93, 1.0, v82\nCV 92,92,93\n"
"v_and_b32 v93, 0xffff0000, v92\nv_lshlrev_b32 v92, 16, v92\nv_lshlrev_b32 v64, 16, v12\nv_and_b32 v65, 0xffff0000, v12\nMU 64,64,92\nMU 65,65,92\nv_lshlrev_b32 v66, 16, v13\n"
"v_and_b32 v67, 0xffff0000, v13\nMU 66,66,92\nMU 67,67,92\nv_lshlrev_b32 v68, 16, v14\nv_and_b32 v69, 0xffff0000, v14\nMU 68,68,92\nMU 69,69,92\nv_lshlrev_b32 v70, 16, v15\n"
"v_and_b32 v71, 0xffff0000, v15\nMU 70,70,92\nMU 71,71,92\nv_lshlrev_b32 v72, 16, v16\nv_and_b32 v73, 0xffff0000, v16\nMU 72,72,93\nMU 73,73,93\nv_lshlrev_b32 v74, 16, v17\n"
"v_and_b32 v75, 0xffff0000, v17\nMU 74,74,93\nMU 75,75,93\nv_lshlrev_b32 v76, 16, v18\nv_and_b32 v77, 0xffff0000, v18\nMU 76,76,93\nMU 77,77,93\nv_lshlrev_b32 v78, 16, v19\n"
"v_and_b32 v79, 0xffff0000, v19\nMU 78,78,93\nMU 79,79,93\ns_mov_b32 s7, 262144\nWV 0\n.Z123:\nv_max_u32 v84, v99, v103\nv_cmp_eq_u32 s[44:45], -1, v84\n.W554:\n"
"s_cbranch_execz .WB21\nv_max_u32 v84, v107, v111\nv_cmp_eq_u32 s[46:47], -1, v84\nv_max_u32 v84, v115, v119\nv_cmp_eq_u32 s[48:49], -1, v84\nv_max_u32 v84, v123, v127\n"
"v_cmp_eq_u32 s[50:51], -1, v84\nv_max_u32 v84, v35, v39\nv_cmp_eq_u32 s[52:53], -1, v84\nv_max_u32 v84, v43, v47\nv_cmp_eq_u32 s[54:55], -1, v84\nv_max_u32 v84, v51, v55\n"
"v_cmp_eq_u32 s[56:57], -1, v84\nv_max_u32 v84, v59, v63\nv_cmp_eq_u32 s[58:59], -1, v84\ns_or_b64 s[0:1], s[44:45], s[46:47]\ns_or_b64 s[2:3], s[48:49], s[50:51]\n"
"s_or_b64 s[0:1], s[0:1], s[52:53]\ns_or_b64 s[2:3], s[2:3], s[54:55]\ns_or_b64 s[0:1], s[0:1], s[56:57]\ns_or_b64 s[2:3], s[2:3], s[58:59]\ns_or_b64 s[0:1], s[0:1], s[2:3]\n"
"s_cbranch_scc1 .Z121\n.Z122:\nZvvv 136,6,96\nMvvv 136,6,104\nMvvv 136,6,112\nMvvv 136,6,120\nMvvv 136,6,32\nMvvv 136,6,40\nMvvv 136,6,48\nMvvv 136,6,56\nZvvv 140,4,96\n"
"Mvvv 140,4,104\nMvvv 140,4,112\nMvvv 140,4,120\nMvvv 140,4,32\nMvvv 140,4,40\nMvvv 140,4,48\nMvvv 140,4,56\nZvvv 144,6,100\nMvvv 144,6,108\nMvvv 144,6,116\nMvvv 144,6,124\n"
"Mvvv 144,6,36\nMvvv 144,6,44\nMvvv 144,6,52\nMvvv 144,6,60\nZvvv 148,4,100\nMvvv 148,4,108\nMvvv 148,4,116\nMvvv 148,4,124\nMvvv 148,4,36\nMvvv 148,4,44\nMvvv 148,4,52\n"
"Mvvv 148,4,60\nFM 64,80,136\nFM 65,80,137\nFM 66,80,138\nFM 67,80,139\nFM 68,80,140\nFM 69,80,141\nFM 70,80,142\nFM 71,80,143\nCV 12,64,65\nCV 13,66,67\nCV 14,68,69\nCV 15,70,71\n"
"Sv 12,91,36,22,0\nFM 72,82,144\nFM 73,82,145\nFM 74,82,146\nFM 75,82,147\nFM 76,82,148\nFM 77,82,149\nFM 78,82,150\nFM 79,82,151\nCV 16,72,73\nCV 17,74,75\nCV 18,76,77\n"
"CV 19,78,79\nSv 16,91,36,23,0\n.Z120:\ns_endpgm\n.EP132:\n.S14:\ns_mov_b32 s7, 262144\n.S16:\nbuffer_load_dwordx4 v[32:35], v1, s[16:19], s6 offen offset:0 sc0 sc1\n"
"buffer_load_dwordx4 v[36:39], v1, s[16:19], s6 offen offset:1024 sc0 sc1\nbuffer_load_dwordx4 v[40:43], v1, s[16:19], s6 offen offset:2048 sc0 sc1\n"
"buffer_load_dwordx4 v[44:47], v1, s[16:19], s6 offen offset:3072 sc0 sc1\nWV 0\nv_xor_b32 v48, s15, v33\nv_xor_b32 v49, s15, v35\nv_xor_b32 v50, s15, v37\nv_xor_b32 v51, s15, v39\n"
"v_xor_b32 v52, s15, v41\nv_xor_b32 v53, s15, v43\nv_xor_b32 v54, s15, v45\nv_xor_b32 v55, s15, v47\nv_or3_b32 v48, v48, v49, v50\nv_or3_b32 v51, v51, v52, v53\n"
"v_or3_b32 v48, v48, v54, v55\nv_or_b32 v48, v48, v51\nv_cmp_ne_u32 vcc, 0, v48\ns_nop 0\ns_cbranch_vccz .S15\ns_sleep 6\ns_sub_u32 s7, s7, 1\ns_cbranch_scc0 .S16\ns_branch .S15\n"
".S17:\ns_mov_b32 s7, 262144\n.S19:\ns_lshl_b32 s5, s73, 8\ns_add_u32 s5, s5, 0xc04ac00\nv_lshrrev_b32 v27, 2, v1\nbuffer_load_dword v27, v27, s[16:19], s5 offen sc0 sc1\nWV 0\n"
"v_xor_b32 v27, s15, v27\nv_and_b32 v255, 0xffffdfff, v27\nv_cmp_ne_u32 vcc, 0, v255\ns_nop 0\ns_cbranch_vccz .S18\ns_sleep 6\ns_sub_u32 s7, s7, 1\ns_cbranch_scc0 .S19\n"
"s_branch .S18\n.T23:\ns_mov_b32 s62, 1\ns_call_b64 s[80:81], .RA10\ns_branch .T24\n.T25:\ns_mov_b32 s62, 1\ns_call_b64 s[80:81], .RA10\ns_branch .T26\n.T28:\ns_mov_b32 s62, 0\n"
"s_call_b64 s[80:81], .RB11\ns_branch .T29\n.T30:\ns_mov_b32 s62, 0\ns_call_b64 s[80:81], .RA10\ns_branch .T31\n.T33:\ns_mov_b32 s62, 0\ns_call_b64 s[80:81], .RB11\ns_branch .T34\n"
".T35:\ns_mov_b32 s62, 0\ns_call_b64 s[80:81], .RA10\ns_branch .T36\n.S38:\ns_mov_b32 s7, 262144\n.S40:\nv_and_b32 v27, 0x70, v1\n"
"buffer_load_dword v27, v27, s[16:19], s61 offen offset:128 sc0 sc1\nWV 0\nv_cmp_ne_u32 vcc, s15, v27\ns_nop 0\ns_cbranch_vccz .S39\ns_sleep 6\ns_sub_u32 s7, s7, 1\n"
"s_cbranch_scc0 .S40\ns_branch .S39\n.S41:\ns_mov_b32 s7, 262144\n.S43:\nv_and_b32 v27, 0x70, v1\nbuffer_load_dword v27, v27, s[16:19], s61 offen offset:136 sc0 sc1\nWV 0\n"
"v_cmp_ne_u32 vcc, s15, v27\ns_nop 0\ns_cbranch_vccz .S42\ns_sleep 6\ns_sub_u32 s7, s7, 1\ns_cbranch_scc0 .S43\ns_branch .S42\n.S44:\ns_mov_b32 s7, 262144\n.S46:\n"
"v_and_b32 v27, 0x70, v1\nbuffer_load_dword v27, v27, s[16:19], s61 offen offset:140 sc0 sc1\nWV 0\nv_cmp_ne_u32 vcc, s15, v27\ns_nop 0\ns_cbranch_vccz .S45\ns_sleep 6\n"
"s_sub_u32 s7, s7, 1\ns_cbranch_scc0 .S46\ns_branch .S45\n.S47:\ns_mov_b32 s7, 262144\n.S49:\nv_and_b32 v27, 0x70, v1\n"
"buffer_load_dword v27, v27, s[16:19], s61 offen offset:12 sc0 sc1\nWV 0\nv_cmp_ne_u32 vcc, s15, v27\ns_nop 0\ns_cbranch_vccz .S48\ns_sleep 6\ns_sub_u32 s7, s7, 1\n"
"s_cbranch_scc0 .S49\ns_branch .S48\n.S50:\ns_mov_b32 s7, 262144\n.S52:\nv_and_b32 v27, 0x70, v1\nbuffer_load_dword v27, v27, s[16:19], s61 offen offset:8 sc0 sc1\nWV 0\n"
"v_cmp_ne_u32 vcc, s15, v27\ns_nop 0\ns_cbranch_vccz .S51\ns_sleep 6\ns_sub_u32 s7, s7, 1\ns_cbranch_scc0 .S52\ns_branch .S51\n.S53:\ns_mov_b32 s7, 262144\n.S55:\n"
"v_and_b32 v27, 0xf0, v1\nbuffer_load_dword v27, v27, s[16:19], s61 offen offset:4 sc0 sc1\nWV 0\nv_cmp_ne_u32 vcc, s15, v27\ns_nop 0\ns_cbranch_vccz .S54\ns_sleep 6\n"
"s_sub_u32 s7, s7, 1\ns_cbranch_scc0 .S55\ns_branch .S54\n.S57:\ns_mov_b32 s7, 262144\n.S59:\nv_lshrrev_b32 v144, 9, v1\nv_mul_u32_u24 v144, 0x18000, v144\n"
"v_lshlrev_b32 v144, 8, v144\nv_and_b32 v145, 0x1f0, v1\nv_add_u32 v144, v144, v145\ns_mov_b32 s6, 0x15c0000\nbuffer_load_dword v136, v144, s[16:19], s6 offen offset:4 sc0 sc1\n"
"s_mov_b32 s6, 0x15c2000\nbuffer_load_dword v137, v144, s[16:19], s6 offen offset:4 sc0 sc1\ns_mov_b32 s6, 0x45c0000\n"
"buffer_load_dword v138, v144, s[16:19], s6 offen offset:4 sc0 sc1\ns_mov_b32 s6, 0x45c2000\nbuffer_load_dword v139, v144, s[16:19], s6 offen offset:4 sc0 sc1\n"
"s_mov_b32 s6, 0x75c0000\nbuffer_load_dword v140, v144, s[16:19], s6 offen offset:4 sc0 sc1\ns_mov_b32 s6, 0x75c2000\n"
"buffer_load_dword v141, v144, s[16:19], s6 offen offset:4 sc0 sc1\ns_mov_b32 s6, 0xa5c0000\nbuffer_load_dword v142, v144, s[16:19], s6 offen offset:4 sc0 sc1\n"
"s_mov_b32 s6, 0xa5c2000\nbuffer_load_dword v143, v144, s[16:19], s6 offen offset:4 sc0 sc1\nWV 0\nv_xor_b32 v136, s15, v136\nv_xor_b32 v137, s15, v137\nv_xor_b32 v138, s15, v138\n"
"v_xor_b32 v139, s15, v139\nv_xor_b32 v140, s15, v140\nv_xor_b32 v141, s15, v141\nv_xor_b32 v142, s15, v142\nv_xor_b32 v143, s15, v143\nv_min_u32 v136, v136, v137\n"
"v_min_u32 v138, v138, v139\nv_min_u32 v140, v140, v141\nv_min_u32 v142, v142, v143\nv_or3_b32 v136, v136, v138, v140\nv_or_b32 v136, v136, v142\nv_cmp_ne_u32 vcc, 0, v136\n"
"s_nop 0\ns_cbranch_vccz .S58\ns_sleep 6\ns_sub_u32 s7, s7, 1\ns_cbranch_scc0 .S59\ns_branch .S58\n.FF56:\nv_lshrrev_b32 v144, 9, v1\nv_mul_u32_u24 v144, 0x18000, v144\n"
"v_lshlrev_b32 v144, 8, v144\nv_and_b32 v145, 0x1f0, v1\nv_add_u32 v144, v144, v145\ns_mov_b32 s6, 0x15c0000\nbuffer_load_dword v136, v144, s[16:19], s6 offen offset:4 sc0 sc1\n"
"s_mov_b32 s6, 0x15c2000\nbuffer_load_dword v137, v144, s[16:19], s6 offen offset:4 sc0 sc1\ns_mov_b32 s6, 0x45c0000\n"
"buffer_load_dword v138, v144, s[16:19], s6 offen offset:4 sc0 sc1\ns_mov_b32 s6, 0x45c2000\nbuffer_load_dword v139, v144, s[16:19], s6 offen offset:4 sc0 sc1\n"
"s_mov_b32 s6, 0x75c0000\nbuffer_load_dword v140, v144, s[16:19], s6 offen offset:4 sc0 sc1\ns_mov_b32 s6, 0x75c2000\n"
"buffer_load_dword v141, v144, s[16:19], s6 offen offset:4 sc0 sc1\ns_mov_b32 s6, 0xa5c0000\nbuffer_load_dword v142, v144, s[16:19], s6 offen offset:4 sc0 sc1\n"
"s_mov_b32 s6, 0xa5c2000\nbuffer_load_dword v143, v144, s[16:19], s6 offen offset:4 sc0 sc1\ns_mov_b32 s90, 0\ns_swappc_b64 s[8:9], s[80:81]\nv_xor_b32 v136, s15, v136\n"
"v_xor_b32 v137, s15, v137\nv_xor_b32 v138, s15, v138\nv_xor_b32 v139, s15, v139\nv_xor_b32 v140, s15, v140\nv_xor_b32 v141, s15, v141\nv_xor_b32 v142, s15, v142\n"
"v_xor_b32 v143, s15, v143\nv_min_u32 v136, v136, v137\nv_min_u32 v138, v138, v139\nv_min_u32 v140, v140, v141\nv_min_u32 v142, v142, v143\nv_or3_b32 v136, v136, v138, v140\n"
"v_or_b32 v136, v136, v142\nv_cmp_ne_u32 vcc, 0, v136\ns_nop 0\ns_cbranch_vccnz .S57\n.S58:\ns_and_b32 s4, s13, 1\ns_lshl1_add_u32 s4, s4, s11\ns_lshl2_add_u32 s4, s12, s4\n"
"s_lshr_b32 s5, s71, 16\ns_cmp_lt_u32 s4, 28\ns_cselect_b32 s5, s71, s5\ns_cselect_b32 s3, 0, 28\ns_and_b32 s5, s5, 0x7ff\ns_sub_u32 s3, s4, s3\ns_mul_i32 s2, s4, 7168\n"
"s_add_u32 s2, s2, 0xc04c800\ns_add_u32 s6, s2, 4096\nbuffer_load_dwordx4 v[136:139], v1, s[16:19], s2 offen offset:0 sc0 sc1\n"
"buffer_load_dwordx4 v[140:143], v1, s[16:19], s2 offen offset:1024 sc0 sc1\nbuffer_load_dwordx4 v[144:147], v1, s[16:19], s2 offen offset:2048 sc0 sc1\n"
"buffer_load_dwordx4 v[148:151], v1, s[16:19], s2 offen offset:3072 sc0 sc1\nbuffer_load_dwordx4 v[152:155], v1, s[16:19], s6 offen offset:0 sc0 sc1\n"
"buffer_load_dwordx4 v[156:159], v1, s[16:19], s6 offen offset:1024 sc0 sc1\nbuffer_load_dwordx4 v[160:163], v1, s[16:19], s6 offen offset:2048 sc0 sc1\nv_lshrrev_b32 v167, 1, v1\n"
"v_and_b32 v167, 24, v167\ns_mul_i32 s2, s5, 896\ns_lshl_b32 s3, s3, 5\ns_add_u32 s2, s2, s3\ns_lshl2_add_u32 s5, s5, 0xc03e000\n"
"buffer_load_dwordx2 v[164:165], v167, s[28:31], s2 offen\nbuffer_load_dword v166, off, s[16:19], s5 sc0 sc1\ns_mov_b32 s90, 0\ns_swappc_b64 s[8:9], s[80:81]\nAD 136,136,140\n"
"AD 137,137,141\nAD 138,138,142\nAD 139,139,143\nAD 144,144,148\nAD 145,145,149\nAD 146,146,150\nAD 147,147,151\nAD 152,152,156\nAD 153,153,157\nAD 154,154,158\nAD 155,155,159\n"
"AD 136,136,144\nAD 137,137,145\nAD 138,138,146\nAD 139,139,147\nAD 152,152,160\nAD 153,153,161\nAD 154,154,162\nAD 155,155,163\nAD 136,136,152\nAD 137,137,153\nAD 138,138,154\n"
"AD 139,139,155\nv_mov_b32 v140, v136\nv_mov_b32 v141, v137\nv_mov_b32 v142, v138\nv_mov_b32 v143, v139\nv_permlane32_swap_b32 v136, v140\nv_permlane32_swap_b32 v137, v141\n"
"v_permlane32_swap_b32 v138, v142\nv_permlane32_swap_b32 v139, v143\nAD 136,136,140\nAD 137,137,141\nAD 138,138,142\nAD 139,139,143\nv_mov_b32 v140, v136\nv_mov_b32 v141, v137\n"
"v_mov_b32 v142, v138\nv_mov_b32 v143, v139\nv_permlane16_swap_b32 v136, v140\nv_permlane16_swap_b32 v137, v141\nv_permlane16_swap_b32 v138, v142\nv_permlane16_swap_b32 v139, v143\n"
"AD 136,136,140\nAD 137,137,141\nAD 138,138,142\nAD 139,139,143\nv_add_f32_dpp v136, v136, v136 row_ror:8 row_mask:0xf bank_mask:0xf\n"
"v_add_f32_dpp v137, v137, v137 row_ror:8 row_mask:0xf bank_mask:0xf\nv_add_f32_dpp v138, v138, v138 row_ror:8 row_mask:0xf bank_mask:0xf\n"
"v_add_f32_dpp v139, v139, v139 row_ror:8 row_mask:0xf bank_mask:0xf\nv_add_f32_dpp v136, v136, v136 row_ror:4 row_mask:0xf bank_mask:0xf\n"
"v_add_f32_dpp v137, v137, v137 row_ror:4 row_mask:0xf bank_mask:0xf\nv_add_f32_dpp v138, v138, v138 row_ror:4 row_mask:0xf bank_mask:0xf\n"
"v_add_f32_dpp v139, v139, v139 row_ror:4 row_mask:0xf bank_mask:0xf\nv_mul_f32 v166, 0xbfb8aa3b, v166\nEX 166,166\nCV 136,136,137\nv_add_f32 v166, 1.0, v166\nv_rcp_f32 v166, v166\n"
"CV 138,138,139\nCV 166,166,166\nv_lshlrev_b32 v166, 16, v166\nv_and_b32 v137, 0xffff0000, v136\nv_lshlrev_b32 v136, 16, v136\nv_and_b32 v139, 0xffff0000, v138\n"
"v_lshlrev_b32 v138, 16, v138\nMU 136,136,166\nMU 137,137,166\nMU 138,138,166\nMU 139,139,166\nCV 136,136,137\nCV 138,138,139\nv_and_b32 v137, 0xffff0000, v136\n"
"v_lshlrev_b32 v136, 16, v136\nv_and_b32 v139, 0xffff0000, v138\nv_lshlrev_b32 v138, 16, v138\nv_lshlrev_b32 v140, 16, v164\nv_and_b32 v141, 0xffff0000, v164\n"
"v_lshlrev_b32 v142, 16, v165\nv_and_b32 v143, 0xffff0000, v165\nAD 136,136,140\nAD 137,137,141\nAD 138,138,142\nAD 139,139,143\nCV 136,136,137\nCV 137,138,139\n"
"v_mov_b32 v138, s15\nv_mov_b32 v139, s71\ns_and_b32 s4, s13, 1\ns_lshl1_add_u32 s4, s4, s11\ns_lshl2_add_u32 s4, s12, s4\ns_lshr_b32 s5, s71, 16\ns_cmp_lt_u32 s4, 28\n"
"s_cselect_b32 s5, s71, s5\ns_cselect_b32 s3, 0, 28\ns_and_b32 s5, s5, 0x7ff\ns_sub_u32 s3, s4, s3\nv_mov_b32 v140, 0x40000000\nv_cmp_gt_u32 vcc, 64, v1\ns_lshl_b32 s4, s4, 6\n"
"s_add_u32 s4, s4, 0xc04b800\nv_cndmask_b32 v140, v140, v1, vcc\nSv 136,140,16,4,0\ns_mov_b32 s90, 100\ns_swappc_b64 s[8:9], s[80:81]\n.T64:\ns_mov_b32 s62, 0\n"
"s_call_b64 s[80:81], .RB11\ns_branch .T65\n.T66:\ns_mov_b32 s62, 0\ns_call_b64 s[80:81], .RA10\ns_branch .T67\n.T70:\ns_mov_b32 s62, 0\ns_call_b64 s[80:81], .RB11\ns_branch .T71\n"
".T72:\ns_mov_b32 s62, 0\ns_call_b64 s[80:81], .RA10\ns_branch .T73\n.T75:\ns_mov_b32 s62, 0\ns_call_b64 s[80:81], .RB11\ns_branch .T76\n.T77:\ns_mov_b32 s62, 0\n"
"s_call_b64 s[80:81], .RA10\ns_branch .T78\n.S83:\ns_mov_b32 s7, 262144\n.S85:\nbuffer_load_dwordx4 v[100:103], v108, s[16:19], s65 offen offset:0 sc0 sc1\n"
"buffer_load_dwordx4 v[104:107], v108, s[16:19], s65 offen offset:256 sc0 sc1\nWV 0\nv_xor_b32 v109, s15, v102\nv_xor_b32 v110, s15, v106\nv_or_b32 v109, v109, v110\n"
"v_cmp_ne_u32 vcc, 0, v109\ns_nop 0\ns_cbranch_vccz .S84\ns_sleep 6\ns_sub_u32 s7, s7, 1\ns_cbranch_scc0 .S85\ns_branch .S84\n.D81:\nv_mov_b32 v130, 0\nv_mov_b32 v131, 0\n"
"s_mov_b32 s63, 0xff800000\ns_mov_b32 s67, 1\ns_branch .D82\n.T87:\ns_lshl3_add_u32 s62, s67, 2\ns_call_b64 s[80:81], .RB11\ns_cmp_eq_u32 s67, 0\ns_cbranch_scc1 .T88\nSU 20,100,88\n"
"v_min_f32 v20, 0, v20\nEX 20,20\ns_nop 0\nFM 86,101,20\nSU 21,104,89\nv_min_f32 v21, 0, v21\nEX 21,21\ns_nop 0\nFM 87,105,21\nAR 22,192\nv_and_b32 v23, 0xffff0000, v22\n"
"v_lshlrev_b32 v22, 16, v22\nMU 22,22,20\nMU 23,23,20\nCV 22,22,23\nAW 192,22\nAR 22,193\nv_and_b32 v23, 0xffff0000, v22\nv_lshlrev_b32 v22, 16, v22\nMU 22,22,20\nMU 23,23,20\n"
"CV 22,22,23\nAW 193,22\nAR 22,194\nv_and_b32 v23, 0xffff0000, v22\nv_lshlrev_b32 v22, 16, v22\nMU 22,22,20\nMU 23,23,20\nCV 22,22,23\nAW 194,22\nAR 22,195\n"
"v_and_b32 v23, 0xffff0000, v22\nv_lshlrev_b32 v22, 16, v22\nMU 22,22,20\nMU 23,23,20\nCV 22,22,23\nAW 195,22\nAR 22,196\nv_and_b32 v23, 0xffff0000, v22\n"
"v_lshlrev_b32 v22, 16, v22\nMU 22,22,21\nMU 23,23,21\nCV 22,22,23\nAW 196,22\nAR 22,197\nv_and_b32 v23, 0xffff0000, v22\nv_lshlrev_b32 v22, 16, v22\nMU 22,22,21\nMU 23,23,21\n"
"CV 22,22,23\nAW 197,22\nAR 22,198\nv_and_b32 v23, 0xffff0000, v22\nv_lshlrev_b32 v22, 16, v22\nMU 22,22,21\nMU 23,23,21\nCV 22,22,23\nAW 198,22\nAR 22,199\n"
"v_and_b32 v23, 0xffff0000, v22\nv_lshlrev_b32 v22, 16, v22\nMU 22,22,21\nMU 23,23,21\nCV 22,22,23\nAW 199,22\nAR 22,200\nv_and_b32 v23, 0xffff0000, v22\n"
"v_lshlrev_b32 v22, 16, v22\nMU 22,22,20\nMU 23,23,20\nCV 22,22,23\nAW 200,22\nAR 22,201\nv_and_b32 v23, 0xffff0000, v22\nv_lshlrev_b32 v22, 16, v22\nMU 22,22,20\nMU 23,23,20\n"
"CV 22,22,23\nAW 201,22\nAR 22,202\nv_and_b32 v23, 0xffff0000, v22\nv_lshlrev_b32 v22, 16, v22\nMU 22,22,20\nMU 23,23,20\nCV 22,22,23\nAW 202,22\nAR 22,203\n"
"v_and_b32 v23, 0xffff0000, v22\nv_lshlrev_b32 v22, 16, v22\nMU 22,22,20\nMU 23,23,20\nCV 22,22,23\nAW 203,22\nAR 22,204\nv_and_b32 v23, 0xffff0000, v22\n"
"v_lshlrev_b32 v22, 16, v22\nMU 22,22,21\nMU 23,23,21\nCV 22,22,23\nAW 204,22\nAR 22,205\nv_and_b32 v23, 0xffff0000, v22\nv_lshlrev_b32 v22, 16, v22\nMU 22,22,21\nMU 23,23,21\n"
"CV 22,22,23\nAW 205,22\nAR 22,206\nv_and_b32 v23, 0xffff0000, v22\nv_lshlrev_b32 v22, 16, v22\nMU 22,22,21\nMU 23,23,21\nCV 22,22,23\nAW 206,22\nAR 22,207\n"
"v_and_b32 v23, 0xffff0000, v22\nv_lshlrev_b32 v22, 16, v22\nMU 22,22,21\nMU 23,23,21\nCV 22,22,23\nAW 207,22\nAR 22,208\nv_and_b32 v23, 0xffff0000, v22\n"
"v_lshlrev_b32 v22, 16, v22\nMU 22,22,20\nMU 23,23,20\nCV 22,22,23\nAW 208,22\nAR 22,209\nv_and_b32 v23, 0xffff0000, v22\nv_lshlrev_b32 v22, 16, v22\nMU 22,22,20\nMU 23,23,20\n"
"CV 22,22,23\nAW 209,22\nAR 22,210\nv_and_b32 v23, 0xffff0000, v22\nv_lshlrev_b32 v22, 16, v22\nMU 22,22,20\nMU 23,23,20\nCV 22,22,23\nAW 210,22\nAR 22,211\n"
"v_and_b32 v23, 0xffff0000, v22\nv_lshlrev_b32 v22, 16, v22\nMU 22,22,20\nMU 23,23,20\nCV 22,22,23\nAW 211,22\nAR 22,212\nv_and_b32 v23, 0xffff0000, v22\n"
"v_lshlrev_b32 v22, 16, v22\nMU 22,22,21\nMU 23,23,21\nCV 22,22,23\nAW 212,22\nAR 22,213\nv_and_b32 v23, 0xffff0000, v22\nv_lshlrev_b32 v22, 16, v22\nMU 22,22,21\nMU 23,23,21\n"
"CV 22,22,23\nAW 213,22\nAR 22,214\nv_and_b32 v23, 0xffff0000, v22\nv_lshlrev_b32 v22, 16, v22\nMU 22,22,21\nMU 23,23,21\nCV 22,22,23\nAW 214,22\nAR 22,215\n"
"v_and_b32 v23, 0xffff0000, v22\nv_lshlrev_b32 v22, 16, v22\nMU 22,22,21\nMU 23,23,21\nCV 22,22,23\nAW 215,22\nAR 22,216\nv_and_b32 v23, 0xffff0000, v22\n"
"v_lshlrev_b32 v22, 16, v22\nMU 22,22,20\nMU 23,23,20\nCV 22,22,23\nAW 216,22\nAR 22,217\nv_and_b32 v23, 0xffff0000, v22\nv_lshlrev_b32 v22, 16, v22\nMU 22,22,20\nMU 23,23,20\n"
"CV 22,22,23\nAW 217,22\nAR 22,218\nv_and_b32 v23, 0xffff0000, v22\nv_lshlrev_b32 v22, 16, v22\nMU 22,22,20\nMU 23,23,20\nCV 22,22,23\nAW 218,22\nAR 22,219\n"
"v_and_b32 v23, 0xffff0000, v22\nv_lshlrev_b32 v22, 16, v22\nMU 22,22,20\nMU 23,23,20\nCV 22,22,23\nAW 219,22\nAR 22,220\nv_and_b32 v23, 0xffff0000, v22\n"
"v_lshlrev_b32 v22, 16, v22\nMU 22,22,21\nMU 23,23,21\nCV 22,22,23\nAW 220,22\nAR 22,221\nv_and_b32 v23, 0xffff0000, v22\nv_lshlrev_b32 v22, 16, v22\nMU 22,22,21\nMU 23,23,21\n"
"CV 22,22,23\nAW 221,22\nAR 22,222\nv_and_b32 v23, 0xffff0000, v22\nv_lshlrev_b32 v22, 16, v22\nMU 22,22,21\nMU 23,23,21\nCV 22,22,23\nAW 222,22\nAR 22,223\n"
"v_and_b32 v23, 0xffff0000, v22\nv_lshlrev_b32 v22, 16, v22\nMU 22,22,21\nMU 23,23,21\nCV 22,22,23\nAW 223,22\nAR 22,224\nv_and_b32 v23, 0xffff0000, v22\n"
"v_lshlrev_b32 v22, 16, v22\nMU 22,22,20\nMU 23,23,20\nCV 22,22,23\nAW 224,22\nAR 22,225\nv_and_b32 v23, 0xffff0000, v22\nv_lshlrev_b32 v22, 16, v22\nMU 22,22,20\nMU 23,23,20\n"
"CV 22,22,23\nAW 225,22\nAR 22,226\nv_and_b32 v23, 0xffff0000, v22\nv_lshlrev_b32 v22, 16, v22\nMU 22,22,20\nMU 23,23,20\nCV 22,22,23\nAW 226,22\nAR 22,227\n"
"v_and_b32 v23, 0xffff0000, v22\nv_lshlrev_b32 v22, 16, v22\nMU 22,22,20\nMU 23,23,20\nCV 22,22,23\nAW 227,22\nAR 22,228\nv_and_b32 v23, 0xffff0000, v22\n"
"v_lshlrev_b32 v22, 16, v22\nMU 22,22,21\nMU 23,23,21\nCV 22,22,23\nAW 228,22\nAR 22,229\nv_and_b32 v23, 0xffff0000, v22\nv_lshlrev_b32 v22, 16, v22\nMU 22,22,21\nMU 23,23,21\n"
"CV 22,22,23\nAW 229,22\nAR 22,230\nv_and_b32 v23, 0xffff0000, v22\nv_lshlrev_b32 v22, 16, v22\nMU 22,22,21\nMU 23,23,21\nCV 22,22,23\nAW 230,22\nAR 22,231\n"
"v_and_b32 v23, 0xffff0000, v22\nv_lshlrev_b32 v22, 16, v22\nMU 22,22,21\nMU 23,23,21\nCV 22,22,23\nAW 231,22\nAR 22,232\nv_and_b32 v23, 0xffff0000, v22\n"
"v_lshlrev_b32 v22, 16, v22\nMU 22,22,20\nMU 23,23,20\nCV 22,22,23\nAW 232,22\nAR 22,233\nv_and_b32 v23, 0xffff0000, v22\nv_lshlrev_b32 v22, 16, v22\nMU 22,22,20\nMU 23,23,20\n"
"CV 22,22,23\nAW 233,22\nAR 22,234\nv_and_b32 v23, 0xffff0000, v22\nv_lshlrev_b32 v22, 16, v22\nMU 22,22,20\nMU 23,23,20\nCV 22,22,23\nAW 234,22\nAR 22,235\n"
"v_and_b32 v23, 0xffff0000, v22\nv_lshlrev_b32 v22, 16, v22\nMU 22,22,20\nMU 23,23,20\nCV 22,22,23\nAW 235,22\nAR 22,236\nv_and_b32 v23, 0xffff0000, v22\n"
"v_lshlrev_b32 v22, 16, v22\nMU 22,22,21\nMU 23,23,21\nCV 22,22,23\nAW 236,22\nAR 22,237\nv_and_b32 v23, 0xffff0000, v22\nv_lshlrev_b32 v22, 16, v22\nMU 22,22,21\nMU 23,23,21\n"
"CV 22,22,23\nAW 237,22\nAR 22,238\nv_and_b32 v23, 0xffff0000, v22\nv_lshlrev_b32 v22, 16, v22\nMU 22,22,21\nMU 23,23,21\nCV 22,22,23\nAW 238,22\nAR 22,239\n"
"v_and_b32 v23, 0xffff0000, v22\nv_lshlrev_b32 v22, 16, v22\nMU 22,22,21\nMU 23,23,21\nCV 22,22,23\nAW 239,22\nAR 22,240\nv_and_b32 v23, 0xffff0000, v22\n"
"v_lshlrev_b32 v22, 16, v22\nMU 22,22,20\nMU 23,23,20\nCV 22,22,23\nAW 240,22\nAR 22,241\nv_and_b32 v23, 0xffff0000, v22\nv_lshlrev_b32 v22, 16, v22\nMU 22,22,20\nMU 23,23,20\n"
"CV 22,22,23\nAW 241,22\nAR 22,242\nv_and_b32 v23, 0xffff0000, v22\nv_lshlrev_b32 v22, 16, v22\nMU 22,22,20\nMU 23,23,20\nCV 22,22,23\nAW 242,22\nAR 22,243\n"
"v_and_b32 v23, 0xffff0000, v22\nv_lshlrev_b32 v22, 16, v22\nMU 22,22,20\nMU 23,23,20\nCV 22,22,23\nAW 243,22\nAR 22,244\nv_and_b32 v23, 0xffff0000, v22\n"
"v_lshlrev_b32 v22, 16, v22\nMU 22,22,21\nMU 23,23,21\nCV 22,22,23\nAW 244,22\nAR 22,245\nv_and_b32 v23, 0xffff0000, v22\nv_lshlrev_b32 v22, 16, v22\nMU 22,22,21\nMU 23,23,21\n"
"CV 22,22,23\nAW 245,22\nAR 22,246\nv_and_b32 v23, 0xffff0000, v22\nv_lshlrev_b32 v22, 16, v22\nMU 22,22,21\nMU 23,23,21\nCV 22,22,23\nAW 246,22\nAR 22,247\n"
"v_and_b32 v23, 0xffff0000, v22\nv_lshlrev_b32 v22, 16, v22\nMU 22,22,21\nMU 23,23,21\nCV 22,22,23\nAW 247,22\nAR 22,248\nv_and_b32 v23, 0xffff0000, v22\n"
"v_lshlrev_b32 v22, 16, v22\nMU 22,22,20\nMU 23,23,20\nCV 22,22,23\nAW 248,22\nAR 22,249\nv_and_b32 v23, 0xffff0000, v22\nv_lshlrev_b32 v22, 16, v22\nMU 22,22,20\nMU 23,23,20\n"
"CV 22,22,23\nAW 249,22\nAR 22,250\nv_and_b32 v23, 0xffff0000, v22\nv_lshlrev_b32 v22, 16, v22\nMU 22,22,20\nMU 23,23,20\nCV 22,22,23\nAW 250,22\nAR 22,251\n"
"v_and_b32 v23, 0xffff0000, v22\nv_lshlrev_b32 v22, 16, v22\nMU 22,22,20\nMU 23,23,20\nCV 22,22,23\nAW 251,22\nAR 22,252\nv_and_b32 v23, 0xffff0000, v22\n"
"v_lshlrev_b32 v22, 16, v22\nMU 22,22,21\nMU 23,23,21\nCV 22,22,23\nAW 252,22\nAR 22,253\nv_and_b32 v23, 0xffff0000, v22\nv_lshlrev_b32 v22, 16, v22\nMU 22,22,21\nMU 23,23,21\n"
"CV 22,22,23\nAW 253,22\nAR 22,254\nv_and_b32 v23, 0xffff0000, v22\nv_lshlrev_b32 v22, 16, v22\nMU 22,22,21\nMU 23,23,21\nCV 22,22,23\nAW 254,22\nAR 22,255\n"
"v_and_b32 v23, 0xffff0000, v22\nv_lshlrev_b32 v22, 16, v22\nMU 22,22,21\nMU 23,23,21\nCV 22,22,23\nAW 255,22\nv_lshrrev_b32 v22, 8, v1\nv_bfe_u32 v23, v1, 6, 2\n"
"v_cmp_eq_u32 vcc, v22, v23\nv_and_b32 v22, 16, v1\nv_mov_b32 v23, 0x3f80\nv_cndmask_b32 v23, 0, v23, vcc\nv_lshlrev_b32 v23, v22, v23\nv_and_b32 v22, 32, v1\n"
"v_cmp_eq_u32 vcc, 0, v22\nv_mov_b32 v128, 0\nv_mov_b32 v129, 0\nv_cndmask_b32 v130, 0, v23, vcc\nv_sub_u32 v131, v23, v130\nv_mov_b32 v132, 0\nv_mov_b32 v133, 0\ns_nop 1\n"
"Mava 0,130,192\nMava 8,128,192\nMava 12,128,196\nMava 4,130,196\nMava 16,130,200\nMava 24,128,200\nMava 28,128,204\nMava 20,130,204\nMava 32,130,208\nMava 40,128,208\n"
"Mava 44,128,212\nMava 36,130,212\nMava 48,130,216\nMava 56,128,216\nMava 60,128,220\nMava 52,130,220\nMava 64,130,224\nMava 72,128,224\nMava 76,128,228\nMava 68,130,228\n"
"Mava 80,130,232\nMava 88,128,232\nMava 92,128,236\nMava 84,130,236\nMava 96,130,240\nMava 104,128,240\nMava 108,128,244\nMava 100,130,244\nMava 112,130,248\nMava 120,128,248\n"
"Mava 124,128,252\nMava 116,130,252\ns_nop 7\ns_branch .T88\n.S90:\ns_mov_b32 s7, 262144\n.S92:\ns_add_u32 s0, s92, 0\n"
"buffer_load_dwordx4 v[208:211], v1, s[16:19], s0 offen offset:0 sc0 sc1\nbuffer_load_dwordx4 v[212:215], v1, s[16:19], s0 offen offset:1024 sc0 sc1\nWV 0\n"
"v_max_u32 v21, v211, v215\nv_cmp_eq_u32 vcc, -1, v21\ns_nop 0\ns_cbranch_vccz .S91\ns_sleep 6\ns_sub_u32 s7, s7, 1\ns_cbranch_scc0 .S92\ns_branch .S91\n.S93:\n"
"s_mov_b32 s7, 262144\n.S95:\ns_add_u32 s0, s92, 131072\nbuffer_load_dwordx4 v[216:219], v1, s[16:19], s0 offen offset:0 sc0 sc1\n"
"buffer_load_dwordx4 v[220:223], v1, s[16:19], s0 offen offset:1024 sc0 sc1\nWV 0\nv_max_u32 v21, v219, v223\nv_cmp_eq_u32 vcc, -1, v21\ns_nop 0\ns_cbranch_vccz .S94\ns_sleep 6\n"
"s_sub_u32 s7, s7, 1\ns_cbranch_scc0 .S95\ns_branch .S94\n.S96:\ns_mov_b32 s7, 262144\n.S98:\ns_add_u32 s0, s92, 262144\n"
"buffer_load_dwordx4 v[224:227], v1, s[16:19], s0 offen offset:0 sc0 sc1\nbuffer_load_dwordx4 v[228:231], v1, s[16:19], s0 offen offset:1024 sc0 sc1\nWV 0\n"
"v_max_u32 v21, v227, v231\nv_cmp_eq_u32 vcc, -1, v21\ns_nop 0\ns_cbranch_vccz .S97\ns_sleep 6\ns_sub_u32 s7, s7, 1\ns_cbranch_scc0 .S98\ns_branch .S97\n.S99:\n"
"s_mov_b32 s7, 262144\n.S101:\ns_add_u32 s0, s92, 393216\nbuffer_load_dwordx4 v[208:211], v1, s[16:19], s0 offen offset:0 sc0 sc1\n"
"buffer_load_dwordx4 v[212:215], v1, s[16:19], s0 offen offset:1024 sc0 sc1\nWV 0\nv_max_u32 v21, v211, v215\nv_cmp_eq_u32 vcc, -1, v21\ns_nop 0\ns_cbranch_vccz .S100\ns_sleep 6\n"
"s_sub_u32 s7, s7, 1\ns_cbranch_scc0 .S101\ns_branch .S100\n.S102:\ns_mov_b32 s7, 262144\n.S104:\ns_add_u32 s0, s92, 524288\n"
"buffer_load_dwordx4 v[216:219], v1, s[16:19], s0 offen offset:0 sc0 sc1\nbuffer_load_dwordx4 v[220:223], v1, s[16:19], s0 offen offset:1024 sc0 sc1\nWV 0\n"
"v_max_u32 v21, v219, v223\nv_cmp_eq_u32 vcc, -1, v21\ns_nop 0\ns_cbranch_vccz .S103\ns_sleep 6\ns_sub_u32 s7, s7, 1\ns_cbranch_scc0 .S104\ns_branch .S103\n.S105:\n"
"s_mov_b32 s7, 262144\n.S107:\ns_add_u32 s0, s92, 655360\nbuffer_load_dwordx4 v[224:227], v1, s[16:19], s0 offen offset:0 sc0 sc1\n"
"buffer_load_dwordx4 v[228:231], v1, s[16:19], s0 offen offset:1024 sc0 sc1\nWV 0\nv_max_u32 v21, v227, v231\nv_cmp_eq_u32 vcc, -1, v21\ns_nop 0\ns_cbranch_vccz .S106\ns_sleep 6\n"
"s_sub_u32 s7, s7, 1\ns_cbranch_scc0 .S107\ns_branch .S106\n.S108:\ns_mov_b32 s7, 262144\n.S110:\ns_add_u32 s0, s92, 786432\n"
"buffer_load_dwordx4 v[208:211], v1, s[16:19], s0 offen offset:0 sc0 sc1\nbuffer_load_dwordx4 v[212:215], v1, s[16:19], s0 offen offset:1024 sc0 sc1\nWV 0\n"
"v_max_u32 v21, v211, v215\nv_cmp_eq_u32 vcc, -1, v21\ns_nop 0\ns_cbranch_vccz .S109\ns_sleep 6\ns_sub_u32 s7, s7, 1\ns_cbranch_scc0 .S110\ns_branch .S109\n.S111:\n"
"s_mov_b32 s7, 262144\n.S113:\nbuffer_load_dwordx4 v[64:67], v89, s[16:19], s40 offen offset:0 sc0 sc1\nbuffer_load_dwordx4 v[68:71], v89, s[16:19], s40 offen offset:16 sc0 sc1\n"
"buffer_load_dwordx4 v[72:75], v89, s[16:19], s40 offen offset:1792 sc0 sc1\nbuffer_load_dwordx4 v[76:79], v89, s[16:19], s40 offen offset:1808 sc0 sc1\n"
"buffer_load_dwordx2 v[80:81], v88, s[16:19], s63 offen offset:0 sc0 sc1\nbuffer_load_dwordx2 v[82:83], v88, s[16:19], s63 offen offset:512 sc0 sc1\nWV 0\nv_xor_b32 v66, s15, v66\n"
"v_xor_b32 v70, s15, v70\nv_xor_b32 v74, s15, v74\nv_xor_b32 v78, s15, v78\nv_xor_b32 v81, s15, v81\nv_xor_b32 v83, s15, v83\nv_or3_b32 v92, v66, v70, v74\n"
"v_or3_b32 v92, v92, v78, v81\nv_or_b32 v92, v92, v83\nv_cmp_ne_u32 vcc, 0, v92\ns_nop 0\ns_cbranch_vccz .S112\ns_sleep 6\ns_sub_u32 s7, s7, 1\ns_cbranch_scc0 .S113\n"
"s_branch .S112\n.S114:\ns_mov_b32 s7, 262144\n.S116:\nbuffer_load_dword v27, off, s[16:19], s60 offset:512 sc0 sc1\nWV 0\nv_cmp_ne_u32 vcc, s15, v27\ns_nop 0\n"
"s_cbranch_vccz .S115\ns_sleep 6\ns_sub_u32 s7, s7, 1\ns_cbranch_scc0 .S116\ns_branch .S115\n.S117:\ns_mov_b32 s7, 262144\n.S119:\n"
"buffer_load_dword v27, v24, s[16:19], s41 offen sc0 sc1\nWV 0\nv_xor_b32 v27, s15, v27\nv_and_b32 v26, 0xffffdfff, v27\nv_cmp_ne_u32 s[64:65], 0, v27\nv_cmp_ne_u32 vcc, 0, v26\n"
"s_nop 0\ns_cbranch_vccz .S118\ns_sleep 6\ns_sub_u32 s7, s7, 1\ns_cbranch_scc0 .S119\ns_branch .S118\n.Z121:\ns_sub_u32 s7, s7, 1\ns_cbranch_scc1 .Z122\ns_cmp_lg_u64 s[44:45], 0\n"
"s_cbranch_scc0 .Z124\nbuffer_load_dwordx4 v[96:99], v1, s[16:19], s24 offen offset:0 sc0 sc1\nbuffer_load_dwordx4 v[100:103], v1, s[16:19], s24 offen offset:1024 sc0 sc1\n.Z124:\n"
"s_cmp_lg_u64 s[46:47], 0\ns_cbranch_scc0 .Z125\nbuffer_load_dwordx4 v[104:107], v1, s[16:19], s25 offen offset:0 sc0 sc1\n"
"buffer_load_dwordx4 v[108:111], v1, s[16:19], s25 offen offset:1024 sc0 sc1\n.Z125:\ns_cmp_lg_u64 s[48:49], 0\ns_cbranch_scc0 .Z126\n"
"buffer_load_dwordx4 v[112:115], v1, s[16:19], s26 offen offset:0 sc0 sc1\nbuffer_load_dwordx4 v[116:119], v1, s[16:19], s26 offen offset:1024 sc0 sc1\n.Z126:\n"
"s_cmp_lg_u64 s[50:51], 0\ns_cbranch_scc0 .Z127\nbuffer_load_dwordx4 v[120:123], v1, s[16:19], s27 offen offset:0 sc0 sc1\n"
"buffer_load_dwordx4 v[124:127], v1, s[16:19], s27 offen offset:1024 sc0 sc1\n.Z127:\ns_cmp_lg_u64 s[52:53], 0\ns_cbranch_scc0 .Z128\n"
"buffer_load_dwordx4 v[32:35], v1, s[16:19], s32 offen offset:0 sc0 sc1\nbuffer_load_dwordx4 v[36:39], v1, s[16:19], s32 offen offset:1024 sc0 sc1\n.Z128:\n"
"s_cmp_lg_u64 s[54:55], 0\ns_cbranch_scc0 .Z129\nbuffer_load_dwordx4 v[40:43], v1, s[16:19], s33 offen offset:0 sc0 sc1\n"
"buffer_load_dwordx4 v[44:47], v1, s[16:19], s33 offen offset:1024 sc0 sc1\n.Z129:\ns_cmp_lg_u64 s[56:57], 0\ns_cbranch_scc0 .Z130\n"
"buffer_load_dwordx4 v[48:51], v1, s[16:19], s34 offen offset:0 sc0 sc1\nbuffer_load_dwordx4 v[52:55], v1, s[16:19], s34 offen offset:1024 sc0 sc1\n.Z130:\n"
"s_cmp_lg_u64 s[58:59], 0\ns_cbranch_scc0 .Z131\nbuffer_load_dwordx4 v[56:59], v1, s[16:19], s35 offen offset:0 sc0 sc1\n"
"buffer_load_dwordx4 v[60:63], v1, s[16:19], s35 offen offset:1024 sc0 sc1\n.Z131:\ns_sleep 8\nWV 0\ns_branch .Z123\n.RA10:\ns_nop 7\ns_nop 3\nv_max3_f32 v22, v32, v33, v34\n"
"v_max3_f32 v22, v22, v35, v40\nv_max3_f32 v22, v22, v41, v42\nv_max_f32 v22, v22, v43\nv_mov_b32 v91, v22\ns_nop 1\nv_permlane32_swap_b32 v22, v91\nv_max_f32 v22, v22, v91\n"
"v_mov_b32 v91, v22\ns_nop 1\nv_permlane16_swap_b32 v22, v91\nv_max_f32 v22, v22, v91\nv_ceil_f32 v22, v22\nv_max3_f32 v23, v36, v37, v38\nv_max3_f32 v23, v23, v39, v44\n"
"v_max3_f32 v23, v23, v45, v46\nv_max_f32 v23, v23, v47\nv_mov_b32 v91, v23\ns_nop 1\nv_permlane32_swap_b32 v23, v91\nv_max_f32 v23, v23, v91\nv_mov_b32 v91, v23\ns_nop 1\n"
"v_permlane16_swap_b32 v23, v91\nv_max_f32 v23, v23, v91\nv_ceil_f32 v23, v23\ns_bitcmp1_b32 s62, 0\ns_cbranch_scc0 .R133\nv_cmp_lt_f32 vcc, 0xff000000, v22\ns_nop 1\n"
"v_cndmask_b32 v22, 0, v22, vcc\nv_cmp_lt_f32 vcc, 0xff000000, v23\ns_nop 1\nv_cndmask_b32 v23, 0, v23, vcc\ns_branch .R134\n.R133:\nv_max_f32 v22, 0, v22\nv_max_f32 v23, 0, v23\n"
"s_bitcmp1_b32 s62, 3\ns_cbranch_scc0 .R134\nv_max_f32 v22, v20, v22\nv_max_f32 v23, v21, v23\n.R134:\nAD 88,88,22\nAD 89,89,23\nSU 32,32,22\nSU 33,33,22\nSU 34,34,22\nSU 35,35,22\n"
"SU 36,36,23\nSU 37,37,23\nSU 38,38,23\nSU 39,39,23\nSU 40,40,22\nSU 41,41,22\nSU 42,42,22\nSU 43,43,22\nSU 44,44,23\nSU 45,45,23\nSU 46,46,23\nSU 47,47,23\nv_mov_b32 v90, v80\n"
"v_mov_b32 v91, v81\nEX 80,32\nEX 81,33\ns_nop 0\nAD 84,80,81\nCV 64,80,81\nEX 82,34\nEX 83,35\nAD 84,84,82\nAD 84,84,83\nCV 65,82,83\nEX 80,36\nEX 81,37\ns_nop 0\nAD 85,80,81\n"
"CV 68,80,81\nEX 82,38\nEX 83,39\nAD 85,85,82\nAD 85,85,83\nCV 69,82,83\nEX 80,40\nEX 81,41\nAD 84,84,80\nAD 84,84,81\nCV 66,80,81\nEX 82,42\nEX 83,43\nAD 84,84,82\nAD 84,84,83\n"
"CV 67,82,83\nEX 80,44\nEX 81,45\nAD 85,85,80\nAD 85,85,81\nCV 70,80,81\nEX 82,46\nEX 83,47\nAD 85,85,82\nAD 85,85,83\nCV 71,82,83\nv_mov_b32 v80, v90\nv_mov_b32 v81, v91\n"
"s_bitcmp1_b32 s62, 1\ns_cbranch_scc1 .R135\nSU 48,48,22\nSU 49,49,22\nSU 50,50,22\nSU 51,51,22\nSU 52,52,23\nSU 53,53,23\nSU 54,54,23\nSU 55,55,23\nSU 56,56,22\nSU 57,57,22\n"
"SU 58,58,22\nSU 59,59,22\nSU 60,60,23\nSU 61,61,23\nSU 62,62,23\nSU 63,63,23\nSU 4,4,22\nSU 5,5,22\nSU 6,6,22\nSU 7,7,22\nSU 8,8,23\nSU 9,9,23\nSU 10,10,23\nSU 11,11,23\n"
"SU 12,12,22\nSU 13,13,22\nSU 14,14,22\nSU 15,15,22\nSU 16,16,23\nSU 17,17,23\nSU 18,18,23\nSU 19,19,23\nEX 80,48\nEX 81,49\n.R135:\ns_bitcmp1_b32 s62, 0\ns_cbranch_scc0 .RS12\n"
"s_nop 1\ns_setpc_b64 s[80:81]\n.RB11:\ns_nop 7\ns_nop 3\nv_max3_f32 v22, v48, v49, v50\nv_max3_f32 v22, v22, v51, v56\nv_max3_f32 v22, v22, v57, v58\nv_max_f32 v22, v22, v59\n"
"v_mov_b32 v91, v22\ns_nop 1\nv_permlane32_swap_b32 v22, v91\nv_max_f32 v22, v22, v91\nv_mov_b32 v91, v22\ns_nop 1\nv_permlane16_swap_b32 v22, v91\nv_max_f32 v22, v22, v91\n"
"v_ceil_f32 v22, v22\nv_max3_f32 v23, v52, v53, v54\nv_max3_f32 v23, v23, v55, v60\nv_max3_f32 v23, v23, v61, v62\nv_max_f32 v23, v23, v63\nv_mov_b32 v91, v23\ns_nop 1\n"
"v_permlane32_swap_b32 v23, v91\nv_max_f32 v23, v23, v91\nv_mov_b32 v91, v23\ns_nop 1\nv_permlane16_swap_b32 v23, v91\nv_max_f32 v23, v23, v91\nv_ceil_f32 v23, v23\n"
"s_bitcmp1_b32 s62, 0\ns_cbranch_scc0 .R138\nv_cmp_lt_f32 vcc, 0xff000000, v22\nv_cndmask_b32 v22, 0, v22, vcc\nv_cmp_lt_f32 vcc, 0xff000000, v23\nv_cndmask_b32 v23, 0, v23, vcc\n"
"s_branch .R139\n.R138:\nv_max_f32 v22, 0, v22\nv_max_f32 v23, 0, v23\ns_bitcmp1_b32 s62, 3\ns_cbranch_scc0 .R139\nv_max_f32 v22, v20, v22\nv_max_f32 v23, v21, v23\n.R139:\n"
"AD 88,88,22\nAD 89,89,23\nSU 48,48,22\nSU 49,49,22\nSU 50,50,22\nSU 51,51,22\nSU 52,52,23\nSU 53,53,23\nSU 54,54,23\nSU 55,55,23\nSU 56,56,22\nSU 57,57,22\nSU 58,58,22\n"
"SU 59,59,22\nSU 60,60,23\nSU 61,61,23\nSU 62,62,23\nSU 63,63,23\nv_mov_b32 v90, v80\nv_mov_b32 v91, v81\nEX 80,48\nEX 81,49\ns_nop 0\nAD 84,80,81\nCV 72,80,81\nEX 82,50\nEX 83,51\n"
"AD 84,84,82\nAD 84,84,83\nCV 73,82,83\nEX 80,52\nEX 81,53\ns_nop 0\nAD 85,80,81\nCV 76,80,81\nEX 82,54\nEX 83,55\nAD 85,85,82\nAD 85,85,83\nCV 77,82,83\nEX 80,56\nEX 81,57\n"
"AD 84,84,80\nAD 84,84,81\nCV 74,80,81\nEX 82,58\nEX 83,59\nAD 84,84,82\nAD 84,84,83\nCV 75,82,83\nEX 80,60\nEX 81,61\nAD 85,85,80\nAD 85,85,81\nCV 78,80,81\nEX 82,62\nEX 83,63\n"
"AD 85,85,82\nAD 85,85,83\nCV 79,82,83\nv_mov_b32 v80, v90\nv_mov_b32 v81, v91\ns_bitcmp1_b32 s62, 1\ns_cbranch_scc1 .R140\nSU 32,32,22\nSU 33,33,22\nSU 34,34,22\nSU 35,35,22\n"
"SU 36,36,23\nSU 37,37,23\nSU 38,38,23\nSU 39,39,23\nSU 40,40,22\nSU 41,41,22\nSU 42,42,22\nSU 43,43,22\nSU 44,44,23\nSU 45,45,23\nSU 46,46,23\nSU 47,47,23\nSU 4,4,22\nSU 5,5,22\n"
"SU 6,6,22\nSU 7,7,22\nSU 8,8,23\nSU 9,9,23\nSU 10,10,23\nSU 11,11,23\nSU 12,12,22\nSU 13,13,22\nSU 14,14,22\nSU 15,15,22\nSU 16,16,23\nSU 17,17,23\nSU 18,18,23\nSU 19,19,23\n"
"EX 80,32\nEX 81,33\n.R140:\ns_bitcmp1_b32 s62, 0\ns_cbranch_scc0 .RS12\ns_nop 1\ns_setpc_b64 s[80:81]\n.RS12:\nv_exp_f32 v22, -v22\nv_exp_f32 v23, -v23\nMU 86,86,22\nMU 87,87,23\n"
"AR 91,0\nMU 91,91,22\nAW 0,91\nAR 91,1\nMU 91,91,22\nAW 1,91\nAR 91,2\nMU 91,91,22\nAW 2,91\nAR 91,3\nMU 91,91,22\nAW 3,91\nAR 91,4\nMU 91,91,23\nAW 4,91\nAR 91,5\nMU 91,91,23\n"
"AW 5,91\nAR 91,6\nMU 91,91,23\nAW 6,91\nAR 91,7\nMU 91,91,23\nAW 7,91\nAR 91,8\nMU 91,91,22\nAW 8,91\nAR 91,9\nMU 91,91,22\nAW 9,91\nAR 91,10\nMU 91,91,22\nAW 10,91\nAR 91,11\n"
"MU 91,91,22\nAW 11,91\nAR 91,12\nMU 91,91,23\nAW 12,91\nAR 91,13\nMU 91,91,23\nAW 13,91\nAR 91,14\nMU 91,91,23\nAW 14,91\nAR 91,15\nMU 91,91,23\nAW 15,91\nAR 91,16\nMU 91,91,22\n"
"AW 16,91\nAR 91,17\nMU 91,91,22\nAW 17,91\nAR 91,18\nMU 91,91,22\nAW 18,91\nAR 91,19\nMU 91,91,22\nAW 19,91\nAR 91,20\nMU 91,91,23\nAW 20,91\nAR 91,21\nMU 91,91,23\nAW 21,91\n"
"AR 91,22\nMU 91,91,23\nAW 22,91\nAR 91,23\nMU 91,91,23\nAW 23,91\nAR 91,24\nMU 91,91,22\nAW 24,91\nAR 91,25\nMU 91,91,22\nAW 25,91\nAR 91,26\nMU 91,91,22\nAW 26,91\nAR 91,27\n"
"MU 91,91,22\nAW 27,91\nAR 91,28\nMU 91,91,23\nAW 28,91\nAR 91,29\nMU 91,91,23\nAW 29,91\nAR 91,30\nMU 91,91,23\nAW 30,91\nAR 91,31\nMU 91,91,23\nAW 31,91\nAR 91,32\nMU 91,91,22\n"
"AW 32,91\nAR 91,33\nMU 91,91,22\nAW 33,91\nAR 91,34\nMU 91,91,22\nAW 34,91\nAR 91,35\nMU 91,91,22\nAW 35,91\nAR 91,36\nMU 91,91,23\nAW 36,91\nAR 91,37\nMU 91,91,23\nAW 37,91\n"
"AR 91,38\nMU 91,91,23\nAW 38,91\nAR 91,39\nMU 91,91,23\nAW 39,91\nAR 91,40\nMU 91,91,22\nAW 40,91\nAR 91,41\nMU 91,91,22\nAW 41,91\nAR 91,42\nMU 91,91,22\nAW 42,91\nAR 91,43\n"
"MU 91,91,22\nAW 43,91\nAR 91,44\nMU 91,91,23\nAW 44,91\nAR 91,45\nMU 91,91,23\nAW 45,91\nAR 91,46\nMU 91,91,23\nAW 46,91\nAR 91,47\nMU 91,91,23\nAW 47,91\nAR 91,48\nMU 91,91,22\n"
"AW 48,91\nAR 91,49\nMU 91,91,22\nAW 49,91\nAR 91,50\nMU 91,91,22\nAW 50,91\nAR 91,51\nMU 91,91,22\nAW 51,91\nAR 91,52\nMU 91,91,23\nAW 52,91\nAR 91,53\nMU 91,91,23\nAW 53,91\n"
"AR 91,54\nMU 91,91,23\nAW 54,91\nAR 91,55\nMU 91,91,23\nAW 55,91\nAR 91,56\nMU 91,91,22\nAW 56,91\nAR 91,57\nMU 91,91,22\nAW 57,91\nAR 91,58\nMU 91,91,22\nAW 58,91\nAR 91,59\n"
"MU 91,91,22\nAW 59,91\nAR 91,60\nMU 91,91,23\nAW 60,91\nAR 91,61\nMU 91,91,23\nAW 61,91\nAR 91,62\nMU 91,91,23\nAW 62,91\nAR 91,63\nMU 91,91,23\nAW 63,91\nAR 91,64\nMU 91,91,22\n"
"AW 64,91\nAR 91,65\nMU 91,91,22\nAW 65,91\nAR 91,66\nMU 91,91,22\nAW 66,91\nAR 91,67\nMU 91,91,22\nAW 67,91\nAR 91,68\nMU 91,91,23\nAW 68,91\nAR 91,69\nMU 91,91,23\nAW 69,91\n"
"AR 91,70\nMU 91,91,23\nAW 70,91\nAR 91,71\nMU 91,91,23\nAW 71,91\nAR 91,72\nMU 91,91,22\nAW 72,91\nAR 91,73\nMU 91,91,22\nAW 73,91\nAR 91,74\nMU 91,91,22\nAW 74,91\nAR 91,75\n"
"MU 91,91,22\nAW 75,91\nAR 91,76\nMU 91,91,23\nAW 76,91\nAR 91,77\nMU 91,91,23\nAW 77,91\nAR 91,78\nMU 91,91,23\nAW 78,91\nAR 91,79\nMU 91,91,23\nAW 79,91\nAR 91,80\nMU 91,91,22\n"
"AW 80,91\nAR 91,81\nMU 91,91,22\nAW 81,91\nAR 91,82\nMU 91,91,22\nAW 82,91\nAR 91,83\nMU 91,91,22\nAW 83,91\nAR 91,84\nMU 91,91,23\nAW 84,91\nAR 91,85\nMU 91,91,23\nAW 85,91\n"
"AR 91,86\nMU 91,91,23\nAW 86,91\nAR 91,87\nMU 91,91,23\nAW 87,91\nAR 91,88\nMU 91,91,22\nAW 88,91\nAR 91,89\nMU 91,91,22\nAW 89,91\nAR 91,90\nMU 91,91,22\nAW 90,91\nAR 91,91\n"
"MU 91,91,22\nAW 91,91\nAR 91,92\nMU 91,91,23\nAW 92,91\nAR 91,93\nMU 91,91,23\nAW 93,91\nAR 91,94\nMU 91,91,23\nAW 94,91\nAR 91,95\nMU 91,91,23\nAW 95,91\nAR 91,96\nMU 91,91,22\n"
"AW 96,91\nAR 91,97\nMU 91,91,22\nAW 97,91\nAR 91,98\nMU 91,91,22\nAW 98,91\nAR 91,99\nMU 91,91,22\nAW 99,91\nAR 91,100\nMU 91,91,23\nAW 100,91\nAR 91,101\nMU 91,91,23\nAW 101,91\n"
"AR 91,102\nMU 91,91,23\nAW 102,91\nAR 91,103\nMU 91,91,23\nAW 103,91\nAR 91,104\nMU 91,91,22\nAW 104,91\nAR 91,105\nMU 91,91,22\nAW 105,91\nAR 91,106\nMU 91,91,22\nAW 106,91\n"
"AR 91,107\nMU 91,91,22\nAW 107,91\nAR 91,108\nMU 91,91,23\nAW 108,91\nAR 91,109\nMU 91,91,23\nAW 109,91\nAR 91,110\nMU 91,91,23\nAW 110,91\nAR 91,111\nMU 91,91,23\nAW 111,91\n"
"AR 91,112\nMU 91,91,22\nAW 112,91\nAR 91,113\nMU 91,91,22\nAW 113,91\nAR 91,114\nMU 91,91,22\nAW 114,91\nAR 91,115\nMU 91,91,22\nAW 115,91\nAR 91,116\nMU 91,91,23\nAW 116,91\n"
"AR 91,117\nMU 91,91,23\nAW 117,91\nAR 91,118\nMU 91,91,23\nAW 118,91\nAR 91,119\nMU 91,91,23\nAW 119,91\nAR 91,120\nMU 91,91,22\nAW 120,91\nAR 91,121\nMU 91,91,22\nAW 121,91\n"
"AR 91,122\nMU 91,91,22\nAW 122,91\nAR 91,123\nMU 91,91,22\nAW 123,91\nAR 91,124\nMU 91,91,23\nAW 124,91\nAR 91,125\nMU 91,91,23\nAW 125,91\nAR 91,126\nMU 91,91,23\nAW 126,91\n"
"AR 91,127\nMU 91,91,23\nAW 127,91\ns_nop 1\ns_setpc_b64 s[80:81]\n.p2align 12\n.PA3:\ns_mov_b64 exec, 0\ns_cmp_eq_u32 s11, 1\ns_cbranch_scc1 .W556\ns_cmp_eq_u32 s11, 2\n"
"s_cbranch_scc1 .W557\ns_cmp_eq_u32 s11, 3\ns_cbranch_scc1 .W558\ns_branch .W555\n.WB152:\ns_mov_b64 exec, -1\nv_add_u32 v128, 0x10000, v12\nv_add_u32 v129, 0x10000, v13\n"
"v_add_u32 v130, 0x10000, v14\nv_add_u32 v131, 0x10000, v15\nv_add_u32 v132, 0x10000, v16\nv_add_u32 v133, 0x10000, v17\nv_add_u32 v134, 0x10000, v18\nv_add_u32 v135, 0x10000, v19\n"
"s_add_u32 s2, s11, 1\ns_lshr_b32 s2, s2, 1\ns_lshl_b32 s2, s2, 12\nv_add_u32 v140, s2, v10\nv_add_u32 v141, s2, v11\nv_xor_b32 v142, 0x1000, v140\nv_xor_b32 v143, 0x1000, v141\n"
"s_add_u32 m0, s47, 0\ns_add_u32 s46, s51, 524288\nD0 6,32,46,0\nD0 7,32,46,1024\nD0 8,32,46,2048\nD0 9,32,46,3072\ns_add_u32 m0, s47, -49152\ns_add_u32 s46, s52, 128\n"
"D0 136,28,46,0\nD0 137,28,46,1024\nD0 138,28,46,2048\nD0 139,28,46,3072\ns_add_u32 m0, s47, 16384\ns_add_u32 s46, s51, 786432\nD0 6,32,46,0\nD0 7,32,46,1024\nD0 8,32,46,2048\n"
"D0 9,32,46,3072\nv_mov_b32 v116, 0x40000000\nv_cmp_gt_u32 vcc, 56, v0\ns_lshl2_add_u32 s2, s10, s11\ns_mul_i32 s2, s2, 1792\nv_cndmask_b32 v117, v116, v1, vcc\ns_mov_b32 s36, s56\n"
"s_and_b32 s37, s57, 0xffff\ns_mov_b32 s38, 0x380\ns_mov_b32 s39, 0x20000\ns_mov_b32 s40, s58\ns_and_b32 s41, s59, 0xffff\ns_mov_b32 s42, 0x380\ns_mov_b32 s43, 0x20000\n"
"buffer_load_dwordx4 v[96:99], v117, s[28:31], s2 offen offset:0\nbuffer_load_dwordx4 v[100:103], v117, s[28:31], s2 offen offset:896\n"
"buffer_load_dwordx4 v[104:107], v117, s[36:39], 0 offen offset:0\nbuffer_load_dwordx4 v[108:111], v117, s[40:43], 0 offen offset:0\ns_add_u32 m0, s47, 32768\n"
"s_add_u32 s46, s51, 1048576\nD0 6,32,46,0\nD0 7,32,46,1024\nD0 8,32,46,2048\nD0 9,32,46,3072\ns_add_u32 m0, s47, -32768\ns_add_u32 s46, s52, 256\nD0 136,28,46,0\n"
"D0 137,28,46,1024\nD0 138,28,46,2048\nD0 139,28,46,3072\nWL 0\ns_mul_i32 s15, s15, 1664525\ns_add_u32 s15, s15, 1013904223\nv_mov_b32 v25, s15\ns_mov_b32 s57, 0x3db8aa3b\n"
"s_and_b32 s59, s11, 1\ns_lshl_b32 s59, s59, 13\ns_mul_i32 s2, s73, 25165824\ns_and_b32 s3, s12, 1\ns_lshl_b32 s3, s3, 21\ns_add_u32 s2, s2, s3\ns_lshl2_add_u32 s3, s13, s11\n"
"s_lshl_b32 s3, s3, 15\ns_add_u32 s2, s2, s3\ns_add_u32 s56, s2, 0x800000\ns_xor_b32 s53, s56, 0xc000\nWV 24\ns_barrier\nRa 192,140,0\nRa 196,141,0\nRa 200,140,2048\n"
"Ra 204,141,2048\nRa 208,142,0\nRa 212,143,0\nRa 216,142,2048\nRa 220,143,2048\nTa 160,128,0\nTa 162,128,2048\nTa 164,128,16384\nTa 166,128,18432\nTa 168,129,0\nTa 170,129,2048\n"
"Ta 172,129,16384\nTa 174,129,18432\nTa 176,130,0\nTa 178,130,2048\nTa 180,130,16384\nTa 182,130,18432\nTa 184,131,0\nTa 186,131,2048\nTa 188,131,16384\nTa 190,131,18432\nWL 0\n"
"Zvaa 32,160,192\nTa 224,132,0\ns_add_u32 m0, s47, 49152\ns_add_u32 s46, s51, 1310720\nMvaa 32,164,196\nTa 226,132,2048\nZaaa 96,164,204\nD0 6,32,46,0\nMaaa 96,160,200\n"
"Ta 228,132,16384\nZvaa 192,160,208\nTa 230,132,18432\nMvaa 192,164,212\nZvaa 224,164,220\nTa 232,133,0\nMvaa 224,160,216\nTa 234,133,2048\nD0 7,32,46,1024\nZvaa 228,168,216\n"
"Mvaa 228,172,220\nTa 236,133,16384\nZvaa 196,172,212\nTa 238,133,18432\nMvaa 196,168,208\nZaaa 100,168,200\nTa 240,134,0\nD0 8,32,46,2048\nMaaa 100,172,204\nTa 242,134,2048\n"
"Zvaa 36,172,196\nMvaa 36,168,192\nTa 244,134,16384\nZvaa 40,176,192\nTa 246,134,18432\nMvaa 40,180,196\nD0 9,32,46,3072\nZaaa 104,180,204\nTa 248,135,0\nMaaa 104,176,200\n"
"Ta 250,135,2048\nZvaa 200,176,208\ns_add_u32 m0, s47, -16384\ns_add_u32 s46, s52, 384\nMvaa 200,180,212\nTa 252,135,16384\nZvaa 232,180,220\nTa 254,135,18432\nD0 136,28,46,0\n"
"Mvaa 232,176,216\nZvaa 236,184,216\nMvaa 236,188,220\nZvaa 204,188,212\nMvaa 204,184,208\nD0 137,28,46,1024\nZaaa 108,184,200\nMaaa 108,188,204\nZvaa 44,188,196\nMvaa 44,184,192\n"
"s_waitcnt vmcnt(18) lgkmcnt(0)\ns_barrier\nZvaa 48,224,192\nRa 128,140,16384\nD0 138,28,46,2048\nMvaa 48,228,196\nRa 132,141,16384\nZaaa 112,228,204\nRa 136,140,18432\n"
"Maaa 112,224,200\nRa 140,141,18432\nZvaa 208,224,208\nRa 144,142,16384\nWV 15\nMvaa 208,228,212\nRa 148,143,16384\nD0 139,28,46,3072\nZvaa 240,228,220\nRa 152,142,18432\n"
"Mvaa 240,224,216\nRa 156,143,18432\nZvaa 244,232,216\nTa 160,12,0\ns_add_u32 m0, s47, 65536\ns_add_u32 s46, s51, 1572864\nMvaa 244,236,220\nTa 162,12,2048\nZvaa 212,236,212\n"
"Ta 164,12,16384\nD0 6,32,46,0\nMvaa 212,232,208\nTa 166,12,18432\nZaaa 116,232,200\nTa 168,13,0\nv_lshlrev_b32 v112, 16, v96\nMaaa 116,236,204\nTa 170,13,2048\nZvaa 52,236,196\n"
"Ta 172,13,16384\nMvaa 52,232,192\nTa 174,13,18432\nD0 7,32,46,1024\nZvaa 56,240,192\nTa 176,14,0\nv_lshlrev_b32 v113, 16, v100\nMvaa 56,244,196\nTa 178,14,2048\nZaaa 120,244,204\n"
"Ta 180,14,16384\nMaaa 120,240,200\nTa 182,14,18432\nZvaa 216,240,208\nTa 184,15,0\nD0 8,32,46,2048\nMvaa 216,244,212\nTa 186,15,2048\nZvaa 248,244,220\nTa 188,15,16384\n"
"Mvaa 248,240,216\nTa 190,15,18432\nZvaa 252,248,216\nv_lshlrev_b32 v114, 16, v104\nv_lshlrev_b32 v115, 16, v108\nMvaa 252,252,220\nD0 9,32,46,3072\nZvaa 220,252,212\n"
"MU 116,112,114\nMvaa 220,248,208\nMU 117,113,114\nZaaa 124,248,200\ns_add_u32 m0, s47, 81920\ns_add_u32 s46, s51, 1835008\nMaaa 124,252,204\nCV 116,116,117\nZvaa 60,252,196\n"
"D0 6,32,46,0\nMvaa 60,248,192\nWL 0\nMvaa 32,160,128\nTa 224,16,0\nv_lshlrev_b32 v118, 16, v116\nMvaa 32,164,132\nTa 226,16,2048\nMaaa 96,164,140\n"
"v_and_b32 v119, 0xffff0000, v116\nMaaa 96,160,136\nTa 228,16,16384\nD0 7,32,46,1024\nMvaa 192,160,144\nTa 230,16,18432\nMU 120,112,115\nMvaa 192,164,148\nMU 121,113,115\n"
"Mvaa 224,164,156\nTa 232,17,0\nMvaa 224,160,152\nTa 234,17,2048\nMvaa 228,168,152\nD0 8,32,46,2048\nv_and_b32 v112, 0xffff0000, v96\nMvaa 228,172,156\nTa 236,17,16384\n"
"Mvaa 196,172,148\nTa 238,17,18432\nMvaa 196,168,144\nv_and_b32 v113, 0xffff0000, v100\nMaaa 100,168,136\nTa 240,18,0\nv_and_b32 v114, 0xffff0000, v104\nMaaa 100,172,140\n"
"Ta 242,18,2048\nD0 9,32,46,3072\nMvaa 36,172,132\nv_and_b32 v115, 0xffff0000, v108\nMvaa 36,168,128\nTa 244,18,16384\nMvaa 40,176,128\nTa 246,18,18432\nMU 116,112,114\n"
"Mvaa 40,180,132\nMU 117,113,114\nMaaa 104,180,140\nTa 248,19,0\nMaaa 104,176,136\nTa 250,19,2048\nMvaa 200,176,144\nCV 116,116,117\nv_and_b32 v117, 0xffff0000, v116\n"
"Mvaa 200,180,148\nTa 252,19,16384\nMvaa 232,180,156\nTa 254,19,18432\nMvaa 232,176,152\nv_lshlrev_b32 v116, 16, v116\nMvaa 236,184,152\nFM 120,112,115\nFM 121,113,115\n"
"Mvaa 236,188,156\nAD 118,118,116\nMvaa 204,188,148\nAD 119,119,117\nMvaa 204,184,144\nv_lshlrev_b32 v112, 16, v97\nMaaa 108,184,136\nv_lshlrev_b32 v113, 16, v101\n"
"v_lshlrev_b32 v114, 16, v105\nMaaa 108,188,140\nv_lshlrev_b32 v115, 16, v109\nMvaa 44,188,132\nMU 116,112,114\nMvaa 44,184,128\ns_waitcnt vmcnt(12) lgkmcnt(0)\ns_barrier\n"
"Mvaa 48,224,128\nRa 192,140,32768\ns_add_u32 m0, s47, -49152\ns_add_u32 s46, s52, 512\nMvaa 48,228,132\nRa 196,141,32768\nMaaa 112,228,140\nRa 200,140,34816\nD0 136,28,46,0\n"
"Maaa 112,224,136\nRa 204,141,34816\nMvaa 208,224,144\nRa 208,142,32768\nMU 117,113,114\nMvaa 208,228,148\nRa 212,143,32768\nMvaa 240,228,156\nRa 216,142,34816\nMvaa 240,224,152\n"
"Ra 220,143,34816\nD0 137,28,46,1024\nMvaa 244,232,152\nTa 160,12,32768\nCV 116,116,117\nMvaa 244,236,156\nTa 162,12,34816\nMvaa 212,236,148\nTa 164,12,49152\nMvaa 212,232,144\n"
"Ta 166,12,51200\nMaaa 116,232,136\nTa 168,13,32768\nD0 138,28,46,2048\nMaaa 116,236,140\nTa 170,13,34816\nMvaa 52,236,132\nTa 172,13,49152\nMvaa 52,232,128\nTa 174,13,51200\n"
"Mvaa 56,240,128\nTa 176,14,32768\nv_and_b32 v117, 0xffff0000, v116\nMvaa 56,244,132\nTa 178,14,34816\nD0 139,28,46,3072\nMaaa 120,244,140\nTa 180,14,49152\nMaaa 120,240,136\n"
"Ta 182,14,51200\nMvaa 216,240,144\nTa 184,15,32768\ns_add_u32 m0, s47, 0\ns_add_u32 s46, s51, 2097152\nMvaa 216,244,148\nTa 186,15,34816\nMvaa 248,244,156\nTa 188,15,49152\n"
"D0 6,32,46,0\nMvaa 248,240,152\nTa 190,15,51200\nMvaa 252,248,152\nv_lshlrev_b32 v116, 16, v116\nFM 120,112,115\nMvaa 252,252,156\nFM 121,113,115\nMvaa 220,252,148\n"
"AD 118,118,116\nMvaa 220,248,144\nD0 7,32,46,1024\nMaaa 124,248,136\nAD 119,119,117\nv_and_b32 v112, 0xffff0000, v97\nMaaa 124,252,140\nv_and_b32 v113, 0xffff0000, v101\n"
"Mvaa 60,252,132\nv_and_b32 v114, 0xffff0000, v105\nMvaa 60,248,128\nWL 0\nMvaa 32,160,192\nTa 224,16,32768\nD0 8,32,46,2048\nMvaa 32,164,196\nTa 226,16,34816\nMaaa 96,164,204\n"
"v_and_b32 v115, 0xffff0000, v109\nMaaa 96,160,200\nTa 228,16,49152\nMvaa 192,160,208\nTa 230,16,51200\nMU 116,112,114\nMvaa 192,164,212\nD0 9,32,46,3072\nMvaa 224,164,220\n"
"Ta 232,17,32768\nMvaa 224,160,216\nTa 234,17,34816\nMvaa 228,168,216\ns_add_u32 m0, s47, 16384\ns_add_u32 s46, s51, 2359296\nMvaa 228,172,220\nTa 236,17,49152\nMvaa 196,172,212\n"
"Ta 238,17,51200\nD0 6,32,46,0\nMvaa 196,168,208\nMU 117,113,114\nMaaa 100,168,200\nTa 240,18,32768\nCV 116,116,117\nMaaa 100,172,204\nTa 242,18,34816\nMvaa 36,172,196\n"
"v_and_b32 v117, 0xffff0000, v116\nMvaa 36,168,192\nTa 244,18,49152\nD0 7,32,46,1024\nMvaa 40,176,192\nTa 246,18,51200\nv_lshlrev_b32 v116, 16, v116\nMvaa 40,180,196\n"
"FM 120,112,115\nMaaa 104,180,204\nTa 248,19,32768\nMaaa 104,176,200\nTa 250,19,34816\nMvaa 200,176,208\nD0 8,32,46,2048\nFM 121,113,115\nMvaa 200,180,212\nTa 252,19,49152\n"
"Mvaa 232,180,220\nTa 254,19,51200\nMvaa 232,176,216\nAD 118,118,116\nMvaa 236,184,216\nAD 119,119,117\nv_lshlrev_b32 v112, 16, v98\nMvaa 236,188,220\nD0 9,32,46,3072\n"
"Mvaa 204,188,212\nv_lshlrev_b32 v113, 16, v102\nMvaa 204,184,208\nv_lshlrev_b32 v114, 16, v106\nMaaa 108,184,200\nv_lshlrev_b32 v115, 16, v110\nMU 116,112,114\nMaaa 108,188,204\n"
"MU 117,113,114\nMvaa 44,188,196\nCV 116,116,117\nMvaa 44,184,192\ns_waitcnt vmcnt(12) lgkmcnt(0)\ns_barrier\nMvaa 48,224,192\nRa 128,140,49152\ns_add_u32 m0, s47, -32768\n"
"s_add_u32 s46, s52, 640\nMvaa 48,228,196\nRa 132,141,49152\nMaaa 112,228,204\nRa 136,140,51200\nD0 136,28,46,0\nMaaa 112,224,200\nRa 140,141,51200\nMvaa 208,224,208\n"
"Ra 144,142,49152\nv_and_b32 v117, 0xffff0000, v116\nMvaa 208,228,212\nRa 148,143,49152\nMvaa 240,228,220\nRa 152,142,51200\nMvaa 240,224,216\nRa 156,143,51200\nD0 137,28,46,1024\n"
"Mvaa 244,232,216\nTa 160,128,0\nv_lshlrev_b32 v116, 16, v116\nMvaa 244,236,220\nTa 162,128,2048\nMvaa 212,236,212\nTa 164,128,16384\nMvaa 212,232,208\nTa 166,128,18432\n"
"Maaa 116,232,200\nTa 168,129,0\nD0 138,28,46,2048\nMaaa 116,236,204\nTa 170,129,2048\nMvaa 52,236,196\nTa 172,129,16384\nMvaa 52,232,192\nTa 174,129,18432\nMvaa 56,240,192\n"
"Ta 176,130,0\nFM 120,112,115\nMvaa 56,244,196\nTa 178,130,2048\nD0 139,28,46,3072\nMaaa 120,244,204\nTa 180,130,16384\nMaaa 120,240,200\nTa 182,130,18432\nMvaa 216,240,208\n"
"Ta 184,131,0\ns_add_u32 m0, s47, 32768\ns_add_u32 s46, s51, 2621440\nMvaa 216,244,212\nTa 186,131,2048\nMvaa 248,244,220\nTa 188,131,16384\nD0 6,32,46,0\nMvaa 248,240,216\n"
"Ta 190,131,18432\nMvaa 252,248,216\nFM 121,113,115\nAD 118,118,116\nMvaa 252,252,220\nAD 119,119,117\nMvaa 220,252,212\nv_and_b32 v112, 0xffff0000, v98\nMvaa 220,248,208\n"
"D0 7,32,46,1024\nMaaa 124,248,200\nv_and_b32 v113, 0xffff0000, v102\nv_and_b32 v114, 0xffff0000, v106\nMaaa 124,252,204\nv_and_b32 v115, 0xffff0000, v110\nMvaa 60,252,196\n"
"MU 116,112,114\nMvaa 60,248,192\nWL 0\nMvaa 32,160,128\nTa 224,132,0\n.W555:\ns_cbranch_execz .W559\nD0 8,32,46,2048\nMvaa 32,164,132\nTa 226,132,2048\nMaaa 96,164,140\n"
"MU 117,113,114\nMaaa 96,160,136\nTa 228,132,16384\nMvaa 192,160,144\nTa 230,132,18432\nCV 116,116,117\nMvaa 192,164,148\nD0 9,32,46,3072\nMvaa 224,164,156\nTa 232,133,0\n"
"Mvaa 224,160,152\nTa 234,133,2048\nMvaa 228,168,152\ns_add_u32 m0, s47, 49152\ns_add_u32 s46, s51, 2883584\nMvaa 228,172,156\nTa 236,133,16384\nMvaa 196,172,148\nTa 238,133,18432\n"
"D0 6,32,46,0\nMvaa 196,168,144\nv_and_b32 v117, 0xffff0000, v116\nMaaa 100,168,136\nTa 240,134,0\nv_lshlrev_b32 v116, 16, v116\nMaaa 100,172,140\nTa 242,134,2048\nMvaa 36,172,132\n"
"FM 120,112,115\nMvaa 36,168,128\nTa 244,134,16384\nD0 7,32,46,1024\nMvaa 40,176,128\nTa 246,134,18432\nFM 121,113,115\nMvaa 40,180,132\nAD 118,118,116\nMaaa 104,180,140\n"
"Ta 248,135,0\nMaaa 104,176,136\nTa 250,135,2048\nMvaa 200,176,144\nD0 8,32,46,2048\nAD 119,119,117\nMvaa 200,180,148\nTa 252,135,16384\nMvaa 232,180,156\nTa 254,135,18432\n"
"Mvaa 232,176,152\nv_lshlrev_b32 v112, 16, v99\nMvaa 236,184,152\nv_lshlrev_b32 v113, 16, v103\nv_lshlrev_b32 v114, 16, v107\nMvaa 236,188,156\nD0 9,32,46,3072\nMvaa 204,188,148\n"
"v_lshlrev_b32 v115, 16, v111\nMvaa 204,184,144\nMU 116,112,114\nMaaa 108,184,136\nMU 117,113,114\nCV 116,116,117\ns_and_b32 s78, s74, 3\ns_lshl2_add_u32 s78, s73, s78\n"
"s_lshl_b32 s78, s78, 12\ns_bitcmp1_b32 s11, 0\ns_cselect_b32 s79, s48, s45\ns_add_u32 s79, s79, 512\ns_and_b32 s79, s79, 0xfffe0000\ns_add_u32 s78, s78, s79\n"
"buffer_load_dword v31, v26, s[20:23], s78 offen\nMaaa 108,188,140\nv_and_b32 v117, 0xffff0000, v116\nMvaa 44,188,132\nv_lshlrev_b32 v116, 16, v116\nMvaa 44,184,128\n"
"s_waitcnt vmcnt(13) lgkmcnt(0)\ns_barrier\nMvaa 48,224,128\nRa 192,140,16384\ns_add_u32 m0, s47, -16384\ns_add_u32 s46, s52, 768\nMvaa 48,228,132\nRa 196,141,16384\n"
"Maaa 112,228,140\nRa 200,140,18432\nD0 136,28,46,0\nMaaa 112,224,136\nRa 204,141,18432\nMvaa 208,224,144\nRa 208,142,16384\nFM 120,112,115\nMvaa 208,228,148\nRa 212,143,16384\n"
"Mvaa 240,228,156\nRa 216,142,18432\nMvaa 240,224,152\nRa 220,143,18432\nD0 137,28,46,1024\nMvaa 244,232,152\nTa 160,12,0\nFM 121,113,115\nMvaa 244,236,156\nTa 162,12,2048\n"
"Mvaa 212,236,148\nTa 164,12,16384\nMvaa 212,232,144\nTa 166,12,18432\nMaaa 116,232,136\nTa 168,13,0\nD0 138,28,46,2048\nMaaa 116,236,140\nTa 170,13,2048\nMvaa 52,236,132\n"
"Ta 172,13,16384\nMvaa 52,232,128\nTa 174,13,18432\nMvaa 56,240,128\nTa 176,14,0\nAD 118,118,116\nMvaa 56,244,132\nTa 178,14,2048\nD0 139,28,46,3072\nMaaa 120,244,140\n"
"Ta 180,14,16384\nMaaa 120,240,136\nTa 182,14,18432\nMvaa 216,240,144\nTa 184,15,0\ns_add_u32 m0, s47, 65536\ns_add_u32 s46, s51, 3145728\nMvaa 216,244,148\nTa 186,15,2048\n"
"Mvaa 248,244,156\nTa 188,15,16384\nD0 6,32,46,0\nMvaa 248,240,152\nTa 190,15,18432\nMvaa 252,248,152\nAD 119,119,117\nv_and_b32 v112, 0xffff0000, v99\nMvaa 252,252,156\n"
"v_and_b32 v113, 0xffff0000, v103\nMvaa 220,252,148\nv_and_b32 v114, 0xffff0000, v107\nMvaa 220,248,144\nD0 7,32,46,1024\nMaaa 124,248,136\nv_and_b32 v115, 0xffff0000, v111\n"
"MU 116,112,114\nMaaa 124,252,140\nMU 117,113,114\nMvaa 60,252,132\nCV 116,116,117\nMvaa 60,248,128\nWL 0\nMvaa 32,160,192\nTa 224,16,0\nD0 8,32,46,2048\nMvaa 32,164,196\n"
"Ta 226,16,2048\nMaaa 96,164,204\nv_and_b32 v117, 0xffff0000, v116\nMaaa 96,160,200\nTa 228,16,16384\nMvaa 192,160,208\nTa 230,16,18432\nv_lshlrev_b32 v116, 16, v116\n"
"Mvaa 192,164,212\nD0 9,32,46,3072\nMvaa 224,164,220\nTa 232,17,0\nMvaa 224,160,216\nTa 234,17,2048\nMvaa 228,168,216\ns_add_u32 m0, s47, 81920\ns_add_u32 s46, s51, 3407872\n"
"Mvaa 228,172,220\nTa 236,17,16384\nMvaa 196,172,212\nTa 238,17,18432\nD0 6,32,46,0\nMvaa 196,168,208\nFM 120,112,115\nMaaa 100,168,200\nTa 240,18,0\nFM 121,113,115\n"
"Maaa 100,172,204\nTa 242,18,2048\nMvaa 36,172,196\nAD 118,118,116\nMvaa 36,168,192\nTa 244,18,16384\nD0 7,32,46,1024\nMvaa 40,176,192\nTa 246,18,18432\nAD 119,119,117\n"
"Mvaa 40,180,196\ns_nop 0\nv_permlane32_swap_b32 v118, v119\nMaaa 104,180,204\nTa 248,19,0\nMaaa 104,176,200\nTa 250,19,2048\nMvaa 200,176,208\nD0 8,32,46,2048\n"
"v_permlane32_swap_b32 v120, v121\nMvaa 200,180,212\nTa 252,19,16384\nMvaa 232,180,220\nTa 254,19,18432\nMvaa 232,176,216\nAD 118,118,119\nMvaa 236,184,216\nAD 120,120,121\n"
"Mvaa 236,188,220\nD0 9,32,46,3072\nMvaa 204,188,212\nv_permlane16_swap_b32 v118, v120\nMvaa 204,184,208\nAD 118,118,120\nMaaa 108,184,200\nMaaa 108,188,204\n"
"v_add_f32_dpp v118, v118, v118 row_ror:8 row_mask:0xf bank_mask:0xf\nMvaa 44,188,196\nMvaa 44,184,192\ns_waitcnt vmcnt(13) lgkmcnt(0)\ns_barrier\nMvaa 48,224,192\n"
"Ra 128,140,32768\ns_add_u32 m0, s47, -65536\ns_add_u32 s45, s45, 128\nMvaa 48,228,196\nRa 132,141,32768\nMaaa 112,228,204\nRa 136,140,34816\nD0 2,20,45,0\nMaaa 112,224,200\n"
"Ra 140,141,34816\nMvaa 208,224,208\nRa 144,142,32768\nv_add_f32_dpp v118, v118, v118 row_ror:4 row_mask:0xf bank_mask:0xf\nMvaa 208,228,212\nRa 148,143,32768\nMvaa 240,228,220\n"
"Ra 152,142,34816\nMvaa 240,224,216\nRa 156,143,34816\nD0 3,20,45,1024\nMvaa 244,232,216\nTa 160,12,32768\nv_add_f32_dpp v118, v118, v118 row_ror:2 row_mask:0xf bank_mask:0xf\n"
"Mvaa 244,236,220\nTa 162,12,34816\nMvaa 212,236,212\nTa 164,12,49152\nMvaa 212,232,208\nTa 166,12,51200\nMaaa 116,232,200\nTa 168,13,32768\nD0 4,20,45,2048\nMaaa 116,236,204\n"
"Ta 170,13,34816\nMvaa 52,236,196\nTa 172,13,49152\nMvaa 52,232,192\nTa 174,13,51200\nMvaa 56,240,192\nTa 176,14,32768\n"
"v_add_f32_dpp v118, v118, v118 row_ror:1 row_mask:0xf bank_mask:0xf\nMvaa 56,244,196\nTa 178,14,34816\nD0 5,20,45,3072\nMaaa 120,244,204\nTa 180,14,49152\nMaaa 120,240,200\n"
"Ta 182,14,51200\nMvaa 216,240,208\nTa 184,15,32768\ns_add_u32 m0, s47, 0\ns_add_u32 s44, s44, 0x40000\nMvaa 216,244,212\nTa 186,15,34816\nMvaa 248,244,220\nTa 188,15,49152\n"
"D0 6,24,44,0\nMvaa 248,240,216\nTa 190,15,51200\nMvaa 252,248,216\nCV 118,118,118\nv_lshlrev_b32 v118, 16, v118\nMvaa 252,252,220\nv_mul_f32 v119, 0xbfb8aa3b, v118\nEX 119,119\n"
"Mvaa 220,252,212\nv_add_f32 v120, 0, v118\nMvaa 220,248,208\nD0 7,24,44,1024\nMaaa 124,248,200\nv_add_f32 v119, 1.0, v119\nMaaa 124,252,204\nv_rcp_f32 v119, v119\nMvaa 60,252,196\n"
"v_ashrrev_i32 v121, 31, v120\nMvaa 60,248,192\nWL 0\nMvaa 32,160,128\nTa 224,16,32768\nD0 8,24,44,2048\nMvaa 32,164,132\nTa 226,16,34816\nMaaa 96,164,140\nCV 119,119,119\n"
"Maaa 96,160,136\nTa 228,16,49152\nMvaa 192,160,144\nTa 230,16,51200\nv_lshlrev_b32 v119, 16, v119\nMvaa 192,164,148\nD0 9,24,44,3072\nMvaa 224,164,156\nTa 232,17,32768\n"
"Mvaa 224,160,152\nTa 234,17,34816\nMvaa 228,168,152\ns_add_u32 m0, s47, 16384\ns_add_u32 s44, s44, 0x40000\nMvaa 228,172,156\nTa 236,17,49152\nMvaa 196,172,148\nTa 238,17,51200\n"
"D0 6,24,44,0\nMvaa 196,168,144\nv_or_b32 v121, 0x80000000, v121\nMaaa 100,168,136\nTa 240,18,32768\nv_xor_b32 v120, v120, v121\nMaaa 100,172,140\nTa 242,18,34816\nMvaa 36,172,132\n"
"v_and_b32 v120, 0xffff0000, v120\nMvaa 36,168,128\nTa 244,18,49152\nD0 7,24,44,1024\nMvaa 40,176,128\nTa 246,18,51200\ns_lshl2_add_u32 s2, s10, s11\nMvaa 40,180,132\n"
"v_lshrrev_b32 v112, 5, v0\nMaaa 104,180,140\nTa 248,19,32768\nMaaa 104,176,136\nTa 250,19,34816\nMvaa 200,176,144\nD0 8,24,44,2048\nv_lshl_add_u32 v112, s2, 1, v112\n"
"Mvaa 200,180,148\nTa 252,19,49152\nMvaa 232,180,156\nTa 254,19,51200\nMvaa 232,176,152\nv_sub_u32 v113, 0xffff, v112\nMvaa 236,184,152\nv_or_b32 v120, v120, v113\n"
"v_mov_b32 v121, s15\nMvaa 236,188,156\nD0 9,24,44,3072\nMvaa 204,188,148\nv_and_b32 v113, 31, v0\nMvaa 204,184,144\nv_mov_b32 v116, 0x40000000\nv_cmp_eq_u32 vcc, 16, v113\n"
"v_lshlrev_b32 v114, 2, v112\nv_lshlrev_b32 v115, 10, v113\nv_cndmask_b32 v114, v116, v114, vcc\nv_cndmask_b32 v120, v119, v120, vcc\nMaaa 108,184,136\n"
"v_lshl_add_u32 v115, v112, 3, v115\nv_and_b32 v113, 15, v0\nMaaa 108,188,140\nv_cmp_eq_u32 vcc, 0, v113\ns_mov_b32 s2, 0xc040000\ns_mov_b32 s3, 0xc03e000\n"
"v_cndmask_b32 v115, v116, v115, vcc\nbuffer_store_dwordx2 v[120:121], v115, s[16:19], s2 offen sc0 sc1\nbuffer_store_dword v118, v114, s[16:19], s3 offen sc0 sc1\nMvaa 44,188,132\n"
"Mvaa 44,184,128\ns_waitcnt vmcnt(14) lgkmcnt(0)\ns_barrier\nMvaa 48,224,128\nRa 192,140,49152\ns_add_u32 m0, s47, -49152\ns_add_u32 s48, s48, 128\nMvaa 48,228,132\n"
"Ra 196,141,49152\nMaaa 112,228,140\nRa 200,140,51200\nD0 2,20,48,0\nMaaa 112,224,136\nRa 204,141,51200\nMvaa 208,224,144\nRa 208,142,49152\nMvaa 208,228,148\nRa 212,143,49152\n"
"Mvaa 240,228,156\nRa 216,142,51200\nMvaa 240,224,152\nRa 220,143,51200\nD0 3,20,48,1024\nMvaa 244,232,152\nTa 160,128,0\nMvaa 244,236,156\nTa 162,128,2048\nMvaa 212,236,148\n"
"Ta 164,128,16384\nMvaa 212,232,144\nTa 166,128,18432\nMaaa 116,232,136\nTa 168,129,0\nD0 4,20,48,2048\nMaaa 116,236,140\nTa 170,129,2048\nMvaa 52,236,132\nTa 172,129,16384\n"
"Mvaa 52,232,128\nTa 174,129,18432\nMvaa 56,240,128\nTa 176,130,0\nMvaa 56,244,132\nTa 178,130,2048\nD0 5,20,48,3072\nMaaa 120,244,140\nTa 180,130,16384\nMaaa 120,240,136\n"
"Ta 182,130,18432\nMvaa 216,240,144\nTa 184,131,0\ns_add_u32 m0, s47, -32768\ns_add_u32 s45, s45, 128\nMvaa 216,244,148\nTa 186,131,2048\nMvaa 248,244,156\nTa 188,131,16384\n"
"D0 2,20,45,0\nMvaa 248,240,152\nTa 190,131,18432\nMvaa 252,248,152\nMvaa 252,252,156\nMvaa 220,252,148\nMvaa 220,248,144\nD0 3,20,45,1024\nMaaa 124,248,136\nMaaa 124,252,140\n"
"Mvaa 60,252,132\nMvaa 60,248,128\nWL 0\nMvaa 32,160,192\nTa 224,132,0\nD0 4,20,45,2048\nMvaa 32,164,196\nTa 226,132,2048\nMvaa 36,172,196\nMvaa 36,168,192\nTa 228,132,16384\n"
"Mvaa 40,176,192\nTa 230,132,18432\nMvaa 40,180,196\nD0 5,20,45,3072\nMvaa 44,188,196\nTa 232,133,0\nMvaa 44,184,192\nTa 234,133,2048\nMaaa 108,184,200\ns_add_u32 m0, s47, 32768\n"
"s_add_u32 s44, s44, 0x40000\nMaaa 108,188,204\nTa 236,133,16384\nMaaa 104,180,204\nTa 238,133,18432\nD0 6,24,44,0\nMaaa 104,176,200\nv_mul_f32 v32, s57, v32\n"
"v_mul_f32 v33, s57, v33\nMaaa 100,168,200\nTa 240,134,0\nCV 32,32,33\nMaaa 100,172,204\nTa 242,134,2048\nv_mul_f32 v34, s57, v34\nMaaa 96,164,204\nv_mul_f32 v35, s57, v35\n"
"CV 33,34,35\nMaaa 96,160,200\nTa 244,134,16384\nD0 7,24,44,1024\nMvaa 192,160,208\nTa 246,134,18432\nv_mul_f32 v36, s57, v36\nMvaa 192,164,212\nv_mul_f32 v37, s57, v37\n"
"Mvaa 196,172,212\nTa 248,135,0\nCV 34,36,37\nMvaa 196,168,208\nTa 250,135,2048\nv_mul_f32 v38, s57, v38\nMvaa 200,176,208\nD0 8,24,44,2048\nv_mul_f32 v39, s57, v39\n"
"Mvaa 200,180,212\nTa 252,135,16384\nCV 35,38,39\nMvaa 204,188,212\nTa 254,135,18432\nv_mul_f32 v40, s57, v40\nMvaa 204,184,208\nv_mul_f32 v41, s57, v41\nCV 40,40,41\n"
"Mvaa 236,184,216\nv_mul_f32 v42, s57, v42\nv_mul_f32 v43, s57, v43\nMvaa 236,188,220\nD0 9,24,44,3072\nMvaa 232,180,220\nCV 41,42,43\nv_mul_f32 v44, s57, v44\nMvaa 232,176,216\n"
"v_mul_f32 v45, s57, v45\nCV 42,44,45\nMvaa 228,168,216\nv_mul_f32 v46, s57, v46\nv_mul_f32 v47, s57, v47\nMvaa 228,172,220\nCV 43,46,47\nAR 20,96\nMvaa 224,164,220\nAR 21,97\n"
"v_mul_f32 v20, s57, v20\nMvaa 224,160,216\ns_waitcnt vmcnt(14) lgkmcnt(0)\ns_barrier\nMvaa 48,224,192\ns_add_u32 m0, s47, 49152\ns_add_u32 s44, s44, 0x40000\nRa 128,10,0\n"
"Mvaa 48,228,196\nRa 132,11,0\nMvaa 52,236,196\nD0 6,24,44,0\nRa 136,10,2048\n.W556:\ns_cbranch_execz .W560\nMvaa 52,232,192\nRa 140,11,2048\nv_mul_f32 v21, s57, v21\n"
"Mvaa 56,240,192\nRa 144,10,8192\nCV 36,20,21\nMvaa 56,244,196\nRa 148,11,8192\nAR 20,98\nMvaa 60,252,196\nD0 7,24,44,1024\nRa 152,10,10240\nMvaa 60,248,192\nRa 156,11,10240\n"
"AR 21,99\nMaaa 124,248,200\nTa 160,12,0\nv_mul_f32 v20, s57, v20\nMaaa 124,252,204\nTa 162,12,2048\nMaaa 120,244,204\nD0 8,24,44,2048\nTa 164,12,16384\nMaaa 120,240,200\n"
"Ta 166,12,18432\nv_mul_f32 v21, s57, v21\nMaaa 116,232,200\nTa 168,13,0\nCV 37,20,21\nMaaa 116,236,204\nTa 170,13,2048\nAR 20,100\nMaaa 112,228,204\nD0 9,24,44,3072\n"
"Ta 172,13,16384\nMaaa 112,224,200\nTa 174,13,18432\nAR 21,101\nMvaa 208,224,208\ns_add_u32 m0, s47, -16384\ns_add_u32 s48, s48, 128\nTa 176,14,0\nMvaa 208,228,212\nTa 178,14,2048\n"
"Mvaa 212,236,212\nD0 2,20,48,0\nTa 180,14,16384\nMvaa 212,232,208\nTa 182,14,18432\nv_mul_f32 v20, s57, v20\nMvaa 216,240,208\nTa 184,15,0\nv_mul_f32 v21, s57, v21\n"
"Mvaa 216,244,212\nTa 186,15,2048\nCV 38,20,21\nMvaa 220,252,212\nD0 3,20,48,1024\nTa 188,15,16384\nMvaa 220,248,208\nTa 190,15,18432\nAR 20,102\nMvaa 252,248,216\nAR 21,103\n"
"v_mul_f32 v20, s57, v20\nMvaa 252,252,220\nv_mul_f32 v21, s57, v21\nMvaa 248,244,220\nD0 4,20,48,2048\nCV 39,20,21\nMvaa 248,240,216\nAR 20,104\nAR 21,105\nMvaa 244,232,216\n"
"v_mul_f32 v20, s57, v20\nv_mul_f32 v21, s57, v21\nMvaa 244,236,220\nCV 44,20,21\nAR 20,106\nMvaa 240,228,220\nD0 5,20,48,3072\nAR 21,107\nMvaa 240,224,216\nWL 0\ns_barrier\n"
"Zvaa 64,128,160\nTa 224,16,0\nv_mul_f32 v20, s57, v20\nMvaa 64,132,164\nTa 226,16,2048\nZvaa 68,140,164\ns_add_u32 m0, s47, -65536\ns_add_u32 s45, s45, 128\nMvaa 68,136,160\n"
"Ta 228,16,16384\nv_mul_f32 v21, s57, v21\nZvaa 128,144,160\nTa 230,16,18432\nCV 45,20,21\nMvaa 128,148,164\nD0 2,20,45,0\nAR 20,108\nZvaa 132,156,164\nTa 232,17,0\nAR 21,109\n"
"Mvaa 132,152,160\nTa 234,17,2048\nv_mul_f32 v20, s57, v20\nZvaa 140,152,168\nv_mul_f32 v21, s57, v21\nCV 46,20,21\nMvaa 140,156,172\nTa 236,17,16384\ns_add_u32 s6, s56, 0\n"
"Sv 32,1,16,6,0\nZvaa 136,148,172\nTa 238,17,18432\nAR 20,110\nMvaa 136,144,168\nAR 21,111\nv_mul_f32 v20, s57, v20\nZvaa 76,136,168\nTa 240,18,0\nv_mul_f32 v21, s57, v21\n"
"Mvaa 76,140,172\nTa 242,18,2048\nCV 47,20,21\nZvaa 72,132,172\nD0 3,20,45,1024\nv_mul_f32 v48, s57, v48\nMvaa 72,128,168\nTa 244,18,16384\nv_mul_f32 v49, s57, v49\n"
"Zvaa 80,128,176\nTa 246,18,18432\nCV 48,48,49\nMvaa 80,132,180\nv_mul_f32 v50, s57, v50\nZvaa 84,140,180\nTa 248,19,0\nv_mul_f32 v51, s57, v51\nMvaa 84,136,176\nTa 250,19,2048\n"
"CV 49,50,51\nZvaa 144,144,176\nv_mul_f32 v52, s57, v52\nv_mul_f32 v53, s57, v53\nMvaa 144,148,180\nTa 252,19,16384\nCV 50,52,53\nZvaa 148,156,180\nTa 254,19,18432\n"
"v_mul_f32 v54, s57, v54\nMvaa 148,152,176\nD0 4,20,45,2048\nv_mul_f32 v55, s57, v55\nZvaa 156,152,184\nCV 51,54,55\nv_mul_f32 v56, s57, v56\nMvaa 156,156,188\n"
"v_mul_f32 v57, s57, v57\ns_add_u32 s6, s56, 0\nSv 40,1,16,6,2048\nZvaa 152,148,188\nCV 56,56,57\nv_mul_f32 v58, s57, v58\nMvaa 152,144,184\nv_mul_f32 v59, s57, v59\nCV 57,58,59\n"
"Zvaa 92,136,184\nv_mul_f32 v60, s57, v60\nv_mul_f32 v61, s57, v61\nMvaa 92,140,188\nD0 5,20,45,3072\nCV 58,60,61\nZvaa 88,132,188\nv_mul_f32 v62, s57, v62\n"
"v_mul_f32 v63, s57, v63\nMvaa 88,128,184\ns_waitcnt vmcnt(18) lgkmcnt(0)\ns_barrier\nZvaa 96,128,224\nRa 192,10,16384\nCV 59,62,63\nMvaa 96,132,228\ns_add_u32 m0, s47, 0\n"
"s_add_u32 s44, s44, 0x40000\nZvaa 100,140,228\nRa 196,11,16384\nAR 20,112\nMvaa 100,136,224\nAR 21,113\nv_mul_f32 v20, s57, v20\nZvaa 160,144,224\nRa 200,10,18432\n"
"v_mul_f32 v21, s57, v21\nMvaa 160,148,228\nD0 6,24,44,0\nCV 52,20,21\nZvaa 164,156,228\nRa 204,11,18432\nAR 20,114\nMvaa 164,152,224\nAR 21,115\nv_mul_f32 v20, s57, v20\n"
"Zvaa 172,152,232\nRa 208,10,24576\nv_mul_f32 v21, s57, v21\nMvaa 172,156,236\nCV 53,20,21\ns_add_u32 s6, s56, 0\nSv 36,1,16,6,1024\nZvaa 168,148,236\nRa 212,11,24576\nAR 20,116\n"
"Mvaa 168,144,232\nAR 21,117\nv_mul_f32 v20, s57, v20\nZvaa 108,136,232\nRa 216,10,26624\nv_mul_f32 v21, s57, v21\nMvaa 108,140,236\nD0 7,24,44,1024\nCV 54,20,21\nZvaa 104,132,236\n"
"Ra 220,11,26624\nAR 20,118\nMvaa 104,128,232\nAR 21,119\nv_mul_f32 v20, s57, v20\nZvaa 112,128,240\nv_mul_f32 v21, s57, v21\nCV 55,20,21\nMvaa 112,132,244\nAR 20,120\n"
"Zvaa 116,140,244\nAR 21,121\nv_mul_f32 v20, s57, v20\nMvaa 116,136,240\nv_mul_f32 v21, s57, v21\nCV 60,20,21\nZvaa 176,144,240\nAR 20,122\nAR 21,123\nMvaa 176,148,244\n"
"D0 8,24,44,2048\nv_mul_f32 v20, s57, v20\nZvaa 180,156,244\nv_mul_f32 v21, s57, v21\nCV 61,20,21\nMvaa 180,152,240\nAR 20,124\nAR 21,125\nZvaa 188,152,248\n"
"v_mul_f32 v20, s57, v20\nv_mul_f32 v21, s57, v21\nMvaa 188,156,252\nCV 62,20,21\ns_add_u32 s6, s56, 0\nSv 44,1,16,6,3072\nZvaa 184,148,252\nAR 20,126\nAR 21,127\nMvaa 184,144,248\n"
"v_mul_f32 v20, s57, v20\nv_mul_f32 v21, s57, v21\nZvaa 124,136,248\nCV 63,20,21\nv_mul_f32 v192, s57, v192\nMvaa 124,140,252\nD0 9,24,44,3072\nv_mul_f32 v193, s57, v193\n"
"Zvaa 120,132,252\nCV 192,192,193\nv_mul_f32 v194, s57, v194\nMvaa 120,128,248\nWL 0\nZaaa 32,192,224\nRa 128,10,32768\nv_mul_f32 v195, s57, v195\nMaaa 32,196,228\n"
"s_add_u32 m0, s47, 16384\ns_add_u32 s44, s44, 0x40000\nZaaa 36,204,228\nRa 132,11,32768\nCV 193,194,195\nMaaa 36,200,224\nv_mul_f32 v196, s57, v196\nv_mul_f32 v197, s57, v197\n"
"Zaaa 96,208,224\nRa 136,10,34816\nCV 194,196,197\nMaaa 96,212,228\nD0 6,24,44,0\nv_mul_f32 v198, s57, v198\nZaaa 100,220,228\nRa 140,11,34816\nv_mul_f32 v199, s57, v199\n"
"Maaa 100,216,224\nCV 195,198,199\nv_mul_f32 v200, s57, v200\nZaaa 108,216,232\nRa 144,10,40960\nv_mul_f32 v201, s57, v201\nMaaa 108,220,236\nCV 200,200,201\n"
"v_mul_f32 v202, s57, v202\ns_add_u32 s6, s56, 4096\nSv 48,1,16,6,0\nZaaa 104,212,236\nRa 148,11,40960\nv_mul_f32 v203, s57, v203\nMaaa 104,208,232\nCV 201,202,203\n"
"v_mul_f32 v204, s57, v204\nZaaa 44,200,232\nRa 152,10,43008\nv_mul_f32 v205, s57, v205\nMaaa 44,204,236\nD0 7,24,44,1024\nCV 202,204,205\nZaaa 40,196,236\nRa 156,11,43008\n"
"v_mul_f32 v206, s57, v206\nMaaa 40,192,232\nv_mul_f32 v207, s57, v207\nCV 203,206,207\nZaaa 48,192,240\nv_mul_f32 v208, s57, v208\nv_mul_f32 v209, s57, v209\nMaaa 48,196,244\n"
"CV 208,208,209\nv_mul_f32 v210, s57, v210\nZaaa 52,204,244\nv_mul_f32 v211, s57, v211\nCV 209,210,211\nMaaa 52,200,240\nv_mul_f32 v212, s57, v212\nv_mul_f32 v213, s57, v213\n"
"Zaaa 112,208,240\nCV 210,212,213\nv_mul_f32 v214, s57, v214\nMaaa 112,212,244\nD0 8,24,44,2048\nv_mul_f32 v215, s57, v215\nZaaa 116,220,244\nCV 211,214,215\n"
"v_mul_f32 v216, s57, v216\nMaaa 116,216,240\nv_mul_f32 v217, s57, v217\nCV 216,216,217\nZaaa 124,216,248\nv_mul_f32 v218, s57, v218\nv_mul_f32 v219, s57, v219\nMaaa 124,220,252\n"
"CV 217,218,219\nv_mul_f32 v220, s57, v220\ns_add_u32 s6, s56, 4096\nSv 56,1,16,6,2048\nZaaa 120,212,252\nv_mul_f32 v221, s57, v221\nCV 218,220,221\nMaaa 120,208,248\n"
"v_mul_f32 v222, s57, v222\nv_mul_f32 v223, s57, v223\nZaaa 60,200,248\nCV 219,222,223\nv_mul_f32 v224, s57, v224\nMaaa 60,204,252\nD0 9,24,44,3072\nv_mul_f32 v225, s57, v225\n"
"Zaaa 56,196,252\nCV 224,224,225\nv_mul_f32 v226, s57, v226\nMaaa 56,192,248\ns_waitcnt vmcnt(22) lgkmcnt(0)\ns_barrier\nZaaa 0,192,160\nTa 224,16,32768\nv_mul_f32 v227, s57, v227\n"
"Maaa 0,196,164\nTa 226,16,34816\nCV 225,226,227\nZaaa 4,204,164\ns_add_u32 m0, s47, -49152\ns_add_u32 s48, s48, 128\nMaaa 4,200,160\nTa 228,16,49152\nv_mul_f32 v228, s57, v228\n"
"Zaaa 64,208,160\nTa 230,16,51200\nv_mul_f32 v229, s57, v229\nMaaa 64,212,164\nD0 2,20,48,0\nCV 226,228,229\nZaaa 68,220,164\nTa 232,17,32768\nv_mul_f32 v230, s57, v230\n"
"Maaa 68,216,160\nTa 234,17,34816\nv_mul_f32 v231, s57, v231\nZaaa 76,216,168\nCV 227,230,231\nv_mul_f32 v232, s57, v232\nMaaa 76,220,172\nTa 236,17,49152\n"
"v_mul_f32 v233, s57, v233\ns_add_u32 s6, s56, 4096\nSv 52,1,16,6,1024\nZaaa 72,212,172\nTa 238,17,51200\nCV 232,232,233\nMaaa 72,208,168\nv_mul_f32 v234, s57, v234\n"
"v_mul_f32 v235, s57, v235\nZaaa 12,200,168\nTa 240,18,32768\nCV 233,234,235\nMaaa 12,204,172\nTa 242,18,34816\nv_mul_f32 v236, s57, v236\nZaaa 8,196,172\nD0 3,20,48,1024\n"
"v_mul_f32 v237, s57, v237\nMaaa 8,192,168\nTa 244,18,49152\nCV 234,236,237\nZaaa 16,192,176\nTa 246,18,51200\nv_mul_f32 v238, s57, v238\nMaaa 16,196,180\n"
"v_mul_f32 v239, s57, v239\nCV 235,238,239\nZaaa 20,204,180\nTa 248,19,32768\nv_mul_f32 v240, s57, v240\nMaaa 20,200,176\nTa 250,19,34816\nv_mul_f32 v241, s57, v241\n"
"Zaaa 80,208,176\nCV 240,240,241\nv_mul_f32 v242, s57, v242\nMaaa 80,212,180\nTa 252,19,49152\nv_mul_f32 v243, s57, v243\nZaaa 84,220,180\nTa 254,19,51200\nCV 241,242,243\n"
"Maaa 84,216,176\nD0 4,20,48,2048\nv_mul_f32 v244, s57, v244\nZaaa 92,216,184\nv_mul_f32 v245, s57, v245\nCV 242,244,245\nMaaa 92,220,188\nv_mul_f32 v246, s57, v246\n"
"v_mul_f32 v247, s57, v247\ns_add_u32 s6, s56, 4096\nSv 60,1,16,6,3072\nZaaa 88,212,188\nCV 243,246,247\nv_mul_f32 v248, s57, v248\nMaaa 88,208,184\nv_mul_f32 v249, s57, v249\n"
"CV 248,248,249\nZaaa 28,200,184\nv_mul_f32 v250, s57, v250\nv_mul_f32 v251, s57, v251\nMaaa 28,204,188\nD0 5,20,48,3072\nCV 249,250,251\nZaaa 24,196,188\n"
"v_mul_f32 v252, s57, v252\nv_mul_f32 v253, s57, v253\nMaaa 24,192,184\nWL 0\nMvaa 96,128,224\nTa 160,12,32768\nCV 250,252,253\nMvaa 96,132,228\nTa 162,12,34816\n"
"v_mul_f32 v254, s57, v254\nMvaa 100,140,228\ns_add_u32 m0, s47, -32768\ns_add_u32 s45, s45, 128\nMvaa 100,136,224\nTa 164,12,49152\nv_mul_f32 v255, s57, v255\nMvaa 160,144,224\n"
"Ta 166,12,51200\nCV 251,254,255\nMvaa 160,148,228\nD0 2,20,45,0\nMvaa 164,156,228\nTa 168,13,32768\nMvaa 164,152,224\nTa 170,13,34816\nMvaa 172,152,232\nMvaa 172,156,236\n"
"Ta 172,13,49152\ns_add_u32 s6, s53, 0\nSv 192,1,16,6,0\nMvaa 168,148,236\nTa 174,13,51200\nMvaa 168,144,232\nMvaa 108,136,232\nTa 176,14,32768\nMvaa 108,140,236\nTa 178,14,34816\n"
"Mvaa 104,132,236\nD0 3,20,45,1024\nMvaa 104,128,232\nTa 180,14,49152\nMvaa 112,128,240\nTa 182,14,51200\nMvaa 112,132,244\nMvaa 116,140,244\nTa 184,15,32768\nMvaa 116,136,240\n"
"Ta 186,15,34816\nMvaa 176,144,240\nMvaa 176,148,244\nTa 188,15,49152\nMvaa 180,156,244\nTa 190,15,51200\nMvaa 180,152,240\nD0 4,20,45,2048\nMvaa 188,152,248\nMvaa 188,156,252\n"
"s_add_u32 s6, s53, 0\nSv 200,1,16,6,2048\nMvaa 184,148,252\nMvaa 184,144,248\nMvaa 124,136,248\nMvaa 124,140,252\nD0 5,20,45,3072\nMvaa 120,132,252\nMvaa 120,128,248\n"
"s_waitcnt vmcnt(24) lgkmcnt(0)\ns_barrier\nMvaa 64,128,160\nRa 192,10,49152\nMvaa 64,132,164\ns_add_u32 m0, s47, 32768\ns_add_u32 s44, s44, 0x40000\nMvaa 68,140,164\n"
"Ra 196,11,49152\nMvaa 68,136,160\nMvaa 128,144,160\nRa 200,10,51200\nMvaa 128,148,164\nD0 6,24,44,0\nMvaa 132,156,164\nRa 204,11,51200\nMvaa 132,152,160\nMvaa 140,152,168\n"
"Ra 208,10,57344\nMvaa 140,156,172\ns_add_u32 s6, s53, 4096\nSv 208,1,16,6,0\nMvaa 136,148,172\nRa 212,11,57344\nMvaa 136,144,168\nMvaa 76,136,168\nRa 216,10,59392\n"
"Mvaa 76,140,172\nD0 7,24,44,1024\nMvaa 72,132,172\nRa 220,11,59392\nMvaa 72,128,168\nMvaa 80,128,176\nMvaa 80,132,180\nMvaa 84,140,180\nMvaa 84,136,176\nMvaa 144,144,176\n"
"Mvaa 144,148,180\n.W557:\ns_cbranch_execz .W561\nD0 8,24,44,2048\nMvaa 148,156,180\nMvaa 148,152,176\nMvaa 156,152,184\nMvaa 156,156,188\ns_add_u32 s6, s53, 4096\n"
"Sv 216,1,16,6,2048\nMvaa 152,148,188\nMvaa 152,144,184\nMvaa 92,136,184\nMvaa 92,140,188\nD0 9,24,44,3072\nMvaa 88,132,188\nMvaa 88,128,184\nWL 0\nMaaa 0,192,160\nRa 128,10,0\n"
"Maaa 0,196,164\ns_add_u32 m0, s47, 49152\ns_add_u32 s44, s44, 0x40000\nMaaa 4,204,164\nRa 132,11,0\nMaaa 4,200,160\nMaaa 64,208,160\nRa 136,10,2048\nMaaa 64,212,164\nD0 6,24,44,0\n"
"Maaa 68,220,164\nRa 140,11,2048\nMaaa 68,216,160\nMaaa 76,216,168\nRa 144,10,8192\nMaaa 76,220,172\ns_add_u32 s6, s53, 0\nSv 224,1,16,6,1024\nMaaa 72,212,172\nRa 148,11,8192\n"
"Maaa 72,208,168\nMaaa 12,200,168\nRa 152,10,10240\nMaaa 12,204,172\nD0 7,24,44,1024\nMaaa 8,196,172\nRa 156,11,10240\nMaaa 8,192,168\nMaaa 16,192,176\nMaaa 16,196,180\n"
"Maaa 20,204,180\nMaaa 20,200,176\nMaaa 80,208,176\nMaaa 80,212,180\nD0 8,24,44,2048\nMaaa 84,220,180\nMaaa 84,216,176\nMaaa 92,216,184\nMaaa 92,220,188\ns_add_u32 s6, s53, 0\n"
"Sv 232,1,16,6,3072\nMaaa 88,212,188\nMaaa 88,208,184\nMaaa 28,200,184\nMaaa 28,204,188\nD0 9,24,44,3072\nMaaa 24,196,188\nMaaa 24,192,184\ns_waitcnt vmcnt(24) lgkmcnt(0)\n"
"s_barrier\nMaaa 32,192,224\nTa 160,12,0\nMaaa 32,196,228\nTa 162,12,2048\nMaaa 36,204,228\ns_add_u32 m0, s47, -16384\ns_add_u32 s48, s48, 128\nMaaa 36,200,224\nTa 164,12,16384\n"
"Maaa 96,208,224\nTa 166,12,18432\nMaaa 96,212,228\nD0 2,20,48,0\nMaaa 100,220,228\nTa 168,13,0\nMaaa 100,216,224\nTa 170,13,2048\nMaaa 108,216,232\nMaaa 108,220,236\n"
"Ta 172,13,16384\ns_add_u32 s6, s53, 4096\nSv 240,1,16,6,1024\nMaaa 104,212,236\nTa 174,13,18432\nMaaa 104,208,232\nMaaa 44,200,232\nTa 176,14,0\nMaaa 44,204,236\nTa 178,14,2048\n"
"Maaa 40,196,236\nD0 3,20,48,1024\nMaaa 40,192,232\nTa 180,14,16384\nMaaa 48,192,240\nTa 182,14,18432\nMaaa 48,196,244\ns_add_u32 s6, s53, 4096\nSv 248,1,16,6,3072\n"
"Maaa 52,204,244\nTa 184,15,0\nMaaa 52,200,240\nTa 186,15,2048\nMaaa 112,208,240\nMaaa 112,212,244\nTa 188,15,16384\nMaaa 116,220,244\nTa 190,15,18432\nMaaa 116,216,240\n"
"D0 4,20,48,2048\nMaaa 124,216,248\nMaaa 124,220,252\nMaaa 120,212,252\nMaaa 120,208,248\ns_lshl3_add_u32 s90, s12, s13\ns_lshl2_add_u32 s90, s90, s11\n"
"s_lshl2_add_u32 s90, s90, 0xc04a400\nMaaa 60,200,248\nMaaa 60,204,252\nD0 5,20,48,3072\nMaaa 56,196,252\nMaaa 56,192,248\nWL 0\ns_mov_b64 s[76:77], s[62:63]\n"
"s_mov_b64 s[78:79], s[64:65]\ns_mov_b32 s42, 1605632\ns_mov_b32 s50, 1\n.GL151:\nMvaa 64,128,160\nTa 224,16,0\nMvaa 64,132,164\nTa 226,16,2048\nMvaa 68,140,164\n"
"s_add_u32 m0, s47, -65536\ns_add_u32 s45, s45, 128\nMvaa 68,136,160\nTa 228,16,16384\nMvaa 128,144,160\nTa 230,16,18432\nMvaa 128,148,164\nD0 2,20,45,0\nMvaa 132,156,164\n"
"Ta 232,17,0\nMvaa 132,152,160\nTa 234,17,2048\nMvaa 140,152,168\nMvaa 140,156,172\nTa 236,17,16384\nMvaa 136,148,172\nTa 238,17,18432\nMvaa 136,144,168\nMvaa 76,136,168\n"
"Ta 240,18,0\nMvaa 76,140,172\nTa 242,18,2048\nMvaa 72,132,172\nD0 3,20,45,1024\nMvaa 72,128,168\nTa 244,18,16384\nMvaa 80,128,176\nTa 246,18,18432\nMvaa 80,132,180\n"
"Mvaa 84,140,180\nTa 248,19,0\nMvaa 84,136,176\nTa 250,19,2048\nMvaa 144,144,176\nMvaa 144,148,180\nTa 252,19,16384\nMvaa 148,156,180\nTa 254,19,18432\nMvaa 148,152,176\n"
"D0 4,20,45,2048\nMvaa 156,152,184\nMvaa 156,156,188\nMvaa 152,148,188\nMvaa 152,144,184\ns_mov_b32 s40, s76\ns_and_b32 s41, s77, 0xffff\nMvaa 92,136,184\nMvaa 92,140,188\n"
"D0 5,20,45,3072\nMvaa 88,132,188\nMvaa 88,128,184\ns_waitcnt vmcnt(17) lgkmcnt(0)\ns_barrier\nMvaa 96,128,224\nRa 192,10,16384\nMvaa 96,132,228\ns_add_u32 m0, s47, 0\n"
"s_add_u32 s44, s44, 0x40000\nbuffer_load_dword v31, v26, s[40:43], s97 offen\ns_mov_b64 s[76:77], s[66:67]\nMvaa 100,140,228\nRa 196,11,16384\nMvaa 100,136,224\nMvaa 160,144,224\n"
"Ra 200,10,18432\nMvaa 160,148,228\nD0 6,24,44,0\nMvaa 164,156,228\nRa 204,11,18432\nMvaa 164,152,224\nMvaa 172,152,232\nRa 208,10,24576\nMvaa 172,156,236\nMvaa 168,148,236\n"
"Ra 212,11,24576\nMvaa 168,144,232\nMvaa 108,136,232\nRa 216,10,26624\nMvaa 108,140,236\nD0 7,24,44,1024\nMvaa 104,132,236\nRa 220,11,26624\nMvaa 104,128,232\nMvaa 112,128,240\n"
"Mvaa 112,132,244\nMvaa 116,140,244\nMvaa 116,136,240\nMvaa 176,144,240\nMvaa 176,148,244\nD0 8,24,44,2048\nMvaa 180,156,244\nMvaa 180,152,240\nMvaa 188,152,248\nMvaa 188,156,252\n"
"Mvaa 184,148,252\nMvaa 184,144,248\nMvaa 124,136,248\nMvaa 124,140,252\nD0 9,24,44,3072\nMvaa 120,132,252\nMvaa 120,128,248\nWL 0\nMaaa 32,192,224\nRa 128,10,32768\n"
"Maaa 32,196,228\ns_add_u32 m0, s47, 16384\ns_add_u32 s44, s44, 0x40000\nMaaa 36,204,228\nRa 132,11,32768\nMaaa 36,200,224\nMaaa 96,208,224\nRa 136,10,34816\nMaaa 96,212,228\n"
"D0 6,24,44,0\nMaaa 100,220,228\nRa 140,11,34816\nMaaa 100,216,224\nMaaa 108,216,232\nRa 144,10,40960\nMaaa 108,220,236\nMaaa 104,212,236\nRa 148,11,40960\nMaaa 104,208,232\n"
"Maaa 44,200,232\nRa 152,10,43008\nMaaa 44,204,236\nD0 7,24,44,1024\nMaaa 40,196,236\nRa 156,11,43008\nMaaa 40,192,232\nMaaa 48,192,240\nMaaa 48,196,244\nMaaa 52,204,244\n"
"Maaa 52,200,240\nMaaa 112,208,240\nMaaa 112,212,244\nD0 8,24,44,2048\nMaaa 116,220,244\nMaaa 116,216,240\nMaaa 124,216,248\nMaaa 124,220,252\nMaaa 120,212,252\nMaaa 120,208,248\n"
"Maaa 60,200,248\nMaaa 60,204,252\nD0 9,24,44,3072\nMaaa 56,196,252\nMaaa 56,192,248\ns_waitcnt vmcnt(17) lgkmcnt(0)\ns_barrier\nMaaa 0,192,160\nTa 224,16,32768\nMaaa 0,196,164\n"
"Ta 226,16,34816\nMaaa 4,204,164\ns_add_u32 m0, s47, -49152\ns_add_u32 s48, s48, 128\nMaaa 4,200,160\nTa 228,16,49152\nMaaa 64,208,160\nTa 230,16,51200\nMaaa 64,212,164\n"
"D0 2,20,48,0\nMaaa 68,220,164\nTa 232,17,32768\nMaaa 68,216,160\nTa 234,17,34816\nMaaa 76,216,168\nMaaa 76,220,172\nTa 236,17,49152\nMaaa 72,212,172\nTa 238,17,51200\n"
"Maaa 72,208,168\nMaaa 12,200,168\nTa 240,18,32768\nMaaa 12,204,172\nTa 242,18,34816\nMaaa 8,196,172\nD0 3,20,48,1024\nMaaa 8,192,168\nTa 244,18,49152\nMaaa 16,192,176\n"
"Ta 246,18,51200\nMaaa 16,196,180\nMaaa 20,204,180\nTa 248,19,32768\nMaaa 20,200,176\nTa 250,19,34816\nMaaa 80,208,176\nMaaa 80,212,180\nTa 252,19,49152\nMaaa 84,220,180\n"
"Ta 254,19,51200\nMaaa 84,216,176\nD0 4,20,48,2048\nMaaa 92,216,184\nMaaa 92,220,188\nMaaa 88,212,188\nMaaa 88,208,184\nMaaa 28,200,184\nMaaa 28,204,188\nD0 5,20,48,3072\n"
"Maaa 24,196,188\nMaaa 24,192,184\nWL 0\nMvaa 96,128,224\nTa 160,12,32768\nMvaa 96,132,228\nTa 162,12,34816\nMvaa 100,140,228\ns_add_u32 m0, s47, -32768\ns_add_u32 s45, s45, 128\n"
"Mvaa 100,136,224\nTa 164,12,49152\nMvaa 160,144,224\nTa 166,12,51200\nMvaa 160,148,228\nD0 2,20,45,0\nMvaa 164,156,228\nTa 168,13,32768\nMvaa 164,152,224\nTa 170,13,34816\n"
"Mvaa 172,152,232\nMvaa 172,156,236\nTa 172,13,49152\nMvaa 168,148,236\nTa 174,13,51200\nMvaa 168,144,232\nMvaa 108,136,232\nTa 176,14,32768\nMvaa 108,140,236\nTa 178,14,34816\n"
"Mvaa 104,132,236\nD0 3,20,45,1024\nMvaa 104,128,232\nTa 180,14,49152\nMvaa 112,128,240\nTa 182,14,51200\nMvaa 112,132,244\nMvaa 116,140,244\nTa 184,15,32768\nMvaa 116,136,240\n"
"Ta 186,15,34816\nMvaa 176,144,240\nMvaa 176,148,244\nTa 188,15,49152\nMvaa 180,156,244\nTa 190,15,51200\nMvaa 180,152,240\nD0 4,20,45,2048\nMvaa 188,152,248\nMvaa 188,156,252\n"
"Mvaa 184,148,252\nMvaa 184,144,248\ns_mov_b32 s40, s78\ns_and_b32 s41, s79, 0xffff\nMvaa 124,136,248\nMvaa 124,140,252\nD0 5,20,45,3072\nMvaa 120,132,252\nMvaa 120,128,248\n"
"s_waitcnt vmcnt(17) lgkmcnt(0)\ns_barrier\nMvaa 64,128,160\nRa 192,10,49152\nMvaa 64,132,164\ns_add_u32 m0, s47, 32768\ns_add_u32 s44, s44, 0x40000\n"
"buffer_load_dword v31, v26, s[40:43], s97 offen\ns_mov_b64 s[78:79], s[66:67]\nMvaa 68,140,164\nRa 196,11,49152\nMvaa 68,136,160\nMvaa 128,144,160\nRa 200,10,51200\n"
"Mvaa 128,148,164\nD0 6,24,44,0\nMvaa 132,156,164\nRa 204,11,51200\nMvaa 132,152,160\nMvaa 140,152,168\nRa 208,10,57344\nMvaa 140,156,172\nMvaa 136,148,172\nRa 212,11,57344\n"
"Mvaa 136,144,168\nMvaa 76,136,168\nRa 216,10,59392\nMvaa 76,140,172\nD0 7,24,44,1024\nMvaa 72,132,172\nRa 220,11,59392\nMvaa 72,128,168\nMvaa 80,128,176\nMvaa 80,132,180\n"
"Mvaa 84,140,180\nMvaa 84,136,176\nMvaa 144,144,176\nMvaa 144,148,180\nD0 8,24,44,2048\nMvaa 148,156,180\nMvaa 148,152,176\nMvaa 156,152,184\nMvaa 156,156,188\nMvaa 152,148,188\n"
"Mvaa 152,144,184\nMvaa 92,136,184\nMvaa 92,140,188\nD0 9,24,44,3072\nMvaa 88,132,188\nMvaa 88,128,184\nWL 0\nMaaa 0,192,160\nRa 128,10,0\nMaaa 0,196,164\ns_add_u32 m0, s47, 49152\n"
"s_add_u32 s44, s44, 0x40000\nMaaa 4,204,164\nRa 132,11,0\nMaaa 4,200,160\nMaaa 64,208,160\nRa 136,10,2048\nMaaa 64,212,164\nD0 6,24,44,0\nMaaa 68,220,164\nRa 140,11,2048\n"
"Maaa 68,216,160\nMaaa 76,216,168\nRa 144,10,8192\nMaaa 76,220,172\nMaaa 72,212,172\nRa 148,11,8192\nMaaa 72,208,168\nMaaa 12,200,168\nRa 152,10,10240\nMaaa 12,204,172\n"
"D0 7,24,44,1024\nMaaa 8,196,172\nRa 156,11,10240\nMaaa 8,192,168\nMaaa 16,192,176\nMaaa 16,196,180\nMaaa 20,204,180\nMaaa 20,200,176\nMaaa 80,208,176\nMaaa 80,212,180\n"
"D0 8,24,44,2048\nMaaa 84,220,180\nMaaa 84,216,176\nMaaa 92,216,184\nMaaa 92,220,188\nMaaa 88,212,188\nMaaa 88,208,184\nMaaa 28,200,184\nMaaa 28,204,188\nD0 9,24,44,3072\n"
"Maaa 24,196,188\nMaaa 24,192,184\ns_waitcnt vmcnt(17) lgkmcnt(0)\ns_barrier\nMaaa 32,192,224\nTa 160,12,0\nMaaa 32,196,228\nTa 162,12,2048\nMaaa 36,204,228\n"
"s_add_u32 m0, s47, -16384\ns_add_u32 s48, s48, 128\nMaaa 36,200,224\nTa 164,12,16384\nMaaa 96,208,224\nTa 166,12,18432\nMaaa 96,212,228\nD0 2,20,48,0\nMaaa 100,220,228\n"
"Ta 168,13,0\nMaaa 100,216,224\nTa 170,13,2048\nMaaa 108,216,232\nMaaa 108,220,236\nTa 172,13,16384\nMaaa 104,212,236\nTa 174,13,18432\nMaaa 104,208,232\nMaaa 44,200,232\n"
"Ta 176,14,0\nMaaa 44,204,236\nTa 178,14,2048\n.W558:\ns_cbranch_execz .W562\nMaaa 40,196,236\nD0 3,20,48,1024\nMaaa 40,192,232\nTa 180,14,16384\nMaaa 48,192,240\nTa 182,14,18432\n"
"Maaa 48,196,244\nMaaa 52,204,244\nTa 184,15,0\nMaaa 52,200,240\nTa 186,15,2048\nMaaa 112,208,240\nMaaa 112,212,244\nTa 188,15,16384\nMaaa 116,220,244\nTa 190,15,18432\n"
"Maaa 116,216,240\nD0 4,20,48,2048\nMaaa 124,216,248\nMaaa 124,220,252\nMaaa 120,212,252\nMaaa 120,208,248\nMaaa 60,200,248\nMaaa 60,204,252\nD0 5,20,48,3072\n"
"s_sub_u32 s50, s50, 1\nMaaa 56,196,252\nMaaa 56,192,248\nWL 0\ns_cbranch_scc0 .GL151\nMvaa 64,128,160\nTa 224,16,0\nMvaa 64,132,164\nTa 226,16,2048\nMvaa 68,140,164\n"
"s_add_u32 m0, s47, -65536\ns_add_u32 s45, s45, 128\nMvaa 68,136,160\nTa 228,16,16384\nMvaa 128,144,160\nTa 230,16,18432\nMvaa 128,148,164\nD0 2,20,45,0\nMvaa 132,156,164\n"
"Ta 232,17,0\nMvaa 132,152,160\nTa 234,17,2048\nMvaa 140,152,168\ns_and_b32 s2, s74, 15\nMvaa 140,156,172\nTa 236,17,16384\n"
"buffer_store_dword v25, off, s[16:19], s90 offset:0 sc0 sc1\nMvaa 136,148,172\nTa 238,17,18432\nMvaa 136,144,168\ns_sub_u32 s2, s2, 2\ns_cmp_lt_u32 s2, 14\n"
"s_cselect_b32 s5, 0, 0x40000000\nMvaa 76,136,168\nTa 240,18,0\nMvaa 76,140,172\nTa 242,18,2048\nMvaa 72,132,172\nD0 3,20,45,1024\nMvaa 72,128,168\nTa 244,18,16384\n"
"Mvaa 80,128,176\nTa 246,18,18432\nMvaa 80,132,180\ns_and_b32 s2, s2, 15\nMvaa 84,140,180\nTa 248,19,0\nMvaa 84,136,176\nTa 250,19,2048\nMvaa 144,144,176\ns_mul_i32 s4, s73, 14\n"
"Mvaa 144,148,180\nTa 252,19,16384\nMvaa 148,156,180\nTa 254,19,18432\nMvaa 148,152,176\nD0 4,20,45,2048\nMvaa 156,152,184\ns_add_u32 s4, s4, s2\nMvaa 156,156,188\n"
"s_mov_b32 s36, s62\nMvaa 152,148,188\ns_and_b32 s37, s63, 0xffff\nMvaa 152,144,184\ns_mov_b32 s38, 0x188000\nMvaa 92,136,184\ns_mov_b32 s39, 0x20000\nMvaa 92,140,188\n"
"D0 5,20,45,3072\nMvaa 88,132,188\ns_mov_b32 s40, s64\nMvaa 88,128,184\ns_waitcnt vmcnt(18) lgkmcnt(0)\ns_barrier\nMvaa 96,128,224\nRa 192,10,16384\nMvaa 96,132,228\n"
"s_add_u32 m0, s47, 0\ns_add_u32 s44, s44, 0x40000\nMvaa 100,140,228\nRa 196,11,16384\nMvaa 100,136,224\ns_and_b32 s41, s65, 0xffff\nMvaa 160,144,224\nRa 200,10,18432\n"
"Mvaa 160,148,228\nD0 6,24,44,0\nMvaa 164,156,228\nRa 204,11,18432\nMvaa 164,152,224\ns_mov_b32 s42, 0x188000\nMvaa 172,152,232\nRa 208,10,24576\nMvaa 172,156,236\n"
"s_mov_b32 s43, 0x20000\nMvaa 168,148,236\nRa 212,11,24576\nMvaa 168,144,232\ns_mov_b32 s92, s66\nMvaa 108,136,232\nRa 216,10,26624\nMvaa 108,140,236\nD0 7,24,44,1024\n"
"Mvaa 104,132,236\nRa 220,11,26624\nMvaa 104,128,232\ns_and_b32 s93, s67, 0xffff\nMvaa 112,128,240\ns_mov_b32 s94, 0x188000\nMvaa 112,132,244\ns_mov_b32 s95, 0x20000\n"
"Mvaa 116,140,244\nv_lshrrev_b32 v252, 1, v0\nMvaa 116,136,240\nv_mul_u32_u24 v253, 14336, v252\nMvaa 176,144,240\nv_and_b32 v254, 1, v0\nMvaa 176,148,244\nD0 8,24,44,2048\n"
"Mvaa 180,156,244\nv_lshl_add_u32 v253, v254, 4, v253\nMvaa 180,152,240\nv_mov_b32 v254, 0x40000000\nv_cmp_gt_u32 vcc, 56, v0\nv_lshlrev_b32 v252, 3, v252\n"
"s_mul_i32 s2, s11, 401408\nv_cndmask_b32 v21, v254, v253, vcc\nv_cndmask_b32 v255, v254, v252, vcc\nMvaa 188,152,248\ns_lshl_b32 s3, s4, 5\nMvaa 188,156,252\ns_add_u32 s2, s2, s3\n"
"Mvaa 184,148,252\ns_or_b32 s86, s2, s5\nMvaa 184,144,248\ns_add_u32 s87, s86, 7168\nMvaa 124,136,248\nv_lshrrev_b32 v251, 4, v0\nMvaa 124,140,252\nD0 9,24,44,3072\n"
"Mvaa 120,132,252\nv_mul_u32_u24 v252, 3584, v251\nMvaa 120,128,248\nWL 0\nMaaa 32,192,224\nRa 128,10,32768\nMaaa 32,196,228\ns_add_u32 m0, s47, 16384\ns_add_u32 s44, s44, 0x40000\n"
"Maaa 36,204,228\nRa 132,11,32768\nMaaa 36,200,224\nv_and_b32 v251, 15, v0\nMaaa 96,208,224\nRa 136,10,34816\nMaaa 96,212,228\nD0 6,24,44,0\nMaaa 100,220,228\nRa 140,11,34816\n"
"Maaa 100,216,224\nv_lshl_add_u32 v252, v251, 4, v252\nv_cmp_gt_u32 vcc, 14, v251\ns_mul_i32 s2, s4, 14336\ns_mul_i32 s3, s11, 224\nv_cndmask_b32 v20, v254, v252, vcc\n"
"Maaa 108,216,232\nRa 144,10,40960\nMaaa 108,220,236\ns_add_u32 s2, s2, s3\nMaaa 104,212,236\nRa 148,11,40960\nMaaa 104,208,232\ns_or_b32 s88, s2, s5\nMaaa 44,200,232\n"
"Ra 152,10,43008\nMaaa 44,204,236\nD0 7,24,44,1024\nMaaa 40,196,236\nRa 156,11,43008\nMaaa 40,192,232\nMaaa 48,192,240\nMaaa 48,196,244\nMaaa 52,204,244\nMaaa 52,200,240\n"
"Maaa 112,208,240\nMaaa 112,212,244\nD0 8,24,44,2048\nMaaa 116,220,244\nMaaa 116,216,240\nMaaa 124,216,248\nMaaa 124,220,252\nMaaa 120,212,252\nMaaa 120,208,248\nMaaa 60,200,248\n"
"Maaa 60,204,252\nD0 9,24,44,3072\nMaaa 56,196,252\nMaaa 56,192,248\ns_waitcnt vmcnt(17) lgkmcnt(0)\ns_barrier\nMaaa 0,192,160\nTa 224,16,32768\nMaaa 0,196,164\nTa 226,16,34816\n"
"buffer_load_dwordx4 v[192:195], v21, s[36:39], s86 offen offset:0\nMaaa 4,204,164\ns_add_u32 m0, s47, -49152\ns_add_u32 s48, s48, 128\nMaaa 4,200,160\nTa 228,16,49152\n"
"s_lshl_b32 s6, s11, 12\ns_add_u32 s6, s6, 0xc044000\nMaaa 64,208,160\nTa 230,16,51200\nMaaa 64,212,164\nD0 2,20,48,0\nMaaa 68,220,164\nTa 232,17,32768\nMaaa 68,216,160\n"
"Ta 234,17,34816\nMaaa 76,216,168\nbuffer_load_dwordx4 v[32:35], v1, s[16:19], s6 offen offset:0 sc0 sc1\nMaaa 76,220,172\nTa 236,17,49152\nMaaa 72,212,172\nTa 238,17,51200\n"
"Maaa 72,208,168\nMaaa 12,200,168\nTa 240,18,32768\nMaaa 12,204,172\nTa 242,18,34816\nMaaa 8,196,172\nD0 3,20,48,1024\nMaaa 8,192,168\nTa 244,18,49152\nMaaa 16,192,176\n"
"Ta 246,18,51200\nMaaa 16,196,180\nbuffer_load_dwordx4 v[36:39], v1, s[16:19], s6 offen offset:1024 sc0 sc1\nMaaa 20,204,180\nTa 248,19,32768\nMaaa 20,200,176\nTa 250,19,34816\n"
"Maaa 80,208,176\nMaaa 80,212,180\nTa 252,19,49152\nMaaa 84,220,180\nTa 254,19,51200\nMaaa 84,216,176\nD0 4,20,48,2048\nMaaa 92,216,184\n"
"buffer_load_dwordx4 v[40:43], v1, s[16:19], s6 offen offset:2048 sc0 sc1\nMaaa 92,220,188\nMaaa 88,212,188\nMaaa 88,208,184\nMaaa 28,200,184\n"
"buffer_load_dwordx4 v[44:47], v1, s[16:19], s6 offen offset:3072 sc0 sc1\nMaaa 28,204,188\nD0 5,20,48,3072\nMaaa 24,196,188\nMaaa 24,192,184\nWL 0\nMvaa 96,128,224\n"
"Ta 160,12,32768\nMvaa 96,132,228\nTa 162,12,34816\nMvaa 100,140,228\ns_add_u32 m0, s47, -32768\ns_add_u32 s45, s45, 128\nMvaa 100,136,224\nTa 164,12,49152\nMvaa 160,144,224\n"
"Ta 166,12,51200\nMvaa 160,148,228\nD0 2,20,45,0\nMvaa 164,156,228\nTa 168,13,32768\nMvaa 164,152,224\nTa 170,13,34816\nMvaa 172,152,232\nMvaa 172,156,236\nTa 172,13,49152\n"
"Mvaa 168,148,236\nTa 174,13,51200\nMvaa 168,144,232\nMvaa 108,136,232\nTa 176,14,32768\nMvaa 108,140,236\nTa 178,14,34816\nMvaa 104,132,236\nD0 3,20,45,1024\nMvaa 104,128,232\n"
"Ta 180,14,49152\nMvaa 112,128,240\nTa 182,14,51200\nMvaa 112,132,244\nMvaa 116,140,244\nTa 184,15,32768\nMvaa 116,136,240\nTa 186,15,34816\nMvaa 176,144,240\nMvaa 176,148,244\n"
"Ta 188,15,49152\nMvaa 180,156,244\nTa 190,15,51200\nMvaa 180,152,240\nD0 4,20,45,2048\nMvaa 188,152,248\nMvaa 188,156,252\nMvaa 184,148,252\nMvaa 184,144,248\nMvaa 124,136,248\n"
"Mvaa 124,140,252\nD0 5,20,45,3072\nMvaa 120,132,252\nMvaa 120,128,248\ns_waitcnt vmcnt(21) lgkmcnt(0)\ns_barrier\nMvaa 64,128,160\nRa 192,10,49152\nMvaa 64,132,164\n"
"s_add_u32 m0, s47, 32768\ns_add_u32 s44, s44, 0x40000\nbuffer_load_dwordx4 v[208:211], v21, s[40:43], s86 offen offset:0\nMvaa 68,140,164\nRa 196,11,49152\nMvaa 68,136,160\n"
"Mvaa 128,144,160\nRa 200,10,51200\nMvaa 128,148,164\nD0 6,24,44,0\nMvaa 132,156,164\nRa 204,11,51200\nMvaa 132,152,160\nMvaa 140,152,168\nRa 208,10,57344\nMvaa 140,156,172\n"
"Mvaa 136,148,172\nRa 212,11,57344\nMvaa 136,144,168\nMvaa 76,136,168\nRa 216,10,59392\nMvaa 76,140,172\nD0 7,24,44,1024\nMvaa 72,132,172\nRa 220,11,59392\nMvaa 72,128,168\n"
"Mvaa 80,128,176\nMvaa 80,132,180\nMvaa 84,140,180\nMvaa 84,136,176\nMvaa 144,144,176\nMvaa 144,148,180\nD0 8,24,44,2048\nMvaa 148,156,180\nMvaa 148,152,176\nMvaa 156,152,184\n"
"Mvaa 156,156,188\nMvaa 152,148,188\nMvaa 152,144,184\nMvaa 92,136,184\nMvaa 92,140,188\nD0 9,24,44,3072\nMvaa 88,132,188\nMvaa 88,128,184\nWL 0\nMaaa 0,192,160\nRa 128,10,0\n"
"Maaa 0,196,164\ns_add_u32 m0, s47, 49152\ns_add_u32 s44, s44, 0x40000\nMaaa 4,204,164\nRa 132,11,0\nMaaa 4,200,160\nMaaa 64,208,160\nRa 136,10,2048\nMaaa 64,212,164\nD0 6,24,44,0\n"
"Maaa 68,220,164\nRa 140,11,2048\nMaaa 68,216,160\nMaaa 76,216,168\nRa 144,10,8192\nMaaa 76,220,172\nMaaa 72,212,172\nRa 148,11,8192\nMaaa 72,208,168\nMaaa 12,200,168\n"
"Ra 152,10,10240\nMaaa 12,204,172\nD0 7,24,44,1024\nMaaa 8,196,172\nRa 156,11,10240\nMaaa 8,192,168\nMaaa 16,192,176\nMaaa 16,196,180\nMaaa 20,204,180\nMaaa 20,200,176\n"
"Maaa 80,208,176\nMaaa 80,212,180\nD0 8,24,44,2048\nMaaa 84,220,180\nMaaa 84,216,176\nMaaa 92,216,184\nMaaa 92,220,188\nMaaa 88,212,188\nMaaa 88,208,184\nMaaa 28,200,184\n"
"Maaa 28,204,188\nD0 9,24,44,3072\nMaaa 24,196,188\nMaaa 24,192,184\ns_waitcnt vmcnt(14) lgkmcnt(0)\ns_barrier\nMaaa 32,192,224\nTa 160,12,0\nv_xor_b32 v48, s15, v33\n"
"Maaa 32,196,228\nTa 162,12,2048\nbuffer_load_dwordx4 v[196:199], v21, s[36:39], s86 offen offset:3584\nMaaa 36,204,228\ns_add_u32 m0, s47, -16384\ns_add_u32 s48, s48, 128\n"
"Maaa 36,200,224\nTa 164,12,16384\nMaaa 96,208,224\nTa 166,12,18432\nMaaa 96,212,228\nD0 2,20,48,0\nMaaa 100,220,228\nTa 168,13,0\nMaaa 100,216,224\nTa 170,13,2048\n"
"Maaa 108,216,232\nv_xor_b32 v49, s15, v35\nMaaa 108,220,236\nTa 172,13,16384\nMaaa 104,212,236\nTa 174,13,18432\nMaaa 104,208,232\nv_xor_b32 v50, s15, v37\nMaaa 44,200,232\n"
"Ta 176,14,0\nMaaa 44,204,236\nTa 178,14,2048\nMaaa 40,196,236\nD0 3,20,48,1024\nMaaa 40,192,232\nTa 180,14,16384\nMaaa 48,192,240\nTa 182,14,18432\nv_xor_b32 v51, s15, v39\n"
"Maaa 48,196,244\nv_xor_b32 v52, s15, v41\nMaaa 52,204,244\nTa 184,15,0\nMaaa 52,200,240\nTa 186,15,2048\nMaaa 112,208,240\nv_xor_b32 v53, s15, v43\nMaaa 112,212,244\n"
"Ta 188,15,16384\nMaaa 116,220,244\nTa 190,15,18432\nMaaa 116,216,240\nD0 4,20,48,2048\nMaaa 124,216,248\nv_xor_b32 v54, s15, v45\nMaaa 124,220,252\nv_xor_b32 v55, s15, v47\n"
"Maaa 120,212,252\nv_or3_b32 v48, v48, v49, v50\nMaaa 120,208,248\nv_or3_b32 v51, v51, v52, v53\nMaaa 60,200,248\nv_or3_b32 v48, v48, v54, v55\nMaaa 60,204,252\nD0 5,20,48,3072\n"
"Maaa 56,196,252\nv_or_b32 v48, v48, v51\nMaaa 56,192,248\nWL 0\nMvaa 64,128,160\nTa 224,16,0\nv_cmp_ne_u32 vcc, 0, v48\nMvaa 64,132,164\nTa 226,16,2048\nMvaa 68,140,164\n"
"s_add_u32 m0, s47, -65536\ns_add_u32 s45, s45, 128\nMvaa 68,136,160\nTa 228,16,16384\nMvaa 128,144,160\nTa 230,16,18432\nMvaa 128,148,164\nD0 2,20,45,0\nMvaa 132,156,164\n"
"Ta 232,17,0\nMvaa 132,152,160\nTa 234,17,2048\nMvaa 140,152,168\ns_cbranch_vccnz .S145\n.S146:\nMvaa 140,156,172\nTa 236,17,16384\nMvaa 136,148,172\nTa 238,17,18432\n"
"Mvaa 136,144,168\nv_max_u32 v48, v32, v34\nMvaa 76,136,168\nTa 240,18,0\nMvaa 76,140,172\nTa 242,18,2048\nMvaa 72,132,172\nD0 3,20,45,1024\n.W559:\ns_cbranch_execz .W563\n"
"Mvaa 72,128,168\nTa 244,18,16384\nMvaa 80,128,176\nTa 246,18,18432\nv_min_u32 v49, v32, v34\nMvaa 80,132,180\nv_med3_u32 v49, v48, v49, v36\nMvaa 84,140,180\nTa 248,19,0\n"
"Mvaa 84,136,176\nTa 250,19,2048\nMvaa 144,144,176\nv_max_u32 v48, v48, v36\nMvaa 144,148,180\nTa 252,19,16384\nMvaa 148,156,180\nTa 254,19,18432\nMvaa 148,152,176\n"
"D0 4,20,45,2048\nMvaa 156,152,184\nv_med3_u32 v49, v48, v49, v38\nMvaa 156,156,188\nv_max_u32 v48, v48, v38\nMvaa 152,148,188\nv_med3_u32 v49, v48, v49, v40\nMvaa 152,144,184\n"
"v_max_u32 v48, v48, v40\nMvaa 92,136,184\nv_med3_u32 v49, v48, v49, v42\nMvaa 92,140,188\nD0 5,20,45,3072\nMvaa 88,132,188\nv_max_u32 v48, v48, v42\nMvaa 88,128,184\n"
"s_waitcnt vmcnt(18) lgkmcnt(0)\ns_barrier\nMvaa 96,128,224\nRa 192,10,16384\nv_med3_u32 v49, v48, v49, v44\nMvaa 96,132,228\ns_add_u32 m0, s47, 0\ns_add_u32 s44, s44, 0x40000\n"
"buffer_load_dwordx4 v[212:215], v21, s[40:43], s86 offen offset:3584\nMvaa 100,140,228\nRa 196,11,16384\nMvaa 100,136,224\nv_max_u32 v48, v48, v44\nMvaa 160,144,224\n"
"Ra 200,10,18432\nMvaa 160,148,228\nD0 6,24,44,0\nMvaa 164,156,228\nRa 204,11,18432\nMvaa 164,152,224\nv_med3_u32 v49, v48, v49, v46\nMvaa 172,152,232\nRa 208,10,24576\n"
"Mvaa 172,156,236\nv_max_u32 v48, v48, v46\nMvaa 168,148,236\nRa 212,11,24576\nMvaa 168,144,232\nv_mov_b32 v50, v48\nMvaa 108,136,232\nRa 216,10,26624\nMvaa 108,140,236\n"
"D0 7,24,44,1024\nMvaa 104,132,236\nRa 220,11,26624\nMvaa 104,128,232\nv_max_u32_dpp v50, v50, v50 row_ror:8 row_mask:0xf bank_mask:0xf\nMvaa 112,128,240\nMvaa 112,132,244\n"
"v_max_u32_dpp v50, v50, v50 row_ror:4 row_mask:0xf bank_mask:0xf\nMvaa 116,140,244\nMvaa 116,136,240\nv_max_u32_dpp v50, v50, v50 row_ror:2 row_mask:0xf bank_mask:0xf\n"
"Mvaa 176,144,240\nMvaa 176,148,244\nD0 8,24,44,2048\nMvaa 180,156,244\nv_max_u32_dpp v50, v50, v50 row_ror:1 row_mask:0xf bank_mask:0xf\nMvaa 180,152,240\nv_mov_b32 v52, v50\n"
"Mvaa 188,152,248\nMvaa 188,156,252\nv_permlane16_swap_b32 v50, v52\nMvaa 184,148,252\nv_max_u32 v50, v50, v52\nMvaa 184,144,248\nv_mov_b32 v52, v50\nMvaa 124,136,248\n"
"Mvaa 124,140,252\nD0 9,24,44,3072\nMvaa 120,132,252\nv_permlane32_swap_b32 v50, v52\nMvaa 120,128,248\nWL 0\nMaaa 32,192,224\nRa 128,10,32768\nv_max_u32 v50, v50, v52\n"
"Maaa 32,196,228\ns_add_u32 m0, s47, 16384\ns_add_u32 s44, s44, 0x40000\nMaaa 36,204,228\nRa 132,11,32768\nv_cmp_eq_u32 vcc, v48, v50\ns_lshl_b32 s2, s11, 3\n"
"s_add_u32 s2, s2, 131072\nv_cndmask_b32 v51, v48, v49, vcc\nMaaa 36,200,224\nMaaa 96,208,224\nRa 136,10,34816\nMaaa 96,212,228\nD0 6,24,44,0\nMaaa 100,220,228\nRa 140,11,34816\n"
"Maaa 100,216,224\nv_max_u32_dpp v51, v51, v51 row_ror:8 row_mask:0xf bank_mask:0xf\nMaaa 108,216,232\nRa 144,10,40960\nMaaa 108,220,236\n"
"v_max_u32_dpp v51, v51, v51 row_ror:4 row_mask:0xf bank_mask:0xf\nMaaa 104,212,236\nRa 148,11,40960\nMaaa 104,208,232\n"
"v_max_u32_dpp v51, v51, v51 row_ror:2 row_mask:0xf bank_mask:0xf\nMaaa 44,200,232\nRa 152,10,43008\nMaaa 44,204,236\nD0 7,24,44,1024\nMaaa 40,196,236\nRa 156,11,43008\n"
"Maaa 40,192,232\nv_max_u32_dpp v51, v51, v51 row_ror:1 row_mask:0xf bank_mask:0xf\nMaaa 48,192,240\nv_mov_b32 v52, v51\nMaaa 48,196,244\nMaaa 52,204,244\n"
"v_permlane16_swap_b32 v51, v52\nMaaa 52,200,240\nv_max_u32 v51, v51, v52\nMaaa 112,208,240\nv_mov_b32 v52, v51\nMaaa 112,212,244\nD0 8,24,44,2048\nMaaa 116,220,244\n"
"v_permlane32_swap_b32 v51, v52\nMaaa 116,216,240\nv_max_u32 v51, v51, v52\nMaaa 124,216,248\nv_mov_b32 v52, s2\nMaaa 124,220,252\nds_write_b64 v52, v[50:51]\nMaaa 120,212,252\n"
"v_mov_b32 v52, 131072\nMaaa 120,208,248\nMaaa 60,200,248\nMaaa 60,204,252\nD0 9,24,44,3072\nMaaa 56,196,252\nMaaa 56,192,248\ns_waitcnt vmcnt(18) lgkmcnt(0)\ns_barrier\n"
"Maaa 0,192,160\nTa 224,16,32768\nMaaa 0,196,164\nTa 226,16,34816\nbuffer_load_dwordx4 v[200:203], v21, s[36:39], s87 offen offset:0\nMaaa 4,204,164\ns_add_u32 m0, s47, -49152\n"
"s_add_u32 s48, s48, 128\nMaaa 4,200,160\nTa 228,16,49152\nMaaa 64,208,160\nTa 230,16,51200\nMaaa 64,212,164\nD0 2,20,48,0\nMaaa 68,220,164\nTa 232,17,32768\nMaaa 68,216,160\n"
"Ta 234,17,34816\nMaaa 76,216,168\nRv 32,52,0\nMaaa 76,220,172\nTa 236,17,49152\nMaaa 72,212,172\nTa 238,17,51200\nMaaa 72,208,168\nRv 36,52,16\nMaaa 12,200,168\nTa 240,18,32768\n"
"Maaa 12,204,172\nTa 242,18,34816\nMaaa 8,196,172\nD0 3,20,48,1024\nMaaa 8,192,168\nTa 244,18,49152\nMaaa 16,192,176\nTa 246,18,51200\nMaaa 16,196,180\nMaaa 20,204,180\n"
"Ta 248,19,32768\nMaaa 20,200,176\nTa 250,19,34816\nMaaa 80,208,176\nMaaa 80,212,180\nTa 252,19,49152\nMaaa 84,220,180\nTa 254,19,51200\nMaaa 84,216,176\nD0 4,20,48,2048\n"
"Maaa 92,216,184\nWL 11\nMaaa 92,220,188\nv_max_u32 v48, v32, v33\nMaaa 88,212,188\nv_min_u32 v49, v32, v33\nMaaa 88,208,184\nv_med3_u32 v49, v48, v49, v34\nMaaa 28,200,184\n"
"v_max_u32 v48, v48, v34\nMaaa 28,204,188\nD0 5,20,48,3072\nMaaa 24,196,188\nv_med3_u32 v49, v48, v49, v35\nMaaa 24,192,184\nWL 0\nMvaa 96,128,224\nTa 160,12,32768\n"
"Mvaa 96,132,228\nTa 162,12,34816\nMvaa 100,140,228\ns_add_u32 m0, s47, -32768\ns_add_u32 s45, s45, 128\nMvaa 100,136,224\nTa 164,12,49152\nMvaa 160,144,224\nTa 166,12,51200\n"
"Mvaa 160,148,228\nD0 2,20,45,0\nMvaa 164,156,228\nTa 168,13,32768\nMvaa 164,152,224\nTa 170,13,34816\nMvaa 172,152,232\nv_max_u32 v48, v48, v35\nMvaa 172,156,236\nTa 172,13,49152\n"
"Mvaa 168,148,236\nTa 174,13,51200\nMvaa 168,144,232\nv_med3_u32 v49, v48, v49, v36\nMvaa 108,136,232\nTa 176,14,32768\nMvaa 108,140,236\nTa 178,14,34816\nMvaa 104,132,236\n"
"D0 3,20,45,1024\nMvaa 104,128,232\nTa 180,14,49152\nMvaa 112,128,240\nTa 182,14,51200\nMvaa 112,132,244\nv_max_u32 v48, v48, v36\nMvaa 116,140,244\nTa 184,15,32768\n"
"Mvaa 116,136,240\nTa 186,15,34816\nMvaa 176,144,240\nv_med3_u32 v49, v48, v49, v37\nMvaa 176,148,244\nTa 188,15,49152\nMvaa 180,156,244\nTa 190,15,51200\nMvaa 180,152,240\n"
"D0 4,20,45,2048\nMvaa 188,152,248\nv_max_u32 v48, v48, v37\nMvaa 188,156,252\nv_med3_u32 v49, v48, v49, v38\nMvaa 184,148,252\nv_max_u32 v48, v48, v38\nMvaa 184,144,248\n"
"v_med3_u32 v49, v48, v49, v39\nMvaa 124,136,248\nv_max_u32 v48, v48, v39\nMvaa 124,140,252\nD0 5,20,45,3072\nMvaa 120,132,252\nv_readfirstlane_b32 s2, v48\nMvaa 120,128,248\n"
"s_waitcnt vmcnt(18) lgkmcnt(0)\ns_barrier\nMvaa 64,128,160\nRa 192,10,49152\nMvaa 64,132,164\ns_add_u32 m0, s47, 32768\ns_add_u32 s44, s44, 0x40000\n"
"buffer_load_dwordx4 v[216:219], v21, s[40:43], s87 offen offset:0\nMvaa 68,140,164\nRa 196,11,49152\nMvaa 68,136,160\nv_readfirstlane_b32 s3, v49\nMvaa 128,144,160\n"
"Ra 200,10,51200\nMvaa 128,148,164\nD0 6,24,44,0\nMvaa 132,156,164\nRa 204,11,51200\nMvaa 132,152,160\ns_andn2_b32 s2, 0x7ff, s2\nMvaa 140,152,168\nRa 208,10,57344\n"
"Mvaa 140,156,172\ns_andn2_b32 s3, 0x7ff, s3\nMvaa 136,148,172\nRa 212,11,57344\nMvaa 136,144,168\ns_lshl_b32 s71, s3, 16\nMvaa 76,136,168\nRa 216,10,59392\nMvaa 76,140,172\n"
"D0 7,24,44,1024\nMvaa 72,132,172\nRa 220,11,59392\nMvaa 72,128,168\ns_or_b32 s71, s71, s2\nMvaa 80,128,176\ns_mul_i32 s2, s2, 896\nMvaa 80,132,180\ns_mul_i32 s3, s3, 896\n"
"Mvaa 84,140,180\ns_mul_i32 s6, s11, 224\nMvaa 84,136,176\ns_add_u32 s2, s2, s6\nMvaa 144,144,176\ns_add_u32 s3, s3, s6\nMvaa 144,148,180\nD0 8,24,44,2048\nMvaa 148,156,180\n"
"buffer_load_dwordx2 v[240:241], v255, s[28:31], s2 offen\nMvaa 148,152,176\nMvaa 156,152,184\nMvaa 156,156,188\nMvaa 152,148,188\n"
"buffer_load_dwordx2 v[242:243], v255, s[28:31], s3 offen\ns_lshl2_add_u32 s2, s10, s11\ns_cmp_eq_u32 s2, 512\ns_cselect_b32 s2, 0, 0x40000000\ns_add_u32 s2, s2, 0xc048000\n"
"Mvaa 152,144,184\nMvaa 92,136,184\nMvaa 92,140,188\nD0 9,24,44,3072\nMvaa 88,132,188\nbuffer_store_dword v25, off, s[16:19], s2 offset:0 sc0 sc1\nMvaa 88,128,184\nWL 0\n"
"Maaa 0,192,160\nRa 128,10,0\nMaaa 0,196,164\ns_add_u32 m0, s47, 49152\ns_add_u32 s44, s44, 0x40000\nMaaa 4,204,164\nRa 132,11,0\nMaaa 4,200,160\nMaaa 64,208,160\nRa 136,10,2048\n"
"Maaa 64,212,164\nD0 6,24,44,0\nMaaa 68,220,164\nRa 140,11,2048\nMaaa 68,216,160\nMaaa 76,216,168\nRa 144,10,8192\nMaaa 76,220,172\nMaaa 72,212,172\nRa 148,11,8192\n"
"Maaa 72,208,168\nMaaa 12,200,168\nRa 152,10,10240\nMaaa 12,204,172\nD0 7,24,44,1024\nMaaa 8,196,172\nRa 156,11,10240\nMaaa 8,192,168\nMaaa 16,192,176\nMaaa 16,196,180\n"
"Maaa 20,204,180\nMaaa 20,200,176\nMaaa 80,208,176\nMaaa 80,212,180\nD0 8,24,44,2048\nMaaa 84,220,180\nMaaa 84,216,176\nMaaa 92,216,184\nMaaa 92,220,188\nMaaa 88,212,188\n"
"Maaa 88,208,184\nMaaa 28,200,184\nMaaa 28,204,188\nD0 9,24,44,3072\nMaaa 24,196,188\nMaaa 24,192,184\ns_waitcnt vmcnt(21) lgkmcnt(0)\ns_barrier\nMaaa 32,192,224\nTa 160,12,0\n"
"Maaa 32,196,228\nTa 162,12,2048\nbuffer_load_dwordx4 v[204:207], v21, s[36:39], s87 offen offset:3584\nMaaa 36,204,228\ns_add_u32 m0, s47, -16384\ns_add_u32 s48, s48, 128\n"
"s_lshl_b32 s5, s73, 8\ns_add_u32 s5, s5, 0xc04ac00\nv_lshrrev_b32 v27, 2, v1\nbuffer_load_dword v27, v27, s[16:19], s5 offen sc0 sc1\nMaaa 36,200,224\nTa 164,12,16384\n"
"Maaa 96,208,224\nTa 166,12,18432\nMaaa 96,212,228\nD0 2,20,48,0\nMaaa 100,220,228\nTa 168,13,0\nMaaa 100,216,224\nTa 170,13,2048\nMaaa 108,216,232\nMaaa 108,220,236\n"
"Ta 172,13,16384\nMaaa 104,212,236\nTa 174,13,18432\nMaaa 104,208,232\nMaaa 44,200,232\nTa 176,14,0\nMaaa 44,204,236\nTa 178,14,2048\nMaaa 40,196,236\nD0 3,20,48,1024\n"
"Maaa 40,192,232\nTa 180,14,16384\nMaaa 48,192,240\nTa 182,14,18432\nMaaa 48,196,244\nMaaa 52,204,244\nTa 184,15,0\nMaaa 52,200,240\nTa 186,15,2048\nMaaa 112,208,240\n"
"Maaa 112,212,244\nTa 188,15,16384\nMaaa 116,220,244\nTa 190,15,18432\nMaaa 116,216,240\nD0 4,20,48,2048\nMaaa 124,216,248\nMaaa 124,220,252\nMaaa 120,212,252\nMaaa 120,208,248\n"
"Maaa 60,200,248\nMaaa 60,204,252\nD0 5,20,48,3072\nMaaa 56,196,252\nMaaa 56,192,248\nWL 0\nMvaa 64,128,160\nTa 224,16,0\nMvaa 64,132,164\nTa 226,16,2048\nMvaa 68,140,164\n"
"s_add_u32 m0, s47, -65536\ns_add_u32 s45, s45, 128\nMvaa 68,136,160\nTa 228,16,16384\nMvaa 128,144,160\nTa 230,16,18432\nMvaa 128,148,164\nD0 2,20,45,0\nMvaa 132,156,164\n"
"Ta 232,17,0\nMvaa 132,152,160\nTa 234,17,2048\nMvaa 140,152,168\nMvaa 140,156,172\nTa 236,17,16384\nMvaa 136,148,172\nTa 238,17,18432\nMvaa 136,144,168\nMvaa 76,136,168\n"
"Ta 240,18,0\nMvaa 76,140,172\nTa 242,18,2048\nMvaa 72,132,172\nD0 3,20,45,1024\nMvaa 72,128,168\nTa 244,18,16384\nMvaa 80,128,176\nTa 246,18,18432\nMvaa 80,132,180\n"
"Mvaa 84,140,180\nTa 248,19,0\nMvaa 84,136,176\nTa 250,19,2048\nMvaa 144,144,176\nMvaa 144,148,180\nTa 252,19,16384\nMvaa 148,156,180\nTa 254,19,18432\nMvaa 148,152,176\n"
"D0 4,20,45,2048\nMvaa 156,152,184\nMvaa 156,156,188\nMvaa 152,148,188\nMvaa 152,144,184\nMvaa 92,136,184\nMvaa 92,140,188\nD0 5,20,45,3072\nMvaa 88,132,188\nMvaa 88,128,184\n"
"s_waitcnt vmcnt(22) lgkmcnt(0)\ns_barrier\nMvaa 96,128,224\nRa 192,10,16384\nWV 16\nMvaa 96,132,228\ns_add_u32 m0, s47, 0\ns_add_u32 s44, s44, 0x40000\n"
"buffer_load_dwordx4 v[220:223], v21, s[40:43], s87 offen offset:3584\nMvaa 100,140,228\nRa 196,11,16384\nv_lshlrev_b32 v244, 16, v240\nMvaa 100,136,224\n"
"v_and_b32 v245, 0xffff0000, v240\nv_lshlrev_b32 v246, 16, v241\nMvaa 160,144,224\nRa 200,10,18432\nv_and_b32 v247, 0xffff0000, v241\nMvaa 160,148,228\nD0 6,24,44,0\n"
"v_lshlrev_b32 v248, 16, v242\nMvaa 164,156,228\n.W560:\ns_cbranch_execz .W564\nRa 204,11,18432\nMvaa 164,152,224\nv_and_b32 v249, 0xffff0000, v242\nv_lshlrev_b32 v250, 16, v243\n"
"Mvaa 172,152,232\nRa 208,10,24576\nv_and_b32 v251, 0xffff0000, v243\nMvaa 172,156,236\nv_lshlrev_b32 v252, 16, v192\nv_and_b32 v253, 0xffff0000, v192\nMvaa 168,148,236\n"
"Ra 212,11,24576\nMU 32,244,252\nMvaa 168,144,232\nMU 33,244,253\nMvaa 108,136,232\nRa 216,10,26624\nMU 40,248,252\nMvaa 108,140,236\nD0 7,24,44,1024\nMU 41,248,253\n"
"Mvaa 104,132,236\nRa 220,11,26624\nv_lshlrev_b32 v252, 16, v193\nMvaa 104,128,232\nv_and_b32 v253, 0xffff0000, v193\nMU 34,244,252\nMvaa 112,128,240\nMU 35,244,253\nMU 42,248,252\n"
"Mvaa 112,132,244\nMU 43,248,253\nMvaa 116,140,244\nv_lshlrev_b32 v252, 16, v194\nv_and_b32 v253, 0xffff0000, v194\nMvaa 116,136,240\nMU 36,244,252\nMU 37,244,253\n"
"Mvaa 176,144,240\nMU 44,248,252\nMU 45,248,253\nMvaa 176,148,244\nD0 8,24,44,2048\nv_lshlrev_b32 v252, 16, v195\nMvaa 180,156,244\nv_and_b32 v253, 0xffff0000, v195\n"
"Mvaa 180,152,240\nMU 38,244,252\nMU 39,244,253\nMvaa 188,152,248\nMU 46,248,252\nMU 47,248,253\nMvaa 188,156,252\nv_lshlrev_b32 v252, 16, v208\nv_and_b32 v253, 0xffff0000, v208\n"
"Mvaa 184,148,252\nMU 48,244,252\nMU 49,244,253\nMvaa 184,144,248\nMU 56,248,252\nMvaa 124,136,248\nMU 57,248,253\nv_lshlrev_b32 v252, 16, v209\nMvaa 124,140,252\nD0 9,24,44,3072\n"
"v_and_b32 v253, 0xffff0000, v209\nMvaa 120,132,252\nMU 50,244,252\nMU 51,244,253\nMvaa 120,128,248\nWL 0\nMaaa 32,192,224\nRa 128,10,32768\nMU 58,248,252\nMaaa 32,196,228\n"
"s_add_u32 m0, s47, 16384\ns_add_u32 s44, s44, 0x40000\nMaaa 36,204,228\nRa 132,11,32768\nMU 59,248,253\nMaaa 36,200,224\nv_lshlrev_b32 v252, 16, v210\n"
"v_and_b32 v253, 0xffff0000, v210\nMaaa 96,208,224\nRa 136,10,34816\nMU 52,244,252\nMaaa 96,212,228\nD0 6,24,44,0\nMU 53,244,253\nMaaa 100,220,228\nRa 140,11,34816\n"
"Maaa 100,216,224\nMU 60,248,252\nMU 61,248,253\nMaaa 108,216,232\nRa 144,10,40960\nv_lshlrev_b32 v252, 16, v211\nMaaa 108,220,236\nv_and_b32 v253, 0xffff0000, v211\nMU 54,244,252\n"
"Maaa 104,212,236\nRa 148,11,40960\nMU 55,244,253\nMaaa 104,208,232\nMU 62,248,252\nMaaa 44,200,232\nRa 152,10,43008\nMU 63,248,253\nMaaa 44,204,236\nD0 7,24,44,1024\n"
"v_lshlrev_b32 v252, 16, v196\nMaaa 40,196,236\nRa 156,11,43008\nv_and_b32 v253, 0xffff0000, v196\nMaaa 40,192,232\nFM 32,245,252\nFM 33,245,253\nMaaa 48,192,240\nFM 40,249,252\n"
"FM 41,249,253\nMaaa 48,196,244\nv_lshlrev_b32 v252, 16, v197\nMaaa 52,204,244\nv_and_b32 v253, 0xffff0000, v197\nFM 34,245,252\nMaaa 52,200,240\nFM 35,245,253\nFM 42,249,252\n"
"Maaa 112,208,240\nFM 43,249,253\nv_lshlrev_b32 v252, 16, v198\nMaaa 112,212,244\nD0 8,24,44,2048\nv_and_b32 v253, 0xffff0000, v198\nMaaa 116,220,244\nFM 36,245,252\n"
"Maaa 116,216,240\nFM 37,245,253\nFM 44,249,252\nMaaa 124,216,248\nFM 45,249,253\nv_lshlrev_b32 v252, 16, v199\nMaaa 124,220,252\nv_and_b32 v253, 0xffff0000, v199\nFM 38,245,252\n"
"Maaa 120,212,252\nFM 39,245,253\nFM 46,249,252\nMaaa 120,208,248\nFM 47,249,253\nMaaa 60,200,248\nv_lshlrev_b32 v252, 16, v212\nv_and_b32 v253, 0xffff0000, v212\nMaaa 60,204,252\n"
"D0 9,24,44,3072\nFM 48,245,252\nMaaa 56,196,252\nFM 49,245,253\nFM 56,249,252\nMaaa 56,192,248\ns_waitcnt vmcnt(19) lgkmcnt(0)\ns_barrier\nMaaa 0,192,160\nTa 224,16,32768\n"
"FM 57,249,253\nMaaa 0,196,164\nTa 226,16,34816\nbuffer_load_dwordx4 v[224:227], v20, s[92:95], s88 offen offset:0\nMaaa 4,204,164\ns_add_u32 m0, s47, -49152\n"
"s_add_u32 s48, s48, 128\nMaaa 4,200,160\nTa 228,16,49152\nv_lshlrev_b32 v252, 16, v213\nMaaa 64,208,160\nTa 230,16,51200\nv_and_b32 v253, 0xffff0000, v213\nMaaa 64,212,164\n"
"D0 2,20,48,0\nFM 50,245,252\nMaaa 68,220,164\nTa 232,17,32768\nMaaa 68,216,160\nTa 234,17,34816\nFM 51,245,253\nMaaa 76,216,168\nFM 58,249,252\nFM 59,249,253\nMaaa 76,220,172\n"
"Ta 236,17,49152\nv_lshlrev_b32 v252, 16, v214\nMaaa 72,212,172\nTa 238,17,51200\nv_and_b32 v253, 0xffff0000, v214\nMaaa 72,208,168\nFM 52,245,252\nMaaa 12,200,168\n"
"Ta 240,18,32768\nFM 53,245,253\nMaaa 12,204,172\nTa 242,18,34816\nFM 60,249,252\nMaaa 8,196,172\nD0 3,20,48,1024\nFM 61,249,253\nMaaa 8,192,168\nTa 244,18,49152\n"
"v_lshlrev_b32 v252, 16, v215\nMaaa 16,192,176\nTa 246,18,51200\nv_and_b32 v253, 0xffff0000, v215\nMaaa 16,196,180\nFM 54,245,252\nMaaa 20,204,180\nTa 248,19,32768\nFM 55,245,253\n"
"Maaa 20,200,176\nTa 250,19,34816\nFM 62,249,252\nMaaa 80,208,176\nFM 63,249,253\nv_lshlrev_b32 v252, 16, v200\nMaaa 80,212,180\nTa 252,19,49152\nv_and_b32 v253, 0xffff0000, v200\n"
"Maaa 84,220,180\nTa 254,19,51200\nMaaa 84,216,176\nD0 4,20,48,2048\nFM 32,246,252\nMaaa 92,216,184\nFM 33,246,253\nFM 40,250,252\nMaaa 92,220,188\nFM 41,250,253\n"
"v_lshlrev_b32 v252, 16, v201\nMaaa 88,212,188\nv_and_b32 v253, 0xffff0000, v201\nFM 34,246,252\nMaaa 88,208,184\nFM 35,246,253\nMaaa 28,200,184\nFM 42,250,252\nFM 43,250,253\n"
"Maaa 28,204,188\nD0 5,20,48,3072\nv_lshlrev_b32 v252, 16, v202\nMaaa 24,196,188\nv_and_b32 v253, 0xffff0000, v202\nFM 36,246,252\nMaaa 24,192,184\nWL 0\nMvaa 96,128,224\n"
"Ta 160,12,32768\nFM 37,246,253\nMvaa 96,132,228\nTa 162,12,34816\nbuffer_load_dwordx4 v[228:231], v20, s[92:95], s88 offen offset:896\nMvaa 100,140,228\ns_add_u32 m0, s47, -32768\n"
"s_add_u32 s45, s45, 128\nMvaa 100,136,224\nTa 164,12,49152\nFM 44,250,252\nMvaa 160,144,224\nTa 166,12,51200\nFM 45,250,253\nMvaa 160,148,228\nD0 2,20,45,0\n"
"v_lshlrev_b32 v252, 16, v203\nMvaa 164,156,228\nTa 168,13,32768\nMvaa 164,152,224\nTa 170,13,34816\nv_and_b32 v253, 0xffff0000, v203\nMvaa 172,152,232\nFM 38,246,252\n"
"FM 39,246,253\nMvaa 172,156,236\nTa 172,13,49152\nFM 46,250,252\nMvaa 168,148,236\nTa 174,13,51200\nFM 47,250,253\nMvaa 168,144,232\nv_lshlrev_b32 v252, 16, v216\n"
"Mvaa 108,136,232\nTa 176,14,32768\nv_and_b32 v253, 0xffff0000, v216\nMvaa 108,140,236\nTa 178,14,34816\nFM 48,246,252\nMvaa 104,132,236\nD0 3,20,45,1024\nFM 49,246,253\n"
"Mvaa 104,128,232\nTa 180,14,49152\nFM 56,250,252\nMvaa 112,128,240\nTa 182,14,51200\nFM 57,250,253\nMvaa 112,132,244\nv_lshlrev_b32 v252, 16, v217\nMvaa 116,140,244\n"
"Ta 184,15,32768\nv_and_b32 v253, 0xffff0000, v217\nMvaa 116,136,240\nTa 186,15,34816\nFM 50,246,252\nMvaa 176,144,240\nFM 51,246,253\nFM 58,250,252\nMvaa 176,148,244\n"
"Ta 188,15,49152\nFM 59,250,253\nMvaa 180,156,244\nTa 190,15,51200\nMvaa 180,152,240\nD0 4,20,45,2048\nv_lshlrev_b32 v252, 16, v218\nMvaa 188,152,248\n"
"v_and_b32 v253, 0xffff0000, v218\nFM 52,246,252\nMvaa 188,156,252\nFM 53,246,253\nFM 60,250,252\nMvaa 184,148,252\nFM 61,250,253\nv_lshlrev_b32 v252, 16, v219\nMvaa 184,144,248\n"
"v_and_b32 v253, 0xffff0000, v219\nMvaa 124,136,248\nFM 54,246,252\nWV 26\nv_xor_b32 v27, s15, v27\nv_and_b32 v255, 0xffffdfff, v27\nv_cmp_ne_u32 vcc, 0, v255\ns_nop 0\n"
"s_cbranch_vccnz .S148\n.S149:\nv_cmp_ne_u32 vcc, 0, v27\ns_nop 3\ns_bcnt1_i32_b64 s4, vcc\ns_cmp_gt_u32 s4, 32\ns_cselect_b32 s4, 1, 0\nMvaa 124,140,252\nD0 5,20,45,3072\n"
"s_xor_b32 s4, s4, s12\nMvaa 120,132,252\ns_and_b32 s4, s4, 1\ns_lshl_b32 s4, s4, 13\nMvaa 120,128,248\ns_waitcnt vmcnt(18) lgkmcnt(0)\ns_barrier\nMvaa 64,128,160\nRa 192,10,49152\n"
"s_mul_i32 s72, s73, 25165824\nMvaa 64,132,164\ns_add_u32 m0, s47, 32768\ns_add_u32 s44, s44, 0x40000\nbuffer_load_dwordx4 v[232:235], v20, s[92:95], s88 offen offset:1792\n"
"Mvaa 68,140,164\nRa 196,11,49152\ns_add_u32 s72, s72, s4\nMvaa 68,136,160\ns_and_b32 s4, s12, 1\ns_lshl_b32 s4, s4, 21\nMvaa 128,144,160\nRa 200,10,51200\ns_add_u32 s72, s72, s4\n"
"Mvaa 128,148,164\nD0 6,24,44,0\ns_lshl3_add_u32 s2, s13, s11\nMvaa 132,156,164\nRa 204,11,51200\nMvaa 132,152,160\ns_lshl_b32 s2, s2, 11\ns_lshl_b32 s3, s14, 22\nMvaa 140,152,168\n"
"Ra 208,10,57344\ns_add_u32 s2, s2, s3\nMvaa 140,156,172\ns_add_u32 s2, s2, s72\ns_add_u32 s49, s2, 0xc00000\nMvaa 136,148,172\nRa 212,11,57344\nFM 55,246,253\nMvaa 136,144,168\n"
"FM 62,250,252\nMvaa 76,136,168\nRa 216,10,59392\nFM 63,250,253\nMvaa 76,140,172\nD0 7,24,44,1024\nv_lshlrev_b32 v252, 16, v204\nMvaa 72,132,172\nRa 220,11,59392\n"
"v_and_b32 v253, 0xffff0000, v204\nMvaa 72,128,168\nFM 32,247,252\nFM 33,247,253\nMvaa 80,128,176\nFM 40,251,252\nFM 41,251,253\nMvaa 80,132,180\nv_lshlrev_b32 v252, 16, v205\n"
"Mvaa 84,140,180\nv_and_b32 v253, 0xffff0000, v205\nFM 34,247,252\nMvaa 84,136,176\nFM 35,247,253\nFM 42,251,252\nMvaa 144,144,176\nFM 43,251,253\nv_lshlrev_b32 v252, 16, v206\n"
"Mvaa 144,148,180\nD0 8,24,44,2048\nv_and_b32 v253, 0xffff0000, v206\nMvaa 148,156,180\nFM 36,247,252\nMvaa 148,152,176\nFM 37,247,253\nFM 44,251,252\nMvaa 156,152,184\n"
"FM 45,251,253\nv_lshlrev_b32 v252, 16, v207\nMvaa 156,156,188\nv_and_b32 v253, 0xffff0000, v207\nFM 38,247,252\nMvaa 152,148,188\nFM 39,247,253\nFM 46,251,252\nMvaa 152,144,184\n"
"FM 47,251,253\nMvaa 92,136,184\nv_lshlrev_b32 v252, 16, v220\nv_and_b32 v253, 0xffff0000, v220\nMvaa 92,140,188\nD0 9,24,44,3072\nFM 48,247,252\nMvaa 88,132,188\nFM 49,247,253\n"
"FM 56,251,252\nMvaa 88,128,184\nWL 0\nMaaa 0,192,160\nRa 128,10,0\nFM 57,251,253\nMaaa 0,196,164\ns_add_u32 m0, s47, 49152\ns_add_u32 s44, s44, 0x40000\n"
"buffer_load_dwordx4 v[236:239], v20, s[92:95], s88 offen offset:2688\nMaaa 4,204,164\nRa 132,11,0\nv_lshlrev_b32 v252, 16, v221\nMaaa 4,200,160\nv_and_b32 v253, 0xffff0000, v221\n"
"FM 50,247,252\nMaaa 64,208,160\nRa 136,10,2048\nFM 51,247,253\nMaaa 64,212,164\nD0 6,24,44,0\nFM 58,251,252\nMaaa 68,220,164\nRa 140,11,2048\nMaaa 68,216,160\nFM 59,251,253\n"
"v_lshlrev_b32 v252, 16, v222\nMaaa 76,216,168\nRa 144,10,8192\nv_and_b32 v253, 0xffff0000, v222\nMaaa 76,220,172\nFM 52,247,252\nFM 53,247,253\nMaaa 72,212,172\nRa 148,11,8192\n"
"FM 60,251,252\nMaaa 72,208,168\nFM 61,251,253\nMaaa 12,200,168\nRa 152,10,10240\nv_lshlrev_b32 v252, 16, v223\nMaaa 12,204,172\nD0 7,24,44,1024\nv_and_b32 v253, 0xffff0000, v223\n"
"Maaa 8,196,172\nRa 156,11,10240\nFM 54,247,252\nMaaa 8,192,168\nFM 55,247,253\nFM 62,251,252\nMaaa 16,192,176\nFM 63,251,253\nMaaa 16,196,180\nv_permlane32_swap_b32 v32, v48\n"
"Maaa 20,204,180\nv_permlane32_swap_b32 v33, v49\nv_permlane32_swap_b32 v34, v50\nMaaa 20,200,176\nv_permlane32_swap_b32 v35, v51\nv_permlane32_swap_b32 v36, v52\nMaaa 80,208,176\n"
"v_permlane32_swap_b32 v37, v53\nv_permlane32_swap_b32 v38, v54\nMaaa 80,212,180\nD0 8,24,44,2048\nv_permlane32_swap_b32 v39, v55\nMaaa 84,220,180\nv_permlane32_swap_b32 v40, v56\n"
"Maaa 84,216,176\nv_permlane32_swap_b32 v41, v57\nv_permlane32_swap_b32 v42, v58\nMaaa 92,216,184\nv_permlane32_swap_b32 v43, v59\nv_permlane32_swap_b32 v44, v60\nMaaa 92,220,188\n"
"v_permlane32_swap_b32 v45, v61\nv_permlane32_swap_b32 v46, v62\nMaaa 88,212,188\nv_permlane32_swap_b32 v47, v63\nAD 32,32,48\nMaaa 88,208,184\nAD 33,33,49\nMaaa 28,200,184\n"
"AD 34,34,50\nAD 35,35,51\nMaaa 28,204,188\nD0 9,24,44,3072\nAD 36,36,52\nMaaa 24,196,188\nAD 37,37,53\nAD 38,38,54\nMaaa 24,192,184\ns_waitcnt vmcnt(20) lgkmcnt(0)\ns_barrier\n"
"Maaa 32,192,224\nTa 160,12,0\nAD 39,39,55\nMaaa 32,196,228\nTa 162,12,2048\nMaaa 36,204,228\ns_add_u32 m0, s47, -16384\ns_add_u32 s48, s48, 128\nMaaa 36,200,224\nTa 164,12,16384\n"
"AD 40,40,56\nMaaa 96,208,224\nTa 166,12,18432\nAD 41,41,57\nMaaa 96,212,228\nD0 2,20,48,0\nAD 42,42,58\nMaaa 100,220,228\nTa 168,13,0\nMaaa 100,216,224\nTa 170,13,2048\n"
"AD 43,43,59\nMaaa 108,216,232\nAD 44,44,60\nAD 45,45,61\nMaaa 108,220,236\nTa 172,13,16384\nAD 46,46,62\nMaaa 104,212,236\nTa 174,13,18432\nAD 47,47,63\nMaaa 104,208,232\n"
"v_permlane16_swap_b32 v32, v40\nMaaa 44,200,232\nTa 176,14,0\nv_permlane16_swap_b32 v33, v41\nMaaa 44,204,236\nTa 178,14,2048\nv_permlane16_swap_b32 v34, v42\nMaaa 40,196,236\n"
"D0 3,20,48,1024\nv_permlane16_swap_b32 v35, v43\nMaaa 40,192,232\nTa 180,14,16384\nv_permlane16_swap_b32 v36, v44\nMaaa 48,192,240\nTa 182,14,18432\n"
"v_permlane16_swap_b32 v37, v45\nMaaa 48,196,244\nv_permlane16_swap_b32 v38, v46\nMaaa 52,204,244\nTa 184,15,0\nv_permlane16_swap_b32 v39, v47\nMaaa 52,200,240\nTa 186,15,2048\n"
"AD 32,32,40\nMaaa 112,208,240\nAD 33,33,41\nAD 34,34,42\nMaaa 112,212,244\nTa 188,15,16384\nAD 35,35,43\nMaaa 116,220,244\nTa 190,15,18432\nMaaa 116,216,240\nD0 4,20,48,2048\n"
"AD 36,36,44\nMaaa 124,216,248\nAD 37,37,45\nAD 38,38,46\nMaaa 124,220,252\nAD 39,39,47\nv_add_f32_dpp v32, v32, v32 row_ror:8 row_mask:0xf bank_mask:0xf\nMaaa 120,212,252\n"
"v_add_f32_dpp v33, v33, v33 row_ror:8 row_mask:0xf bank_mask:0xf\nv_add_f32_dpp v34, v34, v34 row_ror:8 row_mask:0xf bank_mask:0xf\nMaaa 120,208,248\n"
"v_add_f32_dpp v35, v35, v35 row_ror:8 row_mask:0xf bank_mask:0xf\nMaaa 60,200,248\nv_add_f32_dpp v36, v36, v36 row_ror:8 row_mask:0xf bank_mask:0xf\n"
"v_add_f32_dpp v37, v37, v37 row_ror:8 row_mask:0xf bank_mask:0xf\nMaaa 60,204,252\nD0 5,20,48,3072\nv_add_f32_dpp v38, v38, v38 row_ror:8 row_mask:0xf bank_mask:0xf\n"
"Maaa 56,196,252\nv_add_f32_dpp v39, v39, v39 row_ror:8 row_mask:0xf bank_mask:0xf\nv_add_f32_dpp v32, v32, v32 row_ror:4 row_mask:0xf bank_mask:0xf\nMaaa 56,192,248\nWL 0\n"
"Mvaa 64,128,160\nTa 224,16,0\nv_add_f32_dpp v33, v33, v33 row_ror:4 row_mask:0xf bank_mask:0xf\nMvaa 64,132,164\nTa 226,16,2048\nMvaa 68,140,164\ns_add_u32 m0, s47, -65536\n"
"s_add_u32 s45, s45, 128\n.W561:\ns_cbranch_execz .W565\nMvaa 68,136,160\nTa 228,16,16384\nv_add_f32_dpp v34, v34, v34 row_ror:4 row_mask:0xf bank_mask:0xf\nMvaa 128,144,160\n"
"Ta 230,16,18432\nv_add_f32_dpp v35, v35, v35 row_ror:4 row_mask:0xf bank_mask:0xf\nMvaa 128,148,164\nD0 2,20,45,0\n"
"v_add_f32_dpp v36, v36, v36 row_ror:4 row_mask:0xf bank_mask:0xf\nMvaa 132,156,164\nTa 232,17,0\nMvaa 132,152,160\nTa 234,17,2048\n"
"v_add_f32_dpp v37, v37, v37 row_ror:4 row_mask:0xf bank_mask:0xf\nMvaa 140,152,168\nv_add_f32_dpp v38, v38, v38 row_ror:4 row_mask:0xf bank_mask:0xf\n"
"v_add_f32_dpp v39, v39, v39 row_ror:4 row_mask:0xf bank_mask:0xf\nMvaa 140,156,172\nTa 236,17,16384\nv_add_f32_dpp v32, v32, v32 row_ror:2 row_mask:0xf bank_mask:0xf\n"
"Mvaa 136,148,172\nTa 238,17,18432\nv_add_f32_dpp v33, v33, v33 row_ror:2 row_mask:0xf bank_mask:0xf\nMvaa 136,144,168\n"
"v_add_f32_dpp v34, v34, v34 row_ror:2 row_mask:0xf bank_mask:0xf\nMvaa 76,136,168\nTa 240,18,0\nv_add_f32_dpp v35, v35, v35 row_ror:2 row_mask:0xf bank_mask:0xf\nMvaa 76,140,172\n"
"Ta 242,18,2048\nv_add_f32_dpp v36, v36, v36 row_ror:2 row_mask:0xf bank_mask:0xf\nMvaa 72,132,172\nD0 3,20,45,1024\n"
"v_add_f32_dpp v37, v37, v37 row_ror:2 row_mask:0xf bank_mask:0xf\nMvaa 72,128,168\nTa 244,18,16384\nv_add_f32_dpp v38, v38, v38 row_ror:2 row_mask:0xf bank_mask:0xf\n"
"Mvaa 80,128,176\nTa 246,18,18432\nv_add_f32_dpp v39, v39, v39 row_ror:2 row_mask:0xf bank_mask:0xf\nMvaa 80,132,180\nv_lshrrev_b32 v48, 4, v0\nMvaa 84,140,180\nTa 248,19,0\n"
"v_and_b32 v49, 1, v0\nMvaa 84,136,176\nTa 250,19,2048\nv_lshlrev_b32 v49, 5, v49\nMvaa 144,144,176\nv_lshl_add_u32 v49, v48, 6, v49\ns_lshl_b32 s2, s11, 8\nMvaa 144,148,180\n"
"Ta 252,19,16384\ns_add_u32 s2, s2, 132096\nMvaa 148,156,180\nTa 254,19,18432\nMvaa 148,152,176\nD0 4,20,45,2048\nv_add_u32 v49, s2, v49\nMvaa 156,152,184\n"
"ds_write_b128 v49, v[32:35]\nds_write_b128 v49, v[36:39] offset:16\nMvaa 156,156,188\nv_and_b32 v50, 31, v0\nv_lshlrev_b32 v50, 2, v50\nMvaa 152,148,188\ns_lshl_b32 s2, s11, 10\n"
"s_add_u32 s2, s2, 133120\nMvaa 152,144,184\nv_add_u32 v51, s2, v50\nMvaa 92,136,184\nv_add_u32 v50, 132096, v50\nv_lshl_add_u32 v52, v48, 4, s2\nMvaa 92,140,188\nD0 5,20,45,3072\n"
"Mvaa 88,132,188\nMvaa 88,128,184\ns_waitcnt vmcnt(18) lgkmcnt(0)\ns_barrier\nMvaa 96,128,224\nRa 192,10,16384\nds_read_b32 v32, v50 offset:0\nMvaa 96,132,228\n"
"s_add_u32 m0, s47, 0\ns_add_u32 s44, s44, 0x40000\nMvaa 100,140,228\nRa 196,11,16384\nMvaa 100,136,224\nds_read_b32 v33, v50 offset:256\nds_read_b32 v34, v50 offset:512\n"
"Mvaa 160,144,224\nRa 200,10,18432\nMvaa 160,148,228\nD0 6,24,44,0\nMvaa 164,156,228\nRa 204,11,18432\nds_read_b32 v35, v50 offset:768\nMvaa 164,152,224\n"
"ds_read_b32 v36, v50 offset:128\nMvaa 172,152,232\nRa 208,10,24576\nds_read_b32 v37, v50 offset:384\nMvaa 172,156,236\nds_read_b32 v38, v50 offset:640\nMvaa 168,148,236\n"
"Ra 212,11,24576\nMvaa 168,144,232\nds_read_b32 v39, v50 offset:896\nMvaa 108,136,232\nRa 216,10,26624\nMvaa 108,140,236\nD0 7,24,44,1024\nMvaa 104,132,236\nRa 220,11,26624\n"
"Mvaa 104,128,232\nMvaa 112,128,240\nMvaa 112,132,244\nMvaa 116,140,244\nMvaa 116,136,240\nMvaa 176,144,240\nMvaa 176,148,244\nD0 8,24,44,2048\nMvaa 180,156,244\nWL 2\nAD 32,32,33\n"
"Mvaa 180,152,240\nAD 34,34,35\nMvaa 188,152,248\nAD 36,36,37\nAD 38,38,39\nMvaa 188,156,252\nAD 32,32,34\nMvaa 184,148,252\nAD 36,36,38\nMvaa 184,144,248\nCV 32,32,36\n"
"v_and_b32 v36, 0xffff0000, v32\nMvaa 124,136,248\nv_lshlrev_b32 v32, 16, v32\nMvaa 124,140,252\nD0 9,24,44,3072\nMvaa 120,132,252\nv_mul_f32 v33, 0xbfb8aa3b, v32\n"
"Mvaa 120,128,248\nWL 0\nMaaa 32,192,224\nRa 128,10,32768\nMaaa 32,196,228\ns_add_u32 m0, s47, 16384\ns_add_u32 s44, s44, 0x40000\nMaaa 36,204,228\nRa 132,11,32768\nEX 33,33\n"
"Maaa 36,200,224\nv_add_f32 v33, 1.0, v33\nMaaa 96,208,224\nRa 136,10,34816\nv_rcp_f32 v33, v33\nMaaa 96,212,228\nD0 6,24,44,0\nMaaa 100,220,228\nRa 140,11,34816\nMU 33,32,33\n"
"Maaa 100,216,224\nCV 33,33,33\nMaaa 108,216,232\nRa 144,10,40960\nv_lshlrev_b32 v33, 16, v33\nMaaa 108,220,236\nMU 33,33,36\nMaaa 104,212,236\nRa 148,11,40960\nMaaa 104,208,232\n"
"CV 33,33,33\nv_lshlrev_b32 v33, 16, v33\nMaaa 44,200,232\nRa 152,10,43008\nMaaa 44,204,236\nD0 7,24,44,1024\nMaaa 40,196,236\nRa 156,11,43008\nds_write_b32 v51, v33\n"
"Maaa 40,192,232\nRv 244,52,0\nMaaa 48,192,240\nRv 248,52,64\ns_and_b32 s2, s74, 15\nMaaa 48,196,244\ns_sub_u32 s2, s2, 2\ns_cmp_lt_u32 s2, 14\ns_cselect_b32 s5, 0, 0x40000000\n"
"Maaa 52,204,244\ns_and_b32 s2, s2, 15\nMaaa 52,200,240\ns_mul_i32 s4, s73, 14\ns_add_u32 s4, s4, s2\nMaaa 112,208,240\nv_and_b32 v49, 15, v0\nMaaa 112,212,244\nD0 8,24,44,2048\n"
"Maaa 116,220,244\nv_lshlrev_b32 v50, 3, v49\nv_and_b32 v51, 1, v48\nMaaa 116,216,240\nv_lshl_add_u32 v50, v51, 2, v50\nMaaa 124,216,248\ns_mul_i32 s2, s11, 112\n"
"v_add_u32 v50, s2, v50\nMaaa 124,220,252\nv_lshrrev_b32 v51, 4, v50\nMaaa 120,212,252\nv_lshrrev_b32 v48, 1, v48\nMaaa 120,208,248\nv_mad_u32_u24 v51, v48, 28, v51\n"
"v_mul_u32_u24 v51, 7168, v51\nMaaa 60,200,248\nv_and_b32 v50, 15, v50\nMaaa 60,204,252\nD0 9,24,44,3072\nMaaa 56,196,252\nv_lshl_add_u32 v51, v50, 2, v51\n"
"v_cmp_gt_u32 vcc, 14, v49\ns_lshl_b32 s4, s4, 6\ns_add_u32 s4, s4, 0xc04c800\nv_cndmask_b32 v51, v254, v51, vcc\nMaaa 56,192,248\ns_waitcnt vmcnt(16) lgkmcnt(3)\ns_barrier\n"
"Maaa 0,192,160\nTa 224,16,32768\ns_or_b32 s4, s4, s5\nMaaa 0,196,164\nTa 226,16,34816\nMaaa 4,204,164\ns_add_u32 m0, s47, -49152\ns_add_u32 s48, s48, 128\nMaaa 4,200,160\n"
"Ta 228,16,49152\nWL 3\nMaaa 64,208,160\nTa 230,16,51200\nMaaa 64,212,164\nD0 2,20,48,0\nMaaa 68,220,164\nTa 232,17,32768\nv_lshlrev_b32 v252, 16, v224\nMaaa 68,216,160\n"
"Ta 234,17,34816\nMaaa 76,216,168\nv_and_b32 v253, 0xffff0000, v224\nMU 32,244,252\nMaaa 76,220,172\nTa 236,17,49152\nMaaa 72,212,172\nTa 238,17,51200\nMaaa 72,208,168\n"
"MU 33,244,253\nMU 40,248,252\nMaaa 12,200,168\nTa 240,18,32768\nMaaa 12,204,172\nTa 242,18,34816\nMaaa 8,196,172\nD0 3,20,48,1024\nMU 41,248,253\nMaaa 8,192,168\nTa 244,18,49152\n"
"Maaa 16,192,176\nTa 246,18,51200\nv_lshlrev_b32 v252, 16, v225\nMaaa 16,196,180\nv_and_b32 v253, 0xffff0000, v225\nMaaa 20,204,180\nTa 248,19,32768\nMaaa 20,200,176\n"
"Ta 250,19,34816\nMU 34,244,252\nMaaa 80,208,176\nMU 35,244,253\nMaaa 80,212,180\nTa 252,19,49152\nMaaa 84,220,180\nTa 254,19,51200\nMU 42,248,252\nMaaa 84,216,176\n"
"D0 4,20,48,2048\nMaaa 92,216,184\nMU 43,248,253\nv_lshlrev_b32 v252, 16, v226\nMaaa 92,220,188\nv_and_b32 v253, 0xffff0000, v226\nMaaa 88,212,188\nMU 36,244,252\nMaaa 88,208,184\n"
"MU 37,244,253\nMU 44,248,252\nMaaa 28,200,184\nMU 45,248,253\nMaaa 28,204,188\nD0 5,20,48,3072\nMaaa 24,196,188\nv_lshlrev_b32 v252, 16, v227\nv_and_b32 v253, 0xffff0000, v227\n"
"Maaa 24,192,184\nWL 0\nMvaa 96,128,224\nTa 160,12,32768\nMU 38,244,252\nMvaa 96,132,228\nTa 162,12,34816\nMvaa 100,140,228\ns_add_u32 m0, s47, -32768\ns_add_u32 s45, s45, 128\n"
"Mvaa 100,136,224\nTa 164,12,49152\nMU 39,244,253\nMvaa 160,144,224\nTa 166,12,51200\nMvaa 160,148,228\nD0 2,20,45,0\nMvaa 164,156,228\nTa 168,13,32768\nMU 46,248,252\n"
"Mvaa 164,152,224\nTa 170,13,34816\nMvaa 172,152,232\nMU 47,248,253\nv_lshlrev_b32 v252, 16, v228\nMvaa 172,156,236\nTa 172,13,49152\nMvaa 168,148,236\nTa 174,13,51200\n"
"Mvaa 168,144,232\nv_and_b32 v253, 0xffff0000, v228\nFM 32,245,252\nMvaa 108,136,232\nTa 176,14,32768\nMvaa 108,140,236\nTa 178,14,34816\nMvaa 104,132,236\nD0 3,20,45,1024\n"
"FM 33,245,253\nMvaa 104,128,232\nTa 180,14,49152\nMvaa 112,128,240\nTa 182,14,51200\nFM 40,249,252\nMvaa 112,132,244\nFM 41,249,253\nMvaa 116,140,244\nTa 184,15,32768\n"
"Mvaa 116,136,240\nTa 186,15,34816\nv_lshlrev_b32 v252, 16, v229\nMvaa 176,144,240\nv_and_b32 v253, 0xffff0000, v229\nMvaa 176,148,244\nTa 188,15,49152\nMvaa 180,156,244\n"
"Ta 190,15,51200\nFM 34,245,252\nMvaa 180,152,240\nD0 4,20,45,2048\nMvaa 188,152,248\nFM 35,245,253\nFM 42,249,252\nMvaa 188,156,252\nFM 43,249,253\nMvaa 184,148,252\n"
"v_lshlrev_b32 v252, 16, v230\nMvaa 184,144,248\nv_and_b32 v253, 0xffff0000, v230\nFM 36,245,252\nMvaa 124,136,248\nFM 37,245,253\nMvaa 124,140,252\nD0 5,20,45,3072\n"
"Mvaa 120,132,252\nFM 44,249,252\nFM 45,249,253\nMvaa 120,128,248\ns_waitcnt vmcnt(16) lgkmcnt(0)\ns_barrier\nMvaa 64,128,160\nRa 192,10,49152\nv_lshlrev_b32 v252, 16, v231\n"
"Mvaa 64,132,164\ns_add_u32 m0, s47, 32768\ns_add_u32 s44, s44, 0x40000\nMvaa 68,140,164\nRa 196,11,49152\nMvaa 68,136,160\nv_and_b32 v253, 0xffff0000, v231\nFM 38,245,252\n"
"Mvaa 128,144,160\nRa 200,10,51200\nMvaa 128,148,164\nD0 6,24,44,0\nMvaa 132,156,164\nRa 204,11,51200\nFM 39,245,253\nMvaa 132,152,160\nFM 46,249,252\nMvaa 140,152,168\n"
"Ra 208,10,57344\nFM 47,249,253\nMvaa 140,156,172\nv_lshlrev_b32 v252, 16, v232\nMvaa 136,148,172\nRa 212,11,57344\nMvaa 136,144,168\nv_and_b32 v253, 0xffff0000, v232\n"
"FM 32,246,252\nMvaa 76,136,168\nRa 216,10,59392\nMvaa 76,140,172\nD0 7,24,44,1024\nMvaa 72,132,172\nRa 220,11,59392\nFM 33,246,253\nMvaa 72,128,168\nFM 40,250,252\n"
"Mvaa 80,128,176\nFM 41,250,253\nv_lshlrev_b32 v252, 16, v233\nMvaa 80,132,180\nv_and_b32 v253, 0xffff0000, v233\nMvaa 84,140,180\nFM 34,246,252\nMvaa 84,136,176\nFM 35,246,253\n"
"FM 42,250,252\nMvaa 144,144,176\nFM 43,250,253\nMvaa 144,148,180\nD0 8,24,44,2048\nMvaa 148,156,180\nv_lshlrev_b32 v252, 16, v234\nv_and_b32 v253, 0xffff0000, v234\n"
"Mvaa 148,152,176\nFM 36,246,252\nMvaa 156,152,184\nFM 37,246,253\nFM 44,250,252\nMvaa 156,156,188\nFM 45,250,253\nMvaa 152,148,188\nv_lshlrev_b32 v252, 16, v235\nMvaa 152,144,184\n"
"v_and_b32 v253, 0xffff0000, v235\nFM 38,246,252\nMvaa 92,136,184\nFM 39,246,253\nMvaa 92,140,188\nD0 9,24,44,3072\nMvaa 88,132,188\nFM 46,250,252\nFM 47,250,253\nMvaa 88,128,184\n"
"WL 0\nMaaa 0,192,160\nRa 128,10,0\nv_lshlrev_b32 v252, 16, v236\nMaaa 0,196,164\ns_add_u32 m0, s47, 49152\ns_add_u32 s44, s44, 0x40000\nMaaa 4,204,164\nRa 132,11,0\n"
"Maaa 4,200,160\nv_and_b32 v253, 0xffff0000, v236\nFM 32,247,252\nMaaa 64,208,160\nRa 136,10,2048\nMaaa 64,212,164\nD0 6,24,44,0\nMaaa 68,220,164\nRa 140,11,2048\nFM 33,247,253\n"
"Maaa 68,216,160\nFM 40,251,252\nMaaa 76,216,168\nRa 144,10,8192\nFM 41,251,253\nMaaa 76,220,172\nv_lshlrev_b32 v252, 16, v237\nMaaa 72,212,172\nRa 148,11,8192\nMaaa 72,208,168\n"
"v_and_b32 v253, 0xffff0000, v237\nFM 34,247,252\nMaaa 12,200,168\nRa 152,10,10240\nMaaa 12,204,172\nD0 7,24,44,1024\nMaaa 8,196,172\nRa 156,11,10240\nFM 35,247,253\n"
"Maaa 8,192,168\nFM 42,251,252\nMaaa 16,192,176\nFM 43,251,253\nv_lshlrev_b32 v252, 16, v238\nMaaa 16,196,180\nv_and_b32 v253, 0xffff0000, v238\nMaaa 20,204,180\nFM 36,247,252\n"
"Maaa 20,200,176\nFM 37,247,253\nFM 44,251,252\nMaaa 80,208,176\nFM 45,251,253\nMaaa 80,212,180\nD0 8,24,44,2048\nMaaa 84,220,180\nv_lshlrev_b32 v252, 16, v239\n"
"v_and_b32 v253, 0xffff0000, v239\nMaaa 84,216,176\nFM 38,247,252\nMaaa 92,216,184\nFM 39,247,253\nFM 46,251,252\nMaaa 92,220,188\nFM 47,251,253\nMaaa 88,212,188\n"
"v_permlane32_swap_b32 v32, v40\nMaaa 88,208,184\nv_permlane32_swap_b32 v33, v41\nv_permlane32_swap_b32 v34, v42\nMaaa 28,200,184\nv_permlane32_swap_b32 v35, v43\nMaaa 28,204,188\n"
"D0 9,24,44,3072\nMaaa 24,196,188\nv_permlane32_swap_b32 v36, v44\nv_permlane32_swap_b32 v37, v45\nMaaa 24,192,184\nWV 16\ns_barrier\nMaaa 32,192,224\nTa 160,12,0\n"
"v_permlane32_swap_b32 v38, v46\nMaaa 32,196,228\nTa 162,12,2048\nMaaa 36,204,228\ns_add_u32 m0, s47, -16384\ns_add_u32 s48, s48, 128\nMaaa 36,200,224\nTa 164,12,16384\n"
"v_permlane32_swap_b32 v39, v47\nMaaa 96,208,224\nTa 166,12,18432\nMaaa 96,212,228\nD0 2,20,48,0\nMaaa 100,220,228\nTa 168,13,0\nAD 32,32,40\nMaaa 100,216,224\nTa 170,13,2048\n"
"Maaa 108,216,232\nAD 33,33,41\nAD 34,34,42\nMaaa 108,220,236\nTa 172,13,16384\nMaaa 104,212,236\nTa 174,13,18432\nMaaa 104,208,232\nAD 35,35,43\nAD 36,36,44\nMaaa 44,200,232\n"
"Ta 176,14,0\nMaaa 44,204,236\nTa 178,14,2048\nMaaa 40,196,236\nD0 3,20,48,1024\nAD 37,37,45\nMaaa 40,192,232\nTa 180,14,16384\nMaaa 48,192,240\nTa 182,14,18432\nAD 38,38,46\n"
"Maaa 48,196,244\nAD 39,39,47\nMaaa 52,204,244\nTa 184,15,0\nMaaa 52,200,240\nTa 186,15,2048\nv_permlane16_swap_b32 v32, v36\n.W562:\ns_cbranch_execz .W566\nMaaa 112,208,240\n"
"v_permlane16_swap_b32 v33, v37\nMaaa 112,212,244\nTa 188,15,16384\nMaaa 116,220,244\nTa 190,15,18432\nv_permlane16_swap_b32 v34, v38\nMaaa 116,216,240\nD0 4,20,48,2048\n"
"Maaa 124,216,248\nv_permlane16_swap_b32 v35, v39\nAD 32,32,36\nMaaa 124,220,252\nAD 33,33,37\nMaaa 120,212,252\nAD 34,34,38\nMaaa 120,208,248\nAD 35,35,39\nSv 32,51,16,4,0\n"
"Maaa 60,200,248\nMaaa 60,204,252\nD0 5,20,48,3072\nMaaa 56,196,252\nMaaa 56,192,248\nWL 0\nMvaa 64,128,160\nTa 224,16,0\ns_lshr_b32 s3, s11, 1\nMvaa 64,132,164\nTa 226,16,2048\n"
"s_add_u32 s6, s56, 16384\nMvaa 68,140,164\ns_lshl_b32 s3, s3, 13\nMvaa 68,136,160\nTa 228,16,16384\nbuffer_load_dwordx4 v[224:227], v1, s[16:19], s6 offen offset:0 sc1\n"
"Mvaa 128,144,160\nTa 230,16,18432\nMvaa 128,148,164\ns_lshl_b32 s2, s11, 12\nMvaa 132,156,164\nTa 232,17,0\nMvaa 132,152,160\nTa 234,17,2048\nMvaa 140,152,168\n"
"s_add_u32 s2, s2, s3\ns_add_u32 s2, s2, s72\nMvaa 140,156,172\nTa 236,17,16384\nbuffer_load_dwordx4 v[228:231], v1, s[16:19], s6 offen offset:1024 sc1\nMvaa 136,148,172\n"
"Ta 238,17,18432\nMvaa 136,144,168\ns_lshl_b32 s3, s11, 12\nMvaa 76,136,168\nTa 240,18,0\nMvaa 76,140,172\nTa 242,18,2048\nMvaa 72,132,172\ns_add_u32 s89, s3, 0x0\nMvaa 72,128,168\n"
"Ta 244,18,16384\nbuffer_load_dwordx4 v[232:235], v1, s[16:19], s6 offen offset:2048 sc1\nMvaa 80,128,176\nTa 246,18,18432\ns_mov_b32 s88, 0xffff8000\nMvaa 80,132,180\n"
"s_lshl_b32 s4, s13, 1\nMvaa 84,140,180\nTa 248,19,0\nMvaa 84,136,176\nTa 250,19,2048\nMvaa 144,144,176\ns_sub_u32 s79, 15, s4\nMvaa 144,148,180\nTa 252,19,16384\n"
"buffer_load_dwordx4 v[236:239], v1, s[16:19], s6 offen offset:3072 sc1\nMvaa 148,156,180\nTa 254,19,18432\nMvaa 148,152,176\ns_add_u32 s96, s2, 0x1310000\nMvaa 156,152,184\n"
"s_mov_b32 s63, 0x53800000\ns_mov_b32 s64, 0x2b800000\nMvaa 156,156,188\ns_sub_u32 s98, s4, 1\ns_add_u32 s6, s56, 20480\nMvaa 152,148,188\ns_lshl_b32 s5, s13, 5\nMvaa 152,144,184\n"
"s_add_u32 s5, s5, s11\nbuffer_load_dwordx4 v[240:243], v1, s[16:19], s6 offen offset:0 sc1\nMvaa 92,136,184\ns_lshl_b32 s65, s5, 9\nMvaa 92,140,188\ns_add_u32 s65, s65, s72\n"
"Mvaa 88,132,188\ns_add_u32 s65, s65, 0x1580000\nMvaa 88,128,184\ns_waitcnt vmcnt(18) lgkmcnt(0)\ns_barrier\nMvaa 96,128,224\nRa 192,10,16384\ns_and_b32 s66, s72, 0x2000\n"
"Mvaa 96,132,228\ns_add_u32 s66, s66, s56\nbuffer_load_dwordx4 v[244:247], v1, s[16:19], s6 offen offset:1024 sc1\nMvaa 100,140,228\nRa 196,11,16384\nMvaa 100,136,224\n"
"s_add_u32 s66, s66, 0x100000\nMvaa 160,144,224\nRa 200,10,18432\nMvaa 160,148,228\ns_and_b32 s5, s12, 1\nMvaa 164,156,228\nRa 204,11,18432\nMvaa 164,152,224\n"
"s_mul_i32 s58, s5, 0x202000\nbuffer_load_dwordx4 v[248:251], v1, s[16:19], s6 offen offset:2048 sc1\nMvaa 172,152,232\nRa 208,10,24576\ns_xor_b32 s58, s58, s72\nMvaa 172,156,236\n"
"s_add_u32 s58, s58, 0x15c0000\nMvaa 168,148,236\nRa 212,11,24576\nMvaa 168,144,232\ns_lshl_b32 s5, s5, 8\nMvaa 108,136,232\nRa 216,10,26624\nMvaa 108,140,236\n"
"s_add_u32 s61, s58, s5\nbuffer_load_dwordx4 v[252:255], v1, s[16:19], s6 offen offset:3072 sc1\nMvaa 104,132,236\nRa 220,11,26624\nMvaa 104,128,232\ns_lshl4_add_u32 s60, s13, s61\n"
"Mvaa 112,128,240\ns_add_u32 s54, s60, 0\ns_xor_b32 s60, s54, 0x100\nMvaa 112,132,244\ns_cmp_eq_u32 s11, 0\ns_cselect_b32 s54, s54, 0x40000000\ns_add_u32 s6, s56, 0\n"
"Mvaa 116,140,244\ns_getpc_b64 s[8:9]\n.PC168:\ns_add_u32 s8, s8, .HD144-.PC168\ns_addc_u32 s9, s9, 0\nMvaa 116,136,240\ns_mov_b32 s97, 0xff800000\n"
"buffer_load_dwordx4 v[192:195], v1, s[16:19], s6 offen offset:0 sc1\nMvaa 176,144,240\ns_mov_b32 s90, 3\ns_cmp_lt_u32 s11, 2\ns_cselect_b32 s92, 2, 0\nMvaa 176,148,244\n"
"s_add_u32 s86, s2, 0x20000\nMvaa 180,156,244\ns_add_u32 s87, s2, 0x410000\ns_cmp_eq_u32 s13, 7\ns_cselect_b32 s92, s92, 0\nMvaa 180,152,240\ns_cmp_gt_u32 s13, 5\n"
"s_cselect_b32 s4, s12, 15\nMvaa 188,152,248\ns_cmp_lt_u32 s4, 14\ns_cselect_b32 s5, s11, 3\nMvaa 188,156,252\ns_cmp_lt_u32 s5, 2\ns_cselect_b32 s91, 1, 100\n"
"buffer_load_dwordx4 v[196:199], v1, s[16:19], s6 offen offset:1024 sc1\nMvaa 184,148,252\nv_and_b32 v27, 0x70, v1\n"
"buffer_load_dword v27, v27, s[16:19], s61 offen offset:128 sc0 sc1\nMvaa 184,144,248\ns_lshr_b32 s2, s13, 2\ns_bfe_u32 s3, s13, 0x10001\nMvaa 124,136,248\ns_mul_i32 s3, s3, 17\n"
"s_add_u32 s2, s2, s3\nMvaa 124,140,252\ns_lshl2_add_u32 s3, s14, 0\nMvaa 120,132,252\ns_lshl_b32 s3, s3, 2\ns_lshr_b32 s3, 0xe420ca86, s3\nMvaa 120,128,248\nWL 0\nMaaa 32,192,224\n"
"Ra 128,10,32768\ns_and_b32 s3, s3, 15\nbuffer_load_dwordx4 v[200:203], v1, s[16:19], s6 offen offset:2048 sc1\nMaaa 32,196,228\ns_add_u32 s2, s2, s3\ns_cmp_eq_u32 s2, 32\n"
"s_cselect_b32 s4, 16, s2\nMaaa 36,204,228\nRa 132,11,32768\ns_mul_i32 s75, s4, 65536\nMaaa 36,200,224\ns_lshr_b32 s2, s13, 2\ns_bfe_u32 s3, s13, 0x10001\nMaaa 96,208,224\n"
"Ra 136,10,34816\nMaaa 96,212,228\ns_mul_i32 s3, s3, 17\ns_add_u32 s2, s2, s3\nMaaa 100,220,228\nRa 140,11,34816\ns_lshl2_add_u32 s3, s14, 1\n"
"buffer_load_dwordx4 v[204:207], v1, s[16:19], s6 offen offset:3072 sc1\nMaaa 100,216,224\ns_lshl_b32 s3, s3, 2\nMaaa 108,216,232\nRa 144,10,40960\ns_lshr_b32 s3, 0xe420ca86, s3\n"
"Maaa 108,220,236\ns_and_b32 s3, s3, 15\ns_add_u32 s2, s2, s3\nMaaa 104,212,236\nRa 148,11,40960\ns_cmp_eq_u32 s2, 32\ns_cselect_b32 s4, 16, s2\ns_add_u32 s6, s56, 4096\n"
"Maaa 104,208,232\ns_mul_i32 s76, s4, 65536\ns_lshr_b32 s2, s13, 2\nMaaa 44,200,232\nRa 152,10,43008\ns_bfe_u32 s3, s13, 0x10001\n"
"buffer_load_dwordx4 v[208:211], v1, s[16:19], s6 offen offset:0 sc1\nMaaa 44,204,236\ns_mul_i32 s3, s3, 17\nMaaa 40,196,236\nRa 156,11,43008\ns_add_u32 s2, s2, s3\n"
"Maaa 40,192,232\ns_lshl2_add_u32 s3, s14, 2\ns_lshl_b32 s3, s3, 2\nMaaa 48,192,240\ns_lshr_b32 s3, 0xe420ca86, s3\ns_and_b32 s3, s3, 15\nMaaa 48,196,244\ns_add_u32 s2, s2, s3\n"
"s_cmp_eq_u32 s2, 32\ns_cselect_b32 s4, 16, s2\nMaaa 52,204,244\ns_mul_i32 s77, s4, 65536\ns_lshr_b32 s2, s13, 2\n"
"buffer_load_dwordx4 v[212:215], v1, s[16:19], s6 offen offset:1024 sc1\nMaaa 52,200,240\ns_bfe_u32 s3, s13, 0x10001\ns_mul_i32 s3, s3, 17\nMaaa 112,208,240\ns_add_u32 s2, s2, s3\n"
"Maaa 112,212,244\ns_lshl2_add_u32 s3, s14, 3\ns_lshl_b32 s3, s3, 2\nMaaa 116,220,244\ns_lshr_b32 s3, 0xe420ca86, s3\ns_and_b32 s3, s3, 15\nMaaa 116,216,240\ns_add_u32 s2, s2, s3\n"
"s_cmp_eq_u32 s2, 32\ns_cselect_b32 s4, 16, s2\nMaaa 124,216,248\ns_mul_i32 s78, s4, 65536\ns_lshr_b32 s3, s11, 1\n"
"buffer_load_dwordx4 v[216:219], v1, s[16:19], s6 offen offset:2048 sc1\nMaaa 124,220,252\ns_lshl_b32 s3, s3, 15\ns_add_u32 s2, s72, s3\nMaaa 120,212,252\ns_and_b32 s3, s11, 1\n"
"Maaa 120,208,248\ns_lshl_b32 s3, s3, 14\ns_add_u32 s2, s2, s3\nMaaa 60,200,248\ns_add_u32 s2, s2, 0x400000\ns_mov_b32 s55, 4096\nMaaa 60,204,252\ns_add_u32 s75, s75, s2\n"
"Maaa 56,196,252\ns_add_u32 s76, s76, s2\ns_add_u32 s77, s77, s2\nbuffer_load_dwordx4 v[220:223], v1, s[16:19], s6 offen offset:3072 sc1\nMaaa 56,192,248\nWV 22\ns_barrier\n"
"Maaa 0,192,160\nTa 224,16,32768\ns_add_u32 s78, s78, s2\nMaaa 0,196,164\nTa 226,16,34816\nMaaa 4,204,164\nMaaa 4,200,160\nTa 228,16,49152\nMaaa 64,208,160\nTa 230,16,51200\n"
"Maaa 64,212,164\nMaaa 68,220,164\nTa 232,17,32768\nMaaa 68,216,160\nTa 234,17,34816\nMaaa 76,216,168\nWV 6\nv_cmp_ne_u32 vcc, s15, v27\ns_nop 0\ns_cbranch_vccnz .S169\n.S170:\n"
"s_sub_u32 s45, s86, 131072\nMaaa 76,220,172\nTa 236,17,49152\ns_add_u32 m0, s47, 0\ns_add_u32 s46, s45, 0x0\nMaaa 72,212,172\nTa 238,17,51200\nMaaa 72,208,168\nD1 1,16,46,0\n"
"s_mov_b32 s36, s68\nMaaa 12,200,168\nTa 240,18,32768\nMaaa 12,204,172\nTa 242,18,34816\nMaaa 8,196,172\ns_and_b32 s37, s69, 0xffff\nMaaa 8,192,168\nTa 244,18,49152\n"
"Maaa 16,192,176\nTa 246,18,51200\nD1 1,16,46,1024\nMaaa 16,196,180\ns_mov_b32 s38, 0x1c0000\nMaaa 20,204,180\nTa 248,19,32768\nMaaa 20,200,176\nTa 250,19,34816\nMaaa 80,208,176\n"
"s_mov_b32 s39, 0x20000\nMaaa 80,212,180\nTa 252,19,49152\nD1 1,16,46,2048\nMaaa 84,220,180\nTa 254,19,51200\ns_and_b32 s2, s74, 15\nMaaa 84,216,176\ns_max_u32 s2, s2, 2\n"
"Maaa 92,216,184\ns_sub_u32 s2, s2, 2\nMaaa 92,220,188\ns_mul_i32 s3, s73, 14\nMaaa 88,212,188\nD1 1,16,46,3072\nMaaa 88,208,184\ns_add_u32 s2, s2, s3\ns_lshl2_add_u32 s2, s2, s11\n"
"Maaa 28,200,184\ns_mul_i32 s3, s2, 4682\nMaaa 28,204,188\ns_lshr_b32 s3, s3, 16\nMaaa 24,196,188\ns_mul_i32 s22, s3, 14\nMaaa 24,192,184\nWL 0\nMvaa 96,128,224\nTa 160,12,32768\n"
"s_add_u32 m0, s47, -65536\ns_add_u32 s46, s45, 0x10000\nD1 1,16,46,0\nMvaa 96,132,228\nTa 162,12,34816\nMvaa 100,140,228\ns_sub_u32 s22, s2, s22\nMvaa 100,136,224\n"
"Ta 164,12,49152\nMvaa 108,136,232\nTa 166,12,51200\nMvaa 108,140,236\nD1 1,16,46,1024\nMvaa 104,132,236\nTa 168,13,32768\ns_and_b32 s21, s3, 28\nMvaa 104,128,232\nTa 170,13,34816\n"
"Mvaa 112,128,240\ns_add_u32 s21, s21, s3\nMvaa 112,132,244\nTa 172,13,49152\nMvaa 116,140,244\nTa 174,13,51200\nD1 1,16,46,2048\nMvaa 116,136,240\ns_lshl_b32 s21, s21, 4\n"
"s_add_u32 s21, s21, 1024\nMvaa 124,136,248\nTa 176,14,32768\nCV 48,96,97\nMvaa 124,140,252\nTa 178,14,34816\nMvaa 120,132,252\ns_xor_b32 s3, s3, 28\nCV 49,98,99\nMvaa 120,128,248\n"
"Ta 180,14,49152\nD1 1,16,46,3072\nMvaa 160,144,224\nTa 182,14,51200\ns_bfe_u32 s41, s3, 0x30002\nMvaa 160,148,228\ns_cmp_gt_u32 s22, 6\ns_cselect_b32 s2, 1179648, 0\n"
"s_cselect_b32 s1, 16, 0\nMvaa 164,156,228\nTa 184,15,32768\nMvaa 164,152,224\nTa 186,15,34816\nCV 50,100,101\nMvaa 172,152,232\ns_add_u32 s41, s41, s1\nMvaa 172,156,236\n"
"Ta 188,15,49152\nCV 51,102,103\nMvaa 168,148,236\nTa 190,15,51200\ns_lshl4_add_u32 s41, s41, 0xc048400\nMvaa 168,144,232\ns_and_b32 s1, s3, 28\nCV 52,104,105\nMvaa 176,144,240\n"
"s_add_u32 s3, s3, s1\nMvaa 176,148,244\ns_lshl_b32 s3, s3, 11\nMvaa 180,156,244\ns_add_u32 s2, s2, s3\nCV 53,106,107\nMvaa 180,152,240\ns_lshl_b32 s3, s22, 17\n"
"s_add_u32 s2, s2, s3\nMvaa 188,152,248\ns_add_u32 s20, s2, 0xce0000\nCV 54,108,109\nMvaa 188,156,252\ns_lshl_b32 s2, s22, 7\nMvaa 184,148,252\ns_add_u32 s40, s2, 0xc04b800\n"
"CV 55,110,111\nMvaa 184,144,248\nWL 0\ns_barrier\nMvaa 64,128,160\nRa 192,10,49152\ns_add_u32 m0, s47, 32768\ns_add_u32 s46, s45, 0x8000\nMvaa 64,132,164\ns_mul_i32 s2, s21, 896\n"
"Mvaa 68,140,164\nRa 196,11,49152\nD1 1,16,46,0\nMvaa 68,136,160\ns_lshl_b32 s3, s22, 6\nCV 56,112,113\nMvaa 76,136,168\nRa 200,10,51200\nMvaa 76,140,172\ns_add_u32 s22, s2, s3\n"
"CV 57,114,115\nMvaa 72,132,172\nRa 204,11,51200\ns_add_u32 s23, s22, 57344\nMvaa 72,128,168\nD1 1,16,46,1024\nCV 58,116,117\nMvaa 80,128,176\nRa 208,10,57344\nMvaa 80,132,180\n"
"CV 59,118,119\nMvaa 84,140,180\nRa 212,11,57344\nCV 60,120,121\nMvaa 84,136,176\nCV 61,122,123\nMvaa 92,136,184\nRa 216,10,59392\nD1 1,16,46,2048\nMvaa 92,140,188\nCV 62,124,125\n"
"Mvaa 88,132,188\nRa 220,11,59392\nCV 63,126,127\nMvaa 88,128,184\nCV 32,64,65\nMvaa 128,144,160\nCV 33,66,67\nCV 34,68,69\nMvaa 128,148,164\nD1 1,16,46,3072\nMvaa 132,156,164\n"
"CV 35,70,71\nMvaa 132,152,160\nCV 36,72,73\nCV 37,74,75\nMvaa 140,152,168\ns_add_u32 m0, s47, 49152\ns_add_u32 s46, s45, 0x400000\nMvaa 140,156,172\nCV 38,76,77\nCV 39,78,79\n"
"Mvaa 136,148,172\nD1 1,16,46,0\nMvaa 136,144,168\nCV 40,80,81\nCV 41,82,83\nMvaa 144,144,176\nCV 42,84,85\nMvaa 144,148,180\nCV 43,86,87\nMvaa 148,156,180\nCV 44,88,89\n"
"CV 45,90,91\nMvaa 148,152,176\nD1 1,16,46,1024\nMvaa 156,152,184\nCV 46,92,93\nCV 47,94,95\nMvaa 156,156,188\nCV 112,160,161\nMvaa 152,148,188\nCV 113,162,163\nCV 114,164,165\n"
"Mvaa 152,144,184\nWL 0\nMaaa 0,192,160\nD1 1,16,46,2048\nSv 32,1,16,75,0\nMaaa 0,196,164\nCV 115,166,167\nMaaa 4,204,164\nCV 116,168,169\nCV 117,170,171\nMaaa 4,200,160\n"
"CV 118,172,173\nCV 119,174,175\nMaaa 12,200,168\nCV 120,176,177\nCV 121,178,179\nMaaa 12,204,172\nD1 1,16,46,3072\nMaaa 8,196,172\nCV 122,180,181\nCV 123,182,183\nMaaa 8,192,168\n"
"CV 124,184,185\nCV 125,186,187\nMaaa 16,192,176\ns_add_u32 m0, s47, -49152\ns_add_u32 s46, s45, 0x408000\nSv 36,1,16,75,1024\nMaaa 16,196,180\nCV 126,188,189\nMaaa 20,204,180\n"
"D1 1,16,46,0\nCV 127,190,191\nMaaa 20,200,176\nCV 96,128,129\nCV 97,130,131\nMaaa 28,200,184\nCV 98,132,133\nCV 99,134,135\nMaaa 28,204,188\nCV 100,136,137\nMaaa 24,196,188\n"
"CV 101,138,139\nCV 102,140,141\nMaaa 24,192,184\nD1 1,16,46,1024\nCV 103,142,143\nMaaa 64,208,160\nSv 40,1,16,75,2048\n.W563:\ns_cbranch_execz .W567\nCV 104,144,145\n"
"Maaa 64,212,164\nCV 105,146,147\nMaaa 68,220,164\nCV 106,148,149\nCV 107,150,151\nMaaa 68,216,160\nCV 108,152,153\nCV 109,154,155\nMaaa 76,216,168\nD1 1,16,46,2048\n"
"CV 110,156,157\nMaaa 76,220,172\nCV 111,158,159\nMaaa 72,212,172\nv_add_u32 v2, 0x10000, v1\ns_lshr_b32 s2, s59, 1\nMaaa 72,208,168\nv_add_u32 v24, s2, v2\n"
"v_xor_b32 v26, 0x1000, v24\nMaaa 80,208,176\nSv 44,1,16,75,3072\nv_lshrrev_b32 v20, 4, v0\nMaaa 80,212,180\nD1 1,16,46,3072\nMaaa 84,220,180\nv_lshlrev_b32 v20, 2, v20\n"
"v_and_b32 v0, 15, v0\nMaaa 84,216,176\nv_sub_u32 v0, v0, v20\ns_lshl_b32 s2, s11, 4\nMaaa 92,216,184\nv_add_u32 v3, s2, v0\nv_subrev_u32 v0, 32, v3\nMaaa 92,220,188\n"
"v_mov_b32 v4, 0\nMaaa 88,212,188\nv_mov_b32 v5, 0\nv_mov_b32 v6, 0\nMaaa 88,208,184\nWV 20\ns_barrier\nMaaa 32,192,224\ns_add_u32 s75, s75, s55\nSv 48,1,16,75,0\nMaaa 32,196,228\n"
"v_mov_b32 v7, 0\nMaaa 36,204,228\nv_mov_b32 v8, 0\nv_mov_b32 v9, 0\nMaaa 36,200,224\nv_mov_b32 v10, 0\nv_mov_b32 v11, 0\nMaaa 44,200,232\nv_mov_b32 v12, 0\nv_mov_b32 v13, 0\n"
"Maaa 44,204,236\nv_mov_b32 v14, 0\nMaaa 40,196,236\nv_mov_b32 v15, 0\nv_mov_b32 v16, 0\nMaaa 40,192,232\nv_mov_b32 v17, 0\nv_mov_b32 v18, 0\nMaaa 48,192,240\nSv 52,1,16,75,1024\n"
"v_mov_b32 v19, 0\nMaaa 48,196,244\nv_mov_b32 v86, 0\nMaaa 52,204,244\nv_mov_b32 v87, 0\nv_mov_b32 v88, 0\nMaaa 52,200,240\nv_mov_b32 v89, 0\nAR 92,0\nAR 93,1\nMaaa 60,200,248\n"
"CV 128,92,93\nAR 92,2\nAR 93,3\nMaaa 60,204,252\nCV 129,92,93\nAR 92,4\nAR 93,5\nMaaa 56,196,252\nRa 128,24,0\nCV 130,92,93\nAR 92,6\nMaaa 56,192,248\nAR 93,7\nCV 131,92,93\n"
"AR 92,8\nMaaa 96,208,224\nSv 56,1,16,75,2048\nRa 132,24,1024\nAR 93,9\nMaaa 96,212,228\nCV 132,92,93\nAR 92,10\nAR 93,11\nMaaa 100,220,228\nRa 136,24,2048\nCV 133,92,93\nAR 92,12\n"
"Maaa 100,216,224\nAR 93,13\nCV 134,92,93\nAR 92,14\nMaaa 108,216,232\nRa 140,24,3072\nAR 93,15\nCV 135,92,93\nMaaa 108,220,236\nAR 92,16\nAR 93,17\nCV 136,92,93\nMaaa 104,212,236\n"
"Ra 144,26,0\nAR 92,18\nAR 93,19\nMaaa 104,208,232\nCV 137,92,93\nAR 92,20\nAR 93,21\nMaaa 112,208,240\nSv 60,1,16,75,3072\nRa 148,26,1024\nCV 138,92,93\nMaaa 112,212,244\n"
"AR 92,22\nAR 93,23\nCV 139,92,93\nMaaa 116,220,244\nRa 152,26,2048\nAR 92,24\nAR 93,25\nMaaa 116,216,240\nCV 140,92,93\nAR 92,26\nAR 93,27\nMaaa 124,216,248\nRa 156,26,3072\n"
"CV 141,92,93\nAR 92,28\nMaaa 124,220,252\nAR 93,29\nCV 142,92,93\nAR 92,30\nMaaa 120,212,252\nAR 93,31\nCV 143,92,93\nAR 92,32\nMaaa 120,208,248\nWL 4\nCvav 32,128,192,4\n"
"Ra 160,24,8192\nAR 93,33\nCV 144,92,93\nMvav 32,132,200\nAR 92,34\nAR 93,35\nCV 145,92,93\nMvav 32,136,208\nRa 164,24,9216\nAR 92,36\nAR 93,37\nMvav 32,140,216\nCV 146,92,93\n"
"AR 92,38\nAR 93,39\nCvav 36,140,220,8\nRa 168,24,10240\nCV 147,92,93\nAR 92,40\nMvav 36,136,212\ns_add_u32 m0, s89, 32768\nAR 93,41\nCV 148,92,93\nMvav 36,132,204\n"
"Ra 172,24,11264\nWL 4\ns_barrier\nMvav 36,128,196\nAR 92,42\nAR 93,43\nCV 149,92,93\nMvav 36,144,228\nRa 176,26,8192\nAR 92,44\nAR 93,45\nSv 96,1,16,76,0\nMvav 36,148,236\n"
"s_add_u32 s46, s86, s88\nCV 150,92,93\nAR 92,46\nMvav 36,152,244\nRa 180,26,9216\nAR 93,47\nCV 151,92,93\nMvav 36,156,252\nAR 92,48\nAR 93,49\nCV 152,92,93\nMvav 32,156,248\n"
"Ra 184,26,10240\nAR 92,50\nAR 93,51\nMvav 32,152,240\nD1 1,16,46,0\nCV 153,92,93\nAR 92,52\nMvav 32,148,232\nRa 188,26,11264\nWL 4\nAR 93,53\nMvav 32,144,224\nCV 154,92,93\n"
"AR 92,54\nAR 93,55\nCvav 40,160,192,12\nCV 155,92,93\nAR 92,56\nAR 93,57\nMvav 40,164,200\nCV 156,92,93\nAR 92,58\nAR 93,59\nMvav 40,168,208\nCV 157,92,93\nAR 92,60\nAR 93,61\n"
"v_and_b32 v27, 0x70, v1\nbuffer_load_dword v27, v27, s[16:19], s61 offen offset:136 sc0 sc1\nMvav 40,172,216\nCV 158,92,93\nAR 92,62\nAR 93,63\nCvav 44,172,220,16\nCV 159,92,93\n"
"AR 92,64\nAR 93,65\nMvav 44,168,212\nD1 1,16,46,1024\nCV 160,92,93\nAR 92,66\nMvav 44,164,204\nWL 0\nAR 93,67\nCV 161,92,93\nMvav 44,160,196\nAR 92,68\nAR 93,69\nCV 162,92,93\n"
"Mvav 44,176,228\nAR 92,70\nAR 93,71\nCV 163,92,93\nSv 100,1,16,76,1024\nMvav 44,180,236\ns_add_u32 s88, s88, 32768\nEX 80,32\nMvav 44,184,244\nAR 92,72\nAR 93,73\nCV 164,92,93\n"
"Mvav 44,188,252\nEX 81,33\nMvav 40,188,248\nAR 92,74\nAR 93,75\nCV 165,92,93\nMvav 40,184,240\nD1 1,16,46,2048\nAR 92,76\nAR 93,77\nMvav 40,180,232\nCV 166,92,93\nAR 92,78\n"
"AR 93,79\nMvav 40,176,224\nAD 84,80,81\nCV 64,80,81\nCV 167,92,93\nAR 92,80\nAR 93,81\nCV 168,92,93\nEX 82,34\nAR 92,82\nAR 93,83\nCV 169,92,93\nAR 92,84\nWV 17\nEX 83,35\n"
"AD 84,84,82\nAR 93,85\nCV 170,92,93\nD1 1,16,46,3072\ns_add_u32 m0, s89, 49152\nAR 92,86\ns_barrier\nAR 93,87\nCV 171,92,93\nAD 84,84,83\nCV 65,82,83\nAR 92,88\nRa 128,24,32768\n"
"AR 93,89\nCV 172,92,93\nSv 104,1,16,76,2048\ns_add_u32 s46, s87, s88\nEX 80,36\nRa 132,24,33792\nAR 92,90\nAR 93,91\nEX 81,37\nRa 136,24,34816\nCV 173,92,93\nAR 92,92\n"
"D1 1,16,46,0\nAR 93,93\nCV 174,92,93\nRa 140,24,35840\nAR 92,94\nAR 93,95\nAD 85,80,81\nCV 68,80,81\nCV 175,92,93\nRa 144,26,32768\nAR 92,96\nAR 93,97\nEX 82,38\nCV 176,92,93\n"
"Ra 148,26,33792\nAR 92,98\nAR 93,99\nEX 83,39\nRa 152,26,34816\nAD 85,85,82\nCV 177,92,93\nD1 1,16,46,1024\nAR 92,100\nAR 93,101\nRa 156,26,35840\nCV 178,92,93\nAR 92,102\n"
"AD 85,85,83\nCV 69,82,83\nAR 93,103\nRa 160,24,40960\nCV 179,92,93\nAR 92,104\nSv 108,1,16,76,3072\ns_xor_b32 s89, s89, 0x10000\nEX 80,40\nRa 164,24,41984\nAR 93,105\n"
"CV 180,92,93\nWL 6\nEX 81,41\nRa 168,24,43008\nAD 84,84,80\nAR 92,106\nD1 1,16,46,2048\nAR 93,107\nCV 181,92,93\nRa 172,24,44032\nAR 92,108\nAR 93,109\nAD 84,84,81\nCV 66,80,81\n"
"CV 182,92,93\nCvav 48,128,192,4\nRa 176,26,40960\nAR 92,110\nAR 93,111\nMvav 48,132,200\nEX 82,42\nCV 183,92,93\nMvav 48,136,208\nRa 180,26,41984\nAR 92,112\nAR 93,113\n"
"Mvav 48,140,216\nWL 6\nEX 83,43\nWV 9\nv_cmp_ne_u32 vcc, s15, v27\ns_nop 0\ns_cbranch_vccnz .S172\n.S173:\nCvav 52,140,220,8\nRa 184,26,43008\nAD 84,84,82\nCV 184,92,93\n"
"Mvav 52,136,212\nD1 1,16,46,3072\ns_add_u32 m0, s89, 0\nAR 92,114\nMvav 52,132,204\nRa 188,26,44032\nAR 93,115\nCV 185,92,93\nMvav 52,128,196\nAD 84,84,83\nCV 67,82,83\nAR 92,116\n"
"Mvav 52,144,228\nRa 128,2,49152\nAR 93,117\nCV 186,92,93\ns_add_u32 s76, s76, s55\nSv 112,1,16,76,0\nMvav 52,148,236\ns_add_u32 s46, s86, s88\nEX 80,44\nMvav 52,152,244\n"
"Ra 132,2,50176\nv_xor_b32 v24, 0x10000, v24\nAR 92,118\nMvav 52,156,252\nWL 6\nEX 81,45\nMvav 48,156,248\nRa 136,2,51200\nAD 85,85,80\nAR 93,119\nMvav 48,152,240\nD1 1,16,46,0\n"
"CV 187,92,93\nAR 92,120\nMvav 48,148,232\nRa 140,2,52224\nAR 93,121\nCV 188,92,93\nMvav 48,144,224\nAD 85,85,81\nCV 70,80,81\nAR 92,122\nCvav 56,160,192,12\nRa 144,2,53248\n"
"AR 93,123\nCV 189,92,93\nMvav 56,164,200\nEX 82,46\nAR 92,124\nMvav 56,168,208\nRa 148,2,54272\nv_xor_b32 v26, 0x10000, v26\nAR 93,125\nMvav 56,172,216\nWL 6\nEX 83,47\n"
"Cvav 60,172,220,16\nRa 152,2,55296\nAD 85,85,82\nCV 190,92,93\nMvav 60,168,212\nD1 1,16,46,1024\nAR 92,126\nAR 93,127\nMvav 60,164,204\nRa 156,2,56320\nCV 191,92,93\n"
"Mvav 60,160,196\nAD 85,85,83\nCV 71,82,83\nMvav 60,176,228\nRa 160,2,57344\nv_max_f32 v90, v84, v85\nSv 116,1,16,76,1024\nMvav 60,180,236\ns_add_u32 s88, s88, 32768\nEX 80,48\n"
"Mvav 60,184,244\nRa 164,2,58368\nv_cmp_lt_f32 vcc, s63, v90\nMvav 60,188,252\nWL 8\nEX 81,49\nMvav 56,188,248\nRa 168,2,59392\ns_cbranch_vccnz .T154\n.T155:\nv_mov_b32 v20, v84\n"
"v_mov_b32 v21, v85\nv_mov_b32 v22, v20\nv_mov_b32 v23, v21\nMvav 56,184,240\nD1 1,16,46,2048\nv_permlane32_swap_b32 v20, v22\nv_permlane32_swap_b32 v21, v23\nAD 20,20,22\n"
"AD 21,21,23\nv_mov_b32 v22, v20\nv_mov_b32 v23, v21\nMvav 56,180,232\nRa 172,2,60416\nv_permlane16_swap_b32 v20, v22\nv_permlane16_swap_b32 v21, v23\nAD 20,20,22\nAD 21,21,23\n"
"v_min_f32 v90, v20, v21\nv_cmp_gt_f32 vcc, s64, v90\ns_nop 0\ns_cbranch_vccnz .T156\n.T157:\nAD 86,86,84\nMvav 56,176,224\nAD 84,80,81\nCV 72,80,81\nZaav 0,128,64\nRa 176,2,61440\n"
"Zaav 4,128,68\nEX 82,50\nZaav 12,132,68\nRa 180,2,62464\nZaav 8,132,64\nWL 8\nEX 83,51\nZaav 16,136,64\nRa 184,2,63488\nAD 84,84,82\nZaav 20,136,68\nD1 1,16,46,3072\n"
"s_add_u32 m0, s89, 16384\nZaav 28,140,68\nRa 188,2,64512\ns_barrier\nZaav 24,140,64\nAD 84,84,83\nCV 73,82,83\nZaav 32,144,64\nRa 128,24,0\nSv 120,1,16,76,2048\nZaav 36,144,68\n"
"s_add_u32 s46, s87, s88\nEX 80,52\nbuffer_store_dword v25, off, s[16:19], s54 offset:12 sc0 sc1\nZaav 44,148,68\nRa 132,24,1024\nv_xor_b32 v2, 0x10000, v2\nZaav 40,148,64\nWL 8\n"
"EX 81,53\nZaav 48,152,64\nRa 136,24,2048\nAD 87,87,85\nZaav 52,152,68\nD1 1,16,46,0\nZaav 60,156,68\nRa 140,24,3072\nZaav 56,156,64\nAD 85,80,81\nCV 76,80,81\nZaav 64,160,64\n"
"Ra 144,26,0\nZaav 68,160,68\nEX 82,54\nZaav 76,164,68\nRa 148,26,1024\nZaav 72,164,64\nWL 8\nEX 83,55\nZaav 80,168,64\nRa 152,26,2048\nAD 85,85,82\nZaav 84,168,68\n"
"D1 1,16,46,1024\nZaav 92,172,68\nRa 156,26,3072\nZaav 88,172,64\nAD 85,85,83\nCV 77,82,83\nZaav 96,176,64\nRa 160,24,8192\nSv 124,1,16,76,3072\nZaav 100,176,68\n.W564:\n"
"s_cbranch_execz .W568\nEX 80,56\nZaav 108,180,68\nRa 164,24,9216\nZaav 104,180,64\nWL 6\nEX 81,57\nZaav 112,184,64\nRa 168,24,10240\nAD 84,84,80\nZaav 116,184,68\nD1 1,16,46,2048\n"
"Zaav 124,188,68\nRa 172,24,11264\nZaav 120,188,64\nAD 84,84,81\nCV 74,80,81\nCvav 32,128,192,4\nRa 176,26,8192\nMvav 32,132,200\nEX 82,58\nMvav 32,136,208\nRa 180,26,9216\n"
"Mvav 32,140,216\nWL 6\nEX 83,59\nCvav 36,140,220,8\nRa 184,26,10240\nAD 84,84,82\nMvav 36,136,212\nD1 1,16,46,3072\ns_add_u32 m0, s89, 32768\nMvav 36,132,204\nRa 188,26,11264\n"
"Mvav 36,128,196\nAD 84,84,83\nCV 75,82,83\nMvav 36,144,228\nRa 128,2,16384\nSv 128,1,16,77,0\nMvav 36,148,236\ns_add_u32 s46, s86, s88\nEX 80,60\nMvav 36,152,244\nRa 132,2,17408\n"
"Mvav 36,156,252\nWL 6\nEX 81,61\nMvav 32,156,248\nRa 136,2,18432\nAD 85,85,80\nMvav 32,152,240\nD1 1,16,46,0\nMvav 32,148,232\nRa 140,2,19456\nMvav 32,144,224\nAD 85,85,81\n"
"CV 78,80,81\nCvav 40,160,192,12\nRa 144,2,20480\nMvav 40,164,200\nEX 82,62\nMvav 40,168,208\nRa 148,2,21504\nMvav 40,172,216\nWL 6\nEX 83,63\nCvav 44,172,220,16\nRa 152,2,22528\n"
"AD 85,85,82\nMvav 44,168,212\nD1 1,16,46,1024\nMvav 44,164,204\nRa 156,2,23552\nMvav 44,160,196\nAD 85,85,83\nCV 79,82,83\nMvav 44,176,228\nRa 160,2,24576\n"
"v_max_f32 v90, v84, v85\nSv 132,1,16,77,1024\nMvav 44,180,236\ns_add_u32 s88, s88, 32768\nEX 80,32\nMvav 44,184,244\nRa 164,2,25600\nv_cmp_lt_f32 vcc, s63, v90\nMvav 44,188,252\n"
"WL 8\nEX 81,33\nMvav 40,188,248\nRa 168,2,26624\ns_cbranch_vccnz .T159\n.T160:\nMvav 40,184,240\nD1 1,16,46,2048\nMvav 40,180,232\nRa 172,2,27648\nAD 86,86,84\nMvav 40,176,224\n"
"AD 84,80,81\nCV 64,80,81\nMaav 0,128,72\nRa 176,2,28672\nv_mov_b32 v112, -1\nMaav 4,128,76\nEX 82,34\nMaav 12,132,76\nRa 180,2,29696\nMaav 8,132,72\n"
"s_waitcnt vmcnt(18) lgkmcnt(8)\nEX 83,35\nMaav 16,136,72\nRa 184,2,30720\nAD 84,84,82\nMaav 20,136,76\nD1 1,16,46,3072\ns_add_u32 m0, s89, 49152\nMaav 28,140,76\nRa 188,2,31744\n"
"s_barrier\nMaav 24,140,72\nAD 84,84,83\nCV 65,82,83\nMaav 32,144,72\nRa 128,24,32768\nSv 136,1,16,77,2048\nMaav 36,144,76\ns_add_u32 s46, s87, s88\nEX 80,36\nMaav 44,148,76\n"
"Ra 132,24,33792\nMaav 40,148,72\nWL 8\nEX 81,37\nMaav 48,152,72\nRa 136,24,34816\nAD 87,87,85\nMaav 52,152,76\nD1 1,16,46,0\nMaav 60,156,76\nRa 140,24,35840\nMaav 56,156,72\n"
"AD 85,80,81\nCV 68,80,81\nMaav 64,160,72\nRa 144,26,32768\nv_mov_b32 v113, -1\nMaav 68,160,76\nEX 82,38\nMaav 76,164,76\nRa 148,26,33792\nMaav 72,164,72\nWL 8\nEX 83,39\n"
"Maav 80,168,72\nRa 152,26,34816\nAD 85,85,82\nMaav 84,168,76\nD1 1,16,46,1024\nMaav 92,172,76\nRa 156,26,35840\nMaav 88,172,72\nAD 85,85,83\nCV 69,82,83\nMaav 96,176,72\n"
"Ra 160,24,40960\nSv 140,1,16,77,3072\nMaav 100,176,76\ns_xor_b32 s89, s89, 0x10000\nEX 80,40\nMaav 108,180,76\nRa 164,24,41984\nMaav 104,180,72\nWL 6\nEX 81,41\nMaav 112,184,72\n"
"Ra 168,24,43008\nAD 84,84,80\nMaav 116,184,76\nD1 1,16,46,2048\nMaav 124,188,76\nRa 172,24,44032\nMaav 120,188,72\nAD 84,84,81\nCV 66,80,81\nCvav 48,128,192,4\nRa 176,26,40960\n"
"v_mov_b32 v114, -1\nMvav 48,132,200\nEX 82,42\nMvav 48,136,208\nRa 180,26,41984\nMvav 48,140,216\nWL 6\nEX 83,43\nCvav 52,140,220,8\nRa 184,26,43008\nAD 84,84,82\n"
"v_and_b32 v27, 0x70, v1\nbuffer_load_dword v27, v27, s[16:19], s61 offen offset:140 sc0 sc1\nMvav 52,136,212\nD1 1,16,46,3072\ns_add_u32 m0, s89, 0\nMvav 52,132,204\n"
"Ra 188,26,44032\nMvav 52,128,196\nAD 84,84,83\nCV 67,82,83\nMvav 52,144,228\nRa 128,2,49152\ns_add_u32 s77, s77, s55\nSv 144,1,16,77,0\nMvav 52,148,236\ns_add_u32 s46, s86, s88\n"
"EX 80,44\nMvav 52,152,244\nRa 132,2,50176\nv_xor_b32 v24, 0x10000, v24\nMvav 52,156,252\nWL 6\nEX 81,45\nMvav 48,156,248\nRa 136,2,51200\nAD 85,85,80\nMvav 48,152,240\n"
"D1 1,16,46,0\nMvav 48,148,232\nRa 140,2,52224\nMvav 48,144,224\nAD 85,85,81\nCV 70,80,81\nCvav 56,160,192,12\nRa 144,2,53248\nv_mov_b32 v115, -1\nMvav 56,164,200\nEX 82,46\n"
"Mvav 56,168,208\nRa 148,2,54272\nv_xor_b32 v26, 0x10000, v26\nMvav 56,172,216\nWL 6\nEX 83,47\nCvav 60,172,220,16\nRa 152,2,55296\nAD 85,85,82\nMvav 60,168,212\nD1 1,16,46,1024\n"
"Mvav 60,164,204\nRa 156,2,56320\nMvav 60,160,196\nAD 85,85,83\nCV 71,82,83\nMvav 60,176,228\nRa 160,2,57344\nv_max_f32 v90, v84, v85\nSv 148,1,16,77,1024\nMvav 60,180,236\n"
"s_add_u32 s88, s88, 32768\nEX 80,48\nMvav 60,184,244\nRa 164,2,58368\nv_cmp_lt_f32 vcc, s63, v90\nMvav 60,188,252\nWL 8\nEX 81,49\nMvav 56,188,248\nRa 168,2,59392\n"
"s_cbranch_vccnz .T161\n.T162:\nMvav 56,184,240\nD1 1,16,46,2048\nMvav 56,180,232\nRa 172,2,60416\nAD 86,86,84\nMvav 56,176,224\nAD 84,80,81\nCV 72,80,81\nMaav 0,128,64\n"
"Ra 176,2,61440\nMaav 4,128,68\nEX 82,50\nMaav 12,132,68\nRa 180,2,62464\nMaav 8,132,64\ns_waitcnt vmcnt(18) lgkmcnt(8)\nEX 83,51\nMaav 16,136,64\nRa 184,2,63488\nAD 84,84,82\n"
"Maav 20,136,68\nD1 1,16,46,3072\ns_add_u32 m0, s89, 16384\nMaav 28,140,68\nRa 188,2,64512\ns_barrier\nMaav 24,140,64\nAD 84,84,83\nCV 73,82,83\nMaav 32,144,64\nRa 128,24,0\n"
"Sv 152,1,16,77,2048\nMaav 36,144,68\ns_add_u32 s46, s87, s88\nEX 80,52\nMaav 44,148,68\nRa 132,24,1024\nv_xor_b32 v2, 0x10000, v2\nMaav 40,148,64\nWL 8\nEX 81,53\nMaav 48,152,64\n"
"Ra 136,24,2048\nAD 87,87,85\nbuffer_store_dword v25, off, s[16:19], s54 offset:8 sc0 sc1\nMaav 52,152,68\nD1 1,16,46,0\nMaav 60,156,68\nRa 140,24,3072\nMaav 56,156,64\n"
"AD 85,80,81\nCV 76,80,81\nMaav 64,160,64\nRa 144,26,0\nMaav 68,160,68\nEX 82,54\nMaav 76,164,68\nRa 148,26,1024\nMaav 72,164,64\nWL 8\nEX 83,55\nMaav 80,168,64\nRa 152,26,2048\n"
"AD 85,85,82\nMaav 84,168,68\nD1 1,16,46,1024\nMaav 92,172,68\nRa 156,26,3072\nMaav 88,172,64\nAD 85,85,83\nCV 77,82,83\nMaav 96,176,64\nRa 160,24,8192\nSv 156,1,16,77,3072\n"
"Maav 100,176,68\nEX 80,56\nMaav 108,180,68\nRa 164,24,9216\nMaav 104,180,64\nWL 6\nEX 81,57\nMaav 112,184,64\nRa 168,24,10240\nAD 84,84,80\nMaav 116,184,68\nD1 1,16,46,2048\n"
"Maav 124,188,68\nRa 172,24,11264\nMaav 120,188,64\nAD 84,84,81\nCV 74,80,81\nCvav 32,128,192,4\nRa 176,26,8192\nMvav 32,132,200\nEX 82,58\nMvav 32,136,208\nRa 180,26,9216\n"
"Mvav 32,140,216\nWL 6\nEX 83,59\nCvav 36,140,220,8\nRa 184,26,10240\nAD 84,84,82\nMvav 36,136,212\nD1 1,16,46,3072\ns_add_u32 m0, s89, 32768\nMvav 36,132,204\nRa 188,26,11264\n"
"Mvav 36,128,196\nAD 84,84,83\nCV 75,82,83\nMvav 36,144,228\nRa 128,2,16384\nSv 160,1,16,78,0\nMvav 36,148,236\ns_add_u32 s46, s86, s88\nEX 80,60\nMvav 36,152,244\nRa 132,2,17408\n"
"Mvav 36,156,252\nWL 6\nEX 81,61\nMvav 32,156,248\nRa 136,2,18432\nAD 85,85,80\nMvav 32,152,240\nD1 1,16,46,0\nMvav 32,148,232\nRa 140,2,19456\nSv 112,1,16,49,0\nMvav 32,144,224\n"
"AD 85,85,81\nCV 78,80,81\nCvav 40,160,192,12\nRa 144,2,20480\nMvav 40,164,200\nEX 82,62\nMvav 40,168,208\nRa 148,2,21504\nMvav 40,172,216\nWL 6\nEX 83,63\nCvav 44,172,220,16\n"
"Ra 152,2,22528\nAD 85,85,82\nMvav 44,168,212\nD1 1,16,46,1024\nMvav 44,164,204\nRa 156,2,23552\nMvav 44,160,196\nAD 85,85,83\nCV 79,82,83\nMvav 44,176,228\nRa 160,2,24576\n"
"v_max_f32 v90, v84, v85\nSv 164,1,16,78,1024\nMvav 44,180,236\ns_add_u32 s88, s88, 32768\nEX 80,32\nMvav 44,184,244\nRa 164,2,25600\nv_cmp_lt_f32 vcc, s63, v90\nMvav 44,188,252\n"
"WL 8\nEX 81,33\nMvav 40,188,248\nRa 168,2,26624\ns_cbranch_vccnz .T164\n.T165:\nMvav 40,184,240\nD1 1,16,46,2048\nMvav 40,180,232\nRa 172,2,27648\nAD 86,86,84\nMvav 40,176,224\n"
"AD 84,80,81\nCV 64,80,81\nMaav 0,128,72\nRa 176,2,28672\nMaav 4,128,76\nEX 82,34\nMaav 12,132,76\nRa 180,2,29696\nMaav 8,132,72\ns_waitcnt vmcnt(19) lgkmcnt(8)\nEX 83,35\n"
"Maav 16,136,72\nRa 184,2,30720\nAD 84,84,82\nMaav 20,136,76\nD1 1,16,46,3072\ns_add_u32 m0, s89, 49152\nMaav 28,140,76\nRa 188,2,31744\ns_barrier\nMaav 24,140,72\nAD 84,84,83\n"
"CV 65,82,83\nMaav 32,144,72\nRa 128,24,32768\nSv 168,1,16,78,2048\nMaav 36,144,76\ns_add_u32 s46, s87, s88\nEX 80,36\nMaav 44,148,76\nRa 132,24,33792\nMaav 40,148,72\nWL 8\n"
"EX 81,37\nMaav 48,152,72\nRa 136,24,34816\nAD 87,87,85\nMaav 52,152,76\nD1 1,16,46,0\nMaav 60,156,76\nRa 140,24,35840\nMaav 56,156,72\nAD 85,80,81\nCV 68,80,81\nMaav 64,160,72\n"
"Ra 144,26,32768\nMaav 68,160,76\nEX 82,38\nMaav 76,164,76\nRa 148,26,33792\nMaav 72,164,72\nWL 8\nEX 83,39\nMaav 80,168,72\nRa 152,26,34816\nAD 85,85,82\nMaav 84,168,76\n"
"D1 1,16,46,1024\nMaav 92,172,76\nRa 156,26,35840\nMaav 88,172,72\nAD 85,85,83\nCV 69,82,83\nMaav 96,176,72\nRa 160,24,40960\nSv 172,1,16,78,3072\nMaav 100,176,76\n"
"s_xor_b32 s89, s89, 0x10000\nEX 80,40\nMaav 108,180,76\nRa 164,24,41984\nMaav 104,180,72\nWL 6\nEX 81,41\nMaav 112,184,72\nRa 168,24,43008\nAD 84,84,80\nMaav 116,184,76\n"
"D1 1,16,46,2048\nMaav 124,188,76\nRa 172,24,44032\nMaav 120,188,72\nAD 84,84,81\nCV 66,80,81\nCvav 48,128,192,4\nRa 176,26,40960\nMvav 48,132,200\nEX 82,42\nMvav 48,136,208\n"
"Ra 180,26,41984\nMvav 48,140,216\nWL 6\nEX 83,43\nv_cmp_ne_u32 vcc, s15, v27\ns_nop 0\ns_cbranch_vccnz .S175\n.S176:\nv_and_b32 v27, 0x70, v1\n"
"buffer_load_dword v27, v27, s[16:19], s61 offen offset:12 sc0 sc1\nCvav 52,140,220,8\nRa 184,26,43008\nAD 84,84,82\nMvav 52,136,212\nD1 1,16,46,3072\ns_add_u32 m0, s89, 0\n"
"Mvav 52,132,204\nRa 188,26,44032\nMvav 52,128,196\nAD 84,84,83\nCV 67,82,83\nMvav 52,144,228\nRa 128,2,49152\ns_add_u32 s78, s78, s55\nSv 176,1,16,78,0\nMvav 52,148,236\n"
"s_add_u32 s46, s86, s88\nEX 80,44\nMvav 52,152,244\nRa 132,2,50176\nv_xor_b32 v24, 0x10000, v24\nMvav 52,156,252\nWL 6\nEX 81,45\nMvav 48,156,248\nRa 136,2,51200\nAD 85,85,80\n"
"Mvav 48,152,240\nD1 1,16,46,0\nv_lshrrev_b32 v92, 8, v1\nMvav 48,148,232\nRa 140,2,52224\nSv 112,1,16,49,1024\nMvav 48,144,224\nAD 85,85,81\nCV 70,80,81\nCvav 56,160,192,12\n"
"Ra 144,2,53248\nv_bfe_u32 v93, v1, 6, 2\nMvav 56,164,200\ns_add_u32 s49, s49, 131072\nEX 82,46\nMvav 56,168,208\nRa 148,2,54272\nv_xor_b32 v26, 0x10000, v26\nMvav 56,172,216\n"
"WL 6\nEX 83,47\nCvav 60,172,220,16\nRa 152,2,55296\nAD 85,85,82\nMvav 60,168,212\nD1 1,16,46,1024\nv_cmp_eq_u32 vcc, v92, v93\nv_and_b32 v92, 16, v1\nv_mov_b32 v93, 0x3f80\n"
"v_cndmask_b32 v93, 0, v93, vcc\nMvav 60,164,204\nRa 156,2,56320\n.W565:\ns_cbranch_execz .W569\nMvav 60,160,196\nAD 85,85,83\nCV 71,82,83\nMvav 60,176,228\nRa 160,2,57344\n"
"v_max_f32 v90, v84, v85\nSv 180,1,16,78,1024\nMvav 60,180,236\ns_add_u32 s88, s88, 32768\nEX 80,48\nMvav 60,184,244\nRa 164,2,58368\nv_cmp_lt_f32 vcc, s63, v90\nMvav 60,188,252\n"
"WL 8\nEX 81,49\nMvav 56,188,248\nRa 168,2,59392\ns_cbranch_vccnz .T166\n.T167:\nMvav 56,184,240\nD1 1,16,46,2048\nv_lshlrev_b32 v93, v92, v93\nMvav 56,180,232\nRa 172,2,60416\n"
"AD 86,86,84\nMvav 56,176,224\nAD 84,80,81\nCV 72,80,81\nMaav 0,128,64\nRa 176,2,61440\nv_and_b32 v92, 32, v1\nMaav 4,128,68\nEX 82,50\nMaav 12,132,68\nRa 180,2,62464\n"
"v_cmp_eq_u32 vcc, 0, v92\nv_mov_b32 v128, 0\nv_mov_b32 v129, 0\nv_cndmask_b32 v130, 0, v93, vcc\nMaav 8,132,64\ns_waitcnt vmcnt(20) lgkmcnt(8)\nEX 83,51\nMaav 16,136,64\n"
"Ra 184,2,63488\nAD 84,84,82\nMaav 20,136,68\nD1 1,16,46,3072\ns_add_u32 m0, s89, 16384\nMaav 28,140,68\nRa 188,2,64512\ns_barrier\nMaav 24,140,64\nAD 84,84,83\nCV 73,82,83\n"
"Maav 32,144,64\nRa 128,24,0\nSv 184,1,16,78,2048\nMaav 36,144,68\ns_add_u32 s46, s87, s88\nEX 80,52\nMaav 44,148,68\nRa 132,24,1024\nv_xor_b32 v2, 0x10000, v2\nMaav 40,148,64\n"
"WL 8\nEX 81,53\nMaav 48,152,64\nRa 136,24,2048\nAD 87,87,85\nMaav 52,152,68\nD1 1,16,46,0\nv_sub_u32 v131, v93, v130\nMaav 60,156,68\nRa 140,24,3072\nMaav 56,156,64\nAD 85,80,81\n"
"CV 76,80,81\nMaav 64,160,64\nRa 144,26,0\nv_mov_b32 v132, 0\nMaav 68,160,68\nEX 82,54\nMaav 76,164,68\nRa 148,26,1024\nMaav 72,164,64\nWL 8\nEX 83,55\nMaav 80,168,64\n"
"Ra 152,26,2048\nAD 85,85,82\nMaav 84,168,68\nD1 1,16,46,1024\nMaav 92,172,68\nRa 156,26,3072\nMaav 88,172,64\nAD 85,85,83\nCV 77,82,83\nMaav 96,176,64\nRa 160,24,8192\n"
"Sv 188,1,16,78,3072\nMaav 100,176,68\nEX 80,56\nMaav 108,180,68\nRa 164,24,9216\nv_mov_b32 v133, 0\nMaav 104,180,64\nWL 6\nEX 81,57\nMaav 112,184,64\nRa 168,24,10240\nAD 84,84,80\n"
"Maav 116,184,68\nD1 1,16,46,2048\nMaav 124,188,68\nRa 172,24,11264\nMaav 120,188,64\nAD 84,84,81\nCV 74,80,81\ns_mov_b32 s99, 11\ns_sub_u32 s90, s90, 3\n.p2align 6\n.A191:\n"
"Cvav 32,128,192,4\nRa 176,26,8192\nMvav 32,132,200\nEX 82,58\nMvav 32,136,208\nRa 180,26,9216\nMvav 32,140,216\nWL 6\nEX 83,59\nCvav 36,140,220,8\nRa 184,26,10240\nAD 84,84,82\n"
"Mvav 36,136,212\nD1 1,16,46,3072\ns_add_u32 m0, s89, 32768\nMvav 36,132,204\nRa 188,26,11264\nMvav 36,128,196\nAD 84,84,83\nCV 75,82,83\nMvav 36,144,228\nRa 128,2,16384\n"
"Mvav 36,148,236\ns_add_u32 s46, s86, s88\nEX 80,60\nMvav 36,152,244\nRa 132,2,17408\nMvav 36,156,252\nWL 6\nEX 81,61\nMvav 32,156,248\nRa 136,2,18432\nAD 85,85,80\n"
"Mvav 32,152,240\nD1 1,16,46,0\nMvav 32,148,232\nRa 140,2,19456\nSv 112,1,16,49,0\nMvav 32,144,224\nAD 85,85,81\nCV 78,80,81\nCvav 40,160,192,12\nRa 144,2,20480\nMvav 40,164,200\n"
"EX 82,62\nMvav 40,168,208\nRa 148,2,21504\nMvav 40,172,216\nWL 6\nEX 83,63\nCvav 44,172,220,16\nRa 152,2,22528\nAD 85,85,82\nMvav 44,168,212\nD1 1,16,46,1024\nMvav 44,164,204\n"
"Ra 156,2,23552\nMvav 44,160,196\nAD 85,85,83\nCV 79,82,83\nMvav 44,176,228\nRa 160,2,24576\nv_max_f32 v90, v84, v85\nMvav 44,180,236\ns_add_u32 s88, s88, 32768\nEX 80,32\n"
"Mvav 44,184,244\nRa 164,2,25600\nv_cmp_lt_f32 vcc, s63, v90\nMvav 44,188,252\nWL 8\nEX 81,33\nMvav 40,188,248\nRa 168,2,26624\ns_cbranch_vccnz .T195\n.T196:\nMvav 40,184,240\n"
"D1 1,16,46,2048\nMvav 40,180,232\nRa 172,2,27648\nAD 86,86,84\nMvav 40,176,224\nAD 84,80,81\nCV 64,80,81\nMaav 0,128,72\nRa 176,2,28672\nMaav 4,128,76\nEX 82,34\nMaav 12,132,76\n"
"Ra 180,2,29696\nMaav 8,132,72\ns_waitcnt vmcnt(13) lgkmcnt(8)\nEX 83,35\nMaav 16,136,72\nRa 184,2,30720\nAD 84,84,82\nMaav 20,136,76\nD1 1,16,46,3072\ns_add_u32 m0, s89, 49152\n"
"Maav 28,140,76\nRa 188,2,31744\ns_barrier\nMaav 24,140,72\nAD 84,84,83\nCV 65,82,83\nMaav 32,144,72\nRa 128,24,32768\nMaav 36,144,76\ns_add_u32 s46, s87, s88\nEX 80,36\n"
"Maav 44,148,76\nRa 132,24,33792\nMaav 40,148,72\nWL 8\nEX 81,37\nMaav 48,152,72\nRa 136,24,34816\nAD 87,87,85\nMaav 52,152,76\nD1 1,16,46,0\nMaav 60,156,76\nRa 140,24,35840\n"
"Maav 56,156,72\nAD 85,80,81\nCV 68,80,81\nMaav 64,160,72\nRa 144,26,32768\nMaav 68,160,76\nEX 82,38\nMaav 76,164,76\nRa 148,26,33792\nMaav 72,164,72\nWL 8\nEX 83,39\n"
"Maav 80,168,72\nRa 152,26,34816\nAD 85,85,82\nMaav 84,168,76\nD1 1,16,46,1024\nMaav 92,172,76\nRa 156,26,35840\nMaav 88,172,72\nAD 85,85,83\nCV 69,82,83\nMaav 96,176,72\n"
"Ra 160,24,40960\nMaav 100,176,76\ns_xor_b32 s89, s89, 0x10000\nEX 80,40\nMaav 108,180,76\nRa 164,24,41984\nMaav 104,180,72\nWL 6\nEX 81,41\nMaav 112,184,72\nRa 168,24,43008\n"
"AD 84,84,80\nMaav 116,184,76\nD1 1,16,46,2048\nMaav 124,188,76\nRa 172,24,44032\nMaav 120,188,72\nAD 84,84,81\nCV 66,80,81\nCvav 48,128,192,4\nRa 176,26,40960\nMvav 48,132,200\n"
"EX 82,42\nMvav 48,136,208\nRa 180,26,41984\nMvav 48,140,216\nWL 6\nEX 83,43\nCvav 52,140,220,8\nRa 184,26,43008\nAD 84,84,82\nMvav 52,136,212\nD1 1,16,46,3072\n"
"s_add_u32 m0, s89, 0\nMvav 52,132,204\nRa 188,26,44032\nMvav 52,128,196\nAD 84,84,83\nCV 67,82,83\nMvav 52,144,228\nRa 128,2,49152\nMvav 52,148,236\ns_add_u32 s46, s86, s88\n"
"EX 80,44\nMvav 52,152,244\nRa 132,2,50176\nv_xor_b32 v24, 0x10000, v24\nMvav 52,156,252\nWL 6\nEX 81,45\nMvav 48,156,248\nRa 136,2,51200\nAD 85,85,80\nMvav 48,152,240\n"
"D1 1,16,46,0\nMvav 48,148,232\nRa 140,2,52224\nSv 112,1,16,49,1024\nMvav 48,144,224\nAD 85,85,81\nCV 70,80,81\nCvav 56,160,192,12\nRa 144,2,53248\nMvav 56,164,200\n"
"s_add_u32 s49, s49, 131072\nEX 82,46\nMvav 56,168,208\nRa 148,2,54272\nv_xor_b32 v26, 0x10000, v26\nMvav 56,172,216\nWL 6\nEX 83,47\nCvav 60,172,220,16\nRa 152,2,55296\n"
"AD 85,85,82\nMvav 60,168,212\nD1 1,16,46,1024\nMvav 60,164,204\nRa 156,2,56320\nMvav 60,160,196\nAD 85,85,83\nCV 71,82,83\nMvav 60,176,228\nRa 160,2,57344\n"
"v_max_f32 v90, v84, v85\nMvav 60,180,236\ns_add_u32 s88, s88, 32768\nEX 80,48\nMvav 60,184,244\nRa 164,2,58368\nv_cmp_lt_f32 vcc, s63, v90\nMvav 60,188,252\nWL 8\nEX 81,49\n"
"Mvav 56,188,248\nRa 168,2,59392\ns_cbranch_vccnz .T197\n.T198:\nMvav 56,184,240\nD1 1,16,46,2048\nMvav 56,180,232\nRa 172,2,60416\nAD 86,86,84\nMvav 56,176,224\nAD 84,80,81\n"
"CV 72,80,81\nMaav 0,128,64\nRa 176,2,61440\nMaav 4,128,68\nEX 82,50\nMaav 12,132,68\nRa 180,2,62464\nMaav 8,132,64\ns_waitcnt vmcnt(13) lgkmcnt(8)\nEX 83,51\nMaav 16,136,64\n"
"Ra 184,2,63488\nAD 84,84,82\nMaav 20,136,68\nD1 1,16,46,3072\ns_add_u32 m0, s89, 16384\nMaav 28,140,68\nRa 188,2,64512\ns_barrier\nMaav 24,140,64\nAD 84,84,83\nCV 73,82,83\n"
"Maav 32,144,64\nRa 128,24,0\nMaav 36,144,68\ns_add_u32 s46, s87, s88\nEX 80,52\nMaav 44,148,68\nRa 132,24,1024\nv_xor_b32 v2, 0x10000, v2\nMaav 40,148,64\nWL 8\nEX 81,53\n"
"Maav 48,152,64\nRa 136,24,2048\nAD 87,87,85\nMaav 52,152,68\nD1 1,16,46,0\nMaav 60,156,68\nRa 140,24,3072\nMaav 56,156,64\nAD 85,80,81\nCV 76,80,81\nMaav 64,160,64\nRa 144,26,0\n"
"Maav 68,160,68\nEX 82,54\nMaav 76,164,68\nRa 148,26,1024\nMaav 72,164,64\nWL 8\nEX 83,55\nMaav 80,168,64\nRa 152,26,2048\nAD 85,85,82\nMaav 84,168,68\nD1 1,16,46,1024\n"
"Maav 92,172,68\nRa 156,26,3072\nMaav 88,172,64\nAD 85,85,83\nCV 77,82,83\nMaav 96,176,64\nRa 160,24,8192\nMaav 100,176,68\ns_sub_u32 s90, s90, 1\nEX 80,56\nMaav 108,180,68\n"
"Ra 164,24,9216\ns_cbranch_scc1 .E193\n.E194:\nMaav 104,180,64\nWL 6\nEX 81,57\nMaav 112,184,64\nRa 168,24,10240\nAD 84,84,80\nMaav 116,184,68\nD1 1,16,46,2048\n"
"s_sub_u32 s99, s99, 1\nMaav 124,188,68\nRa 172,24,11264\nMaav 120,188,64\nAD 84,84,81\nCV 74,80,81\ns_cbranch_scc0 .A191\ns_branch .A199\n.E193:\ns_swappc_b64 s[80:81], s[8:9]\n"
"s_branch .E194\n.HD144:\nbuffer_store_dword v25, off, s[16:19], s54 offset:4 sc0 sc1\nv_cmp_ne_u32 vcc, s15, v27\ns_nop 0\ns_cbranch_vccnz .S178\n.S179:\nv_and_b32 v27, 0x70, v1\n"
"buffer_load_dword v27, v27, s[16:19], s61 offen offset:8 sc0 sc1\ns_mov_b32 s90, 1\ns_swappc_b64 s[8:9], s[80:81]\nv_cmp_ne_u32 vcc, s15, v27\ns_nop 0\ns_cbranch_vccnz .S181\n"
".S182:\nv_and_b32 v27, 0xf0, v1\nbuffer_load_dword v27, v27, s[16:19], s61 offen offset:4 sc0 sc1\ns_mov_b32 s90, 1\ns_swappc_b64 s[8:9], s[80:81]\nv_cmp_ne_u32 vcc, s15, v27\n"
"s_nop 0\ns_cbranch_vccnz .S184\n.S185:\ns_mov_b32 s90, s91\ns_swappc_b64 s[8:9], s[80:81]\ns_branch .FF187\n.A199:\nCvav 32,128,192,4\nRa 176,26,8192\nMvav 32,132,200\nEX 82,58\n"
"Mvav 32,136,208\nRa 180,26,9216\nMvav 32,140,216\nWL 6\nEX 83,59\nCvav 36,140,220,8\nRa 184,26,10240\nAD 84,84,82\nMvav 36,136,212\nD1 1,16,46,3072\ns_add_u32 m0, s89, 32768\n"
"Mvav 36,132,204\nRa 188,26,11264\nMvav 36,128,196\nAD 84,84,83\nCV 75,82,83\ns_mov_b32 s94, s96\ns_add_u32 s95, s96, 65536\nv_and_b32 v108, 0xf0, v1\nMvav 36,144,228\n"
"Ra 128,2,16384\nMvav 36,148,236\ns_add_u32 s46, s86, s88\nEX 80,60\nMvav 36,152,244\nRa 132,2,17408\nMvav 36,156,252\nWL 6\nEX 81,61\nMvav 32,156,248\nRa 136,2,18432\nAD 85,85,80\n"
"Mvav 32,152,240\nD1 1,16,46,0\nMvav 32,148,232\nRa 140,2,19456\nSv 112,1,16,49,0\nMvav 32,144,224\nAD 85,85,81\nCV 78,80,81\n"
"buffer_load_dwordx4 v[100:103], v108, s[16:19], s65 offen offset:0 sc0 sc1\nbuffer_load_dwordx4 v[104:107], v108, s[16:19], s65 offen offset:256 sc0 sc1\nCvav 40,160,192,12\n"
"Ra 144,2,20480\nMvav 40,164,200\nEX 82,62\nMvav 40,168,208\nRa 148,2,21504\nMvav 40,172,216\nWL 6\nEX 83,63\nCvav 44,172,220,16\nRa 152,2,22528\nAD 85,85,82\nMvav 44,168,212\n"
"D1 1,16,46,1024\nMvav 44,164,204\nRa 156,2,23552\nMvav 44,160,196\nAD 85,85,83\nCV 79,82,83\nMvav 44,176,228\nRa 160,2,24576\nv_max_f32 v90, v84, v85\nMvav 44,180,236\n"
"s_add_u32 s88, s88, 32768\nEX 80,32\nMvav 44,184,244\nRa 164,2,25600\nv_cmp_lt_f32 vcc, s63, v90\nMvav 44,188,252\nWL 8\nEX 81,33\nMvav 40,188,248\nRa 168,2,26624\n"
"s_cbranch_vccnz .T202\n.T203:\nMvav 40,184,240\nD1 1,16,46,2048\nMvav 40,180,232\nRa 172,2,27648\nAD 86,86,84\nMvav 40,176,224\nAD 84,80,81\nCV 64,80,81\nMaav 0,128,72\n"
"Ra 176,2,28672\nMaav 4,128,76\nEX 82,34\nMaav 12,132,76\nRa 180,2,29696\nMaav 8,132,72\ns_waitcnt vmcnt(15) lgkmcnt(8)\nEX 83,35\nMaav 16,136,72\nRa 184,2,30720\nAD 84,84,82\n"
"Maav 20,136,76\nD1 1,16,46,3072\ns_add_u32 m0, s89, 49152\nMaav 28,140,76\nRa 188,2,31744\ns_barrier\nMaav 24,140,72\nAD 84,84,83\nCV 65,82,83\nMaav 32,144,72\nRa 128,24,32768\n"
"Maav 36,144,76\ns_add_u32 s46, s87, s88\nEX 80,36\nMaav 44,148,76\nRa 132,24,33792\nMaav 40,148,72\nWL 8\nEX 81,37\nMaav 48,152,72\nRa 136,24,34816\nAD 87,87,85\nMaav 52,152,76\n"
"D1 1,16,46,0\nMaav 60,156,76\nRa 140,24,35840\nMaav 56,156,72\n.W566:\ns_cbranch_execz .W570\nAD 85,80,81\nCV 68,80,81\nMaav 64,160,72\nRa 144,26,32768\nMaav 68,160,76\nEX 82,38\n"
"Maav 76,164,76\nRa 148,26,33792\nMaav 72,164,72\nWL 8\nEX 83,39\nMaav 80,168,72\nRa 152,26,34816\nAD 85,85,82\nMaav 84,168,76\nD1 1,16,46,1024\nMaav 92,172,76\nRa 156,26,35840\n"
"Maav 88,172,72\nAD 85,85,83\nCV 69,82,83\nMaav 96,176,72\nRa 160,24,40960\nMaav 100,176,76\ns_xor_b32 s89, s89, 0x10000\nEX 80,40\nMaav 108,180,76\nRa 164,24,41984\n"
"Maav 104,180,72\nWL 6\nEX 81,41\nMaav 112,184,72\nRa 168,24,43008\nAD 84,84,80\nMaav 116,184,76\nD1 1,16,46,2048\nMaav 124,188,76\nRa 172,24,44032\nMaav 120,188,72\nAD 84,84,81\n"
"CV 66,80,81\nCvav 48,128,192,4\nRa 176,26,40960\nMvav 48,132,200\ns_mov_b32 s86, s94\nEX 82,42\nMvav 48,136,208\nRa 180,26,41984\nMvav 48,140,216\nWL 6\nEX 83,43\n"
"Cvav 52,140,220,8\nRa 184,26,43008\nAD 84,84,82\nMvav 52,136,212\nD1 1,16,46,3072\ns_add_u32 m0, s89, 0\nMvav 52,132,204\nRa 188,26,44032\nMvav 52,128,196\nAD 84,84,83\n"
"CV 67,82,83\nMvav 52,144,228\nRa 128,2,49152\nMvav 52,148,236\ns_add_u32 s46, s86, s88\nEX 80,44\nMvav 52,152,244\nRa 132,2,50176\nv_xor_b32 v24, 0x10000, v24\nMvav 52,156,252\n"
"WL 6\nEX 81,45\nMvav 48,156,248\nRa 136,2,51200\nAD 85,85,80\nMvav 48,152,240\nD1 1,16,46,0\nMvav 48,148,232\nRa 140,2,52224\nSv 112,1,16,49,1024\nMvav 48,144,224\nAD 85,85,81\n"
"CV 70,80,81\nWV 9\nv_xor_b32 v109, s15, v102\nv_xor_b32 v110, s15, v106\nv_or_b32 v109, v109, v110\nv_cmp_ne_u32 vcc, 0, v109\ns_nop 3\ns_cmp_lg_u64 vcc, 0\n"
"s_cselect_b32 s67, 1, 0\ns_cselect_b32 s6, 0x40000000, s66\nCvav 56,160,192,12\nRa 144,2,53248\ns_add_u32 s5, s6, 0\nMvav 56,164,200\ns_add_u32 s49, s49, 131072\nEX 82,46\n"
"Mvav 56,168,208\nRa 148,2,54272\nv_xor_b32 v26, 0x10000, v26\nbuffer_load_dwordx4 a[192:195], v1, s[16:19], s5 offen offset:0 sc1\nMvav 56,172,216\nWL 6\nEX 83,47\n"
"Cvav 60,172,220,16\nRa 152,2,55296\nAD 85,85,82\nMvav 60,168,212\nD1 1,16,46,1024\nMvav 60,164,204\nRa 156,2,56320\nMvav 60,160,196\nAD 85,85,83\nCV 71,82,83\nMvav 60,176,228\n"
"Ra 160,2,57344\nv_max_f32 v90, v84, v85\nMvav 60,180,236\ns_add_u32 s88, s88, 32768\nEX 80,48\nMvav 60,184,244\nRa 164,2,58368\nv_cmp_lt_f32 vcc, s63, v90\nMvav 60,188,252\nWL 8\n"
"EX 81,49\nMvav 56,188,248\nRa 168,2,59392\ns_cbranch_vccnz .T204\n.T205:\nbuffer_load_dwordx4 a[196:199], v1, s[16:19], s5 offen offset:1024 sc1\nMvav 56,184,240\nD1 1,16,46,2048\n"
"Mvav 56,180,232\nRa 172,2,60416\nAD 86,86,84\nMvav 56,176,224\nAD 84,80,81\nCV 72,80,81\nMaav 0,128,64\nRa 176,2,61440\nMaav 4,128,68\nEX 82,50\nMaav 12,132,68\nRa 180,2,62464\n"
"Maav 8,132,64\nWL 8\nEX 83,51\nMaav 16,136,64\nRa 184,2,63488\nAD 84,84,82\nbuffer_load_dwordx4 a[200:203], v1, s[16:19], s5 offen offset:2048 sc1\nMaav 20,136,68\n"
"D1 1,16,46,3072\ns_add_u32 m0, s89, 16384\nMaav 28,140,68\nRa 188,2,64512\ns_barrier\nMaav 24,140,64\nAD 84,84,83\nCV 73,82,83\nMaav 32,144,64\nRa 128,24,0\nMaav 36,144,68\n"
"s_add_u32 s46, s87, s88\nEX 80,52\nMaav 44,148,68\nRa 132,24,1024\nv_xor_b32 v2, 0x10000, v2\nMaav 40,148,64\nWL 8\nEX 81,53\nMaav 48,152,64\nRa 136,24,2048\nAD 87,87,85\n"
"buffer_load_dwordx4 a[204:207], v1, s[16:19], s5 offen offset:3072 sc1\nMaav 52,152,68\nD1 1,16,46,0\nMaav 60,156,68\nRa 140,24,3072\nMaav 56,156,64\nAD 85,80,81\nCV 76,80,81\n"
"Maav 64,160,64\nRa 144,26,0\nMaav 68,160,68\ns_cmp_lg_u32 s92, 0\nEX 82,54\nMaav 76,164,68\nRa 148,26,1024\ns_cbranch_scc0 .E200\nv_cmpx_gt_i32 vcc, 19, v3\nv_mov_b32 v15, s97\n"
"v_cmpx_gt_i32 vcc, 18, v3\nv_mov_b32 v14, s97\nv_cmpx_gt_i32 vcc, 17, v3\nv_mov_b32 v13, s97\nv_cmpx_gt_i32 vcc, 16, v3\nv_mov_b32 v12, s97\nv_cmpx_gt_i32 vcc, 3, v3\n"
"v_mov_b32 v7, s97\nv_cmpx_gt_i32 vcc, 2, v3\nv_mov_b32 v6, s97\nv_cmpx_gt_i32 vcc, 1, v3\nv_mov_b32 v5, s97\nv_cmpx_gt_i32 vcc, 0, v3\nv_mov_b32 v4, s97\ns_mov_b64 exec, -1\n"
"s_nop 2\n.E200:\ns_add_u32 s5, s6, 4096\nMaav 72,164,64\nWL 8\nEX 83,55\nMaav 80,168,64\nRa 152,26,2048\nAD 85,85,82\n"
"buffer_load_dwordx4 a[208:211], v1, s[16:19], s5 offen offset:0 sc1\nMaav 84,168,68\nD1 1,16,46,1024\nMaav 92,172,68\nRa 156,26,3072\nMaav 88,172,64\nAD 85,85,83\nCV 77,82,83\n"
"Maav 96,176,64\nRa 160,24,8192\nMaav 100,176,68\nEX 80,56\nMaav 108,180,68\nRa 164,24,9216\nMaav 104,180,64\nWL 6\nEX 81,57\nMaav 112,184,64\nRa 168,24,10240\nAD 84,84,80\n"
"buffer_load_dwordx4 a[212:215], v1, s[16:19], s5 offen offset:1024 sc1\nMaav 116,184,68\nD1 1,16,46,2048\nMaav 124,188,68\nRa 172,24,11264\nMaav 120,188,64\nAD 84,84,81\n"
"CV 74,80,81\nCvav 32,128,192,4\nRa 176,26,8192\nMvav 32,132,200\nEX 82,58\nMvav 32,136,208\nRa 180,26,9216\nMvav 32,140,216\nWL 6\nEX 83,59\nCvav 36,140,220,8\nRa 184,26,10240\n"
"AD 84,84,82\nbuffer_load_dwordx4 a[216:219], v1, s[16:19], s5 offen offset:2048 sc1\nMvav 36,136,212\nD1 1,16,46,3072\ns_add_u32 m0, s89, 32768\nMvav 36,132,204\nRa 188,26,11264\n"
"Mvav 36,128,196\nAD 84,84,83\nCV 75,82,83\nMvav 36,144,228\nRa 128,2,16384\nMvav 36,148,236\ns_add_u32 s46, s86, s88\nEX 80,60\nMvav 36,152,244\nRa 132,2,17408\nMvav 36,156,252\n"
"WL 6\nEX 81,61\nMvav 32,156,248\nRa 136,2,18432\nAD 85,85,80\nbuffer_load_dwordx4 a[220:223], v1, s[16:19], s5 offen offset:3072 sc1\nMvav 32,152,240\nD1 1,16,46,0\n"
"Mvav 32,148,232\nRa 140,2,19456\nMvav 32,144,224\nAD 85,85,81\nCV 78,80,81\nCvav 40,160,192,12\nRa 144,2,20480\nMvav 40,164,200\ns_mov_b32 s87, s95\nEX 82,62\nMvav 40,168,208\n"
"Ra 148,2,21504\ns_add_u32 s5, s6, 16384\nMvav 40,172,216\nWL 6\nEX 83,63\nCvav 44,172,220,16\nRa 152,2,22528\nAD 85,85,82\n"
"buffer_load_dwordx4 a[224:227], v1, s[16:19], s5 offen offset:0 sc1\nMvav 44,168,212\nD1 1,16,46,1024\nMvav 44,164,204\nRa 156,2,23552\nMvav 44,160,196\nAD 85,85,83\nCV 79,82,83\n"
"Mvav 44,176,228\nRa 160,2,24576\nv_max_f32 v90, v84, v85\nMvav 44,180,236\ns_add_u32 s88, s88, 32768\nEX 80,32\nMvav 44,184,244\nRa 164,2,25600\nv_cmp_lt_f32 vcc, s63, v90\n"
"Mvav 44,188,252\nWL 8\nEX 81,33\nMvav 40,188,248\nRa 168,2,26624\ns_cbranch_vccnz .T208\n.T209:\nbuffer_load_dwordx4 a[228:231], v1, s[16:19], s5 offen offset:1024 sc1\n"
"Mvav 40,184,240\nD1 1,16,46,2048\nMvav 40,180,232\nRa 172,2,27648\nAD 86,86,84\nMvav 40,176,224\nAD 84,80,81\nCV 64,80,81\nMaav 0,128,72\nRa 176,2,28672\nMaav 4,128,76\nEX 82,34\n"
"Maav 12,132,76\nRa 180,2,29696\nMaav 8,132,72\ns_waitcnt vmcnt(22) lgkmcnt(8)\nEX 83,35\nMaav 16,136,72\nRa 184,2,30720\nAD 84,84,82\n"
"buffer_load_dwordx4 a[232:235], v1, s[16:19], s5 offen offset:2048 sc1\nMaav 20,136,76\nD1 1,16,46,3072\ns_add_u32 m0, s89, 49152\nMaav 28,140,76\nRa 188,2,31744\ns_barrier\n"
"Maav 24,140,72\nAD 84,84,83\nCV 65,82,83\nMaav 32,144,72\nRa 128,24,32768\nMaav 36,144,76\ns_add_u32 s46, s87, s88\nEX 80,36\nMaav 44,148,76\nRa 132,24,33792\nMaav 40,148,72\n"
"WL 8\nEX 81,37\nMaav 48,152,72\nRa 136,24,34816\nAD 87,87,85\nbuffer_load_dwordx4 a[236:239], v1, s[16:19], s5 offen offset:3072 sc1\nMaav 52,152,76\nD1 1,16,46,0\nMaav 60,156,76\n"
"Ra 140,24,35840\nMaav 56,156,72\nAD 85,80,81\nCV 68,80,81\nMaav 64,160,72\nRa 144,26,32768\nMaav 68,160,76\ns_cmp_eq_u32 s13, 7\nEX 82,38\nMaav 76,164,76\nRa 148,26,33792\n"
"s_cbranch_scc0 .E206\nv_cmpx_gt_i32 vcc, 19, v0\nv_mov_b32 v15, s97\nv_cmpx_gt_i32 vcc, 18, v0\nv_mov_b32 v14, s97\nv_cmpx_gt_i32 vcc, 17, v0\nv_mov_b32 v13, s97\n"
"v_cmpx_gt_i32 vcc, 16, v0\nv_mov_b32 v12, s97\nv_cmpx_gt_i32 vcc, 3, v0\nv_mov_b32 v7, s97\nv_cmpx_gt_i32 vcc, 2, v0\nv_mov_b32 v6, s97\nv_cmpx_gt_i32 vcc, 1, v0\n"
"v_mov_b32 v5, s97\nv_cmpx_gt_i32 vcc, 0, v0\nv_mov_b32 v4, s97\ns_mov_b64 exec, -1\ns_nop 2\n.E206:\ns_add_u32 s5, s6, 20480\nMaav 72,164,72\nWL 8\nEX 83,39\nMaav 80,168,72\n"
"Ra 152,26,34816\nAD 85,85,82\nbuffer_load_dwordx4 a[240:243], v1, s[16:19], s5 offen offset:0 sc1\nMaav 84,168,76\nD1 1,16,46,1024\nMaav 92,172,76\nRa 156,26,35840\n"
"Maav 88,172,72\nAD 85,85,83\nCV 69,82,83\nMaav 96,176,72\nRa 160,24,40960\nMaav 100,176,76\ns_xor_b32 s89, s89, 0x10000\nEX 80,40\nMaav 108,180,76\nRa 164,24,41984\n"
"Maav 104,180,72\nWL 6\nEX 81,41\nMaav 112,184,72\nRa 168,24,43008\nAD 84,84,80\nbuffer_load_dwordx4 a[244:247], v1, s[16:19], s5 offen offset:1024 sc1\nMaav 116,184,76\n"
"D1 1,16,46,2048\nMaav 124,188,76\nRa 172,24,44032\nMaav 120,188,72\nAD 84,84,81\nCV 66,80,81\nCvav 48,128,192,4\nRa 176,26,40960\nMvav 48,132,200\nEX 82,42\nMvav 48,136,208\n"
"Ra 180,26,41984\nMvav 48,140,216\nWL 6\nEX 83,43\nCvav 52,140,220,8\nRa 184,26,43008\nAD 84,84,82\nbuffer_load_dwordx4 a[248:251], v1, s[16:19], s5 offen offset:2048 sc1\n"
"Mvav 52,136,212\nD1 1,16,46,3072\ns_add_u32 m0, s89, 0\nMvav 52,132,204\nRa 188,26,44032\nMvav 52,128,196\nAD 84,84,83\nCV 67,82,83\nMvav 52,144,228\nRa 128,2,49152\n"
"Mvav 52,148,236\ns_add_u32 s46, s86, s88\nEX 80,44\nMvav 52,152,244\nRa 132,2,50176\nv_xor_b32 v24, 0x10000, v24\nMvav 52,156,252\nWL 6\nEX 81,45\nMvav 48,156,248\nRa 136,2,51200\n"
"AD 85,85,80\nbuffer_load_dwordx4 a[252:255], v1, s[16:19], s5 offen offset:3072 sc1\nMvav 48,152,240\nD1 1,16,46,0\nMvav 48,148,232\nRa 140,2,52224\nMvav 48,144,224\nAD 85,85,81\n"
"CV 70,80,81\nCvav 56,160,192,12\nRa 144,2,53248\nMvav 56,164,200\nEX 82,46\nMvav 56,168,208\nRa 148,2,54272\nv_xor_b32 v26, 0x10000, v26\nMvav 56,172,216\nWL 6\nEX 83,47\n"
"Cvav 60,172,220,16\nRa 152,2,55296\nAD 85,85,82\nMvav 60,168,212\nD1 1,16,46,1024\nMvav 60,164,204\nRa 156,2,56320\nMvav 60,160,196\nAD 85,85,83\nCV 71,82,83\nMvav 60,176,228\n"
"Ra 160,2,57344\nv_max_f32 v90, v84, v85\nMvav 60,180,236\ns_add_u32 s88, s88, 32768\nEX 80,48\nMvav 60,184,244\nRa 164,2,58368\nv_cmp_lt_f32 vcc, s63, v90\nMvav 60,188,252\nWL 8\n"
"EX 81,49\nMvav 56,188,248\nRa 168,2,59392\ns_cbranch_vccnz .T210\n.T211:\nMvav 56,184,240\nD1 1,16,46,2048\nMvav 56,180,232\nRa 172,2,60416\nAD 86,86,84\nMvav 56,176,224\n"
"AD 84,80,81\nCV 72,80,81\nMaav 0,128,64\nRa 176,2,61440\nbuffer_load_dwordx4 v[100:103], v108, s[16:19], s65 offen offset:0 sc0 sc1\n"
"buffer_load_dwordx4 v[104:107], v108, s[16:19], s65 offen offset:256 sc0 sc1\nMaav 4,128,68\nEX 82,50\nMaav 12,132,68\nRa 180,2,62464\nMaav 8,132,64\n"
"s_waitcnt vmcnt(22) lgkmcnt(8)\nEX 83,51\nMaav 16,136,64\nRa 184,2,63488\nAD 84,84,82\nMaav 20,136,68\nD1 1,16,46,3072\ns_add_u32 m0, s89, 16384\nMaav 28,140,68\nRa 188,2,64512\n"
"s_barrier\nMaav 24,140,64\nAD 84,84,83\nCV 73,82,83\nMaav 32,144,64\nRa 128,24,0\nMaav 36,144,68\ns_add_u32 s46, s87, s88\nEX 80,52\nMaav 44,148,68\nRa 132,24,1024\n"
"v_xor_b32 v2, 0x10000, v2\nMaav 40,148,64\nWL 8\nEX 81,53\nMaav 48,152,64\nRa 136,24,2048\nAD 87,87,85\nMaav 52,152,68\nD1 1,16,46,0\nMaav 60,156,68\nRa 140,24,3072\n"
"Maav 56,156,64\nAD 85,80,81\nCV 76,80,81\nMaav 64,160,64\nRa 144,26,0\nMaav 68,160,68\nEX 82,54\nMaav 76,164,68\nRa 148,26,1024\nMaav 72,164,64\nWL 8\nEX 83,55\nMaav 80,168,64\n"
"Ra 152,26,2048\nAD 85,85,82\nMaav 84,168,68\nD1 1,16,46,1024\nMaav 92,172,68\nRa 156,26,3072\nMaav 88,172,64\nAD 85,85,83\nCV 77,82,83\nMaav 96,176,64\nRa 160,24,8192\n"
"Maav 100,176,68\nEX 80,56\nMaav 108,180,68\nRa 164,24,9216\nMaav 104,180,64\nWL 10\nEX 81,57\nMaav 112,184,64\nRa 168,24,10240\nAD 84,84,80\nMaav 116,184,68\nD1 1,16,46,2048\n"
"Maav 124,188,68\nRa 172,24,11264\nMaav 120,188,64\nAD 84,84,81\nCV 74,80,81\nWV 4\ns_cmp_lg_u32 s67, 0\ns_cbranch_scc0 .E219\nv_xor_b32 v109, s15, v102\nv_xor_b32 v110, s15, v106\n"
"v_or_b32 v109, v109, v110\nv_cmp_ne_u32 vcc, 0, v109\ns_nop 0\ns_cbranch_vccnz .S216\n.S217:\ns_add_u32 s5, s66, 0\n"
"buffer_load_dwordx4 a[192:195], v1, s[16:19], s5 offen offset:0 sc1\nbuffer_load_dwordx4 a[196:199], v1, s[16:19], s5 offen offset:1024 sc1\n.W567:\ns_cbranch_execz .WB152\n"
"buffer_load_dwordx4 a[200:203], v1, s[16:19], s5 offen offset:2048 sc1\nbuffer_load_dwordx4 a[204:207], v1, s[16:19], s5 offen offset:3072 sc1\ns_add_u32 s5, s66, 4096\n"
"buffer_load_dwordx4 a[208:211], v1, s[16:19], s5 offen offset:0 sc1\nbuffer_load_dwordx4 a[212:215], v1, s[16:19], s5 offen offset:1024 sc1\n"
"buffer_load_dwordx4 a[216:219], v1, s[16:19], s5 offen offset:2048 sc1\nbuffer_load_dwordx4 a[220:223], v1, s[16:19], s5 offen offset:3072 sc1\ns_add_u32 s5, s66, 16384\n"
"buffer_load_dwordx4 a[224:227], v1, s[16:19], s5 offen offset:0 sc1\nbuffer_load_dwordx4 a[228:231], v1, s[16:19], s5 offen offset:1024 sc1\n"
"buffer_load_dwordx4 a[232:235], v1, s[16:19], s5 offen offset:2048 sc1\nbuffer_load_dwordx4 a[236:239], v1, s[16:19], s5 offen offset:3072 sc1\ns_add_u32 s5, s66, 20480\n"
"buffer_load_dwordx4 a[240:243], v1, s[16:19], s5 offen offset:0 sc1\nbuffer_load_dwordx4 a[244:247], v1, s[16:19], s5 offen offset:1024 sc1\n"
"buffer_load_dwordx4 a[248:251], v1, s[16:19], s5 offen offset:2048 sc1\nbuffer_load_dwordx4 a[252:255], v1, s[16:19], s5 offen offset:3072 sc1\nWV 0\ns_mov_b32 s67, 0\n.E219:\n"
"v_cmp_lt_f32 vcc, 0, v101\nSU 20,100,88\nv_mul_f32 v101, 0x3e800000, v101\nv_cndmask_b32 v20, 0, v20, vcc\nv_cmp_lt_f32 vcc, 0, v105\nSU 21,104,89\n"
"v_mul_f32 v105, 0x3e800000, v105\nv_cndmask_b32 v21, 0, v21, vcc\nv_or_b32 v91, v20, v21\nv_cmp_ne_u32 vcc, 0, v91\ns_nop 0\ns_cbranch_vccnz .D214\nAD 86,86,101\nAD 87,87,105\n"
".D215:\nMava 0,130,192\nMava 8,128,192\nEX 82,58\nMava 12,128,196\nMava 4,130,196\nEX 83,59\nMava 16,130,200\nAD 84,84,82\nMava 24,128,200\nD1 1,16,46,3072\nMava 28,128,204\n"
"s_add_u32 m0, s89, 32768\ns_add_u32 s46, s86, s88\nMava 20,130,204\nAD 84,84,83\nCV 75,82,83\nMava 32,130,208\nRa 128,2,16384\nv_xor_b32 v3, 0x10000, v2\n"
"v_add_u32 v0, 0x20000, v1\nMava 40,128,208\nEX 80,60\nMava 44,128,212\nRa 132,2,17408\ns_sub_u32 s94, s49, 1835008\nMava 36,130,212\nEX 81,61\nMava 48,130,216\nRa 136,2,18432\n"
"AD 85,85,80\ns_xor_b32 s92, s94, 0x202000\nMava 56,128,216\nD1 1,16,46,0\ns_lshl_b32 s89, s11, 12\nMava 60,128,220\nRa 140,2,19456\ns_lshl1_add_u32 s2, s12, 0\nMava 52,130,220\n"
"AD 85,85,81\nCV 78,80,81\nMava 64,130,224\nRa 144,2,20480\ns_lshl3_add_u32 s2, s2, s13\ns_lshl4_add_u32 s2, s2, 0xc048200\nMava 72,128,224\nEX 82,62\nMava 76,128,228\n"
"Ra 148,2,21504\ns_and_b32 s3, s72, 0x2000\nMava 68,130,228\nEX 83,63\nMava 80,130,232\nRa 152,2,22528\nAD 85,85,82\nMava 88,128,232\nD1 1,16,46,1024\ns_cmp_eq_u32 s11, 1\n"
"s_cselect_b32 s54, s2, s54\ns_cselect_b32 s3, s3, 0\nMava 92,128,236\nRa 156,2,23552\nv_xor_b32 v25, s3, v25\nMava 84,130,236\nAD 85,85,83\nCV 79,82,83\nMava 96,130,240\n"
"Ra 160,2,24576\nv_max_f32 v90, v84, v85\nMava 104,128,240\nv_cmp_lt_f32 vcc, s63, v90\nMava 108,128,244\nRa 164,2,25600\nMava 100,130,244\nWL 8\nMava 112,130,248\nRa 168,2,26624\n"
"Mava 120,128,248\nD1 1,16,46,2048\nMava 124,128,252\nRa 172,2,27648\nMava 116,130,252\ns_cbranch_vccnz .T220\n.T221:\nAD 86,86,84\nAD 87,87,85\nMaav 0,128,72\nRa 176,2,28672\n"
"v_mov_b32 v84, v86\nv_mov_b32 v85, v87\nMaav 4,128,76\nv_permlane32_swap_b32 v86, v84\nv_permlane32_swap_b32 v87, v85\nMaav 12,132,76\nRa 180,2,29696\nAD 86,86,84\nMaav 8,132,72\n"
"WL 8\nAD 87,87,85\nMaav 16,136,72\nRa 184,2,30720\nv_mov_b32 v84, v86\nv_mov_b32 v85, v87\nMaav 20,136,76\nD1 1,16,46,3072\nv_permlane16_swap_b32 v86, v84\nMaav 28,140,76\n"
"Ra 188,2,31744\nv_permlane16_swap_b32 v87, v85\nMaav 24,140,72\nAD 86,86,84\nAD 87,87,85\nMaav 32,144,72\nRa 128,2,0\nv_rcp_f32 v86, v86\nMaav 36,144,76\nv_rcp_f32 v87, v87\n"
"Maav 44,148,76\nRa 132,2,1024\nAR 82,0\nAR 83,1\nMU 82,82,86\nMU 83,83,86\nMaav 40,148,72\nWL 8\ns_add_u32 m0, s89, 131072\ns_add_u32 s46, s96, 1212416\nCV 96,82,83\nAR 82,2\n"
"Maav 48,152,72\nRa 136,2,2048\nAR 83,3\nMU 82,82,86\nMU 83,83,86\nCV 97,82,83\nMaav 52,152,76\nD1 1,16,46,0\nAR 82,4\nAR 83,5\nMU 82,82,87\nMaav 60,156,76\nRa 140,2,3072\n"
"MU 83,83,87\nCV 100,82,83\nAR 82,6\nAR 83,7\nMaav 56,156,72\nMU 82,82,87\nMU 83,83,87\nCV 101,82,83\nAR 82,8\nAR 83,9\nMaav 64,160,72\nRa 144,2,4096\nMU 82,82,86\nMU 83,83,86\n"
"CV 98,82,83\nAR 82,10\nMaav 68,160,76\nAR 83,11\nMU 82,82,86\nMU 83,83,86\nCV 99,82,83\nMaav 76,164,76\nRa 148,2,5120\nAR 82,12\nAR 83,13\nMU 82,82,87\nMU 83,83,87\n"
"Maav 72,164,72\nWL 8\nCV 102,82,83\nAR 82,14\nAR 83,15\nMU 82,82,87\nMaav 80,168,72\nRa 152,2,6144\nMU 83,83,87\nCV 103,82,83\nAR 82,16\nAR 83,17\nMaav 84,168,76\nD1 1,16,46,1024\n"
"MU 82,82,86\nMU 83,83,86\nCV 104,82,83\nMaav 92,172,76\nRa 156,2,7168\nAR 82,18\nAR 83,19\nMU 82,82,86\nMU 83,83,86\nMaav 88,172,72\nCV 105,82,83\nAR 82,20\nAR 83,21\nMU 82,82,87\n"
"MU 83,83,87\nMaav 96,176,72\nRa 160,2,8192\nCV 108,82,83\nAR 82,22\nAR 83,23\nMU 82,82,87\nMaav 100,176,76\nMU 83,83,87\nCV 109,82,83\nAR 82,24\nAR 83,25\nMaav 108,180,76\n"
"Ra 164,2,9216\nMU 82,82,86\nMU 83,83,86\nCV 106,82,83\nAR 82,26\nMaav 104,180,72\nWL 8\nAR 83,27\nMU 82,82,86\nMU 83,83,86\nCV 107,82,83\nMaav 112,184,72\nRa 168,2,10240\n"
"AR 82,28\nAR 83,29\nMU 82,82,87\nMU 83,83,87\nMaav 116,184,76\nD1 1,16,46,2048\nCV 110,82,83\nAR 82,30\nAR 83,31\nMaav 124,188,76\nRa 172,2,11264\nMU 82,82,87\nMU 83,83,87\n"
"CV 111,82,83\nMaav 120,188,72\nAR 82,32\nAR 83,33\nMU 82,82,86\nMU 83,83,86\nZvav 128,128,96\nRa 176,2,12288\nCV 112,82,83\nAR 82,34\nAR 83,35\nMU 82,82,86\nMvav 128,132,104\n"
"MU 83,83,86\nCV 113,82,83\nAR 82,36\nAR 83,37\nZvav 132,132,108\nRa 180,2,13312\nMU 82,82,87\nMU 83,83,87\nCV 116,82,83\nMvav 132,128,100\nWL 8\nAR 82,38\nAR 83,39\nMU 82,82,87\n"
"Zvav 140,136,100\nRa 184,2,14336\nMU 83,83,87\nCV 117,82,83\nAR 82,40\nMvav 140,140,108\nD1 1,16,46,3072\nAR 83,41\nMU 82,82,86\nMU 83,83,86\nZvav 136,140,104\nRa 188,2,15360\n"
"s_barrier\nCV 114,82,83\nAR 82,42\nAR 83,43\nMvav 136,136,96\nMU 82,82,86\nMU 83,83,86\nCV 115,82,83\nAR 82,44\nZvav 144,144,96\nRa 128,2,32768\nAR 83,45\nMU 82,82,87\n"
"MU 83,83,87\nMvav 144,148,104\nCV 118,82,83\nAR 82,46\nAR 83,47\nMU 82,82,87\nbuffer_store_dword v25, off, s[16:19], s54 offset:512 sc0 sc1\nZvav 148,148,108\nRa 132,2,33792\n"
"MU 83,83,87\nCV 119,82,83\nAR 82,48\nMvav 148,144,100\nWL 8\ns_add_u32 m0, s89, 114688\ns_add_u32 s46, s96, 1179648\nAR 83,49\nMU 82,82,86\nZvav 156,152,100\nRa 136,2,34816\n"
"MU 83,83,86\nCV 120,82,83\nAR 82,50\nMvav 156,156,108\nD1 1,16,46,0\nAR 83,51\nMU 82,82,86\nMU 83,83,86\nZvav 152,156,104\nRa 140,2,35840\nCV 121,82,83\nAR 82,52\nAR 83,53\n"
"Mvav 152,152,96\nMU 82,82,87\nMU 83,83,87\nCV 124,82,83\nAR 82,54\nZvav 64,160,96\nRa 144,2,36864\nAR 83,55\nMU 82,82,87\nMU 83,83,87\nCV 125,82,83\nMvav 64,164,104\nAR 82,56\n"
"AR 83,57\nMU 82,82,86\nMU 83,83,86\nZvav 68,164,108\nRa 148,2,37888\nCV 122,82,83\nAR 82,58\nAR 83,59\nMvav 68,160,100\nWL 8\nMU 82,82,86\nMU 83,83,86\nCV 123,82,83\n"
"Zvav 76,168,100\nRa 152,2,38912\nAR 82,60\nAR 83,61\nMU 82,82,87\nMvav 76,172,108\nD1 1,16,46,1024\nMU 83,83,87\nCV 126,82,83\nAR 82,62\nZvav 72,172,104\nRa 156,2,39936\nAR 83,63\n"
"MU 82,82,87\nMU 83,83,87\nCV 127,82,83\nMvav 72,168,96\nAR 82,64\nAR 83,65\nMU 82,82,86\nMU 83,83,86\nMvav 128,176,112\nRa 160,2,40960\nCV 32,82,83\nAR 82,66\nAR 83,67\n"
"Mvav 128,180,120\nMU 82,82,86\nMU 83,83,86\nCV 33,82,83\nAR 82,68\nMvav 132,180,124\nRa 164,2,41984\nAR 83,69\nMU 82,82,87\nMU 83,83,87\nMvav 132,176,116\nWL 8\nCV 36,82,83\n"
"AR 82,70\nAR 83,71\nMU 82,82,87\nMvav 140,184,116\nRa 168,2,43008\nMU 83,83,87\nCV 37,82,83\nAR 82,72\nMvav 140,188,124\nD1 1,16,46,2048\nAR 83,73\nMU 82,82,86\nMU 83,83,86\n"
"Mvav 136,188,120\nRa 172,2,44032\nCV 34,82,83\nAR 82,74\nAR 83,75\nMvav 136,184,112\nMU 82,82,86\nMU 83,83,86\nCV 35,82,83\nAR 82,76\nMvav 144,128,112\nRa 176,2,45056\nAR 83,77\n"
"MU 82,82,87\nMU 83,83,87\nCV 38,82,83\nMvav 144,132,120\nAR 82,78\nAR 83,79\nMU 82,82,87\nMU 83,83,87\nMvav 148,132,124\nRa 180,2,46080\nCV 39,82,83\nAR 82,80\nAR 83,81\n"
"Mvav 148,128,116\nWL 8\nMU 82,82,86\nMU 83,83,86\nCV 40,82,83\nMvav 156,136,116\nRa 184,2,47104\nAR 82,82\nAR 83,83\nMU 82,82,86\nMvav 156,140,124\nD1 1,16,46,3072\nMU 83,83,86\n"
"CV 41,82,83\nAR 82,84\nMvav 152,140,120\nRa 188,2,48128\nAR 83,85\nMU 82,82,87\nMU 83,83,87\nCV 44,82,83\nMvav 152,136,112\nAR 82,86\nAR 83,87\nMU 82,82,87\nMU 83,83,87\n"
"Mvav 64,144,112\nRa 128,2,49152\nCV 45,82,83\nAR 82,88\nAR 83,89\nMvav 64,148,120\nMU 82,82,86\nMU 83,83,86\nCV 42,82,83\nAR 82,90\nMvav 68,148,124\nRa 132,2,50176\nAR 83,91\n"
"MU 82,82,86\nMU 83,83,86\nMvav 68,144,116\nWL 8\ns_add_u32 m0, s89, 147456\ns_add_u32 s46, s96, 1245184\nCV 43,82,83\nAR 82,92\nMvav 76,152,116\nRa 136,2,51200\nAR 83,93\n"
"MU 82,82,87\nMU 83,83,87\nMvav 76,156,124\nD1 1,16,46,0\nCV 46,82,83\nAR 82,94\nAR 83,95\nMvav 72,156,120\nRa 140,2,52224\nMU 82,82,87\nMU 83,83,87\nCV 47,82,83\nMvav 72,152,112\n"
"AR 82,96\nAR 83,97\nMU 82,82,86\nMU 83,83,86\nMvav 128,160,32\nRa 144,2,53248\nCV 48,82,83\nAR 82,98\nAR 83,99\nMU 82,82,86\nMvav 128,164,40\nMU 83,83,86\nCV 49,82,83\nAR 82,100\n"
"AR 83,101\nMvav 132,164,44\nRa 148,2,54272\nMU 82,82,87\nMU 83,83,87\nCV 52,82,83\nMvav 132,160,36\nWL 8\nAR 82,102\nAR 83,103\nMU 82,82,87\nMvav 140,168,36\nRa 152,2,55296\n"
"MU 83,83,87\nCV 53,82,83\nAR 82,104\nMvav 140,172,44\nD1 1,16,46,1024\nAR 83,105\nMU 82,82,86\nMU 83,83,86\nMvav 136,172,40\nRa 156,2,56320\nCV 50,82,83\nAR 82,106\nAR 83,107\n"
"Mvav 136,168,32\nMU 82,82,86\nMU 83,83,86\nCV 51,82,83\nAR 82,108\nMvav 144,176,32\nRa 160,2,57344\nAR 83,109\nMU 82,82,87\nMU 83,83,87\nCV 54,82,83\nMvav 144,180,40\nAR 82,110\n"
"AR 83,111\nMU 82,82,87\nMU 83,83,87\nMvav 148,180,44\nRa 164,2,58368\nCV 55,82,83\nAR 82,112\nAR 83,113\nMvav 148,176,36\nWL 8\nMU 82,82,86\nMU 83,83,86\nCV 56,82,83\n"
"Mvav 156,184,36\nRa 168,2,59392\nAR 82,114\nAR 83,115\nMU 82,82,86\nMvav 156,188,44\nD1 1,16,46,2048\nMU 83,83,86\nCV 57,82,83\nAR 82,116\nMvav 152,188,40\nRa 172,2,60416\n"
"AR 83,117\nMU 82,82,87\nMU 83,83,87\nMvav 152,184,32\nCV 60,82,83\nAR 82,118\nAR 83,119\n.W568:\ns_cbranch_execz .WB152\nMU 82,82,87\nMvav 64,128,32\nRa 176,2,61440\nMU 83,83,87\n"
"CV 61,82,83\nAR 82,120\nAR 83,121\nMvav 64,132,40\nMU 82,82,86\nMU 83,83,86\nCV 58,82,83\nAR 82,122\nMvav 68,132,44\nRa 180,2,62464\nAR 83,123\nMU 82,82,86\nMU 83,83,86\n"
"Mvav 68,128,36\ns_waitcnt vmcnt(16) lgkmcnt(8)\nCV 59,82,83\nAR 82,124\nAR 83,125\nMvav 76,136,36\nRa 184,2,63488\nMU 82,82,87\nMU 83,83,87\nCV 62,82,83\nMvav 76,140,44\n"
"D1 1,16,46,3072\nAR 82,126\nAR 83,127\nMU 82,82,87\nMvav 72,140,40\nRa 188,2,64512\ns_barrier\nMU 83,83,87\nCV 63,82,83\nMvav 72,136,32\nMvav 128,144,48\nRa 128,3,0\n"
"Mvav 128,148,56\nMvav 132,148,60\nRa 132,3,1024\nMvav 132,144,52\nWL 8\nMvav 140,152,52\nRa 136,3,2048\nMvav 140,156,60\nMvav 136,156,56\nRa 140,3,3072\nMvav 136,152,48\n"
"Mvav 144,160,48\nRa 144,3,4096\nMvav 144,164,56\nMvav 148,164,60\nRa 148,3,5120\nCV 132,132,133\nMvav 148,160,52\nWL 8\nCV 133,134,135\nMvav 156,168,52\nRa 152,3,6144\n"
"CV 134,140,141\nMvav 156,172,60\nCV 135,142,143\ns_add_u32 s5, s94, 0\nMvav 152,172,56\nRa 156,3,7168\nSv 132,1,16,5,1024\nMvav 152,168,48\nCV 128,128,129\nCV 129,130,131\n"
"Mvav 64,176,48\nRa 160,3,8192\nCV 130,136,137\nMvav 64,180,56\nbuffer_load_dword v27, off, s[16:19], s60 offset:512 sc0 sc1\nCV 131,138,139\nMvav 68,180,60\nRa 164,3,9216\n"
"Sv 128,1,16,5,0\nMvav 68,176,52\nWL 6\nCV 148,148,149\nMvav 76,184,52\nRa 168,3,10240\nCV 149,150,151\nMvav 76,188,60\nCV 150,156,157\nCV 151,158,159\nMvav 72,188,56\n"
"Ra 172,3,11264\ns_add_u32 s5, s94, 131072\nMvav 72,184,48\nSv 148,1,16,5,1024\nCV 144,144,145\nZvav 160,128,96\nRa 176,3,12288\nCV 145,146,147\nMvav 160,132,104\nCV 146,152,153\n"
"CV 147,154,155\nMvav 160,136,112\nRa 180,3,13312\nSv 144,1,16,5,0\nMvav 160,140,120\nWL 6\nCV 68,68,69\nZvav 164,140,124\nRa 184,3,14336\nCV 69,70,71\nMvav 164,136,116\n"
"CV 70,76,77\nCV 71,78,79\nMvav 164,132,108\nRa 188,3,15360\ns_add_u32 s5, s94, 262144\nMvav 164,128,100\nSv 68,1,16,5,1024\nCV 64,64,65\nMvav 164,144,36\nRa 128,3,16384\n"
"CV 65,66,67\nMvav 164,148,44\nCV 66,72,73\nCV 67,74,75\nMvav 164,152,52\nRa 132,3,17408\nSv 64,1,16,5,0\nMvav 164,156,60\nWL 6\nMvav 160,156,56\nRa 136,3,18432\nMvav 160,152,48\n"
"Mvav 160,148,40\nRa 140,3,19456\nMvav 160,144,32\nZvav 168,160,96\nRa 144,3,20480\nMvav 168,164,104\nMvav 168,168,112\nRa 148,3,21504\nMvav 168,172,120\nWL 6\nZvav 172,172,124\n"
"Ra 152,3,22528\nMvav 172,168,116\nMvav 172,164,108\nRa 156,3,23552\nMvav 172,160,100\nMvav 172,176,36\nRa 160,3,24576\nMvav 172,180,44\nMvav 172,184,52\nRa 164,3,25600\n"
"Mvav 172,188,60\nWL 6\nMvav 168,188,56\nRa 168,3,26624\nMvav 168,184,48\nMvav 168,180,40\nRa 172,3,27648\nMvav 168,176,32\nZvav 176,128,96\nRa 176,3,28672\nCV 164,164,165\n"
"Mvav 176,132,104\nCV 165,166,167\nCV 166,172,173\nMvav 176,136,112\nRa 180,3,29696\nCV 167,174,175\nMvav 176,140,120\ns_waitcnt vmcnt(11) lgkmcnt(6)\ns_add_u32 s5, s94, 393216\n"
"Zvav 180,140,124\nRa 184,3,30720\nSv 164,1,16,5,1024\nMvav 180,136,116\nCV 160,160,161\nCV 161,162,163\nMvav 180,132,108\nRa 188,3,31744\ns_barrier\nMvav 180,128,100\n"
"CV 162,168,169\nCV 163,170,171\nMvav 180,144,36\nRa 128,3,32768\nSv 160,1,16,5,0\nMvav 180,148,44\nMvav 180,152,52\nRa 132,3,33792\nMvav 180,156,60\nWL 6\n"
"s_add_u32 m0, s89, 16384\ns_add_u32 s46, s96, 1277952\nMvav 176,156,56\nRa 136,3,34816\nMvav 176,152,48\nD1 1,16,46,0\nMvav 176,148,40\nRa 140,3,35840\nMvav 176,144,32\n"
"Zvav 184,160,96\nRa 144,3,36864\nMvav 184,164,104\nMvav 184,168,112\nRa 148,3,37888\nMvav 184,172,120\nWL 6\nZvav 188,172,124\nRa 152,3,38912\nMvav 188,168,116\nD1 1,16,46,1024\n"
"Mvav 188,164,108\nRa 156,3,39936\nMvav 188,160,100\nMvav 188,176,36\nRa 160,3,40960\nWV 9\nv_cmp_ne_u32 vcc, s15, v27\ns_nop 0\ns_cbranch_vccnz .S247\n.S248:\nMvav 188,180,44\n"
"s_add_u32 s4, s92, 0\nMvav 188,184,52\nRa 164,3,41984\nMvav 188,188,60\nWL 6\nbuffer_load_dwordx4 v[208:211], v1, s[16:19], s4 offen offset:0 sc0 sc1\nMvav 184,188,56\n"
"Ra 168,3,43008\nMvav 184,184,48\nD1 1,16,46,2048\nMvav 184,180,40\nRa 172,3,44032\nMvav 184,176,32\nbuffer_load_dwordx4 v[212:215], v1, s[16:19], s4 offen offset:1024 sc0 sc1\n"
"Zvav 192,128,96\nRa 176,3,45056\nv_lshrrev_b32 v88, 8, v1\nMvav 192,132,104\nv_lshlrev_b32 v89, 5, v88\nCV 180,180,181\nMvav 192,136,112\nRa 180,3,46080\nCV 181,182,183\n"
"Mvav 192,140,120\nWL 6\nCV 182,188,189\nZvav 196,140,124\nRa 184,3,47104\nv_bfe_u32 v90, v1, 4, 4\nMvav 196,136,116\nD1 1,16,46,3072\nCV 183,190,191\nMvav 196,132,108\n"
"Ra 188,3,48128\ns_add_u32 s5, s94, 524288\nMvav 196,128,100\nv_mul_u32_u24 v91, 896, v90\nSv 180,1,16,5,1024\nMvav 196,144,36\nRa 128,3,49152\nv_lshl_add_u32 v91, v88, 4, v91\n"
"Mvav 196,148,44\nv_add_u32 v90, s21, v90\nCV 176,176,177\nMvav 196,152,52\nRa 132,3,50176\nCV 177,178,179\nMvav 196,156,60\nWL 6\ns_add_u32 m0, s89, 0\n"
"s_add_u32 s46, s96, 1310720\nMvav 192,156,56\nRa 136,3,51200\nv_lshlrev_b32 v88, 3, v90\nMvav 192,152,48\nD1 1,16,46,0\nCV 178,184,185\nMvav 192,148,40\nRa 140,3,52224\n"
"CV 179,186,187\nMvav 192,144,32\nv_add_u32 v94, 64, v90\nSv 176,1,16,5,0\nZvav 200,160,96\nRa 144,3,53248\ns_mov_b32 s63, 0xc040000\nMvav 200,164,104\nv_lshrrev_b32 v92, 8, v1\n"
"Mvav 200,168,112\nRa 148,3,54272\nMvav 200,172,120\nWL 6\nZvav 204,172,124\nRa 152,3,55296\nv_bfe_u32 v93, v1, 6, 2\nMvav 204,168,116\nD1 1,16,46,1024\nv_cmp_eq_u32 vcc, v92, v93\n"
"v_and_b32 v92, 16, v1\nv_mov_b32 v93, 0x3f80\nv_cndmask_b32 v93, 0, v93, vcc\nMvav 204,164,108\nRa 156,3,56320\nMvav 204,160,100\nv_lshlrev_b32 v93, v92, v93\nMvav 204,176,36\n"
"Ra 160,3,57344\nv_and_b32 v92, 32, v1\nMvav 204,180,44\ns_add_u32 s4, s92, 131072\nv_cmp_eq_u32 vcc, 0, v92\nv_mov_b32 v4, 0\nv_mov_b32 v5, 0\nv_cndmask_b32 v6, 0, v93, vcc\n"
"Mvav 204,184,52\nRa 164,3,58368\nMvav 204,188,60\nWL 6\nbuffer_load_dwordx4 v[216:219], v1, s[16:19], s4 offen offset:0 sc0 sc1\nMvav 200,188,56\nRa 168,3,59392\n"
"v_sub_u32 v7, v93, v6\nMvav 200,184,48\nD1 1,16,46,2048\nMvav 200,180,40\nRa 172,3,60416\nMvav 200,176,32\n"
"buffer_load_dwordx4 v[220:223], v1, s[16:19], s4 offen offset:1024 sc0 sc1\nZvav 160,128,96\nRa 176,3,61440\nv_mov_b32 v8, 0\nMvav 160,132,104\nv_mov_b32 v9, 0\nCV 196,196,197\n"
"Mvav 160,136,112\nRa 180,3,62464\nCV 197,198,199\nMvav 160,140,120\nWL 6\nCV 198,204,205\nZvav 164,140,124\nRa 184,3,63488\nCV 199,206,207\nMvav 164,136,116\nD1 1,16,46,3072\n"
"s_add_u32 s5, s94, 655360\nMvav 164,132,108\nRa 188,3,64512\ns_barrier\nMvav 164,128,100\nSv 196,1,16,5,1024\nCV 192,192,193\nMvav 164,144,36\nRa 128,0,0\nCV 193,194,195\n"
"Mvav 164,148,44\nCV 194,200,201\nCV 195,202,203\nMvav 164,152,52\nRa 132,0,1024\nSv 192,1,16,5,0\nMvav 164,156,60\nWL 6\ns_add_u32 m0, s89, 32768\ns_add_u32 s46, s96, 1343488\n"
"Mvav 160,156,56\nRa 136,0,2048\nMvav 160,152,48\nD1 1,16,46,0\nMvav 160,148,40\nRa 140,0,3072\nMvav 160,144,32\nZvav 168,160,96\nRa 144,0,4096\nWV 12\nv_max_u32 v21, v211, v215\n"
"v_cmp_eq_u32 vcc, -1, v21\ns_nop 0\ns_cbranch_vccnz .S223\n.S224:\nMvav 168,164,104\nv_lshlrev_b32 v176, 16, v208\nMvav 168,168,112\nRa 148,0,5120\nMvav 168,172,120\nWL 6\n"
"v_and_b32 v177, 0xffff0000, v208\nZvav 172,172,124\nRa 152,0,6144\nMvav 172,168,116\nD1 1,16,46,1024\nMvav 172,164,108\nRa 156,0,7168\nv_lshlrev_b32 v178, 16, v209\n"
"Mvav 172,160,100\nv_and_b32 v179, 0xffff0000, v209\nMvav 172,176,36\nRa 160,0,8192\nv_lshlrev_b32 v180, 16, v212\nMvav 172,180,44\nv_and_b32 v181, 0xffff0000, v212\n"
"s_add_u32 s4, s92, 262144\nMvav 172,184,52\nRa 164,0,9216\nMvav 172,188,60\nWL 6\nv_lshlrev_b32 v182, 16, v213\n"
"buffer_load_dwordx4 v[224:227], v1, s[16:19], s4 offen offset:0 sc0 sc1\nMvav 168,188,56\nRa 168,0,10240\nMvav 168,184,48\nD1 1,16,46,2048\nMvav 168,180,40\nRa 172,0,11264\n"
"v_and_b32 v183, 0xffff0000, v213\nMvav 168,176,32\nv_lshlrev_b32 v184, 16, v210\nbuffer_load_dwordx4 v[228:231], v1, s[16:19], s4 offen offset:1024 sc0 sc1\nMvav 176,128,96\n"
"Ra 176,0,12288\nv_and_b32 v185, 0xffff0000, v210\nMvav 176,132,104\nv_lshlrev_b32 v186, 16, v211\nCV 164,164,165\nMvav 176,136,112\nRa 180,0,13312\nCV 165,166,167\n"
"Mvav 176,140,120\nWL 6\nCV 166,172,173\nMvav 180,140,124\nRa 184,0,14336\nv_and_b32 v187, 0xffff0000, v211\nMvav 180,136,116\nD1 1,16,46,3072\nCV 167,174,175\nMvav 180,132,108\n"
"Ra 188,0,15360\ns_add_u32 s5, s94, 786432\nMvav 180,128,100\nv_lshlrev_b32 v188, 16, v214\nSv 164,1,16,5,1024\nMvav 180,144,36\nRa 128,0,16384\nv_and_b32 v189, 0xffff0000, v214\n"
"Mvav 180,148,44\nv_lshlrev_b32 v190, 16, v215\nCV 160,160,161\nMvav 180,152,52\nRa 132,0,17408\nCV 161,162,163\nMvav 180,156,60\nWL 6\ns_add_u32 m0, s89, 49152\n"
"s_add_u32 s46, s96, 1376256\nMvav 176,156,56\nRa 136,0,18432\nv_and_b32 v191, 0xffff0000, v215\nMvav 176,152,48\nD1 1,16,46,0\nCV 162,168,169\nMvav 176,148,40\nRa 140,0,19456\n"
"CV 163,170,171\nMvav 176,144,32\nSv 160,1,16,5,0\nMvav 184,160,96\nRa 144,0,20480\nWV 12\nv_max_u32 v21, v219, v223\nv_cmp_eq_u32 vcc, -1, v21\ns_nop 0\ns_cbranch_vccnz .S226\n"
".S227:\nMvav 184,164,104\nv_lshlrev_b32 v192, 16, v216\nMvav 184,168,112\nRa 148,0,21504\nMvav 184,172,120\nWL 6\nv_and_b32 v193, 0xffff0000, v216\nMvav 188,172,124\n"
"Ra 152,0,22528\nMvav 188,168,116\nD1 1,16,46,1024\nMvav 188,164,108\nRa 156,0,23552\nv_lshlrev_b32 v194, 16, v217\nMvav 188,160,100\nv_and_b32 v195, 0xffff0000, v217\n"
"Mvav 188,176,36\nRa 160,0,24576\nv_lshlrev_b32 v196, 16, v220\nMvav 188,180,44\nv_and_b32 v197, 0xffff0000, v220\ns_add_u32 s4, s92, 393216\nMvav 188,184,52\nRa 164,0,25600\n"
"Mvav 188,188,60\nWL 6\nv_lshlrev_b32 v198, 16, v221\nbuffer_load_dwordx4 v[208:211], v1, s[16:19], s4 offen offset:0 sc0 sc1\nMvav 184,188,56\nRa 168,0,26624\nMvav 184,184,48\n"
"D1 1,16,46,2048\nMvav 184,180,40\nRa 172,0,27648\nv_and_b32 v199, 0xffff0000, v221\nMvav 184,176,32\nv_lshlrev_b32 v200, 16, v218\n"
"buffer_load_dwordx4 v[212:215], v1, s[16:19], s4 offen offset:1024 sc0 sc1\nMvav 192,128,96\nRa 176,0,28672\nv_and_b32 v201, 0xffff0000, v218\nMvav 192,132,104\n"
"v_lshlrev_b32 v202, 16, v219\nCV 180,180,181\nMvav 192,136,112\nRa 180,0,29696\nCV 181,182,183\nMvav 192,140,120\ns_waitcnt vmcnt(15) lgkmcnt(6)\nCV 182,188,189\nMvav 196,140,124\n"
"Ra 184,0,30720\nv_and_b32 v203, 0xffff0000, v219\nMvav 196,136,116\nD1 1,16,46,3072\nCV 183,190,191\nMvav 196,132,108\nRa 188,0,31744\ns_barrier\nMvav 196,128,100\n"
"v_lshlrev_b32 v204, 16, v222\ns_add_u32 s5, s94, 917504\nMvav 196,144,36\nRa 128,2,16384\nv_and_b32 v205, 0xffff0000, v222\nMvav 196,148,44\nv_lshlrev_b32 v206, 16, v223\n"
"Sv 180,1,16,5,1024\nMvav 196,152,52\nRa 132,2,17408\nCV 176,176,177\nMvav 196,156,60\nWL 6\ns_add_u32 m0, s89, 65536\ns_add_u32 s46, s96, 1409024\nMvav 192,156,56\nRa 136,2,18432\n"
"v_and_b32 v207, 0xffff0000, v223\nMvav 192,152,48\nD1 1,16,46,0\n.W569:\ns_cbranch_execz .WB152\nCV 177,178,179\nMvav 192,148,40\nRa 140,2,19456\nCV 178,184,185\nMvav 192,144,32\n"
"CV 179,186,187\nSv 176,1,16,5,0\nMvav 200,160,96\nRa 144,2,20480\nWV 12\nv_max_u32 v21, v227, v231\nv_cmp_eq_u32 vcc, -1, v21\ns_nop 0\ns_cbranch_vccnz .S229\n.S230:\n"
"Mvav 200,164,104\nv_lshlrev_b32 v160, 16, v224\nMvav 200,168,112\nRa 148,2,21504\nMvav 200,172,120\nWL 6\nv_and_b32 v161, 0xffff0000, v224\nMvav 204,172,124\nRa 152,2,22528\n"
"Mvav 204,168,116\nD1 1,16,46,1024\nMvav 204,164,108\nRa 156,2,23552\nv_lshlrev_b32 v162, 16, v225\nMvav 204,160,100\nv_and_b32 v163, 0xffff0000, v225\nMvav 204,176,36\n"
"Ra 160,2,24576\nv_lshlrev_b32 v164, 16, v228\nMvav 204,180,44\nv_and_b32 v165, 0xffff0000, v228\ns_add_u32 s4, s92, 524288\nMvav 204,184,52\nRa 164,2,25600\nMvav 204,188,60\nWL 6\n"
"v_lshlrev_b32 v166, 16, v229\nbuffer_load_dwordx4 v[216:219], v1, s[16:19], s4 offen offset:0 sc0 sc1\nMvav 200,188,56\nRa 168,2,26624\nMvav 200,184,48\nD1 1,16,46,2048\n"
"Mvav 200,180,40\nRa 172,2,27648\nv_and_b32 v167, 0xffff0000, v229\nMvav 200,176,32\nv_lshlrev_b32 v168, 16, v226\n"
"buffer_load_dwordx4 v[220:223], v1, s[16:19], s4 offen offset:1024 sc0 sc1\nMvav 160,128,96\nRa 176,2,28672\nv_and_b32 v169, 0xffff0000, v226\nMvav 160,132,104\n"
"v_lshlrev_b32 v170, 16, v227\nCV 196,196,197\nMvav 160,136,112\nRa 180,2,29696\nCV 197,198,199\nMvav 160,140,120\nWL 6\nCV 198,204,205\nMvav 164,140,124\nRa 184,2,30720\n"
"v_and_b32 v24, 0x70, v1\nMvav 164,136,116\nD1 1,16,46,3072\nCV 199,206,207\nMvav 164,132,108\nRa 188,2,31744\nv_and_b32 v171, 0xffff0000, v227\nMvav 164,128,100\n"
"v_lshlrev_b32 v172, 16, v230\ns_add_u32 s5, s94, 1048576\nMvav 164,144,36\nRa 128,2,0\nv_lshlrev_b32 v24, 5, v24\nMvav 164,148,44\nv_and_b32 v173, 0xffff0000, v230\n"
"Sv 196,1,16,5,1024\nMvav 164,152,52\nRa 132,2,1024\nCV 192,192,193\nMvav 164,156,60\nWL 6\nv_lshlrev_b32 v174, 16, v231\nMvav 160,156,56\nRa 136,2,2048\n"
"buffer_load_dword v27, v24, s[16:19], s41 offen sc0 sc1\nMvav 160,152,48\nv_and_b32 v175, 0xffff0000, v231\nCV 193,194,195\nMvav 160,148,40\nRa 140,2,3072\nCV 194,200,201\n"
"Mvav 160,144,32\nCV 195,202,203\nSv 192,1,16,5,0\nMvav 168,160,96\nRa 144,2,4096\nWV 12\nv_max_u32 v21, v211, v215\nv_cmp_eq_u32 vcc, -1, v21\ns_nop 0\ns_cbranch_vccnz .S232\n"
".S233:\nMvav 168,164,104\nv_lshlrev_b32 v176, 16, v208\nMvav 168,168,112\nRa 148,2,5120\nMvav 168,172,120\nWL 6\nMvav 172,172,124\nRa 152,2,6144\nv_and_b32 v177, 0xffff0000, v208\n"
"Mvav 172,168,116\nv_lshlrev_b32 v178, 16, v209\nMvav 172,164,108\nRa 156,2,7168\nMvav 172,160,100\nv_and_b32 v179, 0xffff0000, v209\nMvav 172,176,36\nRa 160,2,8192\n"
"v_lshlrev_b32 v180, 16, v212\nMvav 172,180,44\nv_and_b32 v181, 0xffff0000, v212\ns_add_u32 s4, s92, 655360\nMvav 172,184,52\nRa 164,2,9216\nMvav 172,188,60\nWL 6\n"
"buffer_load_dwordx4 v[224:227], v1, s[16:19], s4 offen offset:0 sc0 sc1\nMvav 168,188,56\nRa 168,2,10240\nv_lshlrev_b32 v182, 16, v213\nMvav 168,184,48\n"
"v_and_b32 v183, 0xffff0000, v213\nMvav 168,180,40\nRa 172,2,11264\nMvav 168,176,32\nv_lshlrev_b32 v184, 16, v210\n"
"buffer_load_dwordx4 v[228:231], v1, s[16:19], s4 offen offset:1024 sc0 sc1\nMvav 176,128,96\nRa 176,2,12288\nv_and_b32 v185, 0xffff0000, v210\nMvav 176,132,104\n"
"v_lshlrev_b32 v186, 16, v211\nCV 164,164,165\nMvav 176,136,112\nRa 180,2,13312\nCV 165,166,167\nMvav 176,140,120\ns_waitcnt vmcnt(13) lgkmcnt(6)\nCV 166,172,173\nMvav 180,140,124\n"
"Ra 184,2,14336\nCV 167,174,175\nMvav 180,136,116\nv_and_b32 v187, 0xffff0000, v211\ns_add_u32 s5, s94, 1179648\nMvav 180,132,108\nRa 188,2,15360\ns_barrier\nMvav 180,128,100\n"
"v_lshlrev_b32 v188, 16, v214\nSv 164,1,16,5,1024\nMvav 180,144,36\nRa 128,2,32768\nv_and_b32 v189, 0xffff0000, v214\nMvav 180,148,44\nv_lshlrev_b32 v190, 16, v215\nCV 160,160,161\n"
"Mvav 180,152,52\nRa 132,2,33792\nCV 161,162,163\nMvav 180,156,60\nWL 6\nCV 162,168,169\nMvav 176,156,56\nRa 136,2,34816\nCV 163,170,171\nMvav 176,152,48\n"
"v_and_b32 v191, 0xffff0000, v215\nSv 160,1,16,5,0\nMvav 176,148,40\nRa 140,2,35840\nMvav 176,144,32\nMvav 184,160,96\nRa 144,2,36864\nWV 8\nv_max_u32 v21, v219, v223\n"
"v_cmp_eq_u32 vcc, -1, v21\ns_nop 0\ns_cbranch_vccnz .S235\n.S236:\nMvav 184,164,104\nv_lshlrev_b32 v192, 16, v216\nMvav 184,168,112\nRa 148,2,37888\nMvav 184,172,120\nWL 6\n"
"Mvav 188,172,124\nRa 152,2,38912\nv_and_b32 v193, 0xffff0000, v216\nMvav 188,168,116\nv_lshlrev_b32 v194, 16, v217\nMvav 188,164,108\nRa 156,2,39936\nMvav 188,160,100\n"
"v_and_b32 v195, 0xffff0000, v217\nMvav 188,176,36\nRa 160,2,40960\nv_lshlrev_b32 v196, 16, v220\nMvav 188,180,44\nv_and_b32 v197, 0xffff0000, v220\ns_add_u32 s4, s92, 786432\n"
"Mvav 188,184,52\nRa 164,2,41984\nMvav 188,188,60\nWL 6\nbuffer_load_dwordx4 v[208:211], v1, s[16:19], s4 offen offset:0 sc0 sc1\nMvav 184,188,56\nRa 168,2,43008\n"
"v_lshlrev_b32 v198, 16, v221\nMvav 184,184,48\nv_and_b32 v199, 0xffff0000, v221\nMvav 184,180,40\nRa 172,2,44032\nMvav 184,176,32\nv_lshlrev_b32 v200, 16, v218\n"
"buffer_load_dwordx4 v[212:215], v1, s[16:19], s4 offen offset:1024 sc0 sc1\nMvav 192,128,96\nRa 176,2,45056\nv_and_b32 v201, 0xffff0000, v218\nMvav 192,132,104\n"
"v_lshlrev_b32 v202, 16, v219\nCV 180,180,181\nMvav 192,136,112\nRa 180,2,46080\nCV 181,182,183\nMvav 192,140,120\nWL 6\nCV 182,188,189\nMvav 196,140,124\nRa 184,2,47104\n"
"CV 183,190,191\nMvav 196,136,116\nv_and_b32 v203, 0xffff0000, v219\ns_add_u32 s5, s94, 1310720\nMvav 196,132,108\nRa 188,2,48128\nSv 180,1,16,5,1024\nMvav 196,128,100\n"
"v_lshlrev_b32 v204, 16, v222\nCV 176,176,177\nMvav 196,144,36\nRa 128,2,49152\nv_and_b32 v205, 0xffff0000, v222\nMvav 196,148,44\nv_lshlrev_b32 v206, 16, v223\nCV 177,178,179\n"
"Mvav 196,152,52\nRa 132,2,50176\nCV 178,184,185\nMvav 196,156,60\nWL 6\nCV 179,186,187\nMvav 192,156,56\nRa 136,2,51200\nSv 176,1,16,5,0\nMvav 192,152,48\n"
"v_and_b32 v207, 0xffff0000, v223\nMvav 192,148,40\nRa 140,2,52224\nMvav 192,144,32\nMvav 200,160,96\nRa 144,2,53248\nWV 6\nv_max_u32 v21, v227, v231\nv_cmp_eq_u32 vcc, -1, v21\n"
"s_nop 0\ns_cbranch_vccnz .S238\n.S239:\nMvav 200,164,104\nv_lshlrev_b32 v160, 16, v224\nv_xor_b32 v27, s15, v27\nv_and_b32 v26, 0xffffdfff, v27\nv_cmp_ne_u32 s[64:65], 0, v27\n"
"v_cmp_ne_u32 vcc, 0, v26\ns_nop 0\ns_cbranch_vccnz .S250\n.S251:\nMvav 200,168,112\nRa 148,2,54272\ns_bitcmp1_b32 s64, 0\ns_cselect_b32 s2, 0x2000, 0\nMvav 200,172,120\nWL 6\n"
"Mvav 204,172,124\nRa 152,2,55296\nv_and_b32 v161, 0xffff0000, v224\nMvav 204,168,116\nv_lshlrev_b32 v162, 16, v225\nMvav 204,164,108\nRa 156,2,56320\ns_xor_b32 s2, s2, s20\n"
"Mvav 204,160,100\nv_and_b32 v163, 0xffff0000, v225\nMvav 204,176,36\nRa 160,2,57344\nv_lshlrev_b32 v164, 16, v228\nMvav 204,180,44\nv_and_b32 v165, 0xffff0000, v228\n"
"Mvav 204,184,52\nRa 164,2,58368\nMvav 204,188,60\nWL 6\ns_add_u32 s24, s2, 0x0\nMvav 200,188,56\nRa 168,2,59392\nv_lshlrev_b32 v166, 16, v229\nMvav 200,184,48\n"
"v_and_b32 v167, 0xffff0000, v229\ns_bitcmp1_b32 s64, 1\ns_cselect_b32 s2, 0x2000, 0\nMvav 200,180,40\nRa 172,2,60416\nMvav 200,176,32\nv_lshlrev_b32 v168, 16, v226\n"
"Mvav 160,128,96\nRa 176,2,61440\nv_and_b32 v169, 0xffff0000, v226\nMvav 160,132,104\nv_lshlrev_b32 v170, 16, v227\nCV 196,196,197\nMvav 160,136,112\nRa 180,2,62464\n"
"CV 197,198,199\nMvav 160,140,120\nWL 6\nCV 198,204,205\nMvav 164,140,124\nRa 184,2,63488\nCV 199,206,207\nMvav 164,136,116\nv_and_b32 v171, 0xffff0000, v227\n"
"s_add_u32 s5, s94, 1441792\nMvav 164,132,108\nRa 188,2,64512\ns_barrier\nMvav 164,128,100\nv_lshlrev_b32 v172, 16, v230\nSv 196,1,16,5,1024\nMvav 164,144,36\nRa 128,3,0\n"
"v_and_b32 v173, 0xffff0000, v230\nMvav 164,148,44\nv_lshlrev_b32 v174, 16, v231\nCV 192,192,193\nMvav 164,152,52\nRa 132,3,1024\nCV 193,194,195\nMvav 164,156,60\nWL 6\n"
"s_xor_b32 s2, s2, s20\nMvav 160,156,56\nRa 136,3,2048\nCV 194,200,201\nMvav 160,152,48\nv_and_b32 v175, 0xffff0000, v231\nCV 195,202,203\nMvav 160,148,40\nRa 140,3,3072\n"
"Sv 192,1,16,5,0\nMvav 160,144,32\ns_add_u32 s25, s2, 0x1800000\ns_bitcmp1_b32 s64, 2\ns_cselect_b32 s2, 0x2000, 0\nMvav 168,160,96\nRa 144,3,4096\nWV 4\nv_max_u32 v21, v211, v215\n"
"v_cmp_eq_u32 vcc, -1, v21\ns_nop 0\ns_cbranch_vccnz .S241\n.S242:\nMvav 168,164,104\nv_lshlrev_b32 v176, 16, v208\nMvav 168,168,112\nRa 148,3,5120\nMvav 168,172,120\nWL 6\n"
"Mvav 172,172,124\nRa 152,3,6144\nv_and_b32 v177, 0xffff0000, v208\nMvav 172,168,116\nv_lshlrev_b32 v178, 16, v209\nMvav 172,164,108\nRa 156,3,7168\ns_xor_b32 s2, s2, s20\n"
"Mvav 172,160,100\nv_and_b32 v179, 0xffff0000, v209\nMvav 172,176,36\nRa 160,3,8192\nv_lshlrev_b32 v180, 16, v212\nMvav 172,180,44\nv_and_b32 v181, 0xffff0000, v212\n"
"Mvav 172,184,52\nRa 164,3,9216\nMvav 172,188,60\nWL 6\ns_add_u32 s26, s2, 0x3000000\nMvav 168,188,56\nRa 168,3,10240\nv_lshlrev_b32 v182, 16, v213\nMvav 168,184,48\n"
"v_and_b32 v183, 0xffff0000, v213\ns_bitcmp1_b32 s64, 3\ns_cselect_b32 s2, 0x2000, 0\nMvav 168,180,40\nRa 172,3,11264\nMvav 168,176,32\nv_lshlrev_b32 v184, 16, v210\n"
"Mvav 176,128,96\nRa 176,3,12288\ns_xor_b32 s2, s2, s20\nMvav 176,132,104\nv_and_b32 v185, 0xffff0000, v210\nCV 164,164,165\nMvav 176,136,112\nRa 180,3,13312\nCV 165,166,167\n"
"Mvav 176,140,120\nWL 6\nCV 166,172,173\nMvav 180,140,124\nRa 184,3,14336\nCV 167,174,175\nMvav 180,136,116\nv_lshlrev_b32 v186, 16, v211\ns_add_u32 s5, s94, 1572864\n"
"Mvav 180,132,108\nRa 188,3,15360\ns_add_u32 s27, s2, 0x4800000\nMvav 180,128,100\nv_and_b32 v187, 0xffff0000, v211\ns_bitcmp1_b32 s64, 4\ns_cselect_b32 s2, 0x2000, 0\n"
"Mvav 180,144,36\nv_lshlrev_b32 v188, 16, v214\nSv 164,1,16,5,1024\nMvav 180,148,44\nv_and_b32 v189, 0xffff0000, v214\nCV 160,160,161\nMvav 180,152,52\n"
"v_lshlrev_b32 v190, 16, v215\nCV 161,162,163\nMvav 180,156,60\nWL 4\ns_xor_b32 s2, s2, s20\nMvav 176,156,56\nv_and_b32 v191, 0xffff0000, v215\nCV 162,168,169\nMvav 176,152,48\n"
"s_add_u32 s32, s2, 0x6000000\ns_bitcmp1_b32 s64, 5\ns_cselect_b32 s2, 0x2000, 0\nMvav 176,148,40\ns_xor_b32 s2, s2, s20\nCV 163,170,171\nMvav 176,144,32\n"
"s_add_u32 s33, s2, 0x7800000\ns_bitcmp1_b32 s64, 6\ns_cselect_b32 s2, 0x2000, 0\nMvav 184,160,96\ns_xor_b32 s2, s2, s20\ns_add_u32 s34, s2, 0x9000000\nMvav 184,164,104\n"
"s_bitcmp1_b32 s64, 7\ns_cselect_b32 s2, 0x2000, 0\nMvav 184,168,112\ns_xor_b32 s2, s2, s20\nSv 160,1,16,5,0\nMvav 184,172,120\nWL 0\nMvav 188,172,124\n"
"s_add_u32 s35, s2, 0xa800000\nMvav 188,168,116\nMvav 188,164,108\nMvav 188,160,100\nMvav 188,176,36\nMvav 188,180,44\nMvav 188,184,52\nMvav 188,188,60\nMvav 184,188,56\n"
"Mvav 184,184,48\nMvav 184,180,40\nMvav 184,176,32\nCV 180,180,181\nCV 181,182,183\ns_nop 1\nCV 182,188,189\nCV 183,190,191\ns_add_u32 s5, s94, 1703936\nSv 180,1,16,5,1024\n"
"CV 176,176,177\nCV 177,178,179\nCV 178,184,185\nCV 179,186,187\nSv 176,1,16,5,0\ns_cmp_eq_u32 s13, 0\ns_cbranch_scc1 .Z253\n"
"buffer_load_dwordx4 v[12:15], v91, s[28:31], s22 offen offset:0\nbuffer_load_dwordx4 v[16:19], v91, s[28:31], s23 offen offset:0\n"
"buffer_load_dwordx4 v[64:67], v89, s[16:19], s40 offen offset:0 sc0 sc1\nbuffer_load_dwordx4 v[68:71], v89, s[16:19], s40 offen offset:16 sc0 sc1\n"
"buffer_load_dwordx4 v[72:75], v89, s[16:19], s40 offen offset:1792 sc0 sc1\nbuffer_load_dwordx4 v[76:79], v89, s[16:19], s40 offen offset:1808 sc0 sc1\n"
"buffer_load_dwordx2 v[80:81], v88, s[16:19], s63 offen offset:0 sc0 sc1\nbuffer_load_dwordx2 v[82:83], v88, s[16:19], s63 offen offset:512 sc0 sc1\n"
"buffer_load_dwordx4 v[96:99], v1, s[16:19], s24 offen offset:0 sc0 sc1\nbuffer_load_dwordx4 v[100:103], v1, s[16:19], s24 offen offset:1024 sc0 sc1\n"
"buffer_load_dwordx4 v[104:107], v1, s[16:19], s25 offen offset:0 sc0 sc1\nbuffer_load_dwordx4 v[108:111], v1, s[16:19], s25 offen offset:1024 sc0 sc1\n"
"buffer_load_dwordx4 v[112:115], v1, s[16:19], s26 offen offset:0 sc0 sc1\nbuffer_load_dwordx4 v[116:119], v1, s[16:19], s26 offen offset:1024 sc0 sc1\n"
"buffer_load_dwordx4 v[120:123], v1, s[16:19], s27 offen offset:0 sc0 sc1\nbuffer_load_dwordx4 v[124:127], v1, s[16:19], s27 offen offset:1024 sc0 sc1\n"
"buffer_load_dwordx4 v[32:35], v1, s[16:19], s32 offen offset:0 sc0 sc1\nbuffer_load_dwordx4 v[36:39], v1, s[16:19], s32 offen offset:1024 sc0 sc1\n"
"buffer_load_dwordx4 v[40:43], v1, s[16:19], s33 offen offset:0 sc0 sc1\nbuffer_load_dwordx4 v[44:47], v1, s[16:19], s33 offen offset:1024 sc0 sc1\n"
"buffer_load_dwordx4 v[48:51], v1, s[16:19], s34 offen offset:0 sc0 sc1\nbuffer_load_dwordx4 v[52:55], v1, s[16:19], s34 offen offset:1024 sc0 sc1\n"
"buffer_load_dwordx4 v[56:59], v1, s[16:19], s35 offen offset:0 sc0 sc1\nbuffer_load_dwordx4 v[60:63], v1, s[16:19], s35 offen offset:1024 sc0 sc1\nWV 16\nv_xor_b32 v66, s15, v66\n"
"v_xor_b32 v70, s15, v70\nv_xor_b32 v74, s15, v74\nv_xor_b32 v78, s15, v78\nv_xor_b32 v81, s15, v81\nv_xor_b32 v83, s15, v83\nv_or3_b32 v92, v66, v70, v74\n"
"v_or3_b32 v92, v92, v78, v81\nv_or_b32 v92, v92, v83\nv_cmp_ne_u32 vcc, 0, v92\ns_nop 0\ns_cbranch_vccnz .S244\n.S245:\nv_and_b32 v92, 0x7ff, v67\nv_lshrrev_b32 v93, 16, v67\n"
"v_cmp_eq_u32 s[44:45], v92, v90\nv_cmp_eq_u32 s[46:47], v93, v90\nv_cmp_eq_u32 s[48:49], v92, v94\nv_cmp_eq_u32 s[50:51], v93, v94\nv_cndmask_b32 v12, v12, v64, s[44:45]\n"
"v_cndmask_b32 v13, v13, v65, s[44:45]\nv_cndmask_b32 v14, v14, v68, s[44:45]\nv_cndmask_b32 v15, v15, v69, s[44:45]\nv_cndmask_b32 v12, v12, v72, s[46:47]\n"
"v_cndmask_b32 v13, v13, v73, s[46:47]\nv_cndmask_b32 v14, v14, v76, s[46:47]\nv_cndmask_b32 v15, v15, v77, s[46:47]\nv_cndmask_b32 v16, v16, v64, s[48:49]\n"
"v_cndmask_b32 v17, v17, v65, s[48:49]\nv_cndmask_b32 v18, v18, v68, s[48:49]\nv_cndmask_b32 v19, v19, v69, s[48:49]\nv_cndmask_b32 v16, v16, v72, s[50:51]\n"
"v_cndmask_b32 v17, v17, v73, s[50:51]\nv_cndmask_b32 v18, v18, v76, s[50:51]\nv_cndmask_b32 v19, v19, v77, s[50:51]\nv_sub_f32 v92, 1.0, v80\nv_sub_f32 v93, 1.0, v82\nCV 92,92,93\n"
"v_and_b32 v93, 0xffff0000, v92\nv_lshlrev_b32 v92, 16, v92\nv_lshlrev_b32 v64, 16, v12\nv_and_b32 v65, 0xffff0000, v12\nMU 64,64,92\nMU 65,65,92\n.W570:\ns_cbranch_execz .WB152\n"
"v_lshlrev_b32 v66, 16, v13\nv_and_b32 v67, 0xffff0000, v13\nMU 66,66,92\nMU 67,67,92\nv_lshlrev_b32 v68, 16, v14\nv_and_b32 v69, 0xffff0000, v14\nMU 68,68,92\nMU 69,69,92\n"
"v_lshlrev_b32 v70, 16, v15\nv_and_b32 v71, 0xffff0000, v15\nMU 70,70,92\nMU 71,71,92\nv_lshlrev_b32 v72, 16, v16\nv_and_b32 v73, 0xffff0000, v16\nMU 72,72,93\nMU 73,73,93\n"
"v_lshlrev_b32 v74, 16, v17\nv_and_b32 v75, 0xffff0000, v17\nMU 74,74,93\nMU 75,75,93\nv_lshlrev_b32 v76, 16, v18\nv_and_b32 v77, 0xffff0000, v18\nMU 76,76,93\nMU 77,77,93\n"
"v_lshlrev_b32 v78, 16, v19\nv_and_b32 v79, 0xffff0000, v19\nMU 78,78,93\nMU 79,79,93\ns_mov_b32 s7, 262144\nWV 0\n.Z256:\nv_max_u32 v84, v99, v103\nv_cmp_eq_u32 s[44:45], -1, v84\n"
"v_max_u32 v84, v107, v111\nv_cmp_eq_u32 s[46:47], -1, v84\nv_max_u32 v84, v115, v119\nv_cmp_eq_u32 s[48:49], -1, v84\nv_max_u32 v84, v123, v127\nv_cmp_eq_u32 s[50:51], -1, v84\n"
"v_max_u32 v84, v35, v39\nv_cmp_eq_u32 s[52:53], -1, v84\nv_max_u32 v84, v43, v47\nv_cmp_eq_u32 s[54:55], -1, v84\nv_max_u32 v84, v51, v55\nv_cmp_eq_u32 s[56:57], -1, v84\n"
"v_max_u32 v84, v59, v63\nv_cmp_eq_u32 s[58:59], -1, v84\ns_or_b64 s[0:1], s[44:45], s[46:47]\ns_or_b64 s[2:3], s[48:49], s[50:51]\ns_or_b64 s[0:1], s[0:1], s[52:53]\n"
"s_or_b64 s[2:3], s[2:3], s[54:55]\ns_or_b64 s[0:1], s[0:1], s[56:57]\ns_or_b64 s[2:3], s[2:3], s[58:59]\ns_or_b64 s[0:1], s[0:1], s[2:3]\ns_cbranch_scc1 .Z254\n.Z255:\n"
"Zvvv 136,6,96\nMvvv 136,6,104\nMvvv 136,6,112\nMvvv 136,6,120\nMvvv 136,6,32\nMvvv 136,6,40\nMvvv 136,6,48\nMvvv 136,6,56\nZvvv 140,4,96\nMvvv 140,4,104\nMvvv 140,4,112\n"
"Mvvv 140,4,120\nMvvv 140,4,32\nMvvv 140,4,40\nMvvv 140,4,48\nMvvv 140,4,56\nZvvv 144,6,100\nMvvv 144,6,108\nMvvv 144,6,116\nMvvv 144,6,124\nMvvv 144,6,36\nMvvv 144,6,44\n"
"Mvvv 144,6,52\nMvvv 144,6,60\nZvvv 148,4,100\nMvvv 148,4,108\nMvvv 148,4,116\nMvvv 148,4,124\nMvvv 148,4,36\nMvvv 148,4,44\nMvvv 148,4,52\nMvvv 148,4,60\nFM 64,80,136\n"
"FM 65,80,137\nFM 66,80,138\nFM 67,80,139\nFM 68,80,140\nFM 69,80,141\nFM 70,80,142\nFM 71,80,143\nCV 12,64,65\nCV 13,66,67\nCV 14,68,69\nCV 15,70,71\nSv 12,91,36,22,0\n"
"FM 72,82,144\nFM 73,82,145\nFM 74,82,146\nFM 75,82,147\nFM 76,82,148\nFM 77,82,149\nFM 78,82,150\nFM 79,82,151\nCV 16,72,73\nCV 17,74,75\nCV 18,76,77\nCV 19,78,79\n"
"Sv 16,91,36,23,0\n.Z253:\ns_endpgm\n.EP265:\n.S145:\ns_mov_b32 s7, 262144\n.S147:\nbuffer_load_dwordx4 v[32:35], v1, s[16:19], s6 offen offset:0 sc0 sc1\n"
"buffer_load_dwordx4 v[36:39], v1, s[16:19], s6 offen offset:1024 sc0 sc1\nbuffer_load_dwordx4 v[40:43], v1, s[16:19], s6 offen offset:2048 sc0 sc1\n"
"buffer_load_dwordx4 v[44:47], v1, s[16:19], s6 offen offset:3072 sc0 sc1\nWV 0\nv_xor_b32 v48, s15, v33\nv_xor_b32 v49, s15, v35\nv_xor_b32 v50, s15, v37\nv_xor_b32 v51, s15, v39\n"
"v_xor_b32 v52, s15, v41\nv_xor_b32 v53, s15, v43\nv_xor_b32 v54, s15, v45\nv_xor_b32 v55, s15, v47\nv_or3_b32 v48, v48, v49, v50\nv_or3_b32 v51, v51, v52, v53\n"
"v_or3_b32 v48, v48, v54, v55\nv_or_b32 v48, v48, v51\nv_cmp_ne_u32 vcc, 0, v48\ns_nop 0\ns_cbranch_vccz .S146\ns_sleep 6\ns_sub_u32 s7, s7, 1\ns_cbranch_scc0 .S147\n"
"s_branch .S146\n.S148:\ns_mov_b32 s7, 262144\n.S150:\ns_lshl_b32 s5, s73, 8\ns_add_u32 s5, s5, 0xc04ac00\nv_lshrrev_b32 v27, 2, v1\n"
"buffer_load_dword v27, v27, s[16:19], s5 offen sc0 sc1\nWV 0\nv_xor_b32 v27, s15, v27\nv_and_b32 v255, 0xffffdfff, v27\nv_cmp_ne_u32 vcc, 0, v255\ns_nop 0\ns_cbranch_vccz .S149\n"
"s_sleep 6\ns_sub_u32 s7, s7, 1\ns_cbranch_scc0 .S150\ns_branch .S149\n.T154:\ns_mov_b32 s62, 1\ns_call_b64 s[80:81], .RA10\ns_branch .T155\n.T156:\ns_mov_b32 s62, 1\n"
"s_call_b64 s[80:81], .RA10\ns_branch .T157\n.T159:\ns_mov_b32 s62, 0\ns_call_b64 s[80:81], .RB11\ns_branch .T160\n.T161:\ns_mov_b32 s62, 0\ns_call_b64 s[80:81], .RA10\n"
"s_branch .T162\n.T164:\ns_mov_b32 s62, 0\ns_call_b64 s[80:81], .RB11\ns_branch .T165\n.T166:\ns_mov_b32 s62, 0\ns_call_b64 s[80:81], .RA10\ns_branch .T167\n.S169:\n"
"s_mov_b32 s7, 262144\n.S171:\nv_and_b32 v27, 0x70, v1\nbuffer_load_dword v27, v27, s[16:19], s61 offen offset:128 sc0 sc1\nWV 0\nv_cmp_ne_u32 vcc, s15, v27\ns_nop 0\n"
"s_cbranch_vccz .S170\ns_sleep 6\ns_sub_u32 s7, s7, 1\ns_cbranch_scc0 .S171\ns_branch .S170\n.S172:\ns_mov_b32 s7, 262144\n.S174:\nv_and_b32 v27, 0x70, v1\n"
"buffer_load_dword v27, v27, s[16:19], s61 offen offset:136 sc0 sc1\nWV 0\nv_cmp_ne_u32 vcc, s15, v27\ns_nop 0\ns_cbranch_vccz .S173\ns_sleep 6\ns_sub_u32 s7, s7, 1\n"
"s_cbranch_scc0 .S174\ns_branch .S173\n.S175:\ns_mov_b32 s7, 262144\n.S177:\nv_and_b32 v27, 0x70, v1\nbuffer_load_dword v27, v27, s[16:19], s61 offen offset:140 sc0 sc1\nWV 0\n"
"v_cmp_ne_u32 vcc, s15, v27\ns_nop 0\ns_cbranch_vccz .S176\ns_sleep 6\ns_sub_u32 s7, s7, 1\ns_cbranch_scc0 .S177\ns_branch .S176\n.S178:\ns_mov_b32 s7, 262144\n.S180:\n"
"v_and_b32 v27, 0x70, v1\nbuffer_load_dword v27, v27, s[16:19], s61 offen offset:12 sc0 sc1\nWV 0\nv_cmp_ne_u32 vcc, s15, v27\ns_nop 0\ns_cbranch_vccz .S179\ns_sleep 6\n"
"s_sub_u32 s7, s7, 1\ns_cbranch_scc0 .S180\ns_branch .S179\n.S181:\ns_mov_b32 s7, 262144\n.S183:\nv_and_b32 v27, 0x70, v1\n"
"buffer_load_dword v27, v27, s[16:19], s61 offen offset:8 sc0 sc1\nWV 0\nv_cmp_ne_u32 vcc, s15, v27\ns_nop 0\ns_cbranch_vccz .S182\ns_sleep 6\ns_sub_u32 s7, s7, 1\n"
"s_cbranch_scc0 .S183\ns_branch .S182\n.S184:\ns_mov_b32 s7, 262144\n.S186:\nv_and_b32 v27, 0xf0, v1\nbuffer_load_dword v27, v27, s[16:19], s61 offen offset:4 sc0 sc1\nWV 0\n"
"v_cmp_ne_u32 vcc, s15, v27\ns_nop 0\ns_cbranch_vccz .S185\ns_sleep 6\ns_sub_u32 s7, s7, 1\ns_cbranch_scc0 .S186\ns_branch .S185\n.S188:\ns_mov_b32 s7, 262144\n.S190:\n"
"v_lshrrev_b32 v144, 9, v1\nv_mul_u32_u24 v144, 0x18000, v144\nv_lshlrev_b32 v144, 8, v144\nv_and_b32 v145, 0x1f0, v1\nv_add_u32 v144, v144, v145\ns_mov_b32 s6, 0x15c0000\n"
"buffer_load_dword v136, v144, s[16:19], s6 offen offset:4 sc0 sc1\ns_mov_b32 s6, 0x15c2000\nbuffer_load_dword v137, v144, s[16:19], s6 offen offset:4 sc0 sc1\n"
"s_mov_b32 s6, 0x45c0000\nbuffer_load_dword v138, v144, s[16:19], s6 offen offset:4 sc0 sc1\ns_mov_b32 s6, 0x45c2000\n"
"buffer_load_dword v139, v144, s[16:19], s6 offen offset:4 sc0 sc1\ns_mov_b32 s6, 0x75c0000\nbuffer_load_dword v140, v144, s[16:19], s6 offen offset:4 sc0 sc1\n"
"s_mov_b32 s6, 0x75c2000\nbuffer_load_dword v141, v144, s[16:19], s6 offen offset:4 sc0 sc1\ns_mov_b32 s6, 0xa5c0000\n"
"buffer_load_dword v142, v144, s[16:19], s6 offen offset:4 sc0 sc1\ns_mov_b32 s6, 0xa5c2000\nbuffer_load_dword v143, v144, s[16:19], s6 offen offset:4 sc0 sc1\nWV 0\n"
"v_xor_b32 v136, s15, v136\nv_xor_b32 v137, s15, v137\nv_xor_b32 v138, s15, v138\nv_xor_b32 v139, s15, v139\nv_xor_b32 v140, s15, v140\nv_xor_b32 v141, s15, v141\n"
"v_xor_b32 v142, s15, v142\nv_xor_b32 v143, s15, v143\nv_min_u32 v136, v136, v137\nv_min_u32 v138, v138, v139\nv_min_u32 v140, v140, v141\nv_min_u32 v142, v142, v143\n"
"v_or3_b32 v136, v136, v138, v140\nv_or_b32 v136, v136, v142\nv_cmp_ne_u32 vcc, 0, v136\ns_nop 0\ns_cbranch_vccz .S189\ns_sleep 6\ns_sub_u32 s7, s7, 1\ns_cbranch_scc0 .S190\n"
"s_branch .S189\n.FF187:\nv_lshrrev_b32 v144, 9, v1\nv_mul_u32_u24 v144, 0x18000, v144\nv_lshlrev_b32 v144, 8, v144\nv_and_b32 v145, 0x1f0, v1\nv_add_u32 v144, v144, v145\n"
"s_mov_b32 s6, 0x15c0000\nbuffer_load_dword v136, v144, s[16:19], s6 offen offset:4 sc0 sc1\ns_mov_b32 s6, 0x15c2000\n"
"buffer_load_dword v137, v144, s[16:19], s6 offen offset:4 sc0 sc1\ns_mov_b32 s6, 0x45c0000\nbuffer_load_dword v138, v144, s[16:19], s6 offen offset:4 sc0 sc1\n"
"s_mov_b32 s6, 0x45c2000\nbuffer_load_dword v139, v144, s[16:19], s6 offen offset:4 sc0 sc1\ns_mov_b32 s6, 0x75c0000\n"
"buffer_load_dword v140, v144, s[16:19], s6 offen offset:4 sc0 sc1\ns_mov_b32 s6, 0x75c2000\nbuffer_load_dword v141, v144, s[16:19], s6 offen offset:4 sc0 sc1\n"
"s_mov_b32 s6, 0xa5c0000\nbuffer_load_dword v142, v144, s[16:19], s6 offen offset:4 sc0 sc1\ns_mov_b32 s6, 0xa5c2000\n"
"buffer_load_dword v143, v144, s[16:19], s6 offen offset:4 sc0 sc1\ns_mov_b32 s90, 0\ns_swappc_b64 s[8:9], s[80:81]\nv_xor_b32 v136, s15, v136\nv_xor_b32 v137, s15, v137\n"
"v_xor_b32 v138, s15, v138\nv_xor_b32 v139, s15, v139\nv_xor_b32 v140, s15, v140\nv_xor_b32 v141, s15, v141\nv_xor_b32 v142, s15, v142\nv_xor_b32 v143, s15, v143\n"
"v_min_u32 v136, v136, v137\nv_min_u32 v138, v138, v139\nv_min_u32 v140, v140, v141\nv_min_u32 v142, v142, v143\nv_or3_b32 v136, v136, v138, v140\nv_or_b32 v136, v136, v142\n"
"v_cmp_ne_u32 vcc, 0, v136\ns_nop 0\ns_cbranch_vccnz .S188\n.S189:\ns_and_b32 s4, s13, 1\ns_lshl1_add_u32 s4, s4, s11\ns_lshl2_add_u32 s4, s12, s4\ns_lshr_b32 s5, s71, 16\n"
"s_cmp_lt_u32 s4, 28\ns_cselect_b32 s5, s71, s5\ns_cselect_b32 s3, 0, 28\ns_and_b32 s5, s5, 0x7ff\ns_sub_u32 s3, s4, s3\ns_mul_i32 s2, s4, 7168\ns_add_u32 s2, s2, 0xc04c800\n"
"s_add_u32 s6, s2, 4096\nbuffer_load_dwordx4 v[136:139], v1, s[16:19], s2 offen offset:0 sc0 sc1\nbuffer_load_dwordx4 v[140:143], v1, s[16:19], s2 offen offset:1024 sc0 sc1\n"
"buffer_load_dwordx4 v[144:147], v1, s[16:19], s2 offen offset:2048 sc0 sc1\nbuffer_load_dwordx4 v[148:151], v1, s[16:19], s2 offen offset:3072 sc0 sc1\n"
"buffer_load_dwordx4 v[152:155], v1, s[16:19], s6 offen offset:0 sc0 sc1\nbuffer_load_dwordx4 v[156:159], v1, s[16:19], s6 offen offset:1024 sc0 sc1\n"
"buffer_load_dwordx4 v[160:163], v1, s[16:19], s6 offen offset:2048 sc0 sc1\nv_lshrrev_b32 v167, 1, v1\nv_and_b32 v167, 24, v167\ns_mul_i32 s2, s5, 896\ns_lshl_b32 s3, s3, 5\n"
"s_add_u32 s2, s2, s3\ns_lshl2_add_u32 s5, s5, 0xc03e000\nbuffer_load_dwordx2 v[164:165], v167, s[28:31], s2 offen\nbuffer_load_dword v166, off, s[16:19], s5 sc0 sc1\n"
"s_mov_b32 s90, 0\ns_swappc_b64 s[8:9], s[80:81]\nAD 136,136,140\nAD 137,137,141\nAD 138,138,142\nAD 139,139,143\nAD 144,144,148\nAD 145,145,149\nAD 146,146,150\nAD 147,147,151\n"
"AD 152,152,156\nAD 153,153,157\nAD 154,154,158\nAD 155,155,159\nAD 136,136,144\nAD 137,137,145\nAD 138,138,146\nAD 139,139,147\nAD 152,152,160\nAD 153,153,161\nAD 154,154,162\n"
"AD 155,155,163\nAD 136,136,152\nAD 137,137,153\nAD 138,138,154\nAD 139,139,155\nv_mov_b32 v140, v136\nv_mov_b32 v141, v137\nv_mov_b32 v142, v138\nv_mov_b32 v143, v139\n"
"v_permlane32_swap_b32 v136, v140\nv_permlane32_swap_b32 v137, v141\nv_permlane32_swap_b32 v138, v142\nv_permlane32_swap_b32 v139, v143\nAD 136,136,140\nAD 137,137,141\n"
"AD 138,138,142\nAD 139,139,143\nv_mov_b32 v140, v136\nv_mov_b32 v141, v137\nv_mov_b32 v142, v138\nv_mov_b32 v143, v139\nv_permlane16_swap_b32 v136, v140\n"
"v_permlane16_swap_b32 v137, v141\nv_permlane16_swap_b32 v138, v142\nv_permlane16_swap_b32 v139, v143\nAD 136,136,140\nAD 137,137,141\nAD 138,138,142\nAD 139,139,143\n"
"v_add_f32_dpp v136, v136, v136 row_ror:8 row_mask:0xf bank_mask:0xf\nv_add_f32_dpp v137, v137, v137 row_ror:8 row_mask:0xf bank_mask:0xf\n"
"v_add_f32_dpp v138, v138, v138 row_ror:8 row_mask:0xf bank_mask:0xf\nv_add_f32_dpp v139, v139, v139 row_ror:8 row_mask:0xf bank_mask:0xf\n"
"v_add_f32_dpp v136, v136, v136 row_ror:4 row_mask:0xf bank_mask:0xf\nv_add_f32_dpp v137, v137, v137 row_ror:4 row_mask:0xf bank_mask:0xf\n"
"v_add_f32_dpp v138, v138, v138 row_ror:4 row_mask:0xf bank_mask:0xf\nv_add_f32_dpp v139, v139, v139 row_ror:4 row_mask:0xf bank_mask:0xf\nv_mul_f32 v166, 0xbfb8aa3b, v166\n"
"EX 166,166\nCV 136,136,137\nv_add_f32 v166, 1.0, v166\nv_rcp_f32 v166, v166\nCV 138,138,139\nCV 166,166,166\nv_lshlrev_b32 v166, 16, v166\nv_and_b32 v137, 0xffff0000, v136\n"
"v_lshlrev_b32 v136, 16, v136\nv_and_b32 v139, 0xffff0000, v138\nv_lshlrev_b32 v138, 16, v138\nMU 136,136,166\nMU 137,137,166\nMU 138,138,166\nMU 139,139,166\nCV 136,136,137\n"
"CV 138,138,139\nv_and_b32 v137, 0xffff0000, v136\nv_lshlrev_b32 v136, 16, v136\nv_and_b32 v139, 0xffff0000, v138\nv_lshlrev_b32 v138, 16, v138\nv_lshlrev_b32 v140, 16, v164\n"
"v_and_b32 v141, 0xffff0000, v164\nv_lshlrev_b32 v142, 16, v165\nv_and_b32 v143, 0xffff0000, v165\nAD 136,136,140\nAD 137,137,141\nAD 138,138,142\nAD 139,139,143\nCV 136,136,137\n"
"CV 137,138,139\nv_mov_b32 v138, s15\nv_mov_b32 v139, s71\ns_and_b32 s4, s13, 1\ns_lshl1_add_u32 s4, s4, s11\ns_lshl2_add_u32 s4, s12, s4\ns_lshr_b32 s5, s71, 16\n"
"s_cmp_lt_u32 s4, 28\ns_cselect_b32 s5, s71, s5\ns_cselect_b32 s3, 0, 28\ns_and_b32 s5, s5, 0x7ff\ns_sub_u32 s3, s4, s3\nv_mov_b32 v140, 0x40000000\nv_cmp_gt_u32 vcc, 64, v1\n"
"s_lshl_b32 s4, s4, 6\ns_add_u32 s4, s4, 0xc04b800\nv_cndmask_b32 v140, v140, v1, vcc\nSv 136,140,16,4,0\ns_mov_b32 s90, 100\ns_swappc_b64 s[8:9], s[80:81]\n.T195:\n"
"s_mov_b32 s62, 0\ns_call_b64 s[80:81], .RB11\ns_branch .T196\n.T197:\ns_mov_b32 s62, 0\ns_call_b64 s[80:81], .RA10\ns_branch .T198\n.T202:\ns_mov_b32 s62, 0\n"
"s_call_b64 s[80:81], .RB11\ns_branch .T203\n.T204:\ns_mov_b32 s62, 0\ns_call_b64 s[80:81], .RA10\ns_branch .T205\n.T208:\ns_mov_b32 s62, 0\ns_call_b64 s[80:81], .RB11\n"
"s_branch .T209\n.T210:\ns_mov_b32 s62, 0\ns_call_b64 s[80:81], .RA10\ns_branch .T211\n.S216:\ns_mov_b32 s7, 262144\n.S218:\n"
"buffer_load_dwordx4 v[100:103], v108, s[16:19], s65 offen offset:0 sc0 sc1\nbuffer_load_dwordx4 v[104:107], v108, s[16:19], s65 offen offset:256 sc0 sc1\nWV 0\n"
"v_xor_b32 v109, s15, v102\nv_xor_b32 v110, s15, v106\nv_or_b32 v109, v109, v110\nv_cmp_ne_u32 vcc, 0, v109\ns_nop 0\ns_cbranch_vccz .S217\ns_sleep 6\ns_sub_u32 s7, s7, 1\n"
"s_cbranch_scc0 .S218\ns_branch .S217\n.D214:\nv_mov_b32 v130, 0\nv_mov_b32 v131, 0\ns_mov_b32 s63, 0xff800000\ns_mov_b32 s67, 1\ns_branch .D215\n.T220:\n"
"s_lshl3_add_u32 s62, s67, 2\ns_call_b64 s[80:81], .RB11\ns_cmp_eq_u32 s67, 0\ns_cbranch_scc1 .T221\nSU 20,100,88\nv_min_f32 v20, 0, v20\nEX 20,20\ns_nop 0\nFM 86,101,20\n"
"SU 21,104,89\nv_min_f32 v21, 0, v21\nEX 21,21\ns_nop 0\nFM 87,105,21\nAR 22,192\nv_and_b32 v23, 0xffff0000, v22\nv_lshlrev_b32 v22, 16, v22\nMU 22,22,20\nMU 23,23,20\nCV 22,22,23\n"
"AW 192,22\nAR 22,193\nv_and_b32 v23, 0xffff0000, v22\nv_lshlrev_b32 v22, 16, v22\nMU 22,22,20\nMU 23,23,20\nCV 22,22,23\nAW 193,22\nAR 22,194\nv_and_b32 v23, 0xffff0000, v22\n"
"v_lshlrev_b32 v22, 16, v22\nMU 22,22,20\nMU 23,23,20\nCV 22,22,23\nAW 194,22\nAR 22,195\nv_and_b32 v23, 0xffff0000, v22\nv_lshlrev_b32 v22, 16, v22\nMU 22,22,20\nMU 23,23,20\n"
"CV 22,22,23\nAW 195,22\nAR 22,196\nv_and_b32 v23, 0xffff0000, v22\nv_lshlrev_b32 v22, 16, v22\nMU 22,22,21\nMU 23,23,21\nCV 22,22,23\nAW 196,22\nAR 22,197\n"
"v_and_b32 v23, 0xffff0000, v22\nv_lshlrev_b32 v22, 16, v22\nMU 22,22,21\nMU 23,23,21\nCV 22,22,23\nAW 197,22\nAR 22,198\nv_and_b32 v23, 0xffff0000, v22\n"
"v_lshlrev_b32 v22, 16, v22\nMU 22,22,21\nMU 23,23,21\nCV 22,22,23\nAW 198,22\nAR 22,199\nv_and_b32 v23, 0xffff0000, v22\nv_lshlrev_b32 v22, 16, v22\nMU 22,22,21\nMU 23,23,21\n"
"CV 22,22,23\nAW 199,22\nAR 22,200\nv_and_b32 v23, 0xffff0000, v22\nv_lshlrev_b32 v22, 16, v22\nMU 22,22,20\nMU 23,23,20\nCV 22,22,23\nAW 200,22\nAR 22,201\n"
"v_and_b32 v23, 0xffff0000, v22\nv_lshlrev_b32 v22, 16, v22\nMU 22,22,20\nMU 23,23,20\nCV 22,22,23\nAW 201,22\nAR 22,202\nv_and_b32 v23, 0xffff0000, v22\n"
"v_lshlrev_b32 v22, 16, v22\nMU 22,22,20\nMU 23,23,20\nCV 22,22,23\nAW 202,22\nAR 22,203\nv_and_b32 v23, 0xffff0000, v22\nv_lshlrev_b32 v22, 16, v22\nMU 22,22,20\nMU 23,23,20\n"
"CV 22,22,23\nAW 203,22\nAR 22,204\nv_and_b32 v23, 0xffff0000, v22\nv_lshlrev_b32 v22, 16, v22\nMU 22,22,21\nMU 23,23,21\nCV 22,22,23\nAW 204,22\nAR 22,205\n"
"v_and_b32 v23, 0xffff0000, v22\nv_lshlrev_b32 v22, 16, v22\nMU 22,22,21\nMU 23,23,21\nCV 22,22,23\nAW 205,22\nAR 22,206\nv_and_b32 v23, 0xffff0000, v22\n"
"v_lshlrev_b32 v22, 16, v22\nMU 22,22,21\nMU 23,23,21\nCV 22,22,23\nAW 206,22\nAR 22,207\nv_and_b32 v23, 0xffff0000, v22\nv_lshlrev_b32 v22, 16, v22\nMU 22,22,21\nMU 23,23,21\n"
"CV 22,22,23\nAW 207,22\nAR 22,208\nv_and_b32 v23, 0xffff0000, v22\nv_lshlrev_b32 v22, 16, v22\nMU 22,22,20\nMU 23,23,20\nCV 22,22,23\nAW 208,22\nAR 22,209\n"
"v_and_b32 v23, 0xffff0000, v22\nv_lshlrev_b32 v22, 16, v22\nMU 22,22,20\nMU 23,23,20\nCV 22,22,23\nAW 209,22\nAR 22,210\nv_and_b32 v23, 0xffff0000, v22\n"
"v_lshlrev_b32 v22, 16, v22\nMU 22,22,20\nMU 23,23,20\nCV 22,22,23\nAW 210,22\nAR 22,211\nv_and_b32 v23, 0xffff0000, v22\nv_lshlrev_b32 v22, 16, v22\nMU 22,22,20\nMU 23,23,20\n"
"CV 22,22,23\nAW 211,22\nAR 22,212\nv_and_b32 v23, 0xffff0000, v22\nv_lshlrev_b32 v22, 16, v22\nMU 22,22,21\nMU 23,23,21\nCV 22,22,23\nAW 212,22\nAR 22,213\n"
"v_and_b32 v23, 0xffff0000, v22\nv_lshlrev_b32 v22, 16, v22\nMU 22,22,21\nMU 23,23,21\nCV 22,22,23\nAW 213,22\nAR 22,214\nv_and_b32 v23, 0xffff0000, v22\n"
"v_lshlrev_b32 v22, 16, v22\nMU 22,22,21\nMU 23,23,21\nCV 22,22,23\nAW 214,22\nAR 22,215\nv_and_b32 v23, 0xffff0000, v22\nv_lshlrev_b32 v22, 16, v22\nMU 22,22,21\nMU 23,23,21\n"
"CV 22,22,23\nAW 215,22\nAR 22,216\nv_and_b32 v23, 0xffff0000, v22\nv_lshlrev_b32 v22, 16, v22\nMU 22,22,20\nMU 23,23,20\nCV 22,22,23\nAW 216,22\nAR 22,217\n"
"v_and_b32 v23, 0xffff0000, v22\nv_lshlrev_b32 v22, 16, v22\nMU 22,22,20\nMU 23,23,20\nCV 22,22,23\nAW 217,22\nAR 22,218\nv_and_b32 v23, 0xffff0000, v22\n"
"v_lshlrev_b32 v22, 16, v22\nMU 22,22,20\nMU 23,23,20\nCV 22,22,23\nAW 218,22\nAR 22,219\nv_and_b32 v23, 0xffff0000, v22\nv_lshlrev_b32 v22, 16, v22\nMU 22,22,20\nMU 23,23,20\n"
"CV 22,22,23\nAW 219,22\nAR 22,220\nv_and_b32 v23, 0xffff0000, v22\nv_lshlrev_b32 v22, 16, v22\nMU 22,22,21\nMU 23,23,21\nCV 22,22,23\nAW 220,22\nAR 22,221\n"
"v_and_b32 v23, 0xffff0000, v22\nv_lshlrev_b32 v22, 16, v22\nMU 22,22,21\nMU 23,23,21\nCV 22,22,23\nAW 221,22\nAR 22,222\nv_and_b32 v23, 0xffff0000, v22\n"
"v_lshlrev_b32 v22, 16, v22\nMU 22,22,21\nMU 23,23,21\nCV 22,22,23\nAW 222,22\nAR 22,223\nv_and_b32 v23, 0xffff0000, v22\nv_lshlrev_b32 v22, 16, v22\nMU 22,22,21\nMU 23,23,21\n"
"CV 22,22,23\nAW 223,22\nAR 22,224\nv_and_b32 v23, 0xffff0000, v22\nv_lshlrev_b32 v22, 16, v22\nMU 22,22,20\nMU 23,23,20\nCV 22,22,23\nAW 224,22\nAR 22,225\n"
"v_and_b32 v23, 0xffff0000, v22\nv_lshlrev_b32 v22, 16, v22\nMU 22,22,20\nMU 23,23,20\nCV 22,22,23\nAW 225,22\nAR 22,226\nv_and_b32 v23, 0xffff0000, v22\n"
"v_lshlrev_b32 v22, 16, v22\nMU 22,22,20\nMU 23,23,20\nCV 22,22,23\nAW 226,22\nAR 22,227\nv_and_b32 v23, 0xffff0000, v22\nv_lshlrev_b32 v22, 16, v22\nMU 22,22,20\nMU 23,23,20\n"
"CV 22,22,23\nAW 227,22\nAR 22,228\nv_and_b32 v23, 0xffff0000, v22\nv_lshlrev_b32 v22, 16, v22\nMU 22,22,21\nMU 23,23,21\nCV 22,22,23\nAW 228,22\nAR 22,229\n"
"v_and_b32 v23, 0xffff0000, v22\nv_lshlrev_b32 v22, 16, v22\nMU 22,22,21\nMU 23,23,21\nCV 22,22,23\nAW 229,22\nAR 22,230\nv_and_b32 v23, 0xffff0000, v22\n"
"v_lshlrev_b32 v22, 16, v22\nMU 22,22,21\nMU 23,23,21\nCV 22,22,23\nAW 230,22\nAR 22,231\nv_and_b32 v23, 0xffff0000, v22\nv_lshlrev_b32 v22, 16, v22\nMU 22,22,21\nMU 23,23,21\n"
"CV 22,22,23\nAW 231,22\nAR 22,232\nv_and_b32 v23, 0xffff0000, v22\nv_lshlrev_b32 v22, 16, v22\nMU 22,22,20\nMU 23,23,20\nCV 22,22,23\nAW 232,22\nAR 22,233\n"
"v_and_b32 v23, 0xffff0000, v22\nv_lshlrev_b32 v22, 16, v22\nMU 22,22,20\nMU 23,23,20\nCV 22,22,23\nAW 233,22\nAR 22,234\nv_and_b32 v23, 0xffff0000, v22\n"
"v_lshlrev_b32 v22, 16, v22\nMU 22,22,20\nMU 23,23,20\nCV 22,22,23\nAW 234,22\nAR 22,235\nv_and_b32 v23, 0xffff0000, v22\nv_lshlrev_b32 v22, 16, v22\nMU 22,22,20\nMU 23,23,20\n"
"CV 22,22,23\nAW 235,22\nAR 22,236\nv_and_b32 v23, 0xffff0000, v22\nv_lshlrev_b32 v22, 16, v22\nMU 22,22,21\nMU 23,23,21\nCV 22,22,23\nAW 236,22\nAR 22,237\n"
"v_and_b32 v23, 0xffff0000, v22\nv_lshlrev_b32 v22, 16, v22\nMU 22,22,21\nMU 23,23,21\nCV 22,22,23\nAW 237,22\nAR 22,238\nv_and_b32 v23, 0xffff0000, v22\n"
"v_lshlrev_b32 v22, 16, v22\nMU 22,22,21\nMU 23,23,21\nCV 22,22,23\nAW 238,22\nAR 22,239\nv_and_b32 v23, 0xffff0000, v22\nv_lshlrev_b32 v22, 16, v22\nMU 22,22,21\nMU 23,23,21\n"
"CV 22,22,23\nAW 239,22\nAR 22,240\nv_and_b32 v23, 0xffff0000, v22\nv_lshlrev_b32 v22, 16, v22\nMU 22,22,20\nMU 23,23,20\nCV 22,22,23\nAW 240,22\nAR 22,241\n"
"v_and_b32 v23, 0xffff0000, v22\nv_lshlrev_b32 v22, 16, v22\nMU 22,22,20\nMU 23,23,20\nCV 22,22,23\nAW 241,22\nAR 22,242\nv_and_b32 v23, 0xffff0000, v22\n"
"v_lshlrev_b32 v22, 16, v22\nMU 22,22,20\nMU 23,23,20\nCV 22,22,23\nAW 242,22\nAR 22,243\nv_and_b32 v23, 0xffff0000, v22\nv_lshlrev_b32 v22, 16, v22\nMU 22,22,20\nMU 23,23,20\n"
"CV 22,22,23\nAW 243,22\nAR 22,244\nv_and_b32 v23, 0xffff0000, v22\nv_lshlrev_b32 v22, 16, v22\nMU 22,22,21\nMU 23,23,21\nCV 22,22,23\nAW 244,22\nAR 22,245\n"
"v_and_b32 v23, 0xffff0000, v22\nv_lshlrev_b32 v22, 16, v22\nMU 22,22,21\nMU 23,23,21\nCV 22,22,23\nAW 245,22\nAR 22,246\nv_and_b32 v23, 0xffff0000, v22\n"
"v_lshlrev_b32 v22, 16, v22\nMU 22,22,21\nMU 23,23,21\nCV 22,22,23\nAW 246,22\nAR 22,247\nv_and_b32 v23, 0xffff0000, v22\nv_lshlrev_b32 v22, 16, v22\nMU 22,22,21\nMU 23,23,21\n"
"CV 22,22,23\nAW 247,22\nAR 22,248\nv_and_b32 v23, 0xffff0000, v22\nv_lshlrev_b32 v22, 16, v22\nMU 22,22,20\nMU 23,23,20\nCV 22,22,23\nAW 248,22\nAR 22,249\n"
"v_and_b32 v23, 0xffff0000, v22\nv_lshlrev_b32 v22, 16, v22\nMU 22,22,20\nMU 23,23,20\nCV 22,22,23\nAW 249,22\nAR 22,250\nv_and_b32 v23, 0xffff0000, v22\n"
"v_lshlrev_b32 v22, 16, v22\nMU 22,22,20\nMU 23,23,20\nCV 22,22,23\nAW 250,22\nAR 22,251\nv_and_b32 v23, 0xffff0000, v22\nv_lshlrev_b32 v22, 16, v22\nMU 22,22,20\nMU 23,23,20\n"
"CV 22,22,23\nAW 251,22\nAR 22,252\nv_and_b32 v23, 0xffff0000, v22\nv_lshlrev_b32 v22, 16, v22\nMU 22,22,21\nMU 23,23,21\nCV 22,22,23\nAW 252,22\nAR 22,253\n"
"v_and_b32 v23, 0xffff0000, v22\nv_lshlrev_b32 v22, 16, v22\nMU 22,22,21\nMU 23,23,21\nCV 22,22,23\nAW 253,22\nAR 22,254\nv_and_b32 v23, 0xffff0000, v22\n"
"v_lshlrev_b32 v22, 16, v22\nMU 22,22,21\nMU 23,23,21\nCV 22,22,23\nAW 254,22\nAR 22,255\nv_and_b32 v23, 0xffff0000, v22\nv_lshlrev_b32 v22, 16, v22\nMU 22,22,21\nMU 23,23,21\n"
"CV 22,22,23\nAW 255,22\nv_lshrrev_b32 v22, 8, v1\nv_bfe_u32 v23, v1, 6, 2\nv_cmp_eq_u32 vcc, v22, v23\nv_and_b32 v22, 16, v1\nv_mov_b32 v23, 0x3f80\n"
"v_cndmask_b32 v23, 0, v23, vcc\nv_lshlrev_b32 v23, v22, v23\nv_and_b32 v22, 32, v1\nv_cmp_eq_u32 vcc, 0, v22\nv_mov_b32 v128, 0\nv_mov_b32 v129, 0\n"
"v_cndmask_b32 v130, 0, v23, vcc\nv_sub_u32 v131, v23, v130\nv_mov_b32 v132, 0\nv_mov_b32 v133, 0\ns_nop 1\nMava 0,130,192\nMava 8,128,192\nMava 12,128,196\nMava 4,130,196\n"
"Mava 16,130,200\nMava 24,128,200\nMava 28,128,204\nMava 20,130,204\nMava 32,130,208\nMava 40,128,208\nMava 44,128,212\nMava 36,130,212\nMava 48,130,216\nMava 56,128,216\n"
"Mava 60,128,220\nMava 52,130,220\nMava 64,130,224\nMava 72,128,224\nMava 76,128,228\nMava 68,130,228\nMava 80,130,232\nMava 88,128,232\nMava 92,128,236\nMava 84,130,236\n"
"Mava 96,130,240\nMava 104,128,240\nMava 108,128,244\nMava 100,130,244\nMava 112,130,248\nMava 120,128,248\nMava 124,128,252\nMava 116,130,252\ns_nop 7\ns_branch .T221\n.S223:\n"
"s_mov_b32 s7, 262144\n.S225:\ns_add_u32 s0, s92, 0\nbuffer_load_dwordx4 v[208:211], v1, s[16:19], s0 offen offset:0 sc0 sc1\n"
"buffer_load_dwordx4 v[212:215], v1, s[16:19], s0 offen offset:1024 sc0 sc1\nWV 0\nv_max_u32 v21, v211, v215\nv_cmp_eq_u32 vcc, -1, v21\ns_nop 0\ns_cbranch_vccz .S224\ns_sleep 6\n"
"s_sub_u32 s7, s7, 1\ns_cbranch_scc0 .S225\ns_branch .S224\n.S226:\ns_mov_b32 s7, 262144\n.S228:\ns_add_u32 s0, s92, 131072\n"
"buffer_load_dwordx4 v[216:219], v1, s[16:19], s0 offen offset:0 sc0 sc1\nbuffer_load_dwordx4 v[220:223], v1, s[16:19], s0 offen offset:1024 sc0 sc1\nWV 0\n"
"v_max_u32 v21, v219, v223\nv_cmp_eq_u32 vcc, -1, v21\ns_nop 0\ns_cbranch_vccz .S227\ns_sleep 6\ns_sub_u32 s7, s7, 1\ns_cbranch_scc0 .S228\ns_branch .S227\n.S229:\n"
"s_mov_b32 s7, 262144\n.S231:\ns_add_u32 s0, s92, 262144\nbuffer_load_dwordx4 v[224:227], v1, s[16:19], s0 offen offset:0 sc0 sc1\n"
"buffer_load_dwordx4 v[228:231], v1, s[16:19], s0 offen offset:1024 sc0 sc1\nWV 0\nv_max_u32 v21, v227, v231\nv_cmp_eq_u32 vcc, -1, v21\ns_nop 0\ns_cbranch_vccz .S230\ns_sleep 6\n"
"s_sub_u32 s7, s7, 1\ns_cbranch_scc0 .S231\ns_branch .S230\n.S232:\ns_mov_b32 s7, 262144\n.S234:\ns_add_u32 s0, s92, 393216\n"
"buffer_load_dwordx4 v[208:211], v1, s[16:19], s0 offen offset:0 sc0 sc1\nbuffer_load_dwordx4 v[212:215], v1, s[16:19], s0 offen offset:1024 sc0 sc1\nWV 0\n"
"v_max_u32 v21, v211, v215\nv_cmp_eq_u32 vcc, -1, v21\ns_nop 0\ns_cbranch_vccz .S233\ns_sleep 6\ns_sub_u32 s7, s7, 1\ns_cbranch_scc0 .S234\ns_branch .S233\n.S235:\n"
"s_mov_b32 s7, 262144\n.S237:\ns_add_u32 s0, s92, 524288\nbuffer_load_dwordx4 v[216:219], v1, s[16:19], s0 offen offset:0 sc0 sc1\n"
"buffer_load_dwordx4 v[220:223], v1, s[16:19], s0 offen offset:1024 sc0 sc1\nWV 0\nv_max_u32 v21, v219, v223\nv_cmp_eq_u32 vcc, -1, v21\ns_nop 0\ns_cbranch_vccz .S236\ns_sleep 6\n"
"s_sub_u32 s7, s7, 1\ns_cbranch_scc0 .S237\ns_branch .S236\n.S238:\ns_mov_b32 s7, 262144\n.S240:\ns_add_u32 s0, s92, 655360\n"
"buffer_load_dwordx4 v[224:227], v1, s[16:19], s0 offen offset:0 sc0 sc1\nbuffer_load_dwordx4 v[228:231], v1, s[16:19], s0 offen offset:1024 sc0 sc1\nWV 0\n"
"v_max_u32 v21, v227, v231\nv_cmp_eq_u32 vcc, -1, v21\ns_nop 0\ns_cbranch_vccz .S239\ns_sleep 6\ns_sub_u32 s7, s7, 1\ns_cbranch_scc0 .S240\ns_branch .S239\n.S241:\n"
"s_mov_b32 s7, 262144\n.S243:\ns_add_u32 s0, s92, 786432\nbuffer_load_dwordx4 v[208:211], v1, s[16:19], s0 offen offset:0 sc0 sc1\n"
"buffer_load_dwordx4 v[212:215], v1, s[16:19], s0 offen offset:1024 sc0 sc1\nWV 0\nv_max_u32 v21, v211, v215\nv_cmp_eq_u32 vcc, -1, v21\ns_nop 0\ns_cbranch_vccz .S242\ns_sleep 6\n"
"s_sub_u32 s7, s7, 1\ns_cbranch_scc0 .S243\ns_branch .S242\n.S244:\ns_mov_b32 s7, 262144\n.S246:\nbuffer_load_dwordx4 v[64:67], v89, s[16:19], s40 offen offset:0 sc0 sc1\n"
"buffer_load_dwordx4 v[68:71], v89, s[16:19], s40 offen offset:16 sc0 sc1\nbuffer_load_dwordx4 v[72:75], v89, s[16:19], s40 offen offset:1792 sc0 sc1\n"
"buffer_load_dwordx4 v[76:79], v89, s[16:19], s40 offen offset:1808 sc0 sc1\nbuffer_load_dwordx2 v[80:81], v88, s[16:19], s63 offen offset:0 sc0 sc1\n"
"buffer_load_dwordx2 v[82:83], v88, s[16:19], s63 offen offset:512 sc0 sc1\nWV 0\nv_xor_b32 v66, s15, v66\nv_xor_b32 v70, s15, v70\nv_xor_b32 v74, s15, v74\n"
"v_xor_b32 v78, s15, v78\nv_xor_b32 v81, s15, v81\nv_xor_b32 v83, s15, v83\nv_or3_b32 v92, v66, v70, v74\nv_or3_b32 v92, v92, v78, v81\nv_or_b32 v92, v92, v83\n"
"v_cmp_ne_u32 vcc, 0, v92\ns_nop 0\ns_cbranch_vccz .S245\ns_sleep 6\ns_sub_u32 s7, s7, 1\ns_cbranch_scc0 .S246\ns_branch .S245\n.S247:\ns_mov_b32 s7, 262144\n.S249:\n"
"buffer_load_dword v27, off, s[16:19], s60 offset:512 sc0 sc1\nWV 0\nv_cmp_ne_u32 vcc, s15, v27\ns_nop 0\ns_cbranch_vccz .S248\ns_sleep 6\ns_sub_u32 s7, s7, 1\n"
"s_cbranch_scc0 .S249\ns_branch .S248\n.S250:\ns_mov_b32 s7, 262144\n.S252:\nbuffer_load_dword v27, v24, s[16:19], s41 offen sc0 sc1\nWV 0\nv_xor_b32 v27, s15, v27\n"
"v_and_b32 v26, 0xffffdfff, v27\nv_cmp_ne_u32 s[64:65], 0, v27\nv_cmp_ne_u32 vcc, 0, v26\ns_nop 0\ns_cbranch_vccz .S251\ns_sleep 6\ns_sub_u32 s7, s7, 1\ns_cbranch_scc0 .S252\n"
"s_branch .S251\n.Z254:\ns_sub_u32 s7, s7, 1\ns_cbranch_scc1 .Z255\ns_cmp_lg_u64 s[44:45], 0\ns_cbranch_scc0 .Z257\n"
"buffer_load_dwordx4 v[96:99], v1, s[16:19], s24 offen offset:0 sc0 sc1\nbuffer_load_dwordx4 v[100:103], v1, s[16:19], s24 offen offset:1024 sc0 sc1\n.Z257:\n"
"s_cmp_lg_u64 s[46:47], 0\ns_cbranch_scc0 .Z258\nbuffer_load_dwordx4 v[104:107], v1, s[16:19], s25 offen offset:0 sc0 sc1\n"
"buffer_load_dwordx4 v[108:111], v1, s[16:19], s25 offen offset:1024 sc0 sc1\n.Z258:\ns_cmp_lg_u64 s[48:49], 0\ns_cbranch_scc0 .Z259\n"
"buffer_load_dwordx4 v[112:115], v1, s[16:19], s26 offen offset:0 sc0 sc1\nbuffer_load_dwordx4 v[116:119], v1, s[16:19], s26 offen offset:1024 sc0 sc1\n.Z259:\n"
"s_cmp_lg_u64 s[50:51], 0\ns_cbranch_scc0 .Z260\nbuffer_load_dwordx4 v[120:123], v1, s[16:19], s27 offen offset:0 sc0 sc1\n"
"buffer_load_dwordx4 v[124:127], v1, s[16:19], s27 offen offset:1024 sc0 sc1\n.Z260:\ns_cmp_lg_u64 s[52:53], 0\ns_cbranch_scc0 .Z261\n"
"buffer_load_dwordx4 v[32:35], v1, s[16:19], s32 offen offset:0 sc0 sc1\nbuffer_load_dwordx4 v[36:39], v1, s[16:19], s32 offen offset:1024 sc0 sc1\n.Z261:\n"
"s_cmp_lg_u64 s[54:55], 0\ns_cbranch_scc0 .Z262\nbuffer_load_dwordx4 v[40:43], v1, s[16:19], s33 offen offset:0 sc0 sc1\n"
"buffer_load_dwordx4 v[44:47], v1, s[16:19], s33 offen offset:1024 sc0 sc1\n.Z262:\ns_cmp_lg_u64 s[56:57], 0\ns_cbranch_scc0 .Z263\n"
"buffer_load_dwordx4 v[48:51], v1, s[16:19], s34 offen offset:0 sc0 sc1\nbuffer_load_dwordx4 v[52:55], v1, s[16:19], s34 offen offset:1024 sc0 sc1\n.Z263:\n"
"s_cmp_lg_u64 s[58:59], 0\ns_cbranch_scc0 .Z264\nbuffer_load_dwordx4 v[56:59], v1, s[16:19], s35 offen offset:0 sc0 sc1\n"
"buffer_load_dwordx4 v[60:63], v1, s[16:19], s35 offen offset:1024 sc0 sc1\n.Z264:\ns_sleep 8\nWV 0\ns_branch .Z256\n.p2align 12\n.PA4:\ns_mov_b64 exec, 0\ns_cmp_eq_u32 s11, 1\n"
"s_cbranch_scc1 .W572\ns_cmp_eq_u32 s11, 2\ns_cbranch_scc1 .W573\ns_cmp_eq_u32 s11, 3\ns_cbranch_scc1 .W574\ns_branch .W571\n.WB274:\ns_mov_b64 exec, -1\ns_add_u32 m0, s47, -49152\n"
"s_add_u32 s48, s48, 128\nD0 2,20,48,0\nD0 3,20,48,1024\nD0 4,20,48,2048\nD0 5,20,48,3072\ns_and_b32 s86, s74, 15\ns_lshl2_add_u32 s86, s86, s11\ns_mov_b32 s88, 0\n"
"s_lshl_b32 s93, s73, 8\ns_lshl2_add_u32 s93, s86, s93\ns_add_u32 s93, s93, 0xc04ac00\ns_lshl_b32 s86, s86, 14\ns_mul_i32 s91, s73, 25165824\ns_add_u32 s86, s86, s91\n"
"s_and_b32 s92, s13, 1\ns_lshl_b32 s92, s92, 13\ns_add_u32 s86, s86, s92\ns_add_u32 s94, s86, 0x1480000\ns_xor_b32 s95, s94, 0x2000\ns_add_u32 s86, s94, 512\n"
"s_load_dword s87, s[54:55], s86\ns_add_u32 m0, s47, -32768\ns_add_u32 s45, s45, 128\nD0 2,20,45,0\nD0 3,20,45,1024\nD0 4,20,45,2048\nD0 5,20,45,3072\ns_add_u32 m0, s47, 32768\n"
"s_add_u32 s44, s44, 0x40000\nD0 6,24,44,0\nD0 7,24,44,1024\nD0 8,24,44,2048\nD0 9,24,44,3072\ns_add_u32 m0, s47, 49152\ns_add_u32 s44, s44, 0x40000\nD0 6,24,44,0\nD0 7,24,44,1024\n"
"D0 8,24,44,2048\nD0 9,24,44,3072\ns_add_u32 m0, s47, -16384\ns_add_u32 s48, s48, 128\nD0 2,20,48,0\nD0 3,20,48,1024\nD0 4,20,48,2048\nD0 5,20,48,3072\nv_mov_b32 v52, 0x40000000\n"
"v_cmp_gt_u32 vcc, 56, v0\ns_lshl2_add_u32 s2, s10, s11\ns_mul_i32 s2, s2, 1792\nv_cndmask_b32 v53, v52, v1, vcc\ns_mov_b32 s36, s56\ns_and_b32 s37, s57, 0xffff\n"
"s_mov_b32 s38, 0x380\ns_mov_b32 s39, 0x20000\ns_mov_b32 s40, s58\ns_and_b32 s41, s59, 0xffff\ns_mov_b32 s42, 0x380\ns_mov_b32 s43, 0x20000\n"
"buffer_load_dwordx4 v[32:35], v53, s[28:31], s2 offen offset:0\nbuffer_load_dwordx4 v[36:39], v53, s[28:31], s2 offen offset:896\n"
"buffer_load_dwordx4 v[40:43], v53, s[36:39], 0 offen offset:0\nbuffer_load_dwordx4 v[44:47], v53, s[40:43], 0 offen offset:0\nWL 0\ns_mul_i32 s15, s15, 1664525\n"
"s_add_u32 s15, s15, 1013904223\nv_mov_b32 v25, s15\ns_mov_b32 s57, 0x3db8aa3b\ns_and_b32 s59, s11, 1\ns_lshl_b32 s59, s59, 13\ns_mul_i32 s2, s73, 25165824\ns_and_b32 s3, s12, 1\n"
"s_lshl_b32 s3, s3, 21\ns_add_u32 s2, s2, s3\ns_lshl2_add_u32 s3, s13, s11\ns_lshl_b32 s3, s3, 15\ns_add_u32 s2, s2, s3\ns_add_u32 s56, s2, 0x800000\ns_xor_b32 s53, s56, 0xc000\n"
"buffer_load_dword v31, v24, s[24:27], s96 offen\ns_add_u32 s96, s96, 0x80000\nbuffer_load_dword v31, v24, s[24:27], s96 offen\ns_add_u32 s96, s96, 0x80000\n"
"v_lshrrev_b32 v60, 3, v0\nv_and_b32 v61, 0x70, v1\nv_lshl_or_b32 v60, v60, 10, v61\nv_mov_b32 v20, 0\nv_mov_b32 v21, 0\nv_mov_b32 v22, 0\nv_mov_b32 v23, 0\nSv 20,60,16,94,0\n"
"Sv 20,60,16,95,0\nWV 28\ns_barrier\nRa 128,10,0\nRa 132,11,0\nRa 136,10,2048\nRa 140,11,2048\nRa 144,10,8192\nRa 148,11,8192\nRa 152,10,10240\nRa 156,11,10240\nTa 160,12,0\n"
"Ta 162,12,2048\nTa 164,12,16384\nTa 166,12,18432\nTa 168,13,0\nTa 170,13,2048\nTa 172,13,16384\nTa 174,13,18432\nTa 176,14,0\nTa 178,14,2048\nTa 180,14,16384\nTa 182,14,18432\n"
"Ta 184,15,0\nTa 186,15,2048\nTa 188,15,16384\nTa 190,15,18432\nWL 0\ns_barrier\nZvaa 64,160,128\nTa 224,16,0\nMvaa 64,164,132\nTa 226,16,2048\nZvaa 96,164,140\n"
"s_add_u32 m0, s47, -65536\ns_add_u32 s45, s45, 128\nMvaa 96,160,136\nTa 228,16,16384\nZvaa 128,160,144\nTa 230,16,18432\nMvaa 128,164,148\nD0 2,20,45,0\nZvaa 160,164,156\n"
"Ta 232,17,0\nMvaa 160,160,152\nTa 234,17,2048\nZvaa 164,168,152\ns_mov_b32 s36, s60\nMvaa 164,172,156\nTa 236,17,16384\nZvaa 132,172,148\nTa 238,17,18432\nMvaa 132,168,144\n"
"s_and_b32 s37, s61, 0xffff\nZvaa 100,168,136\nTa 240,18,0\nMvaa 100,172,140\nTa 242,18,2048\nZvaa 68,172,132\nD0 3,20,45,1024\nMvaa 68,168,128\nTa 244,18,16384\nZvaa 72,176,128\n"
"Ta 246,18,18432\nMvaa 72,180,132\ns_mov_b32 s38, 0x380000\nZvaa 104,180,140\nTa 248,19,0\nMvaa 104,176,136\nTa 250,19,2048\nZvaa 136,176,144\ns_mov_b32 s39, 0x20000\n"
"Mvaa 136,180,148\nTa 252,19,16384\nZvaa 168,180,156\nTa 254,19,18432\nMvaa 168,176,152\nD0 4,20,45,2048\nZvaa 172,184,152\ns_lshl3_add_u32 s75, s12, s13\nMvaa 172,188,156\n"
"s_lshl2_add_u32 s75, s75, s11\nZvaa 140,188,148\ns_mul_i32 s75, s75, 7168\nMvaa 140,184,144\ns_mul_i32 s76, s11, 7168\nZvaa 108,184,136\ns_add_u32 s76, s76, 131072\n"
"Mvaa 108,188,140\nD0 5,20,45,3072\nZvaa 76,188,132\ns_mov_b32 s42, 1605632\nMvaa 76,184,128\ns_waitcnt vmcnt(24) lgkmcnt(0)\ns_barrier\nZvaa 80,224,128\nRa 192,10,16384\n"
"Mvaa 80,228,132\ns_add_u32 m0, s47, 0\ns_add_u32 s44, s44, 0x40000\nZvaa 112,228,140\nRa 196,11,16384\nMvaa 112,224,136\nZvaa 144,224,144\nRa 200,10,18432\nMvaa 144,228,148\n"
"D0 6,24,44,0\nZvaa 176,228,156\nRa 204,11,18432\nMvaa 176,224,152\nZvaa 180,232,152\nRa 208,10,24576\nMvaa 180,236,156\ns_and_b32 s78, s74, 3\ns_lshl2_add_u32 s78, s73, s78\n"
"s_lshl_b32 s78, s78, 12\ns_bitcmp1_b32 s11, 0\ns_cselect_b32 s79, s48, s45\ns_add_u32 s79, s79, 512\ns_and_b32 s79, s79, 0xfffe0000\ns_add_u32 s78, s78, s79\n"
"buffer_load_dword v31, v26, s[20:23], s78 offen\nZvaa 148,236,148\nRa 212,11,24576\nMvaa 148,232,144\nZvaa 116,232,136\nRa 216,10,26624\nMvaa 116,236,140\nD0 7,24,44,1024\n"
"Zvaa 84,236,132\nRa 220,11,26624\nMvaa 84,232,128\nZvaa 88,240,128\nMvaa 88,244,132\nZvaa 120,244,140\nMvaa 120,240,136\nZvaa 152,240,144\nMvaa 152,244,148\nD0 8,24,44,2048\n"
"Zvaa 184,244,156\nMvaa 184,240,152\nZvaa 188,248,152\nMvaa 188,252,156\nZvaa 156,252,148\nMvaa 156,248,144\nZvaa 124,248,136\nMvaa 124,252,140\nD0 9,24,44,3072\nZvaa 92,252,132\n"
"Mvaa 92,248,128\nWL 0\nZaaa 16,224,192\nRa 128,10,32768\nMaaa 16,228,196\ns_add_u32 m0, s47, 16384\ns_add_u32 s44, s44, 0x40000\nZaaa 48,228,204\nRa 132,11,32768\nMaaa 48,224,200\n"
"Zaaa 80,224,208\nRa 136,10,34816\nMaaa 80,228,212\nD0 6,24,44,0\nZaaa 112,228,220\nRa 140,11,34816\nMaaa 112,224,216\nZaaa 116,232,216\nRa 144,10,40960\nMaaa 116,236,220\n"
"Zaaa 84,236,212\nRa 148,11,40960\nMaaa 84,232,208\nZaaa 52,232,200\nRa 152,10,43008\nMaaa 52,236,204\nD0 7,24,44,1024\nZaaa 20,236,196\nRa 156,11,43008\nMaaa 20,232,192\n"
"Zaaa 24,240,192\nMaaa 24,244,196\nZaaa 56,244,204\nMaaa 56,240,200\nZaaa 88,240,208\nMaaa 88,244,212\nD0 8,24,44,2048\nZaaa 120,244,220\nMaaa 120,240,216\nZaaa 124,248,216\n"
"Maaa 124,252,220\nZaaa 92,252,212\nMaaa 92,248,208\nZaaa 60,248,200\nMaaa 60,252,204\nD0 9,24,44,3072\nZaaa 28,252,196\nMaaa 28,248,192\ns_waitcnt vmcnt(25) lgkmcnt(0)\ns_barrier\n"
"Zaaa 0,160,192\nTa 224,16,32768\nWV 17\nMaaa 0,164,196\nTa 226,16,34816\nZaaa 32,164,204\ns_add_u32 m0, s47, -49152\ns_add_u32 s48, s48, 128\nMaaa 32,160,200\nTa 228,16,49152\n"
"Zaaa 64,160,208\nTa 230,16,51200\nbuffer_load_dword v31, v24, s[24:27], s96 offen\ns_add_u32 s96, s96, 0x80000\nMaaa 64,164,212\nD0 2,20,48,0\nv_lshlrev_b32 v48, 16, v32\n"
"Zaaa 96,164,220\nTa 232,17,32768\nMaaa 96,160,216\nTa 234,17,34816\nv_lshlrev_b32 v49, 16, v36\nZaaa 100,168,216\nv_lshlrev_b32 v50, 16, v40\nv_lshlrev_b32 v51, 16, v44\n"
"Maaa 100,172,220\nTa 236,17,49152\nZaaa 68,172,212\nTa 238,17,51200\nMU 52,48,50\nMaaa 68,168,208\nMU 53,49,50\nZaaa 36,168,200\nTa 240,18,32768\nCV 52,52,53\nMaaa 36,172,204\n"
"Ta 242,18,34816\nv_lshlrev_b32 v54, 16, v52\nZaaa 4,172,196\nD0 3,20,48,1024\nMaaa 4,168,192\nTa 244,18,49152\nv_and_b32 v55, 0xffff0000, v52\nZaaa 8,176,192\nTa 246,18,51200\n"
"MU 56,48,51\nMaaa 8,180,196\nMU 57,49,51\nZaaa 40,180,204\nTa 248,19,32768\nv_and_b32 v48, 0xffff0000, v32\nMaaa 40,176,200\nTa 250,19,34816\nZaaa 72,176,208\n"
"v_and_b32 v49, 0xffff0000, v36\nv_and_b32 v50, 0xffff0000, v40\nMaaa 72,180,212\nTa 252,19,49152\nv_and_b32 v51, 0xffff0000, v44\nZaaa 104,180,220\nTa 254,19,51200\n"
"Maaa 104,176,216\nD0 4,20,48,2048\nMU 52,48,50\nZaaa 108,184,216\nMU 53,49,50\nCV 52,52,53\nMaaa 108,188,220\nv_and_b32 v53, 0xffff0000, v52\nZaaa 76,188,212\n"
"v_lshlrev_b32 v52, 16, v52\nFM 56,48,51\nMaaa 76,184,208\nFM 57,49,51\nZaaa 44,184,200\nAD 54,54,52\nAD 55,55,53\nMaaa 44,188,204\nD0 5,20,48,3072\nv_lshlrev_b32 v48, 16, v33\n"
"Zaaa 12,188,196\nv_lshlrev_b32 v49, 16, v37\nMaaa 12,184,192\nWL 0\nMvaa 80,224,128\nTa 160,12,32768\nv_lshlrev_b32 v50, 16, v41\nMvaa 80,228,132\nTa 162,12,34816\n"
"Mvaa 112,228,140\ns_add_u32 m0, s47, -32768\ns_add_u32 s45, s45, 128\nMvaa 112,224,136\nTa 164,12,49152\nMvaa 144,224,144\nTa 166,12,51200\nv_lshlrev_b32 v51, 16, v45\n"
"Mvaa 144,228,148\nD0 2,20,45,0\nMU 52,48,50\nMvaa 176,228,156\nTa 168,13,32768\nMvaa 176,224,152\nTa 170,13,34816\nMU 53,49,50\nMvaa 180,232,152\nCV 52,52,53\n"
"v_and_b32 v53, 0xffff0000, v52\nMvaa 180,236,156\nTa 172,13,49152\nMvaa 148,236,148\nTa 174,13,51200\nv_lshlrev_b32 v52, 16, v52\nMvaa 148,232,144\nFM 56,48,51\nMvaa 116,232,136\n"
"Ta 176,14,32768\nFM 57,49,51\nMvaa 116,236,140\nTa 178,14,34816\nAD 54,54,52\nMvaa 84,236,132\nD0 3,20,45,1024\nMvaa 84,232,128\nTa 180,14,49152\nAD 55,55,53\nMvaa 88,240,128\n"
"Ta 182,14,51200\nv_and_b32 v48, 0xffff0000, v33\nMvaa 88,244,132\nv_and_b32 v49, 0xffff0000, v37\nMvaa 120,244,140\nTa 184,15,32768\nv_and_b32 v50, 0xffff0000, v41\n"
"Mvaa 120,240,136\nTa 186,15,34816\nMvaa 152,240,144\nv_and_b32 v51, 0xffff0000, v45\nMU 52,48,50\nMvaa 152,244,148\nTa 188,15,49152\nMU 53,49,50\nMvaa 184,244,156\n"
"Ta 190,15,51200\nMvaa 184,240,152\nD0 4,20,45,2048\nCV 52,52,53\nMvaa 188,248,152\nv_and_b32 v53, 0xffff0000, v52\nv_lshlrev_b32 v52, 16, v52\nMvaa 188,252,156\nFM 56,48,51\n"
"Mvaa 156,252,148\nFM 57,49,51\nAD 54,54,52\nMvaa 156,248,144\nAD 55,55,53\nMvaa 124,248,136\nv_lshlrev_b32 v48, 16, v34\nv_lshlrev_b32 v49, 16, v38\nMvaa 124,252,140\n"
"D0 5,20,45,3072\nv_lshlrev_b32 v50, 16, v42\nMvaa 92,252,132\nv_lshlrev_b32 v51, 16, v46\nMvaa 92,248,128\ns_waitcnt vmcnt(18) lgkmcnt(0)\ns_barrier\nMvaa 64,160,128\n"
"Ra 192,10,49152\nMU 52,48,50\nMvaa 64,164,132\ns_add_u32 m0, s47, 32768\ns_add_u32 s44, s44, 0x40000\nMvaa 96,164,140\nRa 196,11,49152\nMU 53,49,50\nMvaa 96,160,136\nCV 52,52,53\n"
"Mvaa 128,160,144\nRa 200,10,51200\nv_and_b32 v53, 0xffff0000, v52\nMvaa 128,164,148\nD0 6,24,44,0\nv_lshlrev_b32 v52, 16, v52\nMvaa 160,164,156\nRa 204,11,51200\nMvaa 160,160,152\n"
"FM 56,48,51\nFM 57,49,51\nMvaa 164,168,152\nRa 208,10,57344\nAD 54,54,52\nMvaa 164,172,156\nAD 55,55,53\nMvaa 132,172,148\nRa 212,11,57344\nv_and_b32 v48, 0xffff0000, v34\n"
"Mvaa 132,168,144\nv_and_b32 v49, 0xffff0000, v38\nMvaa 100,168,136\nRa 216,10,59392\nv_and_b32 v50, 0xffff0000, v42\nMvaa 100,172,140\nD0 7,24,44,1024\n"
"v_and_b32 v51, 0xffff0000, v46\nMvaa 68,172,132\nRa 220,11,59392\nMvaa 68,168,128\nMU 52,48,50\nMU 53,49,50\nMvaa 72,176,128\nCV 52,52,53\nv_and_b32 v53, 0xffff0000, v52\n"
"Mvaa 72,180,132\nv_lshlrev_b32 v52, 16, v52\nMvaa 104,180,140\nFM 56,48,51\nFM 57,49,51\nMvaa 104,176,136\nAD 54,54,52\nMvaa 136,176,144\nAD 55,55,53\nv_lshlrev_b32 v48, 16, v35\n"
"Mvaa 136,180,148\nD0 8,24,44,2048\nv_lshlrev_b32 v49, 16, v39\nMvaa 168,180,156\nv_lshlrev_b32 v50, 16, v43\nMvaa 168,176,152\nv_lshlrev_b32 v51, 16, v47\nMU 52,48,50\n"
"Mvaa 172,184,152\nMU 53,49,50\nCV 52,52,53\nMvaa 172,188,156\nv_and_b32 v53, 0xffff0000, v52\nMvaa 140,188,148\nv_lshlrev_b32 v52, 16, v52\nFM 56,48,51\nMvaa 140,184,144\n"
"FM 57,49,51\nMvaa 108,184,136\nAD 54,54,52\nAD 55,55,53\nMvaa 108,188,140\nD0 9,24,44,3072\nv_and_b32 v48, 0xffff0000, v35\nMvaa 76,188,132\nv_and_b32 v49, 0xffff0000, v39\n"
"Mvaa 76,184,128\nWL 0\nMaaa 0,160,192\nRa 128,10,0\nv_and_b32 v50, 0xffff0000, v43\nMaaa 0,164,196\ns_add_u32 m0, s47, 49152\ns_add_u32 s44, s44, 0x40000\nMaaa 32,164,204\n"
"Ra 132,11,0\nv_and_b32 v51, 0xffff0000, v47\nMaaa 32,160,200\nMU 52,48,50\nMaaa 64,160,208\nRa 136,10,2048\nMU 53,49,50\nMaaa 64,164,212\nD0 6,24,44,0\nCV 52,52,53\n"
"Maaa 96,164,220\nRa 140,11,2048\nMaaa 96,160,216\nv_and_b32 v53, 0xffff0000, v52\nv_lshlrev_b32 v52, 16, v52\nMaaa 100,168,216\nRa 144,10,8192\nFM 56,48,51\nMaaa 100,172,220\n"
"FM 57,49,51\n.W571:\ns_cbranch_execz .W575\nMaaa 68,172,212\nRa 148,11,8192\nAD 54,54,52\nMaaa 68,168,208\nAD 55,55,53\nMaaa 36,168,200\nRa 152,10,10240\n"
"v_permlane32_swap_b32 v54, v55\nMaaa 36,172,204\nD0 7,24,44,1024\nv_permlane32_swap_b32 v56, v57\nMaaa 4,172,196\nRa 156,11,10240\nMaaa 4,168,192\nAD 54,54,55\nAD 56,56,57\n"
"Maaa 8,176,192\nMaaa 8,180,196\nv_permlane16_swap_b32 v54, v56\nMaaa 40,180,204\nAD 54,54,56\nMaaa 40,176,200\nMaaa 72,176,208\n"
"v_add_f32_dpp v54, v54, v54 row_ror:8 row_mask:0xf bank_mask:0xf\nMaaa 72,180,212\nD0 8,24,44,2048\nMaaa 104,180,220\n"
"v_add_f32_dpp v54, v54, v54 row_ror:4 row_mask:0xf bank_mask:0xf\nMaaa 104,176,216\nMaaa 108,184,216\nv_add_f32_dpp v54, v54, v54 row_ror:2 row_mask:0xf bank_mask:0xf\n"
"Maaa 108,188,220\nMaaa 76,188,212\nv_add_f32_dpp v54, v54, v54 row_ror:1 row_mask:0xf bank_mask:0xf\nCV 54,54,54\nMaaa 76,184,208\nv_lshlrev_b32 v54, 16, v54\nMaaa 44,184,200\n"
"v_mul_f32 v55, 0xbfb8aa3b, v54\nMaaa 44,188,204\nD0 9,24,44,3072\nMaaa 12,188,196\nEX 55,55\nMaaa 12,184,192\ns_waitcnt vmcnt(12) lgkmcnt(0)\ns_barrier\nMaaa 16,224,192\n"
"Ta 160,12,0\nv_add_f32 v56, 0, v54\nMaaa 16,228,196\nTa 162,12,2048\nMaaa 48,228,204\ns_add_u32 m0, s47, -16384\ns_add_u32 s48, s48, 128\nMaaa 48,224,200\nTa 164,12,16384\n"
"Maaa 80,224,208\nTa 166,12,18432\nbuffer_load_dword v31, v24, s[24:27], s96 offen\ns_add_u32 s96, s96, 0x80000\nMaaa 80,228,212\nD0 2,20,48,0\nv_add_f32 v55, 1.0, v55\n"
"Maaa 112,228,220\nTa 168,13,0\nv_rcp_f32 v55, v55\nMaaa 112,224,216\nTa 170,13,2048\nv_ashrrev_i32 v57, 31, v56\nMaaa 116,232,216\nCV 55,55,55\nv_lshlrev_b32 v55, 16, v55\n"
"Maaa 116,236,220\nTa 172,13,16384\nMaaa 84,236,212\nTa 174,13,18432\nv_or_b32 v57, 0x80000000, v57\nMaaa 84,232,208\nv_xor_b32 v56, v56, v57\nMaaa 52,232,200\nTa 176,14,0\n"
"v_and_b32 v56, 0xffff0000, v56\nMaaa 52,236,204\nTa 178,14,2048\ns_lshl2_add_u32 s2, s10, s11\nMaaa 20,236,196\nD0 3,20,48,1024\nMaaa 20,232,192\nTa 180,14,16384\n"
"v_lshrrev_b32 v48, 5, v0\nMaaa 24,240,192\nTa 182,14,18432\nv_lshl_add_u32 v48, s2, 1, v48\nMaaa 24,244,196\nv_sub_u32 v49, 0xffff, v48\nMaaa 56,244,204\nTa 184,15,0\n"
"v_or_b32 v56, v56, v49\nMaaa 56,240,200\nTa 186,15,2048\nMaaa 88,240,208\nv_mov_b32 v57, s15\nv_and_b32 v49, 31, v0\nMaaa 88,244,212\nTa 188,15,16384\nv_mov_b32 v52, 0x40000000\n"
"Maaa 120,244,220\nTa 190,15,18432\nv_cmp_eq_u32 vcc, 16, v49\nv_lshlrev_b32 v50, 2, v48\nv_lshlrev_b32 v51, 10, v49\nv_cndmask_b32 v50, v52, v50, vcc\n"
"v_cndmask_b32 v56, v55, v56, vcc\nMaaa 120,240,216\nD0 4,20,48,2048\nv_lshl_add_u32 v51, v48, 3, v51\nMaaa 124,248,216\nv_and_b32 v49, 15, v0\nv_cmp_eq_u32 vcc, 0, v49\n"
"s_mov_b32 s2, 0xc040000\ns_mov_b32 s3, 0xc03e000\nv_cndmask_b32 v51, v52, v51, vcc\nbuffer_store_dwordx2 v[56:57], v51, s[16:19], s2 offen sc0 sc1\n"
"buffer_store_dword v54, v50, s[16:19], s3 offen sc0 sc1\nMaaa 124,252,220\nMaaa 92,252,212\nMaaa 92,248,208\nMaaa 60,248,200\nMaaa 60,252,204\nD0 5,20,48,3072\nMaaa 28,252,196\n"
"Maaa 28,248,192\nWL 0\ns_mov_b32 s50, 3\n.GL273:\nMvaa 64,160,128\nTa 224,16,0\nMvaa 64,164,132\nTa 226,16,2048\nMvaa 96,164,140\nTa 228,16,16384\ns_add_u32 m0, s47, -65536\n"
"s_add_u32 s45, s45, 128\nMvaa 96,160,136\nTa 230,16,18432\nMvaa 128,160,144\nTa 232,17,0\nMvaa 128,164,148\nTa 234,17,2048\nD0 2,20,45,0\nMvaa 160,164,156\nTa 236,17,16384\n"
"Mvaa 160,160,152\nTa 238,17,18432\nMvaa 164,168,152\nTa 240,18,0\nMvaa 164,172,156\nTa 242,18,2048\nMvaa 132,172,148\nTa 244,18,16384\nMvaa 132,168,144\nTa 246,18,18432\n"
"Mvaa 100,168,136\nTa 248,19,0\nMvaa 100,172,140\nTa 250,19,2048\nMvaa 68,172,132\nTa 252,19,16384\nD0 3,20,45,1024\nMvaa 68,168,128\nTa 254,19,18432\nMvaa 72,176,128\n"
"Ra 192,10,16384\nMvaa 72,180,132\nRa 196,11,16384\nMvaa 104,180,140\nRa 200,10,18432\nMvaa 104,176,136\nRa 204,11,18432\nMvaa 136,176,144\nRa 208,10,24576\nMvaa 136,180,148\n"
"Ra 212,11,24576\nMvaa 168,180,156\nRa 216,10,26624\nMvaa 168,176,152\nRa 220,11,26624\nD0 4,20,45,2048\nMvaa 172,184,152\nMvaa 172,188,156\nMvaa 140,188,148\nMvaa 140,184,144\n"
"Mvaa 108,184,136\nMvaa 108,188,140\nD0 5,20,45,3072\nMvaa 76,188,132\nMvaa 76,184,128\ns_waitcnt vmcnt(18) lgkmcnt(8)\ns_barrier\nMvaa 80,224,128\nMvaa 80,228,132\n"
"s_add_u32 m0, s47, 0\ns_add_u32 s44, s44, 0x40000\ns_mov_b32 s86, 1\ns_load_dword s86, s[54:55], s94\nMvaa 112,228,140\ns_add_u32 s94, s94, 1024\nMvaa 112,224,136\n"
"Mvaa 144,224,144\nMvaa 144,228,148\nD0 6,24,44,0\nMvaa 176,228,156\nMvaa 176,224,152\nMvaa 180,232,152\nMvaa 180,236,156\nMvaa 148,236,148\nMvaa 148,232,144\n"
"s_add_u32 s88, s88, s86\nMvaa 116,232,136\nMvaa 116,236,140\nD0 7,24,44,1024\ns_add_u32 s88, s88, s86\nMvaa 84,236,132\nMvaa 84,232,128\ns_add_u32 s88, s88, s86\nMvaa 88,240,128\n"
"Mvaa 88,244,132\ns_add_u32 s88, s88, s86\nMvaa 120,244,140\nMvaa 120,240,136\ns_add_u32 s88, s88, s86\nMvaa 152,240,144\nMvaa 152,244,148\nD0 8,24,44,2048\n"
"s_add_u32 s88, s88, s86\nMvaa 184,244,156\nMvaa 184,240,152\ns_add_u32 s88, s88, s86\nMvaa 188,248,152\nMvaa 188,252,156\ns_add_u32 s88, s88, s86\nMvaa 156,252,148\n"
"Mvaa 156,248,144\ns_add_u32 s88, s88, s86\nMvaa 124,248,136\nMvaa 124,252,140\nD0 9,24,44,3072\ns_add_u32 s88, s88, s86\nMvaa 92,252,132\nMvaa 92,248,128\nWL 0\n"
"s_add_u32 s88, s88, s86\nMaaa 16,224,192\nRa 128,10,32768\nMaaa 16,228,196\ns_add_u32 m0, s47, 16384\ns_add_u32 s44, s44, 0x40000\ns_add_u32 s88, s88, s86\nMaaa 48,228,204\n"
"Ra 132,11,32768\nMaaa 48,224,200\ns_add_u32 s88, s88, s86\nMaaa 80,224,208\nRa 136,10,34816\nMaaa 80,228,212\nD0 6,24,44,0\ns_add_u32 s88, s88, s86\nMaaa 112,228,220\n"
"Ra 140,11,34816\nMaaa 112,224,216\ns_add_u32 s88, s88, s86\nMaaa 116,232,216\nRa 144,10,40960\nMaaa 116,236,220\ns_add_u32 s88, s88, s86\nMaaa 84,236,212\nRa 148,11,40960\n"
"Maaa 84,232,208\nMaaa 52,232,200\nRa 152,10,43008\nMaaa 52,236,204\nD0 7,24,44,1024\nMaaa 20,236,196\nRa 156,11,43008\nMaaa 20,232,192\nMaaa 24,240,192\nMaaa 24,244,196\n"
"Maaa 56,244,204\nMaaa 56,240,200\nMaaa 88,240,208\nMaaa 88,244,212\nD0 8,24,44,2048\nMaaa 120,244,220\nMaaa 120,240,216\nMaaa 124,248,216\nMaaa 124,252,220\nMaaa 92,252,212\n"
"Maaa 92,248,208\nMaaa 60,248,200\nMaaa 60,252,204\nD0 9,24,44,3072\nMaaa 28,252,196\nMaaa 28,248,192\ns_waitcnt vmcnt(12) lgkmcnt(0)\ns_barrier\nMaaa 0,160,192\nTa 224,16,32768\n"
"s_mov_b32 m0, s76\ns_add_u32 s76, s76, 1024\nMaaa 0,164,196\nTa 226,16,34816\nD0 1,36,75,0\ns_add_u32 s75, s75, 1024\nMaaa 32,164,204\ns_add_u32 m0, s47, -49152\n"
"s_add_u32 s48, s48, 128\nMaaa 32,160,200\nTa 228,16,49152\nMaaa 64,160,208\nTa 230,16,51200\nbuffer_load_dword v31, v24, s[24:27], s96 offen\ns_add_u32 s96, s96, 0x80000\n"
"Maaa 64,164,212\nD0 2,20,48,0\nMaaa 96,164,220\nTa 232,17,32768\nMaaa 96,160,216\nTa 234,17,34816\nMaaa 100,168,216\nMaaa 100,172,220\nTa 236,17,49152\nMaaa 68,172,212\n"
"Ta 238,17,51200\nMaaa 68,168,208\nMaaa 36,168,200\nTa 240,18,32768\nMaaa 36,172,204\nTa 242,18,34816\nMaaa 4,172,196\nD0 3,20,48,1024\nMaaa 4,168,192\nTa 244,18,49152\n"
"Maaa 8,176,192\nTa 246,18,51200\nMaaa 8,180,196\nMaaa 40,180,204\nTa 248,19,32768\nMaaa 40,176,200\nTa 250,19,34816\nMaaa 72,176,208\nMaaa 72,180,212\nTa 252,19,49152\n"
"Maaa 104,180,220\nTa 254,19,51200\nMaaa 104,176,216\nD0 4,20,48,2048\nMaaa 108,184,216\nMaaa 108,188,220\nMaaa 76,188,212\nMaaa 76,184,208\nMaaa 44,184,200\nMaaa 44,188,204\n"
"D0 5,20,48,3072\nMaaa 12,188,196\nMaaa 12,184,192\nWL 0\nMvaa 80,224,128\nTa 160,12,32768\nMvaa 80,228,132\nTa 162,12,34816\nMvaa 112,228,140\nTa 164,12,49152\n"
"s_add_u32 m0, s47, -32768\ns_add_u32 s45, s45, 128\nMvaa 112,224,136\nTa 166,12,51200\nMvaa 144,224,144\nTa 168,13,32768\nMvaa 144,228,148\nTa 170,13,34816\nD0 2,20,45,0\n"
"Mvaa 176,228,156\nTa 172,13,49152\nMvaa 176,224,152\nTa 174,13,51200\nMvaa 180,232,152\nTa 176,14,32768\nMvaa 180,236,156\nTa 178,14,34816\nMvaa 148,236,148\nTa 180,14,49152\n"
"Mvaa 148,232,144\nTa 182,14,51200\nMvaa 116,232,136\nTa 184,15,32768\nMvaa 116,236,140\nTa 186,15,34816\nMvaa 84,236,132\nTa 188,15,49152\nD0 3,20,45,1024\nMvaa 84,232,128\n"
"Ta 190,15,51200\nMvaa 88,240,128\nRa 192,10,49152\nMvaa 88,244,132\nRa 196,11,49152\nMvaa 120,244,140\nRa 200,10,51200\nMvaa 120,240,136\nRa 204,11,51200\nMvaa 152,240,144\n"
"Ra 208,10,57344\nMvaa 152,244,148\nRa 212,11,57344\nMvaa 184,244,156\nRa 216,10,59392\nMvaa 184,240,152\nRa 220,11,59392\nD0 4,20,45,2048\nMvaa 188,248,152\nMvaa 188,252,156\n"
"Mvaa 156,252,148\nMvaa 156,248,144\nMvaa 124,248,136\nMvaa 124,252,140\nD0 5,20,45,3072\nMvaa 92,252,132\nMvaa 92,248,128\ns_waitcnt vmcnt(18) lgkmcnt(8)\ns_barrier\n"
"Mvaa 64,160,128\nMvaa 64,164,132\ns_add_u32 m0, s47, 32768\ns_add_u32 s44, s44, 0x40000\ns_mov_b32 s86, 1\ns_load_dword s86, s[54:55], s95\nMvaa 96,164,140\n"
"s_add_u32 s95, s95, 1024\nMvaa 96,160,136\nMvaa 128,160,144\nMvaa 128,164,148\nD0 6,24,44,0\nMvaa 160,164,156\nMvaa 160,160,152\nMvaa 164,168,152\nMvaa 164,172,156\n"
"Mvaa 132,172,148\nMvaa 132,168,144\ns_sub_u32 s88, s88, s86\nMvaa 100,168,136\nMvaa 100,172,140\nD0 7,24,44,1024\ns_sub_u32 s88, s88, s86\nMvaa 68,172,132\nMvaa 68,168,128\n"
"s_sub_u32 s88, s88, s86\nMvaa 72,176,128\nMvaa 72,180,132\ns_sub_u32 s88, s88, s86\nMvaa 104,180,140\nMvaa 104,176,136\ns_sub_u32 s88, s88, s86\nMvaa 136,176,144\n"
"Mvaa 136,180,148\nD0 8,24,44,2048\ns_sub_u32 s88, s88, s86\nMvaa 168,180,156\nMvaa 168,176,152\ns_sub_u32 s88, s88, s86\nMvaa 172,184,152\nMvaa 172,188,156\n"
"s_sub_u32 s88, s88, s86\nMvaa 140,188,148\nMvaa 140,184,144\ns_sub_u32 s88, s88, s86\nMvaa 108,184,136\nMvaa 108,188,140\nD0 9,24,44,3072\ns_sub_u32 s88, s88, s86\n"
"Mvaa 76,188,132\nMvaa 76,184,128\nWL 0\ns_sub_u32 s88, s88, s86\nMaaa 0,160,192\nRa 128,10,0\nMaaa 0,164,196\ns_add_u32 m0, s47, 49152\ns_add_u32 s44, s44, 0x40000\n"
"s_sub_u32 s88, s88, s86\nMaaa 32,164,204\nRa 132,11,0\nMaaa 32,160,200\ns_sub_u32 s88, s88, s86\nMaaa 64,160,208\nRa 136,10,2048\nMaaa 64,164,212\nD0 6,24,44,0\n"
"s_sub_u32 s88, s88, s86\nMaaa 96,164,220\nRa 140,11,2048\nMaaa 96,160,216\ns_sub_u32 s88, s88, s86\nMaaa 100,168,216\nRa 144,10,8192\nMaaa 100,172,220\ns_sub_u32 s88, s88, s86\n"
"Maaa 68,172,212\nRa 148,11,8192\nMaaa 68,168,208\nMaaa 36,168,200\nRa 152,10,10240\nMaaa 36,172,204\nD0 7,24,44,1024\nMaaa 4,172,196\nRa 156,11,10240\nMaaa 4,168,192\n"
"Maaa 8,176,192\nMaaa 8,180,196\nMaaa 40,180,204\nMaaa 40,176,200\nMaaa 72,176,208\nMaaa 72,180,212\nD0 8,24,44,2048\nMaaa 104,180,220\nMaaa 104,176,216\nMaaa 108,184,216\n"
"Maaa 108,188,220\nMaaa 76,188,212\nMaaa 76,184,208\nMaaa 44,184,200\nMaaa 44,188,204\nD0 9,24,44,3072\nMaaa 12,188,196\nMaaa 12,184,192\ns_waitcnt vmcnt(12) lgkmcnt(0)\ns_barrier\n"
"Maaa 16,224,192\nTa 160,12,0\ns_mov_b32 m0, s76\ns_add_u32 s76, s76, 1024\nMaaa 16,228,196\nTa 162,12,2048\nD0 1,36,75,0\ns_add_u32 s75, s75, 1024\nMaaa 48,228,204\n"
"s_add_u32 m0, s47, -16384\ns_add_u32 s48, s48, 128\nMaaa 48,224,200\nTa 164,12,16384\nMaaa 80,224,208\nTa 166,12,18432\nbuffer_load_dword v31, v24, s[24:27], s96 offen\n"
"s_add_u32 s96, s96, 0x80000\nMaaa 80,228,212\n.W572:\ns_cbranch_execz .W576\nD0 2,20,48,0\nMaaa 112,228,220\nTa 168,13,0\nMaaa 112,224,216\nTa 170,13,2048\nMaaa 116,232,216\n"
"Maaa 116,236,220\nTa 172,13,16384\nMaaa 84,236,212\nTa 174,13,18432\nMaaa 84,232,208\nMaaa 52,232,200\nTa 176,14,0\nMaaa 52,236,204\nTa 178,14,2048\nMaaa 20,236,196\n"
"D0 3,20,48,1024\nMaaa 20,232,192\nTa 180,14,16384\nMaaa 24,240,192\nTa 182,14,18432\nMaaa 24,244,196\nMaaa 56,244,204\nTa 184,15,0\nMaaa 56,240,200\nTa 186,15,2048\n"
"Maaa 88,240,208\nMaaa 88,244,212\nTa 188,15,16384\nMaaa 120,244,220\nTa 190,15,18432\nMaaa 120,240,216\nD0 4,20,48,2048\nMaaa 124,248,216\nMaaa 124,252,220\nMaaa 92,252,212\n"
"Maaa 92,248,208\nMaaa 60,248,200\ns_xor_b32 s94, s94, 0x2000\ns_xor_b32 s95, s95, 0x2000\ns_sub_u32 s88, 0, s88\nMaaa 60,252,204\nD0 5,20,48,3072\ns_sub_u32 s50, s50, 1\n"
"Maaa 28,252,196\nMaaa 28,248,192\nWL 0\ns_cbranch_scc0 .GL273\nMvaa 64,160,128\nTa 224,16,0\nMvaa 64,164,132\nTa 226,16,2048\ns_cmp_gt_i32 s88, 0\ns_cselect_b32 s88, 0x2000, 0\n"
"Mvaa 96,164,140\ns_add_u32 m0, s47, -65536\ns_add_u32 s45, s45, 128\nMvaa 96,160,136\nTa 228,16,16384\nMvaa 128,160,144\nTa 230,16,18432\nMvaa 128,164,148\nD0 2,20,45,0\n"
"Mvaa 160,164,156\nTa 232,17,0\nMvaa 160,160,152\nTa 234,17,2048\nMvaa 164,168,152\ns_xor_b32 s88, s88, s92\nMvaa 164,172,156\nTa 236,17,16384\nMvaa 132,172,148\nTa 238,17,18432\n"
"Mvaa 132,168,144\nv_xor_b32 v27, s88, v25\nMvaa 100,168,136\nTa 240,18,0\nMvaa 100,172,140\nTa 242,18,2048\nMvaa 68,172,132\nD0 3,20,45,1024\nMvaa 68,168,128\nTa 244,18,16384\n"
"Mvaa 72,176,128\nTa 246,18,18432\nMvaa 72,180,132\nbuffer_store_dword v27, off, s[16:19], s93 sc0 sc1\nMvaa 104,180,140\nTa 248,19,0\nMvaa 104,176,136\nTa 250,19,2048\n"
"Mvaa 136,176,144\nMvaa 136,180,148\nTa 252,19,16384\nMvaa 168,180,156\nTa 254,19,18432\nMvaa 168,176,152\nD0 4,20,45,2048\nMvaa 172,184,152\nMvaa 172,188,156\nMvaa 140,188,148\n"
"Mvaa 140,184,144\nMvaa 108,184,136\nMvaa 108,188,140\nD0 5,20,45,3072\nMvaa 76,188,132\nMvaa 76,184,128\ns_waitcnt vmcnt(19) lgkmcnt(0)\ns_barrier\nMvaa 80,224,128\n"
"Ra 192,10,16384\nMvaa 80,228,132\ns_add_u32 m0, s47, 0\ns_add_u32 s44, s44, 0x40000\nMvaa 112,228,140\nRa 196,11,16384\nMvaa 112,224,136\nMvaa 144,224,144\nRa 200,10,18432\n"
"Mvaa 144,228,148\nD0 6,24,44,0\nMvaa 176,228,156\nRa 204,11,18432\nMvaa 176,224,152\nMvaa 180,232,152\nRa 208,10,24576\nMvaa 180,236,156\nMvaa 148,236,148\nRa 212,11,24576\n"
"Mvaa 148,232,144\nMvaa 116,232,136\nRa 216,10,26624\nMvaa 116,236,140\nD0 7,24,44,1024\nMvaa 84,236,132\nRa 220,11,26624\nMvaa 84,232,128\nMvaa 88,240,128\nMvaa 88,244,132\n"
"Mvaa 120,244,140\nMvaa 120,240,136\nMvaa 152,240,144\nMvaa 152,244,148\nD0 8,24,44,2048\nMvaa 184,244,156\nMvaa 184,240,152\nMvaa 188,248,152\nMvaa 188,252,156\nMvaa 156,252,148\n"
"Mvaa 156,248,144\nMvaa 124,248,136\nMvaa 124,252,140\nD0 9,24,44,3072\nMvaa 92,252,132\nMvaa 92,248,128\nWL 0\nMaaa 16,224,192\nRa 128,10,32768\nMaaa 16,228,196\n"
"s_add_u32 m0, s47, 16384\ns_add_u32 s44, s44, 0x40000\nMaaa 48,228,204\nRa 132,11,32768\nMaaa 48,224,200\nMaaa 80,224,208\nRa 136,10,34816\nMaaa 80,228,212\nD0 6,24,44,0\n"
"Maaa 112,228,220\nRa 140,11,34816\nMaaa 112,224,216\nMaaa 116,232,216\nRa 144,10,40960\nMaaa 116,236,220\nMaaa 84,236,212\nRa 148,11,40960\nMaaa 84,232,208\nMaaa 52,232,200\n"
"Ra 152,10,43008\nMaaa 52,236,204\nD0 7,24,44,1024\nMaaa 20,236,196\nRa 156,11,43008\nMaaa 20,232,192\nMaaa 24,240,192\nMaaa 24,244,196\nMaaa 56,244,204\nMaaa 56,240,200\n"
"Maaa 88,240,208\nMaaa 88,244,212\nD0 8,24,44,2048\nMaaa 120,244,220\nMaaa 120,240,216\nMaaa 124,248,216\nMaaa 124,252,220\nMaaa 92,252,212\nMaaa 92,248,208\nMaaa 60,248,200\n"
"Maaa 60,252,204\nD0 9,24,44,3072\nMaaa 28,252,196\nMaaa 28,248,192\ns_waitcnt vmcnt(19) lgkmcnt(0)\ns_barrier\nMaaa 0,160,192\nTa 224,16,32768\nMaaa 0,164,196\nTa 226,16,34816\n"
"Maaa 32,164,204\ns_add_u32 m0, s47, -49152\ns_add_u32 s48, s48, 128\nMaaa 32,160,200\nTa 228,16,49152\nMaaa 64,160,208\nTa 230,16,51200\n"
"buffer_load_dword v31, v24, s[24:27], s96 offen\ns_add_u32 s96, s96, 0x80000\nMaaa 64,164,212\nD0 2,20,48,0\nMaaa 96,164,220\nTa 232,17,32768\nMaaa 96,160,216\nTa 234,17,34816\n"
"Maaa 100,168,216\nMaaa 100,172,220\nTa 236,17,49152\nMaaa 68,172,212\nTa 238,17,51200\nMaaa 68,168,208\nMaaa 36,168,200\nTa 240,18,32768\nMaaa 36,172,204\nTa 242,18,34816\n"
"Maaa 4,172,196\nD0 3,20,48,1024\nMaaa 4,168,192\nTa 244,18,49152\nMaaa 8,176,192\nTa 246,18,51200\nMaaa 8,180,196\nMaaa 40,180,204\nTa 248,19,32768\nMaaa 40,176,200\n"
"Ta 250,19,34816\nMaaa 72,176,208\nMaaa 72,180,212\nTa 252,19,49152\nMaaa 104,180,220\nTa 254,19,51200\nMaaa 104,176,216\nD0 4,20,48,2048\nMaaa 108,184,216\nMaaa 108,188,220\n"
"Maaa 76,188,212\nMaaa 76,184,208\nMaaa 44,184,200\nMaaa 44,188,204\nD0 5,20,48,3072\nMaaa 12,188,196\nMaaa 12,184,192\nWL 0\nMvaa 80,224,128\nTa 160,12,32768\nMvaa 80,228,132\n"
"Ta 162,12,34816\nMvaa 112,228,140\ns_add_u32 m0, s47, -32768\ns_add_u32 s45, s45, 128\nMvaa 112,224,136\nTa 164,12,49152\nMvaa 144,224,144\nTa 166,12,51200\nMvaa 144,228,148\n"
"D0 2,20,45,0\nMvaa 176,228,156\nTa 168,13,32768\nMvaa 176,224,152\nTa 170,13,34816\nMvaa 180,232,152\nMvaa 180,236,156\nTa 172,13,49152\nMvaa 148,236,148\nTa 174,13,51200\n"
"Mvaa 148,232,144\nMvaa 116,232,136\nTa 176,14,32768\nMvaa 116,236,140\nTa 178,14,34816\nMvaa 84,236,132\nD0 3,20,45,1024\nMvaa 84,232,128\nTa 180,14,49152\nMvaa 88,240,128\n"
"Ta 182,14,51200\nMvaa 88,244,132\nMvaa 120,244,140\nTa 184,15,32768\nMvaa 120,240,136\nTa 186,15,34816\nMvaa 152,240,144\nMvaa 152,244,148\nTa 188,15,49152\nMvaa 184,244,156\n"
"Ta 190,15,51200\nMvaa 184,240,152\nD0 4,20,45,2048\nMvaa 188,248,152\nMvaa 188,252,156\nMvaa 156,252,148\nMvaa 156,248,144\nMvaa 124,248,136\nMvaa 124,252,140\nD0 5,20,45,3072\n"
"Mvaa 92,252,132\nMvaa 92,248,128\ns_waitcnt vmcnt(17) lgkmcnt(0)\ns_barrier\nMvaa 64,160,128\nRa 192,10,49152\nMvaa 64,164,132\ns_add_u32 m0, s47, 32768\n"
"s_add_u32 s44, s44, 0x40000\nMvaa 96,164,140\nRa 196,11,49152\nMvaa 96,160,136\nMvaa 128,160,144\nRa 200,10,51200\nMvaa 128,164,148\nD0 6,24,44,0\nMvaa 160,164,156\n"
"Ra 204,11,51200\nMvaa 160,160,152\nMvaa 164,168,152\nRa 208,10,57344\nMvaa 164,172,156\ns_lshl_b32 s5, s73, 8\ns_add_u32 s5, s5, 0xc04ac00\nv_lshrrev_b32 v27, 2, v1\n"
"buffer_load_dword v27, v27, s[16:19], s5 offen sc0 sc1\nMvaa 132,172,148\nRa 212,11,57344\nMvaa 132,168,144\nMvaa 100,168,136\nRa 216,10,59392\nMvaa 100,172,140\nD0 7,24,44,1024\n"
"Mvaa 68,172,132\nRa 220,11,59392\nMvaa 68,168,128\nMvaa 72,176,128\nMvaa 72,180,132\nMvaa 104,180,140\nMvaa 104,176,136\nMvaa 136,176,144\nMvaa 136,180,148\nD0 8,24,44,2048\n"
"Mvaa 168,180,156\nMvaa 168,176,152\nMvaa 172,184,152\nMvaa 172,188,156\nMvaa 140,188,148\nMvaa 140,184,144\nMvaa 108,184,136\nMvaa 108,188,140\nD0 9,24,44,3072\nMvaa 76,188,132\n"
"Mvaa 76,184,128\nWL 0\nMaaa 0,160,192\nRa 128,10,0\nMaaa 0,164,196\ns_add_u32 m0, s47, 49152\ns_add_u32 s44, s44, 0x40000\nMaaa 32,164,204\nRa 132,11,0\nMaaa 32,160,200\n"
"Maaa 64,160,208\nRa 136,10,2048\nMaaa 64,164,212\nD0 6,24,44,0\nMaaa 96,164,220\nRa 140,11,2048\nMaaa 96,160,216\nMaaa 100,168,216\nRa 144,10,8192\nMaaa 100,172,220\n"
"Maaa 68,172,212\nRa 148,11,8192\nMaaa 68,168,208\nMaaa 36,168,200\nRa 152,10,10240\nMaaa 36,172,204\nD0 7,24,44,1024\nMaaa 4,172,196\nRa 156,11,10240\nMaaa 4,168,192\n"
"Maaa 8,176,192\nMaaa 8,180,196\nMaaa 40,180,204\nMaaa 40,176,200\nMaaa 72,176,208\nMaaa 72,180,212\nD0 8,24,44,2048\nMaaa 104,180,220\nMaaa 104,176,216\nMaaa 108,184,216\n"
"Maaa 108,188,220\nMaaa 76,188,212\nMaaa 76,184,208\nMaaa 44,184,200\nMaaa 44,188,204\nD0 9,24,44,3072\nMaaa 12,188,196\nMaaa 12,184,192\ns_waitcnt vmcnt(18) lgkmcnt(0)\ns_barrier\n"
"Maaa 16,224,192\nTa 160,12,0\nMaaa 16,228,196\nTa 162,12,2048\nMaaa 48,228,204\ns_add_u32 m0, s47, -16384\ns_add_u32 s48, s48, 128\nMaaa 48,224,200\nTa 164,12,16384\n"
"Maaa 80,224,208\nTa 166,12,18432\nbuffer_load_dword v31, v24, s[24:27], s96 offen\ns_add_u32 s96, s96, 0x80000\nMaaa 80,228,212\nD0 2,20,48,0\nMaaa 112,228,220\nTa 168,13,0\n"
"Maaa 112,224,216\nTa 170,13,2048\nMaaa 116,232,216\nMaaa 116,236,220\nTa 172,13,16384\nMaaa 84,236,212\nTa 174,13,18432\nMaaa 84,232,208\nMaaa 52,232,200\nTa 176,14,0\n"
"Maaa 52,236,204\nTa 178,14,2048\nMaaa 20,236,196\nD0 3,20,48,1024\nMaaa 20,232,192\nTa 180,14,16384\nMaaa 24,240,192\nTa 182,14,18432\nMaaa 24,244,196\nMaaa 56,244,204\n"
"Ta 184,15,0\nMaaa 56,240,200\nTa 186,15,2048\nMaaa 88,240,208\nMaaa 88,244,212\nTa 188,15,16384\nMaaa 120,244,220\nTa 190,15,18432\nMaaa 120,240,216\nD0 4,20,48,2048\n"
"Maaa 124,248,216\nMaaa 124,252,220\nMaaa 92,252,212\nMaaa 92,248,208\nMaaa 60,248,200\nMaaa 60,252,204\nD0 5,20,48,3072\nMaaa 28,252,196\nMaaa 28,248,192\nWL 0\nMvaa 64,160,128\n"
"Ta 224,16,0\nWV 12\nv_xor_b32 v27, s15, v27\nv_and_b32 v32, 0xffffdfff, v27\nv_cmp_ne_u32 vcc, 0, v32\ns_nop 0\ns_cbranch_vccnz .S270\n.S271:\nv_cmp_ne_u32 vcc, 0, v27\ns_nop 3\n"
"s_bcnt1_i32_b64 s4, vcc\ns_cmp_gt_u32 s4, 32\ns_cselect_b32 s4, 1, 0\nMvaa 64,164,132\nTa 226,16,2048\nMvaa 96,164,140\ns_add_u32 m0, s47, -65536\ns_add_u32 s45, s45, 128\n"
"Mvaa 96,160,136\nTa 228,16,16384\nMvaa 128,160,144\nTa 230,16,18432\nMvaa 128,164,148\nD0 2,20,45,0\nMvaa 160,164,156\nTa 232,17,0\nMvaa 160,160,152\nTa 234,17,2048\n"
"Mvaa 164,168,152\ns_xor_b32 s4, s4, s12\nMvaa 164,172,156\nTa 236,17,16384\nMvaa 132,172,148\nTa 238,17,18432\nMvaa 132,168,144\ns_and_b32 s4, s4, 1\nMvaa 100,168,136\n"
"Ta 240,18,0\nMvaa 100,172,140\nTa 242,18,2048\nMvaa 68,172,132\nD0 3,20,45,1024\nMvaa 68,168,128\nTa 244,18,16384\nMvaa 72,176,128\nTa 246,18,18432\nMvaa 72,180,132\n"
"s_lshl_b32 s4, s4, 13\nMvaa 104,180,140\nTa 248,19,0\nMvaa 104,176,136\nTa 250,19,2048\nMvaa 136,176,144\ns_mul_i32 s72, s73, 25165824\nMvaa 136,180,148\nTa 252,19,16384\n.W573:\n"
"s_cbranch_execz .W577\nMvaa 168,180,156\nTa 254,19,18432\nMvaa 168,176,152\nD0 4,20,45,2048\nMvaa 172,184,152\ns_add_u32 s72, s72, s4\nMvaa 172,188,156\ns_and_b32 s4, s12, 1\n"
"Mvaa 140,188,148\ns_lshl_b32 s4, s4, 21\nMvaa 140,184,144\ns_add_u32 s72, s72, s4\nMvaa 108,184,136\ns_lshl3_add_u32 s2, s13, s11\nMvaa 108,188,140\nD0 5,20,45,3072\n"
"Mvaa 76,188,132\ns_lshl_b32 s2, s2, 11\nMvaa 76,184,128\nWL 0\ns_barrier\nMvaa 80,224,128\nRa 192,10,16384\nMvaa 80,228,132\ns_add_u32 m0, s47, 0\ns_add_u32 s44, s44, 0x40000\n"
"Mvaa 112,228,140\nRa 196,11,16384\nMvaa 112,224,136\ns_lshl_b32 s3, s14, 22\nMvaa 144,224,144\nRa 200,10,18432\nMvaa 144,228,148\nD0 6,24,44,0\nMvaa 176,228,156\nRa 204,11,18432\n"
"Mvaa 176,224,152\ns_add_u32 s2, s2, s3\nMvaa 180,232,152\nRa 208,10,24576\nMvaa 180,236,156\ns_add_u32 s2, s2, s72\nMvaa 148,236,148\nRa 212,11,24576\nMvaa 148,232,144\n"
"s_add_u32 s49, s2, 0xc00000\nMvaa 116,232,136\nRa 216,10,26624\nMvaa 116,236,140\nD0 7,24,44,1024\nMvaa 84,236,132\nRa 220,11,26624\nMvaa 84,232,128\nMvaa 88,240,128\n"
"Mvaa 88,244,132\nMvaa 120,244,140\nMvaa 120,240,136\nMvaa 152,240,144\nMvaa 152,244,148\nD0 8,24,44,2048\nMvaa 184,244,156\nMvaa 184,240,152\nMvaa 188,248,152\nMvaa 188,252,156\n"
"Mvaa 156,252,148\nMvaa 156,248,144\nMvaa 124,248,136\nMvaa 124,252,140\nD0 9,24,44,3072\nMvaa 92,252,132\nMvaa 92,248,128\nWL 0\nMaaa 16,224,192\nRa 128,10,32768\n"
"s_mov_b32 s4, s13\nMaaa 16,228,196\ns_add_u32 m0, s47, 16384\ns_add_u32 s44, s44, 0x40000\nMaaa 48,228,204\nRa 132,11,32768\nv_lshrrev_b32 v32, 2, v0\nMaaa 48,224,200\n"
"v_mul_u32_u24 v32, 896, v32\nMaaa 80,224,208\nRa 136,10,34816\nv_and_b32 v33, 3, v0\nMaaa 80,228,212\nD0 6,24,44,0\nMaaa 112,228,220\nRa 140,11,34816\n"
"v_lshl_add_u32 v32, v33, 4, v32\nMaaa 112,224,216\ns_lshr_b32 s2, s11, 1\nMaaa 116,232,216\nRa 144,10,40960\ns_mul_i32 s3, s2, 448\nMaaa 116,236,220\ns_and_b32 s5, s11, 1\n"
"Maaa 84,236,212\nRa 148,11,40960\ns_lshl3_add_u32 s3, s5, s3\nMaaa 84,232,208\ns_add_u32 s3, s3, 131072\nMaaa 52,232,200\nRa 152,10,43008\nv_add_u32 v32, s3, v32\nMaaa 52,236,204\n"
"D0 7,24,44,1024\nMaaa 20,236,196\nRa 156,11,43008\nTv 36,32,0\nMaaa 20,232,192\nTv 38,32,14336\nMaaa 24,240,192\nTv 40,32,64\nTv 42,32,14400\nMaaa 24,244,196\nTv 44,32,128\n"
"Maaa 56,244,204\nTv 46,32,14464\nTv 48,32,192\nMaaa 56,240,200\nTv 50,32,14528\nMaaa 88,240,208\nTv 52,32,256\nTv 54,32,14592\nMaaa 88,244,212\nD0 8,24,44,2048\nMaaa 120,244,220\n"
"Tv 56,32,320\nTv 58,32,14656\nMaaa 120,240,216\nTv 60,32,384\nMaaa 124,248,216\nTv 62,32,14720\ns_lshr_b32 s0, s4, 1\nMaaa 124,252,220\ns_mul_i32 s0, s0, 12\nMaaa 92,252,212\n"
"s_lshl1_add_u32 s0, s5, s0\ns_and_b32 s6, s4, 1\nMaaa 92,248,208\ns_add_u32 s0, s0, s6\nMaaa 60,248,200\ns_lshl3_add_u32 s1, s5, s4\ns_add_u32 s77, s1, 48\nMaaa 60,252,204\n"
"D0 9,24,44,3072\nMaaa 28,252,196\ns_and_b32 s4, s12, 1\ns_mov_b32 s7, 0x6543210\nMaaa 28,248,192\ns_waitcnt vmcnt(17) lgkmcnt(14)\ns_barrier\nMaaa 0,160,192\nTa 224,16,32768\n"
"s_cmp_lg_u32 s4, s2\ns_cselect_b32 s6, s0, s1\ns_cselect_b32 s5, 4, 16\ns_cselect_b32 s7, 0x6352410, s7\ns_cselect_b32 s3, 0, 229376\nMaaa 0,164,196\nTa 226,16,34816\n"
"Maaa 32,164,204\ns_add_u32 m0, s47, -49152\ns_add_u32 s48, s48, 128\nMaaa 32,160,200\nTa 228,16,49152\nMaaa 64,160,208\nTa 230,16,51200\ns_add_u32 s3, s3, s72\nMaaa 64,164,212\n"
"D0 2,20,48,0\nMaaa 96,164,220\nTa 232,17,32768\ns_add_u32 s3, s3, 0x1400000\nMaaa 96,160,216\nTa 234,17,34816\nMaaa 100,168,216\nWL 14\ns_lshr_b32 s0, s6, 4\nMaaa 100,172,220\n"
"Ta 236,17,49152\nMaaa 68,172,212\nTa 238,17,51200\ns_lshl_b32 s0, s0, 2\nMaaa 68,168,208\ns_lshr_b32 s0, s7, s0\nMaaa 36,168,200\nTa 240,18,32768\ns_and_b32 s0, s0, 7\n"
"Maaa 36,172,204\nTa 242,18,34816\nMaaa 4,172,196\nD0 3,20,48,1024\ns_lshl_b32 s0, s0, 5\nMaaa 4,168,192\nTa 244,18,49152\nMaaa 8,176,192\nTa 246,18,51200\ns_and_b32 s1, s6, 15\n"
"Maaa 8,180,196\ns_add_u32 s0, s0, s1\nMaaa 40,180,204\nTa 248,19,32768\ns_and_b32 s1, s6, 8\nMaaa 40,176,200\nTa 250,19,34816\nMaaa 72,176,208\ns_add_u32 s0, s0, s1\n"
"s_lshl_b32 s0, s0, 10\nMaaa 72,180,212\nTa 252,19,49152\nMaaa 104,180,220\nTa 254,19,51200\ns_add_u32 s0, s0, s3\nMaaa 104,176,216\nD0 4,20,48,2048\nMaaa 108,184,216\n"
"Sv 36,1,16,0,0\ns_add_u32 s6, s6, s5\nMaaa 108,188,220\nMaaa 76,188,212\nMaaa 76,184,208\nMaaa 44,184,200\nMaaa 44,188,204\nD0 5,20,48,3072\nMaaa 12,188,196\nMaaa 12,184,192\n"
"WL 0\nMvaa 80,224,128\nTa 160,12,32768\ns_lshr_b32 s0, s6, 4\nMvaa 80,228,132\nTa 162,12,34816\nMvaa 112,228,140\ns_add_u32 m0, s47, -32768\ns_add_u32 s45, s45, 128\n"
"Mvaa 112,224,136\nTa 164,12,49152\nMvaa 144,224,144\nTa 166,12,51200\ns_lshl_b32 s0, s0, 2\nMvaa 144,228,148\nD0 2,20,45,0\nMvaa 176,228,156\nTa 168,13,32768\n"
"s_lshr_b32 s0, s7, s0\nMvaa 176,224,152\nTa 170,13,34816\nMvaa 180,232,152\ns_and_b32 s0, s0, 7\ns_lshl_b32 s0, s0, 5\nMvaa 180,236,156\nTa 172,13,49152\nMvaa 148,236,148\n"
"Ta 174,13,51200\ns_and_b32 s1, s6, 15\nMvaa 148,232,144\ns_add_u32 s0, s0, s1\nMvaa 116,232,136\nTa 176,14,32768\ns_and_b32 s1, s6, 8\nMvaa 116,236,140\nTa 178,14,34816\n"
"Mvaa 84,236,132\nD0 3,20,45,1024\ns_add_u32 s0, s0, s1\nMvaa 84,232,128\nTa 180,14,49152\nMvaa 88,240,128\nTa 182,14,51200\ns_lshl_b32 s0, s0, 10\nMvaa 88,244,132\n"
"s_add_u32 s0, s0, s3\nMvaa 120,244,140\nTa 184,15,32768\nSv 40,1,16,0,0\nMvaa 120,240,136\nTa 186,15,34816\nMvaa 152,240,144\ns_add_u32 s6, s6, s5\nMvaa 152,244,148\n"
"Ta 188,15,49152\nMvaa 184,244,156\nTa 190,15,51200\nMvaa 184,240,152\nD0 4,20,45,2048\nMvaa 188,248,152\nMvaa 188,252,156\nMvaa 156,252,148\nMvaa 156,248,144\nMvaa 124,248,136\n"
"s_lshr_b32 s0, s6, 4\ns_lshl_b32 s0, s0, 2\nMvaa 124,252,140\nD0 5,20,45,3072\nMvaa 92,252,132\ns_lshr_b32 s0, s7, s0\ns_and_b32 s0, s0, 7\nMvaa 92,248,128\n"
"s_waitcnt vmcnt(18) lgkmcnt(0)\ns_barrier\nMvaa 64,160,128\nRa 192,10,49152\ns_lshl_b32 s0, s0, 5\nMvaa 64,164,132\ns_add_u32 m0, s47, 32768\ns_add_u32 s44, s44, 0x40000\n"
"Mvaa 96,164,140\nRa 196,11,49152\ns_and_b32 s1, s6, 15\nMvaa 96,160,136\ns_add_u32 s0, s0, s1\nMvaa 128,160,144\nRa 200,10,51200\ns_and_b32 s1, s6, 8\nMvaa 128,164,148\n"
"D0 6,24,44,0\nMvaa 160,164,156\nRa 204,11,51200\ns_add_u32 s0, s0, s1\nMvaa 160,160,152\ns_lshl_b32 s0, s0, 10\nMvaa 164,168,152\nRa 208,10,57344\ns_add_u32 s0, s0, s3\n"
"Mvaa 164,172,156\nSv 44,1,16,0,0\nMvaa 132,172,148\nRa 212,11,57344\ns_add_u32 s6, s6, s5\nMvaa 132,168,144\nMvaa 100,168,136\nRa 216,10,59392\nMvaa 100,172,140\nD0 7,24,44,1024\n"
"Mvaa 68,172,132\nRa 220,11,59392\nMvaa 68,168,128\nMvaa 72,176,128\nMvaa 72,180,132\nMvaa 104,180,140\ns_mov_b32 s6, s77\ns_mov_b32 s5, 16\nMvaa 104,176,136\ns_lshr_b32 s0, s6, 4\n"
"Mvaa 136,176,144\ns_lshl_b32 s0, s0, 2\ns_lshr_b32 s0, s7, s0\nMvaa 136,180,148\nD0 8,24,44,2048\nMvaa 168,180,156\ns_and_b32 s0, s0, 7\ns_lshl_b32 s0, s0, 5\nMvaa 168,176,152\n"
"s_and_b32 s1, s6, 15\nMvaa 172,184,152\ns_add_u32 s0, s0, s1\ns_and_b32 s1, s6, 8\nMvaa 172,188,156\ns_add_u32 s0, s0, s1\nMvaa 140,188,148\ns_lshl_b32 s0, s0, 10\n"
"s_add_u32 s0, s0, s3\nMvaa 140,184,144\nSv 48,1,16,0,0\nMvaa 108,184,136\ns_add_u32 s6, s6, s5\nMvaa 108,188,140\nD0 9,24,44,3072\nMvaa 76,188,132\nMvaa 76,184,128\nWL 0\n"
"Maaa 0,160,192\nRa 128,10,0\nMaaa 0,164,196\ns_add_u32 m0, s47, 49152\ns_add_u32 s44, s44, 0x40000\nMaaa 32,164,204\nRa 132,11,0\nMaaa 32,160,200\nMaaa 64,160,208\nRa 136,10,2048\n"
"s_lshr_b32 s0, s6, 4\nMaaa 64,164,212\nD0 6,24,44,0\nMaaa 96,164,220\nRa 140,11,2048\ns_lshl_b32 s0, s0, 2\nMaaa 96,160,216\ns_lshr_b32 s0, s7, s0\nMaaa 100,168,216\n"
"Ra 144,10,8192\ns_and_b32 s0, s0, 7\nMaaa 100,172,220\ns_lshl_b32 s0, s0, 5\nMaaa 68,172,212\nRa 148,11,8192\ns_and_b32 s1, s6, 15\nMaaa 68,168,208\ns_add_u32 s0, s0, s1\n"
"Maaa 36,168,200\nRa 152,10,10240\ns_and_b32 s1, s6, 8\nMaaa 36,172,204\nD0 7,24,44,1024\nMaaa 4,172,196\nRa 156,11,10240\ns_add_u32 s0, s0, s1\nMaaa 4,168,192\n"
"s_lshl_b32 s0, s0, 10\nMaaa 8,176,192\ns_add_u32 s0, s0, s3\nSv 52,1,16,0,0\nMaaa 8,180,196\ns_add_u32 s6, s6, s5\nMaaa 40,180,204\nMaaa 40,176,200\nMaaa 72,176,208\n"
"Maaa 72,180,212\nD0 8,24,44,2048\nMaaa 104,180,220\nMaaa 104,176,216\nMaaa 108,184,216\nMaaa 108,188,220\ns_lshr_b32 s0, s6, 4\nMaaa 76,188,212\ns_lshl_b32 s0, s0, 2\n"
"s_lshr_b32 s0, s7, s0\nMaaa 76,184,208\ns_and_b32 s0, s0, 7\nMaaa 44,184,200\ns_lshl_b32 s0, s0, 5\ns_and_b32 s1, s6, 15\nMaaa 44,188,204\nD0 9,24,44,3072\nMaaa 12,188,196\n"
"s_add_u32 s0, s0, s1\ns_and_b32 s1, s6, 8\nMaaa 12,184,192\nWV 21\ns_barrier\nMaaa 16,224,192\nTa 160,12,0\ns_add_u32 s0, s0, s1\nMaaa 16,228,196\nTa 162,12,2048\nMaaa 48,228,204\n"
"s_add_u32 m0, s47, -16384\ns_add_u32 s48, s48, 128\nMaaa 48,224,200\nTa 164,12,16384\nMaaa 80,224,208\nTa 166,12,18432\ns_lshl_b32 s0, s0, 10\nMaaa 80,228,212\nD0 2,20,48,0\n"
"Maaa 112,228,220\nTa 168,13,0\ns_add_u32 s0, s0, s3\nMaaa 112,224,216\nTa 170,13,2048\nMaaa 116,232,216\nSv 56,1,16,0,0\ns_add_u32 s6, s6, s5\nMaaa 116,236,220\nTa 172,13,16384\n"
"Maaa 84,236,212\nTa 174,13,18432\nMaaa 84,232,208\nMaaa 52,232,200\nTa 176,14,0\nMaaa 52,236,204\nTa 178,14,2048\nMaaa 20,236,196\nD0 3,20,48,1024\nMaaa 20,232,192\n"
"Ta 180,14,16384\nMaaa 24,240,192\nTa 182,14,18432\ns_lshr_b32 s0, s6, 4\nMaaa 24,244,196\ns_lshl_b32 s0, s0, 2\nMaaa 56,244,204\nTa 184,15,0\ns_lshr_b32 s0, s7, s0\n"
"Maaa 56,240,200\nTa 186,15,2048\nMaaa 88,240,208\ns_and_b32 s0, s0, 7\ns_lshl_b32 s0, s0, 5\nMaaa 88,244,212\nTa 188,15,16384\nMaaa 120,244,220\nTa 190,15,18432\n"
"s_and_b32 s1, s6, 15\nMaaa 120,240,216\nD0 4,20,48,2048\nMaaa 124,248,216\ns_add_u32 s0, s0, s1\ns_and_b32 s1, s6, 8\nMaaa 124,252,220\ns_add_u32 s0, s0, s1\nMaaa 92,252,212\n"
"s_lshl_b32 s0, s0, 10\ns_add_u32 s0, s0, s3\nMaaa 92,248,208\nSv 60,1,16,0,0\nMaaa 60,248,200\ns_add_u32 s6, s6, s5\nMaaa 60,252,204\nD0 5,20,48,3072\nMaaa 28,252,196\n"
"Maaa 28,248,192\nWL 0\nMvaa 64,160,128\nTa 224,16,0\nMvaa 64,164,132\nTa 226,16,2048\ns_add_u32 m0, s47, 65536\ns_add_u32 s46, s51, 0\nMvaa 96,164,140\nv_lshrrev_b32 v48, 4, v0\n"
"Mvaa 96,160,136\nTa 228,16,16384\nD0 6,32,46,0\nMvaa 128,160,144\nTa 230,16,18432\nMvaa 128,164,148\nv_and_b32 v49, 7, v0\nMvaa 160,164,156\nTa 232,17,0\nMvaa 160,160,152\n"
"Ta 234,17,2048\nMvaa 164,168,152\nv_xor_b32 v49, v49, v48\nD0 7,32,46,1024\nMvaa 164,172,156\nTa 236,17,16384\nMvaa 132,172,148\nTa 238,17,18432\nMvaa 132,168,144\n"
"v_lshlrev_b32 v49, 4, v49\nMvaa 100,168,136\nTa 240,18,0\nMvaa 100,172,140\nTa 242,18,2048\nD0 8,32,46,2048\nMvaa 68,172,132\nv_lshrrev_b32 v48, 3, v0\nMvaa 68,168,128\n"
"Ta 244,18,16384\nMvaa 72,176,128\nTa 246,18,18432\nMvaa 72,180,132\nv_mul_u32_u24 v48, 896, v48\nMvaa 104,180,140\nTa 248,19,0\nD0 9,32,46,3072\nMvaa 104,176,136\nTa 250,19,2048\n"
"Mvaa 136,176,144\nv_add_u32 v40, v48, v49\nMvaa 136,180,148\nTa 252,19,16384\ns_add_u32 m0, s47, 81920\ns_add_u32 s46, s51, 262144\nMvaa 168,180,156\nTa 254,19,18432\n"
"Mvaa 168,176,152\nv_xor_b32 v49, 64, v40\nD0 6,32,46,0\nMvaa 172,184,152\nv_add_u32 v41, 6144, v49\nMvaa 172,188,156\nv_add_u32 v42, 55296, v40\nMvaa 140,188,148\n"
"v_add_u32 v43, 61440, v49\n.W574:\ns_cbranch_execz .W578\nMvaa 140,184,144\ns_mov_b32 s2, s13\nMvaa 108,184,136\ns_lshl3_add_u32 s2, s2, s11\nD0 7,32,46,1024\nMvaa 108,188,140\n"
"s_mul_i32 s52, s2, 14336\nMvaa 76,188,132\ns_lshr_b32 s2, s13, 2\ns_bfe_u32 s3, s13, 0x10001\nMvaa 76,184,128\ns_waitcnt vmcnt(23) lgkmcnt(0)\ns_barrier\nMvaa 80,224,128\n"
"Ra 192,10,16384\ns_mul_i32 s3, s3, 17\nMvaa 80,228,132\ns_add_u32 s2, s2, s3\nD0 8,32,46,2048\nMvaa 112,228,140\nRa 196,11,16384\ns_lshl2_add_u32 s3, s14, 0\nMvaa 112,224,136\n"
"s_lshl_b32 s3, s3, 2\nMvaa 144,224,144\nRa 200,10,18432\ns_lshr_b32 s3, 0xe420ca86, s3\nMvaa 144,228,148\ns_and_b32 s3, s3, 15\nMvaa 176,228,156\nRa 204,11,18432\n"
"s_add_u32 s2, s2, s3\nD0 9,32,46,3072\nMvaa 176,224,152\ns_cmp_eq_u32 s2, 32\ns_cselect_b32 s4, 16, s2\nMvaa 180,232,152\nRa 208,10,24576\ns_mul_i32 s75, s4, 65536\n"
"Mvaa 180,236,156\ns_lshr_b32 s2, s13, 2\ns_add_u32 m0, s47, -65536\ns_add_u32 s46, s52, 0\nMvaa 148,236,148\nRa 212,11,24576\ns_bfe_u32 s3, s13, 0x10001\nMvaa 148,232,144\n"
"s_mul_i32 s3, s3, 17\nD0 40,28,46,0\nMvaa 116,232,136\nRa 216,10,26624\ns_add_u32 s2, s2, s3\nMvaa 116,236,140\ns_lshl2_add_u32 s3, s14, 1\nMvaa 84,236,132\nRa 220,11,26624\n"
"s_lshl_b32 s3, s3, 2\nMvaa 84,232,128\ns_lshr_b32 s3, 0xe420ca86, s3\nMvaa 88,240,128\ns_and_b32 s3, s3, 15\ns_add_u32 s2, s2, s3\nD0 41,28,46,1024\nMvaa 88,244,132\n"
"s_cmp_eq_u32 s2, 32\ns_cselect_b32 s4, 16, s2\nMvaa 120,244,140\ns_mul_i32 s76, s4, 65536\ns_lshr_b32 s2, s13, 2\nMvaa 120,240,136\ns_bfe_u32 s3, s13, 0x10001\nMvaa 152,240,144\n"
"s_mul_i32 s3, s3, 17\ns_add_u32 s2, s2, s3\nMvaa 152,244,148\ns_lshl2_add_u32 s3, s14, 2\nD0 42,28,46,2048\nMvaa 184,244,156\ns_lshl_b32 s3, s3, 2\ns_lshr_b32 s3, 0xe420ca86, s3\n"
"Mvaa 184,240,152\ns_and_b32 s3, s3, 15\nMvaa 188,248,152\ns_add_u32 s2, s2, s3\nMvaa 188,252,156\ns_cmp_eq_u32 s2, 32\ns_cselect_b32 s4, 16, s2\nMvaa 156,252,148\n"
"s_mul_i32 s77, s4, 65536\ns_lshr_b32 s2, s13, 2\nD0 43,28,46,3072\nMvaa 156,248,144\ns_bfe_u32 s3, s13, 0x10001\nMvaa 124,248,136\ns_mul_i32 s3, s3, 17\ns_add_u32 s2, s2, s3\n"
"Mvaa 124,252,140\ns_lshl2_add_u32 s3, s14, 3\nMvaa 92,252,132\ns_lshl_b32 s3, s3, 2\ns_lshr_b32 s3, 0xe420ca86, s3\nMvaa 92,248,128\nWL 0\nMaaa 16,224,192\nRa 128,10,32768\n"
"s_and_b32 s3, s3, 15\ns_add_u32 m0, s47, 0\ns_add_u32 s46, s51, 524288\nD0 6,32,46,0\nMaaa 16,228,196\ns_add_u32 s2, s2, s3\ns_cmp_eq_u32 s2, 32\ns_cselect_b32 s4, 16, s2\n"
"Maaa 48,228,204\nRa 132,11,32768\ns_mul_i32 s78, s4, 65536\nMaaa 48,224,200\ns_lshr_b32 s3, s11, 1\nMaaa 80,224,208\nRa 136,10,34816\ns_lshl_b32 s3, s3, 15\nMaaa 80,228,212\n"
"s_add_u32 s2, s72, s3\nD0 7,32,46,1024\nMaaa 112,228,220\nRa 140,11,34816\ns_and_b32 s3, s11, 1\nMaaa 112,224,216\ns_lshl_b32 s3, s3, 12\nMaaa 116,232,216\nRa 144,10,40960\n"
"s_add_u32 s2, s2, s3\nMaaa 116,236,220\ns_add_u32 s2, s2, 0x0\nMaaa 84,236,212\nRa 148,11,40960\ns_mov_b32 s55, 16384\nD0 8,32,46,2048\nMaaa 84,232,208\ns_add_u32 s75, s75, s2\n"
"Maaa 52,232,200\nRa 152,10,43008\ns_add_u32 s76, s76, s2\nMaaa 52,236,204\ns_add_u32 s77, s77, s2\nMaaa 20,236,196\nRa 156,11,43008\ns_add_u32 s78, s78, s2\nMaaa 20,232,192\n"
"v_add_u32 v32, 0x10000, v12\nD0 9,32,46,3072\nMaaa 24,240,192\nv_add_u32 v33, 0x10000, v13\nv_add_u32 v34, 0x10000, v14\nMaaa 24,244,196\nv_add_u32 v35, 0x10000, v15\n"
"Maaa 56,244,204\nv_add_u32 v36, 0x10000, v16\nv_add_u32 v37, 0x10000, v17\ns_add_u32 m0, s47, 16384\ns_add_u32 s46, s51, 786432\nMaaa 56,240,200\nv_add_u32 v38, 0x10000, v18\n"
"Maaa 88,240,208\nv_add_u32 v39, 0x10000, v19\ns_add_u32 s2, s11, 1\nD0 6,32,46,0\nMaaa 88,244,212\ns_lshr_b32 s2, s2, 1\nMaaa 120,244,220\ns_lshl_b32 s2, s2, 12\n"
"v_add_u32 v44, s2, v10\nMaaa 120,240,216\nv_add_u32 v45, s2, v11\nMaaa 124,248,216\nv_xor_b32 v46, 0x1000, v44\nv_xor_b32 v47, 0x1000, v45\nMaaa 124,252,220\n"
"s_lshr_b32 s3, s11, 1\nD0 7,32,46,1024\nMaaa 92,252,212\ns_lshl_b32 s3, s3, 13\ns_lshl_b32 s2, s11, 12\nMaaa 92,248,208\ns_add_u32 s2, s2, s3\nMaaa 60,248,200\n"
"s_add_u32 s2, s2, s72\ns_lshl_b32 s3, s11, 12\nMaaa 60,252,204\ns_add_u32 s89, s3, 0x10000\nMaaa 28,252,196\ns_mov_b32 s88, 0xffff8000\ns_lshl_b32 s4, s13, 1\nD0 8,32,46,2048\n"
"Maaa 28,248,192\ns_waitcnt vmcnt(25) lgkmcnt(0)\ns_barrier\nMaaa 0,160,192\nTa 224,16,32768\ns_sub_u32 s79, 15, s4\nMaaa 0,164,196\nTa 226,16,34816\nMaaa 32,164,204\n"
"s_add_u32 s96, s2, 0x1310000\ns_mov_b32 s63, 0x53800000\nMaaa 32,160,200\nTa 228,16,49152\nD0 9,32,46,3072\nMaaa 64,160,208\nTa 230,16,51200\ns_mov_b32 s64, 0x2b800000\n"
"Maaa 64,164,212\ns_sub_u32 s98, s4, 1\nMaaa 96,164,220\nTa 232,17,32768\ns_lshl_b32 s5, s13, 5\ns_add_u32 m0, s47, -32768\ns_add_u32 s46, s52, 128\nMaaa 96,160,216\n"
"Ta 234,17,34816\nMaaa 100,168,216\ns_add_u32 s5, s5, s11\ns_lshl_b32 s65, s5, 9\nD0 40,28,46,0\nMaaa 100,172,220\nTa 236,17,49152\nMaaa 68,172,212\nTa 238,17,51200\n"
"s_add_u32 s65, s65, s72\nMaaa 68,168,208\ns_add_u32 s65, s65, 0x1580000\nMaaa 36,168,200\nTa 240,18,32768\ns_and_b32 s66, s72, 0x2000\nMaaa 36,172,204\nTa 242,18,34816\n"
"D0 41,28,46,1024\nMaaa 4,172,196\ns_add_u32 s66, s66, s56\ns_add_u32 s66, s66, 0x100000\nMaaa 4,168,192\nTa 244,18,49152\nMaaa 8,176,192\nTa 246,18,51200\ns_and_b32 s5, s12, 1\n"
"Maaa 8,180,196\ns_mul_i32 s58, s5, 0x202000\nMaaa 40,180,204\nTa 248,19,32768\ns_xor_b32 s58, s58, s72\nD0 42,28,46,2048\nMaaa 40,176,200\nTa 250,19,34816\nMaaa 72,176,208\n"
"s_add_u32 s58, s58, 0x15c0000\ns_lshl_b32 s5, s5, 8\nMaaa 72,180,212\nTa 252,19,49152\nMaaa 104,180,220\nTa 254,19,51200\ns_add_u32 s61, s58, s5\nMaaa 104,176,216\n"
"s_lshl4_add_u32 s60, s13, s61\nD0 43,28,46,3072\nMaaa 108,184,216\ns_add_u32 s54, s60, 128\ns_xor_b32 s60, s54, 0x100\nMaaa 108,188,220\ns_cmp_eq_u32 s11, 0\n"
"s_cselect_b32 s54, s54, 0x40000000\nMaaa 76,188,212\ns_getpc_b64 s[8:9]\n.PC283:\ns_add_u32 s8, s8, .HD269-.PC283\ns_addc_u32 s9, s9, 0\ns_add_u32 m0, s47, -49152\n"
"s_add_u32 s46, s52, 256\nMaaa 76,184,208\ns_mov_b32 s97, 0xff800000\nMaaa 44,184,200\ns_mov_b32 s90, 0\nD0 40,28,46,0\nMaaa 44,188,204\ns_cmp_lt_u32 s11, 2\n"
"s_cselect_b32 s92, 2, 0\nMaaa 12,188,196\ns_mul_i32 s4, s79, 65536\ns_sub_u32 s4, s2, s4\nMaaa 12,184,192\nWL 0\nMvaa 80,224,128\nTa 160,12,32768\ns_add_u32 s94, s4, 0x20000\n"
"Mvaa 80,228,132\nTa 162,12,34816\nD0 41,28,46,1024\nMvaa 84,236,132\ns_add_u32 s95, s4, 0x410000\ns_add_u32 s86, s2, 0x130000\nMvaa 84,232,128\nTa 164,12,49152\nMvaa 88,240,128\n"
"Ta 166,12,51200\ns_add_u32 s87, s2, 0x520000\nMvaa 88,244,132\ns_sub_u32 s99, s79, 2\ns_cmp_eq_u32 s13, 7\ns_cselect_b32 s93, 1.0, 0\nMvaa 92,252,132\nTa 168,13,32768\n"
"s_mov_b32 s36, s68\nD0 42,28,46,2048\nMvaa 92,248,128\nTa 170,13,34816\nMvaa 124,248,136\ns_and_b32 s37, s69, 0xffff\ns_mov_b32 s38, 0x1c0000\nMvaa 124,252,140\nTa 172,13,49152\n"
"CV 200,80,81\nMvaa 120,244,140\nTa 174,13,51200\ns_mov_b32 s39, 0x20000\nMvaa 120,240,136\ns_and_b32 s2, s74, 15\nD0 43,28,46,3072\nMvaa 116,232,136\nTa 176,14,32768\n"
"s_min_u32 s2, s2, 13\nMvaa 116,236,140\nTa 178,14,34816\nMvaa 112,228,140\ns_sub_u32 s2, s2, 0\ns_mul_i32 s3, s73, 14\nMvaa 112,224,136\nTa 180,14,49152\nCV 201,82,83\n"
"Mvaa 144,224,144\nTa 182,14,51200\ns_add_u32 s2, s2, s3\nMvaa 144,228,148\ns_lshl2_add_u32 s2, s2, s11\nMvaa 148,236,148\nTa 184,15,32768\ns_mul_i32 s3, s2, 4682\n"
"Mvaa 148,232,144\nTa 186,15,34816\nCV 202,84,85\nMvaa 152,240,144\ns_lshr_b32 s3, s3, 16\ns_mul_i32 s22, s3, 14\nMvaa 152,244,148\nTa 188,15,49152\nCV 203,86,87\nMvaa 156,252,148\n"
"Ta 190,15,51200\ns_sub_u32 s22, s2, s22\nMvaa 156,248,144\ns_and_b32 s21, s3, 28\nMvaa 188,248,152\ns_add_u32 s21, s21, s3\ns_lshl_b32 s21, s21, 4\nMvaa 188,252,156\n"
"s_bfe_u32 s41, s3, 0x30002\ns_cmp_gt_u32 s22, 6\ns_cselect_b32 s2, 1179648, 0\ns_cselect_b32 s1, 16, 0\nMvaa 184,244,156\ns_add_u32 s41, s41, s1\n"
"s_lshl4_add_u32 s41, s41, 0xc048480\nMvaa 184,240,152\ns_and_b32 s1, s3, 28\nCV 204,88,89\nMvaa 180,232,152\ns_add_u32 s3, s3, s1\ns_lshl_b32 s3, s3, 11\nMvaa 180,236,156\n"
"s_add_u32 s2, s2, s3\nMvaa 176,228,156\ns_lshl_b32 s3, s22, 17\ns_add_u32 s2, s2, s3\nMvaa 176,224,152\ns_waitcnt vmcnt(16) lgkmcnt(0)\ns_barrier\nMvaa 64,160,128\n"
"Ra 192,10,49152\ns_add_u32 s20, s2, 0x10e0000\ns_add_u32 m0, s47, 32768\ns_add_u32 s46, s51, 1048576\nMvaa 64,164,132\ns_lshl_b32 s2, s22, 7\nMvaa 68,172,132\nRa 196,11,49152\n"
"s_add_u32 s40, s2, 0xc04b800\nD0 6,32,46,0\nMvaa 68,168,128\ns_mul_i32 s2, s21, 896\nCV 205,90,91\nMvaa 72,176,128\nRa 200,10,51200\ns_lshl_b32 s3, s22, 6\nMvaa 72,180,132\n"
"s_add_u32 s22, s2, s3\nCV 206,92,93\nMvaa 76,188,132\nRa 204,11,51200\ns_add_u32 s23, s22, 57344\nMvaa 76,184,128\nD0 7,32,46,1024\nMvaa 108,184,136\nRa 208,10,57344\n"
"CV 207,94,95\nMvaa 108,188,140\nCV 220,120,121\nCV 221,122,123\nMvaa 104,180,140\nRa 212,11,57344\nMvaa 104,176,136\nCV 222,124,125\nCV 223,126,127\nMvaa 100,168,136\n"
"Ra 216,10,59392\nD0 8,32,46,2048\ns_lshl3_add_u32 s5, s12, s13\ns_lshl2_add_u32 s5, s5, s11\ns_lshl2_add_u32 s5, s5, 0xc04a400\nbuffer_load_dword v27, off, s[16:19], s5 sc0 sc1\n"
"Mvaa 100,172,140\nCV 216,112,113\nMvaa 96,164,140\nRa 220,11,59392\nCV 217,114,115\nMvaa 96,160,136\nCV 218,116,117\nCV 219,118,119\nMvaa 128,160,144\nCV 192,64,65\nCV 193,66,67\n"
"Mvaa 128,164,148\nD0 9,32,46,3072\nMvaa 132,172,148\nCV 194,68,69\nCV 195,70,71\nMvaa 132,168,144\nCV 196,72,73\nCV 197,74,75\nMvaa 136,176,144\ns_add_u32 m0, s47, 49152\n"
"s_add_u32 s46, s51, 1310720\nMvaa 136,180,148\nCV 198,76,77\nCV 199,78,79\nMvaa 140,188,148\nD0 6,32,46,0\nCV 212,104,105\nMvaa 140,184,144\nCV 213,106,107\nMvaa 172,184,152\n"
"CV 214,108,109\nCV 215,110,111\nMvaa 172,188,156\nCV 208,96,97\nCV 209,98,99\nMvaa 168,180,156\nCV 210,100,101\nMvaa 168,176,152\nD0 7,32,46,1024\nCV 211,102,103\n"
"Mvaa 164,168,152\nCV 232,144,145\nMvaa 164,172,156\nCV 233,146,147\nSv 192,1,16,75,0\nMvaa 160,164,156\nCV 234,148,149\nMvaa 160,160,152\nWL 0\nMaaa 0,160,192\nD0 8,32,46,2048\n"
"Ra 128,44,0\nMaaa 0,164,196\nCV 235,150,151\nMaaa 4,172,196\nRa 132,45,0\nMaaa 4,168,192\nCV 236,152,153\nMaaa 8,176,192\nRa 136,44,2048\nMaaa 8,180,196\nD0 9,32,46,3072\n"
"Maaa 12,188,196\nRa 140,45,2048\nSv 196,1,16,75,1024\nMaaa 12,184,192\nCV 237,154,155\nMaaa 44,184,200\nRa 144,46,0\nMaaa 44,188,204\nCV 238,156,157\nMaaa 40,180,204\nRa 148,47,0\n"
"Maaa 40,176,200\nCV 239,158,159\nMaaa 36,168,200\nRa 152,46,2048\nMaaa 36,172,204\nCV 252,184,185\nMaaa 32,164,204\nRa 156,47,2048\nMaaa 32,160,200\nCV 253,186,187\n"
"Sv 200,1,16,75,2048\nMaaa 64,160,208\nCV 254,188,189\nCV 255,190,191\nMaaa 64,164,212\nCV 248,176,177\nMaaa 68,172,212\nCV 249,178,179\nMaaa 68,168,208\nCV 250,180,181\n"
"Maaa 72,176,208\nCV 251,182,183\nMaaa 72,180,212\nCV 224,128,129\nMaaa 76,188,212\nCV 225,130,131\nMaaa 76,184,208\nCV 226,132,133\nMaaa 108,184,216\nCV 227,134,135\n"
"Sv 204,1,16,75,3072\nMaaa 108,188,220\nCV 228,136,137\nMaaa 104,180,220\nCV 229,138,139\nMaaa 104,176,216\nCV 230,140,141\nMaaa 100,168,216\nCV 231,142,143\nMaaa 100,172,220\n"
"CV 244,168,169\nMaaa 96,164,220\nCV 245,170,171\nMaaa 96,160,216\ns_barrier\nMaaa 16,224,192\ns_add_u32 m0, s47, -16384\ns_add_u32 s46, s52, 384\nTa 160,32,0\nMaaa 16,228,196\n"
"Ta 162,32,2048\nMaaa 20,236,196\nD0 40,28,46,0\ns_add_u32 s75, s75, s55\nSv 208,1,16,75,0\nMaaa 20,232,192\nTa 164,32,16384\nMaaa 24,240,192\nTa 166,32,18432\nMaaa 24,244,196\n"
"CV 246,172,173\nMaaa 28,252,196\nTa 168,33,0\nMaaa 28,248,192\nD0 41,28,46,1024\nTa 170,33,2048\nMaaa 60,248,200\nCV 247,174,175\nMaaa 60,252,204\nTa 172,33,16384\n"
"Maaa 56,244,204\nTa 174,33,18432\nMaaa 56,240,200\nCV 240,160,161\n.W575:\ns_cbranch_execz .W579\nSv 212,1,16,75,1024\nMaaa 52,232,200\nD0 42,28,46,2048\nTa 176,34,0\n"
"Maaa 52,236,204\nTa 178,34,2048\nMaaa 48,228,204\nCV 241,162,163\nMaaa 48,224,200\nTa 180,34,16384\nMaaa 80,224,208\nTa 182,34,18432\nCV 242,164,165\nMaaa 80,228,212\n"
"D0 43,28,46,3072\nMaaa 84,236,212\nTa 184,35,0\nMaaa 84,232,208\nTa 186,35,2048\nMaaa 88,240,208\nCV 243,166,167\nSv 216,1,16,75,2048\nMaaa 88,244,212\nTa 188,35,16384\n"
"Maaa 92,252,212\nTa 190,35,18432\nMaaa 92,248,208\nMaaa 124,248,216\nMaaa 124,252,220\nMaaa 120,244,220\nMaaa 120,240,216\nMaaa 116,232,216\nMaaa 116,236,220\nSv 220,1,16,75,3072\n"
"Maaa 112,228,220\nMaaa 112,224,216\nWL 0\nZvaa 64,160,128\nTa 224,36,0\nAR 60,0\nMvaa 64,164,132\nTa 226,36,2048\nZvaa 96,164,140\nAR 61,1\nMvaa 96,160,136\nTa 228,36,16384\n"
"CV 192,60,61\nZvaa 128,160,144\nTa 230,36,18432\nMvaa 128,164,148\nAR 60,2\nAR 61,3\nZvaa 160,164,156\nTa 232,37,0\nSv 224,1,16,76,0\nMvaa 160,160,152\nTa 234,37,2048\n"
"CV 193,60,61\nZvaa 164,168,152\nAR 60,4\nMvaa 164,172,156\nTa 236,37,16384\nZvaa 132,172,148\nTa 238,37,18432\nAR 61,5\nMvaa 132,168,144\nCV 194,60,61\nZvaa 100,168,136\n"
"Ta 240,38,0\nAR 60,6\nMvaa 100,172,140\nTa 242,38,2048\nZvaa 68,172,132\nSv 228,1,16,76,1024\nAR 61,7\nMvaa 68,168,128\nTa 244,38,16384\nZvaa 72,176,128\nTa 246,38,18432\n"
"CV 195,60,61\nMvaa 72,180,132\nAR 60,8\nZvaa 104,180,140\nTa 248,39,0\nMvaa 104,176,136\nTa 250,39,2048\nAR 61,9\nZvaa 136,176,144\nCV 196,60,61\nMvaa 136,180,148\n"
"Ta 252,39,16384\nAR 60,10\nZvaa 168,180,156\nTa 254,39,18432\nSv 232,1,16,76,2048\nMvaa 168,176,152\nAR 61,11\nCV 197,60,61\nZvaa 172,184,152\nAR 60,12\nMvaa 172,188,156\n"
"AR 61,13\nZvaa 140,188,148\nCV 198,60,61\nAR 60,14\nMvaa 140,184,144\nAR 61,15\nZvaa 108,184,136\nCV 199,60,61\nAR 60,16\nMvaa 108,188,140\nSv 236,1,16,76,3072\nZvaa 76,188,132\n"
"AR 61,17\nCV 200,60,61\nMvaa 76,184,128\ns_waitcnt vmcnt(29) lgkmcnt(0)\ns_barrier\nZvaa 80,224,128\nRa 192,44,32768\nAR 60,18\nMvaa 80,228,132\nRa 196,45,32768\n"
"s_add_u32 m0, s47, 65536\ns_add_u32 s46, s51, 1572864\nD0 6,32,46,0\nZvaa 112,228,140\nRa 200,44,34816\nMvaa 112,224,136\nRa 204,45,34816\nAR 61,19\nZvaa 144,224,144\n"
"Ra 208,46,32768\nMvaa 144,228,148\nRa 212,47,32768\nCV 201,60,61\nD0 7,32,46,1024\nZvaa 176,228,156\nRa 216,46,34816\nMvaa 176,224,152\nRa 220,47,34816\nAR 60,20\n"
"Zvaa 180,232,152\nTa 160,12,0\nMvaa 180,236,156\nTa 162,12,2048\nD0 8,32,46,2048\nZvaa 148,236,148\nTa 164,12,16384\nAR 61,21\nMvaa 148,232,144\nTa 166,12,18432\nZvaa 116,232,136\n"
"Ta 168,13,0\nCV 202,60,61\nMvaa 116,236,140\nTa 170,13,2048\nD0 9,32,46,3072\nZvaa 84,236,132\nTa 172,13,16384\nAR 60,22\nMvaa 84,232,128\nTa 174,13,18432\n"
"s_add_u32 m0, s47, 81920\ns_add_u32 s46, s51, 1835008\nZvaa 88,240,128\nTa 176,14,0\nAR 61,23\nMvaa 88,244,132\nTa 178,14,2048\nD0 6,32,46,0\nZvaa 120,244,140\nTa 180,14,16384\n"
"Mvaa 120,240,136\nTa 182,14,18432\nCV 203,60,61\nZvaa 152,240,144\nTa 184,15,0\nMvaa 152,244,148\nTa 186,15,2048\nAR 60,24\nD0 7,32,46,1024\nZvaa 184,244,156\nTa 188,15,16384\n"
"Mvaa 184,240,152\nTa 190,15,18432\nAR 61,25\nZvaa 188,248,152\nCV 204,60,61\nMvaa 188,252,156\nAR 60,26\nD0 8,32,46,2048\nZvaa 156,252,148\nAR 61,27\nCV 205,60,61\n"
"Mvaa 156,248,144\nAR 60,28\nZvaa 124,248,136\nAR 61,29\nCV 206,60,61\nMvaa 124,252,140\nAR 60,30\nD0 9,32,46,3072\nZvaa 92,252,132\nAR 61,31\nCV 207,60,61\nMvaa 92,248,128\nWL 0\n"
"Mvaa 64,160,192\nTa 224,16,0\nAR 60,32\nMvaa 64,164,196\nTa 226,16,2048\ns_add_u32 m0, s47, -65536\ns_add_u32 s46, s52, 512\nD0 40,28,46,0\nMvaa 96,164,204\nAR 61,33\n"
"Mvaa 96,160,200\nTa 228,16,16384\nCV 208,60,61\nMvaa 128,160,208\nTa 230,16,18432\nMvaa 128,164,212\nAR 60,34\nAR 61,35\nD0 41,28,46,1024\nMvaa 160,164,220\nTa 232,17,0\n"
"Mvaa 160,160,216\nTa 234,17,2048\nCV 209,60,61\nMvaa 164,168,216\nAR 60,36\nMvaa 164,172,220\nTa 236,17,16384\nD0 42,28,46,2048\nMvaa 132,172,212\nTa 238,17,18432\nAR 61,37\n"
"Mvaa 132,168,208\nCV 210,60,61\nMvaa 100,168,200\nTa 240,18,0\nAR 60,38\nMvaa 100,172,204\nTa 242,18,2048\nD0 43,28,46,3072\nMvaa 68,172,196\ns_add_u32 s76, s76, s55\n"
"Sv 240,1,16,76,0\nMvaa 68,168,192\nTa 244,18,16384\nMvaa 72,176,192\nTa 246,18,18432\nAR 61,39\nMvaa 72,180,196\nCV 211,60,61\nMvaa 104,180,204\nTa 248,19,0\nMvaa 104,176,200\n"
"Ta 250,19,2048\nSv 244,1,16,76,1024\nMvaa 136,176,208\nAR 60,40\nMvaa 136,180,212\nTa 252,19,16384\nAR 61,41\nMvaa 168,180,220\nTa 254,19,18432\nMvaa 168,176,216\nCV 212,60,61\n"
"AR 60,42\nMvaa 172,184,216\nSv 248,1,16,76,2048\nMvaa 172,188,220\nAR 61,43\nMvaa 140,188,212\nCV 213,60,61\nAR 60,44\nMvaa 140,184,208\nAR 61,45\nMvaa 108,184,200\nCV 214,60,61\n"
"AR 60,46\nMvaa 108,188,204\nSv 252,1,16,76,3072\nMvaa 76,188,196\nAR 61,47\nCV 215,60,61\nMvaa 76,184,192\ns_waitcnt vmcnt(20) lgkmcnt(0)\ns_barrier\nMvaa 80,224,192\n"
"Ra 128,44,16384\nAR 60,48\nv_cmp_ne_u32 vcc, s15, v27\ns_nop 0\ns_cbranch_vccnz .S284\n.S285:\nMvaa 80,228,196\nRa 132,45,16384\n"
"buffer_store_dword v25, off, s[16:19], s54 offset:0 sc0 sc1\ns_add_u32 m0, s47, 0\ns_add_u32 s46, s51, 2097152\nD0 6,32,46,0\nMvaa 112,228,204\nRa 136,44,18432\nMvaa 112,224,200\n"
"Ra 140,45,18432\nAR 61,49\nMvaa 144,224,208\nRa 144,46,16384\nMvaa 144,228,212\nRa 148,47,16384\nCV 216,60,61\nD0 7,32,46,1024\nMvaa 176,228,220\nRa 152,46,18432\n"
"Mvaa 176,224,216\nRa 156,47,18432\nAR 60,50\nMvaa 180,232,216\nTa 160,12,32768\nMvaa 180,236,220\nTa 162,12,34816\nD0 8,32,46,2048\nMvaa 148,236,212\nTa 164,12,49152\nAR 61,51\n"
"Mvaa 148,232,208\nTa 166,12,51200\nMvaa 116,232,200\nTa 168,13,32768\nCV 217,60,61\nMvaa 116,236,204\nTa 170,13,34816\nD0 9,32,46,3072\nMvaa 84,236,196\nTa 172,13,49152\nAR 60,52\n"
"Mvaa 84,232,192\nTa 174,13,51200\ns_add_u32 m0, s47, 16384\ns_add_u32 s46, s51, 2359296\nMvaa 88,240,192\nTa 176,14,32768\nAR 61,53\nMvaa 88,244,196\nTa 178,14,34816\n"
"D0 6,32,46,0\nMvaa 120,244,204\nTa 180,14,49152\nMvaa 120,240,200\nTa 182,14,51200\nCV 218,60,61\nMvaa 152,240,208\nTa 184,15,32768\nMvaa 152,244,212\nTa 186,15,34816\nAR 60,54\n"
"D0 7,32,46,1024\nMvaa 184,244,220\nTa 188,15,49152\nMvaa 184,240,216\nTa 190,15,51200\nAR 61,55\nMvaa 188,248,216\nCV 219,60,61\nMvaa 188,252,220\nAR 60,56\nD0 8,32,46,2048\n"
"Mvaa 156,252,212\nAR 61,57\nCV 220,60,61\nMvaa 156,248,208\nAR 60,58\nMvaa 124,248,200\nAR 61,59\nCV 221,60,61\nMvaa 124,252,204\nAR 60,60\nD0 9,32,46,3072\nMvaa 92,252,196\n"
"AR 61,61\nCV 222,60,61\nMvaa 92,248,192\nWL 0\nMvaa 64,160,128\nTa 224,16,32768\nAR 60,62\nMvaa 64,164,132\nTa 226,16,34816\ns_add_u32 m0, s47, -32768\ns_add_u32 s46, s52, 640\n"
"D0 40,28,46,0\nMvaa 96,164,140\nAR 61,63\nMvaa 96,160,136\nTa 228,16,49152\nCV 223,60,61\nMvaa 128,160,144\nTa 230,16,51200\nMvaa 128,164,148\nD0 41,28,46,1024\nMvaa 160,164,156\n"
"Ta 232,17,32768\nAR 60,126\nMvaa 160,160,152\nTa 234,17,34816\nMvaa 164,168,152\nAR 61,127\nCV 60,60,61\nMvaa 164,172,156\nTa 236,17,49152\nD0 42,28,46,2048\nMvaa 132,172,148\n"
"Ta 238,17,51200\nMvaa 132,168,144\nAW 127,60\nAR 60,124\nMvaa 100,168,136\nTa 240,18,32768\nMvaa 100,172,140\nTa 242,18,34816\nD0 43,28,46,3072\nMvaa 68,172,132\nSv 192,1,16,77,0\n"
"AR 61,125\nMvaa 68,168,128\nTa 244,18,49152\nMvaa 72,176,128\nTa 246,18,51200\nCV 60,60,61\nMvaa 72,180,132\nAW 126,60\nMvaa 104,180,140\nTa 248,19,32768\nMvaa 104,176,136\n"
"Ta 250,19,34816\nSv 196,1,16,77,1024\nMvaa 136,176,144\nAR 60,122\nMvaa 136,180,148\nTa 252,19,49152\nMvaa 168,180,156\nTa 254,19,51200\nAR 61,123\nMvaa 168,176,152\nCV 60,60,61\n"
"Mvaa 172,184,152\nSv 200,1,16,77,2048\nAW 125,60\nMvaa 172,188,156\nAR 60,120\nMvaa 140,188,148\nAR 61,121\nMvaa 140,184,144\nCV 60,60,61\nAW 124,60\nMvaa 108,184,136\nAR 60,118\n"
"Mvaa 108,188,140\nSv 204,1,16,77,3072\nMvaa 76,188,132\nAR 61,119\nCV 60,60,61\nMvaa 76,184,128\ns_waitcnt vmcnt(17) lgkmcnt(0)\ns_barrier\nMvaa 80,224,128\nRa 192,44,49152\n"
"AW 123,60\nMvaa 80,228,132\nRa 196,45,49152\nbuffer_store_dword v25, off, s[16:19], s54 offset:8 sc0 sc1\ns_add_u32 m0, s47, 32768\ns_add_u32 s46, s51, 2621440\nD0 6,32,46,0\n"
"Mvaa 112,228,140\nRa 200,44,51200\nMvaa 112,224,136\nRa 204,45,51200\nAR 60,116\nMvaa 144,224,144\nRa 208,46,49152\nMvaa 144,228,148\nRa 212,47,49152\nD0 7,32,46,1024\n"
"Mvaa 176,228,156\nRa 216,46,51200\nAR 61,117\nMvaa 176,224,152\nRa 220,47,51200\nMvaa 180,232,152\nTa 160,32,0\nCV 60,60,61\nMvaa 180,236,156\nTa 162,32,2048\nD0 8,32,46,2048\n"
"Mvaa 148,236,148\nTa 164,32,16384\nMvaa 148,232,144\nTa 166,32,18432\nAW 122,60\nMvaa 116,232,136\nTa 168,33,0\nMvaa 116,236,140\nTa 170,33,2048\nD0 9,32,46,3072\nMvaa 84,236,132\n"
"Ta 172,33,16384\nAR 60,114\nMvaa 84,232,128\nTa 174,33,18432\ns_add_u32 m0, s47, 49152\ns_add_u32 s46, s51, 2883584\nMvaa 88,240,128\nTa 176,34,0\n.W576:\ns_cbranch_execz .W580\n"
"AR 61,115\nMvaa 88,244,132\nTa 178,34,2048\nD0 6,32,46,0\nMvaa 120,244,140\nTa 180,34,16384\nMvaa 120,240,136\nTa 182,34,18432\nCV 60,60,61\nMvaa 152,240,144\nTa 184,35,0\n"
"Mvaa 152,244,148\nTa 186,35,2048\nD0 7,32,46,1024\nMvaa 184,244,156\nTa 188,35,16384\nAW 121,60\nMvaa 184,240,152\nTa 190,35,18432\nMvaa 188,248,152\nAR 60,112\nAR 61,113\n"
"Mvaa 188,252,156\nD0 8,32,46,2048\nMvaa 156,252,148\nCV 60,60,61\nMvaa 156,248,144\nAW 120,60\nAR 60,110\nMvaa 124,248,136\nAR 61,111\nMvaa 124,252,140\nD0 9,32,46,3072\n"
"Mvaa 92,252,132\nCV 60,60,61\nAW 119,60\nMvaa 92,248,128\nWL 0\nMvaa 64,160,192\nTa 224,36,0\nAR 60,108\nMvaa 64,164,196\nTa 226,36,2048\ns_add_u32 m0, s47, -49152\n"
"s_add_u32 s46, s52, 768\nD0 40,28,46,0\nMvaa 96,164,204\nAR 61,109\nMvaa 96,160,200\nTa 228,36,16384\nCV 60,60,61\nMvaa 128,160,208\nTa 230,36,18432\nMvaa 128,164,212\n"
"D0 41,28,46,1024\nMvaa 160,164,220\nTa 232,37,0\nAW 118,60\nMvaa 160,160,216\nTa 234,37,2048\nMvaa 164,168,216\nAR 60,106\nAR 61,107\nMvaa 164,172,220\nTa 236,37,16384\n"
"D0 42,28,46,2048\nMvaa 132,172,212\nTa 238,37,18432\nMvaa 132,168,208\nCV 60,60,61\nAW 117,60\nMvaa 100,168,200\nTa 240,38,0\nMvaa 100,172,204\nTa 242,38,2048\nD0 43,28,46,3072\n"
"Mvaa 68,172,196\ns_add_u32 s77, s77, s55\nSv 208,1,16,77,0\nMvaa 68,168,192\nTa 244,38,16384\nMvaa 72,176,192\nTa 246,38,18432\nAR 60,104\nMvaa 72,180,196\nAR 61,105\n"
"Mvaa 104,180,204\nTa 248,39,0\nMvaa 104,176,200\nTa 250,39,2048\nSv 212,1,16,77,1024\nMvaa 136,176,208\nCV 60,60,61\nMvaa 136,180,212\nTa 252,39,16384\nMvaa 168,180,220\n"
"Ta 254,39,18432\nAW 116,60\nMvaa 168,176,216\nAR 60,102\nMvaa 172,184,216\nSv 216,1,16,77,2048\nAR 61,103\nMvaa 172,188,220\nCV 60,60,61\nMvaa 140,188,212\nAW 115,60\n"
"Mvaa 140,184,208\nAR 60,100\nAR 61,101\nMvaa 108,184,200\nCV 60,60,61\nMvaa 108,188,204\nSv 220,1,16,77,3072\nMvaa 76,188,196\nAW 114,60\nAR 60,98\nMvaa 76,184,192\n"
"s_waitcnt vmcnt(25) lgkmcnt(0)\ns_barrier\nMvaa 80,224,192\nRa 128,44,0\nv_and_b32 v27, 0x70, v1\nbuffer_load_dword v27, v27, s[16:19], s61 offen offset:128 sc0 sc1\n"
"Mvaa 80,228,196\nRa 132,45,0\ns_add_u32 m0, s47, 65536\ns_add_u32 s46, s51, 3145728\nD0 6,32,46,0\nMvaa 112,228,204\nRa 136,44,2048\nMvaa 112,224,200\nRa 140,45,2048\nAR 61,99\n"
"Mvaa 144,224,208\nRa 144,46,0\nMvaa 144,228,212\nRa 148,47,0\nD0 7,32,46,1024\nMvaa 176,228,220\nRa 152,46,2048\nCV 60,60,61\nMvaa 176,224,216\nRa 156,47,2048\nMvaa 180,232,216\n"
"Ta 160,12,0\nAW 113,60\nMvaa 180,236,220\nTa 162,12,2048\nD0 8,32,46,2048\nMvaa 148,236,212\nTa 164,12,16384\nMvaa 148,232,208\nTa 166,12,18432\nAR 60,96\nMvaa 116,232,200\n"
"Ta 168,13,0\nMvaa 116,236,204\nTa 170,13,2048\nD0 9,32,46,3072\nMvaa 84,236,196\nTa 172,13,16384\nAR 61,97\nMvaa 84,232,192\nTa 174,13,18432\ns_add_u32 m0, s47, 81920\n"
"s_add_u32 s46, s51, 3407872\nMvaa 88,240,192\nTa 176,14,0\nCV 60,60,61\nMvaa 88,244,196\nTa 178,14,2048\nD0 6,32,46,0\nMvaa 120,244,204\nTa 180,14,16384\nMvaa 120,240,200\n"
"Ta 182,14,18432\nAW 112,60\nMvaa 152,240,208\nTa 184,15,0\nMvaa 152,244,212\nTa 186,15,2048\nD0 7,32,46,1024\nMvaa 184,244,220\nTa 188,15,16384\nAR 60,94\nMvaa 184,240,216\n"
"Ta 190,15,18432\nMvaa 188,248,216\nAR 61,95\nCV 60,60,61\nMvaa 188,252,220\nD0 8,32,46,2048\nMvaa 156,252,212\nAW 111,60\nMvaa 156,248,208\nAR 60,92\nAR 61,93\nMvaa 124,248,200\n"
"CV 60,60,61\nMvaa 124,252,204\nD0 9,32,46,3072\nMvaa 92,252,196\nAW 110,60\nAR 60,90\nMvaa 92,248,192\nWL 0\nMvaa 64,160,128\nTa 224,16,0\nAR 61,91\nMvaa 64,164,132\n"
"Ta 226,16,2048\ns_add_u32 s6, s56, 16384\nbuffer_load_dwordx4 v[224:227], v1, s[16:19], s6 offen offset:0 sc1\nMvaa 96,164,140\nCV 60,60,61\nMvaa 96,160,136\nTa 228,16,16384\n"
"AW 109,60\nMvaa 128,160,144\nTa 230,16,18432\nMvaa 128,164,148\nbuffer_load_dwordx4 v[228:231], v1, s[16:19], s6 offen offset:1024 sc1\nMvaa 160,164,156\nTa 232,17,0\nAR 60,88\n"
"Mvaa 160,160,152\nTa 234,17,2048\nMvaa 164,168,152\nAR 61,89\nCV 60,60,61\nMvaa 164,172,156\nTa 236,17,16384\n"
"buffer_load_dwordx4 v[232:235], v1, s[16:19], s6 offen offset:2048 sc1\nMvaa 132,172,148\nTa 238,17,18432\nMvaa 132,168,144\nAW 108,60\nAR 60,86\nMvaa 100,168,136\nTa 240,18,0\n"
"Mvaa 100,172,140\nTa 242,18,2048\nbuffer_load_dwordx4 v[236:239], v1, s[16:19], s6 offen offset:3072 sc1\nMvaa 68,172,132\nAR 61,87\nCV 60,60,61\nMvaa 68,168,128\nTa 244,18,16384\n"
"Mvaa 72,176,128\nTa 246,18,18432\nAW 107,60\nMvaa 72,180,132\nAR 60,84\nMvaa 104,180,140\nTa 248,19,0\nMvaa 104,176,136\nTa 250,19,2048\nAR 61,85\nMvaa 136,176,144\nCV 60,60,61\n"
"Mvaa 136,180,148\nTa 252,19,16384\nMvaa 168,180,156\nTa 254,19,18432\nAW 106,60\nMvaa 168,176,152\nAR 60,82\nMvaa 172,184,152\nAR 61,83\nCV 60,60,61\nMvaa 172,188,156\nAW 105,60\n"
"Mvaa 140,188,148\nAR 60,80\nMvaa 140,184,144\nAR 61,81\nCV 60,60,61\nMvaa 108,184,136\nAW 104,60\nMvaa 108,188,140\nAR 60,78\nMvaa 76,188,132\nAR 61,79\nCV 60,60,61\n"
"Mvaa 76,184,128\ns_waitcnt vmcnt(12) lgkmcnt(0)\ns_barrier\nMvaa 80,224,128\nRa 192,44,32768\nv_cmp_ne_u32 vcc, s15, v27\ns_nop 0\ns_cbranch_vccnz .S287\n.S288:\n"
"s_sub_u32 s45, s86, 131072\ns_cmp_eq_u32 s79, 1\ns_cselect_b32 s86, s94, s86\ns_add_u32 m0, s47, -65536\ns_add_u32 s46, s45, 0x0\nMvaa 80,228,132\nRa 196,45,32768\n"
"Mvaa 112,228,140\nRa 200,44,34816\nD1 1,16,46,0\nMvaa 112,224,136\nRa 204,45,34816\nAW 103,60\nMvaa 144,224,144\nRa 208,46,32768\nMvaa 144,228,148\nRa 212,47,32768\n"
"Mvaa 176,228,156\nRa 216,46,34816\nD1 1,16,46,1024\nMvaa 176,224,152\nRa 220,47,34816\nMvaa 180,232,152\nTa 160,12,32768\nAR 60,76\nMvaa 180,236,156\nTa 162,12,34816\n"
"Mvaa 148,236,148\nTa 164,12,49152\nD1 1,16,46,2048\nMvaa 148,232,144\nTa 166,12,51200\nAR 61,77\nMvaa 116,232,136\nTa 168,13,32768\nMvaa 116,236,140\nTa 170,13,34816\n"
"Mvaa 84,236,132\nTa 172,13,49152\nD1 1,16,46,3072\nMvaa 84,232,128\nTa 174,13,51200\nMvaa 88,240,128\nTa 176,14,32768\ns_add_u32 s6, s56, 20480\nMvaa 88,244,132\nTa 178,14,34816\n"
"Mvaa 120,244,140\nTa 180,14,49152\nbuffer_load_dwordx4 v[240:243], v1, s[16:19], s6 offen offset:0 sc1\nMvaa 120,240,136\nTa 182,14,51200\nCV 60,60,61\nMvaa 152,240,144\n"
"Ta 184,15,32768\nMvaa 152,244,148\nTa 186,15,34816\nMvaa 184,244,156\nTa 188,15,49152\nbuffer_load_dwordx4 v[244:247], v1, s[16:19], s6 offen offset:1024 sc1\nMvaa 184,240,152\n"
"Ta 190,15,51200\nMvaa 188,248,152\nAW 102,60\nAR 60,74\nMvaa 188,252,156\nAR 61,75\nMvaa 156,252,148\nbuffer_load_dwordx4 v[248:251], v1, s[16:19], s6 offen offset:2048 sc1\n"
"Mvaa 156,248,144\nCV 60,60,61\nAW 101,60\nMvaa 124,248,136\nAR 60,72\nMvaa 124,252,140\nAR 61,73\nMvaa 92,252,132\n"
"buffer_load_dwordx4 v[252:255], v1, s[16:19], s6 offen offset:3072 sc1\nCV 60,60,61\nMvaa 92,248,128\nWL 0\nMvaa 64,160,192\nTa 224,16,32768\ns_add_u32 s6, s56, 0\n"
"Mvaa 64,164,196\nTa 226,16,34816\nMvaa 96,164,204\nbuffer_load_dwordx4 v[192:195], v1, s[16:19], s6 offen offset:0 sc1\nMvaa 96,160,200\nTa 228,16,49152\nAW 100,60\n"
"Mvaa 128,160,208\nTa 230,16,51200\nMvaa 128,164,212\nAR 60,70\nMvaa 160,164,220\nTa 232,17,32768\nbuffer_load_dwordx4 v[196:199], v1, s[16:19], s6 offen offset:1024 sc1\n"
"Mvaa 160,160,216\nTa 234,17,34816\nMvaa 164,168,216\nAR 61,71\nCV 60,60,61\nMvaa 164,172,220\nTa 236,17,49152\nMvaa 132,172,212\nTa 238,17,51200\n"
"buffer_load_dwordx4 v[200:203], v1, s[16:19], s6 offen offset:2048 sc1\nMvaa 132,168,208\nAW 99,60\nAR 60,68\nMvaa 100,168,200\nTa 240,18,32768\nMvaa 100,172,204\nTa 242,18,34816\n"
"Mvaa 68,172,196\nbuffer_load_dwordx4 v[204:207], v1, s[16:19], s6 offen offset:3072 sc1\nAR 61,69\nMvaa 68,168,192\nTa 244,18,49152\nMvaa 72,176,192\nTa 246,18,51200\nCV 60,60,61\n"
"Mvaa 72,180,196\nAW 98,60\nMvaa 104,180,204\nTa 248,19,32768\nMvaa 104,176,200\nTa 250,19,34816\nAR 60,66\nMvaa 136,176,208\nAR 61,67\nMvaa 136,180,212\nTa 252,19,49152\n"
"Mvaa 168,180,220\nTa 254,19,51200\nCV 60,60,61\nMvaa 168,176,216\nAW 97,60\nMvaa 172,184,216\nAR 60,64\nAR 61,65\nMvaa 172,188,220\nCV 60,60,61\nMvaa 140,188,212\nAW 96,60\n"
"v_add_u32 v2, 0x0, v1\nMvaa 140,184,208\ns_lshr_b32 s2, s59, 1\nMvaa 108,184,200\nv_add_u32 v24, s2, v2\nv_xor_b32 v26, 0x1000, v24\nMvaa 108,188,204\nv_lshrrev_b32 v20, 4, v0\n"
"Mvaa 76,188,196\nv_lshlrev_b32 v20, 2, v20\nv_and_b32 v0, 15, v0\nMvaa 76,184,192\ns_waitcnt vmcnt(16) lgkmcnt(0)\ns_barrier\nMvaa 80,224,192\nRa 128,44,16384\n"
"v_sub_u32 v0, v0, v20\nMvaa 80,228,196\nRa 132,45,16384\nbuffer_store_dword v25, off, s[16:19], s54 offset:12 sc0 sc1\ns_add_u32 s6, s56, 4096\n"
"buffer_load_dwordx4 v[208:211], v1, s[16:19], s6 offen offset:0 sc1\nMvaa 112,228,204\nRa 136,44,18432\ns_lshl_b32 s2, s11, 4\nMvaa 112,224,200\nRa 140,45,18432\nMvaa 144,224,208\n"
"Ra 144,46,16384\nv_add_u32 v3, s2, v0\nMvaa 144,228,212\nRa 148,47,16384\nbuffer_load_dwordx4 v[212:215], v1, s[16:19], s6 offen offset:1024 sc1\nMvaa 176,228,220\n"
"Ra 152,46,18432\nv_subrev_u32 v0, 32, v3\nMvaa 176,224,216\nRa 156,47,18432\nMvaa 180,232,216\nTa 160,32,0\ns_lshl_b32 s2, s11, 13\nMvaa 180,236,220\nTa 162,32,2048\n"
"buffer_load_dwordx4 v[216:219], v1, s[16:19], s6 offen offset:2048 sc1\nMvaa 148,236,212\nTa 164,32,16384\ns_add_u32 s2, s2, 0x20000\nMvaa 148,232,208\nTa 166,32,18432\n"
"Mvaa 116,232,200\nTa 168,33,0\nv_add_u32 v22, s2, v1\nMvaa 116,236,204\nTa 170,33,2048\nbuffer_load_dwordx4 v[220:223], v1, s[16:19], s6 offen offset:3072 sc1\nMvaa 84,236,196\n"
"Ta 172,33,16384\nv_xor_b32 v23, 0x2000, v22\nMvaa 84,232,192\nTa 174,33,18432\ns_add_u32 m0, s47, -32768\ns_add_u32 s46, s45, 0x8000\nMvaa 88,240,192\nTa 176,34,0\n"
"v_mov_b32 v4, 0\nMvaa 88,244,196\nTa 178,34,2048\nD1 1,16,46,0\nMvaa 120,244,204\nTa 180,34,16384\nv_mov_b32 v5, 0\nMvaa 120,240,200\nTa 182,34,18432\nMvaa 152,240,208\n"
"Ta 184,35,0\nv_mov_b32 v6, 0\nMvaa 152,244,212\nTa 186,35,2048\nD1 1,16,46,1024\nMvaa 184,244,220\nTa 188,35,16384\nv_mov_b32 v7, 0\nMvaa 184,240,216\nTa 190,35,18432\n"
"Mvaa 188,248,216\nv_mov_b32 v8, 0\nv_mov_b32 v9, 0\nMvaa 188,252,220\nD1 1,16,46,2048\nMvaa 156,252,212\n.W577:\ns_cbranch_execz .W581\nv_mov_b32 v10, 0\nv_mov_b32 v11, 0\n"
"Mvaa 156,248,208\nv_mov_b32 v12, 0\nMvaa 124,248,200\nv_mov_b32 v13, 0\nv_mov_b32 v14, 0\nMvaa 124,252,204\nD1 1,16,46,3072\nMvaa 92,252,196\nv_mov_b32 v15, 0\nv_mov_b32 v16, 0\n"
"Mvaa 92,248,192\nWL 0\nMvaa 64,160,128\nTa 224,36,0\nv_mov_b32 v17, 0\nMvaa 64,164,132\nTa 226,36,2048\ns_add_u32 m0, s47, -16384\ns_add_u32 s46, s45, 0x400000\nD1 1,16,46,0\n"
"Mvaa 68,172,132\nv_mov_b32 v18, 0\nv_mov_b32 v19, 0\nMvaa 68,168,128\nTa 228,36,16384\nMvaa 72,176,128\nTa 230,36,18432\nMvaa 72,180,132\nD1 1,16,46,1024\nMvaa 76,188,132\n"
"Ta 232,37,0\nMvaa 76,184,128\nTa 234,37,2048\nMvaa 108,184,136\nv_mul_f32 v64, s57, v64\nv_mul_f32 v65, s57, v65\nMvaa 108,188,140\nTa 236,37,16384\nD1 1,16,46,2048\n"
"Mvaa 104,180,140\nTa 238,37,18432\nCV 64,64,65\nMvaa 104,176,136\nv_mul_f32 v66, s57, v66\nv_mul_f32 v67, s57, v67\nMvaa 100,168,136\nTa 240,38,0\nCV 65,66,67\nMvaa 100,172,140\n"
"Ta 242,38,2048\nD1 1,16,46,3072\nMvaa 96,164,140\nv_mul_f32 v68, s57, v68\nv_mul_f32 v69, s57, v69\nMvaa 96,160,136\nTa 244,38,16384\nCV 66,68,69\nMvaa 128,160,144\n"
"Ta 246,38,18432\nv_mul_f32 v70, s57, v70\nMvaa 128,164,148\nv_mul_f32 v71, s57, v71\nMvaa 132,172,148\nTa 248,39,0\nCV 67,70,71\nMvaa 132,168,144\nTa 250,39,2048\nAW 192,64\n"
"Mvaa 136,176,144\nAW 193,65\nAW 194,66\nMvaa 136,180,148\nTa 252,39,16384\nAW 195,67\nMvaa 140,188,148\nTa 254,39,18432\nv_mul_f32 v72, s57, v72\nMvaa 140,184,144\n"
"v_mul_f32 v73, s57, v73\nCV 72,72,73\nMvaa 172,184,152\nv_mul_f32 v74, s57, v74\nv_mul_f32 v75, s57, v75\nMvaa 172,188,156\nCV 73,74,75\nv_mul_f32 v76, s57, v76\nMvaa 168,180,156\n"
"v_mul_f32 v77, s57, v77\nCV 74,76,77\nMvaa 168,176,152\nv_mul_f32 v78, s57, v78\nv_mul_f32 v79, s57, v79\nMvaa 164,168,152\nCV 75,78,79\nAW 200,72\nMvaa 164,172,156\nAW 201,73\n"
"AW 202,74\nMvaa 160,164,156\nAW 203,75\nv_mul_f32 v96, s57, v96\nMvaa 160,160,152\ns_waitcnt vmcnt(21) lgkmcnt(0)\ns_barrier\nMvaa 80,224,128\nv_mul_f32 v97, s57, v97\n"
"CV 96,96,97\nMvaa 80,228,132\ns_add_u32 m0, s47, 0\ns_add_u32 s46, s86, 0xffff0000\nD1 1,16,46,0\nMvaa 84,236,132\nv_mul_f32 v98, s57, v98\nv_mul_f32 v99, s57, v99\n"
"Mvaa 84,232,128\nCV 97,98,99\nv_mul_f32 v100, s57, v100\nMvaa 88,240,128\nv_mul_f32 v101, s57, v101\nCV 98,100,101\nMvaa 88,244,132\nD1 1,16,46,1024\nv_mul_f32 v102, s57, v102\n"
"Mvaa 92,252,132\nv_mul_f32 v103, s57, v103\nCV 99,102,103\nMvaa 92,248,128\nAW 196,96\nAW 197,97\nMvaa 124,248,136\nAW 198,98\nAW 199,99\nMvaa 124,252,140\nD1 1,16,46,2048\n"
"v_mul_f32 v128, s57, v128\nMvaa 120,244,140\nv_mul_f32 v129, s57, v129\nCV 128,128,129\nMvaa 120,240,136\nv_mul_f32 v130, s57, v130\nv_mul_f32 v131, s57, v131\nMvaa 116,232,136\n"
"Ra 160,24,0\nCV 129,130,131\nMvaa 116,236,140\nD1 1,16,46,3072\nv_mul_f32 v132, s57, v132\nMvaa 112,228,140\nRa 164,24,1024\nv_mul_f32 v133, s57, v133\nMvaa 112,224,136\n"
"s_add_u32 m0, s47, 16384\ns_add_u32 s46, s45, 0x408000\nMvaa 144,224,144\nRa 168,24,2048\nCV 130,132,133\nMvaa 144,228,148\nD1 1,16,46,0\nMvaa 148,236,148\nRa 172,24,3072\n"
"v_mul_f32 v134, s57, v134\nMvaa 148,232,144\nv_mul_f32 v135, s57, v135\nCV 131,134,135\nMvaa 152,240,144\nRa 176,26,0\nds_write_b128 v23, v[128:131] offset:0\nMvaa 152,244,148\n"
"D1 1,16,46,1024\nv_mul_f32 v80, s57, v80\nMvaa 156,252,148\nRa 180,26,1024\nv_mul_f32 v81, s57, v81\nMvaa 156,248,144\nCV 80,80,81\nv_mul_f32 v82, s57, v82\nMvaa 188,248,152\n"
"Ra 184,26,2048\nv_mul_f32 v83, s57, v83\nMvaa 188,252,156\nD1 1,16,46,2048\nCV 81,82,83\nMvaa 184,244,156\nRa 188,26,3072\nv_mul_f32 v84, s57, v84\nMvaa 184,240,152\n"
"v_mul_f32 v85, s57, v85\nCV 82,84,85\nMvaa 180,232,152\nv_mul_f32 v86, s57, v86\nv_mul_f32 v87, s57, v87\nMvaa 180,236,156\nD1 1,16,46,3072\nCV 83,86,87\nMvaa 176,228,156\n"
"AW 208,80\nAW 209,81\nMvaa 176,224,152\ns_waitcnt vmcnt(16) lgkmcnt(5)\nCvav 32,160,192,4\nRa 128,24,8192\nAW 210,82\nMvav 32,164,200\nAW 211,83\nMvav 32,168,208\nRa 132,24,9216\n"
"v_mul_f32 v88, s57, v88\nMvav 32,172,216\nv_mul_f32 v89, s57, v89\nCV 88,88,89\nCvav 36,172,220,8\nRa 136,24,10240\nv_mul_f32 v90, s57, v90\nMvav 36,168,212\n"
"s_add_u32 m0, s89, 32768\nv_mul_f32 v91, s57, v91\nMvav 36,164,204\nRa 140,24,11264\nWL 4\ns_barrier\nMvav 36,160,196\nCV 89,90,91\nv_mul_f32 v92, s57, v92\nMvav 36,176,228\n"
"Ra 144,26,8192\nSa 96,1,16,78,0\nMvav 36,180,236\ns_add_u32 s46, s86, s88\nv_mul_f32 v93, s57, v93\nMvav 36,184,244\nRa 148,26,9216\nCV 90,92,93\nMvav 36,188,252\n"
"v_mul_f32 v94, s57, v94\nv_mul_f32 v95, s57, v95\nMvav 32,188,248\nRa 152,26,10240\nCV 91,94,95\nMvav 32,184,240\nD1 1,16,46,0\nAW 216,88\nMvav 32,180,232\nRa 156,26,11264\nWL 4\n"
"Mvav 32,176,224\nAW 217,89\nAW 218,90\nCvav 40,128,192,12\nSa 100,1,16,78,1024\nAW 219,91\nMvav 40,132,200\nv_mov_b32 v86, 0\nMvav 40,136,208\nv_mov_b32 v87, 0\nv_mov_b32 v88, 0\n"
"Mvav 40,140,216\nv_mov_b32 v89, 0\nv_mul_f32 v136, s57, v136\nv_mul_f32 v137, s57, v137\nCV 136,136,137\nv_mul_f32 v138, s57, v138\nv_mul_f32 v139, s57, v139\nCV 137,138,139\n"
"Cvav 44,140,220,16\nv_and_b32 v27, 0x70, v1\nbuffer_load_dword v27, v27, s[16:19], s61 offen offset:136 sc0 sc1\nv_mul_f32 v140, s57, v140\nv_mul_f32 v141, s57, v141\n"
"CV 138,140,141\nv_mul_f32 v142, s57, v142\nv_mul_f32 v143, s57, v143\nMvav 44,136,212\nD1 1,16,46,1024\nCV 139,142,143\nds_write_b128 v23, v[136:139] offset:2048\n"
"v_mul_f32 v144, s57, v144\nv_mul_f32 v145, s57, v145\nCV 144,144,145\nv_mul_f32 v146, s57, v146\nMvav 44,132,204\nWL 1\nv_mul_f32 v147, s57, v147\nCV 145,146,147\n"
"v_mul_f32 v148, s57, v148\nv_mul_f32 v149, s57, v149\nCV 146,148,149\nMvav 44,128,196\nv_mul_f32 v150, s57, v150\nv_mul_f32 v151, s57, v151\nCV 147,150,151\n"
"ds_write_b128 v23, v[144:147] offset:4096\nv_mul_f32 v152, s57, v152\nv_mul_f32 v153, s57, v153\nCV 152,152,153\nMvav 44,144,228\nSa 104,1,16,78,2048\nv_mul_f32 v154, s57, v154\n"
"v_mul_f32 v155, s57, v155\nCV 153,154,155\nv_mul_f32 v156, s57, v156\nv_mul_f32 v157, s57, v157\nCV 154,156,157\nMvav 44,148,236\ns_add_u32 s88, s88, 32768\nEX 80,32\n"
"v_mul_f32 v158, s57, v158\nv_mul_f32 v159, s57, v159\nCV 155,158,159\nds_write_b128 v23, v[152:155] offset:6144\nMvav 44,152,244\nv_mul_f32 v160, s57, v160\n"
"v_mul_f32 v161, s57, v161\nCV 160,160,161\nv_mul_f32 v162, s57, v162\nv_mul_f32 v163, s57, v163\nCV 161,162,163\nv_mul_f32 v164, s57, v164\nMvav 44,156,252\nEX 81,33\n"
"v_mul_f32 v165, s57, v165\nCV 162,164,165\nv_mul_f32 v166, s57, v166\nMvav 40,156,248\nv_mul_f32 v167, s57, v167\nCV 163,166,167\nds_write_b128 v23, v[160:163] offset:1024\n"
"v_mul_f32 v168, s57, v168\nv_mul_f32 v169, s57, v169\nCV 168,168,169\nv_mul_f32 v170, s57, v170\nMvav 40,152,240\nD1 1,16,46,2048\nv_mul_f32 v171, s57, v171\nCV 169,170,171\n"
"v_mul_f32 v172, s57, v172\nv_mul_f32 v173, s57, v173\nCV 170,172,173\nv_mul_f32 v174, s57, v174\nMvav 40,148,232\nv_mul_f32 v175, s57, v175\nCV 171,174,175\n"
"ds_write_b128 v23, v[168:171] offset:3072\nv_mul_f32 v176, s57, v176\nv_mul_f32 v177, s57, v177\nCV 176,176,177\nv_mul_f32 v178, s57, v178\nMvav 40,144,224\nAD 84,80,81\n"
"CV 64,80,81\nv_mul_f32 v179, s57, v179\nCV 177,178,179\nv_mul_f32 v180, s57, v180\nv_mul_f32 v181, s57, v181\nCV 178,180,181\nSa 108,1,16,78,3072\nv_mul_f32 v182, s57, v182\n"
"v_mul_f32 v183, s57, v183\nCV 179,182,183\nds_write_b128 v23, v[176:179] offset:5120\nv_mul_f32 v184, s57, v184\nv_mul_f32 v185, s57, v185\nEX 82,34\nCV 184,184,185\n"
"v_mul_f32 v186, s57, v186\nv_mul_f32 v187, s57, v187\nCV 185,186,187\nv_mul_f32 v188, s57, v188\nv_mul_f32 v189, s57, v189\nCV 186,188,189\nv_mul_f32 v190, s57, v190\n"
"v_mul_f32 v191, s57, v191\nCV 187,190,191\nds_write_b128 v23, v[184:187] offset:7168\ns_waitcnt vmcnt(16) lgkmcnt(0)\nEX 83,35\nAD 84,84,82\nD1 1,16,46,3072\n"
"s_add_u32 m0, s89, 49152\ns_barrier\nAD 84,84,83\nCV 65,82,83\nRa 128,24,32768\nRa 224,22,0\nRa 228,22,1024\ns_add_u32 s46, s87, s88\nEX 80,36\nRa 132,24,33792\nRa 232,22,2048\n"
"Ra 236,22,3072\nRa 240,22,4096\nEX 81,37\nRa 136,24,34816\nRa 244,22,5120\nRa 248,22,6144\nD1 1,16,46,0\nRa 252,22,7168\nv_mul_f32 v104, s57, v104\nv_mul_f32 v105, s57, v105\n"
"Ra 140,24,35840\nCV 104,104,105\nv_mul_f32 v106, s57, v106\nAD 85,80,81\nCV 68,80,81\nv_mul_f32 v107, s57, v107\nRa 144,26,32768\nCV 105,106,107\nv_mul_f32 v108, s57, v108\n"
"v_mul_f32 v109, s57, v109\nEX 82,38\nCV 106,108,109\nRa 148,26,33792\nv_mul_f32 v110, s57, v110\nv_mul_f32 v111, s57, v111\nEX 83,39\nRa 152,26,34816\nAD 85,85,82\nCV 107,110,111\n"
"AW 204,104\nD1 1,16,46,1024\nAW 205,105\nAW 206,106\nRa 156,26,35840\nAW 207,107\nv_mul_f32 v112, s57, v112\nAD 85,85,83\nCV 69,82,83\nv_mul_f32 v113, s57, v113\nCV 112,112,113\n"
"Ra 160,24,40960\nv_mul_f32 v114, s57, v114\nv_mul_f32 v115, s57, v115\ns_xor_b32 s89, s89, 0x10000\nEX 80,40\nRa 164,24,41984\nCV 113,114,115\nv_mul_f32 v116, s57, v116\n"
"v_mul_f32 v117, s57, v117\nWL 6\nEX 81,41\nRa 168,24,43008\nAD 84,84,80\nCV 114,116,117\nD1 1,16,46,2048\nv_mul_f32 v118, s57, v118\nv_mul_f32 v119, s57, v119\nCV 115,118,119\n"
"Ra 172,24,44032\nAW 212,112\nAW 213,113\nAD 84,84,81\nCV 66,80,81\nAW 214,114\nCvav 48,128,192,4\nRa 176,26,40960\nAW 215,115\nv_mul_f32 v120, s57, v120\n"
"v_mul_f32 v121, s57, v121\nMvav 48,132,200\nEX 82,42\nCV 120,120,121\nMvav 48,136,208\nRa 180,26,41984\nv_mul_f32 v122, s57, v122\nv_mul_f32 v123, s57, v123\nMvav 48,140,216\n"
"WL 6\nEX 83,43\nWV 8\nv_cmp_ne_u32 vcc, s15, v27\ns_nop 0\ns_cbranch_vccnz .S290\n.S291:\nCvav 52,140,220,8\nRa 184,26,43008\nAD 84,84,82\nCV 121,122,123\n"
"v_mul_f32 v124, s57, v124\nMvav 52,136,212\nD1 1,16,46,3072\ns_add_u32 m0, s89, 0\nv_mul_f32 v125, s57, v125\nMvav 52,132,204\nRa 188,26,44032\nCV 122,124,125\n"
"v_mul_f32 v126, s57, v126\nMvav 52,128,196\nAD 84,84,83\nCV 67,82,83\nv_mul_f32 v127, s57, v127\nCV 123,126,127\nMvav 52,144,228\nRa 128,2,49152\ns_add_u32 s78, s78, s55\n"
"Sa 112,1,16,78,0\nMvav 52,148,236\ns_add_u32 s46, s86, s88\nEX 80,44\nMvav 52,152,244\nRa 132,2,50176\nv_xor_b32 v24, 0x10000, v24\nAW 220,120\nAW 221,121\nMvav 52,156,252\nWL 6\n"
"EX 81,45\nMvav 48,156,248\nRa 136,2,51200\nAD 85,85,80\nAW 222,122\nMvav 48,152,240\nD1 1,16,46,0\nAW 223,123\nv_mov_b32 v112, -1\nv_mov_b32 v113, -1\nMvav 48,148,232\n"
"Ra 140,2,52224\nv_mov_b32 v114, -1\nv_mov_b32 v115, -1\nMvav 48,144,224\nAD 85,85,81\nCV 70,80,81\nCvav 56,160,192,12\nRa 144,2,53248\nSa 116,1,16,78,1024\nMvav 56,164,200\n"
"EX 82,46\nMvav 56,168,208\nRa 148,2,54272\nv_xor_b32 v26, 0x10000, v26\nMvav 56,172,216\nWL 6\nEX 83,47\nCvav 60,172,220,16\nRa 152,2,55296\nAD 85,85,82\nMvav 60,168,212\n"
"D1 1,16,46,1024\nMvav 60,164,204\nRa 156,2,56320\nMvav 60,160,196\nAD 85,85,83\nCV 71,82,83\nMvav 60,176,228\nRa 160,2,57344\nv_max_f32 v90, v84, v85\nMvav 60,180,236\n"
"s_add_u32 s88, s88, 32768\nEX 80,48\nMvav 60,184,244\nRa 164,2,58368\nv_cmp_lt_f32 vcc, s63, v90\nMvav 60,188,252\nWL 8\nEX 81,49\nMvav 56,188,248\nRa 168,2,59392\n"
"s_cbranch_vccnz .T279\n.T280:\nv_add_f32 v20, s93, v84\nv_mov_b32 v21, v85\nv_mov_b32 v22, v20\nv_mov_b32 v23, v21\nMvav 56,184,240\nD1 1,16,46,2048\n"
"v_permlane32_swap_b32 v20, v22\nv_permlane32_swap_b32 v21, v23\nAD 20,20,22\nAD 21,21,23\nv_mov_b32 v22, v20\nv_mov_b32 v23, v21\nMvav 56,180,232\nRa 172,2,60416\n"
"v_permlane16_swap_b32 v20, v22\nv_permlane16_swap_b32 v21, v23\nAD 20,20,22\nAD 21,21,23\nv_min_f32 v90, v20, v21\nv_cmp_gt_f32 vcc, s64, v90\ns_nop 0\ns_cbranch_vccnz .T281\n"
".T282:\nAD 86,86,84\nMvav 56,176,224\nAD 84,80,81\nCV 72,80,81\nZaav 0,128,64\nRa 176,2,61440\nSa 120,1,16,78,2048\nZaav 4,128,68\nEX 82,50\nZaav 12,132,68\nRa 180,2,62464\n"
"Zaav 8,132,64\nWL 8\nEX 83,51\nZaav 16,136,64\nRa 184,2,63488\nAD 84,84,82\nZaav 20,136,68\nD1 1,16,46,3072\ns_add_u32 m0, s89, 16384\nZaav 28,140,68\nRa 188,2,64512\ns_barrier\n"
"Zaav 24,140,64\nAD 84,84,83\nCV 73,82,83\nZaav 32,144,64\nRa 128,24,0\nSa 124,1,16,78,3072\nZaav 36,144,68\ns_add_u32 s46, s87, s88\nEX 80,52\nZaav 44,148,68\nRa 132,24,1024\n"
"v_xor_b32 v2, 0x10000, v2\nZaav 40,148,64\nWL 8\nEX 81,53\nZaav 48,152,64\nRa 136,24,2048\nAD 87,87,85\nZaav 52,152,68\nD1 1,16,46,0\nZaav 60,156,68\nRa 140,24,3072\n"
"Zaav 56,156,64\nAD 85,80,81\nCV 76,80,81\nZaav 64,160,64\nRa 144,26,0\nZaav 68,160,68\n.W578:\ns_cbranch_execz .W582\ns_cmp_lt_u32 s99, s92\nEX 82,54\nZaav 76,164,68\n"
"Ra 148,26,1024\ns_cbranch_scc0 .E277\nv_cmpx_gt_i32 vcc, 19, v3\nv_mov_b32 v15, s97\nv_cmpx_gt_i32 vcc, 18, v3\nv_mov_b32 v14, s97\nv_cmpx_gt_i32 vcc, 17, v3\nv_mov_b32 v13, s97\n"
"v_cmpx_gt_i32 vcc, 16, v3\nv_mov_b32 v12, s97\nv_cmpx_gt_i32 vcc, 3, v3\nv_mov_b32 v7, s97\nv_cmpx_gt_i32 vcc, 2, v3\nv_mov_b32 v6, s97\nv_cmpx_gt_i32 vcc, 1, v3\n"
"v_mov_b32 v5, s97\nv_cmpx_gt_i32 vcc, 0, v3\nv_mov_b32 v4, s97\ns_mov_b64 exec, -1\ns_nop 2\n.E277:\ns_nop 0\nZaav 72,164,64\nWL 8\nEX 83,55\nZaav 80,168,64\nRa 152,26,2048\n"
"AD 85,85,82\nZaav 84,168,68\nD1 1,16,46,1024\nZaav 92,172,68\nRa 156,26,3072\nZaav 88,172,64\nAD 85,85,83\nCV 77,82,83\nZaav 96,176,64\nRa 160,24,8192\nZaav 100,176,68\nEX 80,56\n"
"Zaav 108,180,68\nRa 164,24,9216\nZaav 104,180,64\nWL 6\nEX 81,57\nZaav 112,184,64\nRa 168,24,10240\nAD 84,84,80\nv_and_b32 v27, 0x70, v1\n"
"buffer_load_dword v27, v27, s[16:19], s61 offen offset:140 sc0 sc1\nZaav 116,184,68\nD1 1,16,46,2048\nZaav 124,188,68\nRa 172,24,11264\nZaav 120,188,64\nAD 84,84,81\nCV 74,80,81\n"
"s_sub_u32 s99, s99, 1\n.p2align 6\n.A305:\nCvav 32,128,192,4\nRa 176,26,8192\nMvav 32,132,200\nEX 82,58\nMvav 32,136,208\nRa 180,26,9216\nMvav 32,140,216\nWL 6\nEX 83,59\n"
"Cvav 36,140,220,8\nRa 184,26,10240\nAD 84,84,82\nMvav 36,136,212\nD1 1,16,46,3072\ns_add_u32 m0, s89, 32768\nMvav 36,132,204\nRa 188,26,11264\nMvav 36,128,196\nAD 84,84,83\n"
"CV 75,82,83\nMvav 36,144,228\nRa 128,2,16384\nMvav 36,148,236\ns_add_u32 s46, s86, s88\nEX 80,60\nMvav 36,152,244\nRa 132,2,17408\nMvav 36,156,252\nWL 6\nEX 81,61\n"
"Mvav 32,156,248\nRa 136,2,18432\nAD 85,85,80\nMvav 32,152,240\nD1 1,16,46,0\nMvav 32,148,232\nRa 140,2,19456\nSv 112,1,16,49,0\nMvav 32,144,224\nAD 85,85,81\nCV 78,80,81\n"
"Cvav 40,160,192,12\nRa 144,2,20480\nMvav 40,164,200\nEX 82,62\nMvav 40,168,208\nRa 148,2,21504\nMvav 40,172,216\nWL 6\nEX 83,63\nCvav 44,172,220,16\nRa 152,2,22528\nAD 85,85,82\n"
"Mvav 44,168,212\nD1 1,16,46,1024\nMvav 44,164,204\nRa 156,2,23552\nMvav 44,160,196\nAD 85,85,83\nCV 79,82,83\nMvav 44,176,228\nRa 160,2,24576\nv_max_f32 v90, v84, v85\n"
"Mvav 44,180,236\ns_add_u32 s88, s88, 32768\nEX 80,32\nMvav 44,184,244\nRa 164,2,25600\nv_cmp_lt_f32 vcc, s63, v90\nMvav 44,188,252\nWL 8\nEX 81,33\nMvav 40,188,248\n"
"Ra 168,2,26624\ns_cbranch_vccnz .T313\n.T314:\nMvav 40,184,240\nD1 1,16,46,2048\nMvav 40,180,232\nRa 172,2,27648\nAD 86,86,84\nMvav 40,176,224\nAD 84,80,81\nCV 64,80,81\n"
"Maav 0,128,72\nRa 176,2,28672\nMaav 4,128,76\nEX 82,34\nMaav 12,132,76\nRa 180,2,29696\nMaav 8,132,72\ns_waitcnt vmcnt(13) lgkmcnt(8)\nEX 83,35\nMaav 16,136,72\nRa 184,2,30720\n"
"AD 84,84,82\nMaav 20,136,76\nD1 1,16,46,3072\ns_add_u32 m0, s89, 49152\nMaav 28,140,76\nRa 188,2,31744\ns_barrier\nMaav 24,140,72\nAD 84,84,83\nCV 65,82,83\nMaav 32,144,72\n"
"Ra 128,24,32768\nMaav 36,144,76\ns_add_u32 s46, s87, s88\nEX 80,36\nMaav 44,148,76\nRa 132,24,33792\nMaav 40,148,72\nWL 8\nEX 81,37\nMaav 48,152,72\nRa 136,24,34816\nAD 87,87,85\n"
"Maav 52,152,76\nD1 1,16,46,0\nMaav 60,156,76\nRa 140,24,35840\nMaav 56,156,72\nAD 85,80,81\nCV 68,80,81\nMaav 64,160,72\nRa 144,26,32768\nMaav 68,160,76\ns_cmp_eq_u32 s99, 0\n"
"EX 82,38\nMaav 76,164,76\nRa 148,26,33792\ns_cbranch_scc1 .E307\n.E308:\nMaav 72,164,72\nWL 8\nEX 83,39\nMaav 80,168,72\nRa 152,26,34816\nAD 85,85,82\nMaav 84,168,76\n"
"D1 1,16,46,1024\nMaav 92,172,76\nRa 156,26,35840\nMaav 88,172,72\nAD 85,85,83\nCV 69,82,83\nMaav 96,176,72\nRa 160,24,40960\nMaav 100,176,76\ns_xor_b32 s89, s89, 0x10000\n"
"EX 80,40\nMaav 108,180,76\nRa 164,24,41984\nMaav 104,180,72\nWL 6\nEX 81,41\nMaav 112,184,72\nRa 168,24,43008\nAD 84,84,80\nMaav 116,184,76\nD1 1,16,46,2048\nMaav 124,188,76\n"
"Ra 172,24,44032\nMaav 120,188,72\nAD 84,84,81\nCV 66,80,81\nCvav 48,128,192,4\nRa 176,26,40960\nMvav 48,132,200\ns_cmp_eq_u32 s99, 0\nEX 82,42\nMvav 48,136,208\nRa 180,26,41984\n"
"s_cselect_b32 s86, s94, s86\nMvav 48,140,216\nWL 6\nEX 83,43\nCvav 52,140,220,8\nRa 184,26,43008\nAD 84,84,82\nMvav 52,136,212\nD1 1,16,46,3072\ns_add_u32 m0, s89, 0\n"
"Mvav 52,132,204\nRa 188,26,44032\nMvav 52,128,196\nAD 84,84,83\nCV 67,82,83\nMvav 52,144,228\nRa 128,2,49152\nMvav 52,148,236\ns_add_u32 s46, s86, s88\nEX 80,44\nMvav 52,152,244\n"
"Ra 132,2,50176\nv_xor_b32 v24, 0x10000, v24\nMvav 52,156,252\nWL 6\nEX 81,45\nMvav 48,156,248\nRa 136,2,51200\nAD 85,85,80\nMvav 48,152,240\nD1 1,16,46,0\nMvav 48,148,232\n"
"Ra 140,2,52224\nSv 112,1,16,49,1024\nMvav 48,144,224\nAD 85,85,81\nCV 70,80,81\nCvav 56,160,192,12\nRa 144,2,53248\nMvav 56,164,200\ns_add_u32 s49, s49, 131072\nEX 82,46\n"
"Mvav 56,168,208\nRa 148,2,54272\nv_xor_b32 v26, 0x10000, v26\nMvav 56,172,216\nWL 6\nEX 83,47\nCvav 60,172,220,16\nRa 152,2,55296\nAD 85,85,82\nMvav 60,168,212\nD1 1,16,46,1024\n"
"Mvav 60,164,204\nRa 156,2,56320\nMvav 60,160,196\nAD 85,85,83\nCV 71,82,83\nMvav 60,176,228\nRa 160,2,57344\nv_max_f32 v90, v84, v85\nMvav 60,180,236\ns_add_u32 s88, s88, 32768\n"
"EX 80,48\nMvav 60,184,244\nRa 164,2,58368\nv_cmp_lt_f32 vcc, s63, v90\nMvav 60,188,252\nWL 8\nEX 81,49\nMvav 56,188,248\nRa 168,2,59392\ns_cbranch_vccnz .T315\n.T316:\n"
"Mvav 56,184,240\nD1 1,16,46,2048\nMvav 56,180,232\nRa 172,2,60416\nAD 86,86,84\nMvav 56,176,224\nAD 84,80,81\nCV 72,80,81\nMaav 0,128,64\nRa 176,2,61440\nMaav 4,128,68\nEX 82,50\n"
"Maav 12,132,68\nRa 180,2,62464\nMaav 8,132,64\ns_waitcnt vmcnt(13) lgkmcnt(8)\nEX 83,51\nMaav 16,136,64\nRa 184,2,63488\nAD 84,84,82\nMaav 20,136,68\nD1 1,16,46,3072\n"
"s_add_u32 m0, s89, 16384\nMaav 28,140,68\nRa 188,2,64512\ns_barrier\nMaav 24,140,64\nAD 84,84,83\nCV 73,82,83\nMaav 32,144,64\nRa 128,24,0\nMaav 36,144,68\n"
"s_add_u32 s46, s87, s88\nEX 80,52\nMaav 44,148,68\nRa 132,24,1024\nv_xor_b32 v2, 0x10000, v2\nMaav 40,148,64\nWL 8\nEX 81,53\nMaav 48,152,64\nRa 136,24,2048\nAD 87,87,85\n"
"Maav 52,152,68\nD1 1,16,46,0\nMaav 60,156,68\nRa 140,24,3072\nMaav 56,156,64\nAD 85,80,81\nCV 76,80,81\nMaav 64,160,64\nRa 144,26,0\nMaav 68,160,68\ns_cmp_lt_u32 s99, s92\n"
"EX 82,54\nMaav 76,164,68\nRa 148,26,1024\ns_cbranch_scc1 .E309\n.E310:\nMaav 72,164,64\nWL 8\nEX 83,55\nMaav 80,168,64\nRa 152,26,2048\nAD 85,85,82\nMaav 84,168,68\n"
"D1 1,16,46,1024\nMaav 92,172,68\nRa 156,26,3072\nMaav 88,172,64\nAD 85,85,83\nCV 77,82,83\nMaav 96,176,64\nRa 160,24,8192\nMaav 100,176,68\ns_sub_u32 s90, s90, 1\nEX 80,56\n"
"Maav 108,180,68\nRa 164,24,9216\ns_cbranch_scc1 .E311\n.E312:\nMaav 104,180,64\nWL 6\nEX 81,57\nMaav 112,184,64\nRa 168,24,10240\nAD 84,84,80\nMaav 116,184,68\nD1 1,16,46,2048\n"
"s_sub_u32 s99, s99, 1\nMaav 124,188,68\nRa 172,24,11264\nMaav 120,188,64\nAD 84,84,81\nCV 74,80,81\ns_cbranch_scc0 .A305\ns_branch .A317\n.E307:\nv_cmpx_gt_i32 vcc, 19, v0\n"
"v_mov_b32 v15, s97\nv_cmpx_gt_i32 vcc, 18, v0\nv_mov_b32 v14, s97\nv_cmpx_gt_i32 vcc, 17, v0\nv_mov_b32 v13, s97\nv_cmpx_gt_i32 vcc, 16, v0\nv_mov_b32 v12, s97\n"
"v_cmpx_gt_i32 vcc, 3, v0\nv_mov_b32 v7, s97\nv_cmpx_gt_i32 vcc, 2, v0\nv_mov_b32 v6, s97\nv_cmpx_gt_i32 vcc, 1, v0\nv_mov_b32 v5, s97\nv_cmpx_gt_i32 vcc, 0, v0\nv_mov_b32 v4, s97\n"
"s_mov_b64 exec, -1\ns_nop 2\ns_branch .E308\n.E309:\ns_cmp_eq_u32 s99, 0\ns_cbranch_scc1 .K306\nv_cmpx_gt_i32 vcc, 19, v3\nv_mov_b32 v15, s97\nv_cmpx_gt_i32 vcc, 18, v3\n"
"v_mov_b32 v14, s97\nv_cmpx_gt_i32 vcc, 17, v3\nv_mov_b32 v13, s97\nv_cmpx_gt_i32 vcc, 16, v3\nv_mov_b32 v12, s97\nv_cmpx_gt_i32 vcc, 3, v3\nv_mov_b32 v7, s97\n"
"v_cmpx_gt_i32 vcc, 2, v3\nv_mov_b32 v6, s97\nv_cmpx_gt_i32 vcc, 1, v3\nv_mov_b32 v5, s97\nv_cmpx_gt_i32 vcc, 0, v3\nv_mov_b32 v4, s97\ns_mov_b64 exec, -1\ns_nop 2\ns_branch .E310\n"
".K306:\nv_cmpx_gt_i32 vcc, 19, v3\nv_mov_b32 v19, s97\nv_cmpx_gt_i32 vcc, 18, v3\nv_mov_b32 v18, s97\nv_cmpx_gt_i32 vcc, 17, v3\nv_mov_b32 v17, s97\nv_cmpx_gt_i32 vcc, 16, v3\n"
"v_mov_b32 v16, s97\nv_cmpx_gt_i32 vcc, 3, v3\nv_mov_b32 v11, s97\nv_cmpx_gt_i32 vcc, 2, v3\nv_mov_b32 v10, s97\nv_cmpx_gt_i32 vcc, 1, v3\nv_mov_b32 v9, s97\n"
"v_cmpx_gt_i32 vcc, 0, v3\nv_mov_b32 v8, s97\ns_mov_b64 exec, -1\ns_nop 2\ns_branch .E310\n.E311:\ns_swappc_b64 s[80:81], s[8:9]\ns_branch .E312\n.HD269:\n"
"v_cmp_ne_u32 vcc, s15, v27\ns_nop 0\ns_cbranch_vccnz .S293\n.S294:\nbuffer_store_dword v25, off, s[16:19], s54 offset:4 sc0 sc1\nv_and_b32 v27, 0x70, v1\n"
"buffer_load_dword v27, v27, s[16:19], s61 offen offset:12 sc0 sc1\ns_mov_b32 s90, 1\ns_swappc_b64 s[8:9], s[80:81]\nv_cmp_ne_u32 vcc, s15, v27\ns_nop 0\ns_cbranch_vccnz .S296\n"
".S297:\nv_and_b32 v27, 0x70, v1\nbuffer_load_dword v27, v27, s[16:19], s61 offen offset:8 sc0 sc1\ns_mov_b32 s90, 1\ns_swappc_b64 s[8:9], s[80:81]\nv_cmp_ne_u32 vcc, s15, v27\n"
"s_nop 0\ns_cbranch_vccnz .S299\n.S300:\nv_and_b32 v27, 0xf0, v1\nbuffer_load_dword v27, v27, s[16:19], s61 offen offset:4 sc0 sc1\ns_mov_b32 s90, 1\ns_swappc_b64 s[8:9], s[80:81]\n"
"v_cmp_ne_u32 vcc, s15, v27\ns_nop 0\ns_cbranch_vccnz .S302\n.S303:\ns_mov_b32 s90, 100\ns_swappc_b64 s[8:9], s[80:81]\n.A317:\nRa 176,26,8192\nEX 82,58\nRa 180,26,9216\nWL 6\n"
"EX 83,59\nCvav 36,140,220,8\nRa 184,26,10240\nAD 84,84,82\nMvav 36,136,212\nD1 1,16,46,3072\ns_add_u32 m0, s89, 32768\nMvav 36,132,204\nRa 188,26,11264\nMvav 36,128,196\n"
"AD 84,84,83\nCV 75,82,83\nMvav 36,144,228\nRa 128,2,16384\nMvav 36,148,236\ns_add_u32 s46, s86, s88\nEX 80,60\nMvav 36,152,244\nRa 132,2,17408\nMvav 36,156,252\nEX 81,61\n"
"Ra 136,2,18432\nAD 85,85,80\nD1 1,16,46,0\nRa 140,2,19456\nSv 112,1,16,49,0\nAD 85,85,81\nCV 78,80,81\nRa 144,2,20480\ns_mov_b32 s87, s95\nEX 82,62\nRa 148,2,21504\nWL 6\n"
"EX 83,63\nCvav 44,172,220,16\nRa 152,2,22528\nAD 85,85,82\nMvav 44,168,212\nD1 1,16,46,1024\nMvav 44,164,204\nRa 156,2,23552\nMvav 44,160,196\nAD 85,85,83\nCV 79,82,83\n"
"Mvav 44,176,228\nRa 160,2,24576\nv_max_f32 v90, v84, v85\nMvav 44,180,236\ns_add_u32 s88, s88, 32768\nMvav 44,184,244\nRa 164,2,25600\nv_cmp_lt_f32 vcc, s63, v90\nMvav 44,188,252\n"
"WL 8\nRa 168,2,26624\ns_cbranch_vccnz .T319\n.T320:\nD1 1,16,46,2048\nRa 172,2,27648\nAD 86,86,84\nMaav 0,128,72\nRa 176,2,28672\nMaav 4,128,76\nMaav 12,132,76\nRa 180,2,29696\n"
"Maav 8,132,72\ns_waitcnt vmcnt(13) lgkmcnt(8)\nMaav 16,136,72\nRa 184,2,30720\nMaav 20,136,76\nD1 1,16,46,3072\ns_add_u32 m0, s89, 49152\nMaav 28,140,76\nRa 188,2,31744\n"
"s_barrier\nMaav 24,140,72\nMaav 32,144,72\nRa 128,24,32768\nMaav 36,144,76\ns_add_u32 s46, s87, s88\nEX 80,36\nMaav 44,148,76\nRa 132,24,33792\nMaav 40,148,72\nWL 8\nEX 81,37\n"
"Maav 48,152,72\nRa 136,24,34816\nAD 87,87,85\nMaav 52,152,76\nD1 1,16,46,0\nMaav 60,156,76\nRa 140,24,35840\nMaav 56,156,72\nAD 85,80,81\nCV 68,80,81\nMaav 64,160,72\n"
"Ra 144,26,32768\nMaav 68,160,76\ns_nop 0\nEX 82,38\nMaav 76,164,76\nRa 148,26,33792\nv_cmpx_gt_i32 vcc, 19, v0\nv_mov_b32 v19, s97\nv_cmpx_gt_i32 vcc, 18, v0\nv_mov_b32 v18, s97\n"
"v_cmpx_gt_i32 vcc, 17, v0\nv_mov_b32 v17, s97\nv_cmpx_gt_i32 vcc, 16, v0\nv_mov_b32 v16, s97\nv_cmpx_gt_i32 vcc, 3, v0\nv_mov_b32 v11, s97\nv_cmpx_gt_i32 vcc, 2, v0\n"
"v_mov_b32 v10, s97\nv_cmpx_gt_i32 vcc, 1, v0\nv_mov_b32 v9, s97\nv_cmpx_gt_i32 vcc, 0, v0\nv_mov_b32 v8, s97\ns_mov_b64 exec, -1\ns_nop 2\ns_nop 0\nMaav 72,164,72\nWL 8\nEX 83,39\n"
"Maav 80,168,72\nRa 152,26,34816\nAD 85,85,82\nMaav 84,168,76\nD1 1,16,46,1024\nMaav 92,172,76\nRa 156,26,35840\nMaav 88,172,72\nAD 85,85,83\nCV 69,82,83\nMaav 96,176,72\n"
"Ra 160,24,40960\nMaav 100,176,76\ns_xor_b32 s89, s89, 0x10000\nMaav 108,180,76\nRa 164,24,41984\nMaav 104,180,72\nWL 10\nMaav 112,184,72\nRa 168,24,43008\nMaav 116,184,76\n"
"D1 1,16,46,2048\nMaav 124,188,76\nRa 172,24,44032\nMaav 120,188,72\nRa 176,26,40960\nRa 180,26,41984\nWL 6\n.W579:\ns_cbranch_execz .W583\nCvav 52,140,220,8\nRa 184,26,43008\n"
"Mvav 52,136,212\nD1 1,16,46,3072\ns_add_u32 m0, s89, 0\nMvav 52,132,204\nRa 188,26,44032\nMvav 52,128,196\nMvav 52,144,228\nRa 128,2,49152\nMvav 52,148,236\n"
"s_add_u32 s46, s86, s88\nEX 80,44\nMvav 52,152,244\nRa 132,2,50176\nv_xor_b32 v24, 0x10000, v24\nMvav 52,156,252\nEX 81,45\nRa 136,2,51200\nAD 85,85,80\nD1 1,16,46,0\n"
"Ra 140,2,52224\nSv 112,1,16,49,1024\nAD 85,85,81\nCV 70,80,81\nRa 144,2,53248\ns_add_u32 s49, s49, 131072\nEX 82,46\nRa 148,2,54272\nv_xor_b32 v26, 0x10000, v26\nWL 6\nEX 83,47\n"
"Cvav 60,172,220,16\nRa 152,2,55296\nAD 85,85,82\nMvav 60,168,212\nD1 1,16,46,1024\nMvav 60,164,204\nRa 156,2,56320\nMvav 60,160,196\nAD 85,85,83\nCV 71,82,83\nMvav 60,176,228\n"
"Ra 160,2,57344\nMvav 60,180,236\ns_add_u32 s88, s88, 32768\nMvav 60,184,244\nRa 164,2,58368\nv_cmp_lt_f32 vcc, s63, v85\nMvav 60,188,252\nWL 8\nRa 168,2,59392\n"
"s_cbranch_vccnz .T321\n.T322:\nD1 1,16,46,2048\nRa 172,2,60416\nRa 176,2,61440\nMaav 4,128,68\nMaav 12,132,68\nRa 180,2,62464\ns_waitcnt vmcnt(13) lgkmcnt(8)\nRa 184,2,63488\n"
"Maav 20,136,68\nD1 1,16,46,3072\ns_add_u32 m0, s89, 16384\nMaav 28,140,68\nRa 188,2,64512\ns_barrier\nRa 128,24,0\nMaav 36,144,68\ns_add_u32 s46, s87, s88\nEX 80,52\n"
"Maav 44,148,68\nRa 132,24,1024\nv_xor_b32 v2, 0x10000, v2\nWL 8\nEX 81,53\nRa 136,24,2048\nAD 87,87,85\nMaav 52,152,68\nD1 1,16,46,0\nMaav 60,156,68\nRa 140,24,3072\nAD 85,80,81\n"
"CV 76,80,81\nRa 144,26,0\nMaav 68,160,68\nEX 82,54\nMaav 76,164,68\nRa 148,26,1024\nWL 8\nEX 83,55\nRa 152,26,2048\nAD 85,85,82\nMaav 84,168,68\nD1 1,16,46,1024\nMaav 92,172,68\n"
"Ra 156,26,3072\nAD 85,85,83\nCV 77,82,83\nRa 160,24,8192\nMaav 100,176,68\ns_sub_u32 s90, s90, 1\nMaav 108,180,68\nRa 164,24,9216\nWL 6\nRa 168,24,10240\nMaav 116,184,68\n"
"D1 1,16,46,2048\nMaav 124,188,68\nRa 172,24,11264\n.SW275:\nv_mov_b32 v4, 0\nv_mov_b32 v5, 0\nv_mov_b32 v6, 0\nv_mov_b32 v7, 0\nv_mov_b32 v8, 0\nv_mov_b32 v9, 0\nv_mov_b32 v10, 0\n"
"v_mov_b32 v11, 0\nv_mov_b32 v12, 0\nv_mov_b32 v13, 0\nv_mov_b32 v14, 0\nv_mov_b32 v15, 0\nv_mov_b32 v16, 0\nv_mov_b32 v17, 0\nv_mov_b32 v18, 0\nv_mov_b32 v19, 0\n"
"s_add_u32 s99, s98, 1\ns_mov_b32 s94, s96\ns_add_u32 s95, s96, 65536\ns_cmp_lt_u32 s99, s92\ns_cbranch_scc0 .E323\nv_cmpx_gt_i32 vcc, 19, v3\nv_mov_b32 v15, s97\n"
"v_cmpx_gt_i32 vcc, 18, v3\nv_mov_b32 v14, s97\nv_cmpx_gt_i32 vcc, 17, v3\nv_mov_b32 v13, s97\nv_cmpx_gt_i32 vcc, 16, v3\nv_mov_b32 v12, s97\nv_cmpx_gt_i32 vcc, 3, v3\n"
"v_mov_b32 v7, s97\nv_cmpx_gt_i32 vcc, 2, v3\nv_mov_b32 v6, s97\nv_cmpx_gt_i32 vcc, 1, v3\nv_mov_b32 v5, s97\nv_cmpx_gt_i32 vcc, 0, v3\nv_mov_b32 v4, s97\ns_mov_b64 exec, -1\n"
"s_nop 2\n.E323:\ns_nop 0\nCvaa 32,128,192,4\nRa 176,26,8192\nMvaa 32,132,200\nMvaa 32,136,208\nRa 180,26,9216\nMvaa 32,140,216\nWL 6\nCvaa 36,140,220,8\nRa 184,26,10240\n"
"Mvaa 36,136,212\nD1 1,16,46,3072\ns_add_u32 m0, s89, 32768\nMvaa 36,132,204\nRa 188,26,11264\nMvaa 36,128,196\nMvaa 36,144,228\nRa 128,2,16384\nMvaa 36,148,236\n"
"s_add_u32 s46, s86, s88\nEX 80,60\nMvaa 36,152,244\nRa 132,2,17408\nMvaa 36,156,252\nWL 6\nEX 81,61\nMvaa 32,156,248\nRa 136,2,18432\nAD 85,85,80\nMvaa 32,152,240\nD1 1,16,46,0\n"
"Mvaa 32,148,232\nRa 140,2,19456\nMvaa 32,144,224\nAD 85,85,81\nCV 78,80,81\nCvaa 40,160,192,12\nRa 144,2,20480\nMvaa 40,164,200\nEX 82,62\nMvaa 40,168,208\nRa 148,2,21504\n"
"Mvaa 40,172,216\nWL 6\nEX 83,63\nCvaa 44,172,220,16\nRa 152,2,22528\nAD 85,85,82\nMvaa 44,168,212\nD1 1,16,46,1024\nMvaa 44,164,204\nRa 156,2,23552\nMvaa 44,160,196\nAD 85,85,83\n"
"CV 79,82,83\nMvaa 44,176,228\nRa 160,2,24576\nMvaa 44,180,236\ns_add_u32 s88, s88, 32768\nEX 80,32\nMvaa 44,184,244\nRa 164,2,25600\nv_cmp_lt_f32 vcc, s63, v85\nMvaa 44,188,252\n"
"WL 8\nEX 81,33\nMvaa 40,188,248\nRa 168,2,26624\ns_cbranch_vccnz .T328\n.T329:\nMvaa 40,184,240\nD1 1,16,46,2048\nMvaa 40,180,232\nRa 172,2,27648\nMvaa 40,176,224\nAD 84,80,81\n"
"CV 64,80,81\nRa 176,2,28672\nMaav 4,128,76\nEX 82,34\nMaav 12,132,76\nRa 180,2,29696\ns_waitcnt vmcnt(12) lgkmcnt(8)\nEX 83,35\nRa 184,2,30720\nAD 84,84,82\nMaav 20,136,76\n"
"D1 1,16,46,3072\ns_add_u32 m0, s89, 49152\nMaav 28,140,76\nRa 188,2,31744\ns_barrier\nAR 98,0\nAR 99,1\nCV 100,98,99\nAD 84,84,83\nCV 65,82,83\nAR 98,2\nAR 99,3\nCV 101,98,99\n"
"Ra 128,24,32768\nAR 98,8\nAR 99,9\nCV 102,98,99\nAR 98,10\nMaav 36,144,76\ns_add_u32 s46, s87, s88\nEX 80,36\nAR 99,11\nCV 103,98,99\nMaav 44,148,76\nRa 132,24,33792\n"
"s_add_u32 s5, s66, 0\nSv 100,1,16,5,0\nAR 98,4\nAR 99,5\nWL 8\nEX 81,37\nCV 104,98,99\nAR 98,6\nRa 136,24,34816\nAD 87,87,85\nAR 99,7\nCV 105,98,99\nAR 98,12\nMaav 52,152,76\n"
"D1 1,16,46,0\nv_mov_b32 v96, v86\nv_mov_b32 v97, v87\nAR 99,13\nCV 106,98,99\nMaav 60,156,76\nRa 140,24,35840\nv_permlane32_swap_b32 v86, v96\nAR 98,14\nAR 99,15\nCV 107,98,99\n"
"AD 85,80,81\nCV 68,80,81\ns_add_u32 s5, s66, 0\nSv 104,1,16,5,1024\nAR 98,16\nRa 144,26,32768\nv_permlane32_swap_b32 v87, v97\nAD 86,86,96\nAR 99,17\nCV 108,98,99\nMaav 68,160,76\n"
"s_cmp_eq_u32 s99, 0\nEX 82,38\nMaav 76,164,76\nRa 148,26,33792\ns_cbranch_scc0 .E325\nv_cmpx_gt_i32 vcc, 19, v0\nv_mov_b32 v15, s97\nv_cmpx_gt_i32 vcc, 18, v0\nv_mov_b32 v14, s97\n"
"v_cmpx_gt_i32 vcc, 17, v0\nv_mov_b32 v13, s97\nv_cmpx_gt_i32 vcc, 16, v0\nv_mov_b32 v12, s97\nv_cmpx_gt_i32 vcc, 3, v0\nv_mov_b32 v7, s97\nv_cmpx_gt_i32 vcc, 2, v0\n"
"v_mov_b32 v6, s97\nv_cmpx_gt_i32 vcc, 1, v0\nv_mov_b32 v5, s97\nv_cmpx_gt_i32 vcc, 0, v0\nv_mov_b32 v4, s97\ns_mov_b64 exec, -1\ns_nop 2\n.E325:\nWL 8\nEX 83,39\nAR 98,18\n"
"AR 99,19\nRa 152,26,34816\nAD 85,85,82\nAD 87,87,97\nCV 109,98,99\nAR 98,24\nMaav 84,168,76\nD1 1,16,46,1024\nv_mov_b32 v96, v86\nAR 99,25\nCV 110,98,99\nAR 98,26\nMaav 92,172,76\n"
"Ra 156,26,35840\nv_mov_b32 v97, v87\nAR 99,27\nCV 111,98,99\nAD 85,85,83\nCV 69,82,83\nv_permlane16_swap_b32 v86, v96\ns_add_u32 s5, s66, 0\nSv 108,1,16,5,2048\nRa 160,24,40960\n"
"v_permlane16_swap_b32 v87, v97\nAR 98,20\nAR 99,21\nCV 100,98,99\nMaav 100,176,76\ns_xor_b32 s89, s89, 0x10000\nEX 80,40\nAR 98,22\nAR 99,23\nMaav 108,180,76\nRa 164,24,41984\n"
"AD 86,86,96\nAD 87,87,97\nCV 101,98,99\nAR 98,28\nWL 6\nEX 81,41\nAR 99,29\nCV 102,98,99\nRa 168,24,43008\nAD 84,84,80\nAR 98,30\nAR 99,31\nCV 103,98,99\nMaav 116,184,76\n"
"D1 1,16,46,2048\nv_cmp_gt_u32 vcc, 0x100, v1\nv_mov_b32 v94, s15\nv_mov_b32 v95, s15\nv_cndmask_b32 v92, v89, v88, vcc\nv_cndmask_b32 v93, v87, v86, vcc\nMaav 124,188,76\n"
"Ra 172,24,44032\nv_mov_b32 v86, 0\ns_add_u32 s5, s66, 0\nSv 100,1,16,5,3072\nAR 98,32\nAD 84,84,81\nCV 66,80,81\nAR 99,33\nCV 104,98,99\nAR 98,34\nCvaa 48,128,192,4\n"
"Ra 176,26,40960\nv_mov_b32 v87, 0\nv_mov_b32 v88, 0\nAR 99,35\nCV 105,98,99\nMvaa 48,132,200\ns_cmp_eq_u32 s99, 0\nEX 82,42\nMvaa 48,136,208\nRa 180,26,41984\n"
"s_cselect_b32 s86, s94, s86\nAR 98,40\nAR 99,41\nCV 106,98,99\nMvaa 48,140,216\nWL 6\nEX 83,43\nAR 98,42\nAR 99,43\nCvaa 52,140,220,8\nRa 184,26,43008\nAD 84,84,82\n"
"v_mov_b32 v89, 0\nCV 107,98,99\nMvaa 52,136,212\nD1 1,16,46,3072\ns_add_u32 m0, s89, 0\ns_add_u32 s5, s66, 4096\nSv 104,1,16,5,0\nAR 98,36\nMvaa 52,132,204\nRa 188,26,44032\n"
"AR 99,37\nCV 108,98,99\nAR 98,38\nAR 99,39\nMvaa 52,128,196\nAD 84,84,83\nCV 67,82,83\nCV 109,98,99\nAR 98,44\nAR 99,45\nMvaa 52,144,228\nRa 128,2,49152\nCV 110,98,99\nAR 98,46\n"
"AR 99,47\nCV 111,98,99\nMvaa 52,148,236\ns_add_u32 s46, s86, s88\nEX 80,44\ns_add_u32 s5, s66, 4096\nSv 108,1,16,5,1024\nMvaa 52,152,244\nRa 132,2,50176\n"
"v_xor_b32 v24, 0x10000, v24\nAR 98,48\nAR 99,49\nCV 100,98,99\nMvaa 52,156,252\nWL 6\nEX 81,45\nAR 98,50\nAR 99,51\nMvaa 48,156,248\nRa 136,2,51200\nAD 85,85,80\nCV 101,98,99\n"
"AR 98,56\nAR 99,57\nMvaa 48,152,240\nD1 1,16,46,0\nCV 102,98,99\nAR 98,58\nAR 99,59\nCV 103,98,99\nMvaa 48,148,232\nRa 140,2,52224\ns_add_u32 s5, s66, 4096\nSv 100,1,16,5,2048\n"
"AR 98,52\nAR 99,53\nMvaa 48,144,224\nAD 85,85,81\nCV 70,80,81\nCV 104,98,99\nAR 98,54\nAR 99,55\nCvaa 56,160,192,12\nRa 144,2,53248\nCV 105,98,99\nAR 98,60\nAR 99,61\n"
"CV 106,98,99\nMvaa 56,164,200\nEX 82,46\nAR 98,62\nAR 99,63\nCV 107,98,99\nMvaa 56,168,208\nRa 148,2,54272\nv_xor_b32 v26, 0x10000, v26\ns_add_u32 s5, s66, 4096\n"
"Sv 104,1,16,5,3072\nAR 98,64\nMvaa 56,172,216\nWL 6\nEX 83,47\nAR 99,65\nCV 108,98,99\nCvaa 60,172,220,16\nRa 152,2,55296\nAD 85,85,82\nAR 98,66\nAR 99,67\nCV 109,98,99\n"
"Mvaa 60,168,212\nD1 1,16,46,1024\nAR 98,72\nAR 99,73\nCV 110,98,99\nAR 98,74\nMvaa 60,164,204\nRa 156,2,56320\nAR 99,75\nCV 111,98,99\ns_add_u32 s5, s66, 16384\nSv 108,1,16,5,0\n"
"Mvaa 60,160,196\nAD 85,85,83\nCV 71,82,83\nAR 98,68\nAR 99,69\nCV 100,98,99\nMvaa 60,176,228\nRa 160,2,57344\nv_max_f32 v90, v84, v85\nAR 98,70\nAR 99,71\nCV 101,98,99\n"
"Mvaa 60,180,236\ns_add_u32 s88, s88, 32768\nEX 80,48\nAR 98,76\nAR 99,77\nMvaa 60,184,244\nRa 164,2,58368\nv_cmp_lt_f32 vcc, s63, v90\nMvaa 60,188,252\nWL 8\nEX 81,49\n"
"Mvaa 56,188,248\nRa 168,2,59392\ns_cbranch_vccnz .T330\n.T331:\nv_mov_b32 v20, v84\nv_mov_b32 v21, v85\nv_mov_b32 v22, v20\nv_mov_b32 v23, v21\nMvaa 56,184,240\nD1 1,16,46,2048\n"
"v_permlane32_swap_b32 v20, v22\nv_permlane32_swap_b32 v21, v23\nAD 20,20,22\nAD 21,21,23\nv_mov_b32 v22, v20\nv_mov_b32 v23, v21\nMvaa 56,180,232\nRa 172,2,60416\n"
"v_permlane16_swap_b32 v20, v22\nv_permlane16_swap_b32 v21, v23\nAD 20,20,22\nAD 21,21,23\nv_min_f32 v90, v20, v21\nv_cmp_gt_f32 vcc, s64, v90\ns_nop 0\ns_cbranch_vccnz .T332\n"
".T333:\nAD 86,86,84\nMvaa 56,176,224\nAD 84,80,81\nCV 72,80,81\nCV 102,98,99\nAR 98,78\nAR 99,79\nZvav 128,128,64\nRa 176,2,61440\nCV 103,98,99\ns_add_u32 s5, s66, 16384\n"
"Sv 100,1,16,5,1024\nAR 98,80\nZvav 132,128,68\nEX 82,50\nAR 99,81\nCV 104,98,99\nAR 98,82\nZvav 140,132,68\nRa 180,2,62464\nAR 99,83\nCV 105,98,99\nAR 98,88\nAR 99,89\n"
"Zvav 136,132,64\ns_waitcnt vmcnt(21) lgkmcnt(8)\nEX 83,51\nCV 106,98,99\nAR 98,90\nZvav 144,136,64\nRa 184,2,63488\nAD 84,84,82\nAR 99,91\nCV 107,98,99\nZvav 148,136,68\n"
"D1 1,16,46,3072\ns_add_u32 m0, s89, 16384\ns_add_u32 s5, s66, 16384\nSv 104,1,16,5,2048\nAR 98,84\nZvav 156,140,68\nRa 188,2,64512\ns_barrier\nAR 99,85\nCV 108,98,99\nAR 98,86\n"
"Zvav 152,140,64\nAD 84,84,83\nCV 73,82,83\n.W580:\ns_cbranch_execz .W584\nAR 99,87\nCV 109,98,99\nAR 98,92\nZvav 160,144,64\nRa 128,24,0\nAR 99,93\nCV 110,98,99\nAR 98,94\n"
"AR 99,95\nZvav 164,144,68\ns_add_u32 s46, s87, s88\nEX 80,52\nCV 111,98,99\nZvav 172,148,68\nRa 132,24,1024\nv_xor_b32 v2, 0x10000, v2\ns_add_u32 s5, s66, 16384\n"
"Sv 108,1,16,5,3072\nAR 98,96\nZvav 168,148,64\nWL 8\nEX 81,53\nAR 99,97\nCV 100,98,99\nZvav 176,152,64\nRa 136,24,2048\nAD 87,87,85\nAR 98,98\nAR 99,99\nCV 101,98,99\n"
"Zvav 180,152,68\nD1 1,16,46,0\nAR 98,104\nAR 99,105\nCV 102,98,99\nAR 98,106\nZvav 188,156,68\nRa 140,24,3072\nAR 99,107\nCV 103,98,99\ns_add_u32 s5, s66, 20480\nSv 100,1,16,5,0\n"
"Zvav 184,156,64\nAD 85,80,81\nCV 76,80,81\nAR 98,100\nAR 99,101\nCV 104,98,99\nZvav 192,160,64\nRa 144,26,0\nAR 98,102\nAR 99,103\nCV 105,98,99\nAR 98,108\nZvav 196,160,68\n"
"s_cmp_lt_u32 s99, s92\nEX 82,54\nZvav 204,164,68\nRa 148,26,1024\ns_cbranch_scc0 .E326\nv_cmpx_gt_i32 vcc, 19, v3\nv_mov_b32 v19, s97\nv_cmpx_gt_i32 vcc, 18, v3\n"
"v_mov_b32 v18, s97\nv_cmpx_gt_i32 vcc, 17, v3\nv_mov_b32 v17, s97\nv_cmpx_gt_i32 vcc, 16, v3\nv_mov_b32 v16, s97\nv_cmpx_gt_i32 vcc, 3, v3\nv_mov_b32 v11, s97\n"
"v_cmpx_gt_i32 vcc, 2, v3\nv_mov_b32 v10, s97\nv_cmpx_gt_i32 vcc, 1, v3\nv_mov_b32 v9, s97\nv_cmpx_gt_i32 vcc, 0, v3\nv_mov_b32 v8, s97\ns_mov_b64 exec, -1\ns_nop 2\n.E326:\n"
"s_nop 0\nZvav 200,164,64\nWL 8\nEX 83,55\nAR 99,109\nCV 106,98,99\nZvav 208,168,64\nRa 152,26,2048\nAD 85,85,82\nAR 98,110\nAR 99,111\nCV 107,98,99\nZvav 212,168,68\n"
"D1 1,16,46,1024\ns_add_u32 s5, s66, 20480\nSv 104,1,16,5,1024\nAR 98,112\nAR 99,113\nZvav 220,172,68\nRa 156,26,3072\nCV 108,98,99\nAR 98,114\nAR 99,115\nCV 109,98,99\n"
"Zvav 216,172,64\nAD 85,85,83\nCV 77,82,83\nAR 98,120\nAR 99,121\nCV 110,98,99\nZvav 224,176,64\nRa 160,24,8192\nAR 98,122\nAR 99,123\nCV 111,98,99\nZvav 228,176,68\n"
"s_sub_u32 s90, s90, 1\nEX 80,56\nZvav 236,180,68\nRa 164,24,9216\ns_cbranch_scc0 .E324\ns_swappc_b64 s[80:81], s[8:9]\n.E324:\ns_add_u32 s5, s66, 20480\nSv 108,1,16,5,2048\n"
"Zvav 232,180,64\nWL 6\nEX 81,57\nAR 98,116\nAR 99,117\nZvav 240,184,64\nRa 168,24,10240\nAD 84,84,80\nCV 100,98,99\nAR 98,118\nAR 99,119\nZvav 244,184,68\nD1 1,16,46,2048\n"
"CV 101,98,99\nAR 98,124\nAR 99,125\nCV 102,98,99\nZvav 252,188,68\nRa 172,24,11264\nAR 98,126\nAR 99,127\nCV 103,98,99\nZvav 248,188,64\nAD 84,84,81\nCV 74,80,81\n"
"s_add_u32 s5, s66, 20480\nSv 100,1,16,5,3072\ns_sub_u32 s99, s99, 1\ns_cbranch_scc1 .LE276\n.p2align 6\n.A334:\nCvaa 32,128,192,4\nRa 176,26,8192\nMvaa 32,132,200\nEX 82,58\n"
"Mvaa 32,136,208\nRa 180,26,9216\nMvaa 32,140,216\nWL 6\nEX 83,59\nCvaa 36,140,220,8\nRa 184,26,10240\nAD 84,84,82\nMvaa 36,136,212\nD1 1,16,46,3072\ns_add_u32 m0, s89, 32768\n"
"Mvaa 36,132,204\nRa 188,26,11264\nMvaa 36,128,196\nAD 84,84,83\nCV 75,82,83\nMvaa 36,144,228\nRa 128,2,16384\nMvaa 36,148,236\ns_add_u32 s46, s86, s88\nEX 80,60\nMvaa 36,152,244\n"
"Ra 132,2,17408\nMvaa 36,156,252\nWL 6\nEX 81,61\nMvaa 32,156,248\nRa 136,2,18432\nAD 85,85,80\nMvaa 32,152,240\nD1 1,16,46,0\nMvaa 32,148,232\nRa 140,2,19456\nSv 112,1,16,49,0\n"
"Mvaa 32,144,224\nAD 85,85,81\nCV 78,80,81\nCvaa 40,160,192,12\nRa 144,2,20480\nMvaa 40,164,200\nEX 82,62\nMvaa 40,168,208\nRa 148,2,21504\nMvaa 40,172,216\nWL 6\nEX 83,63\n"
"Cvaa 44,172,220,16\nRa 152,2,22528\nAD 85,85,82\nMvaa 44,168,212\nD1 1,16,46,1024\nMvaa 44,164,204\nRa 156,2,23552\nMvaa 44,160,196\nAD 85,85,83\nCV 79,82,83\nMvaa 44,176,228\n"
"Ra 160,2,24576\nv_max_f32 v90, v84, v85\nMvaa 44,180,236\ns_add_u32 s88, s88, 32768\nEX 80,32\nMvaa 44,184,244\nRa 164,2,25600\nv_cmp_lt_f32 vcc, s63, v90\nMvaa 44,188,252\nWL 8\n"
"EX 81,33\nMvaa 40,188,248\nRa 168,2,26624\ns_cbranch_vccnz .T344\n.T345:\nMvaa 40,184,240\nD1 1,16,46,2048\nMvaa 40,180,232\nRa 172,2,27648\nAD 86,86,84\nMvaa 40,176,224\n"
"AD 84,80,81\nCV 64,80,81\nMvav 128,128,72\nRa 176,2,28672\nMvav 132,128,76\ns_cmp_eq_u32 s99, s98\nEX 82,34\nMvav 140,132,76\nRa 180,2,29696\ns_cbranch_scc1 .E342\n.E343:\n"
"Mvav 136,132,72\ns_waitcnt vmcnt(13) lgkmcnt(8)\nEX 83,35\nMvav 144,136,72\nRa 184,2,30720\nAD 84,84,82\nMvav 148,136,76\nD1 1,16,46,3072\ns_add_u32 m0, s89, 49152\n"
"Mvav 156,140,76\nRa 188,2,31744\ns_barrier\nMvav 152,140,72\nAD 84,84,83\nCV 65,82,83\nMvav 160,144,72\nRa 128,24,32768\nMvav 164,144,76\ns_add_u32 s46, s87, s88\nEX 80,36\n"
"Mvav 172,148,76\nRa 132,24,33792\nMvav 168,148,72\nWL 8\nEX 81,37\nMvav 176,152,72\nRa 136,24,34816\nAD 87,87,85\nMvav 180,152,76\nD1 1,16,46,0\nMvav 188,156,76\nRa 140,24,35840\n"
"Mvav 184,156,72\nAD 85,80,81\nCV 68,80,81\nMvav 192,160,72\nRa 144,26,32768\nMvav 196,160,76\ns_cmp_eq_u32 s99, 0\nEX 82,38\nMvav 204,164,76\nRa 148,26,33792\n"
"s_cbranch_scc1 .E336\n.E337:\nMvav 200,164,72\nWL 8\nEX 83,39\nMvav 208,168,72\nRa 152,26,34816\nAD 85,85,82\nMvav 212,168,76\nD1 1,16,46,1024\nMvav 220,172,76\nRa 156,26,35840\n"
"Mvav 216,172,72\nAD 85,85,83\nCV 69,82,83\nMvav 224,176,72\nRa 160,24,40960\nMvav 228,176,76\ns_xor_b32 s89, s89, 0x10000\nEX 80,40\nMvav 236,180,76\nRa 164,24,41984\n"
"Mvav 232,180,72\nWL 6\nEX 81,41\nMvav 240,184,72\nRa 168,24,43008\nAD 84,84,80\nMvav 244,184,76\nD1 1,16,46,2048\nMvav 252,188,76\nRa 172,24,44032\nMvav 248,188,72\nAD 84,84,81\n"
"CV 66,80,81\nCvaa 48,128,192,4\nRa 176,26,40960\nMvaa 48,132,200\ns_cmp_eq_u32 s99, 0\nEX 82,42\nMvaa 48,136,208\nRa 180,26,41984\ns_cselect_b32 s86, s94, s86\nMvaa 48,140,216\n"
"WL 6\nEX 83,43\nCvaa 52,140,220,8\nRa 184,26,43008\nAD 84,84,82\nMvaa 52,136,212\nD1 1,16,46,3072\ns_add_u32 m0, s89, 0\nMvaa 52,132,204\nRa 188,26,44032\nMvaa 52,128,196\n"
"AD 84,84,83\nCV 67,82,83\nMvaa 52,144,228\nRa 128,2,49152\nMvaa 52,148,236\ns_add_u32 s46, s86, s88\nEX 80,44\nMvaa 52,152,244\nRa 132,2,50176\nv_xor_b32 v24, 0x10000, v24\n"
"Mvaa 52,156,252\nWL 6\nEX 81,45\nMvaa 48,156,248\nRa 136,2,51200\nAD 85,85,80\nMvaa 48,152,240\nD1 1,16,46,0\nMvaa 48,148,232\nRa 140,2,52224\nSv 112,1,16,49,1024\n"
"Mvaa 48,144,224\nAD 85,85,81\nCV 70,80,81\nCvaa 56,160,192,12\nRa 144,2,53248\nMvaa 56,164,200\ns_add_u32 s49, s49, 131072\nEX 82,46\nMvaa 56,168,208\nRa 148,2,54272\n"
"v_xor_b32 v26, 0x10000, v26\nMvaa 56,172,216\nWL 6\nEX 83,47\nCvaa 60,172,220,16\nRa 152,2,55296\nAD 85,85,82\nMvaa 60,168,212\nD1 1,16,46,1024\nMvaa 60,164,204\nRa 156,2,56320\n"
"Mvaa 60,160,196\nAD 85,85,83\nCV 71,82,83\nMvaa 60,176,228\nRa 160,2,57344\nv_max_f32 v90, v84, v85\nMvaa 60,180,236\ns_add_u32 s88, s88, 32768\nEX 80,48\nMvaa 60,184,244\n"
"Ra 164,2,58368\nv_cmp_lt_f32 vcc, s63, v90\nMvaa 60,188,252\nWL 8\nEX 81,49\nMvaa 56,188,248\nRa 168,2,59392\ns_cbranch_vccnz .T346\n.T347:\nMvaa 56,184,240\nD1 1,16,46,2048\n"
"Mvaa 56,180,232\nRa 172,2,60416\nAD 86,86,84\nMvaa 56,176,224\nAD 84,80,81\nCV 72,80,81\nMvav 128,128,64\nRa 176,2,61440\nMvav 132,128,68\nEX 82,50\nMvav 140,132,68\n"
"Ra 180,2,62464\nMvav 136,132,64\ns_waitcnt vmcnt(13) lgkmcnt(8)\nEX 83,51\nMvav 144,136,64\nRa 184,2,63488\nAD 84,84,82\nMvav 148,136,68\nD1 1,16,46,3072\n"
"s_add_u32 m0, s89, 16384\nMvav 156,140,68\nRa 188,2,64512\ns_barrier\nMvav 152,140,64\nAD 84,84,83\nCV 73,82,83\nMvav 160,144,64\nRa 128,24,0\nMvav 164,144,68\n"
"s_add_u32 s46, s87, s88\nEX 80,52\nMvav 172,148,68\nRa 132,24,1024\nv_xor_b32 v2, 0x10000, v2\nMvav 168,148,64\nWL 8\nEX 81,53\nMvav 176,152,64\nRa 136,24,2048\nAD 87,87,85\n"
"Mvav 180,152,68\nD1 1,16,46,0\nMvav 188,156,68\nRa 140,24,3072\nMvav 184,156,64\nAD 85,80,81\nCV 76,80,81\nMvav 192,160,64\nRa 144,26,0\nMvav 196,160,68\ns_cmp_lt_u32 s99, s92\n"
"EX 82,54\nMvav 204,164,68\nRa 148,26,1024\ns_cbranch_scc1 .E338\n.E339:\nMvav 200,164,64\nWL 8\nEX 83,55\nMvav 208,168,64\nRa 152,26,2048\nAD 85,85,82\nMvav 212,168,68\n"
"D1 1,16,46,1024\nMvav 220,172,68\nRa 156,26,3072\nMvav 216,172,64\nAD 85,85,83\nCV 77,82,83\nMvav 224,176,64\nRa 160,24,8192\nMvav 228,176,68\ns_sub_u32 s90, s90, 1\nEX 80,56\n"
"Mvav 236,180,68\nRa 164,24,9216\ns_cbranch_scc1 .E340\n.E341:\nMvav 232,180,64\nWL 6\nEX 81,57\nMvav 240,184,64\nRa 168,24,10240\nAD 84,84,80\nMvav 244,184,68\nD1 1,16,46,2048\n"
"s_sub_u32 s99, s99, 1\nMvav 252,188,68\nRa 172,24,11264\nMvav 248,188,64\nAD 84,84,81\nCV 74,80,81\ns_cbranch_scc0 .A334\ns_branch .A348\n.E336:\nv_cmpx_gt_i32 vcc, 19, v0\n"
"v_mov_b32 v15, s97\nv_cmpx_gt_i32 vcc, 18, v0\nv_mov_b32 v14, s97\nv_cmpx_gt_i32 vcc, 17, v0\nv_mov_b32 v13, s97\nv_cmpx_gt_i32 vcc, 16, v0\nv_mov_b32 v12, s97\n"
"v_cmpx_gt_i32 vcc, 3, v0\nv_mov_b32 v7, s97\nv_cmpx_gt_i32 vcc, 2, v0\nv_mov_b32 v6, s97\nv_cmpx_gt_i32 vcc, 1, v0\nv_mov_b32 v5, s97\nv_cmpx_gt_i32 vcc, 0, v0\nv_mov_b32 v4, s97\n"
"s_mov_b64 exec, -1\ns_nop 2\ns_branch .E337\n.E338:\ns_cmp_eq_u32 s99, 0\ns_cbranch_scc1 .K335\nv_cmpx_gt_i32 vcc, 19, v3\nv_mov_b32 v15, s97\nv_cmpx_gt_i32 vcc, 18, v3\n"
"v_mov_b32 v14, s97\nv_cmpx_gt_i32 vcc, 17, v3\nv_mov_b32 v13, s97\nv_cmpx_gt_i32 vcc, 16, v3\nv_mov_b32 v12, s97\nv_cmpx_gt_i32 vcc, 3, v3\nv_mov_b32 v7, s97\n"
"v_cmpx_gt_i32 vcc, 2, v3\nv_mov_b32 v6, s97\nv_cmpx_gt_i32 vcc, 1, v3\nv_mov_b32 v5, s97\nv_cmpx_gt_i32 vcc, 0, v3\nv_mov_b32 v4, s97\ns_mov_b64 exec, -1\ns_nop 2\ns_branch .E339\n"
".K335:\nv_cmpx_gt_i32 vcc, 19, v3\nv_mov_b32 v19, s97\nv_cmpx_gt_i32 vcc, 18, v3\nv_mov_b32 v18, s97\nv_cmpx_gt_i32 vcc, 17, v3\nv_mov_b32 v17, s97\nv_cmpx_gt_i32 vcc, 16, v3\n"
"v_mov_b32 v16, s97\nv_cmpx_gt_i32 vcc, 3, v3\nv_mov_b32 v11, s97\nv_cmpx_gt_i32 vcc, 2, v3\nv_mov_b32 v10, s97\nv_cmpx_gt_i32 vcc, 1, v3\nv_mov_b32 v9, s97\n"
"v_cmpx_gt_i32 vcc, 0, v3\nv_mov_b32 v8, s97\ns_mov_b64 exec, -1\ns_nop 2\ns_branch .E339\n.E340:\ns_swappc_b64 s[80:81], s[8:9]\ns_branch .E341\n.E342:\nv_mov_b32 v91, 0x40000000\n"
"v_cmp_gt_u32 vcc, 0x200, v1\nWV 5\ns_nop 0\nv_cndmask_b32 v91, v91, v1, vcc\nSv 92,91,16,65,0\ns_branch .E343\n.A348:\n.LE276:\nRa 176,26,8192\nEX 82,58\nRa 180,26,9216\nWL 6\n"
"EX 83,59\nCvaa 36,140,220,8\nRa 184,26,10240\nAD 84,84,82\nMvaa 36,136,212\nD1 1,16,46,3072\ns_add_u32 m0, s89, 32768\nMvaa 36,132,204\nRa 188,26,11264\nMvaa 36,128,196\n"
"AD 84,84,83\nCV 75,82,83\nMvaa 36,144,228\nRa 128,2,16384\nMvaa 36,148,236\ns_add_u32 s46, s86, s88\nEX 80,60\nMvaa 36,152,244\nRa 132,2,17408\nMvaa 36,156,252\nEX 81,61\n"
"Ra 136,2,18432\nAD 85,85,80\nD1 1,16,46,0\nRa 140,2,19456\nAD 85,85,81\nCV 78,80,81\nRa 144,2,20480\ns_mov_b32 s87, s95\nEX 82,62\nRa 148,2,21504\nWL 6\nEX 83,63\n"
"Cvaa 44,172,220,16\nRa 152,2,22528\nAD 85,85,82\nMvaa 44,168,212\nD1 1,16,46,1024\nMvaa 44,164,204\nRa 156,2,23552\nMvaa 44,160,196\nAD 85,85,83\nCV 79,82,83\nMvaa 44,176,228\n"
"Ra 160,2,24576\nv_max_f32 v90, v84, v85\nMvaa 44,180,236\ns_add_u32 s88, s88, 32768\nMvaa 44,184,244\nRa 164,2,25600\nv_cmp_lt_f32 vcc, s63, v90\nMvaa 44,188,252\nWL 8\n"
"Ra 168,2,26624\ns_cbranch_vccnz .T351\n.T352:\nD1 1,16,46,2048\nRa 172,2,27648\nAD 86,86,84\nMvav 128,128,72\n.W581:\ns_cbranch_execz .WB274\nRa 176,2,28672\nMvav 132,128,76\n"
"s_cmp_eq_u32 s13, 0\nMvav 140,132,76\nRa 180,2,29696\ns_cbranch_scc0 .E349\nv_mov_b32 v91, 0x40000000\nv_cmp_gt_u32 vcc, 0x200, v1\nWV 4\ns_nop 0\nv_cndmask_b32 v91, v91, v1, vcc\n"
"Sv 92,91,16,65,0\n.E349:\nMvav 136,132,72\ns_waitcnt vmcnt(12) lgkmcnt(8)\nMvav 144,136,72\nRa 184,2,30720\nMvav 148,136,76\nD1 1,16,46,3072\ns_add_u32 m0, s89, 49152\n"
"Mvav 156,140,76\nRa 188,2,31744\ns_barrier\nMvav 152,140,72\nMvav 160,144,72\nRa 128,24,32768\nMvav 164,144,76\ns_add_u32 s46, s87, s88\nEX 80,36\nMvav 172,148,76\n"
"Ra 132,24,33792\nMvav 168,148,72\nWL 8\nEX 81,37\nMvav 176,152,72\nRa 136,24,34816\nAD 87,87,85\nMvav 180,152,76\nD1 1,16,46,0\nMvav 188,156,76\nRa 140,24,35840\nMvav 184,156,72\n"
"AD 85,80,81\nCV 68,80,81\nMvav 192,160,72\nRa 144,26,32768\nMvav 196,160,76\ns_nop 0\nEX 82,38\nMvav 204,164,76\nRa 148,26,33792\nv_cmpx_gt_i32 vcc, 19, v0\nv_mov_b32 v19, s97\n"
"v_cmpx_gt_i32 vcc, 18, v0\nv_mov_b32 v18, s97\nv_cmpx_gt_i32 vcc, 17, v0\nv_mov_b32 v17, s97\nv_cmpx_gt_i32 vcc, 16, v0\nv_mov_b32 v16, s97\nv_cmpx_gt_i32 vcc, 3, v0\n"
"v_mov_b32 v11, s97\nv_cmpx_gt_i32 vcc, 2, v0\nv_mov_b32 v10, s97\nv_cmpx_gt_i32 vcc, 1, v0\nv_mov_b32 v9, s97\nv_cmpx_gt_i32 vcc, 0, v0\nv_mov_b32 v8, s97\ns_mov_b64 exec, -1\n"
"s_nop 2\ns_nop 0\nMvav 200,164,72\nWL 8\nEX 83,39\nMvav 208,168,72\nRa 152,26,34816\nAD 85,85,82\nMvav 212,168,76\nD1 1,16,46,1024\nMvav 220,172,76\nRa 156,26,35840\n"
"Mvav 216,172,72\nAD 85,85,83\nCV 69,82,83\nMvav 224,176,72\nRa 160,24,40960\nMvav 228,176,76\ns_xor_b32 s89, s89, 0x10000\nMvav 236,180,76\nRa 164,24,41984\nMvav 232,180,72\n"
"WL 10\nMvav 240,184,72\nRa 168,24,43008\nMvav 244,184,76\nD1 1,16,46,2048\nMvav 252,188,76\nRa 172,24,44032\nMvav 248,188,72\nRa 176,26,40960\nRa 180,26,41984\nWL 6\n"
"Cvaa 52,140,220,8\nRa 184,26,43008\nMvaa 52,136,212\nD1 1,16,46,3072\ns_add_u32 m0, s89, 0\nMvaa 52,132,204\nRa 188,26,44032\nMvaa 52,128,196\nMvaa 52,144,228\nRa 128,2,49152\n"
"Mvaa 52,148,236\ns_add_u32 s46, s86, s88\nEX 80,44\nMvaa 52,152,244\nRa 132,2,50176\nv_xor_b32 v24, 0x10000, v24\nMvaa 52,156,252\nEX 81,45\nRa 136,2,51200\nAD 85,85,80\n"
"D1 1,16,46,0\nRa 140,2,52224\nAD 85,85,81\nCV 70,80,81\nRa 144,2,53248\nEX 82,46\nRa 148,2,54272\nv_xor_b32 v26, 0x10000, v26\nWL 6\nEX 83,47\nCvaa 60,172,220,16\nRa 152,2,55296\n"
"AD 85,85,82\nMvaa 60,168,212\nD1 1,16,46,1024\nMvaa 60,164,204\nRa 156,2,56320\nMvaa 60,160,196\nAD 85,85,83\nCV 71,82,83\nMvaa 60,176,228\nRa 160,2,57344\nMvaa 60,180,236\n"
"s_add_u32 s88, s88, 32768\nMvaa 60,184,244\nRa 164,2,58368\nv_cmp_lt_f32 vcc, s63, v85\nMvaa 60,188,252\nWL 8\nRa 168,2,59392\ns_cbranch_vccnz .T353\n.T354:\nD1 1,16,46,2048\n"
"Ra 172,2,60416\nRa 176,2,61440\nMvav 132,128,68\nMvav 140,132,68\nRa 180,2,62464\ns_waitcnt vmcnt(11) lgkmcnt(8)\nRa 184,2,63488\nMvav 148,136,68\nD1 1,16,46,3072\n"
"s_add_u32 m0, s89, 16384\nMvav 156,140,68\nRa 188,2,64512\ns_barrier\nRa 128,24,0\nMvav 164,144,68\ns_add_u32 s46, s87, s88\nEX 80,52\nMvav 172,148,68\nRa 132,24,1024\n"
"v_xor_b32 v2, 0x10000, v2\nWL 8\nEX 81,53\nRa 136,24,2048\nAD 87,87,85\nMvav 180,152,68\nD1 1,16,46,0\nMvav 188,156,68\nRa 140,24,3072\nAD 85,80,81\nCV 76,80,81\nRa 144,26,0\n"
"Mvav 196,160,68\nEX 82,54\nMvav 204,164,68\nRa 148,26,1024\nWL 8\nEX 83,55\nRa 152,26,2048\nAD 85,85,82\nMvav 212,168,68\nD1 1,16,46,1024\nMvav 220,172,68\nRa 156,26,3072\n"
"AD 85,85,83\nCV 77,82,83\nRa 160,24,8192\nMvav 228,176,68\nMvav 236,180,68\nRa 164,24,9216\nWL 10\nRa 168,24,10240\nMvav 244,184,68\nD1 1,16,46,2048\nMvav 252,188,68\n"
"Ra 172,24,11264\nD1 1,16,46,3072\ns_add_u32 m0, s89, 32768\ns_add_u32 s46, s86, s88\nRa 128,2,16384\nv_xor_b32 v3, 0x10000, v2\nv_add_u32 v0, 0x20000, v1\nEX 80,60\n"
"Ra 132,2,17408\ns_sub_u32 s94, s49, 1835008\nEX 81,61\nRa 136,2,18432\nAD 85,85,80\ns_xor_b32 s92, s94, 0x202000\nD1 1,16,46,0\ns_lshl_b32 s89, s11, 12\nRa 140,2,19456\n"
"s_lshl1_add_u32 s2, s12, 1\nAD 85,85,81\nCV 78,80,81\nRa 144,2,20480\ns_lshl3_add_u32 s2, s2, s13\nEX 82,62\nRa 148,2,21504\ns_lshl4_add_u32 s2, s2, 0xc048200\nEX 83,63\n"
"Ra 152,2,22528\nAD 85,85,82\nD1 1,16,46,1024\nRa 156,2,23552\ns_and_b32 s3, s72, 0x2000\nAD 85,85,83\nCV 79,82,83\nRa 160,2,24576\ns_cmp_eq_u32 s11, 1\ns_cselect_b32 s54, s2, s54\n"
"s_cselect_b32 s3, s3, 0\nv_cmp_lt_f32 vcc, s63, v85\nRa 164,2,25600\ns_cbranch_vccnz .T355\n.T356:\nAD 87,87,85\nWL 8\nv_mov_b32 v84, v86\nv_mov_b32 v85, v87\nRa 168,2,26624\n"
"v_permlane32_swap_b32 v86, v84\nv_permlane32_swap_b32 v87, v85\nD1 1,16,46,2048\nAD 86,86,84\nRa 172,2,27648\nAD 87,87,85\nv_mov_b32 v84, v86\nv_mov_b32 v85, v87\nRa 176,2,28672\n"
"v_permlane16_swap_b32 v86, v84\nv_permlane16_swap_b32 v87, v85\nMvav 132,128,76\nAD 86,86,84\nAD 87,87,85\nMvav 140,132,76\nRa 180,2,29696\nv_rcp_f32 v86, v86\nWL 8\n"
"v_rcp_f32 v87, v87\nRa 184,2,30720\nv_xor_b32 v25, s3, v25\nMvav 148,136,76\nD1 1,16,46,3072\nMvav 156,140,76\nRa 188,2,31744\nMU 128,128,86\nMU 129,129,86\nCV 96,128,129\n"
"MU 130,130,86\nMU 131,131,86\nRa 128,2,0\nCV 97,130,131\nMU 132,132,87\nMvav 164,144,76\nMU 133,133,87\nCV 100,132,133\nMvav 172,148,76\nRa 132,2,1024\nMU 134,134,87\n"
"MU 135,135,87\nWL 8\ns_add_u32 m0, s89, 131072\ns_add_u32 s46, s96, 1212416\nRa 136,2,2048\nCV 101,134,135\nMU 136,136,86\nMvav 180,152,76\nD1 1,16,46,0\nMU 137,137,86\n"
"Mvav 188,156,76\nRa 140,2,3072\nCV 98,136,137\nMU 138,138,86\nMU 139,139,86\nCV 99,138,139\nMU 140,140,87\nRa 144,2,4096\nMU 141,141,87\nCV 102,140,141\nMvav 196,160,76\n"
"MU 142,142,87\nMU 143,143,87\nMvav 204,164,76\nRa 148,2,5120\nCV 103,142,143\nMU 144,144,86\nWL 8\nMU 145,145,86\nCV 104,144,145\nRa 152,2,6144\nMU 146,146,86\nMU 147,147,86\n"
"Mvav 212,168,76\nD1 1,16,46,1024\nCV 105,146,147\nMvav 220,172,76\nRa 156,2,7168\nMU 148,148,87\nMU 149,149,87\nCV 108,148,149\nMU 150,150,87\nMU 151,151,87\nRa 160,2,8192\n"
"CV 109,150,151\nMU 152,152,86\nMvav 228,176,76\nMU 153,153,86\nCV 106,152,153\nMvav 236,180,76\nRa 164,2,9216\nMU 154,154,86\nMU 155,155,86\nWL 8\nCV 107,154,155\nMU 156,156,87\n"
"Ra 168,2,10240\nMU 157,157,87\nCV 110,156,157\nMvav 244,184,76\nD1 1,16,46,2048\nMU 158,158,87\nMvav 252,188,76\nRa 172,2,11264\nMU 159,159,87\nCV 111,158,159\nMU 160,160,86\n"
"MU 161,161,86\nCV 112,160,161\nZvav 128,128,96\nRa 176,2,12288\nMU 162,162,86\nMU 163,163,86\nMvav 128,132,104\nCV 113,162,163\nMU 164,164,87\nZvav 132,132,108\nRa 180,2,13312\n"
"MU 165,165,87\nCV 116,164,165\nMvav 132,128,100\ns_waitcnt vmcnt(15) lgkmcnt(8)\nMU 166,166,87\nMU 167,167,87\nZvav 140,136,100\nRa 184,2,14336\nCV 117,166,167\nMU 168,168,86\n"
"Mvav 140,140,108\nD1 1,16,46,3072\nMU 169,169,86\nCV 114,168,169\nZvav 136,140,104\nRa 188,2,15360\ns_barrier\nMU 170,170,86\nMvav 136,136,96\nMU 171,171,86\nCV 115,170,171\n"
"MU 172,172,87\nZvav 144,144,96\nRa 128,2,32768\nMU 173,173,87\nCV 118,172,173\nMvav 144,148,104\nMU 174,174,87\nMU 175,175,87\n"
"buffer_store_dword v25, off, s[16:19], s54 offset:512 sc0 sc1\nZvav 148,148,108\nRa 132,2,33792\nCV 119,174,175\nMU 176,176,86\nMvav 148,144,100\nWL 8\ns_add_u32 m0, s89, 49152\n"
"s_add_u32 s46, s96, 1179648\nZvav 156,152,100\nRa 136,2,34816\nMU 177,177,86\nCV 120,176,177\nMvav 156,156,108\nD1 1,16,46,0\nMU 178,178,86\nMU 179,179,86\nZvav 152,156,104\n"
"Ra 140,2,35840\nCV 121,178,179\nMU 180,180,87\nMvav 152,152,96\nMU 181,181,87\nCV 124,180,181\nMU 182,182,87\nZvav 64,160,96\nRa 144,2,36864\nMU 183,183,87\nCV 125,182,183\n"
"Mvav 64,164,104\nMU 184,184,86\nMU 185,185,86\nZvav 68,164,108\nRa 148,2,37888\nCV 122,184,185\nMU 186,186,86\nMvav 68,160,100\nWL 8\nMU 187,187,86\nCV 123,186,187\n"
"Zvav 76,168,100\nRa 152,2,38912\nMU 188,188,87\nMU 189,189,87\nMvav 76,172,108\nD1 1,16,46,1024\nCV 126,188,189\nMU 190,190,87\nZvav 72,172,104\nRa 156,2,39936\nMU 191,191,87\n"
"CV 127,190,191\nMvav 72,168,96\nMU 192,192,86\nMU 193,193,86\nCV 32,192,193\nMvav 128,176,112\nRa 160,2,40960\nMU 194,194,86\nMU 195,195,86\nMvav 128,180,120\nCV 33,194,195\n"
"MU 196,196,87\nMU 197,197,87\nMvav 132,180,124\nRa 164,2,41984\nCV 36,196,197\nMU 198,198,87\nMvav 132,176,116\nWL 8\nMU 199,199,87\nMvav 140,184,116\nRa 168,2,43008\n"
"CV 37,198,199\nMU 200,200,86\nMvav 140,188,124\nD1 1,16,46,2048\nMU 201,201,86\nCV 34,200,201\nMvav 136,188,120\nRa 172,2,44032\nMU 202,202,86\nMU 203,203,86\nMvav 136,184,112\n"
"CV 35,202,203\nMU 204,204,87\nMU 205,205,87\nMvav 144,128,112\nRa 176,2,45056\nCV 38,204,205\nMU 206,206,87\nMvav 144,132,120\nMU 207,207,87\nCV 39,206,207\nMvav 148,132,124\n"
"Ra 180,2,46080\nMU 208,208,86\nMU 209,209,86\nMvav 148,128,116\nWL 8\nCV 40,208,209\nMU 210,210,86\nMvav 156,136,116\nRa 184,2,47104\nMU 211,211,86\nCV 41,210,211\n"
"Mvav 156,140,124\nD1 1,16,46,3072\nMU 212,212,87\nMU 213,213,87\nMvav 152,140,120\nRa 188,2,48128\nCV 44,212,213\nMvav 152,136,112\nMU 214,214,87\nMU 215,215,87\nCV 45,214,215\n"
"Mvav 64,144,112\nRa 128,2,49152\nMU 216,216,86\nMU 217,217,86\nMvav 64,148,120\nCV 42,216,217\nMU 218,218,86\nMU 219,219,86\nMvav 68,148,124\nRa 132,2,50176\nCV 43,218,219\n"
"MU 220,220,87\nMvav 68,144,116\nWL 8\ns_add_u32 m0, s89, 147456\ns_add_u32 s46, s96, 1245184\nMvav 76,152,116\nRa 136,2,51200\nMU 221,221,87\nCV 46,220,221\nMvav 76,156,124\n"
"D1 1,16,46,0\nMU 222,222,87\nMU 223,223,87\nMvav 72,156,120\nRa 140,2,52224\nCV 47,222,223\nMvav 72,152,112\nMU 224,224,86\nMU 225,225,86\nCV 48,224,225\nMvav 128,160,32\n"
"Ra 144,2,53248\nMU 226,226,86\nMU 227,227,86\nMvav 128,164,40\nCV 49,226,227\nMU 228,228,87\nMvav 132,164,44\nRa 148,2,54272\nMU 229,229,87\nCV 52,228,229\nMvav 132,160,36\nWL 8\n"
"MU 230,230,87\nMU 231,231,87\nMvav 140,168,36\nRa 152,2,55296\nCV 53,230,231\nMU 232,232,86\nMvav 140,172,44\nD1 1,16,46,1024\nMU 233,233,86\nCV 50,232,233\nMvav 136,172,40\n"
"Ra 156,2,56320\nMU 234,234,86\nMvav 136,168,32\nMU 235,235,86\nCV 51,234,235\nMU 236,236,87\nMvav 144,176,32\nRa 160,2,57344\nMU 237,237,87\nCV 54,236,237\nMvav 144,180,40\n"
"MU 238,238,87\nMU 239,239,87\nCV 55,238,239\nMvav 148,180,44\nRa 164,2,58368\nMU 240,240,86\nMU 241,241,86\nMvav 148,176,36\nWL 8\nCV 56,240,241\nMvav 156,184,36\nRa 168,2,59392\n"
"MU 242,242,86\nMU 243,243,86\nMvav 156,188,44\nD1 1,16,46,2048\nCV 57,242,243\nMU 244,244,87\nMvav 152,188,40\nRa 172,2,60416\nMU 245,245,87\nCV 60,244,245\nMvav 152,184,32\n"
"MU 246,246,87\nMU 247,247,87\nCV 61,246,247\nMvav 64,128,32\nRa 176,2,61440\nMU 248,248,86\nMU 249,249,86\nMvav 64,132,40\nCV 58,248,249\nMU 250,250,86\nMvav 68,132,44\n"
"Ra 180,2,62464\nMU 251,251,86\nCV 59,250,251\nMvav 68,128,36\ns_waitcnt vmcnt(16) lgkmcnt(8)\nMU 252,252,87\nMU 253,253,87\nMvav 76,136,36\nRa 184,2,63488\nCV 62,252,253\n"
"MU 254,254,87\nMvav 76,140,44\nD1 1,16,46,3072\nMU 255,255,87\nCV 63,254,255\nMvav 72,140,40\nRa 188,2,64512\ns_barrier\nMvav 72,136,32\nMvav 128,144,48\n.W582:\n"
"s_cbranch_execz .WB274\nRa 128,3,0\nMvav 128,148,56\nMvav 132,148,60\nRa 132,3,1024\nMvav 132,144,52\nWL 8\nMvav 140,152,52\nRa 136,3,2048\nMvav 140,156,60\nMvav 136,156,56\n"
"Ra 140,3,3072\nMvav 136,152,48\nMvav 144,160,48\nRa 144,3,4096\nMvav 144,164,56\nMvav 148,164,60\nRa 148,3,5120\nCV 132,132,133\nMvav 148,160,52\nWL 8\nCV 133,134,135\n"
"Mvav 156,168,52\nRa 152,3,6144\nCV 134,140,141\nMvav 156,172,60\nCV 135,142,143\ns_add_u32 s5, s94, 0\nMvav 152,172,56\nRa 156,3,7168\nSv 132,1,16,5,1024\nMvav 152,168,48\n"
"CV 128,128,129\nCV 129,130,131\nMvav 64,176,48\nRa 160,3,8192\nCV 130,136,137\nMvav 64,180,56\nbuffer_load_dword v27, off, s[16:19], s60 offset:512 sc0 sc1\nCV 131,138,139\n"
"Mvav 68,180,60\nRa 164,3,9216\nSv 128,1,16,5,0\nMvav 68,176,52\nWL 6\nCV 148,148,149\nMvav 76,184,52\nRa 168,3,10240\nCV 149,150,151\nMvav 76,188,60\nCV 150,156,157\n"
"CV 151,158,159\nMvav 72,188,56\nRa 172,3,11264\ns_add_u32 s5, s94, 131072\nMvav 72,184,48\nSv 148,1,16,5,1024\nCV 144,144,145\nZvav 160,128,96\nRa 176,3,12288\nCV 145,146,147\n"
"Mvav 160,132,104\nCV 146,152,153\nCV 147,154,155\nMvav 160,136,112\nRa 180,3,13312\nSv 144,1,16,5,0\nMvav 160,140,120\nWL 6\nCV 68,68,69\nZvav 164,140,124\nRa 184,3,14336\n"
"CV 69,70,71\nMvav 164,136,116\nCV 70,76,77\nCV 71,78,79\nMvav 164,132,108\nRa 188,3,15360\ns_add_u32 s5, s94, 262144\nMvav 164,128,100\nSv 68,1,16,5,1024\nCV 64,64,65\n"
"Mvav 164,144,36\nRa 128,3,16384\nCV 65,66,67\nMvav 164,148,44\nCV 66,72,73\nCV 67,74,75\nMvav 164,152,52\nRa 132,3,17408\nSv 64,1,16,5,0\nMvav 164,156,60\nWL 6\nMvav 160,156,56\n"
"Ra 136,3,18432\nMvav 160,152,48\nMvav 160,148,40\nRa 140,3,19456\nMvav 160,144,32\nZvav 168,160,96\nRa 144,3,20480\nMvav 168,164,104\nMvav 168,168,112\nRa 148,3,21504\n"
"Mvav 168,172,120\nWL 6\nZvav 172,172,124\nRa 152,3,22528\nMvav 172,168,116\nMvav 172,164,108\nRa 156,3,23552\nMvav 172,160,100\nMvav 172,176,36\nRa 160,3,24576\nMvav 172,180,44\n"
"Mvav 172,184,52\nRa 164,3,25600\nMvav 172,188,60\nWL 6\nMvav 168,188,56\nRa 168,3,26624\nMvav 168,184,48\nMvav 168,180,40\nRa 172,3,27648\nMvav 168,176,32\nZvav 176,128,96\n"
"Ra 176,3,28672\nCV 164,164,165\nMvav 176,132,104\nCV 165,166,167\nCV 166,172,173\nMvav 176,136,112\nRa 180,3,29696\nCV 167,174,175\nMvav 176,140,120\n"
"s_waitcnt vmcnt(11) lgkmcnt(6)\ns_add_u32 s5, s94, 393216\nZvav 180,140,124\nRa 184,3,30720\nSv 164,1,16,5,1024\nMvav 180,136,116\nCV 160,160,161\nCV 161,162,163\n"
"Mvav 180,132,108\nRa 188,3,31744\ns_barrier\nMvav 180,128,100\nCV 162,168,169\nCV 163,170,171\nMvav 180,144,36\nRa 128,3,32768\nSv 160,1,16,5,0\nMvav 180,148,44\nMvav 180,152,52\n"
"Ra 132,3,33792\nMvav 180,156,60\nWL 6\ns_add_u32 m0, s89, 81920\ns_add_u32 s46, s96, 1277952\nMvav 176,156,56\nRa 136,3,34816\nMvav 176,152,48\nD1 1,16,46,0\nMvav 176,148,40\n"
"Ra 140,3,35840\nMvav 176,144,32\nZvav 184,160,96\nRa 144,3,36864\nMvav 184,164,104\nMvav 184,168,112\nRa 148,3,37888\nMvav 184,172,120\nWL 6\nZvav 188,172,124\nRa 152,3,38912\n"
"Mvav 188,168,116\nD1 1,16,46,1024\nMvav 188,164,108\nRa 156,3,39936\nMvav 188,160,100\nMvav 188,176,36\nRa 160,3,40960\nWV 9\nv_cmp_ne_u32 vcc, s15, v27\ns_nop 0\n"
"s_cbranch_vccnz .S381\n.S382:\nMvav 188,180,44\ns_add_u32 s4, s92, 0\nMvav 188,184,52\nRa 164,3,41984\nMvav 188,188,60\nWL 6\n"
"buffer_load_dwordx4 v[208:211], v1, s[16:19], s4 offen offset:0 sc0 sc1\nMvav 184,188,56\nRa 168,3,43008\nMvav 184,184,48\nD1 1,16,46,2048\nMvav 184,180,40\nRa 172,3,44032\n"
"Mvav 184,176,32\nbuffer_load_dwordx4 v[212:215], v1, s[16:19], s4 offen offset:1024 sc0 sc1\nZvav 192,128,96\nRa 176,3,45056\nv_lshrrev_b32 v88, 8, v1\nMvav 192,132,104\n"
"v_lshlrev_b32 v89, 5, v88\nCV 180,180,181\nMvav 192,136,112\nRa 180,3,46080\nCV 181,182,183\nMvav 192,140,120\nWL 6\nCV 182,188,189\nZvav 196,140,124\nRa 184,3,47104\n"
"v_bfe_u32 v90, v1, 4, 4\nMvav 196,136,116\nD1 1,16,46,3072\nCV 183,190,191\nMvav 196,132,108\nRa 188,3,48128\ns_add_u32 s5, s94, 524288\nMvav 196,128,100\n"
"v_mul_u32_u24 v91, 896, v90\nSv 180,1,16,5,1024\nMvav 196,144,36\nRa 128,3,49152\nv_lshl_add_u32 v91, v88, 4, v91\nMvav 196,148,44\nv_add_u32 v90, s21, v90\nCV 176,176,177\n"
"Mvav 196,152,52\nRa 132,3,50176\nCV 177,178,179\nMvav 196,156,60\nWL 6\ns_add_u32 m0, s89, 65536\ns_add_u32 s46, s96, 1310720\nMvav 192,156,56\nRa 136,3,51200\n"
"v_lshlrev_b32 v88, 3, v90\nMvav 192,152,48\nD1 1,16,46,0\nCV 178,184,185\nMvav 192,148,40\nRa 140,3,52224\nCV 179,186,187\nMvav 192,144,32\nv_add_u32 v94, 64, v90\n"
"Sv 176,1,16,5,0\nZvav 200,160,96\nRa 144,3,53248\ns_mov_b32 s63, 0xc040000\nMvav 200,164,104\nv_lshrrev_b32 v92, 8, v1\nMvav 200,168,112\nRa 148,3,54272\nMvav 200,172,120\nWL 6\n"
"Zvav 204,172,124\nRa 152,3,55296\nv_bfe_u32 v93, v1, 6, 2\nMvav 204,168,116\nD1 1,16,46,1024\nv_cmp_eq_u32 vcc, v92, v93\nv_and_b32 v92, 16, v1\nv_mov_b32 v93, 0x3f80\n"
"v_cndmask_b32 v93, 0, v93, vcc\nMvav 204,164,108\nRa 156,3,56320\nMvav 204,160,100\nv_lshlrev_b32 v93, v92, v93\nMvav 204,176,36\nRa 160,3,57344\nv_and_b32 v92, 32, v1\n"
"Mvav 204,180,44\ns_add_u32 s4, s92, 131072\nv_cmp_eq_u32 vcc, 0, v92\nv_mov_b32 v4, 0\nv_mov_b32 v5, 0\nv_cndmask_b32 v6, 0, v93, vcc\nMvav 204,184,52\nRa 164,3,58368\n"
"Mvav 204,188,60\nWL 6\nbuffer_load_dwordx4 v[216:219], v1, s[16:19], s4 offen offset:0 sc0 sc1\nMvav 200,188,56\nRa 168,3,59392\nv_sub_u32 v7, v93, v6\nMvav 200,184,48\n"
"D1 1,16,46,2048\nMvav 200,180,40\nRa 172,3,60416\nMvav 200,176,32\nbuffer_load_dwordx4 v[220:223], v1, s[16:19], s4 offen offset:1024 sc0 sc1\nZvav 160,128,96\nRa 176,3,61440\n"
"v_mov_b32 v8, 0\nMvav 160,132,104\nv_mov_b32 v9, 0\nCV 196,196,197\nMvav 160,136,112\nRa 180,3,62464\nCV 197,198,199\nMvav 160,140,120\nWL 6\nCV 198,204,205\nZvav 164,140,124\n"
"Ra 184,3,63488\nCV 199,206,207\nMvav 164,136,116\nD1 1,16,46,3072\ns_add_u32 s5, s94, 655360\nMvav 164,132,108\nRa 188,3,64512\ns_barrier\nMvav 164,128,100\nSv 196,1,16,5,1024\n"
"CV 192,192,193\nMvav 164,144,36\nRa 128,0,0\nCV 193,194,195\nMvav 164,148,44\nCV 194,200,201\nCV 195,202,203\nMvav 164,152,52\nRa 132,0,1024\nSv 192,1,16,5,0\nMvav 164,156,60\n"
"WL 6\ns_add_u32 m0, s89, 98304\ns_add_u32 s46, s96, 1343488\nMvav 160,156,56\nRa 136,0,2048\nMvav 160,152,48\nD1 1,16,46,0\nMvav 160,148,40\nRa 140,0,3072\nMvav 160,144,32\n"
"Zvav 168,160,96\nRa 144,0,4096\nWV 12\nv_max_u32 v21, v211, v215\nv_cmp_eq_u32 vcc, -1, v21\ns_nop 0\ns_cbranch_vccnz .S357\n.S358:\nMvav 168,164,104\n"
"v_lshlrev_b32 v176, 16, v208\nMvav 168,168,112\nRa 148,0,5120\nMvav 168,172,120\nWL 6\nv_and_b32 v177, 0xffff0000, v208\nZvav 172,172,124\nRa 152,0,6144\nMvav 172,168,116\n"
"D1 1,16,46,1024\nMvav 172,164,108\nRa 156,0,7168\nv_lshlrev_b32 v178, 16, v209\nMvav 172,160,100\nv_and_b32 v179, 0xffff0000, v209\nMvav 172,176,36\nRa 160,0,8192\n"
"v_lshlrev_b32 v180, 16, v212\nMvav 172,180,44\nv_and_b32 v181, 0xffff0000, v212\ns_add_u32 s4, s92, 262144\nMvav 172,184,52\nRa 164,0,9216\nMvav 172,188,60\nWL 6\n"
"v_lshlrev_b32 v182, 16, v213\nbuffer_load_dwordx4 v[224:227], v1, s[16:19], s4 offen offset:0 sc0 sc1\nMvav 168,188,56\nRa 168,0,10240\nMvav 168,184,48\nD1 1,16,46,2048\n"
"Mvav 168,180,40\nRa 172,0,11264\nv_and_b32 v183, 0xffff0000, v213\nMvav 168,176,32\nv_lshlrev_b32 v184, 16, v210\n"
"buffer_load_dwordx4 v[228:231], v1, s[16:19], s4 offen offset:1024 sc0 sc1\nMvav 176,128,96\nRa 176,0,12288\nv_and_b32 v185, 0xffff0000, v210\nMvav 176,132,104\n"
"v_lshlrev_b32 v186, 16, v211\nCV 164,164,165\nMvav 176,136,112\nRa 180,0,13312\nCV 165,166,167\nMvav 176,140,120\nWL 6\nCV 166,172,173\nMvav 180,140,124\nRa 184,0,14336\n"
"v_and_b32 v187, 0xffff0000, v211\nMvav 180,136,116\nD1 1,16,46,3072\nCV 167,174,175\nMvav 180,132,108\nRa 188,0,15360\ns_add_u32 s5, s94, 786432\nMvav 180,128,100\n"
"v_lshlrev_b32 v188, 16, v214\nSv 164,1,16,5,1024\nMvav 180,144,36\nRa 128,0,16384\nv_and_b32 v189, 0xffff0000, v214\nMvav 180,148,44\nv_lshlrev_b32 v190, 16, v215\nCV 160,160,161\n"
"Mvav 180,152,52\nRa 132,0,17408\nCV 161,162,163\nMvav 180,156,60\nWL 6\ns_add_u32 m0, s89, 114688\ns_add_u32 s46, s96, 1376256\nMvav 176,156,56\nRa 136,0,18432\n"
"v_and_b32 v191, 0xffff0000, v215\nMvav 176,152,48\nD1 1,16,46,0\nCV 162,168,169\nMvav 176,148,40\nRa 140,0,19456\nCV 163,170,171\nMvav 176,144,32\nSv 160,1,16,5,0\n"
"Mvav 184,160,96\nRa 144,0,20480\nWV 12\nv_max_u32 v21, v219, v223\nv_cmp_eq_u32 vcc, -1, v21\ns_nop 0\ns_cbranch_vccnz .S360\n.S361:\nMvav 184,164,104\n"
"v_lshlrev_b32 v192, 16, v216\nMvav 184,168,112\nRa 148,0,21504\nMvav 184,172,120\nWL 6\nv_and_b32 v193, 0xffff0000, v216\nMvav 188,172,124\nRa 152,0,22528\nMvav 188,168,116\n"
"D1 1,16,46,1024\nMvav 188,164,108\nRa 156,0,23552\nv_lshlrev_b32 v194, 16, v217\nMvav 188,160,100\nv_and_b32 v195, 0xffff0000, v217\nMvav 188,176,36\nRa 160,0,24576\n"
"v_lshlrev_b32 v196, 16, v220\nMvav 188,180,44\nv_and_b32 v197, 0xffff0000, v220\ns_add_u32 s4, s92, 393216\nMvav 188,184,52\nRa 164,0,25600\nMvav 188,188,60\nWL 6\n"
"v_lshlrev_b32 v198, 16, v221\nbuffer_load_dwordx4 v[208:211], v1, s[16:19], s4 offen offset:0 sc0 sc1\nMvav 184,188,56\nRa 168,0,26624\nMvav 184,184,48\nD1 1,16,46,2048\n"
"Mvav 184,180,40\nRa 172,0,27648\nv_and_b32 v199, 0xffff0000, v221\nMvav 184,176,32\nv_lshlrev_b32 v200, 16, v218\n"
"buffer_load_dwordx4 v[212:215], v1, s[16:19], s4 offen offset:1024 sc0 sc1\nMvav 192,128,96\nRa 176,0,28672\nv_and_b32 v201, 0xffff0000, v218\nMvav 192,132,104\n"
"v_lshlrev_b32 v202, 16, v219\nCV 180,180,181\nMvav 192,136,112\nRa 180,0,29696\nCV 181,182,183\nMvav 192,140,120\ns_waitcnt vmcnt(15) lgkmcnt(6)\nCV 182,188,189\nMvav 196,140,124\n"
"Ra 184,0,30720\nv_and_b32 v203, 0xffff0000, v219\nMvav 196,136,116\nD1 1,16,46,3072\nCV 183,190,191\nMvav 196,132,108\nRa 188,0,31744\ns_barrier\nMvav 196,128,100\n"
"v_lshlrev_b32 v204, 16, v222\ns_add_u32 s5, s94, 917504\nMvav 196,144,36\nRa 128,2,16384\nv_and_b32 v205, 0xffff0000, v222\nMvav 196,148,44\nv_lshlrev_b32 v206, 16, v223\n"
"Sv 180,1,16,5,1024\nMvav 196,152,52\nRa 132,2,17408\nCV 176,176,177\nMvav 196,156,60\nWL 6\ns_add_u32 m0, s89, 0\ns_add_u32 s46, s96, 1409024\nMvav 192,156,56\nRa 136,2,18432\n"
"v_and_b32 v207, 0xffff0000, v223\nMvav 192,152,48\nD1 1,16,46,0\nCV 177,178,179\nMvav 192,148,40\nRa 140,2,19456\nCV 178,184,185\nMvav 192,144,32\nCV 179,186,187\nSv 176,1,16,5,0\n"
"Mvav 200,160,96\nRa 144,2,20480\nWV 12\nv_max_u32 v21, v227, v231\nv_cmp_eq_u32 vcc, -1, v21\ns_nop 0\ns_cbranch_vccnz .S363\n.S364:\nMvav 200,164,104\n"
"v_lshlrev_b32 v160, 16, v224\nMvav 200,168,112\nRa 148,2,21504\nMvav 200,172,120\nWL 6\nv_and_b32 v161, 0xffff0000, v224\nMvav 204,172,124\nRa 152,2,22528\nMvav 204,168,116\n"
"D1 1,16,46,1024\nMvav 204,164,108\nRa 156,2,23552\nv_lshlrev_b32 v162, 16, v225\nMvav 204,160,100\nv_and_b32 v163, 0xffff0000, v225\nMvav 204,176,36\nRa 160,2,24576\n"
"v_lshlrev_b32 v164, 16, v228\nMvav 204,180,44\nv_and_b32 v165, 0xffff0000, v228\ns_add_u32 s4, s92, 524288\nMvav 204,184,52\nRa 164,2,25600\n.W583:\ns_cbranch_execz .WB274\n"
"Mvav 204,188,60\nWL 6\nv_lshlrev_b32 v166, 16, v229\nbuffer_load_dwordx4 v[216:219], v1, s[16:19], s4 offen offset:0 sc0 sc1\nMvav 200,188,56\nRa 168,2,26624\nMvav 200,184,48\n"
"D1 1,16,46,2048\nMvav 200,180,40\nRa 172,2,27648\nv_and_b32 v167, 0xffff0000, v229\nMvav 200,176,32\nv_lshlrev_b32 v168, 16, v226\n"
"buffer_load_dwordx4 v[220:223], v1, s[16:19], s4 offen offset:1024 sc0 sc1\nMvav 160,128,96\nRa 176,2,28672\nv_and_b32 v169, 0xffff0000, v226\nMvav 160,132,104\n"
"v_lshlrev_b32 v170, 16, v227\nCV 196,196,197\nMvav 160,136,112\nRa 180,2,29696\nCV 197,198,199\nMvav 160,140,120\nWL 6\nCV 198,204,205\nMvav 164,140,124\nRa 184,2,30720\n"
"v_and_b32 v24, 0x70, v1\nMvav 164,136,116\nD1 1,16,46,3072\nCV 199,206,207\nMvav 164,132,108\nRa 188,2,31744\nv_and_b32 v171, 0xffff0000, v227\nMvav 164,128,100\n"
"v_lshlrev_b32 v172, 16, v230\ns_add_u32 s5, s94, 1048576\nMvav 164,144,36\nRa 128,2,0\nv_lshlrev_b32 v24, 5, v24\nMvav 164,148,44\nv_and_b32 v173, 0xffff0000, v230\n"
"Sv 196,1,16,5,1024\nMvav 164,152,52\nRa 132,2,1024\nCV 192,192,193\nMvav 164,156,60\nWL 6\nv_lshlrev_b32 v174, 16, v231\nMvav 160,156,56\nRa 136,2,2048\n"
"buffer_load_dword v27, v24, s[16:19], s41 offen sc0 sc1\nMvav 160,152,48\nv_and_b32 v175, 0xffff0000, v231\nCV 193,194,195\nMvav 160,148,40\nRa 140,2,3072\nCV 194,200,201\n"
"Mvav 160,144,32\nCV 195,202,203\nSv 192,1,16,5,0\nMvav 168,160,96\nRa 144,2,4096\nWV 12\nv_max_u32 v21, v211, v215\nv_cmp_eq_u32 vcc, -1, v21\ns_nop 0\ns_cbranch_vccnz .S366\n"
".S367:\nMvav 168,164,104\nv_lshlrev_b32 v176, 16, v208\nMvav 168,168,112\nRa 148,2,5120\nMvav 168,172,120\nWL 6\nMvav 172,172,124\nRa 152,2,6144\nv_and_b32 v177, 0xffff0000, v208\n"
"Mvav 172,168,116\nv_lshlrev_b32 v178, 16, v209\nMvav 172,164,108\nRa 156,2,7168\nMvav 172,160,100\nv_and_b32 v179, 0xffff0000, v209\nMvav 172,176,36\nRa 160,2,8192\n"
"v_lshlrev_b32 v180, 16, v212\nMvav 172,180,44\nv_and_b32 v181, 0xffff0000, v212\ns_add_u32 s4, s92, 655360\nMvav 172,184,52\nRa 164,2,9216\nMvav 172,188,60\nWL 6\n"
"buffer_load_dwordx4 v[224:227], v1, s[16:19], s4 offen offset:0 sc0 sc1\nMvav 168,188,56\nRa 168,2,10240\nv_lshlrev_b32 v182, 16, v213\nMvav 168,184,48\n"
"v_and_b32 v183, 0xffff0000, v213\nMvav 168,180,40\nRa 172,2,11264\nMvav 168,176,32\nv_lshlrev_b32 v184, 16, v210\n"
"buffer_load_dwordx4 v[228:231], v1, s[16:19], s4 offen offset:1024 sc0 sc1\nMvav 176,128,96\nRa 176,2,12288\nv_and_b32 v185, 0xffff0000, v210\nMvav 176,132,104\n"
"v_lshlrev_b32 v186, 16, v211\nCV 164,164,165\nMvav 176,136,112\nRa 180,2,13312\nCV 165,166,167\nMvav 176,140,120\ns_waitcnt vmcnt(13) lgkmcnt(6)\nCV 166,172,173\nMvav 180,140,124\n"
"Ra 184,2,14336\nCV 167,174,175\nMvav 180,136,116\nv_and_b32 v187, 0xffff0000, v211\ns_add_u32 s5, s94, 1179648\nMvav 180,132,108\nRa 188,2,15360\ns_barrier\nMvav 180,128,100\n"
"v_lshlrev_b32 v188, 16, v214\nSv 164,1,16,5,1024\nMvav 180,144,36\nRa 128,2,32768\nv_and_b32 v189, 0xffff0000, v214\nMvav 180,148,44\nv_lshlrev_b32 v190, 16, v215\nCV 160,160,161\n"
"Mvav 180,152,52\nRa 132,2,33792\nCV 161,162,163\nMvav 180,156,60\nWL 6\nCV 162,168,169\nMvav 176,156,56\nRa 136,2,34816\nCV 163,170,171\nMvav 176,152,48\n"
"v_and_b32 v191, 0xffff0000, v215\nSv 160,1,16,5,0\nMvav 176,148,40\nRa 140,2,35840\nMvav 176,144,32\nMvav 184,160,96\nRa 144,2,36864\nWV 8\nv_max_u32 v21, v219, v223\n"
"v_cmp_eq_u32 vcc, -1, v21\ns_nop 0\ns_cbranch_vccnz .S369\n.S370:\nMvav 184,164,104\nv_lshlrev_b32 v192, 16, v216\nMvav 184,168,112\nRa 148,2,37888\nMvav 184,172,120\nWL 6\n"
"Mvav 188,172,124\nRa 152,2,38912\nv_and_b32 v193, 0xffff0000, v216\nMvav 188,168,116\nv_lshlrev_b32 v194, 16, v217\nMvav 188,164,108\nRa 156,2,39936\nMvav 188,160,100\n"
"v_and_b32 v195, 0xffff0000, v217\nMvav 188,176,36\nRa 160,2,40960\nv_lshlrev_b32 v196, 16, v220\nMvav 188,180,44\nv_and_b32 v197, 0xffff0000, v220\ns_add_u32 s4, s92, 786432\n"
"Mvav 188,184,52\nRa 164,2,41984\nMvav 188,188,60\nWL 6\nbuffer_load_dwordx4 v[208:211], v1, s[16:19], s4 offen offset:0 sc0 sc1\nMvav 184,188,56\nRa 168,2,43008\n"
"v_lshlrev_b32 v198, 16, v221\nMvav 184,184,48\nv_and_b32 v199, 0xffff0000, v221\nMvav 184,180,40\nRa 172,2,44032\nMvav 184,176,32\nv_lshlrev_b32 v200, 16, v218\n"
"buffer_load_dwordx4 v[212:215], v1, s[16:19], s4 offen offset:1024 sc0 sc1\nMvav 192,128,96\nRa 176,2,45056\nv_and_b32 v201, 0xffff0000, v218\nMvav 192,132,104\n"
"v_lshlrev_b32 v202, 16, v219\nCV 180,180,181\nMvav 192,136,112\nRa 180,2,46080\nCV 181,182,183\nMvav 192,140,120\nWL 6\nCV 182,188,189\nMvav 196,140,124\nRa 184,2,47104\n"
"CV 183,190,191\nMvav 196,136,116\nv_and_b32 v203, 0xffff0000, v219\ns_add_u32 s5, s94, 1310720\nMvav 196,132,108\nRa 188,2,48128\nSv 180,1,16,5,1024\nMvav 196,128,100\n"
"v_lshlrev_b32 v204, 16, v222\nCV 176,176,177\nMvav 196,144,36\nRa 128,2,49152\nv_and_b32 v205, 0xffff0000, v222\nMvav 196,148,44\nv_lshlrev_b32 v206, 16, v223\nCV 177,178,179\n"
"Mvav 196,152,52\nRa 132,2,50176\nCV 178,184,185\nMvav 196,156,60\nWL 6\nCV 179,186,187\nMvav 192,156,56\nRa 136,2,51200\nSv 176,1,16,5,0\nMvav 192,152,48\n"
"v_and_b32 v207, 0xffff0000, v223\nMvav 192,148,40\nRa 140,2,52224\nMvav 192,144,32\nMvav 200,160,96\nRa 144,2,53248\nWV 6\nv_max_u32 v21, v227, v231\nv_cmp_eq_u32 vcc, -1, v21\n"
"s_nop 0\ns_cbranch_vccnz .S372\n.S373:\nMvav 200,164,104\nv_lshlrev_b32 v160, 16, v224\nv_xor_b32 v27, s15, v27\nv_and_b32 v26, 0xffffdfff, v27\nv_cmp_ne_u32 s[64:65], 0, v27\n"
"v_cmp_ne_u32 vcc, 0, v26\ns_nop 0\ns_cbranch_vccnz .S384\n.S385:\nMvav 200,168,112\nRa 148,2,54272\ns_bitcmp1_b32 s64, 0\ns_cselect_b32 s2, 0x2000, 0\nMvav 200,172,120\nWL 6\n"
"Mvav 204,172,124\nRa 152,2,55296\nv_and_b32 v161, 0xffff0000, v224\nMvav 204,168,116\nv_lshlrev_b32 v162, 16, v225\nMvav 204,164,108\nRa 156,2,56320\ns_xor_b32 s2, s2, s20\n"
"Mvav 204,160,100\nv_and_b32 v163, 0xffff0000, v225\nMvav 204,176,36\nRa 160,2,57344\nv_lshlrev_b32 v164, 16, v228\nMvav 204,180,44\nv_and_b32 v165, 0xffff0000, v228\n"
"Mvav 204,184,52\nRa 164,2,58368\nMvav 204,188,60\nWL 6\ns_add_u32 s24, s2, 0x0\nMvav 200,188,56\nRa 168,2,59392\nv_lshlrev_b32 v166, 16, v229\nMvav 200,184,48\n"
"v_and_b32 v167, 0xffff0000, v229\ns_bitcmp1_b32 s64, 1\ns_cselect_b32 s2, 0x2000, 0\nMvav 200,180,40\nRa 172,2,60416\nMvav 200,176,32\nv_lshlrev_b32 v168, 16, v226\n"
"Mvav 160,128,96\nRa 176,2,61440\nv_and_b32 v169, 0xffff0000, v226\nMvav 160,132,104\nv_lshlrev_b32 v170, 16, v227\nCV 196,196,197\nMvav 160,136,112\nRa 180,2,62464\n"
"CV 197,198,199\nMvav 160,140,120\nWL 6\nCV 198,204,205\nMvav 164,140,124\nRa 184,2,63488\nCV 199,206,207\nMvav 164,136,116\nv_and_b32 v171, 0xffff0000, v227\n"
"s_add_u32 s5, s94, 1441792\nMvav 164,132,108\nRa 188,2,64512\ns_barrier\nMvav 164,128,100\nv_lshlrev_b32 v172, 16, v230\nSv 196,1,16,5,1024\nMvav 164,144,36\nRa 128,3,0\n"
"v_and_b32 v173, 0xffff0000, v230\nMvav 164,148,44\nv_lshlrev_b32 v174, 16, v231\nCV 192,192,193\nMvav 164,152,52\nRa 132,3,1024\nCV 193,194,195\nMvav 164,156,60\nWL 6\n"
"s_xor_b32 s2, s2, s20\nMvav 160,156,56\nRa 136,3,2048\nCV 194,200,201\nMvav 160,152,48\nv_and_b32 v175, 0xffff0000, v231\nCV 195,202,203\nMvav 160,148,40\nRa 140,3,3072\n"
"Sv 192,1,16,5,0\nMvav 160,144,32\ns_add_u32 s25, s2, 0x1800000\ns_bitcmp1_b32 s64, 2\ns_cselect_b32 s2, 0x2000, 0\nMvav 168,160,96\nRa 144,3,4096\nWV 4\nv_max_u32 v21, v211, v215\n"
"v_cmp_eq_u32 vcc, -1, v21\ns_nop 0\ns_cbranch_vccnz .S375\n.S376:\nMvav 168,164,104\nv_lshlrev_b32 v176, 16, v208\nMvav 168,168,112\nRa 148,3,5120\nMvav 168,172,120\nWL 6\n"
"Mvav 172,172,124\nRa 152,3,6144\nv_and_b32 v177, 0xffff0000, v208\nMvav 172,168,116\nv_lshlrev_b32 v178, 16, v209\nMvav 172,164,108\nRa 156,3,7168\ns_xor_b32 s2, s2, s20\n"
"Mvav 172,160,100\nv_and_b32 v179, 0xffff0000, v209\nMvav 172,176,36\nRa 160,3,8192\nv_lshlrev_b32 v180, 16, v212\nMvav 172,180,44\nv_and_b32 v181, 0xffff0000, v212\n"
"Mvav 172,184,52\nRa 164,3,9216\nMvav 172,188,60\nWL 6\ns_add_u32 s26, s2, 0x3000000\nMvav 168,188,56\nRa 168,3,10240\nv_lshlrev_b32 v182, 16, v213\nMvav 168,184,48\n"
"v_and_b32 v183, 0xffff0000, v213\ns_bitcmp1_b32 s64, 3\ns_cselect_b32 s2, 0x2000, 0\nMvav 168,180,40\nRa 172,3,11264\nMvav 168,176,32\nv_lshlrev_b32 v184, 16, v210\n"
"Mvav 176,128,96\nRa 176,3,12288\ns_xor_b32 s2, s2, s20\nMvav 176,132,104\nv_and_b32 v185, 0xffff0000, v210\nCV 164,164,165\nMvav 176,136,112\nRa 180,3,13312\nCV 165,166,167\n"
"Mvav 176,140,120\nWL 6\nCV 166,172,173\nMvav 180,140,124\nRa 184,3,14336\nCV 167,174,175\nMvav 180,136,116\nv_lshlrev_b32 v186, 16, v211\ns_add_u32 s5, s94, 1572864\n"
"Mvav 180,132,108\nRa 188,3,15360\ns_add_u32 s27, s2, 0x4800000\nMvav 180,128,100\nv_and_b32 v187, 0xffff0000, v211\ns_bitcmp1_b32 s64, 4\ns_cselect_b32 s2, 0x2000, 0\n"
"Mvav 180,144,36\nv_lshlrev_b32 v188, 16, v214\nSv 164,1,16,5,1024\nMvav 180,148,44\nv_and_b32 v189, 0xffff0000, v214\nCV 160,160,161\nMvav 180,152,52\n"
"v_lshlrev_b32 v190, 16, v215\nCV 161,162,163\nMvav 180,156,60\nWL 4\ns_xor_b32 s2, s2, s20\nMvav 176,156,56\nv_and_b32 v191, 0xffff0000, v215\nCV 162,168,169\nMvav 176,152,48\n"
"s_add_u32 s32, s2, 0x6000000\ns_bitcmp1_b32 s64, 5\ns_cselect_b32 s2, 0x2000, 0\nMvav 176,148,40\ns_xor_b32 s2, s2, s20\nCV 163,170,171\nMvav 176,144,32\n"
"s_add_u32 s33, s2, 0x7800000\ns_bitcmp1_b32 s64, 6\ns_cselect_b32 s2, 0x2000, 0\nMvav 184,160,96\ns_xor_b32 s2, s2, s20\ns_add_u32 s34, s2, 0x9000000\nMvav 184,164,104\n"
"s_bitcmp1_b32 s64, 7\ns_cselect_b32 s2, 0x2000, 0\nMvav 184,168,112\ns_xor_b32 s2, s2, s20\nSv 160,1,16,5,0\nMvav 184,172,120\nWL 0\nMvav 188,172,124\n"
"s_add_u32 s35, s2, 0xa800000\nMvav 188,168,116\nMvav 188,164,108\nMvav 188,160,100\nMvav 188,176,36\nMvav 188,180,44\nMvav 188,184,52\nMvav 188,188,60\nMvav 184,188,56\n"
"Mvav 184,184,48\nMvav 184,180,40\nMvav 184,176,32\nCV 180,180,181\nCV 181,182,183\ns_nop 1\nCV 182,188,189\nCV 183,190,191\ns_add_u32 s5, s94, 1703936\nSv 180,1,16,5,1024\n"
"CV 176,176,177\nCV 177,178,179\nCV 178,184,185\nCV 179,186,187\nSv 176,1,16,5,0\ns_cmp_eq_u32 s13, 7\ns_cbranch_scc1 .Z387\n"
"buffer_load_dwordx4 v[12:15], v91, s[28:31], s22 offen offset:0\nbuffer_load_dwordx4 v[16:19], v91, s[28:31], s23 offen offset:0\n"
"buffer_load_dwordx4 v[64:67], v89, s[16:19], s40 offen offset:0 sc0 sc1\nbuffer_load_dwordx4 v[68:71], v89, s[16:19], s40 offen offset:16 sc0 sc1\n"
"buffer_load_dwordx4 v[72:75], v89, s[16:19], s40 offen offset:1792 sc0 sc1\nbuffer_load_dwordx4 v[76:79], v89, s[16:19], s40 offen offset:1808 sc0 sc1\n"
"buffer_load_dwordx2 v[80:81], v88, s[16:19], s63 offen offset:0 sc0 sc1\nbuffer_load_dwordx2 v[82:83], v88, s[16:19], s63 offen offset:512 sc0 sc1\n"
"buffer_load_dwordx4 v[96:99], v1, s[16:19], s24 offen offset:0 sc0 sc1\nbuffer_load_dwordx4 v[100:103], v1, s[16:19], s24 offen offset:1024 sc0 sc1\n"
"buffer_load_dwordx4 v[104:107], v1, s[16:19], s25 offen offset:0 sc0 sc1\nbuffer_load_dwordx4 v[108:111], v1, s[16:19], s25 offen offset:1024 sc0 sc1\n"
"buffer_load_dwordx4 v[112:115], v1, s[16:19], s26 offen offset:0 sc0 sc1\nbuffer_load_dwordx4 v[116:119], v1, s[16:19], s26 offen offset:1024 sc0 sc1\n"
"buffer_load_dwordx4 v[120:123], v1, s[16:19], s27 offen offset:0 sc0 sc1\nbuffer_load_dwordx4 v[124:127], v1, s[16:19], s27 offen offset:1024 sc0 sc1\n"
"buffer_load_dwordx4 v[32:35], v1, s[16:19], s32 offen offset:0 sc0 sc1\nbuffer_load_dwordx4 v[36:39], v1, s[16:19], s32 offen offset:1024 sc0 sc1\n"
"buffer_load_dwordx4 v[40:43], v1, s[16:19], s33 offen offset:0 sc0 sc1\nbuffer_load_dwordx4 v[44:47], v1, s[16:19], s33 offen offset:1024 sc0 sc1\n"
"buffer_load_dwordx4 v[48:51], v1, s[16:19], s34 offen offset:0 sc0 sc1\nbuffer_load_dwordx4 v[52:55], v1, s[16:19], s34 offen offset:1024 sc0 sc1\n"
"buffer_load_dwordx4 v[56:59], v1, s[16:19], s35 offen offset:0 sc0 sc1\nbuffer_load_dwordx4 v[60:63], v1, s[16:19], s35 offen offset:1024 sc0 sc1\nWV 16\nv_xor_b32 v66, s15, v66\n"
"v_xor_b32 v70, s15, v70\nv_xor_b32 v74, s15, v74\nv_xor_b32 v78, s15, v78\nv_xor_b32 v81, s15, v81\nv_xor_b32 v83, s15, v83\nv_or3_b32 v92, v66, v70, v74\n"
"v_or3_b32 v92, v92, v78, v81\nv_or_b32 v92, v92, v83\nv_cmp_ne_u32 vcc, 0, v92\ns_nop 0\ns_cbranch_vccnz .S378\n.S379:\nv_and_b32 v92, 0x7ff, v67\nv_lshrrev_b32 v93, 16, v67\n"
"v_cmp_eq_u32 s[44:45], v92, v90\nv_cmp_eq_u32 s[46:47], v93, v90\nv_cmp_eq_u32 s[48:49], v92, v94\nv_cmp_eq_u32 s[50:51], v93, v94\nv_cndmask_b32 v12, v12, v64, s[44:45]\n"
"v_cndmask_b32 v13, v13, v65, s[44:45]\nv_cndmask_b32 v14, v14, v68, s[44:45]\nv_cndmask_b32 v15, v15, v69, s[44:45]\nv_cndmask_b32 v12, v12, v72, s[46:47]\n"
"v_cndmask_b32 v13, v13, v73, s[46:47]\nv_cndmask_b32 v14, v14, v76, s[46:47]\nv_cndmask_b32 v15, v15, v77, s[46:47]\nv_cndmask_b32 v16, v16, v64, s[48:49]\n"
"v_cndmask_b32 v17, v17, v65, s[48:49]\nv_cndmask_b32 v18, v18, v68, s[48:49]\nv_cndmask_b32 v19, v19, v69, s[48:49]\nv_cndmask_b32 v16, v16, v72, s[50:51]\n"
"v_cndmask_b32 v17, v17, v73, s[50:51]\nv_cndmask_b32 v18, v18, v76, s[50:51]\nv_cndmask_b32 v19, v19, v77, s[50:51]\nv_sub_f32 v92, 1.0, v80\nv_sub_f32 v93, 1.0, v82\nCV 92,92,93\n"
"v_and_b32 v93, 0xffff0000, v92\nv_lshlrev_b32 v92, 16, v92\nv_lshlrev_b32 v64, 16, v12\nv_and_b32 v65, 0xffff0000, v12\nMU 64,64,92\nMU 65,65,92\nv_lshlrev_b32 v66, 16, v13\n"
"v_and_b32 v67, 0xffff0000, v13\nMU 66,66,92\nMU 67,67,92\nv_lshlrev_b32 v68, 16, v14\nv_and_b32 v69, 0xffff0000, v14\nMU 68,68,92\nMU 69,69,92\nv_lshlrev_b32 v70, 16, v15\n"
"v_and_b32 v71, 0xffff0000, v15\nMU 70,70,92\nMU 71,71,92\nv_lshlrev_b32 v72, 16, v16\nv_and_b32 v73, 0xffff0000, v16\nMU 72,72,93\nMU 73,73,93\nv_lshlrev_b32 v74, 16, v17\n"
"v_and_b32 v75, 0xffff0000, v17\nMU 74,74,93\nMU 75,75,93\nv_lshlrev_b32 v76, 16, v18\nv_and_b32 v77, 0xffff0000, v18\nMU 76,76,93\nMU 77,77,93\nv_lshlrev_b32 v78, 16, v19\n"
"v_and_b32 v79, 0xffff0000, v19\nMU 78,78,93\nMU 79,79,93\ns_mov_b32 s7, 262144\nWV 0\n.Z390:\nv_max_u32 v84, v99, v103\nv_cmp_eq_u32 s[44:45], -1, v84\nv_max_u32 v84, v107, v111\n"
"v_cmp_eq_u32 s[46:47], -1, v84\nv_max_u32 v84, v115, v119\nv_cmp_eq_u32 s[48:49], -1, v84\nv_max_u32 v84, v123, v127\nv_cmp_eq_u32 s[50:51], -1, v84\nv_max_u32 v84, v35, v39\n"
"v_cmp_eq_u32 s[52:53], -1, v84\nv_max_u32 v84, v43, v47\nv_cmp_eq_u32 s[54:55], -1, v84\nv_max_u32 v84, v51, v55\nv_cmp_eq_u32 s[56:57], -1, v84\nv_max_u32 v84, v59, v63\n"
"v_cmp_eq_u32 s[58:59], -1, v84\ns_or_b64 s[0:1], s[44:45], s[46:47]\ns_or_b64 s[2:3], s[48:49], s[50:51]\ns_or_b64 s[0:1], s[0:1], s[52:53]\ns_or_b64 s[2:3], s[2:3], s[54:55]\n"
".W584:\ns_cbranch_execz .WB274\ns_or_b64 s[0:1], s[0:1], s[56:57]\ns_or_b64 s[2:3], s[2:3], s[58:59]\ns_or_b64 s[0:1], s[0:1], s[2:3]\ns_cbranch_scc1 .Z388\n.Z389:\nZvvv 136,6,96\n"
"Mvvv 136,6,104\nMvvv 136,6,112\nMvvv 136,6,120\nMvvv 136,6,32\nMvvv 136,6,40\nMvvv 136,6,48\nMvvv 136,6,56\nZvvv 140,4,96\nMvvv 140,4,104\nMvvv 140,4,112\nMvvv 140,4,120\n"
"Mvvv 140,4,32\nMvvv 140,4,40\nMvvv 140,4,48\nMvvv 140,4,56\nZvvv 144,6,100\nMvvv 144,6,108\nMvvv 144,6,116\nMvvv 144,6,124\nMvvv 144,6,36\nMvvv 144,6,44\nMvvv 144,6,52\n"
"Mvvv 144,6,60\nZvvv 148,4,100\nMvvv 148,4,108\nMvvv 148,4,116\nMvvv 148,4,124\nMvvv 148,4,36\nMvvv 148,4,44\nMvvv 148,4,52\nMvvv 148,4,60\nFM 64,80,136\nFM 65,80,137\n"
"FM 66,80,138\nFM 67,80,139\nFM 68,80,140\nFM 69,80,141\nFM 70,80,142\nFM 71,80,143\nCV 12,64,65\nCV 13,66,67\nCV 14,68,69\nCV 15,70,71\nSv 12,91,36,22,0\nFM 72,82,144\n"
"FM 73,82,145\nFM 74,82,146\nFM 75,82,147\nFM 76,82,148\nFM 77,82,149\nFM 78,82,150\nFM 79,82,151\nCV 16,72,73\nCV 17,74,75\nCV 18,76,77\nCV 19,78,79\nSv 16,91,36,23,0\n.Z387:\n"
"s_endpgm\n.EP399:\n.S270:\ns_mov_b32 s7, 262144\n.S272:\ns_lshl_b32 s5, s73, 8\ns_add_u32 s5, s5, 0xc04ac00\nv_lshrrev_b32 v27, 2, v1\n"
"buffer_load_dword v27, v27, s[16:19], s5 offen sc0 sc1\nWV 0\nv_xor_b32 v27, s15, v27\nv_and_b32 v32, 0xffffdfff, v27\nv_cmp_ne_u32 vcc, 0, v32\ns_nop 0\ns_cbranch_vccz .S271\n"
"s_sleep 6\ns_sub_u32 s7, s7, 1\ns_cbranch_scc0 .S272\ns_branch .S271\n.T279:\ns_mov_b32 s62, 1\ns_call_b64 s[80:81], .RA266\ns_branch .T280\n.T281:\ns_mov_b32 s62, 1\n"
"s_call_b64 s[80:81], .RA266\ns_branch .T282\n.S284:\ns_mov_b32 s7, 262144\n.S286:\nbuffer_load_dword v27, off, s[16:19], s5 sc0 sc1\nWV 0\nv_cmp_ne_u32 vcc, s15, v27\ns_nop 0\n"
"s_cbranch_vccz .S285\ns_sleep 6\ns_sub_u32 s7, s7, 1\ns_cbranch_scc0 .S286\ns_branch .S285\n.S287:\ns_mov_b32 s7, 262144\n.S289:\nv_and_b32 v27, 0x70, v1\n"
"buffer_load_dword v27, v27, s[16:19], s61 offen offset:128 sc0 sc1\nWV 0\nv_cmp_ne_u32 vcc, s15, v27\ns_nop 0\ns_cbranch_vccz .S288\ns_sleep 6\ns_sub_u32 s7, s7, 1\n"
"s_cbranch_scc0 .S289\ns_branch .S288\n.S290:\ns_mov_b32 s7, 262144\n.S292:\nv_and_b32 v27, 0x70, v1\nbuffer_load_dword v27, v27, s[16:19], s61 offen offset:136 sc0 sc1\nWV 0\n"
"v_cmp_ne_u32 vcc, s15, v27\ns_nop 0\ns_cbranch_vccz .S291\ns_sleep 6\ns_sub_u32 s7, s7, 1\ns_cbranch_scc0 .S292\ns_branch .S291\n.S293:\ns_mov_b32 s7, 262144\n.S295:\n"
"v_and_b32 v27, 0x70, v1\nbuffer_load_dword v27, v27, s[16:19], s61 offen offset:140 sc0 sc1\nWV 0\nv_cmp_ne_u32 vcc, s15, v27\ns_nop 0\ns_cbranch_vccz .S294\ns_sleep 6\n"
"s_sub_u32 s7, s7, 1\ns_cbranch_scc0 .S295\ns_branch .S294\n.S296:\ns_mov_b32 s7, 262144\n.S298:\nv_and_b32 v27, 0x70, v1\n"
"buffer_load_dword v27, v27, s[16:19], s61 offen offset:12 sc0 sc1\nWV 0\nv_cmp_ne_u32 vcc, s15, v27\ns_nop 0\ns_cbranch_vccz .S297\ns_sleep 6\ns_sub_u32 s7, s7, 1\n"
"s_cbranch_scc0 .S298\ns_branch .S297\n.S299:\ns_mov_b32 s7, 262144\n.S301:\nv_and_b32 v27, 0x70, v1\nbuffer_load_dword v27, v27, s[16:19], s61 offen offset:8 sc0 sc1\nWV 0\n"
"v_cmp_ne_u32 vcc, s15, v27\ns_nop 0\ns_cbranch_vccz .S300\ns_sleep 6\ns_sub_u32 s7, s7, 1\ns_cbranch_scc0 .S301\ns_branch .S300\n.S302:\ns_mov_b32 s7, 262144\n.S304:\n"
"v_and_b32 v27, 0xf0, v1\nbuffer_load_dword v27, v27, s[16:19], s61 offen offset:4 sc0 sc1\nWV 0\nv_cmp_ne_u32 vcc, s15, v27\ns_nop 0\ns_cbranch_vccz .S303\ns_sleep 6\n"
"s_sub_u32 s7, s7, 1\ns_cbranch_scc0 .S304\ns_branch .S303\n.T313:\ns_mov_b32 s62, 0\ns_call_b64 s[80:81], .RB267\ns_branch .T314\n.T315:\ns_mov_b32 s62, 0\n"
"s_call_b64 s[80:81], .RA266\ns_branch .T316\n.T319:\ns_mov_b32 s62, 0\ns_call_b64 s[80:81], .RB267\ns_branch .T320\n.T321:\ns_mov_b32 s62, 0\ns_call_b64 s[80:81], .RA266\n"
"s_branch .T322\n.T328:\ns_mov_b32 s62, 2\ns_call_b64 s[80:81], .RB267\ns_branch .T329\n.T330:\ns_mov_b32 s62, 5\ns_call_b64 s[80:81], .RA266\ns_branch .T331\n.T332:\n"
"s_mov_b32 s62, 5\ns_call_b64 s[80:81], .RA266\ns_branch .T333\n.T344:\ns_mov_b32 s62, 4\ns_call_b64 s[80:81], .RB267\ns_branch .T345\n.T346:\ns_mov_b32 s62, 4\n"
"s_call_b64 s[80:81], .RA266\ns_branch .T347\n.T351:\ns_mov_b32 s62, 4\ns_call_b64 s[80:81], .RB267\ns_branch .T352\n.T353:\ns_mov_b32 s62, 4\ns_call_b64 s[80:81], .RA266\n"
"s_branch .T354\n.T355:\ns_mov_b32 s62, 6\ns_call_b64 s[80:81], .RB267\ns_branch .T356\n.S357:\ns_mov_b32 s7, 262144\n.S359:\ns_add_u32 s0, s92, 0\n"
"buffer_load_dwordx4 v[208:211], v1, s[16:19], s0 offen offset:0 sc0 sc1\nbuffer_load_dwordx4 v[212:215], v1, s[16:19], s0 offen offset:1024 sc0 sc1\nWV 0\n"
"v_max_u32 v21, v211, v215\nv_cmp_eq_u32 vcc, -1, v21\ns_nop 0\ns_cbranch_vccz .S358\ns_sleep 6\ns_sub_u32 s7, s7, 1\ns_cbranch_scc0 .S359\ns_branch .S358\n.S360:\n"
"s_mov_b32 s7, 262144\n.S362:\ns_add_u32 s0, s92, 131072\nbuffer_load_dwordx4 v[216:219], v1, s[16:19], s0 offen offset:0 sc0 sc1\n"
"buffer_load_dwordx4 v[220:223], v1, s[16:19], s0 offen offset:1024 sc0 sc1\nWV 0\nv_max_u32 v21, v219, v223\nv_cmp_eq_u32 vcc, -1, v21\ns_nop 0\ns_cbranch_vccz .S361\ns_sleep 6\n"
"s_sub_u32 s7, s7, 1\ns_cbranch_scc0 .S362\ns_branch .S361\n.S363:\ns_mov_b32 s7, 262144\n.S365:\ns_add_u32 s0, s92, 262144\n"
"buffer_load_dwordx4 v[224:227], v1, s[16:19], s0 offen offset:0 sc0 sc1\nbuffer_load_dwordx4 v[228:231], v1, s[16:19], s0 offen offset:1024 sc0 sc1\nWV 0\n"
"v_max_u32 v21, v227, v231\nv_cmp_eq_u32 vcc, -1, v21\ns_nop 0\ns_cbranch_vccz .S364\ns_sleep 6\ns_sub_u32 s7, s7, 1\ns_cbranch_scc0 .S365\ns_branch .S364\n.S366:\n"
"s_mov_b32 s7, 262144\n.S368:\ns_add_u32 s0, s92, 393216\nbuffer_load_dwordx4 v[208:211], v1, s[16:19], s0 offen offset:0 sc0 sc1\n"
"buffer_load_dwordx4 v[212:215], v1, s[16:19], s0 offen offset:1024 sc0 sc1\nWV 0\nv_max_u32 v21, v211, v215\nv_cmp_eq_u32 vcc, -1, v21\ns_nop 0\ns_cbranch_vccz .S367\ns_sleep 6\n"
"s_sub_u32 s7, s7, 1\ns_cbranch_scc0 .S368\ns_branch .S367\n.S369:\ns_mov_b32 s7, 262144\n.S371:\ns_add_u32 s0, s92, 524288\n"
"buffer_load_dwordx4 v[216:219], v1, s[16:19], s0 offen offset:0 sc0 sc1\nbuffer_load_dwordx4 v[220:223], v1, s[16:19], s0 offen offset:1024 sc0 sc1\nWV 0\n"
"v_max_u32 v21, v219, v223\nv_cmp_eq_u32 vcc, -1, v21\ns_nop 0\ns_cbranch_vccz .S370\ns_sleep 6\ns_sub_u32 s7, s7, 1\ns_cbranch_scc0 .S371\ns_branch .S370\n.S372:\n"
"s_mov_b32 s7, 262144\n.S374:\ns_add_u32 s0, s92, 655360\nbuffer_load_dwordx4 v[224:227], v1, s[16:19], s0 offen offset:0 sc0 sc1\n"
"buffer_load_dwordx4 v[228:231], v1, s[16:19], s0 offen offset:1024 sc0 sc1\nWV 0\nv_max_u32 v21, v227, v231\nv_cmp_eq_u32 vcc, -1, v21\ns_nop 0\ns_cbranch_vccz .S373\ns_sleep 6\n"
"s_sub_u32 s7, s7, 1\ns_cbranch_scc0 .S374\ns_branch .S373\n.S375:\ns_mov_b32 s7, 262144\n.S377:\ns_add_u32 s0, s92, 786432\n"
"buffer_load_dwordx4 v[208:211], v1, s[16:19], s0 offen offset:0 sc0 sc1\nbuffer_load_dwordx4 v[212:215], v1, s[16:19], s0 offen offset:1024 sc0 sc1\nWV 0\n"
"v_max_u32 v21, v211, v215\nv_cmp_eq_u32 vcc, -1, v21\ns_nop 0\ns_cbranch_vccz .S376\ns_sleep 6\ns_sub_u32 s7, s7, 1\ns_cbranch_scc0 .S377\ns_branch .S376\n.S378:\n"
"s_mov_b32 s7, 262144\n.S380:\nbuffer_load_dwordx4 v[64:67], v89, s[16:19], s40 offen offset:0 sc0 sc1\nbuffer_load_dwordx4 v[68:71], v89, s[16:19], s40 offen offset:16 sc0 sc1\n"
"buffer_load_dwordx4 v[72:75], v89, s[16:19], s40 offen offset:1792 sc0 sc1\nbuffer_load_dwordx4 v[76:79], v89, s[16:19], s40 offen offset:1808 sc0 sc1\n"
"buffer_load_dwordx2 v[80:81], v88, s[16:19], s63 offen offset:0 sc0 sc1\nbuffer_load_dwordx2 v[82:83], v88, s[16:19], s63 offen offset:512 sc0 sc1\nWV 0\nv_xor_b32 v66, s15, v66\n"
"v_xor_b32 v70, s15, v70\nv_xor_b32 v74, s15, v74\nv_xor_b32 v78, s15, v78\nv_xor_b32 v81, s15, v81\nv_xor_b32 v83, s15, v83\nv_or3_b32 v92, v66, v70, v74\n"
"v_or3_b32 v92, v92, v78, v81\nv_or_b32 v92, v92, v83\nv_cmp_ne_u32 vcc, 0, v92\ns_nop 0\ns_cbranch_vccz .S379\ns_sleep 6\ns_sub_u32 s7, s7, 1\ns_cbranch_scc0 .S380\n"
"s_branch .S379\n.S381:\ns_mov_b32 s7, 262144\n.S383:\nbuffer_load_dword v27, off, s[16:19], s60 offset:512 sc0 sc1\nWV 0\nv_cmp_ne_u32 vcc, s15, v27\ns_nop 0\n"
"s_cbranch_vccz .S382\ns_sleep 6\ns_sub_u32 s7, s7, 1\ns_cbranch_scc0 .S383\ns_branch .S382\n.S384:\ns_mov_b32 s7, 262144\n.S386:\n"
"buffer_load_dword v27, v24, s[16:19], s41 offen sc0 sc1\nWV 0\nv_xor_b32 v27, s15, v27\nv_and_b32 v26, 0xffffdfff, v27\nv_cmp_ne_u32 s[64:65], 0, v27\nv_cmp_ne_u32 vcc, 0, v26\n"
"s_nop 0\ns_cbranch_vccz .S385\ns_sleep 6\ns_sub_u32 s7, s7, 1\ns_cbranch_scc0 .S386\ns_branch .S385\n.Z388:\ns_sub_u32 s7, s7, 1\ns_cbranch_scc1 .Z389\ns_cmp_lg_u64 s[44:45], 0\n"
"s_cbranch_scc0 .Z391\nbuffer_load_dwordx4 v[96:99], v1, s[16:19], s24 offen offset:0 sc0 sc1\nbuffer_load_dwordx4 v[100:103], v1, s[16:19], s24 offen offset:1024 sc0 sc1\n.Z391:\n"
"s_cmp_lg_u64 s[46:47], 0\ns_cbranch_scc0 .Z392\nbuffer_load_dwordx4 v[104:107], v1, s[16:19], s25 offen offset:0 sc0 sc1\n"
"buffer_load_dwordx4 v[108:111], v1, s[16:19], s25 offen offset:1024 sc0 sc1\n.Z392:\ns_cmp_lg_u64 s[48:49], 0\ns_cbranch_scc0 .Z393\n"
"buffer_load_dwordx4 v[112:115], v1, s[16:19], s26 offen offset:0 sc0 sc1\nbuffer_load_dwordx4 v[116:119], v1, s[16:19], s26 offen offset:1024 sc0 sc1\n.Z393:\n"
"s_cmp_lg_u64 s[50:51], 0\ns_cbranch_scc0 .Z394\nbuffer_load_dwordx4 v[120:123], v1, s[16:19], s27 offen offset:0 sc0 sc1\n"
"buffer_load_dwordx4 v[124:127], v1, s[16:19], s27 offen offset:1024 sc0 sc1\n.Z394:\ns_cmp_lg_u64 s[52:53], 0\ns_cbranch_scc0 .Z395\n"
"buffer_load_dwordx4 v[32:35], v1, s[16:19], s32 offen offset:0 sc0 sc1\nbuffer_load_dwordx4 v[36:39], v1, s[16:19], s32 offen offset:1024 sc0 sc1\n.Z395:\n"
"s_cmp_lg_u64 s[54:55], 0\ns_cbranch_scc0 .Z396\nbuffer_load_dwordx4 v[40:43], v1, s[16:19], s33 offen offset:0 sc0 sc1\n"
"buffer_load_dwordx4 v[44:47], v1, s[16:19], s33 offen offset:1024 sc0 sc1\n.Z396:\ns_cmp_lg_u64 s[56:57], 0\ns_cbranch_scc0 .Z397\n"
"buffer_load_dwordx4 v[48:51], v1, s[16:19], s34 offen offset:0 sc0 sc1\nbuffer_load_dwordx4 v[52:55], v1, s[16:19], s34 offen offset:1024 sc0 sc1\n.Z397:\n"
"s_cmp_lg_u64 s[58:59], 0\ns_cbranch_scc0 .Z398\nbuffer_load_dwordx4 v[56:59], v1, s[16:19], s35 offen offset:0 sc0 sc1\n"
"buffer_load_dwordx4 v[60:63], v1, s[16:19], s35 offen offset:1024 sc0 sc1\n.Z398:\ns_sleep 8\nWV 0\ns_branch .Z390\n.RA266:\ns_nop 7\ns_nop 3\nv_max3_f32 v22, v32, v33, v34\n"
"v_max3_f32 v22, v22, v35, v40\nv_max3_f32 v22, v22, v41, v42\nv_max_f32 v22, v22, v43\nv_mov_b32 v91, v22\ns_nop 1\nv_permlane32_swap_b32 v22, v91\nv_max_f32 v22, v22, v91\n"
"v_mov_b32 v91, v22\ns_nop 1\nv_permlane16_swap_b32 v22, v91\nv_max_f32 v22, v22, v91\nv_ceil_f32 v22, v22\nv_max3_f32 v23, v36, v37, v38\nv_max3_f32 v23, v23, v39, v44\n"
"v_max3_f32 v23, v23, v45, v46\nv_max_f32 v23, v23, v47\nv_mov_b32 v91, v23\ns_nop 1\nv_permlane32_swap_b32 v23, v91\nv_max_f32 v23, v23, v91\nv_mov_b32 v91, v23\ns_nop 1\n"
"v_permlane16_swap_b32 v23, v91\nv_max_f32 v23, v23, v91\nv_ceil_f32 v23, v23\ns_bitcmp1_b32 s62, 0\ns_cbranch_scc0 .R400\nv_cmp_lt_f32 vcc, 0xff000000, v22\ns_nop 1\n"
"v_cndmask_b32 v22, 0, v22, vcc\nv_cmp_lt_f32 vcc, 0xff000000, v23\ns_nop 1\nv_cndmask_b32 v23, 0, v23, vcc\ns_branch .R401\n.R400:\nv_max_f32 v22, 0, v22\nv_max_f32 v23, 0, v23\n"
"s_bitcmp1_b32 s62, 3\ns_cbranch_scc0 .R401\nv_max_f32 v22, v20, v22\nv_max_f32 v23, v21, v23\n.R401:\nAD 88,88,22\nAD 89,89,23\nSU 32,32,22\nSU 33,33,22\nSU 34,34,22\nSU 35,35,22\n"
"SU 36,36,23\nSU 37,37,23\nSU 38,38,23\nSU 39,39,23\nSU 40,40,22\nSU 41,41,22\nSU 42,42,22\nSU 43,43,22\nSU 44,44,23\nSU 45,45,23\nSU 46,46,23\nSU 47,47,23\nv_mov_b32 v90, v80\n"
"v_mov_b32 v91, v81\nEX 80,32\nEX 81,33\ns_nop 0\nAD 84,80,81\nCV 64,80,81\nEX 82,34\nEX 83,35\nAD 84,84,82\nAD 84,84,83\nCV 65,82,83\nEX 80,36\nEX 81,37\ns_nop 0\nAD 85,80,81\n"
"CV 68,80,81\nEX 82,38\nEX 83,39\nAD 85,85,82\nAD 85,85,83\nCV 69,82,83\nEX 80,40\nEX 81,41\nAD 84,84,80\nAD 84,84,81\nCV 66,80,81\nEX 82,42\nEX 83,43\nAD 84,84,82\nAD 84,84,83\n"
"CV 67,82,83\nEX 80,44\nEX 81,45\nAD 85,85,80\nAD 85,85,81\nCV 70,80,81\nEX 82,46\nEX 83,47\nAD 85,85,82\nAD 85,85,83\nCV 71,82,83\nv_mov_b32 v80, v90\nv_mov_b32 v81, v91\n"
"s_bitcmp1_b32 s62, 1\ns_cbranch_scc1 .R402\nSU 48,48,22\nSU 49,49,22\nSU 50,50,22\nSU 51,51,22\nSU 52,52,23\nSU 53,53,23\nSU 54,54,23\nSU 55,55,23\nSU 56,56,22\nSU 57,57,22\n"
"SU 58,58,22\nSU 59,59,22\nSU 60,60,23\nSU 61,61,23\nSU 62,62,23\nSU 63,63,23\nSU 4,4,22\nSU 5,5,22\nSU 6,6,22\nSU 7,7,22\nSU 8,8,23\nSU 9,9,23\nSU 10,10,23\nSU 11,11,23\n"
"SU 12,12,22\nSU 13,13,22\nSU 14,14,22\nSU 15,15,22\nSU 16,16,23\nSU 17,17,23\nSU 18,18,23\nSU 19,19,23\nEX 80,48\nEX 81,49\n.R402:\ns_bitcmp1_b32 s62, 0\ns_cbranch_scc0 .RS268\n"
"s_nop 1\ns_setpc_b64 s[80:81]\n.RB267:\ns_nop 7\ns_nop 3\nv_max3_f32 v22, v48, v49, v50\nv_max3_f32 v22, v22, v51, v56\nv_max3_f32 v22, v22, v57, v58\nv_max_f32 v22, v22, v59\n"
"v_mov_b32 v91, v22\ns_nop 1\nv_permlane32_swap_b32 v22, v91\nv_max_f32 v22, v22, v91\nv_mov_b32 v91, v22\ns_nop 1\nv_permlane16_swap_b32 v22, v91\nv_max_f32 v22, v22, v91\n"
"v_ceil_f32 v22, v22\nv_max3_f32 v23, v52, v53, v54\nv_max3_f32 v23, v23, v55, v60\nv_max3_f32 v23, v23, v61, v62\nv_max_f32 v23, v23, v63\nv_mov_b32 v91, v23\ns_nop 1\n"
"v_permlane32_swap_b32 v23, v91\nv_max_f32 v23, v23, v91\nv_mov_b32 v91, v23\ns_nop 1\nv_permlane16_swap_b32 v23, v91\nv_max_f32 v23, v23, v91\nv_ceil_f32 v23, v23\n"
"s_bitcmp1_b32 s62, 0\ns_cbranch_scc0 .R405\nv_cmp_lt_f32 vcc, 0xff000000, v22\nv_cndmask_b32 v22, 0, v22, vcc\nv_cmp_lt_f32 vcc, 0xff000000, v23\nv_cndmask_b32 v23, 0, v23, vcc\n"
"s_branch .R406\n.R405:\nv_max_f32 v22, 0, v22\nv_max_f32 v23, 0, v23\ns_bitcmp1_b32 s62, 3\ns_cbranch_scc0 .R406\nv_max_f32 v22, v20, v22\nv_max_f32 v23, v21, v23\n.R406:\n"
"AD 88,88,22\nAD 89,89,23\nSU 48,48,22\nSU 49,49,22\nSU 50,50,22\nSU 51,51,22\nSU 52,52,23\nSU 53,53,23\nSU 54,54,23\nSU 55,55,23\nSU 56,56,22\nSU 57,57,22\nSU 58,58,22\n"
"SU 59,59,22\nSU 60,60,23\nSU 61,61,23\nSU 62,62,23\nSU 63,63,23\nv_mov_b32 v90, v80\nv_mov_b32 v91, v81\nEX 80,48\nEX 81,49\ns_nop 0\nAD 84,80,81\nCV 72,80,81\nEX 82,50\nEX 83,51\n"
"AD 84,84,82\nAD 84,84,83\nCV 73,82,83\nEX 80,52\nEX 81,53\ns_nop 0\nAD 85,80,81\nCV 76,80,81\nEX 82,54\nEX 83,55\nAD 85,85,82\nAD 85,85,83\nCV 77,82,83\nEX 80,56\nEX 81,57\n"
"AD 84,84,80\nAD 84,84,81\nCV 74,80,81\nEX 82,58\nEX 83,59\nAD 84,84,82\nAD 84,84,83\nCV 75,82,83\nEX 80,60\nEX 81,61\nAD 85,85,80\nAD 85,85,81\nCV 78,80,81\nEX 82,62\nEX 83,63\n"
"AD 85,85,82\nAD 85,85,83\nCV 79,82,83\nv_mov_b32 v80, v90\nv_mov_b32 v81, v91\ns_bitcmp1_b32 s62, 1\ns_cbranch_scc1 .R407\nSU 32,32,22\nSU 33,33,22\nSU 34,34,22\nSU 35,35,22\n"
"SU 36,36,23\nSU 37,37,23\nSU 38,38,23\nSU 39,39,23\nSU 40,40,22\nSU 41,41,22\nSU 42,42,22\nSU 43,43,22\nSU 44,44,23\nSU 45,45,23\nSU 46,46,23\nSU 47,47,23\nSU 4,4,22\nSU 5,5,22\n"
"SU 6,6,22\nSU 7,7,22\nSU 8,8,23\nSU 9,9,23\nSU 10,10,23\nSU 11,11,23\nSU 12,12,22\nSU 13,13,22\nSU 14,14,22\nSU 15,15,22\nSU 16,16,23\nSU 17,17,23\nSU 18,18,23\nSU 19,19,23\n"
"EX 80,32\nEX 81,33\n.R407:\ns_bitcmp1_b32 s62, 0\ns_cbranch_scc0 .RS268\ns_nop 1\ns_setpc_b64 s[80:81]\n.RS268:\nv_exp_f32 v22, -v22\nv_exp_f32 v23, -v23\nMU 86,86,22\n"
"MU 87,87,23\ns_bitcmp1_b32 s62, 2\ns_cbranch_scc1 .R410\nAR 91,0\nMU 91,91,22\nAW 0,91\nAR 91,1\nMU 91,91,22\nAW 1,91\nAR 91,2\nMU 91,91,22\nAW 2,91\nAR 91,3\nMU 91,91,22\n"
"AW 3,91\nAR 91,4\nMU 91,91,23\nAW 4,91\nAR 91,5\nMU 91,91,23\nAW 5,91\nAR 91,6\nMU 91,91,23\nAW 6,91\nAR 91,7\nMU 91,91,23\nAW 7,91\nAR 91,8\nMU 91,91,22\nAW 8,91\nAR 91,9\n"
"MU 91,91,22\nAW 9,91\nAR 91,10\nMU 91,91,22\nAW 10,91\nAR 91,11\nMU 91,91,22\nAW 11,91\nAR 91,12\nMU 91,91,23\nAW 12,91\nAR 91,13\nMU 91,91,23\nAW 13,91\nAR 91,14\nMU 91,91,23\n"
"AW 14,91\nAR 91,15\nMU 91,91,23\nAW 15,91\nAR 91,16\nMU 91,91,22\nAW 16,91\nAR 91,17\nMU 91,91,22\nAW 17,91\nAR 91,18\nMU 91,91,22\nAW 18,91\nAR 91,19\nMU 91,91,22\nAW 19,91\n"
"AR 91,20\nMU 91,91,23\nAW 20,91\nAR 91,21\nMU 91,91,23\nAW 21,91\nAR 91,22\nMU 91,91,23\nAW 22,91\nAR 91,23\nMU 91,91,23\nAW 23,91\nAR 91,24\nMU 91,91,22\nAW 24,91\nAR 91,25\n"
"MU 91,91,22\nAW 25,91\nAR 91,26\nMU 91,91,22\nAW 26,91\nAR 91,27\nMU 91,91,22\nAW 27,91\nAR 91,28\nMU 91,91,23\nAW 28,91\nAR 91,29\nMU 91,91,23\nAW 29,91\nAR 91,30\nMU 91,91,23\n"
"AW 30,91\nAR 91,31\nMU 91,91,23\nAW 31,91\nAR 91,32\nMU 91,91,22\nAW 32,91\nAR 91,33\nMU 91,91,22\nAW 33,91\nAR 91,34\nMU 91,91,22\nAW 34,91\nAR 91,35\nMU 91,91,22\nAW 35,91\n"
"AR 91,36\nMU 91,91,23\nAW 36,91\nAR 91,37\nMU 91,91,23\nAW 37,91\nAR 91,38\nMU 91,91,23\nAW 38,91\nAR 91,39\nMU 91,91,23\nAW 39,91\nAR 91,40\nMU 91,91,22\nAW 40,91\nAR 91,41\n"
"MU 91,91,22\nAW 41,91\nAR 91,42\nMU 91,91,22\nAW 42,91\nAR 91,43\nMU 91,91,22\nAW 43,91\nAR 91,44\nMU 91,91,23\nAW 44,91\nAR 91,45\nMU 91,91,23\nAW 45,91\nAR 91,46\nMU 91,91,23\n"
"AW 46,91\nAR 91,47\nMU 91,91,23\nAW 47,91\nAR 91,48\nMU 91,91,22\nAW 48,91\nAR 91,49\nMU 91,91,22\nAW 49,91\nAR 91,50\nMU 91,91,22\nAW 50,91\nAR 91,51\nMU 91,91,22\nAW 51,91\n"
"AR 91,52\nMU 91,91,23\nAW 52,91\nAR 91,53\nMU 91,91,23\nAW 53,91\nAR 91,54\nMU 91,91,23\nAW 54,91\nAR 91,55\nMU 91,91,23\nAW 55,91\nAR 91,56\nMU 91,91,22\nAW 56,91\nAR 91,57\n"
"MU 91,91,22\nAW 57,91\nAR 91,58\nMU 91,91,22\nAW 58,91\nAR 91,59\nMU 91,91,22\nAW 59,91\nAR 91,60\nMU 91,91,23\nAW 60,91\nAR 91,61\nMU 91,91,23\nAW 61,91\nAR 91,62\nMU 91,91,23\n"
"AW 62,91\nAR 91,63\nMU 91,91,23\nAW 63,91\nAR 91,64\nMU 91,91,22\nAW 64,91\nAR 91,65\nMU 91,91,22\nAW 65,91\nAR 91,66\nMU 91,91,22\nAW 66,91\nAR 91,67\nMU 91,91,22\nAW 67,91\n"
"AR 91,68\nMU 91,91,23\nAW 68,91\nAR 91,69\nMU 91,91,23\nAW 69,91\nAR 91,70\nMU 91,91,23\nAW 70,91\nAR 91,71\nMU 91,91,23\nAW 71,91\nAR 91,72\nMU 91,91,22\nAW 72,91\nAR 91,73\n"
"MU 91,91,22\nAW 73,91\nAR 91,74\nMU 91,91,22\nAW 74,91\nAR 91,75\nMU 91,91,22\nAW 75,91\nAR 91,76\nMU 91,91,23\nAW 76,91\nAR 91,77\nMU 91,91,23\nAW 77,91\nAR 91,78\nMU 91,91,23\n"
"AW 78,91\nAR 91,79\nMU 91,91,23\nAW 79,91\nAR 91,80\nMU 91,91,22\nAW 80,91\nAR 91,81\nMU 91,91,22\nAW 81,91\nAR 91,82\nMU 91,91,22\nAW 82,91\nAR 91,83\nMU 91,91,22\nAW 83,91\n"
"AR 91,84\nMU 91,91,23\nAW 84,91\nAR 91,85\nMU 91,91,23\nAW 85,91\nAR 91,86\nMU 91,91,23\nAW 86,91\nAR 91,87\nMU 91,91,23\nAW 87,91\nAR 91,88\nMU 91,91,22\nAW 88,91\nAR 91,89\n"
"MU 91,91,22\nAW 89,91\nAR 91,90\nMU 91,91,22\nAW 90,91\nAR 91,91\nMU 91,91,22\nAW 91,91\nAR 91,92\nMU 91,91,23\nAW 92,91\nAR 91,93\nMU 91,91,23\nAW 93,91\nAR 91,94\nMU 91,91,23\n"
"AW 94,91\nAR 91,95\nMU 91,91,23\nAW 95,91\nAR 91,96\nMU 91,91,22\nAW 96,91\nAR 91,97\nMU 91,91,22\nAW 97,91\nAR 91,98\nMU 91,91,22\nAW 98,91\nAR 91,99\nMU 91,91,22\nAW 99,91\n"
"AR 91,100\nMU 91,91,23\nAW 100,91\nAR 91,101\nMU 91,91,23\nAW 101,91\nAR 91,102\nMU 91,91,23\nAW 102,91\nAR 91,103\nMU 91,91,23\nAW 103,91\nAR 91,104\nMU 91,91,22\nAW 104,91\n"
"AR 91,105\nMU 91,91,22\nAW 105,91\nAR 91,106\nMU 91,91,22\nAW 106,91\nAR 91,107\nMU 91,91,22\nAW 107,91\nAR 91,108\nMU 91,91,23\nAW 108,91\nAR 91,109\nMU 91,91,23\nAW 109,91\n"
"AR 91,110\nMU 91,91,23\nAW 110,91\nAR 91,111\nMU 91,91,23\nAW 111,91\nAR 91,112\nMU 91,91,22\nAW 112,91\nAR 91,113\nMU 91,91,22\nAW 113,91\nAR 91,114\nMU 91,91,22\nAW 114,91\n"
"AR 91,115\nMU 91,91,22\nAW 115,91\nAR 91,116\nMU 91,91,23\nAW 116,91\nAR 91,117\nMU 91,91,23\nAW 117,91\nAR 91,118\nMU 91,91,23\nAW 118,91\nAR 91,119\nMU 91,91,23\nAW 119,91\n"
"AR 91,120\nMU 91,91,22\nAW 120,91\nAR 91,121\nMU 91,91,22\nAW 121,91\nAR 91,122\nMU 91,91,22\nAW 122,91\nAR 91,123\nMU 91,91,22\nAW 123,91\nAR 91,124\nMU 91,91,23\nAW 124,91\n"
"AR 91,125\nMU 91,91,23\nAW 125,91\nAR 91,126\nMU 91,91,23\nAW 126,91\nAR 91,127\nMU 91,91,23\nAW 127,91\ns_nop 1\ns_setpc_b64 s[80:81]\n.R410:\nMU 128,128,22\nMU 129,129,22\n"
"MU 130,130,22\nMU 131,131,22\nMU 132,132,23\nMU 133,133,23\nMU 134,134,23\nMU 135,135,23\nMU 136,136,22\nMU 137,137,22\nMU 138,138,22\nMU 139,139,22\nMU 140,140,23\nMU 141,141,23\n"
"MU 142,142,23\nMU 143,143,23\nMU 144,144,22\nMU 145,145,22\nMU 146,146,22\nMU 147,147,22\nMU 148,148,23\nMU 149,149,23\nMU 150,150,23\nMU 151,151,23\nMU 152,152,22\nMU 153,153,22\n"
"MU 154,154,22\nMU 155,155,22\nMU 156,156,23\nMU 157,157,23\nMU 158,158,23\nMU 159,159,23\nMU 160,160,22\nMU 161,161,22\nMU 162,162,22\nMU 163,163,22\nMU 164,164,23\nMU 165,165,23\n"
"MU 166,166,23\nMU 167,167,23\nMU 168,168,22\nMU 169,169,22\nMU 170,170,22\nMU 171,171,22\nMU 172,172,23\nMU 173,173,23\nMU 174,174,23\nMU 175,175,23\nMU 176,176,22\nMU 177,177,22\n"
"MU 178,178,22\nMU 179,179,22\nMU 180,180,23\nMU 181,181,23\nMU 182,182,23\nMU 183,183,23\nMU 184,184,22\nMU 185,185,22\nMU 186,186,22\nMU 187,187,22\nMU 188,188,23\nMU 189,189,23\n"
"MU 190,190,23\nMU 191,191,23\nMU 192,192,22\nMU 193,193,22\nMU 194,194,22\nMU 195,195,22\nMU 196,196,23\nMU 197,197,23\nMU 198,198,23\nMU 199,199,23\nMU 200,200,22\nMU 201,201,22\n"
"MU 202,202,22\nMU 203,203,22\nMU 204,204,23\nMU 205,205,23\nMU 206,206,23\nMU 207,207,23\nMU 208,208,22\nMU 209,209,22\nMU 210,210,22\nMU 211,211,22\nMU 212,212,23\nMU 213,213,23\n"
"MU 214,214,23\nMU 215,215,23\nMU 216,216,22\nMU 217,217,22\nMU 218,218,22\nMU 219,219,22\nMU 220,220,23\nMU 221,221,23\nMU 222,222,23\nMU 223,223,23\nMU 224,224,22\nMU 225,225,22\n"
"MU 226,226,22\nMU 227,227,22\nMU 228,228,23\nMU 229,229,23\nMU 230,230,23\nMU 231,231,23\nMU 232,232,22\nMU 233,233,22\nMU 234,234,22\nMU 235,235,22\nMU 236,236,23\nMU 237,237,23\n"
"MU 238,238,23\nMU 239,239,23\nMU 240,240,22\nMU 241,241,22\nMU 242,242,22\nMU 243,243,22\nMU 244,244,23\nMU 245,245,23\nMU 246,246,23\nMU 247,247,23\nMU 248,248,22\nMU 249,249,22\n"
"MU 250,250,22\nMU 251,251,22\nMU 252,252,23\nMU 253,253,23\nMU 254,254,23\nMU 255,255,23\ns_nop 1\ns_setpc_b64 s[80:81]\n.p2align 12\n.PA5:\ns_mov_b64 exec, 0\n"
"s_cmp_eq_u32 s11, 1\ns_cbranch_scc1 .W586\ns_cmp_eq_u32 s11, 2\ns_cbranch_scc1 .W587\ns_cmp_eq_u32 s11, 3\ns_cbranch_scc1 .W588\ns_branch .W585\n.WB416:\ns_mov_b64 exec, -1\n"
"s_add_u32 m0, s47, -49152\ns_add_u32 s48, s48, 128\nD0 2,20,48,0\nD0 3,20,48,1024\nD0 4,20,48,2048\nD0 5,20,48,3072\ns_and_b32 s86, s74, 15\ns_lshl2_add_u32 s86, s86, s11\n"
"s_mov_b32 s88, 0\ns_lshl_b32 s93, s73, 8\ns_lshl2_add_u32 s93, s86, s93\ns_add_u32 s93, s93, 0xc04ac00\ns_lshl_b32 s86, s86, 14\ns_mul_i32 s91, s73, 25165824\n"
"s_add_u32 s86, s86, s91\ns_and_b32 s92, s13, 1\ns_lshl_b32 s92, s92, 13\ns_add_u32 s86, s86, s92\ns_add_u32 s94, s86, 0x1480000\ns_xor_b32 s95, s94, 0x2000\n"
"s_add_u32 s86, s94, 512\ns_load_dword s87, s[54:55], s86\ns_add_u32 m0, s47, -32768\ns_add_u32 s45, s45, 128\nD0 2,20,45,0\nD0 3,20,45,1024\nD0 4,20,45,2048\nD0 5,20,45,3072\n"
"s_add_u32 m0, s47, 32768\ns_add_u32 s44, s44, 0x40000\nD0 6,24,44,0\nD0 7,24,44,1024\nD0 8,24,44,2048\nD0 9,24,44,3072\ns_add_u32 m0, s47, 49152\ns_add_u32 s44, s44, 0x40000\n"
"D0 6,24,44,0\nD0 7,24,44,1024\nD0 8,24,44,2048\nD0 9,24,44,3072\ns_add_u32 m0, s47, -16384\ns_add_u32 s48, s48, 128\nD0 2,20,48,0\nD0 3,20,48,1024\nD0 4,20,48,2048\n"
"D0 5,20,48,3072\nv_mov_b32 v52, 0x40000000\nv_cmp_gt_u32 vcc, 56, v0\ns_lshl2_add_u32 s2, s10, s11\ns_mul_i32 s2, s2, 1792\nv_cndmask_b32 v53, v52, v1, vcc\ns_mov_b32 s36, s56\n"
"s_and_b32 s37, s57, 0xffff\ns_mov_b32 s38, 0x380\ns_mov_b32 s39, 0x20000\ns_mov_b32 s40, s58\ns_and_b32 s41, s59, 0xffff\ns_mov_b32 s42, 0x380\ns_mov_b32 s43, 0x20000\n"
"buffer_load_dwordx4 v[32:35], v53, s[28:31], s2 offen offset:0\nbuffer_load_dwordx4 v[36:39], v53, s[28:31], s2 offen offset:896\n"
"buffer_load_dwordx4 v[40:43], v53, s[36:39], 0 offen offset:0\nbuffer_load_dwordx4 v[44:47], v53, s[40:43], 0 offen offset:0\nWL 0\ns_mul_i32 s15, s15, 1664525\n"
"s_add_u32 s15, s15, 1013904223\nv_mov_b32 v25, s15\ns_mov_b32 s57, 0x3db8aa3b\ns_and_b32 s59, s11, 1\ns_lshl_b32 s59, s59, 13\ns_mul_i32 s2, s73, 25165824\ns_and_b32 s3, s12, 1\n"
"s_lshl_b32 s3, s3, 21\ns_add_u32 s2, s2, s3\ns_lshl2_add_u32 s3, s13, s11\ns_lshl_b32 s3, s3, 15\ns_add_u32 s2, s2, s3\ns_add_u32 s56, s2, 0x800000\ns_xor_b32 s53, s56, 0xc000\n"
"buffer_load_dword v31, v24, s[24:27], s96 offen\ns_add_u32 s96, s96, 0x80000\nbuffer_load_dword v31, v24, s[24:27], s96 offen\ns_add_u32 s96, s96, 0x80000\n"
"v_lshrrev_b32 v60, 3, v0\nv_and_b32 v61, 0x70, v1\nv_lshl_or_b32 v60, v60, 10, v61\nv_mov_b32 v20, 0\nv_mov_b32 v21, 0\nv_mov_b32 v22, 0\nv_mov_b32 v23, 0\nSv 20,60,16,94,0\n"
"Sv 20,60,16,95,0\nWV 28\ns_barrier\nRa 128,10,0\nRa 132,11,0\nRa 136,10,2048\nRa 140,11,2048\nRa 144,10,8192\nRa 148,11,8192\nRa 152,10,10240\nRa 156,11,10240\nTa 160,12,0\n"
"Ta 162,12,2048\nTa 164,12,16384\nTa 166,12,18432\nTa 168,13,0\nTa 170,13,2048\nTa 172,13,16384\nTa 174,13,18432\nTa 176,14,0\nTa 178,14,2048\nTa 180,14,16384\nTa 182,14,18432\n"
"Ta 184,15,0\nTa 186,15,2048\nTa 188,15,16384\nTa 190,15,18432\nWL 0\ns_barrier\nZvaa 64,128,160\nTa 224,16,0\nMvaa 64,132,164\nTa 226,16,2048\nZvaa 68,140,164\n"
"s_add_u32 m0, s47, -65536\ns_add_u32 s45, s45, 128\nMvaa 68,136,160\nTa 228,16,16384\nZvaa 128,144,160\nTa 230,16,18432\nMvaa 128,148,164\nD0 2,20,45,0\nZvaa 132,156,164\n"
"Ta 232,17,0\nMvaa 132,152,160\nTa 234,17,2048\nZvaa 140,152,168\ns_mov_b32 s36, s60\nMvaa 140,156,172\nTa 236,17,16384\nZvaa 136,148,172\nTa 238,17,18432\nMvaa 136,144,168\n"
"s_and_b32 s37, s61, 0xffff\nZvaa 76,136,168\nTa 240,18,0\nMvaa 76,140,172\nTa 242,18,2048\nZvaa 72,132,172\nD0 3,20,45,1024\nMvaa 72,128,168\nTa 244,18,16384\nZvaa 80,128,176\n"
"Ta 246,18,18432\nMvaa 80,132,180\ns_mov_b32 s38, 0x380000\nZvaa 84,140,180\nTa 248,19,0\nMvaa 84,136,176\nTa 250,19,2048\nZvaa 144,144,176\ns_mov_b32 s39, 0x20000\n"
"Mvaa 144,148,180\nTa 252,19,16384\nZvaa 148,156,180\nTa 254,19,18432\nMvaa 148,152,176\nD0 4,20,45,2048\nZvaa 156,152,184\ns_lshl3_add_u32 s75, s12, s13\nMvaa 156,156,188\n"
"s_lshl2_add_u32 s75, s75, s11\nZvaa 152,148,188\ns_mul_i32 s75, s75, 7168\nMvaa 152,144,184\ns_mul_i32 s76, s11, 7168\nZvaa 92,136,184\ns_add_u32 s76, s76, 131072\n"
"Mvaa 92,140,188\nD0 5,20,45,3072\nZvaa 88,132,188\ns_mov_b32 s42, 1605632\nMvaa 88,128,184\ns_waitcnt vmcnt(24) lgkmcnt(0)\ns_barrier\nZvaa 96,128,224\nRa 192,10,16384\n"
"Mvaa 96,132,228\ns_add_u32 m0, s47, 0\ns_add_u32 s44, s44, 0x40000\nZvaa 100,140,228\nRa 196,11,16384\nMvaa 100,136,224\nZvaa 160,144,224\nRa 200,10,18432\nMvaa 160,148,228\n"
"D0 6,24,44,0\nZvaa 164,156,228\nRa 204,11,18432\nMvaa 164,152,224\nZvaa 172,152,232\nRa 208,10,24576\nMvaa 172,156,236\ns_and_b32 s78, s74, 3\ns_lshl2_add_u32 s78, s73, s78\n"
"s_lshl_b32 s78, s78, 12\ns_bitcmp1_b32 s11, 0\ns_cselect_b32 s79, s48, s45\ns_add_u32 s79, s79, 512\ns_and_b32 s79, s79, 0xfffe0000\ns_add_u32 s78, s78, s79\n"
"buffer_load_dword v31, v26, s[20:23], s78 offen\nZvaa 168,148,236\nRa 212,11,24576\nMvaa 168,144,232\nZvaa 108,136,232\nRa 216,10,26624\nMvaa 108,140,236\nD0 7,24,44,1024\n"
"Zvaa 104,132,236\nRa 220,11,26624\nMvaa 104,128,232\nZvaa 112,128,240\nMvaa 112,132,244\nZvaa 116,140,244\nMvaa 116,136,240\nZvaa 176,144,240\nMvaa 176,148,244\nD0 8,24,44,2048\n"
"Zvaa 180,156,244\nMvaa 180,152,240\nZvaa 188,152,248\nMvaa 188,156,252\nZvaa 184,148,252\nMvaa 184,144,248\nZvaa 124,136,248\nMvaa 124,140,252\nD0 9,24,44,3072\nZvaa 120,132,252\n"
"Mvaa 120,128,248\nWL 0\nZaaa 32,192,224\nRa 128,10,32768\nMaaa 32,196,228\ns_add_u32 m0, s47, 16384\ns_add_u32 s44, s44, 0x40000\nZaaa 36,204,228\nRa 132,11,32768\n"
"Maaa 36,200,224\nZaaa 96,208,224\nRa 136,10,34816\nMaaa 96,212,228\nD0 6,24,44,0\nZaaa 100,220,228\nRa 140,11,34816\nMaaa 100,216,224\nZaaa 108,216,232\nRa 144,10,40960\n"
"Maaa 108,220,236\nZaaa 104,212,236\nRa 148,11,40960\nMaaa 104,208,232\nZaaa 44,200,232\nRa 152,10,43008\nMaaa 44,204,236\nD0 7,24,44,1024\nZaaa 40,196,236\nRa 156,11,43008\n"
"Maaa 40,192,232\nZaaa 48,192,240\nMaaa 48,196,244\nZaaa 52,204,244\nMaaa 52,200,240\nZaaa 112,208,240\nMaaa 112,212,244\nD0 8,24,44,2048\nZaaa 116,220,244\nMaaa 116,216,240\n"
"Zaaa 124,216,248\nMaaa 124,220,252\nZaaa 120,212,252\nMaaa 120,208,248\nZaaa 60,200,248\nMaaa 60,204,252\nD0 9,24,44,3072\nZaaa 56,196,252\nMaaa 56,192,248\n"
"s_waitcnt vmcnt(25) lgkmcnt(0)\ns_barrier\nZaaa 0,192,160\nTa 224,16,32768\nWV 17\nMaaa 0,196,164\nTa 226,16,34816\nZaaa 4,204,164\ns_add_u32 m0, s47, -49152\n"
"s_add_u32 s48, s48, 128\nMaaa 4,200,160\nTa 228,16,49152\nZaaa 64,208,160\nTa 230,16,51200\nbuffer_load_dword v31, v24, s[24:27], s96 offen\ns_add_u32 s96, s96, 0x80000\n"
"Maaa 64,212,164\nD0 2,20,48,0\nv_lshlrev_b32 v48, 16, v32\nZaaa 68,220,164\nTa 232,17,32768\nMaaa 68,216,160\nTa 234,17,34816\nv_lshlrev_b32 v49, 16, v36\nZaaa 76,216,168\n"
"v_lshlrev_b32 v50, 16, v40\nv_lshlrev_b32 v51, 16, v44\nMaaa 76,220,172\nTa 236,17,49152\nZaaa 72,212,172\nTa 238,17,51200\nMU 52,48,50\nMaaa 72,208,168\nMU 53,49,50\n"
"Zaaa 12,200,168\nTa 240,18,32768\nCV 52,52,53\nMaaa 12,204,172\nTa 242,18,34816\nv_lshlrev_b32 v54, 16, v52\nZaaa 8,196,172\nD0 3,20,48,1024\nMaaa 8,192,168\nTa 244,18,49152\n"
"v_and_b32 v55, 0xffff0000, v52\nZaaa 16,192,176\nTa 246,18,51200\nMU 56,48,51\nMaaa 16,196,180\nMU 57,49,51\nZaaa 20,204,180\nTa 248,19,32768\nv_and_b32 v48, 0xffff0000, v32\n"
"Maaa 20,200,176\nTa 250,19,34816\nZaaa 80,208,176\nv_and_b32 v49, 0xffff0000, v36\nv_and_b32 v50, 0xffff0000, v40\nMaaa 80,212,180\nTa 252,19,49152\n"
"v_and_b32 v51, 0xffff0000, v44\nZaaa 84,220,180\nTa 254,19,51200\nMaaa 84,216,176\nD0 4,20,48,2048\nMU 52,48,50\nZaaa 92,216,184\nMU 53,49,50\nCV 52,52,53\nMaaa 92,220,188\n"
"v_and_b32 v53, 0xffff0000, v52\nZaaa 88,212,188\nv_lshlrev_b32 v52, 16, v52\nFM 56,48,51\nMaaa 88,208,184\nFM 57,49,51\nZaaa 28,200,184\nAD 54,54,52\nAD 55,55,53\nMaaa 28,204,188\n"
"D0 5,20,48,3072\nv_lshlrev_b32 v48, 16, v33\nZaaa 24,196,188\nv_lshlrev_b32 v49, 16, v37\nMaaa 24,192,184\nWL 0\nMvaa 96,128,224\nTa 160,12,32768\nv_lshlrev_b32 v50, 16, v41\n"
"Mvaa 96,132,228\nTa 162,12,34816\nMvaa 100,140,228\ns_add_u32 m0, s47, -32768\ns_add_u32 s45, s45, 128\nMvaa 100,136,224\nTa 164,12,49152\nMvaa 160,144,224\nTa 166,12,51200\n"
"v_lshlrev_b32 v51, 16, v45\nMvaa 160,148,228\nD0 2,20,45,0\nMU 52,48,50\nMvaa 164,156,228\nTa 168,13,32768\nMvaa 164,152,224\nTa 170,13,34816\nMU 53,49,50\nMvaa 172,152,232\n"
"CV 52,52,53\nv_and_b32 v53, 0xffff0000, v52\nMvaa 172,156,236\nTa 172,13,49152\nMvaa 168,148,236\nTa 174,13,51200\nv_lshlrev_b32 v52, 16, v52\nMvaa 168,144,232\nFM 56,48,51\n"
"Mvaa 108,136,232\nTa 176,14,32768\nFM 57,49,51\nMvaa 108,140,236\nTa 178,14,34816\nAD 54,54,52\nMvaa 104,132,236\nD0 3,20,45,1024\nMvaa 104,128,232\nTa 180,14,49152\nAD 55,55,53\n"
"Mvaa 112,128,240\nTa 182,14,51200\nv_and_b32 v48, 0xffff0000, v33\nMvaa 112,132,244\nv_and_b32 v49, 0xffff0000, v37\nMvaa 116,140,244\nTa 184,15,32768\n"
"v_and_b32 v50, 0xffff0000, v41\nMvaa 116,136,240\nTa 186,15,34816\nMvaa 176,144,240\nv_and_b32 v51, 0xffff0000, v45\nMU 52,48,50\nMvaa 176,148,244\nTa 188,15,49152\nMU 53,49,50\n"
"Mvaa 180,156,244\nTa 190,15,51200\nMvaa 180,152,240\nD0 4,20,45,2048\nCV 52,52,53\nMvaa 188,152,248\nv_and_b32 v53, 0xffff0000, v52\nv_lshlrev_b32 v52, 16, v52\nMvaa 188,156,252\n"
"FM 56,48,51\nMvaa 184,148,252\nFM 57,49,51\nAD 54,54,52\nMvaa 184,144,248\nAD 55,55,53\nMvaa 124,136,248\nv_lshlrev_b32 v48, 16, v34\nv_lshlrev_b32 v49, 16, v38\nMvaa 124,140,252\n"
"D0 5,20,45,3072\nv_lshlrev_b32 v50, 16, v42\nMvaa 120,132,252\nv_lshlrev_b32 v51, 16, v46\nMvaa 120,128,248\ns_waitcnt vmcnt(18) lgkmcnt(0)\ns_barrier\nMvaa 64,128,160\n"
"Ra 192,10,49152\nMU 52,48,50\nMvaa 64,132,164\ns_add_u32 m0, s47, 32768\ns_add_u32 s44, s44, 0x40000\nMvaa 68,140,164\nRa 196,11,49152\nMU 53,49,50\nMvaa 68,136,160\nCV 52,52,53\n"
"Mvaa 128,144,160\nRa 200,10,51200\nv_and_b32 v53, 0xffff0000, v52\nMvaa 128,148,164\nD0 6,24,44,0\nv_lshlrev_b32 v52, 16, v52\nMvaa 132,156,164\nRa 204,11,51200\nMvaa 132,152,160\n"
"FM 56,48,51\nFM 57,49,51\nMvaa 140,152,168\nRa 208,10,57344\nAD 54,54,52\nMvaa 140,156,172\nAD 55,55,53\nMvaa 136,148,172\nRa 212,11,57344\nv_and_b32 v48, 0xffff0000, v34\n"
"Mvaa 136,144,168\nv_and_b32 v49, 0xffff0000, v38\nMvaa 76,136,168\nRa 216,10,59392\nv_and_b32 v50, 0xffff0000, v42\nMvaa 76,140,172\nD0 7,24,44,1024\n"
"v_and_b32 v51, 0xffff0000, v46\nMvaa 72,132,172\nRa 220,11,59392\nMvaa 72,128,168\nMU 52,48,50\nMU 53,49,50\nMvaa 80,128,176\nCV 52,52,53\nv_and_b32 v53, 0xffff0000, v52\n"
"Mvaa 80,132,180\nv_lshlrev_b32 v52, 16, v52\nMvaa 84,140,180\nFM 56,48,51\nFM 57,49,51\nMvaa 84,136,176\nAD 54,54,52\nMvaa 144,144,176\nAD 55,55,53\nv_lshlrev_b32 v48, 16, v35\n"
"Mvaa 144,148,180\nD0 8,24,44,2048\nv_lshlrev_b32 v49, 16, v39\nMvaa 148,156,180\nv_lshlrev_b32 v50, 16, v43\nMvaa 148,152,176\nv_lshlrev_b32 v51, 16, v47\nMU 52,48,50\n"
"Mvaa 156,152,184\nMU 53,49,50\nCV 52,52,53\nMvaa 156,156,188\nv_and_b32 v53, 0xffff0000, v52\nMvaa 152,148,188\nv_lshlrev_b32 v52, 16, v52\nFM 56,48,51\nMvaa 152,144,184\n"
"FM 57,49,51\nMvaa 92,136,184\nAD 54,54,52\nAD 55,55,53\nMvaa 92,140,188\nD0 9,24,44,3072\nv_and_b32 v48, 0xffff0000, v35\nMvaa 88,132,188\nv_and_b32 v49, 0xffff0000, v39\n"
"Mvaa 88,128,184\nWL 0\nMaaa 0,192,160\nRa 128,10,0\nv_and_b32 v50, 0xffff0000, v43\nMaaa 0,196,164\ns_add_u32 m0, s47, 49152\ns_add_u32 s44, s44, 0x40000\nMaaa 4,204,164\n"
"Ra 132,11,0\nv_and_b32 v51, 0xffff0000, v47\nMaaa 4,200,160\nMU 52,48,50\nMaaa 64,208,160\nRa 136,10,2048\nMU 53,49,50\nMaaa 64,212,164\nD0 6,24,44,0\nCV 52,52,53\n"
"Maaa 68,220,164\nRa 140,11,2048\nMaaa 68,216,160\nv_and_b32 v53, 0xffff0000, v52\nv_lshlrev_b32 v52, 16, v52\nMaaa 76,216,168\nRa 144,10,8192\nFM 56,48,51\nMaaa 76,220,172\n"
"FM 57,49,51\n.W585:\ns_cbranch_execz .W589\nMaaa 72,212,172\nRa 148,11,8192\nAD 54,54,52\nMaaa 72,208,168\nAD 55,55,53\nMaaa 12,200,168\nRa 152,10,10240\n"
"v_permlane32_swap_b32 v54, v55\nMaaa 12,204,172\nD0 7,24,44,1024\nv_permlane32_swap_b32 v56, v57\nMaaa 8,196,172\nRa 156,11,10240\nMaaa 8,192,168\nAD 54,54,55\nAD 56,56,57\n"
"Maaa 16,192,176\nMaaa 16,196,180\nv_permlane16_swap_b32 v54, v56\nMaaa 20,204,180\nAD 54,54,56\nMaaa 20,200,176\nMaaa 80,208,176\n"
"v_add_f32_dpp v54, v54, v54 row_ror:8 row_mask:0xf bank_mask:0xf\nMaaa 80,212,180\nD0 8,24,44,2048\nMaaa 84,220,180\n"
"v_add_f32_dpp v54, v54, v54 row_ror:4 row_mask:0xf bank_mask:0xf\nMaaa 84,216,176\nMaaa 92,216,184\nv_add_f32_dpp v54, v54, v54 row_ror:2 row_mask:0xf bank_mask:0xf\n"
"Maaa 92,220,188\nMaaa 88,212,188\nv_add_f32_dpp v54, v54, v54 row_ror:1 row_mask:0xf bank_mask:0xf\nCV 54,54,54\nMaaa 88,208,184\nv_lshlrev_b32 v54, 16, v54\nMaaa 28,200,184\n"
"v_mul_f32 v55, 0xbfb8aa3b, v54\nMaaa 28,204,188\nD0 9,24,44,3072\nMaaa 24,196,188\nEX 55,55\nMaaa 24,192,184\ns_waitcnt vmcnt(12) lgkmcnt(0)\ns_barrier\nMaaa 32,192,224\n"
"Ta 160,12,0\nv_add_f32 v56, 0, v54\nMaaa 32,196,228\nTa 162,12,2048\nMaaa 36,204,228\ns_add_u32 m0, s47, -16384\ns_add_u32 s48, s48, 128\nMaaa 36,200,224\nTa 164,12,16384\n"
"Maaa 96,208,224\nTa 166,12,18432\nbuffer_load_dword v31, v24, s[24:27], s96 offen\ns_add_u32 s96, s96, 0x80000\nMaaa 96,212,228\nD0 2,20,48,0\nv_add_f32 v55, 1.0, v55\n"
"Maaa 100,220,228\nTa 168,13,0\nv_rcp_f32 v55, v55\nMaaa 100,216,224\nTa 170,13,2048\nv_ashrrev_i32 v57, 31, v56\nMaaa 108,216,232\nCV 55,55,55\nv_lshlrev_b32 v55, 16, v55\n"
"Maaa 108,220,236\nTa 172,13,16384\nMaaa 104,212,236\nTa 174,13,18432\nv_or_b32 v57, 0x80000000, v57\nMaaa 104,208,232\nv_xor_b32 v56, v56, v57\nMaaa 44,200,232\nTa 176,14,0\n"
"v_and_b32 v56, 0xffff0000, v56\nMaaa 44,204,236\nTa 178,14,2048\ns_lshl2_add_u32 s2, s10, s11\nMaaa 40,196,236\nD0 3,20,48,1024\nMaaa 40,192,232\nTa 180,14,16384\n"
"v_lshrrev_b32 v48, 5, v0\nMaaa 48,192,240\nTa 182,14,18432\nv_lshl_add_u32 v48, s2, 1, v48\nMaaa 48,196,244\nv_sub_u32 v49, 0xffff, v48\nMaaa 52,204,244\nTa 184,15,0\n"
"v_or_b32 v56, v56, v49\nMaaa 52,200,240\nTa 186,15,2048\nMaaa 112,208,240\nv_mov_b32 v57, s15\nv_and_b32 v49, 31, v0\nMaaa 112,212,244\nTa 188,15,16384\nv_mov_b32 v52, 0x40000000\n"
"Maaa 116,220,244\nTa 190,15,18432\nv_cmp_eq_u32 vcc, 16, v49\nv_lshlrev_b32 v50, 2, v48\nv_lshlrev_b32 v51, 10, v49\nv_cndmask_b32 v50, v52, v50, vcc\n"
"v_cndmask_b32 v56, v55, v56, vcc\nMaaa 116,216,240\nD0 4,20,48,2048\nv_lshl_add_u32 v51, v48, 3, v51\nMaaa 124,216,248\nv_and_b32 v49, 15, v0\nv_cmp_eq_u32 vcc, 0, v49\n"
"s_mov_b32 s2, 0xc040000\ns_mov_b32 s3, 0xc03e000\nv_cndmask_b32 v51, v52, v51, vcc\nbuffer_store_dwordx2 v[56:57], v51, s[16:19], s2 offen sc0 sc1\n"
"buffer_store_dword v54, v50, s[16:19], s3 offen sc0 sc1\nMaaa 124,220,252\nMaaa 120,212,252\nMaaa 120,208,248\nMaaa 60,200,248\nMaaa 60,204,252\nD0 5,20,48,3072\nMaaa 56,196,252\n"
"Maaa 56,192,248\nWL 0\ns_mov_b32 s50, 3\n.GL415:\nMvaa 64,128,160\nTa 224,16,0\nMvaa 64,132,164\nTa 226,16,2048\nMvaa 68,140,164\nTa 228,16,16384\ns_add_u32 m0, s47, -65536\n"
"s_add_u32 s45, s45, 128\nMvaa 68,136,160\nTa 230,16,18432\nMvaa 128,144,160\nTa 232,17,0\nMvaa 128,148,164\nTa 234,17,2048\nD0 2,20,45,0\nMvaa 132,156,164\nTa 236,17,16384\n"
"Mvaa 132,152,160\nTa 238,17,18432\nMvaa 140,152,168\nTa 240,18,0\nMvaa 140,156,172\nTa 242,18,2048\nMvaa 136,148,172\nTa 244,18,16384\nMvaa 136,144,168\nTa 246,18,18432\n"
"Mvaa 76,136,168\nTa 248,19,0\nMvaa 76,140,172\nTa 250,19,2048\nMvaa 72,132,172\nTa 252,19,16384\nD0 3,20,45,1024\nMvaa 72,128,168\nTa 254,19,18432\nMvaa 80,128,176\n"
"Ra 192,10,16384\nMvaa 80,132,180\nRa 196,11,16384\nMvaa 84,140,180\nRa 200,10,18432\nMvaa 84,136,176\nRa 204,11,18432\nMvaa 144,144,176\nRa 208,10,24576\nMvaa 144,148,180\n"
"Ra 212,11,24576\nMvaa 148,156,180\nRa 216,10,26624\nMvaa 148,152,176\nRa 220,11,26624\nD0 4,20,45,2048\nMvaa 156,152,184\nMvaa 156,156,188\nMvaa 152,148,188\nMvaa 152,144,184\n"
"Mvaa 92,136,184\nMvaa 92,140,188\nD0 5,20,45,3072\nMvaa 88,132,188\nMvaa 88,128,184\ns_waitcnt vmcnt(18) lgkmcnt(8)\ns_barrier\nMvaa 96,128,224\nMvaa 96,132,228\n"
"s_add_u32 m0, s47, 0\ns_add_u32 s44, s44, 0x40000\ns_mov_b32 s86, 1\ns_load_dword s86, s[54:55], s94\nMvaa 100,140,228\ns_add_u32 s94, s94, 1024\nMvaa 100,136,224\n"
"Mvaa 160,144,224\nMvaa 160,148,228\nD0 6,24,44,0\nMvaa 164,156,228\nMvaa 164,152,224\nMvaa 172,152,232\nMvaa 172,156,236\nMvaa 168,148,236\nMvaa 168,144,232\n"
"s_add_u32 s88, s88, s86\nMvaa 108,136,232\nMvaa 108,140,236\nD0 7,24,44,1024\ns_add_u32 s88, s88, s86\nMvaa 104,132,236\nMvaa 104,128,232\ns_add_u32 s88, s88, s86\n"
"Mvaa 112,128,240\nMvaa 112,132,244\ns_add_u32 s88, s88, s86\nMvaa 116,140,244\nMvaa 116,136,240\ns_add_u32 s88, s88, s86\nMvaa 176,144,240\nMvaa 176,148,244\nD0 8,24,44,2048\n"
"s_add_u32 s88, s88, s86\nMvaa 180,156,244\nMvaa 180,152,240\ns_add_u32 s88, s88, s86\nMvaa 188,152,248\nMvaa 188,156,252\ns_add_u32 s88, s88, s86\nMvaa 184,148,252\n"
"Mvaa 184,144,248\ns_add_u32 s88, s88, s86\nMvaa 124,136,248\nMvaa 124,140,252\nD0 9,24,44,3072\ns_add_u32 s88, s88, s86\nMvaa 120,132,252\nMvaa 120,128,248\nWL 0\n"
"s_add_u32 s88, s88, s86\nMaaa 32,192,224\nRa 128,10,32768\nMaaa 32,196,228\ns_add_u32 m0, s47, 16384\ns_add_u32 s44, s44, 0x40000\ns_add_u32 s88, s88, s86\nMaaa 36,204,228\n"
"Ra 132,11,32768\nMaaa 36,200,224\ns_add_u32 s88, s88, s86\nMaaa 96,208,224\nRa 136,10,34816\nMaaa 96,212,228\nD0 6,24,44,0\ns_add_u32 s88, s88, s86\nMaaa 100,220,228\n"
"Ra 140,11,34816\nMaaa 100,216,224\ns_add_u32 s88, s88, s86\nMaaa 108,216,232\nRa 144,10,40960\nMaaa 108,220,236\ns_add_u32 s88, s88, s86\nMaaa 104,212,236\nRa 148,11,40960\n"
"Maaa 104,208,232\nMaaa 44,200,232\nRa 152,10,43008\nMaaa 44,204,236\nD0 7,24,44,1024\nMaaa 40,196,236\nRa 156,11,43008\nMaaa 40,192,232\nMaaa 48,192,240\nMaaa 48,196,244\n"
"Maaa 52,204,244\nMaaa 52,200,240\nMaaa 112,208,240\nMaaa 112,212,244\nD0 8,24,44,2048\nMaaa 116,220,244\nMaaa 116,216,240\nMaaa 124,216,248\nMaaa 124,220,252\nMaaa 120,212,252\n"
"Maaa 120,208,248\nMaaa 60,200,248\nMaaa 60,204,252\nD0 9,24,44,3072\nMaaa 56,196,252\nMaaa 56,192,248\ns_waitcnt vmcnt(12) lgkmcnt(0)\ns_barrier\nMaaa 0,192,160\nTa 224,16,32768\n"
"s_mov_b32 m0, s76\ns_add_u32 s76, s76, 1024\nMaaa 0,196,164\nTa 226,16,34816\nD0 1,36,75,0\ns_add_u32 s75, s75, 1024\nMaaa 4,204,164\ns_add_u32 m0, s47, -49152\n"
"s_add_u32 s48, s48, 128\nMaaa 4,200,160\nTa 228,16,49152\nMaaa 64,208,160\nTa 230,16,51200\nbuffer_load_dword v31, v24, s[24:27], s96 offen\ns_add_u32 s96, s96, 0x80000\n"
"Maaa 64,212,164\nD0 2,20,48,0\nMaaa 68,220,164\nTa 232,17,32768\nMaaa 68,216,160\nTa 234,17,34816\nMaaa 76,216,168\nMaaa 76,220,172\nTa 236,17,49152\nMaaa 72,212,172\n"
"Ta 238,17,51200\nMaaa 72,208,168\nMaaa 12,200,168\nTa 240,18,32768\nMaaa 12,204,172\nTa 242,18,34816\nMaaa 8,196,172\nD0 3,20,48,1024\nMaaa 8,192,168\nTa 244,18,49152\n"
"Maaa 16,192,176\nTa 246,18,51200\nMaaa 16,196,180\nMaaa 20,204,180\nTa 248,19,32768\nMaaa 20,200,176\nTa 250,19,34816\nMaaa 80,208,176\nMaaa 80,212,180\nTa 252,19,49152\n"
"Maaa 84,220,180\nTa 254,19,51200\nMaaa 84,216,176\nD0 4,20,48,2048\nMaaa 92,216,184\nMaaa 92,220,188\nMaaa 88,212,188\nMaaa 88,208,184\nMaaa 28,200,184\nMaaa 28,204,188\n"
"D0 5,20,48,3072\nMaaa 24,196,188\nMaaa 24,192,184\nWL 0\nMvaa 96,128,224\nTa 160,12,32768\nMvaa 96,132,228\nTa 162,12,34816\nMvaa 100,140,228\nTa 164,12,49152\n"
"s_add_u32 m0, s47, -32768\ns_add_u32 s45, s45, 128\nMvaa 100,136,224\nTa 166,12,51200\nMvaa 160,144,224\nTa 168,13,32768\nMvaa 160,148,228\nTa 170,13,34816\nD0 2,20,45,0\n"
"Mvaa 164,156,228\nTa 172,13,49152\nMvaa 164,152,224\nTa 174,13,51200\nMvaa 172,152,232\nTa 176,14,32768\nMvaa 172,156,236\nTa 178,14,34816\nMvaa 168,148,236\nTa 180,14,49152\n"
"Mvaa 168,144,232\nTa 182,14,51200\nMvaa 108,136,232\nTa 184,15,32768\nMvaa 108,140,236\nTa 186,15,34816\nMvaa 104,132,236\nTa 188,15,49152\nD0 3,20,45,1024\nMvaa 104,128,232\n"
"Ta 190,15,51200\nMvaa 112,128,240\nRa 192,10,49152\nMvaa 112,132,244\nRa 196,11,49152\nMvaa 116,140,244\nRa 200,10,51200\nMvaa 116,136,240\nRa 204,11,51200\nMvaa 176,144,240\n"
"Ra 208,10,57344\nMvaa 176,148,244\nRa 212,11,57344\nMvaa 180,156,244\nRa 216,10,59392\nMvaa 180,152,240\nRa 220,11,59392\nD0 4,20,45,2048\nMvaa 188,152,248\nMvaa 188,156,252\n"
"Mvaa 184,148,252\nMvaa 184,144,248\nMvaa 124,136,248\nMvaa 124,140,252\nD0 5,20,45,3072\nMvaa 120,132,252\nMvaa 120,128,248\ns_waitcnt vmcnt(18) lgkmcnt(8)\ns_barrier\n"
"Mvaa 64,128,160\nMvaa 64,132,164\ns_add_u32 m0, s47, 32768\ns_add_u32 s44, s44, 0x40000\ns_mov_b32 s86, 1\ns_load_dword s86, s[54:55], s95\nMvaa 68,140,164\n"
"s_add_u32 s95, s95, 1024\nMvaa 68,136,160\nMvaa 128,144,160\nMvaa 128,148,164\nD0 6,24,44,0\nMvaa 132,156,164\nMvaa 132,152,160\nMvaa 140,152,168\nMvaa 140,156,172\n"
"Mvaa 136,148,172\nMvaa 136,144,168\ns_sub_u32 s88, s88, s86\nMvaa 76,136,168\nMvaa 76,140,172\nD0 7,24,44,1024\ns_sub_u32 s88, s88, s86\nMvaa 72,132,172\nMvaa 72,128,168\n"
"s_sub_u32 s88, s88, s86\nMvaa 80,128,176\nMvaa 80,132,180\ns_sub_u32 s88, s88, s86\nMvaa 84,140,180\nMvaa 84,136,176\ns_sub_u32 s88, s88, s86\nMvaa 144,144,176\nMvaa 144,148,180\n"
"D0 8,24,44,2048\ns_sub_u32 s88, s88, s86\nMvaa 148,156,180\nMvaa 148,152,176\ns_sub_u32 s88, s88, s86\nMvaa 156,152,184\nMvaa 156,156,188\ns_sub_u32 s88, s88, s86\n"
"Mvaa 152,148,188\nMvaa 152,144,184\ns_sub_u32 s88, s88, s86\nMvaa 92,136,184\nMvaa 92,140,188\nD0 9,24,44,3072\ns_sub_u32 s88, s88, s86\nMvaa 88,132,188\nMvaa 88,128,184\nWL 0\n"
"s_sub_u32 s88, s88, s86\nMaaa 0,192,160\nRa 128,10,0\nMaaa 0,196,164\ns_add_u32 m0, s47, 49152\ns_add_u32 s44, s44, 0x40000\ns_sub_u32 s88, s88, s86\nMaaa 4,204,164\nRa 132,11,0\n"
"Maaa 4,200,160\ns_sub_u32 s88, s88, s86\nMaaa 64,208,160\nRa 136,10,2048\nMaaa 64,212,164\nD0 6,24,44,0\ns_sub_u32 s88, s88, s86\nMaaa 68,220,164\nRa 140,11,2048\nMaaa 68,216,160\n"
"s_sub_u32 s88, s88, s86\nMaaa 76,216,168\nRa 144,10,8192\nMaaa 76,220,172\ns_sub_u32 s88, s88, s86\nMaaa 72,212,172\nRa 148,11,8192\nMaaa 72,208,168\nMaaa 12,200,168\n"
"Ra 152,10,10240\nMaaa 12,204,172\nD0 7,24,44,1024\nMaaa 8,196,172\nRa 156,11,10240\nMaaa 8,192,168\nMaaa 16,192,176\nMaaa 16,196,180\nMaaa 20,204,180\nMaaa 20,200,176\n"
"Maaa 80,208,176\nMaaa 80,212,180\nD0 8,24,44,2048\nMaaa 84,220,180\nMaaa 84,216,176\nMaaa 92,216,184\nMaaa 92,220,188\nMaaa 88,212,188\nMaaa 88,208,184\nMaaa 28,200,184\n"
"Maaa 28,204,188\nD0 9,24,44,3072\nMaaa 24,196,188\nMaaa 24,192,184\ns_waitcnt vmcnt(12) lgkmcnt(0)\ns_barrier\nMaaa 32,192,224\nTa 160,12,0\ns_mov_b32 m0, s76\n"
"s_add_u32 s76, s76, 1024\nMaaa 32,196,228\nTa 162,12,2048\nD0 1,36,75,0\ns_add_u32 s75, s75, 1024\nMaaa 36,204,228\ns_add_u32 m0, s47, -16384\ns_add_u32 s48, s48, 128\n"
"Maaa 36,200,224\nTa 164,12,16384\nMaaa 96,208,224\nTa 166,12,18432\nbuffer_load_dword v31, v24, s[24:27], s96 offen\ns_add_u32 s96, s96, 0x80000\nMaaa 96,212,228\n.W586:\n"
"s_cbranch_execz .W590\nD0 2,20,48,0\nMaaa 100,220,228\nTa 168,13,0\nMaaa 100,216,224\nTa 170,13,2048\nMaaa 108,216,232\nMaaa 108,220,236\nTa 172,13,16384\nMaaa 104,212,236\n"
"Ta 174,13,18432\nMaaa 104,208,232\nMaaa 44,200,232\nTa 176,14,0\nMaaa 44,204,236\nTa 178,14,2048\nMaaa 40,196,236\nD0 3,20,48,1024\nMaaa 40,192,232\nTa 180,14,16384\n"
"Maaa 48,192,240\nTa 182,14,18432\nMaaa 48,196,244\nMaaa 52,204,244\nTa 184,15,0\nMaaa 52,200,240\nTa 186,15,2048\nMaaa 112,208,240\nMaaa 112,212,244\nTa 188,15,16384\n"
"Maaa 116,220,244\nTa 190,15,18432\nMaaa 116,216,240\nD0 4,20,48,2048\nMaaa 124,216,248\nMaaa 124,220,252\nMaaa 120,212,252\nMaaa 120,208,248\nMaaa 60,200,248\n"
"s_xor_b32 s94, s94, 0x2000\ns_xor_b32 s95, s95, 0x2000\ns_sub_u32 s88, 0, s88\nMaaa 60,204,252\nD0 5,20,48,3072\ns_sub_u32 s50, s50, 1\nMaaa 56,196,252\nMaaa 56,192,248\nWL 0\n"
"s_cbranch_scc0 .GL415\nMvaa 64,128,160\nTa 224,16,0\nMvaa 64,132,164\nTa 226,16,2048\ns_cmp_gt_i32 s88, 0\ns_cselect_b32 s88, 0x2000, 0\nMvaa 68,140,164\n"
"s_add_u32 m0, s47, -65536\ns_add_u32 s45, s45, 128\nMvaa 68,136,160\nTa 228,16,16384\nMvaa 128,144,160\nTa 230,16,18432\nMvaa 128,148,164\nD0 2,20,45,0\nMvaa 132,156,164\n"
"Ta 232,17,0\nMvaa 132,152,160\nTa 234,17,2048\nMvaa 140,152,168\ns_xor_b32 s88, s88, s92\nMvaa 140,156,172\nTa 236,17,16384\nMvaa 136,148,172\nTa 238,17,18432\nMvaa 136,144,168\n"
"v_xor_b32 v27, s88, v25\nMvaa 76,136,168\nTa 240,18,0\nMvaa 76,140,172\nTa 242,18,2048\nMvaa 72,132,172\nD0 3,20,45,1024\nMvaa 72,128,168\nTa 244,18,16384\nMvaa 80,128,176\n"
"Ta 246,18,18432\nMvaa 80,132,180\nbuffer_store_dword v27, off, s[16:19], s93 sc0 sc1\nMvaa 84,140,180\nTa 248,19,0\nMvaa 84,136,176\nTa 250,19,2048\nMvaa 144,144,176\n"
"Mvaa 144,148,180\nTa 252,19,16384\nMvaa 148,156,180\nTa 254,19,18432\nMvaa 148,152,176\nD0 4,20,45,2048\nMvaa 156,152,184\nMvaa 156,156,188\nMvaa 152,148,188\nMvaa 152,144,184\n"
"Mvaa 92,136,184\nMvaa 92,140,188\nD0 5,20,45,3072\nMvaa 88,132,188\nMvaa 88,128,184\ns_waitcnt vmcnt(19) lgkmcnt(0)\ns_barrier\nMvaa 96,128,224\nRa 192,10,16384\nMvaa 96,132,228\n"
"s_add_u32 m0, s47, 0\ns_add_u32 s44, s44, 0x40000\nMvaa 100,140,228\nRa 196,11,16384\nMvaa 100,136,224\nMvaa 160,144,224\nRa 200,10,18432\nMvaa 160,148,228\nD0 6,24,44,0\n"
"Mvaa 164,156,228\nRa 204,11,18432\nMvaa 164,152,224\nMvaa 172,152,232\nRa 208,10,24576\nMvaa 172,156,236\nMvaa 168,148,236\nRa 212,11,24576\nMvaa 168,144,232\nMvaa 108,136,232\n"
"Ra 216,10,26624\nMvaa 108,140,236\nD0 7,24,44,1024\nMvaa 104,132,236\nRa 220,11,26624\nMvaa 104,128,232\nMvaa 112,128,240\nMvaa 112,132,244\nMvaa 116,140,244\nMvaa 116,136,240\n"
"Mvaa 176,144,240\nMvaa 176,148,244\nD0 8,24,44,2048\nMvaa 180,156,244\nMvaa 180,152,240\nMvaa 188,152,248\nMvaa 188,156,252\nMvaa 184,148,252\nMvaa 184,144,248\nMvaa 124,136,248\n"
"Mvaa 124,140,252\nD0 9,24,44,3072\nMvaa 120,132,252\nMvaa 120,128,248\nWL 0\nMaaa 32,192,224\nRa 128,10,32768\nMaaa 32,196,228\ns_add_u32 m0, s47, 16384\n"
"s_add_u32 s44, s44, 0x40000\nMaaa 36,204,228\nRa 132,11,32768\nMaaa 36,200,224\nMaaa 96,208,224\nRa 136,10,34816\nMaaa 96,212,228\nD0 6,24,44,0\nMaaa 100,220,228\nRa 140,11,34816\n"
"Maaa 100,216,224\nMaaa 108,216,232\nRa 144,10,40960\nMaaa 108,220,236\nMaaa 104,212,236\nRa 148,11,40960\nMaaa 104,208,232\nMaaa 44,200,232\nRa 152,10,43008\nMaaa 44,204,236\n"
"D0 7,24,44,1024\nMaaa 40,196,236\nRa 156,11,43008\nMaaa 40,192,232\nMaaa 48,192,240\nMaaa 48,196,244\nMaaa 52,204,244\nMaaa 52,200,240\nMaaa 112,208,240\nMaaa 112,212,244\n"
"D0 8,24,44,2048\nMaaa 116,220,244\nMaaa 116,216,240\nMaaa 124,216,248\nMaaa 124,220,252\nMaaa 120,212,252\nMaaa 120,208,248\nMaaa 60,200,248\nMaaa 60,204,252\nD0 9,24,44,3072\n"
"Maaa 56,196,252\nMaaa 56,192,248\ns_waitcnt vmcnt(19) lgkmcnt(0)\ns_barrier\nMaaa 0,192,160\nTa 224,16,32768\nMaaa 0,196,164\nTa 226,16,34816\nMaaa 4,204,164\n"
"s_add_u32 m0, s47, -49152\ns_add_u32 s48, s48, 128\nMaaa 4,200,160\nTa 228,16,49152\nMaaa 64,208,160\nTa 230,16,51200\nbuffer_load_dword v31, v24, s[24:27], s96 offen\n"
"s_add_u32 s96, s96, 0x80000\nMaaa 64,212,164\nD0 2,20,48,0\nMaaa 68,220,164\nTa 232,17,32768\nMaaa 68,216,160\nTa 234,17,34816\nMaaa 76,216,168\nMaaa 76,220,172\nTa 236,17,49152\n"
"Maaa 72,212,172\nTa 238,17,51200\nMaaa 72,208,168\nMaaa 12,200,168\nTa 240,18,32768\nMaaa 12,204,172\nTa 242,18,34816\nMaaa 8,196,172\nD0 3,20,48,1024\nMaaa 8,192,168\n"
"Ta 244,18,49152\nMaaa 16,192,176\nTa 246,18,51200\nMaaa 16,196,180\nMaaa 20,204,180\nTa 248,19,32768\nMaaa 20,200,176\nTa 250,19,34816\nMaaa 80,208,176\nMaaa 80,212,180\n"
"Ta 252,19,49152\nMaaa 84,220,180\nTa 254,19,51200\nMaaa 84,216,176\nD0 4,20,48,2048\nMaaa 92,216,184\nMaaa 92,220,188\nMaaa 88,212,188\nMaaa 88,208,184\nMaaa 28,200,184\n"
"Maaa 28,204,188\nD0 5,20,48,3072\nMaaa 24,196,188\nMaaa 24,192,184\nWL 0\nMvaa 96,128,224\nTa 160,12,32768\nMvaa 96,132,228\nTa 162,12,34816\nMvaa 100,140,228\n"
"s_add_u32 m0, s47, -32768\ns_add_u32 s45, s45, 128\nMvaa 100,136,224\nTa 164,12,49152\nMvaa 160,144,224\nTa 166,12,51200\nMvaa 160,148,228\nD0 2,20,45,0\nMvaa 164,156,228\n"
"Ta 168,13,32768\nMvaa 164,152,224\nTa 170,13,34816\nMvaa 172,152,232\nMvaa 172,156,236\nTa 172,13,49152\nMvaa 168,148,236\nTa 174,13,51200\nMvaa 168,144,232\nMvaa 108,136,232\n"
"Ta 176,14,32768\nMvaa 108,140,236\nTa 178,14,34816\nMvaa 104,132,236\nD0 3,20,45,1024\nMvaa 104,128,232\nTa 180,14,49152\nMvaa 112,128,240\nTa 182,14,51200\nMvaa 112,132,244\n"
"Mvaa 116,140,244\nTa 184,15,32768\nMvaa 116,136,240\nTa 186,15,34816\nMvaa 176,144,240\nMvaa 176,148,244\nTa 188,15,49152\nMvaa 180,156,244\nTa 190,15,51200\nMvaa 180,152,240\n"
"D0 4,20,45,2048\nMvaa 188,152,248\nMvaa 188,156,252\nMvaa 184,148,252\nMvaa 184,144,248\nMvaa 124,136,248\nMvaa 124,140,252\nD0 5,20,45,3072\nMvaa 120,132,252\nMvaa 120,128,248\n"
"s_waitcnt vmcnt(17) lgkmcnt(0)\ns_barrier\nMvaa 64,128,160\nRa 192,10,49152\nMvaa 64,132,164\ns_add_u32 m0, s47, 32768\ns_add_u32 s44, s44, 0x40000\nMvaa 68,140,164\n"
"Ra 196,11,49152\nMvaa 68,136,160\nMvaa 128,144,160\nRa 200,10,51200\nMvaa 128,148,164\nD0 6,24,44,0\nMvaa 132,156,164\nRa 204,11,51200\nMvaa 132,152,160\nMvaa 140,152,168\n"
"Ra 208,10,57344\nMvaa 140,156,172\ns_lshl_b32 s5, s73, 8\ns_add_u32 s5, s5, 0xc04ac00\nv_lshrrev_b32 v27, 2, v1\nbuffer_load_dword v27, v27, s[16:19], s5 offen sc0 sc1\n"
"Mvaa 136,148,172\nRa 212,11,57344\nMvaa 136,144,168\nMvaa 76,136,168\nRa 216,10,59392\nMvaa 76,140,172\nD0 7,24,44,1024\nMvaa 72,132,172\nRa 220,11,59392\nMvaa 72,128,168\n"
"Mvaa 80,128,176\nMvaa 80,132,180\nMvaa 84,140,180\nMvaa 84,136,176\nMvaa 144,144,176\nMvaa 144,148,180\nD0 8,24,44,2048\nMvaa 148,156,180\nMvaa 148,152,176\nMvaa 156,152,184\n"
"Mvaa 156,156,188\nMvaa 152,148,188\nMvaa 152,144,184\nMvaa 92,136,184\nMvaa 92,140,188\nD0 9,24,44,3072\nMvaa 88,132,188\nMvaa 88,128,184\nWL 0\nMaaa 0,192,160\nRa 128,10,0\n"
"Maaa 0,196,164\ns_add_u32 m0, s47, 49152\ns_add_u32 s44, s44, 0x40000\nMaaa 4,204,164\nRa 132,11,0\nMaaa 4,200,160\nMaaa 64,208,160\nRa 136,10,2048\nMaaa 64,212,164\nD0 6,24,44,0\n"
"Maaa 68,220,164\nRa 140,11,2048\nMaaa 68,216,160\nMaaa 76,216,168\nRa 144,10,8192\nMaaa 76,220,172\nMaaa 72,212,172\nRa 148,11,8192\nMaaa 72,208,168\nMaaa 12,200,168\n"
"Ra 152,10,10240\nMaaa 12,204,172\nD0 7,24,44,1024\nMaaa 8,196,172\nRa 156,11,10240\nMaaa 8,192,168\nMaaa 16,192,176\nMaaa 16,196,180\nMaaa 20,204,180\nMaaa 20,200,176\n"
"Maaa 80,208,176\nMaaa 80,212,180\nD0 8,24,44,2048\nMaaa 84,220,180\nMaaa 84,216,176\nMaaa 92,216,184\nMaaa 92,220,188\nMaaa 88,212,188\nMaaa 88,208,184\nMaaa 28,200,184\n"
"Maaa 28,204,188\nD0 9,24,44,3072\nMaaa 24,196,188\nMaaa 24,192,184\ns_waitcnt vmcnt(18) lgkmcnt(0)\ns_barrier\nMaaa 32,192,224\nTa 160,12,0\nMaaa 32,196,228\nTa 162,12,2048\n"
"Maaa 36,204,228\ns_add_u32 m0, s47, -16384\ns_add_u32 s48, s48, 128\nMaaa 36,200,224\nTa 164,12,16384\nMaaa 96,208,224\nTa 166,12,18432\n"
"buffer_load_dword v31, v24, s[24:27], s96 offen\ns_add_u32 s96, s96, 0x80000\nMaaa 96,212,228\nD0 2,20,48,0\nMaaa 100,220,228\nTa 168,13,0\nMaaa 100,216,224\nTa 170,13,2048\n"
"Maaa 108,216,232\nMaaa 108,220,236\nTa 172,13,16384\nMaaa 104,212,236\nTa 174,13,18432\nMaaa 104,208,232\nMaaa 44,200,232\nTa 176,14,0\nMaaa 44,204,236\nTa 178,14,2048\n"
"Maaa 40,196,236\nD0 3,20,48,1024\nMaaa 40,192,232\nTa 180,14,16384\nMaaa 48,192,240\nTa 182,14,18432\nMaaa 48,196,244\nMaaa 52,204,244\nTa 184,15,0\nMaaa 52,200,240\n"
"Ta 186,15,2048\nMaaa 112,208,240\nMaaa 112,212,244\nTa 188,15,16384\nMaaa 116,220,244\nTa 190,15,18432\nMaaa 116,216,240\nD0 4,20,48,2048\nMaaa 124,216,248\nMaaa 124,220,252\n"
"Maaa 120,212,252\nMaaa 120,208,248\nMaaa 60,200,248\nMaaa 60,204,252\nD0 5,20,48,3072\nMaaa 56,196,252\nMaaa 56,192,248\nWL 0\nMvaa 64,128,160\nTa 224,16,0\nWV 12\n"
"v_xor_b32 v27, s15, v27\nv_and_b32 v32, 0xffffdfff, v27\nv_cmp_ne_u32 vcc, 0, v32\ns_nop 0\ns_cbranch_vccnz .S412\n.S413:\nv_cmp_ne_u32 vcc, 0, v27\ns_nop 3\n"
"s_bcnt1_i32_b64 s4, vcc\ns_cmp_gt_u32 s4, 32\ns_cselect_b32 s4, 1, 0\nMvaa 64,132,164\nTa 226,16,2048\nMvaa 68,140,164\ns_add_u32 m0, s47, -65536\ns_add_u32 s45, s45, 128\n"
"Mvaa 68,136,160\nTa 228,16,16384\nMvaa 128,144,160\nTa 230,16,18432\nMvaa 128,148,164\nD0 2,20,45,0\nMvaa 132,156,164\nTa 232,17,0\nMvaa 132,152,160\nTa 234,17,2048\n"
"Mvaa 140,152,168\ns_xor_b32 s4, s4, s12\nMvaa 140,156,172\nTa 236,17,16384\nMvaa 136,148,172\nTa 238,17,18432\nMvaa 136,144,168\ns_and_b32 s4, s4, 1\nMvaa 76,136,168\nTa 240,18,0\n"
"Mvaa 76,140,172\nTa 242,18,2048\nMvaa 72,132,172\nD0 3,20,45,1024\nMvaa 72,128,168\nTa 244,18,16384\nMvaa 80,128,176\nTa 246,18,18432\nMvaa 80,132,180\ns_lshl_b32 s4, s4, 13\n"
"Mvaa 84,140,180\nTa 248,19,0\nMvaa 84,136,176\nTa 250,19,2048\nMvaa 144,144,176\ns_mul_i32 s72, s73, 25165824\nMvaa 144,148,180\nTa 252,19,16384\n.W587:\ns_cbranch_execz .W591\n"
"Mvaa 148,156,180\nTa 254,19,18432\nMvaa 148,152,176\nD0 4,20,45,2048\nMvaa 156,152,184\ns_add_u32 s72, s72, s4\nMvaa 156,156,188\ns_and_b32 s4, s12, 1\nMvaa 152,148,188\n"
"s_lshl_b32 s4, s4, 21\nMvaa 152,144,184\ns_add_u32 s72, s72, s4\nMvaa 92,136,184\ns_lshl3_add_u32 s2, s13, s11\nMvaa 92,140,188\nD0 5,20,45,3072\nMvaa 88,132,188\n"
"s_lshl_b32 s2, s2, 11\nMvaa 88,128,184\nWL 0\ns_barrier\nMvaa 96,128,224\nRa 192,10,16384\nMvaa 96,132,228\ns_add_u32 m0, s47, 0\ns_add_u32 s44, s44, 0x40000\nMvaa 100,140,228\n"
"Ra 196,11,16384\nMvaa 100,136,224\ns_lshl_b32 s3, s14, 22\nMvaa 160,144,224\nRa 200,10,18432\nMvaa 160,148,228\nD0 6,24,44,0\nMvaa 164,156,228\nRa 204,11,18432\nMvaa 164,152,224\n"
"s_add_u32 s2, s2, s3\nMvaa 172,152,232\nRa 208,10,24576\nMvaa 172,156,236\ns_add_u32 s2, s2, s72\nMvaa 168,148,236\nRa 212,11,24576\nMvaa 168,144,232\ns_add_u32 s49, s2, 0xc00000\n"
"Mvaa 108,136,232\nRa 216,10,26624\nMvaa 108,140,236\nD0 7,24,44,1024\nMvaa 104,132,236\nRa 220,11,26624\nMvaa 104,128,232\nMvaa 112,128,240\nMvaa 112,132,244\nMvaa 116,140,244\n"
"Mvaa 116,136,240\nMvaa 176,144,240\nMvaa 176,148,244\nD0 8,24,44,2048\nMvaa 180,156,244\nMvaa 180,152,240\nMvaa 188,152,248\nMvaa 188,156,252\nMvaa 184,148,252\nMvaa 184,144,248\n"
"Mvaa 124,136,248\nMvaa 124,140,252\nD0 9,24,44,3072\nMvaa 120,132,252\nMvaa 120,128,248\nWL 0\nMaaa 32,192,224\nRa 128,10,32768\ns_mov_b32 s4, s13\nMaaa 32,196,228\n"
"s_add_u32 m0, s47, 16384\ns_add_u32 s44, s44, 0x40000\nMaaa 36,204,228\nRa 132,11,32768\nv_lshrrev_b32 v32, 2, v0\nMaaa 36,200,224\nv_mul_u32_u24 v32, 896, v32\nMaaa 96,208,224\n"
"Ra 136,10,34816\nv_and_b32 v33, 3, v0\nMaaa 96,212,228\nD0 6,24,44,0\nMaaa 100,220,228\nRa 140,11,34816\nv_lshl_add_u32 v32, v33, 4, v32\nMaaa 100,216,224\ns_lshr_b32 s2, s11, 1\n"
"Maaa 108,216,232\nRa 144,10,40960\ns_mul_i32 s3, s2, 448\nMaaa 108,220,236\ns_and_b32 s5, s11, 1\nMaaa 104,212,236\nRa 148,11,40960\ns_lshl3_add_u32 s3, s5, s3\nMaaa 104,208,232\n"
"s_add_u32 s3, s3, 131072\nMaaa 44,200,232\nRa 152,10,43008\nv_add_u32 v32, s3, v32\nMaaa 44,204,236\nD0 7,24,44,1024\nMaaa 40,196,236\nRa 156,11,43008\nTv 36,32,0\n"
"Maaa 40,192,232\nTv 38,32,14336\nMaaa 48,192,240\nTv 40,32,64\nTv 42,32,14400\nMaaa 48,196,244\nTv 44,32,128\nMaaa 52,204,244\nTv 46,32,14464\nTv 48,32,192\nMaaa 52,200,240\n"
"Tv 50,32,14528\nMaaa 112,208,240\nTv 52,32,256\nTv 54,32,14592\nMaaa 112,212,244\nD0 8,24,44,2048\nMaaa 116,220,244\nTv 56,32,320\nTv 58,32,14656\nMaaa 116,216,240\nTv 60,32,384\n"
"Maaa 124,216,248\nTv 62,32,14720\ns_lshr_b32 s0, s4, 1\nMaaa 124,220,252\ns_mul_i32 s0, s0, 12\nMaaa 120,212,252\ns_lshl1_add_u32 s0, s5, s0\ns_and_b32 s6, s4, 1\n"
"Maaa 120,208,248\ns_add_u32 s0, s0, s6\nMaaa 60,200,248\ns_lshl3_add_u32 s1, s5, s4\ns_add_u32 s77, s1, 48\nMaaa 60,204,252\nD0 9,24,44,3072\nMaaa 56,196,252\n"
"s_and_b32 s4, s12, 1\ns_mov_b32 s7, 0x6543210\nMaaa 56,192,248\ns_waitcnt vmcnt(17) lgkmcnt(14)\ns_barrier\nMaaa 0,192,160\nTa 224,16,32768\ns_cmp_lg_u32 s4, s2\n"
"s_cselect_b32 s6, s0, s1\ns_cselect_b32 s5, 4, 16\ns_cselect_b32 s7, 0x6352410, s7\ns_cselect_b32 s3, 0, 229376\nMaaa 0,196,164\nTa 226,16,34816\nMaaa 4,204,164\n"
"s_add_u32 m0, s47, -49152\ns_add_u32 s48, s48, 128\nMaaa 4,200,160\nTa 228,16,49152\nMaaa 64,208,160\nTa 230,16,51200\ns_add_u32 s3, s3, s72\nMaaa 64,212,164\nD0 2,20,48,0\n"
"Maaa 68,220,164\nTa 232,17,32768\ns_add_u32 s3, s3, 0x1400000\nMaaa 68,216,160\nTa 234,17,34816\nMaaa 76,216,168\nWL 14\ns_lshr_b32 s0, s6, 4\nMaaa 76,220,172\nTa 236,17,49152\n"
"Maaa 72,212,172\nTa 238,17,51200\ns_lshl_b32 s0, s0, 2\nMaaa 72,208,168\ns_lshr_b32 s0, s7, s0\nMaaa 12,200,168\nTa 240,18,32768\ns_and_b32 s0, s0, 7\nMaaa 12,204,172\n"
"Ta 242,18,34816\nMaaa 8,196,172\nD0 3,20,48,1024\ns_lshl_b32 s0, s0, 5\nMaaa 8,192,168\nTa 244,18,49152\nMaaa 16,192,176\nTa 246,18,51200\ns_and_b32 s1, s6, 15\nMaaa 16,196,180\n"
"s_add_u32 s0, s0, s1\nMaaa 20,204,180\nTa 248,19,32768\ns_and_b32 s1, s6, 8\nMaaa 20,200,176\nTa 250,19,34816\nMaaa 80,208,176\ns_add_u32 s0, s0, s1\ns_lshl_b32 s0, s0, 10\n"
"Maaa 80,212,180\nTa 252,19,49152\nMaaa 84,220,180\nTa 254,19,51200\ns_add_u32 s0, s0, s3\nMaaa 84,216,176\nD0 4,20,48,2048\nMaaa 92,216,184\nSv 36,1,16,0,0\ns_add_u32 s6, s6, s5\n"
"Maaa 92,220,188\nMaaa 88,212,188\nMaaa 88,208,184\nMaaa 28,200,184\nMaaa 28,204,188\nD0 5,20,48,3072\nMaaa 24,196,188\nMaaa 24,192,184\nWL 0\nMvaa 96,128,224\nTa 160,12,32768\n"
"s_lshr_b32 s0, s6, 4\nMvaa 96,132,228\nTa 162,12,34816\nMvaa 100,140,228\ns_add_u32 m0, s47, -32768\ns_add_u32 s45, s45, 128\nMvaa 100,136,224\nTa 164,12,49152\nMvaa 160,144,224\n"
"Ta 166,12,51200\ns_lshl_b32 s0, s0, 2\nMvaa 160,148,228\nD0 2,20,45,0\nMvaa 164,156,228\nTa 168,13,32768\ns_lshr_b32 s0, s7, s0\nMvaa 164,152,224\nTa 170,13,34816\n"
"Mvaa 172,152,232\ns_and_b32 s0, s0, 7\ns_lshl_b32 s0, s0, 5\nMvaa 172,156,236\nTa 172,13,49152\nMvaa 168,148,236\nTa 174,13,51200\ns_and_b32 s1, s6, 15\nMvaa 168,144,232\n"
"s_add_u32 s0, s0, s1\nMvaa 108,136,232\nTa 176,14,32768\ns_and_b32 s1, s6, 8\nMvaa 108,140,236\nTa 178,14,34816\nMvaa 104,132,236\nD0 3,20,45,1024\ns_add_u32 s0, s0, s1\n"
"Mvaa 104,128,232\nTa 180,14,49152\nMvaa 112,128,240\nTa 182,14,51200\ns_lshl_b32 s0, s0, 10\nMvaa 112,132,244\ns_add_u32 s0, s0, s3\nMvaa 116,140,244\nTa 184,15,32768\n"
"Sv 40,1,16,0,0\nMvaa 116,136,240\nTa 186,15,34816\nMvaa 176,144,240\ns_add_u32 s6, s6, s5\nMvaa 176,148,244\nTa 188,15,49152\nMvaa 180,156,244\nTa 190,15,51200\nMvaa 180,152,240\n"
"D0 4,20,45,2048\nMvaa 188,152,248\nMvaa 188,156,252\nMvaa 184,148,252\nMvaa 184,144,248\nMvaa 124,136,248\ns_lshr_b32 s0, s6, 4\ns_lshl_b32 s0, s0, 2\nMvaa 124,140,252\n"
"D0 5,20,45,3072\nMvaa 120,132,252\ns_lshr_b32 s0, s7, s0\ns_and_b32 s0, s0, 7\nMvaa 120,128,248\ns_waitcnt vmcnt(18) lgkmcnt(0)\ns_barrier\nMvaa 64,128,160\nRa 192,10,49152\n"
"s_lshl_b32 s0, s0, 5\nMvaa 64,132,164\ns_add_u32 m0, s47, 32768\ns_add_u32 s44, s44, 0x40000\nMvaa 68,140,164\nRa 196,11,49152\ns_and_b32 s1, s6, 15\nMvaa 68,136,160\n"
"s_add_u32 s0, s0, s1\nMvaa 128,144,160\nRa 200,10,51200\ns_and_b32 s1, s6, 8\nMvaa 128,148,164\nD0 6,24,44,0\nMvaa 132,156,164\nRa 204,11,51200\ns_add_u32 s0, s0, s1\n"
"Mvaa 132,152,160\ns_lshl_b32 s0, s0, 10\nMvaa 140,152,168\nRa 208,10,57344\ns_add_u32 s0, s0, s3\nMvaa 140,156,172\nSv 44,1,16,0,0\nMvaa 136,148,172\nRa 212,11,57344\n"
"s_add_u32 s6, s6, s5\nMvaa 136,144,168\nMvaa 76,136,168\nRa 216,10,59392\nMvaa 76,140,172\nD0 7,24,44,1024\nMvaa 72,132,172\nRa 220,11,59392\nMvaa 72,128,168\nMvaa 80,128,176\n"
"Mvaa 80,132,180\nMvaa 84,140,180\ns_mov_b32 s6, s77\ns_mov_b32 s5, 16\nMvaa 84,136,176\ns_lshr_b32 s0, s6, 4\nMvaa 144,144,176\ns_lshl_b32 s0, s0, 2\ns_lshr_b32 s0, s7, s0\n"
"Mvaa 144,148,180\nD0 8,24,44,2048\nMvaa 148,156,180\ns_and_b32 s0, s0, 7\ns_lshl_b32 s0, s0, 5\nMvaa 148,152,176\ns_and_b32 s1, s6, 15\nMvaa 156,152,184\ns_add_u32 s0, s0, s1\n"
"s_and_b32 s1, s6, 8\nMvaa 156,156,188\ns_add_u32 s0, s0, s1\nMvaa 152,148,188\ns_lshl_b32 s0, s0, 10\ns_add_u32 s0, s0, s3\nMvaa 152,144,184\nSv 48,1,16,0,0\nMvaa 92,136,184\n"
"s_add_u32 s6, s6, s5\nMvaa 92,140,188\nD0 9,24,44,3072\nMvaa 88,132,188\nMvaa 88,128,184\nWL 0\nMaaa 0,192,160\nRa 128,10,0\nMaaa 0,196,164\ns_add_u32 m0, s47, 49152\n"
"s_add_u32 s44, s44, 0x40000\nMaaa 4,204,164\nRa 132,11,0\nMaaa 4,200,160\nMaaa 64,208,160\nRa 136,10,2048\ns_lshr_b32 s0, s6, 4\nMaaa 64,212,164\nD0 6,24,44,0\nMaaa 68,220,164\n"
"Ra 140,11,2048\ns_lshl_b32 s0, s0, 2\nMaaa 68,216,160\ns_lshr_b32 s0, s7, s0\nMaaa 76,216,168\nRa 144,10,8192\ns_and_b32 s0, s0, 7\nMaaa 76,220,172\ns_lshl_b32 s0, s0, 5\n"
"Maaa 72,212,172\nRa 148,11,8192\ns_and_b32 s1, s6, 15\nMaaa 72,208,168\ns_add_u32 s0, s0, s1\nMaaa 12,200,168\nRa 152,10,10240\ns_and_b32 s1, s6, 8\nMaaa 12,204,172\n"
"D0 7,24,44,1024\nMaaa 8,196,172\nRa 156,11,10240\ns_add_u32 s0, s0, s1\nMaaa 8,192,168\ns_lshl_b32 s0, s0, 10\nMaaa 16,192,176\ns_add_u32 s0, s0, s3\nSv 52,1,16,0,0\n"
"Maaa 16,196,180\ns_add_u32 s6, s6, s5\nMaaa 20,204,180\nMaaa 20,200,176\nMaaa 80,208,176\nMaaa 80,212,180\nD0 8,24,44,2048\nMaaa 84,220,180\nMaaa 84,216,176\nMaaa 92,216,184\n"
"Maaa 92,220,188\ns_lshr_b32 s0, s6, 4\nMaaa 88,212,188\ns_lshl_b32 s0, s0, 2\ns_lshr_b32 s0, s7, s0\nMaaa 88,208,184\ns_and_b32 s0, s0, 7\nMaaa 28,200,184\ns_lshl_b32 s0, s0, 5\n"
"s_and_b32 s1, s6, 15\nMaaa 28,204,188\nD0 9,24,44,3072\nMaaa 24,196,188\ns_add_u32 s0, s0, s1\ns_and_b32 s1, s6, 8\nMaaa 24,192,184\nWV 21\ns_barrier\nMaaa 32,192,224\n"
"Ta 160,12,0\ns_add_u32 s0, s0, s1\nMaaa 32,196,228\nTa 162,12,2048\nMaaa 36,204,228\ns_add_u32 m0, s47, -16384\ns_add_u32 s48, s48, 128\nMaaa 36,200,224\nTa 164,12,16384\n"
"Maaa 96,208,224\nTa 166,12,18432\ns_lshl_b32 s0, s0, 10\nMaaa 96,212,228\nD0 2,20,48,0\nMaaa 100,220,228\nTa 168,13,0\ns_add_u32 s0, s0, s3\nMaaa 100,216,224\nTa 170,13,2048\n"
"Maaa 108,216,232\nSv 56,1,16,0,0\ns_add_u32 s6, s6, s5\nMaaa 108,220,236\nTa 172,13,16384\nMaaa 104,212,236\nTa 174,13,18432\nMaaa 104,208,232\nMaaa 44,200,232\nTa 176,14,0\n"
"Maaa 44,204,236\nTa 178,14,2048\nMaaa 40,196,236\nD0 3,20,48,1024\nMaaa 40,192,232\nTa 180,14,16384\nMaaa 48,192,240\nTa 182,14,18432\ns_lshr_b32 s0, s6, 4\nMaaa 48,196,244\n"
"s_lshl_b32 s0, s0, 2\nMaaa 52,204,244\nTa 184,15,0\ns_lshr_b32 s0, s7, s0\nMaaa 52,200,240\nTa 186,15,2048\nMaaa 112,208,240\ns_and_b32 s0, s0, 7\ns_lshl_b32 s0, s0, 5\n"
"Maaa 112,212,244\nTa 188,15,16384\nMaaa 116,220,244\nTa 190,15,18432\ns_and_b32 s1, s6, 15\nMaaa 116,216,240\nD0 4,20,48,2048\nMaaa 124,216,248\ns_add_u32 s0, s0, s1\n"
"s_and_b32 s1, s6, 8\nMaaa 124,220,252\ns_add_u32 s0, s0, s1\nMaaa 120,212,252\ns_lshl_b32 s0, s0, 10\ns_add_u32 s0, s0, s3\nMaaa 120,208,248\nSv 60,1,16,0,0\nMaaa 60,200,248\n"
"s_add_u32 s6, s6, s5\nMaaa 60,204,252\nD0 5,20,48,3072\nMaaa 56,196,252\nMaaa 56,192,248\nWL 0\nMvaa 64,128,160\nTa 224,16,0\nMvaa 64,132,164\nTa 226,16,2048\n"
"s_add_u32 m0, s47, 65536\ns_add_u32 s46, s51, 0\nMvaa 68,140,164\nv_lshrrev_b32 v48, 4, v0\nMvaa 68,136,160\nTa 228,16,16384\nD0 6,32,46,0\nMvaa 128,144,160\nTa 230,16,18432\n"
"Mvaa 128,148,164\nv_and_b32 v49, 7, v0\nMvaa 132,156,164\nTa 232,17,0\nMvaa 132,152,160\nTa 234,17,2048\nMvaa 140,152,168\nv_xor_b32 v49, v49, v48\nD0 7,32,46,1024\n"
"Mvaa 140,156,172\nTa 236,17,16384\nMvaa 136,148,172\nTa 238,17,18432\nMvaa 136,144,168\nv_lshlrev_b32 v49, 4, v49\nMvaa 76,136,168\nTa 240,18,0\nMvaa 76,140,172\nTa 242,18,2048\n"
"D0 8,32,46,2048\nMvaa 72,132,172\nv_lshrrev_b32 v48, 3, v0\nMvaa 72,128,168\nTa 244,18,16384\nMvaa 80,128,176\nTa 246,18,18432\nMvaa 80,132,180\nv_mul_u32_u24 v48, 896, v48\n"
"Mvaa 84,140,180\nTa 248,19,0\nD0 9,32,46,3072\nMvaa 84,136,176\nTa 250,19,2048\nMvaa 144,144,176\nv_add_u32 v40, v48, v49\nMvaa 144,148,180\nTa 252,19,16384\n"
"s_add_u32 m0, s47, 81920\ns_add_u32 s46, s51, 262144\nMvaa 148,156,180\nTa 254,19,18432\nMvaa 148,152,176\nv_xor_b32 v49, 64, v40\nD0 6,32,46,0\nMvaa 156,152,184\n"
"v_add_u32 v41, 6144, v49\nMvaa 156,156,188\nv_add_u32 v42, 55296, v40\nMvaa 152,148,188\nv_add_u32 v43, 61440, v49\n.W588:\ns_cbranch_execz .W592\nMvaa 152,144,184\n"
"s_mov_b32 s2, s13\nMvaa 92,136,184\ns_lshl3_add_u32 s2, s2, s11\nD0 7,32,46,1024\nMvaa 92,140,188\ns_mul_i32 s52, s2, 14336\nMvaa 88,132,188\ns_lshr_b32 s2, s13, 2\n"
"s_bfe_u32 s3, s13, 0x10001\nMvaa 88,128,184\ns_waitcnt vmcnt(23) lgkmcnt(0)\ns_barrier\nMvaa 96,128,224\nRa 192,10,16384\ns_mul_i32 s3, s3, 17\nMvaa 96,132,228\n"
"s_add_u32 s2, s2, s3\nD0 8,32,46,2048\nMvaa 100,140,228\nRa 196,11,16384\ns_lshl2_add_u32 s3, s14, 0\nMvaa 100,136,224\ns_lshl_b32 s3, s3, 2\nMvaa 160,144,224\nRa 200,10,18432\n"
"s_lshr_b32 s3, 0xe420ca86, s3\nMvaa 160,148,228\ns_and_b32 s3, s3, 15\nMvaa 164,156,228\nRa 204,11,18432\ns_add_u32 s2, s2, s3\nD0 9,32,46,3072\nMvaa 164,152,224\n"
"s_cmp_eq_u32 s2, 32\ns_cselect_b32 s4, 16, s2\nMvaa 172,152,232\nRa 208,10,24576\ns_mul_i32 s75, s4, 65536\nMvaa 172,156,236\ns_lshr_b32 s2, s13, 2\ns_add_u32 m0, s47, -65536\n"
"s_add_u32 s46, s52, 0\nMvaa 168,148,236\nRa 212,11,24576\ns_bfe_u32 s3, s13, 0x10001\nMvaa 168,144,232\ns_mul_i32 s3, s3, 17\nD0 40,28,46,0\nMvaa 108,136,232\nRa 216,10,26624\n"
"s_add_u32 s2, s2, s3\nMvaa 108,140,236\ns_lshl2_add_u32 s3, s14, 1\nMvaa 104,132,236\nRa 220,11,26624\ns_lshl_b32 s3, s3, 2\nMvaa 104,128,232\ns_lshr_b32 s3, 0xe420ca86, s3\n"
"Mvaa 112,128,240\ns_and_b32 s3, s3, 15\ns_add_u32 s2, s2, s3\nD0 41,28,46,1024\nMvaa 112,132,244\ns_cmp_eq_u32 s2, 32\ns_cselect_b32 s4, 16, s2\nMvaa 116,140,244\n"
"s_mul_i32 s76, s4, 65536\ns_lshr_b32 s2, s13, 2\nMvaa 116,136,240\ns_bfe_u32 s3, s13, 0x10001\nMvaa 176,144,240\ns_mul_i32 s3, s3, 17\ns_add_u32 s2, s2, s3\nMvaa 176,148,244\n"
"s_lshl2_add_u32 s3, s14, 2\nD0 42,28,46,2048\nMvaa 180,156,244\ns_lshl_b32 s3, s3, 2\ns_lshr_b32 s3, 0xe420ca86, s3\nMvaa 180,152,240\ns_and_b32 s3, s3, 15\nMvaa 188,152,248\n"
"s_add_u32 s2, s2, s3\nMvaa 188,156,252\ns_cmp_eq_u32 s2, 32\ns_cselect_b32 s4, 16, s2\nMvaa 184,148,252\ns_mul_i32 s77, s4, 65536\ns_lshr_b32 s2, s13, 2\nD0 43,28,46,3072\n"
"Mvaa 184,144,248\ns_bfe_u32 s3, s13, 0x10001\nMvaa 124,136,248\ns_mul_i32 s3, s3, 17\ns_add_u32 s2, s2, s3\nMvaa 124,140,252\ns_lshl2_add_u32 s3, s14, 3\nMvaa 120,132,252\n"
"s_lshl_b32 s3, s3, 2\ns_lshr_b32 s3, 0xe420ca86, s3\nMvaa 120,128,248\nWL 0\nMaaa 32,192,224\nRa 128,10,32768\ns_and_b32 s3, s3, 15\ns_add_u32 m0, s47, 0\n"
"s_add_u32 s46, s51, 524288\nD0 6,32,46,0\nMaaa 32,196,228\ns_add_u32 s2, s2, s3\ns_cmp_eq_u32 s2, 32\ns_cselect_b32 s4, 16, s2\nMaaa 36,204,228\nRa 132,11,32768\n"
"s_mul_i32 s78, s4, 65536\nMaaa 36,200,224\ns_lshr_b32 s3, s11, 1\nMaaa 96,208,224\nRa 136,10,34816\ns_lshl_b32 s3, s3, 15\nMaaa 96,212,228\ns_add_u32 s2, s72, s3\nD0 7,32,46,1024\n"
"Maaa 100,220,228\nRa 140,11,34816\ns_and_b32 s3, s11, 1\nMaaa 100,216,224\ns_lshl_b32 s3, s3, 14\nMaaa 108,216,232\nRa 144,10,40960\ns_add_u32 s2, s2, s3\nMaaa 108,220,236\n"
"s_add_u32 s2, s2, 0x400000\nMaaa 104,212,236\nRa 148,11,40960\ns_mov_b32 s55, 4096\nD0 8,32,46,2048\nMaaa 104,208,232\ns_add_u32 s75, s75, s2\nMaaa 44,200,232\nRa 152,10,43008\n"
"s_add_u32 s76, s76, s2\nMaaa 44,204,236\ns_add_u32 s77, s77, s2\nMaaa 40,196,236\nRa 156,11,43008\ns_add_u32 s78, s78, s2\nMaaa 40,192,232\nv_add_u32 v32, 0x10000, v12\n"
"D0 9,32,46,3072\nMaaa 48,192,240\nv_add_u32 v33, 0x10000, v13\nv_add_u32 v34, 0x10000, v14\nMaaa 48,196,244\nv_add_u32 v35, 0x10000, v15\nMaaa 52,204,244\n"
"v_add_u32 v36, 0x10000, v16\nv_add_u32 v37, 0x10000, v17\ns_add_u32 m0, s47, 16384\ns_add_u32 s46, s51, 786432\nMaaa 52,200,240\nv_add_u32 v38, 0x10000, v18\nMaaa 112,208,240\n"
"v_add_u32 v39, 0x10000, v19\ns_add_u32 s2, s11, 1\nD0 6,32,46,0\nMaaa 112,212,244\ns_lshr_b32 s2, s2, 1\nMaaa 116,220,244\ns_lshl_b32 s2, s2, 12\nv_add_u32 v44, s2, v10\n"
"Maaa 116,216,240\nv_add_u32 v45, s2, v11\nMaaa 124,216,248\nv_xor_b32 v46, 0x1000, v44\nv_xor_b32 v47, 0x1000, v45\nMaaa 124,220,252\ns_lshr_b32 s3, s11, 1\nD0 7,32,46,1024\n"
"Maaa 120,212,252\ns_lshl_b32 s3, s3, 13\ns_lshl_b32 s2, s11, 12\nMaaa 120,208,248\ns_add_u32 s2, s2, s3\nMaaa 60,200,248\ns_add_u32 s2, s2, s72\ns_lshl_b32 s3, s11, 12\n"
"Maaa 60,204,252\ns_add_u32 s89, s3, 0x10000\nMaaa 56,196,252\ns_mov_b32 s88, 0xffff8000\ns_lshl_b32 s4, s13, 1\nD0 8,32,46,2048\nMaaa 56,192,248\ns_waitcnt vmcnt(25) lgkmcnt(0)\n"
"s_barrier\nMaaa 0,192,160\nTa 224,16,32768\ns_sub_u32 s79, 15, s4\nMaaa 0,196,164\nTa 226,16,34816\nMaaa 4,204,164\ns_add_u32 s96, s2, 0x1310000\ns_mov_b32 s63, 0x53800000\n"
"Maaa 4,200,160\nTa 228,16,49152\nD0 9,32,46,3072\nMaaa 64,208,160\nTa 230,16,51200\ns_mov_b32 s64, 0x2b800000\nMaaa 64,212,164\ns_sub_u32 s98, s4, 1\nMaaa 68,220,164\n"
"Ta 232,17,32768\ns_lshl_b32 s5, s13, 5\ns_add_u32 m0, s47, -32768\ns_add_u32 s46, s52, 128\nMaaa 68,216,160\nTa 234,17,34816\nMaaa 76,216,168\ns_add_u32 s5, s5, s11\n"
"s_lshl_b32 s65, s5, 9\nD0 40,28,46,0\nMaaa 76,220,172\nTa 236,17,49152\nMaaa 72,212,172\nTa 238,17,51200\ns_add_u32 s65, s65, s72\nMaaa 72,208,168\ns_add_u32 s65, s65, 0x1580000\n"
"Maaa 12,200,168\nTa 240,18,32768\ns_and_b32 s66, s72, 0x2000\nMaaa 12,204,172\nTa 242,18,34816\nD0 41,28,46,1024\nMaaa 8,196,172\ns_add_u32 s66, s66, s56\n"
"s_add_u32 s66, s66, 0x100000\nMaaa 8,192,168\nTa 244,18,49152\nMaaa 16,192,176\nTa 246,18,51200\ns_and_b32 s5, s12, 1\nMaaa 16,196,180\ns_mul_i32 s58, s5, 0x202000\n"
"Maaa 20,204,180\nTa 248,19,32768\ns_xor_b32 s58, s58, s72\nD0 42,28,46,2048\nMaaa 20,200,176\nTa 250,19,34816\nMaaa 80,208,176\ns_add_u32 s58, s58, 0x15c0000\n"
"s_lshl_b32 s5, s5, 8\nMaaa 80,212,180\nTa 252,19,49152\nMaaa 84,220,180\nTa 254,19,51200\ns_add_u32 s61, s58, s5\nMaaa 84,216,176\ns_lshl4_add_u32 s60, s13, s61\nD0 43,28,46,3072\n"
"Maaa 92,216,184\ns_add_u32 s54, s60, 128\ns_xor_b32 s60, s54, 0x100\nMaaa 92,220,188\ns_cmp_eq_u32 s11, 0\ns_cselect_b32 s54, s54, 0x40000000\nMaaa 88,212,188\ns_getpc_b64 s[8:9]\n"
".PC425:\ns_add_u32 s8, s8, .HD411-.PC425\ns_addc_u32 s9, s9, 0\ns_add_u32 m0, s47, -49152\ns_add_u32 s46, s52, 256\nMaaa 88,208,184\ns_mov_b32 s97, 0xff800000\nMaaa 28,200,184\n"
"s_mov_b32 s90, 0\nD0 40,28,46,0\nMaaa 28,204,188\ns_cmp_lt_u32 s11, 2\ns_cselect_b32 s92, 2, 0\nMaaa 24,196,188\ns_mul_i32 s4, s79, 65536\ns_sub_u32 s4, s2, s4\nMaaa 24,192,184\n"
"WL 0\nMvaa 96,128,224\nTa 160,12,32768\ns_add_u32 s94, s4, 0x20000\nMvaa 96,132,228\nTa 162,12,34816\nD0 41,28,46,1024\nMvaa 100,140,228\ns_add_u32 s95, s4, 0x410000\n"
"s_add_u32 s86, s2, 0x130000\nMvaa 100,136,224\nTa 164,12,49152\nMvaa 108,136,232\nTa 166,12,51200\ns_add_u32 s87, s2, 0x520000\nMvaa 108,140,236\ns_sub_u32 s99, s79, 2\n"
"s_cmp_eq_u32 s13, 7\ns_cselect_b32 s93, 1.0, 0\nMvaa 104,132,236\nTa 168,13,32768\ns_mov_b32 s36, s68\nD0 42,28,46,2048\nMvaa 104,128,232\nTa 170,13,34816\nMvaa 112,128,240\n"
"s_and_b32 s37, s69, 0xffff\ns_mov_b32 s38, 0x1c0000\nMvaa 112,132,244\nTa 172,13,49152\nCV 208,96,97\nMvaa 116,140,244\nTa 174,13,51200\ns_mov_b32 s39, 0x20000\nMvaa 116,136,240\n"
"s_and_b32 s2, s74, 15\nD0 43,28,46,3072\nMvaa 124,136,248\nTa 176,14,32768\ns_min_u32 s2, s2, 13\nMvaa 124,140,252\nTa 178,14,34816\nMvaa 120,132,252\ns_sub_u32 s2, s2, 0\n"
"s_mul_i32 s3, s73, 14\nMvaa 120,128,248\nTa 180,14,49152\nCV 209,98,99\nMvaa 160,144,224\nTa 182,14,51200\ns_add_u32 s2, s2, s3\nMvaa 160,148,228\ns_lshl2_add_u32 s2, s2, s11\n"
"Mvaa 164,156,228\nTa 184,15,32768\ns_mul_i32 s3, s2, 4682\nMvaa 164,152,224\nTa 186,15,34816\nCV 210,100,101\nMvaa 172,152,232\ns_lshr_b32 s3, s3, 16\ns_mul_i32 s22, s3, 14\n"
"Mvaa 172,156,236\nTa 188,15,49152\nCV 211,102,103\nMvaa 168,148,236\nTa 190,15,51200\ns_sub_u32 s22, s2, s22\nMvaa 168,144,232\ns_and_b32 s21, s3, 28\nMvaa 176,144,240\n"
"s_add_u32 s21, s21, s3\ns_lshl_b32 s21, s21, 4\nMvaa 176,148,244\ns_bfe_u32 s41, s3, 0x30002\ns_cmp_gt_u32 s22, 6\ns_cselect_b32 s2, 1179648, 0\ns_cselect_b32 s1, 16, 0\n"
"Mvaa 180,156,244\ns_add_u32 s41, s41, s1\ns_lshl4_add_u32 s41, s41, 0xc048480\nMvaa 180,152,240\ns_and_b32 s1, s3, 28\nCV 212,104,105\nMvaa 188,152,248\ns_add_u32 s3, s3, s1\n"
"s_lshl_b32 s3, s3, 11\nMvaa 188,156,252\ns_add_u32 s2, s2, s3\nMvaa 184,148,252\ns_lshl_b32 s3, s22, 17\ns_add_u32 s2, s2, s3\nMvaa 184,144,248\ns_waitcnt vmcnt(16) lgkmcnt(0)\n"
"s_barrier\nMvaa 64,128,160\nRa 192,10,49152\ns_add_u32 s20, s2, 0x10e0000\ns_add_u32 m0, s47, 32768\ns_add_u32 s46, s51, 1048576\nMvaa 64,132,164\ns_lshl_b32 s2, s22, 7\n"
"Mvaa 68,140,164\nRa 196,11,49152\ns_add_u32 s40, s2, 0xc04b800\nD0 6,32,46,0\nMvaa 68,136,160\ns_mul_i32 s2, s21, 896\nCV 213,106,107\nMvaa 76,136,168\nRa 200,10,51200\n"
"s_lshl_b32 s3, s22, 6\nMvaa 76,140,172\ns_add_u32 s22, s2, s3\nCV 214,108,109\nMvaa 72,132,172\nRa 204,11,51200\ns_add_u32 s23, s22, 57344\nMvaa 72,128,168\nD0 7,32,46,1024\n"
"Mvaa 80,128,176\nRa 208,10,57344\nCV 215,110,111\nMvaa 80,132,180\nCV 216,112,113\nCV 217,114,115\nMvaa 84,140,180\nRa 212,11,57344\nMvaa 84,136,176\nCV 218,116,117\n"
"CV 219,118,119\nMvaa 92,136,184\nRa 216,10,59392\nD0 8,32,46,2048\ns_lshl3_add_u32 s5, s12, s13\ns_lshl2_add_u32 s5, s5, s11\ns_lshl2_add_u32 s5, s5, 0xc04a400\n"
"buffer_load_dword v27, off, s[16:19], s5 sc0 sc1\nMvaa 92,140,188\nCV 220,120,121\nMvaa 88,132,188\nRa 220,11,59392\nCV 221,122,123\nMvaa 88,128,184\nCV 222,124,125\n"
"CV 223,126,127\nMvaa 128,144,160\nCV 192,64,65\nCV 193,66,67\nMvaa 128,148,164\nD0 9,32,46,3072\nMvaa 132,156,164\nCV 194,68,69\nCV 195,70,71\nMvaa 132,152,160\nCV 196,72,73\n"
"CV 197,74,75\nMvaa 140,152,168\ns_add_u32 m0, s47, 49152\ns_add_u32 s46, s51, 1310720\nMvaa 140,156,172\nCV 198,76,77\nCV 199,78,79\nMvaa 136,148,172\nD0 6,32,46,0\nCV 200,80,81\n"
"Mvaa 136,144,168\nCV 201,82,83\nMvaa 144,144,176\nCV 202,84,85\nCV 203,86,87\nMvaa 144,148,180\nCV 204,88,89\nCV 205,90,91\nMvaa 148,156,180\nCV 206,92,93\nMvaa 148,152,176\n"
"D0 7,32,46,1024\nCV 207,94,95\nMvaa 156,152,184\nCV 240,160,161\nMvaa 156,156,188\nCV 241,162,163\nSv 192,1,16,75,0\nMvaa 152,148,188\nCV 242,164,165\nMvaa 152,144,184\nWL 0\n"
"Maaa 0,192,160\nD0 8,32,46,2048\nRa 128,44,0\nMaaa 0,196,164\nCV 243,166,167\nMaaa 4,204,164\nRa 132,45,0\nMaaa 4,200,160\nCV 244,168,169\nMaaa 12,200,168\nRa 136,44,2048\n"
"Maaa 12,204,172\nD0 9,32,46,3072\nMaaa 8,196,172\nRa 140,45,2048\nSv 196,1,16,75,1024\nMaaa 8,192,168\nCV 245,170,171\nMaaa 16,192,176\nRa 144,46,0\nMaaa 16,196,180\n"
"CV 246,172,173\nMaaa 20,204,180\nRa 148,47,0\nMaaa 20,200,176\nCV 247,174,175\nMaaa 28,200,184\nRa 152,46,2048\nMaaa 28,204,188\nCV 248,176,177\nMaaa 24,196,188\nRa 156,47,2048\n"
"Maaa 24,192,184\nCV 249,178,179\nSv 200,1,16,75,2048\nMaaa 64,208,160\nCV 250,180,181\nCV 251,182,183\nMaaa 64,212,164\nCV 252,184,185\nMaaa 68,220,164\nCV 253,186,187\n"
"Maaa 68,216,160\nCV 254,188,189\nMaaa 76,216,168\nCV 255,190,191\nMaaa 76,220,172\nCV 224,128,129\nMaaa 72,212,172\nCV 225,130,131\nMaaa 72,208,168\nCV 226,132,133\n"
"Maaa 80,208,176\nCV 227,134,135\nSv 204,1,16,75,3072\nMaaa 80,212,180\nCV 228,136,137\nMaaa 84,220,180\nCV 229,138,139\nMaaa 84,216,176\nCV 230,140,141\nMaaa 92,216,184\n"
"CV 231,142,143\nMaaa 92,220,188\nCV 232,144,145\nMaaa 88,212,188\nCV 233,146,147\nMaaa 88,208,184\ns_barrier\nMaaa 32,192,224\ns_add_u32 m0, s47, -16384\ns_add_u32 s46, s52, 384\n"
"Ta 160,32,0\nMaaa 32,196,228\nTa 162,32,2048\nMaaa 36,204,228\nD0 40,28,46,0\ns_add_u32 s75, s75, s55\nSv 208,1,16,75,0\nMaaa 36,200,224\nTa 164,32,16384\nMaaa 44,200,232\n"
"Ta 166,32,18432\nMaaa 44,204,236\nCV 234,148,149\nMaaa 40,196,236\nTa 168,33,0\nMaaa 40,192,232\nD0 41,28,46,1024\nTa 170,33,2048\nMaaa 48,192,240\nCV 235,150,151\n"
"Maaa 48,196,244\nTa 172,33,16384\nMaaa 52,204,244\nTa 174,33,18432\nMaaa 52,200,240\n.W589:\ns_cbranch_execz .W593\nCV 236,152,153\nSv 212,1,16,75,1024\nMaaa 60,200,248\n"
"D0 42,28,46,2048\nTa 176,34,0\nMaaa 60,204,252\nTa 178,34,2048\nMaaa 56,196,252\nCV 237,154,155\nMaaa 56,192,248\nTa 180,34,16384\nMaaa 96,208,224\nTa 182,34,18432\n"
"CV 238,156,157\nMaaa 96,212,228\nD0 43,28,46,3072\nMaaa 100,220,228\nTa 184,35,0\nMaaa 100,216,224\nTa 186,35,2048\nMaaa 108,216,232\nCV 239,158,159\nSv 216,1,16,75,2048\n"
"Maaa 108,220,236\nTa 188,35,16384\nMaaa 104,212,236\nTa 190,35,18432\nMaaa 104,208,232\nMaaa 112,208,240\nMaaa 112,212,244\nMaaa 116,220,244\nMaaa 116,216,240\nMaaa 124,216,248\n"
"Maaa 124,220,252\nSv 220,1,16,75,3072\nMaaa 120,212,252\nMaaa 120,208,248\nWL 0\nZvaa 64,160,128\nTa 224,36,0\nAR 60,0\nMvaa 64,164,132\nTa 226,36,2048\nZvaa 96,164,140\nAR 61,1\n"
"Mvaa 96,160,136\nTa 228,36,16384\nCV 192,60,61\nZvaa 128,160,144\nTa 230,36,18432\nMvaa 128,164,148\nAR 60,2\nAR 61,3\nZvaa 160,164,156\nTa 232,37,0\nSv 224,1,16,76,0\n"
"Mvaa 160,160,152\nTa 234,37,2048\nCV 193,60,61\nZvaa 164,168,152\nAR 60,4\nMvaa 164,172,156\nTa 236,37,16384\nZvaa 132,172,148\nTa 238,37,18432\nAR 61,5\nMvaa 132,168,144\n"
"CV 194,60,61\nZvaa 100,168,136\nTa 240,38,0\nAR 60,6\nMvaa 100,172,140\nTa 242,38,2048\nZvaa 68,172,132\nSv 228,1,16,76,1024\nAR 61,7\nMvaa 68,168,128\nTa 244,38,16384\n"
"Zvaa 72,176,128\nTa 246,38,18432\nCV 195,60,61\nMvaa 72,180,132\nAR 60,8\nZvaa 104,180,140\nTa 248,39,0\nMvaa 104,176,136\nTa 250,39,2048\nAR 61,9\nZvaa 136,176,144\nCV 196,60,61\n"
"Mvaa 136,180,148\nTa 252,39,16384\nAR 60,10\nZvaa 168,180,156\nTa 254,39,18432\nSv 232,1,16,76,2048\nMvaa 168,176,152\nAR 61,11\nCV 197,60,61\nZvaa 172,184,152\nAR 60,12\n"
"Mvaa 172,188,156\nAR 61,13\nZvaa 140,188,148\nCV 198,60,61\nAR 60,14\nMvaa 140,184,144\nAR 61,15\nZvaa 108,184,136\nCV 199,60,61\nAR 60,16\nMvaa 108,188,140\nSv 236,1,16,76,3072\n"
"Zvaa 76,188,132\nAR 61,17\nCV 200,60,61\nMvaa 76,184,128\ns_waitcnt vmcnt(29) lgkmcnt(0)\ns_barrier\nZvaa 80,224,128\nRa 192,44,32768\nAR 60,18\nMvaa 80,228,132\nRa 196,45,32768\n"
"s_add_u32 m0, s47, 65536\ns_add_u32 s46, s51, 1572864\nD0 6,32,46,0\nZvaa 112,228,140\nRa 200,44,34816\nMvaa 112,224,136\nRa 204,45,34816\nAR 61,19\nZvaa 144,224,144\n"
"Ra 208,46,32768\nMvaa 144,228,148\nRa 212,47,32768\nCV 201,60,61\nD0 7,32,46,1024\nZvaa 176,228,156\nRa 216,46,34816\nMvaa 176,224,152\nRa 220,47,34816\nAR 60,20\n"
"Zvaa 180,232,152\nTa 160,12,0\nMvaa 180,236,156\nTa 162,12,2048\nD0 8,32,46,2048\nZvaa 148,236,148\nTa 164,12,16384\nAR 61,21\nMvaa 148,232,144\nTa 166,12,18432\nZvaa 116,232,136\n"
"Ta 168,13,0\nCV 202,60,61\nMvaa 116,236,140\nTa 170,13,2048\nD0 9,32,46,3072\nZvaa 84,236,132\nTa 172,13,16384\nAR 60,22\nMvaa 84,232,128\nTa 174,13,18432\n"
"s_add_u32 m0, s47, 81920\ns_add_u32 s46, s51, 1835008\nZvaa 88,240,128\nTa 176,14,0\nAR 61,23\nMvaa 88,244,132\nTa 178,14,2048\nD0 6,32,46,0\nZvaa 120,244,140\nTa 180,14,16384\n"
"Mvaa 120,240,136\nTa 182,14,18432\nCV 203,60,61\nZvaa 152,240,144\nTa 184,15,0\nMvaa 152,244,148\nTa 186,15,2048\nAR 60,24\nD0 7,32,46,1024\nZvaa 184,244,156\nTa 188,15,16384\n"
"Mvaa 184,240,152\nTa 190,15,18432\nAR 61,25\nZvaa 188,248,152\nCV 204,60,61\nMvaa 188,252,156\nAR 60,26\nD0 8,32,46,2048\nZvaa 156,252,148\nAR 61,27\nCV 205,60,61\n"
"Mvaa 156,248,144\nAR 60,28\nZvaa 124,248,136\nAR 61,29\nCV 206,60,61\nMvaa 124,252,140\nAR 60,30\nD0 9,32,46,3072\nZvaa 92,252,132\nAR 61,31\nCV 207,60,61\nMvaa 92,248,128\nWL 0\n"
"Mvaa 64,160,192\nTa 224,16,0\nAR 60,32\nMvaa 64,164,196\nTa 226,16,2048\ns_add_u32 m0, s47, -65536\ns_add_u32 s46, s52, 512\nD0 40,28,46,0\nMvaa 96,164,204\nAR 61,33\n"
"Mvaa 96,160,200\nTa 228,16,16384\nCV 208,60,61\nMvaa 128,160,208\nTa 230,16,18432\nMvaa 128,164,212\nAR 60,34\nAR 61,35\nD0 41,28,46,1024\nMvaa 160,164,220\nTa 232,17,0\n"
"Mvaa 160,160,216\nTa 234,17,2048\nCV 209,60,61\nMvaa 164,168,216\nAR 60,36\nMvaa 164,172,220\nTa 236,17,16384\nD0 42,28,46,2048\nMvaa 132,172,212\nTa 238,17,18432\nAR 61,37\n"
"Mvaa 132,168,208\nCV 210,60,61\nMvaa 100,168,200\nTa 240,18,0\nAR 60,38\nMvaa 100,172,204\nTa 242,18,2048\nD0 43,28,46,3072\nMvaa 68,172,196\ns_add_u32 s76, s76, s55\n"
"Sv 240,1,16,76,0\nMvaa 68,168,192\nTa 244,18,16384\nMvaa 72,176,192\nTa 246,18,18432\nAR 61,39\nMvaa 72,180,196\nCV 211,60,61\nMvaa 104,180,204\nTa 248,19,0\nMvaa 104,176,200\n"
"Ta 250,19,2048\nSv 244,1,16,76,1024\nMvaa 136,176,208\nAR 60,40\nMvaa 136,180,212\nTa 252,19,16384\nAR 61,41\nMvaa 168,180,220\nTa 254,19,18432\nMvaa 168,176,216\nCV 212,60,61\n"
"AR 60,42\nMvaa 172,184,216\nSv 248,1,16,76,2048\nMvaa 172,188,220\nAR 61,43\nMvaa 140,188,212\nCV 213,60,61\nAR 60,44\nMvaa 140,184,208\nAR 61,45\nMvaa 108,184,200\nCV 214,60,61\n"
"AR 60,46\nMvaa 108,188,204\nSv 252,1,16,76,3072\nMvaa 76,188,196\nAR 61,47\nCV 215,60,61\nMvaa 76,184,192\ns_waitcnt vmcnt(20) lgkmcnt(0)\ns_barrier\nMvaa 80,224,192\n"
"Ra 128,44,16384\nAR 60,48\nv_cmp_ne_u32 vcc, s15, v27\ns_nop 0\ns_cbranch_vccnz .S426\n.S427:\nMvaa 80,228,196\nRa 132,45,16384\n"
"buffer_store_dword v25, off, s[16:19], s54 offset:0 sc0 sc1\ns_add_u32 m0, s47, 0\ns_add_u32 s46, s51, 2097152\nD0 6,32,46,0\nMvaa 112,228,204\nRa 136,44,18432\nMvaa 112,224,200\n"
"Ra 140,45,18432\nAR 61,49\nMvaa 144,224,208\nRa 144,46,16384\nMvaa 144,228,212\nRa 148,47,16384\nCV 216,60,61\nD0 7,32,46,1024\nMvaa 176,228,220\nRa 152,46,18432\n"
"Mvaa 176,224,216\nRa 156,47,18432\nAR 60,50\nMvaa 180,232,216\nTa 160,12,32768\nMvaa 180,236,220\nTa 162,12,34816\nD0 8,32,46,2048\nMvaa 148,236,212\nTa 164,12,49152\nAR 61,51\n"
"Mvaa 148,232,208\nTa 166,12,51200\nMvaa 116,232,200\nTa 168,13,32768\nCV 217,60,61\nMvaa 116,236,204\nTa 170,13,34816\nD0 9,32,46,3072\nMvaa 84,236,196\nTa 172,13,49152\nAR 60,52\n"
"Mvaa 84,232,192\nTa 174,13,51200\ns_add_u32 m0, s47, 16384\ns_add_u32 s46, s51, 2359296\nMvaa 88,240,192\nTa 176,14,32768\nAR 61,53\nMvaa 88,244,196\nTa 178,14,34816\n"
"D0 6,32,46,0\nMvaa 120,244,204\nTa 180,14,49152\nMvaa 120,240,200\nTa 182,14,51200\nCV 218,60,61\nMvaa 152,240,208\nTa 184,15,32768\nMvaa 152,244,212\nTa 186,15,34816\nAR 60,54\n"
"D0 7,32,46,1024\nMvaa 184,244,220\nTa 188,15,49152\nMvaa 184,240,216\nTa 190,15,51200\nAR 61,55\nMvaa 188,248,216\nCV 219,60,61\nMvaa 188,252,220\nAR 60,56\nD0 8,32,46,2048\n"
"Mvaa 156,252,212\nAR 61,57\nCV 220,60,61\nMvaa 156,248,208\nAR 60,58\nMvaa 124,248,200\nAR 61,59\nCV 221,60,61\nMvaa 124,252,204\nAR 60,60\nD0 9,32,46,3072\nMvaa 92,252,196\n"
"AR 61,61\nCV 222,60,61\nMvaa 92,248,192\nWL 0\nMvaa 64,160,128\nTa 224,16,32768\nAR 60,62\nMvaa 64,164,132\nTa 226,16,34816\ns_add_u32 m0, s47, -32768\ns_add_u32 s46, s52, 640\n"
"D0 40,28,46,0\nMvaa 96,164,140\nAR 61,63\nMvaa 96,160,136\nTa 228,16,49152\nCV 223,60,61\nMvaa 128,160,144\nTa 230,16,51200\nMvaa 128,164,148\nD0 41,28,46,1024\nMvaa 160,164,156\n"
"Ta 232,17,32768\nAR 60,126\nMvaa 160,160,152\nTa 234,17,34816\nMvaa 164,168,152\nAR 61,127\nCV 60,60,61\nMvaa 164,172,156\nTa 236,17,49152\nD0 42,28,46,2048\nMvaa 132,172,148\n"
"Ta 238,17,51200\nMvaa 132,168,144\nAW 127,60\nAR 60,124\nMvaa 100,168,136\nTa 240,18,32768\nMvaa 100,172,140\nTa 242,18,34816\nD0 43,28,46,3072\nMvaa 68,172,132\nSv 192,1,16,77,0\n"
"AR 61,125\nMvaa 68,168,128\nTa 244,18,49152\nMvaa 72,176,128\nTa 246,18,51200\nCV 60,60,61\nMvaa 72,180,132\nAW 126,60\nMvaa 104,180,140\nTa 248,19,32768\nMvaa 104,176,136\n"
"Ta 250,19,34816\nSv 196,1,16,77,1024\nMvaa 136,176,144\nAR 60,122\nMvaa 136,180,148\nTa 252,19,49152\nMvaa 168,180,156\nTa 254,19,51200\nAR 61,123\nMvaa 168,176,152\nCV 60,60,61\n"
"Mvaa 172,184,152\nSv 200,1,16,77,2048\nAW 125,60\nMvaa 172,188,156\nAR 60,120\nMvaa 140,188,148\nAR 61,121\nMvaa 140,184,144\nCV 60,60,61\nAW 124,60\nMvaa 108,184,136\nAR 60,118\n"
"Mvaa 108,188,140\nSv 204,1,16,77,3072\nMvaa 76,188,132\nAR 61,119\nCV 60,60,61\nMvaa 76,184,128\ns_waitcnt vmcnt(17) lgkmcnt(0)\ns_barrier\nMvaa 80,224,128\nRa 192,44,49152\n"
"AW 123,60\nMvaa 80,228,132\nRa 196,45,49152\nbuffer_store_dword v25, off, s[16:19], s54 offset:8 sc0 sc1\ns_add_u32 m0, s47, 32768\ns_add_u32 s46, s51, 2621440\nD0 6,32,46,0\n"
"Mvaa 112,228,140\nRa 200,44,51200\nMvaa 112,224,136\nRa 204,45,51200\nAR 60,116\nMvaa 144,224,144\nRa 208,46,49152\nMvaa 144,228,148\nRa 212,47,49152\nD0 7,32,46,1024\n"
"Mvaa 176,228,156\nRa 216,46,51200\nAR 61,117\nMvaa 176,224,152\nRa 220,47,51200\nMvaa 180,232,152\nTa 160,32,0\nCV 60,60,61\nMvaa 180,236,156\nTa 162,32,2048\nD0 8,32,46,2048\n"
"Mvaa 148,236,148\nTa 164,32,16384\nMvaa 148,232,144\nTa 166,32,18432\nAW 122,60\nMvaa 116,232,136\nTa 168,33,0\nMvaa 116,236,140\nTa 170,33,2048\nD0 9,32,46,3072\nMvaa 84,236,132\n"
"Ta 172,33,16384\nAR 60,114\nMvaa 84,232,128\nTa 174,33,18432\ns_add_u32 m0, s47, 49152\ns_add_u32 s46, s51, 2883584\nMvaa 88,240,128\nTa 176,34,0\n.W590:\ns_cbranch_execz .W594\n"
"AR 61,115\nMvaa 88,244,132\nTa 178,34,2048\nD0 6,32,46,0\nMvaa 120,244,140\nTa 180,34,16384\nMvaa 120,240,136\nTa 182,34,18432\nCV 60,60,61\nMvaa 152,240,144\nTa 184,35,0\n"
"Mvaa 152,244,148\nTa 186,35,2048\nD0 7,32,46,1024\nMvaa 184,244,156\nTa 188,35,16384\nAW 121,60\nMvaa 184,240,152\nTa 190,35,18432\nMvaa 188,248,152\nAR 60,112\nAR 61,113\n"
"Mvaa 188,252,156\nD0 8,32,46,2048\nMvaa 156,252,148\nCV 60,60,61\nMvaa 156,248,144\nAW 120,60\nAR 60,110\nMvaa 124,248,136\nAR 61,111\nMvaa 124,252,140\nD0 9,32,46,3072\n"
"Mvaa 92,252,132\nCV 60,60,61\nAW 119,60\nMvaa 92,248,128\nWL 0\nMvaa 64,160,192\nTa 224,36,0\nAR 60,108\nMvaa 64,164,196\nTa 226,36,2048\ns_add_u32 m0, s47, -49152\n"
"s_add_u32 s46, s52, 768\nD0 40,28,46,0\nMvaa 96,164,204\nAR 61,109\nMvaa 96,160,200\nTa 228,36,16384\nCV 60,60,61\nMvaa 128,160,208\nTa 230,36,18432\nMvaa 128,164,212\n"
"D0 41,28,46,1024\nMvaa 160,164,220\nTa 232,37,0\nAW 118,60\nMvaa 160,160,216\nTa 234,37,2048\nMvaa 164,168,216\nAR 60,106\nAR 61,107\nMvaa 164,172,220\nTa 236,37,16384\n"
"D0 42,28,46,2048\nMvaa 132,172,212\nTa 238,37,18432\nMvaa 132,168,208\nCV 60,60,61\nAW 117,60\nMvaa 100,168,200\nTa 240,38,0\nMvaa 100,172,204\nTa 242,38,2048\nD0 43,28,46,3072\n"
"Mvaa 68,172,196\ns_add_u32 s77, s77, s55\nSv 208,1,16,77,0\nMvaa 68,168,192\nTa 244,38,16384\nMvaa 72,176,192\nTa 246,38,18432\nAR 60,104\nMvaa 72,180,196\nAR 61,105\n"
"Mvaa 104,180,204\nTa 248,39,0\nMvaa 104,176,200\nTa 250,39,2048\nSv 212,1,16,77,1024\nMvaa 136,176,208\nCV 60,60,61\nMvaa 136,180,212\nTa 252,39,16384\nMvaa 168,180,220\n"
"Ta 254,39,18432\nAW 116,60\nMvaa 168,176,216\nAR 60,102\nMvaa 172,184,216\nSv 216,1,16,77,2048\nAR 61,103\nMvaa 172,188,220\nCV 60,60,61\nMvaa 140,188,212\nAW 115,60\n"
"Mvaa 140,184,208\nAR 60,100\nAR 61,101\nMvaa 108,184,200\nCV 60,60,61\nMvaa 108,188,204\nSv 220,1,16,77,3072\nMvaa 76,188,196\nAW 114,60\nAR 60,98\nMvaa 76,184,192\n"
"s_waitcnt vmcnt(25) lgkmcnt(0)\ns_barrier\nMvaa 80,224,192\nRa 128,44,0\nv_and_b32 v27, 0x70, v1\nbuffer_load_dword v27, v27, s[16:19], s61 offen offset:128 sc0 sc1\n"
"Mvaa 80,228,196\nRa 132,45,0\ns_add_u32 m0, s47, 65536\ns_add_u32 s46, s51, 3145728\nD0 6,32,46,0\nMvaa 112,228,204\nRa 136,44,2048\nMvaa 112,224,200\nRa 140,45,2048\nAR 61,99\n"
"Mvaa 144,224,208\nRa 144,46,0\nMvaa 144,228,212\nRa 148,47,0\nD0 7,32,46,1024\nMvaa 176,228,220\nRa 152,46,2048\nCV 60,60,61\nMvaa 176,224,216\nRa 156,47,2048\nMvaa 180,232,216\n"
"Ta 160,12,0\nAW 113,60\nMvaa 180,236,220\nTa 162,12,2048\nD0 8,32,46,2048\nMvaa 148,236,212\nTa 164,12,16384\nMvaa 148,232,208\nTa 166,12,18432\nAR 60,96\nMvaa 116,232,200\n"
"Ta 168,13,0\nMvaa 116,236,204\nTa 170,13,2048\nD0 9,32,46,3072\nMvaa 84,236,196\nTa 172,13,16384\nAR 61,97\nMvaa 84,232,192\nTa 174,13,18432\ns_add_u32 m0, s47, 81920\n"
"s_add_u32 s46, s51, 3407872\nMvaa 88,240,192\nTa 176,14,0\nCV 60,60,61\nMvaa 88,244,196\nTa 178,14,2048\nD0 6,32,46,0\nMvaa 120,244,204\nTa 180,14,16384\nMvaa 120,240,200\n"
"Ta 182,14,18432\nAW 112,60\nMvaa 152,240,208\nTa 184,15,0\nMvaa 152,244,212\nTa 186,15,2048\nD0 7,32,46,1024\nMvaa 184,244,220\nTa 188,15,16384\nAR 60,94\nMvaa 184,240,216\n"
"Ta 190,15,18432\nMvaa 188,248,216\nAR 61,95\nCV 60,60,61\nMvaa 188,252,220\nD0 8,32,46,2048\nMvaa 156,252,212\nAW 111,60\nMvaa 156,248,208\nAR 60,92\nAR 61,93\nMvaa 124,248,200\n"
"CV 60,60,61\nMvaa 124,252,204\nD0 9,32,46,3072\nMvaa 92,252,196\nAW 110,60\nAR 60,90\nMvaa 92,248,192\nWL 0\nMvaa 64,160,128\nTa 224,16,0\nAR 61,91\nMvaa 64,164,132\n"
"Ta 226,16,2048\ns_add_u32 s6, s56, 16384\nbuffer_load_dwordx4 v[224:227], v1, s[16:19], s6 offen offset:0 sc1\nMvaa 96,164,140\nCV 60,60,61\nMvaa 96,160,136\nTa 228,16,16384\n"
"AW 109,60\nMvaa 128,160,144\nTa 230,16,18432\nMvaa 128,164,148\nbuffer_load_dwordx4 v[228:231], v1, s[16:19], s6 offen offset:1024 sc1\nMvaa 160,164,156\nTa 232,17,0\nAR 60,88\n"
"Mvaa 160,160,152\nTa 234,17,2048\nMvaa 164,168,152\nAR 61,89\nCV 60,60,61\nMvaa 164,172,156\nTa 236,17,16384\n"
"buffer_load_dwordx4 v[232:235], v1, s[16:19], s6 offen offset:2048 sc1\nMvaa 132,172,148\nTa 238,17,18432\nMvaa 132,168,144\nAW 108,60\nAR 60,86\nMvaa 100,168,136\nTa 240,18,0\n"
"Mvaa 100,172,140\nTa 242,18,2048\nbuffer_load_dwordx4 v[236:239], v1, s[16:19], s6 offen offset:3072 sc1\nMvaa 68,172,132\nAR 61,87\nCV 60,60,61\nMvaa 68,168,128\nTa 244,18,16384\n"
"Mvaa 72,176,128\nTa 246,18,18432\nAW 107,60\nMvaa 72,180,132\nAR 60,84\nMvaa 104,180,140\nTa 248,19,0\nMvaa 104,176,136\nTa 250,19,2048\nAR 61,85\nMvaa 136,176,144\nCV 60,60,61\n"
"Mvaa 136,180,148\nTa 252,19,16384\nMvaa 168,180,156\nTa 254,19,18432\nAW 106,60\nMvaa 168,176,152\nAR 60,82\nMvaa 172,184,152\nAR 61,83\nCV 60,60,61\nMvaa 172,188,156\nAW 105,60\n"
"Mvaa 140,188,148\nAR 60,80\nMvaa 140,184,144\nAR 61,81\nCV 60,60,61\nMvaa 108,184,136\nAW 104,60\nMvaa 108,188,140\nAR 60,78\nMvaa 76,188,132\nAR 61,79\nCV 60,60,61\n"
"Mvaa 76,184,128\ns_waitcnt vmcnt(12) lgkmcnt(0)\ns_barrier\nMvaa 80,224,128\nRa 192,44,32768\nv_cmp_ne_u32 vcc, s15, v27\ns_nop 0\ns_cbranch_vccnz .S429\n.S430:\n"
"s_sub_u32 s45, s86, 131072\ns_cmp_eq_u32 s79, 1\ns_cselect_b32 s86, s94, s86\ns_add_u32 m0, s47, -65536\ns_add_u32 s46, s45, 0x0\nMvaa 80,228,132\nRa 196,45,32768\n"
"Mvaa 112,228,140\nRa 200,44,34816\nD1 1,16,46,0\nMvaa 112,224,136\nRa 204,45,34816\nAW 103,60\nMvaa 144,224,144\nRa 208,46,32768\nMvaa 144,228,148\nRa 212,47,32768\n"
"Mvaa 176,228,156\nRa 216,46,34816\nD1 1,16,46,1024\nMvaa 176,224,152\nRa 220,47,34816\nMvaa 180,232,152\nTa 160,12,32768\nAR 60,76\nMvaa 180,236,156\nTa 162,12,34816\n"
"Mvaa 148,236,148\nTa 164,12,49152\nD1 1,16,46,2048\nMvaa 148,232,144\nTa 166,12,51200\nAR 61,77\nMvaa 116,232,136\nTa 168,13,32768\nMvaa 116,236,140\nTa 170,13,34816\n"
"Mvaa 84,236,132\nTa 172,13,49152\nD1 1,16,46,3072\nMvaa 84,232,128\nTa 174,13,51200\nMvaa 88,240,128\nTa 176,14,32768\ns_add_u32 s6, s56, 20480\nMvaa 88,244,132\nTa 178,14,34816\n"
"Mvaa 120,244,140\nTa 180,14,49152\nbuffer_load_dwordx4 v[240:243], v1, s[16:19], s6 offen offset:0 sc1\nMvaa 120,240,136\nTa 182,14,51200\nCV 60,60,61\nMvaa 152,240,144\n"
"Ta 184,15,32768\nMvaa 152,244,148\nTa 186,15,34816\nMvaa 184,244,156\nTa 188,15,49152\nbuffer_load_dwordx4 v[244:247], v1, s[16:19], s6 offen offset:1024 sc1\nMvaa 184,240,152\n"
"Ta 190,15,51200\nMvaa 188,248,152\nAW 102,60\nAR 60,74\nMvaa 188,252,156\nAR 61,75\nMvaa 156,252,148\nbuffer_load_dwordx4 v[248:251], v1, s[16:19], s6 offen offset:2048 sc1\n"
"Mvaa 156,248,144\nCV 60,60,61\nAW 101,60\nMvaa 124,248,136\nAR 60,72\nMvaa 124,252,140\nAR 61,73\nMvaa 92,252,132\n"
"buffer_load_dwordx4 v[252:255], v1, s[16:19], s6 offen offset:3072 sc1\nCV 60,60,61\nMvaa 92,248,128\nWL 0\nMvaa 64,160,192\nTa 224,16,32768\ns_add_u32 s6, s56, 0\n"
"Mvaa 64,164,196\nTa 226,16,34816\nMvaa 96,164,204\nbuffer_load_dwordx4 v[192:195], v1, s[16:19], s6 offen offset:0 sc1\nMvaa 96,160,200\nTa 228,16,49152\nAW 100,60\n"
"Mvaa 128,160,208\nTa 230,16,51200\nMvaa 128,164,212\nAR 60,70\nMvaa 160,164,220\nTa 232,17,32768\nbuffer_load_dwordx4 v[196:199], v1, s[16:19], s6 offen offset:1024 sc1\n"
"Mvaa 160,160,216\nTa 234,17,34816\nMvaa 164,168,216\nAR 61,71\nCV 60,60,61\nMvaa 164,172,220\nTa 236,17,49152\nMvaa 132,172,212\nTa 238,17,51200\n"
"buffer_load_dwordx4 v[200:203], v1, s[16:19], s6 offen offset:2048 sc1\nMvaa 132,168,208\nAW 99,60\nAR 60,68\nMvaa 100,168,200\nTa 240,18,32768\nMvaa 100,172,204\nTa 242,18,34816\n"
"Mvaa 68,172,196\nbuffer_load_dwordx4 v[204:207], v1, s[16:19], s6 offen offset:3072 sc1\nAR 61,69\nMvaa 68,168,192\nTa 244,18,49152\nMvaa 72,176,192\nTa 246,18,51200\nCV 60,60,61\n"
"Mvaa 72,180,196\nAW 98,60\nMvaa 104,180,204\nTa 248,19,32768\nMvaa 104,176,200\nTa 250,19,34816\nAR 60,66\nMvaa 136,176,208\nAR 61,67\nMvaa 136,180,212\nTa 252,19,49152\n"
"Mvaa 168,180,220\nTa 254,19,51200\nCV 60,60,61\nMvaa 168,176,216\nAW 97,60\nMvaa 172,184,216\nAR 60,64\nAR 61,65\nMvaa 172,188,220\nCV 60,60,61\nMvaa 140,188,212\nAW 96,60\n"
"v_add_u32 v2, 0x0, v1\nMvaa 140,184,208\ns_lshr_b32 s2, s59, 1\nMvaa 108,184,200\nv_add_u32 v24, s2, v2\nv_xor_b32 v26, 0x1000, v24\nMvaa 108,188,204\nv_lshrrev_b32 v20, 4, v0\n"
"Mvaa 76,188,196\nv_lshlrev_b32 v20, 2, v20\nv_and_b32 v0, 15, v0\nMvaa 76,184,192\ns_waitcnt vmcnt(16) lgkmcnt(0)\ns_barrier\nMvaa 80,224,192\nRa 128,44,16384\n"
"v_sub_u32 v0, v0, v20\nMvaa 80,228,196\nRa 132,45,16384\nbuffer_store_dword v25, off, s[16:19], s54 offset:12 sc0 sc1\ns_add_u32 s6, s56, 4096\n"
"buffer_load_dwordx4 v[208:211], v1, s[16:19], s6 offen offset:0 sc1\nMvaa 112,228,204\nRa 136,44,18432\ns_lshl_b32 s2, s11, 4\nMvaa 112,224,200\nRa 140,45,18432\nMvaa 144,224,208\n"
"Ra 144,46,16384\nv_add_u32 v3, s2, v0\nMvaa 144,228,212\nRa 148,47,16384\nbuffer_load_dwordx4 v[212:215], v1, s[16:19], s6 offen offset:1024 sc1\nMvaa 176,228,220\n"
"Ra 152,46,18432\nv_subrev_u32 v0, 32, v3\nMvaa 176,224,216\nRa 156,47,18432\nMvaa 180,232,216\nTa 160,32,0\ns_lshl_b32 s2, s11, 13\nMvaa 180,236,220\nTa 162,32,2048\n"
"buffer_load_dwordx4 v[216:219], v1, s[16:19], s6 offen offset:2048 sc1\nMvaa 148,236,212\nTa 164,32,16384\ns_add_u32 s2, s2, 0x20000\nMvaa 148,232,208\nTa 166,32,18432\n"
"Mvaa 116,232,200\nTa 168,33,0\nv_add_u32 v22, s2, v1\nMvaa 116,236,204\nTa 170,33,2048\nbuffer_load_dwordx4 v[220:223], v1, s[16:19], s6 offen offset:3072 sc1\nMvaa 84,236,196\n"
"Ta 172,33,16384\nv_xor_b32 v23, 0x2000, v22\nMvaa 84,232,192\nTa 174,33,18432\ns_add_u32 m0, s47, -32768\ns_add_u32 s46, s45, 0x8000\nMvaa 88,240,192\nTa 176,34,0\n"
"v_mov_b32 v4, 0\nMvaa 88,244,196\nTa 178,34,2048\nD1 1,16,46,0\nMvaa 120,244,204\nTa 180,34,16384\nv_mov_b32 v5, 0\nMvaa 120,240,200\nTa 182,34,18432\nMvaa 152,240,208\n"
"Ta 184,35,0\nv_mov_b32 v6, 0\nMvaa 152,244,212\nTa 186,35,2048\nD1 1,16,46,1024\nMvaa 184,244,220\nTa 188,35,16384\nv_mov_b32 v7, 0\nMvaa 184,240,216\nTa 190,35,18432\n"
"Mvaa 188,248,216\nv_mov_b32 v8, 0\nv_mov_b32 v9, 0\nMvaa 188,252,220\nD1 1,16,46,2048\nMvaa 156,252,212\n.W591:\ns_cbranch_execz .W595\nv_mov_b32 v10, 0\nv_mov_b32 v11, 0\n"
"Mvaa 156,248,208\nv_mov_b32 v12, 0\nMvaa 124,248,200\nv_mov_b32 v13, 0\nv_mov_b32 v14, 0\nMvaa 124,252,204\nD1 1,16,46,3072\nMvaa 92,252,196\nv_mov_b32 v15, 0\nv_mov_b32 v16, 0\n"
"Mvaa 92,248,192\nWL 0\nMvaa 64,160,128\nTa 224,36,0\nv_mov_b32 v17, 0\nMvaa 64,164,132\nTa 226,36,2048\ns_add_u32 m0, s47, -16384\ns_add_u32 s46, s45, 0x400000\nD1 1,16,46,0\n"
"Mvaa 68,172,132\nv_mov_b32 v18, 0\nv_mov_b32 v19, 0\nMvaa 68,168,128\nTa 228,36,16384\nMvaa 72,176,128\nTa 230,36,18432\nMvaa 72,180,132\nD1 1,16,46,1024\nMvaa 76,188,132\n"
"Ta 232,37,0\nMvaa 76,184,128\nTa 234,37,2048\nMvaa 108,184,136\nv_mul_f32 v64, s57, v64\nv_mul_f32 v65, s57, v65\nMvaa 108,188,140\nTa 236,37,16384\nD1 1,16,46,2048\n"
"Mvaa 104,180,140\nTa 238,37,18432\nCV 64,64,65\nMvaa 104,176,136\nv_mul_f32 v66, s57, v66\nv_mul_f32 v67, s57, v67\nMvaa 100,168,136\nTa 240,38,0\nCV 65,66,67\nMvaa 100,172,140\n"
"Ta 242,38,2048\nD1 1,16,46,3072\nMvaa 96,164,140\nv_mul_f32 v68, s57, v68\nv_mul_f32 v69, s57, v69\nMvaa 96,160,136\nTa 244,38,16384\nCV 66,68,69\nMvaa 128,160,144\n"
"Ta 246,38,18432\nv_mul_f32 v70, s57, v70\nMvaa 128,164,148\nv_mul_f32 v71, s57, v71\nCV 67,70,71\nMvaa 132,172,148\nTa 248,39,0\nAW 192,64\nMvaa 132,168,144\nTa 250,39,2048\n"
"AW 193,65\nMvaa 136,176,144\nAW 194,66\nAW 195,67\nMvaa 136,180,148\nTa 252,39,16384\nv_mul_f32 v72, s57, v72\nMvaa 140,188,148\nTa 254,39,18432\nv_mul_f32 v73, s57, v73\n"
"Mvaa 140,184,144\nCV 72,72,73\nv_mul_f32 v74, s57, v74\nMvaa 172,184,152\nv_mul_f32 v75, s57, v75\nCV 73,74,75\nMvaa 172,188,156\nv_mul_f32 v76, s57, v76\nv_mul_f32 v77, s57, v77\n"
"Mvaa 168,180,156\nCV 74,76,77\nv_mul_f32 v78, s57, v78\nMvaa 168,176,152\nv_mul_f32 v79, s57, v79\nCV 75,78,79\nMvaa 164,168,152\nAW 200,72\nAW 201,73\nMvaa 164,172,156\n"
"AW 202,74\nAW 203,75\nMvaa 160,164,156\nv_mul_f32 v96, s57, v96\nv_mul_f32 v97, s57, v97\nMvaa 160,160,152\ns_waitcnt vmcnt(21) lgkmcnt(0)\ns_barrier\nMvaa 80,224,128\n"
"CV 96,96,97\nv_mul_f32 v98, s57, v98\nMvaa 80,228,132\ns_add_u32 m0, s47, 0\ns_add_u32 s46, s86, 0xffff0000\nD1 1,16,46,0\nMvaa 84,236,132\nv_mul_f32 v99, s57, v99\nCV 97,98,99\n"
"Mvaa 84,232,128\nv_mul_f32 v100, s57, v100\nv_mul_f32 v101, s57, v101\nMvaa 88,240,128\nCV 98,100,101\nv_mul_f32 v102, s57, v102\nMvaa 88,244,132\nD1 1,16,46,1024\n"
"v_mul_f32 v103, s57, v103\nMvaa 92,252,132\nCV 99,102,103\nAW 196,96\nMvaa 92,248,128\nAW 197,97\nAW 198,98\nMvaa 124,248,136\nAW 199,99\nv_mul_f32 v128, s57, v128\n"
"Mvaa 124,252,140\nD1 1,16,46,2048\nv_mul_f32 v129, s57, v129\nMvaa 120,244,140\nCV 128,128,129\nv_mul_f32 v130, s57, v130\nMvaa 120,240,136\nv_mul_f32 v131, s57, v131\n"
"CV 129,130,131\nMvaa 116,232,136\nRa 160,24,0\nv_mul_f32 v132, s57, v132\nMvaa 116,236,140\nD1 1,16,46,3072\nv_mul_f32 v133, s57, v133\nMvaa 112,228,140\nRa 164,24,1024\n"
"CV 130,132,133\nMvaa 112,224,136\ns_add_u32 m0, s47, 16384\ns_add_u32 s46, s45, 0x408000\nMvaa 144,224,144\nRa 168,24,2048\nv_mul_f32 v134, s57, v134\nMvaa 144,228,148\n"
"D1 1,16,46,0\nv_mul_f32 v135, s57, v135\nMvaa 148,236,148\nRa 172,24,3072\nCV 131,134,135\nMvaa 148,232,144\nds_write_b128 v23, v[128:131] offset:0\nv_mul_f32 v80, s57, v80\n"
"Mvaa 152,240,144\nRa 176,26,0\nv_mul_f32 v81, s57, v81\nMvaa 152,244,148\nD1 1,16,46,1024\nCV 80,80,81\nMvaa 156,252,148\nRa 180,26,1024\nv_mul_f32 v82, s57, v82\n"
"Mvaa 156,248,144\nv_mul_f32 v83, s57, v83\nCV 81,82,83\nMvaa 188,248,152\nRa 184,26,2048\nv_mul_f32 v84, s57, v84\nMvaa 188,252,156\nD1 1,16,46,2048\nv_mul_f32 v85, s57, v85\n"
"Mvaa 184,244,156\nRa 188,26,3072\nCV 82,84,85\nMvaa 184,240,152\nv_mul_f32 v86, s57, v86\nv_mul_f32 v87, s57, v87\nMvaa 180,232,152\nCV 83,86,87\nAW 208,80\nMvaa 180,236,156\n"
"D1 1,16,46,3072\nAW 209,81\nMvaa 176,228,156\nAW 210,82\nAW 211,83\nMvaa 176,224,152\ns_waitcnt vmcnt(16) lgkmcnt(5)\ns_cmp_eq_u32 s79, 1\ns_cbranch_scc0 .E432\nv_mov_b32 v4, s97\n"
"v_mov_b32 v5, s97\nv_mov_b32 v6, s97\nv_mov_b32 v7, s97\nv_mov_b32 v12, s97\nv_mov_b32 v13, s97\nv_mov_b32 v14, s97\nv_mov_b32 v15, s97\nv_cmpx_gt_i32 vcc, 19, v3\n"
"v_mov_b32 v19, s97\nv_cmpx_gt_i32 vcc, 18, v3\nv_mov_b32 v18, s97\nv_cmpx_gt_i32 vcc, 17, v3\nv_mov_b32 v17, s97\nv_cmpx_gt_i32 vcc, 16, v3\nv_mov_b32 v16, s97\n"
"v_cmpx_gt_i32 vcc, 3, v3\nv_mov_b32 v11, s97\nv_cmpx_gt_i32 vcc, 2, v3\nv_mov_b32 v10, s97\nv_cmpx_gt_i32 vcc, 1, v3\nv_mov_b32 v9, s97\nv_cmpx_gt_i32 vcc, 0, v3\n"
"v_mov_b32 v8, s97\ns_mov_b64 exec, -1\ns_nop 2\n.E432:\ns_nop 0\nCvav 32,160,192,4\nRa 128,24,8192\nv_mul_f32 v88, s57, v88\nMvav 32,164,200\nv_mul_f32 v89, s57, v89\nCV 88,88,89\n"
"Mvav 32,168,208\nRa 132,24,9216\nv_mul_f32 v90, s57, v90\nMvav 32,172,216\nv_mul_f32 v91, s57, v91\nCV 89,90,91\nCvav 36,172,220,8\nRa 136,24,10240\nv_mul_f32 v92, s57, v92\n"
"Mvav 36,168,212\ns_add_u32 m0, s89, 32768\nv_mul_f32 v93, s57, v93\nMvav 36,164,204\nRa 140,24,11264\nWL 4\ns_barrier\nMvav 36,160,196\nCV 90,92,93\nv_mul_f32 v94, s57, v94\n"
"Mvav 36,176,228\nRa 144,26,8192\nSa 96,1,16,78,0\nMvav 36,180,236\ns_add_u32 s46, s86, s88\nv_mul_f32 v95, s57, v95\nMvav 36,184,244\nRa 148,26,9216\nCV 91,94,95\nMvav 36,188,252\n"
"AW 216,88\nAW 217,89\nMvav 32,188,248\nRa 152,26,10240\nAW 218,90\nMvav 32,184,240\nD1 1,16,46,0\nAW 219,91\nMvav 32,180,232\nRa 156,26,11264\nWL 4\nMvav 32,176,224\n"
"v_mov_b32 v86, 0\nv_mov_b32 v87, 0\nCvav 40,128,192,12\nSa 100,1,16,78,1024\nv_mov_b32 v88, 0\nMvav 40,132,200\ns_cmp_eq_u32 s99, -1\nMvav 40,136,208\ns_cselect_b32 s87, s95, s87\n"
"v_mov_b32 v89, 0\nv_mul_f32 v136, s57, v136\nv_mul_f32 v137, s57, v137\nCV 136,136,137\nv_mul_f32 v138, s57, v138\nv_mul_f32 v139, s57, v139\nMvav 40,140,216\nCV 137,138,139\n"
"v_mul_f32 v140, s57, v140\nv_mul_f32 v141, s57, v141\nCV 138,140,141\nv_mul_f32 v142, s57, v142\nv_mul_f32 v143, s57, v143\nCvav 44,140,220,16\nv_and_b32 v27, 0x70, v1\n"
"buffer_load_dword v27, v27, s[16:19], s61 offen offset:136 sc0 sc1\nCV 139,142,143\nds_write_b128 v23, v[136:139] offset:2048\nv_mul_f32 v144, s57, v144\n"
"v_mul_f32 v145, s57, v145\nCV 144,144,145\nMvav 44,136,212\nD1 1,16,46,1024\nv_mul_f32 v146, s57, v146\nv_mul_f32 v147, s57, v147\nCV 145,146,147\nv_mul_f32 v148, s57, v148\n"
"v_mul_f32 v149, s57, v149\nMvav 44,132,204\nWL 1\nCV 146,148,149\nv_mul_f32 v150, s57, v150\nv_mul_f32 v151, s57, v151\nCV 147,150,151\nds_write_b128 v23, v[144:147] offset:4096\n"
"v_mul_f32 v152, s57, v152\nMvav 44,128,196\nv_mul_f32 v153, s57, v153\nCV 152,152,153\nv_mul_f32 v154, s57, v154\nv_mul_f32 v155, s57, v155\nCV 153,154,155\n"
"v_mul_f32 v156, s57, v156\nMvav 44,144,228\nSa 104,1,16,78,2048\nv_mul_f32 v157, s57, v157\nCV 154,156,157\nv_mul_f32 v158, s57, v158\nv_mul_f32 v159, s57, v159\nCV 155,158,159\n"
"ds_write_b128 v23, v[152:155] offset:6144\nMvav 44,148,236\ns_add_u32 s88, s88, 32768\nEX 80,32\nv_mul_f32 v160, s57, v160\nv_mul_f32 v161, s57, v161\nCV 160,160,161\n"
"Mvav 44,152,244\nv_mul_f32 v162, s57, v162\nv_mul_f32 v163, s57, v163\nCV 161,162,163\nv_mul_f32 v164, s57, v164\nv_mul_f32 v165, s57, v165\nCV 162,164,165\n"
"v_mul_f32 v166, s57, v166\nMvav 44,156,252\nEX 81,33\nv_mul_f32 v167, s57, v167\nCV 163,166,167\nds_write_b128 v23, v[160:163] offset:1024\nMvav 40,156,248\n"
"v_mul_f32 v168, s57, v168\nv_mul_f32 v169, s57, v169\nCV 168,168,169\nv_mul_f32 v170, s57, v170\nv_mul_f32 v171, s57, v171\nCV 169,170,171\nv_mul_f32 v172, s57, v172\n"
"Mvav 40,152,240\nD1 1,16,46,2048\nv_mul_f32 v173, s57, v173\nCV 170,172,173\nv_mul_f32 v174, s57, v174\nv_mul_f32 v175, s57, v175\nCV 171,174,175\nMvav 40,148,232\n"
"ds_write_b128 v23, v[168:171] offset:3072\nv_mul_f32 v176, s57, v176\nv_mul_f32 v177, s57, v177\nCV 176,176,177\nv_mul_f32 v178, s57, v178\nv_mul_f32 v179, s57, v179\n"
"CV 177,178,179\nMvav 40,144,224\nAD 84,80,81\nCV 64,80,81\nv_mul_f32 v180, s57, v180\nv_mul_f32 v181, s57, v181\nCV 178,180,181\nv_mul_f32 v182, s57, v182\nSa 108,1,16,78,3072\n"
"v_mul_f32 v183, s57, v183\nCV 179,182,183\nds_write_b128 v23, v[176:179] offset:5120\nv_mul_f32 v184, s57, v184\nv_mul_f32 v185, s57, v185\nCV 184,184,185\nEX 82,34\n"
"v_mul_f32 v186, s57, v186\nv_mul_f32 v187, s57, v187\nCV 185,186,187\nv_mul_f32 v188, s57, v188\nv_mul_f32 v189, s57, v189\nCV 186,188,189\nv_mul_f32 v190, s57, v190\n"
"v_mul_f32 v191, s57, v191\nCV 187,190,191\nds_write_b128 v23, v[184:187] offset:7168\ns_waitcnt vmcnt(16) lgkmcnt(0)\nEX 83,35\nAD 84,84,82\nD1 1,16,46,3072\n"
"s_add_u32 m0, s89, 49152\ns_barrier\nAD 84,84,83\nCV 65,82,83\nRa 128,24,32768\nRa 224,22,0\nRa 228,22,1024\nRa 232,22,2048\ns_add_u32 s46, s87, s88\nEX 80,36\nRa 132,24,33792\n"
"Ra 236,22,3072\nRa 240,22,4096\nEX 81,37\nRa 244,22,5120\nRa 136,24,34816\nRa 248,22,6144\nRa 252,22,7168\nD1 1,16,46,0\nv_mul_f32 v104, s57, v104\nv_mul_f32 v105, s57, v105\n"
"Ra 140,24,35840\nCV 104,104,105\nv_mul_f32 v106, s57, v106\nv_mul_f32 v107, s57, v107\nAD 85,80,81\nCV 68,80,81\nCV 105,106,107\nRa 144,26,32768\nv_mul_f32 v108, s57, v108\n"
"v_mul_f32 v109, s57, v109\nCV 106,108,109\ns_cmp_eq_u32 s13, 7\nEX 82,38\nRa 148,26,33792\ns_cbranch_scc0 .E419\nv_cmpx_gt_i32 vcc, 19, v0\nv_mov_b32 v19, s97\n"
"v_cmpx_gt_i32 vcc, 18, v0\nv_mov_b32 v18, s97\nv_cmpx_gt_i32 vcc, 17, v0\nv_mov_b32 v17, s97\nv_cmpx_gt_i32 vcc, 16, v0\nv_mov_b32 v16, s97\nv_cmpx_gt_i32 vcc, 3, v0\n"
"v_mov_b32 v11, s97\nv_cmpx_gt_i32 vcc, 2, v0\nv_mov_b32 v10, s97\nv_cmpx_gt_i32 vcc, 1, v0\nv_mov_b32 v9, s97\nv_cmpx_gt_i32 vcc, 0, v0\nv_mov_b32 v8, s97\ns_mov_b64 exec, -1\n"
"s_nop 2\n.E419:\nEX 83,39\nv_mul_f32 v110, s57, v110\nRa 152,26,34816\nAD 85,85,82\nv_mul_f32 v111, s57, v111\nD1 1,16,46,1024\nCV 107,110,111\nAW 204,104\nRa 156,26,35840\n"
"AW 205,105\nAW 206,106\nAW 207,107\nAD 85,85,83\nCV 69,82,83\nv_mul_f32 v112, s57, v112\nRa 160,24,40960\nv_mul_f32 v113, s57, v113\nCV 112,112,113\nv_mul_f32 v114, s57, v114\n"
"s_xor_b32 s89, s89, 0x10000\nEX 80,40\nRa 164,24,41984\nv_mul_f32 v115, s57, v115\nCV 113,114,115\nWL 6\nEX 81,41\nv_mul_f32 v116, s57, v116\nRa 168,24,43008\nAD 84,84,80\n"
"v_mul_f32 v117, s57, v117\nD1 1,16,46,2048\nCV 114,116,117\nv_mul_f32 v118, s57, v118\nRa 172,24,44032\nv_mul_f32 v119, s57, v119\nCV 115,118,119\nAW 212,112\nAD 84,84,81\n"
"CV 66,80,81\nAW 213,113\nCvav 48,128,192,4\nRa 176,26,40960\nAW 214,114\nAW 215,115\nv_mul_f32 v120, s57, v120\nMvav 48,132,200\nEX 82,42\nv_mul_f32 v121, s57, v121\n"
"Mvav 48,136,208\nRa 180,26,41984\nCV 120,120,121\nv_mul_f32 v122, s57, v122\nMvav 48,140,216\nWL 6\nEX 83,43\nWV 8\nv_cmp_ne_u32 vcc, s15, v27\ns_nop 0\ns_cbranch_vccnz .S433\n"
".S434:\nCvav 52,140,220,8\nRa 184,26,43008\nAD 84,84,82\nv_mul_f32 v123, s57, v123\nMvav 52,136,212\nD1 1,16,46,3072\ns_add_u32 m0, s89, 0\nCV 121,122,123\nMvav 52,132,204\n"
"Ra 188,26,44032\nv_mul_f32 v124, s57, v124\nv_mul_f32 v125, s57, v125\nCV 122,124,125\nMvav 52,128,196\nAD 84,84,83\nCV 67,82,83\nv_mul_f32 v126, s57, v126\nMvav 52,144,228\n"
"Ra 128,2,49152\ns_add_u32 s78, s78, s55\nSa 112,1,16,78,0\nv_mul_f32 v127, s57, v127\nMvav 52,148,236\ns_add_u32 s46, s86, s88\nEX 80,44\nMvav 52,152,244\nRa 132,2,50176\n"
"v_xor_b32 v24, 0x10000, v24\nCV 123,126,127\nMvav 52,156,252\nWL 6\nEX 81,45\nAW 220,120\nMvav 48,156,248\nRa 136,2,51200\nAD 85,85,80\nAW 221,121\nMvav 48,152,240\nD1 1,16,46,0\n"
"AW 222,122\nAW 223,123\nMvav 48,148,232\nRa 140,2,52224\nv_mov_b32 v112, -1\nv_mov_b32 v113, -1\nv_mov_b32 v114, -1\nMvav 48,144,224\nAD 85,85,81\nCV 70,80,81\nv_mov_b32 v115, -1\n"
"Cvav 56,160,192,12\nRa 144,2,53248\nSa 116,1,16,78,1024\nMvav 56,164,200\nEX 82,46\nMvav 56,168,208\nRa 148,2,54272\nv_xor_b32 v26, 0x10000, v26\nMvav 56,172,216\nWL 6\nEX 83,47\n"
"Cvav 60,172,220,16\nRa 152,2,55296\nAD 85,85,82\nMvav 60,168,212\nD1 1,16,46,1024\nMvav 60,164,204\nRa 156,2,56320\nMvav 60,160,196\nAD 85,85,83\nCV 71,82,83\nMvav 60,176,228\n"
"Ra 160,2,57344\nv_max_f32 v90, v84, v85\nMvav 60,180,236\ns_add_u32 s88, s88, 32768\nEX 80,48\nMvav 60,184,244\nRa 164,2,58368\nv_cmp_lt_f32 vcc, s63, v90\nMvav 60,188,252\nWL 8\n"
"EX 81,49\nMvav 56,188,248\nRa 168,2,59392\ns_cbranch_vccnz .T421\n.T422:\nv_add_f32 v20, s93, v84\nv_mov_b32 v21, v85\nv_mov_b32 v22, v20\nv_mov_b32 v23, v21\nMvav 56,184,240\n"
"D1 1,16,46,2048\nv_permlane32_swap_b32 v20, v22\nv_permlane32_swap_b32 v21, v23\nAD 20,20,22\nAD 21,21,23\nv_mov_b32 v22, v20\nv_mov_b32 v23, v21\nMvav 56,180,232\nRa 172,2,60416\n"
"v_permlane16_swap_b32 v20, v22\nv_permlane16_swap_b32 v21, v23\nAD 20,20,22\nAD 21,21,23\nv_min_f32 v90, v20, v21\nv_cmp_gt_f32 vcc, s64, v90\ns_nop 0\ns_cbranch_vccnz .T423\n"
".T424:\nAD 86,86,84\nMvav 56,176,224\nAD 84,80,81\nCV 72,80,81\nZaav 0,128,64\nRa 176,2,61440\nSa 120,1,16,78,2048\nZaav 4,128,68\nEX 82,50\nZaav 12,132,68\nRa 180,2,62464\n"
"Zaav 8,132,64\nWL 8\nEX 83,51\nZaav 16,136,64\nRa 184,2,63488\nAD 84,84,82\nZaav 20,136,68\nD1 1,16,46,3072\ns_add_u32 m0, s89, 16384\nZaav 28,140,68\n.W592:\n"
"s_cbranch_execz .W596\nRa 188,2,64512\ns_barrier\nZaav 24,140,64\nAD 84,84,83\nCV 73,82,83\nZaav 32,144,64\nRa 128,24,0\nSa 124,1,16,78,3072\nZaav 36,144,68\n"
"s_add_u32 s46, s87, s88\nEX 80,52\nZaav 44,148,68\nRa 132,24,1024\nv_xor_b32 v2, 0x10000, v2\nZaav 40,148,64\nWL 8\nEX 81,53\nZaav 48,152,64\nRa 136,24,2048\nAD 87,87,85\n"
"Zaav 52,152,68\nD1 1,16,46,0\nZaav 60,156,68\nRa 140,24,3072\nZaav 56,156,64\nAD 85,80,81\nCV 76,80,81\nZaav 64,160,64\nRa 144,26,0\nZaav 68,160,68\nEX 82,54\nZaav 76,164,68\n"
"Ra 148,26,1024\nZaav 72,164,64\nWL 8\nEX 83,55\nZaav 80,168,64\nRa 152,26,2048\nAD 85,85,82\nZaav 84,168,68\nD1 1,16,46,1024\nZaav 92,172,68\nRa 156,26,3072\nZaav 88,172,64\n"
"AD 85,85,83\nCV 77,82,83\nZaav 96,176,64\nRa 160,24,8192\nZaav 100,176,68\nEX 80,56\nZaav 108,180,68\nRa 164,24,9216\nZaav 104,180,64\nWL 6\nEX 81,57\nZaav 112,184,64\n"
"Ra 168,24,10240\nAD 84,84,80\nv_and_b32 v27, 0x70, v1\nbuffer_load_dword v27, v27, s[16:19], s61 offen offset:140 sc0 sc1\nZaav 116,184,68\nD1 1,16,46,2048\nZaav 124,188,68\n"
"Ra 172,24,11264\nZaav 120,188,64\nAD 84,84,81\nCV 74,80,81\ns_sub_u32 s99, s99, 1\ns_cmp_eq_u32 s13, 7\ns_cbranch_scc1 .SW417\n.p2align 6\n.A448:\nCvav 32,128,192,4\n"
"Ra 176,26,8192\nMvav 32,132,200\nEX 82,58\nMvav 32,136,208\nRa 180,26,9216\nMvav 32,140,216\nWL 6\nEX 83,59\nCvav 36,140,220,8\nRa 184,26,10240\nAD 84,84,82\nMvav 36,136,212\n"
"D1 1,16,46,3072\ns_add_u32 m0, s89, 32768\nMvav 36,132,204\nRa 188,26,11264\nMvav 36,128,196\nAD 84,84,83\nCV 75,82,83\nMvav 36,144,228\nRa 128,2,16384\nMvav 36,148,236\n"
"s_add_u32 s46, s86, s88\nEX 80,60\nMvav 36,152,244\nRa 132,2,17408\nMvav 36,156,252\nWL 6\nEX 81,61\nMvav 32,156,248\nRa 136,2,18432\nAD 85,85,80\nMvav 32,152,240\nD1 1,16,46,0\n"
"Mvav 32,148,232\nRa 140,2,19456\nSv 112,1,16,49,0\nMvav 32,144,224\nAD 85,85,81\nCV 78,80,81\nCvav 40,160,192,12\nRa 144,2,20480\nMvav 40,164,200\nEX 82,62\nMvav 40,168,208\n"
"Ra 148,2,21504\nMvav 40,172,216\nWL 6\nEX 83,63\nCvav 44,172,220,16\nRa 152,2,22528\nAD 85,85,82\nMvav 44,168,212\nD1 1,16,46,1024\nMvav 44,164,204\nRa 156,2,23552\n"
"Mvav 44,160,196\nAD 85,85,83\nCV 79,82,83\nMvav 44,176,228\nRa 160,2,24576\nv_max_f32 v90, v84, v85\nMvav 44,180,236\ns_add_u32 s88, s88, 32768\nEX 80,32\nMvav 44,184,244\n"
"Ra 164,2,25600\nv_cmp_lt_f32 vcc, s63, v90\nMvav 44,188,252\nWL 8\nEX 81,33\nMvav 40,188,248\nRa 168,2,26624\ns_cbranch_vccnz .T456\n.T457:\nMvav 40,184,240\nD1 1,16,46,2048\n"
"Mvav 40,180,232\nRa 172,2,27648\nAD 86,86,84\nMvav 40,176,224\nAD 84,80,81\nCV 64,80,81\nMaav 0,128,72\nRa 176,2,28672\nMaav 4,128,76\nEX 82,34\nMaav 12,132,76\nRa 180,2,29696\n"
"Maav 8,132,72\ns_waitcnt vmcnt(13) lgkmcnt(8)\nEX 83,35\nMaav 16,136,72\nRa 184,2,30720\nAD 84,84,82\nMaav 20,136,76\nD1 1,16,46,3072\ns_add_u32 m0, s89, 49152\nMaav 28,140,76\n"
"Ra 188,2,31744\ns_barrier\nMaav 24,140,72\nAD 84,84,83\nCV 65,82,83\nMaav 32,144,72\nRa 128,24,32768\nMaav 36,144,76\ns_add_u32 s46, s87, s88\nEX 80,36\nMaav 44,148,76\n"
"Ra 132,24,33792\nMaav 40,148,72\nWL 8\nEX 81,37\nMaav 48,152,72\nRa 136,24,34816\nAD 87,87,85\nMaav 52,152,76\nD1 1,16,46,0\nMaav 60,156,76\nRa 140,24,35840\nMaav 56,156,72\n"
"AD 85,80,81\nCV 68,80,81\nMaav 64,160,72\nRa 144,26,32768\nMaav 68,160,76\ns_cmp_eq_u32 s99, 0\nEX 82,38\nMaav 76,164,76\nRa 148,26,33792\ns_cbranch_scc1 .E450\n.E451:\n"
"Maav 72,164,72\nWL 8\nEX 83,39\nMaav 80,168,72\nRa 152,26,34816\nAD 85,85,82\nMaav 84,168,76\nD1 1,16,46,1024\nMaav 92,172,76\nRa 156,26,35840\nMaav 88,172,72\nAD 85,85,83\n"
"CV 69,82,83\nMaav 96,176,72\nRa 160,24,40960\nMaav 100,176,76\ns_xor_b32 s89, s89, 0x10000\nEX 80,40\nMaav 108,180,76\nRa 164,24,41984\nMaav 104,180,72\nWL 6\nEX 81,41\n"
"Maav 112,184,72\nRa 168,24,43008\nAD 84,84,80\nMaav 116,184,76\nD1 1,16,46,2048\nMaav 124,188,76\nRa 172,24,44032\nMaav 120,188,72\nAD 84,84,81\nCV 66,80,81\nCvav 48,128,192,4\n"
"Ra 176,26,40960\nMvav 48,132,200\ns_cmp_eq_u32 s99, 0\nEX 82,42\nMvav 48,136,208\nRa 180,26,41984\ns_cselect_b32 s86, s94, s86\nMvav 48,140,216\nWL 6\nEX 83,43\nCvav 52,140,220,8\n"
"Ra 184,26,43008\nAD 84,84,82\nMvav 52,136,212\nD1 1,16,46,3072\ns_add_u32 m0, s89, 0\nMvav 52,132,204\nRa 188,26,44032\nMvav 52,128,196\nAD 84,84,83\nCV 67,82,83\nMvav 52,144,228\n"
"Ra 128,2,49152\nMvav 52,148,236\ns_add_u32 s46, s86, s88\nEX 80,44\nMvav 52,152,244\nRa 132,2,50176\nv_xor_b32 v24, 0x10000, v24\nMvav 52,156,252\nWL 6\nEX 81,45\nMvav 48,156,248\n"
"Ra 136,2,51200\nAD 85,85,80\nMvav 48,152,240\nD1 1,16,46,0\nMvav 48,148,232\nRa 140,2,52224\nSv 112,1,16,49,1024\nMvav 48,144,224\nAD 85,85,81\nCV 70,80,81\nCvav 56,160,192,12\n"
"Ra 144,2,53248\nMvav 56,164,200\ns_add_u32 s49, s49, 131072\nEX 82,46\nMvav 56,168,208\nRa 148,2,54272\nv_xor_b32 v26, 0x10000, v26\nMvav 56,172,216\nWL 6\nEX 83,47\n"
"Cvav 60,172,220,16\nRa 152,2,55296\nAD 85,85,82\nMvav 60,168,212\nD1 1,16,46,1024\nMvav 60,164,204\nRa 156,2,56320\nMvav 60,160,196\nAD 85,85,83\nCV 71,82,83\nMvav 60,176,228\n"
"Ra 160,2,57344\nv_max_f32 v90, v84, v85\nMvav 60,180,236\ns_add_u32 s88, s88, 32768\nEX 80,48\nMvav 60,184,244\nRa 164,2,58368\nv_cmp_lt_f32 vcc, s63, v90\nMvav 60,188,252\nWL 8\n"
"EX 81,49\nMvav 56,188,248\nRa 168,2,59392\ns_cbranch_vccnz .T458\n.T459:\nMvav 56,184,240\nD1 1,16,46,2048\nMvav 56,180,232\nRa 172,2,60416\nAD 86,86,84\nMvav 56,176,224\n"
"AD 84,80,81\nCV 72,80,81\nMaav 0,128,64\nRa 176,2,61440\nMaav 4,128,68\nEX 82,50\nMaav 12,132,68\nRa 180,2,62464\nMaav 8,132,64\ns_waitcnt vmcnt(13) lgkmcnt(8)\nEX 83,51\n"
"Maav 16,136,64\nRa 184,2,63488\nAD 84,84,82\nMaav 20,136,68\nD1 1,16,46,3072\ns_add_u32 m0, s89, 16384\nMaav 28,140,68\nRa 188,2,64512\ns_barrier\nMaav 24,140,64\nAD 84,84,83\n"
"CV 73,82,83\nMaav 32,144,64\nRa 128,24,0\nMaav 36,144,68\ns_add_u32 s46, s87, s88\nEX 80,52\nMaav 44,148,68\nRa 132,24,1024\nv_xor_b32 v2, 0x10000, v2\nMaav 40,148,64\nWL 8\n"
"EX 81,53\nMaav 48,152,64\nRa 136,24,2048\nAD 87,87,85\nMaav 52,152,68\nD1 1,16,46,0\nMaav 60,156,68\nRa 140,24,3072\nMaav 56,156,64\nAD 85,80,81\nCV 76,80,81\nMaav 64,160,64\n"
"Ra 144,26,0\nMaav 68,160,68\ns_cmp_lt_u32 s99, s92\nEX 82,54\nMaav 76,164,68\nRa 148,26,1024\ns_cbranch_scc1 .E452\n.E453:\nMaav 72,164,64\nWL 8\nEX 83,55\nMaav 80,168,64\n"
"Ra 152,26,2048\nAD 85,85,82\nMaav 84,168,68\nD1 1,16,46,1024\nMaav 92,172,68\nRa 156,26,3072\nMaav 88,172,64\nAD 85,85,83\nCV 77,82,83\nMaav 96,176,64\nRa 160,24,8192\n"
"Maav 100,176,68\ns_sub_u32 s90, s90, 1\nEX 80,56\nMaav 108,180,68\nRa 164,24,9216\ns_cbranch_scc1 .E454\n.E455:\nMaav 104,180,64\nWL 6\nEX 81,57\nMaav 112,184,64\nRa 168,24,10240\n"
"AD 84,84,80\nMaav 116,184,68\nD1 1,16,46,2048\ns_sub_u32 s99, s99, 1\nMaav 124,188,68\nRa 172,24,11264\nMaav 120,188,64\nAD 84,84,81\nCV 74,80,81\ns_cbranch_scc0 .A448\n"
"s_branch .A460\n.E450:\nv_cmpx_gt_i32 vcc, 19, v0\nv_mov_b32 v15, s97\nv_cmpx_gt_i32 vcc, 18, v0\nv_mov_b32 v14, s97\nv_cmpx_gt_i32 vcc, 17, v0\nv_mov_b32 v13, s97\n"
"v_cmpx_gt_i32 vcc, 16, v0\nv_mov_b32 v12, s97\nv_cmpx_gt_i32 vcc, 3, v0\nv_mov_b32 v7, s97\nv_cmpx_gt_i32 vcc, 2, v0\nv_mov_b32 v6, s97\nv_cmpx_gt_i32 vcc, 1, v0\n"
"v_mov_b32 v5, s97\nv_cmpx_gt_i32 vcc, 0, v0\nv_mov_b32 v4, s97\ns_mov_b64 exec, -1\ns_nop 2\ns_branch .E451\n.E452:\ns_cmp_eq_u32 s99, 0\ns_cbranch_scc1 .K449\n"
"v_cmpx_gt_i32 vcc, 19, v3\nv_mov_b32 v15, s97\nv_cmpx_gt_i32 vcc, 18, v3\nv_mov_b32 v14, s97\nv_cmpx_gt_i32 vcc, 17, v3\nv_mov_b32 v13, s97\nv_cmpx_gt_i32 vcc, 16, v3\n"
"v_mov_b32 v12, s97\nv_cmpx_gt_i32 vcc, 3, v3\nv_mov_b32 v7, s97\nv_cmpx_gt_i32 vcc, 2, v3\nv_mov_b32 v6, s97\nv_cmpx_gt_i32 vcc, 1, v3\nv_mov_b32 v5, s97\n"
"v_cmpx_gt_i32 vcc, 0, v3\nv_mov_b32 v4, s97\ns_mov_b64 exec, -1\ns_nop 2\ns_branch .E453\n.K449:\nv_cmpx_gt_i32 vcc, 19, v3\nv_mov_b32 v19, s97\nv_cmpx_gt_i32 vcc, 18, v3\n"
"v_mov_b32 v18, s97\nv_cmpx_gt_i32 vcc, 17, v3\nv_mov_b32 v17, s97\nv_cmpx_gt_i32 vcc, 16, v3\nv_mov_b32 v16, s97\nv_cmpx_gt_i32 vcc, 3, v3\nv_mov_b32 v11, s97\n"
"v_cmpx_gt_i32 vcc, 2, v3\nv_mov_b32 v10, s97\nv_cmpx_gt_i32 vcc, 1, v3\nv_mov_b32 v9, s97\nv_cmpx_gt_i32 vcc, 0, v3\nv_mov_b32 v8, s97\ns_mov_b64 exec, -1\ns_nop 2\n"
"s_branch .E453\n.E454:\ns_swappc_b64 s[80:81], s[8:9]\ns_branch .E455\n.HD411:\nv_cmp_ne_u32 vcc, s15, v27\ns_nop 0\ns_cbranch_vccnz .S436\n.S437:\n"
"buffer_store_dword v25, off, s[16:19], s54 offset:4 sc0 sc1\nv_and_b32 v27, 0x70, v1\nbuffer_load_dword v27, v27, s[16:19], s61 offen offset:12 sc0 sc1\ns_mov_b32 s90, 1\n"
"s_swappc_b64 s[8:9], s[80:81]\nv_cmp_ne_u32 vcc, s15, v27\ns_nop 0\ns_cbranch_vccnz .S439\n.S440:\nv_and_b32 v27, 0x70, v1\n"
"buffer_load_dword v27, v27, s[16:19], s61 offen offset:8 sc0 sc1\ns_mov_b32 s90, 1\ns_swappc_b64 s[8:9], s[80:81]\nv_cmp_ne_u32 vcc, s15, v27\ns_nop 0\ns_cbranch_vccnz .S442\n"
".S443:\nv_and_b32 v27, 0xf0, v1\nbuffer_load_dword v27, v27, s[16:19], s61 offen offset:4 sc0 sc1\ns_mov_b32 s90, 1\ns_swappc_b64 s[8:9], s[80:81]\nv_cmp_ne_u32 vcc, s15, v27\n"
"s_nop 0\ns_cbranch_vccnz .S445\n.S446:\ns_mov_b32 s90, 100\ns_swappc_b64 s[8:9], s[80:81]\n.A460:\nRa 176,26,8192\nEX 82,58\nRa 180,26,9216\nWL 6\nEX 83,59\nCvav 36,140,220,8\n"
"Ra 184,26,10240\nAD 84,84,82\nMvav 36,136,212\nD1 1,16,46,3072\ns_add_u32 m0, s89, 32768\nMvav 36,132,204\nRa 188,26,11264\nMvav 36,128,196\nAD 84,84,83\nCV 75,82,83\n"
"Mvav 36,144,228\nRa 128,2,16384\nMvav 36,148,236\ns_add_u32 s46, s86, s88\nEX 80,60\nMvav 36,152,244\nRa 132,2,17408\nMvav 36,156,252\nEX 81,61\nRa 136,2,18432\nAD 85,85,80\n"
"D1 1,16,46,0\nRa 140,2,19456\nSv 112,1,16,49,0\nAD 85,85,81\nCV 78,80,81\nRa 144,2,20480\ns_mov_b32 s87, s95\nEX 82,62\nRa 148,2,21504\nWL 6\nEX 83,63\nCvav 44,172,220,16\n"
"Ra 152,2,22528\nAD 85,85,82\nMvav 44,168,212\nD1 1,16,46,1024\nMvav 44,164,204\nRa 156,2,23552\nMvav 44,160,196\nAD 85,85,83\nCV 79,82,83\nMvav 44,176,228\nRa 160,2,24576\n"
"v_max_f32 v90, v84, v85\nMvav 44,180,236\ns_add_u32 s88, s88, 32768\nMvav 44,184,244\nRa 164,2,25600\nv_cmp_lt_f32 vcc, s63, v90\nMvav 44,188,252\nWL 8\nRa 168,2,26624\n"
"s_cbranch_vccnz .T462\n.T463:\nD1 1,16,46,2048\nRa 172,2,27648\nAD 86,86,84\nMaav 0,128,72\nRa 176,2,28672\nMaav 4,128,76\nMaav 12,132,76\nRa 180,2,29696\nMaav 8,132,72\n"
"s_waitcnt vmcnt(13) lgkmcnt(8)\nMaav 16,136,72\nRa 184,2,30720\nMaav 20,136,76\nD1 1,16,46,3072\ns_add_u32 m0, s89, 49152\nMaav 28,140,76\nRa 188,2,31744\ns_barrier\n"
"Maav 24,140,72\nMaav 32,144,72\nRa 128,24,32768\nMaav 36,144,76\ns_add_u32 s46, s87, s88\nEX 80,36\nMaav 44,148,76\nRa 132,24,33792\nMaav 40,148,72\nWL 8\nEX 81,37\n"
"Maav 48,152,72\nRa 136,24,34816\nAD 87,87,85\nMaav 52,152,76\nD1 1,16,46,0\nMaav 60,156,76\nRa 140,24,35840\nMaav 56,156,72\nAD 85,80,81\nCV 68,80,81\nMaav 64,160,72\n"
"Ra 144,26,32768\nMaav 68,160,76\ns_nop 0\nEX 82,38\nMaav 76,164,76\nRa 148,26,33792\nv_cmpx_gt_i32 vcc, 19, v0\nv_mov_b32 v19, s97\nv_cmpx_gt_i32 vcc, 18, v0\nv_mov_b32 v18, s97\n"
"v_cmpx_gt_i32 vcc, 17, v0\nv_mov_b32 v17, s97\nv_cmpx_gt_i32 vcc, 16, v0\nv_mov_b32 v16, s97\nv_cmpx_gt_i32 vcc, 3, v0\nv_mov_b32 v11, s97\nv_cmpx_gt_i32 vcc, 2, v0\n"
"v_mov_b32 v10, s97\nv_cmpx_gt_i32 vcc, 1, v0\nv_mov_b32 v9, s97\nv_cmpx_gt_i32 vcc, 0, v0\nv_mov_b32 v8, s97\ns_mov_b64 exec, -1\ns_nop 2\ns_nop 0\nMaav 72,164,72\nWL 8\nEX 83,39\n"
"Maav 80,168,72\nRa 152,26,34816\nAD 85,85,82\nMaav 84,168,76\nD1 1,16,46,1024\nMaav 92,172,76\nRa 156,26,35840\nMaav 88,172,72\nAD 85,85,83\nCV 69,82,83\nMaav 96,176,72\n.W593:\n"
"s_cbranch_execz .W597\nRa 160,24,40960\nMaav 100,176,76\ns_xor_b32 s89, s89, 0x10000\nMaav 108,180,76\nRa 164,24,41984\nMaav 104,180,72\nWL 10\nMaav 112,184,72\nRa 168,24,43008\n"
"Maav 116,184,76\nD1 1,16,46,2048\nMaav 124,188,76\nRa 172,24,44032\nMaav 120,188,72\nRa 176,26,40960\nRa 180,26,41984\nWL 6\nCvav 52,140,220,8\nRa 184,26,43008\nMvav 52,136,212\n"
"D1 1,16,46,3072\ns_add_u32 m0, s89, 0\nMvav 52,132,204\nRa 188,26,44032\nMvav 52,128,196\nMvav 52,144,228\nRa 128,2,49152\nMvav 52,148,236\ns_add_u32 s46, s86, s88\nEX 80,44\n"
"Mvav 52,152,244\nRa 132,2,50176\nv_xor_b32 v24, 0x10000, v24\nMvav 52,156,252\nEX 81,45\nRa 136,2,51200\nAD 85,85,80\nD1 1,16,46,0\nRa 140,2,52224\nSv 112,1,16,49,1024\n"
"AD 85,85,81\nCV 70,80,81\nRa 144,2,53248\ns_add_u32 s49, s49, 131072\nEX 82,46\nRa 148,2,54272\nv_xor_b32 v26, 0x10000, v26\nWL 6\nEX 83,47\nCvav 60,172,220,16\nRa 152,2,55296\n"
"AD 85,85,82\nMvav 60,168,212\nD1 1,16,46,1024\nMvav 60,164,204\nRa 156,2,56320\nMvav 60,160,196\nAD 85,85,83\nCV 71,82,83\nMvav 60,176,228\nRa 160,2,57344\nMvav 60,180,236\n"
"s_add_u32 s88, s88, 32768\nMvav 60,184,244\nRa 164,2,58368\nv_cmp_lt_f32 vcc, s63, v85\nMvav 60,188,252\nWL 8\nRa 168,2,59392\ns_cbranch_vccnz .T464\n.T465:\nD1 1,16,46,2048\n"
"Ra 172,2,60416\nRa 176,2,61440\nMaav 4,128,68\nMaav 12,132,68\nRa 180,2,62464\ns_waitcnt vmcnt(13) lgkmcnt(8)\nRa 184,2,63488\nMaav 20,136,68\nD1 1,16,46,3072\n"
"s_add_u32 m0, s89, 16384\nMaav 28,140,68\nRa 188,2,64512\ns_barrier\nRa 128,24,0\nMaav 36,144,68\ns_add_u32 s46, s87, s88\nEX 80,52\nMaav 44,148,68\nRa 132,24,1024\n"
"v_xor_b32 v2, 0x10000, v2\nWL 8\nEX 81,53\nRa 136,24,2048\nAD 87,87,85\nMaav 52,152,68\nD1 1,16,46,0\nMaav 60,156,68\nRa 140,24,3072\nAD 85,80,81\nCV 76,80,81\nRa 144,26,0\n"
"Maav 68,160,68\nEX 82,54\nMaav 76,164,68\nRa 148,26,1024\nWL 8\nEX 83,55\nRa 152,26,2048\nAD 85,85,82\nMaav 84,168,68\nD1 1,16,46,1024\nMaav 92,172,68\nRa 156,26,3072\n"
"AD 85,85,83\nCV 77,82,83\nRa 160,24,8192\nMaav 100,176,68\ns_sub_u32 s90, s90, 1\nMaav 108,180,68\nRa 164,24,9216\nWL 6\nRa 168,24,10240\nMaav 116,184,68\nD1 1,16,46,2048\n"
"Maav 124,188,68\nRa 172,24,11264\n.SW417:\nv_mov_b32 v4, 0\nv_mov_b32 v5, 0\nv_mov_b32 v6, 0\nv_mov_b32 v7, 0\nv_mov_b32 v8, 0\nv_mov_b32 v9, 0\nv_mov_b32 v10, 0\n"
"v_mov_b32 v11, 0\nv_mov_b32 v12, 0\nv_mov_b32 v13, 0\nv_mov_b32 v14, 0\nv_mov_b32 v15, 0\nv_mov_b32 v16, 0\nv_mov_b32 v17, 0\nv_mov_b32 v18, 0\nv_mov_b32 v19, 0\n"
"s_add_u32 s99, s98, 1\ns_mov_b32 s94, s96\ns_add_u32 s95, s96, 65536\nCvaa 32,128,192,4\nRa 176,26,8192\nMvaa 32,132,200\nMvaa 32,136,208\nRa 180,26,9216\nMvaa 32,140,216\nWL 6\n"
"Cvaa 36,140,220,8\nRa 184,26,10240\nMvaa 36,136,212\nD1 1,16,46,3072\ns_add_u32 m0, s89, 32768\nMvaa 36,132,204\nRa 188,26,11264\nMvaa 36,128,196\nMvaa 36,144,228\nRa 128,2,16384\n"
"Mvaa 36,148,236\ns_add_u32 s46, s86, s88\nEX 80,60\nMvaa 36,152,244\nRa 132,2,17408\nMvaa 36,156,252\nWL 6\nEX 81,61\nMvaa 32,156,248\nRa 136,2,18432\nAD 85,85,80\n"
"Mvaa 32,152,240\nD1 1,16,46,0\nMvaa 32,148,232\nRa 140,2,19456\nMvaa 32,144,224\nAD 85,85,81\nCV 78,80,81\nCvaa 40,160,192,12\nRa 144,2,20480\nMvaa 40,164,200\nEX 82,62\n"
"Mvaa 40,168,208\nRa 148,2,21504\nMvaa 40,172,216\nWL 6\nEX 83,63\nCvaa 44,172,220,16\nRa 152,2,22528\nAD 85,85,82\nMvaa 44,168,212\nD1 1,16,46,1024\nMvaa 44,164,204\n"
"Ra 156,2,23552\nMvaa 44,160,196\nAD 85,85,83\nCV 79,82,83\nMvaa 44,176,228\nRa 160,2,24576\nMvaa 44,180,236\ns_add_u32 s88, s88, 32768\nEX 80,32\nMvaa 44,184,244\nRa 164,2,25600\n"
"v_cmp_lt_f32 vcc, s63, v85\nMvaa 44,188,252\nWL 8\nEX 81,33\nMvaa 40,188,248\nRa 168,2,26624\ns_cbranch_vccnz .T468\n.T469:\nMvaa 40,184,240\nD1 1,16,46,2048\nMvaa 40,180,232\n"
"Ra 172,2,27648\nMvaa 40,176,224\nAD 84,80,81\nCV 64,80,81\nRa 176,2,28672\nMaav 4,128,76\nEX 82,34\nMaav 12,132,76\nRa 180,2,29696\ns_waitcnt vmcnt(12) lgkmcnt(8)\nEX 83,35\n"
"Ra 184,2,30720\nAD 84,84,82\nMaav 20,136,76\nD1 1,16,46,3072\ns_add_u32 m0, s89, 49152\nMaav 28,140,76\nRa 188,2,31744\ns_barrier\nAR 98,0\nAR 99,1\nCV 100,98,99\nAD 84,84,83\n"
"CV 65,82,83\nAR 98,2\nAR 99,3\nCV 101,98,99\nRa 128,24,32768\nAR 98,8\nAR 99,9\nCV 102,98,99\nAR 98,10\nMaav 36,144,76\ns_add_u32 s46, s87, s88\nEX 80,36\nAR 99,11\n"
"Maav 44,148,76\nRa 132,24,33792\nCV 103,98,99\ns_add_u32 s5, s66, 0\nSv 100,1,16,5,0\nAR 98,4\nWL 8\nEX 81,37\nAR 99,5\nCV 104,98,99\nRa 136,24,34816\nAD 87,87,85\nAR 98,6\n"
"AR 99,7\nCV 105,98,99\nMaav 52,152,76\nD1 1,16,46,0\nv_mov_b32 v96, v86\nAR 98,12\nAR 99,13\nMaav 60,156,76\nRa 140,24,35840\nv_mov_b32 v97, v87\nCV 106,98,99\nAR 98,14\nAR 99,15\n"
"AD 85,80,81\nCV 68,80,81\nCV 107,98,99\ns_add_u32 s5, s66, 0\nSv 104,1,16,5,1024\nRa 144,26,32768\nv_permlane32_swap_b32 v86, v96\nv_permlane32_swap_b32 v87, v97\nAR 98,16\n"
"AR 99,17\nMaav 68,160,76\nEX 82,38\nCV 108,98,99\nAR 98,18\nMaav 76,164,76\nRa 148,26,33792\nAD 86,86,96\nAR 99,19\nCV 109,98,99\nAR 98,24\nWL 8\nEX 83,39\nAR 99,25\nCV 110,98,99\n"
"Ra 152,26,34816\nAD 85,85,82\nAD 87,87,97\nAR 98,26\nAR 99,27\nMaav 84,168,76\nD1 1,16,46,1024\nv_mov_b32 v96, v86\nCV 111,98,99\nMaav 92,172,76\nRa 156,26,35840\n"
"v_mov_b32 v97, v87\ns_add_u32 s5, s66, 0\nSv 108,1,16,5,2048\nAR 98,20\nAD 85,85,83\nCV 69,82,83\nAR 99,21\nCV 100,98,99\nAR 98,22\nRa 160,24,40960\n"
"v_permlane16_swap_b32 v86, v96\nv_permlane16_swap_b32 v87, v97\nAR 99,23\nCV 101,98,99\nMaav 100,176,76\ns_xor_b32 s89, s89, 0x10000\nEX 80,40\nAR 98,28\nMaav 108,180,76\n"
"Ra 164,24,41984\nAD 86,86,96\nAR 99,29\nCV 102,98,99\nAR 98,30\nWL 6\nEX 81,41\nAR 99,31\nCV 103,98,99\nRa 168,24,43008\nAD 84,84,80\nAD 87,87,97\ns_add_u32 s5, s66, 0\n"
"Sv 100,1,16,5,3072\nMaav 116,184,76\nD1 1,16,46,2048\nv_cmp_gt_u32 vcc, 0x100, v1\nv_mov_b32 v94, s15\nv_mov_b32 v95, s15\nv_cndmask_b32 v92, v89, v88, vcc\n"
"v_cndmask_b32 v93, v87, v86, vcc\nMaav 124,188,76\nRa 172,24,44032\nv_mov_b32 v86, 0\nAR 98,32\nAR 99,33\nCV 104,98,99\nAD 84,84,81\nCV 66,80,81\nAR 98,34\nAR 99,35\nCV 105,98,99\n"
"Cvaa 48,128,192,4\nRa 176,26,40960\nv_mov_b32 v87, 0\nv_mov_b32 v88, 0\nAR 98,40\nAR 99,41\nMvaa 48,132,200\nEX 82,42\nCV 106,98,99\nAR 98,42\nMvaa 48,136,208\nRa 180,26,41984\n"
"v_mov_b32 v89, 0\nAR 99,43\nCV 107,98,99\nMvaa 48,140,216\nWL 6\nEX 83,43\ns_add_u32 s5, s66, 4096\nSv 104,1,16,5,0\nCvaa 52,140,220,8\nRa 184,26,43008\nAD 84,84,82\nAR 98,36\n"
"AR 99,37\nCV 108,98,99\nMvaa 52,136,212\nD1 1,16,46,3072\ns_add_u32 m0, s89, 0\nAR 98,38\nAR 99,39\nMvaa 52,132,204\nRa 188,26,44032\nCV 109,98,99\nAR 98,44\nAR 99,45\n"
"CV 110,98,99\nMvaa 52,128,196\nAD 84,84,83\nCV 67,82,83\nAR 98,46\nAR 99,47\nCV 111,98,99\nMvaa 52,144,228\nRa 128,2,49152\ns_add_u32 s5, s66, 4096\nSv 108,1,16,5,1024\nAR 98,48\n"
"AR 99,49\nMvaa 52,148,236\ns_add_u32 s46, s86, s88\nEX 80,44\nCV 100,98,99\nMvaa 52,152,244\nRa 132,2,50176\nv_xor_b32 v24, 0x10000, v24\nAR 98,50\nAR 99,51\nCV 101,98,99\n"
"Mvaa 52,156,252\nWL 6\nEX 81,45\nAR 98,56\nAR 99,57\nMvaa 48,156,248\nRa 136,2,51200\nAD 85,85,80\nCV 102,98,99\nAR 98,58\nAR 99,59\nMvaa 48,152,240\nD1 1,16,46,0\nCV 103,98,99\n"
"s_add_u32 s5, s66, 4096\nSv 100,1,16,5,2048\nMvaa 48,148,232\nRa 140,2,52224\nAR 98,52\nAR 99,53\nCV 104,98,99\nAR 98,54\nMvaa 48,144,224\nAD 85,85,81\nCV 70,80,81\nAR 99,55\n"
"CV 105,98,99\nAR 98,60\nCvaa 56,160,192,12\nRa 144,2,53248\nAR 99,61\nCV 106,98,99\nAR 98,62\nAR 99,63\nMvaa 56,164,200\nEX 82,46\nCV 107,98,99\nMvaa 56,168,208\nRa 148,2,54272\n"
"v_xor_b32 v26, 0x10000, v26\ns_add_u32 s5, s66, 4096\nSv 104,1,16,5,3072\nAR 98,64\nMvaa 56,172,216\nWL 6\nEX 83,47\nAR 99,65\nCV 108,98,99\nCvaa 60,172,220,16\nRa 152,2,55296\n"
"AD 85,85,82\nAR 98,66\nAR 99,67\nCV 109,98,99\nMvaa 60,168,212\nD1 1,16,46,1024\nAR 98,72\nAR 99,73\nCV 110,98,99\nMvaa 60,164,204\nRa 156,2,56320\nAR 98,74\nAR 99,75\n"
"CV 111,98,99\nMvaa 60,160,196\nAD 85,85,83\nCV 71,82,83\ns_add_u32 s5, s66, 16384\nSv 108,1,16,5,0\nAR 98,68\nMvaa 60,176,228\nRa 160,2,57344\nv_max_f32 v90, v84, v85\nAR 99,69\n"
"CV 100,98,99\nAR 98,70\nMvaa 60,180,236\ns_add_u32 s88, s88, 32768\nEX 80,48\nAR 99,71\nMvaa 60,184,244\nRa 164,2,58368\nv_cmp_lt_f32 vcc, s63, v90\nMvaa 60,188,252\nWL 8\n"
"EX 81,49\nMvaa 56,188,248\nRa 168,2,59392\ns_cbranch_vccnz .T470\n.T471:\nv_mov_b32 v20, v84\nv_mov_b32 v21, v85\nv_mov_b32 v22, v20\nv_mov_b32 v23, v21\nMvaa 56,184,240\n"
"D1 1,16,46,2048\nv_permlane32_swap_b32 v20, v22\nv_permlane32_swap_b32 v21, v23\nAD 20,20,22\nAD 21,21,23\nv_mov_b32 v22, v20\nv_mov_b32 v23, v21\nMvaa 56,180,232\nRa 172,2,60416\n"
"v_permlane16_swap_b32 v20, v22\nv_permlane16_swap_b32 v21, v23\nAD 20,20,22\nAD 21,21,23\nv_min_f32 v90, v20, v21\nv_cmp_gt_f32 vcc, s64, v90\ns_nop 0\ns_cbranch_vccnz .T472\n"
".T473:\nAD 86,86,84\nMvaa 56,176,224\nAD 84,80,81\nCV 72,80,81\nCV 101,98,99\nAR 98,76\nAR 99,77\nZvav 128,128,64\nRa 176,2,61440\nCV 102,98,99\nAR 98,78\nAR 99,79\nCV 103,98,99\n"
"Zvav 132,128,68\nEX 82,50\ns_add_u32 s5, s66, 16384\nSv 100,1,16,5,1024\nZvav 140,132,68\nRa 180,2,62464\nAR 98,80\nAR 99,81\nCV 104,98,99\nAR 98,82\nZvav 136,132,64\n"
"s_waitcnt vmcnt(21) lgkmcnt(8)\nEX 83,51\nAR 99,83\nCV 105,98,99\nZvav 144,136,64\nRa 184,2,63488\nAD 84,84,82\nAR 98,88\nAR 99,89\nCV 106,98,99\nZvav 148,136,68\nD1 1,16,46,3072\n"
"s_add_u32 m0, s89, 16384\nAR 98,90\nAR 99,91\nZvav 156,140,68\nRa 188,2,64512\ns_barrier\nCV 107,98,99\ns_add_u32 s5, s66, 16384\nSv 104,1,16,5,2048\nZvav 152,140,64\nAD 84,84,83\n"
"CV 73,82,83\nAR 98,84\nAR 99,85\nCV 108,98,99\nZvav 160,144,64\nRa 128,24,0\nAR 98,86\nAR 99,87\nCV 109,98,99\nAR 98,92\nZvav 164,144,68\n.W594:\ns_cbranch_execz .W598\n"
"s_add_u32 s46, s87, s88\nEX 80,52\nAR 99,93\nZvav 172,148,68\nRa 132,24,1024\nv_xor_b32 v2, 0x10000, v2\nCV 110,98,99\nAR 98,94\nAR 99,95\nZvav 168,148,64\nWL 8\nEX 81,53\n"
"CV 111,98,99\nZvav 176,152,64\nRa 136,24,2048\nAD 87,87,85\ns_add_u32 s5, s66, 16384\nSv 108,1,16,5,3072\nAR 98,96\nZvav 180,152,68\nD1 1,16,46,0\nAR 99,97\nCV 100,98,99\n"
"AR 98,98\nZvav 188,156,68\nRa 140,24,3072\nAR 99,99\nCV 101,98,99\nAR 98,104\nAR 99,105\nZvav 184,156,64\nAD 85,80,81\nCV 76,80,81\nCV 102,98,99\nAR 98,106\nAR 99,107\n"
"Zvav 192,160,64\nRa 144,26,0\nCV 103,98,99\ns_add_u32 s5, s66, 20480\nSv 100,1,16,5,0\nAR 98,100\nZvav 196,160,68\nEX 82,54\nAR 99,101\nCV 104,98,99\nZvav 204,164,68\n"
"Ra 148,26,1024\nAR 98,102\nAR 99,103\nCV 105,98,99\nAR 98,108\nZvav 200,164,64\nWL 8\nEX 83,55\nAR 99,109\nCV 106,98,99\nZvav 208,168,64\nRa 152,26,2048\nAD 85,85,82\nAR 98,110\n"
"AR 99,111\nCV 107,98,99\nZvav 212,168,68\nD1 1,16,46,1024\ns_add_u32 s5, s66, 20480\nSv 104,1,16,5,1024\nAR 98,112\nZvav 220,172,68\nRa 156,26,3072\nAR 99,113\nCV 108,98,99\n"
"AR 98,114\nAR 99,115\nZvav 216,172,64\nAD 85,85,83\nCV 77,82,83\nCV 109,98,99\nAR 98,120\nAR 99,121\nZvav 224,176,64\nRa 160,24,8192\nCV 110,98,99\nAR 98,122\nAR 99,123\n"
"CV 111,98,99\nZvav 228,176,68\ns_sub_u32 s90, s90, 1\nEX 80,56\nZvav 236,180,68\nRa 164,24,9216\ns_cbranch_scc0 .E466\ns_swappc_b64 s[80:81], s[8:9]\n.E466:\n"
"s_add_u32 s5, s66, 20480\nSv 108,1,16,5,2048\nZvav 232,180,64\nWL 6\nEX 81,57\nAR 98,116\nAR 99,117\nZvav 240,184,64\nRa 168,24,10240\nAD 84,84,80\nCV 100,98,99\nAR 98,118\n"
"AR 99,119\nZvav 244,184,68\nD1 1,16,46,2048\nCV 101,98,99\nAR 98,124\nAR 99,125\nZvav 252,188,68\nRa 172,24,11264\nCV 102,98,99\nAR 98,126\nAR 99,127\nCV 103,98,99\n"
"Zvav 248,188,64\nAD 84,84,81\nCV 74,80,81\ns_add_u32 s5, s66, 20480\nSv 100,1,16,5,3072\ns_sub_u32 s99, s99, 1\n.p2align 6\n.A474:\nCvaa 32,128,192,4\nRa 176,26,8192\n"
"Mvaa 32,132,200\nEX 82,58\nMvaa 32,136,208\nRa 180,26,9216\nMvaa 32,140,216\nWL 6\nEX 83,59\nCvaa 36,140,220,8\nRa 184,26,10240\nAD 84,84,82\nMvaa 36,136,212\nD1 1,16,46,3072\n"
"s_add_u32 m0, s89, 32768\nMvaa 36,132,204\nRa 188,26,11264\nMvaa 36,128,196\nAD 84,84,83\nCV 75,82,83\nMvaa 36,144,228\nRa 128,2,16384\nMvaa 36,148,236\ns_add_u32 s46, s86, s88\n"
"EX 80,60\nMvaa 36,152,244\nRa 132,2,17408\nMvaa 36,156,252\nWL 6\nEX 81,61\nMvaa 32,156,248\nRa 136,2,18432\nAD 85,85,80\nMvaa 32,152,240\nD1 1,16,46,0\nMvaa 32,148,232\n"
"Ra 140,2,19456\nSv 112,1,16,49,0\nMvaa 32,144,224\nAD 85,85,81\nCV 78,80,81\nCvaa 40,160,192,12\nRa 144,2,20480\nMvaa 40,164,200\nEX 82,62\nMvaa 40,168,208\nRa 148,2,21504\n"
"Mvaa 40,172,216\nWL 6\nEX 83,63\nCvaa 44,172,220,16\nRa 152,2,22528\nAD 85,85,82\nMvaa 44,168,212\nD1 1,16,46,1024\nMvaa 44,164,204\nRa 156,2,23552\nMvaa 44,160,196\nAD 85,85,83\n"
"CV 79,82,83\nMvaa 44,176,228\nRa 160,2,24576\nv_max_f32 v90, v84, v85\nMvaa 44,180,236\ns_add_u32 s88, s88, 32768\nEX 80,32\nMvaa 44,184,244\nRa 164,2,25600\n"
"v_cmp_lt_f32 vcc, s63, v90\nMvaa 44,188,252\nWL 8\nEX 81,33\nMvaa 40,188,248\nRa 168,2,26624\ns_cbranch_vccnz .T484\n.T485:\nMvaa 40,184,240\nD1 1,16,46,2048\nMvaa 40,180,232\n"
"Ra 172,2,27648\nAD 86,86,84\nMvaa 40,176,224\nAD 84,80,81\nCV 64,80,81\nMvav 128,128,72\nRa 176,2,28672\nMvav 132,128,76\ns_cmp_eq_u32 s99, s98\nEX 82,34\nMvav 140,132,76\n"
"Ra 180,2,29696\ns_cbranch_scc1 .E482\n.E483:\nMvav 136,132,72\ns_waitcnt vmcnt(13) lgkmcnt(8)\nEX 83,35\nMvav 144,136,72\nRa 184,2,30720\nAD 84,84,82\nMvav 148,136,76\n"
"D1 1,16,46,3072\ns_add_u32 m0, s89, 49152\nMvav 156,140,76\nRa 188,2,31744\ns_barrier\nMvav 152,140,72\nAD 84,84,83\nCV 65,82,83\nMvav 160,144,72\nRa 128,24,32768\n"
"Mvav 164,144,76\ns_add_u32 s46, s87, s88\nEX 80,36\nMvav 172,148,76\nRa 132,24,33792\nMvav 168,148,72\nWL 8\nEX 81,37\nMvav 176,152,72\nRa 136,24,34816\nAD 87,87,85\n"
"Mvav 180,152,76\nD1 1,16,46,0\nMvav 188,156,76\nRa 140,24,35840\nMvav 184,156,72\nAD 85,80,81\nCV 68,80,81\nMvav 192,160,72\nRa 144,26,32768\nMvav 196,160,76\ns_cmp_eq_u32 s99, 0\n"
"EX 82,38\nMvav 204,164,76\nRa 148,26,33792\ns_cbranch_scc1 .E476\n.E477:\nMvav 200,164,72\nWL 8\nEX 83,39\nMvav 208,168,72\nRa 152,26,34816\nAD 85,85,82\nMvav 212,168,76\n"
"D1 1,16,46,1024\nMvav 220,172,76\nRa 156,26,35840\nMvav 216,172,72\nAD 85,85,83\nCV 69,82,83\nMvav 224,176,72\nRa 160,24,40960\nMvav 228,176,76\ns_xor_b32 s89, s89, 0x10000\n"
"EX 80,40\nMvav 236,180,76\nRa 164,24,41984\nMvav 232,180,72\nWL 6\nEX 81,41\nMvav 240,184,72\nRa 168,24,43008\nAD 84,84,80\nMvav 244,184,76\nD1 1,16,46,2048\nMvav 252,188,76\n"
"Ra 172,24,44032\nMvav 248,188,72\nAD 84,84,81\nCV 66,80,81\nCvaa 48,128,192,4\nRa 176,26,40960\nMvaa 48,132,200\ns_cmp_eq_u32 s99, 0\nEX 82,42\nMvaa 48,136,208\nRa 180,26,41984\n"
"s_cselect_b32 s86, s94, s86\nMvaa 48,140,216\nWL 6\nEX 83,43\nCvaa 52,140,220,8\nRa 184,26,43008\nAD 84,84,82\nMvaa 52,136,212\nD1 1,16,46,3072\ns_add_u32 m0, s89, 0\n"
"Mvaa 52,132,204\nRa 188,26,44032\nMvaa 52,128,196\nAD 84,84,83\nCV 67,82,83\nMvaa 52,144,228\nRa 128,2,49152\nMvaa 52,148,236\ns_add_u32 s46, s86, s88\nEX 80,44\nMvaa 52,152,244\n"
"Ra 132,2,50176\nv_xor_b32 v24, 0x10000, v24\nMvaa 52,156,252\nWL 6\nEX 81,45\nMvaa 48,156,248\nRa 136,2,51200\nAD 85,85,80\nMvaa 48,152,240\nD1 1,16,46,0\nMvaa 48,148,232\n"
"Ra 140,2,52224\nSv 112,1,16,49,1024\nMvaa 48,144,224\nAD 85,85,81\nCV 70,80,81\nCvaa 56,160,192,12\nRa 144,2,53248\nMvaa 56,164,200\ns_add_u32 s49, s49, 131072\nEX 82,46\n"
"Mvaa 56,168,208\nRa 148,2,54272\nv_xor_b32 v26, 0x10000, v26\nMvaa 56,172,216\nWL 6\nEX 83,47\nCvaa 60,172,220,16\nRa 152,2,55296\nAD 85,85,82\nMvaa 60,168,212\nD1 1,16,46,1024\n"
"Mvaa 60,164,204\nRa 156,2,56320\nMvaa 60,160,196\nAD 85,85,83\nCV 71,82,83\nMvaa 60,176,228\nRa 160,2,57344\nv_max_f32 v90, v84, v85\nMvaa 60,180,236\ns_add_u32 s88, s88, 32768\n"
"EX 80,48\nMvaa 60,184,244\nRa 164,2,58368\nv_cmp_lt_f32 vcc, s63, v90\nMvaa 60,188,252\nWL 8\nEX 81,49\nMvaa 56,188,248\nRa 168,2,59392\ns_cbranch_vccnz .T486\n.T487:\n"
"Mvaa 56,184,240\nD1 1,16,46,2048\nMvaa 56,180,232\nRa 172,2,60416\nAD 86,86,84\nMvaa 56,176,224\nAD 84,80,81\nCV 72,80,81\nMvav 128,128,64\nRa 176,2,61440\nMvav 132,128,68\n"
"EX 82,50\nMvav 140,132,68\nRa 180,2,62464\nMvav 136,132,64\ns_waitcnt vmcnt(13) lgkmcnt(8)\nEX 83,51\nMvav 144,136,64\nRa 184,2,63488\nAD 84,84,82\nMvav 148,136,68\n"
"D1 1,16,46,3072\ns_add_u32 m0, s89, 16384\nMvav 156,140,68\nRa 188,2,64512\ns_barrier\nMvav 152,140,64\nAD 84,84,83\nCV 73,82,83\nMvav 160,144,64\nRa 128,24,0\nMvav 164,144,68\n"
"s_add_u32 s46, s87, s88\nEX 80,52\nMvav 172,148,68\nRa 132,24,1024\nv_xor_b32 v2, 0x10000, v2\nMvav 168,148,64\nWL 8\nEX 81,53\nMvav 176,152,64\nRa 136,24,2048\nAD 87,87,85\n"
"Mvav 180,152,68\nD1 1,16,46,0\nMvav 188,156,68\nRa 140,24,3072\nMvav 184,156,64\nAD 85,80,81\nCV 76,80,81\nMvav 192,160,64\nRa 144,26,0\nMvav 196,160,68\ns_cmp_lt_u32 s99, s92\n"
"EX 82,54\nMvav 204,164,68\nRa 148,26,1024\ns_cbranch_scc1 .E478\n.E479:\nMvav 200,164,64\nWL 8\nEX 83,55\nMvav 208,168,64\nRa 152,26,2048\nAD 85,85,82\nMvav 212,168,68\n"
"D1 1,16,46,1024\nMvav 220,172,68\nRa 156,26,3072\nMvav 216,172,64\nAD 85,85,83\nCV 77,82,83\nMvav 224,176,64\nRa 160,24,8192\nMvav 228,176,68\ns_sub_u32 s90, s90, 1\nEX 80,56\n"
"Mvav 236,180,68\nRa 164,24,9216\ns_cbranch_scc1 .E480\n.E481:\nMvav 232,180,64\nWL 6\nEX 81,57\nMvav 240,184,64\nRa 168,24,10240\nAD 84,84,80\nMvav 244,184,68\nD1 1,16,46,2048\n"
"s_sub_u32 s99, s99, 1\nMvav 252,188,68\nRa 172,24,11264\nMvav 248,188,64\nAD 84,84,81\nCV 74,80,81\ns_cbranch_scc0 .A474\ns_branch .A488\n.E476:\nv_cmpx_gt_i32 vcc, 19, v0\n"
"v_mov_b32 v15, s97\nv_cmpx_gt_i32 vcc, 18, v0\nv_mov_b32 v14, s97\nv_cmpx_gt_i32 vcc, 17, v0\nv_mov_b32 v13, s97\nv_cmpx_gt_i32 vcc, 16, v0\nv_mov_b32 v12, s97\n"
"v_cmpx_gt_i32 vcc, 3, v0\nv_mov_b32 v7, s97\nv_cmpx_gt_i32 vcc, 2, v0\nv_mov_b32 v6, s97\nv_cmpx_gt_i32 vcc, 1, v0\nv_mov_b32 v5, s97\nv_cmpx_gt_i32 vcc, 0, v0\nv_mov_b32 v4, s97\n"
"s_mov_b64 exec, -1\ns_nop 2\ns_branch .E477\n.E478:\ns_cmp_eq_u32 s99, 0\ns_cbranch_scc1 .K475\nv_cmpx_gt_i32 vcc, 19, v3\nv_mov_b32 v15, s97\nv_cmpx_gt_i32 vcc, 18, v3\n"
"v_mov_b32 v14, s97\nv_cmpx_gt_i32 vcc, 17, v3\nv_mov_b32 v13, s97\nv_cmpx_gt_i32 vcc, 16, v3\nv_mov_b32 v12, s97\nv_cmpx_gt_i32 vcc, 3, v3\nv_mov_b32 v7, s97\n"
"v_cmpx_gt_i32 vcc, 2, v3\nv_mov_b32 v6, s97\nv_cmpx_gt_i32 vcc, 1, v3\nv_mov_b32 v5, s97\nv_cmpx_gt_i32 vcc, 0, v3\nv_mov_b32 v4, s97\ns_mov_b64 exec, -1\ns_nop 2\ns_branch .E479\n"
".K475:\nv_cmpx_gt_i32 vcc, 19, v3\nv_mov_b32 v19, s97\nv_cmpx_gt_i32 vcc, 18, v3\nv_mov_b32 v18, s97\nv_cmpx_gt_i32 vcc, 17, v3\nv_mov_b32 v17, s97\nv_cmpx_gt_i32 vcc, 16, v3\n"
"v_mov_b32 v16, s97\nv_cmpx_gt_i32 vcc, 3, v3\nv_mov_b32 v11, s97\nv_cmpx_gt_i32 vcc, 2, v3\nv_mov_b32 v10, s97\nv_cmpx_gt_i32 vcc, 1, v3\nv_mov_b32 v9, s97\n"
"v_cmpx_gt_i32 vcc, 0, v3\nv_mov_b32 v8, s97\ns_mov_b64 exec, -1\ns_nop 2\ns_branch .E479\n.E480:\ns_swappc_b64 s[80:81], s[8:9]\ns_branch .E481\n.E482:\nv_mov_b32 v91, 0x40000000\n"
"v_cmp_gt_u32 vcc, 0x200, v1\nWV 5\ns_nop 0\nv_cndmask_b32 v91, v91, v1, vcc\nSv 92,91,16,65,0\ns_branch .E483\n.A488:\n.LE418:\nRa 176,26,8192\nEX 82,58\nRa 180,26,9216\nWL 6\n"
"EX 83,59\nCvaa 36,140,220,8\nRa 184,26,10240\nAD 84,84,82\nMvaa 36,136,212\nD1 1,16,46,3072\ns_add_u32 m0, s89, 32768\nMvaa 36,132,204\nRa 188,26,11264\nMvaa 36,128,196\n"
"AD 84,84,83\nCV 75,82,83\nMvaa 36,144,228\nRa 128,2,16384\nMvaa 36,148,236\ns_add_u32 s46, s86, s88\nEX 80,60\nMvaa 36,152,244\nRa 132,2,17408\nMvaa 36,156,252\nEX 81,61\n"
"Ra 136,2,18432\nAD 85,85,80\nD1 1,16,46,0\nRa 140,2,19456\nAD 85,85,81\nCV 78,80,81\nRa 144,2,20480\ns_mov_b32 s87, s95\nEX 82,62\nRa 148,2,21504\nWL 6\nEX 83,63\n"
"Cvaa 44,172,220,16\nRa 152,2,22528\nAD 85,85,82\nMvaa 44,168,212\nD1 1,16,46,1024\nMvaa 44,164,204\nRa 156,2,23552\nMvaa 44,160,196\nAD 85,85,83\nCV 79,82,83\nMvaa 44,176,228\n"
"Ra 160,2,24576\nv_max_f32 v90, v84, v85\nMvaa 44,180,236\ns_add_u32 s88, s88, 32768\nMvaa 44,184,244\nRa 164,2,25600\nv_cmp_lt_f32 vcc, s63, v90\nMvaa 44,188,252\nWL 8\n"
"Ra 168,2,26624\ns_cbranch_vccnz .T490\n.T491:\nD1 1,16,46,2048\nRa 172,2,27648\nAD 86,86,84\nMvav 128,128,72\nRa 176,2,28672\nMvav 132,128,76\nMvav 140,132,76\nRa 180,2,29696\n"
"Mvav 136,132,72\ns_waitcnt vmcnt(12) lgkmcnt(8)\nMvav 144,136,72\nRa 184,2,30720\nMvav 148,136,76\nD1 1,16,46,3072\ns_add_u32 m0, s89, 49152\nMvav 156,140,76\nRa 188,2,31744\n"
"s_barrier\nMvav 152,140,72\nMvav 160,144,72\nRa 128,24,32768\n.W595:\ns_cbranch_execz .WB416\nMvav 164,144,76\ns_add_u32 s46, s87, s88\nEX 80,36\nMvav 172,148,76\nRa 132,24,33792\n"
"Mvav 168,148,72\nWL 8\nEX 81,37\nMvav 176,152,72\nRa 136,24,34816\nAD 87,87,85\nMvav 180,152,76\nD1 1,16,46,0\nMvav 188,156,76\nRa 140,24,35840\nMvav 184,156,72\nAD 85,80,81\n"
"CV 68,80,81\nMvav 192,160,72\nRa 144,26,32768\nMvav 196,160,76\ns_nop 0\nEX 82,38\nMvav 204,164,76\nRa 148,26,33792\nv_cmpx_gt_i32 vcc, 19, v0\nv_mov_b32 v19, s97\n"
"v_cmpx_gt_i32 vcc, 18, v0\nv_mov_b32 v18, s97\nv_cmpx_gt_i32 vcc, 17, v0\nv_mov_b32 v17, s97\nv_cmpx_gt_i32 vcc, 16, v0\nv_mov_b32 v16, s97\nv_cmpx_gt_i32 vcc, 3, v0\n"
"v_mov_b32 v11, s97\nv_cmpx_gt_i32 vcc, 2, v0\nv_mov_b32 v10, s97\nv_cmpx_gt_i32 vcc, 1, v0\nv_mov_b32 v9, s97\nv_cmpx_gt_i32 vcc, 0, v0\nv_mov_b32 v8, s97\ns_mov_b64 exec, -1\n"
"s_nop 2\ns_nop 0\nMvav 200,164,72\nWL 8\nEX 83,39\nMvav 208,168,72\nRa 152,26,34816\nAD 85,85,82\nMvav 212,168,76\nD1 1,16,46,1024\nMvav 220,172,76\nRa 156,26,35840\n"
"Mvav 216,172,72\nAD 85,85,83\nCV 69,82,83\nMvav 224,176,72\nRa 160,24,40960\nMvav 228,176,76\ns_xor_b32 s89, s89, 0x10000\nMvav 236,180,76\nRa 164,24,41984\nMvav 232,180,72\n"
"WL 10\nMvav 240,184,72\nRa 168,24,43008\nMvav 244,184,76\nD1 1,16,46,2048\nMvav 252,188,76\nRa 172,24,44032\nMvav 248,188,72\nRa 176,26,40960\nRa 180,26,41984\nWL 6\n"
"Cvaa 52,140,220,8\nRa 184,26,43008\nMvaa 52,136,212\nD1 1,16,46,3072\ns_add_u32 m0, s89, 0\nMvaa 52,132,204\nRa 188,26,44032\nMvaa 52,128,196\nMvaa 52,144,228\nRa 128,2,49152\n"
"Mvaa 52,148,236\ns_add_u32 s46, s86, s88\nEX 80,44\nMvaa 52,152,244\nRa 132,2,50176\nv_xor_b32 v24, 0x10000, v24\nMvaa 52,156,252\nEX 81,45\nRa 136,2,51200\nAD 85,85,80\n"
"D1 1,16,46,0\nRa 140,2,52224\nAD 85,85,81\nCV 70,80,81\nRa 144,2,53248\nEX 82,46\nRa 148,2,54272\nv_xor_b32 v26, 0x10000, v26\nWL 6\nEX 83,47\nCvaa 60,172,220,16\nRa 152,2,55296\n"
"AD 85,85,82\nMvaa 60,168,212\nD1 1,16,46,1024\nMvaa 60,164,204\nRa 156,2,56320\nMvaa 60,160,196\nAD 85,85,83\nCV 71,82,83\nMvaa 60,176,228\nRa 160,2,57344\nMvaa 60,180,236\n"
"s_add_u32 s88, s88, 32768\nMvaa 60,184,244\nRa 164,2,58368\nv_cmp_lt_f32 vcc, s63, v85\nMvaa 60,188,252\nWL 8\nRa 168,2,59392\ns_cbranch_vccnz .T492\n.T493:\nD1 1,16,46,2048\n"
"Ra 172,2,60416\nRa 176,2,61440\nMvav 132,128,68\nMvav 140,132,68\nRa 180,2,62464\ns_waitcnt vmcnt(11) lgkmcnt(8)\nRa 184,2,63488\nMvav 148,136,68\nD1 1,16,46,3072\n"
"s_add_u32 m0, s89, 16384\nMvav 156,140,68\nRa 188,2,64512\ns_barrier\nRa 128,24,0\nMvav 164,144,68\ns_add_u32 s46, s87, s88\nEX 80,52\nMvav 172,148,68\nRa 132,24,1024\n"
"v_xor_b32 v2, 0x10000, v2\nWL 8\nEX 81,53\nRa 136,24,2048\nAD 87,87,85\nMvav 180,152,68\nD1 1,16,46,0\nMvav 188,156,68\nRa 140,24,3072\nAD 85,80,81\nCV 76,80,81\nRa 144,26,0\n"
"Mvav 196,160,68\nEX 82,54\nMvav 204,164,68\nRa 148,26,1024\nWL 8\nEX 83,55\nRa 152,26,2048\nAD 85,85,82\nMvav 212,168,68\nD1 1,16,46,1024\nMvav 220,172,68\nRa 156,26,3072\n"
"AD 85,85,83\nCV 77,82,83\nRa 160,24,8192\nMvav 228,176,68\nMvav 236,180,68\nRa 164,24,9216\nWL 10\nRa 168,24,10240\nMvav 244,184,68\nD1 1,16,46,2048\nMvav 252,188,68\n"
"Ra 172,24,11264\nD1 1,16,46,3072\ns_add_u32 m0, s89, 32768\ns_add_u32 s46, s86, s88\nRa 128,2,16384\nv_xor_b32 v3, 0x10000, v2\nv_add_u32 v0, 0x20000, v1\nEX 80,60\n"
"Ra 132,2,17408\ns_sub_u32 s94, s49, 1835008\nEX 81,61\nRa 136,2,18432\nAD 85,85,80\ns_xor_b32 s92, s94, 0x202000\nD1 1,16,46,0\ns_lshl_b32 s89, s11, 12\nRa 140,2,19456\n"
"s_lshl1_add_u32 s2, s12, 1\nAD 85,85,81\nCV 78,80,81\nRa 144,2,20480\ns_lshl3_add_u32 s2, s2, s13\nEX 82,62\nRa 148,2,21504\ns_lshl4_add_u32 s2, s2, 0xc048200\nEX 83,63\n"
"Ra 152,2,22528\nAD 85,85,82\nD1 1,16,46,1024\nRa 156,2,23552\ns_and_b32 s3, s72, 0x2000\nAD 85,85,83\nCV 79,82,83\nRa 160,2,24576\ns_cmp_eq_u32 s11, 1\ns_cselect_b32 s54, s2, s54\n"
"s_cselect_b32 s3, s3, 0\nv_cmp_lt_f32 vcc, s63, v85\nRa 164,2,25600\ns_cbranch_vccnz .T494\n.T495:\nAD 87,87,85\nWL 8\nv_mov_b32 v84, v86\nv_mov_b32 v85, v87\nRa 168,2,26624\n"
"v_permlane32_swap_b32 v86, v84\nv_permlane32_swap_b32 v87, v85\nD1 1,16,46,2048\nAD 86,86,84\nRa 172,2,27648\nAD 87,87,85\nv_mov_b32 v84, v86\nv_mov_b32 v85, v87\nRa 176,2,28672\n"
"v_permlane16_swap_b32 v86, v84\nv_permlane16_swap_b32 v87, v85\nMvav 132,128,76\nAD 86,86,84\nAD 87,87,85\nMvav 140,132,76\nRa 180,2,29696\nv_rcp_f32 v86, v86\nWL 8\n"
"v_rcp_f32 v87, v87\nRa 184,2,30720\nv_xor_b32 v25, s3, v25\nMvav 148,136,76\nD1 1,16,46,3072\nMvav 156,140,76\nRa 188,2,31744\nMU 128,128,86\nMU 129,129,86\nCV 96,128,129\n"
"MU 130,130,86\nMU 131,131,86\nRa 128,2,0\nCV 97,130,131\nMU 132,132,87\nMvav 164,144,76\nMU 133,133,87\nCV 100,132,133\nMvav 172,148,76\nRa 132,2,1024\nMU 134,134,87\n"
"MU 135,135,87\nWL 8\ns_add_u32 m0, s89, 131072\ns_add_u32 s46, s96, 1212416\nRa 136,2,2048\nCV 101,134,135\nMU 136,136,86\nMvav 180,152,76\nD1 1,16,46,0\nMU 137,137,86\n"
"Mvav 188,156,76\nRa 140,2,3072\nCV 98,136,137\nMU 138,138,86\nMU 139,139,86\nCV 99,138,139\nMU 140,140,87\nRa 144,2,4096\nMU 141,141,87\nCV 102,140,141\nMvav 196,160,76\n"
"MU 142,142,87\nMU 143,143,87\nMvav 204,164,76\nRa 148,2,5120\nCV 103,142,143\nMU 144,144,86\nWL 8\nMU 145,145,86\nCV 104,144,145\nRa 152,2,6144\nMU 146,146,86\nMU 147,147,86\n"
"Mvav 212,168,76\nD1 1,16,46,1024\nCV 105,146,147\nMvav 220,172,76\nRa 156,2,7168\nMU 148,148,87\nMU 149,149,87\nCV 108,148,149\nMU 150,150,87\nMU 151,151,87\nRa 160,2,8192\n"
"CV 109,150,151\nMU 152,152,86\nMvav 228,176,76\nMU 153,153,86\nCV 106,152,153\nMvav 236,180,76\nRa 164,2,9216\nMU 154,154,86\nMU 155,155,86\nWL 8\nCV 107,154,155\nMU 156,156,87\n"
"Ra 168,2,10240\nMU 157,157,87\nCV 110,156,157\nMvav 244,184,76\nD1 1,16,46,2048\nMU 158,158,87\nMvav 252,188,76\nRa 172,2,11264\nMU 159,159,87\nCV 111,158,159\nMU 160,160,86\n"
"MU 161,161,86\nCV 112,160,161\nZvav 128,128,96\nRa 176,2,12288\nMU 162,162,86\nMU 163,163,86\nMvav 128,132,104\nCV 113,162,163\nMU 164,164,87\nZvav 132,132,108\nRa 180,2,13312\n"
"MU 165,165,87\nCV 116,164,165\nMvav 132,128,100\ns_waitcnt vmcnt(15) lgkmcnt(8)\nMU 166,166,87\nMU 167,167,87\nZvav 140,136,100\nRa 184,2,14336\nCV 117,166,167\nMU 168,168,86\n"
"Mvav 140,140,108\nD1 1,16,46,3072\nMU 169,169,86\nCV 114,168,169\nZvav 136,140,104\nRa 188,2,15360\ns_barrier\nMU 170,170,86\nMvav 136,136,96\nMU 171,171,86\nCV 115,170,171\n"
"MU 172,172,87\nZvav 144,144,96\nRa 128,2,32768\nMU 173,173,87\nCV 118,172,173\nMvav 144,148,104\nMU 174,174,87\nMU 175,175,87\n"
"buffer_store_dword v25, off, s[16:19], s54 offset:512 sc0 sc1\nZvav 148,148,108\nRa 132,2,33792\nCV 119,174,175\nMU 176,176,86\nMvav 148,144,100\nWL 8\ns_add_u32 m0, s89, 49152\n"
"s_add_u32 s46, s96, 1179648\nZvav 156,152,100\nRa 136,2,34816\nMU 177,177,86\nCV 120,176,177\nMvav 156,156,108\nD1 1,16,46,0\nMU 178,178,86\nMU 179,179,86\nZvav 152,156,104\n"
"Ra 140,2,35840\nCV 121,178,179\nMU 180,180,87\nMvav 152,152,96\nMU 181,181,87\nCV 124,180,181\nMU 182,182,87\nZvav 64,160,96\nRa 144,2,36864\nMU 183,183,87\nCV 125,182,183\n"
"Mvav 64,164,104\nMU 184,184,86\nMU 185,185,86\nZvav 68,164,108\nRa 148,2,37888\nCV 122,184,185\nMU 186,186,86\nMvav 68,160,100\nWL 8\nMU 187,187,86\nCV 123,186,187\n"
"Zvav 76,168,100\nRa 152,2,38912\nMU 188,188,87\nMU 189,189,87\nMvav 76,172,108\nD1 1,16,46,1024\nCV 126,188,189\nMU 190,190,87\nZvav 72,172,104\nRa 156,2,39936\nMU 191,191,87\n"
"CV 127,190,191\nMvav 72,168,96\nMU 192,192,86\nMU 193,193,86\nCV 32,192,193\nMvav 128,176,112\nRa 160,2,40960\nMU 194,194,86\nMU 195,195,86\nMvav 128,180,120\nCV 33,194,195\n"
"MU 196,196,87\nMU 197,197,87\nMvav 132,180,124\nRa 164,2,41984\nCV 36,196,197\nMU 198,198,87\nMvav 132,176,116\nWL 8\nMU 199,199,87\nMvav 140,184,116\nRa 168,2,43008\n"
"CV 37,198,199\nMU 200,200,86\nMvav 140,188,124\nD1 1,16,46,2048\nMU 201,201,86\nCV 34,200,201\nMvav 136,188,120\nRa 172,2,44032\nMU 202,202,86\nMU 203,203,86\nMvav 136,184,112\n"
"CV 35,202,203\nMU 204,204,87\nMU 205,205,87\nMvav 144,128,112\nRa 176,2,45056\nCV 38,204,205\nMU 206,206,87\nMvav 144,132,120\nMU 207,207,87\nCV 39,206,207\nMvav 148,132,124\n"
"Ra 180,2,46080\nMU 208,208,86\nMU 209,209,86\nMvav 148,128,116\nWL 8\nCV 40,208,209\nMU 210,210,86\nMvav 156,136,116\nRa 184,2,47104\nMU 211,211,86\nCV 41,210,211\n"
"Mvav 156,140,124\nD1 1,16,46,3072\nMU 212,212,87\nMU 213,213,87\nMvav 152,140,120\nRa 188,2,48128\nCV 44,212,213\nMvav 152,136,112\nMU 214,214,87\nMU 215,215,87\nCV 45,214,215\n"
"Mvav 64,144,112\nRa 128,2,49152\nMU 216,216,86\nMU 217,217,86\nMvav 64,148,120\nCV 42,216,217\nMU 218,218,86\nMU 219,219,86\nMvav 68,148,124\nRa 132,2,50176\nCV 43,218,219\n"
"MU 220,220,87\nMvav 68,144,116\nWL 8\ns_add_u32 m0, s89, 147456\ns_add_u32 s46, s96, 1245184\nMvav 76,152,116\nRa 136,2,51200\nMU 221,221,87\nCV 46,220,221\nMvav 76,156,124\n"
"D1 1,16,46,0\nMU 222,222,87\nMU 223,223,87\nMvav 72,156,120\nRa 140,2,52224\nCV 47,222,223\nMvav 72,152,112\nMU 224,224,86\nMU 225,225,86\nCV 48,224,225\nMvav 128,160,32\n"
"Ra 144,2,53248\nMU 226,226,86\nMU 227,227,86\nMvav 128,164,40\nCV 49,226,227\nMU 228,228,87\nMvav 132,164,44\nRa 148,2,54272\nMU 229,229,87\nCV 52,228,229\nMvav 132,160,36\nWL 8\n"
"MU 230,230,87\nMU 231,231,87\nMvav 140,168,36\nRa 152,2,55296\nCV 53,230,231\nMU 232,232,86\nMvav 140,172,44\nD1 1,16,46,1024\nMU 233,233,86\nCV 50,232,233\nMvav 136,172,40\n"
"Ra 156,2,56320\nMU 234,234,86\nMvav 136,168,32\nMU 235,235,86\nCV 51,234,235\nMU 236,236,87\nMvav 144,176,32\nRa 160,2,57344\nMU 237,237,87\nCV 54,236,237\nMvav 144,180,40\n"
"MU 238,238,87\nMU 239,239,87\nCV 55,238,239\nMvav 148,180,44\nRa 164,2,58368\nMU 240,240,86\nMU 241,241,86\nMvav 148,176,36\nWL 8\nCV 56,240,241\nMvav 156,184,36\nRa 168,2,59392\n"
"MU 242,242,86\nMU 243,243,86\nMvav 156,188,44\nD1 1,16,46,2048\nCV 57,242,243\nMU 244,244,87\nMvav 152,188,40\nRa 172,2,60416\nMU 245,245,87\nCV 60,244,245\nMvav 152,184,32\n"
"MU 246,246,87\nMU 247,247,87\nCV 61,246,247\nMvav 64,128,32\nRa 176,2,61440\nMU 248,248,86\nMU 249,249,86\nMvav 64,132,40\nCV 58,248,249\nMU 250,250,86\nMvav 68,132,44\n"
"Ra 180,2,62464\nMU 251,251,86\nCV 59,250,251\nMvav 68,128,36\ns_waitcnt vmcnt(16) lgkmcnt(8)\nMU 252,252,87\nMU 253,253,87\nMvav 76,136,36\nRa 184,2,63488\nCV 62,252,253\n"
"MU 254,254,87\nMvav 76,140,44\nD1 1,16,46,3072\nMU 255,255,87\nCV 63,254,255\nMvav 72,140,40\nRa 188,2,64512\ns_barrier\nMvav 72,136,32\nMvav 128,144,48\nRa 128,3,0\n"
"Mvav 128,148,56\nMvav 132,148,60\nRa 132,3,1024\nMvav 132,144,52\nWL 8\nMvav 140,152,52\nRa 136,3,2048\nMvav 140,156,60\nMvav 136,156,56\nRa 140,3,3072\nMvav 136,152,48\n"
"Mvav 144,160,48\nRa 144,3,4096\nMvav 144,164,56\nMvav 148,164,60\nRa 148,3,5120\nCV 132,132,133\nMvav 148,160,52\nWL 8\nCV 133,134,135\nMvav 156,168,52\n.W596:\n"
"s_cbranch_execz .WB416\nRa 152,3,6144\nCV 134,140,141\nMvav 156,172,60\nCV 135,142,143\ns_add_u32 s5, s94, 0\nMvav 152,172,56\nRa 156,3,7168\nSv 132,1,16,5,1024\nMvav 152,168,48\n"
"CV 128,128,129\nCV 129,130,131\nMvav 64,176,48\nRa 160,3,8192\nCV 130,136,137\nMvav 64,180,56\nbuffer_load_dword v27, off, s[16:19], s60 offset:512 sc0 sc1\nCV 131,138,139\n"
"Mvav 68,180,60\nRa 164,3,9216\nSv 128,1,16,5,0\nMvav 68,176,52\nWL 6\nCV 148,148,149\nMvav 76,184,52\nRa 168,3,10240\nCV 149,150,151\nMvav 76,188,60\nCV 150,156,157\n"
"CV 151,158,159\nMvav 72,188,56\nRa 172,3,11264\ns_add_u32 s5, s94, 131072\nMvav 72,184,48\nSv 148,1,16,5,1024\nCV 144,144,145\nZvav 160,128,96\nRa 176,3,12288\nCV 145,146,147\n"
"Mvav 160,132,104\nCV 146,152,153\nCV 147,154,155\nMvav 160,136,112\nRa 180,3,13312\nSv 144,1,16,5,0\nMvav 160,140,120\nWL 6\nCV 68,68,69\nZvav 164,140,124\nRa 184,3,14336\n"
"CV 69,70,71\nMvav 164,136,116\nCV 70,76,77\nCV 71,78,79\nMvav 164,132,108\nRa 188,3,15360\ns_add_u32 s5, s94, 262144\nMvav 164,128,100\nSv 68,1,16,5,1024\nCV 64,64,65\n"
"Mvav 164,144,36\nRa 128,3,16384\nCV 65,66,67\nMvav 164,148,44\nCV 66,72,73\nCV 67,74,75\nMvav 164,152,52\nRa 132,3,17408\nSv 64,1,16,5,0\nMvav 164,156,60\nWL 6\nMvav 160,156,56\n"
"Ra 136,3,18432\nMvav 160,152,48\nMvav 160,148,40\nRa 140,3,19456\nMvav 160,144,32\nZvav 168,160,96\nRa 144,3,20480\nMvav 168,164,104\nMvav 168,168,112\nRa 148,3,21504\n"
"Mvav 168,172,120\nWL 6\nZvav 172,172,124\nRa 152,3,22528\nMvav 172,168,116\nMvav 172,164,108\nRa 156,3,23552\nMvav 172,160,100\nMvav 172,176,36\nRa 160,3,24576\nMvav 172,180,44\n"
"Mvav 172,184,52\nRa 164,3,25600\nMvav 172,188,60\nWL 6\nMvav 168,188,56\nRa 168,3,26624\nMvav 168,184,48\nMvav 168,180,40\nRa 172,3,27648\nMvav 168,176,32\nZvav 176,128,96\n"
"Ra 176,3,28672\nCV 164,164,165\nMvav 176,132,104\nCV 165,166,167\nCV 166,172,173\nMvav 176,136,112\nRa 180,3,29696\nCV 167,174,175\nMvav 176,140,120\n"
"s_waitcnt vmcnt(11) lgkmcnt(6)\ns_add_u32 s5, s94, 393216\nZvav 180,140,124\nRa 184,3,30720\nSv 164,1,16,5,1024\nMvav 180,136,116\nCV 160,160,161\nCV 161,162,163\n"
"Mvav 180,132,108\nRa 188,3,31744\ns_barrier\nMvav 180,128,100\nCV 162,168,169\nCV 163,170,171\nMvav 180,144,36\nRa 128,3,32768\nSv 160,1,16,5,0\nMvav 180,148,44\nMvav 180,152,52\n"
"Ra 132,3,33792\nMvav 180,156,60\nWL 6\ns_add_u32 m0, s89, 81920\ns_add_u32 s46, s96, 1277952\nMvav 176,156,56\nRa 136,3,34816\nMvav 176,152,48\nD1 1,16,46,0\nMvav 176,148,40\n"
"Ra 140,3,35840\nMvav 176,144,32\nZvav 184,160,96\nRa 144,3,36864\nMvav 184,164,104\nMvav 184,168,112\nRa 148,3,37888\nMvav 184,172,120\nWL 6\nZvav 188,172,124\nRa 152,3,38912\n"
"Mvav 188,168,116\nD1 1,16,46,1024\nMvav 188,164,108\nRa 156,3,39936\nMvav 188,160,100\nMvav 188,176,36\nRa 160,3,40960\nWV 9\nv_cmp_ne_u32 vcc, s15, v27\ns_nop 0\n"
"s_cbranch_vccnz .S520\n.S521:\nMvav 188,180,44\ns_add_u32 s4, s92, 0\nMvav 188,184,52\nRa 164,3,41984\nMvav 188,188,60\nWL 6\n"
"buffer_load_dwordx4 v[208:211], v1, s[16:19], s4 offen offset:0 sc0 sc1\nMvav 184,188,56\nRa 168,3,43008\nMvav 184,184,48\nD1 1,16,46,2048\nMvav 184,180,40\nRa 172,3,44032\n"
"Mvav 184,176,32\nbuffer_load_dwordx4 v[212:215], v1, s[16:19], s4 offen offset:1024 sc0 sc1\nZvav 192,128,96\nRa 176,3,45056\nv_lshrrev_b32 v88, 8, v1\nMvav 192,132,104\n"
"v_lshlrev_b32 v89, 5, v88\nCV 180,180,181\nMvav 192,136,112\nRa 180,3,46080\nCV 181,182,183\nMvav 192,140,120\nWL 6\nCV 182,188,189\nZvav 196,140,124\nRa 184,3,47104\n"
"v_bfe_u32 v90, v1, 4, 4\nMvav 196,136,116\nD1 1,16,46,3072\nCV 183,190,191\nMvav 196,132,108\nRa 188,3,48128\ns_add_u32 s5, s94, 524288\nMvav 196,128,100\n"
"v_mul_u32_u24 v91, 896, v90\nSv 180,1,16,5,1024\nMvav 196,144,36\nRa 128,3,49152\nv_lshl_add_u32 v91, v88, 4, v91\nMvav 196,148,44\nv_add_u32 v90, s21, v90\nCV 176,176,177\n"
"Mvav 196,152,52\nRa 132,3,50176\nCV 177,178,179\nMvav 196,156,60\nWL 6\ns_add_u32 m0, s89, 65536\ns_add_u32 s46, s96, 1310720\nMvav 192,156,56\nRa 136,3,51200\n"
"v_lshlrev_b32 v88, 3, v90\nMvav 192,152,48\nD1 1,16,46,0\nCV 178,184,185\nMvav 192,148,40\nRa 140,3,52224\nCV 179,186,187\nMvav 192,144,32\nv_add_u32 v94, 64, v90\n"
"Sv 176,1,16,5,0\nZvav 200,160,96\nRa 144,3,53248\ns_mov_b32 s63, 0xc040000\nMvav 200,164,104\nv_lshrrev_b32 v92, 8, v1\nMvav 200,168,112\nRa 148,3,54272\nMvav 200,172,120\nWL 6\n"
"Zvav 204,172,124\nRa 152,3,55296\nv_bfe_u32 v93, v1, 6, 2\nMvav 204,168,116\nD1 1,16,46,1024\nv_cmp_eq_u32 vcc, v92, v93\nv_and_b32 v92, 16, v1\nv_mov_b32 v93, 0x3f80\n"
"v_cndmask_b32 v93, 0, v93, vcc\nMvav 204,164,108\nRa 156,3,56320\nMvav 204,160,100\nv_lshlrev_b32 v93, v92, v93\nMvav 204,176,36\nRa 160,3,57344\nv_and_b32 v92, 32, v1\n"
"Mvav 204,180,44\ns_add_u32 s4, s92, 131072\nv_cmp_eq_u32 vcc, 0, v92\nv_mov_b32 v4, 0\nv_mov_b32 v5, 0\nv_cndmask_b32 v6, 0, v93, vcc\nMvav 204,184,52\nRa 164,3,58368\n"
"Mvav 204,188,60\nWL 6\nbuffer_load_dwordx4 v[216:219], v1, s[16:19], s4 offen offset:0 sc0 sc1\nMvav 200,188,56\nRa 168,3,59392\nv_sub_u32 v7, v93, v6\nMvav 200,184,48\n"
"D1 1,16,46,2048\nMvav 200,180,40\nRa 172,3,60416\nMvav 200,176,32\nbuffer_load_dwordx4 v[220:223], v1, s[16:19], s4 offen offset:1024 sc0 sc1\nZvav 160,128,96\nRa 176,3,61440\n"
"v_mov_b32 v8, 0\nMvav 160,132,104\nv_mov_b32 v9, 0\nCV 196,196,197\nMvav 160,136,112\nRa 180,3,62464\nCV 197,198,199\nMvav 160,140,120\nWL 6\nCV 198,204,205\nZvav 164,140,124\n"
"Ra 184,3,63488\nCV 199,206,207\nMvav 164,136,116\nD1 1,16,46,3072\ns_add_u32 s5, s94, 655360\nMvav 164,132,108\nRa 188,3,64512\ns_barrier\nMvav 164,128,100\nSv 196,1,16,5,1024\n"
"CV 192,192,193\nMvav 164,144,36\nRa 128,0,0\nCV 193,194,195\nMvav 164,148,44\nCV 194,200,201\nCV 195,202,203\nMvav 164,152,52\nRa 132,0,1024\nSv 192,1,16,5,0\nMvav 164,156,60\n"
"WL 6\ns_add_u32 m0, s89, 98304\ns_add_u32 s46, s96, 1343488\nMvav 160,156,56\nRa 136,0,2048\nMvav 160,152,48\nD1 1,16,46,0\nMvav 160,148,40\nRa 140,0,3072\nMvav 160,144,32\n"
"Zvav 168,160,96\nRa 144,0,4096\nWV 12\nv_max_u32 v21, v211, v215\nv_cmp_eq_u32 vcc, -1, v21\ns_nop 0\ns_cbranch_vccnz .S496\n.S497:\nMvav 168,164,104\n"
"v_lshlrev_b32 v176, 16, v208\nMvav 168,168,112\nRa 148,0,5120\nMvav 168,172,120\nWL 6\nv_and_b32 v177, 0xffff0000, v208\nZvav 172,172,124\nRa 152,0,6144\nMvav 172,168,116\n"
"D1 1,16,46,1024\nMvav 172,164,108\nRa 156,0,7168\nv_lshlrev_b32 v178, 16, v209\nMvav 172,160,100\nv_and_b32 v179, 0xffff0000, v209\nMvav 172,176,36\nRa 160,0,8192\n"
"v_lshlrev_b32 v180, 16, v212\nMvav 172,180,44\nv_and_b32 v181, 0xffff0000, v212\ns_add_u32 s4, s92, 262144\nMvav 172,184,52\nRa 164,0,9216\nMvav 172,188,60\nWL 6\n"
"v_lshlrev_b32 v182, 16, v213\nbuffer_load_dwordx4 v[224:227], v1, s[16:19], s4 offen offset:0 sc0 sc1\nMvav 168,188,56\nRa 168,0,10240\nMvav 168,184,48\nD1 1,16,46,2048\n"
"Mvav 168,180,40\nRa 172,0,11264\nv_and_b32 v183, 0xffff0000, v213\nMvav 168,176,32\nv_lshlrev_b32 v184, 16, v210\n"
"buffer_load_dwordx4 v[228:231], v1, s[16:19], s4 offen offset:1024 sc0 sc1\nMvav 176,128,96\nRa 176,0,12288\nv_and_b32 v185, 0xffff0000, v210\nMvav 176,132,104\n"
"v_lshlrev_b32 v186, 16, v211\nCV 164,164,165\nMvav 176,136,112\nRa 180,0,13312\nCV 165,166,167\nMvav 176,140,120\nWL 6\nCV 166,172,173\nMvav 180,140,124\nRa 184,0,14336\n"
"v_and_b32 v187, 0xffff0000, v211\nMvav 180,136,116\nD1 1,16,46,3072\nCV 167,174,175\nMvav 180,132,108\nRa 188,0,15360\ns_add_u32 s5, s94, 786432\nMvav 180,128,100\n"
"v_lshlrev_b32 v188, 16, v214\nSv 164,1,16,5,1024\nMvav 180,144,36\nRa 128,0,16384\nv_and_b32 v189, 0xffff0000, v214\nMvav 180,148,44\nv_lshlrev_b32 v190, 16, v215\nCV 160,160,161\n"
"Mvav 180,152,52\nRa 132,0,17408\nCV 161,162,163\nMvav 180,156,60\nWL 6\ns_add_u32 m0, s89, 114688\ns_add_u32 s46, s96, 1376256\nMvav 176,156,56\nRa 136,0,18432\n"
"v_and_b32 v191, 0xffff0000, v215\nMvav 176,152,48\nD1 1,16,46,0\nCV 162,168,169\nMvav 176,148,40\nRa 140,0,19456\nCV 163,170,171\nMvav 176,144,32\nSv 160,1,16,5,0\n"
"Mvav 184,160,96\nRa 144,0,20480\nWV 12\nv_max_u32 v21, v219, v223\nv_cmp_eq_u32 vcc, -1, v21\ns_nop 0\ns_cbranch_vccnz .S499\n.S500:\nMvav 184,164,104\n"
"v_lshlrev_b32 v192, 16, v216\nMvav 184,168,112\nRa 148,0,21504\nMvav 184,172,120\nWL 6\nv_and_b32 v193, 0xffff0000, v216\nMvav 188,172,124\nRa 152,0,22528\nMvav 188,168,116\n"
"D1 1,16,46,1024\nMvav 188,164,108\nRa 156,0,23552\nv_lshlrev_b32 v194, 16, v217\nMvav 188,160,100\nv_and_b32 v195, 0xffff0000, v217\nMvav 188,176,36\nRa 160,0,24576\n"
"v_lshlrev_b32 v196, 16, v220\nMvav 188,180,44\nv_and_b32 v197, 0xffff0000, v220\ns_add_u32 s4, s92, 393216\nMvav 188,184,52\nRa 164,0,25600\nMvav 188,188,60\nWL 6\n"
"v_lshlrev_b32 v198, 16, v221\nbuffer_load_dwordx4 v[208:211], v1, s[16:19], s4 offen offset:0 sc0 sc1\nMvav 184,188,56\nRa 168,0,26624\nMvav 184,184,48\nD1 1,16,46,2048\n"
"Mvav 184,180,40\nRa 172,0,27648\nv_and_b32 v199, 0xffff0000, v221\nMvav 184,176,32\nv_lshlrev_b32 v200, 16, v218\n"
"buffer_load_dwordx4 v[212:215], v1, s[16:19], s4 offen offset:1024 sc0 sc1\nMvav 192,128,96\nRa 176,0,28672\nv_and_b32 v201, 0xffff0000, v218\nMvav 192,132,104\n"
"v_lshlrev_b32 v202, 16, v219\nCV 180,180,181\nMvav 192,136,112\nRa 180,0,29696\nCV 181,182,183\nMvav 192,140,120\ns_waitcnt vmcnt(15) lgkmcnt(6)\nCV 182,188,189\nMvav 196,140,124\n"
"Ra 184,0,30720\nv_and_b32 v203, 0xffff0000, v219\nMvav 196,136,116\nD1 1,16,46,3072\nCV 183,190,191\nMvav 196,132,108\nRa 188,0,31744\ns_barrier\nMvav 196,128,100\n"
"v_lshlrev_b32 v204, 16, v222\ns_add_u32 s5, s94, 917504\nMvav 196,144,36\nRa 128,2,16384\nv_and_b32 v205, 0xffff0000, v222\nMvav 196,148,44\nv_lshlrev_b32 v206, 16, v223\n"
"Sv 180,1,16,5,1024\nMvav 196,152,52\nRa 132,2,17408\nCV 176,176,177\nMvav 196,156,60\nWL 6\ns_add_u32 m0, s89, 0\ns_add_u32 s46, s96, 1409024\nMvav 192,156,56\nRa 136,2,18432\n"
"v_and_b32 v207, 0xffff0000, v223\nMvav 192,152,48\nD1 1,16,46,0\nCV 177,178,179\nMvav 192,148,40\nRa 140,2,19456\nCV 178,184,185\nMvav 192,144,32\nCV 179,186,187\nSv 176,1,16,5,0\n"
"Mvav 200,160,96\nRa 144,2,20480\nWV 12\nv_max_u32 v21, v227, v231\nv_cmp_eq_u32 vcc, -1, v21\ns_nop 0\ns_cbranch_vccnz .S502\n.S503:\nMvav 200,164,104\n"
"v_lshlrev_b32 v160, 16, v224\nMvav 200,168,112\nRa 148,2,21504\nMvav 200,172,120\nWL 6\nv_and_b32 v161, 0xffff0000, v224\nMvav 204,172,124\nRa 152,2,22528\nMvav 204,168,116\n"
"D1 1,16,46,1024\nMvav 204,164,108\nRa 156,2,23552\nv_lshlrev_b32 v162, 16, v225\nMvav 204,160,100\nv_and_b32 v163, 0xffff0000, v225\nMvav 204,176,36\nRa 160,2,24576\n"
"v_lshlrev_b32 v164, 16, v228\nMvav 204,180,44\nv_and_b32 v165, 0xffff0000, v228\ns_add_u32 s4, s92, 524288\nMvav 204,184,52\nRa 164,2,25600\nMvav 204,188,60\nWL 6\n"
"v_lshlrev_b32 v166, 16, v229\nbuffer_load_dwordx4 v[216:219], v1, s[16:19], s4 offen offset:0 sc0 sc1\nMvav 200,188,56\nRa 168,2,26624\nMvav 200,184,48\nD1 1,16,46,2048\n"
"Mvav 200,180,40\nRa 172,2,27648\nv_and_b32 v167, 0xffff0000, v229\nMvav 200,176,32\nv_lshlrev_b32 v168, 16, v226\n"
"buffer_load_dwordx4 v[220:223], v1, s[16:19], s4 offen offset:1024 sc0 sc1\nMvav 160,128,96\nRa 176,2,28672\nv_and_b32 v169, 0xffff0000, v226\nMvav 160,132,104\n"
"v_lshlrev_b32 v170, 16, v227\nCV 196,196,197\nMvav 160,136,112\nRa 180,2,29696\nCV 197,198,199\n.W597:\ns_cbranch_execz .WB416\nMvav 160,140,120\nWL 6\nCV 198,204,205\n"
"Mvav 164,140,124\nRa 184,2,30720\nv_and_b32 v24, 0x70, v1\nMvav 164,136,116\nD1 1,16,46,3072\nCV 199,206,207\nMvav 164,132,108\nRa 188,2,31744\nv_and_b32 v171, 0xffff0000, v227\n"
"Mvav 164,128,100\nv_lshlrev_b32 v172, 16, v230\ns_add_u32 s5, s94, 1048576\nMvav 164,144,36\nRa 128,2,0\nv_lshlrev_b32 v24, 5, v24\nMvav 164,148,44\n"
"v_and_b32 v173, 0xffff0000, v230\nSv 196,1,16,5,1024\nMvav 164,152,52\nRa 132,2,1024\nCV 192,192,193\nMvav 164,156,60\nWL 6\nv_lshlrev_b32 v174, 16, v231\nMvav 160,156,56\n"
"Ra 136,2,2048\nbuffer_load_dword v27, v24, s[16:19], s41 offen sc0 sc1\nMvav 160,152,48\nv_and_b32 v175, 0xffff0000, v231\nCV 193,194,195\nMvav 160,148,40\nRa 140,2,3072\n"
"CV 194,200,201\nMvav 160,144,32\nCV 195,202,203\nSv 192,1,16,5,0\nMvav 168,160,96\nRa 144,2,4096\nWV 12\nv_max_u32 v21, v211, v215\nv_cmp_eq_u32 vcc, -1, v21\ns_nop 0\n"
"s_cbranch_vccnz .S505\n.S506:\nMvav 168,164,104\nv_lshlrev_b32 v176, 16, v208\nMvav 168,168,112\nRa 148,2,5120\nMvav 168,172,120\nWL 6\nMvav 172,172,124\nRa 152,2,6144\n"
"v_and_b32 v177, 0xffff0000, v208\nMvav 172,168,116\nv_lshlrev_b32 v178, 16, v209\nMvav 172,164,108\nRa 156,2,7168\nMvav 172,160,100\nv_and_b32 v179, 0xffff0000, v209\n"
"Mvav 172,176,36\nRa 160,2,8192\nv_lshlrev_b32 v180, 16, v212\nMvav 172,180,44\nv_and_b32 v181, 0xffff0000, v212\ns_add_u32 s4, s92, 655360\nMvav 172,184,52\nRa 164,2,9216\n"
"Mvav 172,188,60\nWL 6\nbuffer_load_dwordx4 v[224:227], v1, s[16:19], s4 offen offset:0 sc0 sc1\nMvav 168,188,56\nRa 168,2,10240\nv_lshlrev_b32 v182, 16, v213\nMvav 168,184,48\n"
"v_and_b32 v183, 0xffff0000, v213\nMvav 168,180,40\nRa 172,2,11264\nMvav 168,176,32\nv_lshlrev_b32 v184, 16, v210\n"
"buffer_load_dwordx4 v[228:231], v1, s[16:19], s4 offen offset:1024 sc0 sc1\nMvav 176,128,96\nRa 176,2,12288\nv_and_b32 v185, 0xffff0000, v210\nMvav 176,132,104\n"
"v_lshlrev_b32 v186, 16, v211\nCV 164,164,165\nMvav 176,136,112\nRa 180,2,13312\nCV 165,166,167\nMvav 176,140,120\ns_waitcnt vmcnt(13) lgkmcnt(6)\nCV 166,172,173\nMvav 180,140,124\n"
"Ra 184,2,14336\nCV 167,174,175\nMvav 180,136,116\nv_and_b32 v187, 0xffff0000, v211\ns_add_u32 s5, s94, 1179648\nMvav 180,132,108\nRa 188,2,15360\ns_barrier\nMvav 180,128,100\n"
"v_lshlrev_b32 v188, 16, v214\nSv 164,1,16,5,1024\nMvav 180,144,36\nRa 128,2,32768\nv_and_b32 v189, 0xffff0000, v214\nMvav 180,148,44\nv_lshlrev_b32 v190, 16, v215\nCV 160,160,161\n"
"Mvav 180,152,52\nRa 132,2,33792\nCV 161,162,163\nMvav 180,156,60\nWL 6\nCV 162,168,169\nMvav 176,156,56\nRa 136,2,34816\nCV 163,170,171\nMvav 176,152,48\n"
"v_and_b32 v191, 0xffff0000, v215\nSv 160,1,16,5,0\nMvav 176,148,40\nRa 140,2,35840\nMvav 176,144,32\nMvav 184,160,96\nRa 144,2,36864\nWV 8\nv_max_u32 v21, v219, v223\n"
"v_cmp_eq_u32 vcc, -1, v21\ns_nop 0\ns_cbranch_vccnz .S508\n.S509:\nMvav 184,164,104\nv_lshlrev_b32 v192, 16, v216\nMvav 184,168,112\nRa 148,2,37888\nMvav 184,172,120\nWL 6\n"
"Mvav 188,172,124\nRa 152,2,38912\nv_and_b32 v193, 0xffff0000, v216\nMvav 188,168,116\nv_lshlrev_b32 v194, 16, v217\nMvav 188,164,108\nRa 156,2,39936\nMvav 188,160,100\n"
"v_and_b32 v195, 0xffff0000, v217\nMvav 188,176,36\nRa 160,2,40960\nv_lshlrev_b32 v196, 16, v220\nMvav 188,180,44\nv_and_b32 v197, 0xffff0000, v220\ns_add_u32 s4, s92, 786432\n"
"Mvav 188,184,52\nRa 164,2,41984\nMvav 188,188,60\nWL 6\nbuffer_load_dwordx4 v[208:211], v1, s[16:19], s4 offen offset:0 sc0 sc1\nMvav 184,188,56\nRa 168,2,43008\n"
"v_lshlrev_b32 v198, 16, v221\nMvav 184,184,48\nv_and_b32 v199, 0xffff0000, v221\nMvav 184,180,40\nRa 172,2,44032\nMvav 184,176,32\nv_lshlrev_b32 v200, 16, v218\n"
"buffer_load_dwordx4 v[212:215], v1, s[16:19], s4 offen offset:1024 sc0 sc1\nMvav 192,128,96\nRa 176,2,45056\nv_and_b32 v201, 0xffff0000, v218\nMvav 192,132,104\n"
"v_lshlrev_b32 v202, 16, v219\nCV 180,180,181\nMvav 192,136,112\nRa 180,2,46080\nCV 181,182,183\nMvav 192,140,120\nWL 6\nCV 182,188,189\nMvav 196,140,124\nRa 184,2,47104\n"
"CV 183,190,191\nMvav 196,136,116\nv_and_b32 v203, 0xffff0000, v219\ns_add_u32 s5, s94, 1310720\nMvav 196,132,108\nRa 188,2,48128\nSv 180,1,16,5,1024\nMvav 196,128,100\n"
"v_lshlrev_b32 v204, 16, v222\nCV 176,176,177\nMvav 196,144,36\nRa 128,2,49152\nv_and_b32 v205, 0xffff0000, v222\nMvav 196,148,44\nv_lshlrev_b32 v206, 16, v223\nCV 177,178,179\n"
"Mvav 196,152,52\nRa 132,2,50176\nCV 178,184,185\nMvav 196,156,60\nWL 6\nCV 179,186,187\nMvav 192,156,56\nRa 136,2,51200\nSv 176,1,16,5,0\nMvav 192,152,48\n"
"v_and_b32 v207, 0xffff0000, v223\nMvav 192,148,40\nRa 140,2,52224\nMvav 192,144,32\nMvav 200,160,96\nRa 144,2,53248\nWV 6\nv_max_u32 v21, v227, v231\nv_cmp_eq_u32 vcc, -1, v21\n"
"s_nop 0\ns_cbranch_vccnz .S511\n.S512:\nMvav 200,164,104\nv_lshlrev_b32 v160, 16, v224\nv_xor_b32 v27, s15, v27\nv_and_b32 v26, 0xffffdfff, v27\nv_cmp_ne_u32 s[64:65], 0, v27\n"
"v_cmp_ne_u32 vcc, 0, v26\ns_nop 0\ns_cbranch_vccnz .S523\n.S524:\nMvav 200,168,112\nRa 148,2,54272\ns_bitcmp1_b32 s64, 0\ns_cselect_b32 s2, 0x2000, 0\nMvav 200,172,120\nWL 6\n"
"Mvav 204,172,124\nRa 152,2,55296\nv_and_b32 v161, 0xffff0000, v224\nMvav 204,168,116\nv_lshlrev_b32 v162, 16, v225\nMvav 204,164,108\nRa 156,2,56320\ns_xor_b32 s2, s2, s20\n"
"Mvav 204,160,100\nv_and_b32 v163, 0xffff0000, v225\nMvav 204,176,36\nRa 160,2,57344\nv_lshlrev_b32 v164, 16, v228\nMvav 204,180,44\nv_and_b32 v165, 0xffff0000, v228\n"
"Mvav 204,184,52\nRa 164,2,58368\nMvav 204,188,60\nWL 6\ns_add_u32 s24, s2, 0x0\nMvav 200,188,56\nRa 168,2,59392\nv_lshlrev_b32 v166, 16, v229\nMvav 200,184,48\n"
"v_and_b32 v167, 0xffff0000, v229\ns_bitcmp1_b32 s64, 1\ns_cselect_b32 s2, 0x2000, 0\nMvav 200,180,40\nRa 172,2,60416\nMvav 200,176,32\nv_lshlrev_b32 v168, 16, v226\n"
"Mvav 160,128,96\nRa 176,2,61440\nv_and_b32 v169, 0xffff0000, v226\nMvav 160,132,104\nv_lshlrev_b32 v170, 16, v227\nCV 196,196,197\nMvav 160,136,112\nRa 180,2,62464\n"
"CV 197,198,199\nMvav 160,140,120\nWL 6\nCV 198,204,205\nMvav 164,140,124\nRa 184,2,63488\nCV 199,206,207\nMvav 164,136,116\nv_and_b32 v171, 0xffff0000, v227\n"
"s_add_u32 s5, s94, 1441792\nMvav 164,132,108\nRa 188,2,64512\ns_barrier\nMvav 164,128,100\nv_lshlrev_b32 v172, 16, v230\nSv 196,1,16,5,1024\nMvav 164,144,36\nRa 128,3,0\n"
"v_and_b32 v173, 0xffff0000, v230\nMvav 164,148,44\nv_lshlrev_b32 v174, 16, v231\nCV 192,192,193\nMvav 164,152,52\nRa 132,3,1024\nCV 193,194,195\nMvav 164,156,60\nWL 6\n"
"s_xor_b32 s2, s2, s20\nMvav 160,156,56\nRa 136,3,2048\nCV 194,200,201\nMvav 160,152,48\nv_and_b32 v175, 0xffff0000, v231\nCV 195,202,203\nMvav 160,148,40\nRa 140,3,3072\n"
"Sv 192,1,16,5,0\nMvav 160,144,32\ns_add_u32 s25, s2, 0x1800000\ns_bitcmp1_b32 s64, 2\ns_cselect_b32 s2, 0x2000, 0\nMvav 168,160,96\nRa 144,3,4096\nWV 4\nv_max_u32 v21, v211, v215\n"
"v_cmp_eq_u32 vcc, -1, v21\ns_nop 0\ns_cbranch_vccnz .S514\n.S515:\nMvav 168,164,104\nv_lshlrev_b32 v176, 16, v208\nMvav 168,168,112\nRa 148,3,5120\nMvav 168,172,120\nWL 6\n"
"Mvav 172,172,124\nRa 152,3,6144\nv_and_b32 v177, 0xffff0000, v208\nMvav 172,168,116\nv_lshlrev_b32 v178, 16, v209\nMvav 172,164,108\nRa 156,3,7168\ns_xor_b32 s2, s2, s20\n"
"Mvav 172,160,100\nv_and_b32 v179, 0xffff0000, v209\nMvav 172,176,36\nRa 160,3,8192\nv_lshlrev_b32 v180, 16, v212\nMvav 172,180,44\nv_and_b32 v181, 0xffff0000, v212\n"
"Mvav 172,184,52\nRa 164,3,9216\nMvav 172,188,60\nWL 6\ns_add_u32 s26, s2, 0x3000000\nMvav 168,188,56\nRa 168,3,10240\nv_lshlrev_b32 v182, 16, v213\nMvav 168,184,48\n"
"v_and_b32 v183, 0xffff0000, v213\ns_bitcmp1_b32 s64, 3\ns_cselect_b32 s2, 0x2000, 0\nMvav 168,180,40\nRa 172,3,11264\nMvav 168,176,32\nv_lshlrev_b32 v184, 16, v210\n"
"Mvav 176,128,96\nRa 176,3,12288\ns_xor_b32 s2, s2, s20\nMvav 176,132,104\nv_and_b32 v185, 0xffff0000, v210\nCV 164,164,165\nMvav 176,136,112\nRa 180,3,13312\nCV 165,166,167\n"
"Mvav 176,140,120\nWL 6\nCV 166,172,173\nMvav 180,140,124\nRa 184,3,14336\nCV 167,174,175\nMvav 180,136,116\nv_lshlrev_b32 v186, 16, v211\ns_add_u32 s5, s94, 1572864\n"
"Mvav 180,132,108\nRa 188,3,15360\ns_add_u32 s27, s2, 0x4800000\nMvav 180,128,100\nv_and_b32 v187, 0xffff0000, v211\ns_bitcmp1_b32 s64, 4\ns_cselect_b32 s2, 0x2000, 0\n"
"Mvav 180,144,36\nv_lshlrev_b32 v188, 16, v214\nSv 164,1,16,5,1024\nMvav 180,148,44\nv_and_b32 v189, 0xffff0000, v214\nCV 160,160,161\nMvav 180,152,52\n"
"v_lshlrev_b32 v190, 16, v215\nCV 161,162,163\nMvav 180,156,60\nWL 4\ns_xor_b32 s2, s2, s20\nMvav 176,156,56\nv_and_b32 v191, 0xffff0000, v215\nCV 162,168,169\nMvav 176,152,48\n"
"s_add_u32 s32, s2, 0x6000000\ns_bitcmp1_b32 s64, 5\ns_cselect_b32 s2, 0x2000, 0\nMvav 176,148,40\ns_xor_b32 s2, s2, s20\nCV 163,170,171\nMvav 176,144,32\n"
"s_add_u32 s33, s2, 0x7800000\ns_bitcmp1_b32 s64, 6\ns_cselect_b32 s2, 0x2000, 0\nMvav 184,160,96\ns_xor_b32 s2, s2, s20\ns_add_u32 s34, s2, 0x9000000\nMvav 184,164,104\n"
"s_bitcmp1_b32 s64, 7\ns_cselect_b32 s2, 0x2000, 0\nMvav 184,168,112\ns_xor_b32 s2, s2, s20\nSv 160,1,16,5,0\nMvav 184,172,120\nWL 0\nMvav 188,172,124\n"
"s_add_u32 s35, s2, 0xa800000\nMvav 188,168,116\nMvav 188,164,108\nMvav 188,160,100\nMvav 188,176,36\nMvav 188,180,44\nMvav 188,184,52\nMvav 188,188,60\nMvav 184,188,56\n"
"Mvav 184,184,48\nMvav 184,180,40\nMvav 184,176,32\nCV 180,180,181\nCV 181,182,183\ns_nop 1\nCV 182,188,189\nCV 183,190,191\ns_add_u32 s5, s94, 1703936\nSv 180,1,16,5,1024\n"
"CV 176,176,177\nCV 177,178,179\nCV 178,184,185\nCV 179,186,187\nSv 176,1,16,5,0\ns_cmp_eq_u32 s13, 7\ns_cbranch_scc1 .Z526\n"
"buffer_load_dwordx4 v[12:15], v91, s[28:31], s22 offen offset:0\nbuffer_load_dwordx4 v[16:19], v91, s[28:31], s23 offen offset:0\n"
"buffer_load_dwordx4 v[64:67], v89, s[16:19], s40 offen offset:0 sc0 sc1\nbuffer_load_dwordx4 v[68:71], v89, s[16:19], s40 offen offset:16 sc0 sc1\n"
"buffer_load_dwordx4 v[72:75], v89, s[16:19], s40 offen offset:1792 sc0 sc1\nbuffer_load_dwordx4 v[76:79], v89, s[16:19], s40 offen offset:1808 sc0 sc1\n"
"buffer_load_dwordx2 v[80:81], v88, s[16:19], s63 offen offset:0 sc0 sc1\nbuffer_load_dwordx2 v[82:83], v88, s[16:19], s63 offen offset:512 sc0 sc1\n"
"buffer_load_dwordx4 v[96:99], v1, s[16:19], s24 offen offset:0 sc0 sc1\nbuffer_load_dwordx4 v[100:103], v1, s[16:19], s24 offen offset:1024 sc0 sc1\n"
"buffer_load_dwordx4 v[104:107], v1, s[16:19], s25 offen offset:0 sc0 sc1\nbuffer_load_dwordx4 v[108:111], v1, s[16:19], s25 offen offset:1024 sc0 sc1\n"
"buffer_load_dwordx4 v[112:115], v1, s[16:19], s26 offen offset:0 sc0 sc1\nbuffer_load_dwordx4 v[116:119], v1, s[16:19], s26 offen offset:1024 sc0 sc1\n"
"buffer_load_dwordx4 v[120:123], v1, s[16:19], s27 offen offset:0 sc0 sc1\nbuffer_load_dwordx4 v[124:127], v1, s[16:19], s27 offen offset:1024 sc0 sc1\n"
"buffer_load_dwordx4 v[32:35], v1, s[16:19], s32 offen offset:0 sc0 sc1\nbuffer_load_dwordx4 v[36:39], v1, s[16:19], s32 offen offset:1024 sc0 sc1\n"
"buffer_load_dwordx4 v[40:43], v1, s[16:19], s33 offen offset:0 sc0 sc1\nbuffer_load_dwordx4 v[44:47], v1, s[16:19], s33 offen offset:1024 sc0 sc1\n"
"buffer_load_dwordx4 v[48:51], v1, s[16:19], s34 offen offset:0 sc0 sc1\nbuffer_load_dwordx4 v[52:55], v1, s[16:19], s34 offen offset:1024 sc0 sc1\n"
"buffer_load_dwordx4 v[56:59], v1, s[16:19], s35 offen offset:0 sc0 sc1\nbuffer_load_dwordx4 v[60:63], v1, s[16:19], s35 offen offset:1024 sc0 sc1\nWV 16\nv_xor_b32 v66, s15, v66\n"
"v_xor_b32 v70, s15, v70\nv_xor_b32 v74, s15, v74\nv_xor_b32 v78, s15, v78\nv_xor_b32 v81, s15, v81\nv_xor_b32 v83, s15, v83\nv_or3_b32 v92, v66, v70, v74\n"
"v_or3_b32 v92, v92, v78, v81\nv_or_b32 v92, v92, v83\nv_cmp_ne_u32 vcc, 0, v92\ns_nop 0\ns_cbranch_vccnz .S517\n.S518:\nv_and_b32 v92, 0x7ff, v67\nv_lshrrev_b32 v93, 16, v67\n"
"v_cmp_eq_u32 s[44:45], v92, v90\nv_cmp_eq_u32 s[46:47], v93, v90\nv_cmp_eq_u32 s[48:49], v92, v94\nv_cmp_eq_u32 s[50:51], v93, v94\nv_cndmask_b32 v12, v12, v64, s[44:45]\n"
"v_cndmask_b32 v13, v13, v65, s[44:45]\nv_cndmask_b32 v14, v14, v68, s[44:45]\nv_cndmask_b32 v15, v15, v69, s[44:45]\nv_cndmask_b32 v12, v12, v72, s[46:47]\n"
"v_cndmask_b32 v13, v13, v73, s[46:47]\nv_cndmask_b32 v14, v14, v76, s[46:47]\nv_cndmask_b32 v15, v15, v77, s[46:47]\nv_cndmask_b32 v16, v16, v64, s[48:49]\n"
"v_cndmask_b32 v17, v17, v65, s[48:49]\nv_cndmask_b32 v18, v18, v68, s[48:49]\nv_cndmask_b32 v19, v19, v69, s[48:49]\nv_cndmask_b32 v16, v16, v72, s[50:51]\n"
"v_cndmask_b32 v17, v17, v73, s[50:51]\nv_cndmask_b32 v18, v18, v76, s[50:51]\nv_cndmask_b32 v19, v19, v77, s[50:51]\nv_sub_f32 v92, 1.0, v80\nv_sub_f32 v93, 1.0, v82\nCV 92,92,93\n"
"v_and_b32 v93, 0xffff0000, v92\nv_lshlrev_b32 v92, 16, v92\nv_lshlrev_b32 v64, 16, v12\nv_and_b32 v65, 0xffff0000, v12\nMU 64,64,92\nMU 65,65,92\nv_lshlrev_b32 v66, 16, v13\n"
"v_and_b32 v67, 0xffff0000, v13\nMU 66,66,92\nMU 67,67,92\nv_lshlrev_b32 v68, 16, v14\nv_and_b32 v69, 0xffff0000, v14\nMU 68,68,92\nMU 69,69,92\nv_lshlrev_b32 v70, 16, v15\n"
"v_and_b32 v71, 0xffff0000, v15\nMU 70,70,92\nMU 71,71,92\nv_lshlrev_b32 v72, 16, v16\nv_and_b32 v73, 0xffff0000, v16\nMU 72,72,93\nMU 73,73,93\nv_lshlrev_b32 v74, 16, v17\n"
"v_and_b32 v75, 0xffff0000, v17\nMU 74,74,93\nMU 75,75,93\nv_lshlrev_b32 v76, 16, v18\nv_and_b32 v77, 0xffff0000, v18\nMU 76,76,93\nMU 77,77,93\nv_lshlrev_b32 v78, 16, v19\n"
"v_and_b32 v79, 0xffff0000, v19\nMU 78,78,93\nMU 79,79,93\ns_mov_b32 s7, 262144\nWV 0\n.Z529:\nv_max_u32 v84, v99, v103\nv_cmp_eq_u32 s[44:45], -1, v84\nv_max_u32 v84, v107, v111\n"
"v_cmp_eq_u32 s[46:47], -1, v84\nv_max_u32 v84, v115, v119\nv_cmp_eq_u32 s[48:49], -1, v84\nv_max_u32 v84, v123, v127\nv_cmp_eq_u32 s[50:51], -1, v84\nv_max_u32 v84, v35, v39\n"
"v_cmp_eq_u32 s[52:53], -1, v84\nv_max_u32 v84, v43, v47\nv_cmp_eq_u32 s[54:55], -1, v84\nv_max_u32 v84, v51, v55\nv_cmp_eq_u32 s[56:57], -1, v84\nv_max_u32 v84, v59, v63\n"
"v_cmp_eq_u32 s[58:59], -1, v84\ns_or_b64 s[0:1], s[44:45], s[46:47]\ns_or_b64 s[2:3], s[48:49], s[50:51]\ns_or_b64 s[0:1], s[0:1], s[52:53]\ns_or_b64 s[2:3], s[2:3], s[54:55]\n"
"s_or_b64 s[0:1], s[0:1], s[56:57]\ns_or_b64 s[2:3], s[2:3], s[58:59]\ns_or_b64 s[0:1], s[0:1], s[2:3]\ns_cbranch_scc1 .Z527\n.Z528:\nZvvv 136,6,96\nMvvv 136,6,104\nMvvv 136,6,112\n"
"Mvvv 136,6,120\nMvvv 136,6,32\nMvvv 136,6,40\nMvvv 136,6,48\nMvvv 136,6,56\nZvvv 140,4,96\nMvvv 140,4,104\nMvvv 140,4,112\nMvvv 140,4,120\nMvvv 140,4,32\nMvvv 140,4,40\n"
"Mvvv 140,4,48\nMvvv 140,4,56\nZvvv 144,6,100\nMvvv 144,6,108\nMvvv 144,6,116\n.W598:\ns_cbranch_execz .WB416\nMvvv 144,6,124\nMvvv 144,6,36\nMvvv 144,6,44\nMvvv 144,6,52\n"
"Mvvv 144,6,60\nZvvv 148,4,100\nMvvv 148,4,108\nMvvv 148,4,116\nMvvv 148,4,124\nMvvv 148,4,36\nMvvv 148,4,44\nMvvv 148,4,52\nMvvv 148,4,60\nFM 64,80,136\nFM 65,80,137\n"
"FM 66,80,138\nFM 67,80,139\nFM 68,80,140\nFM 69,80,141\nFM 70,80,142\nFM 71,80,143\nCV 12,64,65\nCV 13,66,67\nCV 14,68,69\nCV 15,70,71\nSv 12,91,36,22,0\nFM 72,82,144\n"
"FM 73,82,145\nFM 74,82,146\nFM 75,82,147\nFM 76,82,148\nFM 77,82,149\nFM 78,82,150\nFM 79,82,151\nCV 16,72,73\nCV 17,74,75\nCV 18,76,77\nCV 19,78,79\nSv 16,91,36,23,0\n.Z526:\n"
"s_endpgm\n.EP538:\n.S412:\ns_mov_b32 s7, 262144\n.S414:\ns_lshl_b32 s5, s73, 8\ns_add_u32 s5, s5, 0xc04ac00\nv_lshrrev_b32 v27, 2, v1\n"
"buffer_load_dword v27, v27, s[16:19], s5 offen sc0 sc1\nWV 0\nv_xor_b32 v27, s15, v27\nv_and_b32 v32, 0xffffdfff, v27\nv_cmp_ne_u32 vcc, 0, v32\ns_nop 0\ns_cbranch_vccz .S413\n"
"s_sleep 6\ns_sub_u32 s7, s7, 1\ns_cbranch_scc0 .S414\ns_branch .S413\n.T421:\ns_mov_b32 s62, 1\ns_call_b64 s[80:81], .RA266\ns_branch .T422\n.T423:\ns_mov_b32 s62, 1\n"
"s_call_b64 s[80:81], .RA266\ns_branch .T424\n.S426:\ns_mov_b32 s7, 262144\n.S428:\nbuffer_load_dword v27, off, s[16:19], s5 sc0 sc1\nWV 0\nv_cmp_ne_u32 vcc, s15, v27\ns_nop 0\n"
"s_cbranch_vccz .S427\ns_sleep 6\ns_sub_u32 s7, s7, 1\ns_cbranch_scc0 .S428\ns_branch .S427\n.S429:\ns_mov_b32 s7, 262144\n.S431:\nv_and_b32 v27, 0x70, v1\n"
"buffer_load_dword v27, v27, s[16:19], s61 offen offset:128 sc0 sc1\nWV 0\nv_cmp_ne_u32 vcc, s15, v27\ns_nop 0\ns_cbranch_vccz .S430\ns_sleep 6\ns_sub_u32 s7, s7, 1\n"
"s_cbranch_scc0 .S431\ns_branch .S430\n.S433:\ns_mov_b32 s7, 262144\n.S435:\nv_and_b32 v27, 0x70, v1\nbuffer_load_dword v27, v27, s[16:19], s61 offen offset:136 sc0 sc1\nWV 0\n"
"v_cmp_ne_u32 vcc, s15, v27\ns_nop 0\ns_cbranch_vccz .S434\ns_sleep 6\ns_sub_u32 s7, s7, 1\ns_cbranch_scc0 .S435\ns_branch .S434\n.S436:\ns_mov_b32 s7, 262144\n.S438:\n"
"v_and_b32 v27, 0x70, v1\nbuffer_load_dword v27, v27, s[16:19], s61 offen offset:140 sc0 sc1\nWV 0\nv_cmp_ne_u32 vcc, s15, v27\ns_nop 0\ns_cbranch_vccz .S437\ns_sleep 6\n"
"s_sub_u32 s7, s7, 1\ns_cbranch_scc0 .S438\ns_branch .S437\n.S439:\ns_mov_b32 s7, 262144\n.S441:\nv_and_b32 v27, 0x70, v1\n"
"buffer_load_dword v27, v27, s[16:19], s61 offen offset:12 sc0 sc1\nWV 0\nv_cmp_ne_u32 vcc, s15, v27\ns_nop 0\ns_cbranch_vccz .S440\ns_sleep 6\ns_sub_u32 s7, s7, 1\n"
"s_cbranch_scc0 .S441\ns_branch .S440\n.S442:\ns_mov_b32 s7, 262144\n.S444:\nv_and_b32 v27, 0x70, v1\nbuffer_load_dword v27, v27, s[16:19], s61 offen offset:8 sc0 sc1\nWV 0\n"
"v_cmp_ne_u32 vcc, s15, v27\ns_nop 0\ns_cbranch_vccz .S443\ns_sleep 6\ns_sub_u32 s7, s7, 1\ns_cbranch_scc0 .S444\ns_branch .S443\n.S445:\ns_mov_b32 s7, 262144\n.S447:\n"
"v_and_b32 v27, 0xf0, v1\nbuffer_load_dword v27, v27, s[16:19], s61 offen offset:4 sc0 sc1\nWV 0\nv_cmp_ne_u32 vcc, s15, v27\ns_nop 0\ns_cbranch_vccz .S446\ns_sleep 6\n"
"s_sub_u32 s7, s7, 1\ns_cbranch_scc0 .S447\ns_branch .S446\n.T456:\ns_mov_b32 s62, 0\ns_call_b64 s[80:81], .RB267\ns_branch .T457\n.T458:\ns_mov_b32 s62, 0\n"
"s_call_b64 s[80:81], .RA266\ns_branch .T459\n.T462:\ns_mov_b32 s62, 0\ns_call_b64 s[80:81], .RB267\ns_branch .T463\n.T464:\ns_mov_b32 s62, 0\ns_call_b64 s[80:81], .RA266\n"
"s_branch .T465\n.T468:\ns_mov_b32 s62, 2\ns_call_b64 s[80:81], .RB267\ns_branch .T469\n.T470:\ns_mov_b32 s62, 5\ns_call_b64 s[80:81], .RA266\ns_branch .T471\n.T472:\n"
"s_mov_b32 s62, 5\ns_call_b64 s[80:81], .RA266\ns_branch .T473\n.T484:\ns_mov_b32 s62, 4\ns_call_b64 s[80:81], .RB267\ns_branch .T485\n.T486:\ns_mov_b32 s62, 4\n"
"s_call_b64 s[80:81], .RA266\ns_branch .T487\n.T490:\ns_mov_b32 s62, 4\ns_call_b64 s[80:81], .RB267\ns_branch .T491\n.T492:\ns_mov_b32 s62, 4\ns_call_b64 s[80:81], .RA266\n"
"s_branch .T493\n.T494:\ns_mov_b32 s62, 6\ns_call_b64 s[80:81], .RB267\ns_branch .T495\n.S496:\ns_mov_b32 s7, 262144\n.S498:\ns_add_u32 s0, s92, 0\n"
"buffer_load_dwordx4 v[208:211], v1, s[16:19], s0 offen offset:0 sc0 sc1\nbuffer_load_dwordx4 v[212:215], v1, s[16:19], s0 offen offset:1024 sc0 sc1\nWV 0\n"
"v_max_u32 v21, v211, v215\nv_cmp_eq_u32 vcc, -1, v21\ns_nop 0\ns_cbranch_vccz .S497\ns_sleep 6\ns_sub_u32 s7, s7, 1\ns_cbranch_scc0 .S498\ns_branch .S497\n.S499:\n"
"s_mov_b32 s7, 262144\n.S501:\ns_add_u32 s0, s92, 131072\nbuffer_load_dwordx4 v[216:219], v1, s[16:19], s0 offen offset:0 sc0 sc1\n"
"buffer_load_dwordx4 v[220:223], v1, s[16:19], s0 offen offset:1024 sc0 sc1\nWV 0\nv_max_u32 v21, v219, v223\nv_cmp_eq_u32 vcc, -1, v21\ns_nop 0\ns_cbranch_vccz .S500\ns_sleep 6\n"
"s_sub_u32 s7, s7, 1\ns_cbranch_scc0 .S501\ns_branch .S500\n.S502:\ns_mov_b32 s7, 262144\n.S504:\ns_add_u32 s0, s92, 262144\n"
"buffer_load_dwordx4 v[224:227], v1, s[16:19], s0 offen offset:0 sc0 sc1\nbuffer_load_dwordx4 v[228:231], v1, s[16:19], s0 offen offset:1024 sc0 sc1\nWV 0\n"
"v_max_u32 v21, v227, v231\nv_cmp_eq_u32 vcc, -1, v21\ns_nop 0\ns_cbranch_vccz .S503\ns_sleep 6\ns_sub_u32 s7, s7, 1\ns_cbranch_scc0 .S504\ns_branch .S503\n.S505:\n"
"s_mov_b32 s7, 262144\n.S507:\ns_add_u32 s0, s92, 393216\nbuffer_load_dwordx4 v[208:211], v1, s[16:19], s0 offen offset:0 sc0 sc1\n"
"buffer_load_dwordx4 v[212:215], v1, s[16:19], s0 offen offset:1024 sc0 sc1\nWV 0\nv_max_u32 v21, v211, v215\nv_cmp_eq_u32 vcc, -1, v21\ns_nop 0\ns_cbranch_vccz .S506\ns_sleep 6\n"
"s_sub_u32 s7, s7, 1\ns_cbranch_scc0 .S507\ns_branch .S506\n.S508:\ns_mov_b32 s7, 262144\n.S510:\ns_add_u32 s0, s92, 524288\n"
"buffer_load_dwordx4 v[216:219], v1, s[16:19], s0 offen offset:0 sc0 sc1\nbuffer_load_dwordx4 v[220:223], v1, s[16:19], s0 offen offset:1024 sc0 sc1\nWV 0\n"
"v_max_u32 v21, v219, v223\nv_cmp_eq_u32 vcc, -1, v21\ns_nop 0\ns_cbranch_vccz .S509\ns_sleep 6\ns_sub_u32 s7, s7, 1\ns_cbranch_scc0 .S510\ns_branch .S509\n.S511:\n"
"s_mov_b32 s7, 262144\n.S513:\ns_add_u32 s0, s92, 655360\nbuffer_load_dwordx4 v[224:227], v1, s[16:19], s0 offen offset:0 sc0 sc1\n"
"buffer_load_dwordx4 v[228:231], v1, s[16:19], s0 offen offset:1024 sc0 sc1\nWV 0\nv_max_u32 v21, v227, v231\nv_cmp_eq_u32 vcc, -1, v21\ns_nop 0\ns_cbranch_vccz .S512\ns_sleep 6\n"
"s_sub_u32 s7, s7, 1\ns_cbranch_scc0 .S513\ns_branch .S512\n.S514:\ns_mov_b32 s7, 262144\n.S516:\ns_add_u32 s0, s92, 786432\n"
"buffer_load_dwordx4 v[208:211], v1, s[16:19], s0 offen offset:0 sc0 sc1\nbuffer_load_dwordx4 v[212:215], v1, s[16:19], s0 offen offset:1024 sc0 sc1\nWV 0\n"
"v_max_u32 v21, v211, v215\nv_cmp_eq_u32 vcc, -1, v21\ns_nop 0\ns_cbranch_vccz .S515\ns_sleep 6\ns_sub_u32 s7, s7, 1\ns_cbranch_scc0 .S516\ns_branch .S515\n.S517:\n"
"s_mov_b32 s7, 262144\n.S519:\nbuffer_load_dwordx4 v[64:67], v89, s[16:19], s40 offen offset:0 sc0 sc1\nbuffer_load_dwordx4 v[68:71], v89, s[16:19], s40 offen offset:16 sc0 sc1\n"
"buffer_load_dwordx4 v[72:75], v89, s[16:19], s40 offen offset:1792 sc0 sc1\nbuffer_load_dwordx4 v[76:79], v89, s[16:19], s40 offen offset:1808 sc0 sc1\n"
"buffer_load_dwordx2 v[80:81], v88, s[16:19], s63 offen offset:0 sc0 sc1\nbuffer_load_dwordx2 v[82:83], v88, s[16:19], s63 offen offset:512 sc0 sc1\nWV 0\nv_xor_b32 v66, s15, v66\n"
"v_xor_b32 v70, s15, v70\nv_xor_b32 v74, s15, v74\nv_xor_b32 v78, s15, v78\nv_xor_b32 v81, s15, v81\nv_xor_b32 v83, s15, v83\nv_or3_b32 v92, v66, v70, v74\n"
"v_or3_b32 v92, v92, v78, v81\nv_or_b32 v92, v92, v83\nv_cmp_ne_u32 vcc, 0, v92\ns_nop 0\ns_cbranch_vccz .S518\ns_sleep 6\ns_sub_u32 s7, s7, 1\ns_cbranch_scc0 .S519\n"
"s_branch .S518\n.S520:\ns_mov_b32 s7, 262144\n.S522:\nbuffer_load_dword v27, off, s[16:19], s60 offset:512 sc0 sc1\nWV 0\nv_cmp_ne_u32 vcc, s15, v27\ns_nop 0\n"
"s_cbranch_vccz .S521\ns_sleep 6\ns_sub_u32 s7, s7, 1\ns_cbranch_scc0 .S522\ns_branch .S521\n.S523:\ns_mov_b32 s7, 262144\n.S525:\n"
"buffer_load_dword v27, v24, s[16:19], s41 offen sc0 sc1\nWV 0\nv_xor_b32 v27, s15, v27\nv_and_b32 v26, 0xffffdfff, v27\nv_cmp_ne_u32 s[64:65], 0, v27\nv_cmp_ne_u32 vcc, 0, v26\n"
"s_nop 0\ns_cbranch_vccz .S524\ns_sleep 6\ns_sub_u32 s7, s7, 1\ns_cbranch_scc0 .S525\ns_branch .S524\n.Z527:\ns_sub_u32 s7, s7, 1\ns_cbranch_scc1 .Z528\ns_cmp_lg_u64 s[44:45], 0\n"
"s_cbranch_scc0 .Z530\nbuffer_load_dwordx4 v[96:99], v1, s[16:19], s24 offen offset:0 sc0 sc1\nbuffer_load_dwordx4 v[100:103], v1, s[16:19], s24 offen offset:1024 sc0 sc1\n.Z530:\n"
"s_cmp_lg_u64 s[46:47], 0\ns_cbranch_scc0 .Z531\nbuffer_load_dwordx4 v[104:107], v1, s[16:19], s25 offen offset:0 sc0 sc1\n"
"buffer_load_dwordx4 v[108:111], v1, s[16:19], s25 offen offset:1024 sc0 sc1\n.Z531:\ns_cmp_lg_u64 s[48:49], 0\ns_cbranch_scc0 .Z532\n"
"buffer_load_dwordx4 v[112:115], v1, s[16:19], s26 offen offset:0 sc0 sc1\nbuffer_load_dwordx4 v[116:119], v1, s[16:19], s26 offen offset:1024 sc0 sc1\n.Z532:\n"
"s_cmp_lg_u64 s[50:51], 0\ns_cbranch_scc0 .Z533\nbuffer_load_dwordx4 v[120:123], v1, s[16:19], s27 offen offset:0 sc0 sc1\n"
"buffer_load_dwordx4 v[124:127], v1, s[16:19], s27 offen offset:1024 sc0 sc1\n.Z533:\ns_cmp_lg_u64 s[52:53], 0\ns_cbranch_scc0 .Z534\n"
"buffer_load_dwordx4 v[32:35], v1, s[16:19], s32 offen offset:0 sc0 sc1\nbuffer_load_dwordx4 v[36:39], v1, s[16:19], s32 offen offset:1024 sc0 sc1\n.Z534:\n"
"s_cmp_lg_u64 s[54:55], 0\ns_cbranch_scc0 .Z535\nbuffer_load_dwordx4 v[40:43], v1, s[16:19], s33 offen offset:0 sc0 sc1\n"
"buffer_load_dwordx4 v[44:47], v1, s[16:19], s33 offen offset:1024 sc0 sc1\n.Z535:\ns_cmp_lg_u64 s[56:57], 0\ns_cbranch_scc0 .Z536\n"
"buffer_load_dwordx4 v[48:51], v1, s[16:19], s34 offen offset:0 sc0 sc1\nbuffer_load_dwordx4 v[52:55], v1, s[16:19], s34 offen offset:1024 sc0 sc1\n.Z536:\n"
"s_cmp_lg_u64 s[58:59], 0\ns_cbranch_scc0 .Z537\nbuffer_load_dwordx4 v[56:59], v1, s[16:19], s35 offen offset:0 sc0 sc1\n"
"buffer_load_dwordx4 v[60:63], v1, s[16:19], s35 offen offset:1024 sc0 sc1\n.Z537:\ns_sleep 8\nWV 0\ns_branch .Z529\n.Lcode1:\n.fill 2304, 4, 0xbf810000\n"
  :: "s"(__builtin_amdgcn_kernarg_segment_ptr()), "s"(blockIdx.x), "v"(threadIdx.x)
  : "memory", "scc", "vcc", "v1", "v2", "v3", "v4", "v5", "v6", "v7", "v8", "v9", "v10", "v11", "v12", "v13", "v14", "v15", "v16", "v17", "v18", "v19", "v20", "v21", "v22", "v23", "v24", "v25", "v26", "v27", "v28", "v29", "v30", "v31", "v32", "v33", "v34", "v35", "v36", "v37", "v38", "v39", "v40", "v41", "v42", "v43", "v44", "v45", "v46", "v47", "v48", "v49", "v50", "v51", "v52", "v53", "v54", "v55", "v56", "v57", "v58", "v59", "v60", "v61", "v62", "v63", "v64", "v65", "v66", "v67", "v68", "v69", "v70", "v71", "v72", "v73", "v74", "v75", "v76", "v77", "v78", "v79", "v80", "v81", "v82", "v83", "v84", "v85", "v86", "v87", "v88", "v89", "v90", "v91", "v92", "v93", "v94", "v95", "v96", "v97", "v98", "v99", "v100", "v101", "v102", "v103", "v104", "v105", "v106", "v107", "v108", "v109", "v110", "v111", "v112", "v113", "v114", "v115", "v116", "v117", "v118", "v119", "v120", "v121", "v122", "v123", "v124", "v125", "v126", "v127", "v128", "v129", "v130", "v131", "v132", "v133", "v134", "v135", "v136", "v137", "v138", "v139", "v140", "v141", "v142", "v143", "v144", "v145", "v146", "v147", "v148", "v149", "v150", "v151", "v152", "v153", "v154", "v155", "v156", "v157", "v158", "v159", "v160", "v161", "v162", "v163", "v164", "v165", "v166", "v167", "v168", "v169", "v170", "v171", "v172", "v173", "v174", "v175", "v176", "v177", "v178", "v179", "v180", "v181", "v182", "v183", "v184", "v185", "v186", "v187", "v188", "v189", "v190", "v191", "v192", "v193", "v194", "v195", "v196", "v197", "v198", "v199", "v200", "v201", "v202", "v203", "v204", "v205", "v206", "v207", "v208", "v209", "v210", "v211", "v212", "v213", "v214", "v215", "v216", "v217", "v218", "v219", "v220", "v221", "v222", "v223", "v224", "v225", "v226", "v227", "v228", "v229", "v230", "v231", "v232", "v233", "v234", "v235", "v236", "v237", "v238", "v239", "v240", "v241", "v242", "v243", "v244", "v245", "v246", "v247", "v248", "v249", "v250", "v251", "v252", "v253", "v254", "v255", "a0", "a1", "a2", "a3", "a4", "a5", "a6", "a7", "a8", "a9", "a10", "a11", "a12", "a13", "a14", "a15", "a16", "a17", "a18", "a19", "a20", "a21", "a22", "a23", "a24", "a25", "a26", "a27", "a28", "a29", "a30", "a31", "a32", "a33", "a34", "a35", "a36", "a37", "a38", "a39", "a40", "a41", "a42", "a43", "a44", "a45", "a46", "a47", "a48", "a49", "a50", "a51", "a52", "a53", "a54", "a55", "a56", "a57", "a58", "a59", "a60", "a61", "a62", "a63", "a64", "a65", "a66", "a67", "a68", "a69", "a70", "a71", "a72", "a73", "a74", "a75", "a76", "a77", "a78", "a79", "a80", "a81", "a82", "a83", "a84", "a85", "a86", "a87", "a88", "a89", "a90", "a91", "a92", "a93", "a94", "a95", "a96", "a97", "a98", "a99", "a100", "a101", "a102", "a103", "a104", "a105", "a106", "a107", "a108", "a109", "a110", "a111", "a112", "a113", "a114", "a115", "a116", "a117", "a118", "a119", "a120", "a121", "a122", "a123", "a124", "a125", "a126", "a127", "a128", "a129", "a130", "a131", "a132", "a133", "a134", "a135", "a136", "a137", "a138", "a139", "a140", "a141", "a142", "a143", "a144", "a145", "a146", "a147", "a148", "a149", "a150", "a151", "a152", "a153", "a154", "a155", "a156", "a157", "a158", "a159", "a160", "a161", "a162", "a163", "a164", "a165", "a166", "a167", "a168", "a169", "a170", "a171", "a172", "a173", "a174", "a175", "a176", "a177", "a178", "a179", "a180", "a181", "a182", "a183", "a184", "a185", "a186", "a187", "a188", "a189", "a190", "a191", "a192", "a193", "a194", "a195", "a196", "a197", "a198", "a199", "a200", "a201", "a202", "a203", "a204", "a205", "a206", "a207", "a208", "a209", "a210", "a211", "a212", "a213", "a214", "a215", "a216", "a217", "a218", "a219", "a220", "a221", "a222", "a223", "a224", "a225", "a226", "a227", "a228", "a229", "a230", "a231", "a232", "a233", "a234", "a235", "a236", "a237", "a238", "a239", "a240", "a241", "a242", "a243", "a244", "a245", "a246", "a247", "a248", "a249", "a250", "a251", "a252", "a253", "a254", "a255", "s8", "s9", "s10", "s11", "s12", "s13", "s14", "s15", "s16", "s17", "s18", "s19", "s20", "s21", "s22", "s23", "s24", "s25", "s26", "s27", "s28", "s29", "s30", "s31", "s33", "s34", "s35", "s36", "s37", "s38", "s39", "s40", "s41", "s42", "s43", "s44", "s45", "s46", "s47", "s48", "s49", "s50", "s51", "s52", "s53", "s54", "s55", "s56", "s57", "s58", "s59", "s60", "s61", "s62", "s63", "s64", "s65", "s66", "s67", "s68", "s69", "s70", "s71", "s72", "s73", "s74", "s75", "s76", "s77", "s78", "s79", "s80", "s81", "s82", "s83", "s84", "s85", "s86", "s87", "s88", "s89", "s90", "s91", "s92", "s93", "s94", "s95", "s96", "s97", "s98", "s99");
#endif
}

extern "C" void kernel_launch(void* const* d_in, const int* in_sizes, int n_in,
                              void* d_out, int out_size, void* d_ws, size_t ws_size,
                              hipStream_t stream) {
  (void)hipFuncSetAttribute((const void*)k_main, hipFuncAttributeMaxDynamicSharedMemorySize, 163840);
  k_main<<<256, 256, 163840, stream>>>(d_in[0], d_in[1], d_in[2], d_in[3], d_in[4], d_ws, d_in[11], d_in[7], d_in[5], d_in[8], d_in[9], d_in[10], d_out);
}
```

```cpp
#include <hip/hip_runtime.h>
#include <stdint.h>

extern "C" __global__ __launch_bounds__(256, 1)
void k_main(const void* x, const void* ckv, const void* wq, const void* wuk, const void* wuv, void* ws,
            const void* gate_w, const void* w_r, const void* wo, const void* wg, const void* wu, const void* wd, void* out) {
#if defined(__HIP_DEVICE_COMPILE__)
  asm volatile(
"s_mov_b64 s[8:9], %0\ns_mov_b32 s10, %1\nv_mov_b32 v0, %2\n.macro WL n\ns_waitcnt lgkmcnt(\\n)\n.endm\n.macro D0 x,r,s,o\n"
"buffer_load_dwordx4 v\\x, s[\\r:\\r+3], s\\s offen offset:\\o lds\n.endm\n.macro WV n\ns_waitcnt vmcnt(\\n)\n.endm\n.macro Ra d,x,o\nds_read_b128 a[\\d:\\d+3], v\\x offset:\\o\n"
".endm\n.macro Ta d,x,o\nds_read_b64_tr_b16 a[\\d:\\d+1], v\\x offset:\\o\n.endm\n.macro Zvaa d,x,y\nv_mfma_f32_16x16x32_bf16 v[\\d:\\d+3], a[\\x:\\x+3], a[\\y:\\y+3], 0\n.endm\n"
".macro Mvaa d,x,y\nv_mfma_f32_16x16x32_bf16 v[\\d:\\d+3], a[\\x:\\x+3], a[\\y:\\y+3], v[\\d:\\d+3]\n.endm\n.macro Zaaa d,x,y\n"
"v_mfma_f32_16x16x32_bf16 a[\\d:\\d+3], a[\\x:\\x+3], a[\\y:\\y+3], 0\n.endm\n.macro Maaa d,x,y\nv_mfma_f32_16x16x32_bf16 a[\\d:\\d+3], a[\\x:\\x+3], a[\\y:\\y+3], a[\\d:\\d+3]\n"
".endm\n.macro MU d,x,y\nv_mul_f32 v\\d, v\\x, v\\y\n.endm\n.macro CV d,x,y\nv_cvt_pk_bf16_f32 v\\d, v\\x, v\\y\n.endm\n.macro FM d,x,y\nv_fmac_f32 v\\d, v\\x, v\\y\n.endm\n"
".macro AD d,x,y\nv_add_f32 v\\d, v\\x, v\\y\n.endm\n.macro EX d,x\nv_exp_f32 v\\d, v\\x\n.endm\n.macro AR d,x\nv_accvgpr_read_b32 v\\d, a\\x\n.endm\n.macro Sv d,x,r,s,o\n"
"buffer_store_dwordx4 v[\\d:\\d+3], v\\x, s[\\r:\\r+3], s\\s offen offset:\\o sc0 sc1\n.endm\n.macro Rv d,x,o\nds_read_b128 v[\\d:\\d+3], v\\x offset:\\o\n.endm\n.macro D1 x,r,s,o\n"
"buffer_load_dwordx4 v\\x, s[\\r:\\r+3], s\\s offen offset:\\o sc1 lds\n.endm\n.macro Cvav d,x,y,c\nv_mfma_f32_16x16x32_bf16 v[\\d:\\d+3], a[\\x:\\x+3], v[\\y:\\y+3], v[\\c:\\c+3]\n"
".endm\n.macro Mvav d,x,y\nv_mfma_f32_16x16x32_bf16 v[\\d:\\d+3], a[\\x:\\x+3], v[\\y:\\y+3], v[\\d:\\d+3]\n.endm\n.macro Zaav d,x,y\n"
"v_mfma_f32_16x16x32_bf16 a[\\d:\\d+3], a[\\x:\\x+3], v[\\y:\\y+3], 0\n.endm\n.macro Maav d,x,y\nv_mfma_f32_16x16x32_bf16 a[\\d:\\d+3], a[\\x:\\x+3], v[\\y:\\y+3], a[\\d:\\d+3]\n"
".endm\n.macro SU d,x,y\nv_sub_f32 v\\d, v\\x, v\\y\n.endm\n.macro Mava d,x,y\nv_mfma_f32_16x16x32_bf16 a[\\d:\\d+3], v[\\x:\\x+3], a[\\y:\\y+3], a[\\d:\\d+3]\n.endm\n"
".macro Zvav d,x,y\nv_mfma_f32_16x16x32_bf16 v[\\d:\\d+3], a[\\x:\\x+3], v[\\y:\\y+3], 0\n.endm\n.macro Zvvv d,x,y\n"
"v_mfma_f32_16x16x32_bf16 v[\\d:\\d+3], v[\\x:\\x+3], v[\\y:\\y+3], 0\n.endm\n.macro Mvvv d,x,y\nv_mfma_f32_16x16x32_bf16 v[\\d:\\d+3], v[\\x:\\x+3], v[\\y:\\y+3], v[\\d:\\d+3]\n"
".endm\n.macro AW d,x\nv_accvgpr_write_b32 a\\d, v\\x\n.endm\n.macro Tv d,x,o\nds_read_b64_tr_b16 v[\\d:\\d+1], v\\x offset:\\o\n.endm\n.macro Sa d,x,r,s,o\n"
"buffer_store_dwordx4 a[\\d:\\d+3], v\\x, s[\\r:\\r+3], s\\s offen offset:\\o sc0 sc1\n.endm\n.macro Cvaa d,x,y,c\n"
"v_mfma_f32_16x16x32_bf16 v[\\d:\\d+3], a[\\x:\\x+3], a[\\y:\\y+3], v[\\c:\\c+3]\n.endm\n.Lcode0:\ns_mov_b32 s70, 0\ns_load_dwordx16 s[44:59], s[8:9], 0x0\n"
"s_load_dwordx8 s[60:67], s[8:9], 0x40\ns_load_dwordx2 s[68:69], s[8:9], 0x60\nv_lshrrev_b32 v2, 6, v0\nv_and_b32 v0, 63, v0\nv_readfirstlane_b32 s11, v2\nv_lshlrev_b32 v1, 4, v0\n"
"s_and_b32 s2, s10, 0x17\ns_bfe_u32 s3, s10, 0x10003\ns_lshl_b32 s3, s3, 7\ns_or_b32 s2, s2, s3\ns_bfe_u32 s3, s10, 0x10005\ns_lshl3_add_u32 s2, s3, s2\ns_lshr_b32 s3, s10, 6\n"
"s_lshl_b32 s3, s3, 5\ns_or_b32 s10, s2, s3\ns_and_b32 s73, s10, 7\ns_lshr_b32 s74, s10, 3\ns_and_b32 s2, s74, 1\ns_lshl1_add_u32 s12, s73, s2\ns_bfe_u32 s13, s74, 0x30001\n"
"s_lshr_b32 s14, s74, 4\ns_xor_b32 s14, s14, 1\nv_lshrrev_b32 v32, 5, v0\ns_and_b32 s3, s11, 1\ns_lshl_b32 s2, s3, 3\nv_lshl_or_b32 v33, v32, 1, s2\nv_and_b32 v34, 31, v0\n"
"v_xor_b32 v33, v33, v34\nv_lshlrev_b32 v33, 4, v33\nv_lshl_or_b32 v6, v32, 13, v33\nv_xor_b32 v33, 64, v6\nv_add_u32 v7, 15360, v33\nv_add_u32 v8, 30720, v6\n"
"v_add_u32 v9, 46080, v33\ns_lshl_b32 s2, s11, 16\ns_lshl_b32 s4, s12, 9\ns_add_u32 s5, s2, s4\ns_getpc_b64 s[40:41]\n.PC8:\ns_sub_u32 s40, s40, .PC8-.Lcode0\n"
"s_subb_u32 s41, s41, 0\ns_mov_b32 s42, .Lcode1-.Lcode0\ns_mov_b32 s43, 0x20000\ns_lshl2_add_u32 s6, s74, s11\ns_lshl_b32 s6, s6, 7\nv_lshlrev_b32 v54, 10, v1\n"
"buffer_load_dword v31, v54, s[40:43], s6 offen\nv_lshrrev_b32 v32, 4, v0\nv_and_b32 v33, 7, v0\nv_xor_b32 v33, v33, v32\nv_lshlrev_b32 v33, 4, v33\nv_lshrrev_b32 v32, 3, v0\n"
"v_lshl_or_b32 v2, v32, 11, v33\nv_xor_b32 v33, 64, v2\nv_add_u32 v3, 15360, v33\nv_add_u32 v4, 30720, v2\nv_add_u32 v5, 46080, v33\nv_lshrrev_b32 v144, 4, v0\n"
"v_and_b32 v145, 7, v0\nv_xor_b32 v145, v145, v144\nv_lshlrev_b32 v145, 4, v145\nv_lshrrev_b32 v144, 3, v0\nv_mul_u32_u24 v144, 896, v144\nv_add_u32 v136, v144, v145\n"
"v_xor_b32 v145, 64, v136\nv_add_u32 v137, 6144, v145\nv_add_u32 v138, 55296, v136\nv_add_u32 v139, 61440, v145\ns_lshr_b32 s88, s11, 1\ns_lshr_b32 s2, s13, 2\n"
"s_bfe_u32 s3, s13, 0x10001\ns_mul_i32 s3, s3, 17\ns_add_u32 s2, s2, s3\ns_lshl2_add_u32 s3, s14, s88\ns_lshl_b32 s3, s3, 2\ns_lshr_b32 s3, 0xe420ca86, s3\ns_and_b32 s3, s3, 15\n"
"s_add_u32 s2, s2, s3\ns_cmp_eq_u32 s2, 32\ns_cselect_b32 s86, 16, s2\ns_add_u32 s88, s88, 2\ns_lshr_b32 s2, s13, 2\ns_bfe_u32 s3, s13, 0x10001\ns_mul_i32 s3, s3, 17\n"
"s_add_u32 s2, s2, s3\ns_lshl2_add_u32 s3, s14, s88\ns_lshl_b32 s3, s3, 2\ns_lshr_b32 s3, 0xe420ca86, s3\ns_and_b32 s3, s3, 15\ns_add_u32 s2, s2, s3\ns_cmp_eq_u32 s2, 32\n"
"s_cselect_b32 s88, 16, s2\ns_and_b32 s89, s11, 1\ns_lshl1_add_u32 s86, s86, s89\ns_lshl1_add_u32 s88, s88, s89\ns_sub_u32 s2, 15, s13\ns_lshl3_add_u32 s2, s2, s11\n"
"s_mul_i32 s87, s2, 14336\ns_mov_b32 s18, 0xc0ae800\ns_mov_b32 s30, 0x1c0000\ns_mov_b32 s34, 0x380000\ns_mov_b32 s22, 0x400000\ns_mov_b32 s26, 0x800000\ns_mov_b32 s19, 0x20000\n"
"s_mov_b32 s31, 0x20000\ns_mov_b32 s35, 0x20000\ns_mov_b32 s23, 0x20000\ns_mov_b32 s27, 0x20000\nWL 0\ns_mov_b32 s28, s44\ns_and_b32 s29, s45, 0xffff\ns_mov_b32 s32, s48\n"
"s_and_b32 s33, s49, 0xffff\ns_mov_b32 s20, s46\ns_and_b32 s21, s47, 0xffff\ns_bitcmp1_b32 s13, 0\ns_cselect_b64 s[50:51], s[52:53], s[50:51]\ns_mov_b32 s24, s50\n"
"s_and_b32 s25, s51, 0xffff\ns_sub_u32 s44, s5, 0x40000\ns_mov_b32 s51, s5\ns_lshl_b32 s47, s11, 12\ns_add_u32 s47, s47, 0x10000\ns_lshl_b32 s45, s86, 16\ns_sub_u32 s45, s45, 128\n"
"s_lshl_b32 s48, s88, 16\ns_sub_u32 s48, s48, 128\ns_mov_b32 s52, s87\ns_cmp_eq_u32 s14, 1\ns_cbranch_scc1 .B6\ns_add_u32 m0, s47, 65536\ns_add_u32 s46, s51, 0\nD0 6,32,46,0\n"
"D0 7,32,46,1024\nD0 8,32,46,2048\nD0 9,32,46,3072\ns_add_u32 m0, s47, -65536\ns_add_u32 s46, s52, 0\nD0 136,28,46,0\nD0 137,28,46,1024\nD0 138,28,46,2048\nD0 139,28,46,3072\n"
"s_add_u32 m0, s47, 81920\ns_add_u32 s46, s51, 262144\nD0 6,32,46,0\nD0 7,32,46,1024\nD0 8,32,46,2048\nD0 9,32,46,3072\ns_branch .B7\n.B6:\ns_add_u32 m0, s47, 0\n"
"s_add_u32 s44, s44, 0x40000\nD0 6,24,44,0\nD0 7,24,44,1024\nD0 8,24,44,2048\nD0 9,24,44,3072\ns_add_u32 m0, s47, -65536\ns_add_u32 s45, s45, 128\nD0 2,20,45,0\nD0 3,20,45,1024\n"
"D0 4,20,45,2048\nD0 5,20,45,3072\ns_add_u32 m0, s47, 16384\ns_add_u32 s44, s44, 0x40000\nD0 6,24,44,0\nD0 7,24,44,1024\nD0 8,24,44,2048\nD0 9,24,44,3072\n.B7:\n"
"s_add_u32 s54, s54, 0x1fffff\ns_addc_u32 s55, s55, 0\ns_and_b32 s54, s54, 0xffe00000\ns_mov_b32 s16, s54\ns_and_b32 s17, s55, 0xffff\ns_mov_b32 s2, 0xc048000\n"
"s_load_dword s15, s[54:55], s2\nv_lshrrev_b32 v24, 3, v0\nv_lshlrev_b32 v24, 13, v24\nv_and_b32 v26, 7, v0\nv_lshl_or_b32 v24, v26, 7, v24\nv_lshrrev_b32 v26, 4, v0\n"
"v_lshl_or_b32 v24, v26, 28, v24\nv_lshrrev_b32 v26, 5, v0\nv_lshlrev_b32 v26, 30, v26\nv_lshl_or_b32 v26, v0, 7, v26\ns_and_b32 s2, s13, 6\ns_and_b32 s3, s12, 1\n"
"s_add_u32 s2, s2, s3\ns_lshl2_add_u32 s2, s2, s11\ns_lshl_b32 s2, s2, 14\ns_lshl_b32 s3, s73, 10\ns_add_u32 s2, s2, s3\ns_add_u32 s96, s2, 1048576\ns_and_b32 s2, s10, 127\n"
"s_lshl2_add_u32 s2, s2, s11\ns_lshl_b32 s97, s2, 12\nv_and_b32 v48, 15, v0\nv_lshrrev_b32 v49, 4, v0\nv_lshrrev_b32 v50, 1, v48\nv_xor_b32 v50, v50, v49\n"
"v_lshlrev_b32 v50, 4, v50\nv_lshl_or_b32 v50, v48, 7, v50\ns_lshr_b32 s2, s11, 1\ns_lshl_b32 s2, s2, 12\nv_add_u32 v10, s2, v50\nv_xor_b32 v11, 64, v10\nv_lshrrev_b32 v50, 2, v48\n"
"v_lshl_add_u32 v51, v49, 3, v50\nv_and_b32 v52, 1, v49\nv_lshl_or_b32 v50, v52, 2, v50\nv_lshlrev_b32 v50, 5, v50\nv_lshl_or_b32 v50, v51, 9, v50\nv_and_b32 v52, 3, v0\n"
"v_lshl_or_b32 v50, v52, 3, v50\ns_and_b32 s2, s11, 1\ns_lshl_b32 s2, s2, 8\ns_add_u32 s2, s2, 0x10000\nv_add_u32 v12, s2, v50\nv_xor_b32 v13, 32, v12\nv_xor_b32 v14, 64, v12\n"
"v_xor_b32 v15, 96, v12\nv_xor_b32 v16, 128, v12\nv_xor_b32 v17, 160, v12\nv_xor_b32 v18, 192, v12\nv_xor_b32 v19, 224, v12\ns_getpc_b64 s[0:1]\n.PC9:\ns_mov_b32 s2, .PA2-.PC9\n"
"s_mov_b32 s3, .PA4-.PC9\ns_bitcmp1_b32 s13, 0\ns_cselect_b32 s2, .PA3-.PC9, s2\ns_cselect_b32 s3, .PA5-.PC9, s3\ns_cmp_eq_u32 s14, 0\ns_cselect_b32 s2, s2, s3\n"
"s_add_u32 s0, s0, s2\ns_addc_u32 s1, s1, 0\ns_setpc_b64 s[0:1]\n.p2align 12\n.PA2:\ns_mov_b64 exec, 0\ns_cmp_eq_u32 s11, 1\ns_cbranch_scc1 .W540\ns_cmp_eq_u32 s11, 2\n"
"s_cbranch_scc1 .W541\ns_cmp_eq_u32 s11, 3\ns_cbranch_scc1 .W542\ns_branch .W539\n.WB21:\ns_mov_b64 exec, -1\nv_add_u32 v128, 0x10000, v12\nv_add_u32 v129, 0x10000, v13\n"
"v_add_u32 v130, 0x10000, v14\nv_add_u32 v131, 0x10000, v15\nv_add_u32 v132, 0x10000, v16\nv_add_u32 v133, 0x10000, v17\nv_add_u32 v134, 0x10000, v18\nv_add_u32 v135, 0x10000, v19\n"
"s_add_u32 s2, s11, 1\ns_lshr_b32 s2, s2, 1\ns_lshl_b32 s2, s2, 12\nv_add_u32 v140, s2, v10\nv_add_u32 v141, s2, v11\nv_xor_b32 v142, 0x1000, v140\nv_xor_b32 v143, 0x1000, v141\n"
"s_add_u32 m0, s47, 0\ns_add_u32 s46, s51, 524288\nD0 6,32,46,0\nD0 7,32,46,1024\nD0 8,32,46,2048\nD0 9,32,46,3072\ns_add_u32 m0, s47, -49152\ns_add_u32 s46, s52, 128\n"
"D0 136,28,46,0\nD0 137,28,46,1024\nD0 138,28,46,2048\nD0 139,28,46,3072\ns_add_u32 m0, s47, 16384\ns_add_u32 s46, s51, 786432\nD0 6,32,46,0\nD0 7,32,46,1024\nD0 8,32,46,2048\n"
"D0 9,32,46,3072\nv_mov_b32 v116, 0x40000000\nv_cmp_gt_u32 vcc, 56, v0\ns_lshl2_add_u32 s2, s10, s11\ns_mul_i32 s2, s2, 1792\nv_cndmask_b32 v117, v116, v1, vcc\ns_mov_b32 s36, s56\n"
"s_and_b32 s37, s57, 0xffff\ns_mov_b32 s38, 0x380\ns_mov_b32 s39, 0x20000\ns_mov_b32 s40, s58\ns_and_b32 s41, s59, 0xffff\ns_mov_b32 s42, 0x380\ns_mov_b32 s43, 0x20000\n"
"buffer_load_dwordx4 v[96:99], v117, s[28:31], s2 offen offset:0\nbuffer_load_dwordx4 v[100:103], v117, s[28:31], s2 offen offset:896\n"
"buffer_load_dwordx4 v[104:107], v117, s[36:39], 0 offen offset:0\nbuffer_load_dwordx4 v[108:111], v117, s[40:43], 0 offen offset:0\ns_add_u32 m0, s47, 32768\n"
"s_add_u32 s46, s51, 1048576\nD0 6,32,46,0\nD0 7,32,46,1024\nD0 8,32,46,2048\nD0 9,32,46,3072\ns_add_u32 m0, s47, -32768\ns_add_u32 s46, s52, 256\nD0 136,28,46,0\n"
"D0 137,28,46,1024\nD0 138,28,46,2048\nD0 139,28,46,3072\nWL 0\ns_mul_i32 s15, s15, 1664525\ns_add_u32 s15, s15, 1013904223\nv_mov_b32 v25, s15\ns_mov_b32 s57, 0x3db8aa3b\n"
"s_and_b32 s59, s11, 1\ns_lshl_b32 s59, s59, 13\ns_mul_i32 s2, s73, 25165824\ns_and_b32 s3, s12, 1\ns_lshl_b32 s3, s3, 21\ns_add_u32 s2, s2, s3\ns_lshl2_add_u32 s3, s13, s11\n"
"s_lshl_b32 s3, s3, 15\ns_add_u32 s2, s2, s3\ns_add_u32 s56, s2, 0x800000\ns_xor_b32 s53, s56, 0xc000\nWV 24\ns_barrier\nRa 192,140,0\nRa 196,141,0\nRa 200,140,2048\n"
"Ra 204,141,2048\nRa 208,142,0\nRa 212,143,0\nRa 216,142,2048\nRa 220,143,2048\nTa 160,128,0\nTa 162,128,2048\nTa 164,128,16384\nTa 166,128,18432\nTa 168,129,0\nTa 170,129,2048\n"
"Ta 172,129,16384\nTa 174,129,18432\nTa 176,130,0\nTa 178,130,2048\nTa 180,130,16384\nTa 182,130,18432\nTa 184,131,0\nTa 186,131,2048\nTa 188,131,16384\nTa 190,131,18432\nWL 0\n"
"Zvaa 32,160,192\nTa 224,132,0\ns_add_u32 m0, s47, 49152\ns_add_u32 s46, s51, 1310720\nMvaa 32,164,196\nTa 226,132,2048\nZaaa 96,164,204\nD0 6,32,46,0\nMaaa 96,160,200\n"
"Ta 228,132,16384\nZvaa 192,160,208\nTa 230,132,18432\nMvaa 192,164,212\nZvaa 224,164,220\nTa 232,133,0\nMvaa 224,160,216\nTa 234,133,2048\nD0 7,32,46,1024\nZvaa 228,168,216\n"
"Mvaa 228,172,220\nTa 236,133,16384\nZvaa 196,172,212\nTa 238,133,18432\nMvaa 196,168,208\nZaaa 100,168,200\nTa 240,134,0\nD0 8,32,46,2048\nMaaa 100,172,204\nTa 242,134,2048\n"
"Zvaa 36,172,196\nMvaa 36,168,192\nTa 244,134,16384\nZvaa 40,176,192\nTa 246,134,18432\nMvaa 40,180,196\nD0 9,32,46,3072\nZaaa 104,180,204\nTa 248,135,0\nMaaa 104,176,200\n"
"Ta 250,135,2048\nZvaa 200,176,208\ns_add_u32 m0, s47, -16384\ns_add_u32 s46, s52, 384\nMvaa 200,180,212\nTa 252,135,16384\nZvaa 232,180,220\nTa 254,135,18432\nD0 136,28,46,0\n"
"Mvaa 232,176,216\nZvaa 236,184,216\nMvaa 236,188,220\nZvaa 204,188,212\nMvaa 204,184,208\nD0 137,28,46,1024\nZaaa 108,184,200\nMaaa 108,188,204\nZvaa 44,188,196\nMvaa 44,184,192\n"
"s_waitcnt vmcnt(18) lgkmcnt(0)\ns_barrier\nZvaa 48,224,192\nRa 128,140,16384\nD0 138,28,46,2048\nMvaa 48,228,196\nRa 132,141,16384\nZaaa 112,228,204\nRa 136,140,18432\n"
"Maaa 112,224,200\nRa 140,141,18432\nZvaa 208,224,208\nRa 144,142,16384\nWV 15\nMvaa 208,228,212\nRa 148,143,16384\nD0 139,28,46,3072\nZvaa 240,228,220\nRa 152,142,18432\n"
"Mvaa 240,224,216\nRa 156,143,18432\nZvaa 244,232,216\nTa 160,12,0\ns_add_u32 m0, s47, 65536\ns_add_u32 s46, s51, 1572864\nMvaa 244,236,220\nTa 162,12,2048\nZvaa 212,236,212\n"
"Ta 164,12,16384\nD0 6,32,46,0\nMvaa 212,232,208\nTa 166,12,18432\nZaaa 116,232,200\nTa 168,13,0\nv_lshlrev_b32 v112, 16, v96\nMaaa 116,236,204\nTa 170,13,2048\nZvaa 52,236,196\n"
"Ta 172,13,16384\nMvaa 52,232,192\nTa 174,13,18432\nD0 7,32,46,1024\nZvaa 56,240,192\nTa 176,14,0\nv_lshlrev_b32 v113, 16, v100\nMvaa 56,244,196\nTa 178,14,2048\nZaaa 120,244,204\n"
"Ta 180,14,16384\nMaaa 120,240,200\nTa 182,14,18432\nZvaa 216,240,208\nTa 184,15,0\nD0 8,32,46,2048\nMvaa 216,244,212\nTa 186,15,2048\nZvaa 248,244,220\nTa 188,15,16384\n"
"Mvaa 248,240,216\nTa 190,15,18432\nZvaa 252,248,216\nv_lshlrev_b32 v114, 16, v104\nv_lshlrev_b32 v115, 16, v108\nMvaa 252,252,220\nD0 9,32,46,3072\nZvaa 220,252,212\n"
"MU 116,112,114\nMvaa 220,248,208\nMU 117,113,114\nZaaa 124,248,200\ns_add_u32 m0, s47, 81920\ns_add_u32 s46, s51, 1835008\nMaaa 124,252,204\nCV 116,116,117\nZvaa 60,252,196\n"
"D0 6,32,46,0\nMvaa 60,248,192\nWL 0\nMvaa 32,160,128\nTa 224,16,0\nv_lshlrev_b32 v118, 16, v116\nMvaa 32,164,132\nTa 226,16,2048\nMaaa 96,164,140\n"
"v_and_b32 v119, 0xffff0000, v116\nMaaa 96,160,136\nTa 228,16,16384\nD0 7,32,46,1024\nMvaa 192,160,144\nTa 230,16,18432\nMU 120,112,115\nMvaa 192,164,148\nMU 121,113,115\n"
"Mvaa 224,164,156\nTa 232,17,0\nMvaa 224,160,152\nTa 234,17,2048\nMvaa 228,168,152\nD0 8,32,46,2048\nv_and_b32 v112, 0xffff0000, v96\nMvaa 228,172,156\nTa 236,17,16384\n"
"Mvaa 196,172,148\nTa 238,17,18432\nMvaa 196,168,144\nv_and_b32 v113, 0xffff0000, v100\nMaaa 100,168,136\nTa 240,18,0\nv_and_b32 v114, 0xffff0000, v104\nMaaa 100,172,140\n"
"Ta 242,18,2048\nD0 9,32,46,3072\nMvaa 36,172,132\nv_and_b32 v115, 0xffff0000, v108\nMvaa 36,168,128\nTa 244,18,16384\nMvaa 40,176,128\nTa 246,18,18432\nMU 116,112,114\n"
"Mvaa 40,180,132\nMU 117,113,114\nMaaa 104,180,140\nTa 248,19,0\nMaaa 104,176,136\nTa 250,19,2048\nMvaa 200,176,144\nCV 116,116,117\nv_and_b32 v117, 0xffff0000, v116\n"
"Mvaa 200,180,148\nTa 252,19,16384\nMvaa 232,180,156\nTa 254,19,18432\nMvaa 232,176,152\nv_lshlrev_b32 v116, 16, v116\nMvaa 236,184,152\nFM 120,112,115\nFM 121,113,115\n"
"Mvaa 236,188,156\nAD 118,118,116\nMvaa 204,188,148\nAD 119,119,117\nMvaa 204,184,144\nv_lshlrev_b32 v112, 16, v97\nMaaa 108,184,136\nv_lshlrev_b32 v113, 16, v101\n"
"v_lshlrev_b32 v114, 16, v105\nMaaa 108,188,140\nv_lshlrev_b32 v115, 16, v109\nMvaa 44,188,132\nMU 116,112,114\nMvaa 44,184,128\ns_waitcnt vmcnt(12) lgkmcnt(0)\ns_barrier\n"
"Mvaa 48,224,128\nRa 192,140,32768\ns_add_u32 m0, s47, -49152\ns_add_u32 s46, s52, 512\nMvaa 48,228,132\nRa 196,141,32768\nMaaa 112,228,140\nRa 200,140,34816\nD0 136,28,46,0\n"
"Maaa 112,224,136\nRa 204,141,34816\nMvaa 208,224,144\nRa 208,142,32768\nMU 117,113,114\nMvaa 208,228,148\nRa 212,143,32768\nMvaa 240,228,156\nRa 216,142,34816\nMvaa 240,224,152\n"
"Ra 220,143,34816\nD0 137,28,46,1024\nMvaa 244,232,152\nTa 160,12,32768\nCV 116,116,117\nMvaa 244,236,156\nTa 162,12,34816\nMvaa 212,236,148\nTa 164,12,49152\nMvaa 212,232,144\n"
"Ta 166,12,51200\nMaaa 116,232,136\nTa 168,13,32768\nD0 138,28,46,2048\nMaaa 116,236,140\nTa 170,13,34816\nMvaa 52,236,132\nTa 172,13,49152\nMvaa 52,232,128\nTa 174,13,51200\n"
"Mvaa 56,240,128\nTa 176,14,32768\nv_and_b32 v117, 0xffff0000, v116\nMvaa 56,244,132\nTa 178,14,34816\nD0 139,28,46,3072\nMaaa 120,244,140\nTa 180,14,49152\nMaaa 120,240,136\n"
"Ta 182,14,51200\nMvaa 216,240,144\nTa 184,15,32768\ns_add_u32 m0, s47, 0\ns_add_u32 s46, s51, 2097152\nMvaa 216,244,148\nTa 186,15,34816\nMvaa 248,244,156\nTa 188,15,49152\n"
"D0 6,32,46,0\nMvaa 248,240,152\nTa 190,15,51200\nMvaa 252,248,152\nv_lshlrev_b32 v116, 16, v116\nFM 120,112,115\nMvaa 252,252,156\nFM 121,113,115\nMvaa 220,252,148\n"
"AD 118,118,116\nMvaa 220,248,144\nD0 7,32,46,1024\nMaaa 124,248,136\nAD 119,119,117\nv_and_b32 v112, 0xffff0000, v97\nMaaa 124,252,140\nv_and_b32 v113, 0xffff0000, v101\n"
"Mvaa 60,252,132\nv_and_b32 v114, 0xffff0000, v105\nMvaa 60,248,128\nWL 0\nMvaa 32,160,192\nTa 224,16,32768\nD0 8,32,46,2048\nMvaa 32,164,196\nTa 226,16,34816\nMaaa 96,164,204\n"
"v_and_b32 v115, 0xffff0000, v109\nMaaa 96,160,200\nTa 228,16,49152\nMvaa 192,160,208\nTa 230,16,51200\nMU 116,112,114\nMvaa 192,164,212\nD0 9,32,46,3072\nMvaa 224,164,220\n"
"Ta 232,17,32768\nMvaa 224,160,216\nTa 234,17,34816\nMvaa 228,168,216\ns_add_u32 m0, s47, 16384\ns_add_u32 s46, s51, 2359296\nMvaa 228,172,220\nTa 236,17,49152\nMvaa 196,172,212\n"
"Ta 238,17,51200\nD0 6,32,46,0\nMvaa 196,168,208\nMU 117,113,114\nMaaa 100,168,200\nTa 240,18,32768\nCV 116,116,117\nMaaa 100,172,204\nTa 242,18,34816\nMvaa 36,172,196\n"
"v_and_b32 v117, 0xffff0000, v116\nMvaa 36,168,192\nTa 244,18,49152\nD0 7,32,46,1024\nMvaa 40,176,192\nTa 246,18,51200\nv_lshlrev_b32 v116, 16, v116\nMvaa 40,180,196\n"
"FM 120,112,115\nMaaa 104,180,204\nTa 248,19,32768\nMaaa 104,176,200\nTa 250,19,34816\nMvaa 200,176,208\nD0 8,32,46,2048\nFM 121,113,115\nMvaa 200,180,212\nTa 252,19,49152\n"
"Mvaa 232,180,220\nTa 254,19,51200\nMvaa 232,176,216\nAD 118,118,116\nMvaa 236,184,216\nAD 119,119,117\nv_lshlrev_b32 v112, 16, v98\nMvaa 236,188,220\nD0 9,32,46,3072\n"
"Mvaa 204,188,212\nv_lshlrev_b32 v113, 16, v102\nMvaa 204,184,208\nv_lshlrev_b32 v114, 16, v106\nMaaa 108,184,200\nv_lshlrev_b32 v115, 16, v110\nMU 116,112,114\nMaaa 108,188,204\n"
"MU 117,113,114\nMvaa 44,188,196\nCV 116,116,117\nMvaa 44,184,192\ns_waitcnt vmcnt(12) lgkmcnt(0)\ns_barrier\nMvaa 48,224,192\nRa 128,140,49152\ns_add_u32 m0, s47, -32768\n"
"s_add_u32 s46, s52, 640\nMvaa 48,228,196\nRa 132,141,49152\nMaaa 112,228,204\nRa 136,140,51200\nD0 136,28,46,0\nMaaa 112,224,200\nRa 140,141,51200\nMvaa 208,224,208\n"
"Ra 144,142,49152\nv_and_b32 v117, 0xffff0000, v116\nMvaa 208,228,212\nRa 148,143,49152\nMvaa 240,228,220\nRa 152,142,51200\nMvaa 240,224,216\nRa 156,143,51200\nD0 137,28,46,1024\n"
"Mvaa 244,232,216\nTa 160,128,0\nv_lshlrev_b32 v116, 16, v116\nMvaa 244,236,220\nTa 162,128,2048\nMvaa 212,236,212\nTa 164,128,16384\nMvaa 212,232,208\nTa 166,128,18432\n"
"Maaa 116,232,200\nTa 168,129,0\nD0 138,28,46,2048\nMaaa 116,236,204\nTa 170,129,2048\nMvaa 52,236,196\nTa 172,129,16384\nMvaa 52,232,192\nTa 174,129,18432\nMvaa 56,240,192\n"
"Ta 176,130,0\nFM 120,112,115\nMvaa 56,244,196\nTa 178,130,2048\nD0 139,28,46,3072\nMaaa 120,244,204\nTa 180,130,16384\nMaaa 120,240,200\nTa 182,130,18432\nMvaa 216,240,208\n"
"Ta 184,131,0\ns_add_u32 m0, s47, 32768\ns_add_u32 s46, s51, 2621440\nMvaa 216,244,212\nTa 186,131,2048\nMvaa 248,244,220\nTa 188,131,16384\nD0 6,32,46,0\nMvaa 248,240,216\n"
"Ta 190,131,18432\nMvaa 252,248,216\nFM 121,113,115\nAD 118,118,116\nMvaa 252,252,220\nAD 119,119,117\nMvaa 220,252,212\nv_and_b32 v112, 0xffff0000, v98\nMvaa 220,248,208\n"
"D0 7,32,46,1024\nMaaa 124,248,200\nv_and_b32 v113, 0xffff0000, v102\nv_and_b32 v114, 0xffff0000, v106\nMaaa 124,252,204\nv_and_b32 v115, 0xffff0000, v110\nMvaa 60,252,196\n"
"MU 116,112,114\nMvaa 60,248,192\nWL 0\nMvaa 32,160,128\nTa 224,132,0\n.W539:\ns_cbranch_execz .W543\nD0 8,32,46,2048\nMvaa 32,164,132\nTa 226,132,2048\nMaaa 96,164,140\n"
"MU 117,113,114\nMaaa 96,160,136\nTa 228,132,16384\nMvaa 192,160,144\nTa 230,132,18432\nCV 116,116,117\nMvaa 192,164,148\nD0 9,32,46,3072\nMvaa 224,164,156\nTa 232,133,0\n"
"Mvaa 224,160,152\nTa 234,133,2048\nMvaa 228,168,152\ns_add_u32 m0, s47, 49152\ns_add_u32 s46, s51, 2883584\nMvaa 228,172,156\nTa 236,133,16384\nMvaa 196,172,148\nTa 238,133,18432\n"
"D0 6,32,46,0\nMvaa 196,168,144\nv_and_b32 v117, 0xffff0000, v116\nMaaa 100,168,136\nTa 240,134,0\nv_lshlrev_b32 v116, 16, v116\nMaaa 100,172,140\nTa 242,134,2048\nMvaa 36,172,132\n"
"FM 120,112,115\nMvaa 36,168,128\nTa 244,134,16384\nD0 7,32,46,1024\nMvaa 40,176,128\nTa 246,134,18432\nFM 121,113,115\nMvaa 40,180,132\nAD 118,118,116\nMaaa 104,180,140\n"
"Ta 248,135,0\nMaaa 104,176,136\nTa 250,135,2048\nMvaa 200,176,144\nD0 8,32,46,2048\nAD 119,119,117\nMvaa 200,180,148\nTa 252,135,16384\nMvaa 232,180,156\nTa 254,135,18432\n"
"Mvaa 232,176,152\nv_lshlrev_b32 v112, 16, v99\nMvaa 236,184,152\nv_lshlrev_b32 v113, 16, v103\nv_lshlrev_b32 v114, 16, v107\nMvaa 236,188,156\nD0 9,32,46,3072\nMvaa 204,188,148\n"
"v_lshlrev_b32 v115, 16, v111\nMvaa 204,184,144\nMU 116,112,114\nMaaa 108,184,136\nMU 117,113,114\nCV 116,116,117\ns_and_b32 s78, s74, 3\ns_lshl2_add_u32 s78, s73, s78\n"
"s_lshl_b32 s78, s78, 12\ns_bitcmp1_b32 s11, 0\ns_cselect_b32 s79, s48, s45\ns_add_u32 s79, s79, 512\ns_and_b32 s79, s79, 0xfffe0000\ns_add_u32 s78, s78, s79\n"
"buffer_load_dword v31, v26, s[20:23], s78 offen\nMaaa 108,188,140\nv_and_b32 v117, 0xffff0000, v116\nMvaa 44,188,132\nv_lshlrev_b32 v116, 16, v116\nMvaa 44,184,128\n"
"s_waitcnt vmcnt(13) lgkmcnt(0)\ns_barrier\nMvaa 48,224,128\nRa 192,140,16384\ns_add_u32 m0, s47, -16384\ns_add_u32 s46, s52, 768\nMvaa 48,228,132\nRa 196,141,16384\n"
"Maaa 112,228,140\nRa 200,140,18432\nD0 136,28,46,0\nMaaa 112,224,136\nRa 204,141,18432\nMvaa 208,224,144\nRa 208,142,16384\nFM 120,112,115\nMvaa 208,228,148\nRa 212,143,16384\n"
"Mvaa 240,228,156\nRa 216,142,18432\nMvaa 240,224,152\nRa 220,143,18432\nD0 137,28,46,1024\nMvaa 244,232,152\nTa 160,12,0\nFM 121,113,115\nMvaa 244,236,156\nTa 162,12,2048\n"
"Mvaa 212,236,148\nTa 164,12,16384\nMvaa 212,232,144\nTa 166,12,18432\nMaaa 116,232,136\nTa 168,13,0\nD0 138,28,46,2048\nMaaa 116,236,140\nTa 170,13,2048\nMvaa 52,236,132\n"
"Ta 172,13,16384\nMvaa 52,232,128\nTa 174,13,18432\nMvaa 56,240,128\nTa 176,14,0\nAD 118,118,116\nMvaa 56,244,132\nTa 178,14,2048\nD0 139,28,46,3072\nMaaa 120,244,140\n"
"Ta 180,14,16384\nMaaa 120,240,136\nTa 182,14,18432\nMvaa 216,240,144\nTa 184,15,0\ns_add_u32 m0, s47, 65536\ns_add_u32 s46, s51, 3145728\nMvaa 216,244,148\nTa 186,15,2048\n"
"Mvaa 248,244,156\nTa 188,15,16384\nD0 6,32,46,0\nMvaa 248,240,152\nTa 190,15,18432\nMvaa 252,248,152\nAD 119,119,117\nv_and_b32 v112, 0xffff0000, v99\nMvaa 252,252,156\n"
"v_and_b32 v113, 0xffff0000, v103\nMvaa 220,252,148\nv_and_b32 v114, 0xffff0000, v107\nMvaa 220,248,144\nD0 7,32,46,1024\nMaaa 124,248,136\nv_and_b32 v115, 0xffff0000, v111\n"
"MU 116,112,114\nMaaa 124,252,140\nMU 117,113,114\nMvaa 60,252,132\nCV 116,116,117\nMvaa 60,248,128\nWL 0\nMvaa 32,160,192\nTa 224,16,0\nD0 8,32,46,2048\nMvaa 32,164,196\n"
"Ta 226,16,2048\nMaaa 96,164,204\nv_and_b32 v117, 0xffff0000, v116\nMaaa 96,160,200\nTa 228,16,16384\nMvaa 192,160,208\nTa 230,16,18432\nv_lshlrev_b32 v116, 16, v116\n"
"Mvaa 192,164,212\nD0 9,32,46,3072\nMvaa 224,164,220\nTa 232,17,0\nMvaa 224,160,216\nTa 234,17,2048\nMvaa 228,168,216\ns_add_u32 m0, s47, 81920\ns_add_u32 s46, s51, 3407872\n"
"Mvaa 228,172,220\nTa 236,17,16384\nMvaa 196,172,212\nTa 238,17,18432\nD0 6,32,46,0\nMvaa 196,168,208\nFM 120,112,115\nMaaa 100,168,200\nTa 240,18,0\nFM 121,113,115\n"
"Maaa 100,172,204\nTa 242,18,2048\nMvaa 36,172,196\nAD 118,118,116\nMvaa 36,168,192\nTa 244,18,16384\nD0 7,32,46,1024\nMvaa 40,176,192\nTa 246,18,18432\nAD 119,119,117\n"
"Mvaa 40,180,196\ns_nop 0\nv_permlane32_swap_b32 v118, v119\nMaaa 104,180,204\nTa 248,19,0\nMaaa 104,176,200\nTa 250,19,2048\nMvaa 200,176,208\nD0 8,32,46,2048\n"
"v_permlane32_swap_b32 v120, v121\nMvaa 200,180,212\nTa 252,19,16384\nMvaa 232,180,220\nTa 254,19,18432\nMvaa 232,176,216\nAD 118,118,119\nMvaa 236,184,216\nAD 120,120,121\n"
"Mvaa 236,188,220\nD0 9,32,46,3072\nMvaa 204,188,212\nv_permlane16_swap_b32 v118, v120\nMvaa 204,184,208\nAD 118,118,120\nMaaa 108,184,200\nMaaa 108,188,204\n"
"v_add_f32_dpp v118, v118, v118 row_ror:8 row_mask:0xf bank_mask:0xf\nMvaa 44,188,196\nMvaa 44,184,192\ns_waitcnt vmcnt(13) lgkmcnt(0)\ns_barrier\nMvaa 48,224,192\n"
"Ra 128,140,32768\ns_add_u32 m0, s47, -65536\ns_add_u32 s45, s45, 128\nMvaa 48,228,196\nRa 132,141,32768\nMaaa 112,228,204\nRa 136,140,34816\nD0 2,20,45,0\nMaaa 112,224,200\n"
"Ra 140,141,34816\nMvaa 208,224,208\nRa 144,142,32768\nv_add_f32_dpp v118, v118, v118 row_ror:4 row_mask:0xf bank_mask:0xf\nMvaa 208,228,212\nRa 148,143,32768\nMvaa 240,228,220\n"
"Ra 152,142,34816\nMvaa 240,224,216\nRa 156,143,34816\nD0 3,20,45,1024\nMvaa 244,232,216\nTa 160,12,32768\nv_add_f32_dpp v118, v118, v118 row_ror:2 row_mask:0xf bank_mask:0xf\n"
"Mvaa 244,236,220\nTa 162,12,34816\nMvaa 212,236,212\nTa 164,12,49152\nMvaa 212,232,208\nTa 166,12,51200\nMaaa 116,232,200\nTa 168,13,32768\nD0 4,20,45,2048\nMaaa 116,236,204\n"
"Ta 170,13,34816\nMvaa 52,236,196\nTa 172,13,49152\nMvaa 52,232,192\nTa 174,13,51200\nMvaa 56,240,192\nTa 176,14,32768\n"
"v_add_f32_dpp v118, v118, v118 row_ror:1 row_mask:0xf bank_mask:0xf\nMvaa 56,244,196\nTa 178,14,34816\nD0 5,20,45,3072\nMaaa 120,244,204\nTa 180,14,49152\nMaaa 120,240,200\n"
"Ta 182,14,51200\nMvaa 216,240,208\nTa 184,15,32768\ns_add_u32 m0, s47, 0\ns_add_u32 s44, s44, 0x40000\nMvaa 216,244,212\nTa 186,15,34816\nMvaa 248,244,220\nTa 188,15,49152\n"
"D0 6,24,44,0\nMvaa 248,240,216\nTa 190,15,51200\nMvaa 252,248,216\nCV 118,118,118\nv_lshlrev_b32 v118, 16, v118\nMvaa 252,252,220\nv_mul_f32 v119, 0xbfb8aa3b, v118\nEX 119,119\n"
"Mvaa 220,252,212\nv_add_f32 v120, 0, v118\nMvaa 220,248,208\nD0 7,24,44,1024\nMaaa 124,248,200\nv_add_f32 v119, 1.0, v119\nMaaa 124,252,204\nv_rcp_f32 v119, v119\nMvaa 60,252,196\n"
"v_ashrrev_i32 v121, 31, v120\nMvaa 60,248,192\nWL 0\nMvaa 32,160,128\nTa 224,16,32768\nD0 8,24,44,2048\nMvaa 32,164,132\nTa 226,16,34816\nMaaa 96,164,140\nCV 119,119,119\n"
"Maaa 96,160,136\nTa 228,16,49152\nMvaa 192,160,144\nTa 230,16,51200\nv_lshlrev_b32 v119, 16, v119\nMvaa 192,164,148\nD0 9,24,44,3072\nMvaa 224,164,156\nTa 232,17,32768\n"
"Mvaa 224,160,152\nTa 234,17,34816\nMvaa 228,168,152\ns_add_u32 m0, s47, 16384\ns_add_u32 s44, s44, 0x40000\nMvaa 228,172,156\nTa 236,17,49152\nMvaa 196,172,148\nTa 238,17,51200\n"
"D0 6,24,44,0\nMvaa 196,168,144\nv_or_b32 v121, 0x80000000, v121\nMaaa 100,168,136\nTa 240,18,32768\nv_xor_b32 v120, v120, v121\nMaaa 100,172,140\nTa 242,18,34816\nMvaa 36,172,132\n"
"v_and_b32 v120, 0xffff0000, v120\nMvaa 36,168,128\nTa 244,18,49152\nD0 7,24,44,1024\nMvaa 40,176,128\nTa 246,18,51200\ns_lshl2_add_u32 s2, s10, s11\nMvaa 40,180,132\n"
"v_lshrrev_b32 v112, 5, v0\nMaaa 104,180,140\nTa 248,19,32768\nMaaa 104,176,136\nTa 250,19,34816\nMvaa 200,176,144\nD0 8,24,44,2048\nv_lshl_add_u32 v112, s2, 1, v112\n"
"Mvaa 200,180,148\nTa 252,19,49152\nMvaa 232,180,156\nTa 254,19,51200\nMvaa 232,176,152\nv_sub_u32 v113, 0xffff, v112\nMvaa 236,184,152\nv_or_b32 v120, v120, v113\n"
"v_mov_b32 v121, s15\nMvaa 236,188,156\nD0 9,24,44,3072\nMvaa 204,188,148\nv_and_b32 v113, 31, v0\nMvaa 204,184,144\nv_mov_b32 v116, 0x40000000\nv_cmp_eq_u32 vcc, 16, v113\n"
"v_lshlrev_b32 v114, 2, v112\nv_lshlrev_b32 v115, 10, v113\nv_cndmask_b32 v114, v116, v114, vcc\nv_cndmask_b32 v120, v119, v120, vcc\nMaaa 108,184,136\n"
"v_lshl_add_u32 v115, v112, 3, v115\nv_and_b32 v113, 15, v0\nMaaa 108,188,140\nv_cmp_eq_u32 vcc, 0, v113\ns_mov_b32 s2, 0xc040000\ns_mov_b32 s3, 0xc03e000\n"
"v_cndmask_b32 v115, v116, v115, vcc\nbuffer_store_dwordx2 v[120:121], v115, s[16:19], s2 offen sc0 sc1\nbuffer_store_dword v118, v114, s[16:19], s3 offen sc0 sc1\nMvaa 44,188,132\n"
"Mvaa 44,184,128\ns_waitcnt vmcnt(14) lgkmcnt(0)\ns_barrier\nMvaa 48,224,128\nRa 192,140,49152\ns_add_u32 m0, s47, -49152\ns_add_u32 s48, s48, 128\nMvaa 48,228,132\n"
"Ra 196,141,49152\nMaaa 112,228,140\nRa 200,140,51200\nD0 2,20,48,0\nMaaa 112,224,136\nRa 204,141,51200\nMvaa 208,224,144\nRa 208,142,49152\nMvaa 208,228,148\nRa 212,143,49152\n"
"Mvaa 240,228,156\nRa 216,142,51200\nMvaa 240,224,152\nRa 220,143,51200\nD0 3,20,48,1024\nMvaa 244,232,152\nTa 160,128,0\nMvaa 244,236,156\nTa 162,128,2048\nMvaa 212,236,148\n"
"Ta 164,128,16384\nMvaa 212,232,144\nTa 166,128,18432\nMaaa 116,232,136\nTa 168,129,0\nD0 4,20,48,2048\nMaaa 116,236,140\nTa 170,129,2048\nMvaa 52,236,132\nTa 172,129,16384\n"
"Mvaa 52,232,128\nTa 174,129,18432\nMvaa 56,240,128\nTa 176,130,0\nMvaa 56,244,132\nTa 178,130,2048\nD0 5,20,48,3072\nMaaa 120,244,140\nTa 180,130,16384\nMaaa 120,240,136\n"
"Ta 182,130,18432\nMvaa 216,240,144\nTa 184,131,0\ns_add_u32 m0, s47, -32768\ns_add_u32 s45, s45, 128\nMvaa 216,244,148\nTa 186,131,2048\nMvaa 248,244,156\nTa 188,131,16384\n"
"D0 2,20,45,0\nMvaa 248,240,152\nTa 190,131,18432\nMvaa 252,248,152\nMvaa 252,252,156\nMvaa 220,252,148\nMvaa 220,248,144\nD0 3,20,45,1024\nMaaa 124,248,136\nMaaa 124,252,140\n"
"Mvaa 60,252,132\nMvaa 60,248,128\nWL 0\nMvaa 32,160,192\nTa 224,132,0\nD0 4,20,45,2048\nMvaa 32,164,196\nTa 226,132,2048\nMvaa 36,172,196\nMvaa 36,168,192\nTa 228,132,16384\n"
"Mvaa 40,176,192\nTa 230,132,18432\nMvaa 40,180,196\nD0 5,20,45,3072\nMvaa 44,188,196\nTa 232,133,0\nMvaa 44,184,192\nTa 234,133,2048\nMaaa 108,184,200\ns_add_u32 m0, s47, 32768\n"
"s_add_u32 s44, s44, 0x40000\nMaaa 108,188,204\nTa 236,133,16384\nMaaa 104,180,204\nTa 238,133,18432\nD0 6,24,44,0\nMaaa 104,176,200\nv_mul_f32 v32, s57, v32\n"
"v_mul_f32 v33, s57, v33\nMaaa 100,168,200\nTa 240,134,0\nCV 32,32,33\nMaaa 100,172,204\nTa 242,134,2048\nv_mul_f32 v34, s57, v34\nMaaa 96,164,204\nv_mul_f32 v35, s57, v35\n"
"CV 33,34,35\nMaaa 96,160,200\nTa 244,134,16384\nD0 7,24,44,1024\nMvaa 192,160,208\nTa 246,134,18432\nv_mul_f32 v36, s57, v36\nMvaa 192,164,212\nv_mul_f32 v37, s57, v37\n"
"Mvaa 196,172,212\nTa 248,135,0\nCV 34,36,37\nMvaa 196,168,208\nTa 250,135,2048\nv_mul_f32 v38, s57, v38\nMvaa 200,176,208\nD0 8,24,44,2048\nv_mul_f32 v39, s57, v39\n"
"Mvaa 200,180,212\nTa 252,135,16384\nCV 35,38,39\nMvaa 204,188,212\nTa 254,135,18432\nv_mul_f32 v40, s57, v40\nMvaa 204,184,208\nv_mul_f32 v41, s57, v41\nCV 40,40,41\n"
"Mvaa 236,184,216\nv_mul_f32 v42, s57, v42\nv_mul_f32 v43, s57, v43\nMvaa 236,188,220\nD0 9,24,44,3072\nMvaa 232,180,220\nCV 41,42,43\nv_mul_f32 v44, s57, v44\nMvaa 232,176,216\n"
"v_mul_f32 v45, s57, v45\nCV 42,44,45\nMvaa 228,168,216\nv_mul_f32 v46, s57, v46\nv_mul_f32 v47, s57, v47\nMvaa 228,172,220\nCV 43,46,47\nAR 20,96\nMvaa 224,164,220\nAR 21,97\n"
"v_mul_f32 v20, s57, v20\nMvaa 224,160,216\ns_waitcnt vmcnt(14) lgkmcnt(0)\ns_barrier\nMvaa 48,224,192\ns_add_u32 m0, s47, 49152\ns_add_u32 s44, s44, 0x40000\nRa 128,10,0\n"
"Mvaa 48,228,196\nRa 132,11,0\nMvaa 52,236,196\nD0 6,24,44,0\nRa 136,10,2048\n.W540:\ns_cbranch_execz .W544\nMvaa 52,232,192\nRa 140,11,2048\nv_mul_f32 v21, s57, v21\n"
"Mvaa 56,240,192\nRa 144,10,8192\nCV 36,20,21\nMvaa 56,244,196\nRa 148,11,8192\nAR 20,98\nMvaa 60,252,196\nD0 7,24,44,1024\nRa 152,10,10240\nMvaa 60,248,192\nRa 156,11,10240\n"
"AR 21,99\nMaaa 124,248,200\nTa 160,12,0\nv_mul_f32 v20, s57, v20\nMaaa 124,252,204\nTa 162,12,2048\nMaaa 120,244,204\nD0 8,24,44,2048\nTa 164,12,16384\nMaaa 120,240,200\n"
"Ta 166,12,18432\nv_mul_f32 v21, s57, v21\nMaaa 116,232,200\nTa 168,13,0\nCV 37,20,21\nMaaa 116,236,204\nTa 170,13,2048\nAR 20,100\nMaaa 112,228,204\nD0 9,24,44,3072\n"
"Ta 172,13,16384\nMaaa 112,224,200\nTa 174,13,18432\nAR 21,101\nMvaa 208,224,208\ns_add_u32 m0, s47, -16384\ns_add_u32 s48, s48, 128\nTa 176,14,0\nMvaa 208,228,212\nTa 178,14,2048\n"
"Mvaa 212,236,212\nD0 2,20,48,0\nTa 180,14,16384\nMvaa 212,232,208\nTa 182,14,18432\nv_mul_f32 v20, s57, v20\nMvaa 216,240,208\nTa 184,15,0\nv_mul_f32 v21, s57, v21\n"
"Mvaa 216,244,212\nTa 186,15,2048\nCV 38,20,21\nMvaa 220,252,212\nD0 3,20,48,1024\nTa 188,15,16384\nMvaa 220,248,208\nTa 190,15,18432\nAR 20,102\nMvaa 252,248,216\nAR 21,103\n"
"v_mul_f32 v20, s57, v20\nMvaa 252,252,220\nv_mul_f32 v21, s57, v21\nMvaa 248,244,220\nD0 4,20,48,2048\nCV 39,20,21\nMvaa 248,240,216\nAR 20,104\nAR 21,105\nMvaa 244,232,216\n"
"v_mul_f32 v20, s57, v20\nv_mul_f32 v21, s57, v21\nMvaa 244,236,220\nCV 44,20,21\nAR 20,106\nMvaa 240,228,220\nD0 5,20,48,3072\nAR 21,107\nMvaa 240,224,216\nWL 0\ns_barrier\n"
"Zvaa 64,160,128\nTa 224,16,0\nv_mul_f32 v20, s57, v20\nMvaa 64,164,132\nTa 226,16,2048\nZvaa 96,164,140\ns_add_u32 m0, s47, -65536\ns_add_u32 s45, s45, 128\nMvaa 96,160,136\n"
"Ta 228,16,16384\nv_mul_f32 v21, s57, v21\nZvaa 128,160,144\nTa 230,16,18432\nCV 45,20,21\nMvaa 128,164,148\nD0 2,20,45,0\nAR 20,108\nZvaa 160,164,156\nTa 232,17,0\nAR 21,109\n"
"Mvaa 160,160,152\nTa 234,17,2048\nv_mul_f32 v20, s57, v20\nZvaa 164,168,152\nv_mul_f32 v21, s57, v21\nCV 46,20,21\nMvaa 164,172,156\nTa 236,17,16384\ns_add_u32 s6, s56, 0\n"
"Sv 32,1,16,6,0\nZvaa 132,172,148\nTa 238,17,18432\nAR 20,110\nMvaa 132,168,144\nAR 21,111\nv_mul_f32 v20, s57, v20\nZvaa 100,168,136\nTa 240,18,0\nv_mul_f32 v21, s57, v21\n"
"Mvaa 100,172,140\nTa 242,18,2048\nCV 47,20,21\nZvaa 68,172,132\nD0 3,20,45,1024\nv_mul_f32 v48, s57, v48\nMvaa 68,168,128\nTa 244,18,16384\nv_mul_f32 v49, s57, v49\n"
"Zvaa 72,176,128\nTa 246,18,18432\nCV 48,48,49\nMvaa 72,180,132\nv_mul_f32 v50, s57, v50\nZvaa 104,180,140\nTa 248,19,0\nv_mul_f32 v51, s57, v51\nMvaa 104,176,136\nTa 250,19,2048\n"
"CV 49,50,51\nZvaa 136,176,144\nv_mul_f32 v52, s57, v52\nv_mul_f32 v53, s57, v53\nMvaa 136,180,148\nTa 252,19,16384\nCV 50,52,53\nZvaa 168,180,156\nTa 254,19,18432\n"
"v_mul_f32 v54, s57, v54\nMvaa 168,176,152\nD0 4,20,45,2048\nv_mul_f32 v55, s57, v55\nZvaa 172,184,152\nCV 51,54,55\nv_mul_f32 v56, s57, v56\nMvaa 172,188,156\n"
"v_mul_f32 v57, s57, v57\ns_add_u32 s6, s56, 0\nSv 40,1,16,6,2048\nZvaa 140,188,148\nCV 56,56,57\nv_mul_f32 v58, s57, v58\nMvaa 140,184,144\nv_mul_f32 v59, s57, v59\nCV 57,58,59\n"
"Zvaa 108,184,136\nv_mul_f32 v60, s57, v60\nv_mul_f32 v61, s57, v61\nMvaa 108,188,140\nD0 5,20,45,3072\nCV 58,60,61\nZvaa 76,188,132\nv_mul_f32 v62, s57, v62\n"
"v_mul_f32 v63, s57, v63\nMvaa 76,184,128\ns_waitcnt vmcnt(18) lgkmcnt(0)\ns_barrier\nZvaa 80,224,128\nRa 192,10,16384\nCV 59,62,63\nMvaa 80,228,132\ns_add_u32 m0, s47, 0\n"
"s_add_u32 s44, s44, 0x40000\nZvaa 112,228,140\nRa 196,11,16384\nAR 20,112\nMvaa 112,224,136\nAR 21,113\nv_mul_f32 v20, s57, v20\nZvaa 144,224,144\nRa 200,10,18432\n"
"v_mul_f32 v21, s57, v21\nMvaa 144,228,148\nD0 6,24,44,0\nCV 52,20,21\nZvaa 176,228,156\nRa 204,11,18432\nAR 20,114\nMvaa 176,224,152\nAR 21,115\nv_mul_f32 v20, s57, v20\n"
"Zvaa 180,232,152\nRa 208,10,24576\nv_mul_f32 v21, s57, v21\nMvaa 180,236,156\nCV 53,20,21\ns_add_u32 s6, s56, 0\nSv 36,1,16,6,1024\nZvaa 148,236,148\nRa 212,11,24576\nAR 20,116\n"
"Mvaa 148,232,144\nAR 21,117\nv_mul_f32 v20, s57, v20\nZvaa 116,232,136\nRa 216,10,26624\nv_mul_f32 v21, s57, v21\nMvaa 116,236,140\nD0 7,24,44,1024\nCV 54,20,21\nZvaa 84,236,132\n"
"Ra 220,11,26624\nAR 20,118\nMvaa 84,232,128\nAR 21,119\nv_mul_f32 v20, s57, v20\nZvaa 88,240,128\nv_mul_f32 v21, s57, v21\nCV 55,20,21\nMvaa 88,244,132\nAR 20,120\n"
"Zvaa 120,244,140\nAR 21,121\nv_mul_f32 v20, s57, v20\nMvaa 120,240,136\nv_mul_f32 v21, s57, v21\nCV 60,20,21\nZvaa 152,240,144\nAR 20,122\nAR 21,123\nMvaa 152,244,148\n"
"D0 8,24,44,2048\nv_mul_f32 v20, s57, v20\nZvaa 184,244,156\nv_mul_f32 v21, s57, v21\nCV 61,20,21\nMvaa 184,240,152\nAR 20,124\nAR 21,125\nZvaa 188,248,152\n"
"v_mul_f32 v20, s57, v20\nv_mul_f32 v21, s57, v21\nMvaa 188,252,156\nCV 62,20,21\ns_add_u32 s6, s56, 0\nSv 44,1,16,6,3072\nZvaa 156,252,148\nAR 20,126\nAR 21,127\nMvaa 156,248,144\n"
"v_mul_f32 v20, s57, v20\nv_mul_f32 v21, s57, v21\nZvaa 124,248,136\nCV 63,20,21\nv_mul_f32 v192, s57, v192\nMvaa 124,252,140\nD0 9,24,44,3072\nv_mul_f32 v193, s57, v193\n"
"Zvaa 92,252,132\nCV 192,192,193\nv_mul_f32 v194, s57, v194\nMvaa 92,248,128\nWL 0\nZaaa 16,224,192\nRa 128,10,32768\nv_mul_f32 v195, s57, v195\nMaaa 16,228,196\n"
"s_add_u32 m0, s47, 16384\ns_add_u32 s44, s44, 0x40000\nZaaa 48,228,204\nRa 132,11,32768\nCV 193,194,195\nMaaa 48,224,200\nv_mul_f32 v196, s57, v196\nv_mul_f32 v197, s57, v197\n"
"Zaaa 80,224,208\nRa 136,10,34816\nCV 194,196,197\nMaaa 80,228,212\nD0 6,24,44,0\nv_mul_f32 v198, s57, v198\nZaaa 112,228,220\nRa 140,11,34816\nv_mul_f32 v199, s57, v199\n"
"Maaa 112,224,216\nCV 195,198,199\nv_mul_f32 v200, s57, v200\nZaaa 116,232,216\nRa 144,10,40960\nv_mul_f32 v201, s57, v201\nMaaa 116,236,220\nCV 200,200,201\n"
"v_mul_f32 v202, s57, v202\ns_add_u32 s6, s56, 4096\nSv 48,1,16,6,0\nZaaa 84,236,212\nRa 148,11,40960\nv_mul_f32 v203, s57, v203\nMaaa 84,232,208\nCV 201,202,203\n"
"v_mul_f32 v204, s57, v204\nZaaa 52,232,200\nRa 152,10,43008\nv_mul_f32 v205, s57, v205\nMaaa 52,236,204\nD0 7,24,44,1024\nCV 202,204,205\nZaaa 20,236,196\nRa 156,11,43008\n"
"v_mul_f32 v206, s57, v206\nMaaa 20,232,192\nv_mul_f32 v207, s57, v207\nCV 203,206,207\nZaaa 24,240,192\nv_mul_f32 v208, s57, v208\nv_mul_f32 v209, s57, v209\nMaaa 24,244,196\n"
"CV 208,208,209\nv_mul_f32 v210, s57, v210\nZaaa 56,244,204\nv_mul_f32 v211, s57, v211\nCV 209,210,211\nMaaa 56,240,200\nv_mul_f32 v212, s57, v212\nv_mul_f32 v213, s57, v213\n"
"Zaaa 88,240,208\nCV 210,212,213\nv_mul_f32 v214, s57, v214\nMaaa 88,244,212\nD0 8,24,44,2048\nv_mul_f32 v215, s57, v215\nZaaa 120,244,220\nCV 211,214,215\n"
"v_mul_f32 v216, s57, v216\nMaaa 120,240,216\nv_mul_f32 v217, s57, v217\nCV 216,216,217\nZaaa 124,248,216\nv_mul_f32 v218, s57, v218\nv_mul_f32 v219, s57, v219\nMaaa 124,252,220\n"
"CV 217,218,219\nv_mul_f32 v220, s57, v220\ns_add_u32 s6, s56, 4096\nSv 56,1,16,6,2048\nZaaa 92,252,212\nv_mul_f32 v221, s57, v221\nCV 218,220,221\nMaaa 92,248,208\n"
"v_mul_f32 v222, s57, v222\nv_mul_f32 v223, s57, v223\nZaaa 60,248,200\nCV 219,222,223\nv_mul_f32 v224, s57, v224\nMaaa 60,252,204\nD0 9,24,44,3072\nv_mul_f32 v225, s57, v225\n"
"Zaaa 28,252,196\nCV 224,224,225\nv_mul_f32 v226, s57, v226\nMaaa 28,248,192\ns_waitcnt vmcnt(22) lgkmcnt(0)\ns_barrier\nZaaa 0,160,192\nTa 224,16,32768\nv_mul_f32 v227, s57, v227\n"
"Maaa 0,164,196\nTa 226,16,34816\nCV 225,226,227\nZaaa 32,164,204\ns_add_u32 m0, s47, -49152\ns_add_u32 s48, s48, 128\nMaaa 32,160,200\nTa 228,16,49152\nv_mul_f32 v228, s57, v228\n"
"Zaaa 64,160,208\nTa 230,16,51200\nv_mul_f32 v229, s57, v229\nMaaa 64,164,212\nD0 2,20,48,0\nCV 226,228,229\nZaaa 96,164,220\nTa 232,17,32768\nv_mul_f32 v230, s57, v230\n"
"Maaa 96,160,216\nTa 234,17,34816\nv_mul_f32 v231, s57, v231\nZaaa 100,168,216\nCV 227,230,231\nv_mul_f32 v232, s57, v232\nMaaa 100,172,220\nTa 236,17,49152\n"
"v_mul_f32 v233, s57, v233\ns_add_u32 s6, s56, 4096\nSv 52,1,16,6,1024\nZaaa 68,172,212\nTa 238,17,51200\nCV 232,232,233\nMaaa 68,168,208\nv_mul_f32 v234, s57, v234\n"
"v_mul_f32 v235, s57, v235\nZaaa 36,168,200\nTa 240,18,32768\nCV 233,234,235\nMaaa 36,172,204\nTa 242,18,34816\nv_mul_f32 v236, s57, v236\nZaaa 4,172,196\nD0 3,20,48,1024\n"
"v_mul_f32 v237, s57, v237\nMaaa 4,168,192\nTa 244,18,49152\nCV 234,236,237\nZaaa 8,176,192\nTa 246,18,51200\nv_mul_f32 v238, s57, v238\nMaaa 8,180,196\nv_mul_f32 v239, s57, v239\n"
"CV 235,238,239\nZaaa 40,180,204\nTa 248,19,32768\nv_mul_f32 v240, s57, v240\nMaaa 40,176,200\nTa 250,19,34816\nv_mul_f32 v241, s57, v241\nZaaa 72,176,208\nCV 240,240,241\n"
"v_mul_f32 v242, s57, v242\nMaaa 72,180,212\nTa 252,19,49152\nv_mul_f32 v243, s57, v243\nZaaa 104,180,220\nTa 254,19,51200\nCV 241,242,243\nMaaa 104,176,216\nD0 4,20,48,2048\n"
"v_mul_f32 v244, s57, v244\nZaaa 108,184,216\nv_mul_f32 v245, s57, v245\nCV 242,244,245\nMaaa 108,188,220\nv_mul_f32 v246, s57, v246\nv_mul_f32 v247, s57, v247\n"
"s_add_u32 s6, s56, 4096\nSv 60,1,16,6,3072\nZaaa 76,188,212\nCV 243,246,247\nv_mul_f32 v248, s57, v248\nMaaa 76,184,208\nv_mul_f32 v249, s57, v249\nCV 248,248,249\n"
"Zaaa 44,184,200\nv_mul_f32 v250, s57, v250\nv_mul_f32 v251, s57, v251\nMaaa 44,188,204\nD0 5,20,48,3072\nCV 249,250,251\nZaaa 12,188,196\nv_mul_f32 v252, s57, v252\n"
"v_mul_f32 v253, s57, v253\nMaaa 12,184,192\nWL 0\nMvaa 80,224,128\nTa 160,12,32768\nCV 250,252,253\nMvaa 80,228,132\nTa 162,12,34816\nv_mul_f32 v254, s57, v254\nMvaa 112,228,140\n"
"s_add_u32 m0, s47, -32768\ns_add_u32 s45, s45, 128\nMvaa 112,224,136\nTa 164,12,49152\nv_mul_f32 v255, s57, v255\nMvaa 144,224,144\nTa 166,12,51200\nCV 251,254,255\n"
"Mvaa 144,228,148\nD0 2,20,45,0\nMvaa 176,228,156\nTa 168,13,32768\nMvaa 176,224,152\nTa 170,13,34816\nMvaa 180,232,152\nMvaa 180,236,156\nTa 172,13,49152\ns_add_u32 s6, s53, 0\n"
"Sv 192,1,16,6,0\nMvaa 148,236,148\nTa 174,13,51200\nMvaa 148,232,144\nMvaa 116,232,136\nTa 176,14,32768\nMvaa 116,236,140\nTa 178,14,34816\nMvaa 84,236,132\nD0 3,20,45,1024\n"
"Mvaa 84,232,128\nTa 180,14,49152\nMvaa 88,240,128\nTa 182,14,51200\nMvaa 88,244,132\nMvaa 120,244,140\nTa 184,15,32768\nMvaa 120,240,136\nTa 186,15,34816\nMvaa 152,240,144\n"
"Mvaa 152,244,148\nTa 188,15,49152\nMvaa 184,244,156\nTa 190,15,51200\nMvaa 184,240,152\nD0 4,20,45,2048\nMvaa 188,248,152\nMvaa 188,252,156\ns_add_u32 s6, s53, 0\n"
"Sv 200,1,16,6,2048\nMvaa 156,252,148\nMvaa 156,248,144\nMvaa 124,248,136\nMvaa 124,252,140\nD0 5,20,45,3072\nMvaa 92,252,132\nMvaa 92,248,128\ns_waitcnt vmcnt(24) lgkmcnt(0)\n"
"s_barrier\nMvaa 64,160,128\nRa 192,10,49152\nMvaa 64,164,132\ns_add_u32 m0, s47, 32768\ns_add_u32 s44, s44, 0x40000\nMvaa 96,164,140\nRa 196,11,49152\nMvaa 96,160,136\n"
"Mvaa 128,160,144\nRa 200,10,51200\nMvaa 128,164,148\nD0 6,24,44,0\nMvaa 160,164,156\nRa 204,11,51200\nMvaa 160,160,152\nMvaa 164,168,152\nRa 208,10,57344\nMvaa 164,172,156\n"
"s_add_u32 s6, s53, 4096\nSv 208,1,16,6,0\nMvaa 132,172,148\nRa 212,11,57344\nMvaa 132,168,144\nMvaa 100,168,136\nRa 216,10,59392\nMvaa 100,172,140\nD0 7,24,44,1024\n"
"Mvaa 68,172,132\nRa 220,11,59392\nMvaa 68,168,128\nMvaa 72,176,128\nMvaa 72,180,132\nMvaa 104,180,140\nMvaa 104,176,136\nMvaa 136,176,144\nMvaa 136,180,148\n.W541:\n"
"s_cbranch_execz .W545\nD0 8,24,44,2048\nMvaa 168,180,156\nMvaa 168,176,152\nMvaa 172,184,152\nMvaa 172,188,156\ns_add_u32 s6, s53, 4096\nSv 216,1,16,6,2048\nMvaa 140,188,148\n"
"Mvaa 140,184,144\nMvaa 108,184,136\nMvaa 108,188,140\nD0 9,24,44,3072\nMvaa 76,188,132\nMvaa 76,184,128\nWL 0\nMaaa 0,160,192\nRa 128,10,0\nMaaa 0,164,196\n"
"s_add_u32 m0, s47, 49152\ns_add_u32 s44, s44, 0x40000\nMaaa 32,164,204\nRa 132,11,0\nMaaa 32,160,200\nMaaa 64,160,208\nRa 136,10,2048\nMaaa 64,164,212\nD0 6,24,44,0\n"
"Maaa 96,164,220\nRa 140,11,2048\nMaaa 96,160,216\nMaaa 100,168,216\nRa 144,10,8192\nMaaa 100,172,220\ns_add_u32 s6, s53, 0\nSv 224,1,16,6,1024\nMaaa 68,172,212\nRa 148,11,8192\n"
"Maaa 68,168,208\nMaaa 36,168,200\nRa 152,10,10240\nMaaa 36,172,204\nD0 7,24,44,1024\nMaaa 4,172,196\nRa 156,11,10240\nMaaa 4,168,192\nMaaa 8,176,192\nMaaa 8,180,196\n"
"Maaa 40,180,204\nMaaa 40,176,200\nMaaa 72,176,208\nMaaa 72,180,212\nD0 8,24,44,2048\nMaaa 104,180,220\nMaaa 104,176,216\nMaaa 108,184,216\nMaaa 108,188,220\ns_add_u32 s6, s53, 0\n"
"Sv 232,1,16,6,3072\nMaaa 76,188,212\nMaaa 76,184,208\nMaaa 44,184,200\nMaaa 44,188,204\nD0 9,24,44,3072\nMaaa 12,188,196\nMaaa 12,184,192\ns_waitcnt vmcnt(24) lgkmcnt(0)\n"
"s_barrier\nMaaa 16,224,192\nTa 160,12,0\nMaaa 16,228,196\nTa 162,12,2048\nMaaa 48,228,204\ns_add_u32 m0, s47, -16384\ns_add_u32 s48, s48, 128\nMaaa 48,224,200\nTa 164,12,16384\n"
"Maaa 80,224,208\nTa 166,12,18432\nMaaa 80,228,212\nD0 2,20,48,0\nMaaa 112,228,220\nTa 168,13,0\nMaaa 112,224,216\nTa 170,13,2048\nMaaa 116,232,216\nMaaa 116,236,220\n"
"Ta 172,13,16384\ns_add_u32 s6, s53, 4096\nSv 240,1,16,6,1024\nMaaa 84,236,212\nTa 174,13,18432\nMaaa 84,232,208\nMaaa 52,232,200\nTa 176,14,0\nMaaa 52,236,204\nTa 178,14,2048\n"
"Maaa 20,236,196\nD0 3,20,48,1024\nMaaa 20,232,192\nTa 180,14,16384\nMaaa 24,240,192\nTa 182,14,18432\nMaaa 24,244,196\ns_add_u32 s6, s53, 4096\nSv 248,1,16,6,3072\n"
"Maaa 56,244,204\nTa 184,15,0\nMaaa 56,240,200\nTa 186,15,2048\nMaaa 88,240,208\nMaaa 88,244,212\nTa 188,15,16384\nMaaa 120,244,220\nTa 190,15,18432\nMaaa 120,240,216\n"
"D0 4,20,48,2048\nMaaa 124,248,216\nMaaa 124,252,220\nMaaa 92,252,212\nMaaa 92,248,208\ns_lshl3_add_u32 s90, s12, s13\ns_lshl2_add_u32 s90, s90, s11\n"
"s_lshl2_add_u32 s90, s90, 0xc04a400\nMaaa 60,248,200\nMaaa 60,252,204\nD0 5,20,48,3072\nMaaa 28,252,196\nMaaa 28,248,192\nWL 0\ns_mov_b64 s[76:77], s[62:63]\n"
"s_mov_b64 s[78:79], s[64:65]\ns_mov_b32 s42, 1605632\ns_mov_b32 s50, 1\n.GL20:\nMvaa 64,160,128\nTa 224,16,0\nMvaa 64,164,132\nTa 226,16,2048\nMvaa 96,164,140\n"
"s_add_u32 m0, s47, -65536\ns_add_u32 s45, s45, 128\nMvaa 96,160,136\nTa 228,16,16384\nMvaa 128,160,144\nTa 230,16,18432\nMvaa 128,164,148\nD0 2,20,45,0\nMvaa 160,164,156\n"
"Ta 232,17,0\nMvaa 160,160,152\nTa 234,17,2048\nMvaa 164,168,152\nMvaa 164,172,156\nTa 236,17,16384\nMvaa 132,172,148\nTa 238,17,18432\nMvaa 132,168,144\nMvaa 100,168,136\n"
"Ta 240,18,0\nMvaa 100,172,140\nTa 242,18,2048\nMvaa 68,172,132\nD0 3,20,45,1024\nMvaa 68,168,128\nTa 244,18,16384\nMvaa 72,176,128\nTa 246,18,18432\nMvaa 72,180,132\n"
"Mvaa 104,180,140\nTa 248,19,0\nMvaa 104,176,136\nTa 250,19,2048\nMvaa 136,176,144\nMvaa 136,180,148\nTa 252,19,16384\nMvaa 168,180,156\nTa 254,19,18432\nMvaa 168,176,152\n"
"D0 4,20,45,2048\nMvaa 172,184,152\nMvaa 172,188,156\nMvaa 140,188,148\nMvaa 140,184,144\ns_mov_b32 s40, s76\ns_and_b32 s41, s77, 0xffff\nMvaa 108,184,136\nMvaa 108,188,140\n"
"D0 5,20,45,3072\nMvaa 76,188,132\nMvaa 76,184,128\ns_waitcnt vmcnt(17) lgkmcnt(0)\ns_barrier\nMvaa 80,224,128\nRa 192,10,16384\nMvaa 80,228,132\ns_add_u32 m0, s47, 0\n"
"s_add_u32 s44, s44, 0x40000\nbuffer_load_dword v31, v26, s[40:43], s97 offen\ns_mov_b64 s[76:77], s[66:67]\nMvaa 112,228,140\nRa 196,11,16384\nMvaa 112,224,136\nMvaa 144,224,144\n"
"Ra 200,10,18432\nMvaa 144,228,148\nD0 6,24,44,0\nMvaa 176,228,156\nRa 204,11,18432\nMvaa 176,224,152\nMvaa 180,232,152\nRa 208,10,24576\nMvaa 180,236,156\nMvaa 148,236,148\n"
"Ra 212,11,24576\nMvaa 148,232,144\nMvaa 116,232,136\nRa 216,10,26624\nMvaa 116,236,140\nD0 7,24,44,1024\nMvaa 84,236,132\nRa 220,11,26624\nMvaa 84,232,128\nMvaa 88,240,128\n"
"Mvaa 88,244,132\nMvaa 120,244,140\nMvaa 120,240,136\nMvaa 152,240,144\nMvaa 152,244,148\nD0 8,24,44,2048\nMvaa 184,244,156\nMvaa 184,240,152\nMvaa 188,248,152\nMvaa 188,252,156\n"
"Mvaa 156,252,148\nMvaa 156,248,144\nMvaa 124,248,136\nMvaa 124,252,140\nD0 9,24,44,3072\nMvaa 92,252,132\nMvaa 92,248,128\nWL 0\nMaaa 16,224,192\nRa 128,10,32768\nMaaa 16,228,196\n"
"s_add_u32 m0, s47, 16384\ns_add_u32 s44, s44, 0x40000\nMaaa 48,228,204\nRa 132,11,32768\nMaaa 48,224,200\nMaaa 80,224,208\nRa 136,10,34816\nMaaa 80,228,212\nD0 6,24,44,0\n"
"Maaa 112,228,220\nRa 140,11,34816\nMaaa 112,224,216\nMaaa 116,232,216\nRa 144,10,40960\nMaaa 116,236,220\nMaaa 84,236,212\nRa 148,11,40960\nMaaa 84,232,208\nMaaa 52,232,200\n"
"Ra 152,10,43008\nMaaa 52,236,204\nD0 7,24,44,1024\nMaaa 20,236,196\nRa 156,11,43008\nMaaa 20,232,192\nMaaa 24,240,192\nMaaa 24,244,196\nMaaa 56,244,204\nMaaa 56,240,200\n"
"Maaa 88,240,208\nMaaa 88,244,212\nD0 8,24,44,2048\nMaaa 120,244,220\nMaaa 120,240,216\nMaaa 124,248,216\nMaaa 124,252,220\nMaaa 92,252,212\nMaaa 92,248,208\nMaaa 60,248,200\n"
"Maaa 60,252,204\nD0 9,24,44,3072\nMaaa 28,252,196\nMaaa 28,248,192\ns_waitcnt vmcnt(17) lgkmcnt(0)\ns_barrier\nMaaa 0,160,192\nTa 224,16,32768\nMaaa 0,164,196\nTa 226,16,34816\n"
"Maaa 32,164,204\ns_add_u32 m0, s47, -49152\ns_add_u32 s48, s48, 128\nMaaa 32,160,200\nTa 228,16,49152\nMaaa 64,160,208\nTa 230,16,51200\nMaaa 64,164,212\nD0 2,20,48,0\n"
"Maaa 96,164,220\nTa 232,17,32768\nMaaa 96,160,216\nTa 234,17,34816\nMaaa 100,168,216\nMaaa 100,172,220\nTa 236,17,49152\nMaaa 68,172,212\nTa 238,17,51200\nMaaa 68,168,208\n"
"Maaa 36,168,200\nTa 240,18,32768\nMaaa 36,172,204\nTa 242,18,34816\nMaaa 4,172,196\nD0 3,20,48,1024\nMaaa 4,168,192\nTa 244,18,49152\nMaaa 8,176,192\nTa 246,18,51200\n"
"Maaa 8,180,196\nMaaa 40,180,204\nTa 248,19,32768\nMaaa 40,176,200\nTa 250,19,34816\nMaaa 72,176,208\nMaaa 72,180,212\nTa 252,19,49152\nMaaa 104,180,220\nTa 254,19,51200\n"
"Maaa 104,176,216\nD0 4,20,48,2048\nMaaa 108,184,216\nMaaa 108,188,220\nMaaa 76,188,212\nMaaa 76,184,208\nMaaa 44,184,200\nMaaa 44,188,204\nD0 5,20,48,3072\nMaaa 12,188,196\n"
"Maaa 12,184,192\nWL 0\nMvaa 80,224,128\nTa 160,12,32768\nMvaa 80,228,132\nTa 162,12,34816\nMvaa 112,228,140\ns_add_u32 m0, s47, -32768\ns_add_u32 s45, s45, 128\nMvaa 112,224,136\n"
"Ta 164,12,49152\nMvaa 144,224,144\nTa 166,12,51200\nMvaa 144,228,148\nD0 2,20,45,0\nMvaa 176,228,156\nTa 168,13,32768\nMvaa 176,224,152\nTa 170,13,34816\nMvaa 180,232,152\n"
"Mvaa 180,236,156\nTa 172,13,49152\nMvaa 148,236,148\nTa 174,13,51200\nMvaa 148,232,144\nMvaa 116,232,136\nTa 176,14,32768\nMvaa 116,236,140\nTa 178,14,34816\nMvaa 84,236,132\n"
"D0 3,20,45,1024\nMvaa 84,232,128\nTa 180,14,49152\nMvaa 88,240,128\nTa 182,14,51200\nMvaa 88,244,132\nMvaa 120,244,140\nTa 184,15,32768\nMvaa 120,240,136\nTa 186,15,34816\n"
"Mvaa 152,240,144\nMvaa 152,244,148\nTa 188,15,49152\nMvaa 184,244,156\nTa 190,15,51200\nMvaa 184,240,152\nD0 4,20,45,2048\nMvaa 188,248,152\nMvaa 188,252,156\nMvaa 156,252,148\n"
"Mvaa 156,248,144\ns_mov_b32 s40, s78\ns_and_b32 s41, s79, 0xffff\nMvaa 124,248,136\nMvaa 124,252,140\nD0 5,20,45,3072\nMvaa 92,252,132\nMvaa 92,248,128\n"
"s_waitcnt vmcnt(17) lgkmcnt(0)\ns_barrier\nMvaa 64,160,128\nRa 192,10,49152\nMvaa 64,164,132\ns_add_u32 m0, s47, 32768\ns_add_u32 s44, s44, 0x40000\n"
"buffer_load_dword v31, v26, s[40:43], s97 offen\ns_mov_b64 s[78:79], s[66:67]\nMvaa 96,164,140\nRa 196,11,49152\nMvaa 96,160,136\nMvaa 128,160,144\nRa 200,10,51200\n"
"Mvaa 128,164,148\nD0 6,24,44,0\nMvaa 160,164,156\nRa 204,11,51200\nMvaa 160,160,152\nMvaa 164,168,152\nRa 208,10,57344\nMvaa 164,172,156\nMvaa 132,172,148\nRa 212,11,57344\n"
"Mvaa 132,168,144\nMvaa 100,168,136\nRa 216,10,59392\nMvaa 100,172,140\nD0 7,24,44,1024\nMvaa 68,172,132\nRa 220,11,59392\nMvaa 68,168,128\nMvaa 72,176,128\nMvaa 72,180,132\n"
"Mvaa 104,180,140\nMvaa 104,176,136\nMvaa 136,176,144\nMvaa 136,180,148\nD0 8,24,44,2048\nMvaa 168,180,156\nMvaa 168,176,152\nMvaa 172,184,152\nMvaa 172,188,156\nMvaa 140,188,148\n"
"Mvaa 140,184,144\nMvaa 108,184,136\nMvaa 108,188,140\nD0 9,24,44,3072\nMvaa 76,188,132\nMvaa 76,184,128\nWL 0\nMaaa 0,160,192\nRa 128,10,0\nMaaa 0,164,196\n"
"s_add_u32 m0, s47, 49152\ns_add_u32 s44, s44, 0x40000\nMaaa 32,164,204\nRa 132,11,0\nMaaa 32,160,200\nMaaa 64,160,208\nRa 136,10,2048\nMaaa 64,164,212\nD0 6,24,44,0\n"
"Maaa 96,164,220\nRa 140,11,2048\nMaaa 96,160,216\nMaaa 100,168,216\nRa 144,10,8192\nMaaa 100,172,220\nMaaa 68,172,212\nRa 148,11,8192\nMaaa 68,168,208\nMaaa 36,168,200\n"
"Ra 152,10,10240\nMaaa 36,172,204\nD0 7,24,44,1024\nMaaa 4,172,196\nRa 156,11,10240\nMaaa 4,168,192\nMaaa 8,176,192\nMaaa 8,180,196\nMaaa 40,180,204\nMaaa 40,176,200\n"
"Maaa 72,176,208\nMaaa 72,180,212\nD0 8,24,44,2048\nMaaa 104,180,220\nMaaa 104,176,216\nMaaa 108,184,216\nMaaa 108,188,220\nMaaa 76,188,212\nMaaa 76,184,208\nMaaa 44,184,200\n"
"Maaa 44,188,204\nD0 9,24,44,3072\nMaaa 12,188,196\nMaaa 12,184,192\ns_waitcnt vmcnt(17) lgkmcnt(0)\ns_barrier\nMaaa 16,224,192\nTa 160,12,0\nMaaa 16,228,196\nTa 162,12,2048\n"
"Maaa 48,228,204\ns_add_u32 m0, s47, -16384\ns_add_u32 s48, s48, 128\nMaaa 48,224,200\nTa 164,12,16384\nMaaa 80,224,208\nTa 166,12,18432\nMaaa 80,228,212\nD0 2,20,48,0\n"
"Maaa 112,228,220\nTa 168,13,0\nMaaa 112,224,216\nTa 170,13,2048\nMaaa 116,232,216\nMaaa 116,236,220\nTa 172,13,16384\nMaaa 84,236,212\nTa 174,13,18432\nMaaa 84,232,208\n"
"Maaa 52,232,200\nTa 176,14,0\nMaaa 52,236,204\nTa 178,14,2048\n.W542:\ns_cbranch_execz .W546\nMaaa 20,236,196\nD0 3,20,48,1024\nMaaa 20,232,192\nTa 180,14,16384\nMaaa 24,240,192\n"
"Ta 182,14,18432\nMaaa 24,244,196\nMaaa 56,244,204\nTa 184,15,0\nMaaa 56,240,200\nTa 186,15,2048\nMaaa 88,240,208\nMaaa 88,244,212\nTa 188,15,16384\nMaaa 120,244,220\n"
"Ta 190,15,18432\nMaaa 120,240,216\nD0 4,20,48,2048\nMaaa 124,248,216\nMaaa 124,252,220\nMaaa 92,252,212\nMaaa 92,248,208\nMaaa 60,248,200\nMaaa 60,252,204\nD0 5,20,48,3072\n"
"s_sub_u32 s50, s50, 1\nMaaa 28,252,196\nMaaa 28,248,192\nWL 0\ns_cbranch_scc0 .GL20\nMvaa 64,160,128\nTa 224,16,0\nMvaa 64,164,132\nTa 226,16,2048\nMvaa 96,164,140\n"
"s_add_u32 m0, s47, -65536\ns_add_u32 s45, s45, 128\nMvaa 96,160,136\nTa 228,16,16384\nMvaa 128,160,144\nTa 230,16,18432\nMvaa 128,164,148\nD0 2,20,45,0\nMvaa 160,164,156\n"
"Ta 232,17,0\nMvaa 160,160,152\nTa 234,17,2048\nMvaa 164,168,152\ns_and_b32 s2, s74, 15\nMvaa 164,172,156\nTa 236,17,16384\n"
"buffer_store_dword v25, off, s[16:19], s90 offset:0 sc0 sc1\nMvaa 132,172,148\nTa 238,17,18432\nMvaa 132,168,144\ns_sub_u32 s2, s2, 2\ns_cmp_lt_u32 s2, 14\n"
"s_cselect_b32 s5, 0, 0x40000000\nMvaa 100,168,136\nTa 240,18,0\nMvaa 100,172,140\nTa 242,18,2048\nMvaa 68,172,132\nD0 3,20,45,1024\nMvaa 68,168,128\nTa 244,18,16384\n"
"Mvaa 72,176,128\nTa 246,18,18432\nMvaa 72,180,132\ns_and_b32 s2, s2, 15\nMvaa 104,180,140\nTa 248,19,0\nMvaa 104,176,136\nTa 250,19,2048\nMvaa 136,176,144\ns_mul_i32 s4, s73, 14\n"
"Mvaa 136,180,148\nTa 252,19,16384\nMvaa 168,180,156\nTa 254,19,18432\nMvaa 168,176,152\nD0 4,20,45,2048\nMvaa 172,184,152\ns_add_u32 s4, s4, s2\nMvaa 172,188,156\n"
"s_mov_b32 s36, s62\nMvaa 140,188,148\ns_and_b32 s37, s63, 0xffff\nMvaa 140,184,144\ns_mov_b32 s38, 0x188000\nMvaa 108,184,136\ns_mov_b32 s39, 0x20000\nMvaa 108,188,140\n"
"D0 5,20,45,3072\nMvaa 76,188,132\ns_mov_b32 s40, s64\nMvaa 76,184,128\ns_waitcnt vmcnt(18) lgkmcnt(0)\ns_barrier\nMvaa 80,224,128\nRa 192,10,16384\nMvaa 80,228,132\n"
"s_add_u32 m0, s47, 0\ns_add_u32 s44, s44, 0x40000\nMvaa 112,228,140\nRa 196,11,16384\nMvaa 112,224,136\ns_and_b32 s41, s65, 0xffff\nMvaa 144,224,144\nRa 200,10,18432\n"
"Mvaa 144,228,148\nD0 6,24,44,0\nMvaa 176,228,156\nRa 204,11,18432\nMvaa 176,224,152\ns_mov_b32 s42, 0x188000\nMvaa 180,232,152\nRa 208,10,24576\nMvaa 180,236,156\n"
"s_mov_b32 s43, 0x20000\nMvaa 148,236,148\nRa 212,11,24576\nMvaa 148,232,144\ns_mov_b32 s92, s66\nMvaa 116,232,136\nRa 216,10,26624\nMvaa 116,236,140\nD0 7,24,44,1024\n"
"Mvaa 84,236,132\nRa 220,11,26624\nMvaa 84,232,128\ns_and_b32 s93, s67, 0xffff\nMvaa 88,240,128\ns_mov_b32 s94, 0x188000\nMvaa 88,244,132\ns_mov_b32 s95, 0x20000\nMvaa 120,244,140\n"
"v_lshrrev_b32 v252, 1, v0\nMvaa 120,240,136\nv_mul_u32_u24 v253, 14336, v252\nMvaa 152,240,144\nv_and_b32 v254, 1, v0\nMvaa 152,244,148\nD0 8,24,44,2048\nMvaa 184,244,156\n"
"v_lshl_add_u32 v253, v254, 4, v253\nMvaa 184,240,152\nv_mov_b32 v254, 0x40000000\nv_cmp_gt_u32 vcc, 56, v0\nv_lshlrev_b32 v252, 3, v252\ns_mul_i32 s2, s11, 401408\n"
"v_cndmask_b32 v21, v254, v253, vcc\nv_cndmask_b32 v255, v254, v252, vcc\nMvaa 188,248,152\ns_lshl_b32 s3, s4, 5\nMvaa 188,252,156\ns_add_u32 s2, s2, s3\nMvaa 156,252,148\n"
"s_or_b32 s86, s2, s5\nMvaa 156,248,144\ns_add_u32 s87, s86, 7168\nMvaa 124,248,136\nv_lshrrev_b32 v251, 4, v0\nMvaa 124,252,140\nD0 9,24,44,3072\nMvaa 92,252,132\n"
"v_mul_u32_u24 v252, 3584, v251\nMvaa 92,248,128\nWL 0\nMaaa 16,224,192\nRa 128,10,32768\nMaaa 16,228,196\ns_add_u32 m0, s47, 16384\ns_add_u32 s44, s44, 0x40000\nMaaa 48,228,204\n"
"Ra 132,11,32768\nMaaa 48,224,200\nv_and_b32 v251, 15, v0\nMaaa 80,224,208\nRa 136,10,34816\nMaaa 80,228,212\nD0 6,24,44,0\nMaaa 112,228,220\nRa 140,11,34816\nMaaa 112,224,216\n"
"v_lshl_add_u32 v252, v251, 4, v252\nv_cmp_gt_u32 vcc, 14, v251\ns_mul_i32 s2, s4, 14336\ns_mul_i32 s3, s11, 224\nv_cndmask_b32 v20, v254, v252, vcc\nMaaa 116,232,216\n"
"Ra 144,10,40960\nMaaa 116,236,220\ns_add_u32 s2, s2, s3\nMaaa 84,236,212\nRa 148,11,40960\nMaaa 84,232,208\ns_or_b32 s88, s2, s5\nMaaa 52,232,200\nRa 152,10,43008\n"
"Maaa 52,236,204\nD0 7,24,44,1024\nMaaa 20,236,196\nRa 156,11,43008\nMaaa 20,232,192\nMaaa 24,240,192\nMaaa 24,244,196\nMaaa 56,244,204\nMaaa 56,240,200\nMaaa 88,240,208\n"
"Maaa 88,244,212\nD0 8,24,44,2048\nMaaa 120,244,220\nMaaa 120,240,216\nMaaa 124,248,216\nMaaa 124,252,220\nMaaa 92,252,212\nMaaa 92,248,208\nMaaa 60,248,200\nMaaa 60,252,204\n"
"D0 9,24,44,3072\nMaaa 28,252,196\nMaaa 28,248,192\ns_waitcnt vmcnt(17) lgkmcnt(0)\ns_barrier\nMaaa 0,160,192\nTa 224,16,32768\nMaaa 0,164,196\nTa 226,16,34816\n"
"buffer_load_dwordx4 v[192:195], v21, s[36:39], s86 offen offset:0\nMaaa 32,164,204\ns_add_u32 m0, s47, -49152\ns_add_u32 s48, s48, 128\nMaaa 32,160,200\nTa 228,16,49152\n"
"s_lshl_b32 s6, s11, 12\ns_add_u32 s6, s6, 0xc044000\nMaaa 64,160,208\nTa 230,16,51200\nMaaa 64,164,212\nD0 2,20,48,0\nMaaa 96,164,220\nTa 232,17,32768\nMaaa 96,160,216\n"
"Ta 234,17,34816\nMaaa 100,168,216\nbuffer_load_dwordx4 v[32:35], v1, s[16:19], s6 offen offset:0 sc0 sc1\nMaaa 100,172,220\nTa 236,17,49152\nMaaa 68,172,212\nTa 238,17,51200\n"
"Maaa 68,168,208\nMaaa 36,168,200\nTa 240,18,32768\nMaaa 36,172,204\nTa 242,18,34816\nMaaa 4,172,196\nD0 3,20,48,1024\nMaaa 4,168,192\nTa 244,18,49152\nMaaa 8,176,192\n"
"Ta 246,18,51200\nMaaa 8,180,196\nbuffer_load_dwordx4 v[36:39], v1, s[16:19], s6 offen offset:1024 sc0 sc1\nMaaa 40,180,204\nTa 248,19,32768\nMaaa 40,176,200\nTa 250,19,34816\n"
"Maaa 72,176,208\nMaaa 72,180,212\nTa 252,19,49152\nMaaa 104,180,220\nTa 254,19,51200\nMaaa 104,176,216\nD0 4,20,48,2048\nMaaa 108,184,216\n"
"buffer_load_dwordx4 v[40:43], v1, s[16:19], s6 offen offset:2048 sc0 sc1\nMaaa 108,188,220\nMaaa 76,188,212\nMaaa 76,184,208\nMaaa 44,184,200\n"
"buffer_load_dwordx4 v[44:47], v1, s[16:19], s6 offen offset:3072 sc0 sc1\nMaaa 44,188,204\nD0 5,20,48,3072\nMaaa 12,188,196\nMaaa 12,184,192\nWL 0\nMvaa 80,224,128\n"
"Ta 160,12,32768\nMvaa 80,228,132\nTa 162,12,34816\nMvaa 112,228,140\ns_add_u32 m0, s47, -32768\ns_add_u32 s45, s45, 128\nMvaa 112,224,136\nTa 164,12,49152\nMvaa 144,224,144\n"
"Ta 166,12,51200\nMvaa 144,228,148\nD0 2,20,45,0\nMvaa 176,228,156\nTa 168,13,32768\nMvaa 176,224,152\nTa 170,13,34816\nMvaa 180,232,152\nMvaa 180,236,156\nTa 172,13,49152\n"
"Mvaa 148,236,148\nTa 174,13,51200\nMvaa 148,232,144\nMvaa 116,232,136\nTa 176,14,32768\nMvaa 116,236,140\nTa 178,14,34816\nMvaa 84,236,132\nD0 3,20,45,1024\nMvaa 84,232,128\n"
"Ta 180,14,49152\nMvaa 88,240,128\nTa 182,14,51200\nMvaa 88,244,132\nMvaa 120,244,140\nTa 184,15,32768\nMvaa 120,240,136\nTa 186,15,34816\nMvaa 152,240,144\nMvaa 152,244,148\n"
"Ta 188,15,49152\nMvaa 184,244,156\nTa 190,15,51200\nMvaa 184,240,152\nD0 4,20,45,2048\nMvaa 188,248,152\nMvaa 188,252,156\nMvaa 156,252,148\nMvaa 156,248,144\nMvaa 124,248,136\n"
"Mvaa 124,252,140\nD0 5,20,45,3072\nMvaa 92,252,132\nMvaa 92,248,128\ns_waitcnt vmcnt(21) lgkmcnt(0)\ns_barrier\nMvaa 64,160,128\nRa 192,10,49152\nMvaa 64,164,132\n"
"s_add_u32 m0, s47, 32768\ns_add_u32 s44, s44, 0x40000\nbuffer_load_dwordx4 v[208:211], v21, s[40:43], s86 offen offset:0\nMvaa 96,164,140\nRa 196,11,49152\nMvaa 96,160,136\n"
"Mvaa 128,160,144\nRa 200,10,51200\nMvaa 128,164,148\nD0 6,24,44,0\nMvaa 160,164,156\nRa 204,11,51200\nMvaa 160,160,152\nMvaa 164,168,152\nRa 208,10,57344\nMvaa 164,172,156\n"
"Mvaa 132,172,148\nRa 212,11,57344\nMvaa 132,168,144\nMvaa 100,168,136\nRa 216,10,59392\nMvaa 100,172,140\nD0 7,24,44,1024\nMvaa 68,172,132\nRa 220,11,59392\nMvaa 68,168,128\n"
"Mvaa 72,176,128\nMvaa 72,180,132\nMvaa 104,180,140\nMvaa 104,176,136\nMvaa 136,176,144\nMvaa 136,180,148\nD0 8,24,44,2048\nMvaa 168,180,156\nMvaa 168,176,152\nMvaa 172,184,152\n"
"Mvaa 172,188,156\nMvaa 140,188,148\nMvaa 140,184,144\nMvaa 108,184,136\nMvaa 108,188,140\nD0 9,24,44,3072\nMvaa 76,188,132\nMvaa 76,184,128\nWL 0\nMaaa 0,160,192\nRa 128,10,0\n"
"Maaa 0,164,196\ns_add_u32 m0, s47, 49152\ns_add_u32 s44, s44, 0x40000\nMaaa 32,164,204\nRa 132,11,0\nMaaa 32,160,200\nMaaa 64,160,208\nRa 136,10,2048\nMaaa 64,164,212\n"
"D0 6,24,44,0\nMaaa 96,164,220\nRa 140,11,2048\nMaaa 96,160,216\nMaaa 100,168,216\nRa 144,10,8192\nMaaa 100,172,220\nMaaa 68,172,212\nRa 148,11,8192\nMaaa 68,168,208\n"
"Maaa 36,168,200\nRa 152,10,10240\nMaaa 36,172,204\nD0 7,24,44,1024\nMaaa 4,172,196\nRa 156,11,10240\nMaaa 4,168,192\nMaaa 8,176,192\nMaaa 8,180,196\nMaaa 40,180,204\n"
"Maaa 40,176,200\nMaaa 72,176,208\nMaaa 72,180,212\nD0 8,24,44,2048\nMaaa 104,180,220\nMaaa 104,176,216\nMaaa 108,184,216\nMaaa 108,188,220\nMaaa 76,188,212\nMaaa 76,184,208\n"
"Maaa 44,184,200\nMaaa 44,188,204\nD0 9,24,44,3072\nMaaa 12,188,196\nMaaa 12,184,192\ns_waitcnt vmcnt(14) lgkmcnt(0)\ns_barrier\nMaaa 16,224,192\nTa 160,12,0\n"
"v_xor_b32 v48, s15, v33\nMaaa 16,228,196\nTa 162,12,2048\nbuffer_load_dwordx4 v[196:199], v21, s[36:39], s86 offen offset:3584\nMaaa 48,228,204\ns_add_u32 m0, s47, -16384\n"
"s_add_u32 s48, s48, 128\nMaaa 48,224,200\nTa 164,12,16384\nMaaa 80,224,208\nTa 166,12,18432\nMaaa 80,228,212\nD0 2,20,48,0\nMaaa 112,228,220\nTa 168,13,0\nMaaa 112,224,216\n"
"Ta 170,13,2048\nMaaa 116,232,216\nv_xor_b32 v49, s15, v35\nMaaa 116,236,220\nTa 172,13,16384\nMaaa 84,236,212\nTa 174,13,18432\nMaaa 84,232,208\nv_xor_b32 v50, s15, v37\n"
"Maaa 52,232,200\nTa 176,14,0\nMaaa 52,236,204\nTa 178,14,2048\nMaaa 20,236,196\nD0 3,20,48,1024\nMaaa 20,232,192\nTa 180,14,16384\nMaaa 24,240,192\nTa 182,14,18432\n"
"v_xor_b32 v51, s15, v39\nMaaa 24,244,196\nv_xor_b32 v52, s15, v41\nMaaa 56,244,204\nTa 184,15,0\nMaaa 56,240,200\nTa 186,15,2048\nMaaa 88,240,208\nv_xor_b32 v53, s15, v43\n"
"Maaa 88,244,212\nTa 188,15,16384\nMaaa 120,244,220\nTa 190,15,18432\nMaaa 120,240,216\nD0 4,20,48,2048\nMaaa 124,248,216\nv_xor_b32 v54, s15, v45\nMaaa 124,252,220\n"
"v_xor_b32 v55, s15, v47\nMaaa 92,252,212\nv_or3_b32 v48, v48, v49, v50\nMaaa 92,248,208\nv_or3_b32 v51, v51, v52, v53\nMaaa 60,248,200\nv_or3_b32 v48, v48, v54, v55\n"
"Maaa 60,252,204\nD0 5,20,48,3072\nMaaa 28,252,196\nv_or_b32 v48, v48, v51\nMaaa 28,248,192\nWL 0\nMvaa 64,160,128\nTa 224,16,0\nv_cmp_ne_u32 vcc, 0, v48\nMvaa 64,164,132\n"
"Ta 226,16,2048\nMvaa 96,164,140\ns_add_u32 m0, s47, -65536\ns_add_u32 s45, s45, 128\nMvaa 96,160,136\nTa 228,16,16384\nMvaa 128,160,144\nTa 230,16,18432\nMvaa 128,164,148\n"
"D0 2,20,45,0\nMvaa 160,164,156\nTa 232,17,0\nMvaa 160,160,152\nTa 234,17,2048\nMvaa 164,168,152\ns_cbranch_vccnz .S14\n.S15:\nMvaa 164,172,156\nTa 236,17,16384\nMvaa 132,172,148\n"
"Ta 238,17,18432\nMvaa 132,168,144\nv_max_u32 v48, v32, v34\nMvaa 100,168,136\nTa 240,18,0\nMvaa 100,172,140\nTa 242,18,2048\nMvaa 68,172,132\nD0 3,20,45,1024\n.W543:\n"
"s_cbranch_execz .W547\nMvaa 68,168,128\nTa 244,18,16384\nMvaa 72,176,128\nTa 246,18,18432\nv_min_u32 v49, v32, v34\nMvaa 72,180,132\nv_med3_u32 v49, v48, v49, v36\n"
"Mvaa 104,180,140\nTa 248,19,0\nMvaa 104,176,136\nTa 250,19,2048\nMvaa 136,176,144\nv_max_u32 v48, v48, v36\nMvaa 136,180,148\nTa 252,19,16384\nMvaa 168,180,156\nTa 254,19,18432\n"
"Mvaa 168,176,152\nD0 4,20,45,2048\nMvaa 172,184,152\nv_med3_u32 v49, v48, v49, v38\nMvaa 172,188,156\nv_max_u32 v48, v48, v38\nMvaa 140,188,148\nv_med3_u32 v49, v48, v49, v40\n"
"Mvaa 140,184,144\nv_max_u32 v48, v48, v40\nMvaa 108,184,136\nv_med3_u32 v49, v48, v49, v42\nMvaa 108,188,140\nD0 5,20,45,3072\nMvaa 76,188,132\nv_max_u32 v48, v48, v42\n"
"Mvaa 76,184,128\ns_waitcnt vmcnt(18) lgkmcnt(0)\ns_barrier\nMvaa 80,224,128\nRa 192,10,16384\nv_med3_u32 v49, v48, v49, v44\nMvaa 80,228,132\ns_add_u32 m0, s47, 0\n"
"s_add_u32 s44, s44, 0x40000\nbuffer_load_dwordx4 v[212:215], v21, s[40:43], s86 offen offset:3584\nMvaa 112,228,140\nRa 196,11,16384\nMvaa 112,224,136\nv_max_u32 v48, v48, v44\n"
"Mvaa 144,224,144\nRa 200,10,18432\nMvaa 144,228,148\nD0 6,24,44,0\nMvaa 176,228,156\nRa 204,11,18432\nMvaa 176,224,152\nv_med3_u32 v49, v48, v49, v46\nMvaa 180,232,152\n"
"Ra 208,10,24576\nMvaa 180,236,156\nv_max_u32 v48, v48, v46\nMvaa 148,236,148\nRa 212,11,24576\nMvaa 148,232,144\nv_mov_b32 v50, v48\nMvaa 116,232,136\nRa 216,10,26624\n"
"Mvaa 116,236,140\nD0 7,24,44,1024\nMvaa 84,236,132\nRa 220,11,26624\nMvaa 84,232,128\nv_max_u32_dpp v50, v50, v50 row_ror:8 row_mask:0xf bank_mask:0xf\nMvaa 88,240,128\n"
"Mvaa 88,244,132\nv_max_u32_dpp v50, v50, v50 row_ror:4 row_mask:0xf bank_mask:0xf\nMvaa 120,244,140\nMvaa 120,240,136\n"
"v_max_u32_dpp v50, v50, v50 row_ror:2 row_mask:0xf bank_mask:0xf\nMvaa 152,240,144\nMvaa 152,244,148\nD0 8,24,44,2048\nMvaa 184,244,156\n"
"v_max_u32_dpp v50, v50, v50 row_ror:1 row_mask:0xf bank_mask:0xf\nMvaa 184,240,152\nv_mov_b32 v52, v50\nMvaa 188,248,152\nMvaa 188,252,156\nv_permlane16_swap_b32 v50, v52\n"
"Mvaa 156,252,148\nv_max_u32 v50, v50, v52\nMvaa 156,248,144\nv_mov_b32 v52, v50\nMvaa 124,248,136\nMvaa 124,252,140\nD0 9,24,44,3072\nMvaa 92,252,132\n"
"v_permlane32_swap_b32 v50, v52\nMvaa 92,248,128\nWL 0\nMaaa 16,224,192\nRa 128,10,32768\nv_max_u32 v50, v50, v52\nMaaa 16,228,196\ns_add_u32 m0, s47, 16384\n"
"s_add_u32 s44, s44, 0x40000\nMaaa 48,228,204\nRa 132,11,32768\nv_cmp_eq_u32 vcc, v48, v50\ns_lshl_b32 s2, s11, 3\ns_add_u32 s2, s2, 131072\nv_cndmask_b32 v51, v48, v49, vcc\n"
"Maaa 48,224,200\nMaaa 80,224,208\nRa 136,10,34816\nMaaa 80,228,212\nD0 6,24,44,0\nMaaa 112,228,220\nRa 140,11,34816\nMaaa 112,224,216\n"
"v_max_u32_dpp v51, v51, v51 row_ror:8 row_mask:0xf bank_mask:0xf\nMaaa 116,232,216\nRa 144,10,40960\nMaaa 116,236,220\n"
"v_max_u32_dpp v51, v51, v51 row_ror:4 row_mask:0xf bank_mask:0xf\nMaaa 84,236,212\nRa 148,11,40960\nMaaa 84,232,208\n"
"v_max_u32_dpp v51, v51, v51 row_ror:2 row_mask:0xf bank_mask:0xf\nMaaa 52,232,200\nRa 152,10,43008\nMaaa 52,236,204\nD0 7,24,44,1024\nMaaa 20,236,196\nRa 156,11,43008\n"
"Maaa 20,232,192\nv_max_u32_dpp v51, v51, v51 row_ror:1 row_mask:0xf bank_mask:0xf\nMaaa 24,240,192\nv_mov_b32 v52, v51\nMaaa 24,244,196\nMaaa 56,244,204\n"
"v_permlane16_swap_b32 v51, v52\nMaaa 56,240,200\nv_max_u32 v51, v51, v52\nMaaa 88,240,208\nv_mov_b32 v52, v51\nMaaa 88,244,212\nD0 8,24,44,2048\nMaaa 120,244,220\n"
"v_permlane32_swap_b32 v51, v52\nMaaa 120,240,216\nv_max_u32 v51, v51, v52\nMaaa 124,248,216\nv_mov_b32 v52, s2\nMaaa 124,252,220\nds_write_b64 v52, v[50:51]\nMaaa 92,252,212\n"
"v_mov_b32 v52, 131072\nMaaa 92,248,208\nMaaa 60,248,200\nMaaa 60,252,204\nD0 9,24,44,3072\nMaaa 28,252,196\nMaaa 28,248,192\ns_waitcnt vmcnt(18) lgkmcnt(0)\ns_barrier\n"
"Maaa 0,160,192\nTa 224,16,32768\nMaaa 0,164,196\nTa 226,16,34816\nbuffer_load_dwordx4 v[200:203], v21, s[36:39], s87 offen offset:0\nMaaa 32,164,204\ns_add_u32 m0, s47, -49152\n"
"s_add_u32 s48, s48, 128\nMaaa 32,160,200\nTa 228,16,49152\nMaaa 64,160,208\nTa 230,16,51200\nMaaa 64,164,212\nD0 2,20,48,0\nMaaa 96,164,220\nTa 232,17,32768\nMaaa 96,160,216\n"
"Ta 234,17,34816\nMaaa 100,168,216\nRv 32,52,0\nMaaa 100,172,220\nTa 236,17,49152\nMaaa 68,172,212\nTa 238,17,51200\nMaaa 68,168,208\nRv 36,52,16\nMaaa 36,168,200\nTa 240,18,32768\n"
"Maaa 36,172,204\nTa 242,18,34816\nMaaa 4,172,196\nD0 3,20,48,1024\nMaaa 4,168,192\nTa 244,18,49152\nMaaa 8,176,192\nTa 246,18,51200\nMaaa 8,180,196\nMaaa 40,180,204\n"
"Ta 248,19,32768\nMaaa 40,176,200\nTa 250,19,34816\nMaaa 72,176,208\nMaaa 72,180,212\nTa 252,19,49152\nMaaa 104,180,220\nTa 254,19,51200\nMaaa 104,176,216\nD0 4,20,48,2048\n"
"Maaa 108,184,216\nWL 11\nMaaa 108,188,220\nv_max_u32 v48, v32, v33\nMaaa 76,188,212\nv_min_u32 v49, v32, v33\nMaaa 76,184,208\nv_med3_u32 v49, v48, v49, v34\nMaaa 44,184,200\n"
"v_max_u32 v48, v48, v34\nMaaa 44,188,204\nD0 5,20,48,3072\nMaaa 12,188,196\nv_med3_u32 v49, v48, v49, v35\nMaaa 12,184,192\nWL 0\nMvaa 80,224,128\nTa 160,12,32768\n"
"Mvaa 80,228,132\nTa 162,12,34816\nMvaa 112,228,140\ns_add_u32 m0, s47, -32768\ns_add_u32 s45, s45, 128\nMvaa 112,224,136\nTa 164,12,49152\nMvaa 144,224,144\nTa 166,12,51200\n"
"Mvaa 144,228,148\nD0 2,20,45,0\nMvaa 176,228,156\nTa 168,13,32768\nMvaa 176,224,152\nTa 170,13,34816\nMvaa 180,232,152\nv_max_u32 v48, v48, v35\nMvaa 180,236,156\nTa 172,13,49152\n"
"Mvaa 148,236,148\nTa 174,13,51200\nMvaa 148,232,144\nv_med3_u32 v49, v48, v49, v36\nMvaa 116,232,136\nTa 176,14,32768\nMvaa 116,236,140\nTa 178,14,34816\nMvaa 84,236,132\n"
"D0 3,20,45,1024\nMvaa 84,232,128\nTa 180,14,49152\nMvaa 88,240,128\nTa 182,14,51200\nMvaa 88,244,132\nv_max_u32 v48, v48, v36\nMvaa 120,244,140\nTa 184,15,32768\nMvaa 120,240,136\n"
"Ta 186,15,34816\nMvaa 152,240,144\nv_med3_u32 v49, v48, v49, v37\nMvaa 152,244,148\nTa 188,15,49152\nMvaa 184,244,156\nTa 190,15,51200\nMvaa 184,240,152\nD0 4,20,45,2048\n"
"Mvaa 188,248,152\nv_max_u32 v48, v48, v37\nMvaa 188,252,156\nv_med3_u32 v49, v48, v49, v38\nMvaa 156,252,148\nv_max_u32 v48, v48, v38\nMvaa 156,248,144\n"
"v_med3_u32 v49, v48, v49, v39\nMvaa 124,248,136\nv_max_u32 v48, v48, v39\nMvaa 124,252,140\nD0 5,20,45,3072\nMvaa 92,252,132\nv_readfirstlane_b32 s2, v48\nMvaa 92,248,128\n"
"s_waitcnt vmcnt(18) lgkmcnt(0)\ns_barrier\nMvaa 64,160,128\nRa 192,10,49152\nMvaa 64,164,132\ns_add_u32 m0, s47, 32768\ns_add_u32 s44, s44, 0x40000\n"
"buffer_load_dwordx4 v[216:219], v21, s[40:43], s87 offen offset:0\nMvaa 96,164,140\nRa 196,11,49152\nMvaa 96,160,136\nv_readfirstlane_b32 s3, v49\nMvaa 128,160,144\n"
"Ra 200,10,51200\nMvaa 128,164,148\nD0 6,24,44,0\nMvaa 160,164,156\nRa 204,11,51200\nMvaa 160,160,152\ns_andn2_b32 s2, 0x7ff, s2\nMvaa 164,168,152\nRa 208,10,57344\n"
"Mvaa 164,172,156\ns_andn2_b32 s3, 0x7ff, s3\nMvaa 132,172,148\nRa 212,11,57344\nMvaa 132,168,144\ns_lshl_b32 s71, s3, 16\nMvaa 100,168,136\nRa 216,10,59392\nMvaa 100,172,140\n"
"D0 7,24,44,1024\nMvaa 68,172,132\nRa 220,11,59392\nMvaa 68,168,128\ns_or_b32 s71, s71, s2\nMvaa 72,176,128\ns_mul_i32 s2, s2, 896\nMvaa 72,180,132\ns_mul_i32 s3, s3, 896\n"
"Mvaa 104,180,140\ns_mul_i32 s6, s11, 224\nMvaa 104,176,136\ns_add_u32 s2, s2, s6\nMvaa 136,176,144\ns_add_u32 s3, s3, s6\nMvaa 136,180,148\nD0 8,24,44,2048\nMvaa 168,180,156\n"
"buffer_load_dwordx2 v[240:241], v255, s[28:31], s2 offen\nMvaa 168,176,152\nMvaa 172,184,152\nMvaa 172,188,156\nMvaa 140,188,148\n"
"buffer_load_dwordx2 v[242:243], v255, s[28:31], s3 offen\ns_lshl2_add_u32 s2, s10, s11\ns_cmp_eq_u32 s2, 512\ns_cselect_b32 s2, 0, 0x40000000\ns_add_u32 s2, s2, 0xc048000\n"
"Mvaa 140,184,144\nMvaa 108,184,136\nMvaa 108,188,140\nD0 9,24,44,3072\nMvaa 76,188,132\nbuffer_store_dword v25, off, s[16:19], s2 offset:0 sc0 sc1\nMvaa 76,184,128\nWL 0\n"
"Maaa 0,160,192\nRa 128,10,0\nMaaa 0,164,196\ns_add_u32 m0, s47, 49152\ns_add_u32 s44, s44, 0x40000\nMaaa 32,164,204\nRa 132,11,0\nMaaa 32,160,200\nMaaa 64,160,208\nRa 136,10,2048\n"
"Maaa 64,164,212\nD0 6,24,44,0\nMaaa 96,164,220\nRa 140,11,2048\nMaaa 96,160,216\nMaaa 100,168,216\nRa 144,10,8192\nMaaa 100,172,220\nMaaa 68,172,212\nRa 148,11,8192\n"
"Maaa 68,168,208\nMaaa 36,168,200\nRa 152,10,10240\nMaaa 36,172,204\nD0 7,24,44,1024\nMaaa 4,172,196\nRa 156,11,10240\nMaaa 4,168,192\nMaaa 8,176,192\nMaaa 8,180,196\n"
"Maaa 40,180,204\nMaaa 40,176,200\nMaaa 72,176,208\nMaaa 72,180,212\nD0 8,24,44,2048\nMaaa 104,180,220\nMaaa 104,176,216\nMaaa 108,184,216\nMaaa 108,188,220\nMaaa 76,188,212\n"
"Maaa 76,184,208\nMaaa 44,184,200\nMaaa 44,188,204\nD0 9,24,44,3072\nMaaa 12,188,196\nMaaa 12,184,192\ns_waitcnt vmcnt(21) lgkmcnt(0)\ns_barrier\nMaaa 16,224,192\nTa 160,12,0\n"
"Maaa 16,228,196\nTa 162,12,2048\nbuffer_load_dwordx4 v[204:207], v21, s[36:39], s87 offen offset:3584\nMaaa 48,228,204\ns_add_u32 m0, s47, -16384\ns_add_u32 s48, s48, 128\n"
"s_lshl_b32 s5, s73, 8\ns_add_u32 s5, s5, 0xc04ac00\nv_lshrrev_b32 v27, 2, v1\nbuffer_load_dword v27, v27, s[16:19], s5 offen sc0 sc1\nMaaa 48,224,200\nTa 164,12,16384\n"
"Maaa 80,224,208\nTa 166,12,18432\nMaaa 80,228,212\nD0 2,20,48,0\nMaaa 112,228,220\nTa 168,13,0\nMaaa 112,224,216\nTa 170,13,2048\nMaaa 116,232,216\nMaaa 116,236,220\n"
"Ta 172,13,16384\nMaaa 84,236,212\nTa 174,13,18432\nMaaa 84,232,208\nMaaa 52,232,200\nTa 176,14,0\nMaaa 52,236,204\nTa 178,14,2048\nMaaa 20,236,196\nD0 3,20,48,1024\n"
"Maaa 20,232,192\nTa 180,14,16384\nMaaa 24,240,192\nTa 182,14,18432\nMaaa 24,244,196\nMaaa 56,244,204\nTa 184,15,0\nMaaa 56,240,200\nTa 186,15,2048\nMaaa 88,240,208\n"
"Maaa 88,244,212\nTa 188,15,16384\nMaaa 120,244,220\nTa 190,15,18432\nMaaa 120,240,216\nD0 4,20,48,2048\nMaaa 124,248,216\nMaaa 124,252,220\nMaaa 92,252,212\nMaaa 92,248,208\n"
"Maaa 60,248,200\nMaaa 60,252,204\nD0 5,20,48,3072\nMaaa 28,252,196\nMaaa 28,248,192\nWL 0\nMvaa 64,160,128\nTa 224,16,0\nMvaa 64,164,132\nTa 226,16,2048\nMvaa 96,164,140\n"
"s_add_u32 m0, s47, -65536\ns_add_u32 s45, s45, 128\nMvaa 96,160,136\nTa 228,16,16384\nMvaa 128,160,144\nTa 230,16,18432\nMvaa 128,164,148\nD0 2,20,45,0\nMvaa 160,164,156\n"
"Ta 232,17,0\nMvaa 160,160,152\nTa 234,17,2048\nMvaa 164,168,152\nMvaa 164,172,156\nTa 236,17,16384\nMvaa 132,172,148\nTa 238,17,18432\nMvaa 132,168,144\nMvaa 100,168,136\n"
"Ta 240,18,0\nMvaa 100,172,140\nTa 242,18,2048\nMvaa 68,172,132\nD0 3,20,45,1024\nMvaa 68,168,128\nTa 244,18,16384\nMvaa 72,176,128\nTa 246,18,18432\nMvaa 72,180,132\n"
"Mvaa 104,180,140\nTa 248,19,0\nMvaa 104,176,136\nTa 250,19,2048\nMvaa 136,176,144\nMvaa 136,180,148\nTa 252,19,16384\nMvaa 168,180,156\nTa 254,19,18432\nMvaa 168,176,152\n"
"D0 4,20,45,2048\nMvaa 172,184,152\nMvaa 172,188,156\nMvaa 140,188,148\nMvaa 140,184,144\nMvaa 108,184,136\nMvaa 108,188,140\nD0 5,20,45,3072\nMvaa 76,188,132\nMvaa 76,184,128\n"
"s_waitcnt vmcnt(22) lgkmcnt(0)\ns_barrier\nMvaa 80,224,128\nRa 192,10,16384\nWV 16\nMvaa 80,228,132\ns_add_u32 m0, s47, 0\ns_add_u32 s44, s44, 0x40000\n"
"buffer_load_dwordx4 v[220:223], v21, s[40:43], s87 offen offset:3584\nMvaa 112,228,140\nRa 196,11,16384\nv_lshlrev_b32 v244, 16, v240\nMvaa 112,224,136\n"
"v_and_b32 v245, 0xffff0000, v240\nv_lshlrev_b32 v246, 16, v241\nMvaa 144,224,144\nRa 200,10,18432\nv_and_b32 v247, 0xffff0000, v241\nMvaa 144,228,148\nD0 6,24,44,0\n"
"v_lshlrev_b32 v248, 16, v242\nMvaa 176,228,156\n.W544:\ns_cbranch_execz .W548\nRa 204,11,18432\nMvaa 176,224,152\nv_and_b32 v249, 0xffff0000, v242\nv_lshlrev_b32 v250, 16, v243\n"
"Mvaa 180,232,152\nRa 208,10,24576\nv_and_b32 v251, 0xffff0000, v243\nMvaa 180,236,156\nv_lshlrev_b32 v252, 16, v192\nv_and_b32 v253, 0xffff0000, v192\nMvaa 148,236,148\n"
"Ra 212,11,24576\nMU 32,244,252\nMvaa 148,232,144\nMU 33,244,253\nMvaa 116,232,136\nRa 216,10,26624\nMU 40,248,252\nMvaa 116,236,140\nD0 7,24,44,1024\nMU 41,248,253\n"
"Mvaa 84,236,132\nRa 220,11,26624\nv_lshlrev_b32 v252, 16, v193\nMvaa 84,232,128\nv_and_b32 v253, 0xffff0000, v193\nMU 34,244,252\nMvaa 88,240,128\nMU 35,244,253\nMU 42,248,252\n"
"Mvaa 88,244,132\nMU 43,248,253\nMvaa 120,244,140\nv_lshlrev_b32 v252, 16, v194\nv_and_b32 v253, 0xffff0000, v194\nMvaa 120,240,136\nMU 36,244,252\nMU 37,244,253\nMvaa 152,240,144\n"
"MU 44,248,252\nMU 45,248,253\nMvaa 152,244,148\nD0 8,24,44,2048\nv_lshlrev_b32 v252, 16, v195\nMvaa 184,244,156\nv_and_b32 v253, 0xffff0000, v195\nMvaa 184,240,152\nMU 38,244,252\n"
"MU 39,244,253\nMvaa 188,248,152\nMU 46,248,252\nMU 47,248,253\nMvaa 188,252,156\nv_lshlrev_b32 v252, 16, v208\nv_and_b32 v253, 0xffff0000, v208\nMvaa 156,252,148\nMU 48,244,252\n"
"MU 49,244,253\nMvaa 156,248,144\nMU 56,248,252\nMvaa 124,248,136\nMU 57,248,253\nv_lshlrev_b32 v252, 16, v209\nMvaa 124,252,140\nD0 9,24,44,3072\nv_and_b32 v253, 0xffff0000, v209\n"
"Mvaa 92,252,132\nMU 50,244,252\nMU 51,244,253\nMvaa 92,248,128\nWL 0\nMaaa 16,224,192\nRa 128,10,32768\nMU 58,248,252\nMaaa 16,228,196\ns_add_u32 m0, s47, 16384\n"
"s_add_u32 s44, s44, 0x40000\nMaaa 48,228,204\nRa 132,11,32768\nMU 59,248,253\nMaaa 48,224,200\nv_lshlrev_b32 v252, 16, v210\nv_and_b32 v253, 0xffff0000, v210\nMaaa 80,224,208\n"
"Ra 136,10,34816\nMU 52,244,252\nMaaa 80,228,212\nD0 6,24,44,0\nMU 53,244,253\nMaaa 112,228,220\nRa 140,11,34816\nMaaa 112,224,216\nMU 60,248,252\nMU 61,248,253\nMaaa 116,232,216\n"
"Ra 144,10,40960\nv_lshlrev_b32 v252, 16, v211\nMaaa 116,236,220\nv_and_b32 v253, 0xffff0000, v211\nMU 54,244,252\nMaaa 84,236,212\nRa 148,11,40960\nMU 55,244,253\nMaaa 84,232,208\n"
"MU 62,248,252\nMaaa 52,232,200\nRa 152,10,43008\nMU 63,248,253\nMaaa 52,236,204\nD0 7,24,44,1024\nv_lshlrev_b32 v252, 16, v196\nMaaa 20,236,196\nRa 156,11,43008\n"
"v_and_b32 v253, 0xffff0000, v196\nMaaa 20,232,192\nFM 32,245,252\nFM 33,245,253\nMaaa 24,240,192\nFM 40,249,252\nFM 41,249,253\nMaaa 24,244,196\nv_lshlrev_b32 v252, 16, v197\n"
"Maaa 56,244,204\nv_and_b32 v253, 0xffff0000, v197\nFM 34,245,252\nMaaa 56,240,200\nFM 35,245,253\nFM 42,249,252\nMaaa 88,240,208\nFM 43,249,253\nv_lshlrev_b32 v252, 16, v198\n"
"Maaa 88,244,212\nD0 8,24,44,2048\nv_and_b32 v253, 0xffff0000, v198\nMaaa 120,244,220\nFM 36,245,252\nMaaa 120,240,216\nFM 37,245,253\nFM 44,249,252\nMaaa 124,248,216\n"
"FM 45,249,253\nv_lshlrev_b32 v252, 16, v199\nMaaa 124,252,220\nv_and_b32 v253, 0xffff0000, v199\nFM 38,245,252\nMaaa 92,252,212\nFM 39,245,253\nFM 46,249,252\nMaaa 92,248,208\n"
"FM 47,249,253\nMaaa 60,248,200\nv_lshlrev_b32 v252, 16, v212\nv_and_b32 v253, 0xffff0000, v212\nMaaa 60,252,204\nD0 9,24,44,3072\nFM 48,245,252\nMaaa 28,252,196\nFM 49,245,253\n"
"FM 56,249,252\nMaaa 28,248,192\ns_waitcnt vmcnt(19) lgkmcnt(0)\ns_barrier\nMaaa 0,160,192\nTa 224,16,32768\nFM 57,249,253\nMaaa 0,164,196\nTa 226,16,34816\n"
"buffer_load_dwordx4 v[224:227], v20, s[92:95], s88 offen offset:0\nMaaa 32,164,204\ns_add_u32 m0, s47, -49152\ns_add_u32 s48, s48, 128\nMaaa 32,160,200\nTa 228,16,49152\n"
"v_lshlrev_b32 v252, 16, v213\nMaaa 64,160,208\nTa 230,16,51200\nv_and_b32 v253, 0xffff0000, v213\nMaaa 64,164,212\nD0 2,20,48,0\nFM 50,245,252\nMaaa 96,164,220\nTa 232,17,32768\n"
"Maaa 96,160,216\nTa 234,17,34816\nFM 51,245,253\nMaaa 100,168,216\nFM 58,249,252\nFM 59,249,253\nMaaa 100,172,220\nTa 236,17,49152\nv_lshlrev_b32 v252, 16, v214\nMaaa 68,172,212\n"
"Ta 238,17,51200\nv_and_b32 v253, 0xffff0000, v214\nMaaa 68,168,208\nFM 52,245,252\nMaaa 36,168,200\nTa 240,18,32768\nFM 53,245,253\nMaaa 36,172,204\nTa 242,18,34816\n"
"FM 60,249,252\nMaaa 4,172,196\nD0 3,20,48,1024\nFM 61,249,253\nMaaa 4,168,192\nTa 244,18,49152\nv_lshlrev_b32 v252, 16, v215\nMaaa 8,176,192\nTa 246,18,51200\n"
"v_and_b32 v253, 0xffff0000, v215\nMaaa 8,180,196\nFM 54,245,252\nMaaa 40,180,204\nTa 248,19,32768\nFM 55,245,253\nMaaa 40,176,200\nTa 250,19,34816\nFM 62,249,252\nMaaa 72,176,208\n"
"FM 63,249,253\nv_lshlrev_b32 v252, 16, v200\nMaaa 72,180,212\nTa 252,19,49152\nv_and_b32 v253, 0xffff0000, v200\nMaaa 104,180,220\nTa 254,19,51200\nMaaa 104,176,216\n"
"D0 4,20,48,2048\nFM 32,246,252\nMaaa 108,184,216\nFM 33,246,253\nFM 40,250,252\nMaaa 108,188,220\nFM 41,250,253\nv_lshlrev_b32 v252, 16, v201\nMaaa 76,188,212\n"
"v_and_b32 v253, 0xffff0000, v201\nFM 34,246,252\nMaaa 76,184,208\nFM 35,246,253\nMaaa 44,184,200\nFM 42,250,252\nFM 43,250,253\nMaaa 44,188,204\nD0 5,20,48,3072\n"
"v_lshlrev_b32 v252, 16, v202\nMaaa 12,188,196\nv_and_b32 v253, 0xffff0000, v202\nFM 36,246,252\nMaaa 12,184,192\nWL 0\nMvaa 80,224,128\nTa 160,12,32768\nFM 37,246,253\n"
"Mvaa 80,228,132\nTa 162,12,34816\nbuffer_load_dwordx4 v[228:231], v20, s[92:95], s88 offen offset:896\nMvaa 112,228,140\ns_add_u32 m0, s47, -32768\ns_add_u32 s45, s45, 128\n"
"Mvaa 112,224,136\nTa 164,12,49152\nFM 44,250,252\nMvaa 144,224,144\nTa 166,12,51200\nFM 45,250,253\nMvaa 144,228,148\nD0 2,20,45,0\nv_lshlrev_b32 v252, 16, v203\nMvaa 176,228,156\n"
"Ta 168,13,32768\nMvaa 176,224,152\nTa 170,13,34816\nv_and_b32 v253, 0xffff0000, v203\nMvaa 180,232,152\nFM 38,246,252\nFM 39,246,253\nMvaa 180,236,156\nTa 172,13,49152\n"
"FM 46,250,252\nMvaa 148,236,148\nTa 174,13,51200\nFM 47,250,253\nMvaa 148,232,144\nv_lshlrev_b32 v252, 16, v216\nMvaa 116,232,136\nTa 176,14,32768\n"
"v_and_b32 v253, 0xffff0000, v216\nMvaa 116,236,140\nTa 178,14,34816\nFM 48,246,252\nMvaa 84,236,132\nD0 3,20,45,1024\nFM 49,246,253\nMvaa 84,232,128\nTa 180,14,49152\n"
"FM 56,250,252\nMvaa 88,240,128\nTa 182,14,51200\nFM 57,250,253\nMvaa 88,244,132\nv_lshlrev_b32 v252, 16, v217\nMvaa 120,244,140\nTa 184,15,32768\nv_and_b32 v253, 0xffff0000, v217\n"
"Mvaa 120,240,136\nTa 186,15,34816\nFM 50,246,252\nMvaa 152,240,144\nFM 51,246,253\nFM 58,250,252\nMvaa 152,244,148\nTa 188,15,49152\nFM 59,250,253\nMvaa 184,244,156\n"
"Ta 190,15,51200\nMvaa 184,240,152\nD0 4,20,45,2048\nv_lshlrev_b32 v252, 16, v218\nMvaa 188,248,152\nv_and_b32 v253, 0xffff0000, v218\nFM 52,246,252\nMvaa 188,252,156\n"
"FM 53,246,253\nFM 60,250,252\nMvaa 156,252,148\nFM 61,250,253\nv_lshlrev_b32 v252, 16, v219\nMvaa 156,248,144\nv_and_b32 v253, 0xffff0000, v219\nMvaa 124,248,136\nFM 54,246,252\n"
"WV 26\nv_xor_b32 v27, s15, v27\nv_and_b32 v255, 0xffffdfff, v27\nv_cmp_ne_u32 vcc, 0, v255\ns_nop 0\ns_cbranch_vccnz .S17\n.S18:\nv_cmp_ne_u32 vcc, 0, v27\ns_nop 3\n"
"s_bcnt1_i32_b64 s4, vcc\ns_cmp_gt_u32 s4, 32\ns_cselect_b32 s4, 1, 0\nMvaa 124,252,140\nD0 5,20,45,3072\ns_xor_b32 s4, s4, s12\nMvaa 92,252,132\ns_and_b32 s4, s4, 1\n"
"s_lshl_b32 s4, s4, 13\nMvaa 92,248,128\ns_waitcnt vmcnt(18) lgkmcnt(0)\ns_barrier\nMvaa 64,160,128\nRa 192,10,49152\ns_mul_i32 s72, s73, 25165824\nMvaa 64,164,132\n"
"s_add_u32 m0, s47, 32768\ns_add_u32 s44, s44, 0x40000\nbuffer_load_dwordx4 v[232:235], v20, s[92:95], s88 offen offset:1792\nMvaa 96,164,140\nRa 196,11,49152\n"
"s_add_u32 s72, s72, s4\nMvaa 96,160,136\ns_and_b32 s4, s12, 1\ns_lshl_b32 s4, s4, 21\nMvaa 128,160,144\nRa 200,10,51200\ns_add_u32 s72, s72, s4\nMvaa 128,164,148\nD0 6,24,44,0\n"
"s_lshl3_add_u32 s2, s13, s11\nMvaa 160,164,156\nRa 204,11,51200\nMvaa 160,160,152\ns_lshl_b32 s2, s2, 11\ns_lshl_b32 s3, s14, 22\nMvaa 164,168,152\nRa 208,10,57344\n"
"s_add_u32 s2, s2, s3\nMvaa 164,172,156\ns_add_u32 s2, s2, s72\ns_add_u32 s49, s2, 0xc00000\nMvaa 132,172,148\nRa 212,11,57344\nFM 55,246,253\nMvaa 132,168,144\nFM 62,250,252\n"
"Mvaa 100,168,136\nRa 216,10,59392\nFM 63,250,253\nMvaa 100,172,140\nD0 7,24,44,1024\nv_lshlrev_b32 v252, 16, v204\nMvaa 68,172,132\nRa 220,11,59392\n"
"v_and_b32 v253, 0xffff0000, v204\nMvaa 68,168,128\nFM 32,247,252\nFM 33,247,253\nMvaa 72,176,128\nFM 40,251,252\nFM 41,251,253\nMvaa 72,180,132\nv_lshlrev_b32 v252, 16, v205\n"
"Mvaa 104,180,140\nv_and_b32 v253, 0xffff0000, v205\nFM 34,247,252\nMvaa 104,176,136\nFM 35,247,253\nFM 42,251,252\nMvaa 136,176,144\nFM 43,251,253\nv_lshlrev_b32 v252, 16, v206\n"
"Mvaa 136,180,148\nD0 8,24,44,2048\nv_and_b32 v253, 0xffff0000, v206\nMvaa 168,180,156\nFM 36,247,252\nMvaa 168,176,152\nFM 37,247,253\nFM 44,251,252\nMvaa 172,184,152\n"
"FM 45,251,253\nv_lshlrev_b32 v252, 16, v207\nMvaa 172,188,156\nv_and_b32 v253, 0xffff0000, v207\nFM 38,247,252\nMvaa 140,188,148\nFM 39,247,253\nFM 46,251,252\nMvaa 140,184,144\n"
"FM 47,251,253\nMvaa 108,184,136\nv_lshlrev_b32 v252, 16, v220\nv_and_b32 v253, 0xffff0000, v220\nMvaa 108,188,140\nD0 9,24,44,3072\nFM 48,247,252\nMvaa 76,188,132\nFM 49,247,253\n"
"FM 56,251,252\nMvaa 76,184,128\nWL 0\nMaaa 0,160,192\nRa 128,10,0\nFM 57,251,253\nMaaa 0,164,196\ns_add_u32 m0, s47, 49152\ns_add_u32 s44, s44, 0x40000\n"
"buffer_load_dwordx4 v[236:239], v20, s[92:95], s88 offen offset:2688\nMaaa 32,164,204\nRa 132,11,0\nv_lshlrev_b32 v252, 16, v221\nMaaa 32,160,200\n"
"v_and_b32 v253, 0xffff0000, v221\nFM 50,247,252\nMaaa 64,160,208\nRa 136,10,2048\nFM 51,247,253\nMaaa 64,164,212\nD0 6,24,44,0\nFM 58,251,252\nMaaa 96,164,220\nRa 140,11,2048\n"
"Maaa 96,160,216\nFM 59,251,253\nv_lshlrev_b32 v252, 16, v222\nMaaa 100,168,216\nRa 144,10,8192\nv_and_b32 v253, 0xffff0000, v222\nMaaa 100,172,220\nFM 52,247,252\nFM 53,247,253\n"
"Maaa 68,172,212\nRa 148,11,8192\nFM 60,251,252\nMaaa 68,168,208\nFM 61,251,253\nMaaa 36,168,200\nRa 152,10,10240\nv_lshlrev_b32 v252, 16, v223\nMaaa 36,172,204\nD0 7,24,44,1024\n"
"v_and_b32 v253, 0xffff0000, v223\nMaaa 4,172,196\nRa 156,11,10240\nFM 54,247,252\nMaaa 4,168,192\nFM 55,247,253\nFM 62,251,252\nMaaa 8,176,192\nFM 63,251,253\nMaaa 8,180,196\n"
"v_permlane32_swap_b32 v32, v48\nMaaa 40,180,204\nv_permlane32_swap_b32 v33, v49\nv_permlane32_swap_b32 v34, v50\nMaaa 40,176,200\nv_permlane32_swap_b32 v35, v51\n"
"v_permlane32_swap_b32 v36, v52\nMaaa 72,176,208\nv_permlane32_swap_b32 v37, v53\nv_permlane32_swap_b32 v38, v54\nMaaa 72,180,212\nD0 8,24,44,2048\nv_permlane32_swap_b32 v39, v55\n"
"Maaa 104,180,220\nv_permlane32_swap_b32 v40, v56\nMaaa 104,176,216\nv_permlane32_swap_b32 v41, v57\nv_permlane32_swap_b32 v42, v58\nMaaa 108,184,216\n"
"v_permlane32_swap_b32 v43, v59\nv_permlane32_swap_b32 v44, v60\nMaaa 108,188,220\nv_permlane32_swap_b32 v45, v61\nv_permlane32_swap_b32 v46, v62\nMaaa 76,188,212\n"
"v_permlane32_swap_b32 v47, v63\nAD 32,32,48\nMaaa 76,184,208\nAD 33,33,49\nMaaa 44,184,200\nAD 34,34,50\nAD 35,35,51\nMaaa 44,188,204\nD0 9,24,44,3072\nAD 36,36,52\n"
"Maaa 12,188,196\nAD 37,37,53\nAD 38,38,54\nMaaa 12,184,192\ns_waitcnt vmcnt(20) lgkmcnt(0)\ns_barrier\nMaaa 16,224,192\nTa 160,12,0\nAD 39,39,55\nMaaa 16,228,196\nTa 162,12,2048\n"
"Maaa 48,228,204\ns_add_u32 m0, s47, -16384\ns_add_u32 s48, s48, 128\nMaaa 48,224,200\nTa 164,12,16384\nAD 40,40,56\nMaaa 80,224,208\nTa 166,12,18432\nAD 41,41,57\nMaaa 80,228,212\n"
"D0 2,20,48,0\nAD 42,42,58\nMaaa 112,228,220\nTa 168,13,0\nMaaa 112,224,216\nTa 170,13,2048\nAD 43,43,59\nMaaa 116,232,216\nAD 44,44,60\nAD 45,45,61\nMaaa 116,236,220\n"
"Ta 172,13,16384\nAD 46,46,62\nMaaa 84,236,212\nTa 174,13,18432\nAD 47,47,63\nMaaa 84,232,208\nv_permlane16_swap_b32 v32, v40\nMaaa 52,232,200\nTa 176,14,0\n"
"v_permlane16_swap_b32 v33, v41\nMaaa 52,236,204\nTa 178,14,2048\nv_permlane16_swap_b32 v34, v42\nMaaa 20,236,196\nD0 3,20,48,1024\nv_permlane16_swap_b32 v35, v43\nMaaa 20,232,192\n"
"Ta 180,14,16384\nv_permlane16_swap_b32 v36, v44\nMaaa 24,240,192\nTa 182,14,18432\nv_permlane16_swap_b32 v37, v45\nMaaa 24,244,196\nv_permlane16_swap_b32 v38, v46\n"
"Maaa 56,244,204\nTa 184,15,0\nv_permlane16_swap_b32 v39, v47\nMaaa 56,240,200\nTa 186,15,2048\nAD 32,32,40\nMaaa 88,240,208\nAD 33,33,41\nAD 34,34,42\nMaaa 88,244,212\n"
"Ta 188,15,16384\nAD 35,35,43\nMaaa 120,244,220\nTa 190,15,18432\nMaaa 120,240,216\nD0 4,20,48,2048\nAD 36,36,44\nMaaa 124,248,216\nAD 37,37,45\nAD 38,38,46\nMaaa 124,252,220\n"
"AD 39,39,47\nv_add_f32_dpp v32, v32, v32 row_ror:8 row_mask:0xf bank_mask:0xf\nMaaa 92,252,212\nv_add_f32_dpp v33, v33, v33 row_ror:8 row_mask:0xf bank_mask:0xf\n"
"v_add_f32_dpp v34, v34, v34 row_ror:8 row_mask:0xf bank_mask:0xf\nMaaa 92,248,208\nv_add_f32_dpp v35, v35, v35 row_ror:8 row_mask:0xf bank_mask:0xf\nMaaa 60,248,200\n"
"v_add_f32_dpp v36, v36, v36 row_ror:8 row_mask:0xf bank_mask:0xf\nv_add_f32_dpp v37, v37, v37 row_ror:8 row_mask:0xf bank_mask:0xf\nMaaa 60,252,204\nD0 5,20,48,3072\n"
"v_add_f32_dpp v38, v38, v38 row_ror:8 row_mask:0xf bank_mask:0xf\nMaaa 28,252,196\nv_add_f32_dpp v39, v39, v39 row_ror:8 row_mask:0xf bank_mask:0xf\n"
"v_add_f32_dpp v32, v32, v32 row_ror:4 row_mask:0xf bank_mask:0xf\nMaaa 28,248,192\nWL 0\nMvaa 64,160,128\nTa 224,16,0\n"
"v_add_f32_dpp v33, v33, v33 row_ror:4 row_mask:0xf bank_mask:0xf\nMvaa 64,164,132\nTa 226,16,2048\nMvaa 96,164,140\ns_add_u32 m0, s47, -65536\ns_add_u32 s45, s45, 128\n.W545:\n"
"s_cbranch_execz .W549\nMvaa 96,160,136\nTa 228,16,16384\nv_add_f32_dpp v34, v34, v34 row_ror:4 row_mask:0xf bank_mask:0xf\nMvaa 128,160,144\nTa 230,16,18432\n"
"v_add_f32_dpp v35, v35, v35 row_ror:4 row_mask:0xf bank_mask:0xf\nMvaa 128,164,148\nD0 2,20,45,0\nv_add_f32_dpp v36, v36, v36 row_ror:4 row_mask:0xf bank_mask:0xf\n"
"Mvaa 160,164,156\nTa 232,17,0\nMvaa 160,160,152\nTa 234,17,2048\nv_add_f32_dpp v37, v37, v37 row_ror:4 row_mask:0xf bank_mask:0xf\nMvaa 164,168,152\n"
"v_add_f32_dpp v38, v38, v38 row_ror:4 row_mask:0xf bank_mask:0xf\nv_add_f32_dpp v39, v39, v39 row_ror:4 row_mask:0xf bank_mask:0xf\nMvaa 164,172,156\nTa 236,17,16384\n"
"v_add_f32_dpp v32, v32, v32 row_ror:2 row_mask:0xf bank_mask:0xf\nMvaa 132,172,148\nTa 238,17,18432\nv_add_f32_dpp v33, v33, v33 row_ror:2 row_mask:0xf bank_mask:0xf\n"
"Mvaa 132,168,144\nv_add_f32_dpp v34, v34, v34 row_ror:2 row_mask:0xf bank_mask:0xf\nMvaa 100,168,136\nTa 240,18,0\n"
"v_add_f32_dpp v35, v35, v35 row_ror:2 row_mask:0xf bank_mask:0xf\nMvaa 100,172,140\nTa 242,18,2048\nv_add_f32_dpp v36, v36, v36 row_ror:2 row_mask:0xf bank_mask:0xf\n"
"Mvaa 68,172,132\nD0 3,20,45,1024\nv_add_f32_dpp v37, v37, v37 row_ror:2 row_mask:0xf bank_mask:0xf\nMvaa 68,168,128\nTa 244,18,16384\n"
"v_add_f32_dpp v38, v38, v38 row_ror:2 row_mask:0xf bank_mask:0xf\nMvaa 72,176,128\nTa 246,18,18432\nv_add_f32_dpp v39, v39, v39 row_ror:2 row_mask:0xf bank_mask:0xf\n"
"Mvaa 72,180,132\nv_lshrrev_b32 v48, 4, v0\nMvaa 104,180,140\nTa 248,19,0\nv_and_b32 v49, 1, v0\nMvaa 104,176,136\nTa 250,19,2048\nv_lshlrev_b32 v49, 5, v49\nMvaa 136,176,144\n"
"v_lshl_add_u32 v49, v48, 6, v49\ns_lshl_b32 s2, s11, 8\nMvaa 136,180,148\nTa 252,19,16384\ns_add_u32 s2, s2, 132096\nMvaa 168,180,156\nTa 254,19,18432\nMvaa 168,176,152\n"
"D0 4,20,45,2048\nv_add_u32 v49, s2, v49\nMvaa 172,184,152\nds_write_b128 v49, v[32:35]\nds_write_b128 v49, v[36:39] offset:16\nMvaa 172,188,156\nv_and_b32 v50, 31, v0\n"
"v_lshlrev_b32 v50, 2, v50\nMvaa 140,188,148\ns_lshl_b32 s2, s11, 10\ns_add_u32 s2, s2, 133120\nMvaa 140,184,144\nv_add_u32 v51, s2, v50\nMvaa 108,184,136\n"
"v_add_u32 v50, 132096, v50\nv_lshl_add_u32 v52, v48, 4, s2\nMvaa 108,188,140\nD0 5,20,45,3072\nMvaa 76,188,132\nMvaa 76,184,128\ns_waitcnt vmcnt(18) lgkmcnt(0)\ns_barrier\n"
"Mvaa 80,224,128\nRa 192,10,16384\nds_read_b32 v32, v50 offset:0\nMvaa 80,228,132\ns_add_u32 m0, s47, 0\ns_add_u32 s44, s44, 0x40000\nMvaa 112,228,140\nRa 196,11,16384\n"
"Mvaa 112,224,136\nds_read_b32 v33, v50 offset:256\nds_read_b32 v34, v50 offset:512\nMvaa 144,224,144\nRa 200,10,18432\nMvaa 144,228,148\nD0 6,24,44,0\nMvaa 176,228,156\n"
"Ra 204,11,18432\nds_read_b32 v35, v50 offset:768\nMvaa 176,224,152\nds_read_b32 v36, v50 offset:128\nMvaa 180,232,152\nRa 208,10,24576\nds_read_b32 v37, v50 offset:384\n"
"Mvaa 180,236,156\nds_read_b32 v38, v50 offset:640\nMvaa 148,236,148\nRa 212,11,24576\nMvaa 148,232,144\nds_read_b32 v39, v50 offset:896\nMvaa 116,232,136\nRa 216,10,26624\n"
"Mvaa 116,236,140\nD0 7,24,44,1024\nMvaa 84,236,132\nRa 220,11,26624\nMvaa 84,232,128\nMvaa 88,240,128\nMvaa 88,244,132\nMvaa 120,244,140\nMvaa 120,240,136\nMvaa 152,240,144\n"
"Mvaa 152,244,148\nD0 8,24,44,2048\nMvaa 184,244,156\nWL 2\nAD 32,32,33\nMvaa 184,240,152\nAD 34,34,35\nMvaa 188,248,152\nAD 36,36,37\nAD 38,38,39\nMvaa 188,252,156\nAD 32,32,34\n"
"Mvaa 156,252,148\nAD 36,36,38\nMvaa 156,248,144\nCV 32,32,36\nv_and_b32 v36, 0xffff0000, v32\nMvaa 124,248,136\nv_lshlrev_b32 v32, 16, v32\nMvaa 124,252,140\nD0 9,24,44,3072\n"
"Mvaa 92,252,132\nv_mul_f32 v33, 0xbfb8aa3b, v32\nMvaa 92,248,128\nWL 0\nMaaa 16,224,192\nRa 128,10,32768\nMaaa 16,228,196\ns_add_u32 m0, s47, 16384\ns_add_u32 s44, s44, 0x40000\n"
"Maaa 48,228,204\nRa 132,11,32768\nEX 33,33\nMaaa 48,224,200\nv_add_f32 v33, 1.0, v33\nMaaa 80,224,208\nRa 136,10,34816\nv_rcp_f32 v33, v33\nMaaa 80,228,212\nD0 6,24,44,0\n"
"Maaa 112,228,220\nRa 140,11,34816\nMU 33,32,33\nMaaa 112,224,216\nCV 33,33,33\nMaaa 116,232,216\nRa 144,10,40960\nv_lshlrev_b32 v33, 16, v33\nMaaa 116,236,220\nMU 33,33,36\n"
"Maaa 84,236,212\nRa 148,11,40960\nMaaa 84,232,208\nCV 33,33,33\nv_lshlrev_b32 v33, 16, v33\nMaaa 52,232,200\nRa 152,10,43008\nMaaa 52,236,204\nD0 7,24,44,1024\nMaaa 20,236,196\n"
"Ra 156,11,43008\nds_write_b32 v51, v33\nMaaa 20,232,192\nRv 244,52,0\nMaaa 24,240,192\nRv 248,52,64\ns_and_b32 s2, s74, 15\nMaaa 24,244,196\ns_sub_u32 s2, s2, 2\n"
"s_cmp_lt_u32 s2, 14\ns_cselect_b32 s5, 0, 0x40000000\nMaaa 56,244,204\ns_and_b32 s2, s2, 15\nMaaa 56,240,200\ns_mul_i32 s4, s73, 14\ns_add_u32 s4, s4, s2\nMaaa 88,240,208\n"
"v_and_b32 v49, 15, v0\nMaaa 88,244,212\nD0 8,24,44,2048\nMaaa 120,244,220\nv_lshlrev_b32 v50, 3, v49\nv_and_b32 v51, 1, v48\nMaaa 120,240,216\nv_lshl_add_u32 v50, v51, 2, v50\n"
"Maaa 124,248,216\ns_mul_i32 s2, s11, 112\nv_add_u32 v50, s2, v50\nMaaa 124,252,220\nv_lshrrev_b32 v51, 4, v50\nMaaa 92,252,212\nv_lshrrev_b32 v48, 1, v48\nMaaa 92,248,208\n"
"v_mad_u32_u24 v51, v48, 28, v51\nv_mul_u32_u24 v51, 7168, v51\nMaaa 60,248,200\nv_and_b32 v50, 15, v50\nMaaa 60,252,204\nD0 9,24,44,3072\nMaaa 28,252,196\n"
"v_lshl_add_u32 v51, v50, 2, v51\nv_cmp_gt_u32 vcc, 14, v49\ns_lshl_b32 s4, s4, 6\ns_add_u32 s4, s4, 0xc04c800\nv_cndmask_b32 v51, v254, v51, vcc\nMaaa 28,248,192\n"
"s_waitcnt vmcnt(16) lgkmcnt(3)\ns_barrier\nMaaa 0,160,192\nTa 224,16,32768\ns_or_b32 s4, s4, s5\nMaaa 0,164,196\nTa 226,16,34816\nMaaa 32,164,204\ns_add_u32 m0, s47, -49152\n"
"s_add_u32 s48, s48, 128\nMaaa 32,160,200\nTa 228,16,49152\nWL 3\nMaaa 64,160,208\nTa 230,16,51200\nMaaa 64,164,212\nD0 2,20,48,0\nMaaa 96,164,220\nTa 232,17,32768\n"
"v_lshlrev_b32 v252, 16, v224\nMaaa 96,160,216\nTa 234,17,34816\nMaaa 100,168,216\nv_and_b32 v253, 0xffff0000, v224\nMU 32,244,252\nMaaa 100,172,220\nTa 236,17,49152\n"
"Maaa 68,172,212\nTa 238,17,51200\nMaaa 68,168,208\nMU 33,244,253\nMU 40,248,252\nMaaa 36,168,200\nTa 240,18,32768\nMaaa 36,172,204\nTa 242,18,34816\nMaaa 4,172,196\n"
"D0 3,20,48,1024\nMU 41,248,253\nMaaa 4,168,192\nTa 244,18,49152\nMaaa 8,176,192\nTa 246,18,51200\nv_lshlrev_b32 v252, 16, v225\nMaaa 8,180,196\nv_and_b32 v253, 0xffff0000, v225\n"
"Maaa 40,180,204\nTa 248,19,32768\nMaaa 40,176,200\nTa 250,19,34816\nMU 34,244,252\nMaaa 72,176,208\nMU 35,244,253\nMaaa 72,180,212\nTa 252,19,49152\nMaaa 104,180,220\n"
"Ta 254,19,51200\nMU 42,248,252\nMaaa 104,176,216\nD0 4,20,48,2048\nMaaa 108,184,216\nMU 43,248,253\nv_lshlrev_b32 v252, 16, v226\nMaaa 108,188,220\n"
"v_and_b32 v253, 0xffff0000, v226\nMaaa 76,188,212\nMU 36,244,252\nMaaa 76,184,208\nMU 37,244,253\nMU 44,248,252\nMaaa 44,184,200\nMU 45,248,253\nMaaa 44,188,204\nD0 5,20,48,3072\n"
"Maaa 12,188,196\nv_lshlrev_b32 v252, 16, v227\nv_and_b32 v253, 0xffff0000, v227\nMaaa 12,184,192\nWL 0\nMvaa 80,224,128\nTa 160,12,32768\nMU 38,244,252\nMvaa 80,228,132\n"
"Ta 162,12,34816\nMvaa 112,228,140\ns_add_u32 m0, s47, -32768\ns_add_u32 s45, s45, 128\nMvaa 112,224,136\nTa 164,12,49152\nMU 39,244,253\nMvaa 144,224,144\nTa 166,12,51200\n"
"Mvaa 144,228,148\nD0 2,20,45,0\nMvaa 176,228,156\nTa 168,13,32768\nMU 46,248,252\nMvaa 176,224,152\nTa 170,13,34816\nMvaa 180,232,152\nMU 47,248,253\nv_lshlrev_b32 v252, 16, v228\n"
"Mvaa 180,236,156\nTa 172,13,49152\nMvaa 148,236,148\nTa 174,13,51200\nMvaa 148,232,144\nv_and_b32 v253, 0xffff0000, v228\nFM 32,245,252\nMvaa 116,232,136\nTa 176,14,32768\n"
"Mvaa 116,236,140\nTa 178,14,34816\nMvaa 84,236,132\nD0 3,20,45,1024\nFM 33,245,253\nMvaa 84,232,128\nTa 180,14,49152\nMvaa 88,240,128\nTa 182,14,51200\nFM 40,249,252\n"
"Mvaa 88,244,132\nFM 41,249,253\nMvaa 120,244,140\nTa 184,15,32768\nMvaa 120,240,136\nTa 186,15,34816\nv_lshlrev_b32 v252, 16, v229\nMvaa 152,240,144\n"
"v_and_b32 v253, 0xffff0000, v229\nMvaa 152,244,148\nTa 188,15,49152\nMvaa 184,244,156\nTa 190,15,51200\nFM 34,245,252\nMvaa 184,240,152\nD0 4,20,45,2048\nMvaa 188,248,152\n"
"FM 35,245,253\nFM 42,249,252\nMvaa 188,252,156\nFM 43,249,253\nMvaa 156,252,148\nv_lshlrev_b32 v252, 16, v230\nMvaa 156,248,144\nv_and_b32 v253, 0xffff0000, v230\nFM 36,245,252\n"
"Mvaa 124,248,136\nFM 37,245,253\nMvaa 124,252,140\nD0 5,20,45,3072\nMvaa 92,252,132\nFM 44,249,252\nFM 45,249,253\nMvaa 92,248,128\ns_waitcnt vmcnt(16) lgkmcnt(0)\ns_barrier\n"
"Mvaa 64,160,128\nRa 192,10,49152\nv_lshlrev_b32 v252, 16, v231\nMvaa 64,164,132\ns_add_u32 m0, s47, 32768\ns_add_u32 s44, s44, 0x40000\nMvaa 96,164,140\nRa 196,11,49152\n"
"Mvaa 96,160,136\nv_and_b32 v253, 0xffff0000, v231\nFM 38,245,252\nMvaa 128,160,144\nRa 200,10,51200\nMvaa 128,164,148\nD0 6,24,44,0\nMvaa 160,164,156\nRa 204,11,51200\n"
"FM 39,245,253\nMvaa 160,160,152\nFM 46,249,252\nMvaa 164,168,152\nRa 208,10,57344\nFM 47,249,253\nMvaa 164,172,156\nv_lshlrev_b32 v252, 16, v232\nMvaa 132,172,148\n"
"Ra 212,11,57344\nMvaa 132,168,144\nv_and_b32 v253, 0xffff0000, v232\nFM 32,246,252\nMvaa 100,168,136\nRa 216,10,59392\nMvaa 100,172,140\nD0 7,24,44,1024\nMvaa 68,172,132\n"
"Ra 220,11,59392\nFM 33,246,253\nMvaa 68,168,128\nFM 40,250,252\nMvaa 72,176,128\nFM 41,250,253\nv_lshlrev_b32 v252, 16, v233\nMvaa 72,180,132\nv_and_b32 v253, 0xffff0000, v233\n"
"Mvaa 104,180,140\nFM 34,246,252\nMvaa 104,176,136\nFM 35,246,253\nFM 42,250,252\nMvaa 136,176,144\nFM 43,250,253\nMvaa 136,180,148\nD0 8,24,44,2048\nMvaa 168,180,156\n"
"v_lshlrev_b32 v252, 16, v234\nv_and_b32 v253, 0xffff0000, v234\nMvaa 168,176,152\nFM 36,246,252\nMvaa 172,184,152\nFM 37,246,253\nFM 44,250,252\nMvaa 172,188,156\nFM 45,250,253\n"
"Mvaa 140,188,148\nv_lshlrev_b32 v252, 16, v235\nMvaa 140,184,144\nv_and_b32 v253, 0xffff0000, v235\nFM 38,246,252\nMvaa 108,184,136\nFM 39,246,253\nMvaa 108,188,140\n"
"D0 9,24,44,3072\nMvaa 76,188,132\nFM 46,250,252\nFM 47,250,253\nMvaa 76,184,128\nWL 0\nMaaa 0,160,192\nRa 128,10,0\nv_lshlrev_b32 v252, 16, v236\nMaaa 0,164,196\n"
"s_add_u32 m0, s47, 49152\ns_add_u32 s44, s44, 0x40000\nMaaa 32,164,204\nRa 132,11,0\nMaaa 32,160,200\nv_and_b32 v253, 0xffff0000, v236\nFM 32,247,252\nMaaa 64,160,208\n"
"Ra 136,10,2048\nMaaa 64,164,212\nD0 6,24,44,0\nMaaa 96,164,220\nRa 140,11,2048\nFM 33,247,253\nMaaa 96,160,216\nFM 40,251,252\nMaaa 100,168,216\nRa 144,10,8192\nFM 41,251,253\n"
"Maaa 100,172,220\nv_lshlrev_b32 v252, 16, v237\nMaaa 68,172,212\nRa 148,11,8192\nMaaa 68,168,208\nv_and_b32 v253, 0xffff0000, v237\nFM 34,247,252\nMaaa 36,168,200\n"
"Ra 152,10,10240\nMaaa 36,172,204\nD0 7,24,44,1024\nMaaa 4,172,196\nRa 156,11,10240\nFM 35,247,253\nMaaa 4,168,192\nFM 42,251,252\nMaaa 8,176,192\nFM 43,251,253\n"
"v_lshlrev_b32 v252, 16, v238\nMaaa 8,180,196\nv_and_b32 v253, 0xffff0000, v238\nMaaa 40,180,204\nFM 36,247,252\nMaaa 40,176,200\nFM 37,247,253\nFM 44,251,252\nMaaa 72,176,208\n"
"FM 45,251,253\nMaaa 72,180,212\nD0 8,24,44,2048\nMaaa 104,180,220\nv_lshlrev_b32 v252, 16, v239\nv_and_b32 v253, 0xffff0000, v239\nMaaa 104,176,216\nFM 38,247,252\n"
"Maaa 108,184,216\nFM 39,247,253\nFM 46,251,252\nMaaa 108,188,220\nFM 47,251,253\nMaaa 76,188,212\nv_permlane32_swap_b32 v32, v40\nMaaa 76,184,208\nv_permlane32_swap_b32 v33, v41\n"
"v_permlane32_swap_b32 v34, v42\nMaaa 44,184,200\nv_permlane32_swap_b32 v35, v43\nMaaa 44,188,204\nD0 9,24,44,3072\nMaaa 12,188,196\nv_permlane32_swap_b32 v36, v44\n"
"v_permlane32_swap_b32 v37, v45\nMaaa 12,184,192\nWV 16\ns_barrier\nMaaa 16,224,192\nTa 160,12,0\nv_permlane32_swap_b32 v38, v46\nMaaa 16,228,196\nTa 162,12,2048\nMaaa 48,228,204\n"
"s_add_u32 m0, s47, -16384\ns_add_u32 s48, s48, 128\nMaaa 48,224,200\nTa 164,12,16384\nv_permlane32_swap_b32 v39, v47\nMaaa 80,224,208\nTa 166,12,18432\nMaaa 80,228,212\n"
"D0 2,20,48,0\nMaaa 112,228,220\nTa 168,13,0\nAD 32,32,40\nMaaa 112,224,216\nTa 170,13,2048\nMaaa 116,232,216\nAD 33,33,41\nAD 34,34,42\nMaaa 116,236,220\nTa 172,13,16384\n"
"Maaa 84,236,212\nTa 174,13,18432\nMaaa 84,232,208\nAD 35,35,43\nAD 36,36,44\nMaaa 52,232,200\nTa 176,14,0\nMaaa 52,236,204\nTa 178,14,2048\nMaaa 20,236,196\nD0 3,20,48,1024\n"
"AD 37,37,45\nMaaa 20,232,192\nTa 180,14,16384\nMaaa 24,240,192\nTa 182,14,18432\nAD 38,38,46\nMaaa 24,244,196\nAD 39,39,47\nMaaa 56,244,204\nTa 184,15,0\nMaaa 56,240,200\n"
"Ta 186,15,2048\nv_permlane16_swap_b32 v32, v36\n.W546:\ns_cbranch_execz .W550\nMaaa 88,240,208\nv_permlane16_swap_b32 v33, v37\nMaaa 88,244,212\nTa 188,15,16384\nMaaa 120,244,220\n"
"Ta 190,15,18432\nv_permlane16_swap_b32 v34, v38\nMaaa 120,240,216\nD0 4,20,48,2048\nMaaa 124,248,216\nv_permlane16_swap_b32 v35, v39\nAD 32,32,36\nMaaa 124,252,220\nAD 33,33,37\n"
"Maaa 92,252,212\nAD 34,34,38\nMaaa 92,248,208\nAD 35,35,39\nSv 32,51,16,4,0\nMaaa 60,248,200\nMaaa 60,252,204\nD0 5,20,48,3072\nMaaa 28,252,196\nMaaa 28,248,192\nWL 0\n"
"Mvaa 64,160,128\nTa 224,16,0\ns_lshr_b32 s3, s11, 1\nMvaa 64,164,132\nTa 226,16,2048\ns_add_u32 s6, s56, 16384\nMvaa 96,164,140\ns_lshl_b32 s3, s3, 13\nMvaa 96,160,136\n"
"Ta 228,16,16384\nbuffer_load_dwordx4 v[224:227], v1, s[16:19], s6 offen offset:0 sc1\nMvaa 128,160,144\nTa 230,16,18432\nMvaa 128,164,148\ns_lshl_b32 s2, s11, 12\n"
"Mvaa 160,164,156\nTa 232,17,0\nMvaa 160,160,152\nTa 234,17,2048\nMvaa 164,168,152\ns_add_u32 s2, s2, s3\ns_add_u32 s2, s2, s72\nMvaa 164,172,156\nTa 236,17,16384\n"
"buffer_load_dwordx4 v[228:231], v1, s[16:19], s6 offen offset:1024 sc1\nMvaa 132,172,148\nTa 238,17,18432\nMvaa 132,168,144\ns_lshl_b32 s3, s11, 12\nMvaa 100,168,136\nTa 240,18,0\n"
"Mvaa 100,172,140\nTa 242,18,2048\nMvaa 68,172,132\ns_add_u32 s89, s3, 0x0\nMvaa 68,168,128\nTa 244,18,16384\n"
"buffer_load_dwordx4 v[232:235], v1, s[16:19], s6 offen offset:2048 sc1\nMvaa 72,176,128\nTa 246,18,18432\ns_mov_b32 s88, 0xffff8000\nMvaa 72,180,132\ns_lshl_b32 s4, s13, 1\n"
"Mvaa 104,180,140\nTa 248,19,0\nMvaa 104,176,136\nTa 250,19,2048\nMvaa 136,176,144\ns_sub_u32 s79, 15, s4\nMvaa 136,180,148\nTa 252,19,16384\n"
"buffer_load_dwordx4 v[236:239], v1, s[16:19], s6 offen offset:3072 sc1\nMvaa 168,180,156\nTa 254,19,18432\nMvaa 168,176,152\ns_add_u32 s96, s2, 0x1310000\nMvaa 172,184,152\n"
"s_mov_b32 s63, 0x53800000\ns_mov_b32 s64, 0x2b800000\nMvaa 172,188,156\ns_sub_u32 s98, s4, 1\ns_add_u32 s6, s56, 20480\nMvaa 140,188,148\ns_lshl_b32 s5, s13, 5\nMvaa 140,184,144\n"
"s_add_u32 s5, s5, s11\nbuffer_load_dwordx4 v[240:243], v1, s[16:19], s6 offen offset:0 sc1\nMvaa 108,184,136\ns_lshl_b32 s65, s5, 9\nMvaa 108,188,140\ns_add_u32 s65, s65, s72\n"
"Mvaa 76,188,132\ns_add_u32 s65, s65, 0x1580000\nMvaa 76,184,128\ns_waitcnt vmcnt(18) lgkmcnt(0)\ns_barrier\nMvaa 80,224,128\nRa 192,10,16384\ns_and_b32 s66, s72, 0x2000\n"
"Mvaa 80,228,132\ns_add_u32 s66, s66, s56\nbuffer_load_dwordx4 v[244:247], v1, s[16:19], s6 offen offset:1024 sc1\nMvaa 112,228,140\nRa 196,11,16384\nMvaa 112,224,136\n"
"s_add_u32 s66, s66, 0x100000\nMvaa 144,224,144\nRa 200,10,18432\nMvaa 144,228,148\ns_and_b32 s5, s12, 1\nMvaa 176,228,156\nRa 204,11,18432\nMvaa 176,224,152\n"
"s_mul_i32 s58, s5, 0x202000\nbuffer_load_dwordx4 v[248:251], v1, s[16:19], s6 offen offset:2048 sc1\nMvaa 180,232,152\nRa 208,10,24576\ns_xor_b32 s58, s58, s72\nMvaa 180,236,156\n"
"s_add_u32 s58, s58, 0x15c0000\nMvaa 148,236,148\nRa 212,11,24576\nMvaa 148,232,144\ns_lshl_b32 s5, s5, 8\nMvaa 116,232,136\nRa 216,10,26624\nMvaa 116,236,140\n"
"s_add_u32 s61, s58, s5\nbuffer_load_dwordx4 v[252:255], v1, s[16:19], s6 offen offset:3072 sc1\nMvaa 84,236,132\nRa 220,11,26624\nMvaa 84,232,128\ns_lshl4_add_u32 s60, s13, s61\n"
"Mvaa 88,240,128\ns_add_u32 s54, s60, 0\ns_xor_b32 s60, s54, 0x100\nMvaa 88,244,132\ns_cmp_eq_u32 s11, 0\ns_cselect_b32 s54, s54, 0x40000000\ns_add_u32 s6, s56, 0\n"
"Mvaa 120,244,140\ns_getpc_b64 s[8:9]\n.PC37:\ns_add_u32 s8, s8, .HD13-.PC37\ns_addc_u32 s9, s9, 0\nMvaa 120,240,136\ns_mov_b32 s97, 0xff800000\n"
"buffer_load_dwordx4 v[192:195], v1, s[16:19], s6 offen offset:0 sc1\nMvaa 152,240,144\ns_mov_b32 s90, 3\ns_cmp_lt_u32 s11, 2\ns_cselect_b32 s92, 2, 0\nMvaa 152,244,148\n"
"s_add_u32 s86, s2, 0x20000\nMvaa 184,244,156\ns_add_u32 s87, s2, 0x410000\ns_cmp_eq_u32 s13, 7\ns_cselect_b32 s92, s92, 0\nMvaa 184,240,152\ns_cmp_gt_u32 s13, 5\n"
"s_cselect_b32 s4, s12, 15\nMvaa 188,248,152\ns_cmp_lt_u32 s4, 14\ns_cselect_b32 s5, s11, 3\nMvaa 188,252,156\ns_cmp_lt_u32 s5, 2\ns_cselect_b32 s91, 1, 100\n"
"buffer_load_dwordx4 v[196:199], v1, s[16:19], s6 offen offset:1024 sc1\nMvaa 156,252,148\nv_and_b32 v27, 0x70, v1\n"
"buffer_load_dword v27, v27, s[16:19], s61 offen offset:128 sc0 sc1\nMvaa 156,248,144\ns_lshr_b32 s2, s13, 2\ns_bfe_u32 s3, s13, 0x10001\nMvaa 124,248,136\ns_mul_i32 s3, s3, 17\n"
"s_add_u32 s2, s2, s3\nMvaa 124,252,140\ns_lshl2_add_u32 s3, s14, 0\nMvaa 92,252,132\ns_lshl_b32 s3, s3, 2\ns_lshr_b32 s3, 0xe420ca86, s3\nMvaa 92,248,128\nWL 0\nMaaa 16,224,192\n"
"Ra 128,10,32768\ns_and_b32 s3, s3, 15\nbuffer_load_dwordx4 v[200:203], v1, s[16:19], s6 offen offset:2048 sc1\nMaaa 16,228,196\ns_add_u32 s2, s2, s3\ns_cmp_eq_u32 s2, 32\n"
"s_cselect_b32 s4, 16, s2\nMaaa 48,228,204\nRa 132,11,32768\ns_mul_i32 s75, s4, 65536\nMaaa 48,224,200\ns_lshr_b32 s2, s13, 2\ns_bfe_u32 s3, s13, 0x10001\nMaaa 80,224,208\n"
"Ra 136,10,34816\nMaaa 80,228,212\ns_mul_i32 s3, s3, 17\ns_add_u32 s2, s2, s3\nMaaa 112,228,220\nRa 140,11,34816\ns_lshl2_add_u32 s3, s14, 1\n"
"buffer_load_dwordx4 v[204:207], v1, s[16:19], s6 offen offset:3072 sc1\nMaaa 112,224,216\ns_lshl_b32 s3, s3, 2\nMaaa 116,232,216\nRa 144,10,40960\ns_lshr_b32 s3, 0xe420ca86, s3\n"
"Maaa 116,236,220\ns_and_b32 s3, s3, 15\ns_add_u32 s2, s2, s3\nMaaa 84,236,212\nRa 148,11,40960\ns_cmp_eq_u32 s2, 32\ns_cselect_b32 s4, 16, s2\ns_add_u32 s6, s56, 4096\n"
"Maaa 84,232,208\ns_mul_i32 s76, s4, 65536\ns_lshr_b32 s2, s13, 2\nMaaa 52,232,200\nRa 152,10,43008\ns_bfe_u32 s3, s13, 0x10001\n"
"buffer_load_dwordx4 v[208:211], v1, s[16:19], s6 offen offset:0 sc1\nMaaa 52,236,204\ns_mul_i32 s3, s3, 17\nMaaa 20,236,196\nRa 156,11,43008\ns_add_u32 s2, s2, s3\n"
"Maaa 20,232,192\ns_lshl2_add_u32 s3, s14, 2\ns_lshl_b32 s3, s3, 2\nMaaa 24,240,192\ns_lshr_b32 s3, 0xe420ca86, s3\ns_and_b32 s3, s3, 15\nMaaa 24,244,196\ns_add_u32 s2, s2, s3\n"
"s_cmp_eq_u32 s2, 32\ns_cselect_b32 s4, 16, s2\nMaaa 56,244,204\ns_mul_i32 s77, s4, 65536\ns_lshr_b32 s2, s13, 2\n"
"buffer_load_dwordx4 v[212:215], v1, s[16:19], s6 offen offset:1024 sc1\nMaaa 56,240,200\ns_bfe_u32 s3, s13, 0x10001\ns_mul_i32 s3, s3, 17\nMaaa 88,240,208\ns_add_u32 s2, s2, s3\n"
"Maaa 88,244,212\ns_lshl2_add_u32 s3, s14, 3\ns_lshl_b32 s3, s3, 2\nMaaa 120,244,220\ns_lshr_b32 s3, 0xe420ca86, s3\ns_and_b32 s3, s3, 15\nMaaa 120,240,216\ns_add_u32 s2, s2, s3\n"
"s_cmp_eq_u32 s2, 32\ns_cselect_b32 s4, 16, s2\nMaaa 124,248,216\ns_mul_i32 s78, s4, 65536\ns_lshr_b32 s3, s11, 1\n"
"buffer_load_dwordx4 v[216:219], v1, s[16:19], s6 offen offset:2048 sc1\nMaaa 124,252,220\ns_lshl_b32 s3, s3, 15\ns_add_u32 s2, s72, s3\nMaaa 92,252,212\ns_and_b32 s3, s11, 1\n"
"Maaa 92,248,208\ns_lshl_b32 s3, s3, 12\ns_add_u32 s2, s2, s3\nMaaa 60,248,200\ns_add_u32 s2, s2, 0x0\ns_mov_b32 s55, 16384\nMaaa 60,252,204\ns_add_u32 s75, s75, s2\n"
"Maaa 28,252,196\ns_add_u32 s76, s76, s2\ns_add_u32 s77, s77, s2\nbuffer_load_dwordx4 v[220:223], v1, s[16:19], s6 offen offset:3072 sc1\nMaaa 28,248,192\nWV 22\ns_barrier\n"
"Maaa 0,160,192\nTa 224,16,32768\ns_add_u32 s78, s78, s2\nMaaa 0,164,196\nTa 226,16,34816\nMaaa 32,164,204\nMaaa 32,160,200\nTa 228,16,49152\nMaaa 64,160,208\nTa 230,16,51200\n"
"Maaa 64,164,212\nMaaa 96,164,220\nTa 232,17,32768\nMaaa 96,160,216\nTa 234,17,34816\nMaaa 100,168,216\nWV 6\nv_cmp_ne_u32 vcc, s15, v27\ns_nop 0\ns_cbranch_vccnz .S38\n.S39:\n"
"s_sub_u32 s45, s86, 131072\nMaaa 100,172,220\nTa 236,17,49152\ns_add_u32 m0, s47, 0\ns_add_u32 s46, s45, 0x0\nMaaa 68,172,212\nTa 238,17,51200\nMaaa 68,168,208\nD1 1,16,46,0\n"
"s_mov_b32 s36, s68\nMaaa 36,168,200\nTa 240,18,32768\nMaaa 36,172,204\nTa 242,18,34816\nMaaa 4,172,196\ns_and_b32 s37, s69, 0xffff\nMaaa 4,168,192\nTa 244,18,49152\n"
"Maaa 8,176,192\nTa 246,18,51200\nD1 1,16,46,1024\nMaaa 8,180,196\ns_mov_b32 s38, 0x1c0000\nMaaa 40,180,204\nTa 248,19,32768\nMaaa 40,176,200\nTa 250,19,34816\nMaaa 72,176,208\n"
"s_mov_b32 s39, 0x20000\nMaaa 72,180,212\nTa 252,19,49152\nD1 1,16,46,2048\nMaaa 104,180,220\nTa 254,19,51200\ns_and_b32 s2, s74, 15\nMaaa 104,176,216\ns_max_u32 s2, s2, 2\n"
"Maaa 108,184,216\ns_sub_u32 s2, s2, 2\nMaaa 108,188,220\ns_mul_i32 s3, s73, 14\nMaaa 76,188,212\nD1 1,16,46,3072\nMaaa 76,184,208\ns_add_u32 s2, s2, s3\n"
"s_lshl2_add_u32 s2, s2, s11\nMaaa 44,184,200\ns_mul_i32 s3, s2, 4682\nMaaa 44,188,204\ns_lshr_b32 s3, s3, 16\nMaaa 12,188,196\ns_mul_i32 s22, s3, 14\nMaaa 12,184,192\nWL 0\n"
"Mvaa 80,224,128\nTa 160,12,32768\ns_add_u32 m0, s47, -65536\ns_add_u32 s46, s45, 0x10000\nD1 1,16,46,0\nMvaa 80,228,132\nTa 162,12,34816\nMvaa 84,236,132\ns_sub_u32 s22, s2, s22\n"
"Mvaa 84,232,128\nTa 164,12,49152\nMvaa 88,240,128\nTa 166,12,51200\nMvaa 88,244,132\nD1 1,16,46,1024\nMvaa 92,252,132\nTa 168,13,32768\ns_and_b32 s21, s3, 28\nMvaa 92,248,128\n"
"Ta 170,13,34816\nMvaa 124,248,136\ns_add_u32 s21, s21, s3\nMvaa 124,252,140\nTa 172,13,49152\nMvaa 120,244,140\nTa 174,13,51200\nD1 1,16,46,2048\nMvaa 120,240,136\n"
"s_lshl_b32 s21, s21, 4\ns_add_u32 s21, s21, 1024\nMvaa 116,232,136\nTa 176,14,32768\nCV 40,80,81\nMvaa 116,236,140\nTa 178,14,34816\nMvaa 112,228,140\ns_xor_b32 s3, s3, 28\n"
"CV 41,82,83\nMvaa 112,224,136\nTa 180,14,49152\nD1 1,16,46,3072\nMvaa 144,224,144\nTa 182,14,51200\ns_bfe_u32 s41, s3, 0x30002\nMvaa 144,228,148\ns_cmp_gt_u32 s22, 6\n"
"s_cselect_b32 s2, 1179648, 0\ns_cselect_b32 s1, 16, 0\nMvaa 148,236,148\nTa 184,15,32768\nMvaa 148,232,144\nTa 186,15,34816\nCV 42,84,85\nMvaa 152,240,144\ns_add_u32 s41, s41, s1\n"
"Mvaa 152,244,148\nTa 188,15,49152\nCV 43,86,87\nMvaa 156,252,148\nTa 190,15,51200\ns_lshl4_add_u32 s41, s41, 0xc048400\nMvaa 156,248,144\ns_and_b32 s1, s3, 28\nCV 44,88,89\n"
"Mvaa 188,248,152\ns_add_u32 s3, s3, s1\nMvaa 188,252,156\ns_lshl_b32 s3, s3, 11\nMvaa 184,244,156\ns_add_u32 s2, s2, s3\nCV 45,90,91\nMvaa 184,240,152\ns_lshl_b32 s3, s22, 17\n"
"s_add_u32 s2, s2, s3\nMvaa 180,232,152\ns_add_u32 s20, s2, 0xce0000\nCV 46,92,93\nMvaa 180,236,156\ns_lshl_b32 s2, s22, 7\nMvaa 176,228,156\ns_add_u32 s40, s2, 0xc04b800\n"
"CV 47,94,95\nMvaa 176,224,152\nWL 0\ns_barrier\nMvaa 64,160,128\nRa 192,10,49152\ns_add_u32 m0, s47, 32768\ns_add_u32 s46, s45, 0x8000\nMvaa 64,164,132\ns_mul_i32 s2, s21, 896\n"
"Mvaa 68,172,132\nRa 196,11,49152\nD1 1,16,46,0\nMvaa 68,168,128\ns_lshl_b32 s3, s22, 6\nCV 60,120,121\nMvaa 72,176,128\nRa 200,10,51200\nMvaa 72,180,132\ns_add_u32 s22, s2, s3\n"
"CV 61,122,123\nMvaa 76,188,132\nRa 204,11,51200\ns_add_u32 s23, s22, 57344\nMvaa 76,184,128\nD1 1,16,46,1024\nCV 62,124,125\nMvaa 108,184,136\nRa 208,10,57344\nMvaa 108,188,140\n"
"CV 63,126,127\nMvaa 104,180,140\nRa 212,11,57344\nCV 56,112,113\nMvaa 104,176,136\nCV 57,114,115\nMvaa 100,168,136\nRa 216,10,59392\nD1 1,16,46,2048\nMvaa 100,172,140\n"
"CV 58,116,117\nMvaa 96,164,140\nRa 220,11,59392\nCV 59,118,119\nMvaa 96,160,136\nCV 32,64,65\nMvaa 128,160,144\nCV 33,66,67\nCV 34,68,69\nMvaa 128,164,148\nD1 1,16,46,3072\n"
"Mvaa 132,172,148\nCV 35,70,71\nMvaa 132,168,144\nCV 36,72,73\nCV 37,74,75\nMvaa 136,176,144\ns_add_u32 m0, s47, 49152\ns_add_u32 s46, s45, 0x400000\nMvaa 136,180,148\nCV 38,76,77\n"
"CV 39,78,79\nMvaa 140,188,148\nD1 1,16,46,0\nMvaa 140,184,144\nCV 52,104,105\nCV 53,106,107\nMvaa 172,184,152\nCV 54,108,109\nMvaa 172,188,156\nCV 55,110,111\nMvaa 168,180,156\n"
"CV 48,96,97\nCV 49,98,99\nMvaa 168,176,152\nD1 1,16,46,1024\nMvaa 164,168,152\nCV 50,100,101\nCV 51,102,103\nMvaa 164,172,156\nCV 104,144,145\nMvaa 160,164,156\nCV 105,146,147\n"
"CV 106,148,149\nMvaa 160,160,152\nWL 0\nMaaa 0,160,192\nD1 1,16,46,2048\nSv 32,1,16,75,0\nMaaa 0,164,196\nCV 107,150,151\nMaaa 4,172,196\nCV 108,152,153\nCV 109,154,155\n"
"Maaa 4,168,192\nCV 110,156,157\nCV 111,158,159\nMaaa 8,176,192\nCV 124,184,185\nCV 125,186,187\nMaaa 8,180,196\nD1 1,16,46,3072\nMaaa 12,188,196\nCV 126,188,189\nCV 127,190,191\n"
"Maaa 12,184,192\nCV 120,176,177\nCV 121,178,179\nMaaa 44,184,200\ns_add_u32 m0, s47, -49152\ns_add_u32 s46, s45, 0x408000\nSv 36,1,16,75,1024\nMaaa 44,188,204\nCV 122,180,181\n"
"Maaa 40,180,204\nD1 1,16,46,0\nCV 123,182,183\nMaaa 40,176,200\nCV 96,128,129\nCV 97,130,131\nMaaa 36,168,200\nCV 98,132,133\nCV 99,134,135\nMaaa 36,172,204\nCV 100,136,137\n"
"Maaa 32,164,204\nCV 101,138,139\nCV 102,140,141\nMaaa 32,160,200\nD1 1,16,46,1024\nCV 103,142,143\nMaaa 64,160,208\nSv 40,1,16,75,2048\nCV 116,168,169\n.W547:\n"
"s_cbranch_execz .W551\nMaaa 64,164,212\nCV 117,170,171\nMaaa 68,172,212\nCV 118,172,173\nCV 119,174,175\nMaaa 68,168,208\nCV 112,160,161\nCV 113,162,163\nMaaa 72,176,208\n"
"D1 1,16,46,2048\nCV 114,164,165\nMaaa 72,180,212\nCV 115,166,167\nMaaa 76,188,212\nv_add_u32 v2, 0x10000, v1\ns_lshr_b32 s2, s59, 1\nMaaa 76,184,208\nv_add_u32 v24, s2, v2\n"
"v_xor_b32 v26, 0x1000, v24\nMaaa 108,184,216\nSv 44,1,16,75,3072\nv_lshrrev_b32 v20, 4, v0\nMaaa 108,188,220\nD1 1,16,46,3072\nMaaa 104,180,220\nv_lshlrev_b32 v20, 2, v20\n"
"v_and_b32 v0, 15, v0\nMaaa 104,176,216\nv_sub_u32 v0, v0, v20\ns_lshl_b32 s2, s11, 4\nMaaa 100,168,216\nv_add_u32 v3, s2, v0\nv_subrev_u32 v0, 32, v3\nMaaa 100,172,220\n"
"v_mov_b32 v4, 0\nMaaa 96,164,220\nv_mov_b32 v5, 0\nv_mov_b32 v6, 0\nMaaa 96,160,216\nWV 20\ns_barrier\nMaaa 16,224,192\ns_add_u32 s75, s75, s55\nSv 48,1,16,75,0\nMaaa 16,228,196\n"
"v_mov_b32 v7, 0\nMaaa 20,236,196\nv_mov_b32 v8, 0\nv_mov_b32 v9, 0\nMaaa 20,232,192\nv_mov_b32 v10, 0\nv_mov_b32 v11, 0\nMaaa 24,240,192\nv_mov_b32 v12, 0\nv_mov_b32 v13, 0\n"
"Maaa 24,244,196\nv_mov_b32 v14, 0\nMaaa 28,252,196\nv_mov_b32 v15, 0\nv_mov_b32 v16, 0\nMaaa 28,248,192\nv_mov_b32 v17, 0\nv_mov_b32 v18, 0\nMaaa 60,248,200\nSv 52,1,16,75,1024\n"
"v_mov_b32 v19, 0\nMaaa 60,252,204\nv_mov_b32 v86, 0\nMaaa 56,244,204\nv_mov_b32 v87, 0\nv_mov_b32 v88, 0\nMaaa 56,240,200\nv_mov_b32 v89, 0\nAR 92,0\nAR 93,1\nMaaa 52,232,200\n"
"CV 128,92,93\nAR 92,2\nAR 93,3\nMaaa 52,236,204\nCV 129,92,93\nAR 92,4\nAR 93,5\nMaaa 48,228,204\nRa 128,24,0\nCV 130,92,93\nAR 92,6\nMaaa 48,224,200\nAR 93,7\nCV 131,92,93\n"
"AR 92,8\nMaaa 80,224,208\nSv 56,1,16,75,2048\nRa 132,24,1024\nAR 93,9\nMaaa 80,228,212\nCV 132,92,93\nAR 92,10\nAR 93,11\nMaaa 84,236,212\nRa 136,24,2048\nCV 133,92,93\nAR 92,12\n"
"Maaa 84,232,208\nAR 93,13\nCV 134,92,93\nAR 92,14\nMaaa 88,240,208\nRa 140,24,3072\nAR 93,15\nCV 135,92,93\nMaaa 88,244,212\nAR 92,40\nAR 93,41\nCV 148,92,93\nMaaa 92,252,212\n"
"Ra 144,26,0\nAR 92,42\nAR 93,43\nMaaa 92,248,208\nCV 149,92,93\nAR 92,44\nAR 93,45\nMaaa 124,248,216\nSv 60,1,16,75,3072\nRa 148,26,1024\nCV 150,92,93\nMaaa 124,252,220\nAR 92,46\n"
"AR 93,47\nCV 151,92,93\nMaaa 120,244,220\nRa 152,26,2048\nAR 92,32\nAR 93,33\nMaaa 120,240,216\nCV 144,92,93\nAR 92,34\nAR 93,35\nMaaa 116,232,216\nRa 156,26,3072\nCV 145,92,93\n"
"AR 92,36\nMaaa 116,236,220\nAR 93,37\nCV 146,92,93\nAR 92,38\nMaaa 112,228,220\nAR 93,39\nCV 147,92,93\nAR 92,16\nMaaa 112,224,216\nWL 4\nCvav 32,128,192,4\nRa 160,24,8192\n"
"AR 93,17\nCV 136,92,93\nMvav 32,132,200\nAR 92,18\nAR 93,19\nCV 137,92,93\nMvav 32,136,208\nRa 164,24,9216\nAR 92,20\nAR 93,21\nMvav 32,140,216\nCV 138,92,93\nAR 92,22\nAR 93,23\n"
"Cvav 36,140,220,8\nRa 168,24,10240\nCV 139,92,93\nAR 92,24\nMvav 36,136,212\ns_add_u32 m0, s89, 32768\nAR 93,25\nCV 140,92,93\nMvav 36,132,204\nRa 172,24,11264\nWL 4\ns_barrier\n"
"Mvav 36,128,196\nAR 92,26\nAR 93,27\nCV 141,92,93\nMvav 36,144,228\nRa 176,26,8192\nAR 92,28\nAR 93,29\nSv 96,1,16,76,0\nMvav 36,148,236\ns_add_u32 s46, s86, s88\nCV 142,92,93\n"
"AR 92,30\nMvav 36,152,244\nRa 180,26,9216\nAR 93,31\nCV 143,92,93\nMvav 36,156,252\nAR 92,56\nAR 93,57\nCV 156,92,93\nMvav 32,156,248\nRa 184,26,10240\nAR 92,58\nAR 93,59\n"
"Mvav 32,152,240\nD1 1,16,46,0\nCV 157,92,93\nAR 92,60\nMvav 32,148,232\nRa 188,26,11264\nWL 4\nAR 93,61\nMvav 32,144,224\nCV 158,92,93\nAR 92,62\nAR 93,63\nCvav 40,160,192,12\n"
"CV 159,92,93\nAR 92,48\nAR 93,49\nMvav 40,164,200\nCV 152,92,93\nAR 92,50\nAR 93,51\nMvav 40,168,208\nCV 153,92,93\nAR 92,52\nAR 93,53\nv_and_b32 v27, 0x70, v1\n"
"buffer_load_dword v27, v27, s[16:19], s61 offen offset:136 sc0 sc1\nMvav 40,172,216\nCV 154,92,93\nAR 92,54\nAR 93,55\nCvav 44,172,220,16\nCV 155,92,93\nAR 92,64\nAR 93,65\n"
"Mvav 44,168,212\nD1 1,16,46,1024\nCV 160,92,93\nAR 92,66\nMvav 44,164,204\nWL 0\nAR 93,67\nCV 161,92,93\nMvav 44,160,196\nAR 92,68\nAR 93,69\nCV 162,92,93\nMvav 44,176,228\n"
"AR 92,70\nAR 93,71\nCV 163,92,93\nSv 100,1,16,76,1024\nMvav 44,180,236\ns_add_u32 s88, s88, 32768\nEX 80,32\nMvav 44,184,244\nAR 92,72\nAR 93,73\nCV 164,92,93\nMvav 44,188,252\n"
"EX 81,33\nMvav 40,188,248\nAR 92,74\nAR 93,75\nCV 165,92,93\nMvav 40,184,240\nD1 1,16,46,2048\nAR 92,76\nAR 93,77\nMvav 40,180,232\nCV 166,92,93\nAR 92,78\nAR 93,79\n"
"Mvav 40,176,224\nAD 84,80,81\nCV 64,80,81\nCV 167,92,93\nAR 92,80\nAR 93,81\nCV 168,92,93\nEX 82,34\nAR 92,82\nAR 93,83\nCV 169,92,93\nAR 92,84\nWV 17\nEX 83,35\nAD 84,84,82\n"
"AR 93,85\nCV 170,92,93\nD1 1,16,46,3072\ns_add_u32 m0, s89, 49152\nAR 92,86\ns_barrier\nAR 93,87\nCV 171,92,93\nAD 84,84,83\nCV 65,82,83\nAR 92,88\nRa 128,24,32768\nAR 93,89\n"
"CV 172,92,93\nSv 104,1,16,76,2048\ns_add_u32 s46, s87, s88\nEX 80,36\nRa 132,24,33792\nAR 92,90\nAR 93,91\nEX 81,37\nRa 136,24,34816\nCV 173,92,93\nAR 92,92\nD1 1,16,46,0\n"
"AR 93,93\nCV 174,92,93\nRa 140,24,35840\nAR 92,94\nAR 93,95\nAD 85,80,81\nCV 68,80,81\nCV 175,92,93\nRa 144,26,32768\nAR 92,104\nAR 93,105\nEX 82,38\nCV 180,92,93\n"
"Ra 148,26,33792\nAR 92,106\nAR 93,107\nEX 83,39\nRa 152,26,34816\nAD 85,85,82\nCV 181,92,93\nD1 1,16,46,1024\nAR 92,108\nAR 93,109\nRa 156,26,35840\nCV 182,92,93\nAR 92,110\n"
"AD 85,85,83\nCV 69,82,83\nAR 93,111\nRa 160,24,40960\nCV 183,92,93\nAR 92,96\nSv 108,1,16,76,3072\ns_xor_b32 s89, s89, 0x10000\nEX 80,40\nRa 164,24,41984\nAR 93,97\nCV 176,92,93\n"
"WL 6\nEX 81,41\nRa 168,24,43008\nAD 84,84,80\nAR 92,98\nD1 1,16,46,2048\nAR 93,99\nCV 177,92,93\nRa 172,24,44032\nAR 92,100\nAR 93,101\nAD 84,84,81\nCV 66,80,81\nCV 178,92,93\n"
"Cvav 48,128,192,4\nRa 176,26,40960\nAR 92,102\nAR 93,103\nMvav 48,132,200\nEX 82,42\nCV 179,92,93\nMvav 48,136,208\nRa 180,26,41984\nAR 92,120\nAR 93,121\nMvav 48,140,216\nWL 6\n"
"EX 83,43\nWV 9\nv_cmp_ne_u32 vcc, s15, v27\ns_nop 0\ns_cbranch_vccnz .S41\n.S42:\nCvav 52,140,220,8\nRa 184,26,43008\nAD 84,84,82\nCV 188,92,93\nMvav 52,136,212\nD1 1,16,46,3072\n"
"s_add_u32 m0, s89, 0\nAR 92,122\nMvav 52,132,204\nRa 188,26,44032\nAR 93,123\nCV 189,92,93\nMvav 52,128,196\nAD 84,84,83\nCV 67,82,83\nAR 92,124\nMvav 52,144,228\nRa 128,2,49152\n"
"AR 93,125\nCV 190,92,93\ns_add_u32 s76, s76, s55\nSv 112,1,16,76,0\nMvav 52,148,236\ns_add_u32 s46, s86, s88\nEX 80,44\nMvav 52,152,244\nRa 132,2,50176\n"
"v_xor_b32 v24, 0x10000, v24\nAR 92,126\nMvav 52,156,252\nWL 6\nEX 81,45\nMvav 48,156,248\nRa 136,2,51200\nAD 85,85,80\nAR 93,127\nMvav 48,152,240\nD1 1,16,46,0\nCV 191,92,93\n"
"AR 92,112\nMvav 48,148,232\nRa 140,2,52224\nAR 93,113\nCV 184,92,93\nMvav 48,144,224\nAD 85,85,81\nCV 70,80,81\nAR 92,114\nCvav 56,160,192,12\nRa 144,2,53248\nAR 93,115\n"
"CV 185,92,93\nMvav 56,164,200\nEX 82,46\nAR 92,116\nMvav 56,168,208\nRa 148,2,54272\nv_xor_b32 v26, 0x10000, v26\nAR 93,117\nMvav 56,172,216\nWL 6\nEX 83,47\nCvav 60,172,220,16\n"
"Ra 152,2,55296\nAD 85,85,82\nCV 186,92,93\nMvav 60,168,212\nD1 1,16,46,1024\nAR 92,118\nAR 93,119\nMvav 60,164,204\nRa 156,2,56320\nCV 187,92,93\nMvav 60,160,196\nAD 85,85,83\n"
"CV 71,82,83\nMvav 60,176,228\nRa 160,2,57344\nv_max_f32 v90, v84, v85\nSv 116,1,16,76,1024\nMvav 60,180,236\ns_add_u32 s88, s88, 32768\nEX 80,48\nMvav 60,184,244\nRa 164,2,58368\n"
"v_cmp_lt_f32 vcc, s63, v90\nMvav 60,188,252\nWL 8\nEX 81,49\nMvav 56,188,248\nRa 168,2,59392\ns_cbranch_vccnz .T23\n.T24:\nv_mov_b32 v20, v84\nv_mov_b32 v21, v85\n"
"v_mov_b32 v22, v20\nv_mov_b32 v23, v21\nMvav 56,184,240\nD1 1,16,46,2048\nv_permlane32_swap_b32 v20, v22\nv_permlane32_swap_b32 v21, v23\nAD 20,20,22\nAD 21,21,23\n"
"v_mov_b32 v22, v20\nv_mov_b32 v23, v21\nMvav 56,180,232\nRa 172,2,60416\nv_permlane16_swap_b32 v20, v22\nv_permlane16_swap_b32 v21, v23\nAD 20,20,22\nAD 21,21,23\n"
"v_min_f32 v90, v20, v21\nv_cmp_gt_f32 vcc, s64, v90\ns_nop 0\ns_cbranch_vccnz .T25\n.T26:\nAD 86,86,84\nMvav 56,176,224\nAD 84,80,81\nCV 72,80,81\nZaav 0,128,64\nRa 176,2,61440\n"
"Zaav 4,128,68\nEX 82,50\nZaav 12,132,68\nRa 180,2,62464\nZaav 8,132,64\nWL 8\nEX 83,51\nZaav 16,136,64\nRa 184,2,63488\nAD 84,84,82\nZaav 20,136,68\nD1 1,16,46,3072\n"
"s_add_u32 m0, s89, 16384\nZaav 28,140,68\nRa 188,2,64512\ns_barrier\nZaav 24,140,64\nAD 84,84,83\nCV 73,82,83\nZaav 32,144,64\nRa 128,24,0\nSv 120,1,16,76,2048\nZaav 36,144,68\n"
"s_add_u32 s46, s87, s88\nEX 80,52\nbuffer_store_dword v25, off, s[16:19], s54 offset:12 sc0 sc1\nZaav 44,148,68\nRa 132,24,1024\nv_xor_b32 v2, 0x10000, v2\nZaav 40,148,64\nWL 8\n"
"EX 81,53\nZaav 48,152,64\nRa 136,24,2048\nAD 87,87,85\nZaav 52,152,68\nD1 1,16,46,0\nZaav 60,156,68\nRa 140,24,3072\nZaav 56,156,64\nAD 85,80,81\nCV 76,80,81\nZaav 64,160,64\n"
"Ra 144,26,0\nZaav 68,160,68\nEX 82,54\nZaav 76,164,68\nRa 148,26,1024\nZaav 72,164,64\nWL 8\nEX 83,55\nZaav 80,168,64\nRa 152,26,2048\nAD 85,85,82\nZaav 84,168,68\n"
"D1 1,16,46,1024\nZaav 92,172,68\nRa 156,26,3072\nZaav 88,172,64\nAD 85,85,83\nCV 77,82,83\nZaav 96,176,64\nRa 160,24,8192\nSv 124,1,16,76,3072\nZaav 100,176,68\nEX 80,56\n.W548:\n"
"s_cbranch_execz .W552\nZaav 108,180,68\nRa 164,24,9216\nZaav 104,180,64\nWL 6\nEX 81,57\nZaav 112,184,64\nRa 168,24,10240\nAD 84,84,80\nZaav 116,184,68\nD1 1,16,46,2048\n"
"Zaav 124,188,68\nRa 172,24,11264\nZaav 120,188,64\nAD 84,84,81\nCV 74,80,81\nCvav 32,128,192,4\nRa 176,26,8192\nMvav 32,132,200\nEX 82,58\nMvav 32,136,208\nRa 180,26,9216\n"
"Mvav 32,140,216\nWL 6\nEX 83,59\nCvav 36,140,220,8\nRa 184,26,10240\nAD 84,84,82\nMvav 36,136,212\nD1 1,16,46,3072\ns_add_u32 m0, s89, 32768\nMvav 36,132,204\nRa 188,26,11264\n"
"Mvav 36,128,196\nAD 84,84,83\nCV 75,82,83\nMvav 36,144,228\nRa 128,2,16384\nSv 128,1,16,77,0\nMvav 36,148,236\ns_add_u32 s46, s86, s88\nEX 80,60\nMvav 36,152,244\nRa 132,2,17408\n"
"Mvav 36,156,252\nWL 6\nEX 81,61\nMvav 32,156,248\nRa 136,2,18432\nAD 85,85,80\nMvav 32,152,240\nD1 1,16,46,0\nMvav 32,148,232\nRa 140,2,19456\nMvav 32,144,224\nAD 85,85,81\n"
"CV 78,80,81\nCvav 40,160,192,12\nRa 144,2,20480\nMvav 40,164,200\nEX 82,62\nMvav 40,168,208\nRa 148,2,21504\nMvav 40,172,216\nWL 6\nEX 83,63\nCvav 44,172,220,16\nRa 152,2,22528\n"
"AD 85,85,82\nMvav 44,168,212\nD1 1,16,46,1024\nMvav 44,164,204\nRa 156,2,23552\nMvav 44,160,196\nAD 85,85,83\nCV 79,82,83\nMvav 44,176,228\nRa 160,2,24576\n"
"v_max_f32 v90, v84, v85\nSv 132,1,16,77,1024\nMvav 44,180,236\ns_add_u32 s88, s88, 32768\nEX 80,32\nMvav 44,184,244\nRa 164,2,25600\nv_cmp_lt_f32 vcc, s63, v90\nMvav 44,188,252\n"
"WL 8\nEX 81,33\nMvav 40,188,248\nRa 168,2,26624\ns_cbranch_vccnz .T28\n.T29:\nMvav 40,184,240\nD1 1,16,46,2048\nMvav 40,180,232\nRa 172,2,27648\nAD 86,86,84\nMvav 40,176,224\n"
"AD 84,80,81\nCV 64,80,81\nMaav 0,128,72\nRa 176,2,28672\nv_mov_b32 v112, -1\nMaav 4,128,76\nEX 82,34\nMaav 12,132,76\nRa 180,2,29696\nMaav 8,132,72\n"
"s_waitcnt vmcnt(18) lgkmcnt(8)\nEX 83,35\nMaav 16,136,72\nRa 184,2,30720\nAD 84,84,82\nMaav 20,136,76\nD1 1,16,46,3072\ns_add_u32 m0, s89, 49152\nMaav 28,140,76\nRa 188,2,31744\n"
"s_barrier\nMaav 24,140,72\nAD 84,84,83\nCV 65,82,83\nMaav 32,144,72\nRa 128,24,32768\nSv 136,1,16,77,2048\nMaav 36,144,76\ns_add_u32 s46, s87, s88\nEX 80,36\nMaav 44,148,76\n"
"Ra 132,24,33792\nMaav 40,148,72\nWL 8\nEX 81,37\nMaav 48,152,72\nRa 136,24,34816\nAD 87,87,85\nMaav 52,152,76\nD1 1,16,46,0\nMaav 60,156,76\nRa 140,24,35840\nMaav 56,156,72\n"
"AD 85,80,81\nCV 68,80,81\nMaav 64,160,72\nRa 144,26,32768\nv_mov_b32 v113, -1\nMaav 68,160,76\nEX 82,38\nMaav 76,164,76\nRa 148,26,33792\nMaav 72,164,72\nWL 8\nEX 83,39\n"
"Maav 80,168,72\nRa 152,26,34816\nAD 85,85,82\nMaav 84,168,76\nD1 1,16,46,1024\nMaav 92,172,76\nRa 156,26,35840\nMaav 88,172,72\nAD 85,85,83\nCV 69,82,83\nMaav 96,176,72\n"
"Ra 160,24,40960\nSv 140,1,16,77,3072\nMaav 100,176,76\ns_xor_b32 s89, s89, 0x10000\nEX 80,40\nMaav 108,180,76\nRa 164,24,41984\nMaav 104,180,72\nWL 6\nEX 81,41\nMaav 112,184,72\n"
"Ra 168,24,43008\nAD 84,84,80\nMaav 116,184,76\nD1 1,16,46,2048\nMaav 124,188,76\nRa 172,24,44032\nMaav 120,188,72\nAD 84,84,81\nCV 66,80,81\nCvav 48,128,192,4\nRa 176,26,40960\n"
"v_mov_b32 v114, -1\nMvav 48,132,200\nEX 82,42\nMvav 48,136,208\nRa 180,26,41984\nMvav 48,140,216\nWL 6\nEX 83,43\nCvav 52,140,220,8\nRa 184,26,43008\nAD 84,84,82\n"
"v_and_b32 v27, 0x70, v1\nbuffer_load_dword v27, v27, s[16:19], s61 offen offset:140 sc0 sc1\nMvav 52,136,212\nD1 1,16,46,3072\ns_add_u32 m0, s89, 0\nMvav 52,132,204\n"
"Ra 188,26,44032\nMvav 52,128,196\nAD 84,84,83\nCV 67,82,83\nMvav 52,144,228\nRa 128,2,49152\ns_add_u32 s77, s77, s55\nSv 144,1,16,77,0\nMvav 52,148,236\ns_add_u32 s46, s86, s88\n"
"EX 80,44\nMvav 52,152,244\nRa 132,2,50176\nv_xor_b32 v24, 0x10000, v24\nMvav 52,156,252\nWL 6\nEX 81,45\nMvav 48,156,248\nRa 136,2,51200\nAD 85,85,80\nMvav 48,152,240\n"
"D1 1,16,46,0\nMvav 48,148,232\nRa 140,2,52224\nMvav 48,144,224\nAD 85,85,81\nCV 70,80,81\nCvav 56,160,192,12\nRa 144,2,53248\nv_mov_b32 v115, -1\nMvav 56,164,200\nEX 82,46\n"
"Mvav 56,168,208\nRa 148,2,54272\nv_xor_b32 v26, 0x10000, v26\nMvav 56,172,216\nWL 6\nEX 83,47\nCvav 60,172,220,16\nRa 152,2,55296\nAD 85,85,82\nMvav 60,168,212\nD1 1,16,46,1024\n"
"Mvav 60,164,204\nRa 156,2,56320\nMvav 60,160,196\nAD 85,85,83\nCV 71,82,83\nMvav 60,176,228\nRa 160,2,57344\nv_max_f32 v90, v84, v85\nSv 148,1,16,77,1024\nMvav 60,180,236\n"
"s_add_u32 s88, s88, 32768\nEX 80,48\nMvav 60,184,244\nRa 164,2,58368\nv_cmp_lt_f32 vcc, s63, v90\nMvav 60,188,252\nWL 8\nEX 81,49\nMvav 56,188,248\nRa 168,2,59392\n"
"s_cbranch_vccnz .T30\n.T31:\nMvav 56,184,240\nD1 1,16,46,2048\nMvav 56,180,232\nRa 172,2,60416\nAD 86,86,84\nMvav 56,176,224\nAD 84,80,81\nCV 72,80,81\nMaav 0,128,64\n"
"Ra 176,2,61440\nMaav 4,128,68\nEX 82,50\nMaav 12,132,68\nRa 180,2,62464\nMaav 8,132,64\ns_waitcnt vmcnt(18) lgkmcnt(8)\nEX 83,51\nMaav 16,136,64\nRa 184,2,63488\nAD 84,84,82\n"
"Maav 20,136,68\nD1 1,16,46,3072\ns_add_u32 m0, s89, 16384\nMaav 28,140,68\nRa 188,2,64512\ns_barrier\nMaav 24,140,64\nAD 84,84,83\nCV 73,82,83\nMaav 32,144,64\nRa 128,24,0\n"
"Sv 152,1,16,77,2048\nMaav 36,144,68\ns_add_u32 s46, s87, s88\nEX 80,52\nMaav 44,148,68\nRa 132,24,1024\nv_xor_b32 v2, 0x10000, v2\nMaav 40,148,64\nWL 8\nEX 81,53\nMaav 48,152,64\n"
"Ra 136,24,2048\nAD 87,87,85\nbuffer_store_dword v25, off, s[16:19], s54 offset:8 sc0 sc1\nMaav 52,152,68\nD1 1,16,46,0\nMaav 60,156,68\nRa 140,24,3072\nMaav 56,156,64\n"
"AD 85,80,81\nCV 76,80,81\nMaav 64,160,64\nRa 144,26,0\nMaav 68,160,68\nEX 82,54\nMaav 76,164,68\nRa 148,26,1024\nMaav 72,164,64\nWL 8\nEX 83,55\nMaav 80,168,64\nRa 152,26,2048\n"
"AD 85,85,82\nMaav 84,168,68\nD1 1,16,46,1024\nMaav 92,172,68\nRa 156,26,3072\nMaav 88,172,64\nAD 85,85,83\nCV 77,82,83\nMaav 96,176,64\nRa 160,24,8192\nSv 156,1,16,77,3072\n"
"Maav 100,176,68\nEX 80,56\nMaav 108,180,68\nRa 164,24,9216\nMaav 104,180,64\nWL 6\nEX 81,57\nMaav 112,184,64\nRa 168,24,10240\nAD 84,84,80\nMaav 116,184,68\nD1 1,16,46,2048\n"
"Maav 124,188,68\nRa 172,24,11264\nMaav 120,188,64\nAD 84,84,81\nCV 74,80,81\nCvav 32,128,192,4\nRa 176,26,8192\nMvav 32,132,200\nEX 82,58\nMvav 32,136,208\nRa 180,26,9216\n"
"Mvav 32,140,216\nWL 6\nEX 83,59\nCvav 36,140,220,8\nRa 184,26,10240\nAD 84,84,82\nMvav 36,136,212\nD1 1,16,46,3072\ns_add_u32 m0, s89, 32768\nMvav 36,132,204\nRa 188,26,11264\n"
"Mvav 36,128,196\nAD 84,84,83\nCV 75,82,83\nMvav 36,144,228\nRa 128,2,16384\nSv 160,1,16,78,0\nMvav 36,148,236\ns_add_u32 s46, s86, s88\nEX 80,60\nMvav 36,152,244\nRa 132,2,17408\n"
"Mvav 36,156,252\nWL 6\nEX 81,61\nMvav 32,156,248\nRa 136,2,18432\nAD 85,85,80\nMvav 32,152,240\nD1 1,16,46,0\nMvav 32,148,232\nRa 140,2,19456\nSv 112,1,16,49,0\nMvav 32,144,224\n"
"AD 85,85,81\nCV 78,80,81\nCvav 40,160,192,12\nRa 144,2,20480\nMvav 40,164,200\nEX 82,62\nMvav 40,168,208\nRa 148,2,21504\nMvav 40,172,216\nWL 6\nEX 83,63\nCvav 44,172,220,16\n"
"Ra 152,2,22528\nAD 85,85,82\nMvav 44,168,212\nD1 1,16,46,1024\nMvav 44,164,204\nRa 156,2,23552\nMvav 44,160,196\nAD 85,85,83\nCV 79,82,83\nMvav 44,176,228\nRa 160,2,24576\n"
"v_max_f32 v90, v84, v85\nSv 164,1,16,78,1024\nMvav 44,180,236\ns_add_u32 s88, s88, 32768\nEX 80,32\nMvav 44,184,244\nRa 164,2,25600\nv_cmp_lt_f32 vcc, s63, v90\nMvav 44,188,252\n"
"WL 8\nEX 81,33\nMvav 40,188,248\nRa 168,2,26624\ns_cbranch_vccnz .T33\n.T34:\nMvav 40,184,240\nD1 1,16,46,2048\nMvav 40,180,232\nRa 172,2,27648\nAD 86,86,84\nMvav 40,176,224\n"
"AD 84,80,81\nCV 64,80,81\nMaav 0,128,72\nRa 176,2,28672\nMaav 4,128,76\nEX 82,34\nMaav 12,132,76\nRa 180,2,29696\nMaav 8,132,72\ns_waitcnt vmcnt(19) lgkmcnt(8)\nEX 83,35\n"
"Maav 16,136,72\nRa 184,2,30720\nAD 84,84,82\nMaav 20,136,76\nD1 1,16,46,3072\ns_add_u32 m0, s89, 49152\nMaav 28,140,76\nRa 188,2,31744\ns_barrier\nMaav 24,140,72\nAD 84,84,83\n"
"CV 65,82,83\nMaav 32,144,72\nRa 128,24,32768\nSv 168,1,16,78,2048\nMaav 36,144,76\ns_add_u32 s46, s87, s88\nEX 80,36\nMaav 44,148,76\nRa 132,24,33792\nMaav 40,148,72\nWL 8\n"
"EX 81,37\nMaav 48,152,72\nRa 136,24,34816\nAD 87,87,85\nMaav 52,152,76\nD1 1,16,46,0\nMaav 60,156,76\nRa 140,24,35840\nMaav 56,156,72\nAD 85,80,81\nCV 68,80,81\nMaav 64,160,72\n"
"Ra 144,26,32768\nMaav 68,160,76\nEX 82,38\nMaav 76,164,76\nRa 148,26,33792\nMaav 72,164,72\nWL 8\nEX 83,39\nMaav 80,168,72\nRa 152,26,34816\nAD 85,85,82\nMaav 84,168,76\n"
"D1 1,16,46,1024\nMaav 92,172,76\nRa 156,26,35840\nMaav 88,172,72\nAD 85,85,83\nCV 69,82,83\nMaav 96,176,72\nRa 160,24,40960\nSv 172,1,16,78,3072\nMaav 100,176,76\n"
"s_xor_b32 s89, s89, 0x10000\nEX 80,40\nMaav 108,180,76\nRa 164,24,41984\nMaav 104,180,72\nWL 6\nEX 81,41\nMaav 112,184,72\nRa 168,24,43008\nAD 84,84,80\nMaav 116,184,76\n"
"D1 1,16,46,2048\nMaav 124,188,76\nRa 172,24,44032\nMaav 120,188,72\nAD 84,84,81\nCV 66,80,81\nCvav 48,128,192,4\nRa 176,26,40960\nMvav 48,132,200\nEX 82,42\nMvav 48,136,208\n"
"Ra 180,26,41984\nMvav 48,140,216\nWL 6\nEX 83,43\nv_cmp_ne_u32 vcc, s15, v27\ns_nop 0\ns_cbranch_vccnz .S44\n.S45:\nv_and_b32 v27, 0x70, v1\n"
"buffer_load_dword v27, v27, s[16:19], s61 offen offset:12 sc0 sc1\nCvav 52,140,220,8\nRa 184,26,43008\nAD 84,84,82\nMvav 52,136,212\nD1 1,16,46,3072\ns_add_u32 m0, s89, 0\n"
"Mvav 52,132,204\nRa 188,26,44032\nMvav 52,128,196\nAD 84,84,83\nCV 67,82,83\nMvav 52,144,228\nRa 128,2,49152\ns_add_u32 s78, s78, s55\nSv 176,1,16,78,0\nMvav 52,148,236\n"
"s_add_u32 s46, s86, s88\nEX 80,44\nMvav 52,152,244\nRa 132,2,50176\nv_xor_b32 v24, 0x10000, v24\nMvav 52,156,252\nWL 6\nEX 81,45\nMvav 48,156,248\nRa 136,2,51200\nAD 85,85,80\n"
"Mvav 48,152,240\nD1 1,16,46,0\nMvav 48,148,232\nRa 140,2,52224\nSv 112,1,16,49,1024\nMvav 48,144,224\nAD 85,85,81\nCV 70,80,81\nCvav 56,160,192,12\nRa 144,2,53248\n"
"v_lshrrev_b32 v92, 8, v1\nMvav 56,164,200\ns_add_u32 s49, s49, 131072\nEX 82,46\nMvav 56,168,208\nRa 148,2,54272\nv_xor_b32 v26, 0x10000, v26\nMvav 56,172,216\nWL 6\nEX 83,47\n"
"Cvav 60,172,220,16\nRa 152,2,55296\nAD 85,85,82\nMvav 60,168,212\nD1 1,16,46,1024\nMvav 60,164,204\nRa 156,2,56320\nv_bfe_u32 v93, v1, 6, 2\nMvav 60,160,196\nAD 85,85,83\n"
"CV 71,82,83\nMvav 60,176,228\n.W549:\ns_cbranch_execz .W553\nRa 160,2,57344\nv_max_f32 v90, v84, v85\nSv 180,1,16,78,1024\nMvav 60,180,236\ns_add_u32 s88, s88, 32768\nEX 80,48\n"
"Mvav 60,184,244\nRa 164,2,58368\nv_cmp_lt_f32 vcc, s63, v90\nMvav 60,188,252\nWL 8\nEX 81,49\nMvav 56,188,248\nRa 168,2,59392\ns_cbranch_vccnz .T35\n.T36:\nMvav 56,184,240\n"
"D1 1,16,46,2048\nv_cmp_eq_u32 vcc, v92, v93\nv_and_b32 v92, 16, v1\nv_mov_b32 v93, 0x3f80\nv_cndmask_b32 v93, 0, v93, vcc\nMvav 56,180,232\nRa 172,2,60416\nAD 86,86,84\n"
"Mvav 56,176,224\nAD 84,80,81\nCV 72,80,81\nMaav 0,128,64\nRa 176,2,61440\nv_lshlrev_b32 v93, v92, v93\nMaav 4,128,68\nEX 82,50\nMaav 12,132,68\nRa 180,2,62464\n"
"v_and_b32 v92, 32, v1\nMaav 8,132,64\ns_waitcnt vmcnt(20) lgkmcnt(8)\nEX 83,51\nMaav 16,136,64\nRa 184,2,63488\nAD 84,84,82\nMaav 20,136,68\nD1 1,16,46,3072\n"
"s_add_u32 m0, s89, 16384\nMaav 28,140,68\nRa 188,2,64512\ns_barrier\nMaav 24,140,64\nAD 84,84,83\nCV 73,82,83\nMaav 32,144,64\nRa 128,24,0\nSv 184,1,16,78,2048\nMaav 36,144,68\n"
"s_add_u32 s46, s87, s88\nEX 80,52\nMaav 44,148,68\nRa 132,24,1024\nv_xor_b32 v2, 0x10000, v2\nMaav 40,148,64\nWL 8\nEX 81,53\nMaav 48,152,64\nRa 136,24,2048\nAD 87,87,85\n"
"Maav 52,152,68\nD1 1,16,46,0\nv_cmp_eq_u32 vcc, 0, v92\nv_mov_b32 v128, 0\nv_mov_b32 v129, 0\nv_cndmask_b32 v130, 0, v93, vcc\nMaav 60,156,68\nRa 140,24,3072\n"
"v_sub_u32 v131, v93, v130\nMaav 56,156,64\nAD 85,80,81\nCV 76,80,81\nMaav 64,160,64\nRa 144,26,0\nv_mov_b32 v132, 0\nMaav 68,160,68\nEX 82,54\nMaav 76,164,68\nRa 148,26,1024\n"
"v_mov_b32 v133, 0\nMaav 72,164,64\nWL 8\nEX 83,55\nMaav 80,168,64\nRa 152,26,2048\nAD 85,85,82\nMaav 84,168,68\nD1 1,16,46,1024\nMaav 92,172,68\nRa 156,26,3072\nMaav 88,172,64\n"
"AD 85,85,83\nCV 77,82,83\nMaav 96,176,64\nRa 160,24,8192\nSv 188,1,16,78,3072\nMaav 100,176,68\nEX 80,56\nMaav 108,180,68\nRa 164,24,9216\nMaav 104,180,64\nWL 6\nEX 81,57\n"
"Maav 112,184,64\nRa 168,24,10240\nAD 84,84,80\nMaav 116,184,68\nD1 1,16,46,2048\nMaav 124,188,68\nRa 172,24,11264\nMaav 120,188,64\nAD 84,84,81\nCV 74,80,81\ns_mov_b32 s99, 11\n"
"s_mov_b32 s90, 0\n.p2align 6\n.A60:\nCvav 32,128,192,4\nRa 176,26,8192\nMvav 32,132,200\nEX 82,58\nMvav 32,136,208\nRa 180,26,9216\nMvav 32,140,216\nWL 6\nEX 83,59\n"
"Cvav 36,140,220,8\nRa 184,26,10240\nAD 84,84,82\nMvav 36,136,212\nD1 1,16,46,3072\ns_add_u32 m0, s89, 32768\nMvav 36,132,204\nRa 188,26,11264\nMvav 36,128,196\nAD 84,84,83\n"
"CV 75,82,83\nMvav 36,144,228\nRa 128,2,16384\nMvav 36,148,236\ns_add_u32 s46, s86, s88\nEX 80,60\nMvav 36,152,244\nRa 132,2,17408\nMvav 36,156,252\nWL 6\nEX 81,61\n"
"Mvav 32,156,248\nRa 136,2,18432\nAD 85,85,80\nMvav 32,152,240\nD1 1,16,46,0\nMvav 32,148,232\nRa 140,2,19456\nSv 112,1,16,49,0\nMvav 32,144,224\nAD 85,85,81\nCV 78,80,81\n"
"Cvav 40,160,192,12\nRa 144,2,20480\nMvav 40,164,200\nEX 82,62\nMvav 40,168,208\nRa 148,2,21504\nMvav 40,172,216\nWL 6\nEX 83,63\nCvav 44,172,220,16\nRa 152,2,22528\nAD 85,85,82\n"
"Mvav 44,168,212\nD1 1,16,46,1024\nMvav 44,164,204\nRa 156,2,23552\nMvav 44,160,196\nAD 85,85,83\nCV 79,82,83\nMvav 44,176,228\nRa 160,2,24576\nv_max_f32 v90, v84, v85\n"
"Mvav 44,180,236\ns_add_u32 s88, s88, 32768\nEX 80,32\nMvav 44,184,244\nRa 164,2,25600\nv_cmp_lt_f32 vcc, s63, v90\nMvav 44,188,252\nWL 8\nEX 81,33\nMvav 40,188,248\n"
"Ra 168,2,26624\ns_cbranch_vccnz .T64\n.T65:\nMvav 40,184,240\nD1 1,16,46,2048\nMvav 40,180,232\nRa 172,2,27648\nAD 86,86,84\nMvav 40,176,224\nAD 84,80,81\nCV 64,80,81\n"
"Maav 0,128,72\nRa 176,2,28672\nMaav 4,128,76\nEX 82,34\nMaav 12,132,76\nRa 180,2,29696\nMaav 8,132,72\ns_waitcnt vmcnt(13) lgkmcnt(8)\nEX 83,35\nMaav 16,136,72\nRa 184,2,30720\n"
"AD 84,84,82\nMaav 20,136,76\nD1 1,16,46,3072\ns_add_u32 m0, s89, 49152\nMaav 28,140,76\nRa 188,2,31744\ns_barrier\nMaav 24,140,72\nAD 84,84,83\nCV 65,82,83\nMaav 32,144,72\n"
"Ra 128,24,32768\nMaav 36,144,76\ns_add_u32 s46, s87, s88\nEX 80,36\nMaav 44,148,76\nRa 132,24,33792\nMaav 40,148,72\nWL 8\nEX 81,37\nMaav 48,152,72\nRa 136,24,34816\nAD 87,87,85\n"
"Maav 52,152,76\nD1 1,16,46,0\nMaav 60,156,76\nRa 140,24,35840\nMaav 56,156,72\nAD 85,80,81\nCV 68,80,81\nMaav 64,160,72\nRa 144,26,32768\nMaav 68,160,76\nEX 82,38\nMaav 76,164,76\n"
"Ra 148,26,33792\nMaav 72,164,72\nWL 8\nEX 83,39\nMaav 80,168,72\nRa 152,26,34816\nAD 85,85,82\nMaav 84,168,76\nD1 1,16,46,1024\nMaav 92,172,76\nRa 156,26,35840\nMaav 88,172,72\n"
"AD 85,85,83\nCV 69,82,83\nMaav 96,176,72\nRa 160,24,40960\nMaav 100,176,76\ns_xor_b32 s89, s89, 0x10000\nEX 80,40\nMaav 108,180,76\nRa 164,24,41984\nMaav 104,180,72\nWL 6\n"
"EX 81,41\nMaav 112,184,72\nRa 168,24,43008\nAD 84,84,80\nMaav 116,184,76\nD1 1,16,46,2048\nMaav 124,188,76\nRa 172,24,44032\nMaav 120,188,72\nAD 84,84,81\nCV 66,80,81\n"
"Cvav 48,128,192,4\nRa 176,26,40960\nMvav 48,132,200\nEX 82,42\nMvav 48,136,208\nRa 180,26,41984\nMvav 48,140,216\nWL 6\nEX 83,43\nCvav 52,140,220,8\nRa 184,26,43008\nAD 84,84,82\n"
"Mvav 52,136,212\nD1 1,16,46,3072\ns_add_u32 m0, s89, 0\nMvav 52,132,204\nRa 188,26,44032\nMvav 52,128,196\nAD 84,84,83\nCV 67,82,83\nMvav 52,144,228\nRa 128,2,49152\n"
"Mvav 52,148,236\ns_add_u32 s46, s86, s88\nEX 80,44\nMvav 52,152,244\nRa 132,2,50176\nv_xor_b32 v24, 0x10000, v24\nMvav 52,156,252\nWL 6\nEX 81,45\nMvav 48,156,248\nRa 136,2,51200\n"
"AD 85,85,80\nMvav 48,152,240\nD1 1,16,46,0\nMvav 48,148,232\nRa 140,2,52224\nSv 112,1,16,49,1024\nMvav 48,144,224\nAD 85,85,81\nCV 70,80,81\nCvav 56,160,192,12\nRa 144,2,53248\n"
"Mvav 56,164,200\ns_add_u32 s49, s49, 131072\nEX 82,46\nMvav 56,168,208\nRa 148,2,54272\nv_xor_b32 v26, 0x10000, v26\nMvav 56,172,216\nWL 6\nEX 83,47\nCvav 60,172,220,16\n"
"Ra 152,2,55296\nAD 85,85,82\nMvav 60,168,212\nD1 1,16,46,1024\nMvav 60,164,204\nRa 156,2,56320\nMvav 60,160,196\nAD 85,85,83\nCV 71,82,83\nMvav 60,176,228\nRa 160,2,57344\n"
"v_max_f32 v90, v84, v85\nMvav 60,180,236\ns_add_u32 s88, s88, 32768\nEX 80,48\nMvav 60,184,244\nRa 164,2,58368\nv_cmp_lt_f32 vcc, s63, v90\nMvav 60,188,252\nWL 8\nEX 81,49\n"
"Mvav 56,188,248\nRa 168,2,59392\ns_cbranch_vccnz .T66\n.T67:\nMvav 56,184,240\nD1 1,16,46,2048\nMvav 56,180,232\nRa 172,2,60416\nAD 86,86,84\nMvav 56,176,224\nAD 84,80,81\n"
"CV 72,80,81\nMaav 0,128,64\nRa 176,2,61440\nMaav 4,128,68\nEX 82,50\nMaav 12,132,68\nRa 180,2,62464\nMaav 8,132,64\ns_waitcnt vmcnt(13) lgkmcnt(8)\nEX 83,51\nMaav 16,136,64\n"
"Ra 184,2,63488\nAD 84,84,82\nMaav 20,136,68\nD1 1,16,46,3072\ns_add_u32 m0, s89, 16384\nMaav 28,140,68\nRa 188,2,64512\ns_barrier\nMaav 24,140,64\nAD 84,84,83\nCV 73,82,83\n"
"Maav 32,144,64\nRa 128,24,0\nMaav 36,144,68\ns_add_u32 s46, s87, s88\nEX 80,52\nMaav 44,148,68\nRa 132,24,1024\nv_xor_b32 v2, 0x10000, v2\nMaav 40,148,64\nWL 8\nEX 81,53\n"
"Maav 48,152,64\nRa 136,24,2048\nAD 87,87,85\nMaav 52,152,68\nD1 1,16,46,0\nMaav 60,156,68\nRa 140,24,3072\nMaav 56,156,64\nAD 85,80,81\nCV 76,80,81\nMaav 64,160,64\nRa 144,26,0\n"
"Maav 68,160,68\nEX 82,54\nMaav 76,164,68\nRa 148,26,1024\nMaav 72,164,64\nWL 8\nEX 83,55\nMaav 80,168,64\nRa 152,26,2048\nAD 85,85,82\nMaav 84,168,68\nD1 1,16,46,1024\n"
"Maav 92,172,68\nRa 156,26,3072\nMaav 88,172,64\nAD 85,85,83\nCV 77,82,83\nMaav 96,176,64\nRa 160,24,8192\nMaav 100,176,68\ns_sub_u32 s90, s90, 1\nEX 80,56\nMaav 108,180,68\n"
"Ra 164,24,9216\ns_cbranch_scc1 .E62\n.E63:\nMaav 104,180,64\nWL 6\nEX 81,57\nMaav 112,184,64\nRa 168,24,10240\nAD 84,84,80\nMaav 116,184,68\nD1 1,16,46,2048\n"
"s_sub_u32 s99, s99, 1\nMaav 124,188,68\nRa 172,24,11264\nMaav 120,188,64\nAD 84,84,81\nCV 74,80,81\ns_cbranch_scc0 .A60\ns_branch .A68\n.E62:\ns_swappc_b64 s[80:81], s[8:9]\n"
"s_branch .E63\n.HD13:\nbuffer_store_dword v25, off, s[16:19], s54 offset:4 sc0 sc1\nv_cmp_ne_u32 vcc, s15, v27\ns_nop 0\ns_cbranch_vccnz .S47\n.S48:\nv_and_b32 v27, 0x70, v1\n"
"buffer_load_dword v27, v27, s[16:19], s61 offen offset:8 sc0 sc1\ns_mov_b32 s90, 1\ns_swappc_b64 s[8:9], s[80:81]\nv_cmp_ne_u32 vcc, s15, v27\ns_nop 0\ns_cbranch_vccnz .S50\n"
".S51:\nv_and_b32 v27, 0xf0, v1\nbuffer_load_dword v27, v27, s[16:19], s61 offen offset:4 sc0 sc1\ns_mov_b32 s90, 1\ns_swappc_b64 s[8:9], s[80:81]\nv_cmp_ne_u32 vcc, s15, v27\n"
"s_nop 0\ns_cbranch_vccnz .S53\n.S54:\ns_mov_b32 s90, s91\ns_swappc_b64 s[8:9], s[80:81]\ns_branch .FF56\n.A68:\nCvav 32,128,192,4\nRa 176,26,8192\nMvav 32,132,200\nEX 82,58\n"
"Mvav 32,136,208\nRa 180,26,9216\nMvav 32,140,216\nWL 6\nEX 83,59\nCvav 36,140,220,8\nRa 184,26,10240\nAD 84,84,82\nMvav 36,136,212\nD1 1,16,46,3072\ns_add_u32 m0, s89, 32768\n"
"Mvav 36,132,204\nRa 188,26,11264\nMvav 36,128,196\nAD 84,84,83\nCV 75,82,83\ns_mov_b32 s94, s96\ns_add_u32 s95, s96, 65536\nv_and_b32 v108, 0xf0, v1\nMvav 36,144,228\n"
"Ra 128,2,16384\nMvav 36,148,236\ns_add_u32 s46, s86, s88\nEX 80,60\nMvav 36,152,244\nRa 132,2,17408\nMvav 36,156,252\nWL 6\nEX 81,61\nMvav 32,156,248\nRa 136,2,18432\nAD 85,85,80\n"
"Mvav 32,152,240\nD1 1,16,46,0\nMvav 32,148,232\nRa 140,2,19456\nSv 112,1,16,49,0\nMvav 32,144,224\nAD 85,85,81\nCV 78,80,81\n"
"buffer_load_dwordx4 v[100:103], v108, s[16:19], s65 offen offset:0 sc0 sc1\nbuffer_load_dwordx4 v[104:107], v108, s[16:19], s65 offen offset:256 sc0 sc1\nCvav 40,160,192,12\n"
"Ra 144,2,20480\nMvav 40,164,200\nEX 82,62\nMvav 40,168,208\nRa 148,2,21504\nMvav 40,172,216\nWL 6\nEX 83,63\nCvav 44,172,220,16\nRa 152,2,22528\nAD 85,85,82\nMvav 44,168,212\n"
"D1 1,16,46,1024\nMvav 44,164,204\nRa 156,2,23552\nMvav 44,160,196\nAD 85,85,83\nCV 79,82,83\nMvav 44,176,228\nRa 160,2,24576\nv_max_f32 v90, v84, v85\nMvav 44,180,236\n"
"s_add_u32 s88, s88, 32768\nEX 80,32\nMvav 44,184,244\nRa 164,2,25600\nv_cmp_lt_f32 vcc, s63, v90\nMvav 44,188,252\nWL 8\nEX 81,33\nMvav 40,188,248\nRa 168,2,26624\n"
"s_cbranch_vccnz .T70\n.T71:\nMvav 40,184,240\nD1 1,16,46,2048\nMvav 40,180,232\nRa 172,2,27648\nAD 86,86,84\nMvav 40,176,224\nAD 84,80,81\nCV 64,80,81\nMaav 0,128,72\n"
"Ra 176,2,28672\nMaav 4,128,76\nEX 82,34\nMaav 12,132,76\nRa 180,2,29696\nMaav 8,132,72\ns_waitcnt vmcnt(15) lgkmcnt(8)\nEX 83,35\nMaav 16,136,72\nRa 184,2,30720\nAD 84,84,82\n"
"Maav 20,136,76\nD1 1,16,46,3072\ns_add_u32 m0, s89, 49152\nMaav 28,140,76\nRa 188,2,31744\ns_barrier\nMaav 24,140,72\nAD 84,84,83\nCV 65,82,83\nMaav 32,144,72\nRa 128,24,32768\n"
"Maav 36,144,76\ns_add_u32 s46, s87, s88\nEX 80,36\nMaav 44,148,76\nRa 132,24,33792\nMaav 40,148,72\nWL 8\nEX 81,37\nMaav 48,152,72\nRa 136,24,34816\nAD 87,87,85\nMaav 52,152,76\n"
"D1 1,16,46,0\nMaav 60,156,76\nRa 140,24,35840\nMaav 56,156,72\n.W550:\ns_cbranch_execz .W554\nAD 85,80,81\nCV 68,80,81\nMaav 64,160,72\nRa 144,26,32768\nMaav 68,160,76\nEX 82,38\n"
"Maav 76,164,76\nRa 148,26,33792\nMaav 72,164,72\nWL 8\nEX 83,39\nMaav 80,168,72\nRa 152,26,34816\nAD 85,85,82\nMaav 84,168,76\nD1 1,16,46,1024\nMaav 92,172,76\nRa 156,26,35840\n"
"Maav 88,172,72\nAD 85,85,83\nCV 69,82,83\nMaav 96,176,72\nRa 160,24,40960\nMaav 100,176,76\ns_xor_b32 s89, s89, 0x10000\nEX 80,40\nMaav 108,180,76\nRa 164,24,41984\n"
"Maav 104,180,72\nWL 6\nEX 81,41\nMaav 112,184,72\nRa 168,24,43008\nAD 84,84,80\nMaav 116,184,76\nD1 1,16,46,2048\nMaav 124,188,76\nRa 172,24,44032\nMaav 120,188,72\nAD 84,84,81\n"
"CV 66,80,81\nCvav 48,128,192,4\nRa 176,26,40960\nMvav 48,132,200\ns_mov_b32 s86, s94\nEX 82,42\nMvav 48,136,208\nRa 180,26,41984\nMvav 48,140,216\nWL 6\nEX 83,43\n"
"Cvav 52,140,220,8\nRa 184,26,43008\nAD 84,84,82\nMvav 52,136,212\nD1 1,16,46,3072\ns_add_u32 m0, s89, 0\nMvav 52,132,204\nRa 188,26,44032\nMvav 52,128,196\nAD 84,84,83\n"
"CV 67,82,83\nMvav 52,144,228\nRa 128,2,49152\nMvav 52,148,236\ns_add_u32 s46, s86, s88\nEX 80,44\nMvav 52,152,244\nRa 132,2,50176\nv_xor_b32 v24, 0x10000, v24\nMvav 52,156,252\n"
"WL 6\nEX 81,45\nMvav 48,156,248\nRa 136,2,51200\nAD 85,85,80\nMvav 48,152,240\nD1 1,16,46,0\nMvav 48,148,232\nRa 140,2,52224\nSv 112,1,16,49,1024\nMvav 48,144,224\nAD 85,85,81\n"
"CV 70,80,81\nWV 9\nv_xor_b32 v109, s15, v102\nv_xor_b32 v110, s15, v106\nv_or_b32 v109, v109, v110\nv_cmp_ne_u32 vcc, 0, v109\ns_nop 3\ns_cmp_lg_u64 vcc, 0\n"
"s_cselect_b32 s67, 1, 0\ns_cselect_b32 s6, 0x40000000, s66\nCvav 56,160,192,12\nRa 144,2,53248\ns_add_u32 s5, s6, 0\nMvav 56,164,200\ns_add_u32 s49, s49, 131072\nEX 82,46\n"
"Mvav 56,168,208\nRa 148,2,54272\nv_xor_b32 v26, 0x10000, v26\nbuffer_load_dwordx4 a[192:195], v1, s[16:19], s5 offen offset:0 sc1\nMvav 56,172,216\nWL 6\nEX 83,47\n"
"Cvav 60,172,220,16\nRa 152,2,55296\nAD 85,85,82\nMvav 60,168,212\nD1 1,16,46,1024\nMvav 60,164,204\nRa 156,2,56320\nMvav 60,160,196\nAD 85,85,83\nCV 71,82,83\nMvav 60,176,228\n"
"Ra 160,2,57344\nv_max_f32 v90, v84, v85\nMvav 60,180,236\ns_add_u32 s88, s88, 32768\nEX 80,48\nMvav 60,184,244\nRa 164,2,58368\nv_cmp_lt_f32 vcc, s63, v90\nMvav 60,188,252\nWL 8\n"
"EX 81,49\nMvav 56,188,248\nRa 168,2,59392\ns_cbranch_vccnz .T72\n.T73:\nbuffer_load_dwordx4 a[196:199], v1, s[16:19], s5 offen offset:1024 sc1\nMvav 56,184,240\nD1 1,16,46,2048\n"
"Mvav 56,180,232\nRa 172,2,60416\nAD 86,86,84\nMvav 56,176,224\nAD 84,80,81\nCV 72,80,81\nMaav 0,128,64\nRa 176,2,61440\nMaav 4,128,68\nEX 82,50\nMaav 12,132,68\nRa 180,2,62464\n"
"Maav 8,132,64\nWL 8\nEX 83,51\nMaav 16,136,64\nRa 184,2,63488\nAD 84,84,82\nbuffer_load_dwordx4 a[200:203], v1, s[16:19], s5 offen offset:2048 sc1\nMaav 20,136,68\n"
"D1 1,16,46,3072\ns_add_u32 m0, s89, 16384\nMaav 28,140,68\nRa 188,2,64512\ns_barrier\nMaav 24,140,64\nAD 84,84,83\nCV 73,82,83\nMaav 32,144,64\nRa 128,24,0\nMaav 36,144,68\n"
"s_add_u32 s46, s87, s88\nEX 80,52\nMaav 44,148,68\nRa 132,24,1024\nv_xor_b32 v2, 0x10000, v2\nMaav 40,148,64\nWL 8\nEX 81,53\nMaav 48,152,64\nRa 136,24,2048\nAD 87,87,85\n"
"buffer_load_dwordx4 a[204:207], v1, s[16:19], s5 offen offset:3072 sc1\nMaav 52,152,68\nD1 1,16,46,0\nMaav 60,156,68\nRa 140,24,3072\nMaav 56,156,64\nAD 85,80,81\nCV 76,80,81\n"
"Maav 64,160,64\nRa 144,26,0\nMaav 68,160,68\nEX 82,54\nMaav 76,164,68\nRa 148,26,1024\ns_add_u32 s5, s6, 4096\nMaav 72,164,64\nWL 8\nEX 83,55\nMaav 80,168,64\nRa 152,26,2048\n"
"AD 85,85,82\nbuffer_load_dwordx4 a[208:211], v1, s[16:19], s5 offen offset:0 sc1\nMaav 84,168,68\nD1 1,16,46,1024\nMaav 92,172,68\nRa 156,26,3072\nMaav 88,172,64\nAD 85,85,83\n"
"CV 77,82,83\nMaav 96,176,64\nRa 160,24,8192\nMaav 100,176,68\nEX 80,56\nMaav 108,180,68\nRa 164,24,9216\nMaav 104,180,64\nWL 6\nEX 81,57\nMaav 112,184,64\nRa 168,24,10240\n"
"AD 84,84,80\nbuffer_load_dwordx4 a[212:215], v1, s[16:19], s5 offen offset:1024 sc1\nMaav 116,184,68\nD1 1,16,46,2048\nMaav 124,188,68\nRa 172,24,11264\nMaav 120,188,64\n"
"AD 84,84,81\nCV 74,80,81\nCvav 32,128,192,4\nRa 176,26,8192\nMvav 32,132,200\nEX 82,58\nMvav 32,136,208\nRa 180,26,9216\nMvav 32,140,216\nWL 6\nEX 83,59\nCvav 36,140,220,8\n"
"Ra 184,26,10240\nAD 84,84,82\nbuffer_load_dwordx4 a[216:219], v1, s[16:19], s5 offen offset:2048 sc1\nMvav 36,136,212\nD1 1,16,46,3072\ns_add_u32 m0, s89, 32768\nMvav 36,132,204\n"
"Ra 188,26,11264\nMvav 36,128,196\nAD 84,84,83\nCV 75,82,83\nMvav 36,144,228\nRa 128,2,16384\nMvav 36,148,236\ns_add_u32 s46, s86, s88\nEX 80,60\nMvav 36,152,244\nRa 132,2,17408\n"
"Mvav 36,156,252\nWL 6\nEX 81,61\nMvav 32,156,248\nRa 136,2,18432\nAD 85,85,80\nbuffer_load_dwordx4 a[220:223], v1, s[16:19], s5 offen offset:3072 sc1\nMvav 32,152,240\n"
"D1 1,16,46,0\nMvav 32,148,232\nRa 140,2,19456\nMvav 32,144,224\nAD 85,85,81\nCV 78,80,81\nCvav 40,160,192,12\nRa 144,2,20480\nMvav 40,164,200\ns_mov_b32 s87, s95\nEX 82,62\n"
"Mvav 40,168,208\nRa 148,2,21504\ns_add_u32 s5, s6, 16384\nMvav 40,172,216\nWL 6\nEX 83,63\nCvav 44,172,220,16\nRa 152,2,22528\nAD 85,85,82\n"
"buffer_load_dwordx4 a[224:227], v1, s[16:19], s5 offen offset:0 sc1\nMvav 44,168,212\nD1 1,16,46,1024\nMvav 44,164,204\nRa 156,2,23552\nMvav 44,160,196\nAD 85,85,83\nCV 79,82,83\n"
"Mvav 44,176,228\nRa 160,2,24576\nv_max_f32 v90, v84, v85\nMvav 44,180,236\ns_add_u32 s88, s88, 32768\nEX 80,32\nMvav 44,184,244\nRa 164,2,25600\nv_cmp_lt_f32 vcc, s63, v90\n"
"Mvav 44,188,252\nWL 8\nEX 81,33\nMvav 40,188,248\nRa 168,2,26624\ns_cbranch_vccnz .T75\n.T76:\nbuffer_load_dwordx4 a[228:231], v1, s[16:19], s5 offen offset:1024 sc1\n"
"Mvav 40,184,240\nD1 1,16,46,2048\nMvav 40,180,232\nRa 172,2,27648\nAD 86,86,84\nMvav 40,176,224\nAD 84,80,81\nCV 64,80,81\nMaav 0,128,72\nRa 176,2,28672\nMaav 4,128,76\nEX 82,34\n"
"Maav 12,132,76\nRa 180,2,29696\nMaav 8,132,72\ns_waitcnt vmcnt(22) lgkmcnt(8)\nEX 83,35\nMaav 16,136,72\nRa 184,2,30720\nAD 84,84,82\n"
"buffer_load_dwordx4 a[232:235], v1, s[16:19], s5 offen offset:2048 sc1\nMaav 20,136,76\nD1 1,16,46,3072\ns_add_u32 m0, s89, 49152\nMaav 28,140,76\nRa 188,2,31744\ns_barrier\n"
"Maav 24,140,72\nAD 84,84,83\nCV 65,82,83\nMaav 32,144,72\nRa 128,24,32768\nMaav 36,144,76\ns_add_u32 s46, s87, s88\nEX 80,36\nMaav 44,148,76\nRa 132,24,33792\nMaav 40,148,72\n"
"WL 8\nEX 81,37\nMaav 48,152,72\nRa 136,24,34816\nAD 87,87,85\nbuffer_load_dwordx4 a[236:239], v1, s[16:19], s5 offen offset:3072 sc1\nMaav 52,152,76\nD1 1,16,46,0\nMaav 60,156,76\n"
"Ra 140,24,35840\nMaav 56,156,72\nAD 85,80,81\nCV 68,80,81\nMaav 64,160,72\nRa 144,26,32768\nMaav 68,160,76\nEX 82,38\nMaav 76,164,76\nRa 148,26,33792\ns_add_u32 s5, s6, 20480\n"
"Maav 72,164,72\nWL 8\nEX 83,39\nMaav 80,168,72\nRa 152,26,34816\nAD 85,85,82\nbuffer_load_dwordx4 a[240:243], v1, s[16:19], s5 offen offset:0 sc1\nMaav 84,168,76\nD1 1,16,46,1024\n"
"Maav 92,172,76\nRa 156,26,35840\nMaav 88,172,72\nAD 85,85,83\nCV 69,82,83\nMaav 96,176,72\nRa 160,24,40960\nMaav 100,176,76\ns_xor_b32 s89, s89, 0x10000\nEX 80,40\n"
"Maav 108,180,76\nRa 164,24,41984\nMaav 104,180,72\nWL 6\nEX 81,41\nMaav 112,184,72\nRa 168,24,43008\nAD 84,84,80\n"
"buffer_load_dwordx4 a[244:247], v1, s[16:19], s5 offen offset:1024 sc1\nMaav 116,184,76\nD1 1,16,46,2048\nMaav 124,188,76\nRa 172,24,44032\nMaav 120,188,72\nAD 84,84,81\n"
"CV 66,80,81\nCvav 48,128,192,4\nRa 176,26,40960\nMvav 48,132,200\nEX 82,42\nMvav 48,136,208\nRa 180,26,41984\nMvav 48,140,216\nWL 6\nEX 83,43\nCvav 52,140,220,8\nRa 184,26,43008\n"
"AD 84,84,82\nbuffer_load_dwordx4 a[248:251], v1, s[16:19], s5 offen offset:2048 sc1\nMvav 52,136,212\nD1 1,16,46,3072\ns_add_u32 m0, s89, 0\nMvav 52,132,204\nRa 188,26,44032\n"
"Mvav 52,128,196\nAD 84,84,83\nCV 67,82,83\nMvav 52,144,228\nRa 128,2,49152\nMvav 52,148,236\ns_add_u32 s46, s86, s88\nEX 80,44\nMvav 52,152,244\nRa 132,2,50176\n"
"v_xor_b32 v24, 0x10000, v24\nMvav 52,156,252\nWL 6\nEX 81,45\nMvav 48,156,248\nRa 136,2,51200\nAD 85,85,80\nbuffer_load_dwordx4 a[252:255], v1, s[16:19], s5 offen offset:3072 sc1\n"
"Mvav 48,152,240\nD1 1,16,46,0\nMvav 48,148,232\nRa 140,2,52224\nMvav 48,144,224\nAD 85,85,81\nCV 70,80,81\nCvav 56,160,192,12\nRa 144,2,53248\nMvav 56,164,200\nEX 82,46\n"
"Mvav 56,168,208\nRa 148,2,54272\nv_xor_b32 v26, 0x10000, v26\nMvav 56,172,216\nWL 6\nEX 83,47\nCvav 60,172,220,16\nRa 152,2,55296\nAD 85,85,82\nMvav 60,168,212\nD1 1,16,46,1024\n"
"Mvav 60,164,204\nRa 156,2,56320\nMvav 60,160,196\nAD 85,85,83\nCV 71,82,83\nMvav 60,176,228\nRa 160,2,57344\nv_max_f32 v90, v84, v85\nMvav 60,180,236\ns_add_u32 s88, s88, 32768\n"
"EX 80,48\nMvav 60,184,244\nRa 164,2,58368\nv_cmp_lt_f32 vcc, s63, v90\nMvav 60,188,252\nWL 8\nEX 81,49\nMvav 56,188,248\nRa 168,2,59392\ns_cbranch_vccnz .T77\n.T78:\n"
"Mvav 56,184,240\nD1 1,16,46,2048\nMvav 56,180,232\nRa 172,2,60416\nAD 86,86,84\nMvav 56,176,224\nAD 84,80,81\nCV 72,80,81\nMaav 0,128,64\nRa 176,2,61440\n"
"buffer_load_dwordx4 v[100:103], v108, s[16:19], s65 offen offset:0 sc0 sc1\nbuffer_load_dwordx4 v[104:107], v108, s[16:19], s65 offen offset:256 sc0 sc1\nMaav 4,128,68\nEX 82,50\n"
"Maav 12,132,68\nRa 180,2,62464\nMaav 8,132,64\ns_waitcnt vmcnt(22) lgkmcnt(8)\nEX 83,51\nMaav 16,136,64\nRa 184,2,63488\nAD 84,84,82\nMaav 20,136,68\nD1 1,16,46,3072\n"
"s_add_u32 m0, s89, 16384\nMaav 28,140,68\nRa 188,2,64512\ns_barrier\nMaav 24,140,64\nAD 84,84,83\nCV 73,82,83\nMaav 32,144,64\nRa 128,24,0\nMaav 36,144,68\n"
"s_add_u32 s46, s87, s88\nEX 80,52\nMaav 44,148,68\nRa 132,24,1024\nv_xor_b32 v2, 0x10000, v2\nMaav 40,148,64\nWL 8\nEX 81,53\nMaav 48,152,64\nRa 136,24,2048\nAD 87,87,85\n"
"Maav 52,152,68\nD1 1,16,46,0\nMaav 60,156,68\nRa 140,24,3072\nMaav 56,156,64\nAD 85,80,81\nCV 76,80,81\nMaav 64,160,64\nRa 144,26,0\nMaav 68,160,68\nEX 82,54\nMaav 76,164,68\n"
"Ra 148,26,1024\nMaav 72,164,64\nWL 8\nEX 83,55\nMaav 80,168,64\nRa 152,26,2048\nAD 85,85,82\nMaav 84,168,68\nD1 1,16,46,1024\nMaav 92,172,68\nRa 156,26,3072\nMaav 88,172,64\n"
"AD 85,85,83\nCV 77,82,83\nMaav 96,176,64\nRa 160,24,8192\nMaav 100,176,68\nEX 80,56\nMaav 108,180,68\nRa 164,24,9216\nMaav 104,180,64\nWL 10\nEX 81,57\nMaav 112,184,64\n"
"Ra 168,24,10240\nAD 84,84,80\nMaav 116,184,68\nD1 1,16,46,2048\nMaav 124,188,68\nRa 172,24,11264\nMaav 120,188,64\nAD 84,84,81\nCV 74,80,81\nWV 4\ns_cmp_lg_u32 s67, 0\n"
"s_cbranch_scc0 .E86\nv_xor_b32 v109, s15, v102\nv_xor_b32 v110, s15, v106\nv_or_b32 v109, v109, v110\nv_cmp_ne_u32 vcc, 0, v109\ns_nop 0\ns_cbranch_vccnz .S83\n.S84:\n"
"s_add_u32 s5, s66, 0\nbuffer_load_dwordx4 a[192:195], v1, s[16:19], s5 offen offset:0 sc1\nbuffer_load_dwordx4 a[196:199], v1, s[16:19], s5 offen offset:1024 sc1\n"
"buffer_load_dwordx4 a[200:203], v1, s[16:19], s5 offen offset:2048 sc1\nbuffer_load_dwordx4 a[204:207], v1, s[16:19], s5 offen offset:3072 sc1\ns_add_u32 s5, s66, 4096\n"
"buffer_load_dwordx4 a[208:211], v1, s[16:19], s5 offen offset:0 sc1\nbuffer_load_dwordx4 a[212:215], v1, s[16:19], s5 offen offset:1024 sc1\n"
"buffer_load_dwordx4 a[216:219], v1, s[16:19], s5 offen offset:2048 sc1\nbuffer_load_dwordx4 a[220:223], v1, s[16:19], s5 offen offset:3072 sc1\ns_add_u32 s5, s66, 16384\n"
"buffer_load_dwordx4 a[224:227], v1, s[16:19], s5 offen offset:0 sc1\nbuffer_load_dwordx4 a[228:231], v1, s[16:19], s5 offen offset:1024 sc1\n"
"buffer_load_dwordx4 a[232:235], v1, s[16:19], s5 offen offset:2048 sc1\nbuffer_load_dwordx4 a[236:239], v1, s[16:19], s5 offen offset:3072 sc1\ns_add_u32 s5, s66, 20480\n"
"buffer_load_dwordx4 a[240:243], v1, s[16:19], s5 offen offset:0 sc1\nbuffer_load_dwordx4 a[244:247], v1, s[16:19], s5 offen offset:1024 sc1\n"
"buffer_load_dwordx4 a[248:251], v1, s[16:19], s5 offen offset:2048 sc1\nbuffer_load_dwordx4 a[252:255], v1, s[16:19], s5 offen offset:3072 sc1\nWV 0\ns_mov_b32 s67, 0\n.E86:\n"
"v_cmp_lt_f32 vcc, 0, v101\nSU 20,100,88\nv_mul_f32 v101, 0x3e800000, v101\n.W551:\ns_cbranch_execz .WB21\nv_cndmask_b32 v20, 0, v20, vcc\nv_cmp_lt_f32 vcc, 0, v105\nSU 21,104,89\n"
"v_mul_f32 v105, 0x3e800000, v105\nv_cndmask_b32 v21, 0, v21, vcc\nv_or_b32 v91, v20, v21\nv_cmp_ne_u32 vcc, 0, v91\ns_nop 0\ns_cbranch_vccnz .D81\nAD 86,86,101\nAD 87,87,105\n"
".D82:\nMava 0,130,192\nMava 8,128,192\nEX 82,58\nMava 12,128,196\nMava 4,130,196\nEX 83,59\nMava 16,130,200\nAD 84,84,82\nMava 24,128,200\nD1 1,16,46,3072\nMava 28,128,204\n"
"s_add_u32 m0, s89, 32768\ns_add_u32 s46, s86, s88\nMava 20,130,204\nAD 84,84,83\nCV 75,82,83\nMava 32,130,208\nRa 128,2,16384\nv_xor_b32 v3, 0x10000, v2\n"
"v_add_u32 v0, 0x20000, v1\nMava 40,128,208\nEX 80,60\nMava 44,128,212\nRa 132,2,17408\ns_sub_u32 s94, s49, 1835008\nMava 36,130,212\nEX 81,61\nMava 48,130,216\nRa 136,2,18432\n"
"AD 85,85,80\ns_xor_b32 s92, s94, 0x202000\nMava 56,128,216\nD1 1,16,46,0\ns_lshl_b32 s89, s11, 12\nMava 60,128,220\nRa 140,2,19456\ns_lshl1_add_u32 s2, s12, 0\nMava 52,130,220\n"
"AD 85,85,81\nCV 78,80,81\nMava 64,130,224\nRa 144,2,20480\ns_lshl3_add_u32 s2, s2, s13\ns_lshl4_add_u32 s2, s2, 0xc048200\nMava 72,128,224\nEX 82,62\nMava 76,128,228\n"
"Ra 148,2,21504\ns_and_b32 s3, s72, 0x2000\nMava 68,130,228\nEX 83,63\nMava 80,130,232\nRa 152,2,22528\nAD 85,85,82\nMava 88,128,232\nD1 1,16,46,1024\ns_cmp_eq_u32 s11, 1\n"
"s_cselect_b32 s54, s2, s54\ns_cselect_b32 s3, s3, 0\nMava 92,128,236\nRa 156,2,23552\nv_xor_b32 v25, s3, v25\nMava 84,130,236\nAD 85,85,83\nCV 79,82,83\nMava 96,130,240\n"
"Ra 160,2,24576\nv_max_f32 v90, v84, v85\nMava 104,128,240\nv_cmp_lt_f32 vcc, s63, v90\nMava 108,128,244\nRa 164,2,25600\nMava 100,130,244\nWL 8\nMava 112,130,248\nRa 168,2,26624\n"
"Mava 120,128,248\nD1 1,16,46,2048\nMava 124,128,252\nRa 172,2,27648\nMava 116,130,252\ns_cbranch_vccnz .T87\n.T88:\nAD 86,86,84\nAD 87,87,85\nMaav 0,128,72\nRa 176,2,28672\n"
"v_mov_b32 v84, v86\nv_mov_b32 v85, v87\nMaav 4,128,76\nv_permlane32_swap_b32 v86, v84\nv_permlane32_swap_b32 v87, v85\nMaav 12,132,76\nRa 180,2,29696\nAD 86,86,84\nMaav 8,132,72\n"
"WL 8\nAD 87,87,85\nMaav 16,136,72\nRa 184,2,30720\nv_mov_b32 v84, v86\nv_mov_b32 v85, v87\nMaav 20,136,76\nD1 1,16,46,3072\nv_permlane16_swap_b32 v86, v84\nMaav 28,140,76\n"
"Ra 188,2,31744\nv_permlane16_swap_b32 v87, v85\nMaav 24,140,72\nAD 86,86,84\nAD 87,87,85\nMaav 32,144,72\nRa 128,2,0\nv_rcp_f32 v86, v86\nMaav 36,144,76\nv_rcp_f32 v87, v87\n"
"Maav 44,148,76\nRa 132,2,1024\nAR 82,0\nAR 83,1\nMU 82,82,86\nMU 83,83,86\nMaav 40,148,72\nWL 8\ns_add_u32 m0, s89, 131072\ns_add_u32 s46, s96, 1212416\nCV 96,82,83\nAR 82,2\n"
"Maav 48,152,72\nRa 136,2,2048\nAR 83,3\nMU 82,82,86\nMU 83,83,86\nCV 97,82,83\nMaav 52,152,76\nD1 1,16,46,0\nAR 82,4\nAR 83,5\nMU 82,82,87\nMaav 60,156,76\nRa 140,2,3072\n"
"MU 83,83,87\nCV 100,82,83\nAR 82,6\nAR 83,7\nMaav 56,156,72\nMU 82,82,87\nMU 83,83,87\nCV 101,82,83\nAR 82,8\nAR 83,9\nMaav 64,160,72\nRa 144,2,4096\nMU 82,82,86\nMU 83,83,86\n"
"CV 98,82,83\nAR 82,10\nMaav 68,160,76\nAR 83,11\nMU 82,82,86\nMU 83,83,86\nCV 99,82,83\nMaav 76,164,76\nRa 148,2,5120\nAR 82,12\nAR 83,13\nMU 82,82,87\nMU 83,83,87\n"
"Maav 72,164,72\nWL 8\nCV 102,82,83\nAR 82,14\nAR 83,15\nMU 82,82,87\nMaav 80,168,72\nRa 152,2,6144\nMU 83,83,87\nCV 103,82,83\nAR 82,16\nAR 83,17\nMaav 84,168,76\nD1 1,16,46,1024\n"
"MU 82,82,86\nMU 83,83,86\nCV 104,82,83\nMaav 92,172,76\nRa 156,2,7168\nAR 82,18\nAR 83,19\nMU 82,82,86\nMU 83,83,86\nMaav 88,172,72\nCV 105,82,83\nAR 82,20\nAR 83,21\nMU 82,82,87\n"
"MU 83,83,87\nMaav 96,176,72\nRa 160,2,8192\nCV 108,82,83\nAR 82,22\nAR 83,23\nMU 82,82,87\nMaav 100,176,76\nMU 83,83,87\nCV 109,82,83\nAR 82,24\nAR 83,25\nMaav 108,180,76\n"
"Ra 164,2,9216\nMU 82,82,86\nMU 83,83,86\nCV 106,82,83\nAR 82,26\nMaav 104,180,72\nWL 8\nAR 83,27\nMU 82,82,86\nMU 83,83,86\nCV 107,82,83\nMaav 112,184,72\nRa 168,2,10240\n"
"AR 82,28\nAR 83,29\nMU 82,82,87\nMU 83,83,87\nMaav 116,184,76\nD1 1,16,46,2048\nCV 110,82,83\nAR 82,30\nAR 83,31\nMaav 124,188,76\nRa 172,2,11264\nMU 82,82,87\nMU 83,83,87\n"
"CV 111,82,83\nMaav 120,188,72\nAR 82,32\nAR 83,33\nMU 82,82,86\nMU 83,83,86\nZvav 128,128,96\nRa 176,2,12288\nCV 112,82,83\nAR 82,34\nAR 83,35\nMU 82,82,86\nMvav 128,132,104\n"
"MU 83,83,86\nCV 113,82,83\nAR 82,36\nAR 83,37\nZvav 132,132,108\nRa 180,2,13312\nMU 82,82,87\nMU 83,83,87\nCV 116,82,83\nMvav 132,128,100\nWL 8\nAR 82,38\nAR 83,39\nMU 82,82,87\n"
"Zvav 140,136,100\nRa 184,2,14336\nMU 83,83,87\nCV 117,82,83\nAR 82,40\nMvav 140,140,108\nD1 1,16,46,3072\nAR 83,41\nMU 82,82,86\nMU 83,83,86\nZvav 136,140,104\nRa 188,2,15360\n"
"s_barrier\nCV 114,82,83\nAR 82,42\nAR 83,43\nMvav 136,136,96\nMU 82,82,86\nMU 83,83,86\nCV 115,82,83\nAR 82,44\nZvav 144,144,96\nRa 128,2,32768\nAR 83,45\nMU 82,82,87\n"
"MU 83,83,87\nMvav 144,148,104\nCV 118,82,83\nAR 82,46\nAR 83,47\nMU 82,82,87\nbuffer_store_dword v25, off, s[16:19], s54 offset:512 sc0 sc1\nZvav 148,148,108\nRa 132,2,33792\n"
"MU 83,83,87\nCV 119,82,83\nAR 82,48\nMvav 148,144,100\nWL 8\ns_add_u32 m0, s89, 114688\ns_add_u32 s46, s96, 1179648\nAR 83,49\nMU 82,82,86\nZvav 156,152,100\nRa 136,2,34816\n"
"MU 83,83,86\nCV 120,82,83\nAR 82,50\nMvav 156,156,108\nD1 1,16,46,0\nAR 83,51\nMU 82,82,86\nMU 83,83,86\nZvav 152,156,104\nRa 140,2,35840\nCV 121,82,83\nAR 82,52\nAR 83,53\n"
"Mvav 152,152,96\nMU 82,82,87\nMU 83,83,87\nCV 124,82,83\nAR 82,54\nZvav 64,160,96\nRa 144,2,36864\nAR 83,55\nMU 82,82,87\nMU 83,83,87\nCV 125,82,83\nMvav 64,164,104\nAR 82,56\n"
"AR 83,57\nMU 82,82,86\nMU 83,83,86\nZvav 68,164,108\nRa 148,2,37888\nCV 122,82,83\nAR 82,58\nAR 83,59\nMvav 68,160,100\nWL 8\nMU 82,82,86\nMU 83,83,86\nCV 123,82,83\n"
"Zvav 76,168,100\nRa 152,2,38912\nAR 82,60\nAR 83,61\nMU 82,82,87\nMvav 76,172,108\nD1 1,16,46,1024\nMU 83,83,87\nCV 126,82,83\nAR 82,62\nZvav 72,172,104\nRa 156,2,39936\nAR 83,63\n"
"MU 82,82,87\nMU 83,83,87\nCV 127,82,83\nMvav 72,168,96\nAR 82,64\nAR 83,65\nMU 82,82,86\nMU 83,83,86\nMvav 128,176,112\nRa 160,2,40960\nCV 32,82,83\nAR 82,66\nAR 83,67\n"
"Mvav 128,180,120\nMU 82,82,86\nMU 83,83,86\nCV 33,82,83\nAR 82,68\nMvav 132,180,124\nRa 164,2,41984\nAR 83,69\nMU 82,82,87\nMU 83,83,87\nMvav 132,176,116\nWL 8\nCV 36,82,83\n"
"AR 82,70\nAR 83,71\nMU 82,82,87\nMvav 140,184,116\nRa 168,2,43008\nMU 83,83,87\nCV 37,82,83\nAR 82,72\nMvav 140,188,124\nD1 1,16,46,2048\nAR 83,73\nMU 82,82,86\nMU 83,83,86\n"
"Mvav 136,188,120\nRa 172,2,44032\nCV 34,82,83\nAR 82,74\nAR 83,75\nMvav 136,184,112\nMU 82,82,86\nMU 83,83,86\nCV 35,82,83\nAR 82,76\nMvav 144,128,112\nRa 176,2,45056\nAR 83,77\n"
"MU 82,82,87\nMU 83,83,87\nCV 38,82,83\nMvav 144,132,120\nAR 82,78\nAR 83,79\nMU 82,82,87\nMU 83,83,87\nMvav 148,132,124\nRa 180,2,46080\nCV 39,82,83\nAR 82,80\nAR 83,81\n"
"Mvav 148,128,116\nWL 8\nMU 82,82,86\nMU 83,83,86\nCV 40,82,83\nMvav 156,136,116\nRa 184,2,47104\nAR 82,82\nAR 83,83\nMU 82,82,86\nMvav 156,140,124\nD1 1,16,46,3072\nMU 83,83,86\n"
"CV 41,82,83\nAR 82,84\nMvav 152,140,120\nRa 188,2,48128\nAR 83,85\nMU 82,82,87\nMU 83,83,87\nCV 44,82,83\nMvav 152,136,112\nAR 82,86\nAR 83,87\nMU 82,82,87\nMU 83,83,87\n"
"Mvav 64,144,112\nRa 128,2,49152\nCV 45,82,83\nAR 82,88\nAR 83,89\nMvav 64,148,120\nMU 82,82,86\nMU 83,83,86\nCV 42,82,83\nAR 82,90\nMvav 68,148,124\nRa 132,2,50176\nAR 83,91\n"
"MU 82,82,86\nMU 83,83,86\nMvav 68,144,116\nWL 8\ns_add_u32 m0, s89, 147456\ns_add_u32 s46, s96, 1245184\nCV 43,82,83\nAR 82,92\nMvav 76,152,116\nRa 136,2,51200\nAR 83,93\n"
"MU 82,82,87\nMU 83,83,87\nMvav 76,156,124\nD1 1,16,46,0\nCV 46,82,83\nAR 82,94\nAR 83,95\nMvav 72,156,120\nRa 140,2,52224\nMU 82,82,87\nMU 83,83,87\nCV 47,82,83\nMvav 72,152,112\n"
"AR 82,96\nAR 83,97\nMU 82,82,86\nMU 83,83,86\nMvav 128,160,32\nRa 144,2,53248\nCV 48,82,83\nAR 82,98\nAR 83,99\nMU 82,82,86\nMvav 128,164,40\nMU 83,83,86\nCV 49,82,83\nAR 82,100\n"
"AR 83,101\nMvav 132,164,44\nRa 148,2,54272\nMU 82,82,87\nMU 83,83,87\nCV 52,82,83\nMvav 132,160,36\nWL 8\nAR 82,102\nAR 83,103\nMU 82,82,87\nMvav 140,168,36\nRa 152,2,55296\n"
"MU 83,83,87\nCV 53,82,83\nAR 82,104\nMvav 140,172,44\nD1 1,16,46,1024\nAR 83,105\nMU 82,82,86\nMU 83,83,86\nMvav 136,172,40\nRa 156,2,56320\nCV 50,82,83\nAR 82,106\nAR 83,107\n"
"Mvav 136,168,32\nMU 82,82,86\nMU 83,83,86\nCV 51,82,83\nAR 82,108\nMvav 144,176,32\nRa 160,2,57344\nAR 83,109\nMU 82,82,87\nMU 83,83,87\nCV 54,82,83\nMvav 144,180,40\nAR 82,110\n"
"AR 83,111\nMU 82,82,87\nMU 83,83,87\nMvav 148,180,44\nRa 164,2,58368\nCV 55,82,83\nAR 82,112\nAR 83,113\nMvav 148,176,36\nWL 8\nMU 82,82,86\nMU 83,83,86\nCV 56,82,83\n"
"Mvav 156,184,36\nRa 168,2,59392\nAR 82,114\nAR 83,115\nMU 82,82,86\nMvav 156,188,44\nD1 1,16,46,2048\nMU 83,83,86\nCV 57,82,83\nAR 82,116\nMvav 152,188,40\nRa 172,2,60416\n"
"AR 83,117\nMU 82,82,87\nMU 83,83,87\nMvav 152,184,32\nCV 60,82,83\nAR 82,118\nAR 83,119\nMU 82,82,87\nMvav 64,128,32\nRa 176,2,61440\nMU 83,83,87\nCV 61,82,83\nAR 82,120\n"
"AR 83,121\nMvav 64,132,40\nMU 82,82,86\nMU 83,83,86\nCV 58,82,83\nAR 82,122\nMvav 68,132,44\nRa 180,2,62464\nAR 83,123\nMU 82,82,86\nMU 83,83,86\nMvav 68,128,36\n"
"s_waitcnt vmcnt(16) lgkmcnt(8)\nCV 59,82,83\nAR 82,124\nAR 83,125\nMvav 76,136,36\nRa 184,2,63488\n.W552:\ns_cbranch_execz .WB21\nMU 82,82,87\nMU 83,83,87\nCV 62,82,83\n"
"Mvav 76,140,44\nD1 1,16,46,3072\nAR 82,126\nAR 83,127\nMU 82,82,87\nMvav 72,140,40\nRa 188,2,64512\ns_barrier\nMU 83,83,87\nCV 63,82,83\nMvav 72,136,32\nMvav 128,144,48\n"
"Ra 128,3,0\nMvav 128,148,56\nMvav 132,148,60\nRa 132,3,1024\nMvav 132,144,52\nWL 8\nMvav 140,152,52\nRa 136,3,2048\nMvav 140,156,60\nMvav 136,156,56\nRa 140,3,3072\n"
"Mvav 136,152,48\nMvav 144,160,48\nRa 144,3,4096\nMvav 144,164,56\nMvav 148,164,60\nRa 148,3,5120\nCV 132,132,133\nMvav 148,160,52\nWL 8\nCV 133,134,135\nMvav 156,168,52\n"
"Ra 152,3,6144\nCV 134,140,141\nMvav 156,172,60\nCV 135,142,143\ns_add_u32 s5, s94, 0\nMvav 152,172,56\nRa 156,3,7168\nSv 132,1,16,5,1024\nMvav 152,168,48\nCV 128,128,129\n"
"CV 129,130,131\nMvav 64,176,48\nRa 160,3,8192\nCV 130,136,137\nMvav 64,180,56\nbuffer_load_dword v27, off, s[16:19], s60 offset:512 sc0 sc1\nCV 131,138,139\nMvav 68,180,60\n"
"Ra 164,3,9216\nSv 128,1,16,5,0\nMvav 68,176,52\nWL 6\nCV 148,148,149\nMvav 76,184,52\nRa 168,3,10240\nCV 149,150,151\nMvav 76,188,60\nCV 150,156,157\nCV 151,158,159\n"
"Mvav 72,188,56\nRa 172,3,11264\ns_add_u32 s5, s94, 131072\nMvav 72,184,48\nSv 148,1,16,5,1024\nCV 144,144,145\nZvav 160,128,96\nRa 176,3,12288\nCV 145,146,147\nMvav 160,132,104\n"
"CV 146,152,153\nCV 147,154,155\nMvav 160,136,112\nRa 180,3,13312\nSv 144,1,16,5,0\nMvav 160,140,120\nWL 6\nCV 68,68,69\nZvav 164,140,124\nRa 184,3,14336\nCV 69,70,71\n"
"Mvav 164,136,116\nCV 70,76,77\nCV 71,78,79\nMvav 164,132,108\nRa 188,3,15360\ns_add_u32 s5, s94, 262144\nMvav 164,128,100\nSv 68,1,16,5,1024\nCV 64,64,65\nMvav 164,144,36\n"
"Ra 128,3,16384\nCV 65,66,67\nMvav 164,148,44\nCV 66,72,73\nCV 67,74,75\nMvav 164,152,52\nRa 132,3,17408\nSv 64,1,16,5,0\nMvav 164,156,60\nWL 6\nMvav 160,156,56\nRa 136,3,18432\n"
"Mvav 160,152,48\nMvav 160,148,40\nRa 140,3,19456\nMvav 160,144,32\nZvav 168,160,96\nRa 144,3,20480\nMvav 168,164,104\nMvav 168,168,112\nRa 148,3,21504\nMvav 168,172,120\nWL 6\n"
"Zvav 172,172,124\nRa 152,3,22528\nMvav 172,168,116\nMvav 172,164,108\nRa 156,3,23552\nMvav 172,160,100\nMvav 172,176,36\nRa 160,3,24576\nMvav 172,180,44\nMvav 172,184,52\n"
"Ra 164,3,25600\nMvav 172,188,60\nWL 6\nMvav 168,188,56\nRa 168,3,26624\nMvav 168,184,48\nMvav 168,180,40\nRa 172,3,27648\nMvav 168,176,32\nZvav 176,128,96\nRa 176,3,28672\n"
"CV 164,164,165\nMvav 176,132,104\nCV 165,166,167\nCV 166,172,173\nMvav 176,136,112\nRa 180,3,29696\nCV 167,174,175\nMvav 176,140,120\ns_waitcnt vmcnt(11) lgkmcnt(6)\n"
"s_add_u32 s5, s94, 393216\nZvav 180,140,124\nRa 184,3,30720\nSv 164,1,16,5,1024\nMvav 180,136,116\nCV 160,160,161\nCV 161,162,163\nMvav 180,132,108\nRa 188,3,31744\ns_barrier\n"
"Mvav 180,128,100\nCV 162,168,169\nCV 163,170,171\nMvav 180,144,36\nRa 128,3,32768\nSv 160,1,16,5,0\nMvav 180,148,44\nMvav 180,152,52\nRa 132,3,33792\nMvav 180,156,60\nWL 6\n"
"s_add_u32 m0, s89, 16384\ns_add_u32 s46, s96, 1277952\nMvav 176,156,56\nRa 136,3,34816\nMvav 176,152,48\nD1 1,16,46,0\nMvav 176,148,40\nRa 140,3,35840\nMvav 176,144,32\n"
"Zvav 184,160,96\nRa 144,3,36864\nMvav 184,164,104\nMvav 184,168,112\nRa 148,3,37888\nMvav 184,172,120\nWL 6\nZvav 188,172,124\nRa 152,3,38912\nMvav 188,168,116\nD1 1,16,46,1024\n"
"Mvav 188,164,108\nRa 156,3,39936\nMvav 188,160,100\nMvav 188,176,36\nRa 160,3,40960\nWV 9\nv_cmp_ne_u32 vcc, s15, v27\ns_nop 0\ns_cbranch_vccnz .S114\n.S115:\nMvav 188,180,44\n"
"s_add_u32 s4, s92, 0\nMvav 188,184,52\nRa 164,3,41984\nMvav 188,188,60\nWL 6\nbuffer_load_dwordx4 v[208:211], v1, s[16:19], s4 offen offset:0 sc0 sc1\nMvav 184,188,56\n"
"Ra 168,3,43008\nMvav 184,184,48\nD1 1,16,46,2048\nMvav 184,180,40\nRa 172,3,44032\nMvav 184,176,32\nbuffer_load_dwordx4 v[212:215], v1, s[16:19], s4 offen offset:1024 sc0 sc1\n"
"Zvav 192,128,96\nRa 176,3,45056\nv_lshrrev_b32 v88, 8, v1\nMvav 192,132,104\nv_lshlrev_b32 v89, 5, v88\nCV 180,180,181\nMvav 192,136,112\nRa 180,3,46080\nCV 181,182,183\n"
"Mvav 192,140,120\nWL 6\nCV 182,188,189\nZvav 196,140,124\nRa 184,3,47104\nv_bfe_u32 v90, v1, 4, 4\nMvav 196,136,116\nD1 1,16,46,3072\nCV 183,190,191\nMvav 196,132,108\n"
"Ra 188,3,48128\ns_add_u32 s5, s94, 524288\nMvav 196,128,100\nv_mul_u32_u24 v91, 896, v90\nSv 180,1,16,5,1024\nMvav 196,144,36\nRa 128,3,49152\nv_lshl_add_u32 v91, v88, 4, v91\n"
"Mvav 196,148,44\nv_add_u32 v90, s21, v90\nCV 176,176,177\nMvav 196,152,52\nRa 132,3,50176\nCV 177,178,179\nMvav 196,156,60\nWL 6\ns_add_u32 m0, s89, 0\n"
"s_add_u32 s46, s96, 1310720\nMvav 192,156,56\nRa 136,3,51200\nv_lshlrev_b32 v88, 3, v90\nMvav 192,152,48\nD1 1,16,46,0\nCV 178,184,185\nMvav 192,148,40\nRa 140,3,52224\n"
"CV 179,186,187\nMvav 192,144,32\nv_add_u32 v94, 64, v90\nSv 176,1,16,5,0\nZvav 200,160,96\nRa 144,3,53248\ns_mov_b32 s63, 0xc040000\nMvav 200,164,104\nv_lshrrev_b32 v92, 8, v1\n"
"Mvav 200,168,112\nRa 148,3,54272\nMvav 200,172,120\nWL 6\nZvav 204,172,124\nRa 152,3,55296\nv_bfe_u32 v93, v1, 6, 2\nMvav 204,168,116\nD1 1,16,46,1024\nv_cmp_eq_u32 vcc, v92, v93\n"
"v_and_b32 v92, 16, v1\nv_mov_b32 v93, 0x3f80\nv_cndmask_b32 v93, 0, v93, vcc\nMvav 204,164,108\nRa 156,3,56320\nMvav 204,160,100\nv_lshlrev_b32 v93, v92, v93\nMvav 204,176,36\n"
"Ra 160,3,57344\nv_and_b32 v92, 32, v1\nMvav 204,180,44\ns_add_u32 s4, s92, 131072\nv_cmp_eq_u32 vcc, 0, v92\nv_mov_b32 v4, 0\nv_mov_b32 v5, 0\nv_cndmask_b32 v6, 0, v93, vcc\n"
"Mvav 204,184,52\nRa 164,3,58368\nMvav 204,188,60\nWL 6\nbuffer_load_dwordx4 v[216:219], v1, s[16:19], s4 offen offset:0 sc0 sc1\nMvav 200,188,56\nRa 168,3,59392\n"
"v_sub_u32 v7, v93, v6\nMvav 200,184,48\nD1 1,16,46,2048\nMvav 200,180,40\nRa 172,3,60416\nMvav 200,176,32\n"
"buffer_load_dwordx4 v[220:223], v1, s[16:19], s4 offen offset:1024 sc0 sc1\nZvav 160,128,96\nRa 176,3,61440\nv_mov_b32 v8, 0\nMvav 160,132,104\nv_mov_b32 v9, 0\nCV 196,196,197\n"
"Mvav 160,136,112\nRa 180,3,62464\nCV 197,198,199\nMvav 160,140,120\nWL 6\nCV 198,204,205\nZvav 164,140,124\nRa 184,3,63488\nCV 199,206,207\nMvav 164,136,116\nD1 1,16,46,3072\n"
"s_add_u32 s5, s94, 655360\nMvav 164,132,108\nRa 188,3,64512\ns_barrier\nMvav 164,128,100\nSv 196,1,16,5,1024\nCV 192,192,193\nMvav 164,144,36\nRa 128,0,0\nCV 193,194,195\n"
"Mvav 164,148,44\nCV 194,200,201\nCV 195,202,203\nMvav 164,152,52\nRa 132,0,1024\nSv 192,1,16,5,0\nMvav 164,156,60\nWL 6\ns_add_u32 m0, s89, 32768\ns_add_u32 s46, s96, 1343488\n"
"Mvav 160,156,56\nRa 136,0,2048\nMvav 160,152,48\nD1 1,16,46,0\nMvav 160,148,40\nRa 140,0,3072\nMvav 160,144,32\nZvav 168,160,96\nRa 144,0,4096\nWV 12\nv_max_u32 v21, v211, v215\n"
"v_cmp_eq_u32 vcc, -1, v21\ns_nop 0\ns_cbranch_vccnz .S90\n.S91:\nMvav 168,164,104\nv_lshlrev_b32 v176, 16, v208\nMvav 168,168,112\nRa 148,0,5120\nMvav 168,172,120\nWL 6\n"
"v_and_b32 v177, 0xffff0000, v208\nZvav 172,172,124\nRa 152,0,6144\nMvav 172,168,116\nD1 1,16,46,1024\nMvav 172,164,108\nRa 156,0,7168\nv_lshlrev_b32 v178, 16, v209\n"
"Mvav 172,160,100\nv_and_b32 v179, 0xffff0000, v209\nMvav 172,176,36\nRa 160,0,8192\nv_lshlrev_b32 v180, 16, v212\nMvav 172,180,44\nv_and_b32 v181, 0xffff0000, v212\n"
"s_add_u32 s4, s92, 262144\nMvav 172,184,52\nRa 164,0,9216\nMvav 172,188,60\nWL 6\nv_lshlrev_b32 v182, 16, v213\n"
"buffer_load_dwordx4 v[224:227], v1, s[16:19], s4 offen offset:0 sc0 sc1\nMvav 168,188,56\nRa 168,0,10240\nMvav 168,184,48\nD1 1,16,46,2048\nMvav 168,180,40\nRa 172,0,11264\n"
"v_and_b32 v183, 0xffff0000, v213\nMvav 168,176,32\nv_lshlrev_b32 v184, 16, v210\nbuffer_load_dwordx4 v[228:231], v1, s[16:19], s4 offen offset:1024 sc0 sc1\nMvav 176,128,96\n"
"Ra 176,0,12288\nv_and_b32 v185, 0xffff0000, v210\nMvav 176,132,104\nv_lshlrev_b32 v186, 16, v211\nCV 164,164,165\nMvav 176,136,112\nRa 180,0,13312\nCV 165,166,167\n"
"Mvav 176,140,120\nWL 6\nCV 166,172,173\nMvav 180,140,124\nRa 184,0,14336\nv_and_b32 v187, 0xffff0000, v211\nMvav 180,136,116\nD1 1,16,46,3072\nCV 167,174,175\nMvav 180,132,108\n"
"Ra 188,0,15360\ns_add_u32 s5, s94, 786432\nMvav 180,128,100\nv_lshlrev_b32 v188, 16, v214\nSv 164,1,16,5,1024\nMvav 180,144,36\nRa 128,0,16384\nv_and_b32 v189, 0xffff0000, v214\n"
"Mvav 180,148,44\nv_lshlrev_b32 v190, 16, v215\nCV 160,160,161\nMvav 180,152,52\nRa 132,0,17408\nCV 161,162,163\nMvav 180,156,60\nWL 6\ns_add_u32 m0, s89, 49152\n"
"s_add_u32 s46, s96, 1376256\nMvav 176,156,56\nRa 136,0,18432\nv_and_b32 v191, 0xffff0000, v215\nMvav 176,152,48\nD1 1,16,46,0\nCV 162,168,169\nMvav 176,148,40\nRa 140,0,19456\n"
"CV 163,170,171\nMvav 176,144,32\nSv 160,1,16,5,0\nMvav 184,160,96\nRa 144,0,20480\nWV 12\nv_max_u32 v21, v219, v223\nv_cmp_eq_u32 vcc, -1, v21\ns_nop 0\ns_cbranch_vccnz .S93\n"
".S94:\nMvav 184,164,104\nv_lshlrev_b32 v192, 16, v216\nMvav 184,168,112\nRa 148,0,21504\nMvav 184,172,120\nWL 6\nv_and_b32 v193, 0xffff0000, v216\nMvav 188,172,124\n"
"Ra 152,0,22528\nMvav 188,168,116\nD1 1,16,46,1024\nMvav 188,164,108\nRa 156,0,23552\nv_lshlrev_b32 v194, 16, v217\nMvav 188,160,100\nv_and_b32 v195, 0xffff0000, v217\n"
"Mvav 188,176,36\nRa 160,0,24576\nv_lshlrev_b32 v196, 16, v220\nMvav 188,180,44\nv_and_b32 v197, 0xffff0000, v220\ns_add_u32 s4, s92, 393216\nMvav 188,184,52\nRa 164,0,25600\n"
"Mvav 188,188,60\nWL 6\nv_lshlrev_b32 v198, 16, v221\nbuffer_load_dwordx4 v[208:211], v1, s[16:19], s4 offen offset:0 sc0 sc1\nMvav 184,188,56\nRa 168,0,26624\nMvav 184,184,48\n"
"D1 1,16,46,2048\nMvav 184,180,40\nRa 172,0,27648\nv_and_b32 v199, 0xffff0000, v221\nMvav 184,176,32\nv_lshlrev_b32 v200, 16, v218\n"
"buffer_load_dwordx4 v[212:215], v1, s[16:19], s4 offen offset:1024 sc0 sc1\nMvav 192,128,96\nRa 176,0,28672\nv_and_b32 v201, 0xffff0000, v218\nMvav 192,132,104\n"
"v_lshlrev_b32 v202, 16, v219\nCV 180,180,181\nMvav 192,136,112\nRa 180,0,29696\nCV 181,182,183\nMvav 192,140,120\ns_waitcnt vmcnt(15) lgkmcnt(6)\nCV 182,188,189\nMvav 196,140,124\n"
"Ra 184,0,30720\nv_and_b32 v203, 0xffff0000, v219\nMvav 196,136,116\nD1 1,16,46,3072\nCV 183,190,191\nMvav 196,132,108\nRa 188,0,31744\ns_barrier\nMvav 196,128,100\n"
"v_lshlrev_b32 v204, 16, v222\ns_add_u32 s5, s94, 917504\nMvav 196,144,36\nRa 128,2,16384\nv_and_b32 v205, 0xffff0000, v222\nMvav 196,148,44\nv_lshlrev_b32 v206, 16, v223\n"
"Sv 180,1,16,5,1024\nMvav 196,152,52\nRa 132,2,17408\nCV 176,176,177\nMvav 196,156,60\nWL 6\ns_add_u32 m0, s89, 65536\ns_add_u32 s46, s96, 1409024\nMvav 192,156,56\nRa 136,2,18432\n"
"v_and_b32 v207, 0xffff0000, v223\nMvav 192,152,48\nD1 1,16,46,0\nCV 177,178,179\nMvav 192,148,40\nRa 140,2,19456\nCV 178,184,185\nMvav 192,144,32\nCV 179,186,187\nSv 176,1,16,5,0\n"
"Mvav 200,160,96\nRa 144,2,20480\nWV 12\nv_max_u32 v21, v227, v231\nv_cmp_eq_u32 vcc, -1, v21\ns_nop 0\ns_cbranch_vccnz .S96\n.S97:\nMvav 200,164,104\nv_lshlrev_b32 v160, 16, v224\n"
"Mvav 200,168,112\nRa 148,2,21504\nMvav 200,172,120\nWL 6\nv_and_b32 v161, 0xffff0000, v224\nMvav 204,172,124\nRa 152,2,22528\nMvav 204,168,116\n.W553:\ns_cbranch_execz .WB21\n"
"D1 1,16,46,1024\nMvav 204,164,108\nRa 156,2,23552\nv_lshlrev_b32 v162, 16, v225\nMvav 204,160,100\nv_and_b32 v163, 0xffff0000, v225\nMvav 204,176,36\nRa 160,2,24576\n"
"v_lshlrev_b32 v164, 16, v228\nMvav 204,180,44\nv_and_b32 v165, 0xffff0000, v228\ns_add_u32 s4, s92, 524288\nMvav 204,184,52\nRa 164,2,25600\nMvav 204,188,60\nWL 6\n"
"v_lshlrev_b32 v166, 16, v229\nbuffer_load_dwordx4 v[216:219], v1, s[16:19], s4 offen offset:0 sc0 sc1\nMvav 200,188,56\nRa 168,2,26624\nMvav 200,184,48\nD1 1,16,46,2048\n"
"Mvav 200,180,40\nRa 172,2,27648\nv_and_b32 v167, 0xffff0000, v229\nMvav 200,176,32\nv_lshlrev_b32 v168, 16, v226\n"
"buffer_load_dwordx4 v[220:223], v1, s[16:19], s4 offen offset:1024 sc0 sc1\nMvav 160,128,96\nRa 176,2,28672\nv_and_b32 v169, 0xffff0000, v226\nMvav 160,132,104\n"
"v_lshlrev_b32 v170, 16, v227\nCV 196,196,197\nMvav 160,136,112\nRa 180,2,29696\nCV 197,198,199\nMvav 160,140,120\nWL 6\nCV 198,204,205\nMvav 164,140,124\nRa 184,2,30720\n"
"v_and_b32 v24, 0x70, v1\nMvav 164,136,116\nD1 1,16,46,3072\nCV 199,206,207\nMvav 164,132,108\nRa 188,2,31744\nv_and_b32 v171, 0xffff0000, v227\nMvav 164,128,100\n"
"v_lshlrev_b32 v172, 16, v230\ns_add_u32 s5, s94, 1048576\nMvav 164,144,36\nRa 128,2,0\nv_lshlrev_b32 v24, 5, v24\nMvav 164,148,44\nv_and_b32 v173, 0xffff0000, v230\n"
"Sv 196,1,16,5,1024\nMvav 164,152,52\nRa 132,2,1024\nCV 192,192,193\nMvav 164,156,60\nWL 6\nv_lshlrev_b32 v174, 16, v231\nMvav 160,156,56\nRa 136,2,2048\n"
"buffer_load_dword v27, v24, s[16:19], s41 offen sc0 sc1\nMvav 160,152,48\nv_and_b32 v175, 0xffff0000, v231\nCV 193,194,195\nMvav 160,148,40\nRa 140,2,3072\nCV 194,200,201\n"
"Mvav 160,144,32\nCV 195,202,203\nSv 192,1,16,5,0\nMvav 168,160,96\nRa 144,2,4096\nWV 12\nv_max_u32 v21, v211, v215\nv_cmp_eq_u32 vcc, -1, v21\ns_nop 0\ns_cbranch_vccnz .S99\n"
".S100:\nMvav 168,164,104\nv_lshlrev_b32 v176, 16, v208\nMvav 168,168,112\nRa 148,2,5120\nMvav 168,172,120\nWL 6\nMvav 172,172,124\nRa 152,2,6144\nv_and_b32 v177, 0xffff0000, v208\n"
"Mvav 172,168,116\nv_lshlrev_b32 v178, 16, v209\nMvav 172,164,108\nRa 156,2,7168\nMvav 172,160,100\nv_and_b32 v179, 0xffff0000, v209\nMvav 172,176,36\nRa 160,2,8192\n"
"v_lshlrev_b32 v180, 16, v212\nMvav 172,180,44\nv_and_b32 v181, 0xffff0000, v212\ns_add_u32 s4, s92, 655360\nMvav 172,184,52\nRa 164,2,9216\nMvav 172,188,60\nWL 6\n"
"buffer_load_dwordx4 v[224:227], v1, s[16:19], s4 offen offset:0 sc0 sc1\nMvav 168,188,56\nRa 168,2,10240\nv_lshlrev_b32 v182, 16, v213\nMvav 168,184,48\n"
"v_and_b32 v183, 0xffff0000, v213\nMvav 168,180,40\nRa 172,2,11264\nMvav 168,176,32\nv_lshlrev_b32 v184, 16, v210\n"
"buffer_load_dwordx4 v[228:231], v1, s[16:19], s4 offen offset:1024 sc0 sc1\nMvav 176,128,96\nRa 176,2,12288\nv_and_b32 v185, 0xffff0000, v210\nMvav 176,132,104\n"
"v_lshlrev_b32 v186, 16, v211\nCV 164,164,165\nMvav 176,136,112\nRa 180,2,13312\nCV 165,166,167\nMvav 176,140,120\ns_waitcnt vmcnt(13) lgkmcnt(6)\nCV 166,172,173\nMvav 180,140,124\n"
"Ra 184,2,14336\nCV 167,174,175\nMvav 180,136,116\nv_and_b32 v187, 0xffff0000, v211\ns_add_u32 s5, s94, 1179648\nMvav 180,132,108\nRa 188,2,15360\ns_barrier\nMvav 180,128,100\n"
"v_lshlrev_b32 v188, 16, v214\nSv 164,1,16,5,1024\nMvav 180,144,36\nRa 128,2,32768\nv_and_b32 v189, 0xffff0000, v214\nMvav 180,148,44\nv_lshlrev_b32 v190, 16, v215\nCV 160,160,161\n"
"Mvav 180,152,52\nRa 132,2,33792\nCV 161,162,163\nMvav 180,156,60\nWL 6\nCV 162,168,169\nMvav 176,156,56\nRa 136,2,34816\nCV 163,170,171\nMvav 176,152,48\n"
"v_and_b32 v191, 0xffff0000, v215\nSv 160,1,16,5,0\nMvav 176,148,40\nRa 140,2,35840\nMvav 176,144,32\nMvav 184,160,96\nRa 144,2,36864\nWV 8\nv_max_u32 v21, v219, v223\n"
"v_cmp_eq_u32 vcc, -1, v21\ns_nop 0\ns_cbranch_vccnz .S102\n.S103:\nMvav 184,164,104\nv_lshlrev_b32 v192, 16, v216\nMvav 184,168,112\nRa 148,2,37888\nMvav 184,172,120\nWL 6\n"
"Mvav 188,172,124\nRa 152,2,38912\nv_and_b32 v193, 0xffff0000, v216\nMvav 188,168,116\nv_lshlrev_b32 v194, 16, v217\nMvav 188,164,108\nRa 156,2,39936\nMvav 188,160,100\n"
"v_and_b32 v195, 0xffff0000, v217\nMvav 188,176,36\nRa 160,2,40960\nv_lshlrev_b32 v196, 16, v220\nMvav 188,180,44\nv_and_b32 v197, 0xffff0000, v220\ns_add_u32 s4, s92, 786432\n"
"Mvav 188,184,52\nRa 164,2,41984\nMvav 188,188,60\nWL 6\nbuffer_load_dwordx4 v[208:211], v1, s[16:19], s4 offen offset:0 sc0 sc1\nMvav 184,188,56\nRa 168,2,43008\n"
"v_lshlrev_b32 v198, 16, v221\nMvav 184,184,48\nv_and_b32 v199, 0xffff0000, v221\nMvav 184,180,40\nRa 172,2,44032\nMvav 184,176,32\nv_lshlrev_b32 v200, 16, v218\n"
"buffer_load_dwordx4 v[212:215], v1, s[16:19], s4 offen offset:1024 sc0 sc1\nMvav 192,128,96\nRa 176,2,45056\nv_and_b32 v201, 0xffff0000, v218\nMvav 192,132,104\n"
"v_lshlrev_b32 v202, 16, v219\nCV 180,180,181\nMvav 192,136,112\nRa 180,2,46080\nCV 181,182,183\nMvav 192,140,120\nWL 6\nCV 182,188,189\nMvav 196,140,124\nRa 184,2,47104\n"
"CV 183,190,191\nMvav 196,136,116\nv_and_b32 v203, 0xffff0000, v219\ns_add_u32 s5, s94, 1310720\nMvav 196,132,108\nRa 188,2,48128\nSv 180,1,16,5,1024\nMvav 196,128,100\n"
"v_lshlrev_b32 v204, 16, v222\nCV 176,176,177\nMvav 196,144,36\nRa 128,2,49152\nv_and_b32 v205, 0xffff0000, v222\nMvav 196,148,44\nv_lshlrev_b32 v206, 16, v223\nCV 177,178,179\n"
"Mvav 196,152,52\nRa 132,2,50176\nCV 178,184,185\nMvav 196,156,60\nWL 6\nCV 179,186,187\nMvav 192,156,56\nRa 136,2,51200\nSv 176,1,16,5,0\nMvav 192,152,48\n"
"v_and_b32 v207, 0xffff0000, v223\nMvav 192,148,40\nRa 140,2,52224\nMvav 192,144,32\nMvav 200,160,96\nRa 144,2,53248\nWV 6\nv_max_u32 v21, v227, v231\nv_cmp_eq_u32 vcc, -1, v21\n"
"s_nop 0\ns_cbranch_vccnz .S105\n.S106:\nMvav 200,164,104\nv_lshlrev_b32 v160, 16, v224\nv_xor_b32 v27, s15, v27\nv_and_b32 v26, 0xffffdfff, v27\nv_cmp_ne_u32 s[64:65], 0, v27\n"
"v_cmp_ne_u32 vcc, 0, v26\ns_nop 0\ns_cbranch_vccnz .S117\n.S118:\nMvav 200,168,112\nRa 148,2,54272\ns_bitcmp1_b32 s64, 0\ns_cselect_b32 s2, 0x2000, 0\nMvav 200,172,120\nWL 6\n"
"Mvav 204,172,124\nRa 152,2,55296\nv_and_b32 v161, 0xffff0000, v224\nMvav 204,168,116\nv_lshlrev_b32 v162, 16, v225\nMvav 204,164,108\nRa 156,2,56320\ns_xor_b32 s2, s2, s20\n"
"Mvav 204,160,100\nv_and_b32 v163, 0xffff0000, v225\nMvav 204,176,36\nRa 160,2,57344\nv_lshlrev_b32 v164, 16, v228\nMvav 204,180,44\nv_and_b32 v165, 0xffff0000, v228\n"
"Mvav 204,184,52\nRa 164,2,58368\nMvav 204,188,60\nWL 6\ns_add_u32 s24, s2, 0x0\nMvav 200,188,56\nRa 168,2,59392\nv_lshlrev_b32 v166, 16, v229\nMvav 200,184,48\n"
"v_and_b32 v167, 0xffff0000, v229\ns_bitcmp1_b32 s64, 1\ns_cselect_b32 s2, 0x2000, 0\nMvav 200,180,40\nRa 172,2,60416\nMvav 200,176,32\nv_lshlrev_b32 v168, 16, v226\n"
"Mvav 160,128,96\nRa 176,2,61440\nv_and_b32 v169, 0xffff0000, v226\nMvav 160,132,104\nv_lshlrev_b32 v170, 16, v227\nCV 196,196,197\nMvav 160,136,112\nRa 180,2,62464\n"
"CV 197,198,199\nMvav 160,140,120\nWL 6\nCV 198,204,205\nMvav 164,140,124\nRa 184,2,63488\nCV 199,206,207\nMvav 164,136,116\nv_and_b32 v171, 0xffff0000, v227\n"
"s_add_u32 s5, s94, 1441792\nMvav 164,132,108\nRa 188,2,64512\ns_barrier\nMvav 164,128,100\nv_lshlrev_b32 v172, 16, v230\nSv 196,1,16,5,1024\nMvav 164,144,36\nRa 128,3,0\n"
"v_and_b32 v173, 0xffff0000, v230\nMvav 164,148,44\nv_lshlrev_b32 v174, 16, v231\nCV 192,192,193\nMvav 164,152,52\nRa 132,3,1024\nCV 193,194,195\nMvav 164,156,60\nWL 6\n"
"s_xor_b32 s2, s2, s20\nMvav 160,156,56\nRa 136,3,2048\nCV 194,200,201\nMvav 160,152,48\nv_and_b32 v175, 0xffff0000, v231\nCV 195,202,203\nMvav 160,148,40\nRa 140,3,3072\n"
"Sv 192,1,16,5,0\nMvav 160,144,32\ns_add_u32 s25, s2, 0x1800000\ns_bitcmp1_b32 s64, 2\ns_cselect_b32 s2, 0x2000, 0\nMvav 168,160,96\nRa 144,3,4096\nWV 4\nv_max_u32 v21, v211, v215\n"
"v_cmp_eq_u32 vcc, -1, v21\ns_nop 0\ns_cbranch_vccnz .S108\n.S109:\nMvav 168,164,104\nv_lshlrev_b32 v176, 16, v208\nMvav 168,168,112\nRa 148,3,5120\nMvav 168,172,120\nWL 6\n"
"Mvav 172,172,124\nRa 152,3,6144\nv_and_b32 v177, 0xffff0000, v208\nMvav 172,168,116\nv_lshlrev_b32 v178, 16, v209\nMvav 172,164,108\nRa 156,3,7168\ns_xor_b32 s2, s2, s20\n"
"Mvav 172,160,100\nv_and_b32 v179, 0xffff0000, v209\nMvav 172,176,36\nRa 160,3,8192\nv_lshlrev_b32 v180, 16, v212\nMvav 172,180,44\nv_and_b32 v181, 0xffff0000, v212\n"
"Mvav 172,184,52\nRa 164,3,9216\nMvav 172,188,60\nWL 6\ns_add_u32 s26, s2, 0x3000000\nMvav 168,188,56\nRa 168,3,10240\nv_lshlrev_b32 v182, 16, v213\nMvav 168,184,48\n"
"v_and_b32 v183, 0xffff0000, v213\ns_bitcmp1_b32 s64, 3\ns_cselect_b32 s2, 0x2000, 0\nMvav 168,180,40\nRa 172,3,11264\nMvav 168,176,32\nv_lshlrev_b32 v184, 16, v210\n"
"Mvav 176,128,96\nRa 176,3,12288\ns_xor_b32 s2, s2, s20\nMvav 176,132,104\nv_and_b32 v185, 0xffff0000, v210\nCV 164,164,165\nMvav 176,136,112\nRa 180,3,13312\nCV 165,166,167\n"
"Mvav 176,140,120\nWL 6\nCV 166,172,173\nMvav 180,140,124\nRa 184,3,14336\nCV 167,174,175\nMvav 180,136,116\nv_lshlrev_b32 v186, 16, v211\ns_add_u32 s5, s94, 1572864\n"
"Mvav 180,132,108\nRa 188,3,15360\ns_add_u32 s27, s2, 0x4800000\nMvav 180,128,100\nv_and_b32 v187, 0xffff0000, v211\ns_bitcmp1_b32 s64, 4\ns_cselect_b32 s2, 0x2000, 0\n"
"Mvav 180,144,36\nv_lshlrev_b32 v188, 16, v214\nSv 164,1,16,5,1024\nMvav 180,148,44\nv_and_b32 v189, 0xffff0000, v214\nCV 160,160,161\nMvav 180,152,52\n"
"v_lshlrev_b32 v190, 16, v215\nCV 161,162,163\nMvav 180,156,60\nWL 4\ns_xor_b32 s2, s2, s20\nMvav 176,156,56\nv_and_b32 v191, 0xffff0000, v215\nCV 162,168,169\nMvav 176,152,48\n"
"s_add_u32 s32, s2, 0x6000000\ns_bitcmp1_b32 s64, 5\ns_cselect_b32 s2, 0x2000, 0\nMvav 176,148,40\ns_xor_b32 s2, s2, s20\nCV 163,170,171\nMvav 176,144,32\n"
"s_add_u32 s33, s2, 0x7800000\ns_bitcmp1_b32 s64, 6\ns_cselect_b32 s2, 0x2000, 0\nMvav 184,160,96\ns_xor_b32 s2, s2, s20\ns_add_u32 s34, s2, 0x9000000\nMvav 184,164,104\n"
"s_bitcmp1_b32 s64, 7\ns_cselect_b32 s2, 0x2000, 0\nMvav 184,168,112\ns_xor_b32 s2, s2, s20\nSv 160,1,16,5,0\nMvav 184,172,120\nWL 0\nMvav 188,172,124\n"
"s_add_u32 s35, s2, 0xa800000\nMvav 188,168,116\nMvav 188,164,108\nMvav 188,160,100\nMvav 188,176,36\nMvav 188,180,44\nMvav 188,184,52\nMvav 188,188,60\nMvav 184,188,56\n"
"Mvav 184,184,48\nMvav 184,180,40\nMvav 184,176,32\nCV 180,180,181\nCV 181,182,183\ns_nop 1\nCV 182,188,189\nCV 183,190,191\ns_add_u32 s5, s94, 1703936\nSv 180,1,16,5,1024\n"
"CV 176,176,177\nCV 177,178,179\nCV 178,184,185\nCV 179,186,187\nSv 176,1,16,5,0\ns_cmp_eq_u32 s13, 0\ns_cbranch_scc1 .Z120\n"
"buffer_load_dwordx4 v[12:15], v91, s[28:31], s22 offen offset:0\nbuffer_load_dwordx4 v[16:19], v91, s[28:31], s23 offen offset:0\n"
"buffer_load_dwordx4 v[64:67], v89, s[16:19], s40 offen offset:0 sc0 sc1\nbuffer_load_dwordx4 v[68:71], v89, s[16:19], s40 offen offset:16 sc0 sc1\n"
"buffer_load_dwordx4 v[72:75], v89, s[16:19], s40 offen offset:1792 sc0 sc1\nbuffer_load_dwordx4 v[76:79], v89, s[16:19], s40 offen offset:1808 sc0 sc1\n"
"buffer_load_dwordx2 v[80:81], v88, s[16:19], s63 offen offset:0 sc0 sc1\nbuffer_load_dwordx2 v[82:83], v88, s[16:19], s63 offen offset:512 sc0 sc1\n"
"buffer_load_dwordx4 v[96:99], v1, s[16:19], s24 offen offset:0 sc0 sc1\nbuffer_load_dwordx4 v[100:103], v1, s[16:19], s24 offen offset:1024 sc0 sc1\n"
"buffer_load_dwordx4 v[104:107], v1, s[16:19], s25 offen offset:0 sc0 sc1\nbuffer_load_dwordx4 v[108:111], v1, s[16:19], s25 offen offset:1024 sc0 sc1\n"
"buffer_load_dwordx4 v[112:115], v1, s[16:19], s26 offen offset:0 sc0 sc1\nbuffer_load_dwordx4 v[116:119], v1, s[16:19], s26 offen offset:1024 sc0 sc1\n"
"buffer_load_dwordx4 v[120:123], v1, s[16:19], s27 offen offset:0 sc0 sc1\nbuffer_load_dwordx4 v[124:127], v1, s[16:19], s27 offen offset:1024 sc0 sc1\n"
"buffer_load_dwordx4 v[32:35], v1, s[16:19], s32 offen offset:0 sc0 sc1\nbuffer_load_dwordx4 v[36:39], v1, s[16:19], s32 offen offset:1024 sc0 sc1\n"
"buffer_load_dwordx4 v[40:43], v1, s[16:19], s33 offen offset:0 sc0 sc1\nbuffer_load_dwordx4 v[44:47], v1, s[16:19], s33 offen offset:1024 sc0 sc1\n"
"buffer_load_dwordx4 v[48:51], v1, s[16:19], s34 offen offset:0 sc0 sc1\nbuffer_load_dwordx4 v[52:55], v1, s[16:19], s34 offen offset:1024 sc0 sc1\n"
"buffer_load_dwordx4 v[56:59], v1, s[16:19], s35 offen offset:0 sc0 sc1\nbuffer_load_dwordx4 v[60:63], v1, s[16:19], s35 offen offset:1024 sc0 sc1\nWV 16\nv_xor_b32 v66, s15, v66\n"
"v_xor_b32 v70, s15, v70\nv_xor_b32 v74, s15, v74\nv_xor_b32 v78, s15, v78\nv_xor_b32 v81, s15, v81\nv_xor_b32 v83, s15, v83\nv_or3_b32 v92, v66, v70, v74\n"
"v_or3_b32 v92, v92, v78, v81\nv_or_b32 v92, v92, v83\nv_cmp_ne_u32 vcc, 0, v92\ns_nop 0\ns_cbranch_vccnz .S111\n.S112:\nv_and_b32 v92, 0x7ff, v67\nv_lshrrev_b32 v93, 16, v67\n"
"v_cmp_eq_u32 s[44:45], v92, v90\nv_cmp_eq_u32 s[46:47], v93, v90\nv_cmp_eq_u32 s[48:49], v92, v94\nv_cmp_eq_u32 s[50:51], v93, v94\nv_cndmask_b32 v12, v12, v64, s[44:45]\n"
"v_cndmask_b32 v13, v13, v65, s[44:45]\nv_cndmask_b32 v14, v14, v68, s[44:45]\nv_cndmask_b32 v15, v15, v69, s[44:45]\nv_cndmask_b32 v12, v12, v72, s[46:47]\n"
"v_cndmask_b32 v13, v13, v73, s[46:47]\nv_cndmask_b32 v14, v14, v76, s[46:47]\nv_cndmask_b32 v15, v15, v77, s[46:47]\nv_cndmask_b32 v16, v16, v64, s[48:49]\n"
"v_cndmask_b32 v17, v17, v65, s[48:49]\nv_cndmask_b32 v18, v18, v68, s[48:49]\nv_cndmask_b32 v19, v19, v69, s[48:49]\nv_cndmask_b32 v16, v16, v72, s[50:51]\n"
"v_cndmask_b32 v17, v17, v73, s[50:51]\nv_cndmask_b32 v18, v18, v76, s[50:51]\nv_cndmask_b32 v19, v19, v77, s[50:51]\nv_sub_f32 v92, 1.0, v80\nv_sub_f32 v93, 1.0, v82\nCV 92,92,93\n"
"v_and_b32 v93, 0xffff0000, v92\nv_lshlrev_b32 v92, 16, v92\nv_lshlrev_b32 v64, 16, v12\nv_and_b32 v65, 0xffff0000, v12\nMU 64,64,92\nMU 65,65,92\nv_lshlrev_b32 v66, 16, v13\n"
"v_and_b32 v67, 0xffff0000, v13\nMU 66,66,92\nMU 67,67,92\nv_lshlrev_b32 v68, 16, v14\nv_and_b32 v69, 0xffff0000, v14\nMU 68,68,92\nMU 69,69,92\nv_lshlrev_b32 v70, 16, v15\n"
"v_and_b32 v71, 0xffff0000, v15\nMU 70,70,92\nMU 71,71,92\nv_lshlrev_b32 v72, 16, v16\nv_and_b32 v73, 0xffff0000, v16\nMU 72,72,93\nMU 73,73,93\nv_lshlrev_b32 v74, 16, v17\n"
"v_and_b32 v75, 0xffff0000, v17\nMU 74,74,93\nMU 75,75,93\nv_lshlrev_b32 v76, 16, v18\nv_and_b32 v77, 0xffff0000, v18\nMU 76,76,93\nMU 77,77,93\nv_lshlrev_b32 v78, 16, v19\n"
"v_and_b32 v79, 0xffff0000, v19\nMU 78,78,93\nMU 79,79,93\ns_mov_b32 s7, 262144\nWV 0\n.Z123:\nv_max_u32 v84, v99, v103\nv_cmp_eq_u32 s[44:45], -1, v84\n.W554:\n"
"s_cbranch_execz .WB21\nv_max_u32 v84, v107, v111\nv_cmp_eq_u32 s[46:47], -1, v84\nv_max_u32 v84, v115, v119\nv_cmp_eq_u32 s[48:49], -1, v84\nv_max_u32 v84, v123, v127\n"
"v_cmp_eq_u32 s[50:51], -1, v84\nv_max_u32 v84, v35, v39\nv_cmp_eq_u32 s[52:53], -1, v84\nv_max_u32 v84, v43, v47\nv_cmp_eq_u32 s[54:55], -1, v84\nv_max_u32 v84, v51, v55\n"
"v_cmp_eq_u32 s[56:57], -1, v84\nv_max_u32 v84, v59, v63\nv_cmp_eq_u32 s[58:59], -1, v84\ns_or_b64 s[0:1], s[44:45], s[46:47]\ns_or_b64 s[2:3], s[48:49], s[50:51]\n"
"s_or_b64 s[0:1], s[0:1], s[52:53]\ns_or_b64 s[2:3], s[2:3], s[54:55]\ns_or_b64 s[0:1], s[0:1], s[56:57]\ns_or_b64 s[2:3], s[2:3], s[58:59]\ns_or_b64 s[0:1], s[0:1], s[2:3]\n"
"s_cbranch_scc1 .Z121\n.Z122:\nZvvv 136,6,96\nMvvv 136,6,104\nMvvv 136,6,112\nMvvv 136,6,120\nMvvv 136,6,32\nMvvv 136,6,40\nMvvv 136,6,48\nMvvv 136,6,56\nZvvv 140,4,96\n"
"Mvvv 140,4,104\nMvvv 140,4,112\nMvvv 140,4,120\nMvvv 140,4,32\nMvvv 140,4,40\nMvvv 140,4,48\nMvvv 140,4,56\nZvvv 144,6,100\nMvvv 144,6,108\nMvvv 144,6,116\nMvvv 144,6,124\n"
"Mvvv 144,6,36\nMvvv 144,6,44\nMvvv 144,6,52\nMvvv 144,6,60\nZvvv 148,4,100\nMvvv 148,4,108\nMvvv 148,4,116\nMvvv 148,4,124\nMvvv 148,4,36\nMvvv 148,4,44\nMvvv 148,4,52\n"
"Mvvv 148,4,60\nFM 64,80,136\nFM 65,80,137\nFM 66,80,138\nFM 67,80,139\nFM 68,80,140\nFM 69,80,141\nFM 70,80,142\nFM 71,80,143\nCV 12,64,65\nCV 13,66,67\nCV 14,68,69\nCV 15,70,71\n"
"Sv 12,91,36,22,0\nFM 72,82,144\nFM 73,82,145\nFM 74,82,146\nFM 75,82,147\nFM 76,82,148\nFM 77,82,149\nFM 78,82,150\nFM 79,82,151\nCV 16,72,73\nCV 17,74,75\nCV 18,76,77\n"
"CV 19,78,79\nSv 16,91,36,23,0\n.Z120:\ns_endpgm\n.EP132:\n.S14:\ns_mov_b32 s7, 262144\n.S16:\nbuffer_load_dwordx4 v[32:35], v1, s[16:19], s6 offen offset:0 sc0 sc1\n"
"buffer_load_dwordx4 v[36:39], v1, s[16:19], s6 offen offset:1024 sc0 sc1\nbuffer_load_dwordx4 v[40:43], v1, s[16:19], s6 offen offset:2048 sc0 sc1\n"
"buffer_load_dwordx4 v[44:47], v1, s[16:19], s6 offen offset:3072 sc0 sc1\nWV 0\nv_xor_b32 v48, s15, v33\nv_xor_b32 v49, s15, v35\nv_xor_b32 v50, s15, v37\nv_xor_b32 v51, s15, v39\n"
"v_xor_b32 v52, s15, v41\nv_xor_b32 v53, s15, v43\nv_xor_b32 v54, s15, v45\nv_xor_b32 v55, s15, v47\nv_or3_b32 v48, v48, v49, v50\nv_or3_b32 v51, v51, v52, v53\n"
"v_or3_b32 v48, v48, v54, v55\nv_or_b32 v48, v48, v51\nv_cmp_ne_u32 vcc, 0, v48\ns_nop 0\ns_cbranch_vccz .S15\ns_sleep 6\ns_sub_u32 s7, s7, 1\ns_cbranch_scc0 .S16\ns_branch .S15\n"
".S17:\ns_mov_b32 s7, 262144\n.S19:\ns_lshl_b32 s5, s73, 8\ns_add_u32 s5, s5, 0xc04ac00\nv_lshrrev_b32 v27, 2, v1\nbuffer_load_dword v27, v27, s[16:19], s5 offen sc0 sc1\nWV 0\n"
"v_xor_b32 v27, s15, v27\nv_and_b32 v255, 0xffffdfff, v27\nv_cmp_ne_u32 vcc, 0, v255\ns_nop 0\ns_cbranch_vccz .S18\ns_sleep 6\ns_sub_u32 s7, s7, 1\ns_cbranch_scc0 .S19\n"
"s_branch .S18\n.T23:\ns_mov_b32 s62, 1\ns_call_b64 s[80:81], .RA10\ns_branch .T24\n.T25:\ns_mov_b32 s62, 1\ns_call_b64 s[80:81], .RA10\ns_branch .T26\n.T28:\ns_mov_b32 s62, 0\n"
"s_call_b64 s[80:81], .RB11\ns_branch .T29\n.T30:\ns_mov_b32 s62, 0\ns_call_b64 s[80:81], .RA10\ns_branch .T31\n.T33:\ns_mov_b32 s62, 0\ns_call_b64 s[80:81], .RB11\ns_branch .T34\n"
".T35:\ns_mov_b32 s62, 0\ns_call_b64 s[80:81], .RA10\ns_branch .T36\n.S38:\ns_mov_b32 s7, 262144\n.S40:\nv_and_b32 v27, 0x70, v1\n"
"buffer_load_dword v27, v27, s[16:19], s61 offen offset:128 sc0 sc1\nWV 0\nv_cmp_ne_u32 vcc, s15, v27\ns_nop 0\ns_cbranch_vccz .S39\ns_sleep 6\ns_sub_u32 s7, s7, 1\n"
"s_cbranch_scc0 .S40\ns_branch .S39\n.S41:\ns_mov_b32 s7, 262144\n.S43:\nv_and_b32 v27, 0x70, v1\nbuffer_load_dword v27, v27, s[16:19], s61 offen offset:136 sc0 sc1\nWV 0\n"
"v_cmp_ne_u32 vcc, s15, v27\ns_nop 0\ns_cbranch_vccz .S42\ns_sleep 6\ns_sub_u32 s7, s7, 1\ns_cbranch_scc0 .S43\ns_branch .S42\n.S44:\ns_mov_b32 s7, 262144\n.S46:\n"
"v_and_b32 v27, 0x70, v1\nbuffer_load_dword v27, v27, s[16:19], s61 offen offset:140 sc0 sc1\nWV 0\nv_cmp_ne_u32 vcc, s15, v27\ns_nop 0\ns_cbranch_vccz .S45\ns_sleep 6\n"
"s_sub_u32 s7, s7, 1\ns_cbranch_scc0 .S46\ns_branch .S45\n.S47:\ns_mov_b32 s7, 262144\n.S49:\nv_and_b32 v27, 0x70, v1\n"
"buffer_load_dword v27, v27, s[16:19], s61 offen offset:12 sc0 sc1\nWV 0\nv_cmp_ne_u32 vcc, s15, v27\ns_nop 0\ns_cbranch_vccz .S48\ns_sleep 6\ns_sub_u32 s7, s7, 1\n"
"s_cbranch_scc0 .S49\ns_branch .S48\n.S50:\ns_mov_b32 s7, 262144\n.S52:\nv_and_b32 v27, 0x70, v1\nbuffer_load_dword v27, v27, s[16:19], s61 offen offset:8 sc0 sc1\nWV 0\n"
"v_cmp_ne_u32 vcc, s15, v27\ns_nop 0\ns_cbranch_vccz .S51\ns_sleep 6\ns_sub_u32 s7, s7, 1\ns_cbranch_scc0 .S52\ns_branch .S51\n.S53:\ns_mov_b32 s7, 262144\n.S55:\n"
"v_and_b32 v27, 0xf0, v1\nbuffer_load_dword v27, v27, s[16:19], s61 offen offset:4 sc0 sc1\nWV 0\nv_cmp_ne_u32 vcc, s15, v27\ns_nop 0\ns_cbranch_vccz .S54\ns_sleep 6\n"
"s_sub_u32 s7, s7, 1\ns_cbranch_scc0 .S55\ns_branch .S54\n.S57:\ns_mov_b32 s7, 262144\n.S59:\nv_lshrrev_b32 v144, 9, v1\nv_mul_u32_u24 v144, 0x18000, v144\n"
"v_lshlrev_b32 v144, 8, v144\nv_and_b32 v145, 0x1f0, v1\nv_add_u32 v144, v144, v145\ns_mov_b32 s6, 0x15c0000\nbuffer_load_dword v136, v144, s[16:19], s6 offen offset:4 sc0 sc1\n"
"s_mov_b32 s6, 0x15c2000\nbuffer_load_dword v137, v144, s[16:19], s6 offen offset:4 sc0 sc1\ns_mov_b32 s6, 0x45c0000\n"
"buffer_load_dword v138, v144, s[16:19], s6 offen offset:4 sc0 sc1\ns_mov_b32 s6, 0x45c2000\nbuffer_load_dword v139, v144, s[16:19], s6 offen offset:4 sc0 sc1\n"
"s_mov_b32 s6, 0x75c0000\nbuffer_load_dword v140, v144, s[16:19], s6 offen offset:4 sc0 sc1\ns_mov_b32 s6, 0x75c2000\n"
"buffer_load_dword v141, v144, s[16:19], s6 offen offset:4 sc0 sc1\ns_mov_b32 s6, 0xa5c0000\nbuffer_load_dword v142, v144, s[16:19], s6 offen offset:4 sc0 sc1\n"
"s_mov_b32 s6, 0xa5c2000\nbuffer_load_dword v143, v144, s[16:19], s6 offen offset:4 sc0 sc1\nWV 0\nv_xor_b32 v136, s15, v136\nv_xor_b32 v137, s15, v137\nv_xor_b32 v138, s15, v138\n"
"v_xor_b32 v139, s15, v139\nv_xor_b32 v140, s15, v140\nv_xor_b32 v141, s15, v141\nv_xor_b32 v142, s15, v142\nv_xor_b32 v143, s15, v143\nv_min_u32 v136, v136, v137\n"
"v_min_u32 v138, v138, v139\nv_min_u32 v140, v140, v141\nv_min_u32 v142, v142, v143\nv_or3_b32 v136, v136, v138, v140\nv_or_b32 v136, v136, v142\nv_cmp_ne_u32 vcc, 0, v136\n"
"s_nop 0\ns_cbranch_vccz .S58\ns_sleep 6\ns_sub_u32 s7, s7, 1\ns_cbranch_scc0 .S59\ns_branch .S58\n.FF56:\nv_lshrrev_b32 v144, 9, v1\nv_mul_u32_u24 v144, 0x18000, v144\n"
"v_lshlrev_b32 v144, 8, v144\nv_and_b32 v145, 0x1f0, v1\nv_add_u32 v144, v144, v145\ns_mov_b32 s6, 0x15c0000\nbuffer_load_dword v136, v144, s[16:19], s6 offen offset:4 sc0 sc1\n"
"s_mov_b32 s6, 0x15c2000\nbuffer_load_dword v137, v144, s[16:19], s6 offen offset:4 sc0 sc1\ns_mov_b32 s6, 0x45c0000\n"
"buffer_load_dword v138, v144, s[16:19], s6 offen offset:4 sc0 sc1\ns_mov_b32 s6, 0x45c2000\nbuffer_load_dword v139, v144, s[16:19], s6 offen offset:4 sc0 sc1\n"
"s_mov_b32 s6, 0x75c0000\nbuffer_load_dword v140, v144, s[16:19], s6 offen offset:4 sc0 sc1\ns_mov_b32 s6, 0x75c2000\n"
"buffer_load_dword v141, v144, s[16:19], s6 offen offset:4 sc0 sc1\ns_mov_b32 s6, 0xa5c0000\nbuffer_load_dword v142, v144, s[16:19], s6 offen offset:4 sc0 sc1\n"
"s_mov_b32 s6, 0xa5c2000\nbuffer_load_dword v143, v144, s[16:19], s6 offen offset:4 sc0 sc1\ns_mov_b32 s90, 0\ns_swappc_b64 s[8:9], s[80:81]\nv_xor_b32 v136, s15, v136\n"
"v_xor_b32 v137, s15, v137\nv_xor_b32 v138, s15, v138\nv_xor_b32 v139, s15, v139\nv_xor_b32 v140, s15, v140\nv_xor_b32 v141, s15, v141\nv_xor_b32 v142, s15, v142\n"
"v_xor_b32 v143, s15, v143\nv_min_u32 v136, v136, v137\nv_min_u32 v138, v138, v139\nv_min_u32 v140, v140, v141\nv_min_u32 v142, v142, v143\nv_or3_b32 v136, v136, v138, v140\n"
"v_or_b32 v136, v136, v142\nv_cmp_ne_u32 vcc, 0, v136\ns_nop 0\ns_cbranch_vccnz .S57\n.S58:\ns_and_b32 s4, s13, 1\ns_lshl1_add_u32 s4, s4, s11\ns_lshl2_add_u32 s4, s12, s4\n"
"s_lshr_b32 s5, s71, 16\ns_cmp_lt_u32 s4, 28\ns_cselect_b32 s5, s71, s5\ns_cselect_b32 s3, 0, 28\ns_and_b32 s5, s5, 0x7ff\ns_sub_u32 s3, s4, s3\ns_mul_i32 s2, s4, 7168\n"
"s_add_u32 s2, s2, 0xc04c800\ns_add_u32 s6, s2, 4096\nbuffer_load_dwordx4 v[136:139], v1, s[16:19], s2 offen offset:0 sc0 sc1\n"
"buffer_load_dwordx4 v[140:143], v1, s[16:19], s2 offen offset:1024 sc0 sc1\nbuffer_load_dwordx4 v[144:147], v1, s[16:19], s2 offen offset:2048 sc0 sc1\n"
"buffer_load_dwordx4 v[148:151], v1, s[16:19], s2 offen offset:3072 sc0 sc1\nbuffer_load_dwordx4 v[152:155], v1, s[16:19], s6 offen offset:0 sc0 sc1\n"
"buffer_load_dwordx4 v[156:159], v1, s[16:19], s6 offen offset:1024 sc0 sc1\nbuffer_load_dwordx4 v[160:163], v1, s[16:19], s6 offen offset:2048 sc0 sc1\nv_lshrrev_b32 v167, 1, v1\n"
"v_and_b32 v167, 24, v167\ns_mul_i32 s2, s5, 896\ns_lshl_b32 s3, s3, 5\ns_add_u32 s2, s2, s3\ns_lshl2_add_u32 s5, s5, 0xc03e000\n"
"buffer_load_dwordx2 v[164:165], v167, s[28:31], s2 offen\nbuffer_load_dword v166, off, s[16:19], s5 sc0 sc1\ns_mov_b32 s90, 0\ns_swappc_b64 s[8:9], s[80:81]\nAD 136,136,140\n"
"AD 137,137,141\nAD 138,138,142\nAD 139,139,143\nAD 144,144,148\nAD 145,145,149\nAD 146,146,150\nAD 147,147,151\nAD 152,152,156\nAD 153,153,157\nAD 154,154,158\nAD 155,155,159\n"
"AD 136,136,144\nAD 137,137,145\nAD 138,138,146\nAD 139,139,147\nAD 152,152,160\nAD 153,153,161\nAD 154,154,162\nAD 155,155,163\nAD 136,136,152\nAD 137,137,153\nAD 138,138,154\n"
"AD 139,139,155\nv_mov_b32 v140, v136\nv_mov_b32 v141, v137\nv_mov_b32 v142, v138\nv_mov_b32 v143, v139\nv_permlane32_swap_b32 v136, v140\nv_permlane32_swap_b32 v137, v141\n"
"v_permlane32_swap_b32 v138, v142\nv_permlane32_swap_b32 v139, v143\nAD 136,136,140\nAD 137,137,141\nAD 138,138,142\nAD 139,139,143\nv_mov_b32 v140, v136\nv_mov_b32 v141, v137\n"
"v_mov_b32 v142, v138\nv_mov_b32 v143, v139\nv_permlane16_swap_b32 v136, v140\nv_permlane16_swap_b32 v137, v141\nv_permlane16_swap_b32 v138, v142\nv_permlane16_swap_b32 v139, v143\n"
"AD 136,136,140\nAD 137,137,141\nAD 138,138,142\nAD 139,139,143\nv_add_f32_dpp v136, v136, v136 row_ror:8 row_mask:0xf bank_mask:0xf\n"
"v_add_f32_dpp v137, v137, v137 row_ror:8 row_mask:0xf bank_mask:0xf\nv_add_f32_dpp v138, v138, v138 row_ror:8 row_mask:0xf bank_mask:0xf\n"
"v_add_f32_dpp v139, v139, v139 row_ror:8 row_mask:0xf bank_mask:0xf\nv_add_f32_dpp v136, v136, v136 row_ror:4 row_mask:0xf bank_mask:0xf\n"
"v_add_f32_dpp v137, v137, v137 row_ror:4 row_mask:0xf bank_mask:0xf\nv_add_f32_dpp v138, v138, v138 row_ror:4 row_mask:0xf bank_mask:0xf\n"
"v_add_f32_dpp v139, v139, v139 row_ror:4 row_mask:0xf bank_mask:0xf\nv_mul_f32 v166, 0xbfb8aa3b, v166\nEX 166,166\nCV 136,136,137\nv_add_f32 v166, 1.0, v166\nv_rcp_f32 v166, v166\n"
"CV 138,138,139\nCV 166,166,166\nv_lshlrev_b32 v166, 16, v166\nv_and_b32 v137, 0xffff0000, v136\nv_lshlrev_b32 v136, 16, v136\nv_and_b32 v139, 0xffff0000, v138\n"
"v_lshlrev_b32 v138, 16, v138\nMU 136,136,166\nMU 137,137,166\nMU 138,138,166\nMU 139,139,166\nCV 136,136,137\nCV 138,138,139\nv_and_b32 v137, 0xffff0000, v136\n"
"v_lshlrev_b32 v136, 16, v136\nv_and_b32 v139, 0xffff0000, v138\nv_lshlrev_b32 v138, 16, v138\nv_lshlrev_b32 v140, 16, v164\nv_and_b32 v141, 0xffff0000, v164\n"
"v_lshlrev_b32 v142, 16, v165\nv_and_b32 v143, 0xffff0000, v165\nAD 136,136,140\nAD 137,137,141\nAD 138,138,142\nAD 139,139,143\nCV 136,136,137\nCV 137,138,139\n"
"v_mov_b32 v138, s15\nv_mov_b32 v139, s71\ns_and_b32 s4, s13, 1\ns_lshl1_add_u32 s4, s4, s11\ns_lshl2_add_u32 s4, s12, s4\ns_lshr_b32 s5, s71, 16\ns_cmp_lt_u32 s4, 28\n"
"s_cselect_b32 s5, s71, s5\ns_cselect_b32 s3, 0, 28\ns_and_b32 s5, s5, 0x7ff\ns_sub_u32 s3, s4, s3\nv_mov_b32 v140, 0x40000000\nv_cmp_gt_u32 vcc, 64, v1\ns_lshl_b32 s4, s4, 6\n"
"s_add_u32 s4, s4, 0xc04b800\nv_cndmask_b32 v140, v140, v1, vcc\nSv 136,140,16,4,0\ns_mov_b32 s90, 100\ns_swappc_b64 s[8:9], s[80:81]\n.T64:\ns_mov_b32 s62, 0\n"
"s_call_b64 s[80:81], .RB11\ns_branch .T65\n.T66:\ns_mov_b32 s62, 0\ns_call_b64 s[80:81], .RA10\ns_branch .T67\n.T70:\ns_mov_b32 s62, 0\ns_call_b64 s[80:81], .RB11\ns_branch .T71\n"
".T72:\ns_mov_b32 s62, 0\ns_call_b64 s[80:81], .RA10\ns_branch .T73\n.T75:\ns_mov_b32 s62, 0\ns_call_b64 s[80:81], .RB11\ns_branch .T76\n.T77:\ns_mov_b32 s62, 0\n"
"s_call_b64 s[80:81], .RA10\ns_branch .T78\n.S83:\ns_mov_b32 s7, 262144\n.S85:\nbuffer_load_dwordx4 v[100:103], v108, s[16:19], s65 offen offset:0 sc0 sc1\n"
"buffer_load_dwordx4 v[104:107], v108, s[16:19], s65 offen offset:256 sc0 sc1\nWV 0\nv_xor_b32 v109, s15, v102\nv_xor_b32 v110, s15, v106\nv_or_b32 v109, v109, v110\n"
"v_cmp_ne_u32 vcc, 0, v109\ns_nop 0\ns_cbranch_vccz .S84\ns_sleep 6\ns_sub_u32 s7, s7, 1\ns_cbranch_scc0 .S85\ns_branch .S84\n.D81:\nv_mov_b32 v130, 0\nv_mov_b32 v131, 0\n"
"s_mov_b32 s63, 0xff800000\ns_mov_b32 s67, 1\ns_branch .D82\n.T87:\ns_lshl3_add_u32 s62, s67, 2\ns_call_b64 s[80:81], .RB11\ns_cmp_eq_u32 s67, 0\ns_cbranch_scc1 .T88\nSU 20,100,88\n"
"v_min_f32 v20, 0, v20\nEX 20,20\ns_nop 0\nFM 86,101,20\nSU 21,104,89\nv_min_f32 v21, 0, v21\nEX 21,21\ns_nop 0\nFM 87,105,21\nAR 22,192\nv_and_b32 v23, 0xffff0000, v22\n"
"v_lshlrev_b32 v22, 16, v22\nMU 22,22,20\nMU 23,23,20\nCV 22,22,23\nAW 192,22\nAR 22,193\nv_and_b32 v23, 0xffff0000, v22\nv_lshlrev_b32 v22, 16, v22\nMU 22,22,20\nMU 23,23,20\n"
"CV 22,22,23\nAW 193,22\nAR 22,194\nv_and_b32 v23, 0xffff0000, v22\nv_lshlrev_b32 v22, 16, v22\nMU 22,22,20\nMU 23,23,20\nCV 22,22,23\nAW 194,22\nAR 22,195\n"
"v_and_b32 v23, 0xffff0000, v22\nv_lshlrev_b32 v22, 16, v22\nMU 22,22,20\nMU 23,23,20\nCV 22,22,23\nAW 195,22\nAR 22,196\nv_and_b32 v23, 0xffff0000, v22\n"
"v_lshlrev_b32 v22, 16, v22\nMU 22,22,21\nMU 23,23,21\nCV 22,22,23\nAW 196,22\nAR 22,197\nv_and_b32 v23, 0xffff0000, v22\nv_lshlrev_b32 v22, 16, v22\nMU 22,22,21\nMU 23,23,21\n"
"CV 22,22,23\nAW 197,22\nAR 22,198\nv_and_b32 v23, 0xffff0000, v22\nv_lshlrev_b32 v22, 16, v22\nMU 22,22,21\nMU 23,23,21\nCV 22,22,23\nAW 198,22\nAR 22,199\n"
"v_and_b32 v23, 0xffff0000, v22\nv_lshlrev_b32 v22, 16, v22\nMU 22,22,21\nMU 23,23,21\nCV 22,22,23\nAW 199,22\nAR 22,200\nv_and_b32 v23, 0xffff0000, v22\n"
"v_lshlrev_b32 v22, 16, v22\nMU 22,22,20\nMU 23,23,20\nCV 22,22,23\nAW 200,22\nAR 22,201\nv_and_b32 v23, 0xffff0000, v22\nv_lshlrev_b32 v22, 16, v22\nMU 22,22,20\nMU 23,23,20\n"
"CV 22,22,23\nAW 201,22\nAR 22,202\nv_and_b32 v23, 0xffff0000, v22\nv_lshlrev_b32 v22, 16, v22\nMU 22,22,20\nMU 23,23,20\nCV 22,22,23\nAW 202,22\nAR 22,203\n"
"v_and_b32 v23, 0xffff0000, v22\nv_lshlrev_b32 v22, 16, v22\nMU 22,22,20\nMU 23,23,20\nCV 22,22,23\nAW 203,22\nAR 22,204\nv_and_b32 v23, 0xffff0000, v22\n"
"v_lshlrev_b32 v22, 16, v22\nMU 22,22,21\nMU 23,23,21\nCV 22,22,23\nAW 204,22\nAR 22,205\nv_and_b32 v23, 0xffff0000, v22\nv_lshlrev_b32 v22, 16, v22\nMU 22,22,21\nMU 23,23,21\n"
"CV 22,22,23\nAW 205,22\nAR 22,206\nv_and_b32 v23, 0xffff0000, v22\nv_lshlrev_b32 v22, 16, v22\nMU 22,22,21\nMU 23,23,21\nCV 22,22,23\nAW 206,22\nAR 22,207\n"
"v_and_b32 v23, 0xffff0000, v22\nv_lshlrev_b32 v22, 16, v22\nMU 22,22,21\nMU 23,23,21\nCV 22,22,23\nAW 207,22\nAR 22,208\nv_and_b32 v23, 0xffff0000, v22\n"
"v_lshlrev_b32 v22, 16, v22\nMU 22,22,20\nMU 23,23,20\nCV 22,22,23\nAW 208,22\nAR 22,209\nv_and_b32 v23, 0xffff0000, v22\nv_lshlrev_b32 v22, 16, v22\nMU 22,22,20\nMU 23,23,20\n"
"CV 22,22,23\nAW 209,22\nAR 22,210\nv_and_b32 v23, 0xffff0000, v22\nv_lshlrev_b32 v22, 16, v22\nMU 22,22,20\nMU 23,23,20\nCV 22,22,23\nAW 210,22\nAR 22,211\n"
"v_and_b32 v23, 0xffff0000, v22\nv_lshlrev_b32 v22, 16, v22\nMU 22,22,20\nMU 23,23,20\nCV 22,22,23\nAW 211,22\nAR 22,212\nv_and_b32 v23, 0xffff0000, v22\n"
"v_lshlrev_b32 v22, 16, v22\nMU 22,22,21\nMU 23,23,21\nCV 22,22,23\nAW 212,22\nAR 22,213\nv_and_b32 v23, 0xffff0000, v22\nv_lshlrev_b32 v22, 16, v22\nMU 22,22,21\nMU 23,23,21\n"
"CV 22,22,23\nAW 213,22\nAR 22,214\nv_and_b32 v23, 0xffff0000, v22\nv_lshlrev_b32 v22, 16, v22\nMU 22,22,21\nMU 23,23,21\nCV 22,22,23\nAW 214,22\nAR 22,215\n"
"v_and_b32 v23, 0xffff0000, v22\nv_lshlrev_b32 v22, 16, v22\nMU 22,22,21\nMU 23,23,21\nCV 22,22,23\nAW 215,22\nAR 22,216\nv_and_b32 v23, 0xffff0000, v22\n"
"v_lshlrev_b32 v22, 16, v22\nMU 22,22,20\nMU 23,23,20\nCV 22,22,23\nAW 216,22\nAR 22,217\nv_and_b32 v23, 0xffff0000, v22\nv_lshlrev_b32 v22, 16, v22\nMU 22,22,20\nMU 23,23,20\n"
"CV 22,22,23\nAW 217,22\nAR 22,218\nv_and_b32 v23, 0xffff0000, v22\nv_lshlrev_b32 v22, 16, v22\nMU 22,22,20\nMU 23,23,20\nCV 22,22,23\nAW 218,22\nAR 22,219\n"
"v_and_b32 v23, 0xffff0000, v22\nv_lshlrev_b32 v22, 16, v22\nMU 22,22,20\nMU 23,23,20\nCV 22,22,23\nAW 219,22\nAR 22,220\nv_and_b32 v23, 0xffff0000, v22\n"
"v_lshlrev_b32 v22, 16, v22\nMU 22,22,21\nMU 23,23,21\nCV 22,22,23\nAW 220,22\nAR 22,221\nv_and_b32 v23, 0xffff0000, v22\nv_lshlrev_b32 v22, 16, v22\nMU 22,22,21\nMU 23,23,21\n"
"CV 22,22,23\nAW 221,22\nAR 22,222\nv_and_b32 v23, 0xffff0000, v22\nv_lshlrev_b32 v22, 16, v22\nMU 22,22,21\nMU 23,23,21\nCV 22,22,23\nAW 222,22\nAR 22,223\n"
"v_and_b32 v23, 0xffff0000, v22\nv_lshlrev_b32 v22, 16, v22\nMU 22,22,21\nMU 23,23,21\nCV 22,22,23\nAW 223,22\nAR 22,224\nv_and_b32 v23, 0xffff0000, v22\n"
"v_lshlrev_b32 v22, 16, v22\nMU 22,22,20\nMU 23,23,20\nCV 22,22,23\nAW 224,22\nAR 22,225\nv_and_b32 v23, 0xffff0000, v22\nv_lshlrev_b32 v22, 16, v22\nMU 22,22,20\nMU 23,23,20\n"
"CV 22,22,23\nAW 225,22\nAR 22,226\nv_and_b32 v23, 0xffff0000, v22\nv_lshlrev_b32 v22, 16, v22\nMU 22,22,20\nMU 23,23,20\nCV 22,22,23\nAW 226,22\nAR 22,227\n"
"v_and_b32 v23, 0xffff0000, v22\nv_lshlrev_b32 v22, 16, v22\nMU 22,22,20\nMU 23,23,20\nCV 22,22,23\nAW 227,22\nAR 22,228\nv_and_b32 v23, 0xffff0000, v22\n"
"v_lshlrev_b32 v22, 16, v22\nMU 22,22,21\nMU 23,23,21\nCV 22,22,23\nAW 228,22\nAR 22,229\nv_and_b32 v23, 0xffff0000, v22\nv_lshlrev_b32 v22, 16, v22\nMU 22,22,21\nMU 23,23,21\n"
"CV 22,22,23\nAW 229,22\nAR 22,230\nv_and_b32 v23, 0xffff0000, v22\nv_lshlrev_b32 v22, 16, v22\nMU 22,22,21\nMU 23,23,21\nCV 22,22,23\nAW 230,22\nAR 22,231\n"
"v_and_b32 v23, 0xffff0000, v22\nv_lshlrev_b32 v22, 16, v22\nMU 22,22,21\nMU 23,23,21\nCV 22,22,23\nAW 231,22\nAR 22,232\nv_and_b32 v23, 0xffff0000, v22\n"
"v_lshlrev_b32 v22, 16, v22\nMU 22,22,20\nMU 23,23,20\nCV 22,22,23\nAW 232,22\nAR 22,233\nv_and_b32 v23, 0xffff0000, v22\nv_lshlrev_b32 v22, 16, v22\nMU 22,22,20\nMU 23,23,20\n"
"CV 22,22,23\nAW 233,22\nAR 22,234\nv_and_b32 v23, 0xffff0000, v22\nv_lshlrev_b32 v22, 16, v22\nMU 22,22,20\nMU 23,23,20\nCV 22,22,23\nAW 234,22\nAR 22,235\n"
"v_and_b32 v23, 0xffff0000, v22\nv_lshlrev_b32 v22, 16, v22\nMU 22,22,20\nMU 23,23,20\nCV 22,22,23\nAW 235,22\nAR 22,236\nv_and_b32 v23, 0xffff0000, v22\n"
"v_lshlrev_b32 v22, 16, v22\nMU 22,22,21\nMU 23,23,21\nCV 22,22,23\nAW 236,22\nAR 22,237\nv_and_b32 v23, 0xffff0000, v22\nv_lshlrev_b32 v22, 16, v22\nMU 22,22,21\nMU 23,23,21\n"
"CV 22,22,23\nAW 237,22\nAR 22,238\nv_and_b32 v23, 0xffff0000, v22\nv_lshlrev_b32 v22, 16, v22\nMU 22,22,21\nMU 23,23,21\nCV 22,22,23\nAW 238,22\nAR 22,239\n"
"v_and_b32 v23, 0xffff0000, v22\nv_lshlrev_b32 v22, 16, v22\nMU 22,22,21\nMU 23,23,21\nCV 22,22,23\nAW 239,22\nAR 22,240\nv_and_b32 v23, 0xffff0000, v22\n"
"v_lshlrev_b32 v22, 16, v22\nMU 22,22,20\nMU 23,23,20\nCV 22,22,23\nAW 240,22\nAR 22,241\nv_and_b32 v23, 0xffff0000, v22\nv_lshlrev_b32 v22, 16, v22\nMU 22,22,20\nMU 23,23,20\n"
"CV 22,22,23\nAW 241,22\nAR 22,242\nv_and_b32 v23, 0xffff0000, v22\nv_lshlrev_b32 v22, 16, v22\nMU 22,22,20\nMU 23,23,20\nCV 22,22,23\nAW 242,22\nAR 22,243\n"
"v_and_b32 v23, 0xffff0000, v22\nv_lshlrev_b32 v22, 16, v22\nMU 22,22,20\nMU 23,23,20\nCV 22,22,23\nAW 243,22\nAR 22,244\nv_and_b32 v23, 0xffff0000, v22\n"
"v_lshlrev_b32 v22, 16, v22\nMU 22,22,21\nMU 23,23,21\nCV 22,22,23\nAW 244,22\nAR 22,245\nv_and_b32 v23, 0xffff0000, v22\nv_lshlrev_b32 v22, 16, v22\nMU 22,22,21\nMU 23,23,21\n"
"CV 22,22,23\nAW 245,22\nAR 22,246\nv_and_b32 v23, 0xffff0000, v22\nv_lshlrev_b32 v22, 16, v22\nMU 22,22,21\nMU 23,23,21\nCV 22,22,23\nAW 246,22\nAR 22,247\n"
"v_and_b32 v23, 0xffff0000, v22\nv_lshlrev_b32 v22, 16, v22\nMU 22,22,21\nMU 23,23,21\nCV 22,22,23\nAW 247,22\nAR 22,248\nv_and_b32 v23, 0xffff0000, v22\n"
"v_lshlrev_b32 v22, 16, v22\nMU 22,22,20\nMU 23,23,20\nCV 22,22,23\nAW 248,22\nAR 22,249\nv_and_b32 v23, 0xffff0000, v22\nv_lshlrev_b32 v22, 16, v22\nMU 22,22,20\nMU 23,23,20\n"
"CV 22,22,23\nAW 249,22\nAR 22,250\nv_and_b32 v23, 0xffff0000, v22\nv_lshlrev_b32 v22, 16, v22\nMU 22,22,20\nMU 23,23,20\nCV 22,22,23\nAW 250,22\nAR 22,251\n"
"v_and_b32 v23, 0xffff0000, v22\nv_lshlrev_b32 v22, 16, v22\nMU 22,22,20\nMU 23,23,20\nCV 22,22,23\nAW 251,22\nAR 22,252\nv_and_b32 v23, 0xffff0000, v22\n"
"v_lshlrev_b32 v22, 16, v22\nMU 22,22,21\nMU 23,23,21\nCV 22,22,23\nAW 252,22\nAR 22,253\nv_and_b32 v23, 0xffff0000, v22\nv_lshlrev_b32 v22, 16, v22\nMU 22,22,21\nMU 23,23,21\n"
"CV 22,22,23\nAW 253,22\nAR 22,254\nv_and_b32 v23, 0xffff0000, v22\nv_lshlrev_b32 v22, 16, v22\nMU 22,22,21\nMU 23,23,21\nCV 22,22,23\nAW 254,22\nAR 22,255\n"
"v_and_b32 v23, 0xffff0000, v22\nv_lshlrev_b32 v22, 16, v22\nMU 22,22,21\nMU 23,23,21\nCV 22,22,23\nAW 255,22\nv_lshrrev_b32 v22, 8, v1\nv_bfe_u32 v23, v1, 6, 2\n"
"v_cmp_eq_u32 vcc, v22, v23\nv_and_b32 v22, 16, v1\nv_mov_b32 v23, 0x3f80\nv_cndmask_b32 v23, 0, v23, vcc\nv_lshlrev_b32 v23, v22, v23\nv_and_b32 v22, 32, v1\n"
"v_cmp_eq_u32 vcc, 0, v22\nv_mov_b32 v128, 0\nv_mov_b32 v129, 0\nv_cndmask_b32 v130, 0, v23, vcc\nv_sub_u32 v131, v23, v130\nv_mov_b32 v132, 0\nv_mov_b32 v133, 0\ns_nop 1\n"
"Mava 0,130,192\nMava 8,128,192\nMava 12,128,196\nMava 4,130,196\nMava 16,130,200\nMava 24,128,200\nMava 28,128,204\nMava 20,130,204\nMava 32,130,208\nMava 40,128,208\n"
"Mava 44,128,212\nMava 36,130,212\nMava 48,130,216\nMava 56,128,216\nMava 60,128,220\nMava 52,130,220\nMava 64,130,224\nMava 72,128,224\nMava 76,128,228\nMava 68,130,228\n"
"Mava 80,130,232\nMava 88,128,232\nMava 92,128,236\nMava 84,130,236\nMava 96,130,240\nMava 104,128,240\nMava 108,128,244\nMava 100,130,244\nMava 112,130,248\nMava 120,128,248\n"
"Mava 124,128,252\nMava 116,130,252\ns_nop 7\ns_branch .T88\n.S90:\ns_mov_b32 s7, 262144\n.S92:\ns_add_u32 s0, s92, 0\n"
"buffer_load_dwordx4 v[208:211], v1, s[16:19], s0 offen offset:0 sc0 sc1\nbuffer_load_dwordx4 v[212:215], v1, s[16:19], s0 offen offset:1024 sc0 sc1\nWV 0\n"
"v_max_u32 v21, v211, v215\nv_cmp_eq_u32 vcc, -1, v21\ns_nop 0\ns_cbranch_vccz .S91\ns_sleep 6\ns_sub_u32 s7, s7, 1\ns_cbranch_scc0 .S92\ns_branch .S91\n.S93:\n"
"s_mov_b32 s7, 262144\n.S95:\ns_add_u32 s0, s92, 131072\nbuffer_load_dwordx4 v[216:219], v1, s[16:19], s0 offen offset:0 sc0 sc1\n"
"buffer_load_dwordx4 v[220:223], v1, s[16:19], s0 offen offset:1024 sc0 sc1\nWV 0\nv_max_u32 v21, v219, v223\nv_cmp_eq_u32 vcc, -1, v21\ns_nop 0\ns_cbranch_vccz .S94\ns_sleep 6\n"
"s_sub_u32 s7, s7, 1\ns_cbranch_scc0 .S95\ns_branch .S94\n.S96:\ns_mov_b32 s7, 262144\n.S98:\ns_add_u32 s0, s92, 262144\n"
"buffer_load_dwordx4 v[224:227], v1, s[16:19], s0 offen offset:0 sc0 sc1\nbuffer_load_dwordx4 v[228:231], v1, s[16:19], s0 offen offset:1024 sc0 sc1\nWV 0\n"
"v_max_u32 v21, v227, v231\nv_cmp_eq_u32 vcc, -1, v21\ns_nop 0\ns_cbranch_vccz .S97\ns_sleep 6\ns_sub_u32 s7, s7, 1\ns_cbranch_scc0 .S98\ns_branch .S97\n.S99:\n"
"s_mov_b32 s7, 262144\n.S101:\ns_add_u32 s0, s92, 393216\nbuffer_load_dwordx4 v[208:211], v1, s[16:19], s0 offen offset:0 sc0 sc1\n"
"buffer_load_dwordx4 v[212:215], v1, s[16:19], s0 offen offset:1024 sc0 sc1\nWV 0\nv_max_u32 v21, v211, v215\nv_cmp_eq_u32 vcc, -1, v21\ns_nop 0\ns_cbranch_vccz .S100\ns_sleep 6\n"
"s_sub_u32 s7, s7, 1\ns_cbranch_scc0 .S101\ns_branch .S100\n.S102:\ns_mov_b32 s7, 262144\n.S104:\ns_add_u32 s0, s92, 524288\n"
"buffer_load_dwordx4 v[216:219], v1, s[16:19], s0 offen offset:0 sc0 sc1\nbuffer_load_dwordx4 v[220:223], v1, s[16:19], s0 offen offset:1024 sc0 sc1\nWV 0\n"
"v_max_u32 v21, v219, v223\nv_cmp_eq_u32 vcc, -1, v21\ns_nop 0\ns_cbranch_vccz .S103\ns_sleep 6\ns_sub_u32 s7, s7, 1\ns_cbranch_scc0 .S104\ns_branch .S103\n.S105:\n"
"s_mov_b32 s7, 262144\n.S107:\ns_add_u32 s0, s92, 655360\nbuffer_load_dwordx4 v[224:227], v1, s[16:19], s0 offen offset:0 sc0 sc1\n"
"buffer_load_dwordx4 v[228:231], v1, s[16:19], s0 offen offset:1024 sc0 sc1\nWV 0\nv_max_u32 v21, v227, v231\nv_cmp_eq_u32 vcc, -1, v21\ns_nop 0\ns_cbranch_vccz .S106\ns_sleep 6\n"
"s_sub_u32 s7, s7, 1\ns_cbranch_scc0 .S107\ns_branch .S106\n.S108:\ns_mov_b32 s7, 262144\n.S110:\ns_add_u32 s0, s92, 786432\n"
"buffer_load_dwordx4 v[208:211], v1, s[16:19], s0 offen offset:0 sc0 sc1\nbuffer_load_dwordx4 v[212:215], v1, s[16:19], s0 offen offset:1024 sc0 sc1\nWV 0\n"
"v_max_u32 v21, v211, v215\nv_cmp_eq_u32 vcc, -1, v21\ns_nop 0\ns_cbranch_vccz .S109\ns_sleep 6\ns_sub_u32 s7, s7, 1\ns_cbranch_scc0 .S110\ns_branch .S109\n.S111:\n"
"s_mov_b32 s7, 262144\n.S113:\nbuffer_load_dwordx4 v[64:67], v89, s[16:19], s40 offen offset:0 sc0 sc1\nbuffer_load_dwordx4 v[68:71], v89, s[16:19], s40 offen offset:16 sc0 sc1\n"
"buffer_load_dwordx4 v[72:75], v89, s[16:19], s40 offen offset:1792 sc0 sc1\nbuffer_load_dwordx4 v[76:79], v89, s[16:19], s40 offen offset:1808 sc0 sc1\n"
"buffer_load_dwordx2 v[80:81], v88, s[16:19], s63 offen offset:0 sc0 sc1\nbuffer_load_dwordx2 v[82:83], v88, s[16:19], s63 offen offset:512 sc0 sc1\nWV 0\nv_xor_b32 v66, s15, v66\n"
"v_xor_b32 v70, s15, v70\nv_xor_b32 v74, s15, v74\nv_xor_b32 v78, s15, v78\nv_xor_b32 v81, s15, v81\nv_xor_b32 v83, s15, v83\nv_or3_b32 v92, v66, v70, v74\n"
"v_or3_b32 v92, v92, v78, v81\nv_or_b32 v92, v92, v83\nv_cmp_ne_u32 vcc, 0, v92\ns_nop 0\ns_cbranch_vccz .S112\ns_sleep 6\ns_sub_u32 s7, s7, 1\ns_cbranch_scc0 .S113\n"
"s_branch .S112\n.S114:\ns_mov_b32 s7, 262144\n.S116:\nbuffer_load_dword v27, off, s[16:19], s60 offset:512 sc0 sc1\nWV 0\nv_cmp_ne_u32 vcc, s15, v27\ns_nop 0\n"
"s_cbranch_vccz .S115\ns_sleep 6\ns_sub_u32 s7, s7, 1\ns_cbranch_scc0 .S116\ns_branch .S115\n.S117:\ns_mov_b32 s7, 262144\n.S119:\n"
"buffer_load_dword v27, v24, s[16:19], s41 offen sc0 sc1\nWV 0\nv_xor_b32 v27, s15, v27\nv_and_b32 v26, 0xffffdfff, v27\nv_cmp_ne_u32 s[64:65], 0, v27\nv_cmp_ne_u32 vcc, 0, v26\n"
"s_nop 0\ns_cbranch_vccz .S118\ns_sleep 6\ns_sub_u32 s7, s7, 1\ns_cbranch_scc0 .S119\ns_branch .S118\n.Z121:\ns_sub_u32 s7, s7, 1\ns_cbranch_scc1 .Z122\ns_cmp_lg_u64 s[44:45], 0\n"
"s_cbranch_scc0 .Z124\nbuffer_load_dwordx4 v[96:99], v1, s[16:19], s24 offen offset:0 sc0 sc1\nbuffer_load_dwordx4 v[100:103], v1, s[16:19], s24 offen offset:1024 sc0 sc1\n.Z124:\n"
"s_cmp_lg_u64 s[46:47], 0\ns_cbranch_scc0 .Z125\nbuffer_load_dwordx4 v[104:107], v1, s[16:19], s25 offen offset:0 sc0 sc1\n"
"buffer_load_dwordx4 v[108:111], v1, s[16:19], s25 offen offset:1024 sc0 sc1\n.Z125:\ns_cmp_lg_u64 s[48:49], 0\ns_cbranch_scc0 .Z126\n"
"buffer_load_dwordx4 v[112:115], v1, s[16:19], s26 offen offset:0 sc0 sc1\nbuffer_load_dwordx4 v[116:119], v1, s[16:19], s26 offen offset:1024 sc0 sc1\n.Z126:\n"
"s_cmp_lg_u64 s[50:51], 0\ns_cbranch_scc0 .Z127\nbuffer_load_dwordx4 v[120:123], v1, s[16:19], s27 offen offset:0 sc0 sc1\n"
"buffer_load_dwordx4 v[124:127], v1, s[16:19], s27 offen offset:1024 sc0 sc1\n.Z127:\ns_cmp_lg_u64 s[52:53], 0\ns_cbranch_scc0 .Z128\n"
"buffer_load_dwordx4 v[32:35], v1, s[16:19], s32 offen offset:0 sc0 sc1\nbuffer_load_dwordx4 v[36:39], v1, s[16:19], s32 offen offset:1024 sc0 sc1\n.Z128:\n"
"s_cmp_lg_u64 s[54:55], 0\ns_cbranch_scc0 .Z129\nbuffer_load_dwordx4 v[40:43], v1, s[16:19], s33 offen offset:0 sc0 sc1\n"
"buffer_load_dwordx4 v[44:47], v1, s[16:19], s33 offen offset:1024 sc0 sc1\n.Z129:\ns_cmp_lg_u64 s[56:57], 0\ns_cbranch_scc0 .Z130\n"
"buffer_load_dwordx4 v[48:51], v1, s[16:19], s34 offen offset:0 sc0 sc1\nbuffer_load_dwordx4 v[52:55], v1, s[16:19], s34 offen offset:1024 sc0 sc1\n.Z130:\n"
"s_cmp_lg_u64 s[58:59], 0\ns_cbranch_scc0 .Z131\nbuffer_load_dwordx4 v[56:59], v1, s[16:19], s35 offen offset:0 sc0 sc1\n"
"buffer_load_dwordx4 v[60:63], v1, s[16:19], s35 offen offset:1024 sc0 sc1\n.Z131:\ns_sleep 8\nWV 0\ns_branch .Z123\n.RA10:\ns_nop 7\ns_nop 3\nv_max3_f32 v22, v32, v33, v34\n"
"v_max3_f32 v22, v22, v35, v40\nv_max3_f32 v22, v22, v41, v42\nv_max_f32 v22, v22, v43\nv_mov_b32 v91, v22\ns_nop 1\nv_permlane32_swap_b32 v22, v91\nv_max_f32 v22, v22, v91\n"
"v_mov_b32 v91, v22\ns_nop 1\nv_permlane16_swap_b32 v22, v91\nv_max_f32 v22, v22, v91\nv_ceil_f32 v22, v22\nv_max3_f32 v23, v36, v37, v38\nv_max3_f32 v23, v23, v39, v44\n"
"v_max3_f32 v23, v23, v45, v46\nv_max_f32 v23, v23, v47\nv_mov_b32 v91, v23\ns_nop 1\nv_permlane32_swap_b32 v23, v91\nv_max_f32 v23, v23, v91\nv_mov_b32 v91, v23\ns_nop 1\n"
"v_permlane16_swap_b32 v23, v91\nv_max_f32 v23, v23, v91\nv_ceil_f32 v23, v23\ns_bitcmp1_b32 s62, 0\ns_cbranch_scc0 .R133\nv_cmp_lt_f32 vcc, 0xff000000, v22\ns_nop 1\n"
"v_cndmask_b32 v22, 0, v22, vcc\nv_cmp_lt_f32 vcc, 0xff000000, v23\ns_nop 1\nv_cndmask_b32 v23, 0, v23, vcc\ns_branch .R134\n.R133:\nv_max_f32 v22, 0, v22\nv_max_f32 v23, 0, v23\n"
"s_bitcmp1_b32 s62, 3\ns_cbranch_scc0 .R134\nv_max_f32 v22, v20, v22\nv_max_f32 v23, v21, v23\n.R134:\nAD 88,88,22\nAD 89,89,23\nSU 32,32,22\nSU 33,33,22\nSU 34,34,22\nSU 35,35,22\n"
"SU 36,36,23\nSU 37,37,23\nSU 38,38,23\nSU 39,39,23\nSU 40,40,22\nSU 41,41,22\nSU 42,42,22\nSU 43,43,22\nSU 44,44,23\nSU 45,45,23\nSU 46,46,23\nSU 47,47,23\nv_mov_b32 v90, v80\n"
"v_mov_b32 v91, v81\nEX 80,32\nEX 81,33\ns_nop 0\nAD 84,80,81\nCV 64,80,81\nEX 82,34\nEX 83,35\nAD 84,84,82\nAD 84,84,83\nCV 65,82,83\nEX 80,36\nEX 81,37\ns_nop 0\nAD 85,80,81\n"
"CV 68,80,81\nEX 82,38\nEX 83,39\nAD 85,85,82\nAD 85,85,83\nCV 69,82,83\nEX 80,40\nEX 81,41\nAD 84,84,80\nAD 84,84,81\nCV 66,80,81\nEX 82,42\nEX 83,43\nAD 84,84,82\nAD 84,84,83\n"
"CV 67,82,83\nEX 80,44\nEX 81,45\nAD 85,85,80\nAD 85,85,81\nCV 70,80,81\nEX 82,46\nEX 83,47\nAD 85,85,82\nAD 85,85,83\nCV 71,82,83\nv_mov_b32 v80, v90\nv_mov_b32 v81, v91\n"
"s_bitcmp1_b32 s62, 1\ns_cbranch_scc1 .R135\nSU 48,48,22\nSU 49,49,22\nSU 50,50,22\nSU 51,51,22\nSU 52,52,23\nSU 53,53,23\nSU 54,54,23\nSU 55,55,23\nSU 56,56,22\nSU 57,57,22\n"
"SU 58,58,22\nSU 59,59,22\nSU 60,60,23\nSU 61,61,23\nSU 62,62,23\nSU 63,63,23\nSU 4,4,22\nSU 5,5,22\nSU 6,6,22\nSU 7,7,22\nSU 8,8,23\nSU 9,9,23\nSU 10,10,23\nSU 11,11,23\n"
"SU 12,12,22\nSU 13,13,22\nSU 14,14,22\nSU 15,15,22\nSU 16,16,23\nSU 17,17,23\nSU 18,18,23\nSU 19,19,23\nEX 80,48\nEX 81,49\n.R135:\ns_bitcmp1_b32 s62, 0\ns_cbranch_scc0 .RS12\n"
"s_nop 1\ns_setpc_b64 s[80:81]\n.RB11:\ns_nop 7\ns_nop 3\nv_max3_f32 v22, v48, v49, v50\nv_max3_f32 v22, v22, v51, v56\nv_max3_f32 v22, v22, v57, v58\nv_max_f32 v22, v22, v59\n"
"v_mov_b32 v91, v22\ns_nop 1\nv_permlane32_swap_b32 v22, v91\nv_max_f32 v22, v22, v91\nv_mov_b32 v91, v22\ns_nop 1\nv_permlane16_swap_b32 v22, v91\nv_max_f32 v22, v22, v91\n"
"v_ceil_f32 v22, v22\nv_max3_f32 v23, v52, v53, v54\nv_max3_f32 v23, v23, v55, v60\nv_max3_f32 v23, v23, v61, v62\nv_max_f32 v23, v23, v63\nv_mov_b32 v91, v23\ns_nop 1\n"
"v_permlane32_swap_b32 v23, v91\nv_max_f32 v23, v23, v91\nv_mov_b32 v91, v23\ns_nop 1\nv_permlane16_swap_b32 v23, v91\nv_max_f32 v23, v23, v91\nv_ceil_f32 v23, v23\n"
"s_bitcmp1_b32 s62, 0\ns_cbranch_scc0 .R138\nv_cmp_lt_f32 vcc, 0xff000000, v22\nv_cndmask_b32 v22, 0, v22, vcc\nv_cmp_lt_f32 vcc, 0xff000000, v23\nv_cndmask_b32 v23, 0, v23, vcc\n"
"s_branch .R139\n.R138:\nv_max_f32 v22, 0, v22\nv_max_f32 v23, 0, v23\ns_bitcmp1_b32 s62, 3\ns_cbranch_scc0 .R139\nv_max_f32 v22, v20, v22\nv_max_f32 v23, v21, v23\n.R139:\n"
"AD 88,88,22\nAD 89,89,23\nSU 48,48,22\nSU 49,49,22\nSU 50,50,22\nSU 51,51,22\nSU 52,52,23\nSU 53,53,23\nSU 54,54,23\nSU 55,55,23\nSU 56,56,22\nSU 57,57,22\nSU 58,58,22\n"
"SU 59,59,22\nSU 60,60,23\nSU 61,61,23\nSU 62,62,23\nSU 63,63,23\nv_mov_b32 v90, v80\nv_mov_b32 v91, v81\nEX 80,48\nEX 81,49\ns_nop 0\nAD 84,80,81\nCV 72,80,81\nEX 82,50\nEX 83,51\n"
"AD 84,84,82\nAD 84,84,83\nCV 73,82,83\nEX 80,52\nEX 81,53\ns_nop 0\nAD 85,80,81\nCV 76,80,81\nEX 82,54\nEX 83,55\nAD 85,85,82\nAD 85,85,83\nCV 77,82,83\nEX 80,56\nEX 81,57\n"
"AD 84,84,80\nAD 84,84,81\nCV 74,80,81\nEX 82,58\nEX 83,59\nAD 84,84,82\nAD 84,84,83\nCV 75,82,83\nEX 80,60\nEX 81,61\nAD 85,85,80\nAD 85,85,81\nCV 78,80,81\nEX 82,62\nEX 83,63\n"
"AD 85,85,82\nAD 85,85,83\nCV 79,82,83\nv_mov_b32 v80, v90\nv_mov_b32 v81, v91\ns_bitcmp1_b32 s62, 1\ns_cbranch_scc1 .R140\nSU 32,32,22\nSU 33,33,22\nSU 34,34,22\nSU 35,35,22\n"
"SU 36,36,23\nSU 37,37,23\nSU 38,38,23\nSU 39,39,23\nSU 40,40,22\nSU 41,41,22\nSU 42,42,22\nSU 43,43,22\nSU 44,44,23\nSU 45,45,23\nSU 46,46,23\nSU 47,47,23\nSU 4,4,22\nSU 5,5,22\n"
"SU 6,6,22\nSU 7,7,22\nSU 8,8,23\nSU 9,9,23\nSU 10,10,23\nSU 11,11,23\nSU 12,12,22\nSU 13,13,22\nSU 14,14,22\nSU 15,15,22\nSU 16,16,23\nSU 17,17,23\nSU 18,18,23\nSU 19,19,23\n"
"EX 80,32\nEX 81,33\n.R140:\ns_bitcmp1_b32 s62, 0\ns_cbranch_scc0 .RS12\ns_nop 1\ns_setpc_b64 s[80:81]\n.RS12:\nv_exp_f32 v22, -v22\nv_exp_f32 v23, -v23\nMU 86,86,22\nMU 87,87,23\n"
"AR 91,0\nMU 91,91,22\nAW 0,91\nAR 91,1\nMU 91,91,22\nAW 1,91\nAR 91,2\nMU 91,91,22\nAW 2,91\nAR 91,3\nMU 91,91,22\nAW 3,91\nAR 91,4\nMU 91,91,23\nAW 4,91\nAR 91,5\nMU 91,91,23\n"
"AW 5,91\nAR 91,6\nMU 91,91,23\nAW 6,91\nAR 91,7\nMU 91,91,23\nAW 7,91\nAR 91,8\nMU 91,91,22\nAW 8,91\nAR 91,9\nMU 91,91,22\nAW 9,91\nAR 91,10\nMU 91,91,22\nAW 10,91\nAR 91,11\n"
"MU 91,91,22\nAW 11,91\nAR 91,12\nMU 91,91,23\nAW 12,91\nAR 91,13\nMU 91,91,23\nAW 13,91\nAR 91,14\nMU 91,91,23\nAW 14,91\nAR 91,15\nMU 91,91,23\nAW 15,91\nAR 91,16\nMU 91,91,22\n"
"AW 16,91\nAR 91,17\nMU 91,91,22\nAW 17,91\nAR 91,18\nMU 91,91,22\nAW 18,91\nAR 91,19\nMU 91,91,22\nAW 19,91\nAR 91,20\nMU 91,91,23\nAW 20,91\nAR 91,21\nMU 91,91,23\nAW 21,91\n"
"AR 91,22\nMU 91,91,23\nAW 22,91\nAR 91,23\nMU 91,91,23\nAW 23,91\nAR 91,24\nMU 91,91,22\nAW 24,91\nAR 91,25\nMU 91,91,22\nAW 25,91\nAR 91,26\nMU 91,91,22\nAW 26,91\nAR 91,27\n"
"MU 91,91,22\nAW 27,91\nAR 91,28\nMU 91,91,23\nAW 28,91\nAR 91,29\nMU 91,91,23\nAW 29,91\nAR 91,30\nMU 91,91,23\nAW 30,91\nAR 91,31\nMU 91,91,23\nAW 31,91\nAR 91,32\nMU 91,91,22\n"
"AW 32,91\nAR 91,33\nMU 91,91,22\nAW 33,91\nAR 91,34\nMU 91,91,22\nAW 34,91\nAR 91,35\nMU 91,91,22\nAW 35,91\nAR 91,36\nMU 91,91,23\nAW 36,91\nAR 91,37\nMU 91,91,23\nAW 37,91\n"
"AR 91,38\nMU 91,91,23\nAW 38,91\nAR 91,39\nMU 91,91,23\nAW 39,91\nAR 91,40\nMU 91,91,22\nAW 40,91\nAR 91,41\nMU 91,91,22\nAW 41,91\nAR 91,42\nMU 91,91,22\nAW 42,91\nAR 91,43\n"
"MU 91,91,22\nAW 43,91\nAR 91,44\nMU 91,91,23\nAW 44,91\nAR 91,45\nMU 91,91,23\nAW 45,91\nAR 91,46\nMU 91,91,23\nAW 46,91\nAR 91,47\nMU 91,91,23\nAW 47,91\nAR 91,48\nMU 91,91,22\n"
"AW 48,91\nAR 91,49\nMU 91,91,22\nAW 49,91\nAR 91,50\nMU 91,91,22\nAW 50,91\nAR 91,51\nMU 91,91,22\nAW 51,91\nAR 91,52\nMU 91,91,23\nAW 52,91\nAR 91,53\nMU 91,91,23\nAW 53,91\n"
"AR 91,54\nMU 91,91,23\nAW 54,91\nAR 91,55\nMU 91,91,23\nAW 55,91\nAR 91,56\nMU 91,91,22\nAW 56,91\nAR 91,57\nMU 91,91,22\nAW 57,91\nAR 91,58\nMU 91,91,22\nAW 58,91\nAR 91,59\n"
"MU 91,91,22\nAW 59,91\nAR 91,60\nMU 91,91,23\nAW 60,91\nAR 91,61\nMU 91,91,23\nAW 61,91\nAR 91,62\nMU 91,91,23\nAW 62,91\nAR 91,63\nMU 91,91,23\nAW 63,91\nAR 91,64\nMU 91,91,22\n"
"AW 64,91\nAR 91,65\nMU 91,91,22\nAW 65,91\nAR 91,66\nMU 91,91,22\nAW 66,91\nAR 91,67\nMU 91,91,22\nAW 67,91\nAR 91,68\nMU 91,91,23\nAW 68,91\nAR 91,69\nMU 91,91,23\nAW 69,91\n"
"AR 91,70\nMU 91,91,23\nAW 70,91\nAR 91,71\nMU 91,91,23\nAW 71,91\nAR 91,72\nMU 91,91,22\nAW 72,91\nAR 91,73\nMU 91,91,22\nAW 73,91\nAR 91,74\nMU 91,91,22\nAW 74,91\nAR 91,75\n"
"MU 91,91,22\nAW 75,91\nAR 91,76\nMU 91,91,23\nAW 76,91\nAR 91,77\nMU 91,91,23\nAW 77,91\nAR 91,78\nMU 91,91,23\nAW 78,91\nAR 91,79\nMU 91,91,23\nAW 79,91\nAR 91,80\nMU 91,91,22\n"
"AW 80,91\nAR 91,81\nMU 91,91,22\nAW 81,91\nAR 91,82\nMU 91,91,22\nAW 82,91\nAR 91,83\nMU 91,91,22\nAW 83,91\nAR 91,84\nMU 91,91,23\nAW 84,91\nAR 91,85\nMU 91,91,23\nAW 85,91\n"
"AR 91,86\nMU 91,91,23\nAW 86,91\nAR 91,87\nMU 91,91,23\nAW 87,91\nAR 91,88\nMU 91,91,22\nAW 88,91\nAR 91,89\nMU 91,91,22\nAW 89,91\nAR 91,90\nMU 91,91,22\nAW 90,91\nAR 91,91\n"
"MU 91,91,22\nAW 91,91\nAR 91,92\nMU 91,91,23\nAW 92,91\nAR 91,93\nMU 91,91,23\nAW 93,91\nAR 91,94\nMU 91,91,23\nAW 94,91\nAR 91,95\nMU 91,91,23\nAW 95,91\nAR 91,96\nMU 91,91,22\n"
"AW 96,91\nAR 91,97\nMU 91,91,22\nAW 97,91\nAR 91,98\nMU 91,91,22\nAW 98,91\nAR 91,99\nMU 91,91,22\nAW 99,91\nAR 91,100\nMU 91,91,23\nAW 100,91\nAR 91,101\nMU 91,91,23\nAW 101,91\n"
"AR 91,102\nMU 91,91,23\nAW 102,91\nAR 91,103\nMU 91,91,23\nAW 103,91\nAR 91,104\nMU 91,91,22\nAW 104,91\nAR 91,105\nMU 91,91,22\nAW 105,91\nAR 91,106\nMU 91,91,22\nAW 106,91\n"
"AR 91,107\nMU 91,91,22\nAW 107,91\nAR 91,108\nMU 91,91,23\nAW 108,91\nAR 91,109\nMU 91,91,23\nAW 109,91\nAR 91,110\nMU 91,91,23\nAW 110,91\nAR 91,111\nMU 91,91,23\nAW 111,91\n"
"AR 91,112\nMU 91,91,22\nAW 112,91\nAR 91,113\nMU 91,91,22\nAW 113,91\nAR 91,114\nMU 91,91,22\nAW 114,91\nAR 91,115\nMU 91,91,22\nAW 115,91\nAR 91,116\nMU 91,91,23\nAW 116,91\n"
"AR 91,117\nMU 91,91,23\nAW 117,91\nAR 91,118\nMU 91,91,23\nAW 118,91\nAR 91,119\nMU 91,91,23\nAW 119,91\nAR 91,120\nMU 91,91,22\nAW 120,91\nAR 91,121\nMU 91,91,22\nAW 121,91\n"
"AR 91,122\nMU 91,91,22\nAW 122,91\nAR 91,123\nMU 91,91,22\nAW 123,91\nAR 91,124\nMU 91,91,23\nAW 124,91\nAR 91,125\nMU 91,91,23\nAW 125,91\nAR 91,126\nMU 91,91,23\nAW 126,91\n"
"AR 91,127\nMU 91,91,23\nAW 127,91\ns_nop 1\ns_setpc_b64 s[80:81]\n.p2align 12\n.PA3:\ns_mov_b64 exec, 0\ns_cmp_eq_u32 s11, 1\ns_cbranch_scc1 .W556\ns_cmp_eq_u32 s11, 2\n"
"s_cbranch_scc1 .W557\ns_cmp_eq_u32 s11, 3\ns_cbranch_scc1 .W558\ns_branch .W555\n.WB152:\ns_mov_b64 exec, -1\nv_add_u32 v128, 0x10000, v12\nv_add_u32 v129, 0x10000, v13\n"
"v_add_u32 v130, 0x10000, v14\nv_add_u32 v131, 0x10000, v15\nv_add_u32 v132, 0x10000, v16\nv_add_u32 v133, 0x10000, v17\nv_add_u32 v134, 0x10000, v18\nv_add_u32 v135, 0x10000, v19\n"
"s_add_u32 s2, s11, 1\ns_lshr_b32 s2, s2, 1\ns_lshl_b32 s2, s2, 12\nv_add_u32 v140, s2, v10\nv_add_u32 v141, s2, v11\nv_xor_b32 v142, 0x1000, v140\nv_xor_b32 v143, 0x1000, v141\n"
"s_add_u32 m0, s47, 0\ns_add_u32 s46, s51, 524288\nD0 6,32,46,0\nD0 7,32,46,1024\nD0 8,32,46,2048\nD0 9,32,46,3072\ns_add_u32 m0, s47, -49152\ns_add_u32 s46, s52, 128\n"
"D0 136,28,46,0\nD0 137,28,46,1024\nD0 138,28,46,2048\nD0 139,28,46,3072\ns_add_u32 m0, s47, 16384\ns_add_u32 s46, s51, 786432\nD0 6,32,46,0\nD0 7,32,46,1024\nD0 8,32,46,2048\n"
"D0 9,32,46,3072\nv_mov_b32 v116, 0x40000000\nv_cmp_gt_u32 vcc, 56, v0\ns_lshl2_add_u32 s2, s10, s11\ns_mul_i32 s2, s2, 1792\nv_cndmask_b32 v117, v116, v1, vcc\ns_mov_b32 s36, s56\n"
"s_and_b32 s37, s57, 0xffff\ns_mov_b32 s38, 0x380\ns_mov_b32 s39, 0x20000\ns_mov_b32 s40, s58\ns_and_b32 s41, s59, 0xffff\ns_mov_b32 s42, 0x380\ns_mov_b32 s43, 0x20000\n"
"buffer_load_dwordx4 v[96:99], v117, s[28:31], s2 offen offset:0\nbuffer_load_dwordx4 v[100:103], v117, s[28:31], s2 offen offset:896\n"
"buffer_load_dwordx4 v[104:107], v117, s[36:39], 0 offen offset:0\nbuffer_load_dwordx4 v[108:111], v117, s[40:43], 0 offen offset:0\ns_add_u32 m0, s47, 32768\n"
"s_add_u32 s46, s51, 1048576\nD0 6,32,46,0\nD0 7,32,46,1024\nD0 8,32,46,2048\nD0 9,32,46,3072\ns_add_u32 m0, s47, -32768\ns_add_u32 s46, s52, 256\nD0 136,28,46,0\n"
"D0 137,28,46,1024\nD0 138,28,46,2048\nD0 139,28,46,3072\nWL 0\ns_mul_i32 s15, s15, 1664525\ns_add_u32 s15, s15, 1013904223\nv_mov_b32 v25, s15\ns_mov_b32 s57, 0x3db8aa3b\n"
"s_and_b32 s59, s11, 1\ns_lshl_b32 s59, s59, 13\ns_mul_i32 s2, s73, 25165824\ns_and_b32 s3, s12, 1\ns_lshl_b32 s3, s3, 21\ns_add_u32 s2, s2, s3\ns_lshl2_add_u32 s3, s13, s11\n"
"s_lshl_b32 s3, s3, 15\ns_add_u32 s2, s2, s3\ns_add_u32 s56, s2, 0x800000\ns_xor_b32 s53, s56, 0xc000\nWV 24\ns_barrier\nRa 192,140,0\nRa 196,141,0\nRa 200,140,2048\n"
"Ra 204,141,2048\nRa 208,142,0\nRa 212,143,0\nRa 216,142,2048\nRa 220,143,2048\nTa 160,128,0\nTa 162,128,2048\nTa 164,128,16384\nTa 166,128,18432\nTa 168,129,0\nTa 170,129,2048\n"
"Ta 172,129,16384\nTa 174,129,18432\nTa 176,130,0\nTa 178,130,2048\nTa 180,130,16384\nTa 182,130,18432\nTa 184,131,0\nTa 186,131,2048\nTa 188,131,16384\nTa 190,131,18432\nWL 0\n"
"Zvaa 32,160,192\nTa 224,132,0\ns_add_u32 m0, s47, 49152\ns_add_u32 s46, s51, 1310720\nMvaa 32,164,196\nTa 226,132,2048\nZaaa 96,164,204\nD0 6,32,46,0\nMaaa 96,160,200\n"
"Ta 228,132,16384\nZvaa 192,160,208\nTa 230,132,18432\nMvaa 192,164,212\nZvaa 224,164,220\nTa 232,133,0\nMvaa 224,160,216\nTa 234,133,2048\nD0 7,32,46,1024\nZvaa 228,168,216\n"
"Mvaa 228,172,220\nTa 236,133,16384\nZvaa 196,172,212\nTa 238,133,18432\nMvaa 196,168,208\nZaaa 100,168,200\nTa 240,134,0\nD0 8,32,46,2048\nMaaa 100,172,204\nTa 242,134,2048\n"
"Zvaa 36,172,196\nMvaa 36,168,192\nTa 244,134,16384\nZvaa 40,176,192\nTa 246,134,18432\nMvaa 40,180,196\nD0 9,32,46,3072\nZaaa 104,180,204\nTa 248,135,0\nMaaa 104,176,200\n"
"Ta 250,135,2048\nZvaa 200,176,208\ns_add_u32 m0, s47, -16384\ns_add_u32 s46, s52, 384\nMvaa 200,180,212\nTa 252,135,16384\nZvaa 232,180,220\nTa 254,135,18432\nD0 136,28,46,0\n"
"Mvaa 232,176,216\nZvaa 236,184,216\nMvaa 236,188,220\nZvaa 204,188,212\nMvaa 204,184,208\nD0 137,28,46,1024\nZaaa 108,184,200\nMaaa 108,188,204\nZvaa 44,188,196\nMvaa 44,184,192\n"
"s_waitcnt vmcnt(18) lgkmcnt(0)\ns_barrier\nZvaa 48,224,192\nRa 128,140,16384\nD0 138,28,46,2048\nMvaa 48,228,196\nRa 132,141,16384\nZaaa 112,228,204\nRa 136,140,18432\n"
"Maaa 112,224,200\nRa 140,141,18432\nZvaa 208,224,208\nRa 144,142,16384\nWV 15\nMvaa 208,228,212\nRa 148,143,16384\nD0 139,28,46,3072\nZvaa 240,228,220\nRa 152,142,18432\n"
"Mvaa 240,224,216\nRa 156,143,18432\nZvaa 244,232,216\nTa 160,12,0\ns_add_u32 m0, s47, 65536\ns_add_u32 s46, s51, 1572864\nMvaa 244,236,220\nTa 162,12,2048\nZvaa 212,236,212\n"
"Ta 164,12,16384\nD0 6,32,46,0\nMvaa 212,232,208\nTa 166,12,18432\nZaaa 116,232,200\nTa 168,13,0\nv_lshlrev_b32 v112, 16, v96\nMaaa 116,236,204\nTa 170,13,2048\nZvaa 52,236,196\n"
"Ta 172,13,16384\nMvaa 52,232,192\nTa 174,13,18432\nD0 7,32,46,1024\nZvaa 56,240,192\nTa 176,14,0\nv_lshlrev_b32 v113, 16, v100\nMvaa 56,244,196\nTa 178,14,2048\nZaaa 120,244,204\n"
"Ta 180,14,16384\nMaaa 120,240,200\nTa 182,14,18432\nZvaa 216,240,208\nTa 184,15,0\nD0 8,32,46,2048\nMvaa 216,244,212\nTa 186,15,2048\nZvaa 248,244,220\nTa 188,15,16384\n"
"Mvaa 248,240,216\nTa 190,15,18432\nZvaa 252,248,216\nv_lshlrev_b32 v114, 16, v104\nv_lshlrev_b32 v115, 16, v108\nMvaa 252,252,220\nD0 9,32,46,3072\nZvaa 220,252,212\n"
"MU 116,112,114\nMvaa 220,248,208\nMU 117,113,114\nZaaa 124,248,200\ns_add_u32 m0, s47, 81920\ns_add_u32 s46, s51, 1835008\nMaaa 124,252,204\nCV 116,116,117\nZvaa 60,252,196\n"
"D0 6,32,46,0\nMvaa 60,248,192\nWL 0\nMvaa 32,160,128\nTa 224,16,0\nv_lshlrev_b32 v118, 16, v116\nMvaa 32,164,132\nTa 226,16,2048\nMaaa 96,164,140\n"
"v_and_b32 v119, 0xffff0000, v116\nMaaa 96,160,136\nTa 228,16,16384\nD0 7,32,46,1024\nMvaa 192,160,144\nTa 230,16,18432\nMU 120,112,115\nMvaa 192,164,148\nMU 121,113,115\n"
"Mvaa 224,164,156\nTa 232,17,0\nMvaa 224,160,152\nTa 234,17,2048\nMvaa 228,168,152\nD0 8,32,46,2048\nv_and_b32 v112, 0xffff0000, v96\nMvaa 228,172,156\nTa 236,17,16384\n"
"Mvaa 196,172,148\nTa 238,17,18432\nMvaa 196,168,144\nv_and_b32 v113, 0xffff0000, v100\nMaaa 100,168,136\nTa 240,18,0\nv_and_b32 v114, 0xffff0000, v104\nMaaa 100,172,140\n"
"Ta 242,18,2048\nD0 9,32,46,3072\nMvaa 36,172,132\nv_and_b32 v115, 0xffff0000, v108\nMvaa 36,168,128\nTa 244,18,16384\nMvaa 40,176,128\nTa 246,18,18432\nMU 116,112,114\n"
"Mvaa 40,180,132\nMU 117,113,114\nMaaa 104,180,140\nTa 248,19,0\nMaaa 104,176,136\nTa 250,19,2048\nMvaa 200,176,144\nCV 116,116,117\nv_and_b32 v117, 0xffff0000, v116\n"
"Mvaa 200,180,148\nTa 252,19,16384\nMvaa 232,180,156\nTa 254,19,18432\nMvaa 232,176,152\nv_lshlrev_b32 v116, 16, v116\nMvaa 236,184,152\nFM 120,112,115\nFM 121,113,115\n"
"Mvaa 236,188,156\nAD 118,118,116\nMvaa 204,188,148\nAD 119,119,117\nMvaa 204,184,144\nv_lshlrev_b32 v112, 16, v97\nMaaa 108,184,136\nv_lshlrev_b32 v113, 16, v101\n"
"v_lshlrev_b32 v114, 16, v105\nMaaa 108,188,140\nv_lshlrev_b32 v115, 16, v109\nMvaa 44,188,132\nMU 116,112,114\nMvaa 44,184,128\ns_waitcnt vmcnt(12) lgkmcnt(0)\ns_barrier\n"
"Mvaa 48,224,128\nRa 192,140,32768\ns_add_u32 m0, s47, -49152\ns_add_u32 s46, s52, 512\nMvaa 48,228,132\nRa 196,141,32768\nMaaa 112,228,140\nRa 200,140,34816\nD0 136,28,46,0\n"
"Maaa 112,224,136\nRa 204,141,34816\nMvaa 208,224,144\nRa 208,142,32768\nMU 117,113,114\nMvaa 208,228,148\nRa 212,143,32768\nMvaa 240,228,156\nRa 216,142,34816\nMvaa 240,224,152\n"
"Ra 220,143,34816\nD0 137,28,46,1024\nMvaa 244,232,152\nTa 160,12,32768\nCV 116,116,117\nMvaa 244,236,156\nTa 162,12,34816\nMvaa 212,236,148\nTa 164,12,49152\nMvaa 212,232,144\n"
"Ta 166,12,51200\nMaaa 116,232,136\nTa 168,13,32768\nD0 138,28,46,2048\nMaaa 116,236,140\nTa 170,13,34816\nMvaa 52,236,132\nTa 172,13,49152\nMvaa 52,232,128\nTa 174,13,51200\n"
"Mvaa 56,240,128\nTa 176,14,32768\nv_and_b32 v117, 0xffff0000, v116\nMvaa 56,244,132\nTa 178,14,34816\nD0 139,28,46,3072\nMaaa 120,244,140\nTa 180,14,49152\nMaaa 120,240,136\n"
"Ta 182,14,51200\nMvaa 216,240,144\nTa 184,15,32768\ns_add_u32 m0, s47, 0\ns_add_u32 s46, s51, 2097152\nMvaa 216,244,148\nTa 186,15,34816\nMvaa 248,244,156\nTa 188,15,49152\n"
"D0 6,32,46,0\nMvaa 248,240,152\nTa 190,15,51200\nMvaa 252,248,152\nv_lshlrev_b32 v116, 16, v116\nFM 120,112,115\nMvaa 252,252,156\nFM 121,113,115\nMvaa 220,252,148\n"
"AD 118,118,116\nMvaa 220,248,144\nD0 7,32,46,1024\nMaaa 124,248,136\nAD 119,119,117\nv_and_b32 v112, 0xffff0000, v97\nMaaa 124,252,140\nv_and_b32 v113, 0xffff0000, v101\n"
"Mvaa 60,252,132\nv_and_b32 v114, 0xffff0000, v105\nMvaa 60,248,128\nWL 0\nMvaa 32,160,192\nTa 224,16,32768\nD0 8,32,46,2048\nMvaa 32,164,196\nTa 226,16,34816\nMaaa 96,164,204\n"
"v_and_b32 v115, 0xffff0000, v109\nMaaa 96,160,200\nTa 228,16,49152\nMvaa 192,160,208\nTa 230,16,51200\nMU 116,112,114\nMvaa 192,164,212\nD0 9,32,46,3072\nMvaa 224,164,220\n"
"Ta 232,17,32768\nMvaa 224,160,216\nTa 234,17,34816\nMvaa 228,168,216\ns_add_u32 m0, s47, 16384\ns_add_u32 s46, s51, 2359296\nMvaa 228,172,220\nTa 236,17,49152\nMvaa 196,172,212\n"
"Ta 238,17,51200\nD0 6,32,46,0\nMvaa 196,168,208\nMU 117,113,114\nMaaa 100,168,200\nTa 240,18,32768\nCV 116,116,117\nMaaa 100,172,204\nTa 242,18,34816\nMvaa 36,172,196\n"
"v_and_b32 v117, 0xffff0000, v116\nMvaa 36,168,192\nTa 244,18,49152\nD0 7,32,46,1024\nMvaa 40,176,192\nTa 246,18,51200\nv_lshlrev_b32 v116, 16, v116\nMvaa 40,180,196\n"
"FM 120,112,115\nMaaa 104,180,204\nTa 248,19,32768\nMaaa 104,176,200\nTa 250,19,34816\nMvaa 200,176,208\nD0 8,32,46,2048\nFM 121,113,115\nMvaa 200,180,212\nTa 252,19,49152\n"
"Mvaa 232,180,220\nTa 254,19,51200\nMvaa 232,176,216\nAD 118,118,116\nMvaa 236,184,216\nAD 119,119,117\nv_lshlrev_b32 v112, 16, v98\nMvaa 236,188,220\nD0 9,32,46,3072\n"
"Mvaa 204,188,212\nv_lshlrev_b32 v113, 16, v102\nMvaa 204,184,208\nv_lshlrev_b32 v114, 16, v106\nMaaa 108,184,200\nv_lshlrev_b32 v115, 16, v110\nMU 116,112,114\nMaaa 108,188,204\n"
"MU 117,113,114\nMvaa 44,188,196\nCV 116,116,117\nMvaa 44,184,192\ns_waitcnt vmcnt(12) lgkmcnt(0)\ns_barrier\nMvaa 48,224,192\nRa 128,140,49152\ns_add_u32 m0, s47, -32768\n"
"s_add_u32 s46, s52, 640\nMvaa 48,228,196\nRa 132,141,49152\nMaaa 112,228,204\nRa 136,140,51200\nD0 136,28,46,0\nMaaa 112,224,200\nRa 140,141,51200\nMvaa 208,224,208\n"
"Ra 144,142,49152\nv_and_b32 v117, 0xffff0000, v116\nMvaa 208,228,212\nRa 148,143,49152\nMvaa 240,228,220\nRa 152,142,51200\nMvaa 240,224,216\nRa 156,143,51200\nD0 137,28,46,1024\n"
"Mvaa 244,232,216\nTa 160,128,0\nv_lshlrev_b32 v116, 16, v116\nMvaa 244,236,220\nTa 162,128,2048\nMvaa 212,236,212\nTa 164,128,16384\nMvaa 212,232,208\nTa 166,128,18432\n"
"Maaa 116,232,200\nTa 168,129,0\nD0 138,28,46,2048\nMaaa 116,236,204\nTa 170,129,2048\nMvaa 52,236,196\nTa 172,129,16384\nMvaa 52,232,192\nTa 174,129,18432\nMvaa 56,240,192\n"
"Ta 176,130,0\nFM 120,112,115\nMvaa 56,244,196\nTa 178,130,2048\nD0 139,28,46,3072\nMaaa 120,244,204\nTa 180,130,16384\nMaaa 120,240,200\nTa 182,130,18432\nMvaa 216,240,208\n"
"Ta 184,131,0\ns_add_u32 m0, s47, 32768\ns_add_u32 s46, s51, 2621440\nMvaa 216,244,212\nTa 186,131,2048\nMvaa 248,244,220\nTa 188,131,16384\nD0 6,32,46,0\nMvaa 248,240,216\n"
"Ta 190,131,18432\nMvaa 252,248,216\nFM 121,113,115\nAD 118,118,116\nMvaa 252,252,220\nAD 119,119,117\nMvaa 220,252,212\nv_and_b32 v112, 0xffff0000, v98\nMvaa 220,248,208\n"
"D0 7,32,46,1024\nMaaa 124,248,200\nv_and_b32 v113, 0xffff0000, v102\nv_and_b32 v114, 0xffff0000, v106\nMaaa 124,252,204\nv_and_b32 v115, 0xffff0000, v110\nMvaa 60,252,196\n"
"MU 116,112,114\nMvaa 60,248,192\nWL 0\nMvaa 32,160,128\nTa 224,132,0\n.W555:\ns_cbranch_execz .W559\nD0 8,32,46,2048\nMvaa 32,164,132\nTa 226,132,2048\nMaaa 96,164,140\n"
"MU 117,113,114\nMaaa 96,160,136\nTa 228,132,16384\nMvaa 192,160,144\nTa 230,132,18432\nCV 116,116,117\nMvaa 192,164,148\nD0 9,32,46,3072\nMvaa 224,164,156\nTa 232,133,0\n"
"Mvaa 224,160,152\nTa 234,133,2048\nMvaa 228,168,152\ns_add_u32 m0, s47, 49152\ns_add_u32 s46, s51, 2883584\nMvaa 228,172,156\nTa 236,133,16384\nMvaa 196,172,148\nTa 238,133,18432\n"
"D0 6,32,46,0\nMvaa 196,168,144\nv_and_b32 v117, 0xffff0000, v116\nMaaa 100,168,136\nTa 240,134,0\nv_lshlrev_b32 v116, 16, v116\nMaaa 100,172,140\nTa 242,134,2048\nMvaa 36,172,132\n"
"FM 120,112,115\nMvaa 36,168,128\nTa 244,134,16384\nD0 7,32,46,1024\nMvaa 40,176,128\nTa 246,134,18432\nFM 121,113,115\nMvaa 40,180,132\nAD 118,118,116\nMaaa 104,180,140\n"
"Ta 248,135,0\nMaaa 104,176,136\nTa 250,135,2048\nMvaa 200,176,144\nD0 8,32,46,2048\nAD 119,119,117\nMvaa 200,180,148\nTa 252,135,16384\nMvaa 232,180,156\nTa 254,135,18432\n"
"Mvaa 232,176,152\nv_lshlrev_b32 v112, 16, v99\nMvaa 236,184,152\nv_lshlrev_b32 v113, 16, v103\nv_lshlrev_b32 v114, 16, v107\nMvaa 236,188,156\nD0 9,32,46,3072\nMvaa 204,188,148\n"
"v_lshlrev_b32 v115, 16, v111\nMvaa 204,184,144\nMU 116,112,114\nMaaa 108,184,136\nMU 117,113,114\nCV 116,116,117\ns_and_b32 s78, s74, 3\ns_lshl2_add_u32 s78, s73, s78\n"
"s_lshl_b32 s78, s78, 12\ns_bitcmp1_b32 s11, 0\ns_cselect_b32 s79, s48, s45\ns_add_u32 s79, s79, 512\ns_and_b32 s79, s79, 0xfffe0000\ns_add_u32 s78, s78, s79\n"
"buffer_load_dword v31, v26, s[20:23], s78 offen\nMaaa 108,188,140\nv_and_b32 v117, 0xffff0000, v116\nMvaa 44,188,132\nv_lshlrev_b32 v116, 16, v116\nMvaa 44,184,128\n"
"s_waitcnt vmcnt(13) lgkmcnt(0)\ns_barrier\nMvaa 48,224,128\nRa 192,140,16384\ns_add_u32 m0, s47, -16384\ns_add_u32 s46, s52, 768\nMvaa 48,228,132\nRa 196,141,16384\n"
"Maaa 112,228,140\nRa 200,140,18432\nD0 136,28,46,0\nMaaa 112,224,136\nRa 204,141,18432\nMvaa 208,224,144\nRa 208,142,16384\nFM 120,112,115\nMvaa 208,228,148\nRa 212,143,16384\n"
"Mvaa 240,228,156\nRa 216,142,18432\nMvaa 240,224,152\nRa 220,143,18432\nD0 137,28,46,1024\nMvaa 244,232,152\nTa 160,12,0\nFM 121,113,115\nMvaa 244,236,156\nTa 162,12,2048\n"
"Mvaa 212,236,148\nTa 164,12,16384\nMvaa 212,232,144\nTa 166,12,18432\nMaaa 116,232,136\nTa 168,13,0\nD0 138,28,46,2048\nMaaa 116,236,140\nTa 170,13,2048\nMvaa 52,236,132\n"
"Ta 172,13,16384\nMvaa 52,232,128\nTa 174,13,18432\nMvaa 56,240,128\nTa 176,14,0\nAD 118,118,116\nMvaa 56,244,132\nTa 178,14,2048\nD0 139,28,46,3072\nMaaa 120,244,140\n"
"Ta 180,14,16384\nMaaa 120,240,136\nTa 182,14,18432\nMvaa 216,240,144\nTa 184,15,0\ns_add_u32 m0, s47, 65536\ns_add_u32 s46, s51, 3145728\nMvaa 216,244,148\nTa 186,15,2048\n"
"Mvaa 248,244,156\nTa 188,15,16384\nD0 6,32,46,0\nMvaa 248,240,152\nTa 190,15,18432\nMvaa 252,248,152\nAD 119,119,117\nv_and_b32 v112, 0xffff0000, v99\nMvaa 252,252,156\n"
"v_and_b32 v113, 0xffff0000, v103\nMvaa 220,252,148\nv_and_b32 v114, 0xffff0000, v107\nMvaa 220,248,144\nD0 7,32,46,1024\nMaaa 124,248,136\nv_and_b32 v115, 0xffff0000, v111\n"
"MU 116,112,114\nMaaa 124,252,140\nMU 117,113,114\nMvaa 60,252,132\nCV 116,116,117\nMvaa 60,248,128\nWL 0\nMvaa 32,160,192\nTa 224,16,0\nD0 8,32,46,2048\nMvaa 32,164,196\n"
"Ta 226,16,2048\nMaaa 96,164,204\nv_and_b32 v117, 0xffff0000, v116\nMaaa 96,160,200\nTa 228,16,16384\nMvaa 192,160,208\nTa 230,16,18432\nv_lshlrev_b32 v116, 16, v116\n"
"Mvaa 192,164,212\nD0 9,32,46,3072\nMvaa 224,164,220\nTa 232,17,0\nMvaa 224,160,216\nTa 234,17,2048\nMvaa 228,168,216\ns_add_u32 m0, s47, 81920\ns_add_u32 s46, s51, 3407872\n"
"Mvaa 228,172,220\nTa 236,17,16384\nMvaa 196,172,212\nTa 238,17,18432\nD0 6,32,46,0\nMvaa 196,168,208\nFM 120,112,115\nMaaa 100,168,200\nTa 240,18,0\nFM 121,113,115\n"
"Maaa 100,172,204\nTa 242,18,2048\nMvaa 36,172,196\nAD 118,118,116\nMvaa 36,168,192\nTa 244,18,16384\nD0 7,32,46,1024\nMvaa 40,176,192\nTa 246,18,18432\nAD 119,119,117\n"
"Mvaa 40,180,196\ns_nop 0\nv_permlane32_swap_b32 v118, v119\nMaaa 104,180,204\nTa 248,19,0\nMaaa 104,176,200\nTa 250,19,2048\nMvaa 200,176,208\nD0 8,32,46,2048\n"
"v_permlane32_swap_b32 v120, v121\nMvaa 200,180,212\nTa 252,19,16384\nMvaa 232,180,220\nTa 254,19,18432\nMvaa 232,176,216\nAD 118,118,119\nMvaa 236,184,216\nAD 120,120,121\n"
"Mvaa 236,188,220\nD0 9,32,46,3072\nMvaa 204,188,212\nv_permlane16_swap_b32 v118, v120\nMvaa 204,184,208\nAD 118,118,120\nMaaa 108,184,200\nMaaa 108,188,204\n"
"v_add_f32_dpp v118, v118, v118 row_ror:8 row_mask:0xf bank_mask:0xf\nMvaa 44,188,196\nMvaa 44,184,192\ns_waitcnt vmcnt(13) lgkmcnt(0)\ns_barrier\nMvaa 48,224,192\n"
"Ra 128,140,32768\ns_add_u32 m0, s47, -65536\ns_add_u32 s45, s45, 128\nMvaa 48,228,196\nRa 132,141,32768\nMaaa 112,228,204\nRa 136,140,34816\nD0 2,20,45,0\nMaaa 112,224,200\n"
"Ra 140,141,34816\nMvaa 208,224,208\nRa 144,142,32768\nv_add_f32_dpp v118, v118, v118 row_ror:4 row_mask:0xf bank_mask:0xf\nMvaa 208,228,212\nRa 148,143,32768\nMvaa 240,228,220\n"
"Ra 152,142,34816\nMvaa 240,224,216\nRa 156,143,34816\nD0 3,20,45,1024\nMvaa 244,232,216\nTa 160,12,32768\nv_add_f32_dpp v118, v118, v118 row_ror:2 row_mask:0xf bank_mask:0xf\n"
"Mvaa 244,236,220\nTa 162,12,34816\nMvaa 212,236,212\nTa 164,12,49152\nMvaa 212,232,208\nTa 166,12,51200\nMaaa 116,232,200\nTa 168,13,32768\nD0 4,20,45,2048\nMaaa 116,236,204\n"
"Ta 170,13,34816\nMvaa 52,236,196\nTa 172,13,49152\nMvaa 52,232,192\nTa 174,13,51200\nMvaa 56,240,192\nTa 176,14,32768\n"
"v_add_f32_dpp v118, v118, v118 row_ror:1 row_mask:0xf bank_mask:0xf\nMvaa 56,244,196\nTa 178,14,34816\nD0 5,20,45,3072\nMaaa 120,244,204\nTa 180,14,49152\nMaaa 120,240,200\n"
"Ta 182,14,51200\nMvaa 216,240,208\nTa 184,15,32768\ns_add_u32 m0, s47, 0\ns_add_u32 s44, s44, 0x40000\nMvaa 216,244,212\nTa 186,15,34816\nMvaa 248,244,220\nTa 188,15,49152\n"
"D0 6,24,44,0\nMvaa 248,240,216\nTa 190,15,51200\nMvaa 252,248,216\nCV 118,118,118\nv_lshlrev_b32 v118, 16, v118\nMvaa 252,252,220\nv_mul_f32 v119, 0xbfb8aa3b, v118\nEX 119,119\n"
"Mvaa 220,252,212\nv_add_f32 v120, 0, v118\nMvaa 220,248,208\nD0 7,24,44,1024\nMaaa 124,248,200\nv_add_f32 v119, 1.0, v119\nMaaa 124,252,204\nv_rcp_f32 v119, v119\nMvaa 60,252,196\n"
"v_ashrrev_i32 v121, 31, v120\nMvaa 60,248,192\nWL 0\nMvaa 32,160,128\nTa 224,16,32768\nD0 8,24,44,2048\nMvaa 32,164,132\nTa 226,16,34816\nMaaa 96,164,140\nCV 119,119,119\n"
"Maaa 96,160,136\nTa 228,16,49152\nMvaa 192,160,144\nTa 230,16,51200\nv_lshlrev_b32 v119, 16, v119\nMvaa 192,164,148\nD0 9,24,44,3072\nMvaa 224,164,156\nTa 232,17,32768\n"
"Mvaa 224,160,152\nTa 234,17,34816\nMvaa 228,168,152\ns_add_u32 m0, s47, 16384\ns_add_u32 s44, s44, 0x40000\nMvaa 228,172,156\nTa 236,17,49152\nMvaa 196,172,148\nTa 238,17,51200\n"
"D0 6,24,44,0\nMvaa 196,168,144\nv_or_b32 v121, 0x80000000, v121\nMaaa 100,168,136\nTa 240,18,32768\nv_xor_b32 v120, v120, v121\nMaaa 100,172,140\nTa 242,18,34816\nMvaa 36,172,132\n"
"v_and_b32 v120, 0xffff0000, v120\nMvaa 36,168,128\nTa 244,18,49152\nD0 7,24,44,1024\nMvaa 40,176,128\nTa 246,18,51200\ns_lshl2_add_u32 s2, s10, s11\nMvaa 40,180,132\n"
"v_lshrrev_b32 v112, 5, v0\nMaaa 104,180,140\nTa 248,19,32768\nMaaa 104,176,136\nTa 250,19,34816\nMvaa 200,176,144\nD0 8,24,44,2048\nv_lshl_add_u32 v112, s2, 1, v112\n"
"Mvaa 200,180,148\nTa 252,19,49152\nMvaa 232,180,156\nTa 254,19,51200\nMvaa 232,176,152\nv_sub_u32 v113, 0xffff, v112\nMvaa 236,184,152\nv_or_b32 v120, v120, v113\n"
"v_mov_b32 v121, s15\nMvaa 236,188,156\nD0 9,24,44,3072\nMvaa 204,188,148\nv_and_b32 v113, 31, v0\nMvaa 204,184,144\nv_mov_b32 v116, 0x40000000\nv_cmp_eq_u32 vcc, 16, v113\n"
"v_lshlrev_b32 v114, 2, v112\nv_lshlrev_b32 v115, 10, v113\nv_cndmask_b32 v114, v116, v114, vcc\nv_cndmask_b32 v120, v119, v120, vcc\nMaaa 108,184,136\n"
"v_lshl_add_u32 v115, v112, 3, v115\nv_and_b32 v113, 15, v0\nMaaa 108,188,140\nv_cmp_eq_u32 vcc, 0, v113\ns_mov_b32 s2, 0xc040000\ns_mov_b32 s3, 0xc03e000\n"
"v_cndmask_b32 v115, v116, v115, vcc\nbuffer_store_dwordx2 v[120:121], v115, s[16:19], s2 offen sc0 sc1\nbuffer_store_dword v118, v114, s[16:19], s3 offen sc0 sc1\nMvaa 44,188,132\n"
"Mvaa 44,184,128\ns_waitcnt vmcnt(14) lgkmcnt(0)\ns_barrier\nMvaa 48,224,128\nRa 192,140,49152\ns_add_u32 m0, s47, -49152\ns_add_u32 s48, s48, 128\nMvaa 48,228,132\n"
"Ra 196,141,49152\nMaaa 112,228,140\nRa 200,140,51200\nD0 2,20,48,0\nMaaa 112,224,136\nRa 204,141,51200\nMvaa 208,224,144\nRa 208,142,49152\nMvaa 208,228,148\nRa 212,143,49152\n"
"Mvaa 240,228,156\nRa 216,142,51200\nMvaa 240,224,152\nRa 220,143,51200\nD0 3,20,48,1024\nMvaa 244,232,152\nTa 160,128,0\nMvaa 244,236,156\nTa 162,128,2048\nMvaa 212,236,148\n"
"Ta 164,128,16384\nMvaa 212,232,144\nTa 166,128,18432\nMaaa 116,232,136\nTa 168,129,0\nD0 4,20,48,2048\nMaaa 116,236,140\nTa 170,129,2048\nMvaa 52,236,132\nTa 172,129,16384\n"
"Mvaa 52,232,128\nTa 174,129,18432\nMvaa 56,240,128\nTa 176,130,0\nMvaa 56,244,132\nTa 178,130,2048\nD0 5,20,48,3072\nMaaa 120,244,140\nTa 180,130,16384\nMaaa 120,240,136\n"
"Ta 182,130,18432\nMvaa 216,240,144\nTa 184,131,0\ns_add_u32 m0, s47, -32768\ns_add_u32 s45, s45, 128\nMvaa 216,244,148\nTa 186,131,2048\nMvaa 248,244,156\nTa 188,131,16384\n"
"D0 2,20,45,0\nMvaa 248,240,152\nTa 190,131,18432\nMvaa 252,248,152\nMvaa 252,252,156\nMvaa 220,252,148\nMvaa 220,248,144\nD0 3,20,45,1024\nMaaa 124,248,136\nMaaa 124,252,140\n"
"Mvaa 60,252,132\nMvaa 60,248,128\nWL 0\nMvaa 32,160,192\nTa 224,132,0\nD0 4,20,45,2048\nMvaa 32,164,196\nTa 226,132,2048\nMvaa 36,172,196\nMvaa 36,168,192\nTa 228,132,16384\n"
"Mvaa 40,176,192\nTa 230,132,18432\nMvaa 40,180,196\nD0 5,20,45,3072\nMvaa 44,188,196\nTa 232,133,0\nMvaa 44,184,192\nTa 234,133,2048\nMaaa 108,184,200\ns_add_u32 m0, s47, 32768\n"
"s_add_u32 s44, s44, 0x40000\nMaaa 108,188,204\nTa 236,133,16384\nMaaa 104,180,204\nTa 238,133,18432\nD0 6,24,44,0\nMaaa 104,176,200\nv_mul_f32 v32, s57, v32\n"
"v_mul_f32 v33, s57, v33\nMaaa 100,168,200\nTa 240,134,0\nCV 32,32,33\nMaaa 100,172,204\nTa 242,134,2048\nv_mul_f32 v34, s57, v34\nMaaa 96,164,204\nv_mul_f32 v35, s57, v35\n"
"CV 33,34,35\nMaaa 96,160,200\nTa 244,134,16384\nD0 7,24,44,1024\nMvaa 192,160,208\nTa 246,134,18432\nv_mul_f32 v36, s57, v36\nMvaa 192,164,212\nv_mul_f32 v37, s57, v37\n"
"Mvaa 196,172,212\nTa 248,135,0\nCV 34,36,37\nMvaa 196,168,208\nTa 250,135,2048\nv_mul_f32 v38, s57, v38\nMvaa 200,176,208\nD0 8,24,44,2048\nv_mul_f32 v39, s57, v39\n"
"Mvaa 200,180,212\nTa 252,135,16384\nCV 35,38,39\nMvaa 204,188,212\nTa 254,135,18432\nv_mul_f32 v40, s57, v40\nMvaa 204,184,208\nv_mul_f32 v41, s57, v41\nCV 40,40,41\n"
"Mvaa 236,184,216\nv_mul_f32 v42, s57, v42\nv_mul_f32 v43, s57, v43\nMvaa 236,188,220\nD0 9,24,44,3072\nMvaa 232,180,220\nCV 41,42,43\nv_mul_f32 v44, s57, v44\nMvaa 232,176,216\n"
"v_mul_f32 v45, s57, v45\nCV 42,44,45\nMvaa 228,168,216\nv_mul_f32 v46, s57, v46\nv_mul_f32 v47, s57, v47\nMvaa 228,172,220\nCV 43,46,47\nAR 20,96\nMvaa 224,164,220\nAR 21,97\n"
"v_mul_f32 v20, s57, v20\nMvaa 224,160,216\ns_waitcnt vmcnt(14) lgkmcnt(0)\ns_barrier\nMvaa 48,224,192\ns_add_u32 m0, s47, 49152\ns_add_u32 s44, s44, 0x40000\nRa 128,10,0\n"
"Mvaa 48,228,196\nRa 132,11,0\nMvaa 52,236,196\nD0 6,24,44,0\nRa 136,10,2048\n.W556:\ns_cbranch_execz .W560\nMvaa 52,232,192\nRa 140,11,2048\nv_mul_f32 v21, s57, v21\n"
"Mvaa 56,240,192\nRa 144,10,8192\nCV 36,20,21\nMvaa 56,244,196\nRa 148,11,8192\nAR 20,98\nMvaa 60,252,196\nD0 7,24,44,1024\nRa 152,10,10240\nMvaa 60,248,192\nRa 156,11,10240\n"
"AR 21,99\nMaaa 124,248,200\nTa 160,12,0\nv_mul_f32 v20, s57, v20\nMaaa 124,252,204\nTa 162,12,2048\nMaaa 120,244,204\nD0 8,24,44,2048\nTa 164,12,16384\nMaaa 120,240,200\n"
"Ta 166,12,18432\nv_mul_f32 v21, s57, v21\nMaaa 116,232,200\nTa 168,13,0\nCV 37,20,21\nMaaa 116,236,204\nTa 170,13,2048\nAR 20,100\nMaaa 112,228,204\nD0 9,24,44,3072\n"
"Ta 172,13,16384\nMaaa 112,224,200\nTa 174,13,18432\nAR 21,101\nMvaa 208,224,208\ns_add_u32 m0, s47, -16384\ns_add_u32 s48, s48, 128\nTa 176,14,0\nMvaa 208,228,212\nTa 178,14,2048\n"
"Mvaa 212,236,212\nD0 2,20,48,0\nTa 180,14,16384\nMvaa 212,232,208\nTa 182,14,18432\nv_mul_f32 v20, s57, v20\nMvaa 216,240,208\nTa 184,15,0\nv_mul_f32 v21, s57, v21\n"
"Mvaa 216,244,212\nTa 186,15,2048\nCV 38,20,21\nMvaa 220,252,212\nD0 3,20,48,1024\nTa 188,15,16384\nMvaa 220,248,208\nTa 190,15,18432\nAR 20,102\nMvaa 252,248,216\nAR 21,103\n"
"v_mul_f32 v20, s57, v20\nMvaa 252,252,220\nv_mul_f32 v21, s57, v21\nMvaa 248,244,220\nD0 4,20,48,2048\nCV 39,20,21\nMvaa 248,240,216\nAR 20,104\nAR 21,105\nMvaa 244,232,216\n"
"v_mul_f32 v20, s57, v20\nv_mul_f32 v21, s57, v21\nMvaa 244,236,220\nCV 44,20,21\nAR 20,106\nMvaa 240,228,220\nD0 5,20,48,3072\nAR 21,107\nMvaa 240,224,216\nWL 0\ns_barrier\n"
"Zvaa 64,128,160\nTa 224,16,0\nv_mul_f32 v20, s57, v20\nMvaa 64,132,164\nTa 226,16,2048\nZvaa 68,140,164\ns_add_u32 m0, s47, -65536\ns_add_u32 s45, s45, 128\nMvaa 68,136,160\n"
"Ta 228,16,16384\nv_mul_f32 v21, s57, v21\nZvaa 128,144,160\nTa 230,16,18432\nCV 45,20,21\nMvaa 128,148,164\nD0 2,20,45,0\nAR 20,108\nZvaa 132,156,164\nTa 232,17,0\nAR 21,109\n"
"Mvaa 132,152,160\nTa 234,17,2048\nv_mul_f32 v20, s57, v20\nZvaa 140,152,168\nv_mul_f32 v21, s57, v21\nCV 46,20,21\nMvaa 140,156,172\nTa 236,17,16384\ns_add_u32 s6, s56, 0\n"
"Sv 32,1,16,6,0\nZvaa 136,148,172\nTa 238,17,18432\nAR 20,110\nMvaa 136,144,168\nAR 21,111\nv_mul_f32 v20, s57, v20\nZvaa 76,136,168\nTa 240,18,0\nv_mul_f32 v21, s57, v21\n"
"Mvaa 76,140,172\nTa 242,18,2048\nCV 47,20,21\nZvaa 72,132,172\nD0 3,20,45,1024\nv_mul_f32 v48, s57, v48\nMvaa 72,128,168\nTa 244,18,16384\nv_mul_f32 v49, s57, v49\n"
"Zvaa 80,128,176\nTa 246,18,18432\nCV 48,48,49\nMvaa 80,132,180\nv_mul_f32 v50, s57, v50\nZvaa 84,140,180\nTa 248,19,0\nv_mul_f32 v51, s57, v51\nMvaa 84,136,176\nTa 250,19,2048\n"
"CV 49,50,51\nZvaa 144,144,176\nv_mul_f32 v52, s57, v52\nv_mul_f32 v53, s57, v53\nMvaa 144,148,180\nTa 252,19,16384\nCV 50,52,53\nZvaa 148,156,180\nTa 254,19,18432\n"
"v_mul_f32 v54, s57, v54\nMvaa 148,152,176\nD0 4,20,45,2048\nv_mul_f32 v55, s57, v55\nZvaa 156,152,184\nCV 51,54,55\nv_mul_f32 v56, s57, v56\nMvaa 156,156,188\n"
"v_mul_f32 v57, s57, v57\ns_add_u32 s6, s56, 0\nSv 40,1,16,6,2048\nZvaa 152,148,188\nCV 56,56,57\nv_mul_f32 v58, s57, v58\nMvaa 152,144,184\nv_mul_f32 v59, s57, v59\nCV 57,58,59\n"
"Zvaa 92,136,184\nv_mul_f32 v60, s57, v60\nv_mul_f32 v61, s57, v61\nMvaa 92,140,188\nD0 5,20,45,3072\nCV 58,60,61\nZvaa 88,132,188\nv_mul_f32 v62, s57, v62\n"
"v_mul_f32 v63, s57, v63\nMvaa 88,128,184\ns_waitcnt vmcnt(18) lgkmcnt(0)\ns_barrier\nZvaa 96,128,224\nRa 192,10,16384\nCV 59,62,63\nMvaa 96,132,228\ns_add_u32 m0, s47, 0\n"
"s_add_u32 s44, s44, 0x40000\nZvaa 100,140,228\nRa 196,11,16384\nAR 20,112\nMvaa 100,136,224\nAR 21,113\nv_mul_f32 v20, s57, v20\nZvaa 160,144,224\nRa 200,10,18432\n"
"v_mul_f32 v21, s57, v21\nMvaa 160,148,228\nD0 6,24,44,0\nCV 52,20,21\nZvaa 164,156,228\nRa 204,11,18432\nAR 20,114\nMvaa 164,152,224\nAR 21,115\nv_mul_f32 v20, s57, v20\n"
"Zvaa 172,152,232\nRa 208,10,24576\nv_mul_f32 v21, s57, v21\nMvaa 172,156,236\nCV 53,20,21\ns_add_u32 s6, s56, 0\nSv 36,1,16,6,1024\nZvaa 168,148,236\nRa 212,11,24576\nAR 20,116\n"
"Mvaa 168,144,232\nAR 21,117\nv_mul_f32 v20, s57, v20\nZvaa 108,136,232\nRa 216,10,26624\nv_mul_f32 v21, s57, v21\nMvaa 108,140,236\nD0 7,24,44,1024\nCV 54,20,21\nZvaa 104,132,236\n"
"Ra 220,11,26624\nAR 20,118\nMvaa 104,128,232\nAR 21,119\nv_mul_f32 v20, s57, v20\nZvaa 112,128,240\nv_mul_f32 v21, s57, v21\nCV 55,20,21\nMvaa 112,132,244\nAR 20,120\n"
"Zvaa 116,140,244\nAR 21,121\nv_mul_f32 v20, s57, v20\nMvaa 116,136,240\nv_mul_f32 v21, s57, v21\nCV 60,20,21\nZvaa 176,144,240\nAR 20,122\nAR 21,123\nMvaa 176,148,244\n"
"D0 8,24,44,2048\nv_mul_f32 v20, s57, v20\nZvaa 180,156,244\nv_mul_f32 v21, s57, v21\nCV 61,20,21\nMvaa 180,152,240\nAR 20,124\nAR 21,125\nZvaa 188,152,248\n"
"v_mul_f32 v20, s57, v20\nv_mul_f32 v21, s57, v21\nMvaa 188,156,252\nCV 62,20,21\ns_add_u32 s6, s56, 0\nSv 44,1,16,6,3072\nZvaa 184,148,252\nAR 20,126\nAR 21,127\nMvaa 184,144,248\n"
"v_mul_f32 v20, s57, v20\nv_mul_f32 v21, s57, v21\nZvaa 124,136,248\nCV 63,20,21\nv_mul_f32 v192, s57, v192\nMvaa 124,140,252\nD0 9,24,44,3072\nv_mul_f32 v193, s57, v193\n"
"Zvaa 120,132,252\nCV 192,192,193\nv_mul_f32 v194, s57, v194\nMvaa 120,128,248\nWL 0\nZaaa 32,192,224\nRa 128,10,32768\nv_mul_f32 v195, s57, v195\nMaaa 32,196,228\n"
"s_add_u32 m0, s47, 16384\ns_add_u32 s44, s44, 0x40000\nZaaa 36,204,228\nRa 132,11,32768\nCV 193,194,195\nMaaa 36,200,224\nv_mul_f32 v196, s57, v196\nv_mul_f32 v197, s57, v197\n"
"Zaaa 96,208,224\nRa 136,10,34816\nCV 194,196,197\nMaaa 96,212,228\nD0 6,24,44,0\nv_mul_f32 v198, s57, v198\nZaaa 100,220,228\nRa 140,11,34816\nv_mul_f32 v199, s57, v199\n"
"Maaa 100,216,224\nCV 195,198,199\nv_mul_f32 v200, s57, v200\nZaaa 108,216,232\nRa 144,10,40960\nv_mul_f32 v201, s57, v201\nMaaa 108,220,236\nCV 200,200,201\n"
"v_mul_f32 v202, s57, v202\ns_add_u32 s6, s56, 4096\nSv 48,1,16,6,0\nZaaa 104,212,236\nRa 148,11,40960\nv_mul_f32 v203, s57, v203\nMaaa 104,208,232\nCV 201,202,203\n"
"v_mul_f32 v204, s57, v204\nZaaa 44,200,232\nRa 152,10,43008\nv_mul_f32 v205, s57, v205\nMaaa 44,204,236\nD0 7,24,44,1024\nCV 202,204,205\nZaaa 40,196,236\nRa 156,11,43008\n"
"v_mul_f32 v206, s57, v206\nMaaa 40,192,232\nv_mul_f32 v207, s57, v207\nCV 203,206,207\nZaaa 48,192,240\nv_mul_f32 v208, s57, v208\nv_mul_f32 v209, s57, v209\nMaaa 48,196,244\n"
"CV 208,208,209\nv_mul_f32 v210, s57, v210\nZaaa 52,204,244\nv_mul_f32 v211, s57, v211\nCV 209,210,211\nMaaa 52,200,240\nv_mul_f32 v212, s57, v212\nv_mul_f32 v213, s57, v213\n"
"Zaaa 112,208,240\nCV 210,212,213\nv_mul_f32 v214, s57, v214\nMaaa 112,212,244\nD0 8,24,44,2048\nv_mul_f32 v215, s57, v215\nZaaa 116,220,244\nCV 211,214,215\n"
"v_mul_f32 v216, s57, v216\nMaaa 116,216,240\nv_mul_f32 v217, s57, v217\nCV 216,216,217\nZaaa 124,216,248\nv_mul_f32 v218, s57, v218\nv_mul_f32 v219, s57, v219\nMaaa 124,220,252\n"
"CV 217,218,219\nv_mul_f32 v220, s57, v220\ns_add_u32 s6, s56, 4096\nSv 56,1,16,6,2048\nZaaa 120,212,252\nv_mul_f32 v221, s57, v221\nCV 218,220,221\nMaaa 120,208,248\n"
"v_mul_f32 v222, s57, v222\nv_mul_f32 v223, s57, v223\nZaaa 60,200,248\nCV 219,222,223\nv_mul_f32 v224, s57, v224\nMaaa 60,204,252\nD0 9,24,44,3072\nv_mul_f32 v225, s57, v225\n"
"Zaaa 56,196,252\nCV 224,224,225\nv_mul_f32 v226, s57, v226\nMaaa 56,192,248\ns_waitcnt vmcnt(22) lgkmcnt(0)\ns_barrier\nZaaa 0,192,160\nTa 224,16,32768\nv_mul_f32 v227, s57, v227\n"
"Maaa 0,196,164\nTa 226,16,34816\nCV 225,226,227\nZaaa 4,204,164\ns_add_u32 m0, s47, -49152\ns_add_u32 s48, s48, 128\nMaaa 4,200,160\nTa 228,16,49152\nv_mul_f32 v228, s57, v228\n"
"Zaaa 64,208,160\nTa 230,16,51200\nv_mul_f32 v229, s57, v229\nMaaa 64,212,164\nD0 2,20,48,0\nCV 226,228,229\nZaaa 68,220,164\nTa 232,17,32768\nv_mul_f32 v230, s57, v230\n"
"Maaa 68,216,160\nTa 234,17,34816\nv_mul_f32 v231, s57, v231\nZaaa 76,216,168\nCV 227,230,231\nv_mul_f32 v232, s57, v232\nMaaa 76,220,172\nTa 236,17,49152\n"
"v_mul_f32 v233, s57, v233\ns_add_u32 s6, s56, 4096\nSv 52,1,16,6,1024\nZaaa 72,212,172\nTa 238,17,51200\nCV 232,232,233\nMaaa 72,208,168\nv_mul_f32 v234, s57, v234\n"
"v_mul_f32 v235, s57, v235\nZaaa 12,200,168\nTa 240,18,32768\nCV 233,234,235\nMaaa 12,204,172\nTa 242,18,34816\nv_mul_f32 v236, s57, v236\nZaaa 8,196,172\nD0 3,20,48,1024\n"
"v_mul_f32 v237, s57, v237\nMaaa 8,192,168\nTa 244,18,49152\nCV 234,236,237\nZaaa 16,192,176\nTa 246,18,51200\nv_mul_f32 v238, s57, v238\nMaaa 16,196,180\n"
"v_mul_f32 v239, s57, v239\nCV 235,238,239\nZaaa 20,204,180\nTa 248,19,32768\nv_mul_f32 v240, s57, v240\nMaaa 20,200,176\nTa 250,19,34816\nv_mul_f32 v241, s57, v241\n"
"Zaaa 80,208,176\nCV 240,240,241\nv_mul_f32 v242, s57, v242\nMaaa 80,212,180\nTa 252,19,49152\nv_mul_f32 v243, s57, v243\nZaaa 84,220,180\nTa 254,19,51200\nCV 241,242,243\n"
"Maaa 84,216,176\nD0 4,20,48,2048\nv_mul_f32 v244, s57, v244\nZaaa 92,216,184\nv_mul_f32 v245, s57, v245\nCV 242,244,245\nMaaa 92,220,188\nv_mul_f32 v246, s57, v246\n"
"v_mul_f32 v247, s57, v247\ns_add_u32 s6, s56, 4096\nSv 60,1,16,6,3072\nZaaa 88,212,188\nCV 243,246,247\nv_mul_f32 v248, s57, v248\nMaaa 88,208,184\nv_mul_f32 v249, s57, v249\n"
"CV 248,248,249\nZaaa 28,200,184\nv_mul_f32 v250, s57, v250\nv_mul_f32 v251, s57, v251\nMaaa 28,204,188\nD0 5,20,48,3072\nCV 249,250,251\nZaaa 24,196,188\n"
"v_mul_f32 v252, s57, v252\nv_mul_f32 v253, s57, v253\nMaaa 24,192,184\nWL 0\nMvaa 96,128,224\nTa 160,12,32768\nCV 250,252,253\nMvaa 96,132,228\nTa 162,12,34816\n"
"v_mul_f32 v254, s57, v254\nMvaa 100,140,228\ns_add_u32 m0, s47, -32768\ns_add_u32 s45, s45, 128\nMvaa 100,136,224\nTa 164,12,49152\nv_mul_f32 v255, s57, v255\nMvaa 160,144,224\n"
"Ta 166,12,51200\nCV 251,254,255\nMvaa 160,148,228\nD0 2,20,45,0\nMvaa 164,156,228\nTa 168,13,32768\nMvaa 164,152,224\nTa 170,13,34816\nMvaa 172,152,232\nMvaa 172,156,236\n"
"Ta 172,13,49152\ns_add_u32 s6, s53, 0\nSv 192,1,16,6,0\nMvaa 168,148,236\nTa 174,13,51200\nMvaa 168,144,232\nMvaa 108,136,232\nTa 176,14,32768\nMvaa 108,140,236\nTa 178,14,34816\n"
"Mvaa 104,132,236\nD0 3,20,45,1024\nMvaa 104,128,232\nTa 180,14,49152\nMvaa 112,128,240\nTa 182,14,51200\nMvaa 112,132,244\nMvaa 116,140,244\nTa 184,15,32768\nMvaa 116,136,240\n"
"Ta 186,15,34816\nMvaa 176,144,240\nMvaa 176,148,244\nTa 188,15,49152\nMvaa 180,156,244\nTa 190,15,51200\nMvaa 180,152,240\nD0 4,20,45,2048\nMvaa 188,152,248\nMvaa 188,156,252\n"
"s_add_u32 s6, s53, 0\nSv 200,1,16,6,2048\nMvaa 184,148,252\nMvaa 184,144,248\nMvaa 124,136,248\nMvaa 124,140,252\nD0 5,20,45,3072\nMvaa 120,132,252\nMvaa 120,128,248\n"
"s_waitcnt vmcnt(24) lgkmcnt(0)\ns_barrier\nMvaa 64,128,160\nRa 192,10,49152\nMvaa 64,132,164\ns_add_u32 m0, s47, 32768\ns_add_u32 s44, s44, 0x40000\nMvaa 68,140,164\n"
"Ra 196,11,49152\nMvaa 68,136,160\nMvaa 128,144,160\nRa 200,10,51200\nMvaa 128,148,164\nD0 6,24,44,0\nMvaa 132,156,164\nRa 204,11,51200\nMvaa 132,152,160\nMvaa 140,152,168\n"
"Ra 208,10,57344\nMvaa 140,156,172\ns_add_u32 s6, s53, 4096\nSv 208,1,16,6,0\nMvaa 136,148,172\nRa 212,11,57344\nMvaa 136,144,168\nMvaa 76,136,168\nRa 216,10,59392\n"
"Mvaa 76,140,172\nD0 7,24,44,1024\nMvaa 72,132,172\nRa 220,11,59392\nMvaa 72,128,168\nMvaa 80,128,176\nMvaa 80,132,180\nMvaa 84,140,180\nMvaa 84,136,176\nMvaa 144,144,176\n"
"Mvaa 144,148,180\n.W557:\ns_cbranch_execz .W561\nD0 8,24,44,2048\nMvaa 148,156,180\nMvaa 148,152,176\nMvaa 156,152,184\nMvaa 156,156,188\ns_add_u32 s6, s53, 4096\n"
"Sv 216,1,16,6,2048\nMvaa 152,148,188\nMvaa 152,144,184\nMvaa 92,136,184\nMvaa 92,140,188\nD0 9,24,44,3072\nMvaa 88,132,188\nMvaa 88,128,184\nWL 0\nMaaa 0,192,160\nRa 128,10,0\n"
"Maaa 0,196,164\ns_add_u32 m0, s47, 49152\ns_add_u32 s44, s44, 0x40000\nMaaa 4,204,164\nRa 132,11,0\nMaaa 4,200,160\nMaaa 64,208,160\nRa 136,10,2048\nMaaa 64,212,164\nD0 6,24,44,0\n"
"Maaa 68,220,164\nRa 140,11,2048\nMaaa 68,216,160\nMaaa 76,216,168\nRa 144,10,8192\nMaaa 76,220,172\ns_add_u32 s6, s53, 0\nSv 224,1,16,6,1024\nMaaa 72,212,172\nRa 148,11,8192\n"
"Maaa 72,208,168\nMaaa 12,200,168\nRa 152,10,10240\nMaaa 12,204,172\nD0 7,24,44,1024\nMaaa 8,196,172\nRa 156,11,10240\nMaaa 8,192,168\nMaaa 16,192,176\nMaaa 16,196,180\n"
"Maaa 20,204,180\nMaaa 20,200,176\nMaaa 80,208,176\nMaaa 80,212,180\nD0 8,24,44,2048\nMaaa 84,220,180\nMaaa 84,216,176\nMaaa 92,216,184\nMaaa 92,220,188\ns_add_u32 s6, s53, 0\n"
"Sv 232,1,16,6,3072\nMaaa 88,212,188\nMaaa 88,208,184\nMaaa 28,200,184\nMaaa 28,204,188\nD0 9,24,44,3072\nMaaa 24,196,188\nMaaa 24,192,184\ns_waitcnt vmcnt(24) lgkmcnt(0)\n"
"s_barrier\nMaaa 32,192,224\nTa 160,12,0\nMaaa 32,196,228\nTa 162,12,2048\nMaaa 36,204,228\ns_add_u32 m0, s47, -16384\ns_add_u32 s48, s48, 128\nMaaa 36,200,224\nTa 164,12,16384\n"
"Maaa 96,208,224\nTa 166,12,18432\nMaaa 96,212,228\nD0 2,20,48,0\nMaaa 100,220,228\nTa 168,13,0\nMaaa 100,216,224\nTa 170,13,2048\nMaaa 108,216,232\nMaaa 108,220,236\n"
"Ta 172,13,16384\ns_add_u32 s6, s53, 4096\nSv 240,1,16,6,1024\nMaaa 104,212,236\nTa 174,13,18432\nMaaa 104,208,232\nMaaa 44,200,232\nTa 176,14,0\nMaaa 44,204,236\nTa 178,14,2048\n"
"Maaa 40,196,236\nD0 3,20,48,1024\nMaaa 40,192,232\nTa 180,14,16384\nMaaa 48,192,240\nTa 182,14,18432\nMaaa 48,196,244\ns_add_u32 s6, s53, 4096\nSv 248,1,16,6,3072\n"
"Maaa 52,204,244\nTa 184,15,0\nMaaa 52,200,240\nTa 186,15,2048\nMaaa 112,208,240\nMaaa 112,212,244\nTa 188,15,16384\nMaaa 116,220,244\nTa 190,15,18432\nMaaa 116,216,240\n"
"D0 4,20,48,2048\nMaaa 124,216,248\nMaaa 124,220,252\nMaaa 120,212,252\nMaaa 120,208,248\ns_lshl3_add_u32 s90, s12, s13\ns_lshl2_add_u32 s90, s90, s11\n"
"s_lshl2_add_u32 s90, s90, 0xc04a400\nMaaa 60,200,248\nMaaa 60,204,252\nD0 5,20,48,3072\nMaaa 56,196,252\nMaaa 56,192,248\nWL 0\ns_mov_b64 s[76:77], s[62:63]\n"
"s_mov_b64 s[78:79], s[64:65]\ns_mov_b32 s42, 1605632\ns_mov_b32 s50, 1\n.GL151:\nMvaa 64,128,160\nTa 224,16,0\nMvaa 64,132,164\nTa 226,16,2048\nMvaa 68,140,164\n"
"s_add_u32 m0, s47, -65536\ns_add_u32 s45, s45, 128\nMvaa 68,136,160\nTa 228,16,16384\nMvaa 128,144,160\nTa 230,16,18432\nMvaa 128,148,164\nD0 2,20,45,0\nMvaa 132,156,164\n"
"Ta 232,17,0\nMvaa 132,152,160\nTa 234,17,2048\nMvaa 140,152,168\nMvaa 140,156,172\nTa 236,17,16384\nMvaa 136,148,172\nTa 238,17,18432\nMvaa 136,144,168\nMvaa 76,136,168\n"
"Ta 240,18,0\nMvaa 76,140,172\nTa 242,18,2048\nMvaa 72,132,172\nD0 3,20,45,1024\nMvaa 72,128,168\nTa 244,18,16384\nMvaa 80,128,176\nTa 246,18,18432\nMvaa 80,132,180\n"
"Mvaa 84,140,180\nTa 248,19,0\nMvaa 84,136,176\nTa 250,19,2048\nMvaa 144,144,176\nMvaa 144,148,180\nTa 252,19,16384\nMvaa 148,156,180\nTa 254,19,18432\nMvaa 148,152,176\n"
"D0 4,20,45,2048\nMvaa 156,152,184\nMvaa 156,156,188\nMvaa 152,148,188\nMvaa 152,144,184\ns_mov_b32 s40, s76\ns_and_b32 s41, s77, 0xffff\nMvaa 92,136,184\nMvaa 92,140,188\n"
"D0 5,20,45,3072\nMvaa 88,132,188\nMvaa 88,128,184\ns_waitcnt vmcnt(17) lgkmcnt(0)\ns_barrier\nMvaa 96,128,224\nRa 192,10,16384\nMvaa 96,132,228\ns_add_u32 m0, s47, 0\n"
"s_add_u32 s44, s44, 0x40000\nbuffer_load_dword v31, v26, s[40:43], s97 offen\ns_mov_b64 s[76:77], s[66:67]\nMvaa 100,140,228\nRa 196,11,16384\nMvaa 100,136,224\nMvaa 160,144,224\n"
"Ra 200,10,18432\nMvaa 160,148,228\nD0 6,24,44,0\nMvaa 164,156,228\nRa 204,11,18432\nMvaa 164,152,224\nMvaa 172,152,232\nRa 208,10,24576\nMvaa 172,156,236\nMvaa 168,148,236\n"
"Ra 212,11,24576\nMvaa 168,144,232\nMvaa 108,136,232\nRa 216,10,26624\nMvaa 108,140,236\nD0 7,24,44,1024\nMvaa 104,132,236\nRa 220,11,26624\nMvaa 104,128,232\nMvaa 112,128,240\n"
"Mvaa 112,132,244\nMvaa 116,140,244\nMvaa 116,136,240\nMvaa 176,144,240\nMvaa 176,148,244\nD0 8,24,44,2048\nMvaa 180,156,244\nMvaa 180,152,240\nMvaa 188,152,248\nMvaa 188,156,252\n"
"Mvaa 184,148,252\nMvaa 184,144,248\nMvaa 124,136,248\nMvaa 124,140,252\nD0 9,24,44,3072\nMvaa 120,132,252\nMvaa 120,128,248\nWL 0\nMaaa 32,192,224\nRa 128,10,32768\n"
"Maaa 32,196,228\ns_add_u32 m0, s47, 16384\ns_add_u32 s44, s44, 0x40000\nMaaa 36,204,228\nRa 132,11,32768\nMaaa 36,200,224\nMaaa 96,208,224\nRa 136,10,34816\nMaaa 96,212,228\n"
"D0 6,24,44,0\nMaaa 100,220,228\nRa 140,11,34816\nMaaa 100,216,224\nMaaa 108,216,232\nRa 144,10,40960\nMaaa 108,220,236\nMaaa 104,212,236\nRa 148,11,40960\nMaaa 104,208,232\n"
"Maaa 44,200,232\nRa 152,10,43008\nMaaa 44,204,236\nD0 7,24,44,1024\nMaaa 40,196,236\nRa 156,11,43008\nMaaa 40,192,232\nMaaa 48,192,240\nMaaa 48,196,244\nMaaa 52,204,244\n"
"Maaa 52,200,240\nMaaa 112,208,240\nMaaa 112,212,244\nD0 8,24,44,2048\nMaaa 116,220,244\nMaaa 116,216,240\nMaaa 124,216,248\nMaaa 124,220,252\nMaaa 120,212,252\nMaaa 120,208,248\n"
"Maaa 60,200,248\nMaaa 60,204,252\nD0 9,24,44,3072\nMaaa 56,196,252\nMaaa 56,192,248\ns_waitcnt vmcnt(17) lgkmcnt(0)\ns_barrier\nMaaa 0,192,160\nTa 224,16,32768\nMaaa 0,196,164\n"
"Ta 226,16,34816\nMaaa 4,204,164\ns_add_u32 m0, s47, -49152\ns_add_u32 s48, s48, 128\nMaaa 4,200,160\nTa 228,16,49152\nMaaa 64,208,160\nTa 230,16,51200\nMaaa 64,212,164\n"
"D0 2,20,48,0\nMaaa 68,220,164\nTa 232,17,32768\nMaaa 68,216,160\nTa 234,17,34816\nMaaa 76,216,168\nMaaa 76,220,172\nTa 236,17,49152\nMaaa 72,212,172\nTa 238,17,51200\n"
"Maaa 72,208,168\nMaaa 12,200,168\nTa 240,18,32768\nMaaa 12,204,172\nTa 242,18,34816\nMaaa 8,196,172\nD0 3,20,48,1024\nMaaa 8,192,168\nTa 244,18,49152\nMaaa 16,192,176\n"
"Ta 246,18,51200\nMaaa 16,196,180\nMaaa 20,204,180\nTa 248,19,32768\nMaaa 20,200,176\nTa 250,19,34816\nMaaa 80,208,176\nMaaa 80,212,180\nTa 252,19,49152\nMaaa 84,220,180\n"
"Ta 254,19,51200\nMaaa 84,216,176\nD0 4,20,48,2048\nMaaa 92,216,184\nMaaa 92,220,188\nMaaa 88,212,188\nMaaa 88,208,184\nMaaa 28,200,184\nMaaa 28,204,188\nD0 5,20,48,3072\n"
"Maaa 24,196,188\nMaaa 24,192,184\nWL 0\nMvaa 96,128,224\nTa 160,12,32768\nMvaa 96,132,228\nTa 162,12,34816\nMvaa 100,140,228\ns_add_u32 m0, s47, -32768\ns_add_u32 s45, s45, 128\n"
"Mvaa 100,136,224\nTa 164,12,49152\nMvaa 160,144,224\nTa 166,12,51200\nMvaa 160,148,228\nD0 2,20,45,0\nMvaa 164,156,228\nTa 168,13,32768\nMvaa 164,152,224\nTa 170,13,34816\n"
"Mvaa 172,152,232\nMvaa 172,156,236\nTa 172,13,49152\nMvaa 168,148,236\nTa 174,13,51200\nMvaa 168,144,232\nMvaa 108,136,232\nTa 176,14,32768\nMvaa 108,140,236\nTa 178,14,34816\n"
"Mvaa 104,132,236\nD0 3,20,45,1024\nMvaa 104,128,232\nTa 180,14,49152\nMvaa 112,128,240\nTa 182,14,51200\nMvaa 112,132,244\nMvaa 116,140,244\nTa 184,15,32768\nMvaa 116,136,240\n"
"Ta 186,15,34816\nMvaa 176,144,240\nMvaa 176,148,244\nTa 188,15,49152\nMvaa 180,156,244\nTa 190,15,51200\nMvaa 180,152,240\nD0 4,20,45,2048\nMvaa 188,152,248\nMvaa 188,156,252\n"
"Mvaa 184,148,252\nMvaa 184,144,248\ns_mov_b32 s40, s78\ns_and_b32 s41, s79, 0xffff\nMvaa 124,136,248\nMvaa 124,140,252\nD0 5,20,45,3072\nMvaa 120,132,252\nMvaa 120,128,248\n"
"s_waitcnt vmcnt(17) lgkmcnt(0)\ns_barrier\nMvaa 64,128,160\nRa 192,10,49152\nMvaa 64,132,164\ns_add_u32 m0, s47, 32768\ns_add_u32 s44, s44, 0x40000\n"
"buffer_load_dword v31, v26, s[40:43], s97 offen\ns_mov_b64 s[78:79], s[66:67]\nMvaa 68,140,164\nRa 196,11,49152\nMvaa 68,136,160\nMvaa 128,144,160\nRa 200,10,51200\n"
"Mvaa 128,148,164\nD0 6,24,44,0\nMvaa 132,156,164\nRa 204,11,51200\nMvaa 132,152,160\nMvaa 140,152,168\nRa 208,10,57344\nMvaa 140,156,172\nMvaa 136,148,172\nRa 212,11,57344\n"
"Mvaa 136,144,168\nMvaa 76,136,168\nRa 216,10,59392\nMvaa 76,140,172\nD0 7,24,44,1024\nMvaa 72,132,172\nRa 220,11,59392\nMvaa 72,128,168\nMvaa 80,128,176\nMvaa 80,132,180\n"
"Mvaa 84,140,180\nMvaa 84,136,176\nMvaa 144,144,176\nMvaa 144,148,180\nD0 8,24,44,2048\nMvaa 148,156,180\nMvaa 148,152,176\nMvaa 156,152,184\nMvaa 156,156,188\nMvaa 152,148,188\n"
"Mvaa 152,144,184\nMvaa 92,136,184\nMvaa 92,140,188\nD0 9,24,44,3072\nMvaa 88,132,188\nMvaa 88,128,184\nWL 0\nMaaa 0,192,160\nRa 128,10,0\nMaaa 0,196,164\ns_add_u32 m0, s47, 49152\n"
"s_add_u32 s44, s44, 0x40000\nMaaa 4,204,164\nRa 132,11,0\nMaaa 4,200,160\nMaaa 64,208,160\nRa 136,10,2048\nMaaa 64,212,164\nD0 6,24,44,0\nMaaa 68,220,164\nRa 140,11,2048\n"
"Maaa 68,216,160\nMaaa 76,216,168\nRa 144,10,8192\nMaaa 76,220,172\nMaaa 72,212,172\nRa 148,11,8192\nMaaa 72,208,168\nMaaa 12,200,168\nRa 152,10,10240\nMaaa 12,204,172\n"
"D0 7,24,44,1024\nMaaa 8,196,172\nRa 156,11,10240\nMaaa 8,192,168\nMaaa 16,192,176\nMaaa 16,196,180\nMaaa 20,204,180\nMaaa 20,200,176\nMaaa 80,208,176\nMaaa 80,212,180\n"
"D0 8,24,44,2048\nMaaa 84,220,180\nMaaa 84,216,176\nMaaa 92,216,184\nMaaa 92,220,188\nMaaa 88,212,188\nMaaa 88,208,184\nMaaa 28,200,184\nMaaa 28,204,188\nD0 9,24,44,3072\n"
"Maaa 24,196,188\nMaaa 24,192,184\ns_waitcnt vmcnt(17) lgkmcnt(0)\ns_barrier\nMaaa 32,192,224\nTa 160,12,0\nMaaa 32,196,228\nTa 162,12,2048\nMaaa 36,204,228\n"
"s_add_u32 m0, s47, -16384\ns_add_u32 s48, s48, 128\nMaaa 36,200,224\nTa 164,12,16384\nMaaa 96,208,224\nTa 166,12,18432\nMaaa 96,212,228\nD0 2,20,48,0\nMaaa 100,220,228\n"
"Ta 168,13,0\nMaaa 100,216,224\nTa 170,13,2048\nMaaa 108,216,232\nMaaa 108,220,236\nTa 172,13,16384\nMaaa 104,212,236\nTa 174,13,18432\nMaaa 104,208,232\nMaaa 44,200,232\n"
"Ta 176,14,0\nMaaa 44,204,236\nTa 178,14,2048\n.W558:\ns_cbranch_execz .W562\nMaaa 40,196,236\nD0 3,20,48,1024\nMaaa 40,192,232\nTa 180,14,16384\nMaaa 48,192,240\nTa 182,14,18432\n"
"Maaa 48,196,244\nMaaa 52,204,244\nTa 184,15,0\nMaaa 52,200,240\nTa 186,15,2048\nMaaa 112,208,240\nMaaa 112,212,244\nTa 188,15,16384\nMaaa 116,220,244\nTa 190,15,18432\n"
"Maaa 116,216,240\nD0 4,20,48,2048\nMaaa 124,216,248\nMaaa 124,220,252\nMaaa 120,212,252\nMaaa 120,208,248\nMaaa 60,200,248\nMaaa 60,204,252\nD0 5,20,48,3072\n"
"s_sub_u32 s50, s50, 1\nMaaa 56,196,252\nMaaa 56,192,248\nWL 0\ns_cbranch_scc0 .GL151\nMvaa 64,128,160\nTa 224,16,0\nMvaa 64,132,164\nTa 226,16,2048\nMvaa 68,140,164\n"
"s_add_u32 m0, s47, -65536\ns_add_u32 s45, s45, 128\nMvaa 68,136,160\nTa 228,16,16384\nMvaa 128,144,160\nTa 230,16,18432\nMvaa 128,148,164\nD0 2,20,45,0\nMvaa 132,156,164\n"
"Ta 232,17,0\nMvaa 132,152,160\nTa 234,17,2048\nMvaa 140,152,168\ns_and_b32 s2, s74, 15\nMvaa 140,156,172\nTa 236,17,16384\n"
"buffer_store_dword v25, off, s[16:19], s90 offset:0 sc0 sc1\nMvaa 136,148,172\nTa 238,17,18432\nMvaa 136,144,168\ns_sub_u32 s2, s2, 2\ns_cmp_lt_u32 s2, 14\n"
"s_cselect_b32 s5, 0, 0x40000000\nMvaa 76,136,168\nTa 240,18,0\nMvaa 76,140,172\nTa 242,18,2048\nMvaa 72,132,172\nD0 3,20,45,1024\nMvaa 72,128,168\nTa 244,18,16384\n"
"Mvaa 80,128,176\nTa 246,18,18432\nMvaa 80,132,180\ns_and_b32 s2, s2, 15\nMvaa 84,140,180\nTa 248,19,0\nMvaa 84,136,176\nTa 250,19,2048\nMvaa 144,144,176\ns_mul_i32 s4, s73, 14\n"
"Mvaa 144,148,180\nTa 252,19,16384\nMvaa 148,156,180\nTa 254,19,18432\nMvaa 148,152,176\nD0 4,20,45,2048\nMvaa 156,152,184\ns_add_u32 s4, s4, s2\nMvaa 156,156,188\n"
"s_mov_b32 s36, s62\nMvaa 152,148,188\ns_and_b32 s37, s63, 0xffff\nMvaa 152,144,184\ns_mov_b32 s38, 0x188000\nMvaa 92,136,184\ns_mov_b32 s39, 0x20000\nMvaa 92,140,188\n"
"D0 5,20,45,3072\nMvaa 88,132,188\ns_mov_b32 s40, s64\nMvaa 88,128,184\ns_waitcnt vmcnt(18) lgkmcnt(0)\ns_barrier\nMvaa 96,128,224\nRa 192,10,16384\nMvaa 96,132,228\n"
"s_add_u32 m0, s47, 0\ns_add_u32 s44, s44, 0x40000\nMvaa 100,140,228\nRa 196,11,16384\nMvaa 100,136,224\ns_and_b32 s41, s65, 0xffff\nMvaa 160,144,224\nRa 200,10,18432\n"
"Mvaa 160,148,228\nD0 6,24,44,0\nMvaa 164,156,228\nRa 204,11,18432\nMvaa 164,152,224\ns_mov_b32 s42, 0x188000\nMvaa 172,152,232\nRa 208,10,24576\nMvaa 172,156,236\n"
"s_mov_b32 s43, 0x20000\nMvaa 168,148,236\nRa 212,11,24576\nMvaa 168,144,232\ns_mov_b32 s92, s66\nMvaa 108,136,232\nRa 216,10,26624\nMvaa 108,140,236\nD0 7,24,44,1024\n"
"Mvaa 104,132,236\nRa 220,11,26624\nMvaa 104,128,232\ns_and_b32 s93, s67, 0xffff\nMvaa 112,128,240\ns_mov_b32 s94, 0x188000\nMvaa 112,132,244\ns_mov_b32 s95, 0x20000\n"
"Mvaa 116,140,244\nv_lshrrev_b32 v252, 1, v0\nMvaa 116,136,240\nv_mul_u32_u24 v253, 14336, v252\nMvaa 176,144,240\nv_and_b32 v254, 1, v0\nMvaa 176,148,244\nD0 8,24,44,2048\n"
"Mvaa 180,156,244\nv_lshl_add_u32 v253, v254, 4, v253\nMvaa 180,152,240\nv_mov_b32 v254, 0x40000000\nv_cmp_gt_u32 vcc, 56, v0\nv_lshlrev_b32 v252, 3, v252\n"
"s_mul_i32 s2, s11, 401408\nv_cndmask_b32 v21, v254, v253, vcc\nv_cndmask_b32 v255, v254, v252, vcc\nMvaa 188,152,248\ns_lshl_b32 s3, s4, 5\nMvaa 188,156,252\ns_add_u32 s2, s2, s3\n"
"Mvaa 184,148,252\ns_or_b32 s86, s2, s5\nMvaa 184,144,248\ns_add_u32 s87, s86, 7168\nMvaa 124,136,248\nv_lshrrev_b32 v251, 4, v0\nMvaa 124,140,252\nD0 9,24,44,3072\n"
"Mvaa 120,132,252\nv_mul_u32_u24 v252, 3584, v251\nMvaa 120,128,248\nWL 0\nMaaa 32,192,224\nRa 128,10,32768\nMaaa 32,196,228\ns_add_u32 m0, s47, 16384\ns_add_u32 s44, s44, 0x40000\n"
"Maaa 36,204,228\nRa 132,11,32768\nMaaa 36,200,224\nv_and_b32 v251, 15, v0\nMaaa 96,208,224\nRa 136,10,34816\nMaaa 96,212,228\nD0 6,24,44,0\nMaaa 100,220,228\nRa 140,11,34816\n"
"Maaa 100,216,224\nv_lshl_add_u32 v252, v251, 4, v252\nv_cmp_gt_u32 vcc, 14, v251\ns_mul_i32 s2, s4, 14336\ns_mul_i32 s3, s11, 224\nv_cndmask_b32 v20, v254, v252, vcc\n"
"Maaa 108,216,232\nRa 144,10,40960\nMaaa 108,220,236\ns_add_u32 s2, s2, s3\nMaaa 104,212,236\nRa 148,11,40960\nMaaa 104,208,232\ns_or_b32 s88, s2, s5\nMaaa 44,200,232\n"
"Ra 152,10,43008\nMaaa 44,204,236\nD0 7,24,44,1024\nMaaa 40,196,236\nRa 156,11,43008\nMaaa 40,192,232\nMaaa 48,192,240\nMaaa 48,196,244\nMaaa 52,204,244\nMaaa 52,200,240\n"
"Maaa 112,208,240\nMaaa 112,212,244\nD0 8,24,44,2048\nMaaa 116,220,244\nMaaa 116,216,240\nMaaa 124,216,248\nMaaa 124,220,252\nMaaa 120,212,252\nMaaa 120,208,248\nMaaa 60,200,248\n"
"Maaa 60,204,252\nD0 9,24,44,3072\nMaaa 56,196,252\nMaaa 56,192,248\ns_waitcnt vmcnt(17) lgkmcnt(0)\ns_barrier\nMaaa 0,192,160\nTa 224,16,32768\nMaaa 0,196,164\nTa 226,16,34816\n"
"buffer_load_dwordx4 v[192:195], v21, s[36:39], s86 offen offset:0\nMaaa 4,204,164\ns_add_u32 m0, s47, -49152\ns_add_u32 s48, s48, 128\nMaaa 4,200,160\nTa 228,16,49152\n"
"s_lshl_b32 s6, s11, 12\ns_add_u32 s6, s6, 0xc044000\nMaaa 64,208,160\nTa 230,16,51200\nMaaa 64,212,164\nD0 2,20,48,0\nMaaa 68,220,164\nTa 232,17,32768\nMaaa 68,216,160\n"
"Ta 234,17,34816\nMaaa 76,216,168\nbuffer_load_dwordx4 v[32:35], v1, s[16:19], s6 offen offset:0 sc0 sc1\nMaaa 76,220,172\nTa 236,17,49152\nMaaa 72,212,172\nTa 238,17,51200\n"
"Maaa 72,208,168\nMaaa 12,200,168\nTa 240,18,32768\nMaaa 12,204,172\nTa 242,18,34816\nMaaa 8,196,172\nD0 3,20,48,1024\nMaaa 8,192,168\nTa 244,18,49152\nMaaa 16,192,176\n"
"Ta 246,18,51200\nMaaa 16,196,180\nbuffer_load_dwordx4 v[36:39], v1, s[16:19], s6 offen offset:1024 sc0 sc1\nMaaa 20,204,180\nTa 248,19,32768\nMaaa 20,200,176\nTa 250,19,34816\n"
"Maaa 80,208,176\nMaaa 80,212,180\nTa 252,19,49152\nMaaa 84,220,180\nTa 254,19,51200\nMaaa 84,216,176\nD0 4,20,48,2048\nMaaa 92,216,184\n"
"buffer_load_dwordx4 v[40:43], v1, s[16:19], s6 offen offset:2048 sc0 sc1\nMaaa 92,220,188\nMaaa 88,212,188\nMaaa 88,208,184\nMaaa 28,200,184\n"
"buffer_load_dwordx4 v[44:47], v1, s[16:19], s6 offen offset:3072 sc0 sc1\nMaaa 28,204,188\nD0 5,20,48,3072\nMaaa 24,196,188\nMaaa 24,192,184\nWL 0\nMvaa 96,128,224\n"
"Ta 160,12,32768\nMvaa 96,132,228\nTa 162,12,34816\nMvaa 100,140,228\ns_add_u32 m0, s47, -32768\ns_add_u32 s45, s45, 128\nMvaa 100,136,224\nTa 164,12,49152\nMvaa 160,144,224\n"
"Ta 166,12,51200\nMvaa 160,148,228\nD0 2,20,45,0\nMvaa 164,156,228\nTa 168,13,32768\nMvaa 164,152,224\nTa 170,13,34816\nMvaa 172,152,232\nMvaa 172,156,236\nTa 172,13,49152\n"
"Mvaa 168,148,236\nTa 174,13,51200\nMvaa 168,144,232\nMvaa 108,136,232\nTa 176,14,32768\nMvaa 108,140,236\nTa 178,14,34816\nMvaa 104,132,236\nD0 3,20,45,1024\nMvaa 104,128,232\n"
"Ta 180,14,49152\nMvaa 112,128,240\nTa 182,14,51200\nMvaa 112,132,244\nMvaa 116,140,244\nTa 184,15,32768\nMvaa 116,136,240\nTa 186,15,34816\nMvaa 176,144,240\nMvaa 176,148,244\n"
"Ta 188,15,49152\nMvaa 180,156,244\nTa 190,15,51200\nMvaa 180,152,240\nD0 4,20,45,2048\nMvaa 188,152,248\nMvaa 188,156,252\nMvaa 184,148,252\nMvaa 184,144,248\nMvaa 124,136,248\n"
"Mvaa 124,140,252\nD0 5,20,45,3072\nMvaa 120,132,252\nMvaa 120,128,248\ns_waitcnt vmcnt(21) lgkmcnt(0)\ns_barrier\nMvaa 64,128,160\nRa 192,10,49152\nMvaa 64,132,164\n"
"s_add_u32 m0, s47, 32768\ns_add_u32 s44, s44, 0x40000\nbuffer_load_dwordx4 v[208:211], v21, s[40:43], s86 offen offset:0\nMvaa 68,140,164\nRa 196,11,49152\nMvaa 68,136,160\n"
"Mvaa 128,144,160\nRa 200,10,51200\nMvaa 128,148,164\nD0 6,24,44,0\nMvaa 132,156,164\nRa 204,11,51200\nMvaa 132,152,160\nMvaa 140,152,168\nRa 208,10,57344\nMvaa 140,156,172\n"
"Mvaa 136,148,172\nRa 212,11,57344\nMvaa 136,144,168\nMvaa 76,136,168\nRa 216,10,59392\nMvaa 76,140,172\nD0 7,24,44,1024\nMvaa 72,132,172\nRa 220,11,59392\nMvaa 72,128,168\n"
"Mvaa 80,128,176\nMvaa 80,132,180\nMvaa 84,140,180\nMvaa 84,136,176\nMvaa 144,144,176\nMvaa 144,148,180\nD0 8,24,44,2048\nMvaa 148,156,180\nMvaa 148,152,176\nMvaa 156,152,184\n"
"Mvaa 156,156,188\nMvaa 152,148,188\nMvaa 152,144,184\nMvaa 92,136,184\nMvaa 92,140,188\nD0 9,24,44,3072\nMvaa 88,132,188\nMvaa 88,128,184\nWL 0\nMaaa 0,192,160\nRa 128,10,0\n"
"Maaa 0,196,164\ns_add_u32 m0, s47, 49152\ns_add_u32 s44, s44, 0x40000\nMaaa 4,204,164\nRa 132,11,0\nMaaa 4,200,160\nMaaa 64,208,160\nRa 136,10,2048\nMaaa 64,212,164\nD0 6,24,44,0\n"
"Maaa 68,220,164\nRa 140,11,2048\nMaaa 68,216,160\nMaaa 76,216,168\nRa 144,10,8192\nMaaa 76,220,172\nMaaa 72,212,172\nRa 148,11,8192\nMaaa 72,208,168\nMaaa 12,200,168\n"
"Ra 152,10,10240\nMaaa 12,204,172\nD0 7,24,44,1024\nMaaa 8,196,172\nRa 156,11,10240\nMaaa 8,192,168\nMaaa 16,192,176\nMaaa 16,196,180\nMaaa 20,204,180\nMaaa 20,200,176\n"
"Maaa 80,208,176\nMaaa 80,212,180\nD0 8,24,44,2048\nMaaa 84,220,180\nMaaa 84,216,176\nMaaa 92,216,184\nMaaa 92,220,188\nMaaa 88,212,188\nMaaa 88,208,184\nMaaa 28,200,184\n"
"Maaa 28,204,188\nD0 9,24,44,3072\nMaaa 24,196,188\nMaaa 24,192,184\ns_waitcnt vmcnt(14) lgkmcnt(0)\ns_barrier\nMaaa 32,192,224\nTa 160,12,0\nv_xor_b32 v48, s15, v33\n"
"Maaa 32,196,228\nTa 162,12,2048\nbuffer_load_dwordx4 v[196:199], v21, s[36:39], s86 offen offset:3584\nMaaa 36,204,228\ns_add_u32 m0, s47, -16384\ns_add_u32 s48, s48, 128\n"
"Maaa 36,200,224\nTa 164,12,16384\nMaaa 96,208,224\nTa 166,12,18432\nMaaa 96,212,228\nD0 2,20,48,0\nMaaa 100,220,228\nTa 168,13,0\nMaaa 100,216,224\nTa 170,13,2048\n"
"Maaa 108,216,232\nv_xor_b32 v49, s15, v35\nMaaa 108,220,236\nTa 172,13,16384\nMaaa 104,212,236\nTa 174,13,18432\nMaaa 104,208,232\nv_xor_b32 v50, s15, v37\nMaaa 44,200,232\n"
"Ta 176,14,0\nMaaa 44,204,236\nTa 178,14,2048\nMaaa 40,196,236\nD0 3,20,48,1024\nMaaa 40,192,232\nTa 180,14,16384\nMaaa 48,192,240\nTa 182,14,18432\nv_xor_b32 v51, s15, v39\n"
"Maaa 48,196,244\nv_xor_b32 v52, s15, v41\nMaaa 52,204,244\nTa 184,15,0\nMaaa 52,200,240\nTa 186,15,2048\nMaaa 112,208,240\nv_xor_b32 v53, s15, v43\nMaaa 112,212,244\n"
"Ta 188,15,16384\nMaaa 116,220,244\nTa 190,15,18432\nMaaa 116,216,240\nD0 4,20,48,2048\nMaaa 124,216,248\nv_xor_b32 v54, s15, v45\nMaaa 124,220,252\nv_xor_b32 v55, s15, v47\n"
"Maaa 120,212,252\nv_or3_b32 v48, v48, v49, v50\nMaaa 120,208,248\nv_or3_b32 v51, v51, v52, v53\nMaaa 60,200,248\nv_or3_b32 v48, v48, v54, v55\nMaaa 60,204,252\nD0 5,20,48,3072\n"
"Maaa 56,196,252\nv_or_b32 v48, v48, v51\nMaaa 56,192,248\nWL 0\nMvaa 64,128,160\nTa 224,16,0\nv_cmp_ne_u32 vcc, 0, v48\nMvaa 64,132,164\nTa 226,16,2048\nMvaa 68,140,164\n"
"s_add_u32 m0, s47, -65536\ns_add_u32 s45, s45, 128\nMvaa 68,136,160\nTa 228,16,16384\nMvaa 128,144,160\nTa 230,16,18432\nMvaa 128,148,164\nD0 2,20,45,0\nMvaa 132,156,164\n"
"Ta 232,17,0\nMvaa 132,152,160\nTa 234,17,2048\nMvaa 140,152,168\ns_cbranch_vccnz .S145\n.S146:\nMvaa 140,156,172\nTa 236,17,16384\nMvaa 136,148,172\nTa 238,17,18432\n"
"Mvaa 136,144,168\nv_max_u32 v48, v32, v34\nMvaa 76,136,168\nTa 240,18,0\nMvaa 76,140,172\nTa 242,18,2048\nMvaa 72,132,172\nD0 3,20,45,1024\n.W559:\ns_cbranch_execz .W563\n"
"Mvaa 72,128,168\nTa 244,18,16384\nMvaa 80,128,176\nTa 246,18,18432\nv_min_u32 v49, v32, v34\nMvaa 80,132,180\nv_med3_u32 v49, v48, v49, v36\nMvaa 84,140,180\nTa 248,19,0\n"
"Mvaa 84,136,176\nTa 250,19,2048\nMvaa 144,144,176\nv_max_u32 v48, v48, v36\nMvaa 144,148,180\nTa 252,19,16384\nMvaa 148,156,180\nTa 254,19,18432\nMvaa 148,152,176\n"
"D0 4,20,45,2048\nMvaa 156,152,184\nv_med3_u32 v49, v48, v49, v38\nMvaa 156,156,188\nv_max_u32 v48, v48, v38\nMvaa 152,148,188\nv_med3_u32 v49, v48, v49, v40\nMvaa 152,144,184\n"
"v_max_u32 v48, v48, v40\nMvaa 92,136,184\nv_med3_u32 v49, v48, v49, v42\nMvaa 92,140,188\nD0 5,20,45,3072\nMvaa 88,132,188\nv_max_u32 v48, v48, v42\nMvaa 88,128,184\n"
"s_waitcnt vmcnt(18) lgkmcnt(0)\ns_barrier\nMvaa 96,128,224\nRa 192,10,16384\nv_med3_u32 v49, v48, v49, v44\nMvaa 96,132,228\ns_add_u32 m0, s47, 0\ns_add_u32 s44, s44, 0x40000\n"
"buffer_load_dwordx4 v[212:215], v21, s[40:43], s86 offen offset:3584\nMvaa 100,140,228\nRa 196,11,16384\nMvaa 100,136,224\nv_max_u32 v48, v48, v44\nMvaa 160,144,224\n"
"Ra 200,10,18432\nMvaa 160,148,228\nD0 6,24,44,0\nMvaa 164,156,228\nRa 204,11,18432\nMvaa 164,152,224\nv_med3_u32 v49, v48, v49, v46\nMvaa 172,152,232\nRa 208,10,24576\n"
"Mvaa 172,156,236\nv_max_u32 v48, v48, v46\nMvaa 168,148,236\nRa 212,11,24576\nMvaa 168,144,232\nv_mov_b32 v50, v48\nMvaa 108,136,232\nRa 216,10,26624\nMvaa 108,140,236\n"
"D0 7,24,44,1024\nMvaa 104,132,236\nRa 220,11,26624\nMvaa 104,128,232\nv_max_u32_dpp v50, v50, v50 row_ror:8 row_mask:0xf bank_mask:0xf\nMvaa 112,128,240\nMvaa 112,132,244\n"
"v_max_u32_dpp v50, v50, v50 row_ror:4 row_mask:0xf bank_mask:0xf\nMvaa 116,140,244\nMvaa 116,136,240\nv_max_u32_dpp v50, v50, v50 row_ror:2 row_mask:0xf bank_mask:0xf\n"
"Mvaa 176,144,240\nMvaa 176,148,244\nD0 8,24,44,2048\nMvaa 180,156,244\nv_max_u32_dpp v50, v50, v50 row_ror:1 row_mask:0xf bank_mask:0xf\nMvaa 180,152,240\nv_mov_b32 v52, v50\n"
"Mvaa 188,152,248\nMvaa 188,156,252\nv_permlane16_swap_b32 v50, v52\nMvaa 184,148,252\nv_max_u32 v50, v50, v52\nMvaa 184,144,248\nv_mov_b32 v52, v50\nMvaa 124,136,248\n"
"Mvaa 124,140,252\nD0 9,24,44,3072\nMvaa 120,132,252\nv_permlane32_swap_b32 v50, v52\nMvaa 120,128,248\nWL 0\nMaaa 32,192,224\nRa 128,10,32768\nv_max_u32 v50, v50, v52\n"
"Maaa 32,196,228\ns_add_u32 m0, s47, 16384\ns_add_u32 s44, s44, 0x40000\nMaaa 36,204,228\nRa 132,11,32768\nv_cmp_eq_u32 vcc, v48, v50\ns_lshl_b32 s2, s11, 3\n"
"s_add_u32 s2, s2, 131072\nv_cndmask_b32 v51, v48, v49, vcc\nMaaa 36,200,224\nMaaa 96,208,224\nRa 136,10,34816\nMaaa 96,212,228\nD0 6,24,44,0\nMaaa 100,220,228\nRa 140,11,34816\n"
"Maaa 100,216,224\nv_max_u32_dpp v51, v51, v51 row_ror:8 row_mask:0xf bank_mask:0xf\nMaaa 108,216,232\nRa 144,10,40960\nMaaa 108,220,236\n"
"v_max_u32_dpp v51, v51, v51 row_ror:4 row_mask:0xf bank_mask:0xf\nMaaa 104,212,236\nRa 148,11,40960\nMaaa 104,208,232\n"
"v_max_u32_dpp v51, v51, v51 row_ror:2 row_mask:0xf bank_mask:0xf\nMaaa 44,200,232\nRa 152,10,43008\nMaaa 44,204,236\nD0 7,24,44,1024\nMaaa 40,196,236\nRa 156,11,43008\n"
"Maaa 40,192,232\nv_max_u32_dpp v51, v51, v51 row_ror:1 row_mask:0xf bank_mask:0xf\nMaaa 48,192,240\nv_mov_b32 v52, v51\nMaaa 48,196,244\nMaaa 52,204,244\n"
"v_permlane16_swap_b32 v51, v52\nMaaa 52,200,240\nv_max_u32 v51, v51, v52\nMaaa 112,208,240\nv_mov_b32 v52, v51\nMaaa 112,212,244\nD0 8,24,44,2048\nMaaa 116,220,244\n"
"v_permlane32_swap_b32 v51, v52\nMaaa 116,216,240\nv_max_u32 v51, v51, v52\nMaaa 124,216,248\nv_mov_b32 v52, s2\nMaaa 124,220,252\nds_write_b64 v52, v[50:51]\nMaaa 120,212,252\n"
"v_mov_b32 v52, 131072\nMaaa 120,208,248\nMaaa 60,200,248\nMaaa 60,204,252\nD0 9,24,44,3072\nMaaa 56,196,252\nMaaa 56,192,248\ns_waitcnt vmcnt(18) lgkmcnt(0)\ns_barrier\n"
"Maaa 0,192,160\nTa 224,16,32768\nMaaa 0,196,164\nTa 226,16,34816\nbuffer_load_dwordx4 v[200:203], v21, s[36:39], s87 offen offset:0\nMaaa 4,204,164\ns_add_u32 m0, s47, -49152\n"
"s_add_u32 s48, s48, 128\nMaaa 4,200,160\nTa 228,16,49152\nMaaa 64,208,160\nTa 230,16,51200\nMaaa 64,212,164\nD0 2,20,48,0\nMaaa 68,220,164\nTa 232,17,32768\nMaaa 68,216,160\n"
"Ta 234,17,34816\nMaaa 76,216,168\nRv 32,52,0\nMaaa 76,220,172\nTa 236,17,49152\nMaaa 72,212,172\nTa 238,17,51200\nMaaa 72,208,168\nRv 36,52,16\nMaaa 12,200,168\nTa 240,18,32768\n"
"Maaa 12,204,172\nTa 242,18,34816\nMaaa 8,196,172\nD0 3,20,48,1024\nMaaa 8,192,168\nTa 244,18,49152\nMaaa 16,192,176\nTa 246,18,51200\nMaaa 16,196,180\nMaaa 20,204,180\n"
"Ta 248,19,32768\nMaaa 20,200,176\nTa 250,19,34816\nMaaa 80,208,176\nMaaa 80,212,180\nTa 252,19,49152\nMaaa 84,220,180\nTa 254,19,51200\nMaaa 84,216,176\nD0 4,20,48,2048\n"
"Maaa 92,216,184\nWL 11\nMaaa 92,220,188\nv_max_u32 v48, v32, v33\nMaaa 88,212,188\nv_min_u32 v49, v32, v33\nMaaa 88,208,184\nv_med3_u32 v49, v48, v49, v34\nMaaa 28,200,184\n"
"v_max_u32 v48, v48, v34\nMaaa 28,204,188\nD0 5,20,48,3072\nMaaa 24,196,188\nv_med3_u32 v49, v48, v49, v35\nMaaa 24,192,184\nWL 0\nMvaa 96,128,224\nTa 160,12,32768\n"
"Mvaa 96,132,228\nTa 162,12,34816\nMvaa 100,140,228\ns_add_u32 m0, s47, -32768\ns_add_u32 s45, s45, 128\nMvaa 100,136,224\nTa 164,12,49152\nMvaa 160,144,224\nTa 166,12,51200\n"
"Mvaa 160,148,228\nD0 2,20,45,0\nMvaa 164,156,228\nTa 168,13,32768\nMvaa 164,152,224\nTa 170,13,34816\nMvaa 172,152,232\nv_max_u32 v48, v48, v35\nMvaa 172,156,236\nTa 172,13,49152\n"
"Mvaa 168,148,236\nTa 174,13,51200\nMvaa 168,144,232\nv_med3_u32 v49, v48, v49, v36\nMvaa 108,136,232\nTa 176,14,32768\nMvaa 108,140,236\nTa 178,14,34816\nMvaa 104,132,236\n"
"D0 3,20,45,1024\nMvaa 104,128,232\nTa 180,14,49152\nMvaa 112,128,240\nTa 182,14,51200\nMvaa 112,132,244\nv_max_u32 v48, v48, v36\nMvaa 116,140,244\nTa 184,15,32768\n"
"Mvaa 116,136,240\nTa 186,15,34816\nMvaa 176,144,240\nv_med3_u32 v49, v48, v49, v37\nMvaa 176,148,244\nTa 188,15,49152\nMvaa 180,156,244\nTa 190,15,51200\nMvaa 180,152,240\n"
"D0 4,20,45,2048\nMvaa 188,152,248\nv_max_u32 v48, v48, v37\nMvaa 188,156,252\nv_med3_u32 v49, v48, v49, v38\nMvaa 184,148,252\nv_max_u32 v48, v48, v38\nMvaa 184,144,248\n"
"v_med3_u32 v49, v48, v49, v39\nMvaa 124,136,248\nv_max_u32 v48, v48, v39\nMvaa 124,140,252\nD0 5,20,45,3072\nMvaa 120,132,252\nv_readfirstlane_b32 s2, v48\nMvaa 120,128,248\n"
"s_waitcnt vmcnt(18) lgkmcnt(0)\ns_barrier\nMvaa 64,128,160\nRa 192,10,49152\nMvaa 64,132,164\ns_add_u32 m0, s47, 32768\ns_add_u32 s44, s44, 0x40000\n"
"buffer_load_dwordx4 v[216:219], v21, s[40:43], s87 offen offset:0\nMvaa 68,140,164\nRa 196,11,49152\nMvaa 68,136,160\nv_readfirstlane_b32 s3, v49\nMvaa 128,144,160\n"
"Ra 200,10,51200\nMvaa 128,148,164\nD0 6,24,44,0\nMvaa 132,156,164\nRa 204,11,51200\nMvaa 132,152,160\ns_andn2_b32 s2, 0x7ff, s2\nMvaa 140,152,168\nRa 208,10,57344\n"
"Mvaa 140,156,172\ns_andn2_b32 s3, 0x7ff, s3\nMvaa 136,148,172\nRa 212,11,57344\nMvaa 136,144,168\ns_lshl_b32 s71, s3, 16\nMvaa 76,136,168\nRa 216,10,59392\nMvaa 76,140,172\n"
"D0 7,24,44,1024\nMvaa 72,132,172\nRa 220,11,59392\nMvaa 72,128,168\ns_or_b32 s71, s71, s2\nMvaa 80,128,176\ns_mul_i32 s2, s2, 896\nMvaa 80,132,180\ns_mul_i32 s3, s3, 896\n"
"Mvaa 84,140,180\ns_mul_i32 s6, s11, 224\nMvaa 84,136,176\ns_add_u32 s2, s2, s6\nMvaa 144,144,176\ns_add_u32 s3, s3, s6\nMvaa 144,148,180\nD0 8,24,44,2048\nMvaa 148,156,180\n"
"buffer_load_dwordx2 v[240:241], v255, s[28:31], s2 offen\nMvaa 148,152,176\nMvaa 156,152,184\nMvaa 156,156,188\nMvaa 152,148,188\n"
"buffer_load_dwordx2 v[242:243], v255, s[28:31], s3 offen\ns_lshl2_add_u32 s2, s10, s11\ns_cmp_eq_u32 s2, 512\ns_cselect_b32 s2, 0, 0x40000000\ns_add_u32 s2, s2, 0xc048000\n"
"Mvaa 152,144,184\nMvaa 92,136,184\nMvaa 92,140,188\nD0 9,24,44,3072\nMvaa 88,132,188\nbuffer_store_dword v25, off, s[16:19], s2 offset:0 sc0 sc1\nMvaa 88,128,184\nWL 0\n"
"Maaa 0,192,160\nRa 128,10,0\nMaaa 0,196,164\ns_add_u32 m0, s47, 49152\ns_add_u32 s44, s44, 0x40000\nMaaa 4,204,164\nRa 132,11,0\nMaaa 4,200,160\nMaaa 64,208,160\nRa 136,10,2048\n"
"Maaa 64,212,164\nD0 6,24,44,0\nMaaa 68,220,164\nRa 140,11,2048\nMaaa 68,216,160\nMaaa 76,216,168\nRa 144,10,8192\nMaaa 76,220,172\nMaaa 72,212,172\nRa 148,11,8192\n"
"Maaa 72,208,168\nMaaa 12,200,168\nRa 152,10,10240\nMaaa 12,204,172\nD0 7,24,44,1024\nMaaa 8,196,172\nRa 156,11,10240\nMaaa 8,192,168\nMaaa 16,192,176\nMaaa 16,196,180\n"
"Maaa 20,204,180\nMaaa 20,200,176\nMaaa 80,208,176\nMaaa 80,212,180\nD0 8,24,44,2048\nMaaa 84,220,180\nMaaa 84,216,176\nMaaa 92,216,184\nMaaa 92,220,188\nMaaa 88,212,188\n"
"Maaa 88,208,184\nMaaa 28,200,184\nMaaa 28,204,188\nD0 9,24,44,3072\nMaaa 24,196,188\nMaaa 24,192,184\ns_waitcnt vmcnt(21) lgkmcnt(0)\ns_barrier\nMaaa 32,192,224\nTa 160,12,0\n"
"Maaa 32,196,228\nTa 162,12,2048\nbuffer_load_dwordx4 v[204:207], v21, s[36:39], s87 offen offset:3584\nMaaa 36,204,228\ns_add_u32 m0, s47, -16384\ns_add_u32 s48, s48, 128\n"
"s_lshl_b32 s5, s73, 8\ns_add_u32 s5, s5, 0xc04ac00\nv_lshrrev_b32 v27, 2, v1\nbuffer_load_dword v27, v27, s[16:19], s5 offen sc0 sc1\nMaaa 36,200,224\nTa 164,12,16384\n"
"Maaa 96,208,224\nTa 166,12,18432\nMaaa 96,212,228\nD0 2,20,48,0\nMaaa 100,220,228\nTa 168,13,0\nMaaa 100,216,224\nTa 170,13,2048\nMaaa 108,216,232\nMaaa 108,220,236\n"
"Ta 172,13,16384\nMaaa 104,212,236\nTa 174,13,18432\nMaaa 104,208,232\nMaaa 44,200,232\nTa 176,14,0\nMaaa 44,204,236\nTa 178,14,2048\nMaaa 40,196,236\nD0 3,20,48,1024\n"
"Maaa 40,192,232\nTa 180,14,16384\nMaaa 48,192,240\nTa 182,14,18432\nMaaa 48,196,244\nMaaa 52,204,244\nTa 184,15,0\nMaaa 52,200,240\nTa 186,15,2048\nMaaa 112,208,240\n"
"Maaa 112,212,244\nTa 188,15,16384\nMaaa 116,220,244\nTa 190,15,18432\nMaaa 116,216,240\nD0 4,20,48,2048\nMaaa 124,216,248\nMaaa 124,220,252\nMaaa 120,212,252\nMaaa 120,208,248\n"
"Maaa 60,200,248\nMaaa 60,204,252\nD0 5,20,48,3072\nMaaa 56,196,252\nMaaa 56,192,248\nWL 0\nMvaa 64,128,160\nTa 224,16,0\nMvaa 64,132,164\nTa 226,16,2048\nMvaa 68,140,164\n"
"s_add_u32 m0, s47, -65536\ns_add_u32 s45, s45, 128\nMvaa 68,136,160\nTa 228,16,16384\nMvaa 128,144,160\nTa 230,16,18432\nMvaa 128,148,164\nD0 2,20,45,0\nMvaa 132,156,164\n"
"Ta 232,17,0\nMvaa 132,152,160\nTa 234,17,2048\nMvaa 140,152,168\nMvaa 140,156,172\nTa 236,17,16384\nMvaa 136,148,172\nTa 238,17,18432\nMvaa 136,144,168\nMvaa 76,136,168\n"
"Ta 240,18,0\nMvaa 76,140,172\nTa 242,18,2048\nMvaa 72,132,172\nD0 3,20,45,1024\nMvaa 72,128,168\nTa 244,18,16384\nMvaa 80,128,176\nTa 246,18,18432\nMvaa 80,132,180\n"
"Mvaa 84,140,180\nTa 248,19,0\nMvaa 84,136,176\nTa 250,19,2048\nMvaa 144,144,176\nMvaa 144,148,180\nTa 252,19,16384\nMvaa 148,156,180\nTa 254,19,18432\nMvaa 148,152,176\n"
"D0 4,20,45,2048\nMvaa 156,152,184\nMvaa 156,156,188\nMvaa 152,148,188\nMvaa 152,144,184\nMvaa 92,136,184\nMvaa 92,140,188\nD0 5,20,45,3072\nMvaa 88,132,188\nMvaa 88,128,184\n"
"s_waitcnt vmcnt(22) lgkmcnt(0)\ns_barrier\nMvaa 96,128,224\nRa 192,10,16384\nWV 16\nMvaa 96,132,228\ns_add_u32 m0, s47, 0\ns_add_u32 s44, s44, 0x40000\n"
"buffer_load_dwordx4 v[220:223], v21, s[40:43], s87 offen offset:3584\nMvaa 100,140,228\nRa 196,11,16384\nv_lshlrev_b32 v244, 16, v240\nMvaa 100,136,224\n"
"v_and_b32 v245, 0xffff0000, v240\nv_lshlrev_b32 v246, 16, v241\nMvaa 160,144,224\nRa 200,10,18432\nv_and_b32 v247, 0xffff0000, v241\nMvaa 160,148,228\nD0 6,24,44,0\n"
"v_lshlrev_b32 v248, 16, v242\nMvaa 164,156,228\n.W560:\ns_cbranch_execz .W564\nRa 204,11,18432\nMvaa 164,152,224\nv_and_b32 v249, 0xffff0000, v242\nv_lshlrev_b32 v250, 16, v243\n"
"Mvaa 172,152,232\nRa 208,10,24576\nv_and_b32 v251, 0xffff0000, v243\nMvaa 172,156,236\nv_lshlrev_b32 v252, 16, v192\nv_and_b32 v253, 0xffff0000, v192\nMvaa 168,148,236\n"
"Ra 212,11,24576\nMU 32,244,252\nMvaa 168,144,232\nMU 33,244,253\nMvaa 108,136,232\nRa 216,10,26624\nMU 40,248,252\nMvaa 108,140,236\nD0 7,24,44,1024\nMU 41,248,253\n"
"Mvaa 104,132,236\nRa 220,11,26624\nv_lshlrev_b32 v252, 16, v193\nMvaa 104,128,232\nv_and_b32 v253, 0xffff0000, v193\nMU 34,244,252\nMvaa 112,128,240\nMU 35,244,253\nMU 42,248,252\n"
"Mvaa 112,132,244\nMU 43,248,253\nMvaa 116,140,244\nv_lshlrev_b32 v252, 16, v194\nv_and_b32 v253, 0xffff0000, v194\nMvaa 116,136,240\nMU 36,244,252\nMU 37,244,253\n"
"Mvaa 176,144,240\nMU 44,248,252\nMU 45,248,253\nMvaa 176,148,244\nD0 8,24,44,2048\nv_lshlrev_b32 v252, 16, v195\nMvaa 180,156,244\nv_and_b32 v253, 0xffff0000, v195\n"
"Mvaa 180,152,240\nMU 38,244,252\nMU 39,244,253\nMvaa 188,152,248\nMU 46,248,252\nMU 47,248,253\nMvaa 188,156,252\nv_lshlrev_b32 v252, 16, v208\nv_and_b32 v253, 0xffff0000, v208\n"
"Mvaa 184,148,252\nMU 48,244,252\nMU 49,244,253\nMvaa 184,144,248\nMU 56,248,252\nMvaa 124,136,248\nMU 57,248,253\nv_lshlrev_b32 v252, 16, v209\nMvaa 124,140,252\nD0 9,24,44,3072\n"
"v_and_b32 v253, 0xffff0000, v209\nMvaa 120,132,252\nMU 50,244,252\nMU 51,244,253\nMvaa 120,128,248\nWL 0\nMaaa 32,192,224\nRa 128,10,32768\nMU 58,248,252\nMaaa 32,196,228\n"
"s_add_u32 m0, s47, 16384\ns_add_u32 s44, s44, 0x40000\nMaaa 36,204,228\nRa 132,11,32768\nMU 59,248,253\nMaaa 36,200,224\nv_lshlrev_b32 v252, 16, v210\n"
"v_and_b32 v253, 0xffff0000, v210\nMaaa 96,208,224\nRa 136,10,34816\nMU 52,244,252\nMaaa 96,212,228\nD0 6,24,44,0\nMU 53,244,253\nMaaa 100,220,228\nRa 140,11,34816\n"
"Maaa 100,216,224\nMU 60,248,252\nMU 61,248,253\nMaaa 108,216,232\nRa 144,10,40960\nv_lshlrev_b32 v252, 16, v211\nMaaa 108,220,236\nv_and_b32 v253, 0xffff0000, v211\nMU 54,244,252\n"
"Maaa 104,212,236\nRa 148,11,40960\nMU 55,244,253\nMaaa 104,208,232\nMU 62,248,252\nMaaa 44,200,232\nRa 152,10,43008\nMU 63,248,253\nMaaa 44,204,236\nD0 7,24,44,1024\n"
"v_lshlrev_b32 v252, 16, v196\nMaaa 40,196,236\nRa 156,11,43008\nv_and_b32 v253, 0xffff0000, v196\nMaaa 40,192,232\nFM 32,245,252\nFM 33,245,253\nMaaa 48,192,240\nFM 40,249,252\n"
"FM 41,249,253\nMaaa 48,196,244\nv_lshlrev_b32 v252, 16, v197\nMaaa 52,204,244\nv_and_b32 v253, 0xffff0000, v197\nFM 34,245,252\nMaaa 52,200,240\nFM 35,245,253\nFM 42,249,252\n"
"Maaa 112,208,240\nFM 43,249,253\nv_lshlrev_b32 v252, 16, v198\nMaaa 112,212,244\nD0 8,24,44,2048\nv_and_b32 v253, 0xffff0000, v198\nMaaa 116,220,244\nFM 36,245,252\n"
"Maaa 116,216,240\nFM 37,245,253\nFM 44,249,252\nMaaa 124,216,248\nFM 45,249,253\nv_lshlrev_b32 v252, 16, v199\nMaaa 124,220,252\nv_and_b32 v253, 0xffff0000, v199\nFM 38,245,252\n"
"Maaa 120,212,252\nFM 39,245,253\nFM 46,249,252\nMaaa 120,208,248\nFM 47,249,253\nMaaa 60,200,248\nv_lshlrev_b32 v252, 16, v212\nv_and_b32 v253, 0xffff0000, v212\nMaaa 60,204,252\n"
"D0 9,24,44,3072\nFM 48,245,252\nMaaa 56,196,252\nFM 49,245,253\nFM 56,249,252\nMaaa 56,192,248\ns_waitcnt vmcnt(19) lgkmcnt(0)\ns_barrier\nMaaa 0,192,160\nTa 224,16,32768\n"
"FM 57,249,253\nMaaa 0,196,164\nTa 226,16,34816\nbuffer_load_dwordx4 v[224:227], v20, s[92:95], s88 offen offset:0\nMaaa 4,204,164\ns_add_u32 m0, s47, -49152\n"
"s_add_u32 s48, s48, 128\nMaaa 4,200,160\nTa 228,16,49152\nv_lshlrev_b32 v252, 16, v213\nMaaa 64,208,160\nTa 230,16,51200\nv_and_b32 v253, 0xffff0000, v213\nMaaa 64,212,164\n"
"D0 2,20,48,0\nFM 50,245,252\nMaaa 68,220,164\nTa 232,17,32768\nMaaa 68,216,160\nTa 234,17,34816\nFM 51,245,253\nMaaa 76,216,168\nFM 58,249,252\nFM 59,249,253\nMaaa 76,220,172\n"
"Ta 236,17,49152\nv_lshlrev_b32 v252, 16, v214\nMaaa 72,212,172\nTa 238,17,51200\nv_and_b32 v253, 0xffff0000, v214\nMaaa 72,208,168\nFM 52,245,252\nMaaa 12,200,168\n"
"Ta 240,18,32768\nFM 53,245,253\nMaaa 12,204,172\nTa 242,18,34816\nFM 60,249,252\nMaaa 8,196,172\nD0 3,20,48,1024\nFM 61,249,253\nMaaa 8,192,168\nTa 244,18,49152\n"
"v_lshlrev_b32 v252, 16, v215\nMaaa 16,192,176\nTa 246,18,51200\nv_and_b32 v253, 0xffff0000, v215\nMaaa 16,196,180\nFM 54,245,252\nMaaa 20,204,180\nTa 248,19,32768\nFM 55,245,253\n"
"Maaa 20,200,176\nTa 250,19,34816\nFM 62,249,252\nMaaa 80,208,176\nFM 63,249,253\nv_lshlrev_b32 v252, 16, v200\nMaaa 80,212,180\nTa 252,19,49152\nv_and_b32 v253, 0xffff0000, v200\n"
"Maaa 84,220,180\nTa 254,19,51200\nMaaa 84,216,176\nD0 4,20,48,2048\nFM 32,246,252\nMaaa 92,216,184\nFM 33,246,253\nFM 40,250,252\nMaaa 92,220,188\nFM 41,250,253\n"
"v_lshlrev_b32 v252, 16, v201\nMaaa 88,212,188\nv_and_b32 v253, 0xffff0000, v201\nFM 34,246,252\nMaaa 88,208,184\nFM 35,246,253\nMaaa 28,200,184\nFM 42,250,252\nFM 43,250,253\n"
"Maaa 28,204,188\nD0 5,20,48,3072\nv_lshlrev_b32 v252, 16, v202\nMaaa 24,196,188\nv_and_b32 v253, 0xffff0000, v202\nFM 36,246,252\nMaaa 24,192,184\nWL 0\nMvaa 96,128,224\n"
"Ta 160,12,32768\nFM 37,246,253\nMvaa 96,132,228\nTa 162,12,34816\nbuffer_load_dwordx4 v[228:231], v20, s[92:95], s88 offen offset:896\nMvaa 100,140,228\ns_add_u32 m0, s47, -32768\n"
"s_add_u32 s45, s45, 128\nMvaa 100,136,224\nTa 164,12,49152\nFM 44,250,252\nMvaa 160,144,224\nTa 166,12,51200\nFM 45,250,253\nMvaa 160,148,228\nD0 2,20,45,0\n"
"v_lshlrev_b32 v252, 16, v203\nMvaa 164,156,228\nTa 168,13,32768\nMvaa 164,152,224\nTa 170,13,34816\nv_and_b32 v253, 0xffff0000, v203\nMvaa 172,152,232\nFM 38,246,252\n"
"FM 39,246,253\nMvaa 172,156,236\nTa 172,13,49152\nFM 46,250,252\nMvaa 168,148,236\nTa 174,13,51200\nFM 47,250,253\nMvaa 168,144,232\nv_lshlrev_b32 v252, 16, v216\n"
"Mvaa 108,136,232\nTa 176,14,32768\nv_and_b32 v253, 0xffff0000, v216\nMvaa 108,140,236\nTa 178,14,34816\nFM 48,246,252\nMvaa 104,132,236\nD0 3,20,45,1024\nFM 49,246,253\n"
"Mvaa 104,128,232\nTa 180,14,49152\nFM 56,250,252\nMvaa 112,128,240\nTa 182,14,51200\nFM 57,250,253\nMvaa 112,132,244\nv_lshlrev_b32 v252, 16, v217\nMvaa 116,140,244\n"
"Ta 184,15,32768\nv_and_b32 v253, 0xffff0000, v217\nMvaa 116,136,240\nTa 186,15,34816\nFM 50,246,252\nMvaa 176,144,240\nFM 51,246,253\nFM 58,250,252\nMvaa 176,148,244\n"
"Ta 188,15,49152\nFM 59,250,253\nMvaa 180,156,244\nTa 190,15,51200\nMvaa 180,152,240\nD0 4,20,45,2048\nv_lshlrev_b32 v252, 16, v218\nMvaa 188,152,248\n"
"v_and_b32 v253, 0xffff0000, v218\nFM 52,246,252\nMvaa 188,156,252\nFM 53,246,253\nFM 60,250,252\nMvaa 184,148,252\nFM 61,250,253\nv_lshlrev_b32 v252, 16, v219\nMvaa 184,144,248\n"
"v_and_b32 v253, 0xffff0000, v219\nMvaa 124,136,248\nFM 54,246,252\nWV 26\nv_xor_b32 v27, s15, v27\nv_and_b32 v255, 0xffffdfff, v27\nv_cmp_ne_u32 vcc, 0, v255\ns_nop 0\n"
"s_cbranch_vccnz .S148\n.S149:\nv_cmp_ne_u32 vcc, 0, v27\ns_nop 3\ns_bcnt1_i32_b64 s4, vcc\ns_cmp_gt_u32 s4, 32\ns_cselect_b32 s4, 1, 0\nMvaa 124,140,252\nD0 5,20,45,3072\n"
"s_xor_b32 s4, s4, s12\nMvaa 120,132,252\ns_and_b32 s4, s4, 1\ns_lshl_b32 s4, s4, 13\nMvaa 120,128,248\ns_waitcnt vmcnt(18) lgkmcnt(0)\ns_barrier\nMvaa 64,128,160\nRa 192,10,49152\n"
"s_mul_i32 s72, s73, 25165824\nMvaa 64,132,164\ns_add_u32 m0, s47, 32768\ns_add_u32 s44, s44, 0x40000\nbuffer_load_dwordx4 v[232:235], v20, s[92:95], s88 offen offset:1792\n"
"Mvaa 68,140,164\nRa 196,11,49152\ns_add_u32 s72, s72, s4\nMvaa 68,136,160\ns_and_b32 s4, s12, 1\ns_lshl_b32 s4, s4, 21\nMvaa 128,144,160\nRa 200,10,51200\ns_add_u32 s72, s72, s4\n"
"Mvaa 128,148,164\nD0 6,24,44,0\ns_lshl3_add_u32 s2, s13, s11\nMvaa 132,156,164\nRa 204,11,51200\nMvaa 132,152,160\ns_lshl_b32 s2, s2, 11\ns_lshl_b32 s3, s14, 22\nMvaa 140,152,168\n"
"Ra 208,10,57344\ns_add_u32 s2, s2, s3\nMvaa 140,156,172\ns_add_u32 s2, s2, s72\ns_add_u32 s49, s2, 0xc00000\nMvaa 136,148,172\nRa 212,11,57344\nFM 55,246,253\nMvaa 136,144,168\n"
"FM 62,250,252\nMvaa 76,136,168\nRa 216,10,59392\nFM 63,250,253\nMvaa 76,140,172\nD0 7,24,44,1024\nv_lshlrev_b32 v252, 16, v204\nMvaa 72,132,172\nRa 220,11,59392\n"
"v_and_b32 v253, 0xffff0000, v204\nMvaa 72,128,168\nFM 32,247,252\nFM 33,247,253\nMvaa 80,128,176\nFM 40,251,252\nFM 41,251,253\nMvaa 80,132,180\nv_lshlrev_b32 v252, 16, v205\n"
"Mvaa 84,140,180\nv_and_b32 v253, 0xffff0000, v205\nFM 34,247,252\nMvaa 84,136,176\nFM 35,247,253\nFM 42,251,252\nMvaa 144,144,176\nFM 43,251,253\nv_lshlrev_b32 v252, 16, v206\n"
"Mvaa 144,148,180\nD0 8,24,44,2048\nv_and_b32 v253, 0xffff0000, v206\nMvaa 148,156,180\nFM 36,247,252\nMvaa 148,152,176\nFM 37,247,253\nFM 44,251,252\nMvaa 156,152,184\n"
"FM 45,251,253\nv_lshlrev_b32 v252, 16, v207\nMvaa 156,156,188\nv_and_b32 v253, 0xffff0000, v207\nFM 38,247,252\nMvaa 152,148,188\nFM 39,247,253\nFM 46,251,252\nMvaa 152,144,184\n"
"FM 47,251,253\nMvaa 92,136,184\nv_lshlrev_b32 v252, 16, v220\nv_and_b32 v253, 0xffff0000, v220\nMvaa 92,140,188\nD0 9,24,44,3072\nFM 48,247,252\nMvaa 88,132,188\nFM 49,247,253\n"
"FM 56,251,252\nMvaa 88,128,184\nWL 0\nMaaa 0,192,160\nRa 128,10,0\nFM 57,251,253\nMaaa 0,196,164\ns_add_u32 m0, s47, 49152\ns_add_u32 s44, s44, 0x40000\n"
"buffer_load_dwordx4 v[236:239], v20, s[92:95], s88 offen offset:2688\nMaaa 4,204,164\nRa 132,11,0\nv_lshlrev_b32 v252, 16, v221\nMaaa 4,200,160\nv_and_b32 v253, 0xffff0000, v221\n"
"FM 50,247,252\nMaaa 64,208,160\nRa 136,10,2048\nFM 51,247,253\nMaaa 64,212,164\nD0 6,24,44,0\nFM 58,251,252\nMaaa 68,220,164\nRa 140,11,2048\nMaaa 68,216,160\nFM 59,251,253\n"
"v_lshlrev_b32 v252, 16, v222\nMaaa 76,216,168\nRa 144,10,8192\nv_and_b32 v253, 0xffff0000, v222\nMaaa 76,220,172\nFM 52,247,252\nFM 53,247,253\nMaaa 72,212,172\nRa 148,11,8192\n"
"FM 60,251,252\nMaaa 72,208,168\nFM 61,251,253\nMaaa 12,200,168\nRa 152,10,10240\nv_lshlrev_b32 v252, 16, v223\nMaaa 12,204,172\nD0 7,24,44,1024\nv_and_b32 v253, 0xffff0000, v223\n"
"Maaa 8,196,172\nRa 156,11,10240\nFM 54,247,252\nMaaa 8,192,168\nFM 55,247,253\nFM 62,251,252\nMaaa 16,192,176\nFM 63,251,253\nMaaa 16,196,180\nv_permlane32_swap_b32 v32, v48\n"
"Maaa 20,204,180\nv_permlane32_swap_b32 v33, v49\nv_permlane32_swap_b32 v34, v50\nMaaa 20,200,176\nv_permlane32_swap_b32 v35, v51\nv_permlane32_swap_b32 v36, v52\nMaaa 80,208,176\n"
"v_permlane32_swap_b32 v37, v53\nv_permlane32_swap_b32 v38, v54\nMaaa 80,212,180\nD0 8,24,44,2048\nv_permlane32_swap_b32 v39, v55\nMaaa 84,220,180\nv_permlane32_swap_b32 v40, v56\n"
"Maaa 84,216,176\nv_permlane32_swap_b32 v41, v57\nv_permlane32_swap_b32 v42, v58\nMaaa 92,216,184\nv_permlane32_swap_b32 v43, v59\nv_permlane32_swap_b32 v44, v60\nMaaa 92,220,188\n"
"v_permlane32_swap_b32 v45, v61\nv_permlane32_swap_b32 v46, v62\nMaaa 88,212,188\nv_permlane32_swap_b32 v47, v63\nAD 32,32,48\nMaaa 88,208,184\nAD 33,33,49\nMaaa 28,200,184\n"
"AD 34,34,50\nAD 35,35,51\nMaaa 28,204,188\nD0 9,24,44,3072\nAD 36,36,52\nMaaa 24,196,188\nAD 37,37,53\nAD 38,38,54\nMaaa 24,192,184\ns_waitcnt vmcnt(20) lgkmcnt(0)\ns_barrier\n"
"Maaa 32,192,224\nTa 160,12,0\nAD 39,39,55\nMaaa 32,196,228\nTa 162,12,2048\nMaaa 36,204,228\ns_add_u32 m0, s47, -16384\ns_add_u32 s48, s48, 128\nMaaa 36,200,224\nTa 164,12,16384\n"
"AD 40,40,56\nMaaa 96,208,224\nTa 166,12,18432\nAD 41,41,57\nMaaa 96,212,228\nD0 2,20,48,0\nAD 42,42,58\nMaaa 100,220,228\nTa 168,13,0\nMaaa 100,216,224\nTa 170,13,2048\n"
"AD 43,43,59\nMaaa 108,216,232\nAD 44,44,60\nAD 45,45,61\nMaaa 108,220,236\nTa 172,13,16384\nAD 46,46,62\nMaaa 104,212,236\nTa 174,13,18432\nAD 47,47,63\nMaaa 104,208,232\n"
"v_permlane16_swap_b32 v32, v40\nMaaa 44,200,232\nTa 176,14,0\nv_permlane16_swap_b32 v33, v41\nMaaa 44,204,236\nTa 178,14,2048\nv_permlane16_swap_b32 v34, v42\nMaaa 40,196,236\n"
"D0 3,20,48,1024\nv_permlane16_swap_b32 v35, v43\nMaaa 40,192,232\nTa 180,14,16384\nv_permlane16_swap_b32 v36, v44\nMaaa 48,192,240\nTa 182,14,18432\n"
"v_permlane16_swap_b32 v37, v45\nMaaa 48,196,244\nv_permlane16_swap_b32 v38, v46\nMaaa 52,204,244\nTa 184,15,0\nv_permlane16_swap_b32 v39, v47\nMaaa 52,200,240\nTa 186,15,2048\n"
"AD 32,32,40\nMaaa 112,208,240\nAD 33,33,41\nAD 34,34,42\nMaaa 112,212,244\nTa 188,15,16384\nAD 35,35,43\nMaaa 116,220,244\nTa 190,15,18432\nMaaa 116,216,240\nD0 4,20,48,2048\n"
"AD 36,36,44\nMaaa 124,216,248\nAD 37,37,45\nAD 38,38,46\nMaaa 124,220,252\nAD 39,39,47\nv_add_f32_dpp v32, v32, v32 row_ror:8 row_mask:0xf bank_mask:0xf\nMaaa 120,212,252\n"
"v_add_f32_dpp v33, v33, v33 row_ror:8 row_mask:0xf bank_mask:0xf\nv_add_f32_dpp v34, v34, v34 row_ror:8 row_mask:0xf bank_mask:0xf\nMaaa 120,208,248\n"
"v_add_f32_dpp v35, v35, v35 row_ror:8 row_mask:0xf bank_mask:0xf\nMaaa 60,200,248\nv_add_f32_dpp v36, v36, v36 row_ror:8 row_mask:0xf bank_mask:0xf\n"
"v_add_f32_dpp v37, v37, v37 row_ror:8 row_mask:0xf bank_mask:0xf\nMaaa 60,204,252\nD0 5,20,48,3072\nv_add_f32_dpp v38, v38, v38 row_ror:8 row_mask:0xf bank_mask:0xf\n"
"Maaa 56,196,252\nv_add_f32_dpp v39, v39, v39 row_ror:8 row_mask:0xf bank_mask:0xf\nv_add_f32_dpp v32, v32, v32 row_ror:4 row_mask:0xf bank_mask:0xf\nMaaa 56,192,248\nWL 0\n"
"Mvaa 64,128,160\nTa 224,16,0\nv_add_f32_dpp v33, v33, v33 row_ror:4 row_mask:0xf bank_mask:0xf\nMvaa 64,132,164\nTa 226,16,2048\nMvaa 68,140,164\ns_add_u32 m0, s47, -65536\n"
"s_add_u32 s45, s45, 128\n.W561:\ns_cbranch_execz .W565\nMvaa 68,136,160\nTa 228,16,16384\nv_add_f32_dpp v34, v34, v34 row_ror:4 row_mask:0xf bank_mask:0xf\nMvaa 128,144,160\n"
"Ta 230,16,18432\nv_add_f32_dpp v35, v35, v35 row_ror:4 row_mask:0xf bank_mask:0xf\nMvaa 128,148,164\nD0 2,20,45,0\n"
"v_add_f32_dpp v36, v36, v36 row_ror:4 row_mask:0xf bank_mask:0xf\nMvaa 132,156,164\nTa 232,17,0\nMvaa 132,152,160\nTa 234,17,2048\n"
"v_add_f32_dpp v37, v37, v37 row_ror:4 row_mask:0xf bank_mask:0xf\nMvaa 140,152,168\nv_add_f32_dpp v38, v38, v38 row_ror:4 row_mask:0xf bank_mask:0xf\n"
"v_add_f32_dpp v39, v39, v39 row_ror:4 row_mask:0xf bank_mask:0xf\nMvaa 140,156,172\nTa 236,17,16384\nv_add_f32_dpp v32, v32, v32 row_ror:2 row_mask:0xf bank_mask:0xf\n"
"Mvaa 136,148,172\nTa 238,17,18432\nv_add_f32_dpp v33, v33, v33 row_ror:2 row_mask:0xf bank_mask:0xf\nMvaa 136,144,168\n"
"v_add_f32_dpp v34, v34, v34 row_ror:2 row_mask:0xf bank_mask:0xf\nMvaa 76,136,168\nTa 240,18,0\nv_add_f32_dpp v35, v35, v35 row_ror:2 row_mask:0xf bank_mask:0xf\nMvaa 76,140,172\n"
"Ta 242,18,2048\nv_add_f32_dpp v36, v36, v36 row_ror:2 row_mask:0xf bank_mask:0xf\nMvaa 72,132,172\nD0 3,20,45,1024\n"
"v_add_f32_dpp v37, v37, v37 row_ror:2 row_mask:0xf bank_mask:0xf\nMvaa 72,128,168\nTa 244,18,16384\nv_add_f32_dpp v38, v38, v38 row_ror:2 row_mask:0xf bank_mask:0xf\n"
"Mvaa 80,128,176\nTa 246,18,18432\nv_add_f32_dpp v39, v39, v39 row_ror:2 row_mask:0xf bank_mask:0xf\nMvaa 80,132,180\nv_lshrrev_b32 v48, 4, v0\nMvaa 84,140,180\nTa 248,19,0\n"
"v_and_b32 v49, 1, v0\nMvaa 84,136,176\nTa 250,19,2048\nv_lshlrev_b32 v49, 5, v49\nMvaa 144,144,176\nv_lshl_add_u32 v49, v48, 6, v49\ns_lshl_b32 s2, s11, 8\nMvaa 144,148,180\n"
"Ta 252,19,16384\ns_add_u32 s2, s2, 132096\nMvaa 148,156,180\nTa 254,19,18432\nMvaa 148,152,176\nD0 4,20,45,2048\nv_add_u32 v49, s2, v49\nMvaa 156,152,184\n"
"ds_write_b128 v49, v[32:35]\nds_write_b128 v49, v[36:39] offset:16\nMvaa 156,156,188\nv_and_b32 v50, 31, v0\nv_lshlrev_b32 v50, 2, v50\nMvaa 152,148,188\ns_lshl_b32 s2, s11, 10\n"
"s_add_u32 s2, s2, 133120\nMvaa 152,144,184\nv_add_u32 v51, s2, v50\nMvaa 92,136,184\nv_add_u32 v50, 132096, v50\nv_lshl_add_u32 v52, v48, 4, s2\nMvaa 92,140,188\nD0 5,20,45,3072\n"
"Mvaa 88,132,188\nMvaa 88,128,184\ns_waitcnt vmcnt(18) lgkmcnt(0)\ns_barrier\nMvaa 96,128,224\nRa 192,10,16384\nds_read_b32 v32, v50 offset:0\nMvaa 96,132,228\n"
"s_add_u32 m0, s47, 0\ns_add_u32 s44, s44, 0x40000\nMvaa 100,140,228\nRa 196,11,16384\nMvaa 100,136,224\nds_read_b32 v33, v50 offset:256\nds_read_b32 v34, v50 offset:512\n"
"Mvaa 160,144,224\nRa 200,10,18432\nMvaa 160,148,228\nD0 6,24,44,0\nMvaa 164,156,228\nRa 204,11,18432\nds_read_b32 v35, v50 offset:768\nMvaa 164,152,224\n"
"ds_read_b32 v36, v50 offset:128\nMvaa 172,152,232\nRa 208,10,24576\nds_read_b32 v37, v50 offset:384\nMvaa 172,156,236\nds_read_b32 v38, v50 offset:640\nMvaa 168,148,236\n"
"Ra 212,11,24576\nMvaa 168,144,232\nds_read_b32 v39, v50 offset:896\nMvaa 108,136,232\nRa 216,10,26624\nMvaa 108,140,236\nD0 7,24,44,1024\nMvaa 104,132,236\nRa 220,11,26624\n"
"Mvaa 104,128,232\nMvaa 112,128,240\nMvaa 112,132,244\nMvaa 116,140,244\nMvaa 116,136,240\nMvaa 176,144,240\nMvaa 176,148,244\nD0 8,24,44,2048\nMvaa 180,156,244\nWL 2\nAD 32,32,33\n"
"Mvaa 180,152,240\nAD 34,34,35\nMvaa 188,152,248\nAD 36,36,37\nAD 38,38,39\nMvaa 188,156,252\nAD 32,32,34\nMvaa 184,148,252\nAD 36,36,38\nMvaa 184,144,248\nCV 32,32,36\n"
"v_and_b32 v36, 0xffff0000, v32\nMvaa 124,136,248\nv_lshlrev_b32 v32, 16, v32\nMvaa 124,140,252\nD0 9,24,44,3072\nMvaa 120,132,252\nv_mul_f32 v33, 0xbfb8aa3b, v32\n"
"Mvaa 120,128,248\nWL 0\nMaaa 32,192,224\nRa 128,10,32768\nMaaa 32,196,228\ns_add_u32 m0, s47, 16384\ns_add_u32 s44, s44, 0x40000\nMaaa 36,204,228\nRa 132,11,32768\nEX 33,33\n"
"Maaa 36,200,224\nv_add_f32 v33, 1.0, v33\nMaaa 96,208,224\nRa 136,10,34816\nv_rcp_f32 v33, v33\nMaaa 96,212,228\nD0 6,24,44,0\nMaaa 100,220,228\nRa 140,11,34816\nMU 33,32,33\n"
"Maaa 100,216,224\nCV 33,33,33\nMaaa 108,216,232\nRa 144,10,40960\nv_lshlrev_b32 v33, 16, v33\nMaaa 108,220,236\nMU 33,33,36\nMaaa 104,212,236\nRa 148,11,40960\nMaaa 104,208,232\n"
"CV 33,33,33\nv_lshlrev_b32 v33, 16, v33\nMaaa 44,200,232\nRa 152,10,43008\nMaaa 44,204,236\nD0 7,24,44,1024\nMaaa 40,196,236\nRa 156,11,43008\nds_write_b32 v51, v33\n"
"Maaa 40,192,232\nRv 244,52,0\nMaaa 48,192,240\nRv 248,52,64\ns_and_b32 s2, s74, 15\nMaaa 48,196,244\ns_sub_u32 s2, s2, 2\ns_cmp_lt_u32 s2, 14\ns_cselect_b32 s5, 0, 0x40000000\n"
"Maaa 52,204,244\ns_and_b32 s2, s2, 15\nMaaa 52,200,240\ns_mul_i32 s4, s73, 14\ns_add_u32 s4, s4, s2\nMaaa 112,208,240\nv_and_b32 v49, 15, v0\nMaaa 112,212,244\nD0 8,24,44,2048\n"
"Maaa 116,220,244\nv_lshlrev_b32 v50, 3, v49\nv_and_b32 v51, 1, v48\nMaaa 116,216,240\nv_lshl_add_u32 v50, v51, 2, v50\nMaaa 124,216,248\ns_mul_i32 s2, s11, 112\n"
"v_add_u32 v50, s2, v50\nMaaa 124,220,252\nv_lshrrev_b32 v51, 4, v50\nMaaa 120,212,252\nv_lshrrev_b32 v48, 1, v48\nMaaa 120,208,248\nv_mad_u32_u24 v51, v48, 28, v51\n"
"v_mul_u32_u24 v51, 7168, v51\nMaaa 60,200,248\nv_and_b32 v50, 15, v50\nMaaa 60,204,252\nD0 9,24,44,3072\nMaaa 56,196,252\nv_lshl_add_u32 v51, v50, 2, v51\n"
"v_cmp_gt_u32 vcc, 14, v49\ns_lshl_b32 s4, s4, 6\ns_add_u32 s4, s4, 0xc04c800\nv_cndmask_b32 v51, v254, v51, vcc\nMaaa 56,192,248\ns_waitcnt vmcnt(16) lgkmcnt(3)\ns_barrier\n"
"Maaa 0,192,160\nTa 224,16,32768\ns_or_b32 s4, s4, s5\nMaaa 0,196,164\nTa 226,16,34816\nMaaa 4,204,164\ns_add_u32 m0, s47, -49152\ns_add_u32 s48, s48, 128\nMaaa 4,200,160\n"
"Ta 228,16,49152\nWL 3\nMaaa 64,208,160\nTa 230,16,51200\nMaaa 64,212,164\nD0 2,20,48,0\nMaaa 68,220,164\nTa 232,17,32768\nv_lshlrev_b32 v252, 16, v224\nMaaa 68,216,160\n"
"Ta 234,17,34816\nMaaa 76,216,168\nv_and_b32 v253, 0xffff0000, v224\nMU 32,244,252\nMaaa 76,220,172\nTa 236,17,49152\nMaaa 72,212,172\nTa 238,17,51200\nMaaa 72,208,168\n"
"MU 33,244,253\nMU 40,248,252\nMaaa 12,200,168\nTa 240,18,32768\nMaaa 12,204,172\nTa 242,18,34816\nMaaa 8,196,172\nD0 3,20,48,1024\nMU 41,248,253\nMaaa 8,192,168\nTa 244,18,49152\n"
"Maaa 16,192,176\nTa 246,18,51200\nv_lshlrev_b32 v252, 16, v225\nMaaa 16,196,180\nv_and_b32 v253, 0xffff0000, v225\nMaaa 20,204,180\nTa 248,19,32768\nMaaa 20,200,176\n"
"Ta 250,19,34816\nMU 34,244,252\nMaaa 80,208,176\nMU 35,244,253\nMaaa 80,212,180\nTa 252,19,49152\nMaaa 84,220,180\nTa 254,19,51200\nMU 42,248,252\nMaaa 84,216,176\n"
"D0 4,20,48,2048\nMaaa 92,216,184\nMU 43,248,253\nv_lshlrev_b32 v252, 16, v226\nMaaa 92,220,188\nv_and_b32 v253, 0xffff0000, v226\nMaaa 88,212,188\nMU 36,244,252\nMaaa 88,208,184\n"
"MU 37,244,253\nMU 44,248,252\nMaaa 28,200,184\nMU 45,248,253\nMaaa 28,204,188\nD0 5,20,48,3072\nMaaa 24,196,188\nv_lshlrev_b32 v252, 16, v227\nv_and_b32 v253, 0xffff0000, v227\n"
"Maaa 24,192,184\nWL 0\nMvaa 96,128,224\nTa 160,12,32768\nMU 38,244,252\nMvaa 96,132,228\nTa 162,12,34816\nMvaa 100,140,228\ns_add_u32 m0, s47, -32768\ns_add_u32 s45, s45, 128\n"
"Mvaa 100,136,224\nTa 164,12,49152\nMU 39,244,253\nMvaa 160,144,224\nTa 166,12,51200\nMvaa 160,148,228\nD0 2,20,45,0\nMvaa 164,156,228\nTa 168,13,32768\nMU 46,248,252\n"
"Mvaa 164,152,224\nTa 170,13,34816\nMvaa 172,152,232\nMU 47,248,253\nv_lshlrev_b32 v252, 16, v228\nMvaa 172,156,236\nTa 172,13,49152\nMvaa 168,148,236\nTa 174,13,51200\n"
"Mvaa 168,144,232\nv_and_b32 v253, 0xffff0000, v228\nFM 32,245,252\nMvaa 108,136,232\nTa 176,14,32768\nMvaa 108,140,236\nTa 178,14,34816\nMvaa 104,132,236\nD0 3,20,45,1024\n"
"FM 33,245,253\nMvaa 104,128,232\nTa 180,14,49152\nMvaa 112,128,240\nTa 182,14,51200\nFM 40,249,252\nMvaa 112,132,244\nFM 41,249,253\nMvaa 116,140,244\nTa 184,15,32768\n"
"Mvaa 116,136,240\nTa 186,15,34816\nv_lshlrev_b32 v252, 16, v229\nMvaa 176,144,240\nv_and_b32 v253, 0xffff0000, v229\nMvaa 176,148,244\nTa 188,15,49152\nMvaa 180,156,244\n"
"Ta 190,15,51200\nFM 34,245,252\nMvaa 180,152,240\nD0 4,20,45,2048\nMvaa 188,152,248\nFM 35,245,253\nFM 42,249,252\nMvaa 188,156,252\nFM 43,249,253\nMvaa 184,148,252\n"
"v_lshlrev_b32 v252, 16, v230\nMvaa 184,144,248\nv_and_b32 v253, 0xffff0000, v230\nFM 36,245,252\nMvaa 124,136,248\nFM 37,245,253\nMvaa 124,140,252\nD0 5,20,45,3072\n"
"Mvaa 120,132,252\nFM 44,249,252\nFM 45,249,253\nMvaa 120,128,248\ns_waitcnt vmcnt(16) lgkmcnt(0)\ns_barrier\nMvaa 64,128,160\nRa 192,10,49152\nv_lshlrev_b32 v252, 16, v231\n"
"Mvaa 64,132,164\ns_add_u32 m0, s47, 32768\ns_add_u32 s44, s44, 0x40000\nMvaa 68,140,164\nRa 196,11,49152\nMvaa 68,136,160\nv_and_b32 v253, 0xffff0000, v231\nFM 38,245,252\n"
"Mvaa 128,144,160\nRa 200,10,51200\nMvaa 128,148,164\nD0 6,24,44,0\nMvaa 132,156,164\nRa 204,11,51200\nFM 39,245,253\nMvaa 132,152,160\nFM 46,249,252\nMvaa 140,152,168\n"
"Ra 208,10,57344\nFM 47,249,253\nMvaa 140,156,172\nv_lshlrev_b32 v252, 16, v232\nMvaa 136,148,172\nRa 212,11,57344\nMvaa 136,144,168\nv_and_b32 v253, 0xffff0000, v232\n"
"FM 32,246,252\nMvaa 76,136,168\nRa 216,10,59392\nMvaa 76,140,172\nD0 7,24,44,1024\nMvaa 72,132,172\nRa 220,11,59392\nFM 33,246,253\nMvaa 72,128,168\nFM 40,250,252\n"
"Mvaa 80,128,176\nFM 41,250,253\nv_lshlrev_b32 v252, 16, v233\nMvaa 80,132,180\nv_and_b32 v253, 0xffff0000, v233\nMvaa 84,140,180\nFM 34,246,252\nMvaa 84,136,176\nFM 35,246,253\n"
"FM 42,250,252\nMvaa 144,144,176\nFM 43,250,253\nMvaa 144,148,180\nD0 8,24,44,2048\nMvaa 148,156,180\nv_lshlrev_b32 v252, 16, v234\nv_and_b32 v253, 0xffff0000, v234\n"
"Mvaa 148,152,176\nFM 36,246,252\nMvaa 156,152,184\nFM 37,246,253\nFM 44,250,252\nMvaa 156,156,188\nFM 45,250,253\nMvaa 152,148,188\nv_lshlrev_b32 v252, 16, v235\nMvaa 152,144,184\n"
"v_and_b32 v253, 0xffff0000, v235\nFM 38,246,252\nMvaa 92,136,184\nFM 39,246,253\nMvaa 92,140,188\nD0 9,24,44,3072\nMvaa 88,132,188\nFM 46,250,252\nFM 47,250,253\nMvaa 88,128,184\n"
"WL 0\nMaaa 0,192,160\nRa 128,10,0\nv_lshlrev_b32 v252, 16, v236\nMaaa 0,196,164\ns_add_u32 m0, s47, 49152\ns_add_u32 s44, s44, 0x40000\nMaaa 4,204,164\nRa 132,11,0\n"
"Maaa 4,200,160\nv_and_b32 v253, 0xffff0000, v236\nFM 32,247,252\nMaaa 64,208,160\nRa 136,10,2048\nMaaa 64,212,164\nD0 6,24,44,0\nMaaa 68,220,164\nRa 140,11,2048\nFM 33,247,253\n"
"Maaa 68,216,160\nFM 40,251,252\nMaaa 76,216,168\nRa 144,10,8192\nFM 41,251,253\nMaaa 76,220,172\nv_lshlrev_b32 v252, 16, v237\nMaaa 72,212,172\nRa 148,11,8192\nMaaa 72,208,168\n"
"v_and_b32 v253, 0xffff0000, v237\nFM 34,247,252\nMaaa 12,200,168\nRa 152,10,10240\nMaaa 12,204,172\nD0 7,24,44,1024\nMaaa 8,196,172\nRa 156,11,10240\nFM 35,247,253\n"
"Maaa 8,192,168\nFM 42,251,252\nMaaa 16,192,176\nFM 43,251,253\nv_lshlrev_b32 v252, 16, v238\nMaaa 16,196,180\nv_and_b32 v253, 0xffff0000, v238\nMaaa 20,204,180\nFM 36,247,252\n"
"Maaa 20,200,176\nFM 37,247,253\nFM 44,251,252\nMaaa 80,208,176\nFM 45,251,253\nMaaa 80,212,180\nD0 8,24,44,2048\nMaaa 84,220,180\nv_lshlrev_b32 v252, 16, v239\n"
"v_and_b32 v253, 0xffff0000, v239\nMaaa 84,216,176\nFM 38,247,252\nMaaa 92,216,184\nFM 39,247,253\nFM 46,251,252\nMaaa 92,220,188\nFM 47,251,253\nMaaa 88,212,188\n"
"v_permlane32_swap_b32 v32, v40\nMaaa 88,208,184\nv_permlane32_swap_b32 v33, v41\nv_permlane32_swap_b32 v34, v42\nMaaa 28,200,184\nv_permlane32_swap_b32 v35, v43\nMaaa 28,204,188\n"
"D0 9,24,44,3072\nMaaa 24,196,188\nv_permlane32_swap_b32 v36, v44\nv_permlane32_swap_b32 v37, v45\nMaaa 24,192,184\nWV 16\ns_barrier\nMaaa 32,192,224\nTa 160,12,0\n"
"v_permlane32_swap_b32 v38, v46\nMaaa 32,196,228\nTa 162,12,2048\nMaaa 36,204,228\ns_add_u32 m0, s47, -16384\ns_add_u32 s48, s48, 128\nMaaa 36,200,224\nTa 164,12,16384\n"
"v_permlane32_swap_b32 v39, v47\nMaaa 96,208,224\nTa 166,12,18432\nMaaa 96,212,228\nD0 2,20,48,0\nMaaa 100,220,228\nTa 168,13,0\nAD 32,32,40\nMaaa 100,216,224\nTa 170,13,2048\n"
"Maaa 108,216,232\nAD 33,33,41\nAD 34,34,42\nMaaa 108,220,236\nTa 172,13,16384\nMaaa 104,212,236\nTa 174,13,18432\nMaaa 104,208,232\nAD 35,35,43\nAD 36,36,44\nMaaa 44,200,232\n"
"Ta 176,14,0\nMaaa 44,204,236\nTa 178,14,2048\nMaaa 40,196,236\nD0 3,20,48,1024\nAD 37,37,45\nMaaa 40,192,232\nTa 180,14,16384\nMaaa 48,192,240\nTa 182,14,18432\nAD 38,38,46\n"
"Maaa 48,196,244\nAD 39,39,47\nMaaa 52,204,244\nTa 184,15,0\nMaaa 52,200,240\nTa 186,15,2048\nv_permlane16_swap_b32 v32, v36\n.W562:\ns_cbranch_execz .W566\nMaaa 112,208,240\n"
"v_permlane16_swap_b32 v33, v37\nMaaa 112,212,244\nTa 188,15,16384\nMaaa 116,220,244\nTa 190,15,18432\nv_permlane16_swap_b32 v34, v38\nMaaa 116,216,240\nD0 4,20,48,2048\n"
"Maaa 124,216,248\nv_permlane16_swap_b32 v35, v39\nAD 32,32,36\nMaaa 124,220,252\nAD 33,33,37\nMaaa 120,212,252\nAD 34,34,38\nMaaa 120,208,248\nAD 35,35,39\nSv 32,51,16,4,0\n"
"Maaa 60,200,248\nMaaa 60,204,252\nD0 5,20,48,3072\nMaaa 56,196,252\nMaaa 56,192,248\nWL 0\nMvaa 64,128,160\nTa 224,16,0\ns_lshr_b32 s3, s11, 1\nMvaa 64,132,164\nTa 226,16,2048\n"
"s_add_u32 s6, s56, 16384\nMvaa 68,140,164\ns_lshl_b32 s3, s3, 13\nMvaa 68,136,160\nTa 228,16,16384\nbuffer_load_dwordx4 v[224:227], v1, s[16:19], s6 offen offset:0 sc1\n"
"Mvaa 128,144,160\nTa 230,16,18432\nMvaa 128,148,164\ns_lshl_b32 s2, s11, 12\nMvaa 132,156,164\nTa 232,17,0\nMvaa 132,152,160\nTa 234,17,2048\nMvaa 140,152,168\n"
"s_add_u32 s2, s2, s3\ns_add_u32 s2, s2, s72\nMvaa 140,156,172\nTa 236,17,16384\nbuffer_load_dwordx4 v[228:231], v1, s[16:19], s6 offen offset:1024 sc1\nMvaa 136,148,172\n"
"Ta 238,17,18432\nMvaa 136,144,168\ns_lshl_b32 s3, s11, 12\nMvaa 76,136,168\nTa 240,18,0\nMvaa 76,140,172\nTa 242,18,2048\nMvaa 72,132,172\ns_add_u32 s89, s3, 0x0\nMvaa 72,128,168\n"
"Ta 244,18,16384\nbuffer_load_dwordx4 v[232:235], v1, s[16:19], s6 offen offset:2048 sc1\nMvaa 80,128,176\nTa 246,18,18432\ns_mov_b32 s88, 0xffff8000\nMvaa 80,132,180\n"
"s_lshl_b32 s4, s13, 1\nMvaa 84,140,180\nTa 248,19,0\nMvaa 84,136,176\nTa 250,19,2048\nMvaa 144,144,176\ns_sub_u32 s79, 15, s4\nMvaa 144,148,180\nTa 252,19,16384\n"
"buffer_load_dwordx4 v[236:239], v1, s[16:19], s6 offen offset:3072 sc1\nMvaa 148,156,180\nTa 254,19,18432\nMvaa 148,152,176\ns_add_u32 s96, s2, 0x1310000\nMvaa 156,152,184\n"
"s_mov_b32 s63, 0x53800000\ns_mov_b32 s64, 0x2b800000\nMvaa 156,156,188\ns_sub_u32 s98, s4, 1\ns_add_u32 s6, s56, 20480\nMvaa 152,148,188\ns_lshl_b32 s5, s13, 5\nMvaa 152,144,184\n"
"s_add_u32 s5, s5, s11\nbuffer_load_dwordx4 v[240:243], v1, s[16:19], s6 offen offset:0 sc1\nMvaa 92,136,184\ns_lshl_b32 s65, s5, 9\nMvaa 92,140,188\ns_add_u32 s65, s65, s72\n"
"Mvaa 88,132,188\ns_add_u32 s65, s65, 0x1580000\nMvaa 88,128,184\ns_waitcnt vmcnt(18) lgkmcnt(0)\ns_barrier\nMvaa 96,128,224\nRa 192,10,16384\ns_and_b32 s66, s72, 0x2000\n"
"Mvaa 96,132,228\ns_add_u32 s66, s66, s56\nbuffer_load_dwordx4 v[244:247], v1, s[16:19], s6 offen offset:1024 sc1\nMvaa 100,140,228\nRa 196,11,16384\nMvaa 100,136,224\n"
"s_add_u32 s66, s66, 0x100000\nMvaa 160,144,224\nRa 200,10,18432\nMvaa 160,148,228\ns_and_b32 s5, s12, 1\nMvaa 164,156,228\nRa 204,11,18432\nMvaa 164,152,224\n"
"s_mul_i32 s58, s5, 0x202000\nbuffer_load_dwordx4 v[248:251], v1, s[16:19], s6 offen offset:2048 sc1\nMvaa 172,152,232\nRa 208,10,24576\ns_xor_b32 s58, s58, s72\nMvaa 172,156,236\n"
"s_add_u32 s58, s58, 0x15c0000\nMvaa 168,148,236\nRa 212,11,24576\nMvaa 168,144,232\ns_lshl_b32 s5, s5, 8\nMvaa 108,136,232\nRa 216,10,26624\nMvaa 108,140,236\n"
"s_add_u32 s61, s58, s5\nbuffer_load_dwordx4 v[252:255], v1, s[16:19], s6 offen offset:3072 sc1\nMvaa 104,132,236\nRa 220,11,26624\nMvaa 104,128,232\ns_lshl4_add_u32 s60, s13, s61\n"
"Mvaa 112,128,240\ns_add_u32 s54, s60, 0\ns_xor_b32 s60, s54, 0x100\nMvaa 112,132,244\ns_cmp_eq_u32 s11, 0\ns_cselect_b32 s54, s54, 0x40000000\ns_add_u32 s6, s56, 0\n"
"Mvaa 116,140,244\ns_getpc_b64 s[8:9]\n.PC168:\ns_add_u32 s8, s8, .HD144-.PC168\ns_addc_u32 s9, s9, 0\nMvaa 116,136,240\ns_mov_b32 s97, 0xff800000\n"
"buffer_load_dwordx4 v[192:195], v1, s[16:19], s6 offen offset:0 sc1\nMvaa 176,144,240\ns_mov_b32 s90, 3\ns_cmp_lt_u32 s11, 2\ns_cselect_b32 s92, 2, 0\nMvaa 176,148,244\n"
"s_add_u32 s86, s2, 0x20000\nMvaa 180,156,244\ns_add_u32 s87, s2, 0x410000\ns_cmp_eq_u32 s13, 7\ns_cselect_b32 s92, s92, 0\nMvaa 180,152,240\ns_cmp_gt_u32 s13, 5\n"
"s_cselect_b32 s4, s12, 15\nMvaa 188,152,248\ns_cmp_lt_u32 s4, 14\ns_cselect_b32 s5, s11, 3\nMvaa 188,156,252\ns_cmp_lt_u32 s5, 2\ns_cselect_b32 s91, 1, 100\n"
"buffer_load_dwordx4 v[196:199], v1, s[16:19], s6 offen offset:1024 sc1\nMvaa 184,148,252\nv_and_b32 v27, 0x70, v1\n"
"buffer_load_dword v27, v27, s[16:19], s61 offen offset:128 sc0 sc1\nMvaa 184,144,248\ns_lshr_b32 s2, s13, 2\ns_bfe_u32 s3, s13, 0x10001\nMvaa 124,136,248\ns_mul_i32 s3, s3, 17\n"
"s_add_u32 s2, s2, s3\nMvaa 124,140,252\ns_lshl2_add_u32 s3, s14, 0\nMvaa 120,132,252\ns_lshl_b32 s3, s3, 2\ns_lshr_b32 s3, 0xe420ca86, s3\nMvaa 120,128,248\nWL 0\nMaaa 32,192,224\n"
"Ra 128,10,32768\ns_and_b32 s3, s3, 15\nbuffer_load_dwordx4 v[200:203], v1, s[16:19], s6 offen offset:2048 sc1\nMaaa 32,196,228\ns_add_u32 s2, s2, s3\ns_cmp_eq_u32 s2, 32\n"
"s_cselect_b32 s4, 16, s2\nMaaa 36,204,228\nRa 132,11,32768\ns_mul_i32 s75, s4, 65536\nMaaa 36,200,224\ns_lshr_b32 s2, s13, 2\ns_bfe_u32 s3, s13, 0x10001\nMaaa 96,208,224\n"
"Ra 136,10,34816\nMaaa 96,212,228\ns_mul_i32 s3, s3, 17\ns_add_u32 s2, s2, s3\nMaaa 100,220,228\nRa 140,11,34816\ns_lshl2_add_u32 s3, s14, 1\n"
"buffer_load_dwordx4 v[204:207], v1, s[16:19], s6 offen offset:3072 sc1\nMaaa 100,216,224\ns_lshl_b32 s3, s3, 2\nMaaa 108,216,232\nRa 144,10,40960\ns_lshr_b32 s3, 0xe420ca86, s3\n"
"Maaa 108,220,236\ns_and_b32 s3, s3, 15\ns_add_u32 s2, s2, s3\nMaaa 104,212,236\nRa 148,11,40960\ns_cmp_eq_u32 s2, 32\ns_cselect_b32 s4, 16, s2\ns_add_u32 s6, s56, 4096\n"
"Maaa 104,208,232\ns_mul_i32 s76, s4, 65536\ns_lshr_b32 s2, s13, 2\nMaaa 44,200,232\nRa 152,10,43008\ns_bfe_u32 s3, s13, 0x10001\n"
"buffer_load_dwordx4 v[208:211], v1, s[16:19], s6 offen offset:0 sc1\nMaaa 44,204,236\ns_mul_i32 s3, s3, 17\nMaaa 40,196,236\nRa 156,11,43008\ns_add_u32 s2, s2, s3\n"
"Maaa 40,192,232\ns_lshl2_add_u32 s3, s14, 2\ns_lshl_b32 s3, s3, 2\nMaaa 48,192,240\ns_lshr_b32 s3, 0xe420ca86, s3\ns_and_b32 s3, s3, 15\nMaaa 48,196,244\ns_add_u32 s2, s2, s3\n"
"s_cmp_eq_u32 s2, 32\ns_cselect_b32 s4, 16, s2\nMaaa 52,204,244\ns_mul_i32 s77, s4, 65536\ns_lshr_b32 s2, s13, 2\n"
"buffer_load_dwordx4 v[212:215], v1, s[16:19], s6 offen offset:1024 sc1\nMaaa 52,200,240\ns_bfe_u32 s3, s13, 0x10001\ns_mul_i32 s3, s3, 17\nMaaa 112,208,240\ns_add_u32 s2, s2, s3\n"
"Maaa 112,212,244\ns_lshl2_add_u32 s3, s14, 3\ns_lshl_b32 s3, s3, 2\nMaaa 116,220,244\ns_lshr_b32 s3, 0xe420ca86, s3\ns_and_b32 s3, s3, 15\nMaaa 116,216,240\ns_add_u32 s2, s2, s3\n"
"s_cmp_eq_u32 s2, 32\ns_cselect_b32 s4, 16, s2\nMaaa 124,216,248\ns_mul_i32 s78, s4, 65536\ns_lshr_b32 s3, s11, 1\n"
"buffer_load_dwordx4 v[216:219], v1, s[16:19], s6 offen offset:2048 sc1\nMaaa 124,220,252\ns_lshl_b32 s3, s3, 15\ns_add_u32 s2, s72, s3\nMaaa 120,212,252\ns_and_b32 s3, s11, 1\n"
"Maaa 120,208,248\ns_lshl_b32 s3, s3, 14\ns_add_u32 s2, s2, s3\nMaaa 60,200,248\ns_add_u32 s2, s2, 0x400000\ns_mov_b32 s55, 4096\nMaaa 60,204,252\ns_add_u32 s75, s75, s2\n"
"Maaa 56,196,252\ns_add_u32 s76, s76, s2\ns_add_u32 s77, s77, s2\nbuffer_load_dwordx4 v[220:223], v1, s[16:19], s6 offen offset:3072 sc1\nMaaa 56,192,248\nWV 22\ns_barrier\n"
"Maaa 0,192,160\nTa 224,16,32768\ns_add_u32 s78, s78, s2\nMaaa 0,196,164\nTa 226,16,34816\nMaaa 4,204,164\nMaaa 4,200,160\nTa 228,16,49152\nMaaa 64,208,160\nTa 230,16,51200\n"
"Maaa 64,212,164\nMaaa 68,220,164\nTa 232,17,32768\nMaaa 68,216,160\nTa 234,17,34816\nMaaa 76,216,168\nWV 6\nv_cmp_ne_u32 vcc, s15, v27\ns_nop 0\ns_cbranch_vccnz .S169\n.S170:\n"
"s_sub_u32 s45, s86, 131072\nMaaa 76,220,172\nTa 236,17,49152\ns_add_u32 m0, s47, 0\ns_add_u32 s46, s45, 0x0\nMaaa 72,212,172\nTa 238,17,51200\nMaaa 72,208,168\nD1 1,16,46,0\n"
"s_mov_b32 s36, s68\nMaaa 12,200,168\nTa 240,18,32768\nMaaa 12,204,172\nTa 242,18,34816\nMaaa 8,196,172\ns_and_b32 s37, s69, 0xffff\nMaaa 8,192,168\nTa 244,18,49152\n"
"Maaa 16,192,176\nTa 246,18,51200\nD1 1,16,46,1024\nMaaa 16,196,180\ns_mov_b32 s38, 0x1c0000\nMaaa 20,204,180\nTa 248,19,32768\nMaaa 20,200,176\nTa 250,19,34816\nMaaa 80,208,176\n"
"s_mov_b32 s39, 0x20000\nMaaa 80,212,180\nTa 252,19,49152\nD1 1,16,46,2048\nMaaa 84,220,180\nTa 254,19,51200\ns_and_b32 s2, s74, 15\nMaaa 84,216,176\ns_max_u32 s2, s2, 2\n"
"Maaa 92,216,184\ns_sub_u32 s2, s2, 2\nMaaa 92,220,188\ns_mul_i32 s3, s73, 14\nMaaa 88,212,188\nD1 1,16,46,3072\nMaaa 88,208,184\ns_add_u32 s2, s2, s3\ns_lshl2_add_u32 s2, s2, s11\n"
"Maaa 28,200,184\ns_mul_i32 s3, s2, 4682\nMaaa 28,204,188\ns_lshr_b32 s3, s3, 16\nMaaa 24,196,188\ns_mul_i32 s22, s3, 14\nMaaa 24,192,184\nWL 0\nMvaa 96,128,224\nTa 160,12,32768\n"
"s_add_u32 m0, s47, -65536\ns_add_u32 s46, s45, 0x10000\nD1 1,16,46,0\nMvaa 96,132,228\nTa 162,12,34816\nMvaa 100,140,228\ns_sub_u32 s22, s2, s22\nMvaa 100,136,224\n"
"Ta 164,12,49152\nMvaa 108,136,232\nTa 166,12,51200\nMvaa 108,140,236\nD1 1,16,46,1024\nMvaa 104,132,236\nTa 168,13,32768\ns_and_b32 s21, s3, 28\nMvaa 104,128,232\nTa 170,13,34816\n"
"Mvaa 112,128,240\ns_add_u32 s21, s21, s3\nMvaa 112,132,244\nTa 172,13,49152\nMvaa 116,140,244\nTa 174,13,51200\nD1 1,16,46,2048\nMvaa 116,136,240\ns_lshl_b32 s21, s21, 4\n"
"s_add_u32 s21, s21, 1024\nMvaa 124,136,248\nTa 176,14,32768\nCV 48,96,97\nMvaa 124,140,252\nTa 178,14,34816\nMvaa 120,132,252\ns_xor_b32 s3, s3, 28\nCV 49,98,99\nMvaa 120,128,248\n"
"Ta 180,14,49152\nD1 1,16,46,3072\nMvaa 160,144,224\nTa 182,14,51200\ns_bfe_u32 s41, s3, 0x30002\nMvaa 160,148,228\ns_cmp_gt_u32 s22, 6\ns_cselect_b32 s2, 1179648, 0\n"
"s_cselect_b32 s1, 16, 0\nMvaa 164,156,228\nTa 184,15,32768\nMvaa 164,152,224\nTa 186,15,34816\nCV 50,100,101\nMvaa 172,152,232\ns_add_u32 s41, s41, s1\nMvaa 172,156,236\n"
"Ta 188,15,49152\nCV 51,102,103\nMvaa 168,148,236\nTa 190,15,51200\ns_lshl4_add_u32 s41, s41, 0xc048400\nMvaa 168,144,232\ns_and_b32 s1, s3, 28\nCV 52,104,105\nMvaa 176,144,240\n"
"s_add_u32 s3, s3, s1\nMvaa 176,148,244\ns_lshl_b32 s3, s3, 11\nMvaa 180,156,244\ns_add_u32 s2, s2, s3\nCV 53,106,107\nMvaa 180,152,240\ns_lshl_b32 s3, s22, 17\n"
"s_add_u32 s2, s2, s3\nMvaa 188,152,248\ns_add_u32 s20, s2, 0xce0000\nCV 54,108,109\nMvaa 188,156,252\ns_lshl_b32 s2, s22, 7\nMvaa 184,148,252\ns_add_u32 s40, s2, 0xc04b800\n"
"CV 55,110,111\nMvaa 184,144,248\nWL 0\ns_barrier\nMvaa 64,128,160\nRa 192,10,49152\ns_add_u32 m0, s47, 32768\ns_add_u32 s46, s45, 0x8000\nMvaa 64,132,164\ns_mul_i32 s2, s21, 896\n"
"Mvaa 68,140,164\nRa 196,11,49152\nD1 1,16,46,0\nMvaa 68,136,160\ns_lshl_b32 s3, s22, 6\nCV 56,112,113\nMvaa 76,136,168\nRa 200,10,51200\nMvaa 76,140,172\ns_add_u32 s22, s2, s3\n"
"CV 57,114,115\nMvaa 72,132,172\nRa 204,11,51200\ns_add_u32 s23, s22, 57344\nMvaa 72,128,168\nD1 1,16,46,1024\nCV 58,116,117\nMvaa 80,128,176\nRa 208,10,57344\nMvaa 80,132,180\n"
"CV 59,118,119\nMvaa 84,140,180\nRa 212,11,57344\nCV 60,120,121\nMvaa 84,136,176\nCV 61,122,123\nMvaa 92,136,184\nRa 216,10,59392\nD1 1,16,46,2048\nMvaa 92,140,188\nCV 62,124,125\n"
"Mvaa 88,132,188\nRa 220,11,59392\nCV 63,126,127\nMvaa 88,128,184\nCV 32,64,65\nMvaa 128,144,160\nCV 33,66,67\nCV 34,68,69\nMvaa 128,148,164\nD1 1,16,46,3072\nMvaa 132,156,164\n"
"CV 35,70,71\nMvaa 132,152,160\nCV 36,72,73\nCV 37,74,75\nMvaa 140,152,168\ns_add_u32 m0, s47, 49152\ns_add_u32 s46, s45, 0x400000\nMvaa 140,156,172\nCV 38,76,77\nCV 39,78,79\n"
"Mvaa 136,148,172\nD1 1,16,46,0\nMvaa 136,144,168\nCV 40,80,81\nCV 41,82,83\nMvaa 144,144,176\nCV 42,84,85\nMvaa 144,148,180\nCV 43,86,87\nMvaa 148,156,180\nCV 44,88,89\n"
"CV 45,90,91\nMvaa 148,152,176\nD1 1,16,46,1024\nMvaa 156,152,184\nCV 46,92,93\nCV 47,94,95\nMvaa 156,156,188\nCV 112,160,161\nMvaa 152,148,188\nCV 113,162,163\nCV 114,164,165\n"
"Mvaa 152,144,184\nWL 0\nMaaa 0,192,160\nD1 1,16,46,2048\nSv 32,1,16,75,0\nMaaa 0,196,164\nCV 115,166,167\nMaaa 4,204,164\nCV 116,168,169\nCV 117,170,171\nMaaa 4,200,160\n"
"CV 118,172,173\nCV 119,174,175\nMaaa 12,200,168\nCV 120,176,177\nCV 121,178,179\nMaaa 12,204,172\nD1 1,16,46,3072\nMaaa 8,196,172\nCV 122,180,181\nCV 123,182,183\nMaaa 8,192,168\n"
"CV 124,184,185\nCV 125,186,187\nMaaa 16,192,176\ns_add_u32 m0, s47, -49152\ns_add_u32 s46, s45, 0x408000\nSv 36,1,16,75,1024\nMaaa 16,196,180\nCV 126,188,189\nMaaa 20,204,180\n"
"D1 1,16,46,0\nCV 127,190,191\nMaaa 20,200,176\nCV 96,128,129\nCV 97,130,131\nMaaa 28,200,184\nCV 98,132,133\nCV 99,134,135\nMaaa 28,204,188\nCV 100,136,137\nMaaa 24,196,188\n"
"CV 101,138,139\nCV 102,140,141\nMaaa 24,192,184\nD1 1,16,46,1024\nCV 103,142,143\nMaaa 64,208,160\nSv 40,1,16,75,2048\n.W563:\ns_cbranch_execz .W567\nCV 104,144,145\n"
"Maaa 64,212,164\nCV 105,146,147\nMaaa 68,220,164\nCV 106,148,149\nCV 107,150,151\nMaaa 68,216,160\nCV 108,152,153\nCV 109,154,155\nMaaa 76,216,168\nD1 1,16,46,2048\n"
"CV 110,156,157\nMaaa 76,220,172\nCV 111,158,159\nMaaa 72,212,172\nv_add_u32 v2, 0x10000, v1\ns_lshr_b32 s2, s59, 1\nMaaa 72,208,168\nv_add_u32 v24, s2, v2\n"
"v_xor_b32 v26, 0x1000, v24\nMaaa 80,208,176\nSv 44,1,16,75,3072\nv_lshrrev_b32 v20, 4, v0\nMaaa 80,212,180\nD1 1,16,46,3072\nMaaa 84,220,180\nv_lshlrev_b32 v20, 2, v20\n"
"v_and_b32 v0, 15, v0\nMaaa 84,216,176\nv_sub_u32 v0, v0, v20\ns_lshl_b32 s2, s11, 4\nMaaa 92,216,184\nv_add_u32 v3, s2, v0\nv_subrev_u32 v0, 32, v3\nMaaa 92,220,188\n"
"v_mov_b32 v4, 0\nMaaa 88,212,188\nv_mov_b32 v5, 0\nv_mov_b32 v6, 0\nMaaa 88,208,184\nWV 20\ns_barrier\nMaaa 32,192,224\ns_add_u32 s75, s75, s55\nSv 48,1,16,75,0\nMaaa 32,196,228\n"
"v_mov_b32 v7, 0\nMaaa 36,204,228\nv_mov_b32 v8, 0\nv_mov_b32 v9, 0\nMaaa 36,200,224\nv_mov_b32 v10, 0\nv_mov_b32 v11, 0\nMaaa 44,200,232\nv_mov_b32 v12, 0\nv_mov_b32 v13, 0\n"
"Maaa 44,204,236\nv_mov_b32 v14, 0\nMaaa 40,196,236\nv_mov_b32 v15, 0\nv_mov_b32 v16, 0\nMaaa 40,192,232\nv_mov_b32 v17, 0\nv_mov_b32 v18, 0\nMaaa 48,192,240\nSv 52,1,16,75,1024\n"
"v_mov_b32 v19, 0\nMaaa 48,196,244\nv_mov_b32 v86, 0\nMaaa 52,204,244\nv_mov_b32 v87, 0\nv_mov_b32 v88, 0\nMaaa 52,200,240\nv_mov_b32 v89, 0\nAR 92,0\nAR 93,1\nMaaa 60,200,248\n"
"CV 128,92,93\nAR 92,2\nAR 93,3\nMaaa 60,204,252\nCV 129,92,93\nAR 92,4\nAR 93,5\nMaaa 56,196,252\nRa 128,24,0\nCV 130,92,93\nAR 92,6\nMaaa 56,192,248\nAR 93,7\nCV 131,92,93\n"
"AR 92,8\nMaaa 96,208,224\nSv 56,1,16,75,2048\nRa 132,24,1024\nAR 93,9\nMaaa 96,212,228\nCV 132,92,93\nAR 92,10\nAR 93,11\nMaaa 100,220,228\nRa 136,24,2048\nCV 133,92,93\nAR 92,12\n"
"Maaa 100,216,224\nAR 93,13\nCV 134,92,93\nAR 92,14\nMaaa 108,216,232\nRa 140,24,3072\nAR 93,15\nCV 135,92,93\nMaaa 108,220,236\nAR 92,16\nAR 93,17\nCV 136,92,93\nMaaa 104,212,236\n"
"Ra 144,26,0\nAR 92,18\nAR 93,19\nMaaa 104,208,232\nCV 137,92,93\nAR 92,20\nAR 93,21\nMaaa 112,208,240\nSv 60,1,16,75,3072\nRa 148,26,1024\nCV 138,92,93\nMaaa 112,212,244\n"
"AR 92,22\nAR 93,23\nCV 139,92,93\nMaaa 116,220,244\nRa 152,26,2048\nAR 92,24\nAR 93,25\nMaaa 116,216,240\nCV 140,92,93\nAR 92,26\nAR 93,27\nMaaa 124,216,248\nRa 156,26,3072\n"
"CV 141,92,93\nAR 92,28\nMaaa 124,220,252\nAR 93,29\nCV 142,92,93\nAR 92,30\nMaaa 120,212,252\nAR 93,31\nCV 143,92,93\nAR 92,32\nMaaa 120,208,248\nWL 4\nCvav 32,128,192,4\n"
"Ra 160,24,8192\nAR 93,33\nCV 144,92,93\nMvav 32,132,200\nAR 92,34\nAR 93,35\nCV 145,92,93\nMvav 32,136,208\nRa 164,24,9216\nAR 92,36\nAR 93,37\nMvav 32,140,216\nCV 146,92,93\n"
"AR 92,38\nAR 93,39\nCvav 36,140,220,8\nRa 168,24,10240\nCV 147,92,93\nAR 92,40\nMvav 36,136,212\ns_add_u32 m0, s89, 32768\nAR 93,41\nCV 148,92,93\nMvav 36,132,204\n"
"Ra 172,24,11264\nWL 4\ns_barrier\nMvav 36,128,196\nAR 92,42\nAR 93,43\nCV 149,92,93\nMvav 36,144,228\nRa 176,26,8192\nAR 92,44\nAR 93,45\nSv 96,1,16,76,0\nMvav 36,148,236\n"
"s_add_u32 s46, s86, s88\nCV 150,92,93\nAR 92,46\nMvav 36,152,244\nRa 180,26,9216\nAR 93,47\nCV 151,92,93\nMvav 36,156,252\nAR 92,48\nAR 93,49\nCV 152,92,93\nMvav 32,156,248\n"
"Ra 184,26,10240\nAR 92,50\nAR 93,51\nMvav 32,152,240\nD1 1,16,46,0\nCV 153,92,93\nAR 92,52\nMvav 32,148,232\nRa 188,26,11264\nWL 4\nAR 93,53\nMvav 32,144,224\nCV 154,92,93\n"
"AR 92,54\nAR 93,55\nCvav 40,160,192,12\nCV 155,92,93\nAR 92,56\nAR 93,57\nMvav 40,164,200\nCV 156,92,93\nAR 92,58\nAR 93,59\nMvav 40,168,208\nCV 157,92,93\nAR 92,60\nAR 93,61\n"
"v_and_b32 v27, 0x70, v1\nbuffer_load_dword v27, v27, s[16:19], s61 offen offset:136 sc0 sc1\nMvav 40,172,216\nCV 158,92,93\nAR 92,62\nAR 93,63\nCvav 44,172,220,16\nCV 159,92,93\n"
"AR 92,64\nAR 93,65\nMvav 44,168,212\nD1 1,16,46,1024\nCV 160,92,93\nAR 92,66\nMvav 44,164,204\nWL 0\nAR 93,67\nCV 161,92,93\nMvav 44,160,196\nAR 92,68\nAR 93,69\nCV 162,92,93\n"
"Mvav 44,176,228\nAR 92,70\nAR 93,71\nCV 163,92,93\nSv 100,1,16,76,1024\nMvav 44,180,236\ns_add_u32 s88, s88, 32768\nEX 80,32\nMvav 44,184,244\nAR 92,72\nAR 93,73\nCV 164,92,93\n"
"Mvav 44,188,252\nEX 81,33\nMvav 40,188,248\nAR 92,74\nAR 93,75\nCV 165,92,93\nMvav 40,184,240\nD1 1,16,46,2048\nAR 92,76\nAR 93,77\nMvav 40,180,232\nCV 166,92,93\nAR 92,78\n"
"AR 93,79\nMvav 40,176,224\nAD 84,80,81\nCV 64,80,81\nCV 167,92,93\nAR 92,80\nAR 93,81\nCV 168,92,93\nEX 82,34\nAR 92,82\nAR 93,83\nCV 169,92,93\nAR 92,84\nWV 17\nEX 83,35\n"
"AD 84,84,82\nAR 93,85\nCV 170,92,93\nD1 1,16,46,3072\ns_add_u32 m0, s89, 49152\nAR 92,86\ns_barrier\nAR 93,87\nCV 171,92,93\nAD 84,84,83\nCV 65,82,83\nAR 92,88\nRa 128,24,32768\n"
"AR 93,89\nCV 172,92,93\nSv 104,1,16,76,2048\ns_add_u32 s46, s87, s88\nEX 80,36\nRa 132,24,33792\nAR 92,90\nAR 93,91\nEX 81,37\nRa 136,24,34816\nCV 173,92,93\nAR 92,92\n"
"D1 1,16,46,0\nAR 93,93\nCV 174,92,93\nRa 140,24,35840\nAR 92,94\nAR 93,95\nAD 85,80,81\nCV 68,80,81\nCV 175,92,93\nRa 144,26,32768\nAR 92,96\nAR 93,97\nEX 82,38\nCV 176,92,93\n"
"Ra 148,26,33792\nAR 92,98\nAR 93,99\nEX 83,39\nRa 152,26,34816\nAD 85,85,82\nCV 177,92,93\nD1 1,16,46,1024\nAR 92,100\nAR 93,101\nRa 156,26,35840\nCV 178,92,93\nAR 92,102\n"
"AD 85,85,83\nCV 69,82,83\nAR 93,103\nRa 160,24,40960\nCV 179,92,93\nAR 92,104\nSv 108,1,16,76,3072\ns_xor_b32 s89, s89, 0x10000\nEX 80,40\nRa 164,24,41984\nAR 93,105\n"
"CV 180,92,93\nWL 6\nEX 81,41\nRa 168,24,43008\nAD 84,84,80\nAR 92,106\nD1 1,16,46,2048\nAR 93,107\nCV 181,92,93\nRa 172,24,44032\nAR 92,108\nAR 93,109\nAD 84,84,81\nCV 66,80,81\n"
"CV 182,92,93\nCvav 48,128,192,4\nRa 176,26,40960\nAR 92,110\nAR 93,111\nMvav 48,132,200\nEX 82,42\nCV 183,92,93\nMvav 48,136,208\nRa 180,26,41984\nAR 92,112\nAR 93,113\n"
"Mvav 48,140,216\nWL 6\nEX 83,43\nWV 9\nv_cmp_ne_u32 vcc, s15, v27\ns_nop 0\ns_cbranch_vccnz .S172\n.S173:\nCvav 52,140,220,8\nRa 184,26,43008\nAD 84,84,82\nCV 184,92,93\n"
"Mvav 52,136,212\nD1 1,16,46,3072\ns_add_u32 m0, s89, 0\nAR 92,114\nMvav 52,132,204\nRa 188,26,44032\nAR 93,115\nCV 185,92,93\nMvav 52,128,196\nAD 84,84,83\nCV 67,82,83\nAR 92,116\n"
"Mvav 52,144,228\nRa 128,2,49152\nAR 93,117\nCV 186,92,93\ns_add_u32 s76, s76, s55\nSv 112,1,16,76,0\nMvav 52,148,236\ns_add_u32 s46, s86, s88\nEX 80,44\nMvav 52,152,244\n"
"Ra 132,2,50176\nv_xor_b32 v24, 0x10000, v24\nAR 92,118\nMvav 52,156,252\nWL 6\nEX 81,45\nMvav 48,156,248\nRa 136,2,51200\nAD 85,85,80\nAR 93,119\nMvav 48,152,240\nD1 1,16,46,0\n"
"CV 187,92,93\nAR 92,120\nMvav 48,148,232\nRa 140,2,52224\nAR 93,121\nCV 188,92,93\nMvav 48,144,224\nAD 85,85,81\nCV 70,80,81\nAR 92,122\nCvav 56,160,192,12\nRa 144,2,53248\n"
"AR 93,123\nCV 189,92,93\nMvav 56,164,200\nEX 82,46\nAR 92,124\nMvav 56,168,208\nRa 148,2,54272\nv_xor_b32 v26, 0x10000, v26\nAR 93,125\nMvav 56,172,216\nWL 6\nEX 83,47\n"
"Cvav 60,172,220,16\nRa 152,2,55296\nAD 85,85,82\nCV 190,92,93\nMvav 60,168,212\nD1 1,16,46,1024\nAR 92,126\nAR 93,127\nMvav 60,164,204\nRa 156,2,56320\nCV 191,92,93\n"
"Mvav 60,160,196\nAD 85,85,83\nCV 71,82,83\nMvav 60,176,228\nRa 160,2,57344\nv_max_f32 v90, v84, v85\nSv 116,1,16,76,1024\nMvav 60,180,236\ns_add_u32 s88, s88, 32768\nEX 80,48\n"
"Mvav 60,184,244\nRa 164,2,58368\nv_cmp_lt_f32 vcc, s63, v90\nMvav 60,188,252\nWL 8\nEX 81,49\nMvav 56,188,248\nRa 168,2,59392\ns_cbranch_vccnz .T154\n.T155:\nv_mov_b32 v20, v84\n"
"v_mov_b32 v21, v85\nv_mov_b32 v22, v20\nv_mov_b32 v23, v21\nMvav 56,184,240\nD1 1,16,46,2048\nv_permlane32_swap_b32 v20, v22\nv_permlane32_swap_b32 v21, v23\nAD 20,20,22\n"
"AD 21,21,23\nv_mov_b32 v22, v20\nv_mov_b32 v23, v21\nMvav 56,180,232\nRa 172,2,60416\nv_permlane16_swap_b32 v20, v22\nv_permlane16_swap_b32 v21, v23\nAD 20,20,22\nAD 21,21,23\n"
"v_min_f32 v90, v20, v21\nv_cmp_gt_f32 vcc, s64, v90\ns_nop 0\ns_cbranch_vccnz .T156\n.T157:\nAD 86,86,84\nMvav 56,176,224\nAD 84,80,81\nCV 72,80,81\nZaav 0,128,64\nRa 176,2,61440\n"
"Zaav 4,128,68\nEX 82,50\nZaav 12,132,68\nRa 180,2,62464\nZaav 8,132,64\nWL 8\nEX 83,51\nZaav 16,136,64\nRa 184,2,63488\nAD 84,84,82\nZaav 20,136,68\nD1 1,16,46,3072\n"
"s_add_u32 m0, s89, 16384\nZaav 28,140,68\nRa 188,2,64512\ns_barrier\nZaav 24,140,64\nAD 84,84,83\nCV 73,82,83\nZaav 32,144,64\nRa 128,24,0\nSv 120,1,16,76,2048\nZaav 36,144,68\n"
"s_add_u32 s46, s87, s88\nEX 80,52\nbuffer_store_dword v25, off, s[16:19], s54 offset:12 sc0 sc1\nZaav 44,148,68\nRa 132,24,1024\nv_xor_b32 v2, 0x10000, v2\nZaav 40,148,64\nWL 8\n"
"EX 81,53\nZaav 48,152,64\nRa 136,24,2048\nAD 87,87,85\nZaav 52,152,68\nD1 1,16,46,0\nZaav 60,156,68\nRa 140,24,3072\nZaav 56,156,64\nAD 85,80,81\nCV 76,80,81\nZaav 64,160,64\n"
"Ra 144,26,0\nZaav 68,160,68\nEX 82,54\nZaav 76,164,68\nRa 148,26,1024\nZaav 72,164,64\nWL 8\nEX 83,55\nZaav 80,168,64\nRa 152,26,2048\nAD 85,85,82\nZaav 84,168,68\n"
"D1 1,16,46,1024\nZaav 92,172,68\nRa 156,26,3072\nZaav 88,172,64\nAD 85,85,83\nCV 77,82,83\nZaav 96,176,64\nRa 160,24,8192\nSv 124,1,16,76,3072\nZaav 100,176,68\n.W564:\n"
"s_cbranch_execz .W568\nEX 80,56\nZaav 108,180,68\nRa 164,24,9216\nZaav 104,180,64\nWL 6\nEX 81,57\nZaav 112,184,64\nRa 168,24,10240\nAD 84,84,80\nZaav 116,184,68\nD1 1,16,46,2048\n"
"Zaav 124,188,68\nRa 172,24,11264\nZaav 120,188,64\nAD 84,84,81\nCV 74,80,81\nCvav 32,128,192,4\nRa 176,26,8192\nMvav 32,132,200\nEX 82,58\nMvav 32,136,208\nRa 180,26,9216\n"
"Mvav 32,140,216\nWL 6\nEX 83,59\nCvav 36,140,220,8\nRa 184,26,10240\nAD 84,84,82\nMvav 36,136,212\nD1 1,16,46,3072\ns_add_u32 m0, s89, 32768\nMvav 36,132,204\nRa 188,26,11264\n"
"Mvav 36,128,196\nAD 84,84,83\nCV 75,82,83\nMvav 36,144,228\nRa 128,2,16384\nSv 128,1,16,77,0\nMvav 36,148,236\ns_add_u32 s46, s86, s88\nEX 80,60\nMvav 36,152,244\nRa 132,2,17408\n"
"Mvav 36,156,252\nWL 6\nEX 81,61\nMvav 32,156,248\nRa 136,2,18432\nAD 85,85,80\nMvav 32,152,240\nD1 1,16,46,0\nMvav 32,148,232\nRa 140,2,19456\nMvav 32,144,224\nAD 85,85,81\n"
"CV 78,80,81\nCvav 40,160,192,12\nRa 144,2,20480\nMvav 40,164,200\nEX 82,62\nMvav 40,168,208\nRa 148,2,21504\nMvav 40,172,216\nWL 6\nEX 83,63\nCvav 44,172,220,16\nRa 152,2,22528\n"
"AD 85,85,82\nMvav 44,168,212\nD1 1,16,46,1024\nMvav 44,164,204\nRa 156,2,23552\nMvav 44,160,196\nAD 85,85,83\nCV 79,82,83\nMvav 44,176,228\nRa 160,2,24576\n"
"v_max_f32 v90, v84, v85\nSv 132,1,16,77,1024\nMvav 44,180,236\ns_add_u32 s88, s88, 32768\nEX 80,32\nMvav 44,184,244\nRa 164,2,25600\nv_cmp_lt_f32 vcc, s63, v90\nMvav 44,188,252\n"
"WL 8\nEX 81,33\nMvav 40,188,248\nRa 168,2,26624\ns_cbranch_vccnz .T159\n.T160:\nMvav 40,184,240\nD1 1,16,46,2048\nMvav 40,180,232\nRa 172,2,27648\nAD 86,86,84\nMvav 40,176,224\n"
"AD 84,80,81\nCV 64,80,81\nMaav 0,128,72\nRa 176,2,28672\nv_mov_b32 v112, -1\nMaav 4,128,76\nEX 82,34\nMaav 12,132,76\nRa 180,2,29696\nMaav 8,132,72\n"
"s_waitcnt vmcnt(18) lgkmcnt(8)\nEX 83,35\nMaav 16,136,72\nRa 184,2,30720\nAD 84,84,82\nMaav 20,136,76\nD1 1,16,46,3072\ns_add_u32 m0, s89, 49152\nMaav 28,140,76\nRa 188,2,31744\n"
"s_barrier\nMaav 24,140,72\nAD 84,84,83\nCV 65,82,83\nMaav 32,144,72\nRa 128,24,32768\nSv 136,1,16,77,2048\nMaav 36,144,76\ns_add_u32 s46, s87, s88\nEX 80,36\nMaav 44,148,76\n"
"Ra 132,24,33792\nMaav 40,148,72\nWL 8\nEX 81,37\nMaav 48,152,72\nRa 136,24,34816\nAD 87,87,85\nMaav 52,152,76\nD1 1,16,46,0\nMaav 60,156,76\nRa 140,24,35840\nMaav 56,156,72\n"
"AD 85,80,81\nCV 68,80,81\nMaav 64,160,72\nRa 144,26,32768\nv_mov_b32 v113, -1\nMaav 68,160,76\nEX 82,38\nMaav 76,164,76\nRa 148,26,33792\nMaav 72,164,72\nWL 8\nEX 83,39\n"
"Maav 80,168,72\nRa 152,26,34816\nAD 85,85,82\nMaav 84,168,76\nD1 1,16,46,1024\nMaav 92,172,76\nRa 156,26,35840\nMaav 88,172,72\nAD 85,85,83\nCV 69,82,83\nMaav 96,176,72\n"
"Ra 160,24,40960\nSv 140,1,16,77,3072\nMaav 100,176,76\ns_xor_b32 s89, s89, 0x10000\nEX 80,40\nMaav 108,180,76\nRa 164,24,41984\nMaav 104,180,72\nWL 6\nEX 81,41\nMaav 112,184,72\n"
"Ra 168,24,43008\nAD 84,84,80\nMaav 116,184,76\nD1 1,16,46,2048\nMaav 124,188,76\nRa 172,24,44032\nMaav 120,188,72\nAD 84,84,81\nCV 66,80,81\nCvav 48,128,192,4\nRa 176,26,40960\n"
"v_mov_b32 v114, -1\nMvav 48,132,200\nEX 82,42\nMvav 48,136,208\nRa 180,26,41984\nMvav 48,140,216\nWL 6\nEX 83,43\nCvav 52,140,220,8\nRa 184,26,43008\nAD 84,84,82\n"
"v_and_b32 v27, 0x70, v1\nbuffer_load_dword v27, v27, s[16:19], s61 offen offset:140 sc0 sc1\nMvav 52,136,212\nD1 1,16,46,3072\ns_add_u32 m0, s89, 0\nMvav 52,132,204\n"
"Ra 188,26,44032\nMvav 52,128,196\nAD 84,84,83\nCV 67,82,83\nMvav 52,144,228\nRa 128,2,49152\ns_add_u32 s77, s77, s55\nSv 144,1,16,77,0\nMvav 52,148,236\ns_add_u32 s46, s86, s88\n"
"EX 80,44\nMvav 52,152,244\nRa 132,2,50176\nv_xor_b32 v24, 0x10000, v24\nMvav 52,156,252\nWL 6\nEX 81,45\nMvav 48,156,248\nRa 136,2,51200\nAD 85,85,80\nMvav 48,152,240\n"
"D1 1,16,46,0\nMvav 48,148,232\nRa 140,2,52224\nMvav 48,144,224\nAD 85,85,81\nCV 70,80,81\nCvav 56,160,192,12\nRa 144,2,53248\nv_mov_b32 v115, -1\nMvav 56,164,200\nEX 82,46\n"
"Mvav 56,168,208\nRa 148,2,54272\nv_xor_b32 v26, 0x10000, v26\nMvav 56,172,216\nWL 6\nEX 83,47\nCvav 60,172,220,16\nRa 152,2,55296\nAD 85,85,82\nMvav 60,168,212\nD1 1,16,46,1024\n"
"Mvav 60,164,204\nRa 156,2,56320\nMvav 60,160,196\nAD 85,85,83\nCV 71,82,83\nMvav 60,176,228\nRa 160,2,57344\nv_max_f32 v90, v84, v85\nSv 148,1,16,77,1024\nMvav 60,180,236\n"
"s_add_u32 s88, s88, 32768\nEX 80,48\nMvav 60,184,244\nRa 164,2,58368\nv_cmp_lt_f32 vcc, s63, v90\nMvav 60,188,252\nWL 8\nEX 81,49\nMvav 56,188,248\nRa 168,2,59392\n"
"s_cbranch_vccnz .T161\n.T162:\nMvav 56,184,240\nD1 1,16,46,2048\nMvav 56,180,232\nRa 172,2,60416\nAD 86,86,84\nMvav 56,176,224\nAD 84,80,81\nCV 72,80,81\nMaav 0,128,64\n"
"Ra 176,2,61440\nMaav 4,128,68\nEX 82,50\nMaav 12,132,68\nRa 180,2,62464\nMaav 8,132,64\ns_waitcnt vmcnt(18) lgkmcnt(8)\nEX 83,51\nMaav 16,136,64\nRa 184,2,63488\nAD 84,84,82\n"
"Maav 20,136,68\nD1 1,16,46,3072\ns_add_u32 m0, s89, 16384\nMaav 28,140,68\nRa 188,2,64512\ns_barrier\nMaav 24,140,64\nAD 84,84,83\nCV 73,82,83\nMaav 32,144,64\nRa 128,24,0\n"
"Sv 152,1,16,77,2048\nMaav 36,144,68\ns_add_u32 s46, s87, s88\nEX 80,52\nMaav 44,148,68\nRa 132,24,1024\nv_xor_b32 v2, 0x10000, v2\nMaav 40,148,64\nWL 8\nEX 81,53\nMaav 48,152,64\n"
"Ra 136,24,2048\nAD 87,87,85\nbuffer_store_dword v25, off, s[16:19], s54 offset:8 sc0 sc1\nMaav 52,152,68\nD1 1,16,46,0\nMaav 60,156,68\nRa 140,24,3072\nMaav 56,156,64\n"
"AD 85,80,81\nCV 76,80,81\nMaav 64,160,64\nRa 144,26,0\nMaav 68,160,68\nEX 82,54\nMaav 76,164,68\nRa 148,26,1024\nMaav 72,164,64\nWL 8\nEX 83,55\nMaav 80,168,64\nRa 152,26,2048\n"
"AD 85,85,82\nMaav 84,168,68\nD1 1,16,46,1024\nMaav 92,172,68\nRa 156,26,3072\nMaav 88,172,64\nAD 85,85,83\nCV 77,82,83\nMaav 96,176,64\nRa 160,24,8192\nSv 156,1,16,77,3072\n"
"Maav 100,176,68\nEX 80,56\nMaav 108,180,68\nRa 164,24,9216\nMaav 104,180,64\nWL 6\nEX 81,57\nMaav 112,184,64\nRa 168,24,10240\nAD 84,84,80\nMaav 116,184,68\nD1 1,16,46,2048\n"
"Maav 124,188,68\nRa 172,24,11264\nMaav 120,188,64\nAD 84,84,81\nCV 74,80,81\nCvav 32,128,192,4\nRa 176,26,8192\nMvav 32,132,200\nEX 82,58\nMvav 32,136,208\nRa 180,26,9216\n"
"Mvav 32,140,216\nWL 6\nEX 83,59\nCvav 36,140,220,8\nRa 184,26,10240\nAD 84,84,82\nMvav 36,136,212\nD1 1,16,46,3072\ns_add_u32 m0, s89, 32768\nMvav 36,132,204\nRa 188,26,11264\n"
"Mvav 36,128,196\nAD 84,84,83\nCV 75,82,83\nMvav 36,144,228\nRa 128,2,16384\nSv 160,1,16,78,0\nMvav 36,148,236\ns_add_u32 s46, s86, s88\nEX 80,60\nMvav 36,152,244\nRa 132,2,17408\n"
"Mvav 36,156,252\nWL 6\nEX 81,61\nMvav 32,156,248\nRa 136,2,18432\nAD 85,85,80\nMvav 32,152,240\nD1 1,16,46,0\nMvav 32,148,232\nRa 140,2,19456\nSv 112,1,16,49,0\nMvav 32,144,224\n"
"AD 85,85,81\nCV 78,80,81\nCvav 40,160,192,12\nRa 144,2,20480\nMvav 40,164,200\nEX 82,62\nMvav 40,168,208\nRa 148,2,21504\nMvav 40,172,216\nWL 6\nEX 83,63\nCvav 44,172,220,16\n"
"Ra 152,2,22528\nAD 85,85,82\nMvav 44,168,212\nD1 1,16,46,1024\nMvav 44,164,204\nRa 156,2,23552\nMvav 44,160,196\nAD 85,85,83\nCV 79,82,83\nMvav 44,176,228\nRa 160,2,24576\n"
"v_max_f32 v90, v84, v85\nSv 164,1,16,78,1024\nMvav 44,180,236\ns_add_u32 s88, s88, 32768\nEX 80,32\nMvav 44,184,244\nRa 164,2,25600\nv_cmp_lt_f32 vcc, s63, v90\nMvav 44,188,252\n"
"WL 8\nEX 81,33\nMvav 40,188,248\nRa 168,2,26624\ns_cbranch_vccnz .T164\n.T165:\nMvav 40,184,240\nD1 1,16,46,2048\nMvav 40,180,232\nRa 172,2,27648\nAD 86,86,84\nMvav 40,176,224\n"
"AD 84,80,81\nCV 64,80,81\nMaav 0,128,72\nRa 176,2,28672\nMaav 4,128,76\nEX 82,34\nMaav 12,132,76\nRa 180,2,29696\nMaav 8,132,72\ns_waitcnt vmcnt(19) lgkmcnt(8)\nEX 83,35\n"
"Maav 16,136,72\nRa 184,2,30720\nAD 84,84,82\nMaav 20,136,76\nD1 1,16,46,3072\ns_add_u32 m0, s89, 49152\nMaav 28,140,76\nRa 188,2,31744\ns_barrier\nMaav 24,140,72\nAD 84,84,83\n"
"CV 65,82,83\nMaav 32,144,72\nRa 128,24,32768\nSv 168,1,16,78,2048\nMaav 36,144,76\ns_add_u32 s46, s87, s88\nEX 80,36\nMaav 44,148,76\nRa 132,24,33792\nMaav 40,148,72\nWL 8\n"
"EX 81,37\nMaav 48,152,72\nRa 136,24,34816\nAD 87,87,85\nMaav 52,152,76\nD1 1,16,46,0\nMaav 60,156,76\nRa 140,24,35840\nMaav 56,156,72\nAD 85,80,81\nCV 68,80,81\nMaav 64,160,72\n"
"Ra 144,26,32768\nMaav 68,160,76\nEX 82,38\nMaav 76,164,76\nRa 148,26,33792\nMaav 72,164,72\nWL 8\nEX 83,39\nMaav 80,168,72\nRa 152,26,34816\nAD 85,85,82\nMaav 84,168,76\n"
"D1 1,16,46,1024\nMaav 92,172,76\nRa 156,26,35840\nMaav 88,172,72\nAD 85,85,83\nCV 69,82,83\nMaav 96,176,72\nRa 160,24,40960\nSv 172,1,16,78,3072\nMaav 100,176,76\n"
"s_xor_b32 s89, s89, 0x10000\nEX 80,40\nMaav 108,180,76\nRa 164,24,41984\nMaav 104,180,72\nWL 6\nEX 81,41\nMaav 112,184,72\nRa 168,24,43008\nAD 84,84,80\nMaav 116,184,76\n"
"D1 1,16,46,2048\nMaav 124,188,76\nRa 172,24,44032\nMaav 120,188,72\nAD 84,84,81\nCV 66,80,81\nCvav 48,128,192,4\nRa 176,26,40960\nMvav 48,132,200\nEX 82,42\nMvav 48,136,208\n"
"Ra 180,26,41984\nMvav 48,140,216\nWL 6\nEX 83,43\nv_cmp_ne_u32 vcc, s15, v27\ns_nop 0\ns_cbranch_vccnz .S175\n.S176:\nv_and_b32 v27, 0x70, v1\n"
"buffer_load_dword v27, v27, s[16:19], s61 offen offset:12 sc0 sc1\nCvav 52,140,220,8\nRa 184,26,43008\nAD 84,84,82\nMvav 52,136,212\nD1 1,16,46,3072\ns_add_u32 m0, s89, 0\n"
"Mvav 52,132,204\nRa 188,26,44032\nMvav 52,128,196\nAD 84,84,83\nCV 67,82,83\nMvav 52,144,228\nRa 128,2,49152\ns_add_u32 s78, s78, s55\nSv 176,1,16,78,0\nMvav 52,148,236\n"
"s_add_u32 s46, s86, s88\nEX 80,44\nMvav 52,152,244\nRa 132,2,50176\nv_xor_b32 v24, 0x10000, v24\nMvav 52,156,252\nWL 6\nEX 81,45\nMvav 48,156,248\nRa 136,2,51200\nAD 85,85,80\n"
"Mvav 48,152,240\nD1 1,16,46,0\nMvav 48,148,232\nRa 140,2,52224\nSv 112,1,16,49,1024\nMvav 48,144,224\nAD 85,85,81\nCV 70,80,81\nCvav 56,160,192,12\nRa 144,2,53248\n"
"v_lshrrev_b32 v92, 8, v1\nMvav 56,164,200\ns_add_u32 s49, s49, 131072\nEX 82,46\nMvav 56,168,208\nRa 148,2,54272\nv_xor_b32 v26, 0x10000, v26\nMvav 56,172,216\nWL 6\nEX 83,47\n"
"Cvav 60,172,220,16\nRa 152,2,55296\nAD 85,85,82\nMvav 60,168,212\nD1 1,16,46,1024\nMvav 60,164,204\nRa 156,2,56320\nv_bfe_u32 v93, v1, 6, 2\nMvav 60,160,196\nAD 85,85,83\n"
"CV 71,82,83\n.W565:\ns_cbranch_execz .W569\nMvav 60,176,228\nRa 160,2,57344\nv_max_f32 v90, v84, v85\nSv 180,1,16,78,1024\nMvav 60,180,236\ns_add_u32 s88, s88, 32768\nEX 80,48\n"
"Mvav 60,184,244\nRa 164,2,58368\nv_cmp_lt_f32 vcc, s63, v90\nMvav 60,188,252\nWL 8\nEX 81,49\nMvav 56,188,248\nRa 168,2,59392\ns_cbranch_vccnz .T166\n.T167:\nMvav 56,184,240\n"
"D1 1,16,46,2048\nv_cmp_eq_u32 vcc, v92, v93\nv_and_b32 v92, 16, v1\nv_mov_b32 v93, 0x3f80\nv_cndmask_b32 v93, 0, v93, vcc\nMvav 56,180,232\nRa 172,2,60416\nAD 86,86,84\n"
"Mvav 56,176,224\nAD 84,80,81\nCV 72,80,81\nMaav 0,128,64\nRa 176,2,61440\nv_lshlrev_b32 v93, v92, v93\nMaav 4,128,68\nEX 82,50\nMaav 12,132,68\nRa 180,2,62464\n"
"v_and_b32 v92, 32, v1\nMaav 8,132,64\ns_waitcnt vmcnt(20) lgkmcnt(8)\nEX 83,51\nMaav 16,136,64\nRa 184,2,63488\nAD 84,84,82\nMaav 20,136,68\nD1 1,16,46,3072\n"
"s_add_u32 m0, s89, 16384\nMaav 28,140,68\nRa 188,2,64512\ns_barrier\nMaav 24,140,64\nAD 84,84,83\nCV 73,82,83\nMaav 32,144,64\nRa 128,24,0\nSv 184,1,16,78,2048\nMaav 36,144,68\n"
"s_add_u32 s46, s87, s88\nEX 80,52\nMaav 44,148,68\nRa 132,24,1024\nv_xor_b32 v2, 0x10000, v2\nMaav 40,148,64\nWL 8\nEX 81,53\nMaav 48,152,64\nRa 136,24,2048\nAD 87,87,85\n"
"Maav 52,152,68\nD1 1,16,46,0\nv_cmp_eq_u32 vcc, 0, v92\nv_mov_b32 v128, 0\nv_mov_b32 v129, 0\nv_cndmask_b32 v130, 0, v93, vcc\nMaav 60,156,68\nRa 140,24,3072\n"
"v_sub_u32 v131, v93, v130\nMaav 56,156,64\nAD 85,80,81\nCV 76,80,81\nMaav 64,160,64\nRa 144,26,0\nv_mov_b32 v132, 0\nMaav 68,160,68\nEX 82,54\nMaav 76,164,68\nRa 148,26,1024\n"
"v_mov_b32 v133, 0\nMaav 72,164,64\nWL 8\nEX 83,55\nMaav 80,168,64\nRa 152,26,2048\nAD 85,85,82\nMaav 84,168,68\nD1 1,16,46,1024\nMaav 92,172,68\nRa 156,26,3072\nMaav 88,172,64\n"
"AD 85,85,83\nCV 77,82,83\nMaav 96,176,64\nRa 160,24,8192\nSv 188,1,16,78,3072\nMaav 100,176,68\nEX 80,56\nMaav 108,180,68\nRa 164,24,9216\nMaav 104,180,64\nWL 6\nEX 81,57\n"
"Maav 112,184,64\nRa 168,24,10240\nAD 84,84,80\nMaav 116,184,68\nD1 1,16,46,2048\nMaav 124,188,68\nRa 172,24,11264\nMaav 120,188,64\nAD 84,84,81\nCV 74,80,81\ns_mov_b32 s99, 11\n"
"s_mov_b32 s90, 0\n.p2align 6\n.A191:\nCvav 32,128,192,4\nRa 176,26,8192\nMvav 32,132,200\nEX 82,58\nMvav 32,136,208\nRa 180,26,9216\nMvav 32,140,216\nWL 6\nEX 83,59\n"
"Cvav 36,140,220,8\nRa 184,26,10240\nAD 84,84,82\nMvav 36,136,212\nD1 1,16,46,3072\ns_add_u32 m0, s89, 32768\nMvav 36,132,204\nRa 188,26,11264\nMvav 36,128,196\nAD 84,84,83\n"
"CV 75,82,83\nMvav 36,144,228\nRa 128,2,16384\nMvav 36,148,236\ns_add_u32 s46, s86, s88\nEX 80,60\nMvav 36,152,244\nRa 132,2,17408\nMvav 36,156,252\nWL 6\nEX 81,61\n"
"Mvav 32,156,248\nRa 136,2,18432\nAD 85,85,80\nMvav 32,152,240\nD1 1,16,46,0\nMvav 32,148,232\nRa 140,2,19456\nSv 112,1,16,49,0\nMvav 32,144,224\nAD 85,85,81\nCV 78,80,81\n"
"Cvav 40,160,192,12\nRa 144,2,20480\nMvav 40,164,200\nEX 82,62\nMvav 40,168,208\nRa 148,2,21504\nMvav 40,172,216\nWL 6\nEX 83,63\nCvav 44,172,220,16\nRa 152,2,22528\nAD 85,85,82\n"
"Mvav 44,168,212\nD1 1,16,46,1024\nMvav 44,164,204\nRa 156,2,23552\nMvav 44,160,196\nAD 85,85,83\nCV 79,82,83\nMvav 44,176,228\nRa 160,2,24576\nv_max_f32 v90, v84, v85\n"
"Mvav 44,180,236\ns_add_u32 s88, s88, 32768\nEX 80,32\nMvav 44,184,244\nRa 164,2,25600\nv_cmp_lt_f32 vcc, s63, v90\nMvav 44,188,252\nWL 8\nEX 81,33\nMvav 40,188,248\n"
"Ra 168,2,26624\ns_cbranch_vccnz .T195\n.T196:\nMvav 40,184,240\nD1 1,16,46,2048\nMvav 40,180,232\nRa 172,2,27648\nAD 86,86,84\nMvav 40,176,224\nAD 84,80,81\nCV 64,80,81\n"
"Maav 0,128,72\nRa 176,2,28672\nMaav 4,128,76\nEX 82,34\nMaav 12,132,76\nRa 180,2,29696\nMaav 8,132,72\ns_waitcnt vmcnt(13) lgkmcnt(8)\nEX 83,35\nMaav 16,136,72\nRa 184,2,30720\n"
"AD 84,84,82\nMaav 20,136,76\nD1 1,16,46,3072\ns_add_u32 m0, s89, 49152\nMaav 28,140,76\nRa 188,2,31744\ns_barrier\nMaav 24,140,72\nAD 84,84,83\nCV 65,82,83\nMaav 32,144,72\n"
"Ra 128,24,32768\nMaav 36,144,76\ns_add_u32 s46, s87, s88\nEX 80,36\nMaav 44,148,76\nRa 132,24,33792\nMaav 40,148,72\nWL 8\nEX 81,37\nMaav 48,152,72\nRa 136,24,34816\nAD 87,87,85\n"
"Maav 52,152,76\nD1 1,16,46,0\nMaav 60,156,76\nRa 140,24,35840\nMaav 56,156,72\nAD 85,80,81\nCV 68,80,81\nMaav 64,160,72\nRa 144,26,32768\nMaav 68,160,76\nEX 82,38\nMaav 76,164,76\n"
"Ra 148,26,33792\nMaav 72,164,72\nWL 8\nEX 83,39\nMaav 80,168,72\nRa 152,26,34816\nAD 85,85,82\nMaav 84,168,76\nD1 1,16,46,1024\nMaav 92,172,76\nRa 156,26,35840\nMaav 88,172,72\n"
"AD 85,85,83\nCV 69,82,83\nMaav 96,176,72\nRa 160,24,40960\nMaav 100,176,76\ns_xor_b32 s89, s89, 0x10000\nEX 80,40\nMaav 108,180,76\nRa 164,24,41984\nMaav 104,180,72\nWL 6\n"
"EX 81,41\nMaav 112,184,72\nRa 168,24,43008\nAD 84,84,80\nMaav 116,184,76\nD1 1,16,46,2048\nMaav 124,188,76\nRa 172,24,44032\nMaav 120,188,72\nAD 84,84,81\nCV 66,80,81\n"
"Cvav 48,128,192,4\nRa 176,26,40960\nMvav 48,132,200\nEX 82,42\nMvav 48,136,208\nRa 180,26,41984\nMvav 48,140,216\nWL 6\nEX 83,43\nCvav 52,140,220,8\nRa 184,26,43008\nAD 84,84,82\n"
"Mvav 52,136,212\nD1 1,16,46,3072\ns_add_u32 m0, s89, 0\nMvav 52,132,204\nRa 188,26,44032\nMvav 52,128,196\nAD 84,84,83\nCV 67,82,83\nMvav 52,144,228\nRa 128,2,49152\n"
"Mvav 52,148,236\ns_add_u32 s46, s86, s88\nEX 80,44\nMvav 52,152,244\nRa 132,2,50176\nv_xor_b32 v24, 0x10000, v24\nMvav 52,156,252\nWL 6\nEX 81,45\nMvav 48,156,248\nRa 136,2,51200\n"
"AD 85,85,80\nMvav 48,152,240\nD1 1,16,46,0\nMvav 48,148,232\nRa 140,2,52224\nSv 112,1,16,49,1024\nMvav 48,144,224\nAD 85,85,81\nCV 70,80,81\nCvav 56,160,192,12\nRa 144,2,53248\n"
"Mvav 56,164,200\ns_add_u32 s49, s49, 131072\nEX 82,46\nMvav 56,168,208\nRa 148,2,54272\nv_xor_b32 v26, 0x10000, v26\nMvav 56,172,216\nWL 6\nEX 83,47\nCvav 60,172,220,16\n"
"Ra 152,2,55296\nAD 85,85,82\nMvav 60,168,212\nD1 1,16,46,1024\nMvav 60,164,204\nRa 156,2,56320\nMvav 60,160,196\nAD 85,85,83\nCV 71,82,83\nMvav 60,176,228\nRa 160,2,57344\n"
"v_max_f32 v90, v84, v85\nMvav 60,180,236\ns_add_u32 s88, s88, 32768\nEX 80,48\nMvav 60,184,244\nRa 164,2,58368\nv_cmp_lt_f32 vcc, s63, v90\nMvav 60,188,252\nWL 8\nEX 81,49\n"
"Mvav 56,188,248\nRa 168,2,59392\ns_cbranch_vccnz .T197\n.T198:\nMvav 56,184,240\nD1 1,16,46,2048\nMvav 56,180,232\nRa 172,2,60416\nAD 86,86,84\nMvav 56,176,224\nAD 84,80,81\n"
"CV 72,80,81\nMaav 0,128,64\nRa 176,2,61440\nMaav 4,128,68\nEX 82,50\nMaav 12,132,68\nRa 180,2,62464\nMaav 8,132,64\ns_waitcnt vmcnt(13) lgkmcnt(8)\nEX 83,51\nMaav 16,136,64\n"
"Ra 184,2,63488\nAD 84,84,82\nMaav 20,136,68\nD1 1,16,46,3072\ns_add_u32 m0, s89, 16384\nMaav 28,140,68\nRa 188,2,64512\ns_barrier\nMaav 24,140,64\nAD 84,84,83\nCV 73,82,83\n"
"Maav 32,144,64\nRa 128,24,0\nMaav 36,144,68\ns_add_u32 s46, s87, s88\nEX 80,52\nMaav 44,148,68\nRa 132,24,1024\nv_xor_b32 v2, 0x10000, v2\nMaav 40,148,64\nWL 8\nEX 81,53\n"
"Maav 48,152,64\nRa 136,24,2048\nAD 87,87,85\nMaav 52,152,68\nD1 1,16,46,0\nMaav 60,156,68\nRa 140,24,3072\nMaav 56,156,64\nAD 85,80,81\nCV 76,80,81\nMaav 64,160,64\nRa 144,26,0\n"
"Maav 68,160,68\nEX 82,54\nMaav 76,164,68\nRa 148,26,1024\nMaav 72,164,64\nWL 8\nEX 83,55\nMaav 80,168,64\nRa 152,26,2048\nAD 85,85,82\nMaav 84,168,68\nD1 1,16,46,1024\n"
"Maav 92,172,68\nRa 156,26,3072\nMaav 88,172,64\nAD 85,85,83\nCV 77,82,83\nMaav 96,176,64\nRa 160,24,8192\nMaav 100,176,68\ns_sub_u32 s90, s90, 1\nEX 80,56\nMaav 108,180,68\n"
"Ra 164,24,9216\ns_cbranch_scc1 .E193\n.E194:\nMaav 104,180,64\nWL 6\nEX 81,57\nMaav 112,184,64\nRa 168,24,10240\nAD 84,84,80\nMaav 116,184,68\nD1 1,16,46,2048\n"
"s_sub_u32 s99, s99, 1\nMaav 124,188,68\nRa 172,24,11264\nMaav 120,188,64\nAD 84,84,81\nCV 74,80,81\ns_cbranch_scc0 .A191\ns_branch .A199\n.E193:\ns_swappc_b64 s[80:81], s[8:9]\n"
"s_branch .E194\n.HD144:\nbuffer_store_dword v25, off, s[16:19], s54 offset:4 sc0 sc1\nv_cmp_ne_u32 vcc, s15, v27\ns_nop 0\ns_cbranch_vccnz .S178\n.S179:\nv_and_b32 v27, 0x70, v1\n"
"buffer_load_dword v27, v27, s[16:19], s61 offen offset:8 sc0 sc1\ns_mov_b32 s90, 1\ns_swappc_b64 s[8:9], s[80:81]\nv_cmp_ne_u32 vcc, s15, v27\ns_nop 0\ns_cbranch_vccnz .S181\n"
".S182:\nv_and_b32 v27, 0xf0, v1\nbuffer_load_dword v27, v27, s[16:19], s61 offen offset:4 sc0 sc1\ns_mov_b32 s90, 1\ns_swappc_b64 s[8:9], s[80:81]\nv_cmp_ne_u32 vcc, s15, v27\n"
"s_nop 0\ns_cbranch_vccnz .S184\n.S185:\ns_mov_b32 s90, s91\ns_swappc_b64 s[8:9], s[80:81]\ns_branch .FF187\n.A199:\nCvav 32,128,192,4\nRa 176,26,8192\nMvav 32,132,200\nEX 82,58\n"
"Mvav 32,136,208\nRa 180,26,9216\nMvav 32,140,216\nWL 6\nEX 83,59\nCvav 36,140,220,8\nRa 184,26,10240\nAD 84,84,82\nMvav 36,136,212\nD1 1,16,46,3072\ns_add_u32 m0, s89, 32768\n"
"Mvav 36,132,204\nRa 188,26,11264\nMvav 36,128,196\nAD 84,84,83\nCV 75,82,83\ns_mov_b32 s94, s96\ns_add_u32 s95, s96, 65536\nv_and_b32 v108, 0xf0, v1\nMvav 36,144,228\n"
"Ra 128,2,16384\nMvav 36,148,236\ns_add_u32 s46, s86, s88\nEX 80,60\nMvav 36,152,244\nRa 132,2,17408\nMvav 36,156,252\nWL 6\nEX 81,61\nMvav 32,156,248\nRa 136,2,18432\nAD 85,85,80\n"
"Mvav 32,152,240\nD1 1,16,46,0\nMvav 32,148,232\nRa 140,2,19456\nSv 112,1,16,49,0\nMvav 32,144,224\nAD 85,85,81\nCV 78,80,81\n"
"buffer_load_dwordx4 v[100:103], v108, s[16:19], s65 offen offset:0 sc0 sc1\nbuffer_load_dwordx4 v[104:107], v108, s[16:19], s65 offen offset:256 sc0 sc1\nCvav 40,160,192,12\n"
"Ra 144,2,20480\nMvav 40,164,200\nEX 82,62\nMvav 40,168,208\nRa 148,2,21504\nMvav 40,172,216\nWL 6\nEX 83,63\nCvav 44,172,220,16\nRa 152,2,22528\nAD 85,85,82\nMvav 44,168,212\n"
"D1 1,16,46,1024\nMvav 44,164,204\nRa 156,2,23552\nMvav 44,160,196\nAD 85,85,83\nCV 79,82,83\nMvav 44,176,228\nRa 160,2,24576\nv_max_f32 v90, v84, v85\nMvav 44,180,236\n"
"s_add_u32 s88, s88, 32768\nEX 80,32\nMvav 44,184,244\nRa 164,2,25600\nv_cmp_lt_f32 vcc, s63, v90\nMvav 44,188,252\nWL 8\nEX 81,33\nMvav 40,188,248\nRa 168,2,26624\n"
"s_cbranch_vccnz .T202\n.T203:\nMvav 40,184,240\nD1 1,16,46,2048\nMvav 40,180,232\nRa 172,2,27648\nAD 86,86,84\nMvav 40,176,224\nAD 84,80,81\nCV 64,80,81\nMaav 0,128,72\n"
"Ra 176,2,28672\nMaav 4,128,76\nEX 82,34\nMaav 12,132,76\nRa 180,2,29696\nMaav 8,132,72\ns_waitcnt vmcnt(15) lgkmcnt(8)\nEX 83,35\nMaav 16,136,72\nRa 184,2,30720\nAD 84,84,82\n"
"Maav 20,136,76\nD1 1,16,46,3072\ns_add_u32 m0, s89, 49152\nMaav 28,140,76\nRa 188,2,31744\ns_barrier\nMaav 24,140,72\nAD 84,84,83\nCV 65,82,83\nMaav 32,144,72\nRa 128,24,32768\n"
"Maav 36,144,76\ns_add_u32 s46, s87, s88\nEX 80,36\nMaav 44,148,76\nRa 132,24,33792\nMaav 40,148,72\nWL 8\nEX 81,37\nMaav 48,152,72\nRa 136,24,34816\nAD 87,87,85\nMaav 52,152,76\n"
"D1 1,16,46,0\nMaav 60,156,76\nRa 140,24,35840\nMaav 56,156,72\n.W566:\ns_cbranch_execz .W570\nAD 85,80,81\nCV 68,80,81\nMaav 64,160,72\nRa 144,26,32768\nMaav 68,160,76\nEX 82,38\n"
"Maav 76,164,76\nRa 148,26,33792\nMaav 72,164,72\nWL 8\nEX 83,39\nMaav 80,168,72\nRa 152,26,34816\nAD 85,85,82\nMaav 84,168,76\nD1 1,16,46,1024\nMaav 92,172,76\nRa 156,26,35840\n"
"Maav 88,172,72\nAD 85,85,83\nCV 69,82,83\nMaav 96,176,72\nRa 160,24,40960\nMaav 100,176,76\ns_xor_b32 s89, s89, 0x10000\nEX 80,40\nMaav 108,180,76\nRa 164,24,41984\n"
"Maav 104,180,72\nWL 6\nEX 81,41\nMaav 112,184,72\nRa 168,24,43008\nAD 84,84,80\nMaav 116,184,76\nD1 1,16,46,2048\nMaav 124,188,76\nRa 172,24,44032\nMaav 120,188,72\nAD 84,84,81\n"
"CV 66,80,81\nCvav 48,128,192,4\nRa 176,26,40960\nMvav 48,132,200\ns_mov_b32 s86, s94\nEX 82,42\nMvav 48,136,208\nRa 180,26,41984\nMvav 48,140,216\nWL 6\nEX 83,43\n"
"Cvav 52,140,220,8\nRa 184,26,43008\nAD 84,84,82\nMvav 52,136,212\nD1 1,16,46,3072\ns_add_u32 m0, s89, 0\nMvav 52,132,204\nRa 188,26,44032\nMvav 52,128,196\nAD 84,84,83\n"
"CV 67,82,83\nMvav 52,144,228\nRa 128,2,49152\nMvav 52,148,236\ns_add_u32 s46, s86, s88\nEX 80,44\nMvav 52,152,244\nRa 132,2,50176\nv_xor_b32 v24, 0x10000, v24\nMvav 52,156,252\n"
"WL 6\nEX 81,45\nMvav 48,156,248\nRa 136,2,51200\nAD 85,85,80\nMvav 48,152,240\nD1 1,16,46,0\nMvav 48,148,232\nRa 140,2,52224\nSv 112,1,16,49,1024\nMvav 48,144,224\nAD 85,85,81\n"
"CV 70,80,81\nWV 9\nv_xor_b32 v109, s15, v102\nv_xor_b32 v110, s15, v106\nv_or_b32 v109, v109, v110\nv_cmp_ne_u32 vcc, 0, v109\ns_nop 3\ns_cmp_lg_u64 vcc, 0\n"
"s_cselect_b32 s67, 1, 0\ns_cselect_b32 s6, 0x40000000, s66\nCvav 56,160,192,12\nRa 144,2,53248\ns_add_u32 s5, s6, 0\nMvav 56,164,200\ns_add_u32 s49, s49, 131072\nEX 82,46\n"
"Mvav 56,168,208\nRa 148,2,54272\nv_xor_b32 v26, 0x10000, v26\nbuffer_load_dwordx4 a[192:195], v1, s[16:19], s5 offen offset:0 sc1\nMvav 56,172,216\nWL 6\nEX 83,47\n"
"Cvav 60,172,220,16\nRa 152,2,55296\nAD 85,85,82\nMvav 60,168,212\nD1 1,16,46,1024\nMvav 60,164,204\nRa 156,2,56320\nMvav 60,160,196\nAD 85,85,83\nCV 71,82,83\nMvav 60,176,228\n"
"Ra 160,2,57344\nv_max_f32 v90, v84, v85\nMvav 60,180,236\ns_add_u32 s88, s88, 32768\nEX 80,48\nMvav 60,184,244\nRa 164,2,58368\nv_cmp_lt_f32 vcc, s63, v90\nMvav 60,188,252\nWL 8\n"
"EX 81,49\nMvav 56,188,248\nRa 168,2,59392\ns_cbranch_vccnz .T204\n.T205:\nbuffer_load_dwordx4 a[196:199], v1, s[16:19], s5 offen offset:1024 sc1\nMvav 56,184,240\nD1 1,16,46,2048\n"
"Mvav 56,180,232\nRa 172,2,60416\nAD 86,86,84\nMvav 56,176,224\nAD 84,80,81\nCV 72,80,81\nMaav 0,128,64\nRa 176,2,61440\nMaav 4,128,68\nEX 82,50\nMaav 12,132,68\nRa 180,2,62464\n"
"Maav 8,132,64\nWL 8\nEX 83,51\nMaav 16,136,64\nRa 184,2,63488\nAD 84,84,82\nbuffer_load_dwordx4 a[200:203], v1, s[16:19], s5 offen offset:2048 sc1\nMaav 20,136,68\n"
"D1 1,16,46,3072\ns_add_u32 m0, s89, 16384\nMaav 28,140,68\nRa 188,2,64512\ns_barrier\nMaav 24,140,64\nAD 84,84,83\nCV 73,82,83\nMaav 32,144,64\nRa 128,24,0\nMaav 36,144,68\n"
"s_add_u32 s46, s87, s88\nEX 80,52\nMaav 44,148,68\nRa 132,24,1024\nv_xor_b32 v2, 0x10000, v2\nMaav 40,148,64\nWL 8\nEX 81,53\nMaav 48,152,64\nRa 136,24,2048\nAD 87,87,85\n"
"buffer_load_dwordx4 a[204:207], v1, s[16:19], s5 offen offset:3072 sc1\nMaav 52,152,68\nD1 1,16,46,0\nMaav 60,156,68\nRa 140,24,3072\nMaav 56,156,64\nAD 85,80,81\nCV 76,80,81\n"
"Maav 64,160,64\nRa 144,26,0\nMaav 68,160,68\ns_cmp_lg_u32 s92, 0\nEX 82,54\nMaav 76,164,68\nRa 148,26,1024\ns_cbranch_scc0 .E200\nv_cmpx_gt_i32 vcc, 19, v3\nv_mov_b32 v15, s97\n"
"v_cmpx_gt_i32 vcc, 18, v3\nv_mov_b32 v14, s97\nv_cmpx_gt_i32 vcc, 17, v3\nv_mov_b32 v13, s97\nv_cmpx_gt_i32 vcc, 16, v3\nv_mov_b32 v12, s97\nv_cmpx_gt_i32 vcc, 3, v3\n"
"v_mov_b32 v7, s97\nv_cmpx_gt_i32 vcc, 2, v3\nv_mov_b32 v6, s97\nv_cmpx_gt_i32 vcc, 1, v3\nv_mov_b32 v5, s97\nv_cmpx_gt_i32 vcc, 0, v3\nv_mov_b32 v4, s97\ns_mov_b64 exec, -1\n"
"s_nop 2\n.E200:\ns_add_u32 s5, s6, 4096\nMaav 72,164,64\nWL 8\nEX 83,55\nMaav 80,168,64\nRa 152,26,2048\nAD 85,85,82\n"
"buffer_load_dwordx4 a[208:211], v1, s[16:19], s5 offen offset:0 sc1\nMaav 84,168,68\nD1 1,16,46,1024\nMaav 92,172,68\nRa 156,26,3072\nMaav 88,172,64\nAD 85,85,83\nCV 77,82,83\n"
"Maav 96,176,64\nRa 160,24,8192\nMaav 100,176,68\nEX 80,56\nMaav 108,180,68\nRa 164,24,9216\nMaav 104,180,64\nWL 6\nEX 81,57\nMaav 112,184,64\nRa 168,24,10240\nAD 84,84,80\n"
"buffer_load_dwordx4 a[212:215], v1, s[16:19], s5 offen offset:1024 sc1\nMaav 116,184,68\nD1 1,16,46,2048\nMaav 124,188,68\nRa 172,24,11264\nMaav 120,188,64\nAD 84,84,81\n"
"CV 74,80,81\nCvav 32,128,192,4\nRa 176,26,8192\nMvav 32,132,200\nEX 82,58\nMvav 32,136,208\nRa 180,26,9216\nMvav 32,140,216\nWL 6\nEX 83,59\nCvav 36,140,220,8\nRa 184,26,10240\n"
"AD 84,84,82\nbuffer_load_dwordx4 a[216:219], v1, s[16:19], s5 offen offset:2048 sc1\nMvav 36,136,212\nD1 1,16,46,3072\ns_add_u32 m0, s89, 32768\nMvav 36,132,204\nRa 188,26,11264\n"
"Mvav 36,128,196\nAD 84,84,83\nCV 75,82,83\nMvav 36,144,228\nRa 128,2,16384\nMvav 36,148,236\ns_add_u32 s46, s86, s88\nEX 80,60\nMvav 36,152,244\nRa 132,2,17408\nMvav 36,156,252\n"
"WL 6\nEX 81,61\nMvav 32,156,248\nRa 136,2,18432\nAD 85,85,80\nbuffer_load_dwordx4 a[220:223], v1, s[16:19], s5 offen offset:3072 sc1\nMvav 32,152,240\nD1 1,16,46,0\n"
"Mvav 32,148,232\nRa 140,2,19456\nMvav 32,144,224\nAD 85,85,81\nCV 78,80,81\nCvav 40,160,192,12\nRa 144,2,20480\nMvav 40,164,200\ns_mov_b32 s87, s95\nEX 82,62\nMvav 40,168,208\n"
"Ra 148,2,21504\ns_add_u32 s5, s6, 16384\nMvav 40,172,216\nWL 6\nEX 83,63\nCvav 44,172,220,16\nRa 152,2,22528\nAD 85,85,82\n"
"buffer_load_dwordx4 a[224:227], v1, s[16:19], s5 offen offset:0 sc1\nMvav 44,168,212\nD1 1,16,46,1024\nMvav 44,164,204\nRa 156,2,23552\nMvav 44,160,196\nAD 85,85,83\nCV 79,82,83\n"
"Mvav 44,176,228\nRa 160,2,24576\nv_max_f32 v90, v84, v85\nMvav 44,180,236\ns_add_u32 s88, s88, 32768\nEX 80,32\nMvav 44,184,244\nRa 164,2,25600\nv_cmp_lt_f32 vcc, s63, v90\n"
"Mvav 44,188,252\nWL 8\nEX 81,33\nMvav 40,188,248\nRa 168,2,26624\ns_cbranch_vccnz .T208\n.T209:\nbuffer_load_dwordx4 a[228:231], v1, s[16:19], s5 offen offset:1024 sc1\n"
"Mvav 40,184,240\nD1 1,16,46,2048\nMvav 40,180,232\nRa 172,2,27648\nAD 86,86,84\nMvav 40,176,224\nAD 84,80,81\nCV 64,80,81\nMaav 0,128,72\nRa 176,2,28672\nMaav 4,128,76\nEX 82,34\n"
"Maav 12,132,76\nRa 180,2,29696\nMaav 8,132,72\ns_waitcnt vmcnt(22) lgkmcnt(8)\nEX 83,35\nMaav 16,136,72\nRa 184,2,30720\nAD 84,84,82\n"
"buffer_load_dwordx4 a[232:235], v1, s[16:19], s5 offen offset:2048 sc1\nMaav 20,136,76\nD1 1,16,46,3072\ns_add_u32 m0, s89, 49152\nMaav 28,140,76\nRa 188,2,31744\ns_barrier\n"
"Maav 24,140,72\nAD 84,84,83\nCV 65,82,83\nMaav 32,144,72\nRa 128,24,32768\nMaav 36,144,76\ns_add_u32 s46, s87, s88\nEX 80,36\nMaav 44,148,76\nRa 132,24,33792\nMaav 40,148,72\n"
"WL 8\nEX 81,37\nMaav 48,152,72\nRa 136,24,34816\nAD 87,87,85\nbuffer_load_dwordx4 a[236:239], v1, s[16:19], s5 offen offset:3072 sc1\nMaav 52,152,76\nD1 1,16,46,0\nMaav 60,156,76\n"
"Ra 140,24,35840\nMaav 56,156,72\nAD 85,80,81\nCV 68,80,81\nMaav 64,160,72\nRa 144,26,32768\nMaav 68,160,76\ns_cmp_eq_u32 s13, 7\nEX 82,38\nMaav 76,164,76\nRa 148,26,33792\n"
"s_cbranch_scc0 .E206\nv_cmpx_gt_i32 vcc, 19, v0\nv_mov_b32 v15, s97\nv_cmpx_gt_i32 vcc, 18, v0\nv_mov_b32 v14, s97\nv_cmpx_gt_i32 vcc, 17, v0\nv_mov_b32 v13, s97\n"
"v_cmpx_gt_i32 vcc, 16, v0\nv_mov_b32 v12, s97\nv_cmpx_gt_i32 vcc, 3, v0\nv_mov_b32 v7, s97\nv_cmpx_gt_i32 vcc, 2, v0\nv_mov_b32 v6, s97\nv_cmpx_gt_i32 vcc, 1, v0\n"
"v_mov_b32 v5, s97\nv_cmpx_gt_i32 vcc, 0, v0\nv_mov_b32 v4, s97\ns_mov_b64 exec, -1\ns_nop 2\n.E206:\ns_add_u32 s5, s6, 20480\nMaav 72,164,72\nWL 8\nEX 83,39\nMaav 80,168,72\n"
"Ra 152,26,34816\nAD 85,85,82\nbuffer_load_dwordx4 a[240:243], v1, s[16:19], s5 offen offset:0 sc1\nMaav 84,168,76\nD1 1,16,46,1024\nMaav 92,172,76\nRa 156,26,35840\n"
"Maav 88,172,72\nAD 85,85,83\nCV 69,82,83\nMaav 96,176,72\nRa 160,24,40960\nMaav 100,176,76\ns_xor_b32 s89, s89, 0x10000\nEX 80,40\nMaav 108,180,76\nRa 164,24,41984\n"
"Maav 104,180,72\nWL 6\nEX 81,41\nMaav 112,184,72\nRa 168,24,43008\nAD 84,84,80\nbuffer_load_dwordx4 a[244:247], v1, s[16:19], s5 offen offset:1024 sc1\nMaav 116,184,76\n"
"D1 1,16,46,2048\nMaav 124,188,76\nRa 172,24,44032\nMaav 120,188,72\nAD 84,84,81\nCV 66,80,81\nCvav 48,128,192,4\nRa 176,26,40960\nMvav 48,132,200\nEX 82,42\nMvav 48,136,208\n"
"Ra 180,26,41984\nMvav 48,140,216\nWL 6\nEX 83,43\nCvav 52,140,220,8\nRa 184,26,43008\nAD 84,84,82\nbuffer_load_dwordx4 a[248:251], v1, s[16:19], s5 offen offset:2048 sc1\n"
"Mvav 52,136,212\nD1 1,16,46,3072\ns_add_u32 m0, s89, 0\nMvav 52,132,204\nRa 188,26,44032\nMvav 52,128,196\nAD 84,84,83\nCV 67,82,83\nMvav 52,144,228\nRa 128,2,49152\n"
"Mvav 52,148,236\ns_add_u32 s46, s86, s88\nEX 80,44\nMvav 52,152,244\nRa 132,2,50176\nv_xor_b32 v24, 0x10000, v24\nMvav 52,156,252\nWL 6\nEX 81,45\nMvav 48,156,248\nRa 136,2,51200\n"
"AD 85,85,80\nbuffer_load_dwordx4 a[252:255], v1, s[16:19], s5 offen offset:3072 sc1\nMvav 48,152,240\nD1 1,16,46,0\nMvav 48,148,232\nRa 140,2,52224\nMvav 48,144,224\nAD 85,85,81\n"
"CV 70,80,81\nCvav 56,160,192,12\nRa 144,2,53248\nMvav 56,164,200\nEX 82,46\nMvav 56,168,208\nRa 148,2,54272\nv_xor_b32 v26, 0x10000, v26\nMvav 56,172,216\nWL 6\nEX 83,47\n"
"Cvav 60,172,220,16\nRa 152,2,55296\nAD 85,85,82\nMvav 60,168,212\nD1 1,16,46,1024\nMvav 60,164,204\nRa 156,2,56320\nMvav 60,160,196\nAD 85,85,83\nCV 71,82,83\nMvav 60,176,228\n"
"Ra 160,2,57344\nv_max_f32 v90, v84, v85\nMvav 60,180,236\ns_add_u32 s88, s88, 32768\nEX 80,48\nMvav 60,184,244\nRa 164,2,58368\nv_cmp_lt_f32 vcc, s63, v90\nMvav 60,188,252\nWL 8\n"
"EX 81,49\nMvav 56,188,248\nRa 168,2,59392\ns_cbranch_vccnz .T210\n.T211:\nMvav 56,184,240\nD1 1,16,46,2048\nMvav 56,180,232\nRa 172,2,60416\nAD 86,86,84\nMvav 56,176,224\n"
"AD 84,80,81\nCV 72,80,81\nMaav 0,128,64\nRa 176,2,61440\nbuffer_load_dwordx4 v[100:103], v108, s[16:19], s65 offen offset:0 sc0 sc1\n"
"buffer_load_dwordx4 v[104:107], v108, s[16:19], s65 offen offset:256 sc0 sc1\nMaav 4,128,68\nEX 82,50\nMaav 12,132,68\nRa 180,2,62464\nMaav 8,132,64\n"
"s_waitcnt vmcnt(22) lgkmcnt(8)\nEX 83,51\nMaav 16,136,64\nRa 184,2,63488\nAD 84,84,82\nMaav 20,136,68\nD1 1,16,46,3072\ns_add_u32 m0, s89, 16384\nMaav 28,140,68\nRa 188,2,64512\n"
"s_barrier\nMaav 24,140,64\nAD 84,84,83\nCV 73,82,83\nMaav 32,144,64\nRa 128,24,0\nMaav 36,144,68\ns_add_u32 s46, s87, s88\nEX 80,52\nMaav 44,148,68\nRa 132,24,1024\n"
"v_xor_b32 v2, 0x10000, v2\nMaav 40,148,64\nWL 8\nEX 81,53\nMaav 48,152,64\nRa 136,24,2048\nAD 87,87,85\nMaav 52,152,68\nD1 1,16,46,0\nMaav 60,156,68\nRa 140,24,3072\n"
"Maav 56,156,64\nAD 85,80,81\nCV 76,80,81\nMaav 64,160,64\nRa 144,26,0\nMaav 68,160,68\nEX 82,54\nMaav 76,164,68\nRa 148,26,1024\nMaav 72,164,64\nWL 8\nEX 83,55\nMaav 80,168,64\n"
"Ra 152,26,2048\nAD 85,85,82\nMaav 84,168,68\nD1 1,16,46,1024\nMaav 92,172,68\nRa 156,26,3072\nMaav 88,172,64\nAD 85,85,83\nCV 77,82,83\nMaav 96,176,64\nRa 160,24,8192\n"
"Maav 100,176,68\nEX 80,56\nMaav 108,180,68\nRa 164,24,9216\nMaav 104,180,64\nWL 10\nEX 81,57\nMaav 112,184,64\nRa 168,24,10240\nAD 84,84,80\nMaav 116,184,68\nD1 1,16,46,2048\n"
"Maav 124,188,68\nRa 172,24,11264\nMaav 120,188,64\nAD 84,84,81\nCV 74,80,81\nWV 4\ns_cmp_lg_u32 s67, 0\ns_cbranch_scc0 .E219\nv_xor_b32 v109, s15, v102\nv_xor_b32 v110, s15, v106\n"
"v_or_b32 v109, v109, v110\nv_cmp_ne_u32 vcc, 0, v109\ns_nop 0\ns_cbranch_vccnz .S216\n.S217:\ns_add_u32 s5, s66, 0\n"
"buffer_load_dwordx4 a[192:195], v1, s[16:19], s5 offen offset:0 sc1\nbuffer_load_dwordx4 a[196:199], v1, s[16:19], s5 offen offset:1024 sc1\n.W567:\ns_cbranch_execz .WB152\n"
"buffer_load_dwordx4 a[200:203], v1, s[16:19], s5 offen offset:2048 sc1\nbuffer_load_dwordx4 a[204:207], v1, s[16:19], s5 offen offset:3072 sc1\ns_add_u32 s5, s66, 4096\n"
"buffer_load_dwordx4 a[208:211], v1, s[16:19], s5 offen offset:0 sc1\nbuffer_load_dwordx4 a[212:215], v1, s[16:19], s5 offen offset:1024 sc1\n"
"buffer_load_dwordx4 a[216:219], v1, s[16:19], s5 offen offset:2048 sc1\nbuffer_load_dwordx4 a[220:223], v1, s[16:19], s5 offen offset:3072 sc1\ns_add_u32 s5, s66, 16384\n"
"buffer_load_dwordx4 a[224:227], v1, s[16:19], s5 offen offset:0 sc1\nbuffer_load_dwordx4 a[228:231], v1, s[16:19], s5 offen offset:1024 sc1\n"
"buffer_load_dwordx4 a[232:235], v1, s[16:19], s5 offen offset:2048 sc1\nbuffer_load_dwordx4 a[236:239], v1, s[16:19], s5 offen offset:3072 sc1\ns_add_u32 s5, s66, 20480\n"
"buffer_load_dwordx4 a[240:243], v1, s[16:19], s5 offen offset:0 sc1\nbuffer_load_dwordx4 a[244:247], v1, s[16:19], s5 offen offset:1024 sc1\n"
"buffer_load_dwordx4 a[248:251], v1, s[16:19], s5 offen offset:2048 sc1\nbuffer_load_dwordx4 a[252:255], v1, s[16:19], s5 offen offset:3072 sc1\nWV 0\ns_mov_b32 s67, 0\n.E219:\n"
"v_cmp_lt_f32 vcc, 0, v101\nSU 20,100,88\nv_mul_f32 v101, 0x3e800000, v101\nv_cndmask_b32 v20, 0, v20, vcc\nv_cmp_lt_f32 vcc, 0, v105\nSU 21,104,89\n"
"v_mul_f32 v105, 0x3e800000, v105\nv_cndmask_b32 v21, 0, v21, vcc\nv_or_b32 v91, v20, v21\nv_cmp_ne_u32 vcc, 0, v91\ns_nop 0\ns_cbranch_vccnz .D214\nAD 86,86,101\nAD 87,87,105\n"
".D215:\nMava 0,130,192\nMava 8,128,192\nEX 82,58\nMava 12,128,196\nMava 4,130,196\nEX 83,59\nMava 16,130,200\nAD 84,84,82\nMava 24,128,200\nD1 1,16,46,3072\nMava 28,128,204\n"
"s_add_u32 m0, s89, 32768\ns_add_u32 s46, s86, s88\nMava 20,130,204\nAD 84,84,83\nCV 75,82,83\nMava 32,130,208\nRa 128,2,16384\nv_xor_b32 v3, 0x10000, v2\n"
"v_add_u32 v0, 0x20000, v1\nMava 40,128,208\nEX 80,60\nMava 44,128,212\nRa 132,2,17408\ns_sub_u32 s94, s49, 1835008\nMava 36,130,212\nEX 81,61\nMava 48,130,216\nRa 136,2,18432\n"
"AD 85,85,80\ns_xor_b32 s92, s94, 0x202000\nMava 56,128,216\nD1 1,16,46,0\ns_lshl_b32 s89, s11, 12\nMava 60,128,220\nRa 140,2,19456\ns_lshl1_add_u32 s2, s12, 0\nMava 52,130,220\n"
"AD 85,85,81\nCV 78,80,81\nMava 64,130,224\nRa 144,2,20480\ns_lshl3_add_u32 s2, s2, s13\ns_lshl4_add_u32 s2, s2, 0xc048200\nMava 72,128,224\nEX 82,62\nMava 76,128,228\n"
"Ra 148,2,21504\ns_and_b32 s3, s72, 0x2000\nMava 68,130,228\nEX 83,63\nMava 80,130,232\nRa 152,2,22528\nAD 85,85,82\nMava 88,128,232\nD1 1,16,46,1024\ns_cmp_eq_u32 s11, 1\n"
"s_cselect_b32 s54, s2, s54\ns_cselect_b32 s3, s3, 0\nMava 92,128,236\nRa 156,2,23552\nv_xor_b32 v25, s3, v25\nMava 84,130,236\nAD 85,85,83\nCV 79,82,83\nMava 96,130,240\n"
"Ra 160,2,24576\nv_max_f32 v90, v84, v85\nMava 104,128,240\nv_cmp_lt_f32 vcc, s63, v90\nMava 108,128,244\nRa 164,2,25600\nMava 100,130,244\nWL 8\nMava 112,130,248\nRa 168,2,26624\n"
"Mava 120,128,248\nD1 1,16,46,2048\nMava 124,128,252\nRa 172,2,27648\nMava 116,130,252\ns_cbranch_vccnz .T220\n.T221:\nAD 86,86,84\nAD 87,87,85\nMaav 0,128,72\nRa 176,2,28672\n"
"v_mov_b32 v84, v86\nv_mov_b32 v85, v87\nMaav 4,128,76\nv_permlane32_swap_b32 v86, v84\nv_permlane32_swap_b32 v87, v85\nMaav 12,132,76\nRa 180,2,29696\nAD 86,86,84\nMaav 8,132,72\n"
"WL 8\nAD 87,87,85\nMaav 16,136,72\nRa 184,2,30720\nv_mov_b32 v84, v86\nv_mov_b32 v85, v87\nMaav 20,136,76\nD1 1,16,46,3072\nv_permlane16_swap_b32 v86, v84\nMaav 28,140,76\n"
"Ra 188,2,31744\nv_permlane16_swap_b32 v87, v85\nMaav 24,140,72\nAD 86,86,84\nAD 87,87,85\nMaav 32,144,72\nRa 128,2,0\nv_rcp_f32 v86, v86\nMaav 36,144,76\nv_rcp_f32 v87, v87\n"
"Maav 44,148,76\nRa 132,2,1024\nAR 82,0\nAR 83,1\nMU 82,82,86\nMU 83,83,86\nMaav 40,148,72\nWL 8\ns_add_u32 m0, s89, 131072\ns_add_u32 s46, s96, 1212416\nCV 96,82,83\nAR 82,2\n"
"Maav 48,152,72\nRa 136,2,2048\nAR 83,3\nMU 82,82,86\nMU 83,83,86\nCV 97,82,83\nMaav 52,152,76\nD1 1,16,46,0\nAR 82,4\nAR 83,5\nMU 82,82,87\nMaav 60,156,76\nRa 140,2,3072\n"
"MU 83,83,87\nCV 100,82,83\nAR 82,6\nAR 83,7\nMaav 56,156,72\nMU 82,82,87\nMU 83,83,87\nCV 101,82,83\nAR 82,8\nAR 83,9\nMaav 64,160,72\nRa 144,2,4096\nMU 82,82,86\nMU 83,83,86\n"
"CV 98,82,83\nAR 82,10\nMaav 68,160,76\nAR 83,11\nMU 82,82,86\nMU 83,83,86\nCV 99,82,83\nMaav 76,164,76\nRa 148,2,5120\nAR 82,12\nAR 83,13\nMU 82,82,87\nMU 83,83,87\n"
"Maav 72,164,72\nWL 8\nCV 102,82,83\nAR 82,14\nAR 83,15\nMU 82,82,87\nMaav 80,168,72\nRa 152,2,6144\nMU 83,83,87\nCV 103,82,83\nAR 82,16\nAR 83,17\nMaav 84,168,76\nD1 1,16,46,1024\n"
"MU 82,82,86\nMU 83,83,86\nCV 104,82,83\nMaav 92,172,76\nRa 156,2,7168\nAR 82,18\nAR 83,19\nMU 82,82,86\nMU 83,83,86\nMaav 88,172,72\nCV 105,82,83\nAR 82,20\nAR 83,21\nMU 82,82,87\n"
"MU 83,83,87\nMaav 96,176,72\nRa 160,2,8192\nCV 108,82,83\nAR 82,22\nAR 83,23\nMU 82,82,87\nMaav 100,176,76\nMU 83,83,87\nCV 109,82,83\nAR 82,24\nAR 83,25\nMaav 108,180,76\n"
"Ra 164,2,9216\nMU 82,82,86\nMU 83,83,86\nCV 106,82,83\nAR 82,26\nMaav 104,180,72\nWL 8\nAR 83,27\nMU 82,82,86\nMU 83,83,86\nCV 107,82,83\nMaav 112,184,72\nRa 168,2,10240\n"
"AR 82,28\nAR 83,29\nMU 82,82,87\nMU 83,83,87\nMaav 116,184,76\nD1 1,16,46,2048\nCV 110,82,83\nAR 82,30\nAR 83,31\nMaav 124,188,76\nRa 172,2,11264\nMU 82,82,87\nMU 83,83,87\n"
"CV 111,82,83\nMaav 120,188,72\nAR 82,32\nAR 83,33\nMU 82,82,86\nMU 83,83,86\nZvav 128,128,96\nRa 176,2,12288\nCV 112,82,83\nAR 82,34\nAR 83,35\nMU 82,82,86\nMvav 128,132,104\n"
"MU 83,83,86\nCV 113,82,83\nAR 82,36\nAR 83,37\nZvav 132,132,108\nRa 180,2,13312\nMU 82,82,87\nMU 83,83,87\nCV 116,82,83\nMvav 132,128,100\nWL 8\nAR 82,38\nAR 83,39\nMU 82,82,87\n"
"Zvav 140,136,100\nRa 184,2,14336\nMU 83,83,87\nCV 117,82,83\nAR 82,40\nMvav 140,140,108\nD1 1,16,46,3072\nAR 83,41\nMU 82,82,86\nMU 83,83,86\nZvav 136,140,104\nRa 188,2,15360\n"
"s_barrier\nCV 114,82,83\nAR 82,42\nAR 83,43\nMvav 136,136,96\nMU 82,82,86\nMU 83,83,86\nCV 115,82,83\nAR 82,44\nZvav 144,144,96\nRa 128,2,32768\nAR 83,45\nMU 82,82,87\n"
"MU 83,83,87\nMvav 144,148,104\nCV 118,82,83\nAR 82,46\nAR 83,47\nMU 82,82,87\nbuffer_store_dword v25, off, s[16:19], s54 offset:512 sc0 sc1\nZvav 148,148,108\nRa 132,2,33792\n"
"MU 83,83,87\nCV 119,82,83\nAR 82,48\nMvav 148,144,100\nWL 8\ns_add_u32 m0, s89, 114688\ns_add_u32 s46, s96, 1179648\nAR 83,49\nMU 82,82,86\nZvav 156,152,100\nRa 136,2,34816\n"
"MU 83,83,86\nCV 120,82,83\nAR 82,50\nMvav 156,156,108\nD1 1,16,46,0\nAR 83,51\nMU 82,82,86\nMU 83,83,86\nZvav 152,156,104\nRa 140,2,35840\nCV 121,82,83\nAR 82,52\nAR 83,53\n"
"Mvav 152,152,96\nMU 82,82,87\nMU 83,83,87\nCV 124,82,83\nAR 82,54\nZvav 64,160,96\nRa 144,2,36864\nAR 83,55\nMU 82,82,87\nMU 83,83,87\nCV 125,82,83\nMvav 64,164,104\nAR 82,56\n"
"AR 83,57\nMU 82,82,86\nMU 83,83,86\nZvav 68,164,108\nRa 148,2,37888\nCV 122,82,83\nAR 82,58\nAR 83,59\nMvav 68,160,100\nWL 8\nMU 82,82,86\nMU 83,83,86\nCV 123,82,83\n"
"Zvav 76,168,100\nRa 152,2,38912\nAR 82,60\nAR 83,61\nMU 82,82,87\nMvav 76,172,108\nD1 1,16,46,1024\nMU 83,83,87\nCV 126,82,83\nAR 82,62\nZvav 72,172,104\nRa 156,2,39936\nAR 83,63\n"
"MU 82,82,87\nMU 83,83,87\nCV 127,82,83\nMvav 72,168,96\nAR 82,64\nAR 83,65\nMU 82,82,86\nMU 83,83,86\nMvav 128,176,112\nRa 160,2,40960\nCV 32,82,83\nAR 82,66\nAR 83,67\n"
"Mvav 128,180,120\nMU 82,82,86\nMU 83,83,86\nCV 33,82,83\nAR 82,68\nMvav 132,180,124\nRa 164,2,41984\nAR 83,69\nMU 82,82,87\nMU 83,83,87\nMvav 132,176,116\nWL 8\nCV 36,82,83\n"
"AR 82,70\nAR 83,71\nMU 82,82,87\nMvav 140,184,116\nRa 168,2,43008\nMU 83,83,87\nCV 37,82,83\nAR 82,72\nMvav 140,188,124\nD1 1,16,46,2048\nAR 83,73\nMU 82,82,86\nMU 83,83,86\n"
"Mvav 136,188,120\nRa 172,2,44032\nCV 34,82,83\nAR 82,74\nAR 83,75\nMvav 136,184,112\nMU 82,82,86\nMU 83,83,86\nCV 35,82,83\nAR 82,76\nMvav 144,128,112\nRa 176,2,45056\nAR 83,77\n"
"MU 82,82,87\nMU 83,83,87\nCV 38,82,83\nMvav 144,132,120\nAR 82,78\nAR 83,79\nMU 82,82,87\nMU 83,83,87\nMvav 148,132,124\nRa 180,2,46080\nCV 39,82,83\nAR 82,80\nAR 83,81\n"
"Mvav 148,128,116\nWL 8\nMU 82,82,86\nMU 83,83,86\nCV 40,82,83\nMvav 156,136,116\nRa 184,2,47104\nAR 82,82\nAR 83,83\nMU 82,82,86\nMvav 156,140,124\nD1 1,16,46,3072\nMU 83,83,86\n"
"CV 41,82,83\nAR 82,84\nMvav 152,140,120\nRa 188,2,48128\nAR 83,85\nMU 82,82,87\nMU 83,83,87\nCV 44,82,83\nMvav 152,136,112\nAR 82,86\nAR 83,87\nMU 82,82,87\nMU 83,83,87\n"
"Mvav 64,144,112\nRa 128,2,49152\nCV 45,82,83\nAR 82,88\nAR 83,89\nMvav 64,148,120\nMU 82,82,86\nMU 83,83,86\nCV 42,82,83\nAR 82,90\nMvav 68,148,124\nRa 132,2,50176\nAR 83,91\n"
"MU 82,82,86\nMU 83,83,86\nMvav 68,144,116\nWL 8\ns_add_u32 m0, s89, 147456\ns_add_u32 s46, s96, 1245184\nCV 43,82,83\nAR 82,92\nMvav 76,152,116\nRa 136,2,51200\nAR 83,93\n"
"MU 82,82,87\nMU 83,83,87\nMvav 76,156,124\nD1 1,16,46,0\nCV 46,82,83\nAR 82,94\nAR 83,95\nMvav 72,156,120\nRa 140,2,52224\nMU 82,82,87\nMU 83,83,87\nCV 47,82,83\nMvav 72,152,112\n"
"AR 82,96\nAR 83,97\nMU 82,82,86\nMU 83,83,86\nMvav 128,160,32\nRa 144,2,53248\nCV 48,82,83\nAR 82,98\nAR 83,99\nMU 82,82,86\nMvav 128,164,40\nMU 83,83,86\nCV 49,82,83\nAR 82,100\n"
"AR 83,101\nMvav 132,164,44\nRa 148,2,54272\nMU 82,82,87\nMU 83,83,87\nCV 52,82,83\nMvav 132,160,36\nWL 8\nAR 82,102\nAR 83,103\nMU 82,82,87\nMvav 140,168,36\nRa 152,2,55296\n"
"MU 83,83,87\nCV 53,82,83\nAR 82,104\nMvav 140,172,44\nD1 1,16,46,1024\nAR 83,105\nMU 82,82,86\nMU 83,83,86\nMvav 136,172,40\nRa 156,2,56320\nCV 50,82,83\nAR 82,106\nAR 83,107\n"
"Mvav 136,168,32\nMU 82,82,86\nMU 83,83,86\nCV 51,82,83\nAR 82,108\nMvav 144,176,32\nRa 160,2,57344\nAR 83,109\nMU 82,82,87\nMU 83,83,87\nCV 54,82,83\nMvav 144,180,40\nAR 82,110\n"
"AR 83,111\nMU 82,82,87\nMU 83,83,87\nMvav 148,180,44\nRa 164,2,58368\nCV 55,82,83\nAR 82,112\nAR 83,113\nMvav 148,176,36\nWL 8\nMU 82,82,86\nMU 83,83,86\nCV 56,82,83\n"
"Mvav 156,184,36\nRa 168,2,59392\nAR 82,114\nAR 83,115\nMU 82,82,86\nMvav 156,188,44\nD1 1,16,46,2048\nMU 83,83,86\nCV 57,82,83\nAR 82,116\nMvav 152,188,40\nRa 172,2,60416\n"
"AR 83,117\nMU 82,82,87\nMU 83,83,87\nMvav 152,184,32\nCV 60,82,83\nAR 82,118\nAR 83,119\n.W568:\ns_cbranch_execz .WB152\nMU 82,82,87\nMvav 64,128,32\nRa 176,2,61440\nMU 83,83,87\n"
"CV 61,82,83\nAR 82,120\nAR 83,121\nMvav 64,132,40\nMU 82,82,86\nMU 83,83,86\nCV 58,82,83\nAR 82,122\nMvav 68,132,44\nRa 180,2,62464\nAR 83,123\nMU 82,82,86\nMU 83,83,86\n"
"Mvav 68,128,36\ns_waitcnt vmcnt(16) lgkmcnt(8)\nCV 59,82,83\nAR 82,124\nAR 83,125\nMvav 76,136,36\nRa 184,2,63488\nMU 82,82,87\nMU 83,83,87\nCV 62,82,83\nMvav 76,140,44\n"
"D1 1,16,46,3072\nAR 82,126\nAR 83,127\nMU 82,82,87\nMvav 72,140,40\nRa 188,2,64512\ns_barrier\nMU 83,83,87\nCV 63,82,83\nMvav 72,136,32\nMvav 128,144,48\nRa 128,3,0\n"
"Mvav 128,148,56\nMvav 132,148,60\nRa 132,3,1024\nMvav 132,144,52\nWL 8\nMvav 140,152,52\nRa 136,3,2048\nMvav 140,156,60\nMvav 136,156,56\nRa 140,3,3072\nMvav 136,152,48\n"
"Mvav 144,160,48\nRa 144,3,4096\nMvav 144,164,56\nMvav 148,164,60\nRa 148,3,5120\nCV 132,132,133\nMvav 148,160,52\nWL 8\nCV 133,134,135\nMvav 156,168,52\nRa 152,3,6144\n"
"CV 134,140,141\nMvav 156,172,60\nCV 135,142,143\ns_add_u32 s5, s94, 0\nMvav 152,172,56\nRa 156,3,7168\nSv 132,1,16,5,1024\nMvav 152,168,48\nCV 128,128,129\nCV 129,130,131\n"
"Mvav 64,176,48\nRa 160,3,8192\nCV 130,136,137\nMvav 64,180,56\nbuffer_load_dword v27, off, s[16:19], s60 offset:512 sc0 sc1\nCV 131,138,139\nMvav 68,180,60\nRa 164,3,9216\n"
"Sv 128,1,16,5,0\nMvav 68,176,52\nWL 6\nCV 148,148,149\nMvav 76,184,52\nRa 168,3,10240\nCV 149,150,151\nMvav 76,188,60\nCV 150,156,157\nCV 151,158,159\nMvav 72,188,56\n"
"Ra 172,3,11264\ns_add_u32 s5, s94, 131072\nMvav 72,184,48\nSv 148,1,16,5,1024\nCV 144,144,145\nZvav 160,128,96\nRa 176,3,12288\nCV 145,146,147\nMvav 160,132,104\nCV 146,152,153\n"
"CV 147,154,155\nMvav 160,136,112\nRa 180,3,13312\nSv 144,1,16,5,0\nMvav 160,140,120\nWL 6\nCV 68,68,69\nZvav 164,140,124\nRa 184,3,14336\nCV 69,70,71\nMvav 164,136,116\n"
"CV 70,76,77\nCV 71,78,79\nMvav 164,132,108\nRa 188,3,15360\ns_add_u32 s5, s94, 262144\nMvav 164,128,100\nSv 68,1,16,5,1024\nCV 64,64,65\nMvav 164,144,36\nRa 128,3,16384\n"
"CV 65,66,67\nMvav 164,148,44\nCV 66,72,73\nCV 67,74,75\nMvav 164,152,52\nRa 132,3,17408\nSv 64,1,16,5,0\nMvav 164,156,60\nWL 6\nMvav 160,156,56\nRa 136,3,18432\nMvav 160,152,48\n"
"Mvav 160,148,40\nRa 140,3,19456\nMvav 160,144,32\nZvav 168,160,96\nRa 144,3,20480\nMvav 168,164,104\nMvav 168,168,112\nRa 148,3,21504\nMvav 168,172,120\nWL 6\nZvav 172,172,124\n"
"Ra 152,3,22528\nMvav 172,168,116\nMvav 172,164,108\nRa 156,3,23552\nMvav 172,160,100\nMvav 172,176,36\nRa 160,3,24576\nMvav 172,180,44\nMvav 172,184,52\nRa 164,3,25600\n"
"Mvav 172,188,60\nWL 6\nMvav 168,188,56\nRa 168,3,26624\nMvav 168,184,48\nMvav 168,180,40\nRa 172,3,27648\nMvav 168,176,32\nZvav 176,128,96\nRa 176,3,28672\nCV 164,164,165\n"
"Mvav 176,132,104\nCV 165,166,167\nCV 166,172,173\nMvav 176,136,112\nRa 180,3,29696\nCV 167,174,175\nMvav 176,140,120\ns_waitcnt vmcnt(11) lgkmcnt(6)\ns_add_u32 s5, s94, 393216\n"
"Zvav 180,140,124\nRa 184,3,30720\nSv 164,1,16,5,1024\nMvav 180,136,116\nCV 160,160,161\nCV 161,162,163\nMvav 180,132,108\nRa 188,3,31744\ns_barrier\nMvav 180,128,100\n"
"CV 162,168,169\nCV 163,170,171\nMvav 180,144,36\nRa 128,3,32768\nSv 160,1,16,5,0\nMvav 180,148,44\nMvav 180,152,52\nRa 132,3,33792\nMvav 180,156,60\nWL 6\n"
"s_add_u32 m0, s89, 16384\ns_add_u32 s46, s96, 1277952\nMvav 176,156,56\nRa 136,3,34816\nMvav 176,152,48\nD1 1,16,46,0\nMvav 176,148,40\nRa 140,3,35840\nMvav 176,144,32\n"
"Zvav 184,160,96\nRa 144,3,36864\nMvav 184,164,104\nMvav 184,168,112\nRa 148,3,37888\nMvav 184,172,120\nWL 6\nZvav 188,172,124\nRa 152,3,38912\nMvav 188,168,116\nD1 1,16,46,1024\n"
"Mvav 188,164,108\nRa 156,3,39936\nMvav 188,160,100\nMvav 188,176,36\nRa 160,3,40960\nWV 9\nv_cmp_ne_u32 vcc, s15, v27\ns_nop 0\ns_cbranch_vccnz .S247\n.S248:\nMvav 188,180,44\n"
"s_add_u32 s4, s92, 0\nMvav 188,184,52\nRa 164,3,41984\nMvav 188,188,60\nWL 6\nbuffer_load_dwordx4 v[208:211], v1, s[16:19], s4 offen offset:0 sc0 sc1\nMvav 184,188,56\n"
"Ra 168,3,43008\nMvav 184,184,48\nD1 1,16,46,2048\nMvav 184,180,40\nRa 172,3,44032\nMvav 184,176,32\nbuffer_load_dwordx4 v[212:215], v1, s[16:19], s4 offen offset:1024 sc0 sc1\n"
"Zvav 192,128,96\nRa 176,3,45056\nv_lshrrev_b32 v88, 8, v1\nMvav 192,132,104\nv_lshlrev_b32 v89, 5, v88\nCV 180,180,181\nMvav 192,136,112\nRa 180,3,46080\nCV 181,182,183\n"
"Mvav 192,140,120\nWL 6\nCV 182,188,189\nZvav 196,140,124\nRa 184,3,47104\nv_bfe_u32 v90, v1, 4, 4\nMvav 196,136,116\nD1 1,16,46,3072\nCV 183,190,191\nMvav 196,132,108\n"
"Ra 188,3,48128\ns_add_u32 s5, s94, 524288\nMvav 196,128,100\nv_mul_u32_u24 v91, 896, v90\nSv 180,1,16,5,1024\nMvav 196,144,36\nRa 128,3,49152\nv_lshl_add_u32 v91, v88, 4, v91\n"
"Mvav 196,148,44\nv_add_u32 v90, s21, v90\nCV 176,176,177\nMvav 196,152,52\nRa 132,3,50176\nCV 177,178,179\nMvav 196,156,60\nWL 6\ns_add_u32 m0, s89, 0\n"
"s_add_u32 s46, s96, 1310720\nMvav 192,156,56\nRa 136,3,51200\nv_lshlrev_b32 v88, 3, v90\nMvav 192,152,48\nD1 1,16,46,0\nCV 178,184,185\nMvav 192,148,40\nRa 140,3,52224\n"
"CV 179,186,187\nMvav 192,144,32\nv_add_u32 v94, 64, v90\nSv 176,1,16,5,0\nZvav 200,160,96\nRa 144,3,53248\ns_mov_b32 s63, 0xc040000\nMvav 200,164,104\nv_lshrrev_b32 v92, 8, v1\n"
"Mvav 200,168,112\nRa 148,3,54272\nMvav 200,172,120\nWL 6\nZvav 204,172,124\nRa 152,3,55296\nv_bfe_u32 v93, v1, 6, 2\nMvav 204,168,116\nD1 1,16,46,1024\nv_cmp_eq_u32 vcc, v92, v93\n"
"v_and_b32 v92, 16, v1\nv_mov_b32 v93, 0x3f80\nv_cndmask_b32 v93, 0, v93, vcc\nMvav 204,164,108\nRa 156,3,56320\nMvav 204,160,100\nv_lshlrev_b32 v93, v92, v93\nMvav 204,176,36\n"
"Ra 160,3,57344\nv_and_b32 v92, 32, v1\nMvav 204,180,44\ns_add_u32 s4, s92, 131072\nv_cmp_eq_u32 vcc, 0, v92\nv_mov_b32 v4, 0\nv_mov_b32 v5, 0\nv_cndmask_b32 v6, 0, v93, vcc\n"
"Mvav 204,184,52\nRa 164,3,58368\nMvav 204,188,60\nWL 6\nbuffer_load_dwordx4 v[216:219], v1, s[16:19], s4 offen offset:0 sc0 sc1\nMvav 200,188,56\nRa 168,3,59392\n"
"v_sub_u32 v7, v93, v6\nMvav 200,184,48\nD1 1,16,46,2048\nMvav 200,180,40\nRa 172,3,60416\nMvav 200,176,32\n"
"buffer_load_dwordx4 v[220:223], v1, s[16:19], s4 offen offset:1024 sc0 sc1\nZvav 160,128,96\nRa 176,3,61440\nv_mov_b32 v8, 0\nMvav 160,132,104\nv_mov_b32 v9, 0\nCV 196,196,197\n"
"Mvav 160,136,112\nRa 180,3,62464\nCV 197,198,199\nMvav 160,140,120\nWL 6\nCV 198,204,205\nZvav 164,140,124\nRa 184,3,63488\nCV 199,206,207\nMvav 164,136,116\nD1 1,16,46,3072\n"
"s_add_u32 s5, s94, 655360\nMvav 164,132,108\nRa 188,3,64512\ns_barrier\nMvav 164,128,100\nSv 196,1,16,5,1024\nCV 192,192,193\nMvav 164,144,36\nRa 128,0,0\nCV 193,194,195\n"
"Mvav 164,148,44\nCV 194,200,201\nCV 195,202,203\nMvav 164,152,52\nRa 132,0,1024\nSv 192,1,16,5,0\nMvav 164,156,60\nWL 6\ns_add_u32 m0, s89, 32768\ns_add_u32 s46, s96, 1343488\n"
"Mvav 160,156,56\nRa 136,0,2048\nMvav 160,152,48\nD1 1,16,46,0\nMvav 160,148,40\nRa 140,0,3072\nMvav 160,144,32\nZvav 168,160,96\nRa 144,0,4096\nWV 12\nv_max_u32 v21, v211, v215\n"
"v_cmp_eq_u32 vcc, -1, v21\ns_nop 0\ns_cbranch_vccnz .S223\n.S224:\nMvav 168,164,104\nv_lshlrev_b32 v176, 16, v208\nMvav 168,168,112\nRa 148,0,5120\nMvav 168,172,120\nWL 6\n"
"v_and_b32 v177, 0xffff0000, v208\nZvav 172,172,124\nRa 152,0,6144\nMvav 172,168,116\nD1 1,16,46,1024\nMvav 172,164,108\nRa 156,0,7168\nv_lshlrev_b32 v178, 16, v209\n"
"Mvav 172,160,100\nv_and_b32 v179, 0xffff0000, v209\nMvav 172,176,36\nRa 160,0,8192\nv_lshlrev_b32 v180, 16, v212\nMvav 172,180,44\nv_and_b32 v181, 0xffff0000, v212\n"
"s_add_u32 s4, s92, 262144\nMvav 172,184,52\nRa 164,0,9216\nMvav 172,188,60\nWL 6\nv_lshlrev_b32 v182, 16, v213\n"
"buffer_load_dwordx4 v[224:227], v1, s[16:19], s4 offen offset:0 sc0 sc1\nMvav 168,188,56\nRa 168,0,10240\nMvav 168,184,48\nD1 1,16,46,2048\nMvav 168,180,40\nRa 172,0,11264\n"
"v_and_b32 v183, 0xffff0000, v213\nMvav 168,176,32\nv_lshlrev_b32 v184, 16, v210\nbuffer_load_dwordx4 v[228:231], v1, s[16:19], s4 offen offset:1024 sc0 sc1\nMvav 176,128,96\n"
"Ra 176,0,12288\nv_and_b32 v185, 0xffff0000, v210\nMvav 176,132,104\nv_lshlrev_b32 v186, 16, v211\nCV 164,164,165\nMvav 176,136,112\nRa 180,0,13312\nCV 165,166,167\n"
"Mvav 176,140,120\nWL 6\nCV 166,172,173\nMvav 180,140,124\nRa 184,0,14336\nv_and_b32 v187, 0xffff0000, v211\nMvav 180,136,116\nD1 1,16,46,3072\nCV 167,174,175\nMvav 180,132,108\n"
"Ra 188,0,15360\ns_add_u32 s5, s94, 786432\nMvav 180,128,100\nv_lshlrev_b32 v188, 16, v214\nSv 164,1,16,5,1024\nMvav 180,144,36\nRa 128,0,16384\nv_and_b32 v189, 0xffff0000, v214\n"
"Mvav 180,148,44\nv_lshlrev_b32 v190, 16, v215\nCV 160,160,161\nMvav 180,152,52\nRa 132,0,17408\nCV 161,162,163\nMvav 180,156,60\nWL 6\ns_add_u32 m0, s89, 49152\n"
"s_add_u32 s46, s96, 1376256\nMvav 176,156,56\nRa 136,0,18432\nv_and_b32 v191, 0xffff0000, v215\nMvav 176,152,48\nD1 1,16,46,0\nCV 162,168,169\nMvav 176,148,40\nRa 140,0,19456\n"
"CV 163,170,171\nMvav 176,144,32\nSv 160,1,16,5,0\nMvav 184,160,96\nRa 144,0,20480\nWV 12\nv_max_u32 v21, v219, v223\nv_cmp_eq_u32 vcc, -1, v21\ns_nop 0\ns_cbranch_vccnz .S226\n"
".S227:\nMvav 184,164,104\nv_lshlrev_b32 v192, 16, v216\nMvav 184,168,112\nRa 148,0,21504\nMvav 184,172,120\nWL 6\nv_and_b32 v193, 0xffff0000, v216\nMvav 188,172,124\n"
"Ra 152,0,22528\nMvav 188,168,116\nD1 1,16,46,1024\nMvav 188,164,108\nRa 156,0,23552\nv_lshlrev_b32 v194, 16, v217\nMvav 188,160,100\nv_and_b32 v195, 0xffff0000, v217\n"
"Mvav 188,176,36\nRa 160,0,24576\nv_lshlrev_b32 v196, 16, v220\nMvav 188,180,44\nv_and_b32 v197, 0xffff0000, v220\ns_add_u32 s4, s92, 393216\nMvav 188,184,52\nRa 164,0,25600\n"
"Mvav 188,188,60\nWL 6\nv_lshlrev_b32 v198, 16, v221\nbuffer_load_dwordx4 v[208:211], v1, s[16:19], s4 offen offset:0 sc0 sc1\nMvav 184,188,56\nRa 168,0,26624\nMvav 184,184,48\n"
"D1 1,16,46,2048\nMvav 184,180,40\nRa 172,0,27648\nv_and_b32 v199, 0xffff0000, v221\nMvav 184,176,32\nv_lshlrev_b32 v200, 16, v218\n"
"buffer_load_dwordx4 v[212:215], v1, s[16:19], s4 offen offset:1024 sc0 sc1\nMvav 192,128,96\nRa 176,0,28672\nv_and_b32 v201, 0xffff0000, v218\nMvav 192,132,104\n"
"v_lshlrev_b32 v202, 16, v219\nCV 180,180,181\nMvav 192,136,112\nRa 180,0,29696\nCV 181,182,183\nMvav 192,140,120\ns_waitcnt vmcnt(15) lgkmcnt(6)\nCV 182,188,189\nMvav 196,140,124\n"
"Ra 184,0,30720\nv_and_b32 v203, 0xffff0000, v219\nMvav 196,136,116\nD1 1,16,46,3072\nCV 183,190,191\nMvav 196,132,108\nRa 188,0,31744\ns_barrier\nMvav 196,128,100\n"
"v_lshlrev_b32 v204, 16, v222\ns_add_u32 s5, s94, 917504\nMvav 196,144,36\nRa 128,2,16384\nv_and_b32 v205, 0xffff0000, v222\nMvav 196,148,44\nv_lshlrev_b32 v206, 16, v223\n"
"Sv 180,1,16,5,1024\nMvav 196,152,52\nRa 132,2,17408\nCV 176,176,177\nMvav 196,156,60\nWL 6\ns_add_u32 m0, s89, 65536\ns_add_u32 s46, s96, 1409024\nMvav 192,156,56\nRa 136,2,18432\n"
"v_and_b32 v207, 0xffff0000, v223\nMvav 192,152,48\nD1 1,16,46,0\n.W569:\ns_cbranch_execz .WB152\nCV 177,178,179\nMvav 192,148,40\nRa 140,2,19456\nCV 178,184,185\nMvav 192,144,32\n"
"CV 179,186,187\nSv 176,1,16,5,0\nMvav 200,160,96\nRa 144,2,20480\nWV 12\nv_max_u32 v21, v227, v231\nv_cmp_eq_u32 vcc, -1, v21\ns_nop 0\ns_cbranch_vccnz .S229\n.S230:\n"
"Mvav 200,164,104\nv_lshlrev_b32 v160, 16, v224\nMvav 200,168,112\nRa 148,2,21504\nMvav 200,172,120\nWL 6\nv_and_b32 v161, 0xffff0000, v224\nMvav 204,172,124\nRa 152,2,22528\n"
"Mvav 204,168,116\nD1 1,16,46,1024\nMvav 204,164,108\nRa 156,2,23552\nv_lshlrev_b32 v162, 16, v225\nMvav 204,160,100\nv_and_b32 v163, 0xffff0000, v225\nMvav 204,176,36\n"
"Ra 160,2,24576\nv_lshlrev_b32 v164, 16, v228\nMvav 204,180,44\nv_and_b32 v165, 0xffff0000, v228\ns_add_u32 s4, s92, 524288\nMvav 204,184,52\nRa 164,2,25600\nMvav 204,188,60\nWL 6\n"
"v_lshlrev_b32 v166, 16, v229\nbuffer_load_dwordx4 v[216:219], v1, s[16:19], s4 offen offset:0 sc0 sc1\nMvav 200,188,56\nRa 168,2,26624\nMvav 200,184,48\nD1 1,16,46,2048\n"
"Mvav 200,180,40\nRa 172,2,27648\nv_and_b32 v167, 0xffff0000, v229\nMvav 200,176,32\nv_lshlrev_b32 v168, 16, v226\n"
"buffer_load_dwordx4 v[220:223], v1, s[16:19], s4 offen offset:1024 sc0 sc1\nMvav 160,128,96\nRa 176,2,28672\nv_and_b32 v169, 0xffff0000, v226\nMvav 160,132,104\n"
"v_lshlrev_b32 v170, 16, v227\nCV 196,196,197\nMvav 160,136,112\nRa 180,2,29696\nCV 197,198,199\nMvav 160,140,120\nWL 6\nCV 198,204,205\nMvav 164,140,124\nRa 184,2,30720\n"
"v_and_b32 v24, 0x70, v1\nMvav 164,136,116\nD1 1,16,46,3072\nCV 199,206,207\nMvav 164,132,108\nRa 188,2,31744\nv_and_b32 v171, 0xffff0000, v227\nMvav 164,128,100\n"
"v_lshlrev_b32 v172, 16, v230\ns_add_u32 s5, s94, 1048576\nMvav 164,144,36\nRa 128,2,0\nv_lshlrev_b32 v24, 5, v24\nMvav 164,148,44\nv_and_b32 v173, 0xffff0000, v230\n"
"Sv 196,1,16,5,1024\nMvav 164,152,52\nRa 132,2,1024\nCV 192,192,193\nMvav 164,156,60\nWL 6\nv_lshlrev_b32 v174, 16, v231\nMvav 160,156,56\nRa 136,2,2048\n"
"buffer_load_dword v27, v24, s[16:19], s41 offen sc0 sc1\nMvav 160,152,48\nv_and_b32 v175, 0xffff0000, v231\nCV 193,194,195\nMvav 160,148,40\nRa 140,2,3072\nCV 194,200,201\n"
"Mvav 160,144,32\nCV 195,202,203\nSv 192,1,16,5,0\nMvav 168,160,96\nRa 144,2,4096\nWV 12\nv_max_u32 v21, v211, v215\nv_cmp_eq_u32 vcc, -1, v21\ns_nop 0\ns_cbranch_vccnz .S232\n"
".S233:\nMvav 168,164,104\nv_lshlrev_b32 v176, 16, v208\nMvav 168,168,112\nRa 148,2,5120\nMvav 168,172,120\nWL 6\nMvav 172,172,124\nRa 152,2,6144\nv_and_b32 v177, 0xffff0000, v208\n"
"Mvav 172,168,116\nv_lshlrev_b32 v178, 16, v209\nMvav 172,164,108\nRa 156,2,7168\nMvav 172,160,100\nv_and_b32 v179, 0xffff0000, v209\nMvav 172,176,36\nRa 160,2,8192\n"
"v_lshlrev_b32 v180, 16, v212\nMvav 172,180,44\nv_and_b32 v181, 0xffff0000, v212\ns_add_u32 s4, s92, 655360\nMvav 172,184,52\nRa 164,2,9216\nMvav 172,188,60\nWL 6\n"
"buffer_load_dwordx4 v[224:227], v1, s[16:19], s4 offen offset:0 sc0 sc1\nMvav 168,188,56\nRa 168,2,10240\nv_lshlrev_b32 v182, 16, v213\nMvav 168,184,48\n"
"v_and_b32 v183, 0xffff0000, v213\nMvav 168,180,40\nRa 172,2,11264\nMvav 168,176,32\nv_lshlrev_b32 v184, 16, v210\n"
"buffer_load_dwordx4 v[228:231], v1, s[16:19], s4 offen offset:1024 sc0 sc1\nMvav 176,128,96\nRa 176,2,12288\nv_and_b32 v185, 0xffff0000, v210\nMvav 176,132,104\n"
"v_lshlrev_b32 v186, 16, v211\nCV 164,164,165\nMvav 176,136,112\nRa 180,2,13312\nCV 165,166,167\nMvav 176,140,120\ns_waitcnt vmcnt(13) lgkmcnt(6)\nCV 166,172,173\nMvav 180,140,124\n"
"Ra 184,2,14336\nCV 167,174,175\nMvav 180,136,116\nv_and_b32 v187, 0xffff0000, v211\ns_add_u32 s5, s94, 1179648\nMvav 180,132,108\nRa 188,2,15360\ns_barrier\nMvav 180,128,100\n"
"v_lshlrev_b32 v188, 16, v214\nSv 164,1,16,5,1024\nMvav 180,144,36\nRa 128,2,32768\nv_and_b32 v189, 0xffff0000, v214\nMvav 180,148,44\nv_lshlrev_b32 v190, 16, v215\nCV 160,160,161\n"
"Mvav 180,152,52\nRa 132,2,33792\nCV 161,162,163\nMvav 180,156,60\nWL 6\nCV 162,168,169\nMvav 176,156,56\nRa 136,2,34816\nCV 163,170,171\nMvav 176,152,48\n"
"v_and_b32 v191, 0xffff0000, v215\nSv 160,1,16,5,0\nMvav 176,148,40\nRa 140,2,35840\nMvav 176,144,32\nMvav 184,160,96\nRa 144,2,36864\nWV 8\nv_max_u32 v21, v219, v223\n"
"v_cmp_eq_u32 vcc, -1, v21\ns_nop 0\ns_cbranch_vccnz .S235\n.S236:\nMvav 184,164,104\nv_lshlrev_b32 v192, 16, v216\nMvav 184,168,112\nRa 148,2,37888\nMvav 184,172,120\nWL 6\n"
"Mvav 188,172,124\nRa 152,2,38912\nv_and_b32 v193, 0xffff0000, v216\nMvav 188,168,116\nv_lshlrev_b32 v194, 16, v217\nMvav 188,164,108\nRa 156,2,39936\nMvav 188,160,100\n"
"v_and_b32 v195, 0xffff0000, v217\nMvav 188,176,36\nRa 160,2,40960\nv_lshlrev_b32 v196, 16, v220\nMvav 188,180,44\nv_and_b32 v197, 0xffff0000, v220\ns_add_u32 s4, s92, 786432\n"
"Mvav 188,184,52\nRa 164,2,41984\nMvav 188,188,60\nWL 6\nbuffer_load_dwordx4 v[208:211], v1, s[16:19], s4 offen offset:0 sc0 sc1\nMvav 184,188,56\nRa 168,2,43008\n"
"v_lshlrev_b32 v198, 16, v221\nMvav 184,184,48\nv_and_b32 v199, 0xffff0000, v221\nMvav 184,180,40\nRa 172,2,44032\nMvav 184,176,32\nv_lshlrev_b32 v200, 16, v218\n"
"buffer_load_dwordx4 v[212:215], v1, s[16:19], s4 offen offset:1024 sc0 sc1\nMvav 192,128,96\nRa 176,2,45056\nv_and_b32 v201, 0xffff0000, v218\nMvav 192,132,104\n"
"v_lshlrev_b32 v202, 16, v219\nCV 180,180,181\nMvav 192,136,112\nRa 180,2,46080\nCV 181,182,183\nMvav 192,140,120\nWL 6\nCV 182,188,189\nMvav 196,140,124\nRa 184,2,47104\n"
"CV 183,190,191\nMvav 196,136,116\nv_and_b32 v203, 0xffff0000, v219\ns_add_u32 s5, s94, 1310720\nMvav 196,132,108\nRa 188,2,48128\nSv 180,1,16,5,1024\nMvav 196,128,100\n"
"v_lshlrev_b32 v204, 16, v222\nCV 176,176,177\nMvav 196,144,36\nRa 128,2,49152\nv_and_b32 v205, 0xffff0000, v222\nMvav 196,148,44\nv_lshlrev_b32 v206, 16, v223\nCV 177,178,179\n"
"Mvav 196,152,52\nRa 132,2,50176\nCV 178,184,185\nMvav 196,156,60\nWL 6\nCV 179,186,187\nMvav 192,156,56\nRa 136,2,51200\nSv 176,1,16,5,0\nMvav 192,152,48\n"
"v_and_b32 v207, 0xffff0000, v223\nMvav 192,148,40\nRa 140,2,52224\nMvav 192,144,32\nMvav 200,160,96\nRa 144,2,53248\nWV 6\nv_max_u32 v21, v227, v231\nv_cmp_eq_u32 vcc, -1, v21\n"
"s_nop 0\ns_cbranch_vccnz .S238\n.S239:\nMvav 200,164,104\nv_lshlrev_b32 v160, 16, v224\nv_xor_b32 v27, s15, v27\nv_and_b32 v26, 0xffffdfff, v27\nv_cmp_ne_u32 s[64:65], 0, v27\n"
"v_cmp_ne_u32 vcc, 0, v26\ns_nop 0\ns_cbranch_vccnz .S250\n.S251:\nMvav 200,168,112\nRa 148,2,54272\ns_bitcmp1_b32 s64, 0\ns_cselect_b32 s2, 0x2000, 0\nMvav 200,172,120\nWL 6\n"
"Mvav 204,172,124\nRa 152,2,55296\nv_and_b32 v161, 0xffff0000, v224\nMvav 204,168,116\nv_lshlrev_b32 v162, 16, v225\nMvav 204,164,108\nRa 156,2,56320\ns_xor_b32 s2, s2, s20\n"
"Mvav 204,160,100\nv_and_b32 v163, 0xffff0000, v225\nMvav 204,176,36\nRa 160,2,57344\nv_lshlrev_b32 v164, 16, v228\nMvav 204,180,44\nv_and_b32 v165, 0xffff0000, v228\n"
"Mvav 204,184,52\nRa 164,2,58368\nMvav 204,188,60\nWL 6\ns_add_u32 s24, s2, 0x0\nMvav 200,188,56\nRa 168,2,59392\nv_lshlrev_b32 v166, 16, v229\nMvav 200,184,48\n"
"v_and_b32 v167, 0xffff0000, v229\ns_bitcmp1_b32 s64, 1\ns_cselect_b32 s2, 0x2000, 0\nMvav 200,180,40\nRa 172,2,60416\nMvav 200,176,32\nv_lshlrev_b32 v168, 16, v226\n"
"Mvav 160,128,96\nRa 176,2,61440\nv_and_b32 v169, 0xffff0000, v226\nMvav 160,132,104\nv_lshlrev_b32 v170, 16, v227\nCV 196,196,197\nMvav 160,136,112\nRa 180,2,62464\n"
"CV 197,198,199\nMvav 160,140,120\nWL 6\nCV 198,204,205\nMvav 164,140,124\nRa 184,2,63488\nCV 199,206,207\nMvav 164,136,116\nv_and_b32 v171, 0xffff0000, v227\n"
"s_add_u32 s5, s94, 1441792\nMvav 164,132,108\nRa 188,2,64512\ns_barrier\nMvav 164,128,100\nv_lshlrev_b32 v172, 16, v230\nSv 196,1,16,5,1024\nMvav 164,144,36\nRa 128,3,0\n"
"v_and_b32 v173, 0xffff0000, v230\nMvav 164,148,44\nv_lshlrev_b32 v174, 16, v231\nCV 192,192,193\nMvav 164,152,52\nRa 132,3,1024\nCV 193,194,195\nMvav 164,156,60\nWL 6\n"
"s_xor_b32 s2, s2, s20\nMvav 160,156,56\nRa 136,3,2048\nCV 194,200,201\nMvav 160,152,48\nv_and_b32 v175, 0xffff0000, v231\nCV 195,202,203\nMvav 160,148,40\nRa 140,3,3072\n"
"Sv 192,1,16,5,0\nMvav 160,144,32\ns_add_u32 s25, s2, 0x1800000\ns_bitcmp1_b32 s64, 2\ns_cselect_b32 s2, 0x2000, 0\nMvav 168,160,96\nRa 144,3,4096\nWV 4\nv_max_u32 v21, v211, v215\n"
"v_cmp_eq_u32 vcc, -1, v21\ns_nop 0\ns_cbranch_vccnz .S241\n.S242:\nMvav 168,164,104\nv_lshlrev_b32 v176, 16, v208\nMvav 168,168,112\nRa 148,3,5120\nMvav 168,172,120\nWL 6\n"
"Mvav 172,172,124\nRa 152,3,6144\nv_and_b32 v177, 0xffff0000, v208\nMvav 172,168,116\nv_lshlrev_b32 v178, 16, v209\nMvav 172,164,108\nRa 156,3,7168\ns_xor_b32 s2, s2, s20\n"
"Mvav 172,160,100\nv_and_b32 v179, 0xffff0000, v209\nMvav 172,176,36\nRa 160,3,8192\nv_lshlrev_b32 v180, 16, v212\nMvav 172,180,44\nv_and_b32 v181, 0xffff0000, v212\n"
"Mvav 172,184,52\nRa 164,3,9216\nMvav 172,188,60\nWL 6\ns_add_u32 s26, s2, 0x3000000\nMvav 168,188,56\nRa 168,3,10240\nv_lshlrev_b32 v182, 16, v213\nMvav 168,184,48\n"
"v_and_b32 v183, 0xffff0000, v213\ns_bitcmp1_b32 s64, 3\ns_cselect_b32 s2, 0x2000, 0\nMvav 168,180,40\nRa 172,3,11264\nMvav 168,176,32\nv_lshlrev_b32 v184, 16, v210\n"
"Mvav 176,128,96\nRa 176,3,12288\ns_xor_b32 s2, s2, s20\nMvav 176,132,104\nv_and_b32 v185, 0xffff0000, v210\nCV 164,164,165\nMvav 176,136,112\nRa 180,3,13312\nCV 165,166,167\n"
"Mvav 176,140,120\nWL 6\nCV 166,172,173\nMvav 180,140,124\nRa 184,3,14336\nCV 167,174,175\nMvav 180,136,116\nv_lshlrev_b32 v186, 16, v211\ns_add_u32 s5, s94, 1572864\n"
"Mvav 180,132,108\nRa 188,3,15360\ns_add_u32 s27, s2, 0x4800000\nMvav 180,128,100\nv_and_b32 v187, 0xffff0000, v211\ns_bitcmp1_b32 s64, 4\ns_cselect_b32 s2, 0x2000, 0\n"
"Mvav 180,144,36\nv_lshlrev_b32 v188, 16, v214\nSv 164,1,16,5,1024\nMvav 180,148,44\nv_and_b32 v189, 0xffff0000, v214\nCV 160,160,161\nMvav 180,152,52\n"
"v_lshlrev_b32 v190, 16, v215\nCV 161,162,163\nMvav 180,156,60\nWL 4\ns_xor_b32 s2, s2, s20\nMvav 176,156,56\nv_and_b32 v191, 0xffff0000, v215\nCV 162,168,169\nMvav 176,152,48\n"
"s_add_u32 s32, s2, 0x6000000\ns_bitcmp1_b32 s64, 5\ns_cselect_b32 s2, 0x2000, 0\nMvav 176,148,40\ns_xor_b32 s2, s2, s20\nCV 163,170,171\nMvav 176,144,32\n"
"s_add_u32 s33, s2, 0x7800000\ns_bitcmp1_b32 s64, 6\ns_cselect_b32 s2, 0x2000, 0\nMvav 184,160,96\ns_xor_b32 s2, s2, s20\ns_add_u32 s34, s2, 0x9000000\nMvav 184,164,104\n"
"s_bitcmp1_b32 s64, 7\ns_cselect_b32 s2, 0x2000, 0\nMvav 184,168,112\ns_xor_b32 s2, s2, s20\nSv 160,1,16,5,0\nMvav 184,172,120\nWL 0\nMvav 188,172,124\n"
"s_add_u32 s35, s2, 0xa800000\nMvav 188,168,116\nMvav 188,164,108\nMvav 188,160,100\nMvav 188,176,36\nMvav 188,180,44\nMvav 188,184,52\nMvav 188,188,60\nMvav 184,188,56\n"
"Mvav 184,184,48\nMvav 184,180,40\nMvav 184,176,32\nCV 180,180,181\nCV 181,182,183\ns_nop 1\nCV 182,188,189\nCV 183,190,191\ns_add_u32 s5, s94, 1703936\nSv 180,1,16,5,1024\n"
"CV 176,176,177\nCV 177,178,179\nCV 178,184,185\nCV 179,186,187\nSv 176,1,16,5,0\ns_cmp_eq_u32 s13, 0\ns_cbranch_scc1 .Z253\n"
"buffer_load_dwordx4 v[12:15], v91, s[28:31], s22 offen offset:0\nbuffer_load_dwordx4 v[16:19], v91, s[28:31], s23 offen offset:0\n"
"buffer_load_dwordx4 v[64:67], v89, s[16:19], s40 offen offset:0 sc0 sc1\nbuffer_load_dwordx4 v[68:71], v89, s[16:19], s40 offen offset:16 sc0 sc1\n"
"buffer_load_dwordx4 v[72:75], v89, s[16:19], s40 offen offset:1792 sc0 sc1\nbuffer_load_dwordx4 v[76:79], v89, s[16:19], s40 offen offset:1808 sc0 sc1\n"
"buffer_load_dwordx2 v[80:81], v88, s[16:19], s63 offen offset:0 sc0 sc1\nbuffer_load_dwordx2 v[82:83], v88, s[16:19], s63 offen offset:512 sc0 sc1\n"
"buffer_load_dwordx4 v[96:99], v1, s[16:19], s24 offen offset:0 sc0 sc1\nbuffer_load_dwordx4 v[100:103], v1, s[16:19], s24 offen offset:1024 sc0 sc1\n"
"buffer_load_dwordx4 v[104:107], v1, s[16:19], s25 offen offset:0 sc0 sc1\nbuffer_load_dwordx4 v[108:111], v1, s[16:19], s25 offen offset:1024 sc0 sc1\n"
"buffer_load_dwordx4 v[112:115], v1, s[16:19], s26 offen offset:0 sc0 sc1\nbuffer_load_dwordx4 v[116:119], v1, s[16:19], s26 offen offset:1024 sc0 sc1\n"
"buffer_load_dwordx4 v[120:123], v1, s[16:19], s27 offen offset:0 sc0 sc1\nbuffer_load_dwordx4 v[124:127], v1, s[16:19], s27 offen offset:1024 sc0 sc1\n"
"buffer_load_dwordx4 v[32:35], v1, s[16:19], s32 offen offset:0 sc0 sc1\nbuffer_load_dwordx4 v[36:39], v1, s[16:19], s32 offen offset:1024 sc0 sc1\n"
"buffer_load_dwordx4 v[40:43], v1, s[16:19], s33 offen offset:0 sc0 sc1\nbuffer_load_dwordx4 v[44:47], v1, s[16:19], s33 offen offset:1024 sc0 sc1\n"
"buffer_load_dwordx4 v[48:51], v1, s[16:19], s34 offen offset:0 sc0 sc1\nbuffer_load_dwordx4 v[52:55], v1, s[16:19], s34 offen offset:1024 sc0 sc1\n"
"buffer_load_dwordx4 v[56:59], v1, s[16:19], s35 offen offset:0 sc0 sc1\nbuffer_load_dwordx4 v[60:63], v1, s[16:19], s35 offen offset:1024 sc0 sc1\nWV 16\nv_xor_b32 v66, s15, v66\n"
"v_xor_b32 v70, s15, v70\nv_xor_b32 v74, s15, v74\nv_xor_b32 v78, s15, v78\nv_xor_b32 v81, s15, v81\nv_xor_b32 v83, s15, v83\nv_or3_b32 v92, v66, v70, v74\n"
"v_or3_b32 v92, v92, v78, v81\nv_or_b32 v92, v92, v83\nv_cmp_ne_u32 vcc, 0, v92\ns_nop 0\ns_cbranch_vccnz .S244\n.S245:\nv_and_b32 v92, 0x7ff, v67\nv_lshrrev_b32 v93, 16, v67\n"
"v_cmp_eq_u32 s[44:45], v92, v90\nv_cmp_eq_u32 s[46:47], v93, v90\nv_cmp_eq_u32 s[48:49], v92, v94\nv_cmp_eq_u32 s[50:51], v93, v94\nv_cndmask_b32 v12, v12, v64, s[44:45]\n"
"v_cndmask_b32 v13, v13, v65, s[44:45]\nv_cndmask_b32 v14, v14, v68, s[44:45]\nv_cndmask_b32 v15, v15, v69, s[44:45]\nv_cndmask_b32 v12, v12, v72, s[46:47]\n"
"v_cndmask_b32 v13, v13, v73, s[46:47]\nv_cndmask_b32 v14, v14, v76, s[46:47]\nv_cndmask_b32 v15, v15, v77, s[46:47]\nv_cndmask_b32 v16, v16, v64, s[48:49]\n"
"v_cndmask_b32 v17, v17, v65, s[48:49]\nv_cndmask_b32 v18, v18, v68, s[48:49]\nv_cndmask_b32 v19, v19, v69, s[48:49]\nv_cndmask_b32 v16, v16, v72, s[50:51]\n"
"v_cndmask_b32 v17, v17, v73, s[50:51]\nv_cndmask_b32 v18, v18, v76, s[50:51]\nv_cndmask_b32 v19, v19, v77, s[50:51]\nv_sub_f32 v92, 1.0, v80\nv_sub_f32 v93, 1.0, v82\nCV 92,92,93\n"
"v_and_b32 v93, 0xffff0000, v92\nv_lshlrev_b32 v92, 16, v92\nv_lshlrev_b32 v64, 16, v12\nv_and_b32 v65, 0xffff0000, v12\nMU 64,64,92\nMU 65,65,92\n.W570:\ns_cbranch_execz .WB152\n"
"v_lshlrev_b32 v66, 16, v13\nv_and_b32 v67, 0xffff0000, v13\nMU 66,66,92\nMU 67,67,92\nv_lshlrev_b32 v68, 16, v14\nv_and_b32 v69, 0xffff0000, v14\nMU 68,68,92\nMU 69,69,92\n"
"v_lshlrev_b32 v70, 16, v15\nv_and_b32 v71, 0xffff0000, v15\nMU 70,70,92\nMU 71,71,92\nv_lshlrev_b32 v72, 16, v16\nv_and_b32 v73, 0xffff0000, v16\nMU 72,72,93\nMU 73,73,93\n"
"v_lshlrev_b32 v74, 16, v17\nv_and_b32 v75, 0xffff0000, v17\nMU 74,74,93\nMU 75,75,93\nv_lshlrev_b32 v76, 16, v18\nv_and_b32 v77, 0xffff0000, v18\nMU 76,76,93\nMU 77,77,93\n"
"v_lshlrev_b32 v78, 16, v19\nv_and_b32 v79, 0xffff0000, v19\nMU 78,78,93\nMU 79,79,93\ns_mov_b32 s7, 262144\nWV 0\n.Z256:\nv_max_u32 v84, v99, v103\nv_cmp_eq_u32 s[44:45], -1, v84\n"
"v_max_u32 v84, v107, v111\nv_cmp_eq_u32 s[46:47], -1, v84\nv_max_u32 v84, v115, v119\nv_cmp_eq_u32 s[48:49], -1, v84\nv_max_u32 v84, v123, v127\nv_cmp_eq_u32 s[50:51], -1, v84\n"
"v_max_u32 v84, v35, v39\nv_cmp_eq_u32 s[52:53], -1, v84\nv_max_u32 v84, v43, v47\nv_cmp_eq_u32 s[54:55], -1, v84\nv_max_u32 v84, v51, v55\nv_cmp_eq_u32 s[56:57], -1, v84\n"
"v_max_u32 v84, v59, v63\nv_cmp_eq_u32 s[58:59], -1, v84\ns_or_b64 s[0:1], s[44:45], s[46:47]\ns_or_b64 s[2:3], s[48:49], s[50:51]\ns_or_b64 s[0:1], s[0:1], s[52:53]\n"
"s_or_b64 s[2:3], s[2:3], s[54:55]\ns_or_b64 s[0:1], s[0:1], s[56:57]\ns_or_b64 s[2:3], s[2:3], s[58:59]\ns_or_b64 s[0:1], s[0:1], s[2:3]\ns_cbranch_scc1 .Z254\n.Z255:\n"
"Zvvv 136,6,96\nMvvv 136,6,104\nMvvv 136,6,112\nMvvv 136,6,120\nMvvv 136,6,32\nMvvv 136,6,40\nMvvv 136,6,48\nMvvv 136,6,56\nZvvv 140,4,96\nMvvv 140,4,104\nMvvv 140,4,112\n"
"Mvvv 140,4,120\nMvvv 140,4,32\nMvvv 140,4,40\nMvvv 140,4,48\nMvvv 140,4,56\nZvvv 144,6,100\nMvvv 144,6,108\nMvvv 144,6,116\nMvvv 144,6,124\nMvvv 144,6,36\nMvvv 144,6,44\n"
"Mvvv 144,6,52\nMvvv 144,6,60\nZvvv 148,4,100\nMvvv 148,4,108\nMvvv 148,4,116\nMvvv 148,4,124\nMvvv 148,4,36\nMvvv 148,4,44\nMvvv 148,4,52\nMvvv 148,4,60\nFM 64,80,136\n"
"FM 65,80,137\nFM 66,80,138\nFM 67,80,139\nFM 68,80,140\nFM 69,80,141\nFM 70,80,142\nFM 71,80,143\nCV 12,64,65\nCV 13,66,67\nCV 14,68,69\nCV 15,70,71\nSv 12,91,36,22,0\n"
"FM 72,82,144\nFM 73,82,145\nFM 74,82,146\nFM 75,82,147\nFM 76,82,148\nFM 77,82,149\nFM 78,82,150\nFM 79,82,151\nCV 16,72,73\nCV 17,74,75\nCV 18,76,77\nCV 19,78,79\n"
"Sv 16,91,36,23,0\n.Z253:\ns_endpgm\n.EP265:\n.S145:\ns_mov_b32 s7, 262144\n.S147:\nbuffer_load_dwordx4 v[32:35], v1, s[16:19], s6 offen offset:0 sc0 sc1\n"
"buffer_load_dwordx4 v[36:39], v1, s[16:19], s6 offen offset:1024 sc0 sc1\nbuffer_load_dwordx4 v[40:43], v1, s[16:19], s6 offen offset:2048 sc0 sc1\n"
"buffer_load_dwordx4 v[44:47], v1, s[16:19], s6 offen offset:3072 sc0 sc1\nWV 0\nv_xor_b32 v48, s15, v33\nv_xor_b32 v49, s15, v35\nv_xor_b32 v50, s15, v37\nv_xor_b32 v51, s15, v39\n"
"v_xor_b32 v52, s15, v41\nv_xor_b32 v53, s15, v43\nv_xor_b32 v54, s15, v45\nv_xor_b32 v55, s15, v47\nv_or3_b32 v48, v48, v49, v50\nv_or3_b32 v51, v51, v52, v53\n"
"v_or3_b32 v48, v48, v54, v55\nv_or_b32 v48, v48, v51\nv_cmp_ne_u32 vcc, 0, v48\ns_nop 0\ns_cbranch_vccz .S146\ns_sleep 6\ns_sub_u32 s7, s7, 1\ns_cbranch_scc0 .S147\n"
"s_branch .S146\n.S148:\ns_mov_b32 s7, 262144\n.S150:\ns_lshl_b32 s5, s73, 8\ns_add_u32 s5, s5, 0xc04ac00\nv_lshrrev_b32 v27, 2, v1\n"
"buffer_load_dword v27, v27, s[16:19], s5 offen sc0 sc1\nWV 0\nv_xor_b32 v27, s15, v27\nv_and_b32 v255, 0xffffdfff, v27\nv_cmp_ne_u32 vcc, 0, v255\ns_nop 0\ns_cbranch_vccz .S149\n"
"s_sleep 6\ns_sub_u32 s7, s7, 1\ns_cbranch_scc0 .S150\ns_branch .S149\n.T154:\ns_mov_b32 s62, 1\ns_call_b64 s[80:81], .RA10\ns_branch .T155\n.T156:\ns_mov_b32 s62, 1\n"
"s_call_b64 s[80:81], .RA10\ns_branch .T157\n.T159:\ns_mov_b32 s62, 0\ns_call_b64 s[80:81], .RB11\ns_branch .T160\n.T161:\ns_mov_b32 s62, 0\ns_call_b64 s[80:81], .RA10\n"
"s_branch .T162\n.T164:\ns_mov_b32 s62, 0\ns_call_b64 s[80:81], .RB11\ns_branch .T165\n.T166:\ns_mov_b32 s62, 0\ns_call_b64 s[80:81], .RA10\ns_branch .T167\n.S169:\n"
"s_mov_b32 s7, 262144\n.S171:\nv_and_b32 v27, 0x70, v1\nbuffer_load_dword v27, v27, s[16:19], s61 offen offset:128 sc0 sc1\nWV 0\nv_cmp_ne_u32 vcc, s15, v27\ns_nop 0\n"
"s_cbranch_vccz .S170\ns_sleep 6\ns_sub_u32 s7, s7, 1\ns_cbranch_scc0 .S171\ns_branch .S170\n.S172:\ns_mov_b32 s7, 262144\n.S174:\nv_and_b32 v27, 0x70, v1\n"
"buffer_load_dword v27, v27, s[16:19], s61 offen offset:136 sc0 sc1\nWV 0\nv_cmp_ne_u32 vcc, s15, v27\ns_nop 0\ns_cbranch_vccz .S173\ns_sleep 6\ns_sub_u32 s7, s7, 1\n"
"s_cbranch_scc0 .S174\ns_branch .S173\n.S175:\ns_mov_b32 s7, 262144\n.S177:\nv_and_b32 v27, 0x70, v1\nbuffer_load_dword v27, v27, s[16:19], s61 offen offset:140 sc0 sc1\nWV 0\n"
"v_cmp_ne_u32 vcc, s15, v27\ns_nop 0\ns_cbranch_vccz .S176\ns_sleep 6\ns_sub_u32 s7, s7, 1\ns_cbranch_scc0 .S177\ns_branch .S176\n.S178:\ns_mov_b32 s7, 262144\n.S180:\n"
"v_and_b32 v27, 0x70, v1\nbuffer_load_dword v27, v27, s[16:19], s61 offen offset:12 sc0 sc1\nWV 0\nv_cmp_ne_u32 vcc, s15, v27\ns_nop 0\ns_cbranch_vccz .S179\ns_sleep 6\n"
"s_sub_u32 s7, s7, 1\ns_cbranch_scc0 .S180\ns_branch .S179\n.S181:\ns_mov_b32 s7, 262144\n.S183:\nv_and_b32 v27, 0x70, v1\n"
"buffer_load_dword v27, v27, s[16:19], s61 offen offset:8 sc0 sc1\nWV 0\nv_cmp_ne_u32 vcc, s15, v27\ns_nop 0\ns_cbranch_vccz .S182\ns_sleep 6\ns_sub_u32 s7, s7, 1\n"
"s_cbranch_scc0 .S183\ns_branch .S182\n.S184:\ns_mov_b32 s7, 262144\n.S186:\nv_and_b32 v27, 0xf0, v1\nbuffer_load_dword v27, v27, s[16:19], s61 offen offset:4 sc0 sc1\nWV 0\n"
"v_cmp_ne_u32 vcc, s15, v27\ns_nop 0\ns_cbranch_vccz .S185\ns_sleep 6\ns_sub_u32 s7, s7, 1\ns_cbranch_scc0 .S186\ns_branch .S185\n.S188:\ns_mov_b32 s7, 262144\n.S190:\n"
"v_lshrrev_b32 v144, 9, v1\nv_mul_u32_u24 v144, 0x18000, v144\nv_lshlrev_b32 v144, 8, v144\nv_and_b32 v145, 0x1f0, v1\nv_add_u32 v144, v144, v145\ns_mov_b32 s6, 0x15c0000\n"
"buffer_load_dword v136, v144, s[16:19], s6 offen offset:4 sc0 sc1\ns_mov_b32 s6, 0x15c2000\nbuffer_load_dword v137, v144, s[16:19], s6 offen offset:4 sc0 sc1\n"
"s_mov_b32 s6, 0x45c0000\nbuffer_load_dword v138, v144, s[16:19], s6 offen offset:4 sc0 sc1\ns_mov_b32 s6, 0x45c2000\n"
"buffer_load_dword v139, v144, s[16:19], s6 offen offset:4 sc0 sc1\ns_mov_b32 s6, 0x75c0000\nbuffer_load_dword v140, v144, s[16:19], s6 offen offset:4 sc0 sc1\n"
"s_mov_b32 s6, 0x75c2000\nbuffer_load_dword v141, v144, s[16:19], s6 offen offset:4 sc0 sc1\ns_mov_b32 s6, 0xa5c0000\n"
"buffer_load_dword v142, v144, s[16:19], s6 offen offset:4 sc0 sc1\ns_mov_b32 s6, 0xa5c2000\nbuffer_load_dword v143, v144, s[16:19], s6 offen offset:4 sc0 sc1\nWV 0\n"
"v_xor_b32 v136, s15, v136\nv_xor_b32 v137, s15, v137\nv_xor_b32 v138, s15, v138\nv_xor_b32 v139, s15, v139\nv_xor_b32 v140, s15, v140\nv_xor_b32 v141, s15, v141\n"
"v_xor_b32 v142, s15, v142\nv_xor_b32 v143, s15, v143\nv_min_u32 v136, v136, v137\nv_min_u32 v138, v138, v139\nv_min_u32 v140, v140, v141\nv_min_u32 v142, v142, v143\n"
"v_or3_b32 v136, v136, v138, v140\nv_or_b32 v136, v136, v142\nv_cmp_ne_u32 vcc, 0, v136\ns_nop 0\ns_cbranch_vccz .S189\ns_sleep 6\ns_sub_u32 s7, s7, 1\ns_cbranch_scc0 .S190\n"
"s_branch .S189\n.FF187:\nv_lshrrev_b32 v144, 9, v1\nv_mul_u32_u24 v144, 0x18000, v144\nv_lshlrev_b32 v144, 8, v144\nv_and_b32 v145, 0x1f0, v1\nv_add_u32 v144, v144, v145\n"
"s_mov_b32 s6, 0x15c0000\nbuffer_load_dword v136, v144, s[16:19], s6 offen offset:4 sc0 sc1\ns_mov_b32 s6, 0x15c2000\n"
"buffer_load_dword v137, v144, s[16:19], s6 offen offset:4 sc0 sc1\ns_mov_b32 s6, 0x45c0000\nbuffer_load_dword v138, v144, s[16:19], s6 offen offset:4 sc0 sc1\n"
"s_mov_b32 s6, 0x45c2000\nbuffer_load_dword v139, v144, s[16:19], s6 offen offset:4 sc0 sc1\ns_mov_b32 s6, 0x75c0000\n"
"buffer_load_dword v140, v144, s[16:19], s6 offen offset:4 sc0 sc1\ns_mov_b32 s6, 0x75c2000\nbuffer_load_dword v141, v144, s[16:19], s6 offen offset:4 sc0 sc1\n"
"s_mov_b32 s6, 0xa5c0000\nbuffer_load_dword v142, v144, s[16:19], s6 offen offset:4 sc0 sc1\ns_mov_b32 s6, 0xa5c2000\n"
"buffer_load_dword v143, v144, s[16:19], s6 offen offset:4 sc0 sc1\ns_mov_b32 s90, 0\ns_swappc_b64 s[8:9], s[80:81]\nv_xor_b32 v136, s15, v136\nv_xor_b32 v137, s15, v137\n"
"v_xor_b32 v138, s15, v138\nv_xor_b32 v139, s15, v139\nv_xor_b32 v140, s15, v140\nv_xor_b32 v141, s15, v141\nv_xor_b32 v142, s15, v142\nv_xor_b32 v143, s15, v143\n"
"v_min_u32 v136, v136, v137\nv_min_u32 v138, v138, v139\nv_min_u32 v140, v140, v141\nv_min_u32 v142, v142, v143\nv_or3_b32 v136, v136, v138, v140\nv_or_b32 v136, v136, v142\n"
"v_cmp_ne_u32 vcc, 0, v136\ns_nop 0\ns_cbranch_vccnz .S188\n.S189:\ns_and_b32 s4, s13, 1\ns_lshl1_add_u32 s4, s4, s11\ns_lshl2_add_u32 s4, s12, s4\ns_lshr_b32 s5, s71, 16\n"
"s_cmp_lt_u32 s4, 28\ns_cselect_b32 s5, s71, s5\ns_cselect_b32 s3, 0, 28\ns_and_b32 s5, s5, 0x7ff\ns_sub_u32 s3, s4, s3\ns_mul_i32 s2, s4, 7168\ns_add_u32 s2, s2, 0xc04c800\n"
"s_add_u32 s6, s2, 4096\nbuffer_load_dwordx4 v[136:139], v1, s[16:19], s2 offen offset:0 sc0 sc1\nbuffer_load_dwordx4 v[140:143], v1, s[16:19], s2 offen offset:1024 sc0 sc1\n"
"buffer_load_dwordx4 v[144:147], v1, s[16:19], s2 offen offset:2048 sc0 sc1\nbuffer_load_dwordx4 v[148:151], v1, s[16:19], s2 offen offset:3072 sc0 sc1\n"
"buffer_load_dwordx4 v[152:155], v1, s[16:19], s6 offen offset:0 sc0 sc1\nbuffer_load_dwordx4 v[156:159], v1, s[16:19], s6 offen offset:1024 sc0 sc1\n"
"buffer_load_dwordx4 v[160:163], v1, s[16:19], s6 offen offset:2048 sc0 sc1\nv_lshrrev_b32 v167, 1, v1\nv_and_b32 v167, 24, v167\ns_mul_i32 s2, s5, 896\ns_lshl_b32 s3, s3, 5\n"
"s_add_u32 s2, s2, s3\ns_lshl2_add_u32 s5, s5, 0xc03e000\nbuffer_load_dwordx2 v[164:165], v167, s[28:31], s2 offen\nbuffer_load_dword v166, off, s[16:19], s5 sc0 sc1\n"
"s_mov_b32 s90, 0\ns_swappc_b64 s[8:9], s[80:81]\nAD 136,136,140\nAD 137,137,141\nAD 138,138,142\nAD 139,139,143\nAD 144,144,148\nAD 145,145,149\nAD 146,146,150\nAD 147,147,151\n"
"AD 152,152,156\nAD 153,153,157\nAD 154,154,158\nAD 155,155,159\nAD 136,136,144\nAD 137,137,145\nAD 138,138,146\nAD 139,139,147\nAD 152,152,160\nAD 153,153,161\nAD 154,154,162\n"
"AD 155,155,163\nAD 136,136,152\nAD 137,137,153\nAD 138,138,154\nAD 139,139,155\nv_mov_b32 v140, v136\nv_mov_b32 v141, v137\nv_mov_b32 v142, v138\nv_mov_b32 v143, v139\n"
"v_permlane32_swap_b32 v136, v140\nv_permlane32_swap_b32 v137, v141\nv_permlane32_swap_b32 v138, v142\nv_permlane32_swap_b32 v139, v143\nAD 136,136,140\nAD 137,137,141\n"
"AD 138,138,142\nAD 139,139,143\nv_mov_b32 v140, v136\nv_mov_b32 v141, v137\nv_mov_b32 v142, v138\nv_mov_b32 v143, v139\nv_permlane16_swap_b32 v136, v140\n"
"v_permlane16_swap_b32 v137, v141\nv_permlane16_swap_b32 v138, v142\nv_permlane16_swap_b32 v139, v143\nAD 136,136,140\nAD 137,137,141\nAD 138,138,142\nAD 139,139,143\n"
"v_add_f32_dpp v136, v136, v136 row_ror:8 row_mask:0xf bank_mask:0xf\nv_add_f32_dpp v137, v137, v137 row_ror:8 row_mask:0xf bank_mask:0xf\n"
"v_add_f32_dpp v138, v138, v138 row_ror:8 row_mask:0xf bank_mask:0xf\nv_add_f32_dpp v139, v139, v139 row_ror:8 row_mask:0xf bank_mask:0xf\n"
"v_add_f32_dpp v136, v136, v136 row_ror:4 row_mask:0xf bank_mask:0xf\nv_add_f32_dpp v137, v137, v137 row_ror:4 row_mask:0xf bank_mask:0xf\n"
"v_add_f32_dpp v138, v138, v138 row_ror:4 row_mask:0xf bank_mask:0xf\nv_add_f32_dpp v139, v139, v139 row_ror:4 row_mask:0xf bank_mask:0xf\nv_mul_f32 v166, 0xbfb8aa3b, v166\n"
"EX 166,166\nCV 136,136,137\nv_add_f32 v166, 1.0, v166\nv_rcp_f32 v166, v166\nCV 138,138,139\nCV 166,166,166\nv_lshlrev_b32 v166, 16, v166\nv_and_b32 v137, 0xffff0000, v136\n"
"v_lshlrev_b32 v136, 16, v136\nv_and_b32 v139, 0xffff0000, v138\nv_lshlrev_b32 v138, 16, v138\nMU 136,136,166\nMU 137,137,166\nMU 138,138,166\nMU 139,139,166\nCV 136,136,137\n"
"CV 138,138,139\nv_and_b32 v137, 0xffff0000, v136\nv_lshlrev_b32 v136, 16, v136\nv_and_b32 v139, 0xffff0000, v138\nv_lshlrev_b32 v138, 16, v138\nv_lshlrev_b32 v140, 16, v164\n"
"v_and_b32 v141, 0xffff0000, v164\nv_lshlrev_b32 v142, 16, v165\nv_and_b32 v143, 0xffff0000, v165\nAD 136,136,140\nAD 137,137,141\nAD 138,138,142\nAD 139,139,143\nCV 136,136,137\n"
"CV 137,138,139\nv_mov_b32 v138, s15\nv_mov_b32 v139, s71\ns_and_b32 s4, s13, 1\ns_lshl1_add_u32 s4, s4, s11\ns_lshl2_add_u32 s4, s12, s4\ns_lshr_b32 s5, s71, 16\n"
"s_cmp_lt_u32 s4, 28\ns_cselect_b32 s5, s71, s5\ns_cselect_b32 s3, 0, 28\ns_and_b32 s5, s5, 0x7ff\ns_sub_u32 s3, s4, s3\nv_mov_b32 v140, 0x40000000\nv_cmp_gt_u32 vcc, 64, v1\n"
"s_lshl_b32 s4, s4, 6\ns_add_u32 s4, s4, 0xc04b800\nv_cndmask_b32 v140, v140, v1, vcc\nSv 136,140,16,4,0\ns_mov_b32 s90, 100\ns_swappc_b64 s[8:9], s[80:81]\n.T195:\n"
"s_mov_b32 s62, 0\ns_call_b64 s[80:81], .RB11\ns_branch .T196\n.T197:\ns_mov_b32 s62, 0\ns_call_b64 s[80:81], .RA10\ns_branch .T198\n.T202:\ns_mov_b32 s62, 0\n"
"s_call_b64 s[80:81], .RB11\ns_branch .T203\n.T204:\ns_mov_b32 s62, 0\ns_call_b64 s[80:81], .RA10\ns_branch .T205\n.T208:\ns_mov_b32 s62, 0\ns_call_b64 s[80:81], .RB11\n"
"s_branch .T209\n.T210:\ns_mov_b32 s62, 0\ns_call_b64 s[80:81], .RA10\ns_branch .T211\n.S216:\ns_mov_b32 s7, 262144\n.S218:\n"
"buffer_load_dwordx4 v[100:103], v108, s[16:19], s65 offen offset:0 sc0 sc1\nbuffer_load_dwordx4 v[104:107], v108, s[16:19], s65 offen offset:256 sc0 sc1\nWV 0\n"
"v_xor_b32 v109, s15, v102\nv_xor_b32 v110, s15, v106\nv_or_b32 v109, v109, v110\nv_cmp_ne_u32 vcc, 0, v109\ns_nop 0\ns_cbranch_vccz .S217\ns_sleep 6\ns_sub_u32 s7, s7, 1\n"
"s_cbranch_scc0 .S218\ns_branch .S217\n.D214:\nv_mov_b32 v130, 0\nv_mov_b32 v131, 0\ns_mov_b32 s63, 0xff800000\ns_mov_b32 s67, 1\ns_branch .D215\n.T220:\n"
"s_lshl3_add_u32 s62, s67, 2\ns_call_b64 s[80:81], .RB11\ns_cmp_eq_u32 s67, 0\ns_cbranch_scc1 .T221\nSU 20,100,88\nv_min_f32 v20, 0, v20\nEX 20,20\ns_nop 0\nFM 86,101,20\n"
"SU 21,104,89\nv_min_f32 v21, 0, v21\nEX 21,21\ns_nop 0\nFM 87,105,21\nAR 22,192\nv_and_b32 v23, 0xffff0000, v22\nv_lshlrev_b32 v22, 16, v22\nMU 22,22,20\nMU 23,23,20\nCV 22,22,23\n"
"AW 192,22\nAR 22,193\nv_and_b32 v23, 0xffff0000, v22\nv_lshlrev_b32 v22, 16, v22\nMU 22,22,20\nMU 23,23,20\nCV 22,22,23\nAW 193,22\nAR 22,194\nv_and_b32 v23, 0xffff0000, v22\n"
"v_lshlrev_b32 v22, 16, v22\nMU 22,22,20\nMU 23,23,20\nCV 22,22,23\nAW 194,22\nAR 22,195\nv_and_b32 v23, 0xffff0000, v22\nv_lshlrev_b32 v22, 16, v22\nMU 22,22,20\nMU 23,23,20\n"
"CV 22,22,23\nAW 195,22\nAR 22,196\nv_and_b32 v23, 0xffff0000, v22\nv_lshlrev_b32 v22, 16, v22\nMU 22,22,21\nMU 23,23,21\nCV 22,22,23\nAW 196,22\nAR 22,197\n"
"v_and_b32 v23, 0xffff0000, v22\nv_lshlrev_b32 v22, 16, v22\nMU 22,22,21\nMU 23,23,21\nCV 22,22,23\nAW 197,22\nAR 22,198\nv_and_b32 v23, 0xffff0000, v22\n"
"v_lshlrev_b32 v22, 16, v22\nMU 22,22,21\nMU 23,23,21\nCV 22,22,23\nAW 198,22\nAR 22,199\nv_and_b32 v23, 0xffff0000, v22\nv_lshlrev_b32 v22, 16, v22\nMU 22,22,21\nMU 23,23,21\n"
"CV 22,22,23\nAW 199,22\nAR 22,200\nv_and_b32 v23, 0xffff0000, v22\nv_lshlrev_b32 v22, 16, v22\nMU 22,22,20\nMU 23,23,20\nCV 22,22,23\nAW 200,22\nAR 22,201\n"
"v_and_b32 v23, 0xffff0000, v22\nv_lshlrev_b32 v22, 16, v22\nMU 22,22,20\nMU 23,23,20\nCV 22,22,23\nAW 201,22\nAR 22,202\nv_and_b32 v23, 0xffff0000, v22\n"
"v_lshlrev_b32 v22, 16, v22\nMU 22,22,20\nMU 23,23,20\nCV 22,22,23\nAW 202,22\nAR 22,203\nv_and_b32 v23, 0xffff0000, v22\nv_lshlrev_b32 v22, 16, v22\nMU 22,22,20\nMU 23,23,20\n"
"CV 22,22,23\nAW 203,22\nAR 22,204\nv_and_b32 v23, 0xffff0000, v22\nv_lshlrev_b32 v22, 16, v22\nMU 22,22,21\nMU 23,23,21\nCV 22,22,23\nAW 204,22\nAR 22,205\n"
"v_and_b32 v23, 0xffff0000, v22\nv_lshlrev_b32 v22, 16, v22\nMU 22,22,21\nMU 23,23,21\nCV 22,22,23\nAW 205,22\nAR 22,206\nv_and_b32 v23, 0xffff0000, v22\n"
"v_lshlrev_b32 v22, 16, v22\nMU 22,22,21\nMU 23,23,21\nCV 22,22,23\nAW 206,22\nAR 22,207\nv_and_b32 v23, 0xffff0000, v22\nv_lshlrev_b32 v22, 16, v22\nMU 22,22,21\nMU 23,23,21\n"
"CV 22,22,23\nAW 207,22\nAR 22,208\nv_and_b32 v23, 0xffff0000, v22\nv_lshlrev_b32 v22, 16, v22\nMU 22,22,20\nMU 23,23,20\nCV 22,22,23\nAW 208,22\nAR 22,209\n"
"v_and_b32 v23, 0xffff0000, v22\nv_lshlrev_b32 v22, 16, v22\nMU 22,22,20\nMU 23,23,20\nCV 22,22,23\nAW 209,22\nAR 22,210\nv_and_b32 v23, 0xffff0000, v22\n"
"v_lshlrev_b32 v22, 16, v22\nMU 22,22,20\nMU 23,23,20\nCV 22,22,23\nAW 210,22\nAR 22,211\nv_and_b32 v23, 0xffff0000, v22\nv_lshlrev_b32 v22, 16, v22\nMU 22,22,20\nMU 23,23,20\n"
"CV 22,22,23\nAW 211,22\nAR 22,212\nv_and_b32 v23, 0xffff0000, v22\nv_lshlrev_b32 v22, 16, v22\nMU 22,22,21\nMU 23,23,21\nCV 22,22,23\nAW 212,22\nAR 22,213\n"
"v_and_b32 v23, 0xffff0000, v22\nv_lshlrev_b32 v22, 16, v22\nMU 22,22,21\nMU 23,23,21\nCV 22,22,23\nAW 213,22\nAR 22,214\nv_and_b32 v23, 0xffff0000, v22\n"
"v_lshlrev_b32 v22, 16, v22\nMU 22,22,21\nMU 23,23,21\nCV 22,22,23\nAW 214,22\nAR 22,215\nv_and_b32 v23, 0xffff0000, v22\nv_lshlrev_b32 v22, 16, v22\nMU 22,22,21\nMU 23,23,21\n"
"CV 22,22,23\nAW 215,22\nAR 22,216\nv_and_b32 v23, 0xffff0000, v22\nv_lshlrev_b32 v22, 16, v22\nMU 22,22,20\nMU 23,23,20\nCV 22,22,23\nAW 216,22\nAR 22,217\n"
"v_and_b32 v23, 0xffff0000, v22\nv_lshlrev_b32 v22, 16, v22\nMU 22,22,20\nMU 23,23,20\nCV 22,22,23\nAW 217,22\nAR 22,218\nv_and_b32 v23, 0xffff0000, v22\n"
"v_lshlrev_b32 v22, 16, v22\nMU 22,22,20\nMU 23,23,20\nCV 22,22,23\nAW 218,22\nAR 22,219\nv_and_b32 v23, 0xffff0000, v22\nv_lshlrev_b32 v22, 16, v22\nMU 22,22,20\nMU 23,23,20\n"
"CV 22,22,23\nAW 219,22\nAR 22,220\nv_and_b32 v23, 0xffff0000, v22\nv_lshlrev_b32 v22, 16, v22\nMU 22,22,21\nMU 23,23,21\nCV 22,22,23\nAW 220,22\nAR 22,221\n"
"v_and_b32 v23, 0xffff0000, v22\nv_lshlrev_b32 v22, 16, v22\nMU 22,22,21\nMU 23,23,21\nCV 22,22,23\nAW 221,22\nAR 22,222\nv_and_b32 v23, 0xffff0000, v22\n"
"v_lshlrev_b32 v22, 16, v22\nMU 22,22,21\nMU 23,23,21\nCV 22,22,23\nAW 222,22\nAR 22,223\nv_and_b32 v23, 0xffff0000, v22\nv_lshlrev_b32 v22, 16, v22\nMU 22,22,21\nMU 23,23,21\n"
"CV 22,22,23\nAW 223,22\nAR 22,224\nv_and_b32 v23, 0xffff0000, v22\nv_lshlrev_b32 v22, 16, v22\nMU 22,22,20\nMU 23,23,20\nCV 22,22,23\nAW 224,22\nAR 22,225\n"
"v_and_b32 v23, 0xffff0000, v22\nv_lshlrev_b32 v22, 16, v22\nMU 22,22,20\nMU 23,23,20\nCV 22,22,23\nAW 225,22\nAR 22,226\nv_and_b32 v23, 0xffff0000, v22\n"
"v_lshlrev_b32 v22, 16, v22\nMU 22,22,20\nMU 23,23,20\nCV 22,22,23\nAW 226,22\nAR 22,227\nv_and_b32 v23, 0xffff0000, v22\nv_lshlrev_b32 v22, 16, v22\nMU 22,22,20\nMU 23,23,20\n"
"CV 22,22,23\nAW 227,22\nAR 22,228\nv_and_b32 v23, 0xffff0000, v22\nv_lshlrev_b32 v22, 16, v22\nMU 22,22,21\nMU 23,23,21\nCV 22,22,23\nAW 228,22\nAR 22,229\n"
"v_and_b32 v23, 0xffff0000, v22\nv_lshlrev_b32 v22, 16, v22\nMU 22,22,21\nMU 23,23,21\nCV 22,22,23\nAW 229,22\nAR 22,230\nv_and_b32 v23, 0xffff0000, v22\n"
"v_lshlrev_b32 v22, 16, v22\nMU 22,22,21\nMU 23,23,21\nCV 22,22,23\nAW 230,22\nAR 22,231\nv_and_b32 v23, 0xffff0000, v22\nv_lshlrev_b32 v22, 16, v22\nMU 22,22,21\nMU 23,23,21\n"
"CV 22,22,23\nAW 231,22\nAR 22,232\nv_and_b32 v23, 0xffff0000, v22\nv_lshlrev_b32 v22, 16, v22\nMU 22,22,20\nMU 23,23,20\nCV 22,22,23\nAW 232,22\nAR 22,233\n"
"v_and_b32 v23, 0xffff0000, v22\nv_lshlrev_b32 v22, 16, v22\nMU 22,22,20\nMU 23,23,20\nCV 22,22,23\nAW 233,22\nAR 22,234\nv_and_b32 v23, 0xffff0000, v22\n"
"v_lshlrev_b32 v22, 16, v22\nMU 22,22,20\nMU 23,23,20\nCV 22,22,23\nAW 234,22\nAR 22,235\nv_and_b32 v23, 0xffff0000, v22\nv_lshlrev_b32 v22, 16, v22\nMU 22,22,20\nMU 23,23,20\n"
"CV 22,22,23\nAW 235,22\nAR 22,236\nv_and_b32 v23, 0xffff0000, v22\nv_lshlrev_b32 v22, 16, v22\nMU 22,22,21\nMU 23,23,21\nCV 22,22,23\nAW 236,22\nAR 22,237\n"
"v_and_b32 v23, 0xffff0000, v22\nv_lshlrev_b32 v22, 16, v22\nMU 22,22,21\nMU 23,23,21\nCV 22,22,23\nAW 237,22\nAR 22,238\nv_and_b32 v23, 0xffff0000, v22\n"
"v_lshlrev_b32 v22, 16, v22\nMU 22,22,21\nMU 23,23,21\nCV 22,22,23\nAW 238,22\nAR 22,239\nv_and_b32 v23, 0xffff0000, v22\nv_lshlrev_b32 v22, 16, v22\nMU 22,22,21\nMU 23,23,21\n"
"CV 22,22,23\nAW 239,22\nAR 22,240\nv_and_b32 v23, 0xffff0000, v22\nv_lshlrev_b32 v22, 16, v22\nMU 22,22,20\nMU 23,23,20\nCV 22,22,23\nAW 240,22\nAR 22,241\n"
"v_and_b32 v23, 0xffff0000, v22\nv_lshlrev_b32 v22, 16, v22\nMU 22,22,20\nMU 23,23,20\nCV 22,22,23\nAW 241,22\nAR 22,242\nv_and_b32 v23, 0xffff0000, v22\n"
"v_lshlrev_b32 v22, 16, v22\nMU 22,22,20\nMU 23,23,20\nCV 22,22,23\nAW 242,22\nAR 22,243\nv_and_b32 v23, 0xffff0000, v22\nv_lshlrev_b32 v22, 16, v22\nMU 22,22,20\nMU 23,23,20\n"
"CV 22,22,23\nAW 243,22\nAR 22,244\nv_and_b32 v23, 0xffff0000, v22\nv_lshlrev_b32 v22, 16, v22\nMU 22,22,21\nMU 23,23,21\nCV 22,22,23\nAW 244,22\nAR 22,245\n"
"v_and_b32 v23, 0xffff0000, v22\nv_lshlrev_b32 v22, 16, v22\nMU 22,22,21\nMU 23,23,21\nCV 22,22,23\nAW 245,22\nAR 22,246\nv_and_b32 v23, 0xffff0000, v22\n"
"v_lshlrev_b32 v22, 16, v22\nMU 22,22,21\nMU 23,23,21\nCV 22,22,23\nAW 246,22\nAR 22,247\nv_and_b32 v23, 0xffff0000, v22\nv_lshlrev_b32 v22, 16, v22\nMU 22,22,21\nMU 23,23,21\n"
"CV 22,22,23\nAW 247,22\nAR 22,248\nv_and_b32 v23, 0xffff0000, v22\nv_lshlrev_b32 v22, 16, v22\nMU 22,22,20\nMU 23,23,20\nCV 22,22,23\nAW 248,22\nAR 22,249\n"
"v_and_b32 v23, 0xffff0000, v22\nv_lshlrev_b32 v22, 16, v22\nMU 22,22,20\nMU 23,23,20\nCV 22,22,23\nAW 249,22\nAR 22,250\nv_and_b32 v23, 0xffff0000, v22\n"
"v_lshlrev_b32 v22, 16, v22\nMU 22,22,20\nMU 23,23,20\nCV 22,22,23\nAW 250,22\nAR 22,251\nv_and_b32 v23, 0xffff0000, v22\nv_lshlrev_b32 v22, 16, v22\nMU 22,22,20\nMU 23,23,20\n"
"CV 22,22,23\nAW 251,22\nAR 22,252\nv_and_b32 v23, 0xffff0000, v22\nv_lshlrev_b32 v22, 16, v22\nMU 22,22,21\nMU 23,23,21\nCV 22,22,23\nAW 252,22\nAR 22,253\n"
"v_and_b32 v23, 0xffff0000, v22\nv_lshlrev_b32 v22, 16, v22\nMU 22,22,21\nMU 23,23,21\nCV 22,22,23\nAW 253,22\nAR 22,254\nv_and_b32 v23, 0xffff0000, v22\n"
"v_lshlrev_b32 v22, 16, v22\nMU 22,22,21\nMU 23,23,21\nCV 22,22,23\nAW 254,22\nAR 22,255\nv_and_b32 v23, 0xffff0000, v22\nv_lshlrev_b32 v22, 16, v22\nMU 22,22,21\nMU 23,23,21\n"
"CV 22,22,23\nAW 255,22\nv_lshrrev_b32 v22, 8, v1\nv_bfe_u32 v23, v1, 6, 2\nv_cmp_eq_u32 vcc, v22, v23\nv_and_b32 v22, 16, v1\nv_mov_b32 v23, 0x3f80\n"
"v_cndmask_b32 v23, 0, v23, vcc\nv_lshlrev_b32 v23, v22, v23\nv_and_b32 v22, 32, v1\nv_cmp_eq_u32 vcc, 0, v22\nv_mov_b32 v128, 0\nv_mov_b32 v129, 0\n"
"v_cndmask_b32 v130, 0, v23, vcc\nv_sub_u32 v131, v23, v130\nv_mov_b32 v132, 0\nv_mov_b32 v133, 0\ns_nop 1\nMava 0,130,192\nMava 8,128,192\nMava 12,128,196\nMava 4,130,196\n"
"Mava 16,130,200\nMava 24,128,200\nMava 28,128,204\nMava 20,130,204\nMava 32,130,208\nMava 40,128,208\nMava 44,128,212\nMava 36,130,212\nMava 48,130,216\nMava 56,128,216\n"
"Mava 60,128,220\nMava 52,130,220\nMava 64,130,224\nMava 72,128,224\nMava 76,128,228\nMava 68,130,228\nMava 80,130,232\nMava 88,128,232\nMava 92,128,236\nMava 84,130,236\n"
"Mava 96,130,240\nMava 104,128,240\nMava 108,128,244\nMava 100,130,244\nMava 112,130,248\nMava 120,128,248\nMava 124,128,252\nMava 116,130,252\ns_nop 7\ns_branch .T221\n.S223:\n"
"s_mov_b32 s7, 262144\n.S225:\ns_add_u32 s0, s92, 0\nbuffer_load_dwordx4 v[208:211], v1, s[16:19], s0 offen offset:0 sc0 sc1\n"
"buffer_load_dwordx4 v[212:215], v1, s[16:19], s0 offen offset:1024 sc0 sc1\nWV 0\nv_max_u32 v21, v211, v215\nv_cmp_eq_u32 vcc, -1, v21\ns_nop 0\ns_cbranch_vccz .S224\ns_sleep 6\n"
"s_sub_u32 s7, s7, 1\ns_cbranch_scc0 .S225\ns_branch .S224\n.S226:\ns_mov_b32 s7, 262144\n.S228:\ns_add_u32 s0, s92, 131072\n"
"buffer_load_dwordx4 v[216:219], v1, s[16:19], s0 offen offset:0 sc0 sc1\nbuffer_load_dwordx4 v[220:223], v1, s[16:19], s0 offen offset:1024 sc0 sc1\nWV 0\n"
"v_max_u32 v21, v219, v223\nv_cmp_eq_u32 vcc, -1, v21\ns_nop 0\ns_cbranch_vccz .S227\ns_sleep 6\ns_sub_u32 s7, s7, 1\ns_cbranch_scc0 .S228\ns_branch .S227\n.S229:\n"
"s_mov_b32 s7, 262144\n.S231:\ns_add_u32 s0, s92, 262144\nbuffer_load_dwordx4 v[224:227], v1, s[16:19], s0 offen offset:0 sc0 sc1\n"
"buffer_load_dwordx4 v[228:231], v1, s[16:19], s0 offen offset:1024 sc0 sc1\nWV 0\nv_max_u32 v21, v227, v231\nv_cmp_eq_u32 vcc, -1, v21\ns_nop 0\ns_cbranch_vccz .S230\ns_sleep 6\n"
"s_sub_u32 s7, s7, 1\ns_cbranch_scc0 .S231\ns_branch .S230\n.S232:\ns_mov_b32 s7, 262144\n.S234:\ns_add_u32 s0, s92, 393216\n"
"buffer_load_dwordx4 v[208:211], v1, s[16:19], s0 offen offset:0 sc0 sc1\nbuffer_load_dwordx4 v[212:215], v1, s[16:19], s0 offen offset:1024 sc0 sc1\nWV 0\n"
"v_max_u32 v21, v211, v215\nv_cmp_eq_u32 vcc, -1, v21\ns_nop 0\ns_cbranch_vccz .S233\ns_sleep 6\ns_sub_u32 s7, s7, 1\ns_cbranch_scc0 .S234\ns_branch .S233\n.S235:\n"
"s_mov_b32 s7, 262144\n.S237:\ns_add_u32 s0, s92, 524288\nbuffer_load_dwordx4 v[216:219], v1, s[16:19], s0 offen offset:0 sc0 sc1\n"
"buffer_load_dwordx4 v[220:223], v1, s[16:19], s0 offen offset:1024 sc0 sc1\nWV 0\nv_max_u32 v21, v219, v223\nv_cmp_eq_u32 vcc, -1, v21\ns_nop 0\ns_cbranch_vccz .S236\ns_sleep 6\n"
"s_sub_u32 s7, s7, 1\ns_cbranch_scc0 .S237\ns_branch .S236\n.S238:\ns_mov_b32 s7, 262144\n.S240:\ns_add_u32 s0, s92, 655360\n"
"buffer_load_dwordx4 v[224:227], v1, s[16:19], s0 offen offset:0 sc0 sc1\nbuffer_load_dwordx4 v[228:231], v1, s[16:19], s0 offen offset:1024 sc0 sc1\nWV 0\n"
"v_max_u32 v21, v227, v231\nv_cmp_eq_u32 vcc, -1, v21\ns_nop 0\ns_cbranch_vccz .S239\ns_sleep 6\ns_sub_u32 s7, s7, 1\ns_cbranch_scc0 .S240\ns_branch .S239\n.S241:\n"
"s_mov_b32 s7, 262144\n.S243:\ns_add_u32 s0, s92, 786432\nbuffer_load_dwordx4 v[208:211], v1, s[16:19], s0 offen offset:0 sc0 sc1\n"
"buffer_load_dwordx4 v[212:215], v1, s[16:19], s0 offen offset:1024 sc0 sc1\nWV 0\nv_max_u32 v21, v211, v215\nv_cmp_eq_u32 vcc, -1, v21\ns_nop 0\ns_cbranch_vccz .S242\ns_sleep 6\n"
"s_sub_u32 s7, s7, 1\ns_cbranch_scc0 .S243\ns_branch .S242\n.S244:\ns_mov_b32 s7, 262144\n.S246:\nbuffer_load_dwordx4 v[64:67], v89, s[16:19], s40 offen offset:0 sc0 sc1\n"
"buffer_load_dwordx4 v[68:71], v89, s[16:19], s40 offen offset:16 sc0 sc1\nbuffer_load_dwordx4 v[72:75], v89, s[16:19], s40 offen offset:1792 sc0 sc1\n"
"buffer_load_dwordx4 v[76:79], v89, s[16:19], s40 offen offset:1808 sc0 sc1\nbuffer_load_dwordx2 v[80:81], v88, s[16:19], s63 offen offset:0 sc0 sc1\n"
"buffer_load_dwordx2 v[82:83], v88, s[16:19], s63 offen offset:512 sc0 sc1\nWV 0\nv_xor_b32 v66, s15, v66\nv_xor_b32 v70, s15, v70\nv_xor_b32 v74, s15, v74\n"
"v_xor_b32 v78, s15, v78\nv_xor_b32 v81, s15, v81\nv_xor_b32 v83, s15, v83\nv_or3_b32 v92, v66, v70, v74\nv_or3_b32 v92, v92, v78, v81\nv_or_b32 v92, v92, v83\n"
"v_cmp_ne_u32 vcc, 0, v92\ns_nop 0\ns_cbranch_vccz .S245\ns_sleep 6\ns_sub_u32 s7, s7, 1\ns_cbranch_scc0 .S246\ns_branch .S245\n.S247:\ns_mov_b32 s7, 262144\n.S249:\n"
"buffer_load_dword v27, off, s[16:19], s60 offset:512 sc0 sc1\nWV 0\nv_cmp_ne_u32 vcc, s15, v27\ns_nop 0\ns_cbranch_vccz .S248\ns_sleep 6\ns_sub_u32 s7, s7, 1\n"
"s_cbranch_scc0 .S249\ns_branch .S248\n.S250:\ns_mov_b32 s7, 262144\n.S252:\nbuffer_load_dword v27, v24, s[16:19], s41 offen sc0 sc1\nWV 0\nv_xor_b32 v27, s15, v27\n"
"v_and_b32 v26, 0xffffdfff, v27\nv_cmp_ne_u32 s[64:65], 0, v27\nv_cmp_ne_u32 vcc, 0, v26\ns_nop 0\ns_cbranch_vccz .S251\ns_sleep 6\ns_sub_u32 s7, s7, 1\ns_cbranch_scc0 .S252\n"
"s_branch .S251\n.Z254:\ns_sub_u32 s7, s7, 1\ns_cbranch_scc1 .Z255\ns_cmp_lg_u64 s[44:45], 0\ns_cbranch_scc0 .Z257\n"
"buffer_load_dwordx4 v[96:99], v1, s[16:19], s24 offen offset:0 sc0 sc1\nbuffer_load_dwordx4 v[100:103], v1, s[16:19], s24 offen offset:1024 sc0 sc1\n.Z257:\n"
"s_cmp_lg_u64 s[46:47], 0\ns_cbranch_scc0 .Z258\nbuffer_load_dwordx4 v[104:107], v1, s[16:19], s25 offen offset:0 sc0 sc1\n"
"buffer_load_dwordx4 v[108:111], v1, s[16:19], s25 offen offset:1024 sc0 sc1\n.Z258:\ns_cmp_lg_u64 s[48:49], 0\ns_cbranch_scc0 .Z259\n"
"buffer_load_dwordx4 v[112:115], v1, s[16:19], s26 offen offset:0 sc0 sc1\nbuffer_load_dwordx4 v[116:119], v1, s[16:19], s26 offen offset:1024 sc0 sc1\n.Z259:\n"
"s_cmp_lg_u64 s[50:51], 0\ns_cbranch_scc0 .Z260\nbuffer_load_dwordx4 v[120:123], v1, s[16:19], s27 offen offset:0 sc0 sc1\n"
"buffer_load_dwordx4 v[124:127], v1, s[16:19], s27 offen offset:1024 sc0 sc1\n.Z260:\ns_cmp_lg_u64 s[52:53], 0\ns_cbranch_scc0 .Z261\n"
"buffer_load_dwordx4 v[32:35], v1, s[16:19], s32 offen offset:0 sc0 sc1\nbuffer_load_dwordx4 v[36:39], v1, s[16:19], s32 offen offset:1024 sc0 sc1\n.Z261:\n"
"s_cmp_lg_u64 s[54:55], 0\ns_cbranch_scc0 .Z262\nbuffer_load_dwordx4 v[40:43], v1, s[16:19], s33 offen offset:0 sc0 sc1\n"
"buffer_load_dwordx4 v[44:47], v1, s[16:19], s33 offen offset:1024 sc0 sc1\n.Z262:\ns_cmp_lg_u64 s[56:57], 0\ns_cbranch_scc0 .Z263\n"
"buffer_load_dwordx4 v[48:51], v1, s[16:19], s34 offen offset:0 sc0 sc1\nbuffer_load_dwordx4 v[52:55], v1, s[16:19], s34 offen offset:1024 sc0 sc1\n.Z263:\n"
"s_cmp_lg_u64 s[58:59], 0\ns_cbranch_scc0 .Z264\nbuffer_load_dwordx4 v[56:59], v1, s[16:19], s35 offen offset:0 sc0 sc1\n"
"buffer_load_dwordx4 v[60:63], v1, s[16:19], s35 offen offset:1024 sc0 sc1\n.Z264:\ns_sleep 8\nWV 0\ns_branch .Z256\n.p2align 12\n.PA4:\ns_mov_b64 exec, 0\ns_cmp_eq_u32 s11, 1\n"
"s_cbranch_scc1 .W572\ns_cmp_eq_u32 s11, 2\ns_cbranch_scc1 .W573\ns_cmp_eq_u32 s11, 3\ns_cbranch_scc1 .W574\ns_branch .W571\n.WB274:\ns_mov_b64 exec, -1\ns_add_u32 m0, s47, -49152\n"
"s_add_u32 s48, s48, 128\nD0 2,20,48,0\nD0 3,20,48,1024\nD0 4,20,48,2048\nD0 5,20,48,3072\ns_and_b32 s86, s74, 15\ns_lshl2_add_u32 s86, s86, s11\ns_mov_b32 s88, 0\n"
"s_lshl_b32 s93, s73, 8\ns_lshl2_add_u32 s93, s86, s93\ns_add_u32 s93, s93, 0xc04ac00\ns_lshl_b32 s86, s86, 14\ns_mul_i32 s91, s73, 25165824\ns_add_u32 s86, s86, s91\n"
"s_and_b32 s92, s13, 1\ns_lshl_b32 s92, s92, 13\ns_add_u32 s86, s86, s92\ns_add_u32 s94, s86, 0x1480000\ns_xor_b32 s95, s94, 0x2000\ns_add_u32 s86, s94, 512\n"
"s_load_dword s87, s[54:55], s86\ns_add_u32 m0, s47, -32768\ns_add_u32 s45, s45, 128\nD0 2,20,45,0\nD0 3,20,45,1024\nD0 4,20,45,2048\nD0 5,20,45,3072\ns_add_u32 m0, s47, 32768\n"
"s_add_u32 s44, s44, 0x40000\nD0 6,24,44,0\nD0 7,24,44,1024\nD0 8,24,44,2048\nD0 9,24,44,3072\ns_add_u32 m0, s47, 49152\ns_add_u32 s44, s44, 0x40000\nD0 6,24,44,0\nD0 7,24,44,1024\n"
"D0 8,24,44,2048\nD0 9,24,44,3072\ns_add_u32 m0, s47, -16384\ns_add_u32 s48, s48, 128\nD0 2,20,48,0\nD0 3,20,48,1024\nD0 4,20,48,2048\nD0 5,20,48,3072\nv_mov_b32 v52, 0x40000000\n"
"v_cmp_gt_u32 vcc, 56, v0\ns_lshl2_add_u32 s2, s10, s11\ns_mul_i32 s2, s2, 1792\nv_cndmask_b32 v53, v52, v1, vcc\ns_mov_b32 s36, s56\ns_and_b32 s37, s57, 0xffff\n"
"s_mov_b32 s38, 0x380\ns_mov_b32 s39, 0x20000\ns_mov_b32 s40, s58\ns_and_b32 s41, s59, 0xffff\ns_mov_b32 s42, 0x380\ns_mov_b32 s43, 0x20000\n"
"buffer_load_dwordx4 v[32:35], v53, s[28:31], s2 offen offset:0\nbuffer_load_dwordx4 v[36:39], v53, s[28:31], s2 offen offset:896\n"
"buffer_load_dwordx4 v[40:43], v53, s[36:39], 0 offen offset:0\nbuffer_load_dwordx4 v[44:47], v53, s[40:43], 0 offen offset:0\nWL 0\ns_mul_i32 s15, s15, 1664525\n"
"s_add_u32 s15, s15, 1013904223\nv_mov_b32 v25, s15\ns_mov_b32 s57, 0x3db8aa3b\ns_and_b32 s59, s11, 1\ns_lshl_b32 s59, s59, 13\ns_mul_i32 s2, s73, 25165824\ns_and_b32 s3, s12, 1\n"
"s_lshl_b32 s3, s3, 21\ns_add_u32 s2, s2, s3\ns_lshl2_add_u32 s3, s13, s11\ns_lshl_b32 s3, s3, 15\ns_add_u32 s2, s2, s3\ns_add_u32 s56, s2, 0x800000\ns_xor_b32 s53, s56, 0xc000\n"
"buffer_load_dword v31, v24, s[24:27], s96 offen\ns_add_u32 s96, s96, 0x80000\nbuffer_load_dword v31, v24, s[24:27], s96 offen\ns_add_u32 s96, s96, 0x80000\n"
"v_lshrrev_b32 v60, 3, v0\nv_and_b32 v61, 0x70, v1\nv_lshl_or_b32 v60, v60, 10, v61\nv_mov_b32 v20, 0\nv_mov_b32 v21, 0\nv_mov_b32 v22, 0\nv_mov_b32 v23, 0\nSv 20,60,16,94,0\n"
"Sv 20,60,16,95,0\nWV 28\ns_barrier\nRa 128,10,0\nRa 132,11,0\nRa 136,10,2048\nRa 140,11,2048\nRa 144,10,8192\nRa 148,11,8192\nRa 152,10,10240\nRa 156,11,10240\nTa 160,12,0\n"
"Ta 162,12,2048\nTa 164,12,16384\nTa 166,12,18432\nTa 168,13,0\nTa 170,13,2048\nTa 172,13,16384\nTa 174,13,18432\nTa 176,14,0\nTa 178,14,2048\nTa 180,14,16384\nTa 182,14,18432\n"
"Ta 184,15,0\nTa 186,15,2048\nTa 188,15,16384\nTa 190,15,18432\nWL 0\ns_barrier\nZvaa 64,160,128\nTa 224,16,0\nMvaa 64,164,132\nTa 226,16,2048\nZvaa 96,164,140\n"
"s_add_u32 m0, s47, -65536\ns_add_u32 s45, s45, 128\nMvaa 96,160,136\nTa 228,16,16384\nZvaa 128,160,144\nTa 230,16,18432\nMvaa 128,164,148\nD0 2,20,45,0\nZvaa 160,164,156\n"
"Ta 232,17,0\nMvaa 160,160,152\nTa 234,17,2048\nZvaa 164,168,152\ns_mov_b32 s36, s60\nMvaa 164,172,156\nTa 236,17,16384\nZvaa 132,172,148\nTa 238,17,18432\nMvaa 132,168,144\n"
"s_and_b32 s37, s61, 0xffff\nZvaa 100,168,136\nTa 240,18,0\nMvaa 100,172,140\nTa 242,18,2048\nZvaa 68,172,132\nD0 3,20,45,1024\nMvaa 68,168,128\nTa 244,18,16384\nZvaa 72,176,128\n"
"Ta 246,18,18432\nMvaa 72,180,132\ns_mov_b32 s38, 0x380000\nZvaa 104,180,140\nTa 248,19,0\nMvaa 104,176,136\nTa 250,19,2048\nZvaa 136,176,144\ns_mov_b32 s39, 0x20000\n"
"Mvaa 136,180,148\nTa 252,19,16384\nZvaa 168,180,156\nTa 254,19,18432\nMvaa 168,176,152\nD0 4,20,45,2048\nZvaa 172,184,152\ns_lshl3_add_u32 s75, s12, s13\nMvaa 172,188,156\n"
"s_lshl2_add_u32 s75, s75, s11\nZvaa 140,188,148\ns_mul_i32 s75, s75, 7168\nMvaa 140,184,144\ns_mul_i32 s76, s11, 7168\nZvaa 108,184,136\ns_add_u32 s76, s76, 131072\n"
"Mvaa 108,188,140\nD0 5,20,45,3072\nZvaa 76,188,132\ns_mov_b32 s42, 1605632\nMvaa 76,184,128\ns_waitcnt vmcnt(24) lgkmcnt(0)\ns_barrier\nZvaa 80,224,128\nRa 192,10,16384\n"
"Mvaa 80,228,132\ns_add_u32 m0, s47, 0\ns_add_u32 s44, s44, 0x40000\nZvaa 112,228,140\nRa 196,11,16384\nMvaa 112,224,136\nZvaa 144,224,144\nRa 200,10,18432\nMvaa 144,228,148\n"
"D0 6,24,44,0\nZvaa 176,228,156\nRa 204,11,18432\nMvaa 176,224,152\nZvaa 180,232,152\nRa 208,10,24576\nMvaa 180,236,156\ns_and_b32 s78, s74, 3\ns_lshl2_add_u32 s78, s73, s78\n"
"s_lshl_b32 s78, s78, 12\ns_bitcmp1_b32 s11, 0\ns_cselect_b32 s79, s48, s45\ns_add_u32 s79, s79, 512\ns_and_b32 s79, s79, 0xfffe0000\ns_add_u32 s78, s78, s79\n"
"buffer_load_dword v31, v26, s[20:23], s78 offen\nZvaa 148,236,148\nRa 212,11,24576\nMvaa 148,232,144\nZvaa 116,232,136\nRa 216,10,26624\nMvaa 116,236,140\nD0 7,24,44,1024\n"
"Zvaa 84,236,132\nRa 220,11,26624\nMvaa 84,232,128\nZvaa 88,240,128\nMvaa 88,244,132\nZvaa 120,244,140\nMvaa 120,240,136\nZvaa 152,240,144\nMvaa 152,244,148\nD0 8,24,44,2048\n"
"Zvaa 184,244,156\nMvaa 184,240,152\nZvaa 188,248,152\nMvaa 188,252,156\nZvaa 156,252,148\nMvaa 156,248,144\nZvaa 124,248,136\nMvaa 124,252,140\nD0 9,24,44,3072\nZvaa 92,252,132\n"
"Mvaa 92,248,128\nWL 0\nZaaa 16,224,192\nRa 128,10,32768\nMaaa 16,228,196\ns_add_u32 m0, s47, 16384\ns_add_u32 s44, s44, 0x40000\nZaaa 48,228,204\nRa 132,11,32768\nMaaa 48,224,200\n"
"Zaaa 80,224,208\nRa 136,10,34816\nMaaa 80,228,212\nD0 6,24,44,0\nZaaa 112,228,220\nRa 140,11,34816\nMaaa 112,224,216\nZaaa 116,232,216\nRa 144,10,40960\nMaaa 116,236,220\n"
"Zaaa 84,236,212\nRa 148,11,40960\nMaaa 84,232,208\nZaaa 52,232,200\nRa 152,10,43008\nMaaa 52,236,204\nD0 7,24,44,1024\nZaaa 20,236,196\nRa 156,11,43008\nMaaa 20,232,192\n"
"Zaaa 24,240,192\nMaaa 24,244,196\nZaaa 56,244,204\nMaaa 56,240,200\nZaaa 88,240,208\nMaaa 88,244,212\nD0 8,24,44,2048\nZaaa 120,244,220\nMaaa 120,240,216\nZaaa 124,248,216\n"
"Maaa 124,252,220\nZaaa 92,252,212\nMaaa 92,248,208\nZaaa 60,248,200\nMaaa 60,252,204\nD0 9,24,44,3072\nZaaa 28,252,196\nMaaa 28,248,192\ns_waitcnt vmcnt(25) lgkmcnt(0)\ns_barrier\n"
"Zaaa 0,160,192\nTa 224,16,32768\nWV 17\nMaaa 0,164,196\nTa 226,16,34816\nZaaa 32,164,204\ns_add_u32 m0, s47, -49152\ns_add_u32 s48, s48, 128\nMaaa 32,160,200\nTa 228,16,49152\n"
"Zaaa 64,160,208\nTa 230,16,51200\nbuffer_load_dword v31, v24, s[24:27], s96 offen\ns_add_u32 s96, s96, 0x80000\nMaaa 64,164,212\nD0 2,20,48,0\nv_lshlrev_b32 v48, 16, v32\n"
"Zaaa 96,164,220\nTa 232,17,32768\nMaaa 96,160,216\nTa 234,17,34816\nv_lshlrev_b32 v49, 16, v36\nZaaa 100,168,216\nv_lshlrev_b32 v50, 16, v40\nv_lshlrev_b32 v51, 16, v44\n"
"Maaa 100,172,220\nTa 236,17,49152\nZaaa 68,172,212\nTa 238,17,51200\nMU 52,48,50\nMaaa 68,168,208\nMU 53,49,50\nZaaa 36,168,200\nTa 240,18,32768\nCV 52,52,53\nMaaa 36,172,204\n"
"Ta 242,18,34816\nv_lshlrev_b32 v54, 16, v52\nZaaa 4,172,196\nD0 3,20,48,1024\nMaaa 4,168,192\nTa 244,18,49152\nv_and_b32 v55, 0xffff0000, v52\nZaaa 8,176,192\nTa 246,18,51200\n"
"MU 56,48,51\nMaaa 8,180,196\nMU 57,49,51\nZaaa 40,180,204\nTa 248,19,32768\nv_and_b32 v48, 0xffff0000, v32\nMaaa 40,176,200\nTa 250,19,34816\nZaaa 72,176,208\n"
"v_and_b32 v49, 0xffff0000, v36\nv_and_b32 v50, 0xffff0000, v40\nMaaa 72,180,212\nTa 252,19,49152\nv_and_b32 v51, 0xffff0000, v44\nZaaa 104,180,220\nTa 254,19,51200\n"
"Maaa 104,176,216\nD0 4,20,48,2048\nMU 52,48,50\nZaaa 108,184,216\nMU 53,49,50\nCV 52,52,53\nMaaa 108,188,220\nv_and_b32 v53, 0xffff0000, v52\nZaaa 76,188,212\n"
"v_lshlrev_b32 v52, 16, v52\nFM 56,48,51\nMaaa 76,184,208\nFM 57,49,51\nZaaa 44,184,200\nAD 54,54,52\nAD 55,55,53\nMaaa 44,188,204\nD0 5,20,48,3072\nv_lshlrev_b32 v48, 16, v33\n"
"Zaaa 12,188,196\nv_lshlrev_b32 v49, 16, v37\nMaaa 12,184,192\nWL 0\nMvaa 80,224,128\nTa 160,12,32768\nv_lshlrev_b32 v50, 16, v41\nMvaa 80,228,132\nTa 162,12,34816\n"
"Mvaa 112,228,140\ns_add_u32 m0, s47, -32768\ns_add_u32 s45, s45, 128\nMvaa 112,224,136\nTa 164,12,49152\nMvaa 144,224,144\nTa 166,12,51200\nv_lshlrev_b32 v51, 16, v45\n"
"Mvaa 144,228,148\nD0 2,20,45,0\nMU 52,48,50\nMvaa 176,228,156\nTa 168,13,32768\nMvaa 176,224,152\nTa 170,13,34816\nMU 53,49,50\nMvaa 180,232,152\nCV 52,52,53\n"
"v_and_b32 v53, 0xffff0000, v52\nMvaa 180,236,156\nTa 172,13,49152\nMvaa 148,236,148\nTa 174,13,51200\nv_lshlrev_b32 v52, 16, v52\nMvaa 148,232,144\nFM 56,48,51\nMvaa 116,232,136\n"
"Ta 176,14,32768\nFM 57,49,51\nMvaa 116,236,140\nTa 178,14,34816\nAD 54,54,52\nMvaa 84,236,132\nD0 3,20,45,1024\nMvaa 84,232,128\nTa 180,14,49152\nAD 55,55,53\nMvaa 88,240,128\n"
"Ta 182,14,51200\nv_and_b32 v48, 0xffff0000, v33\nMvaa 88,244,132\nv_and_b32 v49, 0xffff0000, v37\nMvaa 120,244,140\nTa 184,15,32768\nv_and_b32 v50, 0xffff0000, v41\n"
"Mvaa 120,240,136\nTa 186,15,34816\nMvaa 152,240,144\nv_and_b32 v51, 0xffff0000, v45\nMU 52,48,50\nMvaa 152,244,148\nTa 188,15,49152\nMU 53,49,50\nMvaa 184,244,156\n"
"Ta 190,15,51200\nMvaa 184,240,152\nD0 4,20,45,2048\nCV 52,52,53\nMvaa 188,248,152\nv_and_b32 v53, 0xffff0000, v52\nv_lshlrev_b32 v52, 16, v52\nMvaa 188,252,156\nFM 56,48,51\n"
"Mvaa 156,252,148\nFM 57,49,51\nAD 54,54,52\nMvaa 156,248,144\nAD 55,55,53\nMvaa 124,248,136\nv_lshlrev_b32 v48, 16, v34\nv_lshlrev_b32 v49, 16, v38\nMvaa 124,252,140\n"
"D0 5,20,45,3072\nv_lshlrev_b32 v50, 16, v42\nMvaa 92,252,132\nv_lshlrev_b32 v51, 16, v46\nMvaa 92,248,128\ns_waitcnt vmcnt(18) lgkmcnt(0)\ns_barrier\nMvaa 64,160,128\n"
"Ra 192,10,49152\nMU 52,48,50\nMvaa 64,164,132\ns_add_u32 m0, s47, 32768\ns_add_u32 s44, s44, 0x40000\nMvaa 96,164,140\nRa 196,11,49152\nMU 53,49,50\nMvaa 96,160,136\nCV 52,52,53\n"
"Mvaa 128,160,144\nRa 200,10,51200\nv_and_b32 v53, 0xffff0000, v52\nMvaa 128,164,148\nD0 6,24,44,0\nv_lshlrev_b32 v52, 16, v52\nMvaa 160,164,156\nRa 204,11,51200\nMvaa 160,160,152\n"
"FM 56,48,51\nFM 57,49,51\nMvaa 164,168,152\nRa 208,10,57344\nAD 54,54,52\nMvaa 164,172,156\nAD 55,55,53\nMvaa 132,172,148\nRa 212,11,57344\nv_and_b32 v48, 0xffff0000, v34\n"
"Mvaa 132,168,144\nv_and_b32 v49, 0xffff0000, v38\nMvaa 100,168,136\nRa 216,10,59392\nv_and_b32 v50, 0xffff0000, v42\nMvaa 100,172,140\nD0 7,24,44,1024\n"
"v_and_b32 v51, 0xffff0000, v46\nMvaa 68,172,132\nRa 220,11,59392\nMvaa 68,168,128\nMU 52,48,50\nMU 53,49,50\nMvaa 72,176,128\nCV 52,52,53\nv_and_b32 v53, 0xffff0000, v52\n"
"Mvaa 72,180,132\nv_lshlrev_b32 v52, 16, v52\nMvaa 104,180,140\nFM 56,48,51\nFM 57,49,51\nMvaa 104,176,136\nAD 54,54,52\nMvaa 136,176,144\nAD 55,55,53\nv_lshlrev_b32 v48, 16, v35\n"
"Mvaa 136,180,148\nD0 8,24,44,2048\nv_lshlrev_b32 v49, 16, v39\nMvaa 168,180,156\nv_lshlrev_b32 v50, 16, v43\nMvaa 168,176,152\nv_lshlrev_b32 v51, 16, v47\nMU 52,48,50\n"
"Mvaa 172,184,152\nMU 53,49,50\nCV 52,52,53\nMvaa 172,188,156\nv_and_b32 v53, 0xffff0000, v52\nMvaa 140,188,148\nv_lshlrev_b32 v52, 16, v52\nFM 56,48,51\nMvaa 140,184,144\n"
"FM 57,49,51\nMvaa 108,184,136\nAD 54,54,52\nAD 55,55,53\nMvaa 108,188,140\nD0 9,24,44,3072\nv_and_b32 v48, 0xffff0000, v35\nMvaa 76,188,132\nv_and_b32 v49, 0xffff0000, v39\n"
"Mvaa 76,184,128\nWL 0\nMaaa 0,160,192\nRa 128,10,0\nv_and_b32 v50, 0xffff0000, v43\nMaaa 0,164,196\ns_add_u32 m0, s47, 49152\ns_add_u32 s44, s44, 0x40000\nMaaa 32,164,204\n"
"Ra 132,11,0\nv_and_b32 v51, 0xffff0000, v47\nMaaa 32,160,200\nMU 52,48,50\nMaaa 64,160,208\nRa 136,10,2048\nMU 53,49,50\nMaaa 64,164,212\nD0 6,24,44,0\nCV 52,52,53\n"
"Maaa 96,164,220\nRa 140,11,2048\nMaaa 96,160,216\nv_and_b32 v53, 0xffff0000, v52\nv_lshlrev_b32 v52, 16, v52\nMaaa 100,168,216\nRa 144,10,8192\nFM 56,48,51\nMaaa 100,172,220\n"
"FM 57,49,51\n.W571:\ns_cbranch_execz .W575\nMaaa 68,172,212\nRa 148,11,8192\nAD 54,54,52\nMaaa 68,168,208\nAD 55,55,53\nMaaa 36,168,200\nRa 152,10,10240\n"
"v_permlane32_swap_b32 v54, v55\nMaaa 36,172,204\nD0 7,24,44,1024\nv_permlane32_swap_b32 v56, v57\nMaaa 4,172,196\nRa 156,11,10240\nMaaa 4,168,192\nAD 54,54,55\nAD 56,56,57\n"
"Maaa 8,176,192\nMaaa 8,180,196\nv_permlane16_swap_b32 v54, v56\nMaaa 40,180,204\nAD 54,54,56\nMaaa 40,176,200\nMaaa 72,176,208\n"
"v_add_f32_dpp v54, v54, v54 row_ror:8 row_mask:0xf bank_mask:0xf\nMaaa 72,180,212\nD0 8,24,44,2048\nMaaa 104,180,220\n"
"v_add_f32_dpp v54, v54, v54 row_ror:4 row_mask:0xf bank_mask:0xf\nMaaa 104,176,216\nMaaa 108,184,216\nv_add_f32_dpp v54, v54, v54 row_ror:2 row_mask:0xf bank_mask:0xf\n"
"Maaa 108,188,220\nMaaa 76,188,212\nv_add_f32_dpp v54, v54, v54 row_ror:1 row_mask:0xf bank_mask:0xf\nCV 54,54,54\nMaaa 76,184,208\nv_lshlrev_b32 v54, 16, v54\nMaaa 44,184,200\n"
"v_mul_f32 v55, 0xbfb8aa3b, v54\nMaaa 44,188,204\nD0 9,24,44,3072\nMaaa 12,188,196\nEX 55,55\nMaaa 12,184,192\ns_waitcnt vmcnt(12) lgkmcnt(0)\ns_barrier\nMaaa 16,224,192\n"
"Ta 160,12,0\nv_add_f32 v56, 0, v54\nMaaa 16,228,196\nTa 162,12,2048\nMaaa 48,228,204\ns_add_u32 m0, s47, -16384\ns_add_u32 s48, s48, 128\nMaaa 48,224,200\nTa 164,12,16384\n"
"Maaa 80,224,208\nTa 166,12,18432\nbuffer_load_dword v31, v24, s[24:27], s96 offen\ns_add_u32 s96, s96, 0x80000\nMaaa 80,228,212\nD0 2,20,48,0\nv_add_f32 v55, 1.0, v55\n"
"Maaa 112,228,220\nTa 168,13,0\nv_rcp_f32 v55, v55\nMaaa 112,224,216\nTa 170,13,2048\nv_ashrrev_i32 v57, 31, v56\nMaaa 116,232,216\nCV 55,55,55\nv_lshlrev_b32 v55, 16, v55\n"
"Maaa 116,236,220\nTa 172,13,16384\nMaaa 84,236,212\nTa 174,13,18432\nv_or_b32 v57, 0x80000000, v57\nMaaa 84,232,208\nv_xor_b32 v56, v56, v57\nMaaa 52,232,200\nTa 176,14,0\n"
"v_and_b32 v56, 0xffff0000, v56\nMaaa 52,236,204\nTa 178,14,2048\ns_lshl2_add_u32 s2, s10, s11\nMaaa 20,236,196\nD0 3,20,48,1024\nMaaa 20,232,192\nTa 180,14,16384\n"
"v_lshrrev_b32 v48, 5, v0\nMaaa 24,240,192\nTa 182,14,18432\nv_lshl_add_u32 v48, s2, 1, v48\nMaaa 24,244,196\nv_sub_u32 v49, 0xffff, v48\nMaaa 56,244,204\nTa 184,15,0\n"
"v_or_b32 v56, v56, v49\nMaaa 56,240,200\nTa 186,15,2048\nMaaa 88,240,208\nv_mov_b32 v57, s15\nv_and_b32 v49, 31, v0\nMaaa 88,244,212\nTa 188,15,16384\nv_mov_b32 v52, 0x40000000\n"
"Maaa 120,244,220\nTa 190,15,18432\nv_cmp_eq_u32 vcc, 16, v49\nv_lshlrev_b32 v50, 2, v48\nv_lshlrev_b32 v51, 10, v49\nv_cndmask_b32 v50, v52, v50, vcc\n"
"v_cndmask_b32 v56, v55, v56, vcc\nMaaa 120,240,216\nD0 4,20,48,2048\nv_lshl_add_u32 v51, v48, 3, v51\nMaaa 124,248,216\nv_and_b32 v49, 15, v0\nv_cmp_eq_u32 vcc, 0, v49\n"
"s_mov_b32 s2, 0xc040000\ns_mov_b32 s3, 0xc03e000\nv_cndmask_b32 v51, v52, v51, vcc\nbuffer_store_dwordx2 v[56:57], v51, s[16:19], s2 offen sc0 sc1\n"
"buffer_store_dword v54, v50, s[16:19], s3 offen sc0 sc1\nMaaa 124,252,220\nMaaa 92,252,212\nMaaa 92,248,208\nMaaa 60,248,200\nMaaa 60,252,204\nD0 5,20,48,3072\nMaaa 28,252,196\n"
"Maaa 28,248,192\nWL 0\ns_mov_b32 s50, 3\n.GL273:\nMvaa 64,160,128\nTa 224,16,0\nMvaa 64,164,132\nTa 226,16,2048\nMvaa 96,164,140\nTa 228,16,16384\ns_add_u32 m0, s47, -65536\n"
"s_add_u32 s45, s45, 128\nMvaa 96,160,136\nTa 230,16,18432\nMvaa 128,160,144\nTa 232,17,0\nMvaa 128,164,148\nTa 234,17,2048\nD0 2,20,45,0\nMvaa 160,164,156\nTa 236,17,16384\n"
"Mvaa 160,160,152\nTa 238,17,18432\nMvaa 164,168,152\nTa 240,18,0\nMvaa 164,172,156\nTa 242,18,2048\nMvaa 132,172,148\nTa 244,18,16384\nMvaa 132,168,144\nTa 246,18,18432\n"
"Mvaa 100,168,136\nTa 248,19,0\nMvaa 100,172,140\nTa 250,19,2048\nMvaa 68,172,132\nTa 252,19,16384\nD0 3,20,45,1024\nMvaa 68,168,128\nTa 254,19,18432\nMvaa 72,176,128\n"
"Ra 192,10,16384\nMvaa 72,180,132\nRa 196,11,16384\nMvaa 104,180,140\nRa 200,10,18432\nMvaa 104,176,136\nRa 204,11,18432\nMvaa 136,176,144\nRa 208,10,24576\nMvaa 136,180,148\n"
"Ra 212,11,24576\nMvaa 168,180,156\nRa 216,10,26624\nMvaa 168,176,152\nRa 220,11,26624\nD0 4,20,45,2048\nMvaa 172,184,152\nMvaa 172,188,156\nMvaa 140,188,148\nMvaa 140,184,144\n"
"Mvaa 108,184,136\nMvaa 108,188,140\nD0 5,20,45,3072\nMvaa 76,188,132\nMvaa 76,184,128\ns_waitcnt vmcnt(18) lgkmcnt(8)\ns_barrier\nMvaa 80,224,128\nMvaa 80,228,132\n"
"s_add_u32 m0, s47, 0\ns_add_u32 s44, s44, 0x40000\ns_mov_b32 s86, 1\ns_load_dword s86, s[54:55], s94\nMvaa 112,228,140\ns_add_u32 s94, s94, 1024\nMvaa 112,224,136\n"
"Mvaa 144,224,144\nMvaa 144,228,148\nD0 6,24,44,0\nMvaa 176,228,156\nMvaa 176,224,152\nMvaa 180,232,152\nMvaa 180,236,156\nMvaa 148,236,148\nMvaa 148,232,144\n"
"s_add_u32 s88, s88, s86\nMvaa 116,232,136\nMvaa 116,236,140\nD0 7,24,44,1024\ns_add_u32 s88, s88, s86\nMvaa 84,236,132\nMvaa 84,232,128\ns_add_u32 s88, s88, s86\nMvaa 88,240,128\n"
"Mvaa 88,244,132\ns_add_u32 s88, s88, s86\nMvaa 120,244,140\nMvaa 120,240,136\ns_add_u32 s88, s88, s86\nMvaa 152,240,144\nMvaa 152,244,148\nD0 8,24,44,2048\n"
"s_add_u32 s88, s88, s86\nMvaa 184,244,156\nMvaa 184,240,152\ns_add_u32 s88, s88, s86\nMvaa 188,248,152\nMvaa 188,252,156\ns_add_u32 s88, s88, s86\nMvaa 156,252,148\n"
"Mvaa 156,248,144\ns_add_u32 s88, s88, s86\nMvaa 124,248,136\nMvaa 124,252,140\nD0 9,24,44,3072\ns_add_u32 s88, s88, s86\nMvaa 92,252,132\nMvaa 92,248,128\nWL 0\n"
"s_add_u32 s88, s88, s86\nMaaa 16,224,192\nRa 128,10,32768\nMaaa 16,228,196\ns_add_u32 m0, s47, 16384\ns_add_u32 s44, s44, 0x40000\ns_add_u32 s88, s88, s86\nMaaa 48,228,204\n"
"Ra 132,11,32768\nMaaa 48,224,200\ns_add_u32 s88, s88, s86\nMaaa 80,224,208\nRa 136,10,34816\nMaaa 80,228,212\nD0 6,24,44,0\ns_add_u32 s88, s88, s86\nMaaa 112,228,220\n"
"Ra 140,11,34816\nMaaa 112,224,216\ns_add_u32 s88, s88, s86\nMaaa 116,232,216\nRa 144,10,40960\nMaaa 116,236,220\ns_add_u32 s88, s88, s86\nMaaa 84,236,212\nRa 148,11,40960\n"
"Maaa 84,232,208\nMaaa 52,232,200\nRa 152,10,43008\nMaaa 52,236,204\nD0 7,24,44,1024\nMaaa 20,236,196\nRa 156,11,43008\nMaaa 20,232,192\nMaaa 24,240,192\nMaaa 24,244,196\n"
"Maaa 56,244,204\nMaaa 56,240,200\nMaaa 88,240,208\nMaaa 88,244,212\nD0 8,24,44,2048\nMaaa 120,244,220\nMaaa 120,240,216\nMaaa 124,248,216\nMaaa 124,252,220\nMaaa 92,252,212\n"
"Maaa 92,248,208\nMaaa 60,248,200\nMaaa 60,252,204\nD0 9,24,44,3072\nMaaa 28,252,196\nMaaa 28,248,192\ns_waitcnt vmcnt(12) lgkmcnt(0)\ns_barrier\nMaaa 0,160,192\nTa 224,16,32768\n"
"s_mov_b32 m0, s76\ns_add_u32 s76, s76, 1024\nMaaa 0,164,196\nTa 226,16,34816\nD0 1,36,75,0\ns_add_u32 s75, s75, 1024\nMaaa 32,164,204\ns_add_u32 m0, s47, -49152\n"
"s_add_u32 s48, s48, 128\nMaaa 32,160,200\nTa 228,16,49152\nMaaa 64,160,208\nTa 230,16,51200\nbuffer_load_dword v31, v24, s[24:27], s96 offen\ns_add_u32 s96, s96, 0x80000\n"
"Maaa 64,164,212\nD0 2,20,48,0\nMaaa 96,164,220\nTa 232,17,32768\nMaaa 96,160,216\nTa 234,17,34816\nMaaa 100,168,216\nMaaa 100,172,220\nTa 236,17,49152\nMaaa 68,172,212\n"
"Ta 238,17,51200\nMaaa 68,168,208\nMaaa 36,168,200\nTa 240,18,32768\nMaaa 36,172,204\nTa 242,18,34816\nMaaa 4,172,196\nD0 3,20,48,1024\nMaaa 4,168,192\nTa 244,18,49152\n"
"Maaa 8,176,192\nTa 246,18,51200\nMaaa 8,180,196\nMaaa 40,180,204\nTa 248,19,32768\nMaaa 40,176,200\nTa 250,19,34816\nMaaa 72,176,208\nMaaa 72,180,212\nTa 252,19,49152\n"
"Maaa 104,180,220\nTa 254,19,51200\nMaaa 104,176,216\nD0 4,20,48,2048\nMaaa 108,184,216\nMaaa 108,188,220\nMaaa 76,188,212\nMaaa 76,184,208\nMaaa 44,184,200\nMaaa 44,188,204\n"
"D0 5,20,48,3072\nMaaa 12,188,196\nMaaa 12,184,192\nWL 0\nMvaa 80,224,128\nTa 160,12,32768\nMvaa 80,228,132\nTa 162,12,34816\nMvaa 112,228,140\nTa 164,12,49152\n"
"s_add_u32 m0, s47, -32768\ns_add_u32 s45, s45, 128\nMvaa 112,224,136\nTa 166,12,51200\nMvaa 144,224,144\nTa 168,13,32768\nMvaa 144,228,148\nTa 170,13,34816\nD0 2,20,45,0\n"
"Mvaa 176,228,156\nTa 172,13,49152\nMvaa 176,224,152\nTa 174,13,51200\nMvaa 180,232,152\nTa 176,14,32768\nMvaa 180,236,156\nTa 178,14,34816\nMvaa 148,236,148\nTa 180,14,49152\n"
"Mvaa 148,232,144\nTa 182,14,51200\nMvaa 116,232,136\nTa 184,15,32768\nMvaa 116,236,140\nTa 186,15,34816\nMvaa 84,236,132\nTa 188,15,49152\nD0 3,20,45,1024\nMvaa 84,232,128\n"
"Ta 190,15,51200\nMvaa 88,240,128\nRa 192,10,49152\nMvaa 88,244,132\nRa 196,11,49152\nMvaa 120,244,140\nRa 200,10,51200\nMvaa 120,240,136\nRa 204,11,51200\nMvaa 152,240,144\n"
"Ra 208,10,57344\nMvaa 152,244,148\nRa 212,11,57344\nMvaa 184,244,156\nRa 216,10,59392\nMvaa 184,240,152\nRa 220,11,59392\nD0 4,20,45,2048\nMvaa 188,248,152\nMvaa 188,252,156\n"
"Mvaa 156,252,148\nMvaa 156,248,144\nMvaa 124,248,136\nMvaa 124,252,140\nD0 5,20,45,3072\nMvaa 92,252,132\nMvaa 92,248,128\ns_waitcnt vmcnt(18) lgkmcnt(8)\ns_barrier\n"
"Mvaa 64,160,128\nMvaa 64,164,132\ns_add_u32 m0, s47, 32768\ns_add_u32 s44, s44, 0x40000\ns_mov_b32 s86, 1\ns_load_dword s86, s[54:55], s95\nMvaa 96,164,140\n"
"s_add_u32 s95, s95, 1024\nMvaa 96,160,136\nMvaa 128,160,144\nMvaa 128,164,148\nD0 6,24,44,0\nMvaa 160,164,156\nMvaa 160,160,152\nMvaa 164,168,152\nMvaa 164,172,156\n"
"Mvaa 132,172,148\nMvaa 132,168,144\ns_sub_u32 s88, s88, s86\nMvaa 100,168,136\nMvaa 100,172,140\nD0 7,24,44,1024\ns_sub_u32 s88, s88, s86\nMvaa 68,172,132\nMvaa 68,168,128\n"
"s_sub_u32 s88, s88, s86\nMvaa 72,176,128\nMvaa 72,180,132\ns_sub_u32 s88, s88, s86\nMvaa 104,180,140\nMvaa 104,176,136\ns_sub_u32 s88, s88, s86\nMvaa 136,176,144\n"
"Mvaa 136,180,148\nD0 8,24,44,2048\ns_sub_u32 s88, s88, s86\nMvaa 168,180,156\nMvaa 168,176,152\ns_sub_u32 s88, s88, s86\nMvaa 172,184,152\nMvaa 172,188,156\n"
"s_sub_u32 s88, s88, s86\nMvaa 140,188,148\nMvaa 140,184,144\ns_sub_u32 s88, s88, s86\nMvaa 108,184,136\nMvaa 108,188,140\nD0 9,24,44,3072\ns_sub_u32 s88, s88, s86\n"
"Mvaa 76,188,132\nMvaa 76,184,128\nWL 0\ns_sub_u32 s88, s88, s86\nMaaa 0,160,192\nRa 128,10,0\nMaaa 0,164,196\ns_add_u32 m0, s47, 49152\ns_add_u32 s44, s44, 0x40000\n"
"s_sub_u32 s88, s88, s86\nMaaa 32,164,204\nRa 132,11,0\nMaaa 32,160,200\ns_sub_u32 s88, s88, s86\nMaaa 64,160,208\nRa 136,10,2048\nMaaa 64,164,212\nD0 6,24,44,0\n"
"s_sub_u32 s88, s88, s86\nMaaa 96,164,220\nRa 140,11,2048\nMaaa 96,160,216\ns_sub_u32 s88, s88, s86\nMaaa 100,168,216\nRa 144,10,8192\nMaaa 100,172,220\ns_sub_u32 s88, s88, s86\n"
"Maaa 68,172,212\nRa 148,11,8192\nMaaa 68,168,208\nMaaa 36,168,200\nRa 152,10,10240\nMaaa 36,172,204\nD0 7,24,44,1024\nMaaa 4,172,196\nRa 156,11,10240\nMaaa 4,168,192\n"
"Maaa 8,176,192\nMaaa 8,180,196\nMaaa 40,180,204\nMaaa 40,176,200\nMaaa 72,176,208\nMaaa 72,180,212\nD0 8,24,44,2048\nMaaa 104,180,220\nMaaa 104,176,216\nMaaa 108,184,216\n"
"Maaa 108,188,220\nMaaa 76,188,212\nMaaa 76,184,208\nMaaa 44,184,200\nMaaa 44,188,204\nD0 9,24,44,3072\nMaaa 12,188,196\nMaaa 12,184,192\ns_waitcnt vmcnt(12) lgkmcnt(0)\ns_barrier\n"
"Maaa 16,224,192\nTa 160,12,0\ns_mov_b32 m0, s76\ns_add_u32 s76, s76, 1024\nMaaa 16,228,196\nTa 162,12,2048\nD0 1,36,75,0\ns_add_u32 s75, s75, 1024\nMaaa 48,228,204\n"
"s_add_u32 m0, s47, -16384\ns_add_u32 s48, s48, 128\nMaaa 48,224,200\nTa 164,12,16384\nMaaa 80,224,208\nTa 166,12,18432\nbuffer_load_dword v31, v24, s[24:27], s96 offen\n"
"s_add_u32 s96, s96, 0x80000\nMaaa 80,228,212\n.W572:\ns_cbranch_execz .W576\nD0 2,20,48,0\nMaaa 112,228,220\nTa 168,13,0\nMaaa 112,224,216\nTa 170,13,2048\nMaaa 116,232,216\n"
"Maaa 116,236,220\nTa 172,13,16384\nMaaa 84,236,212\nTa 174,13,18432\nMaaa 84,232,208\nMaaa 52,232,200\nTa 176,14,0\nMaaa 52,236,204\nTa 178,14,2048\nMaaa 20,236,196\n"
"D0 3,20,48,1024\nMaaa 20,232,192\nTa 180,14,16384\nMaaa 24,240,192\nTa 182,14,18432\nMaaa 24,244,196\nMaaa 56,244,204\nTa 184,15,0\nMaaa 56,240,200\nTa 186,15,2048\n"
"Maaa 88,240,208\nMaaa 88,244,212\nTa 188,15,16384\nMaaa 120,244,220\nTa 190,15,18432\nMaaa 120,240,216\nD0 4,20,48,2048\nMaaa 124,248,216\nMaaa 124,252,220\nMaaa 92,252,212\n"
"Maaa 92,248,208\nMaaa 60,248,200\ns_xor_b32 s94, s94, 0x2000\ns_xor_b32 s95, s95, 0x2000\ns_sub_u32 s88, 0, s88\nMaaa 60,252,204\nD0 5,20,48,3072\ns_sub_u32 s50, s50, 1\n"
"Maaa 28,252,196\nMaaa 28,248,192\nWL 0\ns_cbranch_scc0 .GL273\nMvaa 64,160,128\nTa 224,16,0\nMvaa 64,164,132\nTa 226,16,2048\ns_cmp_gt_i32 s88, 0\ns_cselect_b32 s88, 0x2000, 0\n"
"Mvaa 96,164,140\ns_add_u32 m0, s47, -65536\ns_add_u32 s45, s45, 128\nMvaa 96,160,136\nTa 228,16,16384\nMvaa 128,160,144\nTa 230,16,18432\nMvaa 128,164,148\nD0 2,20,45,0\n"
"Mvaa 160,164,156\nTa 232,17,0\nMvaa 160,160,152\nTa 234,17,2048\nMvaa 164,168,152\ns_xor_b32 s88, s88, s92\nMvaa 164,172,156\nTa 236,17,16384\nMvaa 132,172,148\nTa 238,17,18432\n"
"Mvaa 132,168,144\nv_xor_b32 v27, s88, v25\nMvaa 100,168,136\nTa 240,18,0\nMvaa 100,172,140\nTa 242,18,2048\nMvaa 68,172,132\nD0 3,20,45,1024\nMvaa 68,168,128\nTa 244,18,16384\n"
"Mvaa 72,176,128\nTa 246,18,18432\nMvaa 72,180,132\nbuffer_store_dword v27, off, s[16:19], s93 sc0 sc1\nMvaa 104,180,140\nTa 248,19,0\nMvaa 104,176,136\nTa 250,19,2048\n"
"Mvaa 136,176,144\nMvaa 136,180,148\nTa 252,19,16384\nMvaa 168,180,156\nTa 254,19,18432\nMvaa 168,176,152\nD0 4,20,45,2048\nMvaa 172,184,152\nMvaa 172,188,156\nMvaa 140,188,148\n"
"Mvaa 140,184,144\nMvaa 108,184,136\nMvaa 108,188,140\nD0 5,20,45,3072\nMvaa 76,188,132\nMvaa 76,184,128\ns_waitcnt vmcnt(19) lgkmcnt(0)\ns_barrier\nMvaa 80,224,128\n"
"Ra 192,10,16384\nMvaa 80,228,132\ns_add_u32 m0, s47, 0\ns_add_u32 s44, s44, 0x40000\nMvaa 112,228,140\nRa 196,11,16384\nMvaa 112,224,136\nMvaa 144,224,144\nRa 200,10,18432\n"
"Mvaa 144,228,148\nD0 6,24,44,0\nMvaa 176,228,156\nRa 204,11,18432\nMvaa 176,224,152\nMvaa 180,232,152\nRa 208,10,24576\nMvaa 180,236,156\nMvaa 148,236,148\nRa 212,11,24576\n"
"Mvaa 148,232,144\nMvaa 116,232,136\nRa 216,10,26624\nMvaa 116,236,140\nD0 7,24,44,1024\nMvaa 84,236,132\nRa 220,11,26624\nMvaa 84,232,128\nMvaa 88,240,128\nMvaa 88,244,132\n"
"Mvaa 120,244,140\nMvaa 120,240,136\nMvaa 152,240,144\nMvaa 152,244,148\nD0 8,24,44,2048\nMvaa 184,244,156\nMvaa 184,240,152\nMvaa 188,248,152\nMvaa 188,252,156\nMvaa 156,252,148\n"
"Mvaa 156,248,144\nMvaa 124,248,136\nMvaa 124,252,140\nD0 9,24,44,3072\nMvaa 92,252,132\nMvaa 92,248,128\nWL 0\nMaaa 16,224,192\nRa 128,10,32768\nMaaa 16,228,196\n"
"s_add_u32 m0, s47, 16384\ns_add_u32 s44, s44, 0x40000\nMaaa 48,228,204\nRa 132,11,32768\nMaaa 48,224,200\nMaaa 80,224,208\nRa 136,10,34816\nMaaa 80,228,212\nD0 6,24,44,0\n"
"Maaa 112,228,220\nRa 140,11,34816\nMaaa 112,224,216\nMaaa 116,232,216\nRa 144,10,40960\nMaaa 116,236,220\nMaaa 84,236,212\nRa 148,11,40960\nMaaa 84,232,208\nMaaa 52,232,200\n"
"Ra 152,10,43008\nMaaa 52,236,204\nD0 7,24,44,1024\nMaaa 20,236,196\nRa 156,11,43008\nMaaa 20,232,192\nMaaa 24,240,192\nMaaa 24,244,196\nMaaa 56,244,204\nMaaa 56,240,200\n"
"Maaa 88,240,208\nMaaa 88,244,212\nD0 8,24,44,2048\nMaaa 120,244,220\nMaaa 120,240,216\nMaaa 124,248,216\nMaaa 124,252,220\nMaaa 92,252,212\nMaaa 92,248,208\nMaaa 60,248,200\n"
"Maaa 60,252,204\nD0 9,24,44,3072\nMaaa 28,252,196\nMaaa 28,248,192\ns_waitcnt vmcnt(19) lgkmcnt(0)\ns_barrier\nMaaa 0,160,192\nTa 224,16,32768\nMaaa 0,164,196\nTa 226,16,34816\n"
"Maaa 32,164,204\ns_add_u32 m0, s47, -49152\ns_add_u32 s48, s48, 128\nMaaa 32,160,200\nTa 228,16,49152\nMaaa 64,160,208\nTa 230,16,51200\n"
"buffer_load_dword v31, v24, s[24:27], s96 offen\ns_add_u32 s96, s96, 0x80000\nMaaa 64,164,212\nD0 2,20,48,0\nMaaa 96,164,220\nTa 232,17,32768\nMaaa 96,160,216\nTa 234,17,34816\n"
"Maaa 100,168,216\nMaaa 100,172,220\nTa 236,17,49152\nMaaa 68,172,212\nTa 238,17,51200\nMaaa 68,168,208\nMaaa 36,168,200\nTa 240,18,32768\nMaaa 36,172,204\nTa 242,18,34816\n"
"Maaa 4,172,196\nD0 3,20,48,1024\nMaaa 4,168,192\nTa 244,18,49152\nMaaa 8,176,192\nTa 246,18,51200\nMaaa 8,180,196\nMaaa 40,180,204\nTa 248,19,32768\nMaaa 40,176,200\n"
"Ta 250,19,34816\nMaaa 72,176,208\nMaaa 72,180,212\nTa 252,19,49152\nMaaa 104,180,220\nTa 254,19,51200\nMaaa 104,176,216\nD0 4,20,48,2048\nMaaa 108,184,216\nMaaa 108,188,220\n"
"Maaa 76,188,212\nMaaa 76,184,208\nMaaa 44,184,200\nMaaa 44,188,204\nD0 5,20,48,3072\nMaaa 12,188,196\nMaaa 12,184,192\nWL 0\nMvaa 80,224,128\nTa 160,12,32768\nMvaa 80,228,132\n"
"Ta 162,12,34816\nMvaa 112,228,140\ns_add_u32 m0, s47, -32768\ns_add_u32 s45, s45, 128\nMvaa 112,224,136\nTa 164,12,49152\nMvaa 144,224,144\nTa 166,12,51200\nMvaa 144,228,148\n"
"D0 2,20,45,0\nMvaa 176,228,156\nTa 168,13,32768\nMvaa 176,224,152\nTa 170,13,34816\nMvaa 180,232,152\nMvaa 180,236,156\nTa 172,13,49152\nMvaa 148,236,148\nTa 174,13,51200\n"
"Mvaa 148,232,144\nMvaa 116,232,136\nTa 176,14,32768\nMvaa 116,236,140\nTa 178,14,34816\nMvaa 84,236,132\nD0 3,20,45,1024\nMvaa 84,232,128\nTa 180,14,49152\nMvaa 88,240,128\n"
"Ta 182,14,51200\nMvaa 88,244,132\nMvaa 120,244,140\nTa 184,15,32768\nMvaa 120,240,136\nTa 186,15,34816\nMvaa 152,240,144\nMvaa 152,244,148\nTa 188,15,49152\nMvaa 184,244,156\n"
"Ta 190,15,51200\nMvaa 184,240,152\nD0 4,20,45,2048\nMvaa 188,248,152\nMvaa 188,252,156\nMvaa 156,252,148\nMvaa 156,248,144\nMvaa 124,248,136\nMvaa 124,252,140\nD0 5,20,45,3072\n"
"Mvaa 92,252,132\nMvaa 92,248,128\ns_waitcnt vmcnt(17) lgkmcnt(0)\ns_barrier\nMvaa 64,160,128\nRa 192,10,49152\nMvaa 64,164,132\ns_add_u32 m0, s47, 32768\n"
"s_add_u32 s44, s44, 0x40000\nMvaa 96,164,140\nRa 196,11,49152\nMvaa 96,160,136\nMvaa 128,160,144\nRa 200,10,51200\nMvaa 128,164,148\nD0 6,24,44,0\nMvaa 160,164,156\n"
"Ra 204,11,51200\nMvaa 160,160,152\nMvaa 164,168,152\nRa 208,10,57344\nMvaa 164,172,156\ns_lshl_b32 s5, s73, 8\ns_add_u32 s5, s5, 0xc04ac00\nv_lshrrev_b32 v27, 2, v1\n"
"buffer_load_dword v27, v27, s[16:19], s5 offen sc0 sc1\nMvaa 132,172,148\nRa 212,11,57344\nMvaa 132,168,144\nMvaa 100,168,136\nRa 216,10,59392\nMvaa 100,172,140\nD0 7,24,44,1024\n"
"Mvaa 68,172,132\nRa 220,11,59392\nMvaa 68,168,128\nMvaa 72,176,128\nMvaa 72,180,132\nMvaa 104,180,140\nMvaa 104,176,136\nMvaa 136,176,144\nMvaa 136,180,148\nD0 8,24,44,2048\n"
"Mvaa 168,180,156\nMvaa 168,176,152\nMvaa 172,184,152\nMvaa 172,188,156\nMvaa 140,188,148\nMvaa 140,184,144\nMvaa 108,184,136\nMvaa 108,188,140\nD0 9,24,44,3072\nMvaa 76,188,132\n"
"Mvaa 76,184,128\nWL 0\nMaaa 0,160,192\nRa 128,10,0\nMaaa 0,164,196\ns_add_u32 m0, s47, 49152\ns_add_u32 s44, s44, 0x40000\nMaaa 32,164,204\nRa 132,11,0\nMaaa 32,160,200\n"
"Maaa 64,160,208\nRa 136,10,2048\nMaaa 64,164,212\nD0 6,24,44,0\nMaaa 96,164,220\nRa 140,11,2048\nMaaa 96,160,216\nMaaa 100,168,216\nRa 144,10,8192\nMaaa 100,172,220\n"
"Maaa 68,172,212\nRa 148,11,8192\nMaaa 68,168,208\nMaaa 36,168,200\nRa 152,10,10240\nMaaa 36,172,204\nD0 7,24,44,1024\nMaaa 4,172,196\nRa 156,11,10240\nMaaa 4,168,192\n"
"Maaa 8,176,192\nMaaa 8,180,196\nMaaa 40,180,204\nMaaa 40,176,200\nMaaa 72,176,208\nMaaa 72,180,212\nD0 8,24,44,2048\nMaaa 104,180,220\nMaaa 104,176,216\nMaaa 108,184,216\n"
"Maaa 108,188,220\nMaaa 76,188,212\nMaaa 76,184,208\nMaaa 44,184,200\nMaaa 44,188,204\nD0 9,24,44,3072\nMaaa 12,188,196\nMaaa 12,184,192\ns_waitcnt vmcnt(18) lgkmcnt(0)\ns_barrier\n"
"Maaa 16,224,192\nTa 160,12,0\nMaaa 16,228,196\nTa 162,12,2048\nMaaa 48,228,204\ns_add_u32 m0, s47, -16384\ns_add_u32 s48, s48, 128\nMaaa 48,224,200\nTa 164,12,16384\n"
"Maaa 80,224,208\nTa 166,12,18432\nbuffer_load_dword v31, v24, s[24:27], s96 offen\ns_add_u32 s96, s96, 0x80000\nMaaa 80,228,212\nD0 2,20,48,0\nMaaa 112,228,220\nTa 168,13,0\n"
"Maaa 112,224,216\nTa 170,13,2048\nMaaa 116,232,216\nMaaa 116,236,220\nTa 172,13,16384\nMaaa 84,236,212\nTa 174,13,18432\nMaaa 84,232,208\nMaaa 52,232,200\nTa 176,14,0\n"
"Maaa 52,236,204\nTa 178,14,2048\nMaaa 20,236,196\nD0 3,20,48,1024\nMaaa 20,232,192\nTa 180,14,16384\nMaaa 24,240,192\nTa 182,14,18432\nMaaa 24,244,196\nMaaa 56,244,204\n"
"Ta 184,15,0\nMaaa 56,240,200\nTa 186,15,2048\nMaaa 88,240,208\nMaaa 88,244,212\nTa 188,15,16384\nMaaa 120,244,220\nTa 190,15,18432\nMaaa 120,240,216\nD0 4,20,48,2048\n"
"Maaa 124,248,216\nMaaa 124,252,220\nMaaa 92,252,212\nMaaa 92,248,208\nMaaa 60,248,200\nMaaa 60,252,204\nD0 5,20,48,3072\nMaaa 28,252,196\nMaaa 28,248,192\nWL 0\nMvaa 64,160,128\n"
"Ta 224,16,0\nWV 12\nv_xor_b32 v27, s15, v27\nv_and_b32 v32, 0xffffdfff, v27\nv_cmp_ne_u32 vcc, 0, v32\ns_nop 0\ns_cbranch_vccnz .S270\n.S271:\nv_cmp_ne_u32 vcc, 0, v27\ns_nop 3\n"
"s_bcnt1_i32_b64 s4, vcc\ns_cmp_gt_u32 s4, 32\ns_cselect_b32 s4, 1, 0\nMvaa 64,164,132\nTa 226,16,2048\nMvaa 96,164,140\ns_add_u32 m0, s47, -65536\ns_add_u32 s45, s45, 128\n"
"Mvaa 96,160,136\nTa 228,16,16384\nMvaa 128,160,144\nTa 230,16,18432\nMvaa 128,164,148\nD0 2,20,45,0\nMvaa 160,164,156\nTa 232,17,0\nMvaa 160,160,152\nTa 234,17,2048\n"
"Mvaa 164,168,152\ns_xor_b32 s4, s4, s12\nMvaa 164,172,156\nTa 236,17,16384\nMvaa 132,172,148\nTa 238,17,18432\nMvaa 132,168,144\ns_and_b32 s4, s4, 1\nMvaa 100,168,136\n"
"Ta 240,18,0\nMvaa 100,172,140\nTa 242,18,2048\nMvaa 68,172,132\nD0 3,20,45,1024\nMvaa 68,168,128\nTa 244,18,16384\nMvaa 72,176,128\nTa 246,18,18432\nMvaa 72,180,132\n"
"s_lshl_b32 s4, s4, 13\nMvaa 104,180,140\nTa 248,19,0\nMvaa 104,176,136\nTa 250,19,2048\nMvaa 136,176,144\ns_mul_i32 s72, s73, 25165824\nMvaa 136,180,148\nTa 252,19,16384\n.W573:\n"
"s_cbranch_execz .W577\nMvaa 168,180,156\nTa 254,19,18432\nMvaa 168,176,152\nD0 4,20,45,2048\nMvaa 172,184,152\ns_add_u32 s72, s72, s4\nMvaa 172,188,156\ns_and_b32 s4, s12, 1\n"
"Mvaa 140,188,148\ns_lshl_b32 s4, s4, 21\nMvaa 140,184,144\ns_add_u32 s72, s72, s4\nMvaa 108,184,136\ns_lshl3_add_u32 s2, s13, s11\nMvaa 108,188,140\nD0 5,20,45,3072\n"
"Mvaa 76,188,132\ns_lshl_b32 s2, s2, 11\nMvaa 76,184,128\nWL 0\ns_barrier\nMvaa 80,224,128\nRa 192,10,16384\nMvaa 80,228,132\ns_add_u32 m0, s47, 0\ns_add_u32 s44, s44, 0x40000\n"
"Mvaa 112,228,140\nRa 196,11,16384\nMvaa 112,224,136\ns_lshl_b32 s3, s14, 22\nMvaa 144,224,144\nRa 200,10,18432\nMvaa 144,228,148\nD0 6,24,44,0\nMvaa 176,228,156\nRa 204,11,18432\n"
"Mvaa 176,224,152\ns_add_u32 s2, s2, s3\nMvaa 180,232,152\nRa 208,10,24576\nMvaa 180,236,156\ns_add_u32 s2, s2, s72\nMvaa 148,236,148\nRa 212,11,24576\nMvaa 148,232,144\n"
"s_add_u32 s49, s2, 0xc00000\nMvaa 116,232,136\nRa 216,10,26624\nMvaa 116,236,140\nD0 7,24,44,1024\nMvaa 84,236,132\nRa 220,11,26624\nMvaa 84,232,128\nMvaa 88,240,128\n"
"Mvaa 88,244,132\nMvaa 120,244,140\nMvaa 120,240,136\nMvaa 152,240,144\nMvaa 152,244,148\nD0 8,24,44,2048\nMvaa 184,244,156\nMvaa 184,240,152\nMvaa 188,248,152\nMvaa 188,252,156\n"
"Mvaa 156,252,148\nMvaa 156,248,144\nMvaa 124,248,136\nMvaa 124,252,140\nD0 9,24,44,3072\nMvaa 92,252,132\nMvaa 92,248,128\nWL 0\nMaaa 16,224,192\nRa 128,10,32768\n"
"s_mov_b32 s4, s13\nMaaa 16,228,196\ns_add_u32 m0, s47, 16384\ns_add_u32 s44, s44, 0x40000\nMaaa 48,228,204\nRa 132,11,32768\nv_lshrrev_b32 v32, 2, v0\nMaaa 48,224,200\n"
"v_mul_u32_u24 v32, 896, v32\nMaaa 80,224,208\nRa 136,10,34816\nv_and_b32 v33, 3, v0\nMaaa 80,228,212\nD0 6,24,44,0\nMaaa 112,228,220\nRa 140,11,34816\n"
"v_lshl_add_u32 v32, v33, 4, v32\nMaaa 112,224,216\ns_lshr_b32 s2, s11, 1\nMaaa 116,232,216\nRa 144,10,40960\ns_mul_i32 s3, s2, 448\nMaaa 116,236,220\ns_and_b32 s5, s11, 1\n"
"Maaa 84,236,212\nRa 148,11,40960\ns_lshl3_add_u32 s3, s5, s3\nMaaa 84,232,208\ns_add_u32 s3, s3, 131072\nMaaa 52,232,200\nRa 152,10,43008\nv_add_u32 v32, s3, v32\nMaaa 52,236,204\n"
"D0 7,24,44,1024\nMaaa 20,236,196\nRa 156,11,43008\nTv 36,32,0\nMaaa 20,232,192\nTv 38,32,14336\nMaaa 24,240,192\nTv 40,32,64\nTv 42,32,14400\nMaaa 24,244,196\nTv 44,32,128\n"
"Maaa 56,244,204\nTv 46,32,14464\nTv 48,32,192\nMaaa 56,240,200\nTv 50,32,14528\nMaaa 88,240,208\nTv 52,32,256\nTv 54,32,14592\nMaaa 88,244,212\nD0 8,24,44,2048\nMaaa 120,244,220\n"
"Tv 56,32,320\nTv 58,32,14656\nMaaa 120,240,216\nTv 60,32,384\nMaaa 124,248,216\nTv 62,32,14720\ns_lshr_b32 s0, s4, 1\nMaaa 124,252,220\ns_mul_i32 s0, s0, 12\nMaaa 92,252,212\n"
"s_lshl1_add_u32 s0, s5, s0\ns_and_b32 s6, s4, 1\nMaaa 92,248,208\ns_add_u32 s0, s0, s6\nMaaa 60,248,200\ns_lshl3_add_u32 s1, s5, s4\ns_add_u32 s77, s1, 48\nMaaa 60,252,204\n"
"D0 9,24,44,3072\nMaaa 28,252,196\ns_and_b32 s4, s12, 1\ns_mov_b32 s7, 0x6543210\nMaaa 28,248,192\ns_waitcnt vmcnt(17) lgkmcnt(14)\ns_barrier\nMaaa 0,160,192\nTa 224,16,32768\n"
"s_cmp_lg_u32 s4, s2\ns_cselect_b32 s6, s0, s1\ns_cselect_b32 s5, 4, 16\ns_cselect_b32 s7, 0x6352410, s7\ns_cselect_b32 s3, 0, 229376\nMaaa 0,164,196\nTa 226,16,34816\n"
"Maaa 32,164,204\ns_add_u32 m0, s47, -49152\ns_add_u32 s48, s48, 128\nMaaa 32,160,200\nTa 228,16,49152\nMaaa 64,160,208\nTa 230,16,51200\ns_add_u32 s3, s3, s72\nMaaa 64,164,212\n"
"D0 2,20,48,0\nMaaa 96,164,220\nTa 232,17,32768\ns_add_u32 s3, s3, 0x1400000\nMaaa 96,160,216\nTa 234,17,34816\nMaaa 100,168,216\nWL 14\ns_lshr_b32 s0, s6, 4\nMaaa 100,172,220\n"
"Ta 236,17,49152\nMaaa 68,172,212\nTa 238,17,51200\ns_lshl_b32 s0, s0, 2\nMaaa 68,168,208\ns_lshr_b32 s0, s7, s0\nMaaa 36,168,200\nTa 240,18,32768\ns_and_b32 s0, s0, 7\n"
"Maaa 36,172,204\nTa 242,18,34816\nMaaa 4,172,196\nD0 3,20,48,1024\ns_lshl_b32 s0, s0, 5\nMaaa 4,168,192\nTa 244,18,49152\nMaaa 8,176,192\nTa 246,18,51200\ns_and_b32 s1, s6, 15\n"
"Maaa 8,180,196\ns_add_u32 s0, s0, s1\nMaaa 40,180,204\nTa 248,19,32768\ns_and_b32 s1, s6, 8\nMaaa 40,176,200\nTa 250,19,34816\nMaaa 72,176,208\ns_add_u32 s0, s0, s1\n"
"s_lshl_b32 s0, s0, 10\nMaaa 72,180,212\nTa 252,19,49152\nMaaa 104,180,220\nTa 254,19,51200\ns_add_u32 s0, s0, s3\nMaaa 104,176,216\nD0 4,20,48,2048\nMaaa 108,184,216\n"
"Sv 36,1,16,0,0\ns_add_u32 s6, s6, s5\nMaaa 108,188,220\nMaaa 76,188,212\nMaaa 76,184,208\nMaaa 44,184,200\nMaaa 44,188,204\nD0 5,20,48,3072\nMaaa 12,188,196\nMaaa 12,184,192\n"
"WL 0\nMvaa 80,224,128\nTa 160,12,32768\ns_lshr_b32 s0, s6, 4\nMvaa 80,228,132\nTa 162,12,34816\nMvaa 112,228,140\ns_add_u32 m0, s47, -32768\ns_add_u32 s45, s45, 128\n"
"Mvaa 112,224,136\nTa 164,12,49152\nMvaa 144,224,144\nTa 166,12,51200\ns_lshl_b32 s0, s0, 2\nMvaa 144,228,148\nD0 2,20,45,0\nMvaa 176,228,156\nTa 168,13,32768\n"
"s_lshr_b32 s0, s7, s0\nMvaa 176,224,152\nTa 170,13,34816\nMvaa 180,232,152\ns_and_b32 s0, s0, 7\ns_lshl_b32 s0, s0, 5\nMvaa 180,236,156\nTa 172,13,49152\nMvaa 148,236,148\n"
"Ta 174,13,51200\ns_and_b32 s1, s6, 15\nMvaa 148,232,144\ns_add_u32 s0, s0, s1\nMvaa 116,232,136\nTa 176,14,32768\ns_and_b32 s1, s6, 8\nMvaa 116,236,140\nTa 178,14,34816\n"
"Mvaa 84,236,132\nD0 3,20,45,1024\ns_add_u32 s0, s0, s1\nMvaa 84,232,128\nTa 180,14,49152\nMvaa 88,240,128\nTa 182,14,51200\ns_lshl_b32 s0, s0, 10\nMvaa 88,244,132\n"
"s_add_u32 s0, s0, s3\nMvaa 120,244,140\nTa 184,15,32768\nSv 40,1,16,0,0\nMvaa 120,240,136\nTa 186,15,34816\nMvaa 152,240,144\ns_add_u32 s6, s6, s5\nMvaa 152,244,148\n"
"Ta 188,15,49152\nMvaa 184,244,156\nTa 190,15,51200\nMvaa 184,240,152\nD0 4,20,45,2048\nMvaa 188,248,152\nMvaa 188,252,156\nMvaa 156,252,148\nMvaa 156,248,144\nMvaa 124,248,136\n"
"s_lshr_b32 s0, s6, 4\ns_lshl_b32 s0, s0, 2\nMvaa 124,252,140\nD0 5,20,45,3072\nMvaa 92,252,132\ns_lshr_b32 s0, s7, s0\ns_and_b32 s0, s0, 7\nMvaa 92,248,128\n"
"s_waitcnt vmcnt(18) lgkmcnt(0)\ns_barrier\nMvaa 64,160,128\nRa 192,10,49152\ns_lshl_b32 s0, s0, 5\nMvaa 64,164,132\ns_add_u32 m0, s47, 32768\ns_add_u32 s44, s44, 0x40000\n"
"Mvaa 96,164,140\nRa 196,11,49152\ns_and_b32 s1, s6, 15\nMvaa 96,160,136\ns_add_u32 s0, s0, s1\nMvaa 128,160,144\nRa 200,10,51200\ns_and_b32 s1, s6, 8\nMvaa 128,164,148\n"
"D0 6,24,44,0\nMvaa 160,164,156\nRa 204,11,51200\ns_add_u32 s0, s0, s1\nMvaa 160,160,152\ns_lshl_b32 s0, s0, 10\nMvaa 164,168,152\nRa 208,10,57344\ns_add_u32 s0, s0, s3\n"
"Mvaa 164,172,156\nSv 44,1,16,0,0\nMvaa 132,172,148\nRa 212,11,57344\ns_add_u32 s6, s6, s5\nMvaa 132,168,144\nMvaa 100,168,136\nRa 216,10,59392\nMvaa 100,172,140\nD0 7,24,44,1024\n"
"Mvaa 68,172,132\nRa 220,11,59392\nMvaa 68,168,128\nMvaa 72,176,128\nMvaa 72,180,132\nMvaa 104,180,140\ns_mov_b32 s6, s77\ns_mov_b32 s5, 16\nMvaa 104,176,136\ns_lshr_b32 s0, s6, 4\n"
"Mvaa 136,176,144\ns_lshl_b32 s0, s0, 2\ns_lshr_b32 s0, s7, s0\nMvaa 136,180,148\nD0 8,24,44,2048\nMvaa 168,180,156\ns_and_b32 s0, s0, 7\ns_lshl_b32 s0, s0, 5\nMvaa 168,176,152\n"
"s_and_b32 s1, s6, 15\nMvaa 172,184,152\ns_add_u32 s0, s0, s1\ns_and_b32 s1, s6, 8\nMvaa 172,188,156\ns_add_u32 s0, s0, s1\nMvaa 140,188,148\ns_lshl_b32 s0, s0, 10\n"
"s_add_u32 s0, s0, s3\nMvaa 140,184,144\nSv 48,1,16,0,0\nMvaa 108,184,136\ns_add_u32 s6, s6, s5\nMvaa 108,188,140\nD0 9,24,44,3072\nMvaa 76,188,132\nMvaa 76,184,128\nWL 0\n"
"Maaa 0,160,192\nRa 128,10,0\nMaaa 0,164,196\ns_add_u32 m0, s47, 49152\ns_add_u32 s44, s44, 0x40000\nMaaa 32,164,204\nRa 132,11,0\nMaaa 32,160,200\nMaaa 64,160,208\nRa 136,10,2048\n"
"s_lshr_b32 s0, s6, 4\nMaaa 64,164,212\nD0 6,24,44,0\nMaaa 96,164,220\nRa 140,11,2048\ns_lshl_b32 s0, s0, 2\nMaaa 96,160,216\ns_lshr_b32 s0, s7, s0\nMaaa 100,168,216\n"
"Ra 144,10,8192\ns_and_b32 s0, s0, 7\nMaaa 100,172,220\ns_lshl_b32 s0, s0, 5\nMaaa 68,172,212\nRa 148,11,8192\ns_and_b32 s1, s6, 15\nMaaa 68,168,208\ns_add_u32 s0, s0, s1\n"
"Maaa 36,168,200\nRa 152,10,10240\ns_and_b32 s1, s6, 8\nMaaa 36,172,204\nD0 7,24,44,1024\nMaaa 4,172,196\nRa 156,11,10240\ns_add_u32 s0, s0, s1\nMaaa 4,168,192\n"
"s_lshl_b32 s0, s0, 10\nMaaa 8,176,192\ns_add_u32 s0, s0, s3\nSv 52,1,16,0,0\nMaaa 8,180,196\ns_add_u32 s6, s6, s5\nMaaa 40,180,204\nMaaa 40,176,200\nMaaa 72,176,208\n"
"Maaa 72,180,212\nD0 8,24,44,2048\nMaaa 104,180,220\nMaaa 104,176,216\nMaaa 108,184,216\nMaaa 108,188,220\ns_lshr_b32 s0, s6, 4\nMaaa 76,188,212\ns_lshl_b32 s0, s0, 2\n"
"s_lshr_b32 s0, s7, s0\nMaaa 76,184,208\ns_and_b32 s0, s0, 7\nMaaa 44,184,200\ns_lshl_b32 s0, s0, 5\ns_and_b32 s1, s6, 15\nMaaa 44,188,204\nD0 9,24,44,3072\nMaaa 12,188,196\n"
"s_add_u32 s0, s0, s1\ns_and_b32 s1, s6, 8\nMaaa 12,184,192\nWV 21\ns_barrier\nMaaa 16,224,192\nTa 160,12,0\ns_add_u32 s0, s0, s1\nMaaa 16,228,196\nTa 162,12,2048\nMaaa 48,228,204\n"
"s_add_u32 m0, s47, -16384\ns_add_u32 s48, s48, 128\nMaaa 48,224,200\nTa 164,12,16384\nMaaa 80,224,208\nTa 166,12,18432\ns_lshl_b32 s0, s0, 10\nMaaa 80,228,212\nD0 2,20,48,0\n"
"Maaa 112,228,220\nTa 168,13,0\ns_add_u32 s0, s0, s3\nMaaa 112,224,216\nTa 170,13,2048\nMaaa 116,232,216\nSv 56,1,16,0,0\ns_add_u32 s6, s6, s5\nMaaa 116,236,220\nTa 172,13,16384\n"
"Maaa 84,236,212\nTa 174,13,18432\nMaaa 84,232,208\nMaaa 52,232,200\nTa 176,14,0\nMaaa 52,236,204\nTa 178,14,2048\nMaaa 20,236,196\nD0 3,20,48,1024\nMaaa 20,232,192\n"
"Ta 180,14,16384\nMaaa 24,240,192\nTa 182,14,18432\ns_lshr_b32 s0, s6, 4\nMaaa 24,244,196\ns_lshl_b32 s0, s0, 2\nMaaa 56,244,204\nTa 184,15,0\ns_lshr_b32 s0, s7, s0\n"
"Maaa 56,240,200\nTa 186,15,2048\nMaaa 88,240,208\ns_and_b32 s0, s0, 7\ns_lshl_b32 s0, s0, 5\nMaaa 88,244,212\nTa 188,15,16384\nMaaa 120,244,220\nTa 190,15,18432\n"
"s_and_b32 s1, s6, 15\nMaaa 120,240,216\nD0 4,20,48,2048\nMaaa 124,248,216\ns_add_u32 s0, s0, s1\ns_and_b32 s1, s6, 8\nMaaa 124,252,220\ns_add_u32 s0, s0, s1\nMaaa 92,252,212\n"
"s_lshl_b32 s0, s0, 10\ns_add_u32 s0, s0, s3\nMaaa 92,248,208\nSv 60,1,16,0,0\nMaaa 60,248,200\ns_add_u32 s6, s6, s5\nMaaa 60,252,204\nD0 5,20,48,3072\nMaaa 28,252,196\n"
"Maaa 28,248,192\nWL 0\nMvaa 64,160,128\nTa 224,16,0\nMvaa 64,164,132\nTa 226,16,2048\ns_add_u32 m0, s47, 65536\ns_add_u32 s46, s51, 0\nMvaa 96,164,140\nv_lshrrev_b32 v48, 4, v0\n"
"Mvaa 96,160,136\nTa 228,16,16384\nD0 6,32,46,0\nMvaa 128,160,144\nTa 230,16,18432\nMvaa 128,164,148\nv_and_b32 v49, 7, v0\nMvaa 160,164,156\nTa 232,17,0\nMvaa 160,160,152\n"
"Ta 234,17,2048\nMvaa 164,168,152\nv_xor_b32 v49, v49, v48\nD0 7,32,46,1024\nMvaa 164,172,156\nTa 236,17,16384\nMvaa 132,172,148\nTa 238,17,18432\nMvaa 132,168,144\n"
"v_lshlrev_b32 v49, 4, v49\nMvaa 100,168,136\nTa 240,18,0\nMvaa 100,172,140\nTa 242,18,2048\nD0 8,32,46,2048\nMvaa 68,172,132\nv_lshrrev_b32 v48, 3, v0\nMvaa 68,168,128\n"
"Ta 244,18,16384\nMvaa 72,176,128\nTa 246,18,18432\nMvaa 72,180,132\nv_mul_u32_u24 v48, 896, v48\nMvaa 104,180,140\nTa 248,19,0\nD0 9,32,46,3072\nMvaa 104,176,136\nTa 250,19,2048\n"
"Mvaa 136,176,144\nv_add_u32 v40, v48, v49\nMvaa 136,180,148\nTa 252,19,16384\ns_add_u32 m0, s47, 81920\ns_add_u32 s46, s51, 262144\nMvaa 168,180,156\nTa 254,19,18432\n"
"Mvaa 168,176,152\nv_xor_b32 v49, 64, v40\nD0 6,32,46,0\nMvaa 172,184,152\nv_add_u32 v41, 6144, v49\nMvaa 172,188,156\nv_add_u32 v42, 55296, v40\nMvaa 140,188,148\n"
"v_add_u32 v43, 61440, v49\n.W574:\ns_cbranch_execz .W578\nMvaa 140,184,144\ns_mov_b32 s2, s13\nMvaa 108,184,136\ns_lshl3_add_u32 s2, s2, s11\nD0 7,32,46,1024\nMvaa 108,188,140\n"
"s_mul_i32 s52, s2, 14336\nMvaa 76,188,132\ns_lshr_b32 s2, s13, 2\ns_bfe_u32 s3, s13, 0x10001\nMvaa 76,184,128\ns_waitcnt vmcnt(23) lgkmcnt(0)\ns_barrier\nMvaa 80,224,128\n"
"Ra 192,10,16384\ns_mul_i32 s3, s3, 17\nMvaa 80,228,132\ns_add_u32 s2, s2, s3\nD0 8,32,46,2048\nMvaa 112,228,140\nRa 196,11,16384\ns_lshl2_add_u32 s3, s14, 0\nMvaa 112,224,136\n"
"s_lshl_b32 s3, s3, 2\nMvaa 144,224,144\nRa 200,10,18432\ns_lshr_b32 s3, 0xe420ca86, s3\nMvaa 144,228,148\ns_and_b32 s3, s3, 15\nMvaa 176,228,156\nRa 204,11,18432\n"
"s_add_u32 s2, s2, s3\nD0 9,32,46,3072\nMvaa 176,224,152\ns_cmp_eq_u32 s2, 32\ns_cselect_b32 s4, 16, s2\nMvaa 180,232,152\nRa 208,10,24576\ns_mul_i32 s75, s4, 65536\n"
"Mvaa 180,236,156\ns_lshr_b32 s2, s13, 2\ns_add_u32 m0, s47, -65536\ns_add_u32 s46, s52, 0\nMvaa 148,236,148\nRa 212,11,24576\ns_bfe_u32 s3, s13, 0x10001\nMvaa 148,232,144\n"
"s_mul_i32 s3, s3, 17\nD0 40,28,46,0\nMvaa 116,232,136\nRa 216,10,26624\ns_add_u32 s2, s2, s3\nMvaa 116,236,140\ns_lshl2_add_u32 s3, s14, 1\nMvaa 84,236,132\nRa 220,11,26624\n"
"s_lshl_b32 s3, s3, 2\nMvaa 84,232,128\ns_lshr_b32 s3, 0xe420ca86, s3\nMvaa 88,240,128\ns_and_b32 s3, s3, 15\ns_add_u32 s2, s2, s3\nD0 41,28,46,1024\nMvaa 88,244,132\n"
"s_cmp_eq_u32 s2, 32\ns_cselect_b32 s4, 16, s2\nMvaa 120,244,140\ns_mul_i32 s76, s4, 65536\ns_lshr_b32 s2, s13, 2\nMvaa 120,240,136\ns_bfe_u32 s3, s13, 0x10001\nMvaa 152,240,144\n"
"s_mul_i32 s3, s3, 17\ns_add_u32 s2, s2, s3\nMvaa 152,244,148\ns_lshl2_add_u32 s3, s14, 2\nD0 42,28,46,2048\nMvaa 184,244,156\ns_lshl_b32 s3, s3, 2\ns_lshr_b32 s3, 0xe420ca86, s3\n"
"Mvaa 184,240,152\ns_and_b32 s3, s3, 15\nMvaa 188,248,152\ns_add_u32 s2, s2, s3\nMvaa 188,252,156\ns_cmp_eq_u32 s2, 32\ns_cselect_b32 s4, 16, s2\nMvaa 156,252,148\n"
"s_mul_i32 s77, s4, 65536\ns_lshr_b32 s2, s13, 2\nD0 43,28,46,3072\nMvaa 156,248,144\ns_bfe_u32 s3, s13, 0x10001\nMvaa 124,248,136\ns_mul_i32 s3, s3, 17\ns_add_u32 s2, s2, s3\n"
"Mvaa 124,252,140\ns_lshl2_add_u32 s3, s14, 3\nMvaa 92,252,132\ns_lshl_b32 s3, s3, 2\ns_lshr_b32 s3, 0xe420ca86, s3\nMvaa 92,248,128\nWL 0\nMaaa 16,224,192\nRa 128,10,32768\n"
"s_and_b32 s3, s3, 15\ns_add_u32 m0, s47, 0\ns_add_u32 s46, s51, 524288\nD0 6,32,46,0\nMaaa 16,228,196\ns_add_u32 s2, s2, s3\ns_cmp_eq_u32 s2, 32\ns_cselect_b32 s4, 16, s2\n"
"Maaa 48,228,204\nRa 132,11,32768\ns_mul_i32 s78, s4, 65536\nMaaa 48,224,200\ns_lshr_b32 s3, s11, 1\nMaaa 80,224,208\nRa 136,10,34816\ns_lshl_b32 s3, s3, 15\nMaaa 80,228,212\n"
"s_add_u32 s2, s72, s3\nD0 7,32,46,1024\nMaaa 112,228,220\nRa 140,11,34816\ns_and_b32 s3, s11, 1\nMaaa 112,224,216\ns_lshl_b32 s3, s3, 12\nMaaa 116,232,216\nRa 144,10,40960\n"
"s_add_u32 s2, s2, s3\nMaaa 116,236,220\ns_add_u32 s2, s2, 0x0\nMaaa 84,236,212\nRa 148,11,40960\ns_mov_b32 s55, 16384\nD0 8,32,46,2048\nMaaa 84,232,208\ns_add_u32 s75, s75, s2\n"
"Maaa 52,232,200\nRa 152,10,43008\ns_add_u32 s76, s76, s2\nMaaa 52,236,204\ns_add_u32 s77, s77, s2\nMaaa 20,236,196\nRa 156,11,43008\ns_add_u32 s78, s78, s2\nMaaa 20,232,192\n"
"v_add_u32 v32, 0x10000, v12\nD0 9,32,46,3072\nMaaa 24,240,192\nv_add_u32 v33, 0x10000, v13\nv_add_u32 v34, 0x10000, v14\nMaaa 24,244,196\nv_add_u32 v35, 0x10000, v15\n"
"Maaa 56,244,204\nv_add_u32 v36, 0x10000, v16\nv_add_u32 v37, 0x10000, v17\ns_add_u32 m0, s47, 16384\ns_add_u32 s46, s51, 786432\nMaaa 56,240,200\nv_add_u32 v38, 0x10000, v18\n"
"Maaa 88,240,208\nv_add_u32 v39, 0x10000, v19\ns_add_u32 s2, s11, 1\nD0 6,32,46,0\nMaaa 88,244,212\ns_lshr_b32 s2, s2, 1\nMaaa 120,244,220\ns_lshl_b32 s2, s2, 12\n"
"v_add_u32 v44, s2, v10\nMaaa 120,240,216\nv_add_u32 v45, s2, v11\nMaaa 124,248,216\nv_xor_b32 v46, 0x1000, v44\nv_xor_b32 v47, 0x1000, v45\nMaaa 124,252,220\n"
"s_lshr_b32 s3, s11, 1\nD0 7,32,46,1024\nMaaa 92,252,212\ns_lshl_b32 s3, s3, 13\ns_lshl_b32 s2, s11, 12\nMaaa 92,248,208\ns_add_u32 s2, s2, s3\nMaaa 60,248,200\n"
"s_add_u32 s2, s2, s72\ns_lshl_b32 s3, s11, 12\nMaaa 60,252,204\ns_add_u32 s89, s3, 0x10000\nMaaa 28,252,196\ns_mov_b32 s88, 0xffff8000\ns_lshl_b32 s4, s13, 1\nD0 8,32,46,2048\n"
"Maaa 28,248,192\ns_waitcnt vmcnt(25) lgkmcnt(0)\ns_barrier\nMaaa 0,160,192\nTa 224,16,32768\ns_sub_u32 s79, 15, s4\nMaaa 0,164,196\nTa 226,16,34816\nMaaa 32,164,204\n"
"s_add_u32 s96, s2, 0x1310000\ns_mov_b32 s63, 0x53800000\nMaaa 32,160,200\nTa 228,16,49152\nD0 9,32,46,3072\nMaaa 64,160,208\nTa 230,16,51200\ns_mov_b32 s64, 0x2b800000\n"
"Maaa 64,164,212\ns_sub_u32 s98, s4, 1\nMaaa 96,164,220\nTa 232,17,32768\ns_lshl_b32 s5, s13, 5\ns_add_u32 m0, s47, -32768\ns_add_u32 s46, s52, 128\nMaaa 96,160,216\n"
"Ta 234,17,34816\nMaaa 100,168,216\ns_add_u32 s5, s5, s11\ns_lshl_b32 s65, s5, 9\nD0 40,28,46,0\nMaaa 100,172,220\nTa 236,17,49152\nMaaa 68,172,212\nTa 238,17,51200\n"
"s_add_u32 s65, s65, s72\nMaaa 68,168,208\ns_add_u32 s65, s65, 0x1580000\nMaaa 36,168,200\nTa 240,18,32768\ns_and_b32 s66, s72, 0x2000\nMaaa 36,172,204\nTa 242,18,34816\n"
"D0 41,28,46,1024\nMaaa 4,172,196\ns_add_u32 s66, s66, s56\ns_add_u32 s66, s66, 0x100000\nMaaa 4,168,192\nTa 244,18,49152\nMaaa 8,176,192\nTa 246,18,51200\ns_and_b32 s5, s12, 1\n"
"Maaa 8,180,196\ns_mul_i32 s58, s5, 0x202000\nMaaa 40,180,204\nTa 248,19,32768\ns_xor_b32 s58, s58, s72\nD0 42,28,46,2048\nMaaa 40,176,200\nTa 250,19,34816\nMaaa 72,176,208\n"
"s_add_u32 s58, s58, 0x15c0000\ns_lshl_b32 s5, s5, 8\nMaaa 72,180,212\nTa 252,19,49152\nMaaa 104,180,220\nTa 254,19,51200\ns_add_u32 s61, s58, s5\nMaaa 104,176,216\n"
"s_lshl4_add_u32 s60, s13, s61\nD0 43,28,46,3072\nMaaa 108,184,216\ns_add_u32 s54, s60, 128\ns_xor_b32 s60, s54, 0x100\nMaaa 108,188,220\ns_cmp_eq_u32 s11, 0\n"
"s_cselect_b32 s54, s54, 0x40000000\nMaaa 76,188,212\ns_getpc_b64 s[8:9]\n.PC283:\ns_add_u32 s8, s8, .HD269-.PC283\ns_addc_u32 s9, s9, 0\ns_add_u32 m0, s47, -49152\n"
"s_add_u32 s46, s52, 256\nMaaa 76,184,208\ns_mov_b32 s97, 0xff800000\nMaaa 44,184,200\ns_mov_b32 s90, 0\nD0 40,28,46,0\nMaaa 44,188,204\ns_cmp_lt_u32 s11, 2\n"
"s_cselect_b32 s92, 2, 0\nMaaa 12,188,196\ns_mul_i32 s4, s79, 65536\ns_sub_u32 s4, s2, s4\nMaaa 12,184,192\nWL 0\nMvaa 80,224,128\nTa 160,12,32768\ns_add_u32 s94, s4, 0x20000\n"
"Mvaa 80,228,132\nTa 162,12,34816\nD0 41,28,46,1024\nMvaa 84,236,132\ns_add_u32 s95, s4, 0x410000\ns_add_u32 s86, s2, 0x130000\nMvaa 84,232,128\nTa 164,12,49152\nMvaa 88,240,128\n"
"Ta 166,12,51200\ns_add_u32 s87, s2, 0x520000\nMvaa 88,244,132\ns_sub_u32 s99, s79, 2\ns_cmp_eq_u32 s13, 7\ns_cselect_b32 s93, 1.0, 0\nMvaa 92,252,132\nTa 168,13,32768\n"
"s_mov_b32 s36, s68\nD0 42,28,46,2048\nMvaa 92,248,128\nTa 170,13,34816\nMvaa 124,248,136\ns_and_b32 s37, s69, 0xffff\ns_mov_b32 s38, 0x1c0000\nMvaa 124,252,140\nTa 172,13,49152\n"
"CV 200,80,81\nMvaa 120,244,140\nTa 174,13,51200\ns_mov_b32 s39, 0x20000\nMvaa 120,240,136\ns_and_b32 s2, s74, 15\nD0 43,28,46,3072\nMvaa 116,232,136\nTa 176,14,32768\n"
"s_min_u32 s2, s2, 13\nMvaa 116,236,140\nTa 178,14,34816\nMvaa 112,228,140\ns_sub_u32 s2, s2, 0\ns_mul_i32 s3, s73, 14\nMvaa 112,224,136\nTa 180,14,49152\nCV 201,82,83\n"
"Mvaa 144,224,144\nTa 182,14,51200\ns_add_u32 s2, s2, s3\nMvaa 144,228,148\ns_lshl2_add_u32 s2, s2, s11\nMvaa 148,236,148\nTa 184,15,32768\ns_mul_i32 s3, s2, 4682\n"
"Mvaa 148,232,144\nTa 186,15,34816\nCV 202,84,85\nMvaa 152,240,144\ns_lshr_b32 s3, s3, 16\ns_mul_i32 s22, s3, 14\nMvaa 152,244,148\nTa 188,15,49152\nCV 203,86,87\nMvaa 156,252,148\n"
"Ta 190,15,51200\ns_sub_u32 s22, s2, s22\nMvaa 156,248,144\ns_and_b32 s21, s3, 28\nMvaa 188,248,152\ns_add_u32 s21, s21, s3\ns_lshl_b32 s21, s21, 4\nMvaa 188,252,156\n"
"s_bfe_u32 s41, s3, 0x30002\ns_cmp_gt_u32 s22, 6\ns_cselect_b32 s2, 1179648, 0\ns_cselect_b32 s1, 16, 0\nMvaa 184,244,156\ns_add_u32 s41, s41, s1\n"
"s_lshl4_add_u32 s41, s41, 0xc048480\nMvaa 184,240,152\ns_and_b32 s1, s3, 28\nCV 204,88,89\nMvaa 180,232,152\ns_add_u32 s3, s3, s1\ns_lshl_b32 s3, s3, 11\nMvaa 180,236,156\n"
"s_add_u32 s2, s2, s3\nMvaa 176,228,156\ns_lshl_b32 s3, s22, 17\ns_add_u32 s2, s2, s3\nMvaa 176,224,152\ns_waitcnt vmcnt(16) lgkmcnt(0)\ns_barrier\nMvaa 64,160,128\n"
"Ra 192,10,49152\ns_add_u32 s20, s2, 0x10e0000\ns_add_u32 m0, s47, 32768\ns_add_u32 s46, s51, 1048576\nMvaa 64,164,132\ns_lshl_b32 s2, s22, 7\nMvaa 68,172,132\nRa 196,11,49152\n"
"s_add_u32 s40, s2, 0xc04b800\nD0 6,32,46,0\nMvaa 68,168,128\ns_mul_i32 s2, s21, 896\nCV 205,90,91\nMvaa 72,176,128\nRa 200,10,51200\ns_lshl_b32 s3, s22, 6\nMvaa 72,180,132\n"
"s_add_u32 s22, s2, s3\nCV 206,92,93\nMvaa 76,188,132\nRa 204,11,51200\ns_add_u32 s23, s22, 57344\nMvaa 76,184,128\nD0 7,32,46,1024\nMvaa 108,184,136\nRa 208,10,57344\n"
"CV 207,94,95\nMvaa 108,188,140\nCV 220,120,121\nCV 221,122,123\nMvaa 104,180,140\nRa 212,11,57344\nMvaa 104,176,136\nCV 222,124,125\nCV 223,126,127\nMvaa 100,168,136\n"
"Ra 216,10,59392\nD0 8,32,46,2048\ns_lshl3_add_u32 s5, s12, s13\ns_lshl2_add_u32 s5, s5, s11\ns_lshl2_add_u32 s5, s5, 0xc04a400\nbuffer_load_dword v27, off, s[16:19], s5 sc0 sc1\n"
"Mvaa 100,172,140\nCV 216,112,113\nMvaa 96,164,140\nRa 220,11,59392\nCV 217,114,115\nMvaa 96,160,136\nCV 218,116,117\nCV 219,118,119\nMvaa 128,160,144\nCV 192,64,65\nCV 193,66,67\n"
"Mvaa 128,164,148\nD0 9,32,46,3072\nMvaa 132,172,148\nCV 194,68,69\nCV 195,70,71\nMvaa 132,168,144\nCV 196,72,73\nCV 197,74,75\nMvaa 136,176,144\ns_add_u32 m0, s47, 49152\n"
"s_add_u32 s46, s51, 1310720\nMvaa 136,180,148\nCV 198,76,77\nCV 199,78,79\nMvaa 140,188,148\nD0 6,32,46,0\nCV 212,104,105\nMvaa 140,184,144\nCV 213,106,107\nMvaa 172,184,152\n"
"CV 214,108,109\nCV 215,110,111\nMvaa 172,188,156\nCV 208,96,97\nCV 209,98,99\nMvaa 168,180,156\nCV 210,100,101\nMvaa 168,176,152\nD0 7,32,46,1024\nCV 211,102,103\n"
"Mvaa 164,168,152\nCV 232,144,145\nMvaa 164,172,156\nCV 233,146,147\nSv 192,1,16,75,0\nMvaa 160,164,156\nCV 234,148,149\nMvaa 160,160,152\nWL 0\nMaaa 0,160,192\nD0 8,32,46,2048\n"
"Ra 128,44,0\nMaaa 0,164,196\nCV 235,150,151\nMaaa 4,172,196\nRa 132,45,0\nMaaa 4,168,192\nCV 236,152,153\nMaaa 8,176,192\nRa 136,44,2048\nMaaa 8,180,196\nD0 9,32,46,3072\n"
"Maaa 12,188,196\nRa 140,45,2048\nSv 196,1,16,75,1024\nMaaa 12,184,192\nCV 237,154,155\nMaaa 44,184,200\nRa 144,46,0\nMaaa 44,188,204\nCV 238,156,157\nMaaa 40,180,204\nRa 148,47,0\n"
"Maaa 40,176,200\nCV 239,158,159\nMaaa 36,168,200\nRa 152,46,2048\nMaaa 36,172,204\nCV 252,184,185\nMaaa 32,164,204\nRa 156,47,2048\nMaaa 32,160,200\nCV 253,186,187\n"
"Sv 200,1,16,75,2048\nMaaa 64,160,208\nCV 254,188,189\nCV 255,190,191\nMaaa 64,164,212\nCV 248,176,177\nMaaa 68,172,212\nCV 249,178,179\nMaaa 68,168,208\nCV 250,180,181\n"
"Maaa 72,176,208\nCV 251,182,183\nMaaa 72,180,212\nCV 224,128,129\nMaaa 76,188,212\nCV 225,130,131\nMaaa 76,184,208\nCV 226,132,133\nMaaa 108,184,216\nCV 227,134,135\n"
"Sv 204,1,16,75,3072\nMaaa 108,188,220\nCV 228,136,137\nMaaa 104,180,220\nCV 229,138,139\nMaaa 104,176,216\nCV 230,140,141\nMaaa 100,168,216\nCV 231,142,143\nMaaa 100,172,220\n"
"CV 244,168,169\nMaaa 96,164,220\nCV 245,170,171\nMaaa 96,160,216\ns_barrier\nMaaa 16,224,192\ns_add_u32 m0, s47, -16384\ns_add_u32 s46, s52, 384\nTa 160,32,0\nMaaa 16,228,196\n"
"Ta 162,32,2048\nMaaa 20,236,196\nD0 40,28,46,0\ns_add_u32 s75, s75, s55\nSv 208,1,16,75,0\nMaaa 20,232,192\nTa 164,32,16384\nMaaa 24,240,192\nTa 166,32,18432\nMaaa 24,244,196\n"
"CV 246,172,173\nMaaa 28,252,196\nTa 168,33,0\nMaaa 28,248,192\nD0 41,28,46,1024\nTa 170,33,2048\nMaaa 60,248,200\nCV 247,174,175\nMaaa 60,252,204\nTa 172,33,16384\n"
"Maaa 56,244,204\nTa 174,33,18432\nMaaa 56,240,200\nCV 240,160,161\n.W575:\ns_cbranch_execz .W579\nSv 212,1,16,75,1024\nMaaa 52,232,200\nD0 42,28,46,2048\nTa 176,34,0\n"
"Maaa 52,236,204\nTa 178,34,2048\nMaaa 48,228,204\nCV 241,162,163\nMaaa 48,224,200\nTa 180,34,16384\nMaaa 80,224,208\nTa 182,34,18432\nCV 242,164,165\nMaaa 80,228,212\n"
"D0 43,28,46,3072\nMaaa 84,236,212\nTa 184,35,0\nMaaa 84,232,208\nTa 186,35,2048\nMaaa 88,240,208\nCV 243,166,167\nSv 216,1,16,75,2048\nMaaa 88,244,212\nTa 188,35,16384\n"
"Maaa 92,252,212\nTa 190,35,18432\nMaaa 92,248,208\nMaaa 124,248,216\nMaaa 124,252,220\nMaaa 120,244,220\nMaaa 120,240,216\nMaaa 116,232,216\nMaaa 116,236,220\nSv 220,1,16,75,3072\n"
"Maaa 112,228,220\nMaaa 112,224,216\nWL 0\nZvaa 64,160,128\nTa 224,36,0\nAR 60,0\nMvaa 64,164,132\nTa 226,36,2048\nZvaa 96,164,140\nAR 61,1\nMvaa 96,160,136\nTa 228,36,16384\n"
"CV 192,60,61\nZvaa 128,160,144\nTa 230,36,18432\nMvaa 128,164,148\nAR 60,2\nAR 61,3\nZvaa 160,164,156\nTa 232,37,0\nSv 224,1,16,76,0\nMvaa 160,160,152\nTa 234,37,2048\n"
"CV 193,60,61\nZvaa 164,168,152\nAR 60,4\nMvaa 164,172,156\nTa 236,37,16384\nZvaa 132,172,148\nTa 238,37,18432\nAR 61,5\nMvaa 132,168,144\nCV 194,60,61\nZvaa 100,168,136\n"
"Ta 240,38,0\nAR 60,6\nMvaa 100,172,140\nTa 242,38,2048\nZvaa 68,172,132\nSv 228,1,16,76,1024\nAR 61,7\nMvaa 68,168,128\nTa 244,38,16384\nZvaa 72,176,128\nTa 246,38,18432\n"
"CV 195,60,61\nMvaa 72,180,132\nAR 60,8\nZvaa 104,180,140\nTa 248,39,0\nMvaa 104,176,136\nTa 250,39,2048\nAR 61,9\nZvaa 136,176,144\nCV 196,60,61\nMvaa 136,180,148\n"
"Ta 252,39,16384\nAR 60,10\nZvaa 168,180,156\nTa 254,39,18432\nSv 232,1,16,76,2048\nMvaa 168,176,152\nAR 61,11\nCV 197,60,61\nZvaa 172,184,152\nAR 60,12\nMvaa 172,188,156\n"
"AR 61,13\nZvaa 140,188,148\nCV 198,60,61\nAR 60,14\nMvaa 140,184,144\nAR 61,15\nZvaa 108,184,136\nCV 199,60,61\nAR 60,16\nMvaa 108,188,140\nSv 236,1,16,76,3072\nZvaa 76,188,132\n"
"AR 61,17\nCV 200,60,61\nMvaa 76,184,128\ns_waitcnt vmcnt(29) lgkmcnt(0)\ns_barrier\nZvaa 80,224,128\nRa 192,44,32768\nAR 60,18\nMvaa 80,228,132\nRa 196,45,32768\n"
"s_add_u32 m0, s47, 65536\ns_add_u32 s46, s51, 1572864\nD0 6,32,46,0\nZvaa 112,228,140\nRa 200,44,34816\nMvaa 112,224,136\nRa 204,45,34816\nAR 61,19\nZvaa 144,224,144\n"
"Ra 208,46,32768\nMvaa 144,228,148\nRa 212,47,32768\nCV 201,60,61\nD0 7,32,46,1024\nZvaa 176,228,156\nRa 216,46,34816\nMvaa 176,224,152\nRa 220,47,34816\nAR 60,20\n"
"Zvaa 180,232,152\nTa 160,12,0\nMvaa 180,236,156\nTa 162,12,2048\nD0 8,32,46,2048\nZvaa 148,236,148\nTa 164,12,16384\nAR 61,21\nMvaa 148,232,144\nTa 166,12,18432\nZvaa 116,232,136\n"
"Ta 168,13,0\nCV 202,60,61\nMvaa 116,236,140\nTa 170,13,2048\nD0 9,32,46,3072\nZvaa 84,236,132\nTa 172,13,16384\nAR 60,22\nMvaa 84,232,128\nTa 174,13,18432\n"
"s_add_u32 m0, s47, 81920\ns_add_u32 s46, s51, 1835008\nZvaa 88,240,128\nTa 176,14,0\nAR 61,23\nMvaa 88,244,132\nTa 178,14,2048\nD0 6,32,46,0\nZvaa 120,244,140\nTa 180,14,16384\n"
"Mvaa 120,240,136\nTa 182,14,18432\nCV 203,60,61\nZvaa 152,240,144\nTa 184,15,0\nMvaa 152,244,148\nTa 186,15,2048\nAR 60,24\nD0 7,32,46,1024\nZvaa 184,244,156\nTa 188,15,16384\n"
"Mvaa 184,240,152\nTa 190,15,18432\nAR 61,25\nZvaa 188,248,152\nCV 204,60,61\nMvaa 188,252,156\nAR 60,26\nD0 8,32,46,2048\nZvaa 156,252,148\nAR 61,27\nCV 205,60,61\n"
"Mvaa 156,248,144\nAR 60,28\nZvaa 124,248,136\nAR 61,29\nCV 206,60,61\nMvaa 124,252,140\nAR 60,30\nD0 9,32,46,3072\nZvaa 92,252,132\nAR 61,31\nCV 207,60,61\nMvaa 92,248,128\nWL 0\n"
"Mvaa 64,160,192\nTa 224,16,0\nAR 60,32\nMvaa 64,164,196\nTa 226,16,2048\ns_add_u32 m0, s47, -65536\ns_add_u32 s46, s52, 512\nD0 40,28,46,0\nMvaa 96,164,204\nAR 61,33\n"
"Mvaa 96,160,200\nTa 228,16,16384\nCV 208,60,61\nMvaa 128,160,208\nTa 230,16,18432\nMvaa 128,164,212\nAR 60,34\nAR 61,35\nD0 41,28,46,1024\nMvaa 160,164,220\nTa 232,17,0\n"
"Mvaa 160,160,216\nTa 234,17,2048\nCV 209,60,61\nMvaa 164,168,216\nAR 60,36\nMvaa 164,172,220\nTa 236,17,16384\nD0 42,28,46,2048\nMvaa 132,172,212\nTa 238,17,18432\nAR 61,37\n"
"Mvaa 132,168,208\nCV 210,60,61\nMvaa 100,168,200\nTa 240,18,0\nAR 60,38\nMvaa 100,172,204\nTa 242,18,2048\nD0 43,28,46,3072\nMvaa 68,172,196\ns_add_u32 s76, s76, s55\n"
"Sv 240,1,16,76,0\nMvaa 68,168,192\nTa 244,18,16384\nMvaa 72,176,192\nTa 246,18,18432\nAR 61,39\nMvaa 72,180,196\nCV 211,60,61\nMvaa 104,180,204\nTa 248,19,0\nMvaa 104,176,200\n"
"Ta 250,19,2048\nSv 244,1,16,76,1024\nMvaa 136,176,208\nAR 60,40\nMvaa 136,180,212\nTa 252,19,16384\nAR 61,41\nMvaa 168,180,220\nTa 254,19,18432\nMvaa 168,176,216\nCV 212,60,61\n"
"AR 60,42\nMvaa 172,184,216\nSv 248,1,16,76,2048\nMvaa 172,188,220\nAR 61,43\nMvaa 140,188,212\nCV 213,60,61\nAR 60,44\nMvaa 140,184,208\nAR 61,45\nMvaa 108,184,200\nCV 214,60,61\n"
"AR 60,46\nMvaa 108,188,204\nSv 252,1,16,76,3072\nMvaa 76,188,196\nAR 61,47\nCV 215,60,61\nMvaa 76,184,192\ns_waitcnt vmcnt(20) lgkmcnt(0)\ns_barrier\nMvaa 80,224,192\n"
"Ra 128,44,16384\nAR 60,48\nv_cmp_ne_u32 vcc, s15, v27\ns_nop 0\ns_cbranch_vccnz .S284\n.S285:\nMvaa 80,228,196\nRa 132,45,16384\n"
"buffer_store_dword v25, off, s[16:19], s54 offset:0 sc0 sc1\ns_add_u32 m0, s47, 0\ns_add_u32 s46, s51, 2097152\nD0 6,32,46,0\nMvaa 112,228,204\nRa 136,44,18432\nMvaa 112,224,200\n"
"Ra 140,45,18432\nAR 61,49\nMvaa 144,224,208\nRa 144,46,16384\nMvaa 144,228,212\nRa 148,47,16384\nCV 216,60,61\nD0 7,32,46,1024\nMvaa 176,228,220\nRa 152,46,18432\n"
"Mvaa 176,224,216\nRa 156,47,18432\nAR 60,50\nMvaa 180,232,216\nTa 160,12,32768\nMvaa 180,236,220\nTa 162,12,34816\nD0 8,32,46,2048\nMvaa 148,236,212\nTa 164,12,49152\nAR 61,51\n"
"Mvaa 148,232,208\nTa 166,12,51200\nMvaa 116,232,200\nTa 168,13,32768\nCV 217,60,61\nMvaa 116,236,204\nTa 170,13,34816\nD0 9,32,46,3072\nMvaa 84,236,196\nTa 172,13,49152\nAR 60,52\n"
"Mvaa 84,232,192\nTa 174,13,51200\ns_add_u32 m0, s47, 16384\ns_add_u32 s46, s51, 2359296\nMvaa 88,240,192\nTa 176,14,32768\nAR 61,53\nMvaa 88,244,196\nTa 178,14,34816\n"
"D0 6,32,46,0\nMvaa 120,244,204\nTa 180,14,49152\nMvaa 120,240,200\nTa 182,14,51200\nCV 218,60,61\nMvaa 152,240,208\nTa 184,15,32768\nMvaa 152,244,212\nTa 186,15,34816\nAR 60,54\n"
"D0 7,32,46,1024\nMvaa 184,244,220\nTa 188,15,49152\nMvaa 184,240,216\nTa 190,15,51200\nAR 61,55\nMvaa 188,248,216\nCV 219,60,61\nMvaa 188,252,220\nAR 60,56\nD0 8,32,46,2048\n"
"Mvaa 156,252,212\nAR 61,57\nCV 220,60,61\nMvaa 156,248,208\nAR 60,58\nMvaa 124,248,200\nAR 61,59\nCV 221,60,61\nMvaa 124,252,204\nAR 60,60\nD0 9,32,46,3072\nMvaa 92,252,196\n"
"AR 61,61\nCV 222,60,61\nMvaa 92,248,192\nWL 0\nMvaa 64,160,128\nTa 224,16,32768\nAR 60,62\nMvaa 64,164,132\nTa 226,16,34816\ns_add_u32 m0, s47, -32768\ns_add_u32 s46, s52, 640\n"
"D0 40,28,46,0\nMvaa 96,164,140\nAR 61,63\nMvaa 96,160,136\nTa 228,16,49152\nCV 223,60,61\nMvaa 128,160,144\nTa 230,16,51200\nMvaa 128,164,148\nD0 41,28,46,1024\nMvaa 160,164,156\n"
"Ta 232,17,32768\nAR 60,126\nMvaa 160,160,152\nTa 234,17,34816\nMvaa 164,168,152\nAR 61,127\nCV 60,60,61\nMvaa 164,172,156\nTa 236,17,49152\nD0 42,28,46,2048\nMvaa 132,172,148\n"
"Ta 238,17,51200\nMvaa 132,168,144\nAW 127,60\nAR 60,124\nMvaa 100,168,136\nTa 240,18,32768\nMvaa 100,172,140\nTa 242,18,34816\nD0 43,28,46,3072\nMvaa 68,172,132\nSv 192,1,16,77,0\n"
"AR 61,125\nMvaa 68,168,128\nTa 244,18,49152\nMvaa 72,176,128\nTa 246,18,51200\nCV 60,60,61\nMvaa 72,180,132\nAW 126,60\nMvaa 104,180,140\nTa 248,19,32768\nMvaa 104,176,136\n"
"Ta 250,19,34816\nSv 196,1,16,77,1024\nMvaa 136,176,144\nAR 60,122\nMvaa 136,180,148\nTa 252,19,49152\nMvaa 168,180,156\nTa 254,19,51200\nAR 61,123\nMvaa 168,176,152\nCV 60,60,61\n"
"Mvaa 172,184,152\nSv 200,1,16,77,2048\nAW 125,60\nMvaa 172,188,156\nAR 60,120\nMvaa 140,188,148\nAR 61,121\nMvaa 140,184,144\nCV 60,60,61\nAW 124,60\nMvaa 108,184,136\nAR 60,118\n"
"Mvaa 108,188,140\nSv 204,1,16,77,3072\nMvaa 76,188,132\nAR 61,119\nCV 60,60,61\nMvaa 76,184,128\ns_waitcnt vmcnt(17) lgkmcnt(0)\ns_barrier\nMvaa 80,224,128\nRa 192,44,49152\n"
"AW 123,60\nMvaa 80,228,132\nRa 196,45,49152\nbuffer_store_dword v25, off, s[16:19], s54 offset:8 sc0 sc1\ns_add_u32 m0, s47, 32768\ns_add_u32 s46, s51, 2621440\nD0 6,32,46,0\n"
"Mvaa 112,228,140\nRa 200,44,51200\nMvaa 112,224,136\nRa 204,45,51200\nAR 60,116\nMvaa 144,224,144\nRa 208,46,49152\nMvaa 144,228,148\nRa 212,47,49152\nD0 7,32,46,1024\n"
"Mvaa 176,228,156\nRa 216,46,51200\nAR 61,117\nMvaa 176,224,152\nRa 220,47,51200\nMvaa 180,232,152\nTa 160,32,0\nCV 60,60,61\nMvaa 180,236,156\nTa 162,32,2048\nD0 8,32,46,2048\n"
"Mvaa 148,236,148\nTa 164,32,16384\nMvaa 148,232,144\nTa 166,32,18432\nAW 122,60\nMvaa 116,232,136\nTa 168,33,0\nMvaa 116,236,140\nTa 170,33,2048\nD0 9,32,46,3072\nMvaa 84,236,132\n"
"Ta 172,33,16384\nAR 60,114\nMvaa 84,232,128\nTa 174,33,18432\ns_add_u32 m0, s47, 49152\ns_add_u32 s46, s51, 2883584\nMvaa 88,240,128\nTa 176,34,0\n.W576:\ns_cbranch_execz .W580\n"
"AR 61,115\nMvaa 88,244,132\nTa 178,34,2048\nD0 6,32,46,0\nMvaa 120,244,140\nTa 180,34,16384\nMvaa 120,240,136\nTa 182,34,18432\nCV 60,60,61\nMvaa 152,240,144\nTa 184,35,0\n"
"Mvaa 152,244,148\nTa 186,35,2048\nD0 7,32,46,1024\nMvaa 184,244,156\nTa 188,35,16384\nAW 121,60\nMvaa 184,240,152\nTa 190,35,18432\nMvaa 188,248,152\nAR 60,112\nAR 61,113\n"
"Mvaa 188,252,156\nD0 8,32,46,2048\nMvaa 156,252,148\nCV 60,60,61\nMvaa 156,248,144\nAW 120,60\nAR 60,110\nMvaa 124,248,136\nAR 61,111\nMvaa 124,252,140\nD0 9,32,46,3072\n"
"Mvaa 92,252,132\nCV 60,60,61\nAW 119,60\nMvaa 92,248,128\nWL 0\nMvaa 64,160,192\nTa 224,36,0\nAR 60,108\nMvaa 64,164,196\nTa 226,36,2048\ns_add_u32 m0, s47, -49152\n"
"s_add_u32 s46, s52, 768\nD0 40,28,46,0\nMvaa 96,164,204\nAR 61,109\nMvaa 96,160,200\nTa 228,36,16384\nCV 60,60,61\nMvaa 128,160,208\nTa 230,36,18432\nMvaa 128,164,212\n"
"D0 41,28,46,1024\nMvaa 160,164,220\nTa 232,37,0\nAW 118,60\nMvaa 160,160,216\nTa 234,37,2048\nMvaa 164,168,216\nAR 60,106\nAR 61,107\nMvaa 164,172,220\nTa 236,37,16384\n"
"D0 42,28,46,2048\nMvaa 132,172,212\nTa 238,37,18432\nMvaa 132,168,208\nCV 60,60,61\nAW 117,60\nMvaa 100,168,200\nTa 240,38,0\nMvaa 100,172,204\nTa 242,38,2048\nD0 43,28,46,3072\n"
"Mvaa 68,172,196\ns_add_u32 s77, s77, s55\nSv 208,1,16,77,0\nMvaa 68,168,192\nTa 244,38,16384\nMvaa 72,176,192\nTa 246,38,18432\nAR 60,104\nMvaa 72,180,196\nAR 61,105\n"
"Mvaa 104,180,204\nTa 248,39,0\nMvaa 104,176,200\nTa 250,39,2048\nSv 212,1,16,77,1024\nMvaa 136,176,208\nCV 60,60,61\nMvaa 136,180,212\nTa 252,39,16384\nMvaa 168,180,220\n"
"Ta 254,39,18432\nAW 116,60\nMvaa 168,176,216\nAR 60,102\nMvaa 172,184,216\nSv 216,1,16,77,2048\nAR 61,103\nMvaa 172,188,220\nCV 60,60,61\nMvaa 140,188,212\nAW 115,60\n"
"Mvaa 140,184,208\nAR 60,100\nAR 61,101\nMvaa 108,184,200\nCV 60,60,61\nMvaa 108,188,204\nSv 220,1,16,77,3072\nMvaa 76,188,196\nAW 114,60\nAR 60,98\nMvaa 76,184,192\n"
"s_waitcnt vmcnt(25) lgkmcnt(0)\ns_barrier\nMvaa 80,224,192\nRa 128,44,0\nv_and_b32 v27, 0x70, v1\nbuffer_load_dword v27, v27, s[16:19], s61 offen offset:128 sc0 sc1\n"
"Mvaa 80,228,196\nRa 132,45,0\ns_add_u32 m0, s47, 65536\ns_add_u32 s46, s51, 3145728\nD0 6,32,46,0\nMvaa 112,228,204\nRa 136,44,2048\nMvaa 112,224,200\nRa 140,45,2048\nAR 61,99\n"
"Mvaa 144,224,208\nRa 144,46,0\nMvaa 144,228,212\nRa 148,47,0\nD0 7,32,46,1024\nMvaa 176,228,220\nRa 152,46,2048\nCV 60,60,61\nMvaa 176,224,216\nRa 156,47,2048\nMvaa 180,232,216\n"
"Ta 160,12,0\nAW 113,60\nMvaa 180,236,220\nTa 162,12,2048\nD0 8,32,46,2048\nMvaa 148,236,212\nTa 164,12,16384\nMvaa 148,232,208\nTa 166,12,18432\nAR 60,96\nMvaa 116,232,200\n"
"Ta 168,13,0\nMvaa 116,236,204\nTa 170,13,2048\nD0 9,32,46,3072\nMvaa 84,236,196\nTa 172,13,16384\nAR 61,97\nMvaa 84,232,192\nTa 174,13,18432\ns_add_u32 m0, s47, 81920\n"
"s_add_u32 s46, s51, 3407872\nMvaa 88,240,192\nTa 176,14,0\nCV 60,60,61\nMvaa 88,244,196\nTa 178,14,2048\nD0 6,32,46,0\nMvaa 120,244,204\nTa 180,14,16384\nMvaa 120,240,200\n"
"Ta 182,14,18432\nAW 112,60\nMvaa 152,240,208\nTa 184,15,0\nMvaa 152,244,212\nTa 186,15,2048\nD0 7,32,46,1024\nMvaa 184,244,220\nTa 188,15,16384\nAR 60,94\nMvaa 184,240,216\n"
"Ta 190,15,18432\nMvaa 188,248,216\nAR 61,95\nCV 60,60,61\nMvaa 188,252,220\nD0 8,32,46,2048\nMvaa 156,252,212\nAW 111,60\nMvaa 156,248,208\nAR 60,92\nAR 61,93\nMvaa 124,248,200\n"
"CV 60,60,61\nMvaa 124,252,204\nD0 9,32,46,3072\nMvaa 92,252,196\nAW 110,60\nAR 60,90\nMvaa 92,248,192\nWL 0\nMvaa 64,160,128\nTa 224,16,0\nAR 61,91\nMvaa 64,164,132\n"
"Ta 226,16,2048\ns_add_u32 s6, s56, 16384\nbuffer_load_dwordx4 v[224:227], v1, s[16:19], s6 offen offset:0 sc1\nMvaa 96,164,140\nCV 60,60,61\nMvaa 96,160,136\nTa 228,16,16384\n"
"AW 109,60\nMvaa 128,160,144\nTa 230,16,18432\nMvaa 128,164,148\nbuffer_load_dwordx4 v[228:231], v1, s[16:19], s6 offen offset:1024 sc1\nMvaa 160,164,156\nTa 232,17,0\nAR 60,88\n"
"Mvaa 160,160,152\nTa 234,17,2048\nMvaa 164,168,152\nAR 61,89\nCV 60,60,61\nMvaa 164,172,156\nTa 236,17,16384\n"
"buffer_load_dwordx4 v[232:235], v1, s[16:19], s6 offen offset:2048 sc1\nMvaa 132,172,148\nTa 238,17,18432\nMvaa 132,168,144\nAW 108,60\nAR 60,86\nMvaa 100,168,136\nTa 240,18,0\n"
"Mvaa 100,172,140\nTa 242,18,2048\nbuffer_load_dwordx4 v[236:239], v1, s[16:19], s6 offen offset:3072 sc1\nMvaa 68,172,132\nAR 61,87\nCV 60,60,61\nMvaa 68,168,128\nTa 244,18,16384\n"
"Mvaa 72,176,128\nTa 246,18,18432\nAW 107,60\nMvaa 72,180,132\nAR 60,84\nMvaa 104,180,140\nTa 248,19,0\nMvaa 104,176,136\nTa 250,19,2048\nAR 61,85\nMvaa 136,176,144\nCV 60,60,61\n"
"Mvaa 136,180,148\nTa 252,19,16384\nMvaa 168,180,156\nTa 254,19,18432\nAW 106,60\nMvaa 168,176,152\nAR 60,82\nMvaa 172,184,152\nAR 61,83\nCV 60,60,61\nMvaa 172,188,156\nAW 105,60\n"
"Mvaa 140,188,148\nAR 60,80\nMvaa 140,184,144\nAR 61,81\nCV 60,60,61\nMvaa 108,184,136\nAW 104,60\nMvaa 108,188,140\nAR 60,78\nMvaa 76,188,132\nAR 61,79\nCV 60,60,61\n"
"Mvaa 76,184,128\ns_waitcnt vmcnt(12) lgkmcnt(0)\ns_barrier\nMvaa 80,224,128\nRa 192,44,32768\nv_cmp_ne_u32 vcc, s15, v27\ns_nop 0\ns_cbranch_vccnz .S287\n.S288:\n"
"s_sub_u32 s45, s86, 131072\ns_cmp_eq_u32 s79, 1\ns_cselect_b32 s86, s94, s86\ns_add_u32 m0, s47, -65536\ns_add_u32 s46, s45, 0x0\nMvaa 80,228,132\nRa 196,45,32768\n"
"Mvaa 112,228,140\nRa 200,44,34816\nD1 1,16,46,0\nMvaa 112,224,136\nRa 204,45,34816\nAW 103,60\nMvaa 144,224,144\nRa 208,46,32768\nMvaa 144,228,148\nRa 212,47,32768\n"
"Mvaa 176,228,156\nRa 216,46,34816\nD1 1,16,46,1024\nMvaa 176,224,152\nRa 220,47,34816\nMvaa 180,232,152\nTa 160,12,32768\nAR 60,76\nMvaa 180,236,156\nTa 162,12,34816\n"
"Mvaa 148,236,148\nTa 164,12,49152\nD1 1,16,46,2048\nMvaa 148,232,144\nTa 166,12,51200\nAR 61,77\nMvaa 116,232,136\nTa 168,13,32768\nMvaa 116,236,140\nTa 170,13,34816\n"
"Mvaa 84,236,132\nTa 172,13,49152\nD1 1,16,46,3072\nMvaa 84,232,128\nTa 174,13,51200\nMvaa 88,240,128\nTa 176,14,32768\ns_add_u32 s6, s56, 20480\nMvaa 88,244,132\nTa 178,14,34816\n"
"Mvaa 120,244,140\nTa 180,14,49152\nbuffer_load_dwordx4 v[240:243], v1, s[16:19], s6 offen offset:0 sc1\nMvaa 120,240,136\nTa 182,14,51200\nCV 60,60,61\nMvaa 152,240,144\n"
"Ta 184,15,32768\nMvaa 152,244,148\nTa 186,15,34816\nMvaa 184,244,156\nTa 188,15,49152\nbuffer_load_dwordx4 v[244:247], v1, s[16:19], s6 offen offset:1024 sc1\nMvaa 184,240,152\n"
"Ta 190,15,51200\nMvaa 188,248,152\nAW 102,60\nAR 60,74\nMvaa 188,252,156\nAR 61,75\nMvaa 156,252,148\nbuffer_load_dwordx4 v[248:251], v1, s[16:19], s6 offen offset:2048 sc1\n"
"Mvaa 156,248,144\nCV 60,60,61\nAW 101,60\nMvaa 124,248,136\nAR 60,72\nMvaa 124,252,140\nAR 61,73\nMvaa 92,252,132\n"
"buffer_load_dwordx4 v[252:255], v1, s[16:19], s6 offen offset:3072 sc1\nCV 60,60,61\nMvaa 92,248,128\nWL 0\nMvaa 64,160,192\nTa 224,16,32768\ns_add_u32 s6, s56, 0\n"
"Mvaa 64,164,196\nTa 226,16,34816\nMvaa 96,164,204\nbuffer_load_dwordx4 v[192:195], v1, s[16:19], s6 offen offset:0 sc1\nMvaa 96,160,200\nTa 228,16,49152\nAW 100,60\n"
"Mvaa 128,160,208\nTa 230,16,51200\nMvaa 128,164,212\nAR 60,70\nMvaa 160,164,220\nTa 232,17,32768\nbuffer_load_dwordx4 v[196:199], v1, s[16:19], s6 offen offset:1024 sc1\n"
"Mvaa 160,160,216\nTa 234,17,34816\nMvaa 164,168,216\nAR 61,71\nCV 60,60,61\nMvaa 164,172,220\nTa 236,17,49152\nMvaa 132,172,212\nTa 238,17,51200\n"
"buffer_load_dwordx4 v[200:203], v1, s[16:19], s6 offen offset:2048 sc1\nMvaa 132,168,208\nAW 99,60\nAR 60,68\nMvaa 100,168,200\nTa 240,18,32768\nMvaa 100,172,204\nTa 242,18,34816\n"
"Mvaa 68,172,196\nbuffer_load_dwordx4 v[204:207], v1, s[16:19], s6 offen offset:3072 sc1\nAR 61,69\nMvaa 68,168,192\nTa 244,18,49152\nMvaa 72,176,192\nTa 246,18,51200\nCV 60,60,61\n"
"Mvaa 72,180,196\nAW 98,60\nMvaa 104,180,204\nTa 248,19,32768\nMvaa 104,176,200\nTa 250,19,34816\nAR 60,66\nMvaa 136,176,208\nAR 61,67\nMvaa 136,180,212\nTa 252,19,49152\n"
"Mvaa 168,180,220\nTa 254,19,51200\nCV 60,60,61\nMvaa 168,176,216\nAW 97,60\nMvaa 172,184,216\nAR 60,64\nAR 61,65\nMvaa 172,188,220\nCV 60,60,61\nMvaa 140,188,212\nAW 96,60\n"
"v_add_u32 v2, 0x0, v1\nMvaa 140,184,208\ns_lshr_b32 s2, s59, 1\nMvaa 108,184,200\nv_add_u32 v24, s2, v2\nv_xor_b32 v26, 0x1000, v24\nMvaa 108,188,204\nv_lshrrev_b32 v20, 4, v0\n"
"Mvaa 76,188,196\nv_lshlrev_b32 v20, 2, v20\nv_and_b32 v0, 15, v0\nMvaa 76,184,192\ns_waitcnt vmcnt(16) lgkmcnt(0)\ns_barrier\nMvaa 80,224,192\nRa 128,44,16384\n"
"v_sub_u32 v0, v0, v20\nMvaa 80,228,196\nRa 132,45,16384\nbuffer_store_dword v25, off, s[16:19], s54 offset:12 sc0 sc1\ns_add_u32 s6, s56, 4096\n"
"buffer_load_dwordx4 v[208:211], v1, s[16:19], s6 offen offset:0 sc1\nMvaa 112,228,204\nRa 136,44,18432\ns_lshl_b32 s2, s11, 4\nMvaa 112,224,200\nRa 140,45,18432\nMvaa 144,224,208\n"
"Ra 144,46,16384\nv_add_u32 v3, s2, v0\nMvaa 144,228,212\nRa 148,47,16384\nbuffer_load_dwordx4 v[212:215], v1, s[16:19], s6 offen offset:1024 sc1\nMvaa 176,228,220\n"
"Ra 152,46,18432\nv_subrev_u32 v0, 32, v3\nMvaa 176,224,216\nRa 156,47,18432\nMvaa 180,232,216\nTa 160,32,0\ns_lshl_b32 s2, s11, 13\nMvaa 180,236,220\nTa 162,32,2048\n"
"buffer_load_dwordx4 v[216:219], v1, s[16:19], s6 offen offset:2048 sc1\nMvaa 148,236,212\nTa 164,32,16384\ns_add_u32 s2, s2, 0x20000\nMvaa 148,232,208\nTa 166,32,18432\n"
"Mvaa 116,232,200\nTa 168,33,0\nv_add_u32 v22, s2, v1\nMvaa 116,236,204\nTa 170,33,2048\nbuffer_load_dwordx4 v[220:223], v1, s[16:19], s6 offen offset:3072 sc1\nMvaa 84,236,196\n"
"Ta 172,33,16384\nv_xor_b32 v23, 0x2000, v22\nMvaa 84,232,192\nTa 174,33,18432\ns_add_u32 m0, s47, -32768\ns_add_u32 s46, s45, 0x8000\nMvaa 88,240,192\nTa 176,34,0\n"
"v_mov_b32 v4, 0\nMvaa 88,244,196\nTa 178,34,2048\nD1 1,16,46,0\nMvaa 120,244,204\nTa 180,34,16384\nv_mov_b32 v5, 0\nMvaa 120,240,200\nTa 182,34,18432\nMvaa 152,240,208\n"
"Ta 184,35,0\nv_mov_b32 v6, 0\nMvaa 152,244,212\nTa 186,35,2048\nD1 1,16,46,1024\nMvaa 184,244,220\nTa 188,35,16384\nv_mov_b32 v7, 0\nMvaa 184,240,216\nTa 190,35,18432\n"
"Mvaa 188,248,216\nv_mov_b32 v8, 0\nv_mov_b32 v9, 0\nMvaa 188,252,220\nD1 1,16,46,2048\nMvaa 156,252,212\n.W577:\ns_cbranch_execz .W581\nv_mov_b32 v10, 0\nv_mov_b32 v11, 0\n"
"Mvaa 156,248,208\nv_mov_b32 v12, 0\nMvaa 124,248,200\nv_mov_b32 v13, 0\nv_mov_b32 v14, 0\nMvaa 124,252,204\nD1 1,16,46,3072\nMvaa 92,252,196\nv_mov_b32 v15, 0\nv_mov_b32 v16, 0\n"
"Mvaa 92,248,192\nWL 0\nMvaa 64,160,128\nTa 224,36,0\nv_mov_b32 v17, 0\nMvaa 64,164,132\nTa 226,36,2048\ns_add_u32 m0, s47, -16384\ns_add_u32 s46, s45, 0x400000\nD1 1,16,46,0\n"
"Mvaa 68,172,132\nv_mov_b32 v18, 0\nv_mov_b32 v19, 0\nMvaa 68,168,128\nTa 228,36,16384\nMvaa 72,176,128\nTa 230,36,18432\nMvaa 72,180,132\nD1 1,16,46,1024\nMvaa 76,188,132\n"
"Ta 232,37,0\nMvaa 76,184,128\nTa 234,37,2048\nMvaa 108,184,136\nv_mul_f32 v64, s57, v64\nv_mul_f32 v65, s57, v65\nMvaa 108,188,140\nTa 236,37,16384\nD1 1,16,46,2048\n"
"Mvaa 104,180,140\nTa 238,37,18432\nCV 64,64,65\nMvaa 104,176,136\nv_mul_f32 v66, s57, v66\nv_mul_f32 v67, s57, v67\nMvaa 100,168,136\nTa 240,38,0\nCV 65,66,67\nMvaa 100,172,140\n"
"Ta 242,38,2048\nD1 1,16,46,3072\nMvaa 96,164,140\nv_mul_f32 v68, s57, v68\nv_mul_f32 v69, s57, v69\nMvaa 96,160,136\nTa 244,38,16384\nCV 66,68,69\nMvaa 128,160,144\n"
"Ta 246,38,18432\nv_mul_f32 v70, s57, v70\nMvaa 128,164,148\nv_mul_f32 v71, s57, v71\nMvaa 132,172,148\nTa 248,39,0\nCV 67,70,71\nMvaa 132,168,144\nTa 250,39,2048\nAW 192,64\n"
"Mvaa 136,176,144\nAW 193,65\nAW 194,66\nMvaa 136,180,148\nTa 252,39,16384\nAW 195,67\nMvaa 140,188,148\nTa 254,39,18432\nv_mul_f32 v72, s57, v72\nMvaa 140,184,144\n"
"v_mul_f32 v73, s57, v73\nCV 72,72,73\nMvaa 172,184,152\nv_mul_f32 v74, s57, v74\nv_mul_f32 v75, s57, v75\nMvaa 172,188,156\nCV 73,74,75\nv_mul_f32 v76, s57, v76\nMvaa 168,180,156\n"
"v_mul_f32 v77, s57, v77\nCV 74,76,77\nMvaa 168,176,152\nv_mul_f32 v78, s57, v78\nv_mul_f32 v79, s57, v79\nMvaa 164,168,152\nCV 75,78,79\nAW 200,72\nMvaa 164,172,156\nAW 201,73\n"
"AW 202,74\nMvaa 160,164,156\nAW 203,75\nv_mul_f32 v96, s57, v96\nMvaa 160,160,152\ns_waitcnt vmcnt(21) lgkmcnt(0)\ns_barrier\nMvaa 80,224,128\nv_mul_f32 v97, s57, v97\n"
"CV 96,96,97\nMvaa 80,228,132\ns_add_u32 m0, s47, 0\ns_add_u32 s46, s86, 0xffff0000\nD1 1,16,46,0\nMvaa 84,236,132\nv_mul_f32 v98, s57, v98\nv_mul_f32 v99, s57, v99\n"
"Mvaa 84,232,128\nCV 97,98,99\nv_mul_f32 v100, s57, v100\nMvaa 88,240,128\nv_mul_f32 v101, s57, v101\nCV 98,100,101\nMvaa 88,244,132\nD1 1,16,46,1024\nv_mul_f32 v102, s57, v102\n"
"Mvaa 92,252,132\nv_mul_f32 v103, s57, v103\nCV 99,102,103\nMvaa 92,248,128\nAW 196,96\nAW 197,97\nMvaa 124,248,136\nAW 198,98\nAW 199,99\nMvaa 124,252,140\nD1 1,16,46,2048\n"
"v_mul_f32 v128, s57, v128\nMvaa 120,244,140\nv_mul_f32 v129, s57, v129\nCV 128,128,129\nMvaa 120,240,136\nv_mul_f32 v130, s57, v130\nv_mul_f32 v131, s57, v131\nMvaa 116,232,136\n"
"Ra 160,24,0\nCV 129,130,131\nMvaa 116,236,140\nD1 1,16,46,3072\nv_mul_f32 v132, s57, v132\nMvaa 112,228,140\nRa 164,24,1024\nv_mul_f32 v133, s57, v133\nMvaa 112,224,136\n"
"s_add_u32 m0, s47, 16384\ns_add_u32 s46, s45, 0x408000\nMvaa 144,224,144\nRa 168,24,2048\nCV 130,132,133\nMvaa 144,228,148\nD1 1,16,46,0\nMvaa 148,236,148\nRa 172,24,3072\n"
"v_mul_f32 v134, s57, v134\nMvaa 148,232,144\nv_mul_f32 v135, s57, v135\nCV 131,134,135\nMvaa 152,240,144\nRa 176,26,0\nds_write_b128 v23, v[128:131] offset:0\nMvaa 152,244,148\n"
"D1 1,16,46,1024\nv_mul_f32 v80, s57, v80\nMvaa 156,252,148\nRa 180,26,1024\nv_mul_f32 v81, s57, v81\nMvaa 156,248,144\nCV 80,80,81\nv_mul_f32 v82, s57, v82\nMvaa 188,248,152\n"
"Ra 184,26,2048\nv_mul_f32 v83, s57, v83\nMvaa 188,252,156\nD1 1,16,46,2048\nCV 81,82,83\nMvaa 184,244,156\nRa 188,26,3072\nv_mul_f32 v84, s57, v84\nMvaa 184,240,152\n"
"v_mul_f32 v85, s57, v85\nCV 82,84,85\nMvaa 180,232,152\nv_mul_f32 v86, s57, v86\nv_mul_f32 v87, s57, v87\nMvaa 180,236,156\nD1 1,16,46,3072\nCV 83,86,87\nMvaa 176,228,156\n"
"AW 208,80\nAW 209,81\nMvaa 176,224,152\ns_waitcnt vmcnt(16) lgkmcnt(5)\nCvav 32,160,192,4\nRa 128,24,8192\nAW 210,82\nMvav 32,164,200\nAW 211,83\nMvav 32,168,208\nRa 132,24,9216\n"
"v_mul_f32 v88, s57, v88\nMvav 32,172,216\nv_mul_f32 v89, s57, v89\nCV 88,88,89\nCvav 36,172,220,8\nRa 136,24,10240\nv_mul_f32 v90, s57, v90\nMvav 36,168,212\n"
"s_add_u32 m0, s89, 32768\nv_mul_f32 v91, s57, v91\nMvav 36,164,204\nRa 140,24,11264\nWL 4\ns_barrier\nMvav 36,160,196\nCV 89,90,91\nv_mul_f32 v92, s57, v92\nMvav 36,176,228\n"
"Ra 144,26,8192\nSa 96,1,16,78,0\nMvav 36,180,236\ns_add_u32 s46, s86, s88\nv_mul_f32 v93, s57, v93\nMvav 36,184,244\nRa 148,26,9216\nCV 90,92,93\nMvav 36,188,252\n"
"v_mul_f32 v94, s57, v94\nv_mul_f32 v95, s57, v95\nMvav 32,188,248\nRa 152,26,10240\nCV 91,94,95\nMvav 32,184,240\nD1 1,16,46,0\nAW 216,88\nMvav 32,180,232\nRa 156,26,11264\nWL 4\n"
"Mvav 32,176,224\nAW 217,89\nAW 218,90\nCvav 40,128,192,12\nSa 100,1,16,78,1024\nAW 219,91\nMvav 40,132,200\nv_mov_b32 v86, 0\nMvav 40,136,208\nv_mov_b32 v87, 0\nv_mov_b32 v88, 0\n"
"Mvav 40,140,216\nv_mov_b32 v89, 0\nv_mul_f32 v136, s57, v136\nv_mul_f32 v137, s57, v137\nCV 136,136,137\nv_mul_f32 v138, s57, v138\nv_mul_f32 v139, s57, v139\nCV 137,138,139\n"
"Cvav 44,140,220,16\nv_and_b32 v27, 0x70, v1\nbuffer_load_dword v27, v27, s[16:19], s61 offen offset:136 sc0 sc1\nv_mul_f32 v140, s57, v140\nv_mul_f32 v141, s57, v141\n"
"CV 138,140,141\nv_mul_f32 v142, s57, v142\nv_mul_f32 v143, s57, v143\nMvav 44,136,212\nD1 1,16,46,1024\nCV 139,142,143\nds_write_b128 v23, v[136:139] offset:2048\n"
"v_mul_f32 v144, s57, v144\nv_mul_f32 v145, s57, v145\nCV 144,144,145\nv_mul_f32 v146, s57, v146\nMvav 44,132,204\nWL 1\nv_mul_f32 v147, s57, v147\nCV 145,146,147\n"
"v_mul_f32 v148, s57, v148\nv_mul_f32 v149, s57, v149\nCV 146,148,149\nMvav 44,128,196\nv_mul_f32 v150, s57, v150\nv_mul_f32 v151, s57, v151\nCV 147,150,151\n"
"ds_write_b128 v23, v[144:147] offset:4096\nv_mul_f32 v152, s57, v152\nv_mul_f32 v153, s57, v153\nCV 152,152,153\nMvav 44,144,228\nSa 104,1,16,78,2048\nv_mul_f32 v154, s57, v154\n"
"v_mul_f32 v155, s57, v155\nCV 153,154,155\nv_mul_f32 v156, s57, v156\nv_mul_f32 v157, s57, v157\nCV 154,156,157\nMvav 44,148,236\ns_add_u32 s88, s88, 32768\nEX 80,32\n"
"v_mul_f32 v158, s57, v158\nv_mul_f32 v159, s57, v159\nCV 155,158,159\nds_write_b128 v23, v[152:155] offset:6144\nMvav 44,152,244\nv_mul_f32 v160, s57, v160\n"
"v_mul_f32 v161, s57, v161\nCV 160,160,161\nv_mul_f32 v162, s57, v162\nv_mul_f32 v163, s57, v163\nCV 161,162,163\nv_mul_f32 v164, s57, v164\nMvav 44,156,252\nEX 81,33\n"
"v_mul_f32 v165, s57, v165\nCV 162,164,165\nv_mul_f32 v166, s57, v166\nMvav 40,156,248\nv_mul_f32 v167, s57, v167\nCV 163,166,167\nds_write_b128 v23, v[160:163] offset:1024\n"
"v_mul_f32 v168, s57, v168\nv_mul_f32 v169, s57, v169\nCV 168,168,169\nv_mul_f32 v170, s57, v170\nMvav 40,152,240\nD1 1,16,46,2048\nv_mul_f32 v171, s57, v171\nCV 169,170,171\n"
"v_mul_f32 v172, s57, v172\nv_mul_f32 v173, s57, v173\nCV 170,172,173\nv_mul_f32 v174, s57, v174\nMvav 40,148,232\nv_mul_f32 v175, s57, v175\nCV 171,174,175\n"
"ds_write_b128 v23, v[168:171] offset:3072\nv_mul_f32 v176, s57, v176\nv_mul_f32 v177, s57, v177\nCV 176,176,177\nv_mul_f32 v178, s57, v178\nMvav 40,144,224\nAD 84,80,81\n"
"CV 64,80,81\nv_mul_f32 v179, s57, v179\nCV 177,178,179\nv_mul_f32 v180, s57, v180\nv_mul_f32 v181, s57, v181\nCV 178,180,181\nSa 108,1,16,78,3072\nv_mul_f32 v182, s57, v182\n"
"v_mul_f32 v183, s57, v183\nCV 179,182,183\nds_write_b128 v23, v[176:179] offset:5120\nv_mul_f32 v184, s57, v184\nv_mul_f32 v185, s57, v185\nEX 82,34\nCV 184,184,185\n"
"v_mul_f32 v186, s57, v186\nv_mul_f32 v187, s57, v187\nCV 185,186,187\nv_mul_f32 v188, s57, v188\nv_mul_f32 v189, s57, v189\nCV 186,188,189\nv_mul_f32 v190, s57, v190\n"
"v_mul_f32 v191, s57, v191\nCV 187,190,191\nds_write_b128 v23, v[184:187] offset:7168\ns_waitcnt vmcnt(16) lgkmcnt(0)\nEX 83,35\nAD 84,84,82\nD1 1,16,46,3072\n"
"s_add_u32 m0, s89, 49152\ns_barrier\nAD 84,84,83\nCV 65,82,83\nRa 128,24,32768\nRa 224,22,0\nRa 228,22,1024\ns_add_u32 s46, s87, s88\nEX 80,36\nRa 132,24,33792\nRa 232,22,2048\n"
"Ra 236,22,3072\nRa 240,22,4096\nEX 81,37\nRa 136,24,34816\nRa 244,22,5120\nRa 248,22,6144\nD1 1,16,46,0\nRa 252,22,7168\nv_mul_f32 v104, s57, v104\nv_mul_f32 v105, s57, v105\n"
"Ra 140,24,35840\nCV 104,104,105\nv_mul_f32 v106, s57, v106\nAD 85,80,81\nCV 68,80,81\nv_mul_f32 v107, s57, v107\nRa 144,26,32768\nCV 105,106,107\nv_mul_f32 v108, s57, v108\n"
"v_mul_f32 v109, s57, v109\nEX 82,38\nCV 106,108,109\nRa 148,26,33792\nv_mul_f32 v110, s57, v110\nv_mul_f32 v111, s57, v111\nEX 83,39\nRa 152,26,34816\nAD 85,85,82\nCV 107,110,111\n"
"AW 204,104\nD1 1,16,46,1024\nAW 205,105\nAW 206,106\nRa 156,26,35840\nAW 207,107\nv_mul_f32 v112, s57, v112\nAD 85,85,83\nCV 69,82,83\nv_mul_f32 v113, s57, v113\nCV 112,112,113\n"
"Ra 160,24,40960\nv_mul_f32 v114, s57, v114\nv_mul_f32 v115, s57, v115\ns_xor_b32 s89, s89, 0x10000\nEX 80,40\nRa 164,24,41984\nCV 113,114,115\nv_mul_f32 v116, s57, v116\n"
"v_mul_f32 v117, s57, v117\nWL 6\nEX 81,41\nRa 168,24,43008\nAD 84,84,80\nCV 114,116,117\nD1 1,16,46,2048\nv_mul_f32 v118, s57, v118\nv_mul_f32 v119, s57, v119\nCV 115,118,119\n"
"Ra 172,24,44032\nAW 212,112\nAW 213,113\nAD 84,84,81\nCV 66,80,81\nAW 214,114\nCvav 48,128,192,4\nRa 176,26,40960\nAW 215,115\nv_mul_f32 v120, s57, v120\n"
"v_mul_f32 v121, s57, v121\nMvav 48,132,200\nEX 82,42\nCV 120,120,121\nMvav 48,136,208\nRa 180,26,41984\nv_mul_f32 v122, s57, v122\nv_mul_f32 v123, s57, v123\nMvav 48,140,216\n"
"WL 6\nEX 83,43\nWV 8\nv_cmp_ne_u32 vcc, s15, v27\ns_nop 0\ns_cbranch_vccnz .S290\n.S291:\nCvav 52,140,220,8\nRa 184,26,43008\nAD 84,84,82\nCV 121,122,123\n"
"v_mul_f32 v124, s57, v124\nMvav 52,136,212\nD1 1,16,46,3072\ns_add_u32 m0, s89, 0\nv_mul_f32 v125, s57, v125\nMvav 52,132,204\nRa 188,26,44032\nCV 122,124,125\n"
"v_mul_f32 v126, s57, v126\nMvav 52,128,196\nAD 84,84,83\nCV 67,82,83\nv_mul_f32 v127, s57, v127\nCV 123,126,127\nMvav 52,144,228\nRa 128,2,49152\ns_add_u32 s78, s78, s55\n"
"Sa 112,1,16,78,0\nMvav 52,148,236\ns_add_u32 s46, s86, s88\nEX 80,44\nMvav 52,152,244\nRa 132,2,50176\nv_xor_b32 v24, 0x10000, v24\nAW 220,120\nAW 221,121\nMvav 52,156,252\nWL 6\n"
"EX 81,45\nMvav 48,156,248\nRa 136,2,51200\nAD 85,85,80\nAW 222,122\nMvav 48,152,240\nD1 1,16,46,0\nAW 223,123\nv_mov_b32 v112, -1\nv_mov_b32 v113, -1\nMvav 48,148,232\n"
"Ra 140,2,52224\nv_mov_b32 v114, -1\nv_mov_b32 v115, -1\nMvav 48,144,224\nAD 85,85,81\nCV 70,80,81\nCvav 56,160,192,12\nRa 144,2,53248\nSa 116,1,16,78,1024\nMvav 56,164,200\n"
"EX 82,46\nMvav 56,168,208\nRa 148,2,54272\nv_xor_b32 v26, 0x10000, v26\nMvav 56,172,216\nWL 6\nEX 83,47\nCvav 60,172,220,16\nRa 152,2,55296\nAD 85,85,82\nMvav 60,168,212\n"
"D1 1,16,46,1024\nMvav 60,164,204\nRa 156,2,56320\nMvav 60,160,196\nAD 85,85,83\nCV 71,82,83\nMvav 60,176,228\nRa 160,2,57344\nv_max_f32 v90, v84, v85\nMvav 60,180,236\n"
"s_add_u32 s88, s88, 32768\nEX 80,48\nMvav 60,184,244\nRa 164,2,58368\nv_cmp_lt_f32 vcc, s63, v90\nMvav 60,188,252\nWL 8\nEX 81,49\nMvav 56,188,248\nRa 168,2,59392\n"
"s_cbranch_vccnz .T279\n.T280:\nv_add_f32 v20, s93, v84\nv_mov_b32 v21, v85\nv_mov_b32 v22, v20\nv_mov_b32 v23, v21\nMvav 56,184,240\nD1 1,16,46,2048\n"
"v_permlane32_swap_b32 v20, v22\nv_permlane32_swap_b32 v21, v23\nAD 20,20,22\nAD 21,21,23\nv_mov_b32 v22, v20\nv_mov_b32 v23, v21\nMvav 56,180,232\nRa 172,2,60416\n"
"v_permlane16_swap_b32 v20, v22\nv_permlane16_swap_b32 v21, v23\nAD 20,20,22\nAD 21,21,23\nv_min_f32 v90, v20, v21\nv_cmp_gt_f32 vcc, s64, v90\ns_nop 0\ns_cbranch_vccnz .T281\n"
".T282:\nAD 86,86,84\nMvav 56,176,224\nAD 84,80,81\nCV 72,80,81\nZaav 0,128,64\nRa 176,2,61440\nSa 120,1,16,78,2048\nZaav 4,128,68\nEX 82,50\nZaav 12,132,68\nRa 180,2,62464\n"
"Zaav 8,132,64\nWL 8\nEX 83,51\nZaav 16,136,64\nRa 184,2,63488\nAD 84,84,82\nZaav 20,136,68\nD1 1,16,46,3072\ns_add_u32 m0, s89, 16384\nZaav 28,140,68\nRa 188,2,64512\ns_barrier\n"
"Zaav 24,140,64\nAD 84,84,83\nCV 73,82,83\nZaav 32,144,64\nRa 128,24,0\nSa 124,1,16,78,3072\nZaav 36,144,68\ns_add_u32 s46, s87, s88\nEX 80,52\nZaav 44,148,68\nRa 132,24,1024\n"
"v_xor_b32 v2, 0x10000, v2\nZaav 40,148,64\nWL 8\nEX 81,53\nZaav 48,152,64\nRa 136,24,2048\nAD 87,87,85\nZaav 52,152,68\nD1 1,16,46,0\nZaav 60,156,68\nRa 140,24,3072\n"
"Zaav 56,156,64\nAD 85,80,81\nCV 76,80,81\nZaav 64,160,64\nRa 144,26,0\nZaav 68,160,68\n.W578:\ns_cbranch_execz .W582\ns_cmp_lt_u32 s99, s92\nEX 82,54\nZaav 76,164,68\n"
"Ra 148,26,1024\ns_cbranch_scc0 .E277\nv_cmpx_gt_i32 vcc, 19, v3\nv_mov_b32 v15, s97\nv_cmpx_gt_i32 vcc, 18, v3\nv_mov_b32 v14, s97\nv_cmpx_gt_i32 vcc, 17, v3\nv_mov_b32 v13, s97\n"
"v_cmpx_gt_i32 vcc, 16, v3\nv_mov_b32 v12, s97\nv_cmpx_gt_i32 vcc, 3, v3\nv_mov_b32 v7, s97\nv_cmpx_gt_i32 vcc, 2, v3\nv_mov_b32 v6, s97\nv_cmpx_gt_i32 vcc, 1, v3\n"
"v_mov_b32 v5, s97\nv_cmpx_gt_i32 vcc, 0, v3\nv_mov_b32 v4, s97\ns_mov_b64 exec, -1\ns_nop 2\n.E277:\ns_nop 0\nZaav 72,164,64\nWL 8\nEX 83,55\nZaav 80,168,64\nRa 152,26,2048\n"
"AD 85,85,82\nZaav 84,168,68\nD1 1,16,46,1024\nZaav 92,172,68\nRa 156,26,3072\nZaav 88,172,64\nAD 85,85,83\nCV 77,82,83\nZaav 96,176,64\nRa 160,24,8192\nZaav 100,176,68\nEX 80,56\n"
"Zaav 108,180,68\nRa 164,24,9216\nZaav 104,180,64\nWL 6\nEX 81,57\nZaav 112,184,64\nRa 168,24,10240\nAD 84,84,80\nv_and_b32 v27, 0x70, v1\n"
"buffer_load_dword v27, v27, s[16:19], s61 offen offset:140 sc0 sc1\nZaav 116,184,68\nD1 1,16,46,2048\nZaav 124,188,68\nRa 172,24,11264\nZaav 120,188,64\nAD 84,84,81\nCV 74,80,81\n"
"s_sub_u32 s99, s99, 1\n.p2align 6\n.A305:\nCvav 32,128,192,4\nRa 176,26,8192\nMvav 32,132,200\nEX 82,58\nMvav 32,136,208\nRa 180,26,9216\nMvav 32,140,216\nWL 6\nEX 83,59\n"
"Cvav 36,140,220,8\nRa 184,26,10240\nAD 84,84,82\nMvav 36,136,212\nD1 1,16,46,3072\ns_add_u32 m0, s89, 32768\nMvav 36,132,204\nRa 188,26,11264\nMvav 36,128,196\nAD 84,84,83\n"
"CV 75,82,83\nMvav 36,144,228\nRa 128,2,16384\nMvav 36,148,236\ns_add_u32 s46, s86, s88\nEX 80,60\nMvav 36,152,244\nRa 132,2,17408\nMvav 36,156,252\nWL 6\nEX 81,61\n"
"Mvav 32,156,248\nRa 136,2,18432\nAD 85,85,80\nMvav 32,152,240\nD1 1,16,46,0\nMvav 32,148,232\nRa 140,2,19456\nSv 112,1,16,49,0\nMvav 32,144,224\nAD 85,85,81\nCV 78,80,81\n"
"Cvav 40,160,192,12\nRa 144,2,20480\nMvav 40,164,200\nEX 82,62\nMvav 40,168,208\nRa 148,2,21504\nMvav 40,172,216\nWL 6\nEX 83,63\nCvav 44,172,220,16\nRa 152,2,22528\nAD 85,85,82\n"
"Mvav 44,168,212\nD1 1,16,46,1024\nMvav 44,164,204\nRa 156,2,23552\nMvav 44,160,196\nAD 85,85,83\nCV 79,82,83\nMvav 44,176,228\nRa 160,2,24576\nv_max_f32 v90, v84, v85\n"
"Mvav 44,180,236\ns_add_u32 s88, s88, 32768\nEX 80,32\nMvav 44,184,244\nRa 164,2,25600\nv_cmp_lt_f32 vcc, s63, v90\nMvav 44,188,252\nWL 8\nEX 81,33\nMvav 40,188,248\n"
"Ra 168,2,26624\ns_cbranch_vccnz .T313\n.T314:\nMvav 40,184,240\nD1 1,16,46,2048\nMvav 40,180,232\nRa 172,2,27648\nAD 86,86,84\nMvav 40,176,224\nAD 84,80,81\nCV 64,80,81\n"
"Maav 0,128,72\nRa 176,2,28672\nMaav 4,128,76\nEX 82,34\nMaav 12,132,76\nRa 180,2,29696\nMaav 8,132,72\ns_waitcnt vmcnt(13) lgkmcnt(8)\nEX 83,35\nMaav 16,136,72\nRa 184,2,30720\n"
"AD 84,84,82\nMaav 20,136,76\nD1 1,16,46,3072\ns_add_u32 m0, s89, 49152\nMaav 28,140,76\nRa 188,2,31744\ns_barrier\nMaav 24,140,72\nAD 84,84,83\nCV 65,82,83\nMaav 32,144,72\n"
"Ra 128,24,32768\nMaav 36,144,76\ns_add_u32 s46, s87, s88\nEX 80,36\nMaav 44,148,76\nRa 132,24,33792\nMaav 40,148,72\nWL 8\nEX 81,37\nMaav 48,152,72\nRa 136,24,34816\nAD 87,87,85\n"
"Maav 52,152,76\nD1 1,16,46,0\nMaav 60,156,76\nRa 140,24,35840\nMaav 56,156,72\nAD 85,80,81\nCV 68,80,81\nMaav 64,160,72\nRa 144,26,32768\nMaav 68,160,76\ns_cmp_eq_u32 s99, 0\n"
"EX 82,38\nMaav 76,164,76\nRa 148,26,33792\ns_cbranch_scc1 .E307\n.E308:\nMaav 72,164,72\nWL 8\nEX 83,39\nMaav 80,168,72\nRa 152,26,34816\nAD 85,85,82\nMaav 84,168,76\n"
"D1 1,16,46,1024\nMaav 92,172,76\nRa 156,26,35840\nMaav 88,172,72\nAD 85,85,83\nCV 69,82,83\nMaav 96,176,72\nRa 160,24,40960\nMaav 100,176,76\ns_xor_b32 s89, s89, 0x10000\n"
"EX 80,40\nMaav 108,180,76\nRa 164,24,41984\nMaav 104,180,72\nWL 6\nEX 81,41\nMaav 112,184,72\nRa 168,24,43008\nAD 84,84,80\nMaav 116,184,76\nD1 1,16,46,2048\nMaav 124,188,76\n"
"Ra 172,24,44032\nMaav 120,188,72\nAD 84,84,81\nCV 66,80,81\nCvav 48,128,192,4\nRa 176,26,40960\nMvav 48,132,200\ns_cmp_eq_u32 s99, 0\nEX 82,42\nMvav 48,136,208\nRa 180,26,41984\n"
"s_cselect_b32 s86, s94, s86\nMvav 48,140,216\nWL 6\nEX 83,43\nCvav 52,140,220,8\nRa 184,26,43008\nAD 84,84,82\nMvav 52,136,212\nD1 1,16,46,3072\ns_add_u32 m0, s89, 0\n"
"Mvav 52,132,204\nRa 188,26,44032\nMvav 52,128,196\nAD 84,84,83\nCV 67,82,83\nMvav 52,144,228\nRa 128,2,49152\nMvav 52,148,236\ns_add_u32 s46, s86, s88\nEX 80,44\nMvav 52,152,244\n"
"Ra 132,2,50176\nv_xor_b32 v24, 0x10000, v24\nMvav 52,156,252\nWL 6\nEX 81,45\nMvav 48,156,248\nRa 136,2,51200\nAD 85,85,80\nMvav 48,152,240\nD1 1,16,46,0\nMvav 48,148,232\n"
"Ra 140,2,52224\nSv 112,1,16,49,1024\nMvav 48,144,224\nAD 85,85,81\nCV 70,80,81\nCvav 56,160,192,12\nRa 144,2,53248\nMvav 56,164,200\ns_add_u32 s49, s49, 131072\nEX 82,46\n"
"Mvav 56,168,208\nRa 148,2,54272\nv_xor_b32 v26, 0x10000, v26\nMvav 56,172,216\nWL 6\nEX 83,47\nCvav 60,172,220,16\nRa 152,2,55296\nAD 85,85,82\nMvav 60,168,212\nD1 1,16,46,1024\n"
"Mvav 60,164,204\nRa 156,2,56320\nMvav 60,160,196\nAD 85,85,83\nCV 71,82,83\nMvav 60,176,228\nRa 160,2,57344\nv_max_f32 v90, v84, v85\nMvav 60,180,236\ns_add_u32 s88, s88, 32768\n"
"EX 80,48\nMvav 60,184,244\nRa 164,2,58368\nv_cmp_lt_f32 vcc, s63, v90\nMvav 60,188,252\nWL 8\nEX 81,49\nMvav 56,188,248\nRa 168,2,59392\ns_cbranch_vccnz .T315\n.T316:\n"
"Mvav 56,184,240\nD1 1,16,46,2048\nMvav 56,180,232\nRa 172,2,60416\nAD 86,86,84\nMvav 56,176,224\nAD 84,80,81\nCV 72,80,81\nMaav 0,128,64\nRa 176,2,61440\nMaav 4,128,68\nEX 82,50\n"
"Maav 12,132,68\nRa 180,2,62464\nMaav 8,132,64\ns_waitcnt vmcnt(13) lgkmcnt(8)\nEX 83,51\nMaav 16,136,64\nRa 184,2,63488\nAD 84,84,82\nMaav 20,136,68\nD1 1,16,46,3072\n"
"s_add_u32 m0, s89, 16384\nMaav 28,140,68\nRa 188,2,64512\ns_barrier\nMaav 24,140,64\nAD 84,84,83\nCV 73,82,83\nMaav 32,144,64\nRa 128,24,0\nMaav 36,144,68\n"
"s_add_u32 s46, s87, s88\nEX 80,52\nMaav 44,148,68\nRa 132,24,1024\nv_xor_b32 v2, 0x10000, v2\nMaav 40,148,64\nWL 8\nEX 81,53\nMaav 48,152,64\nRa 136,24,2048\nAD 87,87,85\n"
"Maav 52,152,68\nD1 1,16,46,0\nMaav 60,156,68\nRa 140,24,3072\nMaav 56,156,64\nAD 85,80,81\nCV 76,80,81\nMaav 64,160,64\nRa 144,26,0\nMaav 68,160,68\ns_cmp_lt_u32 s99, s92\n"
"EX 82,54\nMaav 76,164,68\nRa 148,26,1024\ns_cbranch_scc1 .E309\n.E310:\nMaav 72,164,64\nWL 8\nEX 83,55\nMaav 80,168,64\nRa 152,26,2048\nAD 85,85,82\nMaav 84,168,68\n"
"D1 1,16,46,1024\nMaav 92,172,68\nRa 156,26,3072\nMaav 88,172,64\nAD 85,85,83\nCV 77,82,83\nMaav 96,176,64\nRa 160,24,8192\nMaav 100,176,68\ns_sub_u32 s90, s90, 1\nEX 80,56\n"
"Maav 108,180,68\nRa 164,24,9216\ns_cbranch_scc1 .E311\n.E312:\nMaav 104,180,64\nWL 6\nEX 81,57\nMaav 112,184,64\nRa 168,24,10240\nAD 84,84,80\nMaav 116,184,68\nD1 1,16,46,2048\n"
"s_sub_u32 s99, s99, 1\nMaav 124,188,68\nRa 172,24,11264\nMaav 120,188,64\nAD 84,84,81\nCV 74,80,81\ns_cbranch_scc0 .A305\ns_branch .A317\n.E307:\nv_cmpx_gt_i32 vcc, 19, v0\n"
"v_mov_b32 v15, s97\nv_cmpx_gt_i32 vcc, 18, v0\nv_mov_b32 v14, s97\nv_cmpx_gt_i32 vcc, 17, v0\nv_mov_b32 v13, s97\nv_cmpx_gt_i32 vcc, 16, v0\nv_mov_b32 v12, s97\n"
"v_cmpx_gt_i32 vcc, 3, v0\nv_mov_b32 v7, s97\nv_cmpx_gt_i32 vcc, 2, v0\nv_mov_b32 v6, s97\nv_cmpx_gt_i32 vcc, 1, v0\nv_mov_b32 v5, s97\nv_cmpx_gt_i32 vcc, 0, v0\nv_mov_b32 v4, s97\n"
"s_mov_b64 exec, -1\ns_nop 2\ns_branch .E308\n.E309:\ns_cmp_eq_u32 s99, 0\ns_cbranch_scc1 .K306\nv_cmpx_gt_i32 vcc, 19, v3\nv_mov_b32 v15, s97\nv_cmpx_gt_i32 vcc, 18, v3\n"
"v_mov_b32 v14, s97\nv_cmpx_gt_i32 vcc, 17, v3\nv_mov_b32 v13, s97\nv_cmpx_gt_i32 vcc, 16, v3\nv_mov_b32 v12, s97\nv_cmpx_gt_i32 vcc, 3, v3\nv_mov_b32 v7, s97\n"
"v_cmpx_gt_i32 vcc, 2, v3\nv_mov_b32 v6, s97\nv_cmpx_gt_i32 vcc, 1, v3\nv_mov_b32 v5, s97\nv_cmpx_gt_i32 vcc, 0, v3\nv_mov_b32 v4, s97\ns_mov_b64 exec, -1\ns_nop 2\ns_branch .E310\n"
".K306:\nv_cmpx_gt_i32 vcc, 19, v3\nv_mov_b32 v19, s97\nv_cmpx_gt_i32 vcc, 18, v3\nv_mov_b32 v18, s97\nv_cmpx_gt_i32 vcc, 17, v3\nv_mov_b32 v17, s97\nv_cmpx_gt_i32 vcc, 16, v3\n"
"v_mov_b32 v16, s97\nv_cmpx_gt_i32 vcc, 3, v3\nv_mov_b32 v11, s97\nv_cmpx_gt_i32 vcc, 2, v3\nv_mov_b32 v10, s97\nv_cmpx_gt_i32 vcc, 1, v3\nv_mov_b32 v9, s97\n"
"v_cmpx_gt_i32 vcc, 0, v3\nv_mov_b32 v8, s97\ns_mov_b64 exec, -1\ns_nop 2\ns_branch .E310\n.E311:\ns_swappc_b64 s[80:81], s[8:9]\ns_branch .E312\n.HD269:\n"
"v_cmp_ne_u32 vcc, s15, v27\ns_nop 0\ns_cbranch_vccnz .S293\n.S294:\nbuffer_store_dword v25, off, s[16:19], s54 offset:4 sc0 sc1\nv_and_b32 v27, 0x70, v1\n"
"buffer_load_dword v27, v27, s[16:19], s61 offen offset:12 sc0 sc1\ns_mov_b32 s90, 1\ns_swappc_b64 s[8:9], s[80:81]\nv_cmp_ne_u32 vcc, s15, v27\ns_nop 0\ns_cbranch_vccnz .S296\n"
".S297:\nv_and_b32 v27, 0x70, v1\nbuffer_load_dword v27, v27, s[16:19], s61 offen offset:8 sc0 sc1\ns_mov_b32 s90, 1\ns_swappc_b64 s[8:9], s[80:81]\nv_cmp_ne_u32 vcc, s15, v27\n"
"s_nop 0\ns_cbranch_vccnz .S299\n.S300:\nv_and_b32 v27, 0xf0, v1\nbuffer_load_dword v27, v27, s[16:19], s61 offen offset:4 sc0 sc1\ns_mov_b32 s90, 1\ns_swappc_b64 s[8:9], s[80:81]\n"
"v_cmp_ne_u32 vcc, s15, v27\ns_nop 0\ns_cbranch_vccnz .S302\n.S303:\ns_mov_b32 s90, 100\ns_swappc_b64 s[8:9], s[80:81]\n.A317:\nRa 176,26,8192\nEX 82,58\nRa 180,26,9216\nWL 6\n"
"EX 83,59\nCvav 36,140,220,8\nRa 184,26,10240\nAD 84,84,82\nMvav 36,136,212\nD1 1,16,46,3072\ns_add_u32 m0, s89, 32768\nMvav 36,132,204\nRa 188,26,11264\nMvav 36,128,196\n"
"AD 84,84,83\nCV 75,82,83\nMvav 36,144,228\nRa 128,2,16384\nMvav 36,148,236\ns_add_u32 s46, s86, s88\nEX 80,60\nMvav 36,152,244\nRa 132,2,17408\nMvav 36,156,252\nEX 81,61\n"
"Ra 136,2,18432\nAD 85,85,80\nD1 1,16,46,0\nRa 140,2,19456\nSv 112,1,16,49,0\nAD 85,85,81\nCV 78,80,81\nRa 144,2,20480\ns_mov_b32 s87, s95\nEX 82,62\nRa 148,2,21504\nWL 6\n"
"EX 83,63\nCvav 44,172,220,16\nRa 152,2,22528\nAD 85,85,82\nMvav 44,168,212\nD1 1,16,46,1024\nMvav 44,164,204\nRa 156,2,23552\nMvav 44,160,196\nAD 85,85,83\nCV 79,82,83\n"
"Mvav 44,176,228\nRa 160,2,24576\nv_max_f32 v90, v84, v85\nMvav 44,180,236\ns_add_u32 s88, s88, 32768\nMvav 44,184,244\nRa 164,2,25600\nv_cmp_lt_f32 vcc, s63, v90\nMvav 44,188,252\n"
"WL 8\nRa 168,2,26624\ns_cbranch_vccnz .T319\n.T320:\nD1 1,16,46,2048\nRa 172,2,27648\nAD 86,86,84\nMaav 0,128,72\nRa 176,2,28672\nMaav 4,128,76\nMaav 12,132,76\nRa 180,2,29696\n"
"Maav 8,132,72\ns_waitcnt vmcnt(13) lgkmcnt(8)\nMaav 16,136,72\nRa 184,2,30720\nMaav 20,136,76\nD1 1,16,46,3072\ns_add_u32 m0, s89, 49152\nMaav 28,140,76\nRa 188,2,31744\n"
"s_barrier\nMaav 24,140,72\nMaav 32,144,72\nRa 128,24,32768\nMaav 36,144,76\ns_add_u32 s46, s87, s88\nEX 80,36\nMaav 44,148,76\nRa 132,24,33792\nMaav 40,148,72\nWL 8\nEX 81,37\n"
"Maav 48,152,72\nRa 136,24,34816\nAD 87,87,85\nMaav 52,152,76\nD1 1,16,46,0\nMaav 60,156,76\nRa 140,24,35840\nMaav 56,156,72\nAD 85,80,81\nCV 68,80,81\nMaav 64,160,72\n"
"Ra 144,26,32768\nMaav 68,160,76\ns_nop 0\nEX 82,38\nMaav 76,164,76\nRa 148,26,33792\nv_cmpx_gt_i32 vcc, 19, v0\nv_mov_b32 v19, s97\nv_cmpx_gt_i32 vcc, 18, v0\nv_mov_b32 v18, s97\n"
"v_cmpx_gt_i32 vcc, 17, v0\nv_mov_b32 v17, s97\nv_cmpx_gt_i32 vcc, 16, v0\nv_mov_b32 v16, s97\nv_cmpx_gt_i32 vcc, 3, v0\nv_mov_b32 v11, s97\nv_cmpx_gt_i32 vcc, 2, v0\n"
"v_mov_b32 v10, s97\nv_cmpx_gt_i32 vcc, 1, v0\nv_mov_b32 v9, s97\nv_cmpx_gt_i32 vcc, 0, v0\nv_mov_b32 v8, s97\ns_mov_b64 exec, -1\ns_nop 2\ns_nop 0\nMaav 72,164,72\nWL 8\nEX 83,39\n"
"Maav 80,168,72\nRa 152,26,34816\nAD 85,85,82\nMaav 84,168,76\nD1 1,16,46,1024\nMaav 92,172,76\nRa 156,26,35840\nMaav 88,172,72\nAD 85,85,83\nCV 69,82,83\nMaav 96,176,72\n"
"Ra 160,24,40960\nMaav 100,176,76\ns_xor_b32 s89, s89, 0x10000\nMaav 108,180,76\nRa 164,24,41984\nMaav 104,180,72\nWL 10\nMaav 112,184,72\nRa 168,24,43008\nMaav 116,184,76\n"
"D1 1,16,46,2048\nMaav 124,188,76\nRa 172,24,44032\nMaav 120,188,72\nRa 176,26,40960\nRa 180,26,41984\nWL 6\n.W579:\ns_cbranch_execz .W583\nCvav 52,140,220,8\nRa 184,26,43008\n"
"Mvav 52,136,212\nD1 1,16,46,3072\ns_add_u32 m0, s89, 0\nMvav 52,132,204\nRa 188,26,44032\nMvav 52,128,196\nMvav 52,144,228\nRa 128,2,49152\nMvav 52,148,236\n"
"s_add_u32 s46, s86, s88\nEX 80,44\nMvav 52,152,244\nRa 132,2,50176\nv_xor_b32 v24, 0x10000, v24\nMvav 52,156,252\nEX 81,45\nRa 136,2,51200\nAD 85,85,80\nD1 1,16,46,0\n"
"Ra 140,2,52224\nSv 112,1,16,49,1024\nAD 85,85,81\nCV 70,80,81\nRa 144,2,53248\ns_add_u32 s49, s49, 131072\nEX 82,46\nRa 148,2,54272\nv_xor_b32 v26, 0x10000, v26\nWL 6\nEX 83,47\n"
"Cvav 60,172,220,16\nRa 152,2,55296\nAD 85,85,82\nMvav 60,168,212\nD1 1,16,46,1024\nMvav 60,164,204\nRa 156,2,56320\nMvav 60,160,196\nAD 85,85,83\nCV 71,82,83\nMvav 60,176,228\n"
"Ra 160,2,57344\nMvav 60,180,236\ns_add_u32 s88, s88, 32768\nMvav 60,184,244\nRa 164,2,58368\nv_cmp_lt_f32 vcc, s63, v85\nMvav 60,188,252\nWL 8\nRa 168,2,59392\n"
"s_cbranch_vccnz .T321\n.T322:\nD1 1,16,46,2048\nRa 172,2,60416\nRa 176,2,61440\nMaav 4,128,68\nMaav 12,132,68\nRa 180,2,62464\ns_waitcnt vmcnt(13) lgkmcnt(8)\nRa 184,2,63488\n"
"Maav 20,136,68\nD1 1,16,46,3072\ns_add_u32 m0, s89, 16384\nMaav 28,140,68\nRa 188,2,64512\ns_barrier\nRa 128,24,0\nMaav 36,144,68\ns_add_u32 s46, s87, s88\nEX 80,52\n"
"Maav 44,148,68\nRa 132,24,1024\nv_xor_b32 v2, 0x10000, v2\nWL 8\nEX 81,53\nRa 136,24,2048\nAD 87,87,85\nMaav 52,152,68\nD1 1,16,46,0\nMaav 60,156,68\nRa 140,24,3072\nAD 85,80,81\n"
"CV 76,80,81\nRa 144,26,0\nMaav 68,160,68\nEX 82,54\nMaav 76,164,68\nRa 148,26,1024\nWL 8\nEX 83,55\nRa 152,26,2048\nAD 85,85,82\nMaav 84,168,68\nD1 1,16,46,1024\nMaav 92,172,68\n"
"Ra 156,26,3072\nAD 85,85,83\nCV 77,82,83\nRa 160,24,8192\nMaav 100,176,68\ns_sub_u32 s90, s90, 1\nMaav 108,180,68\nRa 164,24,9216\nWL 6\nRa 168,24,10240\nMaav 116,184,68\n"
"D1 1,16,46,2048\nMaav 124,188,68\nRa 172,24,11264\n.SW275:\nv_mov_b32 v4, 0\nv_mov_b32 v5, 0\nv_mov_b32 v6, 0\nv_mov_b32 v7, 0\nv_mov_b32 v8, 0\nv_mov_b32 v9, 0\nv_mov_b32 v10, 0\n"
"v_mov_b32 v11, 0\nv_mov_b32 v12, 0\nv_mov_b32 v13, 0\nv_mov_b32 v14, 0\nv_mov_b32 v15, 0\nv_mov_b32 v16, 0\nv_mov_b32 v17, 0\nv_mov_b32 v18, 0\nv_mov_b32 v19, 0\n"
"s_add_u32 s99, s98, 1\ns_mov_b32 s94, s96\ns_add_u32 s95, s96, 65536\ns_cmp_lt_u32 s99, s92\ns_cbranch_scc0 .E323\nv_cmpx_gt_i32 vcc, 19, v3\nv_mov_b32 v15, s97\n"
"v_cmpx_gt_i32 vcc, 18, v3\nv_mov_b32 v14, s97\nv_cmpx_gt_i32 vcc, 17, v3\nv_mov_b32 v13, s97\nv_cmpx_gt_i32 vcc, 16, v3\nv_mov_b32 v12, s97\nv_cmpx_gt_i32 vcc, 3, v3\n"
"v_mov_b32 v7, s97\nv_cmpx_gt_i32 vcc, 2, v3\nv_mov_b32 v6, s97\nv_cmpx_gt_i32 vcc, 1, v3\nv_mov_b32 v5, s97\nv_cmpx_gt_i32 vcc, 0, v3\nv_mov_b32 v4, s97\ns_mov_b64 exec, -1\n"
"s_nop 2\n.E323:\ns_nop 0\nCvaa 32,128,192,4\nRa 176,26,8192\nMvaa 32,132,200\nMvaa 32,136,208\nRa 180,26,9216\nMvaa 32,140,216\nWL 6\nCvaa 36,140,220,8\nRa 184,26,10240\n"
"Mvaa 36,136,212\nD1 1,16,46,3072\ns_add_u32 m0, s89, 32768\nMvaa 36,132,204\nRa 188,26,11264\nMvaa 36,128,196\nMvaa 36,144,228\nRa 128,2,16384\nMvaa 36,148,236\n"
"s_add_u32 s46, s86, s88\nEX 80,60\nMvaa 36,152,244\nRa 132,2,17408\nMvaa 36,156,252\nWL 6\nEX 81,61\nMvaa 32,156,248\nRa 136,2,18432\nAD 85,85,80\nMvaa 32,152,240\nD1 1,16,46,0\n"
"Mvaa 32,148,232\nRa 140,2,19456\nMvaa 32,144,224\nAD 85,85,81\nCV 78,80,81\nCvaa 40,160,192,12\nRa 144,2,20480\nMvaa 40,164,200\nEX 82,62\nMvaa 40,168,208\nRa 148,2,21504\n"
"Mvaa 40,172,216\nWL 6\nEX 83,63\nCvaa 44,172,220,16\nRa 152,2,22528\nAD 85,85,82\nMvaa 44,168,212\nD1 1,16,46,1024\nMvaa 44,164,204\nRa 156,2,23552\nMvaa 44,160,196\nAD 85,85,83\n"
"CV 79,82,83\nMvaa 44,176,228\nRa 160,2,24576\nMvaa 44,180,236\ns_add_u32 s88, s88, 32768\nEX 80,32\nMvaa 44,184,244\nRa 164,2,25600\nv_cmp_lt_f32 vcc, s63, v85\nMvaa 44,188,252\n"
"WL 8\nEX 81,33\nMvaa 40,188,248\nRa 168,2,26624\ns_cbranch_vccnz .T328\n.T329:\nMvaa 40,184,240\nD1 1,16,46,2048\nMvaa 40,180,232\nRa 172,2,27648\nMvaa 40,176,224\nAD 84,80,81\n"
"CV 64,80,81\nRa 176,2,28672\nMaav 4,128,76\nEX 82,34\nMaav 12,132,76\nRa 180,2,29696\ns_waitcnt vmcnt(12) lgkmcnt(8)\nEX 83,35\nRa 184,2,30720\nAD 84,84,82\nMaav 20,136,76\n"
"D1 1,16,46,3072\ns_add_u32 m0, s89, 49152\nMaav 28,140,76\nRa 188,2,31744\ns_barrier\nAR 98,0\nAR 99,1\nCV 100,98,99\nAD 84,84,83\nCV 65,82,83\nAR 98,2\nAR 99,3\nCV 101,98,99\n"
"Ra 128,24,32768\nAR 98,8\nAR 99,9\nCV 102,98,99\nAR 98,10\nMaav 36,144,76\ns_add_u32 s46, s87, s88\nEX 80,36\nAR 99,11\nCV 103,98,99\nMaav 44,148,76\nRa 132,24,33792\n"
"s_add_u32 s5, s66, 0\nSv 100,1,16,5,0\nAR 98,4\nAR 99,5\nWL 8\nEX 81,37\nCV 104,98,99\nAR 98,6\nRa 136,24,34816\nAD 87,87,85\nAR 99,7\nCV 105,98,99\nAR 98,12\nMaav 52,152,76\n"
"D1 1,16,46,0\nv_mov_b32 v96, v86\nv_mov_b32 v97, v87\nAR 99,13\nCV 106,98,99\nMaav 60,156,76\nRa 140,24,35840\nv_permlane32_swap_b32 v86, v96\nAR 98,14\nAR 99,15\nCV 107,98,99\n"
"AD 85,80,81\nCV 68,80,81\ns_add_u32 s5, s66, 0\nSv 104,1,16,5,1024\nAR 98,16\nRa 144,26,32768\nv_permlane32_swap_b32 v87, v97\nAD 86,86,96\nAR 99,17\nCV 108,98,99\nMaav 68,160,76\n"
"s_cmp_eq_u32 s99, 0\nEX 82,38\nMaav 76,164,76\nRa 148,26,33792\ns_cbranch_scc0 .E325\nv_cmpx_gt_i32 vcc, 19, v0\nv_mov_b32 v15, s97\nv_cmpx_gt_i32 vcc, 18, v0\nv_mov_b32 v14, s97\n"
"v_cmpx_gt_i32 vcc, 17, v0\nv_mov_b32 v13, s97\nv_cmpx_gt_i32 vcc, 16, v0\nv_mov_b32 v12, s97\nv_cmpx_gt_i32 vcc, 3, v0\nv_mov_b32 v7, s97\nv_cmpx_gt_i32 vcc, 2, v0\n"
"v_mov_b32 v6, s97\nv_cmpx_gt_i32 vcc, 1, v0\nv_mov_b32 v5, s97\nv_cmpx_gt_i32 vcc, 0, v0\nv_mov_b32 v4, s97\ns_mov_b64 exec, -1\ns_nop 2\n.E325:\nWL 8\nEX 83,39\nAR 98,18\n"
"AR 99,19\nRa 152,26,34816\nAD 85,85,82\nAD 87,87,97\nCV 109,98,99\nAR 98,24\nMaav 84,168,76\nD1 1,16,46,1024\nv_mov_b32 v96, v86\nAR 99,25\nCV 110,98,99\nAR 98,26\nMaav 92,172,76\n"
"Ra 156,26,35840\nv_mov_b32 v97, v87\nAR 99,27\nCV 111,98,99\nAD 85,85,83\nCV 69,82,83\nv_permlane16_swap_b32 v86, v96\ns_add_u32 s5, s66, 0\nSv 108,1,16,5,2048\nRa 160,24,40960\n"
"v_permlane16_swap_b32 v87, v97\nAR 98,20\nAR 99,21\nCV 100,98,99\nMaav 100,176,76\ns_xor_b32 s89, s89, 0x10000\nEX 80,40\nAR 98,22\nAR 99,23\nMaav 108,180,76\nRa 164,24,41984\n"
"AD 86,86,96\nAD 87,87,97\nCV 101,98,99\nAR 98,28\nWL 6\nEX 81,41\nAR 99,29\nCV 102,98,99\nRa 168,24,43008\nAD 84,84,80\nAR 98,30\nAR 99,31\nCV 103,98,99\nMaav 116,184,76\n"
"D1 1,16,46,2048\nv_cmp_gt_u32 vcc, 0x100, v1\nv_mov_b32 v94, s15\nv_mov_b32 v95, s15\nv_cndmask_b32 v92, v89, v88, vcc\nv_cndmask_b32 v93, v87, v86, vcc\nMaav 124,188,76\n"
"Ra 172,24,44032\nv_mov_b32 v86, 0\ns_add_u32 s5, s66, 0\nSv 100,1,16,5,3072\nAR 98,32\nAD 84,84,81\nCV 66,80,81\nAR 99,33\nCV 104,98,99\nAR 98,34\nCvaa 48,128,192,4\n"
"Ra 176,26,40960\nv_mov_b32 v87, 0\nv_mov_b32 v88, 0\nAR 99,35\nCV 105,98,99\nMvaa 48,132,200\ns_cmp_eq_u32 s99, 0\nEX 82,42\nMvaa 48,136,208\nRa 180,26,41984\n"
"s_cselect_b32 s86, s94, s86\nAR 98,40\nAR 99,41\nCV 106,98,99\nMvaa 48,140,216\nWL 6\nEX 83,43\nAR 98,42\nAR 99,43\nCvaa 52,140,220,8\nRa 184,26,43008\nAD 84,84,82\n"
"v_mov_b32 v89, 0\nCV 107,98,99\nMvaa 52,136,212\nD1 1,16,46,3072\ns_add_u32 m0, s89, 0\ns_add_u32 s5, s66, 4096\nSv 104,1,16,5,0\nAR 98,36\nMvaa 52,132,204\nRa 188,26,44032\n"
"AR 99,37\nCV 108,98,99\nAR 98,38\nAR 99,39\nMvaa 52,128,196\nAD 84,84,83\nCV 67,82,83\nCV 109,98,99\nAR 98,44\nAR 99,45\nMvaa 52,144,228\nRa 128,2,49152\nCV 110,98,99\nAR 98,46\n"
"AR 99,47\nCV 111,98,99\nMvaa 52,148,236\ns_add_u32 s46, s86, s88\nEX 80,44\ns_add_u32 s5, s66, 4096\nSv 108,1,16,5,1024\nMvaa 52,152,244\nRa 132,2,50176\n"
"v_xor_b32 v24, 0x10000, v24\nAR 98,48\nAR 99,49\nCV 100,98,99\nMvaa 52,156,252\nWL 6\nEX 81,45\nAR 98,50\nAR 99,51\nMvaa 48,156,248\nRa 136,2,51200\nAD 85,85,80\nCV 101,98,99\n"
"AR 98,56\nAR 99,57\nMvaa 48,152,240\nD1 1,16,46,0\nCV 102,98,99\nAR 98,58\nAR 99,59\nCV 103,98,99\nMvaa 48,148,232\nRa 140,2,52224\ns_add_u32 s5, s66, 4096\nSv 100,1,16,5,2048\n"
"AR 98,52\nAR 99,53\nMvaa 48,144,224\nAD 85,85,81\nCV 70,80,81\nCV 104,98,99\nAR 98,54\nAR 99,55\nCvaa 56,160,192,12\nRa 144,2,53248\nCV 105,98,99\nAR 98,60\nAR 99,61\n"
"CV 106,98,99\nMvaa 56,164,200\nEX 82,46\nAR 98,62\nAR 99,63\nCV 107,98,99\nMvaa 56,168,208\nRa 148,2,54272\nv_xor_b32 v26, 0x10000, v26\ns_add_u32 s5, s66, 4096\n"
"Sv 104,1,16,5,3072\nAR 98,64\nMvaa 56,172,216\nWL 6\nEX 83,47\nAR 99,65\nCV 108,98,99\nCvaa 60,172,220,16\nRa 152,2,55296\nAD 85,85,82\nAR 98,66\nAR 99,67\nCV 109,98,99\n"
"Mvaa 60,168,212\nD1 1,16,46,1024\nAR 98,72\nAR 99,73\nCV 110,98,99\nAR 98,74\nMvaa 60,164,204\nRa 156,2,56320\nAR 99,75\nCV 111,98,99\ns_add_u32 s5, s66, 16384\nSv 108,1,16,5,0\n"
"Mvaa 60,160,196\nAD 85,85,83\nCV 71,82,83\nAR 98,68\nAR 99,69\nCV 100,98,99\nMvaa 60,176,228\nRa 160,2,57344\nv_max_f32 v90, v84, v85\nAR 98,70\nAR 99,71\nCV 101,98,99\n"
"Mvaa 60,180,236\ns_add_u32 s88, s88, 32768\nEX 80,48\nAR 98,76\nAR 99,77\nMvaa 60,184,244\nRa 164,2,58368\nv_cmp_lt_f32 vcc, s63, v90\nMvaa 60,188,252\nWL 8\nEX 81,49\n"
"Mvaa 56,188,248\nRa 168,2,59392\ns_cbranch_vccnz .T330\n.T331:\nv_mov_b32 v20, v84\nv_mov_b32 v21, v85\nv_mov_b32 v22, v20\nv_mov_b32 v23, v21\nMvaa 56,184,240\nD1 1,16,46,2048\n"
"v_permlane32_swap_b32 v20, v22\nv_permlane32_swap_b32 v21, v23\nAD 20,20,22\nAD 21,21,23\nv_mov_b32 v22, v20\nv_mov_b32 v23, v21\nMvaa 56,180,232\nRa 172,2,60416\n"
"v_permlane16_swap_b32 v20, v22\nv_permlane16_swap_b32 v21, v23\nAD 20,20,22\nAD 21,21,23\nv_min_f32 v90, v20, v21\nv_cmp_gt_f32 vcc, s64, v90\ns_nop 0\ns_cbranch_vccnz .T332\n"
".T333:\nAD 86,86,84\nMvaa 56,176,224\nAD 84,80,81\nCV 72,80,81\nCV 102,98,99\nAR 98,78\nAR 99,79\nZvav 128,128,64\nRa 176,2,61440\nCV 103,98,99\ns_add_u32 s5, s66, 16384\n"
"Sv 100,1,16,5,1024\nAR 98,80\nZvav 132,128,68\nEX 82,50\nAR 99,81\nCV 104,98,99\nAR 98,82\nZvav 140,132,68\nRa 180,2,62464\nAR 99,83\nCV 105,98,99\nAR 98,88\nAR 99,89\n"
"Zvav 136,132,64\ns_waitcnt vmcnt(21) lgkmcnt(8)\nEX 83,51\nCV 106,98,99\nAR 98,90\nZvav 144,136,64\nRa 184,2,63488\nAD 84,84,82\nAR 99,91\nCV 107,98,99\nZvav 148,136,68\n"
"D1 1,16,46,3072\ns_add_u32 m0, s89, 16384\ns_add_u32 s5, s66, 16384\nSv 104,1,16,5,2048\nAR 98,84\nZvav 156,140,68\nRa 188,2,64512\ns_barrier\nAR 99,85\nCV 108,98,99\nAR 98,86\n"
"Zvav 152,140,64\nAD 84,84,83\nCV 73,82,83\n.W580:\ns_cbranch_execz .W584\nAR 99,87\nCV 109,98,99\nAR 98,92\nZvav 160,144,64\nRa 128,24,0\nAR 99,93\nCV 110,98,99\nAR 98,94\n"
"AR 99,95\nZvav 164,144,68\ns_add_u32 s46, s87, s88\nEX 80,52\nCV 111,98,99\nZvav 172,148,68\nRa 132,24,1024\nv_xor_b32 v2, 0x10000, v2\ns_add_u32 s5, s66, 16384\n"
"Sv 108,1,16,5,3072\nAR 98,96\nZvav 168,148,64\nWL 8\nEX 81,53\nAR 99,97\nCV 100,98,99\nZvav 176,152,64\nRa 136,24,2048\nAD 87,87,85\nAR 98,98\nAR 99,99\nCV 101,98,99\n"
"Zvav 180,152,68\nD1 1,16,46,0\nAR 98,104\nAR 99,105\nCV 102,98,99\nAR 98,106\nZvav 188,156,68\nRa 140,24,3072\nAR 99,107\nCV 103,98,99\ns_add_u32 s5, s66, 20480\nSv 100,1,16,5,0\n"
"Zvav 184,156,64\nAD 85,80,81\nCV 76,80,81\nAR 98,100\nAR 99,101\nCV 104,98,99\nZvav 192,160,64\nRa 144,26,0\nAR 98,102\nAR 99,103\nCV 105,98,99\nAR 98,108\nZvav 196,160,68\n"
"s_cmp_lt_u32 s99, s92\nEX 82,54\nZvav 204,164,68\nRa 148,26,1024\ns_cbranch_scc0 .E326\nv_cmpx_gt_i32 vcc, 19, v3\nv_mov_b32 v19, s97\nv_cmpx_gt_i32 vcc, 18, v3\n"
"v_mov_b32 v18, s97\nv_cmpx_gt_i32 vcc, 17, v3\nv_mov_b32 v17, s97\nv_cmpx_gt_i32 vcc, 16, v3\nv_mov_b32 v16, s97\nv_cmpx_gt_i32 vcc, 3, v3\nv_mov_b32 v11, s97\n"
"v_cmpx_gt_i32 vcc, 2, v3\nv_mov_b32 v10, s97\nv_cmpx_gt_i32 vcc, 1, v3\nv_mov_b32 v9, s97\nv_cmpx_gt_i32 vcc, 0, v3\nv_mov_b32 v8, s97\ns_mov_b64 exec, -1\ns_nop 2\n.E326:\n"
"s_nop 0\nZvav 200,164,64\nWL 8\nEX 83,55\nAR 99,109\nCV 106,98,99\nZvav 208,168,64\nRa 152,26,2048\nAD 85,85,82\nAR 98,110\nAR 99,111\nCV 107,98,99\nZvav 212,168,68\n"
"D1 1,16,46,1024\ns_add_u32 s5, s66, 20480\nSv 104,1,16,5,1024\nAR 98,112\nAR 99,113\nZvav 220,172,68\nRa 156,26,3072\nCV 108,98,99\nAR 98,114\nAR 99,115\nCV 109,98,99\n"
"Zvav 216,172,64\nAD 85,85,83\nCV 77,82,83\nAR 98,120\nAR 99,121\nCV 110,98,99\nZvav 224,176,64\nRa 160,24,8192\nAR 98,122\nAR 99,123\nCV 111,98,99\nZvav 228,176,68\n"
"s_sub_u32 s90, s90, 1\nEX 80,56\nZvav 236,180,68\nRa 164,24,9216\ns_cbranch_scc0 .E324\ns_swappc_b64 s[80:81], s[8:9]\n.E324:\ns_add_u32 s5, s66, 20480\nSv 108,1,16,5,2048\n"
"Zvav 232,180,64\nWL 6\nEX 81,57\nAR 98,116\nAR 99,117\nZvav 240,184,64\nRa 168,24,10240\nAD 84,84,80\nCV 100,98,99\nAR 98,118\nAR 99,119\nZvav 244,184,68\nD1 1,16,46,2048\n"
"CV 101,98,99\nAR 98,124\nAR 99,125\nCV 102,98,99\nZvav 252,188,68\nRa 172,24,11264\nAR 98,126\nAR 99,127\nCV 103,98,99\nZvav 248,188,64\nAD 84,84,81\nCV 74,80,81\n"
"s_add_u32 s5, s66, 20480\nSv 100,1,16,5,3072\ns_sub_u32 s99, s99, 1\ns_cbranch_scc1 .LE276\n.p2align 6\n.A334:\nCvaa 32,128,192,4\nRa 176,26,8192\nMvaa 32,132,200\nEX 82,58\n"
"Mvaa 32,136,208\nRa 180,26,9216\nMvaa 32,140,216\nWL 6\nEX 83,59\nCvaa 36,140,220,8\nRa 184,26,10240\nAD 84,84,82\nMvaa 36,136,212\nD1 1,16,46,3072\ns_add_u32 m0, s89, 32768\n"
"Mvaa 36,132,204\nRa 188,26,11264\nMvaa 36,128,196\nAD 84,84,83\nCV 75,82,83\nMvaa 36,144,228\nRa 128,2,16384\nMvaa 36,148,236\ns_add_u32 s46, s86, s88\nEX 80,60\nMvaa 36,152,244\n"
"Ra 132,2,17408\nMvaa 36,156,252\nWL 6\nEX 81,61\nMvaa 32,156,248\nRa 136,2,18432\nAD 85,85,80\nMvaa 32,152,240\nD1 1,16,46,0\nMvaa 32,148,232\nRa 140,2,19456\nSv 112,1,16,49,0\n"
"Mvaa 32,144,224\nAD 85,85,81\nCV 78,80,81\nCvaa 40,160,192,12\nRa 144,2,20480\nMvaa 40,164,200\nEX 82,62\nMvaa 40,168,208\nRa 148,2,21504\nMvaa 40,172,216\nWL 6\nEX 83,63\n"
"Cvaa 44,172,220,16\nRa 152,2,22528\nAD 85,85,82\nMvaa 44,168,212\nD1 1,16,46,1024\nMvaa 44,164,204\nRa 156,2,23552\nMvaa 44,160,196\nAD 85,85,83\nCV 79,82,83\nMvaa 44,176,228\n"
"Ra 160,2,24576\nv_max_f32 v90, v84, v85\nMvaa 44,180,236\ns_add_u32 s88, s88, 32768\nEX 80,32\nMvaa 44,184,244\nRa 164,2,25600\nv_cmp_lt_f32 vcc, s63, v90\nMvaa 44,188,252\nWL 8\n"
"EX 81,33\nMvaa 40,188,248\nRa 168,2,26624\ns_cbranch_vccnz .T344\n.T345:\nMvaa 40,184,240\nD1 1,16,46,2048\nMvaa 40,180,232\nRa 172,2,27648\nAD 86,86,84\nMvaa 40,176,224\n"
"AD 84,80,81\nCV 64,80,81\nMvav 128,128,72\nRa 176,2,28672\nMvav 132,128,76\ns_cmp_eq_u32 s99, s98\nEX 82,34\nMvav 140,132,76\nRa 180,2,29696\ns_cbranch_scc1 .E342\n.E343:\n"
"Mvav 136,132,72\ns_waitcnt vmcnt(13) lgkmcnt(8)\nEX 83,35\nMvav 144,136,72\nRa 184,2,30720\nAD 84,84,82\nMvav 148,136,76\nD1 1,16,46,3072\ns_add_u32 m0, s89, 49152\n"
"Mvav 156,140,76\nRa 188,2,31744\ns_barrier\nMvav 152,140,72\nAD 84,84,83\nCV 65,82,83\nMvav 160,144,72\nRa 128,24,32768\nMvav 164,144,76\ns_add_u32 s46, s87, s88\nEX 80,36\n"
"Mvav 172,148,76\nRa 132,24,33792\nMvav 168,148,72\nWL 8\nEX 81,37\nMvav 176,152,72\nRa 136,24,34816\nAD 87,87,85\nMvav 180,152,76\nD1 1,16,46,0\nMvav 188,156,76\nRa 140,24,35840\n"
"Mvav 184,156,72\nAD 85,80,81\nCV 68,80,81\nMvav 192,160,72\nRa 144,26,32768\nMvav 196,160,76\ns_cmp_eq_u32 s99, 0\nEX 82,38\nMvav 204,164,76\nRa 148,26,33792\n"
"s_cbranch_scc1 .E336\n.E337:\nMvav 200,164,72\nWL 8\nEX 83,39\nMvav 208,168,72\nRa 152,26,34816\nAD 85,85,82\nMvav 212,168,76\nD1 1,16,46,1024\nMvav 220,172,76\nRa 156,26,35840\n"
"Mvav 216,172,72\nAD 85,85,83\nCV 69,82,83\nMvav 224,176,72\nRa 160,24,40960\nMvav 228,176,76\ns_xor_b32 s89, s89, 0x10000\nEX 80,40\nMvav 236,180,76\nRa 164,24,41984\n"
"Mvav 232,180,72\nWL 6\nEX 81,41\nMvav 240,184,72\nRa 168,24,43008\nAD 84,84,80\nMvav 244,184,76\nD1 1,16,46,2048\nMvav 252,188,76\nRa 172,24,44032\nMvav 248,188,72\nAD 84,84,81\n"
"CV 66,80,81\nCvaa 48,128,192,4\nRa 176,26,40960\nMvaa 48,132,200\ns_cmp_eq_u32 s99, 0\nEX 82,42\nMvaa 48,136,208\nRa 180,26,41984\ns_cselect_b32 s86, s94, s86\nMvaa 48,140,216\n"
"WL 6\nEX 83,43\nCvaa 52,140,220,8\nRa 184,26,43008\nAD 84,84,82\nMvaa 52,136,212\nD1 1,16,46,3072\ns_add_u32 m0, s89, 0\nMvaa 52,132,204\nRa 188,26,44032\nMvaa 52,128,196\n"
"AD 84,84,83\nCV 67,82,83\nMvaa 52,144,228\nRa 128,2,49152\nMvaa 52,148,236\ns_add_u32 s46, s86, s88\nEX 80,44\nMvaa 52,152,244\nRa 132,2,50176\nv_xor_b32 v24, 0x10000, v24\n"
"Mvaa 52,156,252\nWL 6\nEX 81,45\nMvaa 48,156,248\nRa 136,2,51200\nAD 85,85,80\nMvaa 48,152,240\nD1 1,16,46,0\nMvaa 48,148,232\nRa 140,2,52224\nSv 112,1,16,49,1024\n"
"Mvaa 48,144,224\nAD 85,85,81\nCV 70,80,81\nCvaa 56,160,192,12\nRa 144,2,53248\nMvaa 56,164,200\ns_add_u32 s49, s49, 131072\nEX 82,46\nMvaa 56,168,208\nRa 148,2,54272\n"
"v_xor_b32 v26, 0x10000, v26\nMvaa 56,172,216\nWL 6\nEX 83,47\nCvaa 60,172,220,16\nRa 152,2,55296\nAD 85,85,82\nMvaa 60,168,212\nD1 1,16,46,1024\nMvaa 60,164,204\nRa 156,2,56320\n"
"Mvaa 60,160,196\nAD 85,85,83\nCV 71,82,83\nMvaa 60,176,228\nRa 160,2,57344\nv_max_f32 v90, v84, v85\nMvaa 60,180,236\ns_add_u32 s88, s88, 32768\nEX 80,48\nMvaa 60,184,244\n"
"Ra 164,2,58368\nv_cmp_lt_f32 vcc, s63, v90\nMvaa 60,188,252\nWL 8\nEX 81,49\nMvaa 56,188,248\nRa 168,2,59392\ns_cbranch_vccnz .T346\n.T347:\nMvaa 56,184,240\nD1 1,16,46,2048\n"
"Mvaa 56,180,232\nRa 172,2,60416\nAD 86,86,84\nMvaa 56,176,224\nAD 84,80,81\nCV 72,80,81\nMvav 128,128,64\nRa 176,2,61440\nMvav 132,128,68\nEX 82,50\nMvav 140,132,68\n"
"Ra 180,2,62464\nMvav 136,132,64\ns_waitcnt vmcnt(13) lgkmcnt(8)\nEX 83,51\nMvav 144,136,64\nRa 184,2,63488\nAD 84,84,82\nMvav 148,136,68\nD1 1,16,46,3072\n"
"s_add_u32 m0, s89, 16384\nMvav 156,140,68\nRa 188,2,64512\ns_barrier\nMvav 152,140,64\nAD 84,84,83\nCV 73,82,83\nMvav 160,144,64\nRa 128,24,0\nMvav 164,144,68\n"
"s_add_u32 s46, s87, s88\nEX 80,52\nMvav 172,148,68\nRa 132,24,1024\nv_xor_b32 v2, 0x10000, v2\nMvav 168,148,64\nWL 8\nEX 81,53\nMvav 176,152,64\nRa 136,24,2048\nAD 87,87,85\n"
"Mvav 180,152,68\nD1 1,16,46,0\nMvav 188,156,68\nRa 140,24,3072\nMvav 184,156,64\nAD 85,80,81\nCV 76,80,81\nMvav 192,160,64\nRa 144,26,0\nMvav 196,160,68\ns_cmp_lt_u32 s99, s92\n"
"EX 82,54\nMvav 204,164,68\nRa 148,26,1024\ns_cbranch_scc1 .E338\n.E339:\nMvav 200,164,64\nWL 8\nEX 83,55\nMvav 208,168,64\nRa 152,26,2048\nAD 85,85,82\nMvav 212,168,68\n"
"D1 1,16,46,1024\nMvav 220,172,68\nRa 156,26,3072\nMvav 216,172,64\nAD 85,85,83\nCV 77,82,83\nMvav 224,176,64\nRa 160,24,8192\nMvav 228,176,68\ns_sub_u32 s90, s90, 1\nEX 80,56\n"
"Mvav 236,180,68\nRa 164,24,9216\ns_cbranch_scc1 .E340\n.E341:\nMvav 232,180,64\nWL 6\nEX 81,57\nMvav 240,184,64\nRa 168,24,10240\nAD 84,84,80\nMvav 244,184,68\nD1 1,16,46,2048\n"
"s_sub_u32 s99, s99, 1\nMvav 252,188,68\nRa 172,24,11264\nMvav 248,188,64\nAD 84,84,81\nCV 74,80,81\ns_cbranch_scc0 .A334\ns_branch .A348\n.E336:\nv_cmpx_gt_i32 vcc, 19, v0\n"
"v_mov_b32 v15, s97\nv_cmpx_gt_i32 vcc, 18, v0\nv_mov_b32 v14, s97\nv_cmpx_gt_i32 vcc, 17, v0\nv_mov_b32 v13, s97\nv_cmpx_gt_i32 vcc, 16, v0\nv_mov_b32 v12, s97\n"
"v_cmpx_gt_i32 vcc, 3, v0\nv_mov_b32 v7, s97\nv_cmpx_gt_i32 vcc, 2, v0\nv_mov_b32 v6, s97\nv_cmpx_gt_i32 vcc, 1, v0\nv_mov_b32 v5, s97\nv_cmpx_gt_i32 vcc, 0, v0\nv_mov_b32 v4, s97\n"
"s_mov_b64 exec, -1\ns_nop 2\ns_branch .E337\n.E338:\ns_cmp_eq_u32 s99, 0\ns_cbranch_scc1 .K335\nv_cmpx_gt_i32 vcc, 19, v3\nv_mov_b32 v15, s97\nv_cmpx_gt_i32 vcc, 18, v3\n"
"v_mov_b32 v14, s97\nv_cmpx_gt_i32 vcc, 17, v3\nv_mov_b32 v13, s97\nv_cmpx_gt_i32 vcc, 16, v3\nv_mov_b32 v12, s97\nv_cmpx_gt_i32 vcc, 3, v3\nv_mov_b32 v7, s97\n"
"v_cmpx_gt_i32 vcc, 2, v3\nv_mov_b32 v6, s97\nv_cmpx_gt_i32 vcc, 1, v3\nv_mov_b32 v5, s97\nv_cmpx_gt_i32 vcc, 0, v3\nv_mov_b32 v4, s97\ns_mov_b64 exec, -1\ns_nop 2\ns_branch .E339\n"
".K335:\nv_cmpx_gt_i32 vcc, 19, v3\nv_mov_b32 v19, s97\nv_cmpx_gt_i32 vcc, 18, v3\nv_mov_b32 v18, s97\nv_cmpx_gt_i32 vcc, 17, v3\nv_mov_b32 v17, s97\nv_cmpx_gt_i32 vcc, 16, v3\n"
"v_mov_b32 v16, s97\nv_cmpx_gt_i32 vcc, 3, v3\nv_mov_b32 v11, s97\nv_cmpx_gt_i32 vcc, 2, v3\nv_mov_b32 v10, s97\nv_cmpx_gt_i32 vcc, 1, v3\nv_mov_b32 v9, s97\n"
"v_cmpx_gt_i32 vcc, 0, v3\nv_mov_b32 v8, s97\ns_mov_b64 exec, -1\ns_nop 2\ns_branch .E339\n.E340:\ns_swappc_b64 s[80:81], s[8:9]\ns_branch .E341\n.E342:\nv_mov_b32 v91, 0x40000000\n"
"v_cmp_gt_u32 vcc, 0x200, v1\nWV 5\ns_nop 0\nv_cndmask_b32 v91, v91, v1, vcc\nSv 92,91,16,65,0\ns_branch .E343\n.A348:\n.LE276:\nRa 176,26,8192\nEX 82,58\nRa 180,26,9216\nWL 6\n"
"EX 83,59\nCvaa 36,140,220,8\nRa 184,26,10240\nAD 84,84,82\nMvaa 36,136,212\nD1 1,16,46,3072\ns_add_u32 m0, s89, 32768\nMvaa 36,132,204\nRa 188,26,11264\nMvaa 36,128,196\n"
"AD 84,84,83\nCV 75,82,83\nMvaa 36,144,228\nRa 128,2,16384\nMvaa 36,148,236\ns_add_u32 s46, s86, s88\nEX 80,60\nMvaa 36,152,244\nRa 132,2,17408\nMvaa 36,156,252\nEX 81,61\n"
"Ra 136,2,18432\nAD 85,85,80\nD1 1,16,46,0\nRa 140,2,19456\nAD 85,85,81\nCV 78,80,81\nRa 144,2,20480\ns_mov_b32 s87, s95\nEX 82,62\nRa 148,2,21504\nWL 6\nEX 83,63\n"
"Cvaa 44,172,220,16\nRa 152,2,22528\nAD 85,85,82\nMvaa 44,168,212\nD1 1,16,46,1024\nMvaa 44,164,204\nRa 156,2,23552\nMvaa 44,160,196\nAD 85,85,83\nCV 79,82,83\nMvaa 44,176,228\n"
"Ra 160,2,24576\nv_max_f32 v90, v84, v85\nMvaa 44,180,236\ns_add_u32 s88, s88, 32768\nMvaa 44,184,244\nRa 164,2,25600\nv_cmp_lt_f32 vcc, s63, v90\nMvaa 44,188,252\nWL 8\n"
"Ra 168,2,26624\ns_cbranch_vccnz .T351\n.T352:\nD1 1,16,46,2048\nRa 172,2,27648\nAD 86,86,84\nMvav 128,128,72\n.W581:\ns_cbranch_execz .WB274\nRa 176,2,28672\nMvav 132,128,76\n"
"s_cmp_eq_u32 s13, 0\nMvav 140,132,76\nRa 180,2,29696\ns_cbranch_scc0 .E349\nv_mov_b32 v91, 0x40000000\nv_cmp_gt_u32 vcc, 0x200, v1\nWV 4\ns_nop 0\nv_cndmask_b32 v91, v91, v1, vcc\n"
"Sv 92,91,16,65,0\n.E349:\nMvav 136,132,72\ns_waitcnt vmcnt(12) lgkmcnt(8)\nMvav 144,136,72\nRa 184,2,30720\nMvav 148,136,76\nD1 1,16,46,3072\ns_add_u32 m0, s89, 49152\n"
"Mvav 156,140,76\nRa 188,2,31744\ns_barrier\nMvav 152,140,72\nMvav 160,144,72\nRa 128,24,32768\nMvav 164,144,76\ns_add_u32 s46, s87, s88\nEX 80,36\nMvav 172,148,76\n"
"Ra 132,24,33792\nMvav 168,148,72\nWL 8\nEX 81,37\nMvav 176,152,72\nRa 136,24,34816\nAD 87,87,85\nMvav 180,152,76\nD1 1,16,46,0\nMvav 188,156,76\nRa 140,24,35840\nMvav 184,156,72\n"
"AD 85,80,81\nCV 68,80,81\nMvav 192,160,72\nRa 144,26,32768\nMvav 196,160,76\ns_nop 0\nEX 82,38\nMvav 204,164,76\nRa 148,26,33792\nv_cmpx_gt_i32 vcc, 19, v0\nv_mov_b32 v19, s97\n"
"v_cmpx_gt_i32 vcc, 18, v0\nv_mov_b32 v18, s97\nv_cmpx_gt_i32 vcc, 17, v0\nv_mov_b32 v17, s97\nv_cmpx_gt_i32 vcc, 16, v0\nv_mov_b32 v16, s97\nv_cmpx_gt_i32 vcc, 3, v0\n"
"v_mov_b32 v11, s97\nv_cmpx_gt_i32 vcc, 2, v0\nv_mov_b32 v10, s97\nv_cmpx_gt_i32 vcc, 1, v0\nv_mov_b32 v9, s97\nv_cmpx_gt_i32 vcc, 0, v0\nv_mov_b32 v8, s97\ns_mov_b64 exec, -1\n"
"s_nop 2\ns_nop 0\nMvav 200,164,72\nWL 8\nEX 83,39\nMvav 208,168,72\nRa 152,26,34816\nAD 85,85,82\nMvav 212,168,76\nD1 1,16,46,1024\nMvav 220,172,76\nRa 156,26,35840\n"
"Mvav 216,172,72\nAD 85,85,83\nCV 69,82,83\nMvav 224,176,72\nRa 160,24,40960\nMvav 228,176,76\ns_xor_b32 s89, s89, 0x10000\nMvav 236,180,76\nRa 164,24,41984\nMvav 232,180,72\n"
"WL 10\nMvav 240,184,72\nRa 168,24,43008\nMvav 244,184,76\nD1 1,16,46,2048\nMvav 252,188,76\nRa 172,24,44032\nMvav 248,188,72\nRa 176,26,40960\nRa 180,26,41984\nWL 6\n"
"Cvaa 52,140,220,8\nRa 184,26,43008\nMvaa 52,136,212\nD1 1,16,46,3072\ns_add_u32 m0, s89, 0\nMvaa 52,132,204\nRa 188,26,44032\nMvaa 52,128,196\nMvaa 52,144,228\nRa 128,2,49152\n"
"Mvaa 52,148,236\ns_add_u32 s46, s86, s88\nEX 80,44\nMvaa 52,152,244\nRa 132,2,50176\nv_xor_b32 v24, 0x10000, v24\nMvaa 52,156,252\nEX 81,45\nRa 136,2,51200\nAD 85,85,80\n"
"D1 1,16,46,0\nRa 140,2,52224\nAD 85,85,81\nCV 70,80,81\nRa 144,2,53248\nEX 82,46\nRa 148,2,54272\nv_xor_b32 v26, 0x10000, v26\nWL 6\nEX 83,47\nCvaa 60,172,220,16\nRa 152,2,55296\n"
"AD 85,85,82\nMvaa 60,168,212\nD1 1,16,46,1024\nMvaa 60,164,204\nRa 156,2,56320\nMvaa 60,160,196\nAD 85,85,83\nCV 71,82,83\nMvaa 60,176,228\nRa 160,2,57344\nMvaa 60,180,236\n"
"s_add_u32 s88, s88, 32768\nMvaa 60,184,244\nRa 164,2,58368\nv_cmp_lt_f32 vcc, s63, v85\nMvaa 60,188,252\nWL 8\nRa 168,2,59392\ns_cbranch_vccnz .T353\n.T354:\nD1 1,16,46,2048\n"
"Ra 172,2,60416\nRa 176,2,61440\nMvav 132,128,68\nMvav 140,132,68\nRa 180,2,62464\ns_waitcnt vmcnt(11) lgkmcnt(8)\nRa 184,2,63488\nMvav 148,136,68\nD1 1,16,46,3072\n"
"s_add_u32 m0, s89, 16384\nMvav 156,140,68\nRa 188,2,64512\ns_barrier\nRa 128,24,0\nMvav 164,144,68\ns_add_u32 s46, s87, s88\nEX 80,52\nMvav 172,148,68\nRa 132,24,1024\n"
"v_xor_b32 v2, 0x10000, v2\nWL 8\nEX 81,53\nRa 136,24,2048\nAD 87,87,85\nMvav 180,152,68\nD1 1,16,46,0\nMvav 188,156,68\nRa 140,24,3072\nAD 85,80,81\nCV 76,80,81\nRa 144,26,0\n"
"Mvav 196,160,68\nEX 82,54\nMvav 204,164,68\nRa 148,26,1024\nWL 8\nEX 83,55\nRa 152,26,2048\nAD 85,85,82\nMvav 212,168,68\nD1 1,16,46,1024\nMvav 220,172,68\nRa 156,26,3072\n"
"AD 85,85,83\nCV 77,82,83\nRa 160,24,8192\nMvav 228,176,68\nMvav 236,180,68\nRa 164,24,9216\nWL 10\nRa 168,24,10240\nMvav 244,184,68\nD1 1,16,46,2048\nMvav 252,188,68\n"
"Ra 172,24,11264\nD1 1,16,46,3072\ns_add_u32 m0, s89, 32768\ns_add_u32 s46, s86, s88\nRa 128,2,16384\nv_xor_b32 v3, 0x10000, v2\nv_add_u32 v0, 0x20000, v1\nEX 80,60\n"
"Ra 132,2,17408\ns_sub_u32 s94, s49, 1835008\nEX 81,61\nRa 136,2,18432\nAD 85,85,80\ns_xor_b32 s92, s94, 0x202000\nD1 1,16,46,0\ns_lshl_b32 s89, s11, 12\nRa 140,2,19456\n"
"s_lshl1_add_u32 s2, s12, 1\nAD 85,85,81\nCV 78,80,81\nRa 144,2,20480\ns_lshl3_add_u32 s2, s2, s13\nEX 82,62\nRa 148,2,21504\ns_lshl4_add_u32 s2, s2, 0xc048200\nEX 83,63\n"
"Ra 152,2,22528\nAD 85,85,82\nD1 1,16,46,1024\nRa 156,2,23552\ns_and_b32 s3, s72, 0x2000\nAD 85,85,83\nCV 79,82,83\nRa 160,2,24576\ns_cmp_eq_u32 s11, 1\ns_cselect_b32 s54, s2, s54\n"
"s_cselect_b32 s3, s3, 0\nv_cmp_lt_f32 vcc, s63, v85\nRa 164,2,25600\ns_cbranch_vccnz .T355\n.T356:\nAD 87,87,85\nWL 8\nv_mov_b32 v84, v86\nv_mov_b32 v85, v87\nRa 168,2,26624\n"
"v_permlane32_swap_b32 v86, v84\nv_permlane32_swap_b32 v87, v85\nD1 1,16,46,2048\nAD 86,86,84\nRa 172,2,27648\nAD 87,87,85\nv_mov_b32 v84, v86\nv_mov_b32 v85, v87\nRa 176,2,28672\n"
"v_permlane16_swap_b32 v86, v84\nv_permlane16_swap_b32 v87, v85\nMvav 132,128,76\nAD 86,86,84\nAD 87,87,85\nMvav 140,132,76\nRa 180,2,29696\nv_rcp_f32 v86, v86\nWL 8\n"
"v_rcp_f32 v87, v87\nRa 184,2,30720\nv_xor_b32 v25, s3, v25\nMvav 148,136,76\nD1 1,16,46,3072\nMvav 156,140,76\nRa 188,2,31744\nMU 128,128,86\nMU 129,129,86\nCV 96,128,129\n"
"MU 130,130,86\nMU 131,131,86\nRa 128,2,0\nCV 97,130,131\nMU 132,132,87\nMvav 164,144,76\nMU 133,133,87\nCV 100,132,133\nMvav 172,148,76\nRa 132,2,1024\nMU 134,134,87\n"
"MU 135,135,87\nWL 8\ns_add_u32 m0, s89, 131072\ns_add_u32 s46, s96, 1212416\nRa 136,2,2048\nCV 101,134,135\nMU 136,136,86\nMvav 180,152,76\nD1 1,16,46,0\nMU 137,137,86\n"
"Mvav 188,156,76\nRa 140,2,3072\nCV 98,136,137\nMU 138,138,86\nMU 139,139,86\nCV 99,138,139\nMU 140,140,87\nRa 144,2,4096\nMU 141,141,87\nCV 102,140,141\nMvav 196,160,76\n"
"MU 142,142,87\nMU 143,143,87\nMvav 204,164,76\nRa 148,2,5120\nCV 103,142,143\nMU 144,144,86\nWL 8\nMU 145,145,86\nCV 104,144,145\nRa 152,2,6144\nMU 146,146,86\nMU 147,147,86\n"
"Mvav 212,168,76\nD1 1,16,46,1024\nCV 105,146,147\nMvav 220,172,76\nRa 156,2,7168\nMU 148,148,87\nMU 149,149,87\nCV 108,148,149\nMU 150,150,87\nMU 151,151,87\nRa 160,2,8192\n"
"CV 109,150,151\nMU 152,152,86\nMvav 228,176,76\nMU 153,153,86\nCV 106,152,153\nMvav 236,180,76\nRa 164,2,9216\nMU 154,154,86\nMU 155,155,86\nWL 8\nCV 107,154,155\nMU 156,156,87\n"
"Ra 168,2,10240\nMU 157,157,87\nCV 110,156,157\nMvav 244,184,76\nD1 1,16,46,2048\nMU 158,158,87\nMvav 252,188,76\nRa 172,2,11264\nMU 159,159,87\nCV 111,158,159\nMU 160,160,86\n"
"MU 161,161,86\nCV 112,160,161\nZvav 128,128,96\nRa 176,2,12288\nMU 162,162,86\nMU 163,163,86\nMvav 128,132,104\nCV 113,162,163\nMU 164,164,87\nZvav 132,132,108\nRa 180,2,13312\n"
"MU 165,165,87\nCV 116,164,165\nMvav 132,128,100\ns_waitcnt vmcnt(15) lgkmcnt(8)\nMU 166,166,87\nMU 167,167,87\nZvav 140,136,100\nRa 184,2,14336\nCV 117,166,167\nMU 168,168,86\n"
"Mvav 140,140,108\nD1 1,16,46,3072\nMU 169,169,86\nCV 114,168,169\nZvav 136,140,104\nRa 188,2,15360\ns_barrier\nMU 170,170,86\nMvav 136,136,96\nMU 171,171,86\nCV 115,170,171\n"
"MU 172,172,87\nZvav 144,144,96\nRa 128,2,32768\nMU 173,173,87\nCV 118,172,173\nMvav 144,148,104\nMU 174,174,87\nMU 175,175,87\n"
"buffer_store_dword v25, off, s[16:19], s54 offset:512 sc0 sc1\nZvav 148,148,108\nRa 132,2,33792\nCV 119,174,175\nMU 176,176,86\nMvav 148,144,100\nWL 8\ns_add_u32 m0, s89, 49152\n"
"s_add_u32 s46, s96, 1179648\nZvav 156,152,100\nRa 136,2,34816\nMU 177,177,86\nCV 120,176,177\nMvav 156,156,108\nD1 1,16,46,0\nMU 178,178,86\nMU 179,179,86\nZvav 152,156,104\n"
"Ra 140,2,35840\nCV 121,178,179\nMU 180,180,87\nMvav 152,152,96\nMU 181,181,87\nCV 124,180,181\nMU 182,182,87\nZvav 64,160,96\nRa 144,2,36864\nMU 183,183,87\nCV 125,182,183\n"
"Mvav 64,164,104\nMU 184,184,86\nMU 185,185,86\nZvav 68,164,108\nRa 148,2,37888\nCV 122,184,185\nMU 186,186,86\nMvav 68,160,100\nWL 8\nMU 187,187,86\nCV 123,186,187\n"
"Zvav 76,168,100\nRa 152,2,38912\nMU 188,188,87\nMU 189,189,87\nMvav 76,172,108\nD1 1,16,46,1024\nCV 126,188,189\nMU 190,190,87\nZvav 72,172,104\nRa 156,2,39936\nMU 191,191,87\n"
"CV 127,190,191\nMvav 72,168,96\nMU 192,192,86\nMU 193,193,86\nCV 32,192,193\nMvav 128,176,112\nRa 160,2,40960\nMU 194,194,86\nMU 195,195,86\nMvav 128,180,120\nCV 33,194,195\n"
"MU 196,196,87\nMU 197,197,87\nMvav 132,180,124\nRa 164,2,41984\nCV 36,196,197\nMU 198,198,87\nMvav 132,176,116\nWL 8\nMU 199,199,87\nMvav 140,184,116\nRa 168,2,43008\n"
"CV 37,198,199\nMU 200,200,86\nMvav 140,188,124\nD1 1,16,46,2048\nMU 201,201,86\nCV 34,200,201\nMvav 136,188,120\nRa 172,2,44032\nMU 202,202,86\nMU 203,203,86\nMvav 136,184,112\n"
"CV 35,202,203\nMU 204,204,87\nMU 205,205,87\nMvav 144,128,112\nRa 176,2,45056\nCV 38,204,205\nMU 206,206,87\nMvav 144,132,120\nMU 207,207,87\nCV 39,206,207\nMvav 148,132,124\n"
"Ra 180,2,46080\nMU 208,208,86\nMU 209,209,86\nMvav 148,128,116\nWL 8\nCV 40,208,209\nMU 210,210,86\nMvav 156,136,116\nRa 184,2,47104\nMU 211,211,86\nCV 41,210,211\n"
"Mvav 156,140,124\nD1 1,16,46,3072\nMU 212,212,87\nMU 213,213,87\nMvav 152,140,120\nRa 188,2,48128\nCV 44,212,213\nMvav 152,136,112\nMU 214,214,87\nMU 215,215,87\nCV 45,214,215\n"
"Mvav 64,144,112\nRa 128,2,49152\nMU 216,216,86\nMU 217,217,86\nMvav 64,148,120\nCV 42,216,217\nMU 218,218,86\nMU 219,219,86\nMvav 68,148,124\nRa 132,2,50176\nCV 43,218,219\n"
"MU 220,220,87\nMvav 68,144,116\nWL 8\ns_add_u32 m0, s89, 147456\ns_add_u32 s46, s96, 1245184\nMvav 76,152,116\nRa 136,2,51200\nMU 221,221,87\nCV 46,220,221\nMvav 76,156,124\n"
"D1 1,16,46,0\nMU 222,222,87\nMU 223,223,87\nMvav 72,156,120\nRa 140,2,52224\nCV 47,222,223\nMvav 72,152,112\nMU 224,224,86\nMU 225,225,86\nCV 48,224,225\nMvav 128,160,32\n"
"Ra 144,2,53248\nMU 226,226,86\nMU 227,227,86\nMvav 128,164,40\nCV 49,226,227\nMU 228,228,87\nMvav 132,164,44\nRa 148,2,54272\nMU 229,229,87\nCV 52,228,229\nMvav 132,160,36\nWL 8\n"
"MU 230,230,87\nMU 231,231,87\nMvav 140,168,36\nRa 152,2,55296\nCV 53,230,231\nMU 232,232,86\nMvav 140,172,44\nD1 1,16,46,1024\nMU 233,233,86\nCV 50,232,233\nMvav 136,172,40\n"
"Ra 156,2,56320\nMU 234,234,86\nMvav 136,168,32\nMU 235,235,86\nCV 51,234,235\nMU 236,236,87\nMvav 144,176,32\nRa 160,2,57344\nMU 237,237,87\nCV 54,236,237\nMvav 144,180,40\n"
"MU 238,238,87\nMU 239,239,87\nCV 55,238,239\nMvav 148,180,44\nRa 164,2,58368\nMU 240,240,86\nMU 241,241,86\nMvav 148,176,36\nWL 8\nCV 56,240,241\nMvav 156,184,36\nRa 168,2,59392\n"
"MU 242,242,86\nMU 243,243,86\nMvav 156,188,44\nD1 1,16,46,2048\nCV 57,242,243\nMU 244,244,87\nMvav 152,188,40\nRa 172,2,60416\nMU 245,245,87\nCV 60,244,245\nMvav 152,184,32\n"
"MU 246,246,87\nMU 247,247,87\nCV 61,246,247\nMvav 64,128,32\nRa 176,2,61440\nMU 248,248,86\nMU 249,249,86\nMvav 64,132,40\nCV 58,248,249\nMU 250,250,86\nMvav 68,132,44\n"
"Ra 180,2,62464\nMU 251,251,86\nCV 59,250,251\nMvav 68,128,36\ns_waitcnt vmcnt(16) lgkmcnt(8)\nMU 252,252,87\nMU 253,253,87\nMvav 76,136,36\nRa 184,2,63488\nCV 62,252,253\n"
"MU 254,254,87\nMvav 76,140,44\nD1 1,16,46,3072\nMU 255,255,87\nCV 63,254,255\nMvav 72,140,40\nRa 188,2,64512\ns_barrier\nMvav 72,136,32\nMvav 128,144,48\n.W582:\n"
"s_cbranch_execz .WB274\nRa 128,3,0\nMvav 128,148,56\nMvav 132,148,60\nRa 132,3,1024\nMvav 132,144,52\nWL 8\nMvav 140,152,52\nRa 136,3,2048\nMvav 140,156,60\nMvav 136,156,56\n"
"Ra 140,3,3072\nMvav 136,152,48\nMvav 144,160,48\nRa 144,3,4096\nMvav 144,164,56\nMvav 148,164,60\nRa 148,3,5120\nCV 132,132,133\nMvav 148,160,52\nWL 8\nCV 133,134,135\n"
"Mvav 156,168,52\nRa 152,3,6144\nCV 134,140,141\nMvav 156,172,60\nCV 135,142,143\ns_add_u32 s5, s94, 0\nMvav 152,172,56\nRa 156,3,7168\nSv 132,1,16,5,1024\nMvav 152,168,48\n"
"CV 128,128,129\nCV 129,130,131\nMvav 64,176,48\nRa 160,3,8192\nCV 130,136,137\nMvav 64,180,56\nbuffer_load_dword v27, off, s[16:19], s60 offset:512 sc0 sc1\nCV 131,138,139\n"
"Mvav 68,180,60\nRa 164,3,9216\nSv 128,1,16,5,0\nMvav 68,176,52\nWL 6\nCV 148,148,149\nMvav 76,184,52\nRa 168,3,10240\nCV 149,150,151\nMvav 76,188,60\nCV 150,156,157\n"
"CV 151,158,159\nMvav 72,188,56\nRa 172,3,11264\ns_add_u32 s5, s94, 131072\nMvav 72,184,48\nSv 148,1,16,5,1024\nCV 144,144,145\nZvav 160,128,96\nRa 176,3,12288\nCV 145,146,147\n"
"Mvav 160,132,104\nCV 146,152,153\nCV 147,154,155\nMvav 160,136,112\nRa 180,3,13312\nSv 144,1,16,5,0\nMvav 160,140,120\nWL 6\nCV 68,68,69\nZvav 164,140,124\nRa 184,3,14336\n"
"CV 69,70,71\nMvav 164,136,116\nCV 70,76,77\nCV 71,78,79\nMvav 164,132,108\nRa 188,3,15360\ns_add_u32 s5, s94, 262144\nMvav 164,128,100\nSv 68,1,16,5,1024\nCV 64,64,65\n"
"Mvav 164,144,36\nRa 128,3,16384\nCV 65,66,67\nMvav 164,148,44\nCV 66,72,73\nCV 67,74,75\nMvav 164,152,52\nRa 132,3,17408\nSv 64,1,16,5,0\nMvav 164,156,60\nWL 6\nMvav 160,156,56\n"
"Ra 136,3,18432\nMvav 160,152,48\nMvav 160,148,40\nRa 140,3,19456\nMvav 160,144,32\nZvav 168,160,96\nRa 144,3,20480\nMvav 168,164,104\nMvav 168,168,112\nRa 148,3,21504\n"
"Mvav 168,172,120\nWL 6\nZvav 172,172,124\nRa 152,3,22528\nMvav 172,168,116\nMvav 172,164,108\nRa 156,3,23552\nMvav 172,160,100\nMvav 172,176,36\nRa 160,3,24576\nMvav 172,180,44\n"
"Mvav 172,184,52\nRa 164,3,25600\nMvav 172,188,60\nWL 6\nMvav 168,188,56\nRa 168,3,26624\nMvav 168,184,48\nMvav 168,180,40\nRa 172,3,27648\nMvav 168,176,32\nZvav 176,128,96\n"
"Ra 176,3,28672\nCV 164,164,165\nMvav 176,132,104\nCV 165,166,167\nCV 166,172,173\nMvav 176,136,112\nRa 180,3,29696\nCV 167,174,175\nMvav 176,140,120\n"
"s_waitcnt vmcnt(11) lgkmcnt(6)\ns_add_u32 s5, s94, 393216\nZvav 180,140,124\nRa 184,3,30720\nSv 164,1,16,5,1024\nMvav 180,136,116\nCV 160,160,161\nCV 161,162,163\n"
"Mvav 180,132,108\nRa 188,3,31744\ns_barrier\nMvav 180,128,100\nCV 162,168,169\nCV 163,170,171\nMvav 180,144,36\nRa 128,3,32768\nSv 160,1,16,5,0\nMvav 180,148,44\nMvav 180,152,52\n"
"Ra 132,3,33792\nMvav 180,156,60\nWL 6\ns_add_u32 m0, s89, 81920\ns_add_u32 s46, s96, 1277952\nMvav 176,156,56\nRa 136,3,34816\nMvav 176,152,48\nD1 1,16,46,0\nMvav 176,148,40\n"
"Ra 140,3,35840\nMvav 176,144,32\nZvav 184,160,96\nRa 144,3,36864\nMvav 184,164,104\nMvav 184,168,112\nRa 148,3,37888\nMvav 184,172,120\nWL 6\nZvav 188,172,124\nRa 152,3,38912\n"
"Mvav 188,168,116\nD1 1,16,46,1024\nMvav 188,164,108\nRa 156,3,39936\nMvav 188,160,100\nMvav 188,176,36\nRa 160,3,40960\nWV 9\nv_cmp_ne_u32 vcc, s15, v27\ns_nop 0\n"
"s_cbranch_vccnz .S381\n.S382:\nMvav 188,180,44\ns_add_u32 s4, s92, 0\nMvav 188,184,52\nRa 164,3,41984\nMvav 188,188,60\nWL 6\n"
"buffer_load_dwordx4 v[208:211], v1, s[16:19], s4 offen offset:0 sc0 sc1\nMvav 184,188,56\nRa 168,3,43008\nMvav 184,184,48\nD1 1,16,46,2048\nMvav 184,180,40\nRa 172,3,44032\n"
"Mvav 184,176,32\nbuffer_load_dwordx4 v[212:215], v1, s[16:19], s4 offen offset:1024 sc0 sc1\nZvav 192,128,96\nRa 176,3,45056\nv_lshrrev_b32 v88, 8, v1\nMvav 192,132,104\n"
"v_lshlrev_b32 v89, 5, v88\nCV 180,180,181\nMvav 192,136,112\nRa 180,3,46080\nCV 181,182,183\nMvav 192,140,120\nWL 6\nCV 182,188,189\nZvav 196,140,124\nRa 184,3,47104\n"
"v_bfe_u32 v90, v1, 4, 4\nMvav 196,136,116\nD1 1,16,46,3072\nCV 183,190,191\nMvav 196,132,108\nRa 188,3,48128\ns_add_u32 s5, s94, 524288\nMvav 196,128,100\n"
"v_mul_u32_u24 v91, 896, v90\nSv 180,1,16,5,1024\nMvav 196,144,36\nRa 128,3,49152\nv_lshl_add_u32 v91, v88, 4, v91\nMvav 196,148,44\nv_add_u32 v90, s21, v90\nCV 176,176,177\n"
"Mvav 196,152,52\nRa 132,3,50176\nCV 177,178,179\nMvav 196,156,60\nWL 6\ns_add_u32 m0, s89, 65536\ns_add_u32 s46, s96, 1310720\nMvav 192,156,56\nRa 136,3,51200\n"
"v_lshlrev_b32 v88, 3, v90\nMvav 192,152,48\nD1 1,16,46,0\nCV 178,184,185\nMvav 192,148,40\nRa 140,3,52224\nCV 179,186,187\nMvav 192,144,32\nv_add_u32 v94, 64, v90\n"
"Sv 176,1,16,5,0\nZvav 200,160,96\nRa 144,3,53248\ns_mov_b32 s63, 0xc040000\nMvav 200,164,104\nv_lshrrev_b32 v92, 8, v1\nMvav 200,168,112\nRa 148,3,54272\nMvav 200,172,120\nWL 6\n"
"Zvav 204,172,124\nRa 152,3,55296\nv_bfe_u32 v93, v1, 6, 2\nMvav 204,168,116\nD1 1,16,46,1024\nv_cmp_eq_u32 vcc, v92, v93\nv_and_b32 v92, 16, v1\nv_mov_b32 v93, 0x3f80\n"
"v_cndmask_b32 v93, 0, v93, vcc\nMvav 204,164,108\nRa 156,3,56320\nMvav 204,160,100\nv_lshlrev_b32 v93, v92, v93\nMvav 204,176,36\nRa 160,3,57344\nv_and_b32 v92, 32, v1\n"
"Mvav 204,180,44\ns_add_u32 s4, s92, 131072\nv_cmp_eq_u32 vcc, 0, v92\nv_mov_b32 v4, 0\nv_mov_b32 v5, 0\nv_cndmask_b32 v6, 0, v93, vcc\nMvav 204,184,52\nRa 164,3,58368\n"
"Mvav 204,188,60\nWL 6\nbuffer_load_dwordx4 v[216:219], v1, s[16:19], s4 offen offset:0 sc0 sc1\nMvav 200,188,56\nRa 168,3,59392\nv_sub_u32 v7, v93, v6\nMvav 200,184,48\n"
"D1 1,16,46,2048\nMvav 200,180,40\nRa 172,3,60416\nMvav 200,176,32\nbuffer_load_dwordx4 v[220:223], v1, s[16:19], s4 offen offset:1024 sc0 sc1\nZvav 160,128,96\nRa 176,3,61440\n"
"v_mov_b32 v8, 0\nMvav 160,132,104\nv_mov_b32 v9, 0\nCV 196,196,197\nMvav 160,136,112\nRa 180,3,62464\nCV 197,198,199\nMvav 160,140,120\nWL 6\nCV 198,204,205\nZvav 164,140,124\n"
"Ra 184,3,63488\nCV 199,206,207\nMvav 164,136,116\nD1 1,16,46,3072\ns_add_u32 s5, s94, 655360\nMvav 164,132,108\nRa 188,3,64512\ns_barrier\nMvav 164,128,100\nSv 196,1,16,5,1024\n"
"CV 192,192,193\nMvav 164,144,36\nRa 128,0,0\nCV 193,194,195\nMvav 164,148,44\nCV 194,200,201\nCV 195,202,203\nMvav 164,152,52\nRa 132,0,1024\nSv 192,1,16,5,0\nMvav 164,156,60\n"
"WL 6\ns_add_u32 m0, s89, 98304\ns_add_u32 s46, s96, 1343488\nMvav 160,156,56\nRa 136,0,2048\nMvav 160,152,48\nD1 1,16,46,0\nMvav 160,148,40\nRa 140,0,3072\nMvav 160,144,32\n"
"Zvav 168,160,96\nRa 144,0,4096\nWV 12\nv_max_u32 v21, v211, v215\nv_cmp_eq_u32 vcc, -1, v21\ns_nop 0\ns_cbranch_vccnz .S357\n.S358:\nMvav 168,164,104\n"
"v_lshlrev_b32 v176, 16, v208\nMvav 168,168,112\nRa 148,0,5120\nMvav 168,172,120\nWL 6\nv_and_b32 v177, 0xffff0000, v208\nZvav 172,172,124\nRa 152,0,6144\nMvav 172,168,116\n"
"D1 1,16,46,1024\nMvav 172,164,108\nRa 156,0,7168\nv_lshlrev_b32 v178, 16, v209\nMvav 172,160,100\nv_and_b32 v179, 0xffff0000, v209\nMvav 172,176,36\nRa 160,0,8192\n"
"v_lshlrev_b32 v180, 16, v212\nMvav 172,180,44\nv_and_b32 v181, 0xffff0000, v212\ns_add_u32 s4, s92, 262144\nMvav 172,184,52\nRa 164,0,9216\nMvav 172,188,60\nWL 6\n"
"v_lshlrev_b32 v182, 16, v213\nbuffer_load_dwordx4 v[224:227], v1, s[16:19], s4 offen offset:0 sc0 sc1\nMvav 168,188,56\nRa 168,0,10240\nMvav 168,184,48\nD1 1,16,46,2048\n"
"Mvav 168,180,40\nRa 172,0,11264\nv_and_b32 v183, 0xffff0000, v213\nMvav 168,176,32\nv_lshlrev_b32 v184, 16, v210\n"
"buffer_load_dwordx4 v[228:231], v1, s[16:19], s4 offen offset:1024 sc0 sc1\nMvav 176,128,96\nRa 176,0,12288\nv_and_b32 v185, 0xffff0000, v210\nMvav 176,132,104\n"
"v_lshlrev_b32 v186, 16, v211\nCV 164,164,165\nMvav 176,136,112\nRa 180,0,13312\nCV 165,166,167\nMvav 176,140,120\nWL 6\nCV 166,172,173\nMvav 180,140,124\nRa 184,0,14336\n"
"v_and_b32 v187, 0xffff0000, v211\nMvav 180,136,116\nD1 1,16,46,3072\nCV 167,174,175\nMvav 180,132,108\nRa 188,0,15360\ns_add_u32 s5, s94, 786432\nMvav 180,128,100\n"
"v_lshlrev_b32 v188, 16, v214\nSv 164,1,16,5,1024\nMvav 180,144,36\nRa 128,0,16384\nv_and_b32 v189, 0xffff0000, v214\nMvav 180,148,44\nv_lshlrev_b32 v190, 16, v215\nCV 160,160,161\n"
"Mvav 180,152,52\nRa 132,0,17408\nCV 161,162,163\nMvav 180,156,60\nWL 6\ns_add_u32 m0, s89, 114688\ns_add_u32 s46, s96, 1376256\nMvav 176,156,56\nRa 136,0,18432\n"
"v_and_b32 v191, 0xffff0000, v215\nMvav 176,152,48\nD1 1,16,46,0\nCV 162,168,169\nMvav 176,148,40\nRa 140,0,19456\nCV 163,170,171\nMvav 176,144,32\nSv 160,1,16,5,0\n"
"Mvav 184,160,96\nRa 144,0,20480\nWV 12\nv_max_u32 v21, v219, v223\nv_cmp_eq_u32 vcc, -1, v21\ns_nop 0\ns_cbranch_vccnz .S360\n.S361:\nMvav 184,164,104\n"
"v_lshlrev_b32 v192, 16, v216\nMvav 184,168,112\nRa 148,0,21504\nMvav 184,172,120\nWL 6\nv_and_b32 v193, 0xffff0000, v216\nMvav 188,172,124\nRa 152,0,22528\nMvav 188,168,116\n"
"D1 1,16,46,1024\nMvav 188,164,108\nRa 156,0,23552\nv_lshlrev_b32 v194, 16, v217\nMvav 188,160,100\nv_and_b32 v195, 0xffff0000, v217\nMvav 188,176,36\nRa 160,0,24576\n"
"v_lshlrev_b32 v196, 16, v220\nMvav 188,180,44\nv_and_b32 v197, 0xffff0000, v220\ns_add_u32 s4, s92, 393216\nMvav 188,184,52\nRa 164,0,25600\nMvav 188,188,60\nWL 6\n"
"v_lshlrev_b32 v198, 16, v221\nbuffer_load_dwordx4 v[208:211], v1, s[16:19], s4 offen offset:0 sc0 sc1\nMvav 184,188,56\nRa 168,0,26624\nMvav 184,184,48\nD1 1,16,46,2048\n"
"Mvav 184,180,40\nRa 172,0,27648\nv_and_b32 v199, 0xffff0000, v221\nMvav 184,176,32\nv_lshlrev_b32 v200, 16, v218\n"
"buffer_load_dwordx4 v[212:215], v1, s[16:19], s4 offen offset:1024 sc0 sc1\nMvav 192,128,96\nRa 176,0,28672\nv_and_b32 v201, 0xffff0000, v218\nMvav 192,132,104\n"
"v_lshlrev_b32 v202, 16, v219\nCV 180,180,181\nMvav 192,136,112\nRa 180,0,29696\nCV 181,182,183\nMvav 192,140,120\ns_waitcnt vmcnt(15) lgkmcnt(6)\nCV 182,188,189\nMvav 196,140,124\n"
"Ra 184,0,30720\nv_and_b32 v203, 0xffff0000, v219\nMvav 196,136,116\nD1 1,16,46,3072\nCV 183,190,191\nMvav 196,132,108\nRa 188,0,31744\ns_barrier\nMvav 196,128,100\n"
"v_lshlrev_b32 v204, 16, v222\ns_add_u32 s5, s94, 917504\nMvav 196,144,36\nRa 128,2,16384\nv_and_b32 v205, 0xffff0000, v222\nMvav 196,148,44\nv_lshlrev_b32 v206, 16, v223\n"
"Sv 180,1,16,5,1024\nMvav 196,152,52\nRa 132,2,17408\nCV 176,176,177\nMvav 196,156,60\nWL 6\ns_add_u32 m0, s89, 0\ns_add_u32 s46, s96, 1409024\nMvav 192,156,56\nRa 136,2,18432\n"
"v_and_b32 v207, 0xffff0000, v223\nMvav 192,152,48\nD1 1,16,46,0\nCV 177,178,179\nMvav 192,148,40\nRa 140,2,19456\nCV 178,184,185\nMvav 192,144,32\nCV 179,186,187\nSv 176,1,16,5,0\n"
"Mvav 200,160,96\nRa 144,2,20480\nWV 12\nv_max_u32 v21, v227, v231\nv_cmp_eq_u32 vcc, -1, v21\ns_nop 0\ns_cbranch_vccnz .S363\n.S364:\nMvav 200,164,104\n"
"v_lshlrev_b32 v160, 16, v224\nMvav 200,168,112\nRa 148,2,21504\nMvav 200,172,120\nWL 6\nv_and_b32 v161, 0xffff0000, v224\nMvav 204,172,124\nRa 152,2,22528\nMvav 204,168,116\n"
"D1 1,16,46,1024\nMvav 204,164,108\nRa 156,2,23552\nv_lshlrev_b32 v162, 16, v225\nMvav 204,160,100\nv_and_b32 v163, 0xffff0000, v225\nMvav 204,176,36\nRa 160,2,24576\n"
"v_lshlrev_b32 v164, 16, v228\nMvav 204,180,44\nv_and_b32 v165, 0xffff0000, v228\ns_add_u32 s4, s92, 524288\nMvav 204,184,52\nRa 164,2,25600\n.W583:\ns_cbranch_execz .WB274\n"
"Mvav 204,188,60\nWL 6\nv_lshlrev_b32 v166, 16, v229\nbuffer_load_dwordx4 v[216:219], v1, s[16:19], s4 offen offset:0 sc0 sc1\nMvav 200,188,56\nRa 168,2,26624\nMvav 200,184,48\n"
"D1 1,16,46,2048\nMvav 200,180,40\nRa 172,2,27648\nv_and_b32 v167, 0xffff0000, v229\nMvav 200,176,32\nv_lshlrev_b32 v168, 16, v226\n"
"buffer_load_dwordx4 v[220:223], v1, s[16:19], s4 offen offset:1024 sc0 sc1\nMvav 160,128,96\nRa 176,2,28672\nv_and_b32 v169, 0xffff0000, v226\nMvav 160,132,104\n"
"v_lshlrev_b32 v170, 16, v227\nCV 196,196,197\nMvav 160,136,112\nRa 180,2,29696\nCV 197,198,199\nMvav 160,140,120\nWL 6\nCV 198,204,205\nMvav 164,140,124\nRa 184,2,30720\n"
"v_and_b32 v24, 0x70, v1\nMvav 164,136,116\nD1 1,16,46,3072\nCV 199,206,207\nMvav 164,132,108\nRa 188,2,31744\nv_and_b32 v171, 0xffff0000, v227\nMvav 164,128,100\n"
"v_lshlrev_b32 v172, 16, v230\ns_add_u32 s5, s94, 1048576\nMvav 164,144,36\nRa 128,2,0\nv_lshlrev_b32 v24, 5, v24\nMvav 164,148,44\nv_and_b32 v173, 0xffff0000, v230\n"
"Sv 196,1,16,5,1024\nMvav 164,152,52\nRa 132,2,1024\nCV 192,192,193\nMvav 164,156,60\nWL 6\nv_lshlrev_b32 v174, 16, v231\nMvav 160,156,56\nRa 136,2,2048\n"
"buffer_load_dword v27, v24, s[16:19], s41 offen sc0 sc1\nMvav 160,152,48\nv_and_b32 v175, 0xffff0000, v231\nCV 193,194,195\nMvav 160,148,40\nRa 140,2,3072\nCV 194,200,201\n"
"Mvav 160,144,32\nCV 195,202,203\nSv 192,1,16,5,0\nMvav 168,160,96\nRa 144,2,4096\nWV 12\nv_max_u32 v21, v211, v215\nv_cmp_eq_u32 vcc, -1, v21\ns_nop 0\ns_cbranch_vccnz .S366\n"
".S367:\nMvav 168,164,104\nv_lshlrev_b32 v176, 16, v208\nMvav 168,168,112\nRa 148,2,5120\nMvav 168,172,120\nWL 6\nMvav 172,172,124\nRa 152,2,6144\nv_and_b32 v177, 0xffff0000, v208\n"
"Mvav 172,168,116\nv_lshlrev_b32 v178, 16, v209\nMvav 172,164,108\nRa 156,2,7168\nMvav 172,160,100\nv_and_b32 v179, 0xffff0000, v209\nMvav 172,176,36\nRa 160,2,8192\n"
"v_lshlrev_b32 v180, 16, v212\nMvav 172,180,44\nv_and_b32 v181, 0xffff0000, v212\ns_add_u32 s4, s92, 655360\nMvav 172,184,52\nRa 164,2,9216\nMvav 172,188,60\nWL 6\n"
"buffer_load_dwordx4 v[224:227], v1, s[16:19], s4 offen offset:0 sc0 sc1\nMvav 168,188,56\nRa 168,2,10240\nv_lshlrev_b32 v182, 16, v213\nMvav 168,184,48\n"
"v_and_b32 v183, 0xffff0000, v213\nMvav 168,180,40\nRa 172,2,11264\nMvav 168,176,32\nv_lshlrev_b32 v184, 16, v210\n"
"buffer_load_dwordx4 v[228:231], v1, s[16:19], s4 offen offset:1024 sc0 sc1\nMvav 176,128,96\nRa 176,2,12288\nv_and_b32 v185, 0xffff0000, v210\nMvav 176,132,104\n"
"v_lshlrev_b32 v186, 16, v211\nCV 164,164,165\nMvav 176,136,112\nRa 180,2,13312\nCV 165,166,167\nMvav 176,140,120\ns_waitcnt vmcnt(13) lgkmcnt(6)\nCV 166,172,173\nMvav 180,140,124\n"
"Ra 184,2,14336\nCV 167,174,175\nMvav 180,136,116\nv_and_b32 v187, 0xffff0000, v211\ns_add_u32 s5, s94, 1179648\nMvav 180,132,108\nRa 188,2,15360\ns_barrier\nMvav 180,128,100\n"
"v_lshlrev_b32 v188, 16, v214\nSv 164,1,16,5,1024\nMvav 180,144,36\nRa 128,2,32768\nv_and_b32 v189, 0xffff0000, v214\nMvav 180,148,44\nv_lshlrev_b32 v190, 16, v215\nCV 160,160,161\n"
"Mvav 180,152,52\nRa 132,2,33792\nCV 161,162,163\nMvav 180,156,60\nWL 6\nCV 162,168,169\nMvav 176,156,56\nRa 136,2,34816\nCV 163,170,171\nMvav 176,152,48\n"
"v_and_b32 v191, 0xffff0000, v215\nSv 160,1,16,5,0\nMvav 176,148,40\nRa 140,2,35840\nMvav 176,144,32\nMvav 184,160,96\nRa 144,2,36864\nWV 8\nv_max_u32 v21, v219, v223\n"
"v_cmp_eq_u32 vcc, -1, v21\ns_nop 0\ns_cbranch_vccnz .S369\n.S370:\nMvav 184,164,104\nv_lshlrev_b32 v192, 16, v216\nMvav 184,168,112\nRa 148,2,37888\nMvav 184,172,120\nWL 6\n"
"Mvav 188,172,124\nRa 152,2,38912\nv_and_b32 v193, 0xffff0000, v216\nMvav 188,168,116\nv_lshlrev_b32 v194, 16, v217\nMvav 188,164,108\nRa 156,2,39936\nMvav 188,160,100\n"
"v_and_b32 v195, 0xffff0000, v217\nMvav 188,176,36\nRa 160,2,40960\nv_lshlrev_b32 v196, 16, v220\nMvav 188,180,44\nv_and_b32 v197, 0xffff0000, v220\ns_add_u32 s4, s92, 786432\n"
"Mvav 188,184,52\nRa 164,2,41984\nMvav 188,188,60\nWL 6\nbuffer_load_dwordx4 v[208:211], v1, s[16:19], s4 offen offset:0 sc0 sc1\nMvav 184,188,56\nRa 168,2,43008\n"
"v_lshlrev_b32 v198, 16, v221\nMvav 184,184,48\nv_and_b32 v199, 0xffff0000, v221\nMvav 184,180,40\nRa 172,2,44032\nMvav 184,176,32\nv_lshlrev_b32 v200, 16, v218\n"
"buffer_load_dwordx4 v[212:215], v1, s[16:19], s4 offen offset:1024 sc0 sc1\nMvav 192,128,96\nRa 176,2,45056\nv_and_b32 v201, 0xffff0000, v218\nMvav 192,132,104\n"
"v_lshlrev_b32 v202, 16, v219\nCV 180,180,181\nMvav 192,136,112\nRa 180,2,46080\nCV 181,182,183\nMvav 192,140,120\nWL 6\nCV 182,188,189\nMvav 196,140,124\nRa 184,2,47104\n"
"CV 183,190,191\nMvav 196,136,116\nv_and_b32 v203, 0xffff0000, v219\ns_add_u32 s5, s94, 1310720\nMvav 196,132,108\nRa 188,2,48128\nSv 180,1,16,5,1024\nMvav 196,128,100\n"
"v_lshlrev_b32 v204, 16, v222\nCV 176,176,177\nMvav 196,144,36\nRa 128,2,49152\nv_and_b32 v205, 0xffff0000, v222\nMvav 196,148,44\nv_lshlrev_b32 v206, 16, v223\nCV 177,178,179\n"
"Mvav 196,152,52\nRa 132,2,50176\nCV 178,184,185\nMvav 196,156,60\nWL 6\nCV 179,186,187\nMvav 192,156,56\nRa 136,2,51200\nSv 176,1,16,5,0\nMvav 192,152,48\n"
"v_and_b32 v207, 0xffff0000, v223\nMvav 192,148,40\nRa 140,2,52224\nMvav 192,144,32\nMvav 200,160,96\nRa 144,2,53248\nWV 6\nv_max_u32 v21, v227, v231\nv_cmp_eq_u32 vcc, -1, v21\n"
"s_nop 0\ns_cbranch_vccnz .S372\n.S373:\nMvav 200,164,104\nv_lshlrev_b32 v160, 16, v224\nv_xor_b32 v27, s15, v27\nv_and_b32 v26, 0xffffdfff, v27\nv_cmp_ne_u32 s[64:65], 0, v27\n"
"v_cmp_ne_u32 vcc, 0, v26\ns_nop 0\ns_cbranch_vccnz .S384\n.S385:\nMvav 200,168,112\nRa 148,2,54272\ns_bitcmp1_b32 s64, 0\ns_cselect_b32 s2, 0x2000, 0\nMvav 200,172,120\nWL 6\n"
"Mvav 204,172,124\nRa 152,2,55296\nv_and_b32 v161, 0xffff0000, v224\nMvav 204,168,116\nv_lshlrev_b32 v162, 16, v225\nMvav 204,164,108\nRa 156,2,56320\ns_xor_b32 s2, s2, s20\n"
"Mvav 204,160,100\nv_and_b32 v163, 0xffff0000, v225\nMvav 204,176,36\nRa 160,2,57344\nv_lshlrev_b32 v164, 16, v228\nMvav 204,180,44\nv_and_b32 v165, 0xffff0000, v228\n"
"Mvav 204,184,52\nRa 164,2,58368\nMvav 204,188,60\nWL 6\ns_add_u32 s24, s2, 0x0\nMvav 200,188,56\nRa 168,2,59392\nv_lshlrev_b32 v166, 16, v229\nMvav 200,184,48\n"
"v_and_b32 v167, 0xffff0000, v229\ns_bitcmp1_b32 s64, 1\ns_cselect_b32 s2, 0x2000, 0\nMvav 200,180,40\nRa 172,2,60416\nMvav 200,176,32\nv_lshlrev_b32 v168, 16, v226\n"
"Mvav 160,128,96\nRa 176,2,61440\nv_and_b32 v169, 0xffff0000, v226\nMvav 160,132,104\nv_lshlrev_b32 v170, 16, v227\nCV 196,196,197\nMvav 160,136,112\nRa 180,2,62464\n"
"CV 197,198,199\nMvav 160,140,120\nWL 6\nCV 198,204,205\nMvav 164,140,124\nRa 184,2,63488\nCV 199,206,207\nMvav 164,136,116\nv_and_b32 v171, 0xffff0000, v227\n"
"s_add_u32 s5, s94, 1441792\nMvav 164,132,108\nRa 188,2,64512\ns_barrier\nMvav 164,128,100\nv_lshlrev_b32 v172, 16, v230\nSv 196,1,16,5,1024\nMvav 164,144,36\nRa 128,3,0\n"
"v_and_b32 v173, 0xffff0000, v230\nMvav 164,148,44\nv_lshlrev_b32 v174, 16, v231\nCV 192,192,193\nMvav 164,152,52\nRa 132,3,1024\nCV 193,194,195\nMvav 164,156,60\nWL 6\n"
"s_xor_b32 s2, s2, s20\nMvav 160,156,56\nRa 136,3,2048\nCV 194,200,201\nMvav 160,152,48\nv_and_b32 v175, 0xffff0000, v231\nCV 195,202,203\nMvav 160,148,40\nRa 140,3,3072\n"
"Sv 192,1,16,5,0\nMvav 160,144,32\ns_add_u32 s25, s2, 0x1800000\ns_bitcmp1_b32 s64, 2\ns_cselect_b32 s2, 0x2000, 0\nMvav 168,160,96\nRa 144,3,4096\nWV 4\nv_max_u32 v21, v211, v215\n"
"v_cmp_eq_u32 vcc, -1, v21\ns_nop 0\ns_cbranch_vccnz .S375\n.S376:\nMvav 168,164,104\nv_lshlrev_b32 v176, 16, v208\nMvav 168,168,112\nRa 148,3,5120\nMvav 168,172,120\nWL 6\n"
"Mvav 172,172,124\nRa 152,3,6144\nv_and_b32 v177, 0xffff0000, v208\nMvav 172,168,116\nv_lshlrev_b32 v178, 16, v209\nMvav 172,164,108\nRa 156,3,7168\ns_xor_b32 s2, s2, s20\n"
"Mvav 172,160,100\nv_and_b32 v179, 0xffff0000, v209\nMvav 172,176,36\nRa 160,3,8192\nv_lshlrev_b32 v180, 16, v212\nMvav 172,180,44\nv_and_b32 v181, 0xffff0000, v212\n"
"Mvav 172,184,52\nRa 164,3,9216\nMvav 172,188,60\nWL 6\ns_add_u32 s26, s2, 0x3000000\nMvav 168,188,56\nRa 168,3,10240\nv_lshlrev_b32 v182, 16, v213\nMvav 168,184,48\n"
"v_and_b32 v183, 0xffff0000, v213\ns_bitcmp1_b32 s64, 3\ns_cselect_b32 s2, 0x2000, 0\nMvav 168,180,40\nRa 172,3,11264\nMvav 168,176,32\nv_lshlrev_b32 v184, 16, v210\n"
"Mvav 176,128,96\nRa 176,3,12288\ns_xor_b32 s2, s2, s20\nMvav 176,132,104\nv_and_b32 v185, 0xffff0000, v210\nCV 164,164,165\nMvav 176,136,112\nRa 180,3,13312\nCV 165,166,167\n"
"Mvav 176,140,120\nWL 6\nCV 166,172,173\nMvav 180,140,124\nRa 184,3,14336\nCV 167,174,175\nMvav 180,136,116\nv_lshlrev_b32 v186, 16, v211\ns_add_u32 s5, s94, 1572864\n"
"Mvav 180,132,108\nRa 188,3,15360\ns_add_u32 s27, s2, 0x4800000\nMvav 180,128,100\nv_and_b32 v187, 0xffff0000, v211\ns_bitcmp1_b32 s64, 4\ns_cselect_b32 s2, 0x2000, 0\n"
"Mvav 180,144,36\nv_lshlrev_b32 v188, 16, v214\nSv 164,1,16,5,1024\nMvav 180,148,44\nv_and_b32 v189, 0xffff0000, v214\nCV 160,160,161\nMvav 180,152,52\n"
"v_lshlrev_b32 v190, 16, v215\nCV 161,162,163\nMvav 180,156,60\nWL 4\ns_xor_b32 s2, s2, s20\nMvav 176,156,56\nv_and_b32 v191, 0xffff0000, v215\nCV 162,168,169\nMvav 176,152,48\n"
"s_add_u32 s32, s2, 0x6000000\ns_bitcmp1_b32 s64, 5\ns_cselect_b32 s2, 0x2000, 0\nMvav 176,148,40\ns_xor_b32 s2, s2, s20\nCV 163,170,171\nMvav 176,144,32\n"
"s_add_u32 s33, s2, 0x7800000\ns_bitcmp1_b32 s64, 6\ns_cselect_b32 s2, 0x2000, 0\nMvav 184,160,96\ns_xor_b32 s2, s2, s20\ns_add_u32 s34, s2, 0x9000000\nMvav 184,164,104\n"
"s_bitcmp1_b32 s64, 7\ns_cselect_b32 s2, 0x2000, 0\nMvav 184,168,112\ns_xor_b32 s2, s2, s20\nSv 160,1,16,5,0\nMvav 184,172,120\nWL 0\nMvav 188,172,124\n"
"s_add_u32 s35, s2, 0xa800000\nMvav 188,168,116\nMvav 188,164,108\nMvav 188,160,100\nMvav 188,176,36\nMvav 188,180,44\nMvav 188,184,52\nMvav 188,188,60\nMvav 184,188,56\n"
"Mvav 184,184,48\nMvav 184,180,40\nMvav 184,176,32\nCV 180,180,181\nCV 181,182,183\ns_nop 1\nCV 182,188,189\nCV 183,190,191\ns_add_u32 s5, s94, 1703936\nSv 180,1,16,5,1024\n"
"CV 176,176,177\nCV 177,178,179\nCV 178,184,185\nCV 179,186,187\nSv 176,1,16,5,0\ns_cmp_eq_u32 s13, 7\ns_cbranch_scc1 .Z387\n"
"buffer_load_dwordx4 v[12:15], v91, s[28:31], s22 offen offset:0\nbuffer_load_dwordx4 v[16:19], v91, s[28:31], s23 offen offset:0\n"
"buffer_load_dwordx4 v[64:67], v89, s[16:19], s40 offen offset:0 sc0 sc1\nbuffer_load_dwordx4 v[68:71], v89, s[16:19], s40 offen offset:16 sc0 sc1\n"
"buffer_load_dwordx4 v[72:75], v89, s[16:19], s40 offen offset:1792 sc0 sc1\nbuffer_load_dwordx4 v[76:79], v89, s[16:19], s40 offen offset:1808 sc0 sc1\n"
"buffer_load_dwordx2 v[80:81], v88, s[16:19], s63 offen offset:0 sc0 sc1\nbuffer_load_dwordx2 v[82:83], v88, s[16:19], s63 offen offset:512 sc0 sc1\n"
"buffer_load_dwordx4 v[96:99], v1, s[16:19], s24 offen offset:0 sc0 sc1\nbuffer_load_dwordx4 v[100:103], v1, s[16:19], s24 offen offset:1024 sc0 sc1\n"
"buffer_load_dwordx4 v[104:107], v1, s[16:19], s25 offen offset:0 sc0 sc1\nbuffer_load_dwordx4 v[108:111], v1, s[16:19], s25 offen offset:1024 sc0 sc1\n"
"buffer_load_dwordx4 v[112:115], v1, s[16:19], s26 offen offset:0 sc0 sc1\nbuffer_load_dwordx4 v[116:119], v1, s[16:19], s26 offen offset:1024 sc0 sc1\n"
"buffer_load_dwordx4 v[120:123], v1, s[16:19], s27 offen offset:0 sc0 sc1\nbuffer_load_dwordx4 v[124:127], v1, s[16:19], s27 offen offset:1024 sc0 sc1\n"
"buffer_load_dwordx4 v[32:35], v1, s[16:19], s32 offen offset:0 sc0 sc1\nbuffer_load_dwordx4 v[36:39], v1, s[16:19], s32 offen offset:1024 sc0 sc1\n"
"buffer_load_dwordx4 v[40:43], v1, s[16:19], s33 offen offset:0 sc0 sc1\nbuffer_load_dwordx4 v[44:47], v1, s[16:19], s33 offen offset:1024 sc0 sc1\n"
"buffer_load_dwordx4 v[48:51], v1, s[16:19], s34 offen offset:0 sc0 sc1\nbuffer_load_dwordx4 v[52:55], v1, s[16:19], s34 offen offset:1024 sc0 sc1\n"
"buffer_load_dwordx4 v[56:59], v1, s[16:19], s35 offen offset:0 sc0 sc1\nbuffer_load_dwordx4 v[60:63], v1, s[16:19], s35 offen offset:1024 sc0 sc1\nWV 16\nv_xor_b32 v66, s15, v66\n"
"v_xor_b32 v70, s15, v70\nv_xor_b32 v74, s15, v74\nv_xor_b32 v78, s15, v78\nv_xor_b32 v81, s15, v81\nv_xor_b32 v83, s15, v83\nv_or3_b32 v92, v66, v70, v74\n"
"v_or3_b32 v92, v92, v78, v81\nv_or_b32 v92, v92, v83\nv_cmp_ne_u32 vcc, 0, v92\ns_nop 0\ns_cbranch_vccnz .S378\n.S379:\nv_and_b32 v92, 0x7ff, v67\nv_lshrrev_b32 v93, 16, v67\n"
"v_cmp_eq_u32 s[44:45], v92, v90\nv_cmp_eq_u32 s[46:47], v93, v90\nv_cmp_eq_u32 s[48:49], v92, v94\nv_cmp_eq_u32 s[50:51], v93, v94\nv_cndmask_b32 v12, v12, v64, s[44:45]\n"
"v_cndmask_b32 v13, v13, v65, s[44:45]\nv_cndmask_b32 v14, v14, v68, s[44:45]\nv_cndmask_b32 v15, v15, v69, s[44:45]\nv_cndmask_b32 v12, v12, v72, s[46:47]\n"
"v_cndmask_b32 v13, v13, v73, s[46:47]\nv_cndmask_b32 v14, v14, v76, s[46:47]\nv_cndmask_b32 v15, v15, v77, s[46:47]\nv_cndmask_b32 v16, v16, v64, s[48:49]\n"
"v_cndmask_b32 v17, v17, v65, s[48:49]\nv_cndmask_b32 v18, v18, v68, s[48:49]\nv_cndmask_b32 v19, v19, v69, s[48:49]\nv_cndmask_b32 v16, v16, v72, s[50:51]\n"
"v_cndmask_b32 v17, v17, v73, s[50:51]\nv_cndmask_b32 v18, v18, v76, s[50:51]\nv_cndmask_b32 v19, v19, v77, s[50:51]\nv_sub_f32 v92, 1.0, v80\nv_sub_f32 v93, 1.0, v82\nCV 92,92,93\n"
"v_and_b32 v93, 0xffff0000, v92\nv_lshlrev_b32 v92, 16, v92\nv_lshlrev_b32 v64, 16, v12\nv_and_b32 v65, 0xffff0000, v12\nMU 64,64,92\nMU 65,65,92\nv_lshlrev_b32 v66, 16, v13\n"
"v_and_b32 v67, 0xffff0000, v13\nMU 66,66,92\nMU 67,67,92\nv_lshlrev_b32 v68, 16, v14\nv_and_b32 v69, 0xffff0000, v14\nMU 68,68,92\nMU 69,69,92\nv_lshlrev_b32 v70, 16, v15\n"
"v_and_b32 v71, 0xffff0000, v15\nMU 70,70,92\nMU 71,71,92\nv_lshlrev_b32 v72, 16, v16\nv_and_b32 v73, 0xffff0000, v16\nMU 72,72,93\nMU 73,73,93\nv_lshlrev_b32 v74, 16, v17\n"
"v_and_b32 v75, 0xffff0000, v17\nMU 74,74,93\nMU 75,75,93\nv_lshlrev_b32 v76, 16, v18\nv_and_b32 v77, 0xffff0000, v18\nMU 76,76,93\nMU 77,77,93\nv_lshlrev_b32 v78, 16, v19\n"
"v_and_b32 v79, 0xffff0000, v19\nMU 78,78,93\nMU 79,79,93\ns_mov_b32 s7, 262144\nWV 0\n.Z390:\nv_max_u32 v84, v99, v103\nv_cmp_eq_u32 s[44:45], -1, v84\nv_max_u32 v84, v107, v111\n"
"v_cmp_eq_u32 s[46:47], -1, v84\nv_max_u32 v84, v115, v119\nv_cmp_eq_u32 s[48:49], -1, v84\nv_max_u32 v84, v123, v127\nv_cmp_eq_u32 s[50:51], -1, v84\nv_max_u32 v84, v35, v39\n"
"v_cmp_eq_u32 s[52:53], -1, v84\nv_max_u32 v84, v43, v47\nv_cmp_eq_u32 s[54:55], -1, v84\nv_max_u32 v84, v51, v55\nv_cmp_eq_u32 s[56:57], -1, v84\nv_max_u32 v84, v59, v63\n"
"v_cmp_eq_u32 s[58:59], -1, v84\ns_or_b64 s[0:1], s[44:45], s[46:47]\ns_or_b64 s[2:3], s[48:49], s[50:51]\ns_or_b64 s[0:1], s[0:1], s[52:53]\ns_or_b64 s[2:3], s[2:3], s[54:55]\n"
".W584:\ns_cbranch_execz .WB274\ns_or_b64 s[0:1], s[0:1], s[56:57]\ns_or_b64 s[2:3], s[2:3], s[58:59]\ns_or_b64 s[0:1], s[0:1], s[2:3]\ns_cbranch_scc1 .Z388\n.Z389:\nZvvv 136,6,96\n"
"Mvvv 136,6,104\nMvvv 136,6,112\nMvvv 136,6,120\nMvvv 136,6,32\nMvvv 136,6,40\nMvvv 136,6,48\nMvvv 136,6,56\nZvvv 140,4,96\nMvvv 140,4,104\nMvvv 140,4,112\nMvvv 140,4,120\n"
"Mvvv 140,4,32\nMvvv 140,4,40\nMvvv 140,4,48\nMvvv 140,4,56\nZvvv 144,6,100\nMvvv 144,6,108\nMvvv 144,6,116\nMvvv 144,6,124\nMvvv 144,6,36\nMvvv 144,6,44\nMvvv 144,6,52\n"
"Mvvv 144,6,60\nZvvv 148,4,100\nMvvv 148,4,108\nMvvv 148,4,116\nMvvv 148,4,124\nMvvv 148,4,36\nMvvv 148,4,44\nMvvv 148,4,52\nMvvv 148,4,60\nFM 64,80,136\nFM 65,80,137\n"
"FM 66,80,138\nFM 67,80,139\nFM 68,80,140\nFM 69,80,141\nFM 70,80,142\nFM 71,80,143\nCV 12,64,65\nCV 13,66,67\nCV 14,68,69\nCV 15,70,71\nSv 12,91,36,22,0\nFM 72,82,144\n"
"FM 73,82,145\nFM 74,82,146\nFM 75,82,147\nFM 76,82,148\nFM 77,82,149\nFM 78,82,150\nFM 79,82,151\nCV 16,72,73\nCV 17,74,75\nCV 18,76,77\nCV 19,78,79\nSv 16,91,36,23,0\n.Z387:\n"
"s_endpgm\n.EP399:\n.S270:\ns_mov_b32 s7, 262144\n.S272:\ns_lshl_b32 s5, s73, 8\ns_add_u32 s5, s5, 0xc04ac00\nv_lshrrev_b32 v27, 2, v1\n"
"buffer_load_dword v27, v27, s[16:19], s5 offen sc0 sc1\nWV 0\nv_xor_b32 v27, s15, v27\nv_and_b32 v32, 0xffffdfff, v27\nv_cmp_ne_u32 vcc, 0, v32\ns_nop 0\ns_cbranch_vccz .S271\n"
"s_sleep 6\ns_sub_u32 s7, s7, 1\ns_cbranch_scc0 .S272\ns_branch .S271\n.T279:\ns_mov_b32 s62, 1\ns_call_b64 s[80:81], .RA266\ns_branch .T280\n.T281:\ns_mov_b32 s62, 1\n"
"s_call_b64 s[80:81], .RA266\ns_branch .T282\n.S284:\ns_mov_b32 s7, 262144\n.S286:\nbuffer_load_dword v27, off, s[16:19], s5 sc0 sc1\nWV 0\nv_cmp_ne_u32 vcc, s15, v27\ns_nop 0\n"
"s_cbranch_vccz .S285\ns_sleep 6\ns_sub_u32 s7, s7, 1\ns_cbranch_scc0 .S286\ns_branch .S285\n.S287:\ns_mov_b32 s7, 262144\n.S289:\nv_and_b32 v27, 0x70, v1\n"
"buffer_load_dword v27, v27, s[16:19], s61 offen offset:128 sc0 sc1\nWV 0\nv_cmp_ne_u32 vcc, s15, v27\ns_nop 0\ns_cbranch_vccz .S288\ns_sleep 6\ns_sub_u32 s7, s7, 1\n"
"s_cbranch_scc0 .S289\ns_branch .S288\n.S290:\ns_mov_b32 s7, 262144\n.S292:\nv_and_b32 v27, 0x70, v1\nbuffer_load_dword v27, v27, s[16:19], s61 offen offset:136 sc0 sc1\nWV 0\n"
"v_cmp_ne_u32 vcc, s15, v27\ns_nop 0\ns_cbranch_vccz .S291\ns_sleep 6\ns_sub_u32 s7, s7, 1\ns_cbranch_scc0 .S292\ns_branch .S291\n.S293:\ns_mov_b32 s7, 262144\n.S295:\n"
"v_and_b32 v27, 0x70, v1\nbuffer_load_dword v27, v27, s[16:19], s61 offen offset:140 sc0 sc1\nWV 0\nv_cmp_ne_u32 vcc, s15, v27\ns_nop 0\ns_cbranch_vccz .S294\ns_sleep 6\n"
"s_sub_u32 s7, s7, 1\ns_cbranch_scc0 .S295\ns_branch .S294\n.S296:\ns_mov_b32 s7, 262144\n.S298:\nv_and_b32 v27, 0x70, v1\n"
"buffer_load_dword v27, v27, s[16:19], s61 offen offset:12 sc0 sc1\nWV 0\nv_cmp_ne_u32 vcc, s15, v27\ns_nop 0\ns_cbranch_vccz .S297\ns_sleep 6\ns_sub_u32 s7, s7, 1\n"
"s_cbranch_scc0 .S298\ns_branch .S297\n.S299:\ns_mov_b32 s7, 262144\n.S301:\nv_and_b32 v27, 0x70, v1\nbuffer_load_dword v27, v27, s[16:19], s61 offen offset:8 sc0 sc1\nWV 0\n"
"v_cmp_ne_u32 vcc, s15, v27\ns_nop 0\ns_cbranch_vccz .S300\ns_sleep 6\ns_sub_u32 s7, s7, 1\ns_cbranch_scc0 .S301\ns_branch .S300\n.S302:\ns_mov_b32 s7, 262144\n.S304:\n"
"v_and_b32 v27, 0xf0, v1\nbuffer_load_dword v27, v27, s[16:19], s61 offen offset:4 sc0 sc1\nWV 0\nv_cmp_ne_u32 vcc, s15, v27\ns_nop 0\ns_cbranch_vccz .S303\ns_sleep 6\n"
"s_sub_u32 s7, s7, 1\ns_cbranch_scc0 .S304\ns_branch .S303\n.T313:\ns_mov_b32 s62, 0\ns_call_b64 s[80:81], .RB267\ns_branch .T314\n.T315:\ns_mov_b32 s62, 0\n"
"s_call_b64 s[80:81], .RA266\ns_branch .T316\n.T319:\ns_mov_b32 s62, 0\ns_call_b64 s[80:81], .RB267\ns_branch .T320\n.T321:\ns_mov_b32 s62, 0\ns_call_b64 s[80:81], .RA266\n"
"s_branch .T322\n.T328:\ns_mov_b32 s62, 2\ns_call_b64 s[80:81], .RB267\ns_branch .T329\n.T330:\ns_mov_b32 s62, 5\ns_call_b64 s[80:81], .RA266\ns_branch .T331\n.T332:\n"
"s_mov_b32 s62, 5\ns_call_b64 s[80:81], .RA266\ns_branch .T333\n.T344:\ns_mov_b32 s62, 4\ns_call_b64 s[80:81], .RB267\ns_branch .T345\n.T346:\ns_mov_b32 s62, 4\n"
"s_call_b64 s[80:81], .RA266\ns_branch .T347\n.T351:\ns_mov_b32 s62, 4\ns_call_b64 s[80:81], .RB267\ns_branch .T352\n.T353:\ns_mov_b32 s62, 4\ns_call_b64 s[80:81], .RA266\n"
"s_branch .T354\n.T355:\ns_mov_b32 s62, 6\ns_call_b64 s[80:81], .RB267\ns_branch .T356\n.S357:\ns_mov_b32 s7, 262144\n.S359:\ns_add_u32 s0, s92, 0\n"
"buffer_load_dwordx4 v[208:211], v1, s[16:19], s0 offen offset:0 sc0 sc1\nbuffer_load_dwordx4 v[212:215], v1, s[16:19], s0 offen offset:1024 sc0 sc1\nWV 0\n"
"v_max_u32 v21, v211, v215\nv_cmp_eq_u32 vcc, -1, v21\ns_nop 0\ns_cbranch_vccz .S358\ns_sleep 6\ns_sub_u32 s7, s7, 1\ns_cbranch_scc0 .S359\ns_branch .S358\n.S360:\n"
"s_mov_b32 s7, 262144\n.S362:\ns_add_u32 s0, s92, 131072\nbuffer_load_dwordx4 v[216:219], v1, s[16:19], s0 offen offset:0 sc0 sc1\n"
"buffer_load_dwordx4 v[220:223], v1, s[16:19], s0 offen offset:1024 sc0 sc1\nWV 0\nv_max_u32 v21, v219, v223\nv_cmp_eq_u32 vcc, -1, v21\ns_nop 0\ns_cbranch_vccz .S361\ns_sleep 6\n"
"s_sub_u32 s7, s7, 1\ns_cbranch_scc0 .S362\ns_branch .S361\n.S363:\ns_mov_b32 s7, 262144\n.S365:\ns_add_u32 s0, s92, 262144\n"
"buffer_load_dwordx4 v[224:227], v1, s[16:19], s0 offen offset:0 sc0 sc1\nbuffer_load_dwordx4 v[228:231], v1, s[16:19], s0 offen offset:1024 sc0 sc1\nWV 0\n"
"v_max_u32 v21, v227, v231\nv_cmp_eq_u32 vcc, -1, v21\ns_nop 0\ns_cbranch_vccz .S364\ns_sleep 6\ns_sub_u32 s7, s7, 1\ns_cbranch_scc0 .S365\ns_branch .S364\n.S366:\n"
"s_mov_b32 s7, 262144\n.S368:\ns_add_u32 s0, s92, 393216\nbuffer_load_dwordx4 v[208:211], v1, s[16:19], s0 offen offset:0 sc0 sc1\n"
"buffer_load_dwordx4 v[212:215], v1, s[16:19], s0 offen offset:1024 sc0 sc1\nWV 0\nv_max_u32 v21, v211, v215\nv_cmp_eq_u32 vcc, -1, v21\ns_nop 0\ns_cbranch_vccz .S367\ns_sleep 6\n"
"s_sub_u32 s7, s7, 1\ns_cbranch_scc0 .S368\ns_branch .S367\n.S369:\ns_mov_b32 s7, 262144\n.S371:\ns_add_u32 s0, s92, 524288\n"
"buffer_load_dwordx4 v[216:219], v1, s[16:19], s0 offen offset:0 sc0 sc1\nbuffer_load_dwordx4 v[220:223], v1, s[16:19], s0 offen offset:1024 sc0 sc1\nWV 0\n"
"v_max_u32 v21, v219, v223\nv_cmp_eq_u32 vcc, -1, v21\ns_nop 0\ns_cbranch_vccz .S370\ns_sleep 6\ns_sub_u32 s7, s7, 1\ns_cbranch_scc0 .S371\ns_branch .S370\n.S372:\n"
"s_mov_b32 s7, 262144\n.S374:\ns_add_u32 s0, s92, 655360\nbuffer_load_dwordx4 v[224:227], v1, s[16:19], s0 offen offset:0 sc0 sc1\n"
"buffer_load_dwordx4 v[228:231], v1, s[16:19], s0 offen offset:1024 sc0 sc1\nWV 0\nv_max_u32 v21, v227, v231\nv_cmp_eq_u32 vcc, -1, v21\ns_nop 0\ns_cbranch_vccz .S373\ns_sleep 6\n"
"s_sub_u32 s7, s7, 1\ns_cbranch_scc0 .S374\ns_branch .S373\n.S375:\ns_mov_b32 s7, 262144\n.S377:\ns_add_u32 s0, s92, 786432\n"
"buffer_load_dwordx4 v[208:211], v1, s[16:19], s0 offen offset:0 sc0 sc1\nbuffer_load_dwordx4 v[212:215], v1, s[16:19], s0 offen offset:1024 sc0 sc1\nWV 0\n"
"v_max_u32 v21, v211, v215\nv_cmp_eq_u32 vcc, -1, v21\ns_nop 0\ns_cbranch_vccz .S376\ns_sleep 6\ns_sub_u32 s7, s7, 1\ns_cbranch_scc0 .S377\ns_branch .S376\n.S378:\n"
"s_mov_b32 s7, 262144\n.S380:\nbuffer_load_dwordx4 v[64:67], v89, s[16:19], s40 offen offset:0 sc0 sc1\nbuffer_load_dwordx4 v[68:71], v89, s[16:19], s40 offen offset:16 sc0 sc1\n"
"buffer_load_dwordx4 v[72:75], v89, s[16:19], s40 offen offset:1792 sc0 sc1\nbuffer_load_dwordx4 v[76:79], v89, s[16:19], s40 offen offset:1808 sc0 sc1\n"
"buffer_load_dwordx2 v[80:81], v88, s[16:19], s63 offen offset:0 sc0 sc1\nbuffer_load_dwordx2 v[82:83], v88, s[16:19], s63 offen offset:512 sc0 sc1\nWV 0\nv_xor_b32 v66, s15, v66\n"
"v_xor_b32 v70, s15, v70\nv_xor_b32 v74, s15, v74\nv_xor_b32 v78, s15, v78\nv_xor_b32 v81, s15, v81\nv_xor_b32 v83, s15, v83\nv_or3_b32 v92, v66, v70, v74\n"
"v_or3_b32 v92, v92, v78, v81\nv_or_b32 v92, v92, v83\nv_cmp_ne_u32 vcc, 0, v92\ns_nop 0\ns_cbranch_vccz .S379\ns_sleep 6\ns_sub_u32 s7, s7, 1\ns_cbranch_scc0 .S380\n"
"s_branch .S379\n.S381:\ns_mov_b32 s7, 262144\n.S383:\nbuffer_load_dword v27, off, s[16:19], s60 offset:512 sc0 sc1\nWV 0\nv_cmp_ne_u32 vcc, s15, v27\ns_nop 0\n"
"s_cbranch_vccz .S382\ns_sleep 6\ns_sub_u32 s7, s7, 1\ns_cbranch_scc0 .S383\ns_branch .S382\n.S384:\ns_mov_b32 s7, 262144\n.S386:\n"
"buffer_load_dword v27, v24, s[16:19], s41 offen sc0 sc1\nWV 0\nv_xor_b32 v27, s15, v27\nv_and_b32 v26, 0xffffdfff, v27\nv_cmp_ne_u32 s[64:65], 0, v27\nv_cmp_ne_u32 vcc, 0, v26\n"
"s_nop 0\ns_cbranch_vccz .S385\ns_sleep 6\ns_sub_u32 s7, s7, 1\ns_cbranch_scc0 .S386\ns_branch .S385\n.Z388:\ns_sub_u32 s7, s7, 1\ns_cbranch_scc1 .Z389\ns_cmp_lg_u64 s[44:45], 0\n"
"s_cbranch_scc0 .Z391\nbuffer_load_dwordx4 v[96:99], v1, s[16:19], s24 offen offset:0 sc0 sc1\nbuffer_load_dwordx4 v[100:103], v1, s[16:19], s24 offen offset:1024 sc0 sc1\n.Z391:\n"
"s_cmp_lg_u64 s[46:47], 0\ns_cbranch_scc0 .Z392\nbuffer_load_dwordx4 v[104:107], v1, s[16:19], s25 offen offset:0 sc0 sc1\n"
"buffer_load_dwordx4 v[108:111], v1, s[16:19], s25 offen offset:1024 sc0 sc1\n.Z392:\ns_cmp_lg_u64 s[48:49], 0\ns_cbranch_scc0 .Z393\n"
"buffer_load_dwordx4 v[112:115], v1, s[16:19], s26 offen offset:0 sc0 sc1\nbuffer_load_dwordx4 v[116:119], v1, s[16:19], s26 offen offset:1024 sc0 sc1\n.Z393:\n"
"s_cmp_lg_u64 s[50:51], 0\ns_cbranch_scc0 .Z394\nbuffer_load_dwordx4 v[120:123], v1, s[16:19], s27 offen offset:0 sc0 sc1\n"
"buffer_load_dwordx4 v[124:127], v1, s[16:19], s27 offen offset:1024 sc0 sc1\n.Z394:\ns_cmp_lg_u64 s[52:53], 0\ns_cbranch_scc0 .Z395\n"
"buffer_load_dwordx4 v[32:35], v1, s[16:19], s32 offen offset:0 sc0 sc1\nbuffer_load_dwordx4 v[36:39], v1, s[16:19], s32 offen offset:1024 sc0 sc1\n.Z395:\n"
"s_cmp_lg_u64 s[54:55], 0\ns_cbranch_scc0 .Z396\nbuffer_load_dwordx4 v[40:43], v1, s[16:19], s33 offen offset:0 sc0 sc1\n"
"buffer_load_dwordx4 v[44:47], v1, s[16:19], s33 offen offset:1024 sc0 sc1\n.Z396:\ns_cmp_lg_u64 s[56:57], 0\ns_cbranch_scc0 .Z397\n"
"buffer_load_dwordx4 v[48:51], v1, s[16:19], s34 offen offset:0 sc0 sc1\nbuffer_load_dwordx4 v[52:55], v1, s[16:19], s34 offen offset:1024 sc0 sc1\n.Z397:\n"
"s_cmp_lg_u64 s[58:59], 0\ns_cbranch_scc0 .Z398\nbuffer_load_dwordx4 v[56:59], v1, s[16:19], s35 offen offset:0 sc0 sc1\n"
"buffer_load_dwordx4 v[60:63], v1, s[16:19], s35 offen offset:1024 sc0 sc1\n.Z398:\ns_sleep 8\nWV 0\ns_branch .Z390\n.RA266:\ns_nop 7\ns_nop 3\nv_max3_f32 v22, v32, v33, v34\n"
"v_max3_f32 v22, v22, v35, v40\nv_max3_f32 v22, v22, v41, v42\nv_max_f32 v22, v22, v43\nv_mov_b32 v91, v22\ns_nop 1\nv_permlane32_swap_b32 v22, v91\nv_max_f32 v22, v22, v91\n"
"v_mov_b32 v91, v22\ns_nop 1\nv_permlane16_swap_b32 v22, v91\nv_max_f32 v22, v22, v91\nv_ceil_f32 v22, v22\nv_max3_f32 v23, v36, v37, v38\nv_max3_f32 v23, v23, v39, v44\n"
"v_max3_f32 v23, v23, v45, v46\nv_max_f32 v23, v23, v47\nv_mov_b32 v91, v23\ns_nop 1\nv_permlane32_swap_b32 v23, v91\nv_max_f32 v23, v23, v91\nv_mov_b32 v91, v23\ns_nop 1\n"
"v_permlane16_swap_b32 v23, v91\nv_max_f32 v23, v23, v91\nv_ceil_f32 v23, v23\ns_bitcmp1_b32 s62, 0\ns_cbranch_scc0 .R400\nv_cmp_lt_f32 vcc, 0xff000000, v22\ns_nop 1\n"
"v_cndmask_b32 v22, 0, v22, vcc\nv_cmp_lt_f32 vcc, 0xff000000, v23\ns_nop 1\nv_cndmask_b32 v23, 0, v23, vcc\ns_branch .R401\n.R400:\nv_max_f32 v22, 0, v22\nv_max_f32 v23, 0, v23\n"
"s_bitcmp1_b32 s62, 3\ns_cbranch_scc0 .R401\nv_max_f32 v22, v20, v22\nv_max_f32 v23, v21, v23\n.R401:\nAD 88,88,22\nAD 89,89,23\nSU 32,32,22\nSU 33,33,22\nSU 34,34,22\nSU 35,35,22\n"
"SU 36,36,23\nSU 37,37,23\nSU 38,38,23\nSU 39,39,23\nSU 40,40,22\nSU 41,41,22\nSU 42,42,22\nSU 43,43,22\nSU 44,44,23\nSU 45,45,23\nSU 46,46,23\nSU 47,47,23\nv_mov_b32 v90, v80\n"
"v_mov_b32 v91, v81\nEX 80,32\nEX 81,33\ns_nop 0\nAD 84,80,81\nCV 64,80,81\nEX 82,34\nEX 83,35\nAD 84,84,82\nAD 84,84,83\nCV 65,82,83\nEX 80,36\nEX 81,37\ns_nop 0\nAD 85,80,81\n"
"CV 68,80,81\nEX 82,38\nEX 83,39\nAD 85,85,82\nAD 85,85,83\nCV 69,82,83\nEX 80,40\nEX 81,41\nAD 84,84,80\nAD 84,84,81\nCV 66,80,81\nEX 82,42\nEX 83,43\nAD 84,84,82\nAD 84,84,83\n"
"CV 67,82,83\nEX 80,44\nEX 81,45\nAD 85,85,80\nAD 85,85,81\nCV 70,80,81\nEX 82,46\nEX 83,47\nAD 85,85,82\nAD 85,85,83\nCV 71,82,83\nv_mov_b32 v80, v90\nv_mov_b32 v81, v91\n"
"s_bitcmp1_b32 s62, 1\ns_cbranch_scc1 .R402\nSU 48,48,22\nSU 49,49,22\nSU 50,50,22\nSU 51,51,22\nSU 52,52,23\nSU 53,53,23\nSU 54,54,23\nSU 55,55,23\nSU 56,56,22\nSU 57,57,22\n"
"SU 58,58,22\nSU 59,59,22\nSU 60,60,23\nSU 61,61,23\nSU 62,62,23\nSU 63,63,23\nSU 4,4,22\nSU 5,5,22\nSU 6,6,22\nSU 7,7,22\nSU 8,8,23\nSU 9,9,23\nSU 10,10,23\nSU 11,11,23\n"
"SU 12,12,22\nSU 13,13,22\nSU 14,14,22\nSU 15,15,22\nSU 16,16,23\nSU 17,17,23\nSU 18,18,23\nSU 19,19,23\nEX 80,48\nEX 81,49\n.R402:\ns_bitcmp1_b32 s62, 0\ns_cbranch_scc0 .RS268\n"
"s_nop 1\ns_setpc_b64 s[80:81]\n.RB267:\ns_nop 7\ns_nop 3\nv_max3_f32 v22, v48, v49, v50\nv_max3_f32 v22, v22, v51, v56\nv_max3_f32 v22, v22, v57, v58\nv_max_f32 v22, v22, v59\n"
"v_mov_b32 v91, v22\ns_nop 1\nv_permlane32_swap_b32 v22, v91\nv_max_f32 v22, v22, v91\nv_mov_b32 v91, v22\ns_nop 1\nv_permlane16_swap_b32 v22, v91\nv_max_f32 v22, v22, v91\n"
"v_ceil_f32 v22, v22\nv_max3_f32 v23, v52, v53, v54\nv_max3_f32 v23, v23, v55, v60\nv_max3_f32 v23, v23, v61, v62\nv_max_f32 v23, v23, v63\nv_mov_b32 v91, v23\ns_nop 1\n"
"v_permlane32_swap_b32 v23, v91\nv_max_f32 v23, v23, v91\nv_mov_b32 v91, v23\ns_nop 1\nv_permlane16_swap_b32 v23, v91\nv_max_f32 v23, v23, v91\nv_ceil_f32 v23, v23\n"
"s_bitcmp1_b32 s62, 0\ns_cbranch_scc0 .R405\nv_cmp_lt_f32 vcc, 0xff000000, v22\nv_cndmask_b32 v22, 0, v22, vcc\nv_cmp_lt_f32 vcc, 0xff000000, v23\nv_cndmask_b32 v23, 0, v23, vcc\n"
"s_branch .R406\n.R405:\nv_max_f32 v22, 0, v22\nv_max_f32 v23, 0, v23\ns_bitcmp1_b32 s62, 3\ns_cbranch_scc0 .R406\nv_max_f32 v22, v20, v22\nv_max_f32 v23, v21, v23\n.R406:\n"
"AD 88,88,22\nAD 89,89,23\nSU 48,48,22\nSU 49,49,22\nSU 50,50,22\nSU 51,51,22\nSU 52,52,23\nSU 53,53,23\nSU 54,54,23\nSU 55,55,23\nSU 56,56,22\nSU 57,57,22\nSU 58,58,22\n"
"SU 59,59,22\nSU 60,60,23\nSU 61,61,23\nSU 62,62,23\nSU 63,63,23\nv_mov_b32 v90, v80\nv_mov_b32 v91, v81\nEX 80,48\nEX 81,49\ns_nop 0\nAD 84,80,81\nCV 72,80,81\nEX 82,50\nEX 83,51\n"
"AD 84,84,82\nAD 84,84,83\nCV 73,82,83\nEX 80,52\nEX 81,53\ns_nop 0\nAD 85,80,81\nCV 76,80,81\nEX 82,54\nEX 83,55\nAD 85,85,82\nAD 85,85,83\nCV 77,82,83\nEX 80,56\nEX 81,57\n"
"AD 84,84,80\nAD 84,84,81\nCV 74,80,81\nEX 82,58\nEX 83,59\nAD 84,84,82\nAD 84,84,83\nCV 75,82,83\nEX 80,60\nEX 81,61\nAD 85,85,80\nAD 85,85,81\nCV 78,80,81\nEX 82,62\nEX 83,63\n"
"AD 85,85,82\nAD 85,85,83\nCV 79,82,83\nv_mov_b32 v80, v90\nv_mov_b32 v81, v91\ns_bitcmp1_b32 s62, 1\ns_cbranch_scc1 .R407\nSU 32,32,22\nSU 33,33,22\nSU 34,34,22\nSU 35,35,22\n"
"SU 36,36,23\nSU 37,37,23\nSU 38,38,23\nSU 39,39,23\nSU 40,40,22\nSU 41,41,22\nSU 42,42,22\nSU 43,43,22\nSU 44,44,23\nSU 45,45,23\nSU 46,46,23\nSU 47,47,23\nSU 4,4,22\nSU 5,5,22\n"
"SU 6,6,22\nSU 7,7,22\nSU 8,8,23\nSU 9,9,23\nSU 10,10,23\nSU 11,11,23\nSU 12,12,22\nSU 13,13,22\nSU 14,14,22\nSU 15,15,22\nSU 16,16,23\nSU 17,17,23\nSU 18,18,23\nSU 19,19,23\n"
"EX 80,32\nEX 81,33\n.R407:\ns_bitcmp1_b32 s62, 0\ns_cbranch_scc0 .RS268\ns_nop 1\ns_setpc_b64 s[80:81]\n.RS268:\nv_exp_f32 v22, -v22\nv_exp_f32 v23, -v23\nMU 86,86,22\n"
"MU 87,87,23\ns_bitcmp1_b32 s62, 2\ns_cbranch_scc1 .R410\nAR 91,0\nMU 91,91,22\nAW 0,91\nAR 91,1\nMU 91,91,22\nAW 1,91\nAR 91,2\nMU 91,91,22\nAW 2,91\nAR 91,3\nMU 91,91,22\n"
"AW 3,91\nAR 91,4\nMU 91,91,23\nAW 4,91\nAR 91,5\nMU 91,91,23\nAW 5,91\nAR 91,6\nMU 91,91,23\nAW 6,91\nAR 91,7\nMU 91,91,23\nAW 7,91\nAR 91,8\nMU 91,91,22\nAW 8,91\nAR 91,9\n"
"MU 91,91,22\nAW 9,91\nAR 91,10\nMU 91,91,22\nAW 10,91\nAR 91,11\nMU 91,91,22\nAW 11,91\nAR 91,12\nMU 91,91,23\nAW 12,91\nAR 91,13\nMU 91,91,23\nAW 13,91\nAR 91,14\nMU 91,91,23\n"
"AW 14,91\nAR 91,15\nMU 91,91,23\nAW 15,91\nAR 91,16\nMU 91,91,22\nAW 16,91\nAR 91,17\nMU 91,91,22\nAW 17,91\nAR 91,18\nMU 91,91,22\nAW 18,91\nAR 91,19\nMU 91,91,22\nAW 19,91\n"
"AR 91,20\nMU 91,91,23\nAW 20,91\nAR 91,21\nMU 91,91,23\nAW 21,91\nAR 91,22\nMU 91,91,23\nAW 22,91\nAR 91,23\nMU 91,91,23\nAW 23,91\nAR 91,24\nMU 91,91,22\nAW 24,91\nAR 91,25\n"
"MU 91,91,22\nAW 25,91\nAR 91,26\nMU 91,91,22\nAW 26,91\nAR 91,27\nMU 91,91,22\nAW 27,91\nAR 91,28\nMU 91,91,23\nAW 28,91\nAR 91,29\nMU 91,91,23\nAW 29,91\nAR 91,30\nMU 91,91,23\n"
"AW 30,91\nAR 91,31\nMU 91,91,23\nAW 31,91\nAR 91,32\nMU 91,91,22\nAW 32,91\nAR 91,33\nMU 91,91,22\nAW 33,91\nAR 91,34\nMU 91,91,22\nAW 34,91\nAR 91,35\nMU 91,91,22\nAW 35,91\n"
"AR 91,36\nMU 91,91,23\nAW 36,91\nAR 91,37\nMU 91,91,23\nAW 37,91\nAR 91,38\nMU 91,91,23\nAW 38,91\nAR 91,39\nMU 91,91,23\nAW 39,91\nAR 91,40\nMU 91,91,22\nAW 40,91\nAR 91,41\n"
"MU 91,91,22\nAW 41,91\nAR 91,42\nMU 91,91,22\nAW 42,91\nAR 91,43\nMU 91,91,22\nAW 43,91\nAR 91,44\nMU 91,91,23\nAW 44,91\nAR 91,45\nMU 91,91,23\nAW 45,91\nAR 91,46\nMU 91,91,23\n"
"AW 46,91\nAR 91,47\nMU 91,91,23\nAW 47,91\nAR 91,48\nMU 91,91,22\nAW 48,91\nAR 91,49\nMU 91,91,22\nAW 49,91\nAR 91,50\nMU 91,91,22\nAW 50,91\nAR 91,51\nMU 91,91,22\nAW 51,91\n"
"AR 91,52\nMU 91,91,23\nAW 52,91\nAR 91,53\nMU 91,91,23\nAW 53,91\nAR 91,54\nMU 91,91,23\nAW 54,91\nAR 91,55\nMU 91,91,23\nAW 55,91\nAR 91,56\nMU 91,91,22\nAW 56,91\nAR 91,57\n"
"MU 91,91,22\nAW 57,91\nAR 91,58\nMU 91,91,22\nAW 58,91\nAR 91,59\nMU 91,91,22\nAW 59,91\nAR 91,60\nMU 91,91,23\nAW 60,91\nAR 91,61\nMU 91,91,23\nAW 61,91\nAR 91,62\nMU 91,91,23\n"
"AW 62,91\nAR 91,63\nMU 91,91,23\nAW 63,91\nAR 91,64\nMU 91,91,22\nAW 64,91\nAR 91,65\nMU 91,91,22\nAW 65,91\nAR 91,66\nMU 91,91,22\nAW 66,91\nAR 91,67\nMU 91,91,22\nAW 67,91\n"
"AR 91,68\nMU 91,91,23\nAW 68,91\nAR 91,69\nMU 91,91,23\nAW 69,91\nAR 91,70\nMU 91,91,23\nAW 70,91\nAR 91,71\nMU 91,91,23\nAW 71,91\nAR 91,72\nMU 91,91,22\nAW 72,91\nAR 91,73\n"
"MU 91,91,22\nAW 73,91\nAR 91,74\nMU 91,91,22\nAW 74,91\nAR 91,75\nMU 91,91,22\nAW 75,91\nAR 91,76\nMU 91,91,23\nAW 76,91\nAR 91,77\nMU 91,91,23\nAW 77,91\nAR 91,78\nMU 91,91,23\n"
"AW 78,91\nAR 91,79\nMU 91,91,23\nAW 79,91\nAR 91,80\nMU 91,91,22\nAW 80,91\nAR 91,81\nMU 91,91,22\nAW 81,91\nAR 91,82\nMU 91,91,22\nAW 82,91\nAR 91,83\nMU 91,91,22\nAW 83,91\n"
"AR 91,84\nMU 91,91,23\nAW 84,91\nAR 91,85\nMU 91,91,23\nAW 85,91\nAR 91,86\nMU 91,91,23\nAW 86,91\nAR 91,87\nMU 91,91,23\nAW 87,91\nAR 91,88\nMU 91,91,22\nAW 88,91\nAR 91,89\n"
"MU 91,91,22\nAW 89,91\nAR 91,90\nMU 91,91,22\nAW 90,91\nAR 91,91\nMU 91,91,22\nAW 91,91\nAR 91,92\nMU 91,91,23\nAW 92,91\nAR 91,93\nMU 91,91,23\nAW 93,91\nAR 91,94\nMU 91,91,23\n"
"AW 94,91\nAR 91,95\nMU 91,91,23\nAW 95,91\nAR 91,96\nMU 91,91,22\nAW 96,91\nAR 91,97\nMU 91,91,22\nAW 97,91\nAR 91,98\nMU 91,91,22\nAW 98,91\nAR 91,99\nMU 91,91,22\nAW 99,91\n"
"AR 91,100\nMU 91,91,23\nAW 100,91\nAR 91,101\nMU 91,91,23\nAW 101,91\nAR 91,102\nMU 91,91,23\nAW 102,91\nAR 91,103\nMU 91,91,23\nAW 103,91\nAR 91,104\nMU 91,91,22\nAW 104,91\n"
"AR 91,105\nMU 91,91,22\nAW 105,91\nAR 91,106\nMU 91,91,22\nAW 106,91\nAR 91,107\nMU 91,91,22\nAW 107,91\nAR 91,108\nMU 91,91,23\nAW 108,91\nAR 91,109\nMU 91,91,23\nAW 109,91\n"
"AR 91,110\nMU 91,91,23\nAW 110,91\nAR 91,111\nMU 91,91,23\nAW 111,91\nAR 91,112\nMU 91,91,22\nAW 112,91\nAR 91,113\nMU 91,91,22\nAW 113,91\nAR 91,114\nMU 91,91,22\nAW 114,91\n"
"AR 91,115\nMU 91,91,22\nAW 115,91\nAR 91,116\nMU 91,91,23\nAW 116,91\nAR 91,117\nMU 91,91,23\nAW 117,91\nAR 91,118\nMU 91,91,23\nAW 118,91\nAR 91,119\nMU 91,91,23\nAW 119,91\n"
"AR 91,120\nMU 91,91,22\nAW 120,91\nAR 91,121\nMU 91,91,22\nAW 121,91\nAR 91,122\nMU 91,91,22\nAW 122,91\nAR 91,123\nMU 91,91,22\nAW 123,91\nAR 91,124\nMU 91,91,23\nAW 124,91\n"
"AR 91,125\nMU 91,91,23\nAW 125,91\nAR 91,126\nMU 91,91,23\nAW 126,91\nAR 91,127\nMU 91,91,23\nAW 127,91\ns_nop 1\ns_setpc_b64 s[80:81]\n.R410:\nMU 128,128,22\nMU 129,129,22\n"
"MU 130,130,22\nMU 131,131,22\nMU 132,132,23\nMU 133,133,23\nMU 134,134,23\nMU 135,135,23\nMU 136,136,22\nMU 137,137,22\nMU 138,138,22\nMU 139,139,22\nMU 140,140,23\nMU 141,141,23\n"
"MU 142,142,23\nMU 143,143,23\nMU 144,144,22\nMU 145,145,22\nMU 146,146,22\nMU 147,147,22\nMU 148,148,23\nMU 149,149,23\nMU 150,150,23\nMU 151,151,23\nMU 152,152,22\nMU 153,153,22\n"
"MU 154,154,22\nMU 155,155,22\nMU 156,156,23\nMU 157,157,23\nMU 158,158,23\nMU 159,159,23\nMU 160,160,22\nMU 161,161,22\nMU 162,162,22\nMU 163,163,22\nMU 164,164,23\nMU 165,165,23\n"
"MU 166,166,23\nMU 167,167,23\nMU 168,168,22\nMU 169,169,22\nMU 170,170,22\nMU 171,171,22\nMU 172,172,23\nMU 173,173,23\nMU 174,174,23\nMU 175,175,23\nMU 176,176,22\nMU 177,177,22\n"
"MU 178,178,22\nMU 179,179,22\nMU 180,180,23\nMU 181,181,23\nMU 182,182,23\nMU 183,183,23\nMU 184,184,22\nMU 185,185,22\nMU 186,186,22\nMU 187,187,22\nMU 188,188,23\nMU 189,189,23\n"
"MU 190,190,23\nMU 191,191,23\nMU 192,192,22\nMU 193,193,22\nMU 194,194,22\nMU 195,195,22\nMU 196,196,23\nMU 197,197,23\nMU 198,198,23\nMU 199,199,23\nMU 200,200,22\nMU 201,201,22\n"
"MU 202,202,22\nMU 203,203,22\nMU 204,204,23\nMU 205,205,23\nMU 206,206,23\nMU 207,207,23\nMU 208,208,22\nMU 209,209,22\nMU 210,210,22\nMU 211,211,22\nMU 212,212,23\nMU 213,213,23\n"
"MU 214,214,23\nMU 215,215,23\nMU 216,216,22\nMU 217,217,22\nMU 218,218,22\nMU 219,219,22\nMU 220,220,23\nMU 221,221,23\nMU 222,222,23\nMU 223,223,23\nMU 224,224,22\nMU 225,225,22\n"
"MU 226,226,22\nMU 227,227,22\nMU 228,228,23\nMU 229,229,23\nMU 230,230,23\nMU 231,231,23\nMU 232,232,22\nMU 233,233,22\nMU 234,234,22\nMU 235,235,22\nMU 236,236,23\nMU 237,237,23\n"
"MU 238,238,23\nMU 239,239,23\nMU 240,240,22\nMU 241,241,22\nMU 242,242,22\nMU 243,243,22\nMU 244,244,23\nMU 245,245,23\nMU 246,246,23\nMU 247,247,23\nMU 248,248,22\nMU 249,249,22\n"
"MU 250,250,22\nMU 251,251,22\nMU 252,252,23\nMU 253,253,23\nMU 254,254,23\nMU 255,255,23\ns_nop 1\ns_setpc_b64 s[80:81]\n.p2align 12\n.PA5:\ns_mov_b64 exec, 0\n"
"s_cmp_eq_u32 s11, 1\ns_cbranch_scc1 .W586\ns_cmp_eq_u32 s11, 2\ns_cbranch_scc1 .W587\ns_cmp_eq_u32 s11, 3\ns_cbranch_scc1 .W588\ns_branch .W585\n.WB416:\ns_mov_b64 exec, -1\n"
"s_add_u32 m0, s47, -49152\ns_add_u32 s48, s48, 128\nD0 2,20,48,0\nD0 3,20,48,1024\nD0 4,20,48,2048\nD0 5,20,48,3072\ns_and_b32 s86, s74, 15\ns_lshl2_add_u32 s86, s86, s11\n"
"s_mov_b32 s88, 0\ns_lshl_b32 s93, s73, 8\ns_lshl2_add_u32 s93, s86, s93\ns_add_u32 s93, s93, 0xc04ac00\ns_lshl_b32 s86, s86, 14\ns_mul_i32 s91, s73, 25165824\n"
"s_add_u32 s86, s86, s91\ns_and_b32 s92, s13, 1\ns_lshl_b32 s92, s92, 13\ns_add_u32 s86, s86, s92\ns_add_u32 s94, s86, 0x1480000\ns_xor_b32 s95, s94, 0x2000\n"
"s_add_u32 s86, s94, 512\ns_load_dword s87, s[54:55], s86\ns_add_u32 m0, s47, -32768\ns_add_u32 s45, s45, 128\nD0 2,20,45,0\nD0 3,20,45,1024\nD0 4,20,45,2048\nD0 5,20,45,3072\n"
"s_add_u32 m0, s47, 32768\ns_add_u32 s44, s44, 0x40000\nD0 6,24,44,0\nD0 7,24,44,1024\nD0 8,24,44,2048\nD0 9,24,44,3072\ns_add_u32 m0, s47, 49152\ns_add_u32 s44, s44, 0x40000\n"
"D0 6,24,44,0\nD0 7,24,44,1024\nD0 8,24,44,2048\nD0 9,24,44,3072\ns_add_u32 m0, s47, -16384\ns_add_u32 s48, s48, 128\nD0 2,20,48,0\nD0 3,20,48,1024\nD0 4,20,48,2048\n"
"D0 5,20,48,3072\nv_mov_b32 v52, 0x40000000\nv_cmp_gt_u32 vcc, 56, v0\ns_lshl2_add_u32 s2, s10, s11\ns_mul_i32 s2, s2, 1792\nv_cndmask_b32 v53, v52, v1, vcc\ns_mov_b32 s36, s56\n"
"s_and_b32 s37, s57, 0xffff\ns_mov_b32 s38, 0x380\ns_mov_b32 s39, 0x20000\ns_mov_b32 s40, s58\ns_and_b32 s41, s59, 0xffff\ns_mov_b32 s42, 0x380\ns_mov_b32 s43, 0x20000\n"
"buffer_load_dwordx4 v[32:35], v53, s[28:31], s2 offen offset:0\nbuffer_load_dwordx4 v[36:39], v53, s[28:31], s2 offen offset:896\n"
"buffer_load_dwordx4 v[40:43], v53, s[36:39], 0 offen offset:0\nbuffer_load_dwordx4 v[44:47], v53, s[40:43], 0 offen offset:0\nWL 0\ns_mul_i32 s15, s15, 1664525\n"
"s_add_u32 s15, s15, 1013904223\nv_mov_b32 v25, s15\ns_mov_b32 s57, 0x3db8aa3b\ns_and_b32 s59, s11, 1\ns_lshl_b32 s59, s59, 13\ns_mul_i32 s2, s73, 25165824\ns_and_b32 s3, s12, 1\n"
"s_lshl_b32 s3, s3, 21\ns_add_u32 s2, s2, s3\ns_lshl2_add_u32 s3, s13, s11\ns_lshl_b32 s3, s3, 15\ns_add_u32 s2, s2, s3\ns_add_u32 s56, s2, 0x800000\ns_xor_b32 s53, s56, 0xc000\n"
"buffer_load_dword v31, v24, s[24:27], s96 offen\ns_add_u32 s96, s96, 0x80000\nbuffer_load_dword v31, v24, s[24:27], s96 offen\ns_add_u32 s96, s96, 0x80000\n"
"v_lshrrev_b32 v60, 3, v0\nv_and_b32 v61, 0x70, v1\nv_lshl_or_b32 v60, v60, 10, v61\nv_mov_b32 v20, 0\nv_mov_b32 v21, 0\nv_mov_b32 v22, 0\nv_mov_b32 v23, 0\nSv 20,60,16,94,0\n"
"Sv 20,60,16,95,0\nWV 28\ns_barrier\nRa 128,10,0\nRa 132,11,0\nRa 136,10,2048\nRa 140,11,2048\nRa 144,10,8192\nRa 148,11,8192\nRa 152,10,10240\nRa 156,11,10240\nTa 160,12,0\n"
"Ta 162,12,2048\nTa 164,12,16384\nTa 166,12,18432\nTa 168,13,0\nTa 170,13,2048\nTa 172,13,16384\nTa 174,13,18432\nTa 176,14,0\nTa 178,14,2048\nTa 180,14,16384\nTa 182,14,18432\n"
"Ta 184,15,0\nTa 186,15,2048\nTa 188,15,16384\nTa 190,15,18432\nWL 0\ns_barrier\nZvaa 64,128,160\nTa 224,16,0\nMvaa 64,132,164\nTa 226,16,2048\nZvaa 68,140,164\n"
"s_add_u32 m0, s47, -65536\ns_add_u32 s45, s45, 128\nMvaa 68,136,160\nTa 228,16,16384\nZvaa 128,144,160\nTa 230,16,18432\nMvaa 128,148,164\nD0 2,20,45,0\nZvaa 132,156,164\n"
"Ta 232,17,0\nMvaa 132,152,160\nTa 234,17,2048\nZvaa 140,152,168\ns_mov_b32 s36, s60\nMvaa 140,156,172\nTa 236,17,16384\nZvaa 136,148,172\nTa 238,17,18432\nMvaa 136,144,168\n"
"s_and_b32 s37, s61, 0xffff\nZvaa 76,136,168\nTa 240,18,0\nMvaa 76,140,172\nTa 242,18,2048\nZvaa 72,132,172\nD0 3,20,45,1024\nMvaa 72,128,168\nTa 244,18,16384\nZvaa 80,128,176\n"
"Ta 246,18,18432\nMvaa 80,132,180\ns_mov_b32 s38, 0x380000\nZvaa 84,140,180\nTa 248,19,0\nMvaa 84,136,176\nTa 250,19,2048\nZvaa 144,144,176\ns_mov_b32 s39, 0x20000\n"
"Mvaa 144,148,180\nTa 252,19,16384\nZvaa 148,156,180\nTa 254,19,18432\nMvaa 148,152,176\nD0 4,20,45,2048\nZvaa 156,152,184\ns_lshl3_add_u32 s75, s12, s13\nMvaa 156,156,188\n"
"s_lshl2_add_u32 s75, s75, s11\nZvaa 152,148,188\ns_mul_i32 s75, s75, 7168\nMvaa 152,144,184\ns_mul_i32 s76, s11, 7168\nZvaa 92,136,184\ns_add_u32 s76, s76, 131072\n"
"Mvaa 92,140,188\nD0 5,20,45,3072\nZvaa 88,132,188\ns_mov_b32 s42, 1605632\nMvaa 88,128,184\ns_waitcnt vmcnt(24) lgkmcnt(0)\ns_barrier\nZvaa 96,128,224\nRa 192,10,16384\n"
"Mvaa 96,132,228\ns_add_u32 m0, s47, 0\ns_add_u32 s44, s44, 0x40000\nZvaa 100,140,228\nRa 196,11,16384\nMvaa 100,136,224\nZvaa 160,144,224\nRa 200,10,18432\nMvaa 160,148,228\n"
"D0 6,24,44,0\nZvaa 164,156,228\nRa 204,11,18432\nMvaa 164,152,224\nZvaa 172,152,232\nRa 208,10,24576\nMvaa 172,156,236\ns_and_b32 s78, s74, 3\ns_lshl2_add_u32 s78, s73, s78\n"
"s_lshl_b32 s78, s78, 12\ns_bitcmp1_b32 s11, 0\ns_cselect_b32 s79, s48, s45\ns_add_u32 s79, s79, 512\ns_and_b32 s79, s79, 0xfffe0000\ns_add_u32 s78, s78, s79\n"
"buffer_load_dword v31, v26, s[20:23], s78 offen\nZvaa 168,148,236\nRa 212,11,24576\nMvaa 168,144,232\nZvaa 108,136,232\nRa 216,10,26624\nMvaa 108,140,236\nD0 7,24,44,1024\n"
"Zvaa 104,132,236\nRa 220,11,26624\nMvaa 104,128,232\nZvaa 112,128,240\nMvaa 112,132,244\nZvaa 116,140,244\nMvaa 116,136,240\nZvaa 176,144,240\nMvaa 176,148,244\nD0 8,24,44,2048\n"
"Zvaa 180,156,244\nMvaa 180,152,240\nZvaa 188,152,248\nMvaa 188,156,252\nZvaa 184,148,252\nMvaa 184,144,248\nZvaa 124,136,248\nMvaa 124,140,252\nD0 9,24,44,3072\nZvaa 120,132,252\n"
"Mvaa 120,128,248\nWL 0\nZaaa 32,192,224\nRa 128,10,32768\nMaaa 32,196,228\ns_add_u32 m0, s47, 16384\ns_add_u32 s44, s44, 0x40000\nZaaa 36,204,228\nRa 132,11,32768\n"
"Maaa 36,200,224\nZaaa 96,208,224\nRa 136,10,34816\nMaaa 96,212,228\nD0 6,24,44,0\nZaaa 100,220,228\nRa 140,11,34816\nMaaa 100,216,224\nZaaa 108,216,232\nRa 144,10,40960\n"
"Maaa 108,220,236\nZaaa 104,212,236\nRa 148,11,40960\nMaaa 104,208,232\nZaaa 44,200,232\nRa 152,10,43008\nMaaa 44,204,236\nD0 7,24,44,1024\nZaaa 40,196,236\nRa 156,11,43008\n"
"Maaa 40,192,232\nZaaa 48,192,240\nMaaa 48,196,244\nZaaa 52,204,244\nMaaa 52,200,240\nZaaa 112,208,240\nMaaa 112,212,244\nD0 8,24,44,2048\nZaaa 116,220,244\nMaaa 116,216,240\n"
"Zaaa 124,216,248\nMaaa 124,220,252\nZaaa 120,212,252\nMaaa 120,208,248\nZaaa 60,200,248\nMaaa 60,204,252\nD0 9,24,44,3072\nZaaa 56,196,252\nMaaa 56,192,248\n"
"s_waitcnt vmcnt(25) lgkmcnt(0)\ns_barrier\nZaaa 0,192,160\nTa 224,16,32768\nWV 17\nMaaa 0,196,164\nTa 226,16,34816\nZaaa 4,204,164\ns_add_u32 m0, s47, -49152\n"
"s_add_u32 s48, s48, 128\nMaaa 4,200,160\nTa 228,16,49152\nZaaa 64,208,160\nTa 230,16,51200\nbuffer_load_dword v31, v24, s[24:27], s96 offen\ns_add_u32 s96, s96, 0x80000\n"
"Maaa 64,212,164\nD0 2,20,48,0\nv_lshlrev_b32 v48, 16, v32\nZaaa 68,220,164\nTa 232,17,32768\nMaaa 68,216,160\nTa 234,17,34816\nv_lshlrev_b32 v49, 16, v36\nZaaa 76,216,168\n"
"v_lshlrev_b32 v50, 16, v40\nv_lshlrev_b32 v51, 16, v44\nMaaa 76,220,172\nTa 236,17,49152\nZaaa 72,212,172\nTa 238,17,51200\nMU 52,48,50\nMaaa 72,208,168\nMU 53,49,50\n"
"Zaaa 12,200,168\nTa 240,18,32768\nCV 52,52,53\nMaaa 12,204,172\nTa 242,18,34816\nv_lshlrev_b32 v54, 16, v52\nZaaa 8,196,172\nD0 3,20,48,1024\nMaaa 8,192,168\nTa 244,18,49152\n"
"v_and_b32 v55, 0xffff0000, v52\nZaaa 16,192,176\nTa 246,18,51200\nMU 56,48,51\nMaaa 16,196,180\nMU 57,49,51\nZaaa 20,204,180\nTa 248,19,32768\nv_and_b32 v48, 0xffff0000, v32\n"
"Maaa 20,200,176\nTa 250,19,34816\nZaaa 80,208,176\nv_and_b32 v49, 0xffff0000, v36\nv_and_b32 v50, 0xffff0000, v40\nMaaa 80,212,180\nTa 252,19,49152\n"
"v_and_b32 v51, 0xffff0000, v44\nZaaa 84,220,180\nTa 254,19,51200\nMaaa 84,216,176\nD0 4,20,48,2048\nMU 52,48,50\nZaaa 92,216,184\nMU 53,49,50\nCV 52,52,53\nMaaa 92,220,188\n"
"v_and_b32 v53, 0xffff0000, v52\nZaaa 88,212,188\nv_lshlrev_b32 v52, 16, v52\nFM 56,48,51\nMaaa 88,208,184\nFM 57,49,51\nZaaa 28,200,184\nAD 54,54,52\nAD 55,55,53\nMaaa 28,204,188\n"
"D0 5,20,48,3072\nv_lshlrev_b32 v48, 16, v33\nZaaa 24,196,188\nv_lshlrev_b32 v49, 16, v37\nMaaa 24,192,184\nWL 0\nMvaa 96,128,224\nTa 160,12,32768\nv_lshlrev_b32 v50, 16, v41\n"
"Mvaa 96,132,228\nTa 162,12,34816\nMvaa 100,140,228\ns_add_u32 m0, s47, -32768\ns_add_u32 s45, s45, 128\nMvaa 100,136,224\nTa 164,12,49152\nMvaa 160,144,224\nTa 166,12,51200\n"
"v_lshlrev_b32 v51, 16, v45\nMvaa 160,148,228\nD0 2,20,45,0\nMU 52,48,50\nMvaa 164,156,228\nTa 168,13,32768\nMvaa 164,152,224\nTa 170,13,34816\nMU 53,49,50\nMvaa 172,152,232\n"
"CV 52,52,53\nv_and_b32 v53, 0xffff0000, v52\nMvaa 172,156,236\nTa 172,13,49152\nMvaa 168,148,236\nTa 174,13,51200\nv_lshlrev_b32 v52, 16, v52\nMvaa 168,144,232\nFM 56,48,51\n"
"Mvaa 108,136,232\nTa 176,14,32768\nFM 57,49,51\nMvaa 108,140,236\nTa 178,14,34816\nAD 54,54,52\nMvaa 104,132,236\nD0 3,20,45,1024\nMvaa 104,128,232\nTa 180,14,49152\nAD 55,55,53\n"
"Mvaa 112,128,240\nTa 182,14,51200\nv_and_b32 v48, 0xffff0000, v33\nMvaa 112,132,244\nv_and_b32 v49, 0xffff0000, v37\nMvaa 116,140,244\nTa 184,15,32768\n"
"v_and_b32 v50, 0xffff0000, v41\nMvaa 116,136,240\nTa 186,15,34816\nMvaa 176,144,240\nv_and_b32 v51, 0xffff0000, v45\nMU 52,48,50\nMvaa 176,148,244\nTa 188,15,49152\nMU 53,49,50\n"
"Mvaa 180,156,244\nTa 190,15,51200\nMvaa 180,152,240\nD0 4,20,45,2048\nCV 52,52,53\nMvaa 188,152,248\nv_and_b32 v53, 0xffff0000, v52\nv_lshlrev_b32 v52, 16, v52\nMvaa 188,156,252\n"
"FM 56,48,51\nMvaa 184,148,252\nFM 57,49,51\nAD 54,54,52\nMvaa 184,144,248\nAD 55,55,53\nMvaa 124,136,248\nv_lshlrev_b32 v48, 16, v34\nv_lshlrev_b32 v49, 16, v38\nMvaa 124,140,252\n"
"D0 5,20,45,3072\nv_lshlrev_b32 v50, 16, v42\nMvaa 120,132,252\nv_lshlrev_b32 v51, 16, v46\nMvaa 120,128,248\ns_waitcnt vmcnt(18) lgkmcnt(0)\ns_barrier\nMvaa 64,128,160\n"
"Ra 192,10,49152\nMU 52,48,50\nMvaa 64,132,164\ns_add_u32 m0, s47, 32768\ns_add_u32 s44, s44, 0x40000\nMvaa 68,140,164\nRa 196,11,49152\nMU 53,49,50\nMvaa 68,136,160\nCV 52,52,53\n"
"Mvaa 128,144,160\nRa 200,10,51200\nv_and_b32 v53, 0xffff0000, v52\nMvaa 128,148,164\nD0 6,24,44,0\nv_lshlrev_b32 v52, 16, v52\nMvaa 132,156,164\nRa 204,11,51200\nMvaa 132,152,160\n"
"FM 56,48,51\nFM 57,49,51\nMvaa 140,152,168\nRa 208,10,57344\nAD 54,54,52\nMvaa 140,156,172\nAD 55,55,53\nMvaa 136,148,172\nRa 212,11,57344\nv_and_b32 v48, 0xffff0000, v34\n"
"Mvaa 136,144,168\nv_and_b32 v49, 0xffff0000, v38\nMvaa 76,136,168\nRa 216,10,59392\nv_and_b32 v50, 0xffff0000, v42\nMvaa 76,140,172\nD0 7,24,44,1024\n"
"v_and_b32 v51, 0xffff0000, v46\nMvaa 72,132,172\nRa 220,11,59392\nMvaa 72,128,168\nMU 52,48,50\nMU 53,49,50\nMvaa 80,128,176\nCV 52,52,53\nv_and_b32 v53, 0xffff0000, v52\n"
"Mvaa 80,132,180\nv_lshlrev_b32 v52, 16, v52\nMvaa 84,140,180\nFM 56,48,51\nFM 57,49,51\nMvaa 84,136,176\nAD 54,54,52\nMvaa 144,144,176\nAD 55,55,53\nv_lshlrev_b32 v48, 16, v35\n"
"Mvaa 144,148,180\nD0 8,24,44,2048\nv_lshlrev_b32 v49, 16, v39\nMvaa 148,156,180\nv_lshlrev_b32 v50, 16, v43\nMvaa 148,152,176\nv_lshlrev_b32 v51, 16, v47\nMU 52,48,50\n"
"Mvaa 156,152,184\nMU 53,49,50\nCV 52,52,53\nMvaa 156,156,188\nv_and_b32 v53, 0xffff0000, v52\nMvaa 152,148,188\nv_lshlrev_b32 v52, 16, v52\nFM 56,48,51\nMvaa 152,144,184\n"
"FM 57,49,51\nMvaa 92,136,184\nAD 54,54,52\nAD 55,55,53\nMvaa 92,140,188\nD0 9,24,44,3072\nv_and_b32 v48, 0xffff0000, v35\nMvaa 88,132,188\nv_and_b32 v49, 0xffff0000, v39\n"
"Mvaa 88,128,184\nWL 0\nMaaa 0,192,160\nRa 128,10,0\nv_and_b32 v50, 0xffff0000, v43\nMaaa 0,196,164\ns_add_u32 m0, s47, 49152\ns_add_u32 s44, s44, 0x40000\nMaaa 4,204,164\n"
"Ra 132,11,0\nv_and_b32 v51, 0xffff0000, v47\nMaaa 4,200,160\nMU 52,48,50\nMaaa 64,208,160\nRa 136,10,2048\nMU 53,49,50\nMaaa 64,212,164\nD0 6,24,44,0\nCV 52,52,53\n"
"Maaa 68,220,164\nRa 140,11,2048\nMaaa 68,216,160\nv_and_b32 v53, 0xffff0000, v52\nv_lshlrev_b32 v52, 16, v52\nMaaa 76,216,168\nRa 144,10,8192\nFM 56,48,51\nMaaa 76,220,172\n"
"FM 57,49,51\n.W585:\ns_cbranch_execz .W589\nMaaa 72,212,172\nRa 148,11,8192\nAD 54,54,52\nMaaa 72,208,168\nAD 55,55,53\nMaaa 12,200,168\nRa 152,10,10240\n"
"v_permlane32_swap_b32 v54, v55\nMaaa 12,204,172\nD0 7,24,44,1024\nv_permlane32_swap_b32 v56, v57\nMaaa 8,196,172\nRa 156,11,10240\nMaaa 8,192,168\nAD 54,54,55\nAD 56,56,57\n"
"Maaa 16,192,176\nMaaa 16,196,180\nv_permlane16_swap_b32 v54, v56\nMaaa 20,204,180\nAD 54,54,56\nMaaa 20,200,176\nMaaa 80,208,176\n"
"v_add_f32_dpp v54, v54, v54 row_ror:8 row_mask:0xf bank_mask:0xf\nMaaa 80,212,180\nD0 8,24,44,2048\nMaaa 84,220,180\n"
"v_add_f32_dpp v54, v54, v54 row_ror:4 row_mask:0xf bank_mask:0xf\nMaaa 84,216,176\nMaaa 92,216,184\nv_add_f32_dpp v54, v54, v54 row_ror:2 row_mask:0xf bank_mask:0xf\n"
"Maaa 92,220,188\nMaaa 88,212,188\nv_add_f32_dpp v54, v54, v54 row_ror:1 row_mask:0xf bank_mask:0xf\nCV 54,54,54\nMaaa 88,208,184\nv_lshlrev_b32 v54, 16, v54\nMaaa 28,200,184\n"
"v_mul_f32 v55, 0xbfb8aa3b, v54\nMaaa 28,204,188\nD0 9,24,44,3072\nMaaa 24,196,188\nEX 55,55\nMaaa 24,192,184\ns_waitcnt vmcnt(12) lgkmcnt(0)\ns_barrier\nMaaa 32,192,224\n"
"Ta 160,12,0\nv_add_f32 v56, 0, v54\nMaaa 32,196,228\nTa 162,12,2048\nMaaa 36,204,228\ns_add_u32 m0, s47, -16384\ns_add_u32 s48, s48, 128\nMaaa 36,200,224\nTa 164,12,16384\n"
"Maaa 96,208,224\nTa 166,12,18432\nbuffer_load_dword v31, v24, s[24:27], s96 offen\ns_add_u32 s96, s96, 0x80000\nMaaa 96,212,228\nD0 2,20,48,0\nv_add_f32 v55, 1.0, v55\n"
"Maaa 100,220,228\nTa 168,13,0\nv_rcp_f32 v55, v55\nMaaa 100,216,224\nTa 170,13,2048\nv_ashrrev_i32 v57, 31, v56\nMaaa 108,216,232\nCV 55,55,55\nv_lshlrev_b32 v55, 16, v55\n"
"Maaa 108,220,236\nTa 172,13,16384\nMaaa 104,212,236\nTa 174,13,18432\nv_or_b32 v57, 0x80000000, v57\nMaaa 104,208,232\nv_xor_b32 v56, v56, v57\nMaaa 44,200,232\nTa 176,14,0\n"
"v_and_b32 v56, 0xffff0000, v56\nMaaa 44,204,236\nTa 178,14,2048\ns_lshl2_add_u32 s2, s10, s11\nMaaa 40,196,236\nD0 3,20,48,1024\nMaaa 40,192,232\nTa 180,14,16384\n"
"v_lshrrev_b32 v48, 5, v0\nMaaa 48,192,240\nTa 182,14,18432\nv_lshl_add_u32 v48, s2, 1, v48\nMaaa 48,196,244\nv_sub_u32 v49, 0xffff, v48\nMaaa 52,204,244\nTa 184,15,0\n"
"v_or_b32 v56, v56, v49\nMaaa 52,200,240\nTa 186,15,2048\nMaaa 112,208,240\nv_mov_b32 v57, s15\nv_and_b32 v49, 31, v0\nMaaa 112,212,244\nTa 188,15,16384\nv_mov_b32 v52, 0x40000000\n"
"Maaa 116,220,244\nTa 190,15,18432\nv_cmp_eq_u32 vcc, 16, v49\nv_lshlrev_b32 v50, 2, v48\nv_lshlrev_b32 v51, 10, v49\nv_cndmask_b32 v50, v52, v50, vcc\n"
"v_cndmask_b32 v56, v55, v56, vcc\nMaaa 116,216,240\nD0 4,20,48,2048\nv_lshl_add_u32 v51, v48, 3, v51\nMaaa 124,216,248\nv_and_b32 v49, 15, v0\nv_cmp_eq_u32 vcc, 0, v49\n"
"s_mov_b32 s2, 0xc040000\ns_mov_b32 s3, 0xc03e000\nv_cndmask_b32 v51, v52, v51, vcc\nbuffer_store_dwordx2 v[56:57], v51, s[16:19], s2 offen sc0 sc1\n"
"buffer_store_dword v54, v50, s[16:19], s3 offen sc0 sc1\nMaaa 124,220,252\nMaaa 120,212,252\nMaaa 120,208,248\nMaaa 60,200,248\nMaaa 60,204,252\nD0 5,20,48,3072\nMaaa 56,196,252\n"
"Maaa 56,192,248\nWL 0\ns_mov_b32 s50, 3\n.GL415:\nMvaa 64,128,160\nTa 224,16,0\nMvaa 64,132,164\nTa 226,16,2048\nMvaa 68,140,164\nTa 228,16,16384\ns_add_u32 m0, s47, -65536\n"
"s_add_u32 s45, s45, 128\nMvaa 68,136,160\nTa 230,16,18432\nMvaa 128,144,160\nTa 232,17,0\nMvaa 128,148,164\nTa 234,17,2048\nD0 2,20,45,0\nMvaa 132,156,164\nTa 236,17,16384\n"
"Mvaa 132,152,160\nTa 238,17,18432\nMvaa 140,152,168\nTa 240,18,0\nMvaa 140,156,172\nTa 242,18,2048\nMvaa 136,148,172\nTa 244,18,16384\nMvaa 136,144,168\nTa 246,18,18432\n"
"Mvaa 76,136,168\nTa 248,19,0\nMvaa 76,140,172\nTa 250,19,2048\nMvaa 72,132,172\nTa 252,19,16384\nD0 3,20,45,1024\nMvaa 72,128,168\nTa 254,19,18432\nMvaa 80,128,176\n"
"Ra 192,10,16384\nMvaa 80,132,180\nRa 196,11,16384\nMvaa 84,140,180\nRa 200,10,18432\nMvaa 84,136,176\nRa 204,11,18432\nMvaa 144,144,176\nRa 208,10,24576\nMvaa 144,148,180\n"
"Ra 212,11,24576\nMvaa 148,156,180\nRa 216,10,26624\nMvaa 148,152,176\nRa 220,11,26624\nD0 4,20,45,2048\nMvaa 156,152,184\nMvaa 156,156,188\nMvaa 152,148,188\nMvaa 152,144,184\n"
"Mvaa 92,136,184\nMvaa 92,140,188\nD0 5,20,45,3072\nMvaa 88,132,188\nMvaa 88,128,184\ns_waitcnt vmcnt(18) lgkmcnt(8)\ns_barrier\nMvaa 96,128,224\nMvaa 96,132,228\n"
"s_add_u32 m0, s47, 0\ns_add_u32 s44, s44, 0x40000\ns_mov_b32 s86, 1\ns_load_dword s86, s[54:55], s94\nMvaa 100,140,228\ns_add_u32 s94, s94, 1024\nMvaa 100,136,224\n"
"Mvaa 160,144,224\nMvaa 160,148,228\nD0 6,24,44,0\nMvaa 164,156,228\nMvaa 164,152,224\nMvaa 172,152,232\nMvaa 172,156,236\nMvaa 168,148,236\nMvaa 168,144,232\n"
"s_add_u32 s88, s88, s86\nMvaa 108,136,232\nMvaa 108,140,236\nD0 7,24,44,1024\ns_add_u32 s88, s88, s86\nMvaa 104,132,236\nMvaa 104,128,232\ns_add_u32 s88, s88, s86\n"
"Mvaa 112,128,240\nMvaa 112,132,244\ns_add_u32 s88, s88, s86\nMvaa 116,140,244\nMvaa 116,136,240\ns_add_u32 s88, s88, s86\nMvaa 176,144,240\nMvaa 176,148,244\nD0 8,24,44,2048\n"
"s_add_u32 s88, s88, s86\nMvaa 180,156,244\nMvaa 180,152,240\ns_add_u32 s88, s88, s86\nMvaa 188,152,248\nMvaa 188,156,252\ns_add_u32 s88, s88, s86\nMvaa 184,148,252\n"
"Mvaa 184,144,248\ns_add_u32 s88, s88, s86\nMvaa 124,136,248\nMvaa 124,140,252\nD0 9,24,44,3072\ns_add_u32 s88, s88, s86\nMvaa 120,132,252\nMvaa 120,128,248\nWL 0\n"
"s_add_u32 s88, s88, s86\nMaaa 32,192,224\nRa 128,10,32768\nMaaa 32,196,228\ns_add_u32 m0, s47, 16384\ns_add_u32 s44, s44, 0x40000\ns_add_u32 s88, s88, s86\nMaaa 36,204,228\n"
"Ra 132,11,32768\nMaaa 36,200,224\ns_add_u32 s88, s88, s86\nMaaa 96,208,224\nRa 136,10,34816\nMaaa 96,212,228\nD0 6,24,44,0\ns_add_u32 s88, s88, s86\nMaaa 100,220,228\n"
"Ra 140,11,34816\nMaaa 100,216,224\ns_add_u32 s88, s88, s86\nMaaa 108,216,232\nRa 144,10,40960\nMaaa 108,220,236\ns_add_u32 s88, s88, s86\nMaaa 104,212,236\nRa 148,11,40960\n"
"Maaa 104,208,232\nMaaa 44,200,232\nRa 152,10,43008\nMaaa 44,204,236\nD0 7,24,44,1024\nMaaa 40,196,236\nRa 156,11,43008\nMaaa 40,192,232\nMaaa 48,192,240\nMaaa 48,196,244\n"
"Maaa 52,204,244\nMaaa 52,200,240\nMaaa 112,208,240\nMaaa 112,212,244\nD0 8,24,44,2048\nMaaa 116,220,244\nMaaa 116,216,240\nMaaa 124,216,248\nMaaa 124,220,252\nMaaa 120,212,252\n"
"Maaa 120,208,248\nMaaa 60,200,248\nMaaa 60,204,252\nD0 9,24,44,3072\nMaaa 56,196,252\nMaaa 56,192,248\ns_waitcnt vmcnt(12) lgkmcnt(0)\ns_barrier\nMaaa 0,192,160\nTa 224,16,32768\n"
"s_mov_b32 m0, s76\ns_add_u32 s76, s76, 1024\nMaaa 0,196,164\nTa 226,16,34816\nD0 1,36,75,0\ns_add_u32 s75, s75, 1024\nMaaa 4,204,164\ns_add_u32 m0, s47, -49152\n"
"s_add_u32 s48, s48, 128\nMaaa 4,200,160\nTa 228,16,49152\nMaaa 64,208,160\nTa 230,16,51200\nbuffer_load_dword v31, v24, s[24:27], s96 offen\ns_add_u32 s96, s96, 0x80000\n"
"Maaa 64,212,164\nD0 2,20,48,0\nMaaa 68,220,164\nTa 232,17,32768\nMaaa 68,216,160\nTa 234,17,34816\nMaaa 76,216,168\nMaaa 76,220,172\nTa 236,17,49152\nMaaa 72,212,172\n"
"Ta 238,17,51200\nMaaa 72,208,168\nMaaa 12,200,168\nTa 240,18,32768\nMaaa 12,204,172\nTa 242,18,34816\nMaaa 8,196,172\nD0 3,20,48,1024\nMaaa 8,192,168\nTa 244,18,49152\n"
"Maaa 16,192,176\nTa 246,18,51200\nMaaa 16,196,180\nMaaa 20,204,180\nTa 248,19,32768\nMaaa 20,200,176\nTa 250,19,34816\nMaaa 80,208,176\nMaaa 80,212,180\nTa 252,19,49152\n"
"Maaa 84,220,180\nTa 254,19,51200\nMaaa 84,216,176\nD0 4,20,48,2048\nMaaa 92,216,184\nMaaa 92,220,188\nMaaa 88,212,188\nMaaa 88,208,184\nMaaa 28,200,184\nMaaa 28,204,188\n"
"D0 5,20,48,3072\nMaaa 24,196,188\nMaaa 24,192,184\nWL 0\nMvaa 96,128,224\nTa 160,12,32768\nMvaa 96,132,228\nTa 162,12,34816\nMvaa 100,140,228\nTa 164,12,49152\n"
"s_add_u32 m0, s47, -32768\ns_add_u32 s45, s45, 128\nMvaa 100,136,224\nTa 166,12,51200\nMvaa 160,144,224\nTa 168,13,32768\nMvaa 160,148,228\nTa 170,13,34816\nD0 2,20,45,0\n"
"Mvaa 164,156,228\nTa 172,13,49152\nMvaa 164,152,224\nTa 174,13,51200\nMvaa 172,152,232\nTa 176,14,32768\nMvaa 172,156,236\nTa 178,14,34816\nMvaa 168,148,236\nTa 180,14,49152\n"
"Mvaa 168,144,232\nTa 182,14,51200\nMvaa 108,136,232\nTa 184,15,32768\nMvaa 108,140,236\nTa 186,15,34816\nMvaa 104,132,236\nTa 188,15,49152\nD0 3,20,45,1024\nMvaa 104,128,232\n"
"Ta 190,15,51200\nMvaa 112,128,240\nRa 192,10,49152\nMvaa 112,132,244\nRa 196,11,49152\nMvaa 116,140,244\nRa 200,10,51200\nMvaa 116,136,240\nRa 204,11,51200\nMvaa 176,144,240\n"
"Ra 208,10,57344\nMvaa 176,148,244\nRa 212,11,57344\nMvaa 180,156,244\nRa 216,10,59392\nMvaa 180,152,240\nRa 220,11,59392\nD0 4,20,45,2048\nMvaa 188,152,248\nMvaa 188,156,252\n"
"Mvaa 184,148,252\nMvaa 184,144,248\nMvaa 124,136,248\nMvaa 124,140,252\nD0 5,20,45,3072\nMvaa 120,132,252\nMvaa 120,128,248\ns_waitcnt vmcnt(18) lgkmcnt(8)\ns_barrier\n"
"Mvaa 64,128,160\nMvaa 64,132,164\ns_add_u32 m0, s47, 32768\ns_add_u32 s44, s44, 0x40000\ns_mov_b32 s86, 1\ns_load_dword s86, s[54:55], s95\nMvaa 68,140,164\n"
"s_add_u32 s95, s95, 1024\nMvaa 68,136,160\nMvaa 128,144,160\nMvaa 128,148,164\nD0 6,24,44,0\nMvaa 132,156,164\nMvaa 132,152,160\nMvaa 140,152,168\nMvaa 140,156,172\n"
"Mvaa 136,148,172\nMvaa 136,144,168\ns_sub_u32 s88, s88, s86\nMvaa 76,136,168\nMvaa 76,140,172\nD0 7,24,44,1024\ns_sub_u32 s88, s88, s86\nMvaa 72,132,172\nMvaa 72,128,168\n"
"s_sub_u32 s88, s88, s86\nMvaa 80,128,176\nMvaa 80,132,180\ns_sub_u32 s88, s88, s86\nMvaa 84,140,180\nMvaa 84,136,176\ns_sub_u32 s88, s88, s86\nMvaa 144,144,176\nMvaa 144,148,180\n"
"D0 8,24,44,2048\ns_sub_u32 s88, s88, s86\nMvaa 148,156,180\nMvaa 148,152,176\ns_sub_u32 s88, s88, s86\nMvaa 156,152,184\nMvaa 156,156,188\ns_sub_u32 s88, s88, s86\n"
"Mvaa 152,148,188\nMvaa 152,144,184\ns_sub_u32 s88, s88, s86\nMvaa 92,136,184\nMvaa 92,140,188\nD0 9,24,44,3072\ns_sub_u32 s88, s88, s86\nMvaa 88,132,188\nMvaa 88,128,184\nWL 0\n"
"s_sub_u32 s88, s88, s86\nMaaa 0,192,160\nRa 128,10,0\nMaaa 0,196,164\ns_add_u32 m0, s47, 49152\ns_add_u32 s44, s44, 0x40000\ns_sub_u32 s88, s88, s86\nMaaa 4,204,164\nRa 132,11,0\n"
"Maaa 4,200,160\ns_sub_u32 s88, s88, s86\nMaaa 64,208,160\nRa 136,10,2048\nMaaa 64,212,164\nD0 6,24,44,0\ns_sub_u32 s88, s88, s86\nMaaa 68,220,164\nRa 140,11,2048\nMaaa 68,216,160\n"
"s_sub_u32 s88, s88, s86\nMaaa 76,216,168\nRa 144,10,8192\nMaaa 76,220,172\ns_sub_u32 s88, s88, s86\nMaaa 72,212,172\nRa 148,11,8192\nMaaa 72,208,168\nMaaa 12,200,168\n"
"Ra 152,10,10240\nMaaa 12,204,172\nD0 7,24,44,1024\nMaaa 8,196,172\nRa 156,11,10240\nMaaa 8,192,168\nMaaa 16,192,176\nMaaa 16,196,180\nMaaa 20,204,180\nMaaa 20,200,176\n"
"Maaa 80,208,176\nMaaa 80,212,180\nD0 8,24,44,2048\nMaaa 84,220,180\nMaaa 84,216,176\nMaaa 92,216,184\nMaaa 92,220,188\nMaaa 88,212,188\nMaaa 88,208,184\nMaaa 28,200,184\n"
"Maaa 28,204,188\nD0 9,24,44,3072\nMaaa 24,196,188\nMaaa 24,192,184\ns_waitcnt vmcnt(12) lgkmcnt(0)\ns_barrier\nMaaa 32,192,224\nTa 160,12,0\ns_mov_b32 m0, s76\n"
"s_add_u32 s76, s76, 1024\nMaaa 32,196,228\nTa 162,12,2048\nD0 1,36,75,0\ns_add_u32 s75, s75, 1024\nMaaa 36,204,228\ns_add_u32 m0, s47, -16384\ns_add_u32 s48, s48, 128\n"
"Maaa 36,200,224\nTa 164,12,16384\nMaaa 96,208,224\nTa 166,12,18432\nbuffer_load_dword v31, v24, s[24:27], s96 offen\ns_add_u32 s96, s96, 0x80000\nMaaa 96,212,228\n.W586:\n"
"s_cbranch_execz .W590\nD0 2,20,48,0\nMaaa 100,220,228\nTa 168,13,0\nMaaa 100,216,224\nTa 170,13,2048\nMaaa 108,216,232\nMaaa 108,220,236\nTa 172,13,16384\nMaaa 104,212,236\n"
"Ta 174,13,18432\nMaaa 104,208,232\nMaaa 44,200,232\nTa 176,14,0\nMaaa 44,204,236\nTa 178,14,2048\nMaaa 40,196,236\nD0 3,20,48,1024\nMaaa 40,192,232\nTa 180,14,16384\n"
"Maaa 48,192,240\nTa 182,14,18432\nMaaa 48,196,244\nMaaa 52,204,244\nTa 184,15,0\nMaaa 52,200,240\nTa 186,15,2048\nMaaa 112,208,240\nMaaa 112,212,244\nTa 188,15,16384\n"
"Maaa 116,220,244\nTa 190,15,18432\nMaaa 116,216,240\nD0 4,20,48,2048\nMaaa 124,216,248\nMaaa 124,220,252\nMaaa 120,212,252\nMaaa 120,208,248\nMaaa 60,200,248\n"
"s_xor_b32 s94, s94, 0x2000\ns_xor_b32 s95, s95, 0x2000\ns_sub_u32 s88, 0, s88\nMaaa 60,204,252\nD0 5,20,48,3072\ns_sub_u32 s50, s50, 1\nMaaa 56,196,252\nMaaa 56,192,248\nWL 0\n"
"s_cbranch_scc0 .GL415\nMvaa 64,128,160\nTa 224,16,0\nMvaa 64,132,164\nTa 226,16,2048\ns_cmp_gt_i32 s88, 0\ns_cselect_b32 s88, 0x2000, 0\nMvaa 68,140,164\n"
"s_add_u32 m0, s47, -65536\ns_add_u32 s45, s45, 128\nMvaa 68,136,160\nTa 228,16,16384\nMvaa 128,144,160\nTa 230,16,18432\nMvaa 128,148,164\nD0 2,20,45,0\nMvaa 132,156,164\n"
"Ta 232,17,0\nMvaa 132,152,160\nTa 234,17,2048\nMvaa 140,152,168\ns_xor_b32 s88, s88, s92\nMvaa 140,156,172\nTa 236,17,16384\nMvaa 136,148,172\nTa 238,17,18432\nMvaa 136,144,168\n"
"v_xor_b32 v27, s88, v25\nMvaa 76,136,168\nTa 240,18,0\nMvaa 76,140,172\nTa 242,18,2048\nMvaa 72,132,172\nD0 3,20,45,1024\nMvaa 72,128,168\nTa 244,18,16384\nMvaa 80,128,176\n"
"Ta 246,18,18432\nMvaa 80,132,180\nbuffer_store_dword v27, off, s[16:19], s93 sc0 sc1\nMvaa 84,140,180\nTa 248,19,0\nMvaa 84,136,176\nTa 250,19,2048\nMvaa 144,144,176\n"
"Mvaa 144,148,180\nTa 252,19,16384\nMvaa 148,156,180\nTa 254,19,18432\nMvaa 148,152,176\nD0 4,20,45,2048\nMvaa 156,152,184\nMvaa 156,156,188\nMvaa 152,148,188\nMvaa 152,144,184\n"
"Mvaa 92,136,184\nMvaa 92,140,188\nD0 5,20,45,3072\nMvaa 88,132,188\nMvaa 88,128,184\ns_waitcnt vmcnt(19) lgkmcnt(0)\ns_barrier\nMvaa 96,128,224\nRa 192,10,16384\nMvaa 96,132,228\n"
"s_add_u32 m0, s47, 0\ns_add_u32 s44, s44, 0x40000\nMvaa 100,140,228\nRa 196,11,16384\nMvaa 100,136,224\nMvaa 160,144,224\nRa 200,10,18432\nMvaa 160,148,228\nD0 6,24,44,0\n"
"Mvaa 164,156,228\nRa 204,11,18432\nMvaa 164,152,224\nMvaa 172,152,232\nRa 208,10,24576\nMvaa 172,156,236\nMvaa 168,148,236\nRa 212,11,24576\nMvaa 168,144,232\nMvaa 108,136,232\n"
"Ra 216,10,26624\nMvaa 108,140,236\nD0 7,24,44,1024\nMvaa 104,132,236\nRa 220,11,26624\nMvaa 104,128,232\nMvaa 112,128,240\nMvaa 112,132,244\nMvaa 116,140,244\nMvaa 116,136,240\n"
"Mvaa 176,144,240\nMvaa 176,148,244\nD0 8,24,44,2048\nMvaa 180,156,244\nMvaa 180,152,240\nMvaa 188,152,248\nMvaa 188,156,252\nMvaa 184,148,252\nMvaa 184,144,248\nMvaa 124,136,248\n"
"Mvaa 124,140,252\nD0 9,24,44,3072\nMvaa 120,132,252\nMvaa 120,128,248\nWL 0\nMaaa 32,192,224\nRa 128,10,32768\nMaaa 32,196,228\ns_add_u32 m0, s47, 16384\n"
"s_add_u32 s44, s44, 0x40000\nMaaa 36,204,228\nRa 132,11,32768\nMaaa 36,200,224\nMaaa 96,208,224\nRa 136,10,34816\nMaaa 96,212,228\nD0 6,24,44,0\nMaaa 100,220,228\nRa 140,11,34816\n"
"Maaa 100,216,224\nMaaa 108,216,232\nRa 144,10,40960\nMaaa 108,220,236\nMaaa 104,212,236\nRa 148,11,40960\nMaaa 104,208,232\nMaaa 44,200,232\nRa 152,10,43008\nMaaa 44,204,236\n"
"D0 7,24,44,1024\nMaaa 40,196,236\nRa 156,11,43008\nMaaa 40,192,232\nMaaa 48,192,240\nMaaa 48,196,244\nMaaa 52,204,244\nMaaa 52,200,240\nMaaa 112,208,240\nMaaa 112,212,244\n"
"D0 8,24,44,2048\nMaaa 116,220,244\nMaaa 116,216,240\nMaaa 124,216,248\nMaaa 124,220,252\nMaaa 120,212,252\nMaaa 120,208,248\nMaaa 60,200,248\nMaaa 60,204,252\nD0 9,24,44,3072\n"
"Maaa 56,196,252\nMaaa 56,192,248\ns_waitcnt vmcnt(19) lgkmcnt(0)\ns_barrier\nMaaa 0,192,160\nTa 224,16,32768\nMaaa 0,196,164\nTa 226,16,34816\nMaaa 4,204,164\n"
"s_add_u32 m0, s47, -49152\ns_add_u32 s48, s48, 128\nMaaa 4,200,160\nTa 228,16,49152\nMaaa 64,208,160\nTa 230,16,51200\nbuffer_load_dword v31, v24, s[24:27], s96 offen\n"
"s_add_u32 s96, s96, 0x80000\nMaaa 64,212,164\nD0 2,20,48,0\nMaaa 68,220,164\nTa 232,17,32768\nMaaa 68,216,160\nTa 234,17,34816\nMaaa 76,216,168\nMaaa 76,220,172\nTa 236,17,49152\n"
"Maaa 72,212,172\nTa 238,17,51200\nMaaa 72,208,168\nMaaa 12,200,168\nTa 240,18,32768\nMaaa 12,204,172\nTa 242,18,34816\nMaaa 8,196,172\nD0 3,20,48,1024\nMaaa 8,192,168\n"
"Ta 244,18,49152\nMaaa 16,192,176\nTa 246,18,51200\nMaaa 16,196,180\nMaaa 20,204,180\nTa 248,19,32768\nMaaa 20,200,176\nTa 250,19,34816\nMaaa 80,208,176\nMaaa 80,212,180\n"
"Ta 252,19,49152\nMaaa 84,220,180\nTa 254,19,51200\nMaaa 84,216,176\nD0 4,20,48,2048\nMaaa 92,216,184\nMaaa 92,220,188\nMaaa 88,212,188\nMaaa 88,208,184\nMaaa 28,200,184\n"
"Maaa 28,204,188\nD0 5,20,48,3072\nMaaa 24,196,188\nMaaa 24,192,184\nWL 0\nMvaa 96,128,224\nTa 160,12,32768\nMvaa 96,132,228\nTa 162,12,34816\nMvaa 100,140,228\n"
"s_add_u32 m0, s47, -32768\ns_add_u32 s45, s45, 128\nMvaa 100,136,224\nTa 164,12,49152\nMvaa 160,144,224\nTa 166,12,51200\nMvaa 160,148,228\nD0 2,20,45,0\nMvaa 164,156,228\n"
"Ta 168,13,32768\nMvaa 164,152,224\nTa 170,13,34816\nMvaa 172,152,232\nMvaa 172,156,236\nTa 172,13,49152\nMvaa 168,148,236\nTa 174,13,51200\nMvaa 168,144,232\nMvaa 108,136,232\n"
"Ta 176,14,32768\nMvaa 108,140,236\nTa 178,14,34816\nMvaa 104,132,236\nD0 3,20,45,1024\nMvaa 104,128,232\nTa 180,14,49152\nMvaa 112,128,240\nTa 182,14,51200\nMvaa 112,132,244\n"
"Mvaa 116,140,244\nTa 184,15,32768\nMvaa 116,136,240\nTa 186,15,34816\nMvaa 176,144,240\nMvaa 176,148,244\nTa 188,15,49152\nMvaa 180,156,244\nTa 190,15,51200\nMvaa 180,152,240\n"
"D0 4,20,45,2048\nMvaa 188,152,248\nMvaa 188,156,252\nMvaa 184,148,252\nMvaa 184,144,248\nMvaa 124,136,248\nMvaa 124,140,252\nD0 5,20,45,3072\nMvaa 120,132,252\nMvaa 120,128,248\n"
"s_waitcnt vmcnt(17) lgkmcnt(0)\ns_barrier\nMvaa 64,128,160\nRa 192,10,49152\nMvaa 64,132,164\ns_add_u32 m0, s47, 32768\ns_add_u32 s44, s44, 0x40000\nMvaa 68,140,164\n"
"Ra 196,11,49152\nMvaa 68,136,160\nMvaa 128,144,160\nRa 200,10,51200\nMvaa 128,148,164\nD0 6,24,44,0\nMvaa 132,156,164\nRa 204,11,51200\nMvaa 132,152,160\nMvaa 140,152,168\n"
"Ra 208,10,57344\nMvaa 140,156,172\ns_lshl_b32 s5, s73, 8\ns_add_u32 s5, s5, 0xc04ac00\nv_lshrrev_b32 v27, 2, v1\nbuffer_load_dword v27, v27, s[16:19], s5 offen sc0 sc1\n"
"Mvaa 136,148,172\nRa 212,11,57344\nMvaa 136,144,168\nMvaa 76,136,168\nRa 216,10,59392\nMvaa 76,140,172\nD0 7,24,44,1024\nMvaa 72,132,172\nRa 220,11,59392\nMvaa 72,128,168\n"
"Mvaa 80,128,176\nMvaa 80,132,180\nMvaa 84,140,180\nMvaa 84,136,176\nMvaa 144,144,176\nMvaa 144,148,180\nD0 8,24,44,2048\nMvaa 148,156,180\nMvaa 148,152,176\nMvaa 156,152,184\n"
"Mvaa 156,156,188\nMvaa 152,148,188\nMvaa 152,144,184\nMvaa 92,136,184\nMvaa 92,140,188\nD0 9,24,44,3072\nMvaa 88,132,188\nMvaa 88,128,184\nWL 0\nMaaa 0,192,160\nRa 128,10,0\n"
"Maaa 0,196,164\ns_add_u32 m0, s47, 49152\ns_add_u32 s44, s44, 0x40000\nMaaa 4,204,164\nRa 132,11,0\nMaaa 4,200,160\nMaaa 64,208,160\nRa 136,10,2048\nMaaa 64,212,164\nD0 6,24,44,0\n"
"Maaa 68,220,164\nRa 140,11,2048\nMaaa 68,216,160\nMaaa 76,216,168\nRa 144,10,8192\nMaaa 76,220,172\nMaaa 72,212,172\nRa 148,11,8192\nMaaa 72,208,168\nMaaa 12,200,168\n"
"Ra 152,10,10240\nMaaa 12,204,172\nD0 7,24,44,1024\nMaaa 8,196,172\nRa 156,11,10240\nMaaa 8,192,168\nMaaa 16,192,176\nMaaa 16,196,180\nMaaa 20,204,180\nMaaa 20,200,176\n"
"Maaa 80,208,176\nMaaa 80,212,180\nD0 8,24,44,2048\nMaaa 84,220,180\nMaaa 84,216,176\nMaaa 92,216,184\nMaaa 92,220,188\nMaaa 88,212,188\nMaaa 88,208,184\nMaaa 28,200,184\n"
"Maaa 28,204,188\nD0 9,24,44,3072\nMaaa 24,196,188\nMaaa 24,192,184\ns_waitcnt vmcnt(18) lgkmcnt(0)\ns_barrier\nMaaa 32,192,224\nTa 160,12,0\nMaaa 32,196,228\nTa 162,12,2048\n"
"Maaa 36,204,228\ns_add_u32 m0, s47, -16384\ns_add_u32 s48, s48, 128\nMaaa 36,200,224\nTa 164,12,16384\nMaaa 96,208,224\nTa 166,12,18432\n"
"buffer_load_dword v31, v24, s[24:27], s96 offen\ns_add_u32 s96, s96, 0x80000\nMaaa 96,212,228\nD0 2,20,48,0\nMaaa 100,220,228\nTa 168,13,0\nMaaa 100,216,224\nTa 170,13,2048\n"
"Maaa 108,216,232\nMaaa 108,220,236\nTa 172,13,16384\nMaaa 104,212,236\nTa 174,13,18432\nMaaa 104,208,232\nMaaa 44,200,232\nTa 176,14,0\nMaaa 44,204,236\nTa 178,14,2048\n"
"Maaa 40,196,236\nD0 3,20,48,1024\nMaaa 40,192,232\nTa 180,14,16384\nMaaa 48,192,240\nTa 182,14,18432\nMaaa 48,196,244\nMaaa 52,204,244\nTa 184,15,0\nMaaa 52,200,240\n"
"Ta 186,15,2048\nMaaa 112,208,240\nMaaa 112,212,244\nTa 188,15,16384\nMaaa 116,220,244\nTa 190,15,18432\nMaaa 116,216,240\nD0 4,20,48,2048\nMaaa 124,216,248\nMaaa 124,220,252\n"
"Maaa 120,212,252\nMaaa 120,208,248\nMaaa 60,200,248\nMaaa 60,204,252\nD0 5,20,48,3072\nMaaa 56,196,252\nMaaa 56,192,248\nWL 0\nMvaa 64,128,160\nTa 224,16,0\nWV 12\n"
"v_xor_b32 v27, s15, v27\nv_and_b32 v32, 0xffffdfff, v27\nv_cmp_ne_u32 vcc, 0, v32\ns_nop 0\ns_cbranch_vccnz .S412\n.S413:\nv_cmp_ne_u32 vcc, 0, v27\ns_nop 3\n"
"s_bcnt1_i32_b64 s4, vcc\ns_cmp_gt_u32 s4, 32\ns_cselect_b32 s4, 1, 0\nMvaa 64,132,164\nTa 226,16,2048\nMvaa 68,140,164\ns_add_u32 m0, s47, -65536\ns_add_u32 s45, s45, 128\n"
"Mvaa 68,136,160\nTa 228,16,16384\nMvaa 128,144,160\nTa 230,16,18432\nMvaa 128,148,164\nD0 2,20,45,0\nMvaa 132,156,164\nTa 232,17,0\nMvaa 132,152,160\nTa 234,17,2048\n"
"Mvaa 140,152,168\ns_xor_b32 s4, s4, s12\nMvaa 140,156,172\nTa 236,17,16384\nMvaa 136,148,172\nTa 238,17,18432\nMvaa 136,144,168\ns_and_b32 s4, s4, 1\nMvaa 76,136,168\nTa 240,18,0\n"
"Mvaa 76,140,172\nTa 242,18,2048\nMvaa 72,132,172\nD0 3,20,45,1024\nMvaa 72,128,168\nTa 244,18,16384\nMvaa 80,128,176\nTa 246,18,18432\nMvaa 80,132,180\ns_lshl_b32 s4, s4, 13\n"
"Mvaa 84,140,180\nTa 248,19,0\nMvaa 84,136,176\nTa 250,19,2048\nMvaa 144,144,176\ns_mul_i32 s72, s73, 25165824\nMvaa 144,148,180\nTa 252,19,16384\n.W587:\ns_cbranch_execz .W591\n"
"Mvaa 148,156,180\nTa 254,19,18432\nMvaa 148,152,176\nD0 4,20,45,2048\nMvaa 156,152,184\ns_add_u32 s72, s72, s4\nMvaa 156,156,188\ns_and_b32 s4, s12, 1\nMvaa 152,148,188\n"
"s_lshl_b32 s4, s4, 21\nMvaa 152,144,184\ns_add_u32 s72, s72, s4\nMvaa 92,136,184\ns_lshl3_add_u32 s2, s13, s11\nMvaa 92,140,188\nD0 5,20,45,3072\nMvaa 88,132,188\n"
"s_lshl_b32 s2, s2, 11\nMvaa 88,128,184\nWL 0\ns_barrier\nMvaa 96,128,224\nRa 192,10,16384\nMvaa 96,132,228\ns_add_u32 m0, s47, 0\ns_add_u32 s44, s44, 0x40000\nMvaa 100,140,228\n"
"Ra 196,11,16384\nMvaa 100,136,224\ns_lshl_b32 s3, s14, 22\nMvaa 160,144,224\nRa 200,10,18432\nMvaa 160,148,228\nD0 6,24,44,0\nMvaa 164,156,228\nRa 204,11,18432\nMvaa 164,152,224\n"
"s_add_u32 s2, s2, s3\nMvaa 172,152,232\nRa 208,10,24576\nMvaa 172,156,236\ns_add_u32 s2, s2, s72\nMvaa 168,148,236\nRa 212,11,24576\nMvaa 168,144,232\ns_add_u32 s49, s2, 0xc00000\n"
"Mvaa 108,136,232\nRa 216,10,26624\nMvaa 108,140,236\nD0 7,24,44,1024\nMvaa 104,132,236\nRa 220,11,26624\nMvaa 104,128,232\nMvaa 112,128,240\nMvaa 112,132,244\nMvaa 116,140,244\n"
"Mvaa 116,136,240\nMvaa 176,144,240\nMvaa 176,148,244\nD0 8,24,44,2048\nMvaa 180,156,244\nMvaa 180,152,240\nMvaa 188,152,248\nMvaa 188,156,252\nMvaa 184,148,252\nMvaa 184,144,248\n"
"Mvaa 124,136,248\nMvaa 124,140,252\nD0 9,24,44,3072\nMvaa 120,132,252\nMvaa 120,128,248\nWL 0\nMaaa 32,192,224\nRa 128,10,32768\ns_mov_b32 s4, s13\nMaaa 32,196,228\n"
"s_add_u32 m0, s47, 16384\ns_add_u32 s44, s44, 0x40000\nMaaa 36,204,228\nRa 132,11,32768\nv_lshrrev_b32 v32, 2, v0\nMaaa 36,200,224\nv_mul_u32_u24 v32, 896, v32\nMaaa 96,208,224\n"
"Ra 136,10,34816\nv_and_b32 v33, 3, v0\nMaaa 96,212,228\nD0 6,24,44,0\nMaaa 100,220,228\nRa 140,11,34816\nv_lshl_add_u32 v32, v33, 4, v32\nMaaa 100,216,224\ns_lshr_b32 s2, s11, 1\n"
"Maaa 108,216,232\nRa 144,10,40960\ns_mul_i32 s3, s2, 448\nMaaa 108,220,236\ns_and_b32 s5, s11, 1\nMaaa 104,212,236\nRa 148,11,40960\ns_lshl3_add_u32 s3, s5, s3\nMaaa 104,208,232\n"
"s_add_u32 s3, s3, 131072\nMaaa 44,200,232\nRa 152,10,43008\nv_add_u32 v32, s3, v32\nMaaa 44,204,236\nD0 7,24,44,1024\nMaaa 40,196,236\nRa 156,11,43008\nTv 36,32,0\n"
"Maaa 40,192,232\nTv 38,32,14336\nMaaa 48,192,240\nTv 40,32,64\nTv 42,32,14400\nMaaa 48,196,244\nTv 44,32,128\nMaaa 52,204,244\nTv 46,32,14464\nTv 48,32,192\nMaaa 52,200,240\n"
"Tv 50,32,14528\nMaaa 112,208,240\nTv 52,32,256\nTv 54,32,14592\nMaaa 112,212,244\nD0 8,24,44,2048\nMaaa 116,220,244\nTv 56,32,320\nTv 58,32,14656\nMaaa 116,216,240\nTv 60,32,384\n"
"Maaa 124,216,248\nTv 62,32,14720\ns_lshr_b32 s0, s4, 1\nMaaa 124,220,252\ns_mul_i32 s0, s0, 12\nMaaa 120,212,252\ns_lshl1_add_u32 s0, s5, s0\ns_and_b32 s6, s4, 1\n"
"Maaa 120,208,248\ns_add_u32 s0, s0, s6\nMaaa 60,200,248\ns_lshl3_add_u32 s1, s5, s4\ns_add_u32 s77, s1, 48\nMaaa 60,204,252\nD0 9,24,44,3072\nMaaa 56,196,252\n"
"s_and_b32 s4, s12, 1\ns_mov_b32 s7, 0x6543210\nMaaa 56,192,248\ns_waitcnt vmcnt(17) lgkmcnt(14)\ns_barrier\nMaaa 0,192,160\nTa 224,16,32768\ns_cmp_lg_u32 s4, s2\n"
"s_cselect_b32 s6, s0, s1\ns_cselect_b32 s5, 4, 16\ns_cselect_b32 s7, 0x6352410, s7\ns_cselect_b32 s3, 0, 229376\nMaaa 0,196,164\nTa 226,16,34816\nMaaa 4,204,164\n"
"s_add_u32 m0, s47, -49152\ns_add_u32 s48, s48, 128\nMaaa 4,200,160\nTa 228,16,49152\nMaaa 64,208,160\nTa 230,16,51200\ns_add_u32 s3, s3, s72\nMaaa 64,212,164\nD0 2,20,48,0\n"
"Maaa 68,220,164\nTa 232,17,32768\ns_add_u32 s3, s3, 0x1400000\nMaaa 68,216,160\nTa 234,17,34816\nMaaa 76,216,168\nWL 14\ns_lshr_b32 s0, s6, 4\nMaaa 76,220,172\nTa 236,17,49152\n"
"Maaa 72,212,172\nTa 238,17,51200\ns_lshl_b32 s0, s0, 2\nMaaa 72,208,168\ns_lshr_b32 s0, s7, s0\nMaaa 12,200,168\nTa 240,18,32768\ns_and_b32 s0, s0, 7\nMaaa 12,204,172\n"
"Ta 242,18,34816\nMaaa 8,196,172\nD0 3,20,48,1024\ns_lshl_b32 s0, s0, 5\nMaaa 8,192,168\nTa 244,18,49152\nMaaa 16,192,176\nTa 246,18,51200\ns_and_b32 s1, s6, 15\nMaaa 16,196,180\n"
"s_add_u32 s0, s0, s1\nMaaa 20,204,180\nTa 248,19,32768\ns_and_b32 s1, s6, 8\nMaaa 20,200,176\nTa 250,19,34816\nMaaa 80,208,176\ns_add_u32 s0, s0, s1\ns_lshl_b32 s0, s0, 10\n"
"Maaa 80,212,180\nTa 252,19,49152\nMaaa 84,220,180\nTa 254,19,51200\ns_add_u32 s0, s0, s3\nMaaa 84,216,176\nD0 4,20,48,2048\nMaaa 92,216,184\nSv 36,1,16,0,0\ns_add_u32 s6, s6, s5\n"
"Maaa 92,220,188\nMaaa 88,212,188\nMaaa 88,208,184\nMaaa 28,200,184\nMaaa 28,204,188\nD0 5,20,48,3072\nMaaa 24,196,188\nMaaa 24,192,184\nWL 0\nMvaa 96,128,224\nTa 160,12,32768\n"
"s_lshr_b32 s0, s6, 4\nMvaa 96,132,228\nTa 162,12,34816\nMvaa 100,140,228\ns_add_u32 m0, s47, -32768\ns_add_u32 s45, s45, 128\nMvaa 100,136,224\nTa 164,12,49152\nMvaa 160,144,224\n"
"Ta 166,12,51200\ns_lshl_b32 s0, s0, 2\nMvaa 160,148,228\nD0 2,20,45,0\nMvaa 164,156,228\nTa 168,13,32768\ns_lshr_b32 s0, s7, s0\nMvaa 164,152,224\nTa 170,13,34816\n"
"Mvaa 172,152,232\ns_and_b32 s0, s0, 7\ns_lshl_b32 s0, s0, 5\nMvaa 172,156,236\nTa 172,13,49152\nMvaa 168,148,236\nTa 174,13,51200\ns_and_b32 s1, s6, 15\nMvaa 168,144,232\n"
"s_add_u32 s0, s0, s1\nMvaa 108,136,232\nTa 176,14,32768\ns_and_b32 s1, s6, 8\nMvaa 108,140,236\nTa 178,14,34816\nMvaa 104,132,236\nD0 3,20,45,1024\ns_add_u32 s0, s0, s1\n"
"Mvaa 104,128,232\nTa 180,14,49152\nMvaa 112,128,240\nTa 182,14,51200\ns_lshl_b32 s0, s0, 10\nMvaa 112,132,244\ns_add_u32 s0, s0, s3\nMvaa 116,140,244\nTa 184,15,32768\n"
"Sv 40,1,16,0,0\nMvaa 116,136,240\nTa 186,15,34816\nMvaa 176,144,240\ns_add_u32 s6, s6, s5\nMvaa 176,148,244\nTa 188,15,49152\nMvaa 180,156,244\nTa 190,15,51200\nMvaa 180,152,240\n"
"D0 4,20,45,2048\nMvaa 188,152,248\nMvaa 188,156,252\nMvaa 184,148,252\nMvaa 184,144,248\nMvaa 124,136,248\ns_lshr_b32 s0, s6, 4\ns_lshl_b32 s0, s0, 2\nMvaa 124,140,252\n"
"D0 5,20,45,3072\nMvaa 120,132,252\ns_lshr_b32 s0, s7, s0\ns_and_b32 s0, s0, 7\nMvaa 120,128,248\ns_waitcnt vmcnt(18) lgkmcnt(0)\ns_barrier\nMvaa 64,128,160\nRa 192,10,49152\n"
"s_lshl_b32 s0, s0, 5\nMvaa 64,132,164\ns_add_u32 m0, s47, 32768\ns_add_u32 s44, s44, 0x40000\nMvaa 68,140,164\nRa 196,11,49152\ns_and_b32 s1, s6, 15\nMvaa 68,136,160\n"
"s_add_u32 s0, s0, s1\nMvaa 128,144,160\nRa 200,10,51200\ns_and_b32 s1, s6, 8\nMvaa 128,148,164\nD0 6,24,44,0\nMvaa 132,156,164\nRa 204,11,51200\ns_add_u32 s0, s0, s1\n"
"Mvaa 132,152,160\ns_lshl_b32 s0, s0, 10\nMvaa 140,152,168\nRa 208,10,57344\ns_add_u32 s0, s0, s3\nMvaa 140,156,172\nSv 44,1,16,0,0\nMvaa 136,148,172\nRa 212,11,57344\n"
"s_add_u32 s6, s6, s5\nMvaa 136,144,168\nMvaa 76,136,168\nRa 216,10,59392\nMvaa 76,140,172\nD0 7,24,44,1024\nMvaa 72,132,172\nRa 220,11,59392\nMvaa 72,128,168\nMvaa 80,128,176\n"
"Mvaa 80,132,180\nMvaa 84,140,180\ns_mov_b32 s6, s77\ns_mov_b32 s5, 16\nMvaa 84,136,176\ns_lshr_b32 s0, s6, 4\nMvaa 144,144,176\ns_lshl_b32 s0, s0, 2\ns_lshr_b32 s0, s7, s0\n"
"Mvaa 144,148,180\nD0 8,24,44,2048\nMvaa 148,156,180\ns_and_b32 s0, s0, 7\ns_lshl_b32 s0, s0, 5\nMvaa 148,152,176\ns_and_b32 s1, s6, 15\nMvaa 156,152,184\ns_add_u32 s0, s0, s1\n"
"s_and_b32 s1, s6, 8\nMvaa 156,156,188\ns_add_u32 s0, s0, s1\nMvaa 152,148,188\ns_lshl_b32 s0, s0, 10\ns_add_u32 s0, s0, s3\nMvaa 152,144,184\nSv 48,1,16,0,0\nMvaa 92,136,184\n"
"s_add_u32 s6, s6, s5\nMvaa 92,140,188\nD0 9,24,44,3072\nMvaa 88,132,188\nMvaa 88,128,184\nWL 0\nMaaa 0,192,160\nRa 128,10,0\nMaaa 0,196,164\ns_add_u32 m0, s47, 49152\n"
"s_add_u32 s44, s44, 0x40000\nMaaa 4,204,164\nRa 132,11,0\nMaaa 4,200,160\nMaaa 64,208,160\nRa 136,10,2048\ns_lshr_b32 s0, s6, 4\nMaaa 64,212,164\nD0 6,24,44,0\nMaaa 68,220,164\n"
"Ra 140,11,2048\ns_lshl_b32 s0, s0, 2\nMaaa 68,216,160\ns_lshr_b32 s0, s7, s0\nMaaa 76,216,168\nRa 144,10,8192\ns_and_b32 s0, s0, 7\nMaaa 76,220,172\ns_lshl_b32 s0, s0, 5\n"
"Maaa 72,212,172\nRa 148,11,8192\ns_and_b32 s1, s6, 15\nMaaa 72,208,168\ns_add_u32 s0, s0, s1\nMaaa 12,200,168\nRa 152,10,10240\ns_and_b32 s1, s6, 8\nMaaa 12,204,172\n"
"D0 7,24,44,1024\nMaaa 8,196,172\nRa 156,11,10240\ns_add_u32 s0, s0, s1\nMaaa 8,192,168\ns_lshl_b32 s0, s0, 10\nMaaa 16,192,176\ns_add_u32 s0, s0, s3\nSv 52,1,16,0,0\n"
"Maaa 16,196,180\ns_add_u32 s6, s6, s5\nMaaa 20,204,180\nMaaa 20,200,176\nMaaa 80,208,176\nMaaa 80,212,180\nD0 8,24,44,2048\nMaaa 84,220,180\nMaaa 84,216,176\nMaaa 92,216,184\n"
"Maaa 92,220,188\ns_lshr_b32 s0, s6, 4\nMaaa 88,212,188\ns_lshl_b32 s0, s0, 2\ns_lshr_b32 s0, s7, s0\nMaaa 88,208,184\ns_and_b32 s0, s0, 7\nMaaa 28,200,184\ns_lshl_b32 s0, s0, 5\n"
"s_and_b32 s1, s6, 15\nMaaa 28,204,188\nD0 9,24,44,3072\nMaaa 24,196,188\ns_add_u32 s0, s0, s1\ns_and_b32 s1, s6, 8\nMaaa 24,192,184\nWV 21\ns_barrier\nMaaa 32,192,224\n"
"Ta 160,12,0\ns_add_u32 s0, s0, s1\nMaaa 32,196,228\nTa 162,12,2048\nMaaa 36,204,228\ns_add_u32 m0, s47, -16384\ns_add_u32 s48, s48, 128\nMaaa 36,200,224\nTa 164,12,16384\n"
"Maaa 96,208,224\nTa 166,12,18432\ns_lshl_b32 s0, s0, 10\nMaaa 96,212,228\nD0 2,20,48,0\nMaaa 100,220,228\nTa 168,13,0\ns_add_u32 s0, s0, s3\nMaaa 100,216,224\nTa 170,13,2048\n"
"Maaa 108,216,232\nSv 56,1,16,0,0\ns_add_u32 s6, s6, s5\nMaaa 108,220,236\nTa 172,13,16384\nMaaa 104,212,236\nTa 174,13,18432\nMaaa 104,208,232\nMaaa 44,200,232\nTa 176,14,0\n"
"Maaa 44,204,236\nTa 178,14,2048\nMaaa 40,196,236\nD0 3,20,48,1024\nMaaa 40,192,232\nTa 180,14,16384\nMaaa 48,192,240\nTa 182,14,18432\ns_lshr_b32 s0, s6, 4\nMaaa 48,196,244\n"
"s_lshl_b32 s0, s0, 2\nMaaa 52,204,244\nTa 184,15,0\ns_lshr_b32 s0, s7, s0\nMaaa 52,200,240\nTa 186,15,2048\nMaaa 112,208,240\ns_and_b32 s0, s0, 7\ns_lshl_b32 s0, s0, 5\n"
"Maaa 112,212,244\nTa 188,15,16384\nMaaa 116,220,244\nTa 190,15,18432\ns_and_b32 s1, s6, 15\nMaaa 116,216,240\nD0 4,20,48,2048\nMaaa 124,216,248\ns_add_u32 s0, s0, s1\n"
"s_and_b32 s1, s6, 8\nMaaa 124,220,252\ns_add_u32 s0, s0, s1\nMaaa 120,212,252\ns_lshl_b32 s0, s0, 10\ns_add_u32 s0, s0, s3\nMaaa 120,208,248\nSv 60,1,16,0,0\nMaaa 60,200,248\n"
"s_add_u32 s6, s6, s5\nMaaa 60,204,252\nD0 5,20,48,3072\nMaaa 56,196,252\nMaaa 56,192,248\nWL 0\nMvaa 64,128,160\nTa 224,16,0\nMvaa 64,132,164\nTa 226,16,2048\n"
"s_add_u32 m0, s47, 65536\ns_add_u32 s46, s51, 0\nMvaa 68,140,164\nv_lshrrev_b32 v48, 4, v0\nMvaa 68,136,160\nTa 228,16,16384\nD0 6,32,46,0\nMvaa 128,144,160\nTa 230,16,18432\n"
"Mvaa 128,148,164\nv_and_b32 v49, 7, v0\nMvaa 132,156,164\nTa 232,17,0\nMvaa 132,152,160\nTa 234,17,2048\nMvaa 140,152,168\nv_xor_b32 v49, v49, v48\nD0 7,32,46,1024\n"
"Mvaa 140,156,172\nTa 236,17,16384\nMvaa 136,148,172\nTa 238,17,18432\nMvaa 136,144,168\nv_lshlrev_b32 v49, 4, v49\nMvaa 76,136,168\nTa 240,18,0\nMvaa 76,140,172\nTa 242,18,2048\n"
"D0 8,32,46,2048\nMvaa 72,132,172\nv_lshrrev_b32 v48, 3, v0\nMvaa 72,128,168\nTa 244,18,16384\nMvaa 80,128,176\nTa 246,18,18432\nMvaa 80,132,180\nv_mul_u32_u24 v48, 896, v48\n"
"Mvaa 84,140,180\nTa 248,19,0\nD0 9,32,46,3072\nMvaa 84,136,176\nTa 250,19,2048\nMvaa 144,144,176\nv_add_u32 v40, v48, v49\nMvaa 144,148,180\nTa 252,19,16384\n"
"s_add_u32 m0, s47, 81920\ns_add_u32 s46, s51, 262144\nMvaa 148,156,180\nTa 254,19,18432\nMvaa 148,152,176\nv_xor_b32 v49, 64, v40\nD0 6,32,46,0\nMvaa 156,152,184\n"
"v_add_u32 v41, 6144, v49\nMvaa 156,156,188\nv_add_u32 v42, 55296, v40\nMvaa 152,148,188\nv_add_u32 v43, 61440, v49\n.W588:\ns_cbranch_execz .W592\nMvaa 152,144,184\n"
"s_mov_b32 s2, s13\nMvaa 92,136,184\ns_lshl3_add_u32 s2, s2, s11\nD0 7,32,46,1024\nMvaa 92,140,188\ns_mul_i32 s52, s2, 14336\nMvaa 88,132,188\ns_lshr_b32 s2, s13, 2\n"
"s_bfe_u32 s3, s13, 0x10001\nMvaa 88,128,184\ns_waitcnt vmcnt(23) lgkmcnt(0)\ns_barrier\nMvaa 96,128,224\nRa 192,10,16384\ns_mul_i32 s3, s3, 17\nMvaa 96,132,228\n"
"s_add_u32 s2, s2, s3\nD0 8,32,46,2048\nMvaa 100,140,228\nRa 196,11,16384\ns_lshl2_add_u32 s3, s14, 0\nMvaa 100,136,224\ns_lshl_b32 s3, s3, 2\nMvaa 160,144,224\nRa 200,10,18432\n"
"s_lshr_b32 s3, 0xe420ca86, s3\nMvaa 160,148,228\ns_and_b32 s3, s3, 15\nMvaa 164,156,228\nRa 204,11,18432\ns_add_u32 s2, s2, s3\nD0 9,32,46,3072\nMvaa 164,152,224\n"
"s_cmp_eq_u32 s2, 32\ns_cselect_b32 s4, 16, s2\nMvaa 172,152,232\nRa 208,10,24576\ns_mul_i32 s75, s4, 65536\nMvaa 172,156,236\ns_lshr_b32 s2, s13, 2\ns_add_u32 m0, s47, -65536\n"
"s_add_u32 s46, s52, 0\nMvaa 168,148,236\nRa 212,11,24576\ns_bfe_u32 s3, s13, 0x10001\nMvaa 168,144,232\ns_mul_i32 s3, s3, 17\nD0 40,28,46,0\nMvaa 108,136,232\nRa 216,10,26624\n"
"s_add_u32 s2, s2, s3\nMvaa 108,140,236\ns_lshl2_add_u32 s3, s14, 1\nMvaa 104,132,236\nRa 220,11,26624\ns_lshl_b32 s3, s3, 2\nMvaa 104,128,232\ns_lshr_b32 s3, 0xe420ca86, s3\n"
"Mvaa 112,128,240\ns_and_b32 s3, s3, 15\ns_add_u32 s2, s2, s3\nD0 41,28,46,1024\nMvaa 112,132,244\ns_cmp_eq_u32 s2, 32\ns_cselect_b32 s4, 16, s2\nMvaa 116,140,244\n"
"s_mul_i32 s76, s4, 65536\ns_lshr_b32 s2, s13, 2\nMvaa 116,136,240\ns_bfe_u32 s3, s13, 0x10001\nMvaa 176,144,240\ns_mul_i32 s3, s3, 17\ns_add_u32 s2, s2, s3\nMvaa 176,148,244\n"
"s_lshl2_add_u32 s3, s14, 2\nD0 42,28,46,2048\nMvaa 180,156,244\ns_lshl_b32 s3, s3, 2\ns_lshr_b32 s3, 0xe420ca86, s3\nMvaa 180,152,240\ns_and_b32 s3, s3, 15\nMvaa 188,152,248\n"
"s_add_u32 s2, s2, s3\nMvaa 188,156,252\ns_cmp_eq_u32 s2, 32\ns_cselect_b32 s4, 16, s2\nMvaa 184,148,252\ns_mul_i32 s77, s4, 65536\ns_lshr_b32 s2, s13, 2\nD0 43,28,46,3072\n"
"Mvaa 184,144,248\ns_bfe_u32 s3, s13, 0x10001\nMvaa 124,136,248\ns_mul_i32 s3, s3, 17\ns_add_u32 s2, s2, s3\nMvaa 124,140,252\ns_lshl2_add_u32 s3, s14, 3\nMvaa 120,132,252\n"
"s_lshl_b32 s3, s3, 2\ns_lshr_b32 s3, 0xe420ca86, s3\nMvaa 120,128,248\nWL 0\nMaaa 32,192,224\nRa 128,10,32768\ns_and_b32 s3, s3, 15\ns_add_u32 m0, s47, 0\n"
"s_add_u32 s46, s51, 524288\nD0 6,32,46,0\nMaaa 32,196,228\ns_add_u32 s2, s2, s3\ns_cmp_eq_u32 s2, 32\ns_cselect_b32 s4, 16, s2\nMaaa 36,204,228\nRa 132,11,32768\n"
"s_mul_i32 s78, s4, 65536\nMaaa 36,200,224\ns_lshr_b32 s3, s11, 1\nMaaa 96,208,224\nRa 136,10,34816\ns_lshl_b32 s3, s3, 15\nMaaa 96,212,228\ns_add_u32 s2, s72, s3\nD0 7,32,46,1024\n"
"Maaa 100,220,228\nRa 140,11,34816\ns_and_b32 s3, s11, 1\nMaaa 100,216,224\ns_lshl_b32 s3, s3, 14\nMaaa 108,216,232\nRa 144,10,40960\ns_add_u32 s2, s2, s3\nMaaa 108,220,236\n"
"s_add_u32 s2, s2, 0x400000\nMaaa 104,212,236\nRa 148,11,40960\ns_mov_b32 s55, 4096\nD0 8,32,46,2048\nMaaa 104,208,232\ns_add_u32 s75, s75, s2\nMaaa 44,200,232\nRa 152,10,43008\n"
"s_add_u32 s76, s76, s2\nMaaa 44,204,236\ns_add_u32 s77, s77, s2\nMaaa 40,196,236\nRa 156,11,43008\ns_add_u32 s78, s78, s2\nMaaa 40,192,232\nv_add_u32 v32, 0x10000, v12\n"
"D0 9,32,46,3072\nMaaa 48,192,240\nv_add_u32 v33, 0x10000, v13\nv_add_u32 v34, 0x10000, v14\nMaaa 48,196,244\nv_add_u32 v35, 0x10000, v15\nMaaa 52,204,244\n"
"v_add_u32 v36, 0x10000, v16\nv_add_u32 v37, 0x10000, v17\ns_add_u32 m0, s47, 16384\ns_add_u32 s46, s51, 786432\nMaaa 52,200,240\nv_add_u32 v38, 0x10000, v18\nMaaa 112,208,240\n"
"v_add_u32 v39, 0x10000, v19\ns_add_u32 s2, s11, 1\nD0 6,32,46,0\nMaaa 112,212,244\ns_lshr_b32 s2, s2, 1\nMaaa 116,220,244\ns_lshl_b32 s2, s2, 12\nv_add_u32 v44, s2, v10\n"
"Maaa 116,216,240\nv_add_u32 v45, s2, v11\nMaaa 124,216,248\nv_xor_b32 v46, 0x1000, v44\nv_xor_b32 v47, 0x1000, v45\nMaaa 124,220,252\ns_lshr_b32 s3, s11, 1\nD0 7,32,46,1024\n"
"Maaa 120,212,252\ns_lshl_b32 s3, s3, 13\ns_lshl_b32 s2, s11, 12\nMaaa 120,208,248\ns_add_u32 s2, s2, s3\nMaaa 60,200,248\ns_add_u32 s2, s2, s72\ns_lshl_b32 s3, s11, 12\n"
"Maaa 60,204,252\ns_add_u32 s89, s3, 0x10000\nMaaa 56,196,252\ns_mov_b32 s88, 0xffff8000\ns_lshl_b32 s4, s13, 1\nD0 8,32,46,2048\nMaaa 56,192,248\ns_waitcnt vmcnt(25) lgkmcnt(0)\n"
"s_barrier\nMaaa 0,192,160\nTa 224,16,32768\ns_sub_u32 s79, 15, s4\nMaaa 0,196,164\nTa 226,16,34816\nMaaa 4,204,164\ns_add_u32 s96, s2, 0x1310000\ns_mov_b32 s63, 0x53800000\n"
"Maaa 4,200,160\nTa 228,16,49152\nD0 9,32,46,3072\nMaaa 64,208,160\nTa 230,16,51200\ns_mov_b32 s64, 0x2b800000\nMaaa 64,212,164\ns_sub_u32 s98, s4, 1\nMaaa 68,220,164\n"
"Ta 232,17,32768\ns_lshl_b32 s5, s13, 5\ns_add_u32 m0, s47, -32768\ns_add_u32 s46, s52, 128\nMaaa 68,216,160\nTa 234,17,34816\nMaaa 76,216,168\ns_add_u32 s5, s5, s11\n"
"s_lshl_b32 s65, s5, 9\nD0 40,28,46,0\nMaaa 76,220,172\nTa 236,17,49152\nMaaa 72,212,172\nTa 238,17,51200\ns_add_u32 s65, s65, s72\nMaaa 72,208,168\ns_add_u32 s65, s65, 0x1580000\n"
"Maaa 12,200,168\nTa 240,18,32768\ns_and_b32 s66, s72, 0x2000\nMaaa 12,204,172\nTa 242,18,34816\nD0 41,28,46,1024\nMaaa 8,196,172\ns_add_u32 s66, s66, s56\n"
"s_add_u32 s66, s66, 0x100000\nMaaa 8,192,168\nTa 244,18,49152\nMaaa 16,192,176\nTa 246,18,51200\ns_and_b32 s5, s12, 1\nMaaa 16,196,180\ns_mul_i32 s58, s5, 0x202000\n"
"Maaa 20,204,180\nTa 248,19,32768\ns_xor_b32 s58, s58, s72\nD0 42,28,46,2048\nMaaa 20,200,176\nTa 250,19,34816\nMaaa 80,208,176\ns_add_u32 s58, s58, 0x15c0000\n"
"s_lshl_b32 s5, s5, 8\nMaaa 80,212,180\nTa 252,19,49152\nMaaa 84,220,180\nTa 254,19,51200\ns_add_u32 s61, s58, s5\nMaaa 84,216,176\ns_lshl4_add_u32 s60, s13, s61\nD0 43,28,46,3072\n"
"Maaa 92,216,184\ns_add_u32 s54, s60, 128\ns_xor_b32 s60, s54, 0x100\nMaaa 92,220,188\ns_cmp_eq_u32 s11, 0\ns_cselect_b32 s54, s54, 0x40000000\nMaaa 88,212,188\ns_getpc_b64 s[8:9]\n"
".PC425:\ns_add_u32 s8, s8, .HD411-.PC425\ns_addc_u32 s9, s9, 0\ns_add_u32 m0, s47, -49152\ns_add_u32 s46, s52, 256\nMaaa 88,208,184\ns_mov_b32 s97, 0xff800000\nMaaa 28,200,184\n"
"s_mov_b32 s90, 0\nD0 40,28,46,0\nMaaa 28,204,188\ns_cmp_lt_u32 s11, 2\ns_cselect_b32 s92, 2, 0\nMaaa 24,196,188\ns_mul_i32 s4, s79, 65536\ns_sub_u32 s4, s2, s4\nMaaa 24,192,184\n"
"WL 0\nMvaa 96,128,224\nTa 160,12,32768\ns_add_u32 s94, s4, 0x20000\nMvaa 96,132,228\nTa 162,12,34816\nD0 41,28,46,1024\nMvaa 100,140,228\ns_add_u32 s95, s4, 0x410000\n"
"s_add_u32 s86, s2, 0x130000\nMvaa 100,136,224\nTa 164,12,49152\nMvaa 108,136,232\nTa 166,12,51200\ns_add_u32 s87, s2, 0x520000\nMvaa 108,140,236\ns_sub_u32 s99, s79, 2\n"
"s_cmp_eq_u32 s13, 7\ns_cselect_b32 s93, 1.0, 0\nMvaa 104,132,236\nTa 168,13,32768\ns_mov_b32 s36, s68\nD0 42,28,46,2048\nMvaa 104,128,232\nTa 170,13,34816\nMvaa 112,128,240\n"
"s_and_b32 s37, s69, 0xffff\ns_mov_b32 s38, 0x1c0000\nMvaa 112,132,244\nTa 172,13,49152\nCV 208,96,97\nMvaa 116,140,244\nTa 174,13,51200\ns_mov_b32 s39, 0x20000\nMvaa 116,136,240\n"
"s_and_b32 s2, s74, 15\nD0 43,28,46,3072\nMvaa 124,136,248\nTa 176,14,32768\ns_min_u32 s2, s2, 13\nMvaa 124,140,252\nTa 178,14,34816\nMvaa 120,132,252\ns_sub_u32 s2, s2, 0\n"
"s_mul_i32 s3, s73, 14\nMvaa 120,128,248\nTa 180,14,49152\nCV 209,98,99\nMvaa 160,144,224\nTa 182,14,51200\ns_add_u32 s2, s2, s3\nMvaa 160,148,228\ns_lshl2_add_u32 s2, s2, s11\n"
"Mvaa 164,156,228\nTa 184,15,32768\ns_mul_i32 s3, s2, 4682\nMvaa 164,152,224\nTa 186,15,34816\nCV 210,100,101\nMvaa 172,152,232\ns_lshr_b32 s3, s3, 16\ns_mul_i32 s22, s3, 14\n"
"Mvaa 172,156,236\nTa 188,15,49152\nCV 211,102,103\nMvaa 168,148,236\nTa 190,15,51200\ns_sub_u32 s22, s2, s22\nMvaa 168,144,232\ns_and_b32 s21, s3, 28\nMvaa 176,144,240\n"
"s_add_u32 s21, s21, s3\ns_lshl_b32 s21, s21, 4\nMvaa 176,148,244\ns_bfe_u32 s41, s3, 0x30002\ns_cmp_gt_u32 s22, 6\ns_cselect_b32 s2, 1179648, 0\ns_cselect_b32 s1, 16, 0\n"
"Mvaa 180,156,244\ns_add_u32 s41, s41, s1\ns_lshl4_add_u32 s41, s41, 0xc048480\nMvaa 180,152,240\ns_and_b32 s1, s3, 28\nCV 212,104,105\nMvaa 188,152,248\ns_add_u32 s3, s3, s1\n"
"s_lshl_b32 s3, s3, 11\nMvaa 188,156,252\ns_add_u32 s2, s2, s3\nMvaa 184,148,252\ns_lshl_b32 s3, s22, 17\ns_add_u32 s2, s2, s3\nMvaa 184,144,248\ns_waitcnt vmcnt(16) lgkmcnt(0)\n"
"s_barrier\nMvaa 64,128,160\nRa 192,10,49152\ns_add_u32 s20, s2, 0x10e0000\ns_add_u32 m0, s47, 32768\ns_add_u32 s46, s51, 1048576\nMvaa 64,132,164\ns_lshl_b32 s2, s22, 7\n"
"Mvaa 68,140,164\nRa 196,11,49152\ns_add_u32 s40, s2, 0xc04b800\nD0 6,32,46,0\nMvaa 68,136,160\ns_mul_i32 s2, s21, 896\nCV 213,106,107\nMvaa 76,136,168\nRa 200,10,51200\n"
"s_lshl_b32 s3, s22, 6\nMvaa 76,140,172\ns_add_u32 s22, s2, s3\nCV 214,108,109\nMvaa 72,132,172\nRa 204,11,51200\ns_add_u32 s23, s22, 57344\nMvaa 72,128,168\nD0 7,32,46,1024\n"
"Mvaa 80,128,176\nRa 208,10,57344\nCV 215,110,111\nMvaa 80,132,180\nCV 216,112,113\nCV 217,114,115\nMvaa 84,140,180\nRa 212,11,57344\nMvaa 84,136,176\nCV 218,116,117\n"
"CV 219,118,119\nMvaa 92,136,184\nRa 216,10,59392\nD0 8,32,46,2048\ns_lshl3_add_u32 s5, s12, s13\ns_lshl2_add_u32 s5, s5, s11\ns_lshl2_add_u32 s5, s5, 0xc04a400\n"
"buffer_load_dword v27, off, s[16:19], s5 sc0 sc1\nMvaa 92,140,188\nCV 220,120,121\nMvaa 88,132,188\nRa 220,11,59392\nCV 221,122,123\nMvaa 88,128,184\nCV 222,124,125\n"
"CV 223,126,127\nMvaa 128,144,160\nCV 192,64,65\nCV 193,66,67\nMvaa 128,148,164\nD0 9,32,46,3072\nMvaa 132,156,164\nCV 194,68,69\nCV 195,70,71\nMvaa 132,152,160\nCV 196,72,73\n"
"CV 197,74,75\nMvaa 140,152,168\ns_add_u32 m0, s47, 49152\ns_add_u32 s46, s51, 1310720\nMvaa 140,156,172\nCV 198,76,77\nCV 199,78,79\nMvaa 136,148,172\nD0 6,32,46,0\nCV 200,80,81\n"
"Mvaa 136,144,168\nCV 201,82,83\nMvaa 144,144,176\nCV 202,84,85\nCV 203,86,87\nMvaa 144,148,180\nCV 204,88,89\nCV 205,90,91\nMvaa 148,156,180\nCV 206,92,93\nMvaa 148,152,176\n"
"D0 7,32,46,1024\nCV 207,94,95\nMvaa 156,152,184\nCV 240,160,161\nMvaa 156,156,188\nCV 241,162,163\nSv 192,1,16,75,0\nMvaa 152,148,188\nCV 242,164,165\nMvaa 152,144,184\nWL 0\n"
"Maaa 0,192,160\nD0 8,32,46,2048\nRa 128,44,0\nMaaa 0,196,164\nCV 243,166,167\nMaaa 4,204,164\nRa 132,45,0\nMaaa 4,200,160\nCV 244,168,169\nMaaa 12,200,168\nRa 136,44,2048\n"
"Maaa 12,204,172\nD0 9,32,46,3072\nMaaa 8,196,172\nRa 140,45,2048\nSv 196,1,16,75,1024\nMaaa 8,192,168\nCV 245,170,171\nMaaa 16,192,176\nRa 144,46,0\nMaaa 16,196,180\n"
"CV 246,172,173\nMaaa 20,204,180\nRa 148,47,0\nMaaa 20,200,176\nCV 247,174,175\nMaaa 28,200,184\nRa 152,46,2048\nMaaa 28,204,188\nCV 248,176,177\nMaaa 24,196,188\nRa 156,47,2048\n"
"Maaa 24,192,184\nCV 249,178,179\nSv 200,1,16,75,2048\nMaaa 64,208,160\nCV 250,180,181\nCV 251,182,183\nMaaa 64,212,164\nCV 252,184,185\nMaaa 68,220,164\nCV 253,186,187\n"
"Maaa 68,216,160\nCV 254,188,189\nMaaa 76,216,168\nCV 255,190,191\nMaaa 76,220,172\nCV 224,128,129\nMaaa 72,212,172\nCV 225,130,131\nMaaa 72,208,168\nCV 226,132,133\n"
"Maaa 80,208,176\nCV 227,134,135\nSv 204,1,16,75,3072\nMaaa 80,212,180\nCV 228,136,137\nMaaa 84,220,180\nCV 229,138,139\nMaaa 84,216,176\nCV 230,140,141\nMaaa 92,216,184\n"
"CV 231,142,143\nMaaa 92,220,188\nCV 232,144,145\nMaaa 88,212,188\nCV 233,146,147\nMaaa 88,208,184\ns_barrier\nMaaa 32,192,224\ns_add_u32 m0, s47, -16384\ns_add_u32 s46, s52, 384\n"
"Ta 160,32,0\nMaaa 32,196,228\nTa 162,32,2048\nMaaa 36,204,228\nD0 40,28,46,0\ns_add_u32 s75, s75, s55\nSv 208,1,16,75,0\nMaaa 36,200,224\nTa 164,32,16384\nMaaa 44,200,232\n"
"Ta 166,32,18432\nMaaa 44,204,236\nCV 234,148,149\nMaaa 40,196,236\nTa 168,33,0\nMaaa 40,192,232\nD0 41,28,46,1024\nTa 170,33,2048\nMaaa 48,192,240\nCV 235,150,151\n"
"Maaa 48,196,244\nTa 172,33,16384\nMaaa 52,204,244\nTa 174,33,18432\nMaaa 52,200,240\n.W589:\ns_cbranch_execz .W593\nCV 236,152,153\nSv 212,1,16,75,1024\nMaaa 60,200,248\n"
"D0 42,28,46,2048\nTa 176,34,0\nMaaa 60,204,252\nTa 178,34,2048\nMaaa 56,196,252\nCV 237,154,155\nMaaa 56,192,248\nTa 180,34,16384\nMaaa 96,208,224\nTa 182,34,18432\n"
"CV 238,156,157\nMaaa 96,212,228\nD0 43,28,46,3072\nMaaa 100,220,228\nTa 184,35,0\nMaaa 100,216,224\nTa 186,35,2048\nMaaa 108,216,232\nCV 239,158,159\nSv 216,1,16,75,2048\n"
"Maaa 108,220,236\nTa 188,35,16384\nMaaa 104,212,236\nTa 190,35,18432\nMaaa 104,208,232\nMaaa 112,208,240\nMaaa 112,212,244\nMaaa 116,220,244\nMaaa 116,216,240\nMaaa 124,216,248\n"
"Maaa 124,220,252\nSv 220,1,16,75,3072\nMaaa 120,212,252\nMaaa 120,208,248\nWL 0\nZvaa 64,160,128\nTa 224,36,0\nAR 60,0\nMvaa 64,164,132\nTa 226,36,2048\nZvaa 96,164,140\nAR 61,1\n"
"Mvaa 96,160,136\nTa 228,36,16384\nCV 192,60,61\nZvaa 128,160,144\nTa 230,36,18432\nMvaa 128,164,148\nAR 60,2\nAR 61,3\nZvaa 160,164,156\nTa 232,37,0\nSv 224,1,16,76,0\n"
"Mvaa 160,160,152\nTa 234,37,2048\nCV 193,60,61\nZvaa 164,168,152\nAR 60,4\nMvaa 164,172,156\nTa 236,37,16384\nZvaa 132,172,148\nTa 238,37,18432\nAR 61,5\nMvaa 132,168,144\n"
"CV 194,60,61\nZvaa 100,168,136\nTa 240,38,0\nAR 60,6\nMvaa 100,172,140\nTa 242,38,2048\nZvaa 68,172,132\nSv 228,1,16,76,1024\nAR 61,7\nMvaa 68,168,128\nTa 244,38,16384\n"
"Zvaa 72,176,128\nTa 246,38,18432\nCV 195,60,61\nMvaa 72,180,132\nAR 60,8\nZvaa 104,180,140\nTa 248,39,0\nMvaa 104,176,136\nTa 250,39,2048\nAR 61,9\nZvaa 136,176,144\nCV 196,60,61\n"
"Mvaa 136,180,148\nTa 252,39,16384\nAR 60,10\nZvaa 168,180,156\nTa 254,39,18432\nSv 232,1,16,76,2048\nMvaa 168,176,152\nAR 61,11\nCV 197,60,61\nZvaa 172,184,152\nAR 60,12\n"
"Mvaa 172,188,156\nAR 61,13\nZvaa 140,188,148\nCV 198,60,61\nAR 60,14\nMvaa 140,184,144\nAR 61,15\nZvaa 108,184,136\nCV 199,60,61\nAR 60,16\nMvaa 108,188,140\nSv 236,1,16,76,3072\n"
"Zvaa 76,188,132\nAR 61,17\nCV 200,60,61\nMvaa 76,184,128\ns_waitcnt vmcnt(29) lgkmcnt(0)\ns_barrier\nZvaa 80,224,128\nRa 192,44,32768\nAR 60,18\nMvaa 80,228,132\nRa 196,45,32768\n"
"s_add_u32 m0, s47, 65536\ns_add_u32 s46, s51, 1572864\nD0 6,32,46,0\nZvaa 112,228,140\nRa 200,44,34816\nMvaa 112,224,136\nRa 204,45,34816\nAR 61,19\nZvaa 144,224,144\n"
"Ra 208,46,32768\nMvaa 144,228,148\nRa 212,47,32768\nCV 201,60,61\nD0 7,32,46,1024\nZvaa 176,228,156\nRa 216,46,34816\nMvaa 176,224,152\nRa 220,47,34816\nAR 60,20\n"
"Zvaa 180,232,152\nTa 160,12,0\nMvaa 180,236,156\nTa 162,12,2048\nD0 8,32,46,2048\nZvaa 148,236,148\nTa 164,12,16384\nAR 61,21\nMvaa 148,232,144\nTa 166,12,18432\nZvaa 116,232,136\n"
"Ta 168,13,0\nCV 202,60,61\nMvaa 116,236,140\nTa 170,13,2048\nD0 9,32,46,3072\nZvaa 84,236,132\nTa 172,13,16384\nAR 60,22\nMvaa 84,232,128\nTa 174,13,18432\n"
"s_add_u32 m0, s47, 81920\ns_add_u32 s46, s51, 1835008\nZvaa 88,240,128\nTa 176,14,0\nAR 61,23\nMvaa 88,244,132\nTa 178,14,2048\nD0 6,32,46,0\nZvaa 120,244,140\nTa 180,14,16384\n"
"Mvaa 120,240,136\nTa 182,14,18432\nCV 203,60,61\nZvaa 152,240,144\nTa 184,15,0\nMvaa 152,244,148\nTa 186,15,2048\nAR 60,24\nD0 7,32,46,1024\nZvaa 184,244,156\nTa 188,15,16384\n"
"Mvaa 184,240,152\nTa 190,15,18432\nAR 61,25\nZvaa 188,248,152\nCV 204,60,61\nMvaa 188,252,156\nAR 60,26\nD0 8,32,46,2048\nZvaa 156,252,148\nAR 61,27\nCV 205,60,61\n"
"Mvaa 156,248,144\nAR 60,28\nZvaa 124,248,136\nAR 61,29\nCV 206,60,61\nMvaa 124,252,140\nAR 60,30\nD0 9,32,46,3072\nZvaa 92,252,132\nAR 61,31\nCV 207,60,61\nMvaa 92,248,128\nWL 0\n"
"Mvaa 64,160,192\nTa 224,16,0\nAR 60,32\nMvaa 64,164,196\nTa 226,16,2048\ns_add_u32 m0, s47, -65536\ns_add_u32 s46, s52, 512\nD0 40,28,46,0\nMvaa 96,164,204\nAR 61,33\n"
"Mvaa 96,160,200\nTa 228,16,16384\nCV 208,60,61\nMvaa 128,160,208\nTa 230,16,18432\nMvaa 128,164,212\nAR 60,34\nAR 61,35\nD0 41,28,46,1024\nMvaa 160,164,220\nTa 232,17,0\n"
"Mvaa 160,160,216\nTa 234,17,2048\nCV 209,60,61\nMvaa 164,168,216\nAR 60,36\nMvaa 164,172,220\nTa 236,17,16384\nD0 42,28,46,2048\nMvaa 132,172,212\nTa 238,17,18432\nAR 61,37\n"
"Mvaa 132,168,208\nCV 210,60,61\nMvaa 100,168,200\nTa 240,18,0\nAR 60,38\nMvaa 100,172,204\nTa 242,18,2048\nD0 43,28,46,3072\nMvaa 68,172,196\ns_add_u32 s76, s76, s55\n"
"Sv 240,1,16,76,0\nMvaa 68,168,192\nTa 244,18,16384\nMvaa 72,176,192\nTa 246,18,18432\nAR 61,39\nMvaa 72,180,196\nCV 211,60,61\nMvaa 104,180,204\nTa 248,19,0\nMvaa 104,176,200\n"
"Ta 250,19,2048\nSv 244,1,16,76,1024\nMvaa 136,176,208\nAR 60,40\nMvaa 136,180,212\nTa 252,19,16384\nAR 61,41\nMvaa 168,180,220\nTa 254,19,18432\nMvaa 168,176,216\nCV 212,60,61\n"
"AR 60,42\nMvaa 172,184,216\nSv 248,1,16,76,2048\nMvaa 172,188,220\nAR 61,43\nMvaa 140,188,212\nCV 213,60,61\nAR 60,44\nMvaa 140,184,208\nAR 61,45\nMvaa 108,184,200\nCV 214,60,61\n"
"AR 60,46\nMvaa 108,188,204\nSv 252,1,16,76,3072\nMvaa 76,188,196\nAR 61,47\nCV 215,60,61\nMvaa 76,184,192\ns_waitcnt vmcnt(20) lgkmcnt(0)\ns_barrier\nMvaa 80,224,192\n"
"Ra 128,44,16384\nAR 60,48\nv_cmp_ne_u32 vcc, s15, v27\ns_nop 0\ns_cbranch_vccnz .S426\n.S427:\nMvaa 80,228,196\nRa 132,45,16384\n"
"buffer_store_dword v25, off, s[16:19], s54 offset:0 sc0 sc1\ns_add_u32 m0, s47, 0\ns_add_u32 s46, s51, 2097152\nD0 6,32,46,0\nMvaa 112,228,204\nRa 136,44,18432\nMvaa 112,224,200\n"
"Ra 140,45,18432\nAR 61,49\nMvaa 144,224,208\nRa 144,46,16384\nMvaa 144,228,212\nRa 148,47,16384\nCV 216,60,61\nD0 7,32,46,1024\nMvaa 176,228,220\nRa 152,46,18432\n"
"Mvaa 176,224,216\nRa 156,47,18432\nAR 60,50\nMvaa 180,232,216\nTa 160,12,32768\nMvaa 180,236,220\nTa 162,12,34816\nD0 8,32,46,2048\nMvaa 148,236,212\nTa 164,12,49152\nAR 61,51\n"
"Mvaa 148,232,208\nTa 166,12,51200\nMvaa 116,232,200\nTa 168,13,32768\nCV 217,60,61\nMvaa 116,236,204\nTa 170,13,34816\nD0 9,32,46,3072\nMvaa 84,236,196\nTa 172,13,49152\nAR 60,52\n"
"Mvaa 84,232,192\nTa 174,13,51200\ns_add_u32 m0, s47, 16384\ns_add_u32 s46, s51, 2359296\nMvaa 88,240,192\nTa 176,14,32768\nAR 61,53\nMvaa 88,244,196\nTa 178,14,34816\n"
"D0 6,32,46,0\nMvaa 120,244,204\nTa 180,14,49152\nMvaa 120,240,200\nTa 182,14,51200\nCV 218,60,61\nMvaa 152,240,208\nTa 184,15,32768\nMvaa 152,244,212\nTa 186,15,34816\nAR 60,54\n"
"D0 7,32,46,1024\nMvaa 184,244,220\nTa 188,15,49152\nMvaa 184,240,216\nTa 190,15,51200\nAR 61,55\nMvaa 188,248,216\nCV 219,60,61\nMvaa 188,252,220\nAR 60,56\nD0 8,32,46,2048\n"
"Mvaa 156,252,212\nAR 61,57\nCV 220,60,61\nMvaa 156,248,208\nAR 60,58\nMvaa 124,248,200\nAR 61,59\nCV 221,60,61\nMvaa 124,252,204\nAR 60,60\nD0 9,32,46,3072\nMvaa 92,252,196\n"
"AR 61,61\nCV 222,60,61\nMvaa 92,248,192\nWL 0\nMvaa 64,160,128\nTa 224,16,32768\nAR 60,62\nMvaa 64,164,132\nTa 226,16,34816\ns_add_u32 m0, s47, -32768\ns_add_u32 s46, s52, 640\n"
"D0 40,28,46,0\nMvaa 96,164,140\nAR 61,63\nMvaa 96,160,136\nTa 228,16,49152\nCV 223,60,61\nMvaa 128,160,144\nTa 230,16,51200\nMvaa 128,164,148\nD0 41,28,46,1024\nMvaa 160,164,156\n"
"Ta 232,17,32768\nAR 60,126\nMvaa 160,160,152\nTa 234,17,34816\nMvaa 164,168,152\nAR 61,127\nCV 60,60,61\nMvaa 164,172,156\nTa 236,17,49152\nD0 42,28,46,2048\nMvaa 132,172,148\n"
"Ta 238,17,51200\nMvaa 132,168,144\nAW 127,60\nAR 60,124\nMvaa 100,168,136\nTa 240,18,32768\nMvaa 100,172,140\nTa 242,18,34816\nD0 43,28,46,3072\nMvaa 68,172,132\nSv 192,1,16,77,0\n"
"AR 61,125\nMvaa 68,168,128\nTa 244,18,49152\nMvaa 72,176,128\nTa 246,18,51200\nCV 60,60,61\nMvaa 72,180,132\nAW 126,60\nMvaa 104,180,140\nTa 248,19,32768\nMvaa 104,176,136\n"
"Ta 250,19,34816\nSv 196,1,16,77,1024\nMvaa 136,176,144\nAR 60,122\nMvaa 136,180,148\nTa 252,19,49152\nMvaa 168,180,156\nTa 254,19,51200\nAR 61,123\nMvaa 168,176,152\nCV 60,60,61\n"
"Mvaa 172,184,152\nSv 200,1,16,77,2048\nAW 125,60\nMvaa 172,188,156\nAR 60,120\nMvaa 140,188,148\nAR 61,121\nMvaa 140,184,144\nCV 60,60,61\nAW 124,60\nMvaa 108,184,136\nAR 60,118\n"
"Mvaa 108,188,140\nSv 204,1,16,77,3072\nMvaa 76,188,132\nAR 61,119\nCV 60,60,61\nMvaa 76,184,128\ns_waitcnt vmcnt(17) lgkmcnt(0)\ns_barrier\nMvaa 80,224,128\nRa 192,44,49152\n"
"AW 123,60\nMvaa 80,228,132\nRa 196,45,49152\nbuffer_store_dword v25, off, s[16:19], s54 offset:8 sc0 sc1\ns_add_u32 m0, s47, 32768\ns_add_u32 s46, s51, 2621440\nD0 6,32,46,0\n"
"Mvaa 112,228,140\nRa 200,44,51200\nMvaa 112,224,136\nRa 204,45,51200\nAR 60,116\nMvaa 144,224,144\nRa 208,46,49152\nMvaa 144,228,148\nRa 212,47,49152\nD0 7,32,46,1024\n"
"Mvaa 176,228,156\nRa 216,46,51200\nAR 61,117\nMvaa 176,224,152\nRa 220,47,51200\nMvaa 180,232,152\nTa 160,32,0\nCV 60,60,61\nMvaa 180,236,156\nTa 162,32,2048\nD0 8,32,46,2048\n"
"Mvaa 148,236,148\nTa 164,32,16384\nMvaa 148,232,144\nTa 166,32,18432\nAW 122,60\nMvaa 116,232,136\nTa 168,33,0\nMvaa 116,236,140\nTa 170,33,2048\nD0 9,32,46,3072\nMvaa 84,236,132\n"
"Ta 172,33,16384\nAR 60,114\nMvaa 84,232,128\nTa 174,33,18432\ns_add_u32 m0, s47, 49152\ns_add_u32 s46, s51, 2883584\nMvaa 88,240,128\nTa 176,34,0\n.W590:\ns_cbranch_execz .W594\n"
"AR 61,115\nMvaa 88,244,132\nTa 178,34,2048\nD0 6,32,46,0\nMvaa 120,244,140\nTa 180,34,16384\nMvaa 120,240,136\nTa 182,34,18432\nCV 60,60,61\nMvaa 152,240,144\nTa 184,35,0\n"
"Mvaa 152,244,148\nTa 186,35,2048\nD0 7,32,46,1024\nMvaa 184,244,156\nTa 188,35,16384\nAW 121,60\nMvaa 184,240,152\nTa 190,35,18432\nMvaa 188,248,152\nAR 60,112\nAR 61,113\n"
"Mvaa 188,252,156\nD0 8,32,46,2048\nMvaa 156,252,148\nCV 60,60,61\nMvaa 156,248,144\nAW 120,60\nAR 60,110\nMvaa 124,248,136\nAR 61,111\nMvaa 124,252,140\nD0 9,32,46,3072\n"
"Mvaa 92,252,132\nCV 60,60,61\nAW 119,60\nMvaa 92,248,128\nWL 0\nMvaa 64,160,192\nTa 224,36,0\nAR 60,108\nMvaa 64,164,196\nTa 226,36,2048\ns_add_u32 m0, s47, -49152\n"
"s_add_u32 s46, s52, 768\nD0 40,28,46,0\nMvaa 96,164,204\nAR 61,109\nMvaa 96,160,200\nTa 228,36,16384\nCV 60,60,61\nMvaa 128,160,208\nTa 230,36,18432\nMvaa 128,164,212\n"
"D0 41,28,46,1024\nMvaa 160,164,220\nTa 232,37,0\nAW 118,60\nMvaa 160,160,216\nTa 234,37,2048\nMvaa 164,168,216\nAR 60,106\nAR 61,107\nMvaa 164,172,220\nTa 236,37,16384\n"
"D0 42,28,46,2048\nMvaa 132,172,212\nTa 238,37,18432\nMvaa 132,168,208\nCV 60,60,61\nAW 117,60\nMvaa 100,168,200\nTa 240,38,0\nMvaa 100,172,204\nTa 242,38,2048\nD0 43,28,46,3072\n"
"Mvaa 68,172,196\ns_add_u32 s77, s77, s55\nSv 208,1,16,77,0\nMvaa 68,168,192\nTa 244,38,16384\nMvaa 72,176,192\nTa 246,38,18432\nAR 60,104\nMvaa 72,180,196\nAR 61,105\n"
"Mvaa 104,180,204\nTa 248,39,0\nMvaa 104,176,200\nTa 250,39,2048\nSv 212,1,16,77,1024\nMvaa 136,176,208\nCV 60,60,61\nMvaa 136,180,212\nTa 252,39,16384\nMvaa 168,180,220\n"
"Ta 254,39,18432\nAW 116,60\nMvaa 168,176,216\nAR 60,102\nMvaa 172,184,216\nSv 216,1,16,77,2048\nAR 61,103\nMvaa 172,188,220\nCV 60,60,61\nMvaa 140,188,212\nAW 115,60\n"
"Mvaa 140,184,208\nAR 60,100\nAR 61,101\nMvaa 108,184,200\nCV 60,60,61\nMvaa 108,188,204\nSv 220,1,16,77,3072\nMvaa 76,188,196\nAW 114,60\nAR 60,98\nMvaa 76,184,192\n"
"s_waitcnt vmcnt(25) lgkmcnt(0)\ns_barrier\nMvaa 80,224,192\nRa 128,44,0\nv_and_b32 v27, 0x70, v1\nbuffer_load_dword v27, v27, s[16:19], s61 offen offset:128 sc0 sc1\n"
"Mvaa 80,228,196\nRa 132,45,0\ns_add_u32 m0, s47, 65536\ns_add_u32 s46, s51, 3145728\nD0 6,32,46,0\nMvaa 112,228,204\nRa 136,44,2048\nMvaa 112,224,200\nRa 140,45,2048\nAR 61,99\n"
"Mvaa 144,224,208\nRa 144,46,0\nMvaa 144,228,212\nRa 148,47,0\nD0 7,32,46,1024\nMvaa 176,228,220\nRa 152,46,2048\nCV 60,60,61\nMvaa 176,224,216\nRa 156,47,2048\nMvaa 180,232,216\n"
"Ta 160,12,0\nAW 113,60\nMvaa 180,236,220\nTa 162,12,2048\nD0 8,32,46,2048\nMvaa 148,236,212\nTa 164,12,16384\nMvaa 148,232,208\nTa 166,12,18432\nAR 60,96\nMvaa 116,232,200\n"
"Ta 168,13,0\nMvaa 116,236,204\nTa 170,13,2048\nD0 9,32,46,3072\nMvaa 84,236,196\nTa 172,13,16384\nAR 61,97\nMvaa 84,232,192\nTa 174,13,18432\ns_add_u32 m0, s47, 81920\n"
"s_add_u32 s46, s51, 3407872\nMvaa 88,240,192\nTa 176,14,0\nCV 60,60,61\nMvaa 88,244,196\nTa 178,14,2048\nD0 6,32,46,0\nMvaa 120,244,204\nTa 180,14,16384\nMvaa 120,240,200\n"
"Ta 182,14,18432\nAW 112,60\nMvaa 152,240,208\nTa 184,15,0\nMvaa 152,244,212\nTa 186,15,2048\nD0 7,32,46,1024\nMvaa 184,244,220\nTa 188,15,16384\nAR 60,94\nMvaa 184,240,216\n"
"Ta 190,15,18432\nMvaa 188,248,216\nAR 61,95\nCV 60,60,61\nMvaa 188,252,220\nD0 8,32,46,2048\nMvaa 156,252,212\nAW 111,60\nMvaa 156,248,208\nAR 60,92\nAR 61,93\nMvaa 124,248,200\n"
"CV 60,60,61\nMvaa 124,252,204\nD0 9,32,46,3072\nMvaa 92,252,196\nAW 110,60\nAR 60,90\nMvaa 92,248,192\nWL 0\nMvaa 64,160,128\nTa 224,16,0\nAR 61,91\nMvaa 64,164,132\n"
"Ta 226,16,2048\ns_add_u32 s6, s56, 16384\nbuffer_load_dwordx4 v[224:227], v1, s[16:19], s6 offen offset:0 sc1\nMvaa 96,164,140\nCV 60,60,61\nMvaa 96,160,136\nTa 228,16,16384\n"
"AW 109,60\nMvaa 128,160,144\nTa 230,16,18432\nMvaa 128,164,148\nbuffer_load_dwordx4 v[228:231], v1, s[16:19], s6 offen offset:1024 sc1\nMvaa 160,164,156\nTa 232,17,0\nAR 60,88\n"
"Mvaa 160,160,152\nTa 234,17,2048\nMvaa 164,168,152\nAR 61,89\nCV 60,60,61\nMvaa 164,172,156\nTa 236,17,16384\n"
"buffer_load_dwordx4 v[232:235], v1, s[16:19], s6 offen offset:2048 sc1\nMvaa 132,172,148\nTa 238,17,18432\nMvaa 132,168,144\nAW 108,60\nAR 60,86\nMvaa 100,168,136\nTa 240,18,0\n"
"Mvaa 100,172,140\nTa 242,18,2048\nbuffer_load_dwordx4 v[236:239], v1, s[16:19], s6 offen offset:3072 sc1\nMvaa 68,172,132\nAR 61,87\nCV 60,60,61\nMvaa 68,168,128\nTa 244,18,16384\n"
"Mvaa 72,176,128\nTa 246,18,18432\nAW 107,60\nMvaa 72,180,132\nAR 60,84\nMvaa 104,180,140\nTa 248,19,0\nMvaa 104,176,136\nTa 250,19,2048\nAR 61,85\nMvaa 136,176,144\nCV 60,60,61\n"
"Mvaa 136,180,148\nTa 252,19,16384\nMvaa 168,180,156\nTa 254,19,18432\nAW 106,60\nMvaa 168,176,152\nAR 60,82\nMvaa 172,184,152\nAR 61,83\nCV 60,60,61\nMvaa 172,188,156\nAW 105,60\n"
"Mvaa 140,188,148\nAR 60,80\nMvaa 140,184,144\nAR 61,81\nCV 60,60,61\nMvaa 108,184,136\nAW 104,60\nMvaa 108,188,140\nAR 60,78\nMvaa 76,188,132\nAR 61,79\nCV 60,60,61\n"
"Mvaa 76,184,128\ns_waitcnt vmcnt(12) lgkmcnt(0)\ns_barrier\nMvaa 80,224,128\nRa 192,44,32768\nv_cmp_ne_u32 vcc, s15, v27\ns_nop 0\ns_cbranch_vccnz .S429\n.S430:\n"
"s_sub_u32 s45, s86, 131072\ns_cmp_eq_u32 s79, 1\ns_cselect_b32 s86, s94, s86\ns_add_u32 m0, s47, -65536\ns_add_u32 s46, s45, 0x0\nMvaa 80,228,132\nRa 196,45,32768\n"
"Mvaa 112,228,140\nRa 200,44,34816\nD1 1,16,46,0\nMvaa 112,224,136\nRa 204,45,34816\nAW 103,60\nMvaa 144,224,144\nRa 208,46,32768\nMvaa 144,228,148\nRa 212,47,32768\n"
"Mvaa 176,228,156\nRa 216,46,34816\nD1 1,16,46,1024\nMvaa 176,224,152\nRa 220,47,34816\nMvaa 180,232,152\nTa 160,12,32768\nAR 60,76\nMvaa 180,236,156\nTa 162,12,34816\n"
"Mvaa 148,236,148\nTa 164,12,49152\nD1 1,16,46,2048\nMvaa 148,232,144\nTa 166,12,51200\nAR 61,77\nMvaa 116,232,136\nTa 168,13,32768\nMvaa 116,236,140\nTa 170,13,34816\n"
"Mvaa 84,236,132\nTa 172,13,49152\nD1 1,16,46,3072\nMvaa 84,232,128\nTa 174,13,51200\nMvaa 88,240,128\nTa 176,14,32768\ns_add_u32 s6, s56, 20480\nMvaa 88,244,132\nTa 178,14,34816\n"
"Mvaa 120,244,140\nTa 180,14,49152\nbuffer_load_dwordx4 v[240:243], v1, s[16:19], s6 offen offset:0 sc1\nMvaa 120,240,136\nTa 182,14,51200\nCV 60,60,61\nMvaa 152,240,144\n"
"Ta 184,15,32768\nMvaa 152,244,148\nTa 186,15,34816\nMvaa 184,244,156\nTa 188,15,49152\nbuffer_load_dwordx4 v[244:247], v1, s[16:19], s6 offen offset:1024 sc1\nMvaa 184,240,152\n"
"Ta 190,15,51200\nMvaa 188,248,152\nAW 102,60\nAR 60,74\nMvaa 188,252,156\nAR 61,75\nMvaa 156,252,148\nbuffer_load_dwordx4 v[248:251], v1, s[16:19], s6 offen offset:2048 sc1\n"
"Mvaa 156,248,144\nCV 60,60,61\nAW 101,60\nMvaa 124,248,136\nAR 60,72\nMvaa 124,252,140\nAR 61,73\nMvaa 92,252,132\n"
"buffer_load_dwordx4 v[252:255], v1, s[16:19], s6 offen offset:3072 sc1\nCV 60,60,61\nMvaa 92,248,128\nWL 0\nMvaa 64,160,192\nTa 224,16,32768\ns_add_u32 s6, s56, 0\n"
"Mvaa 64,164,196\nTa 226,16,34816\nMvaa 96,164,204\nbuffer_load_dwordx4 v[192:195], v1, s[16:19], s6 offen offset:0 sc1\nMvaa 96,160,200\nTa 228,16,49152\nAW 100,60\n"
"Mvaa 128,160,208\nTa 230,16,51200\nMvaa 128,164,212\nAR 60,70\nMvaa 160,164,220\nTa 232,17,32768\nbuffer_load_dwordx4 v[196:199], v1, s[16:19], s6 offen offset:1024 sc1\n"
"Mvaa 160,160,216\nTa 234,17,34816\nMvaa 164,168,216\nAR 61,71\nCV 60,60,61\nMvaa 164,172,220\nTa 236,17,49152\nMvaa 132,172,212\nTa 238,17,51200\n"
"buffer_load_dwordx4 v[200:203], v1, s[16:19], s6 offen offset:2048 sc1\nMvaa 132,168,208\nAW 99,60\nAR 60,68\nMvaa 100,168,200\nTa 240,18,32768\nMvaa 100,172,204\nTa 242,18,34816\n"
"Mvaa 68,172,196\nbuffer_load_dwordx4 v[204:207], v1, s[16:19], s6 offen offset:3072 sc1\nAR 61,69\nMvaa 68,168,192\nTa 244,18,49152\nMvaa 72,176,192\nTa 246,18,51200\nCV 60,60,61\n"
"Mvaa 72,180,196\nAW 98,60\nMvaa 104,180,204\nTa 248,19,32768\nMvaa 104,176,200\nTa 250,19,34816\nAR 60,66\nMvaa 136,176,208\nAR 61,67\nMvaa 136,180,212\nTa 252,19,49152\n"
"Mvaa 168,180,220\nTa 254,19,51200\nCV 60,60,61\nMvaa 168,176,216\nAW 97,60\nMvaa 172,184,216\nAR 60,64\nAR 61,65\nMvaa 172,188,220\nCV 60,60,61\nMvaa 140,188,212\nAW 96,60\n"
"v_add_u32 v2, 0x0, v1\nMvaa 140,184,208\ns_lshr_b32 s2, s59, 1\nMvaa 108,184,200\nv_add_u32 v24, s2, v2\nv_xor_b32 v26, 0x1000, v24\nMvaa 108,188,204\nv_lshrrev_b32 v20, 4, v0\n"
"Mvaa 76,188,196\nv_lshlrev_b32 v20, 2, v20\nv_and_b32 v0, 15, v0\nMvaa 76,184,192\ns_waitcnt vmcnt(16) lgkmcnt(0)\ns_barrier\nMvaa 80,224,192\nRa 128,44,16384\n"
"v_sub_u32 v0, v0, v20\nMvaa 80,228,196\nRa 132,45,16384\nbuffer_store_dword v25, off, s[16:19], s54 offset:12 sc0 sc1\ns_add_u32 s6, s56, 4096\n"
"buffer_load_dwordx4 v[208:211], v1, s[16:19], s6 offen offset:0 sc1\nMvaa 112,228,204\nRa 136,44,18432\ns_lshl_b32 s2, s11, 4\nMvaa 112,224,200\nRa 140,45,18432\nMvaa 144,224,208\n"
"Ra 144,46,16384\nv_add_u32 v3, s2, v0\nMvaa 144,228,212\nRa 148,47,16384\nbuffer_load_dwordx4 v[212:215], v1, s[16:19], s6 offen offset:1024 sc1\nMvaa 176,228,220\n"
"Ra 152,46,18432\nv_subrev_u32 v0, 32, v3\nMvaa 176,224,216\nRa 156,47,18432\nMvaa 180,232,216\nTa 160,32,0\ns_lshl_b32 s2, s11, 13\nMvaa 180,236,220\nTa 162,32,2048\n"
"buffer_load_dwordx4 v[216:219], v1, s[16:19], s6 offen offset:2048 sc1\nMvaa 148,236,212\nTa 164,32,16384\ns_add_u32 s2, s2, 0x20000\nMvaa 148,232,208\nTa 166,32,18432\n"
"Mvaa 116,232,200\nTa 168,33,0\nv_add_u32 v22, s2, v1\nMvaa 116,236,204\nTa 170,33,2048\nbuffer_load_dwordx4 v[220:223], v1, s[16:19], s6 offen offset:3072 sc1\nMvaa 84,236,196\n"
"Ta 172,33,16384\nv_xor_b32 v23, 0x2000, v22\nMvaa 84,232,192\nTa 174,33,18432\ns_add_u32 m0, s47, -32768\ns_add_u32 s46, s45, 0x8000\nMvaa 88,240,192\nTa 176,34,0\n"
"v_mov_b32 v4, 0\nMvaa 88,244,196\nTa 178,34,2048\nD1 1,16,46,0\nMvaa 120,244,204\nTa 180,34,16384\nv_mov_b32 v5, 0\nMvaa 120,240,200\nTa 182,34,18432\nMvaa 152,240,208\n"
"Ta 184,35,0\nv_mov_b32 v6, 0\nMvaa 152,244,212\nTa 186,35,2048\nD1 1,16,46,1024\nMvaa 184,244,220\nTa 188,35,16384\nv_mov_b32 v7, 0\nMvaa 184,240,216\nTa 190,35,18432\n"
"Mvaa 188,248,216\nv_mov_b32 v8, 0\nv_mov_b32 v9, 0\nMvaa 188,252,220\nD1 1,16,46,2048\nMvaa 156,252,212\n.W591:\ns_cbranch_execz .W595\nv_mov_b32 v10, 0\nv_mov_b32 v11, 0\n"
"Mvaa 156,248,208\nv_mov_b32 v12, 0\nMvaa 124,248,200\nv_mov_b32 v13, 0\nv_mov_b32 v14, 0\nMvaa 124,252,204\nD1 1,16,46,3072\nMvaa 92,252,196\nv_mov_b32 v15, 0\nv_mov_b32 v16, 0\n"
"Mvaa 92,248,192\nWL 0\nMvaa 64,160,128\nTa 224,36,0\nv_mov_b32 v17, 0\nMvaa 64,164,132\nTa 226,36,2048\ns_add_u32 m0, s47, -16384\ns_add_u32 s46, s45, 0x400000\nD1 1,16,46,0\n"
"Mvaa 68,172,132\nv_mov_b32 v18, 0\nv_mov_b32 v19, 0\nMvaa 68,168,128\nTa 228,36,16384\nMvaa 72,176,128\nTa 230,36,18432\nMvaa 72,180,132\nD1 1,16,46,1024\nMvaa 76,188,132\n"
"Ta 232,37,0\nMvaa 76,184,128\nTa 234,37,2048\nMvaa 108,184,136\nv_mul_f32 v64, s57, v64\nv_mul_f32 v65, s57, v65\nMvaa 108,188,140\nTa 236,37,16384\nD1 1,16,46,2048\n"
"Mvaa 104,180,140\nTa 238,37,18432\nCV 64,64,65\nMvaa 104,176,136\nv_mul_f32 v66, s57, v66\nv_mul_f32 v67, s57, v67\nMvaa 100,168,136\nTa 240,38,0\nCV 65,66,67\nMvaa 100,172,140\n"
"Ta 242,38,2048\nD1 1,16,46,3072\nMvaa 96,164,140\nv_mul_f32 v68, s57, v68\nv_mul_f32 v69, s57, v69\nMvaa 96,160,136\nTa 244,38,16384\nCV 66,68,69\nMvaa 128,160,144\n"
"Ta 246,38,18432\nv_mul_f32 v70, s57, v70\nMvaa 128,164,148\nv_mul_f32 v71, s57, v71\nCV 67,70,71\nMvaa 132,172,148\nTa 248,39,0\nAW 192,64\nMvaa 132,168,144\nTa 250,39,2048\n"
"AW 193,65\nMvaa 136,176,144\nAW 194,66\nAW 195,67\nMvaa 136,180,148\nTa 252,39,16384\nv_mul_f32 v72, s57, v72\nMvaa 140,188,148\nTa 254,39,18432\nv_mul_f32 v73, s57, v73\n"
"Mvaa 140,184,144\nCV 72,72,73\nv_mul_f32 v74, s57, v74\nMvaa 172,184,152\nv_mul_f32 v75, s57, v75\nCV 73,74,75\nMvaa 172,188,156\nv_mul_f32 v76, s57, v76\nv_mul_f32 v77, s57, v77\n"
"Mvaa 168,180,156\nCV 74,76,77\nv_mul_f32 v78, s57, v78\nMvaa 168,176,152\nv_mul_f32 v79, s57, v79\nCV 75,78,79\nMvaa 164,168,152\nAW 200,72\nAW 201,73\nMvaa 164,172,156\n"
"AW 202,74\nAW 203,75\nMvaa 160,164,156\nv_mul_f32 v96, s57, v96\nv_mul_f32 v97, s57, v97\nMvaa 160,160,152\ns_waitcnt vmcnt(21) lgkmcnt(0)\ns_barrier\nMvaa 80,224,128\n"
"CV 96,96,97\nv_mul_f32 v98, s57, v98\nMvaa 80,228,132\ns_add_u32 m0, s47, 0\ns_add_u32 s46, s86, 0xffff0000\nD1 1,16,46,0\nMvaa 84,236,132\nv_mul_f32 v99, s57, v99\nCV 97,98,99\n"
"Mvaa 84,232,128\nv_mul_f32 v100, s57, v100\nv_mul_f32 v101, s57, v101\nMvaa 88,240,128\nCV 98,100,101\nv_mul_f32 v102, s57, v102\nMvaa 88,244,132\nD1 1,16,46,1024\n"
"v_mul_f32 v103, s57, v103\nMvaa 92,252,132\nCV 99,102,103\nAW 196,96\nMvaa 92,248,128\nAW 197,97\nAW 198,98\nMvaa 124,248,136\nAW 199,99\nv_mul_f32 v128, s57, v128\n"
"Mvaa 124,252,140\nD1 1,16,46,2048\nv_mul_f32 v129, s57, v129\nMvaa 120,244,140\nCV 128,128,129\nv_mul_f32 v130, s57, v130\nMvaa 120,240,136\nv_mul_f32 v131, s57, v131\n"
"CV 129,130,131\nMvaa 116,232,136\nRa 160,24,0\nv_mul_f32 v132, s57, v132\nMvaa 116,236,140\nD1 1,16,46,3072\nv_mul_f32 v133, s57, v133\nMvaa 112,228,140\nRa 164,24,1024\n"
"CV 130,132,133\nMvaa 112,224,136\ns_add_u32 m0, s47, 16384\ns_add_u32 s46, s45, 0x408000\nMvaa 144,224,144\nRa 168,24,2048\nv_mul_f32 v134, s57, v134\nMvaa 144,228,148\n"
"D1 1,16,46,0\nv_mul_f32 v135, s57, v135\nMvaa 148,236,148\nRa 172,24,3072\nCV 131,134,135\nMvaa 148,232,144\nds_write_b128 v23, v[128:131] offset:0\nv_mul_f32 v80, s57, v80\n"
"Mvaa 152,240,144\nRa 176,26,0\nv_mul_f32 v81, s57, v81\nMvaa 152,244,148\nD1 1,16,46,1024\nCV 80,80,81\nMvaa 156,252,148\nRa 180,26,1024\nv_mul_f32 v82, s57, v82\n"
"Mvaa 156,248,144\nv_mul_f32 v83, s57, v83\nCV 81,82,83\nMvaa 188,248,152\nRa 184,26,2048\nv_mul_f32 v84, s57, v84\nMvaa 188,252,156\nD1 1,16,46,2048\nv_mul_f32 v85, s57, v85\n"
"Mvaa 184,244,156\nRa 188,26,3072\nCV 82,84,85\nMvaa 184,240,152\nv_mul_f32 v86, s57, v86\nv_mul_f32 v87, s57, v87\nMvaa 180,232,152\nCV 83,86,87\nAW 208,80\nMvaa 180,236,156\n"
"D1 1,16,46,3072\nAW 209,81\nMvaa 176,228,156\nAW 210,82\nAW 211,83\nMvaa 176,224,152\ns_waitcnt vmcnt(16) lgkmcnt(5)\ns_cmp_eq_u32 s79, 1\ns_cbranch_scc0 .E432\nv_mov_b32 v4, s97\n"
"v_mov_b32 v5, s97\nv_mov_b32 v6, s97\nv_mov_b32 v7, s97\nv_mov_b32 v12, s97\nv_mov_b32 v13, s97\nv_mov_b32 v14, s97\nv_mov_b32 v15, s97\nv_cmpx_gt_i32 vcc, 19, v3\n"
"v_mov_b32 v19, s97\nv_cmpx_gt_i32 vcc, 18, v3\nv_mov_b32 v18, s97\nv_cmpx_gt_i32 vcc, 17, v3\nv_mov_b32 v17, s97\nv_cmpx_gt_i32 vcc, 16, v3\nv_mov_b32 v16, s97\n"
"v_cmpx_gt_i32 vcc, 3, v3\nv_mov_b32 v11, s97\nv_cmpx_gt_i32 vcc, 2, v3\nv_mov_b32 v10, s97\nv_cmpx_gt_i32 vcc, 1, v3\nv_mov_b32 v9, s97\nv_cmpx_gt_i32 vcc, 0, v3\n"
"v_mov_b32 v8, s97\ns_mov_b64 exec, -1\ns_nop 2\n.E432:\ns_nop 0\nCvav 32,160,192,4\nRa 128,24,8192\nv_mul_f32 v88, s57, v88\nMvav 32,164,200\nv_mul_f32 v89, s57, v89\nCV 88,88,89\n"
"Mvav 32,168,208\nRa 132,24,9216\nv_mul_f32 v90, s57, v90\nMvav 32,172,216\nv_mul_f32 v91, s57, v91\nCV 89,90,91\nCvav 36,172,220,8\nRa 136,24,10240\nv_mul_f32 v92, s57, v92\n"
"Mvav 36,168,212\ns_add_u32 m0, s89, 32768\nv_mul_f32 v93, s57, v93\nMvav 36,164,204\nRa 140,24,11264\nWL 4\ns_barrier\nMvav 36,160,196\nCV 90,92,93\nv_mul_f32 v94, s57, v94\n"
"Mvav 36,176,228\nRa 144,26,8192\nSa 96,1,16,78,0\nMvav 36,180,236\ns_add_u32 s46, s86, s88\nv_mul_f32 v95, s57, v95\nMvav 36,184,244\nRa 148,26,9216\nCV 91,94,95\nMvav 36,188,252\n"
"AW 216,88\nAW 217,89\nMvav 32,188,248\nRa 152,26,10240\nAW 218,90\nMvav 32,184,240\nD1 1,16,46,0\nAW 219,91\nMvav 32,180,232\nRa 156,26,11264\nWL 4\nMvav 32,176,224\n"
"v_mov_b32 v86, 0\nv_mov_b32 v87, 0\nCvav 40,128,192,12\nSa 100,1,16,78,1024\nv_mov_b32 v88, 0\nMvav 40,132,200\ns_cmp_eq_u32 s99, -1\nMvav 40,136,208\ns_cselect_b32 s87, s95, s87\n"
"v_mov_b32 v89, 0\nv_mul_f32 v136, s57, v136\nv_mul_f32 v137, s57, v137\nCV 136,136,137\nv_mul_f32 v138, s57, v138\nv_mul_f32 v139, s57, v139\nMvav 40,140,216\nCV 137,138,139\n"
"v_mul_f32 v140, s57, v140\nv_mul_f32 v141, s57, v141\nCV 138,140,141\nv_mul_f32 v142, s57, v142\nv_mul_f32 v143, s57, v143\nCvav 44,140,220,16\nv_and_b32 v27, 0x70, v1\n"
"buffer_load_dword v27, v27, s[16:19], s61 offen offset:136 sc0 sc1\nCV 139,142,143\nds_write_b128 v23, v[136:139] offset:2048\nv_mul_f32 v144, s57, v144\n"
"v_mul_f32 v145, s57, v145\nCV 144,144,145\nMvav 44,136,212\nD1 1,16,46,1024\nv_mul_f32 v146, s57, v146\nv_mul_f32 v147, s57, v147\nCV 145,146,147\nv_mul_f32 v148, s57, v148\n"
"v_mul_f32 v149, s57, v149\nMvav 44,132,204\nWL 1\nCV 146,148,149\nv_mul_f32 v150, s57, v150\nv_mul_f32 v151, s57, v151\nCV 147,150,151\nds_write_b128 v23, v[144:147] offset:4096\n"
"v_mul_f32 v152, s57, v152\nMvav 44,128,196\nv_mul_f32 v153, s57, v153\nCV 152,152,153\nv_mul_f32 v154, s57, v154\nv_mul_f32 v155, s57, v155\nCV 153,154,155\n"
"v_mul_f32 v156, s57, v156\nMvav 44,144,228\nSa 104,1,16,78,2048\nv_mul_f32 v157, s57, v157\nCV 154,156,157\nv_mul_f32 v158, s57, v158\nv_mul_f32 v159, s57, v159\nCV 155,158,159\n"
"ds_write_b128 v23, v[152:155] offset:6144\nMvav 44,148,236\ns_add_u32 s88, s88, 32768\nEX 80,32\nv_mul_f32 v160, s57, v160\nv_mul_f32 v161, s57, v161\nCV 160,160,161\n"
"Mvav 44,152,244\nv_mul_f32 v162, s57, v162\nv_mul_f32 v163, s57, v163\nCV 161,162,163\nv_mul_f32 v164, s57, v164\nv_mul_f32 v165, s57, v165\nCV 162,164,165\n"
"v_mul_f32 v166, s57, v166\nMvav 44,156,252\nEX 81,33\nv_mul_f32 v167, s57, v167\nCV 163,166,167\nds_write_b128 v23, v[160:163] offset:1024\nMvav 40,156,248\n"
"v_mul_f32 v168, s57, v168\nv_mul_f32 v169, s57, v169\nCV 168,168,169\nv_mul_f32 v170, s57, v170\nv_mul_f32 v171, s57, v171\nCV 169,170,171\nv_mul_f32 v172, s57, v172\n"
"Mvav 40,152,240\nD1 1,16,46,2048\nv_mul_f32 v173, s57, v173\nCV 170,172,173\nv_mul_f32 v174, s57, v174\nv_mul_f32 v175, s57, v175\nCV 171,174,175\nMvav 40,148,232\n"
"ds_write_b128 v23, v[168:171] offset:3072\nv_mul_f32 v176, s57, v176\nv_mul_f32 v177, s57, v177\nCV 176,176,177\nv_mul_f32 v178, s57, v178\nv_mul_f32 v179, s57, v179\n"
"CV 177,178,179\nMvav 40,144,224\nAD 84,80,81\nCV 64,80,81\nv_mul_f32 v180, s57, v180\nv_mul_f32 v181, s57, v181\nCV 178,180,181\nv_mul_f32 v182, s57, v182\nSa 108,1,16,78,3072\n"
"v_mul_f32 v183, s57, v183\nCV 179,182,183\nds_write_b128 v23, v[176:179] offset:5120\nv_mul_f32 v184, s57, v184\nv_mul_f32 v185, s57, v185\nCV 184,184,185\nEX 82,34\n"
"v_mul_f32 v186, s57, v186\nv_mul_f32 v187, s57, v187\nCV 185,186,187\nv_mul_f32 v188, s57, v188\nv_mul_f32 v189, s57, v189\nCV 186,188,189\nv_mul_f32 v190, s57, v190\n"
"v_mul_f32 v191, s57, v191\nCV 187,190,191\nds_write_b128 v23, v[184:187] offset:7168\ns_waitcnt vmcnt(16) lgkmcnt(0)\nEX 83,35\nAD 84,84,82\nD1 1,16,46,3072\n"
"s_add_u32 m0, s89, 49152\ns_barrier\nAD 84,84,83\nCV 65,82,83\nRa 128,24,32768\nRa 224,22,0\nRa 228,22,1024\nRa 232,22,2048\ns_add_u32 s46, s87, s88\nEX 80,36\nRa 132,24,33792\n"
"Ra 236,22,3072\nRa 240,22,4096\nEX 81,37\nRa 244,22,5120\nRa 136,24,34816\nRa 248,22,6144\nRa 252,22,7168\nD1 1,16,46,0\nv_mul_f32 v104, s57, v104\nv_mul_f32 v105, s57, v105\n"
"Ra 140,24,35840\nCV 104,104,105\nv_mul_f32 v106, s57, v106\nv_mul_f32 v107, s57, v107\nAD 85,80,81\nCV 68,80,81\nCV 105,106,107\nRa 144,26,32768\nv_mul_f32 v108, s57, v108\n"
"v_mul_f32 v109, s57, v109\nCV 106,108,109\ns_cmp_eq_u32 s13, 7\nEX 82,38\nRa 148,26,33792\ns_cbranch_scc0 .E419\nv_cmpx_gt_i32 vcc, 19, v0\nv_mov_b32 v19, s97\n"
"v_cmpx_gt_i32 vcc, 18, v0\nv_mov_b32 v18, s97\nv_cmpx_gt_i32 vcc, 17, v0\nv_mov_b32 v17, s97\nv_cmpx_gt_i32 vcc, 16, v0\nv_mov_b32 v16, s97\nv_cmpx_gt_i32 vcc, 3, v0\n"
"v_mov_b32 v11, s97\nv_cmpx_gt_i32 vcc, 2, v0\nv_mov_b32 v10, s97\nv_cmpx_gt_i32 vcc, 1, v0\nv_mov_b32 v9, s97\nv_cmpx_gt_i32 vcc, 0, v0\nv_mov_b32 v8, s97\ns_mov_b64 exec, -1\n"
"s_nop 2\n.E419:\nEX 83,39\nv_mul_f32 v110, s57, v110\nRa 152,26,34816\nAD 85,85,82\nv_mul_f32 v111, s57, v111\nD1 1,16,46,1024\nCV 107,110,111\nAW 204,104\nRa 156,26,35840\n"
"AW 205,105\nAW 206,106\nAW 207,107\nAD 85,85,83\nCV 69,82,83\nv_mul_f32 v112, s57, v112\nRa 160,24,40960\nv_mul_f32 v113, s57, v113\nCV 112,112,113\nv_mul_f32 v114, s57, v114\n"
"s_xor_b32 s89, s89, 0x10000\nEX 80,40\nRa 164,24,41984\nv_mul_f32 v115, s57, v115\nCV 113,114,115\nWL 6\nEX 81,41\nv_mul_f32 v116, s57, v116\nRa 168,24,43008\nAD 84,84,80\n"
"v_mul_f32 v117, s57, v117\nD1 1,16,46,2048\nCV 114,116,117\nv_mul_f32 v118, s57, v118\nRa 172,24,44032\nv_mul_f32 v119, s57, v119\nCV 115,118,119\nAW 212,112\nAD 84,84,81\n"
"CV 66,80,81\nAW 213,113\nCvav 48,128,192,4\nRa 176,26,40960\nAW 214,114\nAW 215,115\nv_mul_f32 v120, s57, v120\nMvav 48,132,200\nEX 82,42\nv_mul_f32 v121, s57, v121\n"
"Mvav 48,136,208\nRa 180,26,41984\nCV 120,120,121\nv_mul_f32 v122, s57, v122\nMvav 48,140,216\nWL 6\nEX 83,43\nWV 8\nv_cmp_ne_u32 vcc, s15, v27\ns_nop 0\ns_cbranch_vccnz .S433\n"
".S434:\nCvav 52,140,220,8\nRa 184,26,43008\nAD 84,84,82\nv_mul_f32 v123, s57, v123\nMvav 52,136,212\nD1 1,16,46,3072\ns_add_u32 m0, s89, 0\nCV 121,122,123\nMvav 52,132,204\n"
"Ra 188,26,44032\nv_mul_f32 v124, s57, v124\nv_mul_f32 v125, s57, v125\nCV 122,124,125\nMvav 52,128,196\nAD 84,84,83\nCV 67,82,83\nv_mul_f32 v126, s57, v126\nMvav 52,144,228\n"
"Ra 128,2,49152\ns_add_u32 s78, s78, s55\nSa 112,1,16,78,0\nv_mul_f32 v127, s57, v127\nMvav 52,148,236\ns_add_u32 s46, s86, s88\nEX 80,44\nMvav 52,152,244\nRa 132,2,50176\n"
"v_xor_b32 v24, 0x10000, v24\nCV 123,126,127\nMvav 52,156,252\nWL 6\nEX 81,45\nAW 220,120\nMvav 48,156,248\nRa 136,2,51200\nAD 85,85,80\nAW 221,121\nMvav 48,152,240\nD1 1,16,46,0\n"
"AW 222,122\nAW 223,123\nMvav 48,148,232\nRa 140,2,52224\nv_mov_b32 v112, -1\nv_mov_b32 v113, -1\nv_mov_b32 v114, -1\nMvav 48,144,224\nAD 85,85,81\nCV 70,80,81\nv_mov_b32 v115, -1\n"
"Cvav 56,160,192,12\nRa 144,2,53248\nSa 116,1,16,78,1024\nMvav 56,164,200\nEX 82,46\nMvav 56,168,208\nRa 148,2,54272\nv_xor_b32 v26, 0x10000, v26\nMvav 56,172,216\nWL 6\nEX 83,47\n"
"Cvav 60,172,220,16\nRa 152,2,55296\nAD 85,85,82\nMvav 60,168,212\nD1 1,16,46,1024\nMvav 60,164,204\nRa 156,2,56320\nMvav 60,160,196\nAD 85,85,83\nCV 71,82,83\nMvav 60,176,228\n"
"Ra 160,2,57344\nv_max_f32 v90, v84, v85\nMvav 60,180,236\ns_add_u32 s88, s88, 32768\nEX 80,48\nMvav 60,184,244\nRa 164,2,58368\nv_cmp_lt_f32 vcc, s63, v90\nMvav 60,188,252\nWL 8\n"
"EX 81,49\nMvav 56,188,248\nRa 168,2,59392\ns_cbranch_vccnz .T421\n.T422:\nv_add_f32 v20, s93, v84\nv_mov_b32 v21, v85\nv_mov_b32 v22, v20\nv_mov_b32 v23, v21\nMvav 56,184,240\n"
"D1 1,16,46,2048\nv_permlane32_swap_b32 v20, v22\nv_permlane32_swap_b32 v21, v23\nAD 20,20,22\nAD 21,21,23\nv_mov_b32 v22, v20\nv_mov_b32 v23, v21\nMvav 56,180,232\nRa 172,2,60416\n"
"v_permlane16_swap_b32 v20, v22\nv_permlane16_swap_b32 v21, v23\nAD 20,20,22\nAD 21,21,23\nv_min_f32 v90, v20, v21\nv_cmp_gt_f32 vcc, s64, v90\ns_nop 0\ns_cbranch_vccnz .T423\n"
".T424:\nAD 86,86,84\nMvav 56,176,224\nAD 84,80,81\nCV 72,80,81\nZaav 0,128,64\nRa 176,2,61440\nSa 120,1,16,78,2048\nZaav 4,128,68\nEX 82,50\nZaav 12,132,68\nRa 180,2,62464\n"
"Zaav 8,132,64\nWL 8\nEX 83,51\nZaav 16,136,64\nRa 184,2,63488\nAD 84,84,82\nZaav 20,136,68\nD1 1,16,46,3072\ns_add_u32 m0, s89, 16384\nZaav 28,140,68\n.W592:\n"
"s_cbranch_execz .W596\nRa 188,2,64512\ns_barrier\nZaav 24,140,64\nAD 84,84,83\nCV 73,82,83\nZaav 32,144,64\nRa 128,24,0\nSa 124,1,16,78,3072\nZaav 36,144,68\n"
"s_add_u32 s46, s87, s88\nEX 80,52\nZaav 44,148,68\nRa 132,24,1024\nv_xor_b32 v2, 0x10000, v2\nZaav 40,148,64\nWL 8\nEX 81,53\nZaav 48,152,64\nRa 136,24,2048\nAD 87,87,85\n"
"Zaav 52,152,68\nD1 1,16,46,0\nZaav 60,156,68\nRa 140,24,3072\nZaav 56,156,64\nAD 85,80,81\nCV 76,80,81\nZaav 64,160,64\nRa 144,26,0\nZaav 68,160,68\nEX 82,54\nZaav 76,164,68\n"
"Ra 148,26,1024\nZaav 72,164,64\nWL 8\nEX 83,55\nZaav 80,168,64\nRa 152,26,2048\nAD 85,85,82\nZaav 84,168,68\nD1 1,16,46,1024\nZaav 92,172,68\nRa 156,26,3072\nZaav 88,172,64\n"
"AD 85,85,83\nCV 77,82,83\nZaav 96,176,64\nRa 160,24,8192\nZaav 100,176,68\nEX 80,56\nZaav 108,180,68\nRa 164,24,9216\nZaav 104,180,64\nWL 6\nEX 81,57\nZaav 112,184,64\n"
"Ra 168,24,10240\nAD 84,84,80\nv_and_b32 v27, 0x70, v1\nbuffer_load_dword v27, v27, s[16:19], s61 offen offset:140 sc0 sc1\nZaav 116,184,68\nD1 1,16,46,2048\nZaav 124,188,68\n"
"Ra 172,24,11264\nZaav 120,188,64\nAD 84,84,81\nCV 74,80,81\ns_sub_u32 s99, s99, 1\ns_cmp_eq_u32 s13, 7\ns_cbranch_scc1 .SW417\n.p2align 6\n.A448:\nCvav 32,128,192,4\n"
"Ra 176,26,8192\nMvav 32,132,200\nEX 82,58\nMvav 32,136,208\nRa 180,26,9216\nMvav 32,140,216\nWL 6\nEX 83,59\nCvav 36,140,220,8\nRa 184,26,10240\nAD 84,84,82\nMvav 36,136,212\n"
"D1 1,16,46,3072\ns_add_u32 m0, s89, 32768\nMvav 36,132,204\nRa 188,26,11264\nMvav 36,128,196\nAD 84,84,83\nCV 75,82,83\nMvav 36,144,228\nRa 128,2,16384\nMvav 36,148,236\n"
"s_add_u32 s46, s86, s88\nEX 80,60\nMvav 36,152,244\nRa 132,2,17408\nMvav 36,156,252\nWL 6\nEX 81,61\nMvav 32,156,248\nRa 136,2,18432\nAD 85,85,80\nMvav 32,152,240\nD1 1,16,46,0\n"
"Mvav 32,148,232\nRa 140,2,19456\nSv 112,1,16,49,0\nMvav 32,144,224\nAD 85,85,81\nCV 78,80,81\nCvav 40,160,192,12\nRa 144,2,20480\nMvav 40,164,200\nEX 82,62\nMvav 40,168,208\n"
"Ra 148,2,21504\nMvav 40,172,216\nWL 6\nEX 83,63\nCvav 44,172,220,16\nRa 152,2,22528\nAD 85,85,82\nMvav 44,168,212\nD1 1,16,46,1024\nMvav 44,164,204\nRa 156,2,23552\n"
"Mvav 44,160,196\nAD 85,85,83\nCV 79,82,83\nMvav 44,176,228\nRa 160,2,24576\nv_max_f32 v90, v84, v85\nMvav 44,180,236\ns_add_u32 s88, s88, 32768\nEX 80,32\nMvav 44,184,244\n"
"Ra 164,2,25600\nv_cmp_lt_f32 vcc, s63, v90\nMvav 44,188,252\nWL 8\nEX 81,33\nMvav 40,188,248\nRa 168,2,26624\ns_cbranch_vccnz .T456\n.T457:\nMvav 40,184,240\nD1 1,16,46,2048\n"
"Mvav 40,180,232\nRa 172,2,27648\nAD 86,86,84\nMvav 40,176,224\nAD 84,80,81\nCV 64,80,81\nMaav 0,128,72\nRa 176,2,28672\nMaav 4,128,76\nEX 82,34\nMaav 12,132,76\nRa 180,2,29696\n"
"Maav 8,132,72\ns_waitcnt vmcnt(13) lgkmcnt(8)\nEX 83,35\nMaav 16,136,72\nRa 184,2,30720\nAD 84,84,82\nMaav 20,136,76\nD1 1,16,46,3072\ns_add_u32 m0, s89, 49152\nMaav 28,140,76\n"
"Ra 188,2,31744\ns_barrier\nMaav 24,140,72\nAD 84,84,83\nCV 65,82,83\nMaav 32,144,72\nRa 128,24,32768\nMaav 36,144,76\ns_add_u32 s46, s87, s88\nEX 80,36\nMaav 44,148,76\n"
"Ra 132,24,33792\nMaav 40,148,72\nWL 8\nEX 81,37\nMaav 48,152,72\nRa 136,24,34816\nAD 87,87,85\nMaav 52,152,76\nD1 1,16,46,0\nMaav 60,156,76\nRa 140,24,35840\nMaav 56,156,72\n"
"AD 85,80,81\nCV 68,80,81\nMaav 64,160,72\nRa 144,26,32768\nMaav 68,160,76\ns_cmp_eq_u32 s99, 0\nEX 82,38\nMaav 76,164,76\nRa 148,26,33792\ns_cbranch_scc1 .E450\n.E451:\n"
"Maav 72,164,72\nWL 8\nEX 83,39\nMaav 80,168,72\nRa 152,26,34816\nAD 85,85,82\nMaav 84,168,76\nD1 1,16,46,1024\nMaav 92,172,76\nRa 156,26,35840\nMaav 88,172,72\nAD 85,85,83\n"
"CV 69,82,83\nMaav 96,176,72\nRa 160,24,40960\nMaav 100,176,76\ns_xor_b32 s89, s89, 0x10000\nEX 80,40\nMaav 108,180,76\nRa 164,24,41984\nMaav 104,180,72\nWL 6\nEX 81,41\n"
"Maav 112,184,72\nRa 168,24,43008\nAD 84,84,80\nMaav 116,184,76\nD1 1,16,46,2048\nMaav 124,188,76\nRa 172,24,44032\nMaav 120,188,72\nAD 84,84,81\nCV 66,80,81\nCvav 48,128,192,4\n"
"Ra 176,26,40960\nMvav 48,132,200\ns_cmp_eq_u32 s99, 0\nEX 82,42\nMvav 48,136,208\nRa 180,26,41984\ns_cselect_b32 s86, s94, s86\nMvav 48,140,216\nWL 6\nEX 83,43\nCvav 52,140,220,8\n"
"Ra 184,26,43008\nAD 84,84,82\nMvav 52,136,212\nD1 1,16,46,3072\ns_add_u32 m0, s89, 0\nMvav 52,132,204\nRa 188,26,44032\nMvav 52,128,196\nAD 84,84,83\nCV 67,82,83\nMvav 52,144,228\n"
"Ra 128,2,49152\nMvav 52,148,236\ns_add_u32 s46, s86, s88\nEX 80,44\nMvav 52,152,244\nRa 132,2,50176\nv_xor_b32 v24, 0x10000, v24\nMvav 52,156,252\nWL 6\nEX 81,45\nMvav 48,156,248\n"
"Ra 136,2,51200\nAD 85,85,80\nMvav 48,152,240\nD1 1,16,46,0\nMvav 48,148,232\nRa 140,2,52224\nSv 112,1,16,49,1024\nMvav 48,144,224\nAD 85,85,81\nCV 70,80,81\nCvav 56,160,192,12\n"
"Ra 144,2,53248\nMvav 56,164,200\ns_add_u32 s49, s49, 131072\nEX 82,46\nMvav 56,168,208\nRa 148,2,54272\nv_xor_b32 v26, 0x10000, v26\nMvav 56,172,216\nWL 6\nEX 83,47\n"
"Cvav 60,172,220,16\nRa 152,2,55296\nAD 85,85,82\nMvav 60,168,212\nD1 1,16,46,1024\nMvav 60,164,204\nRa 156,2,56320\nMvav 60,160,196\nAD 85,85,83\nCV 71,82,83\nMvav 60,176,228\n"
"Ra 160,2,57344\nv_max_f32 v90, v84, v85\nMvav 60,180,236\ns_add_u32 s88, s88, 32768\nEX 80,48\nMvav 60,184,244\nRa 164,2,58368\nv_cmp_lt_f32 vcc, s63, v90\nMvav 60,188,252\nWL 8\n"
"EX 81,49\nMvav 56,188,248\nRa 168,2,59392\ns_cbranch_vccnz .T458\n.T459:\nMvav 56,184,240\nD1 1,16,46,2048\nMvav 56,180,232\nRa 172,2,60416\nAD 86,86,84\nMvav 56,176,224\n"
"AD 84,80,81\nCV 72,80,81\nMaav 0,128,64\nRa 176,2,61440\nMaav 4,128,68\nEX 82,50\nMaav 12,132,68\nRa 180,2,62464\nMaav 8,132,64\ns_waitcnt vmcnt(13) lgkmcnt(8)\nEX 83,51\n"
"Maav 16,136,64\nRa 184,2,63488\nAD 84,84,82\nMaav 20,136,68\nD1 1,16,46,3072\ns_add_u32 m0, s89, 16384\nMaav 28,140,68\nRa 188,2,64512\ns_barrier\nMaav 24,140,64\nAD 84,84,83\n"
"CV 73,82,83\nMaav 32,144,64\nRa 128,24,0\nMaav 36,144,68\ns_add_u32 s46, s87, s88\nEX 80,52\nMaav 44,148,68\nRa 132,24,1024\nv_xor_b32 v2, 0x10000, v2\nMaav 40,148,64\nWL 8\n"
"EX 81,53\nMaav 48,152,64\nRa 136,24,2048\nAD 87,87,85\nMaav 52,152,68\nD1 1,16,46,0\nMaav 60,156,68\nRa 140,24,3072\nMaav 56,156,64\nAD 85,80,81\nCV 76,80,81\nMaav 64,160,64\n"
"Ra 144,26,0\nMaav 68,160,68\ns_cmp_lt_u32 s99, s92\nEX 82,54\nMaav 76,164,68\nRa 148,26,1024\ns_cbranch_scc1 .E452\n.E453:\nMaav 72,164,64\nWL 8\nEX 83,55\nMaav 80,168,64\n"
"Ra 152,26,2048\nAD 85,85,82\nMaav 84,168,68\nD1 1,16,46,1024\nMaav 92,172,68\nRa 156,26,3072\nMaav 88,172,64\nAD 85,85,83\nCV 77,82,83\nMaav 96,176,64\nRa 160,24,8192\n"
"Maav 100,176,68\ns_sub_u32 s90, s90, 1\nEX 80,56\nMaav 108,180,68\nRa 164,24,9216\ns_cbranch_scc1 .E454\n.E455:\nMaav 104,180,64\nWL 6\nEX 81,57\nMaav 112,184,64\nRa 168,24,10240\n"
"AD 84,84,80\nMaav 116,184,68\nD1 1,16,46,2048\ns_sub_u32 s99, s99, 1\nMaav 124,188,68\nRa 172,24,11264\nMaav 120,188,64\nAD 84,84,81\nCV 74,80,81\ns_cbranch_scc0 .A448\n"
"s_branch .A460\n.E450:\nv_cmpx_gt_i32 vcc, 19, v0\nv_mov_b32 v15, s97\nv_cmpx_gt_i32 vcc, 18, v0\nv_mov_b32 v14, s97\nv_cmpx_gt_i32 vcc, 17, v0\nv_mov_b32 v13, s97\n"
"v_cmpx_gt_i32 vcc, 16, v0\nv_mov_b32 v12, s97\nv_cmpx_gt_i32 vcc, 3, v0\nv_mov_b32 v7, s97\nv_cmpx_gt_i32 vcc, 2, v0\nv_mov_b32 v6, s97\nv_cmpx_gt_i32 vcc, 1, v0\n"
"v_mov_b32 v5, s97\nv_cmpx_gt_i32 vcc, 0, v0\nv_mov_b32 v4, s97\ns_mov_b64 exec, -1\ns_nop 2\ns_branch .E451\n.E452:\ns_cmp_eq_u32 s99, 0\ns_cbranch_scc1 .K449\n"
"v_cmpx_gt_i32 vcc, 19, v3\nv_mov_b32 v15, s97\nv_cmpx_gt_i32 vcc, 18, v3\nv_mov_b32 v14, s97\nv_cmpx_gt_i32 vcc, 17, v3\nv_mov_b32 v13, s97\nv_cmpx_gt_i32 vcc, 16, v3\n"
"v_mov_b32 v12, s97\nv_cmpx_gt_i32 vcc, 3, v3\nv_mov_b32 v7, s97\nv_cmpx_gt_i32 vcc, 2, v3\nv_mov_b32 v6, s97\nv_cmpx_gt_i32 vcc, 1, v3\nv_mov_b32 v5, s97\n"
"v_cmpx_gt_i32 vcc, 0, v3\nv_mov_b32 v4, s97\ns_mov_b64 exec, -1\ns_nop 2\ns_branch .E453\n.K449:\nv_cmpx_gt_i32 vcc, 19, v3\nv_mov_b32 v19, s97\nv_cmpx_gt_i32 vcc, 18, v3\n"
"v_mov_b32 v18, s97\nv_cmpx_gt_i32 vcc, 17, v3\nv_mov_b32 v17, s97\nv_cmpx_gt_i32 vcc, 16, v3\nv_mov_b32 v16, s97\nv_cmpx_gt_i32 vcc, 3, v3\nv_mov_b32 v11, s97\n"
"v_cmpx_gt_i32 vcc, 2, v3\nv_mov_b32 v10, s97\nv_cmpx_gt_i32 vcc, 1, v3\nv_mov_b32 v9, s97\nv_cmpx_gt_i32 vcc, 0, v3\nv_mov_b32 v8, s97\ns_mov_b64 exec, -1\ns_nop 2\n"
"s_branch .E453\n.E454:\ns_swappc_b64 s[80:81], s[8:9]\ns_branch .E455\n.HD411:\nv_cmp_ne_u32 vcc, s15, v27\ns_nop 0\ns_cbranch_vccnz .S436\n.S437:\n"
"buffer_store_dword v25, off, s[16:19], s54 offset:4 sc0 sc1\nv_and_b32 v27, 0x70, v1\nbuffer_load_dword v27, v27, s[16:19], s61 offen offset:12 sc0 sc1\ns_mov_b32 s90, 1\n"
"s_swappc_b64 s[8:9], s[80:81]\nv_cmp_ne_u32 vcc, s15, v27\ns_nop 0\ns_cbranch_vccnz .S439\n.S440:\nv_and_b32 v27, 0x70, v1\n"
"buffer_load_dword v27, v27, s[16:19], s61 offen offset:8 sc0 sc1\ns_mov_b32 s90, 1\ns_swappc_b64 s[8:9], s[80:81]\nv_cmp_ne_u32 vcc, s15, v27\ns_nop 0\ns_cbranch_vccnz .S442\n"
".S443:\nv_and_b32 v27, 0xf0, v1\nbuffer_load_dword v27, v27, s[16:19], s61 offen offset:4 sc0 sc1\ns_mov_b32 s90, 1\ns_swappc_b64 s[8:9], s[80:81]\nv_cmp_ne_u32 vcc, s15, v27\n"
"s_nop 0\ns_cbranch_vccnz .S445\n.S446:\ns_mov_b32 s90, 100\ns_swappc_b64 s[8:9], s[80:81]\n.A460:\nRa 176,26,8192\nEX 82,58\nRa 180,26,9216\nWL 6\nEX 83,59\nCvav 36,140,220,8\n"
"Ra 184,26,10240\nAD 84,84,82\nMvav 36,136,212\nD1 1,16,46,3072\ns_add_u32 m0, s89, 32768\nMvav 36,132,204\nRa 188,26,11264\nMvav 36,128,196\nAD 84,84,83\nCV 75,82,83\n"
"Mvav 36,144,228\nRa 128,2,16384\nMvav 36,148,236\ns_add_u32 s46, s86, s88\nEX 80,60\nMvav 36,152,244\nRa 132,2,17408\nMvav 36,156,252\nEX 81,61\nRa 136,2,18432\nAD 85,85,80\n"
"D1 1,16,46,0\nRa 140,2,19456\nSv 112,1,16,49,0\nAD 85,85,81\nCV 78,80,81\nRa 144,2,20480\ns_mov_b32 s87, s95\nEX 82,62\nRa 148,2,21504\nWL 6\nEX 83,63\nCvav 44,172,220,16\n"
"Ra 152,2,22528\nAD 85,85,82\nMvav 44,168,212\nD1 1,16,46,1024\nMvav 44,164,204\nRa 156,2,23552\nMvav 44,160,196\nAD 85,85,83\nCV 79,82,83\nMvav 44,176,228\nRa 160,2,24576\n"
"v_max_f32 v90, v84, v85\nMvav 44,180,236\ns_add_u32 s88, s88, 32768\nMvav 44,184,244\nRa 164,2,25600\nv_cmp_lt_f32 vcc, s63, v90\nMvav 44,188,252\nWL 8\nRa 168,2,26624\n"
"s_cbranch_vccnz .T462\n.T463:\nD1 1,16,46,2048\nRa 172,2,27648\nAD 86,86,84\nMaav 0,128,72\nRa 176,2,28672\nMaav 4,128,76\nMaav 12,132,76\nRa 180,2,29696\nMaav 8,132,72\n"
"s_waitcnt vmcnt(13) lgkmcnt(8)\nMaav 16,136,72\nRa 184,2,30720\nMaav 20,136,76\nD1 1,16,46,3072\ns_add_u32 m0, s89, 49152\nMaav 28,140,76\nRa 188,2,31744\ns_barrier\n"
"Maav 24,140,72\nMaav 32,144,72\nRa 128,24,32768\nMaav 36,144,76\ns_add_u32 s46, s87, s88\nEX 80,36\nMaav 44,148,76\nRa 132,24,33792\nMaav 40,148,72\nWL 8\nEX 81,37\n"
"Maav 48,152,72\nRa 136,24,34816\nAD 87,87,85\nMaav 52,152,76\nD1 1,16,46,0\nMaav 60,156,76\nRa 140,24,35840\nMaav 56,156,72\nAD 85,80,81\nCV 68,80,81\nMaav 64,160,72\n"
"Ra 144,26,32768\nMaav 68,160,76\ns_nop 0\nEX 82,38\nMaav 76,164,76\nRa 148,26,33792\nv_cmpx_gt_i32 vcc, 19, v0\nv_mov_b32 v19, s97\nv_cmpx_gt_i32 vcc, 18, v0\nv_mov_b32 v18, s97\n"
"v_cmpx_gt_i32 vcc, 17, v0\nv_mov_b32 v17, s97\nv_cmpx_gt_i32 vcc, 16, v0\nv_mov_b32 v16, s97\nv_cmpx_gt_i32 vcc, 3, v0\nv_mov_b32 v11, s97\nv_cmpx_gt_i32 vcc, 2, v0\n"
"v_mov_b32 v10, s97\nv_cmpx_gt_i32 vcc, 1, v0\nv_mov_b32 v9, s97\nv_cmpx_gt_i32 vcc, 0, v0\nv_mov_b32 v8, s97\ns_mov_b64 exec, -1\ns_nop 2\ns_nop 0\nMaav 72,164,72\nWL 8\nEX 83,39\n"
"Maav 80,168,72\nRa 152,26,34816\nAD 85,85,82\nMaav 84,168,76\nD1 1,16,46,1024\nMaav 92,172,76\nRa 156,26,35840\nMaav 88,172,72\nAD 85,85,83\nCV 69,82,83\nMaav 96,176,72\n.W593:\n"
"s_cbranch_execz .W597\nRa 160,24,40960\nMaav 100,176,76\ns_xor_b32 s89, s89, 0x10000\nMaav 108,180,76\nRa 164,24,41984\nMaav 104,180,72\nWL 10\nMaav 112,184,72\nRa 168,24,43008\n"
"Maav 116,184,76\nD1 1,16,46,2048\nMaav 124,188,76\nRa 172,24,44032\nMaav 120,188,72\nRa 176,26,40960\nRa 180,26,41984\nWL 6\nCvav 52,140,220,8\nRa 184,26,43008\nMvav 52,136,212\n"
"D1 1,16,46,3072\ns_add_u32 m0, s89, 0\nMvav 52,132,204\nRa 188,26,44032\nMvav 52,128,196\nMvav 52,144,228\nRa 128,2,49152\nMvav 52,148,236\ns_add_u32 s46, s86, s88\nEX 80,44\n"
"Mvav 52,152,244\nRa 132,2,50176\nv_xor_b32 v24, 0x10000, v24\nMvav 52,156,252\nEX 81,45\nRa 136,2,51200\nAD 85,85,80\nD1 1,16,46,0\nRa 140,2,52224\nSv 112,1,16,49,1024\n"
"AD 85,85,81\nCV 70,80,81\nRa 144,2,53248\ns_add_u32 s49, s49, 131072\nEX 82,46\nRa 148,2,54272\nv_xor_b32 v26, 0x10000, v26\nWL 6\nEX 83,47\nCvav 60,172,220,16\nRa 152,2,55296\n"
"AD 85,85,82\nMvav 60,168,212\nD1 1,16,46,1024\nMvav 60,164,204\nRa 156,2,56320\nMvav 60,160,196\nAD 85,85,83\nCV 71,82,83\nMvav 60,176,228\nRa 160,2,57344\nMvav 60,180,236\n"
"s_add_u32 s88, s88, 32768\nMvav 60,184,244\nRa 164,2,58368\nv_cmp_lt_f32 vcc, s63, v85\nMvav 60,188,252\nWL 8\nRa 168,2,59392\ns_cbranch_vccnz .T464\n.T465:\nD1 1,16,46,2048\n"
"Ra 172,2,60416\nRa 176,2,61440\nMaav 4,128,68\nMaav 12,132,68\nRa 180,2,62464\ns_waitcnt vmcnt(13) lgkmcnt(8)\nRa 184,2,63488\nMaav 20,136,68\nD1 1,16,46,3072\n"
"s_add_u32 m0, s89, 16384\nMaav 28,140,68\nRa 188,2,64512\ns_barrier\nRa 128,24,0\nMaav 36,144,68\ns_add_u32 s46, s87, s88\nEX 80,52\nMaav 44,148,68\nRa 132,24,1024\n"
"v_xor_b32 v2, 0x10000, v2\nWL 8\nEX 81,53\nRa 136,24,2048\nAD 87,87,85\nMaav 52,152,68\nD1 1,16,46,0\nMaav 60,156,68\nRa 140,24,3072\nAD 85,80,81\nCV 76,80,81\nRa 144,26,0\n"
"Maav 68,160,68\nEX 82,54\nMaav 76,164,68\nRa 148,26,1024\nWL 8\nEX 83,55\nRa 152,26,2048\nAD 85,85,82\nMaav 84,168,68\nD1 1,16,46,1024\nMaav 92,172,68\nRa 156,26,3072\n"
"AD 85,85,83\nCV 77,82,83\nRa 160,24,8192\nMaav 100,176,68\ns_sub_u32 s90, s90, 1\nMaav 108,180,68\nRa 164,24,9216\nWL 6\nRa 168,24,10240\nMaav 116,184,68\nD1 1,16,46,2048\n"
"Maav 124,188,68\nRa 172,24,11264\n.SW417:\nv_mov_b32 v4, 0\nv_mov_b32 v5, 0\nv_mov_b32 v6, 0\nv_mov_b32 v7, 0\nv_mov_b32 v8, 0\nv_mov_b32 v9, 0\nv_mov_b32 v10, 0\n"
"v_mov_b32 v11, 0\nv_mov_b32 v12, 0\nv_mov_b32 v13, 0\nv_mov_b32 v14, 0\nv_mov_b32 v15, 0\nv_mov_b32 v16, 0\nv_mov_b32 v17, 0\nv_mov_b32 v18, 0\nv_mov_b32 v19, 0\n"
"s_add_u32 s99, s98, 1\ns_mov_b32 s94, s96\ns_add_u32 s95, s96, 65536\nCvaa 32,128,192,4\nRa 176,26,8192\nMvaa 32,132,200\nMvaa 32,136,208\nRa 180,26,9216\nMvaa 32,140,216\nWL 6\n"
"Cvaa 36,140,220,8\nRa 184,26,10240\nMvaa 36,136,212\nD1 1,16,46,3072\ns_add_u32 m0, s89, 32768\nMvaa 36,132,204\nRa 188,26,11264\nMvaa 36,128,196\nMvaa 36,144,228\nRa 128,2,16384\n"
"Mvaa 36,148,236\ns_add_u32 s46, s86, s88\nEX 80,60\nMvaa 36,152,244\nRa 132,2,17408\nMvaa 36,156,252\nWL 6\nEX 81,61\nMvaa 32,156,248\nRa 136,2,18432\nAD 85,85,80\n"
"Mvaa 32,152,240\nD1 1,16,46,0\nMvaa 32,148,232\nRa 140,2,19456\nMvaa 32,144,224\nAD 85,85,81\nCV 78,80,81\nCvaa 40,160,192,12\nRa 144,2,20480\nMvaa 40,164,200\nEX 82,62\n"
"Mvaa 40,168,208\nRa 148,2,21504\nMvaa 40,172,216\nWL 6\nEX 83,63\nCvaa 44,172,220,16\nRa 152,2,22528\nAD 85,85,82\nMvaa 44,168,212\nD1 1,16,46,1024\nMvaa 44,164,204\n"
"Ra 156,2,23552\nMvaa 44,160,196\nAD 85,85,83\nCV 79,82,83\nMvaa 44,176,228\nRa 160,2,24576\nMvaa 44,180,236\ns_add_u32 s88, s88, 32768\nEX 80,32\nMvaa 44,184,244\nRa 164,2,25600\n"
"v_cmp_lt_f32 vcc, s63, v85\nMvaa 44,188,252\nWL 8\nEX 81,33\nMvaa 40,188,248\nRa 168,2,26624\ns_cbranch_vccnz .T468\n.T469:\nMvaa 40,184,240\nD1 1,16,46,2048\nMvaa 40,180,232\n"
"Ra 172,2,27648\nMvaa 40,176,224\nAD 84,80,81\nCV 64,80,81\nRa 176,2,28672\nMaav 4,128,76\nEX 82,34\nMaav 12,132,76\nRa 180,2,29696\ns_waitcnt vmcnt(12) lgkmcnt(8)\nEX 83,35\n"
"Ra 184,2,30720\nAD 84,84,82\nMaav 20,136,76\nD1 1,16,46,3072\ns_add_u32 m0, s89, 49152\nMaav 28,140,76\nRa 188,2,31744\ns_barrier\nAR 98,0\nAR 99,1\nCV 100,98,99\nAD 84,84,83\n"
"CV 65,82,83\nAR 98,2\nAR 99,3\nCV 101,98,99\nRa 128,24,32768\nAR 98,8\nAR 99,9\nCV 102,98,99\nAR 98,10\nMaav 36,144,76\ns_add_u32 s46, s87, s88\nEX 80,36\nAR 99,11\n"
"Maav 44,148,76\nRa 132,24,33792\nCV 103,98,99\ns_add_u32 s5, s66, 0\nSv 100,1,16,5,0\nAR 98,4\nWL 8\nEX 81,37\nAR 99,5\nCV 104,98,99\nRa 136,24,34816\nAD 87,87,85\nAR 98,6\n"
"AR 99,7\nCV 105,98,99\nMaav 52,152,76\nD1 1,16,46,0\nv_mov_b32 v96, v86\nAR 98,12\nAR 99,13\nMaav 60,156,76\nRa 140,24,35840\nv_mov_b32 v97, v87\nCV 106,98,99\nAR 98,14\nAR 99,15\n"
"AD 85,80,81\nCV 68,80,81\nCV 107,98,99\ns_add_u32 s5, s66, 0\nSv 104,1,16,5,1024\nRa 144,26,32768\nv_permlane32_swap_b32 v86, v96\nv_permlane32_swap_b32 v87, v97\nAR 98,16\n"
"AR 99,17\nMaav 68,160,76\nEX 82,38\nCV 108,98,99\nAR 98,18\nMaav 76,164,76\nRa 148,26,33792\nAD 86,86,96\nAR 99,19\nCV 109,98,99\nAR 98,24\nWL 8\nEX 83,39\nAR 99,25\nCV 110,98,99\n"
"Ra 152,26,34816\nAD 85,85,82\nAD 87,87,97\nAR 98,26\nAR 99,27\nMaav 84,168,76\nD1 1,16,46,1024\nv_mov_b32 v96, v86\nCV 111,98,99\nMaav 92,172,76\nRa 156,26,35840\n"
"v_mov_b32 v97, v87\ns_add_u32 s5, s66, 0\nSv 108,1,16,5,2048\nAR 98,20\nAD 85,85,83\nCV 69,82,83\nAR 99,21\nCV 100,98,99\nAR 98,22\nRa 160,24,40960\n"
"v_permlane16_swap_b32 v86, v96\nv_permlane16_swap_b32 v87, v97\nAR 99,23\nCV 101,98,99\nMaav 100,176,76\ns_xor_b32 s89, s89, 0x10000\nEX 80,40\nAR 98,28\nMaav 108,180,76\n"
"Ra 164,24,41984\nAD 86,86,96\nAR 99,29\nCV 102,98,99\nAR 98,30\nWL 6\nEX 81,41\nAR 99,31\nCV 103,98,99\nRa 168,24,43008\nAD 84,84,80\nAD 87,87,97\ns_add_u32 s5, s66, 0\n"
"Sv 100,1,16,5,3072\nMaav 116,184,76\nD1 1,16,46,2048\nv_cmp_gt_u32 vcc, 0x100, v1\nv_mov_b32 v94, s15\nv_mov_b32 v95, s15\nv_cndmask_b32 v92, v89, v88, vcc\n"
"v_cndmask_b32 v93, v87, v86, vcc\nMaav 124,188,76\nRa 172,24,44032\nv_mov_b32 v86, 0\nAR 98,32\nAR 99,33\nCV 104,98,99\nAD 84,84,81\nCV 66,80,81\nAR 98,34\nAR 99,35\nCV 105,98,99\n"
"Cvaa 48,128,192,4\nRa 176,26,40960\nv_mov_b32 v87, 0\nv_mov_b32 v88, 0\nAR 98,40\nAR 99,41\nMvaa 48,132,200\nEX 82,42\nCV 106,98,99\nAR 98,42\nMvaa 48,136,208\nRa 180,26,41984\n"
"v_mov_b32 v89, 0\nAR 99,43\nCV 107,98,99\nMvaa 48,140,216\nWL 6\nEX 83,43\ns_add_u32 s5, s66, 4096\nSv 104,1,16,5,0\nCvaa 52,140,220,8\nRa 184,26,43008\nAD 84,84,82\nAR 98,36\n"
"AR 99,37\nCV 108,98,99\nMvaa 52,136,212\nD1 1,16,46,3072\ns_add_u32 m0, s89, 0\nAR 98,38\nAR 99,39\nMvaa 52,132,204\nRa 188,26,44032\nCV 109,98,99\nAR 98,44\nAR 99,45\n"
"CV 110,98,99\nMvaa 52,128,196\nAD 84,84,83\nCV 67,82,83\nAR 98,46\nAR 99,47\nCV 111,98,99\nMvaa 52,144,228\nRa 128,2,49152\ns_add_u32 s5, s66, 4096\nSv 108,1,16,5,1024\nAR 98,48\n"
"AR 99,49\nMvaa 52,148,236\ns_add_u32 s46, s86, s88\nEX 80,44\nCV 100,98,99\nMvaa 52,152,244\nRa 132,2,50176\nv_xor_b32 v24, 0x10000, v24\nAR 98,50\nAR 99,51\nCV 101,98,99\n"
"Mvaa 52,156,252\nWL 6\nEX 81,45\nAR 98,56\nAR 99,57\nMvaa 48,156,248\nRa 136,2,51200\nAD 85,85,80\nCV 102,98,99\nAR 98,58\nAR 99,59\nMvaa 48,152,240\nD1 1,16,46,0\nCV 103,98,99\n"
"s_add_u32 s5, s66, 4096\nSv 100,1,16,5,2048\nMvaa 48,148,232\nRa 140,2,52224\nAR 98,52\nAR 99,53\nCV 104,98,99\nAR 98,54\nMvaa 48,144,224\nAD 85,85,81\nCV 70,80,81\nAR 99,55\n"
"CV 105,98,99\nAR 98,60\nCvaa 56,160,192,12\nRa 144,2,53248\nAR 99,61\nCV 106,98,99\nAR 98,62\nAR 99,63\nMvaa 56,164,200\nEX 82,46\nCV 107,98,99\nMvaa 56,168,208\nRa 148,2,54272\n"
"v_xor_b32 v26, 0x10000, v26\ns_add_u32 s5, s66, 4096\nSv 104,1,16,5,3072\nAR 98,64\nMvaa 56,172,216\nWL 6\nEX 83,47\nAR 99,65\nCV 108,98,99\nCvaa 60,172,220,16\nRa 152,2,55296\n"
"AD 85,85,82\nAR 98,66\nAR 99,67\nCV 109,98,99\nMvaa 60,168,212\nD1 1,16,46,1024\nAR 98,72\nAR 99,73\nCV 110,98,99\nMvaa 60,164,204\nRa 156,2,56320\nAR 98,74\nAR 99,75\n"
"CV 111,98,99\nMvaa 60,160,196\nAD 85,85,83\nCV 71,82,83\ns_add_u32 s5, s66, 16384\nSv 108,1,16,5,0\nAR 98,68\nMvaa 60,176,228\nRa 160,2,57344\nv_max_f32 v90, v84, v85\nAR 99,69\n"
"CV 100,98,99\nAR 98,70\nMvaa 60,180,236\ns_add_u32 s88, s88, 32768\nEX 80,48\nAR 99,71\nMvaa 60,184,244\nRa 164,2,58368\nv_cmp_lt_f32 vcc, s63, v90\nMvaa 60,188,252\nWL 8\n"
"EX 81,49\nMvaa 56,188,248\nRa 168,2,59392\ns_cbranch_vccnz .T470\n.T471:\nv_mov_b32 v20, v84\nv_mov_b32 v21, v85\nv_mov_b32 v22, v20\nv_mov_b32 v23, v21\nMvaa 56,184,240\n"
"D1 1,16,46,2048\nv_permlane32_swap_b32 v20, v22\nv_permlane32_swap_b32 v21, v23\nAD 20,20,22\nAD 21,21,23\nv_mov_b32 v22, v20\nv_mov_b32 v23, v21\nMvaa 56,180,232\nRa 172,2,60416\n"
"v_permlane16_swap_b32 v20, v22\nv_permlane16_swap_b32 v21, v23\nAD 20,20,22\nAD 21,21,23\nv_min_f32 v90, v20, v21\nv_cmp_gt_f32 vcc, s64, v90\ns_nop 0\ns_cbranch_vccnz .T472\n"
".T473:\nAD 86,86,84\nMvaa 56,176,224\nAD 84,80,81\nCV 72,80,81\nCV 101,98,99\nAR 98,76\nAR 99,77\nZvav 128,128,64\nRa 176,2,61440\nCV 102,98,99\nAR 98,78\nAR 99,79\nCV 103,98,99\n"
"Zvav 132,128,68\nEX 82,50\ns_add_u32 s5, s66, 16384\nSv 100,1,16,5,1024\nZvav 140,132,68\nRa 180,2,62464\nAR 98,80\nAR 99,81\nCV 104,98,99\nAR 98,82\nZvav 136,132,64\n"
"s_waitcnt vmcnt(21) lgkmcnt(8)\nEX 83,51\nAR 99,83\nCV 105,98,99\nZvav 144,136,64\nRa 184,2,63488\nAD 84,84,82\nAR 98,88\nAR 99,89\nCV 106,98,99\nZvav 148,136,68\nD1 1,16,46,3072\n"
"s_add_u32 m0, s89, 16384\nAR 98,90\nAR 99,91\nZvav 156,140,68\nRa 188,2,64512\ns_barrier\nCV 107,98,99\ns_add_u32 s5, s66, 16384\nSv 104,1,16,5,2048\nZvav 152,140,64\nAD 84,84,83\n"
"CV 73,82,83\nAR 98,84\nAR 99,85\nCV 108,98,99\nZvav 160,144,64\nRa 128,24,0\nAR 98,86\nAR 99,87\nCV 109,98,99\nAR 98,92\nZvav 164,144,68\n.W594:\ns_cbranch_execz .W598\n"
"s_add_u32 s46, s87, s88\nEX 80,52\nAR 99,93\nZvav 172,148,68\nRa 132,24,1024\nv_xor_b32 v2, 0x10000, v2\nCV 110,98,99\nAR 98,94\nAR 99,95\nZvav 168,148,64\nWL 8\nEX 81,53\n"
"CV 111,98,99\nZvav 176,152,64\nRa 136,24,2048\nAD 87,87,85\ns_add_u32 s5, s66, 16384\nSv 108,1,16,5,3072\nAR 98,96\nZvav 180,152,68\nD1 1,16,46,0\nAR 99,97\nCV 100,98,99\n"
"AR 98,98\nZvav 188,156,68\nRa 140,24,3072\nAR 99,99\nCV 101,98,99\nAR 98,104\nAR 99,105\nZvav 184,156,64\nAD 85,80,81\nCV 76,80,81\nCV 102,98,99\nAR 98,106\nAR 99,107\n"
"Zvav 192,160,64\nRa 144,26,0\nCV 103,98,99\ns_add_u32 s5, s66, 20480\nSv 100,1,16,5,0\nAR 98,100\nZvav 196,160,68\nEX 82,54\nAR 99,101\nCV 104,98,99\nZvav 204,164,68\n"
"Ra 148,26,1024\nAR 98,102\nAR 99,103\nCV 105,98,99\nAR 98,108\nZvav 200,164,64\nWL 8\nEX 83,55\nAR 99,109\nCV 106,98,99\nZvav 208,168,64\nRa 152,26,2048\nAD 85,85,82\nAR 98,110\n"
"AR 99,111\nCV 107,98,99\nZvav 212,168,68\nD1 1,16,46,1024\ns_add_u32 s5, s66, 20480\nSv 104,1,16,5,1024\nAR 98,112\nZvav 220,172,68\nRa 156,26,3072\nAR 99,113\nCV 108,98,99\n"
"AR 98,114\nAR 99,115\nZvav 216,172,64\nAD 85,85,83\nCV 77,82,83\nCV 109,98,99\nAR 98,120\nAR 99,121\nZvav 224,176,64\nRa 160,24,8192\nCV 110,98,99\nAR 98,122\nAR 99,123\n"
"CV 111,98,99\nZvav 228,176,68\ns_sub_u32 s90, s90, 1\nEX 80,56\nZvav 236,180,68\nRa 164,24,9216\ns_cbranch_scc0 .E466\ns_swappc_b64 s[80:81], s[8:9]\n.E466:\n"
"s_add_u32 s5, s66, 20480\nSv 108,1,16,5,2048\nZvav 232,180,64\nWL 6\nEX 81,57\nAR 98,116\nAR 99,117\nZvav 240,184,64\nRa 168,24,10240\nAD 84,84,80\nCV 100,98,99\nAR 98,118\n"
"AR 99,119\nZvav 244,184,68\nD1 1,16,46,2048\nCV 101,98,99\nAR 98,124\nAR 99,125\nZvav 252,188,68\nRa 172,24,11264\nCV 102,98,99\nAR 98,126\nAR 99,127\nCV 103,98,99\n"
"Zvav 248,188,64\nAD 84,84,81\nCV 74,80,81\ns_add_u32 s5, s66, 20480\nSv 100,1,16,5,3072\ns_sub_u32 s99, s99, 1\n.p2align 6\n.A474:\nCvaa 32,128,192,4\nRa 176,26,8192\n"
"Mvaa 32,132,200\nEX 82,58\nMvaa 32,136,208\nRa 180,26,9216\nMvaa 32,140,216\nWL 6\nEX 83,59\nCvaa 36,140,220,8\nRa 184,26,10240\nAD 84,84,82\nMvaa 36,136,212\nD1 1,16,46,3072\n"
"s_add_u32 m0, s89, 32768\nMvaa 36,132,204\nRa 188,26,11264\nMvaa 36,128,196\nAD 84,84,83\nCV 75,82,83\nMvaa 36,144,228\nRa 128,2,16384\nMvaa 36,148,236\ns_add_u32 s46, s86, s88\n"
"EX 80,60\nMvaa 36,152,244\nRa 132,2,17408\nMvaa 36,156,252\nWL 6\nEX 81,61\nMvaa 32,156,248\nRa 136,2,18432\nAD 85,85,80\nMvaa 32,152,240\nD1 1,16,46,0\nMvaa 32,148,232\n"
"Ra 140,2,19456\nSv 112,1,16,49,0\nMvaa 32,144,224\nAD 85,85,81\nCV 78,80,81\nCvaa 40,160,192,12\nRa 144,2,20480\nMvaa 40,164,200\nEX 82,62\nMvaa 40,168,208\nRa 148,2,21504\n"
"Mvaa 40,172,216\nWL 6\nEX 83,63\nCvaa 44,172,220,16\nRa 152,2,22528\nAD 85,85,82\nMvaa 44,168,212\nD1 1,16,46,1024\nMvaa 44,164,204\nRa 156,2,23552\nMvaa 44,160,196\nAD 85,85,83\n"
"CV 79,82,83\nMvaa 44,176,228\nRa 160,2,24576\nv_max_f32 v90, v84, v85\nMvaa 44,180,236\ns_add_u32 s88, s88, 32768\nEX 80,32\nMvaa 44,184,244\nRa 164,2,25600\n"
"v_cmp_lt_f32 vcc, s63, v90\nMvaa 44,188,252\nWL 8\nEX 81,33\nMvaa 40,188,248\nRa 168,2,26624\ns_cbranch_vccnz .T484\n.T485:\nMvaa 40,184,240\nD1 1,16,46,2048\nMvaa 40,180,232\n"
"Ra 172,2,27648\nAD 86,86,84\nMvaa 40,176,224\nAD 84,80,81\nCV 64,80,81\nMvav 128,128,72\nRa 176,2,28672\nMvav 132,128,76\ns_cmp_eq_u32 s99, s98\nEX 82,34\nMvav 140,132,76\n"
"Ra 180,2,29696\ns_cbranch_scc1 .E482\n.E483:\nMvav 136,132,72\ns_waitcnt vmcnt(13) lgkmcnt(8)\nEX 83,35\nMvav 144,136,72\nRa 184,2,30720\nAD 84,84,82\nMvav 148,136,76\n"
"D1 1,16,46,3072\ns_add_u32 m0, s89, 49152\nMvav 156,140,76\nRa 188,2,31744\ns_barrier\nMvav 152,140,72\nAD 84,84,83\nCV 65,82,83\nMvav 160,144,72\nRa 128,24,32768\n"
"Mvav 164,144,76\ns_add_u32 s46, s87, s88\nEX 80,36\nMvav 172,148,76\nRa 132,24,33792\nMvav 168,148,72\nWL 8\nEX 81,37\nMvav 176,152,72\nRa 136,24,34816\nAD 87,87,85\n"
"Mvav 180,152,76\nD1 1,16,46,0\nMvav 188,156,76\nRa 140,24,35840\nMvav 184,156,72\nAD 85,80,81\nCV 68,80,81\nMvav 192,160,72\nRa 144,26,32768\nMvav 196,160,76\ns_cmp_eq_u32 s99, 0\n"
"EX 82,38\nMvav 204,164,76\nRa 148,26,33792\ns_cbranch_scc1 .E476\n.E477:\nMvav 200,164,72\nWL 8\nEX 83,39\nMvav 208,168,72\nRa 152,26,34816\nAD 85,85,82\nMvav 212,168,76\n"
"D1 1,16,46,1024\nMvav 220,172,76\nRa 156,26,35840\nMvav 216,172,72\nAD 85,85,83\nCV 69,82,83\nMvav 224,176,72\nRa 160,24,40960\nMvav 228,176,76\ns_xor_b32 s89, s89, 0x10000\n"
"EX 80,40\nMvav 236,180,76\nRa 164,24,41984\nMvav 232,180,72\nWL 6\nEX 81,41\nMvav 240,184,72\nRa 168,24,43008\nAD 84,84,80\nMvav 244,184,76\nD1 1,16,46,2048\nMvav 252,188,76\n"
"Ra 172,24,44032\nMvav 248,188,72\nAD 84,84,81\nCV 66,80,81\nCvaa 48,128,192,4\nRa 176,26,40960\nMvaa 48,132,200\ns_cmp_eq_u32 s99, 0\nEX 82,42\nMvaa 48,136,208\nRa 180,26,41984\n"
"s_cselect_b32 s86, s94, s86\nMvaa 48,140,216\nWL 6\nEX 83,43\nCvaa 52,140,220,8\nRa 184,26,43008\nAD 84,84,82\nMvaa 52,136,212\nD1 1,16,46,3072\ns_add_u32 m0, s89, 0\n"
"Mvaa 52,132,204\nRa 188,26,44032\nMvaa 52,128,196\nAD 84,84,83\nCV 67,82,83\nMvaa 52,144,228\nRa 128,2,49152\nMvaa 52,148,236\ns_add_u32 s46, s86, s88\nEX 80,44\nMvaa 52,152,244\n"
"Ra 132,2,50176\nv_xor_b32 v24, 0x10000, v24\nMvaa 52,156,252\nWL 6\nEX 81,45\nMvaa 48,156,248\nRa 136,2,51200\nAD 85,85,80\nMvaa 48,152,240\nD1 1,16,46,0\nMvaa 48,148,232\n"
"Ra 140,2,52224\nSv 112,1,16,49,1024\nMvaa 48,144,224\nAD 85,85,81\nCV 70,80,81\nCvaa 56,160,192,12\nRa 144,2,53248\nMvaa 56,164,200\ns_add_u32 s49, s49, 131072\nEX 82,46\n"
"Mvaa 56,168,208\nRa 148,2,54272\nv_xor_b32 v26, 0x10000, v26\nMvaa 56,172,216\nWL 6\nEX 83,47\nCvaa 60,172,220,16\nRa 152,2,55296\nAD 85,85,82\nMvaa 60,168,212\nD1 1,16,46,1024\n"
"Mvaa 60,164,204\nRa 156,2,56320\nMvaa 60,160,196\nAD 85,85,83\nCV 71,82,83\nMvaa 60,176,228\nRa 160,2,57344\nv_max_f32 v90, v84, v85\nMvaa 60,180,236\ns_add_u32 s88, s88, 32768\n"
"EX 80,48\nMvaa 60,184,244\nRa 164,2,58368\nv_cmp_lt_f32 vcc, s63, v90\nMvaa 60,188,252\nWL 8\nEX 81,49\nMvaa 56,188,248\nRa 168,2,59392\ns_cbranch_vccnz .T486\n.T487:\n"
"Mvaa 56,184,240\nD1 1,16,46,2048\nMvaa 56,180,232\nRa 172,2,60416\nAD 86,86,84\nMvaa 56,176,224\nAD 84,80,81\nCV 72,80,81\nMvav 128,128,64\nRa 176,2,61440\nMvav 132,128,68\n"
"EX 82,50\nMvav 140,132,68\nRa 180,2,62464\nMvav 136,132,64\ns_waitcnt vmcnt(13) lgkmcnt(8)\nEX 83,51\nMvav 144,136,64\nRa 184,2,63488\nAD 84,84,82\nMvav 148,136,68\n"
"D1 1,16,46,3072\ns_add_u32 m0, s89, 16384\nMvav 156,140,68\nRa 188,2,64512\ns_barrier\nMvav 152,140,64\nAD 84,84,83\nCV 73,82,83\nMvav 160,144,64\nRa 128,24,0\nMvav 164,144,68\n"
"s_add_u32 s46, s87, s88\nEX 80,52\nMvav 172,148,68\nRa 132,24,1024\nv_xor_b32 v2, 0x10000, v2\nMvav 168,148,64\nWL 8\nEX 81,53\nMvav 176,152,64\nRa 136,24,2048\nAD 87,87,85\n"
"Mvav 180,152,68\nD1 1,16,46,0\nMvav 188,156,68\nRa 140,24,3072\nMvav 184,156,64\nAD 85,80,81\nCV 76,80,81\nMvav 192,160,64\nRa 144,26,0\nMvav 196,160,68\ns_cmp_lt_u32 s99, s92\n"
"EX 82,54\nMvav 204,164,68\nRa 148,26,1024\ns_cbranch_scc1 .E478\n.E479:\nMvav 200,164,64\nWL 8\nEX 83,55\nMvav 208,168,64\nRa 152,26,2048\nAD 85,85,82\nMvav 212,168,68\n"
"D1 1,16,46,1024\nMvav 220,172,68\nRa 156,26,3072\nMvav 216,172,64\nAD 85,85,83\nCV 77,82,83\nMvav 224,176,64\nRa 160,24,8192\nMvav 228,176,68\ns_sub_u32 s90, s90, 1\nEX 80,56\n"
"Mvav 236,180,68\nRa 164,24,9216\ns_cbranch_scc1 .E480\n.E481:\nMvav 232,180,64\nWL 6\nEX 81,57\nMvav 240,184,64\nRa 168,24,10240\nAD 84,84,80\nMvav 244,184,68\nD1 1,16,46,2048\n"
"s_sub_u32 s99, s99, 1\nMvav 252,188,68\nRa 172,24,11264\nMvav 248,188,64\nAD 84,84,81\nCV 74,80,81\ns_cbranch_scc0 .A474\ns_branch .A488\n.E476:\nv_cmpx_gt_i32 vcc, 19, v0\n"
"v_mov_b32 v15, s97\nv_cmpx_gt_i32 vcc, 18, v0\nv_mov_b32 v14, s97\nv_cmpx_gt_i32 vcc, 17, v0\nv_mov_b32 v13, s97\nv_cmpx_gt_i32 vcc, 16, v0\nv_mov_b32 v12, s97\n"
"v_cmpx_gt_i32 vcc, 3, v0\nv_mov_b32 v7, s97\nv_cmpx_gt_i32 vcc, 2, v0\nv_mov_b32 v6, s97\nv_cmpx_gt_i32 vcc, 1, v0\nv_mov_b32 v5, s97\nv_cmpx_gt_i32 vcc, 0, v0\nv_mov_b32 v4, s97\n"
"s_mov_b64 exec, -1\ns_nop 2\ns_branch .E477\n.E478:\ns_cmp_eq_u32 s99, 0\ns_cbranch_scc1 .K475\nv_cmpx_gt_i32 vcc, 19, v3\nv_mov_b32 v15, s97\nv_cmpx_gt_i32 vcc, 18, v3\n"
"v_mov_b32 v14, s97\nv_cmpx_gt_i32 vcc, 17, v3\nv_mov_b32 v13, s97\nv_cmpx_gt_i32 vcc, 16, v3\nv_mov_b32 v12, s97\nv_cmpx_gt_i32 vcc, 3, v3\nv_mov_b32 v7, s97\n"
"v_cmpx_gt_i32 vcc, 2, v3\nv_mov_b32 v6, s97\nv_cmpx_gt_i32 vcc, 1, v3\nv_mov_b32 v5, s97\nv_cmpx_gt_i32 vcc, 0, v3\nv_mov_b32 v4, s97\ns_mov_b64 exec, -1\ns_nop 2\ns_branch .E479\n"
".K475:\nv_cmpx_gt_i32 vcc, 19, v3\nv_mov_b32 v19, s97\nv_cmpx_gt_i32 vcc, 18, v3\nv_mov_b32 v18, s97\nv_cmpx_gt_i32 vcc, 17, v3\nv_mov_b32 v17, s97\nv_cmpx_gt_i32 vcc, 16, v3\n"
"v_mov_b32 v16, s97\nv_cmpx_gt_i32 vcc, 3, v3\nv_mov_b32 v11, s97\nv_cmpx_gt_i32 vcc, 2, v3\nv_mov_b32 v10, s97\nv_cmpx_gt_i32 vcc, 1, v3\nv_mov_b32 v9, s97\n"
"v_cmpx_gt_i32 vcc, 0, v3\nv_mov_b32 v8, s97\ns_mov_b64 exec, -1\ns_nop 2\ns_branch .E479\n.E480:\ns_swappc_b64 s[80:81], s[8:9]\ns_branch .E481\n.E482:\nv_mov_b32 v91, 0x40000000\n"
"v_cmp_gt_u32 vcc, 0x200, v1\nWV 5\ns_nop 0\nv_cndmask_b32 v91, v91, v1, vcc\nSv 92,91,16,65,0\ns_branch .E483\n.A488:\n.LE418:\nRa 176,26,8192\nEX 82,58\nRa 180,26,9216\nWL 6\n"
"EX 83,59\nCvaa 36,140,220,8\nRa 184,26,10240\nAD 84,84,82\nMvaa 36,136,212\nD1 1,16,46,3072\ns_add_u32 m0, s89, 32768\nMvaa 36,132,204\nRa 188,26,11264\nMvaa 36,128,196\n"
"AD 84,84,83\nCV 75,82,83\nMvaa 36,144,228\nRa 128,2,16384\nMvaa 36,148,236\ns_add_u32 s46, s86, s88\nEX 80,60\nMvaa 36,152,244\nRa 132,2,17408\nMvaa 36,156,252\nEX 81,61\n"
"Ra 136,2,18432\nAD 85,85,80\nD1 1,16,46,0\nRa 140,2,19456\nAD 85,85,81\nCV 78,80,81\nRa 144,2,20480\ns_mov_b32 s87, s95\nEX 82,62\nRa 148,2,21504\nWL 6\nEX 83,63\n"
"Cvaa 44,172,220,16\nRa 152,2,22528\nAD 85,85,82\nMvaa 44,168,212\nD1 1,16,46,1024\nMvaa 44,164,204\nRa 156,2,23552\nMvaa 44,160,196\nAD 85,85,83\nCV 79,82,83\nMvaa 44,176,228\n"
"Ra 160,2,24576\nv_max_f32 v90, v84, v85\nMvaa 44,180,236\ns_add_u32 s88, s88, 32768\nMvaa 44,184,244\nRa 164,2,25600\nv_cmp_lt_f32 vcc, s63, v90\nMvaa 44,188,252\nWL 8\n"
"Ra 168,2,26624\ns_cbranch_vccnz .T490\n.T491:\nD1 1,16,46,2048\nRa 172,2,27648\nAD 86,86,84\nMvav 128,128,72\nRa 176,2,28672\nMvav 132,128,76\nMvav 140,132,76\nRa 180,2,29696\n"
"Mvav 136,132,72\ns_waitcnt vmcnt(12) lgkmcnt(8)\nMvav 144,136,72\nRa 184,2,30720\nMvav 148,136,76\nD1 1,16,46,3072\ns_add_u32 m0, s89, 49152\nMvav 156,140,76\nRa 188,2,31744\n"
"s_barrier\nMvav 152,140,72\nMvav 160,144,72\nRa 128,24,32768\n.W595:\ns_cbranch_execz .WB416\nMvav 164,144,76\ns_add_u32 s46, s87, s88\nEX 80,36\nMvav 172,148,76\nRa 132,24,33792\n"
"Mvav 168,148,72\nWL 8\nEX 81,37\nMvav 176,152,72\nRa 136,24,34816\nAD 87,87,85\nMvav 180,152,76\nD1 1,16,46,0\nMvav 188,156,76\nRa 140,24,35840\nMvav 184,156,72\nAD 85,80,81\n"
"CV 68,80,81\nMvav 192,160,72\nRa 144,26,32768\nMvav 196,160,76\ns_nop 0\nEX 82,38\nMvav 204,164,76\nRa 148,26,33792\nv_cmpx_gt_i32 vcc, 19, v0\nv_mov_b32 v19, s97\n"
"v_cmpx_gt_i32 vcc, 18, v0\nv_mov_b32 v18, s97\nv_cmpx_gt_i32 vcc, 17, v0\nv_mov_b32 v17, s97\nv_cmpx_gt_i32 vcc, 16, v0\nv_mov_b32 v16, s97\nv_cmpx_gt_i32 vcc, 3, v0\n"
"v_mov_b32 v11, s97\nv_cmpx_gt_i32 vcc, 2, v0\nv_mov_b32 v10, s97\nv_cmpx_gt_i32 vcc, 1, v0\nv_mov_b32 v9, s97\nv_cmpx_gt_i32 vcc, 0, v0\nv_mov_b32 v8, s97\ns_mov_b64 exec, -1\n"
"s_nop 2\ns_nop 0\nMvav 200,164,72\nWL 8\nEX 83,39\nMvav 208,168,72\nRa 152,26,34816\nAD 85,85,82\nMvav 212,168,76\nD1 1,16,46,1024\nMvav 220,172,76\nRa 156,26,35840\n"
"Mvav 216,172,72\nAD 85,85,83\nCV 69,82,83\nMvav 224,176,72\nRa 160,24,40960\nMvav 228,176,76\ns_xor_b32 s89, s89, 0x10000\nMvav 236,180,76\nRa 164,24,41984\nMvav 232,180,72\n"
"WL 10\nMvav 240,184,72\nRa 168,24,43008\nMvav 244,184,76\nD1 1,16,46,2048\nMvav 252,188,76\nRa 172,24,44032\nMvav 248,188,72\nRa 176,26,40960\nRa 180,26,41984\nWL 6\n"
"Cvaa 52,140,220,8\nRa 184,26,43008\nMvaa 52,136,212\nD1 1,16,46,3072\ns_add_u32 m0, s89, 0\nMvaa 52,132,204\nRa 188,26,44032\nMvaa 52,128,196\nMvaa 52,144,228\nRa 128,2,49152\n"
"Mvaa 52,148,236\ns_add_u32 s46, s86, s88\nEX 80,44\nMvaa 52,152,244\nRa 132,2,50176\nv_xor_b32 v24, 0x10000, v24\nMvaa 52,156,252\nEX 81,45\nRa 136,2,51200\nAD 85,85,80\n"
"D1 1,16,46,0\nRa 140,2,52224\nAD 85,85,81\nCV 70,80,81\nRa 144,2,53248\nEX 82,46\nRa 148,2,54272\nv_xor_b32 v26, 0x10000, v26\nWL 6\nEX 83,47\nCvaa 60,172,220,16\nRa 152,2,55296\n"
"AD 85,85,82\nMvaa 60,168,212\nD1 1,16,46,1024\nMvaa 60,164,204\nRa 156,2,56320\nMvaa 60,160,196\nAD 85,85,83\nCV 71,82,83\nMvaa 60,176,228\nRa 160,2,57344\nMvaa 60,180,236\n"
"s_add_u32 s88, s88, 32768\nMvaa 60,184,244\nRa 164,2,58368\nv_cmp_lt_f32 vcc, s63, v85\nMvaa 60,188,252\nWL 8\nRa 168,2,59392\ns_cbranch_vccnz .T492\n.T493:\nD1 1,16,46,2048\n"
"Ra 172,2,60416\nRa 176,2,61440\nMvav 132,128,68\nMvav 140,132,68\nRa 180,2,62464\ns_waitcnt vmcnt(11) lgkmcnt(8)\nRa 184,2,63488\nMvav 148,136,68\nD1 1,16,46,3072\n"
"s_add_u32 m0, s89, 16384\nMvav 156,140,68\nRa 188,2,64512\ns_barrier\nRa 128,24,0\nMvav 164,144,68\ns_add_u32 s46, s87, s88\nEX 80,52\nMvav 172,148,68\nRa 132,24,1024\n"
"v_xor_b32 v2, 0x10000, v2\nWL 8\nEX 81,53\nRa 136,24,2048\nAD 87,87,85\nMvav 180,152,68\nD1 1,16,46,0\nMvav 188,156,68\nRa 140,24,3072\nAD 85,80,81\nCV 76,80,81\nRa 144,26,0\n"
"Mvav 196,160,68\nEX 82,54\nMvav 204,164,68\nRa 148,26,1024\nWL 8\nEX 83,55\nRa 152,26,2048\nAD 85,85,82\nMvav 212,168,68\nD1 1,16,46,1024\nMvav 220,172,68\nRa 156,26,3072\n"
"AD 85,85,83\nCV 77,82,83\nRa 160,24,8192\nMvav 228,176,68\nMvav 236,180,68\nRa 164,24,9216\nWL 10\nRa 168,24,10240\nMvav 244,184,68\nD1 1,16,46,2048\nMvav 252,188,68\n"
"Ra 172,24,11264\nD1 1,16,46,3072\ns_add_u32 m0, s89, 32768\ns_add_u32 s46, s86, s88\nRa 128,2,16384\nv_xor_b32 v3, 0x10000, v2\nv_add_u32 v0, 0x20000, v1\nEX 80,60\n"
"Ra 132,2,17408\ns_sub_u32 s94, s49, 1835008\nEX 81,61\nRa 136,2,18432\nAD 85,85,80\ns_xor_b32 s92, s94, 0x202000\nD1 1,16,46,0\ns_lshl_b32 s89, s11, 12\nRa 140,2,19456\n"
"s_lshl1_add_u32 s2, s12, 1\nAD 85,85,81\nCV 78,80,81\nRa 144,2,20480\ns_lshl3_add_u32 s2, s2, s13\nEX 82,62\nRa 148,2,21504\ns_lshl4_add_u32 s2, s2, 0xc048200\nEX 83,63\n"
"Ra 152,2,22528\nAD 85,85,82\nD1 1,16,46,1024\nRa 156,2,23552\ns_and_b32 s3, s72, 0x2000\nAD 85,85,83\nCV 79,82,83\nRa 160,2,24576\ns_cmp_eq_u32 s11, 1\ns_cselect_b32 s54, s2, s54\n"
"s_cselect_b32 s3, s3, 0\nv_cmp_lt_f32 vcc, s63, v85\nRa 164,2,25600\ns_cbranch_vccnz .T494\n.T495:\nAD 87,87,85\nWL 8\nv_mov_b32 v84, v86\nv_mov_b32 v85, v87\nRa 168,2,26624\n"
"v_permlane32_swap_b32 v86, v84\nv_permlane32_swap_b32 v87, v85\nD1 1,16,46,2048\nAD 86,86,84\nRa 172,2,27648\nAD 87,87,85\nv_mov_b32 v84, v86\nv_mov_b32 v85, v87\nRa 176,2,28672\n"
"v_permlane16_swap_b32 v86, v84\nv_permlane16_swap_b32 v87, v85\nMvav 132,128,76\nAD 86,86,84\nAD 87,87,85\nMvav 140,132,76\nRa 180,2,29696\nv_rcp_f32 v86, v86\nWL 8\n"
"v_rcp_f32 v87, v87\nRa 184,2,30720\nv_xor_b32 v25, s3, v25\nMvav 148,136,76\nD1 1,16,46,3072\nMvav 156,140,76\nRa 188,2,31744\nMU 128,128,86\nMU 129,129,86\nCV 96,128,129\n"
"MU 130,130,86\nMU 131,131,86\nRa 128,2,0\nCV 97,130,131\nMU 132,132,87\nMvav 164,144,76\nMU 133,133,87\nCV 100,132,133\nMvav 172,148,76\nRa 132,2,1024\nMU 134,134,87\n"
"MU 135,135,87\nWL 8\ns_add_u32 m0, s89, 131072\ns_add_u32 s46, s96, 1212416\nRa 136,2,2048\nCV 101,134,135\nMU 136,136,86\nMvav 180,152,76\nD1 1,16,46,0\nMU 137,137,86\n"
"Mvav 188,156,76\nRa 140,2,3072\nCV 98,136,137\nMU 138,138,86\nMU 139,139,86\nCV 99,138,139\nMU 140,140,87\nRa 144,2,4096\nMU 141,141,87\nCV 102,140,141\nMvav 196,160,76\n"
"MU 142,142,87\nMU 143,143,87\nMvav 204,164,76\nRa 148,2,5120\nCV 103,142,143\nMU 144,144,86\nWL 8\nMU 145,145,86\nCV 104,144,145\nRa 152,2,6144\nMU 146,146,86\nMU 147,147,86\n"
"Mvav 212,168,76\nD1 1,16,46,1024\nCV 105,146,147\nMvav 220,172,76\nRa 156,2,7168\nMU 148,148,87\nMU 149,149,87\nCV 108,148,149\nMU 150,150,87\nMU 151,151,87\nRa 160,2,8192\n"
"CV 109,150,151\nMU 152,152,86\nMvav 228,176,76\nMU 153,153,86\nCV 106,152,153\nMvav 236,180,76\nRa 164,2,9216\nMU 154,154,86\nMU 155,155,86\nWL 8\nCV 107,154,155\nMU 156,156,87\n"
"Ra 168,2,10240\nMU 157,157,87\nCV 110,156,157\nMvav 244,184,76\nD1 1,16,46,2048\nMU 158,158,87\nMvav 252,188,76\nRa 172,2,11264\nMU 159,159,87\nCV 111,158,159\nMU 160,160,86\n"
"MU 161,161,86\nCV 112,160,161\nZvav 128,128,96\nRa 176,2,12288\nMU 162,162,86\nMU 163,163,86\nMvav 128,132,104\nCV 113,162,163\nMU 164,164,87\nZvav 132,132,108\nRa 180,2,13312\n"
"MU 165,165,87\nCV 116,164,165\nMvav 132,128,100\ns_waitcnt vmcnt(15) lgkmcnt(8)\nMU 166,166,87\nMU 167,167,87\nZvav 140,136,100\nRa 184,2,14336\nCV 117,166,167\nMU 168,168,86\n"
"Mvav 140,140,108\nD1 1,16,46,3072\nMU 169,169,86\nCV 114,168,169\nZvav 136,140,104\nRa 188,2,15360\ns_barrier\nMU 170,170,86\nMvav 136,136,96\nMU 171,171,86\nCV 115,170,171\n"
"MU 172,172,87\nZvav 144,144,96\nRa 128,2,32768\nMU 173,173,87\nCV 118,172,173\nMvav 144,148,104\nMU 174,174,87\nMU 175,175,87\n"
"buffer_store_dword v25, off, s[16:19], s54 offset:512 sc0 sc1\nZvav 148,148,108\nRa 132,2,33792\nCV 119,174,175\nMU 176,176,86\nMvav 148,144,100\nWL 8\ns_add_u32 m0, s89, 49152\n"
"s_add_u32 s46, s96, 1179648\nZvav 156,152,100\nRa 136,2,34816\nMU 177,177,86\nCV 120,176,177\nMvav 156,156,108\nD1 1,16,46,0\nMU 178,178,86\nMU 179,179,86\nZvav 152,156,104\n"
"Ra 140,2,35840\nCV 121,178,179\nMU 180,180,87\nMvav 152,152,96\nMU 181,181,87\nCV 124,180,181\nMU 182,182,87\nZvav 64,160,96\nRa 144,2,36864\nMU 183,183,87\nCV 125,182,183\n"
"Mvav 64,164,104\nMU 184,184,86\nMU 185,185,86\nZvav 68,164,108\nRa 148,2,37888\nCV 122,184,185\nMU 186,186,86\nMvav 68,160,100\nWL 8\nMU 187,187,86\nCV 123,186,187\n"
"Zvav 76,168,100\nRa 152,2,38912\nMU 188,188,87\nMU 189,189,87\nMvav 76,172,108\nD1 1,16,46,1024\nCV 126,188,189\nMU 190,190,87\nZvav 72,172,104\nRa 156,2,39936\nMU 191,191,87\n"
"CV 127,190,191\nMvav 72,168,96\nMU 192,192,86\nMU 193,193,86\nCV 32,192,193\nMvav 128,176,112\nRa 160,2,40960\nMU 194,194,86\nMU 195,195,86\nMvav 128,180,120\nCV 33,194,195\n"
"MU 196,196,87\nMU 197,197,87\nMvav 132,180,124\nRa 164,2,41984\nCV 36,196,197\nMU 198,198,87\nMvav 132,176,116\nWL 8\nMU 199,199,87\nMvav 140,184,116\nRa 168,2,43008\n"
"CV 37,198,199\nMU 200,200,86\nMvav 140,188,124\nD1 1,16,46,2048\nMU 201,201,86\nCV 34,200,201\nMvav 136,188,120\nRa 172,2,44032\nMU 202,202,86\nMU 203,203,86\nMvav 136,184,112\n"
"CV 35,202,203\nMU 204,204,87\nMU 205,205,87\nMvav 144,128,112\nRa 176,2,45056\nCV 38,204,205\nMU 206,206,87\nMvav 144,132,120\nMU 207,207,87\nCV 39,206,207\nMvav 148,132,124\n"
"Ra 180,2,46080\nMU 208,208,86\nMU 209,209,86\nMvav 148,128,116\nWL 8\nCV 40,208,209\nMU 210,210,86\nMvav 156,136,116\nRa 184,2,47104\nMU 211,211,86\nCV 41,210,211\n"
"Mvav 156,140,124\nD1 1,16,46,3072\nMU 212,212,87\nMU 213,213,87\nMvav 152,140,120\nRa 188,2,48128\nCV 44,212,213\nMvav 152,136,112\nMU 214,214,87\nMU 215,215,87\nCV 45,214,215\n"
"Mvav 64,144,112\nRa 128,2,49152\nMU 216,216,86\nMU 217,217,86\nMvav 64,148,120\nCV 42,216,217\nMU 218,218,86\nMU 219,219,86\nMvav 68,148,124\nRa 132,2,50176\nCV 43,218,219\n"
"MU 220,220,87\nMvav 68,144,116\nWL 8\ns_add_u32 m0, s89, 147456\ns_add_u32 s46, s96, 1245184\nMvav 76,152,116\nRa 136,2,51200\nMU 221,221,87\nCV 46,220,221\nMvav 76,156,124\n"
"D1 1,16,46,0\nMU 222,222,87\nMU 223,223,87\nMvav 72,156,120\nRa 140,2,52224\nCV 47,222,223\nMvav 72,152,112\nMU 224,224,86\nMU 225,225,86\nCV 48,224,225\nMvav 128,160,32\n"
"Ra 144,2,53248\nMU 226,226,86\nMU 227,227,86\nMvav 128,164,40\nCV 49,226,227\nMU 228,228,87\nMvav 132,164,44\nRa 148,2,54272\nMU 229,229,87\nCV 52,228,229\nMvav 132,160,36\nWL 8\n"
"MU 230,230,87\nMU 231,231,87\nMvav 140,168,36\nRa 152,2,55296\nCV 53,230,231\nMU 232,232,86\nMvav 140,172,44\nD1 1,16,46,1024\nMU 233,233,86\nCV 50,232,233\nMvav 136,172,40\n"
"Ra 156,2,56320\nMU 234,234,86\nMvav 136,168,32\nMU 235,235,86\nCV 51,234,235\nMU 236,236,87\nMvav 144,176,32\nRa 160,2,57344\nMU 237,237,87\nCV 54,236,237\nMvav 144,180,40\n"
"MU 238,238,87\nMU 239,239,87\nCV 55,238,239\nMvav 148,180,44\nRa 164,2,58368\nMU 240,240,86\nMU 241,241,86\nMvav 148,176,36\nWL 8\nCV 56,240,241\nMvav 156,184,36\nRa 168,2,59392\n"
"MU 242,242,86\nMU 243,243,86\nMvav 156,188,44\nD1 1,16,46,2048\nCV 57,242,243\nMU 244,244,87\nMvav 152,188,40\nRa 172,2,60416\nMU 245,245,87\nCV 60,244,245\nMvav 152,184,32\n"
"MU 246,246,87\nMU 247,247,87\nCV 61,246,247\nMvav 64,128,32\nRa 176,2,61440\nMU 248,248,86\nMU 249,249,86\nMvav 64,132,40\nCV 58,248,249\nMU 250,250,86\nMvav 68,132,44\n"
"Ra 180,2,62464\nMU 251,251,86\nCV 59,250,251\nMvav 68,128,36\ns_waitcnt vmcnt(16) lgkmcnt(8)\nMU 252,252,87\nMU 253,253,87\nMvav 76,136,36\nRa 184,2,63488\nCV 62,252,253\n"
"MU 254,254,87\nMvav 76,140,44\nD1 1,16,46,3072\nMU 255,255,87\nCV 63,254,255\nMvav 72,140,40\nRa 188,2,64512\ns_barrier\nMvav 72,136,32\nMvav 128,144,48\nRa 128,3,0\n"
"Mvav 128,148,56\nMvav 132,148,60\nRa 132,3,1024\nMvav 132,144,52\nWL 8\nMvav 140,152,52\nRa 136,3,2048\nMvav 140,156,60\nMvav 136,156,56\nRa 140,3,3072\nMvav 136,152,48\n"
"Mvav 144,160,48\nRa 144,3,4096\nMvav 144,164,56\nMvav 148,164,60\nRa 148,3,5120\nCV 132,132,133\nMvav 148,160,52\nWL 8\nCV 133,134,135\nMvav 156,168,52\n.W596:\n"
"s_cbranch_execz .WB416\nRa 152,3,6144\nCV 134,140,141\nMvav 156,172,60\nCV 135,142,143\ns_add_u32 s5, s94, 0\nMvav 152,172,56\nRa 156,3,7168\nSv 132,1,16,5,1024\nMvav 152,168,48\n"
"CV 128,128,129\nCV 129,130,131\nMvav 64,176,48\nRa 160,3,8192\nCV 130,136,137\nMvav 64,180,56\nbuffer_load_dword v27, off, s[16:19], s60 offset:512 sc0 sc1\nCV 131,138,139\n"
"Mvav 68,180,60\nRa 164,3,9216\nSv 128,1,16,5,0\nMvav 68,176,52\nWL 6\nCV 148,148,149\nMvav 76,184,52\nRa 168,3,10240\nCV 149,150,151\nMvav 76,188,60\nCV 150,156,157\n"
"CV 151,158,159\nMvav 72,188,56\nRa 172,3,11264\ns_add_u32 s5, s94, 131072\nMvav 72,184,48\nSv 148,1,16,5,1024\nCV 144,144,145\nZvav 160,128,96\nRa 176,3,12288\nCV 145,146,147\n"
"Mvav 160,132,104\nCV 146,152,153\nCV 147,154,155\nMvav 160,136,112\nRa 180,3,13312\nSv 144,1,16,5,0\nMvav 160,140,120\nWL 6\nCV 68,68,69\nZvav 164,140,124\nRa 184,3,14336\n"
"CV 69,70,71\nMvav 164,136,116\nCV 70,76,77\nCV 71,78,79\nMvav 164,132,108\nRa 188,3,15360\ns_add_u32 s5, s94, 262144\nMvav 164,128,100\nSv 68,1,16,5,1024\nCV 64,64,65\n"
"Mvav 164,144,36\nRa 128,3,16384\nCV 65,66,67\nMvav 164,148,44\nCV 66,72,73\nCV 67,74,75\nMvav 164,152,52\nRa 132,3,17408\nSv 64,1,16,5,0\nMvav 164,156,60\nWL 6\nMvav 160,156,56\n"
"Ra 136,3,18432\nMvav 160,152,48\nMvav 160,148,40\nRa 140,3,19456\nMvav 160,144,32\nZvav 168,160,96\nRa 144,3,20480\nMvav 168,164,104\nMvav 168,168,112\nRa 148,3,21504\n"
"Mvav 168,172,120\nWL 6\nZvav 172,172,124\nRa 152,3,22528\nMvav 172,168,116\nMvav 172,164,108\nRa 156,3,23552\nMvav 172,160,100\nMvav 172,176,36\nRa 160,3,24576\nMvav 172,180,44\n"
"Mvav 172,184,52\nRa 164,3,25600\nMvav 172,188,60\nWL 6\nMvav 168,188,56\nRa 168,3,26624\nMvav 168,184,48\nMvav 168,180,40\nRa 172,3,27648\nMvav 168,176,32\nZvav 176,128,96\n"
"Ra 176,3,28672\nCV 164,164,165\nMvav 176,132,104\nCV 165,166,167\nCV 166,172,173\nMvav 176,136,112\nRa 180,3,29696\nCV 167,174,175\nMvav 176,140,120\n"
"s_waitcnt vmcnt(11) lgkmcnt(6)\ns_add_u32 s5, s94, 393216\nZvav 180,140,124\nRa 184,3,30720\nSv 164,1,16,5,1024\nMvav 180,136,116\nCV 160,160,161\nCV 161,162,163\n"
"Mvav 180,132,108\nRa 188,3,31744\ns_barrier\nMvav 180,128,100\nCV 162,168,169\nCV 163,170,171\nMvav 180,144,36\nRa 128,3,32768\nSv 160,1,16,5,0\nMvav 180,148,44\nMvav 180,152,52\n"
"Ra 132,3,33792\nMvav 180,156,60\nWL 6\ns_add_u32 m0, s89, 81920\ns_add_u32 s46, s96, 1277952\nMvav 176,156,56\nRa 136,3,34816\nMvav 176,152,48\nD1 1,16,46,0\nMvav 176,148,40\n"
"Ra 140,3,35840\nMvav 176,144,32\nZvav 184,160,96\nRa 144,3,36864\nMvav 184,164,104\nMvav 184,168,112\nRa 148,3,37888\nMvav 184,172,120\nWL 6\nZvav 188,172,124\nRa 152,3,38912\n"
"Mvav 188,168,116\nD1 1,16,46,1024\nMvav 188,164,108\nRa 156,3,39936\nMvav 188,160,100\nMvav 188,176,36\nRa 160,3,40960\nWV 9\nv_cmp_ne_u32 vcc, s15, v27\ns_nop 0\n"
"s_cbranch_vccnz .S520\n.S521:\nMvav 188,180,44\ns_add_u32 s4, s92, 0\nMvav 188,184,52\nRa 164,3,41984\nMvav 188,188,60\nWL 6\n"
"buffer_load_dwordx4 v[208:211], v1, s[16:19], s4 offen offset:0 sc0 sc1\nMvav 184,188,56\nRa 168,3,43008\nMvav 184,184,48\nD1 1,16,46,2048\nMvav 184,180,40\nRa 172,3,44032\n"
"Mvav 184,176,32\nbuffer_load_dwordx4 v[212:215], v1, s[16:19], s4 offen offset:1024 sc0 sc1\nZvav 192,128,96\nRa 176,3,45056\nv_lshrrev_b32 v88, 8, v1\nMvav 192,132,104\n"
"v_lshlrev_b32 v89, 5, v88\nCV 180,180,181\nMvav 192,136,112\nRa 180,3,46080\nCV 181,182,183\nMvav 192,140,120\nWL 6\nCV 182,188,189\nZvav 196,140,124\nRa 184,3,47104\n"
"v_bfe_u32 v90, v1, 4, 4\nMvav 196,136,116\nD1 1,16,46,3072\nCV 183,190,191\nMvav 196,132,108\nRa 188,3,48128\ns_add_u32 s5, s94, 524288\nMvav 196,128,100\n"
"v_mul_u32_u24 v91, 896, v90\nSv 180,1,16,5,1024\nMvav 196,144,36\nRa 128,3,49152\nv_lshl_add_u32 v91, v88, 4, v91\nMvav 196,148,44\nv_add_u32 v90, s21, v90\nCV 176,176,177\n"
"Mvav 196,152,52\nRa 132,3,50176\nCV 177,178,179\nMvav 196,156,60\nWL 6\ns_add_u32 m0, s89, 65536\ns_add_u32 s46, s96, 1310720\nMvav 192,156,56\nRa 136,3,51200\n"
"v_lshlrev_b32 v88, 3, v90\nMvav 192,152,48\nD1 1,16,46,0\nCV 178,184,185\nMvav 192,148,40\nRa 140,3,52224\nCV 179,186,187\nMvav 192,144,32\nv_add_u32 v94, 64, v90\n"
"Sv 176,1,16,5,0\nZvav 200,160,96\nRa 144,3,53248\ns_mov_b32 s63, 0xc040000\nMvav 200,164,104\nv_lshrrev_b32 v92, 8, v1\nMvav 200,168,112\nRa 148,3,54272\nMvav 200,172,120\nWL 6\n"
"Zvav 204,172,124\nRa 152,3,55296\nv_bfe_u32 v93, v1, 6, 2\nMvav 204,168,116\nD1 1,16,46,1024\nv_cmp_eq_u32 vcc, v92, v93\nv_and_b32 v92, 16, v1\nv_mov_b32 v93, 0x3f80\n"
"v_cndmask_b32 v93, 0, v93, vcc\nMvav 204,164,108\nRa 156,3,56320\nMvav 204,160,100\nv_lshlrev_b32 v93, v92, v93\nMvav 204,176,36\nRa 160,3,57344\nv_and_b32 v92, 32, v1\n"
"Mvav 204,180,44\ns_add_u32 s4, s92, 131072\nv_cmp_eq_u32 vcc, 0, v92\nv_mov_b32 v4, 0\nv_mov_b32 v5, 0\nv_cndmask_b32 v6, 0, v93, vcc\nMvav 204,184,52\nRa 164,3,58368\n"
"Mvav 204,188,60\nWL 6\nbuffer_load_dwordx4 v[216:219], v1, s[16:19], s4 offen offset:0 sc0 sc1\nMvav 200,188,56\nRa 168,3,59392\nv_sub_u32 v7, v93, v6\nMvav 200,184,48\n"
"D1 1,16,46,2048\nMvav 200,180,40\nRa 172,3,60416\nMvav 200,176,32\nbuffer_load_dwordx4 v[220:223], v1, s[16:19], s4 offen offset:1024 sc0 sc1\nZvav 160,128,96\nRa 176,3,61440\n"
"v_mov_b32 v8, 0\nMvav 160,132,104\nv_mov_b32 v9, 0\nCV 196,196,197\nMvav 160,136,112\nRa 180,3,62464\nCV 197,198,199\nMvav 160,140,120\nWL 6\nCV 198,204,205\nZvav 164,140,124\n"
"Ra 184,3,63488\nCV 199,206,207\nMvav 164,136,116\nD1 1,16,46,3072\ns_add_u32 s5, s94, 655360\nMvav 164,132,108\nRa 188,3,64512\ns_barrier\nMvav 164,128,100\nSv 196,1,16,5,1024\n"
"CV 192,192,193\nMvav 164,144,36\nRa 128,0,0\nCV 193,194,195\nMvav 164,148,44\nCV 194,200,201\nCV 195,202,203\nMvav 164,152,52\nRa 132,0,1024\nSv 192,1,16,5,0\nMvav 164,156,60\n"
"WL 6\ns_add_u32 m0, s89, 98304\ns_add_u32 s46, s96, 1343488\nMvav 160,156,56\nRa 136,0,2048\nMvav 160,152,48\nD1 1,16,46,0\nMvav 160,148,40\nRa 140,0,3072\nMvav 160,144,32\n"
"Zvav 168,160,96\nRa 144,0,4096\nWV 12\nv_max_u32 v21, v211, v215\nv_cmp_eq_u32 vcc, -1, v21\ns_nop 0\ns_cbranch_vccnz .S496\n.S497:\nMvav 168,164,104\n"
"v_lshlrev_b32 v176, 16, v208\nMvav 168,168,112\nRa 148,0,5120\nMvav 168,172,120\nWL 6\nv_and_b32 v177, 0xffff0000, v208\nZvav 172,172,124\nRa 152,0,6144\nMvav 172,168,116\n"
"D1 1,16,46,1024\nMvav 172,164,108\nRa 156,0,7168\nv_lshlrev_b32 v178, 16, v209\nMvav 172,160,100\nv_and_b32 v179, 0xffff0000, v209\nMvav 172,176,36\nRa 160,0,8192\n"
"v_lshlrev_b32 v180, 16, v212\nMvav 172,180,44\nv_and_b32 v181, 0xffff0000, v212\ns_add_u32 s4, s92, 262144\nMvav 172,184,52\nRa 164,0,9216\nMvav 172,188,60\nWL 6\n"
"v_lshlrev_b32 v182, 16, v213\nbuffer_load_dwordx4 v[224:227], v1, s[16:19], s4 offen offset:0 sc0 sc1\nMvav 168,188,56\nRa 168,0,10240\nMvav 168,184,48\nD1 1,16,46,2048\n"
"Mvav 168,180,40\nRa 172,0,11264\nv_and_b32 v183, 0xffff0000, v213\nMvav 168,176,32\nv_lshlrev_b32 v184, 16, v210\n"
"buffer_load_dwordx4 v[228:231], v1, s[16:19], s4 offen offset:1024 sc0 sc1\nMvav 176,128,96\nRa 176,0,12288\nv_and_b32 v185, 0xffff0000, v210\nMvav 176,132,104\n"
"v_lshlrev_b32 v186, 16, v211\nCV 164,164,165\nMvav 176,136,112\nRa 180,0,13312\nCV 165,166,167\nMvav 176,140,120\nWL 6\nCV 166,172,173\nMvav 180,140,124\nRa 184,0,14336\n"
"v_and_b32 v187, 0xffff0000, v211\nMvav 180,136,116\nD1 1,16,46,3072\nCV 167,174,175\nMvav 180,132,108\nRa 188,0,15360\ns_add_u32 s5, s94, 786432\nMvav 180,128,100\n"
"v_lshlrev_b32 v188, 16, v214\nSv 164,1,16,5,1024\nMvav 180,144,36\nRa 128,0,16384\nv_and_b32 v189, 0xffff0000, v214\nMvav 180,148,44\nv_lshlrev_b32 v190, 16, v215\nCV 160,160,161\n"
"Mvav 180,152,52\nRa 132,0,17408\nCV 161,162,163\nMvav 180,156,60\nWL 6\ns_add_u32 m0, s89, 114688\ns_add_u32 s46, s96, 1376256\nMvav 176,156,56\nRa 136,0,18432\n"
"v_and_b32 v191, 0xffff0000, v215\nMvav 176,152,48\nD1 1,16,46,0\nCV 162,168,169\nMvav 176,148,40\nRa 140,0,19456\nCV 163,170,171\nMvav 176,144,32\nSv 160,1,16,5,0\n"
"Mvav 184,160,96\nRa 144,0,20480\nWV 12\nv_max_u32 v21, v219, v223\nv_cmp_eq_u32 vcc, -1, v21\ns_nop 0\ns_cbranch_vccnz .S499\n.S500:\nMvav 184,164,104\n"
"v_lshlrev_b32 v192, 16, v216\nMvav 184,168,112\nRa 148,0,21504\nMvav 184,172,120\nWL 6\nv_and_b32 v193, 0xffff0000, v216\nMvav 188,172,124\nRa 152,0,22528\nMvav 188,168,116\n"
"D1 1,16,46,1024\nMvav 188,164,108\nRa 156,0,23552\nv_lshlrev_b32 v194, 16, v217\nMvav 188,160,100\nv_and_b32 v195, 0xffff0000, v217\nMvav 188,176,36\nRa 160,0,24576\n"
"v_lshlrev_b32 v196, 16, v220\nMvav 188,180,44\nv_and_b32 v197, 0xffff0000, v220\ns_add_u32 s4, s92, 393216\nMvav 188,184,52\nRa 164,0,25600\nMvav 188,188,60\nWL 6\n"
"v_lshlrev_b32 v198, 16, v221\nbuffer_load_dwordx4 v[208:211], v1, s[16:19], s4 offen offset:0 sc0 sc1\nMvav 184,188,56\nRa 168,0,26624\nMvav 184,184,48\nD1 1,16,46,2048\n"
"Mvav 184,180,40\nRa 172,0,27648\nv_and_b32 v199, 0xffff0000, v221\nMvav 184,176,32\nv_lshlrev_b32 v200, 16, v218\n"
"buffer_load_dwordx4 v[212:215], v1, s[16:19], s4 offen offset:1024 sc0 sc1\nMvav 192,128,96\nRa 176,0,28672\nv_and_b32 v201, 0xffff0000, v218\nMvav 192,132,104\n"
"v_lshlrev_b32 v202, 16, v219\nCV 180,180,181\nMvav 192,136,112\nRa 180,0,29696\nCV 181,182,183\nMvav 192,140,120\ns_waitcnt vmcnt(15) lgkmcnt(6)\nCV 182,188,189\nMvav 196,140,124\n"
"Ra 184,0,30720\nv_and_b32 v203, 0xffff0000, v219\nMvav 196,136,116\nD1 1,16,46,3072\nCV 183,190,191\nMvav 196,132,108\nRa 188,0,31744\ns_barrier\nMvav 196,128,100\n"
"v_lshlrev_b32 v204, 16, v222\ns_add_u32 s5, s94, 917504\nMvav 196,144,36\nRa 128,2,16384\nv_and_b32 v205, 0xffff0000, v222\nMvav 196,148,44\nv_lshlrev_b32 v206, 16, v223\n"
"Sv 180,1,16,5,1024\nMvav 196,152,52\nRa 132,2,17408\nCV 176,176,177\nMvav 196,156,60\nWL 6\ns_add_u32 m0, s89, 0\ns_add_u32 s46, s96, 1409024\nMvav 192,156,56\nRa 136,2,18432\n"
"v_and_b32 v207, 0xffff0000, v223\nMvav 192,152,48\nD1 1,16,46,0\nCV 177,178,179\nMvav 192,148,40\nRa 140,2,19456\nCV 178,184,185\nMvav 192,144,32\nCV 179,186,187\nSv 176,1,16,5,0\n"
"Mvav 200,160,96\nRa 144,2,20480\nWV 12\nv_max_u32 v21, v227, v231\nv_cmp_eq_u32 vcc, -1, v21\ns_nop 0\ns_cbranch_vccnz .S502\n.S503:\nMvav 200,164,104\n"
"v_lshlrev_b32 v160, 16, v224\nMvav 200,168,112\nRa 148,2,21504\nMvav 200,172,120\nWL 6\nv_and_b32 v161, 0xffff0000, v224\nMvav 204,172,124\nRa 152,2,22528\nMvav 204,168,116\n"
"D1 1,16,46,1024\nMvav 204,164,108\nRa 156,2,23552\nv_lshlrev_b32 v162, 16, v225\nMvav 204,160,100\nv_and_b32 v163, 0xffff0000, v225\nMvav 204,176,36\nRa 160,2,24576\n"
"v_lshlrev_b32 v164, 16, v228\nMvav 204,180,44\nv_and_b32 v165, 0xffff0000, v228\ns_add_u32 s4, s92, 524288\nMvav 204,184,52\nRa 164,2,25600\nMvav 204,188,60\nWL 6\n"
"v_lshlrev_b32 v166, 16, v229\nbuffer_load_dwordx4 v[216:219], v1, s[16:19], s4 offen offset:0 sc0 sc1\nMvav 200,188,56\nRa 168,2,26624\nMvav 200,184,48\nD1 1,16,46,2048\n"
"Mvav 200,180,40\nRa 172,2,27648\nv_and_b32 v167, 0xffff0000, v229\nMvav 200,176,32\nv_lshlrev_b32 v168, 16, v226\n"
"buffer_load_dwordx4 v[220:223], v1, s[16:19], s4 offen offset:1024 sc0 sc1\nMvav 160,128,96\nRa 176,2,28672\nv_and_b32 v169, 0xffff0000, v226\nMvav 160,132,104\n"
"v_lshlrev_b32 v170, 16, v227\nCV 196,196,197\nMvav 160,136,112\nRa 180,2,29696\nCV 197,198,199\n.W597:\ns_cbranch_execz .WB416\nMvav 160,140,120\nWL 6\nCV 198,204,205\n"
"Mvav 164,140,124\nRa 184,2,30720\nv_and_b32 v24, 0x70, v1\nMvav 164,136,116\nD1 1,16,46,3072\nCV 199,206,207\nMvav 164,132,108\nRa 188,2,31744\nv_and_b32 v171, 0xffff0000, v227\n"
"Mvav 164,128,100\nv_lshlrev_b32 v172, 16, v230\ns_add_u32 s5, s94, 1048576\nMvav 164,144,36\nRa 128,2,0\nv_lshlrev_b32 v24, 5, v24\nMvav 164,148,44\n"
"v_and_b32 v173, 0xffff0000, v230\nSv 196,1,16,5,1024\nMvav 164,152,52\nRa 132,2,1024\nCV 192,192,193\nMvav 164,156,60\nWL 6\nv_lshlrev_b32 v174, 16, v231\nMvav 160,156,56\n"
"Ra 136,2,2048\nbuffer_load_dword v27, v24, s[16:19], s41 offen sc0 sc1\nMvav 160,152,48\nv_and_b32 v175, 0xffff0000, v231\nCV 193,194,195\nMvav 160,148,40\nRa 140,2,3072\n"
"CV 194,200,201\nMvav 160,144,32\nCV 195,202,203\nSv 192,1,16,5,0\nMvav 168,160,96\nRa 144,2,4096\nWV 12\nv_max_u32 v21, v211, v215\nv_cmp_eq_u32 vcc, -1, v21\ns_nop 0\n"
"s_cbranch_vccnz .S505\n.S506:\nMvav 168,164,104\nv_lshlrev_b32 v176, 16, v208\nMvav 168,168,112\nRa 148,2,5120\nMvav 168,172,120\nWL 6\nMvav 172,172,124\nRa 152,2,6144\n"
"v_and_b32 v177, 0xffff0000, v208\nMvav 172,168,116\nv_lshlrev_b32 v178, 16, v209\nMvav 172,164,108\nRa 156,2,7168\nMvav 172,160,100\nv_and_b32 v179, 0xffff0000, v209\n"
"Mvav 172,176,36\nRa 160,2,8192\nv_lshlrev_b32 v180, 16, v212\nMvav 172,180,44\nv_and_b32 v181, 0xffff0000, v212\ns_add_u32 s4, s92, 655360\nMvav 172,184,52\nRa 164,2,9216\n"
"Mvav 172,188,60\nWL 6\nbuffer_load_dwordx4 v[224:227], v1, s[16:19], s4 offen offset:0 sc0 sc1\nMvav 168,188,56\nRa 168,2,10240\nv_lshlrev_b32 v182, 16, v213\nMvav 168,184,48\n"
"v_and_b32 v183, 0xffff0000, v213\nMvav 168,180,40\nRa 172,2,11264\nMvav 168,176,32\nv_lshlrev_b32 v184, 16, v210\n"
"buffer_load_dwordx4 v[228:231], v1, s[16:19], s4 offen offset:1024 sc0 sc1\nMvav 176,128,96\nRa 176,2,12288\nv_and_b32 v185, 0xffff0000, v210\nMvav 176,132,104\n"
"v_lshlrev_b32 v186, 16, v211\nCV 164,164,165\nMvav 176,136,112\nRa 180,2,13312\nCV 165,166,167\nMvav 176,140,120\ns_waitcnt vmcnt(13) lgkmcnt(6)\nCV 166,172,173\nMvav 180,140,124\n"
"Ra 184,2,14336\nCV 167,174,175\nMvav 180,136,116\nv_and_b32 v187, 0xffff0000, v211\ns_add_u32 s5, s94, 1179648\nMvav 180,132,108\nRa 188,2,15360\ns_barrier\nMvav 180,128,100\n"
"v_lshlrev_b32 v188, 16, v214\nSv 164,1,16,5,1024\nMvav 180,144,36\nRa 128,2,32768\nv_and_b32 v189, 0xffff0000, v214\nMvav 180,148,44\nv_lshlrev_b32 v190, 16, v215\nCV 160,160,161\n"
"Mvav 180,152,52\nRa 132,2,33792\nCV 161,162,163\nMvav 180,156,60\nWL 6\nCV 162,168,169\nMvav 176,156,56\nRa 136,2,34816\nCV 163,170,171\nMvav 176,152,48\n"
"v_and_b32 v191, 0xffff0000, v215\nSv 160,1,16,5,0\nMvav 176,148,40\nRa 140,2,35840\nMvav 176,144,32\nMvav 184,160,96\nRa 144,2,36864\nWV 8\nv_max_u32 v21, v219, v223\n"
"v_cmp_eq_u32 vcc, -1, v21\ns_nop 0\ns_cbranch_vccnz .S508\n.S509:\nMvav 184,164,104\nv_lshlrev_b32 v192, 16, v216\nMvav 184,168,112\nRa 148,2,37888\nMvav 184,172,120\nWL 6\n"
"Mvav 188,172,124\nRa 152,2,38912\nv_and_b32 v193, 0xffff0000, v216\nMvav 188,168,116\nv_lshlrev_b32 v194, 16, v217\nMvav 188,164,108\nRa 156,2,39936\nMvav 188,160,100\n"
"v_and_b32 v195, 0xffff0000, v217\nMvav 188,176,36\nRa 160,2,40960\nv_lshlrev_b32 v196, 16, v220\nMvav 188,180,44\nv_and_b32 v197, 0xffff0000, v220\ns_add_u32 s4, s92, 786432\n"
"Mvav 188,184,52\nRa 164,2,41984\nMvav 188,188,60\nWL 6\nbuffer_load_dwordx4 v[208:211], v1, s[16:19], s4 offen offset:0 sc0 sc1\nMvav 184,188,56\nRa 168,2,43008\n"
"v_lshlrev_b32 v198, 16, v221\nMvav 184,184,48\nv_and_b32 v199, 0xffff0000, v221\nMvav 184,180,40\nRa 172,2,44032\nMvav 184,176,32\nv_lshlrev_b32 v200, 16, v218\n"
"buffer_load_dwordx4 v[212:215], v1, s[16:19], s4 offen offset:1024 sc0 sc1\nMvav 192,128,96\nRa 176,2,45056\nv_and_b32 v201, 0xffff0000, v218\nMvav 192,132,104\n"
"v_lshlrev_b32 v202, 16, v219\nCV 180,180,181\nMvav 192,136,112\nRa 180,2,46080\nCV 181,182,183\nMvav 192,140,120\nWL 6\nCV 182,188,189\nMvav 196,140,124\nRa 184,2,47104\n"
"CV 183,190,191\nMvav 196,136,116\nv_and_b32 v203, 0xffff0000, v219\ns_add_u32 s5, s94, 1310720\nMvav 196,132,108\nRa 188,2,48128\nSv 180,1,16,5,1024\nMvav 196,128,100\n"
"v_lshlrev_b32 v204, 16, v222\nCV 176,176,177\nMvav 196,144,36\nRa 128,2,49152\nv_and_b32 v205, 0xffff0000, v222\nMvav 196,148,44\nv_lshlrev_b32 v206, 16, v223\nCV 177,178,179\n"
"Mvav 196,152,52\nRa 132,2,50176\nCV 178,184,185\nMvav 196,156,60\nWL 6\nCV 179,186,187\nMvav 192,156,56\nRa 136,2,51200\nSv 176,1,16,5,0\nMvav 192,152,48\n"
"v_and_b32 v207, 0xffff0000, v223\nMvav 192,148,40\nRa 140,2,52224\nMvav 192,144,32\nMvav 200,160,96\nRa 144,2,53248\nWV 6\nv_max_u32 v21, v227, v231\nv_cmp_eq_u32 vcc, -1, v21\n"
"s_nop 0\ns_cbranch_vccnz .S511\n.S512:\nMvav 200,164,104\nv_lshlrev_b32 v160, 16, v224\nv_xor_b32 v27, s15, v27\nv_and_b32 v26, 0xffffdfff, v27\nv_cmp_ne_u32 s[64:65], 0, v27\n"
"v_cmp_ne_u32 vcc, 0, v26\ns_nop 0\ns_cbranch_vccnz .S523\n.S524:\nMvav 200,168,112\nRa 148,2,54272\ns_bitcmp1_b32 s64, 0\ns_cselect_b32 s2, 0x2000, 0\nMvav 200,172,120\nWL 6\n"
"Mvav 204,172,124\nRa 152,2,55296\nv_and_b32 v161, 0xffff0000, v224\nMvav 204,168,116\nv_lshlrev_b32 v162, 16, v225\nMvav 204,164,108\nRa 156,2,56320\ns_xor_b32 s2, s2, s20\n"
"Mvav 204,160,100\nv_and_b32 v163, 0xffff0000, v225\nMvav 204,176,36\nRa 160,2,57344\nv_lshlrev_b32 v164, 16, v228\nMvav 204,180,44\nv_and_b32 v165, 0xffff0000, v228\n"
"Mvav 204,184,52\nRa 164,2,58368\nMvav 204,188,60\nWL 6\ns_add_u32 s24, s2, 0x0\nMvav 200,188,56\nRa 168,2,59392\nv_lshlrev_b32 v166, 16, v229\nMvav 200,184,48\n"
"v_and_b32 v167, 0xffff0000, v229\ns_bitcmp1_b32 s64, 1\ns_cselect_b32 s2, 0x2000, 0\nMvav 200,180,40\nRa 172,2,60416\nMvav 200,176,32\nv_lshlrev_b32 v168, 16, v226\n"
"Mvav 160,128,96\nRa 176,2,61440\nv_and_b32 v169, 0xffff0000, v226\nMvav 160,132,104\nv_lshlrev_b32 v170, 16, v227\nCV 196,196,197\nMvav 160,136,112\nRa 180,2,62464\n"
"CV 197,198,199\nMvav 160,140,120\nWL 6\nCV 198,204,205\nMvav 164,140,124\nRa 184,2,63488\nCV 199,206,207\nMvav 164,136,116\nv_and_b32 v171, 0xffff0000, v227\n"
"s_add_u32 s5, s94, 1441792\nMvav 164,132,108\nRa 188,2,64512\ns_barrier\nMvav 164,128,100\nv_lshlrev_b32 v172, 16, v230\nSv 196,1,16,5,1024\nMvav 164,144,36\nRa 128,3,0\n"
"v_and_b32 v173, 0xffff0000, v230\nMvav 164,148,44\nv_lshlrev_b32 v174, 16, v231\nCV 192,192,193\nMvav 164,152,52\nRa 132,3,1024\nCV 193,194,195\nMvav 164,156,60\nWL 6\n"
"s_xor_b32 s2, s2, s20\nMvav 160,156,56\nRa 136,3,2048\nCV 194,200,201\nMvav 160,152,48\nv_and_b32 v175, 0xffff0000, v231\nCV 195,202,203\nMvav 160,148,40\nRa 140,3,3072\n"
"Sv 192,1,16,5,0\nMvav 160,144,32\ns_add_u32 s25, s2, 0x1800000\ns_bitcmp1_b32 s64, 2\ns_cselect_b32 s2, 0x2000, 0\nMvav 168,160,96\nRa 144,3,4096\nWV 4\nv_max_u32 v21, v211, v215\n"
"v_cmp_eq_u32 vcc, -1, v21\ns_nop 0\ns_cbranch_vccnz .S514\n.S515:\nMvav 168,164,104\nv_lshlrev_b32 v176, 16, v208\nMvav 168,168,112\nRa 148,3,5120\nMvav 168,172,120\nWL 6\n"
"Mvav 172,172,124\nRa 152,3,6144\nv_and_b32 v177, 0xffff0000, v208\nMvav 172,168,116\nv_lshlrev_b32 v178, 16, v209\nMvav 172,164,108\nRa 156,3,7168\ns_xor_b32 s2, s2, s20\n"
"Mvav 172,160,100\nv_and_b32 v179, 0xffff0000, v209\nMvav 172,176,36\nRa 160,3,8192\nv_lshlrev_b32 v180, 16, v212\nMvav 172,180,44\nv_and_b32 v181, 0xffff0000, v212\n"
"Mvav 172,184,52\nRa 164,3,9216\nMvav 172,188,60\nWL 6\ns_add_u32 s26, s2, 0x3000000\nMvav 168,188,56\nRa 168,3,10240\nv_lshlrev_b32 v182, 16, v213\nMvav 168,184,48\n"
"v_and_b32 v183, 0xffff0000, v213\ns_bitcmp1_b32 s64, 3\ns_cselect_b32 s2, 0x2000, 0\nMvav 168,180,40\nRa 172,3,11264\nMvav 168,176,32\nv_lshlrev_b32 v184, 16, v210\n"
"Mvav 176,128,96\nRa 176,3,12288\ns_xor_b32 s2, s2, s20\nMvav 176,132,104\nv_and_b32 v185, 0xffff0000, v210\nCV 164,164,165\nMvav 176,136,112\nRa 180,3,13312\nCV 165,166,167\n"
"Mvav 176,140,120\nWL 6\nCV 166,172,173\nMvav 180,140,124\nRa 184,3,14336\nCV 167,174,175\nMvav 180,136,116\nv_lshlrev_b32 v186, 16, v211\ns_add_u32 s5, s94, 1572864\n"
"Mvav 180,132,108\nRa 188,3,15360\ns_add_u32 s27, s2, 0x4800000\nMvav 180,128,100\nv_and_b32 v187, 0xffff0000, v211\ns_bitcmp1_b32 s64, 4\ns_cselect_b32 s2, 0x2000, 0\n"
"Mvav 180,144,36\nv_lshlrev_b32 v188, 16, v214\nSv 164,1,16,5,1024\nMvav 180,148,44\nv_and_b32 v189, 0xffff0000, v214\nCV 160,160,161\nMvav 180,152,52\n"
"v_lshlrev_b32 v190, 16, v215\nCV 161,162,163\nMvav 180,156,60\nWL 4\ns_xor_b32 s2, s2, s20\nMvav 176,156,56\nv_and_b32 v191, 0xffff0000, v215\nCV 162,168,169\nMvav 176,152,48\n"
"s_add_u32 s32, s2, 0x6000000\ns_bitcmp1_b32 s64, 5\ns_cselect_b32 s2, 0x2000, 0\nMvav 176,148,40\ns_xor_b32 s2, s2, s20\nCV 163,170,171\nMvav 176,144,32\n"
"s_add_u32 s33, s2, 0x7800000\ns_bitcmp1_b32 s64, 6\ns_cselect_b32 s2, 0x2000, 0\nMvav 184,160,96\ns_xor_b32 s2, s2, s20\ns_add_u32 s34, s2, 0x9000000\nMvav 184,164,104\n"
"s_bitcmp1_b32 s64, 7\ns_cselect_b32 s2, 0x2000, 0\nMvav 184,168,112\ns_xor_b32 s2, s2, s20\nSv 160,1,16,5,0\nMvav 184,172,120\nWL 0\nMvav 188,172,124\n"
"s_add_u32 s35, s2, 0xa800000\nMvav 188,168,116\nMvav 188,164,108\nMvav 188,160,100\nMvav 188,176,36\nMvav 188,180,44\nMvav 188,184,52\nMvav 188,188,60\nMvav 184,188,56\n"
"Mvav 184,184,48\nMvav 184,180,40\nMvav 184,176,32\nCV 180,180,181\nCV 181,182,183\ns_nop 1\nCV 182,188,189\nCV 183,190,191\ns_add_u32 s5, s94, 1703936\nSv 180,1,16,5,1024\n"
"CV 176,176,177\nCV 177,178,179\nCV 178,184,185\nCV 179,186,187\nSv 176,1,16,5,0\ns_cmp_eq_u32 s13, 7\ns_cbranch_scc1 .Z526\n"
"buffer_load_dwordx4 v[12:15], v91, s[28:31], s22 offen offset:0\nbuffer_load_dwordx4 v[16:19], v91, s[28:31], s23 offen offset:0\n"
"buffer_load_dwordx4 v[64:67], v89, s[16:19], s40 offen offset:0 sc0 sc1\nbuffer_load_dwordx4 v[68:71], v89, s[16:19], s40 offen offset:16 sc0 sc1\n"
"buffer_load_dwordx4 v[72:75], v89, s[16:19], s40 offen offset:1792 sc0 sc1\nbuffer_load_dwordx4 v[76:79], v89, s[16:19], s40 offen offset:1808 sc0 sc1\n"
"buffer_load_dwordx2 v[80:81], v88, s[16:19], s63 offen offset:0 sc0 sc1\nbuffer_load_dwordx2 v[82:83], v88, s[16:19], s63 offen offset:512 sc0 sc1\n"
"buffer_load_dwordx4 v[96:99], v1, s[16:19], s24 offen offset:0 sc0 sc1\nbuffer_load_dwordx4 v[100:103], v1, s[16:19], s24 offen offset:1024 sc0 sc1\n"
"buffer_load_dwordx4 v[104:107], v1, s[16:19], s25 offen offset:0 sc0 sc1\nbuffer_load_dwordx4 v[108:111], v1, s[16:19], s25 offen offset:1024 sc0 sc1\n"
"buffer_load_dwordx4 v[112:115], v1, s[16:19], s26 offen offset:0 sc0 sc1\nbuffer_load_dwordx4 v[116:119], v1, s[16:19], s26 offen offset:1024 sc0 sc1\n"
"buffer_load_dwordx4 v[120:123], v1, s[16:19], s27 offen offset:0 sc0 sc1\nbuffer_load_dwordx4 v[124:127], v1, s[16:19], s27 offen offset:1024 sc0 sc1\n"
"buffer_load_dwordx4 v[32:35], v1, s[16:19], s32 offen offset:0 sc0 sc1\nbuffer_load_dwordx4 v[36:39], v1, s[16:19], s32 offen offset:1024 sc0 sc1\n"
"buffer_load_dwordx4 v[40:43], v1, s[16:19], s33 offen offset:0 sc0 sc1\nbuffer_load_dwordx4 v[44:47], v1, s[16:19], s33 offen offset:1024 sc0 sc1\n"
"buffer_load_dwordx4 v[48:51], v1, s[16:19], s34 offen offset:0 sc0 sc1\nbuffer_load_dwordx4 v[52:55], v1, s[16:19], s34 offen offset:1024 sc0 sc1\n"
"buffer_load_dwordx4 v[56:59], v1, s[16:19], s35 offen offset:0 sc0 sc1\nbuffer_load_dwordx4 v[60:63], v1, s[16:19], s35 offen offset:1024 sc0 sc1\nWV 16\nv_xor_b32 v66, s15, v66\n"
"v_xor_b32 v70, s15, v70\nv_xor_b32 v74, s15, v74\nv_xor_b32 v78, s15, v78\nv_xor_b32 v81, s15, v81\nv_xor_b32 v83, s15, v83\nv_or3_b32 v92, v66, v70, v74\n"
"v_or3_b32 v92, v92, v78, v81\nv_or_b32 v92, v92, v83\nv_cmp_ne_u32 vcc, 0, v92\ns_nop 0\ns_cbranch_vccnz .S517\n.S518:\nv_and_b32 v92, 0x7ff, v67\nv_lshrrev_b32 v93, 16, v67\n"
"v_cmp_eq_u32 s[44:45], v92, v90\nv_cmp_eq_u32 s[46:47], v93, v90\nv_cmp_eq_u32 s[48:49], v92, v94\nv_cmp_eq_u32 s[50:51], v93, v94\nv_cndmask_b32 v12, v12, v64, s[44:45]\n"
"v_cndmask_b32 v13, v13, v65, s[44:45]\nv_cndmask_b32 v14, v14, v68, s[44:45]\nv_cndmask_b32 v15, v15, v69, s[44:45]\nv_cndmask_b32 v12, v12, v72, s[46:47]\n"
"v_cndmask_b32 v13, v13, v73, s[46:47]\nv_cndmask_b32 v14, v14, v76, s[46:47]\nv_cndmask_b32 v15, v15, v77, s[46:47]\nv_cndmask_b32 v16, v16, v64, s[48:49]\n"
"v_cndmask_b32 v17, v17, v65, s[48:49]\nv_cndmask_b32 v18, v18, v68, s[48:49]\nv_cndmask_b32 v19, v19, v69, s[48:49]\nv_cndmask_b32 v16, v16, v72, s[50:51]\n"
"v_cndmask_b32 v17, v17, v73, s[50:51]\nv_cndmask_b32 v18, v18, v76, s[50:51]\nv_cndmask_b32 v19, v19, v77, s[50:51]\nv_sub_f32 v92, 1.0, v80\nv_sub_f32 v93, 1.0, v82\nCV 92,92,93\n"
"v_and_b32 v93, 0xffff0000, v92\nv_lshlrev_b32 v92, 16, v92\nv_lshlrev_b32 v64, 16, v12\nv_and_b32 v65, 0xffff0000, v12\nMU 64,64,92\nMU 65,65,92\nv_lshlrev_b32 v66, 16, v13\n"
"v_and_b32 v67, 0xffff0000, v13\nMU 66,66,92\nMU 67,67,92\nv_lshlrev_b32 v68, 16, v14\nv_and_b32 v69, 0xffff0000, v14\nMU 68,68,92\nMU 69,69,92\nv_lshlrev_b32 v70, 16, v15\n"
"v_and_b32 v71, 0xffff0000, v15\nMU 70,70,92\nMU 71,71,92\nv_lshlrev_b32 v72, 16, v16\nv_and_b32 v73, 0xffff0000, v16\nMU 72,72,93\nMU 73,73,93\nv_lshlrev_b32 v74, 16, v17\n"
"v_and_b32 v75, 0xffff0000, v17\nMU 74,74,93\nMU 75,75,93\nv_lshlrev_b32 v76, 16, v18\nv_and_b32 v77, 0xffff0000, v18\nMU 76,76,93\nMU 77,77,93\nv_lshlrev_b32 v78, 16, v19\n"
"v_and_b32 v79, 0xffff0000, v19\nMU 78,78,93\nMU 79,79,93\ns_mov_b32 s7, 262144\nWV 0\n.Z529:\nv_max_u32 v84, v99, v103\nv_cmp_eq_u32 s[44:45], -1, v84\nv_max_u32 v84, v107, v111\n"
"v_cmp_eq_u32 s[46:47], -1, v84\nv_max_u32 v84, v115, v119\nv_cmp_eq_u32 s[48:49], -1, v84\nv_max_u32 v84, v123, v127\nv_cmp_eq_u32 s[50:51], -1, v84\nv_max_u32 v84, v35, v39\n"
"v_cmp_eq_u32 s[52:53], -1, v84\nv_max_u32 v84, v43, v47\nv_cmp_eq_u32 s[54:55], -1, v84\nv_max_u32 v84, v51, v55\nv_cmp_eq_u32 s[56:57], -1, v84\nv_max_u32 v84, v59, v63\n"
"v_cmp_eq_u32 s[58:59], -1, v84\ns_or_b64 s[0:1], s[44:45], s[46:47]\ns_or_b64 s[2:3], s[48:49], s[50:51]\ns_or_b64 s[0:1], s[0:1], s[52:53]\ns_or_b64 s[2:3], s[2:3], s[54:55]\n"
"s_or_b64 s[0:1], s[0:1], s[56:57]\ns_or_b64 s[2:3], s[2:3], s[58:59]\ns_or_b64 s[0:1], s[0:1], s[2:3]\ns_cbranch_scc1 .Z527\n.Z528:\nZvvv 136,6,96\nMvvv 136,6,104\nMvvv 136,6,112\n"
"Mvvv 136,6,120\nMvvv 136,6,32\nMvvv 136,6,40\nMvvv 136,6,48\nMvvv 136,6,56\nZvvv 140,4,96\nMvvv 140,4,104\nMvvv 140,4,112\nMvvv 140,4,120\nMvvv 140,4,32\nMvvv 140,4,40\n"
"Mvvv 140,4,48\nMvvv 140,4,56\nZvvv 144,6,100\nMvvv 144,6,108\nMvvv 144,6,116\n.W598:\ns_cbranch_execz .WB416\nMvvv 144,6,124\nMvvv 144,6,36\nMvvv 144,6,44\nMvvv 144,6,52\n"
"Mvvv 144,6,60\nZvvv 148,4,100\nMvvv 148,4,108\nMvvv 148,4,116\nMvvv 148,4,124\nMvvv 148,4,36\nMvvv 148,4,44\nMvvv 148,4,52\nMvvv 148,4,60\nFM 64,80,136\nFM 65,80,137\n"
"FM 66,80,138\nFM 67,80,139\nFM 68,80,140\nFM 69,80,141\nFM 70,80,142\nFM 71,80,143\nCV 12,64,65\nCV 13,66,67\nCV 14,68,69\nCV 15,70,71\nSv 12,91,36,22,0\nFM 72,82,144\n"
"FM 73,82,145\nFM 74,82,146\nFM 75,82,147\nFM 76,82,148\nFM 77,82,149\nFM 78,82,150\nFM 79,82,151\nCV 16,72,73\nCV 17,74,75\nCV 18,76,77\nCV 19,78,79\nSv 16,91,36,23,0\n.Z526:\n"
"s_endpgm\n.EP538:\n.S412:\ns_mov_b32 s7, 262144\n.S414:\ns_lshl_b32 s5, s73, 8\ns_add_u32 s5, s5, 0xc04ac00\nv_lshrrev_b32 v27, 2, v1\n"
"buffer_load_dword v27, v27, s[16:19], s5 offen sc0 sc1\nWV 0\nv_xor_b32 v27, s15, v27\nv_and_b32 v32, 0xffffdfff, v27\nv_cmp_ne_u32 vcc, 0, v32\ns_nop 0\ns_cbranch_vccz .S413\n"
"s_sleep 6\ns_sub_u32 s7, s7, 1\ns_cbranch_scc0 .S414\ns_branch .S413\n.T421:\ns_mov_b32 s62, 1\ns_call_b64 s[80:81], .RA266\ns_branch .T422\n.T423:\ns_mov_b32 s62, 1\n"
"s_call_b64 s[80:81], .RA266\ns_branch .T424\n.S426:\ns_mov_b32 s7, 262144\n.S428:\nbuffer_load_dword v27, off, s[16:19], s5 sc0 sc1\nWV 0\nv_cmp_ne_u32 vcc, s15, v27\ns_nop 0\n"
"s_cbranch_vccz .S427\ns_sleep 6\ns_sub_u32 s7, s7, 1\ns_cbranch_scc0 .S428\ns_branch .S427\n.S429:\ns_mov_b32 s7, 262144\n.S431:\nv_and_b32 v27, 0x70, v1\n"
"buffer_load_dword v27, v27, s[16:19], s61 offen offset:128 sc0 sc1\nWV 0\nv_cmp_ne_u32 vcc, s15, v27\ns_nop 0\ns_cbranch_vccz .S430\ns_sleep 6\ns_sub_u32 s7, s7, 1\n"
"s_cbranch_scc0 .S431\ns_branch .S430\n.S433:\ns_mov_b32 s7, 262144\n.S435:\nv_and_b32 v27, 0x70, v1\nbuffer_load_dword v27, v27, s[16:19], s61 offen offset:136 sc0 sc1\nWV 0\n"
"v_cmp_ne_u32 vcc, s15, v27\ns_nop 0\ns_cbranch_vccz .S434\ns_sleep 6\ns_sub_u32 s7, s7, 1\ns_cbranch_scc0 .S435\ns_branch .S434\n.S436:\ns_mov_b32 s7, 262144\n.S438:\n"
"v_and_b32 v27, 0x70, v1\nbuffer_load_dword v27, v27, s[16:19], s61 offen offset:140 sc0 sc1\nWV 0\nv_cmp_ne_u32 vcc, s15, v27\ns_nop 0\ns_cbranch_vccz .S437\ns_sleep 6\n"
"s_sub_u32 s7, s7, 1\ns_cbranch_scc0 .S438\ns_branch .S437\n.S439:\ns_mov_b32 s7, 262144\n.S441:\nv_and_b32 v27, 0x70, v1\n"
"buffer_load_dword v27, v27, s[16:19], s61 offen offset:12 sc0 sc1\nWV 0\nv_cmp_ne_u32 vcc, s15, v27\ns_nop 0\ns_cbranch_vccz .S440\ns_sleep 6\ns_sub_u32 s7, s7, 1\n"
"s_cbranch_scc0 .S441\ns_branch .S440\n.S442:\ns_mov_b32 s7, 262144\n.S444:\nv_and_b32 v27, 0x70, v1\nbuffer_load_dword v27, v27, s[16:19], s61 offen offset:8 sc0 sc1\nWV 0\n"
"v_cmp_ne_u32 vcc, s15, v27\ns_nop 0\ns_cbranch_vccz .S443\ns_sleep 6\ns_sub_u32 s7, s7, 1\ns_cbranch_scc0 .S444\ns_branch .S443\n.S445:\ns_mov_b32 s7, 262144\n.S447:\n"
"v_and_b32 v27, 0xf0, v1\nbuffer_load_dword v27, v27, s[16:19], s61 offen offset:4 sc0 sc1\nWV 0\nv_cmp_ne_u32 vcc, s15, v27\ns_nop 0\ns_cbranch_vccz .S446\ns_sleep 6\n"
"s_sub_u32 s7, s7, 1\ns_cbranch_scc0 .S447\ns_branch .S446\n.T456:\ns_mov_b32 s62, 0\ns_call_b64 s[80:81], .RB267\ns_branch .T457\n.T458:\ns_mov_b32 s62, 0\n"
"s_call_b64 s[80:81], .RA266\ns_branch .T459\n.T462:\ns_mov_b32 s62, 0\ns_call_b64 s[80:81], .RB267\ns_branch .T463\n.T464:\ns_mov_b32 s62, 0\ns_call_b64 s[80:81], .RA266\n"
"s_branch .T465\n.T468:\ns_mov_b32 s62, 2\ns_call_b64 s[80:81], .RB267\ns_branch .T469\n.T470:\ns_mov_b32 s62, 5\ns_call_b64 s[80:81], .RA266\ns_branch .T471\n.T472:\n"
"s_mov_b32 s62, 5\ns_call_b64 s[80:81], .RA266\ns_branch .T473\n.T484:\ns_mov_b32 s62, 4\ns_call_b64 s[80:81], .RB267\ns_branch .T485\n.T486:\ns_mov_b32 s62, 4\n"
"s_call_b64 s[80:81], .RA266\ns_branch .T487\n.T490:\ns_mov_b32 s62, 4\ns_call_b64 s[80:81], .RB267\ns_branch .T491\n.T492:\ns_mov_b32 s62, 4\ns_call_b64 s[80:81], .RA266\n"
"s_branch .T493\n.T494:\ns_mov_b32 s62, 6\ns_call_b64 s[80:81], .RB267\ns_branch .T495\n.S496:\ns_mov_b32 s7, 262144\n.S498:\ns_add_u32 s0, s92, 0\n"
"buffer_load_dwordx4 v[208:211], v1, s[16:19], s0 offen offset:0 sc0 sc1\nbuffer_load_dwordx4 v[212:215], v1, s[16:19], s0 offen offset:1024 sc0 sc1\nWV 0\n"
"v_max_u32 v21, v211, v215\nv_cmp_eq_u32 vcc, -1, v21\ns_nop 0\ns_cbranch_vccz .S497\ns_sleep 6\ns_sub_u32 s7, s7, 1\ns_cbranch_scc0 .S498\ns_branch .S497\n.S499:\n"
"s_mov_b32 s7, 262144\n.S501:\ns_add_u32 s0, s92, 131072\nbuffer_load_dwordx4 v[216:219], v1, s[16:19], s0 offen offset:0 sc0 sc1\n"
"buffer_load_dwordx4 v[220:223], v1, s[16:19], s0 offen offset:1024 sc0 sc1\nWV 0\nv_max_u32 v21, v219, v223\nv_cmp_eq_u32 vcc, -1, v21\ns_nop 0\ns_cbranch_vccz .S500\ns_sleep 6\n"
"s_sub_u32 s7, s7, 1\ns_cbranch_scc0 .S501\ns_branch .S500\n.S502:\ns_mov_b32 s7, 262144\n.S504:\ns_add_u32 s0, s92, 262144\n"
"buffer_load_dwordx4 v[224:227], v1, s[16:19], s0 offen offset:0 sc0 sc1\nbuffer_load_dwordx4 v[228:231], v1, s[16:19], s0 offen offset:1024 sc0 sc1\nWV 0\n"
"v_max_u32 v21, v227, v231\nv_cmp_eq_u32 vcc, -1, v21\ns_nop 0\ns_cbranch_vccz .S503\ns_sleep 6\ns_sub_u32 s7, s7, 1\ns_cbranch_scc0 .S504\ns_branch .S503\n.S505:\n"
"s_mov_b32 s7, 262144\n.S507:\ns_add_u32 s0, s92, 393216\nbuffer_load_dwordx4 v[208:211], v1, s[16:19], s0 offen offset:0 sc0 sc1\n"
"buffer_load_dwordx4 v[212:215], v1, s[16:19], s0 offen offset:1024 sc0 sc1\nWV 0\nv_max_u32 v21, v211, v215\nv_cmp_eq_u32 vcc, -1, v21\ns_nop 0\ns_cbranch_vccz .S506\ns_sleep 6\n"
"s_sub_u32 s7, s7, 1\ns_cbranch_scc0 .S507\ns_branch .S506\n.S508:\ns_mov_b32 s7, 262144\n.S510:\ns_add_u32 s0, s92, 524288\n"
"buffer_load_dwordx4 v[216:219], v1, s[16:19], s0 offen offset:0 sc0 sc1\nbuffer_load_dwordx4 v[220:223], v1, s[16:19], s0 offen offset:1024 sc0 sc1\nWV 0\n"
"v_max_u32 v21, v219, v223\nv_cmp_eq_u32 vcc, -1, v21\ns_nop 0\ns_cbranch_vccz .S509\ns_sleep 6\ns_sub_u32 s7, s7, 1\ns_cbranch_scc0 .S510\ns_branch .S509\n.S511:\n"
"s_mov_b32 s7, 262144\n.S513:\ns_add_u32 s0, s92, 655360\nbuffer_load_dwordx4 v[224:227], v1, s[16:19], s0 offen offset:0 sc0 sc1\n"
"buffer_load_dwordx4 v[228:231], v1, s[16:19], s0 offen offset:1024 sc0 sc1\nWV 0\nv_max_u32 v21, v227, v231\nv_cmp_eq_u32 vcc, -1, v21\ns_nop 0\ns_cbranch_vccz .S512\ns_sleep 6\n"
"s_sub_u32 s7, s7, 1\ns_cbranch_scc0 .S513\ns_branch .S512\n.S514:\ns_mov_b32 s7, 262144\n.S516:\ns_add_u32 s0, s92, 786432\n"
"buffer_load_dwordx4 v[208:211], v1, s[16:19], s0 offen offset:0 sc0 sc1\nbuffer_load_dwordx4 v[212:215], v1, s[16:19], s0 offen offset:1024 sc0 sc1\nWV 0\n"
"v_max_u32 v21, v211, v215\nv_cmp_eq_u32 vcc, -1, v21\ns_nop 0\ns_cbranch_vccz .S515\ns_sleep 6\ns_sub_u32 s7, s7, 1\ns_cbranch_scc0 .S516\ns_branch .S515\n.S517:\n"
"s_mov_b32 s7, 262144\n.S519:\nbuffer_load_dwordx4 v[64:67], v89, s[16:19], s40 offen offset:0 sc0 sc1\nbuffer_load_dwordx4 v[68:71], v89, s[16:19], s40 offen offset:16 sc0 sc1\n"
"buffer_load_dwordx4 v[72:75], v89, s[16:19], s40 offen offset:1792 sc0 sc1\nbuffer_load_dwordx4 v[76:79], v89, s[16:19], s40 offen offset:1808 sc0 sc1\n"
"buffer_load_dwordx2 v[80:81], v88, s[16:19], s63 offen offset:0 sc0 sc1\nbuffer_load_dwordx2 v[82:83], v88, s[16:19], s63 offen offset:512 sc0 sc1\nWV 0\nv_xor_b32 v66, s15, v66\n"
"v_xor_b32 v70, s15, v70\nv_xor_b32 v74, s15, v74\nv_xor_b32 v78, s15, v78\nv_xor_b32 v81, s15, v81\nv_xor_b32 v83, s15, v83\nv_or3_b32 v92, v66, v70, v74\n"
"v_or3_b32 v92, v92, v78, v81\nv_or_b32 v92, v92, v83\nv_cmp_ne_u32 vcc, 0, v92\ns_nop 0\ns_cbranch_vccz .S518\ns_sleep 6\ns_sub_u32 s7, s7, 1\ns_cbranch_scc0 .S519\n"
"s_branch .S518\n.S520:\ns_mov_b32 s7, 262144\n.S522:\nbuffer_load_dword v27, off, s[16:19], s60 offset:512 sc0 sc1\nWV 0\nv_cmp_ne_u32 vcc, s15, v27\ns_nop 0\n"
"s_cbranch_vccz .S521\ns_sleep 6\ns_sub_u32 s7, s7, 1\ns_cbranch_scc0 .S522\ns_branch .S521\n.S523:\ns_mov_b32 s7, 262144\n.S525:\n"
"buffer_load_dword v27, v24, s[16:19], s41 offen sc0 sc1\nWV 0\nv_xor_b32 v27, s15, v27\nv_and_b32 v26, 0xffffdfff, v27\nv_cmp_ne_u32 s[64:65], 0, v27\nv_cmp_ne_u32 vcc, 0, v26\n"
"s_nop 0\ns_cbranch_vccz .S524\ns_sleep 6\ns_sub_u32 s7, s7, 1\ns_cbranch_scc0 .S525\ns_branch .S524\n.Z527:\ns_sub_u32 s7, s7, 1\ns_cbranch_scc1 .Z528\ns_cmp_lg_u64 s[44:45], 0\n"
"s_cbranch_scc0 .Z530\nbuffer_load_dwordx4 v[96:99], v1, s[16:19], s24 offen offset:0 sc0 sc1\nbuffer_load_dwordx4 v[100:103], v1, s[16:19], s24 offen offset:1024 sc0 sc1\n.Z530:\n"
"s_cmp_lg_u64 s[46:47], 0\ns_cbranch_scc0 .Z531\nbuffer_load_dwordx4 v[104:107], v1, s[16:19], s25 offen offset:0 sc0 sc1\n"
"buffer_load_dwordx4 v[108:111], v1, s[16:19], s25 offen offset:1024 sc0 sc1\n.Z531:\ns_cmp_lg_u64 s[48:49], 0\ns_cbranch_scc0 .Z532\n"
"buffer_load_dwordx4 v[112:115], v1, s[16:19], s26 offen offset:0 sc0 sc1\nbuffer_load_dwordx4 v[116:119], v1, s[16:19], s26 offen offset:1024 sc0 sc1\n.Z532:\n"
"s_cmp_lg_u64 s[50:51], 0\ns_cbranch_scc0 .Z533\nbuffer_load_dwordx4 v[120:123], v1, s[16:19], s27 offen offset:0 sc0 sc1\n"
"buffer_load_dwordx4 v[124:127], v1, s[16:19], s27 offen offset:1024 sc0 sc1\n.Z533:\ns_cmp_lg_u64 s[52:53], 0\ns_cbranch_scc0 .Z534\n"
"buffer_load_dwordx4 v[32:35], v1, s[16:19], s32 offen offset:0 sc0 sc1\nbuffer_load_dwordx4 v[36:39], v1, s[16:19], s32 offen offset:1024 sc0 sc1\n.Z534:\n"
"s_cmp_lg_u64 s[54:55], 0\ns_cbranch_scc0 .Z535\nbuffer_load_dwordx4 v[40:43], v1, s[16:19], s33 offen offset:0 sc0 sc1\n"
"buffer_load_dwordx4 v[44:47], v1, s[16:19], s33 offen offset:1024 sc0 sc1\n.Z535:\ns_cmp_lg_u64 s[56:57], 0\ns_cbranch_scc0 .Z536\n"
"buffer_load_dwordx4 v[48:51], v1, s[16:19], s34 offen offset:0 sc0 sc1\nbuffer_load_dwordx4 v[52:55], v1, s[16:19], s34 offen offset:1024 sc0 sc1\n.Z536:\n"
"s_cmp_lg_u64 s[58:59], 0\ns_cbranch_scc0 .Z537\nbuffer_load_dwordx4 v[56:59], v1, s[16:19], s35 offen offset:0 sc0 sc1\n"
"buffer_load_dwordx4 v[60:63], v1, s[16:19], s35 offen offset:1024 sc0 sc1\n.Z537:\ns_sleep 8\nWV 0\ns_branch .Z529\n.Lcode1:\n"
  :: "s"(__builtin_amdgcn_kernarg_segment_ptr()), "s"(blockIdx.x), "v"(threadIdx.x)
  : "memory", "scc", "vcc", "v1", "v2", "v3", "v4", "v5", "v6", "v7", "v8", "v9", "v10", "v11", "v12", "v13", "v14", "v15", "v16", "v17", "v18", "v19", "v20", "v21", "v22", "v23", "v24", "v25", "v26", "v27", "v28", "v29", "v30", "v31", "v32", "v33", "v34", "v35", "v36", "v37", "v38", "v39", "v40", "v41", "v42", "v43", "v44", "v45", "v46", "v47", "v48", "v49", "v50", "v51", "v52", "v53", "v54", "v55", "v56", "v57", "v58", "v59", "v60", "v61", "v62", "v63", "v64", "v65", "v66", "v67", "v68", "v69", "v70", "v71", "v72", "v73", "v74", "v75", "v76", "v77", "v78", "v79", "v80", "v81", "v82", "v83", "v84", "v85", "v86", "v87", "v88", "v89", "v90", "v91", "v92", "v93", "v94", "v95", "v96", "v97", "v98", "v99", "v100", "v101", "v102", "v103", "v104", "v105", "v106", "v107", "v108", "v109", "v110", "v111", "v112", "v113", "v114", "v115", "v116", "v117", "v118", "v119", "v120", "v121", "v122", "v123", "v124", "v125", "v126", "v127", "v128", "v129", "v130", "v131", "v132", "v133", "v134", "v135", "v136", "v137", "v138", "v139", "v140", "v141", "v142", "v143", "v144", "v145", "v146", "v147", "v148", "v149", "v150", "v151", "v152", "v153", "v154", "v155", "v156", "v157", "v158", "v159", "v160", "v161", "v162", "v163", "v164", "v165", "v166", "v167", "v168", "v169", "v170", "v171", "v172", "v173", "v174", "v175", "v176", "v177", "v178", "v179", "v180", "v181", "v182", "v183", "v184", "v185", "v186", "v187", "v188", "v189", "v190", "v191", "v192", "v193", "v194", "v195", "v196", "v197", "v198", "v199", "v200", "v201", "v202", "v203", "v204", "v205", "v206", "v207", "v208", "v209", "v210", "v211", "v212", "v213", "v214", "v215", "v216", "v217", "v218", "v219", "v220", "v221", "v222", "v223", "v224", "v225", "v226", "v227", "v228", "v229", "v230", "v231", "v232", "v233", "v234", "v235", "v236", "v237", "v238", "v239", "v240", "v241", "v242", "v243", "v244", "v245", "v246", "v247", "v248", "v249", "v250", "v251", "v252", "v253", "v254", "v255", "a0", "a1", "a2", "a3", "a4", "a5", "a6", "a7", "a8", "a9", "a10", "a11", "a12", "a13", "a14", "a15", "a16", "a17", "a18", "a19", "a20", "a21", "a22", "a23", "a24", "a25", "a26", "a27", "a28", "a29", "a30", "a31", "a32", "a33", "a34", "a35", "a36", "a37", "a38", "a39", "a40", "a41", "a42", "a43", "a44", "a45", "a46", "a47", "a48", "a49", "a50", "a51", "a52", "a53", "a54", "a55", "a56", "a57", "a58", "a59", "a60", "a61", "a62", "a63", "a64", "a65", "a66", "a67", "a68", "a69", "a70", "a71", "a72", "a73", "a74", "a75", "a76", "a77", "a78", "a79", "a80", "a81", "a82", "a83", "a84", "a85", "a86", "a87", "a88", "a89", "a90", "a91", "a92", "a93", "a94", "a95", "a96", "a97", "a98", "a99", "a100", "a101", "a102", "a103", "a104", "a105", "a106", "a107", "a108", "a109", "a110", "a111", "a112", "a113", "a114", "a115", "a116", "a117", "a118", "a119", "a120", "a121", "a122", "a123", "a124", "a125", "a126", "a127", "a128", "a129", "a130", "a131", "a132", "a133", "a134", "a135", "a136", "a137", "a138", "a139", "a140", "a141", "a142", "a143", "a144", "a145", "a146", "a147", "a148", "a149", "a150", "a151", "a152", "a153", "a154", "a155", "a156", "a157", "a158", "a159", "a160", "a161", "a162", "a163", "a164", "a165", "a166", "a167", "a168", "a169", "a170", "a171", "a172", "a173", "a174", "a175", "a176", "a177", "a178", "a179", "a180", "a181", "a182", "a183", "a184", "a185", "a186", "a187", "a188", "a189", "a190", "a191", "a192", "a193", "a194", "a195", "a196", "a197", "a198", "a199", "a200", "a201", "a202", "a203", "a204", "a205", "a206", "a207", "a208", "a209", "a210", "a211", "a212", "a213", "a214", "a215", "a216", "a217", "a218", "a219", "a220", "a221", "a222", "a223", "a224", "a225", "a226", "a227", "a228", "a229", "a230", "a231", "a232", "a233", "a234", "a235", "a236", "a237", "a238", "a239", "a240", "a241", "a242", "a243", "a244", "a245", "a246", "a247", "a248", "a249", "a250", "a251", "a252", "a253", "a254", "a255", "s8", "s9", "s10", "s11", "s12", "s13", "s14", "s15", "s16", "s17", "s18", "s19", "s20", "s21", "s22", "s23", "s24", "s25", "s26", "s27", "s28", "s29", "s30", "s31", "s33", "s34", "s35", "s36", "s37", "s38", "s39", "s40", "s41", "s42", "s43", "s44", "s45", "s46", "s47", "s48", "s49", "s50", "s51", "s52", "s53", "s54", "s55", "s56", "s57", "s58", "s59", "s60", "s61", "s62", "s63", "s64", "s65", "s66", "s67", "s68", "s69", "s70", "s71", "s72", "s73", "s74", "s75", "s76", "s77", "s78", "s79", "s80", "s81", "s82", "s83", "s84", "s85", "s86", "s87", "s88", "s89", "s90", "s91", "s92", "s93", "s94", "s95", "s96", "s97", "s98", "s99");
#endif
}

extern "C" void kernel_launch(void* const* d_in, const int* in_sizes, int n_in,
                              void* d_out, int out_size, void* d_ws, size_t ws_size,
                              hipStream_t stream) {
  (void)hipFuncSetAttribute((const void*)k_main, hipFuncAttributeMaxDynamicSharedMemorySize, 163840);
  k_main<<<256, 256, 163840, stream>>>(d_in[0], d_in[1], d_in[2], d_in[3], d_in[4], d_ws, d_in[11], d_in[7], d_in[5], d_in[8], d_in[9], d_in[10], d_out);
}
```

```cpp
#include <hip/hip_runtime.h>
#include <stdint.h>

extern "C" __global__ __launch_bounds__(256, 1)
void k_main(const void* x, const void* ckv, const void* wq, const void* wuk, const void* wuv, void* ws,
            const void* gate_w, const void* w_r, const void* wo, const void* wg, const void* wu, const void* wd, void* out) {
#if defined(__HIP_DEVICE_COMPILE__)
  asm volatile(
"s_mov_b64 s[8:9], %0\ns_mov_b32 s10, %1\nv_mov_b32 v0, %2\n.macro WL n\ns_waitcnt lgkmcnt(\\n)\n.endm\n.macro D0 x,r,s,o\n"
"buffer_load_dwordx4 v\\x, s[\\r:\\r+3], s\\s offen offset:\\o lds\n.endm\n.macro WV n\ns_waitcnt vmcnt(\\n)\n.endm\n.macro Ra d,x,o\nds_read_b128 a[\\d:\\d+3], v\\x offset:\\o\n"
".endm\n.macro Ta d,x,o\nds_read_b64_tr_b16 a[\\d:\\d+1], v\\x offset:\\o\n.endm\n.macro Zvaa d,x,y\nv_mfma_f32_16x16x32_bf16 v[\\d:\\d+3], a[\\x:\\x+3], a[\\y:\\y+3], 0\n.endm\n"
".macro Mvaa d,x,y\nv_mfma_f32_16x16x32_bf16 v[\\d:\\d+3], a[\\x:\\x+3], a[\\y:\\y+3], v[\\d:\\d+3]\n.endm\n.macro Zaaa d,x,y\n"
"v_mfma_f32_16x16x32_bf16 a[\\d:\\d+3], a[\\x:\\x+3], a[\\y:\\y+3], 0\n.endm\n.macro Maaa d,x,y\nv_mfma_f32_16x16x32_bf16 a[\\d:\\d+3], a[\\x:\\x+3], a[\\y:\\y+3], a[\\d:\\d+3]\n"
".endm\n.macro MU d,x,y\nv_mul_f32 v\\d, v\\x, v\\y\n.endm\n.macro CV d,x,y\nv_cvt_pk_bf16_f32 v\\d, v\\x, v\\y\n.endm\n.macro FM d,x,y\nv_fmac_f32 v\\d, v\\x, v\\y\n.endm\n"
".macro AD d,x,y\nv_add_f32 v\\d, v\\x, v\\y\n.endm\n.macro EX d,x\nv_exp_f32 v\\d, v\\x\n.endm\n.macro AR d,x\nv_accvgpr_read_b32 v\\d, a\\x\n.endm\n.macro Sv d,x,r,s,o\n"
"buffer_store_dwordx4 v[\\d:\\d+3], v\\x, s[\\r:\\r+3], s\\s offen offset:\\o sc0 sc1\n.endm\n.macro Rv d,x,o\nds_read_b128 v[\\d:\\d+3], v\\x offset:\\o\n.endm\n.macro D1 x,r,s,o\n"
"buffer_load_dwordx4 v\\x, s[\\r:\\r+3], s\\s offen offset:\\o sc1 lds\n.endm\n.macro Cvaa d,x,y,c\nv_mfma_f32_16x16x32_bf16 v[\\d:\\d+3], a[\\x:\\x+3], a[\\y:\\y+3], v[\\c:\\c+3]\n"
".endm\n.macro Zvav d,x,y\nv_mfma_f32_16x16x32_bf16 v[\\d:\\d+3], a[\\x:\\x+3], v[\\y:\\y+3], 0\n.endm\n.macro Mvav d,x,y\n"
"v_mfma_f32_16x16x32_bf16 v[\\d:\\d+3], a[\\x:\\x+3], v[\\y:\\y+3], v[\\d:\\d+3]\n.endm\n.macro SU d,x,y\nv_sub_f32 v\\d, v\\x, v\\y\n.endm\n.macro Mvva d,x,y\n"
"v_mfma_f32_16x16x32_bf16 v[\\d:\\d+3], v[\\x:\\x+3], a[\\y:\\y+3], v[\\d:\\d+3]\n.endm\n.macro Zvvv d,x,y\nv_mfma_f32_16x16x32_bf16 v[\\d:\\d+3], v[\\x:\\x+3], v[\\y:\\y+3], 0\n"
".endm\n.macro Mvvv d,x,y\nv_mfma_f32_16x16x32_bf16 v[\\d:\\d+3], v[\\x:\\x+3], v[\\y:\\y+3], v[\\d:\\d+3]\n.endm\n.macro AW d,x\nv_accvgpr_write_b32 a\\d, v\\x\n.endm\n"
".macro Tv d,x,o\nds_read_b64_tr_b16 v[\\d:\\d+1], v\\x offset:\\o\n.endm\n.macro Sa d,x,r,s,o\n"
"buffer_store_dwordx4 a[\\d:\\d+3], v\\x, s[\\r:\\r+3], s\\s offen offset:\\o sc0 sc1\n.endm\n.Lcode0:\ns_mov_b32 s70, 0\ns_load_dwordx16 s[44:59], s[8:9], 0x0\n"
"s_load_dwordx8 s[60:67], s[8:9], 0x40\ns_load_dwordx2 s[68:69], s[8:9], 0x60\nv_lshrrev_b32 v2, 6, v0\nv_and_b32 v0, 63, v0\nv_readfirstlane_b32 s11, v2\nv_lshlrev_b32 v1, 4, v0\n"
"s_and_b32 s2, s10, 0x17\ns_bfe_u32 s3, s10, 0x10003\ns_lshl_b32 s3, s3, 7\ns_or_b32 s2, s2, s3\ns_bfe_u32 s3, s10, 0x10005\ns_lshl3_add_u32 s2, s3, s2\ns_lshr_b32 s3, s10, 6\n"
"s_lshl_b32 s3, s3, 5\ns_or_b32 s10, s2, s3\ns_and_b32 s73, s10, 7\ns_lshr_b32 s74, s10, 3\ns_and_b32 s2, s74, 1\ns_lshl1_add_u32 s12, s73, s2\ns_bfe_u32 s13, s74, 0x30001\n"
"s_lshr_b32 s14, s74, 4\ns_xor_b32 s14, s14, 1\nv_lshrrev_b32 v32, 5, v0\ns_and_b32 s3, s11, 1\ns_lshl_b32 s2, s3, 3\nv_lshl_or_b32 v33, v32, 1, s2\nv_and_b32 v34, 31, v0\n"
"v_xor_b32 v33, v33, v34\nv_lshlrev_b32 v33, 4, v33\nv_lshl_or_b32 v6, v32, 13, v33\nv_xor_b32 v33, 64, v6\nv_add_u32 v7, 15360, v33\nv_add_u32 v8, 30720, v6\n"
"v_add_u32 v9, 46080, v33\ns_lshl_b32 s2, s11, 16\ns_lshl_b32 s4, s12, 9\ns_add_u32 s5, s2, s4\ns_getpc_b64 s[40:41]\n.PC8:\ns_sub_u32 s40, s40, .PC8-.Lcode0\n"
"s_subb_u32 s41, s41, 0\ns_mov_b32 s42, .Lcode1-.Lcode0\ns_mov_b32 s43, 0x20000\ns_lshl2_add_u32 s6, s74, s11\ns_lshl_b32 s6, s6, 7\nv_lshlrev_b32 v54, 10, v1\n"
"buffer_load_dword v31, v54, s[40:43], s6 offen\nv_lshrrev_b32 v32, 4, v0\nv_and_b32 v33, 7, v0\nv_xor_b32 v33, v33, v32\nv_lshlrev_b32 v33, 4, v33\nv_lshrrev_b32 v32, 3, v0\n"
"v_lshl_or_b32 v2, v32, 11, v33\nv_xor_b32 v33, 64, v2\nv_add_u32 v3, 15360, v33\nv_add_u32 v4, 30720, v2\nv_add_u32 v5, 46080, v33\nv_lshrrev_b32 v144, 4, v0\n"
"v_and_b32 v145, 7, v0\nv_xor_b32 v145, v145, v144\nv_lshlrev_b32 v145, 4, v145\nv_lshrrev_b32 v144, 3, v0\nv_mul_u32_u24 v144, 896, v144\nv_add_u32 v136, v144, v145\n"
"v_xor_b32 v145, 64, v136\nv_add_u32 v137, 6144, v145\nv_add_u32 v138, 55296, v136\nv_add_u32 v139, 61440, v145\ns_lshr_b32 s88, s11, 1\ns_lshr_b32 s2, s13, 2\n"
"s_bfe_u32 s3, s13, 0x10001\ns_mul_i32 s3, s3, 17\ns_add_u32 s2, s2, s3\ns_lshl2_add_u32 s3, s14, s88\ns_lshl_b32 s3, s3, 2\ns_lshr_b32 s3, 0xe420ca86, s3\ns_and_b32 s3, s3, 15\n"
"s_add_u32 s2, s2, s3\ns_cmp_eq_u32 s2, 32\ns_cselect_b32 s86, 16, s2\ns_add_u32 s88, s88, 2\ns_lshr_b32 s2, s13, 2\ns_bfe_u32 s3, s13, 0x10001\ns_mul_i32 s3, s3, 17\n"
"s_add_u32 s2, s2, s3\ns_lshl2_add_u32 s3, s14, s88\ns_lshl_b32 s3, s3, 2\ns_lshr_b32 s3, 0xe420ca86, s3\ns_and_b32 s3, s3, 15\ns_add_u32 s2, s2, s3\ns_cmp_eq_u32 s2, 32\n"
"s_cselect_b32 s88, 16, s2\ns_and_b32 s89, s11, 1\ns_lshl1_add_u32 s86, s86, s89\ns_lshl1_add_u32 s88, s88, s89\ns_sub_u32 s2, 15, s13\ns_lshl3_add_u32 s2, s2, s11\n"
"s_mul_i32 s87, s2, 14336\ns_mov_b32 s18, 0xc0ae800\ns_mov_b32 s30, 0x1c0000\ns_mov_b32 s34, 0x380000\ns_mov_b32 s22, 0x400000\ns_mov_b32 s26, 0x800000\ns_mov_b32 s19, 0x20000\n"
"s_mov_b32 s31, 0x20000\ns_mov_b32 s35, 0x20000\ns_mov_b32 s23, 0x20000\ns_mov_b32 s27, 0x20000\nWL 0\ns_mov_b32 s28, s44\ns_and_b32 s29, s45, 0xffff\ns_mov_b32 s32, s48\n"
"s_and_b32 s33, s49, 0xffff\ns_mov_b32 s20, s46\ns_and_b32 s21, s47, 0xffff\ns_bitcmp1_b32 s13, 0\ns_cselect_b64 s[50:51], s[52:53], s[50:51]\ns_mov_b32 s24, s50\n"
"s_and_b32 s25, s51, 0xffff\ns_sub_u32 s44, s5, 0x40000\ns_mov_b32 s51, s5\ns_lshl_b32 s47, s11, 12\ns_add_u32 s47, s47, 0x10000\ns_lshl_b32 s45, s86, 16\ns_sub_u32 s45, s45, 128\n"
"s_lshl_b32 s48, s88, 16\ns_sub_u32 s48, s48, 128\ns_mov_b32 s52, s87\ns_cmp_eq_u32 s14, 1\ns_cbranch_scc1 .B6\ns_add_u32 m0, s47, 65536\ns_add_u32 s46, s51, 0\nD0 6,32,46,0\n"
"D0 7,32,46,1024\nD0 8,32,46,2048\nD0 9,32,46,3072\ns_add_u32 m0, s47, -65536\ns_add_u32 s46, s52, 0\nD0 136,28,46,0\nD0 137,28,46,1024\nD0 138,28,46,2048\nD0 139,28,46,3072\n"
"s_add_u32 m0, s47, 81920\ns_add_u32 s46, s51, 262144\nD0 6,32,46,0\nD0 7,32,46,1024\nD0 8,32,46,2048\nD0 9,32,46,3072\ns_branch .B7\n.B6:\ns_add_u32 m0, s47, 0\n"
"s_add_u32 s44, s44, 0x40000\nD0 6,24,44,0\nD0 7,24,44,1024\nD0 8,24,44,2048\nD0 9,24,44,3072\ns_add_u32 m0, s47, -65536\ns_add_u32 s45, s45, 128\nD0 2,20,45,0\nD0 3,20,45,1024\n"
"D0 4,20,45,2048\nD0 5,20,45,3072\ns_add_u32 m0, s47, 16384\ns_add_u32 s44, s44, 0x40000\nD0 6,24,44,0\nD0 7,24,44,1024\nD0 8,24,44,2048\nD0 9,24,44,3072\n.B7:\n"
"s_add_u32 s54, s54, 0x1fffff\ns_addc_u32 s55, s55, 0\ns_and_b32 s54, s54, 0xffe00000\ns_mov_b32 s16, s54\ns_and_b32 s17, s55, 0xffff\ns_mov_b32 s2, 0xc048000\n"
"s_load_dword s15, s[54:55], s2\nv_lshrrev_b32 v24, 3, v0\nv_lshlrev_b32 v24, 13, v24\nv_and_b32 v26, 7, v0\nv_lshl_or_b32 v24, v26, 7, v24\nv_lshrrev_b32 v26, 4, v0\n"
"v_lshl_or_b32 v24, v26, 28, v24\nv_lshrrev_b32 v26, 5, v0\nv_lshlrev_b32 v26, 30, v26\nv_lshl_or_b32 v26, v0, 7, v26\ns_and_b32 s2, s13, 6\ns_and_b32 s3, s12, 1\n"
"s_add_u32 s2, s2, s3\ns_lshl2_add_u32 s2, s2, s11\ns_lshl_b32 s2, s2, 14\ns_lshl_b32 s3, s73, 10\ns_add_u32 s2, s2, s3\ns_add_u32 s96, s2, 1048576\ns_and_b32 s2, s10, 127\n"
"s_lshl2_add_u32 s2, s2, s11\ns_lshl_b32 s97, s2, 12\nv_and_b32 v48, 15, v0\nv_lshrrev_b32 v49, 4, v0\nv_lshrrev_b32 v50, 1, v48\nv_xor_b32 v50, v50, v49\n"
"v_lshlrev_b32 v50, 4, v50\nv_lshl_or_b32 v50, v48, 7, v50\ns_lshr_b32 s2, s11, 1\ns_lshl_b32 s2, s2, 12\nv_add_u32 v10, s2, v50\nv_xor_b32 v11, 64, v10\nv_lshrrev_b32 v50, 2, v48\n"
"v_lshl_add_u32 v51, v49, 3, v50\nv_and_b32 v52, 1, v49\nv_lshl_or_b32 v50, v52, 2, v50\nv_lshlrev_b32 v50, 5, v50\nv_lshl_or_b32 v50, v51, 9, v50\nv_and_b32 v52, 3, v0\n"
"v_lshl_or_b32 v50, v52, 3, v50\ns_and_b32 s2, s11, 1\ns_lshl_b32 s2, s2, 8\ns_add_u32 s2, s2, 0x10000\nv_add_u32 v12, s2, v50\nv_xor_b32 v13, 32, v12\nv_xor_b32 v14, 64, v12\n"
"v_xor_b32 v15, 96, v12\nv_xor_b32 v16, 128, v12\nv_xor_b32 v17, 160, v12\nv_xor_b32 v18, 192, v12\nv_xor_b32 v19, 224, v12\ns_getpc_b64 s[0:1]\n.PC9:\ns_mov_b32 s2, .PA2-.PC9\n"
"s_mov_b32 s3, .PA4-.PC9\ns_bitcmp1_b32 s13, 0\ns_cselect_b32 s2, .PA3-.PC9, s2\ns_cselect_b32 s3, .PA5-.PC9, s3\ns_cmp_eq_u32 s14, 0\ns_cselect_b32 s2, s2, s3\n"
"s_add_u32 s0, s0, s2\ns_addc_u32 s1, s1, 0\ns_setpc_b64 s[0:1]\n.p2align 12\n.PA2:\ns_mov_b64 exec, 0\ns_cmp_eq_u32 s11, 1\ns_cbranch_scc1 .W533\ns_cmp_eq_u32 s11, 2\n"
"s_cbranch_scc1 .W534\ns_cmp_eq_u32 s11, 3\ns_cbranch_scc1 .W535\ns_branch .W532\n.WB21:\ns_mov_b64 exec, -1\nv_add_u32 v128, 0x10000, v12\nv_add_u32 v129, 0x10000, v13\n"
"v_add_u32 v130, 0x10000, v14\nv_add_u32 v131, 0x10000, v15\nv_add_u32 v132, 0x10000, v16\nv_add_u32 v133, 0x10000, v17\nv_add_u32 v134, 0x10000, v18\nv_add_u32 v135, 0x10000, v19\n"
"s_add_u32 s2, s11, 1\ns_lshr_b32 s2, s2, 1\ns_lshl_b32 s2, s2, 12\nv_add_u32 v140, s2, v10\nv_add_u32 v141, s2, v11\nv_xor_b32 v142, 0x1000, v140\nv_xor_b32 v143, 0x1000, v141\n"
"s_add_u32 m0, s47, 0\ns_add_u32 s46, s51, 524288\nD0 6,32,46,0\nD0 7,32,46,1024\nD0 8,32,46,2048\nD0 9,32,46,3072\ns_add_u32 m0, s47, -49152\ns_add_u32 s46, s52, 128\n"
"D0 136,28,46,0\nD0 137,28,46,1024\nD0 138,28,46,2048\nD0 139,28,46,3072\ns_add_u32 m0, s47, 16384\ns_add_u32 s46, s51, 786432\nD0 6,32,46,0\nD0 7,32,46,1024\nD0 8,32,46,2048\n"
"D0 9,32,46,3072\nv_mov_b32 v116, 0x40000000\nv_cmp_gt_u32 vcc, 56, v0\ns_lshl2_add_u32 s2, s10, s11\ns_mul_i32 s2, s2, 1792\nv_cndmask_b32 v117, v116, v1, vcc\ns_mov_b32 s36, s56\n"
"s_and_b32 s37, s57, 0xffff\ns_mov_b32 s38, 0x380\ns_mov_b32 s39, 0x20000\ns_mov_b32 s40, s58\ns_and_b32 s41, s59, 0xffff\ns_mov_b32 s42, 0x380\ns_mov_b32 s43, 0x20000\n"
"buffer_load_dwordx4 v[96:99], v117, s[28:31], s2 offen offset:0\nbuffer_load_dwordx4 v[100:103], v117, s[28:31], s2 offen offset:896\n"
"buffer_load_dwordx4 v[104:107], v117, s[36:39], 0 offen offset:0\nbuffer_load_dwordx4 v[108:111], v117, s[40:43], 0 offen offset:0\ns_add_u32 m0, s47, 32768\n"
"s_add_u32 s46, s51, 1048576\nD0 6,32,46,0\nD0 7,32,46,1024\nD0 8,32,46,2048\nD0 9,32,46,3072\ns_add_u32 m0, s47, -32768\ns_add_u32 s46, s52, 256\nD0 136,28,46,0\n"
"D0 137,28,46,1024\nD0 138,28,46,2048\nD0 139,28,46,3072\nWL 0\ns_mul_i32 s15, s15, 1664525\ns_add_u32 s15, s15, 1013904223\nv_mov_b32 v25, s15\ns_mov_b32 s57, 0x3db8aa3b\n"
"s_and_b32 s59, s11, 1\ns_lshl_b32 s59, s59, 13\ns_mul_i32 s2, s73, 25165824\ns_and_b32 s3, s12, 1\ns_lshl_b32 s3, s3, 21\ns_add_u32 s2, s2, s3\ns_lshl2_add_u32 s3, s13, s11\n"
"s_lshl_b32 s3, s3, 15\ns_add_u32 s2, s2, s3\ns_add_u32 s56, s2, 0x800000\ns_xor_b32 s53, s56, 0xc000\nWV 24\ns_barrier\nRa 192,140,0\nRa 196,141,0\nRa 200,140,2048\n"
"Ra 204,141,2048\nRa 208,142,0\nRa 212,143,0\nRa 216,142,2048\nRa 220,143,2048\nTa 160,128,0\nTa 162,128,2048\nTa 164,128,16384\nTa 166,128,18432\nTa 168,129,0\nTa 170,129,2048\n"
"Ta 172,129,16384\nTa 174,129,18432\nTa 176,130,0\nTa 178,130,2048\nTa 180,130,16384\nTa 182,130,18432\nTa 184,131,0\nTa 186,131,2048\nTa 188,131,16384\nTa 190,131,18432\nWL 0\n"
"Zvaa 32,160,192\nTa 224,132,0\ns_add_u32 m0, s47, 49152\ns_add_u32 s46, s51, 1310720\nMvaa 32,164,196\nTa 226,132,2048\nZaaa 96,164,204\nD0 6,32,46,0\nMaaa 96,160,200\n"
"Ta 228,132,16384\nZvaa 192,160,208\nTa 230,132,18432\nMvaa 192,164,212\nZvaa 224,164,220\nTa 232,133,0\nMvaa 224,160,216\nTa 234,133,2048\nD0 7,32,46,1024\nZvaa 228,168,216\n"
"Mvaa 228,172,220\nTa 236,133,16384\nZvaa 196,172,212\nTa 238,133,18432\nMvaa 196,168,208\nZaaa 100,168,200\nTa 240,134,0\nD0 8,32,46,2048\nMaaa 100,172,204\nTa 242,134,2048\n"
"Zvaa 36,172,196\nMvaa 36,168,192\nTa 244,134,16384\nZvaa 40,176,192\nTa 246,134,18432\nMvaa 40,180,196\nD0 9,32,46,3072\nZaaa 104,180,204\nTa 248,135,0\nMaaa 104,176,200\n"
"Ta 250,135,2048\nZvaa 200,176,208\ns_add_u32 m0, s47, -16384\ns_add_u32 s46, s52, 384\nMvaa 200,180,212\nTa 252,135,16384\nZvaa 232,180,220\nTa 254,135,18432\nD0 136,28,46,0\n"
"Mvaa 232,176,216\nZvaa 236,184,216\nMvaa 236,188,220\nZvaa 204,188,212\nMvaa 204,184,208\nD0 137,28,46,1024\nZaaa 108,184,200\nMaaa 108,188,204\nZvaa 44,188,196\nMvaa 44,184,192\n"
"s_waitcnt vmcnt(18) lgkmcnt(0)\ns_barrier\nZvaa 48,224,192\nRa 128,140,16384\nD0 138,28,46,2048\nMvaa 48,228,196\nRa 132,141,16384\nZaaa 112,228,204\nRa 136,140,18432\n"
"Maaa 112,224,200\nRa 140,141,18432\nZvaa 208,224,208\nRa 144,142,16384\nWV 15\nMvaa 208,228,212\nRa 148,143,16384\nD0 139,28,46,3072\nZvaa 240,228,220\nRa 152,142,18432\n"
"Mvaa 240,224,216\nRa 156,143,18432\nZvaa 244,232,216\nTa 160,12,0\ns_add_u32 m0, s47, 65536\ns_add_u32 s46, s51, 1572864\nMvaa 244,236,220\nTa 162,12,2048\nZvaa 212,236,212\n"
"Ta 164,12,16384\nD0 6,32,46,0\nMvaa 212,232,208\nTa 166,12,18432\nZaaa 116,232,200\nTa 168,13,0\nv_lshlrev_b32 v112, 16, v96\nMaaa 116,236,204\nTa 170,13,2048\nZvaa 52,236,196\n"
"Ta 172,13,16384\nMvaa 52,232,192\nTa 174,13,18432\nD0 7,32,46,1024\nZvaa 56,240,192\nTa 176,14,0\nv_lshlrev_b32 v113, 16, v100\nMvaa 56,244,196\nTa 178,14,2048\nZaaa 120,244,204\n"
"Ta 180,14,16384\nMaaa 120,240,200\nTa 182,14,18432\nZvaa 216,240,208\nTa 184,15,0\nD0 8,32,46,2048\nMvaa 216,244,212\nTa 186,15,2048\nZvaa 248,244,220\nTa 188,15,16384\n"
"Mvaa 248,240,216\nTa 190,15,18432\nZvaa 252,248,216\nv_lshlrev_b32 v114, 16, v104\nv_lshlrev_b32 v115, 16, v108\nMvaa 252,252,220\nD0 9,32,46,3072\nZvaa 220,252,212\n"
"MU 116,112,114\nMvaa 220,248,208\nMU 117,113,114\nZaaa 124,248,200\ns_add_u32 m0, s47, 81920\ns_add_u32 s46, s51, 1835008\nMaaa 124,252,204\nCV 116,116,117\nZvaa 60,252,196\n"
"D0 6,32,46,0\nMvaa 60,248,192\nWL 0\nMvaa 32,160,128\nTa 224,16,0\nv_lshlrev_b32 v118, 16, v116\nMvaa 32,164,132\nTa 226,16,2048\nMaaa 96,164,140\n"
"v_and_b32 v119, 0xffff0000, v116\nMaaa 96,160,136\nTa 228,16,16384\nD0 7,32,46,1024\nMvaa 192,160,144\nTa 230,16,18432\nMU 120,112,115\nMvaa 192,164,148\nMU 121,113,115\n"
"Mvaa 224,164,156\nTa 232,17,0\nMvaa 224,160,152\nTa 234,17,2048\nMvaa 228,168,152\nD0 8,32,46,2048\nv_and_b32 v112, 0xffff0000, v96\nMvaa 228,172,156\nTa 236,17,16384\n"
"Mvaa 196,172,148\nTa 238,17,18432\nMvaa 196,168,144\nv_and_b32 v113, 0xffff0000, v100\nMaaa 100,168,136\nTa 240,18,0\nv_and_b32 v114, 0xffff0000, v104\nMaaa 100,172,140\n"
"Ta 242,18,2048\nD0 9,32,46,3072\nMvaa 36,172,132\nv_and_b32 v115, 0xffff0000, v108\nMvaa 36,168,128\nTa 244,18,16384\nMvaa 40,176,128\nTa 246,18,18432\nMU 116,112,114\n"
"Mvaa 40,180,132\nMU 117,113,114\nMaaa 104,180,140\nTa 248,19,0\nMaaa 104,176,136\nTa 250,19,2048\nMvaa 200,176,144\nCV 116,116,117\nv_and_b32 v117, 0xffff0000, v116\n"
"Mvaa 200,180,148\nTa 252,19,16384\nMvaa 232,180,156\nTa 254,19,18432\nMvaa 232,176,152\nv_lshlrev_b32 v116, 16, v116\nMvaa 236,184,152\nFM 120,112,115\nFM 121,113,115\n"
"Mvaa 236,188,156\nAD 118,118,116\nMvaa 204,188,148\nAD 119,119,117\nMvaa 204,184,144\nv_lshlrev_b32 v112, 16, v97\nMaaa 108,184,136\nv_lshlrev_b32 v113, 16, v101\n"
"v_lshlrev_b32 v114, 16, v105\nMaaa 108,188,140\nv_lshlrev_b32 v115, 16, v109\nMvaa 44,188,132\nMU 116,112,114\nMvaa 44,184,128\ns_waitcnt vmcnt(12) lgkmcnt(0)\ns_barrier\n"
"Mvaa 48,224,128\nRa 192,140,32768\ns_add_u32 m0, s47, -49152\ns_add_u32 s46, s52, 512\nMvaa 48,228,132\nRa 196,141,32768\nMaaa 112,228,140\nRa 200,140,34816\nD0 136,28,46,0\n"
"Maaa 112,224,136\nRa 204,141,34816\nMvaa 208,224,144\nRa 208,142,32768\nMU 117,113,114\nMvaa 208,228,148\nRa 212,143,32768\nMvaa 240,228,156\nRa 216,142,34816\nMvaa 240,224,152\n"
"Ra 220,143,34816\nD0 137,28,46,1024\nMvaa 244,232,152\nTa 160,12,32768\nCV 116,116,117\nMvaa 244,236,156\nTa 162,12,34816\nMvaa 212,236,148\nTa 164,12,49152\nMvaa 212,232,144\n"
"Ta 166,12,51200\nMaaa 116,232,136\nTa 168,13,32768\nD0 138,28,46,2048\nMaaa 116,236,140\nTa 170,13,34816\nMvaa 52,236,132\nTa 172,13,49152\nMvaa 52,232,128\nTa 174,13,51200\n"
"Mvaa 56,240,128\nTa 176,14,32768\nv_and_b32 v117, 0xffff0000, v116\nMvaa 56,244,132\nTa 178,14,34816\nD0 139,28,46,3072\nMaaa 120,244,140\nTa 180,14,49152\nMaaa 120,240,136\n"
"Ta 182,14,51200\nMvaa 216,240,144\nTa 184,15,32768\ns_add_u32 m0, s47, 0\ns_add_u32 s46, s51, 2097152\nMvaa 216,244,148\nTa 186,15,34816\nMvaa 248,244,156\nTa 188,15,49152\n"
"D0 6,32,46,0\nMvaa 248,240,152\nTa 190,15,51200\nMvaa 252,248,152\nv_lshlrev_b32 v116, 16, v116\nFM 120,112,115\nMvaa 252,252,156\nFM 121,113,115\nMvaa 220,252,148\n"
"AD 118,118,116\nMvaa 220,248,144\nD0 7,32,46,1024\nMaaa 124,248,136\nAD 119,119,117\nv_and_b32 v112, 0xffff0000, v97\nMaaa 124,252,140\nv_and_b32 v113, 0xffff0000, v101\n"
"Mvaa 60,252,132\nv_and_b32 v114, 0xffff0000, v105\nMvaa 60,248,128\nWL 0\nMvaa 32,160,192\nTa 224,16,32768\nD0 8,32,46,2048\nMvaa 32,164,196\nTa 226,16,34816\nMaaa 96,164,204\n"
"v_and_b32 v115, 0xffff0000, v109\nMaaa 96,160,200\nTa 228,16,49152\nMvaa 192,160,208\nTa 230,16,51200\nMU 116,112,114\nMvaa 192,164,212\nD0 9,32,46,3072\nMvaa 224,164,220\n"
"Ta 232,17,32768\nMvaa 224,160,216\nTa 234,17,34816\nMvaa 228,168,216\ns_add_u32 m0, s47, 16384\ns_add_u32 s46, s51, 2359296\nMvaa 228,172,220\nTa 236,17,49152\nMvaa 196,172,212\n"
"Ta 238,17,51200\nD0 6,32,46,0\nMvaa 196,168,208\nMU 117,113,114\nMaaa 100,168,200\nTa 240,18,32768\nCV 116,116,117\nMaaa 100,172,204\nTa 242,18,34816\nMvaa 36,172,196\n"
"v_and_b32 v117, 0xffff0000, v116\nMvaa 36,168,192\nTa 244,18,49152\nD0 7,32,46,1024\nMvaa 40,176,192\nTa 246,18,51200\nv_lshlrev_b32 v116, 16, v116\nMvaa 40,180,196\n"
"FM 120,112,115\nMaaa 104,180,204\nTa 248,19,32768\nMaaa 104,176,200\nTa 250,19,34816\nMvaa 200,176,208\nD0 8,32,46,2048\nFM 121,113,115\nMvaa 200,180,212\nTa 252,19,49152\n"
"Mvaa 232,180,220\nTa 254,19,51200\nMvaa 232,176,216\nAD 118,118,116\nMvaa 236,184,216\nAD 119,119,117\nv_lshlrev_b32 v112, 16, v98\nMvaa 236,188,220\nD0 9,32,46,3072\n"
"Mvaa 204,188,212\nv_lshlrev_b32 v113, 16, v102\nMvaa 204,184,208\nv_lshlrev_b32 v114, 16, v106\nMaaa 108,184,200\nv_lshlrev_b32 v115, 16, v110\nMU 116,112,114\nMaaa 108,188,204\n"
"MU 117,113,114\nMvaa 44,188,196\nCV 116,116,117\nMvaa 44,184,192\ns_waitcnt vmcnt(12) lgkmcnt(0)\ns_barrier\nMvaa 48,224,192\nRa 128,140,49152\ns_add_u32 m0, s47, -32768\n"
"s_add_u32 s46, s52, 640\nMvaa 48,228,196\nRa 132,141,49152\nMaaa 112,228,204\nRa 136,140,51200\nD0 136,28,46,0\nMaaa 112,224,200\nRa 140,141,51200\nMvaa 208,224,208\n"
"Ra 144,142,49152\nv_and_b32 v117, 0xffff0000, v116\nMvaa 208,228,212\nRa 148,143,49152\nMvaa 240,228,220\nRa 152,142,51200\nMvaa 240,224,216\nRa 156,143,51200\nD0 137,28,46,1024\n"
"Mvaa 244,232,216\nTa 160,128,0\nv_lshlrev_b32 v116, 16, v116\nMvaa 244,236,220\nTa 162,128,2048\nMvaa 212,236,212\nTa 164,128,16384\nMvaa 212,232,208\nTa 166,128,18432\n"
"Maaa 116,232,200\nTa 168,129,0\nD0 138,28,46,2048\nMaaa 116,236,204\nTa 170,129,2048\nMvaa 52,236,196\nTa 172,129,16384\nMvaa 52,232,192\nTa 174,129,18432\nMvaa 56,240,192\n"
"Ta 176,130,0\nFM 120,112,115\nMvaa 56,244,196\nTa 178,130,2048\nD0 139,28,46,3072\nMaaa 120,244,204\nTa 180,130,16384\nMaaa 120,240,200\nTa 182,130,18432\nMvaa 216,240,208\n"
"Ta 184,131,0\ns_add_u32 m0, s47, 32768\ns_add_u32 s46, s51, 2621440\nMvaa 216,244,212\nTa 186,131,2048\nMvaa 248,244,220\nTa 188,131,16384\nD0 6,32,46,0\nMvaa 248,240,216\n"
"Ta 190,131,18432\nMvaa 252,248,216\nFM 121,113,115\nAD 118,118,116\nMvaa 252,252,220\nAD 119,119,117\nMvaa 220,252,212\nv_and_b32 v112, 0xffff0000, v98\nMvaa 220,248,208\n"
"D0 7,32,46,1024\nMaaa 124,248,200\nv_and_b32 v113, 0xffff0000, v102\nv_and_b32 v114, 0xffff0000, v106\nMaaa 124,252,204\nv_and_b32 v115, 0xffff0000, v110\nMvaa 60,252,196\n"
"MU 116,112,114\nMvaa 60,248,192\nWL 0\nMvaa 32,160,128\nTa 224,132,0\n.W532:\ns_cbranch_execz .W536\nD0 8,32,46,2048\nMvaa 32,164,132\nTa 226,132,2048\nMaaa 96,164,140\n"
"MU 117,113,114\nMaaa 96,160,136\nTa 228,132,16384\nMvaa 192,160,144\nTa 230,132,18432\nCV 116,116,117\nMvaa 192,164,148\nD0 9,32,46,3072\nMvaa 224,164,156\nTa 232,133,0\n"
"Mvaa 224,160,152\nTa 234,133,2048\nMvaa 228,168,152\ns_add_u32 m0, s47, 49152\ns_add_u32 s46, s51, 2883584\nMvaa 228,172,156\nTa 236,133,16384\nMvaa 196,172,148\nTa 238,133,18432\n"
"D0 6,32,46,0\nMvaa 196,168,144\nv_and_b32 v117, 0xffff0000, v116\nMaaa 100,168,136\nTa 240,134,0\nv_lshlrev_b32 v116, 16, v116\nMaaa 100,172,140\nTa 242,134,2048\nMvaa 36,172,132\n"
"FM 120,112,115\nMvaa 36,168,128\nTa 244,134,16384\nD0 7,32,46,1024\nMvaa 40,176,128\nTa 246,134,18432\nFM 121,113,115\nMvaa 40,180,132\nAD 118,118,116\nMaaa 104,180,140\n"
"Ta 248,135,0\nMaaa 104,176,136\nTa 250,135,2048\nMvaa 200,176,144\nD0 8,32,46,2048\nAD 119,119,117\nMvaa 200,180,148\nTa 252,135,16384\nMvaa 232,180,156\nTa 254,135,18432\n"
"Mvaa 232,176,152\nv_lshlrev_b32 v112, 16, v99\nMvaa 236,184,152\nv_lshlrev_b32 v113, 16, v103\nv_lshlrev_b32 v114, 16, v107\nMvaa 236,188,156\nD0 9,32,46,3072\nMvaa 204,188,148\n"
"v_lshlrev_b32 v115, 16, v111\nMvaa 204,184,144\nMU 116,112,114\nMaaa 108,184,136\nMU 117,113,114\nCV 116,116,117\ns_and_b32 s78, s74, 3\ns_lshl2_add_u32 s78, s73, s78\n"
"s_lshl_b32 s78, s78, 12\ns_bitcmp1_b32 s11, 0\ns_cselect_b32 s79, s48, s45\ns_add_u32 s79, s79, 512\ns_and_b32 s79, s79, 0xfffe0000\ns_add_u32 s78, s78, s79\n"
"buffer_load_dword v31, v26, s[20:23], s78 offen\nMaaa 108,188,140\nv_and_b32 v117, 0xffff0000, v116\nMvaa 44,188,132\nv_lshlrev_b32 v116, 16, v116\nMvaa 44,184,128\n"
"s_waitcnt vmcnt(13) lgkmcnt(0)\ns_barrier\nMvaa 48,224,128\nRa 192,140,16384\ns_add_u32 m0, s47, -16384\ns_add_u32 s46, s52, 768\nMvaa 48,228,132\nRa 196,141,16384\n"
"Maaa 112,228,140\nRa 200,140,18432\nD0 136,28,46,0\nMaaa 112,224,136\nRa 204,141,18432\nMvaa 208,224,144\nRa 208,142,16384\nFM 120,112,115\nMvaa 208,228,148\nRa 212,143,16384\n"
"Mvaa 240,228,156\nRa 216,142,18432\nMvaa 240,224,152\nRa 220,143,18432\nD0 137,28,46,1024\nMvaa 244,232,152\nTa 160,12,0\nFM 121,113,115\nMvaa 244,236,156\nTa 162,12,2048\n"
"Mvaa 212,236,148\nTa 164,12,16384\nMvaa 212,232,144\nTa 166,12,18432\nMaaa 116,232,136\nTa 168,13,0\nD0 138,28,46,2048\nMaaa 116,236,140\nTa 170,13,2048\nMvaa 52,236,132\n"
"Ta 172,13,16384\nMvaa 52,232,128\nTa 174,13,18432\nMvaa 56,240,128\nTa 176,14,0\nAD 118,118,116\nMvaa 56,244,132\nTa 178,14,2048\nD0 139,28,46,3072\nMaaa 120,244,140\n"
"Ta 180,14,16384\nMaaa 120,240,136\nTa 182,14,18432\nMvaa 216,240,144\nTa 184,15,0\ns_add_u32 m0, s47, 65536\ns_add_u32 s46, s51, 3145728\nMvaa 216,244,148\nTa 186,15,2048\n"
"Mvaa 248,244,156\nTa 188,15,16384\nD0 6,32,46,0\nMvaa 248,240,152\nTa 190,15,18432\nMvaa 252,248,152\nAD 119,119,117\nv_and_b32 v112, 0xffff0000, v99\nMvaa 252,252,156\n"
"v_and_b32 v113, 0xffff0000, v103\nMvaa 220,252,148\nv_and_b32 v114, 0xffff0000, v107\nMvaa 220,248,144\nD0 7,32,46,1024\nMaaa 124,248,136\nv_and_b32 v115, 0xffff0000, v111\n"
"MU 116,112,114\nMaaa 124,252,140\nMU 117,113,114\nMvaa 60,252,132\nCV 116,116,117\nMvaa 60,248,128\nWL 0\nMvaa 32,160,192\nTa 224,16,0\nD0 8,32,46,2048\nMvaa 32,164,196\n"
"Ta 226,16,2048\nMaaa 96,164,204\nv_and_b32 v117, 0xffff0000, v116\nMaaa 96,160,200\nTa 228,16,16384\nMvaa 192,160,208\nTa 230,16,18432\nv_lshlrev_b32 v116, 16, v116\n"
"Mvaa 192,164,212\nD0 9,32,46,3072\nMvaa 224,164,220\nTa 232,17,0\nMvaa 224,160,216\nTa 234,17,2048\nMvaa 228,168,216\ns_add_u32 m0, s47, 81920\ns_add_u32 s46, s51, 3407872\n"
"Mvaa 228,172,220\nTa 236,17,16384\nMvaa 196,172,212\nTa 238,17,18432\nD0 6,32,46,0\nMvaa 196,168,208\nFM 120,112,115\nMaaa 100,168,200\nTa 240,18,0\nFM 121,113,115\n"
"Maaa 100,172,204\nTa 242,18,2048\nMvaa 36,172,196\nAD 118,118,116\nMvaa 36,168,192\nTa 244,18,16384\nD0 7,32,46,1024\nMvaa 40,176,192\nTa 246,18,18432\nAD 119,119,117\n"
"Mvaa 40,180,196\ns_nop 0\nv_permlane32_swap_b32 v118, v119\nMaaa 104,180,204\nTa 248,19,0\nMaaa 104,176,200\nTa 250,19,2048\nMvaa 200,176,208\nD0 8,32,46,2048\n"
"v_permlane32_swap_b32 v120, v121\nMvaa 200,180,212\nTa 252,19,16384\nMvaa 232,180,220\nTa 254,19,18432\nMvaa 232,176,216\nAD 118,118,119\nMvaa 236,184,216\nAD 120,120,121\n"
"Mvaa 236,188,220\nD0 9,32,46,3072\nMvaa 204,188,212\nv_permlane16_swap_b32 v118, v120\nMvaa 204,184,208\nAD 118,118,120\nMaaa 108,184,200\nMaaa 108,188,204\n"
"v_add_f32_dpp v118, v118, v118 row_ror:8 row_mask:0xf bank_mask:0xf\nMvaa 44,188,196\nMvaa 44,184,192\ns_waitcnt vmcnt(13) lgkmcnt(0)\ns_barrier\nMvaa 48,224,192\n"
"Ra 128,140,32768\ns_add_u32 m0, s47, -65536\ns_add_u32 s45, s45, 128\nMvaa 48,228,196\nRa 132,141,32768\nMaaa 112,228,204\nRa 136,140,34816\nD0 2,20,45,0\nMaaa 112,224,200\n"
"Ra 140,141,34816\nMvaa 208,224,208\nRa 144,142,32768\nv_add_f32_dpp v118, v118, v118 row_ror:4 row_mask:0xf bank_mask:0xf\nMvaa 208,228,212\nRa 148,143,32768\nMvaa 240,228,220\n"
"Ra 152,142,34816\nMvaa 240,224,216\nRa 156,143,34816\nD0 3,20,45,1024\nMvaa 244,232,216\nTa 160,12,32768\nv_add_f32_dpp v118, v118, v118 row_ror:2 row_mask:0xf bank_mask:0xf\n"
"Mvaa 244,236,220\nTa 162,12,34816\nMvaa 212,236,212\nTa 164,12,49152\nMvaa 212,232,208\nTa 166,12,51200\nMaaa 116,232,200\nTa 168,13,32768\nD0 4,20,45,2048\nMaaa 116,236,204\n"
"Ta 170,13,34816\nMvaa 52,236,196\nTa 172,13,49152\nMvaa 52,232,192\nTa 174,13,51200\nMvaa 56,240,192\nTa 176,14,32768\n"
"v_add_f32_dpp v118, v118, v118 row_ror:1 row_mask:0xf bank_mask:0xf\nMvaa 56,244,196\nTa 178,14,34816\nD0 5,20,45,3072\nMaaa 120,244,204\nTa 180,14,49152\nMaaa 120,240,200\n"
"Ta 182,14,51200\nMvaa 216,240,208\nTa 184,15,32768\ns_add_u32 m0, s47, 0\ns_add_u32 s44, s44, 0x40000\nMvaa 216,244,212\nTa 186,15,34816\nMvaa 248,244,220\nTa 188,15,49152\n"
"D0 6,24,44,0\nMvaa 248,240,216\nTa 190,15,51200\nMvaa 252,248,216\nCV 118,118,118\nv_lshlrev_b32 v118, 16, v118\nMvaa 252,252,220\nv_mul_f32 v119, 0xbfb8aa3b, v118\nEX 119,119\n"
"Mvaa 220,252,212\nv_add_f32 v120, 0, v118\nMvaa 220,248,208\nD0 7,24,44,1024\nMaaa 124,248,200\nv_add_f32 v119, 1.0, v119\nMaaa 124,252,204\nv_rcp_f32 v119, v119\nMvaa 60,252,196\n"
"v_ashrrev_i32 v121, 31, v120\nMvaa 60,248,192\nWL 0\nMvaa 32,160,128\nTa 224,16,32768\nD0 8,24,44,2048\nMvaa 32,164,132\nTa 226,16,34816\nMaaa 96,164,140\nCV 119,119,119\n"
"Maaa 96,160,136\nTa 228,16,49152\nMvaa 192,160,144\nTa 230,16,51200\nv_lshlrev_b32 v119, 16, v119\nMvaa 192,164,148\nD0 9,24,44,3072\nMvaa 224,164,156\nTa 232,17,32768\n"
"Mvaa 224,160,152\nTa 234,17,34816\nMvaa 228,168,152\ns_add_u32 m0, s47, 16384\ns_add_u32 s44, s44, 0x40000\nMvaa 228,172,156\nTa 236,17,49152\nMvaa 196,172,148\nTa 238,17,51200\n"
"D0 6,24,44,0\nMvaa 196,168,144\nv_or_b32 v121, 0x80000000, v121\nMaaa 100,168,136\nTa 240,18,32768\nv_xor_b32 v120, v120, v121\nMaaa 100,172,140\nTa 242,18,34816\nMvaa 36,172,132\n"
"v_and_b32 v120, 0xffff0000, v120\nMvaa 36,168,128\nTa 244,18,49152\nD0 7,24,44,1024\nMvaa 40,176,128\nTa 246,18,51200\ns_lshl2_add_u32 s2, s10, s11\nMvaa 40,180,132\n"
"v_lshrrev_b32 v112, 5, v0\nMaaa 104,180,140\nTa 248,19,32768\nMaaa 104,176,136\nTa 250,19,34816\nMvaa 200,176,144\nD0 8,24,44,2048\nv_lshl_add_u32 v112, s2, 1, v112\n"
"Mvaa 200,180,148\nTa 252,19,49152\nMvaa 232,180,156\nTa 254,19,51200\nMvaa 232,176,152\nv_sub_u32 v113, 0xffff, v112\nMvaa 236,184,152\nv_or_b32 v120, v120, v113\n"
"v_mov_b32 v121, s15\nMvaa 236,188,156\nD0 9,24,44,3072\nMvaa 204,188,148\nv_and_b32 v113, 31, v0\nMvaa 204,184,144\nv_mov_b32 v116, 0x40000000\nv_cmp_eq_u32 vcc, 16, v113\n"
"v_lshlrev_b32 v114, 2, v112\nv_lshlrev_b32 v115, 10, v113\nv_cndmask_b32 v114, v116, v114, vcc\nv_cndmask_b32 v120, v119, v120, vcc\nMaaa 108,184,136\n"
"v_lshl_add_u32 v115, v112, 3, v115\nv_and_b32 v113, 15, v0\nMaaa 108,188,140\nv_cmp_eq_u32 vcc, 0, v113\ns_mov_b32 s2, 0xc040000\ns_mov_b32 s3, 0xc03e000\n"
"v_cndmask_b32 v115, v116, v115, vcc\nbuffer_store_dwordx2 v[120:121], v115, s[16:19], s2 offen sc0 sc1\nbuffer_store_dword v118, v114, s[16:19], s3 offen sc0 sc1\nMvaa 44,188,132\n"
"Mvaa 44,184,128\ns_waitcnt vmcnt(14) lgkmcnt(0)\ns_barrier\nMvaa 48,224,128\nRa 192,140,49152\ns_add_u32 m0, s47, -49152\ns_add_u32 s48, s48, 128\nMvaa 48,228,132\n"
"Ra 196,141,49152\nMaaa 112,228,140\nRa 200,140,51200\nD0 2,20,48,0\nMaaa 112,224,136\nRa 204,141,51200\nMvaa 208,224,144\nRa 208,142,49152\nMvaa 208,228,148\nRa 212,143,49152\n"
"Mvaa 240,228,156\nRa 216,142,51200\nMvaa 240,224,152\nRa 220,143,51200\nD0 3,20,48,1024\nMvaa 244,232,152\nTa 160,128,0\nMvaa 244,236,156\nTa 162,128,2048\nMvaa 212,236,148\n"
"Ta 164,128,16384\nMvaa 212,232,144\nTa 166,128,18432\nMaaa 116,232,136\nTa 168,129,0\nD0 4,20,48,2048\nMaaa 116,236,140\nTa 170,129,2048\nMvaa 52,236,132\nTa 172,129,16384\n"
"Mvaa 52,232,128\nTa 174,129,18432\nMvaa 56,240,128\nTa 176,130,0\nMvaa 56,244,132\nTa 178,130,2048\nD0 5,20,48,3072\nMaaa 120,244,140\nTa 180,130,16384\nMaaa 120,240,136\n"
"Ta 182,130,18432\nMvaa 216,240,144\nTa 184,131,0\ns_add_u32 m0, s47, -32768\ns_add_u32 s45, s45, 128\nMvaa 216,244,148\nTa 186,131,2048\nMvaa 248,244,156\nTa 188,131,16384\n"
"D0 2,20,45,0\nMvaa 248,240,152\nTa 190,131,18432\nMvaa 252,248,152\nMvaa 252,252,156\nMvaa 220,252,148\nMvaa 220,248,144\nD0 3,20,45,1024\nMaaa 124,248,136\nMaaa 124,252,140\n"
"Mvaa 60,252,132\nMvaa 60,248,128\nWL 0\nMvaa 32,160,192\nTa 224,132,0\nD0 4,20,45,2048\nMvaa 32,164,196\nTa 226,132,2048\nMvaa 36,172,196\nMvaa 36,168,192\nTa 228,132,16384\n"
"Mvaa 40,176,192\nTa 230,132,18432\nMvaa 40,180,196\nD0 5,20,45,3072\nMvaa 44,188,196\nTa 232,133,0\nMvaa 44,184,192\nTa 234,133,2048\nMaaa 108,184,200\ns_add_u32 m0, s47, 32768\n"
"s_add_u32 s44, s44, 0x40000\nMaaa 108,188,204\nTa 236,133,16384\nMaaa 104,180,204\nTa 238,133,18432\nD0 6,24,44,0\nMaaa 104,176,200\nv_mul_f32 v32, s57, v32\n"
"v_mul_f32 v33, s57, v33\nMaaa 100,168,200\nTa 240,134,0\nCV 32,32,33\nMaaa 100,172,204\nTa 242,134,2048\nv_mul_f32 v34, s57, v34\nMaaa 96,164,204\nv_mul_f32 v35, s57, v35\n"
"CV 33,34,35\nMaaa 96,160,200\nTa 244,134,16384\nD0 7,24,44,1024\nMvaa 192,160,208\nTa 246,134,18432\nv_mul_f32 v36, s57, v36\nMvaa 192,164,212\nv_mul_f32 v37, s57, v37\n"
"Mvaa 196,172,212\nTa 248,135,0\nCV 34,36,37\nMvaa 196,168,208\nTa 250,135,2048\nv_mul_f32 v38, s57, v38\nMvaa 200,176,208\nD0 8,24,44,2048\nv_mul_f32 v39, s57, v39\n"
"Mvaa 200,180,212\nTa 252,135,16384\nCV 35,38,39\nMvaa 204,188,212\nTa 254,135,18432\nv_mul_f32 v40, s57, v40\nMvaa 204,184,208\nv_mul_f32 v41, s57, v41\nCV 40,40,41\n"
"Mvaa 236,184,216\nv_mul_f32 v42, s57, v42\nv_mul_f32 v43, s57, v43\nMvaa 236,188,220\nD0 9,24,44,3072\nMvaa 232,180,220\nCV 41,42,43\nv_mul_f32 v44, s57, v44\nMvaa 232,176,216\n"
"v_mul_f32 v45, s57, v45\nCV 42,44,45\nMvaa 228,168,216\nv_mul_f32 v46, s57, v46\nv_mul_f32 v47, s57, v47\nMvaa 228,172,220\nCV 43,46,47\nAR 20,96\nMvaa 224,164,220\nAR 21,97\n"
"v_mul_f32 v20, s57, v20\nMvaa 224,160,216\ns_waitcnt vmcnt(14) lgkmcnt(0)\ns_barrier\nMvaa 48,224,192\ns_add_u32 m0, s47, 49152\ns_add_u32 s44, s44, 0x40000\nRa 128,10,0\n"
"Mvaa 48,228,196\nRa 132,11,0\nMvaa 52,236,196\nD0 6,24,44,0\nRa 136,10,2048\n.W533:\ns_cbranch_execz .W537\nMvaa 52,232,192\nRa 140,11,2048\nv_mul_f32 v21, s57, v21\n"
"Mvaa 56,240,192\nRa 144,10,8192\nCV 36,20,21\nMvaa 56,244,196\nRa 148,11,8192\nAR 20,98\nMvaa 60,252,196\nD0 7,24,44,1024\nRa 152,10,10240\nMvaa 60,248,192\nRa 156,11,10240\n"
"AR 21,99\nMaaa 124,248,200\nTa 160,12,0\nv_mul_f32 v20, s57, v20\nMaaa 124,252,204\nTa 162,12,2048\nMaaa 120,244,204\nD0 8,24,44,2048\nTa 164,12,16384\nMaaa 120,240,200\n"
"Ta 166,12,18432\nv_mul_f32 v21, s57, v21\nMaaa 116,232,200\nTa 168,13,0\nCV 37,20,21\nMaaa 116,236,204\nTa 170,13,2048\nAR 20,100\nMaaa 112,228,204\nD0 9,24,44,3072\n"
"Ta 172,13,16384\nMaaa 112,224,200\nTa 174,13,18432\nAR 21,101\nMvaa 208,224,208\ns_add_u32 m0, s47, -16384\ns_add_u32 s48, s48, 128\nTa 176,14,0\nMvaa 208,228,212\nTa 178,14,2048\n"
"Mvaa 212,236,212\nD0 2,20,48,0\nTa 180,14,16384\nMvaa 212,232,208\nTa 182,14,18432\nv_mul_f32 v20, s57, v20\nMvaa 216,240,208\nTa 184,15,0\nv_mul_f32 v21, s57, v21\n"
"Mvaa 216,244,212\nTa 186,15,2048\nCV 38,20,21\nMvaa 220,252,212\nD0 3,20,48,1024\nTa 188,15,16384\nMvaa 220,248,208\nTa 190,15,18432\nAR 20,102\nMvaa 252,248,216\nAR 21,103\n"
"v_mul_f32 v20, s57, v20\nMvaa 252,252,220\nv_mul_f32 v21, s57, v21\nMvaa 248,244,220\nD0 4,20,48,2048\nCV 39,20,21\nMvaa 248,240,216\nAR 20,104\nAR 21,105\nMvaa 244,232,216\n"
"v_mul_f32 v20, s57, v20\nv_mul_f32 v21, s57, v21\nMvaa 244,236,220\nCV 44,20,21\nAR 20,106\nMvaa 240,228,220\nD0 5,20,48,3072\nAR 21,107\nMvaa 240,224,216\nWL 0\ns_barrier\n"
"Zvaa 64,160,128\nTa 224,16,0\nv_mul_f32 v20, s57, v20\nMvaa 64,164,132\nTa 226,16,2048\nZvaa 96,164,140\ns_add_u32 m0, s47, -65536\ns_add_u32 s45, s45, 128\nMvaa 96,160,136\n"
"Ta 228,16,16384\nv_mul_f32 v21, s57, v21\nZvaa 128,160,144\nTa 230,16,18432\nCV 45,20,21\nMvaa 128,164,148\nD0 2,20,45,0\nAR 20,108\nZvaa 160,164,156\nTa 232,17,0\nAR 21,109\n"
"Mvaa 160,160,152\nTa 234,17,2048\nv_mul_f32 v20, s57, v20\nZvaa 164,168,152\nv_mul_f32 v21, s57, v21\nCV 46,20,21\nMvaa 164,172,156\nTa 236,17,16384\ns_add_u32 s6, s56, 0\n"
"Sv 32,1,16,6,0\nZvaa 132,172,148\nTa 238,17,18432\nAR 20,110\nMvaa 132,168,144\nAR 21,111\nv_mul_f32 v20, s57, v20\nZvaa 100,168,136\nTa 240,18,0\nv_mul_f32 v21, s57, v21\n"
"Mvaa 100,172,140\nTa 242,18,2048\nCV 47,20,21\nZvaa 68,172,132\nD0 3,20,45,1024\nv_mul_f32 v48, s57, v48\nMvaa 68,168,128\nTa 244,18,16384\nv_mul_f32 v49, s57, v49\n"
"Zvaa 72,176,128\nTa 246,18,18432\nCV 48,48,49\nMvaa 72,180,132\nv_mul_f32 v50, s57, v50\nZvaa 104,180,140\nTa 248,19,0\nv_mul_f32 v51, s57, v51\nMvaa 104,176,136\nTa 250,19,2048\n"
"CV 49,50,51\nZvaa 136,176,144\nv_mul_f32 v52, s57, v52\nv_mul_f32 v53, s57, v53\nMvaa 136,180,148\nTa 252,19,16384\nCV 50,52,53\nZvaa 168,180,156\nTa 254,19,18432\n"
"v_mul_f32 v54, s57, v54\nMvaa 168,176,152\nD0 4,20,45,2048\nv_mul_f32 v55, s57, v55\nZvaa 172,184,152\nCV 51,54,55\nv_mul_f32 v56, s57, v56\nMvaa 172,188,156\n"
"v_mul_f32 v57, s57, v57\ns_add_u32 s6, s56, 0\nSv 40,1,16,6,2048\nZvaa 140,188,148\nCV 56,56,57\nv_mul_f32 v58, s57, v58\nMvaa 140,184,144\nv_mul_f32 v59, s57, v59\nCV 57,58,59\n"
"Zvaa 108,184,136\nv_mul_f32 v60, s57, v60\nv_mul_f32 v61, s57, v61\nMvaa 108,188,140\nD0 5,20,45,3072\nCV 58,60,61\nZvaa 76,188,132\nv_mul_f32 v62, s57, v62\n"
"v_mul_f32 v63, s57, v63\nMvaa 76,184,128\ns_waitcnt vmcnt(18) lgkmcnt(0)\ns_barrier\nZvaa 80,224,128\nRa 192,10,16384\nCV 59,62,63\nMvaa 80,228,132\ns_add_u32 m0, s47, 0\n"
"s_add_u32 s44, s44, 0x40000\nZvaa 112,228,140\nRa 196,11,16384\nAR 20,112\nMvaa 112,224,136\nAR 21,113\nv_mul_f32 v20, s57, v20\nZvaa 144,224,144\nRa 200,10,18432\n"
"v_mul_f32 v21, s57, v21\nMvaa 144,228,148\nD0 6,24,44,0\nCV 52,20,21\nZvaa 176,228,156\nRa 204,11,18432\nAR 20,114\nMvaa 176,224,152\nAR 21,115\nv_mul_f32 v20, s57, v20\n"
"Zvaa 180,232,152\nRa 208,10,24576\nv_mul_f32 v21, s57, v21\nMvaa 180,236,156\nCV 53,20,21\ns_add_u32 s6, s56, 0\nSv 36,1,16,6,1024\nZvaa 148,236,148\nRa 212,11,24576\nAR 20,116\n"
"Mvaa 148,232,144\nAR 21,117\nv_mul_f32 v20, s57, v20\nZvaa 116,232,136\nRa 216,10,26624\nv_mul_f32 v21, s57, v21\nMvaa 116,236,140\nD0 7,24,44,1024\nCV 54,20,21\nZvaa 84,236,132\n"
"Ra 220,11,26624\nAR 20,118\nMvaa 84,232,128\nAR 21,119\nv_mul_f32 v20, s57, v20\nZvaa 88,240,128\nv_mul_f32 v21, s57, v21\nCV 55,20,21\nMvaa 88,244,132\nAR 20,120\n"
"Zvaa 120,244,140\nAR 21,121\nv_mul_f32 v20, s57, v20\nMvaa 120,240,136\nv_mul_f32 v21, s57, v21\nCV 60,20,21\nZvaa 152,240,144\nAR 20,122\nAR 21,123\nMvaa 152,244,148\n"
"D0 8,24,44,2048\nv_mul_f32 v20, s57, v20\nZvaa 184,244,156\nv_mul_f32 v21, s57, v21\nCV 61,20,21\nMvaa 184,240,152\nAR 20,124\nAR 21,125\nZvaa 188,248,152\n"
"v_mul_f32 v20, s57, v20\nv_mul_f32 v21, s57, v21\nMvaa 188,252,156\nCV 62,20,21\ns_add_u32 s6, s56, 0\nSv 44,1,16,6,3072\nZvaa 156,252,148\nAR 20,126\nAR 21,127\nMvaa 156,248,144\n"
"v_mul_f32 v20, s57, v20\nv_mul_f32 v21, s57, v21\nZvaa 124,248,136\nCV 63,20,21\nv_mul_f32 v192, s57, v192\nMvaa 124,252,140\nD0 9,24,44,3072\nv_mul_f32 v193, s57, v193\n"
"Zvaa 92,252,132\nCV 192,192,193\nv_mul_f32 v194, s57, v194\nMvaa 92,248,128\nWL 0\nZaaa 16,224,192\nRa 128,10,32768\nv_mul_f32 v195, s57, v195\nMaaa 16,228,196\n"
"s_add_u32 m0, s47, 16384\ns_add_u32 s44, s44, 0x40000\nZaaa 48,228,204\nRa 132,11,32768\nCV 193,194,195\nMaaa 48,224,200\nv_mul_f32 v196, s57, v196\nv_mul_f32 v197, s57, v197\n"
"Zaaa 80,224,208\nRa 136,10,34816\nCV 194,196,197\nMaaa 80,228,212\nD0 6,24,44,0\nv_mul_f32 v198, s57, v198\nZaaa 112,228,220\nRa 140,11,34816\nv_mul_f32 v199, s57, v199\n"
"Maaa 112,224,216\nCV 195,198,199\nv_mul_f32 v200, s57, v200\nZaaa 116,232,216\nRa 144,10,40960\nv_mul_f32 v201, s57, v201\nMaaa 116,236,220\nCV 200,200,201\n"
"v_mul_f32 v202, s57, v202\ns_add_u32 s6, s56, 4096\nSv 48,1,16,6,0\nZaaa 84,236,212\nRa 148,11,40960\nv_mul_f32 v203, s57, v203\nMaaa 84,232,208\nCV 201,202,203\n"
"v_mul_f32 v204, s57, v204\nZaaa 52,232,200\nRa 152,10,43008\nv_mul_f32 v205, s57, v205\nMaaa 52,236,204\nD0 7,24,44,1024\nCV 202,204,205\nZaaa 20,236,196\nRa 156,11,43008\n"
"v_mul_f32 v206, s57, v206\nMaaa 20,232,192\nv_mul_f32 v207, s57, v207\nCV 203,206,207\nZaaa 24,240,192\nv_mul_f32 v208, s57, v208\nv_mul_f32 v209, s57, v209\nMaaa 24,244,196\n"
"CV 208,208,209\nv_mul_f32 v210, s57, v210\nZaaa 56,244,204\nv_mul_f32 v211, s57, v211\nCV 209,210,211\nMaaa 56,240,200\nv_mul_f32 v212, s57, v212\nv_mul_f32 v213, s57, v213\n"
"Zaaa 88,240,208\nCV 210,212,213\nv_mul_f32 v214, s57, v214\nMaaa 88,244,212\nD0 8,24,44,2048\nv_mul_f32 v215, s57, v215\nZaaa 120,244,220\nCV 211,214,215\n"
"v_mul_f32 v216, s57, v216\nMaaa 120,240,216\nv_mul_f32 v217, s57, v217\nCV 216,216,217\nZaaa 124,248,216\nv_mul_f32 v218, s57, v218\nv_mul_f32 v219, s57, v219\nMaaa 124,252,220\n"
"CV 217,218,219\nv_mul_f32 v220, s57, v220\ns_add_u32 s6, s56, 4096\nSv 56,1,16,6,2048\nZaaa 92,252,212\nv_mul_f32 v221, s57, v221\nCV 218,220,221\nMaaa 92,248,208\n"
"v_mul_f32 v222, s57, v222\nv_mul_f32 v223, s57, v223\nZaaa 60,248,200\nCV 219,222,223\nv_mul_f32 v224, s57, v224\nMaaa 60,252,204\nD0 9,24,44,3072\nv_mul_f32 v225, s57, v225\n"
"Zaaa 28,252,196\nCV 224,224,225\nv_mul_f32 v226, s57, v226\nMaaa 28,248,192\ns_waitcnt vmcnt(22) lgkmcnt(0)\ns_barrier\nZaaa 0,160,192\nTa 224,16,32768\nv_mul_f32 v227, s57, v227\n"
"Maaa 0,164,196\nTa 226,16,34816\nCV 225,226,227\nZaaa 32,164,204\ns_add_u32 m0, s47, -49152\ns_add_u32 s48, s48, 128\nMaaa 32,160,200\nTa 228,16,49152\nv_mul_f32 v228, s57, v228\n"
"Zaaa 64,160,208\nTa 230,16,51200\nv_mul_f32 v229, s57, v229\nMaaa 64,164,212\nD0 2,20,48,0\nCV 226,228,229\nZaaa 96,164,220\nTa 232,17,32768\nv_mul_f32 v230, s57, v230\n"
"Maaa 96,160,216\nTa 234,17,34816\nv_mul_f32 v231, s57, v231\nZaaa 100,168,216\nCV 227,230,231\nv_mul_f32 v232, s57, v232\nMaaa 100,172,220\nTa 236,17,49152\n"
"v_mul_f32 v233, s57, v233\ns_add_u32 s6, s56, 4096\nSv 52,1,16,6,1024\nZaaa 68,172,212\nTa 238,17,51200\nCV 232,232,233\nMaaa 68,168,208\nv_mul_f32 v234, s57, v234\n"
"v_mul_f32 v235, s57, v235\nZaaa 36,168,200\nTa 240,18,32768\nCV 233,234,235\nMaaa 36,172,204\nTa 242,18,34816\nv_mul_f32 v236, s57, v236\nZaaa 4,172,196\nD0 3,20,48,1024\n"
"v_mul_f32 v237, s57, v237\nMaaa 4,168,192\nTa 244,18,49152\nCV 234,236,237\nZaaa 8,176,192\nTa 246,18,51200\nv_mul_f32 v238, s57, v238\nMaaa 8,180,196\nv_mul_f32 v239, s57, v239\n"
"CV 235,238,239\nZaaa 40,180,204\nTa 248,19,32768\nv_mul_f32 v240, s57, v240\nMaaa 40,176,200\nTa 250,19,34816\nv_mul_f32 v241, s57, v241\nZaaa 72,176,208\nCV 240,240,241\n"
"v_mul_f32 v242, s57, v242\nMaaa 72,180,212\nTa 252,19,49152\nv_mul_f32 v243, s57, v243\nZaaa 104,180,220\nTa 254,19,51200\nCV 241,242,243\nMaaa 104,176,216\nD0 4,20,48,2048\n"
"v_mul_f32 v244, s57, v244\nZaaa 108,184,216\nv_mul_f32 v245, s57, v245\nCV 242,244,245\nMaaa 108,188,220\nv_mul_f32 v246, s57, v246\nv_mul_f32 v247, s57, v247\n"
"s_add_u32 s6, s56, 4096\nSv 60,1,16,6,3072\nZaaa 76,188,212\nCV 243,246,247\nv_mul_f32 v248, s57, v248\nMaaa 76,184,208\nv_mul_f32 v249, s57, v249\nCV 248,248,249\n"
"Zaaa 44,184,200\nv_mul_f32 v250, s57, v250\nv_mul_f32 v251, s57, v251\nMaaa 44,188,204\nD0 5,20,48,3072\nCV 249,250,251\nZaaa 12,188,196\nv_mul_f32 v252, s57, v252\n"
"v_mul_f32 v253, s57, v253\nMaaa 12,184,192\nWL 0\nMvaa 80,224,128\nTa 160,12,32768\nCV 250,252,253\nMvaa 80,228,132\nTa 162,12,34816\nv_mul_f32 v254, s57, v254\nMvaa 112,228,140\n"
"s_add_u32 m0, s47, -32768\ns_add_u32 s45, s45, 128\nMvaa 112,224,136\nTa 164,12,49152\nv_mul_f32 v255, s57, v255\nMvaa 144,224,144\nTa 166,12,51200\nCV 251,254,255\n"
"Mvaa 144,228,148\nD0 2,20,45,0\nMvaa 176,228,156\nTa 168,13,32768\nMvaa 176,224,152\nTa 170,13,34816\nMvaa 180,232,152\nMvaa 180,236,156\nTa 172,13,49152\ns_add_u32 s6, s53, 0\n"
"Sv 192,1,16,6,0\nMvaa 148,236,148\nTa 174,13,51200\nMvaa 148,232,144\nMvaa 116,232,136\nTa 176,14,32768\nMvaa 116,236,140\nTa 178,14,34816\nMvaa 84,236,132\nD0 3,20,45,1024\n"
"Mvaa 84,232,128\nTa 180,14,49152\nMvaa 88,240,128\nTa 182,14,51200\nMvaa 88,244,132\nMvaa 120,244,140\nTa 184,15,32768\nMvaa 120,240,136\nTa 186,15,34816\nMvaa 152,240,144\n"
"Mvaa 152,244,148\nTa 188,15,49152\nMvaa 184,244,156\nTa 190,15,51200\nMvaa 184,240,152\nD0 4,20,45,2048\nMvaa 188,248,152\nMvaa 188,252,156\ns_add_u32 s6, s53, 0\n"
"Sv 200,1,16,6,2048\nMvaa 156,252,148\nMvaa 156,248,144\nMvaa 124,248,136\nMvaa 124,252,140\nD0 5,20,45,3072\nMvaa 92,252,132\nMvaa 92,248,128\ns_waitcnt vmcnt(24) lgkmcnt(0)\n"
"s_barrier\nMvaa 64,160,128\nRa 192,10,49152\nMvaa 64,164,132\ns_add_u32 m0, s47, 32768\ns_add_u32 s44, s44, 0x40000\nMvaa 96,164,140\nRa 196,11,49152\nMvaa 96,160,136\n"
"Mvaa 128,160,144\nRa 200,10,51200\nMvaa 128,164,148\nD0 6,24,44,0\nMvaa 160,164,156\nRa 204,11,51200\nMvaa 160,160,152\nMvaa 164,168,152\nRa 208,10,57344\nMvaa 164,172,156\n"
"s_add_u32 s6, s53, 4096\nSv 208,1,16,6,0\nMvaa 132,172,148\nRa 212,11,57344\nMvaa 132,168,144\nMvaa 100,168,136\nRa 216,10,59392\nMvaa 100,172,140\nD0 7,24,44,1024\n"
"Mvaa 68,172,132\nRa 220,11,59392\nMvaa 68,168,128\nMvaa 72,176,128\nMvaa 72,180,132\nMvaa 104,180,140\nMvaa 104,176,136\nMvaa 136,176,144\nMvaa 136,180,148\n.W534:\n"
"s_cbranch_execz .W538\nD0 8,24,44,2048\nMvaa 168,180,156\nMvaa 168,176,152\nMvaa 172,184,152\nMvaa 172,188,156\ns_add_u32 s6, s53, 4096\nSv 216,1,16,6,2048\nMvaa 140,188,148\n"
"Mvaa 140,184,144\nMvaa 108,184,136\nMvaa 108,188,140\nD0 9,24,44,3072\nMvaa 76,188,132\nMvaa 76,184,128\nWL 0\nMaaa 0,160,192\nRa 128,10,0\nMaaa 0,164,196\n"
"s_add_u32 m0, s47, 49152\ns_add_u32 s44, s44, 0x40000\nMaaa 32,164,204\nRa 132,11,0\nMaaa 32,160,200\nMaaa 64,160,208\nRa 136,10,2048\nMaaa 64,164,212\nD0 6,24,44,0\n"
"Maaa 96,164,220\nRa 140,11,2048\nMaaa 96,160,216\nMaaa 100,168,216\nRa 144,10,8192\nMaaa 100,172,220\ns_add_u32 s6, s53, 0\nSv 224,1,16,6,1024\nMaaa 68,172,212\nRa 148,11,8192\n"
"Maaa 68,168,208\nMaaa 36,168,200\nRa 152,10,10240\nMaaa 36,172,204\nD0 7,24,44,1024\nMaaa 4,172,196\nRa 156,11,10240\nMaaa 4,168,192\nMaaa 8,176,192\nMaaa 8,180,196\n"
"Maaa 40,180,204\nMaaa 40,176,200\nMaaa 72,176,208\nMaaa 72,180,212\nD0 8,24,44,2048\nMaaa 104,180,220\nMaaa 104,176,216\nMaaa 108,184,216\nMaaa 108,188,220\ns_add_u32 s6, s53, 0\n"
"Sv 232,1,16,6,3072\nMaaa 76,188,212\nMaaa 76,184,208\nMaaa 44,184,200\nMaaa 44,188,204\nD0 9,24,44,3072\nMaaa 12,188,196\nMaaa 12,184,192\ns_waitcnt vmcnt(24) lgkmcnt(0)\n"
"s_barrier\nMaaa 16,224,192\nTa 160,12,0\nMaaa 16,228,196\nTa 162,12,2048\nMaaa 48,228,204\ns_add_u32 m0, s47, -16384\ns_add_u32 s48, s48, 128\nMaaa 48,224,200\nTa 164,12,16384\n"
"Maaa 80,224,208\nTa 166,12,18432\nMaaa 80,228,212\nD0 2,20,48,0\nMaaa 112,228,220\nTa 168,13,0\nMaaa 112,224,216\nTa 170,13,2048\nMaaa 116,232,216\nMaaa 116,236,220\n"
"Ta 172,13,16384\ns_add_u32 s6, s53, 4096\nSv 240,1,16,6,1024\nMaaa 84,236,212\nTa 174,13,18432\nMaaa 84,232,208\nMaaa 52,232,200\nTa 176,14,0\nMaaa 52,236,204\nTa 178,14,2048\n"
"Maaa 20,236,196\nD0 3,20,48,1024\nMaaa 20,232,192\nTa 180,14,16384\nMaaa 24,240,192\nTa 182,14,18432\nMaaa 24,244,196\ns_add_u32 s6, s53, 4096\nSv 248,1,16,6,3072\n"
"Maaa 56,244,204\nTa 184,15,0\nMaaa 56,240,200\nTa 186,15,2048\nMaaa 88,240,208\nMaaa 88,244,212\nTa 188,15,16384\nMaaa 120,244,220\nTa 190,15,18432\nMaaa 120,240,216\n"
"D0 4,20,48,2048\nMaaa 124,248,216\nMaaa 124,252,220\nMaaa 92,252,212\nMaaa 92,248,208\ns_lshl3_add_u32 s90, s12, s13\ns_lshl2_add_u32 s90, s90, s11\n"
"s_lshl2_add_u32 s90, s90, 0xc04a400\nMaaa 60,248,200\nMaaa 60,252,204\nD0 5,20,48,3072\nMaaa 28,252,196\nMaaa 28,248,192\nWL 0\ns_mov_b64 s[76:77], s[62:63]\n"
"s_mov_b64 s[78:79], s[64:65]\ns_mov_b32 s42, 1605632\ns_mov_b32 s50, 1\n.GL20:\nMvaa 64,160,128\nTa 224,16,0\nMvaa 64,164,132\nTa 226,16,2048\nMvaa 96,164,140\n"
"s_add_u32 m0, s47, -65536\ns_add_u32 s45, s45, 128\nMvaa 96,160,136\nTa 228,16,16384\nMvaa 128,160,144\nTa 230,16,18432\nMvaa 128,164,148\nD0 2,20,45,0\nMvaa 160,164,156\n"
"Ta 232,17,0\nMvaa 160,160,152\nTa 234,17,2048\nMvaa 164,168,152\nMvaa 164,172,156\nTa 236,17,16384\nMvaa 132,172,148\nTa 238,17,18432\nMvaa 132,168,144\nMvaa 100,168,136\n"
"Ta 240,18,0\nMvaa 100,172,140\nTa 242,18,2048\nMvaa 68,172,132\nD0 3,20,45,1024\nMvaa 68,168,128\nTa 244,18,16384\nMvaa 72,176,128\nTa 246,18,18432\nMvaa 72,180,132\n"
"Mvaa 104,180,140\nTa 248,19,0\nMvaa 104,176,136\nTa 250,19,2048\nMvaa 136,176,144\nMvaa 136,180,148\nTa 252,19,16384\nMvaa 168,180,156\nTa 254,19,18432\nMvaa 168,176,152\n"
"D0 4,20,45,2048\nMvaa 172,184,152\nMvaa 172,188,156\nMvaa 140,188,148\nMvaa 140,184,144\ns_mov_b32 s40, s76\ns_and_b32 s41, s77, 0xffff\nMvaa 108,184,136\nMvaa 108,188,140\n"
"D0 5,20,45,3072\nMvaa 76,188,132\nMvaa 76,184,128\ns_waitcnt vmcnt(17) lgkmcnt(0)\ns_barrier\nMvaa 80,224,128\nRa 192,10,16384\nMvaa 80,228,132\ns_add_u32 m0, s47, 0\n"
"s_add_u32 s44, s44, 0x40000\nbuffer_load_dword v31, v26, s[40:43], s97 offen\ns_mov_b64 s[76:77], s[66:67]\nMvaa 112,228,140\nRa 196,11,16384\nMvaa 112,224,136\nMvaa 144,224,144\n"
"Ra 200,10,18432\nMvaa 144,228,148\nD0 6,24,44,0\nMvaa 176,228,156\nRa 204,11,18432\nMvaa 176,224,152\nMvaa 180,232,152\nRa 208,10,24576\nMvaa 180,236,156\nMvaa 148,236,148\n"
"Ra 212,11,24576\nMvaa 148,232,144\nMvaa 116,232,136\nRa 216,10,26624\nMvaa 116,236,140\nD0 7,24,44,1024\nMvaa 84,236,132\nRa 220,11,26624\nMvaa 84,232,128\nMvaa 88,240,128\n"
"Mvaa 88,244,132\nMvaa 120,244,140\nMvaa 120,240,136\nMvaa 152,240,144\nMvaa 152,244,148\nD0 8,24,44,2048\nMvaa 184,244,156\nMvaa 184,240,152\nMvaa 188,248,152\nMvaa 188,252,156\n"
"Mvaa 156,252,148\nMvaa 156,248,144\nMvaa 124,248,136\nMvaa 124,252,140\nD0 9,24,44,3072\nMvaa 92,252,132\nMvaa 92,248,128\nWL 0\nMaaa 16,224,192\nRa 128,10,32768\nMaaa 16,228,196\n"
"s_add_u32 m0, s47, 16384\ns_add_u32 s44, s44, 0x40000\nMaaa 48,228,204\nRa 132,11,32768\nMaaa 48,224,200\nMaaa 80,224,208\nRa 136,10,34816\nMaaa 80,228,212\nD0 6,24,44,0\n"
"Maaa 112,228,220\nRa 140,11,34816\nMaaa 112,224,216\nMaaa 116,232,216\nRa 144,10,40960\nMaaa 116,236,220\nMaaa 84,236,212\nRa 148,11,40960\nMaaa 84,232,208\nMaaa 52,232,200\n"
"Ra 152,10,43008\nMaaa 52,236,204\nD0 7,24,44,1024\nMaaa 20,236,196\nRa 156,11,43008\nMaaa 20,232,192\nMaaa 24,240,192\nMaaa 24,244,196\nMaaa 56,244,204\nMaaa 56,240,200\n"
"Maaa 88,240,208\nMaaa 88,244,212\nD0 8,24,44,2048\nMaaa 120,244,220\nMaaa 120,240,216\nMaaa 124,248,216\nMaaa 124,252,220\nMaaa 92,252,212\nMaaa 92,248,208\nMaaa 60,248,200\n"
"Maaa 60,252,204\nD0 9,24,44,3072\nMaaa 28,252,196\nMaaa 28,248,192\ns_waitcnt vmcnt(17) lgkmcnt(0)\ns_barrier\nMaaa 0,160,192\nTa 224,16,32768\nMaaa 0,164,196\nTa 226,16,34816\n"
"Maaa 32,164,204\ns_add_u32 m0, s47, -49152\ns_add_u32 s48, s48, 128\nMaaa 32,160,200\nTa 228,16,49152\nMaaa 64,160,208\nTa 230,16,51200\nMaaa 64,164,212\nD0 2,20,48,0\n"
"Maaa 96,164,220\nTa 232,17,32768\nMaaa 96,160,216\nTa 234,17,34816\nMaaa 100,168,216\nMaaa 100,172,220\nTa 236,17,49152\nMaaa 68,172,212\nTa 238,17,51200\nMaaa 68,168,208\n"
"Maaa 36,168,200\nTa 240,18,32768\nMaaa 36,172,204\nTa 242,18,34816\nMaaa 4,172,196\nD0 3,20,48,1024\nMaaa 4,168,192\nTa 244,18,49152\nMaaa 8,176,192\nTa 246,18,51200\n"
"Maaa 8,180,196\nMaaa 40,180,204\nTa 248,19,32768\nMaaa 40,176,200\nTa 250,19,34816\nMaaa 72,176,208\nMaaa 72,180,212\nTa 252,19,49152\nMaaa 104,180,220\nTa 254,19,51200\n"
"Maaa 104,176,216\nD0 4,20,48,2048\nMaaa 108,184,216\nMaaa 108,188,220\nMaaa 76,188,212\nMaaa 76,184,208\nMaaa 44,184,200\nMaaa 44,188,204\nD0 5,20,48,3072\nMaaa 12,188,196\n"
"Maaa 12,184,192\nWL 0\nMvaa 80,224,128\nTa 160,12,32768\nMvaa 80,228,132\nTa 162,12,34816\nMvaa 112,228,140\ns_add_u32 m0, s47, -32768\ns_add_u32 s45, s45, 128\nMvaa 112,224,136\n"
"Ta 164,12,49152\nMvaa 144,224,144\nTa 166,12,51200\nMvaa 144,228,148\nD0 2,20,45,0\nMvaa 176,228,156\nTa 168,13,32768\nMvaa 176,224,152\nTa 170,13,34816\nMvaa 180,232,152\n"
"Mvaa 180,236,156\nTa 172,13,49152\nMvaa 148,236,148\nTa 174,13,51200\nMvaa 148,232,144\nMvaa 116,232,136\nTa 176,14,32768\nMvaa 116,236,140\nTa 178,14,34816\nMvaa 84,236,132\n"
"D0 3,20,45,1024\nMvaa 84,232,128\nTa 180,14,49152\nMvaa 88,240,128\nTa 182,14,51200\nMvaa 88,244,132\nMvaa 120,244,140\nTa 184,15,32768\nMvaa 120,240,136\nTa 186,15,34816\n"
"Mvaa 152,240,144\nMvaa 152,244,148\nTa 188,15,49152\nMvaa 184,244,156\nTa 190,15,51200\nMvaa 184,240,152\nD0 4,20,45,2048\nMvaa 188,248,152\nMvaa 188,252,156\nMvaa 156,252,148\n"
"Mvaa 156,248,144\ns_mov_b32 s40, s78\ns_and_b32 s41, s79, 0xffff\nMvaa 124,248,136\nMvaa 124,252,140\nD0 5,20,45,3072\nMvaa 92,252,132\nMvaa 92,248,128\n"
"s_waitcnt vmcnt(17) lgkmcnt(0)\ns_barrier\nMvaa 64,160,128\nRa 192,10,49152\nMvaa 64,164,132\ns_add_u32 m0, s47, 32768\ns_add_u32 s44, s44, 0x40000\n"
"buffer_load_dword v31, v26, s[40:43], s97 offen\ns_mov_b64 s[78:79], s[66:67]\nMvaa 96,164,140\nRa 196,11,49152\nMvaa 96,160,136\nMvaa 128,160,144\nRa 200,10,51200\n"
"Mvaa 128,164,148\nD0 6,24,44,0\nMvaa 160,164,156\nRa 204,11,51200\nMvaa 160,160,152\nMvaa 164,168,152\nRa 208,10,57344\nMvaa 164,172,156\nMvaa 132,172,148\nRa 212,11,57344\n"
"Mvaa 132,168,144\nMvaa 100,168,136\nRa 216,10,59392\nMvaa 100,172,140\nD0 7,24,44,1024\nMvaa 68,172,132\nRa 220,11,59392\nMvaa 68,168,128\nMvaa 72,176,128\nMvaa 72,180,132\n"
"Mvaa 104,180,140\nMvaa 104,176,136\nMvaa 136,176,144\nMvaa 136,180,148\nD0 8,24,44,2048\nMvaa 168,180,156\nMvaa 168,176,152\nMvaa 172,184,152\nMvaa 172,188,156\nMvaa 140,188,148\n"
"Mvaa 140,184,144\nMvaa 108,184,136\nMvaa 108,188,140\nD0 9,24,44,3072\nMvaa 76,188,132\nMvaa 76,184,128\nWL 0\nMaaa 0,160,192\nRa 128,10,0\nMaaa 0,164,196\n"
"s_add_u32 m0, s47, 49152\ns_add_u32 s44, s44, 0x40000\nMaaa 32,164,204\nRa 132,11,0\nMaaa 32,160,200\nMaaa 64,160,208\nRa 136,10,2048\nMaaa 64,164,212\nD0 6,24,44,0\n"
"Maaa 96,164,220\nRa 140,11,2048\nMaaa 96,160,216\nMaaa 100,168,216\nRa 144,10,8192\nMaaa 100,172,220\nMaaa 68,172,212\nRa 148,11,8192\nMaaa 68,168,208\nMaaa 36,168,200\n"
"Ra 152,10,10240\nMaaa 36,172,204\nD0 7,24,44,1024\nMaaa 4,172,196\nRa 156,11,10240\nMaaa 4,168,192\nMaaa 8,176,192\nMaaa 8,180,196\nMaaa 40,180,204\nMaaa 40,176,200\n"
"Maaa 72,176,208\nMaaa 72,180,212\nD0 8,24,44,2048\nMaaa 104,180,220\nMaaa 104,176,216\nMaaa 108,184,216\nMaaa 108,188,220\nMaaa 76,188,212\nMaaa 76,184,208\nMaaa 44,184,200\n"
"Maaa 44,188,204\nD0 9,24,44,3072\nMaaa 12,188,196\nMaaa 12,184,192\ns_waitcnt vmcnt(17) lgkmcnt(0)\ns_barrier\nMaaa 16,224,192\nTa 160,12,0\nMaaa 16,228,196\nTa 162,12,2048\n"
"Maaa 48,228,204\ns_add_u32 m0, s47, -16384\ns_add_u32 s48, s48, 128\nMaaa 48,224,200\nTa 164,12,16384\nMaaa 80,224,208\nTa 166,12,18432\nMaaa 80,228,212\nD0 2,20,48,0\n"
"Maaa 112,228,220\nTa 168,13,0\nMaaa 112,224,216\nTa 170,13,2048\nMaaa 116,232,216\nMaaa 116,236,220\nTa 172,13,16384\nMaaa 84,236,212\nTa 174,13,18432\nMaaa 84,232,208\n"
"Maaa 52,232,200\nTa 176,14,0\nMaaa 52,236,204\nTa 178,14,2048\n.W535:\ns_cbranch_execz .W539\nMaaa 20,236,196\nD0 3,20,48,1024\nMaaa 20,232,192\nTa 180,14,16384\nMaaa 24,240,192\n"
"Ta 182,14,18432\nMaaa 24,244,196\nMaaa 56,244,204\nTa 184,15,0\nMaaa 56,240,200\nTa 186,15,2048\nMaaa 88,240,208\nMaaa 88,244,212\nTa 188,15,16384\nMaaa 120,244,220\n"
"Ta 190,15,18432\nMaaa 120,240,216\nD0 4,20,48,2048\nMaaa 124,248,216\nMaaa 124,252,220\nMaaa 92,252,212\nMaaa 92,248,208\nMaaa 60,248,200\nMaaa 60,252,204\nD0 5,20,48,3072\n"
"s_sub_u32 s50, s50, 1\nMaaa 28,252,196\nMaaa 28,248,192\nWL 0\ns_cbranch_scc0 .GL20\nMvaa 64,160,128\nTa 224,16,0\nMvaa 64,164,132\nTa 226,16,2048\nMvaa 96,164,140\n"
"s_add_u32 m0, s47, -65536\ns_add_u32 s45, s45, 128\nMvaa 96,160,136\nTa 228,16,16384\nMvaa 128,160,144\nTa 230,16,18432\nMvaa 128,164,148\nD0 2,20,45,0\nMvaa 160,164,156\n"
"Ta 232,17,0\nMvaa 160,160,152\nTa 234,17,2048\nMvaa 164,168,152\ns_and_b32 s2, s74, 15\nMvaa 164,172,156\nTa 236,17,16384\n"
"buffer_store_dword v25, off, s[16:19], s90 offset:0 sc0 sc1\nMvaa 132,172,148\nTa 238,17,18432\nMvaa 132,168,144\ns_sub_u32 s2, s2, 2\ns_cmp_lt_u32 s2, 14\n"
"s_cselect_b32 s5, 0, 0x40000000\nMvaa 100,168,136\nTa 240,18,0\nMvaa 100,172,140\nTa 242,18,2048\nMvaa 68,172,132\nD0 3,20,45,1024\nMvaa 68,168,128\nTa 244,18,16384\n"
"Mvaa 72,176,128\nTa 246,18,18432\nMvaa 72,180,132\ns_and_b32 s2, s2, 15\nMvaa 104,180,140\nTa 248,19,0\nMvaa 104,176,136\nTa 250,19,2048\nMvaa 136,176,144\ns_mul_i32 s4, s73, 14\n"
"Mvaa 136,180,148\nTa 252,19,16384\nMvaa 168,180,156\nTa 254,19,18432\nMvaa 168,176,152\nD0 4,20,45,2048\nMvaa 172,184,152\ns_add_u32 s4, s4, s2\nMvaa 172,188,156\n"
"s_mov_b32 s36, s62\nMvaa 140,188,148\ns_and_b32 s37, s63, 0xffff\nMvaa 140,184,144\ns_mov_b32 s38, 0x188000\nMvaa 108,184,136\ns_mov_b32 s39, 0x20000\nMvaa 108,188,140\n"
"D0 5,20,45,3072\nMvaa 76,188,132\ns_mov_b32 s40, s64\nMvaa 76,184,128\ns_waitcnt vmcnt(18) lgkmcnt(0)\ns_barrier\nMvaa 80,224,128\nRa 192,10,16384\nMvaa 80,228,132\n"
"s_add_u32 m0, s47, 0\ns_add_u32 s44, s44, 0x40000\nMvaa 112,228,140\nRa 196,11,16384\nMvaa 112,224,136\ns_and_b32 s41, s65, 0xffff\nMvaa 144,224,144\nRa 200,10,18432\n"
"Mvaa 144,228,148\nD0 6,24,44,0\nMvaa 176,228,156\nRa 204,11,18432\nMvaa 176,224,152\ns_mov_b32 s42, 0x188000\nMvaa 180,232,152\nRa 208,10,24576\nMvaa 180,236,156\n"
"s_mov_b32 s43, 0x20000\nMvaa 148,236,148\nRa 212,11,24576\nMvaa 148,232,144\ns_mov_b32 s92, s66\nMvaa 116,232,136\nRa 216,10,26624\nMvaa 116,236,140\nD0 7,24,44,1024\n"
"Mvaa 84,236,132\nRa 220,11,26624\nMvaa 84,232,128\ns_and_b32 s93, s67, 0xffff\nMvaa 88,240,128\ns_mov_b32 s94, 0x188000\nMvaa 88,244,132\ns_mov_b32 s95, 0x20000\nMvaa 120,244,140\n"
"v_lshrrev_b32 v252, 1, v0\nMvaa 120,240,136\nv_mul_u32_u24 v253, 14336, v252\nMvaa 152,240,144\nv_and_b32 v254, 1, v0\nMvaa 152,244,148\nD0 8,24,44,2048\nMvaa 184,244,156\n"
"v_lshl_add_u32 v253, v254, 4, v253\nMvaa 184,240,152\nv_mov_b32 v254, 0x40000000\nv_cmp_gt_u32 vcc, 56, v0\nv_lshlrev_b32 v252, 3, v252\ns_mul_i32 s2, s11, 401408\n"
"v_cndmask_b32 v21, v254, v253, vcc\nv_cndmask_b32 v255, v254, v252, vcc\nMvaa 188,248,152\ns_lshl_b32 s3, s4, 5\nMvaa 188,252,156\ns_add_u32 s2, s2, s3\nMvaa 156,252,148\n"
"s_or_b32 s86, s2, s5\nMvaa 156,248,144\ns_add_u32 s87, s86, 7168\nMvaa 124,248,136\nv_lshrrev_b32 v251, 4, v0\nMvaa 124,252,140\nD0 9,24,44,3072\nMvaa 92,252,132\n"
"v_mul_u32_u24 v252, 3584, v251\nMvaa 92,248,128\nWL 0\nMaaa 16,224,192\nRa 128,10,32768\nMaaa 16,228,196\ns_add_u32 m0, s47, 16384\ns_add_u32 s44, s44, 0x40000\nMaaa 48,228,204\n"
"Ra 132,11,32768\nMaaa 48,224,200\nv_and_b32 v251, 15, v0\nMaaa 80,224,208\nRa 136,10,34816\nMaaa 80,228,212\nD0 6,24,44,0\nMaaa 112,228,220\nRa 140,11,34816\nMaaa 112,224,216\n"
"v_lshl_add_u32 v252, v251, 4, v252\nv_cmp_gt_u32 vcc, 14, v251\ns_mul_i32 s2, s4, 14336\ns_mul_i32 s3, s11, 224\nv_cndmask_b32 v20, v254, v252, vcc\nMaaa 116,232,216\n"
"Ra 144,10,40960\nMaaa 116,236,220\ns_add_u32 s2, s2, s3\nMaaa 84,236,212\nRa 148,11,40960\nMaaa 84,232,208\ns_or_b32 s88, s2, s5\nMaaa 52,232,200\nRa 152,10,43008\n"
"Maaa 52,236,204\nD0 7,24,44,1024\nMaaa 20,236,196\nRa 156,11,43008\nMaaa 20,232,192\nMaaa 24,240,192\nMaaa 24,244,196\nMaaa 56,244,204\nMaaa 56,240,200\nMaaa 88,240,208\n"
"Maaa 88,244,212\nD0 8,24,44,2048\nMaaa 120,244,220\nMaaa 120,240,216\nMaaa 124,248,216\nMaaa 124,252,220\nMaaa 92,252,212\nMaaa 92,248,208\nMaaa 60,248,200\nMaaa 60,252,204\n"
"D0 9,24,44,3072\nMaaa 28,252,196\nMaaa 28,248,192\ns_waitcnt vmcnt(17) lgkmcnt(0)\ns_barrier\nMaaa 0,160,192\nTa 224,16,32768\nMaaa 0,164,196\nTa 226,16,34816\n"
"buffer_load_dwordx4 v[192:195], v21, s[36:39], s86 offen offset:0\nMaaa 32,164,204\ns_add_u32 m0, s47, -49152\ns_add_u32 s48, s48, 128\nMaaa 32,160,200\nTa 228,16,49152\n"
"s_lshl_b32 s6, s11, 12\ns_add_u32 s6, s6, 0xc044000\nMaaa 64,160,208\nTa 230,16,51200\nMaaa 64,164,212\nD0 2,20,48,0\nMaaa 96,164,220\nTa 232,17,32768\nMaaa 96,160,216\n"
"Ta 234,17,34816\nMaaa 100,168,216\nbuffer_load_dwordx4 v[32:35], v1, s[16:19], s6 offen offset:0 sc0 sc1\nMaaa 100,172,220\nTa 236,17,49152\nMaaa 68,172,212\nTa 238,17,51200\n"
"Maaa 68,168,208\nMaaa 36,168,200\nTa 240,18,32768\nMaaa 36,172,204\nTa 242,18,34816\nMaaa 4,172,196\nD0 3,20,48,1024\nMaaa 4,168,192\nTa 244,18,49152\nMaaa 8,176,192\n"
"Ta 246,18,51200\nMaaa 8,180,196\nbuffer_load_dwordx4 v[36:39], v1, s[16:19], s6 offen offset:1024 sc0 sc1\nMaaa 40,180,204\nTa 248,19,32768\nMaaa 40,176,200\nTa 250,19,34816\n"
"Maaa 72,176,208\nMaaa 72,180,212\nTa 252,19,49152\nMaaa 104,180,220\nTa 254,19,51200\nMaaa 104,176,216\nD0 4,20,48,2048\nMaaa 108,184,216\n"
"buffer_load_dwordx4 v[40:43], v1, s[16:19], s6 offen offset:2048 sc0 sc1\nMaaa 108,188,220\nMaaa 76,188,212\nMaaa 76,184,208\nMaaa 44,184,200\n"
"buffer_load_dwordx4 v[44:47], v1, s[16:19], s6 offen offset:3072 sc0 sc1\nMaaa 44,188,204\nD0 5,20,48,3072\nMaaa 12,188,196\nMaaa 12,184,192\nWL 0\nMvaa 80,224,128\n"
"Ta 160,12,32768\nMvaa 80,228,132\nTa 162,12,34816\nMvaa 112,228,140\ns_add_u32 m0, s47, -32768\ns_add_u32 s45, s45, 128\nMvaa 112,224,136\nTa 164,12,49152\nMvaa 144,224,144\n"
"Ta 166,12,51200\nMvaa 144,228,148\nD0 2,20,45,0\nMvaa 176,228,156\nTa 168,13,32768\nMvaa 176,224,152\nTa 170,13,34816\nMvaa 180,232,152\nMvaa 180,236,156\nTa 172,13,49152\n"
"Mvaa 148,236,148\nTa 174,13,51200\nMvaa 148,232,144\nMvaa 116,232,136\nTa 176,14,32768\nMvaa 116,236,140\nTa 178,14,34816\nMvaa 84,236,132\nD0 3,20,45,1024\nMvaa 84,232,128\n"
"Ta 180,14,49152\nMvaa 88,240,128\nTa 182,14,51200\nMvaa 88,244,132\nMvaa 120,244,140\nTa 184,15,32768\nMvaa 120,240,136\nTa 186,15,34816\nMvaa 152,240,144\nMvaa 152,244,148\n"
"Ta 188,15,49152\nMvaa 184,244,156\nTa 190,15,51200\nMvaa 184,240,152\nD0 4,20,45,2048\nMvaa 188,248,152\nMvaa 188,252,156\nMvaa 156,252,148\nMvaa 156,248,144\nMvaa 124,248,136\n"
"Mvaa 124,252,140\nD0 5,20,45,3072\nMvaa 92,252,132\nMvaa 92,248,128\ns_waitcnt vmcnt(21) lgkmcnt(0)\ns_barrier\nMvaa 64,160,128\nRa 192,10,49152\nMvaa 64,164,132\n"
"s_add_u32 m0, s47, 32768\ns_add_u32 s44, s44, 0x40000\nbuffer_load_dwordx4 v[208:211], v21, s[40:43], s86 offen offset:0\nMvaa 96,164,140\nRa 196,11,49152\nMvaa 96,160,136\n"
"Mvaa 128,160,144\nRa 200,10,51200\nMvaa 128,164,148\nD0 6,24,44,0\nMvaa 160,164,156\nRa 204,11,51200\nMvaa 160,160,152\nMvaa 164,168,152\nRa 208,10,57344\nMvaa 164,172,156\n"
"Mvaa 132,172,148\nRa 212,11,57344\nMvaa 132,168,144\nMvaa 100,168,136\nRa 216,10,59392\nMvaa 100,172,140\nD0 7,24,44,1024\nMvaa 68,172,132\nRa 220,11,59392\nMvaa 68,168,128\n"
"Mvaa 72,176,128\nMvaa 72,180,132\nMvaa 104,180,140\nMvaa 104,176,136\nMvaa 136,176,144\nMvaa 136,180,148\nD0 8,24,44,2048\nMvaa 168,180,156\nMvaa 168,176,152\nMvaa 172,184,152\n"
"Mvaa 172,188,156\nMvaa 140,188,148\nMvaa 140,184,144\nMvaa 108,184,136\nMvaa 108,188,140\nD0 9,24,44,3072\nMvaa 76,188,132\nMvaa 76,184,128\nWL 0\nMaaa 0,160,192\nRa 128,10,0\n"
"Maaa 0,164,196\ns_add_u32 m0, s47, 49152\ns_add_u32 s44, s44, 0x40000\nMaaa 32,164,204\nRa 132,11,0\nMaaa 32,160,200\nMaaa 64,160,208\nRa 136,10,2048\nMaaa 64,164,212\n"
"D0 6,24,44,0\nMaaa 96,164,220\nRa 140,11,2048\nMaaa 96,160,216\nMaaa 100,168,216\nRa 144,10,8192\nMaaa 100,172,220\nMaaa 68,172,212\nRa 148,11,8192\nMaaa 68,168,208\n"
"Maaa 36,168,200\nRa 152,10,10240\nMaaa 36,172,204\nD0 7,24,44,1024\nMaaa 4,172,196\nRa 156,11,10240\nMaaa 4,168,192\nMaaa 8,176,192\nMaaa 8,180,196\nMaaa 40,180,204\n"
"Maaa 40,176,200\nMaaa 72,176,208\nMaaa 72,180,212\nD0 8,24,44,2048\nMaaa 104,180,220\nMaaa 104,176,216\nMaaa 108,184,216\nMaaa 108,188,220\nMaaa 76,188,212\nMaaa 76,184,208\n"
"Maaa 44,184,200\nMaaa 44,188,204\nD0 9,24,44,3072\nMaaa 12,188,196\nMaaa 12,184,192\ns_waitcnt vmcnt(14) lgkmcnt(0)\ns_barrier\nMaaa 16,224,192\nTa 160,12,0\n"
"v_xor_b32 v48, s15, v33\nMaaa 16,228,196\nTa 162,12,2048\nbuffer_load_dwordx4 v[196:199], v21, s[36:39], s86 offen offset:3584\nMaaa 48,228,204\ns_add_u32 m0, s47, -16384\n"
"s_add_u32 s48, s48, 128\nMaaa 48,224,200\nTa 164,12,16384\nMaaa 80,224,208\nTa 166,12,18432\nMaaa 80,228,212\nD0 2,20,48,0\nMaaa 112,228,220\nTa 168,13,0\nMaaa 112,224,216\n"
"Ta 170,13,2048\nMaaa 116,232,216\nv_xor_b32 v49, s15, v35\nMaaa 116,236,220\nTa 172,13,16384\nMaaa 84,236,212\nTa 174,13,18432\nMaaa 84,232,208\nv_xor_b32 v50, s15, v37\n"
"Maaa 52,232,200\nTa 176,14,0\nMaaa 52,236,204\nTa 178,14,2048\nMaaa 20,236,196\nD0 3,20,48,1024\nMaaa 20,232,192\nTa 180,14,16384\nMaaa 24,240,192\nTa 182,14,18432\n"
"v_xor_b32 v51, s15, v39\nMaaa 24,244,196\nv_xor_b32 v52, s15, v41\nMaaa 56,244,204\nTa 184,15,0\nMaaa 56,240,200\nTa 186,15,2048\nMaaa 88,240,208\nv_xor_b32 v53, s15, v43\n"
"Maaa 88,244,212\nTa 188,15,16384\nMaaa 120,244,220\nTa 190,15,18432\nMaaa 120,240,216\nD0 4,20,48,2048\nMaaa 124,248,216\nv_xor_b32 v54, s15, v45\nMaaa 124,252,220\n"
"v_xor_b32 v55, s15, v47\nMaaa 92,252,212\nv_or3_b32 v48, v48, v49, v50\nMaaa 92,248,208\nv_or3_b32 v51, v51, v52, v53\nMaaa 60,248,200\nv_or3_b32 v48, v48, v54, v55\n"
"Maaa 60,252,204\nD0 5,20,48,3072\nMaaa 28,252,196\nv_or_b32 v48, v48, v51\nMaaa 28,248,192\nWL 0\nMvaa 64,160,128\nTa 224,16,0\nv_cmp_ne_u32 vcc, 0, v48\nMvaa 64,164,132\n"
"Ta 226,16,2048\nMvaa 96,164,140\ns_add_u32 m0, s47, -65536\ns_add_u32 s45, s45, 128\nMvaa 96,160,136\nTa 228,16,16384\nMvaa 128,160,144\nTa 230,16,18432\nMvaa 128,164,148\n"
"D0 2,20,45,0\nMvaa 160,164,156\nTa 232,17,0\nMvaa 160,160,152\nTa 234,17,2048\nMvaa 164,168,152\ns_cbranch_vccnz .S14\n.S15:\nMvaa 164,172,156\nTa 236,17,16384\nMvaa 132,172,148\n"
"Ta 238,17,18432\nMvaa 132,168,144\nv_max_u32 v48, v32, v34\nMvaa 100,168,136\nTa 240,18,0\nMvaa 100,172,140\nTa 242,18,2048\nMvaa 68,172,132\nD0 3,20,45,1024\n.W536:\n"
"s_cbranch_execz .W540\nMvaa 68,168,128\nTa 244,18,16384\nMvaa 72,176,128\nTa 246,18,18432\nv_min_u32 v49, v32, v34\nMvaa 72,180,132\nv_med3_u32 v49, v48, v49, v36\n"
"Mvaa 104,180,140\nTa 248,19,0\nMvaa 104,176,136\nTa 250,19,2048\nMvaa 136,176,144\nv_max_u32 v48, v48, v36\nMvaa 136,180,148\nTa 252,19,16384\nMvaa 168,180,156\nTa 254,19,18432\n"
"Mvaa 168,176,152\nD0 4,20,45,2048\nMvaa 172,184,152\nv_med3_u32 v49, v48, v49, v38\nMvaa 172,188,156\nv_max_u32 v48, v48, v38\nMvaa 140,188,148\nv_med3_u32 v49, v48, v49, v40\n"
"Mvaa 140,184,144\nv_max_u32 v48, v48, v40\nMvaa 108,184,136\nv_med3_u32 v49, v48, v49, v42\nMvaa 108,188,140\nD0 5,20,45,3072\nMvaa 76,188,132\nv_max_u32 v48, v48, v42\n"
"Mvaa 76,184,128\ns_waitcnt vmcnt(18) lgkmcnt(0)\ns_barrier\nMvaa 80,224,128\nRa 192,10,16384\nv_med3_u32 v49, v48, v49, v44\nMvaa 80,228,132\ns_add_u32 m0, s47, 0\n"
"s_add_u32 s44, s44, 0x40000\nbuffer_load_dwordx4 v[212:215], v21, s[40:43], s86 offen offset:3584\nMvaa 112,228,140\nRa 196,11,16384\nMvaa 112,224,136\nv_max_u32 v48, v48, v44\n"
"Mvaa 144,224,144\nRa 200,10,18432\nMvaa 144,228,148\nD0 6,24,44,0\nMvaa 176,228,156\nRa 204,11,18432\nMvaa 176,224,152\nv_med3_u32 v49, v48, v49, v46\nMvaa 180,232,152\n"
"Ra 208,10,24576\nMvaa 180,236,156\nv_max_u32 v48, v48, v46\nMvaa 148,236,148\nRa 212,11,24576\nMvaa 148,232,144\nv_mov_b32 v50, v48\nMvaa 116,232,136\nRa 216,10,26624\n"
"Mvaa 116,236,140\nD0 7,24,44,1024\nMvaa 84,236,132\nRa 220,11,26624\nMvaa 84,232,128\nv_max_u32_dpp v50, v50, v50 row_ror:8 row_mask:0xf bank_mask:0xf\nMvaa 88,240,128\n"
"Mvaa 88,244,132\nv_max_u32_dpp v50, v50, v50 row_ror:4 row_mask:0xf bank_mask:0xf\nMvaa 120,244,140\nMvaa 120,240,136\n"
"v_max_u32_dpp v50, v50, v50 row_ror:2 row_mask:0xf bank_mask:0xf\nMvaa 152,240,144\nMvaa 152,244,148\nD0 8,24,44,2048\nMvaa 184,244,156\n"
"v_max_u32_dpp v50, v50, v50 row_ror:1 row_mask:0xf bank_mask:0xf\nMvaa 184,240,152\nv_mov_b32 v52, v50\nMvaa 188,248,152\nMvaa 188,252,156\nv_permlane16_swap_b32 v50, v52\n"
"Mvaa 156,252,148\nv_max_u32 v50, v50, v52\nMvaa 156,248,144\nv_mov_b32 v52, v50\nMvaa 124,248,136\nMvaa 124,252,140\nD0 9,24,44,3072\nMvaa 92,252,132\n"
"v_permlane32_swap_b32 v50, v52\nMvaa 92,248,128\nWL 0\nMaaa 16,224,192\nRa 128,10,32768\nv_max_u32 v50, v50, v52\nMaaa 16,228,196\ns_add_u32 m0, s47, 16384\n"
"s_add_u32 s44, s44, 0x40000\nMaaa 48,228,204\nRa 132,11,32768\nv_cmp_eq_u32 vcc, v48, v50\ns_lshl_b32 s2, s11, 3\ns_add_u32 s2, s2, 131072\nv_cndmask_b32 v51, v48, v49, vcc\n"
"Maaa 48,224,200\nMaaa 80,224,208\nRa 136,10,34816\nMaaa 80,228,212\nD0 6,24,44,0\nMaaa 112,228,220\nRa 140,11,34816\nMaaa 112,224,216\n"
"v_max_u32_dpp v51, v51, v51 row_ror:8 row_mask:0xf bank_mask:0xf\nMaaa 116,232,216\nRa 144,10,40960\nMaaa 116,236,220\n"
"v_max_u32_dpp v51, v51, v51 row_ror:4 row_mask:0xf bank_mask:0xf\nMaaa 84,236,212\nRa 148,11,40960\nMaaa 84,232,208\n"
"v_max_u32_dpp v51, v51, v51 row_ror:2 row_mask:0xf bank_mask:0xf\nMaaa 52,232,200\nRa 152,10,43008\nMaaa 52,236,204\nD0 7,24,44,1024\nMaaa 20,236,196\nRa 156,11,43008\n"
"Maaa 20,232,192\nv_max_u32_dpp v51, v51, v51 row_ror:1 row_mask:0xf bank_mask:0xf\nMaaa 24,240,192\nv_mov_b32 v52, v51\nMaaa 24,244,196\nMaaa 56,244,204\n"
"v_permlane16_swap_b32 v51, v52\nMaaa 56,240,200\nv_max_u32 v51, v51, v52\nMaaa 88,240,208\nv_mov_b32 v52, v51\nMaaa 88,244,212\nD0 8,24,44,2048\nMaaa 120,244,220\n"
"v_permlane32_swap_b32 v51, v52\nMaaa 120,240,216\nv_max_u32 v51, v51, v52\nMaaa 124,248,216\nv_mov_b32 v52, s2\nMaaa 124,252,220\nds_write_b64 v52, v[50:51]\nMaaa 92,252,212\n"
"v_mov_b32 v52, 131072\nMaaa 92,248,208\nMaaa 60,248,200\nMaaa 60,252,204\nD0 9,24,44,3072\nMaaa 28,252,196\nMaaa 28,248,192\ns_waitcnt vmcnt(18) lgkmcnt(0)\ns_barrier\n"
"Maaa 0,160,192\nTa 224,16,32768\nMaaa 0,164,196\nTa 226,16,34816\nbuffer_load_dwordx4 v[200:203], v21, s[36:39], s87 offen offset:0\nMaaa 32,164,204\ns_add_u32 m0, s47, -49152\n"
"s_add_u32 s48, s48, 128\nMaaa 32,160,200\nTa 228,16,49152\nMaaa 64,160,208\nTa 230,16,51200\nMaaa 64,164,212\nD0 2,20,48,0\nMaaa 96,164,220\nTa 232,17,32768\nMaaa 96,160,216\n"
"Ta 234,17,34816\nMaaa 100,168,216\nRv 32,52,0\nMaaa 100,172,220\nTa 236,17,49152\nMaaa 68,172,212\nTa 238,17,51200\nMaaa 68,168,208\nRv 36,52,16\nMaaa 36,168,200\nTa 240,18,32768\n"
"Maaa 36,172,204\nTa 242,18,34816\nMaaa 4,172,196\nD0 3,20,48,1024\nMaaa 4,168,192\nTa 244,18,49152\nMaaa 8,176,192\nTa 246,18,51200\nMaaa 8,180,196\nMaaa 40,180,204\n"
"Ta 248,19,32768\nMaaa 40,176,200\nTa 250,19,34816\nMaaa 72,176,208\nMaaa 72,180,212\nTa 252,19,49152\nMaaa 104,180,220\nTa 254,19,51200\nMaaa 104,176,216\nD0 4,20,48,2048\n"
"Maaa 108,184,216\nWL 11\nMaaa 108,188,220\nv_max_u32 v48, v32, v33\nMaaa 76,188,212\nv_min_u32 v49, v32, v33\nMaaa 76,184,208\nv_med3_u32 v49, v48, v49, v34\nMaaa 44,184,200\n"
"v_max_u32 v48, v48, v34\nMaaa 44,188,204\nD0 5,20,48,3072\nMaaa 12,188,196\nv_med3_u32 v49, v48, v49, v35\nMaaa 12,184,192\nWL 0\nMvaa 80,224,128\nTa 160,12,32768\n"
"Mvaa 80,228,132\nTa 162,12,34816\nMvaa 112,228,140\ns_add_u32 m0, s47, -32768\ns_add_u32 s45, s45, 128\nMvaa 112,224,136\nTa 164,12,49152\nMvaa 144,224,144\nTa 166,12,51200\n"
"Mvaa 144,228,148\nD0 2,20,45,0\nMvaa 176,228,156\nTa 168,13,32768\nMvaa 176,224,152\nTa 170,13,34816\nMvaa 180,232,152\nv_max_u32 v48, v48, v35\nMvaa 180,236,156\nTa 172,13,49152\n"
"Mvaa 148,236,148\nTa 174,13,51200\nMvaa 148,232,144\nv_med3_u32 v49, v48, v49, v36\nMvaa 116,232,136\nTa 176,14,32768\nMvaa 116,236,140\nTa 178,14,34816\nMvaa 84,236,132\n"
"D0 3,20,45,1024\nMvaa 84,232,128\nTa 180,14,49152\nMvaa 88,240,128\nTa 182,14,51200\nMvaa 88,244,132\nv_max_u32 v48, v48, v36\nMvaa 120,244,140\nTa 184,15,32768\nMvaa 120,240,136\n"
"Ta 186,15,34816\nMvaa 152,240,144\nv_med3_u32 v49, v48, v49, v37\nMvaa 152,244,148\nTa 188,15,49152\nMvaa 184,244,156\nTa 190,15,51200\nMvaa 184,240,152\nD0 4,20,45,2048\n"
"Mvaa 188,248,152\nv_max_u32 v48, v48, v37\nMvaa 188,252,156\nv_med3_u32 v49, v48, v49, v38\nMvaa 156,252,148\nv_max_u32 v48, v48, v38\nMvaa 156,248,144\n"
"v_med3_u32 v49, v48, v49, v39\nMvaa 124,248,136\nv_max_u32 v48, v48, v39\nMvaa 124,252,140\nD0 5,20,45,3072\nMvaa 92,252,132\nv_readfirstlane_b32 s2, v48\nMvaa 92,248,128\n"
"s_waitcnt vmcnt(18) lgkmcnt(0)\ns_barrier\nMvaa 64,160,128\nRa 192,10,49152\nMvaa 64,164,132\ns_add_u32 m0, s47, 32768\ns_add_u32 s44, s44, 0x40000\n"
"buffer_load_dwordx4 v[216:219], v21, s[40:43], s87 offen offset:0\nMvaa 96,164,140\nRa 196,11,49152\nMvaa 96,160,136\nv_readfirstlane_b32 s3, v49\nMvaa 128,160,144\n"
"Ra 200,10,51200\nMvaa 128,164,148\nD0 6,24,44,0\nMvaa 160,164,156\nRa 204,11,51200\nMvaa 160,160,152\ns_andn2_b32 s2, 0x7ff, s2\nMvaa 164,168,152\nRa 208,10,57344\n"
"Mvaa 164,172,156\ns_andn2_b32 s3, 0x7ff, s3\nMvaa 132,172,148\nRa 212,11,57344\nMvaa 132,168,144\ns_lshl_b32 s71, s3, 16\nMvaa 100,168,136\nRa 216,10,59392\nMvaa 100,172,140\n"
"D0 7,24,44,1024\nMvaa 68,172,132\nRa 220,11,59392\nMvaa 68,168,128\ns_or_b32 s71, s71, s2\nMvaa 72,176,128\ns_mul_i32 s2, s2, 896\nMvaa 72,180,132\ns_mul_i32 s3, s3, 896\n"
"Mvaa 104,180,140\ns_mul_i32 s6, s11, 224\nMvaa 104,176,136\ns_add_u32 s2, s2, s6\nMvaa 136,176,144\ns_add_u32 s3, s3, s6\nMvaa 136,180,148\nD0 8,24,44,2048\nMvaa 168,180,156\n"
"buffer_load_dwordx2 v[240:241], v255, s[28:31], s2 offen\nMvaa 168,176,152\nMvaa 172,184,152\nMvaa 172,188,156\nMvaa 140,188,148\n"
"buffer_load_dwordx2 v[242:243], v255, s[28:31], s3 offen\ns_lshl2_add_u32 s2, s10, s11\ns_cmp_eq_u32 s2, 512\ns_cselect_b32 s2, 0, 0x40000000\ns_add_u32 s2, s2, 0xc048000\n"
"Mvaa 140,184,144\nMvaa 108,184,136\nMvaa 108,188,140\nD0 9,24,44,3072\nMvaa 76,188,132\nbuffer_store_dword v25, off, s[16:19], s2 offset:0 sc0 sc1\nMvaa 76,184,128\nWL 0\n"
"Maaa 0,160,192\nRa 128,10,0\nMaaa 0,164,196\ns_add_u32 m0, s47, 49152\ns_add_u32 s44, s44, 0x40000\nMaaa 32,164,204\nRa 132,11,0\nMaaa 32,160,200\nMaaa 64,160,208\nRa 136,10,2048\n"
"Maaa 64,164,212\nD0 6,24,44,0\nMaaa 96,164,220\nRa 140,11,2048\nMaaa 96,160,216\nMaaa 100,168,216\nRa 144,10,8192\nMaaa 100,172,220\nMaaa 68,172,212\nRa 148,11,8192\n"
"Maaa 68,168,208\nMaaa 36,168,200\nRa 152,10,10240\nMaaa 36,172,204\nD0 7,24,44,1024\nMaaa 4,172,196\nRa 156,11,10240\nMaaa 4,168,192\nMaaa 8,176,192\nMaaa 8,180,196\n"
"Maaa 40,180,204\nMaaa 40,176,200\nMaaa 72,176,208\nMaaa 72,180,212\nD0 8,24,44,2048\nMaaa 104,180,220\nMaaa 104,176,216\nMaaa 108,184,216\nMaaa 108,188,220\nMaaa 76,188,212\n"
"Maaa 76,184,208\nMaaa 44,184,200\nMaaa 44,188,204\nD0 9,24,44,3072\nMaaa 12,188,196\nMaaa 12,184,192\ns_waitcnt vmcnt(21) lgkmcnt(0)\ns_barrier\nMaaa 16,224,192\nTa 160,12,0\n"
"Maaa 16,228,196\nTa 162,12,2048\nbuffer_load_dwordx4 v[204:207], v21, s[36:39], s87 offen offset:3584\nMaaa 48,228,204\ns_add_u32 m0, s47, -16384\ns_add_u32 s48, s48, 128\n"
"s_lshl_b32 s5, s73, 9\ns_add_u32 s5, s5, 0xc049400\nv_lshrrev_b32 v27, 1, v1\nbuffer_load_dwordx2 v[22:23], v27, s[16:19], s5 offen sc0 sc1\nMaaa 48,224,200\nTa 164,12,16384\n"
"Maaa 80,224,208\nTa 166,12,18432\nMaaa 80,228,212\nD0 2,20,48,0\nMaaa 112,228,220\nTa 168,13,0\nMaaa 112,224,216\nTa 170,13,2048\nMaaa 116,232,216\nMaaa 116,236,220\n"
"Ta 172,13,16384\nMaaa 84,236,212\nTa 174,13,18432\nMaaa 84,232,208\nMaaa 52,232,200\nTa 176,14,0\nMaaa 52,236,204\nTa 178,14,2048\nMaaa 20,236,196\nD0 3,20,48,1024\n"
"Maaa 20,232,192\nTa 180,14,16384\nMaaa 24,240,192\nTa 182,14,18432\nMaaa 24,244,196\nMaaa 56,244,204\nTa 184,15,0\nMaaa 56,240,200\nTa 186,15,2048\nMaaa 88,240,208\n"
"Maaa 88,244,212\nTa 188,15,16384\nMaaa 120,244,220\nTa 190,15,18432\nMaaa 120,240,216\nD0 4,20,48,2048\nMaaa 124,248,216\nMaaa 124,252,220\nMaaa 92,252,212\nMaaa 92,248,208\n"
"Maaa 60,248,200\nMaaa 60,252,204\nD0 5,20,48,3072\nMaaa 28,252,196\nMaaa 28,248,192\nWL 0\nMvaa 64,160,128\nTa 224,16,0\nMvaa 64,164,132\nTa 226,16,2048\nMvaa 96,164,140\n"
"s_add_u32 m0, s47, -65536\ns_add_u32 s45, s45, 128\nMvaa 96,160,136\nTa 228,16,16384\nMvaa 128,160,144\nTa 230,16,18432\nMvaa 128,164,148\nD0 2,20,45,0\nMvaa 160,164,156\n"
"Ta 232,17,0\nMvaa 160,160,152\nTa 234,17,2048\nMvaa 164,168,152\nMvaa 164,172,156\nTa 236,17,16384\nMvaa 132,172,148\nTa 238,17,18432\nMvaa 132,168,144\nMvaa 100,168,136\n"
"Ta 240,18,0\nMvaa 100,172,140\nTa 242,18,2048\nMvaa 68,172,132\nD0 3,20,45,1024\nMvaa 68,168,128\nTa 244,18,16384\nMvaa 72,176,128\nTa 246,18,18432\nMvaa 72,180,132\n"
"Mvaa 104,180,140\nTa 248,19,0\nMvaa 104,176,136\nTa 250,19,2048\nMvaa 136,176,144\nMvaa 136,180,148\nTa 252,19,16384\nMvaa 168,180,156\nTa 254,19,18432\nMvaa 168,176,152\n"
"D0 4,20,45,2048\nMvaa 172,184,152\nMvaa 172,188,156\nMvaa 140,188,148\nMvaa 140,184,144\nMvaa 108,184,136\nMvaa 108,188,140\nD0 5,20,45,3072\nMvaa 76,188,132\nMvaa 76,184,128\n"
"s_waitcnt vmcnt(22) lgkmcnt(0)\ns_barrier\nMvaa 80,224,128\nRa 192,10,16384\nWV 16\nMvaa 80,228,132\ns_add_u32 m0, s47, 0\ns_add_u32 s44, s44, 0x40000\n"
"buffer_load_dwordx4 v[220:223], v21, s[40:43], s87 offen offset:3584\nMvaa 112,228,140\nRa 196,11,16384\nv_lshlrev_b32 v244, 16, v240\nMvaa 112,224,136\n"
"v_and_b32 v245, 0xffff0000, v240\nv_lshlrev_b32 v246, 16, v241\nMvaa 144,224,144\nRa 200,10,18432\nv_and_b32 v247, 0xffff0000, v241\nMvaa 144,228,148\nD0 6,24,44,0\n"
"v_lshlrev_b32 v248, 16, v242\nMvaa 176,228,156\n.W537:\ns_cbranch_execz .W541\nRa 204,11,18432\nv_and_b32 v249, 0xffff0000, v242\nMvaa 176,224,152\nv_lshlrev_b32 v250, 16, v243\n"
"v_and_b32 v251, 0xffff0000, v243\nMvaa 180,232,152\nRa 208,10,24576\nv_lshlrev_b32 v252, 16, v192\nMvaa 180,236,156\nv_and_b32 v253, 0xffff0000, v192\nMU 32,244,252\n"
"Mvaa 148,236,148\nRa 212,11,24576\nMU 33,244,253\nMvaa 148,232,144\nMU 40,248,252\nMU 41,248,253\nMvaa 116,232,136\nRa 216,10,26624\nv_lshlrev_b32 v252, 16, v193\n"
"Mvaa 116,236,140\nD0 7,24,44,1024\nv_and_b32 v253, 0xffff0000, v193\nMvaa 84,236,132\nRa 220,11,26624\nMU 34,244,252\nMvaa 84,232,128\nMU 35,244,253\nMU 42,248,252\n"
"Mvaa 88,240,128\nMU 43,248,253\nv_lshlrev_b32 v252, 16, v194\nMvaa 88,244,132\nv_and_b32 v253, 0xffff0000, v194\nMvaa 120,244,140\nMU 36,244,252\nMU 37,244,253\nMvaa 120,240,136\n"
"MU 44,248,252\nMU 45,248,253\nMvaa 152,240,144\nv_lshlrev_b32 v252, 16, v195\nv_and_b32 v253, 0xffff0000, v195\nMvaa 152,244,148\nD0 8,24,44,2048\nMU 38,244,252\nMvaa 184,244,156\n"
"MU 39,244,253\nMU 46,248,252\nMvaa 184,240,152\nMU 47,248,253\nv_lshlrev_b32 v252, 16, v208\nMvaa 188,248,152\nv_and_b32 v253, 0xffff0000, v208\nMU 48,244,252\nMvaa 188,252,156\n"
"MU 49,244,253\nMU 56,248,252\nMvaa 156,252,148\nMU 57,248,253\nv_lshlrev_b32 v252, 16, v209\nMvaa 156,248,144\nv_and_b32 v253, 0xffff0000, v209\nMU 50,244,252\nMvaa 124,248,136\n"
"MU 51,244,253\nMU 58,248,252\nMvaa 124,252,140\nD0 9,24,44,3072\nMU 59,248,253\nMvaa 92,252,132\nv_lshlrev_b32 v252, 16, v210\nv_and_b32 v253, 0xffff0000, v210\nMvaa 92,248,128\n"
"WL 0\nMaaa 16,224,192\nRa 128,10,32768\nMU 52,244,252\nMaaa 16,228,196\ns_add_u32 m0, s47, 16384\ns_add_u32 s44, s44, 0x40000\nMaaa 48,228,204\nRa 132,11,32768\nMU 53,244,253\n"
"Maaa 48,224,200\nMU 60,248,252\nMU 61,248,253\nMaaa 80,224,208\nRa 136,10,34816\nv_lshlrev_b32 v252, 16, v211\nMaaa 80,228,212\nD0 6,24,44,0\nv_and_b32 v253, 0xffff0000, v211\n"
"Maaa 112,228,220\nRa 140,11,34816\nMU 54,244,252\nMaaa 112,224,216\nMU 55,244,253\nMU 62,248,252\nMaaa 116,232,216\nRa 144,10,40960\nMU 63,248,253\nMaaa 116,236,220\n"
"v_lshlrev_b32 v252, 16, v196\nv_and_b32 v253, 0xffff0000, v196\nMaaa 84,236,212\nRa 148,11,40960\nFM 32,245,252\nMaaa 84,232,208\nFM 33,245,253\nFM 40,249,252\nMaaa 52,232,200\n"
"Ra 152,10,43008\nFM 41,249,253\nMaaa 52,236,204\nD0 7,24,44,1024\nv_lshlrev_b32 v252, 16, v197\nMaaa 20,236,196\nRa 156,11,43008\nv_and_b32 v253, 0xffff0000, v197\n"
"Maaa 20,232,192\nFM 34,245,252\nFM 35,245,253\nMaaa 24,240,192\nFM 42,249,252\nFM 43,249,253\nMaaa 24,244,196\nv_lshlrev_b32 v252, 16, v198\nMaaa 56,244,204\n"
"v_and_b32 v253, 0xffff0000, v198\nFM 36,245,252\nMaaa 56,240,200\nFM 37,245,253\nFM 44,249,252\nMaaa 88,240,208\nFM 45,249,253\nv_lshlrev_b32 v252, 16, v199\nMaaa 88,244,212\n"
"D0 8,24,44,2048\nv_and_b32 v253, 0xffff0000, v199\nMaaa 120,244,220\nFM 38,245,252\nFM 39,245,253\nMaaa 120,240,216\nFM 46,249,252\nFM 47,249,253\nMaaa 124,248,216\n"
"v_lshlrev_b32 v252, 16, v212\nv_and_b32 v253, 0xffff0000, v212\nMaaa 124,252,220\nFM 48,245,252\nFM 49,245,253\nMaaa 92,252,212\nFM 56,249,252\nFM 57,249,253\nMaaa 92,248,208\n"
"v_lshlrev_b32 v252, 16, v213\nv_and_b32 v253, 0xffff0000, v213\nMaaa 60,248,200\nFM 50,245,252\nFM 51,245,253\nMaaa 60,252,204\nD0 9,24,44,3072\nFM 58,249,252\nMaaa 28,252,196\n"
"FM 59,249,253\nv_lshlrev_b32 v252, 16, v214\nMaaa 28,248,192\ns_waitcnt vmcnt(19) lgkmcnt(0)\ns_barrier\nMaaa 0,160,192\nTa 224,16,32768\nv_and_b32 v253, 0xffff0000, v214\n"
"Maaa 0,164,196\nTa 226,16,34816\nbuffer_load_dwordx4 v[224:227], v20, s[92:95], s88 offen offset:0\nMaaa 32,164,204\ns_add_u32 m0, s47, -49152\ns_add_u32 s48, s48, 128\n"
"Maaa 32,160,200\nTa 228,16,49152\nFM 52,245,252\nMaaa 64,160,208\nTa 230,16,51200\nFM 53,245,253\nMaaa 64,164,212\nD0 2,20,48,0\nFM 60,249,252\nMaaa 96,164,220\nTa 232,17,32768\n"
"FM 61,249,253\nMaaa 96,160,216\nTa 234,17,34816\nv_lshlrev_b32 v252, 16, v215\nMaaa 100,168,216\nv_and_b32 v253, 0xffff0000, v215\nFM 54,245,252\nMaaa 100,172,220\n"
"Ta 236,17,49152\nFM 55,245,253\nMaaa 68,172,212\nTa 238,17,51200\nFM 62,249,252\nMaaa 68,168,208\nFM 63,249,253\nv_lshlrev_b32 v252, 16, v200\nMaaa 36,168,200\nTa 240,18,32768\n"
"v_and_b32 v253, 0xffff0000, v200\nMaaa 36,172,204\nTa 242,18,34816\nFM 32,246,252\nMaaa 4,172,196\nD0 3,20,48,1024\nFM 33,246,253\nMaaa 4,168,192\nTa 244,18,49152\nFM 40,250,252\n"
"Maaa 8,176,192\nTa 246,18,51200\nFM 41,250,253\nMaaa 8,180,196\nv_lshlrev_b32 v252, 16, v201\nMaaa 40,180,204\nTa 248,19,32768\nv_and_b32 v253, 0xffff0000, v201\nMaaa 40,176,200\n"
"Ta 250,19,34816\nFM 34,246,252\nMaaa 72,176,208\nFM 35,246,253\nFM 42,250,252\nMaaa 72,180,212\nTa 252,19,49152\nFM 43,250,253\nMaaa 104,180,220\nTa 254,19,51200\n"
"v_lshlrev_b32 v252, 16, v202\nMaaa 104,176,216\nD0 4,20,48,2048\nv_and_b32 v253, 0xffff0000, v202\nMaaa 108,184,216\nFM 36,246,252\nFM 37,246,253\nMaaa 108,188,220\nFM 44,250,252\n"
"FM 45,250,253\nMaaa 76,188,212\nv_lshlrev_b32 v252, 16, v203\nv_and_b32 v253, 0xffff0000, v203\nMaaa 76,184,208\nFM 38,246,252\nFM 39,246,253\nMaaa 44,184,200\nFM 46,250,252\n"
"FM 47,250,253\nMaaa 44,188,204\nD0 5,20,48,3072\nv_lshlrev_b32 v252, 16, v216\nMaaa 12,188,196\nv_and_b32 v253, 0xffff0000, v216\nFM 48,246,252\nMaaa 12,184,192\nWL 0\n"
"Mvaa 80,224,128\nTa 160,12,32768\nFM 49,246,253\nMvaa 80,228,132\nTa 162,12,34816\nbuffer_load_dwordx4 v[228:231], v20, s[92:95], s88 offen offset:896\nMvaa 112,228,140\n"
"s_add_u32 m0, s47, -32768\ns_add_u32 s45, s45, 128\nMvaa 112,224,136\nTa 164,12,49152\nFM 56,250,252\nMvaa 144,224,144\nTa 166,12,51200\nFM 57,250,253\nMvaa 144,228,148\n"
"D0 2,20,45,0\nv_lshlrev_b32 v252, 16, v217\nMvaa 176,228,156\nTa 168,13,32768\nv_and_b32 v253, 0xffff0000, v217\nMvaa 176,224,152\nTa 170,13,34816\nFM 50,246,252\n"
"Mvaa 180,232,152\nFM 51,246,253\nFM 58,250,252\nMvaa 180,236,156\nTa 172,13,49152\nFM 59,250,253\nMvaa 148,236,148\nTa 174,13,51200\nv_lshlrev_b32 v252, 16, v218\n"
"Mvaa 148,232,144\nv_and_b32 v253, 0xffff0000, v218\nFM 52,246,252\nMvaa 116,232,136\nTa 176,14,32768\nFM 53,246,253\nMvaa 116,236,140\nTa 178,14,34816\nFM 60,250,252\n"
"Mvaa 84,236,132\nD0 3,20,45,1024\nFM 61,250,253\nMvaa 84,232,128\nTa 180,14,49152\nv_lshlrev_b32 v252, 16, v219\nMvaa 88,240,128\nTa 182,14,51200\n"
"v_and_b32 v253, 0xffff0000, v219\nMvaa 88,244,132\nFM 54,246,252\nWV 25\nv_cmp_ne_u32 vcc, s15, v23\ns_nop 0\ns_cbranch_vccnz .S17\n.S18:\nMvaa 120,244,140\nTa 184,15,32768\n"
"v_add_u32_dpp v22, v22, v22 row_ror:8 row_mask:0xf bank_mask:0xf\nMvaa 120,240,136\nTa 186,15,34816\nMvaa 152,240,144\n"
"v_add_u32_dpp v22, v22, v22 row_ror:4 row_mask:0xf bank_mask:0xf\nMvaa 152,244,148\nTa 188,15,49152\nMvaa 184,244,156\nTa 190,15,51200\n"
"v_add_u32_dpp v22, v22, v22 row_ror:2 row_mask:0xf bank_mask:0xf\nMvaa 184,240,152\nD0 4,20,45,2048\nMvaa 188,248,152\n"
"v_add_u32_dpp v22, v22, v22 row_ror:1 row_mask:0xf bank_mask:0xf\nMvaa 188,252,156\nMvaa 156,252,148\nv_mov_b32 v23, v22\nMvaa 156,248,144\nMvaa 124,248,136\n"
"v_permlane16_swap_b32 v22, v23\nv_add_u32 v22, v22, v23\nMvaa 124,252,140\nD0 5,20,45,3072\nv_mov_b32 v23, v22\nMvaa 92,252,132\nMvaa 92,248,128\ns_waitcnt vmcnt(19) lgkmcnt(0)\n"
"s_barrier\nMvaa 64,160,128\nRa 192,10,49152\nv_permlane32_swap_b32 v22, v23\nMvaa 64,164,132\ns_add_u32 m0, s47, 32768\ns_add_u32 s44, s44, 0x40000\n"
"buffer_load_dwordx4 v[232:235], v20, s[92:95], s88 offen offset:1792\nMvaa 96,164,140\nRa 196,11,49152\nv_add_u32 v22, v22, v23\nMvaa 96,160,136\nMvaa 128,160,144\n"
"Ra 200,10,51200\nv_readfirstlane_b32 s4, v22\nMvaa 128,164,148\nD0 6,24,44,0\nMvaa 160,164,156\nRa 204,11,51200\nMvaa 160,160,152\ns_cmp_gt_i32 s4, 0\ns_cselect_b32 s4, 1, 0\n"
"Mvaa 164,168,152\nRa 208,10,57344\ns_xor_b32 s4, s4, s12\nMvaa 164,172,156\ns_and_b32 s4, s4, 1\ns_lshl_b32 s4, s4, 13\nMvaa 132,172,148\nRa 212,11,57344\n"
"s_mul_i32 s72, s73, 25165824\nMvaa 132,168,144\ns_add_u32 s72, s72, s4\ns_and_b32 s4, s12, 1\nMvaa 100,168,136\nRa 216,10,59392\ns_lshl_b32 s4, s4, 21\nMvaa 100,172,140\n"
"D0 7,24,44,1024\ns_add_u32 s72, s72, s4\nMvaa 68,172,132\nRa 220,11,59392\ns_lshl3_add_u32 s2, s13, s11\nMvaa 68,168,128\ns_lshl_b32 s2, s2, 11\ns_lshl_b32 s3, s14, 22\n"
"Mvaa 72,176,128\ns_add_u32 s2, s2, s3\ns_add_u32 s2, s2, s72\nMvaa 72,180,132\ns_add_u32 s49, s2, 0xbe0000\nMvaa 104,180,140\nFM 55,246,253\nFM 62,250,252\nMvaa 104,176,136\n"
"FM 63,250,253\nv_lshlrev_b32 v252, 16, v204\nMvaa 136,176,144\nv_and_b32 v253, 0xffff0000, v204\nFM 32,247,252\nMvaa 136,180,148\nD0 8,24,44,2048\nFM 33,247,253\nMvaa 168,180,156\n"
"FM 40,251,252\nFM 41,251,253\nMvaa 168,176,152\nv_lshlrev_b32 v252, 16, v205\nv_and_b32 v253, 0xffff0000, v205\nMvaa 172,184,152\nFM 34,247,252\nFM 35,247,253\nMvaa 172,188,156\n"
"FM 42,251,252\nFM 43,251,253\nMvaa 140,188,148\nv_lshlrev_b32 v252, 16, v206\nv_and_b32 v253, 0xffff0000, v206\nMvaa 140,184,144\nFM 36,247,252\nFM 37,247,253\nMvaa 108,184,136\n"
"FM 44,251,252\nFM 45,251,253\nMvaa 108,188,140\nD0 9,24,44,3072\nv_lshlrev_b32 v252, 16, v207\nMvaa 76,188,132\nv_and_b32 v253, 0xffff0000, v207\nFM 38,247,252\nMvaa 76,184,128\n"
"s_waitcnt vmcnt(23) lgkmcnt(0)\nMaaa 0,160,192\nRa 128,10,0\nFM 39,247,253\nMaaa 0,164,196\ns_add_u32 m0, s47, 49152\ns_add_u32 s44, s44, 0x40000\n"
"buffer_load_dwordx4 v[236:239], v20, s[92:95], s88 offen offset:2688\nMaaa 32,164,204\nRa 132,11,0\nFM 46,251,252\nMaaa 32,160,200\nFM 47,251,253\nv_lshlrev_b32 v252, 16, v220\n"
"Maaa 64,160,208\nRa 136,10,2048\nv_and_b32 v253, 0xffff0000, v220\nMaaa 64,164,212\nD0 6,24,44,0\nFM 48,247,252\nMaaa 96,164,220\nRa 140,11,2048\nFM 49,247,253\nMaaa 96,160,216\n"
"FM 56,251,252\nFM 57,251,253\nMaaa 100,168,216\nRa 144,10,8192\nv_lshlrev_b32 v252, 16, v221\nMaaa 100,172,220\nv_and_b32 v253, 0xffff0000, v221\nFM 50,247,252\nMaaa 68,172,212\n"
"Ra 148,11,8192\nFM 51,247,253\nMaaa 68,168,208\nFM 58,251,252\nFM 59,251,253\nMaaa 36,168,200\nRa 152,10,10240\nv_lshlrev_b32 v252, 16, v222\nMaaa 36,172,204\nD0 7,24,44,1024\n"
"v_and_b32 v253, 0xffff0000, v222\nMaaa 4,172,196\nRa 156,11,10240\nFM 52,247,252\nMaaa 4,168,192\nFM 53,247,253\nFM 60,251,252\nMaaa 8,176,192\nFM 61,251,253\n"
"v_lshlrev_b32 v252, 16, v223\nMaaa 8,180,196\nv_and_b32 v253, 0xffff0000, v223\nMaaa 40,180,204\nFM 54,247,252\nFM 55,247,253\nMaaa 40,176,200\nFM 62,251,252\nFM 63,251,253\n"
"Maaa 72,176,208\nv_permlane32_swap_b32 v32, v48\nv_permlane32_swap_b32 v33, v49\nMaaa 72,180,212\nD0 8,24,44,2048\nv_permlane32_swap_b32 v34, v50\nMaaa 104,180,220\n"
"v_permlane32_swap_b32 v35, v51\nv_permlane32_swap_b32 v36, v52\nMaaa 104,176,216\nv_permlane32_swap_b32 v37, v53\nv_permlane32_swap_b32 v38, v54\nMaaa 108,184,216\n"
"v_permlane32_swap_b32 v39, v55\nv_permlane32_swap_b32 v40, v56\nMaaa 108,188,220\nv_permlane32_swap_b32 v41, v57\nv_permlane32_swap_b32 v42, v58\nMaaa 76,188,212\n"
"v_permlane32_swap_b32 v43, v59\nv_permlane32_swap_b32 v44, v60\nMaaa 76,184,208\nv_permlane32_swap_b32 v45, v61\nv_permlane32_swap_b32 v46, v62\nMaaa 44,184,200\n"
"v_permlane32_swap_b32 v47, v63\nAD 32,32,48\nMaaa 44,188,204\nD0 9,24,44,3072\nAD 33,33,49\nMaaa 12,188,196\nAD 34,34,50\nAD 35,35,51\nMaaa 12,184,192\n"
"s_waitcnt vmcnt(20) lgkmcnt(0)\ns_barrier\nMaaa 16,224,192\nTa 160,12,0\nAD 36,36,52\nMaaa 16,228,196\nTa 162,12,2048\nMaaa 48,228,204\ns_add_u32 m0, s47, -16384\n"
"s_add_u32 s48, s48, 128\nMaaa 48,224,200\nTa 164,12,16384\nAD 37,37,53\nMaaa 80,224,208\nTa 166,12,18432\nAD 38,38,54\nMaaa 80,228,212\nD0 2,20,48,0\nAD 39,39,55\n"
"Maaa 112,228,220\nTa 168,13,0\nAD 40,40,56\nMaaa 112,224,216\nTa 170,13,2048\nAD 41,41,57\nMaaa 116,232,216\nAD 42,42,58\nAD 43,43,59\nMaaa 116,236,220\nTa 172,13,16384\n"
"AD 44,44,60\nMaaa 84,236,212\nTa 174,13,18432\nAD 45,45,61\nMaaa 84,232,208\nAD 46,46,62\nAD 47,47,63\nMaaa 52,232,200\nTa 176,14,0\nv_permlane16_swap_b32 v32, v40\n"
"Maaa 52,236,204\nTa 178,14,2048\nv_permlane16_swap_b32 v33, v41\nMaaa 20,236,196\nD0 3,20,48,1024\nv_permlane16_swap_b32 v34, v42\nMaaa 20,232,192\nTa 180,14,16384\n"
"v_permlane16_swap_b32 v35, v43\nMaaa 24,240,192\nTa 182,14,18432\nv_permlane16_swap_b32 v36, v44\nMaaa 24,244,196\nv_permlane16_swap_b32 v37, v45\nMaaa 56,244,204\nTa 184,15,0\n"
"v_permlane16_swap_b32 v38, v46\nMaaa 56,240,200\nTa 186,15,2048\nv_permlane16_swap_b32 v39, v47\nMaaa 88,240,208\nAD 32,32,40\nAD 33,33,41\nMaaa 88,244,212\nTa 188,15,16384\n"
"AD 34,34,42\nMaaa 120,244,220\nTa 190,15,18432\nAD 35,35,43\nMaaa 120,240,216\nD0 4,20,48,2048\nAD 36,36,44\nMaaa 124,248,216\nAD 37,37,45\nAD 38,38,46\nMaaa 124,252,220\n"
"AD 39,39,47\nv_add_f32_dpp v32, v32, v32 row_ror:8 row_mask:0xf bank_mask:0xf\nMaaa 92,252,212\nv_add_f32_dpp v33, v33, v33 row_ror:8 row_mask:0xf bank_mask:0xf\n"
"v_add_f32_dpp v34, v34, v34 row_ror:8 row_mask:0xf bank_mask:0xf\nMaaa 92,248,208\nv_add_f32_dpp v35, v35, v35 row_ror:8 row_mask:0xf bank_mask:0xf\n"
"v_add_f32_dpp v36, v36, v36 row_ror:8 row_mask:0xf bank_mask:0xf\nMaaa 60,248,200\nv_add_f32_dpp v37, v37, v37 row_ror:8 row_mask:0xf bank_mask:0xf\n"
"v_add_f32_dpp v38, v38, v38 row_ror:8 row_mask:0xf bank_mask:0xf\nMaaa 60,252,204\nD0 5,20,48,3072\nv_add_f32_dpp v39, v39, v39 row_ror:8 row_mask:0xf bank_mask:0xf\n"
"Maaa 28,252,196\nv_add_f32_dpp v32, v32, v32 row_ror:4 row_mask:0xf bank_mask:0xf\nv_add_f32_dpp v33, v33, v33 row_ror:4 row_mask:0xf bank_mask:0xf\nMaaa 28,248,192\nWL 0\n"
"Mvaa 64,160,128\nTa 224,16,0\n.W538:\ns_cbranch_execz .W542\nv_add_f32_dpp v34, v34, v34 row_ror:4 row_mask:0xf bank_mask:0xf\nMvaa 64,164,132\nTa 226,16,2048\nMvaa 96,164,140\n"
"s_add_u32 m0, s47, -65536\ns_add_u32 s45, s45, 128\nMvaa 96,160,136\nTa 228,16,16384\nv_add_f32_dpp v35, v35, v35 row_ror:4 row_mask:0xf bank_mask:0xf\nMvaa 128,160,144\n"
"Ta 230,16,18432\nv_add_f32_dpp v36, v36, v36 row_ror:4 row_mask:0xf bank_mask:0xf\nMvaa 128,164,148\nD0 2,20,45,0\n"
"v_add_f32_dpp v37, v37, v37 row_ror:4 row_mask:0xf bank_mask:0xf\nMvaa 160,164,156\nTa 232,17,0\nv_add_f32_dpp v38, v38, v38 row_ror:4 row_mask:0xf bank_mask:0xf\n"
"Mvaa 160,160,152\nTa 234,17,2048\nv_add_f32_dpp v39, v39, v39 row_ror:4 row_mask:0xf bank_mask:0xf\nMvaa 164,168,152\n"
"v_add_f32_dpp v32, v32, v32 row_ror:2 row_mask:0xf bank_mask:0xf\nv_add_f32_dpp v33, v33, v33 row_ror:2 row_mask:0xf bank_mask:0xf\nMvaa 164,172,156\nTa 236,17,16384\n"
"v_add_f32_dpp v34, v34, v34 row_ror:2 row_mask:0xf bank_mask:0xf\nMvaa 132,172,148\nTa 238,17,18432\nv_add_f32_dpp v35, v35, v35 row_ror:2 row_mask:0xf bank_mask:0xf\n"
"Mvaa 132,168,144\nv_add_f32_dpp v36, v36, v36 row_ror:2 row_mask:0xf bank_mask:0xf\nv_add_f32_dpp v37, v37, v37 row_ror:2 row_mask:0xf bank_mask:0xf\nMvaa 100,168,136\n"
"Ta 240,18,0\nv_add_f32_dpp v38, v38, v38 row_ror:2 row_mask:0xf bank_mask:0xf\nMvaa 100,172,140\nTa 242,18,2048\nv_add_f32_dpp v39, v39, v39 row_ror:2 row_mask:0xf bank_mask:0xf\n"
"Mvaa 68,172,132\nD0 3,20,45,1024\nv_lshrrev_b32 v48, 4, v0\nMvaa 68,168,128\nTa 244,18,16384\nv_and_b32 v49, 1, v0\nMvaa 72,176,128\nTa 246,18,18432\nv_lshlrev_b32 v49, 5, v49\n"
"Mvaa 72,180,132\nv_lshl_add_u32 v49, v48, 6, v49\nMvaa 104,180,140\nTa 248,19,0\ns_lshl_b32 s2, s11, 8\nMvaa 104,176,136\nTa 250,19,2048\ns_add_u32 s2, s2, 132096\n"
"Mvaa 136,176,144\nv_add_u32 v49, s2, v49\nds_write_b128 v49, v[32:35]\nMvaa 136,180,148\nTa 252,19,16384\nds_write_b128 v49, v[36:39] offset:16\nMvaa 168,180,156\nTa 254,19,18432\n"
"v_and_b32 v50, 31, v0\nMvaa 168,176,152\nD0 4,20,45,2048\nv_lshlrev_b32 v50, 2, v50\nMvaa 172,184,152\ns_lshl_b32 s2, s11, 10\ns_add_u32 s2, s2, 133120\nMvaa 172,188,156\n"
"v_add_u32 v51, s2, v50\nv_add_u32 v50, 132096, v50\nMvaa 140,188,148\nv_lshl_add_u32 v52, v48, 4, s2\nMvaa 140,184,144\nMvaa 108,184,136\nMvaa 108,188,140\nD0 5,20,45,3072\n"
"Mvaa 76,188,132\nMvaa 76,184,128\ns_waitcnt vmcnt(18) lgkmcnt(0)\ns_barrier\nMvaa 80,224,128\nRa 192,10,16384\nds_read_b32 v32, v50 offset:0\nMvaa 80,228,132\n"
"s_add_u32 m0, s47, 0\ns_add_u32 s44, s44, 0x40000\nMvaa 112,228,140\nRa 196,11,16384\nMvaa 112,224,136\nds_read_b32 v33, v50 offset:256\nds_read_b32 v34, v50 offset:512\n"
"Mvaa 144,224,144\nRa 200,10,18432\nMvaa 144,228,148\nD0 6,24,44,0\nMvaa 176,228,156\nRa 204,11,18432\nds_read_b32 v35, v50 offset:768\nMvaa 176,224,152\n"
"ds_read_b32 v36, v50 offset:128\nMvaa 180,232,152\nRa 208,10,24576\nds_read_b32 v37, v50 offset:384\nMvaa 180,236,156\nds_read_b32 v38, v50 offset:640\nMvaa 148,236,148\n"
"Ra 212,11,24576\nMvaa 148,232,144\nds_read_b32 v39, v50 offset:896\nMvaa 116,232,136\nRa 216,10,26624\nMvaa 116,236,140\nD0 7,24,44,1024\nMvaa 84,236,132\nRa 220,11,26624\n"
"Mvaa 84,232,128\nMvaa 88,240,128\nMvaa 88,244,132\nMvaa 120,244,140\nMvaa 120,240,136\nMvaa 152,240,144\nMvaa 152,244,148\nD0 8,24,44,2048\nMvaa 184,244,156\nWL 2\nAD 32,32,33\n"
"Mvaa 184,240,152\nAD 34,34,35\nMvaa 188,248,152\nAD 36,36,37\nAD 38,38,39\nMvaa 188,252,156\nAD 32,32,34\nMvaa 156,252,148\nAD 36,36,38\nMvaa 156,248,144\nCV 32,32,36\n"
"v_and_b32 v36, 0xffff0000, v32\nMvaa 124,248,136\nv_lshlrev_b32 v32, 16, v32\nMvaa 124,252,140\nD0 9,24,44,3072\nMvaa 92,252,132\nv_mul_f32 v33, 0xbfb8aa3b, v32\nMvaa 92,248,128\n"
"WL 0\nMaaa 16,224,192\nRa 128,10,32768\nMaaa 16,228,196\ns_add_u32 m0, s47, 16384\ns_add_u32 s44, s44, 0x40000\nMaaa 48,228,204\nRa 132,11,32768\nEX 33,33\nMaaa 48,224,200\n"
"v_add_f32 v33, 1.0, v33\nMaaa 80,224,208\nRa 136,10,34816\nv_rcp_f32 v33, v33\nMaaa 80,228,212\nD0 6,24,44,0\nMaaa 112,228,220\nRa 140,11,34816\nMU 33,32,33\nMaaa 112,224,216\n"
"CV 33,33,33\nMaaa 116,232,216\nRa 144,10,40960\nv_lshlrev_b32 v33, 16, v33\nMaaa 116,236,220\nMU 33,33,36\nMaaa 84,236,212\nRa 148,11,40960\nMaaa 84,232,208\nCV 33,33,33\n"
"v_lshlrev_b32 v33, 16, v33\nMaaa 52,232,200\nRa 152,10,43008\nMaaa 52,236,204\nD0 7,24,44,1024\nMaaa 20,236,196\nRa 156,11,43008\nds_write_b32 v51, v33\nMaaa 20,232,192\n"
"Rv 244,52,0\nMaaa 24,240,192\nRv 248,52,64\ns_and_b32 s2, s74, 15\nMaaa 24,244,196\ns_sub_u32 s2, s2, 2\ns_cmp_lt_u32 s2, 14\ns_cselect_b32 s5, 0, 0x40000000\nMaaa 56,244,204\n"
"s_and_b32 s2, s2, 15\nMaaa 56,240,200\ns_mul_i32 s4, s73, 14\ns_add_u32 s4, s4, s2\nMaaa 88,240,208\nv_and_b32 v49, 15, v0\nMaaa 88,244,212\nD0 8,24,44,2048\nMaaa 120,244,220\n"
"v_lshlrev_b32 v50, 3, v49\nv_and_b32 v51, 1, v48\nMaaa 120,240,216\nv_lshl_add_u32 v50, v51, 2, v50\nMaaa 124,248,216\ns_mul_i32 s2, s11, 112\nv_add_u32 v50, s2, v50\n"
"Maaa 124,252,220\nv_lshrrev_b32 v51, 4, v50\nMaaa 92,252,212\nv_lshrrev_b32 v48, 1, v48\nMaaa 92,248,208\nv_mad_u32_u24 v51, v48, 28, v51\nv_mul_u32_u24 v51, 7168, v51\n"
"Maaa 60,248,200\nv_and_b32 v50, 15, v50\nMaaa 60,252,204\nD0 9,24,44,3072\nMaaa 28,252,196\nv_lshl_add_u32 v51, v50, 2, v51\nv_cmp_gt_u32 vcc, 14, v49\ns_lshl_b32 s4, s4, 6\n"
"s_add_u32 s4, s4, 0xc04c800\nv_cndmask_b32 v51, v254, v51, vcc\nMaaa 28,248,192\ns_waitcnt vmcnt(16) lgkmcnt(3)\ns_barrier\nMaaa 0,160,192\nTa 224,16,32768\ns_or_b32 s4, s4, s5\n"
"Maaa 0,164,196\nTa 226,16,34816\nMaaa 32,164,204\ns_add_u32 m0, s47, -49152\ns_add_u32 s48, s48, 128\nMaaa 32,160,200\nTa 228,16,49152\nWL 3\nMaaa 64,160,208\nTa 230,16,51200\n"
"Maaa 64,164,212\nD0 2,20,48,0\nMaaa 96,164,220\nTa 232,17,32768\nv_lshlrev_b32 v252, 16, v224\nMaaa 96,160,216\nTa 234,17,34816\nMaaa 100,168,216\n"
"v_and_b32 v253, 0xffff0000, v224\nMU 32,244,252\nMaaa 100,172,220\nTa 236,17,49152\nMaaa 68,172,212\nTa 238,17,51200\nMaaa 68,168,208\nMU 33,244,253\nMU 40,248,252\n"
"Maaa 36,168,200\nTa 240,18,32768\nMaaa 36,172,204\nTa 242,18,34816\nMaaa 4,172,196\nD0 3,20,48,1024\nMU 41,248,253\nMaaa 4,168,192\nTa 244,18,49152\nMaaa 8,176,192\n"
"Ta 246,18,51200\nv_lshlrev_b32 v252, 16, v225\nMaaa 8,180,196\nv_and_b32 v253, 0xffff0000, v225\nMaaa 40,180,204\nTa 248,19,32768\nMaaa 40,176,200\nTa 250,19,34816\nMU 34,244,252\n"
"Maaa 72,176,208\nMU 35,244,253\nMaaa 72,180,212\nTa 252,19,49152\nMaaa 104,180,220\nTa 254,19,51200\nMU 42,248,252\nMaaa 104,176,216\nD0 4,20,48,2048\nMaaa 108,184,216\n"
"MU 43,248,253\nv_lshlrev_b32 v252, 16, v226\nMaaa 108,188,220\nv_and_b32 v253, 0xffff0000, v226\nMaaa 76,188,212\nMU 36,244,252\nMaaa 76,184,208\nMU 37,244,253\nMU 44,248,252\n"
"Maaa 44,184,200\nMU 45,248,253\nMaaa 44,188,204\nD0 5,20,48,3072\nMaaa 12,188,196\nv_lshlrev_b32 v252, 16, v227\nv_and_b32 v253, 0xffff0000, v227\nMaaa 12,184,192\nWL 0\n"
"Mvaa 80,224,128\nTa 160,12,32768\nMU 38,244,252\nMvaa 80,228,132\nTa 162,12,34816\nMvaa 112,228,140\ns_add_u32 m0, s47, -32768\ns_add_u32 s45, s45, 128\nMvaa 112,224,136\n"
"Ta 164,12,49152\nMU 39,244,253\nMvaa 144,224,144\nTa 166,12,51200\nMvaa 144,228,148\nD0 2,20,45,0\nMvaa 176,228,156\nTa 168,13,32768\nMU 46,248,252\nMvaa 176,224,152\n"
"Ta 170,13,34816\nMvaa 180,232,152\nMU 47,248,253\nv_lshlrev_b32 v252, 16, v228\nMvaa 180,236,156\nTa 172,13,49152\nMvaa 148,236,148\nTa 174,13,51200\nMvaa 148,232,144\n"
"v_and_b32 v253, 0xffff0000, v228\nFM 32,245,252\nMvaa 116,232,136\nTa 176,14,32768\nMvaa 116,236,140\nTa 178,14,34816\nMvaa 84,236,132\nD0 3,20,45,1024\nFM 33,245,253\n"
"Mvaa 84,232,128\nTa 180,14,49152\nMvaa 88,240,128\nTa 182,14,51200\nFM 40,249,252\nMvaa 88,244,132\nFM 41,249,253\nMvaa 120,244,140\nTa 184,15,32768\nMvaa 120,240,136\n"
"Ta 186,15,34816\nv_lshlrev_b32 v252, 16, v229\nMvaa 152,240,144\nv_and_b32 v253, 0xffff0000, v229\nMvaa 152,244,148\nTa 188,15,49152\nMvaa 184,244,156\nTa 190,15,51200\n"
"FM 34,245,252\nMvaa 184,240,152\nD0 4,20,45,2048\nMvaa 188,248,152\nFM 35,245,253\nFM 42,249,252\nMvaa 188,252,156\nFM 43,249,253\nMvaa 156,252,148\nv_lshlrev_b32 v252, 16, v230\n"
"Mvaa 156,248,144\nv_and_b32 v253, 0xffff0000, v230\nFM 36,245,252\nMvaa 124,248,136\nFM 37,245,253\nMvaa 124,252,140\nD0 5,20,45,3072\nMvaa 92,252,132\nFM 44,249,252\n"
"FM 45,249,253\nMvaa 92,248,128\ns_waitcnt vmcnt(16) lgkmcnt(0)\ns_barrier\nMvaa 64,160,128\nRa 192,10,49152\nv_lshlrev_b32 v252, 16, v231\nMvaa 64,164,132\n"
"s_add_u32 m0, s47, 32768\ns_add_u32 s44, s44, 0x40000\nMvaa 96,164,140\nRa 196,11,49152\nMvaa 96,160,136\nv_and_b32 v253, 0xffff0000, v231\nFM 38,245,252\nMvaa 128,160,144\n"
"Ra 200,10,51200\nMvaa 128,164,148\nD0 6,24,44,0\nMvaa 160,164,156\nRa 204,11,51200\nFM 39,245,253\nMvaa 160,160,152\nFM 46,249,252\nMvaa 164,168,152\nRa 208,10,57344\n"
"FM 47,249,253\nMvaa 164,172,156\nv_lshlrev_b32 v252, 16, v232\nMvaa 132,172,148\nRa 212,11,57344\nMvaa 132,168,144\nv_and_b32 v253, 0xffff0000, v232\nFM 32,246,252\n"
"Mvaa 100,168,136\nRa 216,10,59392\nMvaa 100,172,140\nD0 7,24,44,1024\nMvaa 68,172,132\nRa 220,11,59392\nFM 33,246,253\nMvaa 68,168,128\nFM 40,250,252\nMvaa 72,176,128\n"
"FM 41,250,253\nv_lshlrev_b32 v252, 16, v233\nMvaa 72,180,132\nv_and_b32 v253, 0xffff0000, v233\nMvaa 104,180,140\nFM 34,246,252\nMvaa 104,176,136\nFM 35,246,253\nFM 42,250,252\n"
"Mvaa 136,176,144\nFM 43,250,253\nMvaa 136,180,148\nD0 8,24,44,2048\nMvaa 168,180,156\nv_lshlrev_b32 v252, 16, v234\nv_and_b32 v253, 0xffff0000, v234\nMvaa 168,176,152\n"
"FM 36,246,252\nMvaa 172,184,152\nFM 37,246,253\nFM 44,250,252\nMvaa 172,188,156\nFM 45,250,253\nMvaa 140,188,148\nv_lshlrev_b32 v252, 16, v235\nMvaa 140,184,144\n"
"v_and_b32 v253, 0xffff0000, v235\nFM 38,246,252\nMvaa 108,184,136\nFM 39,246,253\nMvaa 108,188,140\nD0 9,24,44,3072\nMvaa 76,188,132\nFM 46,250,252\nFM 47,250,253\n"
"Mvaa 76,184,128\nWL 0\nMaaa 0,160,192\nRa 128,10,0\nv_lshlrev_b32 v252, 16, v236\nMaaa 0,164,196\ns_add_u32 m0, s47, 49152\ns_add_u32 s44, s44, 0x40000\nMaaa 32,164,204\n"
"Ra 132,11,0\nMaaa 32,160,200\nv_and_b32 v253, 0xffff0000, v236\nFM 32,247,252\nMaaa 64,160,208\nRa 136,10,2048\nMaaa 64,164,212\nD0 6,24,44,0\nMaaa 96,164,220\nRa 140,11,2048\n"
"FM 33,247,253\nMaaa 96,160,216\nFM 40,251,252\nMaaa 100,168,216\nRa 144,10,8192\nFM 41,251,253\nMaaa 100,172,220\nv_lshlrev_b32 v252, 16, v237\nMaaa 68,172,212\nRa 148,11,8192\n"
"Maaa 68,168,208\nv_and_b32 v253, 0xffff0000, v237\nFM 34,247,252\nMaaa 36,168,200\nRa 152,10,10240\nMaaa 36,172,204\nD0 7,24,44,1024\nMaaa 4,172,196\nRa 156,11,10240\n"
"FM 35,247,253\nMaaa 4,168,192\nFM 42,251,252\nMaaa 8,176,192\nFM 43,251,253\nv_lshlrev_b32 v252, 16, v238\nMaaa 8,180,196\nv_and_b32 v253, 0xffff0000, v238\nMaaa 40,180,204\n"
"FM 36,247,252\nMaaa 40,176,200\nFM 37,247,253\nFM 44,251,252\nMaaa 72,176,208\nFM 45,251,253\nMaaa 72,180,212\nD0 8,24,44,2048\nMaaa 104,180,220\nv_lshlrev_b32 v252, 16, v239\n"
"v_and_b32 v253, 0xffff0000, v239\nMaaa 104,176,216\nFM 38,247,252\nMaaa 108,184,216\nFM 39,247,253\nFM 46,251,252\nMaaa 108,188,220\nFM 47,251,253\nMaaa 76,188,212\n"
"v_permlane32_swap_b32 v32, v40\nMaaa 76,184,208\nv_permlane32_swap_b32 v33, v41\nv_permlane32_swap_b32 v34, v42\nMaaa 44,184,200\nv_permlane32_swap_b32 v35, v43\nMaaa 44,188,204\n"
"D0 9,24,44,3072\nMaaa 12,188,196\nv_permlane32_swap_b32 v36, v44\nv_permlane32_swap_b32 v37, v45\nMaaa 12,184,192\nWV 16\ns_barrier\nMaaa 16,224,192\nTa 160,12,0\n"
"v_permlane32_swap_b32 v38, v46\nMaaa 16,228,196\nTa 162,12,2048\nMaaa 48,228,204\ns_add_u32 m0, s47, -16384\ns_add_u32 s48, s48, 128\nMaaa 48,224,200\nTa 164,12,16384\n"
"v_permlane32_swap_b32 v39, v47\nMaaa 80,224,208\nTa 166,12,18432\nMaaa 80,228,212\nD0 2,20,48,0\nMaaa 112,228,220\nTa 168,13,0\nAD 32,32,40\nMaaa 112,224,216\nTa 170,13,2048\n"
"Maaa 116,232,216\nAD 33,33,41\nAD 34,34,42\nMaaa 116,236,220\nTa 172,13,16384\nMaaa 84,236,212\nTa 174,13,18432\nMaaa 84,232,208\nAD 35,35,43\nAD 36,36,44\nMaaa 52,232,200\n"
"Ta 176,14,0\nMaaa 52,236,204\nTa 178,14,2048\nMaaa 20,236,196\nD0 3,20,48,1024\nAD 37,37,45\nMaaa 20,232,192\nTa 180,14,16384\nMaaa 24,240,192\nTa 182,14,18432\nAD 38,38,46\n"
"Maaa 24,244,196\nAD 39,39,47\n.W539:\ns_cbranch_execz .W543\nMaaa 56,244,204\nTa 184,15,0\nMaaa 56,240,200\nTa 186,15,2048\nv_permlane16_swap_b32 v32, v36\nMaaa 88,240,208\n"
"v_permlane16_swap_b32 v33, v37\nMaaa 88,244,212\nTa 188,15,16384\nMaaa 120,244,220\nTa 190,15,18432\nv_permlane16_swap_b32 v34, v38\nMaaa 120,240,216\nD0 4,20,48,2048\n"
"Maaa 124,248,216\nv_permlane16_swap_b32 v35, v39\nAD 32,32,36\nMaaa 124,252,220\nAD 33,33,37\nMaaa 92,252,212\nAD 34,34,38\nMaaa 92,248,208\nAD 35,35,39\nSv 32,51,16,4,0\n"
"Maaa 60,248,200\nMaaa 60,252,204\nD0 5,20,48,3072\nMaaa 28,252,196\nMaaa 28,248,192\nWL 0\nMvaa 64,160,128\nTa 224,16,0\ns_lshr_b32 s3, s11, 1\nMvaa 64,164,132\nTa 226,16,2048\n"
"Mvaa 96,164,140\ns_lshl_b32 s3, s3, 13\nMvaa 96,160,136\nTa 228,16,16384\nMvaa 128,160,144\nTa 230,16,18432\nMvaa 128,164,148\ns_lshl_b32 s2, s11, 12\nMvaa 160,164,156\n"
"Ta 232,17,0\nMvaa 160,160,152\nTa 234,17,2048\nMvaa 164,168,152\ns_add_u32 s2, s2, s3\ns_add_u32 s2, s2, s72\nMvaa 164,172,156\nTa 236,17,16384\nMvaa 132,172,148\nTa 238,17,18432\n"
"Mvaa 132,168,144\ns_lshl_b32 s3, s11, 12\nMvaa 100,168,136\nTa 240,18,0\nMvaa 100,172,140\nTa 242,18,2048\nMvaa 68,172,132\ns_add_u32 s89, s3, 0x0\nMvaa 68,168,128\n"
"Ta 244,18,16384\nMvaa 72,176,128\nTa 246,18,18432\ns_mov_b32 s88, 0xffff8000\nMvaa 72,180,132\ns_lshl_b32 s4, s13, 1\nMvaa 104,180,140\nTa 248,19,0\nMvaa 104,176,136\n"
"Ta 250,19,2048\nMvaa 136,176,144\ns_sub_u32 s79, 15, s4\nMvaa 136,180,148\nTa 252,19,16384\nMvaa 168,180,156\nTa 254,19,18432\nMvaa 168,176,152\ns_add_u32 s96, s2, 0x1310000\n"
"Mvaa 172,184,152\ns_mov_b32 s63, 0x53800000\ns_mov_b32 s64, 0x2b800000\nMvaa 172,188,156\ns_sub_u32 s98, s4, 1\nMvaa 140,188,148\ns_lshl_b32 s5, s13, 5\nMvaa 140,184,144\n"
"s_add_u32 s5, s5, s11\nMvaa 108,184,136\ns_lshl_b32 s65, s5, 9\nMvaa 108,188,140\ns_add_u32 s65, s65, s72\nMvaa 76,188,132\ns_add_u32 s65, s65, 0x1580000\nMvaa 76,184,128\n"
"s_waitcnt vmcnt(13) lgkmcnt(0)\ns_barrier\nMvaa 80,224,128\nRa 192,10,16384\ns_and_b32 s66, s72, 0x2000\ns_add_u32 m0, s47, 65536\ns_add_u32 s46, s56, 0x0\nMvaa 80,228,132\n"
"s_add_u32 s66, s66, s56\nMvaa 112,228,140\nRa 196,11,16384\nD1 1,16,46,0\nMvaa 112,224,136\ns_add_u32 s66, s66, 0x100000\nMvaa 144,224,144\nRa 200,10,18432\nMvaa 144,228,148\n"
"s_and_b32 s5, s12, 1\nMvaa 176,228,156\nRa 204,11,18432\nMvaa 176,224,152\ns_mul_i32 s58, s5, 0x202000\nD1 1,16,46,1024\nMvaa 180,232,152\nRa 208,10,24576\n"
"s_xor_b32 s58, s58, s72\nMvaa 180,236,156\ns_add_u32 s58, s58, 0x15c0000\nMvaa 148,236,148\nRa 212,11,24576\nMvaa 148,232,144\ns_lshl_b32 s5, s5, 8\nMvaa 116,232,136\n"
"Ra 216,10,26624\nD1 1,16,46,2048\nMvaa 116,236,140\ns_add_u32 s61, s58, s5\nMvaa 84,236,132\nRa 220,11,26624\nMvaa 84,232,128\ns_lshl4_add_u32 s60, s13, s61\nMvaa 88,240,128\n"
"s_add_u32 s54, s60, 0\ns_xor_b32 s60, s54, 0x100\nMvaa 88,244,132\ns_cmp_eq_u32 s11, 0\ns_cselect_b32 s54, s54, 0x40000000\nD1 1,16,46,3072\nMvaa 120,244,140\ns_getpc_b64 s[8:9]\n"
".PC37:\ns_add_u32 s8, s8, .HD13-.PC37\ns_addc_u32 s9, s9, 0\nMvaa 120,240,136\ns_mov_b32 s97, 0xff800000\nMvaa 152,240,144\ns_mov_b32 s90, 3\ns_cmp_lt_u32 s11, 2\n"
"s_cselect_b32 s92, 2, 0\ns_add_u32 m0, s47, 81920\ns_add_u32 s46, s56, 0x1000\nMvaa 152,244,148\ns_add_u32 s86, s2, 0x20000\nMvaa 184,244,156\ns_add_u32 s87, s2, 0x410000\n"
"s_cmp_eq_u32 s13, 7\ns_cselect_b32 s92, s92, 0\nD1 1,16,46,0\nMvaa 184,240,152\ns_cmp_gt_u32 s13, 5\ns_cselect_b32 s4, s12, 15\nMvaa 188,248,152\ns_cmp_lt_u32 s4, 14\n"
"s_cselect_b32 s5, s11, 3\nMvaa 188,252,156\ns_cmp_lt_u32 s5, 2\ns_cselect_b32 s91, 1, 100\nMvaa 156,252,148\nv_and_b32 v27, 0x70, v1\n"
"buffer_load_dword v27, v27, s[16:19], s61 offen offset:128 sc0 sc1\nMvaa 156,248,144\ns_lshr_b32 s2, s13, 2\ns_bfe_u32 s3, s13, 0x10001\nD1 1,16,46,1024\nMvaa 124,248,136\n"
"s_mul_i32 s3, s3, 17\ns_add_u32 s2, s2, s3\nMvaa 124,252,140\ns_lshl2_add_u32 s3, s14, 0\nMvaa 92,252,132\ns_lshl_b32 s3, s3, 2\ns_lshr_b32 s3, 0xe420ca86, s3\nMvaa 92,248,128\n"
"WL 0\nMaaa 16,224,192\nRa 128,10,32768\ns_and_b32 s3, s3, 15\nD1 1,16,46,2048\nMaaa 16,228,196\ns_add_u32 s2, s2, s3\ns_cmp_eq_u32 s2, 32\ns_cselect_b32 s4, 16, s2\n"
"Maaa 48,228,204\nRa 132,11,32768\ns_mul_i32 s75, s4, 65536\nMaaa 48,224,200\ns_lshr_b32 s2, s13, 2\ns_bfe_u32 s3, s13, 0x10001\nMaaa 80,224,208\nRa 136,10,34816\nMaaa 80,228,212\n"
"s_mul_i32 s3, s3, 17\ns_add_u32 s2, s2, s3\nD1 1,16,46,3072\nMaaa 112,228,220\nRa 140,11,34816\ns_lshl2_add_u32 s3, s14, 1\nMaaa 112,224,216\ns_lshl_b32 s3, s3, 2\n"
"Maaa 116,232,216\nRa 144,10,40960\ns_lshr_b32 s3, 0xe420ca86, s3\ns_add_u32 m0, s47, 16384\ns_add_u32 s46, s56, 0x4000\nMaaa 116,236,220\ns_and_b32 s3, s3, 15\n"
"s_add_u32 s2, s2, s3\nMaaa 84,236,212\nRa 148,11,40960\ns_cmp_eq_u32 s2, 32\ns_cselect_b32 s4, 16, s2\nD1 1,16,46,0\nMaaa 84,232,208\ns_mul_i32 s76, s4, 65536\n"
"s_lshr_b32 s2, s13, 2\nMaaa 52,232,200\nRa 152,10,43008\ns_bfe_u32 s3, s13, 0x10001\nMaaa 52,236,204\ns_mul_i32 s3, s3, 17\nMaaa 20,236,196\nRa 156,11,43008\ns_add_u32 s2, s2, s3\n"
"Maaa 20,232,192\ns_lshl2_add_u32 s3, s14, 2\ns_lshl_b32 s3, s3, 2\nD1 1,16,46,1024\nMaaa 24,240,192\ns_lshr_b32 s3, 0xe420ca86, s3\ns_and_b32 s3, s3, 15\nMaaa 24,244,196\n"
"s_add_u32 s2, s2, s3\ns_cmp_eq_u32 s2, 32\ns_cselect_b32 s4, 16, s2\nMaaa 56,244,204\ns_mul_i32 s77, s4, 65536\ns_lshr_b32 s2, s13, 2\nMaaa 56,240,200\ns_bfe_u32 s3, s13, 0x10001\n"
"s_mul_i32 s3, s3, 17\nMaaa 88,240,208\ns_add_u32 s2, s2, s3\nD1 1,16,46,2048\nMaaa 88,244,212\ns_lshl2_add_u32 s3, s14, 3\ns_lshl_b32 s3, s3, 2\nMaaa 120,244,220\n"
"s_lshr_b32 s3, 0xe420ca86, s3\ns_and_b32 s3, s3, 15\nMaaa 120,240,216\ns_add_u32 s2, s2, s3\ns_cmp_eq_u32 s2, 32\ns_cselect_b32 s4, 16, s2\nMaaa 124,248,216\n"
"s_mul_i32 s78, s4, 65536\ns_lshr_b32 s3, s11, 1\nMaaa 124,252,220\ns_lshl_b32 s3, s3, 15\ns_add_u32 s2, s72, s3\nD1 1,16,46,3072\nMaaa 92,252,212\ns_and_b32 s3, s11, 1\n"
"Maaa 92,248,208\ns_lshl_b32 s3, s3, 12\ns_add_u32 s2, s2, s3\nMaaa 60,248,200\ns_add_u32 s2, s2, 0x0\ns_mov_b32 s55, 16384\nMaaa 60,252,204\ns_add_u32 s75, s75, s2\n"
"Maaa 28,252,196\ns_add_u32 s76, s76, s2\ns_add_u32 s77, s77, s2\nMaaa 28,248,192\ns_waitcnt vmcnt(18) lgkmcnt(0)\ns_barrier\nMaaa 0,160,192\nTa 224,16,32768\n"
"s_add_u32 s78, s78, s2\nMaaa 0,164,196\nTa 226,16,34816\nMaaa 32,164,204\nMaaa 32,160,200\nTa 228,16,49152\nMaaa 64,160,208\nTa 230,16,51200\nMaaa 64,164,212\nMaaa 96,164,220\n"
"Ta 232,17,32768\nMaaa 96,160,216\nTa 234,17,34816\nMaaa 100,168,216\nWV 7\nv_cmp_ne_u32 vcc, s15, v27\ns_nop 0\ns_cbranch_vccnz .S38\n.S39:\ns_sub_u32 s45, s86, 131072\n"
"Maaa 100,172,220\nTa 236,17,49152\ns_add_u32 m0, s47, 0\ns_add_u32 s46, s45, 0x0\nMaaa 68,172,212\nTa 238,17,51200\nMaaa 68,168,208\nD1 1,16,46,0\nMaaa 36,168,200\n"
"Ta 240,18,32768\ns_mov_b32 s36, s68\nMaaa 36,172,204\nTa 242,18,34816\nMaaa 4,172,196\ns_and_b32 s37, s69, 0xffff\nMaaa 4,168,192\nTa 244,18,49152\nMaaa 8,176,192\n"
"Ta 246,18,51200\nD1 1,16,46,1024\nMaaa 8,180,196\ns_mov_b32 s38, 0x1c0000\nMaaa 40,180,204\nTa 248,19,32768\nMaaa 40,176,200\nTa 250,19,34816\nMaaa 72,176,208\n"
"s_mov_b32 s39, 0x20000\ns_and_b32 s2, s74, 15\nMaaa 72,180,212\nTa 252,19,49152\nD1 1,16,46,2048\nMaaa 104,180,220\nTa 254,19,51200\nMaaa 104,176,216\ns_max_u32 s2, s2, 2\n"
"Maaa 108,184,216\ns_sub_u32 s2, s2, 2\ns_mul_i32 s3, s73, 14\nMaaa 108,188,220\ns_add_u32 s2, s2, s3\nMaaa 76,188,212\nD1 1,16,46,3072\nMaaa 76,184,208\n"
"s_lshl2_add_u32 s2, s2, s11\nMaaa 44,184,200\ns_mul_i32 s3, s2, 4682\ns_lshr_b32 s3, s3, 16\nMaaa 44,188,204\ns_mul_i32 s22, s3, 14\nMaaa 12,188,196\ns_sub_u32 s22, s2, s22\n"
"Maaa 12,184,192\nWL 0\nMvaa 80,224,128\nTa 160,12,32768\ns_add_u32 m0, s47, -32768\ns_add_u32 s46, s56, 0x5000\nD1 1,16,46,0\nMvaa 80,228,132\nTa 162,12,34816\nMvaa 84,236,132\n"
"s_and_b32 s21, s3, 28\nMvaa 84,232,128\nTa 164,12,49152\nMvaa 88,240,128\nTa 166,12,51200\ns_add_u32 s21, s21, s3\nMvaa 88,244,132\nD1 1,16,46,1024\nMvaa 92,252,132\n"
"Ta 168,13,32768\nMvaa 92,248,128\nTa 170,13,34816\nMvaa 124,248,136\ns_lshl_b32 s21, s21, 4\ns_add_u32 s21, s21, 1024\nMvaa 124,252,140\nTa 172,13,49152\nMvaa 120,244,140\n"
"Ta 174,13,51200\nD1 1,16,46,2048\nMvaa 120,240,136\ns_xor_b32 s3, s3, 28\nMvaa 116,232,136\nTa 176,14,32768\ns_bfe_u32 s41, s3, 0x30002\nMvaa 116,236,140\nTa 178,14,34816\n"
"s_cmp_gt_u32 s22, 6\ns_cselect_b32 s2, 1179648, 0\ns_cselect_b32 s1, 16, 0\nMvaa 112,228,140\ns_add_u32 s41, s41, s1\nCV 40,80,81\nMvaa 112,224,136\nTa 180,14,49152\n"
"D1 1,16,46,3072\nMvaa 144,224,144\nTa 182,14,51200\ns_lshl4_add_u32 s41, s41, 0xc048400\nMvaa 144,228,148\ns_and_b32 s1, s3, 28\nMvaa 148,236,148\nTa 184,15,32768\n"
"s_add_u32 m0, s47, -65536\ns_add_u32 s46, s45, 0x10000\nMvaa 148,232,144\nTa 186,15,34816\nCV 41,82,83\nMvaa 152,240,144\nD1 1,16,46,0\ns_add_u32 s3, s3, s1\nMvaa 152,244,148\n"
"Ta 188,15,49152\nCV 42,84,85\nMvaa 156,252,148\nTa 190,15,51200\nMvaa 156,248,144\ns_lshl_b32 s3, s3, 11\nCV 43,86,87\nMvaa 188,248,152\ns_add_u32 s2, s2, s3\n"
"s_lshl_b32 s3, s22, 17\nMvaa 188,252,156\nD1 1,16,46,1024\nMvaa 184,244,156\ns_add_u32 s2, s2, s3\nCV 44,88,89\nMvaa 184,240,152\ns_add_u32 s20, s2, 0xce0000\nMvaa 180,232,152\n"
"s_lshl_b32 s2, s22, 7\ns_add_u32 s40, s2, 0xc04b800\nMvaa 180,236,156\ns_mul_i32 s2, s21, 896\nMvaa 176,228,156\nD1 1,16,46,2048\nCV 45,90,91\nMvaa 176,224,152\nWL 0\ns_barrier\n"
"Mvaa 64,160,128\nRa 192,10,49152\ns_lshl_b32 s3, s22, 6\nMvaa 64,164,132\ns_add_u32 s22, s2, s3\nMvaa 68,172,132\nRa 196,11,49152\nMvaa 68,168,128\nD1 1,16,46,3072\nCV 46,92,93\n"
"Mvaa 72,176,128\nRa 200,10,51200\ns_add_u32 s23, s22, 57344\nMvaa 72,180,132\nCV 47,94,95\nCV 60,120,121\nMvaa 76,188,132\nRa 204,11,51200\ns_add_u32 m0, s47, 32768\n"
"s_add_u32 s46, s45, 0x8000\nMvaa 76,184,128\nCV 61,122,123\nCV 62,124,125\nMvaa 108,184,136\nRa 208,10,57344\nD1 1,16,46,0\nMvaa 108,188,140\nCV 63,126,127\nMvaa 104,180,140\n"
"Ra 212,11,57344\nCV 56,112,113\nMvaa 104,176,136\nCV 57,114,115\nMvaa 100,168,136\nRa 216,10,59392\nCV 58,116,117\nMvaa 100,172,140\nD1 1,16,46,1024\nMvaa 96,164,140\n"
"Ra 220,11,59392\nCV 59,118,119\nMvaa 96,160,136\nCV 32,64,65\nMvaa 128,160,144\nCV 33,66,67\nCV 34,68,69\nMvaa 128,164,148\nCV 35,70,71\nMvaa 132,172,148\nD1 1,16,46,2048\n"
"Mvaa 132,168,144\nCV 36,72,73\nCV 37,74,75\nMvaa 136,176,144\nCV 38,76,77\nMvaa 136,180,148\nCV 39,78,79\nCV 52,104,105\nMvaa 140,188,148\nCV 53,106,107\nMvaa 140,184,144\n"
"D1 1,16,46,3072\nCV 54,108,109\nMvaa 172,184,152\nCV 55,110,111\nMvaa 172,188,156\nCV 48,96,97\nMvaa 168,180,156\ns_add_u32 m0, s47, 49152\ns_add_u32 s46, s45, 0x400000\n"
"Mvaa 168,176,152\nCV 49,98,99\nMvaa 164,168,152\nD1 1,16,46,0\nCV 50,100,101\nMvaa 164,172,156\nCV 51,102,103\nCV 104,144,145\nCV 105,146,147\nMvaa 160,164,156\nCV 106,148,149\n"
"CV 107,150,151\nMvaa 160,160,152\nWL 0\nMaaa 0,160,192\nSv 32,1,16,75,0\nCV 108,152,153\nCV 109,154,155\nMaaa 0,164,196\nD1 1,16,46,1024\nCV 110,156,157\nMaaa 4,172,196\n"
"CV 111,158,159\nCV 124,184,185\nMaaa 4,168,192\nCV 125,186,187\nCV 126,188,189\nMaaa 8,176,192\nCV 127,190,191\nCV 120,176,177\nCV 121,178,179\nMaaa 8,180,196\nCV 122,180,181\n"
"CV 123,182,183\nMaaa 12,188,196\nD1 1,16,46,2048\nCV 96,128,129\nMaaa 12,184,192\nCV 97,130,131\nCV 98,132,133\nCV 99,134,135\nMaaa 44,184,200\nSv 36,1,16,75,1024\nCV 100,136,137\n"
"Maaa 44,188,204\nCV 101,138,139\nCV 102,140,141\nMaaa 40,180,204\nCV 103,142,143\nCV 116,168,169\nCV 117,170,171\nMaaa 40,176,200\nD1 1,16,46,3072\n.W540:\ns_cbranch_execz .W544\n"
"CV 118,172,173\nMaaa 36,168,200\nCV 119,174,175\nCV 112,160,161\nMaaa 36,172,204\nCV 113,162,163\nCV 114,164,165\nCV 115,166,167\nMaaa 32,164,204\ns_add_u32 m0, s47, -49152\n"
"s_add_u32 s46, s45, 0x408000\nMaaa 32,160,200\nv_add_u32 v2, 0x10000, v1\ns_lshr_b32 s2, s59, 1\nMaaa 64,160,208\nD1 1,16,46,0\nSv 40,1,16,75,2048\nv_add_u32 v24, s2, v2\n"
"Maaa 64,164,212\nv_xor_b32 v26, 0x1000, v24\nv_lshrrev_b32 v20, 4, v0\nMaaa 68,172,212\nv_lshlrev_b32 v20, 2, v20\nv_and_b32 v0, 15, v0\nMaaa 68,168,208\nv_sub_u32 v0, v0, v20\n"
"s_lshl_b32 s2, s11, 4\nMaaa 72,176,208\nv_add_u32 v3, s2, v0\nv_subrev_u32 v0, 32, v3\ns_lshl_b32 s2, s11, 12\nMaaa 72,180,212\nD1 1,16,46,1024\nv_add_u32 v22, s2, v1\n"
"Maaa 76,188,212\nv_add_u32 v23, 0x20000, v22\nv_add_u32 v22, 0x8000, v22\nMaaa 76,184,208\nv_mov_b32 v4, 0\nv_mov_b32 v5, 0\nv_mov_b32 v6, 0\nMaaa 108,184,216\nSv 44,1,16,75,3072\n"
"v_mov_b32 v7, 0\nMaaa 108,188,220\nv_mov_b32 v8, 0\nv_mov_b32 v9, 0\nMaaa 104,180,220\nD1 1,16,46,2048\nv_mov_b32 v10, 0\nv_mov_b32 v11, 0\nMaaa 104,176,216\nv_mov_b32 v12, 0\n"
"v_mov_b32 v13, 0\nMaaa 100,168,216\nv_mov_b32 v14, 0\nv_mov_b32 v15, 0\nMaaa 100,172,220\nv_mov_b32 v16, 0\nv_mov_b32 v17, 0\nv_mov_b32 v18, 0\nMaaa 96,164,220\nv_mov_b32 v19, 0\n"
"v_mov_b32 v86, 0\nMaaa 96,160,216\nWV 19\ns_barrier\nMaaa 16,224,192\nD1 1,16,46,3072\ns_add_u32 s75, s75, s55\nSv 48,1,16,75,0\nMaaa 16,228,196\nv_mov_b32 v87, 0\n"
"v_mov_b32 v88, 0\nMaaa 20,236,196\nv_mov_b32 v89, 0\nv_mov_b32 v95, -1\nMaaa 20,232,192\nMaaa 24,240,192\nRa 128,24,0\nMaaa 24,244,196\nMaaa 28,252,196\nRa 196,23,1024\n"
"Ra 132,24,1024\nMaaa 28,248,192\nRa 192,23,0\nMaaa 60,248,200\nSv 52,1,16,75,1024\nRa 136,24,2048\nMaaa 60,252,204\nMaaa 56,244,204\nRa 140,24,3072\nMaaa 56,240,200\n"
"Maaa 52,232,200\nRa 144,26,0\nMaaa 52,236,204\nMaaa 48,228,204\nRa 204,23,3072\nRa 148,26,1024\nMaaa 48,224,200\nRa 200,23,2048\nMaaa 80,224,208\nSv 56,1,16,75,2048\n"
"Ra 152,26,2048\nMaaa 80,228,212\nMaaa 84,236,212\nRa 156,26,3072\nMaaa 84,232,208\nMaaa 88,240,208\nMaaa 88,244,212\nMaaa 92,252,212\nRa 212,23,17408\nMaaa 92,248,208\n"
"Ra 208,23,16384\nMaaa 124,248,216\nRa 248,22,2048\nSv 60,1,16,75,3072\nMaaa 124,252,220\nRa 252,22,3072\nMaaa 120,244,220\nRa 244,22,1024\nMaaa 120,240,216\nRa 240,22,0\n"
"Maaa 116,232,216\nRa 232,22,51200\nMaaa 116,236,220\nRa 236,22,52224\nMaaa 112,228,220\nRa 228,22,50176\nRa 220,23,19456\nMaaa 112,224,216\nRa 224,22,49152\nRa 216,23,18432\nWL 0\n"
"Cvaa 32,128,192,4\nRa 160,24,8192\nMvaa 32,132,200\nMvaa 32,136,208\nRa 164,24,9216\nMvaa 32,140,216\nCvaa 36,140,220,8\nRa 168,24,10240\nMvaa 36,136,212\n"
"s_add_u32 m0, s89, 32768\nMvaa 36,132,204\nRa 172,24,11264\ns_barrier\nMvaa 36,128,196\nMvaa 36,144,228\nRa 176,26,8192\nSv 96,1,16,76,0\nMvaa 36,148,236\ns_add_u32 s46, s86, s88\n"
"Mvaa 36,152,244\nRa 180,26,9216\nMvaa 36,156,252\nAR 92,0\nMvaa 32,156,248\nRa 184,26,10240\nAR 93,1\nMvaa 32,152,240\nD1 1,16,46,0\nMvaa 32,148,232\nRa 188,26,11264\nWL 4\n"
"Mvaa 32,144,224\nCV 96,92,93\nCvaa 40,160,192,12\nAR 92,2\nAR 93,3\nMvaa 40,164,200\nCV 97,92,93\nMvaa 40,168,208\nv_and_b32 v27, 0x70, v1\n"
"buffer_load_dword v27, v27, s[16:19], s61 offen offset:136 sc0 sc1\nMvaa 40,172,216\nAR 92,4\nCvaa 44,172,220,16\nAR 93,5\nCV 98,92,93\nMvaa 44,168,212\nD1 1,16,46,1024\n"
"Mvaa 44,164,204\nWL 0\nMvaa 44,160,196\nAR 92,6\nMvaa 44,176,228\nSv 100,1,16,76,1024\nAR 93,7\nMvaa 44,180,236\ns_add_u32 s88, s88, 32768\nEX 80,32\nMvaa 44,184,244\nCV 99,92,93\n"
"AR 92,8\nMvaa 44,188,252\nEX 81,33\nMvaa 40,188,248\nAR 93,9\nCV 100,92,93\nMvaa 40,184,240\nD1 1,16,46,2048\nMvaa 40,180,232\nAR 92,10\nAR 93,11\nMvaa 40,176,224\nAD 84,80,81\n"
"CV 64,80,81\nCV 101,92,93\nAR 92,12\nEX 82,34\nAR 93,13\nCV 102,92,93\nWV 16\nEX 83,35\nAD 84,84,82\nAR 92,14\nD1 1,16,46,3072\ns_add_u32 m0, s89, 49152\ns_barrier\nAR 93,15\n"
"AD 84,84,83\nCV 65,82,83\nRa 128,24,32768\nSv 104,1,16,76,2048\ns_add_u32 s46, s87, s88\nEX 80,36\nRa 132,24,33792\nCV 103,92,93\nEX 81,37\nRa 136,24,34816\nAR 92,16\nAR 93,17\n"
"D1 1,16,46,0\nCV 104,92,93\nRa 140,24,35840\nAR 92,18\nAD 85,80,81\nCV 68,80,81\nRa 144,26,32768\nAR 93,19\nCV 105,92,93\nEX 82,38\nRa 148,26,33792\nAR 92,20\nEX 83,39\n"
"Ra 152,26,34816\nAD 85,85,82\nAR 93,21\nD1 1,16,46,1024\nCV 106,92,93\nRa 156,26,35840\nAR 92,22\nAD 85,85,83\nCV 69,82,83\nRa 160,24,40960\nSv 108,1,16,76,3072\nAR 93,23\n"
"s_xor_b32 s89, s89, 0x10000\nEX 80,40\nRa 164,24,41984\nCV 107,92,93\nAR 92,24\nWL 6\nEX 81,41\nRa 168,24,43008\nAD 84,84,80\nAR 93,25\nD1 1,16,46,2048\nCV 108,92,93\n"
"Ra 172,24,44032\nAR 92,26\nAR 93,27\nAD 84,84,81\nCV 66,80,81\nCvaa 48,128,192,4\nRa 176,26,40960\nCV 109,92,93\nAR 92,28\nMvaa 48,132,200\nEX 82,42\nMvaa 48,136,208\n"
"Ra 180,26,41984\nAR 93,29\nMvaa 48,140,216\nWL 6\nEX 83,43\nWV 9\nv_cmp_ne_u32 vcc, s15, v27\ns_nop 0\ns_cbranch_vccnz .S41\n.S42:\nCvaa 52,140,220,8\nRa 184,26,43008\n"
"AD 84,84,82\nMvaa 52,136,212\nD1 1,16,46,3072\ns_add_u32 m0, s89, 0\nCV 110,92,93\nMvaa 52,132,204\nRa 188,26,44032\nAR 92,30\nMvaa 52,128,196\nAD 84,84,83\nCV 67,82,83\nAR 93,31\n"
"Mvaa 52,144,228\nRa 128,2,49152\ns_add_u32 s76, s76, s55\nSv 112,1,16,76,0\nMvaa 52,148,236\ns_add_u32 s46, s86, s88\nEX 80,44\nMvaa 52,152,244\nRa 132,2,50176\n"
"v_xor_b32 v24, 0x10000, v24\nCV 111,92,93\nMvaa 52,156,252\nWL 6\nEX 81,45\nMvaa 48,156,248\nRa 136,2,51200\nAD 85,85,80\nAR 92,32\nMvaa 48,152,240\nD1 1,16,46,0\nAR 93,33\n"
"CV 112,92,93\nMvaa 48,148,232\nRa 140,2,52224\nAR 92,34\nMvaa 48,144,224\nAD 85,85,81\nCV 70,80,81\nAR 93,35\nCvaa 56,160,192,12\nRa 144,2,53248\nCV 113,92,93\nAR 92,36\n"
"Mvaa 56,164,200\nEX 82,46\nMvaa 56,168,208\nRa 148,2,54272\nv_xor_b32 v26, 0x10000, v26\nAR 93,37\nMvaa 56,172,216\nWL 6\nEX 83,47\nCvaa 60,172,220,16\nRa 152,2,55296\n"
"AD 85,85,82\nCV 114,92,93\nMvaa 60,168,212\nD1 1,16,46,1024\nAR 92,38\nAR 93,39\nMvaa 60,164,204\nRa 156,2,56320\nCV 115,92,93\nMvaa 60,160,196\nAD 85,85,83\nCV 71,82,83\n"
"Mvaa 60,176,228\nRa 160,2,57344\nv_max_f32 v90, v84, v85\nSv 116,1,16,76,1024\nMvaa 60,180,236\ns_add_u32 s88, s88, 32768\nEX 80,48\nMvaa 60,184,244\nRa 164,2,58368\n"
"v_cmp_lt_f32 vcc, s63, v90\nMvaa 60,188,252\nWL 8\nEX 81,49\nMvaa 56,188,248\nRa 168,2,59392\ns_cbranch_vccnz .T23\n.T24:\nv_mov_b32 v20, v84\nv_mov_b32 v21, v85\n"
"v_mov_b32 v22, v20\nv_mov_b32 v23, v21\nMvaa 56,184,240\nD1 1,16,46,2048\nv_permlane32_swap_b32 v20, v22\nv_permlane32_swap_b32 v21, v23\nAD 20,20,22\nAD 21,21,23\n"
"v_mov_b32 v22, v20\nv_mov_b32 v23, v21\nMvaa 56,180,232\nRa 172,2,60416\nv_permlane16_swap_b32 v20, v22\nv_permlane16_swap_b32 v21, v23\nAD 20,20,22\nAD 21,21,23\n"
"v_min_f32 v90, v20, v21\nv_cmp_gt_f32 vcc, s64, v90\ns_nop 0\ns_cbranch_vccnz .T25\n.T26:\nAD 86,86,84\nMvaa 56,176,224\nAD 84,80,81\nCV 72,80,81\nAR 92,40\nZvav 128,128,64\n"
"Ra 176,2,61440\nAR 93,41\nCV 116,92,93\nZvav 132,128,68\nEX 82,50\nAR 92,42\nZvav 140,132,68\nRa 180,2,62464\nAR 93,43\nCV 117,92,93\nZvav 136,132,64\nWL 8\nEX 83,51\n"
"Zvav 144,136,64\nRa 184,2,63488\nAD 84,84,82\nAR 92,44\nZvav 148,136,68\nD1 1,16,46,3072\ns_add_u32 m0, s89, 16384\nAR 93,45\nZvav 156,140,68\nRa 188,2,64512\ns_barrier\n"
"CV 118,92,93\nZvav 152,140,64\nAD 84,84,83\nCV 73,82,83\nAR 92,46\nZvav 160,144,64\nRa 128,24,0\nSv 120,1,16,76,2048\nAR 93,47\nZvav 164,144,68\ns_add_u32 s46, s87, s88\nEX 80,52\n"
"buffer_store_dword v25, off, s[16:19], s54 offset:12 sc0 sc1\nZvav 172,148,68\nRa 132,24,1024\nv_xor_b32 v2, 0x10000, v2\nCV 119,92,93\nZvav 168,148,64\nWL 8\nEX 81,53\n"
"Zvav 176,152,64\nRa 136,24,2048\nAD 87,87,85\nZvav 180,152,68\nD1 1,16,46,0\nAR 92,48\nAR 93,49\nZvav 188,156,68\nRa 140,24,3072\nCV 120,92,93\nZvav 184,156,64\nAD 85,80,81\n"
"CV 76,80,81\nZvav 192,160,64\nRa 144,26,0\nAR 92,50\nAR 93,51\nZvav 196,160,68\nEX 82,54\nZvav 204,164,68\nRa 148,26,1024\nCV 121,92,93\nZvav 200,164,64\nWL 8\nEX 83,55\n"
"Zvav 208,168,64\nRa 152,26,2048\nAD 85,85,82\nAR 92,52\nZvav 212,168,68\nD1 1,16,46,1024\nAR 93,53\nZvav 220,172,68\nRa 156,26,3072\nCV 122,92,93\nZvav 216,172,64\nAD 85,85,83\n"
"CV 77,82,83\nAR 92,54\nZvav 224,176,64\nRa 160,24,8192\nSv 124,1,16,76,3072\nZvav 228,176,68\nEX 80,56\nZvav 236,180,68\nRa 164,24,9216\nAR 93,55\nCV 123,92,93\nZvav 232,180,64\n"
"WL 6\nEX 81,57\nZvav 240,184,64\nRa 168,24,10240\nAD 84,84,80\nAR 92,56\nZvav 244,184,68\nD1 1,16,46,2048\nAR 93,57\nZvav 252,188,68\nRa 172,24,11264\nCV 124,92,93\nAR 92,58\n"
"Zvav 248,188,64\nAD 84,84,81\nCV 74,80,81\nCvaa 32,128,192,4\nRa 176,26,8192\nAR 93,59\nCV 125,92,93\nMvaa 32,132,200\nEX 82,58\nMvaa 32,136,208\nRa 180,26,9216\nAR 92,60\n"
"Mvaa 32,140,216\nWL 6\nEX 83,59\nCvaa 36,140,220,8\nRa 184,26,10240\nAD 84,84,82\nMvaa 36,136,212\nD1 1,16,46,3072\ns_add_u32 m0, s89, 32768\nAR 93,61\nMvaa 36,132,204\n"
"Ra 188,26,11264\nCV 126,92,93\nMvaa 36,128,196\nAD 84,84,83\nCV 75,82,83\nAR 92,62\nMvaa 36,144,228\nRa 128,2,16384\nSv 96,1,16,77,0\nMvaa 36,148,236\ns_add_u32 s46, s86, s88\n"
"EX 80,60\nMvaa 36,152,244\nRa 132,2,17408\nAR 93,63\nCV 127,92,93\nMvaa 36,156,252\nWL 6\nEX 81,61\nMvaa 32,156,248\nRa 136,2,18432\nAD 85,85,80\nAR 92,64\nMvaa 32,152,240\n"
"D1 1,16,46,0\nAR 93,65\nMvaa 32,148,232\nRa 140,2,19456\nCV 96,92,93\nAR 92,66\nMvaa 32,144,224\nAD 85,85,81\nCV 78,80,81\nCvaa 40,160,192,12\nRa 144,2,20480\nAR 93,67\n"
"CV 97,92,93\nMvaa 40,164,200\nEX 82,62\nMvaa 40,168,208\nRa 148,2,21504\nAR 92,68\nAR 93,69\nMvaa 40,172,216\nWL 6\nEX 83,63\nCvaa 44,172,220,16\nRa 152,2,22528\nAD 85,85,82\n"
"CV 98,92,93\nMvaa 44,168,212\n.W541:\ns_cbranch_execz .W545\nD1 1,16,46,1024\nAR 92,70\nMvaa 44,164,204\nRa 156,2,23552\nAR 93,71\nCV 99,92,93\nMvaa 44,160,196\nAD 85,85,83\n"
"CV 79,82,83\nMvaa 44,176,228\nRa 160,2,24576\nv_max_f32 v90, v84, v85\nSv 100,1,16,77,1024\nMvaa 44,180,236\ns_add_u32 s88, s88, 32768\nEX 80,32\nMvaa 44,184,244\nRa 164,2,25600\n"
"v_cmp_lt_f32 vcc, s63, v90\nMvaa 44,188,252\nWL 8\nEX 81,33\nMvaa 40,188,248\nRa 168,2,26624\ns_cbranch_vccnz .T28\n.T29:\nAR 92,72\nMvaa 40,184,240\nD1 1,16,46,2048\nAR 93,73\n"
"Mvaa 40,180,232\nRa 172,2,27648\nAD 86,86,84\nCV 100,92,93\nMvaa 40,176,224\nAD 84,80,81\nCV 64,80,81\nMvav 128,128,72\nRa 176,2,28672\nAR 92,74\nAR 93,75\nMvav 132,128,76\n"
"EX 82,34\nMvav 140,132,76\nRa 180,2,29696\nCV 101,92,93\nAR 92,76\nMvav 136,132,72\ns_waitcnt vmcnt(18) lgkmcnt(8)\nEX 83,35\nMvav 144,136,72\nRa 184,2,30720\nAD 84,84,82\n"
"AR 93,77\nMvav 148,136,76\nD1 1,16,46,3072\ns_add_u32 m0, s89, 49152\nMvav 156,140,76\nRa 188,2,31744\ns_barrier\nCV 102,92,93\nMvav 152,140,72\nAD 84,84,83\nCV 65,82,83\n"
"Mvav 160,144,72\nRa 128,24,32768\nSv 104,1,16,77,2048\nAR 92,78\nMvav 164,144,76\ns_add_u32 s46, s87, s88\nEX 80,36\nMvav 172,148,76\nRa 132,24,33792\nAR 93,79\nCV 103,92,93\n"
"Mvav 168,148,72\nWL 8\nEX 81,37\nMvav 176,152,72\nRa 136,24,34816\nAD 87,87,85\nMvav 180,152,76\nD1 1,16,46,0\nAR 92,80\nAR 93,81\nMvav 188,156,76\nRa 140,24,35840\nCV 104,92,93\n"
"Mvav 184,156,72\nAD 85,80,81\nCV 68,80,81\nMvav 192,160,72\nRa 144,26,32768\nAR 92,82\nAR 93,83\nMvav 196,160,76\nEX 82,38\nMvav 204,164,76\nRa 148,26,33792\nCV 105,92,93\n"
"Mvav 200,164,72\nWL 8\nEX 83,39\nMvav 208,168,72\nRa 152,26,34816\nAD 85,85,82\nAR 92,84\nMvav 212,168,76\nD1 1,16,46,1024\nAR 93,85\nMvav 220,172,76\nRa 156,26,35840\n"
"CV 106,92,93\nMvav 216,172,72\nAD 85,85,83\nCV 69,82,83\nAR 92,86\nMvav 224,176,72\nRa 160,24,40960\nSv 108,1,16,77,3072\nMvav 228,176,76\ns_xor_b32 s89, s89, 0x10000\nEX 80,40\n"
"Mvav 236,180,76\nRa 164,24,41984\nAR 93,87\nCV 107,92,93\nMvav 232,180,72\nWL 6\nEX 81,41\nMvav 240,184,72\nRa 168,24,43008\nAD 84,84,80\nAR 92,88\nMvav 244,184,76\n"
"D1 1,16,46,2048\nAR 93,89\nCV 108,92,93\nMvav 252,188,76\nRa 172,24,44032\nAR 92,90\nMvav 248,188,72\nAD 84,84,81\nCV 66,80,81\nAR 93,91\nCvaa 48,128,192,4\nRa 176,26,40960\n"
"CV 109,92,93\nAR 92,92\nMvaa 48,132,200\nEX 82,42\nMvaa 48,136,208\nRa 180,26,41984\nAR 93,93\nCV 110,92,93\nMvaa 48,140,216\nWL 6\nEX 83,43\nCvaa 52,140,220,8\nRa 184,26,43008\n"
"AD 84,84,82\nv_and_b32 v27, 0x70, v1\nbuffer_load_dword v27, v27, s[16:19], s61 offen offset:140 sc0 sc1\nMvaa 52,136,212\nD1 1,16,46,3072\ns_add_u32 m0, s89, 0\nAR 92,94\n"
"Mvaa 52,132,204\nRa 188,26,44032\nAR 93,95\nMvaa 52,128,196\nAD 84,84,83\nCV 67,82,83\nCV 111,92,93\nMvaa 52,144,228\nRa 128,2,49152\ns_add_u32 s77, s77, s55\nSv 112,1,16,77,0\n"
"Mvaa 52,148,236\ns_add_u32 s46, s86, s88\nEX 80,44\nMvaa 52,152,244\nRa 132,2,50176\nv_xor_b32 v24, 0x10000, v24\nAR 92,96\nMvaa 52,156,252\nWL 6\nEX 81,45\nMvaa 48,156,248\n"
"Ra 136,2,51200\nAD 85,85,80\nAR 93,97\nMvaa 48,152,240\nD1 1,16,46,0\nCV 112,92,93\nMvaa 48,148,232\nRa 140,2,52224\nAR 92,98\nAR 93,99\nMvaa 48,144,224\nAD 85,85,81\nCV 70,80,81\n"
"Cvaa 56,160,192,12\nRa 144,2,53248\nCV 113,92,93\nAR 92,100\nMvaa 56,164,200\nEX 82,46\nMvaa 56,168,208\nRa 148,2,54272\nv_xor_b32 v26, 0x10000, v26\nAR 93,101\nMvaa 56,172,216\n"
"WL 6\nEX 83,47\nCvaa 60,172,220,16\nRa 152,2,55296\nAD 85,85,82\nCV 114,92,93\nMvaa 60,168,212\nD1 1,16,46,1024\nAR 92,102\nMvaa 60,164,204\nRa 156,2,56320\nAR 93,103\n"
"CV 115,92,93\nMvaa 60,160,196\nAD 85,85,83\nCV 71,82,83\nMvaa 60,176,228\nRa 160,2,57344\nv_max_f32 v90, v84, v85\nSv 116,1,16,77,1024\nMvaa 60,180,236\ns_add_u32 s88, s88, 32768\n"
"EX 80,48\nMvaa 60,184,244\nRa 164,2,58368\nv_cmp_lt_f32 vcc, s63, v90\nMvaa 60,188,252\nWL 8\nEX 81,49\nMvaa 56,188,248\nRa 168,2,59392\ns_cbranch_vccnz .T30\n.T31:\nAR 92,104\n"
"Mvaa 56,184,240\nD1 1,16,46,2048\nAR 93,105\nCV 116,92,93\nMvaa 56,180,232\nRa 172,2,60416\nAD 86,86,84\nMvaa 56,176,224\nAD 84,80,81\nCV 72,80,81\nAR 92,106\nMvav 128,128,64\n"
"Ra 176,2,61440\nAR 93,107\nCV 117,92,93\nMvav 132,128,68\nEX 82,50\nMvav 140,132,68\nRa 180,2,62464\nAR 92,108\nAR 93,109\nMvav 136,132,64\ns_waitcnt vmcnt(18) lgkmcnt(8)\n"
"EX 83,51\nMvav 144,136,64\nRa 184,2,63488\nAD 84,84,82\nCV 118,92,93\nMvav 148,136,68\nD1 1,16,46,3072\ns_add_u32 m0, s89, 16384\nAR 92,110\nMvav 156,140,68\nRa 188,2,64512\n"
"s_barrier\nMvav 152,140,64\nAD 84,84,83\nCV 73,82,83\nAR 93,111\nMvav 160,144,64\nRa 128,24,0\nSv 120,1,16,77,2048\nCV 119,92,93\nMvav 164,144,68\ns_add_u32 s46, s87, s88\n"
"EX 80,52\nMvav 172,148,68\nRa 132,24,1024\nv_xor_b32 v2, 0x10000, v2\nAR 92,112\nMvav 168,148,64\nWL 8\nEX 81,53\nMvav 176,152,64\nRa 136,24,2048\nAD 87,87,85\n"
"buffer_store_dword v25, off, s[16:19], s54 offset:8 sc0 sc1\nMvav 180,152,68\nD1 1,16,46,0\nAR 93,113\nCV 120,92,93\nMvav 188,156,68\nRa 140,24,3072\nAR 92,114\nMvav 184,156,64\n"
"AD 85,80,81\nCV 76,80,81\nMvav 192,160,64\nRa 144,26,0\nAR 93,115\nCV 121,92,93\nMvav 196,160,68\nEX 82,54\nMvav 204,164,68\nRa 148,26,1024\nAR 92,116\nMvav 200,164,64\nWL 8\n"
"EX 83,55\nMvav 208,168,64\nRa 152,26,2048\nAD 85,85,82\nAR 93,117\nMvav 212,168,68\nD1 1,16,46,1024\nCV 122,92,93\nMvav 220,172,68\nRa 156,26,3072\nAR 92,118\nMvav 216,172,64\n"
"AD 85,85,83\nCV 77,82,83\nAR 93,119\nMvav 224,176,64\nRa 160,24,8192\nSv 124,1,16,77,3072\nMvav 228,176,68\nEX 80,56\nMvav 236,180,68\nRa 164,24,9216\nCV 123,92,93\nAR 92,120\n"
"Mvav 232,180,64\nWL 6\nEX 81,57\nMvav 240,184,64\nRa 168,24,10240\nAD 84,84,80\nMvav 244,184,68\nD1 1,16,46,2048\nAR 93,121\nCV 124,92,93\nMvav 252,188,68\nRa 172,24,11264\n"
"AR 92,122\nMvav 248,188,64\nAD 84,84,81\nCV 74,80,81\nCvaa 32,128,192,4\nRa 176,26,8192\nAR 93,123\nCV 125,92,93\nMvaa 32,132,200\ns_add_u32 s49, s49, 131072\nEX 82,58\n"
"Mvaa 32,136,208\nRa 180,26,9216\nAR 92,124\nMvaa 32,140,216\nWL 6\nEX 83,59\nCvaa 36,140,220,8\nRa 184,26,10240\nAD 84,84,82\nAR 93,125\nMvaa 36,136,212\nD1 1,16,46,3072\n"
"s_add_u32 m0, s89, 32768\nMvaa 36,132,204\nRa 188,26,11264\nCV 126,92,93\nMvaa 36,128,196\nAD 84,84,83\nCV 75,82,83\nAR 92,126\nMvaa 36,144,228\nRa 128,2,16384\nSv 96,1,16,78,0\n"
"Mvaa 36,148,236\ns_add_u32 s46, s86, s88\nEX 80,60\nMvaa 36,152,244\nRa 132,2,17408\nAR 93,127\nCV 127,92,93\nMvaa 36,156,252\nWL 6\nEX 81,61\nMvaa 32,156,248\nRa 136,2,18432\n"
"AD 85,85,80\nMvaa 32,152,240\nD1 1,16,46,0\nMvaa 32,148,232\nRa 140,2,19456\nMvaa 32,144,224\nAD 85,85,81\nCV 78,80,81\nCvaa 40,160,192,12\nRa 144,2,20480\nMvaa 40,164,200\n"
"Sv 92,1,16,49,0\nEX 82,62\nMvaa 40,168,208\nRa 148,2,21504\nMvaa 40,172,216\nWL 6\nEX 83,63\nCvaa 44,172,220,16\nRa 152,2,22528\nAD 85,85,82\nMvaa 44,168,212\nD1 1,16,46,1024\n"
"Mvaa 44,164,204\nRa 156,2,23552\nMvaa 44,160,196\nAD 85,85,83\nCV 79,82,83\nMvaa 44,176,228\nRa 160,2,24576\nv_max_f32 v90, v84, v85\nSv 100,1,16,78,1024\nMvaa 44,180,236\n"
"s_add_u32 s88, s88, 32768\nEX 80,32\nMvaa 44,184,244\nRa 164,2,25600\nv_cmp_lt_f32 vcc, s63, v90\nMvaa 44,188,252\nWL 8\nEX 81,33\nMvaa 40,188,248\nRa 168,2,26624\n"
"s_cbranch_vccnz .T33\n.T34:\nMvaa 40,184,240\nD1 1,16,46,2048\nMvaa 40,180,232\nRa 172,2,27648\nAD 86,86,84\nMvaa 40,176,224\nAD 84,80,81\nCV 64,80,81\nMvav 128,128,72\n"
"Ra 176,2,28672\nMvav 132,128,76\nEX 82,34\nMvav 140,132,76\nRa 180,2,29696\nMvav 136,132,72\ns_waitcnt vmcnt(19) lgkmcnt(8)\nEX 83,35\nMvav 144,136,72\nRa 184,2,30720\n"
"AD 84,84,82\nMvav 148,136,76\nD1 1,16,46,3072\ns_add_u32 m0, s89, 49152\nMvav 156,140,76\nRa 188,2,31744\ns_barrier\nMvav 152,140,72\nAD 84,84,83\nCV 65,82,83\nMvav 160,144,72\n"
"Ra 128,24,32768\nSv 104,1,16,78,2048\nMvav 164,144,76\ns_add_u32 s46, s87, s88\nEX 80,36\nMvav 172,148,76\nRa 132,24,33792\nMvav 168,148,72\nWL 8\nEX 81,37\nMvav 176,152,72\n"
"Ra 136,24,34816\nAD 87,87,85\nMvav 180,152,76\nD1 1,16,46,0\nMvav 188,156,76\nRa 140,24,35840\nMvav 184,156,72\nAD 85,80,81\nCV 68,80,81\nMvav 192,160,72\nRa 144,26,32768\n"
"Mvav 196,160,76\nEX 82,38\nMvav 204,164,76\nRa 148,26,33792\nMvav 200,164,72\nWL 8\nEX 83,39\nMvav 208,168,72\nRa 152,26,34816\nAD 85,85,82\nMvav 212,168,76\nD1 1,16,46,1024\n"
"Mvav 220,172,76\nRa 156,26,35840\nMvav 216,172,72\nAD 85,85,83\nCV 69,82,83\nMvav 224,176,72\nRa 160,24,40960\nSv 108,1,16,78,3072\nMvav 228,176,76\ns_xor_b32 s89, s89, 0x10000\n"
"EX 80,40\nMvav 236,180,76\nRa 164,24,41984\nMvav 232,180,72\nWL 6\nEX 81,41\nMvav 240,184,72\nRa 168,24,43008\nAD 84,84,80\nMvav 244,184,76\nD1 1,16,46,2048\nMvav 252,188,76\n"
"Ra 172,24,44032\nMvav 248,188,72\nAD 84,84,81\nCV 66,80,81\nCvaa 48,128,192,4\nRa 176,26,40960\nMvaa 48,132,200\nEX 82,42\nMvaa 48,136,208\nRa 180,26,41984\nMvaa 48,140,216\nWL 6\n"
"EX 83,43\nv_cmp_ne_u32 vcc, s15, v27\ns_nop 0\ns_cbranch_vccnz .S44\n.S45:\nv_and_b32 v27, 0x70, v1\nbuffer_load_dword v27, v27, s[16:19], s61 offen offset:12 sc0 sc1\n"
"Cvaa 52,140,220,8\nRa 184,26,43008\nAD 84,84,82\nMvaa 52,136,212\nD1 1,16,46,3072\ns_add_u32 m0, s89, 0\nMvaa 52,132,204\nRa 188,26,44032\nMvaa 52,128,196\nAD 84,84,83\n"
"CV 67,82,83\nMvaa 52,144,228\nRa 128,2,49152\ns_add_u32 s78, s78, s55\nSv 112,1,16,78,0\nMvaa 52,148,236\ns_add_u32 s46, s86, s88\nEX 80,44\nMvaa 52,152,244\nRa 132,2,50176\n"
"v_xor_b32 v24, 0x10000, v24\nMvaa 52,156,252\nWL 6\nEX 81,45\nMvaa 48,156,248\nRa 136,2,51200\nAD 85,85,80\nMvaa 48,152,240\nD1 1,16,46,0\n.W542:\ns_cbranch_execz .W546\n"
"Mvaa 48,148,232\nRa 140,2,52224\nMvaa 48,144,224\nAD 85,85,81\nCV 70,80,81\nCvaa 56,160,192,12\nRa 144,2,53248\nMvaa 56,164,200\nSv 92,1,16,49,1024\nEX 82,46\nMvaa 56,168,208\n"
"Ra 148,2,54272\nv_xor_b32 v26, 0x10000, v26\nMvaa 56,172,216\nWL 6\nEX 83,47\nCvaa 60,172,220,16\nRa 152,2,55296\nAD 85,85,82\nMvaa 60,168,212\nD1 1,16,46,1024\nMvaa 60,164,204\n"
"Ra 156,2,56320\nMvaa 60,160,196\nAD 85,85,83\nCV 71,82,83\nMvaa 60,176,228\nRa 160,2,57344\nv_max_f32 v90, v84, v85\nSv 116,1,16,78,1024\nMvaa 60,180,236\n"
"s_add_u32 s88, s88, 32768\nEX 80,48\nMvaa 60,184,244\nRa 164,2,58368\nv_cmp_lt_f32 vcc, s63, v90\nMvaa 60,188,252\nWL 8\nEX 81,49\nMvaa 56,188,248\nRa 168,2,59392\n"
"s_cbranch_vccnz .T35\n.T36:\nMvaa 56,184,240\nD1 1,16,46,2048\nMvaa 56,180,232\nRa 172,2,60416\nAD 86,86,84\nMvaa 56,176,224\nAD 84,80,81\nCV 72,80,81\nMvav 128,128,64\n"
"Ra 176,2,61440\nMvav 132,128,68\nEX 82,50\nMvav 140,132,68\nRa 180,2,62464\nMvav 136,132,64\ns_waitcnt vmcnt(20) lgkmcnt(8)\nEX 83,51\nMvav 144,136,64\nRa 184,2,63488\n"
"AD 84,84,82\nMvav 148,136,68\nD1 1,16,46,3072\ns_add_u32 m0, s89, 16384\nMvav 156,140,68\nRa 188,2,64512\ns_barrier\nMvav 152,140,64\nAD 84,84,83\nCV 73,82,83\nMvav 160,144,64\n"
"Ra 128,24,0\nSv 120,1,16,78,2048\nMvav 164,144,68\ns_add_u32 s46, s87, s88\nEX 80,52\nMvav 172,148,68\nRa 132,24,1024\nv_xor_b32 v2, 0x10000, v2\nMvav 168,148,64\nWL 8\nEX 81,53\n"
"Mvav 176,152,64\nRa 136,24,2048\nAD 87,87,85\nMvav 180,152,68\nD1 1,16,46,0\nMvav 188,156,68\nRa 140,24,3072\nMvav 184,156,64\nAD 85,80,81\nCV 76,80,81\nMvav 192,160,64\n"
"Ra 144,26,0\nMvav 196,160,68\nEX 82,54\nMvav 204,164,68\nRa 148,26,1024\nMvav 200,164,64\nWL 8\nEX 83,55\nMvav 208,168,64\nRa 152,26,2048\nAD 85,85,82\nMvav 212,168,68\n"
"D1 1,16,46,1024\nMvav 220,172,68\nRa 156,26,3072\nMvav 216,172,64\nAD 85,85,83\nCV 77,82,83\nMvav 224,176,64\nRa 160,24,8192\nSv 124,1,16,78,3072\nMvav 228,176,68\nEX 80,56\n"
"Mvav 236,180,68\nRa 164,24,9216\nMvav 232,180,64\nWL 6\nEX 81,57\nMvav 240,184,64\nRa 168,24,10240\nAD 84,84,80\nMvav 244,184,68\nD1 1,16,46,2048\nMvav 252,188,68\n"
"Ra 172,24,11264\nMvav 248,188,64\nAD 84,84,81\nCV 74,80,81\ns_mov_b32 s99, 11\ns_mov_b32 s90, 0\n.p2align 6\n.A60:\nCvaa 32,128,192,4\nRa 176,26,8192\nMvaa 32,132,200\n"
"s_add_u32 s49, s49, 131072\nEX 82,58\nMvaa 32,136,208\nRa 180,26,9216\nMvaa 32,140,216\nWL 6\nEX 83,59\nCvaa 36,140,220,8\nRa 184,26,10240\nAD 84,84,82\nMvaa 36,136,212\n"
"D1 1,16,46,3072\ns_add_u32 m0, s89, 32768\nMvaa 36,132,204\nRa 188,26,11264\nMvaa 36,128,196\nAD 84,84,83\nCV 75,82,83\nMvaa 36,144,228\nRa 128,2,16384\nMvaa 36,148,236\n"
"s_add_u32 s46, s86, s88\nEX 80,60\nMvaa 36,152,244\nRa 132,2,17408\nMvaa 36,156,252\nWL 6\nEX 81,61\nMvaa 32,156,248\nRa 136,2,18432\nAD 85,85,80\nMvaa 32,152,240\nD1 1,16,46,0\n"
"Mvaa 32,148,232\nRa 140,2,19456\nMvaa 32,144,224\nAD 85,85,81\nCV 78,80,81\nCvaa 40,160,192,12\nRa 144,2,20480\nMvaa 40,164,200\nSv 92,1,16,49,0\nEX 82,62\nMvaa 40,168,208\n"
"Ra 148,2,21504\nMvaa 40,172,216\nWL 6\nEX 83,63\nCvaa 44,172,220,16\nRa 152,2,22528\nAD 85,85,82\nMvaa 44,168,212\nD1 1,16,46,1024\nMvaa 44,164,204\nRa 156,2,23552\n"
"Mvaa 44,160,196\nAD 85,85,83\nCV 79,82,83\nMvaa 44,176,228\nRa 160,2,24576\nv_max_f32 v90, v84, v85\nMvaa 44,180,236\ns_add_u32 s88, s88, 32768\nEX 80,32\nMvaa 44,184,244\n"
"Ra 164,2,25600\nv_cmp_lt_f32 vcc, s63, v90\nMvaa 44,188,252\nWL 8\nEX 81,33\nMvaa 40,188,248\nRa 168,2,26624\ns_cbranch_vccnz .T64\n.T65:\nMvaa 40,184,240\nD1 1,16,46,2048\n"
"Mvaa 40,180,232\nRa 172,2,27648\nAD 86,86,84\nMvaa 40,176,224\nAD 84,80,81\nCV 64,80,81\nMvav 128,128,72\nRa 176,2,28672\nMvav 132,128,76\nEX 82,34\nMvav 140,132,76\n"
"Ra 180,2,29696\nMvav 136,132,72\ns_waitcnt vmcnt(13) lgkmcnt(8)\nEX 83,35\nMvav 144,136,72\nRa 184,2,30720\nAD 84,84,82\nMvav 148,136,76\nD1 1,16,46,3072\n"
"s_add_u32 m0, s89, 49152\nMvav 156,140,76\nRa 188,2,31744\ns_barrier\nMvav 152,140,72\nAD 84,84,83\nCV 65,82,83\nMvav 160,144,72\nRa 128,24,32768\nMvav 164,144,76\n"
"s_add_u32 s46, s87, s88\nEX 80,36\nMvav 172,148,76\nRa 132,24,33792\nMvav 168,148,72\nWL 8\nEX 81,37\nMvav 176,152,72\nRa 136,24,34816\nAD 87,87,85\nMvav 180,152,76\nD1 1,16,46,0\n"
"Mvav 188,156,76\nRa 140,24,35840\nMvav 184,156,72\nAD 85,80,81\nCV 68,80,81\nMvav 192,160,72\nRa 144,26,32768\nMvav 196,160,76\nEX 82,38\nMvav 204,164,76\nRa 148,26,33792\n"
"Mvav 200,164,72\nWL 8\nEX 83,39\nMvav 208,168,72\nRa 152,26,34816\nAD 85,85,82\nMvav 212,168,76\nD1 1,16,46,1024\nMvav 220,172,76\nRa 156,26,35840\nMvav 216,172,72\nAD 85,85,83\n"
"CV 69,82,83\nMvav 224,176,72\nRa 160,24,40960\nMvav 228,176,76\ns_xor_b32 s89, s89, 0x10000\nEX 80,40\nMvav 236,180,76\nRa 164,24,41984\nMvav 232,180,72\nWL 6\nEX 81,41\n"
"Mvav 240,184,72\nRa 168,24,43008\nAD 84,84,80\nMvav 244,184,76\nD1 1,16,46,2048\nMvav 252,188,76\nRa 172,24,44032\nMvav 248,188,72\nAD 84,84,81\nCV 66,80,81\nCvaa 48,128,192,4\n"
"Ra 176,26,40960\nMvaa 48,132,200\nEX 82,42\nMvaa 48,136,208\nRa 180,26,41984\nMvaa 48,140,216\nWL 6\nEX 83,43\nCvaa 52,140,220,8\nRa 184,26,43008\nAD 84,84,82\nMvaa 52,136,212\n"
"D1 1,16,46,3072\ns_add_u32 m0, s89, 0\nMvaa 52,132,204\nRa 188,26,44032\nMvaa 52,128,196\nAD 84,84,83\nCV 67,82,83\nMvaa 52,144,228\nRa 128,2,49152\nMvaa 52,148,236\n"
"s_add_u32 s46, s86, s88\nEX 80,44\nMvaa 52,152,244\nRa 132,2,50176\nv_xor_b32 v24, 0x10000, v24\nMvaa 52,156,252\nWL 6\nEX 81,45\nMvaa 48,156,248\nRa 136,2,51200\nAD 85,85,80\n"
"Mvaa 48,152,240\nD1 1,16,46,0\nMvaa 48,148,232\nRa 140,2,52224\nMvaa 48,144,224\nAD 85,85,81\nCV 70,80,81\nCvaa 56,160,192,12\nRa 144,2,53248\nMvaa 56,164,200\nSv 92,1,16,49,1024\n"
"EX 82,46\nMvaa 56,168,208\nRa 148,2,54272\nv_xor_b32 v26, 0x10000, v26\nMvaa 56,172,216\nWL 6\nEX 83,47\nCvaa 60,172,220,16\nRa 152,2,55296\nAD 85,85,82\nMvaa 60,168,212\n"
"D1 1,16,46,1024\nMvaa 60,164,204\nRa 156,2,56320\nMvaa 60,160,196\nAD 85,85,83\nCV 71,82,83\nMvaa 60,176,228\nRa 160,2,57344\nv_max_f32 v90, v84, v85\nMvaa 60,180,236\n"
"s_add_u32 s88, s88, 32768\nEX 80,48\nMvaa 60,184,244\nRa 164,2,58368\nv_cmp_lt_f32 vcc, s63, v90\nMvaa 60,188,252\nWL 8\nEX 81,49\nMvaa 56,188,248\nRa 168,2,59392\n"
"s_cbranch_vccnz .T66\n.T67:\nMvaa 56,184,240\nD1 1,16,46,2048\nMvaa 56,180,232\nRa 172,2,60416\nAD 86,86,84\nMvaa 56,176,224\nAD 84,80,81\nCV 72,80,81\nMvav 128,128,64\n"
"Ra 176,2,61440\nMvav 132,128,68\nEX 82,50\nMvav 140,132,68\nRa 180,2,62464\nMvav 136,132,64\ns_waitcnt vmcnt(13) lgkmcnt(8)\nEX 83,51\nMvav 144,136,64\nRa 184,2,63488\n"
"AD 84,84,82\nMvav 148,136,68\nD1 1,16,46,3072\ns_add_u32 m0, s89, 16384\nMvav 156,140,68\nRa 188,2,64512\ns_barrier\nMvav 152,140,64\nAD 84,84,83\nCV 73,82,83\nMvav 160,144,64\n"
"Ra 128,24,0\nMvav 164,144,68\ns_add_u32 s46, s87, s88\nEX 80,52\nMvav 172,148,68\nRa 132,24,1024\nv_xor_b32 v2, 0x10000, v2\nMvav 168,148,64\nWL 8\nEX 81,53\nMvav 176,152,64\n"
"Ra 136,24,2048\nAD 87,87,85\nMvav 180,152,68\nD1 1,16,46,0\nMvav 188,156,68\nRa 140,24,3072\nMvav 184,156,64\nAD 85,80,81\nCV 76,80,81\nMvav 192,160,64\nRa 144,26,0\n"
"Mvav 196,160,68\nEX 82,54\nMvav 204,164,68\nRa 148,26,1024\nMvav 200,164,64\nWL 8\nEX 83,55\nMvav 208,168,64\nRa 152,26,2048\nAD 85,85,82\nMvav 212,168,68\nD1 1,16,46,1024\n"
"Mvav 220,172,68\nRa 156,26,3072\nMvav 216,172,64\nAD 85,85,83\nCV 77,82,83\nMvav 224,176,64\nRa 160,24,8192\nMvav 228,176,68\ns_sub_u32 s90, s90, 1\nEX 80,56\nMvav 236,180,68\n"
"Ra 164,24,9216\ns_cbranch_scc1 .E62\n.E63:\nMvav 232,180,64\nWL 6\nEX 81,57\nMvav 240,184,64\nRa 168,24,10240\nAD 84,84,80\nMvav 244,184,68\nD1 1,16,46,2048\n"
"s_sub_u32 s99, s99, 1\nMvav 252,188,68\nRa 172,24,11264\nMvav 248,188,64\nAD 84,84,81\nCV 74,80,81\ns_cbranch_scc0 .A60\ns_branch .A68\n.E62:\ns_swappc_b64 s[80:81], s[8:9]\n"
"s_branch .E63\n.HD13:\nbuffer_store_dword v25, off, s[16:19], s54 offset:4 sc0 sc1\nv_cmp_ne_u32 vcc, s15, v27\ns_nop 0\ns_cbranch_vccnz .S47\n.S48:\nv_and_b32 v27, 0x70, v1\n"
"buffer_load_dword v27, v27, s[16:19], s61 offen offset:8 sc0 sc1\ns_mov_b32 s90, 1\ns_swappc_b64 s[8:9], s[80:81]\nv_cmp_ne_u32 vcc, s15, v27\ns_nop 0\ns_cbranch_vccnz .S50\n"
".S51:\nv_and_b32 v27, 0xf0, v1\nbuffer_load_dword v27, v27, s[16:19], s61 offen offset:4 sc0 sc1\ns_mov_b32 s90, 1\ns_swappc_b64 s[8:9], s[80:81]\nv_cmp_ne_u32 vcc, s15, v27\n"
"s_nop 0\ns_cbranch_vccnz .S53\n.S54:\ns_mov_b32 s90, s91\ns_swappc_b64 s[8:9], s[80:81]\ns_branch .FF56\n.A68:\nCvaa 32,128,192,4\nRa 176,26,8192\nMvaa 32,132,200\n"
"s_add_u32 s49, s49, 131072\nEX 82,58\nMvaa 32,136,208\nRa 180,26,9216\nMvaa 32,140,216\nWL 6\nEX 83,59\nCvaa 36,140,220,8\nRa 184,26,10240\nAD 84,84,82\nMvaa 36,136,212\n"
"D1 1,16,46,3072\ns_add_u32 m0, s89, 32768\nMvaa 36,132,204\nRa 188,26,11264\nMvaa 36,128,196\nAD 84,84,83\nCV 75,82,83\ns_mov_b32 s94, s96\ns_add_u32 s95, s96, 65536\n"
"v_and_b32 v108, 0xf0, v1\nMvaa 36,144,228\nRa 128,2,16384\nMvaa 36,148,236\ns_add_u32 s46, s86, s88\nEX 80,60\nMvaa 36,152,244\nRa 132,2,17408\nMvaa 36,156,252\nWL 6\nEX 81,61\n"
"Mvaa 32,156,248\nRa 136,2,18432\nAD 85,85,80\nMvaa 32,152,240\nD1 1,16,46,0\nMvaa 32,148,232\nRa 140,2,19456\nMvaa 32,144,224\nAD 85,85,81\nCV 78,80,81\nCvaa 40,160,192,12\n"
"Ra 144,2,20480\nMvaa 40,164,200\nSv 92,1,16,49,0\nEX 82,62\nMvaa 40,168,208\nRa 148,2,21504\nMvaa 40,172,216\nWL 6\nEX 83,63\nCvaa 44,172,220,16\nRa 152,2,22528\nAD 85,85,82\n"
"Mvaa 44,168,212\nD1 1,16,46,1024\nMvaa 44,164,204\nRa 156,2,23552\nMvaa 44,160,196\nAD 85,85,83\nCV 79,82,83\nMvaa 44,176,228\nRa 160,2,24576\nv_max_f32 v90, v84, v85\n"
"Mvaa 44,180,236\ns_add_u32 s88, s88, 32768\nEX 80,32\nMvaa 44,184,244\nRa 164,2,25600\nv_cmp_lt_f32 vcc, s63, v90\nMvaa 44,188,252\nWL 8\nEX 81,33\nMvaa 40,188,248\n"
"Ra 168,2,26624\ns_cbranch_vccnz .T70\n.T71:\nMvaa 40,184,240\nD1 1,16,46,2048\nMvaa 40,180,232\nRa 172,2,27648\nAD 86,86,84\nMvaa 40,176,224\nAD 84,80,81\nCV 64,80,81\n"
"Mvav 128,128,72\nRa 176,2,28672\nv_lshrrev_b32 v92, 8, v1\nMvav 132,128,76\nEX 82,34\nMvav 140,132,76\nRa 180,2,29696\nMvav 136,132,72\ns_waitcnt vmcnt(13) lgkmcnt(8)\nEX 83,35\n"
"Mvav 144,136,72\nRa 184,2,30720\nAD 84,84,82\nMvav 148,136,76\nD1 1,16,46,3072\ns_add_u32 m0, s89, 49152\nMvav 156,140,76\nRa 188,2,31744\ns_barrier\nMvav 152,140,72\nAD 84,84,83\n"
"CV 65,82,83\n.W543:\ns_cbranch_execz .WB21\nMvav 160,144,72\nRa 128,24,32768\nv_bfe_u32 v93, v1, 6, 2\nMvav 164,144,76\ns_add_u32 s46, s87, s88\nEX 80,36\nMvav 172,148,76\n"
"Ra 132,24,33792\nv_cmp_eq_u32 vcc, v92, v93\nv_and_b32 v92, 16, v1\nv_mov_b32 v93, 0x3f80\nv_cndmask_b32 v93, 0, v93, vcc\nMvav 168,148,72\nWL 8\nEX 81,37\nMvav 176,152,72\n"
"Ra 136,24,34816\nAD 87,87,85\nMvav 180,152,76\nD1 1,16,46,0\nMvav 188,156,76\nRa 140,24,35840\nv_lshlrev_b32 v93, v92, v93\nMvav 184,156,72\nAD 85,80,81\nCV 68,80,81\n"
"Mvav 192,160,72\nRa 144,26,32768\nv_and_b32 v92, 32, v1\nMvav 196,160,76\nEX 82,38\nMvav 204,164,76\nRa 148,26,33792\nv_cmp_eq_u32 vcc, 0, v92\nv_mov_b32 v112, 0\n"
"v_mov_b32 v113, 0\nv_cndmask_b32 v114, 0, v93, vcc\nMvav 200,164,72\nWL 8\nEX 83,39\nMvav 208,168,72\nRa 152,26,34816\nAD 85,85,82\nMvav 212,168,76\nD1 1,16,46,1024\n"
"Mvav 220,172,76\nRa 156,26,35840\nv_sub_u32 v115, v93, v114\nMvav 216,172,72\nAD 85,85,83\nCV 69,82,83\nMvav 224,176,72\nRa 160,24,40960\nv_mov_b32 v116, 0\nMvav 228,176,76\n"
"s_xor_b32 s89, s89, 0x10000\nEX 80,40\nMvav 236,180,76\nRa 164,24,41984\nMvav 232,180,72\nWL 6\nEX 81,41\nMvav 240,184,72\nRa 168,24,43008\nAD 84,84,80\nMvav 244,184,76\n"
"D1 1,16,46,2048\nMvav 252,188,76\nRa 172,24,44032\nv_mov_b32 v117, 0\nMvav 248,188,72\nAD 84,84,81\nCV 66,80,81\nCvaa 48,128,192,4\nRa 176,26,40960\nMvaa 48,132,200\n"
"s_mov_b32 s86, s94\nEX 82,42\nMvaa 48,136,208\nRa 180,26,41984\nMvaa 48,140,216\nWL 6\nEX 83,43\nCvaa 52,140,220,8\nRa 184,26,43008\nAD 84,84,82\nMvaa 52,136,212\nD1 1,16,46,3072\n"
"s_add_u32 m0, s89, 0\nMvaa 52,132,204\nRa 188,26,44032\nMvaa 52,128,196\nAD 84,84,83\nCV 67,82,83\nMvaa 52,144,228\nRa 128,2,49152\nMvaa 52,148,236\ns_add_u32 s46, s86, s88\n"
"EX 80,44\nMvaa 52,152,244\nRa 132,2,50176\nv_xor_b32 v24, 0x10000, v24\nMvaa 52,156,252\nWL 6\nEX 81,45\nMvaa 48,156,248\nRa 136,2,51200\nAD 85,85,80\nMvaa 48,152,240\n"
"D1 1,16,46,0\nMvaa 48,148,232\nRa 140,2,52224\nMvaa 48,144,224\nAD 85,85,81\nCV 70,80,81\nCvaa 56,160,192,12\nRa 144,2,53248\nMvaa 56,164,200\nSv 92,1,16,49,1024\nEX 82,46\n"
"Mvaa 56,168,208\nRa 148,2,54272\nv_xor_b32 v26, 0x10000, v26\nMvaa 56,172,216\nWL 6\nEX 83,47\nCvaa 60,172,220,16\nRa 152,2,55296\nAD 85,85,82\nMvaa 60,168,212\nD1 1,16,46,1024\n"
"Mvaa 60,164,204\nRa 156,2,56320\nMvaa 60,160,196\nAD 85,85,83\nCV 71,82,83\nMvaa 60,176,228\nRa 160,2,57344\nv_max_f32 v90, v84, v85\nMvaa 60,180,236\ns_add_u32 s88, s88, 32768\n"
"EX 80,48\nMvaa 60,184,244\nRa 164,2,58368\nv_cmp_lt_f32 vcc, s63, v90\nMvaa 60,188,252\nWL 8\nEX 81,49\nMvaa 56,188,248\nRa 168,2,59392\ns_cbranch_vccnz .T72\n.T73:\n"
"Mvaa 56,184,240\nD1 1,16,46,2048\nMvaa 56,180,232\nRa 172,2,60416\nAD 86,86,84\nMvaa 56,176,224\nAD 84,80,81\nCV 72,80,81\nMvav 128,128,64\nRa 176,2,61440\nMvav 132,128,68\n"
"EX 82,50\nMvav 140,132,68\nRa 180,2,62464\nMvav 136,132,64\ns_waitcnt vmcnt(13) lgkmcnt(8)\nEX 83,51\nMvav 144,136,64\nRa 184,2,63488\nAD 84,84,82\nMvav 148,136,68\n"
"D1 1,16,46,3072\ns_add_u32 m0, s89, 16384\nMvav 156,140,68\nRa 188,2,64512\ns_barrier\nMvav 152,140,64\nAD 84,84,83\nCV 73,82,83\nMvav 160,144,64\nRa 128,24,0\nMvav 164,144,68\n"
"s_add_u32 s46, s87, s88\nEX 80,52\nMvav 172,148,68\nRa 132,24,1024\nv_xor_b32 v2, 0x10000, v2\nMvav 168,148,64\nWL 8\nEX 81,53\nMvav 176,152,64\nRa 136,24,2048\nAD 87,87,85\n"
"Mvav 180,152,68\nD1 1,16,46,0\nMvav 188,156,68\nRa 140,24,3072\nMvav 184,156,64\nAD 85,80,81\nCV 76,80,81\nMvav 192,160,64\nRa 144,26,0\nMvav 196,160,68\nEX 82,54\n"
"Mvav 204,164,68\nRa 148,26,1024\nMvav 200,164,64\nWL 8\nEX 83,55\nMvav 208,168,64\nRa 152,26,2048\nAD 85,85,82\nMvav 212,168,68\nD1 1,16,46,1024\nMvav 220,172,68\nRa 156,26,3072\n"
"Mvav 216,172,64\nAD 85,85,83\nCV 77,82,83\nMvav 224,176,64\nRa 160,24,8192\nMvav 228,176,68\nEX 80,56\nMvav 236,180,68\nRa 164,24,9216\nMvav 232,180,64\nWL 6\nEX 81,57\n"
"Mvav 240,184,64\nRa 168,24,10240\nAD 84,84,80\nMvav 244,184,68\nD1 1,16,46,2048\nMvav 252,188,68\nRa 172,24,11264\nMvav 248,188,64\nAD 84,84,81\nCV 74,80,81\nCvaa 32,128,192,4\n"
"Ra 176,26,8192\nMvaa 32,132,200\nEX 82,58\nMvaa 32,136,208\nRa 180,26,9216\nMvaa 32,140,216\nWL 6\nEX 83,59\nCvaa 36,140,220,8\nRa 184,26,10240\nAD 84,84,82\nMvaa 36,136,212\n"
"D1 1,16,46,3072\ns_add_u32 m0, s89, 32768\nMvaa 36,132,204\nRa 188,26,11264\nMvaa 36,128,196\nAD 84,84,83\nCV 75,82,83\nMvaa 36,144,228\nRa 128,2,16384\nMvaa 36,148,236\n"
"s_add_u32 s46, s86, s88\nEX 80,60\nMvaa 36,152,244\nRa 132,2,17408\nMvaa 36,156,252\nWL 6\nEX 81,61\nMvaa 32,156,248\nRa 136,2,18432\nAD 85,85,80\nMvaa 32,152,240\nD1 1,16,46,0\n"
"Mvaa 32,148,232\nRa 140,2,19456\nMvaa 32,144,224\nAD 85,85,81\nCV 78,80,81\nCvaa 40,160,192,12\nRa 144,2,20480\nMvaa 40,164,200\ns_mov_b32 s87, s95\nEX 82,62\nMvaa 40,168,208\n"
"Ra 148,2,21504\nMvaa 40,172,216\nWL 6\nEX 83,63\nCvaa 44,172,220,16\nRa 152,2,22528\nAD 85,85,82\nMvaa 44,168,212\nD1 1,16,46,1024\nMvaa 44,164,204\nRa 156,2,23552\n"
"Mvaa 44,160,196\nAD 85,85,83\nCV 79,82,83\nMvaa 44,176,228\nRa 160,2,24576\nv_max_f32 v90, v84, v85\nMvaa 44,180,236\ns_add_u32 s88, s88, 32768\nEX 80,32\nMvaa 44,184,244\n"
"Ra 164,2,25600\nv_cmp_lt_f32 vcc, s63, v90\nMvaa 44,188,252\nWL 8\nEX 81,33\nMvaa 40,188,248\nRa 168,2,26624\ns_cbranch_vccnz .T75\n.T76:\nMvaa 40,184,240\nD1 1,16,46,2048\n"
"Mvaa 40,180,232\nRa 172,2,27648\nAD 86,86,84\nMvaa 40,176,224\nAD 84,80,81\nCV 64,80,81\nMvav 128,128,72\nRa 176,2,28672\nMvav 132,128,76\nEX 82,34\nMvav 140,132,76\n"
"Ra 180,2,29696\nMvav 136,132,72\ns_waitcnt vmcnt(12) lgkmcnt(8)\nEX 83,35\nMvav 144,136,72\nRa 184,2,30720\nAD 84,84,82\nMvav 148,136,76\nD1 1,16,46,3072\n"
"s_add_u32 m0, s89, 49152\nMvav 156,140,76\nRa 188,2,31744\ns_barrier\nMvav 152,140,72\nAD 84,84,83\nCV 65,82,83\nMvav 160,144,72\nRa 128,24,32768\nMvav 164,144,76\n"
"s_add_u32 s46, s87, s88\nEX 80,36\nMvav 172,148,76\nRa 132,24,33792\nMvav 168,148,72\nWL 8\nEX 81,37\nMvav 176,152,72\nRa 136,24,34816\nAD 87,87,85\nMvav 180,152,76\nD1 1,16,46,0\n"
"Mvav 188,156,76\nRa 140,24,35840\nMvav 184,156,72\nAD 85,80,81\nCV 68,80,81\nMvav 192,160,72\nRa 144,26,32768\n"
"buffer_load_dwordx4 v[100:103], v108, s[16:19], s65 offen offset:0 sc0 sc1\nbuffer_load_dwordx4 v[104:107], v108, s[16:19], s65 offen offset:256 sc0 sc1\nMvav 196,160,76\n"
"EX 82,38\nMvav 204,164,76\nRa 148,26,33792\nMvav 200,164,72\nWL 8\nEX 83,39\nMvav 208,168,72\nRa 152,26,34816\nAD 85,85,82\nMvav 212,168,76\nD1 1,16,46,1024\nMvav 220,172,76\n"
"Ra 156,26,35840\nMvav 216,172,72\nAD 85,85,83\nCV 69,82,83\nMvav 224,176,72\nRa 160,24,40960\nMvav 228,176,76\ns_xor_b32 s89, s89, 0x10000\nEX 80,40\nMvav 236,180,76\n"
"Ra 164,24,41984\nMvav 232,180,72\nWL 6\nEX 81,41\nMvav 240,184,72\nRa 168,24,43008\nAD 84,84,80\nMvav 244,184,76\nD1 1,16,46,2048\nMvav 252,188,76\nRa 172,24,44032\n"
"Mvav 248,188,72\nAD 84,84,81\nCV 66,80,81\nCvaa 48,128,192,4\nRa 176,26,40960\nMvaa 48,132,200\nEX 82,42\nMvaa 48,136,208\nRa 180,26,41984\nMvaa 48,140,216\nWL 6\nEX 83,43\n"
"Cvaa 52,140,220,8\nRa 184,26,43008\nAD 84,84,82\nMvaa 52,136,212\nD1 1,16,46,3072\ns_add_u32 m0, s89, 0\nMvaa 52,132,204\nRa 188,26,44032\nMvaa 52,128,196\nAD 84,84,83\n"
"CV 67,82,83\nMvaa 52,144,228\nRa 128,2,49152\nWV 3\nv_xor_b32 v109, s15, v102\nv_xor_b32 v110, s15, v106\nv_or_b32 v109, v109, v110\nv_cmp_ne_u32 vcc, 0, v109\ns_nop 3\n"
"s_cmp_lg_u64 vcc, 0\ns_cselect_b32 s67, 1, 0\ns_cselect_b32 s6, 0x40000000, s66\nMvaa 52,148,236\ns_add_u32 s46, s86, s88\nEX 80,44\ns_add_u32 s5, s6, 0\nMvaa 52,152,244\n"
"Ra 132,2,50176\nv_xor_b32 v24, 0x10000, v24\nMvaa 52,156,252\nWL 6\nEX 81,45\nbuffer_load_dwordx4 a[0:3], v1, s[16:19], s5 offen offset:0 sc1\nMvaa 48,156,248\nRa 136,2,51200\n"
"AD 85,85,80\nMvaa 48,152,240\nD1 1,16,46,0\nMvaa 48,148,232\nRa 140,2,52224\nbuffer_load_dwordx4 a[4:7], v1, s[16:19], s5 offen offset:1024 sc1\nMvaa 48,144,224\nAD 85,85,81\n"
"CV 70,80,81\nCvaa 56,160,192,12\nRa 144,2,53248\nMvaa 56,164,200\nEX 82,46\nbuffer_load_dwordx4 a[8:11], v1, s[16:19], s5 offen offset:2048 sc1\nMvaa 56,168,208\nRa 148,2,54272\n"
"v_xor_b32 v26, 0x10000, v26\nMvaa 56,172,216\nWL 6\nEX 83,47\nCvaa 60,172,220,16\nRa 152,2,55296\nAD 85,85,82\n"
"buffer_load_dwordx4 a[12:15], v1, s[16:19], s5 offen offset:3072 sc1\nMvaa 60,168,212\nD1 1,16,46,1024\ns_add_u32 s5, s6, 4096\nMvaa 60,164,204\nRa 156,2,56320\nMvaa 60,160,196\n"
"AD 85,85,83\nCV 71,82,83\nbuffer_load_dwordx4 a[16:19], v1, s[16:19], s5 offen offset:0 sc1\nMvaa 60,176,228\nRa 160,2,57344\nv_max_f32 v90, v84, v85\nMvaa 60,180,236\n"
"s_add_u32 s88, s88, 32768\nEX 80,48\nMvaa 60,184,244\nRa 164,2,58368\nv_cmp_lt_f32 vcc, s63, v90\nMvaa 60,188,252\nWL 8\nEX 81,49\nMvaa 56,188,248\nRa 168,2,59392\n"
"s_cbranch_vccnz .T77\n.T78:\nbuffer_load_dwordx4 a[20:23], v1, s[16:19], s5 offen offset:1024 sc1\nMvaa 56,184,240\nD1 1,16,46,2048\nMvaa 56,180,232\nRa 172,2,60416\nAD 86,86,84\n"
"Mvaa 56,176,224\nAD 84,80,81\nCV 72,80,81\nbuffer_load_dwordx4 a[24:27], v1, s[16:19], s5 offen offset:2048 sc1\nMvav 128,128,64\nRa 176,2,61440\nMvav 132,128,68\nEX 82,50\n"
"Mvav 140,132,68\nRa 180,2,62464\nbuffer_load_dwordx4 a[28:31], v1, s[16:19], s5 offen offset:3072 sc1\nMvav 136,132,64\nWL 8\nEX 83,51\ns_add_u32 s5, s6, 16384\nMvav 144,136,64\n"
"Ra 184,2,63488\nAD 84,84,82\nMvav 148,136,68\nD1 1,16,46,3072\ns_add_u32 m0, s89, 16384\nbuffer_load_dwordx4 a[32:35], v1, s[16:19], s5 offen offset:0 sc1\nMvav 156,140,68\n"
"Ra 188,2,64512\ns_barrier\nMvav 152,140,64\nAD 84,84,83\nCV 73,82,83\nMvav 160,144,64\nRa 128,24,0\nbuffer_load_dwordx4 a[36:39], v1, s[16:19], s5 offen offset:1024 sc1\n"
"Mvav 164,144,68\ns_add_u32 s46, s87, s88\nEX 80,52\nMvav 172,148,68\nRa 132,24,1024\nv_xor_b32 v2, 0x10000, v2\nMvav 168,148,64\nWL 8\nEX 81,53\n"
"buffer_load_dwordx4 a[40:43], v1, s[16:19], s5 offen offset:2048 sc1\nMvav 176,152,64\nRa 136,24,2048\nAD 87,87,85\nMvav 180,152,68\nD1 1,16,46,0\nMvav 188,156,68\nRa 140,24,3072\n"
"buffer_load_dwordx4 a[44:47], v1, s[16:19], s5 offen offset:3072 sc1\nMvav 184,156,64\nAD 85,80,81\nCV 76,80,81\ns_add_u32 s5, s6, 20480\nMvav 192,160,64\nRa 144,26,0\n"
"Mvav 196,160,68\nEX 82,54\nbuffer_load_dwordx4 a[48:51], v1, s[16:19], s5 offen offset:0 sc1\nMvav 204,164,68\nRa 148,26,1024\nMvav 200,164,64\nWL 8\nEX 83,55\nMvav 208,168,64\n"
"Ra 152,26,2048\nAD 85,85,82\nbuffer_load_dwordx4 a[52:55], v1, s[16:19], s5 offen offset:1024 sc1\nMvav 212,168,68\nD1 1,16,46,1024\nMvav 220,172,68\nRa 156,26,3072\n"
"Mvav 216,172,64\nAD 85,85,83\nCV 77,82,83\nbuffer_load_dwordx4 a[56:59], v1, s[16:19], s5 offen offset:2048 sc1\nMvav 224,176,64\nRa 160,24,8192\nMvav 228,176,68\nEX 80,56\n"
"Mvav 236,180,68\nRa 164,24,9216\nbuffer_load_dwordx4 a[60:63], v1, s[16:19], s5 offen offset:3072 sc1\nMvav 232,180,64\nWL 10\nEX 81,57\nMvav 240,184,64\nRa 168,24,10240\n"
"AD 84,84,80\nMvav 244,184,68\nD1 1,16,46,2048\nMvav 252,188,68\nRa 172,24,11264\nMvav 248,188,64\nAD 84,84,81\nCV 74,80,81\nWV 20\ns_cmp_lg_u32 s67, 0\ns_cbranch_scc0 .E86\n"
"v_xor_b32 v109, s15, v102\nv_xor_b32 v110, s15, v106\nv_or_b32 v109, v109, v110\nv_cmp_ne_u32 vcc, 0, v109\ns_nop 0\ns_cbranch_vccnz .S83\n.S84:\n.W544:\ns_cbranch_execz .WB21\n"
"s_add_u32 s5, s66, 0\nbuffer_load_dwordx4 a[0:3], v1, s[16:19], s5 offen offset:0 sc1\nbuffer_load_dwordx4 a[4:7], v1, s[16:19], s5 offen offset:1024 sc1\n"
"buffer_load_dwordx4 a[8:11], v1, s[16:19], s5 offen offset:2048 sc1\nbuffer_load_dwordx4 a[12:15], v1, s[16:19], s5 offen offset:3072 sc1\ns_add_u32 s5, s66, 4096\n"
"buffer_load_dwordx4 a[16:19], v1, s[16:19], s5 offen offset:0 sc1\nbuffer_load_dwordx4 a[20:23], v1, s[16:19], s5 offen offset:1024 sc1\n"
"buffer_load_dwordx4 a[24:27], v1, s[16:19], s5 offen offset:2048 sc1\nbuffer_load_dwordx4 a[28:31], v1, s[16:19], s5 offen offset:3072 sc1\ns_add_u32 s5, s66, 16384\n"
"buffer_load_dwordx4 a[32:35], v1, s[16:19], s5 offen offset:0 sc1\nbuffer_load_dwordx4 a[36:39], v1, s[16:19], s5 offen offset:1024 sc1\n"
"buffer_load_dwordx4 a[40:43], v1, s[16:19], s5 offen offset:2048 sc1\nbuffer_load_dwordx4 a[44:47], v1, s[16:19], s5 offen offset:3072 sc1\ns_add_u32 s5, s66, 20480\n"
"buffer_load_dwordx4 a[48:51], v1, s[16:19], s5 offen offset:0 sc1\nbuffer_load_dwordx4 a[52:55], v1, s[16:19], s5 offen offset:1024 sc1\n"
"buffer_load_dwordx4 a[56:59], v1, s[16:19], s5 offen offset:2048 sc1\nbuffer_load_dwordx4 a[60:63], v1, s[16:19], s5 offen offset:3072 sc1\nWV 0\ns_mov_b32 s67, 0\n.E86:\n"
"v_cmp_lt_f32 vcc, 0, v101\nSU 20,100,88\nv_mul_f32 v101, 0x3e800000, v101\nv_cndmask_b32 v20, 0, v20, vcc\nv_cmp_lt_f32 vcc, 0, v105\nSU 21,104,89\n"
"v_mul_f32 v105, 0x3e800000, v105\nv_cndmask_b32 v21, 0, v21, vcc\nv_or_b32 v91, v20, v21\nv_cmp_ne_u32 vcc, 0, v91\ns_nop 0\ns_cbranch_vccnz .D81\nAD 86,86,101\nAD 87,87,105\n"
".D82:\nMvva 128,114,0\nMvva 136,112,0\nEX 82,58\nMvva 140,112,4\nMvva 132,114,4\nWV 15\nEX 83,59\nMvva 144,114,8\nAD 84,84,82\nMvva 152,112,8\nD1 1,16,46,3072\nMvva 156,112,12\n"
"s_add_u32 m0, s89, 32768\ns_add_u32 s46, s86, s88\nMvva 148,114,12\nAD 84,84,83\nCV 75,82,83\nMvva 160,114,16\nRa 128,2,16384\nv_xor_b32 v3, 0x10000, v2\n"
"v_add_u32 v0, 0x20000, v1\nMvva 168,112,16\nEX 80,60\nMvva 172,112,20\nRa 132,2,17408\ns_sub_u32 s94, s49, 1703936\nMvva 164,114,20\nWV 10\nEX 81,61\nMvva 176,114,24\n"
"Ra 136,2,18432\nAD 85,85,80\ns_xor_b32 s92, s94, 0x202000\nMvva 184,112,24\nD1 1,16,46,0\ns_lshl_b32 s89, s11, 12\nMvva 188,112,28\nRa 140,2,19456\ns_lshl1_add_u32 s2, s12, 0\n"
"Mvva 180,114,28\nAD 85,85,81\nCV 78,80,81\nMvva 192,114,32\nRa 144,2,20480\ns_lshl3_add_u32 s2, s2, s13\ns_lshl4_add_u32 s2, s2, 0xc048200\nMvva 200,112,32\nEX 82,62\n"
"Mvva 204,112,36\nRa 148,2,21504\ns_and_b32 s3, s72, 0x2000\nMvva 196,114,36\nWV 6\nEX 83,63\nMvva 208,114,40\nRa 152,2,22528\nAD 85,85,82\nMvva 216,112,40\nD1 1,16,46,1024\n"
"s_cmp_eq_u32 s11, 1\ns_cselect_b32 s54, s2, s54\ns_cselect_b32 s3, s3, 0\nMvva 220,112,44\nRa 156,2,23552\nv_xor_b32 v25, s3, v25\nMvva 212,114,44\nAD 85,85,83\nCV 79,82,83\n"
"Mvva 224,114,48\nRa 160,2,24576\nv_max_f32 v90, v84, v85\nMvva 232,112,48\nv_cmp_lt_f32 vcc, s63, v90\nMvva 236,112,52\nRa 164,2,25600\nMvva 228,114,52\n"
"s_waitcnt vmcnt(4) lgkmcnt(8)\nMvva 240,114,56\nRa 168,2,26624\nMvva 248,112,56\nD1 1,16,46,2048\nMvva 252,112,60\nRa 172,2,27648\nMvva 244,114,60\ns_cbranch_vccnz .T87\n.T88:\n"
"AD 86,86,84\nAD 87,87,85\nMvav 128,128,72\nRa 176,2,28672\nv_mov_b32 v84, v86\nv_mov_b32 v85, v87\nMvav 132,128,76\nv_permlane32_swap_b32 v86, v84\nv_permlane32_swap_b32 v87, v85\n"
"Mvav 140,132,76\nRa 180,2,29696\nAD 86,86,84\nMvav 136,132,72\nWL 8\nAD 87,87,85\nMvav 144,136,72\nRa 184,2,30720\nv_mov_b32 v84, v86\nv_mov_b32 v85, v87\nMvav 148,136,76\n"
"D1 1,16,46,3072\nv_permlane16_swap_b32 v86, v84\nMvav 156,140,76\nRa 188,2,31744\nv_permlane16_swap_b32 v87, v85\nMvav 152,140,72\nAD 86,86,84\nAD 87,87,85\nMvav 160,144,72\n"
"Ra 128,2,0\nv_rcp_f32 v86, v86\nMvav 164,144,76\nv_rcp_f32 v87, v87\nMvav 172,148,76\nRa 132,2,1024\nMU 128,128,86\nMU 129,129,86\nMvav 168,148,72\nWL 8\n"
"s_add_u32 m0, s89, 131072\ns_add_u32 s46, s96, 1212416\nCV 96,128,129\nMvav 176,152,72\nRa 136,2,2048\nMU 130,130,86\nMU 131,131,86\nMvav 180,152,76\nD1 1,16,46,0\nCV 97,130,131\n"
"MU 132,132,87\nMvav 188,156,76\nRa 140,2,3072\nMU 133,133,87\nCV 100,132,133\nMvav 184,156,72\nMU 134,134,87\nMU 135,135,87\nCV 101,134,135\nMvav 192,160,72\nRa 144,2,4096\n"
"MU 136,136,86\nMU 137,137,86\nCV 98,136,137\nMvav 196,160,76\nMU 138,138,86\nMU 139,139,86\nCV 99,138,139\nMvav 204,164,76\nRa 148,2,5120\nMU 140,140,87\nMU 141,141,87\n"
"Mvav 200,164,72\nWL 8\nCV 102,140,141\nMU 142,142,87\nMvav 208,168,72\nRa 152,2,6144\nMU 143,143,87\nCV 103,142,143\nMvav 212,168,76\nD1 1,16,46,1024\nMU 144,144,86\n"
"MU 145,145,86\nMvav 220,172,76\nRa 156,2,7168\nCV 104,144,145\nMU 146,146,86\nMU 147,147,86\nMvav 216,172,72\nCV 105,146,147\nMU 148,148,87\nMU 149,149,87\nMvav 224,176,72\n"
"Ra 160,2,8192\nCV 108,148,149\nMU 150,150,87\nMvav 228,176,76\nMU 151,151,87\nCV 109,150,151\nMU 152,152,86\nMvav 236,180,76\nRa 164,2,9216\nMU 153,153,86\nCV 106,152,153\n"
"Mvav 232,180,72\nWL 8\nMU 154,154,86\nMU 155,155,86\nCV 107,154,155\nMvav 240,184,72\nRa 168,2,10240\nMU 156,156,87\nMU 157,157,87\nMvav 244,184,76\nD1 1,16,46,2048\n"
"CV 110,156,157\nMU 158,158,87\nMvav 252,188,76\nRa 172,2,11264\nMU 159,159,87\nCV 111,158,159\nMvav 248,188,72\nMU 160,160,86\nMU 161,161,86\nCV 112,160,161\nZvav 128,128,96\n"
"Ra 176,2,12288\nMU 162,162,86\nMU 163,163,86\nMvav 128,132,104\nCV 113,162,163\nMU 164,164,87\nZvav 132,132,108\nRa 180,2,13312\nMU 165,165,87\nCV 116,164,165\nMvav 132,128,100\n"
"WL 8\nMU 166,166,87\nMU 167,167,87\nZvav 140,136,100\nRa 184,2,14336\nCV 117,166,167\nMU 168,168,86\nMvav 140,140,108\nD1 1,16,46,3072\nMU 169,169,86\nCV 114,168,169\n"
"Zvav 136,140,104\nRa 188,2,15360\ns_barrier\nMU 170,170,86\nMvav 136,136,96\nMU 171,171,86\nCV 115,170,171\nMU 172,172,87\nZvav 144,144,96\nRa 128,2,32768\nMU 173,173,87\n"
"CV 118,172,173\nMvav 144,148,104\nMU 174,174,87\nMU 175,175,87\nbuffer_store_dword v25, off, s[16:19], s54 offset:512 sc0 sc1\nZvav 148,148,108\nRa 132,2,33792\nCV 119,174,175\n"
"MU 176,176,86\nMvav 148,144,100\nWL 8\ns_add_u32 m0, s89, 114688\ns_add_u32 s46, s96, 1179648\nZvav 156,152,100\nRa 136,2,34816\nMU 177,177,86\nCV 120,176,177\nMvav 156,156,108\n"
"D1 1,16,46,0\nMU 178,178,86\nMU 179,179,86\nZvav 152,156,104\nRa 140,2,35840\nCV 121,178,179\nMU 180,180,87\nMvav 152,152,96\nMU 181,181,87\nCV 124,180,181\nMU 182,182,87\n"
"Zvav 64,160,96\nRa 144,2,36864\nMU 183,183,87\nCV 125,182,183\nMvav 64,164,104\nMU 184,184,86\nMU 185,185,86\nZvav 68,164,108\nRa 148,2,37888\nCV 122,184,185\nMU 186,186,86\n"
"Mvav 68,160,100\nWL 8\nMU 187,187,86\nCV 123,186,187\nZvav 76,168,100\nRa 152,2,38912\nMU 188,188,87\nMU 189,189,87\nMvav 76,172,108\nD1 1,16,46,1024\nCV 126,188,189\n"
"MU 190,190,87\nZvav 72,172,104\nRa 156,2,39936\nMU 191,191,87\nCV 127,190,191\nMvav 72,168,96\nMU 192,192,86\nMU 193,193,86\nCV 32,192,193\nMvav 128,176,112\nRa 160,2,40960\n"
"MU 194,194,86\nMU 195,195,86\nMvav 128,180,120\nCV 33,194,195\nMU 196,196,87\nMU 197,197,87\nMvav 132,180,124\nRa 164,2,41984\nCV 36,196,197\nMU 198,198,87\nMvav 132,176,116\n"
"WL 8\nMU 199,199,87\nMvav 140,184,116\nRa 168,2,43008\nCV 37,198,199\nMU 200,200,86\nMvav 140,188,124\nD1 1,16,46,2048\nMU 201,201,86\nCV 34,200,201\nMvav 136,188,120\n"
"Ra 172,2,44032\nMU 202,202,86\nMU 203,203,86\nMvav 136,184,112\nCV 35,202,203\nMU 204,204,87\nMU 205,205,87\nMvav 144,128,112\nRa 176,2,45056\nCV 38,204,205\nMU 206,206,87\n"
"Mvav 144,132,120\nMU 207,207,87\nCV 39,206,207\nMvav 148,132,124\nRa 180,2,46080\nMU 208,208,86\nMU 209,209,86\nMvav 148,128,116\nWL 8\nCV 40,208,209\nMU 210,210,86\n"
"Mvav 156,136,116\nRa 184,2,47104\nMU 211,211,86\nCV 41,210,211\nMvav 156,140,124\nD1 1,16,46,3072\nMU 212,212,87\nMU 213,213,87\nMvav 152,140,120\nRa 188,2,48128\nCV 44,212,213\n"
"Mvav 152,136,112\nMU 214,214,87\nMU 215,215,87\nCV 45,214,215\nMvav 64,144,112\nRa 128,2,49152\nMU 216,216,86\nMU 217,217,86\nMvav 64,148,120\nCV 42,216,217\nMU 218,218,86\n"
"MU 219,219,86\nMvav 68,148,124\nRa 132,2,50176\nCV 43,218,219\nMU 220,220,87\nMvav 68,144,116\nWL 8\ns_add_u32 m0, s89, 147456\ns_add_u32 s46, s96, 1245184\nMvav 76,152,116\n"
"Ra 136,2,51200\nMU 221,221,87\nCV 46,220,221\nMvav 76,156,124\nD1 1,16,46,0\nMU 222,222,87\nMU 223,223,87\nMvav 72,156,120\nRa 140,2,52224\nCV 47,222,223\nMvav 72,152,112\n"
"MU 224,224,86\nMU 225,225,86\nCV 48,224,225\nMvav 128,160,32\nRa 144,2,53248\nMU 226,226,86\nMU 227,227,86\nMvav 128,164,40\nCV 49,226,227\nMU 228,228,87\nMvav 132,164,44\n"
"Ra 148,2,54272\nMU 229,229,87\nCV 52,228,229\nMvav 132,160,36\nWL 8\nMU 230,230,87\nMU 231,231,87\nMvav 140,168,36\nRa 152,2,55296\nCV 53,230,231\nMU 232,232,86\nMvav 140,172,44\n"
"D1 1,16,46,1024\nMU 233,233,86\nCV 50,232,233\nMvav 136,172,40\nRa 156,2,56320\nMU 234,234,86\nMvav 136,168,32\nMU 235,235,86\nCV 51,234,235\nMU 236,236,87\nMvav 144,176,32\n"
"Ra 160,2,57344\nMU 237,237,87\nCV 54,236,237\nMvav 144,180,40\nMU 238,238,87\nMU 239,239,87\nCV 55,238,239\nMvav 148,180,44\nRa 164,2,58368\nMU 240,240,86\nMU 241,241,86\n"
"Mvav 148,176,36\nWL 8\nCV 56,240,241\nMvav 156,184,36\nRa 168,2,59392\nMU 242,242,86\nMU 243,243,86\nMvav 156,188,44\nD1 1,16,46,2048\nCV 57,242,243\nMU 244,244,87\n"
"Mvav 152,188,40\nRa 172,2,60416\nMU 245,245,87\nCV 60,244,245\nMvav 152,184,32\nMU 246,246,87\nMU 247,247,87\nCV 61,246,247\nMvav 64,128,32\nRa 176,2,61440\nMU 248,248,86\n"
"MU 249,249,86\nMvav 64,132,40\nCV 58,248,249\nMU 250,250,86\nMvav 68,132,44\nRa 180,2,62464\nMU 251,251,86\nCV 59,250,251\nMvav 68,128,36\ns_waitcnt vmcnt(16) lgkmcnt(8)\n"
"MU 252,252,87\nMU 253,253,87\nMvav 76,136,36\nRa 184,2,63488\nCV 62,252,253\nMU 254,254,87\nMvav 76,140,44\nD1 1,16,46,3072\nMU 255,255,87\nCV 63,254,255\nMvav 72,140,40\n"
"Ra 188,2,64512\ns_barrier\nMvav 72,136,32\nMvav 128,144,48\nRa 128,3,0\nMvav 128,148,56\nMvav 132,148,60\nRa 132,3,1024\nMvav 132,144,52\nWL 8\nMvav 140,152,52\nRa 136,3,2048\n"
"Mvav 140,156,60\nMvav 136,156,56\nRa 140,3,3072\nMvav 136,152,48\nMvav 144,160,48\nRa 144,3,4096\nMvav 144,164,56\nMvav 148,164,60\nRa 148,3,5120\nCV 132,132,133\nMvav 148,160,52\n"
"WL 8\nCV 133,134,135\nMvav 156,168,52\nRa 152,3,6144\nCV 134,140,141\nMvav 156,172,60\nCV 135,142,143\ns_add_u32 s5, s94, 0\nMvav 152,172,56\nRa 156,3,7168\nSv 132,1,16,5,1024\n"
"Mvav 152,168,48\nCV 128,128,129\nCV 129,130,131\nMvav 64,176,48\nRa 160,3,8192\nCV 130,136,137\nMvav 64,180,56\nbuffer_load_dword v27, off, s[16:19], s60 offset:512 sc0 sc1\n"
"CV 131,138,139\nMvav 68,180,60\nRa 164,3,9216\nSv 128,1,16,5,0\nMvav 68,176,52\nWL 6\nCV 148,148,149\nMvav 76,184,52\nRa 168,3,10240\nCV 149,150,151\nMvav 76,188,60\n"
"CV 150,156,157\nCV 151,158,159\nMvav 72,188,56\nRa 172,3,11264\ns_add_u32 s5, s94, 131072\nMvav 72,184,48\nSv 148,1,16,5,1024\nCV 144,144,145\nZvav 160,128,96\nRa 176,3,12288\n"
"CV 145,146,147\nMvav 160,132,104\nCV 146,152,153\nCV 147,154,155\nMvav 160,136,112\nRa 180,3,13312\nSv 144,1,16,5,0\nMvav 160,140,120\nWL 6\nCV 68,68,69\nZvav 164,140,124\n"
"Ra 184,3,14336\nCV 69,70,71\nMvav 164,136,116\nCV 70,76,77\nCV 71,78,79\nMvav 164,132,108\nRa 188,3,15360\ns_add_u32 s5, s94, 262144\nMvav 164,128,100\nSv 68,1,16,5,1024\n"
"CV 64,64,65\nMvav 164,144,36\nRa 128,3,16384\nCV 65,66,67\nMvav 164,148,44\nCV 66,72,73\nCV 67,74,75\nMvav 164,152,52\nRa 132,3,17408\nSv 64,1,16,5,0\nMvav 164,156,60\nWL 6\n"
"Mvav 160,156,56\nRa 136,3,18432\n.W545:\ns_cbranch_execz .WB21\nMvav 160,152,48\nMvav 160,148,40\nRa 140,3,19456\nMvav 160,144,32\nZvav 168,160,96\nRa 144,3,20480\n"
"Mvav 168,164,104\nMvav 168,168,112\nRa 148,3,21504\nMvav 168,172,120\nWL 6\nZvav 172,172,124\nRa 152,3,22528\nMvav 172,168,116\nMvav 172,164,108\nRa 156,3,23552\nMvav 172,160,100\n"
"Mvav 172,176,36\nRa 160,3,24576\nMvav 172,180,44\nMvav 172,184,52\nRa 164,3,25600\nMvav 172,188,60\nWL 6\nMvav 168,188,56\nRa 168,3,26624\nMvav 168,184,48\nMvav 168,180,40\n"
"Ra 172,3,27648\nMvav 168,176,32\nZvav 176,128,96\nRa 176,3,28672\nCV 164,164,165\nMvav 176,132,104\nCV 165,166,167\nCV 166,172,173\nMvav 176,136,112\nRa 180,3,29696\n"
"CV 167,174,175\nMvav 176,140,120\ns_waitcnt vmcnt(11) lgkmcnt(6)\ns_add_u32 s5, s94, 393216\nZvav 180,140,124\nRa 184,3,30720\nSv 164,1,16,5,1024\nMvav 180,136,116\n"
"CV 160,160,161\nCV 161,162,163\nMvav 180,132,108\nRa 188,3,31744\ns_barrier\nMvav 180,128,100\nCV 162,168,169\nCV 163,170,171\nMvav 180,144,36\nRa 128,3,32768\nSv 160,1,16,5,0\n"
"Mvav 180,148,44\nMvav 180,152,52\nRa 132,3,33792\nMvav 180,156,60\nWL 6\ns_add_u32 m0, s89, 16384\ns_add_u32 s46, s96, 1277952\nMvav 176,156,56\nRa 136,3,34816\nMvav 176,152,48\n"
"D1 1,16,46,0\nMvav 176,148,40\nRa 140,3,35840\nMvav 176,144,32\nZvav 184,160,96\nRa 144,3,36864\nMvav 184,164,104\nMvav 184,168,112\nRa 148,3,37888\nMvav 184,172,120\nWL 6\n"
"Zvav 188,172,124\nRa 152,3,38912\nMvav 188,168,116\nD1 1,16,46,1024\nMvav 188,164,108\nRa 156,3,39936\nMvav 188,160,100\nMvav 188,176,36\nRa 160,3,40960\nWV 9\n"
"v_cmp_ne_u32 vcc, s15, v27\ns_nop 0\ns_cbranch_vccnz .S114\n.S115:\nMvav 188,180,44\ns_add_u32 s4, s92, 0\nMvav 188,184,52\nRa 164,3,41984\nMvav 188,188,60\nWL 6\n"
"buffer_load_dwordx4 v[208:211], v1, s[16:19], s4 offen offset:0 sc0 sc1\nMvav 184,188,56\nRa 168,3,43008\nMvav 184,184,48\nD1 1,16,46,2048\nMvav 184,180,40\nRa 172,3,44032\n"
"Mvav 184,176,32\nbuffer_load_dwordx4 v[212:215], v1, s[16:19], s4 offen offset:1024 sc0 sc1\nZvav 192,128,96\nRa 176,3,45056\nv_lshrrev_b32 v88, 8, v1\nMvav 192,132,104\n"
"v_lshlrev_b32 v89, 5, v88\nCV 180,180,181\nMvav 192,136,112\nRa 180,3,46080\nCV 181,182,183\nMvav 192,140,120\nWL 6\nCV 182,188,189\nZvav 196,140,124\nRa 184,3,47104\n"
"v_bfe_u32 v90, v1, 4, 4\nMvav 196,136,116\nD1 1,16,46,3072\nCV 183,190,191\nMvav 196,132,108\nRa 188,3,48128\ns_add_u32 s5, s94, 524288\nMvav 196,128,100\n"
"v_mul_u32_u24 v91, 896, v90\nSv 180,1,16,5,1024\nMvav 196,144,36\nRa 128,3,49152\nv_lshl_add_u32 v91, v88, 4, v91\nMvav 196,148,44\nv_add_u32 v90, s21, v90\nCV 176,176,177\n"
"Mvav 196,152,52\nRa 132,3,50176\nCV 177,178,179\nMvav 196,156,60\nWL 6\ns_add_u32 m0, s89, 0\ns_add_u32 s46, s96, 1310720\nMvav 192,156,56\nRa 136,3,51200\n"
"v_lshlrev_b32 v88, 3, v90\nMvav 192,152,48\nD1 1,16,46,0\nCV 178,184,185\nMvav 192,148,40\nRa 140,3,52224\nCV 179,186,187\nMvav 192,144,32\nv_add_u32 v94, 64, v90\n"
"Sv 176,1,16,5,0\nZvav 200,160,96\nRa 144,3,53248\ns_mov_b32 s63, 0xc040000\nMvav 200,164,104\nv_lshrrev_b32 v92, 8, v1\nMvav 200,168,112\nRa 148,3,54272\nMvav 200,172,120\nWL 6\n"
"Zvav 204,172,124\nRa 152,3,55296\nv_bfe_u32 v93, v1, 6, 2\nMvav 204,168,116\nD1 1,16,46,1024\nv_cmp_eq_u32 vcc, v92, v93\nv_and_b32 v92, 16, v1\nv_mov_b32 v93, 0x3f80\n"
"v_cndmask_b32 v93, 0, v93, vcc\nMvav 204,164,108\nRa 156,3,56320\nMvav 204,160,100\nv_lshlrev_b32 v93, v92, v93\nMvav 204,176,36\nRa 160,3,57344\nv_and_b32 v92, 32, v1\n"
"Mvav 204,180,44\ns_add_u32 s4, s92, 131072\nv_cmp_eq_u32 vcc, 0, v92\nv_mov_b32 v4, 0\nv_mov_b32 v5, 0\nv_cndmask_b32 v6, 0, v93, vcc\nMvav 204,184,52\nRa 164,3,58368\n"
"Mvav 204,188,60\nWL 6\nbuffer_load_dwordx4 v[216:219], v1, s[16:19], s4 offen offset:0 sc0 sc1\nMvav 200,188,56\nRa 168,3,59392\nv_sub_u32 v7, v93, v6\nMvav 200,184,48\n"
"D1 1,16,46,2048\nMvav 200,180,40\nRa 172,3,60416\nMvav 200,176,32\nbuffer_load_dwordx4 v[220:223], v1, s[16:19], s4 offen offset:1024 sc0 sc1\nZvav 160,128,96\nRa 176,3,61440\n"
"v_mov_b32 v8, 0\nMvav 160,132,104\nv_mov_b32 v9, 0\nCV 196,196,197\nMvav 160,136,112\nRa 180,3,62464\nCV 197,198,199\nMvav 160,140,120\nWL 6\nCV 198,204,205\nZvav 164,140,124\n"
"Ra 184,3,63488\nCV 199,206,207\nMvav 164,136,116\nD1 1,16,46,3072\ns_add_u32 s5, s94, 655360\nMvav 164,132,108\nRa 188,3,64512\ns_barrier\nMvav 164,128,100\nSv 196,1,16,5,1024\n"
"CV 192,192,193\nMvav 164,144,36\nRa 128,0,0\nCV 193,194,195\nMvav 164,148,44\nCV 194,200,201\nCV 195,202,203\nMvav 164,152,52\nRa 132,0,1024\nSv 192,1,16,5,0\nMvav 164,156,60\n"
"WL 6\ns_add_u32 m0, s89, 32768\ns_add_u32 s46, s96, 1343488\nMvav 160,156,56\nRa 136,0,2048\nMvav 160,152,48\nD1 1,16,46,0\nMvav 160,148,40\nRa 140,0,3072\nMvav 160,144,32\n"
"Zvav 168,160,96\nRa 144,0,4096\nWV 12\nv_max_u32 v21, v211, v215\nv_cmp_eq_u32 vcc, -1, v21\ns_nop 0\ns_cbranch_vccnz .S90\n.S91:\nMvav 168,164,104\nv_lshlrev_b32 v176, 16, v208\n"
"Mvav 168,168,112\nRa 148,0,5120\nMvav 168,172,120\nWL 6\nv_and_b32 v177, 0xffff0000, v208\nZvav 172,172,124\nRa 152,0,6144\nMvav 172,168,116\nD1 1,16,46,1024\nMvav 172,164,108\n"
"Ra 156,0,7168\nv_lshlrev_b32 v178, 16, v209\nMvav 172,160,100\nv_and_b32 v179, 0xffff0000, v209\nMvav 172,176,36\nRa 160,0,8192\nv_lshlrev_b32 v180, 16, v212\nMvav 172,180,44\n"
"v_and_b32 v181, 0xffff0000, v212\ns_add_u32 s4, s92, 262144\nMvav 172,184,52\nRa 164,0,9216\nMvav 172,188,60\nWL 6\nv_lshlrev_b32 v182, 16, v213\n"
"buffer_load_dwordx4 v[224:227], v1, s[16:19], s4 offen offset:0 sc0 sc1\nMvav 168,188,56\nRa 168,0,10240\nMvav 168,184,48\nD1 1,16,46,2048\nMvav 168,180,40\nRa 172,0,11264\n"
"v_and_b32 v183, 0xffff0000, v213\nMvav 168,176,32\nv_lshlrev_b32 v184, 16, v210\nbuffer_load_dwordx4 v[228:231], v1, s[16:19], s4 offen offset:1024 sc0 sc1\nMvav 176,128,96\n"
"Ra 176,0,12288\nv_and_b32 v185, 0xffff0000, v210\nMvav 176,132,104\nv_lshlrev_b32 v186, 16, v211\nCV 164,164,165\nMvav 176,136,112\nRa 180,0,13312\nCV 165,166,167\n"
"Mvav 176,140,120\nWL 6\nCV 166,172,173\nMvav 180,140,124\nRa 184,0,14336\nv_and_b32 v187, 0xffff0000, v211\nMvav 180,136,116\nD1 1,16,46,3072\nCV 167,174,175\nMvav 180,132,108\n"
"Ra 188,0,15360\ns_add_u32 s5, s94, 786432\nMvav 180,128,100\nv_lshlrev_b32 v188, 16, v214\nSv 164,1,16,5,1024\nMvav 180,144,36\nRa 128,0,16384\nv_and_b32 v189, 0xffff0000, v214\n"
"Mvav 180,148,44\nv_lshlrev_b32 v190, 16, v215\nCV 160,160,161\nMvav 180,152,52\nRa 132,0,17408\nCV 161,162,163\nMvav 180,156,60\nWL 6\ns_add_u32 m0, s89, 49152\n"
"s_add_u32 s46, s96, 1376256\nMvav 176,156,56\nRa 136,0,18432\nv_and_b32 v191, 0xffff0000, v215\nMvav 176,152,48\nD1 1,16,46,0\nCV 162,168,169\nMvav 176,148,40\nRa 140,0,19456\n"
"CV 163,170,171\nMvav 176,144,32\nSv 160,1,16,5,0\nMvav 184,160,96\nRa 144,0,20480\nWV 12\nv_max_u32 v21, v219, v223\nv_cmp_eq_u32 vcc, -1, v21\ns_nop 0\ns_cbranch_vccnz .S93\n"
".S94:\nMvav 184,164,104\nv_lshlrev_b32 v192, 16, v216\nMvav 184,168,112\nRa 148,0,21504\nMvav 184,172,120\nWL 6\nv_and_b32 v193, 0xffff0000, v216\nMvav 188,172,124\n"
"Ra 152,0,22528\nMvav 188,168,116\nD1 1,16,46,1024\nMvav 188,164,108\nRa 156,0,23552\nv_lshlrev_b32 v194, 16, v217\nMvav 188,160,100\nv_and_b32 v195, 0xffff0000, v217\n"
"Mvav 188,176,36\nRa 160,0,24576\nv_lshlrev_b32 v196, 16, v220\nMvav 188,180,44\nv_and_b32 v197, 0xffff0000, v220\ns_add_u32 s4, s92, 393216\nMvav 188,184,52\nRa 164,0,25600\n"
"Mvav 188,188,60\nWL 6\nv_lshlrev_b32 v198, 16, v221\nbuffer_load_dwordx4 v[208:211], v1, s[16:19], s4 offen offset:0 sc0 sc1\nMvav 184,188,56\nRa 168,0,26624\nMvav 184,184,48\n"
"D1 1,16,46,2048\nMvav 184,180,40\nRa 172,0,27648\nv_and_b32 v199, 0xffff0000, v221\nMvav 184,176,32\nv_lshlrev_b32 v200, 16, v218\n"
"buffer_load_dwordx4 v[212:215], v1, s[16:19], s4 offen offset:1024 sc0 sc1\nMvav 192,128,96\nRa 176,0,28672\nv_and_b32 v201, 0xffff0000, v218\nMvav 192,132,104\n"
"v_lshlrev_b32 v202, 16, v219\nCV 180,180,181\nMvav 192,136,112\nRa 180,0,29696\nCV 181,182,183\nMvav 192,140,120\ns_waitcnt vmcnt(15) lgkmcnt(6)\nCV 182,188,189\nMvav 196,140,124\n"
"Ra 184,0,30720\nv_and_b32 v203, 0xffff0000, v219\nMvav 196,136,116\nD1 1,16,46,3072\nCV 183,190,191\nMvav 196,132,108\nRa 188,0,31744\ns_barrier\nMvav 196,128,100\n"
"v_lshlrev_b32 v204, 16, v222\ns_add_u32 s5, s94, 917504\nMvav 196,144,36\nRa 128,2,16384\nv_and_b32 v205, 0xffff0000, v222\nMvav 196,148,44\nv_lshlrev_b32 v206, 16, v223\n"
"Sv 180,1,16,5,1024\nMvav 196,152,52\nRa 132,2,17408\nCV 176,176,177\nMvav 196,156,60\nWL 6\ns_add_u32 m0, s89, 65536\ns_add_u32 s46, s96, 1409024\nMvav 192,156,56\nRa 136,2,18432\n"
"v_and_b32 v207, 0xffff0000, v223\nMvav 192,152,48\nD1 1,16,46,0\nCV 177,178,179\nMvav 192,148,40\nRa 140,2,19456\nCV 178,184,185\nMvav 192,144,32\nCV 179,186,187\nSv 176,1,16,5,0\n"
"Mvav 200,160,96\nRa 144,2,20480\nWV 12\nv_max_u32 v21, v227, v231\nv_cmp_eq_u32 vcc, -1, v21\ns_nop 0\ns_cbranch_vccnz .S96\n.S97:\nMvav 200,164,104\nv_lshlrev_b32 v160, 16, v224\n"
"Mvav 200,168,112\nRa 148,2,21504\nMvav 200,172,120\nWL 6\nv_and_b32 v161, 0xffff0000, v224\nMvav 204,172,124\nRa 152,2,22528\nMvav 204,168,116\nD1 1,16,46,1024\nMvav 204,164,108\n"
"Ra 156,2,23552\nv_lshlrev_b32 v162, 16, v225\nMvav 204,160,100\nv_and_b32 v163, 0xffff0000, v225\nMvav 204,176,36\nRa 160,2,24576\nv_lshlrev_b32 v164, 16, v228\nMvav 204,180,44\n"
"v_and_b32 v165, 0xffff0000, v228\ns_add_u32 s4, s92, 524288\nMvav 204,184,52\nRa 164,2,25600\nMvav 204,188,60\nWL 6\nv_lshlrev_b32 v166, 16, v229\n"
"buffer_load_dwordx4 v[216:219], v1, s[16:19], s4 offen offset:0 sc0 sc1\nMvav 200,188,56\nRa 168,2,26624\nMvav 200,184,48\nD1 1,16,46,2048\nMvav 200,180,40\nRa 172,2,27648\n"
"v_and_b32 v167, 0xffff0000, v229\nMvav 200,176,32\nv_lshlrev_b32 v168, 16, v226\nbuffer_load_dwordx4 v[220:223], v1, s[16:19], s4 offen offset:1024 sc0 sc1\nMvav 160,128,96\n"
"Ra 176,2,28672\nv_and_b32 v169, 0xffff0000, v226\nMvav 160,132,104\nv_lshlrev_b32 v170, 16, v227\nCV 196,196,197\nMvav 160,136,112\nRa 180,2,29696\nCV 197,198,199\n"
"Mvav 160,140,120\nWL 6\nCV 198,204,205\nMvav 164,140,124\nRa 184,2,30720\nv_and_b32 v24, 0x70, v1\nMvav 164,136,116\nD1 1,16,46,3072\nCV 199,206,207\nMvav 164,132,108\n"
"Ra 188,2,31744\nv_and_b32 v171, 0xffff0000, v227\nMvav 164,128,100\nv_lshlrev_b32 v172, 16, v230\ns_add_u32 s5, s94, 1048576\nMvav 164,144,36\nRa 128,2,0\n"
"v_lshlrev_b32 v24, 5, v24\nMvav 164,148,44\nv_and_b32 v173, 0xffff0000, v230\nSv 196,1,16,5,1024\nMvav 164,152,52\nRa 132,2,1024\nCV 192,192,193\nMvav 164,156,60\nWL 6\n"
"v_lshlrev_b32 v174, 16, v231\nMvav 160,156,56\nRa 136,2,2048\nbuffer_load_dword v27, v24, s[16:19], s41 offen sc0 sc1\nMvav 160,152,48\nv_and_b32 v175, 0xffff0000, v231\n"
"CV 193,194,195\nMvav 160,148,40\nRa 140,2,3072\nCV 194,200,201\nMvav 160,144,32\nCV 195,202,203\nSv 192,1,16,5,0\nMvav 168,160,96\nRa 144,2,4096\nWV 12\nv_max_u32 v21, v211, v215\n"
"v_cmp_eq_u32 vcc, -1, v21\ns_nop 0\ns_cbranch_vccnz .S99\n.S100:\nMvav 168,164,104\nv_lshlrev_b32 v176, 16, v208\nMvav 168,168,112\nRa 148,2,5120\nMvav 168,172,120\nWL 6\n"
"Mvav 172,172,124\nRa 152,2,6144\nv_and_b32 v177, 0xffff0000, v208\nMvav 172,168,116\nv_lshlrev_b32 v178, 16, v209\nMvav 172,164,108\nRa 156,2,7168\nMvav 172,160,100\n"
"v_and_b32 v179, 0xffff0000, v209\nMvav 172,176,36\nRa 160,2,8192\nv_lshlrev_b32 v180, 16, v212\nMvav 172,180,44\nv_and_b32 v181, 0xffff0000, v212\ns_add_u32 s4, s92, 655360\n"
"Mvav 172,184,52\nRa 164,2,9216\nMvav 172,188,60\nWL 6\nbuffer_load_dwordx4 v[224:227], v1, s[16:19], s4 offen offset:0 sc0 sc1\nMvav 168,188,56\nRa 168,2,10240\n"
"v_lshlrev_b32 v182, 16, v213\nMvav 168,184,48\nv_and_b32 v183, 0xffff0000, v213\nMvav 168,180,40\n.W546:\ns_cbranch_execz .WB21\nRa 172,2,11264\nMvav 168,176,32\n"
"v_lshlrev_b32 v184, 16, v210\nbuffer_load_dwordx4 v[228:231], v1, s[16:19], s4 offen offset:1024 sc0 sc1\nMvav 176,128,96\nRa 176,2,12288\nv_and_b32 v185, 0xffff0000, v210\n"
"Mvav 176,132,104\nv_lshlrev_b32 v186, 16, v211\nCV 164,164,165\nMvav 176,136,112\nRa 180,2,13312\nCV 165,166,167\nMvav 176,140,120\ns_waitcnt vmcnt(13) lgkmcnt(6)\nCV 166,172,173\n"
"Mvav 180,140,124\nRa 184,2,14336\nCV 167,174,175\nMvav 180,136,116\nv_and_b32 v187, 0xffff0000, v211\ns_add_u32 s5, s94, 1179648\nMvav 180,132,108\nRa 188,2,15360\ns_barrier\n"
"Mvav 180,128,100\nv_lshlrev_b32 v188, 16, v214\nSv 164,1,16,5,1024\nMvav 180,144,36\nRa 128,2,32768\nv_and_b32 v189, 0xffff0000, v214\nMvav 180,148,44\n"
"v_lshlrev_b32 v190, 16, v215\nCV 160,160,161\nMvav 180,152,52\nRa 132,2,33792\nCV 161,162,163\nMvav 180,156,60\nWL 6\nCV 162,168,169\nMvav 176,156,56\nRa 136,2,34816\n"
"CV 163,170,171\nMvav 176,152,48\nv_and_b32 v191, 0xffff0000, v215\nSv 160,1,16,5,0\nMvav 176,148,40\nRa 140,2,35840\nMvav 176,144,32\nMvav 184,160,96\nRa 144,2,36864\nWV 8\n"
"v_max_u32 v21, v219, v223\nv_cmp_eq_u32 vcc, -1, v21\ns_nop 0\ns_cbranch_vccnz .S102\n.S103:\nMvav 184,164,104\nv_lshlrev_b32 v192, 16, v216\nMvav 184,168,112\nRa 148,2,37888\n"
"Mvav 184,172,120\nWL 6\nMvav 188,172,124\nRa 152,2,38912\nv_and_b32 v193, 0xffff0000, v216\nMvav 188,168,116\nv_lshlrev_b32 v194, 16, v217\nMvav 188,164,108\nRa 156,2,39936\n"
"Mvav 188,160,100\nv_and_b32 v195, 0xffff0000, v217\nMvav 188,176,36\nRa 160,2,40960\nv_lshlrev_b32 v196, 16, v220\nMvav 188,180,44\nv_and_b32 v197, 0xffff0000, v220\n"
"s_add_u32 s4, s92, 786432\nMvav 188,184,52\nRa 164,2,41984\nMvav 188,188,60\nWL 6\nbuffer_load_dwordx4 v[208:211], v1, s[16:19], s4 offen offset:0 sc0 sc1\nMvav 184,188,56\n"
"Ra 168,2,43008\nv_lshlrev_b32 v198, 16, v221\nMvav 184,184,48\nv_and_b32 v199, 0xffff0000, v221\nMvav 184,180,40\nRa 172,2,44032\nMvav 184,176,32\nv_lshlrev_b32 v200, 16, v218\n"
"buffer_load_dwordx4 v[212:215], v1, s[16:19], s4 offen offset:1024 sc0 sc1\nMvav 192,128,96\nRa 176,2,45056\nv_and_b32 v201, 0xffff0000, v218\nMvav 192,132,104\n"
"v_lshlrev_b32 v202, 16, v219\nCV 180,180,181\nMvav 192,136,112\nRa 180,2,46080\nCV 181,182,183\nMvav 192,140,120\nWL 6\nCV 182,188,189\nMvav 196,140,124\nRa 184,2,47104\n"
"CV 183,190,191\nMvav 196,136,116\nv_and_b32 v203, 0xffff0000, v219\ns_add_u32 s5, s94, 1310720\nMvav 196,132,108\nRa 188,2,48128\nSv 180,1,16,5,1024\nMvav 196,128,100\n"
"v_lshlrev_b32 v204, 16, v222\nCV 176,176,177\nMvav 196,144,36\nRa 128,2,49152\nv_and_b32 v205, 0xffff0000, v222\nMvav 196,148,44\nv_lshlrev_b32 v206, 16, v223\nCV 177,178,179\n"
"Mvav 196,152,52\nRa 132,2,50176\nCV 178,184,185\nMvav 196,156,60\nWL 6\nCV 179,186,187\nMvav 192,156,56\nRa 136,2,51200\nSv 176,1,16,5,0\nMvav 192,152,48\n"
"v_and_b32 v207, 0xffff0000, v223\nMvav 192,148,40\nRa 140,2,52224\nMvav 192,144,32\nMvav 200,160,96\nRa 144,2,53248\nWV 6\nv_max_u32 v21, v227, v231\nv_cmp_eq_u32 vcc, -1, v21\n"
"s_nop 0\ns_cbranch_vccnz .S105\n.S106:\nMvav 200,164,104\nv_lshlrev_b32 v160, 16, v224\nv_xor_b32 v27, s15, v27\nv_and_b32 v26, 0xffffdfff, v27\nv_cmp_ne_u32 s[64:65], 0, v27\n"
"v_cmp_ne_u32 vcc, 0, v26\ns_nop 0\ns_cbranch_vccnz .S117\n.S118:\nMvav 200,168,112\nRa 148,2,54272\ns_bitcmp1_b32 s64, 0\ns_cselect_b32 s2, 0x2000, 0\nMvav 200,172,120\nWL 6\n"
"Mvav 204,172,124\nRa 152,2,55296\nv_and_b32 v161, 0xffff0000, v224\nMvav 204,168,116\nv_lshlrev_b32 v162, 16, v225\nMvav 204,164,108\nRa 156,2,56320\ns_xor_b32 s2, s2, s20\n"
"Mvav 204,160,100\nv_and_b32 v163, 0xffff0000, v225\nMvav 204,176,36\nRa 160,2,57344\nv_lshlrev_b32 v164, 16, v228\nMvav 204,180,44\nv_and_b32 v165, 0xffff0000, v228\n"
"Mvav 204,184,52\nRa 164,2,58368\nMvav 204,188,60\nWL 6\ns_add_u32 s24, s2, 0x0\nMvav 200,188,56\nRa 168,2,59392\nv_lshlrev_b32 v166, 16, v229\nMvav 200,184,48\n"
"v_and_b32 v167, 0xffff0000, v229\ns_bitcmp1_b32 s64, 1\ns_cselect_b32 s2, 0x2000, 0\nMvav 200,180,40\nRa 172,2,60416\nMvav 200,176,32\nv_lshlrev_b32 v168, 16, v226\n"
"Mvav 160,128,96\nRa 176,2,61440\nv_and_b32 v169, 0xffff0000, v226\nMvav 160,132,104\nv_lshlrev_b32 v170, 16, v227\nCV 196,196,197\nMvav 160,136,112\nRa 180,2,62464\n"
"CV 197,198,199\nMvav 160,140,120\nWL 6\nCV 198,204,205\nMvav 164,140,124\nRa 184,2,63488\nCV 199,206,207\nMvav 164,136,116\nv_and_b32 v171, 0xffff0000, v227\n"
"s_add_u32 s5, s94, 1441792\nMvav 164,132,108\nRa 188,2,64512\ns_barrier\nMvav 164,128,100\nv_lshlrev_b32 v172, 16, v230\nSv 196,1,16,5,1024\nMvav 164,144,36\nRa 128,3,0\n"
"v_and_b32 v173, 0xffff0000, v230\nMvav 164,148,44\nv_lshlrev_b32 v174, 16, v231\nCV 192,192,193\nMvav 164,152,52\nRa 132,3,1024\nCV 193,194,195\nMvav 164,156,60\nWL 6\n"
"s_xor_b32 s2, s2, s20\nMvav 160,156,56\nRa 136,3,2048\nCV 194,200,201\nMvav 160,152,48\nv_and_b32 v175, 0xffff0000, v231\nCV 195,202,203\nMvav 160,148,40\nRa 140,3,3072\n"
"Sv 192,1,16,5,0\nMvav 160,144,32\ns_add_u32 s25, s2, 0x1800000\ns_bitcmp1_b32 s64, 2\ns_cselect_b32 s2, 0x2000, 0\nMvav 168,160,96\nRa 144,3,4096\nWV 4\nv_max_u32 v21, v211, v215\n"
"v_cmp_eq_u32 vcc, -1, v21\ns_nop 0\ns_cbranch_vccnz .S108\n.S109:\nMvav 168,164,104\nv_lshlrev_b32 v176, 16, v208\nMvav 168,168,112\nRa 148,3,5120\nMvav 168,172,120\nWL 6\n"
"Mvav 172,172,124\nRa 152,3,6144\nv_and_b32 v177, 0xffff0000, v208\nMvav 172,168,116\nv_lshlrev_b32 v178, 16, v209\nMvav 172,164,108\nRa 156,3,7168\ns_xor_b32 s2, s2, s20\n"
"Mvav 172,160,100\nv_and_b32 v179, 0xffff0000, v209\nMvav 172,176,36\nRa 160,3,8192\nv_lshlrev_b32 v180, 16, v212\nMvav 172,180,44\nv_and_b32 v181, 0xffff0000, v212\n"
"Mvav 172,184,52\nRa 164,3,9216\nMvav 172,188,60\nWL 6\ns_add_u32 s26, s2, 0x3000000\nMvav 168,188,56\nRa 168,3,10240\nv_lshlrev_b32 v182, 16, v213\nMvav 168,184,48\n"
"v_and_b32 v183, 0xffff0000, v213\ns_bitcmp1_b32 s64, 3\ns_cselect_b32 s2, 0x2000, 0\nMvav 168,180,40\nRa 172,3,11264\nMvav 168,176,32\nv_lshlrev_b32 v184, 16, v210\n"
"Mvav 176,128,96\nRa 176,3,12288\ns_xor_b32 s2, s2, s20\nMvav 176,132,104\nv_and_b32 v185, 0xffff0000, v210\nCV 164,164,165\nMvav 176,136,112\nRa 180,3,13312\nCV 165,166,167\n"
"Mvav 176,140,120\nWL 6\nCV 166,172,173\nMvav 180,140,124\nRa 184,3,14336\nCV 167,174,175\nMvav 180,136,116\nv_lshlrev_b32 v186, 16, v211\ns_add_u32 s5, s94, 1572864\n"
"Mvav 180,132,108\nRa 188,3,15360\ns_add_u32 s27, s2, 0x4800000\nMvav 180,128,100\nv_and_b32 v187, 0xffff0000, v211\ns_bitcmp1_b32 s64, 4\ns_cselect_b32 s2, 0x2000, 0\n"
"Mvav 180,144,36\nv_lshlrev_b32 v188, 16, v214\nSv 164,1,16,5,1024\nMvav 180,148,44\nv_and_b32 v189, 0xffff0000, v214\nCV 160,160,161\nMvav 180,152,52\n"
"v_lshlrev_b32 v190, 16, v215\nCV 161,162,163\nMvav 180,156,60\nWL 4\ns_xor_b32 s2, s2, s20\nMvav 176,156,56\nv_and_b32 v191, 0xffff0000, v215\nCV 162,168,169\nMvav 176,152,48\n"
"s_add_u32 s32, s2, 0x6000000\ns_bitcmp1_b32 s64, 5\ns_cselect_b32 s2, 0x2000, 0\nMvav 176,148,40\ns_xor_b32 s2, s2, s20\nCV 163,170,171\nMvav 176,144,32\n"
"s_add_u32 s33, s2, 0x7800000\ns_bitcmp1_b32 s64, 6\ns_cselect_b32 s2, 0x2000, 0\nMvav 184,160,96\ns_xor_b32 s2, s2, s20\ns_add_u32 s34, s2, 0x9000000\nMvav 184,164,104\n"
"s_bitcmp1_b32 s64, 7\ns_cselect_b32 s2, 0x2000, 0\nMvav 184,168,112\ns_xor_b32 s2, s2, s20\nSv 160,1,16,5,0\nMvav 184,172,120\nWL 0\nMvav 188,172,124\n"
"s_add_u32 s35, s2, 0xa800000\nMvav 188,168,116\nMvav 188,164,108\nMvav 188,160,100\nMvav 188,176,36\nMvav 188,180,44\nMvav 188,184,52\nMvav 188,188,60\nMvav 184,188,56\n"
"Mvav 184,184,48\nMvav 184,180,40\nMvav 184,176,32\nCV 180,180,181\nCV 181,182,183\ns_nop 1\nCV 182,188,189\nCV 183,190,191\ns_add_u32 s5, s94, 1703936\nSv 180,1,16,5,1024\n"
"CV 176,176,177\nCV 177,178,179\nCV 178,184,185\nCV 179,186,187\nSv 176,1,16,5,0\ns_cmp_eq_u32 s13, 0\ns_cbranch_scc1 .Z120\n"
"buffer_load_dwordx4 v[12:15], v91, s[28:31], s22 offen offset:0\nbuffer_load_dwordx4 v[16:19], v91, s[28:31], s23 offen offset:0\n"
"buffer_load_dwordx4 v[64:67], v89, s[16:19], s40 offen offset:0 sc0 sc1\nbuffer_load_dwordx4 v[68:71], v89, s[16:19], s40 offen offset:16 sc0 sc1\n"
"buffer_load_dwordx4 v[72:75], v89, s[16:19], s40 offen offset:1792 sc0 sc1\nbuffer_load_dwordx4 v[76:79], v89, s[16:19], s40 offen offset:1808 sc0 sc1\n"
"buffer_load_dwordx2 v[80:81], v88, s[16:19], s63 offen offset:0 sc0 sc1\nbuffer_load_dwordx2 v[82:83], v88, s[16:19], s63 offen offset:512 sc0 sc1\n"
"buffer_load_dwordx4 v[96:99], v1, s[16:19], s24 offen offset:0 sc0 sc1\nbuffer_load_dwordx4 v[100:103], v1, s[16:19], s24 offen offset:1024 sc0 sc1\n"
"buffer_load_dwordx4 v[104:107], v1, s[16:19], s25 offen offset:0 sc0 sc1\nbuffer_load_dwordx4 v[108:111], v1, s[16:19], s25 offen offset:1024 sc0 sc1\n"
"buffer_load_dwordx4 v[112:115], v1, s[16:19], s26 offen offset:0 sc0 sc1\nbuffer_load_dwordx4 v[116:119], v1, s[16:19], s26 offen offset:1024 sc0 sc1\n"
"buffer_load_dwordx4 v[120:123], v1, s[16:19], s27 offen offset:0 sc0 sc1\nbuffer_load_dwordx4 v[124:127], v1, s[16:19], s27 offen offset:1024 sc0 sc1\n"
"buffer_load_dwordx4 v[32:35], v1, s[16:19], s32 offen offset:0 sc0 sc1\nbuffer_load_dwordx4 v[36:39], v1, s[16:19], s32 offen offset:1024 sc0 sc1\n"
"buffer_load_dwordx4 v[40:43], v1, s[16:19], s33 offen offset:0 sc0 sc1\nbuffer_load_dwordx4 v[44:47], v1, s[16:19], s33 offen offset:1024 sc0 sc1\n"
"buffer_load_dwordx4 v[48:51], v1, s[16:19], s34 offen offset:0 sc0 sc1\nbuffer_load_dwordx4 v[52:55], v1, s[16:19], s34 offen offset:1024 sc0 sc1\n"
"buffer_load_dwordx4 v[56:59], v1, s[16:19], s35 offen offset:0 sc0 sc1\nbuffer_load_dwordx4 v[60:63], v1, s[16:19], s35 offen offset:1024 sc0 sc1\nWV 16\nv_xor_b32 v66, s15, v66\n"
"v_xor_b32 v70, s15, v70\nv_xor_b32 v74, s15, v74\nv_xor_b32 v78, s15, v78\nv_xor_b32 v81, s15, v81\nv_xor_b32 v83, s15, v83\nv_or3_b32 v92, v66, v70, v74\n"
"v_or3_b32 v92, v92, v78, v81\nv_or_b32 v92, v92, v83\nv_cmp_ne_u32 vcc, 0, v92\ns_nop 0\ns_cbranch_vccnz .S111\n.S112:\nv_and_b32 v92, 0x7ff, v67\nv_lshrrev_b32 v93, 16, v67\n"
"v_cmp_eq_u32 s[44:45], v92, v90\nv_cmp_eq_u32 s[46:47], v93, v90\nv_cmp_eq_u32 s[48:49], v92, v94\nv_cmp_eq_u32 s[50:51], v93, v94\nv_cndmask_b32 v12, v12, v64, s[44:45]\n"
"v_cndmask_b32 v13, v13, v65, s[44:45]\nv_cndmask_b32 v14, v14, v68, s[44:45]\nv_cndmask_b32 v15, v15, v69, s[44:45]\nv_cndmask_b32 v12, v12, v72, s[46:47]\n"
"v_cndmask_b32 v13, v13, v73, s[46:47]\nv_cndmask_b32 v14, v14, v76, s[46:47]\nv_cndmask_b32 v15, v15, v77, s[46:47]\nv_cndmask_b32 v16, v16, v64, s[48:49]\n"
"v_cndmask_b32 v17, v17, v65, s[48:49]\nv_cndmask_b32 v18, v18, v68, s[48:49]\nv_cndmask_b32 v19, v19, v69, s[48:49]\nv_cndmask_b32 v16, v16, v72, s[50:51]\n"
"v_cndmask_b32 v17, v17, v73, s[50:51]\nv_cndmask_b32 v18, v18, v76, s[50:51]\nv_cndmask_b32 v19, v19, v77, s[50:51]\nv_sub_f32 v92, 1.0, v80\nv_sub_f32 v93, 1.0, v82\nCV 92,92,93\n"
"v_and_b32 v93, 0xffff0000, v92\nv_lshlrev_b32 v92, 16, v92\nv_lshlrev_b32 v64, 16, v12\nv_and_b32 v65, 0xffff0000, v12\nMU 64,64,92\nMU 65,65,92\nv_lshlrev_b32 v66, 16, v13\n"
"v_and_b32 v67, 0xffff0000, v13\nMU 66,66,92\nMU 67,67,92\nv_lshlrev_b32 v68, 16, v14\nv_and_b32 v69, 0xffff0000, v14\nMU 68,68,92\nMU 69,69,92\nv_lshlrev_b32 v70, 16, v15\n"
"v_and_b32 v71, 0xffff0000, v15\nMU 70,70,92\nMU 71,71,92\nv_lshlrev_b32 v72, 16, v16\nv_and_b32 v73, 0xffff0000, v16\nMU 72,72,93\nMU 73,73,93\nv_lshlrev_b32 v74, 16, v17\n"
"v_and_b32 v75, 0xffff0000, v17\nMU 74,74,93\nMU 75,75,93\nv_lshlrev_b32 v76, 16, v18\nv_and_b32 v77, 0xffff0000, v18\nMU 76,76,93\nMU 77,77,93\nv_lshlrev_b32 v78, 16, v19\n"
"v_and_b32 v79, 0xffff0000, v19\nMU 78,78,93\nMU 79,79,93\ns_mov_b32 s7, 262144\nWV 0\n.Z123:\nv_max_u32 v84, v99, v103\nv_cmp_eq_u32 s[44:45], -1, v84\nv_max_u32 v84, v107, v111\n"
"v_cmp_eq_u32 s[46:47], -1, v84\nv_max_u32 v84, v115, v119\nv_cmp_eq_u32 s[48:49], -1, v84\nv_max_u32 v84, v123, v127\nv_cmp_eq_u32 s[50:51], -1, v84\nv_max_u32 v84, v35, v39\n"
"v_cmp_eq_u32 s[52:53], -1, v84\nv_max_u32 v84, v43, v47\nv_cmp_eq_u32 s[54:55], -1, v84\nv_max_u32 v84, v51, v55\nv_cmp_eq_u32 s[56:57], -1, v84\nv_max_u32 v84, v59, v63\n"
"v_cmp_eq_u32 s[58:59], -1, v84\ns_or_b64 s[0:1], s[44:45], s[46:47]\ns_or_b64 s[2:3], s[48:49], s[50:51]\ns_or_b64 s[0:1], s[0:1], s[52:53]\ns_or_b64 s[2:3], s[2:3], s[54:55]\n"
"s_or_b64 s[0:1], s[0:1], s[56:57]\ns_or_b64 s[2:3], s[2:3], s[58:59]\ns_or_b64 s[0:1], s[0:1], s[2:3]\ns_cbranch_scc1 .Z121\n.Z122:\nZvvv 136,6,96\nMvvv 136,6,104\nMvvv 136,6,112\n"
"Mvvv 136,6,120\nMvvv 136,6,32\nMvvv 136,6,40\nMvvv 136,6,48\nMvvv 136,6,56\nZvvv 140,4,96\nMvvv 140,4,104\nMvvv 140,4,112\nMvvv 140,4,120\nMvvv 140,4,32\nMvvv 140,4,40\n"
"Mvvv 140,4,48\nMvvv 140,4,56\nZvvv 144,6,100\nMvvv 144,6,108\nMvvv 144,6,116\nMvvv 144,6,124\nMvvv 144,6,36\nMvvv 144,6,44\nMvvv 144,6,52\nMvvv 144,6,60\nZvvv 148,4,100\n"
"Mvvv 148,4,108\nMvvv 148,4,116\nMvvv 148,4,124\nMvvv 148,4,36\nMvvv 148,4,44\nMvvv 148,4,52\nMvvv 148,4,60\nFM 64,80,136\nFM 65,80,137\nFM 66,80,138\nFM 67,80,139\nFM 68,80,140\n"
"FM 69,80,141\nFM 70,80,142\nFM 71,80,143\nCV 12,64,65\nCV 13,66,67\nCV 14,68,69\nCV 15,70,71\nSv 12,91,36,22,0\nFM 72,82,144\nFM 73,82,145\nFM 74,82,146\nFM 75,82,147\n"
"FM 76,82,148\nFM 77,82,149\nFM 78,82,150\nFM 79,82,151\nCV 16,72,73\nCV 17,74,75\nCV 18,76,77\nCV 19,78,79\nSv 16,91,36,23,0\n.Z120:\ns_endpgm\n.EP132:\n.S14:\n"
"s_mov_b32 s7, 262144\n.S16:\nbuffer_load_dwordx4 v[32:35], v1, s[16:19], s6 offen offset:0 sc0 sc1\nbuffer_load_dwordx4 v[36:39], v1, s[16:19], s6 offen offset:1024 sc0 sc1\n"
"buffer_load_dwordx4 v[40:43], v1, s[16:19], s6 offen offset:2048 sc0 sc1\nbuffer_load_dwordx4 v[44:47], v1, s[16:19], s6 offen offset:3072 sc0 sc1\nWV 0\nv_xor_b32 v48, s15, v33\n"
"v_xor_b32 v49, s15, v35\nv_xor_b32 v50, s15, v37\nv_xor_b32 v51, s15, v39\nv_xor_b32 v52, s15, v41\nv_xor_b32 v53, s15, v43\nv_xor_b32 v54, s15, v45\nv_xor_b32 v55, s15, v47\n"
"v_or3_b32 v48, v48, v49, v50\nv_or3_b32 v51, v51, v52, v53\nv_or3_b32 v48, v48, v54, v55\nv_or_b32 v48, v48, v51\nv_cmp_ne_u32 vcc, 0, v48\ns_nop 0\ns_cbranch_vccz .S15\n"
"s_sleep 6\ns_sub_u32 s7, s7, 1\ns_cbranch_scc0 .S16\ns_branch .S15\n.S17:\ns_mov_b32 s7, 262144\n.S19:\ns_lshl_b32 s5, s73, 9\ns_add_u32 s5, s5, 0xc049400\n"
"v_lshrrev_b32 v27, 1, v1\nbuffer_load_dwordx2 v[22:23], v27, s[16:19], s5 offen sc0 sc1\nWV 0\nv_cmp_ne_u32 vcc, s15, v23\ns_nop 0\ns_cbranch_vccz .S18\ns_sleep 6\n"
"s_sub_u32 s7, s7, 1\ns_cbranch_scc0 .S19\ns_branch .S18\n.T23:\ns_mov_b32 s62, 1\ns_call_b64 s[80:81], .RA10\ns_branch .T24\n.T25:\ns_mov_b32 s62, 1\ns_call_b64 s[80:81], .RA10\n"
"s_branch .T26\n.T28:\ns_mov_b32 s62, 0\ns_call_b64 s[80:81], .RB11\ns_branch .T29\n.T30:\ns_mov_b32 s62, 0\ns_call_b64 s[80:81], .RA10\ns_branch .T31\n.T33:\ns_mov_b32 s62, 0\n"
"s_call_b64 s[80:81], .RB11\ns_branch .T34\n.T35:\ns_mov_b32 s62, 0\ns_call_b64 s[80:81], .RA10\ns_branch .T36\n.S38:\ns_mov_b32 s7, 262144\n.S40:\nv_and_b32 v27, 0x70, v1\n"
"buffer_load_dword v27, v27, s[16:19], s61 offen offset:128 sc0 sc1\nWV 0\nv_cmp_ne_u32 vcc, s15, v27\ns_nop 0\ns_cbranch_vccz .S39\ns_sleep 6\ns_sub_u32 s7, s7, 1\n"
"s_cbranch_scc0 .S40\ns_branch .S39\n.S41:\ns_mov_b32 s7, 262144\n.S43:\nv_and_b32 v27, 0x70, v1\nbuffer_load_dword v27, v27, s[16:19], s61 offen offset:136 sc0 sc1\nWV 0\n"
"v_cmp_ne_u32 vcc, s15, v27\ns_nop 0\ns_cbranch_vccz .S42\ns_sleep 6\ns_sub_u32 s7, s7, 1\ns_cbranch_scc0 .S43\ns_branch .S42\n.S44:\ns_mov_b32 s7, 262144\n.S46:\n"
"v_and_b32 v27, 0x70, v1\nbuffer_load_dword v27, v27, s[16:19], s61 offen offset:140 sc0 sc1\nWV 0\nv_cmp_ne_u32 vcc, s15, v27\ns_nop 0\ns_cbranch_vccz .S45\ns_sleep 6\n"
"s_sub_u32 s7, s7, 1\ns_cbranch_scc0 .S46\ns_branch .S45\n.S47:\ns_mov_b32 s7, 262144\n.S49:\nv_and_b32 v27, 0x70, v1\n"
"buffer_load_dword v27, v27, s[16:19], s61 offen offset:12 sc0 sc1\nWV 0\nv_cmp_ne_u32 vcc, s15, v27\ns_nop 0\ns_cbranch_vccz .S48\ns_sleep 6\ns_sub_u32 s7, s7, 1\n"
"s_cbranch_scc0 .S49\ns_branch .S48\n.S50:\ns_mov_b32 s7, 262144\n.S52:\nv_and_b32 v27, 0x70, v1\nbuffer_load_dword v27, v27, s[16:19], s61 offen offset:8 sc0 sc1\nWV 0\n"
"v_cmp_ne_u32 vcc, s15, v27\ns_nop 0\ns_cbranch_vccz .S51\ns_sleep 6\ns_sub_u32 s7, s7, 1\ns_cbranch_scc0 .S52\ns_branch .S51\n.S53:\ns_mov_b32 s7, 262144\n.S55:\n"
"v_and_b32 v27, 0xf0, v1\nbuffer_load_dword v27, v27, s[16:19], s61 offen offset:4 sc0 sc1\nWV 0\nv_cmp_ne_u32 vcc, s15, v27\ns_nop 0\ns_cbranch_vccz .S54\ns_sleep 6\n"
"s_sub_u32 s7, s7, 1\ns_cbranch_scc0 .S55\ns_branch .S54\n.S57:\ns_mov_b32 s7, 262144\n.S59:\nv_lshrrev_b32 v104, 9, v1\nv_mul_u32_u24 v104, 0x18000, v104\n"
"v_lshlrev_b32 v104, 8, v104\nv_and_b32 v105, 0x1f0, v1\nv_add_u32 v104, v104, v105\ns_mov_b32 s6, 0x15c0000\nbuffer_load_dword v96, v104, s[16:19], s6 offen offset:4 sc0 sc1\n"
"s_mov_b32 s6, 0x15c2000\nbuffer_load_dword v97, v104, s[16:19], s6 offen offset:4 sc0 sc1\ns_mov_b32 s6, 0x45c0000\n"
"buffer_load_dword v98, v104, s[16:19], s6 offen offset:4 sc0 sc1\ns_mov_b32 s6, 0x45c2000\nbuffer_load_dword v99, v104, s[16:19], s6 offen offset:4 sc0 sc1\n"
"s_mov_b32 s6, 0x75c0000\nbuffer_load_dword v100, v104, s[16:19], s6 offen offset:4 sc0 sc1\ns_mov_b32 s6, 0x75c2000\n"
"buffer_load_dword v101, v104, s[16:19], s6 offen offset:4 sc0 sc1\ns_mov_b32 s6, 0xa5c0000\nbuffer_load_dword v102, v104, s[16:19], s6 offen offset:4 sc0 sc1\n"
"s_mov_b32 s6, 0xa5c2000\nbuffer_load_dword v103, v104, s[16:19], s6 offen offset:4 sc0 sc1\nWV 0\nv_xor_b32 v96, s15, v96\nv_xor_b32 v97, s15, v97\nv_xor_b32 v98, s15, v98\n"
"v_xor_b32 v99, s15, v99\nv_xor_b32 v100, s15, v100\nv_xor_b32 v101, s15, v101\nv_xor_b32 v102, s15, v102\nv_xor_b32 v103, s15, v103\nv_min_u32 v96, v96, v97\n"
"v_min_u32 v98, v98, v99\nv_min_u32 v100, v100, v101\nv_min_u32 v102, v102, v103\nv_or3_b32 v96, v96, v98, v100\nv_or_b32 v96, v96, v102\nv_cmp_ne_u32 vcc, 0, v96\ns_nop 0\n"
"s_cbranch_vccz .S58\ns_sleep 6\ns_sub_u32 s7, s7, 1\ns_cbranch_scc0 .S59\ns_branch .S58\n.FF56:\nv_lshrrev_b32 v104, 9, v1\nv_mul_u32_u24 v104, 0x18000, v104\n"
"v_lshlrev_b32 v104, 8, v104\nv_and_b32 v105, 0x1f0, v1\nv_add_u32 v104, v104, v105\ns_mov_b32 s6, 0x15c0000\nbuffer_load_dword v96, v104, s[16:19], s6 offen offset:4 sc0 sc1\n"
"s_mov_b32 s6, 0x15c2000\nbuffer_load_dword v97, v104, s[16:19], s6 offen offset:4 sc0 sc1\ns_mov_b32 s6, 0x45c0000\n"
"buffer_load_dword v98, v104, s[16:19], s6 offen offset:4 sc0 sc1\ns_mov_b32 s6, 0x45c2000\nbuffer_load_dword v99, v104, s[16:19], s6 offen offset:4 sc0 sc1\n"
"s_mov_b32 s6, 0x75c0000\nbuffer_load_dword v100, v104, s[16:19], s6 offen offset:4 sc0 sc1\ns_mov_b32 s6, 0x75c2000\n"
"buffer_load_dword v101, v104, s[16:19], s6 offen offset:4 sc0 sc1\ns_mov_b32 s6, 0xa5c0000\nbuffer_load_dword v102, v104, s[16:19], s6 offen offset:4 sc0 sc1\n"
"s_mov_b32 s6, 0xa5c2000\nbuffer_load_dword v103, v104, s[16:19], s6 offen offset:4 sc0 sc1\ns_mov_b32 s90, 0\ns_swappc_b64 s[8:9], s[80:81]\nv_xor_b32 v96, s15, v96\n"
"v_xor_b32 v97, s15, v97\nv_xor_b32 v98, s15, v98\nv_xor_b32 v99, s15, v99\nv_xor_b32 v100, s15, v100\nv_xor_b32 v101, s15, v101\nv_xor_b32 v102, s15, v102\n"
"v_xor_b32 v103, s15, v103\nv_min_u32 v96, v96, v97\nv_min_u32 v98, v98, v99\nv_min_u32 v100, v100, v101\nv_min_u32 v102, v102, v103\nv_or3_b32 v96, v96, v98, v100\n"
"v_or_b32 v96, v96, v102\nv_cmp_ne_u32 vcc, 0, v96\ns_nop 0\ns_cbranch_vccnz .S57\n.S58:\ns_and_b32 s4, s13, 1\ns_lshl1_add_u32 s4, s4, s11\ns_lshl2_add_u32 s4, s12, s4\n"
"s_lshr_b32 s5, s71, 16\ns_cmp_lt_u32 s4, 28\ns_cselect_b32 s5, s71, s5\ns_cselect_b32 s3, 0, 28\ns_and_b32 s5, s5, 0x7ff\ns_sub_u32 s3, s4, s3\ns_mul_i32 s2, s4, 7168\n"
"s_add_u32 s2, s2, 0xc04c800\ns_add_u32 s6, s2, 4096\nbuffer_load_dwordx4 v[96:99], v1, s[16:19], s2 offen offset:0 sc0 sc1\n"
"buffer_load_dwordx4 v[100:103], v1, s[16:19], s2 offen offset:1024 sc0 sc1\nbuffer_load_dwordx4 v[104:107], v1, s[16:19], s2 offen offset:2048 sc0 sc1\n"
"buffer_load_dwordx4 v[108:111], v1, s[16:19], s2 offen offset:3072 sc0 sc1\nbuffer_load_dwordx4 v[112:115], v1, s[16:19], s6 offen offset:0 sc0 sc1\n"
"buffer_load_dwordx4 v[116:119], v1, s[16:19], s6 offen offset:1024 sc0 sc1\nbuffer_load_dwordx4 v[120:123], v1, s[16:19], s6 offen offset:2048 sc0 sc1\nv_lshrrev_b32 v127, 1, v1\n"
"v_and_b32 v127, 24, v127\ns_mul_i32 s2, s5, 896\ns_lshl_b32 s3, s3, 5\ns_add_u32 s2, s2, s3\ns_lshl2_add_u32 s5, s5, 0xc03e000\n"
"buffer_load_dwordx2 v[124:125], v127, s[28:31], s2 offen\nbuffer_load_dword v126, off, s[16:19], s5 sc0 sc1\ns_mov_b32 s90, 0\ns_swappc_b64 s[8:9], s[80:81]\nAD 96,96,100\n"
"AD 97,97,101\nAD 98,98,102\nAD 99,99,103\nAD 104,104,108\nAD 105,105,109\nAD 106,106,110\nAD 107,107,111\nAD 112,112,116\nAD 113,113,117\nAD 114,114,118\nAD 115,115,119\n"
"AD 96,96,104\nAD 97,97,105\nAD 98,98,106\nAD 99,99,107\nAD 112,112,120\nAD 113,113,121\nAD 114,114,122\nAD 115,115,123\nAD 96,96,112\nAD 97,97,113\nAD 98,98,114\nAD 99,99,115\n"
"v_mov_b32 v100, v96\nv_mov_b32 v101, v97\nv_mov_b32 v102, v98\nv_mov_b32 v103, v99\nv_permlane32_swap_b32 v96, v100\nv_permlane32_swap_b32 v97, v101\n"
"v_permlane32_swap_b32 v98, v102\nv_permlane32_swap_b32 v99, v103\nAD 96,96,100\nAD 97,97,101\nAD 98,98,102\nAD 99,99,103\nv_mov_b32 v100, v96\nv_mov_b32 v101, v97\n"
"v_mov_b32 v102, v98\nv_mov_b32 v103, v99\nv_permlane16_swap_b32 v96, v100\nv_permlane16_swap_b32 v97, v101\nv_permlane16_swap_b32 v98, v102\nv_permlane16_swap_b32 v99, v103\n"
"AD 96,96,100\nAD 97,97,101\nAD 98,98,102\nAD 99,99,103\nv_add_f32_dpp v96, v96, v96 row_ror:8 row_mask:0xf bank_mask:0xf\n"
"v_add_f32_dpp v97, v97, v97 row_ror:8 row_mask:0xf bank_mask:0xf\nv_add_f32_dpp v98, v98, v98 row_ror:8 row_mask:0xf bank_mask:0xf\n"
"v_add_f32_dpp v99, v99, v99 row_ror:8 row_mask:0xf bank_mask:0xf\nv_add_f32_dpp v96, v96, v96 row_ror:4 row_mask:0xf bank_mask:0xf\n"
"v_add_f32_dpp v97, v97, v97 row_ror:4 row_mask:0xf bank_mask:0xf\nv_add_f32_dpp v98, v98, v98 row_ror:4 row_mask:0xf bank_mask:0xf\n"
"v_add_f32_dpp v99, v99, v99 row_ror:4 row_mask:0xf bank_mask:0xf\nv_mul_f32 v126, 0xbfb8aa3b, v126\nEX 126,126\nCV 96,96,97\nv_add_f32 v126, 1.0, v126\nv_rcp_f32 v126, v126\n"
"CV 98,98,99\nCV 126,126,126\nv_lshlrev_b32 v126, 16, v126\nv_and_b32 v97, 0xffff0000, v96\nv_lshlrev_b32 v96, 16, v96\nv_and_b32 v99, 0xffff0000, v98\nv_lshlrev_b32 v98, 16, v98\n"
"MU 96,96,126\nMU 97,97,126\nMU 98,98,126\nMU 99,99,126\nCV 96,96,97\nCV 98,98,99\nv_and_b32 v97, 0xffff0000, v96\nv_lshlrev_b32 v96, 16, v96\nv_and_b32 v99, 0xffff0000, v98\n"
"v_lshlrev_b32 v98, 16, v98\nv_lshlrev_b32 v100, 16, v124\nv_and_b32 v101, 0xffff0000, v124\nv_lshlrev_b32 v102, 16, v125\nv_and_b32 v103, 0xffff0000, v125\nAD 96,96,100\n"
"AD 97,97,101\nAD 98,98,102\nAD 99,99,103\nCV 96,96,97\nCV 97,98,99\nv_mov_b32 v98, s15\nv_mov_b32 v99, s71\ns_and_b32 s4, s13, 1\ns_lshl1_add_u32 s4, s4, s11\n"
"s_lshl2_add_u32 s4, s12, s4\ns_lshr_b32 s5, s71, 16\ns_cmp_lt_u32 s4, 28\ns_cselect_b32 s5, s71, s5\ns_cselect_b32 s3, 0, 28\ns_and_b32 s5, s5, 0x7ff\ns_sub_u32 s3, s4, s3\n"
"v_mov_b32 v100, 0x40000000\nv_cmp_gt_u32 vcc, 64, v1\ns_lshl_b32 s4, s4, 6\ns_add_u32 s4, s4, 0xc04b800\nv_cndmask_b32 v100, v100, v1, vcc\nSv 96,100,16,4,0\ns_mov_b32 s90, 100\n"
"s_swappc_b64 s[8:9], s[80:81]\n.T64:\ns_mov_b32 s62, 0\ns_call_b64 s[80:81], .RB11\ns_branch .T65\n.T66:\ns_mov_b32 s62, 0\ns_call_b64 s[80:81], .RA10\ns_branch .T67\n.T70:\n"
"s_mov_b32 s62, 0\ns_call_b64 s[80:81], .RB11\ns_branch .T71\n.T72:\ns_mov_b32 s62, 0\ns_call_b64 s[80:81], .RA10\ns_branch .T73\n.T75:\ns_mov_b32 s62, 0\n"
"s_call_b64 s[80:81], .RB11\ns_branch .T76\n.T77:\ns_mov_b32 s62, 0\ns_call_b64 s[80:81], .RA10\ns_branch .T78\n.S83:\ns_mov_b32 s7, 262144\n.S85:\n"
"buffer_load_dwordx4 v[100:103], v108, s[16:19], s65 offen offset:0 sc0 sc1\nbuffer_load_dwordx4 v[104:107], v108, s[16:19], s65 offen offset:256 sc0 sc1\nWV 0\n"
"v_xor_b32 v109, s15, v102\nv_xor_b32 v110, s15, v106\nv_or_b32 v109, v109, v110\nv_cmp_ne_u32 vcc, 0, v109\ns_nop 0\ns_cbranch_vccz .S84\ns_sleep 6\ns_sub_u32 s7, s7, 1\n"
"s_cbranch_scc0 .S85\ns_branch .S84\n.D81:\nv_mov_b32 v114, 0\nv_mov_b32 v115, 0\ns_mov_b32 s63, 0xff800000\ns_mov_b32 s67, 1\ns_branch .D82\n.T87:\ns_lshl3_add_u32 s62, s67, 2\n"
"s_call_b64 s[80:81], .RB11\ns_cmp_eq_u32 s67, 0\ns_cbranch_scc1 .T88\nSU 20,100,88\nv_min_f32 v20, 0, v20\nEX 20,20\ns_nop 0\nFM 86,101,20\nSU 21,104,89\nv_min_f32 v21, 0, v21\n"
"EX 21,21\ns_nop 0\nFM 87,105,21\nAR 22,0\nv_and_b32 v23, 0xffff0000, v22\nv_lshlrev_b32 v22, 16, v22\nMU 22,22,20\nMU 23,23,20\nCV 22,22,23\nAW 0,22\nAR 22,1\n"
"v_and_b32 v23, 0xffff0000, v22\nv_lshlrev_b32 v22, 16, v22\nMU 22,22,20\nMU 23,23,20\nCV 22,22,23\nAW 1,22\nAR 22,2\nv_and_b32 v23, 0xffff0000, v22\nv_lshlrev_b32 v22, 16, v22\n"
"MU 22,22,20\nMU 23,23,20\nCV 22,22,23\nAW 2,22\nAR 22,3\nv_and_b32 v23, 0xffff0000, v22\nv_lshlrev_b32 v22, 16, v22\nMU 22,22,20\nMU 23,23,20\nCV 22,22,23\nAW 3,22\nAR 22,4\n"
"v_and_b32 v23, 0xffff0000, v22\nv_lshlrev_b32 v22, 16, v22\nMU 22,22,21\nMU 23,23,21\nCV 22,22,23\nAW 4,22\nAR 22,5\nv_and_b32 v23, 0xffff0000, v22\nv_lshlrev_b32 v22, 16, v22\n"
"MU 22,22,21\nMU 23,23,21\nCV 22,22,23\nAW 5,22\nAR 22,6\nv_and_b32 v23, 0xffff0000, v22\nv_lshlrev_b32 v22, 16, v22\nMU 22,22,21\nMU 23,23,21\nCV 22,22,23\nAW 6,22\nAR 22,7\n"
"v_and_b32 v23, 0xffff0000, v22\nv_lshlrev_b32 v22, 16, v22\nMU 22,22,21\nMU 23,23,21\nCV 22,22,23\nAW 7,22\nAR 22,8\nv_and_b32 v23, 0xffff0000, v22\nv_lshlrev_b32 v22, 16, v22\n"
"MU 22,22,20\nMU 23,23,20\nCV 22,22,23\nAW 8,22\nAR 22,9\nv_and_b32 v23, 0xffff0000, v22\nv_lshlrev_b32 v22, 16, v22\nMU 22,22,20\nMU 23,23,20\nCV 22,22,23\nAW 9,22\nAR 22,10\n"
"v_and_b32 v23, 0xffff0000, v22\nv_lshlrev_b32 v22, 16, v22\nMU 22,22,20\nMU 23,23,20\nCV 22,22,23\nAW 10,22\nAR 22,11\nv_and_b32 v23, 0xffff0000, v22\nv_lshlrev_b32 v22, 16, v22\n"
"MU 22,22,20\nMU 23,23,20\nCV 22,22,23\nAW 11,22\nAR 22,12\nv_and_b32 v23, 0xffff0000, v22\nv_lshlrev_b32 v22, 16, v22\nMU 22,22,21\nMU 23,23,21\nCV 22,22,23\nAW 12,22\nAR 22,13\n"
"v_and_b32 v23, 0xffff0000, v22\nv_lshlrev_b32 v22, 16, v22\nMU 22,22,21\nMU 23,23,21\nCV 22,22,23\nAW 13,22\nAR 22,14\nv_and_b32 v23, 0xffff0000, v22\nv_lshlrev_b32 v22, 16, v22\n"
"MU 22,22,21\nMU 23,23,21\nCV 22,22,23\nAW 14,22\nAR 22,15\nv_and_b32 v23, 0xffff0000, v22\nv_lshlrev_b32 v22, 16, v22\nMU 22,22,21\nMU 23,23,21\nCV 22,22,23\nAW 15,22\nAR 22,16\n"
"v_and_b32 v23, 0xffff0000, v22\nv_lshlrev_b32 v22, 16, v22\nMU 22,22,20\nMU 23,23,20\nCV 22,22,23\nAW 16,22\nAR 22,17\nv_and_b32 v23, 0xffff0000, v22\nv_lshlrev_b32 v22, 16, v22\n"
"MU 22,22,20\nMU 23,23,20\nCV 22,22,23\nAW 17,22\nAR 22,18\nv_and_b32 v23, 0xffff0000, v22\nv_lshlrev_b32 v22, 16, v22\nMU 22,22,20\nMU 23,23,20\nCV 22,22,23\nAW 18,22\nAR 22,19\n"
"v_and_b32 v23, 0xffff0000, v22\nv_lshlrev_b32 v22, 16, v22\nMU 22,22,20\nMU 23,23,20\nCV 22,22,23\nAW 19,22\nAR 22,20\nv_and_b32 v23, 0xffff0000, v22\nv_lshlrev_b32 v22, 16, v22\n"
"MU 22,22,21\nMU 23,23,21\nCV 22,22,23\nAW 20,22\nAR 22,21\nv_and_b32 v23, 0xffff0000, v22\nv_lshlrev_b32 v22, 16, v22\nMU 22,22,21\nMU 23,23,21\nCV 22,22,23\nAW 21,22\nAR 22,22\n"
"v_and_b32 v23, 0xffff0000, v22\nv_lshlrev_b32 v22, 16, v22\nMU 22,22,21\nMU 23,23,21\nCV 22,22,23\nAW 22,22\nAR 22,23\nv_and_b32 v23, 0xffff0000, v22\nv_lshlrev_b32 v22, 16, v22\n"
"MU 22,22,21\nMU 23,23,21\nCV 22,22,23\nAW 23,22\nAR 22,24\nv_and_b32 v23, 0xffff0000, v22\nv_lshlrev_b32 v22, 16, v22\nMU 22,22,20\nMU 23,23,20\nCV 22,22,23\nAW 24,22\nAR 22,25\n"
"v_and_b32 v23, 0xffff0000, v22\nv_lshlrev_b32 v22, 16, v22\nMU 22,22,20\nMU 23,23,20\nCV 22,22,23\nAW 25,22\nAR 22,26\nv_and_b32 v23, 0xffff0000, v22\nv_lshlrev_b32 v22, 16, v22\n"
"MU 22,22,20\nMU 23,23,20\nCV 22,22,23\nAW 26,22\nAR 22,27\nv_and_b32 v23, 0xffff0000, v22\nv_lshlrev_b32 v22, 16, v22\nMU 22,22,20\nMU 23,23,20\nCV 22,22,23\nAW 27,22\nAR 22,28\n"
"v_and_b32 v23, 0xffff0000, v22\nv_lshlrev_b32 v22, 16, v22\nMU 22,22,21\nMU 23,23,21\nCV 22,22,23\nAW 28,22\nAR 22,29\nv_and_b32 v23, 0xffff0000, v22\nv_lshlrev_b32 v22, 16, v22\n"
"MU 22,22,21\nMU 23,23,21\nCV 22,22,23\nAW 29,22\nAR 22,30\nv_and_b32 v23, 0xffff0000, v22\nv_lshlrev_b32 v22, 16, v22\nMU 22,22,21\nMU 23,23,21\nCV 22,22,23\nAW 30,22\nAR 22,31\n"
"v_and_b32 v23, 0xffff0000, v22\nv_lshlrev_b32 v22, 16, v22\nMU 22,22,21\nMU 23,23,21\nCV 22,22,23\nAW 31,22\nAR 22,32\nv_and_b32 v23, 0xffff0000, v22\nv_lshlrev_b32 v22, 16, v22\n"
"MU 22,22,20\nMU 23,23,20\nCV 22,22,23\nAW 32,22\nAR 22,33\nv_and_b32 v23, 0xffff0000, v22\nv_lshlrev_b32 v22, 16, v22\nMU 22,22,20\nMU 23,23,20\nCV 22,22,23\nAW 33,22\nAR 22,34\n"
"v_and_b32 v23, 0xffff0000, v22\nv_lshlrev_b32 v22, 16, v22\nMU 22,22,20\nMU 23,23,20\nCV 22,22,23\nAW 34,22\nAR 22,35\nv_and_b32 v23, 0xffff0000, v22\nv_lshlrev_b32 v22, 16, v22\n"
"MU 22,22,20\nMU 23,23,20\nCV 22,22,23\nAW 35,22\nAR 22,36\nv_and_b32 v23, 0xffff0000, v22\nv_lshlrev_b32 v22, 16, v22\nMU 22,22,21\nMU 23,23,21\nCV 22,22,23\nAW 36,22\nAR 22,37\n"
"v_and_b32 v23, 0xffff0000, v22\nv_lshlrev_b32 v22, 16, v22\nMU 22,22,21\nMU 23,23,21\nCV 22,22,23\nAW 37,22\nAR 22,38\nv_and_b32 v23, 0xffff0000, v22\nv_lshlrev_b32 v22, 16, v22\n"
"MU 22,22,21\nMU 23,23,21\nCV 22,22,23\nAW 38,22\nAR 22,39\nv_and_b32 v23, 0xffff0000, v22\nv_lshlrev_b32 v22, 16, v22\nMU 22,22,21\nMU 23,23,21\nCV 22,22,23\nAW 39,22\nAR 22,40\n"
"v_and_b32 v23, 0xffff0000, v22\nv_lshlrev_b32 v22, 16, v22\nMU 22,22,20\nMU 23,23,20\nCV 22,22,23\nAW 40,22\nAR 22,41\nv_and_b32 v23, 0xffff0000, v22\nv_lshlrev_b32 v22, 16, v22\n"
"MU 22,22,20\nMU 23,23,20\nCV 22,22,23\nAW 41,22\nAR 22,42\nv_and_b32 v23, 0xffff0000, v22\nv_lshlrev_b32 v22, 16, v22\nMU 22,22,20\nMU 23,23,20\nCV 22,22,23\nAW 42,22\nAR 22,43\n"
"v_and_b32 v23, 0xffff0000, v22\nv_lshlrev_b32 v22, 16, v22\nMU 22,22,20\nMU 23,23,20\nCV 22,22,23\nAW 43,22\nAR 22,44\nv_and_b32 v23, 0xffff0000, v22\nv_lshlrev_b32 v22, 16, v22\n"
"MU 22,22,21\nMU 23,23,21\nCV 22,22,23\nAW 44,22\nAR 22,45\nv_and_b32 v23, 0xffff0000, v22\nv_lshlrev_b32 v22, 16, v22\nMU 22,22,21\nMU 23,23,21\nCV 22,22,23\nAW 45,22\nAR 22,46\n"
"v_and_b32 v23, 0xffff0000, v22\nv_lshlrev_b32 v22, 16, v22\nMU 22,22,21\nMU 23,23,21\nCV 22,22,23\nAW 46,22\nAR 22,47\nv_and_b32 v23, 0xffff0000, v22\nv_lshlrev_b32 v22, 16, v22\n"
"MU 22,22,21\nMU 23,23,21\nCV 22,22,23\nAW 47,22\nAR 22,48\nv_and_b32 v23, 0xffff0000, v22\nv_lshlrev_b32 v22, 16, v22\nMU 22,22,20\nMU 23,23,20\nCV 22,22,23\nAW 48,22\nAR 22,49\n"
"v_and_b32 v23, 0xffff0000, v22\nv_lshlrev_b32 v22, 16, v22\nMU 22,22,20\nMU 23,23,20\nCV 22,22,23\nAW 49,22\nAR 22,50\nv_and_b32 v23, 0xffff0000, v22\nv_lshlrev_b32 v22, 16, v22\n"
"MU 22,22,20\nMU 23,23,20\nCV 22,22,23\nAW 50,22\nAR 22,51\nv_and_b32 v23, 0xffff0000, v22\nv_lshlrev_b32 v22, 16, v22\nMU 22,22,20\nMU 23,23,20\nCV 22,22,23\nAW 51,22\nAR 22,52\n"
"v_and_b32 v23, 0xffff0000, v22\nv_lshlrev_b32 v22, 16, v22\nMU 22,22,21\nMU 23,23,21\nCV 22,22,23\nAW 52,22\nAR 22,53\nv_and_b32 v23, 0xffff0000, v22\nv_lshlrev_b32 v22, 16, v22\n"
"MU 22,22,21\nMU 23,23,21\nCV 22,22,23\nAW 53,22\nAR 22,54\nv_and_b32 v23, 0xffff0000, v22\nv_lshlrev_b32 v22, 16, v22\nMU 22,22,21\nMU 23,23,21\nCV 22,22,23\nAW 54,22\nAR 22,55\n"
"v_and_b32 v23, 0xffff0000, v22\nv_lshlrev_b32 v22, 16, v22\nMU 22,22,21\nMU 23,23,21\nCV 22,22,23\nAW 55,22\nAR 22,56\nv_and_b32 v23, 0xffff0000, v22\nv_lshlrev_b32 v22, 16, v22\n"
"MU 22,22,20\nMU 23,23,20\nCV 22,22,23\nAW 56,22\nAR 22,57\nv_and_b32 v23, 0xffff0000, v22\nv_lshlrev_b32 v22, 16, v22\nMU 22,22,20\nMU 23,23,20\nCV 22,22,23\nAW 57,22\nAR 22,58\n"
"v_and_b32 v23, 0xffff0000, v22\nv_lshlrev_b32 v22, 16, v22\nMU 22,22,20\nMU 23,23,20\nCV 22,22,23\nAW 58,22\nAR 22,59\nv_and_b32 v23, 0xffff0000, v22\nv_lshlrev_b32 v22, 16, v22\n"
"MU 22,22,20\nMU 23,23,20\nCV 22,22,23\nAW 59,22\nAR 22,60\nv_and_b32 v23, 0xffff0000, v22\nv_lshlrev_b32 v22, 16, v22\nMU 22,22,21\nMU 23,23,21\nCV 22,22,23\nAW 60,22\nAR 22,61\n"
"v_and_b32 v23, 0xffff0000, v22\nv_lshlrev_b32 v22, 16, v22\nMU 22,22,21\nMU 23,23,21\nCV 22,22,23\nAW 61,22\nAR 22,62\nv_and_b32 v23, 0xffff0000, v22\nv_lshlrev_b32 v22, 16, v22\n"
"MU 22,22,21\nMU 23,23,21\nCV 22,22,23\nAW 62,22\nAR 22,63\nv_and_b32 v23, 0xffff0000, v22\nv_lshlrev_b32 v22, 16, v22\nMU 22,22,21\nMU 23,23,21\nCV 22,22,23\nAW 63,22\n"
"v_lshrrev_b32 v22, 8, v1\nv_bfe_u32 v23, v1, 6, 2\nv_cmp_eq_u32 vcc, v22, v23\nv_and_b32 v22, 16, v1\nv_mov_b32 v23, 0x3f80\nv_cndmask_b32 v23, 0, v23, vcc\n"
"v_lshlrev_b32 v23, v22, v23\nv_and_b32 v22, 32, v1\nv_cmp_eq_u32 vcc, 0, v22\nv_mov_b32 v112, 0\nv_mov_b32 v113, 0\nv_cndmask_b32 v114, 0, v23, vcc\nv_sub_u32 v115, v23, v114\n"
"v_mov_b32 v116, 0\nv_mov_b32 v117, 0\ns_nop 1\nMvva 128,114,0\nMvva 136,112,0\nMvva 140,112,4\nMvva 132,114,4\nMvva 144,114,8\nMvva 152,112,8\nMvva 156,112,12\nMvva 148,114,12\n"
"Mvva 160,114,16\nMvva 168,112,16\nMvva 172,112,20\nMvva 164,114,20\nMvva 176,114,24\nMvva 184,112,24\nMvva 188,112,28\nMvva 180,114,28\nMvva 192,114,32\nMvva 200,112,32\n"
"Mvva 204,112,36\nMvva 196,114,36\nMvva 208,114,40\nMvva 216,112,40\nMvva 220,112,44\nMvva 212,114,44\nMvva 224,114,48\nMvva 232,112,48\nMvva 236,112,52\nMvva 228,114,52\n"
"Mvva 240,114,56\nMvva 248,112,56\nMvva 252,112,60\nMvva 244,114,60\ns_nop 7\ns_branch .T88\n.S90:\ns_mov_b32 s7, 262144\n.S92:\ns_add_u32 s0, s92, 0\n"
"buffer_load_dwordx4 v[208:211], v1, s[16:19], s0 offen offset:0 sc0 sc1\nbuffer_load_dwordx4 v[212:215], v1, s[16:19], s0 offen offset:1024 sc0 sc1\nWV 0\n"
"v_max_u32 v21, v211, v215\nv_cmp_eq_u32 vcc, -1, v21\ns_nop 0\ns_cbranch_vccz .S91\ns_sleep 6\ns_sub_u32 s7, s7, 1\ns_cbranch_scc0 .S92\ns_branch .S91\n.S93:\n"
"s_mov_b32 s7, 262144\n.S95:\ns_add_u32 s0, s92, 131072\nbuffer_load_dwordx4 v[216:219], v1, s[16:19], s0 offen offset:0 sc0 sc1\n"
"buffer_load_dwordx4 v[220:223], v1, s[16:19], s0 offen offset:1024 sc0 sc1\nWV 0\nv_max_u32 v21, v219, v223\nv_cmp_eq_u32 vcc, -1, v21\ns_nop 0\ns_cbranch_vccz .S94\ns_sleep 6\n"
"s_sub_u32 s7, s7, 1\ns_cbranch_scc0 .S95\ns_branch .S94\n.S96:\ns_mov_b32 s7, 262144\n.S98:\ns_add_u32 s0, s92, 262144\n"
"buffer_load_dwordx4 v[224:227], v1, s[16:19], s0 offen offset:0 sc0 sc1\nbuffer_load_dwordx4 v[228:231], v1, s[16:19], s0 offen offset:1024 sc0 sc1\nWV 0\n"
"v_max_u32 v21, v227, v231\nv_cmp_eq_u32 vcc, -1, v21\ns_nop 0\ns_cbranch_vccz .S97\ns_sleep 6\ns_sub_u32 s7, s7, 1\ns_cbranch_scc0 .S98\ns_branch .S97\n.S99:\n"
"s_mov_b32 s7, 262144\n.S101:\ns_add_u32 s0, s92, 393216\nbuffer_load_dwordx4 v[208:211], v1, s[16:19], s0 offen offset:0 sc0 sc1\n"
"buffer_load_dwordx4 v[212:215], v1, s[16:19], s0 offen offset:1024 sc0 sc1\nWV 0\nv_max_u32 v21, v211, v215\nv_cmp_eq_u32 vcc, -1, v21\ns_nop 0\ns_cbranch_vccz .S100\ns_sleep 6\n"
"s_sub_u32 s7, s7, 1\ns_cbranch_scc0 .S101\ns_branch .S100\n.S102:\ns_mov_b32 s7, 262144\n.S104:\ns_add_u32 s0, s92, 524288\n"
"buffer_load_dwordx4 v[216:219], v1, s[16:19], s0 offen offset:0 sc0 sc1\nbuffer_load_dwordx4 v[220:223], v1, s[16:19], s0 offen offset:1024 sc0 sc1\nWV 0\n"
"v_max_u32 v21, v219, v223\nv_cmp_eq_u32 vcc, -1, v21\ns_nop 0\ns_cbranch_vccz .S103\ns_sleep 6\ns_sub_u32 s7, s7, 1\ns_cbranch_scc0 .S104\ns_branch .S103\n.S105:\n"
"s_mov_b32 s7, 262144\n.S107:\ns_add_u32 s0, s92, 655360\nbuffer_load_dwordx4 v[224:227], v1, s[16:19], s0 offen offset:0 sc0 sc1\n"
"buffer_load_dwordx4 v[228:231], v1, s[16:19], s0 offen offset:1024 sc0 sc1\nWV 0\nv_max_u32 v21, v227, v231\nv_cmp_eq_u32 vcc, -1, v21\ns_nop 0\ns_cbranch_vccz .S106\ns_sleep 6\n"
"s_sub_u32 s7, s7, 1\ns_cbranch_scc0 .S107\ns_branch .S106\n.S108:\ns_mov_b32 s7, 262144\n.S110:\ns_add_u32 s0, s92, 786432\n"
"buffer_load_dwordx4 v[208:211], v1, s[16:19], s0 offen offset:0 sc0 sc1\nbuffer_load_dwordx4 v[212:215], v1, s[16:19], s0 offen offset:1024 sc0 sc1\nWV 0\n"
"v_max_u32 v21, v211, v215\nv_cmp_eq_u32 vcc, -1, v21\ns_nop 0\ns_cbranch_vccz .S109\ns_sleep 6\ns_sub_u32 s7, s7, 1\ns_cbranch_scc0 .S110\ns_branch .S109\n.S111:\n"
"s_mov_b32 s7, 262144\n.S113:\nbuffer_load_dwordx4 v[64:67], v89, s[16:19], s40 offen offset:0 sc0 sc1\nbuffer_load_dwordx4 v[68:71], v89, s[16:19], s40 offen offset:16 sc0 sc1\n"
"buffer_load_dwordx4 v[72:75], v89, s[16:19], s40 offen offset:1792 sc0 sc1\nbuffer_load_dwordx4 v[76:79], v89, s[16:19], s40 offen offset:1808 sc0 sc1\n"
"buffer_load_dwordx2 v[80:81], v88, s[16:19], s63 offen offset:0 sc0 sc1\nbuffer_load_dwordx2 v[82:83], v88, s[16:19], s63 offen offset:512 sc0 sc1\nWV 0\nv_xor_b32 v66, s15, v66\n"
"v_xor_b32 v70, s15, v70\nv_xor_b32 v74, s15, v74\nv_xor_b32 v78, s15, v78\nv_xor_b32 v81, s15, v81\nv_xor_b32 v83, s15, v83\nv_or3_b32 v92, v66, v70, v74\n"
"v_or3_b32 v92, v92, v78, v81\nv_or_b32 v92, v92, v83\nv_cmp_ne_u32 vcc, 0, v92\ns_nop 0\ns_cbranch_vccz .S112\ns_sleep 6\ns_sub_u32 s7, s7, 1\ns_cbranch_scc0 .S113\n"
"s_branch .S112\n.S114:\ns_mov_b32 s7, 262144\n.S116:\nbuffer_load_dword v27, off, s[16:19], s60 offset:512 sc0 sc1\nWV 0\nv_cmp_ne_u32 vcc, s15, v27\ns_nop 0\n"
"s_cbranch_vccz .S115\ns_sleep 6\ns_sub_u32 s7, s7, 1\ns_cbranch_scc0 .S116\ns_branch .S115\n.S117:\ns_mov_b32 s7, 262144\n.S119:\n"
"buffer_load_dword v27, v24, s[16:19], s41 offen sc0 sc1\nWV 0\nv_xor_b32 v27, s15, v27\nv_and_b32 v26, 0xffffdfff, v27\nv_cmp_ne_u32 s[64:65], 0, v27\nv_cmp_ne_u32 vcc, 0, v26\n"
"s_nop 0\ns_cbranch_vccz .S118\ns_sleep 6\ns_sub_u32 s7, s7, 1\ns_cbranch_scc0 .S119\ns_branch .S118\n.Z121:\ns_sub_u32 s7, s7, 1\ns_cbranch_scc1 .Z122\ns_cmp_lg_u64 s[44:45], 0\n"
"s_cbranch_scc0 .Z124\nbuffer_load_dwordx4 v[96:99], v1, s[16:19], s24 offen offset:0 sc0 sc1\nbuffer_load_dwordx4 v[100:103], v1, s[16:19], s24 offen offset:1024 sc0 sc1\n.Z124:\n"
"s_cmp_lg_u64 s[46:47], 0\ns_cbranch_scc0 .Z125\nbuffer_load_dwordx4 v[104:107], v1, s[16:19], s25 offen offset:0 sc0 sc1\n"
"buffer_load_dwordx4 v[108:111], v1, s[16:19], s25 offen offset:1024 sc0 sc1\n.Z125:\ns_cmp_lg_u64 s[48:49], 0\ns_cbranch_scc0 .Z126\n"
"buffer_load_dwordx4 v[112:115], v1, s[16:19], s26 offen offset:0 sc0 sc1\nbuffer_load_dwordx4 v[116:119], v1, s[16:19], s26 offen offset:1024 sc0 sc1\n.Z126:\n"
"s_cmp_lg_u64 s[50:51], 0\ns_cbranch_scc0 .Z127\nbuffer_load_dwordx4 v[120:123], v1, s[16:19], s27 offen offset:0 sc0 sc1\n"
"buffer_load_dwordx4 v[124:127], v1, s[16:19], s27 offen offset:1024 sc0 sc1\n.Z127:\ns_cmp_lg_u64 s[52:53], 0\ns_cbranch_scc0 .Z128\n"
"buffer_load_dwordx4 v[32:35], v1, s[16:19], s32 offen offset:0 sc0 sc1\nbuffer_load_dwordx4 v[36:39], v1, s[16:19], s32 offen offset:1024 sc0 sc1\n.Z128:\n"
"s_cmp_lg_u64 s[54:55], 0\ns_cbranch_scc0 .Z129\nbuffer_load_dwordx4 v[40:43], v1, s[16:19], s33 offen offset:0 sc0 sc1\n"
"buffer_load_dwordx4 v[44:47], v1, s[16:19], s33 offen offset:1024 sc0 sc1\n.Z129:\ns_cmp_lg_u64 s[56:57], 0\ns_cbranch_scc0 .Z130\n"
"buffer_load_dwordx4 v[48:51], v1, s[16:19], s34 offen offset:0 sc0 sc1\nbuffer_load_dwordx4 v[52:55], v1, s[16:19], s34 offen offset:1024 sc0 sc1\n.Z130:\n"
"s_cmp_lg_u64 s[58:59], 0\ns_cbranch_scc0 .Z131\nbuffer_load_dwordx4 v[56:59], v1, s[16:19], s35 offen offset:0 sc0 sc1\n"
"buffer_load_dwordx4 v[60:63], v1, s[16:19], s35 offen offset:1024 sc0 sc1\n.Z131:\ns_sleep 8\nWV 0\ns_branch .Z123\n.RA10:\ns_nop 7\ns_nop 3\nv_max3_f32 v22, v32, v33, v34\n"
"v_max3_f32 v22, v22, v35, v40\nv_max3_f32 v22, v22, v41, v42\nv_max_f32 v22, v22, v43\nv_mov_b32 v91, v22\ns_nop 1\nv_permlane32_swap_b32 v22, v91\nv_max_f32 v22, v22, v91\n"
"v_mov_b32 v91, v22\ns_nop 1\nv_permlane16_swap_b32 v22, v91\nv_max_f32 v22, v22, v91\nv_ceil_f32 v22, v22\nv_max3_f32 v23, v36, v37, v38\nv_max3_f32 v23, v23, v39, v44\n"
"v_max3_f32 v23, v23, v45, v46\nv_max_f32 v23, v23, v47\nv_mov_b32 v91, v23\ns_nop 1\nv_permlane32_swap_b32 v23, v91\nv_max_f32 v23, v23, v91\nv_mov_b32 v91, v23\ns_nop 1\n"
"v_permlane16_swap_b32 v23, v91\nv_max_f32 v23, v23, v91\nv_ceil_f32 v23, v23\ns_bitcmp1_b32 s62, 0\ns_cbranch_scc0 .R133\nv_cmp_lt_f32 vcc, 0xff000000, v22\ns_nop 1\n"
"v_cndmask_b32 v22, 0, v22, vcc\nv_cmp_lt_f32 vcc, 0xff000000, v23\ns_nop 1\nv_cndmask_b32 v23, 0, v23, vcc\ns_branch .R134\n.R133:\nv_max_f32 v22, 0, v22\nv_max_f32 v23, 0, v23\n"
"s_bitcmp1_b32 s62, 3\ns_cbranch_scc0 .R134\nv_max_f32 v22, v20, v22\nv_max_f32 v23, v21, v23\n.R134:\nAD 88,88,22\nAD 89,89,23\nSU 32,32,22\nSU 33,33,22\nSU 34,34,22\nSU 35,35,22\n"
"SU 36,36,23\nSU 37,37,23\nSU 38,38,23\nSU 39,39,23\nSU 40,40,22\nSU 41,41,22\nSU 42,42,22\nSU 43,43,22\nSU 44,44,23\nSU 45,45,23\nSU 46,46,23\nSU 47,47,23\nv_mov_b32 v90, v80\n"
"v_mov_b32 v91, v81\nEX 80,32\nEX 81,33\ns_nop 0\nAD 84,80,81\nCV 64,80,81\nEX 82,34\nEX 83,35\nAD 84,84,82\nAD 84,84,83\nCV 65,82,83\nEX 80,36\nEX 81,37\ns_nop 0\nAD 85,80,81\n"
"CV 68,80,81\nEX 82,38\nEX 83,39\nAD 85,85,82\nAD 85,85,83\nCV 69,82,83\nEX 80,40\nEX 81,41\nAD 84,84,80\nAD 84,84,81\nCV 66,80,81\nEX 82,42\nEX 83,43\nAD 84,84,82\nAD 84,84,83\n"
"CV 67,82,83\nEX 80,44\nEX 81,45\nAD 85,85,80\nAD 85,85,81\nCV 70,80,81\nEX 82,46\nEX 83,47\nAD 85,85,82\nAD 85,85,83\nCV 71,82,83\nv_mov_b32 v80, v90\nv_mov_b32 v81, v91\n"
"s_bitcmp1_b32 s62, 1\ns_cbranch_scc1 .R135\nSU 48,48,22\nSU 49,49,22\nSU 50,50,22\nSU 51,51,22\nSU 52,52,23\nSU 53,53,23\nSU 54,54,23\nSU 55,55,23\nSU 56,56,22\nSU 57,57,22\n"
"SU 58,58,22\nSU 59,59,22\nSU 60,60,23\nSU 61,61,23\nSU 62,62,23\nSU 63,63,23\nSU 4,4,22\nSU 5,5,22\nSU 6,6,22\nSU 7,7,22\nSU 8,8,23\nSU 9,9,23\nSU 10,10,23\nSU 11,11,23\n"
"SU 12,12,22\nSU 13,13,22\nSU 14,14,22\nSU 15,15,22\nSU 16,16,23\nSU 17,17,23\nSU 18,18,23\nSU 19,19,23\nEX 80,48\nEX 81,49\n.R135:\ns_bitcmp1_b32 s62, 0\ns_cbranch_scc0 .RS12\n"
"s_nop 1\ns_setpc_b64 s[80:81]\n.RB11:\ns_nop 7\ns_nop 3\nv_max3_f32 v22, v48, v49, v50\nv_max3_f32 v22, v22, v51, v56\nv_max3_f32 v22, v22, v57, v58\nv_max_f32 v22, v22, v59\n"
"v_mov_b32 v91, v22\ns_nop 1\nv_permlane32_swap_b32 v22, v91\nv_max_f32 v22, v22, v91\nv_mov_b32 v91, v22\ns_nop 1\nv_permlane16_swap_b32 v22, v91\nv_max_f32 v22, v22, v91\n"
"v_ceil_f32 v22, v22\nv_max3_f32 v23, v52, v53, v54\nv_max3_f32 v23, v23, v55, v60\nv_max3_f32 v23, v23, v61, v62\nv_max_f32 v23, v23, v63\nv_mov_b32 v91, v23\ns_nop 1\n"
"v_permlane32_swap_b32 v23, v91\nv_max_f32 v23, v23, v91\nv_mov_b32 v91, v23\ns_nop 1\nv_permlane16_swap_b32 v23, v91\nv_max_f32 v23, v23, v91\nv_ceil_f32 v23, v23\n"
"s_bitcmp1_b32 s62, 0\ns_cbranch_scc0 .R138\nv_cmp_lt_f32 vcc, 0xff000000, v22\nv_cndmask_b32 v22, 0, v22, vcc\nv_cmp_lt_f32 vcc, 0xff000000, v23\nv_cndmask_b32 v23, 0, v23, vcc\n"
"s_branch .R139\n.R138:\nv_max_f32 v22, 0, v22\nv_max_f32 v23, 0, v23\ns_bitcmp1_b32 s62, 3\ns_cbranch_scc0 .R139\nv_max_f32 v22, v20, v22\nv_max_f32 v23, v21, v23\n.R139:\n"
"AD 88,88,22\nAD 89,89,23\nSU 48,48,22\nSU 49,49,22\nSU 50,50,22\nSU 51,51,22\nSU 52,52,23\nSU 53,53,23\nSU 54,54,23\nSU 55,55,23\nSU 56,56,22\nSU 57,57,22\nSU 58,58,22\n"
"SU 59,59,22\nSU 60,60,23\nSU 61,61,23\nSU 62,62,23\nSU 63,63,23\nv_mov_b32 v90, v80\nv_mov_b32 v91, v81\nEX 80,48\nEX 81,49\ns_nop 0\nAD 84,80,81\nCV 72,80,81\nEX 82,50\nEX 83,51\n"
"AD 84,84,82\nAD 84,84,83\nCV 73,82,83\nEX 80,52\nEX 81,53\ns_nop 0\nAD 85,80,81\nCV 76,80,81\nEX 82,54\nEX 83,55\nAD 85,85,82\nAD 85,85,83\nCV 77,82,83\nEX 80,56\nEX 81,57\n"
"AD 84,84,80\nAD 84,84,81\nCV 74,80,81\nEX 82,58\nEX 83,59\nAD 84,84,82\nAD 84,84,83\nCV 75,82,83\nEX 80,60\nEX 81,61\nAD 85,85,80\nAD 85,85,81\nCV 78,80,81\nEX 82,62\nEX 83,63\n"
"AD 85,85,82\nAD 85,85,83\nCV 79,82,83\nv_mov_b32 v80, v90\nv_mov_b32 v81, v91\ns_bitcmp1_b32 s62, 1\ns_cbranch_scc1 .R140\nSU 32,32,22\nSU 33,33,22\nSU 34,34,22\nSU 35,35,22\n"
"SU 36,36,23\nSU 37,37,23\nSU 38,38,23\nSU 39,39,23\nSU 40,40,22\nSU 41,41,22\nSU 42,42,22\nSU 43,43,22\nSU 44,44,23\nSU 45,45,23\nSU 46,46,23\nSU 47,47,23\nSU 4,4,22\nSU 5,5,22\n"
"SU 6,6,22\nSU 7,7,22\nSU 8,8,23\nSU 9,9,23\nSU 10,10,23\nSU 11,11,23\nSU 12,12,22\nSU 13,13,22\nSU 14,14,22\nSU 15,15,22\nSU 16,16,23\nSU 17,17,23\nSU 18,18,23\nSU 19,19,23\n"
"EX 80,32\nEX 81,33\n.R140:\ns_bitcmp1_b32 s62, 0\ns_cbranch_scc0 .RS12\ns_nop 1\ns_setpc_b64 s[80:81]\n.RS12:\nv_exp_f32 v22, -v22\nv_exp_f32 v23, -v23\nMU 86,86,22\nMU 87,87,23\n"
"MU 128,128,22\nMU 129,129,22\nMU 130,130,22\nMU 131,131,22\nMU 132,132,23\nMU 133,133,23\nMU 134,134,23\nMU 135,135,23\nMU 136,136,22\nMU 137,137,22\nMU 138,138,22\nMU 139,139,22\n"
"MU 140,140,23\nMU 141,141,23\nMU 142,142,23\nMU 143,143,23\nMU 144,144,22\nMU 145,145,22\nMU 146,146,22\nMU 147,147,22\nMU 148,148,23\nMU 149,149,23\nMU 150,150,23\nMU 151,151,23\n"
"MU 152,152,22\nMU 153,153,22\nMU 154,154,22\nMU 155,155,22\nMU 156,156,23\nMU 157,157,23\nMU 158,158,23\nMU 159,159,23\nMU 160,160,22\nMU 161,161,22\nMU 162,162,22\nMU 163,163,22\n"
"MU 164,164,23\nMU 165,165,23\nMU 166,166,23\nMU 167,167,23\nMU 168,168,22\nMU 169,169,22\nMU 170,170,22\nMU 171,171,22\nMU 172,172,23\nMU 173,173,23\nMU 174,174,23\nMU 175,175,23\n"
"MU 176,176,22\nMU 177,177,22\nMU 178,178,22\nMU 179,179,22\nMU 180,180,23\nMU 181,181,23\nMU 182,182,23\nMU 183,183,23\nMU 184,184,22\nMU 185,185,22\nMU 186,186,22\nMU 187,187,22\n"
"MU 188,188,23\nMU 189,189,23\nMU 190,190,23\nMU 191,191,23\nMU 192,192,22\nMU 193,193,22\nMU 194,194,22\nMU 195,195,22\nMU 196,196,23\nMU 197,197,23\nMU 198,198,23\nMU 199,199,23\n"
"MU 200,200,22\nMU 201,201,22\nMU 202,202,22\nMU 203,203,22\nMU 204,204,23\nMU 205,205,23\nMU 206,206,23\nMU 207,207,23\nMU 208,208,22\nMU 209,209,22\nMU 210,210,22\nMU 211,211,22\n"
"MU 212,212,23\nMU 213,213,23\nMU 214,214,23\nMU 215,215,23\nMU 216,216,22\nMU 217,217,22\nMU 218,218,22\nMU 219,219,22\nMU 220,220,23\nMU 221,221,23\nMU 222,222,23\nMU 223,223,23\n"
"MU 224,224,22\nMU 225,225,22\nMU 226,226,22\nMU 227,227,22\nMU 228,228,23\nMU 229,229,23\nMU 230,230,23\nMU 231,231,23\nMU 232,232,22\nMU 233,233,22\nMU 234,234,22\nMU 235,235,22\n"
"MU 236,236,23\nMU 237,237,23\nMU 238,238,23\nMU 239,239,23\nMU 240,240,22\nMU 241,241,22\nMU 242,242,22\nMU 243,243,22\nMU 244,244,23\nMU 245,245,23\nMU 246,246,23\nMU 247,247,23\n"
"MU 248,248,22\nMU 249,249,22\nMU 250,250,22\nMU 251,251,22\nMU 252,252,23\nMU 253,253,23\nMU 254,254,23\nMU 255,255,23\ns_nop 1\ns_setpc_b64 s[80:81]\n.p2align 12\n.PA3:\n"
"s_mov_b64 exec, 0\ns_cmp_eq_u32 s11, 1\ns_cbranch_scc1 .W548\ns_cmp_eq_u32 s11, 2\ns_cbranch_scc1 .W549\ns_cmp_eq_u32 s11, 3\ns_cbranch_scc1 .W550\ns_branch .W547\n.WB151:\n"
"s_mov_b64 exec, -1\nv_add_u32 v128, 0x10000, v12\nv_add_u32 v129, 0x10000, v13\nv_add_u32 v130, 0x10000, v14\nv_add_u32 v131, 0x10000, v15\nv_add_u32 v132, 0x10000, v16\n"
"v_add_u32 v133, 0x10000, v17\nv_add_u32 v134, 0x10000, v18\nv_add_u32 v135, 0x10000, v19\ns_add_u32 s2, s11, 1\ns_lshr_b32 s2, s2, 1\ns_lshl_b32 s2, s2, 12\n"
"v_add_u32 v140, s2, v10\nv_add_u32 v141, s2, v11\nv_xor_b32 v142, 0x1000, v140\nv_xor_b32 v143, 0x1000, v141\ns_add_u32 m0, s47, 0\ns_add_u32 s46, s51, 524288\nD0 6,32,46,0\n"
"D0 7,32,46,1024\nD0 8,32,46,2048\nD0 9,32,46,3072\ns_add_u32 m0, s47, -49152\ns_add_u32 s46, s52, 128\nD0 136,28,46,0\nD0 137,28,46,1024\nD0 138,28,46,2048\nD0 139,28,46,3072\n"
"s_add_u32 m0, s47, 16384\ns_add_u32 s46, s51, 786432\nD0 6,32,46,0\nD0 7,32,46,1024\nD0 8,32,46,2048\nD0 9,32,46,3072\nv_mov_b32 v116, 0x40000000\nv_cmp_gt_u32 vcc, 56, v0\n"
"s_lshl2_add_u32 s2, s10, s11\ns_mul_i32 s2, s2, 1792\nv_cndmask_b32 v117, v116, v1, vcc\ns_mov_b32 s36, s56\ns_and_b32 s37, s57, 0xffff\ns_mov_b32 s38, 0x380\n"
"s_mov_b32 s39, 0x20000\ns_mov_b32 s40, s58\ns_and_b32 s41, s59, 0xffff\ns_mov_b32 s42, 0x380\ns_mov_b32 s43, 0x20000\n"
"buffer_load_dwordx4 v[96:99], v117, s[28:31], s2 offen offset:0\nbuffer_load_dwordx4 v[100:103], v117, s[28:31], s2 offen offset:896\n"
"buffer_load_dwordx4 v[104:107], v117, s[36:39], 0 offen offset:0\nbuffer_load_dwordx4 v[108:111], v117, s[40:43], 0 offen offset:0\ns_add_u32 m0, s47, 32768\n"
"s_add_u32 s46, s51, 1048576\nD0 6,32,46,0\nD0 7,32,46,1024\nD0 8,32,46,2048\nD0 9,32,46,3072\ns_add_u32 m0, s47, -32768\ns_add_u32 s46, s52, 256\nD0 136,28,46,0\n"
"D0 137,28,46,1024\nD0 138,28,46,2048\nD0 139,28,46,3072\nWL 0\ns_mul_i32 s15, s15, 1664525\ns_add_u32 s15, s15, 1013904223\nv_mov_b32 v25, s15\ns_mov_b32 s57, 0x3db8aa3b\n"
"s_and_b32 s59, s11, 1\ns_lshl_b32 s59, s59, 13\ns_mul_i32 s2, s73, 25165824\ns_and_b32 s3, s12, 1\ns_lshl_b32 s3, s3, 21\ns_add_u32 s2, s2, s3\ns_lshl2_add_u32 s3, s13, s11\n"
"s_lshl_b32 s3, s3, 15\ns_add_u32 s2, s2, s3\ns_add_u32 s56, s2, 0x800000\ns_xor_b32 s53, s56, 0xc000\nWV 24\ns_barrier\nRa 192,140,0\nRa 196,141,0\nRa 200,140,2048\n"
"Ra 204,141,2048\nRa 208,142,0\nRa 212,143,0\nRa 216,142,2048\nRa 220,143,2048\nTa 160,128,0\nTa 162,128,2048\nTa 164,128,16384\nTa 166,128,18432\nTa 168,129,0\nTa 170,129,2048\n"
"Ta 172,129,16384\nTa 174,129,18432\nTa 176,130,0\nTa 178,130,2048\nTa 180,130,16384\nTa 182,130,18432\nTa 184,131,0\nTa 186,131,2048\nTa 188,131,16384\nTa 190,131,18432\nWL 0\n"
"Zvaa 32,160,192\nTa 224,132,0\ns_add_u32 m0, s47, 49152\ns_add_u32 s46, s51, 1310720\nMvaa 32,164,196\nTa 226,132,2048\nZaaa 96,164,204\nD0 6,32,46,0\nMaaa 96,160,200\n"
"Ta 228,132,16384\nZvaa 192,160,208\nTa 230,132,18432\nMvaa 192,164,212\nZvaa 224,164,220\nTa 232,133,0\nMvaa 224,160,216\nTa 234,133,2048\nD0 7,32,46,1024\nZvaa 228,168,216\n"
"Mvaa 228,172,220\nTa 236,133,16384\nZvaa 196,172,212\nTa 238,133,18432\nMvaa 196,168,208\nZaaa 100,168,200\nTa 240,134,0\nD0 8,32,46,2048\nMaaa 100,172,204\nTa 242,134,2048\n"
"Zvaa 36,172,196\nMvaa 36,168,192\nTa 244,134,16384\nZvaa 40,176,192\nTa 246,134,18432\nMvaa 40,180,196\nD0 9,32,46,3072\nZaaa 104,180,204\nTa 248,135,0\nMaaa 104,176,200\n"
"Ta 250,135,2048\nZvaa 200,176,208\ns_add_u32 m0, s47, -16384\ns_add_u32 s46, s52, 384\nMvaa 200,180,212\nTa 252,135,16384\nZvaa 232,180,220\nTa 254,135,18432\nD0 136,28,46,0\n"
"Mvaa 232,176,216\nZvaa 236,184,216\nMvaa 236,188,220\nZvaa 204,188,212\nMvaa 204,184,208\nD0 137,28,46,1024\nZaaa 108,184,200\nMaaa 108,188,204\nZvaa 44,188,196\nMvaa 44,184,192\n"
"s_waitcnt vmcnt(18) lgkmcnt(0)\ns_barrier\nZvaa 48,224,192\nRa 128,140,16384\nD0 138,28,46,2048\nMvaa 48,228,196\nRa 132,141,16384\nZaaa 112,228,204\nRa 136,140,18432\n"
"Maaa 112,224,200\nRa 140,141,18432\nZvaa 208,224,208\nRa 144,142,16384\nWV 15\nMvaa 208,228,212\nRa 148,143,16384\nD0 139,28,46,3072\nZvaa 240,228,220\nRa 152,142,18432\n"
"Mvaa 240,224,216\nRa 156,143,18432\nZvaa 244,232,216\nTa 160,12,0\ns_add_u32 m0, s47, 65536\ns_add_u32 s46, s51, 1572864\nMvaa 244,236,220\nTa 162,12,2048\nZvaa 212,236,212\n"
"Ta 164,12,16384\nD0 6,32,46,0\nMvaa 212,232,208\nTa 166,12,18432\nZaaa 116,232,200\nTa 168,13,0\nv_lshlrev_b32 v112, 16, v96\nMaaa 116,236,204\nTa 170,13,2048\nZvaa 52,236,196\n"
"Ta 172,13,16384\nMvaa 52,232,192\nTa 174,13,18432\nD0 7,32,46,1024\nZvaa 56,240,192\nTa 176,14,0\nv_lshlrev_b32 v113, 16, v100\nMvaa 56,244,196\nTa 178,14,2048\nZaaa 120,244,204\n"
"Ta 180,14,16384\nMaaa 120,240,200\nTa 182,14,18432\nZvaa 216,240,208\nTa 184,15,0\nD0 8,32,46,2048\nMvaa 216,244,212\nTa 186,15,2048\nZvaa 248,244,220\nTa 188,15,16384\n"
"Mvaa 248,240,216\nTa 190,15,18432\nZvaa 252,248,216\nv_lshlrev_b32 v114, 16, v104\nv_lshlrev_b32 v115, 16, v108\nMvaa 252,252,220\nD0 9,32,46,3072\nZvaa 220,252,212\n"
"MU 116,112,114\nMvaa 220,248,208\nMU 117,113,114\nZaaa 124,248,200\ns_add_u32 m0, s47, 81920\ns_add_u32 s46, s51, 1835008\nMaaa 124,252,204\nCV 116,116,117\nZvaa 60,252,196\n"
"D0 6,32,46,0\nMvaa 60,248,192\nWL 0\nMvaa 32,160,128\nTa 224,16,0\nv_lshlrev_b32 v118, 16, v116\nMvaa 32,164,132\nTa 226,16,2048\nMaaa 96,164,140\n"
"v_and_b32 v119, 0xffff0000, v116\nMaaa 96,160,136\nTa 228,16,16384\nD0 7,32,46,1024\nMvaa 192,160,144\nTa 230,16,18432\nMU 120,112,115\nMvaa 192,164,148\nMU 121,113,115\n"
"Mvaa 224,164,156\nTa 232,17,0\nMvaa 224,160,152\nTa 234,17,2048\nMvaa 228,168,152\nD0 8,32,46,2048\nv_and_b32 v112, 0xffff0000, v96\nMvaa 228,172,156\nTa 236,17,16384\n"
"Mvaa 196,172,148\nTa 238,17,18432\nMvaa 196,168,144\nv_and_b32 v113, 0xffff0000, v100\nMaaa 100,168,136\nTa 240,18,0\nv_and_b32 v114, 0xffff0000, v104\nMaaa 100,172,140\n"
"Ta 242,18,2048\nD0 9,32,46,3072\nMvaa 36,172,132\nv_and_b32 v115, 0xffff0000, v108\nMvaa 36,168,128\nTa 244,18,16384\nMvaa 40,176,128\nTa 246,18,18432\nMU 116,112,114\n"
"Mvaa 40,180,132\nMU 117,113,114\nMaaa 104,180,140\nTa 248,19,0\nMaaa 104,176,136\nTa 250,19,2048\nMvaa 200,176,144\nCV 116,116,117\nv_and_b32 v117, 0xffff0000, v116\n"
"Mvaa 200,180,148\nTa 252,19,16384\nMvaa 232,180,156\nTa 254,19,18432\nMvaa 232,176,152\nv_lshlrev_b32 v116, 16, v116\nMvaa 236,184,152\nFM 120,112,115\nFM 121,113,115\n"
"Mvaa 236,188,156\nAD 118,118,116\nMvaa 204,188,148\nAD 119,119,117\nMvaa 204,184,144\nv_lshlrev_b32 v112, 16, v97\nMaaa 108,184,136\nv_lshlrev_b32 v113, 16, v101\n"
"v_lshlrev_b32 v114, 16, v105\nMaaa 108,188,140\nv_lshlrev_b32 v115, 16, v109\nMvaa 44,188,132\nMU 116,112,114\nMvaa 44,184,128\ns_waitcnt vmcnt(12) lgkmcnt(0)\ns_barrier\n"
"Mvaa 48,224,128\nRa 192,140,32768\ns_add_u32 m0, s47, -49152\ns_add_u32 s46, s52, 512\nMvaa 48,228,132\nRa 196,141,32768\nMaaa 112,228,140\nRa 200,140,34816\nD0 136,28,46,0\n"
"Maaa 112,224,136\nRa 204,141,34816\nMvaa 208,224,144\nRa 208,142,32768\nMU 117,113,114\nMvaa 208,228,148\nRa 212,143,32768\nMvaa 240,228,156\nRa 216,142,34816\nMvaa 240,224,152\n"
"Ra 220,143,34816\nD0 137,28,46,1024\nMvaa 244,232,152\nTa 160,12,32768\nCV 116,116,117\nMvaa 244,236,156\nTa 162,12,34816\nMvaa 212,236,148\nTa 164,12,49152\nMvaa 212,232,144\n"
"Ta 166,12,51200\nMaaa 116,232,136\nTa 168,13,32768\nD0 138,28,46,2048\nMaaa 116,236,140\nTa 170,13,34816\nMvaa 52,236,132\nTa 172,13,49152\nMvaa 52,232,128\nTa 174,13,51200\n"
"Mvaa 56,240,128\nTa 176,14,32768\nv_and_b32 v117, 0xffff0000, v116\nMvaa 56,244,132\nTa 178,14,34816\nD0 139,28,46,3072\nMaaa 120,244,140\nTa 180,14,49152\nMaaa 120,240,136\n"
"Ta 182,14,51200\nMvaa 216,240,144\nTa 184,15,32768\ns_add_u32 m0, s47, 0\ns_add_u32 s46, s51, 2097152\nMvaa 216,244,148\nTa 186,15,34816\nMvaa 248,244,156\nTa 188,15,49152\n"
"D0 6,32,46,0\nMvaa 248,240,152\nTa 190,15,51200\nMvaa 252,248,152\nv_lshlrev_b32 v116, 16, v116\nFM 120,112,115\nMvaa 252,252,156\nFM 121,113,115\nMvaa 220,252,148\n"
"AD 118,118,116\nMvaa 220,248,144\nD0 7,32,46,1024\nMaaa 124,248,136\nAD 119,119,117\nv_and_b32 v112, 0xffff0000, v97\nMaaa 124,252,140\nv_and_b32 v113, 0xffff0000, v101\n"
"Mvaa 60,252,132\nv_and_b32 v114, 0xffff0000, v105\nMvaa 60,248,128\nWL 0\nMvaa 32,160,192\nTa 224,16,32768\nD0 8,32,46,2048\nMvaa 32,164,196\nTa 226,16,34816\nMaaa 96,164,204\n"
"v_and_b32 v115, 0xffff0000, v109\nMaaa 96,160,200\nTa 228,16,49152\nMvaa 192,160,208\nTa 230,16,51200\nMU 116,112,114\nMvaa 192,164,212\nD0 9,32,46,3072\nMvaa 224,164,220\n"
"Ta 232,17,32768\nMvaa 224,160,216\nTa 234,17,34816\nMvaa 228,168,216\ns_add_u32 m0, s47, 16384\ns_add_u32 s46, s51, 2359296\nMvaa 228,172,220\nTa 236,17,49152\nMvaa 196,172,212\n"
"Ta 238,17,51200\nD0 6,32,46,0\nMvaa 196,168,208\nMU 117,113,114\nMaaa 100,168,200\nTa 240,18,32768\nCV 116,116,117\nMaaa 100,172,204\nTa 242,18,34816\nMvaa 36,172,196\n"
"v_and_b32 v117, 0xffff0000, v116\nMvaa 36,168,192\nTa 244,18,49152\nD0 7,32,46,1024\nMvaa 40,176,192\nTa 246,18,51200\nv_lshlrev_b32 v116, 16, v116\nMvaa 40,180,196\n"
"FM 120,112,115\nMaaa 104,180,204\nTa 248,19,32768\nMaaa 104,176,200\nTa 250,19,34816\nMvaa 200,176,208\nD0 8,32,46,2048\nFM 121,113,115\nMvaa 200,180,212\nTa 252,19,49152\n"
"Mvaa 232,180,220\nTa 254,19,51200\nMvaa 232,176,216\nAD 118,118,116\nMvaa 236,184,216\nAD 119,119,117\nv_lshlrev_b32 v112, 16, v98\nMvaa 236,188,220\nD0 9,32,46,3072\n"
"Mvaa 204,188,212\nv_lshlrev_b32 v113, 16, v102\nMvaa 204,184,208\nv_lshlrev_b32 v114, 16, v106\nMaaa 108,184,200\nv_lshlrev_b32 v115, 16, v110\nMU 116,112,114\nMaaa 108,188,204\n"
"MU 117,113,114\nMvaa 44,188,196\nCV 116,116,117\nMvaa 44,184,192\ns_waitcnt vmcnt(12) lgkmcnt(0)\ns_barrier\nMvaa 48,224,192\nRa 128,140,49152\ns_add_u32 m0, s47, -32768\n"
"s_add_u32 s46, s52, 640\nMvaa 48,228,196\nRa 132,141,49152\nMaaa 112,228,204\nRa 136,140,51200\nD0 136,28,46,0\nMaaa 112,224,200\nRa 140,141,51200\nMvaa 208,224,208\n"
"Ra 144,142,49152\nv_and_b32 v117, 0xffff0000, v116\nMvaa 208,228,212\nRa 148,143,49152\nMvaa 240,228,220\nRa 152,142,51200\nMvaa 240,224,216\nRa 156,143,51200\nD0 137,28,46,1024\n"
"Mvaa 244,232,216\nTa 160,128,0\nv_lshlrev_b32 v116, 16, v116\nMvaa 244,236,220\nTa 162,128,2048\nMvaa 212,236,212\nTa 164,128,16384\nMvaa 212,232,208\nTa 166,128,18432\n"
"Maaa 116,232,200\nTa 168,129,0\nD0 138,28,46,2048\nMaaa 116,236,204\nTa 170,129,2048\nMvaa 52,236,196\nTa 172,129,16384\nMvaa 52,232,192\nTa 174,129,18432\nMvaa 56,240,192\n"
"Ta 176,130,0\nFM 120,112,115\nMvaa 56,244,196\nTa 178,130,2048\nD0 139,28,46,3072\nMaaa 120,244,204\nTa 180,130,16384\nMaaa 120,240,200\nTa 182,130,18432\nMvaa 216,240,208\n"
"Ta 184,131,0\ns_add_u32 m0, s47, 32768\ns_add_u32 s46, s51, 2621440\nMvaa 216,244,212\nTa 186,131,2048\nMvaa 248,244,220\nTa 188,131,16384\nD0 6,32,46,0\nMvaa 248,240,216\n"
"Ta 190,131,18432\nMvaa 252,248,216\nFM 121,113,115\nAD 118,118,116\nMvaa 252,252,220\nAD 119,119,117\nMvaa 220,252,212\nv_and_b32 v112, 0xffff0000, v98\nMvaa 220,248,208\n"
"D0 7,32,46,1024\nMaaa 124,248,200\nv_and_b32 v113, 0xffff0000, v102\nv_and_b32 v114, 0xffff0000, v106\nMaaa 124,252,204\nv_and_b32 v115, 0xffff0000, v110\nMvaa 60,252,196\n"
"MU 116,112,114\nMvaa 60,248,192\nWL 0\nMvaa 32,160,128\nTa 224,132,0\n.W547:\ns_cbranch_execz .W551\nD0 8,32,46,2048\nMvaa 32,164,132\nTa 226,132,2048\nMaaa 96,164,140\n"
"MU 117,113,114\nMaaa 96,160,136\nTa 228,132,16384\nMvaa 192,160,144\nTa 230,132,18432\nCV 116,116,117\nMvaa 192,164,148\nD0 9,32,46,3072\nMvaa 224,164,156\nTa 232,133,0\n"
"Mvaa 224,160,152\nTa 234,133,2048\nMvaa 228,168,152\ns_add_u32 m0, s47, 49152\ns_add_u32 s46, s51, 2883584\nMvaa 228,172,156\nTa 236,133,16384\nMvaa 196,172,148\nTa 238,133,18432\n"
"D0 6,32,46,0\nMvaa 196,168,144\nv_and_b32 v117, 0xffff0000, v116\nMaaa 100,168,136\nTa 240,134,0\nv_lshlrev_b32 v116, 16, v116\nMaaa 100,172,140\nTa 242,134,2048\nMvaa 36,172,132\n"
"FM 120,112,115\nMvaa 36,168,128\nTa 244,134,16384\nD0 7,32,46,1024\nMvaa 40,176,128\nTa 246,134,18432\nFM 121,113,115\nMvaa 40,180,132\nAD 118,118,116\nMaaa 104,180,140\n"
"Ta 248,135,0\nMaaa 104,176,136\nTa 250,135,2048\nMvaa 200,176,144\nD0 8,32,46,2048\nAD 119,119,117\nMvaa 200,180,148\nTa 252,135,16384\nMvaa 232,180,156\nTa 254,135,18432\n"
"Mvaa 232,176,152\nv_lshlrev_b32 v112, 16, v99\nMvaa 236,184,152\nv_lshlrev_b32 v113, 16, v103\nv_lshlrev_b32 v114, 16, v107\nMvaa 236,188,156\nD0 9,32,46,3072\nMvaa 204,188,148\n"
"v_lshlrev_b32 v115, 16, v111\nMvaa 204,184,144\nMU 116,112,114\nMaaa 108,184,136\nMU 117,113,114\nCV 116,116,117\ns_and_b32 s78, s74, 3\ns_lshl2_add_u32 s78, s73, s78\n"
"s_lshl_b32 s78, s78, 12\ns_bitcmp1_b32 s11, 0\ns_cselect_b32 s79, s48, s45\ns_add_u32 s79, s79, 512\ns_and_b32 s79, s79, 0xfffe0000\ns_add_u32 s78, s78, s79\n"
"buffer_load_dword v31, v26, s[20:23], s78 offen\nMaaa 108,188,140\nv_and_b32 v117, 0xffff0000, v116\nMvaa 44,188,132\nv_lshlrev_b32 v116, 16, v116\nMvaa 44,184,128\n"
"s_waitcnt vmcnt(13) lgkmcnt(0)\ns_barrier\nMvaa 48,224,128\nRa 192,140,16384\ns_add_u32 m0, s47, -16384\ns_add_u32 s46, s52, 768\nMvaa 48,228,132\nRa 196,141,16384\n"
"Maaa 112,228,140\nRa 200,140,18432\nD0 136,28,46,0\nMaaa 112,224,136\nRa 204,141,18432\nMvaa 208,224,144\nRa 208,142,16384\nFM 120,112,115\nMvaa 208,228,148\nRa 212,143,16384\n"
"Mvaa 240,228,156\nRa 216,142,18432\nMvaa 240,224,152\nRa 220,143,18432\nD0 137,28,46,1024\nMvaa 244,232,152\nTa 160,12,0\nFM 121,113,115\nMvaa 244,236,156\nTa 162,12,2048\n"
"Mvaa 212,236,148\nTa 164,12,16384\nMvaa 212,232,144\nTa 166,12,18432\nMaaa 116,232,136\nTa 168,13,0\nD0 138,28,46,2048\nMaaa 116,236,140\nTa 170,13,2048\nMvaa 52,236,132\n"
"Ta 172,13,16384\nMvaa 52,232,128\nTa 174,13,18432\nMvaa 56,240,128\nTa 176,14,0\nAD 118,118,116\nMvaa 56,244,132\nTa 178,14,2048\nD0 139,28,46,3072\nMaaa 120,244,140\n"
"Ta 180,14,16384\nMaaa 120,240,136\nTa 182,14,18432\nMvaa 216,240,144\nTa 184,15,0\ns_add_u32 m0, s47, 65536\ns_add_u32 s46, s51, 3145728\nMvaa 216,244,148\nTa 186,15,2048\n"
"Mvaa 248,244,156\nTa 188,15,16384\nD0 6,32,46,0\nMvaa 248,240,152\nTa 190,15,18432\nMvaa 252,248,152\nAD 119,119,117\nv_and_b32 v112, 0xffff0000, v99\nMvaa 252,252,156\n"
"v_and_b32 v113, 0xffff0000, v103\nMvaa 220,252,148\nv_and_b32 v114, 0xffff0000, v107\nMvaa 220,248,144\nD0 7,32,46,1024\nMaaa 124,248,136\nv_and_b32 v115, 0xffff0000, v111\n"
"MU 116,112,114\nMaaa 124,252,140\nMU 117,113,114\nMvaa 60,252,132\nCV 116,116,117\nMvaa 60,248,128\nWL 0\nMvaa 32,160,192\nTa 224,16,0\nD0 8,32,46,2048\nMvaa 32,164,196\n"
"Ta 226,16,2048\nMaaa 96,164,204\nv_and_b32 v117, 0xffff0000, v116\nMaaa 96,160,200\nTa 228,16,16384\nMvaa 192,160,208\nTa 230,16,18432\nv_lshlrev_b32 v116, 16, v116\n"
"Mvaa 192,164,212\nD0 9,32,46,3072\nMvaa 224,164,220\nTa 232,17,0\nMvaa 224,160,216\nTa 234,17,2048\nMvaa 228,168,216\ns_add_u32 m0, s47, 81920\ns_add_u32 s46, s51, 3407872\n"
"Mvaa 228,172,220\nTa 236,17,16384\nMvaa 196,172,212\nTa 238,17,18432\nD0 6,32,46,0\nMvaa 196,168,208\nFM 120,112,115\nMaaa 100,168,200\nTa 240,18,0\nFM 121,113,115\n"
"Maaa 100,172,204\nTa 242,18,2048\nMvaa 36,172,196\nAD 118,118,116\nMvaa 36,168,192\nTa 244,18,16384\nD0 7,32,46,1024\nMvaa 40,176,192\nTa 246,18,18432\nAD 119,119,117\n"
"Mvaa 40,180,196\ns_nop 0\nv_permlane32_swap_b32 v118, v119\nMaaa 104,180,204\nTa 248,19,0\nMaaa 104,176,200\nTa 250,19,2048\nMvaa 200,176,208\nD0 8,32,46,2048\n"
"v_permlane32_swap_b32 v120, v121\nMvaa 200,180,212\nTa 252,19,16384\nMvaa 232,180,220\nTa 254,19,18432\nMvaa 232,176,216\nAD 118,118,119\nMvaa 236,184,216\nAD 120,120,121\n"
"Mvaa 236,188,220\nD0 9,32,46,3072\nMvaa 204,188,212\nv_permlane16_swap_b32 v118, v120\nMvaa 204,184,208\nAD 118,118,120\nMaaa 108,184,200\nMaaa 108,188,204\n"
"v_add_f32_dpp v118, v118, v118 row_ror:8 row_mask:0xf bank_mask:0xf\nMvaa 44,188,196\nMvaa 44,184,192\ns_waitcnt vmcnt(13) lgkmcnt(0)\ns_barrier\nMvaa 48,224,192\n"
"Ra 128,140,32768\ns_add_u32 m0, s47, -65536\ns_add_u32 s45, s45, 128\nMvaa 48,228,196\nRa 132,141,32768\nMaaa 112,228,204\nRa 136,140,34816\nD0 2,20,45,0\nMaaa 112,224,200\n"
"Ra 140,141,34816\nMvaa 208,224,208\nRa 144,142,32768\nv_add_f32_dpp v118, v118, v118 row_ror:4 row_mask:0xf bank_mask:0xf\nMvaa 208,228,212\nRa 148,143,32768\nMvaa 240,228,220\n"
"Ra 152,142,34816\nMvaa 240,224,216\nRa 156,143,34816\nD0 3,20,45,1024\nMvaa 244,232,216\nTa 160,12,32768\nv_add_f32_dpp v118, v118, v118 row_ror:2 row_mask:0xf bank_mask:0xf\n"
"Mvaa 244,236,220\nTa 162,12,34816\nMvaa 212,236,212\nTa 164,12,49152\nMvaa 212,232,208\nTa 166,12,51200\nMaaa 116,232,200\nTa 168,13,32768\nD0 4,20,45,2048\nMaaa 116,236,204\n"
"Ta 170,13,34816\nMvaa 52,236,196\nTa 172,13,49152\nMvaa 52,232,192\nTa 174,13,51200\nMvaa 56,240,192\nTa 176,14,32768\n"
"v_add_f32_dpp v118, v118, v118 row_ror:1 row_mask:0xf bank_mask:0xf\nMvaa 56,244,196\nTa 178,14,34816\nD0 5,20,45,3072\nMaaa 120,244,204\nTa 180,14,49152\nMaaa 120,240,200\n"
"Ta 182,14,51200\nMvaa 216,240,208\nTa 184,15,32768\ns_add_u32 m0, s47, 0\ns_add_u32 s44, s44, 0x40000\nMvaa 216,244,212\nTa 186,15,34816\nMvaa 248,244,220\nTa 188,15,49152\n"
"D0 6,24,44,0\nMvaa 248,240,216\nTa 190,15,51200\nMvaa 252,248,216\nCV 118,118,118\nv_lshlrev_b32 v118, 16, v118\nMvaa 252,252,220\nv_mul_f32 v119, 0xbfb8aa3b, v118\nEX 119,119\n"
"Mvaa 220,252,212\nv_add_f32 v120, 0, v118\nMvaa 220,248,208\nD0 7,24,44,1024\nMaaa 124,248,200\nv_add_f32 v119, 1.0, v119\nMaaa 124,252,204\nv_rcp_f32 v119, v119\nMvaa 60,252,196\n"
"v_ashrrev_i32 v121, 31, v120\nMvaa 60,248,192\nWL 0\nMvaa 32,160,128\nTa 224,16,32768\nD0 8,24,44,2048\nMvaa 32,164,132\nTa 226,16,34816\nMaaa 96,164,140\nCV 119,119,119\n"
"Maaa 96,160,136\nTa 228,16,49152\nMvaa 192,160,144\nTa 230,16,51200\nv_lshlrev_b32 v119, 16, v119\nMvaa 192,164,148\nD0 9,24,44,3072\nMvaa 224,164,156\nTa 232,17,32768\n"
"Mvaa 224,160,152\nTa 234,17,34816\nMvaa 228,168,152\ns_add_u32 m0, s47, 16384\ns_add_u32 s44, s44, 0x40000\nMvaa 228,172,156\nTa 236,17,49152\nMvaa 196,172,148\nTa 238,17,51200\n"
"D0 6,24,44,0\nMvaa 196,168,144\nv_or_b32 v121, 0x80000000, v121\nMaaa 100,168,136\nTa 240,18,32768\nv_xor_b32 v120, v120, v121\nMaaa 100,172,140\nTa 242,18,34816\nMvaa 36,172,132\n"
"v_and_b32 v120, 0xffff0000, v120\nMvaa 36,168,128\nTa 244,18,49152\nD0 7,24,44,1024\nMvaa 40,176,128\nTa 246,18,51200\ns_lshl2_add_u32 s2, s10, s11\nMvaa 40,180,132\n"
"v_lshrrev_b32 v112, 5, v0\nMaaa 104,180,140\nTa 248,19,32768\nMaaa 104,176,136\nTa 250,19,34816\nMvaa 200,176,144\nD0 8,24,44,2048\nv_lshl_add_u32 v112, s2, 1, v112\n"
"Mvaa 200,180,148\nTa 252,19,49152\nMvaa 232,180,156\nTa 254,19,51200\nMvaa 232,176,152\nv_sub_u32 v113, 0xffff, v112\nMvaa 236,184,152\nv_or_b32 v120, v120, v113\n"
"v_mov_b32 v121, s15\nMvaa 236,188,156\nD0 9,24,44,3072\nMvaa 204,188,148\nv_and_b32 v113, 31, v0\nMvaa 204,184,144\nv_mov_b32 v116, 0x40000000\nv_cmp_eq_u32 vcc, 16, v113\n"
"v_lshlrev_b32 v114, 2, v112\nv_lshlrev_b32 v115, 10, v113\nv_cndmask_b32 v114, v116, v114, vcc\nv_cndmask_b32 v120, v119, v120, vcc\nMaaa 108,184,136\n"
"v_lshl_add_u32 v115, v112, 3, v115\nv_and_b32 v113, 15, v0\nMaaa 108,188,140\nv_cmp_eq_u32 vcc, 0, v113\ns_mov_b32 s2, 0xc040000\ns_mov_b32 s3, 0xc03e000\n"
"v_cndmask_b32 v115, v116, v115, vcc\nbuffer_store_dwordx2 v[120:121], v115, s[16:19], s2 offen sc0 sc1\nbuffer_store_dword v118, v114, s[16:19], s3 offen sc0 sc1\nMvaa 44,188,132\n"
"Mvaa 44,184,128\ns_waitcnt vmcnt(14) lgkmcnt(0)\ns_barrier\nMvaa 48,224,128\nRa 192,140,49152\ns_add_u32 m0, s47, -49152\ns_add_u32 s48, s48, 128\nMvaa 48,228,132\n"
"Ra 196,141,49152\nMaaa 112,228,140\nRa 200,140,51200\nD0 2,20,48,0\nMaaa 112,224,136\nRa 204,141,51200\nMvaa 208,224,144\nRa 208,142,49152\nMvaa 208,228,148\nRa 212,143,49152\n"
"Mvaa 240,228,156\nRa 216,142,51200\nMvaa 240,224,152\nRa 220,143,51200\nD0 3,20,48,1024\nMvaa 244,232,152\nTa 160,128,0\nMvaa 244,236,156\nTa 162,128,2048\nMvaa 212,236,148\n"
"Ta 164,128,16384\nMvaa 212,232,144\nTa 166,128,18432\nMaaa 116,232,136\nTa 168,129,0\nD0 4,20,48,2048\nMaaa 116,236,140\nTa 170,129,2048\nMvaa 52,236,132\nTa 172,129,16384\n"
"Mvaa 52,232,128\nTa 174,129,18432\nMvaa 56,240,128\nTa 176,130,0\nMvaa 56,244,132\nTa 178,130,2048\nD0 5,20,48,3072\nMaaa 120,244,140\nTa 180,130,16384\nMaaa 120,240,136\n"
"Ta 182,130,18432\nMvaa 216,240,144\nTa 184,131,0\ns_add_u32 m0, s47, -32768\ns_add_u32 s45, s45, 128\nMvaa 216,244,148\nTa 186,131,2048\nMvaa 248,244,156\nTa 188,131,16384\n"
"D0 2,20,45,0\nMvaa 248,240,152\nTa 190,131,18432\nMvaa 252,248,152\nMvaa 252,252,156\nMvaa 220,252,148\nMvaa 220,248,144\nD0 3,20,45,1024\nMaaa 124,248,136\nMaaa 124,252,140\n"
"Mvaa 60,252,132\nMvaa 60,248,128\nWL 0\nMvaa 32,160,192\nTa 224,132,0\nD0 4,20,45,2048\nMvaa 32,164,196\nTa 226,132,2048\nMvaa 36,172,196\nMvaa 36,168,192\nTa 228,132,16384\n"
"Mvaa 40,176,192\nTa 230,132,18432\nMvaa 40,180,196\nD0 5,20,45,3072\nMvaa 44,188,196\nTa 232,133,0\nMvaa 44,184,192\nTa 234,133,2048\nMaaa 108,184,200\ns_add_u32 m0, s47, 32768\n"
"s_add_u32 s44, s44, 0x40000\nMaaa 108,188,204\nTa 236,133,16384\nMaaa 104,180,204\nTa 238,133,18432\nD0 6,24,44,0\nMaaa 104,176,200\nv_mul_f32 v32, s57, v32\n"
"v_mul_f32 v33, s57, v33\nMaaa 100,168,200\nTa 240,134,0\nCV 32,32,33\nMaaa 100,172,204\nTa 242,134,2048\nv_mul_f32 v34, s57, v34\nMaaa 96,164,204\nv_mul_f32 v35, s57, v35\n"
"CV 33,34,35\nMaaa 96,160,200\nTa 244,134,16384\nD0 7,24,44,1024\nMvaa 192,160,208\nTa 246,134,18432\nv_mul_f32 v36, s57, v36\nMvaa 192,164,212\nv_mul_f32 v37, s57, v37\n"
"Mvaa 196,172,212\nTa 248,135,0\nCV 34,36,37\nMvaa 196,168,208\nTa 250,135,2048\nv_mul_f32 v38, s57, v38\nMvaa 200,176,208\nD0 8,24,44,2048\nv_mul_f32 v39, s57, v39\n"
"Mvaa 200,180,212\nTa 252,135,16384\nCV 35,38,39\nMvaa 204,188,212\nTa 254,135,18432\nv_mul_f32 v40, s57, v40\nMvaa 204,184,208\nv_mul_f32 v41, s57, v41\nCV 40,40,41\n"
"Mvaa 236,184,216\nv_mul_f32 v42, s57, v42\nv_mul_f32 v43, s57, v43\nMvaa 236,188,220\nD0 9,24,44,3072\nMvaa 232,180,220\nCV 41,42,43\nv_mul_f32 v44, s57, v44\nMvaa 232,176,216\n"
"v_mul_f32 v45, s57, v45\nCV 42,44,45\nMvaa 228,168,216\nv_mul_f32 v46, s57, v46\nv_mul_f32 v47, s57, v47\nMvaa 228,172,220\nCV 43,46,47\nAR 20,96\nMvaa 224,164,220\nAR 21,97\n"
"v_mul_f32 v20, s57, v20\nMvaa 224,160,216\ns_waitcnt vmcnt(14) lgkmcnt(0)\ns_barrier\nMvaa 48,224,192\ns_add_u32 m0, s47, 49152\ns_add_u32 s44, s44, 0x40000\nRa 128,10,0\n"
"Mvaa 48,228,196\nRa 132,11,0\nMvaa 52,236,196\nD0 6,24,44,0\nRa 136,10,2048\n.W548:\ns_cbranch_execz .W552\nMvaa 52,232,192\nRa 140,11,2048\nv_mul_f32 v21, s57, v21\n"
"Mvaa 56,240,192\nRa 144,10,8192\nCV 36,20,21\nMvaa 56,244,196\nRa 148,11,8192\nAR 20,98\nMvaa 60,252,196\nD0 7,24,44,1024\nRa 152,10,10240\nMvaa 60,248,192\nRa 156,11,10240\n"
"AR 21,99\nMaaa 124,248,200\nTa 160,12,0\nv_mul_f32 v20, s57, v20\nMaaa 124,252,204\nTa 162,12,2048\nMaaa 120,244,204\nD0 8,24,44,2048\nTa 164,12,16384\nMaaa 120,240,200\n"
"Ta 166,12,18432\nv_mul_f32 v21, s57, v21\nMaaa 116,232,200\nTa 168,13,0\nCV 37,20,21\nMaaa 116,236,204\nTa 170,13,2048\nAR 20,100\nMaaa 112,228,204\nD0 9,24,44,3072\n"
"Ta 172,13,16384\nMaaa 112,224,200\nTa 174,13,18432\nAR 21,101\nMvaa 208,224,208\ns_add_u32 m0, s47, -16384\ns_add_u32 s48, s48, 128\nTa 176,14,0\nMvaa 208,228,212\nTa 178,14,2048\n"
"Mvaa 212,236,212\nD0 2,20,48,0\nTa 180,14,16384\nMvaa 212,232,208\nTa 182,14,18432\nv_mul_f32 v20, s57, v20\nMvaa 216,240,208\nTa 184,15,0\nv_mul_f32 v21, s57, v21\n"
"Mvaa 216,244,212\nTa 186,15,2048\nCV 38,20,21\nMvaa 220,252,212\nD0 3,20,48,1024\nTa 188,15,16384\nMvaa 220,248,208\nTa 190,15,18432\nAR 20,102\nMvaa 252,248,216\nAR 21,103\n"
"v_mul_f32 v20, s57, v20\nMvaa 252,252,220\nv_mul_f32 v21, s57, v21\nMvaa 248,244,220\nD0 4,20,48,2048\nCV 39,20,21\nMvaa 248,240,216\nAR 20,104\nAR 21,105\nMvaa 244,232,216\n"
"v_mul_f32 v20, s57, v20\nv_mul_f32 v21, s57, v21\nMvaa 244,236,220\nCV 44,20,21\nAR 20,106\nMvaa 240,228,220\nD0 5,20,48,3072\nAR 21,107\nMvaa 240,224,216\nWL 0\ns_barrier\n"
"Zvaa 64,128,160\nTa 224,16,0\nv_mul_f32 v20, s57, v20\nMvaa 64,132,164\nTa 226,16,2048\nZvaa 68,140,164\ns_add_u32 m0, s47, -65536\ns_add_u32 s45, s45, 128\nMvaa 68,136,160\n"
"Ta 228,16,16384\nv_mul_f32 v21, s57, v21\nZvaa 128,144,160\nTa 230,16,18432\nCV 45,20,21\nMvaa 128,148,164\nD0 2,20,45,0\nAR 20,108\nZvaa 132,156,164\nTa 232,17,0\nAR 21,109\n"
"Mvaa 132,152,160\nTa 234,17,2048\nv_mul_f32 v20, s57, v20\nZvaa 140,152,168\nv_mul_f32 v21, s57, v21\nCV 46,20,21\nMvaa 140,156,172\nTa 236,17,16384\ns_add_u32 s6, s56, 0\n"
"Sv 32,1,16,6,0\nZvaa 136,148,172\nTa 238,17,18432\nAR 20,110\nMvaa 136,144,168\nAR 21,111\nv_mul_f32 v20, s57, v20\nZvaa 76,136,168\nTa 240,18,0\nv_mul_f32 v21, s57, v21\n"
"Mvaa 76,140,172\nTa 242,18,2048\nCV 47,20,21\nZvaa 72,132,172\nD0 3,20,45,1024\nv_mul_f32 v48, s57, v48\nMvaa 72,128,168\nTa 244,18,16384\nv_mul_f32 v49, s57, v49\n"
"Zvaa 80,128,176\nTa 246,18,18432\nCV 48,48,49\nMvaa 80,132,180\nv_mul_f32 v50, s57, v50\nZvaa 84,140,180\nTa 248,19,0\nv_mul_f32 v51, s57, v51\nMvaa 84,136,176\nTa 250,19,2048\n"
"CV 49,50,51\nZvaa 144,144,176\nv_mul_f32 v52, s57, v52\nv_mul_f32 v53, s57, v53\nMvaa 144,148,180\nTa 252,19,16384\nCV 50,52,53\nZvaa 148,156,180\nTa 254,19,18432\n"
"v_mul_f32 v54, s57, v54\nMvaa 148,152,176\nD0 4,20,45,2048\nv_mul_f32 v55, s57, v55\nZvaa 156,152,184\nCV 51,54,55\nv_mul_f32 v56, s57, v56\nMvaa 156,156,188\n"
"v_mul_f32 v57, s57, v57\ns_add_u32 s6, s56, 0\nSv 40,1,16,6,2048\nZvaa 152,148,188\nCV 56,56,57\nv_mul_f32 v58, s57, v58\nMvaa 152,144,184\nv_mul_f32 v59, s57, v59\nCV 57,58,59\n"
"Zvaa 92,136,184\nv_mul_f32 v60, s57, v60\nv_mul_f32 v61, s57, v61\nMvaa 92,140,188\nD0 5,20,45,3072\nCV 58,60,61\nZvaa 88,132,188\nv_mul_f32 v62, s57, v62\n"
"v_mul_f32 v63, s57, v63\nMvaa 88,128,184\ns_waitcnt vmcnt(18) lgkmcnt(0)\ns_barrier\nZvaa 96,128,224\nRa 192,10,16384\nCV 59,62,63\nMvaa 96,132,228\ns_add_u32 m0, s47, 0\n"
"s_add_u32 s44, s44, 0x40000\nZvaa 100,140,228\nRa 196,11,16384\nAR 20,112\nMvaa 100,136,224\nAR 21,113\nv_mul_f32 v20, s57, v20\nZvaa 160,144,224\nRa 200,10,18432\n"
"v_mul_f32 v21, s57, v21\nMvaa 160,148,228\nD0 6,24,44,0\nCV 52,20,21\nZvaa 164,156,228\nRa 204,11,18432\nAR 20,114\nMvaa 164,152,224\nAR 21,115\nv_mul_f32 v20, s57, v20\n"
"Zvaa 172,152,232\nRa 208,10,24576\nv_mul_f32 v21, s57, v21\nMvaa 172,156,236\nCV 53,20,21\ns_add_u32 s6, s56, 0\nSv 36,1,16,6,1024\nZvaa 168,148,236\nRa 212,11,24576\nAR 20,116\n"
"Mvaa 168,144,232\nAR 21,117\nv_mul_f32 v20, s57, v20\nZvaa 108,136,232\nRa 216,10,26624\nv_mul_f32 v21, s57, v21\nMvaa 108,140,236\nD0 7,24,44,1024\nCV 54,20,21\nZvaa 104,132,236\n"
"Ra 220,11,26624\nAR 20,118\nMvaa 104,128,232\nAR 21,119\nv_mul_f32 v20, s57, v20\nZvaa 112,128,240\nv_mul_f32 v21, s57, v21\nCV 55,20,21\nMvaa 112,132,244\nAR 20,120\n"
"Zvaa 116,140,244\nAR 21,121\nv_mul_f32 v20, s57, v20\nMvaa 116,136,240\nv_mul_f32 v21, s57, v21\nCV 60,20,21\nZvaa 176,144,240\nAR 20,122\nAR 21,123\nMvaa 176,148,244\n"
"D0 8,24,44,2048\nv_mul_f32 v20, s57, v20\nZvaa 180,156,244\nv_mul_f32 v21, s57, v21\nCV 61,20,21\nMvaa 180,152,240\nAR 20,124\nAR 21,125\nZvaa 188,152,248\n"
"v_mul_f32 v20, s57, v20\nv_mul_f32 v21, s57, v21\nMvaa 188,156,252\nCV 62,20,21\ns_add_u32 s6, s56, 0\nSv 44,1,16,6,3072\nZvaa 184,148,252\nAR 20,126\nAR 21,127\nMvaa 184,144,248\n"
"v_mul_f32 v20, s57, v20\nv_mul_f32 v21, s57, v21\nZvaa 124,136,248\nCV 63,20,21\nv_mul_f32 v192, s57, v192\nMvaa 124,140,252\nD0 9,24,44,3072\nv_mul_f32 v193, s57, v193\n"
"Zvaa 120,132,252\nCV 192,192,193\nv_mul_f32 v194, s57, v194\nMvaa 120,128,248\nWL 0\nZaaa 32,192,224\nRa 128,10,32768\nv_mul_f32 v195, s57, v195\nMaaa 32,196,228\n"
"s_add_u32 m0, s47, 16384\ns_add_u32 s44, s44, 0x40000\nZaaa 36,204,228\nRa 132,11,32768\nCV 193,194,195\nMaaa 36,200,224\nv_mul_f32 v196, s57, v196\nv_mul_f32 v197, s57, v197\n"
"Zaaa 96,208,224\nRa 136,10,34816\nCV 194,196,197\nMaaa 96,212,228\nD0 6,24,44,0\nv_mul_f32 v198, s57, v198\nZaaa 100,220,228\nRa 140,11,34816\nv_mul_f32 v199, s57, v199\n"
"Maaa 100,216,224\nCV 195,198,199\nv_mul_f32 v200, s57, v200\nZaaa 108,216,232\nRa 144,10,40960\nv_mul_f32 v201, s57, v201\nMaaa 108,220,236\nCV 200,200,201\n"
"v_mul_f32 v202, s57, v202\ns_add_u32 s6, s56, 4096\nSv 48,1,16,6,0\nZaaa 104,212,236\nRa 148,11,40960\nv_mul_f32 v203, s57, v203\nMaaa 104,208,232\nCV 201,202,203\n"
"v_mul_f32 v204, s57, v204\nZaaa 44,200,232\nRa 152,10,43008\nv_mul_f32 v205, s57, v205\nMaaa 44,204,236\nD0 7,24,44,1024\nCV 202,204,205\nZaaa 40,196,236\nRa 156,11,43008\n"
"v_mul_f32 v206, s57, v206\nMaaa 40,192,232\nv_mul_f32 v207, s57, v207\nCV 203,206,207\nZaaa 48,192,240\nv_mul_f32 v208, s57, v208\nv_mul_f32 v209, s57, v209\nMaaa 48,196,244\n"
"CV 208,208,209\nv_mul_f32 v210, s57, v210\nZaaa 52,204,244\nv_mul_f32 v211, s57, v211\nCV 209,210,211\nMaaa 52,200,240\nv_mul_f32 v212, s57, v212\nv_mul_f32 v213, s57, v213\n"
"Zaaa 112,208,240\nCV 210,212,213\nv_mul_f32 v214, s57, v214\nMaaa 112,212,244\nD0 8,24,44,2048\nv_mul_f32 v215, s57, v215\nZaaa 116,220,244\nCV 211,214,215\n"
"v_mul_f32 v216, s57, v216\nMaaa 116,216,240\nv_mul_f32 v217, s57, v217\nCV 216,216,217\nZaaa 124,216,248\nv_mul_f32 v218, s57, v218\nv_mul_f32 v219, s57, v219\nMaaa 124,220,252\n"
"CV 217,218,219\nv_mul_f32 v220, s57, v220\ns_add_u32 s6, s56, 4096\nSv 56,1,16,6,2048\nZaaa 120,212,252\nv_mul_f32 v221, s57, v221\nCV 218,220,221\nMaaa 120,208,248\n"
"v_mul_f32 v222, s57, v222\nv_mul_f32 v223, s57, v223\nZaaa 60,200,248\nCV 219,222,223\nv_mul_f32 v224, s57, v224\nMaaa 60,204,252\nD0 9,24,44,3072\nv_mul_f32 v225, s57, v225\n"
"Zaaa 56,196,252\nCV 224,224,225\nv_mul_f32 v226, s57, v226\nMaaa 56,192,248\ns_waitcnt vmcnt(22) lgkmcnt(0)\ns_barrier\nZaaa 0,192,160\nTa 224,16,32768\nv_mul_f32 v227, s57, v227\n"
"Maaa 0,196,164\nTa 226,16,34816\nCV 225,226,227\nZaaa 4,204,164\ns_add_u32 m0, s47, -49152\ns_add_u32 s48, s48, 128\nMaaa 4,200,160\nTa 228,16,49152\nv_mul_f32 v228, s57, v228\n"
"Zaaa 64,208,160\nTa 230,16,51200\nv_mul_f32 v229, s57, v229\nMaaa 64,212,164\nD0 2,20,48,0\nCV 226,228,229\nZaaa 68,220,164\nTa 232,17,32768\nv_mul_f32 v230, s57, v230\n"
"Maaa 68,216,160\nTa 234,17,34816\nv_mul_f32 v231, s57, v231\nZaaa 76,216,168\nCV 227,230,231\nv_mul_f32 v232, s57, v232\nMaaa 76,220,172\nTa 236,17,49152\n"
"v_mul_f32 v233, s57, v233\ns_add_u32 s6, s56, 4096\nSv 52,1,16,6,1024\nZaaa 72,212,172\nTa 238,17,51200\nCV 232,232,233\nMaaa 72,208,168\nv_mul_f32 v234, s57, v234\n"
"v_mul_f32 v235, s57, v235\nZaaa 12,200,168\nTa 240,18,32768\nCV 233,234,235\nMaaa 12,204,172\nTa 242,18,34816\nv_mul_f32 v236, s57, v236\nZaaa 8,196,172\nD0 3,20,48,1024\n"
"v_mul_f32 v237, s57, v237\nMaaa 8,192,168\nTa 244,18,49152\nCV 234,236,237\nZaaa 16,192,176\nTa 246,18,51200\nv_mul_f32 v238, s57, v238\nMaaa 16,196,180\n"
"v_mul_f32 v239, s57, v239\nCV 235,238,239\nZaaa 20,204,180\nTa 248,19,32768\nv_mul_f32 v240, s57, v240\nMaaa 20,200,176\nTa 250,19,34816\nv_mul_f32 v241, s57, v241\n"
"Zaaa 80,208,176\nCV 240,240,241\nv_mul_f32 v242, s57, v242\nMaaa 80,212,180\nTa 252,19,49152\nv_mul_f32 v243, s57, v243\nZaaa 84,220,180\nTa 254,19,51200\nCV 241,242,243\n"
"Maaa 84,216,176\nD0 4,20,48,2048\nv_mul_f32 v244, s57, v244\nZaaa 92,216,184\nv_mul_f32 v245, s57, v245\nCV 242,244,245\nMaaa 92,220,188\nv_mul_f32 v246, s57, v246\n"
"v_mul_f32 v247, s57, v247\ns_add_u32 s6, s56, 4096\nSv 60,1,16,6,3072\nZaaa 88,212,188\nCV 243,246,247\nv_mul_f32 v248, s57, v248\nMaaa 88,208,184\nv_mul_f32 v249, s57, v249\n"
"CV 248,248,249\nZaaa 28,200,184\nv_mul_f32 v250, s57, v250\nv_mul_f32 v251, s57, v251\nMaaa 28,204,188\nD0 5,20,48,3072\nCV 249,250,251\nZaaa 24,196,188\n"
"v_mul_f32 v252, s57, v252\nv_mul_f32 v253, s57, v253\nMaaa 24,192,184\nWL 0\nMvaa 96,128,224\nTa 160,12,32768\nCV 250,252,253\nMvaa 96,132,228\nTa 162,12,34816\n"
"v_mul_f32 v254, s57, v254\nMvaa 100,140,228\ns_add_u32 m0, s47, -32768\ns_add_u32 s45, s45, 128\nMvaa 100,136,224\nTa 164,12,49152\nv_mul_f32 v255, s57, v255\nMvaa 160,144,224\n"
"Ta 166,12,51200\nCV 251,254,255\nMvaa 160,148,228\nD0 2,20,45,0\nMvaa 164,156,228\nTa 168,13,32768\nMvaa 164,152,224\nTa 170,13,34816\nMvaa 172,152,232\nMvaa 172,156,236\n"
"Ta 172,13,49152\ns_add_u32 s6, s53, 0\nSv 192,1,16,6,0\nMvaa 168,148,236\nTa 174,13,51200\nMvaa 168,144,232\nMvaa 108,136,232\nTa 176,14,32768\nMvaa 108,140,236\nTa 178,14,34816\n"
"Mvaa 104,132,236\nD0 3,20,45,1024\nMvaa 104,128,232\nTa 180,14,49152\nMvaa 112,128,240\nTa 182,14,51200\nMvaa 112,132,244\nMvaa 116,140,244\nTa 184,15,32768\nMvaa 116,136,240\n"
"Ta 186,15,34816\nMvaa 176,144,240\nMvaa 176,148,244\nTa 188,15,49152\nMvaa 180,156,244\nTa 190,15,51200\nMvaa 180,152,240\nD0 4,20,45,2048\nMvaa 188,152,248\nMvaa 188,156,252\n"
"s_add_u32 s6, s53, 0\nSv 200,1,16,6,2048\nMvaa 184,148,252\nMvaa 184,144,248\nMvaa 124,136,248\nMvaa 124,140,252\nD0 5,20,45,3072\nMvaa 120,132,252\nMvaa 120,128,248\n"
"s_waitcnt vmcnt(24) lgkmcnt(0)\ns_barrier\nMvaa 64,128,160\nRa 192,10,49152\nMvaa 64,132,164\ns_add_u32 m0, s47, 32768\ns_add_u32 s44, s44, 0x40000\nMvaa 68,140,164\n"
"Ra 196,11,49152\nMvaa 68,136,160\nMvaa 128,144,160\nRa 200,10,51200\nMvaa 128,148,164\nD0 6,24,44,0\nMvaa 132,156,164\nRa 204,11,51200\nMvaa 132,152,160\nMvaa 140,152,168\n"
"Ra 208,10,57344\nMvaa 140,156,172\ns_add_u32 s6, s53, 4096\nSv 208,1,16,6,0\nMvaa 136,148,172\nRa 212,11,57344\nMvaa 136,144,168\nMvaa 76,136,168\nRa 216,10,59392\n"
"Mvaa 76,140,172\nD0 7,24,44,1024\nMvaa 72,132,172\nRa 220,11,59392\nMvaa 72,128,168\nMvaa 80,128,176\nMvaa 80,132,180\nMvaa 84,140,180\nMvaa 84,136,176\nMvaa 144,144,176\n"
"Mvaa 144,148,180\n.W549:\ns_cbranch_execz .W553\nD0 8,24,44,2048\nMvaa 148,156,180\nMvaa 148,152,176\nMvaa 156,152,184\nMvaa 156,156,188\ns_add_u32 s6, s53, 4096\n"
"Sv 216,1,16,6,2048\nMvaa 152,148,188\nMvaa 152,144,184\nMvaa 92,136,184\nMvaa 92,140,188\nD0 9,24,44,3072\nMvaa 88,132,188\nMvaa 88,128,184\nWL 0\nMaaa 0,192,160\nRa 128,10,0\n"
"Maaa 0,196,164\ns_add_u32 m0, s47, 49152\ns_add_u32 s44, s44, 0x40000\nMaaa 4,204,164\nRa 132,11,0\nMaaa 4,200,160\nMaaa 64,208,160\nRa 136,10,2048\nMaaa 64,212,164\nD0 6,24,44,0\n"
"Maaa 68,220,164\nRa 140,11,2048\nMaaa 68,216,160\nMaaa 76,216,168\nRa 144,10,8192\nMaaa 76,220,172\ns_add_u32 s6, s53, 0\nSv 224,1,16,6,1024\nMaaa 72,212,172\nRa 148,11,8192\n"
"Maaa 72,208,168\nMaaa 12,200,168\nRa 152,10,10240\nMaaa 12,204,172\nD0 7,24,44,1024\nMaaa 8,196,172\nRa 156,11,10240\nMaaa 8,192,168\nMaaa 16,192,176\nMaaa 16,196,180\n"
"Maaa 20,204,180\nMaaa 20,200,176\nMaaa 80,208,176\nMaaa 80,212,180\nD0 8,24,44,2048\nMaaa 84,220,180\nMaaa 84,216,176\nMaaa 92,216,184\nMaaa 92,220,188\ns_add_u32 s6, s53, 0\n"
"Sv 232,1,16,6,3072\nMaaa 88,212,188\nMaaa 88,208,184\nMaaa 28,200,184\nMaaa 28,204,188\nD0 9,24,44,3072\nMaaa 24,196,188\nMaaa 24,192,184\ns_waitcnt vmcnt(24) lgkmcnt(0)\n"
"s_barrier\nMaaa 32,192,224\nTa 160,12,0\nMaaa 32,196,228\nTa 162,12,2048\nMaaa 36,204,228\ns_add_u32 m0, s47, -16384\ns_add_u32 s48, s48, 128\nMaaa 36,200,224\nTa 164,12,16384\n"
"Maaa 96,208,224\nTa 166,12,18432\nMaaa 96,212,228\nD0 2,20,48,0\nMaaa 100,220,228\nTa 168,13,0\nMaaa 100,216,224\nTa 170,13,2048\nMaaa 108,216,232\nMaaa 108,220,236\n"
"Ta 172,13,16384\ns_add_u32 s6, s53, 4096\nSv 240,1,16,6,1024\nMaaa 104,212,236\nTa 174,13,18432\nMaaa 104,208,232\nMaaa 44,200,232\nTa 176,14,0\nMaaa 44,204,236\nTa 178,14,2048\n"
"Maaa 40,196,236\nD0 3,20,48,1024\nMaaa 40,192,232\nTa 180,14,16384\nMaaa 48,192,240\nTa 182,14,18432\nMaaa 48,196,244\ns_add_u32 s6, s53, 4096\nSv 248,1,16,6,3072\n"
"Maaa 52,204,244\nTa 184,15,0\nMaaa 52,200,240\nTa 186,15,2048\nMaaa 112,208,240\nMaaa 112,212,244\nTa 188,15,16384\nMaaa 116,220,244\nTa 190,15,18432\nMaaa 116,216,240\n"
"D0 4,20,48,2048\nMaaa 124,216,248\nMaaa 124,220,252\nMaaa 120,212,252\nMaaa 120,208,248\ns_lshl3_add_u32 s90, s12, s13\ns_lshl2_add_u32 s90, s90, s11\n"
"s_lshl2_add_u32 s90, s90, 0xc04a400\nMaaa 60,200,248\nMaaa 60,204,252\nD0 5,20,48,3072\nMaaa 56,196,252\nMaaa 56,192,248\nWL 0\ns_mov_b64 s[76:77], s[62:63]\n"
"s_mov_b64 s[78:79], s[64:65]\ns_mov_b32 s42, 1605632\ns_mov_b32 s50, 1\n.GL150:\nMvaa 64,128,160\nTa 224,16,0\nMvaa 64,132,164\nTa 226,16,2048\nMvaa 68,140,164\n"
"s_add_u32 m0, s47, -65536\ns_add_u32 s45, s45, 128\nMvaa 68,136,160\nTa 228,16,16384\nMvaa 128,144,160\nTa 230,16,18432\nMvaa 128,148,164\nD0 2,20,45,0\nMvaa 132,156,164\n"
"Ta 232,17,0\nMvaa 132,152,160\nTa 234,17,2048\nMvaa 140,152,168\nMvaa 140,156,172\nTa 236,17,16384\nMvaa 136,148,172\nTa 238,17,18432\nMvaa 136,144,168\nMvaa 76,136,168\n"
"Ta 240,18,0\nMvaa 76,140,172\nTa 242,18,2048\nMvaa 72,132,172\nD0 3,20,45,1024\nMvaa 72,128,168\nTa 244,18,16384\nMvaa 80,128,176\nTa 246,18,18432\nMvaa 80,132,180\n"
"Mvaa 84,140,180\nTa 248,19,0\nMvaa 84,136,176\nTa 250,19,2048\nMvaa 144,144,176\nMvaa 144,148,180\nTa 252,19,16384\nMvaa 148,156,180\nTa 254,19,18432\nMvaa 148,152,176\n"
"D0 4,20,45,2048\nMvaa 156,152,184\nMvaa 156,156,188\nMvaa 152,148,188\nMvaa 152,144,184\ns_mov_b32 s40, s76\ns_and_b32 s41, s77, 0xffff\nMvaa 92,136,184\nMvaa 92,140,188\n"
"D0 5,20,45,3072\nMvaa 88,132,188\nMvaa 88,128,184\ns_waitcnt vmcnt(17) lgkmcnt(0)\ns_barrier\nMvaa 96,128,224\nRa 192,10,16384\nMvaa 96,132,228\ns_add_u32 m0, s47, 0\n"
"s_add_u32 s44, s44, 0x40000\nbuffer_load_dword v31, v26, s[40:43], s97 offen\ns_mov_b64 s[76:77], s[66:67]\nMvaa 100,140,228\nRa 196,11,16384\nMvaa 100,136,224\nMvaa 160,144,224\n"
"Ra 200,10,18432\nMvaa 160,148,228\nD0 6,24,44,0\nMvaa 164,156,228\nRa 204,11,18432\nMvaa 164,152,224\nMvaa 172,152,232\nRa 208,10,24576\nMvaa 172,156,236\nMvaa 168,148,236\n"
"Ra 212,11,24576\nMvaa 168,144,232\nMvaa 108,136,232\nRa 216,10,26624\nMvaa 108,140,236\nD0 7,24,44,1024\nMvaa 104,132,236\nRa 220,11,26624\nMvaa 104,128,232\nMvaa 112,128,240\n"
"Mvaa 112,132,244\nMvaa 116,140,244\nMvaa 116,136,240\nMvaa 176,144,240\nMvaa 176,148,244\nD0 8,24,44,2048\nMvaa 180,156,244\nMvaa 180,152,240\nMvaa 188,152,248\nMvaa 188,156,252\n"
"Mvaa 184,148,252\nMvaa 184,144,248\nMvaa 124,136,248\nMvaa 124,140,252\nD0 9,24,44,3072\nMvaa 120,132,252\nMvaa 120,128,248\nWL 0\nMaaa 32,192,224\nRa 128,10,32768\n"
"Maaa 32,196,228\ns_add_u32 m0, s47, 16384\ns_add_u32 s44, s44, 0x40000\nMaaa 36,204,228\nRa 132,11,32768\nMaaa 36,200,224\nMaaa 96,208,224\nRa 136,10,34816\nMaaa 96,212,228\n"
"D0 6,24,44,0\nMaaa 100,220,228\nRa 140,11,34816\nMaaa 100,216,224\nMaaa 108,216,232\nRa 144,10,40960\nMaaa 108,220,236\nMaaa 104,212,236\nRa 148,11,40960\nMaaa 104,208,232\n"
"Maaa 44,200,232\nRa 152,10,43008\nMaaa 44,204,236\nD0 7,24,44,1024\nMaaa 40,196,236\nRa 156,11,43008\nMaaa 40,192,232\nMaaa 48,192,240\nMaaa 48,196,244\nMaaa 52,204,244\n"
"Maaa 52,200,240\nMaaa 112,208,240\nMaaa 112,212,244\nD0 8,24,44,2048\nMaaa 116,220,244\nMaaa 116,216,240\nMaaa 124,216,248\nMaaa 124,220,252\nMaaa 120,212,252\nMaaa 120,208,248\n"
"Maaa 60,200,248\nMaaa 60,204,252\nD0 9,24,44,3072\nMaaa 56,196,252\nMaaa 56,192,248\ns_waitcnt vmcnt(17) lgkmcnt(0)\ns_barrier\nMaaa 0,192,160\nTa 224,16,32768\nMaaa 0,196,164\n"
"Ta 226,16,34816\nMaaa 4,204,164\ns_add_u32 m0, s47, -49152\ns_add_u32 s48, s48, 128\nMaaa 4,200,160\nTa 228,16,49152\nMaaa 64,208,160\nTa 230,16,51200\nMaaa 64,212,164\n"
"D0 2,20,48,0\nMaaa 68,220,164\nTa 232,17,32768\nMaaa 68,216,160\nTa 234,17,34816\nMaaa 76,216,168\nMaaa 76,220,172\nTa 236,17,49152\nMaaa 72,212,172\nTa 238,17,51200\n"
"Maaa 72,208,168\nMaaa 12,200,168\nTa 240,18,32768\nMaaa 12,204,172\nTa 242,18,34816\nMaaa 8,196,172\nD0 3,20,48,1024\nMaaa 8,192,168\nTa 244,18,49152\nMaaa 16,192,176\n"
"Ta 246,18,51200\nMaaa 16,196,180\nMaaa 20,204,180\nTa 248,19,32768\nMaaa 20,200,176\nTa 250,19,34816\nMaaa 80,208,176\nMaaa 80,212,180\nTa 252,19,49152\nMaaa 84,220,180\n"
"Ta 254,19,51200\nMaaa 84,216,176\nD0 4,20,48,2048\nMaaa 92,216,184\nMaaa 92,220,188\nMaaa 88,212,188\nMaaa 88,208,184\nMaaa 28,200,184\nMaaa 28,204,188\nD0 5,20,48,3072\n"
"Maaa 24,196,188\nMaaa 24,192,184\nWL 0\nMvaa 96,128,224\nTa 160,12,32768\nMvaa 96,132,228\nTa 162,12,34816\nMvaa 100,140,228\ns_add_u32 m0, s47, -32768\ns_add_u32 s45, s45, 128\n"
"Mvaa 100,136,224\nTa 164,12,49152\nMvaa 160,144,224\nTa 166,12,51200\nMvaa 160,148,228\nD0 2,20,45,0\nMvaa 164,156,228\nTa 168,13,32768\nMvaa 164,152,224\nTa 170,13,34816\n"
"Mvaa 172,152,232\nMvaa 172,156,236\nTa 172,13,49152\nMvaa 168,148,236\nTa 174,13,51200\nMvaa 168,144,232\nMvaa 108,136,232\nTa 176,14,32768\nMvaa 108,140,236\nTa 178,14,34816\n"
"Mvaa 104,132,236\nD0 3,20,45,1024\nMvaa 104,128,232\nTa 180,14,49152\nMvaa 112,128,240\nTa 182,14,51200\nMvaa 112,132,244\nMvaa 116,140,244\nTa 184,15,32768\nMvaa 116,136,240\n"
"Ta 186,15,34816\nMvaa 176,144,240\nMvaa 176,148,244\nTa 188,15,49152\nMvaa 180,156,244\nTa 190,15,51200\nMvaa 180,152,240\nD0 4,20,45,2048\nMvaa 188,152,248\nMvaa 188,156,252\n"
"Mvaa 184,148,252\nMvaa 184,144,248\ns_mov_b32 s40, s78\ns_and_b32 s41, s79, 0xffff\nMvaa 124,136,248\nMvaa 124,140,252\nD0 5,20,45,3072\nMvaa 120,132,252\nMvaa 120,128,248\n"
"s_waitcnt vmcnt(17) lgkmcnt(0)\ns_barrier\nMvaa 64,128,160\nRa 192,10,49152\nMvaa 64,132,164\ns_add_u32 m0, s47, 32768\ns_add_u32 s44, s44, 0x40000\n"
"buffer_load_dword v31, v26, s[40:43], s97 offen\ns_mov_b64 s[78:79], s[66:67]\nMvaa 68,140,164\nRa 196,11,49152\nMvaa 68,136,160\nMvaa 128,144,160\nRa 200,10,51200\n"
"Mvaa 128,148,164\nD0 6,24,44,0\nMvaa 132,156,164\nRa 204,11,51200\nMvaa 132,152,160\nMvaa 140,152,168\nRa 208,10,57344\nMvaa 140,156,172\nMvaa 136,148,172\nRa 212,11,57344\n"
"Mvaa 136,144,168\nMvaa 76,136,168\nRa 216,10,59392\nMvaa 76,140,172\nD0 7,24,44,1024\nMvaa 72,132,172\nRa 220,11,59392\nMvaa 72,128,168\nMvaa 80,128,176\nMvaa 80,132,180\n"
"Mvaa 84,140,180\nMvaa 84,136,176\nMvaa 144,144,176\nMvaa 144,148,180\nD0 8,24,44,2048\nMvaa 148,156,180\nMvaa 148,152,176\nMvaa 156,152,184\nMvaa 156,156,188\nMvaa 152,148,188\n"
"Mvaa 152,144,184\nMvaa 92,136,184\nMvaa 92,140,188\nD0 9,24,44,3072\nMvaa 88,132,188\nMvaa 88,128,184\nWL 0\nMaaa 0,192,160\nRa 128,10,0\nMaaa 0,196,164\ns_add_u32 m0, s47, 49152\n"
"s_add_u32 s44, s44, 0x40000\nMaaa 4,204,164\nRa 132,11,0\nMaaa 4,200,160\nMaaa 64,208,160\nRa 136,10,2048\nMaaa 64,212,164\nD0 6,24,44,0\nMaaa 68,220,164\nRa 140,11,2048\n"
"Maaa 68,216,160\nMaaa 76,216,168\nRa 144,10,8192\nMaaa 76,220,172\nMaaa 72,212,172\nRa 148,11,8192\nMaaa 72,208,168\nMaaa 12,200,168\nRa 152,10,10240\nMaaa 12,204,172\n"
"D0 7,24,44,1024\nMaaa 8,196,172\nRa 156,11,10240\nMaaa 8,192,168\nMaaa 16,192,176\nMaaa 16,196,180\nMaaa 20,204,180\nMaaa 20,200,176\nMaaa 80,208,176\nMaaa 80,212,180\n"
"D0 8,24,44,2048\nMaaa 84,220,180\nMaaa 84,216,176\nMaaa 92,216,184\nMaaa 92,220,188\nMaaa 88,212,188\nMaaa 88,208,184\nMaaa 28,200,184\nMaaa 28,204,188\nD0 9,24,44,3072\n"
"Maaa 24,196,188\nMaaa 24,192,184\ns_waitcnt vmcnt(17) lgkmcnt(0)\ns_barrier\nMaaa 32,192,224\nTa 160,12,0\nMaaa 32,196,228\nTa 162,12,2048\nMaaa 36,204,228\n"
"s_add_u32 m0, s47, -16384\ns_add_u32 s48, s48, 128\nMaaa 36,200,224\nTa 164,12,16384\nMaaa 96,208,224\nTa 166,12,18432\nMaaa 96,212,228\nD0 2,20,48,0\nMaaa 100,220,228\n"
"Ta 168,13,0\nMaaa 100,216,224\nTa 170,13,2048\nMaaa 108,216,232\nMaaa 108,220,236\nTa 172,13,16384\nMaaa 104,212,236\nTa 174,13,18432\nMaaa 104,208,232\nMaaa 44,200,232\n"
"Ta 176,14,0\nMaaa 44,204,236\nTa 178,14,2048\n.W550:\ns_cbranch_execz .W554\nMaaa 40,196,236\nD0 3,20,48,1024\nMaaa 40,192,232\nTa 180,14,16384\nMaaa 48,192,240\nTa 182,14,18432\n"
"Maaa 48,196,244\nMaaa 52,204,244\nTa 184,15,0\nMaaa 52,200,240\nTa 186,15,2048\nMaaa 112,208,240\nMaaa 112,212,244\nTa 188,15,16384\nMaaa 116,220,244\nTa 190,15,18432\n"
"Maaa 116,216,240\nD0 4,20,48,2048\nMaaa 124,216,248\nMaaa 124,220,252\nMaaa 120,212,252\nMaaa 120,208,248\nMaaa 60,200,248\nMaaa 60,204,252\nD0 5,20,48,3072\n"
"s_sub_u32 s50, s50, 1\nMaaa 56,196,252\nMaaa 56,192,248\nWL 0\ns_cbranch_scc0 .GL150\nMvaa 64,128,160\nTa 224,16,0\nMvaa 64,132,164\nTa 226,16,2048\nMvaa 68,140,164\n"
"s_add_u32 m0, s47, -65536\ns_add_u32 s45, s45, 128\nMvaa 68,136,160\nTa 228,16,16384\nMvaa 128,144,160\nTa 230,16,18432\nMvaa 128,148,164\nD0 2,20,45,0\nMvaa 132,156,164\n"
"Ta 232,17,0\nMvaa 132,152,160\nTa 234,17,2048\nMvaa 140,152,168\ns_and_b32 s2, s74, 15\nMvaa 140,156,172\nTa 236,17,16384\n"
"buffer_store_dword v25, off, s[16:19], s90 offset:0 sc0 sc1\nMvaa 136,148,172\nTa 238,17,18432\nMvaa 136,144,168\ns_sub_u32 s2, s2, 2\ns_cmp_lt_u32 s2, 14\n"
"s_cselect_b32 s5, 0, 0x40000000\nMvaa 76,136,168\nTa 240,18,0\nMvaa 76,140,172\nTa 242,18,2048\nMvaa 72,132,172\nD0 3,20,45,1024\nMvaa 72,128,168\nTa 244,18,16384\n"
"Mvaa 80,128,176\nTa 246,18,18432\nMvaa 80,132,180\ns_and_b32 s2, s2, 15\nMvaa 84,140,180\nTa 248,19,0\nMvaa 84,136,176\nTa 250,19,2048\nMvaa 144,144,176\ns_mul_i32 s4, s73, 14\n"
"Mvaa 144,148,180\nTa 252,19,16384\nMvaa 148,156,180\nTa 254,19,18432\nMvaa 148,152,176\nD0 4,20,45,2048\nMvaa 156,152,184\ns_add_u32 s4, s4, s2\nMvaa 156,156,188\n"
"s_mov_b32 s36, s62\nMvaa 152,148,188\ns_and_b32 s37, s63, 0xffff\nMvaa 152,144,184\ns_mov_b32 s38, 0x188000\nMvaa 92,136,184\ns_mov_b32 s39, 0x20000\nMvaa 92,140,188\n"
"D0 5,20,45,3072\nMvaa 88,132,188\ns_mov_b32 s40, s64\nMvaa 88,128,184\ns_waitcnt vmcnt(18) lgkmcnt(0)\ns_barrier\nMvaa 96,128,224\nRa 192,10,16384\nMvaa 96,132,228\n"
"s_add_u32 m0, s47, 0\ns_add_u32 s44, s44, 0x40000\nMvaa 100,140,228\nRa 196,11,16384\nMvaa 100,136,224\ns_and_b32 s41, s65, 0xffff\nMvaa 160,144,224\nRa 200,10,18432\n"
"Mvaa 160,148,228\nD0 6,24,44,0\nMvaa 164,156,228\nRa 204,11,18432\nMvaa 164,152,224\ns_mov_b32 s42, 0x188000\nMvaa 172,152,232\nRa 208,10,24576\nMvaa 172,156,236\n"
"s_mov_b32 s43, 0x20000\nMvaa 168,148,236\nRa 212,11,24576\nMvaa 168,144,232\ns_mov_b32 s92, s66\nMvaa 108,136,232\nRa 216,10,26624\nMvaa 108,140,236\nD0 7,24,44,1024\n"
"Mvaa 104,132,236\nRa 220,11,26624\nMvaa 104,128,232\ns_and_b32 s93, s67, 0xffff\nMvaa 112,128,240\ns_mov_b32 s94, 0x188000\nMvaa 112,132,244\ns_mov_b32 s95, 0x20000\n"
"Mvaa 116,140,244\nv_lshrrev_b32 v252, 1, v0\nMvaa 116,136,240\nv_mul_u32_u24 v253, 14336, v252\nMvaa 176,144,240\nv_and_b32 v254, 1, v0\nMvaa 176,148,244\nD0 8,24,44,2048\n"
"Mvaa 180,156,244\nv_lshl_add_u32 v253, v254, 4, v253\nMvaa 180,152,240\nv_mov_b32 v254, 0x40000000\nv_cmp_gt_u32 vcc, 56, v0\nv_lshlrev_b32 v252, 3, v252\n"
"s_mul_i32 s2, s11, 401408\nv_cndmask_b32 v21, v254, v253, vcc\nv_cndmask_b32 v255, v254, v252, vcc\nMvaa 188,152,248\ns_lshl_b32 s3, s4, 5\nMvaa 188,156,252\ns_add_u32 s2, s2, s3\n"
"Mvaa 184,148,252\ns_or_b32 s86, s2, s5\nMvaa 184,144,248\ns_add_u32 s87, s86, 7168\nMvaa 124,136,248\nv_lshrrev_b32 v251, 4, v0\nMvaa 124,140,252\nD0 9,24,44,3072\n"
"Mvaa 120,132,252\nv_mul_u32_u24 v252, 3584, v251\nMvaa 120,128,248\nWL 0\nMaaa 32,192,224\nRa 128,10,32768\nMaaa 32,196,228\ns_add_u32 m0, s47, 16384\ns_add_u32 s44, s44, 0x40000\n"
"Maaa 36,204,228\nRa 132,11,32768\nMaaa 36,200,224\nv_and_b32 v251, 15, v0\nMaaa 96,208,224\nRa 136,10,34816\nMaaa 96,212,228\nD0 6,24,44,0\nMaaa 100,220,228\nRa 140,11,34816\n"
"Maaa 100,216,224\nv_lshl_add_u32 v252, v251, 4, v252\nv_cmp_gt_u32 vcc, 14, v251\ns_mul_i32 s2, s4, 14336\ns_mul_i32 s3, s11, 224\nv_cndmask_b32 v20, v254, v252, vcc\n"
"Maaa 108,216,232\nRa 144,10,40960\nMaaa 108,220,236\ns_add_u32 s2, s2, s3\nMaaa 104,212,236\nRa 148,11,40960\nMaaa 104,208,232\ns_or_b32 s88, s2, s5\nMaaa 44,200,232\n"
"Ra 152,10,43008\nMaaa 44,204,236\nD0 7,24,44,1024\nMaaa 40,196,236\nRa 156,11,43008\nMaaa 40,192,232\nMaaa 48,192,240\nMaaa 48,196,244\nMaaa 52,204,244\nMaaa 52,200,240\n"
"Maaa 112,208,240\nMaaa 112,212,244\nD0 8,24,44,2048\nMaaa 116,220,244\nMaaa 116,216,240\nMaaa 124,216,248\nMaaa 124,220,252\nMaaa 120,212,252\nMaaa 120,208,248\nMaaa 60,200,248\n"
"Maaa 60,204,252\nD0 9,24,44,3072\nMaaa 56,196,252\nMaaa 56,192,248\ns_waitcnt vmcnt(17) lgkmcnt(0)\ns_barrier\nMaaa 0,192,160\nTa 224,16,32768\nMaaa 0,196,164\nTa 226,16,34816\n"
"buffer_load_dwordx4 v[192:195], v21, s[36:39], s86 offen offset:0\nMaaa 4,204,164\ns_add_u32 m0, s47, -49152\ns_add_u32 s48, s48, 128\nMaaa 4,200,160\nTa 228,16,49152\n"
"s_lshl_b32 s6, s11, 12\ns_add_u32 s6, s6, 0xc044000\nMaaa 64,208,160\nTa 230,16,51200\nMaaa 64,212,164\nD0 2,20,48,0\nMaaa 68,220,164\nTa 232,17,32768\nMaaa 68,216,160\n"
"Ta 234,17,34816\nMaaa 76,216,168\nbuffer_load_dwordx4 v[32:35], v1, s[16:19], s6 offen offset:0 sc0 sc1\nMaaa 76,220,172\nTa 236,17,49152\nMaaa 72,212,172\nTa 238,17,51200\n"
"Maaa 72,208,168\nMaaa 12,200,168\nTa 240,18,32768\nMaaa 12,204,172\nTa 242,18,34816\nMaaa 8,196,172\nD0 3,20,48,1024\nMaaa 8,192,168\nTa 244,18,49152\nMaaa 16,192,176\n"
"Ta 246,18,51200\nMaaa 16,196,180\nbuffer_load_dwordx4 v[36:39], v1, s[16:19], s6 offen offset:1024 sc0 sc1\nMaaa 20,204,180\nTa 248,19,32768\nMaaa 20,200,176\nTa 250,19,34816\n"
"Maaa 80,208,176\nMaaa 80,212,180\nTa 252,19,49152\nMaaa 84,220,180\nTa 254,19,51200\nMaaa 84,216,176\nD0 4,20,48,2048\nMaaa 92,216,184\n"
"buffer_load_dwordx4 v[40:43], v1, s[16:19], s6 offen offset:2048 sc0 sc1\nMaaa 92,220,188\nMaaa 88,212,188\nMaaa 88,208,184\nMaaa 28,200,184\n"
"buffer_load_dwordx4 v[44:47], v1, s[16:19], s6 offen offset:3072 sc0 sc1\nMaaa 28,204,188\nD0 5,20,48,3072\nMaaa 24,196,188\nMaaa 24,192,184\nWL 0\nMvaa 96,128,224\n"
"Ta 160,12,32768\nMvaa 96,132,228\nTa 162,12,34816\nMvaa 100,140,228\ns_add_u32 m0, s47, -32768\ns_add_u32 s45, s45, 128\nMvaa 100,136,224\nTa 164,12,49152\nMvaa 160,144,224\n"
"Ta 166,12,51200\nMvaa 160,148,228\nD0 2,20,45,0\nMvaa 164,156,228\nTa 168,13,32768\nMvaa 164,152,224\nTa 170,13,34816\nMvaa 172,152,232\nMvaa 172,156,236\nTa 172,13,49152\n"
"Mvaa 168,148,236\nTa 174,13,51200\nMvaa 168,144,232\nMvaa 108,136,232\nTa 176,14,32768\nMvaa 108,140,236\nTa 178,14,34816\nMvaa 104,132,236\nD0 3,20,45,1024\nMvaa 104,128,232\n"
"Ta 180,14,49152\nMvaa 112,128,240\nTa 182,14,51200\nMvaa 112,132,244\nMvaa 116,140,244\nTa 184,15,32768\nMvaa 116,136,240\nTa 186,15,34816\nMvaa 176,144,240\nMvaa 176,148,244\n"
"Ta 188,15,49152\nMvaa 180,156,244\nTa 190,15,51200\nMvaa 180,152,240\nD0 4,20,45,2048\nMvaa 188,152,248\nMvaa 188,156,252\nMvaa 184,148,252\nMvaa 184,144,248\nMvaa 124,136,248\n"
"Mvaa 124,140,252\nD0 5,20,45,3072\nMvaa 120,132,252\nMvaa 120,128,248\ns_waitcnt vmcnt(21) lgkmcnt(0)\ns_barrier\nMvaa 64,128,160\nRa 192,10,49152\nMvaa 64,132,164\n"
"s_add_u32 m0, s47, 32768\ns_add_u32 s44, s44, 0x40000\nbuffer_load_dwordx4 v[208:211], v21, s[40:43], s86 offen offset:0\nMvaa 68,140,164\nRa 196,11,49152\nMvaa 68,136,160\n"
"Mvaa 128,144,160\nRa 200,10,51200\nMvaa 128,148,164\nD0 6,24,44,0\nMvaa 132,156,164\nRa 204,11,51200\nMvaa 132,152,160\nMvaa 140,152,168\nRa 208,10,57344\nMvaa 140,156,172\n"
"Mvaa 136,148,172\nRa 212,11,57344\nMvaa 136,144,168\nMvaa 76,136,168\nRa 216,10,59392\nMvaa 76,140,172\nD0 7,24,44,1024\nMvaa 72,132,172\nRa 220,11,59392\nMvaa 72,128,168\n"
"Mvaa 80,128,176\nMvaa 80,132,180\nMvaa 84,140,180\nMvaa 84,136,176\nMvaa 144,144,176\nMvaa 144,148,180\nD0 8,24,44,2048\nMvaa 148,156,180\nMvaa 148,152,176\nMvaa 156,152,184\n"
"Mvaa 156,156,188\nMvaa 152,148,188\nMvaa 152,144,184\nMvaa 92,136,184\nMvaa 92,140,188\nD0 9,24,44,3072\nMvaa 88,132,188\nMvaa 88,128,184\nWL 0\nMaaa 0,192,160\nRa 128,10,0\n"
"Maaa 0,196,164\ns_add_u32 m0, s47, 49152\ns_add_u32 s44, s44, 0x40000\nMaaa 4,204,164\nRa 132,11,0\nMaaa 4,200,160\nMaaa 64,208,160\nRa 136,10,2048\nMaaa 64,212,164\nD0 6,24,44,0\n"
"Maaa 68,220,164\nRa 140,11,2048\nMaaa 68,216,160\nMaaa 76,216,168\nRa 144,10,8192\nMaaa 76,220,172\nMaaa 72,212,172\nRa 148,11,8192\nMaaa 72,208,168\nMaaa 12,200,168\n"
"Ra 152,10,10240\nMaaa 12,204,172\nD0 7,24,44,1024\nMaaa 8,196,172\nRa 156,11,10240\nMaaa 8,192,168\nMaaa 16,192,176\nMaaa 16,196,180\nMaaa 20,204,180\nMaaa 20,200,176\n"
"Maaa 80,208,176\nMaaa 80,212,180\nD0 8,24,44,2048\nMaaa 84,220,180\nMaaa 84,216,176\nMaaa 92,216,184\nMaaa 92,220,188\nMaaa 88,212,188\nMaaa 88,208,184\nMaaa 28,200,184\n"
"Maaa 28,204,188\nD0 9,24,44,3072\nMaaa 24,196,188\nMaaa 24,192,184\ns_waitcnt vmcnt(14) lgkmcnt(0)\ns_barrier\nMaaa 32,192,224\nTa 160,12,0\nv_xor_b32 v48, s15, v33\n"
"Maaa 32,196,228\nTa 162,12,2048\nbuffer_load_dwordx4 v[196:199], v21, s[36:39], s86 offen offset:3584\nMaaa 36,204,228\ns_add_u32 m0, s47, -16384\ns_add_u32 s48, s48, 128\n"
"Maaa 36,200,224\nTa 164,12,16384\nMaaa 96,208,224\nTa 166,12,18432\nMaaa 96,212,228\nD0 2,20,48,0\nMaaa 100,220,228\nTa 168,13,0\nMaaa 100,216,224\nTa 170,13,2048\n"
"Maaa 108,216,232\nv_xor_b32 v49, s15, v35\nMaaa 108,220,236\nTa 172,13,16384\nMaaa 104,212,236\nTa 174,13,18432\nMaaa 104,208,232\nv_xor_b32 v50, s15, v37\nMaaa 44,200,232\n"
"Ta 176,14,0\nMaaa 44,204,236\nTa 178,14,2048\nMaaa 40,196,236\nD0 3,20,48,1024\nMaaa 40,192,232\nTa 180,14,16384\nMaaa 48,192,240\nTa 182,14,18432\nv_xor_b32 v51, s15, v39\n"
"Maaa 48,196,244\nv_xor_b32 v52, s15, v41\nMaaa 52,204,244\nTa 184,15,0\nMaaa 52,200,240\nTa 186,15,2048\nMaaa 112,208,240\nv_xor_b32 v53, s15, v43\nMaaa 112,212,244\n"
"Ta 188,15,16384\nMaaa 116,220,244\nTa 190,15,18432\nMaaa 116,216,240\nD0 4,20,48,2048\nMaaa 124,216,248\nv_xor_b32 v54, s15, v45\nMaaa 124,220,252\nv_xor_b32 v55, s15, v47\n"
"Maaa 120,212,252\nv_or3_b32 v48, v48, v49, v50\nMaaa 120,208,248\nv_or3_b32 v51, v51, v52, v53\nMaaa 60,200,248\nv_or3_b32 v48, v48, v54, v55\nMaaa 60,204,252\nD0 5,20,48,3072\n"
"Maaa 56,196,252\nv_or_b32 v48, v48, v51\nMaaa 56,192,248\nWL 0\nMvaa 64,128,160\nTa 224,16,0\nv_cmp_ne_u32 vcc, 0, v48\nMvaa 64,132,164\nTa 226,16,2048\nMvaa 68,140,164\n"
"s_add_u32 m0, s47, -65536\ns_add_u32 s45, s45, 128\nMvaa 68,136,160\nTa 228,16,16384\nMvaa 128,144,160\nTa 230,16,18432\nMvaa 128,148,164\nD0 2,20,45,0\nMvaa 132,156,164\n"
"Ta 232,17,0\nMvaa 132,152,160\nTa 234,17,2048\nMvaa 140,152,168\ns_cbranch_vccnz .S144\n.S145:\nMvaa 140,156,172\nTa 236,17,16384\nMvaa 136,148,172\nTa 238,17,18432\n"
"Mvaa 136,144,168\nv_max_u32 v48, v32, v34\nMvaa 76,136,168\nTa 240,18,0\nMvaa 76,140,172\nTa 242,18,2048\nMvaa 72,132,172\nD0 3,20,45,1024\n.W551:\ns_cbranch_execz .W555\n"
"Mvaa 72,128,168\nTa 244,18,16384\nMvaa 80,128,176\nTa 246,18,18432\nv_min_u32 v49, v32, v34\nMvaa 80,132,180\nv_med3_u32 v49, v48, v49, v36\nMvaa 84,140,180\nTa 248,19,0\n"
"Mvaa 84,136,176\nTa 250,19,2048\nMvaa 144,144,176\nv_max_u32 v48, v48, v36\nMvaa 144,148,180\nTa 252,19,16384\nMvaa 148,156,180\nTa 254,19,18432\nMvaa 148,152,176\n"
"D0 4,20,45,2048\nMvaa 156,152,184\nv_med3_u32 v49, v48, v49, v38\nMvaa 156,156,188\nv_max_u32 v48, v48, v38\nMvaa 152,148,188\nv_med3_u32 v49, v48, v49, v40\nMvaa 152,144,184\n"
"v_max_u32 v48, v48, v40\nMvaa 92,136,184\nv_med3_u32 v49, v48, v49, v42\nMvaa 92,140,188\nD0 5,20,45,3072\nMvaa 88,132,188\nv_max_u32 v48, v48, v42\nMvaa 88,128,184\n"
"s_waitcnt vmcnt(18) lgkmcnt(0)\ns_barrier\nMvaa 96,128,224\nRa 192,10,16384\nv_med3_u32 v49, v48, v49, v44\nMvaa 96,132,228\ns_add_u32 m0, s47, 0\ns_add_u32 s44, s44, 0x40000\n"
"buffer_load_dwordx4 v[212:215], v21, s[40:43], s86 offen offset:3584\nMvaa 100,140,228\nRa 196,11,16384\nMvaa 100,136,224\nv_max_u32 v48, v48, v44\nMvaa 160,144,224\n"
"Ra 200,10,18432\nMvaa 160,148,228\nD0 6,24,44,0\nMvaa 164,156,228\nRa 204,11,18432\nMvaa 164,152,224\nv_med3_u32 v49, v48, v49, v46\nMvaa 172,152,232\nRa 208,10,24576\n"
"Mvaa 172,156,236\nv_max_u32 v48, v48, v46\nMvaa 168,148,236\nRa 212,11,24576\nMvaa 168,144,232\nv_mov_b32 v50, v48\nMvaa 108,136,232\nRa 216,10,26624\nMvaa 108,140,236\n"
"D0 7,24,44,1024\nMvaa 104,132,236\nRa 220,11,26624\nMvaa 104,128,232\nv_max_u32_dpp v50, v50, v50 row_ror:8 row_mask:0xf bank_mask:0xf\nMvaa 112,128,240\nMvaa 112,132,244\n"
"v_max_u32_dpp v50, v50, v50 row_ror:4 row_mask:0xf bank_mask:0xf\nMvaa 116,140,244\nMvaa 116,136,240\nv_max_u32_dpp v50, v50, v50 row_ror:2 row_mask:0xf bank_mask:0xf\n"
"Mvaa 176,144,240\nMvaa 176,148,244\nD0 8,24,44,2048\nMvaa 180,156,244\nv_max_u32_dpp v50, v50, v50 row_ror:1 row_mask:0xf bank_mask:0xf\nMvaa 180,152,240\nv_mov_b32 v52, v50\n"
"Mvaa 188,152,248\nMvaa 188,156,252\nv_permlane16_swap_b32 v50, v52\nMvaa 184,148,252\nv_max_u32 v50, v50, v52\nMvaa 184,144,248\nv_mov_b32 v52, v50\nMvaa 124,136,248\n"
"Mvaa 124,140,252\nD0 9,24,44,3072\nMvaa 120,132,252\nv_permlane32_swap_b32 v50, v52\nMvaa 120,128,248\nWL 0\nMaaa 32,192,224\nRa 128,10,32768\nv_max_u32 v50, v50, v52\n"
"Maaa 32,196,228\ns_add_u32 m0, s47, 16384\ns_add_u32 s44, s44, 0x40000\nMaaa 36,204,228\nRa 132,11,32768\nv_cmp_eq_u32 vcc, v48, v50\ns_lshl_b32 s2, s11, 3\n"
"s_add_u32 s2, s2, 131072\nv_cndmask_b32 v51, v48, v49, vcc\nMaaa 36,200,224\nMaaa 96,208,224\nRa 136,10,34816\nMaaa 96,212,228\nD0 6,24,44,0\nMaaa 100,220,228\nRa 140,11,34816\n"
"Maaa 100,216,224\nv_max_u32_dpp v51, v51, v51 row_ror:8 row_mask:0xf bank_mask:0xf\nMaaa 108,216,232\nRa 144,10,40960\nMaaa 108,220,236\n"
"v_max_u32_dpp v51, v51, v51 row_ror:4 row_mask:0xf bank_mask:0xf\nMaaa 104,212,236\nRa 148,11,40960\nMaaa 104,208,232\n"
"v_max_u32_dpp v51, v51, v51 row_ror:2 row_mask:0xf bank_mask:0xf\nMaaa 44,200,232\nRa 152,10,43008\nMaaa 44,204,236\nD0 7,24,44,1024\nMaaa 40,196,236\nRa 156,11,43008\n"
"Maaa 40,192,232\nv_max_u32_dpp v51, v51, v51 row_ror:1 row_mask:0xf bank_mask:0xf\nMaaa 48,192,240\nv_mov_b32 v52, v51\nMaaa 48,196,244\nMaaa 52,204,244\n"
"v_permlane16_swap_b32 v51, v52\nMaaa 52,200,240\nv_max_u32 v51, v51, v52\nMaaa 112,208,240\nv_mov_b32 v52, v51\nMaaa 112,212,244\nD0 8,24,44,2048\nMaaa 116,220,244\n"
"v_permlane32_swap_b32 v51, v52\nMaaa 116,216,240\nv_max_u32 v51, v51, v52\nMaaa 124,216,248\nv_mov_b32 v52, s2\nMaaa 124,220,252\nds_write_b64 v52, v[50:51]\nMaaa 120,212,252\n"
"v_mov_b32 v52, 131072\nMaaa 120,208,248\nMaaa 60,200,248\nMaaa 60,204,252\nD0 9,24,44,3072\nMaaa 56,196,252\nMaaa 56,192,248\ns_waitcnt vmcnt(18) lgkmcnt(0)\ns_barrier\n"
"Maaa 0,192,160\nTa 224,16,32768\nMaaa 0,196,164\nTa 226,16,34816\nbuffer_load_dwordx4 v[200:203], v21, s[36:39], s87 offen offset:0\nMaaa 4,204,164\ns_add_u32 m0, s47, -49152\n"
"s_add_u32 s48, s48, 128\nMaaa 4,200,160\nTa 228,16,49152\nMaaa 64,208,160\nTa 230,16,51200\nMaaa 64,212,164\nD0 2,20,48,0\nMaaa 68,220,164\nTa 232,17,32768\nMaaa 68,216,160\n"
"Ta 234,17,34816\nMaaa 76,216,168\nRv 32,52,0\nMaaa 76,220,172\nTa 236,17,49152\nMaaa 72,212,172\nTa 238,17,51200\nMaaa 72,208,168\nRv 36,52,16\nMaaa 12,200,168\nTa 240,18,32768\n"
"Maaa 12,204,172\nTa 242,18,34816\nMaaa 8,196,172\nD0 3,20,48,1024\nMaaa 8,192,168\nTa 244,18,49152\nMaaa 16,192,176\nTa 246,18,51200\nMaaa 16,196,180\nMaaa 20,204,180\n"
"Ta 248,19,32768\nMaaa 20,200,176\nTa 250,19,34816\nMaaa 80,208,176\nMaaa 80,212,180\nTa 252,19,49152\nMaaa 84,220,180\nTa 254,19,51200\nMaaa 84,216,176\nD0 4,20,48,2048\n"
"Maaa 92,216,184\nWL 11\nMaaa 92,220,188\nv_max_u32 v48, v32, v33\nMaaa 88,212,188\nv_min_u32 v49, v32, v33\nMaaa 88,208,184\nv_med3_u32 v49, v48, v49, v34\nMaaa 28,200,184\n"
"v_max_u32 v48, v48, v34\nMaaa 28,204,188\nD0 5,20,48,3072\nMaaa 24,196,188\nv_med3_u32 v49, v48, v49, v35\nMaaa 24,192,184\nWL 0\nMvaa 96,128,224\nTa 160,12,32768\n"
"Mvaa 96,132,228\nTa 162,12,34816\nMvaa 100,140,228\ns_add_u32 m0, s47, -32768\ns_add_u32 s45, s45, 128\nMvaa 100,136,224\nTa 164,12,49152\nMvaa 160,144,224\nTa 166,12,51200\n"
"Mvaa 160,148,228\nD0 2,20,45,0\nMvaa 164,156,228\nTa 168,13,32768\nMvaa 164,152,224\nTa 170,13,34816\nMvaa 172,152,232\nv_max_u32 v48, v48, v35\nMvaa 172,156,236\nTa 172,13,49152\n"
"Mvaa 168,148,236\nTa 174,13,51200\nMvaa 168,144,232\nv_med3_u32 v49, v48, v49, v36\nMvaa 108,136,232\nTa 176,14,32768\nMvaa 108,140,236\nTa 178,14,34816\nMvaa 104,132,236\n"
"D0 3,20,45,1024\nMvaa 104,128,232\nTa 180,14,49152\nMvaa 112,128,240\nTa 182,14,51200\nMvaa 112,132,244\nv_max_u32 v48, v48, v36\nMvaa 116,140,244\nTa 184,15,32768\n"
"Mvaa 116,136,240\nTa 186,15,34816\nMvaa 176,144,240\nv_med3_u32 v49, v48, v49, v37\nMvaa 176,148,244\nTa 188,15,49152\nMvaa 180,156,244\nTa 190,15,51200\nMvaa 180,152,240\n"
"D0 4,20,45,2048\nMvaa 188,152,248\nv_max_u32 v48, v48, v37\nMvaa 188,156,252\nv_med3_u32 v49, v48, v49, v38\nMvaa 184,148,252\nv_max_u32 v48, v48, v38\nMvaa 184,144,248\n"
"v_med3_u32 v49, v48, v49, v39\nMvaa 124,136,248\nv_max_u32 v48, v48, v39\nMvaa 124,140,252\nD0 5,20,45,3072\nMvaa 120,132,252\nv_readfirstlane_b32 s2, v48\nMvaa 120,128,248\n"
"s_waitcnt vmcnt(18) lgkmcnt(0)\ns_barrier\nMvaa 64,128,160\nRa 192,10,49152\nMvaa 64,132,164\ns_add_u32 m0, s47, 32768\ns_add_u32 s44, s44, 0x40000\n"
"buffer_load_dwordx4 v[216:219], v21, s[40:43], s87 offen offset:0\nMvaa 68,140,164\nRa 196,11,49152\nMvaa 68,136,160\nv_readfirstlane_b32 s3, v49\nMvaa 128,144,160\n"
"Ra 200,10,51200\nMvaa 128,148,164\nD0 6,24,44,0\nMvaa 132,156,164\nRa 204,11,51200\nMvaa 132,152,160\ns_andn2_b32 s2, 0x7ff, s2\nMvaa 140,152,168\nRa 208,10,57344\n"
"Mvaa 140,156,172\ns_andn2_b32 s3, 0x7ff, s3\nMvaa 136,148,172\nRa 212,11,57344\nMvaa 136,144,168\ns_lshl_b32 s71, s3, 16\nMvaa 76,136,168\nRa 216,10,59392\nMvaa 76,140,172\n"
"D0 7,24,44,1024\nMvaa 72,132,172\nRa 220,11,59392\nMvaa 72,128,168\ns_or_b32 s71, s71, s2\nMvaa 80,128,176\ns_mul_i32 s2, s2, 896\nMvaa 80,132,180\ns_mul_i32 s3, s3, 896\n"
"Mvaa 84,140,180\ns_mul_i32 s6, s11, 224\nMvaa 84,136,176\ns_add_u32 s2, s2, s6\nMvaa 144,144,176\ns_add_u32 s3, s3, s6\nMvaa 144,148,180\nD0 8,24,44,2048\nMvaa 148,156,180\n"
"buffer_load_dwordx2 v[240:241], v255, s[28:31], s2 offen\nMvaa 148,152,176\nMvaa 156,152,184\nMvaa 156,156,188\nMvaa 152,148,188\n"
"buffer_load_dwordx2 v[242:243], v255, s[28:31], s3 offen\ns_lshl2_add_u32 s2, s10, s11\ns_cmp_eq_u32 s2, 512\ns_cselect_b32 s2, 0, 0x40000000\ns_add_u32 s2, s2, 0xc048000\n"
"Mvaa 152,144,184\nMvaa 92,136,184\nMvaa 92,140,188\nD0 9,24,44,3072\nMvaa 88,132,188\nbuffer_store_dword v25, off, s[16:19], s2 offset:0 sc0 sc1\nMvaa 88,128,184\nWL 0\n"
"Maaa 0,192,160\nRa 128,10,0\nMaaa 0,196,164\ns_add_u32 m0, s47, 49152\ns_add_u32 s44, s44, 0x40000\nMaaa 4,204,164\nRa 132,11,0\nMaaa 4,200,160\nMaaa 64,208,160\nRa 136,10,2048\n"
"Maaa 64,212,164\nD0 6,24,44,0\nMaaa 68,220,164\nRa 140,11,2048\nMaaa 68,216,160\nMaaa 76,216,168\nRa 144,10,8192\nMaaa 76,220,172\nMaaa 72,212,172\nRa 148,11,8192\n"
"Maaa 72,208,168\nMaaa 12,200,168\nRa 152,10,10240\nMaaa 12,204,172\nD0 7,24,44,1024\nMaaa 8,196,172\nRa 156,11,10240\nMaaa 8,192,168\nMaaa 16,192,176\nMaaa 16,196,180\n"
"Maaa 20,204,180\nMaaa 20,200,176\nMaaa 80,208,176\nMaaa 80,212,180\nD0 8,24,44,2048\nMaaa 84,220,180\nMaaa 84,216,176\nMaaa 92,216,184\nMaaa 92,220,188\nMaaa 88,212,188\n"
"Maaa 88,208,184\nMaaa 28,200,184\nMaaa 28,204,188\nD0 9,24,44,3072\nMaaa 24,196,188\nMaaa 24,192,184\ns_waitcnt vmcnt(21) lgkmcnt(0)\ns_barrier\nMaaa 32,192,224\nTa 160,12,0\n"
"Maaa 32,196,228\nTa 162,12,2048\nbuffer_load_dwordx4 v[204:207], v21, s[36:39], s87 offen offset:3584\nMaaa 36,204,228\ns_add_u32 m0, s47, -16384\ns_add_u32 s48, s48, 128\n"
"s_lshl_b32 s5, s73, 9\ns_add_u32 s5, s5, 0xc049400\nv_lshrrev_b32 v27, 1, v1\nbuffer_load_dwordx2 v[22:23], v27, s[16:19], s5 offen sc0 sc1\nMaaa 36,200,224\nTa 164,12,16384\n"
"Maaa 96,208,224\nTa 166,12,18432\nMaaa 96,212,228\nD0 2,20,48,0\nMaaa 100,220,228\nTa 168,13,0\nMaaa 100,216,224\nTa 170,13,2048\nMaaa 108,216,232\nMaaa 108,220,236\n"
"Ta 172,13,16384\nMaaa 104,212,236\nTa 174,13,18432\nMaaa 104,208,232\nMaaa 44,200,232\nTa 176,14,0\nMaaa 44,204,236\nTa 178,14,2048\nMaaa 40,196,236\nD0 3,20,48,1024\n"
"Maaa 40,192,232\nTa 180,14,16384\nMaaa 48,192,240\nTa 182,14,18432\nMaaa 48,196,244\nMaaa 52,204,244\nTa 184,15,0\nMaaa 52,200,240\nTa 186,15,2048\nMaaa 112,208,240\n"
"Maaa 112,212,244\nTa 188,15,16384\nMaaa 116,220,244\nTa 190,15,18432\nMaaa 116,216,240\nD0 4,20,48,2048\nMaaa 124,216,248\nMaaa 124,220,252\nMaaa 120,212,252\nMaaa 120,208,248\n"
"Maaa 60,200,248\nMaaa 60,204,252\nD0 5,20,48,3072\nMaaa 56,196,252\nMaaa 56,192,248\nWL 0\nMvaa 64,128,160\nTa 224,16,0\nMvaa 64,132,164\nTa 226,16,2048\nMvaa 68,140,164\n"
"s_add_u32 m0, s47, -65536\ns_add_u32 s45, s45, 128\nMvaa 68,136,160\nTa 228,16,16384\nMvaa 128,144,160\nTa 230,16,18432\nMvaa 128,148,164\nD0 2,20,45,0\nMvaa 132,156,164\n"
"Ta 232,17,0\nMvaa 132,152,160\nTa 234,17,2048\nMvaa 140,152,168\nMvaa 140,156,172\nTa 236,17,16384\nMvaa 136,148,172\nTa 238,17,18432\nMvaa 136,144,168\nMvaa 76,136,168\n"
"Ta 240,18,0\nMvaa 76,140,172\nTa 242,18,2048\nMvaa 72,132,172\nD0 3,20,45,1024\nMvaa 72,128,168\nTa 244,18,16384\nMvaa 80,128,176\nTa 246,18,18432\nMvaa 80,132,180\n"
"Mvaa 84,140,180\nTa 248,19,0\nMvaa 84,136,176\nTa 250,19,2048\nMvaa 144,144,176\nMvaa 144,148,180\nTa 252,19,16384\nMvaa 148,156,180\nTa 254,19,18432\nMvaa 148,152,176\n"
"D0 4,20,45,2048\nMvaa 156,152,184\nMvaa 156,156,188\nMvaa 152,148,188\nMvaa 152,144,184\nMvaa 92,136,184\nMvaa 92,140,188\nD0 5,20,45,3072\nMvaa 88,132,188\nMvaa 88,128,184\n"
"s_waitcnt vmcnt(22) lgkmcnt(0)\ns_barrier\nMvaa 96,128,224\nRa 192,10,16384\nWV 16\nMvaa 96,132,228\ns_add_u32 m0, s47, 0\ns_add_u32 s44, s44, 0x40000\n"
"buffer_load_dwordx4 v[220:223], v21, s[40:43], s87 offen offset:3584\nMvaa 100,140,228\nRa 196,11,16384\nv_lshlrev_b32 v244, 16, v240\nMvaa 100,136,224\n"
"v_and_b32 v245, 0xffff0000, v240\nv_lshlrev_b32 v246, 16, v241\nMvaa 160,144,224\nRa 200,10,18432\nv_and_b32 v247, 0xffff0000, v241\nMvaa 160,148,228\nD0 6,24,44,0\n"
"v_lshlrev_b32 v248, 16, v242\nMvaa 164,156,228\n.W552:\ns_cbranch_execz .W556\nRa 204,11,18432\nv_and_b32 v249, 0xffff0000, v242\nMvaa 164,152,224\nv_lshlrev_b32 v250, 16, v243\n"
"v_and_b32 v251, 0xffff0000, v243\nMvaa 172,152,232\nRa 208,10,24576\nv_lshlrev_b32 v252, 16, v192\nMvaa 172,156,236\nv_and_b32 v253, 0xffff0000, v192\nMU 32,244,252\n"
"Mvaa 168,148,236\nRa 212,11,24576\nMU 33,244,253\nMvaa 168,144,232\nMU 40,248,252\nMU 41,248,253\nMvaa 108,136,232\nRa 216,10,26624\nv_lshlrev_b32 v252, 16, v193\n"
"Mvaa 108,140,236\nD0 7,24,44,1024\nv_and_b32 v253, 0xffff0000, v193\nMvaa 104,132,236\nRa 220,11,26624\nMU 34,244,252\nMvaa 104,128,232\nMU 35,244,253\nMU 42,248,252\n"
"Mvaa 112,128,240\nMU 43,248,253\nv_lshlrev_b32 v252, 16, v194\nMvaa 112,132,244\nv_and_b32 v253, 0xffff0000, v194\nMvaa 116,140,244\nMU 36,244,252\nMU 37,244,253\n"
"Mvaa 116,136,240\nMU 44,248,252\nMU 45,248,253\nMvaa 176,144,240\nv_lshlrev_b32 v252, 16, v195\nv_and_b32 v253, 0xffff0000, v195\nMvaa 176,148,244\nD0 8,24,44,2048\nMU 38,244,252\n"
"Mvaa 180,156,244\nMU 39,244,253\nMU 46,248,252\nMvaa 180,152,240\nMU 47,248,253\nv_lshlrev_b32 v252, 16, v208\nMvaa 188,152,248\nv_and_b32 v253, 0xffff0000, v208\nMU 48,244,252\n"
"Mvaa 188,156,252\nMU 49,244,253\nMU 56,248,252\nMvaa 184,148,252\nMU 57,248,253\nv_lshlrev_b32 v252, 16, v209\nMvaa 184,144,248\nv_and_b32 v253, 0xffff0000, v209\nMU 50,244,252\n"
"Mvaa 124,136,248\nMU 51,244,253\nMU 58,248,252\nMvaa 124,140,252\nD0 9,24,44,3072\nMU 59,248,253\nMvaa 120,132,252\nv_lshlrev_b32 v252, 16, v210\nv_and_b32 v253, 0xffff0000, v210\n"
"Mvaa 120,128,248\nWL 0\nMaaa 32,192,224\nRa 128,10,32768\nMU 52,244,252\nMaaa 32,196,228\ns_add_u32 m0, s47, 16384\ns_add_u32 s44, s44, 0x40000\nMaaa 36,204,228\nRa 132,11,32768\n"
"MU 53,244,253\nMaaa 36,200,224\nMU 60,248,252\nMU 61,248,253\nMaaa 96,208,224\nRa 136,10,34816\nv_lshlrev_b32 v252, 16, v211\nMaaa 96,212,228\nD0 6,24,44,0\n"
"v_and_b32 v253, 0xffff0000, v211\nMaaa 100,220,228\nRa 140,11,34816\nMU 54,244,252\nMaaa 100,216,224\nMU 55,244,253\nMU 62,248,252\nMaaa 108,216,232\nRa 144,10,40960\n"
"MU 63,248,253\nMaaa 108,220,236\nv_lshlrev_b32 v252, 16, v196\nv_and_b32 v253, 0xffff0000, v196\nMaaa 104,212,236\nRa 148,11,40960\nFM 32,245,252\nMaaa 104,208,232\nFM 33,245,253\n"
"FM 40,249,252\nMaaa 44,200,232\nRa 152,10,43008\nFM 41,249,253\nMaaa 44,204,236\nD0 7,24,44,1024\nv_lshlrev_b32 v252, 16, v197\nMaaa 40,196,236\nRa 156,11,43008\n"
"v_and_b32 v253, 0xffff0000, v197\nMaaa 40,192,232\nFM 34,245,252\nFM 35,245,253\nMaaa 48,192,240\nFM 42,249,252\nFM 43,249,253\nMaaa 48,196,244\nv_lshlrev_b32 v252, 16, v198\n"
"Maaa 52,204,244\nv_and_b32 v253, 0xffff0000, v198\nFM 36,245,252\nMaaa 52,200,240\nFM 37,245,253\nFM 44,249,252\nMaaa 112,208,240\nFM 45,249,253\nv_lshlrev_b32 v252, 16, v199\n"
"Maaa 112,212,244\nD0 8,24,44,2048\nv_and_b32 v253, 0xffff0000, v199\nMaaa 116,220,244\nFM 38,245,252\nFM 39,245,253\nMaaa 116,216,240\nFM 46,249,252\nFM 47,249,253\n"
"Maaa 124,216,248\nv_lshlrev_b32 v252, 16, v212\nv_and_b32 v253, 0xffff0000, v212\nMaaa 124,220,252\nFM 48,245,252\nFM 49,245,253\nMaaa 120,212,252\nFM 56,249,252\nFM 57,249,253\n"
"Maaa 120,208,248\nv_lshlrev_b32 v252, 16, v213\nv_and_b32 v253, 0xffff0000, v213\nMaaa 60,200,248\nFM 50,245,252\nFM 51,245,253\nMaaa 60,204,252\nD0 9,24,44,3072\nFM 58,249,252\n"
"Maaa 56,196,252\nFM 59,249,253\nv_lshlrev_b32 v252, 16, v214\nMaaa 56,192,248\ns_waitcnt vmcnt(19) lgkmcnt(0)\ns_barrier\nMaaa 0,192,160\nTa 224,16,32768\n"
"v_and_b32 v253, 0xffff0000, v214\nMaaa 0,196,164\nTa 226,16,34816\nbuffer_load_dwordx4 v[224:227], v20, s[92:95], s88 offen offset:0\nMaaa 4,204,164\ns_add_u32 m0, s47, -49152\n"
"s_add_u32 s48, s48, 128\nMaaa 4,200,160\nTa 228,16,49152\nFM 52,245,252\nMaaa 64,208,160\nTa 230,16,51200\nFM 53,245,253\nMaaa 64,212,164\nD0 2,20,48,0\nFM 60,249,252\n"
"Maaa 68,220,164\nTa 232,17,32768\nFM 61,249,253\nMaaa 68,216,160\nTa 234,17,34816\nv_lshlrev_b32 v252, 16, v215\nMaaa 76,216,168\nv_and_b32 v253, 0xffff0000, v215\nFM 54,245,252\n"
"Maaa 76,220,172\nTa 236,17,49152\nFM 55,245,253\nMaaa 72,212,172\nTa 238,17,51200\nFM 62,249,252\nMaaa 72,208,168\nFM 63,249,253\nv_lshlrev_b32 v252, 16, v200\nMaaa 12,200,168\n"
"Ta 240,18,32768\nv_and_b32 v253, 0xffff0000, v200\nMaaa 12,204,172\nTa 242,18,34816\nFM 32,246,252\nMaaa 8,196,172\nD0 3,20,48,1024\nFM 33,246,253\nMaaa 8,192,168\n"
"Ta 244,18,49152\nFM 40,250,252\nMaaa 16,192,176\nTa 246,18,51200\nFM 41,250,253\nMaaa 16,196,180\nv_lshlrev_b32 v252, 16, v201\nMaaa 20,204,180\nTa 248,19,32768\n"
"v_and_b32 v253, 0xffff0000, v201\nMaaa 20,200,176\nTa 250,19,34816\nFM 34,246,252\nMaaa 80,208,176\nFM 35,246,253\nFM 42,250,252\nMaaa 80,212,180\nTa 252,19,49152\nFM 43,250,253\n"
"Maaa 84,220,180\nTa 254,19,51200\nv_lshlrev_b32 v252, 16, v202\nMaaa 84,216,176\nD0 4,20,48,2048\nv_and_b32 v253, 0xffff0000, v202\nMaaa 92,216,184\nFM 36,246,252\nFM 37,246,253\n"
"Maaa 92,220,188\nFM 44,250,252\nFM 45,250,253\nMaaa 88,212,188\nv_lshlrev_b32 v252, 16, v203\nv_and_b32 v253, 0xffff0000, v203\nMaaa 88,208,184\nFM 38,246,252\nFM 39,246,253\n"
"Maaa 28,200,184\nFM 46,250,252\nFM 47,250,253\nMaaa 28,204,188\nD0 5,20,48,3072\nv_lshlrev_b32 v252, 16, v216\nMaaa 24,196,188\nv_and_b32 v253, 0xffff0000, v216\nFM 48,246,252\n"
"Maaa 24,192,184\nWL 0\nMvaa 96,128,224\nTa 160,12,32768\nFM 49,246,253\nMvaa 96,132,228\nTa 162,12,34816\nbuffer_load_dwordx4 v[228:231], v20, s[92:95], s88 offen offset:896\n"
"Mvaa 100,140,228\ns_add_u32 m0, s47, -32768\ns_add_u32 s45, s45, 128\nMvaa 100,136,224\nTa 164,12,49152\nFM 56,250,252\nMvaa 160,144,224\nTa 166,12,51200\nFM 57,250,253\n"
"Mvaa 160,148,228\nD0 2,20,45,0\nv_lshlrev_b32 v252, 16, v217\nMvaa 164,156,228\nTa 168,13,32768\nv_and_b32 v253, 0xffff0000, v217\nMvaa 164,152,224\nTa 170,13,34816\n"
"FM 50,246,252\nMvaa 172,152,232\nFM 51,246,253\nFM 58,250,252\nMvaa 172,156,236\nTa 172,13,49152\nFM 59,250,253\nMvaa 168,148,236\nTa 174,13,51200\nv_lshlrev_b32 v252, 16, v218\n"
"Mvaa 168,144,232\nv_and_b32 v253, 0xffff0000, v218\nFM 52,246,252\nMvaa 108,136,232\nTa 176,14,32768\nFM 53,246,253\nMvaa 108,140,236\nTa 178,14,34816\nFM 60,250,252\n"
"Mvaa 104,132,236\nD0 3,20,45,1024\nFM 61,250,253\nMvaa 104,128,232\nTa 180,14,49152\nv_lshlrev_b32 v252, 16, v219\nMvaa 112,128,240\nTa 182,14,51200\n"
"v_and_b32 v253, 0xffff0000, v219\nMvaa 112,132,244\nFM 54,246,252\nWV 25\nv_cmp_ne_u32 vcc, s15, v23\ns_nop 0\ns_cbranch_vccnz .S147\n.S148:\nMvaa 116,140,244\nTa 184,15,32768\n"
"v_add_u32_dpp v22, v22, v22 row_ror:8 row_mask:0xf bank_mask:0xf\nMvaa 116,136,240\nTa 186,15,34816\nMvaa 176,144,240\n"
"v_add_u32_dpp v22, v22, v22 row_ror:4 row_mask:0xf bank_mask:0xf\nMvaa 176,148,244\nTa 188,15,49152\nMvaa 180,156,244\nTa 190,15,51200\n"
"v_add_u32_dpp v22, v22, v22 row_ror:2 row_mask:0xf bank_mask:0xf\nMvaa 180,152,240\nD0 4,20,45,2048\nMvaa 188,152,248\n"
"v_add_u32_dpp v22, v22, v22 row_ror:1 row_mask:0xf bank_mask:0xf\nMvaa 188,156,252\nMvaa 184,148,252\nv_mov_b32 v23, v22\nMvaa 184,144,248\nMvaa 124,136,248\n"
"v_permlane16_swap_b32 v22, v23\nv_add_u32 v22, v22, v23\nMvaa 124,140,252\nD0 5,20,45,3072\nv_mov_b32 v23, v22\nMvaa 120,132,252\nMvaa 120,128,248\ns_waitcnt vmcnt(19) lgkmcnt(0)\n"
"s_barrier\nMvaa 64,128,160\nRa 192,10,49152\nv_permlane32_swap_b32 v22, v23\nMvaa 64,132,164\ns_add_u32 m0, s47, 32768\ns_add_u32 s44, s44, 0x40000\n"
"buffer_load_dwordx4 v[232:235], v20, s[92:95], s88 offen offset:1792\nMvaa 68,140,164\nRa 196,11,49152\nv_add_u32 v22, v22, v23\nMvaa 68,136,160\nMvaa 128,144,160\n"
"Ra 200,10,51200\nv_readfirstlane_b32 s4, v22\nMvaa 128,148,164\nD0 6,24,44,0\nMvaa 132,156,164\nRa 204,11,51200\nMvaa 132,152,160\ns_cmp_gt_i32 s4, 0\ns_cselect_b32 s4, 1, 0\n"
"Mvaa 140,152,168\nRa 208,10,57344\ns_xor_b32 s4, s4, s12\nMvaa 140,156,172\ns_and_b32 s4, s4, 1\ns_lshl_b32 s4, s4, 13\nMvaa 136,148,172\nRa 212,11,57344\n"
"s_mul_i32 s72, s73, 25165824\nMvaa 136,144,168\ns_add_u32 s72, s72, s4\ns_and_b32 s4, s12, 1\nMvaa 76,136,168\nRa 216,10,59392\ns_lshl_b32 s4, s4, 21\nMvaa 76,140,172\n"
"D0 7,24,44,1024\ns_add_u32 s72, s72, s4\nMvaa 72,132,172\nRa 220,11,59392\ns_lshl3_add_u32 s2, s13, s11\nMvaa 72,128,168\ns_lshl_b32 s2, s2, 11\ns_lshl_b32 s3, s14, 22\n"
"Mvaa 80,128,176\ns_add_u32 s2, s2, s3\ns_add_u32 s2, s2, s72\nMvaa 80,132,180\ns_add_u32 s49, s2, 0xbe0000\nMvaa 84,140,180\nFM 55,246,253\nFM 62,250,252\nMvaa 84,136,176\n"
"FM 63,250,253\nv_lshlrev_b32 v252, 16, v204\nMvaa 144,144,176\nv_and_b32 v253, 0xffff0000, v204\nFM 32,247,252\nMvaa 144,148,180\nD0 8,24,44,2048\nFM 33,247,253\nMvaa 148,156,180\n"
"FM 40,251,252\nFM 41,251,253\nMvaa 148,152,176\nv_lshlrev_b32 v252, 16, v205\nv_and_b32 v253, 0xffff0000, v205\nMvaa 156,152,184\nFM 34,247,252\nFM 35,247,253\nMvaa 156,156,188\n"
"FM 42,251,252\nFM 43,251,253\nMvaa 152,148,188\nv_lshlrev_b32 v252, 16, v206\nv_and_b32 v253, 0xffff0000, v206\nMvaa 152,144,184\nFM 36,247,252\nFM 37,247,253\nMvaa 92,136,184\n"
"FM 44,251,252\nFM 45,251,253\nMvaa 92,140,188\nD0 9,24,44,3072\nv_lshlrev_b32 v252, 16, v207\nMvaa 88,132,188\nv_and_b32 v253, 0xffff0000, v207\nFM 38,247,252\nMvaa 88,128,184\n"
"s_waitcnt vmcnt(23) lgkmcnt(0)\nMaaa 0,192,160\nRa 128,10,0\nFM 39,247,253\nMaaa 0,196,164\ns_add_u32 m0, s47, 49152\ns_add_u32 s44, s44, 0x40000\n"
"buffer_load_dwordx4 v[236:239], v20, s[92:95], s88 offen offset:2688\nMaaa 4,204,164\nRa 132,11,0\nFM 46,251,252\nMaaa 4,200,160\nFM 47,251,253\nv_lshlrev_b32 v252, 16, v220\n"
"Maaa 64,208,160\nRa 136,10,2048\nv_and_b32 v253, 0xffff0000, v220\nMaaa 64,212,164\nD0 6,24,44,0\nFM 48,247,252\nMaaa 68,220,164\nRa 140,11,2048\nFM 49,247,253\nMaaa 68,216,160\n"
"FM 56,251,252\nFM 57,251,253\nMaaa 76,216,168\nRa 144,10,8192\nv_lshlrev_b32 v252, 16, v221\nMaaa 76,220,172\nv_and_b32 v253, 0xffff0000, v221\nFM 50,247,252\nMaaa 72,212,172\n"
"Ra 148,11,8192\nFM 51,247,253\nMaaa 72,208,168\nFM 58,251,252\nFM 59,251,253\nMaaa 12,200,168\nRa 152,10,10240\nv_lshlrev_b32 v252, 16, v222\nMaaa 12,204,172\nD0 7,24,44,1024\n"
"v_and_b32 v253, 0xffff0000, v222\nMaaa 8,196,172\nRa 156,11,10240\nFM 52,247,252\nMaaa 8,192,168\nFM 53,247,253\nFM 60,251,252\nMaaa 16,192,176\nFM 61,251,253\n"
"v_lshlrev_b32 v252, 16, v223\nMaaa 16,196,180\nv_and_b32 v253, 0xffff0000, v223\nMaaa 20,204,180\nFM 54,247,252\nFM 55,247,253\nMaaa 20,200,176\nFM 62,251,252\nFM 63,251,253\n"
"Maaa 80,208,176\nv_permlane32_swap_b32 v32, v48\nv_permlane32_swap_b32 v33, v49\nMaaa 80,212,180\nD0 8,24,44,2048\nv_permlane32_swap_b32 v34, v50\nMaaa 84,220,180\n"
"v_permlane32_swap_b32 v35, v51\nv_permlane32_swap_b32 v36, v52\nMaaa 84,216,176\nv_permlane32_swap_b32 v37, v53\nv_permlane32_swap_b32 v38, v54\nMaaa 92,216,184\n"
"v_permlane32_swap_b32 v39, v55\nv_permlane32_swap_b32 v40, v56\nMaaa 92,220,188\nv_permlane32_swap_b32 v41, v57\nv_permlane32_swap_b32 v42, v58\nMaaa 88,212,188\n"
"v_permlane32_swap_b32 v43, v59\nv_permlane32_swap_b32 v44, v60\nMaaa 88,208,184\nv_permlane32_swap_b32 v45, v61\nv_permlane32_swap_b32 v46, v62\nMaaa 28,200,184\n"
"v_permlane32_swap_b32 v47, v63\nAD 32,32,48\nMaaa 28,204,188\nD0 9,24,44,3072\nAD 33,33,49\nMaaa 24,196,188\nAD 34,34,50\nAD 35,35,51\nMaaa 24,192,184\n"
"s_waitcnt vmcnt(20) lgkmcnt(0)\ns_barrier\nMaaa 32,192,224\nTa 160,12,0\nAD 36,36,52\nMaaa 32,196,228\nTa 162,12,2048\nMaaa 36,204,228\ns_add_u32 m0, s47, -16384\n"
"s_add_u32 s48, s48, 128\nMaaa 36,200,224\nTa 164,12,16384\nAD 37,37,53\nMaaa 96,208,224\nTa 166,12,18432\nAD 38,38,54\nMaaa 96,212,228\nD0 2,20,48,0\nAD 39,39,55\n"
"Maaa 100,220,228\nTa 168,13,0\nAD 40,40,56\nMaaa 100,216,224\nTa 170,13,2048\nAD 41,41,57\nMaaa 108,216,232\nAD 42,42,58\nAD 43,43,59\nMaaa 108,220,236\nTa 172,13,16384\n"
"AD 44,44,60\nMaaa 104,212,236\nTa 174,13,18432\nAD 45,45,61\nMaaa 104,208,232\nAD 46,46,62\nAD 47,47,63\nMaaa 44,200,232\nTa 176,14,0\nv_permlane16_swap_b32 v32, v40\n"
"Maaa 44,204,236\nTa 178,14,2048\nv_permlane16_swap_b32 v33, v41\nMaaa 40,196,236\nD0 3,20,48,1024\nv_permlane16_swap_b32 v34, v42\nMaaa 40,192,232\nTa 180,14,16384\n"
"v_permlane16_swap_b32 v35, v43\nMaaa 48,192,240\nTa 182,14,18432\nv_permlane16_swap_b32 v36, v44\nMaaa 48,196,244\nv_permlane16_swap_b32 v37, v45\nMaaa 52,204,244\nTa 184,15,0\n"
"v_permlane16_swap_b32 v38, v46\nMaaa 52,200,240\nTa 186,15,2048\nv_permlane16_swap_b32 v39, v47\nMaaa 112,208,240\nAD 32,32,40\nAD 33,33,41\nMaaa 112,212,244\nTa 188,15,16384\n"
"AD 34,34,42\nMaaa 116,220,244\nTa 190,15,18432\nAD 35,35,43\nMaaa 116,216,240\nD0 4,20,48,2048\nAD 36,36,44\nMaaa 124,216,248\nAD 37,37,45\nAD 38,38,46\nMaaa 124,220,252\n"
"AD 39,39,47\nv_add_f32_dpp v32, v32, v32 row_ror:8 row_mask:0xf bank_mask:0xf\nMaaa 120,212,252\nv_add_f32_dpp v33, v33, v33 row_ror:8 row_mask:0xf bank_mask:0xf\n"
"v_add_f32_dpp v34, v34, v34 row_ror:8 row_mask:0xf bank_mask:0xf\nMaaa 120,208,248\nv_add_f32_dpp v35, v35, v35 row_ror:8 row_mask:0xf bank_mask:0xf\n"
"v_add_f32_dpp v36, v36, v36 row_ror:8 row_mask:0xf bank_mask:0xf\nMaaa 60,200,248\nv_add_f32_dpp v37, v37, v37 row_ror:8 row_mask:0xf bank_mask:0xf\n"
"v_add_f32_dpp v38, v38, v38 row_ror:8 row_mask:0xf bank_mask:0xf\nMaaa 60,204,252\nD0 5,20,48,3072\nv_add_f32_dpp v39, v39, v39 row_ror:8 row_mask:0xf bank_mask:0xf\n"
"Maaa 56,196,252\nv_add_f32_dpp v32, v32, v32 row_ror:4 row_mask:0xf bank_mask:0xf\nv_add_f32_dpp v33, v33, v33 row_ror:4 row_mask:0xf bank_mask:0xf\nMaaa 56,192,248\nWL 0\n"
"Mvaa 64,128,160\nTa 224,16,0\n.W553:\ns_cbranch_execz .W557\nv_add_f32_dpp v34, v34, v34 row_ror:4 row_mask:0xf bank_mask:0xf\nMvaa 64,132,164\nTa 226,16,2048\nMvaa 68,140,164\n"
"s_add_u32 m0, s47, -65536\ns_add_u32 s45, s45, 128\nMvaa 68,136,160\nTa 228,16,16384\nv_add_f32_dpp v35, v35, v35 row_ror:4 row_mask:0xf bank_mask:0xf\nMvaa 128,144,160\n"
"Ta 230,16,18432\nv_add_f32_dpp v36, v36, v36 row_ror:4 row_mask:0xf bank_mask:0xf\nMvaa 128,148,164\nD0 2,20,45,0\n"
"v_add_f32_dpp v37, v37, v37 row_ror:4 row_mask:0xf bank_mask:0xf\nMvaa 132,156,164\nTa 232,17,0\nv_add_f32_dpp v38, v38, v38 row_ror:4 row_mask:0xf bank_mask:0xf\n"
"Mvaa 132,152,160\nTa 234,17,2048\nv_add_f32_dpp v39, v39, v39 row_ror:4 row_mask:0xf bank_mask:0xf\nMvaa 140,152,168\n"
"v_add_f32_dpp v32, v32, v32 row_ror:2 row_mask:0xf bank_mask:0xf\nv_add_f32_dpp v33, v33, v33 row_ror:2 row_mask:0xf bank_mask:0xf\nMvaa 140,156,172\nTa 236,17,16384\n"
"v_add_f32_dpp v34, v34, v34 row_ror:2 row_mask:0xf bank_mask:0xf\nMvaa 136,148,172\nTa 238,17,18432\nv_add_f32_dpp v35, v35, v35 row_ror:2 row_mask:0xf bank_mask:0xf\n"
"Mvaa 136,144,168\nv_add_f32_dpp v36, v36, v36 row_ror:2 row_mask:0xf bank_mask:0xf\nv_add_f32_dpp v37, v37, v37 row_ror:2 row_mask:0xf bank_mask:0xf\nMvaa 76,136,168\nTa 240,18,0\n"
"v_add_f32_dpp v38, v38, v38 row_ror:2 row_mask:0xf bank_mask:0xf\nMvaa 76,140,172\nTa 242,18,2048\nv_add_f32_dpp v39, v39, v39 row_ror:2 row_mask:0xf bank_mask:0xf\n"
"Mvaa 72,132,172\nD0 3,20,45,1024\nv_lshrrev_b32 v48, 4, v0\nMvaa 72,128,168\nTa 244,18,16384\nv_and_b32 v49, 1, v0\nMvaa 80,128,176\nTa 246,18,18432\nv_lshlrev_b32 v49, 5, v49\n"
"Mvaa 80,132,180\nv_lshl_add_u32 v49, v48, 6, v49\nMvaa 84,140,180\nTa 248,19,0\ns_lshl_b32 s2, s11, 8\nMvaa 84,136,176\nTa 250,19,2048\ns_add_u32 s2, s2, 132096\nMvaa 144,144,176\n"
"v_add_u32 v49, s2, v49\nds_write_b128 v49, v[32:35]\nMvaa 144,148,180\nTa 252,19,16384\nds_write_b128 v49, v[36:39] offset:16\nMvaa 148,156,180\nTa 254,19,18432\n"
"v_and_b32 v50, 31, v0\nMvaa 148,152,176\nD0 4,20,45,2048\nv_lshlrev_b32 v50, 2, v50\nMvaa 156,152,184\ns_lshl_b32 s2, s11, 10\ns_add_u32 s2, s2, 133120\nMvaa 156,156,188\n"
"v_add_u32 v51, s2, v50\nv_add_u32 v50, 132096, v50\nMvaa 152,148,188\nv_lshl_add_u32 v52, v48, 4, s2\nMvaa 152,144,184\nMvaa 92,136,184\nMvaa 92,140,188\nD0 5,20,45,3072\n"
"Mvaa 88,132,188\nMvaa 88,128,184\ns_waitcnt vmcnt(18) lgkmcnt(0)\ns_barrier\nMvaa 96,128,224\nRa 192,10,16384\nds_read_b32 v32, v50 offset:0\nMvaa 96,132,228\n"
"s_add_u32 m0, s47, 0\ns_add_u32 s44, s44, 0x40000\nMvaa 100,140,228\nRa 196,11,16384\nMvaa 100,136,224\nds_read_b32 v33, v50 offset:256\nds_read_b32 v34, v50 offset:512\n"
"Mvaa 160,144,224\nRa 200,10,18432\nMvaa 160,148,228\nD0 6,24,44,0\nMvaa 164,156,228\nRa 204,11,18432\nds_read_b32 v35, v50 offset:768\nMvaa 164,152,224\n"
"ds_read_b32 v36, v50 offset:128\nMvaa 172,152,232\nRa 208,10,24576\nds_read_b32 v37, v50 offset:384\nMvaa 172,156,236\nds_read_b32 v38, v50 offset:640\nMvaa 168,148,236\n"
"Ra 212,11,24576\nMvaa 168,144,232\nds_read_b32 v39, v50 offset:896\nMvaa 108,136,232\nRa 216,10,26624\nMvaa 108,140,236\nD0 7,24,44,1024\nMvaa 104,132,236\nRa 220,11,26624\n"
"Mvaa 104,128,232\nMvaa 112,128,240\nMvaa 112,132,244\nMvaa 116,140,244\nMvaa 116,136,240\nMvaa 176,144,240\nMvaa 176,148,244\nD0 8,24,44,2048\nMvaa 180,156,244\nWL 2\nAD 32,32,33\n"
"Mvaa 180,152,240\nAD 34,34,35\nMvaa 188,152,248\nAD 36,36,37\nAD 38,38,39\nMvaa 188,156,252\nAD 32,32,34\nMvaa 184,148,252\nAD 36,36,38\nMvaa 184,144,248\nCV 32,32,36\n"
"v_and_b32 v36, 0xffff0000, v32\nMvaa 124,136,248\nv_lshlrev_b32 v32, 16, v32\nMvaa 124,140,252\nD0 9,24,44,3072\nMvaa 120,132,252\nv_mul_f32 v33, 0xbfb8aa3b, v32\n"
"Mvaa 120,128,248\nWL 0\nMaaa 32,192,224\nRa 128,10,32768\nMaaa 32,196,228\ns_add_u32 m0, s47, 16384\ns_add_u32 s44, s44, 0x40000\nMaaa 36,204,228\nRa 132,11,32768\nEX 33,33\n"
"Maaa 36,200,224\nv_add_f32 v33, 1.0, v33\nMaaa 96,208,224\nRa 136,10,34816\nv_rcp_f32 v33, v33\nMaaa 96,212,228\nD0 6,24,44,0\nMaaa 100,220,228\nRa 140,11,34816\nMU 33,32,33\n"
"Maaa 100,216,224\nCV 33,33,33\nMaaa 108,216,232\nRa 144,10,40960\nv_lshlrev_b32 v33, 16, v33\nMaaa 108,220,236\nMU 33,33,36\nMaaa 104,212,236\nRa 148,11,40960\nMaaa 104,208,232\n"
"CV 33,33,33\nv_lshlrev_b32 v33, 16, v33\nMaaa 44,200,232\nRa 152,10,43008\nMaaa 44,204,236\nD0 7,24,44,1024\nMaaa 40,196,236\nRa 156,11,43008\nds_write_b32 v51, v33\n"
"Maaa 40,192,232\nRv 244,52,0\nMaaa 48,192,240\nRv 248,52,64\ns_and_b32 s2, s74, 15\nMaaa 48,196,244\ns_sub_u32 s2, s2, 2\ns_cmp_lt_u32 s2, 14\ns_cselect_b32 s5, 0, 0x40000000\n"
"Maaa 52,204,244\ns_and_b32 s2, s2, 15\nMaaa 52,200,240\ns_mul_i32 s4, s73, 14\ns_add_u32 s4, s4, s2\nMaaa 112,208,240\nv_and_b32 v49, 15, v0\nMaaa 112,212,244\nD0 8,24,44,2048\n"
"Maaa 116,220,244\nv_lshlrev_b32 v50, 3, v49\nv_and_b32 v51, 1, v48\nMaaa 116,216,240\nv_lshl_add_u32 v50, v51, 2, v50\nMaaa 124,216,248\ns_mul_i32 s2, s11, 112\n"
"v_add_u32 v50, s2, v50\nMaaa 124,220,252\nv_lshrrev_b32 v51, 4, v50\nMaaa 120,212,252\nv_lshrrev_b32 v48, 1, v48\nMaaa 120,208,248\nv_mad_u32_u24 v51, v48, 28, v51\n"
"v_mul_u32_u24 v51, 7168, v51\nMaaa 60,200,248\nv_and_b32 v50, 15, v50\nMaaa 60,204,252\nD0 9,24,44,3072\nMaaa 56,196,252\nv_lshl_add_u32 v51, v50, 2, v51\n"
"v_cmp_gt_u32 vcc, 14, v49\ns_lshl_b32 s4, s4, 6\ns_add_u32 s4, s4, 0xc04c800\nv_cndmask_b32 v51, v254, v51, vcc\nMaaa 56,192,248\ns_waitcnt vmcnt(16) lgkmcnt(3)\ns_barrier\n"
"Maaa 0,192,160\nTa 224,16,32768\ns_or_b32 s4, s4, s5\nMaaa 0,196,164\nTa 226,16,34816\nMaaa 4,204,164\ns_add_u32 m0, s47, -49152\ns_add_u32 s48, s48, 128\nMaaa 4,200,160\n"
"Ta 228,16,49152\nWL 3\nMaaa 64,208,160\nTa 230,16,51200\nMaaa 64,212,164\nD0 2,20,48,0\nMaaa 68,220,164\nTa 232,17,32768\nv_lshlrev_b32 v252, 16, v224\nMaaa 68,216,160\n"
"Ta 234,17,34816\nMaaa 76,216,168\nv_and_b32 v253, 0xffff0000, v224\nMU 32,244,252\nMaaa 76,220,172\nTa 236,17,49152\nMaaa 72,212,172\nTa 238,17,51200\nMaaa 72,208,168\n"
"MU 33,244,253\nMU 40,248,252\nMaaa 12,200,168\nTa 240,18,32768\nMaaa 12,204,172\nTa 242,18,34816\nMaaa 8,196,172\nD0 3,20,48,1024\nMU 41,248,253\nMaaa 8,192,168\nTa 244,18,49152\n"
"Maaa 16,192,176\nTa 246,18,51200\nv_lshlrev_b32 v252, 16, v225\nMaaa 16,196,180\nv_and_b32 v253, 0xffff0000, v225\nMaaa 20,204,180\nTa 248,19,32768\nMaaa 20,200,176\n"
"Ta 250,19,34816\nMU 34,244,252\nMaaa 80,208,176\nMU 35,244,253\nMaaa 80,212,180\nTa 252,19,49152\nMaaa 84,220,180\nTa 254,19,51200\nMU 42,248,252\nMaaa 84,216,176\n"
"D0 4,20,48,2048\nMaaa 92,216,184\nMU 43,248,253\nv_lshlrev_b32 v252, 16, v226\nMaaa 92,220,188\nv_and_b32 v253, 0xffff0000, v226\nMaaa 88,212,188\nMU 36,244,252\nMaaa 88,208,184\n"
"MU 37,244,253\nMU 44,248,252\nMaaa 28,200,184\nMU 45,248,253\nMaaa 28,204,188\nD0 5,20,48,3072\nMaaa 24,196,188\nv_lshlrev_b32 v252, 16, v227\nv_and_b32 v253, 0xffff0000, v227\n"
"Maaa 24,192,184\nWL 0\nMvaa 96,128,224\nTa 160,12,32768\nMU 38,244,252\nMvaa 96,132,228\nTa 162,12,34816\nMvaa 100,140,228\ns_add_u32 m0, s47, -32768\ns_add_u32 s45, s45, 128\n"
"Mvaa 100,136,224\nTa 164,12,49152\nMU 39,244,253\nMvaa 160,144,224\nTa 166,12,51200\nMvaa 160,148,228\nD0 2,20,45,0\nMvaa 164,156,228\nTa 168,13,32768\nMU 46,248,252\n"
"Mvaa 164,152,224\nTa 170,13,34816\nMvaa 172,152,232\nMU 47,248,253\nv_lshlrev_b32 v252, 16, v228\nMvaa 172,156,236\nTa 172,13,49152\nMvaa 168,148,236\nTa 174,13,51200\n"
"Mvaa 168,144,232\nv_and_b32 v253, 0xffff0000, v228\nFM 32,245,252\nMvaa 108,136,232\nTa 176,14,32768\nMvaa 108,140,236\nTa 178,14,34816\nMvaa 104,132,236\nD0 3,20,45,1024\n"
"FM 33,245,253\nMvaa 104,128,232\nTa 180,14,49152\nMvaa 112,128,240\nTa 182,14,51200\nFM 40,249,252\nMvaa 112,132,244\nFM 41,249,253\nMvaa 116,140,244\nTa 184,15,32768\n"
"Mvaa 116,136,240\nTa 186,15,34816\nv_lshlrev_b32 v252, 16, v229\nMvaa 176,144,240\nv_and_b32 v253, 0xffff0000, v229\nMvaa 176,148,244\nTa 188,15,49152\nMvaa 180,156,244\n"
"Ta 190,15,51200\nFM 34,245,252\nMvaa 180,152,240\nD0 4,20,45,2048\nMvaa 188,152,248\nFM 35,245,253\nFM 42,249,252\nMvaa 188,156,252\nFM 43,249,253\nMvaa 184,148,252\n"
"v_lshlrev_b32 v252, 16, v230\nMvaa 184,144,248\nv_and_b32 v253, 0xffff0000, v230\nFM 36,245,252\nMvaa 124,136,248\nFM 37,245,253\nMvaa 124,140,252\nD0 5,20,45,3072\n"
"Mvaa 120,132,252\nFM 44,249,252\nFM 45,249,253\nMvaa 120,128,248\ns_waitcnt vmcnt(16) lgkmcnt(0)\ns_barrier\nMvaa 64,128,160\nRa 192,10,49152\nv_lshlrev_b32 v252, 16, v231\n"
"Mvaa 64,132,164\ns_add_u32 m0, s47, 32768\ns_add_u32 s44, s44, 0x40000\nMvaa 68,140,164\nRa 196,11,49152\nMvaa 68,136,160\nv_and_b32 v253, 0xffff0000, v231\nFM 38,245,252\n"
"Mvaa 128,144,160\nRa 200,10,51200\nMvaa 128,148,164\nD0 6,24,44,0\nMvaa 132,156,164\nRa 204,11,51200\nFM 39,245,253\nMvaa 132,152,160\nFM 46,249,252\nMvaa 140,152,168\n"
"Ra 208,10,57344\nFM 47,249,253\nMvaa 140,156,172\nv_lshlrev_b32 v252, 16, v232\nMvaa 136,148,172\nRa 212,11,57344\nMvaa 136,144,168\nv_and_b32 v253, 0xffff0000, v232\n"
"FM 32,246,252\nMvaa 76,136,168\nRa 216,10,59392\nMvaa 76,140,172\nD0 7,24,44,1024\nMvaa 72,132,172\nRa 220,11,59392\nFM 33,246,253\nMvaa 72,128,168\nFM 40,250,252\n"
"Mvaa 80,128,176\nFM 41,250,253\nv_lshlrev_b32 v252, 16, v233\nMvaa 80,132,180\nv_and_b32 v253, 0xffff0000, v233\nMvaa 84,140,180\nFM 34,246,252\nMvaa 84,136,176\nFM 35,246,253\n"
"FM 42,250,252\nMvaa 144,144,176\nFM 43,250,253\nMvaa 144,148,180\nD0 8,24,44,2048\nMvaa 148,156,180\nv_lshlrev_b32 v252, 16, v234\nv_and_b32 v253, 0xffff0000, v234\n"
"Mvaa 148,152,176\nFM 36,246,252\nMvaa 156,152,184\nFM 37,246,253\nFM 44,250,252\nMvaa 156,156,188\nFM 45,250,253\nMvaa 152,148,188\nv_lshlrev_b32 v252, 16, v235\nMvaa 152,144,184\n"
"v_and_b32 v253, 0xffff0000, v235\nFM 38,246,252\nMvaa 92,136,184\nFM 39,246,253\nMvaa 92,140,188\nD0 9,24,44,3072\nMvaa 88,132,188\nFM 46,250,252\nFM 47,250,253\nMvaa 88,128,184\n"
"WL 0\nMaaa 0,192,160\nRa 128,10,0\nv_lshlrev_b32 v252, 16, v236\nMaaa 0,196,164\ns_add_u32 m0, s47, 49152\ns_add_u32 s44, s44, 0x40000\nMaaa 4,204,164\nRa 132,11,0\n"
"Maaa 4,200,160\nv_and_b32 v253, 0xffff0000, v236\nFM 32,247,252\nMaaa 64,208,160\nRa 136,10,2048\nMaaa 64,212,164\nD0 6,24,44,0\nMaaa 68,220,164\nRa 140,11,2048\nFM 33,247,253\n"
"Maaa 68,216,160\nFM 40,251,252\nMaaa 76,216,168\nRa 144,10,8192\nFM 41,251,253\nMaaa 76,220,172\nv_lshlrev_b32 v252, 16, v237\nMaaa 72,212,172\nRa 148,11,8192\nMaaa 72,208,168\n"
"v_and_b32 v253, 0xffff0000, v237\nFM 34,247,252\nMaaa 12,200,168\nRa 152,10,10240\nMaaa 12,204,172\nD0 7,24,44,1024\nMaaa 8,196,172\nRa 156,11,10240\nFM 35,247,253\n"
"Maaa 8,192,168\nFM 42,251,252\nMaaa 16,192,176\nFM 43,251,253\nv_lshlrev_b32 v252, 16, v238\nMaaa 16,196,180\nv_and_b32 v253, 0xffff0000, v238\nMaaa 20,204,180\nFM 36,247,252\n"
"Maaa 20,200,176\nFM 37,247,253\nFM 44,251,252\nMaaa 80,208,176\nFM 45,251,253\nMaaa 80,212,180\nD0 8,24,44,2048\nMaaa 84,220,180\nv_lshlrev_b32 v252, 16, v239\n"
"v_and_b32 v253, 0xffff0000, v239\nMaaa 84,216,176\nFM 38,247,252\nMaaa 92,216,184\nFM 39,247,253\nFM 46,251,252\nMaaa 92,220,188\nFM 47,251,253\nMaaa 88,212,188\n"
"v_permlane32_swap_b32 v32, v40\nMaaa 88,208,184\nv_permlane32_swap_b32 v33, v41\nv_permlane32_swap_b32 v34, v42\nMaaa 28,200,184\nv_permlane32_swap_b32 v35, v43\nMaaa 28,204,188\n"
"D0 9,24,44,3072\nMaaa 24,196,188\nv_permlane32_swap_b32 v36, v44\nv_permlane32_swap_b32 v37, v45\nMaaa 24,192,184\nWV 16\ns_barrier\nMaaa 32,192,224\nTa 160,12,0\n"
"v_permlane32_swap_b32 v38, v46\nMaaa 32,196,228\nTa 162,12,2048\nMaaa 36,204,228\ns_add_u32 m0, s47, -16384\ns_add_u32 s48, s48, 128\nMaaa 36,200,224\nTa 164,12,16384\n"
"v_permlane32_swap_b32 v39, v47\nMaaa 96,208,224\nTa 166,12,18432\nMaaa 96,212,228\nD0 2,20,48,0\nMaaa 100,220,228\nTa 168,13,0\nAD 32,32,40\nMaaa 100,216,224\nTa 170,13,2048\n"
"Maaa 108,216,232\nAD 33,33,41\nAD 34,34,42\nMaaa 108,220,236\nTa 172,13,16384\nMaaa 104,212,236\nTa 174,13,18432\nMaaa 104,208,232\nAD 35,35,43\nAD 36,36,44\nMaaa 44,200,232\n"
"Ta 176,14,0\nMaaa 44,204,236\nTa 178,14,2048\nMaaa 40,196,236\nD0 3,20,48,1024\nAD 37,37,45\nMaaa 40,192,232\nTa 180,14,16384\nMaaa 48,192,240\nTa 182,14,18432\nAD 38,38,46\n"
"Maaa 48,196,244\nAD 39,39,47\n.W554:\ns_cbranch_execz .W558\nMaaa 52,204,244\nTa 184,15,0\nMaaa 52,200,240\nTa 186,15,2048\nv_permlane16_swap_b32 v32, v36\nMaaa 112,208,240\n"
"v_permlane16_swap_b32 v33, v37\nMaaa 112,212,244\nTa 188,15,16384\nMaaa 116,220,244\nTa 190,15,18432\nv_permlane16_swap_b32 v34, v38\nMaaa 116,216,240\nD0 4,20,48,2048\n"
"Maaa 124,216,248\nv_permlane16_swap_b32 v35, v39\nAD 32,32,36\nMaaa 124,220,252\nAD 33,33,37\nMaaa 120,212,252\nAD 34,34,38\nMaaa 120,208,248\nAD 35,35,39\nSv 32,51,16,4,0\n"
"Maaa 60,200,248\nMaaa 60,204,252\nD0 5,20,48,3072\nMaaa 56,196,252\nMaaa 56,192,248\nWL 0\nMvaa 64,128,160\nTa 224,16,0\ns_lshr_b32 s3, s11, 1\nMvaa 64,132,164\nTa 226,16,2048\n"
"Mvaa 68,140,164\ns_lshl_b32 s3, s3, 13\nMvaa 68,136,160\nTa 228,16,16384\nMvaa 128,144,160\nTa 230,16,18432\nMvaa 128,148,164\ns_lshl_b32 s2, s11, 12\nMvaa 132,156,164\n"
"Ta 232,17,0\nMvaa 132,152,160\nTa 234,17,2048\nMvaa 140,152,168\ns_add_u32 s2, s2, s3\ns_add_u32 s2, s2, s72\nMvaa 140,156,172\nTa 236,17,16384\nMvaa 136,148,172\nTa 238,17,18432\n"
"Mvaa 136,144,168\ns_lshl_b32 s3, s11, 12\nMvaa 76,136,168\nTa 240,18,0\nMvaa 76,140,172\nTa 242,18,2048\nMvaa 72,132,172\ns_add_u32 s89, s3, 0x0\nMvaa 72,128,168\nTa 244,18,16384\n"
"Mvaa 80,128,176\nTa 246,18,18432\ns_mov_b32 s88, 0xffff8000\nMvaa 80,132,180\ns_lshl_b32 s4, s13, 1\nMvaa 84,140,180\nTa 248,19,0\nMvaa 84,136,176\nTa 250,19,2048\n"
"Mvaa 144,144,176\ns_sub_u32 s79, 15, s4\nMvaa 144,148,180\nTa 252,19,16384\nMvaa 148,156,180\nTa 254,19,18432\nMvaa 148,152,176\ns_add_u32 s96, s2, 0x1310000\nMvaa 156,152,184\n"
"s_mov_b32 s63, 0x53800000\ns_mov_b32 s64, 0x2b800000\nMvaa 156,156,188\ns_sub_u32 s98, s4, 1\nMvaa 152,148,188\ns_lshl_b32 s5, s13, 5\nMvaa 152,144,184\ns_add_u32 s5, s5, s11\n"
"Mvaa 92,136,184\ns_lshl_b32 s65, s5, 9\nMvaa 92,140,188\ns_add_u32 s65, s65, s72\nMvaa 88,132,188\ns_add_u32 s65, s65, 0x1580000\nMvaa 88,128,184\ns_waitcnt vmcnt(13) lgkmcnt(0)\n"
"s_barrier\nMvaa 96,128,224\nRa 192,10,16384\ns_and_b32 s66, s72, 0x2000\ns_add_u32 m0, s47, 65536\ns_add_u32 s46, s56, 0x0\nMvaa 96,132,228\ns_add_u32 s66, s66, s56\n"
"Mvaa 100,140,228\nRa 196,11,16384\nD1 1,16,46,0\nMvaa 100,136,224\ns_add_u32 s66, s66, 0x100000\nMvaa 160,144,224\nRa 200,10,18432\nMvaa 160,148,228\ns_and_b32 s5, s12, 1\n"
"Mvaa 164,156,228\nRa 204,11,18432\nMvaa 164,152,224\ns_mul_i32 s58, s5, 0x202000\nD1 1,16,46,1024\nMvaa 172,152,232\nRa 208,10,24576\ns_xor_b32 s58, s58, s72\nMvaa 172,156,236\n"
"s_add_u32 s58, s58, 0x15c0000\nMvaa 168,148,236\nRa 212,11,24576\nMvaa 168,144,232\ns_lshl_b32 s5, s5, 8\nMvaa 108,136,232\nRa 216,10,26624\nD1 1,16,46,2048\nMvaa 108,140,236\n"
"s_add_u32 s61, s58, s5\nMvaa 104,132,236\nRa 220,11,26624\nMvaa 104,128,232\ns_lshl4_add_u32 s60, s13, s61\nMvaa 112,128,240\ns_add_u32 s54, s60, 0\ns_xor_b32 s60, s54, 0x100\n"
"Mvaa 112,132,244\ns_cmp_eq_u32 s11, 0\ns_cselect_b32 s54, s54, 0x40000000\nD1 1,16,46,3072\nMvaa 116,140,244\ns_getpc_b64 s[8:9]\n.PC167:\ns_add_u32 s8, s8, .HD143-.PC167\n"
"s_addc_u32 s9, s9, 0\nMvaa 116,136,240\ns_mov_b32 s97, 0xff800000\nMvaa 176,144,240\ns_mov_b32 s90, 3\ns_cmp_lt_u32 s11, 2\ns_cselect_b32 s92, 2, 0\ns_add_u32 m0, s47, 81920\n"
"s_add_u32 s46, s56, 0x1000\nMvaa 176,148,244\ns_add_u32 s86, s2, 0x20000\nMvaa 180,156,244\ns_add_u32 s87, s2, 0x410000\ns_cmp_eq_u32 s13, 7\ns_cselect_b32 s92, s92, 0\n"
"D1 1,16,46,0\nMvaa 180,152,240\ns_cmp_gt_u32 s13, 5\ns_cselect_b32 s4, s12, 15\nMvaa 188,152,248\ns_cmp_lt_u32 s4, 14\ns_cselect_b32 s5, s11, 3\nMvaa 188,156,252\n"
"s_cmp_lt_u32 s5, 2\ns_cselect_b32 s91, 1, 100\nMvaa 184,148,252\nv_and_b32 v27, 0x70, v1\nbuffer_load_dword v27, v27, s[16:19], s61 offen offset:128 sc0 sc1\nMvaa 184,144,248\n"
"s_lshr_b32 s2, s13, 2\ns_bfe_u32 s3, s13, 0x10001\nD1 1,16,46,1024\nMvaa 124,136,248\ns_mul_i32 s3, s3, 17\ns_add_u32 s2, s2, s3\nMvaa 124,140,252\ns_lshl2_add_u32 s3, s14, 0\n"
"Mvaa 120,132,252\ns_lshl_b32 s3, s3, 2\ns_lshr_b32 s3, 0xe420ca86, s3\nMvaa 120,128,248\nWL 0\nMaaa 32,192,224\nRa 128,10,32768\ns_and_b32 s3, s3, 15\nD1 1,16,46,2048\n"
"Maaa 32,196,228\ns_add_u32 s2, s2, s3\ns_cmp_eq_u32 s2, 32\ns_cselect_b32 s4, 16, s2\nMaaa 36,204,228\nRa 132,11,32768\ns_mul_i32 s75, s4, 65536\nMaaa 36,200,224\n"
"s_lshr_b32 s2, s13, 2\ns_bfe_u32 s3, s13, 0x10001\nMaaa 96,208,224\nRa 136,10,34816\nMaaa 96,212,228\ns_mul_i32 s3, s3, 17\ns_add_u32 s2, s2, s3\nD1 1,16,46,3072\n"
"Maaa 100,220,228\nRa 140,11,34816\ns_lshl2_add_u32 s3, s14, 1\nMaaa 100,216,224\ns_lshl_b32 s3, s3, 2\nMaaa 108,216,232\nRa 144,10,40960\ns_lshr_b32 s3, 0xe420ca86, s3\n"
"s_add_u32 m0, s47, 16384\ns_add_u32 s46, s56, 0x4000\nMaaa 108,220,236\ns_and_b32 s3, s3, 15\ns_add_u32 s2, s2, s3\nMaaa 104,212,236\nRa 148,11,40960\ns_cmp_eq_u32 s2, 32\n"
"s_cselect_b32 s4, 16, s2\nD1 1,16,46,0\nMaaa 104,208,232\ns_mul_i32 s76, s4, 65536\ns_lshr_b32 s2, s13, 2\nMaaa 44,200,232\nRa 152,10,43008\ns_bfe_u32 s3, s13, 0x10001\n"
"Maaa 44,204,236\ns_mul_i32 s3, s3, 17\nMaaa 40,196,236\nRa 156,11,43008\ns_add_u32 s2, s2, s3\nMaaa 40,192,232\ns_lshl2_add_u32 s3, s14, 2\ns_lshl_b32 s3, s3, 2\nD1 1,16,46,1024\n"
"Maaa 48,192,240\ns_lshr_b32 s3, 0xe420ca86, s3\ns_and_b32 s3, s3, 15\nMaaa 48,196,244\ns_add_u32 s2, s2, s3\ns_cmp_eq_u32 s2, 32\ns_cselect_b32 s4, 16, s2\nMaaa 52,204,244\n"
"s_mul_i32 s77, s4, 65536\ns_lshr_b32 s2, s13, 2\nMaaa 52,200,240\ns_bfe_u32 s3, s13, 0x10001\ns_mul_i32 s3, s3, 17\nMaaa 112,208,240\ns_add_u32 s2, s2, s3\nD1 1,16,46,2048\n"
"Maaa 112,212,244\ns_lshl2_add_u32 s3, s14, 3\ns_lshl_b32 s3, s3, 2\nMaaa 116,220,244\ns_lshr_b32 s3, 0xe420ca86, s3\ns_and_b32 s3, s3, 15\nMaaa 116,216,240\ns_add_u32 s2, s2, s3\n"
"s_cmp_eq_u32 s2, 32\ns_cselect_b32 s4, 16, s2\nMaaa 124,216,248\ns_mul_i32 s78, s4, 65536\ns_lshr_b32 s3, s11, 1\nMaaa 124,220,252\ns_lshl_b32 s3, s3, 15\ns_add_u32 s2, s72, s3\n"
"D1 1,16,46,3072\nMaaa 120,212,252\ns_and_b32 s3, s11, 1\nMaaa 120,208,248\ns_lshl_b32 s3, s3, 14\ns_add_u32 s2, s2, s3\nMaaa 60,200,248\ns_add_u32 s2, s2, 0x400000\n"
"s_mov_b32 s55, 4096\nMaaa 60,204,252\ns_add_u32 s75, s75, s2\nMaaa 56,196,252\ns_add_u32 s76, s76, s2\ns_add_u32 s77, s77, s2\nMaaa 56,192,248\ns_waitcnt vmcnt(18) lgkmcnt(0)\n"
"s_barrier\nMaaa 0,192,160\nTa 224,16,32768\ns_add_u32 s78, s78, s2\nMaaa 0,196,164\nTa 226,16,34816\nMaaa 4,204,164\nMaaa 4,200,160\nTa 228,16,49152\nMaaa 64,208,160\n"
"Ta 230,16,51200\nMaaa 64,212,164\nMaaa 68,220,164\nTa 232,17,32768\nMaaa 68,216,160\nTa 234,17,34816\nMaaa 76,216,168\nWV 7\nv_cmp_ne_u32 vcc, s15, v27\ns_nop 0\n"
"s_cbranch_vccnz .S168\n.S169:\ns_sub_u32 s45, s86, 131072\nMaaa 76,220,172\nTa 236,17,49152\ns_add_u32 m0, s47, 0\ns_add_u32 s46, s45, 0x0\nMaaa 72,212,172\nTa 238,17,51200\n"
"Maaa 72,208,168\nD1 1,16,46,0\nMaaa 12,200,168\nTa 240,18,32768\ns_mov_b32 s36, s68\nMaaa 12,204,172\nTa 242,18,34816\nMaaa 8,196,172\ns_and_b32 s37, s69, 0xffff\nMaaa 8,192,168\n"
"Ta 244,18,49152\nMaaa 16,192,176\nTa 246,18,51200\nD1 1,16,46,1024\nMaaa 16,196,180\ns_mov_b32 s38, 0x1c0000\nMaaa 20,204,180\nTa 248,19,32768\nMaaa 20,200,176\nTa 250,19,34816\n"
"Maaa 80,208,176\ns_mov_b32 s39, 0x20000\ns_and_b32 s2, s74, 15\nMaaa 80,212,180\nTa 252,19,49152\nD1 1,16,46,2048\nMaaa 84,220,180\nTa 254,19,51200\nMaaa 84,216,176\n"
"s_max_u32 s2, s2, 2\nMaaa 92,216,184\ns_sub_u32 s2, s2, 2\ns_mul_i32 s3, s73, 14\nMaaa 92,220,188\ns_add_u32 s2, s2, s3\nMaaa 88,212,188\nD1 1,16,46,3072\nMaaa 88,208,184\n"
"s_lshl2_add_u32 s2, s2, s11\nMaaa 28,200,184\ns_mul_i32 s3, s2, 4682\ns_lshr_b32 s3, s3, 16\nMaaa 28,204,188\ns_mul_i32 s22, s3, 14\nMaaa 24,196,188\ns_sub_u32 s22, s2, s22\n"
"Maaa 24,192,184\nWL 0\nMvaa 96,128,224\nTa 160,12,32768\ns_add_u32 m0, s47, -32768\ns_add_u32 s46, s56, 0x5000\nD1 1,16,46,0\nMvaa 96,132,228\nTa 162,12,34816\nMvaa 100,140,228\n"
"s_and_b32 s21, s3, 28\nMvaa 100,136,224\nTa 164,12,49152\nMvaa 108,136,232\nTa 166,12,51200\ns_add_u32 s21, s21, s3\nMvaa 108,140,236\nD1 1,16,46,1024\nMvaa 104,132,236\n"
"Ta 168,13,32768\nMvaa 104,128,232\nTa 170,13,34816\nMvaa 112,128,240\ns_lshl_b32 s21, s21, 4\ns_add_u32 s21, s21, 1024\nMvaa 112,132,244\nTa 172,13,49152\nMvaa 116,140,244\n"
"Ta 174,13,51200\nD1 1,16,46,2048\nMvaa 116,136,240\ns_xor_b32 s3, s3, 28\nMvaa 124,136,248\nTa 176,14,32768\ns_bfe_u32 s41, s3, 0x30002\nMvaa 124,140,252\nTa 178,14,34816\n"
"s_cmp_gt_u32 s22, 6\ns_cselect_b32 s2, 1179648, 0\ns_cselect_b32 s1, 16, 0\nMvaa 120,132,252\ns_add_u32 s41, s41, s1\nCV 48,96,97\nMvaa 120,128,248\nTa 180,14,49152\n"
"D1 1,16,46,3072\nMvaa 160,144,224\nTa 182,14,51200\ns_lshl4_add_u32 s41, s41, 0xc048400\nMvaa 160,148,228\ns_and_b32 s1, s3, 28\nMvaa 164,156,228\nTa 184,15,32768\n"
"s_add_u32 m0, s47, -65536\ns_add_u32 s46, s45, 0x10000\nMvaa 164,152,224\nTa 186,15,34816\nCV 49,98,99\nMvaa 172,152,232\nD1 1,16,46,0\ns_add_u32 s3, s3, s1\nMvaa 172,156,236\n"
"Ta 188,15,49152\nCV 50,100,101\nMvaa 168,148,236\nTa 190,15,51200\nMvaa 168,144,232\ns_lshl_b32 s3, s3, 11\nCV 51,102,103\nMvaa 176,144,240\ns_add_u32 s2, s2, s3\n"
"s_lshl_b32 s3, s22, 17\nMvaa 176,148,244\nD1 1,16,46,1024\nMvaa 180,156,244\ns_add_u32 s2, s2, s3\nCV 52,104,105\nMvaa 180,152,240\ns_add_u32 s20, s2, 0xce0000\nMvaa 188,152,248\n"
"s_lshl_b32 s2, s22, 7\ns_add_u32 s40, s2, 0xc04b800\nMvaa 188,156,252\ns_mul_i32 s2, s21, 896\nMvaa 184,148,252\nD1 1,16,46,2048\nCV 53,106,107\nMvaa 184,144,248\nWL 0\ns_barrier\n"
"Mvaa 64,128,160\nRa 192,10,49152\ns_lshl_b32 s3, s22, 6\nMvaa 64,132,164\ns_add_u32 s22, s2, s3\nMvaa 68,140,164\nRa 196,11,49152\nMvaa 68,136,160\nD1 1,16,46,3072\nCV 54,108,109\n"
"Mvaa 76,136,168\nRa 200,10,51200\ns_add_u32 s23, s22, 57344\nMvaa 76,140,172\nCV 55,110,111\nCV 56,112,113\nMvaa 72,132,172\nRa 204,11,51200\ns_add_u32 m0, s47, 32768\n"
"s_add_u32 s46, s45, 0x8000\nMvaa 72,128,168\nCV 57,114,115\nCV 58,116,117\nMvaa 80,128,176\nRa 208,10,57344\nD1 1,16,46,0\nMvaa 80,132,180\nCV 59,118,119\nMvaa 84,140,180\n"
"Ra 212,11,57344\nCV 60,120,121\nMvaa 84,136,176\nCV 61,122,123\nMvaa 92,136,184\nRa 216,10,59392\nCV 62,124,125\nMvaa 92,140,188\nD1 1,16,46,1024\nMvaa 88,132,188\n"
"Ra 220,11,59392\nCV 63,126,127\nMvaa 88,128,184\nCV 32,64,65\nMvaa 128,144,160\nCV 33,66,67\nCV 34,68,69\nMvaa 128,148,164\nCV 35,70,71\nMvaa 132,156,164\nD1 1,16,46,2048\n"
"Mvaa 132,152,160\nCV 36,72,73\nCV 37,74,75\nMvaa 140,152,168\nCV 38,76,77\nMvaa 140,156,172\nCV 39,78,79\nCV 40,80,81\nMvaa 136,148,172\nCV 41,82,83\nMvaa 136,144,168\n"
"D1 1,16,46,3072\nCV 42,84,85\nMvaa 144,144,176\nCV 43,86,87\nMvaa 144,148,180\nCV 44,88,89\nMvaa 148,156,180\ns_add_u32 m0, s47, 49152\ns_add_u32 s46, s45, 0x400000\n"
"Mvaa 148,152,176\nCV 45,90,91\nMvaa 156,152,184\nD1 1,16,46,0\nCV 46,92,93\nMvaa 156,156,188\nCV 47,94,95\nCV 112,160,161\nCV 113,162,163\nMvaa 152,148,188\nCV 114,164,165\n"
"CV 115,166,167\nMvaa 152,144,184\nWL 0\nMaaa 0,192,160\nSv 32,1,16,75,0\nCV 116,168,169\nCV 117,170,171\nMaaa 0,196,164\nD1 1,16,46,1024\nCV 118,172,173\nMaaa 4,204,164\n"
"CV 119,174,175\nCV 120,176,177\nMaaa 4,200,160\nCV 121,178,179\nCV 122,180,181\nMaaa 12,200,168\nCV 123,182,183\nCV 124,184,185\nCV 125,186,187\nMaaa 12,204,172\nCV 126,188,189\n"
"CV 127,190,191\nMaaa 8,196,172\nD1 1,16,46,2048\nCV 96,128,129\nMaaa 8,192,168\nCV 97,130,131\nCV 98,132,133\nCV 99,134,135\nMaaa 16,192,176\nSv 36,1,16,75,1024\nCV 100,136,137\n"
"Maaa 16,196,180\nCV 101,138,139\nCV 102,140,141\nMaaa 20,204,180\nCV 103,142,143\nCV 104,144,145\nCV 105,146,147\nMaaa 20,200,176\nD1 1,16,46,3072\n.W555:\ns_cbranch_execz .W559\n"
"CV 106,148,149\nMaaa 28,200,184\nCV 107,150,151\nCV 108,152,153\nMaaa 28,204,188\nCV 109,154,155\nCV 110,156,157\nCV 111,158,159\nMaaa 24,196,188\ns_add_u32 m0, s47, -49152\n"
"s_add_u32 s46, s45, 0x408000\nMaaa 24,192,184\nv_add_u32 v2, 0x10000, v1\ns_lshr_b32 s2, s59, 1\nMaaa 64,208,160\nD1 1,16,46,0\nSv 40,1,16,75,2048\nv_add_u32 v24, s2, v2\n"
"Maaa 64,212,164\nv_xor_b32 v26, 0x1000, v24\nv_lshrrev_b32 v20, 4, v0\nMaaa 68,220,164\nv_lshlrev_b32 v20, 2, v20\nv_and_b32 v0, 15, v0\nMaaa 68,216,160\nv_sub_u32 v0, v0, v20\n"
"s_lshl_b32 s2, s11, 4\nMaaa 76,216,168\nv_add_u32 v3, s2, v0\nv_subrev_u32 v0, 32, v3\ns_lshl_b32 s2, s11, 12\nMaaa 76,220,172\nD1 1,16,46,1024\nv_add_u32 v22, s2, v1\n"
"Maaa 72,212,172\nv_add_u32 v23, 0x20000, v22\nv_add_u32 v22, 0x8000, v22\nMaaa 72,208,168\nv_mov_b32 v4, 0\nv_mov_b32 v5, 0\nv_mov_b32 v6, 0\nMaaa 80,208,176\nSv 44,1,16,75,3072\n"
"v_mov_b32 v7, 0\nMaaa 80,212,180\nv_mov_b32 v8, 0\nv_mov_b32 v9, 0\nMaaa 84,220,180\nD1 1,16,46,2048\nv_mov_b32 v10, 0\nv_mov_b32 v11, 0\nMaaa 84,216,176\nv_mov_b32 v12, 0\n"
"v_mov_b32 v13, 0\nMaaa 92,216,184\nv_mov_b32 v14, 0\nv_mov_b32 v15, 0\nMaaa 92,220,188\nv_mov_b32 v16, 0\nv_mov_b32 v17, 0\nv_mov_b32 v18, 0\nMaaa 88,212,188\nv_mov_b32 v19, 0\n"
"v_mov_b32 v86, 0\nMaaa 88,208,184\nWV 19\ns_barrier\nMaaa 32,192,224\nD1 1,16,46,3072\ns_add_u32 s75, s75, s55\nSv 48,1,16,75,0\nMaaa 32,196,228\nv_mov_b32 v87, 0\n"
"v_mov_b32 v88, 0\nMaaa 36,204,228\nv_mov_b32 v89, 0\nv_mov_b32 v95, -1\nMaaa 36,200,224\nMaaa 44,200,232\nRa 128,24,0\nMaaa 44,204,236\nMaaa 40,196,236\nRa 132,24,1024\n"
"Maaa 40,192,232\nMaaa 48,192,240\nSv 52,1,16,75,1024\nRa 136,24,2048\nMaaa 48,196,244\nMaaa 52,204,244\nRa 140,24,3072\nMaaa 52,200,240\nMaaa 60,200,248\nRa 200,23,2048\n"
"Ra 144,26,0\nMaaa 60,204,252\nRa 204,23,3072\nMaaa 56,196,252\nRa 196,23,1024\nRa 148,26,1024\nMaaa 56,192,248\nRa 192,23,0\nMaaa 96,208,224\nSv 56,1,16,75,2048\nRa 152,26,2048\n"
"Maaa 96,212,228\nMaaa 100,220,228\nRa 228,22,50176\nRa 156,26,3072\nMaaa 100,216,224\nRa 224,22,49152\nMaaa 108,216,232\nMaaa 108,220,236\nMaaa 104,212,236\nRa 236,22,52224\n"
"Maaa 104,208,232\nRa 232,22,51200\nMaaa 112,208,240\nSv 60,1,16,75,3072\nMaaa 112,212,244\nMaaa 116,220,244\nRa 244,22,1024\nMaaa 116,216,240\nRa 240,22,0\nMaaa 124,216,248\n"
"Ra 216,23,18432\nMaaa 124,220,252\nRa 220,23,19456\nMaaa 120,212,252\nRa 252,22,3072\nRa 212,23,17408\nMaaa 120,208,248\nRa 248,22,2048\nRa 208,23,16384\nWL 0\nCvaa 32,128,192,4\n"
"Ra 160,24,8192\nMvaa 32,132,200\nMvaa 32,136,208\nRa 164,24,9216\nMvaa 32,140,216\nCvaa 36,140,220,8\nRa 168,24,10240\nMvaa 36,136,212\ns_add_u32 m0, s89, 32768\nMvaa 36,132,204\n"
"Ra 172,24,11264\ns_barrier\nMvaa 36,128,196\nMvaa 36,144,228\nRa 176,26,8192\nSv 96,1,16,76,0\nMvaa 36,148,236\ns_add_u32 s46, s86, s88\nMvaa 36,152,244\nRa 180,26,9216\n"
"Mvaa 36,156,252\nAR 92,0\nMvaa 32,156,248\nRa 184,26,10240\nAR 93,1\nMvaa 32,152,240\nD1 1,16,46,0\nMvaa 32,148,232\nRa 188,26,11264\nWL 4\nMvaa 32,144,224\nCV 96,92,93\n"
"Cvaa 40,160,192,12\nAR 92,2\nAR 93,3\nMvaa 40,164,200\nCV 97,92,93\nMvaa 40,168,208\nv_and_b32 v27, 0x70, v1\nbuffer_load_dword v27, v27, s[16:19], s61 offen offset:136 sc0 sc1\n"
"Mvaa 40,172,216\nAR 92,4\nCvaa 44,172,220,16\nAR 93,5\nCV 98,92,93\nMvaa 44,168,212\nD1 1,16,46,1024\nMvaa 44,164,204\nWL 0\nMvaa 44,160,196\nAR 92,6\nMvaa 44,176,228\n"
"Sv 100,1,16,76,1024\nAR 93,7\nMvaa 44,180,236\ns_add_u32 s88, s88, 32768\nEX 80,32\nMvaa 44,184,244\nCV 99,92,93\nAR 92,8\nMvaa 44,188,252\nEX 81,33\nMvaa 40,188,248\nAR 93,9\n"
"CV 100,92,93\nMvaa 40,184,240\nD1 1,16,46,2048\nMvaa 40,180,232\nAR 92,10\nAR 93,11\nMvaa 40,176,224\nAD 84,80,81\nCV 64,80,81\nCV 101,92,93\nAR 92,12\nEX 82,34\nAR 93,13\n"
"CV 102,92,93\nWV 16\nEX 83,35\nAD 84,84,82\nAR 92,14\nD1 1,16,46,3072\ns_add_u32 m0, s89, 49152\ns_barrier\nAR 93,15\nAD 84,84,83\nCV 65,82,83\nRa 128,24,32768\n"
"Sv 104,1,16,76,2048\ns_add_u32 s46, s87, s88\nEX 80,36\nRa 132,24,33792\nCV 103,92,93\nEX 81,37\nRa 136,24,34816\nAR 92,16\nAR 93,17\nD1 1,16,46,0\nCV 104,92,93\nRa 140,24,35840\n"
"AR 92,18\nAD 85,80,81\nCV 68,80,81\nRa 144,26,32768\nAR 93,19\nCV 105,92,93\nEX 82,38\nRa 148,26,33792\nAR 92,20\nEX 83,39\nRa 152,26,34816\nAD 85,85,82\nAR 93,21\n"
"D1 1,16,46,1024\nCV 106,92,93\nRa 156,26,35840\nAR 92,22\nAD 85,85,83\nCV 69,82,83\nRa 160,24,40960\nSv 108,1,16,76,3072\nAR 93,23\ns_xor_b32 s89, s89, 0x10000\nEX 80,40\n"
"Ra 164,24,41984\nCV 107,92,93\nAR 92,24\nWL 6\nEX 81,41\nRa 168,24,43008\nAD 84,84,80\nAR 93,25\nD1 1,16,46,2048\nCV 108,92,93\nRa 172,24,44032\nAR 92,26\nAR 93,27\nAD 84,84,81\n"
"CV 66,80,81\nCvaa 48,128,192,4\nRa 176,26,40960\nCV 109,92,93\nAR 92,28\nMvaa 48,132,200\nEX 82,42\nMvaa 48,136,208\nRa 180,26,41984\nAR 93,29\nMvaa 48,140,216\nWL 6\nEX 83,43\n"
"WV 9\nv_cmp_ne_u32 vcc, s15, v27\ns_nop 0\ns_cbranch_vccnz .S171\n.S172:\nCvaa 52,140,220,8\nRa 184,26,43008\nAD 84,84,82\nMvaa 52,136,212\nD1 1,16,46,3072\ns_add_u32 m0, s89, 0\n"
"CV 110,92,93\nMvaa 52,132,204\nRa 188,26,44032\nAR 92,30\nMvaa 52,128,196\nAD 84,84,83\nCV 67,82,83\nAR 93,31\nMvaa 52,144,228\nRa 128,2,49152\ns_add_u32 s76, s76, s55\n"
"Sv 112,1,16,76,0\nMvaa 52,148,236\ns_add_u32 s46, s86, s88\nEX 80,44\nMvaa 52,152,244\nRa 132,2,50176\nv_xor_b32 v24, 0x10000, v24\nCV 111,92,93\nMvaa 52,156,252\nWL 6\nEX 81,45\n"
"Mvaa 48,156,248\nRa 136,2,51200\nAD 85,85,80\nAR 92,32\nMvaa 48,152,240\nD1 1,16,46,0\nAR 93,33\nCV 112,92,93\nMvaa 48,148,232\nRa 140,2,52224\nAR 92,34\nMvaa 48,144,224\n"
"AD 85,85,81\nCV 70,80,81\nAR 93,35\nCvaa 56,160,192,12\nRa 144,2,53248\nCV 113,92,93\nAR 92,36\nMvaa 56,164,200\nEX 82,46\nMvaa 56,168,208\nRa 148,2,54272\n"
"v_xor_b32 v26, 0x10000, v26\nAR 93,37\nMvaa 56,172,216\nWL 6\nEX 83,47\nCvaa 60,172,220,16\nRa 152,2,55296\nAD 85,85,82\nCV 114,92,93\nMvaa 60,168,212\nD1 1,16,46,1024\nAR 92,38\n"
"AR 93,39\nMvaa 60,164,204\nRa 156,2,56320\nCV 115,92,93\nMvaa 60,160,196\nAD 85,85,83\nCV 71,82,83\nMvaa 60,176,228\nRa 160,2,57344\nv_max_f32 v90, v84, v85\nSv 116,1,16,76,1024\n"
"Mvaa 60,180,236\ns_add_u32 s88, s88, 32768\nEX 80,48\nMvaa 60,184,244\nRa 164,2,58368\nv_cmp_lt_f32 vcc, s63, v90\nMvaa 60,188,252\nWL 8\nEX 81,49\nMvaa 56,188,248\n"
"Ra 168,2,59392\ns_cbranch_vccnz .T153\n.T154:\nv_mov_b32 v20, v84\nv_mov_b32 v21, v85\nv_mov_b32 v22, v20\nv_mov_b32 v23, v21\nMvaa 56,184,240\nD1 1,16,46,2048\n"
"v_permlane32_swap_b32 v20, v22\nv_permlane32_swap_b32 v21, v23\nAD 20,20,22\nAD 21,21,23\nv_mov_b32 v22, v20\nv_mov_b32 v23, v21\nMvaa 56,180,232\nRa 172,2,60416\n"
"v_permlane16_swap_b32 v20, v22\nv_permlane16_swap_b32 v21, v23\nAD 20,20,22\nAD 21,21,23\nv_min_f32 v90, v20, v21\nv_cmp_gt_f32 vcc, s64, v90\ns_nop 0\ns_cbranch_vccnz .T155\n"
".T156:\nAD 86,86,84\nMvaa 56,176,224\nAD 84,80,81\nCV 72,80,81\nAR 92,40\nZvav 128,128,64\nRa 176,2,61440\nAR 93,41\nCV 116,92,93\nZvav 132,128,68\nEX 82,50\nAR 92,42\n"
"Zvav 140,132,68\nRa 180,2,62464\nAR 93,43\nCV 117,92,93\nZvav 136,132,64\nWL 8\nEX 83,51\nZvav 144,136,64\nRa 184,2,63488\nAD 84,84,82\nAR 92,44\nZvav 148,136,68\nD1 1,16,46,3072\n"
"s_add_u32 m0, s89, 16384\nAR 93,45\nZvav 156,140,68\nRa 188,2,64512\ns_barrier\nCV 118,92,93\nZvav 152,140,64\nAD 84,84,83\nCV 73,82,83\nAR 92,46\nZvav 160,144,64\nRa 128,24,0\n"
"Sv 120,1,16,76,2048\nAR 93,47\nZvav 164,144,68\ns_add_u32 s46, s87, s88\nEX 80,52\nbuffer_store_dword v25, off, s[16:19], s54 offset:12 sc0 sc1\nZvav 172,148,68\nRa 132,24,1024\n"
"v_xor_b32 v2, 0x10000, v2\nCV 119,92,93\nZvav 168,148,64\nWL 8\nEX 81,53\nZvav 176,152,64\nRa 136,24,2048\nAD 87,87,85\nZvav 180,152,68\nD1 1,16,46,0\nAR 92,48\nAR 93,49\n"
"Zvav 188,156,68\nRa 140,24,3072\nCV 120,92,93\nZvav 184,156,64\nAD 85,80,81\nCV 76,80,81\nZvav 192,160,64\nRa 144,26,0\nAR 92,50\nAR 93,51\nZvav 196,160,68\nEX 82,54\n"
"Zvav 204,164,68\nRa 148,26,1024\nCV 121,92,93\nZvav 200,164,64\nWL 8\nEX 83,55\nZvav 208,168,64\nRa 152,26,2048\nAD 85,85,82\nAR 92,52\nZvav 212,168,68\nD1 1,16,46,1024\nAR 93,53\n"
"Zvav 220,172,68\nRa 156,26,3072\nCV 122,92,93\nZvav 216,172,64\nAD 85,85,83\nCV 77,82,83\nAR 92,54\nZvav 224,176,64\nRa 160,24,8192\nSv 124,1,16,76,3072\nZvav 228,176,68\n"
"EX 80,56\nZvav 236,180,68\nRa 164,24,9216\nAR 93,55\nCV 123,92,93\nZvav 232,180,64\nWL 6\nEX 81,57\nZvav 240,184,64\nRa 168,24,10240\nAD 84,84,80\nAR 92,56\nZvav 244,184,68\n"
"D1 1,16,46,2048\nAR 93,57\nZvav 252,188,68\nRa 172,24,11264\nCV 124,92,93\nAR 92,58\nZvav 248,188,64\nAD 84,84,81\nCV 74,80,81\nCvaa 32,128,192,4\nRa 176,26,8192\nAR 93,59\n"
"CV 125,92,93\nMvaa 32,132,200\nEX 82,58\nMvaa 32,136,208\nRa 180,26,9216\nAR 92,60\nMvaa 32,140,216\nWL 6\nEX 83,59\nCvaa 36,140,220,8\nRa 184,26,10240\nAD 84,84,82\n"
"Mvaa 36,136,212\nD1 1,16,46,3072\ns_add_u32 m0, s89, 32768\nAR 93,61\nMvaa 36,132,204\nRa 188,26,11264\nCV 126,92,93\nMvaa 36,128,196\nAD 84,84,83\nCV 75,82,83\nAR 92,62\n"
"Mvaa 36,144,228\nRa 128,2,16384\nSv 96,1,16,77,0\nMvaa 36,148,236\ns_add_u32 s46, s86, s88\nEX 80,60\nMvaa 36,152,244\nRa 132,2,17408\nAR 93,63\nCV 127,92,93\nMvaa 36,156,252\n"
"WL 6\nEX 81,61\nMvaa 32,156,248\nRa 136,2,18432\nAD 85,85,80\nAR 92,64\nMvaa 32,152,240\nD1 1,16,46,0\nAR 93,65\nMvaa 32,148,232\nRa 140,2,19456\nCV 96,92,93\nAR 92,66\n"
"Mvaa 32,144,224\nAD 85,85,81\nCV 78,80,81\nCvaa 40,160,192,12\nRa 144,2,20480\nAR 93,67\nCV 97,92,93\nMvaa 40,164,200\nEX 82,62\nMvaa 40,168,208\nRa 148,2,21504\nAR 92,68\n"
"AR 93,69\nMvaa 40,172,216\nWL 6\nEX 83,63\nCvaa 44,172,220,16\nRa 152,2,22528\nAD 85,85,82\nCV 98,92,93\nMvaa 44,168,212\n.W556:\ns_cbranch_execz .W560\nD1 1,16,46,1024\nAR 92,70\n"
"Mvaa 44,164,204\nRa 156,2,23552\nAR 93,71\nCV 99,92,93\nMvaa 44,160,196\nAD 85,85,83\nCV 79,82,83\nMvaa 44,176,228\nRa 160,2,24576\nv_max_f32 v90, v84, v85\nSv 100,1,16,77,1024\n"
"Mvaa 44,180,236\ns_add_u32 s88, s88, 32768\nEX 80,32\nMvaa 44,184,244\nRa 164,2,25600\nv_cmp_lt_f32 vcc, s63, v90\nMvaa 44,188,252\nWL 8\nEX 81,33\nMvaa 40,188,248\n"
"Ra 168,2,26624\ns_cbranch_vccnz .T158\n.T159:\nAR 92,72\nMvaa 40,184,240\nD1 1,16,46,2048\nAR 93,73\nMvaa 40,180,232\nRa 172,2,27648\nAD 86,86,84\nCV 100,92,93\nMvaa 40,176,224\n"
"AD 84,80,81\nCV 64,80,81\nMvav 128,128,72\nRa 176,2,28672\nAR 92,74\nAR 93,75\nMvav 132,128,76\nEX 82,34\nMvav 140,132,76\nRa 180,2,29696\nCV 101,92,93\nAR 92,76\nMvav 136,132,72\n"
"s_waitcnt vmcnt(18) lgkmcnt(8)\nEX 83,35\nMvav 144,136,72\nRa 184,2,30720\nAD 84,84,82\nAR 93,77\nMvav 148,136,76\nD1 1,16,46,3072\ns_add_u32 m0, s89, 49152\nMvav 156,140,76\n"
"Ra 188,2,31744\ns_barrier\nCV 102,92,93\nMvav 152,140,72\nAD 84,84,83\nCV 65,82,83\nMvav 160,144,72\nRa 128,24,32768\nSv 104,1,16,77,2048\nAR 92,78\nMvav 164,144,76\n"
"s_add_u32 s46, s87, s88\nEX 80,36\nMvav 172,148,76\nRa 132,24,33792\nAR 93,79\nCV 103,92,93\nMvav 168,148,72\nWL 8\nEX 81,37\nMvav 176,152,72\nRa 136,24,34816\nAD 87,87,85\n"
"Mvav 180,152,76\nD1 1,16,46,0\nAR 92,80\nAR 93,81\nMvav 188,156,76\nRa 140,24,35840\nCV 104,92,93\nMvav 184,156,72\nAD 85,80,81\nCV 68,80,81\nMvav 192,160,72\nRa 144,26,32768\n"
"AR 92,82\nAR 93,83\nMvav 196,160,76\nEX 82,38\nMvav 204,164,76\nRa 148,26,33792\nCV 105,92,93\nMvav 200,164,72\nWL 8\nEX 83,39\nMvav 208,168,72\nRa 152,26,34816\nAD 85,85,82\n"
"AR 92,84\nMvav 212,168,76\nD1 1,16,46,1024\nAR 93,85\nMvav 220,172,76\nRa 156,26,35840\nCV 106,92,93\nMvav 216,172,72\nAD 85,85,83\nCV 69,82,83\nAR 92,86\nMvav 224,176,72\n"
"Ra 160,24,40960\nSv 108,1,16,77,3072\nMvav 228,176,76\ns_xor_b32 s89, s89, 0x10000\nEX 80,40\nMvav 236,180,76\nRa 164,24,41984\nAR 93,87\nCV 107,92,93\nMvav 232,180,72\nWL 6\n"
"EX 81,41\nMvav 240,184,72\nRa 168,24,43008\nAD 84,84,80\nAR 92,88\nMvav 244,184,76\nD1 1,16,46,2048\nAR 93,89\nCV 108,92,93\nMvav 252,188,76\nRa 172,24,44032\nAR 92,90\n"
"Mvav 248,188,72\nAD 84,84,81\nCV 66,80,81\nAR 93,91\nCvaa 48,128,192,4\nRa 176,26,40960\nCV 109,92,93\nAR 92,92\nMvaa 48,132,200\nEX 82,42\nMvaa 48,136,208\nRa 180,26,41984\n"
"AR 93,93\nCV 110,92,93\nMvaa 48,140,216\nWL 6\nEX 83,43\nCvaa 52,140,220,8\nRa 184,26,43008\nAD 84,84,82\nv_and_b32 v27, 0x70, v1\n"
"buffer_load_dword v27, v27, s[16:19], s61 offen offset:140 sc0 sc1\nMvaa 52,136,212\nD1 1,16,46,3072\ns_add_u32 m0, s89, 0\nAR 92,94\nMvaa 52,132,204\nRa 188,26,44032\nAR 93,95\n"
"Mvaa 52,128,196\nAD 84,84,83\nCV 67,82,83\nCV 111,92,93\nMvaa 52,144,228\nRa 128,2,49152\ns_add_u32 s77, s77, s55\nSv 112,1,16,77,0\nMvaa 52,148,236\ns_add_u32 s46, s86, s88\n"
"EX 80,44\nMvaa 52,152,244\nRa 132,2,50176\nv_xor_b32 v24, 0x10000, v24\nAR 92,96\nMvaa 52,156,252\nWL 6\nEX 81,45\nMvaa 48,156,248\nRa 136,2,51200\nAD 85,85,80\nAR 93,97\n"
"Mvaa 48,152,240\nD1 1,16,46,0\nCV 112,92,93\nMvaa 48,148,232\nRa 140,2,52224\nAR 92,98\nAR 93,99\nMvaa 48,144,224\nAD 85,85,81\nCV 70,80,81\nCvaa 56,160,192,12\nRa 144,2,53248\n"
"CV 113,92,93\nAR 92,100\nMvaa 56,164,200\nEX 82,46\nMvaa 56,168,208\nRa 148,2,54272\nv_xor_b32 v26, 0x10000, v26\nAR 93,101\nMvaa 56,172,216\nWL 6\nEX 83,47\nCvaa 60,172,220,16\n"
"Ra 152,2,55296\nAD 85,85,82\nCV 114,92,93\nMvaa 60,168,212\nD1 1,16,46,1024\nAR 92,102\nMvaa 60,164,204\nRa 156,2,56320\nAR 93,103\nCV 115,92,93\nMvaa 60,160,196\nAD 85,85,83\n"
"CV 71,82,83\nMvaa 60,176,228\nRa 160,2,57344\nv_max_f32 v90, v84, v85\nSv 116,1,16,77,1024\nMvaa 60,180,236\ns_add_u32 s88, s88, 32768\nEX 80,48\nMvaa 60,184,244\nRa 164,2,58368\n"
"v_cmp_lt_f32 vcc, s63, v90\nMvaa 60,188,252\nWL 8\nEX 81,49\nMvaa 56,188,248\nRa 168,2,59392\ns_cbranch_vccnz .T160\n.T161:\nAR 92,104\nMvaa 56,184,240\nD1 1,16,46,2048\n"
"AR 93,105\nCV 116,92,93\nMvaa 56,180,232\nRa 172,2,60416\nAD 86,86,84\nMvaa 56,176,224\nAD 84,80,81\nCV 72,80,81\nAR 92,106\nMvav 128,128,64\nRa 176,2,61440\nAR 93,107\n"
"CV 117,92,93\nMvav 132,128,68\nEX 82,50\nMvav 140,132,68\nRa 180,2,62464\nAR 92,108\nAR 93,109\nMvav 136,132,64\ns_waitcnt vmcnt(18) lgkmcnt(8)\nEX 83,51\nMvav 144,136,64\n"
"Ra 184,2,63488\nAD 84,84,82\nCV 118,92,93\nMvav 148,136,68\nD1 1,16,46,3072\ns_add_u32 m0, s89, 16384\nAR 92,110\nMvav 156,140,68\nRa 188,2,64512\ns_barrier\nMvav 152,140,64\n"
"AD 84,84,83\nCV 73,82,83\nAR 93,111\nMvav 160,144,64\nRa 128,24,0\nSv 120,1,16,77,2048\nCV 119,92,93\nMvav 164,144,68\ns_add_u32 s46, s87, s88\nEX 80,52\nMvav 172,148,68\n"
"Ra 132,24,1024\nv_xor_b32 v2, 0x10000, v2\nAR 92,112\nMvav 168,148,64\nWL 8\nEX 81,53\nMvav 176,152,64\nRa 136,24,2048\nAD 87,87,85\n"
"buffer_store_dword v25, off, s[16:19], s54 offset:8 sc0 sc1\nMvav 180,152,68\nD1 1,16,46,0\nAR 93,113\nCV 120,92,93\nMvav 188,156,68\nRa 140,24,3072\nAR 92,114\nMvav 184,156,64\n"
"AD 85,80,81\nCV 76,80,81\nMvav 192,160,64\nRa 144,26,0\nAR 93,115\nCV 121,92,93\nMvav 196,160,68\nEX 82,54\nMvav 204,164,68\nRa 148,26,1024\nAR 92,116\nMvav 200,164,64\nWL 8\n"
"EX 83,55\nMvav 208,168,64\nRa 152,26,2048\nAD 85,85,82\nAR 93,117\nMvav 212,168,68\nD1 1,16,46,1024\nCV 122,92,93\nMvav 220,172,68\nRa 156,26,3072\nAR 92,118\nMvav 216,172,64\n"
"AD 85,85,83\nCV 77,82,83\nAR 93,119\nMvav 224,176,64\nRa 160,24,8192\nSv 124,1,16,77,3072\nMvav 228,176,68\nEX 80,56\nMvav 236,180,68\nRa 164,24,9216\nCV 123,92,93\nAR 92,120\n"
"Mvav 232,180,64\nWL 6\nEX 81,57\nMvav 240,184,64\nRa 168,24,10240\nAD 84,84,80\nMvav 244,184,68\nD1 1,16,46,2048\nAR 93,121\nCV 124,92,93\nMvav 252,188,68\nRa 172,24,11264\n"
"AR 92,122\nMvav 248,188,64\nAD 84,84,81\nCV 74,80,81\nCvaa 32,128,192,4\nRa 176,26,8192\nAR 93,123\nCV 125,92,93\nMvaa 32,132,200\ns_add_u32 s49, s49, 131072\nEX 82,58\n"
"Mvaa 32,136,208\nRa 180,26,9216\nAR 92,124\nMvaa 32,140,216\nWL 6\nEX 83,59\nCvaa 36,140,220,8\nRa 184,26,10240\nAD 84,84,82\nAR 93,125\nMvaa 36,136,212\nD1 1,16,46,3072\n"
"s_add_u32 m0, s89, 32768\nMvaa 36,132,204\nRa 188,26,11264\nCV 126,92,93\nMvaa 36,128,196\nAD 84,84,83\nCV 75,82,83\nAR 92,126\nMvaa 36,144,228\nRa 128,2,16384\nSv 96,1,16,78,0\n"
"Mvaa 36,148,236\ns_add_u32 s46, s86, s88\nEX 80,60\nMvaa 36,152,244\nRa 132,2,17408\nAR 93,127\nCV 127,92,93\nMvaa 36,156,252\nWL 6\nEX 81,61\nMvaa 32,156,248\nRa 136,2,18432\n"
"AD 85,85,80\nMvaa 32,152,240\nD1 1,16,46,0\nMvaa 32,148,232\nRa 140,2,19456\nMvaa 32,144,224\nAD 85,85,81\nCV 78,80,81\nCvaa 40,160,192,12\nRa 144,2,20480\nMvaa 40,164,200\n"
"Sv 92,1,16,49,0\nEX 82,62\nMvaa 40,168,208\nRa 148,2,21504\nMvaa 40,172,216\nWL 6\nEX 83,63\nCvaa 44,172,220,16\nRa 152,2,22528\nAD 85,85,82\nMvaa 44,168,212\nD1 1,16,46,1024\n"
"Mvaa 44,164,204\nRa 156,2,23552\nMvaa 44,160,196\nAD 85,85,83\nCV 79,82,83\nMvaa 44,176,228\nRa 160,2,24576\nv_max_f32 v90, v84, v85\nSv 100,1,16,78,1024\nMvaa 44,180,236\n"
"s_add_u32 s88, s88, 32768\nEX 80,32\nMvaa 44,184,244\nRa 164,2,25600\nv_cmp_lt_f32 vcc, s63, v90\nMvaa 44,188,252\nWL 8\nEX 81,33\nMvaa 40,188,248\nRa 168,2,26624\n"
"s_cbranch_vccnz .T163\n.T164:\nMvaa 40,184,240\nD1 1,16,46,2048\nMvaa 40,180,232\nRa 172,2,27648\nAD 86,86,84\nMvaa 40,176,224\nAD 84,80,81\nCV 64,80,81\nMvav 128,128,72\n"
"Ra 176,2,28672\nMvav 132,128,76\nEX 82,34\nMvav 140,132,76\nRa 180,2,29696\nMvav 136,132,72\ns_waitcnt vmcnt(19) lgkmcnt(8)\nEX 83,35\nMvav 144,136,72\nRa 184,2,30720\n"
"AD 84,84,82\nMvav 148,136,76\nD1 1,16,46,3072\ns_add_u32 m0, s89, 49152\nMvav 156,140,76\nRa 188,2,31744\ns_barrier\nMvav 152,140,72\nAD 84,84,83\nCV 65,82,83\nMvav 160,144,72\n"
"Ra 128,24,32768\nSv 104,1,16,78,2048\nMvav 164,144,76\ns_add_u32 s46, s87, s88\nEX 80,36\nMvav 172,148,76\nRa 132,24,33792\nMvav 168,148,72\nWL 8\nEX 81,37\nMvav 176,152,72\n"
"Ra 136,24,34816\nAD 87,87,85\nMvav 180,152,76\nD1 1,16,46,0\nMvav 188,156,76\nRa 140,24,35840\nMvav 184,156,72\nAD 85,80,81\nCV 68,80,81\nMvav 192,160,72\nRa 144,26,32768\n"
"Mvav 196,160,76\nEX 82,38\nMvav 204,164,76\nRa 148,26,33792\nMvav 200,164,72\nWL 8\nEX 83,39\nMvav 208,168,72\nRa 152,26,34816\nAD 85,85,82\nMvav 212,168,76\nD1 1,16,46,1024\n"
"Mvav 220,172,76\nRa 156,26,35840\nMvav 216,172,72\nAD 85,85,83\nCV 69,82,83\nMvav 224,176,72\nRa 160,24,40960\nSv 108,1,16,78,3072\nMvav 228,176,76\ns_xor_b32 s89, s89, 0x10000\n"
"EX 80,40\nMvav 236,180,76\nRa 164,24,41984\nMvav 232,180,72\nWL 6\nEX 81,41\nMvav 240,184,72\nRa 168,24,43008\nAD 84,84,80\nMvav 244,184,76\nD1 1,16,46,2048\nMvav 252,188,76\n"
"Ra 172,24,44032\nMvav 248,188,72\nAD 84,84,81\nCV 66,80,81\nCvaa 48,128,192,4\nRa 176,26,40960\nMvaa 48,132,200\nEX 82,42\nMvaa 48,136,208\nRa 180,26,41984\nMvaa 48,140,216\nWL 6\n"
"EX 83,43\nv_cmp_ne_u32 vcc, s15, v27\ns_nop 0\ns_cbranch_vccnz .S174\n.S175:\nv_and_b32 v27, 0x70, v1\nbuffer_load_dword v27, v27, s[16:19], s61 offen offset:12 sc0 sc1\n"
"Cvaa 52,140,220,8\nRa 184,26,43008\nAD 84,84,82\nMvaa 52,136,212\nD1 1,16,46,3072\ns_add_u32 m0, s89, 0\nMvaa 52,132,204\nRa 188,26,44032\nMvaa 52,128,196\nAD 84,84,83\n"
"CV 67,82,83\nMvaa 52,144,228\nRa 128,2,49152\ns_add_u32 s78, s78, s55\nSv 112,1,16,78,0\nMvaa 52,148,236\ns_add_u32 s46, s86, s88\nEX 80,44\nMvaa 52,152,244\nRa 132,2,50176\n"
"v_xor_b32 v24, 0x10000, v24\nMvaa 52,156,252\nWL 6\nEX 81,45\nMvaa 48,156,248\nRa 136,2,51200\nAD 85,85,80\nMvaa 48,152,240\n.W557:\ns_cbranch_execz .W561\nD1 1,16,46,0\n"
"Mvaa 48,148,232\nRa 140,2,52224\nMvaa 48,144,224\nAD 85,85,81\nCV 70,80,81\nCvaa 56,160,192,12\nRa 144,2,53248\nMvaa 56,164,200\nSv 92,1,16,49,1024\nEX 82,46\nMvaa 56,168,208\n"
"Ra 148,2,54272\nv_xor_b32 v26, 0x10000, v26\nMvaa 56,172,216\nWL 6\nEX 83,47\nCvaa 60,172,220,16\nRa 152,2,55296\nAD 85,85,82\nMvaa 60,168,212\nD1 1,16,46,1024\nMvaa 60,164,204\n"
"Ra 156,2,56320\nMvaa 60,160,196\nAD 85,85,83\nCV 71,82,83\nMvaa 60,176,228\nRa 160,2,57344\nv_max_f32 v90, v84, v85\nSv 116,1,16,78,1024\nMvaa 60,180,236\n"
"s_add_u32 s88, s88, 32768\nEX 80,48\nMvaa 60,184,244\nRa 164,2,58368\nv_cmp_lt_f32 vcc, s63, v90\nMvaa 60,188,252\nWL 8\nEX 81,49\nMvaa 56,188,248\nRa 168,2,59392\n"
"s_cbranch_vccnz .T165\n.T166:\nMvaa 56,184,240\nD1 1,16,46,2048\nMvaa 56,180,232\nRa 172,2,60416\nAD 86,86,84\nMvaa 56,176,224\nAD 84,80,81\nCV 72,80,81\nMvav 128,128,64\n"
"Ra 176,2,61440\nMvav 132,128,68\nEX 82,50\nMvav 140,132,68\nRa 180,2,62464\nMvav 136,132,64\ns_waitcnt vmcnt(20) lgkmcnt(8)\nEX 83,51\nMvav 144,136,64\nRa 184,2,63488\n"
"AD 84,84,82\nMvav 148,136,68\nD1 1,16,46,3072\ns_add_u32 m0, s89, 16384\nMvav 156,140,68\nRa 188,2,64512\ns_barrier\nMvav 152,140,64\nAD 84,84,83\nCV 73,82,83\nMvav 160,144,64\n"
"Ra 128,24,0\nSv 120,1,16,78,2048\nMvav 164,144,68\ns_add_u32 s46, s87, s88\nEX 80,52\nMvav 172,148,68\nRa 132,24,1024\nv_xor_b32 v2, 0x10000, v2\nMvav 168,148,64\nWL 8\nEX 81,53\n"
"Mvav 176,152,64\nRa 136,24,2048\nAD 87,87,85\nMvav 180,152,68\nD1 1,16,46,0\nMvav 188,156,68\nRa 140,24,3072\nMvav 184,156,64\nAD 85,80,81\nCV 76,80,81\nMvav 192,160,64\n"
"Ra 144,26,0\nMvav 196,160,68\nEX 82,54\nMvav 204,164,68\nRa 148,26,1024\nMvav 200,164,64\nWL 8\nEX 83,55\nMvav 208,168,64\nRa 152,26,2048\nAD 85,85,82\nMvav 212,168,68\n"
"D1 1,16,46,1024\nMvav 220,172,68\nRa 156,26,3072\nMvav 216,172,64\nAD 85,85,83\nCV 77,82,83\nMvav 224,176,64\nRa 160,24,8192\nSv 124,1,16,78,3072\nMvav 228,176,68\nEX 80,56\n"
"Mvav 236,180,68\nRa 164,24,9216\nMvav 232,180,64\nWL 6\nEX 81,57\nMvav 240,184,64\nRa 168,24,10240\nAD 84,84,80\nMvav 244,184,68\nD1 1,16,46,2048\nMvav 252,188,68\n"
"Ra 172,24,11264\nMvav 248,188,64\nAD 84,84,81\nCV 74,80,81\ns_mov_b32 s99, 11\ns_mov_b32 s90, 0\n.p2align 6\n.A190:\nCvaa 32,128,192,4\nRa 176,26,8192\nMvaa 32,132,200\n"
"s_add_u32 s49, s49, 131072\nEX 82,58\nMvaa 32,136,208\nRa 180,26,9216\nMvaa 32,140,216\nWL 6\nEX 83,59\nCvaa 36,140,220,8\nRa 184,26,10240\nAD 84,84,82\nMvaa 36,136,212\n"
"D1 1,16,46,3072\ns_add_u32 m0, s89, 32768\nMvaa 36,132,204\nRa 188,26,11264\nMvaa 36,128,196\nAD 84,84,83\nCV 75,82,83\nMvaa 36,144,228\nRa 128,2,16384\nMvaa 36,148,236\n"
"s_add_u32 s46, s86, s88\nEX 80,60\nMvaa 36,152,244\nRa 132,2,17408\nMvaa 36,156,252\nWL 6\nEX 81,61\nMvaa 32,156,248\nRa 136,2,18432\nAD 85,85,80\nMvaa 32,152,240\nD1 1,16,46,0\n"
"Mvaa 32,148,232\nRa 140,2,19456\nMvaa 32,144,224\nAD 85,85,81\nCV 78,80,81\nCvaa 40,160,192,12\nRa 144,2,20480\nMvaa 40,164,200\nSv 92,1,16,49,0\nEX 82,62\nMvaa 40,168,208\n"
"Ra 148,2,21504\nMvaa 40,172,216\nWL 6\nEX 83,63\nCvaa 44,172,220,16\nRa 152,2,22528\nAD 85,85,82\nMvaa 44,168,212\nD1 1,16,46,1024\nMvaa 44,164,204\nRa 156,2,23552\n"
"Mvaa 44,160,196\nAD 85,85,83\nCV 79,82,83\nMvaa 44,176,228\nRa 160,2,24576\nv_max_f32 v90, v84, v85\nMvaa 44,180,236\ns_add_u32 s88, s88, 32768\nEX 80,32\nMvaa 44,184,244\n"
"Ra 164,2,25600\nv_cmp_lt_f32 vcc, s63, v90\nMvaa 44,188,252\nWL 8\nEX 81,33\nMvaa 40,188,248\nRa 168,2,26624\ns_cbranch_vccnz .T194\n.T195:\nMvaa 40,184,240\nD1 1,16,46,2048\n"
"Mvaa 40,180,232\nRa 172,2,27648\nAD 86,86,84\nMvaa 40,176,224\nAD 84,80,81\nCV 64,80,81\nMvav 128,128,72\nRa 176,2,28672\nMvav 132,128,76\nEX 82,34\nMvav 140,132,76\n"
"Ra 180,2,29696\nMvav 136,132,72\ns_waitcnt vmcnt(13) lgkmcnt(8)\nEX 83,35\nMvav 144,136,72\nRa 184,2,30720\nAD 84,84,82\nMvav 148,136,76\nD1 1,16,46,3072\n"
"s_add_u32 m0, s89, 49152\nMvav 156,140,76\nRa 188,2,31744\ns_barrier\nMvav 152,140,72\nAD 84,84,83\nCV 65,82,83\nMvav 160,144,72\nRa 128,24,32768\nMvav 164,144,76\n"
"s_add_u32 s46, s87, s88\nEX 80,36\nMvav 172,148,76\nRa 132,24,33792\nMvav 168,148,72\nWL 8\nEX 81,37\nMvav 176,152,72\nRa 136,24,34816\nAD 87,87,85\nMvav 180,152,76\nD1 1,16,46,0\n"
"Mvav 188,156,76\nRa 140,24,35840\nMvav 184,156,72\nAD 85,80,81\nCV 68,80,81\nMvav 192,160,72\nRa 144,26,32768\nMvav 196,160,76\nEX 82,38\nMvav 204,164,76\nRa 148,26,33792\n"
"Mvav 200,164,72\nWL 8\nEX 83,39\nMvav 208,168,72\nRa 152,26,34816\nAD 85,85,82\nMvav 212,168,76\nD1 1,16,46,1024\nMvav 220,172,76\nRa 156,26,35840\nMvav 216,172,72\nAD 85,85,83\n"
"CV 69,82,83\nMvav 224,176,72\nRa 160,24,40960\nMvav 228,176,76\ns_xor_b32 s89, s89, 0x10000\nEX 80,40\nMvav 236,180,76\nRa 164,24,41984\nMvav 232,180,72\nWL 6\nEX 81,41\n"
"Mvav 240,184,72\nRa 168,24,43008\nAD 84,84,80\nMvav 244,184,76\nD1 1,16,46,2048\nMvav 252,188,76\nRa 172,24,44032\nMvav 248,188,72\nAD 84,84,81\nCV 66,80,81\nCvaa 48,128,192,4\n"
"Ra 176,26,40960\nMvaa 48,132,200\nEX 82,42\nMvaa 48,136,208\nRa 180,26,41984\nMvaa 48,140,216\nWL 6\nEX 83,43\nCvaa 52,140,220,8\nRa 184,26,43008\nAD 84,84,82\nMvaa 52,136,212\n"
"D1 1,16,46,3072\ns_add_u32 m0, s89, 0\nMvaa 52,132,204\nRa 188,26,44032\nMvaa 52,128,196\nAD 84,84,83\nCV 67,82,83\nMvaa 52,144,228\nRa 128,2,49152\nMvaa 52,148,236\n"
"s_add_u32 s46, s86, s88\nEX 80,44\nMvaa 52,152,244\nRa 132,2,50176\nv_xor_b32 v24, 0x10000, v24\nMvaa 52,156,252\nWL 6\nEX 81,45\nMvaa 48,156,248\nRa 136,2,51200\nAD 85,85,80\n"
"Mvaa 48,152,240\nD1 1,16,46,0\nMvaa 48,148,232\nRa 140,2,52224\nMvaa 48,144,224\nAD 85,85,81\nCV 70,80,81\nCvaa 56,160,192,12\nRa 144,2,53248\nMvaa 56,164,200\nSv 92,1,16,49,1024\n"
"EX 82,46\nMvaa 56,168,208\nRa 148,2,54272\nv_xor_b32 v26, 0x10000, v26\nMvaa 56,172,216\nWL 6\nEX 83,47\nCvaa 60,172,220,16\nRa 152,2,55296\nAD 85,85,82\nMvaa 60,168,212\n"
"D1 1,16,46,1024\nMvaa 60,164,204\nRa 156,2,56320\nMvaa 60,160,196\nAD 85,85,83\nCV 71,82,83\nMvaa 60,176,228\nRa 160,2,57344\nv_max_f32 v90, v84, v85\nMvaa 60,180,236\n"
"s_add_u32 s88, s88, 32768\nEX 80,48\nMvaa 60,184,244\nRa 164,2,58368\nv_cmp_lt_f32 vcc, s63, v90\nMvaa 60,188,252\nWL 8\nEX 81,49\nMvaa 56,188,248\nRa 168,2,59392\n"
"s_cbranch_vccnz .T196\n.T197:\nMvaa 56,184,240\nD1 1,16,46,2048\nMvaa 56,180,232\nRa 172,2,60416\nAD 86,86,84\nMvaa 56,176,224\nAD 84,80,81\nCV 72,80,81\nMvav 128,128,64\n"
"Ra 176,2,61440\nMvav 132,128,68\nEX 82,50\nMvav 140,132,68\nRa 180,2,62464\nMvav 136,132,64\ns_waitcnt vmcnt(13) lgkmcnt(8)\nEX 83,51\nMvav 144,136,64\nRa 184,2,63488\n"
"AD 84,84,82\nMvav 148,136,68\nD1 1,16,46,3072\ns_add_u32 m0, s89, 16384\nMvav 156,140,68\nRa 188,2,64512\ns_barrier\nMvav 152,140,64\nAD 84,84,83\nCV 73,82,83\nMvav 160,144,64\n"
"Ra 128,24,0\nMvav 164,144,68\ns_add_u32 s46, s87, s88\nEX 80,52\nMvav 172,148,68\nRa 132,24,1024\nv_xor_b32 v2, 0x10000, v2\nMvav 168,148,64\nWL 8\nEX 81,53\nMvav 176,152,64\n"
"Ra 136,24,2048\nAD 87,87,85\nMvav 180,152,68\nD1 1,16,46,0\nMvav 188,156,68\nRa 140,24,3072\nMvav 184,156,64\nAD 85,80,81\nCV 76,80,81\nMvav 192,160,64\nRa 144,26,0\n"
"Mvav 196,160,68\nEX 82,54\nMvav 204,164,68\nRa 148,26,1024\nMvav 200,164,64\nWL 8\nEX 83,55\nMvav 208,168,64\nRa 152,26,2048\nAD 85,85,82\nMvav 212,168,68\nD1 1,16,46,1024\n"
"Mvav 220,172,68\nRa 156,26,3072\nMvav 216,172,64\nAD 85,85,83\nCV 77,82,83\nMvav 224,176,64\nRa 160,24,8192\nMvav 228,176,68\ns_sub_u32 s90, s90, 1\nEX 80,56\nMvav 236,180,68\n"
"Ra 164,24,9216\ns_cbranch_scc1 .E192\n.E193:\nMvav 232,180,64\nWL 6\nEX 81,57\nMvav 240,184,64\nRa 168,24,10240\nAD 84,84,80\nMvav 244,184,68\nD1 1,16,46,2048\n"
"s_sub_u32 s99, s99, 1\nMvav 252,188,68\nRa 172,24,11264\nMvav 248,188,64\nAD 84,84,81\nCV 74,80,81\ns_cbranch_scc0 .A190\ns_branch .A198\n.E192:\ns_swappc_b64 s[80:81], s[8:9]\n"
"s_branch .E193\n.HD143:\nbuffer_store_dword v25, off, s[16:19], s54 offset:4 sc0 sc1\nv_cmp_ne_u32 vcc, s15, v27\ns_nop 0\ns_cbranch_vccnz .S177\n.S178:\nv_and_b32 v27, 0x70, v1\n"
"buffer_load_dword v27, v27, s[16:19], s61 offen offset:8 sc0 sc1\ns_mov_b32 s90, 1\ns_swappc_b64 s[8:9], s[80:81]\nv_cmp_ne_u32 vcc, s15, v27\ns_nop 0\ns_cbranch_vccnz .S180\n"
".S181:\nv_and_b32 v27, 0xf0, v1\nbuffer_load_dword v27, v27, s[16:19], s61 offen offset:4 sc0 sc1\ns_mov_b32 s90, 1\ns_swappc_b64 s[8:9], s[80:81]\nv_cmp_ne_u32 vcc, s15, v27\n"
"s_nop 0\ns_cbranch_vccnz .S183\n.S184:\ns_mov_b32 s90, s91\ns_swappc_b64 s[8:9], s[80:81]\ns_branch .FF186\n.A198:\nCvaa 32,128,192,4\nRa 176,26,8192\nMvaa 32,132,200\n"
"s_add_u32 s49, s49, 131072\nEX 82,58\nMvaa 32,136,208\nRa 180,26,9216\nMvaa 32,140,216\nWL 6\nEX 83,59\nCvaa 36,140,220,8\nRa 184,26,10240\nAD 84,84,82\nMvaa 36,136,212\n"
"D1 1,16,46,3072\ns_add_u32 m0, s89, 32768\nMvaa 36,132,204\nRa 188,26,11264\nMvaa 36,128,196\nAD 84,84,83\nCV 75,82,83\ns_mov_b32 s94, s96\ns_add_u32 s95, s96, 65536\n"
"v_and_b32 v108, 0xf0, v1\nMvaa 36,144,228\nRa 128,2,16384\nMvaa 36,148,236\ns_add_u32 s46, s86, s88\nEX 80,60\nMvaa 36,152,244\nRa 132,2,17408\nMvaa 36,156,252\nWL 6\nEX 81,61\n"
"Mvaa 32,156,248\nRa 136,2,18432\nAD 85,85,80\nMvaa 32,152,240\nD1 1,16,46,0\nMvaa 32,148,232\nRa 140,2,19456\nMvaa 32,144,224\nAD 85,85,81\nCV 78,80,81\nCvaa 40,160,192,12\n"
"Ra 144,2,20480\nMvaa 40,164,200\nSv 92,1,16,49,0\nEX 82,62\nMvaa 40,168,208\nRa 148,2,21504\nMvaa 40,172,216\nWL 6\nEX 83,63\nCvaa 44,172,220,16\nRa 152,2,22528\nAD 85,85,82\n"
"Mvaa 44,168,212\nD1 1,16,46,1024\nMvaa 44,164,204\nRa 156,2,23552\nMvaa 44,160,196\nAD 85,85,83\nCV 79,82,83\nMvaa 44,176,228\nRa 160,2,24576\nv_max_f32 v90, v84, v85\n"
"Mvaa 44,180,236\ns_add_u32 s88, s88, 32768\nEX 80,32\nMvaa 44,184,244\nRa 164,2,25600\nv_cmp_lt_f32 vcc, s63, v90\nMvaa 44,188,252\nWL 8\nEX 81,33\nMvaa 40,188,248\n"
"Ra 168,2,26624\ns_cbranch_vccnz .T201\n.T202:\nMvaa 40,184,240\nD1 1,16,46,2048\nMvaa 40,180,232\nRa 172,2,27648\nAD 86,86,84\nMvaa 40,176,224\nAD 84,80,81\nCV 64,80,81\n"
"Mvav 128,128,72\nRa 176,2,28672\nv_lshrrev_b32 v92, 8, v1\nMvav 132,128,76\nEX 82,34\nMvav 140,132,76\nRa 180,2,29696\nMvav 136,132,72\ns_waitcnt vmcnt(13) lgkmcnt(8)\nEX 83,35\n"
"Mvav 144,136,72\nRa 184,2,30720\nAD 84,84,82\nMvav 148,136,76\nD1 1,16,46,3072\ns_add_u32 m0, s89, 49152\nMvav 156,140,76\nRa 188,2,31744\ns_barrier\nMvav 152,140,72\nAD 84,84,83\n"
"CV 65,82,83\n.W558:\ns_cbranch_execz .WB151\nMvav 160,144,72\nRa 128,24,32768\nv_bfe_u32 v93, v1, 6, 2\nMvav 164,144,76\ns_add_u32 s46, s87, s88\nEX 80,36\nMvav 172,148,76\n"
"Ra 132,24,33792\nv_cmp_eq_u32 vcc, v92, v93\nv_and_b32 v92, 16, v1\nv_mov_b32 v93, 0x3f80\nv_cndmask_b32 v93, 0, v93, vcc\nMvav 168,148,72\nWL 8\nEX 81,37\nMvav 176,152,72\n"
"Ra 136,24,34816\nAD 87,87,85\nMvav 180,152,76\nD1 1,16,46,0\nMvav 188,156,76\nRa 140,24,35840\nv_lshlrev_b32 v93, v92, v93\nMvav 184,156,72\nAD 85,80,81\nCV 68,80,81\n"
"Mvav 192,160,72\nRa 144,26,32768\nv_and_b32 v92, 32, v1\nMvav 196,160,76\nEX 82,38\nMvav 204,164,76\nRa 148,26,33792\nv_cmp_eq_u32 vcc, 0, v92\nv_mov_b32 v112, 0\n"
"v_mov_b32 v113, 0\nv_cndmask_b32 v114, 0, v93, vcc\nMvav 200,164,72\nWL 8\nEX 83,39\nMvav 208,168,72\nRa 152,26,34816\nAD 85,85,82\nMvav 212,168,76\nD1 1,16,46,1024\n"
"Mvav 220,172,76\nRa 156,26,35840\nv_sub_u32 v115, v93, v114\nMvav 216,172,72\nAD 85,85,83\nCV 69,82,83\nMvav 224,176,72\nRa 160,24,40960\nv_mov_b32 v116, 0\nMvav 228,176,76\n"
"s_xor_b32 s89, s89, 0x10000\nEX 80,40\nMvav 236,180,76\nRa 164,24,41984\nMvav 232,180,72\nWL 6\nEX 81,41\nMvav 240,184,72\nRa 168,24,43008\nAD 84,84,80\nMvav 244,184,76\n"
"D1 1,16,46,2048\nMvav 252,188,76\nRa 172,24,44032\nv_mov_b32 v117, 0\nMvav 248,188,72\nAD 84,84,81\nCV 66,80,81\nCvaa 48,128,192,4\nRa 176,26,40960\nMvaa 48,132,200\n"
"s_mov_b32 s86, s94\nEX 82,42\nMvaa 48,136,208\nRa 180,26,41984\nMvaa 48,140,216\nWL 6\nEX 83,43\nCvaa 52,140,220,8\nRa 184,26,43008\nAD 84,84,82\nMvaa 52,136,212\nD1 1,16,46,3072\n"
"s_add_u32 m0, s89, 0\nMvaa 52,132,204\nRa 188,26,44032\nMvaa 52,128,196\nAD 84,84,83\nCV 67,82,83\nMvaa 52,144,228\nRa 128,2,49152\nMvaa 52,148,236\ns_add_u32 s46, s86, s88\n"
"EX 80,44\nMvaa 52,152,244\nRa 132,2,50176\nv_xor_b32 v24, 0x10000, v24\nMvaa 52,156,252\nWL 6\nEX 81,45\nMvaa 48,156,248\nRa 136,2,51200\nAD 85,85,80\nMvaa 48,152,240\n"
"D1 1,16,46,0\nMvaa 48,148,232\nRa 140,2,52224\nMvaa 48,144,224\nAD 85,85,81\nCV 70,80,81\nCvaa 56,160,192,12\nRa 144,2,53248\nMvaa 56,164,200\nSv 92,1,16,49,1024\nEX 82,46\n"
"Mvaa 56,168,208\nRa 148,2,54272\nv_xor_b32 v26, 0x10000, v26\nMvaa 56,172,216\nWL 6\nEX 83,47\nCvaa 60,172,220,16\nRa 152,2,55296\nAD 85,85,82\nMvaa 60,168,212\nD1 1,16,46,1024\n"
"Mvaa 60,164,204\nRa 156,2,56320\nMvaa 60,160,196\nAD 85,85,83\nCV 71,82,83\nMvaa 60,176,228\nRa 160,2,57344\nv_max_f32 v90, v84, v85\nMvaa 60,180,236\ns_add_u32 s88, s88, 32768\n"
"EX 80,48\nMvaa 60,184,244\nRa 164,2,58368\nv_cmp_lt_f32 vcc, s63, v90\nMvaa 60,188,252\nWL 8\nEX 81,49\nMvaa 56,188,248\nRa 168,2,59392\ns_cbranch_vccnz .T203\n.T204:\n"
"Mvaa 56,184,240\nD1 1,16,46,2048\nMvaa 56,180,232\nRa 172,2,60416\nAD 86,86,84\nMvaa 56,176,224\nAD 84,80,81\nCV 72,80,81\nMvav 128,128,64\nRa 176,2,61440\nMvav 132,128,68\n"
"EX 82,50\nMvav 140,132,68\nRa 180,2,62464\nMvav 136,132,64\ns_waitcnt vmcnt(13) lgkmcnt(8)\nEX 83,51\nMvav 144,136,64\nRa 184,2,63488\nAD 84,84,82\nMvav 148,136,68\n"
"D1 1,16,46,3072\ns_add_u32 m0, s89, 16384\nMvav 156,140,68\nRa 188,2,64512\ns_barrier\nMvav 152,140,64\nAD 84,84,83\nCV 73,82,83\nMvav 160,144,64\nRa 128,24,0\nMvav 164,144,68\n"
"s_add_u32 s46, s87, s88\nEX 80,52\nMvav 172,148,68\nRa 132,24,1024\nv_xor_b32 v2, 0x10000, v2\nMvav 168,148,64\nWL 8\nEX 81,53\nMvav 176,152,64\nRa 136,24,2048\nAD 87,87,85\n"
"Mvav 180,152,68\nD1 1,16,46,0\nMvav 188,156,68\nRa 140,24,3072\nMvav 184,156,64\nAD 85,80,81\nCV 76,80,81\nMvav 192,160,64\nRa 144,26,0\nMvav 196,160,68\ns_cmp_lg_u32 s92, 0\n"
"EX 82,54\nMvav 204,164,68\nRa 148,26,1024\ns_cbranch_scc0 .E199\nv_cmpx_gt_i32 vcc, 19, v3\nv_mov_b32 v15, s97\nv_cmpx_gt_i32 vcc, 18, v3\nv_mov_b32 v14, s97\n"
"v_cmpx_gt_i32 vcc, 17, v3\nv_mov_b32 v13, s97\nv_cmpx_gt_i32 vcc, 16, v3\nv_mov_b32 v12, s97\nv_cmpx_gt_i32 vcc, 3, v3\nv_mov_b32 v7, s97\nv_cmpx_gt_i32 vcc, 2, v3\n"
"v_mov_b32 v6, s97\nv_cmpx_gt_i32 vcc, 1, v3\nv_mov_b32 v5, s97\nv_cmpx_gt_i32 vcc, 0, v3\nv_mov_b32 v4, s97\ns_mov_b64 exec, -1\ns_nop 2\n.E199:\ns_nop 0\nMvav 200,164,64\nWL 8\n"
"EX 83,55\nMvav 208,168,64\nRa 152,26,2048\nAD 85,85,82\nMvav 212,168,68\nD1 1,16,46,1024\nMvav 220,172,68\nRa 156,26,3072\nMvav 216,172,64\nAD 85,85,83\nCV 77,82,83\n"
"Mvav 224,176,64\nRa 160,24,8192\nMvav 228,176,68\nEX 80,56\nMvav 236,180,68\nRa 164,24,9216\nMvav 232,180,64\nWL 6\nEX 81,57\nMvav 240,184,64\nRa 168,24,10240\nAD 84,84,80\n"
"Mvav 244,184,68\nD1 1,16,46,2048\nMvav 252,188,68\nRa 172,24,11264\nMvav 248,188,64\nAD 84,84,81\nCV 74,80,81\nCvaa 32,128,192,4\nRa 176,26,8192\nMvaa 32,132,200\nEX 82,58\n"
"Mvaa 32,136,208\nRa 180,26,9216\nMvaa 32,140,216\nWL 6\nEX 83,59\nCvaa 36,140,220,8\nRa 184,26,10240\nAD 84,84,82\nMvaa 36,136,212\nD1 1,16,46,3072\ns_add_u32 m0, s89, 32768\n"
"Mvaa 36,132,204\nRa 188,26,11264\nMvaa 36,128,196\nAD 84,84,83\nCV 75,82,83\nMvaa 36,144,228\nRa 128,2,16384\nMvaa 36,148,236\ns_add_u32 s46, s86, s88\nEX 80,60\nMvaa 36,152,244\n"
"Ra 132,2,17408\nMvaa 36,156,252\nWL 6\nEX 81,61\nMvaa 32,156,248\nRa 136,2,18432\nAD 85,85,80\nMvaa 32,152,240\nD1 1,16,46,0\nMvaa 32,148,232\nRa 140,2,19456\nMvaa 32,144,224\n"
"AD 85,85,81\nCV 78,80,81\nCvaa 40,160,192,12\nRa 144,2,20480\nMvaa 40,164,200\ns_mov_b32 s87, s95\nEX 82,62\nMvaa 40,168,208\nRa 148,2,21504\nMvaa 40,172,216\nWL 6\nEX 83,63\n"
"Cvaa 44,172,220,16\nRa 152,2,22528\nAD 85,85,82\nMvaa 44,168,212\nD1 1,16,46,1024\nMvaa 44,164,204\nRa 156,2,23552\nMvaa 44,160,196\nAD 85,85,83\nCV 79,82,83\nMvaa 44,176,228\n"
"Ra 160,2,24576\nv_max_f32 v90, v84, v85\nMvaa 44,180,236\ns_add_u32 s88, s88, 32768\nEX 80,32\nMvaa 44,184,244\nRa 164,2,25600\nv_cmp_lt_f32 vcc, s63, v90\nMvaa 44,188,252\nWL 8\n"
"EX 81,33\nMvaa 40,188,248\nRa 168,2,26624\ns_cbranch_vccnz .T207\n.T208:\nMvaa 40,184,240\nD1 1,16,46,2048\nMvaa 40,180,232\nRa 172,2,27648\nAD 86,86,84\nMvaa 40,176,224\n"
"AD 84,80,81\nCV 64,80,81\nMvav 128,128,72\nRa 176,2,28672\nMvav 132,128,76\nEX 82,34\nMvav 140,132,76\nRa 180,2,29696\nMvav 136,132,72\ns_waitcnt vmcnt(12) lgkmcnt(8)\nEX 83,35\n"
"Mvav 144,136,72\nRa 184,2,30720\nAD 84,84,82\nMvav 148,136,76\nD1 1,16,46,3072\ns_add_u32 m0, s89, 49152\nMvav 156,140,76\nRa 188,2,31744\ns_barrier\nMvav 152,140,72\nAD 84,84,83\n"
"CV 65,82,83\nMvav 160,144,72\nRa 128,24,32768\nMvav 164,144,76\ns_add_u32 s46, s87, s88\nEX 80,36\nMvav 172,148,76\nRa 132,24,33792\nMvav 168,148,72\nWL 8\nEX 81,37\n"
"Mvav 176,152,72\nRa 136,24,34816\nAD 87,87,85\nMvav 180,152,76\nD1 1,16,46,0\nMvav 188,156,76\nRa 140,24,35840\nMvav 184,156,72\nAD 85,80,81\nCV 68,80,81\nMvav 192,160,72\n"
"Ra 144,26,32768\nbuffer_load_dwordx4 v[100:103], v108, s[16:19], s65 offen offset:0 sc0 sc1\nbuffer_load_dwordx4 v[104:107], v108, s[16:19], s65 offen offset:256 sc0 sc1\n"
"Mvav 196,160,76\ns_cmp_eq_u32 s13, 7\nEX 82,38\nMvav 204,164,76\nRa 148,26,33792\ns_cbranch_scc0 .E205\nv_cmpx_gt_i32 vcc, 19, v0\nv_mov_b32 v15, s97\nv_cmpx_gt_i32 vcc, 18, v0\n"
"v_mov_b32 v14, s97\nv_cmpx_gt_i32 vcc, 17, v0\nv_mov_b32 v13, s97\nv_cmpx_gt_i32 vcc, 16, v0\nv_mov_b32 v12, s97\nv_cmpx_gt_i32 vcc, 3, v0\nv_mov_b32 v7, s97\n"
"v_cmpx_gt_i32 vcc, 2, v0\nv_mov_b32 v6, s97\nv_cmpx_gt_i32 vcc, 1, v0\nv_mov_b32 v5, s97\nv_cmpx_gt_i32 vcc, 0, v0\nv_mov_b32 v4, s97\ns_mov_b64 exec, -1\ns_nop 2\n.E205:\n"
"s_nop 0\nMvav 200,164,72\nWL 8\nEX 83,39\nMvav 208,168,72\nRa 152,26,34816\nAD 85,85,82\nMvav 212,168,76\nD1 1,16,46,1024\nMvav 220,172,76\nRa 156,26,35840\nMvav 216,172,72\n"
"AD 85,85,83\nCV 69,82,83\nMvav 224,176,72\nRa 160,24,40960\nMvav 228,176,76\ns_xor_b32 s89, s89, 0x10000\nEX 80,40\nMvav 236,180,76\nRa 164,24,41984\nMvav 232,180,72\nWL 6\n"
"EX 81,41\nMvav 240,184,72\nRa 168,24,43008\nAD 84,84,80\nMvav 244,184,76\nD1 1,16,46,2048\nMvav 252,188,76\nRa 172,24,44032\nMvav 248,188,72\nAD 84,84,81\nCV 66,80,81\n"
"Cvaa 48,128,192,4\nRa 176,26,40960\nMvaa 48,132,200\nEX 82,42\nMvaa 48,136,208\nRa 180,26,41984\nMvaa 48,140,216\nWL 6\nEX 83,43\nCvaa 52,140,220,8\nRa 184,26,43008\nAD 84,84,82\n"
"Mvaa 52,136,212\nD1 1,16,46,3072\ns_add_u32 m0, s89, 0\nMvaa 52,132,204\nRa 188,26,44032\nMvaa 52,128,196\nAD 84,84,83\nCV 67,82,83\nMvaa 52,144,228\nRa 128,2,49152\nWV 3\n"
"v_xor_b32 v109, s15, v102\nv_xor_b32 v110, s15, v106\nv_or_b32 v109, v109, v110\nv_cmp_ne_u32 vcc, 0, v109\ns_nop 3\ns_cmp_lg_u64 vcc, 0\ns_cselect_b32 s67, 1, 0\n"
"s_cselect_b32 s6, 0x40000000, s66\nMvaa 52,148,236\ns_add_u32 s46, s86, s88\nEX 80,44\ns_add_u32 s5, s6, 0\nMvaa 52,152,244\nRa 132,2,50176\nv_xor_b32 v24, 0x10000, v24\n"
"Mvaa 52,156,252\nWL 6\nEX 81,45\nbuffer_load_dwordx4 a[0:3], v1, s[16:19], s5 offen offset:0 sc1\nMvaa 48,156,248\nRa 136,2,51200\nAD 85,85,80\nMvaa 48,152,240\nD1 1,16,46,0\n"
"Mvaa 48,148,232\nRa 140,2,52224\nbuffer_load_dwordx4 a[4:7], v1, s[16:19], s5 offen offset:1024 sc1\nMvaa 48,144,224\nAD 85,85,81\nCV 70,80,81\nCvaa 56,160,192,12\nRa 144,2,53248\n"
"Mvaa 56,164,200\nEX 82,46\nbuffer_load_dwordx4 a[8:11], v1, s[16:19], s5 offen offset:2048 sc1\nMvaa 56,168,208\nRa 148,2,54272\nv_xor_b32 v26, 0x10000, v26\nMvaa 56,172,216\n"
"WL 6\nEX 83,47\nCvaa 60,172,220,16\nRa 152,2,55296\nAD 85,85,82\nbuffer_load_dwordx4 a[12:15], v1, s[16:19], s5 offen offset:3072 sc1\nMvaa 60,168,212\nD1 1,16,46,1024\n"
"s_add_u32 s5, s6, 4096\nMvaa 60,164,204\nRa 156,2,56320\nMvaa 60,160,196\nAD 85,85,83\nCV 71,82,83\nbuffer_load_dwordx4 a[16:19], v1, s[16:19], s5 offen offset:0 sc1\n"
"Mvaa 60,176,228\nRa 160,2,57344\nv_max_f32 v90, v84, v85\nMvaa 60,180,236\ns_add_u32 s88, s88, 32768\nEX 80,48\nMvaa 60,184,244\nRa 164,2,58368\nv_cmp_lt_f32 vcc, s63, v90\n"
"Mvaa 60,188,252\nWL 8\nEX 81,49\nMvaa 56,188,248\nRa 168,2,59392\ns_cbranch_vccnz .T209\n.T210:\nbuffer_load_dwordx4 a[20:23], v1, s[16:19], s5 offen offset:1024 sc1\n"
"Mvaa 56,184,240\nD1 1,16,46,2048\nMvaa 56,180,232\nRa 172,2,60416\nAD 86,86,84\nMvaa 56,176,224\nAD 84,80,81\nCV 72,80,81\n"
"buffer_load_dwordx4 a[24:27], v1, s[16:19], s5 offen offset:2048 sc1\nMvav 128,128,64\nRa 176,2,61440\nMvav 132,128,68\nEX 82,50\nMvav 140,132,68\nRa 180,2,62464\n"
"buffer_load_dwordx4 a[28:31], v1, s[16:19], s5 offen offset:3072 sc1\nMvav 136,132,64\nWL 8\nEX 83,51\ns_add_u32 s5, s6, 16384\nMvav 144,136,64\nRa 184,2,63488\nAD 84,84,82\n"
"Mvav 148,136,68\nD1 1,16,46,3072\ns_add_u32 m0, s89, 16384\nbuffer_load_dwordx4 a[32:35], v1, s[16:19], s5 offen offset:0 sc1\nMvav 156,140,68\nRa 188,2,64512\ns_barrier\n"
"Mvav 152,140,64\nAD 84,84,83\nCV 73,82,83\nMvav 160,144,64\nRa 128,24,0\nbuffer_load_dwordx4 a[36:39], v1, s[16:19], s5 offen offset:1024 sc1\nMvav 164,144,68\n"
"s_add_u32 s46, s87, s88\nEX 80,52\nMvav 172,148,68\nRa 132,24,1024\nv_xor_b32 v2, 0x10000, v2\nMvav 168,148,64\nWL 8\nEX 81,53\n"
"buffer_load_dwordx4 a[40:43], v1, s[16:19], s5 offen offset:2048 sc1\nMvav 176,152,64\nRa 136,24,2048\nAD 87,87,85\nMvav 180,152,68\nD1 1,16,46,0\nMvav 188,156,68\nRa 140,24,3072\n"
"buffer_load_dwordx4 a[44:47], v1, s[16:19], s5 offen offset:3072 sc1\nMvav 184,156,64\nAD 85,80,81\nCV 76,80,81\ns_add_u32 s5, s6, 20480\nMvav 192,160,64\nRa 144,26,0\n"
"Mvav 196,160,68\nEX 82,54\nbuffer_load_dwordx4 a[48:51], v1, s[16:19], s5 offen offset:0 sc1\nMvav 204,164,68\nRa 148,26,1024\nMvav 200,164,64\nWL 8\nEX 83,55\nMvav 208,168,64\n"
"Ra 152,26,2048\nAD 85,85,82\nbuffer_load_dwordx4 a[52:55], v1, s[16:19], s5 offen offset:1024 sc1\nMvav 212,168,68\nD1 1,16,46,1024\nMvav 220,172,68\nRa 156,26,3072\n"
"Mvav 216,172,64\nAD 85,85,83\nCV 77,82,83\nbuffer_load_dwordx4 a[56:59], v1, s[16:19], s5 offen offset:2048 sc1\nMvav 224,176,64\n.W559:\ns_cbranch_execz .WB151\nRa 160,24,8192\n"
"Mvav 228,176,68\nEX 80,56\nMvav 236,180,68\nRa 164,24,9216\nbuffer_load_dwordx4 a[60:63], v1, s[16:19], s5 offen offset:3072 sc1\nMvav 232,180,64\nWL 10\nEX 81,57\n"
"Mvav 240,184,64\nRa 168,24,10240\nAD 84,84,80\nMvav 244,184,68\nD1 1,16,46,2048\nMvav 252,188,68\nRa 172,24,11264\nMvav 248,188,64\nAD 84,84,81\nCV 74,80,81\nWV 20\n"
"s_cmp_lg_u32 s67, 0\ns_cbranch_scc0 .E218\nv_xor_b32 v109, s15, v102\nv_xor_b32 v110, s15, v106\nv_or_b32 v109, v109, v110\nv_cmp_ne_u32 vcc, 0, v109\ns_nop 0\n"
"s_cbranch_vccnz .S215\n.S216:\ns_add_u32 s5, s66, 0\nbuffer_load_dwordx4 a[0:3], v1, s[16:19], s5 offen offset:0 sc1\n"
"buffer_load_dwordx4 a[4:7], v1, s[16:19], s5 offen offset:1024 sc1\nbuffer_load_dwordx4 a[8:11], v1, s[16:19], s5 offen offset:2048 sc1\n"
"buffer_load_dwordx4 a[12:15], v1, s[16:19], s5 offen offset:3072 sc1\ns_add_u32 s5, s66, 4096\nbuffer_load_dwordx4 a[16:19], v1, s[16:19], s5 offen offset:0 sc1\n"
"buffer_load_dwordx4 a[20:23], v1, s[16:19], s5 offen offset:1024 sc1\nbuffer_load_dwordx4 a[24:27], v1, s[16:19], s5 offen offset:2048 sc1\n"
"buffer_load_dwordx4 a[28:31], v1, s[16:19], s5 offen offset:3072 sc1\ns_add_u32 s5, s66, 16384\nbuffer_load_dwordx4 a[32:35], v1, s[16:19], s5 offen offset:0 sc1\n"
"buffer_load_dwordx4 a[36:39], v1, s[16:19], s5 offen offset:1024 sc1\nbuffer_load_dwordx4 a[40:43], v1, s[16:19], s5 offen offset:2048 sc1\n"
"buffer_load_dwordx4 a[44:47], v1, s[16:19], s5 offen offset:3072 sc1\ns_add_u32 s5, s66, 20480\nbuffer_load_dwordx4 a[48:51], v1, s[16:19], s5 offen offset:0 sc1\n"
"buffer_load_dwordx4 a[52:55], v1, s[16:19], s5 offen offset:1024 sc1\nbuffer_load_dwordx4 a[56:59], v1, s[16:19], s5 offen offset:2048 sc1\n"
"buffer_load_dwordx4 a[60:63], v1, s[16:19], s5 offen offset:3072 sc1\nWV 0\ns_mov_b32 s67, 0\n.E218:\nv_cmp_lt_f32 vcc, 0, v101\nSU 20,100,88\nv_mul_f32 v101, 0x3e800000, v101\n"
"v_cndmask_b32 v20, 0, v20, vcc\nv_cmp_lt_f32 vcc, 0, v105\nSU 21,104,89\nv_mul_f32 v105, 0x3e800000, v105\nv_cndmask_b32 v21, 0, v21, vcc\nv_or_b32 v91, v20, v21\n"
"v_cmp_ne_u32 vcc, 0, v91\ns_nop 0\ns_cbranch_vccnz .D213\nAD 86,86,101\nAD 87,87,105\n.D214:\nMvva 128,114,0\nMvva 136,112,0\nEX 82,58\nMvva 140,112,4\nMvva 132,114,4\nWV 15\n"
"EX 83,59\nMvva 144,114,8\nAD 84,84,82\nMvva 152,112,8\nD1 1,16,46,3072\nMvva 156,112,12\ns_add_u32 m0, s89, 32768\ns_add_u32 s46, s86, s88\nMvva 148,114,12\nAD 84,84,83\n"
"CV 75,82,83\nMvva 160,114,16\nRa 128,2,16384\nv_xor_b32 v3, 0x10000, v2\nv_add_u32 v0, 0x20000, v1\nMvva 168,112,16\nEX 80,60\nMvva 172,112,20\nRa 132,2,17408\n"
"s_sub_u32 s94, s49, 1703936\nMvva 164,114,20\nWV 10\nEX 81,61\nMvva 176,114,24\nRa 136,2,18432\nAD 85,85,80\ns_xor_b32 s92, s94, 0x202000\nMvva 184,112,24\nD1 1,16,46,0\n"
"s_lshl_b32 s89, s11, 12\nMvva 188,112,28\nRa 140,2,19456\ns_lshl1_add_u32 s2, s12, 0\nMvva 180,114,28\nAD 85,85,81\nCV 78,80,81\nMvva 192,114,32\nRa 144,2,20480\n"
"s_lshl3_add_u32 s2, s2, s13\ns_lshl4_add_u32 s2, s2, 0xc048200\nMvva 200,112,32\nEX 82,62\nMvva 204,112,36\nRa 148,2,21504\ns_and_b32 s3, s72, 0x2000\nMvva 196,114,36\nWV 6\n"
"EX 83,63\nMvva 208,114,40\nRa 152,2,22528\nAD 85,85,82\nMvva 216,112,40\nD1 1,16,46,1024\ns_cmp_eq_u32 s11, 1\ns_cselect_b32 s54, s2, s54\ns_cselect_b32 s3, s3, 0\n"
"Mvva 220,112,44\nRa 156,2,23552\nv_xor_b32 v25, s3, v25\nMvva 212,114,44\nAD 85,85,83\nCV 79,82,83\nMvva 224,114,48\nRa 160,2,24576\nv_max_f32 v90, v84, v85\nMvva 232,112,48\n"
"v_cmp_lt_f32 vcc, s63, v90\nMvva 236,112,52\nRa 164,2,25600\nMvva 228,114,52\ns_waitcnt vmcnt(4) lgkmcnt(8)\nMvva 240,114,56\nRa 168,2,26624\nMvva 248,112,56\nD1 1,16,46,2048\n"
"Mvva 252,112,60\nRa 172,2,27648\nMvva 244,114,60\ns_cbranch_vccnz .T219\n.T220:\nAD 86,86,84\nAD 87,87,85\nMvav 128,128,72\nRa 176,2,28672\nv_mov_b32 v84, v86\nv_mov_b32 v85, v87\n"
"Mvav 132,128,76\nv_permlane32_swap_b32 v86, v84\nv_permlane32_swap_b32 v87, v85\nMvav 140,132,76\nRa 180,2,29696\nAD 86,86,84\nMvav 136,132,72\nWL 8\nAD 87,87,85\nMvav 144,136,72\n"
"Ra 184,2,30720\nv_mov_b32 v84, v86\nv_mov_b32 v85, v87\nMvav 148,136,76\nD1 1,16,46,3072\nv_permlane16_swap_b32 v86, v84\nMvav 156,140,76\nRa 188,2,31744\n"
"v_permlane16_swap_b32 v87, v85\nMvav 152,140,72\nAD 86,86,84\nAD 87,87,85\nMvav 160,144,72\nRa 128,2,0\nv_rcp_f32 v86, v86\nMvav 164,144,76\nv_rcp_f32 v87, v87\nMvav 172,148,76\n"
"Ra 132,2,1024\nMU 128,128,86\nMU 129,129,86\nMvav 168,148,72\nWL 8\ns_add_u32 m0, s89, 131072\ns_add_u32 s46, s96, 1212416\nCV 96,128,129\nMvav 176,152,72\nRa 136,2,2048\n"
"MU 130,130,86\nMU 131,131,86\nMvav 180,152,76\nD1 1,16,46,0\nCV 97,130,131\nMU 132,132,87\nMvav 188,156,76\nRa 140,2,3072\nMU 133,133,87\nCV 100,132,133\nMvav 184,156,72\n"
"MU 134,134,87\nMU 135,135,87\nCV 101,134,135\nMvav 192,160,72\nRa 144,2,4096\nMU 136,136,86\nMU 137,137,86\nCV 98,136,137\nMvav 196,160,76\nMU 138,138,86\nMU 139,139,86\n"
"CV 99,138,139\nMvav 204,164,76\nRa 148,2,5120\nMU 140,140,87\nMU 141,141,87\nMvav 200,164,72\nWL 8\nCV 102,140,141\nMU 142,142,87\nMvav 208,168,72\nRa 152,2,6144\nMU 143,143,87\n"
"CV 103,142,143\nMvav 212,168,76\nD1 1,16,46,1024\nMU 144,144,86\nMU 145,145,86\nMvav 220,172,76\nRa 156,2,7168\nCV 104,144,145\nMU 146,146,86\nMU 147,147,86\nMvav 216,172,72\n"
"CV 105,146,147\nMU 148,148,87\nMU 149,149,87\nMvav 224,176,72\nRa 160,2,8192\nCV 108,148,149\nMU 150,150,87\nMvav 228,176,76\nMU 151,151,87\nCV 109,150,151\nMU 152,152,86\n"
"Mvav 236,180,76\nRa 164,2,9216\nMU 153,153,86\nCV 106,152,153\nMvav 232,180,72\nWL 8\nMU 154,154,86\nMU 155,155,86\nCV 107,154,155\nMvav 240,184,72\nRa 168,2,10240\nMU 156,156,87\n"
"MU 157,157,87\nMvav 244,184,76\nD1 1,16,46,2048\nCV 110,156,157\nMU 158,158,87\nMvav 252,188,76\nRa 172,2,11264\nMU 159,159,87\nCV 111,158,159\nMvav 248,188,72\nMU 160,160,86\n"
"MU 161,161,86\nCV 112,160,161\nZvav 128,128,96\nRa 176,2,12288\nMU 162,162,86\nMU 163,163,86\nMvav 128,132,104\nCV 113,162,163\nMU 164,164,87\nZvav 132,132,108\nRa 180,2,13312\n"
"MU 165,165,87\nCV 116,164,165\nMvav 132,128,100\nWL 8\nMU 166,166,87\nMU 167,167,87\nZvav 140,136,100\nRa 184,2,14336\nCV 117,166,167\nMU 168,168,86\nMvav 140,140,108\n"
"D1 1,16,46,3072\nMU 169,169,86\nCV 114,168,169\nZvav 136,140,104\nRa 188,2,15360\ns_barrier\nMU 170,170,86\nMvav 136,136,96\nMU 171,171,86\nCV 115,170,171\nMU 172,172,87\n"
"Zvav 144,144,96\nRa 128,2,32768\nMU 173,173,87\nCV 118,172,173\nMvav 144,148,104\nMU 174,174,87\nMU 175,175,87\nbuffer_store_dword v25, off, s[16:19], s54 offset:512 sc0 sc1\n"
"Zvav 148,148,108\nRa 132,2,33792\nCV 119,174,175\nMU 176,176,86\nMvav 148,144,100\nWL 8\ns_add_u32 m0, s89, 114688\ns_add_u32 s46, s96, 1179648\nZvav 156,152,100\nRa 136,2,34816\n"
"MU 177,177,86\nCV 120,176,177\nMvav 156,156,108\nD1 1,16,46,0\nMU 178,178,86\nMU 179,179,86\nZvav 152,156,104\nRa 140,2,35840\nCV 121,178,179\nMU 180,180,87\nMvav 152,152,96\n"
"MU 181,181,87\nCV 124,180,181\nMU 182,182,87\nZvav 64,160,96\nRa 144,2,36864\nMU 183,183,87\nCV 125,182,183\nMvav 64,164,104\nMU 184,184,86\nMU 185,185,86\nZvav 68,164,108\n"
"Ra 148,2,37888\nCV 122,184,185\nMU 186,186,86\nMvav 68,160,100\nWL 8\nMU 187,187,86\nCV 123,186,187\nZvav 76,168,100\nRa 152,2,38912\nMU 188,188,87\nMU 189,189,87\n"
"Mvav 76,172,108\nD1 1,16,46,1024\nCV 126,188,189\nMU 190,190,87\nZvav 72,172,104\nRa 156,2,39936\nMU 191,191,87\nCV 127,190,191\nMvav 72,168,96\nMU 192,192,86\nMU 193,193,86\n"
"CV 32,192,193\nMvav 128,176,112\nRa 160,2,40960\nMU 194,194,86\nMU 195,195,86\nMvav 128,180,120\nCV 33,194,195\nMU 196,196,87\nMU 197,197,87\nMvav 132,180,124\nRa 164,2,41984\n"
"CV 36,196,197\nMU 198,198,87\nMvav 132,176,116\nWL 8\nMU 199,199,87\nMvav 140,184,116\nRa 168,2,43008\nCV 37,198,199\nMU 200,200,86\nMvav 140,188,124\nD1 1,16,46,2048\n"
"MU 201,201,86\nCV 34,200,201\nMvav 136,188,120\nRa 172,2,44032\nMU 202,202,86\nMU 203,203,86\nMvav 136,184,112\nCV 35,202,203\nMU 204,204,87\nMU 205,205,87\nMvav 144,128,112\n"
"Ra 176,2,45056\nCV 38,204,205\nMU 206,206,87\nMvav 144,132,120\nMU 207,207,87\nCV 39,206,207\nMvav 148,132,124\nRa 180,2,46080\nMU 208,208,86\nMU 209,209,86\nMvav 148,128,116\n"
"WL 8\nCV 40,208,209\nMU 210,210,86\nMvav 156,136,116\nRa 184,2,47104\nMU 211,211,86\nCV 41,210,211\nMvav 156,140,124\nD1 1,16,46,3072\nMU 212,212,87\nMU 213,213,87\n"
"Mvav 152,140,120\nRa 188,2,48128\nCV 44,212,213\nMvav 152,136,112\nMU 214,214,87\nMU 215,215,87\nCV 45,214,215\nMvav 64,144,112\nRa 128,2,49152\nMU 216,216,86\nMU 217,217,86\n"
"Mvav 64,148,120\nCV 42,216,217\nMU 218,218,86\nMU 219,219,86\nMvav 68,148,124\nRa 132,2,50176\nCV 43,218,219\nMU 220,220,87\nMvav 68,144,116\nWL 8\ns_add_u32 m0, s89, 147456\n"
"s_add_u32 s46, s96, 1245184\nMvav 76,152,116\nRa 136,2,51200\nMU 221,221,87\nCV 46,220,221\nMvav 76,156,124\nD1 1,16,46,0\nMU 222,222,87\nMU 223,223,87\nMvav 72,156,120\n"
"Ra 140,2,52224\nCV 47,222,223\nMvav 72,152,112\nMU 224,224,86\nMU 225,225,86\nCV 48,224,225\nMvav 128,160,32\nRa 144,2,53248\nMU 226,226,86\nMU 227,227,86\nMvav 128,164,40\n"
"CV 49,226,227\nMU 228,228,87\nMvav 132,164,44\nRa 148,2,54272\nMU 229,229,87\nCV 52,228,229\nMvav 132,160,36\nWL 8\nMU 230,230,87\nMU 231,231,87\nMvav 140,168,36\nRa 152,2,55296\n"
"CV 53,230,231\nMU 232,232,86\nMvav 140,172,44\nD1 1,16,46,1024\nMU 233,233,86\nCV 50,232,233\nMvav 136,172,40\nRa 156,2,56320\nMU 234,234,86\nMvav 136,168,32\nMU 235,235,86\n"
"CV 51,234,235\nMU 236,236,87\nMvav 144,176,32\nRa 160,2,57344\nMU 237,237,87\nCV 54,236,237\nMvav 144,180,40\nMU 238,238,87\nMU 239,239,87\nCV 55,238,239\nMvav 148,180,44\n"
"Ra 164,2,58368\nMU 240,240,86\nMU 241,241,86\nMvav 148,176,36\nWL 8\nCV 56,240,241\nMvav 156,184,36\nRa 168,2,59392\nMU 242,242,86\nMU 243,243,86\nMvav 156,188,44\n"
"D1 1,16,46,2048\nCV 57,242,243\nMU 244,244,87\nMvav 152,188,40\nRa 172,2,60416\nMU 245,245,87\nCV 60,244,245\nMvav 152,184,32\nMU 246,246,87\nMU 247,247,87\nCV 61,246,247\n"
"Mvav 64,128,32\nRa 176,2,61440\nMU 248,248,86\nMU 249,249,86\nMvav 64,132,40\nCV 58,248,249\nMU 250,250,86\nMvav 68,132,44\nRa 180,2,62464\nMU 251,251,86\nCV 59,250,251\n"
"Mvav 68,128,36\ns_waitcnt vmcnt(16) lgkmcnt(8)\nMU 252,252,87\nMU 253,253,87\nMvav 76,136,36\nRa 184,2,63488\nCV 62,252,253\nMU 254,254,87\nMvav 76,140,44\nD1 1,16,46,3072\n"
"MU 255,255,87\nCV 63,254,255\nMvav 72,140,40\nRa 188,2,64512\ns_barrier\nMvav 72,136,32\nMvav 128,144,48\nRa 128,3,0\nMvav 128,148,56\nMvav 132,148,60\nRa 132,3,1024\n"
"Mvav 132,144,52\nWL 8\nMvav 140,152,52\nRa 136,3,2048\nMvav 140,156,60\nMvav 136,156,56\nRa 140,3,3072\nMvav 136,152,48\nMvav 144,160,48\nRa 144,3,4096\nMvav 144,164,56\n"
"Mvav 148,164,60\nRa 148,3,5120\nCV 132,132,133\nMvav 148,160,52\nWL 8\nCV 133,134,135\nMvav 156,168,52\nRa 152,3,6144\nCV 134,140,141\nMvav 156,172,60\nCV 135,142,143\n"
"s_add_u32 s5, s94, 0\nMvav 152,172,56\nRa 156,3,7168\nSv 132,1,16,5,1024\nMvav 152,168,48\nCV 128,128,129\nCV 129,130,131\nMvav 64,176,48\nRa 160,3,8192\nCV 130,136,137\n"
"Mvav 64,180,56\nbuffer_load_dword v27, off, s[16:19], s60 offset:512 sc0 sc1\nCV 131,138,139\nMvav 68,180,60\nRa 164,3,9216\nSv 128,1,16,5,0\nMvav 68,176,52\nWL 6\nCV 148,148,149\n"
"Mvav 76,184,52\nRa 168,3,10240\nCV 149,150,151\nMvav 76,188,60\nCV 150,156,157\nCV 151,158,159\nMvav 72,188,56\nRa 172,3,11264\ns_add_u32 s5, s94, 131072\nMvav 72,184,48\n"
"Sv 148,1,16,5,1024\nCV 144,144,145\nZvav 160,128,96\nRa 176,3,12288\nCV 145,146,147\nMvav 160,132,104\nCV 146,152,153\nCV 147,154,155\nMvav 160,136,112\nRa 180,3,13312\n"
"Sv 144,1,16,5,0\nMvav 160,140,120\nWL 6\nCV 68,68,69\nZvav 164,140,124\nRa 184,3,14336\nCV 69,70,71\nMvav 164,136,116\n.W560:\ns_cbranch_execz .WB151\nCV 70,76,77\nCV 71,78,79\n"
"Mvav 164,132,108\nRa 188,3,15360\ns_add_u32 s5, s94, 262144\nMvav 164,128,100\nSv 68,1,16,5,1024\nCV 64,64,65\nMvav 164,144,36\nRa 128,3,16384\nCV 65,66,67\nMvav 164,148,44\n"
"CV 66,72,73\nCV 67,74,75\nMvav 164,152,52\nRa 132,3,17408\nSv 64,1,16,5,0\nMvav 164,156,60\nWL 6\nMvav 160,156,56\nRa 136,3,18432\nMvav 160,152,48\nMvav 160,148,40\n"
"Ra 140,3,19456\nMvav 160,144,32\nZvav 168,160,96\nRa 144,3,20480\nMvav 168,164,104\nMvav 168,168,112\nRa 148,3,21504\nMvav 168,172,120\nWL 6\nZvav 172,172,124\nRa 152,3,22528\n"
"Mvav 172,168,116\nMvav 172,164,108\nRa 156,3,23552\nMvav 172,160,100\nMvav 172,176,36\nRa 160,3,24576\nMvav 172,180,44\nMvav 172,184,52\nRa 164,3,25600\nMvav 172,188,60\nWL 6\n"
"Mvav 168,188,56\nRa 168,3,26624\nMvav 168,184,48\nMvav 168,180,40\nRa 172,3,27648\nMvav 168,176,32\nZvav 176,128,96\nRa 176,3,28672\nCV 164,164,165\nMvav 176,132,104\n"
"CV 165,166,167\nCV 166,172,173\nMvav 176,136,112\nRa 180,3,29696\nCV 167,174,175\nMvav 176,140,120\ns_waitcnt vmcnt(11) lgkmcnt(6)\ns_add_u32 s5, s94, 393216\nZvav 180,140,124\n"
"Ra 184,3,30720\nSv 164,1,16,5,1024\nMvav 180,136,116\nCV 160,160,161\nCV 161,162,163\nMvav 180,132,108\nRa 188,3,31744\ns_barrier\nMvav 180,128,100\nCV 162,168,169\n"
"CV 163,170,171\nMvav 180,144,36\nRa 128,3,32768\nSv 160,1,16,5,0\nMvav 180,148,44\nMvav 180,152,52\nRa 132,3,33792\nMvav 180,156,60\nWL 6\ns_add_u32 m0, s89, 16384\n"
"s_add_u32 s46, s96, 1277952\nMvav 176,156,56\nRa 136,3,34816\nMvav 176,152,48\nD1 1,16,46,0\nMvav 176,148,40\nRa 140,3,35840\nMvav 176,144,32\nZvav 184,160,96\nRa 144,3,36864\n"
"Mvav 184,164,104\nMvav 184,168,112\nRa 148,3,37888\nMvav 184,172,120\nWL 6\nZvav 188,172,124\nRa 152,3,38912\nMvav 188,168,116\nD1 1,16,46,1024\nMvav 188,164,108\nRa 156,3,39936\n"
"Mvav 188,160,100\nMvav 188,176,36\nRa 160,3,40960\nWV 9\nv_cmp_ne_u32 vcc, s15, v27\ns_nop 0\ns_cbranch_vccnz .S246\n.S247:\nMvav 188,180,44\ns_add_u32 s4, s92, 0\n"
"Mvav 188,184,52\nRa 164,3,41984\nMvav 188,188,60\nWL 6\nbuffer_load_dwordx4 v[208:211], v1, s[16:19], s4 offen offset:0 sc0 sc1\nMvav 184,188,56\nRa 168,3,43008\nMvav 184,184,48\n"
"D1 1,16,46,2048\nMvav 184,180,40\nRa 172,3,44032\nMvav 184,176,32\nbuffer_load_dwordx4 v[212:215], v1, s[16:19], s4 offen offset:1024 sc0 sc1\nZvav 192,128,96\nRa 176,3,45056\n"
"v_lshrrev_b32 v88, 8, v1\nMvav 192,132,104\nv_lshlrev_b32 v89, 5, v88\nCV 180,180,181\nMvav 192,136,112\nRa 180,3,46080\nCV 181,182,183\nMvav 192,140,120\nWL 6\nCV 182,188,189\n"
"Zvav 196,140,124\nRa 184,3,47104\nv_bfe_u32 v90, v1, 4, 4\nMvav 196,136,116\nD1 1,16,46,3072\nCV 183,190,191\nMvav 196,132,108\nRa 188,3,48128\ns_add_u32 s5, s94, 524288\n"
"Mvav 196,128,100\nv_mul_u32_u24 v91, 896, v90\nSv 180,1,16,5,1024\nMvav 196,144,36\nRa 128,3,49152\nv_lshl_add_u32 v91, v88, 4, v91\nMvav 196,148,44\nv_add_u32 v90, s21, v90\n"
"CV 176,176,177\nMvav 196,152,52\nRa 132,3,50176\nCV 177,178,179\nMvav 196,156,60\nWL 6\ns_add_u32 m0, s89, 0\ns_add_u32 s46, s96, 1310720\nMvav 192,156,56\nRa 136,3,51200\n"
"v_lshlrev_b32 v88, 3, v90\nMvav 192,152,48\nD1 1,16,46,0\nCV 178,184,185\nMvav 192,148,40\nRa 140,3,52224\nCV 179,186,187\nMvav 192,144,32\nv_add_u32 v94, 64, v90\n"
"Sv 176,1,16,5,0\nZvav 200,160,96\nRa 144,3,53248\ns_mov_b32 s63, 0xc040000\nMvav 200,164,104\nv_lshrrev_b32 v92, 8, v1\nMvav 200,168,112\nRa 148,3,54272\nMvav 200,172,120\nWL 6\n"
"Zvav 204,172,124\nRa 152,3,55296\nv_bfe_u32 v93, v1, 6, 2\nMvav 204,168,116\nD1 1,16,46,1024\nv_cmp_eq_u32 vcc, v92, v93\nv_and_b32 v92, 16, v1\nv_mov_b32 v93, 0x3f80\n"
"v_cndmask_b32 v93, 0, v93, vcc\nMvav 204,164,108\nRa 156,3,56320\nMvav 204,160,100\nv_lshlrev_b32 v93, v92, v93\nMvav 204,176,36\nRa 160,3,57344\nv_and_b32 v92, 32, v1\n"
"Mvav 204,180,44\ns_add_u32 s4, s92, 131072\nv_cmp_eq_u32 vcc, 0, v92\nv_mov_b32 v4, 0\nv_mov_b32 v5, 0\nv_cndmask_b32 v6, 0, v93, vcc\nMvav 204,184,52\nRa 164,3,58368\n"
"Mvav 204,188,60\nWL 6\nbuffer_load_dwordx4 v[216:219], v1, s[16:19], s4 offen offset:0 sc0 sc1\nMvav 200,188,56\nRa 168,3,59392\nv_sub_u32 v7, v93, v6\nMvav 200,184,48\n"
"D1 1,16,46,2048\nMvav 200,180,40\nRa 172,3,60416\nMvav 200,176,32\nbuffer_load_dwordx4 v[220:223], v1, s[16:19], s4 offen offset:1024 sc0 sc1\nZvav 160,128,96\nRa 176,3,61440\n"
"v_mov_b32 v8, 0\nMvav 160,132,104\nv_mov_b32 v9, 0\nCV 196,196,197\nMvav 160,136,112\nRa 180,3,62464\nCV 197,198,199\nMvav 160,140,120\nWL 6\nCV 198,204,205\nZvav 164,140,124\n"
"Ra 184,3,63488\nCV 199,206,207\nMvav 164,136,116\nD1 1,16,46,3072\ns_add_u32 s5, s94, 655360\nMvav 164,132,108\nRa 188,3,64512\ns_barrier\nMvav 164,128,100\nSv 196,1,16,5,1024\n"
"CV 192,192,193\nMvav 164,144,36\nRa 128,0,0\nCV 193,194,195\nMvav 164,148,44\nCV 194,200,201\nCV 195,202,203\nMvav 164,152,52\nRa 132,0,1024\nSv 192,1,16,5,0\nMvav 164,156,60\n"
"WL 6\ns_add_u32 m0, s89, 32768\ns_add_u32 s46, s96, 1343488\nMvav 160,156,56\nRa 136,0,2048\nMvav 160,152,48\nD1 1,16,46,0\nMvav 160,148,40\nRa 140,0,3072\nMvav 160,144,32\n"
"Zvav 168,160,96\nRa 144,0,4096\nWV 12\nv_max_u32 v21, v211, v215\nv_cmp_eq_u32 vcc, -1, v21\ns_nop 0\ns_cbranch_vccnz .S222\n.S223:\nMvav 168,164,104\n"
"v_lshlrev_b32 v176, 16, v208\nMvav 168,168,112\nRa 148,0,5120\nMvav 168,172,120\nWL 6\nv_and_b32 v177, 0xffff0000, v208\nZvav 172,172,124\nRa 152,0,6144\nMvav 172,168,116\n"
"D1 1,16,46,1024\nMvav 172,164,108\nRa 156,0,7168\nv_lshlrev_b32 v178, 16, v209\nMvav 172,160,100\nv_and_b32 v179, 0xffff0000, v209\nMvav 172,176,36\nRa 160,0,8192\n"
"v_lshlrev_b32 v180, 16, v212\nMvav 172,180,44\nv_and_b32 v181, 0xffff0000, v212\ns_add_u32 s4, s92, 262144\nMvav 172,184,52\nRa 164,0,9216\nMvav 172,188,60\nWL 6\n"
"v_lshlrev_b32 v182, 16, v213\nbuffer_load_dwordx4 v[224:227], v1, s[16:19], s4 offen offset:0 sc0 sc1\nMvav 168,188,56\nRa 168,0,10240\nMvav 168,184,48\nD1 1,16,46,2048\n"
"Mvav 168,180,40\nRa 172,0,11264\nv_and_b32 v183, 0xffff0000, v213\nMvav 168,176,32\nv_lshlrev_b32 v184, 16, v210\n"
"buffer_load_dwordx4 v[228:231], v1, s[16:19], s4 offen offset:1024 sc0 sc1\nMvav 176,128,96\nRa 176,0,12288\nv_and_b32 v185, 0xffff0000, v210\nMvav 176,132,104\n"
"v_lshlrev_b32 v186, 16, v211\nCV 164,164,165\nMvav 176,136,112\nRa 180,0,13312\nCV 165,166,167\nMvav 176,140,120\nWL 6\nCV 166,172,173\nMvav 180,140,124\nRa 184,0,14336\n"
"v_and_b32 v187, 0xffff0000, v211\nMvav 180,136,116\nD1 1,16,46,3072\nCV 167,174,175\nMvav 180,132,108\nRa 188,0,15360\ns_add_u32 s5, s94, 786432\nMvav 180,128,100\n"
"v_lshlrev_b32 v188, 16, v214\nSv 164,1,16,5,1024\nMvav 180,144,36\nRa 128,0,16384\nv_and_b32 v189, 0xffff0000, v214\nMvav 180,148,44\nv_lshlrev_b32 v190, 16, v215\nCV 160,160,161\n"
"Mvav 180,152,52\nRa 132,0,17408\nCV 161,162,163\nMvav 180,156,60\nWL 6\ns_add_u32 m0, s89, 49152\ns_add_u32 s46, s96, 1376256\nMvav 176,156,56\nRa 136,0,18432\n"
"v_and_b32 v191, 0xffff0000, v215\nMvav 176,152,48\nD1 1,16,46,0\nCV 162,168,169\nMvav 176,148,40\nRa 140,0,19456\nCV 163,170,171\nMvav 176,144,32\nSv 160,1,16,5,0\n"
"Mvav 184,160,96\nRa 144,0,20480\nWV 12\nv_max_u32 v21, v219, v223\nv_cmp_eq_u32 vcc, -1, v21\ns_nop 0\ns_cbranch_vccnz .S225\n.S226:\nMvav 184,164,104\n"
"v_lshlrev_b32 v192, 16, v216\nMvav 184,168,112\nRa 148,0,21504\nMvav 184,172,120\nWL 6\nv_and_b32 v193, 0xffff0000, v216\nMvav 188,172,124\nRa 152,0,22528\nMvav 188,168,116\n"
"D1 1,16,46,1024\nMvav 188,164,108\nRa 156,0,23552\nv_lshlrev_b32 v194, 16, v217\nMvav 188,160,100\nv_and_b32 v195, 0xffff0000, v217\nMvav 188,176,36\nRa 160,0,24576\n"
"v_lshlrev_b32 v196, 16, v220\nMvav 188,180,44\nv_and_b32 v197, 0xffff0000, v220\ns_add_u32 s4, s92, 393216\nMvav 188,184,52\nRa 164,0,25600\nMvav 188,188,60\nWL 6\n"
"v_lshlrev_b32 v198, 16, v221\nbuffer_load_dwordx4 v[208:211], v1, s[16:19], s4 offen offset:0 sc0 sc1\nMvav 184,188,56\nRa 168,0,26624\nMvav 184,184,48\nD1 1,16,46,2048\n"
"Mvav 184,180,40\nRa 172,0,27648\nv_and_b32 v199, 0xffff0000, v221\nMvav 184,176,32\nv_lshlrev_b32 v200, 16, v218\n"
"buffer_load_dwordx4 v[212:215], v1, s[16:19], s4 offen offset:1024 sc0 sc1\nMvav 192,128,96\nRa 176,0,28672\nv_and_b32 v201, 0xffff0000, v218\nMvav 192,132,104\n"
"v_lshlrev_b32 v202, 16, v219\nCV 180,180,181\nMvav 192,136,112\nRa 180,0,29696\nCV 181,182,183\nMvav 192,140,120\ns_waitcnt vmcnt(15) lgkmcnt(6)\nCV 182,188,189\nMvav 196,140,124\n"
"Ra 184,0,30720\nv_and_b32 v203, 0xffff0000, v219\nMvav 196,136,116\nD1 1,16,46,3072\nCV 183,190,191\nMvav 196,132,108\nRa 188,0,31744\ns_barrier\nMvav 196,128,100\n"
"v_lshlrev_b32 v204, 16, v222\ns_add_u32 s5, s94, 917504\nMvav 196,144,36\nRa 128,2,16384\nv_and_b32 v205, 0xffff0000, v222\nMvav 196,148,44\nv_lshlrev_b32 v206, 16, v223\n"
"Sv 180,1,16,5,1024\nMvav 196,152,52\nRa 132,2,17408\nCV 176,176,177\nMvav 196,156,60\nWL 6\ns_add_u32 m0, s89, 65536\ns_add_u32 s46, s96, 1409024\nMvav 192,156,56\nRa 136,2,18432\n"
"v_and_b32 v207, 0xffff0000, v223\nMvav 192,152,48\nD1 1,16,46,0\nCV 177,178,179\nMvav 192,148,40\nRa 140,2,19456\nCV 178,184,185\nMvav 192,144,32\nCV 179,186,187\nSv 176,1,16,5,0\n"
"Mvav 200,160,96\nRa 144,2,20480\nWV 12\nv_max_u32 v21, v227, v231\nv_cmp_eq_u32 vcc, -1, v21\ns_nop 0\ns_cbranch_vccnz .S228\n.S229:\nMvav 200,164,104\n"
"v_lshlrev_b32 v160, 16, v224\nMvav 200,168,112\nRa 148,2,21504\nMvav 200,172,120\nWL 6\nv_and_b32 v161, 0xffff0000, v224\nMvav 204,172,124\nRa 152,2,22528\nMvav 204,168,116\n"
"D1 1,16,46,1024\nMvav 204,164,108\nRa 156,2,23552\nv_lshlrev_b32 v162, 16, v225\nMvav 204,160,100\nv_and_b32 v163, 0xffff0000, v225\nMvav 204,176,36\nRa 160,2,24576\n"
"v_lshlrev_b32 v164, 16, v228\nMvav 204,180,44\nv_and_b32 v165, 0xffff0000, v228\ns_add_u32 s4, s92, 524288\nMvav 204,184,52\nRa 164,2,25600\nMvav 204,188,60\nWL 6\n"
"v_lshlrev_b32 v166, 16, v229\nbuffer_load_dwordx4 v[216:219], v1, s[16:19], s4 offen offset:0 sc0 sc1\nMvav 200,188,56\nRa 168,2,26624\nMvav 200,184,48\nD1 1,16,46,2048\n"
"Mvav 200,180,40\nRa 172,2,27648\nv_and_b32 v167, 0xffff0000, v229\nMvav 200,176,32\nv_lshlrev_b32 v168, 16, v226\n"
"buffer_load_dwordx4 v[220:223], v1, s[16:19], s4 offen offset:1024 sc0 sc1\nMvav 160,128,96\nRa 176,2,28672\nv_and_b32 v169, 0xffff0000, v226\nMvav 160,132,104\n"
"v_lshlrev_b32 v170, 16, v227\nCV 196,196,197\nMvav 160,136,112\nRa 180,2,29696\nCV 197,198,199\nMvav 160,140,120\nWL 6\nCV 198,204,205\nMvav 164,140,124\nRa 184,2,30720\n"
"v_and_b32 v24, 0x70, v1\nMvav 164,136,116\nD1 1,16,46,3072\nCV 199,206,207\nMvav 164,132,108\nRa 188,2,31744\nv_and_b32 v171, 0xffff0000, v227\nMvav 164,128,100\n"
"v_lshlrev_b32 v172, 16, v230\ns_add_u32 s5, s94, 1048576\nMvav 164,144,36\nRa 128,2,0\nv_lshlrev_b32 v24, 5, v24\nMvav 164,148,44\nv_and_b32 v173, 0xffff0000, v230\n"
"Sv 196,1,16,5,1024\nMvav 164,152,52\nRa 132,2,1024\nCV 192,192,193\nMvav 164,156,60\nWL 6\nv_lshlrev_b32 v174, 16, v231\nMvav 160,156,56\nRa 136,2,2048\n"
"buffer_load_dword v27, v24, s[16:19], s41 offen sc0 sc1\nMvav 160,152,48\nv_and_b32 v175, 0xffff0000, v231\nCV 193,194,195\nMvav 160,148,40\nRa 140,2,3072\nCV 194,200,201\n"
"Mvav 160,144,32\nCV 195,202,203\nSv 192,1,16,5,0\nMvav 168,160,96\nRa 144,2,4096\nWV 12\nv_max_u32 v21, v211, v215\nv_cmp_eq_u32 vcc, -1, v21\ns_nop 0\ns_cbranch_vccnz .S231\n"
".S232:\nMvav 168,164,104\nv_lshlrev_b32 v176, 16, v208\nMvav 168,168,112\nRa 148,2,5120\nMvav 168,172,120\nWL 6\nMvav 172,172,124\nRa 152,2,6144\nv_and_b32 v177, 0xffff0000, v208\n"
".W561:\ns_cbranch_execz .WB151\nMvav 172,168,116\nv_lshlrev_b32 v178, 16, v209\nMvav 172,164,108\nRa 156,2,7168\nMvav 172,160,100\nv_and_b32 v179, 0xffff0000, v209\n"
"Mvav 172,176,36\nRa 160,2,8192\nv_lshlrev_b32 v180, 16, v212\nMvav 172,180,44\nv_and_b32 v181, 0xffff0000, v212\ns_add_u32 s4, s92, 655360\nMvav 172,184,52\nRa 164,2,9216\n"
"Mvav 172,188,60\nWL 6\nbuffer_load_dwordx4 v[224:227], v1, s[16:19], s4 offen offset:0 sc0 sc1\nMvav 168,188,56\nRa 168,2,10240\nv_lshlrev_b32 v182, 16, v213\nMvav 168,184,48\n"
"v_and_b32 v183, 0xffff0000, v213\nMvav 168,180,40\nRa 172,2,11264\nMvav 168,176,32\nv_lshlrev_b32 v184, 16, v210\n"
"buffer_load_dwordx4 v[228:231], v1, s[16:19], s4 offen offset:1024 sc0 sc1\nMvav 176,128,96\nRa 176,2,12288\nv_and_b32 v185, 0xffff0000, v210\nMvav 176,132,104\n"
"v_lshlrev_b32 v186, 16, v211\nCV 164,164,165\nMvav 176,136,112\nRa 180,2,13312\nCV 165,166,167\nMvav 176,140,120\ns_waitcnt vmcnt(13) lgkmcnt(6)\nCV 166,172,173\nMvav 180,140,124\n"
"Ra 184,2,14336\nCV 167,174,175\nMvav 180,136,116\nv_and_b32 v187, 0xffff0000, v211\ns_add_u32 s5, s94, 1179648\nMvav 180,132,108\nRa 188,2,15360\ns_barrier\nMvav 180,128,100\n"
"v_lshlrev_b32 v188, 16, v214\nSv 164,1,16,5,1024\nMvav 180,144,36\nRa 128,2,32768\nv_and_b32 v189, 0xffff0000, v214\nMvav 180,148,44\nv_lshlrev_b32 v190, 16, v215\nCV 160,160,161\n"
"Mvav 180,152,52\nRa 132,2,33792\nCV 161,162,163\nMvav 180,156,60\nWL 6\nCV 162,168,169\nMvav 176,156,56\nRa 136,2,34816\nCV 163,170,171\nMvav 176,152,48\n"
"v_and_b32 v191, 0xffff0000, v215\nSv 160,1,16,5,0\nMvav 176,148,40\nRa 140,2,35840\nMvav 176,144,32\nMvav 184,160,96\nRa 144,2,36864\nWV 8\nv_max_u32 v21, v219, v223\n"
"v_cmp_eq_u32 vcc, -1, v21\ns_nop 0\ns_cbranch_vccnz .S234\n.S235:\nMvav 184,164,104\nv_lshlrev_b32 v192, 16, v216\nMvav 184,168,112\nRa 148,2,37888\nMvav 184,172,120\nWL 6\n"
"Mvav 188,172,124\nRa 152,2,38912\nv_and_b32 v193, 0xffff0000, v216\nMvav 188,168,116\nv_lshlrev_b32 v194, 16, v217\nMvav 188,164,108\nRa 156,2,39936\nMvav 188,160,100\n"
"v_and_b32 v195, 0xffff0000, v217\nMvav 188,176,36\nRa 160,2,40960\nv_lshlrev_b32 v196, 16, v220\nMvav 188,180,44\nv_and_b32 v197, 0xffff0000, v220\ns_add_u32 s4, s92, 786432\n"
"Mvav 188,184,52\nRa 164,2,41984\nMvav 188,188,60\nWL 6\nbuffer_load_dwordx4 v[208:211], v1, s[16:19], s4 offen offset:0 sc0 sc1\nMvav 184,188,56\nRa 168,2,43008\n"
"v_lshlrev_b32 v198, 16, v221\nMvav 184,184,48\nv_and_b32 v199, 0xffff0000, v221\nMvav 184,180,40\nRa 172,2,44032\nMvav 184,176,32\nv_lshlrev_b32 v200, 16, v218\n"
"buffer_load_dwordx4 v[212:215], v1, s[16:19], s4 offen offset:1024 sc0 sc1\nMvav 192,128,96\nRa 176,2,45056\nv_and_b32 v201, 0xffff0000, v218\nMvav 192,132,104\n"
"v_lshlrev_b32 v202, 16, v219\nCV 180,180,181\nMvav 192,136,112\nRa 180,2,46080\nCV 181,182,183\nMvav 192,140,120\nWL 6\nCV 182,188,189\nMvav 196,140,124\nRa 184,2,47104\n"
"CV 183,190,191\nMvav 196,136,116\nv_and_b32 v203, 0xffff0000, v219\ns_add_u32 s5, s94, 1310720\nMvav 196,132,108\nRa 188,2,48128\nSv 180,1,16,5,1024\nMvav 196,128,100\n"
"v_lshlrev_b32 v204, 16, v222\nCV 176,176,177\nMvav 196,144,36\nRa 128,2,49152\nv_and_b32 v205, 0xffff0000, v222\nMvav 196,148,44\nv_lshlrev_b32 v206, 16, v223\nCV 177,178,179\n"
"Mvav 196,152,52\nRa 132,2,50176\nCV 178,184,185\nMvav 196,156,60\nWL 6\nCV 179,186,187\nMvav 192,156,56\nRa 136,2,51200\nSv 176,1,16,5,0\nMvav 192,152,48\n"
"v_and_b32 v207, 0xffff0000, v223\nMvav 192,148,40\nRa 140,2,52224\nMvav 192,144,32\nMvav 200,160,96\nRa 144,2,53248\nWV 6\nv_max_u32 v21, v227, v231\nv_cmp_eq_u32 vcc, -1, v21\n"
"s_nop 0\ns_cbranch_vccnz .S237\n.S238:\nMvav 200,164,104\nv_lshlrev_b32 v160, 16, v224\nv_xor_b32 v27, s15, v27\nv_and_b32 v26, 0xffffdfff, v27\nv_cmp_ne_u32 s[64:65], 0, v27\n"
"v_cmp_ne_u32 vcc, 0, v26\ns_nop 0\ns_cbranch_vccnz .S249\n.S250:\nMvav 200,168,112\nRa 148,2,54272\ns_bitcmp1_b32 s64, 0\ns_cselect_b32 s2, 0x2000, 0\nMvav 200,172,120\nWL 6\n"
"Mvav 204,172,124\nRa 152,2,55296\nv_and_b32 v161, 0xffff0000, v224\nMvav 204,168,116\nv_lshlrev_b32 v162, 16, v225\nMvav 204,164,108\nRa 156,2,56320\ns_xor_b32 s2, s2, s20\n"
"Mvav 204,160,100\nv_and_b32 v163, 0xffff0000, v225\nMvav 204,176,36\nRa 160,2,57344\nv_lshlrev_b32 v164, 16, v228\nMvav 204,180,44\nv_and_b32 v165, 0xffff0000, v228\n"
"Mvav 204,184,52\nRa 164,2,58368\nMvav 204,188,60\nWL 6\ns_add_u32 s24, s2, 0x0\nMvav 200,188,56\nRa 168,2,59392\nv_lshlrev_b32 v166, 16, v229\nMvav 200,184,48\n"
"v_and_b32 v167, 0xffff0000, v229\ns_bitcmp1_b32 s64, 1\ns_cselect_b32 s2, 0x2000, 0\nMvav 200,180,40\nRa 172,2,60416\nMvav 200,176,32\nv_lshlrev_b32 v168, 16, v226\n"
"Mvav 160,128,96\nRa 176,2,61440\nv_and_b32 v169, 0xffff0000, v226\nMvav 160,132,104\nv_lshlrev_b32 v170, 16, v227\nCV 196,196,197\nMvav 160,136,112\nRa 180,2,62464\n"
"CV 197,198,199\nMvav 160,140,120\nWL 6\nCV 198,204,205\nMvav 164,140,124\nRa 184,2,63488\nCV 199,206,207\nMvav 164,136,116\nv_and_b32 v171, 0xffff0000, v227\n"
"s_add_u32 s5, s94, 1441792\nMvav 164,132,108\nRa 188,2,64512\ns_barrier\nMvav 164,128,100\nv_lshlrev_b32 v172, 16, v230\nSv 196,1,16,5,1024\nMvav 164,144,36\nRa 128,3,0\n"
"v_and_b32 v173, 0xffff0000, v230\nMvav 164,148,44\nv_lshlrev_b32 v174, 16, v231\nCV 192,192,193\nMvav 164,152,52\nRa 132,3,1024\nCV 193,194,195\nMvav 164,156,60\nWL 6\n"
"s_xor_b32 s2, s2, s20\nMvav 160,156,56\nRa 136,3,2048\nCV 194,200,201\nMvav 160,152,48\nv_and_b32 v175, 0xffff0000, v231\nCV 195,202,203\nMvav 160,148,40\nRa 140,3,3072\n"
"Sv 192,1,16,5,0\nMvav 160,144,32\ns_add_u32 s25, s2, 0x1800000\ns_bitcmp1_b32 s64, 2\ns_cselect_b32 s2, 0x2000, 0\nMvav 168,160,96\nRa 144,3,4096\nWV 4\nv_max_u32 v21, v211, v215\n"
"v_cmp_eq_u32 vcc, -1, v21\ns_nop 0\ns_cbranch_vccnz .S240\n.S241:\nMvav 168,164,104\nv_lshlrev_b32 v176, 16, v208\nMvav 168,168,112\nRa 148,3,5120\nMvav 168,172,120\nWL 6\n"
"Mvav 172,172,124\nRa 152,3,6144\nv_and_b32 v177, 0xffff0000, v208\nMvav 172,168,116\nv_lshlrev_b32 v178, 16, v209\nMvav 172,164,108\nRa 156,3,7168\ns_xor_b32 s2, s2, s20\n"
"Mvav 172,160,100\nv_and_b32 v179, 0xffff0000, v209\nMvav 172,176,36\nRa 160,3,8192\nv_lshlrev_b32 v180, 16, v212\nMvav 172,180,44\nv_and_b32 v181, 0xffff0000, v212\n"
"Mvav 172,184,52\nRa 164,3,9216\nMvav 172,188,60\nWL 6\ns_add_u32 s26, s2, 0x3000000\nMvav 168,188,56\nRa 168,3,10240\nv_lshlrev_b32 v182, 16, v213\nMvav 168,184,48\n"
"v_and_b32 v183, 0xffff0000, v213\ns_bitcmp1_b32 s64, 3\ns_cselect_b32 s2, 0x2000, 0\nMvav 168,180,40\nRa 172,3,11264\nMvav 168,176,32\nv_lshlrev_b32 v184, 16, v210\n"
"Mvav 176,128,96\nRa 176,3,12288\ns_xor_b32 s2, s2, s20\nMvav 176,132,104\nv_and_b32 v185, 0xffff0000, v210\nCV 164,164,165\nMvav 176,136,112\nRa 180,3,13312\nCV 165,166,167\n"
"Mvav 176,140,120\nWL 6\nCV 166,172,173\nMvav 180,140,124\nRa 184,3,14336\nCV 167,174,175\nMvav 180,136,116\nv_lshlrev_b32 v186, 16, v211\ns_add_u32 s5, s94, 1572864\n"
"Mvav 180,132,108\nRa 188,3,15360\ns_add_u32 s27, s2, 0x4800000\nMvav 180,128,100\nv_and_b32 v187, 0xffff0000, v211\ns_bitcmp1_b32 s64, 4\ns_cselect_b32 s2, 0x2000, 0\n"
"Mvav 180,144,36\nv_lshlrev_b32 v188, 16, v214\nSv 164,1,16,5,1024\nMvav 180,148,44\nv_and_b32 v189, 0xffff0000, v214\nCV 160,160,161\nMvav 180,152,52\n"
"v_lshlrev_b32 v190, 16, v215\nCV 161,162,163\nMvav 180,156,60\nWL 4\ns_xor_b32 s2, s2, s20\nMvav 176,156,56\nv_and_b32 v191, 0xffff0000, v215\nCV 162,168,169\nMvav 176,152,48\n"
"s_add_u32 s32, s2, 0x6000000\ns_bitcmp1_b32 s64, 5\ns_cselect_b32 s2, 0x2000, 0\nMvav 176,148,40\ns_xor_b32 s2, s2, s20\nCV 163,170,171\nMvav 176,144,32\n"
"s_add_u32 s33, s2, 0x7800000\ns_bitcmp1_b32 s64, 6\ns_cselect_b32 s2, 0x2000, 0\nMvav 184,160,96\ns_xor_b32 s2, s2, s20\ns_add_u32 s34, s2, 0x9000000\nMvav 184,164,104\n"
"s_bitcmp1_b32 s64, 7\ns_cselect_b32 s2, 0x2000, 0\nMvav 184,168,112\ns_xor_b32 s2, s2, s20\nSv 160,1,16,5,0\nMvav 184,172,120\nWL 0\nMvav 188,172,124\n"
"s_add_u32 s35, s2, 0xa800000\nMvav 188,168,116\nMvav 188,164,108\nMvav 188,160,100\nMvav 188,176,36\nMvav 188,180,44\nMvav 188,184,52\nMvav 188,188,60\nMvav 184,188,56\n"
"Mvav 184,184,48\nMvav 184,180,40\nMvav 184,176,32\nCV 180,180,181\nCV 181,182,183\ns_nop 1\nCV 182,188,189\nCV 183,190,191\ns_add_u32 s5, s94, 1703936\nSv 180,1,16,5,1024\n"
"CV 176,176,177\nCV 177,178,179\nCV 178,184,185\nCV 179,186,187\nSv 176,1,16,5,0\ns_cmp_eq_u32 s13, 0\ns_cbranch_scc1 .Z252\n"
"buffer_load_dwordx4 v[12:15], v91, s[28:31], s22 offen offset:0\nbuffer_load_dwordx4 v[16:19], v91, s[28:31], s23 offen offset:0\n"
"buffer_load_dwordx4 v[64:67], v89, s[16:19], s40 offen offset:0 sc0 sc1\nbuffer_load_dwordx4 v[68:71], v89, s[16:19], s40 offen offset:16 sc0 sc1\n"
"buffer_load_dwordx4 v[72:75], v89, s[16:19], s40 offen offset:1792 sc0 sc1\nbuffer_load_dwordx4 v[76:79], v89, s[16:19], s40 offen offset:1808 sc0 sc1\n"
"buffer_load_dwordx2 v[80:81], v88, s[16:19], s63 offen offset:0 sc0 sc1\nbuffer_load_dwordx2 v[82:83], v88, s[16:19], s63 offen offset:512 sc0 sc1\n"
"buffer_load_dwordx4 v[96:99], v1, s[16:19], s24 offen offset:0 sc0 sc1\nbuffer_load_dwordx4 v[100:103], v1, s[16:19], s24 offen offset:1024 sc0 sc1\n"
"buffer_load_dwordx4 v[104:107], v1, s[16:19], s25 offen offset:0 sc0 sc1\nbuffer_load_dwordx4 v[108:111], v1, s[16:19], s25 offen offset:1024 sc0 sc1\n"
"buffer_load_dwordx4 v[112:115], v1, s[16:19], s26 offen offset:0 sc0 sc1\nbuffer_load_dwordx4 v[116:119], v1, s[16:19], s26 offen offset:1024 sc0 sc1\n"
"buffer_load_dwordx4 v[120:123], v1, s[16:19], s27 offen offset:0 sc0 sc1\nbuffer_load_dwordx4 v[124:127], v1, s[16:19], s27 offen offset:1024 sc0 sc1\n"
"buffer_load_dwordx4 v[32:35], v1, s[16:19], s32 offen offset:0 sc0 sc1\nbuffer_load_dwordx4 v[36:39], v1, s[16:19], s32 offen offset:1024 sc0 sc1\n"
"buffer_load_dwordx4 v[40:43], v1, s[16:19], s33 offen offset:0 sc0 sc1\nbuffer_load_dwordx4 v[44:47], v1, s[16:19], s33 offen offset:1024 sc0 sc1\n"
"buffer_load_dwordx4 v[48:51], v1, s[16:19], s34 offen offset:0 sc0 sc1\nbuffer_load_dwordx4 v[52:55], v1, s[16:19], s34 offen offset:1024 sc0 sc1\n"
"buffer_load_dwordx4 v[56:59], v1, s[16:19], s35 offen offset:0 sc0 sc1\nbuffer_load_dwordx4 v[60:63], v1, s[16:19], s35 offen offset:1024 sc0 sc1\nWV 16\nv_xor_b32 v66, s15, v66\n"
"v_xor_b32 v70, s15, v70\nv_xor_b32 v74, s15, v74\nv_xor_b32 v78, s15, v78\nv_xor_b32 v81, s15, v81\nv_xor_b32 v83, s15, v83\nv_or3_b32 v92, v66, v70, v74\n"
"v_or3_b32 v92, v92, v78, v81\nv_or_b32 v92, v92, v83\nv_cmp_ne_u32 vcc, 0, v92\ns_nop 0\ns_cbranch_vccnz .S243\n.S244:\nv_and_b32 v92, 0x7ff, v67\nv_lshrrev_b32 v93, 16, v67\n"
"v_cmp_eq_u32 s[44:45], v92, v90\nv_cmp_eq_u32 s[46:47], v93, v90\nv_cmp_eq_u32 s[48:49], v92, v94\nv_cmp_eq_u32 s[50:51], v93, v94\nv_cndmask_b32 v12, v12, v64, s[44:45]\n"
"v_cndmask_b32 v13, v13, v65, s[44:45]\nv_cndmask_b32 v14, v14, v68, s[44:45]\nv_cndmask_b32 v15, v15, v69, s[44:45]\nv_cndmask_b32 v12, v12, v72, s[46:47]\n"
"v_cndmask_b32 v13, v13, v73, s[46:47]\nv_cndmask_b32 v14, v14, v76, s[46:47]\nv_cndmask_b32 v15, v15, v77, s[46:47]\nv_cndmask_b32 v16, v16, v64, s[48:49]\n"
"v_cndmask_b32 v17, v17, v65, s[48:49]\nv_cndmask_b32 v18, v18, v68, s[48:49]\nv_cndmask_b32 v19, v19, v69, s[48:49]\nv_cndmask_b32 v16, v16, v72, s[50:51]\n"
"v_cndmask_b32 v17, v17, v73, s[50:51]\nv_cndmask_b32 v18, v18, v76, s[50:51]\nv_cndmask_b32 v19, v19, v77, s[50:51]\nv_sub_f32 v92, 1.0, v80\nv_sub_f32 v93, 1.0, v82\nCV 92,92,93\n"
"v_and_b32 v93, 0xffff0000, v92\nv_lshlrev_b32 v92, 16, v92\nv_lshlrev_b32 v64, 16, v12\nv_and_b32 v65, 0xffff0000, v12\nMU 64,64,92\nMU 65,65,92\nv_lshlrev_b32 v66, 16, v13\n"
"v_and_b32 v67, 0xffff0000, v13\nMU 66,66,92\nMU 67,67,92\nv_lshlrev_b32 v68, 16, v14\nv_and_b32 v69, 0xffff0000, v14\nMU 68,68,92\nMU 69,69,92\nv_lshlrev_b32 v70, 16, v15\n"
"v_and_b32 v71, 0xffff0000, v15\nMU 70,70,92\nMU 71,71,92\nv_lshlrev_b32 v72, 16, v16\nv_and_b32 v73, 0xffff0000, v16\nMU 72,72,93\nMU 73,73,93\nv_lshlrev_b32 v74, 16, v17\n"
"v_and_b32 v75, 0xffff0000, v17\nMU 74,74,93\nMU 75,75,93\nv_lshlrev_b32 v76, 16, v18\nv_and_b32 v77, 0xffff0000, v18\nMU 76,76,93\nMU 77,77,93\nv_lshlrev_b32 v78, 16, v19\n"
"v_and_b32 v79, 0xffff0000, v19\nMU 78,78,93\nMU 79,79,93\ns_mov_b32 s7, 262144\nWV 0\n.Z255:\nv_max_u32 v84, v99, v103\nv_cmp_eq_u32 s[44:45], -1, v84\nv_max_u32 v84, v107, v111\n"
"v_cmp_eq_u32 s[46:47], -1, v84\nv_max_u32 v84, v115, v119\nv_cmp_eq_u32 s[48:49], -1, v84\nv_max_u32 v84, v123, v127\nv_cmp_eq_u32 s[50:51], -1, v84\nv_max_u32 v84, v35, v39\n"
"v_cmp_eq_u32 s[52:53], -1, v84\nv_max_u32 v84, v43, v47\nv_cmp_eq_u32 s[54:55], -1, v84\nv_max_u32 v84, v51, v55\nv_cmp_eq_u32 s[56:57], -1, v84\nv_max_u32 v84, v59, v63\n"
"v_cmp_eq_u32 s[58:59], -1, v84\ns_or_b64 s[0:1], s[44:45], s[46:47]\ns_or_b64 s[2:3], s[48:49], s[50:51]\ns_or_b64 s[0:1], s[0:1], s[52:53]\ns_or_b64 s[2:3], s[2:3], s[54:55]\n"
"s_or_b64 s[0:1], s[0:1], s[56:57]\ns_or_b64 s[2:3], s[2:3], s[58:59]\ns_or_b64 s[0:1], s[0:1], s[2:3]\ns_cbranch_scc1 .Z253\n.Z254:\nZvvv 136,6,96\nMvvv 136,6,104\nMvvv 136,6,112\n"
"Mvvv 136,6,120\nMvvv 136,6,32\nMvvv 136,6,40\nMvvv 136,6,48\nMvvv 136,6,56\nZvvv 140,4,96\nMvvv 140,4,104\nMvvv 140,4,112\nMvvv 140,4,120\nMvvv 140,4,32\nMvvv 140,4,40\n"
"Mvvv 140,4,48\nMvvv 140,4,56\nZvvv 144,6,100\nMvvv 144,6,108\nMvvv 144,6,116\nMvvv 144,6,124\nMvvv 144,6,36\nMvvv 144,6,44\nMvvv 144,6,52\nMvvv 144,6,60\nZvvv 148,4,100\n"
"Mvvv 148,4,108\nMvvv 148,4,116\nMvvv 148,4,124\nMvvv 148,4,36\nMvvv 148,4,44\nMvvv 148,4,52\nMvvv 148,4,60\nFM 64,80,136\nFM 65,80,137\nFM 66,80,138\nFM 67,80,139\nFM 68,80,140\n"
"FM 69,80,141\nFM 70,80,142\nFM 71,80,143\nCV 12,64,65\nCV 13,66,67\nCV 14,68,69\nCV 15,70,71\nSv 12,91,36,22,0\nFM 72,82,144\nFM 73,82,145\nFM 74,82,146\nFM 75,82,147\n"
"FM 76,82,148\nFM 77,82,149\nFM 78,82,150\nFM 79,82,151\nCV 16,72,73\nCV 17,74,75\nCV 18,76,77\nCV 19,78,79\nSv 16,91,36,23,0\n.Z252:\ns_endpgm\n.EP264:\n.S144:\n"
"s_mov_b32 s7, 262144\n.S146:\nbuffer_load_dwordx4 v[32:35], v1, s[16:19], s6 offen offset:0 sc0 sc1\nbuffer_load_dwordx4 v[36:39], v1, s[16:19], s6 offen offset:1024 sc0 sc1\n"
"buffer_load_dwordx4 v[40:43], v1, s[16:19], s6 offen offset:2048 sc0 sc1\nbuffer_load_dwordx4 v[44:47], v1, s[16:19], s6 offen offset:3072 sc0 sc1\nWV 0\nv_xor_b32 v48, s15, v33\n"
"v_xor_b32 v49, s15, v35\nv_xor_b32 v50, s15, v37\nv_xor_b32 v51, s15, v39\nv_xor_b32 v52, s15, v41\nv_xor_b32 v53, s15, v43\nv_xor_b32 v54, s15, v45\nv_xor_b32 v55, s15, v47\n"
"v_or3_b32 v48, v48, v49, v50\nv_or3_b32 v51, v51, v52, v53\nv_or3_b32 v48, v48, v54, v55\nv_or_b32 v48, v48, v51\nv_cmp_ne_u32 vcc, 0, v48\ns_nop 0\ns_cbranch_vccz .S145\n"
"s_sleep 6\ns_sub_u32 s7, s7, 1\ns_cbranch_scc0 .S146\ns_branch .S145\n.S147:\ns_mov_b32 s7, 262144\n.S149:\ns_lshl_b32 s5, s73, 9\ns_add_u32 s5, s5, 0xc049400\n"
"v_lshrrev_b32 v27, 1, v1\nbuffer_load_dwordx2 v[22:23], v27, s[16:19], s5 offen sc0 sc1\nWV 0\nv_cmp_ne_u32 vcc, s15, v23\ns_nop 0\ns_cbranch_vccz .S148\ns_sleep 6\n"
"s_sub_u32 s7, s7, 1\ns_cbranch_scc0 .S149\ns_branch .S148\n.T153:\ns_mov_b32 s62, 1\ns_call_b64 s[80:81], .RA10\ns_branch .T154\n.T155:\ns_mov_b32 s62, 1\n"
"s_call_b64 s[80:81], .RA10\ns_branch .T156\n.T158:\ns_mov_b32 s62, 0\ns_call_b64 s[80:81], .RB11\ns_branch .T159\n.T160:\ns_mov_b32 s62, 0\ns_call_b64 s[80:81], .RA10\n"
"s_branch .T161\n.T163:\ns_mov_b32 s62, 0\ns_call_b64 s[80:81], .RB11\ns_branch .T164\n.T165:\ns_mov_b32 s62, 0\ns_call_b64 s[80:81], .RA10\ns_branch .T166\n.S168:\n"
"s_mov_b32 s7, 262144\n.S170:\nv_and_b32 v27, 0x70, v1\nbuffer_load_dword v27, v27, s[16:19], s61 offen offset:128 sc0 sc1\nWV 0\nv_cmp_ne_u32 vcc, s15, v27\ns_nop 0\n"
"s_cbranch_vccz .S169\ns_sleep 6\ns_sub_u32 s7, s7, 1\ns_cbranch_scc0 .S170\ns_branch .S169\n.S171:\ns_mov_b32 s7, 262144\n.S173:\nv_and_b32 v27, 0x70, v1\n"
"buffer_load_dword v27, v27, s[16:19], s61 offen offset:136 sc0 sc1\nWV 0\nv_cmp_ne_u32 vcc, s15, v27\ns_nop 0\ns_cbranch_vccz .S172\ns_sleep 6\ns_sub_u32 s7, s7, 1\n"
"s_cbranch_scc0 .S173\ns_branch .S172\n.S174:\ns_mov_b32 s7, 262144\n.S176:\nv_and_b32 v27, 0x70, v1\nbuffer_load_dword v27, v27, s[16:19], s61 offen offset:140 sc0 sc1\nWV 0\n"
"v_cmp_ne_u32 vcc, s15, v27\ns_nop 0\ns_cbranch_vccz .S175\ns_sleep 6\ns_sub_u32 s7, s7, 1\ns_cbranch_scc0 .S176\ns_branch .S175\n.S177:\ns_mov_b32 s7, 262144\n.S179:\n"
"v_and_b32 v27, 0x70, v1\nbuffer_load_dword v27, v27, s[16:19], s61 offen offset:12 sc0 sc1\nWV 0\nv_cmp_ne_u32 vcc, s15, v27\ns_nop 0\ns_cbranch_vccz .S178\ns_sleep 6\n"
"s_sub_u32 s7, s7, 1\ns_cbranch_scc0 .S179\ns_branch .S178\n.S180:\ns_mov_b32 s7, 262144\n.S182:\nv_and_b32 v27, 0x70, v1\n"
"buffer_load_dword v27, v27, s[16:19], s61 offen offset:8 sc0 sc1\nWV 0\nv_cmp_ne_u32 vcc, s15, v27\ns_nop 0\ns_cbranch_vccz .S181\ns_sleep 6\ns_sub_u32 s7, s7, 1\n"
"s_cbranch_scc0 .S182\ns_branch .S181\n.S183:\ns_mov_b32 s7, 262144\n.S185:\nv_and_b32 v27, 0xf0, v1\nbuffer_load_dword v27, v27, s[16:19], s61 offen offset:4 sc0 sc1\nWV 0\n"
"v_cmp_ne_u32 vcc, s15, v27\ns_nop 0\ns_cbranch_vccz .S184\ns_sleep 6\ns_sub_u32 s7, s7, 1\ns_cbranch_scc0 .S185\ns_branch .S184\n.S187:\ns_mov_b32 s7, 262144\n.S189:\n"
"v_lshrrev_b32 v104, 9, v1\nv_mul_u32_u24 v104, 0x18000, v104\nv_lshlrev_b32 v104, 8, v104\nv_and_b32 v105, 0x1f0, v1\nv_add_u32 v104, v104, v105\ns_mov_b32 s6, 0x15c0000\n"
"buffer_load_dword v96, v104, s[16:19], s6 offen offset:4 sc0 sc1\ns_mov_b32 s6, 0x15c2000\nbuffer_load_dword v97, v104, s[16:19], s6 offen offset:4 sc0 sc1\n"
"s_mov_b32 s6, 0x45c0000\nbuffer_load_dword v98, v104, s[16:19], s6 offen offset:4 sc0 sc1\ns_mov_b32 s6, 0x45c2000\n"
"buffer_load_dword v99, v104, s[16:19], s6 offen offset:4 sc0 sc1\ns_mov_b32 s6, 0x75c0000\nbuffer_load_dword v100, v104, s[16:19], s6 offen offset:4 sc0 sc1\n"
"s_mov_b32 s6, 0x75c2000\nbuffer_load_dword v101, v104, s[16:19], s6 offen offset:4 sc0 sc1\ns_mov_b32 s6, 0xa5c0000\n"
"buffer_load_dword v102, v104, s[16:19], s6 offen offset:4 sc0 sc1\ns_mov_b32 s6, 0xa5c2000\nbuffer_load_dword v103, v104, s[16:19], s6 offen offset:4 sc0 sc1\nWV 0\n"
"v_xor_b32 v96, s15, v96\nv_xor_b32 v97, s15, v97\nv_xor_b32 v98, s15, v98\nv_xor_b32 v99, s15, v99\nv_xor_b32 v100, s15, v100\nv_xor_b32 v101, s15, v101\n"
"v_xor_b32 v102, s15, v102\nv_xor_b32 v103, s15, v103\nv_min_u32 v96, v96, v97\nv_min_u32 v98, v98, v99\nv_min_u32 v100, v100, v101\nv_min_u32 v102, v102, v103\n"
"v_or3_b32 v96, v96, v98, v100\nv_or_b32 v96, v96, v102\nv_cmp_ne_u32 vcc, 0, v96\ns_nop 0\ns_cbranch_vccz .S188\ns_sleep 6\ns_sub_u32 s7, s7, 1\ns_cbranch_scc0 .S189\n"
"s_branch .S188\n.FF186:\nv_lshrrev_b32 v104, 9, v1\nv_mul_u32_u24 v104, 0x18000, v104\nv_lshlrev_b32 v104, 8, v104\nv_and_b32 v105, 0x1f0, v1\nv_add_u32 v104, v104, v105\n"
"s_mov_b32 s6, 0x15c0000\nbuffer_load_dword v96, v104, s[16:19], s6 offen offset:4 sc0 sc1\ns_mov_b32 s6, 0x15c2000\n"
"buffer_load_dword v97, v104, s[16:19], s6 offen offset:4 sc0 sc1\ns_mov_b32 s6, 0x45c0000\nbuffer_load_dword v98, v104, s[16:19], s6 offen offset:4 sc0 sc1\n"
"s_mov_b32 s6, 0x45c2000\nbuffer_load_dword v99, v104, s[16:19], s6 offen offset:4 sc0 sc1\ns_mov_b32 s6, 0x75c0000\n"
"buffer_load_dword v100, v104, s[16:19], s6 offen offset:4 sc0 sc1\ns_mov_b32 s6, 0x75c2000\nbuffer_load_dword v101, v104, s[16:19], s6 offen offset:4 sc0 sc1\n"
"s_mov_b32 s6, 0xa5c0000\nbuffer_load_dword v102, v104, s[16:19], s6 offen offset:4 sc0 sc1\ns_mov_b32 s6, 0xa5c2000\n"
"buffer_load_dword v103, v104, s[16:19], s6 offen offset:4 sc0 sc1\ns_mov_b32 s90, 0\ns_swappc_b64 s[8:9], s[80:81]\nv_xor_b32 v96, s15, v96\nv_xor_b32 v97, s15, v97\n"
"v_xor_b32 v98, s15, v98\nv_xor_b32 v99, s15, v99\nv_xor_b32 v100, s15, v100\nv_xor_b32 v101, s15, v101\nv_xor_b32 v102, s15, v102\nv_xor_b32 v103, s15, v103\n"
"v_min_u32 v96, v96, v97\nv_min_u32 v98, v98, v99\nv_min_u32 v100, v100, v101\nv_min_u32 v102, v102, v103\nv_or3_b32 v96, v96, v98, v100\nv_or_b32 v96, v96, v102\n"
"v_cmp_ne_u32 vcc, 0, v96\ns_nop 0\ns_cbranch_vccnz .S187\n.S188:\ns_and_b32 s4, s13, 1\ns_lshl1_add_u32 s4, s4, s11\ns_lshl2_add_u32 s4, s12, s4\ns_lshr_b32 s5, s71, 16\n"
"s_cmp_lt_u32 s4, 28\ns_cselect_b32 s5, s71, s5\ns_cselect_b32 s3, 0, 28\ns_and_b32 s5, s5, 0x7ff\ns_sub_u32 s3, s4, s3\ns_mul_i32 s2, s4, 7168\ns_add_u32 s2, s2, 0xc04c800\n"
"s_add_u32 s6, s2, 4096\nbuffer_load_dwordx4 v[96:99], v1, s[16:19], s2 offen offset:0 sc0 sc1\nbuffer_load_dwordx4 v[100:103], v1, s[16:19], s2 offen offset:1024 sc0 sc1\n"
"buffer_load_dwordx4 v[104:107], v1, s[16:19], s2 offen offset:2048 sc0 sc1\nbuffer_load_dwordx4 v[108:111], v1, s[16:19], s2 offen offset:3072 sc0 sc1\n"
"buffer_load_dwordx4 v[112:115], v1, s[16:19], s6 offen offset:0 sc0 sc1\nbuffer_load_dwordx4 v[116:119], v1, s[16:19], s6 offen offset:1024 sc0 sc1\n"
"buffer_load_dwordx4 v[120:123], v1, s[16:19], s6 offen offset:2048 sc0 sc1\nv_lshrrev_b32 v127, 1, v1\nv_and_b32 v127, 24, v127\ns_mul_i32 s2, s5, 896\ns_lshl_b32 s3, s3, 5\n"
"s_add_u32 s2, s2, s3\ns_lshl2_add_u32 s5, s5, 0xc03e000\nbuffer_load_dwordx2 v[124:125], v127, s[28:31], s2 offen\nbuffer_load_dword v126, off, s[16:19], s5 sc0 sc1\n"
"s_mov_b32 s90, 0\ns_swappc_b64 s[8:9], s[80:81]\nAD 96,96,100\nAD 97,97,101\nAD 98,98,102\nAD 99,99,103\nAD 104,104,108\nAD 105,105,109\nAD 106,106,110\nAD 107,107,111\n"
"AD 112,112,116\nAD 113,113,117\nAD 114,114,118\nAD 115,115,119\nAD 96,96,104\nAD 97,97,105\nAD 98,98,106\nAD 99,99,107\nAD 112,112,120\nAD 113,113,121\nAD 114,114,122\n"
"AD 115,115,123\nAD 96,96,112\nAD 97,97,113\nAD 98,98,114\nAD 99,99,115\nv_mov_b32 v100, v96\nv_mov_b32 v101, v97\nv_mov_b32 v102, v98\nv_mov_b32 v103, v99\n"
"v_permlane32_swap_b32 v96, v100\nv_permlane32_swap_b32 v97, v101\nv_permlane32_swap_b32 v98, v102\nv_permlane32_swap_b32 v99, v103\nAD 96,96,100\nAD 97,97,101\nAD 98,98,102\n"
"AD 99,99,103\nv_mov_b32 v100, v96\nv_mov_b32 v101, v97\nv_mov_b32 v102, v98\nv_mov_b32 v103, v99\nv_permlane16_swap_b32 v96, v100\nv_permlane16_swap_b32 v97, v101\n"
"v_permlane16_swap_b32 v98, v102\nv_permlane16_swap_b32 v99, v103\nAD 96,96,100\nAD 97,97,101\nAD 98,98,102\nAD 99,99,103\n"
"v_add_f32_dpp v96, v96, v96 row_ror:8 row_mask:0xf bank_mask:0xf\nv_add_f32_dpp v97, v97, v97 row_ror:8 row_mask:0xf bank_mask:0xf\n"
"v_add_f32_dpp v98, v98, v98 row_ror:8 row_mask:0xf bank_mask:0xf\nv_add_f32_dpp v99, v99, v99 row_ror:8 row_mask:0xf bank_mask:0xf\n"
"v_add_f32_dpp v96, v96, v96 row_ror:4 row_mask:0xf bank_mask:0xf\nv_add_f32_dpp v97, v97, v97 row_ror:4 row_mask:0xf bank_mask:0xf\n"
"v_add_f32_dpp v98, v98, v98 row_ror:4 row_mask:0xf bank_mask:0xf\nv_add_f32_dpp v99, v99, v99 row_ror:4 row_mask:0xf bank_mask:0xf\nv_mul_f32 v126, 0xbfb8aa3b, v126\nEX 126,126\n"
"CV 96,96,97\nv_add_f32 v126, 1.0, v126\nv_rcp_f32 v126, v126\nCV 98,98,99\nCV 126,126,126\nv_lshlrev_b32 v126, 16, v126\nv_and_b32 v97, 0xffff0000, v96\n"
"v_lshlrev_b32 v96, 16, v96\nv_and_b32 v99, 0xffff0000, v98\nv_lshlrev_b32 v98, 16, v98\nMU 96,96,126\nMU 97,97,126\nMU 98,98,126\nMU 99,99,126\nCV 96,96,97\nCV 98,98,99\n"
"v_and_b32 v97, 0xffff0000, v96\nv_lshlrev_b32 v96, 16, v96\nv_and_b32 v99, 0xffff0000, v98\nv_lshlrev_b32 v98, 16, v98\nv_lshlrev_b32 v100, 16, v124\n"
"v_and_b32 v101, 0xffff0000, v124\nv_lshlrev_b32 v102, 16, v125\nv_and_b32 v103, 0xffff0000, v125\nAD 96,96,100\nAD 97,97,101\nAD 98,98,102\nAD 99,99,103\nCV 96,96,97\nCV 97,98,99\n"
"v_mov_b32 v98, s15\nv_mov_b32 v99, s71\ns_and_b32 s4, s13, 1\ns_lshl1_add_u32 s4, s4, s11\ns_lshl2_add_u32 s4, s12, s4\ns_lshr_b32 s5, s71, 16\ns_cmp_lt_u32 s4, 28\n"
"s_cselect_b32 s5, s71, s5\ns_cselect_b32 s3, 0, 28\ns_and_b32 s5, s5, 0x7ff\ns_sub_u32 s3, s4, s3\nv_mov_b32 v100, 0x40000000\nv_cmp_gt_u32 vcc, 64, v1\ns_lshl_b32 s4, s4, 6\n"
"s_add_u32 s4, s4, 0xc04b800\nv_cndmask_b32 v100, v100, v1, vcc\nSv 96,100,16,4,0\ns_mov_b32 s90, 100\ns_swappc_b64 s[8:9], s[80:81]\n.T194:\ns_mov_b32 s62, 0\n"
"s_call_b64 s[80:81], .RB11\ns_branch .T195\n.T196:\ns_mov_b32 s62, 0\ns_call_b64 s[80:81], .RA10\ns_branch .T197\n.T201:\ns_mov_b32 s62, 0\ns_call_b64 s[80:81], .RB11\n"
"s_branch .T202\n.T203:\ns_mov_b32 s62, 0\ns_call_b64 s[80:81], .RA10\ns_branch .T204\n.T207:\ns_mov_b32 s62, 0\ns_call_b64 s[80:81], .RB11\ns_branch .T208\n.T209:\n"
"s_mov_b32 s62, 0\ns_call_b64 s[80:81], .RA10\ns_branch .T210\n.S215:\ns_mov_b32 s7, 262144\n.S217:\nbuffer_load_dwordx4 v[100:103], v108, s[16:19], s65 offen offset:0 sc0 sc1\n"
"buffer_load_dwordx4 v[104:107], v108, s[16:19], s65 offen offset:256 sc0 sc1\nWV 0\nv_xor_b32 v109, s15, v102\nv_xor_b32 v110, s15, v106\nv_or_b32 v109, v109, v110\n"
"v_cmp_ne_u32 vcc, 0, v109\ns_nop 0\ns_cbranch_vccz .S216\ns_sleep 6\ns_sub_u32 s7, s7, 1\ns_cbranch_scc0 .S217\ns_branch .S216\n.D213:\nv_mov_b32 v114, 0\nv_mov_b32 v115, 0\n"
"s_mov_b32 s63, 0xff800000\ns_mov_b32 s67, 1\ns_branch .D214\n.T219:\ns_lshl3_add_u32 s62, s67, 2\ns_call_b64 s[80:81], .RB11\ns_cmp_eq_u32 s67, 0\ns_cbranch_scc1 .T220\n"
"SU 20,100,88\nv_min_f32 v20, 0, v20\nEX 20,20\ns_nop 0\nFM 86,101,20\nSU 21,104,89\nv_min_f32 v21, 0, v21\nEX 21,21\ns_nop 0\nFM 87,105,21\nAR 22,0\n"
"v_and_b32 v23, 0xffff0000, v22\nv_lshlrev_b32 v22, 16, v22\nMU 22,22,20\nMU 23,23,20\nCV 22,22,23\nAW 0,22\nAR 22,1\nv_and_b32 v23, 0xffff0000, v22\nv_lshlrev_b32 v22, 16, v22\n"
"MU 22,22,20\nMU 23,23,20\nCV 22,22,23\nAW 1,22\nAR 22,2\nv_and_b32 v23, 0xffff0000, v22\nv_lshlrev_b32 v22, 16, v22\nMU 22,22,20\nMU 23,23,20\nCV 22,22,23\nAW 2,22\nAR 22,3\n"
"v_and_b32 v23, 0xffff0000, v22\nv_lshlrev_b32 v22, 16, v22\nMU 22,22,20\nMU 23,23,20\nCV 22,22,23\nAW 3,22\nAR 22,4\nv_and_b32 v23, 0xffff0000, v22\nv_lshlrev_b32 v22, 16, v22\n"
"MU 22,22,21\nMU 23,23,21\nCV 22,22,23\nAW 4,22\nAR 22,5\nv_and_b32 v23, 0xffff0000, v22\nv_lshlrev_b32 v22, 16, v22\nMU 22,22,21\nMU 23,23,21\nCV 22,22,23\nAW 5,22\nAR 22,6\n"
"v_and_b32 v23, 0xffff0000, v22\nv_lshlrev_b32 v22, 16, v22\nMU 22,22,21\nMU 23,23,21\nCV 22,22,23\nAW 6,22\nAR 22,7\nv_and_b32 v23, 0xffff0000, v22\nv_lshlrev_b32 v22, 16, v22\n"
"MU 22,22,21\nMU 23,23,21\nCV 22,22,23\nAW 7,22\nAR 22,8\nv_and_b32 v23, 0xffff0000, v22\nv_lshlrev_b32 v22, 16, v22\nMU 22,22,20\nMU 23,23,20\nCV 22,22,23\nAW 8,22\nAR 22,9\n"
"v_and_b32 v23, 0xffff0000, v22\nv_lshlrev_b32 v22, 16, v22\nMU 22,22,20\nMU 23,23,20\nCV 22,22,23\nAW 9,22\nAR 22,10\nv_and_b32 v23, 0xffff0000, v22\nv_lshlrev_b32 v22, 16, v22\n"
"MU 22,22,20\nMU 23,23,20\nCV 22,22,23\nAW 10,22\nAR 22,11\nv_and_b32 v23, 0xffff0000, v22\nv_lshlrev_b32 v22, 16, v22\nMU 22,22,20\nMU 23,23,20\nCV 22,22,23\nAW 11,22\nAR 22,12\n"
"v_and_b32 v23, 0xffff0000, v22\nv_lshlrev_b32 v22, 16, v22\nMU 22,22,21\nMU 23,23,21\nCV 22,22,23\nAW 12,22\nAR 22,13\nv_and_b32 v23, 0xffff0000, v22\nv_lshlrev_b32 v22, 16, v22\n"
"MU 22,22,21\nMU 23,23,21\nCV 22,22,23\nAW 13,22\nAR 22,14\nv_and_b32 v23, 0xffff0000, v22\nv_lshlrev_b32 v22, 16, v22\nMU 22,22,21\nMU 23,23,21\nCV 22,22,23\nAW 14,22\nAR 22,15\n"
"v_and_b32 v23, 0xffff0000, v22\nv_lshlrev_b32 v22, 16, v22\nMU 22,22,21\nMU 23,23,21\nCV 22,22,23\nAW 15,22\nAR 22,16\nv_and_b32 v23, 0xffff0000, v22\nv_lshlrev_b32 v22, 16, v22\n"
"MU 22,22,20\nMU 23,23,20\nCV 22,22,23\nAW 16,22\nAR 22,17\nv_and_b32 v23, 0xffff0000, v22\nv_lshlrev_b32 v22, 16, v22\nMU 22,22,20\nMU 23,23,20\nCV 22,22,23\nAW 17,22\nAR 22,18\n"
"v_and_b32 v23, 0xffff0000, v22\nv_lshlrev_b32 v22, 16, v22\nMU 22,22,20\nMU 23,23,20\nCV 22,22,23\nAW 18,22\nAR 22,19\nv_and_b32 v23, 0xffff0000, v22\nv_lshlrev_b32 v22, 16, v22\n"
"MU 22,22,20\nMU 23,23,20\nCV 22,22,23\nAW 19,22\nAR 22,20\nv_and_b32 v23, 0xffff0000, v22\nv_lshlrev_b32 v22, 16, v22\nMU 22,22,21\nMU 23,23,21\nCV 22,22,23\nAW 20,22\nAR 22,21\n"
"v_and_b32 v23, 0xffff0000, v22\nv_lshlrev_b32 v22, 16, v22\nMU 22,22,21\nMU 23,23,21\nCV 22,22,23\nAW 21,22\nAR 22,22\nv_and_b32 v23, 0xffff0000, v22\nv_lshlrev_b32 v22, 16, v22\n"
"MU 22,22,21\nMU 23,23,21\nCV 22,22,23\nAW 22,22\nAR 22,23\nv_and_b32 v23, 0xffff0000, v22\nv_lshlrev_b32 v22, 16, v22\nMU 22,22,21\nMU 23,23,21\nCV 22,22,23\nAW 23,22\nAR 22,24\n"
"v_and_b32 v23, 0xffff0000, v22\nv_lshlrev_b32 v22, 16, v22\nMU 22,22,20\nMU 23,23,20\nCV 22,22,23\nAW 24,22\nAR 22,25\nv_and_b32 v23, 0xffff0000, v22\nv_lshlrev_b32 v22, 16, v22\n"
"MU 22,22,20\nMU 23,23,20\nCV 22,22,23\nAW 25,22\nAR 22,26\nv_and_b32 v23, 0xffff0000, v22\nv_lshlrev_b32 v22, 16, v22\nMU 22,22,20\nMU 23,23,20\nCV 22,22,23\nAW 26,22\nAR 22,27\n"
"v_and_b32 v23, 0xffff0000, v22\nv_lshlrev_b32 v22, 16, v22\nMU 22,22,20\nMU 23,23,20\nCV 22,22,23\nAW 27,22\nAR 22,28\nv_and_b32 v23, 0xffff0000, v22\nv_lshlrev_b32 v22, 16, v22\n"
"MU 22,22,21\nMU 23,23,21\nCV 22,22,23\nAW 28,22\nAR 22,29\nv_and_b32 v23, 0xffff0000, v22\nv_lshlrev_b32 v22, 16, v22\nMU 22,22,21\nMU 23,23,21\nCV 22,22,23\nAW 29,22\nAR 22,30\n"
"v_and_b32 v23, 0xffff0000, v22\nv_lshlrev_b32 v22, 16, v22\nMU 22,22,21\nMU 23,23,21\nCV 22,22,23\nAW 30,22\nAR 22,31\nv_and_b32 v23, 0xffff0000, v22\nv_lshlrev_b32 v22, 16, v22\n"
"MU 22,22,21\nMU 23,23,21\nCV 22,22,23\nAW 31,22\nAR 22,32\nv_and_b32 v23, 0xffff0000, v22\nv_lshlrev_b32 v22, 16, v22\nMU 22,22,20\nMU 23,23,20\nCV 22,22,23\nAW 32,22\nAR 22,33\n"
"v_and_b32 v23, 0xffff0000, v22\nv_lshlrev_b32 v22, 16, v22\nMU 22,22,20\nMU 23,23,20\nCV 22,22,23\nAW 33,22\nAR 22,34\nv_and_b32 v23, 0xffff0000, v22\nv_lshlrev_b32 v22, 16, v22\n"
"MU 22,22,20\nMU 23,23,20\nCV 22,22,23\nAW 34,22\nAR 22,35\nv_and_b32 v23, 0xffff0000, v22\nv_lshlrev_b32 v22, 16, v22\nMU 22,22,20\nMU 23,23,20\nCV 22,22,23\nAW 35,22\nAR 22,36\n"
"v_and_b32 v23, 0xffff0000, v22\nv_lshlrev_b32 v22, 16, v22\nMU 22,22,21\nMU 23,23,21\nCV 22,22,23\nAW 36,22\nAR 22,37\nv_and_b32 v23, 0xffff0000, v22\nv_lshlrev_b32 v22, 16, v22\n"
"MU 22,22,21\nMU 23,23,21\nCV 22,22,23\nAW 37,22\nAR 22,38\nv_and_b32 v23, 0xffff0000, v22\nv_lshlrev_b32 v22, 16, v22\nMU 22,22,21\nMU 23,23,21\nCV 22,22,23\nAW 38,22\nAR 22,39\n"
"v_and_b32 v23, 0xffff0000, v22\nv_lshlrev_b32 v22, 16, v22\nMU 22,22,21\nMU 23,23,21\nCV 22,22,23\nAW 39,22\nAR 22,40\nv_and_b32 v23, 0xffff0000, v22\nv_lshlrev_b32 v22, 16, v22\n"
"MU 22,22,20\nMU 23,23,20\nCV 22,22,23\nAW 40,22\nAR 22,41\nv_and_b32 v23, 0xffff0000, v22\nv_lshlrev_b32 v22, 16, v22\nMU 22,22,20\nMU 23,23,20\nCV 22,22,23\nAW 41,22\nAR 22,42\n"
"v_and_b32 v23, 0xffff0000, v22\nv_lshlrev_b32 v22, 16, v22\nMU 22,22,20\nMU 23,23,20\nCV 22,22,23\nAW 42,22\nAR 22,43\nv_and_b32 v23, 0xffff0000, v22\nv_lshlrev_b32 v22, 16, v22\n"
"MU 22,22,20\nMU 23,23,20\nCV 22,22,23\nAW 43,22\nAR 22,44\nv_and_b32 v23, 0xffff0000, v22\nv_lshlrev_b32 v22, 16, v22\nMU 22,22,21\nMU 23,23,21\nCV 22,22,23\nAW 44,22\nAR 22,45\n"
"v_and_b32 v23, 0xffff0000, v22\nv_lshlrev_b32 v22, 16, v22\nMU 22,22,21\nMU 23,23,21\nCV 22,22,23\nAW 45,22\nAR 22,46\nv_and_b32 v23, 0xffff0000, v22\nv_lshlrev_b32 v22, 16, v22\n"
"MU 22,22,21\nMU 23,23,21\nCV 22,22,23\nAW 46,22\nAR 22,47\nv_and_b32 v23, 0xffff0000, v22\nv_lshlrev_b32 v22, 16, v22\nMU 22,22,21\nMU 23,23,21\nCV 22,22,23\nAW 47,22\nAR 22,48\n"
"v_and_b32 v23, 0xffff0000, v22\nv_lshlrev_b32 v22, 16, v22\nMU 22,22,20\nMU 23,23,20\nCV 22,22,23\nAW 48,22\nAR 22,49\nv_and_b32 v23, 0xffff0000, v22\nv_lshlrev_b32 v22, 16, v22\n"
"MU 22,22,20\nMU 23,23,20\nCV 22,22,23\nAW 49,22\nAR 22,50\nv_and_b32 v23, 0xffff0000, v22\nv_lshlrev_b32 v22, 16, v22\nMU 22,22,20\nMU 23,23,20\nCV 22,22,23\nAW 50,22\nAR 22,51\n"
"v_and_b32 v23, 0xffff0000, v22\nv_lshlrev_b32 v22, 16, v22\nMU 22,22,20\nMU 23,23,20\nCV 22,22,23\nAW 51,22\nAR 22,52\nv_and_b32 v23, 0xffff0000, v22\nv_lshlrev_b32 v22, 16, v22\n"
"MU 22,22,21\nMU 23,23,21\nCV 22,22,23\nAW 52,22\nAR 22,53\nv_and_b32 v23, 0xffff0000, v22\nv_lshlrev_b32 v22, 16, v22\nMU 22,22,21\nMU 23,23,21\nCV 22,22,23\nAW 53,22\nAR 22,54\n"
"v_and_b32 v23, 0xffff0000, v22\nv_lshlrev_b32 v22, 16, v22\nMU 22,22,21\nMU 23,23,21\nCV 22,22,23\nAW 54,22\nAR 22,55\nv_and_b32 v23, 0xffff0000, v22\nv_lshlrev_b32 v22, 16, v22\n"
"MU 22,22,21\nMU 23,23,21\nCV 22,22,23\nAW 55,22\nAR 22,56\nv_and_b32 v23, 0xffff0000, v22\nv_lshlrev_b32 v22, 16, v22\nMU 22,22,20\nMU 23,23,20\nCV 22,22,23\nAW 56,22\nAR 22,57\n"
"v_and_b32 v23, 0xffff0000, v22\nv_lshlrev_b32 v22, 16, v22\nMU 22,22,20\nMU 23,23,20\nCV 22,22,23\nAW 57,22\nAR 22,58\nv_and_b32 v23, 0xffff0000, v22\nv_lshlrev_b32 v22, 16, v22\n"
"MU 22,22,20\nMU 23,23,20\nCV 22,22,23\nAW 58,22\nAR 22,59\nv_and_b32 v23, 0xffff0000, v22\nv_lshlrev_b32 v22, 16, v22\nMU 22,22,20\nMU 23,23,20\nCV 22,22,23\nAW 59,22\nAR 22,60\n"
"v_and_b32 v23, 0xffff0000, v22\nv_lshlrev_b32 v22, 16, v22\nMU 22,22,21\nMU 23,23,21\nCV 22,22,23\nAW 60,22\nAR 22,61\nv_and_b32 v23, 0xffff0000, v22\nv_lshlrev_b32 v22, 16, v22\n"
"MU 22,22,21\nMU 23,23,21\nCV 22,22,23\nAW 61,22\nAR 22,62\nv_and_b32 v23, 0xffff0000, v22\nv_lshlrev_b32 v22, 16, v22\nMU 22,22,21\nMU 23,23,21\nCV 22,22,23\nAW 62,22\nAR 22,63\n"
"v_and_b32 v23, 0xffff0000, v22\nv_lshlrev_b32 v22, 16, v22\nMU 22,22,21\nMU 23,23,21\nCV 22,22,23\nAW 63,22\nv_lshrrev_b32 v22, 8, v1\nv_bfe_u32 v23, v1, 6, 2\n"
"v_cmp_eq_u32 vcc, v22, v23\nv_and_b32 v22, 16, v1\nv_mov_b32 v23, 0x3f80\nv_cndmask_b32 v23, 0, v23, vcc\nv_lshlrev_b32 v23, v22, v23\nv_and_b32 v22, 32, v1\n"
"v_cmp_eq_u32 vcc, 0, v22\nv_mov_b32 v112, 0\nv_mov_b32 v113, 0\nv_cndmask_b32 v114, 0, v23, vcc\nv_sub_u32 v115, v23, v114\nv_mov_b32 v116, 0\nv_mov_b32 v117, 0\ns_nop 1\n"
"Mvva 128,114,0\nMvva 136,112,0\nMvva 140,112,4\nMvva 132,114,4\nMvva 144,114,8\nMvva 152,112,8\nMvva 156,112,12\nMvva 148,114,12\nMvva 160,114,16\nMvva 168,112,16\n"
"Mvva 172,112,20\nMvva 164,114,20\nMvva 176,114,24\nMvva 184,112,24\nMvva 188,112,28\nMvva 180,114,28\nMvva 192,114,32\nMvva 200,112,32\nMvva 204,112,36\nMvva 196,114,36\n"
"Mvva 208,114,40\nMvva 216,112,40\nMvva 220,112,44\nMvva 212,114,44\nMvva 224,114,48\nMvva 232,112,48\nMvva 236,112,52\nMvva 228,114,52\nMvva 240,114,56\nMvva 248,112,56\n"
"Mvva 252,112,60\nMvva 244,114,60\ns_nop 7\ns_branch .T220\n.S222:\ns_mov_b32 s7, 262144\n.S224:\ns_add_u32 s0, s92, 0\n"
"buffer_load_dwordx4 v[208:211], v1, s[16:19], s0 offen offset:0 sc0 sc1\nbuffer_load_dwordx4 v[212:215], v1, s[16:19], s0 offen offset:1024 sc0 sc1\nWV 0\n"
"v_max_u32 v21, v211, v215\nv_cmp_eq_u32 vcc, -1, v21\ns_nop 0\ns_cbranch_vccz .S223\ns_sleep 6\ns_sub_u32 s7, s7, 1\ns_cbranch_scc0 .S224\ns_branch .S223\n.S225:\n"
"s_mov_b32 s7, 262144\n.S227:\ns_add_u32 s0, s92, 131072\nbuffer_load_dwordx4 v[216:219], v1, s[16:19], s0 offen offset:0 sc0 sc1\n"
"buffer_load_dwordx4 v[220:223], v1, s[16:19], s0 offen offset:1024 sc0 sc1\nWV 0\nv_max_u32 v21, v219, v223\nv_cmp_eq_u32 vcc, -1, v21\ns_nop 0\ns_cbranch_vccz .S226\ns_sleep 6\n"
"s_sub_u32 s7, s7, 1\ns_cbranch_scc0 .S227\ns_branch .S226\n.S228:\ns_mov_b32 s7, 262144\n.S230:\ns_add_u32 s0, s92, 262144\n"
"buffer_load_dwordx4 v[224:227], v1, s[16:19], s0 offen offset:0 sc0 sc1\nbuffer_load_dwordx4 v[228:231], v1, s[16:19], s0 offen offset:1024 sc0 sc1\nWV 0\n"
"v_max_u32 v21, v227, v231\nv_cmp_eq_u32 vcc, -1, v21\ns_nop 0\ns_cbranch_vccz .S229\ns_sleep 6\ns_sub_u32 s7, s7, 1\ns_cbranch_scc0 .S230\ns_branch .S229\n.S231:\n"
"s_mov_b32 s7, 262144\n.S233:\ns_add_u32 s0, s92, 393216\nbuffer_load_dwordx4 v[208:211], v1, s[16:19], s0 offen offset:0 sc0 sc1\n"
"buffer_load_dwordx4 v[212:215], v1, s[16:19], s0 offen offset:1024 sc0 sc1\nWV 0\nv_max_u32 v21, v211, v215\nv_cmp_eq_u32 vcc, -1, v21\ns_nop 0\ns_cbranch_vccz .S232\ns_sleep 6\n"
"s_sub_u32 s7, s7, 1\ns_cbranch_scc0 .S233\ns_branch .S232\n.S234:\ns_mov_b32 s7, 262144\n.S236:\ns_add_u32 s0, s92, 524288\n"
"buffer_load_dwordx4 v[216:219], v1, s[16:19], s0 offen offset:0 sc0 sc1\nbuffer_load_dwordx4 v[220:223], v1, s[16:19], s0 offen offset:1024 sc0 sc1\nWV 0\n"
"v_max_u32 v21, v219, v223\nv_cmp_eq_u32 vcc, -1, v21\ns_nop 0\ns_cbranch_vccz .S235\ns_sleep 6\ns_sub_u32 s7, s7, 1\ns_cbranch_scc0 .S236\ns_branch .S235\n.S237:\n"
"s_mov_b32 s7, 262144\n.S239:\ns_add_u32 s0, s92, 655360\nbuffer_load_dwordx4 v[224:227], v1, s[16:19], s0 offen offset:0 sc0 sc1\n"
"buffer_load_dwordx4 v[228:231], v1, s[16:19], s0 offen offset:1024 sc0 sc1\nWV 0\nv_max_u32 v21, v227, v231\nv_cmp_eq_u32 vcc, -1, v21\ns_nop 0\ns_cbranch_vccz .S238\ns_sleep 6\n"
"s_sub_u32 s7, s7, 1\ns_cbranch_scc0 .S239\ns_branch .S238\n.S240:\ns_mov_b32 s7, 262144\n.S242:\ns_add_u32 s0, s92, 786432\n"
"buffer_load_dwordx4 v[208:211], v1, s[16:19], s0 offen offset:0 sc0 sc1\nbuffer_load_dwordx4 v[212:215], v1, s[16:19], s0 offen offset:1024 sc0 sc1\nWV 0\n"
"v_max_u32 v21, v211, v215\nv_cmp_eq_u32 vcc, -1, v21\ns_nop 0\ns_cbranch_vccz .S241\ns_sleep 6\ns_sub_u32 s7, s7, 1\ns_cbranch_scc0 .S242\ns_branch .S241\n.S243:\n"
"s_mov_b32 s7, 262144\n.S245:\nbuffer_load_dwordx4 v[64:67], v89, s[16:19], s40 offen offset:0 sc0 sc1\nbuffer_load_dwordx4 v[68:71], v89, s[16:19], s40 offen offset:16 sc0 sc1\n"
"buffer_load_dwordx4 v[72:75], v89, s[16:19], s40 offen offset:1792 sc0 sc1\nbuffer_load_dwordx4 v[76:79], v89, s[16:19], s40 offen offset:1808 sc0 sc1\n"
"buffer_load_dwordx2 v[80:81], v88, s[16:19], s63 offen offset:0 sc0 sc1\nbuffer_load_dwordx2 v[82:83], v88, s[16:19], s63 offen offset:512 sc0 sc1\nWV 0\nv_xor_b32 v66, s15, v66\n"
"v_xor_b32 v70, s15, v70\nv_xor_b32 v74, s15, v74\nv_xor_b32 v78, s15, v78\nv_xor_b32 v81, s15, v81\nv_xor_b32 v83, s15, v83\nv_or3_b32 v92, v66, v70, v74\n"
"v_or3_b32 v92, v92, v78, v81\nv_or_b32 v92, v92, v83\nv_cmp_ne_u32 vcc, 0, v92\ns_nop 0\ns_cbranch_vccz .S244\ns_sleep 6\ns_sub_u32 s7, s7, 1\ns_cbranch_scc0 .S245\n"
"s_branch .S244\n.S246:\ns_mov_b32 s7, 262144\n.S248:\nbuffer_load_dword v27, off, s[16:19], s60 offset:512 sc0 sc1\nWV 0\nv_cmp_ne_u32 vcc, s15, v27\ns_nop 0\n"
"s_cbranch_vccz .S247\ns_sleep 6\ns_sub_u32 s7, s7, 1\ns_cbranch_scc0 .S248\ns_branch .S247\n.S249:\ns_mov_b32 s7, 262144\n.S251:\n"
"buffer_load_dword v27, v24, s[16:19], s41 offen sc0 sc1\nWV 0\nv_xor_b32 v27, s15, v27\nv_and_b32 v26, 0xffffdfff, v27\nv_cmp_ne_u32 s[64:65], 0, v27\nv_cmp_ne_u32 vcc, 0, v26\n"
"s_nop 0\ns_cbranch_vccz .S250\ns_sleep 6\ns_sub_u32 s7, s7, 1\ns_cbranch_scc0 .S251\ns_branch .S250\n.Z253:\ns_sub_u32 s7, s7, 1\ns_cbranch_scc1 .Z254\ns_cmp_lg_u64 s[44:45], 0\n"
"s_cbranch_scc0 .Z256\nbuffer_load_dwordx4 v[96:99], v1, s[16:19], s24 offen offset:0 sc0 sc1\nbuffer_load_dwordx4 v[100:103], v1, s[16:19], s24 offen offset:1024 sc0 sc1\n.Z256:\n"
"s_cmp_lg_u64 s[46:47], 0\ns_cbranch_scc0 .Z257\nbuffer_load_dwordx4 v[104:107], v1, s[16:19], s25 offen offset:0 sc0 sc1\n"
"buffer_load_dwordx4 v[108:111], v1, s[16:19], s25 offen offset:1024 sc0 sc1\n.Z257:\ns_cmp_lg_u64 s[48:49], 0\ns_cbranch_scc0 .Z258\n"
"buffer_load_dwordx4 v[112:115], v1, s[16:19], s26 offen offset:0 sc0 sc1\nbuffer_load_dwordx4 v[116:119], v1, s[16:19], s26 offen offset:1024 sc0 sc1\n.Z258:\n"
"s_cmp_lg_u64 s[50:51], 0\ns_cbranch_scc0 .Z259\nbuffer_load_dwordx4 v[120:123], v1, s[16:19], s27 offen offset:0 sc0 sc1\n"
"buffer_load_dwordx4 v[124:127], v1, s[16:19], s27 offen offset:1024 sc0 sc1\n.Z259:\ns_cmp_lg_u64 s[52:53], 0\ns_cbranch_scc0 .Z260\n"
"buffer_load_dwordx4 v[32:35], v1, s[16:19], s32 offen offset:0 sc0 sc1\nbuffer_load_dwordx4 v[36:39], v1, s[16:19], s32 offen offset:1024 sc0 sc1\n.Z260:\n"
"s_cmp_lg_u64 s[54:55], 0\ns_cbranch_scc0 .Z261\nbuffer_load_dwordx4 v[40:43], v1, s[16:19], s33 offen offset:0 sc0 sc1\n"
"buffer_load_dwordx4 v[44:47], v1, s[16:19], s33 offen offset:1024 sc0 sc1\n.Z261:\ns_cmp_lg_u64 s[56:57], 0\ns_cbranch_scc0 .Z262\n"
"buffer_load_dwordx4 v[48:51], v1, s[16:19], s34 offen offset:0 sc0 sc1\nbuffer_load_dwordx4 v[52:55], v1, s[16:19], s34 offen offset:1024 sc0 sc1\n.Z262:\n"
"s_cmp_lg_u64 s[58:59], 0\ns_cbranch_scc0 .Z263\nbuffer_load_dwordx4 v[56:59], v1, s[16:19], s35 offen offset:0 sc0 sc1\n"
"buffer_load_dwordx4 v[60:63], v1, s[16:19], s35 offen offset:1024 sc0 sc1\n.Z263:\ns_sleep 8\nWV 0\ns_branch .Z255\n.p2align 12\n.PA4:\ns_mov_b64 exec, 0\ns_cmp_eq_u32 s11, 1\n"
"s_cbranch_scc1 .W563\ns_cmp_eq_u32 s11, 2\ns_cbranch_scc1 .W564\ns_cmp_eq_u32 s11, 3\ns_cbranch_scc1 .W565\ns_branch .W562\n.WB273:\ns_mov_b64 exec, -1\ns_add_u32 m0, s47, -49152\n"
"s_add_u32 s48, s48, 128\nD0 2,20,48,0\nD0 3,20,48,1024\nD0 4,20,48,2048\nD0 5,20,48,3072\ns_and_b32 s86, s74, 15\ns_lshl2_add_u32 s86, s86, s11\ns_mov_b32 s88, 0\n"
"s_lshl_b32 s93, s73, 9\ns_lshl3_add_u32 s93, s86, s93\ns_add_u32 s93, s93, 0xc049400\ns_lshl_b32 s86, s86, 14\ns_mul_i32 s91, s73, 25165824\ns_add_u32 s86, s86, s91\n"
"s_and_b32 s92, s13, 1\ns_lshl_b32 s92, s92, 13\ns_add_u32 s86, s86, s92\ns_add_u32 s94, s86, 0x1480000\ns_xor_b32 s95, s94, 0x2000\ns_add_u32 s86, s94, 512\n"
"s_load_dword s87, s[54:55], s86\ns_add_u32 m0, s47, -32768\ns_add_u32 s45, s45, 128\nD0 2,20,45,0\nD0 3,20,45,1024\nD0 4,20,45,2048\nD0 5,20,45,3072\ns_add_u32 m0, s47, 32768\n"
"s_add_u32 s44, s44, 0x40000\nD0 6,24,44,0\nD0 7,24,44,1024\nD0 8,24,44,2048\nD0 9,24,44,3072\ns_add_u32 m0, s47, 49152\ns_add_u32 s44, s44, 0x40000\nD0 6,24,44,0\nD0 7,24,44,1024\n"
"D0 8,24,44,2048\nD0 9,24,44,3072\ns_add_u32 m0, s47, -16384\ns_add_u32 s48, s48, 128\nD0 2,20,48,0\nD0 3,20,48,1024\nD0 4,20,48,2048\nD0 5,20,48,3072\nv_mov_b32 v52, 0x40000000\n"
"v_cmp_gt_u32 vcc, 56, v0\ns_lshl2_add_u32 s2, s10, s11\ns_mul_i32 s2, s2, 1792\nv_cndmask_b32 v53, v52, v1, vcc\ns_mov_b32 s36, s56\ns_and_b32 s37, s57, 0xffff\n"
"s_mov_b32 s38, 0x380\ns_mov_b32 s39, 0x20000\ns_mov_b32 s40, s58\ns_and_b32 s41, s59, 0xffff\ns_mov_b32 s42, 0x380\ns_mov_b32 s43, 0x20000\n"
"buffer_load_dwordx4 v[32:35], v53, s[28:31], s2 offen offset:0\nbuffer_load_dwordx4 v[36:39], v53, s[28:31], s2 offen offset:896\n"
"buffer_load_dwordx4 v[40:43], v53, s[36:39], 0 offen offset:0\nbuffer_load_dwordx4 v[44:47], v53, s[40:43], 0 offen offset:0\nWL 0\ns_mul_i32 s15, s15, 1664525\n"
"s_add_u32 s15, s15, 1013904223\nv_mov_b32 v25, s15\ns_mov_b32 s57, 0x3db8aa3b\ns_and_b32 s59, s11, 1\ns_lshl_b32 s59, s59, 13\ns_mul_i32 s2, s73, 25165824\ns_and_b32 s3, s12, 1\n"
"s_lshl_b32 s3, s3, 21\ns_add_u32 s2, s2, s3\ns_lshl2_add_u32 s3, s13, s11\ns_lshl_b32 s3, s3, 15\ns_add_u32 s2, s2, s3\ns_add_u32 s56, s2, 0x800000\ns_xor_b32 s53, s56, 0xc000\n"
"buffer_load_dword v31, v24, s[24:27], s96 offen\ns_add_u32 s96, s96, 0x80000\nbuffer_load_dword v31, v24, s[24:27], s96 offen\ns_add_u32 s96, s96, 0x80000\n"
"v_lshrrev_b32 v60, 3, v0\nv_and_b32 v61, 0x70, v1\nv_lshl_or_b32 v60, v60, 10, v61\nv_mov_b32 v20, 0\nv_mov_b32 v21, 0\nv_mov_b32 v22, 0\nv_mov_b32 v23, 0\nSv 20,60,16,94,0\n"
"Sv 20,60,16,95,0\nWV 28\ns_barrier\nRa 128,10,0\nRa 132,11,0\nRa 136,10,2048\nRa 140,11,2048\nRa 144,10,8192\nRa 148,11,8192\nRa 152,10,10240\nRa 156,11,10240\nTa 160,12,0\n"
"Ta 162,12,2048\nTa 164,12,16384\nTa 166,12,18432\nTa 168,13,0\nTa 170,13,2048\nTa 172,13,16384\nTa 174,13,18432\nTa 176,14,0\nTa 178,14,2048\nTa 180,14,16384\nTa 182,14,18432\n"
"Ta 184,15,0\nTa 186,15,2048\nTa 188,15,16384\nTa 190,15,18432\nWL 0\ns_barrier\nZvaa 64,160,128\nTa 224,16,0\nMvaa 64,164,132\nTa 226,16,2048\nZvaa 96,164,140\n"
"s_add_u32 m0, s47, -65536\ns_add_u32 s45, s45, 128\nMvaa 96,160,136\nTa 228,16,16384\nZvaa 128,160,144\nTa 230,16,18432\nMvaa 128,164,148\nD0 2,20,45,0\nZvaa 160,164,156\n"
"Ta 232,17,0\nMvaa 160,160,152\nTa 234,17,2048\nZvaa 164,168,152\ns_mov_b32 s36, s60\nMvaa 164,172,156\nTa 236,17,16384\nZvaa 132,172,148\nTa 238,17,18432\nMvaa 132,168,144\n"
"s_and_b32 s37, s61, 0xffff\nZvaa 100,168,136\nTa 240,18,0\nMvaa 100,172,140\nTa 242,18,2048\nZvaa 68,172,132\nD0 3,20,45,1024\nMvaa 68,168,128\nTa 244,18,16384\nZvaa 72,176,128\n"
"Ta 246,18,18432\nMvaa 72,180,132\ns_mov_b32 s38, 0x380000\nZvaa 104,180,140\nTa 248,19,0\nMvaa 104,176,136\nTa 250,19,2048\nZvaa 136,176,144\ns_mov_b32 s39, 0x20000\n"
"Mvaa 136,180,148\nTa 252,19,16384\nZvaa 168,180,156\nTa 254,19,18432\nMvaa 168,176,152\nD0 4,20,45,2048\nZvaa 172,184,152\ns_lshl3_add_u32 s75, s12, s13\nMvaa 172,188,156\n"
"s_lshl2_add_u32 s75, s75, s11\nZvaa 140,188,148\ns_mul_i32 s75, s75, 7168\nMvaa 140,184,144\ns_mul_i32 s76, s11, 7168\nZvaa 108,184,136\ns_add_u32 s76, s76, 131072\n"
"Mvaa 108,188,140\nD0 5,20,45,3072\nZvaa 76,188,132\ns_mov_b32 s42, 1605632\nMvaa 76,184,128\ns_waitcnt vmcnt(24) lgkmcnt(0)\ns_barrier\nZvaa 80,224,128\nRa 192,10,16384\n"
"Mvaa 80,228,132\ns_add_u32 m0, s47, 0\ns_add_u32 s44, s44, 0x40000\nZvaa 112,228,140\nRa 196,11,16384\nMvaa 112,224,136\nZvaa 144,224,144\nRa 200,10,18432\nMvaa 144,228,148\n"
"D0 6,24,44,0\nZvaa 176,228,156\nRa 204,11,18432\nMvaa 176,224,152\nZvaa 180,232,152\nRa 208,10,24576\nMvaa 180,236,156\ns_and_b32 s78, s74, 3\ns_lshl2_add_u32 s78, s73, s78\n"
"s_lshl_b32 s78, s78, 12\ns_bitcmp1_b32 s11, 0\ns_cselect_b32 s79, s48, s45\ns_add_u32 s79, s79, 512\ns_and_b32 s79, s79, 0xfffe0000\ns_add_u32 s78, s78, s79\n"
"buffer_load_dword v31, v26, s[20:23], s78 offen\nZvaa 148,236,148\nRa 212,11,24576\nMvaa 148,232,144\nZvaa 116,232,136\nRa 216,10,26624\nMvaa 116,236,140\nD0 7,24,44,1024\n"
"Zvaa 84,236,132\nRa 220,11,26624\nMvaa 84,232,128\nZvaa 88,240,128\nMvaa 88,244,132\nZvaa 120,244,140\nMvaa 120,240,136\nZvaa 152,240,144\nMvaa 152,244,148\nD0 8,24,44,2048\n"
"Zvaa 184,244,156\nMvaa 184,240,152\nZvaa 188,248,152\nMvaa 188,252,156\nZvaa 156,252,148\nMvaa 156,248,144\nZvaa 124,248,136\nMvaa 124,252,140\nD0 9,24,44,3072\nZvaa 92,252,132\n"
"Mvaa 92,248,128\nWL 0\nZaaa 16,224,192\nRa 128,10,32768\nMaaa 16,228,196\ns_add_u32 m0, s47, 16384\ns_add_u32 s44, s44, 0x40000\nZaaa 48,228,204\nRa 132,11,32768\nMaaa 48,224,200\n"
"Zaaa 80,224,208\nRa 136,10,34816\nMaaa 80,228,212\nD0 6,24,44,0\nZaaa 112,228,220\nRa 140,11,34816\nMaaa 112,224,216\nZaaa 116,232,216\nRa 144,10,40960\nMaaa 116,236,220\n"
"Zaaa 84,236,212\nRa 148,11,40960\nMaaa 84,232,208\nZaaa 52,232,200\nRa 152,10,43008\nMaaa 52,236,204\nD0 7,24,44,1024\nZaaa 20,236,196\nRa 156,11,43008\nMaaa 20,232,192\n"
"Zaaa 24,240,192\nMaaa 24,244,196\nZaaa 56,244,204\nMaaa 56,240,200\nZaaa 88,240,208\nMaaa 88,244,212\nD0 8,24,44,2048\nZaaa 120,244,220\nMaaa 120,240,216\nZaaa 124,248,216\n"
"Maaa 124,252,220\nZaaa 92,252,212\nMaaa 92,248,208\nZaaa 60,248,200\nMaaa 60,252,204\nD0 9,24,44,3072\nZaaa 28,252,196\nMaaa 28,248,192\ns_waitcnt vmcnt(25) lgkmcnt(0)\ns_barrier\n"
"Zaaa 0,160,192\nTa 224,16,32768\nWV 17\nMaaa 0,164,196\nTa 226,16,34816\nZaaa 32,164,204\ns_add_u32 m0, s47, -49152\ns_add_u32 s48, s48, 128\nMaaa 32,160,200\nTa 228,16,49152\n"
"Zaaa 64,160,208\nTa 230,16,51200\nbuffer_load_dword v31, v24, s[24:27], s96 offen\ns_add_u32 s96, s96, 0x80000\nMaaa 64,164,212\nD0 2,20,48,0\nv_lshlrev_b32 v48, 16, v32\n"
"Zaaa 96,164,220\nTa 232,17,32768\nMaaa 96,160,216\nTa 234,17,34816\nv_lshlrev_b32 v49, 16, v36\nZaaa 100,168,216\nv_lshlrev_b32 v50, 16, v40\nv_lshlrev_b32 v51, 16, v44\n"
"Maaa 100,172,220\nTa 236,17,49152\nZaaa 68,172,212\nTa 238,17,51200\nMU 52,48,50\nMaaa 68,168,208\nMU 53,49,50\nZaaa 36,168,200\nTa 240,18,32768\nCV 52,52,53\nMaaa 36,172,204\n"
"Ta 242,18,34816\nv_lshlrev_b32 v54, 16, v52\nZaaa 4,172,196\nD0 3,20,48,1024\nMaaa 4,168,192\nTa 244,18,49152\nv_and_b32 v55, 0xffff0000, v52\nZaaa 8,176,192\nTa 246,18,51200\n"
"MU 56,48,51\nMaaa 8,180,196\nMU 57,49,51\nZaaa 40,180,204\nTa 248,19,32768\nv_and_b32 v48, 0xffff0000, v32\nMaaa 40,176,200\nTa 250,19,34816\nZaaa 72,176,208\n"
"v_and_b32 v49, 0xffff0000, v36\nv_and_b32 v50, 0xffff0000, v40\nMaaa 72,180,212\nTa 252,19,49152\nv_and_b32 v51, 0xffff0000, v44\nZaaa 104,180,220\nTa 254,19,51200\n"
"Maaa 104,176,216\nD0 4,20,48,2048\nMU 52,48,50\nZaaa 108,184,216\nMU 53,49,50\nCV 52,52,53\nMaaa 108,188,220\nv_and_b32 v53, 0xffff0000, v52\nZaaa 76,188,212\n"
"v_lshlrev_b32 v52, 16, v52\nFM 56,48,51\nMaaa 76,184,208\nFM 57,49,51\nZaaa 44,184,200\nAD 54,54,52\nAD 55,55,53\nMaaa 44,188,204\nD0 5,20,48,3072\nv_lshlrev_b32 v48, 16, v33\n"
"Zaaa 12,188,196\nv_lshlrev_b32 v49, 16, v37\nMaaa 12,184,192\nWL 0\nMvaa 80,224,128\nTa 160,12,32768\nv_lshlrev_b32 v50, 16, v41\nMvaa 80,228,132\nTa 162,12,34816\n"
"Mvaa 112,228,140\ns_add_u32 m0, s47, -32768\ns_add_u32 s45, s45, 128\nMvaa 112,224,136\nTa 164,12,49152\nMvaa 144,224,144\nTa 166,12,51200\nv_lshlrev_b32 v51, 16, v45\n"
"Mvaa 144,228,148\nD0 2,20,45,0\nMU 52,48,50\nMvaa 176,228,156\nTa 168,13,32768\nMvaa 176,224,152\nTa 170,13,34816\nMU 53,49,50\nMvaa 180,232,152\nCV 52,52,53\n"
"v_and_b32 v53, 0xffff0000, v52\nMvaa 180,236,156\nTa 172,13,49152\nMvaa 148,236,148\nTa 174,13,51200\nv_lshlrev_b32 v52, 16, v52\nMvaa 148,232,144\nFM 56,48,51\nMvaa 116,232,136\n"
"Ta 176,14,32768\nFM 57,49,51\nMvaa 116,236,140\nTa 178,14,34816\nAD 54,54,52\nMvaa 84,236,132\nD0 3,20,45,1024\nMvaa 84,232,128\nTa 180,14,49152\nAD 55,55,53\nMvaa 88,240,128\n"
"Ta 182,14,51200\nv_and_b32 v48, 0xffff0000, v33\nMvaa 88,244,132\nv_and_b32 v49, 0xffff0000, v37\nMvaa 120,244,140\nTa 184,15,32768\nv_and_b32 v50, 0xffff0000, v41\n"
"Mvaa 120,240,136\nTa 186,15,34816\nMvaa 152,240,144\nv_and_b32 v51, 0xffff0000, v45\nMU 52,48,50\nMvaa 152,244,148\nTa 188,15,49152\nMU 53,49,50\nMvaa 184,244,156\n"
"Ta 190,15,51200\nMvaa 184,240,152\nD0 4,20,45,2048\nCV 52,52,53\nMvaa 188,248,152\nv_and_b32 v53, 0xffff0000, v52\nv_lshlrev_b32 v52, 16, v52\nMvaa 188,252,156\nFM 56,48,51\n"
"Mvaa 156,252,148\nFM 57,49,51\nAD 54,54,52\nMvaa 156,248,144\nAD 55,55,53\nMvaa 124,248,136\nv_lshlrev_b32 v48, 16, v34\nv_lshlrev_b32 v49, 16, v38\nMvaa 124,252,140\n"
"D0 5,20,45,3072\nv_lshlrev_b32 v50, 16, v42\nMvaa 92,252,132\nv_lshlrev_b32 v51, 16, v46\nMvaa 92,248,128\ns_waitcnt vmcnt(18) lgkmcnt(0)\ns_barrier\nMvaa 64,160,128\n"
"Ra 192,10,49152\nMU 52,48,50\nMvaa 64,164,132\ns_add_u32 m0, s47, 32768\ns_add_u32 s44, s44, 0x40000\nMvaa 96,164,140\nRa 196,11,49152\nMU 53,49,50\nMvaa 96,160,136\nCV 52,52,53\n"
"Mvaa 128,160,144\nRa 200,10,51200\nv_and_b32 v53, 0xffff0000, v52\nMvaa 128,164,148\nD0 6,24,44,0\nv_lshlrev_b32 v52, 16, v52\nMvaa 160,164,156\nRa 204,11,51200\nMvaa 160,160,152\n"
"FM 56,48,51\nFM 57,49,51\nMvaa 164,168,152\nRa 208,10,57344\nAD 54,54,52\nMvaa 164,172,156\nAD 55,55,53\nMvaa 132,172,148\nRa 212,11,57344\nv_and_b32 v48, 0xffff0000, v34\n"
"Mvaa 132,168,144\nv_and_b32 v49, 0xffff0000, v38\nMvaa 100,168,136\nRa 216,10,59392\nv_and_b32 v50, 0xffff0000, v42\nMvaa 100,172,140\nD0 7,24,44,1024\n"
"v_and_b32 v51, 0xffff0000, v46\nMvaa 68,172,132\nRa 220,11,59392\nMvaa 68,168,128\nMU 52,48,50\nMU 53,49,50\nMvaa 72,176,128\nCV 52,52,53\nv_and_b32 v53, 0xffff0000, v52\n"
"Mvaa 72,180,132\nv_lshlrev_b32 v52, 16, v52\nMvaa 104,180,140\nFM 56,48,51\nFM 57,49,51\nMvaa 104,176,136\nAD 54,54,52\nMvaa 136,176,144\nAD 55,55,53\nv_lshlrev_b32 v48, 16, v35\n"
"Mvaa 136,180,148\nD0 8,24,44,2048\nv_lshlrev_b32 v49, 16, v39\nMvaa 168,180,156\nv_lshlrev_b32 v50, 16, v43\nMvaa 168,176,152\nv_lshlrev_b32 v51, 16, v47\nMU 52,48,50\n"
"Mvaa 172,184,152\nMU 53,49,50\nCV 52,52,53\nMvaa 172,188,156\nv_and_b32 v53, 0xffff0000, v52\nMvaa 140,188,148\nv_lshlrev_b32 v52, 16, v52\nFM 56,48,51\nMvaa 140,184,144\n"
"FM 57,49,51\nMvaa 108,184,136\nAD 54,54,52\nAD 55,55,53\nMvaa 108,188,140\nD0 9,24,44,3072\nv_and_b32 v48, 0xffff0000, v35\nMvaa 76,188,132\nv_and_b32 v49, 0xffff0000, v39\n"
"Mvaa 76,184,128\nWL 0\nMaaa 0,160,192\nRa 128,10,0\nv_and_b32 v50, 0xffff0000, v43\nMaaa 0,164,196\ns_add_u32 m0, s47, 49152\ns_add_u32 s44, s44, 0x40000\nMaaa 32,164,204\n"
"Ra 132,11,0\nv_and_b32 v51, 0xffff0000, v47\nMaaa 32,160,200\nMU 52,48,50\nMaaa 64,160,208\nRa 136,10,2048\nMU 53,49,50\nMaaa 64,164,212\nD0 6,24,44,0\nCV 52,52,53\n"
"Maaa 96,164,220\nRa 140,11,2048\nMaaa 96,160,216\nv_and_b32 v53, 0xffff0000, v52\nv_lshlrev_b32 v52, 16, v52\nMaaa 100,168,216\nRa 144,10,8192\nFM 56,48,51\nMaaa 100,172,220\n"
"FM 57,49,51\n.W562:\ns_cbranch_execz .W566\nMaaa 68,172,212\nRa 148,11,8192\nAD 54,54,52\nMaaa 68,168,208\nAD 55,55,53\nMaaa 36,168,200\nRa 152,10,10240\n"
"v_permlane32_swap_b32 v54, v55\nMaaa 36,172,204\nD0 7,24,44,1024\nv_permlane32_swap_b32 v56, v57\nMaaa 4,172,196\nRa 156,11,10240\nMaaa 4,168,192\nAD 54,54,55\nAD 56,56,57\n"
"Maaa 8,176,192\nMaaa 8,180,196\nv_permlane16_swap_b32 v54, v56\nMaaa 40,180,204\nAD 54,54,56\nMaaa 40,176,200\nMaaa 72,176,208\n"
"v_add_f32_dpp v54, v54, v54 row_ror:8 row_mask:0xf bank_mask:0xf\nMaaa 72,180,212\nD0 8,24,44,2048\nMaaa 104,180,220\n"
"v_add_f32_dpp v54, v54, v54 row_ror:4 row_mask:0xf bank_mask:0xf\nMaaa 104,176,216\nMaaa 108,184,216\nv_add_f32_dpp v54, v54, v54 row_ror:2 row_mask:0xf bank_mask:0xf\n"
"Maaa 108,188,220\nMaaa 76,188,212\nv_add_f32_dpp v54, v54, v54 row_ror:1 row_mask:0xf bank_mask:0xf\nCV 54,54,54\nMaaa 76,184,208\nv_lshlrev_b32 v54, 16, v54\nMaaa 44,184,200\n"
"v_mul_f32 v55, 0xbfb8aa3b, v54\nMaaa 44,188,204\nD0 9,24,44,3072\nMaaa 12,188,196\nEX 55,55\nMaaa 12,184,192\ns_waitcnt vmcnt(12) lgkmcnt(0)\ns_barrier\nMaaa 16,224,192\n"
"Ta 160,12,0\nv_add_f32 v56, 0, v54\nMaaa 16,228,196\nTa 162,12,2048\nMaaa 48,228,204\ns_add_u32 m0, s47, -16384\ns_add_u32 s48, s48, 128\nMaaa 48,224,200\nTa 164,12,16384\n"
"Maaa 80,224,208\nTa 166,12,18432\nbuffer_load_dword v31, v24, s[24:27], s96 offen\ns_add_u32 s96, s96, 0x80000\nMaaa 80,228,212\nD0 2,20,48,0\nv_add_f32 v55, 1.0, v55\n"
"Maaa 112,228,220\nTa 168,13,0\nv_rcp_f32 v55, v55\nMaaa 112,224,216\nTa 170,13,2048\nv_ashrrev_i32 v57, 31, v56\nMaaa 116,232,216\nCV 55,55,55\nv_lshlrev_b32 v55, 16, v55\n"
"Maaa 116,236,220\nTa 172,13,16384\nMaaa 84,236,212\nTa 174,13,18432\nv_or_b32 v57, 0x80000000, v57\nMaaa 84,232,208\nv_xor_b32 v56, v56, v57\nMaaa 52,232,200\nTa 176,14,0\n"
"v_and_b32 v56, 0xffff0000, v56\nMaaa 52,236,204\nTa 178,14,2048\ns_lshl2_add_u32 s2, s10, s11\nMaaa 20,236,196\nD0 3,20,48,1024\nMaaa 20,232,192\nTa 180,14,16384\n"
"v_lshrrev_b32 v48, 5, v0\nMaaa 24,240,192\nTa 182,14,18432\nv_lshl_add_u32 v48, s2, 1, v48\nMaaa 24,244,196\nv_sub_u32 v49, 0xffff, v48\nMaaa 56,244,204\nTa 184,15,0\n"
"v_or_b32 v56, v56, v49\nMaaa 56,240,200\nTa 186,15,2048\nMaaa 88,240,208\nv_mov_b32 v57, s15\nv_and_b32 v49, 31, v0\nMaaa 88,244,212\nTa 188,15,16384\nv_mov_b32 v52, 0x40000000\n"
"Maaa 120,244,220\nTa 190,15,18432\nv_cmp_eq_u32 vcc, 16, v49\nv_lshlrev_b32 v50, 2, v48\nv_lshlrev_b32 v51, 10, v49\nv_cndmask_b32 v50, v52, v50, vcc\n"
"v_cndmask_b32 v56, v55, v56, vcc\nMaaa 120,240,216\nD0 4,20,48,2048\nv_lshl_add_u32 v51, v48, 3, v51\nMaaa 124,248,216\nv_and_b32 v49, 15, v0\nv_cmp_eq_u32 vcc, 0, v49\n"
"s_mov_b32 s2, 0xc040000\ns_mov_b32 s3, 0xc03e000\nv_cndmask_b32 v51, v52, v51, vcc\nbuffer_store_dwordx2 v[56:57], v51, s[16:19], s2 offen sc0 sc1\n"
"buffer_store_dword v54, v50, s[16:19], s3 offen sc0 sc1\nMaaa 124,252,220\nMaaa 92,252,212\nMaaa 92,248,208\nMaaa 60,248,200\nMaaa 60,252,204\nD0 5,20,48,3072\nMaaa 28,252,196\n"
"Maaa 28,248,192\nWL 0\ns_mov_b32 s50, 3\n.GL272:\nMvaa 64,160,128\nTa 224,16,0\nMvaa 64,164,132\nTa 226,16,2048\nMvaa 96,164,140\nTa 228,16,16384\ns_add_u32 m0, s47, -65536\n"
"s_add_u32 s45, s45, 128\nMvaa 96,160,136\nTa 230,16,18432\nMvaa 128,160,144\nTa 232,17,0\nMvaa 128,164,148\nTa 234,17,2048\nD0 2,20,45,0\nMvaa 160,164,156\nTa 236,17,16384\n"
"Mvaa 160,160,152\nTa 238,17,18432\nMvaa 164,168,152\nTa 240,18,0\nMvaa 164,172,156\nTa 242,18,2048\nMvaa 132,172,148\nTa 244,18,16384\nMvaa 132,168,144\nTa 246,18,18432\n"
"Mvaa 100,168,136\nTa 248,19,0\nMvaa 100,172,140\nTa 250,19,2048\nMvaa 68,172,132\nTa 252,19,16384\nD0 3,20,45,1024\nMvaa 68,168,128\nTa 254,19,18432\nMvaa 72,176,128\n"
"Ra 192,10,16384\nMvaa 72,180,132\nRa 196,11,16384\nMvaa 104,180,140\nRa 200,10,18432\nMvaa 104,176,136\nRa 204,11,18432\nMvaa 136,176,144\nRa 208,10,24576\nMvaa 136,180,148\n"
"Ra 212,11,24576\nMvaa 168,180,156\nRa 216,10,26624\nMvaa 168,176,152\nRa 220,11,26624\nD0 4,20,45,2048\nMvaa 172,184,152\nMvaa 172,188,156\nMvaa 140,188,148\nMvaa 140,184,144\n"
"Mvaa 108,184,136\nMvaa 108,188,140\nD0 5,20,45,3072\nMvaa 76,188,132\nMvaa 76,184,128\ns_waitcnt vmcnt(18) lgkmcnt(8)\ns_barrier\nMvaa 80,224,128\nMvaa 80,228,132\n"
"s_add_u32 m0, s47, 0\ns_add_u32 s44, s44, 0x40000\ns_mov_b32 s86, 1\ns_load_dword s86, s[54:55], s94\nMvaa 112,228,140\ns_add_u32 s94, s94, 1024\nMvaa 112,224,136\n"
"Mvaa 144,224,144\nMvaa 144,228,148\nD0 6,24,44,0\nMvaa 176,228,156\nMvaa 176,224,152\nMvaa 180,232,152\nMvaa 180,236,156\nMvaa 148,236,148\nMvaa 148,232,144\n"
"s_add_u32 s88, s88, s86\nMvaa 116,232,136\nMvaa 116,236,140\nD0 7,24,44,1024\ns_add_u32 s88, s88, s86\nMvaa 84,236,132\nMvaa 84,232,128\ns_add_u32 s88, s88, s86\nMvaa 88,240,128\n"
"Mvaa 88,244,132\ns_add_u32 s88, s88, s86\nMvaa 120,244,140\nMvaa 120,240,136\ns_add_u32 s88, s88, s86\nMvaa 152,240,144\nMvaa 152,244,148\nD0 8,24,44,2048\n"
"s_add_u32 s88, s88, s86\nMvaa 184,244,156\nMvaa 184,240,152\ns_add_u32 s88, s88, s86\nMvaa 188,248,152\nMvaa 188,252,156\ns_add_u32 s88, s88, s86\nMvaa 156,252,148\n"
"Mvaa 156,248,144\ns_add_u32 s88, s88, s86\nMvaa 124,248,136\nMvaa 124,252,140\nD0 9,24,44,3072\ns_add_u32 s88, s88, s86\nMvaa 92,252,132\nMvaa 92,248,128\nWL 0\n"
"s_add_u32 s88, s88, s86\nMaaa 16,224,192\nRa 128,10,32768\nMaaa 16,228,196\ns_add_u32 m0, s47, 16384\ns_add_u32 s44, s44, 0x40000\ns_add_u32 s88, s88, s86\nMaaa 48,228,204\n"
"Ra 132,11,32768\nMaaa 48,224,200\ns_add_u32 s88, s88, s86\nMaaa 80,224,208\nRa 136,10,34816\nMaaa 80,228,212\nD0 6,24,44,0\ns_add_u32 s88, s88, s86\nMaaa 112,228,220\n"
"Ra 140,11,34816\nMaaa 112,224,216\ns_add_u32 s88, s88, s86\nMaaa 116,232,216\nRa 144,10,40960\nMaaa 116,236,220\ns_add_u32 s88, s88, s86\nMaaa 84,236,212\nRa 148,11,40960\n"
"Maaa 84,232,208\nMaaa 52,232,200\nRa 152,10,43008\nMaaa 52,236,204\nD0 7,24,44,1024\nMaaa 20,236,196\nRa 156,11,43008\nMaaa 20,232,192\nMaaa 24,240,192\nMaaa 24,244,196\n"
"Maaa 56,244,204\nMaaa 56,240,200\nMaaa 88,240,208\nMaaa 88,244,212\nD0 8,24,44,2048\nMaaa 120,244,220\nMaaa 120,240,216\nMaaa 124,248,216\nMaaa 124,252,220\nMaaa 92,252,212\n"
"Maaa 92,248,208\nMaaa 60,248,200\nMaaa 60,252,204\nD0 9,24,44,3072\nMaaa 28,252,196\nMaaa 28,248,192\ns_waitcnt vmcnt(12) lgkmcnt(0)\ns_barrier\nMaaa 0,160,192\nTa 224,16,32768\n"
"s_mov_b32 m0, s76\ns_add_u32 s76, s76, 1024\nMaaa 0,164,196\nTa 226,16,34816\nD0 1,36,75,0\ns_add_u32 s75, s75, 1024\nMaaa 32,164,204\ns_add_u32 m0, s47, -49152\n"
"s_add_u32 s48, s48, 128\nMaaa 32,160,200\nTa 228,16,49152\nMaaa 64,160,208\nTa 230,16,51200\nbuffer_load_dword v31, v24, s[24:27], s96 offen\ns_add_u32 s96, s96, 0x80000\n"
"Maaa 64,164,212\nD0 2,20,48,0\nMaaa 96,164,220\nTa 232,17,32768\nMaaa 96,160,216\nTa 234,17,34816\nMaaa 100,168,216\nMaaa 100,172,220\nTa 236,17,49152\nMaaa 68,172,212\n"
"Ta 238,17,51200\nMaaa 68,168,208\nMaaa 36,168,200\nTa 240,18,32768\nMaaa 36,172,204\nTa 242,18,34816\nMaaa 4,172,196\nD0 3,20,48,1024\nMaaa 4,168,192\nTa 244,18,49152\n"
"Maaa 8,176,192\nTa 246,18,51200\nMaaa 8,180,196\nMaaa 40,180,204\nTa 248,19,32768\nMaaa 40,176,200\nTa 250,19,34816\nMaaa 72,176,208\nMaaa 72,180,212\nTa 252,19,49152\n"
"Maaa 104,180,220\nTa 254,19,51200\nMaaa 104,176,216\nD0 4,20,48,2048\nMaaa 108,184,216\nMaaa 108,188,220\nMaaa 76,188,212\nMaaa 76,184,208\nMaaa 44,184,200\nMaaa 44,188,204\n"
"D0 5,20,48,3072\nMaaa 12,188,196\nMaaa 12,184,192\nWL 0\nMvaa 80,224,128\nTa 160,12,32768\nMvaa 80,228,132\nTa 162,12,34816\nMvaa 112,228,140\nTa 164,12,49152\n"
"s_add_u32 m0, s47, -32768\ns_add_u32 s45, s45, 128\nMvaa 112,224,136\nTa 166,12,51200\nMvaa 144,224,144\nTa 168,13,32768\nMvaa 144,228,148\nTa 170,13,34816\nD0 2,20,45,0\n"
"Mvaa 176,228,156\nTa 172,13,49152\nMvaa 176,224,152\nTa 174,13,51200\nMvaa 180,232,152\nTa 176,14,32768\nMvaa 180,236,156\nTa 178,14,34816\nMvaa 148,236,148\nTa 180,14,49152\n"
"Mvaa 148,232,144\nTa 182,14,51200\nMvaa 116,232,136\nTa 184,15,32768\nMvaa 116,236,140\nTa 186,15,34816\nMvaa 84,236,132\nTa 188,15,49152\nD0 3,20,45,1024\nMvaa 84,232,128\n"
"Ta 190,15,51200\nMvaa 88,240,128\nRa 192,10,49152\nMvaa 88,244,132\nRa 196,11,49152\nMvaa 120,244,140\nRa 200,10,51200\nMvaa 120,240,136\nRa 204,11,51200\nMvaa 152,240,144\n"
"Ra 208,10,57344\nMvaa 152,244,148\nRa 212,11,57344\nMvaa 184,244,156\nRa 216,10,59392\nMvaa 184,240,152\nRa 220,11,59392\nD0 4,20,45,2048\nMvaa 188,248,152\nMvaa 188,252,156\n"
"Mvaa 156,252,148\nMvaa 156,248,144\nMvaa 124,248,136\nMvaa 124,252,140\nD0 5,20,45,3072\nMvaa 92,252,132\nMvaa 92,248,128\ns_waitcnt vmcnt(18) lgkmcnt(8)\ns_barrier\n"
"Mvaa 64,160,128\nMvaa 64,164,132\ns_add_u32 m0, s47, 32768\ns_add_u32 s44, s44, 0x40000\ns_mov_b32 s86, 1\ns_load_dword s86, s[54:55], s95\nMvaa 96,164,140\n"
"s_add_u32 s95, s95, 1024\nMvaa 96,160,136\nMvaa 128,160,144\nMvaa 128,164,148\nD0 6,24,44,0\nMvaa 160,164,156\nMvaa 160,160,152\nMvaa 164,168,152\nMvaa 164,172,156\n"
"Mvaa 132,172,148\nMvaa 132,168,144\ns_sub_u32 s88, s88, s86\nMvaa 100,168,136\nMvaa 100,172,140\nD0 7,24,44,1024\ns_sub_u32 s88, s88, s86\nMvaa 68,172,132\nMvaa 68,168,128\n"
"s_sub_u32 s88, s88, s86\nMvaa 72,176,128\nMvaa 72,180,132\ns_sub_u32 s88, s88, s86\nMvaa 104,180,140\nMvaa 104,176,136\ns_sub_u32 s88, s88, s86\nMvaa 136,176,144\n"
"Mvaa 136,180,148\nD0 8,24,44,2048\ns_sub_u32 s88, s88, s86\nMvaa 168,180,156\nMvaa 168,176,152\ns_sub_u32 s88, s88, s86\nMvaa 172,184,152\nMvaa 172,188,156\n"
"s_sub_u32 s88, s88, s86\nMvaa 140,188,148\nMvaa 140,184,144\ns_sub_u32 s88, s88, s86\nMvaa 108,184,136\nMvaa 108,188,140\nD0 9,24,44,3072\ns_sub_u32 s88, s88, s86\n"
"Mvaa 76,188,132\nMvaa 76,184,128\nWL 0\ns_sub_u32 s88, s88, s86\nMaaa 0,160,192\nRa 128,10,0\nMaaa 0,164,196\ns_add_u32 m0, s47, 49152\ns_add_u32 s44, s44, 0x40000\n"
"s_sub_u32 s88, s88, s86\nMaaa 32,164,204\nRa 132,11,0\nMaaa 32,160,200\ns_sub_u32 s88, s88, s86\nMaaa 64,160,208\nRa 136,10,2048\nMaaa 64,164,212\nD0 6,24,44,0\n"
"s_sub_u32 s88, s88, s86\nMaaa 96,164,220\nRa 140,11,2048\nMaaa 96,160,216\ns_sub_u32 s88, s88, s86\nMaaa 100,168,216\nRa 144,10,8192\nMaaa 100,172,220\ns_sub_u32 s88, s88, s86\n"
"Maaa 68,172,212\nRa 148,11,8192\nMaaa 68,168,208\nMaaa 36,168,200\nRa 152,10,10240\nMaaa 36,172,204\nD0 7,24,44,1024\nMaaa 4,172,196\nRa 156,11,10240\nMaaa 4,168,192\n"
"Maaa 8,176,192\nMaaa 8,180,196\nMaaa 40,180,204\nMaaa 40,176,200\nMaaa 72,176,208\nMaaa 72,180,212\nD0 8,24,44,2048\nMaaa 104,180,220\nMaaa 104,176,216\nMaaa 108,184,216\n"
"Maaa 108,188,220\nMaaa 76,188,212\nMaaa 76,184,208\nMaaa 44,184,200\nMaaa 44,188,204\nD0 9,24,44,3072\nMaaa 12,188,196\nMaaa 12,184,192\ns_waitcnt vmcnt(12) lgkmcnt(0)\ns_barrier\n"
"Maaa 16,224,192\nTa 160,12,0\ns_mov_b32 m0, s76\ns_add_u32 s76, s76, 1024\nMaaa 16,228,196\nTa 162,12,2048\nD0 1,36,75,0\ns_add_u32 s75, s75, 1024\nMaaa 48,228,204\n"
"s_add_u32 m0, s47, -16384\ns_add_u32 s48, s48, 128\nMaaa 48,224,200\nTa 164,12,16384\nMaaa 80,224,208\nTa 166,12,18432\nbuffer_load_dword v31, v24, s[24:27], s96 offen\n"
"s_add_u32 s96, s96, 0x80000\nMaaa 80,228,212\n.W563:\ns_cbranch_execz .W567\nD0 2,20,48,0\nMaaa 112,228,220\nTa 168,13,0\nMaaa 112,224,216\nTa 170,13,2048\nMaaa 116,232,216\n"
"Maaa 116,236,220\nTa 172,13,16384\nMaaa 84,236,212\nTa 174,13,18432\nMaaa 84,232,208\nMaaa 52,232,200\nTa 176,14,0\nMaaa 52,236,204\nTa 178,14,2048\nMaaa 20,236,196\n"
"D0 3,20,48,1024\nMaaa 20,232,192\nTa 180,14,16384\nMaaa 24,240,192\nTa 182,14,18432\nMaaa 24,244,196\nMaaa 56,244,204\nTa 184,15,0\nMaaa 56,240,200\nTa 186,15,2048\n"
"Maaa 88,240,208\nMaaa 88,244,212\nTa 188,15,16384\nMaaa 120,244,220\nTa 190,15,18432\nMaaa 120,240,216\nD0 4,20,48,2048\nMaaa 124,248,216\nMaaa 124,252,220\nMaaa 92,252,212\n"
"Maaa 92,248,208\nMaaa 60,248,200\ns_xor_b32 s94, s94, 0x2000\ns_xor_b32 s95, s95, 0x2000\ns_sub_u32 s88, 0, s88\nMaaa 60,252,204\nD0 5,20,48,3072\ns_sub_u32 s50, s50, 1\n"
"Maaa 28,252,196\nMaaa 28,248,192\nWL 0\ns_cbranch_scc0 .GL272\nMvaa 64,160,128\nTa 224,16,0\nMvaa 64,164,132\nTa 226,16,2048\nMvaa 96,164,140\ns_add_u32 m0, s47, -65536\n"
"s_add_u32 s45, s45, 128\nMvaa 96,160,136\nTa 228,16,16384\nMvaa 128,160,144\nTa 230,16,18432\nMvaa 128,164,148\nD0 2,20,45,0\nMvaa 160,164,156\nTa 232,17,0\nMvaa 160,160,152\n"
"Ta 234,17,2048\nMvaa 164,168,152\ns_sub_u32 s89, 0, s88\ns_cmp_eq_u32 s92, 0\ns_cselect_b32 s88, s88, s89\nMvaa 164,172,156\nTa 236,17,16384\nMvaa 132,172,148\nTa 238,17,18432\n"
"Mvaa 132,168,144\nv_mov_b32 v22, s88\nMvaa 100,168,136\nTa 240,18,0\nMvaa 100,172,140\nTa 242,18,2048\nMvaa 68,172,132\nD0 3,20,45,1024\nMvaa 68,168,128\nTa 244,18,16384\n"
"Mvaa 72,176,128\nTa 246,18,18432\nMvaa 72,180,132\nv_mov_b32 v23, s15\nMvaa 104,180,140\nTa 248,19,0\nMvaa 104,176,136\nTa 250,19,2048\nMvaa 136,176,144\n"
"buffer_store_dwordx2 v[22:23], off, s[16:19], s93 sc0 sc1\nMvaa 136,180,148\nTa 252,19,16384\nMvaa 168,180,156\nTa 254,19,18432\nMvaa 168,176,152\nD0 4,20,45,2048\n"
"Mvaa 172,184,152\nMvaa 172,188,156\nMvaa 140,188,148\nMvaa 140,184,144\nMvaa 108,184,136\nMvaa 108,188,140\nD0 5,20,45,3072\nMvaa 76,188,132\nMvaa 76,184,128\n"
"s_waitcnt vmcnt(19) lgkmcnt(0)\ns_barrier\nMvaa 80,224,128\nRa 192,10,16384\nMvaa 80,228,132\ns_add_u32 m0, s47, 0\ns_add_u32 s44, s44, 0x40000\nMvaa 112,228,140\nRa 196,11,16384\n"
"Mvaa 112,224,136\nMvaa 144,224,144\nRa 200,10,18432\nMvaa 144,228,148\nD0 6,24,44,0\nMvaa 176,228,156\nRa 204,11,18432\nMvaa 176,224,152\nMvaa 180,232,152\nRa 208,10,24576\n"
"Mvaa 180,236,156\nMvaa 148,236,148\nRa 212,11,24576\nMvaa 148,232,144\nMvaa 116,232,136\nRa 216,10,26624\nMvaa 116,236,140\nD0 7,24,44,1024\nMvaa 84,236,132\nRa 220,11,26624\n"
"Mvaa 84,232,128\nMvaa 88,240,128\nMvaa 88,244,132\nMvaa 120,244,140\nMvaa 120,240,136\nMvaa 152,240,144\nMvaa 152,244,148\nD0 8,24,44,2048\nMvaa 184,244,156\nMvaa 184,240,152\n"
"Mvaa 188,248,152\nMvaa 188,252,156\nMvaa 156,252,148\nMvaa 156,248,144\nMvaa 124,248,136\nMvaa 124,252,140\nD0 9,24,44,3072\nMvaa 92,252,132\nMvaa 92,248,128\nWL 0\n"
"Maaa 16,224,192\nRa 128,10,32768\nMaaa 16,228,196\ns_add_u32 m0, s47, 16384\ns_add_u32 s44, s44, 0x40000\nMaaa 48,228,204\nRa 132,11,32768\nMaaa 48,224,200\nMaaa 80,224,208\n"
"Ra 136,10,34816\nMaaa 80,228,212\nD0 6,24,44,0\nMaaa 112,228,220\nRa 140,11,34816\nMaaa 112,224,216\nMaaa 116,232,216\nRa 144,10,40960\nMaaa 116,236,220\nMaaa 84,236,212\n"
"Ra 148,11,40960\nMaaa 84,232,208\nMaaa 52,232,200\nRa 152,10,43008\nMaaa 52,236,204\nD0 7,24,44,1024\nMaaa 20,236,196\nRa 156,11,43008\nMaaa 20,232,192\nMaaa 24,240,192\n"
"Maaa 24,244,196\nMaaa 56,244,204\nMaaa 56,240,200\nMaaa 88,240,208\nMaaa 88,244,212\nD0 8,24,44,2048\nMaaa 120,244,220\nMaaa 120,240,216\nMaaa 124,248,216\nMaaa 124,252,220\n"
"Maaa 92,252,212\nMaaa 92,248,208\nMaaa 60,248,200\nMaaa 60,252,204\nD0 9,24,44,3072\nMaaa 28,252,196\nMaaa 28,248,192\ns_waitcnt vmcnt(19) lgkmcnt(0)\ns_barrier\nMaaa 0,160,192\n"
"Ta 224,16,32768\nMaaa 0,164,196\nTa 226,16,34816\nMaaa 32,164,204\ns_add_u32 m0, s47, -49152\ns_add_u32 s48, s48, 128\nMaaa 32,160,200\nTa 228,16,49152\nMaaa 64,160,208\n"
"Ta 230,16,51200\nbuffer_load_dword v31, v24, s[24:27], s96 offen\ns_add_u32 s96, s96, 0x80000\nMaaa 64,164,212\nD0 2,20,48,0\nMaaa 96,164,220\nTa 232,17,32768\nMaaa 96,160,216\n"
"Ta 234,17,34816\nMaaa 100,168,216\nMaaa 100,172,220\nTa 236,17,49152\nMaaa 68,172,212\nTa 238,17,51200\nMaaa 68,168,208\nMaaa 36,168,200\nTa 240,18,32768\nMaaa 36,172,204\n"
"Ta 242,18,34816\nMaaa 4,172,196\nD0 3,20,48,1024\nMaaa 4,168,192\nTa 244,18,49152\nMaaa 8,176,192\nTa 246,18,51200\nMaaa 8,180,196\nMaaa 40,180,204\nTa 248,19,32768\n"
"Maaa 40,176,200\nTa 250,19,34816\nMaaa 72,176,208\nMaaa 72,180,212\nTa 252,19,49152\nMaaa 104,180,220\nTa 254,19,51200\nMaaa 104,176,216\nD0 4,20,48,2048\nMaaa 108,184,216\n"
"Maaa 108,188,220\nMaaa 76,188,212\nMaaa 76,184,208\nMaaa 44,184,200\nMaaa 44,188,204\nD0 5,20,48,3072\nMaaa 12,188,196\nMaaa 12,184,192\nWL 0\nMvaa 80,224,128\nTa 160,12,32768\n"
"Mvaa 80,228,132\nTa 162,12,34816\nMvaa 112,228,140\ns_add_u32 m0, s47, -32768\ns_add_u32 s45, s45, 128\nMvaa 112,224,136\nTa 164,12,49152\nMvaa 144,224,144\nTa 166,12,51200\n"
"Mvaa 144,228,148\nD0 2,20,45,0\nMvaa 176,228,156\nTa 168,13,32768\nMvaa 176,224,152\nTa 170,13,34816\nMvaa 180,232,152\nMvaa 180,236,156\nTa 172,13,49152\nMvaa 148,236,148\n"
"Ta 174,13,51200\nMvaa 148,232,144\nMvaa 116,232,136\nTa 176,14,32768\nMvaa 116,236,140\nTa 178,14,34816\nMvaa 84,236,132\nD0 3,20,45,1024\nMvaa 84,232,128\nTa 180,14,49152\n"
"Mvaa 88,240,128\nTa 182,14,51200\nMvaa 88,244,132\nMvaa 120,244,140\nTa 184,15,32768\nMvaa 120,240,136\nTa 186,15,34816\nMvaa 152,240,144\nMvaa 152,244,148\nTa 188,15,49152\n"
"Mvaa 184,244,156\nTa 190,15,51200\nMvaa 184,240,152\nD0 4,20,45,2048\nMvaa 188,248,152\nMvaa 188,252,156\nMvaa 156,252,148\nMvaa 156,248,144\nMvaa 124,248,136\nMvaa 124,252,140\n"
"D0 5,20,45,3072\nMvaa 92,252,132\nMvaa 92,248,128\ns_waitcnt vmcnt(17) lgkmcnt(0)\ns_barrier\nMvaa 64,160,128\nRa 192,10,49152\nMvaa 64,164,132\ns_add_u32 m0, s47, 32768\n"
"s_add_u32 s44, s44, 0x40000\nMvaa 96,164,140\nRa 196,11,49152\nMvaa 96,160,136\nMvaa 128,160,144\nRa 200,10,51200\nMvaa 128,164,148\nD0 6,24,44,0\nMvaa 160,164,156\n"
"Ra 204,11,51200\nMvaa 160,160,152\nMvaa 164,168,152\nRa 208,10,57344\nMvaa 164,172,156\ns_lshl_b32 s5, s73, 9\ns_add_u32 s5, s5, 0xc049400\nv_lshrrev_b32 v27, 1, v1\n"
"buffer_load_dwordx2 v[22:23], v27, s[16:19], s5 offen sc0 sc1\nMvaa 132,172,148\nRa 212,11,57344\nMvaa 132,168,144\nMvaa 100,168,136\nRa 216,10,59392\nMvaa 100,172,140\n"
"D0 7,24,44,1024\nMvaa 68,172,132\nRa 220,11,59392\nMvaa 68,168,128\nMvaa 72,176,128\nMvaa 72,180,132\nMvaa 104,180,140\nMvaa 104,176,136\nMvaa 136,176,144\nMvaa 136,180,148\n"
"D0 8,24,44,2048\nMvaa 168,180,156\nMvaa 168,176,152\nMvaa 172,184,152\nMvaa 172,188,156\nMvaa 140,188,148\nMvaa 140,184,144\nMvaa 108,184,136\nMvaa 108,188,140\nD0 9,24,44,3072\n"
"Mvaa 76,188,132\nMvaa 76,184,128\nWL 0\nMaaa 0,160,192\nRa 128,10,0\nMaaa 0,164,196\ns_add_u32 m0, s47, 49152\ns_add_u32 s44, s44, 0x40000\nMaaa 32,164,204\nRa 132,11,0\n"
"Maaa 32,160,200\nMaaa 64,160,208\nRa 136,10,2048\nMaaa 64,164,212\nD0 6,24,44,0\nMaaa 96,164,220\nRa 140,11,2048\nMaaa 96,160,216\nMaaa 100,168,216\nRa 144,10,8192\n"
"Maaa 100,172,220\nMaaa 68,172,212\nRa 148,11,8192\nMaaa 68,168,208\nMaaa 36,168,200\nRa 152,10,10240\nMaaa 36,172,204\nD0 7,24,44,1024\nMaaa 4,172,196\nRa 156,11,10240\n"
"Maaa 4,168,192\nMaaa 8,176,192\nMaaa 8,180,196\nMaaa 40,180,204\nMaaa 40,176,200\nMaaa 72,176,208\nMaaa 72,180,212\nD0 8,24,44,2048\nMaaa 104,180,220\nMaaa 104,176,216\n"
"Maaa 108,184,216\nMaaa 108,188,220\nMaaa 76,188,212\nMaaa 76,184,208\nMaaa 44,184,200\nMaaa 44,188,204\nD0 9,24,44,3072\nMaaa 12,188,196\nMaaa 12,184,192\n"
"s_waitcnt vmcnt(18) lgkmcnt(0)\ns_barrier\nMaaa 16,224,192\nTa 160,12,0\nMaaa 16,228,196\nTa 162,12,2048\nMaaa 48,228,204\ns_add_u32 m0, s47, -16384\ns_add_u32 s48, s48, 128\n"
"Maaa 48,224,200\nTa 164,12,16384\nMaaa 80,224,208\nTa 166,12,18432\nbuffer_load_dword v31, v24, s[24:27], s96 offen\ns_add_u32 s96, s96, 0x80000\nMaaa 80,228,212\nD0 2,20,48,0\n"
"Maaa 112,228,220\nTa 168,13,0\nMaaa 112,224,216\nTa 170,13,2048\nMaaa 116,232,216\nMaaa 116,236,220\nTa 172,13,16384\nMaaa 84,236,212\nTa 174,13,18432\nMaaa 84,232,208\n"
"Maaa 52,232,200\nTa 176,14,0\nMaaa 52,236,204\nTa 178,14,2048\nMaaa 20,236,196\nD0 3,20,48,1024\nMaaa 20,232,192\nTa 180,14,16384\nMaaa 24,240,192\nTa 182,14,18432\n"
"Maaa 24,244,196\nMaaa 56,244,204\nTa 184,15,0\nMaaa 56,240,200\nTa 186,15,2048\nMaaa 88,240,208\nMaaa 88,244,212\nTa 188,15,16384\nMaaa 120,244,220\nTa 190,15,18432\n"
"Maaa 120,240,216\nD0 4,20,48,2048\nMaaa 124,248,216\nMaaa 124,252,220\nMaaa 92,252,212\nMaaa 92,248,208\nMaaa 60,248,200\nMaaa 60,252,204\nD0 5,20,48,3072\nMaaa 28,252,196\n"
"Maaa 28,248,192\nWL 0\nMvaa 64,160,128\nTa 224,16,0\nWV 12\nv_cmp_ne_u32 vcc, s15, v23\ns_nop 0\ns_cbranch_vccnz .S269\n.S270:\nMvaa 64,164,132\nTa 226,16,2048\nMvaa 96,164,140\n"
"s_add_u32 m0, s47, -65536\ns_add_u32 s45, s45, 128\nMvaa 96,160,136\nTa 228,16,16384\nMvaa 128,160,144\nTa 230,16,18432\nMvaa 128,164,148\nD0 2,20,45,0\nMvaa 160,164,156\n"
"Ta 232,17,0\nMvaa 160,160,152\nTa 234,17,2048\nMvaa 164,168,152\nv_add_u32_dpp v22, v22, v22 row_ror:8 row_mask:0xf bank_mask:0xf\nMvaa 164,172,156\nTa 236,17,16384\n"
"Mvaa 132,172,148\nTa 238,17,18432\nMvaa 132,168,144\nv_add_u32_dpp v22, v22, v22 row_ror:4 row_mask:0xf bank_mask:0xf\nMvaa 100,168,136\nTa 240,18,0\nMvaa 100,172,140\n"
"Ta 242,18,2048\nMvaa 68,172,132\nD0 3,20,45,1024\nMvaa 68,168,128\nTa 244,18,16384\nMvaa 72,176,128\nTa 246,18,18432\nMvaa 72,180,132\n"
"v_add_u32_dpp v22, v22, v22 row_ror:2 row_mask:0xf bank_mask:0xf\nMvaa 104,180,140\nTa 248,19,0\nMvaa 104,176,136\nTa 250,19,2048\nMvaa 136,176,144\n"
"v_add_u32_dpp v22, v22, v22 row_ror:1 row_mask:0xf bank_mask:0xf\nMvaa 136,180,148\nTa 252,19,16384\nMvaa 168,180,156\nTa 254,19,18432\n.W564:\ns_cbranch_execz .W568\n"
"Mvaa 168,176,152\nD0 4,20,45,2048\nMvaa 172,184,152\nv_mov_b32 v23, v22\nMvaa 172,188,156\nMvaa 140,188,148\nv_permlane16_swap_b32 v22, v23\nMvaa 140,184,144\n"
"v_add_u32 v22, v22, v23\nMvaa 108,184,136\nv_mov_b32 v23, v22\nMvaa 108,188,140\nD0 5,20,45,3072\nMvaa 76,188,132\nv_permlane32_swap_b32 v22, v23\nMvaa 76,184,128\nWL 0\n"
"s_barrier\nMvaa 80,224,128\nRa 192,10,16384\nMvaa 80,228,132\ns_add_u32 m0, s47, 0\ns_add_u32 s44, s44, 0x40000\nMvaa 112,228,140\nRa 196,11,16384\nMvaa 112,224,136\n"
"v_add_u32 v22, v22, v23\nMvaa 144,224,144\nRa 200,10,18432\nMvaa 144,228,148\nD0 6,24,44,0\nMvaa 176,228,156\nRa 204,11,18432\nMvaa 176,224,152\nv_readfirstlane_b32 s4, v22\n"
"Mvaa 180,232,152\nRa 208,10,24576\nMvaa 180,236,156\nMvaa 148,236,148\nRa 212,11,24576\ns_cmp_gt_i32 s4, 0\ns_cselect_b32 s4, 1, 0\nMvaa 148,232,144\ns_xor_b32 s4, s4, s12\n"
"Mvaa 116,232,136\nRa 216,10,26624\nMvaa 116,236,140\nD0 7,24,44,1024\nMvaa 84,236,132\nRa 220,11,26624\nMvaa 84,232,128\ns_and_b32 s4, s4, 1\nMvaa 88,240,128\n"
"s_lshl_b32 s4, s4, 13\nMvaa 88,244,132\ns_mul_i32 s72, s73, 25165824\nMvaa 120,244,140\ns_add_u32 s72, s72, s4\nMvaa 120,240,136\ns_and_b32 s4, s12, 1\nMvaa 152,240,144\n"
"s_lshl_b32 s4, s4, 21\nMvaa 152,244,148\nD0 8,24,44,2048\nMvaa 184,244,156\ns_add_u32 s72, s72, s4\nMvaa 184,240,152\ns_lshl3_add_u32 s2, s13, s11\nMvaa 188,248,152\n"
"s_lshl_b32 s2, s2, 11\nMvaa 188,252,156\ns_lshl_b32 s3, s14, 22\nMvaa 156,252,148\ns_add_u32 s2, s2, s3\nMvaa 156,248,144\ns_add_u32 s2, s2, s72\nMvaa 124,248,136\n"
"s_add_u32 s49, s2, 0xbe0000\nMvaa 124,252,140\nD0 9,24,44,3072\nMvaa 92,252,132\nMvaa 92,248,128\nWL 0\nMaaa 16,224,192\nRa 128,10,32768\ns_mov_b32 s4, s13\nMaaa 16,228,196\n"
"s_add_u32 m0, s47, 16384\ns_add_u32 s44, s44, 0x40000\nMaaa 48,228,204\nRa 132,11,32768\nv_lshrrev_b32 v32, 2, v0\nMaaa 48,224,200\nv_mul_u32_u24 v32, 896, v32\nMaaa 80,224,208\n"
"Ra 136,10,34816\nv_and_b32 v33, 3, v0\nMaaa 80,228,212\nD0 6,24,44,0\nMaaa 112,228,220\nRa 140,11,34816\nv_lshl_add_u32 v32, v33, 4, v32\nMaaa 112,224,216\ns_lshr_b32 s2, s11, 1\n"
"Maaa 116,232,216\nRa 144,10,40960\ns_mul_i32 s3, s2, 448\nMaaa 116,236,220\ns_and_b32 s5, s11, 1\nMaaa 84,236,212\nRa 148,11,40960\ns_lshl3_add_u32 s3, s5, s3\nMaaa 84,232,208\n"
"s_add_u32 s3, s3, 131072\nMaaa 52,232,200\nRa 152,10,43008\nv_add_u32 v32, s3, v32\nMaaa 52,236,204\nD0 7,24,44,1024\nMaaa 20,236,196\nRa 156,11,43008\nTv 36,32,0\n"
"Maaa 20,232,192\nTv 38,32,14336\nMaaa 24,240,192\nTv 40,32,64\nTv 42,32,14400\nMaaa 24,244,196\nTv 44,32,128\nMaaa 56,244,204\nTv 46,32,14464\nTv 48,32,192\nMaaa 56,240,200\n"
"Tv 50,32,14528\nMaaa 88,240,208\nTv 52,32,256\nTv 54,32,14592\nMaaa 88,244,212\nD0 8,24,44,2048\nMaaa 120,244,220\nTv 56,32,320\nTv 58,32,14656\nMaaa 120,240,216\nTv 60,32,384\n"
"Maaa 124,248,216\nTv 62,32,14720\ns_lshr_b32 s0, s4, 1\nMaaa 124,252,220\ns_mul_i32 s0, s0, 12\nMaaa 92,252,212\ns_lshl1_add_u32 s0, s5, s0\ns_and_b32 s6, s4, 1\nMaaa 92,248,208\n"
"s_add_u32 s0, s0, s6\nMaaa 60,248,200\ns_lshl3_add_u32 s1, s5, s4\ns_add_u32 s77, s1, 48\nMaaa 60,252,204\nD0 9,24,44,3072\nMaaa 28,252,196\ns_and_b32 s4, s12, 1\n"
"s_mov_b32 s7, 0x6543210\nMaaa 28,248,192\ns_waitcnt vmcnt(17) lgkmcnt(14)\ns_barrier\nMaaa 0,160,192\nTa 224,16,32768\ns_cmp_lg_u32 s4, s2\ns_cselect_b32 s6, s0, s1\n"
"s_cselect_b32 s5, 4, 16\ns_cselect_b32 s7, 0x6352410, s7\ns_cselect_b32 s3, 0, 229376\nMaaa 0,164,196\nTa 226,16,34816\nMaaa 32,164,204\ns_add_u32 m0, s47, -49152\n"
"s_add_u32 s48, s48, 128\nMaaa 32,160,200\nTa 228,16,49152\nMaaa 64,160,208\nTa 230,16,51200\ns_add_u32 s3, s3, s72\nMaaa 64,164,212\nD0 2,20,48,0\nMaaa 96,164,220\n"
"Ta 232,17,32768\ns_add_u32 s3, s3, 0x1400000\nMaaa 96,160,216\nTa 234,17,34816\nMaaa 100,168,216\nWL 14\ns_lshr_b32 s0, s6, 4\nMaaa 100,172,220\nTa 236,17,49152\nMaaa 68,172,212\n"
"Ta 238,17,51200\ns_lshl_b32 s0, s0, 2\nMaaa 68,168,208\ns_lshr_b32 s0, s7, s0\nMaaa 36,168,200\nTa 240,18,32768\ns_and_b32 s0, s0, 7\nMaaa 36,172,204\nTa 242,18,34816\n"
"Maaa 4,172,196\nD0 3,20,48,1024\ns_lshl_b32 s0, s0, 5\nMaaa 4,168,192\nTa 244,18,49152\nMaaa 8,176,192\nTa 246,18,51200\ns_and_b32 s1, s6, 15\nMaaa 8,180,196\n"
"s_add_u32 s0, s0, s1\nMaaa 40,180,204\nTa 248,19,32768\ns_and_b32 s1, s6, 8\nMaaa 40,176,200\nTa 250,19,34816\nMaaa 72,176,208\ns_add_u32 s0, s0, s1\ns_lshl_b32 s0, s0, 10\n"
"Maaa 72,180,212\nTa 252,19,49152\nMaaa 104,180,220\nTa 254,19,51200\ns_add_u32 s0, s0, s3\nMaaa 104,176,216\nD0 4,20,48,2048\nMaaa 108,184,216\nSv 36,1,16,0,0\n"
"s_add_u32 s6, s6, s5\nMaaa 108,188,220\nMaaa 76,188,212\nMaaa 76,184,208\nMaaa 44,184,200\nMaaa 44,188,204\nD0 5,20,48,3072\nMaaa 12,188,196\nMaaa 12,184,192\nWL 0\n"
"Mvaa 80,224,128\nTa 160,12,32768\ns_lshr_b32 s0, s6, 4\nMvaa 80,228,132\nTa 162,12,34816\nMvaa 112,228,140\ns_add_u32 m0, s47, -32768\ns_add_u32 s45, s45, 128\nMvaa 112,224,136\n"
"Ta 164,12,49152\nMvaa 144,224,144\nTa 166,12,51200\ns_lshl_b32 s0, s0, 2\nMvaa 144,228,148\nD0 2,20,45,0\nMvaa 176,228,156\nTa 168,13,32768\ns_lshr_b32 s0, s7, s0\n"
"Mvaa 176,224,152\nTa 170,13,34816\nMvaa 180,232,152\ns_and_b32 s0, s0, 7\ns_lshl_b32 s0, s0, 5\nMvaa 180,236,156\nTa 172,13,49152\nMvaa 148,236,148\nTa 174,13,51200\n"
"s_and_b32 s1, s6, 15\nMvaa 148,232,144\ns_add_u32 s0, s0, s1\nMvaa 116,232,136\nTa 176,14,32768\ns_and_b32 s1, s6, 8\nMvaa 116,236,140\nTa 178,14,34816\nMvaa 84,236,132\n"
"D0 3,20,45,1024\ns_add_u32 s0, s0, s1\nMvaa 84,232,128\nTa 180,14,49152\nMvaa 88,240,128\nTa 182,14,51200\ns_lshl_b32 s0, s0, 10\nMvaa 88,244,132\ns_add_u32 s0, s0, s3\n"
"Mvaa 120,244,140\nTa 184,15,32768\nSv 40,1,16,0,0\nMvaa 120,240,136\nTa 186,15,34816\nMvaa 152,240,144\ns_add_u32 s6, s6, s5\nMvaa 152,244,148\nTa 188,15,49152\nMvaa 184,244,156\n"
"Ta 190,15,51200\nMvaa 184,240,152\nD0 4,20,45,2048\nMvaa 188,248,152\nMvaa 188,252,156\nMvaa 156,252,148\nMvaa 156,248,144\nMvaa 124,248,136\ns_lshr_b32 s0, s6, 4\n"
"s_lshl_b32 s0, s0, 2\nMvaa 124,252,140\nD0 5,20,45,3072\nMvaa 92,252,132\ns_lshr_b32 s0, s7, s0\ns_and_b32 s0, s0, 7\nMvaa 92,248,128\ns_waitcnt vmcnt(18) lgkmcnt(0)\ns_barrier\n"
"Mvaa 64,160,128\nRa 192,10,49152\ns_lshl_b32 s0, s0, 5\nMvaa 64,164,132\ns_add_u32 m0, s47, 32768\ns_add_u32 s44, s44, 0x40000\nMvaa 96,164,140\nRa 196,11,49152\n"
"s_and_b32 s1, s6, 15\nMvaa 96,160,136\ns_add_u32 s0, s0, s1\nMvaa 128,160,144\nRa 200,10,51200\ns_and_b32 s1, s6, 8\nMvaa 128,164,148\nD0 6,24,44,0\nMvaa 160,164,156\n"
"Ra 204,11,51200\ns_add_u32 s0, s0, s1\nMvaa 160,160,152\ns_lshl_b32 s0, s0, 10\nMvaa 164,168,152\nRa 208,10,57344\ns_add_u32 s0, s0, s3\nMvaa 164,172,156\nSv 44,1,16,0,0\n"
"Mvaa 132,172,148\nRa 212,11,57344\ns_add_u32 s6, s6, s5\nMvaa 132,168,144\nMvaa 100,168,136\nRa 216,10,59392\nMvaa 100,172,140\nD0 7,24,44,1024\nMvaa 68,172,132\nRa 220,11,59392\n"
"Mvaa 68,168,128\nMvaa 72,176,128\nMvaa 72,180,132\nMvaa 104,180,140\ns_mov_b32 s6, s77\ns_mov_b32 s5, 16\nMvaa 104,176,136\ns_lshr_b32 s0, s6, 4\nMvaa 136,176,144\n"
"s_lshl_b32 s0, s0, 2\ns_lshr_b32 s0, s7, s0\nMvaa 136,180,148\nD0 8,24,44,2048\nMvaa 168,180,156\ns_and_b32 s0, s0, 7\ns_lshl_b32 s0, s0, 5\nMvaa 168,176,152\n"
"s_and_b32 s1, s6, 15\nMvaa 172,184,152\ns_add_u32 s0, s0, s1\ns_and_b32 s1, s6, 8\nMvaa 172,188,156\ns_add_u32 s0, s0, s1\nMvaa 140,188,148\ns_lshl_b32 s0, s0, 10\n"
"s_add_u32 s0, s0, s3\nMvaa 140,184,144\nSv 48,1,16,0,0\nMvaa 108,184,136\ns_add_u32 s6, s6, s5\nMvaa 108,188,140\nD0 9,24,44,3072\nMvaa 76,188,132\nMvaa 76,184,128\nWL 0\n"
"Maaa 0,160,192\nRa 128,10,0\nMaaa 0,164,196\ns_add_u32 m0, s47, 49152\ns_add_u32 s44, s44, 0x40000\nMaaa 32,164,204\nRa 132,11,0\nMaaa 32,160,200\nMaaa 64,160,208\nRa 136,10,2048\n"
"s_lshr_b32 s0, s6, 4\nMaaa 64,164,212\nD0 6,24,44,0\nMaaa 96,164,220\nRa 140,11,2048\ns_lshl_b32 s0, s0, 2\nMaaa 96,160,216\ns_lshr_b32 s0, s7, s0\nMaaa 100,168,216\n"
"Ra 144,10,8192\ns_and_b32 s0, s0, 7\nMaaa 100,172,220\ns_lshl_b32 s0, s0, 5\nMaaa 68,172,212\nRa 148,11,8192\ns_and_b32 s1, s6, 15\nMaaa 68,168,208\ns_add_u32 s0, s0, s1\n"
"Maaa 36,168,200\nRa 152,10,10240\ns_and_b32 s1, s6, 8\nMaaa 36,172,204\nD0 7,24,44,1024\nMaaa 4,172,196\nRa 156,11,10240\ns_add_u32 s0, s0, s1\nMaaa 4,168,192\n"
"s_lshl_b32 s0, s0, 10\nMaaa 8,176,192\ns_add_u32 s0, s0, s3\nSv 52,1,16,0,0\nMaaa 8,180,196\ns_add_u32 s6, s6, s5\nMaaa 40,180,204\nMaaa 40,176,200\nMaaa 72,176,208\n"
"Maaa 72,180,212\nD0 8,24,44,2048\nMaaa 104,180,220\nMaaa 104,176,216\nMaaa 108,184,216\nMaaa 108,188,220\ns_lshr_b32 s0, s6, 4\nMaaa 76,188,212\ns_lshl_b32 s0, s0, 2\n"
"s_lshr_b32 s0, s7, s0\nMaaa 76,184,208\ns_and_b32 s0, s0, 7\nMaaa 44,184,200\ns_lshl_b32 s0, s0, 5\ns_and_b32 s1, s6, 15\nMaaa 44,188,204\nD0 9,24,44,3072\nMaaa 12,188,196\n"
"s_add_u32 s0, s0, s1\ns_and_b32 s1, s6, 8\nMaaa 12,184,192\nWV 21\ns_barrier\nMaaa 16,224,192\nTa 160,12,0\ns_add_u32 s0, s0, s1\nMaaa 16,228,196\nTa 162,12,2048\nMaaa 48,228,204\n"
"s_add_u32 m0, s47, -16384\ns_add_u32 s48, s48, 128\nMaaa 48,224,200\nTa 164,12,16384\nMaaa 80,224,208\nTa 166,12,18432\ns_lshl_b32 s0, s0, 10\nMaaa 80,228,212\nD0 2,20,48,0\n"
"Maaa 112,228,220\nTa 168,13,0\ns_add_u32 s0, s0, s3\nMaaa 112,224,216\nTa 170,13,2048\nMaaa 116,232,216\nSv 56,1,16,0,0\ns_add_u32 s6, s6, s5\nMaaa 116,236,220\nTa 172,13,16384\n"
"Maaa 84,236,212\nTa 174,13,18432\nMaaa 84,232,208\nMaaa 52,232,200\nTa 176,14,0\nMaaa 52,236,204\nTa 178,14,2048\nMaaa 20,236,196\nD0 3,20,48,1024\nMaaa 20,232,192\n"
"Ta 180,14,16384\nMaaa 24,240,192\nTa 182,14,18432\ns_lshr_b32 s0, s6, 4\nMaaa 24,244,196\ns_lshl_b32 s0, s0, 2\nMaaa 56,244,204\nTa 184,15,0\ns_lshr_b32 s0, s7, s0\n"
"Maaa 56,240,200\nTa 186,15,2048\nMaaa 88,240,208\ns_and_b32 s0, s0, 7\ns_lshl_b32 s0, s0, 5\nMaaa 88,244,212\nTa 188,15,16384\nMaaa 120,244,220\nTa 190,15,18432\n"
"s_and_b32 s1, s6, 15\nMaaa 120,240,216\nD0 4,20,48,2048\nMaaa 124,248,216\ns_add_u32 s0, s0, s1\ns_and_b32 s1, s6, 8\nMaaa 124,252,220\ns_add_u32 s0, s0, s1\nMaaa 92,252,212\n"
"s_lshl_b32 s0, s0, 10\ns_add_u32 s0, s0, s3\nMaaa 92,248,208\nSv 60,1,16,0,0\nMaaa 60,248,200\ns_add_u32 s6, s6, s5\nMaaa 60,252,204\nD0 5,20,48,3072\nMaaa 28,252,196\n"
"Maaa 28,248,192\nWL 0\nMvaa 64,160,128\nTa 224,16,0\nMvaa 64,164,132\nTa 226,16,2048\ns_add_u32 m0, s47, 65536\ns_add_u32 s46, s51, 0\nMvaa 96,164,140\nv_lshrrev_b32 v48, 4, v0\n"
"Mvaa 96,160,136\nTa 228,16,16384\nD0 6,32,46,0\nMvaa 128,160,144\nTa 230,16,18432\nMvaa 128,164,148\nv_and_b32 v49, 7, v0\nMvaa 160,164,156\nTa 232,17,0\nMvaa 160,160,152\n"
"Ta 234,17,2048\nMvaa 164,168,152\nv_xor_b32 v49, v49, v48\nD0 7,32,46,1024\nMvaa 164,172,156\nTa 236,17,16384\nMvaa 132,172,148\nTa 238,17,18432\nMvaa 132,168,144\n"
"v_lshlrev_b32 v49, 4, v49\nMvaa 100,168,136\nTa 240,18,0\nMvaa 100,172,140\nTa 242,18,2048\nD0 8,32,46,2048\nMvaa 68,172,132\nv_lshrrev_b32 v48, 3, v0\nMvaa 68,168,128\n"
"Ta 244,18,16384\nMvaa 72,176,128\nTa 246,18,18432\nMvaa 72,180,132\nv_mul_u32_u24 v48, 896, v48\nMvaa 104,180,140\nTa 248,19,0\nD0 9,32,46,3072\nMvaa 104,176,136\nTa 250,19,2048\n"
"Mvaa 136,176,144\nv_add_u32 v40, v48, v49\nMvaa 136,180,148\nTa 252,19,16384\ns_add_u32 m0, s47, 81920\ns_add_u32 s46, s51, 262144\nMvaa 168,180,156\nTa 254,19,18432\n"
"Mvaa 168,176,152\nv_xor_b32 v49, 64, v40\nD0 6,32,46,0\nMvaa 172,184,152\nv_add_u32 v41, 6144, v49\n.W565:\ns_cbranch_execz .W569\nMvaa 172,188,156\nv_add_u32 v42, 55296, v40\n"
"Mvaa 140,188,148\nv_add_u32 v43, 61440, v49\nMvaa 140,184,144\ns_mov_b32 s2, s13\nMvaa 108,184,136\ns_lshl3_add_u32 s2, s2, s11\nD0 7,32,46,1024\nMvaa 108,188,140\n"
"s_mul_i32 s52, s2, 14336\nMvaa 76,188,132\ns_lshr_b32 s2, s13, 2\ns_bfe_u32 s3, s13, 0x10001\nMvaa 76,184,128\ns_waitcnt vmcnt(23) lgkmcnt(0)\ns_barrier\nMvaa 80,224,128\n"
"Ra 192,10,16384\ns_mul_i32 s3, s3, 17\nMvaa 80,228,132\ns_add_u32 s2, s2, s3\nD0 8,32,46,2048\nMvaa 112,228,140\nRa 196,11,16384\ns_lshl2_add_u32 s3, s14, 0\nMvaa 112,224,136\n"
"s_lshl_b32 s3, s3, 2\nMvaa 144,224,144\nRa 200,10,18432\ns_lshr_b32 s3, 0xe420ca86, s3\nMvaa 144,228,148\ns_and_b32 s3, s3, 15\nMvaa 176,228,156\nRa 204,11,18432\n"
"s_add_u32 s2, s2, s3\nD0 9,32,46,3072\nMvaa 176,224,152\ns_cmp_eq_u32 s2, 32\ns_cselect_b32 s4, 16, s2\nMvaa 180,232,152\nRa 208,10,24576\ns_mul_i32 s75, s4, 65536\n"
"Mvaa 180,236,156\ns_lshr_b32 s2, s13, 2\ns_add_u32 m0, s47, -65536\ns_add_u32 s46, s52, 0\nMvaa 148,236,148\nRa 212,11,24576\ns_bfe_u32 s3, s13, 0x10001\nMvaa 148,232,144\n"
"s_mul_i32 s3, s3, 17\nD0 40,28,46,0\nMvaa 116,232,136\nRa 216,10,26624\ns_add_u32 s2, s2, s3\nMvaa 116,236,140\ns_lshl2_add_u32 s3, s14, 1\nMvaa 84,236,132\nRa 220,11,26624\n"
"s_lshl_b32 s3, s3, 2\nMvaa 84,232,128\ns_lshr_b32 s3, 0xe420ca86, s3\nMvaa 88,240,128\ns_and_b32 s3, s3, 15\ns_add_u32 s2, s2, s3\nD0 41,28,46,1024\nMvaa 88,244,132\n"
"s_cmp_eq_u32 s2, 32\ns_cselect_b32 s4, 16, s2\nMvaa 120,244,140\ns_mul_i32 s76, s4, 65536\ns_lshr_b32 s2, s13, 2\nMvaa 120,240,136\ns_bfe_u32 s3, s13, 0x10001\nMvaa 152,240,144\n"
"s_mul_i32 s3, s3, 17\ns_add_u32 s2, s2, s3\nMvaa 152,244,148\ns_lshl2_add_u32 s3, s14, 2\nD0 42,28,46,2048\nMvaa 184,244,156\ns_lshl_b32 s3, s3, 2\ns_lshr_b32 s3, 0xe420ca86, s3\n"
"Mvaa 184,240,152\ns_and_b32 s3, s3, 15\nMvaa 188,248,152\ns_add_u32 s2, s2, s3\nMvaa 188,252,156\ns_cmp_eq_u32 s2, 32\ns_cselect_b32 s4, 16, s2\nMvaa 156,252,148\n"
"s_mul_i32 s77, s4, 65536\ns_lshr_b32 s2, s13, 2\nD0 43,28,46,3072\nMvaa 156,248,144\ns_bfe_u32 s3, s13, 0x10001\nMvaa 124,248,136\ns_mul_i32 s3, s3, 17\ns_add_u32 s2, s2, s3\n"
"Mvaa 124,252,140\ns_lshl2_add_u32 s3, s14, 3\nMvaa 92,252,132\ns_lshl_b32 s3, s3, 2\ns_lshr_b32 s3, 0xe420ca86, s3\nMvaa 92,248,128\nWL 0\nMaaa 16,224,192\nRa 128,10,32768\n"
"s_and_b32 s3, s3, 15\ns_add_u32 m0, s47, 0\ns_add_u32 s46, s51, 524288\nD0 6,32,46,0\nMaaa 16,228,196\ns_add_u32 s2, s2, s3\ns_cmp_eq_u32 s2, 32\ns_cselect_b32 s4, 16, s2\n"
"Maaa 48,228,204\nRa 132,11,32768\ns_mul_i32 s78, s4, 65536\nMaaa 48,224,200\ns_lshr_b32 s3, s11, 1\nMaaa 80,224,208\nRa 136,10,34816\ns_lshl_b32 s3, s3, 15\nMaaa 80,228,212\n"
"s_add_u32 s2, s72, s3\nD0 7,32,46,1024\nMaaa 112,228,220\nRa 140,11,34816\ns_and_b32 s3, s11, 1\nMaaa 112,224,216\ns_lshl_b32 s3, s3, 12\nMaaa 116,232,216\nRa 144,10,40960\n"
"s_add_u32 s2, s2, s3\nMaaa 116,236,220\ns_add_u32 s2, s2, 0x0\nMaaa 84,236,212\nRa 148,11,40960\ns_mov_b32 s55, 16384\nD0 8,32,46,2048\nMaaa 84,232,208\ns_add_u32 s75, s75, s2\n"
"Maaa 52,232,200\nRa 152,10,43008\ns_add_u32 s76, s76, s2\nMaaa 52,236,204\ns_add_u32 s77, s77, s2\nMaaa 20,236,196\nRa 156,11,43008\ns_add_u32 s78, s78, s2\nMaaa 20,232,192\n"
"v_add_u32 v32, 0x10000, v12\nD0 9,32,46,3072\nMaaa 24,240,192\nv_add_u32 v33, 0x10000, v13\nv_add_u32 v34, 0x10000, v14\nMaaa 24,244,196\nv_add_u32 v35, 0x10000, v15\n"
"Maaa 56,244,204\nv_add_u32 v36, 0x10000, v16\nv_add_u32 v37, 0x10000, v17\ns_add_u32 m0, s47, 16384\ns_add_u32 s46, s51, 786432\nMaaa 56,240,200\nv_add_u32 v38, 0x10000, v18\n"
"Maaa 88,240,208\nv_add_u32 v39, 0x10000, v19\ns_add_u32 s2, s11, 1\nD0 6,32,46,0\nMaaa 88,244,212\ns_lshr_b32 s2, s2, 1\nMaaa 120,244,220\ns_lshl_b32 s2, s2, 12\n"
"v_add_u32 v44, s2, v10\nMaaa 120,240,216\nv_add_u32 v45, s2, v11\nMaaa 124,248,216\nv_xor_b32 v46, 0x1000, v44\nv_xor_b32 v47, 0x1000, v45\nMaaa 124,252,220\n"
"s_lshr_b32 s3, s11, 1\nD0 7,32,46,1024\nMaaa 92,252,212\ns_lshl_b32 s3, s3, 13\ns_lshl_b32 s2, s11, 12\nMaaa 92,248,208\ns_add_u32 s2, s2, s3\nMaaa 60,248,200\n"
"s_add_u32 s2, s2, s72\ns_lshl_b32 s3, s11, 12\nMaaa 60,252,204\ns_add_u32 s89, s3, 0x10000\nMaaa 28,252,196\ns_mov_b32 s88, 0xffff8000\ns_lshl_b32 s4, s13, 1\nD0 8,32,46,2048\n"
"Maaa 28,248,192\ns_waitcnt vmcnt(25) lgkmcnt(0)\ns_barrier\nMaaa 0,160,192\nTa 224,16,32768\ns_sub_u32 s79, 15, s4\nMaaa 0,164,196\nTa 226,16,34816\nMaaa 32,164,204\n"
"s_add_u32 s96, s2, 0x1310000\ns_mov_b32 s63, 0x53800000\nMaaa 32,160,200\nTa 228,16,49152\nD0 9,32,46,3072\nMaaa 64,160,208\nTa 230,16,51200\ns_mov_b32 s64, 0x2b800000\n"
"Maaa 64,164,212\ns_sub_u32 s98, s4, 1\nMaaa 96,164,220\nTa 232,17,32768\ns_lshl_b32 s5, s13, 5\ns_add_u32 m0, s47, -32768\ns_add_u32 s46, s52, 128\nMaaa 96,160,216\n"
"Ta 234,17,34816\nMaaa 100,168,216\ns_add_u32 s5, s5, s11\ns_lshl_b32 s65, s5, 9\nD0 40,28,46,0\nMaaa 100,172,220\nTa 236,17,49152\nMaaa 68,172,212\nTa 238,17,51200\n"
"s_add_u32 s65, s65, s72\nMaaa 68,168,208\ns_add_u32 s65, s65, 0x1580000\nMaaa 36,168,200\nTa 240,18,32768\ns_and_b32 s66, s72, 0x2000\nMaaa 36,172,204\nTa 242,18,34816\n"
"D0 41,28,46,1024\nMaaa 4,172,196\ns_add_u32 s66, s66, s56\ns_add_u32 s66, s66, 0x100000\nMaaa 4,168,192\nTa 244,18,49152\nMaaa 8,176,192\nTa 246,18,51200\ns_and_b32 s5, s12, 1\n"
"Maaa 8,180,196\ns_mul_i32 s58, s5, 0x202000\nMaaa 40,180,204\nTa 248,19,32768\ns_xor_b32 s58, s58, s72\nD0 42,28,46,2048\nMaaa 40,176,200\nTa 250,19,34816\nMaaa 72,176,208\n"
"s_add_u32 s58, s58, 0x15c0000\ns_lshl_b32 s5, s5, 8\nMaaa 72,180,212\nTa 252,19,49152\nMaaa 104,180,220\nTa 254,19,51200\ns_add_u32 s61, s58, s5\nMaaa 104,176,216\n"
"s_lshl4_add_u32 s60, s13, s61\nD0 43,28,46,3072\nMaaa 108,184,216\ns_add_u32 s54, s60, 128\ns_xor_b32 s60, s54, 0x100\nMaaa 108,188,220\ns_cmp_eq_u32 s11, 0\n"
"s_cselect_b32 s54, s54, 0x40000000\nMaaa 76,188,212\ns_getpc_b64 s[8:9]\n.PC282:\ns_add_u32 s8, s8, .HD268-.PC282\ns_addc_u32 s9, s9, 0\ns_add_u32 m0, s47, -49152\n"
"s_add_u32 s46, s52, 256\nMaaa 76,184,208\ns_mov_b32 s97, 0xff800000\nMaaa 44,184,200\ns_mov_b32 s90, 0\nD0 40,28,46,0\nMaaa 44,188,204\ns_cmp_lt_u32 s11, 2\n"
"s_cselect_b32 s92, 2, 0\nMaaa 12,188,196\ns_mul_i32 s4, s79, 65536\ns_sub_u32 s4, s2, s4\nMaaa 12,184,192\nWL 0\nMvaa 80,224,128\nTa 160,12,32768\ns_add_u32 s94, s4, 0x20000\n"
"Mvaa 80,228,132\nTa 162,12,34816\nD0 41,28,46,1024\nMvaa 84,236,132\ns_add_u32 s95, s4, 0x410000\ns_add_u32 s86, s2, 0x130000\nMvaa 84,232,128\nTa 164,12,49152\nMvaa 88,240,128\n"
"Ta 166,12,51200\ns_add_u32 s87, s2, 0x520000\nMvaa 88,244,132\ns_sub_u32 s99, s79, 2\ns_cmp_eq_u32 s13, 7\ns_cselect_b32 s93, 1.0, 0\nMvaa 92,252,132\nTa 168,13,32768\n"
"s_mov_b32 s36, s68\nD0 42,28,46,2048\nMvaa 92,248,128\nTa 170,13,34816\nMvaa 124,248,136\ns_and_b32 s37, s69, 0xffff\ns_mov_b32 s38, 0x1c0000\nMvaa 124,252,140\nTa 172,13,49152\n"
"CV 200,80,81\nMvaa 120,244,140\nTa 174,13,51200\ns_mov_b32 s39, 0x20000\nMvaa 120,240,136\ns_and_b32 s2, s74, 15\nD0 43,28,46,3072\nMvaa 116,232,136\nTa 176,14,32768\n"
"s_min_u32 s2, s2, 13\nMvaa 116,236,140\nTa 178,14,34816\nMvaa 112,228,140\ns_sub_u32 s2, s2, 0\ns_mul_i32 s3, s73, 14\nMvaa 112,224,136\nTa 180,14,49152\nCV 201,82,83\n"
"Mvaa 144,224,144\nTa 182,14,51200\ns_add_u32 s2, s2, s3\nMvaa 144,228,148\ns_lshl2_add_u32 s2, s2, s11\nMvaa 148,236,148\nTa 184,15,32768\ns_mul_i32 s3, s2, 4682\n"
"Mvaa 148,232,144\nTa 186,15,34816\nCV 202,84,85\nMvaa 152,240,144\ns_lshr_b32 s3, s3, 16\ns_mul_i32 s22, s3, 14\nMvaa 152,244,148\nTa 188,15,49152\nCV 203,86,87\nMvaa 156,252,148\n"
"Ta 190,15,51200\ns_sub_u32 s22, s2, s22\nMvaa 156,248,144\ns_and_b32 s21, s3, 28\nMvaa 188,248,152\ns_add_u32 s21, s21, s3\ns_lshl_b32 s21, s21, 4\nMvaa 188,252,156\n"
"s_bfe_u32 s41, s3, 0x30002\ns_cmp_gt_u32 s22, 6\ns_cselect_b32 s2, 1179648, 0\ns_cselect_b32 s1, 16, 0\nMvaa 184,244,156\ns_add_u32 s41, s41, s1\n"
"s_lshl4_add_u32 s41, s41, 0xc048480\nMvaa 184,240,152\ns_and_b32 s1, s3, 28\nCV 204,88,89\nMvaa 180,232,152\ns_add_u32 s3, s3, s1\ns_lshl_b32 s3, s3, 11\nMvaa 180,236,156\n"
"s_add_u32 s2, s2, s3\nMvaa 176,228,156\ns_lshl_b32 s3, s22, 17\ns_add_u32 s2, s2, s3\nMvaa 176,224,152\ns_waitcnt vmcnt(16) lgkmcnt(0)\ns_barrier\nMvaa 64,160,128\n"
"Ra 192,10,49152\ns_add_u32 s20, s2, 0x10e0000\ns_add_u32 m0, s47, 32768\ns_add_u32 s46, s51, 1048576\nMvaa 64,164,132\ns_lshl_b32 s2, s22, 7\nMvaa 68,172,132\nRa 196,11,49152\n"
"s_add_u32 s40, s2, 0xc04b800\nD0 6,32,46,0\nMvaa 68,168,128\ns_mul_i32 s2, s21, 896\nCV 205,90,91\nMvaa 72,176,128\nRa 200,10,51200\ns_lshl_b32 s3, s22, 6\nMvaa 72,180,132\n"
"s_add_u32 s22, s2, s3\nCV 206,92,93\nMvaa 76,188,132\nRa 204,11,51200\ns_add_u32 s23, s22, 57344\nMvaa 76,184,128\nD0 7,32,46,1024\nMvaa 108,184,136\nRa 208,10,57344\n"
"CV 207,94,95\nMvaa 108,188,140\nCV 220,120,121\nCV 221,122,123\nMvaa 104,180,140\nRa 212,11,57344\nMvaa 104,176,136\nCV 222,124,125\nCV 223,126,127\nMvaa 100,168,136\n"
"Ra 216,10,59392\nD0 8,32,46,2048\ns_lshl3_add_u32 s5, s12, s13\ns_lshl2_add_u32 s5, s5, s11\ns_lshl2_add_u32 s5, s5, 0xc04a400\nbuffer_load_dword v27, off, s[16:19], s5 sc0 sc1\n"
"Mvaa 100,172,140\nCV 216,112,113\nMvaa 96,164,140\nRa 220,11,59392\nCV 217,114,115\nMvaa 96,160,136\nCV 218,116,117\nCV 219,118,119\nMvaa 128,160,144\nCV 192,64,65\nCV 193,66,67\n"
"Mvaa 128,164,148\nD0 9,32,46,3072\nMvaa 132,172,148\nCV 194,68,69\nCV 195,70,71\nMvaa 132,168,144\nCV 196,72,73\nCV 197,74,75\nMvaa 136,176,144\ns_add_u32 m0, s47, 49152\n"
"s_add_u32 s46, s51, 1310720\nMvaa 136,180,148\nCV 198,76,77\nCV 199,78,79\nMvaa 140,188,148\nD0 6,32,46,0\nCV 212,104,105\nMvaa 140,184,144\nCV 213,106,107\nMvaa 172,184,152\n"
"CV 214,108,109\nCV 215,110,111\nMvaa 172,188,156\nCV 208,96,97\nCV 209,98,99\nMvaa 168,180,156\nCV 210,100,101\nMvaa 168,176,152\nD0 7,32,46,1024\nCV 211,102,103\n"
"Mvaa 164,168,152\nCV 232,144,145\nMvaa 164,172,156\nCV 233,146,147\nSv 192,1,16,75,0\nMvaa 160,164,156\nCV 234,148,149\nMvaa 160,160,152\nWL 0\nMaaa 0,160,192\nD0 8,32,46,2048\n"
"Ra 128,44,0\nMaaa 0,164,196\nCV 235,150,151\nMaaa 4,172,196\nRa 132,45,0\nMaaa 4,168,192\nCV 236,152,153\nMaaa 8,176,192\nRa 136,44,2048\nMaaa 8,180,196\nD0 9,32,46,3072\n"
"Maaa 12,188,196\nRa 140,45,2048\nSv 196,1,16,75,1024\nMaaa 12,184,192\nCV 237,154,155\nMaaa 44,184,200\nRa 144,46,0\nMaaa 44,188,204\nCV 238,156,157\nMaaa 40,180,204\nRa 148,47,0\n"
"Maaa 40,176,200\nCV 239,158,159\nMaaa 36,168,200\nRa 152,46,2048\nMaaa 36,172,204\nCV 252,184,185\nMaaa 32,164,204\nRa 156,47,2048\nMaaa 32,160,200\nCV 253,186,187\n"
"Sv 200,1,16,75,2048\nMaaa 64,160,208\nCV 254,188,189\nCV 255,190,191\nMaaa 64,164,212\nCV 248,176,177\nMaaa 68,172,212\nCV 249,178,179\nMaaa 68,168,208\nCV 250,180,181\n"
"Maaa 72,176,208\nCV 251,182,183\nMaaa 72,180,212\nCV 224,128,129\nMaaa 76,188,212\nCV 225,130,131\nMaaa 76,184,208\nCV 226,132,133\nMaaa 108,184,216\nCV 227,134,135\n"
"Sv 204,1,16,75,3072\nMaaa 108,188,220\nCV 228,136,137\nMaaa 104,180,220\nCV 229,138,139\nMaaa 104,176,216\nCV 230,140,141\nMaaa 100,168,216\nCV 231,142,143\nMaaa 100,172,220\n"
"CV 244,168,169\nMaaa 96,164,220\nCV 245,170,171\nMaaa 96,160,216\ns_barrier\nMaaa 16,224,192\ns_add_u32 m0, s47, -16384\ns_add_u32 s46, s52, 384\nTa 160,32,0\nMaaa 16,228,196\n"
"Ta 162,32,2048\nMaaa 20,236,196\nD0 40,28,46,0\ns_add_u32 s75, s75, s55\nSv 208,1,16,75,0\nMaaa 20,232,192\nTa 164,32,16384\nMaaa 24,240,192\nTa 166,32,18432\nMaaa 24,244,196\n"
"CV 246,172,173\nMaaa 28,252,196\nTa 168,33,0\nMaaa 28,248,192\nD0 41,28,46,1024\nTa 170,33,2048\nMaaa 60,248,200\nCV 247,174,175\nMaaa 60,252,204\n.W566:\ns_cbranch_execz .W570\n"
"Ta 172,33,16384\nMaaa 56,244,204\nTa 174,33,18432\nMaaa 56,240,200\nCV 240,160,161\nSv 212,1,16,75,1024\nMaaa 52,232,200\nD0 42,28,46,2048\nTa 176,34,0\nMaaa 52,236,204\n"
"Ta 178,34,2048\nMaaa 48,228,204\nCV 241,162,163\nMaaa 48,224,200\nTa 180,34,16384\nMaaa 80,224,208\nTa 182,34,18432\nCV 242,164,165\nMaaa 80,228,212\nD0 43,28,46,3072\n"
"Maaa 84,236,212\nTa 184,35,0\nMaaa 84,232,208\nTa 186,35,2048\nMaaa 88,240,208\nCV 243,166,167\nSv 216,1,16,75,2048\nMaaa 88,244,212\nTa 188,35,16384\nMaaa 92,252,212\n"
"Ta 190,35,18432\nMaaa 92,248,208\nMaaa 124,248,216\nMaaa 124,252,220\nMaaa 120,244,220\nMaaa 120,240,216\nMaaa 116,232,216\nMaaa 116,236,220\nSv 220,1,16,75,3072\n"
"Maaa 112,228,220\nMaaa 112,224,216\nWL 0\nZvaa 64,160,128\nTa 224,36,0\nAR 60,0\nMvaa 64,164,132\nTa 226,36,2048\nZvaa 96,164,140\nAR 61,1\nMvaa 96,160,136\nTa 228,36,16384\n"
"CV 192,60,61\nZvaa 128,160,144\nTa 230,36,18432\nMvaa 128,164,148\nAR 60,2\nAR 61,3\nZvaa 160,164,156\nTa 232,37,0\nSv 224,1,16,76,0\nMvaa 160,160,152\nTa 234,37,2048\n"
"CV 193,60,61\nZvaa 164,168,152\nAR 60,4\nMvaa 164,172,156\nTa 236,37,16384\nZvaa 132,172,148\nTa 238,37,18432\nAR 61,5\nMvaa 132,168,144\nCV 194,60,61\nZvaa 100,168,136\n"
"Ta 240,38,0\nAR 60,6\nMvaa 100,172,140\nTa 242,38,2048\nZvaa 68,172,132\nSv 228,1,16,76,1024\nAR 61,7\nMvaa 68,168,128\nTa 244,38,16384\nZvaa 72,176,128\nTa 246,38,18432\n"
"CV 195,60,61\nMvaa 72,180,132\nAR 60,8\nZvaa 104,180,140\nTa 248,39,0\nMvaa 104,176,136\nTa 250,39,2048\nAR 61,9\nZvaa 136,176,144\nCV 196,60,61\nMvaa 136,180,148\n"
"Ta 252,39,16384\nAR 60,10\nZvaa 168,180,156\nTa 254,39,18432\nSv 232,1,16,76,2048\nMvaa 168,176,152\nAR 61,11\nCV 197,60,61\nZvaa 172,184,152\nAR 60,12\nMvaa 172,188,156\n"
"AR 61,13\nZvaa 140,188,148\nCV 198,60,61\nAR 60,14\nMvaa 140,184,144\nAR 61,15\nZvaa 108,184,136\nCV 199,60,61\nAR 60,16\nMvaa 108,188,140\nSv 236,1,16,76,3072\nZvaa 76,188,132\n"
"AR 61,17\nCV 200,60,61\nMvaa 76,184,128\ns_waitcnt vmcnt(29) lgkmcnt(0)\ns_barrier\nZvaa 80,224,128\nRa 192,44,32768\nAR 60,18\nMvaa 80,228,132\nRa 196,45,32768\n"
"s_add_u32 m0, s47, 65536\ns_add_u32 s46, s51, 1572864\nD0 6,32,46,0\nZvaa 112,228,140\nRa 200,44,34816\nMvaa 112,224,136\nRa 204,45,34816\nAR 61,19\nZvaa 144,224,144\n"
"Ra 208,46,32768\nMvaa 144,228,148\nRa 212,47,32768\nCV 201,60,61\nD0 7,32,46,1024\nZvaa 176,228,156\nRa 216,46,34816\nMvaa 176,224,152\nRa 220,47,34816\nAR 60,20\n"
"Zvaa 180,232,152\nTa 160,12,0\nMvaa 180,236,156\nTa 162,12,2048\nD0 8,32,46,2048\nZvaa 148,236,148\nTa 164,12,16384\nAR 61,21\nMvaa 148,232,144\nTa 166,12,18432\nZvaa 116,232,136\n"
"Ta 168,13,0\nCV 202,60,61\nMvaa 116,236,140\nTa 170,13,2048\nD0 9,32,46,3072\nZvaa 84,236,132\nTa 172,13,16384\nAR 60,22\nMvaa 84,232,128\nTa 174,13,18432\n"
"s_add_u32 m0, s47, 81920\ns_add_u32 s46, s51, 1835008\nZvaa 88,240,128\nTa 176,14,0\nAR 61,23\nMvaa 88,244,132\nTa 178,14,2048\nD0 6,32,46,0\nZvaa 120,244,140\nTa 180,14,16384\n"
"Mvaa 120,240,136\nTa 182,14,18432\nCV 203,60,61\nZvaa 152,240,144\nTa 184,15,0\nMvaa 152,244,148\nTa 186,15,2048\nAR 60,24\nD0 7,32,46,1024\nZvaa 184,244,156\nTa 188,15,16384\n"
"Mvaa 184,240,152\nTa 190,15,18432\nAR 61,25\nZvaa 188,248,152\nCV 204,60,61\nMvaa 188,252,156\nAR 60,26\nD0 8,32,46,2048\nZvaa 156,252,148\nAR 61,27\nCV 205,60,61\n"
"Mvaa 156,248,144\nAR 60,28\nZvaa 124,248,136\nAR 61,29\nCV 206,60,61\nMvaa 124,252,140\nAR 60,30\nD0 9,32,46,3072\nZvaa 92,252,132\nAR 61,31\nCV 207,60,61\nMvaa 92,248,128\nWL 0\n"
"Mvaa 64,160,192\nTa 224,16,0\nAR 60,32\nMvaa 64,164,196\nTa 226,16,2048\ns_add_u32 m0, s47, -65536\ns_add_u32 s46, s52, 512\nD0 40,28,46,0\nMvaa 96,164,204\nAR 61,33\n"
"Mvaa 96,160,200\nTa 228,16,16384\nCV 208,60,61\nMvaa 128,160,208\nTa 230,16,18432\nMvaa 128,164,212\nAR 60,34\nAR 61,35\nD0 41,28,46,1024\nMvaa 160,164,220\nTa 232,17,0\n"
"Mvaa 160,160,216\nTa 234,17,2048\nCV 209,60,61\nMvaa 164,168,216\nAR 60,36\nMvaa 164,172,220\nTa 236,17,16384\nD0 42,28,46,2048\nMvaa 132,172,212\nTa 238,17,18432\nAR 61,37\n"
"Mvaa 132,168,208\nCV 210,60,61\nMvaa 100,168,200\nTa 240,18,0\nAR 60,38\nMvaa 100,172,204\nTa 242,18,2048\nD0 43,28,46,3072\nMvaa 68,172,196\ns_add_u32 s76, s76, s55\n"
"Sv 240,1,16,76,0\nMvaa 68,168,192\nTa 244,18,16384\nMvaa 72,176,192\nTa 246,18,18432\nAR 61,39\nMvaa 72,180,196\nCV 211,60,61\nMvaa 104,180,204\nTa 248,19,0\nMvaa 104,176,200\n"
"Ta 250,19,2048\nSv 244,1,16,76,1024\nMvaa 136,176,208\nAR 60,40\nMvaa 136,180,212\nTa 252,19,16384\nAR 61,41\nMvaa 168,180,220\nTa 254,19,18432\nMvaa 168,176,216\nCV 212,60,61\n"
"AR 60,42\nMvaa 172,184,216\nSv 248,1,16,76,2048\nMvaa 172,188,220\nAR 61,43\nMvaa 140,188,212\nCV 213,60,61\nAR 60,44\nMvaa 140,184,208\nAR 61,45\nMvaa 108,184,200\nCV 214,60,61\n"
"AR 60,46\nMvaa 108,188,204\nSv 252,1,16,76,3072\nMvaa 76,188,196\nAR 61,47\nCV 215,60,61\nMvaa 76,184,192\ns_waitcnt vmcnt(20) lgkmcnt(0)\ns_barrier\nMvaa 80,224,192\n"
"Ra 128,44,16384\nAR 60,48\nv_cmp_ne_u32 vcc, s15, v27\ns_nop 0\ns_cbranch_vccnz .S283\n.S284:\nMvaa 80,228,196\nRa 132,45,16384\n"
"buffer_store_dword v25, off, s[16:19], s54 offset:0 sc0 sc1\ns_add_u32 m0, s47, 0\ns_add_u32 s46, s51, 2097152\nD0 6,32,46,0\nMvaa 112,228,204\nRa 136,44,18432\nMvaa 112,224,200\n"
"Ra 140,45,18432\nAR 61,49\nMvaa 144,224,208\nRa 144,46,16384\nMvaa 144,228,212\nRa 148,47,16384\nCV 216,60,61\nD0 7,32,46,1024\nMvaa 176,228,220\nRa 152,46,18432\n"
"Mvaa 176,224,216\nRa 156,47,18432\nAR 60,50\nMvaa 180,232,216\nTa 160,12,32768\nMvaa 180,236,220\nTa 162,12,34816\nD0 8,32,46,2048\nMvaa 148,236,212\nTa 164,12,49152\nAR 61,51\n"
"Mvaa 148,232,208\nTa 166,12,51200\nMvaa 116,232,200\nTa 168,13,32768\nCV 217,60,61\nMvaa 116,236,204\nTa 170,13,34816\nD0 9,32,46,3072\nMvaa 84,236,196\nTa 172,13,49152\nAR 60,52\n"
"Mvaa 84,232,192\nTa 174,13,51200\ns_add_u32 m0, s47, 16384\ns_add_u32 s46, s51, 2359296\nMvaa 88,240,192\nTa 176,14,32768\nAR 61,53\nMvaa 88,244,196\nTa 178,14,34816\n"
"D0 6,32,46,0\nMvaa 120,244,204\nTa 180,14,49152\nMvaa 120,240,200\nTa 182,14,51200\nCV 218,60,61\nMvaa 152,240,208\nTa 184,15,32768\nMvaa 152,244,212\nTa 186,15,34816\nAR 60,54\n"
"D0 7,32,46,1024\nMvaa 184,244,220\nTa 188,15,49152\nMvaa 184,240,216\nTa 190,15,51200\nAR 61,55\nMvaa 188,248,216\nCV 219,60,61\nMvaa 188,252,220\nAR 60,56\nD0 8,32,46,2048\n"
"Mvaa 156,252,212\nAR 61,57\nCV 220,60,61\nMvaa 156,248,208\nAR 60,58\nMvaa 124,248,200\nAR 61,59\nCV 221,60,61\nMvaa 124,252,204\nAR 60,60\nD0 9,32,46,3072\nMvaa 92,252,196\n"
"AR 61,61\nCV 222,60,61\nMvaa 92,248,192\nWL 0\nMvaa 64,160,128\nTa 224,16,32768\nAR 60,62\nMvaa 64,164,132\nTa 226,16,34816\ns_add_u32 m0, s47, -32768\ns_add_u32 s46, s52, 640\n"
"D0 40,28,46,0\nMvaa 96,164,140\nAR 61,63\nMvaa 96,160,136\nTa 228,16,49152\nCV 223,60,61\nMvaa 128,160,144\nTa 230,16,51200\nMvaa 128,164,148\nD0 41,28,46,1024\nMvaa 160,164,156\n"
"Ta 232,17,32768\nAR 60,126\nMvaa 160,160,152\nTa 234,17,34816\nMvaa 164,168,152\nAR 61,127\nCV 60,60,61\nMvaa 164,172,156\nTa 236,17,49152\nD0 42,28,46,2048\nMvaa 132,172,148\n"
"Ta 238,17,51200\nMvaa 132,168,144\nAW 127,60\nAR 60,124\nMvaa 100,168,136\nTa 240,18,32768\nMvaa 100,172,140\nTa 242,18,34816\nD0 43,28,46,3072\nMvaa 68,172,132\nSv 192,1,16,77,0\n"
"AR 61,125\nMvaa 68,168,128\nTa 244,18,49152\nMvaa 72,176,128\nTa 246,18,51200\nCV 60,60,61\nMvaa 72,180,132\nAW 126,60\nMvaa 104,180,140\nTa 248,19,32768\nMvaa 104,176,136\n"
"Ta 250,19,34816\nSv 196,1,16,77,1024\nMvaa 136,176,144\nAR 60,122\nMvaa 136,180,148\nTa 252,19,49152\nMvaa 168,180,156\nTa 254,19,51200\nAR 61,123\nMvaa 168,176,152\nCV 60,60,61\n"
"Mvaa 172,184,152\nSv 200,1,16,77,2048\nAW 125,60\nMvaa 172,188,156\nAR 60,120\nMvaa 140,188,148\nAR 61,121\nMvaa 140,184,144\nCV 60,60,61\nAW 124,60\nMvaa 108,184,136\nAR 60,118\n"
"Mvaa 108,188,140\nSv 204,1,16,77,3072\nMvaa 76,188,132\nAR 61,119\nCV 60,60,61\nMvaa 76,184,128\ns_waitcnt vmcnt(17) lgkmcnt(0)\ns_barrier\nMvaa 80,224,128\nRa 192,44,49152\n"
"AW 123,60\nMvaa 80,228,132\nRa 196,45,49152\nbuffer_store_dword v25, off, s[16:19], s54 offset:8 sc0 sc1\ns_add_u32 m0, s47, 32768\ns_add_u32 s46, s51, 2621440\nD0 6,32,46,0\n"
"Mvaa 112,228,140\nRa 200,44,51200\nMvaa 112,224,136\nRa 204,45,51200\nAR 60,116\nMvaa 144,224,144\nRa 208,46,49152\nMvaa 144,228,148\nRa 212,47,49152\nD0 7,32,46,1024\n"
"Mvaa 176,228,156\nRa 216,46,51200\nAR 61,117\nMvaa 176,224,152\nRa 220,47,51200\nMvaa 180,232,152\nTa 160,32,0\nCV 60,60,61\nMvaa 180,236,156\nTa 162,32,2048\nD0 8,32,46,2048\n"
"Mvaa 148,236,148\nTa 164,32,16384\nMvaa 148,232,144\nTa 166,32,18432\nAW 122,60\nMvaa 116,232,136\nTa 168,33,0\nMvaa 116,236,140\nTa 170,33,2048\nD0 9,32,46,3072\nMvaa 84,236,132\n"
"Ta 172,33,16384\nAR 60,114\nMvaa 84,232,128\nTa 174,33,18432\n.W567:\ns_cbranch_execz .W571\ns_add_u32 m0, s47, 49152\ns_add_u32 s46, s51, 2883584\nMvaa 88,240,128\nTa 176,34,0\n"
"AR 61,115\nMvaa 88,244,132\nTa 178,34,2048\nD0 6,32,46,0\nMvaa 120,244,140\nTa 180,34,16384\nMvaa 120,240,136\nTa 182,34,18432\nCV 60,60,61\nMvaa 152,240,144\nTa 184,35,0\n"
"Mvaa 152,244,148\nTa 186,35,2048\nD0 7,32,46,1024\nMvaa 184,244,156\nTa 188,35,16384\nAW 121,60\nMvaa 184,240,152\nTa 190,35,18432\nMvaa 188,248,152\nAR 60,112\nAR 61,113\n"
"Mvaa 188,252,156\nD0 8,32,46,2048\nMvaa 156,252,148\nCV 60,60,61\nMvaa 156,248,144\nAW 120,60\nAR 60,110\nMvaa 124,248,136\nAR 61,111\nMvaa 124,252,140\nD0 9,32,46,3072\n"
"Mvaa 92,252,132\nCV 60,60,61\nAW 119,60\nMvaa 92,248,128\nWL 0\nMvaa 64,160,192\nTa 224,36,0\nAR 60,108\nMvaa 64,164,196\nTa 226,36,2048\ns_add_u32 m0, s47, -49152\n"
"s_add_u32 s46, s52, 768\nD0 40,28,46,0\nMvaa 96,164,204\nAR 61,109\nMvaa 96,160,200\nTa 228,36,16384\nCV 60,60,61\nMvaa 128,160,208\nTa 230,36,18432\nMvaa 128,164,212\n"
"D0 41,28,46,1024\nMvaa 160,164,220\nTa 232,37,0\nAW 118,60\nMvaa 160,160,216\nTa 234,37,2048\nMvaa 164,168,216\nAR 60,106\nAR 61,107\nMvaa 164,172,220\nTa 236,37,16384\n"
"D0 42,28,46,2048\nMvaa 132,172,212\nTa 238,37,18432\nMvaa 132,168,208\nCV 60,60,61\nAW 117,60\nMvaa 100,168,200\nTa 240,38,0\nMvaa 100,172,204\nTa 242,38,2048\nD0 43,28,46,3072\n"
"Mvaa 68,172,196\ns_add_u32 s77, s77, s55\nSv 208,1,16,77,0\nMvaa 68,168,192\nTa 244,38,16384\nMvaa 72,176,192\nTa 246,38,18432\nAR 60,104\nMvaa 72,180,196\nAR 61,105\n"
"Mvaa 104,180,204\nTa 248,39,0\nMvaa 104,176,200\nTa 250,39,2048\nSv 212,1,16,77,1024\nMvaa 136,176,208\nCV 60,60,61\nMvaa 136,180,212\nTa 252,39,16384\nMvaa 168,180,220\n"
"Ta 254,39,18432\nAW 116,60\nMvaa 168,176,216\nAR 60,102\nMvaa 172,184,216\nSv 216,1,16,77,2048\nAR 61,103\nMvaa 172,188,220\nCV 60,60,61\nMvaa 140,188,212\nAW 115,60\n"
"Mvaa 140,184,208\nAR 60,100\nAR 61,101\nMvaa 108,184,200\nCV 60,60,61\nMvaa 108,188,204\nSv 220,1,16,77,3072\nMvaa 76,188,196\nAW 114,60\nAR 60,98\nMvaa 76,184,192\n"
"s_waitcnt vmcnt(25) lgkmcnt(0)\ns_barrier\nMvaa 80,224,192\nRa 128,44,0\nv_and_b32 v27, 0x70, v1\nbuffer_load_dword v27, v27, s[16:19], s61 offen offset:128 sc0 sc1\n"
"Mvaa 80,228,196\nRa 132,45,0\ns_add_u32 m0, s47, 65536\ns_add_u32 s46, s51, 3145728\nD0 6,32,46,0\nMvaa 112,228,204\nRa 136,44,2048\nMvaa 112,224,200\nRa 140,45,2048\nAR 61,99\n"
"Mvaa 144,224,208\nRa 144,46,0\nMvaa 144,228,212\nRa 148,47,0\nD0 7,32,46,1024\nMvaa 176,228,220\nRa 152,46,2048\nCV 60,60,61\nMvaa 176,224,216\nRa 156,47,2048\nMvaa 180,232,216\n"
"Ta 160,12,0\nAW 113,60\nMvaa 180,236,220\nTa 162,12,2048\nD0 8,32,46,2048\nMvaa 148,236,212\nTa 164,12,16384\nMvaa 148,232,208\nTa 166,12,18432\nAR 60,96\nMvaa 116,232,200\n"
"Ta 168,13,0\nMvaa 116,236,204\nTa 170,13,2048\nD0 9,32,46,3072\nMvaa 84,236,196\nTa 172,13,16384\nAR 61,97\nMvaa 84,232,192\nTa 174,13,18432\ns_add_u32 m0, s47, 81920\n"
"s_add_u32 s46, s51, 3407872\nMvaa 88,240,192\nTa 176,14,0\nCV 60,60,61\nMvaa 88,244,196\nTa 178,14,2048\nD0 6,32,46,0\nMvaa 120,244,204\nTa 180,14,16384\nMvaa 120,240,200\n"
"Ta 182,14,18432\nAW 112,60\nMvaa 152,240,208\nTa 184,15,0\nMvaa 152,244,212\nTa 186,15,2048\nD0 7,32,46,1024\nMvaa 184,244,220\nTa 188,15,16384\nAR 60,94\nMvaa 184,240,216\n"
"Ta 190,15,18432\nMvaa 188,248,216\nAR 61,95\nCV 60,60,61\nMvaa 188,252,220\nD0 8,32,46,2048\nMvaa 156,252,212\nAW 111,60\nMvaa 156,248,208\nAR 60,92\nAR 61,93\nMvaa 124,248,200\n"
"CV 60,60,61\nMvaa 124,252,204\nD0 9,32,46,3072\nMvaa 92,252,196\nAW 110,60\nAR 60,90\nMvaa 92,248,192\nWL 0\nMvaa 64,160,128\nTa 224,16,0\nAR 61,91\nMvaa 64,164,132\n"
"Ta 226,16,2048\ns_add_u32 s6, s56, 0\nbuffer_load_dwordx4 a[0:3], v1, s[16:19], s6 offen offset:0 sc1\nMvaa 96,164,140\nCV 60,60,61\nMvaa 96,160,136\nTa 228,16,16384\nAW 109,60\n"
"Mvaa 128,160,144\nTa 230,16,18432\nMvaa 128,164,148\nbuffer_load_dwordx4 a[4:7], v1, s[16:19], s6 offen offset:1024 sc1\nMvaa 160,164,156\nTa 232,17,0\nAR 60,88\nMvaa 160,160,152\n"
"Ta 234,17,2048\nMvaa 164,168,152\nAR 61,89\nCV 60,60,61\nMvaa 164,172,156\nTa 236,17,16384\nbuffer_load_dwordx4 a[8:11], v1, s[16:19], s6 offen offset:2048 sc1\nMvaa 132,172,148\n"
"Ta 238,17,18432\nMvaa 132,168,144\nAW 108,60\nAR 60,86\nMvaa 100,168,136\nTa 240,18,0\nMvaa 100,172,140\nTa 242,18,2048\n"
"buffer_load_dwordx4 a[12:15], v1, s[16:19], s6 offen offset:3072 sc1\nMvaa 68,172,132\nAR 61,87\nCV 60,60,61\nMvaa 68,168,128\nTa 244,18,16384\nMvaa 72,176,128\nTa 246,18,18432\n"
"AW 107,60\nMvaa 72,180,132\nAR 60,84\nMvaa 104,180,140\nTa 248,19,0\nMvaa 104,176,136\nTa 250,19,2048\nAR 61,85\nMvaa 136,176,144\nCV 60,60,61\nMvaa 136,180,148\nTa 252,19,16384\n"
"Mvaa 168,180,156\nTa 254,19,18432\nAW 106,60\nMvaa 168,176,152\nAR 60,82\nMvaa 172,184,152\nAR 61,83\nCV 60,60,61\nMvaa 172,188,156\nAW 105,60\nMvaa 140,188,148\nAR 60,80\n"
"Mvaa 140,184,144\nAR 61,81\nCV 60,60,61\nMvaa 108,184,136\nAW 104,60\nMvaa 108,188,140\nAR 60,78\nMvaa 76,188,132\nAR 61,79\nCV 60,60,61\nMvaa 76,184,128\n"
"s_waitcnt vmcnt(12) lgkmcnt(0)\ns_barrier\nMvaa 80,224,128\nRa 192,44,32768\nv_cmp_ne_u32 vcc, s15, v27\ns_nop 0\ns_cbranch_vccnz .S286\n.S287:\ns_sub_u32 s45, s86, 131072\n"
"s_cmp_eq_u32 s79, 1\ns_cselect_b32 s86, s94, s86\ns_add_u32 m0, s47, -65536\ns_add_u32 s46, s45, 0x0\nMvaa 80,228,132\nRa 196,45,32768\nMvaa 112,228,140\nRa 200,44,34816\n"
"D1 1,16,46,0\nMvaa 112,224,136\nRa 204,45,34816\nAW 103,60\nMvaa 144,224,144\nRa 208,46,32768\nMvaa 144,228,148\nRa 212,47,32768\nMvaa 176,228,156\nRa 216,46,34816\n"
"D1 1,16,46,1024\nMvaa 176,224,152\nRa 220,47,34816\nMvaa 180,232,152\nTa 160,12,32768\nAR 60,76\nMvaa 180,236,156\nTa 162,12,34816\nMvaa 148,236,148\nTa 164,12,49152\n"
"D1 1,16,46,2048\nMvaa 148,232,144\nTa 166,12,51200\nAR 61,77\nMvaa 116,232,136\nTa 168,13,32768\nMvaa 116,236,140\nTa 170,13,34816\nMvaa 84,236,132\nTa 172,13,49152\n"
"D1 1,16,46,3072\nMvaa 84,232,128\nTa 174,13,51200\nMvaa 88,240,128\nTa 176,14,32768\ns_add_u32 s6, s56, 4096\nMvaa 88,244,132\nTa 178,14,34816\nMvaa 120,244,140\nTa 180,14,49152\n"
"buffer_load_dwordx4 a[16:19], v1, s[16:19], s6 offen offset:0 sc1\nMvaa 120,240,136\nTa 182,14,51200\nCV 60,60,61\nMvaa 152,240,144\nTa 184,15,32768\nMvaa 152,244,148\n"
"Ta 186,15,34816\nMvaa 184,244,156\nTa 188,15,49152\nbuffer_load_dwordx4 a[20:23], v1, s[16:19], s6 offen offset:1024 sc1\nMvaa 184,240,152\nTa 190,15,51200\nMvaa 188,248,152\n"
"AW 102,60\nAR 60,74\nMvaa 188,252,156\nAR 61,75\nMvaa 156,252,148\nbuffer_load_dwordx4 a[24:27], v1, s[16:19], s6 offen offset:2048 sc1\nMvaa 156,248,144\nCV 60,60,61\nAW 101,60\n"
"Mvaa 124,248,136\nAR 60,72\nMvaa 124,252,140\nAR 61,73\nMvaa 92,252,132\nbuffer_load_dwordx4 a[28:31], v1, s[16:19], s6 offen offset:3072 sc1\nCV 60,60,61\nMvaa 92,248,128\nWL 0\n"
"Mvaa 64,160,192\nTa 224,16,32768\ns_add_u32 s6, s56, 16384\nMvaa 64,164,196\nTa 226,16,34816\nMvaa 96,164,204\nbuffer_load_dwordx4 a[32:35], v1, s[16:19], s6 offen offset:0 sc1\n"
"Mvaa 96,160,200\nTa 228,16,49152\nAW 100,60\nMvaa 128,160,208\nTa 230,16,51200\nMvaa 128,164,212\nAR 60,70\nMvaa 160,164,220\nTa 232,17,32768\n"
"buffer_load_dwordx4 a[36:39], v1, s[16:19], s6 offen offset:1024 sc1\nMvaa 160,160,216\nTa 234,17,34816\nMvaa 164,168,216\nAR 61,71\nCV 60,60,61\nMvaa 164,172,220\n"
"Ta 236,17,49152\nMvaa 132,172,212\nTa 238,17,51200\nbuffer_load_dwordx4 a[40:43], v1, s[16:19], s6 offen offset:2048 sc1\nMvaa 132,168,208\nAW 99,60\nAR 60,68\nMvaa 100,168,200\n"
"Ta 240,18,32768\nMvaa 100,172,204\nTa 242,18,34816\nMvaa 68,172,196\nbuffer_load_dwordx4 a[44:47], v1, s[16:19], s6 offen offset:3072 sc1\nAR 61,69\nMvaa 68,168,192\n"
"Ta 244,18,49152\nMvaa 72,176,192\nTa 246,18,51200\nCV 60,60,61\nMvaa 72,180,196\nAW 98,60\nMvaa 104,180,204\nTa 248,19,32768\nMvaa 104,176,200\nTa 250,19,34816\nAR 60,66\n"
"Mvaa 136,176,208\nAR 61,67\nMvaa 136,180,212\nTa 252,19,49152\nMvaa 168,180,220\nTa 254,19,51200\nCV 60,60,61\nMvaa 168,176,216\nAW 97,60\nMvaa 172,184,216\nAR 60,64\nAR 61,65\n"
"Mvaa 172,188,220\nCV 60,60,61\nMvaa 140,188,212\nAW 96,60\nv_add_u32 v2, 0x0, v1\nMvaa 140,184,208\ns_lshr_b32 s2, s59, 1\nMvaa 108,184,200\nv_add_u32 v24, s2, v2\n"
"v_xor_b32 v26, 0x1000, v24\nMvaa 108,188,204\nv_lshrrev_b32 v20, 4, v0\nMvaa 76,188,196\nv_lshlrev_b32 v20, 2, v20\nv_and_b32 v0, 15, v0\nMvaa 76,184,192\n"
"s_waitcnt vmcnt(16) lgkmcnt(0)\ns_barrier\nMvaa 80,224,192\nRa 128,44,16384\nv_sub_u32 v0, v0, v20\nMvaa 80,228,196\nRa 132,45,16384\n"
"buffer_store_dword v25, off, s[16:19], s54 offset:12 sc0 sc1\ns_add_u32 s6, s56, 20480\nbuffer_load_dwordx4 a[48:51], v1, s[16:19], s6 offen offset:0 sc1\nMvaa 112,228,204\n"
"Ra 136,44,18432\ns_lshl_b32 s2, s11, 4\nMvaa 112,224,200\nRa 140,45,18432\nMvaa 144,224,208\nRa 144,46,16384\nv_add_u32 v3, s2, v0\nMvaa 144,228,212\nRa 148,47,16384\n"
"buffer_load_dwordx4 a[52:55], v1, s[16:19], s6 offen offset:1024 sc1\nMvaa 176,228,220\nRa 152,46,18432\nv_subrev_u32 v0, 32, v3\nMvaa 176,224,216\nRa 156,47,18432\n"
"Mvaa 180,232,216\nTa 160,32,0\ns_lshl_b32 s2, s11, 13\nMvaa 180,236,220\nTa 162,32,2048\nbuffer_load_dwordx4 a[56:59], v1, s[16:19], s6 offen offset:2048 sc1\nMvaa 148,236,212\n"
"Ta 164,32,16384\ns_add_u32 s2, s2, 0x20000\nMvaa 148,232,208\nTa 166,32,18432\nMvaa 116,232,200\nTa 168,33,0\nv_add_u32 v22, s2, v1\nMvaa 116,236,204\nTa 170,33,2048\n"
"buffer_load_dwordx4 a[60:63], v1, s[16:19], s6 offen offset:3072 sc1\nMvaa 84,236,196\nTa 172,33,16384\nv_xor_b32 v23, 0x2000, v22\nMvaa 84,232,192\nTa 174,33,18432\n"
"s_add_u32 m0, s47, -32768\ns_add_u32 s46, s45, 0x8000\nMvaa 88,240,192\nTa 176,34,0\nv_mov_b32 v4, 0\nMvaa 88,244,196\nTa 178,34,2048\nD1 1,16,46,0\nMvaa 120,244,204\n"
"Ta 180,34,16384\nv_mov_b32 v5, 0\nMvaa 120,240,200\nTa 182,34,18432\nMvaa 152,240,208\nTa 184,35,0\nv_mov_b32 v6, 0\nMvaa 152,244,212\nTa 186,35,2048\nD1 1,16,46,1024\n"
"Mvaa 184,244,220\nTa 188,35,16384\nv_mov_b32 v7, 0\nMvaa 184,240,216\nTa 190,35,18432\nMvaa 188,248,216\n.W568:\ns_cbranch_execz .W572\nv_mov_b32 v8, 0\nv_mov_b32 v9, 0\n"
"Mvaa 188,252,220\nD1 1,16,46,2048\nMvaa 156,252,212\nv_mov_b32 v10, 0\nv_mov_b32 v11, 0\nMvaa 156,248,208\nv_mov_b32 v12, 0\nMvaa 124,248,200\nv_mov_b32 v13, 0\nv_mov_b32 v14, 0\n"
"Mvaa 124,252,204\nD1 1,16,46,3072\nMvaa 92,252,196\nv_mov_b32 v15, 0\nv_mov_b32 v16, 0\nMvaa 92,248,192\nWL 0\nMvaa 64,160,128\nTa 224,36,0\nv_mov_b32 v17, 0\nMvaa 64,164,132\n"
"Ta 226,36,2048\ns_add_u32 m0, s47, -16384\ns_add_u32 s46, s45, 0x400000\nD1 1,16,46,0\nMvaa 68,172,132\nv_mov_b32 v18, 0\nv_mov_b32 v19, 0\nMvaa 68,168,128\nTa 228,36,16384\n"
"Mvaa 72,176,128\nTa 230,36,18432\nMvaa 72,180,132\nD1 1,16,46,1024\nMvaa 76,188,132\nTa 232,37,0\nMvaa 76,184,128\nTa 234,37,2048\nMvaa 108,184,136\nv_mul_f32 v64, s57, v64\n"
"v_mul_f32 v65, s57, v65\nMvaa 108,188,140\nTa 236,37,16384\nD1 1,16,46,2048\nMvaa 104,180,140\nTa 238,37,18432\nCV 64,64,65\nMvaa 104,176,136\nv_mul_f32 v66, s57, v66\n"
"v_mul_f32 v67, s57, v67\nMvaa 100,168,136\nTa 240,38,0\nCV 65,66,67\nMvaa 100,172,140\nTa 242,38,2048\nD1 1,16,46,3072\nMvaa 96,164,140\nv_mul_f32 v68, s57, v68\n"
"v_mul_f32 v69, s57, v69\nMvaa 96,160,136\nTa 244,38,16384\nCV 66,68,69\nMvaa 128,160,144\nTa 246,38,18432\nv_mul_f32 v70, s57, v70\nMvaa 128,164,148\nv_mul_f32 v71, s57, v71\n"
"Mvaa 132,172,148\nTa 248,39,0\nCV 67,70,71\nMvaa 132,168,144\nTa 250,39,2048\nAW 192,64\nMvaa 136,176,144\nAW 193,65\nAW 194,66\nMvaa 136,180,148\nTa 252,39,16384\nAW 195,67\n"
"Mvaa 140,188,148\nTa 254,39,18432\nv_mul_f32 v72, s57, v72\nMvaa 140,184,144\nv_mul_f32 v73, s57, v73\nCV 72,72,73\nMvaa 172,184,152\nv_mul_f32 v74, s57, v74\n"
"v_mul_f32 v75, s57, v75\nMvaa 172,188,156\nCV 73,74,75\nv_mul_f32 v76, s57, v76\nMvaa 168,180,156\nv_mul_f32 v77, s57, v77\nCV 74,76,77\nMvaa 168,176,152\nv_mul_f32 v78, s57, v78\n"
"v_mul_f32 v79, s57, v79\nMvaa 164,168,152\nCV 75,78,79\nAW 200,72\nMvaa 164,172,156\nAW 201,73\nAW 202,74\nMvaa 160,164,156\nAW 203,75\nv_mul_f32 v96, s57, v96\nMvaa 160,160,152\n"
"s_waitcnt vmcnt(21) lgkmcnt(0)\ns_barrier\nMvaa 80,224,128\nv_mul_f32 v97, s57, v97\nCV 96,96,97\nMvaa 80,228,132\ns_add_u32 m0, s47, 0\ns_add_u32 s46, s86, 0xffff0000\n"
"D1 1,16,46,0\nMvaa 84,236,132\nv_mul_f32 v98, s57, v98\nv_mul_f32 v99, s57, v99\nMvaa 84,232,128\nCV 97,98,99\nv_mul_f32 v100, s57, v100\nMvaa 88,240,128\n"
"v_mul_f32 v101, s57, v101\nCV 98,100,101\nMvaa 88,244,132\nD1 1,16,46,1024\nv_mul_f32 v102, s57, v102\nMvaa 92,252,132\nv_mul_f32 v103, s57, v103\nCV 99,102,103\nMvaa 92,248,128\n"
"AW 196,96\nAW 197,97\nMvaa 124,248,136\nAW 198,98\nAW 199,99\nMvaa 124,252,140\nD1 1,16,46,2048\nv_mul_f32 v128, s57, v128\nMvaa 120,244,140\nv_mul_f32 v129, s57, v129\n"
"CV 128,128,129\nMvaa 120,240,136\nv_mul_f32 v130, s57, v130\nv_mul_f32 v131, s57, v131\nMvaa 116,232,136\nRa 160,24,0\nCV 129,130,131\nMvaa 116,236,140\nD1 1,16,46,3072\n"
"v_mul_f32 v132, s57, v132\nMvaa 112,228,140\nRa 164,24,1024\nv_mul_f32 v133, s57, v133\nMvaa 112,224,136\ns_add_u32 m0, s47, 16384\ns_add_u32 s46, s45, 0x408000\nMvaa 144,224,144\n"
"Ra 168,24,2048\nCV 130,132,133\nMvaa 144,228,148\nD1 1,16,46,0\nMvaa 148,236,148\nRa 172,24,3072\nv_mul_f32 v134, s57, v134\nMvaa 148,232,144\nv_mul_f32 v135, s57, v135\n"
"CV 131,134,135\nMvaa 152,240,144\nRa 176,26,0\nds_write_b128 v23, v[128:131] offset:0\nMvaa 152,244,148\nD1 1,16,46,1024\nv_mul_f32 v80, s57, v80\nMvaa 156,252,148\n"
"Ra 180,26,1024\nv_mul_f32 v81, s57, v81\nMvaa 156,248,144\nCV 80,80,81\nv_mul_f32 v82, s57, v82\nMvaa 188,248,152\nRa 184,26,2048\nv_mul_f32 v83, s57, v83\nMvaa 188,252,156\n"
"D1 1,16,46,2048\nCV 81,82,83\nMvaa 184,244,156\nRa 188,26,3072\nv_mul_f32 v84, s57, v84\nMvaa 184,240,152\nv_mul_f32 v85, s57, v85\nCV 82,84,85\nMvaa 180,232,152\n"
"v_mul_f32 v86, s57, v86\nv_mul_f32 v87, s57, v87\nMvaa 180,236,156\nD1 1,16,46,3072\nCV 83,86,87\nMvaa 176,228,156\nAW 208,80\nAW 209,81\nMvaa 176,224,152\n"
"s_waitcnt vmcnt(25) lgkmcnt(5)\nCvaa 32,160,0,4\nRa 128,24,8192\nAW 210,82\nMvaa 32,164,8\nAW 211,83\nMvaa 32,168,16\nRa 132,24,9216\nv_mul_f32 v88, s57, v88\nMvaa 32,172,24\n"
"v_mul_f32 v89, s57, v89\nCV 88,88,89\nCvaa 36,172,28,8\nRa 136,24,10240\nv_mul_f32 v90, s57, v90\nMvaa 36,168,20\ns_add_u32 m0, s89, 32768\nv_mul_f32 v91, s57, v91\n"
"Mvaa 36,164,12\nRa 140,24,11264\ns_waitcnt vmcnt(16) lgkmcnt(4)\ns_barrier\nMvaa 36,160,4\nCV 89,90,91\nv_mul_f32 v92, s57, v92\nMvaa 36,176,36\nRa 144,26,8192\nSa 96,1,16,78,0\n"
"Mvaa 36,180,44\ns_add_u32 s46, s86, s88\nv_mul_f32 v93, s57, v93\nMvaa 36,184,52\nRa 148,26,9216\nCV 90,92,93\nMvaa 36,188,60\nv_mul_f32 v94, s57, v94\nv_mul_f32 v95, s57, v95\n"
"Mvaa 32,188,56\nRa 152,26,10240\nCV 91,94,95\nMvaa 32,184,48\nD1 1,16,46,0\nAW 216,88\nMvaa 32,180,40\nRa 156,26,11264\nWL 4\nMvaa 32,176,32\nAW 217,89\nAW 218,90\n"
"Cvaa 40,128,0,12\nSa 100,1,16,78,1024\nAW 219,91\nMvaa 40,132,8\nv_mov_b32 v86, 0\nMvaa 40,136,16\nv_mov_b32 v87, 0\nv_mov_b32 v88, 0\nMvaa 40,140,24\nv_mov_b32 v89, 0\n"
"v_mul_f32 v136, s57, v136\nv_mul_f32 v137, s57, v137\nCV 136,136,137\nv_mul_f32 v138, s57, v138\nv_mul_f32 v139, s57, v139\nCV 137,138,139\nCvaa 44,140,28,16\n"
"v_and_b32 v27, 0x70, v1\nbuffer_load_dword v27, v27, s[16:19], s61 offen offset:136 sc0 sc1\nv_mul_f32 v140, s57, v140\nv_mul_f32 v141, s57, v141\nCV 138,140,141\n"
"v_mul_f32 v142, s57, v142\nv_mul_f32 v143, s57, v143\nMvaa 44,136,20\nD1 1,16,46,1024\nCV 139,142,143\nds_write_b128 v23, v[136:139] offset:2048\nv_mul_f32 v144, s57, v144\n"
"v_mul_f32 v145, s57, v145\nCV 144,144,145\nv_mul_f32 v146, s57, v146\nMvaa 44,132,12\nWL 1\nv_mul_f32 v147, s57, v147\nCV 145,146,147\nv_mul_f32 v148, s57, v148\n"
"v_mul_f32 v149, s57, v149\nCV 146,148,149\nMvaa 44,128,4\nv_mul_f32 v150, s57, v150\nv_mul_f32 v151, s57, v151\nCV 147,150,151\nds_write_b128 v23, v[144:147] offset:4096\n"
"v_mul_f32 v152, s57, v152\nv_mul_f32 v153, s57, v153\nCV 152,152,153\nMvaa 44,144,36\nSa 104,1,16,78,2048\nv_mul_f32 v154, s57, v154\nv_mul_f32 v155, s57, v155\nCV 153,154,155\n"
"v_mul_f32 v156, s57, v156\nv_mul_f32 v157, s57, v157\nCV 154,156,157\nMvaa 44,148,44\ns_add_u32 s88, s88, 32768\nEX 80,32\nv_mul_f32 v158, s57, v158\nv_mul_f32 v159, s57, v159\n"
"CV 155,158,159\nds_write_b128 v23, v[152:155] offset:6144\nMvaa 44,152,52\nv_mul_f32 v160, s57, v160\nv_mul_f32 v161, s57, v161\nCV 160,160,161\nv_mul_f32 v162, s57, v162\n"
"v_mul_f32 v163, s57, v163\nCV 161,162,163\nv_mul_f32 v164, s57, v164\nMvaa 44,156,60\nEX 81,33\nv_mul_f32 v165, s57, v165\nCV 162,164,165\nv_mul_f32 v166, s57, v166\n"
"Mvaa 40,156,56\nv_mul_f32 v167, s57, v167\nCV 163,166,167\nds_write_b128 v23, v[160:163] offset:1024\nv_mul_f32 v168, s57, v168\nv_mul_f32 v169, s57, v169\nCV 168,168,169\n"
"v_mul_f32 v170, s57, v170\nMvaa 40,152,48\nD1 1,16,46,2048\nv_mul_f32 v171, s57, v171\nCV 169,170,171\nv_mul_f32 v172, s57, v172\nv_mul_f32 v173, s57, v173\nCV 170,172,173\n"
"v_mul_f32 v174, s57, v174\nMvaa 40,148,40\nv_mul_f32 v175, s57, v175\nCV 171,174,175\nds_write_b128 v23, v[168:171] offset:3072\nv_mul_f32 v176, s57, v176\n"
"v_mul_f32 v177, s57, v177\nCV 176,176,177\nv_mul_f32 v178, s57, v178\nMvaa 40,144,32\nAD 84,80,81\nCV 64,80,81\nv_mul_f32 v179, s57, v179\nCV 177,178,179\n"
"v_mul_f32 v180, s57, v180\nv_mul_f32 v181, s57, v181\nCV 178,180,181\nSa 108,1,16,78,3072\nv_mul_f32 v182, s57, v182\nv_mul_f32 v183, s57, v183\nCV 179,182,183\n"
"ds_write_b128 v23, v[176:179] offset:5120\nv_mul_f32 v184, s57, v184\nv_mul_f32 v185, s57, v185\nEX 82,34\nCV 184,184,185\nv_mul_f32 v186, s57, v186\nv_mul_f32 v187, s57, v187\n"
"CV 185,186,187\nv_mul_f32 v188, s57, v188\nv_mul_f32 v189, s57, v189\nCV 186,188,189\nv_mul_f32 v190, s57, v190\nv_mul_f32 v191, s57, v191\nCV 187,190,191\n"
"ds_write_b128 v23, v[184:187] offset:7168\ns_waitcnt vmcnt(16) lgkmcnt(0)\nEX 83,35\nAD 84,84,82\nD1 1,16,46,3072\ns_add_u32 m0, s89, 49152\ns_barrier\nAD 84,84,83\nCV 65,82,83\n"
"Ra 128,24,32768\nRa 224,22,0\nRa 228,22,1024\nRa 232,22,2048\ns_add_u32 s46, s87, s88\nEX 80,36\nRa 132,24,33792\nRa 236,22,3072\nRa 240,22,4096\nEX 81,37\nRa 136,24,34816\n"
"Ra 244,22,5120\nRa 248,22,6144\nRa 252,22,7168\nD1 1,16,46,0\nv_mul_f32 v104, s57, v104\nv_mul_f32 v105, s57, v105\nRa 140,24,35840\nCV 104,104,105\nv_mul_f32 v106, s57, v106\n"
"AD 85,80,81\nCV 68,80,81\nv_mul_f32 v107, s57, v107\nRa 144,26,32768\nCV 105,106,107\nv_mul_f32 v108, s57, v108\nv_mul_f32 v109, s57, v109\nEX 82,38\nCV 106,108,109\n"
"Ra 148,26,33792\nv_mul_f32 v110, s57, v110\nv_mul_f32 v111, s57, v111\nEX 83,39\nRa 152,26,34816\nAD 85,85,82\nCV 107,110,111\nAW 204,104\nD1 1,16,46,1024\nAW 205,105\nAW 206,106\n"
"Ra 156,26,35840\nAW 207,107\nv_mul_f32 v112, s57, v112\nAD 85,85,83\nCV 69,82,83\nv_mul_f32 v113, s57, v113\nRa 160,24,40960\nCV 112,112,113\nv_mul_f32 v114, s57, v114\n"
"v_mul_f32 v115, s57, v115\ns_xor_b32 s89, s89, 0x10000\nEX 80,40\nRa 164,24,41984\nCV 113,114,115\nv_mul_f32 v116, s57, v116\nWL 6\nEX 81,41\nRa 168,24,43008\nAD 84,84,80\n"
"v_mul_f32 v117, s57, v117\nCV 114,116,117\nD1 1,16,46,2048\nv_mul_f32 v118, s57, v118\nv_mul_f32 v119, s57, v119\nRa 172,24,44032\nCV 115,118,119\nAW 212,112\nAD 84,84,81\n"
"CV 66,80,81\nAW 213,113\nCvaa 48,128,0,4\nRa 176,26,40960\nAW 214,114\nAW 215,115\nv_mul_f32 v120, s57, v120\nMvaa 48,132,8\nEX 82,42\nv_mul_f32 v121, s57, v121\nMvaa 48,136,16\n"
"Ra 180,26,41984\nCV 120,120,121\nv_mul_f32 v122, s57, v122\nMvaa 48,140,24\nWL 6\nEX 83,43\nWV 8\nv_cmp_ne_u32 vcc, s15, v27\ns_nop 0\ns_cbranch_vccnz .S289\n.S290:\n"
"Cvaa 52,140,28,8\nRa 184,26,43008\nAD 84,84,82\nv_mul_f32 v123, s57, v123\nCV 121,122,123\nMvaa 52,136,20\nD1 1,16,46,3072\ns_add_u32 m0, s89, 0\nv_mul_f32 v124, s57, v124\n"
"Mvaa 52,132,12\nRa 188,26,44032\nv_mul_f32 v125, s57, v125\nCV 122,124,125\nMvaa 52,128,4\nAD 84,84,83\nCV 67,82,83\nv_mul_f32 v126, s57, v126\nMvaa 52,144,36\nRa 128,2,49152\n"
"s_add_u32 s78, s78, s55\nSa 112,1,16,78,0\nv_mul_f32 v127, s57, v127\nMvaa 52,148,44\ns_add_u32 s46, s86, s88\nEX 80,44\nMvaa 52,152,52\nRa 132,2,50176\n"
"v_xor_b32 v24, 0x10000, v24\nCV 123,126,127\nMvaa 52,156,60\nWL 6\nEX 81,45\nMvaa 48,156,56\nRa 136,2,51200\nAD 85,85,80\nAW 220,120\nAW 221,121\nMvaa 48,152,48\nD1 1,16,46,0\n"
"AW 222,122\nAW 223,123\nMvaa 48,148,40\nRa 140,2,52224\nv_mov_b32 v115, -1\nMvaa 48,144,32\nAD 85,85,81\nCV 70,80,81\nCvaa 56,160,0,12\nRa 144,2,53248\nSa 116,1,16,78,1024\n"
"Mvaa 56,164,8\nEX 82,46\nMvaa 56,168,16\nRa 148,2,54272\nv_xor_b32 v26, 0x10000, v26\nMvaa 56,172,24\nWL 6\nEX 83,47\nCvaa 60,172,28,16\nRa 152,2,55296\nAD 85,85,82\n"
"Mvaa 60,168,20\nD1 1,16,46,1024\nMvaa 60,164,12\nRa 156,2,56320\nMvaa 60,160,4\nAD 85,85,83\nCV 71,82,83\nMvaa 60,176,36\nRa 160,2,57344\nv_max_f32 v90, v84, v85\nMvaa 60,180,44\n"
"s_add_u32 s88, s88, 32768\nEX 80,48\nMvaa 60,184,52\nRa 164,2,58368\nv_cmp_lt_f32 vcc, s63, v90\nMvaa 60,188,60\nWL 8\nEX 81,49\nMvaa 56,188,56\nRa 168,2,59392\n"
"s_cbranch_vccnz .T278\n.T279:\nv_add_f32 v20, s93, v84\nv_mov_b32 v21, v85\nv_mov_b32 v22, v20\nv_mov_b32 v23, v21\nMvaa 56,184,48\nD1 1,16,46,2048\n"
"v_permlane32_swap_b32 v20, v22\nv_permlane32_swap_b32 v21, v23\nAD 20,20,22\nAD 21,21,23\nv_mov_b32 v22, v20\nv_mov_b32 v23, v21\nMvaa 56,180,40\nRa 172,2,60416\n"
"v_permlane16_swap_b32 v20, v22\nv_permlane16_swap_b32 v21, v23\nAD 20,20,22\nAD 21,21,23\nv_min_f32 v90, v20, v21\nv_cmp_gt_f32 vcc, s64, v90\ns_nop 0\ns_cbranch_vccnz .T280\n"
".T281:\nAD 86,86,84\nMvaa 56,176,32\nAD 84,80,81\nCV 72,80,81\nZvav 128,128,64\nRa 176,2,61440\nSa 120,1,16,78,2048\nZvav 132,128,68\nEX 82,50\nZvav 140,132,68\nRa 180,2,62464\n"
"Zvav 136,132,64\nWL 8\nEX 83,51\nZvav 144,136,64\nRa 184,2,63488\nAD 84,84,82\nZvav 148,136,68\nD1 1,16,46,3072\ns_add_u32 m0, s89, 16384\nZvav 156,140,68\nRa 188,2,64512\n"
"s_barrier\nZvav 152,140,64\nAD 84,84,83\nCV 73,82,83\nZvav 160,144,64\nRa 128,24,0\nSa 124,1,16,78,3072\nZvav 164,144,68\ns_add_u32 s46, s87, s88\nEX 80,52\nZvav 172,148,68\n"
"Ra 132,24,1024\nv_xor_b32 v2, 0x10000, v2\nZvav 168,148,64\nWL 8\nEX 81,53\nZvav 176,152,64\nRa 136,24,2048\nAD 87,87,85\nZvav 180,152,68\nD1 1,16,46,0\nZvav 188,156,68\n"
"Ra 140,24,3072\nZvav 184,156,64\nAD 85,80,81\nCV 76,80,81\n.W569:\ns_cbranch_execz .W573\nZvav 192,160,64\nRa 144,26,0\nZvav 196,160,68\ns_cmp_lt_u32 s99, s92\nEX 82,54\n"
"Zvav 204,164,68\nRa 148,26,1024\ns_cbranch_scc0 .E276\nv_cmpx_gt_i32 vcc, 19, v3\nv_mov_b32 v15, s97\nv_cmpx_gt_i32 vcc, 18, v3\nv_mov_b32 v14, s97\nv_cmpx_gt_i32 vcc, 17, v3\n"
"v_mov_b32 v13, s97\nv_cmpx_gt_i32 vcc, 16, v3\nv_mov_b32 v12, s97\nv_cmpx_gt_i32 vcc, 3, v3\nv_mov_b32 v7, s97\nv_cmpx_gt_i32 vcc, 2, v3\nv_mov_b32 v6, s97\n"
"v_cmpx_gt_i32 vcc, 1, v3\nv_mov_b32 v5, s97\nv_cmpx_gt_i32 vcc, 0, v3\nv_mov_b32 v4, s97\ns_mov_b64 exec, -1\ns_nop 2\n.E276:\ns_nop 0\nZvav 200,164,64\nWL 8\nEX 83,55\n"
"Zvav 208,168,64\nRa 152,26,2048\nAD 85,85,82\nZvav 212,168,68\nD1 1,16,46,1024\nZvav 220,172,68\nRa 156,26,3072\nZvav 216,172,64\nAD 85,85,83\nCV 77,82,83\nZvav 224,176,64\n"
"Ra 160,24,8192\nZvav 228,176,68\nEX 80,56\nZvav 236,180,68\nRa 164,24,9216\nZvav 232,180,64\nWL 6\nEX 81,57\nZvav 240,184,64\nRa 168,24,10240\nAD 84,84,80\n"
"v_and_b32 v27, 0x70, v1\nbuffer_load_dword v27, v27, s[16:19], s61 offen offset:140 sc0 sc1\nZvav 244,184,68\nD1 1,16,46,2048\nZvav 252,188,68\nRa 172,24,11264\nZvav 248,188,64\n"
"AD 84,84,81\nCV 74,80,81\ns_sub_u32 s99, s99, 1\n.p2align 6\n.A304:\nCvaa 32,128,0,4\nRa 176,26,8192\nMvaa 32,132,8\ns_add_u32 s49, s49, 131072\nEX 82,58\nMvaa 32,136,16\n"
"Ra 180,26,9216\nMvaa 32,140,24\nWL 6\nEX 83,59\nCvaa 36,140,28,8\nRa 184,26,10240\nAD 84,84,82\nMvaa 36,136,20\nD1 1,16,46,3072\ns_add_u32 m0, s89, 32768\nMvaa 36,132,12\n"
"Ra 188,26,11264\nMvaa 36,128,4\nAD 84,84,83\nCV 75,82,83\nMvaa 36,144,36\nRa 128,2,16384\nMvaa 36,148,44\ns_add_u32 s46, s86, s88\nEX 80,60\nMvaa 36,152,52\nRa 132,2,17408\n"
"Mvaa 36,156,60\nWL 6\nEX 81,61\nMvaa 32,156,56\nRa 136,2,18432\nAD 85,85,80\nMvaa 32,152,48\nD1 1,16,46,0\nMvaa 32,148,40\nRa 140,2,19456\nMvaa 32,144,32\nAD 85,85,81\n"
"CV 78,80,81\nCvaa 40,160,0,12\nRa 144,2,20480\nMvaa 40,164,8\nSv 112,1,16,49,0\nEX 82,62\nMvaa 40,168,16\nRa 148,2,21504\nMvaa 40,172,24\nWL 6\nEX 83,63\nCvaa 44,172,28,16\n"
"Ra 152,2,22528\nAD 85,85,82\nMvaa 44,168,20\nD1 1,16,46,1024\nMvaa 44,164,12\nRa 156,2,23552\nMvaa 44,160,4\nAD 85,85,83\nCV 79,82,83\nMvaa 44,176,36\nRa 160,2,24576\n"
"v_max_f32 v90, v84, v85\nMvaa 44,180,44\ns_add_u32 s88, s88, 32768\nEX 80,32\nMvaa 44,184,52\nRa 164,2,25600\nv_cmp_lt_f32 vcc, s63, v90\nMvaa 44,188,60\nWL 8\nEX 81,33\n"
"Mvaa 40,188,56\nRa 168,2,26624\ns_cbranch_vccnz .T312\n.T313:\nMvaa 40,184,48\nD1 1,16,46,2048\nMvaa 40,180,40\nRa 172,2,27648\nAD 86,86,84\nMvaa 40,176,32\nAD 84,80,81\n"
"CV 64,80,81\nMvav 128,128,72\nRa 176,2,28672\nMvav 132,128,76\nEX 82,34\nMvav 140,132,76\nRa 180,2,29696\nMvav 136,132,72\ns_waitcnt vmcnt(13) lgkmcnt(8)\nEX 83,35\n"
"Mvav 144,136,72\nRa 184,2,30720\nAD 84,84,82\nMvav 148,136,76\nD1 1,16,46,3072\ns_add_u32 m0, s89, 49152\nMvav 156,140,76\nRa 188,2,31744\ns_barrier\nMvav 152,140,72\nAD 84,84,83\n"
"CV 65,82,83\nMvav 160,144,72\nRa 128,24,32768\nMvav 164,144,76\ns_add_u32 s46, s87, s88\nEX 80,36\nMvav 172,148,76\nRa 132,24,33792\nMvav 168,148,72\nWL 8\nEX 81,37\n"
"Mvav 176,152,72\nRa 136,24,34816\nAD 87,87,85\nMvav 180,152,76\nD1 1,16,46,0\nMvav 188,156,76\nRa 140,24,35840\nMvav 184,156,72\nAD 85,80,81\nCV 68,80,81\nMvav 192,160,72\n"
"Ra 144,26,32768\nMvav 196,160,76\ns_cmp_eq_u32 s99, 0\nEX 82,38\nMvav 204,164,76\nRa 148,26,33792\ns_cbranch_scc1 .E306\n.E307:\nMvav 200,164,72\nWL 8\nEX 83,39\nMvav 208,168,72\n"
"Ra 152,26,34816\nAD 85,85,82\nMvav 212,168,76\nD1 1,16,46,1024\nMvav 220,172,76\nRa 156,26,35840\nMvav 216,172,72\nAD 85,85,83\nCV 69,82,83\nMvav 224,176,72\nRa 160,24,40960\n"
"Mvav 228,176,76\ns_xor_b32 s89, s89, 0x10000\nEX 80,40\nMvav 236,180,76\nRa 164,24,41984\nMvav 232,180,72\nWL 6\nEX 81,41\nMvav 240,184,72\nRa 168,24,43008\nAD 84,84,80\n"
"Mvav 244,184,76\nD1 1,16,46,2048\nMvav 252,188,76\nRa 172,24,44032\nMvav 248,188,72\nAD 84,84,81\nCV 66,80,81\nCvaa 48,128,0,4\nRa 176,26,40960\nMvaa 48,132,8\n"
"s_cmp_eq_u32 s99, 0\nEX 82,42\nMvaa 48,136,16\nRa 180,26,41984\ns_cselect_b32 s86, s94, s86\nMvaa 48,140,24\nWL 6\nEX 83,43\nCvaa 52,140,28,8\nRa 184,26,43008\nAD 84,84,82\n"
"Mvaa 52,136,20\nD1 1,16,46,3072\ns_add_u32 m0, s89, 0\nMvaa 52,132,12\nRa 188,26,44032\nMvaa 52,128,4\nAD 84,84,83\nCV 67,82,83\nMvaa 52,144,36\nRa 128,2,49152\nMvaa 52,148,44\n"
"s_add_u32 s46, s86, s88\nEX 80,44\nMvaa 52,152,52\nRa 132,2,50176\nv_xor_b32 v24, 0x10000, v24\nMvaa 52,156,60\nWL 6\nEX 81,45\nMvaa 48,156,56\nRa 136,2,51200\nAD 85,85,80\n"
"Mvaa 48,152,48\nD1 1,16,46,0\nMvaa 48,148,40\nRa 140,2,52224\nMvaa 48,144,32\nAD 85,85,81\nCV 70,80,81\nCvaa 56,160,0,12\nRa 144,2,53248\nMvaa 56,164,8\nSv 112,1,16,49,1024\n"
"EX 82,46\nMvaa 56,168,16\nRa 148,2,54272\nv_xor_b32 v26, 0x10000, v26\nMvaa 56,172,24\nWL 6\nEX 83,47\nCvaa 60,172,28,16\nRa 152,2,55296\nAD 85,85,82\nMvaa 60,168,20\n"
"D1 1,16,46,1024\nMvaa 60,164,12\nRa 156,2,56320\nMvaa 60,160,4\nAD 85,85,83\nCV 71,82,83\nMvaa 60,176,36\nRa 160,2,57344\nv_max_f32 v90, v84, v85\nMvaa 60,180,44\n"
"s_add_u32 s88, s88, 32768\nEX 80,48\nMvaa 60,184,52\nRa 164,2,58368\nv_cmp_lt_f32 vcc, s63, v90\nMvaa 60,188,60\nWL 8\nEX 81,49\nMvaa 56,188,56\nRa 168,2,59392\n"
"s_cbranch_vccnz .T314\n.T315:\nMvaa 56,184,48\nD1 1,16,46,2048\nMvaa 56,180,40\nRa 172,2,60416\nAD 86,86,84\nMvaa 56,176,32\nAD 84,80,81\nCV 72,80,81\nMvav 128,128,64\n"
"Ra 176,2,61440\nMvav 132,128,68\nEX 82,50\nMvav 140,132,68\nRa 180,2,62464\nMvav 136,132,64\ns_waitcnt vmcnt(13) lgkmcnt(8)\nEX 83,51\nMvav 144,136,64\nRa 184,2,63488\n"
"AD 84,84,82\nMvav 148,136,68\nD1 1,16,46,3072\ns_add_u32 m0, s89, 16384\nMvav 156,140,68\nRa 188,2,64512\ns_barrier\nMvav 152,140,64\nAD 84,84,83\nCV 73,82,83\nMvav 160,144,64\n"
"Ra 128,24,0\nMvav 164,144,68\ns_add_u32 s46, s87, s88\nEX 80,52\nMvav 172,148,68\nRa 132,24,1024\nv_xor_b32 v2, 0x10000, v2\nMvav 168,148,64\nWL 8\nEX 81,53\nMvav 176,152,64\n"
"Ra 136,24,2048\nAD 87,87,85\nMvav 180,152,68\nD1 1,16,46,0\nMvav 188,156,68\nRa 140,24,3072\nMvav 184,156,64\nAD 85,80,81\nCV 76,80,81\nMvav 192,160,64\nRa 144,26,0\n"
"Mvav 196,160,68\ns_cmp_lt_u32 s99, s92\nEX 82,54\nMvav 204,164,68\nRa 148,26,1024\ns_cbranch_scc1 .E308\n.E309:\nMvav 200,164,64\nWL 8\nEX 83,55\nMvav 208,168,64\nRa 152,26,2048\n"
"AD 85,85,82\nMvav 212,168,68\nD1 1,16,46,1024\nMvav 220,172,68\nRa 156,26,3072\nMvav 216,172,64\nAD 85,85,83\nCV 77,82,83\nMvav 224,176,64\nRa 160,24,8192\nMvav 228,176,68\n"
"s_sub_u32 s90, s90, 1\nEX 80,56\nMvav 236,180,68\nRa 164,24,9216\ns_cbranch_scc1 .E310\n.E311:\nMvav 232,180,64\nWL 6\nEX 81,57\nMvav 240,184,64\nRa 168,24,10240\nAD 84,84,80\n"
"Mvav 244,184,68\nD1 1,16,46,2048\ns_sub_u32 s99, s99, 1\nMvav 252,188,68\nRa 172,24,11264\nMvav 248,188,64\nAD 84,84,81\nCV 74,80,81\ns_cbranch_scc0 .A304\ns_branch .A316\n.E306:\n"
"v_cmpx_gt_i32 vcc, 19, v0\nv_mov_b32 v15, s97\nv_cmpx_gt_i32 vcc, 18, v0\nv_mov_b32 v14, s97\nv_cmpx_gt_i32 vcc, 17, v0\nv_mov_b32 v13, s97\nv_cmpx_gt_i32 vcc, 16, v0\n"
"v_mov_b32 v12, s97\nv_cmpx_gt_i32 vcc, 3, v0\nv_mov_b32 v7, s97\nv_cmpx_gt_i32 vcc, 2, v0\nv_mov_b32 v6, s97\nv_cmpx_gt_i32 vcc, 1, v0\nv_mov_b32 v5, s97\n"
"v_cmpx_gt_i32 vcc, 0, v0\nv_mov_b32 v4, s97\ns_mov_b64 exec, -1\ns_nop 2\ns_branch .E307\n.E308:\ns_cmp_eq_u32 s99, 0\ns_cbranch_scc1 .K305\nv_cmpx_gt_i32 vcc, 19, v3\n"
"v_mov_b32 v15, s97\nv_cmpx_gt_i32 vcc, 18, v3\nv_mov_b32 v14, s97\nv_cmpx_gt_i32 vcc, 17, v3\nv_mov_b32 v13, s97\nv_cmpx_gt_i32 vcc, 16, v3\nv_mov_b32 v12, s97\n"
"v_cmpx_gt_i32 vcc, 3, v3\nv_mov_b32 v7, s97\nv_cmpx_gt_i32 vcc, 2, v3\nv_mov_b32 v6, s97\nv_cmpx_gt_i32 vcc, 1, v3\nv_mov_b32 v5, s97\nv_cmpx_gt_i32 vcc, 0, v3\nv_mov_b32 v4, s97\n"
"s_mov_b64 exec, -1\ns_nop 2\ns_branch .E309\n.K305:\nv_cmpx_gt_i32 vcc, 19, v3\nv_mov_b32 v19, s97\nv_cmpx_gt_i32 vcc, 18, v3\nv_mov_b32 v18, s97\nv_cmpx_gt_i32 vcc, 17, v3\n"
"v_mov_b32 v17, s97\nv_cmpx_gt_i32 vcc, 16, v3\nv_mov_b32 v16, s97\nv_cmpx_gt_i32 vcc, 3, v3\nv_mov_b32 v11, s97\nv_cmpx_gt_i32 vcc, 2, v3\nv_mov_b32 v10, s97\n"
"v_cmpx_gt_i32 vcc, 1, v3\nv_mov_b32 v9, s97\nv_cmpx_gt_i32 vcc, 0, v3\nv_mov_b32 v8, s97\ns_mov_b64 exec, -1\ns_nop 2\ns_branch .E309\n.E310:\ns_swappc_b64 s[80:81], s[8:9]\n"
"s_branch .E311\n.HD268:\nv_cmp_ne_u32 vcc, s15, v27\ns_nop 0\ns_cbranch_vccnz .S292\n.S293:\nbuffer_store_dword v25, off, s[16:19], s54 offset:4 sc0 sc1\nv_and_b32 v27, 0x70, v1\n"
"buffer_load_dword v27, v27, s[16:19], s61 offen offset:12 sc0 sc1\ns_mov_b32 s90, 1\ns_swappc_b64 s[8:9], s[80:81]\nv_cmp_ne_u32 vcc, s15, v27\ns_nop 0\ns_cbranch_vccnz .S295\n"
".S296:\nv_and_b32 v27, 0x70, v1\nbuffer_load_dword v27, v27, s[16:19], s61 offen offset:8 sc0 sc1\ns_mov_b32 s90, 1\ns_swappc_b64 s[8:9], s[80:81]\nv_cmp_ne_u32 vcc, s15, v27\n"
"s_nop 0\ns_cbranch_vccnz .S298\n.S299:\nv_and_b32 v27, 0xf0, v1\nbuffer_load_dword v27, v27, s[16:19], s61 offen offset:4 sc0 sc1\ns_mov_b32 s90, 1\ns_swappc_b64 s[8:9], s[80:81]\n"
"v_cmp_ne_u32 vcc, s15, v27\ns_nop 0\ns_cbranch_vccnz .S301\n.S302:\ns_mov_b32 s90, 100\ns_swappc_b64 s[8:9], s[80:81]\n.A316:\nRa 176,26,8192\ns_add_u32 s49, s49, 131072\n"
"EX 82,58\nRa 180,26,9216\nWL 6\nEX 83,59\nCvaa 36,140,28,8\nRa 184,26,10240\nAD 84,84,82\nMvaa 36,136,20\nD1 1,16,46,3072\ns_add_u32 m0, s89, 32768\nMvaa 36,132,12\n"
"Ra 188,26,11264\nMvaa 36,128,4\nAD 84,84,83\nCV 75,82,83\nMvaa 36,144,36\nRa 128,2,16384\nMvaa 36,148,44\ns_add_u32 s46, s86, s88\nEX 80,60\nMvaa 36,152,52\nRa 132,2,17408\n"
"Mvaa 36,156,60\nEX 81,61\nRa 136,2,18432\nAD 85,85,80\nD1 1,16,46,0\nRa 140,2,19456\nAD 85,85,81\nCV 78,80,81\nRa 144,2,20480\nSv 112,1,16,49,0\ns_mov_b32 s87, s95\nEX 82,62\n"
"Ra 148,2,21504\nWL 6\nEX 83,63\nCvaa 44,172,28,16\nRa 152,2,22528\nAD 85,85,82\nMvaa 44,168,20\nD1 1,16,46,1024\nMvaa 44,164,12\nRa 156,2,23552\nMvaa 44,160,4\nAD 85,85,83\n"
"CV 79,82,83\nMvaa 44,176,36\nRa 160,2,24576\nv_max_f32 v90, v84, v85\nMvaa 44,180,44\ns_add_u32 s88, s88, 32768\nMvaa 44,184,52\nRa 164,2,25600\nv_cmp_lt_f32 vcc, s63, v90\n"
"Mvaa 44,188,60\nWL 8\nRa 168,2,26624\ns_cbranch_vccnz .T318\n.T319:\nD1 1,16,46,2048\nRa 172,2,27648\nAD 86,86,84\nMvav 128,128,72\nRa 176,2,28672\nMvav 132,128,76\n"
"Mvav 140,132,76\nRa 180,2,29696\nMvav 136,132,72\ns_waitcnt vmcnt(13) lgkmcnt(8)\nMvav 144,136,72\nRa 184,2,30720\nMvav 148,136,76\nD1 1,16,46,3072\ns_add_u32 m0, s89, 49152\n"
"Mvav 156,140,76\nRa 188,2,31744\ns_barrier\nMvav 152,140,72\nMvav 160,144,72\nRa 128,24,32768\nMvav 164,144,76\ns_add_u32 s46, s87, s88\nEX 80,36\nMvav 172,148,76\n"
"Ra 132,24,33792\nMvav 168,148,72\nWL 8\nEX 81,37\nMvav 176,152,72\nRa 136,24,34816\nAD 87,87,85\nMvav 180,152,76\nD1 1,16,46,0\nMvav 188,156,76\nRa 140,24,35840\nMvav 184,156,72\n"
"AD 85,80,81\nCV 68,80,81\nMvav 192,160,72\nRa 144,26,32768\nMvav 196,160,76\ns_nop 0\nEX 82,38\nMvav 204,164,76\nRa 148,26,33792\nv_cmpx_gt_i32 vcc, 19, v0\nv_mov_b32 v19, s97\n"
"v_cmpx_gt_i32 vcc, 18, v0\nv_mov_b32 v18, s97\nv_cmpx_gt_i32 vcc, 17, v0\nv_mov_b32 v17, s97\nv_cmpx_gt_i32 vcc, 16, v0\nv_mov_b32 v16, s97\nv_cmpx_gt_i32 vcc, 3, v0\n"
"v_mov_b32 v11, s97\nv_cmpx_gt_i32 vcc, 2, v0\nv_mov_b32 v10, s97\nv_cmpx_gt_i32 vcc, 1, v0\nv_mov_b32 v9, s97\nv_cmpx_gt_i32 vcc, 0, v0\nv_mov_b32 v8, s97\ns_mov_b64 exec, -1\n"
"s_nop 2\ns_nop 0\nMvav 200,164,72\nWL 8\nEX 83,39\nMvav 208,168,72\nRa 152,26,34816\nAD 85,85,82\nMvav 212,168,76\nD1 1,16,46,1024\nMvav 220,172,76\nRa 156,26,35840\n"
"Mvav 216,172,72\nAD 85,85,83\nCV 69,82,83\nMvav 224,176,72\nRa 160,24,40960\nMvav 228,176,76\ns_xor_b32 s89, s89, 0x10000\nMvav 236,180,76\nRa 164,24,41984\nMvav 232,180,72\n"
"WL 10\nMvav 240,184,72\nRa 168,24,43008\nMvav 244,184,76\nD1 1,16,46,2048\nMvav 252,188,76\nRa 172,24,44032\nMvav 248,188,72\nRa 176,26,40960\nRa 180,26,41984\n.W570:\n"
"s_cbranch_execz .W574\nWL 6\nCvaa 52,140,28,8\nRa 184,26,43008\nMvaa 52,136,20\nD1 1,16,46,3072\ns_add_u32 m0, s89, 0\nMvaa 52,132,12\nRa 188,26,44032\nMvaa 52,128,4\n"
"Mvaa 52,144,36\nRa 128,2,49152\nMvaa 52,148,44\ns_add_u32 s46, s86, s88\nEX 80,44\nMvaa 52,152,52\nRa 132,2,50176\nv_xor_b32 v24, 0x10000, v24\nMvaa 52,156,60\nEX 81,45\n"
"Ra 136,2,51200\nAD 85,85,80\nD1 1,16,46,0\nRa 140,2,52224\nAD 85,85,81\nCV 70,80,81\nRa 144,2,53248\nSv 112,1,16,49,1024\nEX 82,46\nRa 148,2,54272\nv_xor_b32 v26, 0x10000, v26\n"
"WL 6\nEX 83,47\nCvaa 60,172,28,16\nRa 152,2,55296\nAD 85,85,82\nMvaa 60,168,20\nD1 1,16,46,1024\nMvaa 60,164,12\nRa 156,2,56320\nMvaa 60,160,4\nAD 85,85,83\nCV 71,82,83\n"
"Mvaa 60,176,36\nRa 160,2,57344\nMvaa 60,180,44\ns_add_u32 s88, s88, 32768\nMvaa 60,184,52\nRa 164,2,58368\nv_cmp_lt_f32 vcc, s63, v85\nMvaa 60,188,60\nWL 8\nRa 168,2,59392\n"
"s_cbranch_vccnz .T320\n.T321:\nD1 1,16,46,2048\nRa 172,2,60416\nRa 176,2,61440\nMvav 132,128,68\nMvav 140,132,68\nRa 180,2,62464\ns_waitcnt vmcnt(13) lgkmcnt(8)\nRa 184,2,63488\n"
"Mvav 148,136,68\nD1 1,16,46,3072\ns_add_u32 m0, s89, 16384\nMvav 156,140,68\nRa 188,2,64512\ns_barrier\nRa 128,24,0\nMvav 164,144,68\ns_add_u32 s46, s87, s88\nEX 80,52\n"
"Mvav 172,148,68\nRa 132,24,1024\nv_xor_b32 v2, 0x10000, v2\nWL 8\nEX 81,53\nRa 136,24,2048\nAD 87,87,85\nMvav 180,152,68\nD1 1,16,46,0\nMvav 188,156,68\nRa 140,24,3072\n"
"AD 85,80,81\nCV 76,80,81\nRa 144,26,0\nMvav 196,160,68\nEX 82,54\nMvav 204,164,68\nRa 148,26,1024\nWL 8\nEX 83,55\nRa 152,26,2048\nAD 85,85,82\nMvav 212,168,68\nD1 1,16,46,1024\n"
"Mvav 220,172,68\nRa 156,26,3072\nAD 85,85,83\nCV 77,82,83\nRa 160,24,8192\nMvav 228,176,68\ns_sub_u32 s90, s90, 1\nMvav 236,180,68\nRa 164,24,9216\nWL 6\nRa 168,24,10240\n"
"Mvav 244,184,68\nD1 1,16,46,2048\nMvav 252,188,68\nRa 172,24,11264\n.SW274:\nv_mov_b32 v4, 0\nv_mov_b32 v5, 0\nv_mov_b32 v6, 0\nv_mov_b32 v7, 0\nv_mov_b32 v8, 0\nv_mov_b32 v9, 0\n"
"v_mov_b32 v10, 0\nv_mov_b32 v11, 0\nv_mov_b32 v12, 0\nv_mov_b32 v13, 0\nv_mov_b32 v14, 0\nv_mov_b32 v15, 0\nv_mov_b32 v16, 0\nv_mov_b32 v17, 0\nv_mov_b32 v18, 0\nv_mov_b32 v19, 0\n"
"s_add_u32 s99, s98, 1\ns_mov_b32 s94, s96\ns_add_u32 s95, s96, 65536\ns_cmp_lt_u32 s99, s92\ns_cbranch_scc0 .E322\nv_cmpx_gt_i32 vcc, 19, v3\nv_mov_b32 v15, s97\n"
"v_cmpx_gt_i32 vcc, 18, v3\nv_mov_b32 v14, s97\nv_cmpx_gt_i32 vcc, 17, v3\nv_mov_b32 v13, s97\nv_cmpx_gt_i32 vcc, 16, v3\nv_mov_b32 v12, s97\nv_cmpx_gt_i32 vcc, 3, v3\n"
"v_mov_b32 v7, s97\nv_cmpx_gt_i32 vcc, 2, v3\nv_mov_b32 v6, s97\nv_cmpx_gt_i32 vcc, 1, v3\nv_mov_b32 v5, s97\nv_cmpx_gt_i32 vcc, 0, v3\nv_mov_b32 v4, s97\ns_mov_b64 exec, -1\n"
"s_nop 2\n.E322:\ns_nop 0\nCvaa 32,128,192,4\nRa 176,26,8192\nMvaa 32,132,200\nMvaa 32,136,208\nRa 180,26,9216\nMvaa 32,140,216\nWL 6\nCvaa 36,140,220,8\nRa 184,26,10240\n"
"Mvaa 36,136,212\nD1 1,16,46,3072\ns_add_u32 m0, s89, 32768\nMvaa 36,132,204\nRa 188,26,11264\nMvaa 36,128,196\nMvaa 36,144,228\nRa 128,2,16384\nMvaa 36,148,236\n"
"s_add_u32 s46, s86, s88\nEX 80,60\nMvaa 36,152,244\nRa 132,2,17408\nMvaa 36,156,252\nWL 6\nEX 81,61\nMvaa 32,156,248\nRa 136,2,18432\nAD 85,85,80\nMvaa 32,152,240\nD1 1,16,46,0\n"
"Mvaa 32,148,232\nRa 140,2,19456\nMvaa 32,144,224\nAD 85,85,81\nCV 78,80,81\nCvaa 40,160,192,12\nRa 144,2,20480\nMvaa 40,164,200\nEX 82,62\nMvaa 40,168,208\nRa 148,2,21504\n"
"Mvaa 40,172,216\nWL 6\nEX 83,63\nCvaa 44,172,220,16\nRa 152,2,22528\nAD 85,85,82\nMvaa 44,168,212\nD1 1,16,46,1024\nMvaa 44,164,204\nRa 156,2,23552\nMvaa 44,160,196\nAD 85,85,83\n"
"CV 79,82,83\nMvaa 44,176,228\nRa 160,2,24576\nMvaa 44,180,236\ns_add_u32 s88, s88, 32768\nEX 80,32\nMvaa 44,184,244\nRa 164,2,25600\nv_cmp_lt_f32 vcc, s63, v85\nMvaa 44,188,252\n"
"WL 8\nEX 81,33\nMvaa 40,188,248\nRa 168,2,26624\ns_cbranch_vccnz .T327\n.T328:\nMvaa 40,184,240\nD1 1,16,46,2048\nCV 128,128,129\nCV 129,130,131\nMvaa 40,180,232\nRa 172,2,27648\n"
"CV 130,136,137\nCV 131,138,139\nMvaa 40,176,224\nAD 84,80,81\nCV 64,80,81\ns_add_u32 s5, s66, 0\nSv 128,1,16,5,0\nRa 176,2,28672\nCV 144,144,145\nCV 145,146,147\nMvav 132,128,76\n"
"EX 82,34\nMvav 140,132,76\nRa 180,2,29696\nCV 146,152,153\nCV 147,154,155\ns_waitcnt vmcnt(13) lgkmcnt(8)\nEX 83,35\ns_add_u32 s5, s66, 0\nSv 144,1,16,5,2048\nRa 184,2,30720\n"
"AD 84,84,82\nCV 160,160,161\nMvav 148,136,76\nD1 1,16,46,3072\ns_add_u32 m0, s89, 49152\nCV 161,162,163\nMvav 156,140,76\nRa 188,2,31744\ns_barrier\nCV 162,168,169\n"
"CV 163,170,171\nAD 84,84,83\nCV 65,82,83\ns_add_u32 s5, s66, 4096\nSv 160,1,16,5,0\nRa 128,24,32768\nCV 176,176,177\nCV 177,178,179\nMvav 164,144,76\ns_add_u32 s46, s87, s88\n"
"EX 80,36\nMvav 172,148,76\nRa 132,24,33792\nCV 178,184,185\nCV 179,186,187\nCV 132,132,133\nWL 8\nEX 81,37\ns_add_u32 s5, s66, 4096\nSv 176,1,16,5,2048\nRa 136,24,34816\n"
"AD 87,87,85\nCV 133,134,135\nMvav 180,152,76\nD1 1,16,46,0\nv_mov_b32 v96, v86\nv_mov_b32 v97, v87\nCV 134,140,141\nMvav 188,156,76\nRa 140,24,35840\n"
"v_permlane32_swap_b32 v86, v96\nCV 135,142,143\nCV 148,148,149\nCV 149,150,151\nAD 85,80,81\nCV 68,80,81\ns_add_u32 s5, s66, 0\nSv 132,1,16,5,1024\nRa 144,26,32768\n"
"v_permlane32_swap_b32 v87, v97\nAD 86,86,96\nCV 150,156,157\nCV 151,158,159\nMvav 196,160,76\ns_cmp_eq_u32 s99, 0\nEX 82,38\nMvav 204,164,76\nRa 148,26,33792\n"
"s_cbranch_scc0 .E324\nv_cmpx_gt_i32 vcc, 19, v0\nv_mov_b32 v15, s97\nv_cmpx_gt_i32 vcc, 18, v0\nv_mov_b32 v14, s97\nv_cmpx_gt_i32 vcc, 17, v0\nv_mov_b32 v13, s97\n"
"v_cmpx_gt_i32 vcc, 16, v0\nv_mov_b32 v12, s97\nv_cmpx_gt_i32 vcc, 3, v0\nv_mov_b32 v7, s97\nv_cmpx_gt_i32 vcc, 2, v0\nv_mov_b32 v6, s97\nv_cmpx_gt_i32 vcc, 1, v0\n"
"v_mov_b32 v5, s97\nv_cmpx_gt_i32 vcc, 0, v0\nv_mov_b32 v4, s97\ns_mov_b64 exec, -1\ns_nop 2\n.E324:\nWL 8\nEX 83,39\ns_add_u32 s5, s66, 0\nSv 148,1,16,5,3072\nRa 152,26,34816\n"
"AD 85,85,82\nAD 87,87,97\nCV 192,192,193\nMvav 212,168,76\nD1 1,16,46,1024\nv_mov_b32 v96, v86\nCV 193,194,195\nMvav 220,172,76\nRa 156,26,35840\nv_mov_b32 v97, v87\n"
"CV 194,200,201\nCV 195,202,203\nAD 85,85,83\nCV 69,82,83\nv_permlane16_swap_b32 v86, v96\ns_add_u32 s5, s66, 16384\nSv 192,1,16,5,0\nRa 160,24,40960\n"
"v_permlane16_swap_b32 v87, v97\nCV 164,164,165\nCV 165,166,167\nMvav 228,176,76\ns_xor_b32 s89, s89, 0x10000\nEX 80,40\nCV 166,172,173\nMvav 236,180,76\nRa 164,24,41984\n"
"AD 86,86,96\nAD 87,87,97\nCV 167,174,175\nWL 6\nEX 81,41\ns_add_u32 s5, s66, 4096\nSv 164,1,16,5,1024\nRa 168,24,43008\nAD 84,84,80\nCV 208,208,209\nCV 209,210,211\n"
"Mvav 244,184,76\nD1 1,16,46,2048\nv_cmp_gt_u32 vcc, 0x100, v1\nv_mov_b32 v94, s15\nv_mov_b32 v95, s15\nv_cndmask_b32 v92, v89, v88, vcc\nv_cndmask_b32 v93, v87, v86, vcc\n"
"Mvav 252,188,76\nRa 172,24,44032\nv_mov_b32 v86, 0\nCV 210,216,217\nCV 211,218,219\nAD 84,84,81\nCV 66,80,81\ns_add_u32 s5, s66, 16384\nSv 208,1,16,5,2048\nCvaa 48,128,192,4\n"
"Ra 176,26,40960\nv_mov_b32 v87, 0\nv_mov_b32 v88, 0\nCV 180,180,181\nCV 181,182,183\nMvaa 48,132,200\ns_cmp_eq_u32 s99, 0\nEX 82,42\nMvaa 48,136,208\nRa 180,26,41984\n"
"s_cselect_b32 s86, s94, s86\nCV 182,188,189\nCV 183,190,191\nMvaa 48,140,216\nWL 6\nEX 83,43\ns_add_u32 s5, s66, 4096\nSv 180,1,16,5,3072\nCvaa 52,140,220,8\nRa 184,26,43008\n"
"AD 84,84,82\nv_mov_b32 v89, 0\nMvaa 52,136,212\nD1 1,16,46,3072\ns_add_u32 m0, s89, 0\nCV 224,224,225\nMvaa 52,132,204\nRa 188,26,44032\nCV 225,226,227\nCV 226,232,233\n"
"CV 227,234,235\nMvaa 52,128,196\nAD 84,84,83\nCV 67,82,83\ns_add_u32 s5, s66, 20480\nSv 224,1,16,5,0\nMvaa 52,144,228\nRa 128,2,49152\nCV 196,196,197\nCV 197,198,199\n"
"CV 198,204,205\nMvaa 52,148,236\ns_add_u32 s46, s86, s88\nEX 80,44\nMvaa 52,152,244\nRa 132,2,50176\nv_xor_b32 v24, 0x10000, v24\nCV 199,206,207\nMvaa 52,156,252\nWL 6\nEX 81,45\n"
"s_add_u32 s5, s66, 16384\nSv 196,1,16,5,1024\nMvaa 48,156,248\nRa 136,2,51200\nAD 85,85,80\nCV 240,240,241\nMvaa 48,152,240\nD1 1,16,46,0\nCV 241,242,243\nMvaa 48,148,232\n"
"Ra 140,2,52224\nCV 242,248,249\nCV 243,250,251\nMvaa 48,144,224\nAD 85,85,81\nCV 70,80,81\ns_add_u32 s5, s66, 20480\nSv 240,1,16,5,2048\nCvaa 56,160,192,12\nRa 144,2,53248\n"
"CV 212,212,213\nCV 213,214,215\nMvaa 56,164,200\nEX 82,46\nCV 214,220,221\nMvaa 56,168,208\nRa 148,2,54272\nv_xor_b32 v26, 0x10000, v26\nCV 215,222,223\nMvaa 56,172,216\nWL 6\n"
"EX 83,47\ns_add_u32 s5, s66, 16384\nSv 212,1,16,5,3072\nCvaa 60,172,220,16\nRa 152,2,55296\nAD 85,85,82\nCV 228,228,229\nMvaa 60,168,212\nD1 1,16,46,1024\nCV 229,230,231\n"
"Mvaa 60,164,204\nRa 156,2,56320\nCV 230,236,237\nCV 231,238,239\nCV 244,244,245\nMvaa 60,160,196\nAD 85,85,83\nCV 71,82,83\ns_add_u32 s5, s66, 20480\nSv 228,1,16,5,1024\n"
"Mvaa 60,176,228\nRa 160,2,57344\nv_max_f32 v90, v84, v85\nCV 245,246,247\nCV 246,252,253\nMvaa 60,180,236\ns_add_u32 s88, s88, 32768\nEX 80,48\nCV 247,254,255\nMvaa 60,184,244\n"
"Ra 164,2,58368\nv_cmp_lt_f32 vcc, s63, v90\nMvaa 60,188,252\nWL 8\nEX 81,49\nMvaa 56,188,248\nRa 168,2,59392\ns_cbranch_vccnz .T329\n.T330:\nv_mov_b32 v20, v84\n"
"v_mov_b32 v21, v85\nv_mov_b32 v22, v20\nv_mov_b32 v23, v21\ns_add_u32 s5, s66, 20480\nSv 244,1,16,5,3072\nMvaa 56,184,240\nD1 1,16,46,2048\nv_permlane32_swap_b32 v20, v22\n"
"v_permlane32_swap_b32 v21, v23\nAD 20,20,22\nAD 21,21,23\nv_mov_b32 v22, v20\nv_mov_b32 v23, v21\nMvaa 56,180,232\nRa 172,2,60416\nv_permlane16_swap_b32 v20, v22\n"
"v_permlane16_swap_b32 v21, v23\nAD 20,20,22\nAD 21,21,23\nv_min_f32 v90, v20, v21\nv_cmp_gt_f32 vcc, s64, v90\ns_nop 0\ns_cbranch_vccnz .T331\n.T332:\nAD 86,86,84\n"
"Mvaa 56,176,224\nAD 84,80,81\nCV 72,80,81\nZvav 128,128,64\nRa 176,2,61440\nZvav 132,128,68\nEX 82,50\nZvav 140,132,68\nRa 180,2,62464\nZvav 136,132,64\n"
"s_waitcnt vmcnt(27) lgkmcnt(8)\nEX 83,51\nZvav 144,136,64\nRa 184,2,63488\nAD 84,84,82\nZvav 148,136,68\nD1 1,16,46,3072\ns_add_u32 m0, s89, 16384\nZvav 156,140,68\n"
"Ra 188,2,64512\ns_barrier\nZvav 152,140,64\nAD 84,84,83\nCV 73,82,83\nZvav 160,144,64\nRa 128,24,0\nZvav 164,144,68\ns_add_u32 s46, s87, s88\nEX 80,52\nZvav 172,148,68\n"
"Ra 132,24,1024\nv_xor_b32 v2, 0x10000, v2\nZvav 168,148,64\nWL 8\nEX 81,53\nZvav 176,152,64\nRa 136,24,2048\nAD 87,87,85\nZvav 180,152,68\nD1 1,16,46,0\nZvav 188,156,68\n"
"Ra 140,24,3072\nZvav 184,156,64\nAD 85,80,81\nCV 76,80,81\nZvav 192,160,64\nRa 144,26,0\nZvav 196,160,68\ns_cmp_lt_u32 s99, s92\nEX 82,54\nZvav 204,164,68\nRa 148,26,1024\n"
"s_cbranch_scc0 .E325\nv_cmpx_gt_i32 vcc, 19, v3\nv_mov_b32 v19, s97\nv_cmpx_gt_i32 vcc, 18, v3\nv_mov_b32 v18, s97\nv_cmpx_gt_i32 vcc, 17, v3\nv_mov_b32 v17, s97\n"
"v_cmpx_gt_i32 vcc, 16, v3\nv_mov_b32 v16, s97\nv_cmpx_gt_i32 vcc, 3, v3\nv_mov_b32 v11, s97\nv_cmpx_gt_i32 vcc, 2, v3\nv_mov_b32 v10, s97\nv_cmpx_gt_i32 vcc, 1, v3\n"
"v_mov_b32 v9, s97\nv_cmpx_gt_i32 vcc, 0, v3\nv_mov_b32 v8, s97\ns_mov_b64 exec, -1\ns_nop 2\n.E325:\ns_nop 0\nZvav 200,164,64\nWL 8\nEX 83,55\nZvav 208,168,64\nRa 152,26,2048\n"
"AD 85,85,82\nZvav 212,168,68\nD1 1,16,46,1024\nZvav 220,172,68\nRa 156,26,3072\nZvav 216,172,64\nAD 85,85,83\nCV 77,82,83\nZvav 224,176,64\nRa 160,24,8192\nZvav 228,176,68\n"
"s_sub_u32 s90, s90, 1\nEX 80,56\nZvav 236,180,68\nRa 164,24,9216\ns_cbranch_scc0 .E323\ns_swappc_b64 s[80:81], s[8:9]\n.E323:\nZvav 232,180,64\nWL 6\nEX 81,57\nZvav 240,184,64\n"
"Ra 168,24,10240\nAD 84,84,80\nZvav 244,184,68\nD1 1,16,46,2048\nZvav 252,188,68\nRa 172,24,11264\nZvav 248,188,64\nAD 84,84,81\n.W571:\ns_cbranch_execz .WB273\nCV 74,80,81\n"
"v_mov_b32 v91, 0x40000000\nv_cmp_gt_u32 vcc, 0x200, v1\nWV 5\ns_nop 0\nv_cndmask_b32 v91, v91, v1, vcc\nSv 92,91,16,65,0\ns_sub_u32 s99, s99, 1\ns_cbranch_scc1 .LE275\n.p2align 6\n"
".A333:\nCvaa 32,128,192,4\nRa 176,26,8192\nMvaa 32,132,200\ns_add_u32 s49, s49, 131072\nEX 82,58\nMvaa 32,136,208\nRa 180,26,9216\nMvaa 32,140,216\nWL 6\nEX 83,59\n"
"Cvaa 36,140,220,8\nRa 184,26,10240\nAD 84,84,82\nMvaa 36,136,212\nD1 1,16,46,3072\ns_add_u32 m0, s89, 32768\nMvaa 36,132,204\nRa 188,26,11264\nMvaa 36,128,196\nAD 84,84,83\n"
"CV 75,82,83\nMvaa 36,144,228\nRa 128,2,16384\nMvaa 36,148,236\ns_add_u32 s46, s86, s88\nEX 80,60\nMvaa 36,152,244\nRa 132,2,17408\nMvaa 36,156,252\nWL 6\nEX 81,61\n"
"Mvaa 32,156,248\nRa 136,2,18432\nAD 85,85,80\nMvaa 32,152,240\nD1 1,16,46,0\nMvaa 32,148,232\nRa 140,2,19456\nMvaa 32,144,224\nAD 85,85,81\nCV 78,80,81\nCvaa 40,160,192,12\n"
"Ra 144,2,20480\nMvaa 40,164,200\nSv 112,1,16,49,0\nEX 82,62\nMvaa 40,168,208\nRa 148,2,21504\nMvaa 40,172,216\nWL 6\nEX 83,63\nCvaa 44,172,220,16\nRa 152,2,22528\nAD 85,85,82\n"
"Mvaa 44,168,212\nD1 1,16,46,1024\nMvaa 44,164,204\nRa 156,2,23552\nMvaa 44,160,196\nAD 85,85,83\nCV 79,82,83\nMvaa 44,176,228\nRa 160,2,24576\nv_max_f32 v90, v84, v85\n"
"Mvaa 44,180,236\ns_add_u32 s88, s88, 32768\nEX 80,32\nMvaa 44,184,244\nRa 164,2,25600\nv_cmp_lt_f32 vcc, s63, v90\nMvaa 44,188,252\nWL 8\nEX 81,33\nMvaa 40,188,248\n"
"Ra 168,2,26624\ns_cbranch_vccnz .T341\n.T342:\nMvaa 40,184,240\nD1 1,16,46,2048\nMvaa 40,180,232\nRa 172,2,27648\nAD 86,86,84\nMvaa 40,176,224\nAD 84,80,81\nCV 64,80,81\n"
"Mvav 128,128,72\nRa 176,2,28672\nMvav 132,128,76\nEX 82,34\nMvav 140,132,76\nRa 180,2,29696\nMvav 136,132,72\ns_waitcnt vmcnt(13) lgkmcnt(8)\nEX 83,35\nMvav 144,136,72\n"
"Ra 184,2,30720\nAD 84,84,82\nMvav 148,136,76\nD1 1,16,46,3072\ns_add_u32 m0, s89, 49152\nMvav 156,140,76\nRa 188,2,31744\ns_barrier\nMvav 152,140,72\nAD 84,84,83\nCV 65,82,83\n"
"Mvav 160,144,72\nRa 128,24,32768\nMvav 164,144,76\ns_add_u32 s46, s87, s88\nEX 80,36\nMvav 172,148,76\nRa 132,24,33792\nMvav 168,148,72\nWL 8\nEX 81,37\nMvav 176,152,72\n"
"Ra 136,24,34816\nAD 87,87,85\nMvav 180,152,76\nD1 1,16,46,0\nMvav 188,156,76\nRa 140,24,35840\nMvav 184,156,72\nAD 85,80,81\nCV 68,80,81\nMvav 192,160,72\nRa 144,26,32768\n"
"Mvav 196,160,76\ns_cmp_eq_u32 s99, 0\nEX 82,38\nMvav 204,164,76\nRa 148,26,33792\ns_cbranch_scc1 .E335\n.E336:\nMvav 200,164,72\nWL 8\nEX 83,39\nMvav 208,168,72\nRa 152,26,34816\n"
"AD 85,85,82\nMvav 212,168,76\nD1 1,16,46,1024\nMvav 220,172,76\nRa 156,26,35840\nMvav 216,172,72\nAD 85,85,83\nCV 69,82,83\nMvav 224,176,72\nRa 160,24,40960\nMvav 228,176,76\n"
"s_xor_b32 s89, s89, 0x10000\nEX 80,40\nMvav 236,180,76\nRa 164,24,41984\nMvav 232,180,72\nWL 6\nEX 81,41\nMvav 240,184,72\nRa 168,24,43008\nAD 84,84,80\nMvav 244,184,76\n"
"D1 1,16,46,2048\nMvav 252,188,76\nRa 172,24,44032\nMvav 248,188,72\nAD 84,84,81\nCV 66,80,81\nCvaa 48,128,192,4\nRa 176,26,40960\nMvaa 48,132,200\ns_cmp_eq_u32 s99, 0\nEX 82,42\n"
"Mvaa 48,136,208\nRa 180,26,41984\ns_cselect_b32 s86, s94, s86\nMvaa 48,140,216\nWL 6\nEX 83,43\nCvaa 52,140,220,8\nRa 184,26,43008\nAD 84,84,82\nMvaa 52,136,212\nD1 1,16,46,3072\n"
"s_add_u32 m0, s89, 0\nMvaa 52,132,204\nRa 188,26,44032\nMvaa 52,128,196\nAD 84,84,83\nCV 67,82,83\nMvaa 52,144,228\nRa 128,2,49152\nMvaa 52,148,236\ns_add_u32 s46, s86, s88\n"
"EX 80,44\nMvaa 52,152,244\nRa 132,2,50176\nv_xor_b32 v24, 0x10000, v24\nMvaa 52,156,252\nWL 6\nEX 81,45\nMvaa 48,156,248\nRa 136,2,51200\nAD 85,85,80\nMvaa 48,152,240\n"
"D1 1,16,46,0\nMvaa 48,148,232\nRa 140,2,52224\nMvaa 48,144,224\nAD 85,85,81\nCV 70,80,81\nCvaa 56,160,192,12\nRa 144,2,53248\nMvaa 56,164,200\nSv 112,1,16,49,1024\nEX 82,46\n"
"Mvaa 56,168,208\nRa 148,2,54272\nv_xor_b32 v26, 0x10000, v26\nMvaa 56,172,216\nWL 6\nEX 83,47\nCvaa 60,172,220,16\nRa 152,2,55296\nAD 85,85,82\nMvaa 60,168,212\nD1 1,16,46,1024\n"
"Mvaa 60,164,204\nRa 156,2,56320\nMvaa 60,160,196\nAD 85,85,83\nCV 71,82,83\nMvaa 60,176,228\nRa 160,2,57344\nv_max_f32 v90, v84, v85\nMvaa 60,180,236\ns_add_u32 s88, s88, 32768\n"
"EX 80,48\nMvaa 60,184,244\nRa 164,2,58368\nv_cmp_lt_f32 vcc, s63, v90\nMvaa 60,188,252\nWL 8\nEX 81,49\nMvaa 56,188,248\nRa 168,2,59392\ns_cbranch_vccnz .T343\n.T344:\n"
"Mvaa 56,184,240\nD1 1,16,46,2048\nMvaa 56,180,232\nRa 172,2,60416\nAD 86,86,84\nMvaa 56,176,224\nAD 84,80,81\nCV 72,80,81\nMvav 128,128,64\nRa 176,2,61440\nMvav 132,128,68\n"
"EX 82,50\nMvav 140,132,68\nRa 180,2,62464\nMvav 136,132,64\ns_waitcnt vmcnt(13) lgkmcnt(8)\nEX 83,51\nMvav 144,136,64\nRa 184,2,63488\nAD 84,84,82\nMvav 148,136,68\n"
"D1 1,16,46,3072\ns_add_u32 m0, s89, 16384\nMvav 156,140,68\nRa 188,2,64512\ns_barrier\nMvav 152,140,64\nAD 84,84,83\nCV 73,82,83\nMvav 160,144,64\nRa 128,24,0\nMvav 164,144,68\n"
"s_add_u32 s46, s87, s88\nEX 80,52\nMvav 172,148,68\nRa 132,24,1024\nv_xor_b32 v2, 0x10000, v2\nMvav 168,148,64\nWL 8\nEX 81,53\nMvav 176,152,64\nRa 136,24,2048\nAD 87,87,85\n"
"Mvav 180,152,68\nD1 1,16,46,0\nMvav 188,156,68\nRa 140,24,3072\nMvav 184,156,64\nAD 85,80,81\nCV 76,80,81\nMvav 192,160,64\nRa 144,26,0\nMvav 196,160,68\ns_cmp_lt_u32 s99, s92\n"
"EX 82,54\nMvav 204,164,68\nRa 148,26,1024\ns_cbranch_scc1 .E337\n.E338:\nMvav 200,164,64\nWL 8\nEX 83,55\nMvav 208,168,64\nRa 152,26,2048\nAD 85,85,82\nMvav 212,168,68\n"
"D1 1,16,46,1024\nMvav 220,172,68\nRa 156,26,3072\nMvav 216,172,64\nAD 85,85,83\nCV 77,82,83\nMvav 224,176,64\nRa 160,24,8192\nMvav 228,176,68\ns_sub_u32 s90, s90, 1\nEX 80,56\n"
"Mvav 236,180,68\nRa 164,24,9216\ns_cbranch_scc1 .E339\n.E340:\nMvav 232,180,64\nWL 6\nEX 81,57\nMvav 240,184,64\nRa 168,24,10240\nAD 84,84,80\nMvav 244,184,68\nD1 1,16,46,2048\n"
"s_sub_u32 s99, s99, 1\nMvav 252,188,68\nRa 172,24,11264\nMvav 248,188,64\nAD 84,84,81\nCV 74,80,81\ns_cbranch_scc0 .A333\ns_branch .A345\n.E335:\nv_cmpx_gt_i32 vcc, 19, v0\n"
"v_mov_b32 v15, s97\nv_cmpx_gt_i32 vcc, 18, v0\nv_mov_b32 v14, s97\nv_cmpx_gt_i32 vcc, 17, v0\nv_mov_b32 v13, s97\nv_cmpx_gt_i32 vcc, 16, v0\nv_mov_b32 v12, s97\n"
"v_cmpx_gt_i32 vcc, 3, v0\nv_mov_b32 v7, s97\nv_cmpx_gt_i32 vcc, 2, v0\nv_mov_b32 v6, s97\nv_cmpx_gt_i32 vcc, 1, v0\nv_mov_b32 v5, s97\nv_cmpx_gt_i32 vcc, 0, v0\nv_mov_b32 v4, s97\n"
"s_mov_b64 exec, -1\ns_nop 2\ns_branch .E336\n.E337:\ns_cmp_eq_u32 s99, 0\ns_cbranch_scc1 .K334\nv_cmpx_gt_i32 vcc, 19, v3\nv_mov_b32 v15, s97\nv_cmpx_gt_i32 vcc, 18, v3\n"
"v_mov_b32 v14, s97\nv_cmpx_gt_i32 vcc, 17, v3\nv_mov_b32 v13, s97\nv_cmpx_gt_i32 vcc, 16, v3\nv_mov_b32 v12, s97\nv_cmpx_gt_i32 vcc, 3, v3\nv_mov_b32 v7, s97\n"
"v_cmpx_gt_i32 vcc, 2, v3\nv_mov_b32 v6, s97\nv_cmpx_gt_i32 vcc, 1, v3\nv_mov_b32 v5, s97\nv_cmpx_gt_i32 vcc, 0, v3\nv_mov_b32 v4, s97\ns_mov_b64 exec, -1\ns_nop 2\ns_branch .E338\n"
".K334:\nv_cmpx_gt_i32 vcc, 19, v3\nv_mov_b32 v19, s97\nv_cmpx_gt_i32 vcc, 18, v3\nv_mov_b32 v18, s97\nv_cmpx_gt_i32 vcc, 17, v3\nv_mov_b32 v17, s97\nv_cmpx_gt_i32 vcc, 16, v3\n"
"v_mov_b32 v16, s97\nv_cmpx_gt_i32 vcc, 3, v3\nv_mov_b32 v11, s97\nv_cmpx_gt_i32 vcc, 2, v3\nv_mov_b32 v10, s97\nv_cmpx_gt_i32 vcc, 1, v3\nv_mov_b32 v9, s97\n"
"v_cmpx_gt_i32 vcc, 0, v3\nv_mov_b32 v8, s97\ns_mov_b64 exec, -1\ns_nop 2\ns_branch .E338\n.E339:\ns_swappc_b64 s[80:81], s[8:9]\ns_branch .E340\n.A345:\n.LE275:\nRa 176,26,8192\n"
"EX 82,58\nRa 180,26,9216\nWL 6\nEX 83,59\nCvaa 36,140,220,8\nRa 184,26,10240\nAD 84,84,82\nMvaa 36,136,212\nD1 1,16,46,3072\ns_add_u32 m0, s89, 32768\nMvaa 36,132,204\n"
"Ra 188,26,11264\nMvaa 36,128,196\nAD 84,84,83\nCV 75,82,83\nMvaa 36,144,228\nRa 128,2,16384\nMvaa 36,148,236\ns_add_u32 s46, s86, s88\nEX 80,60\nMvaa 36,152,244\nRa 132,2,17408\n"
"Mvaa 36,156,252\nEX 81,61\nRa 136,2,18432\nAD 85,85,80\nD1 1,16,46,0\nRa 140,2,19456\nAD 85,85,81\nCV 78,80,81\nRa 144,2,20480\ns_mov_b32 s87, s95\nEX 82,62\nRa 148,2,21504\nWL 6\n"
"EX 83,63\nCvaa 44,172,220,16\nRa 152,2,22528\nAD 85,85,82\nMvaa 44,168,212\nD1 1,16,46,1024\nMvaa 44,164,204\nRa 156,2,23552\nMvaa 44,160,196\nAD 85,85,83\nCV 79,82,83\n"
"Mvaa 44,176,228\nRa 160,2,24576\nv_max_f32 v90, v84, v85\nMvaa 44,180,236\ns_add_u32 s88, s88, 32768\nMvaa 44,184,244\nRa 164,2,25600\nv_cmp_lt_f32 vcc, s63, v90\nMvaa 44,188,252\n"
"WL 8\nRa 168,2,26624\ns_cbranch_vccnz .T347\n.T348:\nD1 1,16,46,2048\nRa 172,2,27648\nAD 86,86,84\nMvav 128,128,72\nRa 176,2,28672\nMvav 132,128,76\nMvav 140,132,76\n"
"Ra 180,2,29696\nMvav 136,132,72\ns_waitcnt vmcnt(12) lgkmcnt(8)\nMvav 144,136,72\nRa 184,2,30720\nMvav 148,136,76\nD1 1,16,46,3072\ns_add_u32 m0, s89, 49152\nMvav 156,140,76\n"
"Ra 188,2,31744\ns_barrier\nMvav 152,140,72\nMvav 160,144,72\nRa 128,24,32768\nMvav 164,144,76\ns_add_u32 s46, s87, s88\nEX 80,36\nMvav 172,148,76\nRa 132,24,33792\n"
"Mvav 168,148,72\nWL 8\nEX 81,37\nMvav 176,152,72\nRa 136,24,34816\nAD 87,87,85\nMvav 180,152,76\nD1 1,16,46,0\nMvav 188,156,76\nRa 140,24,35840\nMvav 184,156,72\nAD 85,80,81\n"
"CV 68,80,81\nMvav 192,160,72\nRa 144,26,32768\nMvav 196,160,76\ns_nop 0\nEX 82,38\nMvav 204,164,76\nRa 148,26,33792\nv_cmpx_gt_i32 vcc, 19, v0\nv_mov_b32 v19, s97\n"
"v_cmpx_gt_i32 vcc, 18, v0\nv_mov_b32 v18, s97\nv_cmpx_gt_i32 vcc, 17, v0\nv_mov_b32 v17, s97\nv_cmpx_gt_i32 vcc, 16, v0\nv_mov_b32 v16, s97\nv_cmpx_gt_i32 vcc, 3, v0\n"
"v_mov_b32 v11, s97\nv_cmpx_gt_i32 vcc, 2, v0\nv_mov_b32 v10, s97\nv_cmpx_gt_i32 vcc, 1, v0\nv_mov_b32 v9, s97\nv_cmpx_gt_i32 vcc, 0, v0\nv_mov_b32 v8, s97\ns_mov_b64 exec, -1\n"
"s_nop 2\ns_nop 0\nMvav 200,164,72\nWL 8\nEX 83,39\nMvav 208,168,72\nRa 152,26,34816\nAD 85,85,82\nMvav 212,168,76\nD1 1,16,46,1024\nMvav 220,172,76\nRa 156,26,35840\n"
"Mvav 216,172,72\nAD 85,85,83\nCV 69,82,83\nMvav 224,176,72\nRa 160,24,40960\nMvav 228,176,76\ns_xor_b32 s89, s89, 0x10000\nMvav 236,180,76\nRa 164,24,41984\nMvav 232,180,72\n"
"WL 10\nMvav 240,184,72\nRa 168,24,43008\nMvav 244,184,76\nD1 1,16,46,2048\nMvav 252,188,76\nRa 172,24,44032\nMvav 248,188,72\nRa 176,26,40960\nRa 180,26,41984\nWL 6\n"
"Cvaa 52,140,220,8\nRa 184,26,43008\nMvaa 52,136,212\nD1 1,16,46,3072\ns_add_u32 m0, s89, 0\nMvaa 52,132,204\nRa 188,26,44032\nMvaa 52,128,196\nMvaa 52,144,228\nRa 128,2,49152\n"
"Mvaa 52,148,236\ns_add_u32 s46, s86, s88\nEX 80,44\nMvaa 52,152,244\nRa 132,2,50176\nv_xor_b32 v24, 0x10000, v24\nMvaa 52,156,252\nEX 81,45\nRa 136,2,51200\nAD 85,85,80\n"
"D1 1,16,46,0\nRa 140,2,52224\nAD 85,85,81\nCV 70,80,81\nRa 144,2,53248\nEX 82,46\nRa 148,2,54272\nv_xor_b32 v26, 0x10000, v26\nWL 6\nEX 83,47\nCvaa 60,172,220,16\nRa 152,2,55296\n"
"AD 85,85,82\nMvaa 60,168,212\nD1 1,16,46,1024\nMvaa 60,164,204\nRa 156,2,56320\nMvaa 60,160,196\nAD 85,85,83\nCV 71,82,83\nMvaa 60,176,228\nRa 160,2,57344\nMvaa 60,180,236\n"
"s_add_u32 s88, s88, 32768\nMvaa 60,184,244\nRa 164,2,58368\nv_cmp_lt_f32 vcc, s63, v85\nMvaa 60,188,252\nWL 8\nRa 168,2,59392\ns_cbranch_vccnz .T349\n.T350:\nD1 1,16,46,2048\n"
"Ra 172,2,60416\nRa 176,2,61440\nMvav 132,128,68\nMvav 140,132,68\nRa 180,2,62464\ns_waitcnt vmcnt(11) lgkmcnt(8)\nRa 184,2,63488\nMvav 148,136,68\nD1 1,16,46,3072\n"
"s_add_u32 m0, s89, 16384\nMvav 156,140,68\nRa 188,2,64512\ns_barrier\nRa 128,24,0\nMvav 164,144,68\n.W572:\ns_cbranch_execz .WB273\ns_add_u32 s46, s87, s88\nEX 80,52\n"
"Mvav 172,148,68\nRa 132,24,1024\nv_xor_b32 v2, 0x10000, v2\nWL 8\nEX 81,53\nRa 136,24,2048\nAD 87,87,85\nMvav 180,152,68\nD1 1,16,46,0\nMvav 188,156,68\nRa 140,24,3072\n"
"AD 85,80,81\nCV 76,80,81\nRa 144,26,0\nMvav 196,160,68\nEX 82,54\nMvav 204,164,68\nRa 148,26,1024\nWL 8\nEX 83,55\nRa 152,26,2048\nAD 85,85,82\nMvav 212,168,68\nD1 1,16,46,1024\n"
"Mvav 220,172,68\nRa 156,26,3072\nAD 85,85,83\nCV 77,82,83\nRa 160,24,8192\nMvav 228,176,68\nMvav 236,180,68\nRa 164,24,9216\nWL 10\nRa 168,24,10240\nMvav 244,184,68\n"
"D1 1,16,46,2048\nMvav 252,188,68\nRa 172,24,11264\nD1 1,16,46,3072\ns_add_u32 m0, s89, 32768\ns_add_u32 s46, s86, s88\nRa 128,2,16384\nv_xor_b32 v3, 0x10000, v2\n"
"v_add_u32 v0, 0x20000, v1\nEX 80,60\nRa 132,2,17408\ns_sub_u32 s94, s49, 1703936\nEX 81,61\nRa 136,2,18432\nAD 85,85,80\ns_xor_b32 s92, s94, 0x202000\nD1 1,16,46,0\n"
"s_lshl_b32 s89, s11, 12\nRa 140,2,19456\ns_lshl1_add_u32 s2, s12, 1\nAD 85,85,81\nCV 78,80,81\nRa 144,2,20480\ns_lshl3_add_u32 s2, s2, s13\nEX 82,62\nRa 148,2,21504\n"
"s_lshl4_add_u32 s2, s2, 0xc048200\nEX 83,63\nRa 152,2,22528\nAD 85,85,82\nD1 1,16,46,1024\nRa 156,2,23552\ns_and_b32 s3, s72, 0x2000\nAD 85,85,83\nCV 79,82,83\nRa 160,2,24576\n"
"s_cmp_eq_u32 s11, 1\ns_cselect_b32 s54, s2, s54\ns_cselect_b32 s3, s3, 0\nv_cmp_lt_f32 vcc, s63, v85\nRa 164,2,25600\ns_cbranch_vccnz .T351\n.T352:\nAD 87,87,85\nWL 8\n"
"v_mov_b32 v84, v86\nv_mov_b32 v85, v87\nRa 168,2,26624\nv_permlane32_swap_b32 v86, v84\nv_permlane32_swap_b32 v87, v85\nD1 1,16,46,2048\nAD 86,86,84\nRa 172,2,27648\nAD 87,87,85\n"
"v_mov_b32 v84, v86\nv_mov_b32 v85, v87\nRa 176,2,28672\nv_permlane16_swap_b32 v86, v84\nv_permlane16_swap_b32 v87, v85\nMvav 132,128,76\nAD 86,86,84\nAD 87,87,85\nMvav 140,132,76\n"
"Ra 180,2,29696\nv_rcp_f32 v86, v86\nWL 8\nv_rcp_f32 v87, v87\nRa 184,2,30720\nv_xor_b32 v25, s3, v25\nMvav 148,136,76\nD1 1,16,46,3072\nMvav 156,140,76\nRa 188,2,31744\n"
"MU 128,128,86\nMU 129,129,86\nCV 96,128,129\nMU 130,130,86\nMU 131,131,86\nRa 128,2,0\nCV 97,130,131\nMU 132,132,87\nMvav 164,144,76\nMU 133,133,87\nCV 100,132,133\n"
"Mvav 172,148,76\nRa 132,2,1024\nMU 134,134,87\nMU 135,135,87\nWL 8\ns_add_u32 m0, s89, 131072\ns_add_u32 s46, s96, 1212416\nRa 136,2,2048\nCV 101,134,135\nMU 136,136,86\n"
"Mvav 180,152,76\nD1 1,16,46,0\nMU 137,137,86\nMvav 188,156,76\nRa 140,2,3072\nCV 98,136,137\nMU 138,138,86\nMU 139,139,86\nCV 99,138,139\nMU 140,140,87\nRa 144,2,4096\n"
"MU 141,141,87\nCV 102,140,141\nMvav 196,160,76\nMU 142,142,87\nMU 143,143,87\nMvav 204,164,76\nRa 148,2,5120\nCV 103,142,143\nMU 144,144,86\nWL 8\nMU 145,145,86\nCV 104,144,145\n"
"Ra 152,2,6144\nMU 146,146,86\nMU 147,147,86\nMvav 212,168,76\nD1 1,16,46,1024\nCV 105,146,147\nMvav 220,172,76\nRa 156,2,7168\nMU 148,148,87\nMU 149,149,87\nCV 108,148,149\n"
"MU 150,150,87\nMU 151,151,87\nRa 160,2,8192\nCV 109,150,151\nMU 152,152,86\nMvav 228,176,76\nMU 153,153,86\nCV 106,152,153\nMvav 236,180,76\nRa 164,2,9216\nMU 154,154,86\n"
"MU 155,155,86\nWL 8\nCV 107,154,155\nMU 156,156,87\nRa 168,2,10240\nMU 157,157,87\nCV 110,156,157\nMvav 244,184,76\nD1 1,16,46,2048\nMU 158,158,87\nMvav 252,188,76\n"
"Ra 172,2,11264\nMU 159,159,87\nCV 111,158,159\nMU 160,160,86\nMU 161,161,86\nCV 112,160,161\nZvav 128,128,96\nRa 176,2,12288\nMU 162,162,86\nMU 163,163,86\nMvav 128,132,104\n"
"CV 113,162,163\nMU 164,164,87\nZvav 132,132,108\nRa 180,2,13312\nMU 165,165,87\nCV 116,164,165\nMvav 132,128,100\ns_waitcnt vmcnt(15) lgkmcnt(8)\nMU 166,166,87\nMU 167,167,87\n"
"Zvav 140,136,100\nRa 184,2,14336\nCV 117,166,167\nMU 168,168,86\nMvav 140,140,108\nD1 1,16,46,3072\nMU 169,169,86\nCV 114,168,169\nZvav 136,140,104\nRa 188,2,15360\ns_barrier\n"
"MU 170,170,86\nMvav 136,136,96\nMU 171,171,86\nCV 115,170,171\nMU 172,172,87\nZvav 144,144,96\nRa 128,2,32768\nMU 173,173,87\nCV 118,172,173\nMvav 144,148,104\nMU 174,174,87\n"
"MU 175,175,87\nbuffer_store_dword v25, off, s[16:19], s54 offset:512 sc0 sc1\nZvav 148,148,108\nRa 132,2,33792\nCV 119,174,175\nMU 176,176,86\nMvav 148,144,100\nWL 8\n"
"s_add_u32 m0, s89, 49152\ns_add_u32 s46, s96, 1179648\nZvav 156,152,100\nRa 136,2,34816\nMU 177,177,86\nCV 120,176,177\nMvav 156,156,108\nD1 1,16,46,0\nMU 178,178,86\n"
"MU 179,179,86\nZvav 152,156,104\nRa 140,2,35840\nCV 121,178,179\nMU 180,180,87\nMvav 152,152,96\nMU 181,181,87\nCV 124,180,181\nMU 182,182,87\nZvav 64,160,96\nRa 144,2,36864\n"
"MU 183,183,87\nCV 125,182,183\nMvav 64,164,104\nMU 184,184,86\nMU 185,185,86\nZvav 68,164,108\nRa 148,2,37888\nCV 122,184,185\nMU 186,186,86\nMvav 68,160,100\nWL 8\nMU 187,187,86\n"
"CV 123,186,187\nZvav 76,168,100\nRa 152,2,38912\nMU 188,188,87\nMU 189,189,87\nMvav 76,172,108\nD1 1,16,46,1024\nCV 126,188,189\nMU 190,190,87\nZvav 72,172,104\nRa 156,2,39936\n"
"MU 191,191,87\nCV 127,190,191\nMvav 72,168,96\nMU 192,192,86\nMU 193,193,86\nCV 32,192,193\nMvav 128,176,112\nRa 160,2,40960\nMU 194,194,86\nMU 195,195,86\nMvav 128,180,120\n"
"CV 33,194,195\nMU 196,196,87\nMU 197,197,87\nMvav 132,180,124\nRa 164,2,41984\nCV 36,196,197\nMU 198,198,87\nMvav 132,176,116\nWL 8\nMU 199,199,87\nMvav 140,184,116\n"
"Ra 168,2,43008\nCV 37,198,199\nMU 200,200,86\nMvav 140,188,124\nD1 1,16,46,2048\nMU 201,201,86\nCV 34,200,201\nMvav 136,188,120\nRa 172,2,44032\nMU 202,202,86\nMU 203,203,86\n"
"Mvav 136,184,112\nCV 35,202,203\nMU 204,204,87\nMU 205,205,87\nMvav 144,128,112\nRa 176,2,45056\nCV 38,204,205\nMU 206,206,87\nMvav 144,132,120\nMU 207,207,87\nCV 39,206,207\n"
"Mvav 148,132,124\nRa 180,2,46080\nMU 208,208,86\nMU 209,209,86\nMvav 148,128,116\nWL 8\nCV 40,208,209\nMU 210,210,86\nMvav 156,136,116\nRa 184,2,47104\nMU 211,211,86\n"
"CV 41,210,211\nMvav 156,140,124\nD1 1,16,46,3072\nMU 212,212,87\nMU 213,213,87\nMvav 152,140,120\nRa 188,2,48128\nCV 44,212,213\nMvav 152,136,112\nMU 214,214,87\nMU 215,215,87\n"
"CV 45,214,215\nMvav 64,144,112\nRa 128,2,49152\nMU 216,216,86\nMU 217,217,86\nMvav 64,148,120\nCV 42,216,217\nMU 218,218,86\nMU 219,219,86\nMvav 68,148,124\nRa 132,2,50176\n"
"CV 43,218,219\nMU 220,220,87\nMvav 68,144,116\nWL 8\ns_add_u32 m0, s89, 147456\ns_add_u32 s46, s96, 1245184\nMvav 76,152,116\nRa 136,2,51200\nMU 221,221,87\nCV 46,220,221\n"
"Mvav 76,156,124\nD1 1,16,46,0\nMU 222,222,87\nMU 223,223,87\nMvav 72,156,120\nRa 140,2,52224\nCV 47,222,223\nMvav 72,152,112\nMU 224,224,86\nMU 225,225,86\nCV 48,224,225\n"
"Mvav 128,160,32\nRa 144,2,53248\nMU 226,226,86\nMU 227,227,86\nMvav 128,164,40\nCV 49,226,227\nMU 228,228,87\nMvav 132,164,44\nRa 148,2,54272\nMU 229,229,87\nCV 52,228,229\n"
"Mvav 132,160,36\nWL 8\nMU 230,230,87\nMU 231,231,87\nMvav 140,168,36\nRa 152,2,55296\nCV 53,230,231\nMU 232,232,86\nMvav 140,172,44\nD1 1,16,46,1024\nMU 233,233,86\nCV 50,232,233\n"
"Mvav 136,172,40\nRa 156,2,56320\nMU 234,234,86\nMvav 136,168,32\nMU 235,235,86\nCV 51,234,235\nMU 236,236,87\nMvav 144,176,32\nRa 160,2,57344\nMU 237,237,87\nCV 54,236,237\n"
"Mvav 144,180,40\nMU 238,238,87\nMU 239,239,87\nCV 55,238,239\nMvav 148,180,44\nRa 164,2,58368\nMU 240,240,86\nMU 241,241,86\nMvav 148,176,36\nWL 8\nCV 56,240,241\nMvav 156,184,36\n"
"Ra 168,2,59392\nMU 242,242,86\nMU 243,243,86\nMvav 156,188,44\nD1 1,16,46,2048\nCV 57,242,243\nMU 244,244,87\nMvav 152,188,40\nRa 172,2,60416\nMU 245,245,87\nCV 60,244,245\n"
"Mvav 152,184,32\nMU 246,246,87\nMU 247,247,87\nCV 61,246,247\nMvav 64,128,32\nRa 176,2,61440\nMU 248,248,86\nMU 249,249,86\nMvav 64,132,40\nCV 58,248,249\nMU 250,250,86\n"
"Mvav 68,132,44\nRa 180,2,62464\nMU 251,251,86\nCV 59,250,251\nMvav 68,128,36\ns_waitcnt vmcnt(16) lgkmcnt(8)\nMU 252,252,87\nMU 253,253,87\nMvav 76,136,36\nRa 184,2,63488\n"
"CV 62,252,253\nMU 254,254,87\nMvav 76,140,44\nD1 1,16,46,3072\nMU 255,255,87\nCV 63,254,255\nMvav 72,140,40\nRa 188,2,64512\ns_barrier\nMvav 72,136,32\nMvav 128,144,48\n"
"Ra 128,3,0\nMvav 128,148,56\nMvav 132,148,60\nRa 132,3,1024\nMvav 132,144,52\nWL 8\nMvav 140,152,52\nRa 136,3,2048\nMvav 140,156,60\nMvav 136,156,56\nRa 140,3,3072\n"
"Mvav 136,152,48\nMvav 144,160,48\nRa 144,3,4096\nMvav 144,164,56\nMvav 148,164,60\nRa 148,3,5120\nCV 132,132,133\nMvav 148,160,52\nWL 8\nCV 133,134,135\nMvav 156,168,52\n"
"Ra 152,3,6144\nCV 134,140,141\nMvav 156,172,60\nCV 135,142,143\ns_add_u32 s5, s94, 0\nMvav 152,172,56\nRa 156,3,7168\nSv 132,1,16,5,1024\nMvav 152,168,48\nCV 128,128,129\n"
"CV 129,130,131\nMvav 64,176,48\nRa 160,3,8192\nCV 130,136,137\nMvav 64,180,56\nbuffer_load_dword v27, off, s[16:19], s60 offset:512 sc0 sc1\nCV 131,138,139\nMvav 68,180,60\n"
"Ra 164,3,9216\nSv 128,1,16,5,0\nMvav 68,176,52\nWL 6\nCV 148,148,149\nMvav 76,184,52\nRa 168,3,10240\nCV 149,150,151\nMvav 76,188,60\nCV 150,156,157\nCV 151,158,159\n"
"Mvav 72,188,56\nRa 172,3,11264\ns_add_u32 s5, s94, 131072\nMvav 72,184,48\nSv 148,1,16,5,1024\nCV 144,144,145\nZvav 160,128,96\nRa 176,3,12288\nCV 145,146,147\nMvav 160,132,104\n"
"CV 146,152,153\nCV 147,154,155\nMvav 160,136,112\nRa 180,3,13312\nSv 144,1,16,5,0\nMvav 160,140,120\nWL 6\nCV 68,68,69\nZvav 164,140,124\nRa 184,3,14336\nCV 69,70,71\n"
"Mvav 164,136,116\nCV 70,76,77\nCV 71,78,79\nMvav 164,132,108\nRa 188,3,15360\ns_add_u32 s5, s94, 262144\nMvav 164,128,100\nSv 68,1,16,5,1024\nCV 64,64,65\nMvav 164,144,36\n"
"Ra 128,3,16384\nCV 65,66,67\nMvav 164,148,44\nCV 66,72,73\nCV 67,74,75\nMvav 164,152,52\nRa 132,3,17408\nSv 64,1,16,5,0\nMvav 164,156,60\nWL 6\nMvav 160,156,56\nRa 136,3,18432\n"
"Mvav 160,152,48\nMvav 160,148,40\nRa 140,3,19456\nMvav 160,144,32\nZvav 168,160,96\nRa 144,3,20480\nMvav 168,164,104\nMvav 168,168,112\nRa 148,3,21504\nMvav 168,172,120\nWL 6\n"
"Zvav 172,172,124\nRa 152,3,22528\nMvav 172,168,116\nMvav 172,164,108\nRa 156,3,23552\nMvav 172,160,100\nMvav 172,176,36\nRa 160,3,24576\nMvav 172,180,44\nMvav 172,184,52\n"
"Ra 164,3,25600\nMvav 172,188,60\nWL 6\nMvav 168,188,56\nRa 168,3,26624\nMvav 168,184,48\nMvav 168,180,40\nRa 172,3,27648\nMvav 168,176,32\nZvav 176,128,96\nRa 176,3,28672\n"
"CV 164,164,165\nMvav 176,132,104\nCV 165,166,167\nCV 166,172,173\nMvav 176,136,112\nRa 180,3,29696\nCV 167,174,175\nMvav 176,140,120\ns_waitcnt vmcnt(11) lgkmcnt(6)\n"
"s_add_u32 s5, s94, 393216\nZvav 180,140,124\nRa 184,3,30720\nSv 164,1,16,5,1024\nMvav 180,136,116\nCV 160,160,161\nCV 161,162,163\nMvav 180,132,108\n.W573:\n"
"s_cbranch_execz .WB273\nRa 188,3,31744\ns_barrier\nMvav 180,128,100\nCV 162,168,169\nCV 163,170,171\nMvav 180,144,36\nRa 128,3,32768\nSv 160,1,16,5,0\nMvav 180,148,44\n"
"Mvav 180,152,52\nRa 132,3,33792\nMvav 180,156,60\nWL 6\ns_add_u32 m0, s89, 81920\ns_add_u32 s46, s96, 1277952\nMvav 176,156,56\nRa 136,3,34816\nMvav 176,152,48\nD1 1,16,46,0\n"
"Mvav 176,148,40\nRa 140,3,35840\nMvav 176,144,32\nZvav 184,160,96\nRa 144,3,36864\nMvav 184,164,104\nMvav 184,168,112\nRa 148,3,37888\nMvav 184,172,120\nWL 6\nZvav 188,172,124\n"
"Ra 152,3,38912\nMvav 188,168,116\nD1 1,16,46,1024\nMvav 188,164,108\nRa 156,3,39936\nMvav 188,160,100\nMvav 188,176,36\nRa 160,3,40960\nWV 9\nv_cmp_ne_u32 vcc, s15, v27\ns_nop 0\n"
"s_cbranch_vccnz .S377\n.S378:\nMvav 188,180,44\ns_add_u32 s4, s92, 0\nMvav 188,184,52\nRa 164,3,41984\nMvav 188,188,60\nWL 6\n"
"buffer_load_dwordx4 v[208:211], v1, s[16:19], s4 offen offset:0 sc0 sc1\nMvav 184,188,56\nRa 168,3,43008\nMvav 184,184,48\nD1 1,16,46,2048\nMvav 184,180,40\nRa 172,3,44032\n"
"Mvav 184,176,32\nbuffer_load_dwordx4 v[212:215], v1, s[16:19], s4 offen offset:1024 sc0 sc1\nZvav 192,128,96\nRa 176,3,45056\nv_lshrrev_b32 v88, 8, v1\nMvav 192,132,104\n"
"v_lshlrev_b32 v89, 5, v88\nCV 180,180,181\nMvav 192,136,112\nRa 180,3,46080\nCV 181,182,183\nMvav 192,140,120\nWL 6\nCV 182,188,189\nZvav 196,140,124\nRa 184,3,47104\n"
"v_bfe_u32 v90, v1, 4, 4\nMvav 196,136,116\nD1 1,16,46,3072\nCV 183,190,191\nMvav 196,132,108\nRa 188,3,48128\ns_add_u32 s5, s94, 524288\nMvav 196,128,100\n"
"v_mul_u32_u24 v91, 896, v90\nSv 180,1,16,5,1024\nMvav 196,144,36\nRa 128,3,49152\nv_lshl_add_u32 v91, v88, 4, v91\nMvav 196,148,44\nv_add_u32 v90, s21, v90\nCV 176,176,177\n"
"Mvav 196,152,52\nRa 132,3,50176\nCV 177,178,179\nMvav 196,156,60\nWL 6\ns_add_u32 m0, s89, 65536\ns_add_u32 s46, s96, 1310720\nMvav 192,156,56\nRa 136,3,51200\n"
"v_lshlrev_b32 v88, 3, v90\nMvav 192,152,48\nD1 1,16,46,0\nCV 178,184,185\nMvav 192,148,40\nRa 140,3,52224\nCV 179,186,187\nMvav 192,144,32\nv_add_u32 v94, 64, v90\n"
"Sv 176,1,16,5,0\nZvav 200,160,96\nRa 144,3,53248\ns_mov_b32 s63, 0xc040000\nMvav 200,164,104\nv_lshrrev_b32 v92, 8, v1\nMvav 200,168,112\nRa 148,3,54272\nMvav 200,172,120\nWL 6\n"
"Zvav 204,172,124\nRa 152,3,55296\nv_bfe_u32 v93, v1, 6, 2\nMvav 204,168,116\nD1 1,16,46,1024\nv_cmp_eq_u32 vcc, v92, v93\nv_and_b32 v92, 16, v1\nv_mov_b32 v93, 0x3f80\n"
"v_cndmask_b32 v93, 0, v93, vcc\nMvav 204,164,108\nRa 156,3,56320\nMvav 204,160,100\nv_lshlrev_b32 v93, v92, v93\nMvav 204,176,36\nRa 160,3,57344\nv_and_b32 v92, 32, v1\n"
"Mvav 204,180,44\ns_add_u32 s4, s92, 131072\nv_cmp_eq_u32 vcc, 0, v92\nv_mov_b32 v4, 0\nv_mov_b32 v5, 0\nv_cndmask_b32 v6, 0, v93, vcc\nMvav 204,184,52\nRa 164,3,58368\n"
"Mvav 204,188,60\nWL 6\nbuffer_load_dwordx4 v[216:219], v1, s[16:19], s4 offen offset:0 sc0 sc1\nMvav 200,188,56\nRa 168,3,59392\nv_sub_u32 v7, v93, v6\nMvav 200,184,48\n"
"D1 1,16,46,2048\nMvav 200,180,40\nRa 172,3,60416\nMvav 200,176,32\nbuffer_load_dwordx4 v[220:223], v1, s[16:19], s4 offen offset:1024 sc0 sc1\nZvav 160,128,96\nRa 176,3,61440\n"
"v_mov_b32 v8, 0\nMvav 160,132,104\nv_mov_b32 v9, 0\nCV 196,196,197\nMvav 160,136,112\nRa 180,3,62464\nCV 197,198,199\nMvav 160,140,120\nWL 6\nCV 198,204,205\nZvav 164,140,124\n"
"Ra 184,3,63488\nCV 199,206,207\nMvav 164,136,116\nD1 1,16,46,3072\ns_add_u32 s5, s94, 655360\nMvav 164,132,108\nRa 188,3,64512\ns_barrier\nMvav 164,128,100\nSv 196,1,16,5,1024\n"
"CV 192,192,193\nMvav 164,144,36\nRa 128,0,0\nCV 193,194,195\nMvav 164,148,44\nCV 194,200,201\nCV 195,202,203\nMvav 164,152,52\nRa 132,0,1024\nSv 192,1,16,5,0\nMvav 164,156,60\n"
"WL 6\ns_add_u32 m0, s89, 98304\ns_add_u32 s46, s96, 1343488\nMvav 160,156,56\nRa 136,0,2048\nMvav 160,152,48\nD1 1,16,46,0\nMvav 160,148,40\nRa 140,0,3072\nMvav 160,144,32\n"
"Zvav 168,160,96\nRa 144,0,4096\nWV 12\nv_max_u32 v21, v211, v215\nv_cmp_eq_u32 vcc, -1, v21\ns_nop 0\ns_cbranch_vccnz .S353\n.S354:\nMvav 168,164,104\n"
"v_lshlrev_b32 v176, 16, v208\nMvav 168,168,112\nRa 148,0,5120\nMvav 168,172,120\nWL 6\nv_and_b32 v177, 0xffff0000, v208\nZvav 172,172,124\nRa 152,0,6144\nMvav 172,168,116\n"
"D1 1,16,46,1024\nMvav 172,164,108\nRa 156,0,7168\nv_lshlrev_b32 v178, 16, v209\nMvav 172,160,100\nv_and_b32 v179, 0xffff0000, v209\nMvav 172,176,36\nRa 160,0,8192\n"
"v_lshlrev_b32 v180, 16, v212\nMvav 172,180,44\nv_and_b32 v181, 0xffff0000, v212\ns_add_u32 s4, s92, 262144\nMvav 172,184,52\nRa 164,0,9216\nMvav 172,188,60\nWL 6\n"
"v_lshlrev_b32 v182, 16, v213\nbuffer_load_dwordx4 v[224:227], v1, s[16:19], s4 offen offset:0 sc0 sc1\nMvav 168,188,56\nRa 168,0,10240\nMvav 168,184,48\nD1 1,16,46,2048\n"
"Mvav 168,180,40\nRa 172,0,11264\nv_and_b32 v183, 0xffff0000, v213\nMvav 168,176,32\nv_lshlrev_b32 v184, 16, v210\n"
"buffer_load_dwordx4 v[228:231], v1, s[16:19], s4 offen offset:1024 sc0 sc1\nMvav 176,128,96\nRa 176,0,12288\nv_and_b32 v185, 0xffff0000, v210\nMvav 176,132,104\n"
"v_lshlrev_b32 v186, 16, v211\nCV 164,164,165\nMvav 176,136,112\nRa 180,0,13312\nCV 165,166,167\nMvav 176,140,120\nWL 6\nCV 166,172,173\nMvav 180,140,124\nRa 184,0,14336\n"
"v_and_b32 v187, 0xffff0000, v211\nMvav 180,136,116\nD1 1,16,46,3072\nCV 167,174,175\nMvav 180,132,108\nRa 188,0,15360\ns_add_u32 s5, s94, 786432\nMvav 180,128,100\n"
"v_lshlrev_b32 v188, 16, v214\nSv 164,1,16,5,1024\nMvav 180,144,36\nRa 128,0,16384\nv_and_b32 v189, 0xffff0000, v214\nMvav 180,148,44\nv_lshlrev_b32 v190, 16, v215\nCV 160,160,161\n"
"Mvav 180,152,52\nRa 132,0,17408\nCV 161,162,163\nMvav 180,156,60\nWL 6\ns_add_u32 m0, s89, 114688\ns_add_u32 s46, s96, 1376256\nMvav 176,156,56\nRa 136,0,18432\n"
"v_and_b32 v191, 0xffff0000, v215\nMvav 176,152,48\nD1 1,16,46,0\nCV 162,168,169\nMvav 176,148,40\nRa 140,0,19456\nCV 163,170,171\nMvav 176,144,32\nSv 160,1,16,5,0\n"
"Mvav 184,160,96\nRa 144,0,20480\nWV 12\nv_max_u32 v21, v219, v223\nv_cmp_eq_u32 vcc, -1, v21\ns_nop 0\ns_cbranch_vccnz .S356\n.S357:\nMvav 184,164,104\n"
"v_lshlrev_b32 v192, 16, v216\nMvav 184,168,112\nRa 148,0,21504\nMvav 184,172,120\nWL 6\nv_and_b32 v193, 0xffff0000, v216\nMvav 188,172,124\nRa 152,0,22528\nMvav 188,168,116\n"
"D1 1,16,46,1024\nMvav 188,164,108\nRa 156,0,23552\nv_lshlrev_b32 v194, 16, v217\nMvav 188,160,100\nv_and_b32 v195, 0xffff0000, v217\nMvav 188,176,36\nRa 160,0,24576\n"
"v_lshlrev_b32 v196, 16, v220\nMvav 188,180,44\nv_and_b32 v197, 0xffff0000, v220\ns_add_u32 s4, s92, 393216\nMvav 188,184,52\nRa 164,0,25600\nMvav 188,188,60\nWL 6\n"
"v_lshlrev_b32 v198, 16, v221\nbuffer_load_dwordx4 v[208:211], v1, s[16:19], s4 offen offset:0 sc0 sc1\nMvav 184,188,56\nRa 168,0,26624\nMvav 184,184,48\nD1 1,16,46,2048\n"
"Mvav 184,180,40\nRa 172,0,27648\nv_and_b32 v199, 0xffff0000, v221\nMvav 184,176,32\nv_lshlrev_b32 v200, 16, v218\n"
"buffer_load_dwordx4 v[212:215], v1, s[16:19], s4 offen offset:1024 sc0 sc1\nMvav 192,128,96\nRa 176,0,28672\nv_and_b32 v201, 0xffff0000, v218\nMvav 192,132,104\n"
"v_lshlrev_b32 v202, 16, v219\nCV 180,180,181\nMvav 192,136,112\nRa 180,0,29696\nCV 181,182,183\nMvav 192,140,120\ns_waitcnt vmcnt(15) lgkmcnt(6)\nCV 182,188,189\nMvav 196,140,124\n"
"Ra 184,0,30720\nv_and_b32 v203, 0xffff0000, v219\nMvav 196,136,116\nD1 1,16,46,3072\nCV 183,190,191\nMvav 196,132,108\nRa 188,0,31744\ns_barrier\nMvav 196,128,100\n"
"v_lshlrev_b32 v204, 16, v222\ns_add_u32 s5, s94, 917504\nMvav 196,144,36\nRa 128,2,16384\nv_and_b32 v205, 0xffff0000, v222\nMvav 196,148,44\nv_lshlrev_b32 v206, 16, v223\n"
"Sv 180,1,16,5,1024\nMvav 196,152,52\nRa 132,2,17408\nCV 176,176,177\nMvav 196,156,60\nWL 6\ns_add_u32 m0, s89, 0\ns_add_u32 s46, s96, 1409024\nMvav 192,156,56\nRa 136,2,18432\n"
"v_and_b32 v207, 0xffff0000, v223\nMvav 192,152,48\nD1 1,16,46,0\nCV 177,178,179\nMvav 192,148,40\nRa 140,2,19456\nCV 178,184,185\nMvav 192,144,32\nCV 179,186,187\nSv 176,1,16,5,0\n"
"Mvav 200,160,96\nRa 144,2,20480\nWV 12\nv_max_u32 v21, v227, v231\nv_cmp_eq_u32 vcc, -1, v21\ns_nop 0\ns_cbranch_vccnz .S359\n.S360:\nMvav 200,164,104\n"
"v_lshlrev_b32 v160, 16, v224\nMvav 200,168,112\nRa 148,2,21504\nMvav 200,172,120\nWL 6\nv_and_b32 v161, 0xffff0000, v224\nMvav 204,172,124\nRa 152,2,22528\nMvav 204,168,116\n"
"D1 1,16,46,1024\nMvav 204,164,108\nRa 156,2,23552\nv_lshlrev_b32 v162, 16, v225\nMvav 204,160,100\nv_and_b32 v163, 0xffff0000, v225\nMvav 204,176,36\nRa 160,2,24576\n"
"v_lshlrev_b32 v164, 16, v228\nMvav 204,180,44\nv_and_b32 v165, 0xffff0000, v228\ns_add_u32 s4, s92, 524288\nMvav 204,184,52\nRa 164,2,25600\nMvav 204,188,60\nWL 6\n"
"v_lshlrev_b32 v166, 16, v229\nbuffer_load_dwordx4 v[216:219], v1, s[16:19], s4 offen offset:0 sc0 sc1\nMvav 200,188,56\nRa 168,2,26624\nMvav 200,184,48\nD1 1,16,46,2048\n"
"Mvav 200,180,40\nRa 172,2,27648\nv_and_b32 v167, 0xffff0000, v229\nMvav 200,176,32\nv_lshlrev_b32 v168, 16, v226\n"
"buffer_load_dwordx4 v[220:223], v1, s[16:19], s4 offen offset:1024 sc0 sc1\nMvav 160,128,96\nRa 176,2,28672\nv_and_b32 v169, 0xffff0000, v226\nMvav 160,132,104\n"
"v_lshlrev_b32 v170, 16, v227\nCV 196,196,197\nMvav 160,136,112\nRa 180,2,29696\nCV 197,198,199\nMvav 160,140,120\nWL 6\nCV 198,204,205\nMvav 164,140,124\nRa 184,2,30720\n"
"v_and_b32 v24, 0x70, v1\nMvav 164,136,116\nD1 1,16,46,3072\nCV 199,206,207\nMvav 164,132,108\nRa 188,2,31744\nv_and_b32 v171, 0xffff0000, v227\nMvav 164,128,100\n"
"v_lshlrev_b32 v172, 16, v230\ns_add_u32 s5, s94, 1048576\nMvav 164,144,36\nRa 128,2,0\nv_lshlrev_b32 v24, 5, v24\nMvav 164,148,44\nv_and_b32 v173, 0xffff0000, v230\n"
"Sv 196,1,16,5,1024\nMvav 164,152,52\nRa 132,2,1024\nCV 192,192,193\nMvav 164,156,60\nWL 6\nv_lshlrev_b32 v174, 16, v231\nMvav 160,156,56\nRa 136,2,2048\n"
"buffer_load_dword v27, v24, s[16:19], s41 offen sc0 sc1\nMvav 160,152,48\nv_and_b32 v175, 0xffff0000, v231\nCV 193,194,195\nMvav 160,148,40\nRa 140,2,3072\nCV 194,200,201\n"
"Mvav 160,144,32\nCV 195,202,203\nSv 192,1,16,5,0\nMvav 168,160,96\nRa 144,2,4096\nWV 12\nv_max_u32 v21, v211, v215\nv_cmp_eq_u32 vcc, -1, v21\ns_nop 0\ns_cbranch_vccnz .S362\n"
".S363:\nMvav 168,164,104\nv_lshlrev_b32 v176, 16, v208\nMvav 168,168,112\nRa 148,2,5120\nMvav 168,172,120\nWL 6\nMvav 172,172,124\nRa 152,2,6144\nv_and_b32 v177, 0xffff0000, v208\n"
"Mvav 172,168,116\nv_lshlrev_b32 v178, 16, v209\nMvav 172,164,108\nRa 156,2,7168\nMvav 172,160,100\nv_and_b32 v179, 0xffff0000, v209\nMvav 172,176,36\nRa 160,2,8192\n"
"v_lshlrev_b32 v180, 16, v212\nMvav 172,180,44\nv_and_b32 v181, 0xffff0000, v212\ns_add_u32 s4, s92, 655360\nMvav 172,184,52\nRa 164,2,9216\nMvav 172,188,60\nWL 6\n"
"buffer_load_dwordx4 v[224:227], v1, s[16:19], s4 offen offset:0 sc0 sc1\nMvav 168,188,56\nRa 168,2,10240\nv_lshlrev_b32 v182, 16, v213\nMvav 168,184,48\n"
"v_and_b32 v183, 0xffff0000, v213\nMvav 168,180,40\nRa 172,2,11264\nMvav 168,176,32\nv_lshlrev_b32 v184, 16, v210\n"
"buffer_load_dwordx4 v[228:231], v1, s[16:19], s4 offen offset:1024 sc0 sc1\nMvav 176,128,96\nRa 176,2,12288\nv_and_b32 v185, 0xffff0000, v210\nMvav 176,132,104\n"
"v_lshlrev_b32 v186, 16, v211\nCV 164,164,165\nMvav 176,136,112\nRa 180,2,13312\nCV 165,166,167\nMvav 176,140,120\ns_waitcnt vmcnt(13) lgkmcnt(6)\nCV 166,172,173\nMvav 180,140,124\n"
"Ra 184,2,14336\nCV 167,174,175\nMvav 180,136,116\nv_and_b32 v187, 0xffff0000, v211\ns_add_u32 s5, s94, 1179648\nMvav 180,132,108\nRa 188,2,15360\ns_barrier\nMvav 180,128,100\n"
"v_lshlrev_b32 v188, 16, v214\nSv 164,1,16,5,1024\nMvav 180,144,36\nRa 128,2,32768\nv_and_b32 v189, 0xffff0000, v214\nMvav 180,148,44\nv_lshlrev_b32 v190, 16, v215\nCV 160,160,161\n"
"Mvav 180,152,52\nRa 132,2,33792\nCV 161,162,163\nMvav 180,156,60\nWL 6\nCV 162,168,169\nMvav 176,156,56\nRa 136,2,34816\nCV 163,170,171\nMvav 176,152,48\n"
"v_and_b32 v191, 0xffff0000, v215\nSv 160,1,16,5,0\nMvav 176,148,40\nRa 140,2,35840\nMvav 176,144,32\nMvav 184,160,96\nRa 144,2,36864\n.W574:\ns_cbranch_execz .WB273\nWV 8\n"
"v_max_u32 v21, v219, v223\nv_cmp_eq_u32 vcc, -1, v21\ns_nop 0\ns_cbranch_vccnz .S365\n.S366:\nMvav 184,164,104\nv_lshlrev_b32 v192, 16, v216\nMvav 184,168,112\nRa 148,2,37888\n"
"Mvav 184,172,120\nWL 6\nMvav 188,172,124\nRa 152,2,38912\nv_and_b32 v193, 0xffff0000, v216\nMvav 188,168,116\nv_lshlrev_b32 v194, 16, v217\nMvav 188,164,108\nRa 156,2,39936\n"
"Mvav 188,160,100\nv_and_b32 v195, 0xffff0000, v217\nMvav 188,176,36\nRa 160,2,40960\nv_lshlrev_b32 v196, 16, v220\nMvav 188,180,44\nv_and_b32 v197, 0xffff0000, v220\n"
"s_add_u32 s4, s92, 786432\nMvav 188,184,52\nRa 164,2,41984\nMvav 188,188,60\nWL 6\nbuffer_load_dwordx4 v[208:211], v1, s[16:19], s4 offen offset:0 sc0 sc1\nMvav 184,188,56\n"
"Ra 168,2,43008\nv_lshlrev_b32 v198, 16, v221\nMvav 184,184,48\nv_and_b32 v199, 0xffff0000, v221\nMvav 184,180,40\nRa 172,2,44032\nMvav 184,176,32\nv_lshlrev_b32 v200, 16, v218\n"
"buffer_load_dwordx4 v[212:215], v1, s[16:19], s4 offen offset:1024 sc0 sc1\nMvav 192,128,96\nRa 176,2,45056\nv_and_b32 v201, 0xffff0000, v218\nMvav 192,132,104\n"
"v_lshlrev_b32 v202, 16, v219\nCV 180,180,181\nMvav 192,136,112\nRa 180,2,46080\nCV 181,182,183\nMvav 192,140,120\nWL 6\nCV 182,188,189\nMvav 196,140,124\nRa 184,2,47104\n"
"CV 183,190,191\nMvav 196,136,116\nv_and_b32 v203, 0xffff0000, v219\ns_add_u32 s5, s94, 1310720\nMvav 196,132,108\nRa 188,2,48128\nSv 180,1,16,5,1024\nMvav 196,128,100\n"
"v_lshlrev_b32 v204, 16, v222\nCV 176,176,177\nMvav 196,144,36\nRa 128,2,49152\nv_and_b32 v205, 0xffff0000, v222\nMvav 196,148,44\nv_lshlrev_b32 v206, 16, v223\nCV 177,178,179\n"
"Mvav 196,152,52\nRa 132,2,50176\nCV 178,184,185\nMvav 196,156,60\nWL 6\nCV 179,186,187\nMvav 192,156,56\nRa 136,2,51200\nSv 176,1,16,5,0\nMvav 192,152,48\n"
"v_and_b32 v207, 0xffff0000, v223\nMvav 192,148,40\nRa 140,2,52224\nMvav 192,144,32\nMvav 200,160,96\nRa 144,2,53248\nWV 6\nv_max_u32 v21, v227, v231\nv_cmp_eq_u32 vcc, -1, v21\n"
"s_nop 0\ns_cbranch_vccnz .S368\n.S369:\nMvav 200,164,104\nv_lshlrev_b32 v160, 16, v224\nv_xor_b32 v27, s15, v27\nv_and_b32 v26, 0xffffdfff, v27\nv_cmp_ne_u32 s[64:65], 0, v27\n"
"v_cmp_ne_u32 vcc, 0, v26\ns_nop 0\ns_cbranch_vccnz .S380\n.S381:\nMvav 200,168,112\nRa 148,2,54272\ns_bitcmp1_b32 s64, 0\ns_cselect_b32 s2, 0x2000, 0\nMvav 200,172,120\nWL 6\n"
"Mvav 204,172,124\nRa 152,2,55296\nv_and_b32 v161, 0xffff0000, v224\nMvav 204,168,116\nv_lshlrev_b32 v162, 16, v225\nMvav 204,164,108\nRa 156,2,56320\ns_xor_b32 s2, s2, s20\n"
"Mvav 204,160,100\nv_and_b32 v163, 0xffff0000, v225\nMvav 204,176,36\nRa 160,2,57344\nv_lshlrev_b32 v164, 16, v228\nMvav 204,180,44\nv_and_b32 v165, 0xffff0000, v228\n"
"Mvav 204,184,52\nRa 164,2,58368\nMvav 204,188,60\nWL 6\ns_add_u32 s24, s2, 0x0\nMvav 200,188,56\nRa 168,2,59392\nv_lshlrev_b32 v166, 16, v229\nMvav 200,184,48\n"
"v_and_b32 v167, 0xffff0000, v229\ns_bitcmp1_b32 s64, 1\ns_cselect_b32 s2, 0x2000, 0\nMvav 200,180,40\nRa 172,2,60416\nMvav 200,176,32\nv_lshlrev_b32 v168, 16, v226\n"
"Mvav 160,128,96\nRa 176,2,61440\nv_and_b32 v169, 0xffff0000, v226\nMvav 160,132,104\nv_lshlrev_b32 v170, 16, v227\nCV 196,196,197\nMvav 160,136,112\nRa 180,2,62464\n"
"CV 197,198,199\nMvav 160,140,120\nWL 6\nCV 198,204,205\nMvav 164,140,124\nRa 184,2,63488\nCV 199,206,207\nMvav 164,136,116\nv_and_b32 v171, 0xffff0000, v227\n"
"s_add_u32 s5, s94, 1441792\nMvav 164,132,108\nRa 188,2,64512\ns_barrier\nMvav 164,128,100\nv_lshlrev_b32 v172, 16, v230\nSv 196,1,16,5,1024\nMvav 164,144,36\nRa 128,3,0\n"
"v_and_b32 v173, 0xffff0000, v230\nMvav 164,148,44\nv_lshlrev_b32 v174, 16, v231\nCV 192,192,193\nMvav 164,152,52\nRa 132,3,1024\nCV 193,194,195\nMvav 164,156,60\nWL 6\n"
"s_xor_b32 s2, s2, s20\nMvav 160,156,56\nRa 136,3,2048\nCV 194,200,201\nMvav 160,152,48\nv_and_b32 v175, 0xffff0000, v231\nCV 195,202,203\nMvav 160,148,40\nRa 140,3,3072\n"
"Sv 192,1,16,5,0\nMvav 160,144,32\ns_add_u32 s25, s2, 0x1800000\ns_bitcmp1_b32 s64, 2\ns_cselect_b32 s2, 0x2000, 0\nMvav 168,160,96\nRa 144,3,4096\nWV 4\nv_max_u32 v21, v211, v215\n"
"v_cmp_eq_u32 vcc, -1, v21\ns_nop 0\ns_cbranch_vccnz .S371\n.S372:\nMvav 168,164,104\nv_lshlrev_b32 v176, 16, v208\nMvav 168,168,112\nRa 148,3,5120\nMvav 168,172,120\nWL 6\n"
"Mvav 172,172,124\nRa 152,3,6144\nv_and_b32 v177, 0xffff0000, v208\nMvav 172,168,116\nv_lshlrev_b32 v178, 16, v209\nMvav 172,164,108\nRa 156,3,7168\ns_xor_b32 s2, s2, s20\n"
"Mvav 172,160,100\nv_and_b32 v179, 0xffff0000, v209\nMvav 172,176,36\nRa 160,3,8192\nv_lshlrev_b32 v180, 16, v212\nMvav 172,180,44\nv_and_b32 v181, 0xffff0000, v212\n"
"Mvav 172,184,52\nRa 164,3,9216\nMvav 172,188,60\nWL 6\ns_add_u32 s26, s2, 0x3000000\nMvav 168,188,56\nRa 168,3,10240\nv_lshlrev_b32 v182, 16, v213\nMvav 168,184,48\n"
"v_and_b32 v183, 0xffff0000, v213\ns_bitcmp1_b32 s64, 3\ns_cselect_b32 s2, 0x2000, 0\nMvav 168,180,40\nRa 172,3,11264\nMvav 168,176,32\nv_lshlrev_b32 v184, 16, v210\n"
"Mvav 176,128,96\nRa 176,3,12288\ns_xor_b32 s2, s2, s20\nMvav 176,132,104\nv_and_b32 v185, 0xffff0000, v210\nCV 164,164,165\nMvav 176,136,112\nRa 180,3,13312\nCV 165,166,167\n"
"Mvav 176,140,120\nWL 6\nCV 166,172,173\nMvav 180,140,124\nRa 184,3,14336\nCV 167,174,175\nMvav 180,136,116\nv_lshlrev_b32 v186, 16, v211\ns_add_u32 s5, s94, 1572864\n"
"Mvav 180,132,108\nRa 188,3,15360\ns_add_u32 s27, s2, 0x4800000\nMvav 180,128,100\nv_and_b32 v187, 0xffff0000, v211\ns_bitcmp1_b32 s64, 4\ns_cselect_b32 s2, 0x2000, 0\n"
"Mvav 180,144,36\nv_lshlrev_b32 v188, 16, v214\nSv 164,1,16,5,1024\nMvav 180,148,44\nv_and_b32 v189, 0xffff0000, v214\nCV 160,160,161\nMvav 180,152,52\n"
"v_lshlrev_b32 v190, 16, v215\nCV 161,162,163\nMvav 180,156,60\nWL 4\ns_xor_b32 s2, s2, s20\nMvav 176,156,56\nv_and_b32 v191, 0xffff0000, v215\nCV 162,168,169\nMvav 176,152,48\n"
"s_add_u32 s32, s2, 0x6000000\ns_bitcmp1_b32 s64, 5\ns_cselect_b32 s2, 0x2000, 0\nMvav 176,148,40\ns_xor_b32 s2, s2, s20\nCV 163,170,171\nMvav 176,144,32\n"
"s_add_u32 s33, s2, 0x7800000\ns_bitcmp1_b32 s64, 6\ns_cselect_b32 s2, 0x2000, 0\nMvav 184,160,96\ns_xor_b32 s2, s2, s20\ns_add_u32 s34, s2, 0x9000000\nMvav 184,164,104\n"
"s_bitcmp1_b32 s64, 7\ns_cselect_b32 s2, 0x2000, 0\nMvav 184,168,112\ns_xor_b32 s2, s2, s20\nSv 160,1,16,5,0\nMvav 184,172,120\nWL 0\nMvav 188,172,124\n"
"s_add_u32 s35, s2, 0xa800000\nMvav 188,168,116\nMvav 188,164,108\nMvav 188,160,100\nMvav 188,176,36\nMvav 188,180,44\nMvav 188,184,52\nMvav 188,188,60\nMvav 184,188,56\n"
"Mvav 184,184,48\nMvav 184,180,40\nMvav 184,176,32\nCV 180,180,181\nCV 181,182,183\ns_nop 1\nCV 182,188,189\nCV 183,190,191\ns_add_u32 s5, s94, 1703936\nSv 180,1,16,5,1024\n"
"CV 176,176,177\nCV 177,178,179\nCV 178,184,185\nCV 179,186,187\nSv 176,1,16,5,0\ns_cmp_eq_u32 s13, 7\ns_cbranch_scc1 .Z383\n"
"buffer_load_dwordx4 v[12:15], v91, s[28:31], s22 offen offset:0\nbuffer_load_dwordx4 v[16:19], v91, s[28:31], s23 offen offset:0\n"
"buffer_load_dwordx4 v[64:67], v89, s[16:19], s40 offen offset:0 sc0 sc1\nbuffer_load_dwordx4 v[68:71], v89, s[16:19], s40 offen offset:16 sc0 sc1\n"
"buffer_load_dwordx4 v[72:75], v89, s[16:19], s40 offen offset:1792 sc0 sc1\nbuffer_load_dwordx4 v[76:79], v89, s[16:19], s40 offen offset:1808 sc0 sc1\n"
"buffer_load_dwordx2 v[80:81], v88, s[16:19], s63 offen offset:0 sc0 sc1\nbuffer_load_dwordx2 v[82:83], v88, s[16:19], s63 offen offset:512 sc0 sc1\n"
"buffer_load_dwordx4 v[96:99], v1, s[16:19], s24 offen offset:0 sc0 sc1\nbuffer_load_dwordx4 v[100:103], v1, s[16:19], s24 offen offset:1024 sc0 sc1\n"
"buffer_load_dwordx4 v[104:107], v1, s[16:19], s25 offen offset:0 sc0 sc1\nbuffer_load_dwordx4 v[108:111], v1, s[16:19], s25 offen offset:1024 sc0 sc1\n"
"buffer_load_dwordx4 v[112:115], v1, s[16:19], s26 offen offset:0 sc0 sc1\nbuffer_load_dwordx4 v[116:119], v1, s[16:19], s26 offen offset:1024 sc0 sc1\n"
"buffer_load_dwordx4 v[120:123], v1, s[16:19], s27 offen offset:0 sc0 sc1\nbuffer_load_dwordx4 v[124:127], v1, s[16:19], s27 offen offset:1024 sc0 sc1\n"
"buffer_load_dwordx4 v[32:35], v1, s[16:19], s32 offen offset:0 sc0 sc1\nbuffer_load_dwordx4 v[36:39], v1, s[16:19], s32 offen offset:1024 sc0 sc1\n"
"buffer_load_dwordx4 v[40:43], v1, s[16:19], s33 offen offset:0 sc0 sc1\nbuffer_load_dwordx4 v[44:47], v1, s[16:19], s33 offen offset:1024 sc0 sc1\n"
"buffer_load_dwordx4 v[48:51], v1, s[16:19], s34 offen offset:0 sc0 sc1\nbuffer_load_dwordx4 v[52:55], v1, s[16:19], s34 offen offset:1024 sc0 sc1\n"
"buffer_load_dwordx4 v[56:59], v1, s[16:19], s35 offen offset:0 sc0 sc1\nbuffer_load_dwordx4 v[60:63], v1, s[16:19], s35 offen offset:1024 sc0 sc1\nWV 16\nv_xor_b32 v66, s15, v66\n"
"v_xor_b32 v70, s15, v70\nv_xor_b32 v74, s15, v74\nv_xor_b32 v78, s15, v78\nv_xor_b32 v81, s15, v81\nv_xor_b32 v83, s15, v83\nv_or3_b32 v92, v66, v70, v74\n"
"v_or3_b32 v92, v92, v78, v81\nv_or_b32 v92, v92, v83\nv_cmp_ne_u32 vcc, 0, v92\ns_nop 0\ns_cbranch_vccnz .S374\n.S375:\nv_and_b32 v92, 0x7ff, v67\nv_lshrrev_b32 v93, 16, v67\n"
"v_cmp_eq_u32 s[44:45], v92, v90\nv_cmp_eq_u32 s[46:47], v93, v90\nv_cmp_eq_u32 s[48:49], v92, v94\nv_cmp_eq_u32 s[50:51], v93, v94\nv_cndmask_b32 v12, v12, v64, s[44:45]\n"
"v_cndmask_b32 v13, v13, v65, s[44:45]\nv_cndmask_b32 v14, v14, v68, s[44:45]\nv_cndmask_b32 v15, v15, v69, s[44:45]\nv_cndmask_b32 v12, v12, v72, s[46:47]\n"
"v_cndmask_b32 v13, v13, v73, s[46:47]\nv_cndmask_b32 v14, v14, v76, s[46:47]\nv_cndmask_b32 v15, v15, v77, s[46:47]\nv_cndmask_b32 v16, v16, v64, s[48:49]\n"
"v_cndmask_b32 v17, v17, v65, s[48:49]\nv_cndmask_b32 v18, v18, v68, s[48:49]\nv_cndmask_b32 v19, v19, v69, s[48:49]\nv_cndmask_b32 v16, v16, v72, s[50:51]\n"
"v_cndmask_b32 v17, v17, v73, s[50:51]\nv_cndmask_b32 v18, v18, v76, s[50:51]\nv_cndmask_b32 v19, v19, v77, s[50:51]\nv_sub_f32 v92, 1.0, v80\nv_sub_f32 v93, 1.0, v82\nCV 92,92,93\n"
"v_and_b32 v93, 0xffff0000, v92\nv_lshlrev_b32 v92, 16, v92\nv_lshlrev_b32 v64, 16, v12\nv_and_b32 v65, 0xffff0000, v12\nMU 64,64,92\nMU 65,65,92\nv_lshlrev_b32 v66, 16, v13\n"
"v_and_b32 v67, 0xffff0000, v13\nMU 66,66,92\nMU 67,67,92\nv_lshlrev_b32 v68, 16, v14\nv_and_b32 v69, 0xffff0000, v14\nMU 68,68,92\nMU 69,69,92\nv_lshlrev_b32 v70, 16, v15\n"
"v_and_b32 v71, 0xffff0000, v15\nMU 70,70,92\nMU 71,71,92\nv_lshlrev_b32 v72, 16, v16\nv_and_b32 v73, 0xffff0000, v16\nMU 72,72,93\nMU 73,73,93\nv_lshlrev_b32 v74, 16, v17\n"
"v_and_b32 v75, 0xffff0000, v17\nMU 74,74,93\nMU 75,75,93\nv_lshlrev_b32 v76, 16, v18\nv_and_b32 v77, 0xffff0000, v18\nMU 76,76,93\nMU 77,77,93\nv_lshlrev_b32 v78, 16, v19\n"
"v_and_b32 v79, 0xffff0000, v19\nMU 78,78,93\nMU 79,79,93\ns_mov_b32 s7, 262144\nWV 0\n.Z386:\nv_max_u32 v84, v99, v103\nv_cmp_eq_u32 s[44:45], -1, v84\nv_max_u32 v84, v107, v111\n"
"v_cmp_eq_u32 s[46:47], -1, v84\nv_max_u32 v84, v115, v119\nv_cmp_eq_u32 s[48:49], -1, v84\nv_max_u32 v84, v123, v127\nv_cmp_eq_u32 s[50:51], -1, v84\nv_max_u32 v84, v35, v39\n"
"v_cmp_eq_u32 s[52:53], -1, v84\nv_max_u32 v84, v43, v47\nv_cmp_eq_u32 s[54:55], -1, v84\nv_max_u32 v84, v51, v55\nv_cmp_eq_u32 s[56:57], -1, v84\nv_max_u32 v84, v59, v63\n"
"v_cmp_eq_u32 s[58:59], -1, v84\ns_or_b64 s[0:1], s[44:45], s[46:47]\ns_or_b64 s[2:3], s[48:49], s[50:51]\ns_or_b64 s[0:1], s[0:1], s[52:53]\ns_or_b64 s[2:3], s[2:3], s[54:55]\n"
"s_or_b64 s[0:1], s[0:1], s[56:57]\ns_or_b64 s[2:3], s[2:3], s[58:59]\ns_or_b64 s[0:1], s[0:1], s[2:3]\ns_cbranch_scc1 .Z384\n.Z385:\nZvvv 136,6,96\nMvvv 136,6,104\nMvvv 136,6,112\n"
"Mvvv 136,6,120\nMvvv 136,6,32\nMvvv 136,6,40\nMvvv 136,6,48\nMvvv 136,6,56\nZvvv 140,4,96\nMvvv 140,4,104\nMvvv 140,4,112\nMvvv 140,4,120\nMvvv 140,4,32\nMvvv 140,4,40\n"
"Mvvv 140,4,48\nMvvv 140,4,56\nZvvv 144,6,100\nMvvv 144,6,108\nMvvv 144,6,116\nMvvv 144,6,124\nMvvv 144,6,36\nMvvv 144,6,44\nMvvv 144,6,52\nMvvv 144,6,60\nZvvv 148,4,100\n"
"Mvvv 148,4,108\nMvvv 148,4,116\nMvvv 148,4,124\nMvvv 148,4,36\nMvvv 148,4,44\nMvvv 148,4,52\nMvvv 148,4,60\nFM 64,80,136\nFM 65,80,137\nFM 66,80,138\nFM 67,80,139\nFM 68,80,140\n"
"FM 69,80,141\nFM 70,80,142\nFM 71,80,143\nCV 12,64,65\nCV 13,66,67\nCV 14,68,69\nCV 15,70,71\nSv 12,91,36,22,0\nFM 72,82,144\nFM 73,82,145\nFM 74,82,146\nFM 75,82,147\n"
"FM 76,82,148\nFM 77,82,149\nFM 78,82,150\nFM 79,82,151\nCV 16,72,73\nCV 17,74,75\nCV 18,76,77\nCV 19,78,79\nSv 16,91,36,23,0\n.Z383:\ns_endpgm\n.EP395:\n.S269:\n"
"s_mov_b32 s7, 262144\n.S271:\ns_lshl_b32 s5, s73, 9\ns_add_u32 s5, s5, 0xc049400\nv_lshrrev_b32 v27, 1, v1\nbuffer_load_dwordx2 v[22:23], v27, s[16:19], s5 offen sc0 sc1\nWV 0\n"
"v_cmp_ne_u32 vcc, s15, v23\ns_nop 0\ns_cbranch_vccz .S270\ns_sleep 6\ns_sub_u32 s7, s7, 1\ns_cbranch_scc0 .S271\ns_branch .S270\n.T278:\ns_mov_b32 s62, 1\n"
"s_call_b64 s[80:81], .RA265\ns_branch .T279\n.T280:\ns_mov_b32 s62, 1\ns_call_b64 s[80:81], .RA265\ns_branch .T281\n.S283:\ns_mov_b32 s7, 262144\n.S285:\n"
"buffer_load_dword v27, off, s[16:19], s5 sc0 sc1\nWV 0\nv_cmp_ne_u32 vcc, s15, v27\ns_nop 0\ns_cbranch_vccz .S284\ns_sleep 6\ns_sub_u32 s7, s7, 1\ns_cbranch_scc0 .S285\n"
"s_branch .S284\n.S286:\ns_mov_b32 s7, 262144\n.S288:\nv_and_b32 v27, 0x70, v1\nbuffer_load_dword v27, v27, s[16:19], s61 offen offset:128 sc0 sc1\nWV 0\n"
"v_cmp_ne_u32 vcc, s15, v27\ns_nop 0\ns_cbranch_vccz .S287\ns_sleep 6\ns_sub_u32 s7, s7, 1\ns_cbranch_scc0 .S288\ns_branch .S287\n.S289:\ns_mov_b32 s7, 262144\n.S291:\n"
"v_and_b32 v27, 0x70, v1\nbuffer_load_dword v27, v27, s[16:19], s61 offen offset:136 sc0 sc1\nWV 0\nv_cmp_ne_u32 vcc, s15, v27\ns_nop 0\ns_cbranch_vccz .S290\ns_sleep 6\n"
"s_sub_u32 s7, s7, 1\ns_cbranch_scc0 .S291\ns_branch .S290\n.S292:\ns_mov_b32 s7, 262144\n.S294:\nv_and_b32 v27, 0x70, v1\n"
"buffer_load_dword v27, v27, s[16:19], s61 offen offset:140 sc0 sc1\nWV 0\nv_cmp_ne_u32 vcc, s15, v27\ns_nop 0\ns_cbranch_vccz .S293\ns_sleep 6\ns_sub_u32 s7, s7, 1\n"
"s_cbranch_scc0 .S294\ns_branch .S293\n.S295:\ns_mov_b32 s7, 262144\n.S297:\nv_and_b32 v27, 0x70, v1\nbuffer_load_dword v27, v27, s[16:19], s61 offen offset:12 sc0 sc1\nWV 0\n"
"v_cmp_ne_u32 vcc, s15, v27\ns_nop 0\ns_cbranch_vccz .S296\ns_sleep 6\ns_sub_u32 s7, s7, 1\ns_cbranch_scc0 .S297\ns_branch .S296\n.S298:\ns_mov_b32 s7, 262144\n.S300:\n"
"v_and_b32 v27, 0x70, v1\nbuffer_load_dword v27, v27, s[16:19], s61 offen offset:8 sc0 sc1\nWV 0\nv_cmp_ne_u32 vcc, s15, v27\ns_nop 0\ns_cbranch_vccz .S299\ns_sleep 6\n"
"s_sub_u32 s7, s7, 1\ns_cbranch_scc0 .S300\ns_branch .S299\n.S301:\ns_mov_b32 s7, 262144\n.S303:\nv_and_b32 v27, 0xf0, v1\n"
"buffer_load_dword v27, v27, s[16:19], s61 offen offset:4 sc0 sc1\nWV 0\nv_cmp_ne_u32 vcc, s15, v27\ns_nop 0\ns_cbranch_vccz .S302\ns_sleep 6\ns_sub_u32 s7, s7, 1\n"
"s_cbranch_scc0 .S303\ns_branch .S302\n.T312:\ns_mov_b32 s62, 0\ns_call_b64 s[80:81], .RB266\ns_branch .T313\n.T314:\ns_mov_b32 s62, 0\ns_call_b64 s[80:81], .RA265\ns_branch .T315\n"
".T318:\ns_mov_b32 s62, 0\ns_call_b64 s[80:81], .RB266\ns_branch .T319\n.T320:\ns_mov_b32 s62, 0\ns_call_b64 s[80:81], .RA265\ns_branch .T321\n.T327:\ns_mov_b32 s62, 2\n"
"s_call_b64 s[80:81], .RB266\ns_branch .T328\n.T329:\ns_mov_b32 s62, 1\ns_call_b64 s[80:81], .RA265\ns_branch .T330\n.T331:\ns_mov_b32 s62, 1\ns_call_b64 s[80:81], .RA265\n"
"s_branch .T332\n.T341:\ns_mov_b32 s62, 0\ns_call_b64 s[80:81], .RB266\ns_branch .T342\n.T343:\ns_mov_b32 s62, 0\ns_call_b64 s[80:81], .RA265\ns_branch .T344\n.T347:\n"
"s_mov_b32 s62, 0\ns_call_b64 s[80:81], .RB266\ns_branch .T348\n.T349:\ns_mov_b32 s62, 0\ns_call_b64 s[80:81], .RA265\ns_branch .T350\n.T351:\ns_mov_b32 s62, 2\n"
"s_call_b64 s[80:81], .RB266\ns_branch .T352\n.S353:\ns_mov_b32 s7, 262144\n.S355:\ns_add_u32 s0, s92, 0\nbuffer_load_dwordx4 v[208:211], v1, s[16:19], s0 offen offset:0 sc0 sc1\n"
"buffer_load_dwordx4 v[212:215], v1, s[16:19], s0 offen offset:1024 sc0 sc1\nWV 0\nv_max_u32 v21, v211, v215\nv_cmp_eq_u32 vcc, -1, v21\ns_nop 0\ns_cbranch_vccz .S354\ns_sleep 6\n"
"s_sub_u32 s7, s7, 1\ns_cbranch_scc0 .S355\ns_branch .S354\n.S356:\ns_mov_b32 s7, 262144\n.S358:\ns_add_u32 s0, s92, 131072\n"
"buffer_load_dwordx4 v[216:219], v1, s[16:19], s0 offen offset:0 sc0 sc1\nbuffer_load_dwordx4 v[220:223], v1, s[16:19], s0 offen offset:1024 sc0 sc1\nWV 0\n"
"v_max_u32 v21, v219, v223\nv_cmp_eq_u32 vcc, -1, v21\ns_nop 0\ns_cbranch_vccz .S357\ns_sleep 6\ns_sub_u32 s7, s7, 1\ns_cbranch_scc0 .S358\ns_branch .S357\n.S359:\n"
"s_mov_b32 s7, 262144\n.S361:\ns_add_u32 s0, s92, 262144\nbuffer_load_dwordx4 v[224:227], v1, s[16:19], s0 offen offset:0 sc0 sc1\n"
"buffer_load_dwordx4 v[228:231], v1, s[16:19], s0 offen offset:1024 sc0 sc1\nWV 0\nv_max_u32 v21, v227, v231\nv_cmp_eq_u32 vcc, -1, v21\ns_nop 0\ns_cbranch_vccz .S360\ns_sleep 6\n"
"s_sub_u32 s7, s7, 1\ns_cbranch_scc0 .S361\ns_branch .S360\n.S362:\ns_mov_b32 s7, 262144\n.S364:\ns_add_u32 s0, s92, 393216\n"
"buffer_load_dwordx4 v[208:211], v1, s[16:19], s0 offen offset:0 sc0 sc1\nbuffer_load_dwordx4 v[212:215], v1, s[16:19], s0 offen offset:1024 sc0 sc1\nWV 0\n"
"v_max_u32 v21, v211, v215\nv_cmp_eq_u32 vcc, -1, v21\ns_nop 0\ns_cbranch_vccz .S363\ns_sleep 6\ns_sub_u32 s7, s7, 1\ns_cbranch_scc0 .S364\ns_branch .S363\n.S365:\n"
"s_mov_b32 s7, 262144\n.S367:\ns_add_u32 s0, s92, 524288\nbuffer_load_dwordx4 v[216:219], v1, s[16:19], s0 offen offset:0 sc0 sc1\n"
"buffer_load_dwordx4 v[220:223], v1, s[16:19], s0 offen offset:1024 sc0 sc1\nWV 0\nv_max_u32 v21, v219, v223\nv_cmp_eq_u32 vcc, -1, v21\ns_nop 0\ns_cbranch_vccz .S366\ns_sleep 6\n"
"s_sub_u32 s7, s7, 1\ns_cbranch_scc0 .S367\ns_branch .S366\n.S368:\ns_mov_b32 s7, 262144\n.S370:\ns_add_u32 s0, s92, 655360\n"
"buffer_load_dwordx4 v[224:227], v1, s[16:19], s0 offen offset:0 sc0 sc1\nbuffer_load_dwordx4 v[228:231], v1, s[16:19], s0 offen offset:1024 sc0 sc1\nWV 0\n"
"v_max_u32 v21, v227, v231\nv_cmp_eq_u32 vcc, -1, v21\ns_nop 0\ns_cbranch_vccz .S369\ns_sleep 6\ns_sub_u32 s7, s7, 1\ns_cbranch_scc0 .S370\ns_branch .S369\n.S371:\n"
"s_mov_b32 s7, 262144\n.S373:\ns_add_u32 s0, s92, 786432\nbuffer_load_dwordx4 v[208:211], v1, s[16:19], s0 offen offset:0 sc0 sc1\n"
"buffer_load_dwordx4 v[212:215], v1, s[16:19], s0 offen offset:1024 sc0 sc1\nWV 0\nv_max_u32 v21, v211, v215\nv_cmp_eq_u32 vcc, -1, v21\ns_nop 0\ns_cbranch_vccz .S372\ns_sleep 6\n"
"s_sub_u32 s7, s7, 1\ns_cbranch_scc0 .S373\ns_branch .S372\n.S374:\ns_mov_b32 s7, 262144\n.S376:\nbuffer_load_dwordx4 v[64:67], v89, s[16:19], s40 offen offset:0 sc0 sc1\n"
"buffer_load_dwordx4 v[68:71], v89, s[16:19], s40 offen offset:16 sc0 sc1\nbuffer_load_dwordx4 v[72:75], v89, s[16:19], s40 offen offset:1792 sc0 sc1\n"
"buffer_load_dwordx4 v[76:79], v89, s[16:19], s40 offen offset:1808 sc0 sc1\nbuffer_load_dwordx2 v[80:81], v88, s[16:19], s63 offen offset:0 sc0 sc1\n"
"buffer_load_dwordx2 v[82:83], v88, s[16:19], s63 offen offset:512 sc0 sc1\nWV 0\nv_xor_b32 v66, s15, v66\nv_xor_b32 v70, s15, v70\nv_xor_b32 v74, s15, v74\n"
"v_xor_b32 v78, s15, v78\nv_xor_b32 v81, s15, v81\nv_xor_b32 v83, s15, v83\nv_or3_b32 v92, v66, v70, v74\nv_or3_b32 v92, v92, v78, v81\nv_or_b32 v92, v92, v83\n"
"v_cmp_ne_u32 vcc, 0, v92\ns_nop 0\ns_cbranch_vccz .S375\ns_sleep 6\ns_sub_u32 s7, s7, 1\ns_cbranch_scc0 .S376\ns_branch .S375\n.S377:\ns_mov_b32 s7, 262144\n.S379:\n"
"buffer_load_dword v27, off, s[16:19], s60 offset:512 sc0 sc1\nWV 0\nv_cmp_ne_u32 vcc, s15, v27\ns_nop 0\ns_cbranch_vccz .S378\ns_sleep 6\ns_sub_u32 s7, s7, 1\n"
"s_cbranch_scc0 .S379\ns_branch .S378\n.S380:\ns_mov_b32 s7, 262144\n.S382:\nbuffer_load_dword v27, v24, s[16:19], s41 offen sc0 sc1\nWV 0\nv_xor_b32 v27, s15, v27\n"
"v_and_b32 v26, 0xffffdfff, v27\nv_cmp_ne_u32 s[64:65], 0, v27\nv_cmp_ne_u32 vcc, 0, v26\ns_nop 0\ns_cbranch_vccz .S381\ns_sleep 6\ns_sub_u32 s7, s7, 1\ns_cbranch_scc0 .S382\n"
"s_branch .S381\n.Z384:\ns_sub_u32 s7, s7, 1\ns_cbranch_scc1 .Z385\ns_cmp_lg_u64 s[44:45], 0\ns_cbranch_scc0 .Z387\n"
"buffer_load_dwordx4 v[96:99], v1, s[16:19], s24 offen offset:0 sc0 sc1\nbuffer_load_dwordx4 v[100:103], v1, s[16:19], s24 offen offset:1024 sc0 sc1\n.Z387:\n"
"s_cmp_lg_u64 s[46:47], 0\ns_cbranch_scc0 .Z388\nbuffer_load_dwordx4 v[104:107], v1, s[16:19], s25 offen offset:0 sc0 sc1\n"
"buffer_load_dwordx4 v[108:111], v1, s[16:19], s25 offen offset:1024 sc0 sc1\n.Z388:\ns_cmp_lg_u64 s[48:49], 0\ns_cbranch_scc0 .Z389\n"
"buffer_load_dwordx4 v[112:115], v1, s[16:19], s26 offen offset:0 sc0 sc1\nbuffer_load_dwordx4 v[116:119], v1, s[16:19], s26 offen offset:1024 sc0 sc1\n.Z389:\n"
"s_cmp_lg_u64 s[50:51], 0\ns_cbranch_scc0 .Z390\nbuffer_load_dwordx4 v[120:123], v1, s[16:19], s27 offen offset:0 sc0 sc1\n"
"buffer_load_dwordx4 v[124:127], v1, s[16:19], s27 offen offset:1024 sc0 sc1\n.Z390:\ns_cmp_lg_u64 s[52:53], 0\ns_cbranch_scc0 .Z391\n"
"buffer_load_dwordx4 v[32:35], v1, s[16:19], s32 offen offset:0 sc0 sc1\nbuffer_load_dwordx4 v[36:39], v1, s[16:19], s32 offen offset:1024 sc0 sc1\n.Z391:\n"
"s_cmp_lg_u64 s[54:55], 0\ns_cbranch_scc0 .Z392\nbuffer_load_dwordx4 v[40:43], v1, s[16:19], s33 offen offset:0 sc0 sc1\n"
"buffer_load_dwordx4 v[44:47], v1, s[16:19], s33 offen offset:1024 sc0 sc1\n.Z392:\ns_cmp_lg_u64 s[56:57], 0\ns_cbranch_scc0 .Z393\n"
"buffer_load_dwordx4 v[48:51], v1, s[16:19], s34 offen offset:0 sc0 sc1\nbuffer_load_dwordx4 v[52:55], v1, s[16:19], s34 offen offset:1024 sc0 sc1\n.Z393:\n"
"s_cmp_lg_u64 s[58:59], 0\ns_cbranch_scc0 .Z394\nbuffer_load_dwordx4 v[56:59], v1, s[16:19], s35 offen offset:0 sc0 sc1\n"
"buffer_load_dwordx4 v[60:63], v1, s[16:19], s35 offen offset:1024 sc0 sc1\n.Z394:\ns_sleep 8\nWV 0\ns_branch .Z386\n.RA265:\ns_nop 7\ns_nop 3\nv_max3_f32 v22, v32, v33, v34\n"
"v_max3_f32 v22, v22, v35, v40\nv_max3_f32 v22, v22, v41, v42\nv_max_f32 v22, v22, v43\nv_mov_b32 v91, v22\ns_nop 1\nv_permlane32_swap_b32 v22, v91\nv_max_f32 v22, v22, v91\n"
"v_mov_b32 v91, v22\ns_nop 1\nv_permlane16_swap_b32 v22, v91\nv_max_f32 v22, v22, v91\nv_ceil_f32 v22, v22\nv_max3_f32 v23, v36, v37, v38\nv_max3_f32 v23, v23, v39, v44\n"
"v_max3_f32 v23, v23, v45, v46\nv_max_f32 v23, v23, v47\nv_mov_b32 v91, v23\ns_nop 1\nv_permlane32_swap_b32 v23, v91\nv_max_f32 v23, v23, v91\nv_mov_b32 v91, v23\ns_nop 1\n"
"v_permlane16_swap_b32 v23, v91\nv_max_f32 v23, v23, v91\nv_ceil_f32 v23, v23\ns_bitcmp1_b32 s62, 0\ns_cbranch_scc0 .R396\nv_cmp_lt_f32 vcc, 0xff000000, v22\ns_nop 1\n"
"v_cndmask_b32 v22, 0, v22, vcc\nv_cmp_lt_f32 vcc, 0xff000000, v23\ns_nop 1\nv_cndmask_b32 v23, 0, v23, vcc\ns_branch .R397\n.R396:\nv_max_f32 v22, 0, v22\nv_max_f32 v23, 0, v23\n"
"s_bitcmp1_b32 s62, 3\ns_cbranch_scc0 .R397\nv_max_f32 v22, v20, v22\nv_max_f32 v23, v21, v23\n.R397:\nAD 88,88,22\nAD 89,89,23\nSU 32,32,22\nSU 33,33,22\nSU 34,34,22\nSU 35,35,22\n"
"SU 36,36,23\nSU 37,37,23\nSU 38,38,23\nSU 39,39,23\nSU 40,40,22\nSU 41,41,22\nSU 42,42,22\nSU 43,43,22\nSU 44,44,23\nSU 45,45,23\nSU 46,46,23\nSU 47,47,23\nv_mov_b32 v90, v80\n"
"v_mov_b32 v91, v81\nEX 80,32\nEX 81,33\ns_nop 0\nAD 84,80,81\nCV 64,80,81\nEX 82,34\nEX 83,35\nAD 84,84,82\nAD 84,84,83\nCV 65,82,83\nEX 80,36\nEX 81,37\ns_nop 0\nAD 85,80,81\n"
"CV 68,80,81\nEX 82,38\nEX 83,39\nAD 85,85,82\nAD 85,85,83\nCV 69,82,83\nEX 80,40\nEX 81,41\nAD 84,84,80\nAD 84,84,81\nCV 66,80,81\nEX 82,42\nEX 83,43\nAD 84,84,82\nAD 84,84,83\n"
"CV 67,82,83\nEX 80,44\nEX 81,45\nAD 85,85,80\nAD 85,85,81\nCV 70,80,81\nEX 82,46\nEX 83,47\nAD 85,85,82\nAD 85,85,83\nCV 71,82,83\nv_mov_b32 v80, v90\nv_mov_b32 v81, v91\n"
"s_bitcmp1_b32 s62, 1\ns_cbranch_scc1 .R398\nSU 48,48,22\nSU 49,49,22\nSU 50,50,22\nSU 51,51,22\nSU 52,52,23\nSU 53,53,23\nSU 54,54,23\nSU 55,55,23\nSU 56,56,22\nSU 57,57,22\n"
"SU 58,58,22\nSU 59,59,22\nSU 60,60,23\nSU 61,61,23\nSU 62,62,23\nSU 63,63,23\nSU 4,4,22\nSU 5,5,22\nSU 6,6,22\nSU 7,7,22\nSU 8,8,23\nSU 9,9,23\nSU 10,10,23\nSU 11,11,23\n"
"SU 12,12,22\nSU 13,13,22\nSU 14,14,22\nSU 15,15,22\nSU 16,16,23\nSU 17,17,23\nSU 18,18,23\nSU 19,19,23\nEX 80,48\nEX 81,49\n.R398:\ns_bitcmp1_b32 s62, 0\ns_cbranch_scc0 .RS267\n"
"s_nop 1\ns_setpc_b64 s[80:81]\n.RB266:\ns_nop 7\ns_nop 3\nv_max3_f32 v22, v48, v49, v50\nv_max3_f32 v22, v22, v51, v56\nv_max3_f32 v22, v22, v57, v58\nv_max_f32 v22, v22, v59\n"
"v_mov_b32 v91, v22\ns_nop 1\nv_permlane32_swap_b32 v22, v91\nv_max_f32 v22, v22, v91\nv_mov_b32 v91, v22\ns_nop 1\nv_permlane16_swap_b32 v22, v91\nv_max_f32 v22, v22, v91\n"
"v_ceil_f32 v22, v22\nv_max3_f32 v23, v52, v53, v54\nv_max3_f32 v23, v23, v55, v60\nv_max3_f32 v23, v23, v61, v62\nv_max_f32 v23, v23, v63\nv_mov_b32 v91, v23\ns_nop 1\n"
"v_permlane32_swap_b32 v23, v91\nv_max_f32 v23, v23, v91\nv_mov_b32 v91, v23\ns_nop 1\nv_permlane16_swap_b32 v23, v91\nv_max_f32 v23, v23, v91\nv_ceil_f32 v23, v23\n"
"s_bitcmp1_b32 s62, 0\ns_cbranch_scc0 .R401\nv_cmp_lt_f32 vcc, 0xff000000, v22\nv_cndmask_b32 v22, 0, v22, vcc\nv_cmp_lt_f32 vcc, 0xff000000, v23\nv_cndmask_b32 v23, 0, v23, vcc\n"
"s_branch .R402\n.R401:\nv_max_f32 v22, 0, v22\nv_max_f32 v23, 0, v23\ns_bitcmp1_b32 s62, 3\ns_cbranch_scc0 .R402\nv_max_f32 v22, v20, v22\nv_max_f32 v23, v21, v23\n.R402:\n"
"AD 88,88,22\nAD 89,89,23\nSU 48,48,22\nSU 49,49,22\nSU 50,50,22\nSU 51,51,22\nSU 52,52,23\nSU 53,53,23\nSU 54,54,23\nSU 55,55,23\nSU 56,56,22\nSU 57,57,22\nSU 58,58,22\n"
"SU 59,59,22\nSU 60,60,23\nSU 61,61,23\nSU 62,62,23\nSU 63,63,23\nv_mov_b32 v90, v80\nv_mov_b32 v91, v81\nEX 80,48\nEX 81,49\ns_nop 0\nAD 84,80,81\nCV 72,80,81\nEX 82,50\nEX 83,51\n"
"AD 84,84,82\nAD 84,84,83\nCV 73,82,83\nEX 80,52\nEX 81,53\ns_nop 0\nAD 85,80,81\nCV 76,80,81\nEX 82,54\nEX 83,55\nAD 85,85,82\nAD 85,85,83\nCV 77,82,83\nEX 80,56\nEX 81,57\n"
"AD 84,84,80\nAD 84,84,81\nCV 74,80,81\nEX 82,58\nEX 83,59\nAD 84,84,82\nAD 84,84,83\nCV 75,82,83\nEX 80,60\nEX 81,61\nAD 85,85,80\nAD 85,85,81\nCV 78,80,81\nEX 82,62\nEX 83,63\n"
"AD 85,85,82\nAD 85,85,83\nCV 79,82,83\nv_mov_b32 v80, v90\nv_mov_b32 v81, v91\ns_bitcmp1_b32 s62, 1\ns_cbranch_scc1 .R403\nSU 32,32,22\nSU 33,33,22\nSU 34,34,22\nSU 35,35,22\n"
"SU 36,36,23\nSU 37,37,23\nSU 38,38,23\nSU 39,39,23\nSU 40,40,22\nSU 41,41,22\nSU 42,42,22\nSU 43,43,22\nSU 44,44,23\nSU 45,45,23\nSU 46,46,23\nSU 47,47,23\nSU 4,4,22\nSU 5,5,22\n"
"SU 6,6,22\nSU 7,7,22\nSU 8,8,23\nSU 9,9,23\nSU 10,10,23\nSU 11,11,23\nSU 12,12,22\nSU 13,13,22\nSU 14,14,22\nSU 15,15,22\nSU 16,16,23\nSU 17,17,23\nSU 18,18,23\nSU 19,19,23\n"
"EX 80,32\nEX 81,33\n.R403:\ns_bitcmp1_b32 s62, 0\ns_cbranch_scc0 .RS267\ns_nop 1\ns_setpc_b64 s[80:81]\n.RS267:\nv_exp_f32 v22, -v22\nv_exp_f32 v23, -v23\nMU 86,86,22\n"
"MU 87,87,23\nMU 128,128,22\nMU 129,129,22\nMU 130,130,22\nMU 131,131,22\nMU 132,132,23\nMU 133,133,23\nMU 134,134,23\nMU 135,135,23\nMU 136,136,22\nMU 137,137,22\nMU 138,138,22\n"
"MU 139,139,22\nMU 140,140,23\nMU 141,141,23\nMU 142,142,23\nMU 143,143,23\nMU 144,144,22\nMU 145,145,22\nMU 146,146,22\nMU 147,147,22\nMU 148,148,23\nMU 149,149,23\nMU 150,150,23\n"
"MU 151,151,23\nMU 152,152,22\nMU 153,153,22\nMU 154,154,22\nMU 155,155,22\nMU 156,156,23\nMU 157,157,23\nMU 158,158,23\nMU 159,159,23\nMU 160,160,22\nMU 161,161,22\nMU 162,162,22\n"
"MU 163,163,22\nMU 164,164,23\nMU 165,165,23\nMU 166,166,23\nMU 167,167,23\nMU 168,168,22\nMU 169,169,22\nMU 170,170,22\nMU 171,171,22\nMU 172,172,23\nMU 173,173,23\nMU 174,174,23\n"
"MU 175,175,23\nMU 176,176,22\nMU 177,177,22\nMU 178,178,22\nMU 179,179,22\nMU 180,180,23\nMU 181,181,23\nMU 182,182,23\nMU 183,183,23\nMU 184,184,22\nMU 185,185,22\nMU 186,186,22\n"
"MU 187,187,22\nMU 188,188,23\nMU 189,189,23\nMU 190,190,23\nMU 191,191,23\nMU 192,192,22\nMU 193,193,22\nMU 194,194,22\nMU 195,195,22\nMU 196,196,23\nMU 197,197,23\nMU 198,198,23\n"
"MU 199,199,23\nMU 200,200,22\nMU 201,201,22\nMU 202,202,22\nMU 203,203,22\nMU 204,204,23\nMU 205,205,23\nMU 206,206,23\nMU 207,207,23\nMU 208,208,22\nMU 209,209,22\nMU 210,210,22\n"
"MU 211,211,22\nMU 212,212,23\nMU 213,213,23\nMU 214,214,23\nMU 215,215,23\nMU 216,216,22\nMU 217,217,22\nMU 218,218,22\nMU 219,219,22\nMU 220,220,23\nMU 221,221,23\nMU 222,222,23\n"
"MU 223,223,23\nMU 224,224,22\nMU 225,225,22\nMU 226,226,22\nMU 227,227,22\nMU 228,228,23\nMU 229,229,23\nMU 230,230,23\nMU 231,231,23\nMU 232,232,22\nMU 233,233,22\nMU 234,234,22\n"
"MU 235,235,22\nMU 236,236,23\nMU 237,237,23\nMU 238,238,23\nMU 239,239,23\nMU 240,240,22\nMU 241,241,22\nMU 242,242,22\nMU 243,243,22\nMU 244,244,23\nMU 245,245,23\nMU 246,246,23\n"
"MU 247,247,23\nMU 248,248,22\nMU 249,249,22\nMU 250,250,22\nMU 251,251,22\nMU 252,252,23\nMU 253,253,23\nMU 254,254,23\nMU 255,255,23\ns_nop 1\ns_setpc_b64 s[80:81]\n.p2align 12\n"
".PA5:\ns_mov_b64 exec, 0\ns_cmp_eq_u32 s11, 1\ns_cbranch_scc1 .W576\ns_cmp_eq_u32 s11, 2\ns_cbranch_scc1 .W577\ns_cmp_eq_u32 s11, 3\ns_cbranch_scc1 .W578\ns_branch .W575\n.WB411:\n"
"s_mov_b64 exec, -1\ns_add_u32 m0, s47, -49152\ns_add_u32 s48, s48, 128\nD0 2,20,48,0\nD0 3,20,48,1024\nD0 4,20,48,2048\nD0 5,20,48,3072\ns_and_b32 s86, s74, 15\n"
"s_lshl2_add_u32 s86, s86, s11\ns_mov_b32 s88, 0\ns_lshl_b32 s93, s73, 9\ns_lshl3_add_u32 s93, s86, s93\ns_add_u32 s93, s93, 0xc049400\ns_lshl_b32 s86, s86, 14\n"
"s_mul_i32 s91, s73, 25165824\ns_add_u32 s86, s86, s91\ns_and_b32 s92, s13, 1\ns_lshl_b32 s92, s92, 13\ns_add_u32 s86, s86, s92\ns_add_u32 s94, s86, 0x1480000\n"
"s_xor_b32 s95, s94, 0x2000\ns_add_u32 s86, s94, 512\ns_load_dword s87, s[54:55], s86\ns_add_u32 m0, s47, -32768\ns_add_u32 s45, s45, 128\nD0 2,20,45,0\nD0 3,20,45,1024\n"
"D0 4,20,45,2048\nD0 5,20,45,3072\ns_add_u32 m0, s47, 32768\ns_add_u32 s44, s44, 0x40000\nD0 6,24,44,0\nD0 7,24,44,1024\nD0 8,24,44,2048\nD0 9,24,44,3072\ns_add_u32 m0, s47, 49152\n"
"s_add_u32 s44, s44, 0x40000\nD0 6,24,44,0\nD0 7,24,44,1024\nD0 8,24,44,2048\nD0 9,24,44,3072\ns_add_u32 m0, s47, -16384\ns_add_u32 s48, s48, 128\nD0 2,20,48,0\nD0 3,20,48,1024\n"
"D0 4,20,48,2048\nD0 5,20,48,3072\nv_mov_b32 v52, 0x40000000\nv_cmp_gt_u32 vcc, 56, v0\ns_lshl2_add_u32 s2, s10, s11\ns_mul_i32 s2, s2, 1792\nv_cndmask_b32 v53, v52, v1, vcc\n"
"s_mov_b32 s36, s56\ns_and_b32 s37, s57, 0xffff\ns_mov_b32 s38, 0x380\ns_mov_b32 s39, 0x20000\ns_mov_b32 s40, s58\ns_and_b32 s41, s59, 0xffff\ns_mov_b32 s42, 0x380\n"
"s_mov_b32 s43, 0x20000\nbuffer_load_dwordx4 v[32:35], v53, s[28:31], s2 offen offset:0\nbuffer_load_dwordx4 v[36:39], v53, s[28:31], s2 offen offset:896\n"
"buffer_load_dwordx4 v[40:43], v53, s[36:39], 0 offen offset:0\nbuffer_load_dwordx4 v[44:47], v53, s[40:43], 0 offen offset:0\nWL 0\ns_mul_i32 s15, s15, 1664525\n"
"s_add_u32 s15, s15, 1013904223\nv_mov_b32 v25, s15\ns_mov_b32 s57, 0x3db8aa3b\ns_and_b32 s59, s11, 1\ns_lshl_b32 s59, s59, 13\ns_mul_i32 s2, s73, 25165824\ns_and_b32 s3, s12, 1\n"
"s_lshl_b32 s3, s3, 21\ns_add_u32 s2, s2, s3\ns_lshl2_add_u32 s3, s13, s11\ns_lshl_b32 s3, s3, 15\ns_add_u32 s2, s2, s3\ns_add_u32 s56, s2, 0x800000\ns_xor_b32 s53, s56, 0xc000\n"
"buffer_load_dword v31, v24, s[24:27], s96 offen\ns_add_u32 s96, s96, 0x80000\nbuffer_load_dword v31, v24, s[24:27], s96 offen\ns_add_u32 s96, s96, 0x80000\n"
"v_lshrrev_b32 v60, 3, v0\nv_and_b32 v61, 0x70, v1\nv_lshl_or_b32 v60, v60, 10, v61\nv_mov_b32 v20, 0\nv_mov_b32 v21, 0\nv_mov_b32 v22, 0\nv_mov_b32 v23, 0\nSv 20,60,16,94,0\n"
"Sv 20,60,16,95,0\nWV 28\ns_barrier\nRa 128,10,0\nRa 132,11,0\nRa 136,10,2048\nRa 140,11,2048\nRa 144,10,8192\nRa 148,11,8192\nRa 152,10,10240\nRa 156,11,10240\nTa 160,12,0\n"
"Ta 162,12,2048\nTa 164,12,16384\nTa 166,12,18432\nTa 168,13,0\nTa 170,13,2048\nTa 172,13,16384\nTa 174,13,18432\nTa 176,14,0\nTa 178,14,2048\nTa 180,14,16384\nTa 182,14,18432\n"
"Ta 184,15,0\nTa 186,15,2048\nTa 188,15,16384\nTa 190,15,18432\nWL 0\ns_barrier\nZvaa 64,128,160\nTa 224,16,0\nMvaa 64,132,164\nTa 226,16,2048\nZvaa 68,140,164\n"
"s_add_u32 m0, s47, -65536\ns_add_u32 s45, s45, 128\nMvaa 68,136,160\nTa 228,16,16384\nZvaa 128,144,160\nTa 230,16,18432\nMvaa 128,148,164\nD0 2,20,45,0\nZvaa 132,156,164\n"
"Ta 232,17,0\nMvaa 132,152,160\nTa 234,17,2048\nZvaa 140,152,168\ns_mov_b32 s36, s60\nMvaa 140,156,172\nTa 236,17,16384\nZvaa 136,148,172\nTa 238,17,18432\nMvaa 136,144,168\n"
"s_and_b32 s37, s61, 0xffff\nZvaa 76,136,168\nTa 240,18,0\nMvaa 76,140,172\nTa 242,18,2048\nZvaa 72,132,172\nD0 3,20,45,1024\nMvaa 72,128,168\nTa 244,18,16384\nZvaa 80,128,176\n"
"Ta 246,18,18432\nMvaa 80,132,180\ns_mov_b32 s38, 0x380000\nZvaa 84,140,180\nTa 248,19,0\nMvaa 84,136,176\nTa 250,19,2048\nZvaa 144,144,176\ns_mov_b32 s39, 0x20000\n"
"Mvaa 144,148,180\nTa 252,19,16384\nZvaa 148,156,180\nTa 254,19,18432\nMvaa 148,152,176\nD0 4,20,45,2048\nZvaa 156,152,184\ns_lshl3_add_u32 s75, s12, s13\nMvaa 156,156,188\n"
"s_lshl2_add_u32 s75, s75, s11\nZvaa 152,148,188\ns_mul_i32 s75, s75, 7168\nMvaa 152,144,184\ns_mul_i32 s76, s11, 7168\nZvaa 92,136,184\ns_add_u32 s76, s76, 131072\n"
"Mvaa 92,140,188\nD0 5,20,45,3072\nZvaa 88,132,188\ns_mov_b32 s42, 1605632\nMvaa 88,128,184\ns_waitcnt vmcnt(24) lgkmcnt(0)\ns_barrier\nZvaa 96,128,224\nRa 192,10,16384\n"
"Mvaa 96,132,228\ns_add_u32 m0, s47, 0\ns_add_u32 s44, s44, 0x40000\nZvaa 100,140,228\nRa 196,11,16384\nMvaa 100,136,224\nZvaa 160,144,224\nRa 200,10,18432\nMvaa 160,148,228\n"
"D0 6,24,44,0\nZvaa 164,156,228\nRa 204,11,18432\nMvaa 164,152,224\nZvaa 172,152,232\nRa 208,10,24576\nMvaa 172,156,236\ns_and_b32 s78, s74, 3\ns_lshl2_add_u32 s78, s73, s78\n"
"s_lshl_b32 s78, s78, 12\ns_bitcmp1_b32 s11, 0\ns_cselect_b32 s79, s48, s45\ns_add_u32 s79, s79, 512\ns_and_b32 s79, s79, 0xfffe0000\ns_add_u32 s78, s78, s79\n"
"buffer_load_dword v31, v26, s[20:23], s78 offen\nZvaa 168,148,236\nRa 212,11,24576\nMvaa 168,144,232\nZvaa 108,136,232\nRa 216,10,26624\nMvaa 108,140,236\nD0 7,24,44,1024\n"
"Zvaa 104,132,236\nRa 220,11,26624\nMvaa 104,128,232\nZvaa 112,128,240\nMvaa 112,132,244\nZvaa 116,140,244\nMvaa 116,136,240\nZvaa 176,144,240\nMvaa 176,148,244\nD0 8,24,44,2048\n"
"Zvaa 180,156,244\nMvaa 180,152,240\nZvaa 188,152,248\nMvaa 188,156,252\nZvaa 184,148,252\nMvaa 184,144,248\nZvaa 124,136,248\nMvaa 124,140,252\nD0 9,24,44,3072\nZvaa 120,132,252\n"
"Mvaa 120,128,248\nWL 0\nZaaa 32,192,224\nRa 128,10,32768\nMaaa 32,196,228\ns_add_u32 m0, s47, 16384\ns_add_u32 s44, s44, 0x40000\nZaaa 36,204,228\nRa 132,11,32768\n"
"Maaa 36,200,224\nZaaa 96,208,224\nRa 136,10,34816\nMaaa 96,212,228\nD0 6,24,44,0\nZaaa 100,220,228\nRa 140,11,34816\nMaaa 100,216,224\nZaaa 108,216,232\nRa 144,10,40960\n"
"Maaa 108,220,236\nZaaa 104,212,236\nRa 148,11,40960\nMaaa 104,208,232\nZaaa 44,200,232\nRa 152,10,43008\nMaaa 44,204,236\nD0 7,24,44,1024\nZaaa 40,196,236\nRa 156,11,43008\n"
"Maaa 40,192,232\nZaaa 48,192,240\nMaaa 48,196,244\nZaaa 52,204,244\nMaaa 52,200,240\nZaaa 112,208,240\nMaaa 112,212,244\nD0 8,24,44,2048\nZaaa 116,220,244\nMaaa 116,216,240\n"
"Zaaa 124,216,248\nMaaa 124,220,252\nZaaa 120,212,252\nMaaa 120,208,248\nZaaa 60,200,248\nMaaa 60,204,252\nD0 9,24,44,3072\nZaaa 56,196,252\nMaaa 56,192,248\n"
"s_waitcnt vmcnt(25) lgkmcnt(0)\ns_barrier\nZaaa 0,192,160\nTa 224,16,32768\nWV 17\nMaaa 0,196,164\nTa 226,16,34816\nZaaa 4,204,164\ns_add_u32 m0, s47, -49152\n"
"s_add_u32 s48, s48, 128\nMaaa 4,200,160\nTa 228,16,49152\nZaaa 64,208,160\nTa 230,16,51200\nbuffer_load_dword v31, v24, s[24:27], s96 offen\ns_add_u32 s96, s96, 0x80000\n"
"Maaa 64,212,164\nD0 2,20,48,0\nv_lshlrev_b32 v48, 16, v32\nZaaa 68,220,164\nTa 232,17,32768\nMaaa 68,216,160\nTa 234,17,34816\nv_lshlrev_b32 v49, 16, v36\nZaaa 76,216,168\n"
"v_lshlrev_b32 v50, 16, v40\nv_lshlrev_b32 v51, 16, v44\nMaaa 76,220,172\nTa 236,17,49152\nZaaa 72,212,172\nTa 238,17,51200\nMU 52,48,50\nMaaa 72,208,168\nMU 53,49,50\n"
"Zaaa 12,200,168\nTa 240,18,32768\nCV 52,52,53\nMaaa 12,204,172\nTa 242,18,34816\nv_lshlrev_b32 v54, 16, v52\nZaaa 8,196,172\nD0 3,20,48,1024\nMaaa 8,192,168\nTa 244,18,49152\n"
"v_and_b32 v55, 0xffff0000, v52\nZaaa 16,192,176\nTa 246,18,51200\nMU 56,48,51\nMaaa 16,196,180\nMU 57,49,51\nZaaa 20,204,180\nTa 248,19,32768\nv_and_b32 v48, 0xffff0000, v32\n"
"Maaa 20,200,176\nTa 250,19,34816\nZaaa 80,208,176\nv_and_b32 v49, 0xffff0000, v36\nv_and_b32 v50, 0xffff0000, v40\nMaaa 80,212,180\nTa 252,19,49152\n"
"v_and_b32 v51, 0xffff0000, v44\nZaaa 84,220,180\nTa 254,19,51200\nMaaa 84,216,176\nD0 4,20,48,2048\nMU 52,48,50\nZaaa 92,216,184\nMU 53,49,50\nCV 52,52,53\nMaaa 92,220,188\n"
"v_and_b32 v53, 0xffff0000, v52\nZaaa 88,212,188\nv_lshlrev_b32 v52, 16, v52\nFM 56,48,51\nMaaa 88,208,184\nFM 57,49,51\nZaaa 28,200,184\nAD 54,54,52\nAD 55,55,53\nMaaa 28,204,188\n"
"D0 5,20,48,3072\nv_lshlrev_b32 v48, 16, v33\nZaaa 24,196,188\nv_lshlrev_b32 v49, 16, v37\nMaaa 24,192,184\nWL 0\nMvaa 96,128,224\nTa 160,12,32768\nv_lshlrev_b32 v50, 16, v41\n"
"Mvaa 96,132,228\nTa 162,12,34816\nMvaa 100,140,228\ns_add_u32 m0, s47, -32768\ns_add_u32 s45, s45, 128\nMvaa 100,136,224\nTa 164,12,49152\nMvaa 160,144,224\nTa 166,12,51200\n"
"v_lshlrev_b32 v51, 16, v45\nMvaa 160,148,228\nD0 2,20,45,0\nMU 52,48,50\nMvaa 164,156,228\nTa 168,13,32768\nMvaa 164,152,224\nTa 170,13,34816\nMU 53,49,50\nMvaa 172,152,232\n"
"CV 52,52,53\nv_and_b32 v53, 0xffff0000, v52\nMvaa 172,156,236\nTa 172,13,49152\nMvaa 168,148,236\nTa 174,13,51200\nv_lshlrev_b32 v52, 16, v52\nMvaa 168,144,232\nFM 56,48,51\n"
"Mvaa 108,136,232\nTa 176,14,32768\nFM 57,49,51\nMvaa 108,140,236\nTa 178,14,34816\nAD 54,54,52\nMvaa 104,132,236\nD0 3,20,45,1024\nMvaa 104,128,232\nTa 180,14,49152\nAD 55,55,53\n"
"Mvaa 112,128,240\nTa 182,14,51200\nv_and_b32 v48, 0xffff0000, v33\nMvaa 112,132,244\nv_and_b32 v49, 0xffff0000, v37\nMvaa 116,140,244\nTa 184,15,32768\n"
"v_and_b32 v50, 0xffff0000, v41\nMvaa 116,136,240\nTa 186,15,34816\nMvaa 176,144,240\nv_and_b32 v51, 0xffff0000, v45\nMU 52,48,50\nMvaa 176,148,244\nTa 188,15,49152\nMU 53,49,50\n"
"Mvaa 180,156,244\nTa 190,15,51200\nMvaa 180,152,240\nD0 4,20,45,2048\nCV 52,52,53\nMvaa 188,152,248\nv_and_b32 v53, 0xffff0000, v52\nv_lshlrev_b32 v52, 16, v52\nMvaa 188,156,252\n"
"FM 56,48,51\nMvaa 184,148,252\nFM 57,49,51\nAD 54,54,52\nMvaa 184,144,248\nAD 55,55,53\nMvaa 124,136,248\nv_lshlrev_b32 v48, 16, v34\nv_lshlrev_b32 v49, 16, v38\nMvaa 124,140,252\n"
"D0 5,20,45,3072\nv_lshlrev_b32 v50, 16, v42\nMvaa 120,132,252\nv_lshlrev_b32 v51, 16, v46\nMvaa 120,128,248\ns_waitcnt vmcnt(18) lgkmcnt(0)\ns_barrier\nMvaa 64,128,160\n"
"Ra 192,10,49152\nMU 52,48,50\nMvaa 64,132,164\ns_add_u32 m0, s47, 32768\ns_add_u32 s44, s44, 0x40000\nMvaa 68,140,164\nRa 196,11,49152\nMU 53,49,50\nMvaa 68,136,160\nCV 52,52,53\n"
"Mvaa 128,144,160\nRa 200,10,51200\nv_and_b32 v53, 0xffff0000, v52\nMvaa 128,148,164\nD0 6,24,44,0\nv_lshlrev_b32 v52, 16, v52\nMvaa 132,156,164\nRa 204,11,51200\nMvaa 132,152,160\n"
"FM 56,48,51\nFM 57,49,51\nMvaa 140,152,168\nRa 208,10,57344\nAD 54,54,52\nMvaa 140,156,172\nAD 55,55,53\nMvaa 136,148,172\nRa 212,11,57344\nv_and_b32 v48, 0xffff0000, v34\n"
"Mvaa 136,144,168\nv_and_b32 v49, 0xffff0000, v38\nMvaa 76,136,168\nRa 216,10,59392\nv_and_b32 v50, 0xffff0000, v42\nMvaa 76,140,172\nD0 7,24,44,1024\n"
"v_and_b32 v51, 0xffff0000, v46\nMvaa 72,132,172\nRa 220,11,59392\nMvaa 72,128,168\nMU 52,48,50\nMU 53,49,50\nMvaa 80,128,176\nCV 52,52,53\nv_and_b32 v53, 0xffff0000, v52\n"
"Mvaa 80,132,180\nv_lshlrev_b32 v52, 16, v52\nMvaa 84,140,180\nFM 56,48,51\nFM 57,49,51\nMvaa 84,136,176\nAD 54,54,52\nMvaa 144,144,176\nAD 55,55,53\nv_lshlrev_b32 v48, 16, v35\n"
"Mvaa 144,148,180\nD0 8,24,44,2048\nv_lshlrev_b32 v49, 16, v39\nMvaa 148,156,180\nv_lshlrev_b32 v50, 16, v43\nMvaa 148,152,176\nv_lshlrev_b32 v51, 16, v47\nMU 52,48,50\n"
"Mvaa 156,152,184\nMU 53,49,50\nCV 52,52,53\nMvaa 156,156,188\nv_and_b32 v53, 0xffff0000, v52\nMvaa 152,148,188\nv_lshlrev_b32 v52, 16, v52\nFM 56,48,51\nMvaa 152,144,184\n"
"FM 57,49,51\nMvaa 92,136,184\nAD 54,54,52\nAD 55,55,53\nMvaa 92,140,188\nD0 9,24,44,3072\nv_and_b32 v48, 0xffff0000, v35\nMvaa 88,132,188\nv_and_b32 v49, 0xffff0000, v39\n"
"Mvaa 88,128,184\nWL 0\nMaaa 0,192,160\nRa 128,10,0\nv_and_b32 v50, 0xffff0000, v43\nMaaa 0,196,164\ns_add_u32 m0, s47, 49152\ns_add_u32 s44, s44, 0x40000\nMaaa 4,204,164\n"
"Ra 132,11,0\nv_and_b32 v51, 0xffff0000, v47\nMaaa 4,200,160\nMU 52,48,50\nMaaa 64,208,160\nRa 136,10,2048\nMU 53,49,50\nMaaa 64,212,164\nD0 6,24,44,0\nCV 52,52,53\n"
"Maaa 68,220,164\nRa 140,11,2048\nMaaa 68,216,160\nv_and_b32 v53, 0xffff0000, v52\nv_lshlrev_b32 v52, 16, v52\nMaaa 76,216,168\nRa 144,10,8192\nFM 56,48,51\nMaaa 76,220,172\n"
"FM 57,49,51\n.W575:\ns_cbranch_execz .W579\nMaaa 72,212,172\nRa 148,11,8192\nAD 54,54,52\nMaaa 72,208,168\nAD 55,55,53\nMaaa 12,200,168\nRa 152,10,10240\n"
"v_permlane32_swap_b32 v54, v55\nMaaa 12,204,172\nD0 7,24,44,1024\nv_permlane32_swap_b32 v56, v57\nMaaa 8,196,172\nRa 156,11,10240\nMaaa 8,192,168\nAD 54,54,55\nAD 56,56,57\n"
"Maaa 16,192,176\nMaaa 16,196,180\nv_permlane16_swap_b32 v54, v56\nMaaa 20,204,180\nAD 54,54,56\nMaaa 20,200,176\nMaaa 80,208,176\n"
"v_add_f32_dpp v54, v54, v54 row_ror:8 row_mask:0xf bank_mask:0xf\nMaaa 80,212,180\nD0 8,24,44,2048\nMaaa 84,220,180\n"
"v_add_f32_dpp v54, v54, v54 row_ror:4 row_mask:0xf bank_mask:0xf\nMaaa 84,216,176\nMaaa 92,216,184\nv_add_f32_dpp v54, v54, v54 row_ror:2 row_mask:0xf bank_mask:0xf\n"
"Maaa 92,220,188\nMaaa 88,212,188\nv_add_f32_dpp v54, v54, v54 row_ror:1 row_mask:0xf bank_mask:0xf\nCV 54,54,54\nMaaa 88,208,184\nv_lshlrev_b32 v54, 16, v54\nMaaa 28,200,184\n"
"v_mul_f32 v55, 0xbfb8aa3b, v54\nMaaa 28,204,188\nD0 9,24,44,3072\nMaaa 24,196,188\nEX 55,55\nMaaa 24,192,184\ns_waitcnt vmcnt(12) lgkmcnt(0)\ns_barrier\nMaaa 32,192,224\n"
"Ta 160,12,0\nv_add_f32 v56, 0, v54\nMaaa 32,196,228\nTa 162,12,2048\nMaaa 36,204,228\ns_add_u32 m0, s47, -16384\ns_add_u32 s48, s48, 128\nMaaa 36,200,224\nTa 164,12,16384\n"
"Maaa 96,208,224\nTa 166,12,18432\nbuffer_load_dword v31, v24, s[24:27], s96 offen\ns_add_u32 s96, s96, 0x80000\nMaaa 96,212,228\nD0 2,20,48,0\nv_add_f32 v55, 1.0, v55\n"
"Maaa 100,220,228\nTa 168,13,0\nv_rcp_f32 v55, v55\nMaaa 100,216,224\nTa 170,13,2048\nv_ashrrev_i32 v57, 31, v56\nMaaa 108,216,232\nCV 55,55,55\nv_lshlrev_b32 v55, 16, v55\n"
"Maaa 108,220,236\nTa 172,13,16384\nMaaa 104,212,236\nTa 174,13,18432\nv_or_b32 v57, 0x80000000, v57\nMaaa 104,208,232\nv_xor_b32 v56, v56, v57\nMaaa 44,200,232\nTa 176,14,0\n"
"v_and_b32 v56, 0xffff0000, v56\nMaaa 44,204,236\nTa 178,14,2048\ns_lshl2_add_u32 s2, s10, s11\nMaaa 40,196,236\nD0 3,20,48,1024\nMaaa 40,192,232\nTa 180,14,16384\n"
"v_lshrrev_b32 v48, 5, v0\nMaaa 48,192,240\nTa 182,14,18432\nv_lshl_add_u32 v48, s2, 1, v48\nMaaa 48,196,244\nv_sub_u32 v49, 0xffff, v48\nMaaa 52,204,244\nTa 184,15,0\n"
"v_or_b32 v56, v56, v49\nMaaa 52,200,240\nTa 186,15,2048\nMaaa 112,208,240\nv_mov_b32 v57, s15\nv_and_b32 v49, 31, v0\nMaaa 112,212,244\nTa 188,15,16384\nv_mov_b32 v52, 0x40000000\n"
"Maaa 116,220,244\nTa 190,15,18432\nv_cmp_eq_u32 vcc, 16, v49\nv_lshlrev_b32 v50, 2, v48\nv_lshlrev_b32 v51, 10, v49\nv_cndmask_b32 v50, v52, v50, vcc\n"
"v_cndmask_b32 v56, v55, v56, vcc\nMaaa 116,216,240\nD0 4,20,48,2048\nv_lshl_add_u32 v51, v48, 3, v51\nMaaa 124,216,248\nv_and_b32 v49, 15, v0\nv_cmp_eq_u32 vcc, 0, v49\n"
"s_mov_b32 s2, 0xc040000\ns_mov_b32 s3, 0xc03e000\nv_cndmask_b32 v51, v52, v51, vcc\nbuffer_store_dwordx2 v[56:57], v51, s[16:19], s2 offen sc0 sc1\n"
"buffer_store_dword v54, v50, s[16:19], s3 offen sc0 sc1\nMaaa 124,220,252\nMaaa 120,212,252\nMaaa 120,208,248\nMaaa 60,200,248\nMaaa 60,204,252\nD0 5,20,48,3072\nMaaa 56,196,252\n"
"Maaa 56,192,248\nWL 0\ns_mov_b32 s50, 3\n.GL410:\nMvaa 64,128,160\nTa 224,16,0\nMvaa 64,132,164\nTa 226,16,2048\nMvaa 68,140,164\nTa 228,16,16384\ns_add_u32 m0, s47, -65536\n"
"s_add_u32 s45, s45, 128\nMvaa 68,136,160\nTa 230,16,18432\nMvaa 128,144,160\nTa 232,17,0\nMvaa 128,148,164\nTa 234,17,2048\nD0 2,20,45,0\nMvaa 132,156,164\nTa 236,17,16384\n"
"Mvaa 132,152,160\nTa 238,17,18432\nMvaa 140,152,168\nTa 240,18,0\nMvaa 140,156,172\nTa 242,18,2048\nMvaa 136,148,172\nTa 244,18,16384\nMvaa 136,144,168\nTa 246,18,18432\n"
"Mvaa 76,136,168\nTa 248,19,0\nMvaa 76,140,172\nTa 250,19,2048\nMvaa 72,132,172\nTa 252,19,16384\nD0 3,20,45,1024\nMvaa 72,128,168\nTa 254,19,18432\nMvaa 80,128,176\n"
"Ra 192,10,16384\nMvaa 80,132,180\nRa 196,11,16384\nMvaa 84,140,180\nRa 200,10,18432\nMvaa 84,136,176\nRa 204,11,18432\nMvaa 144,144,176\nRa 208,10,24576\nMvaa 144,148,180\n"
"Ra 212,11,24576\nMvaa 148,156,180\nRa 216,10,26624\nMvaa 148,152,176\nRa 220,11,26624\nD0 4,20,45,2048\nMvaa 156,152,184\nMvaa 156,156,188\nMvaa 152,148,188\nMvaa 152,144,184\n"
"Mvaa 92,136,184\nMvaa 92,140,188\nD0 5,20,45,3072\nMvaa 88,132,188\nMvaa 88,128,184\ns_waitcnt vmcnt(18) lgkmcnt(8)\ns_barrier\nMvaa 96,128,224\nMvaa 96,132,228\n"
"s_add_u32 m0, s47, 0\ns_add_u32 s44, s44, 0x40000\ns_mov_b32 s86, 1\ns_load_dword s86, s[54:55], s94\nMvaa 100,140,228\ns_add_u32 s94, s94, 1024\nMvaa 100,136,224\n"
"Mvaa 160,144,224\nMvaa 160,148,228\nD0 6,24,44,0\nMvaa 164,156,228\nMvaa 164,152,224\nMvaa 172,152,232\nMvaa 172,156,236\nMvaa 168,148,236\nMvaa 168,144,232\n"
"s_add_u32 s88, s88, s86\nMvaa 108,136,232\nMvaa 108,140,236\nD0 7,24,44,1024\ns_add_u32 s88, s88, s86\nMvaa 104,132,236\nMvaa 104,128,232\ns_add_u32 s88, s88, s86\n"
"Mvaa 112,128,240\nMvaa 112,132,244\ns_add_u32 s88, s88, s86\nMvaa 116,140,244\nMvaa 116,136,240\ns_add_u32 s88, s88, s86\nMvaa 176,144,240\nMvaa 176,148,244\nD0 8,24,44,2048\n"
"s_add_u32 s88, s88, s86\nMvaa 180,156,244\nMvaa 180,152,240\ns_add_u32 s88, s88, s86\nMvaa 188,152,248\nMvaa 188,156,252\ns_add_u32 s88, s88, s86\nMvaa 184,148,252\n"
"Mvaa 184,144,248\ns_add_u32 s88, s88, s86\nMvaa 124,136,248\nMvaa 124,140,252\nD0 9,24,44,3072\ns_add_u32 s88, s88, s86\nMvaa 120,132,252\nMvaa 120,128,248\nWL 0\n"
"s_add_u32 s88, s88, s86\nMaaa 32,192,224\nRa 128,10,32768\nMaaa 32,196,228\ns_add_u32 m0, s47, 16384\ns_add_u32 s44, s44, 0x40000\ns_add_u32 s88, s88, s86\nMaaa 36,204,228\n"
"Ra 132,11,32768\nMaaa 36,200,224\ns_add_u32 s88, s88, s86\nMaaa 96,208,224\nRa 136,10,34816\nMaaa 96,212,228\nD0 6,24,44,0\ns_add_u32 s88, s88, s86\nMaaa 100,220,228\n"
"Ra 140,11,34816\nMaaa 100,216,224\ns_add_u32 s88, s88, s86\nMaaa 108,216,232\nRa 144,10,40960\nMaaa 108,220,236\ns_add_u32 s88, s88, s86\nMaaa 104,212,236\nRa 148,11,40960\n"
"Maaa 104,208,232\nMaaa 44,200,232\nRa 152,10,43008\nMaaa 44,204,236\nD0 7,24,44,1024\nMaaa 40,196,236\nRa 156,11,43008\nMaaa 40,192,232\nMaaa 48,192,240\nMaaa 48,196,244\n"
"Maaa 52,204,244\nMaaa 52,200,240\nMaaa 112,208,240\nMaaa 112,212,244\nD0 8,24,44,2048\nMaaa 116,220,244\nMaaa 116,216,240\nMaaa 124,216,248\nMaaa 124,220,252\nMaaa 120,212,252\n"
"Maaa 120,208,248\nMaaa 60,200,248\nMaaa 60,204,252\nD0 9,24,44,3072\nMaaa 56,196,252\nMaaa 56,192,248\ns_waitcnt vmcnt(12) lgkmcnt(0)\ns_barrier\nMaaa 0,192,160\nTa 224,16,32768\n"
"s_mov_b32 m0, s76\ns_add_u32 s76, s76, 1024\nMaaa 0,196,164\nTa 226,16,34816\nD0 1,36,75,0\ns_add_u32 s75, s75, 1024\nMaaa 4,204,164\ns_add_u32 m0, s47, -49152\n"
"s_add_u32 s48, s48, 128\nMaaa 4,200,160\nTa 228,16,49152\nMaaa 64,208,160\nTa 230,16,51200\nbuffer_load_dword v31, v24, s[24:27], s96 offen\ns_add_u32 s96, s96, 0x80000\n"
"Maaa 64,212,164\nD0 2,20,48,0\nMaaa 68,220,164\nTa 232,17,32768\nMaaa 68,216,160\nTa 234,17,34816\nMaaa 76,216,168\nMaaa 76,220,172\nTa 236,17,49152\nMaaa 72,212,172\n"
"Ta 238,17,51200\nMaaa 72,208,168\nMaaa 12,200,168\nTa 240,18,32768\nMaaa 12,204,172\nTa 242,18,34816\nMaaa 8,196,172\nD0 3,20,48,1024\nMaaa 8,192,168\nTa 244,18,49152\n"
"Maaa 16,192,176\nTa 246,18,51200\nMaaa 16,196,180\nMaaa 20,204,180\nTa 248,19,32768\nMaaa 20,200,176\nTa 250,19,34816\nMaaa 80,208,176\nMaaa 80,212,180\nTa 252,19,49152\n"
"Maaa 84,220,180\nTa 254,19,51200\nMaaa 84,216,176\nD0 4,20,48,2048\nMaaa 92,216,184\nMaaa 92,220,188\nMaaa 88,212,188\nMaaa 88,208,184\nMaaa 28,200,184\nMaaa 28,204,188\n"
"D0 5,20,48,3072\nMaaa 24,196,188\nMaaa 24,192,184\nWL 0\nMvaa 96,128,224\nTa 160,12,32768\nMvaa 96,132,228\nTa 162,12,34816\nMvaa 100,140,228\nTa 164,12,49152\n"
"s_add_u32 m0, s47, -32768\ns_add_u32 s45, s45, 128\nMvaa 100,136,224\nTa 166,12,51200\nMvaa 160,144,224\nTa 168,13,32768\nMvaa 160,148,228\nTa 170,13,34816\nD0 2,20,45,0\n"
"Mvaa 164,156,228\nTa 172,13,49152\nMvaa 164,152,224\nTa 174,13,51200\nMvaa 172,152,232\nTa 176,14,32768\nMvaa 172,156,236\nTa 178,14,34816\nMvaa 168,148,236\nTa 180,14,49152\n"
"Mvaa 168,144,232\nTa 182,14,51200\nMvaa 108,136,232\nTa 184,15,32768\nMvaa 108,140,236\nTa 186,15,34816\nMvaa 104,132,236\nTa 188,15,49152\nD0 3,20,45,1024\nMvaa 104,128,232\n"
"Ta 190,15,51200\nMvaa 112,128,240\nRa 192,10,49152\nMvaa 112,132,244\nRa 196,11,49152\nMvaa 116,140,244\nRa 200,10,51200\nMvaa 116,136,240\nRa 204,11,51200\nMvaa 176,144,240\n"
"Ra 208,10,57344\nMvaa 176,148,244\nRa 212,11,57344\nMvaa 180,156,244\nRa 216,10,59392\nMvaa 180,152,240\nRa 220,11,59392\nD0 4,20,45,2048\nMvaa 188,152,248\nMvaa 188,156,252\n"
"Mvaa 184,148,252\nMvaa 184,144,248\nMvaa 124,136,248\nMvaa 124,140,252\nD0 5,20,45,3072\nMvaa 120,132,252\nMvaa 120,128,248\ns_waitcnt vmcnt(18) lgkmcnt(8)\ns_barrier\n"
"Mvaa 64,128,160\nMvaa 64,132,164\ns_add_u32 m0, s47, 32768\ns_add_u32 s44, s44, 0x40000\ns_mov_b32 s86, 1\ns_load_dword s86, s[54:55], s95\nMvaa 68,140,164\n"
"s_add_u32 s95, s95, 1024\nMvaa 68,136,160\nMvaa 128,144,160\nMvaa 128,148,164\nD0 6,24,44,0\nMvaa 132,156,164\nMvaa 132,152,160\nMvaa 140,152,168\nMvaa 140,156,172\n"
"Mvaa 136,148,172\nMvaa 136,144,168\ns_sub_u32 s88, s88, s86\nMvaa 76,136,168\nMvaa 76,140,172\nD0 7,24,44,1024\ns_sub_u32 s88, s88, s86\nMvaa 72,132,172\nMvaa 72,128,168\n"
"s_sub_u32 s88, s88, s86\nMvaa 80,128,176\nMvaa 80,132,180\ns_sub_u32 s88, s88, s86\nMvaa 84,140,180\nMvaa 84,136,176\ns_sub_u32 s88, s88, s86\nMvaa 144,144,176\nMvaa 144,148,180\n"
"D0 8,24,44,2048\ns_sub_u32 s88, s88, s86\nMvaa 148,156,180\nMvaa 148,152,176\ns_sub_u32 s88, s88, s86\nMvaa 156,152,184\nMvaa 156,156,188\ns_sub_u32 s88, s88, s86\n"
"Mvaa 152,148,188\nMvaa 152,144,184\ns_sub_u32 s88, s88, s86\nMvaa 92,136,184\nMvaa 92,140,188\nD0 9,24,44,3072\ns_sub_u32 s88, s88, s86\nMvaa 88,132,188\nMvaa 88,128,184\nWL 0\n"
"s_sub_u32 s88, s88, s86\nMaaa 0,192,160\nRa 128,10,0\nMaaa 0,196,164\ns_add_u32 m0, s47, 49152\ns_add_u32 s44, s44, 0x40000\ns_sub_u32 s88, s88, s86\nMaaa 4,204,164\nRa 132,11,0\n"
"Maaa 4,200,160\ns_sub_u32 s88, s88, s86\nMaaa 64,208,160\nRa 136,10,2048\nMaaa 64,212,164\nD0 6,24,44,0\ns_sub_u32 s88, s88, s86\nMaaa 68,220,164\nRa 140,11,2048\nMaaa 68,216,160\n"
"s_sub_u32 s88, s88, s86\nMaaa 76,216,168\nRa 144,10,8192\nMaaa 76,220,172\ns_sub_u32 s88, s88, s86\nMaaa 72,212,172\nRa 148,11,8192\nMaaa 72,208,168\nMaaa 12,200,168\n"
"Ra 152,10,10240\nMaaa 12,204,172\nD0 7,24,44,1024\nMaaa 8,196,172\nRa 156,11,10240\nMaaa 8,192,168\nMaaa 16,192,176\nMaaa 16,196,180\nMaaa 20,204,180\nMaaa 20,200,176\n"
"Maaa 80,208,176\nMaaa 80,212,180\nD0 8,24,44,2048\nMaaa 84,220,180\nMaaa 84,216,176\nMaaa 92,216,184\nMaaa 92,220,188\nMaaa 88,212,188\nMaaa 88,208,184\nMaaa 28,200,184\n"
"Maaa 28,204,188\nD0 9,24,44,3072\nMaaa 24,196,188\nMaaa 24,192,184\ns_waitcnt vmcnt(12) lgkmcnt(0)\ns_barrier\nMaaa 32,192,224\nTa 160,12,0\ns_mov_b32 m0, s76\n"
"s_add_u32 s76, s76, 1024\nMaaa 32,196,228\nTa 162,12,2048\nD0 1,36,75,0\ns_add_u32 s75, s75, 1024\nMaaa 36,204,228\ns_add_u32 m0, s47, -16384\ns_add_u32 s48, s48, 128\n"
"Maaa 36,200,224\nTa 164,12,16384\nMaaa 96,208,224\nTa 166,12,18432\nbuffer_load_dword v31, v24, s[24:27], s96 offen\ns_add_u32 s96, s96, 0x80000\nMaaa 96,212,228\n.W576:\n"
"s_cbranch_execz .W580\nD0 2,20,48,0\nMaaa 100,220,228\nTa 168,13,0\nMaaa 100,216,224\nTa 170,13,2048\nMaaa 108,216,232\nMaaa 108,220,236\nTa 172,13,16384\nMaaa 104,212,236\n"
"Ta 174,13,18432\nMaaa 104,208,232\nMaaa 44,200,232\nTa 176,14,0\nMaaa 44,204,236\nTa 178,14,2048\nMaaa 40,196,236\nD0 3,20,48,1024\nMaaa 40,192,232\nTa 180,14,16384\n"
"Maaa 48,192,240\nTa 182,14,18432\nMaaa 48,196,244\nMaaa 52,204,244\nTa 184,15,0\nMaaa 52,200,240\nTa 186,15,2048\nMaaa 112,208,240\nMaaa 112,212,244\nTa 188,15,16384\n"
"Maaa 116,220,244\nTa 190,15,18432\nMaaa 116,216,240\nD0 4,20,48,2048\nMaaa 124,216,248\nMaaa 124,220,252\nMaaa 120,212,252\nMaaa 120,208,248\nMaaa 60,200,248\n"
"s_xor_b32 s94, s94, 0x2000\ns_xor_b32 s95, s95, 0x2000\ns_sub_u32 s88, 0, s88\nMaaa 60,204,252\nD0 5,20,48,3072\ns_sub_u32 s50, s50, 1\nMaaa 56,196,252\nMaaa 56,192,248\nWL 0\n"
"s_cbranch_scc0 .GL410\nMvaa 64,128,160\nTa 224,16,0\nMvaa 64,132,164\nTa 226,16,2048\nMvaa 68,140,164\ns_add_u32 m0, s47, -65536\ns_add_u32 s45, s45, 128\nMvaa 68,136,160\n"
"Ta 228,16,16384\nMvaa 128,144,160\nTa 230,16,18432\nMvaa 128,148,164\nD0 2,20,45,0\nMvaa 132,156,164\nTa 232,17,0\nMvaa 132,152,160\nTa 234,17,2048\nMvaa 140,152,168\n"
"s_sub_u32 s89, 0, s88\ns_cmp_eq_u32 s92, 0\ns_cselect_b32 s88, s88, s89\nMvaa 140,156,172\nTa 236,17,16384\nMvaa 136,148,172\nTa 238,17,18432\nMvaa 136,144,168\n"
"v_mov_b32 v22, s88\nMvaa 76,136,168\nTa 240,18,0\nMvaa 76,140,172\nTa 242,18,2048\nMvaa 72,132,172\nD0 3,20,45,1024\nMvaa 72,128,168\nTa 244,18,16384\nMvaa 80,128,176\n"
"Ta 246,18,18432\nMvaa 80,132,180\nv_mov_b32 v23, s15\nMvaa 84,140,180\nTa 248,19,0\nMvaa 84,136,176\nTa 250,19,2048\nMvaa 144,144,176\n"
"buffer_store_dwordx2 v[22:23], off, s[16:19], s93 sc0 sc1\nMvaa 144,148,180\nTa 252,19,16384\nMvaa 148,156,180\nTa 254,19,18432\nMvaa 148,152,176\nD0 4,20,45,2048\n"
"Mvaa 156,152,184\nMvaa 156,156,188\nMvaa 152,148,188\nMvaa 152,144,184\nMvaa 92,136,184\nMvaa 92,140,188\nD0 5,20,45,3072\nMvaa 88,132,188\nMvaa 88,128,184\n"
"s_waitcnt vmcnt(19) lgkmcnt(0)\ns_barrier\nMvaa 96,128,224\nRa 192,10,16384\nMvaa 96,132,228\ns_add_u32 m0, s47, 0\ns_add_u32 s44, s44, 0x40000\nMvaa 100,140,228\nRa 196,11,16384\n"
"Mvaa 100,136,224\nMvaa 160,144,224\nRa 200,10,18432\nMvaa 160,148,228\nD0 6,24,44,0\nMvaa 164,156,228\nRa 204,11,18432\nMvaa 164,152,224\nMvaa 172,152,232\nRa 208,10,24576\n"
"Mvaa 172,156,236\nMvaa 168,148,236\nRa 212,11,24576\nMvaa 168,144,232\nMvaa 108,136,232\nRa 216,10,26624\nMvaa 108,140,236\nD0 7,24,44,1024\nMvaa 104,132,236\nRa 220,11,26624\n"
"Mvaa 104,128,232\nMvaa 112,128,240\nMvaa 112,132,244\nMvaa 116,140,244\nMvaa 116,136,240\nMvaa 176,144,240\nMvaa 176,148,244\nD0 8,24,44,2048\nMvaa 180,156,244\nMvaa 180,152,240\n"
"Mvaa 188,152,248\nMvaa 188,156,252\nMvaa 184,148,252\nMvaa 184,144,248\nMvaa 124,136,248\nMvaa 124,140,252\nD0 9,24,44,3072\nMvaa 120,132,252\nMvaa 120,128,248\nWL 0\n"
"Maaa 32,192,224\nRa 128,10,32768\nMaaa 32,196,228\ns_add_u32 m0, s47, 16384\ns_add_u32 s44, s44, 0x40000\nMaaa 36,204,228\nRa 132,11,32768\nMaaa 36,200,224\nMaaa 96,208,224\n"
"Ra 136,10,34816\nMaaa 96,212,228\nD0 6,24,44,0\nMaaa 100,220,228\nRa 140,11,34816\nMaaa 100,216,224\nMaaa 108,216,232\nRa 144,10,40960\nMaaa 108,220,236\nMaaa 104,212,236\n"
"Ra 148,11,40960\nMaaa 104,208,232\nMaaa 44,200,232\nRa 152,10,43008\nMaaa 44,204,236\nD0 7,24,44,1024\nMaaa 40,196,236\nRa 156,11,43008\nMaaa 40,192,232\nMaaa 48,192,240\n"
"Maaa 48,196,244\nMaaa 52,204,244\nMaaa 52,200,240\nMaaa 112,208,240\nMaaa 112,212,244\nD0 8,24,44,2048\nMaaa 116,220,244\nMaaa 116,216,240\nMaaa 124,216,248\nMaaa 124,220,252\n"
"Maaa 120,212,252\nMaaa 120,208,248\nMaaa 60,200,248\nMaaa 60,204,252\nD0 9,24,44,3072\nMaaa 56,196,252\nMaaa 56,192,248\ns_waitcnt vmcnt(19) lgkmcnt(0)\ns_barrier\nMaaa 0,192,160\n"
"Ta 224,16,32768\nMaaa 0,196,164\nTa 226,16,34816\nMaaa 4,204,164\ns_add_u32 m0, s47, -49152\ns_add_u32 s48, s48, 128\nMaaa 4,200,160\nTa 228,16,49152\nMaaa 64,208,160\n"
"Ta 230,16,51200\nbuffer_load_dword v31, v24, s[24:27], s96 offen\ns_add_u32 s96, s96, 0x80000\nMaaa 64,212,164\nD0 2,20,48,0\nMaaa 68,220,164\nTa 232,17,32768\nMaaa 68,216,160\n"
"Ta 234,17,34816\nMaaa 76,216,168\nMaaa 76,220,172\nTa 236,17,49152\nMaaa 72,212,172\nTa 238,17,51200\nMaaa 72,208,168\nMaaa 12,200,168\nTa 240,18,32768\nMaaa 12,204,172\n"
"Ta 242,18,34816\nMaaa 8,196,172\nD0 3,20,48,1024\nMaaa 8,192,168\nTa 244,18,49152\nMaaa 16,192,176\nTa 246,18,51200\nMaaa 16,196,180\nMaaa 20,204,180\nTa 248,19,32768\n"
"Maaa 20,200,176\nTa 250,19,34816\nMaaa 80,208,176\nMaaa 80,212,180\nTa 252,19,49152\nMaaa 84,220,180\nTa 254,19,51200\nMaaa 84,216,176\nD0 4,20,48,2048\nMaaa 92,216,184\n"
"Maaa 92,220,188\nMaaa 88,212,188\nMaaa 88,208,184\nMaaa 28,200,184\nMaaa 28,204,188\nD0 5,20,48,3072\nMaaa 24,196,188\nMaaa 24,192,184\nWL 0\nMvaa 96,128,224\nTa 160,12,32768\n"
"Mvaa 96,132,228\nTa 162,12,34816\nMvaa 100,140,228\ns_add_u32 m0, s47, -32768\ns_add_u32 s45, s45, 128\nMvaa 100,136,224\nTa 164,12,49152\nMvaa 160,144,224\nTa 166,12,51200\n"
"Mvaa 160,148,228\nD0 2,20,45,0\nMvaa 164,156,228\nTa 168,13,32768\nMvaa 164,152,224\nTa 170,13,34816\nMvaa 172,152,232\nMvaa 172,156,236\nTa 172,13,49152\nMvaa 168,148,236\n"
"Ta 174,13,51200\nMvaa 168,144,232\nMvaa 108,136,232\nTa 176,14,32768\nMvaa 108,140,236\nTa 178,14,34816\nMvaa 104,132,236\nD0 3,20,45,1024\nMvaa 104,128,232\nTa 180,14,49152\n"
"Mvaa 112,128,240\nTa 182,14,51200\nMvaa 112,132,244\nMvaa 116,140,244\nTa 184,15,32768\nMvaa 116,136,240\nTa 186,15,34816\nMvaa 176,144,240\nMvaa 176,148,244\nTa 188,15,49152\n"
"Mvaa 180,156,244\nTa 190,15,51200\nMvaa 180,152,240\nD0 4,20,45,2048\nMvaa 188,152,248\nMvaa 188,156,252\nMvaa 184,148,252\nMvaa 184,144,248\nMvaa 124,136,248\nMvaa 124,140,252\n"
"D0 5,20,45,3072\nMvaa 120,132,252\nMvaa 120,128,248\ns_waitcnt vmcnt(17) lgkmcnt(0)\ns_barrier\nMvaa 64,128,160\nRa 192,10,49152\nMvaa 64,132,164\ns_add_u32 m0, s47, 32768\n"
"s_add_u32 s44, s44, 0x40000\nMvaa 68,140,164\nRa 196,11,49152\nMvaa 68,136,160\nMvaa 128,144,160\nRa 200,10,51200\nMvaa 128,148,164\nD0 6,24,44,0\nMvaa 132,156,164\n"
"Ra 204,11,51200\nMvaa 132,152,160\nMvaa 140,152,168\nRa 208,10,57344\nMvaa 140,156,172\ns_lshl_b32 s5, s73, 9\ns_add_u32 s5, s5, 0xc049400\nv_lshrrev_b32 v27, 1, v1\n"
"buffer_load_dwordx2 v[22:23], v27, s[16:19], s5 offen sc0 sc1\nMvaa 136,148,172\nRa 212,11,57344\nMvaa 136,144,168\nMvaa 76,136,168\nRa 216,10,59392\nMvaa 76,140,172\n"
"D0 7,24,44,1024\nMvaa 72,132,172\nRa 220,11,59392\nMvaa 72,128,168\nMvaa 80,128,176\nMvaa 80,132,180\nMvaa 84,140,180\nMvaa 84,136,176\nMvaa 144,144,176\nMvaa 144,148,180\n"
"D0 8,24,44,2048\nMvaa 148,156,180\nMvaa 148,152,176\nMvaa 156,152,184\nMvaa 156,156,188\nMvaa 152,148,188\nMvaa 152,144,184\nMvaa 92,136,184\nMvaa 92,140,188\nD0 9,24,44,3072\n"
"Mvaa 88,132,188\nMvaa 88,128,184\nWL 0\nMaaa 0,192,160\nRa 128,10,0\nMaaa 0,196,164\ns_add_u32 m0, s47, 49152\ns_add_u32 s44, s44, 0x40000\nMaaa 4,204,164\nRa 132,11,0\n"
"Maaa 4,200,160\nMaaa 64,208,160\nRa 136,10,2048\nMaaa 64,212,164\nD0 6,24,44,0\nMaaa 68,220,164\nRa 140,11,2048\nMaaa 68,216,160\nMaaa 76,216,168\nRa 144,10,8192\nMaaa 76,220,172\n"
"Maaa 72,212,172\nRa 148,11,8192\nMaaa 72,208,168\nMaaa 12,200,168\nRa 152,10,10240\nMaaa 12,204,172\nD0 7,24,44,1024\nMaaa 8,196,172\nRa 156,11,10240\nMaaa 8,192,168\n"
"Maaa 16,192,176\nMaaa 16,196,180\nMaaa 20,204,180\nMaaa 20,200,176\nMaaa 80,208,176\nMaaa 80,212,180\nD0 8,24,44,2048\nMaaa 84,220,180\nMaaa 84,216,176\nMaaa 92,216,184\n"
"Maaa 92,220,188\nMaaa 88,212,188\nMaaa 88,208,184\nMaaa 28,200,184\nMaaa 28,204,188\nD0 9,24,44,3072\nMaaa 24,196,188\nMaaa 24,192,184\ns_waitcnt vmcnt(18) lgkmcnt(0)\ns_barrier\n"
"Maaa 32,192,224\nTa 160,12,0\nMaaa 32,196,228\nTa 162,12,2048\nMaaa 36,204,228\ns_add_u32 m0, s47, -16384\ns_add_u32 s48, s48, 128\nMaaa 36,200,224\nTa 164,12,16384\n"
"Maaa 96,208,224\nTa 166,12,18432\nbuffer_load_dword v31, v24, s[24:27], s96 offen\ns_add_u32 s96, s96, 0x80000\nMaaa 96,212,228\nD0 2,20,48,0\nMaaa 100,220,228\nTa 168,13,0\n"
"Maaa 100,216,224\nTa 170,13,2048\nMaaa 108,216,232\nMaaa 108,220,236\nTa 172,13,16384\nMaaa 104,212,236\nTa 174,13,18432\nMaaa 104,208,232\nMaaa 44,200,232\nTa 176,14,0\n"
"Maaa 44,204,236\nTa 178,14,2048\nMaaa 40,196,236\nD0 3,20,48,1024\nMaaa 40,192,232\nTa 180,14,16384\nMaaa 48,192,240\nTa 182,14,18432\nMaaa 48,196,244\nMaaa 52,204,244\n"
"Ta 184,15,0\nMaaa 52,200,240\nTa 186,15,2048\nMaaa 112,208,240\nMaaa 112,212,244\nTa 188,15,16384\nMaaa 116,220,244\nTa 190,15,18432\nMaaa 116,216,240\nD0 4,20,48,2048\n"
"Maaa 124,216,248\nMaaa 124,220,252\nMaaa 120,212,252\nMaaa 120,208,248\nMaaa 60,200,248\nMaaa 60,204,252\nD0 5,20,48,3072\nMaaa 56,196,252\nMaaa 56,192,248\nWL 0\nMvaa 64,128,160\n"
"Ta 224,16,0\nWV 12\nv_cmp_ne_u32 vcc, s15, v23\ns_nop 0\ns_cbranch_vccnz .S407\n.S408:\nMvaa 64,132,164\nTa 226,16,2048\nMvaa 68,140,164\ns_add_u32 m0, s47, -65536\n"
"s_add_u32 s45, s45, 128\nMvaa 68,136,160\nTa 228,16,16384\nMvaa 128,144,160\nTa 230,16,18432\nMvaa 128,148,164\nD0 2,20,45,0\nMvaa 132,156,164\nTa 232,17,0\nMvaa 132,152,160\n"
"Ta 234,17,2048\nMvaa 140,152,168\nv_add_u32_dpp v22, v22, v22 row_ror:8 row_mask:0xf bank_mask:0xf\nMvaa 140,156,172\nTa 236,17,16384\nMvaa 136,148,172\nTa 238,17,18432\n"
"Mvaa 136,144,168\nv_add_u32_dpp v22, v22, v22 row_ror:4 row_mask:0xf bank_mask:0xf\nMvaa 76,136,168\nTa 240,18,0\nMvaa 76,140,172\nTa 242,18,2048\nMvaa 72,132,172\n"
"D0 3,20,45,1024\nMvaa 72,128,168\nTa 244,18,16384\nMvaa 80,128,176\nTa 246,18,18432\nMvaa 80,132,180\nv_add_u32_dpp v22, v22, v22 row_ror:2 row_mask:0xf bank_mask:0xf\n"
"Mvaa 84,140,180\nTa 248,19,0\nMvaa 84,136,176\nTa 250,19,2048\nMvaa 144,144,176\nv_add_u32_dpp v22, v22, v22 row_ror:1 row_mask:0xf bank_mask:0xf\nMvaa 144,148,180\n"
"Ta 252,19,16384\nMvaa 148,156,180\nTa 254,19,18432\n.W577:\ns_cbranch_execz .W581\nMvaa 148,152,176\nD0 4,20,45,2048\nMvaa 156,152,184\nv_mov_b32 v23, v22\nMvaa 156,156,188\n"
"Mvaa 152,148,188\nv_permlane16_swap_b32 v22, v23\nMvaa 152,144,184\nv_add_u32 v22, v22, v23\nMvaa 92,136,184\nv_mov_b32 v23, v22\nMvaa 92,140,188\nD0 5,20,45,3072\n"
"Mvaa 88,132,188\nv_permlane32_swap_b32 v22, v23\nMvaa 88,128,184\nWL 0\ns_barrier\nMvaa 96,128,224\nRa 192,10,16384\nMvaa 96,132,228\ns_add_u32 m0, s47, 0\n"
"s_add_u32 s44, s44, 0x40000\nMvaa 100,140,228\nRa 196,11,16384\nMvaa 100,136,224\nv_add_u32 v22, v22, v23\nMvaa 160,144,224\nRa 200,10,18432\nMvaa 160,148,228\nD0 6,24,44,0\n"
"Mvaa 164,156,228\nRa 204,11,18432\nMvaa 164,152,224\nv_readfirstlane_b32 s4, v22\nMvaa 172,152,232\nRa 208,10,24576\nMvaa 172,156,236\nMvaa 168,148,236\nRa 212,11,24576\n"
"s_cmp_gt_i32 s4, 0\ns_cselect_b32 s4, 1, 0\nMvaa 168,144,232\ns_xor_b32 s4, s4, s12\nMvaa 108,136,232\nRa 216,10,26624\nMvaa 108,140,236\nD0 7,24,44,1024\nMvaa 104,132,236\n"
"Ra 220,11,26624\nMvaa 104,128,232\ns_and_b32 s4, s4, 1\nMvaa 112,128,240\ns_lshl_b32 s4, s4, 13\nMvaa 112,132,244\ns_mul_i32 s72, s73, 25165824\nMvaa 116,140,244\n"
"s_add_u32 s72, s72, s4\nMvaa 116,136,240\ns_and_b32 s4, s12, 1\nMvaa 176,144,240\ns_lshl_b32 s4, s4, 21\nMvaa 176,148,244\nD0 8,24,44,2048\nMvaa 180,156,244\n"
"s_add_u32 s72, s72, s4\nMvaa 180,152,240\ns_lshl3_add_u32 s2, s13, s11\nMvaa 188,152,248\ns_lshl_b32 s2, s2, 11\nMvaa 188,156,252\ns_lshl_b32 s3, s14, 22\nMvaa 184,148,252\n"
"s_add_u32 s2, s2, s3\nMvaa 184,144,248\ns_add_u32 s2, s2, s72\nMvaa 124,136,248\ns_add_u32 s49, s2, 0xbe0000\nMvaa 124,140,252\nD0 9,24,44,3072\nMvaa 120,132,252\n"
"Mvaa 120,128,248\nWL 0\nMaaa 32,192,224\nRa 128,10,32768\ns_mov_b32 s4, s13\nMaaa 32,196,228\ns_add_u32 m0, s47, 16384\ns_add_u32 s44, s44, 0x40000\nMaaa 36,204,228\n"
"Ra 132,11,32768\nv_lshrrev_b32 v32, 2, v0\nMaaa 36,200,224\nv_mul_u32_u24 v32, 896, v32\nMaaa 96,208,224\nRa 136,10,34816\nv_and_b32 v33, 3, v0\nMaaa 96,212,228\nD0 6,24,44,0\n"
"Maaa 100,220,228\nRa 140,11,34816\nv_lshl_add_u32 v32, v33, 4, v32\nMaaa 100,216,224\ns_lshr_b32 s2, s11, 1\nMaaa 108,216,232\nRa 144,10,40960\ns_mul_i32 s3, s2, 448\n"
"Maaa 108,220,236\ns_and_b32 s5, s11, 1\nMaaa 104,212,236\nRa 148,11,40960\ns_lshl3_add_u32 s3, s5, s3\nMaaa 104,208,232\ns_add_u32 s3, s3, 131072\nMaaa 44,200,232\n"
"Ra 152,10,43008\nv_add_u32 v32, s3, v32\nMaaa 44,204,236\nD0 7,24,44,1024\nMaaa 40,196,236\nRa 156,11,43008\nTv 36,32,0\nMaaa 40,192,232\nTv 38,32,14336\nMaaa 48,192,240\n"
"Tv 40,32,64\nTv 42,32,14400\nMaaa 48,196,244\nTv 44,32,128\nMaaa 52,204,244\nTv 46,32,14464\nTv 48,32,192\nMaaa 52,200,240\nTv 50,32,14528\nMaaa 112,208,240\nTv 52,32,256\n"
"Tv 54,32,14592\nMaaa 112,212,244\nD0 8,24,44,2048\nMaaa 116,220,244\nTv 56,32,320\nTv 58,32,14656\nMaaa 116,216,240\nTv 60,32,384\nMaaa 124,216,248\nTv 62,32,14720\n"
"s_lshr_b32 s0, s4, 1\nMaaa 124,220,252\ns_mul_i32 s0, s0, 12\nMaaa 120,212,252\ns_lshl1_add_u32 s0, s5, s0\ns_and_b32 s6, s4, 1\nMaaa 120,208,248\ns_add_u32 s0, s0, s6\n"
"Maaa 60,200,248\ns_lshl3_add_u32 s1, s5, s4\ns_add_u32 s77, s1, 48\nMaaa 60,204,252\nD0 9,24,44,3072\nMaaa 56,196,252\ns_and_b32 s4, s12, 1\ns_mov_b32 s7, 0x6543210\n"
"Maaa 56,192,248\ns_waitcnt vmcnt(17) lgkmcnt(14)\ns_barrier\nMaaa 0,192,160\nTa 224,16,32768\ns_cmp_lg_u32 s4, s2\ns_cselect_b32 s6, s0, s1\ns_cselect_b32 s5, 4, 16\n"
"s_cselect_b32 s7, 0x6352410, s7\ns_cselect_b32 s3, 0, 229376\nMaaa 0,196,164\nTa 226,16,34816\nMaaa 4,204,164\ns_add_u32 m0, s47, -49152\ns_add_u32 s48, s48, 128\nMaaa 4,200,160\n"
"Ta 228,16,49152\nMaaa 64,208,160\nTa 230,16,51200\ns_add_u32 s3, s3, s72\nMaaa 64,212,164\nD0 2,20,48,0\nMaaa 68,220,164\nTa 232,17,32768\ns_add_u32 s3, s3, 0x1400000\n"
"Maaa 68,216,160\nTa 234,17,34816\nMaaa 76,216,168\nWL 14\ns_lshr_b32 s0, s6, 4\nMaaa 76,220,172\nTa 236,17,49152\nMaaa 72,212,172\nTa 238,17,51200\ns_lshl_b32 s0, s0, 2\n"
"Maaa 72,208,168\ns_lshr_b32 s0, s7, s0\nMaaa 12,200,168\nTa 240,18,32768\ns_and_b32 s0, s0, 7\nMaaa 12,204,172\nTa 242,18,34816\nMaaa 8,196,172\nD0 3,20,48,1024\n"
"s_lshl_b32 s0, s0, 5\nMaaa 8,192,168\nTa 244,18,49152\nMaaa 16,192,176\nTa 246,18,51200\ns_and_b32 s1, s6, 15\nMaaa 16,196,180\ns_add_u32 s0, s0, s1\nMaaa 20,204,180\n"
"Ta 248,19,32768\ns_and_b32 s1, s6, 8\nMaaa 20,200,176\nTa 250,19,34816\nMaaa 80,208,176\ns_add_u32 s0, s0, s1\ns_lshl_b32 s0, s0, 10\nMaaa 80,212,180\nTa 252,19,49152\n"
"Maaa 84,220,180\nTa 254,19,51200\ns_add_u32 s0, s0, s3\nMaaa 84,216,176\nD0 4,20,48,2048\nMaaa 92,216,184\nSv 36,1,16,0,0\ns_add_u32 s6, s6, s5\nMaaa 92,220,188\nMaaa 88,212,188\n"
"Maaa 88,208,184\nMaaa 28,200,184\nMaaa 28,204,188\nD0 5,20,48,3072\nMaaa 24,196,188\nMaaa 24,192,184\nWL 0\nMvaa 96,128,224\nTa 160,12,32768\ns_lshr_b32 s0, s6, 4\n"
"Mvaa 96,132,228\nTa 162,12,34816\nMvaa 100,140,228\ns_add_u32 m0, s47, -32768\ns_add_u32 s45, s45, 128\nMvaa 100,136,224\nTa 164,12,49152\nMvaa 160,144,224\nTa 166,12,51200\n"
"s_lshl_b32 s0, s0, 2\nMvaa 160,148,228\nD0 2,20,45,0\nMvaa 164,156,228\nTa 168,13,32768\ns_lshr_b32 s0, s7, s0\nMvaa 164,152,224\nTa 170,13,34816\nMvaa 172,152,232\n"
"s_and_b32 s0, s0, 7\ns_lshl_b32 s0, s0, 5\nMvaa 172,156,236\nTa 172,13,49152\nMvaa 168,148,236\nTa 174,13,51200\ns_and_b32 s1, s6, 15\nMvaa 168,144,232\ns_add_u32 s0, s0, s1\n"
"Mvaa 108,136,232\nTa 176,14,32768\ns_and_b32 s1, s6, 8\nMvaa 108,140,236\nTa 178,14,34816\nMvaa 104,132,236\nD0 3,20,45,1024\ns_add_u32 s0, s0, s1\nMvaa 104,128,232\n"
"Ta 180,14,49152\nMvaa 112,128,240\nTa 182,14,51200\ns_lshl_b32 s0, s0, 10\nMvaa 112,132,244\ns_add_u32 s0, s0, s3\nMvaa 116,140,244\nTa 184,15,32768\nSv 40,1,16,0,0\n"
"Mvaa 116,136,240\nTa 186,15,34816\nMvaa 176,144,240\ns_add_u32 s6, s6, s5\nMvaa 176,148,244\nTa 188,15,49152\nMvaa 180,156,244\nTa 190,15,51200\nMvaa 180,152,240\nD0 4,20,45,2048\n"
"Mvaa 188,152,248\nMvaa 188,156,252\nMvaa 184,148,252\nMvaa 184,144,248\nMvaa 124,136,248\ns_lshr_b32 s0, s6, 4\ns_lshl_b32 s0, s0, 2\nMvaa 124,140,252\nD0 5,20,45,3072\n"
"Mvaa 120,132,252\ns_lshr_b32 s0, s7, s0\ns_and_b32 s0, s0, 7\nMvaa 120,128,248\ns_waitcnt vmcnt(18) lgkmcnt(0)\ns_barrier\nMvaa 64,128,160\nRa 192,10,49152\ns_lshl_b32 s0, s0, 5\n"
"Mvaa 64,132,164\ns_add_u32 m0, s47, 32768\ns_add_u32 s44, s44, 0x40000\nMvaa 68,140,164\nRa 196,11,49152\ns_and_b32 s1, s6, 15\nMvaa 68,136,160\ns_add_u32 s0, s0, s1\n"
"Mvaa 128,144,160\nRa 200,10,51200\ns_and_b32 s1, s6, 8\nMvaa 128,148,164\nD0 6,24,44,0\nMvaa 132,156,164\nRa 204,11,51200\ns_add_u32 s0, s0, s1\nMvaa 132,152,160\n"
"s_lshl_b32 s0, s0, 10\nMvaa 140,152,168\nRa 208,10,57344\ns_add_u32 s0, s0, s3\nMvaa 140,156,172\nSv 44,1,16,0,0\nMvaa 136,148,172\nRa 212,11,57344\ns_add_u32 s6, s6, s5\n"
"Mvaa 136,144,168\nMvaa 76,136,168\nRa 216,10,59392\nMvaa 76,140,172\nD0 7,24,44,1024\nMvaa 72,132,172\nRa 220,11,59392\nMvaa 72,128,168\nMvaa 80,128,176\nMvaa 80,132,180\n"
"Mvaa 84,140,180\ns_mov_b32 s6, s77\ns_mov_b32 s5, 16\nMvaa 84,136,176\ns_lshr_b32 s0, s6, 4\nMvaa 144,144,176\ns_lshl_b32 s0, s0, 2\ns_lshr_b32 s0, s7, s0\nMvaa 144,148,180\n"
"D0 8,24,44,2048\nMvaa 148,156,180\ns_and_b32 s0, s0, 7\ns_lshl_b32 s0, s0, 5\nMvaa 148,152,176\ns_and_b32 s1, s6, 15\nMvaa 156,152,184\ns_add_u32 s0, s0, s1\ns_and_b32 s1, s6, 8\n"
"Mvaa 156,156,188\ns_add_u32 s0, s0, s1\nMvaa 152,148,188\ns_lshl_b32 s0, s0, 10\ns_add_u32 s0, s0, s3\nMvaa 152,144,184\nSv 48,1,16,0,0\nMvaa 92,136,184\ns_add_u32 s6, s6, s5\n"
"Mvaa 92,140,188\nD0 9,24,44,3072\nMvaa 88,132,188\nMvaa 88,128,184\nWL 0\nMaaa 0,192,160\nRa 128,10,0\nMaaa 0,196,164\ns_add_u32 m0, s47, 49152\ns_add_u32 s44, s44, 0x40000\n"
"Maaa 4,204,164\nRa 132,11,0\nMaaa 4,200,160\nMaaa 64,208,160\nRa 136,10,2048\ns_lshr_b32 s0, s6, 4\nMaaa 64,212,164\nD0 6,24,44,0\nMaaa 68,220,164\nRa 140,11,2048\n"
"s_lshl_b32 s0, s0, 2\nMaaa 68,216,160\ns_lshr_b32 s0, s7, s0\nMaaa 76,216,168\nRa 144,10,8192\ns_and_b32 s0, s0, 7\nMaaa 76,220,172\ns_lshl_b32 s0, s0, 5\nMaaa 72,212,172\n"
"Ra 148,11,8192\ns_and_b32 s1, s6, 15\nMaaa 72,208,168\ns_add_u32 s0, s0, s1\nMaaa 12,200,168\nRa 152,10,10240\ns_and_b32 s1, s6, 8\nMaaa 12,204,172\nD0 7,24,44,1024\n"
"Maaa 8,196,172\nRa 156,11,10240\ns_add_u32 s0, s0, s1\nMaaa 8,192,168\ns_lshl_b32 s0, s0, 10\nMaaa 16,192,176\ns_add_u32 s0, s0, s3\nSv 52,1,16,0,0\nMaaa 16,196,180\n"
"s_add_u32 s6, s6, s5\nMaaa 20,204,180\nMaaa 20,200,176\nMaaa 80,208,176\nMaaa 80,212,180\nD0 8,24,44,2048\nMaaa 84,220,180\nMaaa 84,216,176\nMaaa 92,216,184\nMaaa 92,220,188\n"
"s_lshr_b32 s0, s6, 4\nMaaa 88,212,188\ns_lshl_b32 s0, s0, 2\ns_lshr_b32 s0, s7, s0\nMaaa 88,208,184\ns_and_b32 s0, s0, 7\nMaaa 28,200,184\ns_lshl_b32 s0, s0, 5\n"
"s_and_b32 s1, s6, 15\nMaaa 28,204,188\nD0 9,24,44,3072\nMaaa 24,196,188\ns_add_u32 s0, s0, s1\ns_and_b32 s1, s6, 8\nMaaa 24,192,184\nWV 21\ns_barrier\nMaaa 32,192,224\n"
"Ta 160,12,0\ns_add_u32 s0, s0, s1\nMaaa 32,196,228\nTa 162,12,2048\nMaaa 36,204,228\ns_add_u32 m0, s47, -16384\ns_add_u32 s48, s48, 128\nMaaa 36,200,224\nTa 164,12,16384\n"
"Maaa 96,208,224\nTa 166,12,18432\ns_lshl_b32 s0, s0, 10\nMaaa 96,212,228\nD0 2,20,48,0\nMaaa 100,220,228\nTa 168,13,0\ns_add_u32 s0, s0, s3\nMaaa 100,216,224\nTa 170,13,2048\n"
"Maaa 108,216,232\nSv 56,1,16,0,0\ns_add_u32 s6, s6, s5\nMaaa 108,220,236\nTa 172,13,16384\nMaaa 104,212,236\nTa 174,13,18432\nMaaa 104,208,232\nMaaa 44,200,232\nTa 176,14,0\n"
"Maaa 44,204,236\nTa 178,14,2048\nMaaa 40,196,236\nD0 3,20,48,1024\nMaaa 40,192,232\nTa 180,14,16384\nMaaa 48,192,240\nTa 182,14,18432\ns_lshr_b32 s0, s6, 4\nMaaa 48,196,244\n"
"s_lshl_b32 s0, s0, 2\nMaaa 52,204,244\nTa 184,15,0\ns_lshr_b32 s0, s7, s0\nMaaa 52,200,240\nTa 186,15,2048\nMaaa 112,208,240\ns_and_b32 s0, s0, 7\ns_lshl_b32 s0, s0, 5\n"
"Maaa 112,212,244\nTa 188,15,16384\nMaaa 116,220,244\nTa 190,15,18432\ns_and_b32 s1, s6, 15\nMaaa 116,216,240\nD0 4,20,48,2048\nMaaa 124,216,248\ns_add_u32 s0, s0, s1\n"
"s_and_b32 s1, s6, 8\nMaaa 124,220,252\ns_add_u32 s0, s0, s1\nMaaa 120,212,252\ns_lshl_b32 s0, s0, 10\ns_add_u32 s0, s0, s3\nMaaa 120,208,248\nSv 60,1,16,0,0\nMaaa 60,200,248\n"
"s_add_u32 s6, s6, s5\nMaaa 60,204,252\nD0 5,20,48,3072\nMaaa 56,196,252\nMaaa 56,192,248\nWL 0\nMvaa 64,128,160\nTa 224,16,0\nMvaa 64,132,164\nTa 226,16,2048\n"
"s_add_u32 m0, s47, 65536\ns_add_u32 s46, s51, 0\nMvaa 68,140,164\nv_lshrrev_b32 v48, 4, v0\nMvaa 68,136,160\nTa 228,16,16384\nD0 6,32,46,0\nMvaa 128,144,160\nTa 230,16,18432\n"
"Mvaa 128,148,164\nv_and_b32 v49, 7, v0\nMvaa 132,156,164\nTa 232,17,0\nMvaa 132,152,160\nTa 234,17,2048\nMvaa 140,152,168\nv_xor_b32 v49, v49, v48\nD0 7,32,46,1024\n"
"Mvaa 140,156,172\nTa 236,17,16384\nMvaa 136,148,172\nTa 238,17,18432\nMvaa 136,144,168\nv_lshlrev_b32 v49, 4, v49\nMvaa 76,136,168\nTa 240,18,0\nMvaa 76,140,172\nTa 242,18,2048\n"
"D0 8,32,46,2048\nMvaa 72,132,172\nv_lshrrev_b32 v48, 3, v0\nMvaa 72,128,168\nTa 244,18,16384\nMvaa 80,128,176\nTa 246,18,18432\nMvaa 80,132,180\nv_mul_u32_u24 v48, 896, v48\n"
"Mvaa 84,140,180\nTa 248,19,0\nD0 9,32,46,3072\nMvaa 84,136,176\nTa 250,19,2048\nMvaa 144,144,176\nv_add_u32 v40, v48, v49\nMvaa 144,148,180\nTa 252,19,16384\n"
"s_add_u32 m0, s47, 81920\ns_add_u32 s46, s51, 262144\nMvaa 148,156,180\nTa 254,19,18432\nMvaa 148,152,176\nv_xor_b32 v49, 64, v40\nD0 6,32,46,0\nMvaa 156,152,184\n"
"v_add_u32 v41, 6144, v49\n.W578:\ns_cbranch_execz .W582\nMvaa 156,156,188\nv_add_u32 v42, 55296, v40\nMvaa 152,148,188\nv_add_u32 v43, 61440, v49\nMvaa 152,144,184\n"
"s_mov_b32 s2, s13\nMvaa 92,136,184\ns_lshl3_add_u32 s2, s2, s11\nD0 7,32,46,1024\nMvaa 92,140,188\ns_mul_i32 s52, s2, 14336\nMvaa 88,132,188\ns_lshr_b32 s2, s13, 2\n"
"s_bfe_u32 s3, s13, 0x10001\nMvaa 88,128,184\ns_waitcnt vmcnt(23) lgkmcnt(0)\ns_barrier\nMvaa 96,128,224\nRa 192,10,16384\ns_mul_i32 s3, s3, 17\nMvaa 96,132,228\n"
"s_add_u32 s2, s2, s3\nD0 8,32,46,2048\nMvaa 100,140,228\nRa 196,11,16384\ns_lshl2_add_u32 s3, s14, 0\nMvaa 100,136,224\ns_lshl_b32 s3, s3, 2\nMvaa 160,144,224\nRa 200,10,18432\n"
"s_lshr_b32 s3, 0xe420ca86, s3\nMvaa 160,148,228\ns_and_b32 s3, s3, 15\nMvaa 164,156,228\nRa 204,11,18432\ns_add_u32 s2, s2, s3\nD0 9,32,46,3072\nMvaa 164,152,224\n"
"s_cmp_eq_u32 s2, 32\ns_cselect_b32 s4, 16, s2\nMvaa 172,152,232\nRa 208,10,24576\ns_mul_i32 s75, s4, 65536\nMvaa 172,156,236\ns_lshr_b32 s2, s13, 2\ns_add_u32 m0, s47, -65536\n"
"s_add_u32 s46, s52, 0\nMvaa 168,148,236\nRa 212,11,24576\ns_bfe_u32 s3, s13, 0x10001\nMvaa 168,144,232\ns_mul_i32 s3, s3, 17\nD0 40,28,46,0\nMvaa 108,136,232\nRa 216,10,26624\n"
"s_add_u32 s2, s2, s3\nMvaa 108,140,236\ns_lshl2_add_u32 s3, s14, 1\nMvaa 104,132,236\nRa 220,11,26624\ns_lshl_b32 s3, s3, 2\nMvaa 104,128,232\ns_lshr_b32 s3, 0xe420ca86, s3\n"
"Mvaa 112,128,240\ns_and_b32 s3, s3, 15\ns_add_u32 s2, s2, s3\nD0 41,28,46,1024\nMvaa 112,132,244\ns_cmp_eq_u32 s2, 32\ns_cselect_b32 s4, 16, s2\nMvaa 116,140,244\n"
"s_mul_i32 s76, s4, 65536\ns_lshr_b32 s2, s13, 2\nMvaa 116,136,240\ns_bfe_u32 s3, s13, 0x10001\nMvaa 176,144,240\ns_mul_i32 s3, s3, 17\ns_add_u32 s2, s2, s3\nMvaa 176,148,244\n"
"s_lshl2_add_u32 s3, s14, 2\nD0 42,28,46,2048\nMvaa 180,156,244\ns_lshl_b32 s3, s3, 2\ns_lshr_b32 s3, 0xe420ca86, s3\nMvaa 180,152,240\ns_and_b32 s3, s3, 15\nMvaa 188,152,248\n"
"s_add_u32 s2, s2, s3\nMvaa 188,156,252\ns_cmp_eq_u32 s2, 32\ns_cselect_b32 s4, 16, s2\nMvaa 184,148,252\ns_mul_i32 s77, s4, 65536\ns_lshr_b32 s2, s13, 2\nD0 43,28,46,3072\n"
"Mvaa 184,144,248\ns_bfe_u32 s3, s13, 0x10001\nMvaa 124,136,248\ns_mul_i32 s3, s3, 17\ns_add_u32 s2, s2, s3\nMvaa 124,140,252\ns_lshl2_add_u32 s3, s14, 3\nMvaa 120,132,252\n"
"s_lshl_b32 s3, s3, 2\ns_lshr_b32 s3, 0xe420ca86, s3\nMvaa 120,128,248\nWL 0\nMaaa 32,192,224\nRa 128,10,32768\ns_and_b32 s3, s3, 15\ns_add_u32 m0, s47, 0\n"
"s_add_u32 s46, s51, 524288\nD0 6,32,46,0\nMaaa 32,196,228\ns_add_u32 s2, s2, s3\ns_cmp_eq_u32 s2, 32\ns_cselect_b32 s4, 16, s2\nMaaa 36,204,228\nRa 132,11,32768\n"
"s_mul_i32 s78, s4, 65536\nMaaa 36,200,224\ns_lshr_b32 s3, s11, 1\nMaaa 96,208,224\nRa 136,10,34816\ns_lshl_b32 s3, s3, 15\nMaaa 96,212,228\ns_add_u32 s2, s72, s3\nD0 7,32,46,1024\n"
"Maaa 100,220,228\nRa 140,11,34816\ns_and_b32 s3, s11, 1\nMaaa 100,216,224\ns_lshl_b32 s3, s3, 14\nMaaa 108,216,232\nRa 144,10,40960\ns_add_u32 s2, s2, s3\nMaaa 108,220,236\n"
"s_add_u32 s2, s2, 0x400000\nMaaa 104,212,236\nRa 148,11,40960\ns_mov_b32 s55, 4096\nD0 8,32,46,2048\nMaaa 104,208,232\ns_add_u32 s75, s75, s2\nMaaa 44,200,232\nRa 152,10,43008\n"
"s_add_u32 s76, s76, s2\nMaaa 44,204,236\ns_add_u32 s77, s77, s2\nMaaa 40,196,236\nRa 156,11,43008\ns_add_u32 s78, s78, s2\nMaaa 40,192,232\nv_add_u32 v32, 0x10000, v12\n"
"D0 9,32,46,3072\nMaaa 48,192,240\nv_add_u32 v33, 0x10000, v13\nv_add_u32 v34, 0x10000, v14\nMaaa 48,196,244\nv_add_u32 v35, 0x10000, v15\nMaaa 52,204,244\n"
"v_add_u32 v36, 0x10000, v16\nv_add_u32 v37, 0x10000, v17\ns_add_u32 m0, s47, 16384\ns_add_u32 s46, s51, 786432\nMaaa 52,200,240\nv_add_u32 v38, 0x10000, v18\nMaaa 112,208,240\n"
"v_add_u32 v39, 0x10000, v19\ns_add_u32 s2, s11, 1\nD0 6,32,46,0\nMaaa 112,212,244\ns_lshr_b32 s2, s2, 1\nMaaa 116,220,244\ns_lshl_b32 s2, s2, 12\nv_add_u32 v44, s2, v10\n"
"Maaa 116,216,240\nv_add_u32 v45, s2, v11\nMaaa 124,216,248\nv_xor_b32 v46, 0x1000, v44\nv_xor_b32 v47, 0x1000, v45\nMaaa 124,220,252\ns_lshr_b32 s3, s11, 1\nD0 7,32,46,1024\n"
"Maaa 120,212,252\ns_lshl_b32 s3, s3, 13\ns_lshl_b32 s2, s11, 12\nMaaa 120,208,248\ns_add_u32 s2, s2, s3\nMaaa 60,200,248\ns_add_u32 s2, s2, s72\ns_lshl_b32 s3, s11, 12\n"
"Maaa 60,204,252\ns_add_u32 s89, s3, 0x10000\nMaaa 56,196,252\ns_mov_b32 s88, 0xffff8000\ns_lshl_b32 s4, s13, 1\nD0 8,32,46,2048\nMaaa 56,192,248\ns_waitcnt vmcnt(25) lgkmcnt(0)\n"
"s_barrier\nMaaa 0,192,160\nTa 224,16,32768\ns_sub_u32 s79, 15, s4\nMaaa 0,196,164\nTa 226,16,34816\nMaaa 4,204,164\ns_add_u32 s96, s2, 0x1310000\ns_mov_b32 s63, 0x53800000\n"
"Maaa 4,200,160\nTa 228,16,49152\nD0 9,32,46,3072\nMaaa 64,208,160\nTa 230,16,51200\ns_mov_b32 s64, 0x2b800000\nMaaa 64,212,164\ns_sub_u32 s98, s4, 1\nMaaa 68,220,164\n"
"Ta 232,17,32768\ns_lshl_b32 s5, s13, 5\ns_add_u32 m0, s47, -32768\ns_add_u32 s46, s52, 128\nMaaa 68,216,160\nTa 234,17,34816\nMaaa 76,216,168\ns_add_u32 s5, s5, s11\n"
"s_lshl_b32 s65, s5, 9\nD0 40,28,46,0\nMaaa 76,220,172\nTa 236,17,49152\nMaaa 72,212,172\nTa 238,17,51200\ns_add_u32 s65, s65, s72\nMaaa 72,208,168\ns_add_u32 s65, s65, 0x1580000\n"
"Maaa 12,200,168\nTa 240,18,32768\ns_and_b32 s66, s72, 0x2000\nMaaa 12,204,172\nTa 242,18,34816\nD0 41,28,46,1024\nMaaa 8,196,172\ns_add_u32 s66, s66, s56\n"
"s_add_u32 s66, s66, 0x100000\nMaaa 8,192,168\nTa 244,18,49152\nMaaa 16,192,176\nTa 246,18,51200\ns_and_b32 s5, s12, 1\nMaaa 16,196,180\ns_mul_i32 s58, s5, 0x202000\n"
"Maaa 20,204,180\nTa 248,19,32768\ns_xor_b32 s58, s58, s72\nD0 42,28,46,2048\nMaaa 20,200,176\nTa 250,19,34816\nMaaa 80,208,176\ns_add_u32 s58, s58, 0x15c0000\n"
"s_lshl_b32 s5, s5, 8\nMaaa 80,212,180\nTa 252,19,49152\nMaaa 84,220,180\nTa 254,19,51200\ns_add_u32 s61, s58, s5\nMaaa 84,216,176\ns_lshl4_add_u32 s60, s13, s61\nD0 43,28,46,3072\n"
"Maaa 92,216,184\ns_add_u32 s54, s60, 128\ns_xor_b32 s60, s54, 0x100\nMaaa 92,220,188\ns_cmp_eq_u32 s11, 0\ns_cselect_b32 s54, s54, 0x40000000\nMaaa 88,212,188\ns_getpc_b64 s[8:9]\n"
".PC420:\ns_add_u32 s8, s8, .HD406-.PC420\ns_addc_u32 s9, s9, 0\ns_add_u32 m0, s47, -49152\ns_add_u32 s46, s52, 256\nMaaa 88,208,184\ns_mov_b32 s97, 0xff800000\nMaaa 28,200,184\n"
"s_mov_b32 s90, 0\nD0 40,28,46,0\nMaaa 28,204,188\ns_cmp_lt_u32 s11, 2\ns_cselect_b32 s92, 2, 0\nMaaa 24,196,188\ns_mul_i32 s4, s79, 65536\ns_sub_u32 s4, s2, s4\nMaaa 24,192,184\n"
"WL 0\nMvaa 96,128,224\nTa 160,12,32768\ns_add_u32 s94, s4, 0x20000\nMvaa 96,132,228\nTa 162,12,34816\nD0 41,28,46,1024\nMvaa 100,140,228\ns_add_u32 s95, s4, 0x410000\n"
"s_add_u32 s86, s2, 0x130000\nMvaa 100,136,224\nTa 164,12,49152\nMvaa 108,136,232\nTa 166,12,51200\ns_add_u32 s87, s2, 0x520000\nMvaa 108,140,236\ns_sub_u32 s99, s79, 2\n"
"s_cmp_eq_u32 s13, 7\ns_cselect_b32 s93, 1.0, 0\nMvaa 104,132,236\nTa 168,13,32768\ns_mov_b32 s36, s68\nD0 42,28,46,2048\nMvaa 104,128,232\nTa 170,13,34816\nMvaa 112,128,240\n"
"s_and_b32 s37, s69, 0xffff\ns_mov_b32 s38, 0x1c0000\nMvaa 112,132,244\nTa 172,13,49152\nCV 208,96,97\nMvaa 116,140,244\nTa 174,13,51200\ns_mov_b32 s39, 0x20000\nMvaa 116,136,240\n"
"s_and_b32 s2, s74, 15\nD0 43,28,46,3072\nMvaa 124,136,248\nTa 176,14,32768\ns_min_u32 s2, s2, 13\nMvaa 124,140,252\nTa 178,14,34816\nMvaa 120,132,252\ns_sub_u32 s2, s2, 0\n"
"s_mul_i32 s3, s73, 14\nMvaa 120,128,248\nTa 180,14,49152\nCV 209,98,99\nMvaa 160,144,224\nTa 182,14,51200\ns_add_u32 s2, s2, s3\nMvaa 160,148,228\ns_lshl2_add_u32 s2, s2, s11\n"
"Mvaa 164,156,228\nTa 184,15,32768\ns_mul_i32 s3, s2, 4682\nMvaa 164,152,224\nTa 186,15,34816\nCV 210,100,101\nMvaa 172,152,232\ns_lshr_b32 s3, s3, 16\ns_mul_i32 s22, s3, 14\n"
"Mvaa 172,156,236\nTa 188,15,49152\nCV 211,102,103\nMvaa 168,148,236\nTa 190,15,51200\ns_sub_u32 s22, s2, s22\nMvaa 168,144,232\ns_and_b32 s21, s3, 28\nMvaa 176,144,240\n"
"s_add_u32 s21, s21, s3\ns_lshl_b32 s21, s21, 4\nMvaa 176,148,244\ns_bfe_u32 s41, s3, 0x30002\ns_cmp_gt_u32 s22, 6\ns_cselect_b32 s2, 1179648, 0\ns_cselect_b32 s1, 16, 0\n"
"Mvaa 180,156,244\ns_add_u32 s41, s41, s1\ns_lshl4_add_u32 s41, s41, 0xc048480\nMvaa 180,152,240\ns_and_b32 s1, s3, 28\nCV 212,104,105\nMvaa 188,152,248\ns_add_u32 s3, s3, s1\n"
"s_lshl_b32 s3, s3, 11\nMvaa 188,156,252\ns_add_u32 s2, s2, s3\nMvaa 184,148,252\ns_lshl_b32 s3, s22, 17\ns_add_u32 s2, s2, s3\nMvaa 184,144,248\ns_waitcnt vmcnt(16) lgkmcnt(0)\n"
"s_barrier\nMvaa 64,128,160\nRa 192,10,49152\ns_add_u32 s20, s2, 0x10e0000\ns_add_u32 m0, s47, 32768\ns_add_u32 s46, s51, 1048576\nMvaa 64,132,164\ns_lshl_b32 s2, s22, 7\n"
"Mvaa 68,140,164\nRa 196,11,49152\ns_add_u32 s40, s2, 0xc04b800\nD0 6,32,46,0\nMvaa 68,136,160\ns_mul_i32 s2, s21, 896\nCV 213,106,107\nMvaa 76,136,168\nRa 200,10,51200\n"
"s_lshl_b32 s3, s22, 6\nMvaa 76,140,172\ns_add_u32 s22, s2, s3\nCV 214,108,109\nMvaa 72,132,172\nRa 204,11,51200\ns_add_u32 s23, s22, 57344\nMvaa 72,128,168\nD0 7,32,46,1024\n"
"Mvaa 80,128,176\nRa 208,10,57344\nCV 215,110,111\nMvaa 80,132,180\nCV 216,112,113\nCV 217,114,115\nMvaa 84,140,180\nRa 212,11,57344\nMvaa 84,136,176\nCV 218,116,117\n"
"CV 219,118,119\nMvaa 92,136,184\nRa 216,10,59392\nD0 8,32,46,2048\ns_lshl3_add_u32 s5, s12, s13\ns_lshl2_add_u32 s5, s5, s11\ns_lshl2_add_u32 s5, s5, 0xc04a400\n"
"buffer_load_dword v27, off, s[16:19], s5 sc0 sc1\nMvaa 92,140,188\nCV 220,120,121\nMvaa 88,132,188\nRa 220,11,59392\nCV 221,122,123\nMvaa 88,128,184\nCV 222,124,125\n"
"CV 223,126,127\nMvaa 128,144,160\nCV 192,64,65\nCV 193,66,67\nMvaa 128,148,164\nD0 9,32,46,3072\nMvaa 132,156,164\nCV 194,68,69\nCV 195,70,71\nMvaa 132,152,160\nCV 196,72,73\n"
"CV 197,74,75\nMvaa 140,152,168\ns_add_u32 m0, s47, 49152\ns_add_u32 s46, s51, 1310720\nMvaa 140,156,172\nCV 198,76,77\nCV 199,78,79\nMvaa 136,148,172\nD0 6,32,46,0\nCV 200,80,81\n"
"Mvaa 136,144,168\nCV 201,82,83\nMvaa 144,144,176\nCV 202,84,85\nCV 203,86,87\nMvaa 144,148,180\nCV 204,88,89\nCV 205,90,91\nMvaa 148,156,180\nCV 206,92,93\nMvaa 148,152,176\n"
"D0 7,32,46,1024\nCV 207,94,95\nMvaa 156,152,184\nCV 240,160,161\nMvaa 156,156,188\nCV 241,162,163\nSv 192,1,16,75,0\nMvaa 152,148,188\nCV 242,164,165\nMvaa 152,144,184\nWL 0\n"
"Maaa 0,192,160\nD0 8,32,46,2048\nRa 128,44,0\nMaaa 0,196,164\nCV 243,166,167\nMaaa 4,204,164\nRa 132,45,0\nMaaa 4,200,160\nCV 244,168,169\nMaaa 12,200,168\nRa 136,44,2048\n"
"Maaa 12,204,172\nD0 9,32,46,3072\nMaaa 8,196,172\nRa 140,45,2048\nSv 196,1,16,75,1024\nMaaa 8,192,168\nCV 245,170,171\nMaaa 16,192,176\nRa 144,46,0\nMaaa 16,196,180\n"
"CV 246,172,173\nMaaa 20,204,180\nRa 148,47,0\nMaaa 20,200,176\nCV 247,174,175\nMaaa 28,200,184\nRa 152,46,2048\nMaaa 28,204,188\nCV 248,176,177\nMaaa 24,196,188\nRa 156,47,2048\n"
"Maaa 24,192,184\nCV 249,178,179\nSv 200,1,16,75,2048\nMaaa 64,208,160\nCV 250,180,181\nCV 251,182,183\nMaaa 64,212,164\nCV 252,184,185\nMaaa 68,220,164\nCV 253,186,187\n"
"Maaa 68,216,160\nCV 254,188,189\nMaaa 76,216,168\nCV 255,190,191\nMaaa 76,220,172\nCV 224,128,129\nMaaa 72,212,172\nCV 225,130,131\nMaaa 72,208,168\nCV 226,132,133\n"
"Maaa 80,208,176\nCV 227,134,135\nSv 204,1,16,75,3072\nMaaa 80,212,180\nCV 228,136,137\nMaaa 84,220,180\nCV 229,138,139\nMaaa 84,216,176\nCV 230,140,141\nMaaa 92,216,184\n"
"CV 231,142,143\nMaaa 92,220,188\nCV 232,144,145\nMaaa 88,212,188\nCV 233,146,147\nMaaa 88,208,184\ns_barrier\nMaaa 32,192,224\ns_add_u32 m0, s47, -16384\ns_add_u32 s46, s52, 384\n"
"Ta 160,32,0\nMaaa 32,196,228\nTa 162,32,2048\nMaaa 36,204,228\nD0 40,28,46,0\ns_add_u32 s75, s75, s55\nSv 208,1,16,75,0\nMaaa 36,200,224\nTa 164,32,16384\nMaaa 44,200,232\n"
"Ta 166,32,18432\nMaaa 44,204,236\nCV 234,148,149\nMaaa 40,196,236\nTa 168,33,0\nMaaa 40,192,232\nD0 41,28,46,1024\nTa 170,33,2048\nMaaa 48,192,240\nCV 235,150,151\n"
"Maaa 48,196,244\n.W579:\ns_cbranch_execz .W583\nTa 172,33,16384\nMaaa 52,204,244\nTa 174,33,18432\nMaaa 52,200,240\nCV 236,152,153\nSv 212,1,16,75,1024\nMaaa 60,200,248\n"
"D0 42,28,46,2048\nTa 176,34,0\nMaaa 60,204,252\nTa 178,34,2048\nMaaa 56,196,252\nCV 237,154,155\nMaaa 56,192,248\nTa 180,34,16384\nMaaa 96,208,224\nTa 182,34,18432\n"
"CV 238,156,157\nMaaa 96,212,228\nD0 43,28,46,3072\nMaaa 100,220,228\nTa 184,35,0\nMaaa 100,216,224\nTa 186,35,2048\nMaaa 108,216,232\nCV 239,158,159\nSv 216,1,16,75,2048\n"
"Maaa 108,220,236\nTa 188,35,16384\nMaaa 104,212,236\nTa 190,35,18432\nMaaa 104,208,232\nMaaa 112,208,240\nMaaa 112,212,244\nMaaa 116,220,244\nMaaa 116,216,240\nMaaa 124,216,248\n"
"Maaa 124,220,252\nSv 220,1,16,75,3072\nMaaa 120,212,252\nMaaa 120,208,248\nWL 0\nZvaa 64,160,128\nTa 224,36,0\nAR 60,0\nMvaa 64,164,132\nTa 226,36,2048\nZvaa 96,164,140\nAR 61,1\n"
"Mvaa 96,160,136\nTa 228,36,16384\nCV 192,60,61\nZvaa 128,160,144\nTa 230,36,18432\nMvaa 128,164,148\nAR 60,2\nAR 61,3\nZvaa 160,164,156\nTa 232,37,0\nSv 224,1,16,76,0\n"
"Mvaa 160,160,152\nTa 234,37,2048\nCV 193,60,61\nZvaa 164,168,152\nAR 60,4\nMvaa 164,172,156\nTa 236,37,16384\nZvaa 132,172,148\nTa 238,37,18432\nAR 61,5\nMvaa 132,168,144\n"
"CV 194,60,61\nZvaa 100,168,136\nTa 240,38,0\nAR 60,6\nMvaa 100,172,140\nTa 242,38,2048\nZvaa 68,172,132\nSv 228,1,16,76,1024\nAR 61,7\nMvaa 68,168,128\nTa 244,38,16384\n"
"Zvaa 72,176,128\nTa 246,38,18432\nCV 195,60,61\nMvaa 72,180,132\nAR 60,8\nZvaa 104,180,140\nTa 248,39,0\nMvaa 104,176,136\nTa 250,39,2048\nAR 61,9\nZvaa 136,176,144\nCV 196,60,61\n"
"Mvaa 136,180,148\nTa 252,39,16384\nAR 60,10\nZvaa 168,180,156\nTa 254,39,18432\nSv 232,1,16,76,2048\nMvaa 168,176,152\nAR 61,11\nCV 197,60,61\nZvaa 172,184,152\nAR 60,12\n"
"Mvaa 172,188,156\nAR 61,13\nZvaa 140,188,148\nCV 198,60,61\nAR 60,14\nMvaa 140,184,144\nAR 61,15\nZvaa 108,184,136\nCV 199,60,61\nAR 60,16\nMvaa 108,188,140\nSv 236,1,16,76,3072\n"
"Zvaa 76,188,132\nAR 61,17\nCV 200,60,61\nMvaa 76,184,128\ns_waitcnt vmcnt(29) lgkmcnt(0)\ns_barrier\nZvaa 80,224,128\nRa 192,44,32768\nAR 60,18\nMvaa 80,228,132\nRa 196,45,32768\n"
"s_add_u32 m0, s47, 65536\ns_add_u32 s46, s51, 1572864\nD0 6,32,46,0\nZvaa 112,228,140\nRa 200,44,34816\nMvaa 112,224,136\nRa 204,45,34816\nAR 61,19\nZvaa 144,224,144\n"
"Ra 208,46,32768\nMvaa 144,228,148\nRa 212,47,32768\nCV 201,60,61\nD0 7,32,46,1024\nZvaa 176,228,156\nRa 216,46,34816\nMvaa 176,224,152\nRa 220,47,34816\nAR 60,20\n"
"Zvaa 180,232,152\nTa 160,12,0\nMvaa 180,236,156\nTa 162,12,2048\nD0 8,32,46,2048\nZvaa 148,236,148\nTa 164,12,16384\nAR 61,21\nMvaa 148,232,144\nTa 166,12,18432\nZvaa 116,232,136\n"
"Ta 168,13,0\nCV 202,60,61\nMvaa 116,236,140\nTa 170,13,2048\nD0 9,32,46,3072\nZvaa 84,236,132\nTa 172,13,16384\nAR 60,22\nMvaa 84,232,128\nTa 174,13,18432\n"
"s_add_u32 m0, s47, 81920\ns_add_u32 s46, s51, 1835008\nZvaa 88,240,128\nTa 176,14,0\nAR 61,23\nMvaa 88,244,132\nTa 178,14,2048\nD0 6,32,46,0\nZvaa 120,244,140\nTa 180,14,16384\n"
"Mvaa 120,240,136\nTa 182,14,18432\nCV 203,60,61\nZvaa 152,240,144\nTa 184,15,0\nMvaa 152,244,148\nTa 186,15,2048\nAR 60,24\nD0 7,32,46,1024\nZvaa 184,244,156\nTa 188,15,16384\n"
"Mvaa 184,240,152\nTa 190,15,18432\nAR 61,25\nZvaa 188,248,152\nCV 204,60,61\nMvaa 188,252,156\nAR 60,26\nD0 8,32,46,2048\nZvaa 156,252,148\nAR 61,27\nCV 205,60,61\n"
"Mvaa 156,248,144\nAR 60,28\nZvaa 124,248,136\nAR 61,29\nCV 206,60,61\nMvaa 124,252,140\nAR 60,30\nD0 9,32,46,3072\nZvaa 92,252,132\nAR 61,31\nCV 207,60,61\nMvaa 92,248,128\nWL 0\n"
"Mvaa 64,160,192\nTa 224,16,0\nAR 60,32\nMvaa 64,164,196\nTa 226,16,2048\ns_add_u32 m0, s47, -65536\ns_add_u32 s46, s52, 512\nD0 40,28,46,0\nMvaa 96,164,204\nAR 61,33\n"
"Mvaa 96,160,200\nTa 228,16,16384\nCV 208,60,61\nMvaa 128,160,208\nTa 230,16,18432\nMvaa 128,164,212\nAR 60,34\nAR 61,35\nD0 41,28,46,1024\nMvaa 160,164,220\nTa 232,17,0\n"
"Mvaa 160,160,216\nTa 234,17,2048\nCV 209,60,61\nMvaa 164,168,216\nAR 60,36\nMvaa 164,172,220\nTa 236,17,16384\nD0 42,28,46,2048\nMvaa 132,172,212\nTa 238,17,18432\nAR 61,37\n"
"Mvaa 132,168,208\nCV 210,60,61\nMvaa 100,168,200\nTa 240,18,0\nAR 60,38\nMvaa 100,172,204\nTa 242,18,2048\nD0 43,28,46,3072\nMvaa 68,172,196\ns_add_u32 s76, s76, s55\n"
"Sv 240,1,16,76,0\nMvaa 68,168,192\nTa 244,18,16384\nMvaa 72,176,192\nTa 246,18,18432\nAR 61,39\nMvaa 72,180,196\nCV 211,60,61\nMvaa 104,180,204\nTa 248,19,0\nMvaa 104,176,200\n"
"Ta 250,19,2048\nSv 244,1,16,76,1024\nMvaa 136,176,208\nAR 60,40\nMvaa 136,180,212\nTa 252,19,16384\nAR 61,41\nMvaa 168,180,220\nTa 254,19,18432\nMvaa 168,176,216\nCV 212,60,61\n"
"AR 60,42\nMvaa 172,184,216\nSv 248,1,16,76,2048\nMvaa 172,188,220\nAR 61,43\nMvaa 140,188,212\nCV 213,60,61\nAR 60,44\nMvaa 140,184,208\nAR 61,45\nMvaa 108,184,200\nCV 214,60,61\n"
"AR 60,46\nMvaa 108,188,204\nSv 252,1,16,76,3072\nMvaa 76,188,196\nAR 61,47\nCV 215,60,61\nMvaa 76,184,192\ns_waitcnt vmcnt(20) lgkmcnt(0)\ns_barrier\nMvaa 80,224,192\n"
"Ra 128,44,16384\nAR 60,48\nv_cmp_ne_u32 vcc, s15, v27\ns_nop 0\ns_cbranch_vccnz .S421\n.S422:\nMvaa 80,228,196\nRa 132,45,16384\n"
"buffer_store_dword v25, off, s[16:19], s54 offset:0 sc0 sc1\ns_add_u32 m0, s47, 0\ns_add_u32 s46, s51, 2097152\nD0 6,32,46,0\nMvaa 112,228,204\nRa 136,44,18432\nMvaa 112,224,200\n"
"Ra 140,45,18432\nAR 61,49\nMvaa 144,224,208\nRa 144,46,16384\nMvaa 144,228,212\nRa 148,47,16384\nCV 216,60,61\nD0 7,32,46,1024\nMvaa 176,228,220\nRa 152,46,18432\n"
"Mvaa 176,224,216\nRa 156,47,18432\nAR 60,50\nMvaa 180,232,216\nTa 160,12,32768\nMvaa 180,236,220\nTa 162,12,34816\nD0 8,32,46,2048\nMvaa 148,236,212\nTa 164,12,49152\nAR 61,51\n"
"Mvaa 148,232,208\nTa 166,12,51200\nMvaa 116,232,200\nTa 168,13,32768\nCV 217,60,61\nMvaa 116,236,204\nTa 170,13,34816\nD0 9,32,46,3072\nMvaa 84,236,196\nTa 172,13,49152\nAR 60,52\n"
"Mvaa 84,232,192\nTa 174,13,51200\ns_add_u32 m0, s47, 16384\ns_add_u32 s46, s51, 2359296\nMvaa 88,240,192\nTa 176,14,32768\nAR 61,53\nMvaa 88,244,196\nTa 178,14,34816\n"
"D0 6,32,46,0\nMvaa 120,244,204\nTa 180,14,49152\nMvaa 120,240,200\nTa 182,14,51200\nCV 218,60,61\nMvaa 152,240,208\nTa 184,15,32768\nMvaa 152,244,212\nTa 186,15,34816\nAR 60,54\n"
"D0 7,32,46,1024\nMvaa 184,244,220\nTa 188,15,49152\nMvaa 184,240,216\nTa 190,15,51200\nAR 61,55\nMvaa 188,248,216\nCV 219,60,61\nMvaa 188,252,220\nAR 60,56\nD0 8,32,46,2048\n"
"Mvaa 156,252,212\nAR 61,57\nCV 220,60,61\nMvaa 156,248,208\nAR 60,58\nMvaa 124,248,200\nAR 61,59\nCV 221,60,61\nMvaa 124,252,204\nAR 60,60\nD0 9,32,46,3072\nMvaa 92,252,196\n"
"AR 61,61\nCV 222,60,61\nMvaa 92,248,192\nWL 0\nMvaa 64,160,128\nTa 224,16,32768\nAR 60,62\nMvaa 64,164,132\nTa 226,16,34816\ns_add_u32 m0, s47, -32768\ns_add_u32 s46, s52, 640\n"
"D0 40,28,46,0\nMvaa 96,164,140\nAR 61,63\nMvaa 96,160,136\nTa 228,16,49152\nCV 223,60,61\nMvaa 128,160,144\nTa 230,16,51200\nMvaa 128,164,148\nD0 41,28,46,1024\nMvaa 160,164,156\n"
"Ta 232,17,32768\nAR 60,126\nMvaa 160,160,152\nTa 234,17,34816\nMvaa 164,168,152\nAR 61,127\nCV 60,60,61\nMvaa 164,172,156\nTa 236,17,49152\nD0 42,28,46,2048\nMvaa 132,172,148\n"
"Ta 238,17,51200\nMvaa 132,168,144\nAW 127,60\nAR 60,124\nMvaa 100,168,136\nTa 240,18,32768\nMvaa 100,172,140\nTa 242,18,34816\nD0 43,28,46,3072\nMvaa 68,172,132\nSv 192,1,16,77,0\n"
"AR 61,125\nMvaa 68,168,128\nTa 244,18,49152\nMvaa 72,176,128\nTa 246,18,51200\nCV 60,60,61\nMvaa 72,180,132\nAW 126,60\nMvaa 104,180,140\nTa 248,19,32768\nMvaa 104,176,136\n"
"Ta 250,19,34816\nSv 196,1,16,77,1024\nMvaa 136,176,144\nAR 60,122\nMvaa 136,180,148\nTa 252,19,49152\nMvaa 168,180,156\nTa 254,19,51200\nAR 61,123\nMvaa 168,176,152\nCV 60,60,61\n"
"Mvaa 172,184,152\nSv 200,1,16,77,2048\nAW 125,60\nMvaa 172,188,156\nAR 60,120\nMvaa 140,188,148\nAR 61,121\nMvaa 140,184,144\nCV 60,60,61\nAW 124,60\nMvaa 108,184,136\nAR 60,118\n"
"Mvaa 108,188,140\nSv 204,1,16,77,3072\nMvaa 76,188,132\nAR 61,119\nCV 60,60,61\nMvaa 76,184,128\ns_waitcnt vmcnt(17) lgkmcnt(0)\ns_barrier\nMvaa 80,224,128\nRa 192,44,49152\n"
"AW 123,60\nMvaa 80,228,132\nRa 196,45,49152\nbuffer_store_dword v25, off, s[16:19], s54 offset:8 sc0 sc1\ns_add_u32 m0, s47, 32768\ns_add_u32 s46, s51, 2621440\nD0 6,32,46,0\n"
"Mvaa 112,228,140\nRa 200,44,51200\nMvaa 112,224,136\nRa 204,45,51200\nAR 60,116\nMvaa 144,224,144\nRa 208,46,49152\nMvaa 144,228,148\nRa 212,47,49152\nD0 7,32,46,1024\n"
"Mvaa 176,228,156\nRa 216,46,51200\nAR 61,117\nMvaa 176,224,152\nRa 220,47,51200\nMvaa 180,232,152\nTa 160,32,0\nCV 60,60,61\nMvaa 180,236,156\nTa 162,32,2048\nD0 8,32,46,2048\n"
"Mvaa 148,236,148\nTa 164,32,16384\nMvaa 148,232,144\nTa 166,32,18432\nAW 122,60\nMvaa 116,232,136\nTa 168,33,0\nMvaa 116,236,140\nTa 170,33,2048\nD0 9,32,46,3072\nMvaa 84,236,132\n"
"Ta 172,33,16384\nAR 60,114\nMvaa 84,232,128\n.W580:\ns_cbranch_execz .W584\nTa 174,33,18432\ns_add_u32 m0, s47, 49152\ns_add_u32 s46, s51, 2883584\nMvaa 88,240,128\nTa 176,34,0\n"
"AR 61,115\nMvaa 88,244,132\nTa 178,34,2048\nD0 6,32,46,0\nMvaa 120,244,140\nTa 180,34,16384\nMvaa 120,240,136\nTa 182,34,18432\nCV 60,60,61\nMvaa 152,240,144\nTa 184,35,0\n"
"Mvaa 152,244,148\nTa 186,35,2048\nD0 7,32,46,1024\nMvaa 184,244,156\nTa 188,35,16384\nAW 121,60\nMvaa 184,240,152\nTa 190,35,18432\nMvaa 188,248,152\nAR 60,112\nAR 61,113\n"
"Mvaa 188,252,156\nD0 8,32,46,2048\nMvaa 156,252,148\nCV 60,60,61\nMvaa 156,248,144\nAW 120,60\nAR 60,110\nMvaa 124,248,136\nAR 61,111\nMvaa 124,252,140\nD0 9,32,46,3072\n"
"Mvaa 92,252,132\nCV 60,60,61\nAW 119,60\nMvaa 92,248,128\nWL 0\nMvaa 64,160,192\nTa 224,36,0\nAR 60,108\nMvaa 64,164,196\nTa 226,36,2048\ns_add_u32 m0, s47, -49152\n"
"s_add_u32 s46, s52, 768\nD0 40,28,46,0\nMvaa 96,164,204\nAR 61,109\nMvaa 96,160,200\nTa 228,36,16384\nCV 60,60,61\nMvaa 128,160,208\nTa 230,36,18432\nMvaa 128,164,212\n"
"D0 41,28,46,1024\nMvaa 160,164,220\nTa 232,37,0\nAW 118,60\nMvaa 160,160,216\nTa 234,37,2048\nMvaa 164,168,216\nAR 60,106\nAR 61,107\nMvaa 164,172,220\nTa 236,37,16384\n"
"D0 42,28,46,2048\nMvaa 132,172,212\nTa 238,37,18432\nMvaa 132,168,208\nCV 60,60,61\nAW 117,60\nMvaa 100,168,200\nTa 240,38,0\nMvaa 100,172,204\nTa 242,38,2048\nD0 43,28,46,3072\n"
"Mvaa 68,172,196\ns_add_u32 s77, s77, s55\nSv 208,1,16,77,0\nMvaa 68,168,192\nTa 244,38,16384\nMvaa 72,176,192\nTa 246,38,18432\nAR 60,104\nMvaa 72,180,196\nAR 61,105\n"
"Mvaa 104,180,204\nTa 248,39,0\nMvaa 104,176,200\nTa 250,39,2048\nSv 212,1,16,77,1024\nMvaa 136,176,208\nCV 60,60,61\nMvaa 136,180,212\nTa 252,39,16384\nMvaa 168,180,220\n"
"Ta 254,39,18432\nAW 116,60\nMvaa 168,176,216\nAR 60,102\nMvaa 172,184,216\nSv 216,1,16,77,2048\nAR 61,103\nMvaa 172,188,220\nCV 60,60,61\nMvaa 140,188,212\nAW 115,60\n"
"Mvaa 140,184,208\nAR 60,100\nAR 61,101\nMvaa 108,184,200\nCV 60,60,61\nMvaa 108,188,204\nSv 220,1,16,77,3072\nMvaa 76,188,196\nAW 114,60\nAR 60,98\nMvaa 76,184,192\n"
"s_waitcnt vmcnt(25) lgkmcnt(0)\ns_barrier\nMvaa 80,224,192\nRa 128,44,0\nv_and_b32 v27, 0x70, v1\nbuffer_load_dword v27, v27, s[16:19], s61 offen offset:128 sc0 sc1\n"
"Mvaa 80,228,196\nRa 132,45,0\ns_add_u32 m0, s47, 65536\ns_add_u32 s46, s51, 3145728\nD0 6,32,46,0\nMvaa 112,228,204\nRa 136,44,2048\nMvaa 112,224,200\nRa 140,45,2048\nAR 61,99\n"
"Mvaa 144,224,208\nRa 144,46,0\nMvaa 144,228,212\nRa 148,47,0\nD0 7,32,46,1024\nMvaa 176,228,220\nRa 152,46,2048\nCV 60,60,61\nMvaa 176,224,216\nRa 156,47,2048\nMvaa 180,232,216\n"
"Ta 160,12,0\nAW 113,60\nMvaa 180,236,220\nTa 162,12,2048\nD0 8,32,46,2048\nMvaa 148,236,212\nTa 164,12,16384\nMvaa 148,232,208\nTa 166,12,18432\nAR 60,96\nMvaa 116,232,200\n"
"Ta 168,13,0\nMvaa 116,236,204\nTa 170,13,2048\nD0 9,32,46,3072\nMvaa 84,236,196\nTa 172,13,16384\nAR 61,97\nMvaa 84,232,192\nTa 174,13,18432\ns_add_u32 m0, s47, 81920\n"
"s_add_u32 s46, s51, 3407872\nMvaa 88,240,192\nTa 176,14,0\nCV 60,60,61\nMvaa 88,244,196\nTa 178,14,2048\nD0 6,32,46,0\nMvaa 120,244,204\nTa 180,14,16384\nMvaa 120,240,200\n"
"Ta 182,14,18432\nAW 112,60\nMvaa 152,240,208\nTa 184,15,0\nMvaa 152,244,212\nTa 186,15,2048\nD0 7,32,46,1024\nMvaa 184,244,220\nTa 188,15,16384\nAR 60,94\nMvaa 184,240,216\n"
"Ta 190,15,18432\nMvaa 188,248,216\nAR 61,95\nCV 60,60,61\nMvaa 188,252,220\nD0 8,32,46,2048\nMvaa 156,252,212\nAW 111,60\nMvaa 156,248,208\nAR 60,92\nAR 61,93\nMvaa 124,248,200\n"
"CV 60,60,61\nMvaa 124,252,204\nD0 9,32,46,3072\nMvaa 92,252,196\nAW 110,60\nAR 60,90\nMvaa 92,248,192\nWL 0\nMvaa 64,160,128\nTa 224,16,0\nAR 61,91\nMvaa 64,164,132\n"
"Ta 226,16,2048\ns_add_u32 s6, s56, 0\nbuffer_load_dwordx4 a[0:3], v1, s[16:19], s6 offen offset:0 sc1\nMvaa 96,164,140\nCV 60,60,61\nMvaa 96,160,136\nTa 228,16,16384\nAW 109,60\n"
"Mvaa 128,160,144\nTa 230,16,18432\nMvaa 128,164,148\nbuffer_load_dwordx4 a[4:7], v1, s[16:19], s6 offen offset:1024 sc1\nMvaa 160,164,156\nTa 232,17,0\nAR 60,88\nMvaa 160,160,152\n"
"Ta 234,17,2048\nMvaa 164,168,152\nAR 61,89\nCV 60,60,61\nMvaa 164,172,156\nTa 236,17,16384\nbuffer_load_dwordx4 a[8:11], v1, s[16:19], s6 offen offset:2048 sc1\nMvaa 132,172,148\n"
"Ta 238,17,18432\nMvaa 132,168,144\nAW 108,60\nAR 60,86\nMvaa 100,168,136\nTa 240,18,0\nMvaa 100,172,140\nTa 242,18,2048\n"
"buffer_load_dwordx4 a[12:15], v1, s[16:19], s6 offen offset:3072 sc1\nMvaa 68,172,132\nAR 61,87\nCV 60,60,61\nMvaa 68,168,128\nTa 244,18,16384\nMvaa 72,176,128\nTa 246,18,18432\n"
"AW 107,60\nMvaa 72,180,132\nAR 60,84\nMvaa 104,180,140\nTa 248,19,0\nMvaa 104,176,136\nTa 250,19,2048\nAR 61,85\nMvaa 136,176,144\nCV 60,60,61\nMvaa 136,180,148\nTa 252,19,16384\n"
"Mvaa 168,180,156\nTa 254,19,18432\nAW 106,60\nMvaa 168,176,152\nAR 60,82\nMvaa 172,184,152\nAR 61,83\nCV 60,60,61\nMvaa 172,188,156\nAW 105,60\nMvaa 140,188,148\nAR 60,80\n"
"Mvaa 140,184,144\nAR 61,81\nCV 60,60,61\nMvaa 108,184,136\nAW 104,60\nMvaa 108,188,140\nAR 60,78\nMvaa 76,188,132\nAR 61,79\nCV 60,60,61\nMvaa 76,184,128\n"
"s_waitcnt vmcnt(12) lgkmcnt(0)\ns_barrier\nMvaa 80,224,128\nRa 192,44,32768\nv_cmp_ne_u32 vcc, s15, v27\ns_nop 0\ns_cbranch_vccnz .S424\n.S425:\ns_sub_u32 s45, s86, 131072\n"
"s_cmp_eq_u32 s79, 1\ns_cselect_b32 s86, s94, s86\ns_add_u32 m0, s47, -65536\ns_add_u32 s46, s45, 0x0\nMvaa 80,228,132\nRa 196,45,32768\nMvaa 112,228,140\nRa 200,44,34816\n"
"D1 1,16,46,0\nMvaa 112,224,136\nRa 204,45,34816\nAW 103,60\nMvaa 144,224,144\nRa 208,46,32768\nMvaa 144,228,148\nRa 212,47,32768\nMvaa 176,228,156\nRa 216,46,34816\n"
"D1 1,16,46,1024\nMvaa 176,224,152\nRa 220,47,34816\nMvaa 180,232,152\nTa 160,12,32768\nAR 60,76\nMvaa 180,236,156\nTa 162,12,34816\nMvaa 148,236,148\nTa 164,12,49152\n"
"D1 1,16,46,2048\nMvaa 148,232,144\nTa 166,12,51200\nAR 61,77\nMvaa 116,232,136\nTa 168,13,32768\nMvaa 116,236,140\nTa 170,13,34816\nMvaa 84,236,132\nTa 172,13,49152\n"
"D1 1,16,46,3072\nMvaa 84,232,128\nTa 174,13,51200\nMvaa 88,240,128\nTa 176,14,32768\ns_add_u32 s6, s56, 4096\nMvaa 88,244,132\nTa 178,14,34816\nMvaa 120,244,140\nTa 180,14,49152\n"
"buffer_load_dwordx4 a[16:19], v1, s[16:19], s6 offen offset:0 sc1\nMvaa 120,240,136\nTa 182,14,51200\nCV 60,60,61\nMvaa 152,240,144\nTa 184,15,32768\nMvaa 152,244,148\n"
"Ta 186,15,34816\nMvaa 184,244,156\nTa 188,15,49152\nbuffer_load_dwordx4 a[20:23], v1, s[16:19], s6 offen offset:1024 sc1\nMvaa 184,240,152\nTa 190,15,51200\nMvaa 188,248,152\n"
"AW 102,60\nAR 60,74\nMvaa 188,252,156\nAR 61,75\nMvaa 156,252,148\nbuffer_load_dwordx4 a[24:27], v1, s[16:19], s6 offen offset:2048 sc1\nMvaa 156,248,144\nCV 60,60,61\nAW 101,60\n"
"Mvaa 124,248,136\nAR 60,72\nMvaa 124,252,140\nAR 61,73\nMvaa 92,252,132\nbuffer_load_dwordx4 a[28:31], v1, s[16:19], s6 offen offset:3072 sc1\nCV 60,60,61\nMvaa 92,248,128\nWL 0\n"
"Mvaa 64,160,192\nTa 224,16,32768\ns_add_u32 s6, s56, 16384\nMvaa 64,164,196\nTa 226,16,34816\nMvaa 96,164,204\nbuffer_load_dwordx4 a[32:35], v1, s[16:19], s6 offen offset:0 sc1\n"
"Mvaa 96,160,200\nTa 228,16,49152\nAW 100,60\nMvaa 128,160,208\nTa 230,16,51200\nMvaa 128,164,212\nAR 60,70\nMvaa 160,164,220\nTa 232,17,32768\n"
"buffer_load_dwordx4 a[36:39], v1, s[16:19], s6 offen offset:1024 sc1\nMvaa 160,160,216\nTa 234,17,34816\nMvaa 164,168,216\nAR 61,71\nCV 60,60,61\nMvaa 164,172,220\n"
"Ta 236,17,49152\nMvaa 132,172,212\nTa 238,17,51200\nbuffer_load_dwordx4 a[40:43], v1, s[16:19], s6 offen offset:2048 sc1\nMvaa 132,168,208\nAW 99,60\nAR 60,68\nMvaa 100,168,200\n"
"Ta 240,18,32768\nMvaa 100,172,204\nTa 242,18,34816\nMvaa 68,172,196\nbuffer_load_dwordx4 a[44:47], v1, s[16:19], s6 offen offset:3072 sc1\nAR 61,69\nMvaa 68,168,192\n"
"Ta 244,18,49152\nMvaa 72,176,192\nTa 246,18,51200\nCV 60,60,61\nMvaa 72,180,196\nAW 98,60\nMvaa 104,180,204\nTa 248,19,32768\nMvaa 104,176,200\nTa 250,19,34816\nAR 60,66\n"
"Mvaa 136,176,208\nAR 61,67\nMvaa 136,180,212\nTa 252,19,49152\nMvaa 168,180,220\nTa 254,19,51200\nCV 60,60,61\nMvaa 168,176,216\nAW 97,60\nMvaa 172,184,216\nAR 60,64\nAR 61,65\n"
"Mvaa 172,188,220\nCV 60,60,61\nMvaa 140,188,212\nAW 96,60\nv_add_u32 v2, 0x0, v1\nMvaa 140,184,208\ns_lshr_b32 s2, s59, 1\nMvaa 108,184,200\nv_add_u32 v24, s2, v2\n"
"v_xor_b32 v26, 0x1000, v24\nMvaa 108,188,204\nv_lshrrev_b32 v20, 4, v0\nMvaa 76,188,196\nv_lshlrev_b32 v20, 2, v20\nv_and_b32 v0, 15, v0\nMvaa 76,184,192\n"
"s_waitcnt vmcnt(16) lgkmcnt(0)\ns_barrier\nMvaa 80,224,192\nRa 128,44,16384\nv_sub_u32 v0, v0, v20\nMvaa 80,228,196\nRa 132,45,16384\n"
"buffer_store_dword v25, off, s[16:19], s54 offset:12 sc0 sc1\ns_add_u32 s6, s56, 20480\nbuffer_load_dwordx4 a[48:51], v1, s[16:19], s6 offen offset:0 sc1\nMvaa 112,228,204\n"
"Ra 136,44,18432\ns_lshl_b32 s2, s11, 4\nMvaa 112,224,200\nRa 140,45,18432\nMvaa 144,224,208\nRa 144,46,16384\nv_add_u32 v3, s2, v0\nMvaa 144,228,212\nRa 148,47,16384\n"
"buffer_load_dwordx4 a[52:55], v1, s[16:19], s6 offen offset:1024 sc1\nMvaa 176,228,220\nRa 152,46,18432\nv_subrev_u32 v0, 32, v3\nMvaa 176,224,216\nRa 156,47,18432\n"
"Mvaa 180,232,216\nTa 160,32,0\ns_lshl_b32 s2, s11, 13\nMvaa 180,236,220\nTa 162,32,2048\nbuffer_load_dwordx4 a[56:59], v1, s[16:19], s6 offen offset:2048 sc1\nMvaa 148,236,212\n"
"Ta 164,32,16384\ns_add_u32 s2, s2, 0x20000\nMvaa 148,232,208\nTa 166,32,18432\nMvaa 116,232,200\nTa 168,33,0\nv_add_u32 v22, s2, v1\nMvaa 116,236,204\nTa 170,33,2048\n"
"buffer_load_dwordx4 a[60:63], v1, s[16:19], s6 offen offset:3072 sc1\nMvaa 84,236,196\nTa 172,33,16384\nv_xor_b32 v23, 0x2000, v22\nMvaa 84,232,192\nTa 174,33,18432\n"
"s_add_u32 m0, s47, -32768\ns_add_u32 s46, s45, 0x8000\nMvaa 88,240,192\nTa 176,34,0\nv_mov_b32 v4, 0\nMvaa 88,244,196\nTa 178,34,2048\nD1 1,16,46,0\nMvaa 120,244,204\n"
"Ta 180,34,16384\nv_mov_b32 v5, 0\nMvaa 120,240,200\nTa 182,34,18432\nMvaa 152,240,208\nTa 184,35,0\nv_mov_b32 v6, 0\nMvaa 152,244,212\nTa 186,35,2048\nD1 1,16,46,1024\n"
"Mvaa 184,244,220\nTa 188,35,16384\nv_mov_b32 v7, 0\nMvaa 184,240,216\nTa 190,35,18432\n.W581:\ns_cbranch_execz .W585\nMvaa 188,248,216\nv_mov_b32 v8, 0\nv_mov_b32 v9, 0\n"
"Mvaa 188,252,220\nD1 1,16,46,2048\nMvaa 156,252,212\nv_mov_b32 v10, 0\nv_mov_b32 v11, 0\nMvaa 156,248,208\nv_mov_b32 v12, 0\nMvaa 124,248,200\nv_mov_b32 v13, 0\nv_mov_b32 v14, 0\n"
"Mvaa 124,252,204\nD1 1,16,46,3072\nMvaa 92,252,196\nv_mov_b32 v15, 0\nv_mov_b32 v16, 0\nMvaa 92,248,192\nWL 0\nMvaa 64,160,128\nTa 224,36,0\nv_mov_b32 v17, 0\nMvaa 64,164,132\n"
"Ta 226,36,2048\ns_add_u32 m0, s47, -16384\ns_add_u32 s46, s45, 0x400000\nD1 1,16,46,0\nMvaa 68,172,132\nv_mov_b32 v18, 0\nv_mov_b32 v19, 0\nMvaa 68,168,128\nTa 228,36,16384\n"
"Mvaa 72,176,128\nTa 230,36,18432\nMvaa 72,180,132\nD1 1,16,46,1024\nMvaa 76,188,132\nTa 232,37,0\nMvaa 76,184,128\nTa 234,37,2048\nMvaa 108,184,136\nv_mul_f32 v64, s57, v64\n"
"v_mul_f32 v65, s57, v65\nMvaa 108,188,140\nTa 236,37,16384\nD1 1,16,46,2048\nMvaa 104,180,140\nTa 238,37,18432\nCV 64,64,65\nMvaa 104,176,136\nv_mul_f32 v66, s57, v66\n"
"v_mul_f32 v67, s57, v67\nMvaa 100,168,136\nTa 240,38,0\nCV 65,66,67\nMvaa 100,172,140\nTa 242,38,2048\nD1 1,16,46,3072\nMvaa 96,164,140\nv_mul_f32 v68, s57, v68\n"
"v_mul_f32 v69, s57, v69\nMvaa 96,160,136\nTa 244,38,16384\nCV 66,68,69\nMvaa 128,160,144\nTa 246,38,18432\nv_mul_f32 v70, s57, v70\nMvaa 128,164,148\nv_mul_f32 v71, s57, v71\n"
"CV 67,70,71\nMvaa 132,172,148\nTa 248,39,0\nAW 192,64\nMvaa 132,168,144\nTa 250,39,2048\nAW 193,65\nMvaa 136,176,144\nAW 194,66\nAW 195,67\nMvaa 136,180,148\nTa 252,39,16384\n"
"v_mul_f32 v72, s57, v72\nMvaa 140,188,148\nTa 254,39,18432\nv_mul_f32 v73, s57, v73\nMvaa 140,184,144\nCV 72,72,73\nv_mul_f32 v74, s57, v74\nMvaa 172,184,152\n"
"v_mul_f32 v75, s57, v75\nCV 73,74,75\nMvaa 172,188,156\nv_mul_f32 v76, s57, v76\nv_mul_f32 v77, s57, v77\nMvaa 168,180,156\nCV 74,76,77\nv_mul_f32 v78, s57, v78\nMvaa 168,176,152\n"
"v_mul_f32 v79, s57, v79\nCV 75,78,79\nMvaa 164,168,152\nAW 200,72\nAW 201,73\nMvaa 164,172,156\nAW 202,74\nAW 203,75\nMvaa 160,164,156\nv_mul_f32 v96, s57, v96\n"
"v_mul_f32 v97, s57, v97\nMvaa 160,160,152\ns_waitcnt vmcnt(21) lgkmcnt(0)\ns_barrier\nMvaa 80,224,128\nCV 96,96,97\nv_mul_f32 v98, s57, v98\nMvaa 80,228,132\ns_add_u32 m0, s47, 0\n"
"s_add_u32 s46, s86, 0xffff0000\nD1 1,16,46,0\nMvaa 84,236,132\nv_mul_f32 v99, s57, v99\nCV 97,98,99\nMvaa 84,232,128\nv_mul_f32 v100, s57, v100\nv_mul_f32 v101, s57, v101\n"
"Mvaa 88,240,128\nCV 98,100,101\nv_mul_f32 v102, s57, v102\nMvaa 88,244,132\nD1 1,16,46,1024\nv_mul_f32 v103, s57, v103\nMvaa 92,252,132\nCV 99,102,103\nAW 196,96\nMvaa 92,248,128\n"
"AW 197,97\nAW 198,98\nMvaa 124,248,136\nAW 199,99\nv_mul_f32 v128, s57, v128\nMvaa 124,252,140\nD1 1,16,46,2048\nv_mul_f32 v129, s57, v129\nMvaa 120,244,140\nCV 128,128,129\n"
"v_mul_f32 v130, s57, v130\nMvaa 120,240,136\nv_mul_f32 v131, s57, v131\nCV 129,130,131\nMvaa 116,232,136\nRa 160,24,0\nv_mul_f32 v132, s57, v132\nMvaa 116,236,140\n"
"D1 1,16,46,3072\nv_mul_f32 v133, s57, v133\nMvaa 112,228,140\nRa 164,24,1024\nCV 130,132,133\nMvaa 112,224,136\ns_add_u32 m0, s47, 16384\ns_add_u32 s46, s45, 0x408000\n"
"Mvaa 144,224,144\nRa 168,24,2048\nv_mul_f32 v134, s57, v134\nMvaa 144,228,148\nD1 1,16,46,0\nv_mul_f32 v135, s57, v135\nMvaa 148,236,148\nRa 172,24,3072\nCV 131,134,135\n"
"Mvaa 148,232,144\nds_write_b128 v23, v[128:131] offset:0\nv_mul_f32 v80, s57, v80\nMvaa 152,240,144\nRa 176,26,0\nv_mul_f32 v81, s57, v81\nMvaa 152,244,148\nD1 1,16,46,1024\n"
"CV 80,80,81\nMvaa 156,252,148\nRa 180,26,1024\nv_mul_f32 v82, s57, v82\nMvaa 156,248,144\nv_mul_f32 v83, s57, v83\nCV 81,82,83\nMvaa 188,248,152\nRa 184,26,2048\n"
"v_mul_f32 v84, s57, v84\nMvaa 188,252,156\nD1 1,16,46,2048\nv_mul_f32 v85, s57, v85\nMvaa 184,244,156\nRa 188,26,3072\nCV 82,84,85\nMvaa 184,240,152\nv_mul_f32 v86, s57, v86\n"
"v_mul_f32 v87, s57, v87\nMvaa 180,232,152\nCV 83,86,87\nAW 208,80\nMvaa 180,236,156\nD1 1,16,46,3072\nAW 209,81\nMvaa 176,228,156\nAW 210,82\nAW 211,83\nMvaa 176,224,152\n"
"s_waitcnt vmcnt(25) lgkmcnt(5)\ns_cmp_eq_u32 s79, 1\ns_cbranch_scc0 .E427\nv_mov_b32 v4, s97\nv_mov_b32 v5, s97\nv_mov_b32 v6, s97\nv_mov_b32 v7, s97\nv_mov_b32 v12, s97\n"
"v_mov_b32 v13, s97\nv_mov_b32 v14, s97\nv_mov_b32 v15, s97\nv_cmpx_gt_i32 vcc, 19, v3\nv_mov_b32 v19, s97\nv_cmpx_gt_i32 vcc, 18, v3\nv_mov_b32 v18, s97\n"
"v_cmpx_gt_i32 vcc, 17, v3\nv_mov_b32 v17, s97\nv_cmpx_gt_i32 vcc, 16, v3\nv_mov_b32 v16, s97\nv_cmpx_gt_i32 vcc, 3, v3\nv_mov_b32 v11, s97\nv_cmpx_gt_i32 vcc, 2, v3\n"
"v_mov_b32 v10, s97\nv_cmpx_gt_i32 vcc, 1, v3\nv_mov_b32 v9, s97\nv_cmpx_gt_i32 vcc, 0, v3\nv_mov_b32 v8, s97\ns_mov_b64 exec, -1\ns_nop 2\n.E427:\ns_nop 0\nCvaa 32,160,0,4\n"
"Ra 128,24,8192\nv_mul_f32 v88, s57, v88\nMvaa 32,164,8\nv_mul_f32 v89, s57, v89\nCV 88,88,89\nMvaa 32,168,16\nRa 132,24,9216\nv_mul_f32 v90, s57, v90\nMvaa 32,172,24\n"
"v_mul_f32 v91, s57, v91\nCV 89,90,91\nCvaa 36,172,28,8\nRa 136,24,10240\nv_mul_f32 v92, s57, v92\nMvaa 36,168,20\ns_add_u32 m0, s89, 32768\nv_mul_f32 v93, s57, v93\n"
"Mvaa 36,164,12\nRa 140,24,11264\ns_waitcnt vmcnt(16) lgkmcnt(4)\ns_barrier\nMvaa 36,160,4\nCV 90,92,93\nv_mul_f32 v94, s57, v94\nMvaa 36,176,36\nRa 144,26,8192\nSa 96,1,16,78,0\n"
"Mvaa 36,180,44\ns_add_u32 s46, s86, s88\nv_mul_f32 v95, s57, v95\nMvaa 36,184,52\nRa 148,26,9216\nCV 91,94,95\nMvaa 36,188,60\nAW 216,88\nAW 217,89\nMvaa 32,188,56\n"
"Ra 152,26,10240\nAW 218,90\nMvaa 32,184,48\nD1 1,16,46,0\nAW 219,91\nMvaa 32,180,40\nRa 156,26,11264\nWL 4\nMvaa 32,176,32\nv_mov_b32 v86, 0\nv_mov_b32 v87, 0\nCvaa 40,128,0,12\n"
"Sa 100,1,16,78,1024\nv_mov_b32 v88, 0\nMvaa 40,132,8\ns_cmp_eq_u32 s99, -1\nMvaa 40,136,16\ns_cselect_b32 s87, s95, s87\nv_mov_b32 v89, 0\nv_mul_f32 v136, s57, v136\n"
"v_mul_f32 v137, s57, v137\nCV 136,136,137\nv_mul_f32 v138, s57, v138\nv_mul_f32 v139, s57, v139\nMvaa 40,140,24\nCV 137,138,139\nv_mul_f32 v140, s57, v140\n"
"v_mul_f32 v141, s57, v141\nCV 138,140,141\nv_mul_f32 v142, s57, v142\nv_mul_f32 v143, s57, v143\nCvaa 44,140,28,16\nv_and_b32 v27, 0x70, v1\n"
"buffer_load_dword v27, v27, s[16:19], s61 offen offset:136 sc0 sc1\nCV 139,142,143\nds_write_b128 v23, v[136:139] offset:2048\nv_mul_f32 v144, s57, v144\n"
"v_mul_f32 v145, s57, v145\nCV 144,144,145\nMvaa 44,136,20\nD1 1,16,46,1024\nv_mul_f32 v146, s57, v146\nv_mul_f32 v147, s57, v147\nCV 145,146,147\nv_mul_f32 v148, s57, v148\n"
"v_mul_f32 v149, s57, v149\nMvaa 44,132,12\nWL 1\nCV 146,148,149\nv_mul_f32 v150, s57, v150\nv_mul_f32 v151, s57, v151\nCV 147,150,151\nds_write_b128 v23, v[144:147] offset:4096\n"
"v_mul_f32 v152, s57, v152\nMvaa 44,128,4\nv_mul_f32 v153, s57, v153\nCV 152,152,153\nv_mul_f32 v154, s57, v154\nv_mul_f32 v155, s57, v155\nCV 153,154,155\n"
"v_mul_f32 v156, s57, v156\nMvaa 44,144,36\nSa 104,1,16,78,2048\nv_mul_f32 v157, s57, v157\nCV 154,156,157\nv_mul_f32 v158, s57, v158\nv_mul_f32 v159, s57, v159\nCV 155,158,159\n"
"ds_write_b128 v23, v[152:155] offset:6144\nMvaa 44,148,44\ns_add_u32 s88, s88, 32768\nEX 80,32\nv_mul_f32 v160, s57, v160\nv_mul_f32 v161, s57, v161\nCV 160,160,161\n"
"Mvaa 44,152,52\nv_mul_f32 v162, s57, v162\nv_mul_f32 v163, s57, v163\nCV 161,162,163\nv_mul_f32 v164, s57, v164\nv_mul_f32 v165, s57, v165\nCV 162,164,165\n"
"v_mul_f32 v166, s57, v166\nMvaa 44,156,60\nEX 81,33\nv_mul_f32 v167, s57, v167\nCV 163,166,167\nds_write_b128 v23, v[160:163] offset:1024\nMvaa 40,156,56\n"
"v_mul_f32 v168, s57, v168\nv_mul_f32 v169, s57, v169\nCV 168,168,169\nv_mul_f32 v170, s57, v170\nv_mul_f32 v171, s57, v171\nCV 169,170,171\nv_mul_f32 v172, s57, v172\n"
"Mvaa 40,152,48\nD1 1,16,46,2048\nv_mul_f32 v173, s57, v173\nCV 170,172,173\nv_mul_f32 v174, s57, v174\nv_mul_f32 v175, s57, v175\nCV 171,174,175\nMvaa 40,148,40\n"
"ds_write_b128 v23, v[168:171] offset:3072\nv_mul_f32 v176, s57, v176\nv_mul_f32 v177, s57, v177\nCV 176,176,177\nv_mul_f32 v178, s57, v178\nv_mul_f32 v179, s57, v179\n"
"CV 177,178,179\nMvaa 40,144,32\nAD 84,80,81\nCV 64,80,81\nv_mul_f32 v180, s57, v180\nv_mul_f32 v181, s57, v181\nCV 178,180,181\nv_mul_f32 v182, s57, v182\nSa 108,1,16,78,3072\n"
"v_mul_f32 v183, s57, v183\nCV 179,182,183\nds_write_b128 v23, v[176:179] offset:5120\nv_mul_f32 v184, s57, v184\nv_mul_f32 v185, s57, v185\nCV 184,184,185\nEX 82,34\n"
"v_mul_f32 v186, s57, v186\nv_mul_f32 v187, s57, v187\nCV 185,186,187\nv_mul_f32 v188, s57, v188\nv_mul_f32 v189, s57, v189\nCV 186,188,189\nv_mul_f32 v190, s57, v190\n"
"v_mul_f32 v191, s57, v191\nCV 187,190,191\nds_write_b128 v23, v[184:187] offset:7168\ns_waitcnt vmcnt(16) lgkmcnt(0)\nEX 83,35\nAD 84,84,82\nD1 1,16,46,3072\n"
"s_add_u32 m0, s89, 49152\ns_barrier\nAD 84,84,83\nCV 65,82,83\nRa 128,24,32768\nRa 224,22,0\nRa 228,22,1024\ns_add_u32 s46, s87, s88\nEX 80,36\nRa 132,24,33792\nRa 232,22,2048\n"
"Ra 236,22,3072\nRa 240,22,4096\nEX 81,37\nRa 136,24,34816\nRa 244,22,5120\nRa 248,22,6144\nD1 1,16,46,0\nRa 252,22,7168\nv_mul_f32 v104, s57, v104\nv_mul_f32 v105, s57, v105\n"
"Ra 140,24,35840\nCV 104,104,105\nv_mul_f32 v106, s57, v106\nAD 85,80,81\nCV 68,80,81\nv_mul_f32 v107, s57, v107\nRa 144,26,32768\nCV 105,106,107\nv_mul_f32 v108, s57, v108\n"
"v_mul_f32 v109, s57, v109\ns_cmp_eq_u32 s13, 7\nEX 82,38\nRa 148,26,33792\ns_cbranch_scc0 .E414\nv_cmpx_gt_i32 vcc, 19, v0\nv_mov_b32 v19, s97\nv_cmpx_gt_i32 vcc, 18, v0\n"
"v_mov_b32 v18, s97\nv_cmpx_gt_i32 vcc, 17, v0\nv_mov_b32 v17, s97\nv_cmpx_gt_i32 vcc, 16, v0\nv_mov_b32 v16, s97\nv_cmpx_gt_i32 vcc, 3, v0\nv_mov_b32 v11, s97\n"
"v_cmpx_gt_i32 vcc, 2, v0\nv_mov_b32 v10, s97\nv_cmpx_gt_i32 vcc, 1, v0\nv_mov_b32 v9, s97\nv_cmpx_gt_i32 vcc, 0, v0\nv_mov_b32 v8, s97\ns_mov_b64 exec, -1\ns_nop 2\n.E414:\n"
"EX 83,39\nRa 152,26,34816\nAD 85,85,82\nCV 106,108,109\nv_mul_f32 v110, s57, v110\nD1 1,16,46,1024\nv_mul_f32 v111, s57, v111\nCV 107,110,111\nRa 156,26,35840\nAW 204,104\n"
"AW 205,105\nAD 85,85,83\nCV 69,82,83\nAW 206,106\nAW 207,107\nRa 160,24,40960\nv_mul_f32 v112, s57, v112\nv_mul_f32 v113, s57, v113\ns_xor_b32 s89, s89, 0x10000\nEX 80,40\n"
"Ra 164,24,41984\nCV 112,112,113\nv_mul_f32 v114, s57, v114\nv_mul_f32 v115, s57, v115\nWL 6\nEX 81,41\nRa 168,24,43008\nAD 84,84,80\nCV 113,114,115\nD1 1,16,46,2048\n"
"v_mul_f32 v116, s57, v116\nv_mul_f32 v117, s57, v117\nCV 114,116,117\nRa 172,24,44032\nv_mul_f32 v118, s57, v118\nv_mul_f32 v119, s57, v119\nAD 84,84,81\nCV 66,80,81\n"
"CV 115,118,119\nCvaa 48,128,0,4\nRa 176,26,40960\nAW 212,112\nAW 213,113\nAW 214,114\nMvaa 48,132,8\nEX 82,42\nAW 215,115\nMvaa 48,136,16\nRa 180,26,41984\n"
"v_mul_f32 v120, s57, v120\nv_mul_f32 v121, s57, v121\nMvaa 48,140,24\nWL 6\nEX 83,43\nWV 8\nv_cmp_ne_u32 vcc, s15, v27\ns_nop 0\ns_cbranch_vccnz .S428\n.S429:\nCvaa 52,140,28,8\n"
"Ra 184,26,43008\nAD 84,84,82\nCV 120,120,121\nv_mul_f32 v122, s57, v122\nMvaa 52,136,20\nD1 1,16,46,3072\ns_add_u32 m0, s89, 0\nv_mul_f32 v123, s57, v123\nMvaa 52,132,12\n"
"Ra 188,26,44032\nCV 121,122,123\nv_mul_f32 v124, s57, v124\nMvaa 52,128,4\nAD 84,84,83\nCV 67,82,83\nv_mul_f32 v125, s57, v125\nCV 122,124,125\nMvaa 52,144,36\nRa 128,2,49152\n"
"s_add_u32 s78, s78, s55\nSa 112,1,16,78,0\nMvaa 52,148,44\ns_add_u32 s46, s86, s88\nEX 80,44\nMvaa 52,152,52\nRa 132,2,50176\nv_xor_b32 v24, 0x10000, v24\n"
"v_mul_f32 v126, s57, v126\nv_mul_f32 v127, s57, v127\nMvaa 52,156,60\nWL 6\nEX 81,45\nMvaa 48,156,56\nRa 136,2,51200\nAD 85,85,80\nCV 123,126,127\nMvaa 48,152,48\nD1 1,16,46,0\n"
"AW 220,120\nAW 221,121\nAW 222,122\nMvaa 48,148,40\nRa 140,2,52224\nAW 223,123\nv_mov_b32 v115, -1\nMvaa 48,144,32\nAD 85,85,81\nCV 70,80,81\nCvaa 56,160,0,12\nRa 144,2,53248\n"
"Sa 116,1,16,78,1024\nMvaa 56,164,8\nEX 82,46\nMvaa 56,168,16\nRa 148,2,54272\nv_xor_b32 v26, 0x10000, v26\nMvaa 56,172,24\nWL 6\nEX 83,47\nCvaa 60,172,28,16\nRa 152,2,55296\n"
"AD 85,85,82\nMvaa 60,168,20\nD1 1,16,46,1024\nMvaa 60,164,12\nRa 156,2,56320\nMvaa 60,160,4\nAD 85,85,83\nCV 71,82,83\nMvaa 60,176,36\nRa 160,2,57344\nv_max_f32 v90, v84, v85\n"
"Mvaa 60,180,44\ns_add_u32 s88, s88, 32768\nEX 80,48\nMvaa 60,184,52\nRa 164,2,58368\nv_cmp_lt_f32 vcc, s63, v90\nMvaa 60,188,60\nWL 8\nEX 81,49\nMvaa 56,188,56\nRa 168,2,59392\n"
"s_cbranch_vccnz .T416\n.T417:\nv_add_f32 v20, s93, v84\nv_mov_b32 v21, v85\nv_mov_b32 v22, v20\nv_mov_b32 v23, v21\nMvaa 56,184,48\nD1 1,16,46,2048\n"
"v_permlane32_swap_b32 v20, v22\nv_permlane32_swap_b32 v21, v23\nAD 20,20,22\nAD 21,21,23\nv_mov_b32 v22, v20\nv_mov_b32 v23, v21\nMvaa 56,180,40\nRa 172,2,60416\n"
"v_permlane16_swap_b32 v20, v22\nv_permlane16_swap_b32 v21, v23\nAD 20,20,22\nAD 21,21,23\nv_min_f32 v90, v20, v21\nv_cmp_gt_f32 vcc, s64, v90\ns_nop 0\ns_cbranch_vccnz .T418\n"
".T419:\nAD 86,86,84\nMvaa 56,176,32\nAD 84,80,81\nCV 72,80,81\nZvav 128,128,64\nRa 176,2,61440\nSa 120,1,16,78,2048\nZvav 132,128,68\nEX 82,50\nZvav 140,132,68\nRa 180,2,62464\n"
"Zvav 136,132,64\nWL 8\nEX 83,51\nZvav 144,136,64\nRa 184,2,63488\nAD 84,84,82\nZvav 148,136,68\n.W582:\ns_cbranch_execz .W586\nD1 1,16,46,3072\ns_add_u32 m0, s89, 16384\n"
"Zvav 156,140,68\nRa 188,2,64512\ns_barrier\nZvav 152,140,64\nAD 84,84,83\nCV 73,82,83\nZvav 160,144,64\nRa 128,24,0\nSa 124,1,16,78,3072\nZvav 164,144,68\ns_add_u32 s46, s87, s88\n"
"EX 80,52\nZvav 172,148,68\nRa 132,24,1024\nv_xor_b32 v2, 0x10000, v2\nZvav 168,148,64\nWL 8\nEX 81,53\nZvav 176,152,64\nRa 136,24,2048\nAD 87,87,85\nZvav 180,152,68\nD1 1,16,46,0\n"
"Zvav 188,156,68\nRa 140,24,3072\nZvav 184,156,64\nAD 85,80,81\nCV 76,80,81\nZvav 192,160,64\nRa 144,26,0\nZvav 196,160,68\nEX 82,54\nZvav 204,164,68\nRa 148,26,1024\n"
"Zvav 200,164,64\nWL 8\nEX 83,55\nZvav 208,168,64\nRa 152,26,2048\nAD 85,85,82\nZvav 212,168,68\nD1 1,16,46,1024\nZvav 220,172,68\nRa 156,26,3072\nZvav 216,172,64\nAD 85,85,83\n"
"CV 77,82,83\nZvav 224,176,64\nRa 160,24,8192\nZvav 228,176,68\nEX 80,56\nZvav 236,180,68\nRa 164,24,9216\nZvav 232,180,64\nWL 6\nEX 81,57\nZvav 240,184,64\nRa 168,24,10240\n"
"AD 84,84,80\nv_and_b32 v27, 0x70, v1\nbuffer_load_dword v27, v27, s[16:19], s61 offen offset:140 sc0 sc1\nZvav 244,184,68\nD1 1,16,46,2048\nZvav 252,188,68\nRa 172,24,11264\n"
"Zvav 248,188,64\nAD 84,84,81\nCV 74,80,81\ns_sub_u32 s99, s99, 1\ns_cmp_eq_u32 s13, 7\ns_cbranch_scc1 .SW412\n.p2align 6\n.A443:\nCvaa 32,128,0,4\nRa 176,26,8192\nMvaa 32,132,8\n"
"s_add_u32 s49, s49, 131072\nEX 82,58\nMvaa 32,136,16\nRa 180,26,9216\nMvaa 32,140,24\nWL 6\nEX 83,59\nCvaa 36,140,28,8\nRa 184,26,10240\nAD 84,84,82\nMvaa 36,136,20\n"
"D1 1,16,46,3072\ns_add_u32 m0, s89, 32768\nMvaa 36,132,12\nRa 188,26,11264\nMvaa 36,128,4\nAD 84,84,83\nCV 75,82,83\nMvaa 36,144,36\nRa 128,2,16384\nMvaa 36,148,44\n"
"s_add_u32 s46, s86, s88\nEX 80,60\nMvaa 36,152,52\nRa 132,2,17408\nMvaa 36,156,60\nWL 6\nEX 81,61\nMvaa 32,156,56\nRa 136,2,18432\nAD 85,85,80\nMvaa 32,152,48\nD1 1,16,46,0\n"
"Mvaa 32,148,40\nRa 140,2,19456\nMvaa 32,144,32\nAD 85,85,81\nCV 78,80,81\nCvaa 40,160,0,12\nRa 144,2,20480\nMvaa 40,164,8\nSv 112,1,16,49,0\nEX 82,62\nMvaa 40,168,16\n"
"Ra 148,2,21504\nMvaa 40,172,24\nWL 6\nEX 83,63\nCvaa 44,172,28,16\nRa 152,2,22528\nAD 85,85,82\nMvaa 44,168,20\nD1 1,16,46,1024\nMvaa 44,164,12\nRa 156,2,23552\nMvaa 44,160,4\n"
"AD 85,85,83\nCV 79,82,83\nMvaa 44,176,36\nRa 160,2,24576\nv_max_f32 v90, v84, v85\nMvaa 44,180,44\ns_add_u32 s88, s88, 32768\nEX 80,32\nMvaa 44,184,52\nRa 164,2,25600\n"
"v_cmp_lt_f32 vcc, s63, v90\nMvaa 44,188,60\nWL 8\nEX 81,33\nMvaa 40,188,56\nRa 168,2,26624\ns_cbranch_vccnz .T451\n.T452:\nMvaa 40,184,48\nD1 1,16,46,2048\nMvaa 40,180,40\n"
"Ra 172,2,27648\nAD 86,86,84\nMvaa 40,176,32\nAD 84,80,81\nCV 64,80,81\nMvav 128,128,72\nRa 176,2,28672\nMvav 132,128,76\nEX 82,34\nMvav 140,132,76\nRa 180,2,29696\n"
"Mvav 136,132,72\ns_waitcnt vmcnt(13) lgkmcnt(8)\nEX 83,35\nMvav 144,136,72\nRa 184,2,30720\nAD 84,84,82\nMvav 148,136,76\nD1 1,16,46,3072\ns_add_u32 m0, s89, 49152\n"
"Mvav 156,140,76\nRa 188,2,31744\ns_barrier\nMvav 152,140,72\nAD 84,84,83\nCV 65,82,83\nMvav 160,144,72\nRa 128,24,32768\nMvav 164,144,76\ns_add_u32 s46, s87, s88\nEX 80,36\n"
"Mvav 172,148,76\nRa 132,24,33792\nMvav 168,148,72\nWL 8\nEX 81,37\nMvav 176,152,72\nRa 136,24,34816\nAD 87,87,85\nMvav 180,152,76\nD1 1,16,46,0\nMvav 188,156,76\nRa 140,24,35840\n"
"Mvav 184,156,72\nAD 85,80,81\nCV 68,80,81\nMvav 192,160,72\nRa 144,26,32768\nMvav 196,160,76\ns_cmp_eq_u32 s99, 0\nEX 82,38\nMvav 204,164,76\nRa 148,26,33792\n"
"s_cbranch_scc1 .E445\n.E446:\nMvav 200,164,72\nWL 8\nEX 83,39\nMvav 208,168,72\nRa 152,26,34816\nAD 85,85,82\nMvav 212,168,76\nD1 1,16,46,1024\nMvav 220,172,76\nRa 156,26,35840\n"
"Mvav 216,172,72\nAD 85,85,83\nCV 69,82,83\nMvav 224,176,72\nRa 160,24,40960\nMvav 228,176,76\ns_xor_b32 s89, s89, 0x10000\nEX 80,40\nMvav 236,180,76\nRa 164,24,41984\n"
"Mvav 232,180,72\nWL 6\nEX 81,41\nMvav 240,184,72\nRa 168,24,43008\nAD 84,84,80\nMvav 244,184,76\nD1 1,16,46,2048\nMvav 252,188,76\nRa 172,24,44032\nMvav 248,188,72\nAD 84,84,81\n"
"CV 66,80,81\nCvaa 48,128,0,4\nRa 176,26,40960\nMvaa 48,132,8\ns_cmp_eq_u32 s99, 0\nEX 82,42\nMvaa 48,136,16\nRa 180,26,41984\ns_cselect_b32 s86, s94, s86\nMvaa 48,140,24\nWL 6\n"
"EX 83,43\nCvaa 52,140,28,8\nRa 184,26,43008\nAD 84,84,82\nMvaa 52,136,20\nD1 1,16,46,3072\ns_add_u32 m0, s89, 0\nMvaa 52,132,12\nRa 188,26,44032\nMvaa 52,128,4\nAD 84,84,83\n"
"CV 67,82,83\nMvaa 52,144,36\nRa 128,2,49152\nMvaa 52,148,44\ns_add_u32 s46, s86, s88\nEX 80,44\nMvaa 52,152,52\nRa 132,2,50176\nv_xor_b32 v24, 0x10000, v24\nMvaa 52,156,60\nWL 6\n"
"EX 81,45\nMvaa 48,156,56\nRa 136,2,51200\nAD 85,85,80\nMvaa 48,152,48\nD1 1,16,46,0\nMvaa 48,148,40\nRa 140,2,52224\nMvaa 48,144,32\nAD 85,85,81\nCV 70,80,81\nCvaa 56,160,0,12\n"
"Ra 144,2,53248\nMvaa 56,164,8\nSv 112,1,16,49,1024\nEX 82,46\nMvaa 56,168,16\nRa 148,2,54272\nv_xor_b32 v26, 0x10000, v26\nMvaa 56,172,24\nWL 6\nEX 83,47\nCvaa 60,172,28,16\n"
"Ra 152,2,55296\nAD 85,85,82\nMvaa 60,168,20\nD1 1,16,46,1024\nMvaa 60,164,12\nRa 156,2,56320\nMvaa 60,160,4\nAD 85,85,83\nCV 71,82,83\nMvaa 60,176,36\nRa 160,2,57344\n"
"v_max_f32 v90, v84, v85\nMvaa 60,180,44\ns_add_u32 s88, s88, 32768\nEX 80,48\nMvaa 60,184,52\nRa 164,2,58368\nv_cmp_lt_f32 vcc, s63, v90\nMvaa 60,188,60\nWL 8\nEX 81,49\n"
"Mvaa 56,188,56\nRa 168,2,59392\ns_cbranch_vccnz .T453\n.T454:\nMvaa 56,184,48\nD1 1,16,46,2048\nMvaa 56,180,40\nRa 172,2,60416\nAD 86,86,84\nMvaa 56,176,32\nAD 84,80,81\n"
"CV 72,80,81\nMvav 128,128,64\nRa 176,2,61440\nMvav 132,128,68\nEX 82,50\nMvav 140,132,68\nRa 180,2,62464\nMvav 136,132,64\ns_waitcnt vmcnt(13) lgkmcnt(8)\nEX 83,51\n"
"Mvav 144,136,64\nRa 184,2,63488\nAD 84,84,82\nMvav 148,136,68\nD1 1,16,46,3072\ns_add_u32 m0, s89, 16384\nMvav 156,140,68\nRa 188,2,64512\ns_barrier\nMvav 152,140,64\nAD 84,84,83\n"
"CV 73,82,83\nMvav 160,144,64\nRa 128,24,0\nMvav 164,144,68\ns_add_u32 s46, s87, s88\nEX 80,52\nMvav 172,148,68\nRa 132,24,1024\nv_xor_b32 v2, 0x10000, v2\nMvav 168,148,64\nWL 8\n"
"EX 81,53\nMvav 176,152,64\nRa 136,24,2048\nAD 87,87,85\nMvav 180,152,68\nD1 1,16,46,0\nMvav 188,156,68\nRa 140,24,3072\nMvav 184,156,64\nAD 85,80,81\nCV 76,80,81\nMvav 192,160,64\n"
"Ra 144,26,0\nMvav 196,160,68\ns_cmp_lt_u32 s99, s92\nEX 82,54\nMvav 204,164,68\nRa 148,26,1024\ns_cbranch_scc1 .E447\n.E448:\nMvav 200,164,64\nWL 8\nEX 83,55\nMvav 208,168,64\n"
"Ra 152,26,2048\nAD 85,85,82\nMvav 212,168,68\nD1 1,16,46,1024\nMvav 220,172,68\nRa 156,26,3072\nMvav 216,172,64\nAD 85,85,83\nCV 77,82,83\nMvav 224,176,64\nRa 160,24,8192\n"
"Mvav 228,176,68\ns_sub_u32 s90, s90, 1\nEX 80,56\nMvav 236,180,68\nRa 164,24,9216\ns_cbranch_scc1 .E449\n.E450:\nMvav 232,180,64\nWL 6\nEX 81,57\nMvav 240,184,64\nRa 168,24,10240\n"
"AD 84,84,80\nMvav 244,184,68\nD1 1,16,46,2048\ns_sub_u32 s99, s99, 1\nMvav 252,188,68\nRa 172,24,11264\nMvav 248,188,64\nAD 84,84,81\nCV 74,80,81\ns_cbranch_scc0 .A443\n"
"s_branch .A455\n.E445:\nv_cmpx_gt_i32 vcc, 19, v0\nv_mov_b32 v15, s97\nv_cmpx_gt_i32 vcc, 18, v0\nv_mov_b32 v14, s97\nv_cmpx_gt_i32 vcc, 17, v0\nv_mov_b32 v13, s97\n"
"v_cmpx_gt_i32 vcc, 16, v0\nv_mov_b32 v12, s97\nv_cmpx_gt_i32 vcc, 3, v0\nv_mov_b32 v7, s97\nv_cmpx_gt_i32 vcc, 2, v0\nv_mov_b32 v6, s97\nv_cmpx_gt_i32 vcc, 1, v0\n"
"v_mov_b32 v5, s97\nv_cmpx_gt_i32 vcc, 0, v0\nv_mov_b32 v4, s97\ns_mov_b64 exec, -1\ns_nop 2\ns_branch .E446\n.E447:\ns_cmp_eq_u32 s99, 0\ns_cbranch_scc1 .K444\n"
"v_cmpx_gt_i32 vcc, 19, v3\nv_mov_b32 v15, s97\nv_cmpx_gt_i32 vcc, 18, v3\nv_mov_b32 v14, s97\nv_cmpx_gt_i32 vcc, 17, v3\nv_mov_b32 v13, s97\nv_cmpx_gt_i32 vcc, 16, v3\n"
"v_mov_b32 v12, s97\nv_cmpx_gt_i32 vcc, 3, v3\nv_mov_b32 v7, s97\nv_cmpx_gt_i32 vcc, 2, v3\nv_mov_b32 v6, s97\nv_cmpx_gt_i32 vcc, 1, v3\nv_mov_b32 v5, s97\n"
"v_cmpx_gt_i32 vcc, 0, v3\nv_mov_b32 v4, s97\ns_mov_b64 exec, -1\ns_nop 2\ns_branch .E448\n.K444:\nv_cmpx_gt_i32 vcc, 19, v3\nv_mov_b32 v19, s97\nv_cmpx_gt_i32 vcc, 18, v3\n"
"v_mov_b32 v18, s97\nv_cmpx_gt_i32 vcc, 17, v3\nv_mov_b32 v17, s97\nv_cmpx_gt_i32 vcc, 16, v3\nv_mov_b32 v16, s97\nv_cmpx_gt_i32 vcc, 3, v3\nv_mov_b32 v11, s97\n"
"v_cmpx_gt_i32 vcc, 2, v3\nv_mov_b32 v10, s97\nv_cmpx_gt_i32 vcc, 1, v3\nv_mov_b32 v9, s97\nv_cmpx_gt_i32 vcc, 0, v3\nv_mov_b32 v8, s97\ns_mov_b64 exec, -1\ns_nop 2\n"
"s_branch .E448\n.E449:\ns_swappc_b64 s[80:81], s[8:9]\ns_branch .E450\n.HD406:\nv_cmp_ne_u32 vcc, s15, v27\ns_nop 0\ns_cbranch_vccnz .S431\n.S432:\n"
"buffer_store_dword v25, off, s[16:19], s54 offset:4 sc0 sc1\nv_and_b32 v27, 0x70, v1\nbuffer_load_dword v27, v27, s[16:19], s61 offen offset:12 sc0 sc1\ns_mov_b32 s90, 1\n"
"s_swappc_b64 s[8:9], s[80:81]\nv_cmp_ne_u32 vcc, s15, v27\ns_nop 0\ns_cbranch_vccnz .S434\n.S435:\nv_and_b32 v27, 0x70, v1\n"
"buffer_load_dword v27, v27, s[16:19], s61 offen offset:8 sc0 sc1\ns_mov_b32 s90, 1\ns_swappc_b64 s[8:9], s[80:81]\nv_cmp_ne_u32 vcc, s15, v27\ns_nop 0\ns_cbranch_vccnz .S437\n"
".S438:\nv_and_b32 v27, 0xf0, v1\nbuffer_load_dword v27, v27, s[16:19], s61 offen offset:4 sc0 sc1\ns_mov_b32 s90, 1\ns_swappc_b64 s[8:9], s[80:81]\nv_cmp_ne_u32 vcc, s15, v27\n"
"s_nop 0\ns_cbranch_vccnz .S440\n.S441:\ns_mov_b32 s90, 100\ns_swappc_b64 s[8:9], s[80:81]\n.A455:\nRa 176,26,8192\ns_add_u32 s49, s49, 131072\nEX 82,58\nRa 180,26,9216\nWL 6\n"
"EX 83,59\nCvaa 36,140,28,8\nRa 184,26,10240\nAD 84,84,82\nMvaa 36,136,20\nD1 1,16,46,3072\ns_add_u32 m0, s89, 32768\nMvaa 36,132,12\nRa 188,26,11264\nMvaa 36,128,4\nAD 84,84,83\n"
"CV 75,82,83\nMvaa 36,144,36\nRa 128,2,16384\nMvaa 36,148,44\ns_add_u32 s46, s86, s88\nEX 80,60\nMvaa 36,152,52\nRa 132,2,17408\nMvaa 36,156,60\nEX 81,61\nRa 136,2,18432\n"
"AD 85,85,80\nD1 1,16,46,0\nRa 140,2,19456\nAD 85,85,81\nCV 78,80,81\nRa 144,2,20480\nSv 112,1,16,49,0\ns_mov_b32 s87, s95\nEX 82,62\nRa 148,2,21504\nWL 6\nEX 83,63\n"
"Cvaa 44,172,28,16\nRa 152,2,22528\nAD 85,85,82\nMvaa 44,168,20\nD1 1,16,46,1024\nMvaa 44,164,12\nRa 156,2,23552\nMvaa 44,160,4\nAD 85,85,83\nCV 79,82,83\nMvaa 44,176,36\n"
"Ra 160,2,24576\nv_max_f32 v90, v84, v85\nMvaa 44,180,44\ns_add_u32 s88, s88, 32768\nMvaa 44,184,52\nRa 164,2,25600\nv_cmp_lt_f32 vcc, s63, v90\nMvaa 44,188,60\nWL 8\n"
"Ra 168,2,26624\ns_cbranch_vccnz .T457\n.T458:\nD1 1,16,46,2048\nRa 172,2,27648\nAD 86,86,84\nMvav 128,128,72\nRa 176,2,28672\nMvav 132,128,76\nMvav 140,132,76\nRa 180,2,29696\n"
"Mvav 136,132,72\ns_waitcnt vmcnt(13) lgkmcnt(8)\nMvav 144,136,72\nRa 184,2,30720\nMvav 148,136,76\nD1 1,16,46,3072\ns_add_u32 m0, s89, 49152\nMvav 156,140,76\nRa 188,2,31744\n"
"s_barrier\nMvav 152,140,72\nMvav 160,144,72\nRa 128,24,32768\nMvav 164,144,76\ns_add_u32 s46, s87, s88\nEX 80,36\nMvav 172,148,76\nRa 132,24,33792\nMvav 168,148,72\nWL 8\n"
"EX 81,37\nMvav 176,152,72\nRa 136,24,34816\nAD 87,87,85\nMvav 180,152,76\nD1 1,16,46,0\nMvav 188,156,76\nRa 140,24,35840\nMvav 184,156,72\nAD 85,80,81\nCV 68,80,81\n"
"Mvav 192,160,72\nRa 144,26,32768\nMvav 196,160,76\ns_nop 0\nEX 82,38\nMvav 204,164,76\nRa 148,26,33792\nv_cmpx_gt_i32 vcc, 19, v0\nv_mov_b32 v19, s97\nv_cmpx_gt_i32 vcc, 18, v0\n"
"v_mov_b32 v18, s97\nv_cmpx_gt_i32 vcc, 17, v0\nv_mov_b32 v17, s97\nv_cmpx_gt_i32 vcc, 16, v0\nv_mov_b32 v16, s97\nv_cmpx_gt_i32 vcc, 3, v0\nv_mov_b32 v11, s97\n"
"v_cmpx_gt_i32 vcc, 2, v0\nv_mov_b32 v10, s97\nv_cmpx_gt_i32 vcc, 1, v0\nv_mov_b32 v9, s97\nv_cmpx_gt_i32 vcc, 0, v0\nv_mov_b32 v8, s97\ns_mov_b64 exec, -1\ns_nop 2\ns_nop 0\n"
"Mvav 200,164,72\nWL 8\nEX 83,39\nMvav 208,168,72\nRa 152,26,34816\nAD 85,85,82\nMvav 212,168,76\nD1 1,16,46,1024\nMvav 220,172,76\nRa 156,26,35840\nMvav 216,172,72\nAD 85,85,83\n"
"CV 69,82,83\n.W583:\ns_cbranch_execz .W587\nMvav 224,176,72\nRa 160,24,40960\nMvav 228,176,76\ns_xor_b32 s89, s89, 0x10000\nMvav 236,180,76\nRa 164,24,41984\nMvav 232,180,72\n"
"WL 10\nMvav 240,184,72\nRa 168,24,43008\nMvav 244,184,76\nD1 1,16,46,2048\nMvav 252,188,76\nRa 172,24,44032\nMvav 248,188,72\nRa 176,26,40960\nRa 180,26,41984\nWL 6\n"
"Cvaa 52,140,28,8\nRa 184,26,43008\nMvaa 52,136,20\nD1 1,16,46,3072\ns_add_u32 m0, s89, 0\nMvaa 52,132,12\nRa 188,26,44032\nMvaa 52,128,4\nMvaa 52,144,36\nRa 128,2,49152\n"
"Mvaa 52,148,44\ns_add_u32 s46, s86, s88\nEX 80,44\nMvaa 52,152,52\nRa 132,2,50176\nv_xor_b32 v24, 0x10000, v24\nMvaa 52,156,60\nEX 81,45\nRa 136,2,51200\nAD 85,85,80\n"
"D1 1,16,46,0\nRa 140,2,52224\nAD 85,85,81\nCV 70,80,81\nRa 144,2,53248\nSv 112,1,16,49,1024\nEX 82,46\nRa 148,2,54272\nv_xor_b32 v26, 0x10000, v26\nWL 6\nEX 83,47\n"
"Cvaa 60,172,28,16\nRa 152,2,55296\nAD 85,85,82\nMvaa 60,168,20\nD1 1,16,46,1024\nMvaa 60,164,12\nRa 156,2,56320\nMvaa 60,160,4\nAD 85,85,83\nCV 71,82,83\nMvaa 60,176,36\n"
"Ra 160,2,57344\nMvaa 60,180,44\ns_add_u32 s88, s88, 32768\nMvaa 60,184,52\nRa 164,2,58368\nv_cmp_lt_f32 vcc, s63, v85\nMvaa 60,188,60\nWL 8\nRa 168,2,59392\ns_cbranch_vccnz .T459\n"
".T460:\nD1 1,16,46,2048\nRa 172,2,60416\nRa 176,2,61440\nMvav 132,128,68\nMvav 140,132,68\nRa 180,2,62464\ns_waitcnt vmcnt(13) lgkmcnt(8)\nRa 184,2,63488\nMvav 148,136,68\n"
"D1 1,16,46,3072\ns_add_u32 m0, s89, 16384\nMvav 156,140,68\nRa 188,2,64512\ns_barrier\nRa 128,24,0\nMvav 164,144,68\ns_add_u32 s46, s87, s88\nEX 80,52\nMvav 172,148,68\n"
"Ra 132,24,1024\nv_xor_b32 v2, 0x10000, v2\nWL 8\nEX 81,53\nRa 136,24,2048\nAD 87,87,85\nMvav 180,152,68\nD1 1,16,46,0\nMvav 188,156,68\nRa 140,24,3072\nAD 85,80,81\nCV 76,80,81\n"
"Ra 144,26,0\nMvav 196,160,68\nEX 82,54\nMvav 204,164,68\nRa 148,26,1024\nWL 8\nEX 83,55\nRa 152,26,2048\nAD 85,85,82\nMvav 212,168,68\nD1 1,16,46,1024\nMvav 220,172,68\n"
"Ra 156,26,3072\nAD 85,85,83\nCV 77,82,83\nRa 160,24,8192\nMvav 228,176,68\ns_sub_u32 s90, s90, 1\nMvav 236,180,68\nRa 164,24,9216\nWL 6\nRa 168,24,10240\nMvav 244,184,68\n"
"D1 1,16,46,2048\nMvav 252,188,68\nRa 172,24,11264\n.SW412:\nv_mov_b32 v4, 0\nv_mov_b32 v5, 0\nv_mov_b32 v6, 0\nv_mov_b32 v7, 0\nv_mov_b32 v8, 0\nv_mov_b32 v9, 0\nv_mov_b32 v10, 0\n"
"v_mov_b32 v11, 0\nv_mov_b32 v12, 0\nv_mov_b32 v13, 0\nv_mov_b32 v14, 0\nv_mov_b32 v15, 0\nv_mov_b32 v16, 0\nv_mov_b32 v17, 0\nv_mov_b32 v18, 0\nv_mov_b32 v19, 0\n"
"s_add_u32 s99, s98, 1\ns_mov_b32 s94, s96\ns_add_u32 s95, s96, 65536\nCvaa 32,128,192,4\nRa 176,26,8192\nMvaa 32,132,200\nMvaa 32,136,208\nRa 180,26,9216\nMvaa 32,140,216\nWL 6\n"
"Cvaa 36,140,220,8\nRa 184,26,10240\nMvaa 36,136,212\nD1 1,16,46,3072\ns_add_u32 m0, s89, 32768\nMvaa 36,132,204\nRa 188,26,11264\nMvaa 36,128,196\nMvaa 36,144,228\nRa 128,2,16384\n"
"Mvaa 36,148,236\ns_add_u32 s46, s86, s88\nEX 80,60\nMvaa 36,152,244\nRa 132,2,17408\nMvaa 36,156,252\nWL 6\nEX 81,61\nMvaa 32,156,248\nRa 136,2,18432\nAD 85,85,80\n"
"Mvaa 32,152,240\nD1 1,16,46,0\nMvaa 32,148,232\nRa 140,2,19456\nMvaa 32,144,224\nAD 85,85,81\nCV 78,80,81\nCvaa 40,160,192,12\nRa 144,2,20480\nMvaa 40,164,200\nEX 82,62\n"
"Mvaa 40,168,208\nRa 148,2,21504\nMvaa 40,172,216\nWL 6\nEX 83,63\nCvaa 44,172,220,16\nRa 152,2,22528\nAD 85,85,82\nMvaa 44,168,212\nD1 1,16,46,1024\nMvaa 44,164,204\n"
"Ra 156,2,23552\nMvaa 44,160,196\nAD 85,85,83\nCV 79,82,83\nMvaa 44,176,228\nRa 160,2,24576\nMvaa 44,180,236\ns_add_u32 s88, s88, 32768\nEX 80,32\nMvaa 44,184,244\nRa 164,2,25600\n"
"v_cmp_lt_f32 vcc, s63, v85\nMvaa 44,188,252\nWL 8\nEX 81,33\nMvaa 40,188,248\nRa 168,2,26624\ns_cbranch_vccnz .T463\n.T464:\nMvaa 40,184,240\nD1 1,16,46,2048\nCV 128,128,129\n"
"CV 129,130,131\nMvaa 40,180,232\nRa 172,2,27648\nCV 130,136,137\nCV 131,138,139\nMvaa 40,176,224\nAD 84,80,81\nCV 64,80,81\ns_add_u32 s5, s66, 0\nSv 128,1,16,5,0\nRa 176,2,28672\n"
"CV 144,144,145\nCV 145,146,147\nMvav 132,128,76\nEX 82,34\nMvav 140,132,76\nRa 180,2,29696\nCV 146,152,153\nCV 147,154,155\ns_waitcnt vmcnt(13) lgkmcnt(8)\nEX 83,35\n"
"s_add_u32 s5, s66, 0\nSv 144,1,16,5,2048\nRa 184,2,30720\nAD 84,84,82\nCV 160,160,161\nMvav 148,136,76\nD1 1,16,46,3072\ns_add_u32 m0, s89, 49152\nCV 161,162,163\nMvav 156,140,76\n"
"Ra 188,2,31744\ns_barrier\nCV 162,168,169\nCV 163,170,171\nAD 84,84,83\nCV 65,82,83\ns_add_u32 s5, s66, 4096\nSv 160,1,16,5,0\nRa 128,24,32768\nCV 176,176,177\nCV 177,178,179\n"
"Mvav 164,144,76\ns_add_u32 s46, s87, s88\nEX 80,36\nMvav 172,148,76\nRa 132,24,33792\nCV 178,184,185\nCV 179,186,187\nWL 8\nEX 81,37\ns_add_u32 s5, s66, 4096\nSv 176,1,16,5,2048\n"
"Ra 136,24,34816\nAD 87,87,85\nCV 132,132,133\nCV 133,134,135\nMvav 180,152,76\nD1 1,16,46,0\nv_mov_b32 v96, v86\nCV 134,140,141\nMvav 188,156,76\nRa 140,24,35840\n"
"v_mov_b32 v97, v87\nCV 135,142,143\nCV 148,148,149\nAD 85,80,81\nCV 68,80,81\ns_add_u32 s5, s66, 0\nSv 132,1,16,5,1024\nRa 144,26,32768\nv_permlane32_swap_b32 v86, v96\n"
"v_permlane32_swap_b32 v87, v97\nCV 149,150,151\nMvav 196,160,76\nEX 82,38\nCV 150,156,157\nMvav 204,164,76\nRa 148,26,33792\nAD 86,86,96\nCV 151,158,159\nCV 192,192,193\nWL 8\n"
"EX 83,39\ns_add_u32 s5, s66, 0\nSv 148,1,16,5,3072\nRa 152,26,34816\nAD 85,85,82\nAD 87,87,97\nCV 193,194,195\nMvav 212,168,76\nD1 1,16,46,1024\nv_mov_b32 v96, v86\n"
"CV 194,200,201\nMvav 220,172,76\nRa 156,26,35840\nv_mov_b32 v97, v87\nCV 195,202,203\nCV 164,164,165\nAD 85,85,83\nCV 69,82,83\ns_add_u32 s5, s66, 16384\nSv 192,1,16,5,0\n"
"Ra 160,24,40960\nv_permlane16_swap_b32 v86, v96\nv_permlane16_swap_b32 v87, v97\nCV 165,166,167\nMvav 228,176,76\ns_xor_b32 s89, s89, 0x10000\nEX 80,40\nMvav 236,180,76\n"
"Ra 164,24,41984\nAD 86,86,96\nCV 166,172,173\nCV 167,174,175\nWL 6\nEX 81,41\ns_add_u32 s5, s66, 4096\nSv 164,1,16,5,1024\nRa 168,24,43008\nAD 84,84,80\nAD 87,87,97\n"
"CV 208,208,209\nCV 209,210,211\nMvav 244,184,76\nD1 1,16,46,2048\nv_cmp_gt_u32 vcc, 0x100, v1\nv_mov_b32 v94, s15\nv_mov_b32 v95, s15\nv_cndmask_b32 v92, v89, v88, vcc\n"
"v_cndmask_b32 v93, v87, v86, vcc\nMvav 252,188,76\nRa 172,24,44032\nv_mov_b32 v86, 0\nCV 210,216,217\nCV 211,218,219\nAD 84,84,81\nCV 66,80,81\ns_add_u32 s5, s66, 16384\n"
"Sv 208,1,16,5,2048\nCvaa 48,128,192,4\nRa 176,26,40960\nv_mov_b32 v87, 0\nv_mov_b32 v88, 0\nCV 180,180,181\nMvaa 48,132,200\nEX 82,42\nCV 181,182,183\nMvaa 48,136,208\n"
"Ra 180,26,41984\nv_mov_b32 v89, 0\nCV 182,188,189\nCV 183,190,191\nMvaa 48,140,216\nWL 6\nEX 83,43\ns_add_u32 s5, s66, 4096\nSv 180,1,16,5,3072\nCvaa 52,140,220,8\n"
"Ra 184,26,43008\nAD 84,84,82\nCV 224,224,225\nMvaa 52,136,212\nD1 1,16,46,3072\ns_add_u32 m0, s89, 0\nCV 225,226,227\nMvaa 52,132,204\nRa 188,26,44032\nCV 226,232,233\n"
"CV 227,234,235\nMvaa 52,128,196\nAD 84,84,83\nCV 67,82,83\ns_add_u32 s5, s66, 20480\nSv 224,1,16,5,0\nMvaa 52,144,228\nRa 128,2,49152\nCV 196,196,197\nCV 197,198,199\n"
"CV 198,204,205\nMvaa 52,148,236\ns_add_u32 s46, s86, s88\nEX 80,44\nMvaa 52,152,244\nRa 132,2,50176\nv_xor_b32 v24, 0x10000, v24\nCV 199,206,207\nMvaa 52,156,252\nWL 6\nEX 81,45\n"
"s_add_u32 s5, s66, 16384\nSv 196,1,16,5,1024\nMvaa 48,156,248\nRa 136,2,51200\nAD 85,85,80\nCV 240,240,241\nMvaa 48,152,240\nD1 1,16,46,0\nCV 241,242,243\nMvaa 48,148,232\n"
"Ra 140,2,52224\nCV 242,248,249\nCV 243,250,251\nMvaa 48,144,224\nAD 85,85,81\nCV 70,80,81\ns_add_u32 s5, s66, 20480\nSv 240,1,16,5,2048\nCvaa 56,160,192,12\nRa 144,2,53248\n"
"CV 212,212,213\nCV 213,214,215\nMvaa 56,164,200\nEX 82,46\nCV 214,220,221\nMvaa 56,168,208\nRa 148,2,54272\nv_xor_b32 v26, 0x10000, v26\nCV 215,222,223\nMvaa 56,172,216\nWL 6\n"
"EX 83,47\ns_add_u32 s5, s66, 16384\nSv 212,1,16,5,3072\nCvaa 60,172,220,16\nRa 152,2,55296\nAD 85,85,82\nCV 228,228,229\nMvaa 60,168,212\nD1 1,16,46,1024\nCV 229,230,231\n"
"Mvaa 60,164,204\nRa 156,2,56320\nCV 230,236,237\nCV 231,238,239\nCV 244,244,245\nMvaa 60,160,196\nAD 85,85,83\nCV 71,82,83\ns_add_u32 s5, s66, 20480\nSv 228,1,16,5,1024\n"
"Mvaa 60,176,228\nRa 160,2,57344\nv_max_f32 v90, v84, v85\nCV 245,246,247\nCV 246,252,253\nMvaa 60,180,236\ns_add_u32 s88, s88, 32768\nEX 80,48\nCV 247,254,255\nMvaa 60,184,244\n"
"Ra 164,2,58368\nv_cmp_lt_f32 vcc, s63, v90\nMvaa 60,188,252\nWL 8\nEX 81,49\nMvaa 56,188,248\nRa 168,2,59392\ns_cbranch_vccnz .T465\n.T466:\nv_mov_b32 v20, v84\n"
"v_mov_b32 v21, v85\nv_mov_b32 v22, v20\nv_mov_b32 v23, v21\ns_add_u32 s5, s66, 20480\nSv 244,1,16,5,3072\nMvaa 56,184,240\nD1 1,16,46,2048\nv_permlane32_swap_b32 v20, v22\n"
"v_permlane32_swap_b32 v21, v23\nAD 20,20,22\nAD 21,21,23\nv_mov_b32 v22, v20\nv_mov_b32 v23, v21\nMvaa 56,180,232\nRa 172,2,60416\nv_permlane16_swap_b32 v20, v22\n"
"v_permlane16_swap_b32 v21, v23\nAD 20,20,22\nAD 21,21,23\nv_min_f32 v90, v20, v21\nv_cmp_gt_f32 vcc, s64, v90\ns_nop 0\ns_cbranch_vccnz .T467\n.T468:\nAD 86,86,84\n"
"Mvaa 56,176,224\nAD 84,80,81\nCV 72,80,81\nZvav 128,128,64\nRa 176,2,61440\nZvav 132,128,68\nEX 82,50\nZvav 140,132,68\nRa 180,2,62464\nZvav 136,132,64\n"
"s_waitcnt vmcnt(27) lgkmcnt(8)\nEX 83,51\nZvav 144,136,64\nRa 184,2,63488\nAD 84,84,82\nZvav 148,136,68\nD1 1,16,46,3072\ns_add_u32 m0, s89, 16384\nZvav 156,140,68\n"
"Ra 188,2,64512\ns_barrier\nZvav 152,140,64\nAD 84,84,83\nCV 73,82,83\nZvav 160,144,64\nRa 128,24,0\nZvav 164,144,68\ns_add_u32 s46, s87, s88\nEX 80,52\nZvav 172,148,68\n"
"Ra 132,24,1024\nv_xor_b32 v2, 0x10000, v2\nZvav 168,148,64\nWL 8\nEX 81,53\nZvav 176,152,64\nRa 136,24,2048\nAD 87,87,85\nZvav 180,152,68\nD1 1,16,46,0\nZvav 188,156,68\n"
"Ra 140,24,3072\nZvav 184,156,64\nAD 85,80,81\nCV 76,80,81\nZvav 192,160,64\nRa 144,26,0\nZvav 196,160,68\nEX 82,54\nZvav 204,164,68\nRa 148,26,1024\nZvav 200,164,64\nWL 8\n"
"EX 83,55\nZvav 208,168,64\nRa 152,26,2048\nAD 85,85,82\nZvav 212,168,68\nD1 1,16,46,1024\nZvav 220,172,68\nRa 156,26,3072\nZvav 216,172,64\nAD 85,85,83\nCV 77,82,83\n"
"Zvav 224,176,64\nRa 160,24,8192\nZvav 228,176,68\ns_sub_u32 s90, s90, 1\nEX 80,56\nZvav 236,180,68\nRa 164,24,9216\ns_cbranch_scc0 .E461\ns_swappc_b64 s[80:81], s[8:9]\n.E461:\n"
"Zvav 232,180,64\nWL 6\nEX 81,57\nZvav 240,184,64\nRa 168,24,10240\nAD 84,84,80\nZvav 244,184,68\nD1 1,16,46,2048\nZvav 252,188,68\nRa 172,24,11264\nZvav 248,188,64\nAD 84,84,81\n"
"CV 74,80,81\nv_mov_b32 v91, 0x40000000\nv_cmp_gt_u32 vcc, 0x200, v1\nWV 5\ns_nop 0\nv_cndmask_b32 v91, v91, v1, vcc\nSv 92,91,16,65,0\ns_sub_u32 s99, s99, 1\n.p2align 6\n.A469:\n"
"Cvaa 32,128,192,4\nRa 176,26,8192\nMvaa 32,132,200\ns_add_u32 s49, s49, 131072\nEX 82,58\nMvaa 32,136,208\nRa 180,26,9216\nMvaa 32,140,216\nWL 6\n.W584:\ns_cbranch_execz .WB411\n"
"EX 83,59\nCvaa 36,140,220,8\nRa 184,26,10240\nAD 84,84,82\nMvaa 36,136,212\nD1 1,16,46,3072\ns_add_u32 m0, s89, 32768\nMvaa 36,132,204\nRa 188,26,11264\nMvaa 36,128,196\n"
"AD 84,84,83\nCV 75,82,83\nMvaa 36,144,228\nRa 128,2,16384\nMvaa 36,148,236\ns_add_u32 s46, s86, s88\nEX 80,60\nMvaa 36,152,244\nRa 132,2,17408\nMvaa 36,156,252\nWL 6\nEX 81,61\n"
"Mvaa 32,156,248\nRa 136,2,18432\nAD 85,85,80\nMvaa 32,152,240\nD1 1,16,46,0\nMvaa 32,148,232\nRa 140,2,19456\nMvaa 32,144,224\nAD 85,85,81\nCV 78,80,81\nCvaa 40,160,192,12\n"
"Ra 144,2,20480\nMvaa 40,164,200\nSv 112,1,16,49,0\nEX 82,62\nMvaa 40,168,208\nRa 148,2,21504\nMvaa 40,172,216\nWL 6\nEX 83,63\nCvaa 44,172,220,16\nRa 152,2,22528\nAD 85,85,82\n"
"Mvaa 44,168,212\nD1 1,16,46,1024\nMvaa 44,164,204\nRa 156,2,23552\nMvaa 44,160,196\nAD 85,85,83\nCV 79,82,83\nMvaa 44,176,228\nRa 160,2,24576\nv_max_f32 v90, v84, v85\n"
"Mvaa 44,180,236\ns_add_u32 s88, s88, 32768\nEX 80,32\nMvaa 44,184,244\nRa 164,2,25600\nv_cmp_lt_f32 vcc, s63, v90\nMvaa 44,188,252\nWL 8\nEX 81,33\nMvaa 40,188,248\n"
"Ra 168,2,26624\ns_cbranch_vccnz .T477\n.T478:\nMvaa 40,184,240\nD1 1,16,46,2048\nMvaa 40,180,232\nRa 172,2,27648\nAD 86,86,84\nMvaa 40,176,224\nAD 84,80,81\nCV 64,80,81\n"
"Mvav 128,128,72\nRa 176,2,28672\nMvav 132,128,76\nEX 82,34\nMvav 140,132,76\nRa 180,2,29696\nMvav 136,132,72\ns_waitcnt vmcnt(13) lgkmcnt(8)\nEX 83,35\nMvav 144,136,72\n"
"Ra 184,2,30720\nAD 84,84,82\nMvav 148,136,76\nD1 1,16,46,3072\ns_add_u32 m0, s89, 49152\nMvav 156,140,76\nRa 188,2,31744\ns_barrier\nMvav 152,140,72\nAD 84,84,83\nCV 65,82,83\n"
"Mvav 160,144,72\nRa 128,24,32768\nMvav 164,144,76\ns_add_u32 s46, s87, s88\nEX 80,36\nMvav 172,148,76\nRa 132,24,33792\nMvav 168,148,72\nWL 8\nEX 81,37\nMvav 176,152,72\n"
"Ra 136,24,34816\nAD 87,87,85\nMvav 180,152,76\nD1 1,16,46,0\nMvav 188,156,76\nRa 140,24,35840\nMvav 184,156,72\nAD 85,80,81\nCV 68,80,81\nMvav 192,160,72\nRa 144,26,32768\n"
"Mvav 196,160,76\ns_cmp_eq_u32 s99, 0\nEX 82,38\nMvav 204,164,76\nRa 148,26,33792\ns_cbranch_scc1 .E471\n.E472:\nMvav 200,164,72\nWL 8\nEX 83,39\nMvav 208,168,72\nRa 152,26,34816\n"
"AD 85,85,82\nMvav 212,168,76\nD1 1,16,46,1024\nMvav 220,172,76\nRa 156,26,35840\nMvav 216,172,72\nAD 85,85,83\nCV 69,82,83\nMvav 224,176,72\nRa 160,24,40960\nMvav 228,176,76\n"
"s_xor_b32 s89, s89, 0x10000\nEX 80,40\nMvav 236,180,76\nRa 164,24,41984\nMvav 232,180,72\nWL 6\nEX 81,41\nMvav 240,184,72\nRa 168,24,43008\nAD 84,84,80\nMvav 244,184,76\n"
"D1 1,16,46,2048\nMvav 252,188,76\nRa 172,24,44032\nMvav 248,188,72\nAD 84,84,81\nCV 66,80,81\nCvaa 48,128,192,4\nRa 176,26,40960\nMvaa 48,132,200\ns_cmp_eq_u32 s99, 0\nEX 82,42\n"
"Mvaa 48,136,208\nRa 180,26,41984\ns_cselect_b32 s86, s94, s86\nMvaa 48,140,216\nWL 6\nEX 83,43\nCvaa 52,140,220,8\nRa 184,26,43008\nAD 84,84,82\nMvaa 52,136,212\nD1 1,16,46,3072\n"
"s_add_u32 m0, s89, 0\nMvaa 52,132,204\nRa 188,26,44032\nMvaa 52,128,196\nAD 84,84,83\nCV 67,82,83\nMvaa 52,144,228\nRa 128,2,49152\nMvaa 52,148,236\ns_add_u32 s46, s86, s88\n"
"EX 80,44\nMvaa 52,152,244\nRa 132,2,50176\nv_xor_b32 v24, 0x10000, v24\nMvaa 52,156,252\nWL 6\nEX 81,45\nMvaa 48,156,248\nRa 136,2,51200\nAD 85,85,80\nMvaa 48,152,240\n"
"D1 1,16,46,0\nMvaa 48,148,232\nRa 140,2,52224\nMvaa 48,144,224\nAD 85,85,81\nCV 70,80,81\nCvaa 56,160,192,12\nRa 144,2,53248\nMvaa 56,164,200\nSv 112,1,16,49,1024\nEX 82,46\n"
"Mvaa 56,168,208\nRa 148,2,54272\nv_xor_b32 v26, 0x10000, v26\nMvaa 56,172,216\nWL 6\nEX 83,47\nCvaa 60,172,220,16\nRa 152,2,55296\nAD 85,85,82\nMvaa 60,168,212\nD1 1,16,46,1024\n"
"Mvaa 60,164,204\nRa 156,2,56320\nMvaa 60,160,196\nAD 85,85,83\nCV 71,82,83\nMvaa 60,176,228\nRa 160,2,57344\nv_max_f32 v90, v84, v85\nMvaa 60,180,236\ns_add_u32 s88, s88, 32768\n"
"EX 80,48\nMvaa 60,184,244\nRa 164,2,58368\nv_cmp_lt_f32 vcc, s63, v90\nMvaa 60,188,252\nWL 8\nEX 81,49\nMvaa 56,188,248\nRa 168,2,59392\ns_cbranch_vccnz .T479\n.T480:\n"
"Mvaa 56,184,240\nD1 1,16,46,2048\nMvaa 56,180,232\nRa 172,2,60416\nAD 86,86,84\nMvaa 56,176,224\nAD 84,80,81\nCV 72,80,81\nMvav 128,128,64\nRa 176,2,61440\nMvav 132,128,68\n"
"EX 82,50\nMvav 140,132,68\nRa 180,2,62464\nMvav 136,132,64\ns_waitcnt vmcnt(13) lgkmcnt(8)\nEX 83,51\nMvav 144,136,64\nRa 184,2,63488\nAD 84,84,82\nMvav 148,136,68\n"
"D1 1,16,46,3072\ns_add_u32 m0, s89, 16384\nMvav 156,140,68\nRa 188,2,64512\ns_barrier\nMvav 152,140,64\nAD 84,84,83\nCV 73,82,83\nMvav 160,144,64\nRa 128,24,0\nMvav 164,144,68\n"
"s_add_u32 s46, s87, s88\nEX 80,52\nMvav 172,148,68\nRa 132,24,1024\nv_xor_b32 v2, 0x10000, v2\nMvav 168,148,64\nWL 8\nEX 81,53\nMvav 176,152,64\nRa 136,24,2048\nAD 87,87,85\n"
"Mvav 180,152,68\nD1 1,16,46,0\nMvav 188,156,68\nRa 140,24,3072\nMvav 184,156,64\nAD 85,80,81\nCV 76,80,81\nMvav 192,160,64\nRa 144,26,0\nMvav 196,160,68\ns_cmp_lt_u32 s99, s92\n"
"EX 82,54\nMvav 204,164,68\nRa 148,26,1024\ns_cbranch_scc1 .E473\n.E474:\nMvav 200,164,64\nWL 8\nEX 83,55\nMvav 208,168,64\nRa 152,26,2048\nAD 85,85,82\nMvav 212,168,68\n"
"D1 1,16,46,1024\nMvav 220,172,68\nRa 156,26,3072\nMvav 216,172,64\nAD 85,85,83\nCV 77,82,83\nMvav 224,176,64\nRa 160,24,8192\nMvav 228,176,68\ns_sub_u32 s90, s90, 1\nEX 80,56\n"
"Mvav 236,180,68\nRa 164,24,9216\ns_cbranch_scc1 .E475\n.E476:\nMvav 232,180,64\nWL 6\nEX 81,57\nMvav 240,184,64\nRa 168,24,10240\nAD 84,84,80\nMvav 244,184,68\nD1 1,16,46,2048\n"
"s_sub_u32 s99, s99, 1\nMvav 252,188,68\nRa 172,24,11264\nMvav 248,188,64\nAD 84,84,81\nCV 74,80,81\ns_cbranch_scc0 .A469\ns_branch .A481\n.E471:\nv_cmpx_gt_i32 vcc, 19, v0\n"
"v_mov_b32 v15, s97\nv_cmpx_gt_i32 vcc, 18, v0\nv_mov_b32 v14, s97\nv_cmpx_gt_i32 vcc, 17, v0\nv_mov_b32 v13, s97\nv_cmpx_gt_i32 vcc, 16, v0\nv_mov_b32 v12, s97\n"
"v_cmpx_gt_i32 vcc, 3, v0\nv_mov_b32 v7, s97\nv_cmpx_gt_i32 vcc, 2, v0\nv_mov_b32 v6, s97\nv_cmpx_gt_i32 vcc, 1, v0\nv_mov_b32 v5, s97\nv_cmpx_gt_i32 vcc, 0, v0\nv_mov_b32 v4, s97\n"
"s_mov_b64 exec, -1\ns_nop 2\ns_branch .E472\n.E473:\ns_cmp_eq_u32 s99, 0\ns_cbranch_scc1 .K470\nv_cmpx_gt_i32 vcc, 19, v3\nv_mov_b32 v15, s97\nv_cmpx_gt_i32 vcc, 18, v3\n"
"v_mov_b32 v14, s97\nv_cmpx_gt_i32 vcc, 17, v3\nv_mov_b32 v13, s97\nv_cmpx_gt_i32 vcc, 16, v3\nv_mov_b32 v12, s97\nv_cmpx_gt_i32 vcc, 3, v3\nv_mov_b32 v7, s97\n"
"v_cmpx_gt_i32 vcc, 2, v3\nv_mov_b32 v6, s97\nv_cmpx_gt_i32 vcc, 1, v3\nv_mov_b32 v5, s97\nv_cmpx_gt_i32 vcc, 0, v3\nv_mov_b32 v4, s97\ns_mov_b64 exec, -1\ns_nop 2\ns_branch .E474\n"
".K470:\nv_cmpx_gt_i32 vcc, 19, v3\nv_mov_b32 v19, s97\nv_cmpx_gt_i32 vcc, 18, v3\nv_mov_b32 v18, s97\nv_cmpx_gt_i32 vcc, 17, v3\nv_mov_b32 v17, s97\nv_cmpx_gt_i32 vcc, 16, v3\n"
"v_mov_b32 v16, s97\nv_cmpx_gt_i32 vcc, 3, v3\nv_mov_b32 v11, s97\nv_cmpx_gt_i32 vcc, 2, v3\nv_mov_b32 v10, s97\nv_cmpx_gt_i32 vcc, 1, v3\nv_mov_b32 v9, s97\n"
"v_cmpx_gt_i32 vcc, 0, v3\nv_mov_b32 v8, s97\ns_mov_b64 exec, -1\ns_nop 2\ns_branch .E474\n.E475:\ns_swappc_b64 s[80:81], s[8:9]\ns_branch .E476\n.A481:\n.LE413:\nRa 176,26,8192\n"
"EX 82,58\nRa 180,26,9216\nWL 6\nEX 83,59\nCvaa 36,140,220,8\nRa 184,26,10240\nAD 84,84,82\nMvaa 36,136,212\nD1 1,16,46,3072\ns_add_u32 m0, s89, 32768\nMvaa 36,132,204\n"
"Ra 188,26,11264\nMvaa 36,128,196\nAD 84,84,83\nCV 75,82,83\nMvaa 36,144,228\nRa 128,2,16384\nMvaa 36,148,236\ns_add_u32 s46, s86, s88\nEX 80,60\nMvaa 36,152,244\nRa 132,2,17408\n"
"Mvaa 36,156,252\nEX 81,61\nRa 136,2,18432\nAD 85,85,80\nD1 1,16,46,0\nRa 140,2,19456\nAD 85,85,81\nCV 78,80,81\nRa 144,2,20480\ns_mov_b32 s87, s95\nEX 82,62\nRa 148,2,21504\nWL 6\n"
"EX 83,63\nCvaa 44,172,220,16\nRa 152,2,22528\nAD 85,85,82\nMvaa 44,168,212\nD1 1,16,46,1024\nMvaa 44,164,204\nRa 156,2,23552\nMvaa 44,160,196\nAD 85,85,83\nCV 79,82,83\n"
"Mvaa 44,176,228\nRa 160,2,24576\nv_max_f32 v90, v84, v85\nMvaa 44,180,236\ns_add_u32 s88, s88, 32768\nMvaa 44,184,244\nRa 164,2,25600\nv_cmp_lt_f32 vcc, s63, v90\nMvaa 44,188,252\n"
"WL 8\nRa 168,2,26624\ns_cbranch_vccnz .T483\n.T484:\nD1 1,16,46,2048\nRa 172,2,27648\nAD 86,86,84\nMvav 128,128,72\nRa 176,2,28672\nMvav 132,128,76\nMvav 140,132,76\n"
"Ra 180,2,29696\nMvav 136,132,72\ns_waitcnt vmcnt(12) lgkmcnt(8)\nMvav 144,136,72\nRa 184,2,30720\nMvav 148,136,76\nD1 1,16,46,3072\ns_add_u32 m0, s89, 49152\nMvav 156,140,76\n"
"Ra 188,2,31744\ns_barrier\nMvav 152,140,72\nMvav 160,144,72\nRa 128,24,32768\nMvav 164,144,76\ns_add_u32 s46, s87, s88\nEX 80,36\nMvav 172,148,76\nRa 132,24,33792\n"
"Mvav 168,148,72\nWL 8\nEX 81,37\nMvav 176,152,72\nRa 136,24,34816\nAD 87,87,85\nMvav 180,152,76\nD1 1,16,46,0\nMvav 188,156,76\nRa 140,24,35840\nMvav 184,156,72\nAD 85,80,81\n"
"CV 68,80,81\nMvav 192,160,72\nRa 144,26,32768\nMvav 196,160,76\ns_nop 0\nEX 82,38\nMvav 204,164,76\nRa 148,26,33792\nv_cmpx_gt_i32 vcc, 19, v0\nv_mov_b32 v19, s97\n"
"v_cmpx_gt_i32 vcc, 18, v0\nv_mov_b32 v18, s97\nv_cmpx_gt_i32 vcc, 17, v0\nv_mov_b32 v17, s97\nv_cmpx_gt_i32 vcc, 16, v0\nv_mov_b32 v16, s97\nv_cmpx_gt_i32 vcc, 3, v0\n"
"v_mov_b32 v11, s97\nv_cmpx_gt_i32 vcc, 2, v0\nv_mov_b32 v10, s97\nv_cmpx_gt_i32 vcc, 1, v0\nv_mov_b32 v9, s97\nv_cmpx_gt_i32 vcc, 0, v0\nv_mov_b32 v8, s97\ns_mov_b64 exec, -1\n"
"s_nop 2\ns_nop 0\nMvav 200,164,72\nWL 8\nEX 83,39\nMvav 208,168,72\nRa 152,26,34816\nAD 85,85,82\nMvav 212,168,76\nD1 1,16,46,1024\nMvav 220,172,76\nRa 156,26,35840\n"
"Mvav 216,172,72\nAD 85,85,83\nCV 69,82,83\nMvav 224,176,72\nRa 160,24,40960\nMvav 228,176,76\ns_xor_b32 s89, s89, 0x10000\nMvav 236,180,76\nRa 164,24,41984\nMvav 232,180,72\n"
"WL 10\nMvav 240,184,72\nRa 168,24,43008\nMvav 244,184,76\nD1 1,16,46,2048\nMvav 252,188,76\nRa 172,24,44032\nMvav 248,188,72\nRa 176,26,40960\nRa 180,26,41984\nWL 6\n"
"Cvaa 52,140,220,8\nRa 184,26,43008\nMvaa 52,136,212\nD1 1,16,46,3072\ns_add_u32 m0, s89, 0\nMvaa 52,132,204\nRa 188,26,44032\nMvaa 52,128,196\nMvaa 52,144,228\nRa 128,2,49152\n"
"Mvaa 52,148,236\ns_add_u32 s46, s86, s88\nEX 80,44\nMvaa 52,152,244\nRa 132,2,50176\nv_xor_b32 v24, 0x10000, v24\nMvaa 52,156,252\nEX 81,45\nRa 136,2,51200\nAD 85,85,80\n"
"D1 1,16,46,0\nRa 140,2,52224\nAD 85,85,81\nCV 70,80,81\nRa 144,2,53248\nEX 82,46\nRa 148,2,54272\nv_xor_b32 v26, 0x10000, v26\nWL 6\nEX 83,47\nCvaa 60,172,220,16\nRa 152,2,55296\n"
"AD 85,85,82\nMvaa 60,168,212\nD1 1,16,46,1024\nMvaa 60,164,204\nRa 156,2,56320\nMvaa 60,160,196\nAD 85,85,83\nCV 71,82,83\nMvaa 60,176,228\nRa 160,2,57344\nMvaa 60,180,236\n"
"s_add_u32 s88, s88, 32768\nMvaa 60,184,244\nRa 164,2,58368\nv_cmp_lt_f32 vcc, s63, v85\nMvaa 60,188,252\nWL 8\nRa 168,2,59392\ns_cbranch_vccnz .T485\n.T486:\nD1 1,16,46,2048\n"
"Ra 172,2,60416\nRa 176,2,61440\nMvav 132,128,68\nMvav 140,132,68\nRa 180,2,62464\ns_waitcnt vmcnt(11) lgkmcnt(8)\nRa 184,2,63488\nMvav 148,136,68\nD1 1,16,46,3072\n"
"s_add_u32 m0, s89, 16384\nMvav 156,140,68\nRa 188,2,64512\ns_barrier\nRa 128,24,0\nMvav 164,144,68\ns_add_u32 s46, s87, s88\nEX 80,52\nMvav 172,148,68\nRa 132,24,1024\n"
"v_xor_b32 v2, 0x10000, v2\nWL 8\nEX 81,53\nRa 136,24,2048\nAD 87,87,85\nMvav 180,152,68\nD1 1,16,46,0\nMvav 188,156,68\nRa 140,24,3072\nAD 85,80,81\nCV 76,80,81\nRa 144,26,0\n"
"Mvav 196,160,68\nEX 82,54\nMvav 204,164,68\n.W585:\ns_cbranch_execz .WB411\nRa 148,26,1024\nWL 8\nEX 83,55\nRa 152,26,2048\nAD 85,85,82\nMvav 212,168,68\nD1 1,16,46,1024\n"
"Mvav 220,172,68\nRa 156,26,3072\nAD 85,85,83\nCV 77,82,83\nRa 160,24,8192\nMvav 228,176,68\nMvav 236,180,68\nRa 164,24,9216\nWL 10\nRa 168,24,10240\nMvav 244,184,68\n"
"D1 1,16,46,2048\nMvav 252,188,68\nRa 172,24,11264\nD1 1,16,46,3072\ns_add_u32 m0, s89, 32768\ns_add_u32 s46, s86, s88\nRa 128,2,16384\nv_xor_b32 v3, 0x10000, v2\n"
"v_add_u32 v0, 0x20000, v1\nEX 80,60\nRa 132,2,17408\ns_sub_u32 s94, s49, 1703936\nEX 81,61\nRa 136,2,18432\nAD 85,85,80\ns_xor_b32 s92, s94, 0x202000\nD1 1,16,46,0\n"
"s_lshl_b32 s89, s11, 12\nRa 140,2,19456\ns_lshl1_add_u32 s2, s12, 1\nAD 85,85,81\nCV 78,80,81\nRa 144,2,20480\ns_lshl3_add_u32 s2, s2, s13\nEX 82,62\nRa 148,2,21504\n"
"s_lshl4_add_u32 s2, s2, 0xc048200\nEX 83,63\nRa 152,2,22528\nAD 85,85,82\nD1 1,16,46,1024\nRa 156,2,23552\ns_and_b32 s3, s72, 0x2000\nAD 85,85,83\nCV 79,82,83\nRa 160,2,24576\n"
"s_cmp_eq_u32 s11, 1\ns_cselect_b32 s54, s2, s54\ns_cselect_b32 s3, s3, 0\nv_cmp_lt_f32 vcc, s63, v85\nRa 164,2,25600\ns_cbranch_vccnz .T487\n.T488:\nAD 87,87,85\nWL 8\n"
"v_mov_b32 v84, v86\nv_mov_b32 v85, v87\nRa 168,2,26624\nv_permlane32_swap_b32 v86, v84\nv_permlane32_swap_b32 v87, v85\nD1 1,16,46,2048\nAD 86,86,84\nRa 172,2,27648\nAD 87,87,85\n"
"v_mov_b32 v84, v86\nv_mov_b32 v85, v87\nRa 176,2,28672\nv_permlane16_swap_b32 v86, v84\nv_permlane16_swap_b32 v87, v85\nMvav 132,128,76\nAD 86,86,84\nAD 87,87,85\nMvav 140,132,76\n"
"Ra 180,2,29696\nv_rcp_f32 v86, v86\nWL 8\nv_rcp_f32 v87, v87\nRa 184,2,30720\nv_xor_b32 v25, s3, v25\nMvav 148,136,76\nD1 1,16,46,3072\nMvav 156,140,76\nRa 188,2,31744\n"
"MU 128,128,86\nMU 129,129,86\nCV 96,128,129\nMU 130,130,86\nMU 131,131,86\nRa 128,2,0\nCV 97,130,131\nMU 132,132,87\nMvav 164,144,76\nMU 133,133,87\nCV 100,132,133\n"
"Mvav 172,148,76\nRa 132,2,1024\nMU 134,134,87\nMU 135,135,87\nWL 8\ns_add_u32 m0, s89, 131072\ns_add_u32 s46, s96, 1212416\nRa 136,2,2048\nCV 101,134,135\nMU 136,136,86\n"
"Mvav 180,152,76\nD1 1,16,46,0\nMU 137,137,86\nMvav 188,156,76\nRa 140,2,3072\nCV 98,136,137\nMU 138,138,86\nMU 139,139,86\nCV 99,138,139\nMU 140,140,87\nRa 144,2,4096\n"
"MU 141,141,87\nCV 102,140,141\nMvav 196,160,76\nMU 142,142,87\nMU 143,143,87\nMvav 204,164,76\nRa 148,2,5120\nCV 103,142,143\nMU 144,144,86\nWL 8\nMU 145,145,86\nCV 104,144,145\n"
"Ra 152,2,6144\nMU 146,146,86\nMU 147,147,86\nMvav 212,168,76\nD1 1,16,46,1024\nCV 105,146,147\nMvav 220,172,76\nRa 156,2,7168\nMU 148,148,87\nMU 149,149,87\nCV 108,148,149\n"
"MU 150,150,87\nMU 151,151,87\nRa 160,2,8192\nCV 109,150,151\nMU 152,152,86\nMvav 228,176,76\nMU 153,153,86\nCV 106,152,153\nMvav 236,180,76\nRa 164,2,9216\nMU 154,154,86\n"
"MU 155,155,86\nWL 8\nCV 107,154,155\nMU 156,156,87\nRa 168,2,10240\nMU 157,157,87\nCV 110,156,157\nMvav 244,184,76\nD1 1,16,46,2048\nMU 158,158,87\nMvav 252,188,76\n"
"Ra 172,2,11264\nMU 159,159,87\nCV 111,158,159\nMU 160,160,86\nMU 161,161,86\nCV 112,160,161\nZvav 128,128,96\nRa 176,2,12288\nMU 162,162,86\nMU 163,163,86\nMvav 128,132,104\n"
"CV 113,162,163\nMU 164,164,87\nZvav 132,132,108\nRa 180,2,13312\nMU 165,165,87\nCV 116,164,165\nMvav 132,128,100\ns_waitcnt vmcnt(15) lgkmcnt(8)\nMU 166,166,87\nMU 167,167,87\n"
"Zvav 140,136,100\nRa 184,2,14336\nCV 117,166,167\nMU 168,168,86\nMvav 140,140,108\nD1 1,16,46,3072\nMU 169,169,86\nCV 114,168,169\nZvav 136,140,104\nRa 188,2,15360\ns_barrier\n"
"MU 170,170,86\nMvav 136,136,96\nMU 171,171,86\nCV 115,170,171\nMU 172,172,87\nZvav 144,144,96\nRa 128,2,32768\nMU 173,173,87\nCV 118,172,173\nMvav 144,148,104\nMU 174,174,87\n"
"MU 175,175,87\nbuffer_store_dword v25, off, s[16:19], s54 offset:512 sc0 sc1\nZvav 148,148,108\nRa 132,2,33792\nCV 119,174,175\nMU 176,176,86\nMvav 148,144,100\nWL 8\n"
"s_add_u32 m0, s89, 49152\ns_add_u32 s46, s96, 1179648\nZvav 156,152,100\nRa 136,2,34816\nMU 177,177,86\nCV 120,176,177\nMvav 156,156,108\nD1 1,16,46,0\nMU 178,178,86\n"
"MU 179,179,86\nZvav 152,156,104\nRa 140,2,35840\nCV 121,178,179\nMU 180,180,87\nMvav 152,152,96\nMU 181,181,87\nCV 124,180,181\nMU 182,182,87\nZvav 64,160,96\nRa 144,2,36864\n"
"MU 183,183,87\nCV 125,182,183\nMvav 64,164,104\nMU 184,184,86\nMU 185,185,86\nZvav 68,164,108\nRa 148,2,37888\nCV 122,184,185\nMU 186,186,86\nMvav 68,160,100\nWL 8\nMU 187,187,86\n"
"CV 123,186,187\nZvav 76,168,100\nRa 152,2,38912\nMU 188,188,87\nMU 189,189,87\nMvav 76,172,108\nD1 1,16,46,1024\nCV 126,188,189\nMU 190,190,87\nZvav 72,172,104\nRa 156,2,39936\n"
"MU 191,191,87\nCV 127,190,191\nMvav 72,168,96\nMU 192,192,86\nMU 193,193,86\nCV 32,192,193\nMvav 128,176,112\nRa 160,2,40960\nMU 194,194,86\nMU 195,195,86\nMvav 128,180,120\n"
"CV 33,194,195\nMU 196,196,87\nMU 197,197,87\nMvav 132,180,124\nRa 164,2,41984\nCV 36,196,197\nMU 198,198,87\nMvav 132,176,116\nWL 8\nMU 199,199,87\nMvav 140,184,116\n"
"Ra 168,2,43008\nCV 37,198,199\nMU 200,200,86\nMvav 140,188,124\nD1 1,16,46,2048\nMU 201,201,86\nCV 34,200,201\nMvav 136,188,120\nRa 172,2,44032\nMU 202,202,86\nMU 203,203,86\n"
"Mvav 136,184,112\nCV 35,202,203\nMU 204,204,87\nMU 205,205,87\nMvav 144,128,112\nRa 176,2,45056\nCV 38,204,205\nMU 206,206,87\nMvav 144,132,120\nMU 207,207,87\nCV 39,206,207\n"
"Mvav 148,132,124\nRa 180,2,46080\nMU 208,208,86\nMU 209,209,86\nMvav 148,128,116\nWL 8\nCV 40,208,209\nMU 210,210,86\nMvav 156,136,116\nRa 184,2,47104\nMU 211,211,86\n"
"CV 41,210,211\nMvav 156,140,124\nD1 1,16,46,3072\nMU 212,212,87\nMU 213,213,87\nMvav 152,140,120\nRa 188,2,48128\nCV 44,212,213\nMvav 152,136,112\nMU 214,214,87\nMU 215,215,87\n"
"CV 45,214,215\nMvav 64,144,112\nRa 128,2,49152\nMU 216,216,86\nMU 217,217,86\nMvav 64,148,120\nCV 42,216,217\nMU 218,218,86\nMU 219,219,86\nMvav 68,148,124\nRa 132,2,50176\n"
"CV 43,218,219\nMU 220,220,87\nMvav 68,144,116\nWL 8\ns_add_u32 m0, s89, 147456\ns_add_u32 s46, s96, 1245184\nMvav 76,152,116\nRa 136,2,51200\nMU 221,221,87\nCV 46,220,221\n"
"Mvav 76,156,124\nD1 1,16,46,0\nMU 222,222,87\nMU 223,223,87\nMvav 72,156,120\nRa 140,2,52224\nCV 47,222,223\nMvav 72,152,112\nMU 224,224,86\nMU 225,225,86\nCV 48,224,225\n"
"Mvav 128,160,32\nRa 144,2,53248\nMU 226,226,86\nMU 227,227,86\nMvav 128,164,40\nCV 49,226,227\nMU 228,228,87\nMvav 132,164,44\nRa 148,2,54272\nMU 229,229,87\nCV 52,228,229\n"
"Mvav 132,160,36\nWL 8\nMU 230,230,87\nMU 231,231,87\nMvav 140,168,36\nRa 152,2,55296\nCV 53,230,231\nMU 232,232,86\nMvav 140,172,44\nD1 1,16,46,1024\nMU 233,233,86\nCV 50,232,233\n"
"Mvav 136,172,40\nRa 156,2,56320\nMU 234,234,86\nMvav 136,168,32\nMU 235,235,86\nCV 51,234,235\nMU 236,236,87\nMvav 144,176,32\nRa 160,2,57344\nMU 237,237,87\nCV 54,236,237\n"
"Mvav 144,180,40\nMU 238,238,87\nMU 239,239,87\nCV 55,238,239\nMvav 148,180,44\nRa 164,2,58368\nMU 240,240,86\nMU 241,241,86\nMvav 148,176,36\nWL 8\nCV 56,240,241\nMvav 156,184,36\n"
"Ra 168,2,59392\nMU 242,242,86\nMU 243,243,86\nMvav 156,188,44\nD1 1,16,46,2048\nCV 57,242,243\nMU 244,244,87\nMvav 152,188,40\nRa 172,2,60416\nMU 245,245,87\nCV 60,244,245\n"
"Mvav 152,184,32\nMU 246,246,87\nMU 247,247,87\nCV 61,246,247\nMvav 64,128,32\nRa 176,2,61440\nMU 248,248,86\nMU 249,249,86\nMvav 64,132,40\nCV 58,248,249\nMU 250,250,86\n"
"Mvav 68,132,44\nRa 180,2,62464\nMU 251,251,86\nCV 59,250,251\nMvav 68,128,36\ns_waitcnt vmcnt(16) lgkmcnt(8)\nMU 252,252,87\nMU 253,253,87\nMvav 76,136,36\nRa 184,2,63488\n"
"CV 62,252,253\nMU 254,254,87\nMvav 76,140,44\nD1 1,16,46,3072\nMU 255,255,87\nCV 63,254,255\nMvav 72,140,40\nRa 188,2,64512\ns_barrier\nMvav 72,136,32\nMvav 128,144,48\n"
"Ra 128,3,0\nMvav 128,148,56\nMvav 132,148,60\nRa 132,3,1024\nMvav 132,144,52\nWL 8\nMvav 140,152,52\nRa 136,3,2048\nMvav 140,156,60\nMvav 136,156,56\nRa 140,3,3072\n"
"Mvav 136,152,48\nMvav 144,160,48\nRa 144,3,4096\nMvav 144,164,56\nMvav 148,164,60\nRa 148,3,5120\nCV 132,132,133\nMvav 148,160,52\nWL 8\nCV 133,134,135\nMvav 156,168,52\n"
"Ra 152,3,6144\nCV 134,140,141\nMvav 156,172,60\nCV 135,142,143\ns_add_u32 s5, s94, 0\nMvav 152,172,56\nRa 156,3,7168\nSv 132,1,16,5,1024\nMvav 152,168,48\nCV 128,128,129\n"
"CV 129,130,131\nMvav 64,176,48\nRa 160,3,8192\nCV 130,136,137\nMvav 64,180,56\nbuffer_load_dword v27, off, s[16:19], s60 offset:512 sc0 sc1\nCV 131,138,139\nMvav 68,180,60\n"
"Ra 164,3,9216\nSv 128,1,16,5,0\nMvav 68,176,52\nWL 6\nCV 148,148,149\nMvav 76,184,52\nRa 168,3,10240\nCV 149,150,151\nMvav 76,188,60\nCV 150,156,157\nCV 151,158,159\n"
"Mvav 72,188,56\nRa 172,3,11264\ns_add_u32 s5, s94, 131072\nMvav 72,184,48\nSv 148,1,16,5,1024\nCV 144,144,145\nZvav 160,128,96\nRa 176,3,12288\nCV 145,146,147\nMvav 160,132,104\n"
"CV 146,152,153\nCV 147,154,155\nMvav 160,136,112\nRa 180,3,13312\nSv 144,1,16,5,0\nMvav 160,140,120\nWL 6\nCV 68,68,69\nZvav 164,140,124\nRa 184,3,14336\nCV 69,70,71\n"
"Mvav 164,136,116\nCV 70,76,77\nCV 71,78,79\nMvav 164,132,108\nRa 188,3,15360\ns_add_u32 s5, s94, 262144\nMvav 164,128,100\nSv 68,1,16,5,1024\nCV 64,64,65\nMvav 164,144,36\n"
"Ra 128,3,16384\nCV 65,66,67\nMvav 164,148,44\nCV 66,72,73\nCV 67,74,75\nMvav 164,152,52\nRa 132,3,17408\nSv 64,1,16,5,0\nMvav 164,156,60\nWL 6\nMvav 160,156,56\nRa 136,3,18432\n"
"Mvav 160,152,48\nMvav 160,148,40\nRa 140,3,19456\nMvav 160,144,32\nZvav 168,160,96\nRa 144,3,20480\nMvav 168,164,104\nMvav 168,168,112\nRa 148,3,21504\nMvav 168,172,120\nWL 6\n"
"Zvav 172,172,124\nRa 152,3,22528\nMvav 172,168,116\nMvav 172,164,108\nRa 156,3,23552\nMvav 172,160,100\nMvav 172,176,36\nRa 160,3,24576\nMvav 172,180,44\nMvav 172,184,52\n"
"Ra 164,3,25600\nMvav 172,188,60\nWL 6\nMvav 168,188,56\nRa 168,3,26624\nMvav 168,184,48\nMvav 168,180,40\nRa 172,3,27648\nMvav 168,176,32\nZvav 176,128,96\nRa 176,3,28672\n"
"CV 164,164,165\nMvav 176,132,104\nCV 165,166,167\nCV 166,172,173\nMvav 176,136,112\nRa 180,3,29696\nCV 167,174,175\nMvav 176,140,120\ns_waitcnt vmcnt(11) lgkmcnt(6)\n"
"s_add_u32 s5, s94, 393216\nZvav 180,140,124\nRa 184,3,30720\nSv 164,1,16,5,1024\nMvav 180,136,116\nCV 160,160,161\nCV 161,162,163\nMvav 180,132,108\nRa 188,3,31744\ns_barrier\n"
"Mvav 180,128,100\nCV 162,168,169\nCV 163,170,171\nMvav 180,144,36\nRa 128,3,32768\nSv 160,1,16,5,0\nMvav 180,148,44\nMvav 180,152,52\nRa 132,3,33792\nMvav 180,156,60\nWL 6\n"
"s_add_u32 m0, s89, 81920\ns_add_u32 s46, s96, 1277952\nMvav 176,156,56\nRa 136,3,34816\n.W586:\ns_cbranch_execz .WB411\nMvav 176,152,48\nD1 1,16,46,0\nMvav 176,148,40\n"
"Ra 140,3,35840\nMvav 176,144,32\nZvav 184,160,96\nRa 144,3,36864\nMvav 184,164,104\nMvav 184,168,112\nRa 148,3,37888\nMvav 184,172,120\nWL 6\nZvav 188,172,124\nRa 152,3,38912\n"
"Mvav 188,168,116\nD1 1,16,46,1024\nMvav 188,164,108\nRa 156,3,39936\nMvav 188,160,100\nMvav 188,176,36\nRa 160,3,40960\nWV 9\nv_cmp_ne_u32 vcc, s15, v27\ns_nop 0\n"
"s_cbranch_vccnz .S513\n.S514:\nMvav 188,180,44\ns_add_u32 s4, s92, 0\nMvav 188,184,52\nRa 164,3,41984\nMvav 188,188,60\nWL 6\n"
"buffer_load_dwordx4 v[208:211], v1, s[16:19], s4 offen offset:0 sc0 sc1\nMvav 184,188,56\nRa 168,3,43008\nMvav 184,184,48\nD1 1,16,46,2048\nMvav 184,180,40\nRa 172,3,44032\n"
"Mvav 184,176,32\nbuffer_load_dwordx4 v[212:215], v1, s[16:19], s4 offen offset:1024 sc0 sc1\nZvav 192,128,96\nRa 176,3,45056\nv_lshrrev_b32 v88, 8, v1\nMvav 192,132,104\n"
"v_lshlrev_b32 v89, 5, v88\nCV 180,180,181\nMvav 192,136,112\nRa 180,3,46080\nCV 181,182,183\nMvav 192,140,120\nWL 6\nCV 182,188,189\nZvav 196,140,124\nRa 184,3,47104\n"
"v_bfe_u32 v90, v1, 4, 4\nMvav 196,136,116\nD1 1,16,46,3072\nCV 183,190,191\nMvav 196,132,108\nRa 188,3,48128\ns_add_u32 s5, s94, 524288\nMvav 196,128,100\n"
"v_mul_u32_u24 v91, 896, v90\nSv 180,1,16,5,1024\nMvav 196,144,36\nRa 128,3,49152\nv_lshl_add_u32 v91, v88, 4, v91\nMvav 196,148,44\nv_add_u32 v90, s21, v90\nCV 176,176,177\n"
"Mvav 196,152,52\nRa 132,3,50176\nCV 177,178,179\nMvav 196,156,60\nWL 6\ns_add_u32 m0, s89, 65536\ns_add_u32 s46, s96, 1310720\nMvav 192,156,56\nRa 136,3,51200\n"
"v_lshlrev_b32 v88, 3, v90\nMvav 192,152,48\nD1 1,16,46,0\nCV 178,184,185\nMvav 192,148,40\nRa 140,3,52224\nCV 179,186,187\nMvav 192,144,32\nv_add_u32 v94, 64, v90\n"
"Sv 176,1,16,5,0\nZvav 200,160,96\nRa 144,3,53248\ns_mov_b32 s63, 0xc040000\nMvav 200,164,104\nv_lshrrev_b32 v92, 8, v1\nMvav 200,168,112\nRa 148,3,54272\nMvav 200,172,120\nWL 6\n"
"Zvav 204,172,124\nRa 152,3,55296\nv_bfe_u32 v93, v1, 6, 2\nMvav 204,168,116\nD1 1,16,46,1024\nv_cmp_eq_u32 vcc, v92, v93\nv_and_b32 v92, 16, v1\nv_mov_b32 v93, 0x3f80\n"
"v_cndmask_b32 v93, 0, v93, vcc\nMvav 204,164,108\nRa 156,3,56320\nMvav 204,160,100\nv_lshlrev_b32 v93, v92, v93\nMvav 204,176,36\nRa 160,3,57344\nv_and_b32 v92, 32, v1\n"
"Mvav 204,180,44\ns_add_u32 s4, s92, 131072\nv_cmp_eq_u32 vcc, 0, v92\nv_mov_b32 v4, 0\nv_mov_b32 v5, 0\nv_cndmask_b32 v6, 0, v93, vcc\nMvav 204,184,52\nRa 164,3,58368\n"
"Mvav 204,188,60\nWL 6\nbuffer_load_dwordx4 v[216:219], v1, s[16:19], s4 offen offset:0 sc0 sc1\nMvav 200,188,56\nRa 168,3,59392\nv_sub_u32 v7, v93, v6\nMvav 200,184,48\n"
"D1 1,16,46,2048\nMvav 200,180,40\nRa 172,3,60416\nMvav 200,176,32\nbuffer_load_dwordx4 v[220:223], v1, s[16:19], s4 offen offset:1024 sc0 sc1\nZvav 160,128,96\nRa 176,3,61440\n"
"v_mov_b32 v8, 0\nMvav 160,132,104\nv_mov_b32 v9, 0\nCV 196,196,197\nMvav 160,136,112\nRa 180,3,62464\nCV 197,198,199\nMvav 160,140,120\nWL 6\nCV 198,204,205\nZvav 164,140,124\n"
"Ra 184,3,63488\nCV 199,206,207\nMvav 164,136,116\nD1 1,16,46,3072\ns_add_u32 s5, s94, 655360\nMvav 164,132,108\nRa 188,3,64512\ns_barrier\nMvav 164,128,100\nSv 196,1,16,5,1024\n"
"CV 192,192,193\nMvav 164,144,36\nRa 128,0,0\nCV 193,194,195\nMvav 164,148,44\nCV 194,200,201\nCV 195,202,203\nMvav 164,152,52\nRa 132,0,1024\nSv 192,1,16,5,0\nMvav 164,156,60\n"
"WL 6\ns_add_u32 m0, s89, 98304\ns_add_u32 s46, s96, 1343488\nMvav 160,156,56\nRa 136,0,2048\nMvav 160,152,48\nD1 1,16,46,0\nMvav 160,148,40\nRa 140,0,3072\nMvav 160,144,32\n"
"Zvav 168,160,96\nRa 144,0,4096\nWV 12\nv_max_u32 v21, v211, v215\nv_cmp_eq_u32 vcc, -1, v21\ns_nop 0\ns_cbranch_vccnz .S489\n.S490:\nMvav 168,164,104\n"
"v_lshlrev_b32 v176, 16, v208\nMvav 168,168,112\nRa 148,0,5120\nMvav 168,172,120\nWL 6\nv_and_b32 v177, 0xffff0000, v208\nZvav 172,172,124\nRa 152,0,6144\nMvav 172,168,116\n"
"D1 1,16,46,1024\nMvav 172,164,108\nRa 156,0,7168\nv_lshlrev_b32 v178, 16, v209\nMvav 172,160,100\nv_and_b32 v179, 0xffff0000, v209\nMvav 172,176,36\nRa 160,0,8192\n"
"v_lshlrev_b32 v180, 16, v212\nMvav 172,180,44\nv_and_b32 v181, 0xffff0000, v212\ns_add_u32 s4, s92, 262144\nMvav 172,184,52\nRa 164,0,9216\nMvav 172,188,60\nWL 6\n"
"v_lshlrev_b32 v182, 16, v213\nbuffer_load_dwordx4 v[224:227], v1, s[16:19], s4 offen offset:0 sc0 sc1\nMvav 168,188,56\nRa 168,0,10240\nMvav 168,184,48\nD1 1,16,46,2048\n"
"Mvav 168,180,40\nRa 172,0,11264\nv_and_b32 v183, 0xffff0000, v213\nMvav 168,176,32\nv_lshlrev_b32 v184, 16, v210\n"
"buffer_load_dwordx4 v[228:231], v1, s[16:19], s4 offen offset:1024 sc0 sc1\nMvav 176,128,96\nRa 176,0,12288\nv_and_b32 v185, 0xffff0000, v210\nMvav 176,132,104\n"
"v_lshlrev_b32 v186, 16, v211\nCV 164,164,165\nMvav 176,136,112\nRa 180,0,13312\nCV 165,166,167\nMvav 176,140,120\nWL 6\nCV 166,172,173\nMvav 180,140,124\nRa 184,0,14336\n"
"v_and_b32 v187, 0xffff0000, v211\nMvav 180,136,116\nD1 1,16,46,3072\nCV 167,174,175\nMvav 180,132,108\nRa 188,0,15360\ns_add_u32 s5, s94, 786432\nMvav 180,128,100\n"
"v_lshlrev_b32 v188, 16, v214\nSv 164,1,16,5,1024\nMvav 180,144,36\nRa 128,0,16384\nv_and_b32 v189, 0xffff0000, v214\nMvav 180,148,44\nv_lshlrev_b32 v190, 16, v215\nCV 160,160,161\n"
"Mvav 180,152,52\nRa 132,0,17408\nCV 161,162,163\nMvav 180,156,60\nWL 6\ns_add_u32 m0, s89, 114688\ns_add_u32 s46, s96, 1376256\nMvav 176,156,56\nRa 136,0,18432\n"
"v_and_b32 v191, 0xffff0000, v215\nMvav 176,152,48\nD1 1,16,46,0\nCV 162,168,169\nMvav 176,148,40\nRa 140,0,19456\nCV 163,170,171\nMvav 176,144,32\nSv 160,1,16,5,0\n"
"Mvav 184,160,96\nRa 144,0,20480\nWV 12\nv_max_u32 v21, v219, v223\nv_cmp_eq_u32 vcc, -1, v21\ns_nop 0\ns_cbranch_vccnz .S492\n.S493:\nMvav 184,164,104\n"
"v_lshlrev_b32 v192, 16, v216\nMvav 184,168,112\nRa 148,0,21504\nMvav 184,172,120\nWL 6\nv_and_b32 v193, 0xffff0000, v216\nMvav 188,172,124\nRa 152,0,22528\nMvav 188,168,116\n"
"D1 1,16,46,1024\nMvav 188,164,108\nRa 156,0,23552\nv_lshlrev_b32 v194, 16, v217\nMvav 188,160,100\nv_and_b32 v195, 0xffff0000, v217\nMvav 188,176,36\nRa 160,0,24576\n"
"v_lshlrev_b32 v196, 16, v220\nMvav 188,180,44\nv_and_b32 v197, 0xffff0000, v220\ns_add_u32 s4, s92, 393216\nMvav 188,184,52\nRa 164,0,25600\nMvav 188,188,60\nWL 6\n"
"v_lshlrev_b32 v198, 16, v221\nbuffer_load_dwordx4 v[208:211], v1, s[16:19], s4 offen offset:0 sc0 sc1\nMvav 184,188,56\nRa 168,0,26624\nMvav 184,184,48\nD1 1,16,46,2048\n"
"Mvav 184,180,40\nRa 172,0,27648\nv_and_b32 v199, 0xffff0000, v221\nMvav 184,176,32\nv_lshlrev_b32 v200, 16, v218\n"
"buffer_load_dwordx4 v[212:215], v1, s[16:19], s4 offen offset:1024 sc0 sc1\nMvav 192,128,96\nRa 176,0,28672\nv_and_b32 v201, 0xffff0000, v218\nMvav 192,132,104\n"
"v_lshlrev_b32 v202, 16, v219\nCV 180,180,181\nMvav 192,136,112\nRa 180,0,29696\nCV 181,182,183\nMvav 192,140,120\ns_waitcnt vmcnt(15) lgkmcnt(6)\nCV 182,188,189\nMvav 196,140,124\n"
"Ra 184,0,30720\nv_and_b32 v203, 0xffff0000, v219\nMvav 196,136,116\nD1 1,16,46,3072\nCV 183,190,191\nMvav 196,132,108\nRa 188,0,31744\ns_barrier\nMvav 196,128,100\n"
"v_lshlrev_b32 v204, 16, v222\ns_add_u32 s5, s94, 917504\nMvav 196,144,36\nRa 128,2,16384\nv_and_b32 v205, 0xffff0000, v222\nMvav 196,148,44\nv_lshlrev_b32 v206, 16, v223\n"
"Sv 180,1,16,5,1024\nMvav 196,152,52\nRa 132,2,17408\nCV 176,176,177\nMvav 196,156,60\nWL 6\ns_add_u32 m0, s89, 0\ns_add_u32 s46, s96, 1409024\nMvav 192,156,56\nRa 136,2,18432\n"
"v_and_b32 v207, 0xffff0000, v223\nMvav 192,152,48\nD1 1,16,46,0\nCV 177,178,179\nMvav 192,148,40\nRa 140,2,19456\nCV 178,184,185\nMvav 192,144,32\nCV 179,186,187\nSv 176,1,16,5,0\n"
"Mvav 200,160,96\nRa 144,2,20480\nWV 12\nv_max_u32 v21, v227, v231\nv_cmp_eq_u32 vcc, -1, v21\ns_nop 0\ns_cbranch_vccnz .S495\n.S496:\nMvav 200,164,104\n"
"v_lshlrev_b32 v160, 16, v224\nMvav 200,168,112\nRa 148,2,21504\nMvav 200,172,120\nWL 6\nv_and_b32 v161, 0xffff0000, v224\nMvav 204,172,124\nRa 152,2,22528\nMvav 204,168,116\n"
"D1 1,16,46,1024\nMvav 204,164,108\nRa 156,2,23552\nv_lshlrev_b32 v162, 16, v225\nMvav 204,160,100\nv_and_b32 v163, 0xffff0000, v225\nMvav 204,176,36\nRa 160,2,24576\n"
"v_lshlrev_b32 v164, 16, v228\nMvav 204,180,44\nv_and_b32 v165, 0xffff0000, v228\ns_add_u32 s4, s92, 524288\nMvav 204,184,52\nRa 164,2,25600\nMvav 204,188,60\nWL 6\n"
"v_lshlrev_b32 v166, 16, v229\nbuffer_load_dwordx4 v[216:219], v1, s[16:19], s4 offen offset:0 sc0 sc1\nMvav 200,188,56\nRa 168,2,26624\nMvav 200,184,48\nD1 1,16,46,2048\n"
"Mvav 200,180,40\nRa 172,2,27648\nv_and_b32 v167, 0xffff0000, v229\nMvav 200,176,32\nv_lshlrev_b32 v168, 16, v226\n"
"buffer_load_dwordx4 v[220:223], v1, s[16:19], s4 offen offset:1024 sc0 sc1\nMvav 160,128,96\nRa 176,2,28672\nv_and_b32 v169, 0xffff0000, v226\nMvav 160,132,104\n"
"v_lshlrev_b32 v170, 16, v227\nCV 196,196,197\nMvav 160,136,112\nRa 180,2,29696\nCV 197,198,199\nMvav 160,140,120\nWL 6\nCV 198,204,205\nMvav 164,140,124\nRa 184,2,30720\n"
"v_and_b32 v24, 0x70, v1\nMvav 164,136,116\nD1 1,16,46,3072\nCV 199,206,207\nMvav 164,132,108\nRa 188,2,31744\nv_and_b32 v171, 0xffff0000, v227\nMvav 164,128,100\n"
"v_lshlrev_b32 v172, 16, v230\ns_add_u32 s5, s94, 1048576\nMvav 164,144,36\nRa 128,2,0\nv_lshlrev_b32 v24, 5, v24\nMvav 164,148,44\nv_and_b32 v173, 0xffff0000, v230\n"
"Sv 196,1,16,5,1024\nMvav 164,152,52\nRa 132,2,1024\nCV 192,192,193\nMvav 164,156,60\nWL 6\nv_lshlrev_b32 v174, 16, v231\nMvav 160,156,56\nRa 136,2,2048\n"
"buffer_load_dword v27, v24, s[16:19], s41 offen sc0 sc1\nMvav 160,152,48\nv_and_b32 v175, 0xffff0000, v231\nCV 193,194,195\nMvav 160,148,40\nRa 140,2,3072\nCV 194,200,201\n"
"Mvav 160,144,32\nCV 195,202,203\nSv 192,1,16,5,0\nMvav 168,160,96\nRa 144,2,4096\nWV 12\nv_max_u32 v21, v211, v215\nv_cmp_eq_u32 vcc, -1, v21\ns_nop 0\ns_cbranch_vccnz .S498\n"
".S499:\nMvav 168,164,104\nv_lshlrev_b32 v176, 16, v208\nMvav 168,168,112\nRa 148,2,5120\nMvav 168,172,120\nWL 6\nMvav 172,172,124\nRa 152,2,6144\nv_and_b32 v177, 0xffff0000, v208\n"
"Mvav 172,168,116\nv_lshlrev_b32 v178, 16, v209\nMvav 172,164,108\nRa 156,2,7168\nMvav 172,160,100\nv_and_b32 v179, 0xffff0000, v209\nMvav 172,176,36\nRa 160,2,8192\n"
"v_lshlrev_b32 v180, 16, v212\nMvav 172,180,44\nv_and_b32 v181, 0xffff0000, v212\ns_add_u32 s4, s92, 655360\nMvav 172,184,52\nRa 164,2,9216\nMvav 172,188,60\nWL 6\n"
"buffer_load_dwordx4 v[224:227], v1, s[16:19], s4 offen offset:0 sc0 sc1\nMvav 168,188,56\nRa 168,2,10240\nv_lshlrev_b32 v182, 16, v213\nMvav 168,184,48\n"
"v_and_b32 v183, 0xffff0000, v213\nMvav 168,180,40\nRa 172,2,11264\nMvav 168,176,32\nv_lshlrev_b32 v184, 16, v210\n"
"buffer_load_dwordx4 v[228:231], v1, s[16:19], s4 offen offset:1024 sc0 sc1\nMvav 176,128,96\nRa 176,2,12288\nv_and_b32 v185, 0xffff0000, v210\nMvav 176,132,104\n"
"v_lshlrev_b32 v186, 16, v211\nCV 164,164,165\nMvav 176,136,112\nRa 180,2,13312\nCV 165,166,167\nMvav 176,140,120\ns_waitcnt vmcnt(13) lgkmcnt(6)\nCV 166,172,173\nMvav 180,140,124\n"
"Ra 184,2,14336\nCV 167,174,175\nMvav 180,136,116\nv_and_b32 v187, 0xffff0000, v211\ns_add_u32 s5, s94, 1179648\nMvav 180,132,108\nRa 188,2,15360\ns_barrier\nMvav 180,128,100\n"
"v_lshlrev_b32 v188, 16, v214\nSv 164,1,16,5,1024\nMvav 180,144,36\nRa 128,2,32768\nv_and_b32 v189, 0xffff0000, v214\nMvav 180,148,44\nv_lshlrev_b32 v190, 16, v215\nCV 160,160,161\n"
"Mvav 180,152,52\nRa 132,2,33792\nCV 161,162,163\nMvav 180,156,60\nWL 6\nCV 162,168,169\nMvav 176,156,56\nRa 136,2,34816\nCV 163,170,171\nMvav 176,152,48\n"
"v_and_b32 v191, 0xffff0000, v215\nSv 160,1,16,5,0\nMvav 176,148,40\nRa 140,2,35840\nMvav 176,144,32\nMvav 184,160,96\nRa 144,2,36864\nWV 8\nv_max_u32 v21, v219, v223\n"
"v_cmp_eq_u32 vcc, -1, v21\ns_nop 0\ns_cbranch_vccnz .S501\n.S502:\nMvav 184,164,104\nv_lshlrev_b32 v192, 16, v216\nMvav 184,168,112\nRa 148,2,37888\nMvav 184,172,120\nWL 6\n"
"Mvav 188,172,124\nRa 152,2,38912\nv_and_b32 v193, 0xffff0000, v216\nMvav 188,168,116\nv_lshlrev_b32 v194, 16, v217\nMvav 188,164,108\nRa 156,2,39936\nMvav 188,160,100\n.W587:\n"
"s_cbranch_execz .WB411\nv_and_b32 v195, 0xffff0000, v217\nMvav 188,176,36\nRa 160,2,40960\nv_lshlrev_b32 v196, 16, v220\nMvav 188,180,44\nv_and_b32 v197, 0xffff0000, v220\n"
"s_add_u32 s4, s92, 786432\nMvav 188,184,52\nRa 164,2,41984\nMvav 188,188,60\nWL 6\nbuffer_load_dwordx4 v[208:211], v1, s[16:19], s4 offen offset:0 sc0 sc1\nMvav 184,188,56\n"
"Ra 168,2,43008\nv_lshlrev_b32 v198, 16, v221\nMvav 184,184,48\nv_and_b32 v199, 0xffff0000, v221\nMvav 184,180,40\nRa 172,2,44032\nMvav 184,176,32\nv_lshlrev_b32 v200, 16, v218\n"
"buffer_load_dwordx4 v[212:215], v1, s[16:19], s4 offen offset:1024 sc0 sc1\nMvav 192,128,96\nRa 176,2,45056\nv_and_b32 v201, 0xffff0000, v218\nMvav 192,132,104\n"
"v_lshlrev_b32 v202, 16, v219\nCV 180,180,181\nMvav 192,136,112\nRa 180,2,46080\nCV 181,182,183\nMvav 192,140,120\nWL 6\nCV 182,188,189\nMvav 196,140,124\nRa 184,2,47104\n"
"CV 183,190,191\nMvav 196,136,116\nv_and_b32 v203, 0xffff0000, v219\ns_add_u32 s5, s94, 1310720\nMvav 196,132,108\nRa 188,2,48128\nSv 180,1,16,5,1024\nMvav 196,128,100\n"
"v_lshlrev_b32 v204, 16, v222\nCV 176,176,177\nMvav 196,144,36\nRa 128,2,49152\nv_and_b32 v205, 0xffff0000, v222\nMvav 196,148,44\nv_lshlrev_b32 v206, 16, v223\nCV 177,178,179\n"
"Mvav 196,152,52\nRa 132,2,50176\nCV 178,184,185\nMvav 196,156,60\nWL 6\nCV 179,186,187\nMvav 192,156,56\nRa 136,2,51200\nSv 176,1,16,5,0\nMvav 192,152,48\n"
"v_and_b32 v207, 0xffff0000, v223\nMvav 192,148,40\nRa 140,2,52224\nMvav 192,144,32\nMvav 200,160,96\nRa 144,2,53248\nWV 6\nv_max_u32 v21, v227, v231\nv_cmp_eq_u32 vcc, -1, v21\n"
"s_nop 0\ns_cbranch_vccnz .S504\n.S505:\nMvav 200,164,104\nv_lshlrev_b32 v160, 16, v224\nv_xor_b32 v27, s15, v27\nv_and_b32 v26, 0xffffdfff, v27\nv_cmp_ne_u32 s[64:65], 0, v27\n"
"v_cmp_ne_u32 vcc, 0, v26\ns_nop 0\ns_cbranch_vccnz .S516\n.S517:\nMvav 200,168,112\nRa 148,2,54272\ns_bitcmp1_b32 s64, 0\ns_cselect_b32 s2, 0x2000, 0\nMvav 200,172,120\nWL 6\n"
"Mvav 204,172,124\nRa 152,2,55296\nv_and_b32 v161, 0xffff0000, v224\nMvav 204,168,116\nv_lshlrev_b32 v162, 16, v225\nMvav 204,164,108\nRa 156,2,56320\ns_xor_b32 s2, s2, s20\n"
"Mvav 204,160,100\nv_and_b32 v163, 0xffff0000, v225\nMvav 204,176,36\nRa 160,2,57344\nv_lshlrev_b32 v164, 16, v228\nMvav 204,180,44\nv_and_b32 v165, 0xffff0000, v228\n"
"Mvav 204,184,52\nRa 164,2,58368\nMvav 204,188,60\nWL 6\ns_add_u32 s24, s2, 0x0\nMvav 200,188,56\nRa 168,2,59392\nv_lshlrev_b32 v166, 16, v229\nMvav 200,184,48\n"
"v_and_b32 v167, 0xffff0000, v229\ns_bitcmp1_b32 s64, 1\ns_cselect_b32 s2, 0x2000, 0\nMvav 200,180,40\nRa 172,2,60416\nMvav 200,176,32\nv_lshlrev_b32 v168, 16, v226\n"
"Mvav 160,128,96\nRa 176,2,61440\nv_and_b32 v169, 0xffff0000, v226\nMvav 160,132,104\nv_lshlrev_b32 v170, 16, v227\nCV 196,196,197\nMvav 160,136,112\nRa 180,2,62464\n"
"CV 197,198,199\nMvav 160,140,120\nWL 6\nCV 198,204,205\nMvav 164,140,124\nRa 184,2,63488\nCV 199,206,207\nMvav 164,136,116\nv_and_b32 v171, 0xffff0000, v227\n"
"s_add_u32 s5, s94, 1441792\nMvav 164,132,108\nRa 188,2,64512\ns_barrier\nMvav 164,128,100\nv_lshlrev_b32 v172, 16, v230\nSv 196,1,16,5,1024\nMvav 164,144,36\nRa 128,3,0\n"
"v_and_b32 v173, 0xffff0000, v230\nMvav 164,148,44\nv_lshlrev_b32 v174, 16, v231\nCV 192,192,193\nMvav 164,152,52\nRa 132,3,1024\nCV 193,194,195\nMvav 164,156,60\nWL 6\n"
"s_xor_b32 s2, s2, s20\nMvav 160,156,56\nRa 136,3,2048\nCV 194,200,201\nMvav 160,152,48\nv_and_b32 v175, 0xffff0000, v231\nCV 195,202,203\nMvav 160,148,40\nRa 140,3,3072\n"
"Sv 192,1,16,5,0\nMvav 160,144,32\ns_add_u32 s25, s2, 0x1800000\ns_bitcmp1_b32 s64, 2\ns_cselect_b32 s2, 0x2000, 0\nMvav 168,160,96\nRa 144,3,4096\nWV 4\nv_max_u32 v21, v211, v215\n"
"v_cmp_eq_u32 vcc, -1, v21\ns_nop 0\ns_cbranch_vccnz .S507\n.S508:\nMvav 168,164,104\nv_lshlrev_b32 v176, 16, v208\nMvav 168,168,112\nRa 148,3,5120\nMvav 168,172,120\nWL 6\n"
"Mvav 172,172,124\nRa 152,3,6144\nv_and_b32 v177, 0xffff0000, v208\nMvav 172,168,116\nv_lshlrev_b32 v178, 16, v209\nMvav 172,164,108\nRa 156,3,7168\ns_xor_b32 s2, s2, s20\n"
"Mvav 172,160,100\nv_and_b32 v179, 0xffff0000, v209\nMvav 172,176,36\nRa 160,3,8192\nv_lshlrev_b32 v180, 16, v212\nMvav 172,180,44\nv_and_b32 v181, 0xffff0000, v212\n"
"Mvav 172,184,52\nRa 164,3,9216\nMvav 172,188,60\nWL 6\ns_add_u32 s26, s2, 0x3000000\nMvav 168,188,56\nRa 168,3,10240\nv_lshlrev_b32 v182, 16, v213\nMvav 168,184,48\n"
"v_and_b32 v183, 0xffff0000, v213\ns_bitcmp1_b32 s64, 3\ns_cselect_b32 s2, 0x2000, 0\nMvav 168,180,40\nRa 172,3,11264\nMvav 168,176,32\nv_lshlrev_b32 v184, 16, v210\n"
"Mvav 176,128,96\nRa 176,3,12288\ns_xor_b32 s2, s2, s20\nMvav 176,132,104\nv_and_b32 v185, 0xffff0000, v210\nCV 164,164,165\nMvav 176,136,112\nRa 180,3,13312\nCV 165,166,167\n"
"Mvav 176,140,120\nWL 6\nCV 166,172,173\nMvav 180,140,124\nRa 184,3,14336\nCV 167,174,175\nMvav 180,136,116\nv_lshlrev_b32 v186, 16, v211\ns_add_u32 s5, s94, 1572864\n"
"Mvav 180,132,108\nRa 188,3,15360\ns_add_u32 s27, s2, 0x4800000\nMvav 180,128,100\nv_and_b32 v187, 0xffff0000, v211\ns_bitcmp1_b32 s64, 4\ns_cselect_b32 s2, 0x2000, 0\n"
"Mvav 180,144,36\nv_lshlrev_b32 v188, 16, v214\nSv 164,1,16,5,1024\nMvav 180,148,44\nv_and_b32 v189, 0xffff0000, v214\nCV 160,160,161\nMvav 180,152,52\n"
"v_lshlrev_b32 v190, 16, v215\nCV 161,162,163\nMvav 180,156,60\nWL 4\ns_xor_b32 s2, s2, s20\nMvav 176,156,56\nv_and_b32 v191, 0xffff0000, v215\nCV 162,168,169\nMvav 176,152,48\n"
"s_add_u32 s32, s2, 0x6000000\ns_bitcmp1_b32 s64, 5\ns_cselect_b32 s2, 0x2000, 0\nMvav 176,148,40\ns_xor_b32 s2, s2, s20\nCV 163,170,171\nMvav 176,144,32\n"
"s_add_u32 s33, s2, 0x7800000\ns_bitcmp1_b32 s64, 6\ns_cselect_b32 s2, 0x2000, 0\nMvav 184,160,96\ns_xor_b32 s2, s2, s20\ns_add_u32 s34, s2, 0x9000000\nMvav 184,164,104\n"
"s_bitcmp1_b32 s64, 7\ns_cselect_b32 s2, 0x2000, 0\nMvav 184,168,112\ns_xor_b32 s2, s2, s20\nSv 160,1,16,5,0\nMvav 184,172,120\nWL 0\nMvav 188,172,124\n"
"s_add_u32 s35, s2, 0xa800000\nMvav 188,168,116\nMvav 188,164,108\nMvav 188,160,100\nMvav 188,176,36\nMvav 188,180,44\nMvav 188,184,52\nMvav 188,188,60\nMvav 184,188,56\n"
"Mvav 184,184,48\nMvav 184,180,40\nMvav 184,176,32\nCV 180,180,181\nCV 181,182,183\ns_nop 1\nCV 182,188,189\nCV 183,190,191\ns_add_u32 s5, s94, 1703936\nSv 180,1,16,5,1024\n"
"CV 176,176,177\nCV 177,178,179\nCV 178,184,185\nCV 179,186,187\nSv 176,1,16,5,0\ns_cmp_eq_u32 s13, 7\ns_cbranch_scc1 .Z519\n"
"buffer_load_dwordx4 v[12:15], v91, s[28:31], s22 offen offset:0\nbuffer_load_dwordx4 v[16:19], v91, s[28:31], s23 offen offset:0\n"
"buffer_load_dwordx4 v[64:67], v89, s[16:19], s40 offen offset:0 sc0 sc1\nbuffer_load_dwordx4 v[68:71], v89, s[16:19], s40 offen offset:16 sc0 sc1\n"
"buffer_load_dwordx4 v[72:75], v89, s[16:19], s40 offen offset:1792 sc0 sc1\nbuffer_load_dwordx4 v[76:79], v89, s[16:19], s40 offen offset:1808 sc0 sc1\n"
"buffer_load_dwordx2 v[80:81], v88, s[16:19], s63 offen offset:0 sc0 sc1\nbuffer_load_dwordx2 v[82:83], v88, s[16:19], s63 offen offset:512 sc0 sc1\n"
"buffer_load_dwordx4 v[96:99], v1, s[16:19], s24 offen offset:0 sc0 sc1\nbuffer_load_dwordx4 v[100:103], v1, s[16:19], s24 offen offset:1024 sc0 sc1\n"
"buffer_load_dwordx4 v[104:107], v1, s[16:19], s25 offen offset:0 sc0 sc1\nbuffer_load_dwordx4 v[108:111], v1, s[16:19], s25 offen offset:1024 sc0 sc1\n"
"buffer_load_dwordx4 v[112:115], v1, s[16:19], s26 offen offset:0 sc0 sc1\nbuffer_load_dwordx4 v[116:119], v1, s[16:19], s26 offen offset:1024 sc0 sc1\n"
"buffer_load_dwordx4 v[120:123], v1, s[16:19], s27 offen offset:0 sc0 sc1\nbuffer_load_dwordx4 v[124:127], v1, s[16:19], s27 offen offset:1024 sc0 sc1\n"
"buffer_load_dwordx4 v[32:35], v1, s[16:19], s32 offen offset:0 sc0 sc1\nbuffer_load_dwordx4 v[36:39], v1, s[16:19], s32 offen offset:1024 sc0 sc1\n"
"buffer_load_dwordx4 v[40:43], v1, s[16:19], s33 offen offset:0 sc0 sc1\nbuffer_load_dwordx4 v[44:47], v1, s[16:19], s33 offen offset:1024 sc0 sc1\n"
"buffer_load_dwordx4 v[48:51], v1, s[16:19], s34 offen offset:0 sc0 sc1\nbuffer_load_dwordx4 v[52:55], v1, s[16:19], s34 offen offset:1024 sc0 sc1\n"
"buffer_load_dwordx4 v[56:59], v1, s[16:19], s35 offen offset:0 sc0 sc1\nbuffer_load_dwordx4 v[60:63], v1, s[16:19], s35 offen offset:1024 sc0 sc1\nWV 16\nv_xor_b32 v66, s15, v66\n"
"v_xor_b32 v70, s15, v70\nv_xor_b32 v74, s15, v74\nv_xor_b32 v78, s15, v78\nv_xor_b32 v81, s15, v81\nv_xor_b32 v83, s15, v83\nv_or3_b32 v92, v66, v70, v74\n"
"v_or3_b32 v92, v92, v78, v81\nv_or_b32 v92, v92, v83\nv_cmp_ne_u32 vcc, 0, v92\ns_nop 0\ns_cbranch_vccnz .S510\n.S511:\nv_and_b32 v92, 0x7ff, v67\nv_lshrrev_b32 v93, 16, v67\n"
"v_cmp_eq_u32 s[44:45], v92, v90\nv_cmp_eq_u32 s[46:47], v93, v90\nv_cmp_eq_u32 s[48:49], v92, v94\nv_cmp_eq_u32 s[50:51], v93, v94\nv_cndmask_b32 v12, v12, v64, s[44:45]\n"
"v_cndmask_b32 v13, v13, v65, s[44:45]\nv_cndmask_b32 v14, v14, v68, s[44:45]\nv_cndmask_b32 v15, v15, v69, s[44:45]\nv_cndmask_b32 v12, v12, v72, s[46:47]\n"
"v_cndmask_b32 v13, v13, v73, s[46:47]\nv_cndmask_b32 v14, v14, v76, s[46:47]\nv_cndmask_b32 v15, v15, v77, s[46:47]\nv_cndmask_b32 v16, v16, v64, s[48:49]\n"
"v_cndmask_b32 v17, v17, v65, s[48:49]\nv_cndmask_b32 v18, v18, v68, s[48:49]\nv_cndmask_b32 v19, v19, v69, s[48:49]\nv_cndmask_b32 v16, v16, v72, s[50:51]\n"
"v_cndmask_b32 v17, v17, v73, s[50:51]\nv_cndmask_b32 v18, v18, v76, s[50:51]\nv_cndmask_b32 v19, v19, v77, s[50:51]\nv_sub_f32 v92, 1.0, v80\nv_sub_f32 v93, 1.0, v82\nCV 92,92,93\n"
"v_and_b32 v93, 0xffff0000, v92\nv_lshlrev_b32 v92, 16, v92\nv_lshlrev_b32 v64, 16, v12\nv_and_b32 v65, 0xffff0000, v12\nMU 64,64,92\nMU 65,65,92\nv_lshlrev_b32 v66, 16, v13\n"
"v_and_b32 v67, 0xffff0000, v13\nMU 66,66,92\nMU 67,67,92\nv_lshlrev_b32 v68, 16, v14\nv_and_b32 v69, 0xffff0000, v14\nMU 68,68,92\nMU 69,69,92\nv_lshlrev_b32 v70, 16, v15\n"
"v_and_b32 v71, 0xffff0000, v15\nMU 70,70,92\nMU 71,71,92\nv_lshlrev_b32 v72, 16, v16\nv_and_b32 v73, 0xffff0000, v16\nMU 72,72,93\nMU 73,73,93\nv_lshlrev_b32 v74, 16, v17\n"
"v_and_b32 v75, 0xffff0000, v17\nMU 74,74,93\nMU 75,75,93\nv_lshlrev_b32 v76, 16, v18\nv_and_b32 v77, 0xffff0000, v18\nMU 76,76,93\nMU 77,77,93\nv_lshlrev_b32 v78, 16, v19\n"
"v_and_b32 v79, 0xffff0000, v19\nMU 78,78,93\nMU 79,79,93\ns_mov_b32 s7, 262144\nWV 0\n.Z522:\nv_max_u32 v84, v99, v103\nv_cmp_eq_u32 s[44:45], -1, v84\nv_max_u32 v84, v107, v111\n"
"v_cmp_eq_u32 s[46:47], -1, v84\nv_max_u32 v84, v115, v119\nv_cmp_eq_u32 s[48:49], -1, v84\nv_max_u32 v84, v123, v127\nv_cmp_eq_u32 s[50:51], -1, v84\nv_max_u32 v84, v35, v39\n"
"v_cmp_eq_u32 s[52:53], -1, v84\nv_max_u32 v84, v43, v47\nv_cmp_eq_u32 s[54:55], -1, v84\nv_max_u32 v84, v51, v55\nv_cmp_eq_u32 s[56:57], -1, v84\nv_max_u32 v84, v59, v63\n"
"v_cmp_eq_u32 s[58:59], -1, v84\ns_or_b64 s[0:1], s[44:45], s[46:47]\ns_or_b64 s[2:3], s[48:49], s[50:51]\ns_or_b64 s[0:1], s[0:1], s[52:53]\ns_or_b64 s[2:3], s[2:3], s[54:55]\n"
"s_or_b64 s[0:1], s[0:1], s[56:57]\ns_or_b64 s[2:3], s[2:3], s[58:59]\ns_or_b64 s[0:1], s[0:1], s[2:3]\ns_cbranch_scc1 .Z520\n.Z521:\nZvvv 136,6,96\nMvvv 136,6,104\nMvvv 136,6,112\n"
"Mvvv 136,6,120\nMvvv 136,6,32\nMvvv 136,6,40\nMvvv 136,6,48\nMvvv 136,6,56\nZvvv 140,4,96\nMvvv 140,4,104\nMvvv 140,4,112\nMvvv 140,4,120\nMvvv 140,4,32\nMvvv 140,4,40\n"
"Mvvv 140,4,48\nMvvv 140,4,56\nZvvv 144,6,100\nMvvv 144,6,108\nMvvv 144,6,116\nMvvv 144,6,124\nMvvv 144,6,36\nMvvv 144,6,44\nMvvv 144,6,52\nMvvv 144,6,60\nZvvv 148,4,100\n"
"Mvvv 148,4,108\nMvvv 148,4,116\nMvvv 148,4,124\nMvvv 148,4,36\nMvvv 148,4,44\nMvvv 148,4,52\nMvvv 148,4,60\nFM 64,80,136\nFM 65,80,137\nFM 66,80,138\nFM 67,80,139\nFM 68,80,140\n"
"FM 69,80,141\nFM 70,80,142\nFM 71,80,143\nCV 12,64,65\nCV 13,66,67\nCV 14,68,69\nCV 15,70,71\nSv 12,91,36,22,0\nFM 72,82,144\nFM 73,82,145\nFM 74,82,146\nFM 75,82,147\n"
"FM 76,82,148\nFM 77,82,149\nFM 78,82,150\nFM 79,82,151\nCV 16,72,73\nCV 17,74,75\nCV 18,76,77\nCV 19,78,79\nSv 16,91,36,23,0\n.Z519:\ns_endpgm\n.EP531:\n.S407:\n"
"s_mov_b32 s7, 262144\n.S409:\ns_lshl_b32 s5, s73, 9\ns_add_u32 s5, s5, 0xc049400\nv_lshrrev_b32 v27, 1, v1\nbuffer_load_dwordx2 v[22:23], v27, s[16:19], s5 offen sc0 sc1\nWV 0\n"
"v_cmp_ne_u32 vcc, s15, v23\ns_nop 0\ns_cbranch_vccz .S408\ns_sleep 6\ns_sub_u32 s7, s7, 1\ns_cbranch_scc0 .S409\ns_branch .S408\n.T416:\ns_mov_b32 s62, 1\n"
"s_call_b64 s[80:81], .RA265\ns_branch .T417\n.T418:\ns_mov_b32 s62, 1\ns_call_b64 s[80:81], .RA265\ns_branch .T419\n.S421:\ns_mov_b32 s7, 262144\n.S423:\n"
"buffer_load_dword v27, off, s[16:19], s5 sc0 sc1\nWV 0\nv_cmp_ne_u32 vcc, s15, v27\ns_nop 0\ns_cbranch_vccz .S422\ns_sleep 6\ns_sub_u32 s7, s7, 1\ns_cbranch_scc0 .S423\n"
"s_branch .S422\n.S424:\ns_mov_b32 s7, 262144\n.S426:\nv_and_b32 v27, 0x70, v1\nbuffer_load_dword v27, v27, s[16:19], s61 offen offset:128 sc0 sc1\nWV 0\n"
"v_cmp_ne_u32 vcc, s15, v27\ns_nop 0\ns_cbranch_vccz .S425\ns_sleep 6\ns_sub_u32 s7, s7, 1\ns_cbranch_scc0 .S426\ns_branch .S425\n.S428:\ns_mov_b32 s7, 262144\n.S430:\n"
"v_and_b32 v27, 0x70, v1\nbuffer_load_dword v27, v27, s[16:19], s61 offen offset:136 sc0 sc1\nWV 0\nv_cmp_ne_u32 vcc, s15, v27\ns_nop 0\ns_cbranch_vccz .S429\ns_sleep 6\n"
"s_sub_u32 s7, s7, 1\ns_cbranch_scc0 .S430\ns_branch .S429\n.S431:\ns_mov_b32 s7, 262144\n.S433:\nv_and_b32 v27, 0x70, v1\n"
"buffer_load_dword v27, v27, s[16:19], s61 offen offset:140 sc0 sc1\nWV 0\nv_cmp_ne_u32 vcc, s15, v27\ns_nop 0\ns_cbranch_vccz .S432\ns_sleep 6\ns_sub_u32 s7, s7, 1\n"
"s_cbranch_scc0 .S433\ns_branch .S432\n.S434:\ns_mov_b32 s7, 262144\n.S436:\nv_and_b32 v27, 0x70, v1\nbuffer_load_dword v27, v27, s[16:19], s61 offen offset:12 sc0 sc1\nWV 0\n"
"v_cmp_ne_u32 vcc, s15, v27\ns_nop 0\ns_cbranch_vccz .S435\ns_sleep 6\ns_sub_u32 s7, s7, 1\ns_cbranch_scc0 .S436\ns_branch .S435\n.S437:\ns_mov_b32 s7, 262144\n.S439:\n"
"v_and_b32 v27, 0x70, v1\nbuffer_load_dword v27, v27, s[16:19], s61 offen offset:8 sc0 sc1\nWV 0\nv_cmp_ne_u32 vcc, s15, v27\ns_nop 0\ns_cbranch_vccz .S438\ns_sleep 6\n"
"s_sub_u32 s7, s7, 1\ns_cbranch_scc0 .S439\ns_branch .S438\n.S440:\ns_mov_b32 s7, 262144\n.S442:\nv_and_b32 v27, 0xf0, v1\n"
"buffer_load_dword v27, v27, s[16:19], s61 offen offset:4 sc0 sc1\nWV 0\nv_cmp_ne_u32 vcc, s15, v27\ns_nop 0\ns_cbranch_vccz .S441\ns_sleep 6\ns_sub_u32 s7, s7, 1\n"
"s_cbranch_scc0 .S442\ns_branch .S441\n.T451:\ns_mov_b32 s62, 0\ns_call_b64 s[80:81], .RB266\ns_branch .T452\n.T453:\ns_mov_b32 s62, 0\ns_call_b64 s[80:81], .RA265\ns_branch .T454\n"
".T457:\ns_mov_b32 s62, 0\ns_call_b64 s[80:81], .RB266\ns_branch .T458\n.T459:\ns_mov_b32 s62, 0\ns_call_b64 s[80:81], .RA265\ns_branch .T460\n.T463:\ns_mov_b32 s62, 2\n"
"s_call_b64 s[80:81], .RB266\ns_branch .T464\n.T465:\ns_mov_b32 s62, 1\ns_call_b64 s[80:81], .RA265\ns_branch .T466\n.T467:\ns_mov_b32 s62, 1\ns_call_b64 s[80:81], .RA265\n"
"s_branch .T468\n.T477:\ns_mov_b32 s62, 0\ns_call_b64 s[80:81], .RB266\ns_branch .T478\n.T479:\ns_mov_b32 s62, 0\ns_call_b64 s[80:81], .RA265\ns_branch .T480\n.T483:\n"
"s_mov_b32 s62, 0\ns_call_b64 s[80:81], .RB266\ns_branch .T484\n.T485:\ns_mov_b32 s62, 0\ns_call_b64 s[80:81], .RA265\ns_branch .T486\n.T487:\ns_mov_b32 s62, 2\n"
"s_call_b64 s[80:81], .RB266\ns_branch .T488\n.S489:\ns_mov_b32 s7, 262144\n.S491:\ns_add_u32 s0, s92, 0\nbuffer_load_dwordx4 v[208:211], v1, s[16:19], s0 offen offset:0 sc0 sc1\n"
"buffer_load_dwordx4 v[212:215], v1, s[16:19], s0 offen offset:1024 sc0 sc1\nWV 0\nv_max_u32 v21, v211, v215\nv_cmp_eq_u32 vcc, -1, v21\ns_nop 0\ns_cbranch_vccz .S490\ns_sleep 6\n"
"s_sub_u32 s7, s7, 1\ns_cbranch_scc0 .S491\ns_branch .S490\n.S492:\ns_mov_b32 s7, 262144\n.S494:\ns_add_u32 s0, s92, 131072\n"
"buffer_load_dwordx4 v[216:219], v1, s[16:19], s0 offen offset:0 sc0 sc1\nbuffer_load_dwordx4 v[220:223], v1, s[16:19], s0 offen offset:1024 sc0 sc1\nWV 0\n"
"v_max_u32 v21, v219, v223\nv_cmp_eq_u32 vcc, -1, v21\ns_nop 0\ns_cbranch_vccz .S493\ns_sleep 6\ns_sub_u32 s7, s7, 1\ns_cbranch_scc0 .S494\ns_branch .S493\n.S495:\n"
"s_mov_b32 s7, 262144\n.S497:\ns_add_u32 s0, s92, 262144\nbuffer_load_dwordx4 v[224:227], v1, s[16:19], s0 offen offset:0 sc0 sc1\n"
"buffer_load_dwordx4 v[228:231], v1, s[16:19], s0 offen offset:1024 sc0 sc1\nWV 0\nv_max_u32 v21, v227, v231\nv_cmp_eq_u32 vcc, -1, v21\ns_nop 0\ns_cbranch_vccz .S496\ns_sleep 6\n"
"s_sub_u32 s7, s7, 1\ns_cbranch_scc0 .S497\ns_branch .S496\n.S498:\ns_mov_b32 s7, 262144\n.S500:\ns_add_u32 s0, s92, 393216\n"
"buffer_load_dwordx4 v[208:211], v1, s[16:19], s0 offen offset:0 sc0 sc1\nbuffer_load_dwordx4 v[212:215], v1, s[16:19], s0 offen offset:1024 sc0 sc1\nWV 0\n"
"v_max_u32 v21, v211, v215\nv_cmp_eq_u32 vcc, -1, v21\ns_nop 0\ns_cbranch_vccz .S499\ns_sleep 6\ns_sub_u32 s7, s7, 1\ns_cbranch_scc0 .S500\ns_branch .S499\n.S501:\n"
"s_mov_b32 s7, 262144\n.S503:\ns_add_u32 s0, s92, 524288\nbuffer_load_dwordx4 v[216:219], v1, s[16:19], s0 offen offset:0 sc0 sc1\n"
"buffer_load_dwordx4 v[220:223], v1, s[16:19], s0 offen offset:1024 sc0 sc1\nWV 0\nv_max_u32 v21, v219, v223\nv_cmp_eq_u32 vcc, -1, v21\ns_nop 0\ns_cbranch_vccz .S502\ns_sleep 6\n"
"s_sub_u32 s7, s7, 1\ns_cbranch_scc0 .S503\ns_branch .S502\n.S504:\ns_mov_b32 s7, 262144\n.S506:\ns_add_u32 s0, s92, 655360\n"
"buffer_load_dwordx4 v[224:227], v1, s[16:19], s0 offen offset:0 sc0 sc1\nbuffer_load_dwordx4 v[228:231], v1, s[16:19], s0 offen offset:1024 sc0 sc1\nWV 0\n"
"v_max_u32 v21, v227, v231\nv_cmp_eq_u32 vcc, -1, v21\ns_nop 0\ns_cbranch_vccz .S505\ns_sleep 6\ns_sub_u32 s7, s7, 1\ns_cbranch_scc0 .S506\ns_branch .S505\n.S507:\n"
"s_mov_b32 s7, 262144\n.S509:\ns_add_u32 s0, s92, 786432\nbuffer_load_dwordx4 v[208:211], v1, s[16:19], s0 offen offset:0 sc0 sc1\n"
"buffer_load_dwordx4 v[212:215], v1, s[16:19], s0 offen offset:1024 sc0 sc1\nWV 0\nv_max_u32 v21, v211, v215\nv_cmp_eq_u32 vcc, -1, v21\ns_nop 0\ns_cbranch_vccz .S508\ns_sleep 6\n"
"s_sub_u32 s7, s7, 1\ns_cbranch_scc0 .S509\ns_branch .S508\n.S510:\ns_mov_b32 s7, 262144\n.S512:\nbuffer_load_dwordx4 v[64:67], v89, s[16:19], s40 offen offset:0 sc0 sc1\n"
"buffer_load_dwordx4 v[68:71], v89, s[16:19], s40 offen offset:16 sc0 sc1\nbuffer_load_dwordx4 v[72:75], v89, s[16:19], s40 offen offset:1792 sc0 sc1\n"
"buffer_load_dwordx4 v[76:79], v89, s[16:19], s40 offen offset:1808 sc0 sc1\nbuffer_load_dwordx2 v[80:81], v88, s[16:19], s63 offen offset:0 sc0 sc1\n"
"buffer_load_dwordx2 v[82:83], v88, s[16:19], s63 offen offset:512 sc0 sc1\nWV 0\nv_xor_b32 v66, s15, v66\nv_xor_b32 v70, s15, v70\nv_xor_b32 v74, s15, v74\n"
"v_xor_b32 v78, s15, v78\nv_xor_b32 v81, s15, v81\nv_xor_b32 v83, s15, v83\nv_or3_b32 v92, v66, v70, v74\nv_or3_b32 v92, v92, v78, v81\nv_or_b32 v92, v92, v83\n"
"v_cmp_ne_u32 vcc, 0, v92\ns_nop 0\ns_cbranch_vccz .S511\ns_sleep 6\ns_sub_u32 s7, s7, 1\ns_cbranch_scc0 .S512\ns_branch .S511\n.S513:\ns_mov_b32 s7, 262144\n.S515:\n"
"buffer_load_dword v27, off, s[16:19], s60 offset:512 sc0 sc1\nWV 0\nv_cmp_ne_u32 vcc, s15, v27\ns_nop 0\ns_cbranch_vccz .S514\ns_sleep 6\ns_sub_u32 s7, s7, 1\n"
"s_cbranch_scc0 .S515\ns_branch .S514\n.S516:\ns_mov_b32 s7, 262144\n.S518:\nbuffer_load_dword v27, v24, s[16:19], s41 offen sc0 sc1\nWV 0\nv_xor_b32 v27, s15, v27\n"
"v_and_b32 v26, 0xffffdfff, v27\nv_cmp_ne_u32 s[64:65], 0, v27\nv_cmp_ne_u32 vcc, 0, v26\ns_nop 0\ns_cbranch_vccz .S517\ns_sleep 6\ns_sub_u32 s7, s7, 1\ns_cbranch_scc0 .S518\n"
"s_branch .S517\n.Z520:\ns_sub_u32 s7, s7, 1\ns_cbranch_scc1 .Z521\ns_cmp_lg_u64 s[44:45], 0\ns_cbranch_scc0 .Z523\n"
"buffer_load_dwordx4 v[96:99], v1, s[16:19], s24 offen offset:0 sc0 sc1\nbuffer_load_dwordx4 v[100:103], v1, s[16:19], s24 offen offset:1024 sc0 sc1\n.Z523:\n"
"s_cmp_lg_u64 s[46:47], 0\ns_cbranch_scc0 .Z524\nbuffer_load_dwordx4 v[104:107], v1, s[16:19], s25 offen offset:0 sc0 sc1\n"
"buffer_load_dwordx4 v[108:111], v1, s[16:19], s25 offen offset:1024 sc0 sc1\n.Z524:\ns_cmp_lg_u64 s[48:49], 0\ns_cbranch_scc0 .Z525\n"
"buffer_load_dwordx4 v[112:115], v1, s[16:19], s26 offen offset:0 sc0 sc1\nbuffer_load_dwordx4 v[116:119], v1, s[16:19], s26 offen offset:1024 sc0 sc1\n.Z525:\n"
"s_cmp_lg_u64 s[50:51], 0\ns_cbranch_scc0 .Z526\nbuffer_load_dwordx4 v[120:123], v1, s[16:19], s27 offen offset:0 sc0 sc1\n"
"buffer_load_dwordx4 v[124:127], v1, s[16:19], s27 offen offset:1024 sc0 sc1\n.Z526:\ns_cmp_lg_u64 s[52:53], 0\ns_cbranch_scc0 .Z527\n"
"buffer_load_dwordx4 v[32:35], v1, s[16:19], s32 offen offset:0 sc0 sc1\nbuffer_load_dwordx4 v[36:39], v1, s[16:19], s32 offen offset:1024 sc0 sc1\n.Z527:\n"
"s_cmp_lg_u64 s[54:55], 0\ns_cbranch_scc0 .Z528\nbuffer_load_dwordx4 v[40:43], v1, s[16:19], s33 offen offset:0 sc0 sc1\n"
"buffer_load_dwordx4 v[44:47], v1, s[16:19], s33 offen offset:1024 sc0 sc1\n.Z528:\ns_cmp_lg_u64 s[56:57], 0\ns_cbranch_scc0 .Z529\n"
"buffer_load_dwordx4 v[48:51], v1, s[16:19], s34 offen offset:0 sc0 sc1\nbuffer_load_dwordx4 v[52:55], v1, s[16:19], s34 offen offset:1024 sc0 sc1\n.Z529:\n"
"s_cmp_lg_u64 s[58:59], 0\ns_cbranch_scc0 .Z530\nbuffer_load_dwordx4 v[56:59], v1, s[16:19], s35 offen offset:0 sc0 sc1\n"
"buffer_load_dwordx4 v[60:63], v1, s[16:19], s35 offen offset:1024 sc0 sc1\n.Z530:\ns_sleep 8\nWV 0\ns_branch .Z522\n.Lcode1:\n"
  :: "s"(__builtin_amdgcn_kernarg_segment_ptr()), "s"(blockIdx.x), "v"(threadIdx.x)
  : "memory", "scc", "vcc", "v1", "v2", "v3", "v4", "v5", "v6", "v7", "v8", "v9", "v10", "v11", "v12", "v13", "v14", "v15", "v16", "v17", "v18", "v19", "v20", "v21", "v22", "v23", "v24", "v25", "v26", "v27", "v28", "v29", "v30", "v31", "v32", "v33", "v34", "v35", "v36", "v37", "v38", "v39", "v40", "v41", "v42", "v43", "v44", "v45", "v46", "v47", "v48", "v49", "v50", "v51", "v52", "v53", "v54", "v55", "v56", "v57", "v58", "v59", "v60", "v61", "v62", "v63", "v64", "v65", "v66", "v67", "v68", "v69", "v70", "v71", "v72", "v73", "v74", "v75", "v76", "v77", "v78", "v79", "v80", "v81", "v82", "v83", "v84", "v85", "v86", "v87", "v88", "v89", "v90", "v91", "v92", "v93", "v94", "v95", "v96", "v97", "v98", "v99", "v100", "v101", "v102", "v103", "v104", "v105", "v106", "v107", "v108", "v109", "v110", "v111", "v112", "v113", "v114", "v115", "v116", "v117", "v118", "v119", "v120", "v121", "v122", "v123", "v124", "v125", "v126", "v127", "v128", "v129", "v130", "v131", "v132", "v133", "v134", "v135", "v136", "v137", "v138", "v139", "v140", "v141", "v142", "v143", "v144", "v145", "v146", "v147", "v148", "v149", "v150", "v151", "v152", "v153", "v154", "v155", "v156", "v157", "v158", "v159", "v160", "v161", "v162", "v163", "v164", "v165", "v166", "v167", "v168", "v169", "v170", "v171", "v172", "v173", "v174", "v175", "v176", "v177", "v178", "v179", "v180", "v181", "v182", "v183", "v184", "v185", "v186", "v187", "v188", "v189", "v190", "v191", "v192", "v193", "v194", "v195", "v196", "v197", "v198", "v199", "v200", "v201", "v202", "v203", "v204", "v205", "v206", "v207", "v208", "v209", "v210", "v211", "v212", "v213", "v214", "v215", "v216", "v217", "v218", "v219", "v220", "v221", "v222", "v223", "v224", "v225", "v226", "v227", "v228", "v229", "v230", "v231", "v232", "v233", "v234", "v235", "v236", "v237", "v238", "v239", "v240", "v241", "v242", "v243", "v244", "v245", "v246", "v247", "v248", "v249", "v250", "v251", "v252", "v253", "v254", "v255", "a0", "a1", "a2", "a3", "a4", "a5", "a6", "a7", "a8", "a9", "a10", "a11", "a12", "a13", "a14", "a15", "a16", "a17", "a18", "a19", "a20", "a21", "a22", "a23", "a24", "a25", "a26", "a27", "a28", "a29", "a30", "a31", "a32", "a33", "a34", "a35", "a36", "a37", "a38", "a39", "a40", "a41", "a42", "a43", "a44", "a45", "a46", "a47", "a48", "a49", "a50", "a51", "a52", "a53", "a54", "a55", "a56", "a57", "a58", "a59", "a60", "a61", "a62", "a63", "a64", "a65", "a66", "a67", "a68", "a69", "a70", "a71", "a72", "a73", "a74", "a75", "a76", "a77", "a78", "a79", "a80", "a81", "a82", "a83", "a84", "a85", "a86", "a87", "a88", "a89", "a90", "a91", "a92", "a93", "a94", "a95", "a96", "a97", "a98", "a99", "a100", "a101", "a102", "a103", "a104", "a105", "a106", "a107", "a108", "a109", "a110", "a111", "a112", "a113", "a114", "a115", "a116", "a117", "a118", "a119", "a120", "a121", "a122", "a123", "a124", "a125", "a126", "a127", "a128", "a129", "a130", "a131", "a132", "a133", "a134", "a135", "a136", "a137", "a138", "a139", "a140", "a141", "a142", "a143", "a144", "a145", "a146", "a147", "a148", "a149", "a150", "a151", "a152", "a153", "a154", "a155", "a156", "a157", "a158", "a159", "a160", "a161", "a162", "a163", "a164", "a165", "a166", "a167", "a168", "a169", "a170", "a171", "a172", "a173", "a174", "a175", "a176", "a177", "a178", "a179", "a180", "a181", "a182", "a183", "a184", "a185", "a186", "a187", "a188", "a189", "a190", "a191", "a192", "a193", "a194", "a195", "a196", "a197", "a198", "a199", "a200", "a201", "a202", "a203", "a204", "a205", "a206", "a207", "a208", "a209", "a210", "a211", "a212", "a213", "a214", "a215", "a216", "a217", "a218", "a219", "a220", "a221", "a222", "a223", "a224", "a225", "a226", "a227", "a228", "a229", "a230", "a231", "a232", "a233", "a234", "a235", "a236", "a237", "a238", "a239", "a240", "a241", "a242", "a243", "a244", "a245", "a246", "a247", "a248", "a249", "a250", "a251", "a252", "a253", "a254", "a255", "s8", "s9", "s10", "s11", "s12", "s13", "s14", "s15", "s16", "s17", "s18", "s19", "s20", "s21", "s22", "s23", "s24", "s25", "s26", "s27", "s28", "s29", "s30", "s31", "s33", "s34", "s35", "s36", "s37", "s38", "s39", "s40", "s41", "s42", "s43", "s44", "s45", "s46", "s47", "s48", "s49", "s50", "s51", "s52", "s53", "s54", "s55", "s56", "s57", "s58", "s59", "s60", "s61", "s62", "s63", "s64", "s65", "s66", "s67", "s68", "s69", "s70", "s71", "s72", "s73", "s74", "s75", "s76", "s77", "s78", "s79", "s80", "s81", "s82", "s83", "s84", "s85", "s86", "s87", "s88", "s89", "s90", "s91", "s92", "s93", "s94", "s95", "s96", "s97", "s98", "s99");
#endif
}

extern "C" void kernel_launch(void* const* d_in, const int* in_sizes, int n_in,
                              void* d_out, int out_size, void* d_ws, size_t ws_size,
                              hipStream_t stream) {
  (void)hipFuncSetAttribute((const void*)k_main, hipFuncAttributeMaxDynamicSharedMemorySize, 163840);
  k_main<<<256, 256, 163840, stream>>>(d_in[0], d_in[1], d_in[2], d_in[3], d_in[4], d_ws, d_in[11], d_in[7], d_in[5], d_in[8], d_in[9], d_in[10], d_out);
}
```

```cpp
#include <hip/hip_runtime.h>
#include <stdint.h>

extern "C" __global__ __launch_bounds__(256, 1)
void k_main(const void* x, const void* ckv, const void* wq, const void* wuk, const void* wuv, void* ws,
            const void* gate_w, const void* w_r, const void* wo, const void* wg, const void* wu, const void* wd, void* out) {
#if defined(__HIP_DEVICE_COMPILE__)
  asm volatile(
"s_mov_b64 s[8:9], %0\ns_mov_b32 s10, %1\nv_mov_b32 v0, %2\n.macro WL n\ns_waitcnt lgkmcnt(\\n)\n.endm\n.macro D0 x,r,s,o\n"
"buffer_load_dwordx4 v\\x, s[\\r:\\r+3], s\\s offen offset:\\o lds\n.endm\n.macro WV n\ns_waitcnt vmcnt(\\n)\n.endm\n.macro Ra d,x,o\nds_read_b128 a[\\d:\\d+3], v\\x offset:\\o\n"
".endm\n.macro Ta d,x,o\nds_read_b64_tr_b16 a[\\d:\\d+1], v\\x offset:\\o\n.endm\n.macro Zvaa d,x,y\nv_mfma_f32_16x16x32_bf16 v[\\d:\\d+3], a[\\x:\\x+3], a[\\y:\\y+3], 0\n.endm\n"
".macro Mvaa d,x,y\nv_mfma_f32_16x16x32_bf16 v[\\d:\\d+3], a[\\x:\\x+3], a[\\y:\\y+3], v[\\d:\\d+3]\n.endm\n.macro Zaaa d,x,y\n"
"v_mfma_f32_16x16x32_bf16 a[\\d:\\d+3], a[\\x:\\x+3], a[\\y:\\y+3], 0\n.endm\n.macro Maaa d,x,y\nv_mfma_f32_16x16x32_bf16 a[\\d:\\d+3], a[\\x:\\x+3], a[\\y:\\y+3], a[\\d:\\d+3]\n"
".endm\n.macro MU d,x,y\nv_mul_f32 v\\d, v\\x, v\\y\n.endm\n.macro CV d,x,y\nv_cvt_pk_bf16_f32 v\\d, v\\x, v\\y\n.endm\n.macro FM d,x,y\nv_fmac_f32 v\\d, v\\x, v\\y\n.endm\n"
".macro AD d,x,y\nv_add_f32 v\\d, v\\x, v\\y\n.endm\n.macro EX d,x\nv_exp_f32 v\\d, v\\x\n.endm\n.macro AR d,x\nv_accvgpr_read_b32 v\\d, a\\x\n.endm\n.macro Sv d,x,r,s,o\n"
"buffer_store_dwordx4 v[\\d:\\d+3], v\\x, s[\\r:\\r+3], s\\s offen offset:\\o sc0 sc1\n.endm\n.macro Rv d,x,o\nds_read_b128 v[\\d:\\d+3], v\\x offset:\\o\n.endm\n.macro D1 x,r,s,o\n"
"buffer_load_dwordx4 v\\x, s[\\r:\\r+3], s\\s offen offset:\\o sc1 lds\n.endm\n.macro Cvaa d,x,y,c\nv_mfma_f32_16x16x32_bf16 v[\\d:\\d+3], a[\\x:\\x+3], a[\\y:\\y+3], v[\\c:\\c+3]\n"
".endm\n.macro Zvav d,x,y\nv_mfma_f32_16x16x32_bf16 v[\\d:\\d+3], a[\\x:\\x+3], v[\\y:\\y+3], 0\n.endm\n.macro Mvav d,x,y\n"
"v_mfma_f32_16x16x32_bf16 v[\\d:\\d+3], a[\\x:\\x+3], v[\\y:\\y+3], v[\\d:\\d+3]\n.endm\n.macro SU d,x,y\nv_sub_f32 v\\d, v\\x, v\\y\n.endm\n.macro Mvva d,x,y\n"
"v_mfma_f32_16x16x32_bf16 v[\\d:\\d+3], v[\\x:\\x+3], a[\\y:\\y+3], v[\\d:\\d+3]\n.endm\n.macro Zvvv d,x,y\nv_mfma_f32_16x16x32_bf16 v[\\d:\\d+3], v[\\x:\\x+3], v[\\y:\\y+3], 0\n"
".endm\n.macro Mvvv d,x,y\nv_mfma_f32_16x16x32_bf16 v[\\d:\\d+3], v[\\x:\\x+3], v[\\y:\\y+3], v[\\d:\\d+3]\n.endm\n.macro AW d,x\nv_accvgpr_write_b32 a\\d, v\\x\n.endm\n"
".macro Tv d,x,o\nds_read_b64_tr_b16 v[\\d:\\d+1], v\\x offset:\\o\n.endm\n.macro Sa d,x,r,s,o\n"
"buffer_store_dwordx4 a[\\d:\\d+3], v\\x, s[\\r:\\r+3], s\\s offen offset:\\o sc0 sc1\n.endm\n.Lcode0:\ns_mov_b32 s70, 0\ns_load_dwordx16 s[44:59], s[8:9], 0x0\n"
"s_load_dwordx8 s[60:67], s[8:9], 0x40\ns_load_dwordx2 s[68:69], s[8:9], 0x60\nv_lshrrev_b32 v2, 6, v0\nv_and_b32 v0, 63, v0\nv_readfirstlane_b32 s11, v2\nv_lshlrev_b32 v1, 4, v0\n"
"s_and_b32 s2, s10, 0x17\ns_bfe_u32 s3, s10, 0x10003\ns_lshl_b32 s3, s3, 7\ns_or_b32 s2, s2, s3\ns_bfe_u32 s3, s10, 0x10005\ns_lshl3_add_u32 s2, s3, s2\ns_lshr_b32 s3, s10, 6\n"
"s_lshl_b32 s3, s3, 5\ns_or_b32 s10, s2, s3\ns_and_b32 s73, s10, 7\ns_lshr_b32 s74, s10, 3\ns_and_b32 s2, s74, 1\ns_lshl1_add_u32 s12, s73, s2\ns_bfe_u32 s13, s74, 0x30001\n"
"s_lshr_b32 s14, s74, 4\ns_xor_b32 s14, s14, 1\nv_lshrrev_b32 v32, 5, v0\ns_and_b32 s3, s11, 1\ns_lshl_b32 s2, s3, 3\nv_lshl_or_b32 v33, v32, 1, s2\nv_and_b32 v34, 31, v0\n"
"v_xor_b32 v33, v33, v34\nv_lshlrev_b32 v33, 4, v33\nv_lshl_or_b32 v6, v32, 13, v33\nv_xor_b32 v33, 64, v6\nv_add_u32 v7, 15360, v33\nv_add_u32 v8, 30720, v6\n"
"v_add_u32 v9, 46080, v33\ns_lshl_b32 s2, s11, 16\ns_lshl_b32 s4, s12, 9\ns_add_u32 s5, s2, s4\ns_getpc_b64 s[40:41]\n.PC8:\ns_sub_u32 s40, s40, .PC8-.Lcode0\n"
"s_subb_u32 s41, s41, 0\ns_mov_b32 s42, .Lcode1-.Lcode0\ns_mov_b32 s43, 0x20000\ns_lshl2_add_u32 s6, s74, s11\ns_lshl_b32 s6, s6, 7\nv_lshlrev_b32 v54, 10, v1\n"
"buffer_load_dword v31, v54, s[40:43], s6 offen\nv_lshrrev_b32 v32, 4, v0\nv_and_b32 v33, 7, v0\nv_xor_b32 v33, v33, v32\nv_lshlrev_b32 v33, 4, v33\nv_lshrrev_b32 v32, 3, v0\n"
"v_lshl_or_b32 v2, v32, 11, v33\nv_xor_b32 v33, 64, v2\nv_add_u32 v3, 15360, v33\nv_add_u32 v4, 30720, v2\nv_add_u32 v5, 46080, v33\nv_lshrrev_b32 v144, 4, v0\n"
"v_and_b32 v145, 7, v0\nv_xor_b32 v145, v145, v144\nv_lshlrev_b32 v145, 4, v145\nv_lshrrev_b32 v144, 3, v0\nv_mul_u32_u24 v144, 896, v144\nv_add_u32 v136, v144, v145\n"
"v_xor_b32 v145, 64, v136\nv_add_u32 v137, 6144, v145\nv_add_u32 v138, 55296, v136\nv_add_u32 v139, 61440, v145\ns_lshr_b32 s88, s11, 1\ns_lshr_b32 s2, s13, 2\n"
"s_bfe_u32 s3, s13, 0x10001\ns_mul_i32 s3, s3, 17\ns_add_u32 s2, s2, s3\ns_lshl2_add_u32 s3, s14, s88\ns_lshl_b32 s3, s3, 2\ns_lshr_b32 s3, 0xe420ca86, s3\ns_and_b32 s3, s3, 15\n"
"s_add_u32 s2, s2, s3\ns_cmp_eq_u32 s2, 32\ns_cselect_b32 s86, 16, s2\ns_add_u32 s88, s88, 2\ns_lshr_b32 s2, s13, 2\ns_bfe_u32 s3, s13, 0x10001\ns_mul_i32 s3, s3, 17\n"
"s_add_u32 s2, s2, s3\ns_lshl2_add_u32 s3, s14, s88\ns_lshl_b32 s3, s3, 2\ns_lshr_b32 s3, 0xe420ca86, s3\ns_and_b32 s3, s3, 15\ns_add_u32 s2, s2, s3\ns_cmp_eq_u32 s2, 32\n"
"s_cselect_b32 s88, 16, s2\ns_and_b32 s89, s11, 1\ns_lshl1_add_u32 s86, s86, s89\ns_lshl1_add_u32 s88, s88, s89\ns_sub_u32 s2, 15, s13\ns_lshl3_add_u32 s2, s2, s11\n"
"s_mul_i32 s87, s2, 14336\ns_mov_b32 s18, 0xc0ae800\ns_mov_b32 s30, 0x1c0000\ns_mov_b32 s34, 0x380000\ns_mov_b32 s22, 0x400000\ns_mov_b32 s26, 0x800000\ns_mov_b32 s19, 0x20000\n"
"s_mov_b32 s31, 0x20000\ns_mov_b32 s35, 0x20000\ns_mov_b32 s23, 0x20000\ns_mov_b32 s27, 0x20000\nWL 0\ns_mov_b32 s28, s44\ns_and_b32 s29, s45, 0xffff\ns_mov_b32 s32, s48\n"
"s_and_b32 s33, s49, 0xffff\ns_mov_b32 s20, s46\ns_and_b32 s21, s47, 0xffff\ns_bitcmp1_b32 s13, 0\ns_cselect_b64 s[50:51], s[52:53], s[50:51]\ns_mov_b32 s24, s50\n"
"s_and_b32 s25, s51, 0xffff\ns_sub_u32 s44, s5, 0x40000\ns_mov_b32 s51, s5\ns_lshl_b32 s47, s11, 12\ns_add_u32 s47, s47, 0x10000\ns_lshl_b32 s45, s86, 16\ns_sub_u32 s45, s45, 128\n"
"s_lshl_b32 s48, s88, 16\ns_sub_u32 s48, s48, 128\ns_mov_b32 s52, s87\ns_cmp_eq_u32 s14, 1\ns_cbranch_scc1 .B6\ns_add_u32 m0, s47, 65536\ns_add_u32 s46, s51, 0\nD0 6,32,46,0\n"
"D0 7,32,46,1024\nD0 8,32,46,2048\nD0 9,32,46,3072\ns_add_u32 m0, s47, -65536\ns_add_u32 s46, s52, 0\nD0 136,28,46,0\nD0 137,28,46,1024\nD0 138,28,46,2048\nD0 139,28,46,3072\n"
"s_add_u32 m0, s47, 81920\ns_add_u32 s46, s51, 262144\nD0 6,32,46,0\nD0 7,32,46,1024\nD0 8,32,46,2048\nD0 9,32,46,3072\ns_branch .B7\n.B6:\ns_add_u32 m0, s47, 0\n"
"s_add_u32 s44, s44, 0x40000\nD0 6,24,44,0\nD0 7,24,44,1024\nD0 8,24,44,2048\nD0 9,24,44,3072\ns_add_u32 m0, s47, -65536\ns_add_u32 s45, s45, 128\nD0 2,20,45,0\nD0 3,20,45,1024\n"
"D0 4,20,45,2048\nD0 5,20,45,3072\ns_add_u32 m0, s47, 16384\ns_add_u32 s44, s44, 0x40000\nD0 6,24,44,0\nD0 7,24,44,1024\nD0 8,24,44,2048\nD0 9,24,44,3072\n.B7:\n"
"s_add_u32 s54, s54, 0x1fffff\ns_addc_u32 s55, s55, 0\ns_and_b32 s54, s54, 0xffe00000\ns_mov_b32 s16, s54\ns_and_b32 s17, s55, 0xffff\ns_mov_b32 s2, 0xc048000\n"
"s_load_dword s15, s[54:55], s2\nv_lshrrev_b32 v24, 3, v0\nv_lshlrev_b32 v24, 13, v24\nv_and_b32 v26, 7, v0\nv_lshl_or_b32 v24, v26, 7, v24\nv_lshrrev_b32 v26, 4, v0\n"
"v_lshl_or_b32 v24, v26, 28, v24\nv_lshrrev_b32 v26, 5, v0\nv_lshlrev_b32 v26, 30, v26\nv_lshl_or_b32 v26, v0, 7, v26\ns_and_b32 s2, s13, 6\ns_and_b32 s3, s12, 1\n"
"s_add_u32 s2, s2, s3\ns_lshl2_add_u32 s2, s2, s11\ns_lshl_b32 s2, s2, 14\ns_lshl_b32 s3, s73, 10\ns_add_u32 s2, s2, s3\ns_add_u32 s96, s2, 1048576\ns_and_b32 s2, s10, 127\n"
"s_lshl2_add_u32 s2, s2, s11\ns_lshl_b32 s97, s2, 12\nv_and_b32 v48, 15, v0\nv_lshrrev_b32 v49, 4, v0\nv_lshrrev_b32 v50, 1, v48\nv_xor_b32 v50, v50, v49\n"
"v_lshlrev_b32 v50, 4, v50\nv_lshl_or_b32 v50, v48, 7, v50\ns_lshr_b32 s2, s11, 1\ns_lshl_b32 s2, s2, 12\nv_add_u32 v10, s2, v50\nv_xor_b32 v11, 64, v10\nv_lshrrev_b32 v50, 2, v48\n"
"v_lshl_add_u32 v51, v49, 3, v50\nv_and_b32 v52, 1, v49\nv_lshl_or_b32 v50, v52, 2, v50\nv_lshlrev_b32 v50, 5, v50\nv_lshl_or_b32 v50, v51, 9, v50\nv_and_b32 v52, 3, v0\n"
"v_lshl_or_b32 v50, v52, 3, v50\ns_and_b32 s2, s11, 1\ns_lshl_b32 s2, s2, 8\ns_add_u32 s2, s2, 0x10000\nv_add_u32 v12, s2, v50\nv_xor_b32 v13, 32, v12\nv_xor_b32 v14, 64, v12\n"
"v_xor_b32 v15, 96, v12\nv_xor_b32 v16, 128, v12\nv_xor_b32 v17, 160, v12\nv_xor_b32 v18, 192, v12\nv_xor_b32 v19, 224, v12\ns_getpc_b64 s[0:1]\n.PC9:\ns_mov_b32 s2, .PA2-.PC9\n"
"s_mov_b32 s3, .PA4-.PC9\ns_bitcmp1_b32 s13, 0\ns_cselect_b32 s2, .PA3-.PC9, s2\ns_cselect_b32 s3, .PA5-.PC9, s3\ns_cmp_eq_u32 s14, 0\ns_cselect_b32 s2, s2, s3\n"
"s_add_u32 s0, s0, s2\ns_addc_u32 s1, s1, 0\ns_setpc_b64 s[0:1]\n.p2align 12\n.PA2:\ns_mov_b64 exec, 0\ns_cmp_eq_u32 s11, 1\ns_cbranch_scc1 .W538\ns_cmp_eq_u32 s11, 2\n"
"s_cbranch_scc1 .W539\ns_cmp_eq_u32 s11, 3\ns_cbranch_scc1 .W540\ns_branch .W537\n.WB21:\ns_mov_b64 exec, -1\nv_add_u32 v128, 0x10000, v12\nv_add_u32 v129, 0x10000, v13\n"
"v_add_u32 v130, 0x10000, v14\nv_add_u32 v131, 0x10000, v15\nv_add_u32 v132, 0x10000, v16\nv_add_u32 v133, 0x10000, v17\nv_add_u32 v134, 0x10000, v18\nv_add_u32 v135, 0x10000, v19\n"
"s_add_u32 s2, s11, 1\ns_lshr_b32 s2, s2, 1\ns_lshl_b32 s2, s2, 12\nv_add_u32 v140, s2, v10\nv_add_u32 v141, s2, v11\nv_xor_b32 v142, 0x1000, v140\nv_xor_b32 v143, 0x1000, v141\n"
"s_add_u32 m0, s47, 0\ns_add_u32 s46, s51, 524288\nD0 6,32,46,0\nD0 7,32,46,1024\nD0 8,32,46,2048\nD0 9,32,46,3072\ns_add_u32 m0, s47, -49152\ns_add_u32 s46, s52, 128\n"
"D0 136,28,46,0\nD0 137,28,46,1024\nD0 138,28,46,2048\nD0 139,28,46,3072\ns_add_u32 m0, s47, 16384\ns_add_u32 s46, s51, 786432\nD0 6,32,46,0\nD0 7,32,46,1024\nD0 8,32,46,2048\n"
"D0 9,32,46,3072\nv_mov_b32 v116, 0x40000000\nv_cmp_gt_u32 vcc, 56, v0\ns_lshl2_add_u32 s2, s10, s11\ns_mul_i32 s2, s2, 1792\nv_cndmask_b32 v117, v116, v1, vcc\ns_mov_b32 s36, s56\n"
"s_and_b32 s37, s57, 0xffff\ns_mov_b32 s38, 0x380\ns_mov_b32 s39, 0x20000\ns_mov_b32 s40, s58\ns_and_b32 s41, s59, 0xffff\ns_mov_b32 s42, 0x380\ns_mov_b32 s43, 0x20000\n"
"buffer_load_dwordx4 v[96:99], v117, s[28:31], s2 offen offset:0\nbuffer_load_dwordx4 v[100:103], v117, s[28:31], s2 offen offset:896\n"
"buffer_load_dwordx4 v[104:107], v117, s[36:39], 0 offen offset:0\nbuffer_load_dwordx4 v[108:111], v117, s[40:43], 0 offen offset:0\ns_add_u32 m0, s47, 32768\n"
"s_add_u32 s46, s51, 1048576\nD0 6,32,46,0\nD0 7,32,46,1024\nD0 8,32,46,2048\nD0 9,32,46,3072\ns_add_u32 m0, s47, -32768\ns_add_u32 s46, s52, 256\nD0 136,28,46,0\n"
"D0 137,28,46,1024\nD0 138,28,46,2048\nD0 139,28,46,3072\nWL 0\ns_mul_i32 s15, s15, 1664525\ns_add_u32 s15, s15, 1013904223\nv_mov_b32 v25, s15\ns_mov_b32 s57, 0x3db8aa3b\n"
"s_and_b32 s59, s11, 1\ns_lshl_b32 s59, s59, 13\ns_mul_i32 s2, s73, 25165824\ns_and_b32 s3, s12, 1\ns_lshl_b32 s3, s3, 21\ns_add_u32 s2, s2, s3\ns_lshl2_add_u32 s3, s13, s11\n"
"s_lshl_b32 s3, s3, 15\ns_add_u32 s2, s2, s3\ns_add_u32 s56, s2, 0x800000\ns_xor_b32 s53, s56, 0xc000\nWV 24\ns_barrier\nRa 192,140,0\nRa 196,141,0\nRa 200,140,2048\n"
"Ra 204,141,2048\nRa 208,142,0\nRa 212,143,0\nRa 216,142,2048\nRa 220,143,2048\nTa 160,128,0\nTa 162,128,2048\nTa 164,128,16384\nTa 166,128,18432\nTa 168,129,0\nTa 170,129,2048\n"
"Ta 172,129,16384\nTa 174,129,18432\nTa 176,130,0\nTa 178,130,2048\nTa 180,130,16384\nTa 182,130,18432\nTa 184,131,0\nTa 186,131,2048\nTa 188,131,16384\nTa 190,131,18432\nWL 0\n"
"Zvaa 32,160,192\nTa 224,132,0\ns_add_u32 m0, s47, 49152\ns_add_u32 s46, s51, 1310720\nMvaa 32,164,196\nTa 226,132,2048\nZaaa 96,164,204\nD0 6,32,46,0\nMaaa 96,160,200\n"
"Ta 228,132,16384\nZvaa 192,160,208\nTa 230,132,18432\nMvaa 192,164,212\nZvaa 224,164,220\nTa 232,133,0\nMvaa 224,160,216\nTa 234,133,2048\nD0 7,32,46,1024\nZvaa 228,168,216\n"
"Mvaa 228,172,220\nTa 236,133,16384\nZvaa 196,172,212\nTa 238,133,18432\nMvaa 196,168,208\nZaaa 100,168,200\nTa 240,134,0\nD0 8,32,46,2048\nMaaa 100,172,204\nTa 242,134,2048\n"
"Zvaa 36,172,196\nMvaa 36,168,192\nTa 244,134,16384\nZvaa 40,176,192\nTa 246,134,18432\nMvaa 40,180,196\nD0 9,32,46,3072\nZaaa 104,180,204\nTa 248,135,0\nMaaa 104,176,200\n"
"Ta 250,135,2048\nZvaa 200,176,208\ns_add_u32 m0, s47, -16384\ns_add_u32 s46, s52, 384\nMvaa 200,180,212\nTa 252,135,16384\nZvaa 232,180,220\nTa 254,135,18432\nD0 136,28,46,0\n"
"Mvaa 232,176,216\nZvaa 236,184,216\nMvaa 236,188,220\nZvaa 204,188,212\nMvaa 204,184,208\nD0 137,28,46,1024\nZaaa 108,184,200\nMaaa 108,188,204\nZvaa 44,188,196\nMvaa 44,184,192\n"
"s_waitcnt vmcnt(18) lgkmcnt(0)\ns_barrier\nZvaa 48,224,192\nRa 128,140,16384\nD0 138,28,46,2048\nMvaa 48,228,196\nRa 132,141,16384\nZaaa 112,228,204\nRa 136,140,18432\n"
"Maaa 112,224,200\nRa 140,141,18432\nZvaa 208,224,208\nRa 144,142,16384\nWV 15\nMvaa 208,228,212\nRa 148,143,16384\nD0 139,28,46,3072\nZvaa 240,228,220\nRa 152,142,18432\n"
"Mvaa 240,224,216\nRa 156,143,18432\nZvaa 244,232,216\nTa 160,12,0\ns_add_u32 m0, s47, 65536\ns_add_u32 s46, s51, 1572864\nMvaa 244,236,220\nTa 162,12,2048\nZvaa 212,236,212\n"
"Ta 164,12,16384\nD0 6,32,46,0\nMvaa 212,232,208\nTa 166,12,18432\nZaaa 116,232,200\nTa 168,13,0\nv_lshlrev_b32 v112, 16, v96\nMaaa 116,236,204\nTa 170,13,2048\nZvaa 52,236,196\n"
"Ta 172,13,16384\nMvaa 52,232,192\nTa 174,13,18432\nD0 7,32,46,1024\nZvaa 56,240,192\nTa 176,14,0\nv_lshlrev_b32 v113, 16, v100\nMvaa 56,244,196\nTa 178,14,2048\nZaaa 120,244,204\n"
"Ta 180,14,16384\nMaaa 120,240,200\nTa 182,14,18432\nZvaa 216,240,208\nTa 184,15,0\nD0 8,32,46,2048\nMvaa 216,244,212\nTa 186,15,2048\nZvaa 248,244,220\nTa 188,15,16384\n"
"Mvaa 248,240,216\nTa 190,15,18432\nZvaa 252,248,216\nv_lshlrev_b32 v114, 16, v104\nv_lshlrev_b32 v115, 16, v108\nMvaa 252,252,220\nD0 9,32,46,3072\nZvaa 220,252,212\n"
"MU 116,112,114\nMvaa 220,248,208\nMU 117,113,114\nZaaa 124,248,200\ns_add_u32 m0, s47, 81920\ns_add_u32 s46, s51, 1835008\nMaaa 124,252,204\nCV 116,116,117\nZvaa 60,252,196\n"
"D0 6,32,46,0\nMvaa 60,248,192\nWL 0\nMvaa 32,160,128\nTa 224,16,0\nv_lshlrev_b32 v118, 16, v116\nMvaa 32,164,132\nTa 226,16,2048\nMaaa 96,164,140\n"
"v_and_b32 v119, 0xffff0000, v116\nMaaa 96,160,136\nTa 228,16,16384\nD0 7,32,46,1024\nMvaa 192,160,144\nTa 230,16,18432\nMU 120,112,115\nMvaa 192,164,148\nMU 121,113,115\n"
"Mvaa 224,164,156\nTa 232,17,0\nMvaa 224,160,152\nTa 234,17,2048\nMvaa 228,168,152\nD0 8,32,46,2048\nv_and_b32 v112, 0xffff0000, v96\nMvaa 228,172,156\nTa 236,17,16384\n"
"Mvaa 196,172,148\nTa 238,17,18432\nMvaa 196,168,144\nv_and_b32 v113, 0xffff0000, v100\nMaaa 100,168,136\nTa 240,18,0\nv_and_b32 v114, 0xffff0000, v104\nMaaa 100,172,140\n"
"Ta 242,18,2048\nD0 9,32,46,3072\nMvaa 36,172,132\nv_and_b32 v115, 0xffff0000, v108\nMvaa 36,168,128\nTa 244,18,16384\nMvaa 40,176,128\nTa 246,18,18432\nMU 116,112,114\n"
"Mvaa 40,180,132\nMU 117,113,114\nMaaa 104,180,140\nTa 248,19,0\nMaaa 104,176,136\nTa 250,19,2048\nMvaa 200,176,144\nCV 116,116,117\nv_and_b32 v117, 0xffff0000, v116\n"
"Mvaa 200,180,148\nTa 252,19,16384\nMvaa 232,180,156\nTa 254,19,18432\nMvaa 232,176,152\nv_lshlrev_b32 v116, 16, v116\nMvaa 236,184,152\nFM 120,112,115\nFM 121,113,115\n"
"Mvaa 236,188,156\nAD 118,118,116\nMvaa 204,188,148\nAD 119,119,117\nMvaa 204,184,144\nv_lshlrev_b32 v112, 16, v97\nMaaa 108,184,136\nv_lshlrev_b32 v113, 16, v101\n"
"v_lshlrev_b32 v114, 16, v105\nMaaa 108,188,140\nv_lshlrev_b32 v115, 16, v109\nMvaa 44,188,132\nMU 116,112,114\nMvaa 44,184,128\ns_waitcnt vmcnt(12) lgkmcnt(0)\ns_barrier\n"
"Mvaa 48,224,128\nRa 192,140,32768\ns_add_u32 m0, s47, -49152\ns_add_u32 s46, s52, 512\nMvaa 48,228,132\nRa 196,141,32768\nMaaa 112,228,140\nRa 200,140,34816\nD0 136,28,46,0\n"
"Maaa 112,224,136\nRa 204,141,34816\nMvaa 208,224,144\nRa 208,142,32768\nMU 117,113,114\nMvaa 208,228,148\nRa 212,143,32768\nMvaa 240,228,156\nRa 216,142,34816\nMvaa 240,224,152\n"
"Ra 220,143,34816\nD0 137,28,46,1024\nMvaa 244,232,152\nTa 160,12,32768\nCV 116,116,117\nMvaa 244,236,156\nTa 162,12,34816\nMvaa 212,236,148\nTa 164,12,49152\nMvaa 212,232,144\n"
"Ta 166,12,51200\nMaaa 116,232,136\nTa 168,13,32768\nD0 138,28,46,2048\nMaaa 116,236,140\nTa 170,13,34816\nMvaa 52,236,132\nTa 172,13,49152\nMvaa 52,232,128\nTa 174,13,51200\n"
"Mvaa 56,240,128\nTa 176,14,32768\nv_and_b32 v117, 0xffff0000, v116\nMvaa 56,244,132\nTa 178,14,34816\nD0 139,28,46,3072\nMaaa 120,244,140\nTa 180,14,49152\nMaaa 120,240,136\n"
"Ta 182,14,51200\nMvaa 216,240,144\nTa 184,15,32768\ns_add_u32 m0, s47, 0\ns_add_u32 s46, s51, 2097152\nMvaa 216,244,148\nTa 186,15,34816\nMvaa 248,244,156\nTa 188,15,49152\n"
"D0 6,32,46,0\nMvaa 248,240,152\nTa 190,15,51200\nMvaa 252,248,152\nv_lshlrev_b32 v116, 16, v116\nFM 120,112,115\nMvaa 252,252,156\nFM 121,113,115\nMvaa 220,252,148\n"
"AD 118,118,116\nMvaa 220,248,144\nD0 7,32,46,1024\nMaaa 124,248,136\nAD 119,119,117\nv_and_b32 v112, 0xffff0000, v97\nMaaa 124,252,140\nv_and_b32 v113, 0xffff0000, v101\n"
"Mvaa 60,252,132\nv_and_b32 v114, 0xffff0000, v105\nMvaa 60,248,128\nWL 0\nMvaa 32,160,192\nTa 224,16,32768\nD0 8,32,46,2048\nMvaa 32,164,196\nTa 226,16,34816\nMaaa 96,164,204\n"
"v_and_b32 v115, 0xffff0000, v109\nMaaa 96,160,200\nTa 228,16,49152\nMvaa 192,160,208\nTa 230,16,51200\nMU 116,112,114\nMvaa 192,164,212\nD0 9,32,46,3072\nMvaa 224,164,220\n"
"Ta 232,17,32768\nMvaa 224,160,216\nTa 234,17,34816\nMvaa 228,168,216\ns_add_u32 m0, s47, 16384\ns_add_u32 s46, s51, 2359296\nMvaa 228,172,220\nTa 236,17,49152\nMvaa 196,172,212\n"
"Ta 238,17,51200\nD0 6,32,46,0\nMvaa 196,168,208\nMU 117,113,114\nMaaa 100,168,200\nTa 240,18,32768\nCV 116,116,117\nMaaa 100,172,204\nTa 242,18,34816\nMvaa 36,172,196\n"
"v_and_b32 v117, 0xffff0000, v116\nMvaa 36,168,192\nTa 244,18,49152\nD0 7,32,46,1024\nMvaa 40,176,192\nTa 246,18,51200\nv_lshlrev_b32 v116, 16, v116\nMvaa 40,180,196\n"
"FM 120,112,115\nMaaa 104,180,204\nTa 248,19,32768\nMaaa 104,176,200\nTa 250,19,34816\nMvaa 200,176,208\nD0 8,32,46,2048\nFM 121,113,115\nMvaa 200,180,212\nTa 252,19,49152\n"
"Mvaa 232,180,220\nTa 254,19,51200\nMvaa 232,176,216\nAD 118,118,116\nMvaa 236,184,216\nAD 119,119,117\nv_lshlrev_b32 v112, 16, v98\nMvaa 236,188,220\nD0 9,32,46,3072\n"
"Mvaa 204,188,212\nv_lshlrev_b32 v113, 16, v102\nMvaa 204,184,208\nv_lshlrev_b32 v114, 16, v106\nMaaa 108,184,200\nv_lshlrev_b32 v115, 16, v110\nMU 116,112,114\nMaaa 108,188,204\n"
"MU 117,113,114\nMvaa 44,188,196\nCV 116,116,117\nMvaa 44,184,192\ns_waitcnt vmcnt(12) lgkmcnt(0)\ns_barrier\nMvaa 48,224,192\nRa 128,140,49152\ns_add_u32 m0, s47, -32768\n"
"s_add_u32 s46, s52, 640\nMvaa 48,228,196\nRa 132,141,49152\nMaaa 112,228,204\nRa 136,140,51200\nD0 136,28,46,0\nMaaa 112,224,200\nRa 140,141,51200\nMvaa 208,224,208\n"
"Ra 144,142,49152\nv_and_b32 v117, 0xffff0000, v116\nMvaa 208,228,212\nRa 148,143,49152\nMvaa 240,228,220\nRa 152,142,51200\nMvaa 240,224,216\nRa 156,143,51200\nD0 137,28,46,1024\n"
"Mvaa 244,232,216\nTa 160,128,0\nv_lshlrev_b32 v116, 16, v116\nMvaa 244,236,220\nTa 162,128,2048\nMvaa 212,236,212\nTa 164,128,16384\nMvaa 212,232,208\nTa 166,128,18432\n"
"Maaa 116,232,200\nTa 168,129,0\nD0 138,28,46,2048\nMaaa 116,236,204\nTa 170,129,2048\nMvaa 52,236,196\nTa 172,129,16384\nMvaa 52,232,192\nTa 174,129,18432\nMvaa 56,240,192\n"
"Ta 176,130,0\nFM 120,112,115\nMvaa 56,244,196\nTa 178,130,2048\nD0 139,28,46,3072\nMaaa 120,244,204\nTa 180,130,16384\nMaaa 120,240,200\nTa 182,130,18432\nMvaa 216,240,208\n"
"Ta 184,131,0\ns_add_u32 m0, s47, 32768\ns_add_u32 s46, s51, 2621440\nMvaa 216,244,212\nTa 186,131,2048\nMvaa 248,244,220\nTa 188,131,16384\nD0 6,32,46,0\nMvaa 248,240,216\n"
"Ta 190,131,18432\nMvaa 252,248,216\nFM 121,113,115\nAD 118,118,116\nMvaa 252,252,220\nAD 119,119,117\nMvaa 220,252,212\nv_and_b32 v112, 0xffff0000, v98\nMvaa 220,248,208\n"
"D0 7,32,46,1024\nMaaa 124,248,200\nv_and_b32 v113, 0xffff0000, v102\nv_and_b32 v114, 0xffff0000, v106\nMaaa 124,252,204\nv_and_b32 v115, 0xffff0000, v110\nMvaa 60,252,196\n"
"MU 116,112,114\nMvaa 60,248,192\nWL 0\nMvaa 32,160,128\nTa 224,132,0\n.W537:\ns_cbranch_execz .W541\nD0 8,32,46,2048\nMvaa 32,164,132\nTa 226,132,2048\nMaaa 96,164,140\n"
"MU 117,113,114\nMaaa 96,160,136\nTa 228,132,16384\nMvaa 192,160,144\nTa 230,132,18432\nCV 116,116,117\nMvaa 192,164,148\nD0 9,32,46,3072\nMvaa 224,164,156\nTa 232,133,0\n"
"Mvaa 224,160,152\nTa 234,133,2048\nMvaa 228,168,152\ns_add_u32 m0, s47, 49152\ns_add_u32 s46, s51, 2883584\nMvaa 228,172,156\nTa 236,133,16384\nMvaa 196,172,148\nTa 238,133,18432\n"
"D0 6,32,46,0\nMvaa 196,168,144\nv_and_b32 v117, 0xffff0000, v116\nMaaa 100,168,136\nTa 240,134,0\nv_lshlrev_b32 v116, 16, v116\nMaaa 100,172,140\nTa 242,134,2048\nMvaa 36,172,132\n"
"FM 120,112,115\nMvaa 36,168,128\nTa 244,134,16384\nD0 7,32,46,1024\nMvaa 40,176,128\nTa 246,134,18432\nFM 121,113,115\nMvaa 40,180,132\nAD 118,118,116\nMaaa 104,180,140\n"
"Ta 248,135,0\nMaaa 104,176,136\nTa 250,135,2048\nMvaa 200,176,144\nD0 8,32,46,2048\nAD 119,119,117\nMvaa 200,180,148\nTa 252,135,16384\nMvaa 232,180,156\nTa 254,135,18432\n"
"Mvaa 232,176,152\nv_lshlrev_b32 v112, 16, v99\nMvaa 236,184,152\nv_lshlrev_b32 v113, 16, v103\nv_lshlrev_b32 v114, 16, v107\nMvaa 236,188,156\nD0 9,32,46,3072\nMvaa 204,188,148\n"
"v_lshlrev_b32 v115, 16, v111\nMvaa 204,184,144\nMU 116,112,114\nMaaa 108,184,136\nMU 117,113,114\nCV 116,116,117\ns_and_b32 s78, s74, 3\ns_lshl2_add_u32 s78, s73, s78\n"
"s_lshl_b32 s78, s78, 12\ns_bitcmp1_b32 s11, 0\ns_cselect_b32 s79, s48, s45\ns_add_u32 s79, s79, 512\ns_and_b32 s79, s79, 0xfffe0000\ns_add_u32 s78, s78, s79\n"
"buffer_load_dword v31, v26, s[20:23], s78 offen\nMaaa 108,188,140\nv_and_b32 v117, 0xffff0000, v116\nMvaa 44,188,132\nv_lshlrev_b32 v116, 16, v116\nMvaa 44,184,128\n"
"s_waitcnt vmcnt(13) lgkmcnt(0)\ns_barrier\nMvaa 48,224,128\nRa 192,140,16384\ns_add_u32 m0, s47, -16384\ns_add_u32 s46, s52, 768\nMvaa 48,228,132\nRa 196,141,16384\n"
"Maaa 112,228,140\nRa 200,140,18432\nD0 136,28,46,0\nMaaa 112,224,136\nRa 204,141,18432\nMvaa 208,224,144\nRa 208,142,16384\nFM 120,112,115\nMvaa 208,228,148\nRa 212,143,16384\n"
"Mvaa 240,228,156\nRa 216,142,18432\nMvaa 240,224,152\nRa 220,143,18432\nD0 137,28,46,1024\nMvaa 244,232,152\nTa 160,12,0\nFM 121,113,115\nMvaa 244,236,156\nTa 162,12,2048\n"
"Mvaa 212,236,148\nTa 164,12,16384\nMvaa 212,232,144\nTa 166,12,18432\nMaaa 116,232,136\nTa 168,13,0\nD0 138,28,46,2048\nMaaa 116,236,140\nTa 170,13,2048\nMvaa 52,236,132\n"
"Ta 172,13,16384\nMvaa 52,232,128\nTa 174,13,18432\nMvaa 56,240,128\nTa 176,14,0\nAD 118,118,116\nMvaa 56,244,132\nTa 178,14,2048\nD0 139,28,46,3072\nMaaa 120,244,140\n"
"Ta 180,14,16384\nMaaa 120,240,136\nTa 182,14,18432\nMvaa 216,240,144\nTa 184,15,0\ns_add_u32 m0, s47, 65536\ns_add_u32 s46, s51, 3145728\nMvaa 216,244,148\nTa 186,15,2048\n"
"Mvaa 248,244,156\nTa 188,15,16384\nD0 6,32,46,0\nMvaa 248,240,152\nTa 190,15,18432\nMvaa 252,248,152\nAD 119,119,117\nv_and_b32 v112, 0xffff0000, v99\nMvaa 252,252,156\n"
"v_and_b32 v113, 0xffff0000, v103\nMvaa 220,252,148\nv_and_b32 v114, 0xffff0000, v107\nMvaa 220,248,144\nD0 7,32,46,1024\nMaaa 124,248,136\nv_and_b32 v115, 0xffff0000, v111\n"
"MU 116,112,114\nMaaa 124,252,140\nMU 117,113,114\nMvaa 60,252,132\nCV 116,116,117\nMvaa 60,248,128\nWL 0\nMvaa 32,160,192\nTa 224,16,0\nD0 8,32,46,2048\nMvaa 32,164,196\n"
"Ta 226,16,2048\nMaaa 96,164,204\nv_and_b32 v117, 0xffff0000, v116\nMaaa 96,160,200\nTa 228,16,16384\nMvaa 192,160,208\nTa 230,16,18432\nv_lshlrev_b32 v116, 16, v116\n"
"Mvaa 192,164,212\nD0 9,32,46,3072\nMvaa 224,164,220\nTa 232,17,0\nMvaa 224,160,216\nTa 234,17,2048\nMvaa 228,168,216\ns_add_u32 m0, s47, 81920\ns_add_u32 s46, s51, 3407872\n"
"Mvaa 228,172,220\nTa 236,17,16384\nMvaa 196,172,212\nTa 238,17,18432\nD0 6,32,46,0\nMvaa 196,168,208\nFM 120,112,115\nMaaa 100,168,200\nTa 240,18,0\nFM 121,113,115\n"
"Maaa 100,172,204\nTa 242,18,2048\nMvaa 36,172,196\nAD 118,118,116\nMvaa 36,168,192\nTa 244,18,16384\nD0 7,32,46,1024\nMvaa 40,176,192\nTa 246,18,18432\nAD 119,119,117\n"
"Mvaa 40,180,196\ns_nop 0\nv_permlane32_swap_b32 v118, v119\nMaaa 104,180,204\nTa 248,19,0\nMaaa 104,176,200\nTa 250,19,2048\nMvaa 200,176,208\nD0 8,32,46,2048\n"
"v_permlane32_swap_b32 v120, v121\nMvaa 200,180,212\nTa 252,19,16384\nMvaa 232,180,220\nTa 254,19,18432\nMvaa 232,176,216\nAD 118,118,119\nMvaa 236,184,216\nAD 120,120,121\n"
"Mvaa 236,188,220\nD0 9,32,46,3072\nMvaa 204,188,212\nv_permlane16_swap_b32 v118, v120\nMvaa 204,184,208\nAD 118,118,120\nMaaa 108,184,200\nMaaa 108,188,204\n"
"v_add_f32_dpp v118, v118, v118 row_ror:8 row_mask:0xf bank_mask:0xf\nMvaa 44,188,196\nMvaa 44,184,192\ns_waitcnt vmcnt(13) lgkmcnt(0)\ns_barrier\nMvaa 48,224,192\n"
"Ra 128,140,32768\ns_add_u32 m0, s47, -65536\ns_add_u32 s45, s45, 128\nMvaa 48,228,196\nRa 132,141,32768\nMaaa 112,228,204\nRa 136,140,34816\nD0 2,20,45,0\nMaaa 112,224,200\n"
"Ra 140,141,34816\nMvaa 208,224,208\nRa 144,142,32768\nv_add_f32_dpp v118, v118, v118 row_ror:4 row_mask:0xf bank_mask:0xf\nMvaa 208,228,212\nRa 148,143,32768\nMvaa 240,228,220\n"
"Ra 152,142,34816\nMvaa 240,224,216\nRa 156,143,34816\nD0 3,20,45,1024\nMvaa 244,232,216\nTa 160,12,32768\nv_add_f32_dpp v118, v118, v118 row_ror:2 row_mask:0xf bank_mask:0xf\n"
"Mvaa 244,236,220\nTa 162,12,34816\nMvaa 212,236,212\nTa 164,12,49152\nMvaa 212,232,208\nTa 166,12,51200\nMaaa 116,232,200\nTa 168,13,32768\nD0 4,20,45,2048\nMaaa 116,236,204\n"
"Ta 170,13,34816\nMvaa 52,236,196\nTa 172,13,49152\nMvaa 52,232,192\nTa 174,13,51200\nMvaa 56,240,192\nTa 176,14,32768\n"
"v_add_f32_dpp v118, v118, v118 row_ror:1 row_mask:0xf bank_mask:0xf\nMvaa 56,244,196\nTa 178,14,34816\nD0 5,20,45,3072\nMaaa 120,244,204\nTa 180,14,49152\nMaaa 120,240,200\n"
"Ta 182,14,51200\nMvaa 216,240,208\nTa 184,15,32768\ns_add_u32 m0, s47, 0\ns_add_u32 s44, s44, 0x40000\nMvaa 216,244,212\nTa 186,15,34816\nMvaa 248,244,220\nTa 188,15,49152\n"
"D0 6,24,44,0\nMvaa 248,240,216\nTa 190,15,51200\nMvaa 252,248,216\nCV 118,118,118\nv_lshlrev_b32 v118, 16, v118\nMvaa 252,252,220\nv_mul_f32 v119, 0xbfb8aa3b, v118\nEX 119,119\n"
"Mvaa 220,252,212\nv_add_f32 v120, 0, v118\nMvaa 220,248,208\nD0 7,24,44,1024\nMaaa 124,248,200\nv_add_f32 v119, 1.0, v119\nMaaa 124,252,204\nv_rcp_f32 v119, v119\nMvaa 60,252,196\n"
"v_ashrrev_i32 v121, 31, v120\nMvaa 60,248,192\nWL 0\nMvaa 32,160,128\nTa 224,16,32768\nD0 8,24,44,2048\nMvaa 32,164,132\nTa 226,16,34816\nMaaa 96,164,140\nCV 119,119,119\n"
"Maaa 96,160,136\nTa 228,16,49152\nMvaa 192,160,144\nTa 230,16,51200\nv_lshlrev_b32 v119, 16, v119\nMvaa 192,164,148\nD0 9,24,44,3072\nMvaa 224,164,156\nTa 232,17,32768\n"
"Mvaa 224,160,152\nTa 234,17,34816\nMvaa 228,168,152\ns_add_u32 m0, s47, 16384\ns_add_u32 s44, s44, 0x40000\nMvaa 228,172,156\nTa 236,17,49152\nMvaa 196,172,148\nTa 238,17,51200\n"
"D0 6,24,44,0\nMvaa 196,168,144\nv_or_b32 v121, 0x80000000, v121\nMaaa 100,168,136\nTa 240,18,32768\nv_xor_b32 v120, v120, v121\nMaaa 100,172,140\nTa 242,18,34816\nMvaa 36,172,132\n"
"v_and_b32 v120, 0xffff0000, v120\nMvaa 36,168,128\nTa 244,18,49152\nD0 7,24,44,1024\nMvaa 40,176,128\nTa 246,18,51200\ns_lshl2_add_u32 s2, s10, s11\nMvaa 40,180,132\n"
"v_lshrrev_b32 v112, 5, v0\nMaaa 104,180,140\nTa 248,19,32768\nMaaa 104,176,136\nTa 250,19,34816\nMvaa 200,176,144\nD0 8,24,44,2048\nv_lshl_add_u32 v112, s2, 1, v112\n"
"Mvaa 200,180,148\nTa 252,19,49152\nMvaa 232,180,156\nTa 254,19,51200\nMvaa 232,176,152\nv_sub_u32 v113, 0xffff, v112\nMvaa 236,184,152\nv_or_b32 v120, v120, v113\n"
"v_mov_b32 v121, s15\nMvaa 236,188,156\nD0 9,24,44,3072\nMvaa 204,188,148\nv_and_b32 v113, 31, v0\nMvaa 204,184,144\nv_mov_b32 v116, 0x40000000\nv_cmp_eq_u32 vcc, 16, v113\n"
"v_lshlrev_b32 v114, 2, v112\nv_lshlrev_b32 v115, 10, v113\nv_cndmask_b32 v114, v116, v114, vcc\nv_cndmask_b32 v120, v119, v120, vcc\nMaaa 108,184,136\n"
"v_lshl_add_u32 v115, v112, 3, v115\nv_and_b32 v113, 15, v0\nMaaa 108,188,140\nv_cmp_eq_u32 vcc, 0, v113\ns_mov_b32 s2, 0xc040000\ns_mov_b32 s3, 0xc03e000\n"
"v_cndmask_b32 v115, v116, v115, vcc\nbuffer_store_dwordx2 v[120:121], v115, s[16:19], s2 offen sc0 sc1\nbuffer_store_dword v118, v114, s[16:19], s3 offen sc0 sc1\nMvaa 44,188,132\n"
"Mvaa 44,184,128\ns_waitcnt vmcnt(14) lgkmcnt(0)\ns_barrier\nMvaa 48,224,128\nRa 192,140,49152\ns_add_u32 m0, s47, -49152\ns_add_u32 s48, s48, 128\nMvaa 48,228,132\n"
"Ra 196,141,49152\nMaaa 112,228,140\nRa 200,140,51200\nD0 2,20,48,0\nMaaa 112,224,136\nRa 204,141,51200\nMvaa 208,224,144\nRa 208,142,49152\nMvaa 208,228,148\nRa 212,143,49152\n"
"Mvaa 240,228,156\nRa 216,142,51200\nMvaa 240,224,152\nRa 220,143,51200\nD0 3,20,48,1024\nMvaa 244,232,152\nTa 160,128,0\nMvaa 244,236,156\nTa 162,128,2048\nMvaa 212,236,148\n"
"Ta 164,128,16384\nMvaa 212,232,144\nTa 166,128,18432\nMaaa 116,232,136\nTa 168,129,0\nD0 4,20,48,2048\nMaaa 116,236,140\nTa 170,129,2048\nMvaa 52,236,132\nTa 172,129,16384\n"
"Mvaa 52,232,128\nTa 174,129,18432\nMvaa 56,240,128\nTa 176,130,0\nMvaa 56,244,132\nTa 178,130,2048\nD0 5,20,48,3072\nMaaa 120,244,140\nTa 180,130,16384\nMaaa 120,240,136\n"
"Ta 182,130,18432\nMvaa 216,240,144\nTa 184,131,0\ns_add_u32 m0, s47, -32768\ns_add_u32 s45, s45, 128\nMvaa 216,244,148\nTa 186,131,2048\nMvaa 248,244,156\nTa 188,131,16384\n"
"D0 2,20,45,0\nMvaa 248,240,152\nTa 190,131,18432\nMvaa 252,248,152\nMvaa 252,252,156\nMvaa 220,252,148\nMvaa 220,248,144\nD0 3,20,45,1024\nMaaa 124,248,136\nMaaa 124,252,140\n"
"Mvaa 60,252,132\nMvaa 60,248,128\nWL 0\nMvaa 32,160,192\nTa 224,132,0\nD0 4,20,45,2048\nMvaa 32,164,196\nTa 226,132,2048\nMvaa 36,172,196\nMvaa 36,168,192\nTa 228,132,16384\n"
"Mvaa 40,176,192\nTa 230,132,18432\nMvaa 40,180,196\nD0 5,20,45,3072\nMvaa 44,188,196\nTa 232,133,0\nMvaa 44,184,192\nTa 234,133,2048\nMaaa 108,184,200\ns_add_u32 m0, s47, 32768\n"
"s_add_u32 s44, s44, 0x40000\nMaaa 108,188,204\nTa 236,133,16384\nMaaa 104,180,204\nTa 238,133,18432\nD0 6,24,44,0\nMaaa 104,176,200\nv_mul_f32 v32, s57, v32\n"
"v_mul_f32 v33, s57, v33\nMaaa 100,168,200\nTa 240,134,0\nCV 32,32,33\nMaaa 100,172,204\nTa 242,134,2048\nv_mul_f32 v34, s57, v34\nMaaa 96,164,204\nv_mul_f32 v35, s57, v35\n"
"CV 33,34,35\nMaaa 96,160,200\nTa 244,134,16384\nD0 7,24,44,1024\nMvaa 192,160,208\nTa 246,134,18432\nv_mul_f32 v36, s57, v36\nMvaa 192,164,212\nv_mul_f32 v37, s57, v37\n"
"Mvaa 196,172,212\nTa 248,135,0\nCV 34,36,37\nMvaa 196,168,208\nTa 250,135,2048\nv_mul_f32 v38, s57, v38\nMvaa 200,176,208\nD0 8,24,44,2048\nv_mul_f32 v39, s57, v39\n"
"Mvaa 200,180,212\nTa 252,135,16384\nCV 35,38,39\nMvaa 204,188,212\nTa 254,135,18432\nv_mul_f32 v40, s57, v40\nMvaa 204,184,208\nv_mul_f32 v41, s57, v41\nCV 40,40,41\n"
"Mvaa 236,184,216\nv_mul_f32 v42, s57, v42\nv_mul_f32 v43, s57, v43\nMvaa 236,188,220\nD0 9,24,44,3072\nMvaa 232,180,220\nCV 41,42,43\nv_mul_f32 v44, s57, v44\nMvaa 232,176,216\n"
"v_mul_f32 v45, s57, v45\nCV 42,44,45\nMvaa 228,168,216\nv_mul_f32 v46, s57, v46\nv_mul_f32 v47, s57, v47\nMvaa 228,172,220\nCV 43,46,47\nAR 20,96\nMvaa 224,164,220\nAR 21,97\n"
"v_mul_f32 v20, s57, v20\nMvaa 224,160,216\ns_waitcnt vmcnt(14) lgkmcnt(0)\ns_barrier\nMvaa 48,224,192\ns_add_u32 m0, s47, 49152\ns_add_u32 s44, s44, 0x40000\nRa 128,10,0\n"
"Mvaa 48,228,196\nRa 132,11,0\nMvaa 52,236,196\nD0 6,24,44,0\nRa 136,10,2048\n.W538:\ns_cbranch_execz .W542\nMvaa 52,232,192\nRa 140,11,2048\nv_mul_f32 v21, s57, v21\n"
"Mvaa 56,240,192\nRa 144,10,8192\nCV 36,20,21\nMvaa 56,244,196\nRa 148,11,8192\nAR 20,98\nMvaa 60,252,196\nD0 7,24,44,1024\nRa 152,10,10240\nMvaa 60,248,192\nRa 156,11,10240\n"
"AR 21,99\nMaaa 124,248,200\nTa 160,12,0\nv_mul_f32 v20, s57, v20\nMaaa 124,252,204\nTa 162,12,2048\nMaaa 120,244,204\nD0 8,24,44,2048\nTa 164,12,16384\nMaaa 120,240,200\n"
"Ta 166,12,18432\nv_mul_f32 v21, s57, v21\nMaaa 116,232,200\nTa 168,13,0\nCV 37,20,21\nMaaa 116,236,204\nTa 170,13,2048\nAR 20,100\nMaaa 112,228,204\nD0 9,24,44,3072\n"
"Ta 172,13,16384\nMaaa 112,224,200\nTa 174,13,18432\nAR 21,101\nMvaa 208,224,208\ns_add_u32 m0, s47, -16384\ns_add_u32 s48, s48, 128\nTa 176,14,0\nMvaa 208,228,212\nTa 178,14,2048\n"
"Mvaa 212,236,212\nD0 2,20,48,0\nTa 180,14,16384\nMvaa 212,232,208\nTa 182,14,18432\nv_mul_f32 v20, s57, v20\nMvaa 216,240,208\nTa 184,15,0\nv_mul_f32 v21, s57, v21\n"
"Mvaa 216,244,212\nTa 186,15,2048\nCV 38,20,21\nMvaa 220,252,212\nD0 3,20,48,1024\nTa 188,15,16384\nMvaa 220,248,208\nTa 190,15,18432\nAR 20,102\nMvaa 252,248,216\nAR 21,103\n"
"v_mul_f32 v20, s57, v20\nMvaa 252,252,220\nv_mul_f32 v21, s57, v21\nMvaa 248,244,220\nD0 4,20,48,2048\nCV 39,20,21\nMvaa 248,240,216\nAR 20,104\nAR 21,105\nMvaa 244,232,216\n"
"v_mul_f32 v20, s57, v20\nv_mul_f32 v21, s57, v21\nMvaa 244,236,220\nCV 44,20,21\nAR 20,106\nMvaa 240,228,220\nD0 5,20,48,3072\nAR 21,107\nMvaa 240,224,216\nWL 0\ns_barrier\n"
"Zvaa 64,160,128\nTa 224,16,0\nv_mul_f32 v20, s57, v20\nMvaa 64,164,132\nTa 226,16,2048\nZvaa 96,164,140\ns_add_u32 m0, s47, -65536\ns_add_u32 s45, s45, 128\nMvaa 96,160,136\n"
"Ta 228,16,16384\nv_mul_f32 v21, s57, v21\nZvaa 128,160,144\nTa 230,16,18432\nCV 45,20,21\nMvaa 128,164,148\nD0 2,20,45,0\nAR 20,108\nZvaa 160,164,156\nTa 232,17,0\nAR 21,109\n"
"Mvaa 160,160,152\nTa 234,17,2048\nv_mul_f32 v20, s57, v20\nZvaa 164,168,152\nv_mul_f32 v21, s57, v21\nCV 46,20,21\nMvaa 164,172,156\nTa 236,17,16384\ns_add_u32 s6, s56, 0\n"
"Sv 32,1,16,6,0\nZvaa 132,172,148\nTa 238,17,18432\nAR 20,110\nMvaa 132,168,144\nAR 21,111\nv_mul_f32 v20, s57, v20\nZvaa 100,168,136\nTa 240,18,0\nv_mul_f32 v21, s57, v21\n"
"Mvaa 100,172,140\nTa 242,18,2048\nCV 47,20,21\nZvaa 68,172,132\nD0 3,20,45,1024\nv_mul_f32 v48, s57, v48\nMvaa 68,168,128\nTa 244,18,16384\nv_mul_f32 v49, s57, v49\n"
"Zvaa 72,176,128\nTa 246,18,18432\nCV 48,48,49\nMvaa 72,180,132\nv_mul_f32 v50, s57, v50\nZvaa 104,180,140\nTa 248,19,0\nv_mul_f32 v51, s57, v51\nMvaa 104,176,136\nTa 250,19,2048\n"
"CV 49,50,51\nZvaa 136,176,144\nv_mul_f32 v52, s57, v52\nv_mul_f32 v53, s57, v53\nMvaa 136,180,148\nTa 252,19,16384\nCV 50,52,53\nZvaa 168,180,156\nTa 254,19,18432\n"
"v_mul_f32 v54, s57, v54\nMvaa 168,176,152\nD0 4,20,45,2048\nv_mul_f32 v55, s57, v55\nZvaa 172,184,152\nCV 51,54,55\nv_mul_f32 v56, s57, v56\nMvaa 172,188,156\n"
"v_mul_f32 v57, s57, v57\ns_add_u32 s6, s56, 0\nSv 40,1,16,6,2048\nZvaa 140,188,148\nCV 56,56,57\nv_mul_f32 v58, s57, v58\nMvaa 140,184,144\nv_mul_f32 v59, s57, v59\nCV 57,58,59\n"
"Zvaa 108,184,136\nv_mul_f32 v60, s57, v60\nv_mul_f32 v61, s57, v61\nMvaa 108,188,140\nD0 5,20,45,3072\nCV 58,60,61\nZvaa 76,188,132\nv_mul_f32 v62, s57, v62\n"
"v_mul_f32 v63, s57, v63\nMvaa 76,184,128\ns_waitcnt vmcnt(18) lgkmcnt(0)\ns_barrier\nZvaa 80,224,128\nRa 192,10,16384\nCV 59,62,63\nMvaa 80,228,132\ns_add_u32 m0, s47, 0\n"
"s_add_u32 s44, s44, 0x40000\nZvaa 112,228,140\nRa 196,11,16384\nAR 20,112\nMvaa 112,224,136\nAR 21,113\nv_mul_f32 v20, s57, v20\nZvaa 144,224,144\nRa 200,10,18432\n"
"v_mul_f32 v21, s57, v21\nMvaa 144,228,148\nD0 6,24,44,0\nCV 52,20,21\nZvaa 176,228,156\nRa 204,11,18432\nAR 20,114\nMvaa 176,224,152\nAR 21,115\nv_mul_f32 v20, s57, v20\n"
"Zvaa 180,232,152\nRa 208,10,24576\nv_mul_f32 v21, s57, v21\nMvaa 180,236,156\nCV 53,20,21\ns_add_u32 s6, s56, 0\nSv 36,1,16,6,1024\nZvaa 148,236,148\nRa 212,11,24576\nAR 20,116\n"
"Mvaa 148,232,144\nAR 21,117\nv_mul_f32 v20, s57, v20\nZvaa 116,232,136\nRa 216,10,26624\nv_mul_f32 v21, s57, v21\nMvaa 116,236,140\nD0 7,24,44,1024\nCV 54,20,21\nZvaa 84,236,132\n"
"Ra 220,11,26624\nAR 20,118\nMvaa 84,232,128\nAR 21,119\nv_mul_f32 v20, s57, v20\nZvaa 88,240,128\nv_mul_f32 v21, s57, v21\nCV 55,20,21\nMvaa 88,244,132\nAR 20,120\n"
"Zvaa 120,244,140\nAR 21,121\nv_mul_f32 v20, s57, v20\nMvaa 120,240,136\nv_mul_f32 v21, s57, v21\nCV 60,20,21\nZvaa 152,240,144\nAR 20,122\nAR 21,123\nMvaa 152,244,148\n"
"D0 8,24,44,2048\nv_mul_f32 v20, s57, v20\nZvaa 184,244,156\nv_mul_f32 v21, s57, v21\nCV 61,20,21\nMvaa 184,240,152\nAR 20,124\nAR 21,125\nZvaa 188,248,152\n"
"v_mul_f32 v20, s57, v20\nv_mul_f32 v21, s57, v21\nMvaa 188,252,156\nCV 62,20,21\ns_add_u32 s6, s56, 0\nSv 44,1,16,6,3072\nZvaa 156,252,148\nAR 20,126\nAR 21,127\nMvaa 156,248,144\n"
"v_mul_f32 v20, s57, v20\nv_mul_f32 v21, s57, v21\nZvaa 124,248,136\nCV 63,20,21\nv_mul_f32 v192, s57, v192\nMvaa 124,252,140\nD0 9,24,44,3072\nv_mul_f32 v193, s57, v193\n"
"Zvaa 92,252,132\nCV 192,192,193\nv_mul_f32 v194, s57, v194\nMvaa 92,248,128\nWL 0\nZaaa 16,224,192\nRa 128,10,32768\nv_mul_f32 v195, s57, v195\nMaaa 16,228,196\n"
"s_add_u32 m0, s47, 16384\ns_add_u32 s44, s44, 0x40000\nZaaa 48,228,204\nRa 132,11,32768\nCV 193,194,195\nMaaa 48,224,200\nv_mul_f32 v196, s57, v196\nv_mul_f32 v197, s57, v197\n"
"Zaaa 80,224,208\nRa 136,10,34816\nCV 194,196,197\nMaaa 80,228,212\nD0 6,24,44,0\nv_mul_f32 v198, s57, v198\nZaaa 112,228,220\nRa 140,11,34816\nv_mul_f32 v199, s57, v199\n"
"Maaa 112,224,216\nCV 195,198,199\nv_mul_f32 v200, s57, v200\nZaaa 116,232,216\nRa 144,10,40960\nv_mul_f32 v201, s57, v201\nMaaa 116,236,220\nCV 200,200,201\n"
"v_mul_f32 v202, s57, v202\ns_add_u32 s6, s56, 4096\nSv 48,1,16,6,0\nZaaa 84,236,212\nRa 148,11,40960\nv_mul_f32 v203, s57, v203\nMaaa 84,232,208\nCV 201,202,203\n"
"v_mul_f32 v204, s57, v204\nZaaa 52,232,200\nRa 152,10,43008\nv_mul_f32 v205, s57, v205\nMaaa 52,236,204\nD0 7,24,44,1024\nCV 202,204,205\nZaaa 20,236,196\nRa 156,11,43008\n"
"v_mul_f32 v206, s57, v206\nMaaa 20,232,192\nv_mul_f32 v207, s57, v207\nCV 203,206,207\nZaaa 24,240,192\nv_mul_f32 v208, s57, v208\nv_mul_f32 v209, s57, v209\nMaaa 24,244,196\n"
"CV 208,208,209\nv_mul_f32 v210, s57, v210\nZaaa 56,244,204\nv_mul_f32 v211, s57, v211\nCV 209,210,211\nMaaa 56,240,200\nv_mul_f32 v212, s57, v212\nv_mul_f32 v213, s57, v213\n"
"Zaaa 88,240,208\nCV 210,212,213\nv_mul_f32 v214, s57, v214\nMaaa 88,244,212\nD0 8,24,44,2048\nv_mul_f32 v215, s57, v215\nZaaa 120,244,220\nCV 211,214,215\n"
"v_mul_f32 v216, s57, v216\nMaaa 120,240,216\nv_mul_f32 v217, s57, v217\nCV 216,216,217\nZaaa 124,248,216\nv_mul_f32 v218, s57, v218\nv_mul_f32 v219, s57, v219\nMaaa 124,252,220\n"
"CV 217,218,219\nv_mul_f32 v220, s57, v220\ns_add_u32 s6, s56, 4096\nSv 56,1,16,6,2048\nZaaa 92,252,212\nv_mul_f32 v221, s57, v221\nCV 218,220,221\nMaaa 92,248,208\n"
"v_mul_f32 v222, s57, v222\nv_mul_f32 v223, s57, v223\nZaaa 60,248,200\nCV 219,222,223\nv_mul_f32 v224, s57, v224\nMaaa 60,252,204\nD0 9,24,44,3072\nv_mul_f32 v225, s57, v225\n"
"Zaaa 28,252,196\nCV 224,224,225\nv_mul_f32 v226, s57, v226\nMaaa 28,248,192\ns_waitcnt vmcnt(22) lgkmcnt(0)\ns_barrier\nZaaa 0,160,192\nTa 224,16,32768\nv_mul_f32 v227, s57, v227\n"
"Maaa 0,164,196\nTa 226,16,34816\nCV 225,226,227\nZaaa 32,164,204\ns_add_u32 m0, s47, -49152\ns_add_u32 s48, s48, 128\nMaaa 32,160,200\nTa 228,16,49152\nv_mul_f32 v228, s57, v228\n"
"Zaaa 64,160,208\nTa 230,16,51200\nv_mul_f32 v229, s57, v229\nMaaa 64,164,212\nD0 2,20,48,0\nCV 226,228,229\nZaaa 96,164,220\nTa 232,17,32768\nv_mul_f32 v230, s57, v230\n"
"Maaa 96,160,216\nTa 234,17,34816\nv_mul_f32 v231, s57, v231\nZaaa 100,168,216\nCV 227,230,231\nv_mul_f32 v232, s57, v232\nMaaa 100,172,220\nTa 236,17,49152\n"
"v_mul_f32 v233, s57, v233\ns_add_u32 s6, s56, 4096\nSv 52,1,16,6,1024\nZaaa 68,172,212\nTa 238,17,51200\nCV 232,232,233\nMaaa 68,168,208\nv_mul_f32 v234, s57, v234\n"
"v_mul_f32 v235, s57, v235\nZaaa 36,168,200\nTa 240,18,32768\nCV 233,234,235\nMaaa 36,172,204\nTa 242,18,34816\nv_mul_f32 v236, s57, v236\nZaaa 4,172,196\nD0 3,20,48,1024\n"
"v_mul_f32 v237, s57, v237\nMaaa 4,168,192\nTa 244,18,49152\nCV 234,236,237\nZaaa 8,176,192\nTa 246,18,51200\nv_mul_f32 v238, s57, v238\nMaaa 8,180,196\nv_mul_f32 v239, s57, v239\n"
"CV 235,238,239\nZaaa 40,180,204\nTa 248,19,32768\nv_mul_f32 v240, s57, v240\nMaaa 40,176,200\nTa 250,19,34816\nv_mul_f32 v241, s57, v241\nZaaa 72,176,208\nCV 240,240,241\n"
"v_mul_f32 v242, s57, v242\nMaaa 72,180,212\nTa 252,19,49152\nv_mul_f32 v243, s57, v243\nZaaa 104,180,220\nTa 254,19,51200\nCV 241,242,243\nMaaa 104,176,216\nD0 4,20,48,2048\n"
"v_mul_f32 v244, s57, v244\nZaaa 108,184,216\nv_mul_f32 v245, s57, v245\nCV 242,244,245\nMaaa 108,188,220\nv_mul_f32 v246, s57, v246\nv_mul_f32 v247, s57, v247\n"
"s_add_u32 s6, s56, 4096\nSv 60,1,16,6,3072\nZaaa 76,188,212\nCV 243,246,247\nv_mul_f32 v248, s57, v248\nMaaa 76,184,208\nv_mul_f32 v249, s57, v249\nCV 248,248,249\n"
"Zaaa 44,184,200\nv_mul_f32 v250, s57, v250\nv_mul_f32 v251, s57, v251\nMaaa 44,188,204\nD0 5,20,48,3072\nCV 249,250,251\nZaaa 12,188,196\nv_mul_f32 v252, s57, v252\n"
"v_mul_f32 v253, s57, v253\nMaaa 12,184,192\nWL 0\nMvaa 80,224,128\nTa 160,12,32768\nCV 250,252,253\nMvaa 80,228,132\nTa 162,12,34816\nv_mul_f32 v254, s57, v254\nMvaa 112,228,140\n"
"s_add_u32 m0, s47, -32768\ns_add_u32 s45, s45, 128\nMvaa 112,224,136\nTa 164,12,49152\nv_mul_f32 v255, s57, v255\nMvaa 144,224,144\nTa 166,12,51200\nCV 251,254,255\n"
"Mvaa 144,228,148\nD0 2,20,45,0\nMvaa 176,228,156\nTa 168,13,32768\nMvaa 176,224,152\nTa 170,13,34816\nMvaa 180,232,152\nMvaa 180,236,156\nTa 172,13,49152\ns_add_u32 s6, s53, 0\n"
"Sv 192,1,16,6,0\nMvaa 148,236,148\nTa 174,13,51200\nMvaa 148,232,144\nMvaa 116,232,136\nTa 176,14,32768\nMvaa 116,236,140\nTa 178,14,34816\nMvaa 84,236,132\nD0 3,20,45,1024\n"
"Mvaa 84,232,128\nTa 180,14,49152\nMvaa 88,240,128\nTa 182,14,51200\nMvaa 88,244,132\nMvaa 120,244,140\nTa 184,15,32768\nMvaa 120,240,136\nTa 186,15,34816\nMvaa 152,240,144\n"
"Mvaa 152,244,148\nTa 188,15,49152\nMvaa 184,244,156\nTa 190,15,51200\nMvaa 184,240,152\nD0 4,20,45,2048\nMvaa 188,248,152\nMvaa 188,252,156\ns_add_u32 s6, s53, 0\n"
"Sv 200,1,16,6,2048\nMvaa 156,252,148\nMvaa 156,248,144\nMvaa 124,248,136\nMvaa 124,252,140\nD0 5,20,45,3072\nMvaa 92,252,132\nMvaa 92,248,128\ns_waitcnt vmcnt(24) lgkmcnt(0)\n"
"s_barrier\nMvaa 64,160,128\nRa 192,10,49152\nMvaa 64,164,132\ns_add_u32 m0, s47, 32768\ns_add_u32 s44, s44, 0x40000\nMvaa 96,164,140\nRa 196,11,49152\nMvaa 96,160,136\n"
"Mvaa 128,160,144\nRa 200,10,51200\nMvaa 128,164,148\nD0 6,24,44,0\nMvaa 160,164,156\nRa 204,11,51200\nMvaa 160,160,152\nMvaa 164,168,152\nRa 208,10,57344\nMvaa 164,172,156\n"
"s_add_u32 s6, s53, 4096\nSv 208,1,16,6,0\nMvaa 132,172,148\nRa 212,11,57344\nMvaa 132,168,144\nMvaa 100,168,136\nRa 216,10,59392\nMvaa 100,172,140\nD0 7,24,44,1024\n"
"Mvaa 68,172,132\nRa 220,11,59392\nMvaa 68,168,128\nMvaa 72,176,128\nMvaa 72,180,132\nMvaa 104,180,140\nMvaa 104,176,136\nMvaa 136,176,144\nMvaa 136,180,148\n.W539:\n"
"s_cbranch_execz .W543\nD0 8,24,44,2048\nMvaa 168,180,156\nMvaa 168,176,152\nMvaa 172,184,152\nMvaa 172,188,156\ns_add_u32 s6, s53, 4096\nSv 216,1,16,6,2048\nMvaa 140,188,148\n"
"Mvaa 140,184,144\nMvaa 108,184,136\nMvaa 108,188,140\nD0 9,24,44,3072\nMvaa 76,188,132\nMvaa 76,184,128\nWL 0\nMaaa 0,160,192\nRa 128,10,0\nMaaa 0,164,196\n"
"s_add_u32 m0, s47, 49152\ns_add_u32 s44, s44, 0x40000\nMaaa 32,164,204\nRa 132,11,0\nMaaa 32,160,200\nMaaa 64,160,208\nRa 136,10,2048\nMaaa 64,164,212\nD0 6,24,44,0\n"
"Maaa 96,164,220\nRa 140,11,2048\nMaaa 96,160,216\nMaaa 100,168,216\nRa 144,10,8192\nMaaa 100,172,220\ns_add_u32 s6, s53, 0\nSv 224,1,16,6,1024\nMaaa 68,172,212\nRa 148,11,8192\n"
"Maaa 68,168,208\nMaaa 36,168,200\nRa 152,10,10240\nMaaa 36,172,204\nD0 7,24,44,1024\nMaaa 4,172,196\nRa 156,11,10240\nMaaa 4,168,192\nMaaa 8,176,192\nMaaa 8,180,196\n"
"Maaa 40,180,204\nMaaa 40,176,200\nMaaa 72,176,208\nMaaa 72,180,212\nD0 8,24,44,2048\nMaaa 104,180,220\nMaaa 104,176,216\nMaaa 108,184,216\nMaaa 108,188,220\ns_add_u32 s6, s53, 0\n"
"Sv 232,1,16,6,3072\nMaaa 76,188,212\nMaaa 76,184,208\nMaaa 44,184,200\nMaaa 44,188,204\nD0 9,24,44,3072\nMaaa 12,188,196\nMaaa 12,184,192\ns_waitcnt vmcnt(24) lgkmcnt(0)\n"
"s_barrier\nMaaa 16,224,192\nTa 160,12,0\nMaaa 16,228,196\nTa 162,12,2048\nMaaa 48,228,204\ns_add_u32 m0, s47, -16384\ns_add_u32 s48, s48, 128\nMaaa 48,224,200\nTa 164,12,16384\n"
"Maaa 80,224,208\nTa 166,12,18432\nMaaa 80,228,212\nD0 2,20,48,0\nMaaa 112,228,220\nTa 168,13,0\nMaaa 112,224,216\nTa 170,13,2048\nMaaa 116,232,216\nMaaa 116,236,220\n"
"Ta 172,13,16384\ns_add_u32 s6, s53, 4096\nSv 240,1,16,6,1024\nMaaa 84,236,212\nTa 174,13,18432\nMaaa 84,232,208\nMaaa 52,232,200\nTa 176,14,0\nMaaa 52,236,204\nTa 178,14,2048\n"
"Maaa 20,236,196\nD0 3,20,48,1024\nMaaa 20,232,192\nTa 180,14,16384\nMaaa 24,240,192\nTa 182,14,18432\nMaaa 24,244,196\ns_add_u32 s6, s53, 4096\nSv 248,1,16,6,3072\n"
"Maaa 56,244,204\nTa 184,15,0\nMaaa 56,240,200\nTa 186,15,2048\nMaaa 88,240,208\nMaaa 88,244,212\nTa 188,15,16384\nMaaa 120,244,220\nTa 190,15,18432\nMaaa 120,240,216\n"
"D0 4,20,48,2048\nMaaa 124,248,216\nMaaa 124,252,220\nMaaa 92,252,212\nMaaa 92,248,208\ns_lshl3_add_u32 s90, s12, s13\ns_lshl2_add_u32 s90, s90, s11\n"
"s_lshl2_add_u32 s90, s90, 0xc04a400\nMaaa 60,248,200\nMaaa 60,252,204\nD0 5,20,48,3072\nMaaa 28,252,196\nMaaa 28,248,192\nWL 0\ns_mov_b64 s[76:77], s[62:63]\n"
"s_mov_b64 s[78:79], s[64:65]\ns_mov_b32 s42, 1605632\ns_mov_b32 s50, 1\n.GL20:\nMvaa 64,160,128\nTa 224,16,0\nMvaa 64,164,132\nTa 226,16,2048\nMvaa 96,164,140\n"
"s_add_u32 m0, s47, -65536\ns_add_u32 s45, s45, 128\nMvaa 96,160,136\nTa 228,16,16384\nMvaa 128,160,144\nTa 230,16,18432\nMvaa 128,164,148\nD0 2,20,45,0\nMvaa 160,164,156\n"
"Ta 232,17,0\nMvaa 160,160,152\nTa 234,17,2048\nMvaa 164,168,152\nMvaa 164,172,156\nTa 236,17,16384\nMvaa 132,172,148\nTa 238,17,18432\nMvaa 132,168,144\nMvaa 100,168,136\n"
"Ta 240,18,0\nMvaa 100,172,140\nTa 242,18,2048\nMvaa 68,172,132\nD0 3,20,45,1024\nMvaa 68,168,128\nTa 244,18,16384\nMvaa 72,176,128\nTa 246,18,18432\nMvaa 72,180,132\n"
"Mvaa 104,180,140\nTa 248,19,0\nMvaa 104,176,136\nTa 250,19,2048\nMvaa 136,176,144\nMvaa 136,180,148\nTa 252,19,16384\nMvaa 168,180,156\nTa 254,19,18432\nMvaa 168,176,152\n"
"D0 4,20,45,2048\nMvaa 172,184,152\nMvaa 172,188,156\nMvaa 140,188,148\nMvaa 140,184,144\ns_mov_b32 s40, s76\ns_and_b32 s41, s77, 0xffff\nMvaa 108,184,136\nMvaa 108,188,140\n"
"D0 5,20,45,3072\nMvaa 76,188,132\nMvaa 76,184,128\ns_waitcnt vmcnt(17) lgkmcnt(0)\ns_barrier\nMvaa 80,224,128\nRa 192,10,16384\nMvaa 80,228,132\ns_add_u32 m0, s47, 0\n"
"s_add_u32 s44, s44, 0x40000\nbuffer_load_dword v31, v26, s[40:43], s97 offen\ns_mov_b64 s[76:77], s[66:67]\nMvaa 112,228,140\nRa 196,11,16384\nMvaa 112,224,136\nMvaa 144,224,144\n"
"Ra 200,10,18432\nMvaa 144,228,148\nD0 6,24,44,0\nMvaa 176,228,156\nRa 204,11,18432\nMvaa 176,224,152\nMvaa 180,232,152\nRa 208,10,24576\nMvaa 180,236,156\nMvaa 148,236,148\n"
"Ra 212,11,24576\nMvaa 148,232,144\nMvaa 116,232,136\nRa 216,10,26624\nMvaa 116,236,140\nD0 7,24,44,1024\nMvaa 84,236,132\nRa 220,11,26624\nMvaa 84,232,128\nMvaa 88,240,128\n"
"Mvaa 88,244,132\nMvaa 120,244,140\nMvaa 120,240,136\nMvaa 152,240,144\nMvaa 152,244,148\nD0 8,24,44,2048\nMvaa 184,244,156\nMvaa 184,240,152\nMvaa 188,248,152\nMvaa 188,252,156\n"
"Mvaa 156,252,148\nMvaa 156,248,144\nMvaa 124,248,136\nMvaa 124,252,140\nD0 9,24,44,3072\nMvaa 92,252,132\nMvaa 92,248,128\nWL 0\nMaaa 16,224,192\nRa 128,10,32768\nMaaa 16,228,196\n"
"s_add_u32 m0, s47, 16384\ns_add_u32 s44, s44, 0x40000\nMaaa 48,228,204\nRa 132,11,32768\nMaaa 48,224,200\nMaaa 80,224,208\nRa 136,10,34816\nMaaa 80,228,212\nD0 6,24,44,0\n"
"Maaa 112,228,220\nRa 140,11,34816\nMaaa 112,224,216\nMaaa 116,232,216\nRa 144,10,40960\nMaaa 116,236,220\nMaaa 84,236,212\nRa 148,11,40960\nMaaa 84,232,208\nMaaa 52,232,200\n"
"Ra 152,10,43008\nMaaa 52,236,204\nD0 7,24,44,1024\nMaaa 20,236,196\nRa 156,11,43008\nMaaa 20,232,192\nMaaa 24,240,192\nMaaa 24,244,196\nMaaa 56,244,204\nMaaa 56,240,200\n"
"Maaa 88,240,208\nMaaa 88,244,212\nD0 8,24,44,2048\nMaaa 120,244,220\nMaaa 120,240,216\nMaaa 124,248,216\nMaaa 124,252,220\nMaaa 92,252,212\nMaaa 92,248,208\nMaaa 60,248,200\n"
"Maaa 60,252,204\nD0 9,24,44,3072\nMaaa 28,252,196\nMaaa 28,248,192\ns_waitcnt vmcnt(17) lgkmcnt(0)\ns_barrier\nMaaa 0,160,192\nTa 224,16,32768\nMaaa 0,164,196\nTa 226,16,34816\n"
"Maaa 32,164,204\ns_add_u32 m0, s47, -49152\ns_add_u32 s48, s48, 128\nMaaa 32,160,200\nTa 228,16,49152\nMaaa 64,160,208\nTa 230,16,51200\nMaaa 64,164,212\nD0 2,20,48,0\n"
"Maaa 96,164,220\nTa 232,17,32768\nMaaa 96,160,216\nTa 234,17,34816\nMaaa 100,168,216\nMaaa 100,172,220\nTa 236,17,49152\nMaaa 68,172,212\nTa 238,17,51200\nMaaa 68,168,208\n"
"Maaa 36,168,200\nTa 240,18,32768\nMaaa 36,172,204\nTa 242,18,34816\nMaaa 4,172,196\nD0 3,20,48,1024\nMaaa 4,168,192\nTa 244,18,49152\nMaaa 8,176,192\nTa 246,18,51200\n"
"Maaa 8,180,196\nMaaa 40,180,204\nTa 248,19,32768\nMaaa 40,176,200\nTa 250,19,34816\nMaaa 72,176,208\nMaaa 72,180,212\nTa 252,19,49152\nMaaa 104,180,220\nTa 254,19,51200\n"
"Maaa 104,176,216\nD0 4,20,48,2048\nMaaa 108,184,216\nMaaa 108,188,220\nMaaa 76,188,212\nMaaa 76,184,208\nMaaa 44,184,200\nMaaa 44,188,204\nD0 5,20,48,3072\nMaaa 12,188,196\n"
"Maaa 12,184,192\nWL 0\nMvaa 80,224,128\nTa 160,12,32768\nMvaa 80,228,132\nTa 162,12,34816\nMvaa 112,228,140\ns_add_u32 m0, s47, -32768\ns_add_u32 s45, s45, 128\nMvaa 112,224,136\n"
"Ta 164,12,49152\nMvaa 144,224,144\nTa 166,12,51200\nMvaa 144,228,148\nD0 2,20,45,0\nMvaa 176,228,156\nTa 168,13,32768\nMvaa 176,224,152\nTa 170,13,34816\nMvaa 180,232,152\n"
"Mvaa 180,236,156\nTa 172,13,49152\nMvaa 148,236,148\nTa 174,13,51200\nMvaa 148,232,144\nMvaa 116,232,136\nTa 176,14,32768\nMvaa 116,236,140\nTa 178,14,34816\nMvaa 84,236,132\n"
"D0 3,20,45,1024\nMvaa 84,232,128\nTa 180,14,49152\nMvaa 88,240,128\nTa 182,14,51200\nMvaa 88,244,132\nMvaa 120,244,140\nTa 184,15,32768\nMvaa 120,240,136\nTa 186,15,34816\n"
"Mvaa 152,240,144\nMvaa 152,244,148\nTa 188,15,49152\nMvaa 184,244,156\nTa 190,15,51200\nMvaa 184,240,152\nD0 4,20,45,2048\nMvaa 188,248,152\nMvaa 188,252,156\nMvaa 156,252,148\n"
"Mvaa 156,248,144\ns_mov_b32 s40, s78\ns_and_b32 s41, s79, 0xffff\nMvaa 124,248,136\nMvaa 124,252,140\nD0 5,20,45,3072\nMvaa 92,252,132\nMvaa 92,248,128\n"
"s_waitcnt vmcnt(17) lgkmcnt(0)\ns_barrier\nMvaa 64,160,128\nRa 192,10,49152\nMvaa 64,164,132\ns_add_u32 m0, s47, 32768\ns_add_u32 s44, s44, 0x40000\n"
"buffer_load_dword v31, v26, s[40:43], s97 offen\ns_mov_b64 s[78:79], s[66:67]\nMvaa 96,164,140\nRa 196,11,49152\nMvaa 96,160,136\nMvaa 128,160,144\nRa 200,10,51200\n"
"Mvaa 128,164,148\nD0 6,24,44,0\nMvaa 160,164,156\nRa 204,11,51200\nMvaa 160,160,152\nMvaa 164,168,152\nRa 208,10,57344\nMvaa 164,172,156\nMvaa 132,172,148\nRa 212,11,57344\n"
"Mvaa 132,168,144\nMvaa 100,168,136\nRa 216,10,59392\nMvaa 100,172,140\nD0 7,24,44,1024\nMvaa 68,172,132\nRa 220,11,59392\nMvaa 68,168,128\nMvaa 72,176,128\nMvaa 72,180,132\n"
"Mvaa 104,180,140\nMvaa 104,176,136\nMvaa 136,176,144\nMvaa 136,180,148\nD0 8,24,44,2048\nMvaa 168,180,156\nMvaa 168,176,152\nMvaa 172,184,152\nMvaa 172,188,156\nMvaa 140,188,148\n"
"Mvaa 140,184,144\nMvaa 108,184,136\nMvaa 108,188,140\nD0 9,24,44,3072\nMvaa 76,188,132\nMvaa 76,184,128\nWL 0\nMaaa 0,160,192\nRa 128,10,0\nMaaa 0,164,196\n"
"s_add_u32 m0, s47, 49152\ns_add_u32 s44, s44, 0x40000\nMaaa 32,164,204\nRa 132,11,0\nMaaa 32,160,200\nMaaa 64,160,208\nRa 136,10,2048\nMaaa 64,164,212\nD0 6,24,44,0\n"
"Maaa 96,164,220\nRa 140,11,2048\nMaaa 96,160,216\nMaaa 100,168,216\nRa 144,10,8192\nMaaa 100,172,220\nMaaa 68,172,212\nRa 148,11,8192\nMaaa 68,168,208\nMaaa 36,168,200\n"
"Ra 152,10,10240\nMaaa 36,172,204\nD0 7,24,44,1024\nMaaa 4,172,196\nRa 156,11,10240\nMaaa 4,168,192\nMaaa 8,176,192\nMaaa 8,180,196\nMaaa 40,180,204\nMaaa 40,176,200\n"
"Maaa 72,176,208\nMaaa 72,180,212\nD0 8,24,44,2048\nMaaa 104,180,220\nMaaa 104,176,216\nMaaa 108,184,216\nMaaa 108,188,220\nMaaa 76,188,212\nMaaa 76,184,208\nMaaa 44,184,200\n"
"Maaa 44,188,204\nD0 9,24,44,3072\nMaaa 12,188,196\nMaaa 12,184,192\ns_waitcnt vmcnt(17) lgkmcnt(0)\ns_barrier\nMaaa 16,224,192\nTa 160,12,0\nMaaa 16,228,196\nTa 162,12,2048\n"
"Maaa 48,228,204\ns_add_u32 m0, s47, -16384\ns_add_u32 s48, s48, 128\nMaaa 48,224,200\nTa 164,12,16384\nMaaa 80,224,208\nTa 166,12,18432\nMaaa 80,228,212\nD0 2,20,48,0\n"
"Maaa 112,228,220\nTa 168,13,0\nMaaa 112,224,216\nTa 170,13,2048\nMaaa 116,232,216\nMaaa 116,236,220\nTa 172,13,16384\nMaaa 84,236,212\nTa 174,13,18432\nMaaa 84,232,208\n"
"Maaa 52,232,200\nTa 176,14,0\nMaaa 52,236,204\nTa 178,14,2048\n.W540:\ns_cbranch_execz .W544\nMaaa 20,236,196\nD0 3,20,48,1024\nMaaa 20,232,192\nTa 180,14,16384\nMaaa 24,240,192\n"
"Ta 182,14,18432\nMaaa 24,244,196\nMaaa 56,244,204\nTa 184,15,0\nMaaa 56,240,200\nTa 186,15,2048\nMaaa 88,240,208\nMaaa 88,244,212\nTa 188,15,16384\nMaaa 120,244,220\n"
"Ta 190,15,18432\nMaaa 120,240,216\nD0 4,20,48,2048\nMaaa 124,248,216\nMaaa 124,252,220\nMaaa 92,252,212\nMaaa 92,248,208\nMaaa 60,248,200\nMaaa 60,252,204\nD0 5,20,48,3072\n"
"s_sub_u32 s50, s50, 1\nMaaa 28,252,196\nMaaa 28,248,192\nWL 0\ns_cbranch_scc0 .GL20\nMvaa 64,160,128\nTa 224,16,0\nMvaa 64,164,132\nTa 226,16,2048\nMvaa 96,164,140\n"
"s_add_u32 m0, s47, -65536\ns_add_u32 s45, s45, 128\nMvaa 96,160,136\nTa 228,16,16384\nMvaa 128,160,144\nTa 230,16,18432\nMvaa 128,164,148\nD0 2,20,45,0\nMvaa 160,164,156\n"
"Ta 232,17,0\nMvaa 160,160,152\nTa 234,17,2048\nMvaa 164,168,152\ns_and_b32 s2, s74, 15\nMvaa 164,172,156\nTa 236,17,16384\n"
"buffer_store_dword v25, off, s[16:19], s90 offset:0 sc0 sc1\nMvaa 132,172,148\nTa 238,17,18432\nMvaa 132,168,144\ns_sub_u32 s2, s2, 2\ns_cmp_lt_u32 s2, 14\n"
"s_cselect_b32 s5, 0, 0x40000000\nMvaa 100,168,136\nTa 240,18,0\nMvaa 100,172,140\nTa 242,18,2048\nMvaa 68,172,132\nD0 3,20,45,1024\nMvaa 68,168,128\nTa 244,18,16384\n"
"Mvaa 72,176,128\nTa 246,18,18432\nMvaa 72,180,132\ns_and_b32 s2, s2, 15\nMvaa 104,180,140\nTa 248,19,0\nMvaa 104,176,136\nTa 250,19,2048\nMvaa 136,176,144\ns_mul_i32 s4, s73, 14\n"
"Mvaa 136,180,148\nTa 252,19,16384\nMvaa 168,180,156\nTa 254,19,18432\nMvaa 168,176,152\nD0 4,20,45,2048\nMvaa 172,184,152\ns_add_u32 s4, s4, s2\nMvaa 172,188,156\n"
"s_mov_b32 s36, s62\nMvaa 140,188,148\ns_and_b32 s37, s63, 0xffff\nMvaa 140,184,144\ns_mov_b32 s38, 0x188000\nMvaa 108,184,136\ns_mov_b32 s39, 0x20000\nMvaa 108,188,140\n"
"D0 5,20,45,3072\nMvaa 76,188,132\ns_mov_b32 s40, s64\nMvaa 76,184,128\ns_waitcnt vmcnt(18) lgkmcnt(0)\ns_barrier\nMvaa 80,224,128\nRa 192,10,16384\nMvaa 80,228,132\n"
"s_add_u32 m0, s47, 0\ns_add_u32 s44, s44, 0x40000\nMvaa 112,228,140\nRa 196,11,16384\nMvaa 112,224,136\ns_and_b32 s41, s65, 0xffff\nMvaa 144,224,144\nRa 200,10,18432\n"
"Mvaa 144,228,148\nD0 6,24,44,0\nMvaa 176,228,156\nRa 204,11,18432\nMvaa 176,224,152\ns_mov_b32 s42, 0x188000\nMvaa 180,232,152\nRa 208,10,24576\nMvaa 180,236,156\n"
"s_mov_b32 s43, 0x20000\nMvaa 148,236,148\nRa 212,11,24576\nMvaa 148,232,144\ns_mov_b32 s92, s66\nMvaa 116,232,136\nRa 216,10,26624\nMvaa 116,236,140\nD0 7,24,44,1024\n"
"Mvaa 84,236,132\nRa 220,11,26624\nMvaa 84,232,128\ns_and_b32 s93, s67, 0xffff\nMvaa 88,240,128\ns_mov_b32 s94, 0x188000\nMvaa 88,244,132\ns_mov_b32 s95, 0x20000\nMvaa 120,244,140\n"
"v_lshrrev_b32 v252, 1, v0\nMvaa 120,240,136\nv_mul_u32_u24 v253, 14336, v252\nMvaa 152,240,144\nv_and_b32 v254, 1, v0\nMvaa 152,244,148\nD0 8,24,44,2048\nMvaa 184,244,156\n"
"v_lshl_add_u32 v253, v254, 4, v253\nMvaa 184,240,152\nv_mov_b32 v254, 0x40000000\nv_cmp_gt_u32 vcc, 56, v0\nv_lshlrev_b32 v252, 3, v252\ns_mul_i32 s2, s11, 401408\n"
"v_cndmask_b32 v21, v254, v253, vcc\nv_cndmask_b32 v255, v254, v252, vcc\nMvaa 188,248,152\ns_lshl_b32 s3, s4, 5\nMvaa 188,252,156\ns_add_u32 s2, s2, s3\nMvaa 156,252,148\n"
"s_or_b32 s86, s2, s5\nMvaa 156,248,144\ns_add_u32 s87, s86, 7168\nMvaa 124,248,136\nv_lshrrev_b32 v251, 4, v0\nMvaa 124,252,140\nD0 9,24,44,3072\nMvaa 92,252,132\n"
"v_mul_u32_u24 v252, 3584, v251\nMvaa 92,248,128\nWL 0\nMaaa 16,224,192\nRa 128,10,32768\nMaaa 16,228,196\ns_add_u32 m0, s47, 16384\ns_add_u32 s44, s44, 0x40000\nMaaa 48,228,204\n"
"Ra 132,11,32768\nMaaa 48,224,200\nv_and_b32 v251, 15, v0\nMaaa 80,224,208\nRa 136,10,34816\nMaaa 80,228,212\nD0 6,24,44,0\nMaaa 112,228,220\nRa 140,11,34816\nMaaa 112,224,216\n"
"v_lshl_add_u32 v252, v251, 4, v252\nv_cmp_gt_u32 vcc, 14, v251\ns_mul_i32 s2, s4, 14336\ns_mul_i32 s3, s11, 224\nv_cndmask_b32 v20, v254, v252, vcc\nMaaa 116,232,216\n"
"Ra 144,10,40960\nMaaa 116,236,220\ns_add_u32 s2, s2, s3\nMaaa 84,236,212\nRa 148,11,40960\nMaaa 84,232,208\ns_or_b32 s88, s2, s5\nMaaa 52,232,200\nRa 152,10,43008\n"
"Maaa 52,236,204\nD0 7,24,44,1024\nMaaa 20,236,196\nRa 156,11,43008\nMaaa 20,232,192\nMaaa 24,240,192\nMaaa 24,244,196\nMaaa 56,244,204\nMaaa 56,240,200\nMaaa 88,240,208\n"
"Maaa 88,244,212\nD0 8,24,44,2048\nMaaa 120,244,220\nMaaa 120,240,216\nMaaa 124,248,216\nMaaa 124,252,220\nMaaa 92,252,212\nMaaa 92,248,208\nMaaa 60,248,200\nMaaa 60,252,204\n"
"D0 9,24,44,3072\nMaaa 28,252,196\nMaaa 28,248,192\ns_waitcnt vmcnt(17) lgkmcnt(0)\ns_barrier\nMaaa 0,160,192\nTa 224,16,32768\nMaaa 0,164,196\nTa 226,16,34816\n"
"buffer_load_dwordx4 v[192:195], v21, s[36:39], s86 offen offset:0\nMaaa 32,164,204\ns_add_u32 m0, s47, -49152\ns_add_u32 s48, s48, 128\nMaaa 32,160,200\nTa 228,16,49152\n"
"s_lshl_b32 s6, s11, 12\ns_add_u32 s6, s6, 0xc044000\nMaaa 64,160,208\nTa 230,16,51200\nMaaa 64,164,212\nD0 2,20,48,0\nMaaa 96,164,220\nTa 232,17,32768\nMaaa 96,160,216\n"
"Ta 234,17,34816\nMaaa 100,168,216\nbuffer_load_dwordx4 v[32:35], v1, s[16:19], s6 offen offset:0 sc0 sc1\nMaaa 100,172,220\nTa 236,17,49152\nMaaa 68,172,212\nTa 238,17,51200\n"
"Maaa 68,168,208\nMaaa 36,168,200\nTa 240,18,32768\nMaaa 36,172,204\nTa 242,18,34816\nMaaa 4,172,196\nD0 3,20,48,1024\nMaaa 4,168,192\nTa 244,18,49152\nMaaa 8,176,192\n"
"Ta 246,18,51200\nMaaa 8,180,196\nbuffer_load_dwordx4 v[36:39], v1, s[16:19], s6 offen offset:1024 sc0 sc1\nMaaa 40,180,204\nTa 248,19,32768\nMaaa 40,176,200\nTa 250,19,34816\n"
"Maaa 72,176,208\nMaaa 72,180,212\nTa 252,19,49152\nMaaa 104,180,220\nTa 254,19,51200\nMaaa 104,176,216\nD0 4,20,48,2048\nMaaa 108,184,216\n"
"buffer_load_dwordx4 v[40:43], v1, s[16:19], s6 offen offset:2048 sc0 sc1\nMaaa 108,188,220\nMaaa 76,188,212\nMaaa 76,184,208\nMaaa 44,184,200\n"
"buffer_load_dwordx4 v[44:47], v1, s[16:19], s6 offen offset:3072 sc0 sc1\nMaaa 44,188,204\nD0 5,20,48,3072\nMaaa 12,188,196\nMaaa 12,184,192\nWL 0\nMvaa 80,224,128\n"
"Ta 160,12,32768\nMvaa 80,228,132\nTa 162,12,34816\nMvaa 112,228,140\ns_add_u32 m0, s47, -32768\ns_add_u32 s45, s45, 128\nMvaa 112,224,136\nTa 164,12,49152\nMvaa 144,224,144\n"
"Ta 166,12,51200\nMvaa 144,228,148\nD0 2,20,45,0\nMvaa 176,228,156\nTa 168,13,32768\nMvaa 176,224,152\nTa 170,13,34816\nMvaa 180,232,152\nMvaa 180,236,156\nTa 172,13,49152\n"
"Mvaa 148,236,148\nTa 174,13,51200\nMvaa 148,232,144\nMvaa 116,232,136\nTa 176,14,32768\nMvaa 116,236,140\nTa 178,14,34816\nMvaa 84,236,132\nD0 3,20,45,1024\nMvaa 84,232,128\n"
"Ta 180,14,49152\nMvaa 88,240,128\nTa 182,14,51200\nMvaa 88,244,132\nMvaa 120,244,140\nTa 184,15,32768\nMvaa 120,240,136\nTa 186,15,34816\nMvaa 152,240,144\nMvaa 152,244,148\n"
"Ta 188,15,49152\nMvaa 184,244,156\nTa 190,15,51200\nMvaa 184,240,152\nD0 4,20,45,2048\nMvaa 188,248,152\nMvaa 188,252,156\nMvaa 156,252,148\nMvaa 156,248,144\nMvaa 124,248,136\n"
"Mvaa 124,252,140\nD0 5,20,45,3072\nMvaa 92,252,132\nMvaa 92,248,128\ns_waitcnt vmcnt(21) lgkmcnt(0)\ns_barrier\nMvaa 64,160,128\nRa 192,10,49152\nMvaa 64,164,132\n"
"s_add_u32 m0, s47, 32768\ns_add_u32 s44, s44, 0x40000\nbuffer_load_dwordx4 v[208:211], v21, s[40:43], s86 offen offset:0\nMvaa 96,164,140\nRa 196,11,49152\nMvaa 96,160,136\n"
"Mvaa 128,160,144\nRa 200,10,51200\nMvaa 128,164,148\nD0 6,24,44,0\nMvaa 160,164,156\nRa 204,11,51200\nMvaa 160,160,152\nMvaa 164,168,152\nRa 208,10,57344\nMvaa 164,172,156\n"
"Mvaa 132,172,148\nRa 212,11,57344\nMvaa 132,168,144\nMvaa 100,168,136\nRa 216,10,59392\nMvaa 100,172,140\nD0 7,24,44,1024\nMvaa 68,172,132\nRa 220,11,59392\nMvaa 68,168,128\n"
"Mvaa 72,176,128\nMvaa 72,180,132\nMvaa 104,180,140\nMvaa 104,176,136\nMvaa 136,176,144\nMvaa 136,180,148\nD0 8,24,44,2048\nMvaa 168,180,156\nMvaa 168,176,152\nMvaa 172,184,152\n"
"Mvaa 172,188,156\nMvaa 140,188,148\nMvaa 140,184,144\nMvaa 108,184,136\nMvaa 108,188,140\nD0 9,24,44,3072\nMvaa 76,188,132\nMvaa 76,184,128\nWL 0\nMaaa 0,160,192\nRa 128,10,0\n"
"Maaa 0,164,196\ns_add_u32 m0, s47, 49152\ns_add_u32 s44, s44, 0x40000\nMaaa 32,164,204\nRa 132,11,0\nMaaa 32,160,200\nMaaa 64,160,208\nRa 136,10,2048\nMaaa 64,164,212\n"
"D0 6,24,44,0\nMaaa 96,164,220\nRa 140,11,2048\nMaaa 96,160,216\nMaaa 100,168,216\nRa 144,10,8192\nMaaa 100,172,220\nMaaa 68,172,212\nRa 148,11,8192\nMaaa 68,168,208\n"
"Maaa 36,168,200\nRa 152,10,10240\nMaaa 36,172,204\nD0 7,24,44,1024\nMaaa 4,172,196\nRa 156,11,10240\nMaaa 4,168,192\nMaaa 8,176,192\nMaaa 8,180,196\nMaaa 40,180,204\n"
"Maaa 40,176,200\nMaaa 72,176,208\nMaaa 72,180,212\nD0 8,24,44,2048\nMaaa 104,180,220\nMaaa 104,176,216\nMaaa 108,184,216\nMaaa 108,188,220\nMaaa 76,188,212\nMaaa 76,184,208\n"
"Maaa 44,184,200\nMaaa 44,188,204\nD0 9,24,44,3072\nMaaa 12,188,196\nMaaa 12,184,192\ns_waitcnt vmcnt(14) lgkmcnt(0)\ns_barrier\nMaaa 16,224,192\nTa 160,12,0\n"
"v_xor_b32 v48, s15, v33\nMaaa 16,228,196\nTa 162,12,2048\nbuffer_load_dwordx4 v[196:199], v21, s[36:39], s86 offen offset:3584\nMaaa 48,228,204\ns_add_u32 m0, s47, -16384\n"
"s_add_u32 s48, s48, 128\nMaaa 48,224,200\nTa 164,12,16384\nMaaa 80,224,208\nTa 166,12,18432\nMaaa 80,228,212\nD0 2,20,48,0\nMaaa 112,228,220\nTa 168,13,0\nMaaa 112,224,216\n"
"Ta 170,13,2048\nMaaa 116,232,216\nv_xor_b32 v49, s15, v35\nMaaa 116,236,220\nTa 172,13,16384\nMaaa 84,236,212\nTa 174,13,18432\nMaaa 84,232,208\nv_xor_b32 v50, s15, v37\n"
"Maaa 52,232,200\nTa 176,14,0\nMaaa 52,236,204\nTa 178,14,2048\nMaaa 20,236,196\nD0 3,20,48,1024\nMaaa 20,232,192\nTa 180,14,16384\nMaaa 24,240,192\nTa 182,14,18432\n"
"v_xor_b32 v51, s15, v39\nMaaa 24,244,196\nv_xor_b32 v52, s15, v41\nMaaa 56,244,204\nTa 184,15,0\nMaaa 56,240,200\nTa 186,15,2048\nMaaa 88,240,208\nv_xor_b32 v53, s15, v43\n"
"Maaa 88,244,212\nTa 188,15,16384\nMaaa 120,244,220\nTa 190,15,18432\nMaaa 120,240,216\nD0 4,20,48,2048\nMaaa 124,248,216\nv_xor_b32 v54, s15, v45\nMaaa 124,252,220\n"
"v_xor_b32 v55, s15, v47\nMaaa 92,252,212\nv_or3_b32 v48, v48, v49, v50\nMaaa 92,248,208\nv_or3_b32 v51, v51, v52, v53\nMaaa 60,248,200\nv_or3_b32 v48, v48, v54, v55\n"
"Maaa 60,252,204\nD0 5,20,48,3072\nMaaa 28,252,196\nv_or_b32 v48, v48, v51\nMaaa 28,248,192\nWL 0\nMvaa 64,160,128\nTa 224,16,0\nv_cmp_ne_u32 vcc, 0, v48\nMvaa 64,164,132\n"
"Ta 226,16,2048\nMvaa 96,164,140\ns_add_u32 m0, s47, -65536\ns_add_u32 s45, s45, 128\nMvaa 96,160,136\nTa 228,16,16384\nMvaa 128,160,144\nTa 230,16,18432\nMvaa 128,164,148\n"
"D0 2,20,45,0\nMvaa 160,164,156\nTa 232,17,0\nMvaa 160,160,152\nTa 234,17,2048\nMvaa 164,168,152\ns_cbranch_vccnz .S14\n.S15:\nMvaa 164,172,156\nTa 236,17,16384\nMvaa 132,172,148\n"
"Ta 238,17,18432\nMvaa 132,168,144\nv_max_u32 v48, v32, v34\nMvaa 100,168,136\nTa 240,18,0\nMvaa 100,172,140\nTa 242,18,2048\nMvaa 68,172,132\nD0 3,20,45,1024\n.W541:\n"
"s_cbranch_execz .W545\nMvaa 68,168,128\nTa 244,18,16384\nMvaa 72,176,128\nTa 246,18,18432\nv_min_u32 v49, v32, v34\nMvaa 72,180,132\nv_med3_u32 v49, v48, v49, v36\n"
"Mvaa 104,180,140\nTa 248,19,0\nMvaa 104,176,136\nTa 250,19,2048\nMvaa 136,176,144\nv_max_u32 v48, v48, v36\nMvaa 136,180,148\nTa 252,19,16384\nMvaa 168,180,156\nTa 254,19,18432\n"
"Mvaa 168,176,152\nD0 4,20,45,2048\nMvaa 172,184,152\nv_med3_u32 v49, v48, v49, v38\nMvaa 172,188,156\nv_max_u32 v48, v48, v38\nMvaa 140,188,148\nv_med3_u32 v49, v48, v49, v40\n"
"Mvaa 140,184,144\nv_max_u32 v48, v48, v40\nMvaa 108,184,136\nv_med3_u32 v49, v48, v49, v42\nMvaa 108,188,140\nD0 5,20,45,3072\nMvaa 76,188,132\nv_max_u32 v48, v48, v42\n"
"Mvaa 76,184,128\ns_waitcnt vmcnt(18) lgkmcnt(0)\ns_barrier\nMvaa 80,224,128\nRa 192,10,16384\nv_med3_u32 v49, v48, v49, v44\nMvaa 80,228,132\ns_add_u32 m0, s47, 0\n"
"s_add_u32 s44, s44, 0x40000\nbuffer_load_dwordx4 v[212:215], v21, s[40:43], s86 offen offset:3584\nMvaa 112,228,140\nRa 196,11,16384\nMvaa 112,224,136\nv_max_u32 v48, v48, v44\n"
"Mvaa 144,224,144\nRa 200,10,18432\nMvaa 144,228,148\nD0 6,24,44,0\nMvaa 176,228,156\nRa 204,11,18432\nMvaa 176,224,152\nv_med3_u32 v49, v48, v49, v46\nMvaa 180,232,152\n"
"Ra 208,10,24576\nMvaa 180,236,156\nv_max_u32 v48, v48, v46\nMvaa 148,236,148\nRa 212,11,24576\nMvaa 148,232,144\nv_mov_b32 v50, v48\nMvaa 116,232,136\nRa 216,10,26624\n"
"Mvaa 116,236,140\nD0 7,24,44,1024\nMvaa 84,236,132\nRa 220,11,26624\nMvaa 84,232,128\nv_max_u32_dpp v50, v50, v50 row_ror:8 row_mask:0xf bank_mask:0xf\nMvaa 88,240,128\n"
"Mvaa 88,244,132\nv_max_u32_dpp v50, v50, v50 row_ror:4 row_mask:0xf bank_mask:0xf\nMvaa 120,244,140\nMvaa 120,240,136\n"
"v_max_u32_dpp v50, v50, v50 row_ror:2 row_mask:0xf bank_mask:0xf\nMvaa 152,240,144\nMvaa 152,244,148\nD0 8,24,44,2048\nMvaa 184,244,156\n"
"v_max_u32_dpp v50, v50, v50 row_ror:1 row_mask:0xf bank_mask:0xf\nMvaa 184,240,152\nv_mov_b32 v52, v50\nMvaa 188,248,152\nMvaa 188,252,156\nv_permlane16_swap_b32 v50, v52\n"
"Mvaa 156,252,148\nv_max_u32 v50, v50, v52\nMvaa 156,248,144\nv_mov_b32 v52, v50\nMvaa 124,248,136\nMvaa 124,252,140\nD0 9,24,44,3072\nMvaa 92,252,132\n"
"v_permlane32_swap_b32 v50, v52\nMvaa 92,248,128\nWL 0\nMaaa 16,224,192\nRa 128,10,32768\nv_max_u32 v50, v50, v52\nMaaa 16,228,196\ns_add_u32 m0, s47, 16384\n"
"s_add_u32 s44, s44, 0x40000\nMaaa 48,228,204\nRa 132,11,32768\nv_cmp_eq_u32 vcc, v48, v50\ns_lshl_b32 s2, s11, 3\ns_add_u32 s2, s2, 131072\nv_cndmask_b32 v51, v48, v49, vcc\n"
"Maaa 48,224,200\nMaaa 80,224,208\nRa 136,10,34816\nMaaa 80,228,212\nD0 6,24,44,0\nMaaa 112,228,220\nRa 140,11,34816\nMaaa 112,224,216\n"
"v_max_u32_dpp v51, v51, v51 row_ror:8 row_mask:0xf bank_mask:0xf\nMaaa 116,232,216\nRa 144,10,40960\nMaaa 116,236,220\n"
"v_max_u32_dpp v51, v51, v51 row_ror:4 row_mask:0xf bank_mask:0xf\nMaaa 84,236,212\nRa 148,11,40960\nMaaa 84,232,208\n"
"v_max_u32_dpp v51, v51, v51 row_ror:2 row_mask:0xf bank_mask:0xf\nMaaa 52,232,200\nRa 152,10,43008\nMaaa 52,236,204\nD0 7,24,44,1024\nMaaa 20,236,196\nRa 156,11,43008\n"
"Maaa 20,232,192\nv_max_u32_dpp v51, v51, v51 row_ror:1 row_mask:0xf bank_mask:0xf\nMaaa 24,240,192\nv_mov_b32 v52, v51\nMaaa 24,244,196\nMaaa 56,244,204\n"
"v_permlane16_swap_b32 v51, v52\nMaaa 56,240,200\nv_max_u32 v51, v51, v52\nMaaa 88,240,208\nv_mov_b32 v52, v51\nMaaa 88,244,212\nD0 8,24,44,2048\nMaaa 120,244,220\n"
"v_permlane32_swap_b32 v51, v52\nMaaa 120,240,216\nv_max_u32 v51, v51, v52\nMaaa 124,248,216\nv_mov_b32 v52, s2\nMaaa 124,252,220\nds_write_b64 v52, v[50:51]\nMaaa 92,252,212\n"
"v_mov_b32 v52, 131072\nMaaa 92,248,208\nMaaa 60,248,200\nMaaa 60,252,204\nD0 9,24,44,3072\nMaaa 28,252,196\nMaaa 28,248,192\ns_waitcnt vmcnt(18) lgkmcnt(0)\ns_barrier\n"
"Maaa 0,160,192\nTa 224,16,32768\nMaaa 0,164,196\nTa 226,16,34816\nbuffer_load_dwordx4 v[200:203], v21, s[36:39], s87 offen offset:0\nMaaa 32,164,204\ns_add_u32 m0, s47, -49152\n"
"s_add_u32 s48, s48, 128\nMaaa 32,160,200\nTa 228,16,49152\nMaaa 64,160,208\nTa 230,16,51200\nMaaa 64,164,212\nD0 2,20,48,0\nMaaa 96,164,220\nTa 232,17,32768\nMaaa 96,160,216\n"
"Ta 234,17,34816\nMaaa 100,168,216\nRv 32,52,0\nMaaa 100,172,220\nTa 236,17,49152\nMaaa 68,172,212\nTa 238,17,51200\nMaaa 68,168,208\nRv 36,52,16\nMaaa 36,168,200\nTa 240,18,32768\n"
"Maaa 36,172,204\nTa 242,18,34816\nMaaa 4,172,196\nD0 3,20,48,1024\nMaaa 4,168,192\nTa 244,18,49152\nMaaa 8,176,192\nTa 246,18,51200\nMaaa 8,180,196\nMaaa 40,180,204\n"
"Ta 248,19,32768\nMaaa 40,176,200\nTa 250,19,34816\nMaaa 72,176,208\nMaaa 72,180,212\nTa 252,19,49152\nMaaa 104,180,220\nTa 254,19,51200\nMaaa 104,176,216\nD0 4,20,48,2048\n"
"Maaa 108,184,216\nWL 11\nMaaa 108,188,220\nv_max_u32 v48, v32, v33\nMaaa 76,188,212\nv_min_u32 v49, v32, v33\nMaaa 76,184,208\nv_med3_u32 v49, v48, v49, v34\nMaaa 44,184,200\n"
"v_max_u32 v48, v48, v34\nMaaa 44,188,204\nD0 5,20,48,3072\nMaaa 12,188,196\nv_med3_u32 v49, v48, v49, v35\nMaaa 12,184,192\nWL 0\nMvaa 80,224,128\nTa 160,12,32768\n"
"Mvaa 80,228,132\nTa 162,12,34816\nMvaa 112,228,140\ns_add_u32 m0, s47, -32768\ns_add_u32 s45, s45, 128\nMvaa 112,224,136\nTa 164,12,49152\nMvaa 144,224,144\nTa 166,12,51200\n"
"Mvaa 144,228,148\nD0 2,20,45,0\nMvaa 176,228,156\nTa 168,13,32768\nMvaa 176,224,152\nTa 170,13,34816\nMvaa 180,232,152\nv_max_u32 v48, v48, v35\nMvaa 180,236,156\nTa 172,13,49152\n"
"Mvaa 148,236,148\nTa 174,13,51200\nMvaa 148,232,144\nv_med3_u32 v49, v48, v49, v36\nMvaa 116,232,136\nTa 176,14,32768\nMvaa 116,236,140\nTa 178,14,34816\nMvaa 84,236,132\n"
"D0 3,20,45,1024\nMvaa 84,232,128\nTa 180,14,49152\nMvaa 88,240,128\nTa 182,14,51200\nMvaa 88,244,132\nv_max_u32 v48, v48, v36\nMvaa 120,244,140\nTa 184,15,32768\nMvaa 120,240,136\n"
"Ta 186,15,34816\nMvaa 152,240,144\nv_med3_u32 v49, v48, v49, v37\nMvaa 152,244,148\nTa 188,15,49152\nMvaa 184,244,156\nTa 190,15,51200\nMvaa 184,240,152\nD0 4,20,45,2048\n"
"Mvaa 188,248,152\nv_max_u32 v48, v48, v37\nMvaa 188,252,156\nv_med3_u32 v49, v48, v49, v38\nMvaa 156,252,148\nv_max_u32 v48, v48, v38\nMvaa 156,248,144\n"
"v_med3_u32 v49, v48, v49, v39\nMvaa 124,248,136\nv_max_u32 v48, v48, v39\nMvaa 124,252,140\nD0 5,20,45,3072\nMvaa 92,252,132\nv_readfirstlane_b32 s2, v48\nMvaa 92,248,128\n"
"s_waitcnt vmcnt(18) lgkmcnt(0)\ns_barrier\nMvaa 64,160,128\nRa 192,10,49152\nMvaa 64,164,132\ns_add_u32 m0, s47, 32768\ns_add_u32 s44, s44, 0x40000\n"
"buffer_load_dwordx4 v[216:219], v21, s[40:43], s87 offen offset:0\nMvaa 96,164,140\nRa 196,11,49152\nMvaa 96,160,136\nv_readfirstlane_b32 s3, v49\nMvaa 128,160,144\n"
"Ra 200,10,51200\nMvaa 128,164,148\nD0 6,24,44,0\nMvaa 160,164,156\nRa 204,11,51200\nMvaa 160,160,152\ns_andn2_b32 s2, 0x7ff, s2\nMvaa 164,168,152\nRa 208,10,57344\n"
"Mvaa 164,172,156\ns_andn2_b32 s3, 0x7ff, s3\nMvaa 132,172,148\nRa 212,11,57344\nMvaa 132,168,144\ns_lshl_b32 s71, s3, 16\nMvaa 100,168,136\nRa 216,10,59392\nMvaa 100,172,140\n"
"D0 7,24,44,1024\nMvaa 68,172,132\nRa 220,11,59392\nMvaa 68,168,128\ns_or_b32 s71, s71, s2\nMvaa 72,176,128\ns_mul_i32 s2, s2, 896\nMvaa 72,180,132\ns_mul_i32 s3, s3, 896\n"
"Mvaa 104,180,140\ns_mul_i32 s6, s11, 224\nMvaa 104,176,136\ns_add_u32 s2, s2, s6\nMvaa 136,176,144\ns_add_u32 s3, s3, s6\nMvaa 136,180,148\nD0 8,24,44,2048\nMvaa 168,180,156\n"
"buffer_load_dwordx2 v[240:241], v255, s[28:31], s2 offen\nMvaa 168,176,152\nMvaa 172,184,152\nMvaa 172,188,156\nMvaa 140,188,148\n"
"buffer_load_dwordx2 v[242:243], v255, s[28:31], s3 offen\ns_lshl2_add_u32 s2, s10, s11\ns_cmp_eq_u32 s2, 512\ns_cselect_b32 s2, 0, 0x40000000\ns_add_u32 s2, s2, 0xc048000\n"
"Mvaa 140,184,144\nMvaa 108,184,136\nMvaa 108,188,140\nD0 9,24,44,3072\nMvaa 76,188,132\nbuffer_store_dword v25, off, s[16:19], s2 offset:0 sc0 sc1\nMvaa 76,184,128\nWL 0\n"
"Maaa 0,160,192\nRa 128,10,0\nMaaa 0,164,196\ns_add_u32 m0, s47, 49152\ns_add_u32 s44, s44, 0x40000\nMaaa 32,164,204\nRa 132,11,0\nMaaa 32,160,200\nMaaa 64,160,208\nRa 136,10,2048\n"
"Maaa 64,164,212\nD0 6,24,44,0\nMaaa 96,164,220\nRa 140,11,2048\nMaaa 96,160,216\nMaaa 100,168,216\nRa 144,10,8192\nMaaa 100,172,220\nMaaa 68,172,212\nRa 148,11,8192\n"
"Maaa 68,168,208\nMaaa 36,168,200\nRa 152,10,10240\nMaaa 36,172,204\nD0 7,24,44,1024\nMaaa 4,172,196\nRa 156,11,10240\nMaaa 4,168,192\nMaaa 8,176,192\nMaaa 8,180,196\n"
"Maaa 40,180,204\nMaaa 40,176,200\nMaaa 72,176,208\nMaaa 72,180,212\nD0 8,24,44,2048\nMaaa 104,180,220\nMaaa 104,176,216\nMaaa 108,184,216\nMaaa 108,188,220\nMaaa 76,188,212\n"
"Maaa 76,184,208\nMaaa 44,184,200\nMaaa 44,188,204\nD0 9,24,44,3072\nMaaa 12,188,196\nMaaa 12,184,192\ns_waitcnt vmcnt(21) lgkmcnt(0)\ns_barrier\nMaaa 16,224,192\nTa 160,12,0\n"
"Maaa 16,228,196\nTa 162,12,2048\nbuffer_load_dwordx4 v[204:207], v21, s[36:39], s87 offen offset:3584\nMaaa 48,228,204\ns_add_u32 m0, s47, -16384\ns_add_u32 s48, s48, 128\n"
"s_lshl_b32 s5, s73, 9\ns_add_u32 s5, s5, 0xc049400\nv_lshrrev_b32 v27, 1, v1\nbuffer_load_dwordx2 v[22:23], v27, s[16:19], s5 offen sc0 sc1\nMaaa 48,224,200\nTa 164,12,16384\n"
"Maaa 80,224,208\nTa 166,12,18432\nMaaa 80,228,212\nD0 2,20,48,0\nMaaa 112,228,220\nTa 168,13,0\nMaaa 112,224,216\nTa 170,13,2048\nMaaa 116,232,216\nMaaa 116,236,220\n"
"Ta 172,13,16384\nMaaa 84,236,212\nTa 174,13,18432\nMaaa 84,232,208\nMaaa 52,232,200\nTa 176,14,0\nMaaa 52,236,204\nTa 178,14,2048\nMaaa 20,236,196\nD0 3,20,48,1024\n"
"Maaa 20,232,192\nTa 180,14,16384\nMaaa 24,240,192\nTa 182,14,18432\nMaaa 24,244,196\nMaaa 56,244,204\nTa 184,15,0\nMaaa 56,240,200\nTa 186,15,2048\nMaaa 88,240,208\n"
"Maaa 88,244,212\nTa 188,15,16384\nMaaa 120,244,220\nTa 190,15,18432\nMaaa 120,240,216\nD0 4,20,48,2048\nMaaa 124,248,216\nMaaa 124,252,220\nMaaa 92,252,212\nMaaa 92,248,208\n"
"Maaa 60,248,200\nMaaa 60,252,204\nD0 5,20,48,3072\nMaaa 28,252,196\nMaaa 28,248,192\nWL 0\nMvaa 64,160,128\nTa 224,16,0\nMvaa 64,164,132\nTa 226,16,2048\nMvaa 96,164,140\n"
"s_add_u32 m0, s47, -65536\ns_add_u32 s45, s45, 128\nMvaa 96,160,136\nTa 228,16,16384\nMvaa 128,160,144\nTa 230,16,18432\nMvaa 128,164,148\nD0 2,20,45,0\nMvaa 160,164,156\n"
"Ta 232,17,0\nMvaa 160,160,152\nTa 234,17,2048\nMvaa 164,168,152\nMvaa 164,172,156\nTa 236,17,16384\nMvaa 132,172,148\nTa 238,17,18432\nMvaa 132,168,144\nMvaa 100,168,136\n"
"Ta 240,18,0\nMvaa 100,172,140\nTa 242,18,2048\nMvaa 68,172,132\nD0 3,20,45,1024\nMvaa 68,168,128\nTa 244,18,16384\nMvaa 72,176,128\nTa 246,18,18432\nMvaa 72,180,132\n"
"Mvaa 104,180,140\nTa 248,19,0\nMvaa 104,176,136\nTa 250,19,2048\nMvaa 136,176,144\nMvaa 136,180,148\nTa 252,19,16384\nMvaa 168,180,156\nTa 254,19,18432\nMvaa 168,176,152\n"
"D0 4,20,45,2048\nMvaa 172,184,152\nMvaa 172,188,156\nMvaa 140,188,148\nMvaa 140,184,144\nMvaa 108,184,136\nMvaa 108,188,140\nD0 5,20,45,3072\nMvaa 76,188,132\nMvaa 76,184,128\n"
"s_waitcnt vmcnt(22) lgkmcnt(0)\ns_barrier\nMvaa 80,224,128\nRa 192,10,16384\nWV 16\nMvaa 80,228,132\ns_add_u32 m0, s47, 0\ns_add_u32 s44, s44, 0x40000\n"
"buffer_load_dwordx4 v[220:223], v21, s[40:43], s87 offen offset:3584\nMvaa 112,228,140\nRa 196,11,16384\nv_lshlrev_b32 v244, 16, v240\nMvaa 112,224,136\n"
"v_and_b32 v245, 0xffff0000, v240\nv_lshlrev_b32 v246, 16, v241\nMvaa 144,224,144\nRa 200,10,18432\nv_and_b32 v247, 0xffff0000, v241\nMvaa 144,228,148\nD0 6,24,44,0\n"
"v_lshlrev_b32 v248, 16, v242\nMvaa 176,228,156\n.W542:\ns_cbranch_execz .W546\nRa 204,11,18432\nv_and_b32 v249, 0xffff0000, v242\nMvaa 176,224,152\nv_lshlrev_b32 v250, 16, v243\n"
"v_and_b32 v251, 0xffff0000, v243\nMvaa 180,232,152\nRa 208,10,24576\nv_lshlrev_b32 v252, 16, v192\nMvaa 180,236,156\nv_and_b32 v253, 0xffff0000, v192\nMU 32,244,252\n"
"Mvaa 148,236,148\nRa 212,11,24576\nMU 33,244,253\nMvaa 148,232,144\nMU 40,248,252\nMU 41,248,253\nMvaa 116,232,136\nRa 216,10,26624\nv_lshlrev_b32 v252, 16, v193\n"
"Mvaa 116,236,140\nD0 7,24,44,1024\nv_and_b32 v253, 0xffff0000, v193\nMvaa 84,236,132\nRa 220,11,26624\nMU 34,244,252\nMvaa 84,232,128\nMU 35,244,253\nMU 42,248,252\n"
"Mvaa 88,240,128\nMU 43,248,253\nv_lshlrev_b32 v252, 16, v194\nMvaa 88,244,132\nv_and_b32 v253, 0xffff0000, v194\nMvaa 120,244,140\nMU 36,244,252\nMU 37,244,253\nMvaa 120,240,136\n"
"MU 44,248,252\nMU 45,248,253\nMvaa 152,240,144\nv_lshlrev_b32 v252, 16, v195\nv_and_b32 v253, 0xffff0000, v195\nMvaa 152,244,148\nD0 8,24,44,2048\nMU 38,244,252\nMvaa 184,244,156\n"
"MU 39,244,253\nMU 46,248,252\nMvaa 184,240,152\nMU 47,248,253\nv_lshlrev_b32 v252, 16, v208\nMvaa 188,248,152\nv_and_b32 v253, 0xffff0000, v208\nMU 48,244,252\nMvaa 188,252,156\n"
"MU 49,244,253\nMU 56,248,252\nMvaa 156,252,148\nMU 57,248,253\nv_lshlrev_b32 v252, 16, v209\nMvaa 156,248,144\nv_and_b32 v253, 0xffff0000, v209\nMU 50,244,252\nMvaa 124,248,136\n"
"MU 51,244,253\nMU 58,248,252\nMvaa 124,252,140\nD0 9,24,44,3072\nMU 59,248,253\nMvaa 92,252,132\nv_lshlrev_b32 v252, 16, v210\nv_and_b32 v253, 0xffff0000, v210\nMvaa 92,248,128\n"
"WL 0\nMaaa 16,224,192\nRa 128,10,32768\nMU 52,244,252\nMaaa 16,228,196\ns_add_u32 m0, s47, 16384\ns_add_u32 s44, s44, 0x40000\nMaaa 48,228,204\nRa 132,11,32768\nMU 53,244,253\n"
"Maaa 48,224,200\nMU 60,248,252\nMU 61,248,253\nMaaa 80,224,208\nRa 136,10,34816\nv_lshlrev_b32 v252, 16, v211\nMaaa 80,228,212\nD0 6,24,44,0\nv_and_b32 v253, 0xffff0000, v211\n"
"Maaa 112,228,220\nRa 140,11,34816\nMU 54,244,252\nMaaa 112,224,216\nMU 55,244,253\nMU 62,248,252\nMaaa 116,232,216\nRa 144,10,40960\nMU 63,248,253\nMaaa 116,236,220\n"
"v_lshlrev_b32 v252, 16, v196\nv_and_b32 v253, 0xffff0000, v196\nMaaa 84,236,212\nRa 148,11,40960\nFM 32,245,252\nMaaa 84,232,208\nFM 33,245,253\nFM 40,249,252\nMaaa 52,232,200\n"
"Ra 152,10,43008\nFM 41,249,253\nMaaa 52,236,204\nD0 7,24,44,1024\nv_lshlrev_b32 v252, 16, v197\nMaaa 20,236,196\nRa 156,11,43008\nv_and_b32 v253, 0xffff0000, v197\n"
"Maaa 20,232,192\nFM 34,245,252\nFM 35,245,253\nMaaa 24,240,192\nFM 42,249,252\nFM 43,249,253\nMaaa 24,244,196\nv_lshlrev_b32 v252, 16, v198\nMaaa 56,244,204\n"
"v_and_b32 v253, 0xffff0000, v198\nFM 36,245,252\nMaaa 56,240,200\nFM 37,245,253\nFM 44,249,252\nMaaa 88,240,208\nFM 45,249,253\nv_lshlrev_b32 v252, 16, v199\nMaaa 88,244,212\n"
"D0 8,24,44,2048\nv_and_b32 v253, 0xffff0000, v199\nMaaa 120,244,220\nFM 38,245,252\nFM 39,245,253\nMaaa 120,240,216\nFM 46,249,252\nFM 47,249,253\nMaaa 124,248,216\n"
"v_lshlrev_b32 v252, 16, v212\nv_and_b32 v253, 0xffff0000, v212\nMaaa 124,252,220\nFM 48,245,252\nFM 49,245,253\nMaaa 92,252,212\nFM 56,249,252\nFM 57,249,253\nMaaa 92,248,208\n"
"v_lshlrev_b32 v252, 16, v213\nv_and_b32 v253, 0xffff0000, v213\nMaaa 60,248,200\nFM 50,245,252\nFM 51,245,253\nMaaa 60,252,204\nD0 9,24,44,3072\nFM 58,249,252\nMaaa 28,252,196\n"
"FM 59,249,253\nv_lshlrev_b32 v252, 16, v214\nMaaa 28,248,192\ns_waitcnt vmcnt(19) lgkmcnt(0)\ns_barrier\nMaaa 0,160,192\nTa 224,16,32768\nv_and_b32 v253, 0xffff0000, v214\n"
"Maaa 0,164,196\nTa 226,16,34816\nbuffer_load_dwordx4 v[224:227], v20, s[92:95], s88 offen offset:0\nMaaa 32,164,204\ns_add_u32 m0, s47, -49152\ns_add_u32 s48, s48, 128\n"
"Maaa 32,160,200\nTa 228,16,49152\nFM 52,245,252\nMaaa 64,160,208\nTa 230,16,51200\nFM 53,245,253\nMaaa 64,164,212\nD0 2,20,48,0\nFM 60,249,252\nMaaa 96,164,220\nTa 232,17,32768\n"
"FM 61,249,253\nMaaa 96,160,216\nTa 234,17,34816\nv_lshlrev_b32 v252, 16, v215\nMaaa 100,168,216\nv_and_b32 v253, 0xffff0000, v215\nFM 54,245,252\nMaaa 100,172,220\n"
"Ta 236,17,49152\nFM 55,245,253\nMaaa 68,172,212\nTa 238,17,51200\nFM 62,249,252\nMaaa 68,168,208\nFM 63,249,253\nv_lshlrev_b32 v252, 16, v200\nMaaa 36,168,200\nTa 240,18,32768\n"
"v_and_b32 v253, 0xffff0000, v200\nMaaa 36,172,204\nTa 242,18,34816\nFM 32,246,252\nMaaa 4,172,196\nD0 3,20,48,1024\nFM 33,246,253\nMaaa 4,168,192\nTa 244,18,49152\nFM 40,250,252\n"
"Maaa 8,176,192\nTa 246,18,51200\nFM 41,250,253\nMaaa 8,180,196\nv_lshlrev_b32 v252, 16, v201\nMaaa 40,180,204\nTa 248,19,32768\nv_and_b32 v253, 0xffff0000, v201\nMaaa 40,176,200\n"
"Ta 250,19,34816\nFM 34,246,252\nMaaa 72,176,208\nFM 35,246,253\nFM 42,250,252\nMaaa 72,180,212\nTa 252,19,49152\nFM 43,250,253\nMaaa 104,180,220\nTa 254,19,51200\n"
"v_lshlrev_b32 v252, 16, v202\nMaaa 104,176,216\nD0 4,20,48,2048\nv_and_b32 v253, 0xffff0000, v202\nMaaa 108,184,216\nFM 36,246,252\nFM 37,246,253\nMaaa 108,188,220\nFM 44,250,252\n"
"FM 45,250,253\nMaaa 76,188,212\nv_lshlrev_b32 v252, 16, v203\nv_and_b32 v253, 0xffff0000, v203\nMaaa 76,184,208\nFM 38,246,252\nFM 39,246,253\nMaaa 44,184,200\nFM 46,250,252\n"
"FM 47,250,253\nMaaa 44,188,204\nD0 5,20,48,3072\nv_lshlrev_b32 v252, 16, v216\nMaaa 12,188,196\nv_and_b32 v253, 0xffff0000, v216\nFM 48,246,252\nMaaa 12,184,192\nWL 0\n"
"Mvaa 80,224,128\nTa 160,12,32768\nFM 49,246,253\nMvaa 80,228,132\nTa 162,12,34816\nbuffer_load_dwordx4 v[228:231], v20, s[92:95], s88 offen offset:896\nMvaa 112,228,140\n"
"s_add_u32 m0, s47, -32768\ns_add_u32 s45, s45, 128\nMvaa 112,224,136\nTa 164,12,49152\nFM 56,250,252\nMvaa 144,224,144\nTa 166,12,51200\nFM 57,250,253\nMvaa 144,228,148\n"
"D0 2,20,45,0\nv_lshlrev_b32 v252, 16, v217\nMvaa 176,228,156\nTa 168,13,32768\nv_and_b32 v253, 0xffff0000, v217\nMvaa 176,224,152\nTa 170,13,34816\nFM 50,246,252\n"
"Mvaa 180,232,152\nFM 51,246,253\nFM 58,250,252\nMvaa 180,236,156\nTa 172,13,49152\nFM 59,250,253\nMvaa 148,236,148\nTa 174,13,51200\nv_lshlrev_b32 v252, 16, v218\n"
"Mvaa 148,232,144\nv_and_b32 v253, 0xffff0000, v218\nFM 52,246,252\nMvaa 116,232,136\nTa 176,14,32768\nFM 53,246,253\nMvaa 116,236,140\nTa 178,14,34816\nFM 60,250,252\n"
"Mvaa 84,236,132\nD0 3,20,45,1024\nFM 61,250,253\nMvaa 84,232,128\nTa 180,14,49152\nv_lshlrev_b32 v252, 16, v219\nMvaa 88,240,128\nTa 182,14,51200\n"
"v_and_b32 v253, 0xffff0000, v219\nMvaa 88,244,132\nFM 54,246,252\nWV 25\nv_cmp_ne_u32 vcc, s15, v23\ns_nop 0\ns_cbranch_vccnz .S17\n.S18:\nMvaa 120,244,140\nTa 184,15,32768\n"
"v_add_u32_dpp v22, v22, v22 row_ror:8 row_mask:0xf bank_mask:0xf\nMvaa 120,240,136\nTa 186,15,34816\nMvaa 152,240,144\n"
"v_add_u32_dpp v22, v22, v22 row_ror:4 row_mask:0xf bank_mask:0xf\nMvaa 152,244,148\nTa 188,15,49152\nMvaa 184,244,156\nTa 190,15,51200\n"
"v_add_u32_dpp v22, v22, v22 row_ror:2 row_mask:0xf bank_mask:0xf\nMvaa 184,240,152\nD0 4,20,45,2048\nMvaa 188,248,152\n"
"v_add_u32_dpp v22, v22, v22 row_ror:1 row_mask:0xf bank_mask:0xf\nMvaa 188,252,156\nMvaa 156,252,148\nv_mov_b32 v23, v22\nMvaa 156,248,144\nMvaa 124,248,136\n"
"v_permlane16_swap_b32 v22, v23\nv_add_u32 v22, v22, v23\nMvaa 124,252,140\nD0 5,20,45,3072\nv_mov_b32 v23, v22\nMvaa 92,252,132\nMvaa 92,248,128\ns_waitcnt vmcnt(19) lgkmcnt(0)\n"
"s_barrier\nMvaa 64,160,128\nRa 192,10,49152\nv_permlane32_swap_b32 v22, v23\nMvaa 64,164,132\ns_add_u32 m0, s47, 32768\ns_add_u32 s44, s44, 0x40000\n"
"buffer_load_dwordx4 v[232:235], v20, s[92:95], s88 offen offset:1792\nMvaa 96,164,140\nRa 196,11,49152\nv_add_u32 v22, v22, v23\nMvaa 96,160,136\nMvaa 128,160,144\n"
"Ra 200,10,51200\nv_readfirstlane_b32 s4, v22\nMvaa 128,164,148\nD0 6,24,44,0\nMvaa 160,164,156\nRa 204,11,51200\nMvaa 160,160,152\ns_cmp_gt_i32 s4, 0\ns_cselect_b32 s4, 1, 0\n"
"Mvaa 164,168,152\nRa 208,10,57344\ns_xor_b32 s4, s4, s12\nMvaa 164,172,156\ns_and_b32 s4, s4, 1\ns_lshl_b32 s4, s4, 13\nMvaa 132,172,148\nRa 212,11,57344\n"
"s_mul_i32 s72, s73, 25165824\nMvaa 132,168,144\ns_add_u32 s72, s72, s4\ns_and_b32 s4, s12, 1\nMvaa 100,168,136\nRa 216,10,59392\ns_lshl_b32 s4, s4, 21\nMvaa 100,172,140\n"
"D0 7,24,44,1024\ns_add_u32 s72, s72, s4\nMvaa 68,172,132\nRa 220,11,59392\ns_lshl3_add_u32 s2, s13, s11\nMvaa 68,168,128\ns_lshl_b32 s2, s2, 11\ns_lshl_b32 s3, s14, 22\n"
"Mvaa 72,176,128\ns_add_u32 s2, s2, s3\ns_add_u32 s2, s2, s72\nMvaa 72,180,132\ns_add_u32 s49, s2, 0xbe0000\nMvaa 104,180,140\nFM 55,246,253\nFM 62,250,252\nMvaa 104,176,136\n"
"FM 63,250,253\nv_lshlrev_b32 v252, 16, v204\nMvaa 136,176,144\nv_and_b32 v253, 0xffff0000, v204\nFM 32,247,252\nMvaa 136,180,148\nD0 8,24,44,2048\nFM 33,247,253\nMvaa 168,180,156\n"
"FM 40,251,252\nFM 41,251,253\nMvaa 168,176,152\nv_lshlrev_b32 v252, 16, v205\nv_and_b32 v253, 0xffff0000, v205\nMvaa 172,184,152\nFM 34,247,252\nFM 35,247,253\nMvaa 172,188,156\n"
"FM 42,251,252\nFM 43,251,253\nMvaa 140,188,148\nv_lshlrev_b32 v252, 16, v206\nv_and_b32 v253, 0xffff0000, v206\nMvaa 140,184,144\nFM 36,247,252\nFM 37,247,253\nMvaa 108,184,136\n"
"FM 44,251,252\nFM 45,251,253\nMvaa 108,188,140\nD0 9,24,44,3072\nv_lshlrev_b32 v252, 16, v207\nMvaa 76,188,132\nv_and_b32 v253, 0xffff0000, v207\nFM 38,247,252\nMvaa 76,184,128\n"
"s_waitcnt vmcnt(23) lgkmcnt(0)\nMaaa 0,160,192\nRa 128,10,0\nFM 39,247,253\nMaaa 0,164,196\ns_add_u32 m0, s47, 49152\ns_add_u32 s44, s44, 0x40000\n"
"buffer_load_dwordx4 v[236:239], v20, s[92:95], s88 offen offset:2688\nMaaa 32,164,204\nRa 132,11,0\nFM 46,251,252\nMaaa 32,160,200\nFM 47,251,253\nv_lshlrev_b32 v252, 16, v220\n"
"Maaa 64,160,208\nRa 136,10,2048\nv_and_b32 v253, 0xffff0000, v220\nMaaa 64,164,212\nD0 6,24,44,0\nFM 48,247,252\nMaaa 96,164,220\nRa 140,11,2048\nFM 49,247,253\nMaaa 96,160,216\n"
"FM 56,251,252\nFM 57,251,253\nMaaa 100,168,216\nRa 144,10,8192\nv_lshlrev_b32 v252, 16, v221\nMaaa 100,172,220\nv_and_b32 v253, 0xffff0000, v221\nFM 50,247,252\nMaaa 68,172,212\n"
"Ra 148,11,8192\nFM 51,247,253\nMaaa 68,168,208\nFM 58,251,252\nFM 59,251,253\nMaaa 36,168,200\nRa 152,10,10240\nv_lshlrev_b32 v252, 16, v222\nMaaa 36,172,204\nD0 7,24,44,1024\n"
"v_and_b32 v253, 0xffff0000, v222\nMaaa 4,172,196\nRa 156,11,10240\nFM 52,247,252\nMaaa 4,168,192\nFM 53,247,253\nFM 60,251,252\nMaaa 8,176,192\nFM 61,251,253\n"
"v_lshlrev_b32 v252, 16, v223\nMaaa 8,180,196\nv_and_b32 v253, 0xffff0000, v223\nMaaa 40,180,204\nFM 54,247,252\nFM 55,247,253\nMaaa 40,176,200\nFM 62,251,252\nFM 63,251,253\n"
"Maaa 72,176,208\nv_permlane32_swap_b32 v32, v48\nv_permlane32_swap_b32 v33, v49\nMaaa 72,180,212\nD0 8,24,44,2048\nv_permlane32_swap_b32 v34, v50\nMaaa 104,180,220\n"
"v_permlane32_swap_b32 v35, v51\nv_permlane32_swap_b32 v36, v52\nMaaa 104,176,216\nv_permlane32_swap_b32 v37, v53\nv_permlane32_swap_b32 v38, v54\nMaaa 108,184,216\n"
"v_permlane32_swap_b32 v39, v55\nv_permlane32_swap_b32 v40, v56\nMaaa 108,188,220\nv_permlane32_swap_b32 v41, v57\nv_permlane32_swap_b32 v42, v58\nMaaa 76,188,212\n"
"v_permlane32_swap_b32 v43, v59\nv_permlane32_swap_b32 v44, v60\nMaaa 76,184,208\nv_permlane32_swap_b32 v45, v61\nv_permlane32_swap_b32 v46, v62\nMaaa 44,184,200\n"
"v_permlane32_swap_b32 v47, v63\nAD 32,32,48\nMaaa 44,188,204\nD0 9,24,44,3072\nAD 33,33,49\nMaaa 12,188,196\nAD 34,34,50\nAD 35,35,51\nMaaa 12,184,192\n"
"s_waitcnt vmcnt(20) lgkmcnt(0)\ns_barrier\nMaaa 16,224,192\nTa 160,12,0\nAD 36,36,52\nMaaa 16,228,196\nTa 162,12,2048\nMaaa 48,228,204\ns_add_u32 m0, s47, -16384\n"
"s_add_u32 s48, s48, 128\nMaaa 48,224,200\nTa 164,12,16384\nAD 37,37,53\nMaaa 80,224,208\nTa 166,12,18432\nAD 38,38,54\nMaaa 80,228,212\nD0 2,20,48,0\nAD 39,39,55\n"
"Maaa 112,228,220\nTa 168,13,0\nAD 40,40,56\nMaaa 112,224,216\nTa 170,13,2048\nAD 41,41,57\nMaaa 116,232,216\nAD 42,42,58\nAD 43,43,59\nMaaa 116,236,220\nTa 172,13,16384\n"
"AD 44,44,60\nMaaa 84,236,212\nTa 174,13,18432\nAD 45,45,61\nMaaa 84,232,208\nAD 46,46,62\nAD 47,47,63\nMaaa 52,232,200\nTa 176,14,0\nv_permlane16_swap_b32 v32, v40\n"
"Maaa 52,236,204\nTa 178,14,2048\nv_permlane16_swap_b32 v33, v41\nMaaa 20,236,196\nD0 3,20,48,1024\nv_permlane16_swap_b32 v34, v42\nMaaa 20,232,192\nTa 180,14,16384\n"
"v_permlane16_swap_b32 v35, v43\nMaaa 24,240,192\nTa 182,14,18432\nv_permlane16_swap_b32 v36, v44\nMaaa 24,244,196\nv_permlane16_swap_b32 v37, v45\nMaaa 56,244,204\nTa 184,15,0\n"
"v_permlane16_swap_b32 v38, v46\nMaaa 56,240,200\nTa 186,15,2048\nv_permlane16_swap_b32 v39, v47\nMaaa 88,240,208\nAD 32,32,40\nAD 33,33,41\nMaaa 88,244,212\nTa 188,15,16384\n"
"AD 34,34,42\nMaaa 120,244,220\nTa 190,15,18432\nAD 35,35,43\nMaaa 120,240,216\nD0 4,20,48,2048\nAD 36,36,44\nMaaa 124,248,216\nAD 37,37,45\nAD 38,38,46\nMaaa 124,252,220\n"
"AD 39,39,47\nv_add_f32_dpp v32, v32, v32 row_ror:8 row_mask:0xf bank_mask:0xf\nMaaa 92,252,212\nv_add_f32_dpp v33, v33, v33 row_ror:8 row_mask:0xf bank_mask:0xf\n"
"v_add_f32_dpp v34, v34, v34 row_ror:8 row_mask:0xf bank_mask:0xf\nMaaa 92,248,208\nv_add_f32_dpp v35, v35, v35 row_ror:8 row_mask:0xf bank_mask:0xf\n"
"v_add_f32_dpp v36, v36, v36 row_ror:8 row_mask:0xf bank_mask:0xf\nMaaa 60,248,200\nv_add_f32_dpp v37, v37, v37 row_ror:8 row_mask:0xf bank_mask:0xf\n"
"v_add_f32_dpp v38, v38, v38 row_ror:8 row_mask:0xf bank_mask:0xf\nMaaa 60,252,204\nD0 5,20,48,3072\nv_add_f32_dpp v39, v39, v39 row_ror:8 row_mask:0xf bank_mask:0xf\n"
"Maaa 28,252,196\nv_add_f32_dpp v32, v32, v32 row_ror:4 row_mask:0xf bank_mask:0xf\nv_add_f32_dpp v33, v33, v33 row_ror:4 row_mask:0xf bank_mask:0xf\nMaaa 28,248,192\nWL 0\n"
"Mvaa 64,160,128\nTa 224,16,0\n.W543:\ns_cbranch_execz .W547\nv_add_f32_dpp v34, v34, v34 row_ror:4 row_mask:0xf bank_mask:0xf\nMvaa 64,164,132\nTa 226,16,2048\nMvaa 96,164,140\n"
"s_add_u32 m0, s47, -65536\ns_add_u32 s45, s45, 128\nMvaa 96,160,136\nTa 228,16,16384\nv_add_f32_dpp v35, v35, v35 row_ror:4 row_mask:0xf bank_mask:0xf\nMvaa 128,160,144\n"
"Ta 230,16,18432\nv_add_f32_dpp v36, v36, v36 row_ror:4 row_mask:0xf bank_mask:0xf\nMvaa 128,164,148\nD0 2,20,45,0\n"
"v_add_f32_dpp v37, v37, v37 row_ror:4 row_mask:0xf bank_mask:0xf\nMvaa 160,164,156\nTa 232,17,0\nv_add_f32_dpp v38, v38, v38 row_ror:4 row_mask:0xf bank_mask:0xf\n"
"Mvaa 160,160,152\nTa 234,17,2048\nv_add_f32_dpp v39, v39, v39 row_ror:4 row_mask:0xf bank_mask:0xf\nMvaa 164,168,152\n"
"v_add_f32_dpp v32, v32, v32 row_ror:2 row_mask:0xf bank_mask:0xf\nv_add_f32_dpp v33, v33, v33 row_ror:2 row_mask:0xf bank_mask:0xf\nMvaa 164,172,156\nTa 236,17,16384\n"
"v_add_f32_dpp v34, v34, v34 row_ror:2 row_mask:0xf bank_mask:0xf\nMvaa 132,172,148\nTa 238,17,18432\nv_add_f32_dpp v35, v35, v35 row_ror:2 row_mask:0xf bank_mask:0xf\n"
"Mvaa 132,168,144\nv_add_f32_dpp v36, v36, v36 row_ror:2 row_mask:0xf bank_mask:0xf\nv_add_f32_dpp v37, v37, v37 row_ror:2 row_mask:0xf bank_mask:0xf\nMvaa 100,168,136\n"
"Ta 240,18,0\nv_add_f32_dpp v38, v38, v38 row_ror:2 row_mask:0xf bank_mask:0xf\nMvaa 100,172,140\nTa 242,18,2048\nv_add_f32_dpp v39, v39, v39 row_ror:2 row_mask:0xf bank_mask:0xf\n"
"Mvaa 68,172,132\nD0 3,20,45,1024\nv_lshrrev_b32 v48, 4, v0\nMvaa 68,168,128\nTa 244,18,16384\nv_and_b32 v49, 1, v0\nMvaa 72,176,128\nTa 246,18,18432\nv_lshlrev_b32 v49, 5, v49\n"
"Mvaa 72,180,132\nv_lshl_add_u32 v49, v48, 6, v49\nMvaa 104,180,140\nTa 248,19,0\ns_lshl_b32 s2, s11, 8\nMvaa 104,176,136\nTa 250,19,2048\ns_add_u32 s2, s2, 132096\n"
"Mvaa 136,176,144\nv_add_u32 v49, s2, v49\nds_write_b128 v49, v[32:35]\nMvaa 136,180,148\nTa 252,19,16384\nds_write_b128 v49, v[36:39] offset:16\nMvaa 168,180,156\nTa 254,19,18432\n"
"v_and_b32 v50, 31, v0\nMvaa 168,176,152\nD0 4,20,45,2048\nv_lshlrev_b32 v50, 2, v50\nMvaa 172,184,152\ns_lshl_b32 s2, s11, 10\ns_add_u32 s2, s2, 133120\nMvaa 172,188,156\n"
"v_add_u32 v51, s2, v50\nv_add_u32 v50, 132096, v50\nMvaa 140,188,148\nv_lshl_add_u32 v52, v48, 4, s2\nMvaa 140,184,144\nMvaa 108,184,136\nMvaa 108,188,140\nD0 5,20,45,3072\n"
"Mvaa 76,188,132\nMvaa 76,184,128\ns_waitcnt vmcnt(18) lgkmcnt(0)\ns_barrier\nMvaa 80,224,128\nRa 192,10,16384\nds_read_b32 v32, v50 offset:0\nMvaa 80,228,132\n"
"s_add_u32 m0, s47, 0\ns_add_u32 s44, s44, 0x40000\nMvaa 112,228,140\nRa 196,11,16384\nMvaa 112,224,136\nds_read_b32 v33, v50 offset:256\nds_read_b32 v34, v50 offset:512\n"
"Mvaa 144,224,144\nRa 200,10,18432\nMvaa 144,228,148\nD0 6,24,44,0\nMvaa 176,228,156\nRa 204,11,18432\nds_read_b32 v35, v50 offset:768\nMvaa 176,224,152\n"
"ds_read_b32 v36, v50 offset:128\nMvaa 180,232,152\nRa 208,10,24576\nds_read_b32 v37, v50 offset:384\nMvaa 180,236,156\nds_read_b32 v38, v50 offset:640\nMvaa 148,236,148\n"
"Ra 212,11,24576\nMvaa 148,232,144\nds_read_b32 v39, v50 offset:896\nMvaa 116,232,136\nRa 216,10,26624\nMvaa 116,236,140\nD0 7,24,44,1024\nMvaa 84,236,132\nRa 220,11,26624\n"
"Mvaa 84,232,128\nMvaa 88,240,128\nMvaa 88,244,132\nMvaa 120,244,140\nMvaa 120,240,136\nMvaa 152,240,144\nMvaa 152,244,148\nD0 8,24,44,2048\nMvaa 184,244,156\nWL 2\nAD 32,32,33\n"
"Mvaa 184,240,152\nAD 34,34,35\nMvaa 188,248,152\nAD 36,36,37\nAD 38,38,39\nMvaa 188,252,156\nAD 32,32,34\nMvaa 156,252,148\nAD 36,36,38\nMvaa 156,248,144\nCV 32,32,36\n"
"v_and_b32 v36, 0xffff0000, v32\nMvaa 124,248,136\nv_lshlrev_b32 v32, 16, v32\nMvaa 124,252,140\nD0 9,24,44,3072\nMvaa 92,252,132\nv_mul_f32 v33, 0xbfb8aa3b, v32\nMvaa 92,248,128\n"
"WL 0\nMaaa 16,224,192\nRa 128,10,32768\nMaaa 16,228,196\ns_add_u32 m0, s47, 16384\ns_add_u32 s44, s44, 0x40000\nMaaa 48,228,204\nRa 132,11,32768\nEX 33,33\nMaaa 48,224,200\n"
"v_add_f32 v33, 1.0, v33\nMaaa 80,224,208\nRa 136,10,34816\nv_rcp_f32 v33, v33\nMaaa 80,228,212\nD0 6,24,44,0\nMaaa 112,228,220\nRa 140,11,34816\nMU 33,32,33\nMaaa 112,224,216\n"
"CV 33,33,33\nMaaa 116,232,216\nRa 144,10,40960\nv_lshlrev_b32 v33, 16, v33\nMaaa 116,236,220\nMU 33,33,36\nMaaa 84,236,212\nRa 148,11,40960\nMaaa 84,232,208\nCV 33,33,33\n"
"v_lshlrev_b32 v33, 16, v33\nMaaa 52,232,200\nRa 152,10,43008\nMaaa 52,236,204\nD0 7,24,44,1024\nMaaa 20,236,196\nRa 156,11,43008\nds_write_b32 v51, v33\nMaaa 20,232,192\n"
"Rv 244,52,0\nMaaa 24,240,192\nRv 248,52,64\ns_and_b32 s2, s74, 15\nMaaa 24,244,196\ns_sub_u32 s2, s2, 2\ns_cmp_lt_u32 s2, 14\ns_cselect_b32 s5, 0, 0x40000000\nMaaa 56,244,204\n"
"s_and_b32 s2, s2, 15\nMaaa 56,240,200\ns_mul_i32 s4, s73, 14\ns_add_u32 s4, s4, s2\nMaaa 88,240,208\nv_and_b32 v49, 15, v0\nMaaa 88,244,212\nD0 8,24,44,2048\nMaaa 120,244,220\n"
"v_lshlrev_b32 v50, 3, v49\nv_and_b32 v51, 1, v48\nMaaa 120,240,216\nv_lshl_add_u32 v50, v51, 2, v50\nMaaa 124,248,216\ns_mul_i32 s2, s11, 112\nv_add_u32 v50, s2, v50\n"
"Maaa 124,252,220\nv_lshrrev_b32 v51, 4, v50\nMaaa 92,252,212\nv_lshrrev_b32 v48, 1, v48\nMaaa 92,248,208\nv_mad_u32_u24 v51, v48, 28, v51\nv_mul_u32_u24 v51, 7168, v51\n"
"Maaa 60,248,200\nv_and_b32 v50, 15, v50\nMaaa 60,252,204\nD0 9,24,44,3072\nMaaa 28,252,196\nv_lshl_add_u32 v51, v50, 2, v51\nv_cmp_gt_u32 vcc, 14, v49\ns_lshl_b32 s4, s4, 6\n"
"s_add_u32 s4, s4, 0xc04c800\nv_cndmask_b32 v51, v254, v51, vcc\nMaaa 28,248,192\ns_waitcnt vmcnt(16) lgkmcnt(3)\ns_barrier\nMaaa 0,160,192\nTa 224,16,32768\ns_or_b32 s4, s4, s5\n"
"Maaa 0,164,196\nTa 226,16,34816\nMaaa 32,164,204\ns_add_u32 m0, s47, -49152\ns_add_u32 s48, s48, 128\nMaaa 32,160,200\nTa 228,16,49152\nWL 3\nMaaa 64,160,208\nTa 230,16,51200\n"
"Maaa 64,164,212\nD0 2,20,48,0\nMaaa 96,164,220\nTa 232,17,32768\nv_lshlrev_b32 v252, 16, v224\nMaaa 96,160,216\nTa 234,17,34816\nMaaa 100,168,216\n"
"v_and_b32 v253, 0xffff0000, v224\nMU 32,244,252\nMaaa 100,172,220\nTa 236,17,49152\nMaaa 68,172,212\nTa 238,17,51200\nMaaa 68,168,208\nMU 33,244,253\nMU 40,248,252\n"
"Maaa 36,168,200\nTa 240,18,32768\nMaaa 36,172,204\nTa 242,18,34816\nMaaa 4,172,196\nD0 3,20,48,1024\nMU 41,248,253\nMaaa 4,168,192\nTa 244,18,49152\nMaaa 8,176,192\n"
"Ta 246,18,51200\nv_lshlrev_b32 v252, 16, v225\nMaaa 8,180,196\nv_and_b32 v253, 0xffff0000, v225\nMaaa 40,180,204\nTa 248,19,32768\nMaaa 40,176,200\nTa 250,19,34816\nMU 34,244,252\n"
"Maaa 72,176,208\nMU 35,244,253\nMaaa 72,180,212\nTa 252,19,49152\nMaaa 104,180,220\nTa 254,19,51200\nMU 42,248,252\nMaaa 104,176,216\nD0 4,20,48,2048\nMaaa 108,184,216\n"
"MU 43,248,253\nv_lshlrev_b32 v252, 16, v226\nMaaa 108,188,220\nv_and_b32 v253, 0xffff0000, v226\nMaaa 76,188,212\nMU 36,244,252\nMaaa 76,184,208\nMU 37,244,253\nMU 44,248,252\n"
"Maaa 44,184,200\nMU 45,248,253\nMaaa 44,188,204\nD0 5,20,48,3072\nMaaa 12,188,196\nv_lshlrev_b32 v252, 16, v227\nv_and_b32 v253, 0xffff0000, v227\nMaaa 12,184,192\nWL 0\n"
"Mvaa 80,224,128\nTa 160,12,32768\nMU 38,244,252\nMvaa 80,228,132\nTa 162,12,34816\nMvaa 112,228,140\ns_add_u32 m0, s47, -32768\ns_add_u32 s45, s45, 128\nMvaa 112,224,136\n"
"Ta 164,12,49152\nMU 39,244,253\nMvaa 144,224,144\nTa 166,12,51200\nMvaa 144,228,148\nD0 2,20,45,0\nMvaa 176,228,156\nTa 168,13,32768\nMU 46,248,252\nMvaa 176,224,152\n"
"Ta 170,13,34816\nMvaa 180,232,152\nMU 47,248,253\nv_lshlrev_b32 v252, 16, v228\nMvaa 180,236,156\nTa 172,13,49152\nMvaa 148,236,148\nTa 174,13,51200\nMvaa 148,232,144\n"
"v_and_b32 v253, 0xffff0000, v228\nFM 32,245,252\nMvaa 116,232,136\nTa 176,14,32768\nMvaa 116,236,140\nTa 178,14,34816\nMvaa 84,236,132\nD0 3,20,45,1024\nFM 33,245,253\n"
"Mvaa 84,232,128\nTa 180,14,49152\nMvaa 88,240,128\nTa 182,14,51200\nFM 40,249,252\nMvaa 88,244,132\nFM 41,249,253\nMvaa 120,244,140\nTa 184,15,32768\nMvaa 120,240,136\n"
"Ta 186,15,34816\nv_lshlrev_b32 v252, 16, v229\nMvaa 152,240,144\nv_and_b32 v253, 0xffff0000, v229\nMvaa 152,244,148\nTa 188,15,49152\nMvaa 184,244,156\nTa 190,15,51200\n"
"FM 34,245,252\nMvaa 184,240,152\nD0 4,20,45,2048\nMvaa 188,248,152\nFM 35,245,253\nFM 42,249,252\nMvaa 188,252,156\nFM 43,249,253\nMvaa 156,252,148\nv_lshlrev_b32 v252, 16, v230\n"
"Mvaa 156,248,144\nv_and_b32 v253, 0xffff0000, v230\nFM 36,245,252\nMvaa 124,248,136\nFM 37,245,253\nMvaa 124,252,140\nD0 5,20,45,3072\nMvaa 92,252,132\nFM 44,249,252\n"
"FM 45,249,253\nMvaa 92,248,128\ns_waitcnt vmcnt(16) lgkmcnt(0)\ns_barrier\nMvaa 64,160,128\nRa 192,10,49152\nv_lshlrev_b32 v252, 16, v231\nMvaa 64,164,132\n"
"s_add_u32 m0, s47, 32768\ns_add_u32 s44, s44, 0x40000\nMvaa 96,164,140\nRa 196,11,49152\nMvaa 96,160,136\nv_and_b32 v253, 0xffff0000, v231\nFM 38,245,252\nMvaa 128,160,144\n"
"Ra 200,10,51200\nMvaa 128,164,148\nD0 6,24,44,0\nMvaa 160,164,156\nRa 204,11,51200\nFM 39,245,253\nMvaa 160,160,152\nFM 46,249,252\nMvaa 164,168,152\nRa 208,10,57344\n"
"FM 47,249,253\nMvaa 164,172,156\nv_lshlrev_b32 v252, 16, v232\nMvaa 132,172,148\nRa 212,11,57344\nMvaa 132,168,144\nv_and_b32 v253, 0xffff0000, v232\nFM 32,246,252\n"
"Mvaa 100,168,136\nRa 216,10,59392\nMvaa 100,172,140\nD0 7,24,44,1024\nMvaa 68,172,132\nRa 220,11,59392\nFM 33,246,253\nMvaa 68,168,128\nFM 40,250,252\nMvaa 72,176,128\n"
"FM 41,250,253\nv_lshlrev_b32 v252, 16, v233\nMvaa 72,180,132\nv_and_b32 v253, 0xffff0000, v233\nMvaa 104,180,140\nFM 34,246,252\nMvaa 104,176,136\nFM 35,246,253\nFM 42,250,252\n"
"Mvaa 136,176,144\nFM 43,250,253\nMvaa 136,180,148\nD0 8,24,44,2048\nMvaa 168,180,156\nv_lshlrev_b32 v252, 16, v234\nv_and_b32 v253, 0xffff0000, v234\nMvaa 168,176,152\n"
"FM 36,246,252\nMvaa 172,184,152\nFM 37,246,253\nFM 44,250,252\nMvaa 172,188,156\nFM 45,250,253\nMvaa 140,188,148\nv_lshlrev_b32 v252, 16, v235\nMvaa 140,184,144\n"
"v_and_b32 v253, 0xffff0000, v235\nFM 38,246,252\nMvaa 108,184,136\nFM 39,246,253\nMvaa 108,188,140\nD0 9,24,44,3072\nMvaa 76,188,132\nFM 46,250,252\nFM 47,250,253\n"
"Mvaa 76,184,128\nWL 0\nMaaa 0,160,192\nRa 128,10,0\nv_lshlrev_b32 v252, 16, v236\nMaaa 0,164,196\ns_add_u32 m0, s47, 49152\ns_add_u32 s44, s44, 0x40000\nMaaa 32,164,204\n"
"Ra 132,11,0\nMaaa 32,160,200\nv_and_b32 v253, 0xffff0000, v236\nFM 32,247,252\nMaaa 64,160,208\nRa 136,10,2048\nMaaa 64,164,212\nD0 6,24,44,0\nMaaa 96,164,220\nRa 140,11,2048\n"
"FM 33,247,253\nMaaa 96,160,216\nFM 40,251,252\nMaaa 100,168,216\nRa 144,10,8192\nFM 41,251,253\nMaaa 100,172,220\nv_lshlrev_b32 v252, 16, v237\nMaaa 68,172,212\nRa 148,11,8192\n"
"Maaa 68,168,208\nv_and_b32 v253, 0xffff0000, v237\nFM 34,247,252\nMaaa 36,168,200\nRa 152,10,10240\nMaaa 36,172,204\nD0 7,24,44,1024\nMaaa 4,172,196\nRa 156,11,10240\n"
"FM 35,247,253\nMaaa 4,168,192\nFM 42,251,252\nMaaa 8,176,192\nFM 43,251,253\nv_lshlrev_b32 v252, 16, v238\nMaaa 8,180,196\nv_and_b32 v253, 0xffff0000, v238\nMaaa 40,180,204\n"
"FM 36,247,252\nMaaa 40,176,200\nFM 37,247,253\nFM 44,251,252\nMaaa 72,176,208\nFM 45,251,253\nMaaa 72,180,212\nD0 8,24,44,2048\nMaaa 104,180,220\nv_lshlrev_b32 v252, 16, v239\n"
"v_and_b32 v253, 0xffff0000, v239\nMaaa 104,176,216\nFM 38,247,252\nMaaa 108,184,216\nFM 39,247,253\nFM 46,251,252\nMaaa 108,188,220\nFM 47,251,253\nMaaa 76,188,212\n"
"v_permlane32_swap_b32 v32, v40\nMaaa 76,184,208\nv_permlane32_swap_b32 v33, v41\nv_permlane32_swap_b32 v34, v42\nMaaa 44,184,200\nv_permlane32_swap_b32 v35, v43\nMaaa 44,188,204\n"
"D0 9,24,44,3072\nMaaa 12,188,196\nv_permlane32_swap_b32 v36, v44\nv_permlane32_swap_b32 v37, v45\nMaaa 12,184,192\nWV 16\ns_barrier\nMaaa 16,224,192\nTa 160,12,0\n"
"v_permlane32_swap_b32 v38, v46\nMaaa 16,228,196\nTa 162,12,2048\nMaaa 48,228,204\ns_add_u32 m0, s47, -16384\ns_add_u32 s48, s48, 128\nMaaa 48,224,200\nTa 164,12,16384\n"
"v_permlane32_swap_b32 v39, v47\nMaaa 80,224,208\nTa 166,12,18432\nMaaa 80,228,212\nD0 2,20,48,0\nMaaa 112,228,220\nTa 168,13,0\nAD 32,32,40\nMaaa 112,224,216\nTa 170,13,2048\n"
"Maaa 116,232,216\nAD 33,33,41\nAD 34,34,42\nMaaa 116,236,220\nTa 172,13,16384\nMaaa 84,236,212\nTa 174,13,18432\nMaaa 84,232,208\nAD 35,35,43\nAD 36,36,44\nMaaa 52,232,200\n"
"Ta 176,14,0\nMaaa 52,236,204\nTa 178,14,2048\nMaaa 20,236,196\nD0 3,20,48,1024\nAD 37,37,45\nMaaa 20,232,192\nTa 180,14,16384\nMaaa 24,240,192\nTa 182,14,18432\nAD 38,38,46\n"
"Maaa 24,244,196\nAD 39,39,47\n.W544:\ns_cbranch_execz .W548\nMaaa 56,244,204\nTa 184,15,0\nMaaa 56,240,200\nTa 186,15,2048\nv_permlane16_swap_b32 v32, v36\nMaaa 88,240,208\n"
"v_permlane16_swap_b32 v33, v37\nMaaa 88,244,212\nTa 188,15,16384\nMaaa 120,244,220\nTa 190,15,18432\nv_permlane16_swap_b32 v34, v38\nMaaa 120,240,216\nD0 4,20,48,2048\n"
"Maaa 124,248,216\nv_permlane16_swap_b32 v35, v39\nAD 32,32,36\nMaaa 124,252,220\nAD 33,33,37\nMaaa 92,252,212\nAD 34,34,38\nMaaa 92,248,208\nAD 35,35,39\nSv 32,51,16,4,0\n"
"Maaa 60,248,200\nMaaa 60,252,204\nD0 5,20,48,3072\nMaaa 28,252,196\nMaaa 28,248,192\nWL 0\nMvaa 64,160,128\nTa 224,16,0\ns_lshr_b32 s3, s11, 1\nMvaa 64,164,132\nTa 226,16,2048\n"
"Mvaa 96,164,140\ns_lshl_b32 s3, s3, 13\nMvaa 96,160,136\nTa 228,16,16384\nMvaa 128,160,144\nTa 230,16,18432\nMvaa 128,164,148\ns_lshl_b32 s2, s11, 12\nMvaa 160,164,156\n"
"Ta 232,17,0\nMvaa 160,160,152\nTa 234,17,2048\nMvaa 164,168,152\ns_add_u32 s2, s2, s3\ns_add_u32 s2, s2, s72\nMvaa 164,172,156\nTa 236,17,16384\nMvaa 132,172,148\nTa 238,17,18432\n"
"Mvaa 132,168,144\ns_lshl_b32 s3, s11, 12\nMvaa 100,168,136\nTa 240,18,0\nMvaa 100,172,140\nTa 242,18,2048\nMvaa 68,172,132\ns_add_u32 s89, s3, 0x0\nMvaa 68,168,128\n"
"Ta 244,18,16384\nMvaa 72,176,128\nTa 246,18,18432\ns_mov_b32 s88, 0xffff8000\nMvaa 72,180,132\ns_lshl_b32 s4, s13, 1\nMvaa 104,180,140\nTa 248,19,0\nMvaa 104,176,136\n"
"Ta 250,19,2048\nMvaa 136,176,144\ns_sub_u32 s79, 15, s4\nMvaa 136,180,148\nTa 252,19,16384\nMvaa 168,180,156\nTa 254,19,18432\nMvaa 168,176,152\ns_add_u32 s96, s2, 0x1310000\n"
"Mvaa 172,184,152\ns_mov_b32 s63, 0x53800000\ns_mov_b32 s64, 0x2b800000\nMvaa 172,188,156\ns_sub_u32 s98, s4, 1\nMvaa 140,188,148\ns_lshl_b32 s5, s13, 5\nMvaa 140,184,144\n"
"s_add_u32 s5, s5, s11\nMvaa 108,184,136\ns_lshl_b32 s65, s5, 9\nMvaa 108,188,140\ns_add_u32 s65, s65, s72\nMvaa 76,188,132\ns_add_u32 s65, s65, 0x1580000\nMvaa 76,184,128\n"
"s_waitcnt vmcnt(13) lgkmcnt(0)\ns_barrier\nMvaa 80,224,128\nRa 192,10,16384\ns_and_b32 s66, s72, 0x2000\ns_add_u32 m0, s47, 65536\ns_add_u32 s46, s56, 0x0\nMvaa 80,228,132\n"
"s_add_u32 s66, s66, s56\nMvaa 112,228,140\nRa 196,11,16384\nD1 1,16,46,0\nMvaa 112,224,136\ns_add_u32 s66, s66, 0x100000\nMvaa 144,224,144\nRa 200,10,18432\nMvaa 144,228,148\n"
"s_and_b32 s5, s12, 1\nMvaa 176,228,156\nRa 204,11,18432\nMvaa 176,224,152\ns_mul_i32 s58, s5, 0x202000\nD1 1,16,46,1024\nMvaa 180,232,152\nRa 208,10,24576\n"
"s_xor_b32 s58, s58, s72\nMvaa 180,236,156\ns_add_u32 s58, s58, 0x15c0000\nMvaa 148,236,148\nRa 212,11,24576\nMvaa 148,232,144\ns_lshl_b32 s5, s5, 8\nMvaa 116,232,136\n"
"Ra 216,10,26624\nD1 1,16,46,2048\nMvaa 116,236,140\ns_add_u32 s61, s58, s5\nMvaa 84,236,132\nRa 220,11,26624\nMvaa 84,232,128\ns_lshl4_add_u32 s60, s13, s61\nMvaa 88,240,128\n"
"s_add_u32 s54, s60, 0\ns_xor_b32 s60, s54, 0x100\nMvaa 88,244,132\ns_cmp_eq_u32 s11, 0\ns_cselect_b32 s54, s54, 0x40000000\nD1 1,16,46,3072\nMvaa 120,244,140\ns_getpc_b64 s[8:9]\n"
".PC37:\ns_add_u32 s8, s8, .HD13-.PC37\ns_addc_u32 s9, s9, 0\nMvaa 120,240,136\ns_mov_b32 s97, 0xff800000\nMvaa 152,240,144\ns_mov_b32 s90, 3\ns_cmp_lt_u32 s11, 2\n"
"s_cselect_b32 s92, 2, 0\ns_add_u32 m0, s47, 81920\ns_add_u32 s46, s56, 0x1000\nMvaa 152,244,148\ns_add_u32 s86, s2, 0x20000\nMvaa 184,244,156\ns_add_u32 s87, s2, 0x410000\n"
"s_cmp_eq_u32 s13, 7\ns_cselect_b32 s92, s92, 0\nD1 1,16,46,0\nMvaa 184,240,152\ns_cmp_gt_u32 s13, 5\ns_cselect_b32 s4, s12, 15\nMvaa 188,248,152\ns_cmp_lt_u32 s4, 14\n"
"s_cselect_b32 s5, s11, 3\nMvaa 188,252,156\ns_cmp_lt_u32 s5, 2\ns_cselect_b32 s91, 1, 100\nMvaa 156,252,148\nv_and_b32 v27, 0x70, v1\n"
"buffer_load_dword v27, v27, s[16:19], s61 offen offset:128 sc0 sc1\nMvaa 156,248,144\ns_lshr_b32 s2, s13, 2\ns_bfe_u32 s3, s13, 0x10001\nD1 1,16,46,1024\nMvaa 124,248,136\n"
"s_mul_i32 s3, s3, 17\ns_add_u32 s2, s2, s3\nMvaa 124,252,140\ns_lshl2_add_u32 s3, s14, 0\nMvaa 92,252,132\ns_lshl_b32 s3, s3, 2\ns_lshr_b32 s3, 0xe420ca86, s3\nMvaa 92,248,128\n"
"WL 0\nMaaa 16,224,192\nRa 128,10,32768\ns_and_b32 s3, s3, 15\nD1 1,16,46,2048\nMaaa 16,228,196\ns_add_u32 s2, s2, s3\ns_cmp_eq_u32 s2, 32\ns_cselect_b32 s4, 16, s2\n"
"Maaa 48,228,204\nRa 132,11,32768\ns_mul_i32 s75, s4, 65536\nMaaa 48,224,200\ns_lshr_b32 s2, s13, 2\ns_bfe_u32 s3, s13, 0x10001\nMaaa 80,224,208\nRa 136,10,34816\nMaaa 80,228,212\n"
"s_mul_i32 s3, s3, 17\ns_add_u32 s2, s2, s3\nD1 1,16,46,3072\nMaaa 112,228,220\nRa 140,11,34816\ns_lshl2_add_u32 s3, s14, 1\nMaaa 112,224,216\ns_lshl_b32 s3, s3, 2\n"
"Maaa 116,232,216\nRa 144,10,40960\ns_lshr_b32 s3, 0xe420ca86, s3\ns_add_u32 m0, s47, 16384\ns_add_u32 s46, s56, 0x4000\nMaaa 116,236,220\ns_and_b32 s3, s3, 15\n"
"s_add_u32 s2, s2, s3\nMaaa 84,236,212\nRa 148,11,40960\ns_cmp_eq_u32 s2, 32\ns_cselect_b32 s4, 16, s2\nD1 1,16,46,0\nMaaa 84,232,208\ns_mul_i32 s76, s4, 65536\n"
"s_lshr_b32 s2, s13, 2\nMaaa 52,232,200\nRa 152,10,43008\ns_bfe_u32 s3, s13, 0x10001\nMaaa 52,236,204\ns_mul_i32 s3, s3, 17\nMaaa 20,236,196\nRa 156,11,43008\ns_add_u32 s2, s2, s3\n"
"Maaa 20,232,192\ns_lshl2_add_u32 s3, s14, 2\ns_lshl_b32 s3, s3, 2\nD1 1,16,46,1024\nMaaa 24,240,192\ns_lshr_b32 s3, 0xe420ca86, s3\ns_and_b32 s3, s3, 15\nMaaa 24,244,196\n"
"s_add_u32 s2, s2, s3\ns_cmp_eq_u32 s2, 32\ns_cselect_b32 s4, 16, s2\nMaaa 56,244,204\ns_mul_i32 s77, s4, 65536\ns_lshr_b32 s2, s13, 2\nMaaa 56,240,200\ns_bfe_u32 s3, s13, 0x10001\n"
"s_mul_i32 s3, s3, 17\nMaaa 88,240,208\ns_add_u32 s2, s2, s3\nD1 1,16,46,2048\nMaaa 88,244,212\ns_lshl2_add_u32 s3, s14, 3\ns_lshl_b32 s3, s3, 2\nMaaa 120,244,220\n"
"s_lshr_b32 s3, 0xe420ca86, s3\ns_and_b32 s3, s3, 15\nMaaa 120,240,216\ns_add_u32 s2, s2, s3\ns_cmp_eq_u32 s2, 32\ns_cselect_b32 s4, 16, s2\nMaaa 124,248,216\n"
"s_mul_i32 s78, s4, 65536\ns_lshr_b32 s3, s11, 1\nMaaa 124,252,220\ns_lshl_b32 s3, s3, 15\ns_add_u32 s2, s72, s3\nD1 1,16,46,3072\nMaaa 92,252,212\ns_and_b32 s3, s11, 1\n"
"Maaa 92,248,208\ns_lshl_b32 s3, s3, 12\ns_add_u32 s2, s2, s3\nMaaa 60,248,200\ns_add_u32 s2, s2, 0x0\ns_mov_b32 s55, 16384\nMaaa 60,252,204\ns_add_u32 s75, s75, s2\n"
"Maaa 28,252,196\ns_add_u32 s76, s76, s2\ns_add_u32 s77, s77, s2\nMaaa 28,248,192\ns_waitcnt vmcnt(18) lgkmcnt(0)\ns_barrier\nMaaa 0,160,192\nTa 224,16,32768\n"
"s_add_u32 s78, s78, s2\nMaaa 0,164,196\nTa 226,16,34816\nMaaa 32,164,204\nMaaa 32,160,200\nTa 228,16,49152\nMaaa 64,160,208\nTa 230,16,51200\nMaaa 64,164,212\nMaaa 96,164,220\n"
"Ta 232,17,32768\nMaaa 96,160,216\nTa 234,17,34816\nMaaa 100,168,216\nWV 7\nv_cmp_ne_u32 vcc, s15, v27\ns_nop 0\ns_cbranch_vccnz .S38\n.S39:\ns_sub_u32 s45, s86, 131072\n"
"Maaa 100,172,220\nTa 236,17,49152\ns_add_u32 m0, s47, 0\ns_add_u32 s46, s45, 0x0\nMaaa 68,172,212\nTa 238,17,51200\nMaaa 68,168,208\nD1 1,16,46,0\nMaaa 36,168,200\n"
"Ta 240,18,32768\ns_mov_b32 s36, s68\nMaaa 36,172,204\nTa 242,18,34816\nMaaa 4,172,196\ns_and_b32 s37, s69, 0xffff\nMaaa 4,168,192\nTa 244,18,49152\nMaaa 8,176,192\n"
"Ta 246,18,51200\nD1 1,16,46,1024\nMaaa 8,180,196\ns_mov_b32 s38, 0x1c0000\nMaaa 40,180,204\nTa 248,19,32768\nMaaa 40,176,200\nTa 250,19,34816\nMaaa 72,176,208\n"
"s_mov_b32 s39, 0x20000\ns_and_b32 s2, s74, 15\nMaaa 72,180,212\nTa 252,19,49152\nD1 1,16,46,2048\nMaaa 104,180,220\nTa 254,19,51200\nMaaa 104,176,216\ns_max_u32 s2, s2, 2\n"
"Maaa 108,184,216\ns_sub_u32 s2, s2, 2\ns_mul_i32 s3, s73, 14\nMaaa 108,188,220\ns_add_u32 s2, s2, s3\nMaaa 76,188,212\nD1 1,16,46,3072\nMaaa 76,184,208\n"
"s_lshl2_add_u32 s2, s2, s11\nMaaa 44,184,200\ns_mul_i32 s3, s2, 4682\ns_lshr_b32 s3, s3, 16\nMaaa 44,188,204\ns_mul_i32 s22, s3, 14\nMaaa 12,188,196\ns_sub_u32 s22, s2, s22\n"
"Maaa 12,184,192\nWL 0\nMvaa 80,224,128\nTa 160,12,32768\ns_add_u32 m0, s47, -32768\ns_add_u32 s46, s56, 0x5000\nD1 1,16,46,0\nMvaa 80,228,132\nTa 162,12,34816\nMvaa 84,236,132\n"
"s_and_b32 s21, s3, 28\nMvaa 84,232,128\nTa 164,12,49152\nMvaa 88,240,128\nTa 166,12,51200\ns_add_u32 s21, s21, s3\nMvaa 88,244,132\nD1 1,16,46,1024\nMvaa 92,252,132\n"
"Ta 168,13,32768\nMvaa 92,248,128\nTa 170,13,34816\nMvaa 124,248,136\ns_lshl_b32 s21, s21, 4\ns_add_u32 s21, s21, 1024\nMvaa 124,252,140\nTa 172,13,49152\nMvaa 120,244,140\n"
"Ta 174,13,51200\nD1 1,16,46,2048\nMvaa 120,240,136\ns_xor_b32 s3, s3, 28\nMvaa 116,232,136\nTa 176,14,32768\ns_bfe_u32 s41, s3, 0x30002\nMvaa 116,236,140\nTa 178,14,34816\n"
"s_cmp_gt_u32 s22, 6\ns_cselect_b32 s2, 1179648, 0\ns_cselect_b32 s1, 16, 0\nMvaa 112,228,140\ns_add_u32 s41, s41, s1\nCV 40,80,81\nMvaa 112,224,136\nTa 180,14,49152\n"
"D1 1,16,46,3072\nMvaa 144,224,144\nTa 182,14,51200\ns_lshl4_add_u32 s41, s41, 0xc048400\nMvaa 144,228,148\ns_and_b32 s1, s3, 28\nMvaa 148,236,148\nTa 184,15,32768\n"
"s_add_u32 m0, s47, -65536\ns_add_u32 s46, s45, 0x10000\nMvaa 148,232,144\nTa 186,15,34816\nCV 41,82,83\nMvaa 152,240,144\nD1 1,16,46,0\ns_add_u32 s3, s3, s1\nMvaa 152,244,148\n"
"Ta 188,15,49152\nCV 42,84,85\nMvaa 156,252,148\nTa 190,15,51200\nMvaa 156,248,144\ns_lshl_b32 s3, s3, 11\nCV 43,86,87\nMvaa 188,248,152\ns_add_u32 s2, s2, s3\n"
"s_lshl_b32 s3, s22, 17\nMvaa 188,252,156\nD1 1,16,46,1024\nMvaa 184,244,156\ns_add_u32 s2, s2, s3\nCV 44,88,89\nMvaa 184,240,152\ns_add_u32 s20, s2, 0xce0000\nMvaa 180,232,152\n"
"s_lshl_b32 s2, s22, 7\ns_add_u32 s40, s2, 0xc04b800\nMvaa 180,236,156\ns_mul_i32 s2, s21, 896\nMvaa 176,228,156\nD1 1,16,46,2048\nCV 45,90,91\nMvaa 176,224,152\nWL 0\ns_barrier\n"
"Mvaa 64,160,128\nRa 192,10,49152\ns_lshl_b32 s3, s22, 6\nMvaa 64,164,132\ns_add_u32 s22, s2, s3\nMvaa 68,172,132\nRa 196,11,49152\nMvaa 68,168,128\nD1 1,16,46,3072\nCV 46,92,93\n"
"Mvaa 72,176,128\nRa 200,10,51200\ns_add_u32 s23, s22, 57344\nMvaa 72,180,132\nCV 47,94,95\nCV 60,120,121\nMvaa 76,188,132\nRa 204,11,51200\ns_add_u32 m0, s47, 32768\n"
"s_add_u32 s46, s45, 0x8000\nMvaa 76,184,128\nCV 61,122,123\nCV 62,124,125\nMvaa 108,184,136\nRa 208,10,57344\nD1 1,16,46,0\nMvaa 108,188,140\nCV 63,126,127\nMvaa 104,180,140\n"
"Ra 212,11,57344\nCV 56,112,113\nMvaa 104,176,136\nCV 57,114,115\nMvaa 100,168,136\nRa 216,10,59392\nCV 58,116,117\nMvaa 100,172,140\nD1 1,16,46,1024\nMvaa 96,164,140\n"
"Ra 220,11,59392\nCV 59,118,119\nMvaa 96,160,136\nCV 32,64,65\nMvaa 128,160,144\nCV 33,66,67\nCV 34,68,69\nMvaa 128,164,148\nCV 35,70,71\nMvaa 132,172,148\nD1 1,16,46,2048\n"
"Mvaa 132,168,144\nCV 36,72,73\nCV 37,74,75\nMvaa 136,176,144\nCV 38,76,77\nMvaa 136,180,148\nCV 39,78,79\nCV 52,104,105\nMvaa 140,188,148\nCV 53,106,107\nMvaa 140,184,144\n"
"D1 1,16,46,3072\nCV 54,108,109\nMvaa 172,184,152\nCV 55,110,111\nMvaa 172,188,156\nCV 48,96,97\nMvaa 168,180,156\ns_add_u32 m0, s47, 49152\ns_add_u32 s46, s45, 0x400000\n"
"Mvaa 168,176,152\nCV 49,98,99\nMvaa 164,168,152\nD1 1,16,46,0\nCV 50,100,101\nMvaa 164,172,156\nCV 51,102,103\nCV 104,144,145\nCV 105,146,147\nMvaa 160,164,156\nCV 106,148,149\n"
"CV 107,150,151\nMvaa 160,160,152\nWL 0\nMaaa 0,160,192\nSv 32,1,16,75,0\nCV 108,152,153\nCV 109,154,155\nMaaa 0,164,196\nD1 1,16,46,1024\nCV 110,156,157\nMaaa 4,172,196\n"
"CV 111,158,159\nCV 124,184,185\nMaaa 4,168,192\nCV 125,186,187\nCV 126,188,189\nMaaa 8,176,192\nCV 127,190,191\nCV 120,176,177\nCV 121,178,179\nMaaa 8,180,196\nCV 122,180,181\n"
"CV 123,182,183\nMaaa 12,188,196\nD1 1,16,46,2048\nCV 96,128,129\nMaaa 12,184,192\nCV 97,130,131\nCV 98,132,133\nCV 99,134,135\nMaaa 44,184,200\nSv 36,1,16,75,1024\nCV 100,136,137\n"
"Maaa 44,188,204\nCV 101,138,139\nCV 102,140,141\nMaaa 40,180,204\nCV 103,142,143\nCV 116,168,169\nCV 117,170,171\nMaaa 40,176,200\nD1 1,16,46,3072\n.W545:\ns_cbranch_execz .W549\n"
"CV 118,172,173\nMaaa 36,168,200\nCV 119,174,175\nCV 112,160,161\nMaaa 36,172,204\nCV 113,162,163\nCV 114,164,165\nCV 115,166,167\nMaaa 32,164,204\ns_add_u32 m0, s47, -49152\n"
"s_add_u32 s46, s45, 0x408000\nMaaa 32,160,200\nv_add_u32 v2, 0x10000, v1\ns_lshr_b32 s2, s59, 1\nMaaa 64,160,208\nD1 1,16,46,0\nSv 40,1,16,75,2048\nv_add_u32 v24, s2, v2\n"
"Maaa 64,164,212\nv_xor_b32 v26, 0x1000, v24\nv_lshrrev_b32 v20, 4, v0\nMaaa 68,172,212\nv_lshlrev_b32 v20, 2, v20\nv_and_b32 v0, 15, v0\nMaaa 68,168,208\nv_sub_u32 v0, v0, v20\n"
"s_lshl_b32 s2, s11, 4\nMaaa 72,176,208\nv_add_u32 v3, s2, v0\nv_subrev_u32 v0, 32, v3\ns_lshl_b32 s2, s11, 12\nMaaa 72,180,212\nD1 1,16,46,1024\nv_add_u32 v22, s2, v1\n"
"Maaa 76,188,212\nv_add_u32 v23, 0x20000, v22\nv_add_u32 v22, 0x8000, v22\nMaaa 76,184,208\nv_mov_b32 v4, 0\nv_mov_b32 v5, 0\nv_mov_b32 v6, 0\nMaaa 108,184,216\nSv 44,1,16,75,3072\n"
"v_mov_b32 v7, 0\nMaaa 108,188,220\nv_mov_b32 v8, 0\nv_mov_b32 v9, 0\nMaaa 104,180,220\nD1 1,16,46,2048\nv_mov_b32 v10, 0\nv_mov_b32 v11, 0\nMaaa 104,176,216\nv_mov_b32 v12, 0\n"
"v_mov_b32 v13, 0\nMaaa 100,168,216\nv_mov_b32 v14, 0\nv_mov_b32 v15, 0\nMaaa 100,172,220\nv_mov_b32 v16, 0\nv_mov_b32 v17, 0\nv_mov_b32 v18, 0\nMaaa 96,164,220\nv_mov_b32 v19, 0\n"
"v_mov_b32 v86, 0\nMaaa 96,160,216\nWV 19\ns_barrier\nMaaa 16,224,192\nD1 1,16,46,3072\ns_add_u32 s75, s75, s55\nSv 48,1,16,75,0\nMaaa 16,228,196\nv_mov_b32 v87, 0\n"
"v_mov_b32 v88, 0\nMaaa 20,236,196\nv_mov_b32 v89, 0\nv_mov_b32 v95, -1\nMaaa 20,232,192\nMaaa 24,240,192\nRa 128,24,0\nMaaa 24,244,196\nMaaa 28,252,196\nRa 196,23,1024\n"
"Ra 132,24,1024\nMaaa 28,248,192\nRa 192,23,0\nMaaa 60,248,200\nSv 52,1,16,75,1024\nRa 136,24,2048\nMaaa 60,252,204\nMaaa 56,244,204\nRa 140,24,3072\nMaaa 56,240,200\n"
"Maaa 52,232,200\nRa 144,26,0\nMaaa 52,236,204\nMaaa 48,228,204\nRa 204,23,3072\nRa 148,26,1024\nMaaa 48,224,200\nRa 200,23,2048\nMaaa 80,224,208\nSv 56,1,16,75,2048\n"
"Ra 152,26,2048\nMaaa 80,228,212\nMaaa 84,236,212\nRa 156,26,3072\nMaaa 84,232,208\nMaaa 88,240,208\nMaaa 88,244,212\nMaaa 92,252,212\nRa 212,23,17408\nMaaa 92,248,208\n"
"Ra 208,23,16384\nMaaa 124,248,216\nRa 248,22,2048\nSv 60,1,16,75,3072\nMaaa 124,252,220\nRa 252,22,3072\nMaaa 120,244,220\nRa 244,22,1024\nMaaa 120,240,216\nRa 240,22,0\n"
"Maaa 116,232,216\nRa 232,22,51200\nMaaa 116,236,220\nRa 236,22,52224\nMaaa 112,228,220\nRa 228,22,50176\nRa 220,23,19456\nMaaa 112,224,216\nRa 224,22,49152\nRa 216,23,18432\nWL 0\n"
"Cvaa 32,128,192,4\nRa 160,24,8192\nMvaa 32,132,200\nMvaa 32,136,208\nRa 164,24,9216\nMvaa 32,140,216\nCvaa 36,140,220,8\nRa 168,24,10240\nMvaa 36,136,212\n"
"s_add_u32 m0, s89, 32768\nMvaa 36,132,204\nRa 172,24,11264\ns_barrier\nMvaa 36,128,196\nMvaa 36,144,228\nRa 176,26,8192\nSv 96,1,16,76,0\nMvaa 36,148,236\ns_add_u32 s46, s86, s88\n"
"Mvaa 36,152,244\nRa 180,26,9216\nMvaa 36,156,252\nAR 92,0\nMvaa 32,156,248\nRa 184,26,10240\nAR 93,1\nMvaa 32,152,240\nD1 1,16,46,0\nMvaa 32,148,232\nRa 188,26,11264\nWL 4\n"
"Mvaa 32,144,224\nCV 96,92,93\nCvaa 40,160,192,12\nAR 92,2\nAR 93,3\nMvaa 40,164,200\nCV 97,92,93\nMvaa 40,168,208\nv_and_b32 v27, 0x70, v1\n"
"buffer_load_dword v27, v27, s[16:19], s61 offen offset:136 sc0 sc1\nMvaa 40,172,216\nAR 92,4\nCvaa 44,172,220,16\nAR 93,5\nCV 98,92,93\nMvaa 44,168,212\nD1 1,16,46,1024\n"
"Mvaa 44,164,204\nWL 0\nMvaa 44,160,196\nAR 92,6\nMvaa 44,176,228\nSv 100,1,16,76,1024\nAR 93,7\nMvaa 44,180,236\ns_add_u32 s88, s88, 32768\nEX 80,32\nMvaa 44,184,244\nCV 99,92,93\n"
"AR 92,8\nMvaa 44,188,252\nEX 81,33\nMvaa 40,188,248\nAR 93,9\nCV 100,92,93\nMvaa 40,184,240\nD1 1,16,46,2048\nMvaa 40,180,232\nAR 92,10\nAR 93,11\nMvaa 40,176,224\nAD 84,80,81\n"
"CV 64,80,81\nCV 101,92,93\nAR 92,12\nEX 82,34\nAR 93,13\nCV 102,92,93\nWV 16\nEX 83,35\nAD 84,84,82\nAR 92,14\nD1 1,16,46,3072\ns_add_u32 m0, s89, 49152\ns_barrier\nAR 93,15\n"
"AD 84,84,83\nCV 65,82,83\nRa 128,24,32768\nSv 104,1,16,76,2048\ns_add_u32 s46, s87, s88\nEX 80,36\nRa 132,24,33792\nCV 103,92,93\nEX 81,37\nRa 136,24,34816\nAR 92,16\nAR 93,17\n"
"D1 1,16,46,0\nCV 104,92,93\nRa 140,24,35840\nAR 92,18\nAD 85,80,81\nCV 68,80,81\nRa 144,26,32768\nAR 93,19\nCV 105,92,93\nEX 82,38\nRa 148,26,33792\nAR 92,20\nEX 83,39\n"
"Ra 152,26,34816\nAD 85,85,82\nAR 93,21\nD1 1,16,46,1024\nCV 106,92,93\nRa 156,26,35840\nAR 92,22\nAD 85,85,83\nCV 69,82,83\nRa 160,24,40960\nSv 108,1,16,76,3072\nAR 93,23\n"
"s_xor_b32 s89, s89, 0x10000\nEX 80,40\nRa 164,24,41984\nCV 107,92,93\nAR 92,24\nWL 6\nEX 81,41\nRa 168,24,43008\nAD 84,84,80\nAR 93,25\nD1 1,16,46,2048\nCV 108,92,93\n"
"Ra 172,24,44032\nAR 92,26\nAR 93,27\nAD 84,84,81\nCV 66,80,81\nCvaa 48,128,192,4\nRa 176,26,40960\nCV 109,92,93\nAR 92,28\nMvaa 48,132,200\nEX 82,42\nMvaa 48,136,208\n"
"Ra 180,26,41984\nAR 93,29\nMvaa 48,140,216\nWL 6\nEX 83,43\nWV 9\nv_cmp_ne_u32 vcc, s15, v27\ns_nop 0\ns_cbranch_vccnz .S41\n.S42:\nCvaa 52,140,220,8\nRa 184,26,43008\n"
"AD 84,84,82\nMvaa 52,136,212\nD1 1,16,46,3072\ns_add_u32 m0, s89, 0\nCV 110,92,93\nMvaa 52,132,204\nRa 188,26,44032\nAR 92,30\nMvaa 52,128,196\nAD 84,84,83\nCV 67,82,83\nAR 93,31\n"
"Mvaa 52,144,228\nRa 128,2,49152\ns_add_u32 s76, s76, s55\nSv 112,1,16,76,0\nMvaa 52,148,236\ns_add_u32 s46, s86, s88\nEX 80,44\nMvaa 52,152,244\nRa 132,2,50176\n"
"v_xor_b32 v24, 0x10000, v24\nCV 111,92,93\nMvaa 52,156,252\nWL 6\nEX 81,45\nMvaa 48,156,248\nRa 136,2,51200\nAD 85,85,80\nAR 92,32\nMvaa 48,152,240\nD1 1,16,46,0\nAR 93,33\n"
"CV 112,92,93\nMvaa 48,148,232\nRa 140,2,52224\nAR 92,34\nMvaa 48,144,224\nAD 85,85,81\nCV 70,80,81\nAR 93,35\nCvaa 56,160,192,12\nRa 144,2,53248\nCV 113,92,93\nAR 92,36\n"
"Mvaa 56,164,200\nEX 82,46\nMvaa 56,168,208\nRa 148,2,54272\nv_xor_b32 v26, 0x10000, v26\nAR 93,37\nMvaa 56,172,216\nWL 6\nEX 83,47\nCvaa 60,172,220,16\nRa 152,2,55296\n"
"AD 85,85,82\nCV 114,92,93\nMvaa 60,168,212\nD1 1,16,46,1024\nAR 92,38\nAR 93,39\nMvaa 60,164,204\nRa 156,2,56320\nCV 115,92,93\nMvaa 60,160,196\nAD 85,85,83\nCV 71,82,83\n"
"Mvaa 60,176,228\nRa 160,2,57344\nv_max_f32 v90, v84, v85\nSv 116,1,16,76,1024\nMvaa 60,180,236\ns_add_u32 s88, s88, 32768\nEX 80,48\nMvaa 60,184,244\nRa 164,2,58368\n"
"v_cmp_lt_f32 vcc, s63, v90\nMvaa 60,188,252\nWL 8\nEX 81,49\nMvaa 56,188,248\nRa 168,2,59392\ns_cbranch_vccnz .T23\n.T24:\nv_mov_b32 v20, v84\nv_mov_b32 v21, v85\n"
"v_mov_b32 v22, v20\nv_mov_b32 v23, v21\nMvaa 56,184,240\nD1 1,16,46,2048\nv_permlane32_swap_b32 v20, v22\nv_permlane32_swap_b32 v21, v23\nAD 20,20,22\nAD 21,21,23\n"
"v_mov_b32 v22, v20\nv_mov_b32 v23, v21\nMvaa 56,180,232\nRa 172,2,60416\nv_permlane16_swap_b32 v20, v22\nv_permlane16_swap_b32 v21, v23\nAD 20,20,22\nAD 21,21,23\n"
"v_min_f32 v90, v20, v21\nv_cmp_gt_f32 vcc, s64, v90\ns_nop 0\ns_cbranch_vccnz .T25\n.T26:\nAD 86,86,84\nMvaa 56,176,224\nAD 84,80,81\nCV 72,80,81\nAR 92,40\nZvav 128,128,64\n"
"Ra 176,2,61440\nAR 93,41\nCV 116,92,93\nZvav 132,128,68\nEX 82,50\nAR 92,42\nZvav 140,132,68\nRa 180,2,62464\nAR 93,43\nCV 117,92,93\nZvav 136,132,64\nWL 8\nEX 83,51\n"
"Zvav 144,136,64\nRa 184,2,63488\nAD 84,84,82\nAR 92,44\nZvav 148,136,68\nD1 1,16,46,3072\ns_add_u32 m0, s89, 16384\nAR 93,45\nZvav 156,140,68\nRa 188,2,64512\ns_barrier\n"
"CV 118,92,93\nZvav 152,140,64\nAD 84,84,83\nCV 73,82,83\nAR 92,46\nZvav 160,144,64\nRa 128,24,0\nSv 120,1,16,76,2048\nAR 93,47\nZvav 164,144,68\ns_add_u32 s46, s87, s88\nEX 80,52\n"
"buffer_store_dword v25, off, s[16:19], s54 offset:12 sc0 sc1\nZvav 172,148,68\nRa 132,24,1024\nv_xor_b32 v2, 0x10000, v2\nCV 119,92,93\nZvav 168,148,64\nWL 8\nEX 81,53\n"
"Zvav 176,152,64\nRa 136,24,2048\nAD 87,87,85\nZvav 180,152,68\nD1 1,16,46,0\nAR 92,48\nAR 93,49\nZvav 188,156,68\nRa 140,24,3072\nCV 120,92,93\nZvav 184,156,64\nAD 85,80,81\n"
"CV 76,80,81\nZvav 192,160,64\nRa 144,26,0\nAR 92,50\nAR 93,51\nZvav 196,160,68\nEX 82,54\nZvav 204,164,68\nRa 148,26,1024\nCV 121,92,93\nZvav 200,164,64\nWL 8\nEX 83,55\n"
"Zvav 208,168,64\nRa 152,26,2048\nAD 85,85,82\nAR 92,52\nZvav 212,168,68\nD1 1,16,46,1024\nAR 93,53\nZvav 220,172,68\nRa 156,26,3072\nCV 122,92,93\nZvav 216,172,64\nAD 85,85,83\n"
"CV 77,82,83\nAR 92,54\nZvav 224,176,64\nRa 160,24,8192\nSv 124,1,16,76,3072\nZvav 228,176,68\nEX 80,56\nZvav 236,180,68\nRa 164,24,9216\nAR 93,55\nCV 123,92,93\nZvav 232,180,64\n"
"WL 6\nEX 81,57\nZvav 240,184,64\nRa 168,24,10240\nAD 84,84,80\nAR 92,56\nZvav 244,184,68\nD1 1,16,46,2048\nAR 93,57\nZvav 252,188,68\nRa 172,24,11264\nCV 124,92,93\nAR 92,58\n"
"Zvav 248,188,64\nAD 84,84,81\nCV 74,80,81\nCvaa 32,128,192,4\nRa 176,26,8192\nAR 93,59\nCV 125,92,93\nMvaa 32,132,200\nEX 82,58\nMvaa 32,136,208\nRa 180,26,9216\nAR 92,60\n"
"Mvaa 32,140,216\nWL 6\nEX 83,59\nCvaa 36,140,220,8\nRa 184,26,10240\nAD 84,84,82\nMvaa 36,136,212\nD1 1,16,46,3072\ns_add_u32 m0, s89, 32768\nAR 93,61\nMvaa 36,132,204\n"
"Ra 188,26,11264\nCV 126,92,93\nMvaa 36,128,196\nAD 84,84,83\nCV 75,82,83\nAR 92,62\nMvaa 36,144,228\nRa 128,2,16384\nSv 96,1,16,77,0\nMvaa 36,148,236\ns_add_u32 s46, s86, s88\n"
"EX 80,60\nMvaa 36,152,244\nRa 132,2,17408\nAR 93,63\nCV 127,92,93\nMvaa 36,156,252\nWL 6\nEX 81,61\nMvaa 32,156,248\nRa 136,2,18432\nAD 85,85,80\nAR 92,64\nMvaa 32,152,240\n"
"D1 1,16,46,0\nAR 93,65\nMvaa 32,148,232\nRa 140,2,19456\nCV 96,92,93\nAR 92,66\nMvaa 32,144,224\nAD 85,85,81\nCV 78,80,81\nCvaa 40,160,192,12\nRa 144,2,20480\nAR 93,67\n"
"CV 97,92,93\nMvaa 40,164,200\nEX 82,62\nMvaa 40,168,208\nRa 148,2,21504\nAR 92,68\nAR 93,69\nMvaa 40,172,216\nWL 6\nEX 83,63\nCvaa 44,172,220,16\nRa 152,2,22528\nAD 85,85,82\n"
"CV 98,92,93\nMvaa 44,168,212\n.W546:\ns_cbranch_execz .W550\nD1 1,16,46,1024\nAR 92,70\nMvaa 44,164,204\nRa 156,2,23552\nAR 93,71\nCV 99,92,93\nMvaa 44,160,196\nAD 85,85,83\n"
"CV 79,82,83\nMvaa 44,176,228\nRa 160,2,24576\nv_max_f32 v90, v84, v85\nSv 100,1,16,77,1024\nMvaa 44,180,236\ns_add_u32 s88, s88, 32768\nEX 80,32\nMvaa 44,184,244\nRa 164,2,25600\n"
"v_cmp_lt_f32 vcc, s63, v90\nMvaa 44,188,252\nWL 8\nEX 81,33\nMvaa 40,188,248\nRa 168,2,26624\ns_cbranch_vccnz .T28\n.T29:\nAR 92,72\nMvaa 40,184,240\nD1 1,16,46,2048\nAR 93,73\n"
"Mvaa 40,180,232\nRa 172,2,27648\nAD 86,86,84\nCV 100,92,93\nMvaa 40,176,224\nAD 84,80,81\nCV 64,80,81\nMvav 128,128,72\nRa 176,2,28672\nAR 92,74\nAR 93,75\nMvav 132,128,76\n"
"EX 82,34\nMvav 140,132,76\nRa 180,2,29696\nCV 101,92,93\nAR 92,76\nMvav 136,132,72\ns_waitcnt vmcnt(18) lgkmcnt(8)\nEX 83,35\nMvav 144,136,72\nRa 184,2,30720\nAD 84,84,82\n"
"AR 93,77\nMvav 148,136,76\nD1 1,16,46,3072\ns_add_u32 m0, s89, 49152\nMvav 156,140,76\nRa 188,2,31744\ns_barrier\nCV 102,92,93\nMvav 152,140,72\nAD 84,84,83\nCV 65,82,83\n"
"Mvav 160,144,72\nRa 128,24,32768\nSv 104,1,16,77,2048\nAR 92,78\nMvav 164,144,76\ns_add_u32 s46, s87, s88\nEX 80,36\nMvav 172,148,76\nRa 132,24,33792\nAR 93,79\nCV 103,92,93\n"
"Mvav 168,148,72\nWL 8\nEX 81,37\nMvav 176,152,72\nRa 136,24,34816\nAD 87,87,85\nMvav 180,152,76\nD1 1,16,46,0\nAR 92,80\nAR 93,81\nMvav 188,156,76\nRa 140,24,35840\nCV 104,92,93\n"
"Mvav 184,156,72\nAD 85,80,81\nCV 68,80,81\nMvav 192,160,72\nRa 144,26,32768\nAR 92,82\nAR 93,83\nMvav 196,160,76\nEX 82,38\nMvav 204,164,76\nRa 148,26,33792\nCV 105,92,93\n"
"Mvav 200,164,72\nWL 8\nEX 83,39\nMvav 208,168,72\nRa 152,26,34816\nAD 85,85,82\nAR 92,84\nMvav 212,168,76\nD1 1,16,46,1024\nAR 93,85\nMvav 220,172,76\nRa 156,26,35840\n"
"CV 106,92,93\nMvav 216,172,72\nAD 85,85,83\nCV 69,82,83\nAR 92,86\nMvav 224,176,72\nRa 160,24,40960\nSv 108,1,16,77,3072\nMvav 228,176,76\ns_xor_b32 s89, s89, 0x10000\nEX 80,40\n"
"Mvav 236,180,76\nRa 164,24,41984\nAR 93,87\nCV 107,92,93\nMvav 232,180,72\nWL 6\nEX 81,41\nMvav 240,184,72\nRa 168,24,43008\nAD 84,84,80\nAR 92,88\nMvav 244,184,76\n"
"D1 1,16,46,2048\nAR 93,89\nCV 108,92,93\nMvav 252,188,76\nRa 172,24,44032\nAR 92,90\nMvav 248,188,72\nAD 84,84,81\nCV 66,80,81\nAR 93,91\nCvaa 48,128,192,4\nRa 176,26,40960\n"
"CV 109,92,93\nAR 92,92\nMvaa 48,132,200\nEX 82,42\nMvaa 48,136,208\nRa 180,26,41984\nAR 93,93\nCV 110,92,93\nMvaa 48,140,216\nWL 6\nEX 83,43\nCvaa 52,140,220,8\nRa 184,26,43008\n"
"AD 84,84,82\nv_and_b32 v27, 0x70, v1\nbuffer_load_dword v27, v27, s[16:19], s61 offen offset:140 sc0 sc1\nMvaa 52,136,212\nD1 1,16,46,3072\ns_add_u32 m0, s89, 0\nAR 92,94\n"
"Mvaa 52,132,204\nRa 188,26,44032\nAR 93,95\nMvaa 52,128,196\nAD 84,84,83\nCV 67,82,83\nCV 111,92,93\nMvaa 52,144,228\nRa 128,2,49152\ns_add_u32 s77, s77, s55\nSv 112,1,16,77,0\n"
"Mvaa 52,148,236\ns_add_u32 s46, s86, s88\nEX 80,44\nMvaa 52,152,244\nRa 132,2,50176\nv_xor_b32 v24, 0x10000, v24\nAR 92,96\nMvaa 52,156,252\nWL 6\nEX 81,45\nMvaa 48,156,248\n"
"Ra 136,2,51200\nAD 85,85,80\nAR 93,97\nMvaa 48,152,240\nD1 1,16,46,0\nCV 112,92,93\nMvaa 48,148,232\nRa 140,2,52224\nAR 92,98\nAR 93,99\nMvaa 48,144,224\nAD 85,85,81\nCV 70,80,81\n"
"Cvaa 56,160,192,12\nRa 144,2,53248\nCV 113,92,93\nAR 92,100\nMvaa 56,164,200\nEX 82,46\nMvaa 56,168,208\nRa 148,2,54272\nv_xor_b32 v26, 0x10000, v26\nAR 93,101\nMvaa 56,172,216\n"
"WL 6\nEX 83,47\nCvaa 60,172,220,16\nRa 152,2,55296\nAD 85,85,82\nCV 114,92,93\nMvaa 60,168,212\nD1 1,16,46,1024\nAR 92,102\nMvaa 60,164,204\nRa 156,2,56320\nAR 93,103\n"
"CV 115,92,93\nMvaa 60,160,196\nAD 85,85,83\nCV 71,82,83\nMvaa 60,176,228\nRa 160,2,57344\nv_max_f32 v90, v84, v85\nSv 116,1,16,77,1024\nMvaa 60,180,236\ns_add_u32 s88, s88, 32768\n"
"EX 80,48\nMvaa 60,184,244\nRa 164,2,58368\nv_cmp_lt_f32 vcc, s63, v90\nMvaa 60,188,252\nWL 8\nEX 81,49\nMvaa 56,188,248\nRa 168,2,59392\ns_cbranch_vccnz .T30\n.T31:\nAR 92,104\n"
"Mvaa 56,184,240\nD1 1,16,46,2048\nAR 93,105\nCV 116,92,93\nMvaa 56,180,232\nRa 172,2,60416\nAD 86,86,84\nMvaa 56,176,224\nAD 84,80,81\nCV 72,80,81\nAR 92,106\nMvav 128,128,64\n"
"Ra 176,2,61440\nAR 93,107\nCV 117,92,93\nMvav 132,128,68\nEX 82,50\nMvav 140,132,68\nRa 180,2,62464\nAR 92,108\nAR 93,109\nMvav 136,132,64\ns_waitcnt vmcnt(18) lgkmcnt(8)\n"
"EX 83,51\nMvav 144,136,64\nRa 184,2,63488\nAD 84,84,82\nCV 118,92,93\nMvav 148,136,68\nD1 1,16,46,3072\ns_add_u32 m0, s89, 16384\nAR 92,110\nMvav 156,140,68\nRa 188,2,64512\n"
"s_barrier\nMvav 152,140,64\nAD 84,84,83\nCV 73,82,83\nAR 93,111\nMvav 160,144,64\nRa 128,24,0\nSv 120,1,16,77,2048\nCV 119,92,93\nMvav 164,144,68\ns_add_u32 s46, s87, s88\n"
"EX 80,52\nMvav 172,148,68\nRa 132,24,1024\nv_xor_b32 v2, 0x10000, v2\nAR 92,112\nMvav 168,148,64\nWL 8\nEX 81,53\nMvav 176,152,64\nRa 136,24,2048\nAD 87,87,85\n"
"buffer_store_dword v25, off, s[16:19], s54 offset:8 sc0 sc1\nMvav 180,152,68\nD1 1,16,46,0\nAR 93,113\nCV 120,92,93\nMvav 188,156,68\nRa 140,24,3072\nAR 92,114\nMvav 184,156,64\n"
"AD 85,80,81\nCV 76,80,81\nMvav 192,160,64\nRa 144,26,0\nAR 93,115\nCV 121,92,93\nMvav 196,160,68\nEX 82,54\nMvav 204,164,68\nRa 148,26,1024\nAR 92,116\nMvav 200,164,64\nWL 8\n"
"EX 83,55\nMvav 208,168,64\nRa 152,26,2048\nAD 85,85,82\nAR 93,117\nMvav 212,168,68\nD1 1,16,46,1024\nCV 122,92,93\nMvav 220,172,68\nRa 156,26,3072\nAR 92,118\nMvav 216,172,64\n"
"AD 85,85,83\nCV 77,82,83\nAR 93,119\nMvav 224,176,64\nRa 160,24,8192\nSv 124,1,16,77,3072\nMvav 228,176,68\nEX 80,56\nMvav 236,180,68\nRa 164,24,9216\nCV 123,92,93\nAR 92,120\n"
"Mvav 232,180,64\nWL 6\nEX 81,57\nMvav 240,184,64\nRa 168,24,10240\nAD 84,84,80\nMvav 244,184,68\nD1 1,16,46,2048\nAR 93,121\nCV 124,92,93\nMvav 252,188,68\nRa 172,24,11264\n"
"AR 92,122\nMvav 248,188,64\nAD 84,84,81\nCV 74,80,81\nCvaa 32,128,192,4\nRa 176,26,8192\nAR 93,123\nCV 125,92,93\nMvaa 32,132,200\ns_add_u32 s49, s49, 131072\nEX 82,58\n"
"Mvaa 32,136,208\nRa 180,26,9216\nAR 92,124\nMvaa 32,140,216\nWL 6\nEX 83,59\nCvaa 36,140,220,8\nRa 184,26,10240\nAD 84,84,82\nAR 93,125\nMvaa 36,136,212\nD1 1,16,46,3072\n"
"s_add_u32 m0, s89, 32768\nMvaa 36,132,204\nRa 188,26,11264\nCV 126,92,93\nMvaa 36,128,196\nAD 84,84,83\nCV 75,82,83\nAR 92,126\nMvaa 36,144,228\nRa 128,2,16384\nSv 96,1,16,78,0\n"
"Mvaa 36,148,236\ns_add_u32 s46, s86, s88\nEX 80,60\nMvaa 36,152,244\nRa 132,2,17408\nAR 93,127\nCV 127,92,93\nMvaa 36,156,252\nWL 6\nEX 81,61\nMvaa 32,156,248\nRa 136,2,18432\n"
"AD 85,85,80\nMvaa 32,152,240\nD1 1,16,46,0\nMvaa 32,148,232\nRa 140,2,19456\nMvaa 32,144,224\nAD 85,85,81\nCV 78,80,81\nCvaa 40,160,192,12\nRa 144,2,20480\nMvaa 40,164,200\n"
"Sv 92,1,16,49,0\nEX 82,62\nMvaa 40,168,208\nRa 148,2,21504\nMvaa 40,172,216\nWL 6\nEX 83,63\nCvaa 44,172,220,16\nRa 152,2,22528\nAD 85,85,82\nMvaa 44,168,212\nD1 1,16,46,1024\n"
"Mvaa 44,164,204\nRa 156,2,23552\nMvaa 44,160,196\nAD 85,85,83\nCV 79,82,83\nMvaa 44,176,228\nRa 160,2,24576\nv_max_f32 v90, v84, v85\nSv 100,1,16,78,1024\nMvaa 44,180,236\n"
"s_add_u32 s88, s88, 32768\nEX 80,32\nMvaa 44,184,244\nRa 164,2,25600\nv_cmp_lt_f32 vcc, s63, v90\nMvaa 44,188,252\nWL 8\nEX 81,33\nMvaa 40,188,248\nRa 168,2,26624\n"
"s_cbranch_vccnz .T33\n.T34:\nMvaa 40,184,240\nD1 1,16,46,2048\nMvaa 40,180,232\nRa 172,2,27648\nAD 86,86,84\nMvaa 40,176,224\nAD 84,80,81\nCV 64,80,81\nMvav 128,128,72\n"
"Ra 176,2,28672\nMvav 132,128,76\nEX 82,34\nMvav 140,132,76\nRa 180,2,29696\nMvav 136,132,72\ns_waitcnt vmcnt(19) lgkmcnt(8)\nEX 83,35\nMvav 144,136,72\nRa 184,2,30720\n"
"AD 84,84,82\nMvav 148,136,76\nD1 1,16,46,3072\ns_add_u32 m0, s89, 49152\nMvav 156,140,76\nRa 188,2,31744\ns_barrier\nMvav 152,140,72\nAD 84,84,83\nCV 65,82,83\nMvav 160,144,72\n"
"Ra 128,24,32768\nSv 104,1,16,78,2048\nMvav 164,144,76\ns_add_u32 s46, s87, s88\nEX 80,36\nMvav 172,148,76\nRa 132,24,33792\nMvav 168,148,72\nWL 8\nEX 81,37\nMvav 176,152,72\n"
"Ra 136,24,34816\nAD 87,87,85\nMvav 180,152,76\nD1 1,16,46,0\nMvav 188,156,76\nRa 140,24,35840\nMvav 184,156,72\nAD 85,80,81\nCV 68,80,81\nMvav 192,160,72\nRa 144,26,32768\n"
"Mvav 196,160,76\nEX 82,38\nMvav 204,164,76\nRa 148,26,33792\nMvav 200,164,72\nWL 8\nEX 83,39\nMvav 208,168,72\nRa 152,26,34816\nAD 85,85,82\nMvav 212,168,76\nD1 1,16,46,1024\n"
"Mvav 220,172,76\nRa 156,26,35840\nMvav 216,172,72\nAD 85,85,83\nCV 69,82,83\nMvav 224,176,72\nRa 160,24,40960\nSv 108,1,16,78,3072\nMvav 228,176,76\ns_xor_b32 s89, s89, 0x10000\n"
"EX 80,40\nMvav 236,180,76\nRa 164,24,41984\nMvav 232,180,72\nWL 6\nEX 81,41\nMvav 240,184,72\nRa 168,24,43008\nAD 84,84,80\nMvav 244,184,76\nD1 1,16,46,2048\nMvav 252,188,76\n"
"Ra 172,24,44032\nMvav 248,188,72\nAD 84,84,81\nCV 66,80,81\nCvaa 48,128,192,4\nRa 176,26,40960\nMvaa 48,132,200\nEX 82,42\nMvaa 48,136,208\nRa 180,26,41984\nMvaa 48,140,216\nWL 6\n"
"EX 83,43\nv_cmp_ne_u32 vcc, s15, v27\ns_nop 0\ns_cbranch_vccnz .S44\n.S45:\nv_and_b32 v27, 0x70, v1\nbuffer_load_dword v27, v27, s[16:19], s61 offen offset:12 sc0 sc1\n"
"Cvaa 52,140,220,8\nRa 184,26,43008\nAD 84,84,82\nMvaa 52,136,212\nD1 1,16,46,3072\ns_add_u32 m0, s89, 0\nMvaa 52,132,204\nRa 188,26,44032\nMvaa 52,128,196\nAD 84,84,83\n"
"CV 67,82,83\nMvaa 52,144,228\nRa 128,2,49152\ns_add_u32 s78, s78, s55\nSv 112,1,16,78,0\nMvaa 52,148,236\ns_add_u32 s46, s86, s88\nEX 80,44\nMvaa 52,152,244\nRa 132,2,50176\n"
"v_xor_b32 v24, 0x10000, v24\nMvaa 52,156,252\nWL 6\nEX 81,45\nMvaa 48,156,248\nRa 136,2,51200\nAD 85,85,80\nMvaa 48,152,240\nD1 1,16,46,0\n.W547:\ns_cbranch_execz .W551\n"
"Mvaa 48,148,232\nRa 140,2,52224\nMvaa 48,144,224\nAD 85,85,81\nCV 70,80,81\nCvaa 56,160,192,12\nRa 144,2,53248\nMvaa 56,164,200\nSv 92,1,16,49,1024\nEX 82,46\nMvaa 56,168,208\n"
"Ra 148,2,54272\nv_xor_b32 v26, 0x10000, v26\nMvaa 56,172,216\nWL 6\nEX 83,47\nCvaa 60,172,220,16\nRa 152,2,55296\nAD 85,85,82\nMvaa 60,168,212\nD1 1,16,46,1024\nMvaa 60,164,204\n"
"Ra 156,2,56320\nMvaa 60,160,196\nAD 85,85,83\nCV 71,82,83\nMvaa 60,176,228\nRa 160,2,57344\nv_max_f32 v90, v84, v85\nSv 116,1,16,78,1024\nMvaa 60,180,236\n"
"s_add_u32 s88, s88, 32768\nEX 80,48\nMvaa 60,184,244\nRa 164,2,58368\nv_cmp_lt_f32 vcc, s63, v90\nMvaa 60,188,252\nWL 8\nEX 81,49\nMvaa 56,188,248\nRa 168,2,59392\n"
"s_cbranch_vccnz .T35\n.T36:\nMvaa 56,184,240\nD1 1,16,46,2048\nMvaa 56,180,232\nRa 172,2,60416\nAD 86,86,84\nMvaa 56,176,224\nAD 84,80,81\nCV 72,80,81\nMvav 128,128,64\n"
"Ra 176,2,61440\nMvav 132,128,68\nEX 82,50\nMvav 140,132,68\nRa 180,2,62464\nMvav 136,132,64\ns_waitcnt vmcnt(20) lgkmcnt(8)\nEX 83,51\nMvav 144,136,64\nRa 184,2,63488\n"
"AD 84,84,82\nMvav 148,136,68\nD1 1,16,46,3072\ns_add_u32 m0, s89, 16384\nMvav 156,140,68\nRa 188,2,64512\ns_barrier\nMvav 152,140,64\nAD 84,84,83\nCV 73,82,83\nMvav 160,144,64\n"
"Ra 128,24,0\nSv 120,1,16,78,2048\nMvav 164,144,68\ns_add_u32 s46, s87, s88\nEX 80,52\nMvav 172,148,68\nRa 132,24,1024\nv_xor_b32 v2, 0x10000, v2\nMvav 168,148,64\nWL 8\nEX 81,53\n"
"Mvav 176,152,64\nRa 136,24,2048\nAD 87,87,85\nMvav 180,152,68\nD1 1,16,46,0\nMvav 188,156,68\nRa 140,24,3072\nMvav 184,156,64\nAD 85,80,81\nCV 76,80,81\nMvav 192,160,64\n"
"Ra 144,26,0\nMvav 196,160,68\nEX 82,54\nMvav 204,164,68\nRa 148,26,1024\nMvav 200,164,64\nWL 8\nEX 83,55\nMvav 208,168,64\nRa 152,26,2048\nAD 85,85,82\nMvav 212,168,68\n"
"D1 1,16,46,1024\nMvav 220,172,68\nRa 156,26,3072\nMvav 216,172,64\nAD 85,85,83\nCV 77,82,83\nMvav 224,176,64\nRa 160,24,8192\nSv 124,1,16,78,3072\nMvav 228,176,68\nEX 80,56\n"
"Mvav 236,180,68\nRa 164,24,9216\nMvav 232,180,64\nWL 6\nEX 81,57\nMvav 240,184,64\nRa 168,24,10240\nAD 84,84,80\nMvav 244,184,68\nD1 1,16,46,2048\nMvav 252,188,68\n"
"Ra 172,24,11264\nMvav 248,188,64\nAD 84,84,81\nCV 74,80,81\ns_mov_b32 s99, 11\ns_mov_b32 s90, 0\n.p2align 6\n.A60:\nCvaa 32,128,192,4\nRa 176,26,8192\nMvaa 32,132,200\n"
"s_add_u32 s49, s49, 131072\nEX 82,58\nMvaa 32,136,208\nRa 180,26,9216\nMvaa 32,140,216\nWL 6\nEX 83,59\nCvaa 36,140,220,8\nRa 184,26,10240\nAD 84,84,82\nMvaa 36,136,212\n"
"D1 1,16,46,3072\ns_add_u32 m0, s89, 32768\nMvaa 36,132,204\nRa 188,26,11264\nMvaa 36,128,196\nAD 84,84,83\nCV 75,82,83\nMvaa 36,144,228\nRa 128,2,16384\nMvaa 36,148,236\n"
"s_add_u32 s46, s86, s88\nEX 80,60\nMvaa 36,152,244\nRa 132,2,17408\nMvaa 36,156,252\nWL 6\nEX 81,61\nMvaa 32,156,248\nRa 136,2,18432\nAD 85,85,80\nMvaa 32,152,240\nD1 1,16,46,0\n"
"Mvaa 32,148,232\nRa 140,2,19456\nMvaa 32,144,224\nAD 85,85,81\nCV 78,80,81\nCvaa 40,160,192,12\nRa 144,2,20480\nMvaa 40,164,200\nSv 92,1,16,49,0\nEX 82,62\nMvaa 40,168,208\n"
"Ra 148,2,21504\nMvaa 40,172,216\nWL 6\nEX 83,63\nCvaa 44,172,220,16\nRa 152,2,22528\nAD 85,85,82\nMvaa 44,168,212\nD1 1,16,46,1024\nMvaa 44,164,204\nRa 156,2,23552\n"
"Mvaa 44,160,196\nAD 85,85,83\nCV 79,82,83\nMvaa 44,176,228\nRa 160,2,24576\nv_max_f32 v90, v84, v85\nMvaa 44,180,236\ns_add_u32 s88, s88, 32768\nEX 80,32\nMvaa 44,184,244\n"
"Ra 164,2,25600\nv_cmp_lt_f32 vcc, s63, v90\nMvaa 44,188,252\nWL 8\nEX 81,33\nMvaa 40,188,248\nRa 168,2,26624\ns_cbranch_vccnz .T64\n.T65:\nMvaa 40,184,240\nD1 1,16,46,2048\n"
"Mvaa 40,180,232\nRa 172,2,27648\nAD 86,86,84\nMvaa 40,176,224\nAD 84,80,81\nCV 64,80,81\nMvav 128,128,72\nRa 176,2,28672\nMvav 132,128,76\nEX 82,34\nMvav 140,132,76\n"
"Ra 180,2,29696\nMvav 136,132,72\ns_waitcnt vmcnt(13) lgkmcnt(8)\nEX 83,35\nMvav 144,136,72\nRa 184,2,30720\nAD 84,84,82\nMvav 148,136,76\nD1 1,16,46,3072\n"
"s_add_u32 m0, s89, 49152\nMvav 156,140,76\nRa 188,2,31744\ns_barrier\nMvav 152,140,72\nAD 84,84,83\nCV 65,82,83\nMvav 160,144,72\nRa 128,24,32768\nMvav 164,144,76\n"
"s_add_u32 s46, s87, s88\nEX 80,36\nMvav 172,148,76\nRa 132,24,33792\nMvav 168,148,72\nWL 8\nEX 81,37\nMvav 176,152,72\nRa 136,24,34816\nAD 87,87,85\nMvav 180,152,76\nD1 1,16,46,0\n"
"Mvav 188,156,76\nRa 140,24,35840\nMvav 184,156,72\nAD 85,80,81\nCV 68,80,81\nMvav 192,160,72\nRa 144,26,32768\nMvav 196,160,76\nEX 82,38\nMvav 204,164,76\nRa 148,26,33792\n"
"Mvav 200,164,72\nWL 8\nEX 83,39\nMvav 208,168,72\nRa 152,26,34816\nAD 85,85,82\nMvav 212,168,76\nD1 1,16,46,1024\nMvav 220,172,76\nRa 156,26,35840\nMvav 216,172,72\nAD 85,85,83\n"
"CV 69,82,83\nMvav 224,176,72\nRa 160,24,40960\nMvav 228,176,76\ns_xor_b32 s89, s89, 0x10000\nEX 80,40\nMvav 236,180,76\nRa 164,24,41984\nMvav 232,180,72\nWL 6\nEX 81,41\n"
"Mvav 240,184,72\nRa 168,24,43008\nAD 84,84,80\nMvav 244,184,76\nD1 1,16,46,2048\nMvav 252,188,76\nRa 172,24,44032\nMvav 248,188,72\nAD 84,84,81\nCV 66,80,81\nCvaa 48,128,192,4\n"
"Ra 176,26,40960\nMvaa 48,132,200\nEX 82,42\nMvaa 48,136,208\nRa 180,26,41984\nMvaa 48,140,216\nWL 6\nEX 83,43\nCvaa 52,140,220,8\nRa 184,26,43008\nAD 84,84,82\nMvaa 52,136,212\n"
"D1 1,16,46,3072\ns_add_u32 m0, s89, 0\nMvaa 52,132,204\nRa 188,26,44032\nMvaa 52,128,196\nAD 84,84,83\nCV 67,82,83\nMvaa 52,144,228\nRa 128,2,49152\nMvaa 52,148,236\n"
"s_add_u32 s46, s86, s88\nEX 80,44\nMvaa 52,152,244\nRa 132,2,50176\nv_xor_b32 v24, 0x10000, v24\nMvaa 52,156,252\nWL 6\nEX 81,45\nMvaa 48,156,248\nRa 136,2,51200\nAD 85,85,80\n"
"Mvaa 48,152,240\nD1 1,16,46,0\nMvaa 48,148,232\nRa 140,2,52224\nMvaa 48,144,224\nAD 85,85,81\nCV 70,80,81\nCvaa 56,160,192,12\nRa 144,2,53248\nMvaa 56,164,200\nSv 92,1,16,49,1024\n"
"EX 82,46\nMvaa 56,168,208\nRa 148,2,54272\nv_xor_b32 v26, 0x10000, v26\nMvaa 56,172,216\nWL 6\nEX 83,47\nCvaa 60,172,220,16\nRa 152,2,55296\nAD 85,85,82\nMvaa 60,168,212\n"
"D1 1,16,46,1024\nMvaa 60,164,204\nRa 156,2,56320\nMvaa 60,160,196\nAD 85,85,83\nCV 71,82,83\nMvaa 60,176,228\nRa 160,2,57344\nv_max_f32 v90, v84, v85\nMvaa 60,180,236\n"
"s_add_u32 s88, s88, 32768\nEX 80,48\nMvaa 60,184,244\nRa 164,2,58368\nv_cmp_lt_f32 vcc, s63, v90\nMvaa 60,188,252\nWL 8\nEX 81,49\nMvaa 56,188,248\nRa 168,2,59392\n"
"s_cbranch_vccnz .T66\n.T67:\nMvaa 56,184,240\nD1 1,16,46,2048\nMvaa 56,180,232\nRa 172,2,60416\nAD 86,86,84\nMvaa 56,176,224\nAD 84,80,81\nCV 72,80,81\nMvav 128,128,64\n"
"Ra 176,2,61440\nMvav 132,128,68\nEX 82,50\nMvav 140,132,68\nRa 180,2,62464\nMvav 136,132,64\ns_waitcnt vmcnt(13) lgkmcnt(8)\nEX 83,51\nMvav 144,136,64\nRa 184,2,63488\n"
"AD 84,84,82\nMvav 148,136,68\nD1 1,16,46,3072\ns_add_u32 m0, s89, 16384\nMvav 156,140,68\nRa 188,2,64512\ns_barrier\nMvav 152,140,64\nAD 84,84,83\nCV 73,82,83\nMvav 160,144,64\n"
"Ra 128,24,0\nMvav 164,144,68\ns_add_u32 s46, s87, s88\nEX 80,52\nMvav 172,148,68\nRa 132,24,1024\nv_xor_b32 v2, 0x10000, v2\nMvav 168,148,64\nWL 8\nEX 81,53\nMvav 176,152,64\n"
"Ra 136,24,2048\nAD 87,87,85\nMvav 180,152,68\nD1 1,16,46,0\nMvav 188,156,68\nRa 140,24,3072\nMvav 184,156,64\nAD 85,80,81\nCV 76,80,81\nMvav 192,160,64\nRa 144,26,0\n"
"Mvav 196,160,68\nEX 82,54\nMvav 204,164,68\nRa 148,26,1024\nMvav 200,164,64\nWL 8\nEX 83,55\nMvav 208,168,64\nRa 152,26,2048\nAD 85,85,82\nMvav 212,168,68\nD1 1,16,46,1024\n"
"Mvav 220,172,68\nRa 156,26,3072\nMvav 216,172,64\nAD 85,85,83\nCV 77,82,83\nMvav 224,176,64\nRa 160,24,8192\nMvav 228,176,68\ns_sub_u32 s90, s90, 1\nEX 80,56\nMvav 236,180,68\n"
"Ra 164,24,9216\ns_cbranch_scc1 .E62\n.E63:\nMvav 232,180,64\nWL 6\nEX 81,57\nMvav 240,184,64\nRa 168,24,10240\nAD 84,84,80\nMvav 244,184,68\nD1 1,16,46,2048\n"
"s_sub_u32 s99, s99, 1\nMvav 252,188,68\nRa 172,24,11264\nMvav 248,188,64\nAD 84,84,81\nCV 74,80,81\ns_cbranch_scc0 .A60\ns_branch .A68\n.E62:\ns_swappc_b64 s[80:81], s[8:9]\n"
"s_branch .E63\n.HD13:\nbuffer_store_dword v25, off, s[16:19], s54 offset:4 sc0 sc1\nv_cmp_ne_u32 vcc, s15, v27\ns_nop 0\ns_cbranch_vccnz .S47\n.S48:\nv_and_b32 v27, 0x70, v1\n"
"buffer_load_dword v27, v27, s[16:19], s61 offen offset:8 sc0 sc1\ns_mov_b32 s90, 1\ns_swappc_b64 s[8:9], s[80:81]\nv_cmp_ne_u32 vcc, s15, v27\ns_nop 0\ns_cbranch_vccnz .S50\n"
".S51:\nv_and_b32 v27, 0xf0, v1\nbuffer_load_dword v27, v27, s[16:19], s61 offen offset:4 sc0 sc1\ns_mov_b32 s90, 1\ns_swappc_b64 s[8:9], s[80:81]\nv_cmp_ne_u32 vcc, s15, v27\n"
"s_nop 0\ns_cbranch_vccnz .S53\n.S54:\ns_mov_b32 s90, s91\ns_swappc_b64 s[8:9], s[80:81]\ns_branch .FF56\n.A68:\nCvaa 32,128,192,4\nRa 176,26,8192\nMvaa 32,132,200\n"
"s_add_u32 s49, s49, 131072\nEX 82,58\nMvaa 32,136,208\nRa 180,26,9216\nMvaa 32,140,216\nWL 6\nEX 83,59\nCvaa 36,140,220,8\nRa 184,26,10240\nAD 84,84,82\nMvaa 36,136,212\n"
"D1 1,16,46,3072\ns_add_u32 m0, s89, 32768\nMvaa 36,132,204\nRa 188,26,11264\nMvaa 36,128,196\nAD 84,84,83\nCV 75,82,83\ns_mov_b32 s94, s96\ns_add_u32 s95, s96, 65536\n"
"v_and_b32 v108, 0xf0, v1\nMvaa 36,144,228\nRa 128,2,16384\nMvaa 36,148,236\ns_add_u32 s46, s86, s88\nEX 80,60\nMvaa 36,152,244\nRa 132,2,17408\nMvaa 36,156,252\nWL 6\nEX 81,61\n"
"Mvaa 32,156,248\nRa 136,2,18432\nAD 85,85,80\nMvaa 32,152,240\nD1 1,16,46,0\nMvaa 32,148,232\nRa 140,2,19456\nMvaa 32,144,224\nAD 85,85,81\nCV 78,80,81\nCvaa 40,160,192,12\n"
"Ra 144,2,20480\nMvaa 40,164,200\nSv 92,1,16,49,0\nEX 82,62\nMvaa 40,168,208\nRa 148,2,21504\nMvaa 40,172,216\nWL 6\nEX 83,63\nCvaa 44,172,220,16\nRa 152,2,22528\nAD 85,85,82\n"
"Mvaa 44,168,212\nD1 1,16,46,1024\nMvaa 44,164,204\nRa 156,2,23552\nMvaa 44,160,196\nAD 85,85,83\nCV 79,82,83\nMvaa 44,176,228\nRa 160,2,24576\nv_max_f32 v90, v84, v85\n"
"Mvaa 44,180,236\ns_add_u32 s88, s88, 32768\nEX 80,32\nMvaa 44,184,244\nRa 164,2,25600\nv_cmp_lt_f32 vcc, s63, v90\nMvaa 44,188,252\nWL 8\nEX 81,33\nMvaa 40,188,248\n"
"Ra 168,2,26624\ns_cbranch_vccnz .T70\n.T71:\nMvaa 40,184,240\nD1 1,16,46,2048\nMvaa 40,180,232\nRa 172,2,27648\nAD 86,86,84\nMvaa 40,176,224\nAD 84,80,81\nCV 64,80,81\n"
"Mvav 128,128,72\nRa 176,2,28672\nv_lshrrev_b32 v92, 8, v1\nMvav 132,128,76\nEX 82,34\nMvav 140,132,76\nRa 180,2,29696\nMvav 136,132,72\ns_waitcnt vmcnt(13) lgkmcnt(8)\nEX 83,35\n"
"Mvav 144,136,72\nRa 184,2,30720\nAD 84,84,82\nMvav 148,136,76\nD1 1,16,46,3072\ns_add_u32 m0, s89, 49152\nMvav 156,140,76\nRa 188,2,31744\ns_barrier\nMvav 152,140,72\nAD 84,84,83\n"
"CV 65,82,83\n.W548:\ns_cbranch_execz .WB21\nMvav 160,144,72\nRa 128,24,32768\nv_bfe_u32 v93, v1, 6, 2\nMvav 164,144,76\ns_add_u32 s46, s87, s88\nEX 80,36\nMvav 172,148,76\n"
"Ra 132,24,33792\nv_cmp_eq_u32 vcc, v92, v93\nv_and_b32 v92, 16, v1\nv_mov_b32 v93, 0x3f80\nv_cndmask_b32 v93, 0, v93, vcc\nMvav 168,148,72\nWL 8\nEX 81,37\nMvav 176,152,72\n"
"Ra 136,24,34816\nAD 87,87,85\nMvav 180,152,76\nD1 1,16,46,0\nMvav 188,156,76\nRa 140,24,35840\nv_lshlrev_b32 v93, v92, v93\nMvav 184,156,72\nAD 85,80,81\nCV 68,80,81\n"
"Mvav 192,160,72\nRa 144,26,32768\nv_and_b32 v92, 32, v1\nMvav 196,160,76\nEX 82,38\nMvav 204,164,76\nRa 148,26,33792\nv_cmp_eq_u32 vcc, 0, v92\nv_mov_b32 v112, 0\n"
"v_mov_b32 v113, 0\nv_cndmask_b32 v114, 0, v93, vcc\nMvav 200,164,72\nWL 8\nEX 83,39\nMvav 208,168,72\nRa 152,26,34816\nAD 85,85,82\nMvav 212,168,76\nD1 1,16,46,1024\n"
"Mvav 220,172,76\nRa 156,26,35840\nv_sub_u32 v115, v93, v114\nMvav 216,172,72\nAD 85,85,83\nCV 69,82,83\nMvav 224,176,72\nRa 160,24,40960\nv_mov_b32 v116, 0\nMvav 228,176,76\n"
"s_xor_b32 s89, s89, 0x10000\nEX 80,40\nMvav 236,180,76\nRa 164,24,41984\nMvav 232,180,72\nWL 6\nEX 81,41\nMvav 240,184,72\nRa 168,24,43008\nAD 84,84,80\nMvav 244,184,76\n"
"D1 1,16,46,2048\nMvav 252,188,76\nRa 172,24,44032\nv_mov_b32 v117, 0\nMvav 248,188,72\nAD 84,84,81\nCV 66,80,81\nCvaa 48,128,192,4\nRa 176,26,40960\nMvaa 48,132,200\n"
"s_mov_b32 s86, s94\nEX 82,42\nMvaa 48,136,208\nRa 180,26,41984\nMvaa 48,140,216\nWL 6\nEX 83,43\nCvaa 52,140,220,8\nRa 184,26,43008\nAD 84,84,82\nMvaa 52,136,212\nD1 1,16,46,3072\n"
"s_add_u32 m0, s89, 0\nMvaa 52,132,204\nRa 188,26,44032\nMvaa 52,128,196\nAD 84,84,83\nCV 67,82,83\nMvaa 52,144,228\nRa 128,2,49152\nMvaa 52,148,236\ns_add_u32 s46, s86, s88\n"
"EX 80,44\nMvaa 52,152,244\nRa 132,2,50176\nv_xor_b32 v24, 0x10000, v24\nMvaa 52,156,252\nWL 6\nEX 81,45\nMvaa 48,156,248\nRa 136,2,51200\nAD 85,85,80\nMvaa 48,152,240\n"
"D1 1,16,46,0\nMvaa 48,148,232\nRa 140,2,52224\nMvaa 48,144,224\nAD 85,85,81\nCV 70,80,81\nCvaa 56,160,192,12\nRa 144,2,53248\nMvaa 56,164,200\nSv 92,1,16,49,1024\nEX 82,46\n"
"Mvaa 56,168,208\nRa 148,2,54272\nv_xor_b32 v26, 0x10000, v26\nMvaa 56,172,216\nWL 6\nEX 83,47\nCvaa 60,172,220,16\nRa 152,2,55296\nAD 85,85,82\nMvaa 60,168,212\nD1 1,16,46,1024\n"
"Mvaa 60,164,204\nRa 156,2,56320\nMvaa 60,160,196\nAD 85,85,83\nCV 71,82,83\nMvaa 60,176,228\nRa 160,2,57344\nv_max_f32 v90, v84, v85\nMvaa 60,180,236\ns_add_u32 s88, s88, 32768\n"
"EX 80,48\nMvaa 60,184,244\nRa 164,2,58368\nv_cmp_lt_f32 vcc, s63, v90\nMvaa 60,188,252\nWL 8\nEX 81,49\nMvaa 56,188,248\nRa 168,2,59392\ns_cbranch_vccnz .T72\n.T73:\n"
"Mvaa 56,184,240\nD1 1,16,46,2048\nMvaa 56,180,232\nRa 172,2,60416\nAD 86,86,84\nMvaa 56,176,224\nAD 84,80,81\nCV 72,80,81\nMvav 128,128,64\nRa 176,2,61440\nMvav 132,128,68\n"
"EX 82,50\nMvav 140,132,68\nRa 180,2,62464\nMvav 136,132,64\ns_waitcnt vmcnt(13) lgkmcnt(8)\nEX 83,51\nMvav 144,136,64\nRa 184,2,63488\nAD 84,84,82\nMvav 148,136,68\n"
"D1 1,16,46,3072\ns_add_u32 m0, s89, 16384\nMvav 156,140,68\nRa 188,2,64512\ns_barrier\nMvav 152,140,64\nAD 84,84,83\nCV 73,82,83\nMvav 160,144,64\nRa 128,24,0\nMvav 164,144,68\n"
"s_add_u32 s46, s87, s88\nEX 80,52\nMvav 172,148,68\nRa 132,24,1024\nv_xor_b32 v2, 0x10000, v2\nMvav 168,148,64\nWL 8\nEX 81,53\nMvav 176,152,64\nRa 136,24,2048\nAD 87,87,85\n"
"Mvav 180,152,68\nD1 1,16,46,0\nMvav 188,156,68\nRa 140,24,3072\nMvav 184,156,64\nAD 85,80,81\nCV 76,80,81\nMvav 192,160,64\nRa 144,26,0\nMvav 196,160,68\nEX 82,54\n"
"Mvav 204,164,68\nRa 148,26,1024\nMvav 200,164,64\nWL 8\nEX 83,55\nMvav 208,168,64\nRa 152,26,2048\nAD 85,85,82\nMvav 212,168,68\nD1 1,16,46,1024\nMvav 220,172,68\nRa 156,26,3072\n"
"Mvav 216,172,64\nAD 85,85,83\nCV 77,82,83\nMvav 224,176,64\nRa 160,24,8192\nMvav 228,176,68\nEX 80,56\nMvav 236,180,68\nRa 164,24,9216\nMvav 232,180,64\nWL 6\nEX 81,57\n"
"Mvav 240,184,64\nRa 168,24,10240\nAD 84,84,80\nMvav 244,184,68\nD1 1,16,46,2048\nMvav 252,188,68\nRa 172,24,11264\nMvav 248,188,64\nAD 84,84,81\nCV 74,80,81\nCvaa 32,128,192,4\n"
"Ra 176,26,8192\nMvaa 32,132,200\nEX 82,58\nMvaa 32,136,208\nRa 180,26,9216\nMvaa 32,140,216\nWL 6\nEX 83,59\nCvaa 36,140,220,8\nRa 184,26,10240\nAD 84,84,82\nMvaa 36,136,212\n"
"D1 1,16,46,3072\ns_add_u32 m0, s89, 32768\nMvaa 36,132,204\nRa 188,26,11264\nMvaa 36,128,196\nAD 84,84,83\nCV 75,82,83\nMvaa 36,144,228\nRa 128,2,16384\nMvaa 36,148,236\n"
"s_add_u32 s46, s86, s88\nEX 80,60\nMvaa 36,152,244\nRa 132,2,17408\nMvaa 36,156,252\nWL 6\nEX 81,61\nMvaa 32,156,248\nRa 136,2,18432\nAD 85,85,80\nMvaa 32,152,240\nD1 1,16,46,0\n"
"Mvaa 32,148,232\nRa 140,2,19456\nMvaa 32,144,224\nAD 85,85,81\nCV 78,80,81\nCvaa 40,160,192,12\nRa 144,2,20480\nMvaa 40,164,200\ns_mov_b32 s87, s95\nEX 82,62\nMvaa 40,168,208\n"
"Ra 148,2,21504\nMvaa 40,172,216\nWL 6\nEX 83,63\nCvaa 44,172,220,16\nRa 152,2,22528\nAD 85,85,82\nMvaa 44,168,212\nD1 1,16,46,1024\nMvaa 44,164,204\nRa 156,2,23552\n"
"Mvaa 44,160,196\nAD 85,85,83\nCV 79,82,83\nMvaa 44,176,228\nRa 160,2,24576\nv_max_f32 v90, v84, v85\nMvaa 44,180,236\ns_add_u32 s88, s88, 32768\nEX 80,32\nMvaa 44,184,244\n"
"Ra 164,2,25600\nv_cmp_lt_f32 vcc, s63, v90\nMvaa 44,188,252\nWL 8\nEX 81,33\nMvaa 40,188,248\nRa 168,2,26624\ns_cbranch_vccnz .T75\n.T76:\nMvaa 40,184,240\nD1 1,16,46,2048\n"
"Mvaa 40,180,232\nRa 172,2,27648\nAD 86,86,84\nMvaa 40,176,224\nAD 84,80,81\nCV 64,80,81\nMvav 128,128,72\nRa 176,2,28672\nMvav 132,128,76\nEX 82,34\nMvav 140,132,76\n"
"Ra 180,2,29696\nMvav 136,132,72\ns_waitcnt vmcnt(12) lgkmcnt(8)\nEX 83,35\nMvav 144,136,72\nRa 184,2,30720\nAD 84,84,82\nMvav 148,136,76\nD1 1,16,46,3072\n"
"s_add_u32 m0, s89, 49152\nMvav 156,140,76\nRa 188,2,31744\ns_barrier\nMvav 152,140,72\nAD 84,84,83\nCV 65,82,83\nMvav 160,144,72\nRa 128,24,32768\nMvav 164,144,76\n"
"s_add_u32 s46, s87, s88\nEX 80,36\nMvav 172,148,76\nRa 132,24,33792\nMvav 168,148,72\nWL 8\nEX 81,37\nMvav 176,152,72\nRa 136,24,34816\nAD 87,87,85\nMvav 180,152,76\nD1 1,16,46,0\n"
"Mvav 188,156,76\nRa 140,24,35840\nMvav 184,156,72\nAD 85,80,81\nCV 68,80,81\nMvav 192,160,72\nRa 144,26,32768\n"
"buffer_load_dwordx4 v[100:103], v108, s[16:19], s65 offen offset:0 sc0 sc1\nbuffer_load_dwordx4 v[104:107], v108, s[16:19], s65 offen offset:256 sc0 sc1\nMvav 196,160,76\n"
"EX 82,38\nMvav 204,164,76\nRa 148,26,33792\nMvav 200,164,72\nWL 8\nEX 83,39\nMvav 208,168,72\nRa 152,26,34816\nAD 85,85,82\nMvav 212,168,76\nD1 1,16,46,1024\nMvav 220,172,76\n"
"Ra 156,26,35840\nMvav 216,172,72\nAD 85,85,83\nCV 69,82,83\nMvav 224,176,72\nRa 160,24,40960\nMvav 228,176,76\ns_xor_b32 s89, s89, 0x10000\nEX 80,40\nMvav 236,180,76\n"
"Ra 164,24,41984\nMvav 232,180,72\nWL 6\nEX 81,41\nMvav 240,184,72\nRa 168,24,43008\nAD 84,84,80\nMvav 244,184,76\nD1 1,16,46,2048\nMvav 252,188,76\nRa 172,24,44032\n"
"Mvav 248,188,72\nAD 84,84,81\nCV 66,80,81\nCvaa 48,128,192,4\nRa 176,26,40960\nMvaa 48,132,200\nEX 82,42\nMvaa 48,136,208\nRa 180,26,41984\nMvaa 48,140,216\nWL 6\nEX 83,43\n"
"Cvaa 52,140,220,8\nRa 184,26,43008\nAD 84,84,82\nMvaa 52,136,212\nD1 1,16,46,3072\ns_add_u32 m0, s89, 0\nMvaa 52,132,204\nRa 188,26,44032\nMvaa 52,128,196\nAD 84,84,83\n"
"CV 67,82,83\nMvaa 52,144,228\nRa 128,2,49152\nWV 3\nv_xor_b32 v109, s15, v102\nv_xor_b32 v110, s15, v106\nv_or_b32 v109, v109, v110\nv_cmp_ne_u32 vcc, 0, v109\ns_nop 3\n"
"s_cmp_lg_u64 vcc, 0\ns_cselect_b32 s67, 1, 0\ns_cselect_b32 s6, 0x40000000, s66\nMvaa 52,148,236\ns_add_u32 s46, s86, s88\nEX 80,44\ns_add_u32 s5, s6, 0\nMvaa 52,152,244\n"
"Ra 132,2,50176\nv_xor_b32 v24, 0x10000, v24\nMvaa 52,156,252\nWL 6\nEX 81,45\nbuffer_load_dwordx4 a[0:3], v1, s[16:19], s5 offen offset:0 sc1\nMvaa 48,156,248\nRa 136,2,51200\n"
"AD 85,85,80\nMvaa 48,152,240\nD1 1,16,46,0\nMvaa 48,148,232\nRa 140,2,52224\nbuffer_load_dwordx4 a[4:7], v1, s[16:19], s5 offen offset:1024 sc1\nMvaa 48,144,224\nAD 85,85,81\n"
"CV 70,80,81\nCvaa 56,160,192,12\nRa 144,2,53248\nMvaa 56,164,200\nEX 82,46\nbuffer_load_dwordx4 a[8:11], v1, s[16:19], s5 offen offset:2048 sc1\nMvaa 56,168,208\nRa 148,2,54272\n"
"v_xor_b32 v26, 0x10000, v26\nMvaa 56,172,216\nWL 6\nEX 83,47\nCvaa 60,172,220,16\nRa 152,2,55296\nAD 85,85,82\n"
"buffer_load_dwordx4 a[12:15], v1, s[16:19], s5 offen offset:3072 sc1\nMvaa 60,168,212\nD1 1,16,46,1024\ns_add_u32 s5, s6, 4096\nMvaa 60,164,204\nRa 156,2,56320\nMvaa 60,160,196\n"
"AD 85,85,83\nCV 71,82,83\nbuffer_load_dwordx4 a[16:19], v1, s[16:19], s5 offen offset:0 sc1\nMvaa 60,176,228\nRa 160,2,57344\nv_max_f32 v90, v84, v85\nMvaa 60,180,236\n"
"s_add_u32 s88, s88, 32768\nEX 80,48\nMvaa 60,184,244\nRa 164,2,58368\nv_cmp_lt_f32 vcc, s63, v90\nMvaa 60,188,252\nWL 8\nEX 81,49\nMvaa 56,188,248\nRa 168,2,59392\n"
"s_cbranch_vccnz .T77\n.T78:\nbuffer_load_dwordx4 a[20:23], v1, s[16:19], s5 offen offset:1024 sc1\nMvaa 56,184,240\nD1 1,16,46,2048\nMvaa 56,180,232\nRa 172,2,60416\nAD 86,86,84\n"
"Mvaa 56,176,224\nAD 84,80,81\nCV 72,80,81\nbuffer_load_dwordx4 a[24:27], v1, s[16:19], s5 offen offset:2048 sc1\nMvav 128,128,64\nRa 176,2,61440\nMvav 132,128,68\nEX 82,50\n"
"Mvav 140,132,68\nRa 180,2,62464\nbuffer_load_dwordx4 a[28:31], v1, s[16:19], s5 offen offset:3072 sc1\nMvav 136,132,64\nWL 8\nEX 83,51\ns_add_u32 s5, s6, 16384\nMvav 144,136,64\n"
"Ra 184,2,63488\nAD 84,84,82\nMvav 148,136,68\nD1 1,16,46,3072\ns_add_u32 m0, s89, 16384\nbuffer_load_dwordx4 a[32:35], v1, s[16:19], s5 offen offset:0 sc1\nMvav 156,140,68\n"
"Ra 188,2,64512\ns_barrier\nMvav 152,140,64\nAD 84,84,83\nCV 73,82,83\nMvav 160,144,64\nRa 128,24,0\nbuffer_load_dwordx4 a[36:39], v1, s[16:19], s5 offen offset:1024 sc1\n"
"Mvav 164,144,68\ns_add_u32 s46, s87, s88\nEX 80,52\nMvav 172,148,68\nRa 132,24,1024\nv_xor_b32 v2, 0x10000, v2\nMvav 168,148,64\nWL 8\nEX 81,53\n"
"buffer_load_dwordx4 a[40:43], v1, s[16:19], s5 offen offset:2048 sc1\nMvav 176,152,64\nRa 136,24,2048\nAD 87,87,85\nMvav 180,152,68\nD1 1,16,46,0\nMvav 188,156,68\nRa 140,24,3072\n"
"buffer_load_dwordx4 a[44:47], v1, s[16:19], s5 offen offset:3072 sc1\nMvav 184,156,64\nAD 85,80,81\nCV 76,80,81\ns_add_u32 s5, s6, 20480\nMvav 192,160,64\nRa 144,26,0\n"
"Mvav 196,160,68\nEX 82,54\nbuffer_load_dwordx4 a[48:51], v1, s[16:19], s5 offen offset:0 sc1\nMvav 204,164,68\nRa 148,26,1024\nMvav 200,164,64\nWL 8\nEX 83,55\nMvav 208,168,64\n"
"Ra 152,26,2048\nAD 85,85,82\nbuffer_load_dwordx4 a[52:55], v1, s[16:19], s5 offen offset:1024 sc1\nMvav 212,168,68\nD1 1,16,46,1024\nMvav 220,172,68\nRa 156,26,3072\n"
"Mvav 216,172,64\nAD 85,85,83\nCV 77,82,83\nbuffer_load_dwordx4 a[56:59], v1, s[16:19], s5 offen offset:2048 sc1\nMvav 224,176,64\nRa 160,24,8192\nMvav 228,176,68\nEX 80,56\n"
"Mvav 236,180,68\nRa 164,24,9216\nbuffer_load_dwordx4 a[60:63], v1, s[16:19], s5 offen offset:3072 sc1\nMvav 232,180,64\nWL 10\nEX 81,57\nMvav 240,184,64\nRa 168,24,10240\n"
"AD 84,84,80\nMvav 244,184,68\nD1 1,16,46,2048\nMvav 252,188,68\nRa 172,24,11264\nMvav 248,188,64\nAD 84,84,81\nCV 74,80,81\nWV 20\ns_cmp_lg_u32 s67, 0\ns_cbranch_scc0 .E86\n"
"v_xor_b32 v109, s15, v102\nv_xor_b32 v110, s15, v106\nv_or_b32 v109, v109, v110\nv_cmp_ne_u32 vcc, 0, v109\ns_nop 0\ns_cbranch_vccnz .S83\n.S84:\n.W549:\ns_cbranch_execz .WB21\n"
"s_add_u32 s5, s66, 0\nbuffer_load_dwordx4 a[0:3], v1, s[16:19], s5 offen offset:0 sc1\nbuffer_load_dwordx4 a[4:7], v1, s[16:19], s5 offen offset:1024 sc1\n"
"buffer_load_dwordx4 a[8:11], v1, s[16:19], s5 offen offset:2048 sc1\nbuffer_load_dwordx4 a[12:15], v1, s[16:19], s5 offen offset:3072 sc1\ns_add_u32 s5, s66, 4096\n"
"buffer_load_dwordx4 a[16:19], v1, s[16:19], s5 offen offset:0 sc1\nbuffer_load_dwordx4 a[20:23], v1, s[16:19], s5 offen offset:1024 sc1\n"
"buffer_load_dwordx4 a[24:27], v1, s[16:19], s5 offen offset:2048 sc1\nbuffer_load_dwordx4 a[28:31], v1, s[16:19], s5 offen offset:3072 sc1\ns_add_u32 s5, s66, 16384\n"
"buffer_load_dwordx4 a[32:35], v1, s[16:19], s5 offen offset:0 sc1\nbuffer_load_dwordx4 a[36:39], v1, s[16:19], s5 offen offset:1024 sc1\n"
"buffer_load_dwordx4 a[40:43], v1, s[16:19], s5 offen offset:2048 sc1\nbuffer_load_dwordx4 a[44:47], v1, s[16:19], s5 offen offset:3072 sc1\ns_add_u32 s5, s66, 20480\n"
"buffer_load_dwordx4 a[48:51], v1, s[16:19], s5 offen offset:0 sc1\nbuffer_load_dwordx4 a[52:55], v1, s[16:19], s5 offen offset:1024 sc1\n"
"buffer_load_dwordx4 a[56:59], v1, s[16:19], s5 offen offset:2048 sc1\nbuffer_load_dwordx4 a[60:63], v1, s[16:19], s5 offen offset:3072 sc1\nWV 0\ns_mov_b32 s67, 0\n.E86:\n"
"v_cmp_lt_f32 vcc, 0, v101\nSU 20,100,88\nv_mul_f32 v101, 0x3e800000, v101\nv_cndmask_b32 v20, 0, v20, vcc\nv_cmp_lt_f32 vcc, 0, v105\nSU 21,104,89\n"
"v_mul_f32 v105, 0x3e800000, v105\nv_cndmask_b32 v21, 0, v21, vcc\nv_or_b32 v91, v20, v21\nv_cmp_ne_u32 vcc, 0, v91\ns_nop 0\ns_cbranch_vccnz .D81\nAD 86,86,101\nAD 87,87,105\n"
".D82:\nMvva 128,114,0\nMvva 136,112,0\nEX 82,58\nMvva 140,112,4\nMvva 132,114,4\nWV 15\nEX 83,59\nMvva 144,114,8\nAD 84,84,82\nMvva 152,112,8\nD1 1,16,46,3072\nMvva 156,112,12\n"
"s_add_u32 m0, s89, 32768\ns_add_u32 s46, s86, s88\nMvva 148,114,12\nAD 84,84,83\nCV 75,82,83\nMvva 160,114,16\nRa 128,2,16384\nv_xor_b32 v3, 0x10000, v2\n"
"v_add_u32 v0, 0x20000, v1\nMvva 168,112,16\nEX 80,60\nMvva 172,112,20\nRa 132,2,17408\ns_sub_u32 s94, s49, 1703936\nMvva 164,114,20\nWV 10\nEX 81,61\nMvva 176,114,24\n"
"Ra 136,2,18432\nAD 85,85,80\ns_xor_b32 s92, s94, 0x202000\nMvva 184,112,24\nD1 1,16,46,0\ns_lshl_b32 s89, s11, 12\nMvva 188,112,28\nRa 140,2,19456\ns_lshl1_add_u32 s2, s12, 0\n"
"Mvva 180,114,28\nAD 85,85,81\nCV 78,80,81\nMvva 192,114,32\nRa 144,2,20480\ns_lshl3_add_u32 s2, s2, s13\ns_lshl4_add_u32 s2, s2, 0xc048200\nMvva 200,112,32\nEX 82,62\n"
"Mvva 204,112,36\nRa 148,2,21504\ns_and_b32 s3, s72, 0x2000\nMvva 196,114,36\nWV 6\nEX 83,63\nMvva 208,114,40\nRa 152,2,22528\nAD 85,85,82\nMvva 216,112,40\nD1 1,16,46,1024\n"
"s_cmp_eq_u32 s11, 1\ns_cselect_b32 s54, s2, s54\ns_cselect_b32 s3, s3, 0\nMvva 220,112,44\nRa 156,2,23552\nv_xor_b32 v25, s3, v25\nMvva 212,114,44\nAD 85,85,83\nCV 79,82,83\n"
"Mvva 224,114,48\nRa 160,2,24576\nv_max_f32 v90, v84, v85\nMvva 232,112,48\nv_cmp_lt_f32 vcc, s63, v90\nMvva 236,112,52\nRa 164,2,25600\nMvva 228,114,52\n"
"s_waitcnt vmcnt(4) lgkmcnt(8)\nMvva 240,114,56\nRa 168,2,26624\nMvva 248,112,56\nD1 1,16,46,2048\nMvva 252,112,60\nRa 172,2,27648\nMvva 244,114,60\ns_cbranch_vccnz .T87\n.T88:\n"
"AD 86,86,84\nAD 87,87,85\nMvav 128,128,72\nRa 176,2,28672\nv_mov_b32 v84, v86\nv_mov_b32 v85, v87\nMvav 132,128,76\nv_permlane32_swap_b32 v86, v84\nv_permlane32_swap_b32 v87, v85\n"
"Mvav 140,132,76\nRa 180,2,29696\nAD 86,86,84\nMvav 136,132,72\nWL 8\nAD 87,87,85\nMvav 144,136,72\nRa 184,2,30720\nv_mov_b32 v84, v86\nv_mov_b32 v85, v87\nMvav 148,136,76\n"
"D1 1,16,46,3072\nv_permlane16_swap_b32 v86, v84\nMvav 156,140,76\nRa 188,2,31744\nv_permlane16_swap_b32 v87, v85\nMvav 152,140,72\nAD 86,86,84\nAD 87,87,85\nMvav 160,144,72\n"
"Ra 128,2,0\nv_rcp_f32 v86, v86\nMvav 164,144,76\nv_rcp_f32 v87, v87\nMvav 172,148,76\nRa 132,2,1024\nMU 128,128,86\nMU 129,129,86\nMvav 168,148,72\nWL 8\n"
"s_add_u32 m0, s89, 131072\ns_add_u32 s46, s96, 1212416\nCV 96,128,129\nMvav 176,152,72\nRa 136,2,2048\nMU 130,130,86\nMU 131,131,86\nMvav 180,152,76\nD1 1,16,46,0\nCV 97,130,131\n"
"MU 132,132,87\nMvav 188,156,76\nRa 140,2,3072\nMU 133,133,87\nCV 100,132,133\nMvav 184,156,72\nMU 134,134,87\nMU 135,135,87\nCV 101,134,135\nMvav 192,160,72\nRa 144,2,4096\n"
"MU 136,136,86\nMU 137,137,86\nCV 98,136,137\nMvav 196,160,76\nMU 138,138,86\nMU 139,139,86\nCV 99,138,139\nMvav 204,164,76\nRa 148,2,5120\nMU 140,140,87\nMU 141,141,87\n"
"Mvav 200,164,72\nWL 8\nCV 102,140,141\nMU 142,142,87\nMvav 208,168,72\nRa 152,2,6144\nMU 143,143,87\nCV 103,142,143\nMvav 212,168,76\nD1 1,16,46,1024\nMU 144,144,86\n"
"MU 145,145,86\nMvav 220,172,76\nRa 156,2,7168\nCV 104,144,145\nMU 146,146,86\nMU 147,147,86\nMvav 216,172,72\nCV 105,146,147\nMU 148,148,87\nMU 149,149,87\nMvav 224,176,72\n"
"Ra 160,2,8192\nCV 108,148,149\nMU 150,150,87\nMvav 228,176,76\nMU 151,151,87\nCV 109,150,151\nMU 152,152,86\nMvav 236,180,76\nRa 164,2,9216\nMU 153,153,86\nCV 106,152,153\n"
"Mvav 232,180,72\nWL 8\nMU 154,154,86\nMU 155,155,86\nCV 107,154,155\nMvav 240,184,72\nRa 168,2,10240\nMU 156,156,87\nMU 157,157,87\nMvav 244,184,76\nD1 1,16,46,2048\n"
"CV 110,156,157\nMU 158,158,87\nMvav 252,188,76\nRa 172,2,11264\nMU 159,159,87\nCV 111,158,159\nMvav 248,188,72\nMU 160,160,86\nMU 161,161,86\nCV 112,160,161\nZvav 128,128,96\n"
"Ra 176,2,12288\nMU 162,162,86\nMU 163,163,86\nMvav 128,132,104\nCV 113,162,163\nMU 164,164,87\nZvav 132,132,108\nRa 180,2,13312\nMU 165,165,87\nCV 116,164,165\nMvav 132,128,100\n"
"WL 8\nMU 166,166,87\nMU 167,167,87\nZvav 140,136,100\nRa 184,2,14336\nCV 117,166,167\nMU 168,168,86\nMvav 140,140,108\nD1 1,16,46,3072\nMU 169,169,86\nCV 114,168,169\n"
"Zvav 136,140,104\nRa 188,2,15360\ns_barrier\nMU 170,170,86\nMvav 136,136,96\nMU 171,171,86\nCV 115,170,171\nMU 172,172,87\nZvav 144,144,96\nRa 128,2,32768\nMU 173,173,87\n"
"CV 118,172,173\nMvav 144,148,104\nMU 174,174,87\nMU 175,175,87\nbuffer_store_dword v25, off, s[16:19], s54 offset:512 sc0 sc1\nZvav 148,148,108\nRa 132,2,33792\nCV 119,174,175\n"
"MU 176,176,86\nMvav 148,144,100\nWL 8\ns_add_u32 m0, s89, 114688\ns_add_u32 s46, s96, 1179648\nZvav 156,152,100\nRa 136,2,34816\nMU 177,177,86\nCV 120,176,177\nMvav 156,156,108\n"
"D1 1,16,46,0\nMU 178,178,86\nMU 179,179,86\nZvav 152,156,104\nRa 140,2,35840\nCV 121,178,179\nMU 180,180,87\nMvav 152,152,96\nMU 181,181,87\nCV 124,180,181\nMU 182,182,87\n"
"Zvav 64,160,96\nRa 144,2,36864\nMU 183,183,87\nCV 125,182,183\nMvav 64,164,104\nMU 184,184,86\nMU 185,185,86\nZvav 68,164,108\nRa 148,2,37888\nCV 122,184,185\nMU 186,186,86\n"
"Mvav 68,160,100\nWL 8\nMU 187,187,86\nCV 123,186,187\nZvav 76,168,100\nRa 152,2,38912\nMU 188,188,87\nMU 189,189,87\nMvav 76,172,108\nD1 1,16,46,1024\nCV 126,188,189\n"
"MU 190,190,87\nZvav 72,172,104\nRa 156,2,39936\nMU 191,191,87\nCV 127,190,191\nMvav 72,168,96\nMU 192,192,86\nMU 193,193,86\nCV 32,192,193\nMvav 128,176,112\nRa 160,2,40960\n"
"MU 194,194,86\nMU 195,195,86\nMvav 128,180,120\nCV 33,194,195\nMU 196,196,87\nMU 197,197,87\nMvav 132,180,124\nRa 164,2,41984\nCV 36,196,197\nMU 198,198,87\nMvav 132,176,116\n"
"WL 8\nMU 199,199,87\nMvav 140,184,116\nRa 168,2,43008\nCV 37,198,199\nMU 200,200,86\nMvav 140,188,124\nD1 1,16,46,2048\nMU 201,201,86\nCV 34,200,201\nMvav 136,188,120\n"
"Ra 172,2,44032\nMU 202,202,86\nMU 203,203,86\nMvav 136,184,112\nCV 35,202,203\nMU 204,204,87\nMU 205,205,87\nMvav 144,128,112\nRa 176,2,45056\nCV 38,204,205\nMU 206,206,87\n"
"Mvav 144,132,120\nMU 207,207,87\nCV 39,206,207\nMvav 148,132,124\nRa 180,2,46080\nMU 208,208,86\nMU 209,209,86\nMvav 148,128,116\nWL 8\nCV 40,208,209\nMU 210,210,86\n"
"Mvav 156,136,116\nRa 184,2,47104\nMU 211,211,86\nCV 41,210,211\nMvav 156,140,124\nD1 1,16,46,3072\nMU 212,212,87\nMU 213,213,87\nMvav 152,140,120\nRa 188,2,48128\nCV 44,212,213\n"
"Mvav 152,136,112\nMU 214,214,87\nMU 215,215,87\nCV 45,214,215\nMvav 64,144,112\nRa 128,2,49152\nMU 216,216,86\nMU 217,217,86\nMvav 64,148,120\nCV 42,216,217\nMU 218,218,86\n"
"MU 219,219,86\nMvav 68,148,124\nRa 132,2,50176\nCV 43,218,219\nMU 220,220,87\nMvav 68,144,116\nWL 8\ns_add_u32 m0, s89, 147456\ns_add_u32 s46, s96, 1245184\nMvav 76,152,116\n"
"Ra 136,2,51200\nMU 221,221,87\nCV 46,220,221\nMvav 76,156,124\nD1 1,16,46,0\nMU 222,222,87\nMU 223,223,87\nMvav 72,156,120\nRa 140,2,52224\nCV 47,222,223\nMvav 72,152,112\n"
"MU 224,224,86\nMU 225,225,86\nCV 48,224,225\nMvav 128,160,32\nRa 144,2,53248\nMU 226,226,86\nMU 227,227,86\nMvav 128,164,40\nCV 49,226,227\nMU 228,228,87\nMvav 132,164,44\n"
"Ra 148,2,54272\nMU 229,229,87\nCV 52,228,229\nMvav 132,160,36\nWL 8\nMU 230,230,87\nMU 231,231,87\nMvav 140,168,36\nRa 152,2,55296\nCV 53,230,231\nMU 232,232,86\nMvav 140,172,44\n"
"D1 1,16,46,1024\nMU 233,233,86\nCV 50,232,233\nMvav 136,172,40\nRa 156,2,56320\nMU 234,234,86\nMvav 136,168,32\nMU 235,235,86\nCV 51,234,235\nMU 236,236,87\nMvav 144,176,32\n"
"Ra 160,2,57344\nMU 237,237,87\nCV 54,236,237\nMvav 144,180,40\nMU 238,238,87\nMU 239,239,87\nCV 55,238,239\nMvav 148,180,44\nRa 164,2,58368\nMU 240,240,86\nMU 241,241,86\n"
"Mvav 148,176,36\nWL 8\nCV 56,240,241\nMvav 156,184,36\nRa 168,2,59392\nMU 242,242,86\nMU 243,243,86\nMvav 156,188,44\nD1 1,16,46,2048\nCV 57,242,243\nMU 244,244,87\n"
"Mvav 152,188,40\nRa 172,2,60416\nMU 245,245,87\nCV 60,244,245\nMvav 152,184,32\nMU 246,246,87\nMU 247,247,87\nCV 61,246,247\nMvav 64,128,32\nRa 176,2,61440\nMU 248,248,86\n"
"MU 249,249,86\nMvav 64,132,40\nCV 58,248,249\nMU 250,250,86\nMvav 68,132,44\nRa 180,2,62464\nMU 251,251,86\nCV 59,250,251\nMvav 68,128,36\ns_waitcnt vmcnt(16) lgkmcnt(8)\n"
"MU 252,252,87\nMU 253,253,87\nMvav 76,136,36\nRa 184,2,63488\nCV 62,252,253\nMU 254,254,87\nMvav 76,140,44\nD1 1,16,46,3072\nMU 255,255,87\nCV 63,254,255\nMvav 72,140,40\n"
"Ra 188,2,64512\ns_barrier\nMvav 72,136,32\nMvav 128,144,48\nRa 128,3,0\nMvav 128,148,56\nMvav 132,148,60\nRa 132,3,1024\nMvav 132,144,52\nWL 8\nMvav 140,152,52\nRa 136,3,2048\n"
"Mvav 140,156,60\nMvav 136,156,56\nRa 140,3,3072\nMvav 136,152,48\nMvav 144,160,48\nRa 144,3,4096\nMvav 144,164,56\nMvav 148,164,60\nRa 148,3,5120\nCV 132,132,133\nMvav 148,160,52\n"
"WL 8\nCV 133,134,135\nMvav 156,168,52\nRa 152,3,6144\nCV 134,140,141\nMvav 156,172,60\nCV 135,142,143\ns_add_u32 s5, s94, 0\nMvav 152,172,56\nRa 156,3,7168\nSv 132,1,16,5,1024\n"
"Mvav 152,168,48\nCV 128,128,129\nCV 129,130,131\nMvav 64,176,48\nRa 160,3,8192\nCV 130,136,137\nMvav 64,180,56\nbuffer_load_dword v27, off, s[16:19], s60 offset:512 sc0 sc1\n"
"CV 131,138,139\nMvav 68,180,60\nRa 164,3,9216\nSv 128,1,16,5,0\nMvav 68,176,52\nWL 6\nCV 148,148,149\nMvav 76,184,52\nRa 168,3,10240\nCV 149,150,151\nMvav 76,188,60\n"
"CV 150,156,157\nCV 151,158,159\nMvav 72,188,56\nRa 172,3,11264\ns_add_u32 s5, s94, 131072\nMvav 72,184,48\nSv 148,1,16,5,1024\nCV 144,144,145\nZvav 160,128,96\nRa 176,3,12288\n"
"CV 145,146,147\nMvav 160,132,104\nCV 146,152,153\nCV 147,154,155\nMvav 160,136,112\nRa 180,3,13312\nSv 144,1,16,5,0\nMvav 160,140,120\nWL 6\nCV 68,68,69\nZvav 164,140,124\n"
"Ra 184,3,14336\nCV 69,70,71\nMvav 164,136,116\nCV 70,76,77\nCV 71,78,79\nMvav 164,132,108\nRa 188,3,15360\ns_add_u32 s5, s94, 262144\nMvav 164,128,100\nSv 68,1,16,5,1024\n"
"CV 64,64,65\nMvav 164,144,36\nRa 128,3,16384\nCV 65,66,67\nMvav 164,148,44\nCV 66,72,73\nCV 67,74,75\nMvav 164,152,52\nRa 132,3,17408\nSv 64,1,16,5,0\nMvav 164,156,60\nWL 6\n"
"Mvav 160,156,56\nRa 136,3,18432\n.W550:\ns_cbranch_execz .WB21\nMvav 160,152,48\nMvav 160,148,40\nRa 140,3,19456\nMvav 160,144,32\nZvav 168,160,96\nRa 144,3,20480\n"
"Mvav 168,164,104\nMvav 168,168,112\nRa 148,3,21504\nMvav 168,172,120\nWL 6\nZvav 172,172,124\nRa 152,3,22528\nMvav 172,168,116\nMvav 172,164,108\nRa 156,3,23552\nMvav 172,160,100\n"
"Mvav 172,176,36\nRa 160,3,24576\nMvav 172,180,44\nMvav 172,184,52\nRa 164,3,25600\nMvav 172,188,60\nWL 6\nMvav 168,188,56\nRa 168,3,26624\nMvav 168,184,48\nMvav 168,180,40\n"
"Ra 172,3,27648\nMvav 168,176,32\nZvav 176,128,96\nRa 176,3,28672\nCV 164,164,165\nMvav 176,132,104\nCV 165,166,167\nCV 166,172,173\nMvav 176,136,112\nRa 180,3,29696\n"
"CV 167,174,175\nMvav 176,140,120\ns_waitcnt vmcnt(11) lgkmcnt(6)\ns_add_u32 s5, s94, 393216\nZvav 180,140,124\nRa 184,3,30720\nSv 164,1,16,5,1024\nMvav 180,136,116\n"
"CV 160,160,161\nCV 161,162,163\nMvav 180,132,108\nRa 188,3,31744\ns_barrier\nMvav 180,128,100\nCV 162,168,169\nCV 163,170,171\nMvav 180,144,36\nRa 128,3,32768\nSv 160,1,16,5,0\n"
"Mvav 180,148,44\nMvav 180,152,52\nRa 132,3,33792\nMvav 180,156,60\nWL 6\ns_add_u32 m0, s89, 16384\ns_add_u32 s46, s96, 1277952\nMvav 176,156,56\nRa 136,3,34816\nMvav 176,152,48\n"
"D1 1,16,46,0\nMvav 176,148,40\nRa 140,3,35840\nMvav 176,144,32\nZvav 184,160,96\nRa 144,3,36864\nMvav 184,164,104\nMvav 184,168,112\nRa 148,3,37888\nMvav 184,172,120\nWL 6\n"
"Zvav 188,172,124\nRa 152,3,38912\nMvav 188,168,116\nD1 1,16,46,1024\nMvav 188,164,108\nRa 156,3,39936\nMvav 188,160,100\nMvav 188,176,36\nRa 160,3,40960\nWV 9\n"
"v_cmp_ne_u32 vcc, s15, v27\ns_nop 0\ns_cbranch_vccnz .S114\n.S115:\nMvav 188,180,44\ns_add_u32 s4, s92, 0\nMvav 188,184,52\nRa 164,3,41984\nMvav 188,188,60\nWL 6\n"
"buffer_load_dwordx4 v[208:211], v1, s[16:19], s4 offen offset:0 sc0 sc1\nMvav 184,188,56\nRa 168,3,43008\nMvav 184,184,48\nD1 1,16,46,2048\nMvav 184,180,40\nRa 172,3,44032\n"
"Mvav 184,176,32\nbuffer_load_dwordx4 v[212:215], v1, s[16:19], s4 offen offset:1024 sc0 sc1\nZvav 192,128,96\nRa 176,3,45056\nv_lshrrev_b32 v88, 8, v1\nMvav 192,132,104\n"
"v_lshlrev_b32 v89, 5, v88\nCV 180,180,181\nMvav 192,136,112\nRa 180,3,46080\nCV 181,182,183\nMvav 192,140,120\nWL 6\nCV 182,188,189\nZvav 196,140,124\nRa 184,3,47104\n"
"v_bfe_u32 v90, v1, 4, 4\nMvav 196,136,116\nD1 1,16,46,3072\nCV 183,190,191\nMvav 196,132,108\nRa 188,3,48128\ns_add_u32 s5, s94, 524288\nMvav 196,128,100\n"
"v_mul_u32_u24 v91, 896, v90\nSv 180,1,16,5,1024\nMvav 196,144,36\nRa 128,3,49152\nv_lshl_add_u32 v91, v88, 4, v91\nMvav 196,148,44\nv_add_u32 v90, s21, v90\nCV 176,176,177\n"
"Mvav 196,152,52\nRa 132,3,50176\nCV 177,178,179\nMvav 196,156,60\nWL 6\ns_add_u32 m0, s89, 0\ns_add_u32 s46, s96, 1310720\nMvav 192,156,56\nRa 136,3,51200\n"
"v_lshlrev_b32 v88, 3, v90\nMvav 192,152,48\nD1 1,16,46,0\nCV 178,184,185\nMvav 192,148,40\nRa 140,3,52224\nCV 179,186,187\nMvav 192,144,32\nv_add_u32 v94, 64, v90\n"
"Sv 176,1,16,5,0\nZvav 200,160,96\nRa 144,3,53248\ns_mov_b32 s63, 0xc040000\nMvav 200,164,104\nv_lshrrev_b32 v92, 8, v1\nMvav 200,168,112\nRa 148,3,54272\nMvav 200,172,120\nWL 6\n"
"Zvav 204,172,124\nRa 152,3,55296\nv_bfe_u32 v93, v1, 6, 2\nMvav 204,168,116\nD1 1,16,46,1024\nv_cmp_eq_u32 vcc, v92, v93\nv_and_b32 v92, 16, v1\nv_mov_b32 v93, 0x3f80\n"
"v_cndmask_b32 v93, 0, v93, vcc\nMvav 204,164,108\nRa 156,3,56320\nMvav 204,160,100\nv_lshlrev_b32 v93, v92, v93\nMvav 204,176,36\nRa 160,3,57344\nv_and_b32 v92, 32, v1\n"
"Mvav 204,180,44\ns_add_u32 s4, s92, 131072\nv_cmp_eq_u32 vcc, 0, v92\nv_mov_b32 v4, 0\nv_mov_b32 v5, 0\nv_cndmask_b32 v6, 0, v93, vcc\nMvav 204,184,52\nRa 164,3,58368\n"
"Mvav 204,188,60\nWL 6\nbuffer_load_dwordx4 v[216:219], v1, s[16:19], s4 offen offset:0 sc0 sc1\nMvav 200,188,56\nRa 168,3,59392\nv_sub_u32 v7, v93, v6\nMvav 200,184,48\n"
"D1 1,16,46,2048\nMvav 200,180,40\nRa 172,3,60416\nMvav 200,176,32\nbuffer_load_dwordx4 v[220:223], v1, s[16:19], s4 offen offset:1024 sc0 sc1\nZvav 160,128,96\nRa 176,3,61440\n"
"v_mov_b32 v8, 0\nMvav 160,132,104\nv_mov_b32 v9, 0\nCV 196,196,197\nMvav 160,136,112\nRa 180,3,62464\nCV 197,198,199\nMvav 160,140,120\nWL 6\nCV 198,204,205\nZvav 164,140,124\n"
"Ra 184,3,63488\nCV 199,206,207\nMvav 164,136,116\nD1 1,16,46,3072\ns_add_u32 s5, s94, 655360\nMvav 164,132,108\nRa 188,3,64512\ns_barrier\nMvav 164,128,100\nSv 196,1,16,5,1024\n"
"CV 192,192,193\nMvav 164,144,36\nRa 128,0,0\nCV 193,194,195\nMvav 164,148,44\nCV 194,200,201\nCV 195,202,203\nMvav 164,152,52\nRa 132,0,1024\nSv 192,1,16,5,0\nMvav 164,156,60\n"
"WL 6\ns_add_u32 m0, s89, 32768\ns_add_u32 s46, s96, 1343488\nMvav 160,156,56\nRa 136,0,2048\nMvav 160,152,48\nD1 1,16,46,0\nMvav 160,148,40\nRa 140,0,3072\nMvav 160,144,32\n"
"Zvav 168,160,96\nRa 144,0,4096\nWV 12\nv_max_u32 v21, v211, v215\nv_cmp_eq_u32 vcc, -1, v21\ns_nop 0\ns_cbranch_vccnz .S90\n.S91:\nMvav 168,164,104\nv_lshlrev_b32 v176, 16, v208\n"
"Mvav 168,168,112\nRa 148,0,5120\nMvav 168,172,120\nWL 6\nv_and_b32 v177, 0xffff0000, v208\nZvav 172,172,124\nRa 152,0,6144\nMvav 172,168,116\nD1 1,16,46,1024\nMvav 172,164,108\n"
"Ra 156,0,7168\nv_lshlrev_b32 v178, 16, v209\nMvav 172,160,100\nv_and_b32 v179, 0xffff0000, v209\nMvav 172,176,36\nRa 160,0,8192\nv_lshlrev_b32 v180, 16, v212\nMvav 172,180,44\n"
"v_and_b32 v181, 0xffff0000, v212\ns_add_u32 s4, s92, 262144\nMvav 172,184,52\nRa 164,0,9216\nMvav 172,188,60\nWL 6\nv_lshlrev_b32 v182, 16, v213\n"
"buffer_load_dwordx4 v[224:227], v1, s[16:19], s4 offen offset:0 sc0 sc1\nMvav 168,188,56\nRa 168,0,10240\nMvav 168,184,48\nD1 1,16,46,2048\nMvav 168,180,40\nRa 172,0,11264\n"
"v_and_b32 v183, 0xffff0000, v213\nMvav 168,176,32\nv_lshlrev_b32 v184, 16, v210\nbuffer_load_dwordx4 v[228:231], v1, s[16:19], s4 offen offset:1024 sc0 sc1\nMvav 176,128,96\n"
"Ra 176,0,12288\nv_and_b32 v185, 0xffff0000, v210\nMvav 176,132,104\nv_lshlrev_b32 v186, 16, v211\nCV 164,164,165\nMvav 176,136,112\nRa 180,0,13312\nCV 165,166,167\n"
"Mvav 176,140,120\nWL 6\nCV 166,172,173\nMvav 180,140,124\nRa 184,0,14336\nv_and_b32 v187, 0xffff0000, v211\nMvav 180,136,116\nD1 1,16,46,3072\nCV 167,174,175\nMvav 180,132,108\n"
"Ra 188,0,15360\ns_add_u32 s5, s94, 786432\nMvav 180,128,100\nv_lshlrev_b32 v188, 16, v214\nSv 164,1,16,5,1024\nMvav 180,144,36\nRa 128,0,16384\nv_and_b32 v189, 0xffff0000, v214\n"
"Mvav 180,148,44\nv_lshlrev_b32 v190, 16, v215\nCV 160,160,161\nMvav 180,152,52\nRa 132,0,17408\nCV 161,162,163\nMvav 180,156,60\nWL 6\ns_add_u32 m0, s89, 49152\n"
"s_add_u32 s46, s96, 1376256\nMvav 176,156,56\nRa 136,0,18432\nv_and_b32 v191, 0xffff0000, v215\nMvav 176,152,48\nD1 1,16,46,0\nCV 162,168,169\nMvav 176,148,40\nRa 140,0,19456\n"
"CV 163,170,171\nMvav 176,144,32\nSv 160,1,16,5,0\nMvav 184,160,96\nRa 144,0,20480\nWV 12\nv_max_u32 v21, v219, v223\nv_cmp_eq_u32 vcc, -1, v21\ns_nop 0\ns_cbranch_vccnz .S93\n"
".S94:\nMvav 184,164,104\nv_lshlrev_b32 v192, 16, v216\nMvav 184,168,112\nRa 148,0,21504\nMvav 184,172,120\nWL 6\nv_and_b32 v193, 0xffff0000, v216\nMvav 188,172,124\n"
"Ra 152,0,22528\nMvav 188,168,116\nD1 1,16,46,1024\nMvav 188,164,108\nRa 156,0,23552\nv_lshlrev_b32 v194, 16, v217\nMvav 188,160,100\nv_and_b32 v195, 0xffff0000, v217\n"
"Mvav 188,176,36\nRa 160,0,24576\nv_lshlrev_b32 v196, 16, v220\nMvav 188,180,44\nv_and_b32 v197, 0xffff0000, v220\ns_add_u32 s4, s92, 393216\nMvav 188,184,52\nRa 164,0,25600\n"
"Mvav 188,188,60\nWL 6\nv_lshlrev_b32 v198, 16, v221\nbuffer_load_dwordx4 v[208:211], v1, s[16:19], s4 offen offset:0 sc0 sc1\nMvav 184,188,56\nRa 168,0,26624\nMvav 184,184,48\n"
"D1 1,16,46,2048\nMvav 184,180,40\nRa 172,0,27648\nv_and_b32 v199, 0xffff0000, v221\nMvav 184,176,32\nv_lshlrev_b32 v200, 16, v218\n"
"buffer_load_dwordx4 v[212:215], v1, s[16:19], s4 offen offset:1024 sc0 sc1\nMvav 192,128,96\nRa 176,0,28672\nv_and_b32 v201, 0xffff0000, v218\nMvav 192,132,104\n"
"v_lshlrev_b32 v202, 16, v219\nCV 180,180,181\nMvav 192,136,112\nRa 180,0,29696\nCV 181,182,183\nMvav 192,140,120\ns_waitcnt vmcnt(15) lgkmcnt(6)\nCV 182,188,189\nMvav 196,140,124\n"
"Ra 184,0,30720\nv_and_b32 v203, 0xffff0000, v219\nMvav 196,136,116\nD1 1,16,46,3072\nCV 183,190,191\nMvav 196,132,108\nRa 188,0,31744\ns_barrier\nMvav 196,128,100\n"
"v_lshlrev_b32 v204, 16, v222\ns_add_u32 s5, s94, 917504\nMvav 196,144,36\nRa 128,2,16384\nv_and_b32 v205, 0xffff0000, v222\nMvav 196,148,44\nv_lshlrev_b32 v206, 16, v223\n"
"Sv 180,1,16,5,1024\nMvav 196,152,52\nRa 132,2,17408\nCV 176,176,177\nMvav 196,156,60\nWL 6\ns_add_u32 m0, s89, 65536\ns_add_u32 s46, s96, 1409024\nMvav 192,156,56\nRa 136,2,18432\n"
"v_and_b32 v207, 0xffff0000, v223\nMvav 192,152,48\nD1 1,16,46,0\nCV 177,178,179\nMvav 192,148,40\nRa 140,2,19456\nCV 178,184,185\nMvav 192,144,32\nCV 179,186,187\nSv 176,1,16,5,0\n"
"Mvav 200,160,96\nRa 144,2,20480\nWV 12\nv_max_u32 v21, v227, v231\nv_cmp_eq_u32 vcc, -1, v21\ns_nop 0\ns_cbranch_vccnz .S96\n.S97:\nMvav 200,164,104\nv_lshlrev_b32 v160, 16, v224\n"
"Mvav 200,168,112\nRa 148,2,21504\nMvav 200,172,120\nWL 6\nv_and_b32 v161, 0xffff0000, v224\nMvav 204,172,124\nRa 152,2,22528\nMvav 204,168,116\nD1 1,16,46,1024\nMvav 204,164,108\n"
"Ra 156,2,23552\nv_lshlrev_b32 v162, 16, v225\nMvav 204,160,100\nv_and_b32 v163, 0xffff0000, v225\nMvav 204,176,36\nRa 160,2,24576\nv_lshlrev_b32 v164, 16, v228\nMvav 204,180,44\n"
"v_and_b32 v165, 0xffff0000, v228\ns_add_u32 s4, s92, 524288\nMvav 204,184,52\nRa 164,2,25600\nMvav 204,188,60\nWL 6\nv_lshlrev_b32 v166, 16, v229\n"
"buffer_load_dwordx4 v[216:219], v1, s[16:19], s4 offen offset:0 sc0 sc1\nMvav 200,188,56\nRa 168,2,26624\nMvav 200,184,48\nD1 1,16,46,2048\nMvav 200,180,40\nRa 172,2,27648\n"
"v_and_b32 v167, 0xffff0000, v229\nMvav 200,176,32\nv_lshlrev_b32 v168, 16, v226\nbuffer_load_dwordx4 v[220:223], v1, s[16:19], s4 offen offset:1024 sc0 sc1\nMvav 160,128,96\n"
"Ra 176,2,28672\nv_and_b32 v169, 0xffff0000, v226\nMvav 160,132,104\nv_lshlrev_b32 v170, 16, v227\nCV 196,196,197\nMvav 160,136,112\nRa 180,2,29696\nCV 197,198,199\n"
"Mvav 160,140,120\nWL 6\nCV 198,204,205\nMvav 164,140,124\nRa 184,2,30720\nv_and_b32 v24, 0x70, v1\nMvav 164,136,116\nD1 1,16,46,3072\nCV 199,206,207\nMvav 164,132,108\n"
"Ra 188,2,31744\nv_and_b32 v171, 0xffff0000, v227\nMvav 164,128,100\nv_lshlrev_b32 v172, 16, v230\ns_add_u32 s5, s94, 1048576\nMvav 164,144,36\nRa 128,2,0\n"
"v_lshlrev_b32 v24, 5, v24\nMvav 164,148,44\nv_and_b32 v173, 0xffff0000, v230\nSv 196,1,16,5,1024\nMvav 164,152,52\nRa 132,2,1024\nCV 192,192,193\nMvav 164,156,60\nWL 6\n"
"v_lshlrev_b32 v174, 16, v231\nMvav 160,156,56\nRa 136,2,2048\nbuffer_load_dword v27, v24, s[16:19], s41 offen sc0 sc1\nMvav 160,152,48\nv_and_b32 v175, 0xffff0000, v231\n"
"CV 193,194,195\nMvav 160,148,40\nRa 140,2,3072\nCV 194,200,201\nMvav 160,144,32\nCV 195,202,203\nSv 192,1,16,5,0\nMvav 168,160,96\nRa 144,2,4096\nWV 12\nv_max_u32 v21, v211, v215\n"
"v_cmp_eq_u32 vcc, -1, v21\ns_nop 0\ns_cbranch_vccnz .S99\n.S100:\nMvav 168,164,104\nv_lshlrev_b32 v176, 16, v208\nMvav 168,168,112\nRa 148,2,5120\nMvav 168,172,120\nWL 6\n"
"Mvav 172,172,124\nRa 152,2,6144\nv_and_b32 v177, 0xffff0000, v208\nMvav 172,168,116\nv_lshlrev_b32 v178, 16, v209\nMvav 172,164,108\nRa 156,2,7168\nMvav 172,160,100\n"
"v_and_b32 v179, 0xffff0000, v209\nMvav 172,176,36\nRa 160,2,8192\nv_lshlrev_b32 v180, 16, v212\nMvav 172,180,44\nv_and_b32 v181, 0xffff0000, v212\ns_add_u32 s4, s92, 655360\n"
"Mvav 172,184,52\nRa 164,2,9216\nMvav 172,188,60\nWL 6\nbuffer_load_dwordx4 v[224:227], v1, s[16:19], s4 offen offset:0 sc0 sc1\nMvav 168,188,56\nRa 168,2,10240\n"
"v_lshlrev_b32 v182, 16, v213\nMvav 168,184,48\nv_and_b32 v183, 0xffff0000, v213\nMvav 168,180,40\n.W551:\ns_cbranch_execz .WB21\nRa 172,2,11264\nMvav 168,176,32\n"
"v_lshlrev_b32 v184, 16, v210\nbuffer_load_dwordx4 v[228:231], v1, s[16:19], s4 offen offset:1024 sc0 sc1\nMvav 176,128,96\nRa 176,2,12288\nv_and_b32 v185, 0xffff0000, v210\n"
"Mvav 176,132,104\nv_lshlrev_b32 v186, 16, v211\nCV 164,164,165\nMvav 176,136,112\nRa 180,2,13312\nCV 165,166,167\nMvav 176,140,120\ns_waitcnt vmcnt(13) lgkmcnt(6)\nCV 166,172,173\n"
"Mvav 180,140,124\nRa 184,2,14336\nCV 167,174,175\nMvav 180,136,116\nv_and_b32 v187, 0xffff0000, v211\ns_add_u32 s5, s94, 1179648\nMvav 180,132,108\nRa 188,2,15360\ns_barrier\n"
"Mvav 180,128,100\nv_lshlrev_b32 v188, 16, v214\nSv 164,1,16,5,1024\nMvav 180,144,36\nRa 128,2,32768\nv_and_b32 v189, 0xffff0000, v214\nMvav 180,148,44\n"
"v_lshlrev_b32 v190, 16, v215\nCV 160,160,161\nMvav 180,152,52\nRa 132,2,33792\nCV 161,162,163\nMvav 180,156,60\nWL 6\nCV 162,168,169\nMvav 176,156,56\nRa 136,2,34816\n"
"CV 163,170,171\nMvav 176,152,48\nv_and_b32 v191, 0xffff0000, v215\nSv 160,1,16,5,0\nMvav 176,148,40\nRa 140,2,35840\nMvav 176,144,32\nMvav 184,160,96\nRa 144,2,36864\nWV 8\n"
"v_max_u32 v21, v219, v223\nv_cmp_eq_u32 vcc, -1, v21\ns_nop 0\ns_cbranch_vccnz .S102\n.S103:\nMvav 184,164,104\nv_lshlrev_b32 v192, 16, v216\nMvav 184,168,112\nRa 148,2,37888\n"
"Mvav 184,172,120\nWL 6\nMvav 188,172,124\nRa 152,2,38912\nv_and_b32 v193, 0xffff0000, v216\nMvav 188,168,116\nv_lshlrev_b32 v194, 16, v217\nMvav 188,164,108\nRa 156,2,39936\n"
"Mvav 188,160,100\nv_and_b32 v195, 0xffff0000, v217\nMvav 188,176,36\nRa 160,2,40960\nv_lshlrev_b32 v196, 16, v220\nMvav 188,180,44\nv_and_b32 v197, 0xffff0000, v220\n"
"s_add_u32 s4, s92, 786432\nMvav 188,184,52\nRa 164,2,41984\nMvav 188,188,60\nWL 6\nbuffer_load_dwordx4 v[208:211], v1, s[16:19], s4 offen offset:0 sc0 sc1\nMvav 184,188,56\n"
"Ra 168,2,43008\nv_lshlrev_b32 v198, 16, v221\nMvav 184,184,48\nv_and_b32 v199, 0xffff0000, v221\nMvav 184,180,40\nRa 172,2,44032\nMvav 184,176,32\nv_lshlrev_b32 v200, 16, v218\n"
"buffer_load_dwordx4 v[212:215], v1, s[16:19], s4 offen offset:1024 sc0 sc1\nMvav 192,128,96\nRa 176,2,45056\nv_and_b32 v201, 0xffff0000, v218\nMvav 192,132,104\n"
"v_lshlrev_b32 v202, 16, v219\nCV 180,180,181\nMvav 192,136,112\nRa 180,2,46080\nCV 181,182,183\nMvav 192,140,120\nWL 6\nCV 182,188,189\nMvav 196,140,124\nRa 184,2,47104\n"
"CV 183,190,191\nMvav 196,136,116\nv_and_b32 v203, 0xffff0000, v219\ns_add_u32 s5, s94, 1310720\nMvav 196,132,108\nRa 188,2,48128\nSv 180,1,16,5,1024\nMvav 196,128,100\n"
"v_lshlrev_b32 v204, 16, v222\nCV 176,176,177\nMvav 196,144,36\nRa 128,2,49152\nv_and_b32 v205, 0xffff0000, v222\nMvav 196,148,44\nv_lshlrev_b32 v206, 16, v223\nCV 177,178,179\n"
"Mvav 196,152,52\nRa 132,2,50176\nCV 178,184,185\nMvav 196,156,60\nWL 6\nCV 179,186,187\nMvav 192,156,56\nRa 136,2,51200\nSv 176,1,16,5,0\nMvav 192,152,48\n"
"v_and_b32 v207, 0xffff0000, v223\nMvav 192,148,40\nRa 140,2,52224\nMvav 192,144,32\nMvav 200,160,96\nRa 144,2,53248\nWV 6\nv_max_u32 v21, v227, v231\nv_cmp_eq_u32 vcc, -1, v21\n"
"s_nop 0\ns_cbranch_vccnz .S105\n.S106:\nMvav 200,164,104\nv_lshlrev_b32 v160, 16, v224\nv_xor_b32 v27, s15, v27\nv_and_b32 v26, 0xffffdfff, v27\nv_cmp_ne_u32 s[64:65], 0, v27\n"
"v_cmp_ne_u32 vcc, 0, v26\ns_nop 0\ns_cbranch_vccnz .S117\n.S118:\nMvav 200,168,112\nRa 148,2,54272\ns_bitcmp1_b32 s64, 0\ns_cselect_b32 s2, 0x2000, 0\nMvav 200,172,120\nWL 6\n"
"Mvav 204,172,124\nRa 152,2,55296\nv_and_b32 v161, 0xffff0000, v224\nMvav 204,168,116\nv_lshlrev_b32 v162, 16, v225\nMvav 204,164,108\nRa 156,2,56320\ns_xor_b32 s2, s2, s20\n"
"Mvav 204,160,100\nv_and_b32 v163, 0xffff0000, v225\nMvav 204,176,36\nRa 160,2,57344\nv_lshlrev_b32 v164, 16, v228\nMvav 204,180,44\nv_and_b32 v165, 0xffff0000, v228\n"
"Mvav 204,184,52\nRa 164,2,58368\nMvav 204,188,60\nWL 6\ns_add_u32 s24, s2, 0x0\nMvav 200,188,56\nRa 168,2,59392\nv_lshlrev_b32 v166, 16, v229\nMvav 200,184,48\n"
"v_and_b32 v167, 0xffff0000, v229\ns_bitcmp1_b32 s64, 1\ns_cselect_b32 s2, 0x2000, 0\nMvav 200,180,40\nRa 172,2,60416\nMvav 200,176,32\nv_lshlrev_b32 v168, 16, v226\n"
"Mvav 160,128,96\nRa 176,2,61440\nv_and_b32 v169, 0xffff0000, v226\nMvav 160,132,104\nv_lshlrev_b32 v170, 16, v227\nCV 196,196,197\nMvav 160,136,112\nRa 180,2,62464\n"
"CV 197,198,199\nMvav 160,140,120\nWL 6\nCV 198,204,205\nMvav 164,140,124\nRa 184,2,63488\nCV 199,206,207\nMvav 164,136,116\nv_and_b32 v171, 0xffff0000, v227\n"
"s_add_u32 s5, s94, 1441792\nMvav 164,132,108\nRa 188,2,64512\ns_barrier\nMvav 164,128,100\nv_lshlrev_b32 v172, 16, v230\nSv 196,1,16,5,1024\nMvav 164,144,36\nRa 128,3,0\n"
"v_and_b32 v173, 0xffff0000, v230\nMvav 164,148,44\nv_lshlrev_b32 v174, 16, v231\nCV 192,192,193\nMvav 164,152,52\nRa 132,3,1024\nCV 193,194,195\nMvav 164,156,60\nWL 6\n"
"s_xor_b32 s2, s2, s20\nMvav 160,156,56\nRa 136,3,2048\nCV 194,200,201\nMvav 160,152,48\nv_and_b32 v175, 0xffff0000, v231\nCV 195,202,203\nMvav 160,148,40\nRa 140,3,3072\n"
"Sv 192,1,16,5,0\nMvav 160,144,32\ns_add_u32 s25, s2, 0x1800000\ns_bitcmp1_b32 s64, 2\ns_cselect_b32 s2, 0x2000, 0\nMvav 168,160,96\nRa 144,3,4096\nWV 4\nv_max_u32 v21, v211, v215\n"
"v_cmp_eq_u32 vcc, -1, v21\ns_nop 0\ns_cbranch_vccnz .S108\n.S109:\nMvav 168,164,104\nv_lshlrev_b32 v176, 16, v208\nMvav 168,168,112\nRa 148,3,5120\nMvav 168,172,120\nWL 6\n"
"Mvav 172,172,124\nRa 152,3,6144\nv_and_b32 v177, 0xffff0000, v208\nMvav 172,168,116\nv_lshlrev_b32 v178, 16, v209\nMvav 172,164,108\nRa 156,3,7168\ns_xor_b32 s2, s2, s20\n"
"Mvav 172,160,100\nv_and_b32 v179, 0xffff0000, v209\nMvav 172,176,36\nRa 160,3,8192\nv_lshlrev_b32 v180, 16, v212\nMvav 172,180,44\nv_and_b32 v181, 0xffff0000, v212\n"
"Mvav 172,184,52\nRa 164,3,9216\nMvav 172,188,60\nWL 6\ns_add_u32 s26, s2, 0x3000000\nMvav 168,188,56\nRa 168,3,10240\nv_lshlrev_b32 v182, 16, v213\nMvav 168,184,48\n"
"v_and_b32 v183, 0xffff0000, v213\ns_bitcmp1_b32 s64, 3\ns_cselect_b32 s2, 0x2000, 0\nMvav 168,180,40\nRa 172,3,11264\nMvav 168,176,32\nv_lshlrev_b32 v184, 16, v210\n"
"Mvav 176,128,96\nRa 176,3,12288\ns_xor_b32 s2, s2, s20\nMvav 176,132,104\nv_and_b32 v185, 0xffff0000, v210\nCV 164,164,165\nMvav 176,136,112\nRa 180,3,13312\nCV 165,166,167\n"
"Mvav 176,140,120\nWL 6\nCV 166,172,173\nMvav 180,140,124\nRa 184,3,14336\nCV 167,174,175\nMvav 180,136,116\nv_lshlrev_b32 v186, 16, v211\ns_add_u32 s5, s94, 1572864\n"
"Mvav 180,132,108\nRa 188,3,15360\ns_add_u32 s27, s2, 0x4800000\nMvav 180,128,100\nv_and_b32 v187, 0xffff0000, v211\ns_bitcmp1_b32 s64, 4\ns_cselect_b32 s2, 0x2000, 0\n"
"Mvav 180,144,36\nv_lshlrev_b32 v188, 16, v214\nSv 164,1,16,5,1024\nMvav 180,148,44\nv_and_b32 v189, 0xffff0000, v214\nCV 160,160,161\nMvav 180,152,52\n"
"v_lshlrev_b32 v190, 16, v215\nCV 161,162,163\nMvav 180,156,60\nWL 4\ns_xor_b32 s2, s2, s20\nMvav 176,156,56\nv_and_b32 v191, 0xffff0000, v215\nCV 162,168,169\nMvav 176,152,48\n"
"s_add_u32 s32, s2, 0x6000000\ns_bitcmp1_b32 s64, 5\ns_cselect_b32 s2, 0x2000, 0\nMvav 176,148,40\ns_xor_b32 s2, s2, s20\nCV 163,170,171\nMvav 176,144,32\n"
"s_add_u32 s33, s2, 0x7800000\ns_bitcmp1_b32 s64, 6\ns_cselect_b32 s2, 0x2000, 0\nMvav 184,160,96\ns_xor_b32 s2, s2, s20\ns_add_u32 s34, s2, 0x9000000\nMvav 184,164,104\n"
"s_bitcmp1_b32 s64, 7\ns_cselect_b32 s2, 0x2000, 0\nMvav 184,168,112\ns_xor_b32 s2, s2, s20\nSv 160,1,16,5,0\nMvav 184,172,120\nWL 0\nMvav 188,172,124\n"
"s_add_u32 s35, s2, 0xa800000\nMvav 188,168,116\nMvav 188,164,108\nMvav 188,160,100\nMvav 188,176,36\nMvav 188,180,44\nMvav 188,184,52\nMvav 188,188,60\nMvav 184,188,56\n"
"Mvav 184,184,48\nMvav 184,180,40\nMvav 184,176,32\nCV 180,180,181\nCV 181,182,183\ns_nop 1\nCV 182,188,189\nCV 183,190,191\ns_add_u32 s5, s94, 1703936\nSv 180,1,16,5,1024\n"
"CV 176,176,177\nCV 177,178,179\nCV 178,184,185\nCV 179,186,187\nSv 176,1,16,5,0\ns_cmp_eq_u32 s13, 0\ns_cbranch_scc1 .Z120\n"
"buffer_load_dwordx4 v[12:15], v91, s[28:31], s22 offen offset:0\nbuffer_load_dwordx4 v[16:19], v91, s[28:31], s23 offen offset:0\n"
"buffer_load_dwordx4 v[64:67], v89, s[16:19], s40 offen offset:0 sc0 sc1\nbuffer_load_dwordx4 v[68:71], v89, s[16:19], s40 offen offset:16 sc0 sc1\n"
"buffer_load_dwordx4 v[72:75], v89, s[16:19], s40 offen offset:1792 sc0 sc1\nbuffer_load_dwordx4 v[76:79], v89, s[16:19], s40 offen offset:1808 sc0 sc1\n"
"buffer_load_dwordx2 v[80:81], v88, s[16:19], s63 offen offset:0 sc0 sc1\nbuffer_load_dwordx2 v[82:83], v88, s[16:19], s63 offen offset:512 sc0 sc1\n"
"buffer_load_dwordx4 v[96:99], v1, s[16:19], s24 offen offset:0 sc0 sc1\nbuffer_load_dwordx4 v[100:103], v1, s[16:19], s24 offen offset:1024 sc0 sc1\n"
"buffer_load_dwordx4 v[104:107], v1, s[16:19], s25 offen offset:0 sc0 sc1\nbuffer_load_dwordx4 v[108:111], v1, s[16:19], s25 offen offset:1024 sc0 sc1\n"
"buffer_load_dwordx4 v[112:115], v1, s[16:19], s26 offen offset:0 sc0 sc1\nbuffer_load_dwordx4 v[116:119], v1, s[16:19], s26 offen offset:1024 sc0 sc1\n"
"buffer_load_dwordx4 v[120:123], v1, s[16:19], s27 offen offset:0 sc0 sc1\nbuffer_load_dwordx4 v[124:127], v1, s[16:19], s27 offen offset:1024 sc0 sc1\n"
"buffer_load_dwordx4 v[32:35], v1, s[16:19], s32 offen offset:0 sc0 sc1\nbuffer_load_dwordx4 v[36:39], v1, s[16:19], s32 offen offset:1024 sc0 sc1\n"
"buffer_load_dwordx4 v[40:43], v1, s[16:19], s33 offen offset:0 sc0 sc1\nbuffer_load_dwordx4 v[44:47], v1, s[16:19], s33 offen offset:1024 sc0 sc1\n"
"buffer_load_dwordx4 v[48:51], v1, s[16:19], s34 offen offset:0 sc0 sc1\nbuffer_load_dwordx4 v[52:55], v1, s[16:19], s34 offen offset:1024 sc0 sc1\n"
"buffer_load_dwordx4 v[56:59], v1, s[16:19], s35 offen offset:0 sc0 sc1\nbuffer_load_dwordx4 v[60:63], v1, s[16:19], s35 offen offset:1024 sc0 sc1\nWV 16\nv_xor_b32 v66, s15, v66\n"
"v_xor_b32 v70, s15, v70\nv_xor_b32 v74, s15, v74\nv_xor_b32 v78, s15, v78\nv_xor_b32 v81, s15, v81\nv_xor_b32 v83, s15, v83\nv_or3_b32 v92, v66, v70, v74\n"
"v_or3_b32 v92, v92, v78, v81\nv_or_b32 v92, v92, v83\nv_cmp_ne_u32 vcc, 0, v92\ns_nop 0\ns_cbranch_vccnz .S111\n.S112:\nv_and_b32 v92, 0x7ff, v67\nv_lshrrev_b32 v93, 16, v67\n"
"v_cmp_eq_u32 s[44:45], v92, v90\nv_cmp_eq_u32 s[46:47], v93, v90\nv_cmp_eq_u32 s[48:49], v92, v94\nv_cmp_eq_u32 s[50:51], v93, v94\nv_cndmask_b32 v12, v12, v64, s[44:45]\n"
"v_cndmask_b32 v13, v13, v65, s[44:45]\nv_cndmask_b32 v14, v14, v68, s[44:45]\nv_cndmask_b32 v15, v15, v69, s[44:45]\nv_cndmask_b32 v12, v12, v72, s[46:47]\n"
"v_cndmask_b32 v13, v13, v73, s[46:47]\nv_cndmask_b32 v14, v14, v76, s[46:47]\nv_cndmask_b32 v15, v15, v77, s[46:47]\nv_cndmask_b32 v16, v16, v64, s[48:49]\n"
"v_cndmask_b32 v17, v17, v65, s[48:49]\nv_cndmask_b32 v18, v18, v68, s[48:49]\nv_cndmask_b32 v19, v19, v69, s[48:49]\nv_cndmask_b32 v16, v16, v72, s[50:51]\n"
"v_cndmask_b32 v17, v17, v73, s[50:51]\nv_cndmask_b32 v18, v18, v76, s[50:51]\nv_cndmask_b32 v19, v19, v77, s[50:51]\nv_sub_f32 v92, 1.0, v80\nv_sub_f32 v93, 1.0, v82\nCV 92,92,93\n"
"v_and_b32 v93, 0xffff0000, v92\nv_lshlrev_b32 v92, 16, v92\nv_lshlrev_b32 v64, 16, v12\nv_and_b32 v65, 0xffff0000, v12\nMU 64,64,92\nMU 65,65,92\nv_lshlrev_b32 v66, 16, v13\n"
"v_and_b32 v67, 0xffff0000, v13\nMU 66,66,92\nMU 67,67,92\nv_lshlrev_b32 v68, 16, v14\nv_and_b32 v69, 0xffff0000, v14\nMU 68,68,92\nMU 69,69,92\nv_lshlrev_b32 v70, 16, v15\n"
"v_and_b32 v71, 0xffff0000, v15\nMU 70,70,92\nMU 71,71,92\nv_lshlrev_b32 v72, 16, v16\nv_and_b32 v73, 0xffff0000, v16\nMU 72,72,93\nMU 73,73,93\nv_lshlrev_b32 v74, 16, v17\n"
"v_and_b32 v75, 0xffff0000, v17\nMU 74,74,93\nMU 75,75,93\nv_lshlrev_b32 v76, 16, v18\nv_and_b32 v77, 0xffff0000, v18\nMU 76,76,93\nMU 77,77,93\nv_lshlrev_b32 v78, 16, v19\n"
"v_and_b32 v79, 0xffff0000, v19\nMU 78,78,93\nMU 79,79,93\ns_mov_b32 s7, 262144\nWV 0\n.Z123:\nv_max_u32 v84, v99, v103\nv_cmp_eq_u32 s[44:45], -1, v84\nv_max_u32 v84, v107, v111\n"
"v_cmp_eq_u32 s[46:47], -1, v84\nv_max_u32 v84, v115, v119\nv_cmp_eq_u32 s[48:49], -1, v84\nv_max_u32 v84, v123, v127\nv_cmp_eq_u32 s[50:51], -1, v84\nv_max_u32 v84, v35, v39\n"
"v_cmp_eq_u32 s[52:53], -1, v84\nv_max_u32 v84, v43, v47\nv_cmp_eq_u32 s[54:55], -1, v84\nv_max_u32 v84, v51, v55\nv_cmp_eq_u32 s[56:57], -1, v84\nv_max_u32 v84, v59, v63\n"
"v_cmp_eq_u32 s[58:59], -1, v84\ns_or_b64 s[0:1], s[44:45], s[46:47]\ns_or_b64 s[2:3], s[48:49], s[50:51]\ns_or_b64 s[0:1], s[0:1], s[52:53]\ns_or_b64 s[2:3], s[2:3], s[54:55]\n"
"s_or_b64 s[0:1], s[0:1], s[56:57]\ns_or_b64 s[2:3], s[2:3], s[58:59]\ns_or_b64 s[0:1], s[0:1], s[2:3]\ns_cbranch_scc1 .Z121\n.Z122:\nZvvv 136,6,96\nMvvv 136,6,104\nMvvv 136,6,112\n"
"Mvvv 136,6,120\nMvvv 136,6,32\nMvvv 136,6,40\nMvvv 136,6,48\nMvvv 136,6,56\nZvvv 140,4,96\nMvvv 140,4,104\nMvvv 140,4,112\nMvvv 140,4,120\nMvvv 140,4,32\nMvvv 140,4,40\n"
"Mvvv 140,4,48\nMvvv 140,4,56\nZvvv 144,6,100\nMvvv 144,6,108\nMvvv 144,6,116\nMvvv 144,6,124\nMvvv 144,6,36\nMvvv 144,6,44\nMvvv 144,6,52\nMvvv 144,6,60\nZvvv 148,4,100\n"
"Mvvv 148,4,108\nMvvv 148,4,116\nMvvv 148,4,124\nMvvv 148,4,36\nMvvv 148,4,44\nMvvv 148,4,52\nMvvv 148,4,60\nFM 64,80,136\nFM 65,80,137\nFM 66,80,138\nFM 67,80,139\nFM 68,80,140\n"
"FM 69,80,141\nFM 70,80,142\nFM 71,80,143\nCV 12,64,65\nCV 13,66,67\nCV 14,68,69\nCV 15,70,71\nSv 12,91,36,22,0\nFM 72,82,144\nFM 73,82,145\nFM 74,82,146\nFM 75,82,147\n"
"FM 76,82,148\nFM 77,82,149\nFM 78,82,150\nFM 79,82,151\nCV 16,72,73\nCV 17,74,75\nCV 18,76,77\nCV 19,78,79\nSv 16,91,36,23,0\n.Z120:\ns_endpgm\n.EP132:\n.S14:\n"
"s_mov_b32 s7, 262144\n.S16:\nbuffer_load_dwordx4 v[32:35], v1, s[16:19], s6 offen offset:0 sc0 sc1\nbuffer_load_dwordx4 v[36:39], v1, s[16:19], s6 offen offset:1024 sc0 sc1\n"
"buffer_load_dwordx4 v[40:43], v1, s[16:19], s6 offen offset:2048 sc0 sc1\nbuffer_load_dwordx4 v[44:47], v1, s[16:19], s6 offen offset:3072 sc0 sc1\nWV 0\nv_xor_b32 v48, s15, v33\n"
"v_xor_b32 v49, s15, v35\nv_xor_b32 v50, s15, v37\nv_xor_b32 v51, s15, v39\nv_xor_b32 v52, s15, v41\nv_xor_b32 v53, s15, v43\nv_xor_b32 v54, s15, v45\nv_xor_b32 v55, s15, v47\n"
"v_or3_b32 v48, v48, v49, v50\nv_or3_b32 v51, v51, v52, v53\nv_or3_b32 v48, v48, v54, v55\nv_or_b32 v48, v48, v51\nv_cmp_ne_u32 vcc, 0, v48\ns_nop 0\ns_cbranch_vccz .S15\n"
"s_sleep 6\ns_sub_u32 s7, s7, 1\ns_cbranch_scc0 .S16\ns_branch .S15\n.S17:\ns_mov_b32 s7, 262144\n.S19:\ns_lshl_b32 s5, s73, 9\ns_add_u32 s5, s5, 0xc049400\n"
"v_lshrrev_b32 v27, 1, v1\nbuffer_load_dwordx2 v[22:23], v27, s[16:19], s5 offen sc0 sc1\nWV 0\nv_cmp_ne_u32 vcc, s15, v23\ns_nop 0\ns_cbranch_vccz .S18\ns_sleep 6\n"
"s_sub_u32 s7, s7, 1\ns_cbranch_scc0 .S19\ns_branch .S18\n.T23:\ns_mov_b32 s62, 1\ns_call_b64 s[80:81], .RA10\ns_branch .T24\n.T25:\ns_mov_b32 s62, 1\ns_call_b64 s[80:81], .RA10\n"
"s_branch .T26\n.T28:\ns_mov_b32 s62, 0\ns_call_b64 s[80:81], .RB11\ns_branch .T29\n.T30:\ns_mov_b32 s62, 0\ns_call_b64 s[80:81], .RA10\ns_branch .T31\n.T33:\ns_mov_b32 s62, 0\n"
"s_call_b64 s[80:81], .RB11\ns_branch .T34\n.T35:\ns_mov_b32 s62, 0\ns_call_b64 s[80:81], .RA10\ns_branch .T36\n.S38:\ns_mov_b32 s7, 262144\n.S40:\nv_and_b32 v27, 0x70, v1\n"
"buffer_load_dword v27, v27, s[16:19], s61 offen offset:128 sc0 sc1\nWV 0\nv_cmp_ne_u32 vcc, s15, v27\ns_nop 0\ns_cbranch_vccz .S39\ns_sleep 6\ns_sub_u32 s7, s7, 1\n"
"s_cbranch_scc0 .S40\ns_branch .S39\n.S41:\ns_mov_b32 s7, 262144\n.S43:\nv_and_b32 v27, 0x70, v1\nbuffer_load_dword v27, v27, s[16:19], s61 offen offset:136 sc0 sc1\nWV 0\n"
"v_cmp_ne_u32 vcc, s15, v27\ns_nop 0\ns_cbranch_vccz .S42\ns_sleep 6\ns_sub_u32 s7, s7, 1\ns_cbranch_scc0 .S43\ns_branch .S42\n.S44:\ns_mov_b32 s7, 262144\n.S46:\n"
"v_and_b32 v27, 0x70, v1\nbuffer_load_dword v27, v27, s[16:19], s61 offen offset:140 sc0 sc1\nWV 0\nv_cmp_ne_u32 vcc, s15, v27\ns_nop 0\ns_cbranch_vccz .S45\ns_sleep 6\n"
"s_sub_u32 s7, s7, 1\ns_cbranch_scc0 .S46\ns_branch .S45\n.S47:\ns_mov_b32 s7, 262144\n.S49:\nv_and_b32 v27, 0x70, v1\n"
"buffer_load_dword v27, v27, s[16:19], s61 offen offset:12 sc0 sc1\nWV 0\nv_cmp_ne_u32 vcc, s15, v27\ns_nop 0\ns_cbranch_vccz .S48\ns_sleep 6\ns_sub_u32 s7, s7, 1\n"
"s_cbranch_scc0 .S49\ns_branch .S48\n.S50:\ns_mov_b32 s7, 262144\n.S52:\nv_and_b32 v27, 0x70, v1\nbuffer_load_dword v27, v27, s[16:19], s61 offen offset:8 sc0 sc1\nWV 0\n"
"v_cmp_ne_u32 vcc, s15, v27\ns_nop 0\ns_cbranch_vccz .S51\ns_sleep 6\ns_sub_u32 s7, s7, 1\ns_cbranch_scc0 .S52\ns_branch .S51\n.S53:\ns_mov_b32 s7, 262144\n.S55:\n"
"v_and_b32 v27, 0xf0, v1\nbuffer_load_dword v27, v27, s[16:19], s61 offen offset:4 sc0 sc1\nWV 0\nv_cmp_ne_u32 vcc, s15, v27\ns_nop 0\ns_cbranch_vccz .S54\ns_sleep 6\n"
"s_sub_u32 s7, s7, 1\ns_cbranch_scc0 .S55\ns_branch .S54\n.S57:\ns_mov_b32 s7, 262144\n.S59:\nv_lshrrev_b32 v104, 9, v1\nv_mul_u32_u24 v104, 0x18000, v104\n"
"v_lshlrev_b32 v104, 8, v104\nv_and_b32 v105, 0x1f0, v1\nv_add_u32 v104, v104, v105\ns_mov_b32 s6, 0x15c0000\nbuffer_load_dword v96, v104, s[16:19], s6 offen offset:4 sc0 sc1\n"
"s_mov_b32 s6, 0x15c2000\nbuffer_load_dword v97, v104, s[16:19], s6 offen offset:4 sc0 sc1\ns_mov_b32 s6, 0x45c0000\n"
"buffer_load_dword v98, v104, s[16:19], s6 offen offset:4 sc0 sc1\ns_mov_b32 s6, 0x45c2000\nbuffer_load_dword v99, v104, s[16:19], s6 offen offset:4 sc0 sc1\n"
"s_mov_b32 s6, 0x75c0000\nbuffer_load_dword v100, v104, s[16:19], s6 offen offset:4 sc0 sc1\ns_mov_b32 s6, 0x75c2000\n"
"buffer_load_dword v101, v104, s[16:19], s6 offen offset:4 sc0 sc1\ns_mov_b32 s6, 0xa5c0000\nbuffer_load_dword v102, v104, s[16:19], s6 offen offset:4 sc0 sc1\n"
"s_mov_b32 s6, 0xa5c2000\nbuffer_load_dword v103, v104, s[16:19], s6 offen offset:4 sc0 sc1\nWV 0\nv_xor_b32 v96, s15, v96\nv_xor_b32 v97, s15, v97\nv_xor_b32 v98, s15, v98\n"
"v_xor_b32 v99, s15, v99\nv_xor_b32 v100, s15, v100\nv_xor_b32 v101, s15, v101\nv_xor_b32 v102, s15, v102\nv_xor_b32 v103, s15, v103\nv_min_u32 v96, v96, v97\n"
"v_min_u32 v98, v98, v99\nv_min_u32 v100, v100, v101\nv_min_u32 v102, v102, v103\nv_or3_b32 v96, v96, v98, v100\nv_or_b32 v96, v96, v102\nv_cmp_ne_u32 vcc, 0, v96\ns_nop 0\n"
"s_cbranch_vccz .S58\ns_sleep 6\ns_sub_u32 s7, s7, 1\ns_cbranch_scc0 .S59\ns_branch .S58\n.FF56:\nv_lshrrev_b32 v104, 9, v1\nv_mul_u32_u24 v104, 0x18000, v104\n"
"v_lshlrev_b32 v104, 8, v104\nv_and_b32 v105, 0x1f0, v1\nv_add_u32 v104, v104, v105\ns_mov_b32 s6, 0x15c0000\nbuffer_load_dword v96, v104, s[16:19], s6 offen offset:4 sc0 sc1\n"
"s_mov_b32 s6, 0x15c2000\nbuffer_load_dword v97, v104, s[16:19], s6 offen offset:4 sc0 sc1\ns_mov_b32 s6, 0x45c0000\n"
"buffer_load_dword v98, v104, s[16:19], s6 offen offset:4 sc0 sc1\ns_mov_b32 s6, 0x45c2000\nbuffer_load_dword v99, v104, s[16:19], s6 offen offset:4 sc0 sc1\n"
"s_mov_b32 s6, 0x75c0000\nbuffer_load_dword v100, v104, s[16:19], s6 offen offset:4 sc0 sc1\ns_mov_b32 s6, 0x75c2000\n"
"buffer_load_dword v101, v104, s[16:19], s6 offen offset:4 sc0 sc1\ns_mov_b32 s6, 0xa5c0000\nbuffer_load_dword v102, v104, s[16:19], s6 offen offset:4 sc0 sc1\n"
"s_mov_b32 s6, 0xa5c2000\nbuffer_load_dword v103, v104, s[16:19], s6 offen offset:4 sc0 sc1\ns_mov_b32 s90, 0\ns_swappc_b64 s[8:9], s[80:81]\nv_xor_b32 v96, s15, v96\n"
"v_xor_b32 v97, s15, v97\nv_xor_b32 v98, s15, v98\nv_xor_b32 v99, s15, v99\nv_xor_b32 v100, s15, v100\nv_xor_b32 v101, s15, v101\nv_xor_b32 v102, s15, v102\n"
"v_xor_b32 v103, s15, v103\nv_min_u32 v96, v96, v97\nv_min_u32 v98, v98, v99\nv_min_u32 v100, v100, v101\nv_min_u32 v102, v102, v103\nv_or3_b32 v96, v96, v98, v100\n"
"v_or_b32 v96, v96, v102\nv_cmp_ne_u32 vcc, 0, v96\ns_nop 0\ns_cbranch_vccnz .S57\n.S58:\ns_and_b32 s4, s13, 1\ns_lshl1_add_u32 s4, s4, s11\ns_lshl2_add_u32 s4, s12, s4\n"
"s_lshr_b32 s5, s71, 16\ns_cmp_lt_u32 s4, 28\ns_cselect_b32 s5, s71, s5\ns_cselect_b32 s3, 0, 28\ns_and_b32 s5, s5, 0x7ff\ns_sub_u32 s3, s4, s3\ns_mul_i32 s2, s4, 7168\n"
"s_add_u32 s2, s2, 0xc04c800\ns_add_u32 s6, s2, 4096\nbuffer_load_dwordx4 v[96:99], v1, s[16:19], s2 offen offset:0 sc0 sc1\n"
"buffer_load_dwordx4 v[100:103], v1, s[16:19], s2 offen offset:1024 sc0 sc1\nbuffer_load_dwordx4 v[104:107], v1, s[16:19], s2 offen offset:2048 sc0 sc1\n"
"buffer_load_dwordx4 v[108:111], v1, s[16:19], s2 offen offset:3072 sc0 sc1\nbuffer_load_dwordx4 v[112:115], v1, s[16:19], s6 offen offset:0 sc0 sc1\n"
"buffer_load_dwordx4 v[116:119], v1, s[16:19], s6 offen offset:1024 sc0 sc1\nbuffer_load_dwordx4 v[120:123], v1, s[16:19], s6 offen offset:2048 sc0 sc1\nv_lshrrev_b32 v127, 1, v1\n"
"v_and_b32 v127, 24, v127\ns_mul_i32 s2, s5, 896\ns_lshl_b32 s3, s3, 5\ns_add_u32 s2, s2, s3\ns_lshl2_add_u32 s5, s5, 0xc03e000\n"
"buffer_load_dwordx2 v[124:125], v127, s[28:31], s2 offen\nbuffer_load_dword v126, off, s[16:19], s5 sc0 sc1\ns_mov_b32 s90, 0\ns_swappc_b64 s[8:9], s[80:81]\nAD 96,96,100\n"
"AD 97,97,101\nAD 98,98,102\nAD 99,99,103\nAD 104,104,108\nAD 105,105,109\nAD 106,106,110\nAD 107,107,111\nAD 112,112,116\nAD 113,113,117\nAD 114,114,118\nAD 115,115,119\n"
"AD 96,96,104\nAD 97,97,105\nAD 98,98,106\nAD 99,99,107\nAD 112,112,120\nAD 113,113,121\nAD 114,114,122\nAD 115,115,123\nAD 96,96,112\nAD 97,97,113\nAD 98,98,114\nAD 99,99,115\n"
"v_mov_b32 v100, v96\nv_mov_b32 v101, v97\nv_mov_b32 v102, v98\nv_mov_b32 v103, v99\nv_permlane32_swap_b32 v96, v100\nv_permlane32_swap_b32 v97, v101\n"
"v_permlane32_swap_b32 v98, v102\nv_permlane32_swap_b32 v99, v103\nAD 96,96,100\nAD 97,97,101\nAD 98,98,102\nAD 99,99,103\nv_mov_b32 v100, v96\nv_mov_b32 v101, v97\n"
"v_mov_b32 v102, v98\nv_mov_b32 v103, v99\nv_permlane16_swap_b32 v96, v100\nv_permlane16_swap_b32 v97, v101\nv_permlane16_swap_b32 v98, v102\nv_permlane16_swap_b32 v99, v103\n"
"AD 96,96,100\nAD 97,97,101\nAD 98,98,102\nAD 99,99,103\nv_add_f32_dpp v96, v96, v96 row_ror:8 row_mask:0xf bank_mask:0xf\n"
"v_add_f32_dpp v97, v97, v97 row_ror:8 row_mask:0xf bank_mask:0xf\nv_add_f32_dpp v98, v98, v98 row_ror:8 row_mask:0xf bank_mask:0xf\n"
"v_add_f32_dpp v99, v99, v99 row_ror:8 row_mask:0xf bank_mask:0xf\nv_add_f32_dpp v96, v96, v96 row_ror:4 row_mask:0xf bank_mask:0xf\n"
"v_add_f32_dpp v97, v97, v97 row_ror:4 row_mask:0xf bank_mask:0xf\nv_add_f32_dpp v98, v98, v98 row_ror:4 row_mask:0xf bank_mask:0xf\n"
"v_add_f32_dpp v99, v99, v99 row_ror:4 row_mask:0xf bank_mask:0xf\nv_mul_f32 v126, 0xbfb8aa3b, v126\nEX 126,126\nCV 96,96,97\nv_add_f32 v126, 1.0, v126\nv_rcp_f32 v126, v126\n"
"CV 98,98,99\nCV 126,126,126\nv_lshlrev_b32 v126, 16, v126\nv_and_b32 v97, 0xffff0000, v96\nv_lshlrev_b32 v96, 16, v96\nv_and_b32 v99, 0xffff0000, v98\nv_lshlrev_b32 v98, 16, v98\n"
"MU 96,96,126\nMU 97,97,126\nMU 98,98,126\nMU 99,99,126\nCV 96,96,97\nCV 98,98,99\nv_and_b32 v97, 0xffff0000, v96\nv_lshlrev_b32 v96, 16, v96\nv_and_b32 v99, 0xffff0000, v98\n"
"v_lshlrev_b32 v98, 16, v98\nv_lshlrev_b32 v100, 16, v124\nv_and_b32 v101, 0xffff0000, v124\nv_lshlrev_b32 v102, 16, v125\nv_and_b32 v103, 0xffff0000, v125\nAD 96,96,100\n"
"AD 97,97,101\nAD 98,98,102\nAD 99,99,103\nCV 96,96,97\nCV 97,98,99\nv_mov_b32 v98, s15\nv_mov_b32 v99, s71\ns_and_b32 s4, s13, 1\ns_lshl1_add_u32 s4, s4, s11\n"
"s_lshl2_add_u32 s4, s12, s4\ns_lshr_b32 s5, s71, 16\ns_cmp_lt_u32 s4, 28\ns_cselect_b32 s5, s71, s5\ns_cselect_b32 s3, 0, 28\ns_and_b32 s5, s5, 0x7ff\ns_sub_u32 s3, s4, s3\n"
"v_mov_b32 v100, 0x40000000\nv_cmp_gt_u32 vcc, 64, v1\ns_lshl_b32 s4, s4, 6\ns_add_u32 s4, s4, 0xc04b800\nv_cndmask_b32 v100, v100, v1, vcc\nSv 96,100,16,4,0\ns_mov_b32 s90, 100\n"
"s_swappc_b64 s[8:9], s[80:81]\n.T64:\ns_mov_b32 s62, 0\ns_call_b64 s[80:81], .RB11\ns_branch .T65\n.T66:\ns_mov_b32 s62, 0\ns_call_b64 s[80:81], .RA10\ns_branch .T67\n.T70:\n"
"s_mov_b32 s62, 0\ns_call_b64 s[80:81], .RB11\ns_branch .T71\n.T72:\ns_mov_b32 s62, 0\ns_call_b64 s[80:81], .RA10\ns_branch .T73\n.T75:\ns_mov_b32 s62, 0\n"
"s_call_b64 s[80:81], .RB11\ns_branch .T76\n.T77:\ns_mov_b32 s62, 0\ns_call_b64 s[80:81], .RA10\ns_branch .T78\n.S83:\ns_mov_b32 s7, 262144\n.S85:\n"
"buffer_load_dwordx4 v[100:103], v108, s[16:19], s65 offen offset:0 sc0 sc1\nbuffer_load_dwordx4 v[104:107], v108, s[16:19], s65 offen offset:256 sc0 sc1\nWV 0\n"
"v_xor_b32 v109, s15, v102\nv_xor_b32 v110, s15, v106\nv_or_b32 v109, v109, v110\nv_cmp_ne_u32 vcc, 0, v109\ns_nop 0\ns_cbranch_vccz .S84\ns_sleep 6\ns_sub_u32 s7, s7, 1\n"
"s_cbranch_scc0 .S85\ns_branch .S84\n.D81:\nv_mov_b32 v114, 0\nv_mov_b32 v115, 0\ns_mov_b32 s63, 0xff800000\ns_mov_b32 s67, 1\ns_branch .D82\n.T87:\ns_lshl3_add_u32 s62, s67, 2\n"
"s_call_b64 s[80:81], .RB11\ns_cmp_eq_u32 s67, 0\ns_cbranch_scc1 .T88\nSU 20,100,88\nv_min_f32 v20, 0, v20\nEX 20,20\ns_nop 0\nFM 86,101,20\nSU 21,104,89\nv_min_f32 v21, 0, v21\n"
"EX 21,21\ns_nop 0\nFM 87,105,21\nAR 22,0\nv_and_b32 v23, 0xffff0000, v22\nv_lshlrev_b32 v22, 16, v22\nMU 22,22,20\nMU 23,23,20\nCV 22,22,23\nAW 0,22\nAR 22,1\n"
"v_and_b32 v23, 0xffff0000, v22\nv_lshlrev_b32 v22, 16, v22\nMU 22,22,20\nMU 23,23,20\nCV 22,22,23\nAW 1,22\nAR 22,2\nv_and_b32 v23, 0xffff0000, v22\nv_lshlrev_b32 v22, 16, v22\n"
"MU 22,22,20\nMU 23,23,20\nCV 22,22,23\nAW 2,22\nAR 22,3\nv_and_b32 v23, 0xffff0000, v22\nv_lshlrev_b32 v22, 16, v22\nMU 22,22,20\nMU 23,23,20\nCV 22,22,23\nAW 3,22\nAR 22,4\n"
"v_and_b32 v23, 0xffff0000, v22\nv_lshlrev_b32 v22, 16, v22\nMU 22,22,21\nMU 23,23,21\nCV 22,22,23\nAW 4,22\nAR 22,5\nv_and_b32 v23, 0xffff0000, v22\nv_lshlrev_b32 v22, 16, v22\n"
"MU 22,22,21\nMU 23,23,21\nCV 22,22,23\nAW 5,22\nAR 22,6\nv_and_b32 v23, 0xffff0000, v22\nv_lshlrev_b32 v22, 16, v22\nMU 22,22,21\nMU 23,23,21\nCV 22,22,23\nAW 6,22\nAR 22,7\n"
"v_and_b32 v23, 0xffff0000, v22\nv_lshlrev_b32 v22, 16, v22\nMU 22,22,21\nMU 23,23,21\nCV 22,22,23\nAW 7,22\nAR 22,8\nv_and_b32 v23, 0xffff0000, v22\nv_lshlrev_b32 v22, 16, v22\n"
"MU 22,22,20\nMU 23,23,20\nCV 22,22,23\nAW 8,22\nAR 22,9\nv_and_b32 v23, 0xffff0000, v22\nv_lshlrev_b32 v22, 16, v22\nMU 22,22,20\nMU 23,23,20\nCV 22,22,23\nAW 9,22\nAR 22,10\n"
"v_and_b32 v23, 0xffff0000, v22\nv_lshlrev_b32 v22, 16, v22\nMU 22,22,20\nMU 23,23,20\nCV 22,22,23\nAW 10,22\nAR 22,11\nv_and_b32 v23, 0xffff0000, v22\nv_lshlrev_b32 v22, 16, v22\n"
"MU 22,22,20\nMU 23,23,20\nCV 22,22,23\nAW 11,22\nAR 22,12\nv_and_b32 v23, 0xffff0000, v22\nv_lshlrev_b32 v22, 16, v22\nMU 22,22,21\nMU 23,23,21\nCV 22,22,23\nAW 12,22\nAR 22,13\n"
"v_and_b32 v23, 0xffff0000, v22\nv_lshlrev_b32 v22, 16, v22\nMU 22,22,21\nMU 23,23,21\nCV 22,22,23\nAW 13,22\nAR 22,14\nv_and_b32 v23, 0xffff0000, v22\nv_lshlrev_b32 v22, 16, v22\n"
"MU 22,22,21\nMU 23,23,21\nCV 22,22,23\nAW 14,22\nAR 22,15\nv_and_b32 v23, 0xffff0000, v22\nv_lshlrev_b32 v22, 16, v22\nMU 22,22,21\nMU 23,23,21\nCV 22,22,23\nAW 15,22\nAR 22,16\n"
"v_and_b32 v23, 0xffff0000, v22\nv_lshlrev_b32 v22, 16, v22\nMU 22,22,20\nMU 23,23,20\nCV 22,22,23\nAW 16,22\nAR 22,17\nv_and_b32 v23, 0xffff0000, v22\nv_lshlrev_b32 v22, 16, v22\n"
"MU 22,22,20\nMU 23,23,20\nCV 22,22,23\nAW 17,22\nAR 22,18\nv_and_b32 v23, 0xffff0000, v22\nv_lshlrev_b32 v22, 16, v22\nMU 22,22,20\nMU 23,23,20\nCV 22,22,23\nAW 18,22\nAR 22,19\n"
"v_and_b32 v23, 0xffff0000, v22\nv_lshlrev_b32 v22, 16, v22\nMU 22,22,20\nMU 23,23,20\nCV 22,22,23\nAW 19,22\nAR 22,20\nv_and_b32 v23, 0xffff0000, v22\nv_lshlrev_b32 v22, 16, v22\n"
"MU 22,22,21\nMU 23,23,21\nCV 22,22,23\nAW 20,22\nAR 22,21\nv_and_b32 v23, 0xffff0000, v22\nv_lshlrev_b32 v22, 16, v22\nMU 22,22,21\nMU 23,23,21\nCV 22,22,23\nAW 21,22\nAR 22,22\n"
"v_and_b32 v23, 0xffff0000, v22\nv_lshlrev_b32 v22, 16, v22\nMU 22,22,21\nMU 23,23,21\nCV 22,22,23\nAW 22,22\nAR 22,23\nv_and_b32 v23, 0xffff0000, v22\nv_lshlrev_b32 v22, 16, v22\n"
"MU 22,22,21\nMU 23,23,21\nCV 22,22,23\nAW 23,22\nAR 22,24\nv_and_b32 v23, 0xffff0000, v22\nv_lshlrev_b32 v22, 16, v22\nMU 22,22,20\nMU 23,23,20\nCV 22,22,23\nAW 24,22\nAR 22,25\n"
"v_and_b32 v23, 0xffff0000, v22\nv_lshlrev_b32 v22, 16, v22\nMU 22,22,20\nMU 23,23,20\nCV 22,22,23\nAW 25,22\nAR 22,26\nv_and_b32 v23, 0xffff0000, v22\nv_lshlrev_b32 v22, 16, v22\n"
"MU 22,22,20\nMU 23,23,20\nCV 22,22,23\nAW 26,22\nAR 22,27\nv_and_b32 v23, 0xffff0000, v22\nv_lshlrev_b32 v22, 16, v22\nMU 22,22,20\nMU 23,23,20\nCV 22,22,23\nAW 27,22\nAR 22,28\n"
"v_and_b32 v23, 0xffff0000, v22\nv_lshlrev_b32 v22, 16, v22\nMU 22,22,21\nMU 23,23,21\nCV 22,22,23\nAW 28,22\nAR 22,29\nv_and_b32 v23, 0xffff0000, v22\nv_lshlrev_b32 v22, 16, v22\n"
"MU 22,22,21\nMU 23,23,21\nCV 22,22,23\nAW 29,22\nAR 22,30\nv_and_b32 v23, 0xffff0000, v22\nv_lshlrev_b32 v22, 16, v22\nMU 22,22,21\nMU 23,23,21\nCV 22,22,23\nAW 30,22\nAR 22,31\n"
"v_and_b32 v23, 0xffff0000, v22\nv_lshlrev_b32 v22, 16, v22\nMU 22,22,21\nMU 23,23,21\nCV 22,22,23\nAW 31,22\nAR 22,32\nv_and_b32 v23, 0xffff0000, v22\nv_lshlrev_b32 v22, 16, v22\n"
"MU 22,22,20\nMU 23,23,20\nCV 22,22,23\nAW 32,22\nAR 22,33\nv_and_b32 v23, 0xffff0000, v22\nv_lshlrev_b32 v22, 16, v22\nMU 22,22,20\nMU 23,23,20\nCV 22,22,23\nAW 33,22\nAR 22,34\n"
"v_and_b32 v23, 0xffff0000, v22\nv_lshlrev_b32 v22, 16, v22\nMU 22,22,20\nMU 23,23,20\nCV 22,22,23\nAW 34,22\nAR 22,35\nv_and_b32 v23, 0xffff0000, v22\nv_lshlrev_b32 v22, 16, v22\n"
"MU 22,22,20\nMU 23,23,20\nCV 22,22,23\nAW 35,22\nAR 22,36\nv_and_b32 v23, 0xffff0000, v22\nv_lshlrev_b32 v22, 16, v22\nMU 22,22,21\nMU 23,23,21\nCV 22,22,23\nAW 36,22\nAR 22,37\n"
"v_and_b32 v23, 0xffff0000, v22\nv_lshlrev_b32 v22, 16, v22\nMU 22,22,21\nMU 23,23,21\nCV 22,22,23\nAW 37,22\nAR 22,38\nv_and_b32 v23, 0xffff0000, v22\nv_lshlrev_b32 v22, 16, v22\n"
"MU 22,22,21\nMU 23,23,21\nCV 22,22,23\nAW 38,22\nAR 22,39\nv_and_b32 v23, 0xffff0000, v22\nv_lshlrev_b32 v22, 16, v22\nMU 22,22,21\nMU 23,23,21\nCV 22,22,23\nAW 39,22\nAR 22,40\n"
"v_and_b32 v23, 0xffff0000, v22\nv_lshlrev_b32 v22, 16, v22\nMU 22,22,20\nMU 23,23,20\nCV 22,22,23\nAW 40,22\nAR 22,41\nv_and_b32 v23, 0xffff0000, v22\nv_lshlrev_b32 v22, 16, v22\n"
"MU 22,22,20\nMU 23,23,20\nCV 22,22,23\nAW 41,22\nAR 22,42\nv_and_b32 v23, 0xffff0000, v22\nv_lshlrev_b32 v22, 16, v22\nMU 22,22,20\nMU 23,23,20\nCV 22,22,23\nAW 42,22\nAR 22,43\n"
"v_and_b32 v23, 0xffff0000, v22\nv_lshlrev_b32 v22, 16, v22\nMU 22,22,20\nMU 23,23,20\nCV 22,22,23\nAW 43,22\nAR 22,44\nv_and_b32 v23, 0xffff0000, v22\nv_lshlrev_b32 v22, 16, v22\n"
"MU 22,22,21\nMU 23,23,21\nCV 22,22,23\nAW 44,22\nAR 22,45\nv_and_b32 v23, 0xffff0000, v22\nv_lshlrev_b32 v22, 16, v22\nMU 22,22,21\nMU 23,23,21\nCV 22,22,23\nAW 45,22\nAR 22,46\n"
"v_and_b32 v23, 0xffff0000, v22\nv_lshlrev_b32 v22, 16, v22\nMU 22,22,21\nMU 23,23,21\nCV 22,22,23\nAW 46,22\nAR 22,47\nv_and_b32 v23, 0xffff0000, v22\nv_lshlrev_b32 v22, 16, v22\n"
"MU 22,22,21\nMU 23,23,21\nCV 22,22,23\nAW 47,22\nAR 22,48\nv_and_b32 v23, 0xffff0000, v22\nv_lshlrev_b32 v22, 16, v22\nMU 22,22,20\nMU 23,23,20\nCV 22,22,23\nAW 48,22\nAR 22,49\n"
"v_and_b32 v23, 0xffff0000, v22\nv_lshlrev_b32 v22, 16, v22\nMU 22,22,20\nMU 23,23,20\nCV 22,22,23\nAW 49,22\nAR 22,50\nv_and_b32 v23, 0xffff0000, v22\nv_lshlrev_b32 v22, 16, v22\n"
"MU 22,22,20\nMU 23,23,20\nCV 22,22,23\nAW 50,22\nAR 22,51\nv_and_b32 v23, 0xffff0000, v22\nv_lshlrev_b32 v22, 16, v22\nMU 22,22,20\nMU 23,23,20\nCV 22,22,23\nAW 51,22\nAR 22,52\n"
"v_and_b32 v23, 0xffff0000, v22\nv_lshlrev_b32 v22, 16, v22\nMU 22,22,21\nMU 23,23,21\nCV 22,22,23\nAW 52,22\nAR 22,53\nv_and_b32 v23, 0xffff0000, v22\nv_lshlrev_b32 v22, 16, v22\n"
"MU 22,22,21\nMU 23,23,21\nCV 22,22,23\nAW 53,22\nAR 22,54\nv_and_b32 v23, 0xffff0000, v22\nv_lshlrev_b32 v22, 16, v22\nMU 22,22,21\nMU 23,23,21\nCV 22,22,23\nAW 54,22\nAR 22,55\n"
"v_and_b32 v23, 0xffff0000, v22\nv_lshlrev_b32 v22, 16, v22\nMU 22,22,21\nMU 23,23,21\nCV 22,22,23\nAW 55,22\nAR 22,56\nv_and_b32 v23, 0xffff0000, v22\nv_lshlrev_b32 v22, 16, v22\n"
"MU 22,22,20\nMU 23,23,20\nCV 22,22,23\nAW 56,22\nAR 22,57\nv_and_b32 v23, 0xffff0000, v22\nv_lshlrev_b32 v22, 16, v22\nMU 22,22,20\nMU 23,23,20\nCV 22,22,23\nAW 57,22\nAR 22,58\n"
"v_and_b32 v23, 0xffff0000, v22\nv_lshlrev_b32 v22, 16, v22\nMU 22,22,20\nMU 23,23,20\nCV 22,22,23\nAW 58,22\nAR 22,59\nv_and_b32 v23, 0xffff0000, v22\nv_lshlrev_b32 v22, 16, v22\n"
"MU 22,22,20\nMU 23,23,20\nCV 22,22,23\nAW 59,22\nAR 22,60\nv_and_b32 v23, 0xffff0000, v22\nv_lshlrev_b32 v22, 16, v22\nMU 22,22,21\nMU 23,23,21\nCV 22,22,23\nAW 60,22\nAR 22,61\n"
"v_and_b32 v23, 0xffff0000, v22\nv_lshlrev_b32 v22, 16, v22\nMU 22,22,21\nMU 23,23,21\nCV 22,22,23\nAW 61,22\nAR 22,62\nv_and_b32 v23, 0xffff0000, v22\nv_lshlrev_b32 v22, 16, v22\n"
"MU 22,22,21\nMU 23,23,21\nCV 22,22,23\nAW 62,22\nAR 22,63\nv_and_b32 v23, 0xffff0000, v22\nv_lshlrev_b32 v22, 16, v22\nMU 22,22,21\nMU 23,23,21\nCV 22,22,23\nAW 63,22\n"
"v_lshrrev_b32 v22, 8, v1\nv_bfe_u32 v23, v1, 6, 2\nv_cmp_eq_u32 vcc, v22, v23\nv_and_b32 v22, 16, v1\nv_mov_b32 v23, 0x3f80\nv_cndmask_b32 v23, 0, v23, vcc\n"
"v_lshlrev_b32 v23, v22, v23\nv_and_b32 v22, 32, v1\nv_cmp_eq_u32 vcc, 0, v22\nv_mov_b32 v112, 0\nv_mov_b32 v113, 0\nv_cndmask_b32 v114, 0, v23, vcc\nv_sub_u32 v115, v23, v114\n"
"v_mov_b32 v116, 0\nv_mov_b32 v117, 0\ns_nop 1\nMvva 128,114,0\nMvva 136,112,0\nMvva 140,112,4\nMvva 132,114,4\nMvva 144,114,8\nMvva 152,112,8\nMvva 156,112,12\nMvva 148,114,12\n"
"Mvva 160,114,16\nMvva 168,112,16\nMvva 172,112,20\nMvva 164,114,20\nMvva 176,114,24\nMvva 184,112,24\nMvva 188,112,28\nMvva 180,114,28\nMvva 192,114,32\nMvva 200,112,32\n"
"Mvva 204,112,36\nMvva 196,114,36\nMvva 208,114,40\nMvva 216,112,40\nMvva 220,112,44\nMvva 212,114,44\nMvva 224,114,48\nMvva 232,112,48\nMvva 236,112,52\nMvva 228,114,52\n"
"Mvva 240,114,56\nMvva 248,112,56\nMvva 252,112,60\nMvva 244,114,60\ns_nop 7\ns_branch .T88\n.S90:\ns_mov_b32 s7, 262144\n.S92:\ns_add_u32 s0, s92, 0\n"
"buffer_load_dwordx4 v[208:211], v1, s[16:19], s0 offen offset:0 sc0 sc1\nbuffer_load_dwordx4 v[212:215], v1, s[16:19], s0 offen offset:1024 sc0 sc1\nWV 0\n"
"v_max_u32 v21, v211, v215\nv_cmp_eq_u32 vcc, -1, v21\ns_nop 0\ns_cbranch_vccz .S91\ns_sleep 6\ns_sub_u32 s7, s7, 1\ns_cbranch_scc0 .S92\ns_branch .S91\n.S93:\n"
"s_mov_b32 s7, 262144\n.S95:\ns_add_u32 s0, s92, 131072\nbuffer_load_dwordx4 v[216:219], v1, s[16:19], s0 offen offset:0 sc0 sc1\n"
"buffer_load_dwordx4 v[220:223], v1, s[16:19], s0 offen offset:1024 sc0 sc1\nWV 0\nv_max_u32 v21, v219, v223\nv_cmp_eq_u32 vcc, -1, v21\ns_nop 0\ns_cbranch_vccz .S94\ns_sleep 6\n"
"s_sub_u32 s7, s7, 1\ns_cbranch_scc0 .S95\ns_branch .S94\n.S96:\ns_mov_b32 s7, 262144\n.S98:\ns_add_u32 s0, s92, 262144\n"
"buffer_load_dwordx4 v[224:227], v1, s[16:19], s0 offen offset:0 sc0 sc1\nbuffer_load_dwordx4 v[228:231], v1, s[16:19], s0 offen offset:1024 sc0 sc1\nWV 0\n"
"v_max_u32 v21, v227, v231\nv_cmp_eq_u32 vcc, -1, v21\ns_nop 0\ns_cbranch_vccz .S97\ns_sleep 6\ns_sub_u32 s7, s7, 1\ns_cbranch_scc0 .S98\ns_branch .S97\n.S99:\n"
"s_mov_b32 s7, 262144\n.S101:\ns_add_u32 s0, s92, 393216\nbuffer_load_dwordx4 v[208:211], v1, s[16:19], s0 offen offset:0 sc0 sc1\n"
"buffer_load_dwordx4 v[212:215], v1, s[16:19], s0 offen offset:1024 sc0 sc1\nWV 0\nv_max_u32 v21, v211, v215\nv_cmp_eq_u32 vcc, -1, v21\ns_nop 0\ns_cbranch_vccz .S100\ns_sleep 6\n"
"s_sub_u32 s7, s7, 1\ns_cbranch_scc0 .S101\ns_branch .S100\n.S102:\ns_mov_b32 s7, 262144\n.S104:\ns_add_u32 s0, s92, 524288\n"
"buffer_load_dwordx4 v[216:219], v1, s[16:19], s0 offen offset:0 sc0 sc1\nbuffer_load_dwordx4 v[220:223], v1, s[16:19], s0 offen offset:1024 sc0 sc1\nWV 0\n"
"v_max_u32 v21, v219, v223\nv_cmp_eq_u32 vcc, -1, v21\ns_nop 0\ns_cbranch_vccz .S103\ns_sleep 6\ns_sub_u32 s7, s7, 1\ns_cbranch_scc0 .S104\ns_branch .S103\n.S105:\n"
"s_mov_b32 s7, 262144\n.S107:\ns_add_u32 s0, s92, 655360\nbuffer_load_dwordx4 v[224:227], v1, s[16:19], s0 offen offset:0 sc0 sc1\n"
"buffer_load_dwordx4 v[228:231], v1, s[16:19], s0 offen offset:1024 sc0 sc1\nWV 0\nv_max_u32 v21, v227, v231\nv_cmp_eq_u32 vcc, -1, v21\ns_nop 0\ns_cbranch_vccz .S106\ns_sleep 6\n"
"s_sub_u32 s7, s7, 1\ns_cbranch_scc0 .S107\ns_branch .S106\n.S108:\ns_mov_b32 s7, 262144\n.S110:\ns_add_u32 s0, s92, 786432\n"
"buffer_load_dwordx4 v[208:211], v1, s[16:19], s0 offen offset:0 sc0 sc1\nbuffer_load_dwordx4 v[212:215], v1, s[16:19], s0 offen offset:1024 sc0 sc1\nWV 0\n"
"v_max_u32 v21, v211, v215\nv_cmp_eq_u32 vcc, -1, v21\ns_nop 0\ns_cbranch_vccz .S109\ns_sleep 6\ns_sub_u32 s7, s7, 1\ns_cbranch_scc0 .S110\ns_branch .S109\n.S111:\n"
"s_mov_b32 s7, 262144\n.S113:\nbuffer_load_dwordx4 v[64:67], v89, s[16:19], s40 offen offset:0 sc0 sc1\nbuffer_load_dwordx4 v[68:71], v89, s[16:19], s40 offen offset:16 sc0 sc1\n"
"buffer_load_dwordx4 v[72:75], v89, s[16:19], s40 offen offset:1792 sc0 sc1\nbuffer_load_dwordx4 v[76:79], v89, s[16:19], s40 offen offset:1808 sc0 sc1\n"
"buffer_load_dwordx2 v[80:81], v88, s[16:19], s63 offen offset:0 sc0 sc1\nbuffer_load_dwordx2 v[82:83], v88, s[16:19], s63 offen offset:512 sc0 sc1\nWV 0\nv_xor_b32 v66, s15, v66\n"
"v_xor_b32 v70, s15, v70\nv_xor_b32 v74, s15, v74\nv_xor_b32 v78, s15, v78\nv_xor_b32 v81, s15, v81\nv_xor_b32 v83, s15, v83\nv_or3_b32 v92, v66, v70, v74\n"
"v_or3_b32 v92, v92, v78, v81\nv_or_b32 v92, v92, v83\nv_cmp_ne_u32 vcc, 0, v92\ns_nop 0\ns_cbranch_vccz .S112\ns_sleep 6\ns_sub_u32 s7, s7, 1\ns_cbranch_scc0 .S113\n"
"s_branch .S112\n.S114:\ns_mov_b32 s7, 262144\n.S116:\nbuffer_load_dword v27, off, s[16:19], s60 offset:512 sc0 sc1\nWV 0\nv_cmp_ne_u32 vcc, s15, v27\ns_nop 0\n"
"s_cbranch_vccz .S115\ns_sleep 6\ns_sub_u32 s7, s7, 1\ns_cbranch_scc0 .S116\ns_branch .S115\n.S117:\ns_mov_b32 s7, 262144\n.S119:\n"
"buffer_load_dword v27, v24, s[16:19], s41 offen sc0 sc1\nWV 0\nv_xor_b32 v27, s15, v27\nv_and_b32 v26, 0xffffdfff, v27\nv_cmp_ne_u32 s[64:65], 0, v27\nv_cmp_ne_u32 vcc, 0, v26\n"
"s_nop 0\ns_cbranch_vccz .S118\ns_sleep 6\ns_sub_u32 s7, s7, 1\ns_cbranch_scc0 .S119\ns_branch .S118\n.Z121:\ns_sub_u32 s7, s7, 1\ns_cbranch_scc1 .Z122\ns_cmp_lg_u64 s[44:45], 0\n"
"s_cbranch_scc0 .Z124\nbuffer_load_dwordx4 v[96:99], v1, s[16:19], s24 offen offset:0 sc0 sc1\nbuffer_load_dwordx4 v[100:103], v1, s[16:19], s24 offen offset:1024 sc0 sc1\n.Z124:\n"
"s_cmp_lg_u64 s[46:47], 0\ns_cbranch_scc0 .Z125\nbuffer_load_dwordx4 v[104:107], v1, s[16:19], s25 offen offset:0 sc0 sc1\n"
"buffer_load_dwordx4 v[108:111], v1, s[16:19], s25 offen offset:1024 sc0 sc1\n.Z125:\ns_cmp_lg_u64 s[48:49], 0\ns_cbranch_scc0 .Z126\n"
"buffer_load_dwordx4 v[112:115], v1, s[16:19], s26 offen offset:0 sc0 sc1\nbuffer_load_dwordx4 v[116:119], v1, s[16:19], s26 offen offset:1024 sc0 sc1\n.Z126:\n"
"s_cmp_lg_u64 s[50:51], 0\ns_cbranch_scc0 .Z127\nbuffer_load_dwordx4 v[120:123], v1, s[16:19], s27 offen offset:0 sc0 sc1\n"
"buffer_load_dwordx4 v[124:127], v1, s[16:19], s27 offen offset:1024 sc0 sc1\n.Z127:\ns_cmp_lg_u64 s[52:53], 0\ns_cbranch_scc0 .Z128\n"
"buffer_load_dwordx4 v[32:35], v1, s[16:19], s32 offen offset:0 sc0 sc1\nbuffer_load_dwordx4 v[36:39], v1, s[16:19], s32 offen offset:1024 sc0 sc1\n.Z128:\n"
"s_cmp_lg_u64 s[54:55], 0\ns_cbranch_scc0 .Z129\nbuffer_load_dwordx4 v[40:43], v1, s[16:19], s33 offen offset:0 sc0 sc1\n"
"buffer_load_dwordx4 v[44:47], v1, s[16:19], s33 offen offset:1024 sc0 sc1\n.Z129:\ns_cmp_lg_u64 s[56:57], 0\ns_cbranch_scc0 .Z130\n"
"buffer_load_dwordx4 v[48:51], v1, s[16:19], s34 offen offset:0 sc0 sc1\nbuffer_load_dwordx4 v[52:55], v1, s[16:19], s34 offen offset:1024 sc0 sc1\n.Z130:\n"
"s_cmp_lg_u64 s[58:59], 0\ns_cbranch_scc0 .Z131\nbuffer_load_dwordx4 v[56:59], v1, s[16:19], s35 offen offset:0 sc0 sc1\n"
"buffer_load_dwordx4 v[60:63], v1, s[16:19], s35 offen offset:1024 sc0 sc1\n.Z131:\ns_sleep 8\nWV 0\ns_branch .Z123\n.RA10:\ns_nop 7\ns_nop 3\nv_max3_f32 v22, v32, v33, v34\n"
"v_max3_f32 v22, v22, v35, v40\nv_max3_f32 v22, v22, v41, v42\nv_max_f32 v22, v22, v43\nv_mov_b32 v91, v22\ns_nop 1\nv_permlane32_swap_b32 v22, v91\nv_max_f32 v22, v22, v91\n"
"v_mov_b32 v91, v22\ns_nop 1\nv_permlane16_swap_b32 v22, v91\nv_max_f32 v22, v22, v91\nv_ceil_f32 v22, v22\nv_max3_f32 v23, v36, v37, v38\nv_max3_f32 v23, v23, v39, v44\n"
"v_max3_f32 v23, v23, v45, v46\nv_max_f32 v23, v23, v47\nv_mov_b32 v91, v23\ns_nop 1\nv_permlane32_swap_b32 v23, v91\nv_max_f32 v23, v23, v91\nv_mov_b32 v91, v23\ns_nop 1\n"
"v_permlane16_swap_b32 v23, v91\nv_max_f32 v23, v23, v91\nv_ceil_f32 v23, v23\ns_bitcmp1_b32 s62, 0\ns_cbranch_scc0 .R133\nv_cmp_lt_f32 vcc, 0xff000000, v22\ns_nop 1\n"
"v_cndmask_b32 v22, 0, v22, vcc\nv_cmp_lt_f32 vcc, 0xff000000, v23\ns_nop 1\nv_cndmask_b32 v23, 0, v23, vcc\ns_branch .R134\n.R133:\nv_max_f32 v22, 0, v22\nv_max_f32 v23, 0, v23\n"
"s_bitcmp1_b32 s62, 3\ns_cbranch_scc0 .R134\nv_max_f32 v22, v20, v22\nv_max_f32 v23, v21, v23\n.R134:\nAD 88,88,22\nAD 89,89,23\nSU 32,32,22\nSU 33,33,22\nSU 34,34,22\nSU 35,35,22\n"
"SU 36,36,23\nSU 37,37,23\nSU 38,38,23\nSU 39,39,23\nSU 40,40,22\nSU 41,41,22\nSU 42,42,22\nSU 43,43,22\nSU 44,44,23\nSU 45,45,23\nSU 46,46,23\nSU 47,47,23\nv_mov_b32 v90, v80\n"
"v_mov_b32 v91, v81\nEX 80,32\nEX 81,33\ns_nop 0\nAD 84,80,81\nCV 64,80,81\nEX 82,34\nEX 83,35\nAD 84,84,82\nAD 84,84,83\nCV 65,82,83\nEX 80,36\nEX 81,37\ns_nop 0\nAD 85,80,81\n"
"CV 68,80,81\nEX 82,38\nEX 83,39\nAD 85,85,82\nAD 85,85,83\nCV 69,82,83\nEX 80,40\nEX 81,41\nAD 84,84,80\nAD 84,84,81\nCV 66,80,81\nEX 82,42\nEX 83,43\nAD 84,84,82\nAD 84,84,83\n"
"CV 67,82,83\nEX 80,44\nEX 81,45\nAD 85,85,80\nAD 85,85,81\nCV 70,80,81\nEX 82,46\nEX 83,47\nAD 85,85,82\nAD 85,85,83\nCV 71,82,83\nv_mov_b32 v80, v90\nv_mov_b32 v81, v91\n"
"s_bitcmp1_b32 s62, 1\ns_cbranch_scc1 .R135\nSU 48,48,22\nSU 49,49,22\nSU 50,50,22\nSU 51,51,22\nSU 52,52,23\nSU 53,53,23\nSU 54,54,23\nSU 55,55,23\nSU 56,56,22\nSU 57,57,22\n"
"SU 58,58,22\nSU 59,59,22\nSU 60,60,23\nSU 61,61,23\nSU 62,62,23\nSU 63,63,23\nSU 4,4,22\nSU 5,5,22\nSU 6,6,22\nSU 7,7,22\nSU 8,8,23\nSU 9,9,23\nSU 10,10,23\nSU 11,11,23\n"
"SU 12,12,22\nSU 13,13,22\nSU 14,14,22\nSU 15,15,22\nSU 16,16,23\nSU 17,17,23\nSU 18,18,23\nSU 19,19,23\nEX 80,48\nEX 81,49\n.R135:\ns_bitcmp1_b32 s62, 0\ns_cbranch_scc0 .RS12\n"
"s_nop 1\ns_setpc_b64 s[80:81]\n.RB11:\ns_nop 7\ns_nop 3\nv_max3_f32 v22, v48, v49, v50\nv_max3_f32 v22, v22, v51, v56\nv_max3_f32 v22, v22, v57, v58\nv_max_f32 v22, v22, v59\n"
"v_mov_b32 v91, v22\ns_nop 1\nv_permlane32_swap_b32 v22, v91\nv_max_f32 v22, v22, v91\nv_mov_b32 v91, v22\ns_nop 1\nv_permlane16_swap_b32 v22, v91\nv_max_f32 v22, v22, v91\n"
"v_ceil_f32 v22, v22\nv_max3_f32 v23, v52, v53, v54\nv_max3_f32 v23, v23, v55, v60\nv_max3_f32 v23, v23, v61, v62\nv_max_f32 v23, v23, v63\nv_mov_b32 v91, v23\ns_nop 1\n"
"v_permlane32_swap_b32 v23, v91\nv_max_f32 v23, v23, v91\nv_mov_b32 v91, v23\ns_nop 1\nv_permlane16_swap_b32 v23, v91\nv_max_f32 v23, v23, v91\nv_ceil_f32 v23, v23\n"
"s_bitcmp1_b32 s62, 0\ns_cbranch_scc0 .R138\nv_cmp_lt_f32 vcc, 0xff000000, v22\nv_cndmask_b32 v22, 0, v22, vcc\nv_cmp_lt_f32 vcc, 0xff000000, v23\nv_cndmask_b32 v23, 0, v23, vcc\n"
"s_branch .R139\n.R138:\nv_max_f32 v22, 0, v22\nv_max_f32 v23, 0, v23\ns_bitcmp1_b32 s62, 3\ns_cbranch_scc0 .R139\nv_max_f32 v22, v20, v22\nv_max_f32 v23, v21, v23\n.R139:\n"
"AD 88,88,22\nAD 89,89,23\nSU 48,48,22\nSU 49,49,22\nSU 50,50,22\nSU 51,51,22\nSU 52,52,23\nSU 53,53,23\nSU 54,54,23\nSU 55,55,23\nSU 56,56,22\nSU 57,57,22\nSU 58,58,22\n"
"SU 59,59,22\nSU 60,60,23\nSU 61,61,23\nSU 62,62,23\nSU 63,63,23\nv_mov_b32 v90, v80\nv_mov_b32 v91, v81\nEX 80,48\nEX 81,49\ns_nop 0\nAD 84,80,81\nCV 72,80,81\nEX 82,50\nEX 83,51\n"
"AD 84,84,82\nAD 84,84,83\nCV 73,82,83\nEX 80,52\nEX 81,53\ns_nop 0\nAD 85,80,81\nCV 76,80,81\nEX 82,54\nEX 83,55\nAD 85,85,82\nAD 85,85,83\nCV 77,82,83\nEX 80,56\nEX 81,57\n"
"AD 84,84,80\nAD 84,84,81\nCV 74,80,81\nEX 82,58\nEX 83,59\nAD 84,84,82\nAD 84,84,83\nCV 75,82,83\nEX 80,60\nEX 81,61\nAD 85,85,80\nAD 85,85,81\nCV 78,80,81\nEX 82,62\nEX 83,63\n"
"AD 85,85,82\nAD 85,85,83\nCV 79,82,83\nv_mov_b32 v80, v90\nv_mov_b32 v81, v91\ns_bitcmp1_b32 s62, 1\ns_cbranch_scc1 .R140\nSU 32,32,22\nSU 33,33,22\nSU 34,34,22\nSU 35,35,22\n"
"SU 36,36,23\nSU 37,37,23\nSU 38,38,23\nSU 39,39,23\nSU 40,40,22\nSU 41,41,22\nSU 42,42,22\nSU 43,43,22\nSU 44,44,23\nSU 45,45,23\nSU 46,46,23\nSU 47,47,23\nSU 4,4,22\nSU 5,5,22\n"
"SU 6,6,22\nSU 7,7,22\nSU 8,8,23\nSU 9,9,23\nSU 10,10,23\nSU 11,11,23\nSU 12,12,22\nSU 13,13,22\nSU 14,14,22\nSU 15,15,22\nSU 16,16,23\nSU 17,17,23\nSU 18,18,23\nSU 19,19,23\n"
"EX 80,32\nEX 81,33\n.R140:\ns_bitcmp1_b32 s62, 0\ns_cbranch_scc0 .RS12\ns_nop 1\ns_setpc_b64 s[80:81]\n.RS12:\nv_exp_f32 v22, -v22\nv_exp_f32 v23, -v23\nMU 86,86,22\nMU 87,87,23\n"
"MU 128,128,22\nMU 129,129,22\nMU 130,130,22\nMU 131,131,22\nMU 132,132,23\nMU 133,133,23\nMU 134,134,23\nMU 135,135,23\nMU 136,136,22\nMU 137,137,22\nMU 138,138,22\nMU 139,139,22\n"
"MU 140,140,23\nMU 141,141,23\nMU 142,142,23\nMU 143,143,23\nMU 144,144,22\nMU 145,145,22\nMU 146,146,22\nMU 147,147,22\nMU 148,148,23\nMU 149,149,23\nMU 150,150,23\nMU 151,151,23\n"
"MU 152,152,22\nMU 153,153,22\nMU 154,154,22\nMU 155,155,22\nMU 156,156,23\nMU 157,157,23\nMU 158,158,23\nMU 159,159,23\nMU 160,160,22\nMU 161,161,22\nMU 162,162,22\nMU 163,163,22\n"
"MU 164,164,23\nMU 165,165,23\nMU 166,166,23\nMU 167,167,23\nMU 168,168,22\nMU 169,169,22\nMU 170,170,22\nMU 171,171,22\nMU 172,172,23\nMU 173,173,23\nMU 174,174,23\nMU 175,175,23\n"
"MU 176,176,22\nMU 177,177,22\nMU 178,178,22\nMU 179,179,22\nMU 180,180,23\nMU 181,181,23\nMU 182,182,23\nMU 183,183,23\nMU 184,184,22\nMU 185,185,22\nMU 186,186,22\nMU 187,187,22\n"
"MU 188,188,23\nMU 189,189,23\nMU 190,190,23\nMU 191,191,23\nMU 192,192,22\nMU 193,193,22\nMU 194,194,22\nMU 195,195,22\nMU 196,196,23\nMU 197,197,23\nMU 198,198,23\nMU 199,199,23\n"
"MU 200,200,22\nMU 201,201,22\nMU 202,202,22\nMU 203,203,22\nMU 204,204,23\nMU 205,205,23\nMU 206,206,23\nMU 207,207,23\nMU 208,208,22\nMU 209,209,22\nMU 210,210,22\nMU 211,211,22\n"
"MU 212,212,23\nMU 213,213,23\nMU 214,214,23\nMU 215,215,23\nMU 216,216,22\nMU 217,217,22\nMU 218,218,22\nMU 219,219,22\nMU 220,220,23\nMU 221,221,23\nMU 222,222,23\nMU 223,223,23\n"
"MU 224,224,22\nMU 225,225,22\nMU 226,226,22\nMU 227,227,22\nMU 228,228,23\nMU 229,229,23\nMU 230,230,23\nMU 231,231,23\nMU 232,232,22\nMU 233,233,22\nMU 234,234,22\nMU 235,235,22\n"
"MU 236,236,23\nMU 237,237,23\nMU 238,238,23\nMU 239,239,23\nMU 240,240,22\nMU 241,241,22\nMU 242,242,22\nMU 243,243,22\nMU 244,244,23\nMU 245,245,23\nMU 246,246,23\nMU 247,247,23\n"
"MU 248,248,22\nMU 249,249,22\nMU 250,250,22\nMU 251,251,22\nMU 252,252,23\nMU 253,253,23\nMU 254,254,23\nMU 255,255,23\ns_nop 1\ns_setpc_b64 s[80:81]\n.p2align 12\n.PA3:\n"
"s_mov_b64 exec, 0\ns_cmp_eq_u32 s11, 1\ns_cbranch_scc1 .W553\ns_cmp_eq_u32 s11, 2\ns_cbranch_scc1 .W554\ns_cmp_eq_u32 s11, 3\ns_cbranch_scc1 .W555\ns_branch .W552\n.WB151:\n"
"s_mov_b64 exec, -1\nv_add_u32 v128, 0x10000, v12\nv_add_u32 v129, 0x10000, v13\nv_add_u32 v130, 0x10000, v14\nv_add_u32 v131, 0x10000, v15\nv_add_u32 v132, 0x10000, v16\n"
"v_add_u32 v133, 0x10000, v17\nv_add_u32 v134, 0x10000, v18\nv_add_u32 v135, 0x10000, v19\ns_add_u32 s2, s11, 1\ns_lshr_b32 s2, s2, 1\ns_lshl_b32 s2, s2, 12\n"
"v_add_u32 v140, s2, v10\nv_add_u32 v141, s2, v11\nv_xor_b32 v142, 0x1000, v140\nv_xor_b32 v143, 0x1000, v141\ns_add_u32 m0, s47, 0\ns_add_u32 s46, s51, 524288\nD0 6,32,46,0\n"
"D0 7,32,46,1024\nD0 8,32,46,2048\nD0 9,32,46,3072\ns_add_u32 m0, s47, -49152\ns_add_u32 s46, s52, 128\nD0 136,28,46,0\nD0 137,28,46,1024\nD0 138,28,46,2048\nD0 139,28,46,3072\n"
"s_add_u32 m0, s47, 16384\ns_add_u32 s46, s51, 786432\nD0 6,32,46,0\nD0 7,32,46,1024\nD0 8,32,46,2048\nD0 9,32,46,3072\nv_mov_b32 v116, 0x40000000\nv_cmp_gt_u32 vcc, 56, v0\n"
"s_lshl2_add_u32 s2, s10, s11\ns_mul_i32 s2, s2, 1792\nv_cndmask_b32 v117, v116, v1, vcc\ns_mov_b32 s36, s56\ns_and_b32 s37, s57, 0xffff\ns_mov_b32 s38, 0x380\n"
"s_mov_b32 s39, 0x20000\ns_mov_b32 s40, s58\ns_and_b32 s41, s59, 0xffff\ns_mov_b32 s42, 0x380\ns_mov_b32 s43, 0x20000\n"
"buffer_load_dwordx4 v[96:99], v117, s[28:31], s2 offen offset:0\nbuffer_load_dwordx4 v[100:103], v117, s[28:31], s2 offen offset:896\n"
"buffer_load_dwordx4 v[104:107], v117, s[36:39], 0 offen offset:0\nbuffer_load_dwordx4 v[108:111], v117, s[40:43], 0 offen offset:0\ns_add_u32 m0, s47, 32768\n"
"s_add_u32 s46, s51, 1048576\nD0 6,32,46,0\nD0 7,32,46,1024\nD0 8,32,46,2048\nD0 9,32,46,3072\ns_add_u32 m0, s47, -32768\ns_add_u32 s46, s52, 256\nD0 136,28,46,0\n"
"D0 137,28,46,1024\nD0 138,28,46,2048\nD0 139,28,46,3072\nWL 0\ns_mul_i32 s15, s15, 1664525\ns_add_u32 s15, s15, 1013904223\nv_mov_b32 v25, s15\ns_mov_b32 s57, 0x3db8aa3b\n"
"s_and_b32 s59, s11, 1\ns_lshl_b32 s59, s59, 13\ns_mul_i32 s2, s73, 25165824\ns_and_b32 s3, s12, 1\ns_lshl_b32 s3, s3, 21\ns_add_u32 s2, s2, s3\ns_lshl2_add_u32 s3, s13, s11\n"
"s_lshl_b32 s3, s3, 15\ns_add_u32 s2, s2, s3\ns_add_u32 s56, s2, 0x800000\ns_xor_b32 s53, s56, 0xc000\nWV 24\ns_barrier\nRa 192,140,0\nRa 196,141,0\nRa 200,140,2048\n"
"Ra 204,141,2048\nRa 208,142,0\nRa 212,143,0\nRa 216,142,2048\nRa 220,143,2048\nTa 160,128,0\nTa 162,128,2048\nTa 164,128,16384\nTa 166,128,18432\nTa 168,129,0\nTa 170,129,2048\n"
"Ta 172,129,16384\nTa 174,129,18432\nTa 176,130,0\nTa 178,130,2048\nTa 180,130,16384\nTa 182,130,18432\nTa 184,131,0\nTa 186,131,2048\nTa 188,131,16384\nTa 190,131,18432\nWL 0\n"
"Zvaa 32,160,192\nTa 224,132,0\ns_add_u32 m0, s47, 49152\ns_add_u32 s46, s51, 1310720\nMvaa 32,164,196\nTa 226,132,2048\nZaaa 96,164,204\nD0 6,32,46,0\nMaaa 96,160,200\n"
"Ta 228,132,16384\nZvaa 192,160,208\nTa 230,132,18432\nMvaa 192,164,212\nZvaa 224,164,220\nTa 232,133,0\nMvaa 224,160,216\nTa 234,133,2048\nD0 7,32,46,1024\nZvaa 228,168,216\n"
"Mvaa 228,172,220\nTa 236,133,16384\nZvaa 196,172,212\nTa 238,133,18432\nMvaa 196,168,208\nZaaa 100,168,200\nTa 240,134,0\nD0 8,32,46,2048\nMaaa 100,172,204\nTa 242,134,2048\n"
"Zvaa 36,172,196\nMvaa 36,168,192\nTa 244,134,16384\nZvaa 40,176,192\nTa 246,134,18432\nMvaa 40,180,196\nD0 9,32,46,3072\nZaaa 104,180,204\nTa 248,135,0\nMaaa 104,176,200\n"
"Ta 250,135,2048\nZvaa 200,176,208\ns_add_u32 m0, s47, -16384\ns_add_u32 s46, s52, 384\nMvaa 200,180,212\nTa 252,135,16384\nZvaa 232,180,220\nTa 254,135,18432\nD0 136,28,46,0\n"
"Mvaa 232,176,216\nZvaa 236,184,216\nMvaa 236,188,220\nZvaa 204,188,212\nMvaa 204,184,208\nD0 137,28,46,1024\nZaaa 108,184,200\nMaaa 108,188,204\nZvaa 44,188,196\nMvaa 44,184,192\n"
"s_waitcnt vmcnt(18) lgkmcnt(0)\ns_barrier\nZvaa 48,224,192\nRa 128,140,16384\nD0 138,28,46,2048\nMvaa 48,228,196\nRa 132,141,16384\nZaaa 112,228,204\nRa 136,140,18432\n"
"Maaa 112,224,200\nRa 140,141,18432\nZvaa 208,224,208\nRa 144,142,16384\nWV 15\nMvaa 208,228,212\nRa 148,143,16384\nD0 139,28,46,3072\nZvaa 240,228,220\nRa 152,142,18432\n"
"Mvaa 240,224,216\nRa 156,143,18432\nZvaa 244,232,216\nTa 160,12,0\ns_add_u32 m0, s47, 65536\ns_add_u32 s46, s51, 1572864\nMvaa 244,236,220\nTa 162,12,2048\nZvaa 212,236,212\n"
"Ta 164,12,16384\nD0 6,32,46,0\nMvaa 212,232,208\nTa 166,12,18432\nZaaa 116,232,200\nTa 168,13,0\nv_lshlrev_b32 v112, 16, v96\nMaaa 116,236,204\nTa 170,13,2048\nZvaa 52,236,196\n"
"Ta 172,13,16384\nMvaa 52,232,192\nTa 174,13,18432\nD0 7,32,46,1024\nZvaa 56,240,192\nTa 176,14,0\nv_lshlrev_b32 v113, 16, v100\nMvaa 56,244,196\nTa 178,14,2048\nZaaa 120,244,204\n"
"Ta 180,14,16384\nMaaa 120,240,200\nTa 182,14,18432\nZvaa 216,240,208\nTa 184,15,0\nD0 8,32,46,2048\nMvaa 216,244,212\nTa 186,15,2048\nZvaa 248,244,220\nTa 188,15,16384\n"
"Mvaa 248,240,216\nTa 190,15,18432\nZvaa 252,248,216\nv_lshlrev_b32 v114, 16, v104\nv_lshlrev_b32 v115, 16, v108\nMvaa 252,252,220\nD0 9,32,46,3072\nZvaa 220,252,212\n"
"MU 116,112,114\nMvaa 220,248,208\nMU 117,113,114\nZaaa 124,248,200\ns_add_u32 m0, s47, 81920\ns_add_u32 s46, s51, 1835008\nMaaa 124,252,204\nCV 116,116,117\nZvaa 60,252,196\n"
"D0 6,32,46,0\nMvaa 60,248,192\nWL 0\nMvaa 32,160,128\nTa 224,16,0\nv_lshlrev_b32 v118, 16, v116\nMvaa 32,164,132\nTa 226,16,2048\nMaaa 96,164,140\n"
"v_and_b32 v119, 0xffff0000, v116\nMaaa 96,160,136\nTa 228,16,16384\nD0 7,32,46,1024\nMvaa 192,160,144\nTa 230,16,18432\nMU 120,112,115\nMvaa 192,164,148\nMU 121,113,115\n"
"Mvaa 224,164,156\nTa 232,17,0\nMvaa 224,160,152\nTa 234,17,2048\nMvaa 228,168,152\nD0 8,32,46,2048\nv_and_b32 v112, 0xffff0000, v96\nMvaa 228,172,156\nTa 236,17,16384\n"
"Mvaa 196,172,148\nTa 238,17,18432\nMvaa 196,168,144\nv_and_b32 v113, 0xffff0000, v100\nMaaa 100,168,136\nTa 240,18,0\nv_and_b32 v114, 0xffff0000, v104\nMaaa 100,172,140\n"
"Ta 242,18,2048\nD0 9,32,46,3072\nMvaa 36,172,132\nv_and_b32 v115, 0xffff0000, v108\nMvaa 36,168,128\nTa 244,18,16384\nMvaa 40,176,128\nTa 246,18,18432\nMU 116,112,114\n"
"Mvaa 40,180,132\nMU 117,113,114\nMaaa 104,180,140\nTa 248,19,0\nMaaa 104,176,136\nTa 250,19,2048\nMvaa 200,176,144\nCV 116,116,117\nv_and_b32 v117, 0xffff0000, v116\n"
"Mvaa 200,180,148\nTa 252,19,16384\nMvaa 232,180,156\nTa 254,19,18432\nMvaa 232,176,152\nv_lshlrev_b32 v116, 16, v116\nMvaa 236,184,152\nFM 120,112,115\nFM 121,113,115\n"
"Mvaa 236,188,156\nAD 118,118,116\nMvaa 204,188,148\nAD 119,119,117\nMvaa 204,184,144\nv_lshlrev_b32 v112, 16, v97\nMaaa 108,184,136\nv_lshlrev_b32 v113, 16, v101\n"
"v_lshlrev_b32 v114, 16, v105\nMaaa 108,188,140\nv_lshlrev_b32 v115, 16, v109\nMvaa 44,188,132\nMU 116,112,114\nMvaa 44,184,128\ns_waitcnt vmcnt(12) lgkmcnt(0)\ns_barrier\n"
"Mvaa 48,224,128\nRa 192,140,32768\ns_add_u32 m0, s47, -49152\ns_add_u32 s46, s52, 512\nMvaa 48,228,132\nRa 196,141,32768\nMaaa 112,228,140\nRa 200,140,34816\nD0 136,28,46,0\n"
"Maaa 112,224,136\nRa 204,141,34816\nMvaa 208,224,144\nRa 208,142,32768\nMU 117,113,114\nMvaa 208,228,148\nRa 212,143,32768\nMvaa 240,228,156\nRa 216,142,34816\nMvaa 240,224,152\n"
"Ra 220,143,34816\nD0 137,28,46,1024\nMvaa 244,232,152\nTa 160,12,32768\nCV 116,116,117\nMvaa 244,236,156\nTa 162,12,34816\nMvaa 212,236,148\nTa 164,12,49152\nMvaa 212,232,144\n"
"Ta 166,12,51200\nMaaa 116,232,136\nTa 168,13,32768\nD0 138,28,46,2048\nMaaa 116,236,140\nTa 170,13,34816\nMvaa 52,236,132\nTa 172,13,49152\nMvaa 52,232,128\nTa 174,13,51200\n"
"Mvaa 56,240,128\nTa 176,14,32768\nv_and_b32 v117, 0xffff0000, v116\nMvaa 56,244,132\nTa 178,14,34816\nD0 139,28,46,3072\nMaaa 120,244,140\nTa 180,14,49152\nMaaa 120,240,136\n"
"Ta 182,14,51200\nMvaa 216,240,144\nTa 184,15,32768\ns_add_u32 m0, s47, 0\ns_add_u32 s46, s51, 2097152\nMvaa 216,244,148\nTa 186,15,34816\nMvaa 248,244,156\nTa 188,15,49152\n"
"D0 6,32,46,0\nMvaa 248,240,152\nTa 190,15,51200\nMvaa 252,248,152\nv_lshlrev_b32 v116, 16, v116\nFM 120,112,115\nMvaa 252,252,156\nFM 121,113,115\nMvaa 220,252,148\n"
"AD 118,118,116\nMvaa 220,248,144\nD0 7,32,46,1024\nMaaa 124,248,136\nAD 119,119,117\nv_and_b32 v112, 0xffff0000, v97\nMaaa 124,252,140\nv_and_b32 v113, 0xffff0000, v101\n"
"Mvaa 60,252,132\nv_and_b32 v114, 0xffff0000, v105\nMvaa 60,248,128\nWL 0\nMvaa 32,160,192\nTa 224,16,32768\nD0 8,32,46,2048\nMvaa 32,164,196\nTa 226,16,34816\nMaaa 96,164,204\n"
"v_and_b32 v115, 0xffff0000, v109\nMaaa 96,160,200\nTa 228,16,49152\nMvaa 192,160,208\nTa 230,16,51200\nMU 116,112,114\nMvaa 192,164,212\nD0 9,32,46,3072\nMvaa 224,164,220\n"
"Ta 232,17,32768\nMvaa 224,160,216\nTa 234,17,34816\nMvaa 228,168,216\ns_add_u32 m0, s47, 16384\ns_add_u32 s46, s51, 2359296\nMvaa 228,172,220\nTa 236,17,49152\nMvaa 196,172,212\n"
"Ta 238,17,51200\nD0 6,32,46,0\nMvaa 196,168,208\nMU 117,113,114\nMaaa 100,168,200\nTa 240,18,32768\nCV 116,116,117\nMaaa 100,172,204\nTa 242,18,34816\nMvaa 36,172,196\n"
"v_and_b32 v117, 0xffff0000, v116\nMvaa 36,168,192\nTa 244,18,49152\nD0 7,32,46,1024\nMvaa 40,176,192\nTa 246,18,51200\nv_lshlrev_b32 v116, 16, v116\nMvaa 40,180,196\n"
"FM 120,112,115\nMaaa 104,180,204\nTa 248,19,32768\nMaaa 104,176,200\nTa 250,19,34816\nMvaa 200,176,208\nD0 8,32,46,2048\nFM 121,113,115\nMvaa 200,180,212\nTa 252,19,49152\n"
"Mvaa 232,180,220\nTa 254,19,51200\nMvaa 232,176,216\nAD 118,118,116\nMvaa 236,184,216\nAD 119,119,117\nv_lshlrev_b32 v112, 16, v98\nMvaa 236,188,220\nD0 9,32,46,3072\n"
"Mvaa 204,188,212\nv_lshlrev_b32 v113, 16, v102\nMvaa 204,184,208\nv_lshlrev_b32 v114, 16, v106\nMaaa 108,184,200\nv_lshlrev_b32 v115, 16, v110\nMU 116,112,114\nMaaa 108,188,204\n"
"MU 117,113,114\nMvaa 44,188,196\nCV 116,116,117\nMvaa 44,184,192\ns_waitcnt vmcnt(12) lgkmcnt(0)\ns_barrier\nMvaa 48,224,192\nRa 128,140,49152\ns_add_u32 m0, s47, -32768\n"
"s_add_u32 s46, s52, 640\nMvaa 48,228,196\nRa 132,141,49152\nMaaa 112,228,204\nRa 136,140,51200\nD0 136,28,46,0\nMaaa 112,224,200\nRa 140,141,51200\nMvaa 208,224,208\n"
"Ra 144,142,49152\nv_and_b32 v117, 0xffff0000, v116\nMvaa 208,228,212\nRa 148,143,49152\nMvaa 240,228,220\nRa 152,142,51200\nMvaa 240,224,216\nRa 156,143,51200\nD0 137,28,46,1024\n"
"Mvaa 244,232,216\nTa 160,128,0\nv_lshlrev_b32 v116, 16, v116\nMvaa 244,236,220\nTa 162,128,2048\nMvaa 212,236,212\nTa 164,128,16384\nMvaa 212,232,208\nTa 166,128,18432\n"
"Maaa 116,232,200\nTa 168,129,0\nD0 138,28,46,2048\nMaaa 116,236,204\nTa 170,129,2048\nMvaa 52,236,196\nTa 172,129,16384\nMvaa 52,232,192\nTa 174,129,18432\nMvaa 56,240,192\n"
"Ta 176,130,0\nFM 120,112,115\nMvaa 56,244,196\nTa 178,130,2048\nD0 139,28,46,3072\nMaaa 120,244,204\nTa 180,130,16384\nMaaa 120,240,200\nTa 182,130,18432\nMvaa 216,240,208\n"
"Ta 184,131,0\ns_add_u32 m0, s47, 32768\ns_add_u32 s46, s51, 2621440\nMvaa 216,244,212\nTa 186,131,2048\nMvaa 248,244,220\nTa 188,131,16384\nD0 6,32,46,0\nMvaa 248,240,216\n"
"Ta 190,131,18432\nMvaa 252,248,216\nFM 121,113,115\nAD 118,118,116\nMvaa 252,252,220\nAD 119,119,117\nMvaa 220,252,212\nv_and_b32 v112, 0xffff0000, v98\nMvaa 220,248,208\n"
"D0 7,32,46,1024\nMaaa 124,248,200\nv_and_b32 v113, 0xffff0000, v102\nv_and_b32 v114, 0xffff0000, v106\nMaaa 124,252,204\nv_and_b32 v115, 0xffff0000, v110\nMvaa 60,252,196\n"
"MU 116,112,114\nMvaa 60,248,192\nWL 0\nMvaa 32,160,128\nTa 224,132,0\n.W552:\ns_cbranch_execz .W556\nD0 8,32,46,2048\nMvaa 32,164,132\nTa 226,132,2048\nMaaa 96,164,140\n"
"MU 117,113,114\nMaaa 96,160,136\nTa 228,132,16384\nMvaa 192,160,144\nTa 230,132,18432\nCV 116,116,117\nMvaa 192,164,148\nD0 9,32,46,3072\nMvaa 224,164,156\nTa 232,133,0\n"
"Mvaa 224,160,152\nTa 234,133,2048\nMvaa 228,168,152\ns_add_u32 m0, s47, 49152\ns_add_u32 s46, s51, 2883584\nMvaa 228,172,156\nTa 236,133,16384\nMvaa 196,172,148\nTa 238,133,18432\n"
"D0 6,32,46,0\nMvaa 196,168,144\nv_and_b32 v117, 0xffff0000, v116\nMaaa 100,168,136\nTa 240,134,0\nv_lshlrev_b32 v116, 16, v116\nMaaa 100,172,140\nTa 242,134,2048\nMvaa 36,172,132\n"
"FM 120,112,115\nMvaa 36,168,128\nTa 244,134,16384\nD0 7,32,46,1024\nMvaa 40,176,128\nTa 246,134,18432\nFM 121,113,115\nMvaa 40,180,132\nAD 118,118,116\nMaaa 104,180,140\n"
"Ta 248,135,0\nMaaa 104,176,136\nTa 250,135,2048\nMvaa 200,176,144\nD0 8,32,46,2048\nAD 119,119,117\nMvaa 200,180,148\nTa 252,135,16384\nMvaa 232,180,156\nTa 254,135,18432\n"
"Mvaa 232,176,152\nv_lshlrev_b32 v112, 16, v99\nMvaa 236,184,152\nv_lshlrev_b32 v113, 16, v103\nv_lshlrev_b32 v114, 16, v107\nMvaa 236,188,156\nD0 9,32,46,3072\nMvaa 204,188,148\n"
"v_lshlrev_b32 v115, 16, v111\nMvaa 204,184,144\nMU 116,112,114\nMaaa 108,184,136\nMU 117,113,114\nCV 116,116,117\ns_and_b32 s78, s74, 3\ns_lshl2_add_u32 s78, s73, s78\n"
"s_lshl_b32 s78, s78, 12\ns_bitcmp1_b32 s11, 0\ns_cselect_b32 s79, s48, s45\ns_add_u32 s79, s79, 512\ns_and_b32 s79, s79, 0xfffe0000\ns_add_u32 s78, s78, s79\n"
"buffer_load_dword v31, v26, s[20:23], s78 offen\nMaaa 108,188,140\nv_and_b32 v117, 0xffff0000, v116\nMvaa 44,188,132\nv_lshlrev_b32 v116, 16, v116\nMvaa 44,184,128\n"
"s_waitcnt vmcnt(13) lgkmcnt(0)\ns_barrier\nMvaa 48,224,128\nRa 192,140,16384\ns_add_u32 m0, s47, -16384\ns_add_u32 s46, s52, 768\nMvaa 48,228,132\nRa 196,141,16384\n"
"Maaa 112,228,140\nRa 200,140,18432\nD0 136,28,46,0\nMaaa 112,224,136\nRa 204,141,18432\nMvaa 208,224,144\nRa 208,142,16384\nFM 120,112,115\nMvaa 208,228,148\nRa 212,143,16384\n"
"Mvaa 240,228,156\nRa 216,142,18432\nMvaa 240,224,152\nRa 220,143,18432\nD0 137,28,46,1024\nMvaa 244,232,152\nTa 160,12,0\nFM 121,113,115\nMvaa 244,236,156\nTa 162,12,2048\n"
"Mvaa 212,236,148\nTa 164,12,16384\nMvaa 212,232,144\nTa 166,12,18432\nMaaa 116,232,136\nTa 168,13,0\nD0 138,28,46,2048\nMaaa 116,236,140\nTa 170,13,2048\nMvaa 52,236,132\n"
"Ta 172,13,16384\nMvaa 52,232,128\nTa 174,13,18432\nMvaa 56,240,128\nTa 176,14,0\nAD 118,118,116\nMvaa 56,244,132\nTa 178,14,2048\nD0 139,28,46,3072\nMaaa 120,244,140\n"
"Ta 180,14,16384\nMaaa 120,240,136\nTa 182,14,18432\nMvaa 216,240,144\nTa 184,15,0\ns_add_u32 m0, s47, 65536\ns_add_u32 s46, s51, 3145728\nMvaa 216,244,148\nTa 186,15,2048\n"
"Mvaa 248,244,156\nTa 188,15,16384\nD0 6,32,46,0\nMvaa 248,240,152\nTa 190,15,18432\nMvaa 252,248,152\nAD 119,119,117\nv_and_b32 v112, 0xffff0000, v99\nMvaa 252,252,156\n"
"v_and_b32 v113, 0xffff0000, v103\nMvaa 220,252,148\nv_and_b32 v114, 0xffff0000, v107\nMvaa 220,248,144\nD0 7,32,46,1024\nMaaa 124,248,136\nv_and_b32 v115, 0xffff0000, v111\n"
"MU 116,112,114\nMaaa 124,252,140\nMU 117,113,114\nMvaa 60,252,132\nCV 116,116,117\nMvaa 60,248,128\nWL 0\nMvaa 32,160,192\nTa 224,16,0\nD0 8,32,46,2048\nMvaa 32,164,196\n"
"Ta 226,16,2048\nMaaa 96,164,204\nv_and_b32 v117, 0xffff0000, v116\nMaaa 96,160,200\nTa 228,16,16384\nMvaa 192,160,208\nTa 230,16,18432\nv_lshlrev_b32 v116, 16, v116\n"
"Mvaa 192,164,212\nD0 9,32,46,3072\nMvaa 224,164,220\nTa 232,17,0\nMvaa 224,160,216\nTa 234,17,2048\nMvaa 228,168,216\ns_add_u32 m0, s47, 81920\ns_add_u32 s46, s51, 3407872\n"
"Mvaa 228,172,220\nTa 236,17,16384\nMvaa 196,172,212\nTa 238,17,18432\nD0 6,32,46,0\nMvaa 196,168,208\nFM 120,112,115\nMaaa 100,168,200\nTa 240,18,0\nFM 121,113,115\n"
"Maaa 100,172,204\nTa 242,18,2048\nMvaa 36,172,196\nAD 118,118,116\nMvaa 36,168,192\nTa 244,18,16384\nD0 7,32,46,1024\nMvaa 40,176,192\nTa 246,18,18432\nAD 119,119,117\n"
"Mvaa 40,180,196\ns_nop 0\nv_permlane32_swap_b32 v118, v119\nMaaa 104,180,204\nTa 248,19,0\nMaaa 104,176,200\nTa 250,19,2048\nMvaa 200,176,208\nD0 8,32,46,2048\n"
"v_permlane32_swap_b32 v120, v121\nMvaa 200,180,212\nTa 252,19,16384\nMvaa 232,180,220\nTa 254,19,18432\nMvaa 232,176,216\nAD 118,118,119\nMvaa 236,184,216\nAD 120,120,121\n"
"Mvaa 236,188,220\nD0 9,32,46,3072\nMvaa 204,188,212\nv_permlane16_swap_b32 v118, v120\nMvaa 204,184,208\nAD 118,118,120\nMaaa 108,184,200\nMaaa 108,188,204\n"
"v_add_f32_dpp v118, v118, v118 row_ror:8 row_mask:0xf bank_mask:0xf\nMvaa 44,188,196\nMvaa 44,184,192\ns_waitcnt vmcnt(13) lgkmcnt(0)\ns_barrier\nMvaa 48,224,192\n"
"Ra 128,140,32768\ns_add_u32 m0, s47, -65536\ns_add_u32 s45, s45, 128\nMvaa 48,228,196\nRa 132,141,32768\nMaaa 112,228,204\nRa 136,140,34816\nD0 2,20,45,0\nMaaa 112,224,200\n"
"Ra 140,141,34816\nMvaa 208,224,208\nRa 144,142,32768\nv_add_f32_dpp v118, v118, v118 row_ror:4 row_mask:0xf bank_mask:0xf\nMvaa 208,228,212\nRa 148,143,32768\nMvaa 240,228,220\n"
"Ra 152,142,34816\nMvaa 240,224,216\nRa 156,143,34816\nD0 3,20,45,1024\nMvaa 244,232,216\nTa 160,12,32768\nv_add_f32_dpp v118, v118, v118 row_ror:2 row_mask:0xf bank_mask:0xf\n"
"Mvaa 244,236,220\nTa 162,12,34816\nMvaa 212,236,212\nTa 164,12,49152\nMvaa 212,232,208\nTa 166,12,51200\nMaaa 116,232,200\nTa 168,13,32768\nD0 4,20,45,2048\nMaaa 116,236,204\n"
"Ta 170,13,34816\nMvaa 52,236,196\nTa 172,13,49152\nMvaa 52,232,192\nTa 174,13,51200\nMvaa 56,240,192\nTa 176,14,32768\n"
"v_add_f32_dpp v118, v118, v118 row_ror:1 row_mask:0xf bank_mask:0xf\nMvaa 56,244,196\nTa 178,14,34816\nD0 5,20,45,3072\nMaaa 120,244,204\nTa 180,14,49152\nMaaa 120,240,200\n"
"Ta 182,14,51200\nMvaa 216,240,208\nTa 184,15,32768\ns_add_u32 m0, s47, 0\ns_add_u32 s44, s44, 0x40000\nMvaa 216,244,212\nTa 186,15,34816\nMvaa 248,244,220\nTa 188,15,49152\n"
"D0 6,24,44,0\nMvaa 248,240,216\nTa 190,15,51200\nMvaa 252,248,216\nCV 118,118,118\nv_lshlrev_b32 v118, 16, v118\nMvaa 252,252,220\nv_mul_f32 v119, 0xbfb8aa3b, v118\nEX 119,119\n"
"Mvaa 220,252,212\nv_add_f32 v120, 0, v118\nMvaa 220,248,208\nD0 7,24,44,1024\nMaaa 124,248,200\nv_add_f32 v119, 1.0, v119\nMaaa 124,252,204\nv_rcp_f32 v119, v119\nMvaa 60,252,196\n"
"v_ashrrev_i32 v121, 31, v120\nMvaa 60,248,192\nWL 0\nMvaa 32,160,128\nTa 224,16,32768\nD0 8,24,44,2048\nMvaa 32,164,132\nTa 226,16,34816\nMaaa 96,164,140\nCV 119,119,119\n"
"Maaa 96,160,136\nTa 228,16,49152\nMvaa 192,160,144\nTa 230,16,51200\nv_lshlrev_b32 v119, 16, v119\nMvaa 192,164,148\nD0 9,24,44,3072\nMvaa 224,164,156\nTa 232,17,32768\n"
"Mvaa 224,160,152\nTa 234,17,34816\nMvaa 228,168,152\ns_add_u32 m0, s47, 16384\ns_add_u32 s44, s44, 0x40000\nMvaa 228,172,156\nTa 236,17,49152\nMvaa 196,172,148\nTa 238,17,51200\n"
"D0 6,24,44,0\nMvaa 196,168,144\nv_or_b32 v121, 0x80000000, v121\nMaaa 100,168,136\nTa 240,18,32768\nv_xor_b32 v120, v120, v121\nMaaa 100,172,140\nTa 242,18,34816\nMvaa 36,172,132\n"
"v_and_b32 v120, 0xffff0000, v120\nMvaa 36,168,128\nTa 244,18,49152\nD0 7,24,44,1024\nMvaa 40,176,128\nTa 246,18,51200\ns_lshl2_add_u32 s2, s10, s11\nMvaa 40,180,132\n"
"v_lshrrev_b32 v112, 5, v0\nMaaa 104,180,140\nTa 248,19,32768\nMaaa 104,176,136\nTa 250,19,34816\nMvaa 200,176,144\nD0 8,24,44,2048\nv_lshl_add_u32 v112, s2, 1, v112\n"
"Mvaa 200,180,148\nTa 252,19,49152\nMvaa 232,180,156\nTa 254,19,51200\nMvaa 232,176,152\nv_sub_u32 v113, 0xffff, v112\nMvaa 236,184,152\nv_or_b32 v120, v120, v113\n"
"v_mov_b32 v121, s15\nMvaa 236,188,156\nD0 9,24,44,3072\nMvaa 204,188,148\nv_and_b32 v113, 31, v0\nMvaa 204,184,144\nv_mov_b32 v116, 0x40000000\nv_cmp_eq_u32 vcc, 16, v113\n"
"v_lshlrev_b32 v114, 2, v112\nv_lshlrev_b32 v115, 10, v113\nv_cndmask_b32 v114, v116, v114, vcc\nv_cndmask_b32 v120, v119, v120, vcc\nMaaa 108,184,136\n"
"v_lshl_add_u32 v115, v112, 3, v115\nv_and_b32 v113, 15, v0\nMaaa 108,188,140\nv_cmp_eq_u32 vcc, 0, v113\ns_mov_b32 s2, 0xc040000\ns_mov_b32 s3, 0xc03e000\n"
"v_cndmask_b32 v115, v116, v115, vcc\nbuffer_store_dwordx2 v[120:121], v115, s[16:19], s2 offen sc0 sc1\nbuffer_store_dword v118, v114, s[16:19], s3 offen sc0 sc1\nMvaa 44,188,132\n"
"Mvaa 44,184,128\ns_waitcnt vmcnt(14) lgkmcnt(0)\ns_barrier\nMvaa 48,224,128\nRa 192,140,49152\ns_add_u32 m0, s47, -49152\ns_add_u32 s48, s48, 128\nMvaa 48,228,132\n"
"Ra 196,141,49152\nMaaa 112,228,140\nRa 200,140,51200\nD0 2,20,48,0\nMaaa 112,224,136\nRa 204,141,51200\nMvaa 208,224,144\nRa 208,142,49152\nMvaa 208,228,148\nRa 212,143,49152\n"
"Mvaa 240,228,156\nRa 216,142,51200\nMvaa 240,224,152\nRa 220,143,51200\nD0 3,20,48,1024\nMvaa 244,232,152\nTa 160,128,0\nMvaa 244,236,156\nTa 162,128,2048\nMvaa 212,236,148\n"
"Ta 164,128,16384\nMvaa 212,232,144\nTa 166,128,18432\nMaaa 116,232,136\nTa 168,129,0\nD0 4,20,48,2048\nMaaa 116,236,140\nTa 170,129,2048\nMvaa 52,236,132\nTa 172,129,16384\n"
"Mvaa 52,232,128\nTa 174,129,18432\nMvaa 56,240,128\nTa 176,130,0\nMvaa 56,244,132\nTa 178,130,2048\nD0 5,20,48,3072\nMaaa 120,244,140\nTa 180,130,16384\nMaaa 120,240,136\n"
"Ta 182,130,18432\nMvaa 216,240,144\nTa 184,131,0\ns_add_u32 m0, s47, -32768\ns_add_u32 s45, s45, 128\nMvaa 216,244,148\nTa 186,131,2048\nMvaa 248,244,156\nTa 188,131,16384\n"
"D0 2,20,45,0\nMvaa 248,240,152\nTa 190,131,18432\nMvaa 252,248,152\nMvaa 252,252,156\nMvaa 220,252,148\nMvaa 220,248,144\nD0 3,20,45,1024\nMaaa 124,248,136\nMaaa 124,252,140\n"
"Mvaa 60,252,132\nMvaa 60,248,128\nWL 0\nMvaa 32,160,192\nTa 224,132,0\nD0 4,20,45,2048\nMvaa 32,164,196\nTa 226,132,2048\nMvaa 36,172,196\nMvaa 36,168,192\nTa 228,132,16384\n"
"Mvaa 40,176,192\nTa 230,132,18432\nMvaa 40,180,196\nD0 5,20,45,3072\nMvaa 44,188,196\nTa 232,133,0\nMvaa 44,184,192\nTa 234,133,2048\nMaaa 108,184,200\ns_add_u32 m0, s47, 32768\n"
"s_add_u32 s44, s44, 0x40000\nMaaa 108,188,204\nTa 236,133,16384\nMaaa 104,180,204\nTa 238,133,18432\nD0 6,24,44,0\nMaaa 104,176,200\nv_mul_f32 v32, s57, v32\n"
"v_mul_f32 v33, s57, v33\nMaaa 100,168,200\nTa 240,134,0\nCV 32,32,33\nMaaa 100,172,204\nTa 242,134,2048\nv_mul_f32 v34, s57, v34\nMaaa 96,164,204\nv_mul_f32 v35, s57, v35\n"
"CV 33,34,35\nMaaa 96,160,200\nTa 244,134,16384\nD0 7,24,44,1024\nMvaa 192,160,208\nTa 246,134,18432\nv_mul_f32 v36, s57, v36\nMvaa 192,164,212\nv_mul_f32 v37, s57, v37\n"
"Mvaa 196,172,212\nTa 248,135,0\nCV 34,36,37\nMvaa 196,168,208\nTa 250,135,2048\nv_mul_f32 v38, s57, v38\nMvaa 200,176,208\nD0 8,24,44,2048\nv_mul_f32 v39, s57, v39\n"
"Mvaa 200,180,212\nTa 252,135,16384\nCV 35,38,39\nMvaa 204,188,212\nTa 254,135,18432\nv_mul_f32 v40, s57, v40\nMvaa 204,184,208\nv_mul_f32 v41, s57, v41\nCV 40,40,41\n"
"Mvaa 236,184,216\nv_mul_f32 v42, s57, v42\nv_mul_f32 v43, s57, v43\nMvaa 236,188,220\nD0 9,24,44,3072\nMvaa 232,180,220\nCV 41,42,43\nv_mul_f32 v44, s57, v44\nMvaa 232,176,216\n"
"v_mul_f32 v45, s57, v45\nCV 42,44,45\nMvaa 228,168,216\nv_mul_f32 v46, s57, v46\nv_mul_f32 v47, s57, v47\nMvaa 228,172,220\nCV 43,46,47\nAR 20,96\nMvaa 224,164,220\nAR 21,97\n"
"v_mul_f32 v20, s57, v20\nMvaa 224,160,216\ns_waitcnt vmcnt(14) lgkmcnt(0)\ns_barrier\nMvaa 48,224,192\ns_add_u32 m0, s47, 49152\ns_add_u32 s44, s44, 0x40000\nRa 128,10,0\n"
"Mvaa 48,228,196\nRa 132,11,0\nMvaa 52,236,196\nD0 6,24,44,0\nRa 136,10,2048\n.W553:\ns_cbranch_execz .W557\nMvaa 52,232,192\nRa 140,11,2048\nv_mul_f32 v21, s57, v21\n"
"Mvaa 56,240,192\nRa 144,10,8192\nCV 36,20,21\nMvaa 56,244,196\nRa 148,11,8192\nAR 20,98\nMvaa 60,252,196\nD0 7,24,44,1024\nRa 152,10,10240\nMvaa 60,248,192\nRa 156,11,10240\n"
"AR 21,99\nMaaa 124,248,200\nTa 160,12,0\nv_mul_f32 v20, s57, v20\nMaaa 124,252,204\nTa 162,12,2048\nMaaa 120,244,204\nD0 8,24,44,2048\nTa 164,12,16384\nMaaa 120,240,200\n"
"Ta 166,12,18432\nv_mul_f32 v21, s57, v21\nMaaa 116,232,200\nTa 168,13,0\nCV 37,20,21\nMaaa 116,236,204\nTa 170,13,2048\nAR 20,100\nMaaa 112,228,204\nD0 9,24,44,3072\n"
"Ta 172,13,16384\nMaaa 112,224,200\nTa 174,13,18432\nAR 21,101\nMvaa 208,224,208\ns_add_u32 m0, s47, -16384\ns_add_u32 s48, s48, 128\nTa 176,14,0\nMvaa 208,228,212\nTa 178,14,2048\n"
"Mvaa 212,236,212\nD0 2,20,48,0\nTa 180,14,16384\nMvaa 212,232,208\nTa 182,14,18432\nv_mul_f32 v20, s57, v20\nMvaa 216,240,208\nTa 184,15,0\nv_mul_f32 v21, s57, v21\n"
"Mvaa 216,244,212\nTa 186,15,2048\nCV 38,20,21\nMvaa 220,252,212\nD0 3,20,48,1024\nTa 188,15,16384\nMvaa 220,248,208\nTa 190,15,18432\nAR 20,102\nMvaa 252,248,216\nAR 21,103\n"
"v_mul_f32 v20, s57, v20\nMvaa 252,252,220\nv_mul_f32 v21, s57, v21\nMvaa 248,244,220\nD0 4,20,48,2048\nCV 39,20,21\nMvaa 248,240,216\nAR 20,104\nAR 21,105\nMvaa 244,232,216\n"
"v_mul_f32 v20, s57, v20\nv_mul_f32 v21, s57, v21\nMvaa 244,236,220\nCV 44,20,21\nAR 20,106\nMvaa 240,228,220\nD0 5,20,48,3072\nAR 21,107\nMvaa 240,224,216\nWL 0\ns_barrier\n"
"Zvaa 64,128,160\nTa 224,16,0\nv_mul_f32 v20, s57, v20\nMvaa 64,132,164\nTa 226,16,2048\nZvaa 68,140,164\ns_add_u32 m0, s47, -65536\ns_add_u32 s45, s45, 128\nMvaa 68,136,160\n"
"Ta 228,16,16384\nv_mul_f32 v21, s57, v21\nZvaa 128,144,160\nTa 230,16,18432\nCV 45,20,21\nMvaa 128,148,164\nD0 2,20,45,0\nAR 20,108\nZvaa 132,156,164\nTa 232,17,0\nAR 21,109\n"
"Mvaa 132,152,160\nTa 234,17,2048\nv_mul_f32 v20, s57, v20\nZvaa 140,152,168\nv_mul_f32 v21, s57, v21\nCV 46,20,21\nMvaa 140,156,172\nTa 236,17,16384\ns_add_u32 s6, s56, 0\n"
"Sv 32,1,16,6,0\nZvaa 136,148,172\nTa 238,17,18432\nAR 20,110\nMvaa 136,144,168\nAR 21,111\nv_mul_f32 v20, s57, v20\nZvaa 76,136,168\nTa 240,18,0\nv_mul_f32 v21, s57, v21\n"
"Mvaa 76,140,172\nTa 242,18,2048\nCV 47,20,21\nZvaa 72,132,172\nD0 3,20,45,1024\nv_mul_f32 v48, s57, v48\nMvaa 72,128,168\nTa 244,18,16384\nv_mul_f32 v49, s57, v49\n"
"Zvaa 80,128,176\nTa 246,18,18432\nCV 48,48,49\nMvaa 80,132,180\nv_mul_f32 v50, s57, v50\nZvaa 84,140,180\nTa 248,19,0\nv_mul_f32 v51, s57, v51\nMvaa 84,136,176\nTa 250,19,2048\n"
"CV 49,50,51\nZvaa 144,144,176\nv_mul_f32 v52, s57, v52\nv_mul_f32 v53, s57, v53\nMvaa 144,148,180\nTa 252,19,16384\nCV 50,52,53\nZvaa 148,156,180\nTa 254,19,18432\n"
"v_mul_f32 v54, s57, v54\nMvaa 148,152,176\nD0 4,20,45,2048\nv_mul_f32 v55, s57, v55\nZvaa 156,152,184\nCV 51,54,55\nv_mul_f32 v56, s57, v56\nMvaa 156,156,188\n"
"v_mul_f32 v57, s57, v57\ns_add_u32 s6, s56, 0\nSv 40,1,16,6,2048\nZvaa 152,148,188\nCV 56,56,57\nv_mul_f32 v58, s57, v58\nMvaa 152,144,184\nv_mul_f32 v59, s57, v59\nCV 57,58,59\n"
"Zvaa 92,136,184\nv_mul_f32 v60, s57, v60\nv_mul_f32 v61, s57, v61\nMvaa 92,140,188\nD0 5,20,45,3072\nCV 58,60,61\nZvaa 88,132,188\nv_mul_f32 v62, s57, v62\n"
"v_mul_f32 v63, s57, v63\nMvaa 88,128,184\ns_waitcnt vmcnt(18) lgkmcnt(0)\ns_barrier\nZvaa 96,128,224\nRa 192,10,16384\nCV 59,62,63\nMvaa 96,132,228\ns_add_u32 m0, s47, 0\n"
"s_add_u32 s44, s44, 0x40000\nZvaa 100,140,228\nRa 196,11,16384\nAR 20,112\nMvaa 100,136,224\nAR 21,113\nv_mul_f32 v20, s57, v20\nZvaa 160,144,224\nRa 200,10,18432\n"
"v_mul_f32 v21, s57, v21\nMvaa 160,148,228\nD0 6,24,44,0\nCV 52,20,21\nZvaa 164,156,228\nRa 204,11,18432\nAR 20,114\nMvaa 164,152,224\nAR 21,115\nv_mul_f32 v20, s57, v20\n"
"Zvaa 172,152,232\nRa 208,10,24576\nv_mul_f32 v21, s57, v21\nMvaa 172,156,236\nCV 53,20,21\ns_add_u32 s6, s56, 0\nSv 36,1,16,6,1024\nZvaa 168,148,236\nRa 212,11,24576\nAR 20,116\n"
"Mvaa 168,144,232\nAR 21,117\nv_mul_f32 v20, s57, v20\nZvaa 108,136,232\nRa 216,10,26624\nv_mul_f32 v21, s57, v21\nMvaa 108,140,236\nD0 7,24,44,1024\nCV 54,20,21\nZvaa 104,132,236\n"
"Ra 220,11,26624\nAR 20,118\nMvaa 104,128,232\nAR 21,119\nv_mul_f32 v20, s57, v20\nZvaa 112,128,240\nv_mul_f32 v21, s57, v21\nCV 55,20,21\nMvaa 112,132,244\nAR 20,120\n"
"Zvaa 116,140,244\nAR 21,121\nv_mul_f32 v20, s57, v20\nMvaa 116,136,240\nv_mul_f32 v21, s57, v21\nCV 60,20,21\nZvaa 176,144,240\nAR 20,122\nAR 21,123\nMvaa 176,148,244\n"
"D0 8,24,44,2048\nv_mul_f32 v20, s57, v20\nZvaa 180,156,244\nv_mul_f32 v21, s57, v21\nCV 61,20,21\nMvaa 180,152,240\nAR 20,124\nAR 21,125\nZvaa 188,152,248\n"
"v_mul_f32 v20, s57, v20\nv_mul_f32 v21, s57, v21\nMvaa 188,156,252\nCV 62,20,21\ns_add_u32 s6, s56, 0\nSv 44,1,16,6,3072\nZvaa 184,148,252\nAR 20,126\nAR 21,127\nMvaa 184,144,248\n"
"v_mul_f32 v20, s57, v20\nv_mul_f32 v21, s57, v21\nZvaa 124,136,248\nCV 63,20,21\nv_mul_f32 v192, s57, v192\nMvaa 124,140,252\nD0 9,24,44,3072\nv_mul_f32 v193, s57, v193\n"
"Zvaa 120,132,252\nCV 192,192,193\nv_mul_f32 v194, s57, v194\nMvaa 120,128,248\nWL 0\nZaaa 32,192,224\nRa 128,10,32768\nv_mul_f32 v195, s57, v195\nMaaa 32,196,228\n"
"s_add_u32 m0, s47, 16384\ns_add_u32 s44, s44, 0x40000\nZaaa 36,204,228\nRa 132,11,32768\nCV 193,194,195\nMaaa 36,200,224\nv_mul_f32 v196, s57, v196\nv_mul_f32 v197, s57, v197\n"
"Zaaa 96,208,224\nRa 136,10,34816\nCV 194,196,197\nMaaa 96,212,228\nD0 6,24,44,0\nv_mul_f32 v198, s57, v198\nZaaa 100,220,228\nRa 140,11,34816\nv_mul_f32 v199, s57, v199\n"
"Maaa 100,216,224\nCV 195,198,199\nv_mul_f32 v200, s57, v200\nZaaa 108,216,232\nRa 144,10,40960\nv_mul_f32 v201, s57, v201\nMaaa 108,220,236\nCV 200,200,201\n"
"v_mul_f32 v202, s57, v202\ns_add_u32 s6, s56, 4096\nSv 48,1,16,6,0\nZaaa 104,212,236\nRa 148,11,40960\nv_mul_f32 v203, s57, v203\nMaaa 104,208,232\nCV 201,202,203\n"
"v_mul_f32 v204, s57, v204\nZaaa 44,200,232\nRa 152,10,43008\nv_mul_f32 v205, s57, v205\nMaaa 44,204,236\nD0 7,24,44,1024\nCV 202,204,205\nZaaa 40,196,236\nRa 156,11,43008\n"
"v_mul_f32 v206, s57, v206\nMaaa 40,192,232\nv_mul_f32 v207, s57, v207\nCV 203,206,207\nZaaa 48,192,240\nv_mul_f32 v208, s57, v208\nv_mul_f32 v209, s57, v209\nMaaa 48,196,244\n"
"CV 208,208,209\nv_mul_f32 v210, s57, v210\nZaaa 52,204,244\nv_mul_f32 v211, s57, v211\nCV 209,210,211\nMaaa 52,200,240\nv_mul_f32 v212, s57, v212\nv_mul_f32 v213, s57, v213\n"
"Zaaa 112,208,240\nCV 210,212,213\nv_mul_f32 v214, s57, v214\nMaaa 112,212,244\nD0 8,24,44,2048\nv_mul_f32 v215, s57, v215\nZaaa 116,220,244\nCV 211,214,215\n"
"v_mul_f32 v216, s57, v216\nMaaa 116,216,240\nv_mul_f32 v217, s57, v217\nCV 216,216,217\nZaaa 124,216,248\nv_mul_f32 v218, s57, v218\nv_mul_f32 v219, s57, v219\nMaaa 124,220,252\n"
"CV 217,218,219\nv_mul_f32 v220, s57, v220\ns_add_u32 s6, s56, 4096\nSv 56,1,16,6,2048\nZaaa 120,212,252\nv_mul_f32 v221, s57, v221\nCV 218,220,221\nMaaa 120,208,248\n"
"v_mul_f32 v222, s57, v222\nv_mul_f32 v223, s57, v223\nZaaa 60,200,248\nCV 219,222,223\nv_mul_f32 v224, s57, v224\nMaaa 60,204,252\nD0 9,24,44,3072\nv_mul_f32 v225, s57, v225\n"
"Zaaa 56,196,252\nCV 224,224,225\nv_mul_f32 v226, s57, v226\nMaaa 56,192,248\ns_waitcnt vmcnt(22) lgkmcnt(0)\ns_barrier\nZaaa 0,192,160\nTa 224,16,32768\nv_mul_f32 v227, s57, v227\n"
"Maaa 0,196,164\nTa 226,16,34816\nCV 225,226,227\nZaaa 4,204,164\ns_add_u32 m0, s47, -49152\ns_add_u32 s48, s48, 128\nMaaa 4,200,160\nTa 228,16,49152\nv_mul_f32 v228, s57, v228\n"
"Zaaa 64,208,160\nTa 230,16,51200\nv_mul_f32 v229, s57, v229\nMaaa 64,212,164\nD0 2,20,48,0\nCV 226,228,229\nZaaa 68,220,164\nTa 232,17,32768\nv_mul_f32 v230, s57, v230\n"
"Maaa 68,216,160\nTa 234,17,34816\nv_mul_f32 v231, s57, v231\nZaaa 76,216,168\nCV 227,230,231\nv_mul_f32 v232, s57, v232\nMaaa 76,220,172\nTa 236,17,49152\n"
"v_mul_f32 v233, s57, v233\ns_add_u32 s6, s56, 4096\nSv 52,1,16,6,1024\nZaaa 72,212,172\nTa 238,17,51200\nCV 232,232,233\nMaaa 72,208,168\nv_mul_f32 v234, s57, v234\n"
"v_mul_f32 v235, s57, v235\nZaaa 12,200,168\nTa 240,18,32768\nCV 233,234,235\nMaaa 12,204,172\nTa 242,18,34816\nv_mul_f32 v236, s57, v236\nZaaa 8,196,172\nD0 3,20,48,1024\n"
"v_mul_f32 v237, s57, v237\nMaaa 8,192,168\nTa 244,18,49152\nCV 234,236,237\nZaaa 16,192,176\nTa 246,18,51200\nv_mul_f32 v238, s57, v238\nMaaa 16,196,180\n"
"v_mul_f32 v239, s57, v239\nCV 235,238,239\nZaaa 20,204,180\nTa 248,19,32768\nv_mul_f32 v240, s57, v240\nMaaa 20,200,176\nTa 250,19,34816\nv_mul_f32 v241, s57, v241\n"
"Zaaa 80,208,176\nCV 240,240,241\nv_mul_f32 v242, s57, v242\nMaaa 80,212,180\nTa 252,19,49152\nv_mul_f32 v243, s57, v243\nZaaa 84,220,180\nTa 254,19,51200\nCV 241,242,243\n"
"Maaa 84,216,176\nD0 4,20,48,2048\nv_mul_f32 v244, s57, v244\nZaaa 92,216,184\nv_mul_f32 v245, s57, v245\nCV 242,244,245\nMaaa 92,220,188\nv_mul_f32 v246, s57, v246\n"
"v_mul_f32 v247, s57, v247\ns_add_u32 s6, s56, 4096\nSv 60,1,16,6,3072\nZaaa 88,212,188\nCV 243,246,247\nv_mul_f32 v248, s57, v248\nMaaa 88,208,184\nv_mul_f32 v249, s57, v249\n"
"CV 248,248,249\nZaaa 28,200,184\nv_mul_f32 v250, s57, v250\nv_mul_f32 v251, s57, v251\nMaaa 28,204,188\nD0 5,20,48,3072\nCV 249,250,251\nZaaa 24,196,188\n"
"v_mul_f32 v252, s57, v252\nv_mul_f32 v253, s57, v253\nMaaa 24,192,184\nWL 0\nMvaa 96,128,224\nTa 160,12,32768\nCV 250,252,253\nMvaa 96,132,228\nTa 162,12,34816\n"
"v_mul_f32 v254, s57, v254\nMvaa 100,140,228\ns_add_u32 m0, s47, -32768\ns_add_u32 s45, s45, 128\nMvaa 100,136,224\nTa 164,12,49152\nv_mul_f32 v255, s57, v255\nMvaa 160,144,224\n"
"Ta 166,12,51200\nCV 251,254,255\nMvaa 160,148,228\nD0 2,20,45,0\nMvaa 164,156,228\nTa 168,13,32768\nMvaa 164,152,224\nTa 170,13,34816\nMvaa 172,152,232\nMvaa 172,156,236\n"
"Ta 172,13,49152\ns_add_u32 s6, s53, 0\nSv 192,1,16,6,0\nMvaa 168,148,236\nTa 174,13,51200\nMvaa 168,144,232\nMvaa 108,136,232\nTa 176,14,32768\nMvaa 108,140,236\nTa 178,14,34816\n"
"Mvaa 104,132,236\nD0 3,20,45,1024\nMvaa 104,128,232\nTa 180,14,49152\nMvaa 112,128,240\nTa 182,14,51200\nMvaa 112,132,244\nMvaa 116,140,244\nTa 184,15,32768\nMvaa 116,136,240\n"
"Ta 186,15,34816\nMvaa 176,144,240\nMvaa 176,148,244\nTa 188,15,49152\nMvaa 180,156,244\nTa 190,15,51200\nMvaa 180,152,240\nD0 4,20,45,2048\nMvaa 188,152,248\nMvaa 188,156,252\n"
"s_add_u32 s6, s53, 0\nSv 200,1,16,6,2048\nMvaa 184,148,252\nMvaa 184,144,248\nMvaa 124,136,248\nMvaa 124,140,252\nD0 5,20,45,3072\nMvaa 120,132,252\nMvaa 120,128,248\n"
"s_waitcnt vmcnt(24) lgkmcnt(0)\ns_barrier\nMvaa 64,128,160\nRa 192,10,49152\nMvaa 64,132,164\ns_add_u32 m0, s47, 32768\ns_add_u32 s44, s44, 0x40000\nMvaa 68,140,164\n"
"Ra 196,11,49152\nMvaa 68,136,160\nMvaa 128,144,160\nRa 200,10,51200\nMvaa 128,148,164\nD0 6,24,44,0\nMvaa 132,156,164\nRa 204,11,51200\nMvaa 132,152,160\nMvaa 140,152,168\n"
"Ra 208,10,57344\nMvaa 140,156,172\ns_add_u32 s6, s53, 4096\nSv 208,1,16,6,0\nMvaa 136,148,172\nRa 212,11,57344\nMvaa 136,144,168\nMvaa 76,136,168\nRa 216,10,59392\n"
"Mvaa 76,140,172\nD0 7,24,44,1024\nMvaa 72,132,172\nRa 220,11,59392\nMvaa 72,128,168\nMvaa 80,128,176\nMvaa 80,132,180\nMvaa 84,140,180\nMvaa 84,136,176\nMvaa 144,144,176\n"
"Mvaa 144,148,180\n.W554:\ns_cbranch_execz .W558\nD0 8,24,44,2048\nMvaa 148,156,180\nMvaa 148,152,176\nMvaa 156,152,184\nMvaa 156,156,188\ns_add_u32 s6, s53, 4096\n"
"Sv 216,1,16,6,2048\nMvaa 152,148,188\nMvaa 152,144,184\nMvaa 92,136,184\nMvaa 92,140,188\nD0 9,24,44,3072\nMvaa 88,132,188\nMvaa 88,128,184\nWL 0\nMaaa 0,192,160\nRa 128,10,0\n"
"Maaa 0,196,164\ns_add_u32 m0, s47, 49152\ns_add_u32 s44, s44, 0x40000\nMaaa 4,204,164\nRa 132,11,0\nMaaa 4,200,160\nMaaa 64,208,160\nRa 136,10,2048\nMaaa 64,212,164\nD0 6,24,44,0\n"
"Maaa 68,220,164\nRa 140,11,2048\nMaaa 68,216,160\nMaaa 76,216,168\nRa 144,10,8192\nMaaa 76,220,172\ns_add_u32 s6, s53, 0\nSv 224,1,16,6,1024\nMaaa 72,212,172\nRa 148,11,8192\n"
"Maaa 72,208,168\nMaaa 12,200,168\nRa 152,10,10240\nMaaa 12,204,172\nD0 7,24,44,1024\nMaaa 8,196,172\nRa 156,11,10240\nMaaa 8,192,168\nMaaa 16,192,176\nMaaa 16,196,180\n"
"Maaa 20,204,180\nMaaa 20,200,176\nMaaa 80,208,176\nMaaa 80,212,180\nD0 8,24,44,2048\nMaaa 84,220,180\nMaaa 84,216,176\nMaaa 92,216,184\nMaaa 92,220,188\ns_add_u32 s6, s53, 0\n"
"Sv 232,1,16,6,3072\nMaaa 88,212,188\nMaaa 88,208,184\nMaaa 28,200,184\nMaaa 28,204,188\nD0 9,24,44,3072\nMaaa 24,196,188\nMaaa 24,192,184\ns_waitcnt vmcnt(24) lgkmcnt(0)\n"
"s_barrier\nMaaa 32,192,224\nTa 160,12,0\nMaaa 32,196,228\nTa 162,12,2048\nMaaa 36,204,228\ns_add_u32 m0, s47, -16384\ns_add_u32 s48, s48, 128\nMaaa 36,200,224\nTa 164,12,16384\n"
"Maaa 96,208,224\nTa 166,12,18432\nMaaa 96,212,228\nD0 2,20,48,0\nMaaa 100,220,228\nTa 168,13,0\nMaaa 100,216,224\nTa 170,13,2048\nMaaa 108,216,232\nMaaa 108,220,236\n"
"Ta 172,13,16384\ns_add_u32 s6, s53, 4096\nSv 240,1,16,6,1024\nMaaa 104,212,236\nTa 174,13,18432\nMaaa 104,208,232\nMaaa 44,200,232\nTa 176,14,0\nMaaa 44,204,236\nTa 178,14,2048\n"
"Maaa 40,196,236\nD0 3,20,48,1024\nMaaa 40,192,232\nTa 180,14,16384\nMaaa 48,192,240\nTa 182,14,18432\nMaaa 48,196,244\ns_add_u32 s6, s53, 4096\nSv 248,1,16,6,3072\n"
"Maaa 52,204,244\nTa 184,15,0\nMaaa 52,200,240\nTa 186,15,2048\nMaaa 112,208,240\nMaaa 112,212,244\nTa 188,15,16384\nMaaa 116,220,244\nTa 190,15,18432\nMaaa 116,216,240\n"
"D0 4,20,48,2048\nMaaa 124,216,248\nMaaa 124,220,252\nMaaa 120,212,252\nMaaa 120,208,248\ns_lshl3_add_u32 s90, s12, s13\ns_lshl2_add_u32 s90, s90, s11\n"
"s_lshl2_add_u32 s90, s90, 0xc04a400\nMaaa 60,200,248\nMaaa 60,204,252\nD0 5,20,48,3072\nMaaa 56,196,252\nMaaa 56,192,248\nWL 0\ns_mov_b64 s[76:77], s[62:63]\n"
"s_mov_b64 s[78:79], s[64:65]\ns_mov_b32 s42, 1605632\ns_mov_b32 s50, 1\n.GL150:\nMvaa 64,128,160\nTa 224,16,0\nMvaa 64,132,164\nTa 226,16,2048\nMvaa 68,140,164\n"
"s_add_u32 m0, s47, -65536\ns_add_u32 s45, s45, 128\nMvaa 68,136,160\nTa 228,16,16384\nMvaa 128,144,160\nTa 230,16,18432\nMvaa 128,148,164\nD0 2,20,45,0\nMvaa 132,156,164\n"
"Ta 232,17,0\nMvaa 132,152,160\nTa 234,17,2048\nMvaa 140,152,168\nMvaa 140,156,172\nTa 236,17,16384\nMvaa 136,148,172\nTa 238,17,18432\nMvaa 136,144,168\nMvaa 76,136,168\n"
"Ta 240,18,0\nMvaa 76,140,172\nTa 242,18,2048\nMvaa 72,132,172\nD0 3,20,45,1024\nMvaa 72,128,168\nTa 244,18,16384\nMvaa 80,128,176\nTa 246,18,18432\nMvaa 80,132,180\n"
"Mvaa 84,140,180\nTa 248,19,0\nMvaa 84,136,176\nTa 250,19,2048\nMvaa 144,144,176\nMvaa 144,148,180\nTa 252,19,16384\nMvaa 148,156,180\nTa 254,19,18432\nMvaa 148,152,176\n"
"D0 4,20,45,2048\nMvaa 156,152,184\nMvaa 156,156,188\nMvaa 152,148,188\nMvaa 152,144,184\ns_mov_b32 s40, s76\ns_and_b32 s41, s77, 0xffff\nMvaa 92,136,184\nMvaa 92,140,188\n"
"D0 5,20,45,3072\nMvaa 88,132,188\nMvaa 88,128,184\ns_waitcnt vmcnt(17) lgkmcnt(0)\ns_barrier\nMvaa 96,128,224\nRa 192,10,16384\nMvaa 96,132,228\ns_add_u32 m0, s47, 0\n"
"s_add_u32 s44, s44, 0x40000\nbuffer_load_dword v31, v26, s[40:43], s97 offen\ns_mov_b64 s[76:77], s[66:67]\nMvaa 100,140,228\nRa 196,11,16384\nMvaa 100,136,224\nMvaa 160,144,224\n"
"Ra 200,10,18432\nMvaa 160,148,228\nD0 6,24,44,0\nMvaa 164,156,228\nRa 204,11,18432\nMvaa 164,152,224\nMvaa 172,152,232\nRa 208,10,24576\nMvaa 172,156,236\nMvaa 168,148,236\n"
"Ra 212,11,24576\nMvaa 168,144,232\nMvaa 108,136,232\nRa 216,10,26624\nMvaa 108,140,236\nD0 7,24,44,1024\nMvaa 104,132,236\nRa 220,11,26624\nMvaa 104,128,232\nMvaa 112,128,240\n"
"Mvaa 112,132,244\nMvaa 116,140,244\nMvaa 116,136,240\nMvaa 176,144,240\nMvaa 176,148,244\nD0 8,24,44,2048\nMvaa 180,156,244\nMvaa 180,152,240\nMvaa 188,152,248\nMvaa 188,156,252\n"
"Mvaa 184,148,252\nMvaa 184,144,248\nMvaa 124,136,248\nMvaa 124,140,252\nD0 9,24,44,3072\nMvaa 120,132,252\nMvaa 120,128,248\nWL 0\nMaaa 32,192,224\nRa 128,10,32768\n"
"Maaa 32,196,228\ns_add_u32 m0, s47, 16384\ns_add_u32 s44, s44, 0x40000\nMaaa 36,204,228\nRa 132,11,32768\nMaaa 36,200,224\nMaaa 96,208,224\nRa 136,10,34816\nMaaa 96,212,228\n"
"D0 6,24,44,0\nMaaa 100,220,228\nRa 140,11,34816\nMaaa 100,216,224\nMaaa 108,216,232\nRa 144,10,40960\nMaaa 108,220,236\nMaaa 104,212,236\nRa 148,11,40960\nMaaa 104,208,232\n"
"Maaa 44,200,232\nRa 152,10,43008\nMaaa 44,204,236\nD0 7,24,44,1024\nMaaa 40,196,236\nRa 156,11,43008\nMaaa 40,192,232\nMaaa 48,192,240\nMaaa 48,196,244\nMaaa 52,204,244\n"
"Maaa 52,200,240\nMaaa 112,208,240\nMaaa 112,212,244\nD0 8,24,44,2048\nMaaa 116,220,244\nMaaa 116,216,240\nMaaa 124,216,248\nMaaa 124,220,252\nMaaa 120,212,252\nMaaa 120,208,248\n"
"Maaa 60,200,248\nMaaa 60,204,252\nD0 9,24,44,3072\nMaaa 56,196,252\nMaaa 56,192,248\ns_waitcnt vmcnt(17) lgkmcnt(0)\ns_barrier\nMaaa 0,192,160\nTa 224,16,32768\nMaaa 0,196,164\n"
"Ta 226,16,34816\nMaaa 4,204,164\ns_add_u32 m0, s47, -49152\ns_add_u32 s48, s48, 128\nMaaa 4,200,160\nTa 228,16,49152\nMaaa 64,208,160\nTa 230,16,51200\nMaaa 64,212,164\n"
"D0 2,20,48,0\nMaaa 68,220,164\nTa 232,17,32768\nMaaa 68,216,160\nTa 234,17,34816\nMaaa 76,216,168\nMaaa 76,220,172\nTa 236,17,49152\nMaaa 72,212,172\nTa 238,17,51200\n"
"Maaa 72,208,168\nMaaa 12,200,168\nTa 240,18,32768\nMaaa 12,204,172\nTa 242,18,34816\nMaaa 8,196,172\nD0 3,20,48,1024\nMaaa 8,192,168\nTa 244,18,49152\nMaaa 16,192,176\n"
"Ta 246,18,51200\nMaaa 16,196,180\nMaaa 20,204,180\nTa 248,19,32768\nMaaa 20,200,176\nTa 250,19,34816\nMaaa 80,208,176\nMaaa 80,212,180\nTa 252,19,49152\nMaaa 84,220,180\n"
"Ta 254,19,51200\nMaaa 84,216,176\nD0 4,20,48,2048\nMaaa 92,216,184\nMaaa 92,220,188\nMaaa 88,212,188\nMaaa 88,208,184\nMaaa 28,200,184\nMaaa 28,204,188\nD0 5,20,48,3072\n"
"Maaa 24,196,188\nMaaa 24,192,184\nWL 0\nMvaa 96,128,224\nTa 160,12,32768\nMvaa 96,132,228\nTa 162,12,34816\nMvaa 100,140,228\ns_add_u32 m0, s47, -32768\ns_add_u32 s45, s45, 128\n"
"Mvaa 100,136,224\nTa 164,12,49152\nMvaa 160,144,224\nTa 166,12,51200\nMvaa 160,148,228\nD0 2,20,45,0\nMvaa 164,156,228\nTa 168,13,32768\nMvaa 164,152,224\nTa 170,13,34816\n"
"Mvaa 172,152,232\nMvaa 172,156,236\nTa 172,13,49152\nMvaa 168,148,236\nTa 174,13,51200\nMvaa 168,144,232\nMvaa 108,136,232\nTa 176,14,32768\nMvaa 108,140,236\nTa 178,14,34816\n"
"Mvaa 104,132,236\nD0 3,20,45,1024\nMvaa 104,128,232\nTa 180,14,49152\nMvaa 112,128,240\nTa 182,14,51200\nMvaa 112,132,244\nMvaa 116,140,244\nTa 184,15,32768\nMvaa 116,136,240\n"
"Ta 186,15,34816\nMvaa 176,144,240\nMvaa 176,148,244\nTa 188,15,49152\nMvaa 180,156,244\nTa 190,15,51200\nMvaa 180,152,240\nD0 4,20,45,2048\nMvaa 188,152,248\nMvaa 188,156,252\n"
"Mvaa 184,148,252\nMvaa 184,144,248\ns_mov_b32 s40, s78\ns_and_b32 s41, s79, 0xffff\nMvaa 124,136,248\nMvaa 124,140,252\nD0 5,20,45,3072\nMvaa 120,132,252\nMvaa 120,128,248\n"
"s_waitcnt vmcnt(17) lgkmcnt(0)\ns_barrier\nMvaa 64,128,160\nRa 192,10,49152\nMvaa 64,132,164\ns_add_u32 m0, s47, 32768\ns_add_u32 s44, s44, 0x40000\n"
"buffer_load_dword v31, v26, s[40:43], s97 offen\ns_mov_b64 s[78:79], s[66:67]\nMvaa 68,140,164\nRa 196,11,49152\nMvaa 68,136,160\nMvaa 128,144,160\nRa 200,10,51200\n"
"Mvaa 128,148,164\nD0 6,24,44,0\nMvaa 132,156,164\nRa 204,11,51200\nMvaa 132,152,160\nMvaa 140,152,168\nRa 208,10,57344\nMvaa 140,156,172\nMvaa 136,148,172\nRa 212,11,57344\n"
"Mvaa 136,144,168\nMvaa 76,136,168\nRa 216,10,59392\nMvaa 76,140,172\nD0 7,24,44,1024\nMvaa 72,132,172\nRa 220,11,59392\nMvaa 72,128,168\nMvaa 80,128,176\nMvaa 80,132,180\n"
"Mvaa 84,140,180\nMvaa 84,136,176\nMvaa 144,144,176\nMvaa 144,148,180\nD0 8,24,44,2048\nMvaa 148,156,180\nMvaa 148,152,176\nMvaa 156,152,184\nMvaa 156,156,188\nMvaa 152,148,188\n"
"Mvaa 152,144,184\nMvaa 92,136,184\nMvaa 92,140,188\nD0 9,24,44,3072\nMvaa 88,132,188\nMvaa 88,128,184\nWL 0\nMaaa 0,192,160\nRa 128,10,0\nMaaa 0,196,164\ns_add_u32 m0, s47, 49152\n"
"s_add_u32 s44, s44, 0x40000\nMaaa 4,204,164\nRa 132,11,0\nMaaa 4,200,160\nMaaa 64,208,160\nRa 136,10,2048\nMaaa 64,212,164\nD0 6,24,44,0\nMaaa 68,220,164\nRa 140,11,2048\n"
"Maaa 68,216,160\nMaaa 76,216,168\nRa 144,10,8192\nMaaa 76,220,172\nMaaa 72,212,172\nRa 148,11,8192\nMaaa 72,208,168\nMaaa 12,200,168\nRa 152,10,10240\nMaaa 12,204,172\n"
"D0 7,24,44,1024\nMaaa 8,196,172\nRa 156,11,10240\nMaaa 8,192,168\nMaaa 16,192,176\nMaaa 16,196,180\nMaaa 20,204,180\nMaaa 20,200,176\nMaaa 80,208,176\nMaaa 80,212,180\n"
"D0 8,24,44,2048\nMaaa 84,220,180\nMaaa 84,216,176\nMaaa 92,216,184\nMaaa 92,220,188\nMaaa 88,212,188\nMaaa 88,208,184\nMaaa 28,200,184\nMaaa 28,204,188\nD0 9,24,44,3072\n"
"Maaa 24,196,188\nMaaa 24,192,184\ns_waitcnt vmcnt(17) lgkmcnt(0)\ns_barrier\nMaaa 32,192,224\nTa 160,12,0\nMaaa 32,196,228\nTa 162,12,2048\nMaaa 36,204,228\n"
"s_add_u32 m0, s47, -16384\ns_add_u32 s48, s48, 128\nMaaa 36,200,224\nTa 164,12,16384\nMaaa 96,208,224\nTa 166,12,18432\nMaaa 96,212,228\nD0 2,20,48,0\nMaaa 100,220,228\n"
"Ta 168,13,0\nMaaa 100,216,224\nTa 170,13,2048\nMaaa 108,216,232\nMaaa 108,220,236\nTa 172,13,16384\nMaaa 104,212,236\nTa 174,13,18432\nMaaa 104,208,232\nMaaa 44,200,232\n"
"Ta 176,14,0\nMaaa 44,204,236\nTa 178,14,2048\n.W555:\ns_cbranch_execz .W559\nMaaa 40,196,236\nD0 3,20,48,1024\nMaaa 40,192,232\nTa 180,14,16384\nMaaa 48,192,240\nTa 182,14,18432\n"
"Maaa 48,196,244\nMaaa 52,204,244\nTa 184,15,0\nMaaa 52,200,240\nTa 186,15,2048\nMaaa 112,208,240\nMaaa 112,212,244\nTa 188,15,16384\nMaaa 116,220,244\nTa 190,15,18432\n"
"Maaa 116,216,240\nD0 4,20,48,2048\nMaaa 124,216,248\nMaaa 124,220,252\nMaaa 120,212,252\nMaaa 120,208,248\nMaaa 60,200,248\nMaaa 60,204,252\nD0 5,20,48,3072\n"
"s_sub_u32 s50, s50, 1\nMaaa 56,196,252\nMaaa 56,192,248\nWL 0\ns_cbranch_scc0 .GL150\nMvaa 64,128,160\nTa 224,16,0\nMvaa 64,132,164\nTa 226,16,2048\nMvaa 68,140,164\n"
"s_add_u32 m0, s47, -65536\ns_add_u32 s45, s45, 128\nMvaa 68,136,160\nTa 228,16,16384\nMvaa 128,144,160\nTa 230,16,18432\nMvaa 128,148,164\nD0 2,20,45,0\nMvaa 132,156,164\n"
"Ta 232,17,0\nMvaa 132,152,160\nTa 234,17,2048\nMvaa 140,152,168\ns_and_b32 s2, s74, 15\nMvaa 140,156,172\nTa 236,17,16384\n"
"buffer_store_dword v25, off, s[16:19], s90 offset:0 sc0 sc1\nMvaa 136,148,172\nTa 238,17,18432\nMvaa 136,144,168\ns_sub_u32 s2, s2, 2\ns_cmp_lt_u32 s2, 14\n"
"s_cselect_b32 s5, 0, 0x40000000\nMvaa 76,136,168\nTa 240,18,0\nMvaa 76,140,172\nTa 242,18,2048\nMvaa 72,132,172\nD0 3,20,45,1024\nMvaa 72,128,168\nTa 244,18,16384\n"
"Mvaa 80,128,176\nTa 246,18,18432\nMvaa 80,132,180\ns_and_b32 s2, s2, 15\nMvaa 84,140,180\nTa 248,19,0\nMvaa 84,136,176\nTa 250,19,2048\nMvaa 144,144,176\ns_mul_i32 s4, s73, 14\n"
"Mvaa 144,148,180\nTa 252,19,16384\nMvaa 148,156,180\nTa 254,19,18432\nMvaa 148,152,176\nD0 4,20,45,2048\nMvaa 156,152,184\ns_add_u32 s4, s4, s2\nMvaa 156,156,188\n"
"s_mov_b32 s36, s62\nMvaa 152,148,188\ns_and_b32 s37, s63, 0xffff\nMvaa 152,144,184\ns_mov_b32 s38, 0x188000\nMvaa 92,136,184\ns_mov_b32 s39, 0x20000\nMvaa 92,140,188\n"
"D0 5,20,45,3072\nMvaa 88,132,188\ns_mov_b32 s40, s64\nMvaa 88,128,184\ns_waitcnt vmcnt(18) lgkmcnt(0)\ns_barrier\nMvaa 96,128,224\nRa 192,10,16384\nMvaa 96,132,228\n"
"s_add_u32 m0, s47, 0\ns_add_u32 s44, s44, 0x40000\nMvaa 100,140,228\nRa 196,11,16384\nMvaa 100,136,224\ns_and_b32 s41, s65, 0xffff\nMvaa 160,144,224\nRa 200,10,18432\n"
"Mvaa 160,148,228\nD0 6,24,44,0\nMvaa 164,156,228\nRa 204,11,18432\nMvaa 164,152,224\ns_mov_b32 s42, 0x188000\nMvaa 172,152,232\nRa 208,10,24576\nMvaa 172,156,236\n"
"s_mov_b32 s43, 0x20000\nMvaa 168,148,236\nRa 212,11,24576\nMvaa 168,144,232\ns_mov_b32 s92, s66\nMvaa 108,136,232\nRa 216,10,26624\nMvaa 108,140,236\nD0 7,24,44,1024\n"
"Mvaa 104,132,236\nRa 220,11,26624\nMvaa 104,128,232\ns_and_b32 s93, s67, 0xffff\nMvaa 112,128,240\ns_mov_b32 s94, 0x188000\nMvaa 112,132,244\ns_mov_b32 s95, 0x20000\n"
"Mvaa 116,140,244\nv_lshrrev_b32 v252, 1, v0\nMvaa 116,136,240\nv_mul_u32_u24 v253, 14336, v252\nMvaa 176,144,240\nv_and_b32 v254, 1, v0\nMvaa 176,148,244\nD0 8,24,44,2048\n"
"Mvaa 180,156,244\nv_lshl_add_u32 v253, v254, 4, v253\nMvaa 180,152,240\nv_mov_b32 v254, 0x40000000\nv_cmp_gt_u32 vcc, 56, v0\nv_lshlrev_b32 v252, 3, v252\n"
"s_mul_i32 s2, s11, 401408\nv_cndmask_b32 v21, v254, v253, vcc\nv_cndmask_b32 v255, v254, v252, vcc\nMvaa 188,152,248\ns_lshl_b32 s3, s4, 5\nMvaa 188,156,252\ns_add_u32 s2, s2, s3\n"
"Mvaa 184,148,252\ns_or_b32 s86, s2, s5\nMvaa 184,144,248\ns_add_u32 s87, s86, 7168\nMvaa 124,136,248\nv_lshrrev_b32 v251, 4, v0\nMvaa 124,140,252\nD0 9,24,44,3072\n"
"Mvaa 120,132,252\nv_mul_u32_u24 v252, 3584, v251\nMvaa 120,128,248\nWL 0\nMaaa 32,192,224\nRa 128,10,32768\nMaaa 32,196,228\ns_add_u32 m0, s47, 16384\ns_add_u32 s44, s44, 0x40000\n"
"Maaa 36,204,228\nRa 132,11,32768\nMaaa 36,200,224\nv_and_b32 v251, 15, v0\nMaaa 96,208,224\nRa 136,10,34816\nMaaa 96,212,228\nD0 6,24,44,0\nMaaa 100,220,228\nRa 140,11,34816\n"
"Maaa 100,216,224\nv_lshl_add_u32 v252, v251, 4, v252\nv_cmp_gt_u32 vcc, 14, v251\ns_mul_i32 s2, s4, 14336\ns_mul_i32 s3, s11, 224\nv_cndmask_b32 v20, v254, v252, vcc\n"
"Maaa 108,216,232\nRa 144,10,40960\nMaaa 108,220,236\ns_add_u32 s2, s2, s3\nMaaa 104,212,236\nRa 148,11,40960\nMaaa 104,208,232\ns_or_b32 s88, s2, s5\nMaaa 44,200,232\n"
"Ra 152,10,43008\nMaaa 44,204,236\nD0 7,24,44,1024\nMaaa 40,196,236\nRa 156,11,43008\nMaaa 40,192,232\nMaaa 48,192,240\nMaaa 48,196,244\nMaaa 52,204,244\nMaaa 52,200,240\n"
"Maaa 112,208,240\nMaaa 112,212,244\nD0 8,24,44,2048\nMaaa 116,220,244\nMaaa 116,216,240\nMaaa 124,216,248\nMaaa 124,220,252\nMaaa 120,212,252\nMaaa 120,208,248\nMaaa 60,200,248\n"
"Maaa 60,204,252\nD0 9,24,44,3072\nMaaa 56,196,252\nMaaa 56,192,248\ns_waitcnt vmcnt(17) lgkmcnt(0)\ns_barrier\nMaaa 0,192,160\nTa 224,16,32768\nMaaa 0,196,164\nTa 226,16,34816\n"
"buffer_load_dwordx4 v[192:195], v21, s[36:39], s86 offen offset:0\nMaaa 4,204,164\ns_add_u32 m0, s47, -49152\ns_add_u32 s48, s48, 128\nMaaa 4,200,160\nTa 228,16,49152\n"
"s_lshl_b32 s6, s11, 12\ns_add_u32 s6, s6, 0xc044000\nMaaa 64,208,160\nTa 230,16,51200\nMaaa 64,212,164\nD0 2,20,48,0\nMaaa 68,220,164\nTa 232,17,32768\nMaaa 68,216,160\n"
"Ta 234,17,34816\nMaaa 76,216,168\nbuffer_load_dwordx4 v[32:35], v1, s[16:19], s6 offen offset:0 sc0 sc1\nMaaa 76,220,172\nTa 236,17,49152\nMaaa 72,212,172\nTa 238,17,51200\n"
"Maaa 72,208,168\nMaaa 12,200,168\nTa 240,18,32768\nMaaa 12,204,172\nTa 242,18,34816\nMaaa 8,196,172\nD0 3,20,48,1024\nMaaa 8,192,168\nTa 244,18,49152\nMaaa 16,192,176\n"
"Ta 246,18,51200\nMaaa 16,196,180\nbuffer_load_dwordx4 v[36:39], v1, s[16:19], s6 offen offset:1024 sc0 sc1\nMaaa 20,204,180\nTa 248,19,32768\nMaaa 20,200,176\nTa 250,19,34816\n"
"Maaa 80,208,176\nMaaa 80,212,180\nTa 252,19,49152\nMaaa 84,220,180\nTa 254,19,51200\nMaaa 84,216,176\nD0 4,20,48,2048\nMaaa 92,216,184\n"
"buffer_load_dwordx4 v[40:43], v1, s[16:19], s6 offen offset:2048 sc0 sc1\nMaaa 92,220,188\nMaaa 88,212,188\nMaaa 88,208,184\nMaaa 28,200,184\n"
"buffer_load_dwordx4 v[44:47], v1, s[16:19], s6 offen offset:3072 sc0 sc1\nMaaa 28,204,188\nD0 5,20,48,3072\nMaaa 24,196,188\nMaaa 24,192,184\nWL 0\nMvaa 96,128,224\n"
"Ta 160,12,32768\nMvaa 96,132,228\nTa 162,12,34816\nMvaa 100,140,228\ns_add_u32 m0, s47, -32768\ns_add_u32 s45, s45, 128\nMvaa 100,136,224\nTa 164,12,49152\nMvaa 160,144,224\n"
"Ta 166,12,51200\nMvaa 160,148,228\nD0 2,20,45,0\nMvaa 164,156,228\nTa 168,13,32768\nMvaa 164,152,224\nTa 170,13,34816\nMvaa 172,152,232\nMvaa 172,156,236\nTa 172,13,49152\n"
"Mvaa 168,148,236\nTa 174,13,51200\nMvaa 168,144,232\nMvaa 108,136,232\nTa 176,14,32768\nMvaa 108,140,236\nTa 178,14,34816\nMvaa 104,132,236\nD0 3,20,45,1024\nMvaa 104,128,232\n"
"Ta 180,14,49152\nMvaa 112,128,240\nTa 182,14,51200\nMvaa 112,132,244\nMvaa 116,140,244\nTa 184,15,32768\nMvaa 116,136,240\nTa 186,15,34816\nMvaa 176,144,240\nMvaa 176,148,244\n"
"Ta 188,15,49152\nMvaa 180,156,244\nTa 190,15,51200\nMvaa 180,152,240\nD0 4,20,45,2048\nMvaa 188,152,248\nMvaa 188,156,252\nMvaa 184,148,252\nMvaa 184,144,248\nMvaa 124,136,248\n"
"Mvaa 124,140,252\nD0 5,20,45,3072\nMvaa 120,132,252\nMvaa 120,128,248\ns_waitcnt vmcnt(21) lgkmcnt(0)\ns_barrier\nMvaa 64,128,160\nRa 192,10,49152\nMvaa 64,132,164\n"
"s_add_u32 m0, s47, 32768\ns_add_u32 s44, s44, 0x40000\nbuffer_load_dwordx4 v[208:211], v21, s[40:43], s86 offen offset:0\nMvaa 68,140,164\nRa 196,11,49152\nMvaa 68,136,160\n"
"Mvaa 128,144,160\nRa 200,10,51200\nMvaa 128,148,164\nD0 6,24,44,0\nMvaa 132,156,164\nRa 204,11,51200\nMvaa 132,152,160\nMvaa 140,152,168\nRa 208,10,57344\nMvaa 140,156,172\n"
"Mvaa 136,148,172\nRa 212,11,57344\nMvaa 136,144,168\nMvaa 76,136,168\nRa 216,10,59392\nMvaa 76,140,172\nD0 7,24,44,1024\nMvaa 72,132,172\nRa 220,11,59392\nMvaa 72,128,168\n"
"Mvaa 80,128,176\nMvaa 80,132,180\nMvaa 84,140,180\nMvaa 84,136,176\nMvaa 144,144,176\nMvaa 144,148,180\nD0 8,24,44,2048\nMvaa 148,156,180\nMvaa 148,152,176\nMvaa 156,152,184\n"
"Mvaa 156,156,188\nMvaa 152,148,188\nMvaa 152,144,184\nMvaa 92,136,184\nMvaa 92,140,188\nD0 9,24,44,3072\nMvaa 88,132,188\nMvaa 88,128,184\nWL 0\nMaaa 0,192,160\nRa 128,10,0\n"
"Maaa 0,196,164\ns_add_u32 m0, s47, 49152\ns_add_u32 s44, s44, 0x40000\nMaaa 4,204,164\nRa 132,11,0\nMaaa 4,200,160\nMaaa 64,208,160\nRa 136,10,2048\nMaaa 64,212,164\nD0 6,24,44,0\n"
"Maaa 68,220,164\nRa 140,11,2048\nMaaa 68,216,160\nMaaa 76,216,168\nRa 144,10,8192\nMaaa 76,220,172\nMaaa 72,212,172\nRa 148,11,8192\nMaaa 72,208,168\nMaaa 12,200,168\n"
"Ra 152,10,10240\nMaaa 12,204,172\nD0 7,24,44,1024\nMaaa 8,196,172\nRa 156,11,10240\nMaaa 8,192,168\nMaaa 16,192,176\nMaaa 16,196,180\nMaaa 20,204,180\nMaaa 20,200,176\n"
"Maaa 80,208,176\nMaaa 80,212,180\nD0 8,24,44,2048\nMaaa 84,220,180\nMaaa 84,216,176\nMaaa 92,216,184\nMaaa 92,220,188\nMaaa 88,212,188\nMaaa 88,208,184\nMaaa 28,200,184\n"
"Maaa 28,204,188\nD0 9,24,44,3072\nMaaa 24,196,188\nMaaa 24,192,184\ns_waitcnt vmcnt(14) lgkmcnt(0)\ns_barrier\nMaaa 32,192,224\nTa 160,12,0\nv_xor_b32 v48, s15, v33\n"
"Maaa 32,196,228\nTa 162,12,2048\nbuffer_load_dwordx4 v[196:199], v21, s[36:39], s86 offen offset:3584\nMaaa 36,204,228\ns_add_u32 m0, s47, -16384\ns_add_u32 s48, s48, 128\n"
"Maaa 36,200,224\nTa 164,12,16384\nMaaa 96,208,224\nTa 166,12,18432\nMaaa 96,212,228\nD0 2,20,48,0\nMaaa 100,220,228\nTa 168,13,0\nMaaa 100,216,224\nTa 170,13,2048\n"
"Maaa 108,216,232\nv_xor_b32 v49, s15, v35\nMaaa 108,220,236\nTa 172,13,16384\nMaaa 104,212,236\nTa 174,13,18432\nMaaa 104,208,232\nv_xor_b32 v50, s15, v37\nMaaa 44,200,232\n"
"Ta 176,14,0\nMaaa 44,204,236\nTa 178,14,2048\nMaaa 40,196,236\nD0 3,20,48,1024\nMaaa 40,192,232\nTa 180,14,16384\nMaaa 48,192,240\nTa 182,14,18432\nv_xor_b32 v51, s15, v39\n"
"Maaa 48,196,244\nv_xor_b32 v52, s15, v41\nMaaa 52,204,244\nTa 184,15,0\nMaaa 52,200,240\nTa 186,15,2048\nMaaa 112,208,240\nv_xor_b32 v53, s15, v43\nMaaa 112,212,244\n"
"Ta 188,15,16384\nMaaa 116,220,244\nTa 190,15,18432\nMaaa 116,216,240\nD0 4,20,48,2048\nMaaa 124,216,248\nv_xor_b32 v54, s15, v45\nMaaa 124,220,252\nv_xor_b32 v55, s15, v47\n"
"Maaa 120,212,252\nv_or3_b32 v48, v48, v49, v50\nMaaa 120,208,248\nv_or3_b32 v51, v51, v52, v53\nMaaa 60,200,248\nv_or3_b32 v48, v48, v54, v55\nMaaa 60,204,252\nD0 5,20,48,3072\n"
"Maaa 56,196,252\nv_or_b32 v48, v48, v51\nMaaa 56,192,248\nWL 0\nMvaa 64,128,160\nTa 224,16,0\nv_cmp_ne_u32 vcc, 0, v48\nMvaa 64,132,164\nTa 226,16,2048\nMvaa 68,140,164\n"
"s_add_u32 m0, s47, -65536\ns_add_u32 s45, s45, 128\nMvaa 68,136,160\nTa 228,16,16384\nMvaa 128,144,160\nTa 230,16,18432\nMvaa 128,148,164\nD0 2,20,45,0\nMvaa 132,156,164\n"
"Ta 232,17,0\nMvaa 132,152,160\nTa 234,17,2048\nMvaa 140,152,168\ns_cbranch_vccnz .S144\n.S145:\nMvaa 140,156,172\nTa 236,17,16384\nMvaa 136,148,172\nTa 238,17,18432\n"
"Mvaa 136,144,168\nv_max_u32 v48, v32, v34\nMvaa 76,136,168\nTa 240,18,0\nMvaa 76,140,172\nTa 242,18,2048\nMvaa 72,132,172\nD0 3,20,45,1024\n.W556:\ns_cbranch_execz .W560\n"
"Mvaa 72,128,168\nTa 244,18,16384\nMvaa 80,128,176\nTa 246,18,18432\nv_min_u32 v49, v32, v34\nMvaa 80,132,180\nv_med3_u32 v49, v48, v49, v36\nMvaa 84,140,180\nTa 248,19,0\n"
"Mvaa 84,136,176\nTa 250,19,2048\nMvaa 144,144,176\nv_max_u32 v48, v48, v36\nMvaa 144,148,180\nTa 252,19,16384\nMvaa 148,156,180\nTa 254,19,18432\nMvaa 148,152,176\n"
"D0 4,20,45,2048\nMvaa 156,152,184\nv_med3_u32 v49, v48, v49, v38\nMvaa 156,156,188\nv_max_u32 v48, v48, v38\nMvaa 152,148,188\nv_med3_u32 v49, v48, v49, v40\nMvaa 152,144,184\n"
"v_max_u32 v48, v48, v40\nMvaa 92,136,184\nv_med3_u32 v49, v48, v49, v42\nMvaa 92,140,188\nD0 5,20,45,3072\nMvaa 88,132,188\nv_max_u32 v48, v48, v42\nMvaa 88,128,184\n"
"s_waitcnt vmcnt(18) lgkmcnt(0)\ns_barrier\nMvaa 96,128,224\nRa 192,10,16384\nv_med3_u32 v49, v48, v49, v44\nMvaa 96,132,228\ns_add_u32 m0, s47, 0\ns_add_u32 s44, s44, 0x40000\n"
"buffer_load_dwordx4 v[212:215], v21, s[40:43], s86 offen offset:3584\nMvaa 100,140,228\nRa 196,11,16384\nMvaa 100,136,224\nv_max_u32 v48, v48, v44\nMvaa 160,144,224\n"
"Ra 200,10,18432\nMvaa 160,148,228\nD0 6,24,44,0\nMvaa 164,156,228\nRa 204,11,18432\nMvaa 164,152,224\nv_med3_u32 v49, v48, v49, v46\nMvaa 172,152,232\nRa 208,10,24576\n"
"Mvaa 172,156,236\nv_max_u32 v48, v48, v46\nMvaa 168,148,236\nRa 212,11,24576\nMvaa 168,144,232\nv_mov_b32 v50, v48\nMvaa 108,136,232\nRa 216,10,26624\nMvaa 108,140,236\n"
"D0 7,24,44,1024\nMvaa 104,132,236\nRa 220,11,26624\nMvaa 104,128,232\nv_max_u32_dpp v50, v50, v50 row_ror:8 row_mask:0xf bank_mask:0xf\nMvaa 112,128,240\nMvaa 112,132,244\n"
"v_max_u32_dpp v50, v50, v50 row_ror:4 row_mask:0xf bank_mask:0xf\nMvaa 116,140,244\nMvaa 116,136,240\nv_max_u32_dpp v50, v50, v50 row_ror:2 row_mask:0xf bank_mask:0xf\n"
"Mvaa 176,144,240\nMvaa 176,148,244\nD0 8,24,44,2048\nMvaa 180,156,244\nv_max_u32_dpp v50, v50, v50 row_ror:1 row_mask:0xf bank_mask:0xf\nMvaa 180,152,240\nv_mov_b32 v52, v50\n"
"Mvaa 188,152,248\nMvaa 188,156,252\nv_permlane16_swap_b32 v50, v52\nMvaa 184,148,252\nv_max_u32 v50, v50, v52\nMvaa 184,144,248\nv_mov_b32 v52, v50\nMvaa 124,136,248\n"
"Mvaa 124,140,252\nD0 9,24,44,3072\nMvaa 120,132,252\nv_permlane32_swap_b32 v50, v52\nMvaa 120,128,248\nWL 0\nMaaa 32,192,224\nRa 128,10,32768\nv_max_u32 v50, v50, v52\n"
"Maaa 32,196,228\ns_add_u32 m0, s47, 16384\ns_add_u32 s44, s44, 0x40000\nMaaa 36,204,228\nRa 132,11,32768\nv_cmp_eq_u32 vcc, v48, v50\ns_lshl_b32 s2, s11, 3\n"
"s_add_u32 s2, s2, 131072\nv_cndmask_b32 v51, v48, v49, vcc\nMaaa 36,200,224\nMaaa 96,208,224\nRa 136,10,34816\nMaaa 96,212,228\nD0 6,24,44,0\nMaaa 100,220,228\nRa 140,11,34816\n"
"Maaa 100,216,224\nv_max_u32_dpp v51, v51, v51 row_ror:8 row_mask:0xf bank_mask:0xf\nMaaa 108,216,232\nRa 144,10,40960\nMaaa 108,220,236\n"
"v_max_u32_dpp v51, v51, v51 row_ror:4 row_mask:0xf bank_mask:0xf\nMaaa 104,212,236\nRa 148,11,40960\nMaaa 104,208,232\n"
"v_max_u32_dpp v51, v51, v51 row_ror:2 row_mask:0xf bank_mask:0xf\nMaaa 44,200,232\nRa 152,10,43008\nMaaa 44,204,236\nD0 7,24,44,1024\nMaaa 40,196,236\nRa 156,11,43008\n"
"Maaa 40,192,232\nv_max_u32_dpp v51, v51, v51 row_ror:1 row_mask:0xf bank_mask:0xf\nMaaa 48,192,240\nv_mov_b32 v52, v51\nMaaa 48,196,244\nMaaa 52,204,244\n"
"v_permlane16_swap_b32 v51, v52\nMaaa 52,200,240\nv_max_u32 v51, v51, v52\nMaaa 112,208,240\nv_mov_b32 v52, v51\nMaaa 112,212,244\nD0 8,24,44,2048\nMaaa 116,220,244\n"
"v_permlane32_swap_b32 v51, v52\nMaaa 116,216,240\nv_max_u32 v51, v51, v52\nMaaa 124,216,248\nv_mov_b32 v52, s2\nMaaa 124,220,252\nds_write_b64 v52, v[50:51]\nMaaa 120,212,252\n"
"v_mov_b32 v52, 131072\nMaaa 120,208,248\nMaaa 60,200,248\nMaaa 60,204,252\nD0 9,24,44,3072\nMaaa 56,196,252\nMaaa 56,192,248\ns_waitcnt vmcnt(18) lgkmcnt(0)\ns_barrier\n"
"Maaa 0,192,160\nTa 224,16,32768\nMaaa 0,196,164\nTa 226,16,34816\nbuffer_load_dwordx4 v[200:203], v21, s[36:39], s87 offen offset:0\nMaaa 4,204,164\ns_add_u32 m0, s47, -49152\n"
"s_add_u32 s48, s48, 128\nMaaa 4,200,160\nTa 228,16,49152\nMaaa 64,208,160\nTa 230,16,51200\nMaaa 64,212,164\nD0 2,20,48,0\nMaaa 68,220,164\nTa 232,17,32768\nMaaa 68,216,160\n"
"Ta 234,17,34816\nMaaa 76,216,168\nRv 32,52,0\nMaaa 76,220,172\nTa 236,17,49152\nMaaa 72,212,172\nTa 238,17,51200\nMaaa 72,208,168\nRv 36,52,16\nMaaa 12,200,168\nTa 240,18,32768\n"
"Maaa 12,204,172\nTa 242,18,34816\nMaaa 8,196,172\nD0 3,20,48,1024\nMaaa 8,192,168\nTa 244,18,49152\nMaaa 16,192,176\nTa 246,18,51200\nMaaa 16,196,180\nMaaa 20,204,180\n"
"Ta 248,19,32768\nMaaa 20,200,176\nTa 250,19,34816\nMaaa 80,208,176\nMaaa 80,212,180\nTa 252,19,49152\nMaaa 84,220,180\nTa 254,19,51200\nMaaa 84,216,176\nD0 4,20,48,2048\n"
"Maaa 92,216,184\nWL 11\nMaaa 92,220,188\nv_max_u32 v48, v32, v33\nMaaa 88,212,188\nv_min_u32 v49, v32, v33\nMaaa 88,208,184\nv_med3_u32 v49, v48, v49, v34\nMaaa 28,200,184\n"
"v_max_u32 v48, v48, v34\nMaaa 28,204,188\nD0 5,20,48,3072\nMaaa 24,196,188\nv_med3_u32 v49, v48, v49, v35\nMaaa 24,192,184\nWL 0\nMvaa 96,128,224\nTa 160,12,32768\n"
"Mvaa 96,132,228\nTa 162,12,34816\nMvaa 100,140,228\ns_add_u32 m0, s47, -32768\ns_add_u32 s45, s45, 128\nMvaa 100,136,224\nTa 164,12,49152\nMvaa 160,144,224\nTa 166,12,51200\n"
"Mvaa 160,148,228\nD0 2,20,45,0\nMvaa 164,156,228\nTa 168,13,32768\nMvaa 164,152,224\nTa 170,13,34816\nMvaa 172,152,232\nv_max_u32 v48, v48, v35\nMvaa 172,156,236\nTa 172,13,49152\n"
"Mvaa 168,148,236\nTa 174,13,51200\nMvaa 168,144,232\nv_med3_u32 v49, v48, v49, v36\nMvaa 108,136,232\nTa 176,14,32768\nMvaa 108,140,236\nTa 178,14,34816\nMvaa 104,132,236\n"
"D0 3,20,45,1024\nMvaa 104,128,232\nTa 180,14,49152\nMvaa 112,128,240\nTa 182,14,51200\nMvaa 112,132,244\nv_max_u32 v48, v48, v36\nMvaa 116,140,244\nTa 184,15,32768\n"
"Mvaa 116,136,240\nTa 186,15,34816\nMvaa 176,144,240\nv_med3_u32 v49, v48, v49, v37\nMvaa 176,148,244\nTa 188,15,49152\nMvaa 180,156,244\nTa 190,15,51200\nMvaa 180,152,240\n"
"D0 4,20,45,2048\nMvaa 188,152,248\nv_max_u32 v48, v48, v37\nMvaa 188,156,252\nv_med3_u32 v49, v48, v49, v38\nMvaa 184,148,252\nv_max_u32 v48, v48, v38\nMvaa 184,144,248\n"
"v_med3_u32 v49, v48, v49, v39\nMvaa 124,136,248\nv_max_u32 v48, v48, v39\nMvaa 124,140,252\nD0 5,20,45,3072\nMvaa 120,132,252\nv_readfirstlane_b32 s2, v48\nMvaa 120,128,248\n"
"s_waitcnt vmcnt(18) lgkmcnt(0)\ns_barrier\nMvaa 64,128,160\nRa 192,10,49152\nMvaa 64,132,164\ns_add_u32 m0, s47, 32768\ns_add_u32 s44, s44, 0x40000\n"
"buffer_load_dwordx4 v[216:219], v21, s[40:43], s87 offen offset:0\nMvaa 68,140,164\nRa 196,11,49152\nMvaa 68,136,160\nv_readfirstlane_b32 s3, v49\nMvaa 128,144,160\n"
"Ra 200,10,51200\nMvaa 128,148,164\nD0 6,24,44,0\nMvaa 132,156,164\nRa 204,11,51200\nMvaa 132,152,160\ns_andn2_b32 s2, 0x7ff, s2\nMvaa 140,152,168\nRa 208,10,57344\n"
"Mvaa 140,156,172\ns_andn2_b32 s3, 0x7ff, s3\nMvaa 136,148,172\nRa 212,11,57344\nMvaa 136,144,168\ns_lshl_b32 s71, s3, 16\nMvaa 76,136,168\nRa 216,10,59392\nMvaa 76,140,172\n"
"D0 7,24,44,1024\nMvaa 72,132,172\nRa 220,11,59392\nMvaa 72,128,168\ns_or_b32 s71, s71, s2\nMvaa 80,128,176\ns_mul_i32 s2, s2, 896\nMvaa 80,132,180\ns_mul_i32 s3, s3, 896\n"
"Mvaa 84,140,180\ns_mul_i32 s6, s11, 224\nMvaa 84,136,176\ns_add_u32 s2, s2, s6\nMvaa 144,144,176\ns_add_u32 s3, s3, s6\nMvaa 144,148,180\nD0 8,24,44,2048\nMvaa 148,156,180\n"
"buffer_load_dwordx2 v[240:241], v255, s[28:31], s2 offen\nMvaa 148,152,176\nMvaa 156,152,184\nMvaa 156,156,188\nMvaa 152,148,188\n"
"buffer_load_dwordx2 v[242:243], v255, s[28:31], s3 offen\ns_lshl2_add_u32 s2, s10, s11\ns_cmp_eq_u32 s2, 512\ns_cselect_b32 s2, 0, 0x40000000\ns_add_u32 s2, s2, 0xc048000\n"
"Mvaa 152,144,184\nMvaa 92,136,184\nMvaa 92,140,188\nD0 9,24,44,3072\nMvaa 88,132,188\nbuffer_store_dword v25, off, s[16:19], s2 offset:0 sc0 sc1\nMvaa 88,128,184\nWL 0\n"
"Maaa 0,192,160\nRa 128,10,0\nMaaa 0,196,164\ns_add_u32 m0, s47, 49152\ns_add_u32 s44, s44, 0x40000\nMaaa 4,204,164\nRa 132,11,0\nMaaa 4,200,160\nMaaa 64,208,160\nRa 136,10,2048\n"
"Maaa 64,212,164\nD0 6,24,44,0\nMaaa 68,220,164\nRa 140,11,2048\nMaaa 68,216,160\nMaaa 76,216,168\nRa 144,10,8192\nMaaa 76,220,172\nMaaa 72,212,172\nRa 148,11,8192\n"
"Maaa 72,208,168\nMaaa 12,200,168\nRa 152,10,10240\nMaaa 12,204,172\nD0 7,24,44,1024\nMaaa 8,196,172\nRa 156,11,10240\nMaaa 8,192,168\nMaaa 16,192,176\nMaaa 16,196,180\n"
"Maaa 20,204,180\nMaaa 20,200,176\nMaaa 80,208,176\nMaaa 80,212,180\nD0 8,24,44,2048\nMaaa 84,220,180\nMaaa 84,216,176\nMaaa 92,216,184\nMaaa 92,220,188\nMaaa 88,212,188\n"
"Maaa 88,208,184\nMaaa 28,200,184\nMaaa 28,204,188\nD0 9,24,44,3072\nMaaa 24,196,188\nMaaa 24,192,184\ns_waitcnt vmcnt(21) lgkmcnt(0)\ns_barrier\nMaaa 32,192,224\nTa 160,12,0\n"
"Maaa 32,196,228\nTa 162,12,2048\nbuffer_load_dwordx4 v[204:207], v21, s[36:39], s87 offen offset:3584\nMaaa 36,204,228\ns_add_u32 m0, s47, -16384\ns_add_u32 s48, s48, 128\n"
"s_lshl_b32 s5, s73, 9\ns_add_u32 s5, s5, 0xc049400\nv_lshrrev_b32 v27, 1, v1\nbuffer_load_dwordx2 v[22:23], v27, s[16:19], s5 offen sc0 sc1\nMaaa 36,200,224\nTa 164,12,16384\n"
"Maaa 96,208,224\nTa 166,12,18432\nMaaa 96,212,228\nD0 2,20,48,0\nMaaa 100,220,228\nTa 168,13,0\nMaaa 100,216,224\nTa 170,13,2048\nMaaa 108,216,232\nMaaa 108,220,236\n"
"Ta 172,13,16384\nMaaa 104,212,236\nTa 174,13,18432\nMaaa 104,208,232\nMaaa 44,200,232\nTa 176,14,0\nMaaa 44,204,236\nTa 178,14,2048\nMaaa 40,196,236\nD0 3,20,48,1024\n"
"Maaa 40,192,232\nTa 180,14,16384\nMaaa 48,192,240\nTa 182,14,18432\nMaaa 48,196,244\nMaaa 52,204,244\nTa 184,15,0\nMaaa 52,200,240\nTa 186,15,2048\nMaaa 112,208,240\n"
"Maaa 112,212,244\nTa 188,15,16384\nMaaa 116,220,244\nTa 190,15,18432\nMaaa 116,216,240\nD0 4,20,48,2048\nMaaa 124,216,248\nMaaa 124,220,252\nMaaa 120,212,252\nMaaa 120,208,248\n"
"Maaa 60,200,248\nMaaa 60,204,252\nD0 5,20,48,3072\nMaaa 56,196,252\nMaaa 56,192,248\nWL 0\nMvaa 64,128,160\nTa 224,16,0\nMvaa 64,132,164\nTa 226,16,2048\nMvaa 68,140,164\n"
"s_add_u32 m0, s47, -65536\ns_add_u32 s45, s45, 128\nMvaa 68,136,160\nTa 228,16,16384\nMvaa 128,144,160\nTa 230,16,18432\nMvaa 128,148,164\nD0 2,20,45,0\nMvaa 132,156,164\n"
"Ta 232,17,0\nMvaa 132,152,160\nTa 234,17,2048\nMvaa 140,152,168\nMvaa 140,156,172\nTa 236,17,16384\nMvaa 136,148,172\nTa 238,17,18432\nMvaa 136,144,168\nMvaa 76,136,168\n"
"Ta 240,18,0\nMvaa 76,140,172\nTa 242,18,2048\nMvaa 72,132,172\nD0 3,20,45,1024\nMvaa 72,128,168\nTa 244,18,16384\nMvaa 80,128,176\nTa 246,18,18432\nMvaa 80,132,180\n"
"Mvaa 84,140,180\nTa 248,19,0\nMvaa 84,136,176\nTa 250,19,2048\nMvaa 144,144,176\nMvaa 144,148,180\nTa 252,19,16384\nMvaa 148,156,180\nTa 254,19,18432\nMvaa 148,152,176\n"
"D0 4,20,45,2048\nMvaa 156,152,184\nMvaa 156,156,188\nMvaa 152,148,188\nMvaa 152,144,184\nMvaa 92,136,184\nMvaa 92,140,188\nD0 5,20,45,3072\nMvaa 88,132,188\nMvaa 88,128,184\n"
"s_waitcnt vmcnt(22) lgkmcnt(0)\ns_barrier\nMvaa 96,128,224\nRa 192,10,16384\nWV 16\nMvaa 96,132,228\ns_add_u32 m0, s47, 0\ns_add_u32 s44, s44, 0x40000\n"
"buffer_load_dwordx4 v[220:223], v21, s[40:43], s87 offen offset:3584\nMvaa 100,140,228\nRa 196,11,16384\nv_lshlrev_b32 v244, 16, v240\nMvaa 100,136,224\n"
"v_and_b32 v245, 0xffff0000, v240\nv_lshlrev_b32 v246, 16, v241\nMvaa 160,144,224\nRa 200,10,18432\nv_and_b32 v247, 0xffff0000, v241\nMvaa 160,148,228\nD0 6,24,44,0\n"
"v_lshlrev_b32 v248, 16, v242\nMvaa 164,156,228\n.W557:\ns_cbranch_execz .W561\nRa 204,11,18432\nv_and_b32 v249, 0xffff0000, v242\nMvaa 164,152,224\nv_lshlrev_b32 v250, 16, v243\n"
"v_and_b32 v251, 0xffff0000, v243\nMvaa 172,152,232\nRa 208,10,24576\nv_lshlrev_b32 v252, 16, v192\nMvaa 172,156,236\nv_and_b32 v253, 0xffff0000, v192\nMU 32,244,252\n"
"Mvaa 168,148,236\nRa 212,11,24576\nMU 33,244,253\nMvaa 168,144,232\nMU 40,248,252\nMU 41,248,253\nMvaa 108,136,232\nRa 216,10,26624\nv_lshlrev_b32 v252, 16, v193\n"
"Mvaa 108,140,236\nD0 7,24,44,1024\nv_and_b32 v253, 0xffff0000, v193\nMvaa 104,132,236\nRa 220,11,26624\nMU 34,244,252\nMvaa 104,128,232\nMU 35,244,253\nMU 42,248,252\n"
"Mvaa 112,128,240\nMU 43,248,253\nv_lshlrev_b32 v252, 16, v194\nMvaa 112,132,244\nv_and_b32 v253, 0xffff0000, v194\nMvaa 116,140,244\nMU 36,244,252\nMU 37,244,253\n"
"Mvaa 116,136,240\nMU 44,248,252\nMU 45,248,253\nMvaa 176,144,240\nv_lshlrev_b32 v252, 16, v195\nv_and_b32 v253, 0xffff0000, v195\nMvaa 176,148,244\nD0 8,24,44,2048\nMU 38,244,252\n"
"Mvaa 180,156,244\nMU 39,244,253\nMU 46,248,252\nMvaa 180,152,240\nMU 47,248,253\nv_lshlrev_b32 v252, 16, v208\nMvaa 188,152,248\nv_and_b32 v253, 0xffff0000, v208\nMU 48,244,252\n"
"Mvaa 188,156,252\nMU 49,244,253\nMU 56,248,252\nMvaa 184,148,252\nMU 57,248,253\nv_lshlrev_b32 v252, 16, v209\nMvaa 184,144,248\nv_and_b32 v253, 0xffff0000, v209\nMU 50,244,252\n"
"Mvaa 124,136,248\nMU 51,244,253\nMU 58,248,252\nMvaa 124,140,252\nD0 9,24,44,3072\nMU 59,248,253\nMvaa 120,132,252\nv_lshlrev_b32 v252, 16, v210\nv_and_b32 v253, 0xffff0000, v210\n"
"Mvaa 120,128,248\nWL 0\nMaaa 32,192,224\nRa 128,10,32768\nMU 52,244,252\nMaaa 32,196,228\ns_add_u32 m0, s47, 16384\ns_add_u32 s44, s44, 0x40000\nMaaa 36,204,228\nRa 132,11,32768\n"
"MU 53,244,253\nMaaa 36,200,224\nMU 60,248,252\nMU 61,248,253\nMaaa 96,208,224\nRa 136,10,34816\nv_lshlrev_b32 v252, 16, v211\nMaaa 96,212,228\nD0 6,24,44,0\n"
"v_and_b32 v253, 0xffff0000, v211\nMaaa 100,220,228\nRa 140,11,34816\nMU 54,244,252\nMaaa 100,216,224\nMU 55,244,253\nMU 62,248,252\nMaaa 108,216,232\nRa 144,10,40960\n"
"MU 63,248,253\nMaaa 108,220,236\nv_lshlrev_b32 v252, 16, v196\nv_and_b32 v253, 0xffff0000, v196\nMaaa 104,212,236\nRa 148,11,40960\nFM 32,245,252\nMaaa 104,208,232\nFM 33,245,253\n"
"FM 40,249,252\nMaaa 44,200,232\nRa 152,10,43008\nFM 41,249,253\nMaaa 44,204,236\nD0 7,24,44,1024\nv_lshlrev_b32 v252, 16, v197\nMaaa 40,196,236\nRa 156,11,43008\n"
"v_and_b32 v253, 0xffff0000, v197\nMaaa 40,192,232\nFM 34,245,252\nFM 35,245,253\nMaaa 48,192,240\nFM 42,249,252\nFM 43,249,253\nMaaa 48,196,244\nv_lshlrev_b32 v252, 16, v198\n"
"Maaa 52,204,244\nv_and_b32 v253, 0xffff0000, v198\nFM 36,245,252\nMaaa 52,200,240\nFM 37,245,253\nFM 44,249,252\nMaaa 112,208,240\nFM 45,249,253\nv_lshlrev_b32 v252, 16, v199\n"
"Maaa 112,212,244\nD0 8,24,44,2048\nv_and_b32 v253, 0xffff0000, v199\nMaaa 116,220,244\nFM 38,245,252\nFM 39,245,253\nMaaa 116,216,240\nFM 46,249,252\nFM 47,249,253\n"
"Maaa 124,216,248\nv_lshlrev_b32 v252, 16, v212\nv_and_b32 v253, 0xffff0000, v212\nMaaa 124,220,252\nFM 48,245,252\nFM 49,245,253\nMaaa 120,212,252\nFM 56,249,252\nFM 57,249,253\n"
"Maaa 120,208,248\nv_lshlrev_b32 v252, 16, v213\nv_and_b32 v253, 0xffff0000, v213\nMaaa 60,200,248\nFM 50,245,252\nFM 51,245,253\nMaaa 60,204,252\nD0 9,24,44,3072\nFM 58,249,252\n"
"Maaa 56,196,252\nFM 59,249,253\nv_lshlrev_b32 v252, 16, v214\nMaaa 56,192,248\ns_waitcnt vmcnt(19) lgkmcnt(0)\ns_barrier\nMaaa 0,192,160\nTa 224,16,32768\n"
"v_and_b32 v253, 0xffff0000, v214\nMaaa 0,196,164\nTa 226,16,34816\nbuffer_load_dwordx4 v[224:227], v20, s[92:95], s88 offen offset:0\nMaaa 4,204,164\ns_add_u32 m0, s47, -49152\n"
"s_add_u32 s48, s48, 128\nMaaa 4,200,160\nTa 228,16,49152\nFM 52,245,252\nMaaa 64,208,160\nTa 230,16,51200\nFM 53,245,253\nMaaa 64,212,164\nD0 2,20,48,0\nFM 60,249,252\n"
"Maaa 68,220,164\nTa 232,17,32768\nFM 61,249,253\nMaaa 68,216,160\nTa 234,17,34816\nv_lshlrev_b32 v252, 16, v215\nMaaa 76,216,168\nv_and_b32 v253, 0xffff0000, v215\nFM 54,245,252\n"
"Maaa 76,220,172\nTa 236,17,49152\nFM 55,245,253\nMaaa 72,212,172\nTa 238,17,51200\nFM 62,249,252\nMaaa 72,208,168\nFM 63,249,253\nv_lshlrev_b32 v252, 16, v200\nMaaa 12,200,168\n"
"Ta 240,18,32768\nv_and_b32 v253, 0xffff0000, v200\nMaaa 12,204,172\nTa 242,18,34816\nFM 32,246,252\nMaaa 8,196,172\nD0 3,20,48,1024\nFM 33,246,253\nMaaa 8,192,168\n"
"Ta 244,18,49152\nFM 40,250,252\nMaaa 16,192,176\nTa 246,18,51200\nFM 41,250,253\nMaaa 16,196,180\nv_lshlrev_b32 v252, 16, v201\nMaaa 20,204,180\nTa 248,19,32768\n"
"v_and_b32 v253, 0xffff0000, v201\nMaaa 20,200,176\nTa 250,19,34816\nFM 34,246,252\nMaaa 80,208,176\nFM 35,246,253\nFM 42,250,252\nMaaa 80,212,180\nTa 252,19,49152\nFM 43,250,253\n"
"Maaa 84,220,180\nTa 254,19,51200\nv_lshlrev_b32 v252, 16, v202\nMaaa 84,216,176\nD0 4,20,48,2048\nv_and_b32 v253, 0xffff0000, v202\nMaaa 92,216,184\nFM 36,246,252\nFM 37,246,253\n"
"Maaa 92,220,188\nFM 44,250,252\nFM 45,250,253\nMaaa 88,212,188\nv_lshlrev_b32 v252, 16, v203\nv_and_b32 v253, 0xffff0000, v203\nMaaa 88,208,184\nFM 38,246,252\nFM 39,246,253\n"
"Maaa 28,200,184\nFM 46,250,252\nFM 47,250,253\nMaaa 28,204,188\nD0 5,20,48,3072\nv_lshlrev_b32 v252, 16, v216\nMaaa 24,196,188\nv_and_b32 v253, 0xffff0000, v216\nFM 48,246,252\n"
"Maaa 24,192,184\nWL 0\nMvaa 96,128,224\nTa 160,12,32768\nFM 49,246,253\nMvaa 96,132,228\nTa 162,12,34816\nbuffer_load_dwordx4 v[228:231], v20, s[92:95], s88 offen offset:896\n"
"Mvaa 100,140,228\ns_add_u32 m0, s47, -32768\ns_add_u32 s45, s45, 128\nMvaa 100,136,224\nTa 164,12,49152\nFM 56,250,252\nMvaa 160,144,224\nTa 166,12,51200\nFM 57,250,253\n"
"Mvaa 160,148,228\nD0 2,20,45,0\nv_lshlrev_b32 v252, 16, v217\nMvaa 164,156,228\nTa 168,13,32768\nv_and_b32 v253, 0xffff0000, v217\nMvaa 164,152,224\nTa 170,13,34816\n"
"FM 50,246,252\nMvaa 172,152,232\nFM 51,246,253\nFM 58,250,252\nMvaa 172,156,236\nTa 172,13,49152\nFM 59,250,253\nMvaa 168,148,236\nTa 174,13,51200\nv_lshlrev_b32 v252, 16, v218\n"
"Mvaa 168,144,232\nv_and_b32 v253, 0xffff0000, v218\nFM 52,246,252\nMvaa 108,136,232\nTa 176,14,32768\nFM 53,246,253\nMvaa 108,140,236\nTa 178,14,34816\nFM 60,250,252\n"
"Mvaa 104,132,236\nD0 3,20,45,1024\nFM 61,250,253\nMvaa 104,128,232\nTa 180,14,49152\nv_lshlrev_b32 v252, 16, v219\nMvaa 112,128,240\nTa 182,14,51200\n"
"v_and_b32 v253, 0xffff0000, v219\nMvaa 112,132,244\nFM 54,246,252\nWV 25\nv_cmp_ne_u32 vcc, s15, v23\ns_nop 0\ns_cbranch_vccnz .S147\n.S148:\nMvaa 116,140,244\nTa 184,15,32768\n"
"v_add_u32_dpp v22, v22, v22 row_ror:8 row_mask:0xf bank_mask:0xf\nMvaa 116,136,240\nTa 186,15,34816\nMvaa 176,144,240\n"
"v_add_u32_dpp v22, v22, v22 row_ror:4 row_mask:0xf bank_mask:0xf\nMvaa 176,148,244\nTa 188,15,49152\nMvaa 180,156,244\nTa 190,15,51200\n"
"v_add_u32_dpp v22, v22, v22 row_ror:2 row_mask:0xf bank_mask:0xf\nMvaa 180,152,240\nD0 4,20,45,2048\nMvaa 188,152,248\n"
"v_add_u32_dpp v22, v22, v22 row_ror:1 row_mask:0xf bank_mask:0xf\nMvaa 188,156,252\nMvaa 184,148,252\nv_mov_b32 v23, v22\nMvaa 184,144,248\nMvaa 124,136,248\n"
"v_permlane16_swap_b32 v22, v23\nv_add_u32 v22, v22, v23\nMvaa 124,140,252\nD0 5,20,45,3072\nv_mov_b32 v23, v22\nMvaa 120,132,252\nMvaa 120,128,248\ns_waitcnt vmcnt(19) lgkmcnt(0)\n"
"s_barrier\nMvaa 64,128,160\nRa 192,10,49152\nv_permlane32_swap_b32 v22, v23\nMvaa 64,132,164\ns_add_u32 m0, s47, 32768\ns_add_u32 s44, s44, 0x40000\n"
"buffer_load_dwordx4 v[232:235], v20, s[92:95], s88 offen offset:1792\nMvaa 68,140,164\nRa 196,11,49152\nv_add_u32 v22, v22, v23\nMvaa 68,136,160\nMvaa 128,144,160\n"
"Ra 200,10,51200\nv_readfirstlane_b32 s4, v22\nMvaa 128,148,164\nD0 6,24,44,0\nMvaa 132,156,164\nRa 204,11,51200\nMvaa 132,152,160\ns_cmp_gt_i32 s4, 0\ns_cselect_b32 s4, 1, 0\n"
"Mvaa 140,152,168\nRa 208,10,57344\ns_xor_b32 s4, s4, s12\nMvaa 140,156,172\ns_and_b32 s4, s4, 1\ns_lshl_b32 s4, s4, 13\nMvaa 136,148,172\nRa 212,11,57344\n"
"s_mul_i32 s72, s73, 25165824\nMvaa 136,144,168\ns_add_u32 s72, s72, s4\ns_and_b32 s4, s12, 1\nMvaa 76,136,168\nRa 216,10,59392\ns_lshl_b32 s4, s4, 21\nMvaa 76,140,172\n"
"D0 7,24,44,1024\ns_add_u32 s72, s72, s4\nMvaa 72,132,172\nRa 220,11,59392\ns_lshl3_add_u32 s2, s13, s11\nMvaa 72,128,168\ns_lshl_b32 s2, s2, 11\ns_lshl_b32 s3, s14, 22\n"
"Mvaa 80,128,176\ns_add_u32 s2, s2, s3\ns_add_u32 s2, s2, s72\nMvaa 80,132,180\ns_add_u32 s49, s2, 0xbe0000\nMvaa 84,140,180\nFM 55,246,253\nFM 62,250,252\nMvaa 84,136,176\n"
"FM 63,250,253\nv_lshlrev_b32 v252, 16, v204\nMvaa 144,144,176\nv_and_b32 v253, 0xffff0000, v204\nFM 32,247,252\nMvaa 144,148,180\nD0 8,24,44,2048\nFM 33,247,253\nMvaa 148,156,180\n"
"FM 40,251,252\nFM 41,251,253\nMvaa 148,152,176\nv_lshlrev_b32 v252, 16, v205\nv_and_b32 v253, 0xffff0000, v205\nMvaa 156,152,184\nFM 34,247,252\nFM 35,247,253\nMvaa 156,156,188\n"
"FM 42,251,252\nFM 43,251,253\nMvaa 152,148,188\nv_lshlrev_b32 v252, 16, v206\nv_and_b32 v253, 0xffff0000, v206\nMvaa 152,144,184\nFM 36,247,252\nFM 37,247,253\nMvaa 92,136,184\n"
"FM 44,251,252\nFM 45,251,253\nMvaa 92,140,188\nD0 9,24,44,3072\nv_lshlrev_b32 v252, 16, v207\nMvaa 88,132,188\nv_and_b32 v253, 0xffff0000, v207\nFM 38,247,252\nMvaa 88,128,184\n"
"s_waitcnt vmcnt(23) lgkmcnt(0)\nMaaa 0,192,160\nRa 128,10,0\nFM 39,247,253\nMaaa 0,196,164\ns_add_u32 m0, s47, 49152\ns_add_u32 s44, s44, 0x40000\n"
"buffer_load_dwordx4 v[236:239], v20, s[92:95], s88 offen offset:2688\nMaaa 4,204,164\nRa 132,11,0\nFM 46,251,252\nMaaa 4,200,160\nFM 47,251,253\nv_lshlrev_b32 v252, 16, v220\n"
"Maaa 64,208,160\nRa 136,10,2048\nv_and_b32 v253, 0xffff0000, v220\nMaaa 64,212,164\nD0 6,24,44,0\nFM 48,247,252\nMaaa 68,220,164\nRa 140,11,2048\nFM 49,247,253\nMaaa 68,216,160\n"
"FM 56,251,252\nFM 57,251,253\nMaaa 76,216,168\nRa 144,10,8192\nv_lshlrev_b32 v252, 16, v221\nMaaa 76,220,172\nv_and_b32 v253, 0xffff0000, v221\nFM 50,247,252\nMaaa 72,212,172\n"
"Ra 148,11,8192\nFM 51,247,253\nMaaa 72,208,168\nFM 58,251,252\nFM 59,251,253\nMaaa 12,200,168\nRa 152,10,10240\nv_lshlrev_b32 v252, 16, v222\nMaaa 12,204,172\nD0 7,24,44,1024\n"
"v_and_b32 v253, 0xffff0000, v222\nMaaa 8,196,172\nRa 156,11,10240\nFM 52,247,252\nMaaa 8,192,168\nFM 53,247,253\nFM 60,251,252\nMaaa 16,192,176\nFM 61,251,253\n"
"v_lshlrev_b32 v252, 16, v223\nMaaa 16,196,180\nv_and_b32 v253, 0xffff0000, v223\nMaaa 20,204,180\nFM 54,247,252\nFM 55,247,253\nMaaa 20,200,176\nFM 62,251,252\nFM 63,251,253\n"
"Maaa 80,208,176\nv_permlane32_swap_b32 v32, v48\nv_permlane32_swap_b32 v33, v49\nMaaa 80,212,180\nD0 8,24,44,2048\nv_permlane32_swap_b32 v34, v50\nMaaa 84,220,180\n"
"v_permlane32_swap_b32 v35, v51\nv_permlane32_swap_b32 v36, v52\nMaaa 84,216,176\nv_permlane32_swap_b32 v37, v53\nv_permlane32_swap_b32 v38, v54\nMaaa 92,216,184\n"
"v_permlane32_swap_b32 v39, v55\nv_permlane32_swap_b32 v40, v56\nMaaa 92,220,188\nv_permlane32_swap_b32 v41, v57\nv_permlane32_swap_b32 v42, v58\nMaaa 88,212,188\n"
"v_permlane32_swap_b32 v43, v59\nv_permlane32_swap_b32 v44, v60\nMaaa 88,208,184\nv_permlane32_swap_b32 v45, v61\nv_permlane32_swap_b32 v46, v62\nMaaa 28,200,184\n"
"v_permlane32_swap_b32 v47, v63\nAD 32,32,48\nMaaa 28,204,188\nD0 9,24,44,3072\nAD 33,33,49\nMaaa 24,196,188\nAD 34,34,50\nAD 35,35,51\nMaaa 24,192,184\n"
"s_waitcnt vmcnt(20) lgkmcnt(0)\ns_barrier\nMaaa 32,192,224\nTa 160,12,0\nAD 36,36,52\nMaaa 32,196,228\nTa 162,12,2048\nMaaa 36,204,228\ns_add_u32 m0, s47, -16384\n"
"s_add_u32 s48, s48, 128\nMaaa 36,200,224\nTa 164,12,16384\nAD 37,37,53\nMaaa 96,208,224\nTa 166,12,18432\nAD 38,38,54\nMaaa 96,212,228\nD0 2,20,48,0\nAD 39,39,55\n"
"Maaa 100,220,228\nTa 168,13,0\nAD 40,40,56\nMaaa 100,216,224\nTa 170,13,2048\nAD 41,41,57\nMaaa 108,216,232\nAD 42,42,58\nAD 43,43,59\nMaaa 108,220,236\nTa 172,13,16384\n"
"AD 44,44,60\nMaaa 104,212,236\nTa 174,13,18432\nAD 45,45,61\nMaaa 104,208,232\nAD 46,46,62\nAD 47,47,63\nMaaa 44,200,232\nTa 176,14,0\nv_permlane16_swap_b32 v32, v40\n"
"Maaa 44,204,236\nTa 178,14,2048\nv_permlane16_swap_b32 v33, v41\nMaaa 40,196,236\nD0 3,20,48,1024\nv_permlane16_swap_b32 v34, v42\nMaaa 40,192,232\nTa 180,14,16384\n"
"v_permlane16_swap_b32 v35, v43\nMaaa 48,192,240\nTa 182,14,18432\nv_permlane16_swap_b32 v36, v44\nMaaa 48,196,244\nv_permlane16_swap_b32 v37, v45\nMaaa 52,204,244\nTa 184,15,0\n"
"v_permlane16_swap_b32 v38, v46\nMaaa 52,200,240\nTa 186,15,2048\nv_permlane16_swap_b32 v39, v47\nMaaa 112,208,240\nAD 32,32,40\nAD 33,33,41\nMaaa 112,212,244\nTa 188,15,16384\n"
"AD 34,34,42\nMaaa 116,220,244\nTa 190,15,18432\nAD 35,35,43\nMaaa 116,216,240\nD0 4,20,48,2048\nAD 36,36,44\nMaaa 124,216,248\nAD 37,37,45\nAD 38,38,46\nMaaa 124,220,252\n"
"AD 39,39,47\nv_add_f32_dpp v32, v32, v32 row_ror:8 row_mask:0xf bank_mask:0xf\nMaaa 120,212,252\nv_add_f32_dpp v33, v33, v33 row_ror:8 row_mask:0xf bank_mask:0xf\n"
"v_add_f32_dpp v34, v34, v34 row_ror:8 row_mask:0xf bank_mask:0xf\nMaaa 120,208,248\nv_add_f32_dpp v35, v35, v35 row_ror:8 row_mask:0xf bank_mask:0xf\n"
"v_add_f32_dpp v36, v36, v36 row_ror:8 row_mask:0xf bank_mask:0xf\nMaaa 60,200,248\nv_add_f32_dpp v37, v37, v37 row_ror:8 row_mask:0xf bank_mask:0xf\n"
"v_add_f32_dpp v38, v38, v38 row_ror:8 row_mask:0xf bank_mask:0xf\nMaaa 60,204,252\nD0 5,20,48,3072\nv_add_f32_dpp v39, v39, v39 row_ror:8 row_mask:0xf bank_mask:0xf\n"
"Maaa 56,196,252\nv_add_f32_dpp v32, v32, v32 row_ror:4 row_mask:0xf bank_mask:0xf\nv_add_f32_dpp v33, v33, v33 row_ror:4 row_mask:0xf bank_mask:0xf\nMaaa 56,192,248\nWL 0\n"
"Mvaa 64,128,160\nTa 224,16,0\n.W558:\ns_cbranch_execz .W562\nv_add_f32_dpp v34, v34, v34 row_ror:4 row_mask:0xf bank_mask:0xf\nMvaa 64,132,164\nTa 226,16,2048\nMvaa 68,140,164\n"
"s_add_u32 m0, s47, -65536\ns_add_u32 s45, s45, 128\nMvaa 68,136,160\nTa 228,16,16384\nv_add_f32_dpp v35, v35, v35 row_ror:4 row_mask:0xf bank_mask:0xf\nMvaa 128,144,160\n"
"Ta 230,16,18432\nv_add_f32_dpp v36, v36, v36 row_ror:4 row_mask:0xf bank_mask:0xf\nMvaa 128,148,164\nD0 2,20,45,0\n"
"v_add_f32_dpp v37, v37, v37 row_ror:4 row_mask:0xf bank_mask:0xf\nMvaa 132,156,164\nTa 232,17,0\nv_add_f32_dpp v38, v38, v38 row_ror:4 row_mask:0xf bank_mask:0xf\n"
"Mvaa 132,152,160\nTa 234,17,2048\nv_add_f32_dpp v39, v39, v39 row_ror:4 row_mask:0xf bank_mask:0xf\nMvaa 140,152,168\n"
"v_add_f32_dpp v32, v32, v32 row_ror:2 row_mask:0xf bank_mask:0xf\nv_add_f32_dpp v33, v33, v33 row_ror:2 row_mask:0xf bank_mask:0xf\nMvaa 140,156,172\nTa 236,17,16384\n"
"v_add_f32_dpp v34, v34, v34 row_ror:2 row_mask:0xf bank_mask:0xf\nMvaa 136,148,172\nTa 238,17,18432\nv_add_f32_dpp v35, v35, v35 row_ror:2 row_mask:0xf bank_mask:0xf\n"
"Mvaa 136,144,168\nv_add_f32_dpp v36, v36, v36 row_ror:2 row_mask:0xf bank_mask:0xf\nv_add_f32_dpp v37, v37, v37 row_ror:2 row_mask:0xf bank_mask:0xf\nMvaa 76,136,168\nTa 240,18,0\n"
"v_add_f32_dpp v38, v38, v38 row_ror:2 row_mask:0xf bank_mask:0xf\nMvaa 76,140,172\nTa 242,18,2048\nv_add_f32_dpp v39, v39, v39 row_ror:2 row_mask:0xf bank_mask:0xf\n"
"Mvaa 72,132,172\nD0 3,20,45,1024\nv_lshrrev_b32 v48, 4, v0\nMvaa 72,128,168\nTa 244,18,16384\nv_and_b32 v49, 1, v0\nMvaa 80,128,176\nTa 246,18,18432\nv_lshlrev_b32 v49, 5, v49\n"
"Mvaa 80,132,180\nv_lshl_add_u32 v49, v48, 6, v49\nMvaa 84,140,180\nTa 248,19,0\ns_lshl_b32 s2, s11, 8\nMvaa 84,136,176\nTa 250,19,2048\ns_add_u32 s2, s2, 132096\nMvaa 144,144,176\n"
"v_add_u32 v49, s2, v49\nds_write_b128 v49, v[32:35]\nMvaa 144,148,180\nTa 252,19,16384\nds_write_b128 v49, v[36:39] offset:16\nMvaa 148,156,180\nTa 254,19,18432\n"
"v_and_b32 v50, 31, v0\nMvaa 148,152,176\nD0 4,20,45,2048\nv_lshlrev_b32 v50, 2, v50\nMvaa 156,152,184\ns_lshl_b32 s2, s11, 10\ns_add_u32 s2, s2, 133120\nMvaa 156,156,188\n"
"v_add_u32 v51, s2, v50\nv_add_u32 v50, 132096, v50\nMvaa 152,148,188\nv_lshl_add_u32 v52, v48, 4, s2\nMvaa 152,144,184\nMvaa 92,136,184\nMvaa 92,140,188\nD0 5,20,45,3072\n"
"Mvaa 88,132,188\nMvaa 88,128,184\ns_waitcnt vmcnt(18) lgkmcnt(0)\ns_barrier\nMvaa 96,128,224\nRa 192,10,16384\nds_read_b32 v32, v50 offset:0\nMvaa 96,132,228\n"
"s_add_u32 m0, s47, 0\ns_add_u32 s44, s44, 0x40000\nMvaa 100,140,228\nRa 196,11,16384\nMvaa 100,136,224\nds_read_b32 v33, v50 offset:256\nds_read_b32 v34, v50 offset:512\n"
"Mvaa 160,144,224\nRa 200,10,18432\nMvaa 160,148,228\nD0 6,24,44,0\nMvaa 164,156,228\nRa 204,11,18432\nds_read_b32 v35, v50 offset:768\nMvaa 164,152,224\n"
"ds_read_b32 v36, v50 offset:128\nMvaa 172,152,232\nRa 208,10,24576\nds_read_b32 v37, v50 offset:384\nMvaa 172,156,236\nds_read_b32 v38, v50 offset:640\nMvaa 168,148,236\n"
"Ra 212,11,24576\nMvaa 168,144,232\nds_read_b32 v39, v50 offset:896\nMvaa 108,136,232\nRa 216,10,26624\nMvaa 108,140,236\nD0 7,24,44,1024\nMvaa 104,132,236\nRa 220,11,26624\n"
"Mvaa 104,128,232\nMvaa 112,128,240\nMvaa 112,132,244\nMvaa 116,140,244\nMvaa 116,136,240\nMvaa 176,144,240\nMvaa 176,148,244\nD0 8,24,44,2048\nMvaa 180,156,244\nWL 2\nAD 32,32,33\n"
"Mvaa 180,152,240\nAD 34,34,35\nMvaa 188,152,248\nAD 36,36,37\nAD 38,38,39\nMvaa 188,156,252\nAD 32,32,34\nMvaa 184,148,252\nAD 36,36,38\nMvaa 184,144,248\nCV 32,32,36\n"
"v_and_b32 v36, 0xffff0000, v32\nMvaa 124,136,248\nv_lshlrev_b32 v32, 16, v32\nMvaa 124,140,252\nD0 9,24,44,3072\nMvaa 120,132,252\nv_mul_f32 v33, 0xbfb8aa3b, v32\n"
"Mvaa 120,128,248\nWL 0\nMaaa 32,192,224\nRa 128,10,32768\nMaaa 32,196,228\ns_add_u32 m0, s47, 16384\ns_add_u32 s44, s44, 0x40000\nMaaa 36,204,228\nRa 132,11,32768\nEX 33,33\n"
"Maaa 36,200,224\nv_add_f32 v33, 1.0, v33\nMaaa 96,208,224\nRa 136,10,34816\nv_rcp_f32 v33, v33\nMaaa 96,212,228\nD0 6,24,44,0\nMaaa 100,220,228\nRa 140,11,34816\nMU 33,32,33\n"
"Maaa 100,216,224\nCV 33,33,33\nMaaa 108,216,232\nRa 144,10,40960\nv_lshlrev_b32 v33, 16, v33\nMaaa 108,220,236\nMU 33,33,36\nMaaa 104,212,236\nRa 148,11,40960\nMaaa 104,208,232\n"
"CV 33,33,33\nv_lshlrev_b32 v33, 16, v33\nMaaa 44,200,232\nRa 152,10,43008\nMaaa 44,204,236\nD0 7,24,44,1024\nMaaa 40,196,236\nRa 156,11,43008\nds_write_b32 v51, v33\n"
"Maaa 40,192,232\nRv 244,52,0\nMaaa 48,192,240\nRv 248,52,64\ns_and_b32 s2, s74, 15\nMaaa 48,196,244\ns_sub_u32 s2, s2, 2\ns_cmp_lt_u32 s2, 14\ns_cselect_b32 s5, 0, 0x40000000\n"
"Maaa 52,204,244\ns_and_b32 s2, s2, 15\nMaaa 52,200,240\ns_mul_i32 s4, s73, 14\ns_add_u32 s4, s4, s2\nMaaa 112,208,240\nv_and_b32 v49, 15, v0\nMaaa 112,212,244\nD0 8,24,44,2048\n"
"Maaa 116,220,244\nv_lshlrev_b32 v50, 3, v49\nv_and_b32 v51, 1, v48\nMaaa 116,216,240\nv_lshl_add_u32 v50, v51, 2, v50\nMaaa 124,216,248\ns_mul_i32 s2, s11, 112\n"
"v_add_u32 v50, s2, v50\nMaaa 124,220,252\nv_lshrrev_b32 v51, 4, v50\nMaaa 120,212,252\nv_lshrrev_b32 v48, 1, v48\nMaaa 120,208,248\nv_mad_u32_u24 v51, v48, 28, v51\n"
"v_mul_u32_u24 v51, 7168, v51\nMaaa 60,200,248\nv_and_b32 v50, 15, v50\nMaaa 60,204,252\nD0 9,24,44,3072\nMaaa 56,196,252\nv_lshl_add_u32 v51, v50, 2, v51\n"
"v_cmp_gt_u32 vcc, 14, v49\ns_lshl_b32 s4, s4, 6\ns_add_u32 s4, s4, 0xc04c800\nv_cndmask_b32 v51, v254, v51, vcc\nMaaa 56,192,248\ns_waitcnt vmcnt(16) lgkmcnt(3)\ns_barrier\n"
"Maaa 0,192,160\nTa 224,16,32768\ns_or_b32 s4, s4, s5\nMaaa 0,196,164\nTa 226,16,34816\nMaaa 4,204,164\ns_add_u32 m0, s47, -49152\ns_add_u32 s48, s48, 128\nMaaa 4,200,160\n"
"Ta 228,16,49152\nWL 3\nMaaa 64,208,160\nTa 230,16,51200\nMaaa 64,212,164\nD0 2,20,48,0\nMaaa 68,220,164\nTa 232,17,32768\nv_lshlrev_b32 v252, 16, v224\nMaaa 68,216,160\n"
"Ta 234,17,34816\nMaaa 76,216,168\nv_and_b32 v253, 0xffff0000, v224\nMU 32,244,252\nMaaa 76,220,172\nTa 236,17,49152\nMaaa 72,212,172\nTa 238,17,51200\nMaaa 72,208,168\n"
"MU 33,244,253\nMU 40,248,252\nMaaa 12,200,168\nTa 240,18,32768\nMaaa 12,204,172\nTa 242,18,34816\nMaaa 8,196,172\nD0 3,20,48,1024\nMU 41,248,253\nMaaa 8,192,168\nTa 244,18,49152\n"
"Maaa 16,192,176\nTa 246,18,51200\nv_lshlrev_b32 v252, 16, v225\nMaaa 16,196,180\nv_and_b32 v253, 0xffff0000, v225\nMaaa 20,204,180\nTa 248,19,32768\nMaaa 20,200,176\n"
"Ta 250,19,34816\nMU 34,244,252\nMaaa 80,208,176\nMU 35,244,253\nMaaa 80,212,180\nTa 252,19,49152\nMaaa 84,220,180\nTa 254,19,51200\nMU 42,248,252\nMaaa 84,216,176\n"
"D0 4,20,48,2048\nMaaa 92,216,184\nMU 43,248,253\nv_lshlrev_b32 v252, 16, v226\nMaaa 92,220,188\nv_and_b32 v253, 0xffff0000, v226\nMaaa 88,212,188\nMU 36,244,252\nMaaa 88,208,184\n"
"MU 37,244,253\nMU 44,248,252\nMaaa 28,200,184\nMU 45,248,253\nMaaa 28,204,188\nD0 5,20,48,3072\nMaaa 24,196,188\nv_lshlrev_b32 v252, 16, v227\nv_and_b32 v253, 0xffff0000, v227\n"
"Maaa 24,192,184\nWL 0\nMvaa 96,128,224\nTa 160,12,32768\nMU 38,244,252\nMvaa 96,132,228\nTa 162,12,34816\nMvaa 100,140,228\ns_add_u32 m0, s47, -32768\ns_add_u32 s45, s45, 128\n"
"Mvaa 100,136,224\nTa 164,12,49152\nMU 39,244,253\nMvaa 160,144,224\nTa 166,12,51200\nMvaa 160,148,228\nD0 2,20,45,0\nMvaa 164,156,228\nTa 168,13,32768\nMU 46,248,252\n"
"Mvaa 164,152,224\nTa 170,13,34816\nMvaa 172,152,232\nMU 47,248,253\nv_lshlrev_b32 v252, 16, v228\nMvaa 172,156,236\nTa 172,13,49152\nMvaa 168,148,236\nTa 174,13,51200\n"
"Mvaa 168,144,232\nv_and_b32 v253, 0xffff0000, v228\nFM 32,245,252\nMvaa 108,136,232\nTa 176,14,32768\nMvaa 108,140,236\nTa 178,14,34816\nMvaa 104,132,236\nD0 3,20,45,1024\n"
"FM 33,245,253\nMvaa 104,128,232\nTa 180,14,49152\nMvaa 112,128,240\nTa 182,14,51200\nFM 40,249,252\nMvaa 112,132,244\nFM 41,249,253\nMvaa 116,140,244\nTa 184,15,32768\n"
"Mvaa 116,136,240\nTa 186,15,34816\nv_lshlrev_b32 v252, 16, v229\nMvaa 176,144,240\nv_and_b32 v253, 0xffff0000, v229\nMvaa 176,148,244\nTa 188,15,49152\nMvaa 180,156,244\n"
"Ta 190,15,51200\nFM 34,245,252\nMvaa 180,152,240\nD0 4,20,45,2048\nMvaa 188,152,248\nFM 35,245,253\nFM 42,249,252\nMvaa 188,156,252\nFM 43,249,253\nMvaa 184,148,252\n"
"v_lshlrev_b32 v252, 16, v230\nMvaa 184,144,248\nv_and_b32 v253, 0xffff0000, v230\nFM 36,245,252\nMvaa 124,136,248\nFM 37,245,253\nMvaa 124,140,252\nD0 5,20,45,3072\n"
"Mvaa 120,132,252\nFM 44,249,252\nFM 45,249,253\nMvaa 120,128,248\ns_waitcnt vmcnt(16) lgkmcnt(0)\ns_barrier\nMvaa 64,128,160\nRa 192,10,49152\nv_lshlrev_b32 v252, 16, v231\n"
"Mvaa 64,132,164\ns_add_u32 m0, s47, 32768\ns_add_u32 s44, s44, 0x40000\nMvaa 68,140,164\nRa 196,11,49152\nMvaa 68,136,160\nv_and_b32 v253, 0xffff0000, v231\nFM 38,245,252\n"
"Mvaa 128,144,160\nRa 200,10,51200\nMvaa 128,148,164\nD0 6,24,44,0\nMvaa 132,156,164\nRa 204,11,51200\nFM 39,245,253\nMvaa 132,152,160\nFM 46,249,252\nMvaa 140,152,168\n"
"Ra 208,10,57344\nFM 47,249,253\nMvaa 140,156,172\nv_lshlrev_b32 v252, 16, v232\nMvaa 136,148,172\nRa 212,11,57344\nMvaa 136,144,168\nv_and_b32 v253, 0xffff0000, v232\n"
"FM 32,246,252\nMvaa 76,136,168\nRa 216,10,59392\nMvaa 76,140,172\nD0 7,24,44,1024\nMvaa 72,132,172\nRa 220,11,59392\nFM 33,246,253\nMvaa 72,128,168\nFM 40,250,252\n"
"Mvaa 80,128,176\nFM 41,250,253\nv_lshlrev_b32 v252, 16, v233\nMvaa 80,132,180\nv_and_b32 v253, 0xffff0000, v233\nMvaa 84,140,180\nFM 34,246,252\nMvaa 84,136,176\nFM 35,246,253\n"
"FM 42,250,252\nMvaa 144,144,176\nFM 43,250,253\nMvaa 144,148,180\nD0 8,24,44,2048\nMvaa 148,156,180\nv_lshlrev_b32 v252, 16, v234\nv_and_b32 v253, 0xffff0000, v234\n"
"Mvaa 148,152,176\nFM 36,246,252\nMvaa 156,152,184\nFM 37,246,253\nFM 44,250,252\nMvaa 156,156,188\nFM 45,250,253\nMvaa 152,148,188\nv_lshlrev_b32 v252, 16, v235\nMvaa 152,144,184\n"
"v_and_b32 v253, 0xffff0000, v235\nFM 38,246,252\nMvaa 92,136,184\nFM 39,246,253\nMvaa 92,140,188\nD0 9,24,44,3072\nMvaa 88,132,188\nFM 46,250,252\nFM 47,250,253\nMvaa 88,128,184\n"
"WL 0\nMaaa 0,192,160\nRa 128,10,0\nv_lshlrev_b32 v252, 16, v236\nMaaa 0,196,164\ns_add_u32 m0, s47, 49152\ns_add_u32 s44, s44, 0x40000\nMaaa 4,204,164\nRa 132,11,0\n"
"Maaa 4,200,160\nv_and_b32 v253, 0xffff0000, v236\nFM 32,247,252\nMaaa 64,208,160\nRa 136,10,2048\nMaaa 64,212,164\nD0 6,24,44,0\nMaaa 68,220,164\nRa 140,11,2048\nFM 33,247,253\n"
"Maaa 68,216,160\nFM 40,251,252\nMaaa 76,216,168\nRa 144,10,8192\nFM 41,251,253\nMaaa 76,220,172\nv_lshlrev_b32 v252, 16, v237\nMaaa 72,212,172\nRa 148,11,8192\nMaaa 72,208,168\n"
"v_and_b32 v253, 0xffff0000, v237\nFM 34,247,252\nMaaa 12,200,168\nRa 152,10,10240\nMaaa 12,204,172\nD0 7,24,44,1024\nMaaa 8,196,172\nRa 156,11,10240\nFM 35,247,253\n"
"Maaa 8,192,168\nFM 42,251,252\nMaaa 16,192,176\nFM 43,251,253\nv_lshlrev_b32 v252, 16, v238\nMaaa 16,196,180\nv_and_b32 v253, 0xffff0000, v238\nMaaa 20,204,180\nFM 36,247,252\n"
"Maaa 20,200,176\nFM 37,247,253\nFM 44,251,252\nMaaa 80,208,176\nFM 45,251,253\nMaaa 80,212,180\nD0 8,24,44,2048\nMaaa 84,220,180\nv_lshlrev_b32 v252, 16, v239\n"
"v_and_b32 v253, 0xffff0000, v239\nMaaa 84,216,176\nFM 38,247,252\nMaaa 92,216,184\nFM 39,247,253\nFM 46,251,252\nMaaa 92,220,188\nFM 47,251,253\nMaaa 88,212,188\n"
"v_permlane32_swap_b32 v32, v40\nMaaa 88,208,184\nv_permlane32_swap_b32 v33, v41\nv_permlane32_swap_b32 v34, v42\nMaaa 28,200,184\nv_permlane32_swap_b32 v35, v43\nMaaa 28,204,188\n"
"D0 9,24,44,3072\nMaaa 24,196,188\nv_permlane32_swap_b32 v36, v44\nv_permlane32_swap_b32 v37, v45\nMaaa 24,192,184\nWV 16\ns_barrier\nMaaa 32,192,224\nTa 160,12,0\n"
"v_permlane32_swap_b32 v38, v46\nMaaa 32,196,228\nTa 162,12,2048\nMaaa 36,204,228\ns_add_u32 m0, s47, -16384\ns_add_u32 s48, s48, 128\nMaaa 36,200,224\nTa 164,12,16384\n"
"v_permlane32_swap_b32 v39, v47\nMaaa 96,208,224\nTa 166,12,18432\nMaaa 96,212,228\nD0 2,20,48,0\nMaaa 100,220,228\nTa 168,13,0\nAD 32,32,40\nMaaa 100,216,224\nTa 170,13,2048\n"
"Maaa 108,216,232\nAD 33,33,41\nAD 34,34,42\nMaaa 108,220,236\nTa 172,13,16384\nMaaa 104,212,236\nTa 174,13,18432\nMaaa 104,208,232\nAD 35,35,43\nAD 36,36,44\nMaaa 44,200,232\n"
"Ta 176,14,0\nMaaa 44,204,236\nTa 178,14,2048\nMaaa 40,196,236\nD0 3,20,48,1024\nAD 37,37,45\nMaaa 40,192,232\nTa 180,14,16384\nMaaa 48,192,240\nTa 182,14,18432\nAD 38,38,46\n"
"Maaa 48,196,244\nAD 39,39,47\n.W559:\ns_cbranch_execz .W563\nMaaa 52,204,244\nTa 184,15,0\nMaaa 52,200,240\nTa 186,15,2048\nv_permlane16_swap_b32 v32, v36\nMaaa 112,208,240\n"
"v_permlane16_swap_b32 v33, v37\nMaaa 112,212,244\nTa 188,15,16384\nMaaa 116,220,244\nTa 190,15,18432\nv_permlane16_swap_b32 v34, v38\nMaaa 116,216,240\nD0 4,20,48,2048\n"
"Maaa 124,216,248\nv_permlane16_swap_b32 v35, v39\nAD 32,32,36\nMaaa 124,220,252\nAD 33,33,37\nMaaa 120,212,252\nAD 34,34,38\nMaaa 120,208,248\nAD 35,35,39\nSv 32,51,16,4,0\n"
"Maaa 60,200,248\nMaaa 60,204,252\nD0 5,20,48,3072\nMaaa 56,196,252\nMaaa 56,192,248\nWL 0\nMvaa 64,128,160\nTa 224,16,0\ns_lshr_b32 s3, s11, 1\nMvaa 64,132,164\nTa 226,16,2048\n"
"Mvaa 68,140,164\ns_lshl_b32 s3, s3, 13\nMvaa 68,136,160\nTa 228,16,16384\nMvaa 128,144,160\nTa 230,16,18432\nMvaa 128,148,164\ns_lshl_b32 s2, s11, 12\nMvaa 132,156,164\n"
"Ta 232,17,0\nMvaa 132,152,160\nTa 234,17,2048\nMvaa 140,152,168\ns_add_u32 s2, s2, s3\ns_add_u32 s2, s2, s72\nMvaa 140,156,172\nTa 236,17,16384\nMvaa 136,148,172\nTa 238,17,18432\n"
"Mvaa 136,144,168\ns_lshl_b32 s3, s11, 12\nMvaa 76,136,168\nTa 240,18,0\nMvaa 76,140,172\nTa 242,18,2048\nMvaa 72,132,172\ns_add_u32 s89, s3, 0x0\nMvaa 72,128,168\nTa 244,18,16384\n"
"Mvaa 80,128,176\nTa 246,18,18432\ns_mov_b32 s88, 0xffff8000\nMvaa 80,132,180\ns_lshl_b32 s4, s13, 1\nMvaa 84,140,180\nTa 248,19,0\nMvaa 84,136,176\nTa 250,19,2048\n"
"Mvaa 144,144,176\ns_sub_u32 s79, 15, s4\nMvaa 144,148,180\nTa 252,19,16384\nMvaa 148,156,180\nTa 254,19,18432\nMvaa 148,152,176\ns_add_u32 s96, s2, 0x1310000\nMvaa 156,152,184\n"
"s_mov_b32 s63, 0x53800000\ns_mov_b32 s64, 0x2b800000\nMvaa 156,156,188\ns_sub_u32 s98, s4, 1\nMvaa 152,148,188\ns_lshl_b32 s5, s13, 5\nMvaa 152,144,184\ns_add_u32 s5, s5, s11\n"
"Mvaa 92,136,184\ns_lshl_b32 s65, s5, 9\nMvaa 92,140,188\ns_add_u32 s65, s65, s72\nMvaa 88,132,188\ns_add_u32 s65, s65, 0x1580000\nMvaa 88,128,184\ns_waitcnt vmcnt(13) lgkmcnt(0)\n"
"s_barrier\nMvaa 96,128,224\nRa 192,10,16384\ns_and_b32 s66, s72, 0x2000\ns_add_u32 m0, s47, 65536\ns_add_u32 s46, s56, 0x0\nMvaa 96,132,228\ns_add_u32 s66, s66, s56\n"
"Mvaa 100,140,228\nRa 196,11,16384\nD1 1,16,46,0\nMvaa 100,136,224\ns_add_u32 s66, s66, 0x100000\nMvaa 160,144,224\nRa 200,10,18432\nMvaa 160,148,228\ns_and_b32 s5, s12, 1\n"
"Mvaa 164,156,228\nRa 204,11,18432\nMvaa 164,152,224\ns_mul_i32 s58, s5, 0x202000\nD1 1,16,46,1024\nMvaa 172,152,232\nRa 208,10,24576\ns_xor_b32 s58, s58, s72\nMvaa 172,156,236\n"
"s_add_u32 s58, s58, 0x15c0000\nMvaa 168,148,236\nRa 212,11,24576\nMvaa 168,144,232\ns_lshl_b32 s5, s5, 8\nMvaa 108,136,232\nRa 216,10,26624\nD1 1,16,46,2048\nMvaa 108,140,236\n"
"s_add_u32 s61, s58, s5\nMvaa 104,132,236\nRa 220,11,26624\nMvaa 104,128,232\ns_lshl4_add_u32 s60, s13, s61\nMvaa 112,128,240\ns_add_u32 s54, s60, 0\ns_xor_b32 s60, s54, 0x100\n"
"Mvaa 112,132,244\ns_cmp_eq_u32 s11, 0\ns_cselect_b32 s54, s54, 0x40000000\nD1 1,16,46,3072\nMvaa 116,140,244\ns_getpc_b64 s[8:9]\n.PC167:\ns_add_u32 s8, s8, .HD143-.PC167\n"
"s_addc_u32 s9, s9, 0\nMvaa 116,136,240\ns_mov_b32 s97, 0xff800000\nMvaa 176,144,240\ns_mov_b32 s90, 3\ns_cmp_lt_u32 s11, 2\ns_cselect_b32 s92, 2, 0\ns_add_u32 m0, s47, 81920\n"
"s_add_u32 s46, s56, 0x1000\nMvaa 176,148,244\ns_add_u32 s86, s2, 0x20000\nMvaa 180,156,244\ns_add_u32 s87, s2, 0x410000\ns_cmp_eq_u32 s13, 7\ns_cselect_b32 s92, s92, 0\n"
"D1 1,16,46,0\nMvaa 180,152,240\ns_cmp_gt_u32 s13, 5\ns_cselect_b32 s4, s12, 15\nMvaa 188,152,248\ns_cmp_lt_u32 s4, 14\ns_cselect_b32 s5, s11, 3\nMvaa 188,156,252\n"
"s_cmp_lt_u32 s5, 2\ns_cselect_b32 s91, 1, 100\nMvaa 184,148,252\nv_and_b32 v27, 0x70, v1\nbuffer_load_dword v27, v27, s[16:19], s61 offen offset:128 sc0 sc1\nMvaa 184,144,248\n"
"s_lshr_b32 s2, s13, 2\ns_bfe_u32 s3, s13, 0x10001\nD1 1,16,46,1024\nMvaa 124,136,248\ns_mul_i32 s3, s3, 17\ns_add_u32 s2, s2, s3\nMvaa 124,140,252\ns_lshl2_add_u32 s3, s14, 0\n"
"Mvaa 120,132,252\ns_lshl_b32 s3, s3, 2\ns_lshr_b32 s3, 0xe420ca86, s3\nMvaa 120,128,248\nWL 0\nMaaa 32,192,224\nRa 128,10,32768\ns_and_b32 s3, s3, 15\nD1 1,16,46,2048\n"
"Maaa 32,196,228\ns_add_u32 s2, s2, s3\ns_cmp_eq_u32 s2, 32\ns_cselect_b32 s4, 16, s2\nMaaa 36,204,228\nRa 132,11,32768\ns_mul_i32 s75, s4, 65536\nMaaa 36,200,224\n"
"s_lshr_b32 s2, s13, 2\ns_bfe_u32 s3, s13, 0x10001\nMaaa 96,208,224\nRa 136,10,34816\nMaaa 96,212,228\ns_mul_i32 s3, s3, 17\ns_add_u32 s2, s2, s3\nD1 1,16,46,3072\n"
"Maaa 100,220,228\nRa 140,11,34816\ns_lshl2_add_u32 s3, s14, 1\nMaaa 100,216,224\ns_lshl_b32 s3, s3, 2\nMaaa 108,216,232\nRa 144,10,40960\ns_lshr_b32 s3, 0xe420ca86, s3\n"
"s_add_u32 m0, s47, 16384\ns_add_u32 s46, s56, 0x4000\nMaaa 108,220,236\ns_and_b32 s3, s3, 15\ns_add_u32 s2, s2, s3\nMaaa 104,212,236\nRa 148,11,40960\ns_cmp_eq_u32 s2, 32\n"
"s_cselect_b32 s4, 16, s2\nD1 1,16,46,0\nMaaa 104,208,232\ns_mul_i32 s76, s4, 65536\ns_lshr_b32 s2, s13, 2\nMaaa 44,200,232\nRa 152,10,43008\ns_bfe_u32 s3, s13, 0x10001\n"
"Maaa 44,204,236\ns_mul_i32 s3, s3, 17\nMaaa 40,196,236\nRa 156,11,43008\ns_add_u32 s2, s2, s3\nMaaa 40,192,232\ns_lshl2_add_u32 s3, s14, 2\ns_lshl_b32 s3, s3, 2\nD1 1,16,46,1024\n"
"Maaa 48,192,240\ns_lshr_b32 s3, 0xe420ca86, s3\ns_and_b32 s3, s3, 15\nMaaa 48,196,244\ns_add_u32 s2, s2, s3\ns_cmp_eq_u32 s2, 32\ns_cselect_b32 s4, 16, s2\nMaaa 52,204,244\n"
"s_mul_i32 s77, s4, 65536\ns_lshr_b32 s2, s13, 2\nMaaa 52,200,240\ns_bfe_u32 s3, s13, 0x10001\ns_mul_i32 s3, s3, 17\nMaaa 112,208,240\ns_add_u32 s2, s2, s3\nD1 1,16,46,2048\n"
"Maaa 112,212,244\ns_lshl2_add_u32 s3, s14, 3\ns_lshl_b32 s3, s3, 2\nMaaa 116,220,244\ns_lshr_b32 s3, 0xe420ca86, s3\ns_and_b32 s3, s3, 15\nMaaa 116,216,240\ns_add_u32 s2, s2, s3\n"
"s_cmp_eq_u32 s2, 32\ns_cselect_b32 s4, 16, s2\nMaaa 124,216,248\ns_mul_i32 s78, s4, 65536\ns_lshr_b32 s3, s11, 1\nMaaa 124,220,252\ns_lshl_b32 s3, s3, 15\ns_add_u32 s2, s72, s3\n"
"D1 1,16,46,3072\nMaaa 120,212,252\ns_and_b32 s3, s11, 1\nMaaa 120,208,248\ns_lshl_b32 s3, s3, 14\ns_add_u32 s2, s2, s3\nMaaa 60,200,248\ns_add_u32 s2, s2, 0x400000\n"
"s_mov_b32 s55, 4096\nMaaa 60,204,252\ns_add_u32 s75, s75, s2\nMaaa 56,196,252\ns_add_u32 s76, s76, s2\ns_add_u32 s77, s77, s2\nMaaa 56,192,248\ns_waitcnt vmcnt(18) lgkmcnt(0)\n"
"s_barrier\nMaaa 0,192,160\nTa 224,16,32768\ns_add_u32 s78, s78, s2\nMaaa 0,196,164\nTa 226,16,34816\nMaaa 4,204,164\nMaaa 4,200,160\nTa 228,16,49152\nMaaa 64,208,160\n"
"Ta 230,16,51200\nMaaa 64,212,164\nMaaa 68,220,164\nTa 232,17,32768\nMaaa 68,216,160\nTa 234,17,34816\nMaaa 76,216,168\nWV 7\nv_cmp_ne_u32 vcc, s15, v27\ns_nop 0\n"
"s_cbranch_vccnz .S168\n.S169:\ns_sub_u32 s45, s86, 131072\nMaaa 76,220,172\nTa 236,17,49152\ns_add_u32 m0, s47, 0\ns_add_u32 s46, s45, 0x0\nMaaa 72,212,172\nTa 238,17,51200\n"
"Maaa 72,208,168\nD1 1,16,46,0\nMaaa 12,200,168\nTa 240,18,32768\ns_mov_b32 s36, s68\nMaaa 12,204,172\nTa 242,18,34816\nMaaa 8,196,172\ns_and_b32 s37, s69, 0xffff\nMaaa 8,192,168\n"
"Ta 244,18,49152\nMaaa 16,192,176\nTa 246,18,51200\nD1 1,16,46,1024\nMaaa 16,196,180\ns_mov_b32 s38, 0x1c0000\nMaaa 20,204,180\nTa 248,19,32768\nMaaa 20,200,176\nTa 250,19,34816\n"
"Maaa 80,208,176\ns_mov_b32 s39, 0x20000\ns_and_b32 s2, s74, 15\nMaaa 80,212,180\nTa 252,19,49152\nD1 1,16,46,2048\nMaaa 84,220,180\nTa 254,19,51200\nMaaa 84,216,176\n"
"s_max_u32 s2, s2, 2\nMaaa 92,216,184\ns_sub_u32 s2, s2, 2\ns_mul_i32 s3, s73, 14\nMaaa 92,220,188\ns_add_u32 s2, s2, s3\nMaaa 88,212,188\nD1 1,16,46,3072\nMaaa 88,208,184\n"
"s_lshl2_add_u32 s2, s2, s11\nMaaa 28,200,184\ns_mul_i32 s3, s2, 4682\ns_lshr_b32 s3, s3, 16\nMaaa 28,204,188\ns_mul_i32 s22, s3, 14\nMaaa 24,196,188\ns_sub_u32 s22, s2, s22\n"
"Maaa 24,192,184\nWL 0\nMvaa 96,128,224\nTa 160,12,32768\ns_add_u32 m0, s47, -32768\ns_add_u32 s46, s56, 0x5000\nD1 1,16,46,0\nMvaa 96,132,228\nTa 162,12,34816\nMvaa 100,140,228\n"
"s_and_b32 s21, s3, 28\nMvaa 100,136,224\nTa 164,12,49152\nMvaa 108,136,232\nTa 166,12,51200\ns_add_u32 s21, s21, s3\nMvaa 108,140,236\nD1 1,16,46,1024\nMvaa 104,132,236\n"
"Ta 168,13,32768\nMvaa 104,128,232\nTa 170,13,34816\nMvaa 112,128,240\ns_lshl_b32 s21, s21, 4\ns_add_u32 s21, s21, 1024\nMvaa 112,132,244\nTa 172,13,49152\nMvaa 116,140,244\n"
"Ta 174,13,51200\nD1 1,16,46,2048\nMvaa 116,136,240\ns_xor_b32 s3, s3, 28\nMvaa 124,136,248\nTa 176,14,32768\ns_bfe_u32 s41, s3, 0x30002\nMvaa 124,140,252\nTa 178,14,34816\n"
"s_cmp_gt_u32 s22, 6\ns_cselect_b32 s2, 1179648, 0\ns_cselect_b32 s1, 16, 0\nMvaa 120,132,252\ns_add_u32 s41, s41, s1\nCV 48,96,97\nMvaa 120,128,248\nTa 180,14,49152\n"
"D1 1,16,46,3072\nMvaa 160,144,224\nTa 182,14,51200\ns_lshl4_add_u32 s41, s41, 0xc048400\nMvaa 160,148,228\ns_and_b32 s1, s3, 28\nMvaa 164,156,228\nTa 184,15,32768\n"
"s_add_u32 m0, s47, -65536\ns_add_u32 s46, s45, 0x10000\nMvaa 164,152,224\nTa 186,15,34816\nCV 49,98,99\nMvaa 172,152,232\nD1 1,16,46,0\ns_add_u32 s3, s3, s1\nMvaa 172,156,236\n"
"Ta 188,15,49152\nCV 50,100,101\nMvaa 168,148,236\nTa 190,15,51200\nMvaa 168,144,232\ns_lshl_b32 s3, s3, 11\nCV 51,102,103\nMvaa 176,144,240\ns_add_u32 s2, s2, s3\n"
"s_lshl_b32 s3, s22, 17\nMvaa 176,148,244\nD1 1,16,46,1024\nMvaa 180,156,244\ns_add_u32 s2, s2, s3\nCV 52,104,105\nMvaa 180,152,240\ns_add_u32 s20, s2, 0xce0000\nMvaa 188,152,248\n"
"s_lshl_b32 s2, s22, 7\ns_add_u32 s40, s2, 0xc04b800\nMvaa 188,156,252\ns_mul_i32 s2, s21, 896\nMvaa 184,148,252\nD1 1,16,46,2048\nCV 53,106,107\nMvaa 184,144,248\nWL 0\ns_barrier\n"
"Mvaa 64,128,160\nRa 192,10,49152\ns_lshl_b32 s3, s22, 6\nMvaa 64,132,164\ns_add_u32 s22, s2, s3\nMvaa 68,140,164\nRa 196,11,49152\nMvaa 68,136,160\nD1 1,16,46,3072\nCV 54,108,109\n"
"Mvaa 76,136,168\nRa 200,10,51200\ns_add_u32 s23, s22, 57344\nMvaa 76,140,172\nCV 55,110,111\nCV 56,112,113\nMvaa 72,132,172\nRa 204,11,51200\ns_add_u32 m0, s47, 32768\n"
"s_add_u32 s46, s45, 0x8000\nMvaa 72,128,168\nCV 57,114,115\nCV 58,116,117\nMvaa 80,128,176\nRa 208,10,57344\nD1 1,16,46,0\nMvaa 80,132,180\nCV 59,118,119\nMvaa 84,140,180\n"
"Ra 212,11,57344\nCV 60,120,121\nMvaa 84,136,176\nCV 61,122,123\nMvaa 92,136,184\nRa 216,10,59392\nCV 62,124,125\nMvaa 92,140,188\nD1 1,16,46,1024\nMvaa 88,132,188\n"
"Ra 220,11,59392\nCV 63,126,127\nMvaa 88,128,184\nCV 32,64,65\nMvaa 128,144,160\nCV 33,66,67\nCV 34,68,69\nMvaa 128,148,164\nCV 35,70,71\nMvaa 132,156,164\nD1 1,16,46,2048\n"
"Mvaa 132,152,160\nCV 36,72,73\nCV 37,74,75\nMvaa 140,152,168\nCV 38,76,77\nMvaa 140,156,172\nCV 39,78,79\nCV 40,80,81\nMvaa 136,148,172\nCV 41,82,83\nMvaa 136,144,168\n"
"D1 1,16,46,3072\nCV 42,84,85\nMvaa 144,144,176\nCV 43,86,87\nMvaa 144,148,180\nCV 44,88,89\nMvaa 148,156,180\ns_add_u32 m0, s47, 49152\ns_add_u32 s46, s45, 0x400000\n"
"Mvaa 148,152,176\nCV 45,90,91\nMvaa 156,152,184\nD1 1,16,46,0\nCV 46,92,93\nMvaa 156,156,188\nCV 47,94,95\nCV 112,160,161\nCV 113,162,163\nMvaa 152,148,188\nCV 114,164,165\n"
"CV 115,166,167\nMvaa 152,144,184\nWL 0\nMaaa 0,192,160\nSv 32,1,16,75,0\nCV 116,168,169\nCV 117,170,171\nMaaa 0,196,164\nD1 1,16,46,1024\nCV 118,172,173\nMaaa 4,204,164\n"
"CV 119,174,175\nCV 120,176,177\nMaaa 4,200,160\nCV 121,178,179\nCV 122,180,181\nMaaa 12,200,168\nCV 123,182,183\nCV 124,184,185\nCV 125,186,187\nMaaa 12,204,172\nCV 126,188,189\n"
"CV 127,190,191\nMaaa 8,196,172\nD1 1,16,46,2048\nCV 96,128,129\nMaaa 8,192,168\nCV 97,130,131\nCV 98,132,133\nCV 99,134,135\nMaaa 16,192,176\nSv 36,1,16,75,1024\nCV 100,136,137\n"
"Maaa 16,196,180\nCV 101,138,139\nCV 102,140,141\nMaaa 20,204,180\nCV 103,142,143\nCV 104,144,145\nCV 105,146,147\nMaaa 20,200,176\nD1 1,16,46,3072\n.W560:\ns_cbranch_execz .W564\n"
"CV 106,148,149\nMaaa 28,200,184\nCV 107,150,151\nCV 108,152,153\nMaaa 28,204,188\nCV 109,154,155\nCV 110,156,157\nCV 111,158,159\nMaaa 24,196,188\ns_add_u32 m0, s47, -49152\n"
"s_add_u32 s46, s45, 0x408000\nMaaa 24,192,184\nv_add_u32 v2, 0x10000, v1\ns_lshr_b32 s2, s59, 1\nMaaa 64,208,160\nD1 1,16,46,0\nSv 40,1,16,75,2048\nv_add_u32 v24, s2, v2\n"
"Maaa 64,212,164\nv_xor_b32 v26, 0x1000, v24\nv_lshrrev_b32 v20, 4, v0\nMaaa 68,220,164\nv_lshlrev_b32 v20, 2, v20\nv_and_b32 v0, 15, v0\nMaaa 68,216,160\nv_sub_u32 v0, v0, v20\n"
"s_lshl_b32 s2, s11, 4\nMaaa 76,216,168\nv_add_u32 v3, s2, v0\nv_subrev_u32 v0, 32, v3\ns_lshl_b32 s2, s11, 12\nMaaa 76,220,172\nD1 1,16,46,1024\nv_add_u32 v22, s2, v1\n"
"Maaa 72,212,172\nv_add_u32 v23, 0x20000, v22\nv_add_u32 v22, 0x8000, v22\nMaaa 72,208,168\nv_mov_b32 v4, 0\nv_mov_b32 v5, 0\nv_mov_b32 v6, 0\nMaaa 80,208,176\nSv 44,1,16,75,3072\n"
"v_mov_b32 v7, 0\nMaaa 80,212,180\nv_mov_b32 v8, 0\nv_mov_b32 v9, 0\nMaaa 84,220,180\nD1 1,16,46,2048\nv_mov_b32 v10, 0\nv_mov_b32 v11, 0\nMaaa 84,216,176\nv_mov_b32 v12, 0\n"
"v_mov_b32 v13, 0\nMaaa 92,216,184\nv_mov_b32 v14, 0\nv_mov_b32 v15, 0\nMaaa 92,220,188\nv_mov_b32 v16, 0\nv_mov_b32 v17, 0\nv_mov_b32 v18, 0\nMaaa 88,212,188\nv_mov_b32 v19, 0\n"
"v_mov_b32 v86, 0\nMaaa 88,208,184\nWV 19\ns_barrier\nMaaa 32,192,224\nD1 1,16,46,3072\ns_add_u32 s75, s75, s55\nSv 48,1,16,75,0\nMaaa 32,196,228\nv_mov_b32 v87, 0\n"
"v_mov_b32 v88, 0\nMaaa 36,204,228\nv_mov_b32 v89, 0\nv_mov_b32 v95, -1\nMaaa 36,200,224\nMaaa 44,200,232\nRa 128,24,0\nMaaa 44,204,236\nMaaa 40,196,236\nRa 132,24,1024\n"
"Maaa 40,192,232\nMaaa 48,192,240\nSv 52,1,16,75,1024\nRa 136,24,2048\nMaaa 48,196,244\nMaaa 52,204,244\nRa 140,24,3072\nMaaa 52,200,240\nMaaa 60,200,248\nRa 200,23,2048\n"
"Ra 144,26,0\nMaaa 60,204,252\nRa 204,23,3072\nMaaa 56,196,252\nRa 196,23,1024\nRa 148,26,1024\nMaaa 56,192,248\nRa 192,23,0\nMaaa 96,208,224\nSv 56,1,16,75,2048\nRa 152,26,2048\n"
"Maaa 96,212,228\nMaaa 100,220,228\nRa 228,22,50176\nRa 156,26,3072\nMaaa 100,216,224\nRa 224,22,49152\nMaaa 108,216,232\nMaaa 108,220,236\nMaaa 104,212,236\nRa 236,22,52224\n"
"Maaa 104,208,232\nRa 232,22,51200\nMaaa 112,208,240\nSv 60,1,16,75,3072\nMaaa 112,212,244\nMaaa 116,220,244\nRa 244,22,1024\nMaaa 116,216,240\nRa 240,22,0\nMaaa 124,216,248\n"
"Ra 216,23,18432\nMaaa 124,220,252\nRa 220,23,19456\nMaaa 120,212,252\nRa 252,22,3072\nRa 212,23,17408\nMaaa 120,208,248\nRa 248,22,2048\nRa 208,23,16384\nWL 0\nCvaa 32,128,192,4\n"
"Ra 160,24,8192\nMvaa 32,132,200\nMvaa 32,136,208\nRa 164,24,9216\nMvaa 32,140,216\nCvaa 36,140,220,8\nRa 168,24,10240\nMvaa 36,136,212\ns_add_u32 m0, s89, 32768\nMvaa 36,132,204\n"
"Ra 172,24,11264\ns_barrier\nMvaa 36,128,196\nMvaa 36,144,228\nRa 176,26,8192\nSv 96,1,16,76,0\nMvaa 36,148,236\ns_add_u32 s46, s86, s88\nMvaa 36,152,244\nRa 180,26,9216\n"
"Mvaa 36,156,252\nAR 92,0\nMvaa 32,156,248\nRa 184,26,10240\nAR 93,1\nMvaa 32,152,240\nD1 1,16,46,0\nMvaa 32,148,232\nRa 188,26,11264\nWL 4\nMvaa 32,144,224\nCV 96,92,93\n"
"Cvaa 40,160,192,12\nAR 92,2\nAR 93,3\nMvaa 40,164,200\nCV 97,92,93\nMvaa 40,168,208\nv_and_b32 v27, 0x70, v1\nbuffer_load_dword v27, v27, s[16:19], s61 offen offset:136 sc0 sc1\n"
"Mvaa 40,172,216\nAR 92,4\nCvaa 44,172,220,16\nAR 93,5\nCV 98,92,93\nMvaa 44,168,212\nD1 1,16,46,1024\nMvaa 44,164,204\nWL 0\nMvaa 44,160,196\nAR 92,6\nMvaa 44,176,228\n"
"Sv 100,1,16,76,1024\nAR 93,7\nMvaa 44,180,236\ns_add_u32 s88, s88, 32768\nEX 80,32\nMvaa 44,184,244\nCV 99,92,93\nAR 92,8\nMvaa 44,188,252\nEX 81,33\nMvaa 40,188,248\nAR 93,9\n"
"CV 100,92,93\nMvaa 40,184,240\nD1 1,16,46,2048\nMvaa 40,180,232\nAR 92,10\nAR 93,11\nMvaa 40,176,224\nAD 84,80,81\nCV 64,80,81\nCV 101,92,93\nAR 92,12\nEX 82,34\nAR 93,13\n"
"CV 102,92,93\nWV 16\nEX 83,35\nAD 84,84,82\nAR 92,14\nD1 1,16,46,3072\ns_add_u32 m0, s89, 49152\ns_barrier\nAR 93,15\nAD 84,84,83\nCV 65,82,83\nRa 128,24,32768\n"
"Sv 104,1,16,76,2048\ns_add_u32 s46, s87, s88\nEX 80,36\nRa 132,24,33792\nCV 103,92,93\nEX 81,37\nRa 136,24,34816\nAR 92,16\nAR 93,17\nD1 1,16,46,0\nCV 104,92,93\nRa 140,24,35840\n"
"AR 92,18\nAD 85,80,81\nCV 68,80,81\nRa 144,26,32768\nAR 93,19\nCV 105,92,93\nEX 82,38\nRa 148,26,33792\nAR 92,20\nEX 83,39\nRa 152,26,34816\nAD 85,85,82\nAR 93,21\n"
"D1 1,16,46,1024\nCV 106,92,93\nRa 156,26,35840\nAR 92,22\nAD 85,85,83\nCV 69,82,83\nRa 160,24,40960\nSv 108,1,16,76,3072\nAR 93,23\ns_xor_b32 s89, s89, 0x10000\nEX 80,40\n"
"Ra 164,24,41984\nCV 107,92,93\nAR 92,24\nWL 6\nEX 81,41\nRa 168,24,43008\nAD 84,84,80\nAR 93,25\nD1 1,16,46,2048\nCV 108,92,93\nRa 172,24,44032\nAR 92,26\nAR 93,27\nAD 84,84,81\n"
"CV 66,80,81\nCvaa 48,128,192,4\nRa 176,26,40960\nCV 109,92,93\nAR 92,28\nMvaa 48,132,200\nEX 82,42\nMvaa 48,136,208\nRa 180,26,41984\nAR 93,29\nMvaa 48,140,216\nWL 6\nEX 83,43\n"
"WV 9\nv_cmp_ne_u32 vcc, s15, v27\ns_nop 0\ns_cbranch_vccnz .S171\n.S172:\nCvaa 52,140,220,8\nRa 184,26,43008\nAD 84,84,82\nMvaa 52,136,212\nD1 1,16,46,3072\ns_add_u32 m0, s89, 0\n"
"CV 110,92,93\nMvaa 52,132,204\nRa 188,26,44032\nAR 92,30\nMvaa 52,128,196\nAD 84,84,83\nCV 67,82,83\nAR 93,31\nMvaa 52,144,228\nRa 128,2,49152\ns_add_u32 s76, s76, s55\n"
"Sv 112,1,16,76,0\nMvaa 52,148,236\ns_add_u32 s46, s86, s88\nEX 80,44\nMvaa 52,152,244\nRa 132,2,50176\nv_xor_b32 v24, 0x10000, v24\nCV 111,92,93\nMvaa 52,156,252\nWL 6\nEX 81,45\n"
"Mvaa 48,156,248\nRa 136,2,51200\nAD 85,85,80\nAR 92,32\nMvaa 48,152,240\nD1 1,16,46,0\nAR 93,33\nCV 112,92,93\nMvaa 48,148,232\nRa 140,2,52224\nAR 92,34\nMvaa 48,144,224\n"
"AD 85,85,81\nCV 70,80,81\nAR 93,35\nCvaa 56,160,192,12\nRa 144,2,53248\nCV 113,92,93\nAR 92,36\nMvaa 56,164,200\nEX 82,46\nMvaa 56,168,208\nRa 148,2,54272\n"
"v_xor_b32 v26, 0x10000, v26\nAR 93,37\nMvaa 56,172,216\nWL 6\nEX 83,47\nCvaa 60,172,220,16\nRa 152,2,55296\nAD 85,85,82\nCV 114,92,93\nMvaa 60,168,212\nD1 1,16,46,1024\nAR 92,38\n"
"AR 93,39\nMvaa 60,164,204\nRa 156,2,56320\nCV 115,92,93\nMvaa 60,160,196\nAD 85,85,83\nCV 71,82,83\nMvaa 60,176,228\nRa 160,2,57344\nv_max_f32 v90, v84, v85\nSv 116,1,16,76,1024\n"
"Mvaa 60,180,236\ns_add_u32 s88, s88, 32768\nEX 80,48\nMvaa 60,184,244\nRa 164,2,58368\nv_cmp_lt_f32 vcc, s63, v90\nMvaa 60,188,252\nWL 8\nEX 81,49\nMvaa 56,188,248\n"
"Ra 168,2,59392\ns_cbranch_vccnz .T153\n.T154:\nv_mov_b32 v20, v84\nv_mov_b32 v21, v85\nv_mov_b32 v22, v20\nv_mov_b32 v23, v21\nMvaa 56,184,240\nD1 1,16,46,2048\n"
"v_permlane32_swap_b32 v20, v22\nv_permlane32_swap_b32 v21, v23\nAD 20,20,22\nAD 21,21,23\nv_mov_b32 v22, v20\nv_mov_b32 v23, v21\nMvaa 56,180,232\nRa 172,2,60416\n"
"v_permlane16_swap_b32 v20, v22\nv_permlane16_swap_b32 v21, v23\nAD 20,20,22\nAD 21,21,23\nv_min_f32 v90, v20, v21\nv_cmp_gt_f32 vcc, s64, v90\ns_nop 0\ns_cbranch_vccnz .T155\n"
".T156:\nAD 86,86,84\nMvaa 56,176,224\nAD 84,80,81\nCV 72,80,81\nAR 92,40\nZvav 128,128,64\nRa 176,2,61440\nAR 93,41\nCV 116,92,93\nZvav 132,128,68\nEX 82,50\nAR 92,42\n"
"Zvav 140,132,68\nRa 180,2,62464\nAR 93,43\nCV 117,92,93\nZvav 136,132,64\nWL 8\nEX 83,51\nZvav 144,136,64\nRa 184,2,63488\nAD 84,84,82\nAR 92,44\nZvav 148,136,68\nD1 1,16,46,3072\n"
"s_add_u32 m0, s89, 16384\nAR 93,45\nZvav 156,140,68\nRa 188,2,64512\ns_barrier\nCV 118,92,93\nZvav 152,140,64\nAD 84,84,83\nCV 73,82,83\nAR 92,46\nZvav 160,144,64\nRa 128,24,0\n"
"Sv 120,1,16,76,2048\nAR 93,47\nZvav 164,144,68\ns_add_u32 s46, s87, s88\nEX 80,52\nbuffer_store_dword v25, off, s[16:19], s54 offset:12 sc0 sc1\nZvav 172,148,68\nRa 132,24,1024\n"
"v_xor_b32 v2, 0x10000, v2\nCV 119,92,93\nZvav 168,148,64\nWL 8\nEX 81,53\nZvav 176,152,64\nRa 136,24,2048\nAD 87,87,85\nZvav 180,152,68\nD1 1,16,46,0\nAR 92,48\nAR 93,49\n"
"Zvav 188,156,68\nRa 140,24,3072\nCV 120,92,93\nZvav 184,156,64\nAD 85,80,81\nCV 76,80,81\nZvav 192,160,64\nRa 144,26,0\nAR 92,50\nAR 93,51\nZvav 196,160,68\nEX 82,54\n"
"Zvav 204,164,68\nRa 148,26,1024\nCV 121,92,93\nZvav 200,164,64\nWL 8\nEX 83,55\nZvav 208,168,64\nRa 152,26,2048\nAD 85,85,82\nAR 92,52\nZvav 212,168,68\nD1 1,16,46,1024\nAR 93,53\n"
"Zvav 220,172,68\nRa 156,26,3072\nCV 122,92,93\nZvav 216,172,64\nAD 85,85,83\nCV 77,82,83\nAR 92,54\nZvav 224,176,64\nRa 160,24,8192\nSv 124,1,16,76,3072\nZvav 228,176,68\n"
"EX 80,56\nZvav 236,180,68\nRa 164,24,9216\nAR 93,55\nCV 123,92,93\nZvav 232,180,64\nWL 6\nEX 81,57\nZvav 240,184,64\nRa 168,24,10240\nAD 84,84,80\nAR 92,56\nZvav 244,184,68\n"
"D1 1,16,46,2048\nAR 93,57\nZvav 252,188,68\nRa 172,24,11264\nCV 124,92,93\nAR 92,58\nZvav 248,188,64\nAD 84,84,81\nCV 74,80,81\nCvaa 32,128,192,4\nRa 176,26,8192\nAR 93,59\n"
"CV 125,92,93\nMvaa 32,132,200\nEX 82,58\nMvaa 32,136,208\nRa 180,26,9216\nAR 92,60\nMvaa 32,140,216\nWL 6\nEX 83,59\nCvaa 36,140,220,8\nRa 184,26,10240\nAD 84,84,82\n"
"Mvaa 36,136,212\nD1 1,16,46,3072\ns_add_u32 m0, s89, 32768\nAR 93,61\nMvaa 36,132,204\nRa 188,26,11264\nCV 126,92,93\nMvaa 36,128,196\nAD 84,84,83\nCV 75,82,83\nAR 92,62\n"
"Mvaa 36,144,228\nRa 128,2,16384\nSv 96,1,16,77,0\nMvaa 36,148,236\ns_add_u32 s46, s86, s88\nEX 80,60\nMvaa 36,152,244\nRa 132,2,17408\nAR 93,63\nCV 127,92,93\nMvaa 36,156,252\n"
"WL 6\nEX 81,61\nMvaa 32,156,248\nRa 136,2,18432\nAD 85,85,80\nAR 92,64\nMvaa 32,152,240\nD1 1,16,46,0\nAR 93,65\nMvaa 32,148,232\nRa 140,2,19456\nCV 96,92,93\nAR 92,66\n"
"Mvaa 32,144,224\nAD 85,85,81\nCV 78,80,81\nCvaa 40,160,192,12\nRa 144,2,20480\nAR 93,67\nCV 97,92,93\nMvaa 40,164,200\nEX 82,62\nMvaa 40,168,208\nRa 148,2,21504\nAR 92,68\n"
"AR 93,69\nMvaa 40,172,216\nWL 6\nEX 83,63\nCvaa 44,172,220,16\nRa 152,2,22528\nAD 85,85,82\nCV 98,92,93\nMvaa 44,168,212\n.W561:\ns_cbranch_execz .W565\nD1 1,16,46,1024\nAR 92,70\n"
"Mvaa 44,164,204\nRa 156,2,23552\nAR 93,71\nCV 99,92,93\nMvaa 44,160,196\nAD 85,85,83\nCV 79,82,83\nMvaa 44,176,228\nRa 160,2,24576\nv_max_f32 v90, v84, v85\nSv 100,1,16,77,1024\n"
"Mvaa 44,180,236\ns_add_u32 s88, s88, 32768\nEX 80,32\nMvaa 44,184,244\nRa 164,2,25600\nv_cmp_lt_f32 vcc, s63, v90\nMvaa 44,188,252\nWL 8\nEX 81,33\nMvaa 40,188,248\n"
"Ra 168,2,26624\ns_cbranch_vccnz .T158\n.T159:\nAR 92,72\nMvaa 40,184,240\nD1 1,16,46,2048\nAR 93,73\nMvaa 40,180,232\nRa 172,2,27648\nAD 86,86,84\nCV 100,92,93\nMvaa 40,176,224\n"
"AD 84,80,81\nCV 64,80,81\nMvav 128,128,72\nRa 176,2,28672\nAR 92,74\nAR 93,75\nMvav 132,128,76\nEX 82,34\nMvav 140,132,76\nRa 180,2,29696\nCV 101,92,93\nAR 92,76\nMvav 136,132,72\n"
"s_waitcnt vmcnt(18) lgkmcnt(8)\nEX 83,35\nMvav 144,136,72\nRa 184,2,30720\nAD 84,84,82\nAR 93,77\nMvav 148,136,76\nD1 1,16,46,3072\ns_add_u32 m0, s89, 49152\nMvav 156,140,76\n"
"Ra 188,2,31744\ns_barrier\nCV 102,92,93\nMvav 152,140,72\nAD 84,84,83\nCV 65,82,83\nMvav 160,144,72\nRa 128,24,32768\nSv 104,1,16,77,2048\nAR 92,78\nMvav 164,144,76\n"
"s_add_u32 s46, s87, s88\nEX 80,36\nMvav 172,148,76\nRa 132,24,33792\nAR 93,79\nCV 103,92,93\nMvav 168,148,72\nWL 8\nEX 81,37\nMvav 176,152,72\nRa 136,24,34816\nAD 87,87,85\n"
"Mvav 180,152,76\nD1 1,16,46,0\nAR 92,80\nAR 93,81\nMvav 188,156,76\nRa 140,24,35840\nCV 104,92,93\nMvav 184,156,72\nAD 85,80,81\nCV 68,80,81\nMvav 192,160,72\nRa 144,26,32768\n"
"AR 92,82\nAR 93,83\nMvav 196,160,76\nEX 82,38\nMvav 204,164,76\nRa 148,26,33792\nCV 105,92,93\nMvav 200,164,72\nWL 8\nEX 83,39\nMvav 208,168,72\nRa 152,26,34816\nAD 85,85,82\n"
"AR 92,84\nMvav 212,168,76\nD1 1,16,46,1024\nAR 93,85\nMvav 220,172,76\nRa 156,26,35840\nCV 106,92,93\nMvav 216,172,72\nAD 85,85,83\nCV 69,82,83\nAR 92,86\nMvav 224,176,72\n"
"Ra 160,24,40960\nSv 108,1,16,77,3072\nMvav 228,176,76\ns_xor_b32 s89, s89, 0x10000\nEX 80,40\nMvav 236,180,76\nRa 164,24,41984\nAR 93,87\nCV 107,92,93\nMvav 232,180,72\nWL 6\n"
"EX 81,41\nMvav 240,184,72\nRa 168,24,43008\nAD 84,84,80\nAR 92,88\nMvav 244,184,76\nD1 1,16,46,2048\nAR 93,89\nCV 108,92,93\nMvav 252,188,76\nRa 172,24,44032\nAR 92,90\n"
"Mvav 248,188,72\nAD 84,84,81\nCV 66,80,81\nAR 93,91\nCvaa 48,128,192,4\nRa 176,26,40960\nCV 109,92,93\nAR 92,92\nMvaa 48,132,200\nEX 82,42\nMvaa 48,136,208\nRa 180,26,41984\n"
"AR 93,93\nCV 110,92,93\nMvaa 48,140,216\nWL 6\nEX 83,43\nCvaa 52,140,220,8\nRa 184,26,43008\nAD 84,84,82\nv_and_b32 v27, 0x70, v1\n"
"buffer_load_dword v27, v27, s[16:19], s61 offen offset:140 sc0 sc1\nMvaa 52,136,212\nD1 1,16,46,3072\ns_add_u32 m0, s89, 0\nAR 92,94\nMvaa 52,132,204\nRa 188,26,44032\nAR 93,95\n"
"Mvaa 52,128,196\nAD 84,84,83\nCV 67,82,83\nCV 111,92,93\nMvaa 52,144,228\nRa 128,2,49152\ns_add_u32 s77, s77, s55\nSv 112,1,16,77,0\nMvaa 52,148,236\ns_add_u32 s46, s86, s88\n"
"EX 80,44\nMvaa 52,152,244\nRa 132,2,50176\nv_xor_b32 v24, 0x10000, v24\nAR 92,96\nMvaa 52,156,252\nWL 6\nEX 81,45\nMvaa 48,156,248\nRa 136,2,51200\nAD 85,85,80\nAR 93,97\n"
"Mvaa 48,152,240\nD1 1,16,46,0\nCV 112,92,93\nMvaa 48,148,232\nRa 140,2,52224\nAR 92,98\nAR 93,99\nMvaa 48,144,224\nAD 85,85,81\nCV 70,80,81\nCvaa 56,160,192,12\nRa 144,2,53248\n"
"CV 113,92,93\nAR 92,100\nMvaa 56,164,200\nEX 82,46\nMvaa 56,168,208\nRa 148,2,54272\nv_xor_b32 v26, 0x10000, v26\nAR 93,101\nMvaa 56,172,216\nWL 6\nEX 83,47\nCvaa 60,172,220,16\n"
"Ra 152,2,55296\nAD 85,85,82\nCV 114,92,93\nMvaa 60,168,212\nD1 1,16,46,1024\nAR 92,102\nMvaa 60,164,204\nRa 156,2,56320\nAR 93,103\nCV 115,92,93\nMvaa 60,160,196\nAD 85,85,83\n"
"CV 71,82,83\nMvaa 60,176,228\nRa 160,2,57344\nv_max_f32 v90, v84, v85\nSv 116,1,16,77,1024\nMvaa 60,180,236\ns_add_u32 s88, s88, 32768\nEX 80,48\nMvaa 60,184,244\nRa 164,2,58368\n"
"v_cmp_lt_f32 vcc, s63, v90\nMvaa 60,188,252\nWL 8\nEX 81,49\nMvaa 56,188,248\nRa 168,2,59392\ns_cbranch_vccnz .T160\n.T161:\nAR 92,104\nMvaa 56,184,240\nD1 1,16,46,2048\n"
"AR 93,105\nCV 116,92,93\nMvaa 56,180,232\nRa 172,2,60416\nAD 86,86,84\nMvaa 56,176,224\nAD 84,80,81\nCV 72,80,81\nAR 92,106\nMvav 128,128,64\nRa 176,2,61440\nAR 93,107\n"
"CV 117,92,93\nMvav 132,128,68\nEX 82,50\nMvav 140,132,68\nRa 180,2,62464\nAR 92,108\nAR 93,109\nMvav 136,132,64\ns_waitcnt vmcnt(18) lgkmcnt(8)\nEX 83,51\nMvav 144,136,64\n"
"Ra 184,2,63488\nAD 84,84,82\nCV 118,92,93\nMvav 148,136,68\nD1 1,16,46,3072\ns_add_u32 m0, s89, 16384\nAR 92,110\nMvav 156,140,68\nRa 188,2,64512\ns_barrier\nMvav 152,140,64\n"
"AD 84,84,83\nCV 73,82,83\nAR 93,111\nMvav 160,144,64\nRa 128,24,0\nSv 120,1,16,77,2048\nCV 119,92,93\nMvav 164,144,68\ns_add_u32 s46, s87, s88\nEX 80,52\nMvav 172,148,68\n"
"Ra 132,24,1024\nv_xor_b32 v2, 0x10000, v2\nAR 92,112\nMvav 168,148,64\nWL 8\nEX 81,53\nMvav 176,152,64\nRa 136,24,2048\nAD 87,87,85\n"
"buffer_store_dword v25, off, s[16:19], s54 offset:8 sc0 sc1\nMvav 180,152,68\nD1 1,16,46,0\nAR 93,113\nCV 120,92,93\nMvav 188,156,68\nRa 140,24,3072\nAR 92,114\nMvav 184,156,64\n"
"AD 85,80,81\nCV 76,80,81\nMvav 192,160,64\nRa 144,26,0\nAR 93,115\nCV 121,92,93\nMvav 196,160,68\nEX 82,54\nMvav 204,164,68\nRa 148,26,1024\nAR 92,116\nMvav 200,164,64\nWL 8\n"
"EX 83,55\nMvav 208,168,64\nRa 152,26,2048\nAD 85,85,82\nAR 93,117\nMvav 212,168,68\nD1 1,16,46,1024\nCV 122,92,93\nMvav 220,172,68\nRa 156,26,3072\nAR 92,118\nMvav 216,172,64\n"
"AD 85,85,83\nCV 77,82,83\nAR 93,119\nMvav 224,176,64\nRa 160,24,8192\nSv 124,1,16,77,3072\nMvav 228,176,68\nEX 80,56\nMvav 236,180,68\nRa 164,24,9216\nCV 123,92,93\nAR 92,120\n"
"Mvav 232,180,64\nWL 6\nEX 81,57\nMvav 240,184,64\nRa 168,24,10240\nAD 84,84,80\nMvav 244,184,68\nD1 1,16,46,2048\nAR 93,121\nCV 124,92,93\nMvav 252,188,68\nRa 172,24,11264\n"
"AR 92,122\nMvav 248,188,64\nAD 84,84,81\nCV 74,80,81\nCvaa 32,128,192,4\nRa 176,26,8192\nAR 93,123\nCV 125,92,93\nMvaa 32,132,200\ns_add_u32 s49, s49, 131072\nEX 82,58\n"
"Mvaa 32,136,208\nRa 180,26,9216\nAR 92,124\nMvaa 32,140,216\nWL 6\nEX 83,59\nCvaa 36,140,220,8\nRa 184,26,10240\nAD 84,84,82\nAR 93,125\nMvaa 36,136,212\nD1 1,16,46,3072\n"
"s_add_u32 m0, s89, 32768\nMvaa 36,132,204\nRa 188,26,11264\nCV 126,92,93\nMvaa 36,128,196\nAD 84,84,83\nCV 75,82,83\nAR 92,126\nMvaa 36,144,228\nRa 128,2,16384\nSv 96,1,16,78,0\n"
"Mvaa 36,148,236\ns_add_u32 s46, s86, s88\nEX 80,60\nMvaa 36,152,244\nRa 132,2,17408\nAR 93,127\nCV 127,92,93\nMvaa 36,156,252\nWL 6\nEX 81,61\nMvaa 32,156,248\nRa 136,2,18432\n"
"AD 85,85,80\nMvaa 32,152,240\nD1 1,16,46,0\nMvaa 32,148,232\nRa 140,2,19456\nMvaa 32,144,224\nAD 85,85,81\nCV 78,80,81\nCvaa 40,160,192,12\nRa 144,2,20480\nMvaa 40,164,200\n"
"Sv 92,1,16,49,0\nEX 82,62\nMvaa 40,168,208\nRa 148,2,21504\nMvaa 40,172,216\nWL 6\nEX 83,63\nCvaa 44,172,220,16\nRa 152,2,22528\nAD 85,85,82\nMvaa 44,168,212\nD1 1,16,46,1024\n"
"Mvaa 44,164,204\nRa 156,2,23552\nMvaa 44,160,196\nAD 85,85,83\nCV 79,82,83\nMvaa 44,176,228\nRa 160,2,24576\nv_max_f32 v90, v84, v85\nSv 100,1,16,78,1024\nMvaa 44,180,236\n"
"s_add_u32 s88, s88, 32768\nEX 80,32\nMvaa 44,184,244\nRa 164,2,25600\nv_cmp_lt_f32 vcc, s63, v90\nMvaa 44,188,252\nWL 8\nEX 81,33\nMvaa 40,188,248\nRa 168,2,26624\n"
"s_cbranch_vccnz .T163\n.T164:\nMvaa 40,184,240\nD1 1,16,46,2048\nMvaa 40,180,232\nRa 172,2,27648\nAD 86,86,84\nMvaa 40,176,224\nAD 84,80,81\nCV 64,80,81\nMvav 128,128,72\n"
"Ra 176,2,28672\nMvav 132,128,76\nEX 82,34\nMvav 140,132,76\nRa 180,2,29696\nMvav 136,132,72\ns_waitcnt vmcnt(19) lgkmcnt(8)\nEX 83,35\nMvav 144,136,72\nRa 184,2,30720\n"
"AD 84,84,82\nMvav 148,136,76\nD1 1,16,46,3072\ns_add_u32 m0, s89, 49152\nMvav 156,140,76\nRa 188,2,31744\ns_barrier\nMvav 152,140,72\nAD 84,84,83\nCV 65,82,83\nMvav 160,144,72\n"
"Ra 128,24,32768\nSv 104,1,16,78,2048\nMvav 164,144,76\ns_add_u32 s46, s87, s88\nEX 80,36\nMvav 172,148,76\nRa 132,24,33792\nMvav 168,148,72\nWL 8\nEX 81,37\nMvav 176,152,72\n"
"Ra 136,24,34816\nAD 87,87,85\nMvav 180,152,76\nD1 1,16,46,0\nMvav 188,156,76\nRa 140,24,35840\nMvav 184,156,72\nAD 85,80,81\nCV 68,80,81\nMvav 192,160,72\nRa 144,26,32768\n"
"Mvav 196,160,76\nEX 82,38\nMvav 204,164,76\nRa 148,26,33792\nMvav 200,164,72\nWL 8\nEX 83,39\nMvav 208,168,72\nRa 152,26,34816\nAD 85,85,82\nMvav 212,168,76\nD1 1,16,46,1024\n"
"Mvav 220,172,76\nRa 156,26,35840\nMvav 216,172,72\nAD 85,85,83\nCV 69,82,83\nMvav 224,176,72\nRa 160,24,40960\nSv 108,1,16,78,3072\nMvav 228,176,76\ns_xor_b32 s89, s89, 0x10000\n"
"EX 80,40\nMvav 236,180,76\nRa 164,24,41984\nMvav 232,180,72\nWL 6\nEX 81,41\nMvav 240,184,72\nRa 168,24,43008\nAD 84,84,80\nMvav 244,184,76\nD1 1,16,46,2048\nMvav 252,188,76\n"
"Ra 172,24,44032\nMvav 248,188,72\nAD 84,84,81\nCV 66,80,81\nCvaa 48,128,192,4\nRa 176,26,40960\nMvaa 48,132,200\nEX 82,42\nMvaa 48,136,208\nRa 180,26,41984\nMvaa 48,140,216\nWL 6\n"
"EX 83,43\nv_cmp_ne_u32 vcc, s15, v27\ns_nop 0\ns_cbranch_vccnz .S174\n.S175:\nv_and_b32 v27, 0x70, v1\nbuffer_load_dword v27, v27, s[16:19], s61 offen offset:12 sc0 sc1\n"
"Cvaa 52,140,220,8\nRa 184,26,43008\nAD 84,84,82\nMvaa 52,136,212\nD1 1,16,46,3072\ns_add_u32 m0, s89, 0\nMvaa 52,132,204\nRa 188,26,44032\nMvaa 52,128,196\nAD 84,84,83\n"
"CV 67,82,83\nMvaa 52,144,228\nRa 128,2,49152\ns_add_u32 s78, s78, s55\nSv 112,1,16,78,0\nMvaa 52,148,236\ns_add_u32 s46, s86, s88\nEX 80,44\nMvaa 52,152,244\nRa 132,2,50176\n"
"v_xor_b32 v24, 0x10000, v24\nMvaa 52,156,252\nWL 6\nEX 81,45\nMvaa 48,156,248\nRa 136,2,51200\nAD 85,85,80\nMvaa 48,152,240\n.W562:\ns_cbranch_execz .W566\nD1 1,16,46,0\n"
"Mvaa 48,148,232\nRa 140,2,52224\nMvaa 48,144,224\nAD 85,85,81\nCV 70,80,81\nCvaa 56,160,192,12\nRa 144,2,53248\nMvaa 56,164,200\nSv 92,1,16,49,1024\nEX 82,46\nMvaa 56,168,208\n"
"Ra 148,2,54272\nv_xor_b32 v26, 0x10000, v26\nMvaa 56,172,216\nWL 6\nEX 83,47\nCvaa 60,172,220,16\nRa 152,2,55296\nAD 85,85,82\nMvaa 60,168,212\nD1 1,16,46,1024\nMvaa 60,164,204\n"
"Ra 156,2,56320\nMvaa 60,160,196\nAD 85,85,83\nCV 71,82,83\nMvaa 60,176,228\nRa 160,2,57344\nv_max_f32 v90, v84, v85\nSv 116,1,16,78,1024\nMvaa 60,180,236\n"
"s_add_u32 s88, s88, 32768\nEX 80,48\nMvaa 60,184,244\nRa 164,2,58368\nv_cmp_lt_f32 vcc, s63, v90\nMvaa 60,188,252\nWL 8\nEX 81,49\nMvaa 56,188,248\nRa 168,2,59392\n"
"s_cbranch_vccnz .T165\n.T166:\nMvaa 56,184,240\nD1 1,16,46,2048\nMvaa 56,180,232\nRa 172,2,60416\nAD 86,86,84\nMvaa 56,176,224\nAD 84,80,81\nCV 72,80,81\nMvav 128,128,64\n"
"Ra 176,2,61440\nMvav 132,128,68\nEX 82,50\nMvav 140,132,68\nRa 180,2,62464\nMvav 136,132,64\ns_waitcnt vmcnt(20) lgkmcnt(8)\nEX 83,51\nMvav 144,136,64\nRa 184,2,63488\n"
"AD 84,84,82\nMvav 148,136,68\nD1 1,16,46,3072\ns_add_u32 m0, s89, 16384\nMvav 156,140,68\nRa 188,2,64512\ns_barrier\nMvav 152,140,64\nAD 84,84,83\nCV 73,82,83\nMvav 160,144,64\n"
"Ra 128,24,0\nSv 120,1,16,78,2048\nMvav 164,144,68\ns_add_u32 s46, s87, s88\nEX 80,52\nMvav 172,148,68\nRa 132,24,1024\nv_xor_b32 v2, 0x10000, v2\nMvav 168,148,64\nWL 8\nEX 81,53\n"
"Mvav 176,152,64\nRa 136,24,2048\nAD 87,87,85\nMvav 180,152,68\nD1 1,16,46,0\nMvav 188,156,68\nRa 140,24,3072\nMvav 184,156,64\nAD 85,80,81\nCV 76,80,81\nMvav 192,160,64\n"
"Ra 144,26,0\nMvav 196,160,68\nEX 82,54\nMvav 204,164,68\nRa 148,26,1024\nMvav 200,164,64\nWL 8\nEX 83,55\nMvav 208,168,64\nRa 152,26,2048\nAD 85,85,82\nMvav 212,168,68\n"
"D1 1,16,46,1024\nMvav 220,172,68\nRa 156,26,3072\nMvav 216,172,64\nAD 85,85,83\nCV 77,82,83\nMvav 224,176,64\nRa 160,24,8192\nSv 124,1,16,78,3072\nMvav 228,176,68\nEX 80,56\n"
"Mvav 236,180,68\nRa 164,24,9216\nMvav 232,180,64\nWL 6\nEX 81,57\nMvav 240,184,64\nRa 168,24,10240\nAD 84,84,80\nMvav 244,184,68\nD1 1,16,46,2048\nMvav 252,188,68\n"
"Ra 172,24,11264\nMvav 248,188,64\nAD 84,84,81\nCV 74,80,81\ns_mov_b32 s99, 11\ns_mov_b32 s90, 0\n.p2align 6\n.A190:\nCvaa 32,128,192,4\nRa 176,26,8192\nMvaa 32,132,200\n"
"s_add_u32 s49, s49, 131072\nEX 82,58\nMvaa 32,136,208\nRa 180,26,9216\nMvaa 32,140,216\nWL 6\nEX 83,59\nCvaa 36,140,220,8\nRa 184,26,10240\nAD 84,84,82\nMvaa 36,136,212\n"
"D1 1,16,46,3072\ns_add_u32 m0, s89, 32768\nMvaa 36,132,204\nRa 188,26,11264\nMvaa 36,128,196\nAD 84,84,83\nCV 75,82,83\nMvaa 36,144,228\nRa 128,2,16384\nMvaa 36,148,236\n"
"s_add_u32 s46, s86, s88\nEX 80,60\nMvaa 36,152,244\nRa 132,2,17408\nMvaa 36,156,252\nWL 6\nEX 81,61\nMvaa 32,156,248\nRa 136,2,18432\nAD 85,85,80\nMvaa 32,152,240\nD1 1,16,46,0\n"
"Mvaa 32,148,232\nRa 140,2,19456\nMvaa 32,144,224\nAD 85,85,81\nCV 78,80,81\nCvaa 40,160,192,12\nRa 144,2,20480\nMvaa 40,164,200\nSv 92,1,16,49,0\nEX 82,62\nMvaa 40,168,208\n"
"Ra 148,2,21504\nMvaa 40,172,216\nWL 6\nEX 83,63\nCvaa 44,172,220,16\nRa 152,2,22528\nAD 85,85,82\nMvaa 44,168,212\nD1 1,16,46,1024\nMvaa 44,164,204\nRa 156,2,23552\n"
"Mvaa 44,160,196\nAD 85,85,83\nCV 79,82,83\nMvaa 44,176,228\nRa 160,2,24576\nv_max_f32 v90, v84, v85\nMvaa 44,180,236\ns_add_u32 s88, s88, 32768\nEX 80,32\nMvaa 44,184,244\n"
"Ra 164,2,25600\nv_cmp_lt_f32 vcc, s63, v90\nMvaa 44,188,252\nWL 8\nEX 81,33\nMvaa 40,188,248\nRa 168,2,26624\ns_cbranch_vccnz .T194\n.T195:\nMvaa 40,184,240\nD1 1,16,46,2048\n"
"Mvaa 40,180,232\nRa 172,2,27648\nAD 86,86,84\nMvaa 40,176,224\nAD 84,80,81\nCV 64,80,81\nMvav 128,128,72\nRa 176,2,28672\nMvav 132,128,76\nEX 82,34\nMvav 140,132,76\n"
"Ra 180,2,29696\nMvav 136,132,72\ns_waitcnt vmcnt(13) lgkmcnt(8)\nEX 83,35\nMvav 144,136,72\nRa 184,2,30720\nAD 84,84,82\nMvav 148,136,76\nD1 1,16,46,3072\n"
"s_add_u32 m0, s89, 49152\nMvav 156,140,76\nRa 188,2,31744\ns_barrier\nMvav 152,140,72\nAD 84,84,83\nCV 65,82,83\nMvav 160,144,72\nRa 128,24,32768\nMvav 164,144,76\n"
"s_add_u32 s46, s87, s88\nEX 80,36\nMvav 172,148,76\nRa 132,24,33792\nMvav 168,148,72\nWL 8\nEX 81,37\nMvav 176,152,72\nRa 136,24,34816\nAD 87,87,85\nMvav 180,152,76\nD1 1,16,46,0\n"
"Mvav 188,156,76\nRa 140,24,35840\nMvav 184,156,72\nAD 85,80,81\nCV 68,80,81\nMvav 192,160,72\nRa 144,26,32768\nMvav 196,160,76\nEX 82,38\nMvav 204,164,76\nRa 148,26,33792\n"
"Mvav 200,164,72\nWL 8\nEX 83,39\nMvav 208,168,72\nRa 152,26,34816\nAD 85,85,82\nMvav 212,168,76\nD1 1,16,46,1024\nMvav 220,172,76\nRa 156,26,35840\nMvav 216,172,72\nAD 85,85,83\n"
"CV 69,82,83\nMvav 224,176,72\nRa 160,24,40960\nMvav 228,176,76\ns_xor_b32 s89, s89, 0x10000\nEX 80,40\nMvav 236,180,76\nRa 164,24,41984\nMvav 232,180,72\nWL 6\nEX 81,41\n"
"Mvav 240,184,72\nRa 168,24,43008\nAD 84,84,80\nMvav 244,184,76\nD1 1,16,46,2048\nMvav 252,188,76\nRa 172,24,44032\nMvav 248,188,72\nAD 84,84,81\nCV 66,80,81\nCvaa 48,128,192,4\n"
"Ra 176,26,40960\nMvaa 48,132,200\nEX 82,42\nMvaa 48,136,208\nRa 180,26,41984\nMvaa 48,140,216\nWL 6\nEX 83,43\nCvaa 52,140,220,8\nRa 184,26,43008\nAD 84,84,82\nMvaa 52,136,212\n"
"D1 1,16,46,3072\ns_add_u32 m0, s89, 0\nMvaa 52,132,204\nRa 188,26,44032\nMvaa 52,128,196\nAD 84,84,83\nCV 67,82,83\nMvaa 52,144,228\nRa 128,2,49152\nMvaa 52,148,236\n"
"s_add_u32 s46, s86, s88\nEX 80,44\nMvaa 52,152,244\nRa 132,2,50176\nv_xor_b32 v24, 0x10000, v24\nMvaa 52,156,252\nWL 6\nEX 81,45\nMvaa 48,156,248\nRa 136,2,51200\nAD 85,85,80\n"
"Mvaa 48,152,240\nD1 1,16,46,0\nMvaa 48,148,232\nRa 140,2,52224\nMvaa 48,144,224\nAD 85,85,81\nCV 70,80,81\nCvaa 56,160,192,12\nRa 144,2,53248\nMvaa 56,164,200\nSv 92,1,16,49,1024\n"
"EX 82,46\nMvaa 56,168,208\nRa 148,2,54272\nv_xor_b32 v26, 0x10000, v26\nMvaa 56,172,216\nWL 6\nEX 83,47\nCvaa 60,172,220,16\nRa 152,2,55296\nAD 85,85,82\nMvaa 60,168,212\n"
"D1 1,16,46,1024\nMvaa 60,164,204\nRa 156,2,56320\nMvaa 60,160,196\nAD 85,85,83\nCV 71,82,83\nMvaa 60,176,228\nRa 160,2,57344\nv_max_f32 v90, v84, v85\nMvaa 60,180,236\n"
"s_add_u32 s88, s88, 32768\nEX 80,48\nMvaa 60,184,244\nRa 164,2,58368\nv_cmp_lt_f32 vcc, s63, v90\nMvaa 60,188,252\nWL 8\nEX 81,49\nMvaa 56,188,248\nRa 168,2,59392\n"
"s_cbranch_vccnz .T196\n.T197:\nMvaa 56,184,240\nD1 1,16,46,2048\nMvaa 56,180,232\nRa 172,2,60416\nAD 86,86,84\nMvaa 56,176,224\nAD 84,80,81\nCV 72,80,81\nMvav 128,128,64\n"
"Ra 176,2,61440\nMvav 132,128,68\nEX 82,50\nMvav 140,132,68\nRa 180,2,62464\nMvav 136,132,64\ns_waitcnt vmcnt(13) lgkmcnt(8)\nEX 83,51\nMvav 144,136,64\nRa 184,2,63488\n"
"AD 84,84,82\nMvav 148,136,68\nD1 1,16,46,3072\ns_add_u32 m0, s89, 16384\nMvav 156,140,68\nRa 188,2,64512\ns_barrier\nMvav 152,140,64\nAD 84,84,83\nCV 73,82,83\nMvav 160,144,64\n"
"Ra 128,24,0\nMvav 164,144,68\ns_add_u32 s46, s87, s88\nEX 80,52\nMvav 172,148,68\nRa 132,24,1024\nv_xor_b32 v2, 0x10000, v2\nMvav 168,148,64\nWL 8\nEX 81,53\nMvav 176,152,64\n"
"Ra 136,24,2048\nAD 87,87,85\nMvav 180,152,68\nD1 1,16,46,0\nMvav 188,156,68\nRa 140,24,3072\nMvav 184,156,64\nAD 85,80,81\nCV 76,80,81\nMvav 192,160,64\nRa 144,26,0\n"
"Mvav 196,160,68\nEX 82,54\nMvav 204,164,68\nRa 148,26,1024\nMvav 200,164,64\nWL 8\nEX 83,55\nMvav 208,168,64\nRa 152,26,2048\nAD 85,85,82\nMvav 212,168,68\nD1 1,16,46,1024\n"
"Mvav 220,172,68\nRa 156,26,3072\nMvav 216,172,64\nAD 85,85,83\nCV 77,82,83\nMvav 224,176,64\nRa 160,24,8192\nMvav 228,176,68\ns_sub_u32 s90, s90, 1\nEX 80,56\nMvav 236,180,68\n"
"Ra 164,24,9216\ns_cbranch_scc1 .E192\n.E193:\nMvav 232,180,64\nWL 6\nEX 81,57\nMvav 240,184,64\nRa 168,24,10240\nAD 84,84,80\nMvav 244,184,68\nD1 1,16,46,2048\n"
"s_sub_u32 s99, s99, 1\nMvav 252,188,68\nRa 172,24,11264\nMvav 248,188,64\nAD 84,84,81\nCV 74,80,81\ns_cbranch_scc0 .A190\ns_branch .A198\n.E192:\ns_swappc_b64 s[80:81], s[8:9]\n"
"s_branch .E193\n.HD143:\nbuffer_store_dword v25, off, s[16:19], s54 offset:4 sc0 sc1\nv_cmp_ne_u32 vcc, s15, v27\ns_nop 0\ns_cbranch_vccnz .S177\n.S178:\nv_and_b32 v27, 0x70, v1\n"
"buffer_load_dword v27, v27, s[16:19], s61 offen offset:8 sc0 sc1\ns_mov_b32 s90, 1\ns_swappc_b64 s[8:9], s[80:81]\nv_cmp_ne_u32 vcc, s15, v27\ns_nop 0\ns_cbranch_vccnz .S180\n"
".S181:\nv_and_b32 v27, 0xf0, v1\nbuffer_load_dword v27, v27, s[16:19], s61 offen offset:4 sc0 sc1\ns_mov_b32 s90, 1\ns_swappc_b64 s[8:9], s[80:81]\nv_cmp_ne_u32 vcc, s15, v27\n"
"s_nop 0\ns_cbranch_vccnz .S183\n.S184:\ns_mov_b32 s90, s91\ns_swappc_b64 s[8:9], s[80:81]\ns_branch .FF186\n.A198:\nCvaa 32,128,192,4\nRa 176,26,8192\nMvaa 32,132,200\n"
"s_add_u32 s49, s49, 131072\nEX 82,58\nMvaa 32,136,208\nRa 180,26,9216\nMvaa 32,140,216\nWL 6\nEX 83,59\nCvaa 36,140,220,8\nRa 184,26,10240\nAD 84,84,82\nMvaa 36,136,212\n"
"D1 1,16,46,3072\ns_add_u32 m0, s89, 32768\nMvaa 36,132,204\nRa 188,26,11264\nMvaa 36,128,196\nAD 84,84,83\nCV 75,82,83\ns_mov_b32 s94, s96\ns_add_u32 s95, s96, 65536\n"
"v_and_b32 v108, 0xf0, v1\nMvaa 36,144,228\nRa 128,2,16384\nMvaa 36,148,236\ns_add_u32 s46, s86, s88\nEX 80,60\nMvaa 36,152,244\nRa 132,2,17408\nMvaa 36,156,252\nWL 6\nEX 81,61\n"
"Mvaa 32,156,248\nRa 136,2,18432\nAD 85,85,80\nMvaa 32,152,240\nD1 1,16,46,0\nMvaa 32,148,232\nRa 140,2,19456\nMvaa 32,144,224\nAD 85,85,81\nCV 78,80,81\nCvaa 40,160,192,12\n"
"Ra 144,2,20480\nMvaa 40,164,200\nSv 92,1,16,49,0\nEX 82,62\nMvaa 40,168,208\nRa 148,2,21504\nMvaa 40,172,216\nWL 6\nEX 83,63\nCvaa 44,172,220,16\nRa 152,2,22528\nAD 85,85,82\n"
"Mvaa 44,168,212\nD1 1,16,46,1024\nMvaa 44,164,204\nRa 156,2,23552\nMvaa 44,160,196\nAD 85,85,83\nCV 79,82,83\nMvaa 44,176,228\nRa 160,2,24576\nv_max_f32 v90, v84, v85\n"
"Mvaa 44,180,236\ns_add_u32 s88, s88, 32768\nEX 80,32\nMvaa 44,184,244\nRa 164,2,25600\nv_cmp_lt_f32 vcc, s63, v90\nMvaa 44,188,252\nWL 8\nEX 81,33\nMvaa 40,188,248\n"
"Ra 168,2,26624\ns_cbranch_vccnz .T201\n.T202:\nMvaa 40,184,240\nD1 1,16,46,2048\nMvaa 40,180,232\nRa 172,2,27648\nAD 86,86,84\nMvaa 40,176,224\nAD 84,80,81\nCV 64,80,81\n"
"Mvav 128,128,72\nRa 176,2,28672\nv_lshrrev_b32 v92, 8, v1\nMvav 132,128,76\nEX 82,34\nMvav 140,132,76\nRa 180,2,29696\nMvav 136,132,72\ns_waitcnt vmcnt(13) lgkmcnt(8)\nEX 83,35\n"
"Mvav 144,136,72\nRa 184,2,30720\nAD 84,84,82\nMvav 148,136,76\nD1 1,16,46,3072\ns_add_u32 m0, s89, 49152\nMvav 156,140,76\nRa 188,2,31744\ns_barrier\nMvav 152,140,72\nAD 84,84,83\n"
"CV 65,82,83\n.W563:\ns_cbranch_execz .WB151\nMvav 160,144,72\nRa 128,24,32768\nv_bfe_u32 v93, v1, 6, 2\nMvav 164,144,76\ns_add_u32 s46, s87, s88\nEX 80,36\nMvav 172,148,76\n"
"Ra 132,24,33792\nv_cmp_eq_u32 vcc, v92, v93\nv_and_b32 v92, 16, v1\nv_mov_b32 v93, 0x3f80\nv_cndmask_b32 v93, 0, v93, vcc\nMvav 168,148,72\nWL 8\nEX 81,37\nMvav 176,152,72\n"
"Ra 136,24,34816\nAD 87,87,85\nMvav 180,152,76\nD1 1,16,46,0\nMvav 188,156,76\nRa 140,24,35840\nv_lshlrev_b32 v93, v92, v93\nMvav 184,156,72\nAD 85,80,81\nCV 68,80,81\n"
"Mvav 192,160,72\nRa 144,26,32768\nv_and_b32 v92, 32, v1\nMvav 196,160,76\nEX 82,38\nMvav 204,164,76\nRa 148,26,33792\nv_cmp_eq_u32 vcc, 0, v92\nv_mov_b32 v112, 0\n"
"v_mov_b32 v113, 0\nv_cndmask_b32 v114, 0, v93, vcc\nMvav 200,164,72\nWL 8\nEX 83,39\nMvav 208,168,72\nRa 152,26,34816\nAD 85,85,82\nMvav 212,168,76\nD1 1,16,46,1024\n"
"Mvav 220,172,76\nRa 156,26,35840\nv_sub_u32 v115, v93, v114\nMvav 216,172,72\nAD 85,85,83\nCV 69,82,83\nMvav 224,176,72\nRa 160,24,40960\nv_mov_b32 v116, 0\nMvav 228,176,76\n"
"s_xor_b32 s89, s89, 0x10000\nEX 80,40\nMvav 236,180,76\nRa 164,24,41984\nMvav 232,180,72\nWL 6\nEX 81,41\nMvav 240,184,72\nRa 168,24,43008\nAD 84,84,80\nMvav 244,184,76\n"
"D1 1,16,46,2048\nMvav 252,188,76\nRa 172,24,44032\nv_mov_b32 v117, 0\nMvav 248,188,72\nAD 84,84,81\nCV 66,80,81\nCvaa 48,128,192,4\nRa 176,26,40960\nMvaa 48,132,200\n"
"s_mov_b32 s86, s94\nEX 82,42\nMvaa 48,136,208\nRa 180,26,41984\nMvaa 48,140,216\nWL 6\nEX 83,43\nCvaa 52,140,220,8\nRa 184,26,43008\nAD 84,84,82\nMvaa 52,136,212\nD1 1,16,46,3072\n"
"s_add_u32 m0, s89, 0\nMvaa 52,132,204\nRa 188,26,44032\nMvaa 52,128,196\nAD 84,84,83\nCV 67,82,83\nMvaa 52,144,228\nRa 128,2,49152\nMvaa 52,148,236\ns_add_u32 s46, s86, s88\n"
"EX 80,44\nMvaa 52,152,244\nRa 132,2,50176\nv_xor_b32 v24, 0x10000, v24\nMvaa 52,156,252\nWL 6\nEX 81,45\nMvaa 48,156,248\nRa 136,2,51200\nAD 85,85,80\nMvaa 48,152,240\n"
"D1 1,16,46,0\nMvaa 48,148,232\nRa 140,2,52224\nMvaa 48,144,224\nAD 85,85,81\nCV 70,80,81\nCvaa 56,160,192,12\nRa 144,2,53248\nMvaa 56,164,200\nSv 92,1,16,49,1024\nEX 82,46\n"
"Mvaa 56,168,208\nRa 148,2,54272\nv_xor_b32 v26, 0x10000, v26\nMvaa 56,172,216\nWL 6\nEX 83,47\nCvaa 60,172,220,16\nRa 152,2,55296\nAD 85,85,82\nMvaa 60,168,212\nD1 1,16,46,1024\n"
"Mvaa 60,164,204\nRa 156,2,56320\nMvaa 60,160,196\nAD 85,85,83\nCV 71,82,83\nMvaa 60,176,228\nRa 160,2,57344\nv_max_f32 v90, v84, v85\nMvaa 60,180,236\ns_add_u32 s88, s88, 32768\n"
"EX 80,48\nMvaa 60,184,244\nRa 164,2,58368\nv_cmp_lt_f32 vcc, s63, v90\nMvaa 60,188,252\nWL 8\nEX 81,49\nMvaa 56,188,248\nRa 168,2,59392\ns_cbranch_vccnz .T203\n.T204:\n"
"Mvaa 56,184,240\nD1 1,16,46,2048\nMvaa 56,180,232\nRa 172,2,60416\nAD 86,86,84\nMvaa 56,176,224\nAD 84,80,81\nCV 72,80,81\nMvav 128,128,64\nRa 176,2,61440\nMvav 132,128,68\n"
"EX 82,50\nMvav 140,132,68\nRa 180,2,62464\nMvav 136,132,64\ns_waitcnt vmcnt(13) lgkmcnt(8)\nEX 83,51\nMvav 144,136,64\nRa 184,2,63488\nAD 84,84,82\nMvav 148,136,68\n"
"D1 1,16,46,3072\ns_add_u32 m0, s89, 16384\nMvav 156,140,68\nRa 188,2,64512\ns_barrier\nMvav 152,140,64\nAD 84,84,83\nCV 73,82,83\nMvav 160,144,64\nRa 128,24,0\nMvav 164,144,68\n"
"s_add_u32 s46, s87, s88\nEX 80,52\nMvav 172,148,68\nRa 132,24,1024\nv_xor_b32 v2, 0x10000, v2\nMvav 168,148,64\nWL 8\nEX 81,53\nMvav 176,152,64\nRa 136,24,2048\nAD 87,87,85\n"
"Mvav 180,152,68\nD1 1,16,46,0\nMvav 188,156,68\nRa 140,24,3072\nMvav 184,156,64\nAD 85,80,81\nCV 76,80,81\nMvav 192,160,64\nRa 144,26,0\nMvav 196,160,68\ns_cmp_lg_u32 s92, 0\n"
"EX 82,54\nMvav 204,164,68\nRa 148,26,1024\ns_cbranch_scc0 .E199\nv_cmpx_gt_i32 vcc, 19, v3\nv_mov_b32 v15, s97\nv_cmpx_gt_i32 vcc, 18, v3\nv_mov_b32 v14, s97\n"
"v_cmpx_gt_i32 vcc, 17, v3\nv_mov_b32 v13, s97\nv_cmpx_gt_i32 vcc, 16, v3\nv_mov_b32 v12, s97\nv_cmpx_gt_i32 vcc, 3, v3\nv_mov_b32 v7, s97\nv_cmpx_gt_i32 vcc, 2, v3\n"
"v_mov_b32 v6, s97\nv_cmpx_gt_i32 vcc, 1, v3\nv_mov_b32 v5, s97\nv_cmpx_gt_i32 vcc, 0, v3\nv_mov_b32 v4, s97\ns_mov_b64 exec, -1\ns_nop 2\n.E199:\ns_nop 0\nMvav 200,164,64\nWL 8\n"
"EX 83,55\nMvav 208,168,64\nRa 152,26,2048\nAD 85,85,82\nMvav 212,168,68\nD1 1,16,46,1024\nMvav 220,172,68\nRa 156,26,3072\nMvav 216,172,64\nAD 85,85,83\nCV 77,82,83\n"
"Mvav 224,176,64\nRa 160,24,8192\nMvav 228,176,68\nEX 80,56\nMvav 236,180,68\nRa 164,24,9216\nMvav 232,180,64\nWL 6\nEX 81,57\nMvav 240,184,64\nRa 168,24,10240\nAD 84,84,80\n"
"Mvav 244,184,68\nD1 1,16,46,2048\nMvav 252,188,68\nRa 172,24,11264\nMvav 248,188,64\nAD 84,84,81\nCV 74,80,81\nCvaa 32,128,192,4\nRa 176,26,8192\nMvaa 32,132,200\nEX 82,58\n"
"Mvaa 32,136,208\nRa 180,26,9216\nMvaa 32,140,216\nWL 6\nEX 83,59\nCvaa 36,140,220,8\nRa 184,26,10240\nAD 84,84,82\nMvaa 36,136,212\nD1 1,16,46,3072\ns_add_u32 m0, s89, 32768\n"
"Mvaa 36,132,204\nRa 188,26,11264\nMvaa 36,128,196\nAD 84,84,83\nCV 75,82,83\nMvaa 36,144,228\nRa 128,2,16384\nMvaa 36,148,236\ns_add_u32 s46, s86, s88\nEX 80,60\nMvaa 36,152,244\n"
"Ra 132,2,17408\nMvaa 36,156,252\nWL 6\nEX 81,61\nMvaa 32,156,248\nRa 136,2,18432\nAD 85,85,80\nMvaa 32,152,240\nD1 1,16,46,0\nMvaa 32,148,232\nRa 140,2,19456\nMvaa 32,144,224\n"
"AD 85,85,81\nCV 78,80,81\nCvaa 40,160,192,12\nRa 144,2,20480\nMvaa 40,164,200\ns_mov_b32 s87, s95\nEX 82,62\nMvaa 40,168,208\nRa 148,2,21504\nMvaa 40,172,216\nWL 6\nEX 83,63\n"
"Cvaa 44,172,220,16\nRa 152,2,22528\nAD 85,85,82\nMvaa 44,168,212\nD1 1,16,46,1024\nMvaa 44,164,204\nRa 156,2,23552\nMvaa 44,160,196\nAD 85,85,83\nCV 79,82,83\nMvaa 44,176,228\n"
"Ra 160,2,24576\nv_max_f32 v90, v84, v85\nMvaa 44,180,236\ns_add_u32 s88, s88, 32768\nEX 80,32\nMvaa 44,184,244\nRa 164,2,25600\nv_cmp_lt_f32 vcc, s63, v90\nMvaa 44,188,252\nWL 8\n"
"EX 81,33\nMvaa 40,188,248\nRa 168,2,26624\ns_cbranch_vccnz .T207\n.T208:\nMvaa 40,184,240\nD1 1,16,46,2048\nMvaa 40,180,232\nRa 172,2,27648\nAD 86,86,84\nMvaa 40,176,224\n"
"AD 84,80,81\nCV 64,80,81\nMvav 128,128,72\nRa 176,2,28672\nMvav 132,128,76\nEX 82,34\nMvav 140,132,76\nRa 180,2,29696\nMvav 136,132,72\ns_waitcnt vmcnt(12) lgkmcnt(8)\nEX 83,35\n"
"Mvav 144,136,72\nRa 184,2,30720\nAD 84,84,82\nMvav 148,136,76\nD1 1,16,46,3072\ns_add_u32 m0, s89, 49152\nMvav 156,140,76\nRa 188,2,31744\ns_barrier\nMvav 152,140,72\nAD 84,84,83\n"
"CV 65,82,83\nMvav 160,144,72\nRa 128,24,32768\nMvav 164,144,76\ns_add_u32 s46, s87, s88\nEX 80,36\nMvav 172,148,76\nRa 132,24,33792\nMvav 168,148,72\nWL 8\nEX 81,37\n"
"Mvav 176,152,72\nRa 136,24,34816\nAD 87,87,85\nMvav 180,152,76\nD1 1,16,46,0\nMvav 188,156,76\nRa 140,24,35840\nMvav 184,156,72\nAD 85,80,81\nCV 68,80,81\nMvav 192,160,72\n"
"Ra 144,26,32768\nbuffer_load_dwordx4 v[100:103], v108, s[16:19], s65 offen offset:0 sc0 sc1\nbuffer_load_dwordx4 v[104:107], v108, s[16:19], s65 offen offset:256 sc0 sc1\n"
"Mvav 196,160,76\ns_cmp_eq_u32 s13, 7\nEX 82,38\nMvav 204,164,76\nRa 148,26,33792\ns_cbranch_scc0 .E205\nv_cmpx_gt_i32 vcc, 19, v0\nv_mov_b32 v15, s97\nv_cmpx_gt_i32 vcc, 18, v0\n"
"v_mov_b32 v14, s97\nv_cmpx_gt_i32 vcc, 17, v0\nv_mov_b32 v13, s97\nv_cmpx_gt_i32 vcc, 16, v0\nv_mov_b32 v12, s97\nv_cmpx_gt_i32 vcc, 3, v0\nv_mov_b32 v7, s97\n"
"v_cmpx_gt_i32 vcc, 2, v0\nv_mov_b32 v6, s97\nv_cmpx_gt_i32 vcc, 1, v0\nv_mov_b32 v5, s97\nv_cmpx_gt_i32 vcc, 0, v0\nv_mov_b32 v4, s97\ns_mov_b64 exec, -1\ns_nop 2\n.E205:\n"
"s_nop 0\nMvav 200,164,72\nWL 8\nEX 83,39\nMvav 208,168,72\nRa 152,26,34816\nAD 85,85,82\nMvav 212,168,76\nD1 1,16,46,1024\nMvav 220,172,76\nRa 156,26,35840\nMvav 216,172,72\n"
"AD 85,85,83\nCV 69,82,83\nMvav 224,176,72\nRa 160,24,40960\nMvav 228,176,76\ns_xor_b32 s89, s89, 0x10000\nEX 80,40\nMvav 236,180,76\nRa 164,24,41984\nMvav 232,180,72\nWL 6\n"
"EX 81,41\nMvav 240,184,72\nRa 168,24,43008\nAD 84,84,80\nMvav 244,184,76\nD1 1,16,46,2048\nMvav 252,188,76\nRa 172,24,44032\nMvav 248,188,72\nAD 84,84,81\nCV 66,80,81\n"
"Cvaa 48,128,192,4\nRa 176,26,40960\nMvaa 48,132,200\nEX 82,42\nMvaa 48,136,208\nRa 180,26,41984\nMvaa 48,140,216\nWL 6\nEX 83,43\nCvaa 52,140,220,8\nRa 184,26,43008\nAD 84,84,82\n"
"Mvaa 52,136,212\nD1 1,16,46,3072\ns_add_u32 m0, s89, 0\nMvaa 52,132,204\nRa 188,26,44032\nMvaa 52,128,196\nAD 84,84,83\nCV 67,82,83\nMvaa 52,144,228\nRa 128,2,49152\nWV 3\n"
"v_xor_b32 v109, s15, v102\nv_xor_b32 v110, s15, v106\nv_or_b32 v109, v109, v110\nv_cmp_ne_u32 vcc, 0, v109\ns_nop 3\ns_cmp_lg_u64 vcc, 0\ns_cselect_b32 s67, 1, 0\n"
"s_cselect_b32 s6, 0x40000000, s66\nMvaa 52,148,236\ns_add_u32 s46, s86, s88\nEX 80,44\ns_add_u32 s5, s6, 0\nMvaa 52,152,244\nRa 132,2,50176\nv_xor_b32 v24, 0x10000, v24\n"
"Mvaa 52,156,252\nWL 6\nEX 81,45\nbuffer_load_dwordx4 a[0:3], v1, s[16:19], s5 offen offset:0 sc1\nMvaa 48,156,248\nRa 136,2,51200\nAD 85,85,80\nMvaa 48,152,240\nD1 1,16,46,0\n"
"Mvaa 48,148,232\nRa 140,2,52224\nbuffer_load_dwordx4 a[4:7], v1, s[16:19], s5 offen offset:1024 sc1\nMvaa 48,144,224\nAD 85,85,81\nCV 70,80,81\nCvaa 56,160,192,12\nRa 144,2,53248\n"
"Mvaa 56,164,200\nEX 82,46\nbuffer_load_dwordx4 a[8:11], v1, s[16:19], s5 offen offset:2048 sc1\nMvaa 56,168,208\nRa 148,2,54272\nv_xor_b32 v26, 0x10000, v26\nMvaa 56,172,216\n"
"WL 6\nEX 83,47\nCvaa 60,172,220,16\nRa 152,2,55296\nAD 85,85,82\nbuffer_load_dwordx4 a[12:15], v1, s[16:19], s5 offen offset:3072 sc1\nMvaa 60,168,212\nD1 1,16,46,1024\n"
"s_add_u32 s5, s6, 4096\nMvaa 60,164,204\nRa 156,2,56320\nMvaa 60,160,196\nAD 85,85,83\nCV 71,82,83\nbuffer_load_dwordx4 a[16:19], v1, s[16:19], s5 offen offset:0 sc1\n"
"Mvaa 60,176,228\nRa 160,2,57344\nv_max_f32 v90, v84, v85\nMvaa 60,180,236\ns_add_u32 s88, s88, 32768\nEX 80,48\nMvaa 60,184,244\nRa 164,2,58368\nv_cmp_lt_f32 vcc, s63, v90\n"
"Mvaa 60,188,252\nWL 8\nEX 81,49\nMvaa 56,188,248\nRa 168,2,59392\ns_cbranch_vccnz .T209\n.T210:\nbuffer_load_dwordx4 a[20:23], v1, s[16:19], s5 offen offset:1024 sc1\n"
"Mvaa 56,184,240\nD1 1,16,46,2048\nMvaa 56,180,232\nRa 172,2,60416\nAD 86,86,84\nMvaa 56,176,224\nAD 84,80,81\nCV 72,80,81\n"
"buffer_load_dwordx4 a[24:27], v1, s[16:19], s5 offen offset:2048 sc1\nMvav 128,128,64\nRa 176,2,61440\nMvav 132,128,68\nEX 82,50\nMvav 140,132,68\nRa 180,2,62464\n"
"buffer_load_dwordx4 a[28:31], v1, s[16:19], s5 offen offset:3072 sc1\nMvav 136,132,64\nWL 8\nEX 83,51\ns_add_u32 s5, s6, 16384\nMvav 144,136,64\nRa 184,2,63488\nAD 84,84,82\n"
"Mvav 148,136,68\nD1 1,16,46,3072\ns_add_u32 m0, s89, 16384\nbuffer_load_dwordx4 a[32:35], v1, s[16:19], s5 offen offset:0 sc1\nMvav 156,140,68\nRa 188,2,64512\ns_barrier\n"
"Mvav 152,140,64\nAD 84,84,83\nCV 73,82,83\nMvav 160,144,64\nRa 128,24,0\nbuffer_load_dwordx4 a[36:39], v1, s[16:19], s5 offen offset:1024 sc1\nMvav 164,144,68\n"
"s_add_u32 s46, s87, s88\nEX 80,52\nMvav 172,148,68\nRa 132,24,1024\nv_xor_b32 v2, 0x10000, v2\nMvav 168,148,64\nWL 8\nEX 81,53\n"
"buffer_load_dwordx4 a[40:43], v1, s[16:19], s5 offen offset:2048 sc1\nMvav 176,152,64\nRa 136,24,2048\nAD 87,87,85\nMvav 180,152,68\nD1 1,16,46,0\nMvav 188,156,68\nRa 140,24,3072\n"
"buffer_load_dwordx4 a[44:47], v1, s[16:19], s5 offen offset:3072 sc1\nMvav 184,156,64\nAD 85,80,81\nCV 76,80,81\ns_add_u32 s5, s6, 20480\nMvav 192,160,64\nRa 144,26,0\n"
"Mvav 196,160,68\nEX 82,54\nbuffer_load_dwordx4 a[48:51], v1, s[16:19], s5 offen offset:0 sc1\nMvav 204,164,68\nRa 148,26,1024\nMvav 200,164,64\nWL 8\nEX 83,55\nMvav 208,168,64\n"
"Ra 152,26,2048\nAD 85,85,82\nbuffer_load_dwordx4 a[52:55], v1, s[16:19], s5 offen offset:1024 sc1\nMvav 212,168,68\nD1 1,16,46,1024\nMvav 220,172,68\nRa 156,26,3072\n"
"Mvav 216,172,64\nAD 85,85,83\nCV 77,82,83\nbuffer_load_dwordx4 a[56:59], v1, s[16:19], s5 offen offset:2048 sc1\nMvav 224,176,64\n.W564:\ns_cbranch_execz .WB151\nRa 160,24,8192\n"
"Mvav 228,176,68\nEX 80,56\nMvav 236,180,68\nRa 164,24,9216\nbuffer_load_dwordx4 a[60:63], v1, s[16:19], s5 offen offset:3072 sc1\nMvav 232,180,64\nWL 10\nEX 81,57\n"
"Mvav 240,184,64\nRa 168,24,10240\nAD 84,84,80\nMvav 244,184,68\nD1 1,16,46,2048\nMvav 252,188,68\nRa 172,24,11264\nMvav 248,188,64\nAD 84,84,81\nCV 74,80,81\nWV 20\n"
"s_cmp_lg_u32 s67, 0\ns_cbranch_scc0 .E218\nv_xor_b32 v109, s15, v102\nv_xor_b32 v110, s15, v106\nv_or_b32 v109, v109, v110\nv_cmp_ne_u32 vcc, 0, v109\ns_nop 0\n"
"s_cbranch_vccnz .S215\n.S216:\ns_add_u32 s5, s66, 0\nbuffer_load_dwordx4 a[0:3], v1, s[16:19], s5 offen offset:0 sc1\n"
"buffer_load_dwordx4 a[4:7], v1, s[16:19], s5 offen offset:1024 sc1\nbuffer_load_dwordx4 a[8:11], v1, s[16:19], s5 offen offset:2048 sc1\n"
"buffer_load_dwordx4 a[12:15], v1, s[16:19], s5 offen offset:3072 sc1\ns_add_u32 s5, s66, 4096\nbuffer_load_dwordx4 a[16:19], v1, s[16:19], s5 offen offset:0 sc1\n"
"buffer_load_dwordx4 a[20:23], v1, s[16:19], s5 offen offset:1024 sc1\nbuffer_load_dwordx4 a[24:27], v1, s[16:19], s5 offen offset:2048 sc1\n"
"buffer_load_dwordx4 a[28:31], v1, s[16:19], s5 offen offset:3072 sc1\ns_add_u32 s5, s66, 16384\nbuffer_load_dwordx4 a[32:35], v1, s[16:19], s5 offen offset:0 sc1\n"
"buffer_load_dwordx4 a[36:39], v1, s[16:19], s5 offen offset:1024 sc1\nbuffer_load_dwordx4 a[40:43], v1, s[16:19], s5 offen offset:2048 sc1\n"
"buffer_load_dwordx4 a[44:47], v1, s[16:19], s5 offen offset:3072 sc1\ns_add_u32 s5, s66, 20480\nbuffer_load_dwordx4 a[48:51], v1, s[16:19], s5 offen offset:0 sc1\n"
"buffer_load_dwordx4 a[52:55], v1, s[16:19], s5 offen offset:1024 sc1\nbuffer_load_dwordx4 a[56:59], v1, s[16:19], s5 offen offset:2048 sc1\n"
"buffer_load_dwordx4 a[60:63], v1, s[16:19], s5 offen offset:3072 sc1\nWV 0\ns_mov_b32 s67, 0\n.E218:\nv_cmp_lt_f32 vcc, 0, v101\nSU 20,100,88\nv_mul_f32 v101, 0x3e800000, v101\n"
"v_cndmask_b32 v20, 0, v20, vcc\nv_cmp_lt_f32 vcc, 0, v105\nSU 21,104,89\nv_mul_f32 v105, 0x3e800000, v105\nv_cndmask_b32 v21, 0, v21, vcc\nv_or_b32 v91, v20, v21\n"
"v_cmp_ne_u32 vcc, 0, v91\ns_nop 0\ns_cbranch_vccnz .D213\nAD 86,86,101\nAD 87,87,105\n.D214:\nMvva 128,114,0\nMvva 136,112,0\nEX 82,58\nMvva 140,112,4\nMvva 132,114,4\nWV 15\n"
"EX 83,59\nMvva 144,114,8\nAD 84,84,82\nMvva 152,112,8\nD1 1,16,46,3072\nMvva 156,112,12\ns_add_u32 m0, s89, 32768\ns_add_u32 s46, s86, s88\nMvva 148,114,12\nAD 84,84,83\n"
"CV 75,82,83\nMvva 160,114,16\nRa 128,2,16384\nv_xor_b32 v3, 0x10000, v2\nv_add_u32 v0, 0x20000, v1\nMvva 168,112,16\nEX 80,60\nMvva 172,112,20\nRa 132,2,17408\n"
"s_sub_u32 s94, s49, 1703936\nMvva 164,114,20\nWV 10\nEX 81,61\nMvva 176,114,24\nRa 136,2,18432\nAD 85,85,80\ns_xor_b32 s92, s94, 0x202000\nMvva 184,112,24\nD1 1,16,46,0\n"
"s_lshl_b32 s89, s11, 12\nMvva 188,112,28\nRa 140,2,19456\ns_lshl1_add_u32 s2, s12, 0\nMvva 180,114,28\nAD 85,85,81\nCV 78,80,81\nMvva 192,114,32\nRa 144,2,20480\n"
"s_lshl3_add_u32 s2, s2, s13\ns_lshl4_add_u32 s2, s2, 0xc048200\nMvva 200,112,32\nEX 82,62\nMvva 204,112,36\nRa 148,2,21504\ns_and_b32 s3, s72, 0x2000\nMvva 196,114,36\nWV 6\n"
"EX 83,63\nMvva 208,114,40\nRa 152,2,22528\nAD 85,85,82\nMvva 216,112,40\nD1 1,16,46,1024\ns_cmp_eq_u32 s11, 1\ns_cselect_b32 s54, s2, s54\ns_cselect_b32 s3, s3, 0\n"
"Mvva 220,112,44\nRa 156,2,23552\nv_xor_b32 v25, s3, v25\nMvva 212,114,44\nAD 85,85,83\nCV 79,82,83\nMvva 224,114,48\nRa 160,2,24576\nv_max_f32 v90, v84, v85\nMvva 232,112,48\n"
"v_cmp_lt_f32 vcc, s63, v90\nMvva 236,112,52\nRa 164,2,25600\nMvva 228,114,52\ns_waitcnt vmcnt(4) lgkmcnt(8)\nMvva 240,114,56\nRa 168,2,26624\nMvva 248,112,56\nD1 1,16,46,2048\n"
"Mvva 252,112,60\nRa 172,2,27648\nMvva 244,114,60\ns_cbranch_vccnz .T219\n.T220:\nAD 86,86,84\nAD 87,87,85\nMvav 128,128,72\nRa 176,2,28672\nv_mov_b32 v84, v86\nv_mov_b32 v85, v87\n"
"Mvav 132,128,76\nv_permlane32_swap_b32 v86, v84\nv_permlane32_swap_b32 v87, v85\nMvav 140,132,76\nRa 180,2,29696\nAD 86,86,84\nMvav 136,132,72\nWL 8\nAD 87,87,85\nMvav 144,136,72\n"
"Ra 184,2,30720\nv_mov_b32 v84, v86\nv_mov_b32 v85, v87\nMvav 148,136,76\nD1 1,16,46,3072\nv_permlane16_swap_b32 v86, v84\nMvav 156,140,76\nRa 188,2,31744\n"
"v_permlane16_swap_b32 v87, v85\nMvav 152,140,72\nAD 86,86,84\nAD 87,87,85\nMvav 160,144,72\nRa 128,2,0\nv_rcp_f32 v86, v86\nMvav 164,144,76\nv_rcp_f32 v87, v87\nMvav 172,148,76\n"
"Ra 132,2,1024\nMU 128,128,86\nMU 129,129,86\nMvav 168,148,72\nWL 8\ns_add_u32 m0, s89, 131072\ns_add_u32 s46, s96, 1212416\nCV 96,128,129\nMvav 176,152,72\nRa 136,2,2048\n"
"MU 130,130,86\nMU 131,131,86\nMvav 180,152,76\nD1 1,16,46,0\nCV 97,130,131\nMU 132,132,87\nMvav 188,156,76\nRa 140,2,3072\nMU 133,133,87\nCV 100,132,133\nMvav 184,156,72\n"
"MU 134,134,87\nMU 135,135,87\nCV 101,134,135\nMvav 192,160,72\nRa 144,2,4096\nMU 136,136,86\nMU 137,137,86\nCV 98,136,137\nMvav 196,160,76\nMU 138,138,86\nMU 139,139,86\n"
"CV 99,138,139\nMvav 204,164,76\nRa 148,2,5120\nMU 140,140,87\nMU 141,141,87\nMvav 200,164,72\nWL 8\nCV 102,140,141\nMU 142,142,87\nMvav 208,168,72\nRa 152,2,6144\nMU 143,143,87\n"
"CV 103,142,143\nMvav 212,168,76\nD1 1,16,46,1024\nMU 144,144,86\nMU 145,145,86\nMvav 220,172,76\nRa 156,2,7168\nCV 104,144,145\nMU 146,146,86\nMU 147,147,86\nMvav 216,172,72\n"
"CV 105,146,147\nMU 148,148,87\nMU 149,149,87\nMvav 224,176,72\nRa 160,2,8192\nCV 108,148,149\nMU 150,150,87\nMvav 228,176,76\nMU 151,151,87\nCV 109,150,151\nMU 152,152,86\n"
"Mvav 236,180,76\nRa 164,2,9216\nMU 153,153,86\nCV 106,152,153\nMvav 232,180,72\nWL 8\nMU 154,154,86\nMU 155,155,86\nCV 107,154,155\nMvav 240,184,72\nRa 168,2,10240\nMU 156,156,87\n"
"MU 157,157,87\nMvav 244,184,76\nD1 1,16,46,2048\nCV 110,156,157\nMU 158,158,87\nMvav 252,188,76\nRa 172,2,11264\nMU 159,159,87\nCV 111,158,159\nMvav 248,188,72\nMU 160,160,86\n"
"MU 161,161,86\nCV 112,160,161\nZvav 128,128,96\nRa 176,2,12288\nMU 162,162,86\nMU 163,163,86\nMvav 128,132,104\nCV 113,162,163\nMU 164,164,87\nZvav 132,132,108\nRa 180,2,13312\n"
"MU 165,165,87\nCV 116,164,165\nMvav 132,128,100\nWL 8\nMU 166,166,87\nMU 167,167,87\nZvav 140,136,100\nRa 184,2,14336\nCV 117,166,167\nMU 168,168,86\nMvav 140,140,108\n"
"D1 1,16,46,3072\nMU 169,169,86\nCV 114,168,169\nZvav 136,140,104\nRa 188,2,15360\ns_barrier\nMU 170,170,86\nMvav 136,136,96\nMU 171,171,86\nCV 115,170,171\nMU 172,172,87\n"
"Zvav 144,144,96\nRa 128,2,32768\nMU 173,173,87\nCV 118,172,173\nMvav 144,148,104\nMU 174,174,87\nMU 175,175,87\nbuffer_store_dword v25, off, s[16:19], s54 offset:512 sc0 sc1\n"
"Zvav 148,148,108\nRa 132,2,33792\nCV 119,174,175\nMU 176,176,86\nMvav 148,144,100\nWL 8\ns_add_u32 m0, s89, 114688\ns_add_u32 s46, s96, 1179648\nZvav 156,152,100\nRa 136,2,34816\n"
"MU 177,177,86\nCV 120,176,177\nMvav 156,156,108\nD1 1,16,46,0\nMU 178,178,86\nMU 179,179,86\nZvav 152,156,104\nRa 140,2,35840\nCV 121,178,179\nMU 180,180,87\nMvav 152,152,96\n"
"MU 181,181,87\nCV 124,180,181\nMU 182,182,87\nZvav 64,160,96\nRa 144,2,36864\nMU 183,183,87\nCV 125,182,183\nMvav 64,164,104\nMU 184,184,86\nMU 185,185,86\nZvav 68,164,108\n"
"Ra 148,2,37888\nCV 122,184,185\nMU 186,186,86\nMvav 68,160,100\nWL 8\nMU 187,187,86\nCV 123,186,187\nZvav 76,168,100\nRa 152,2,38912\nMU 188,188,87\nMU 189,189,87\n"
"Mvav 76,172,108\nD1 1,16,46,1024\nCV 126,188,189\nMU 190,190,87\nZvav 72,172,104\nRa 156,2,39936\nMU 191,191,87\nCV 127,190,191\nMvav 72,168,96\nMU 192,192,86\nMU 193,193,86\n"
"CV 32,192,193\nMvav 128,176,112\nRa 160,2,40960\nMU 194,194,86\nMU 195,195,86\nMvav 128,180,120\nCV 33,194,195\nMU 196,196,87\nMU 197,197,87\nMvav 132,180,124\nRa 164,2,41984\n"
"CV 36,196,197\nMU 198,198,87\nMvav 132,176,116\nWL 8\nMU 199,199,87\nMvav 140,184,116\nRa 168,2,43008\nCV 37,198,199\nMU 200,200,86\nMvav 140,188,124\nD1 1,16,46,2048\n"
"MU 201,201,86\nCV 34,200,201\nMvav 136,188,120\nRa 172,2,44032\nMU 202,202,86\nMU 203,203,86\nMvav 136,184,112\nCV 35,202,203\nMU 204,204,87\nMU 205,205,87\nMvav 144,128,112\n"
"Ra 176,2,45056\nCV 38,204,205\nMU 206,206,87\nMvav 144,132,120\nMU 207,207,87\nCV 39,206,207\nMvav 148,132,124\nRa 180,2,46080\nMU 208,208,86\nMU 209,209,86\nMvav 148,128,116\n"
"WL 8\nCV 40,208,209\nMU 210,210,86\nMvav 156,136,116\nRa 184,2,47104\nMU 211,211,86\nCV 41,210,211\nMvav 156,140,124\nD1 1,16,46,3072\nMU 212,212,87\nMU 213,213,87\n"
"Mvav 152,140,120\nRa 188,2,48128\nCV 44,212,213\nMvav 152,136,112\nMU 214,214,87\nMU 215,215,87\nCV 45,214,215\nMvav 64,144,112\nRa 128,2,49152\nMU 216,216,86\nMU 217,217,86\n"
"Mvav 64,148,120\nCV 42,216,217\nMU 218,218,86\nMU 219,219,86\nMvav 68,148,124\nRa 132,2,50176\nCV 43,218,219\nMU 220,220,87\nMvav 68,144,116\nWL 8\ns_add_u32 m0, s89, 147456\n"
"s_add_u32 s46, s96, 1245184\nMvav 76,152,116\nRa 136,2,51200\nMU 221,221,87\nCV 46,220,221\nMvav 76,156,124\nD1 1,16,46,0\nMU 222,222,87\nMU 223,223,87\nMvav 72,156,120\n"
"Ra 140,2,52224\nCV 47,222,223\nMvav 72,152,112\nMU 224,224,86\nMU 225,225,86\nCV 48,224,225\nMvav 128,160,32\nRa 144,2,53248\nMU 226,226,86\nMU 227,227,86\nMvav 128,164,40\n"
"CV 49,226,227\nMU 228,228,87\nMvav 132,164,44\nRa 148,2,54272\nMU 229,229,87\nCV 52,228,229\nMvav 132,160,36\nWL 8\nMU 230,230,87\nMU 231,231,87\nMvav 140,168,36\nRa 152,2,55296\n"
"CV 53,230,231\nMU 232,232,86\nMvav 140,172,44\nD1 1,16,46,1024\nMU 233,233,86\nCV 50,232,233\nMvav 136,172,40\nRa 156,2,56320\nMU 234,234,86\nMvav 136,168,32\nMU 235,235,86\n"
"CV 51,234,235\nMU 236,236,87\nMvav 144,176,32\nRa 160,2,57344\nMU 237,237,87\nCV 54,236,237\nMvav 144,180,40\nMU 238,238,87\nMU 239,239,87\nCV 55,238,239\nMvav 148,180,44\n"
"Ra 164,2,58368\nMU 240,240,86\nMU 241,241,86\nMvav 148,176,36\nWL 8\nCV 56,240,241\nMvav 156,184,36\nRa 168,2,59392\nMU 242,242,86\nMU 243,243,86\nMvav 156,188,44\n"
"D1 1,16,46,2048\nCV 57,242,243\nMU 244,244,87\nMvav 152,188,40\nRa 172,2,60416\nMU 245,245,87\nCV 60,244,245\nMvav 152,184,32\nMU 246,246,87\nMU 247,247,87\nCV 61,246,247\n"
"Mvav 64,128,32\nRa 176,2,61440\nMU 248,248,86\nMU 249,249,86\nMvav 64,132,40\nCV 58,248,249\nMU 250,250,86\nMvav 68,132,44\nRa 180,2,62464\nMU 251,251,86\nCV 59,250,251\n"
"Mvav 68,128,36\ns_waitcnt vmcnt(16) lgkmcnt(8)\nMU 252,252,87\nMU 253,253,87\nMvav 76,136,36\nRa 184,2,63488\nCV 62,252,253\nMU 254,254,87\nMvav 76,140,44\nD1 1,16,46,3072\n"
"MU 255,255,87\nCV 63,254,255\nMvav 72,140,40\nRa 188,2,64512\ns_barrier\nMvav 72,136,32\nMvav 128,144,48\nRa 128,3,0\nMvav 128,148,56\nMvav 132,148,60\nRa 132,3,1024\n"
"Mvav 132,144,52\nWL 8\nMvav 140,152,52\nRa 136,3,2048\nMvav 140,156,60\nMvav 136,156,56\nRa 140,3,3072\nMvav 136,152,48\nMvav 144,160,48\nRa 144,3,4096\nMvav 144,164,56\n"
"Mvav 148,164,60\nRa 148,3,5120\nCV 132,132,133\nMvav 148,160,52\nWL 8\nCV 133,134,135\nMvav 156,168,52\nRa 152,3,6144\nCV 134,140,141\nMvav 156,172,60\nCV 135,142,143\n"
"s_add_u32 s5, s94, 0\nMvav 152,172,56\nRa 156,3,7168\nSv 132,1,16,5,1024\nMvav 152,168,48\nCV 128,128,129\nCV 129,130,131\nMvav 64,176,48\nRa 160,3,8192\nCV 130,136,137\n"
"Mvav 64,180,56\nbuffer_load_dword v27, off, s[16:19], s60 offset:512 sc0 sc1\nCV 131,138,139\nMvav 68,180,60\nRa 164,3,9216\nSv 128,1,16,5,0\nMvav 68,176,52\nWL 6\nCV 148,148,149\n"
"Mvav 76,184,52\nRa 168,3,10240\nCV 149,150,151\nMvav 76,188,60\nCV 150,156,157\nCV 151,158,159\nMvav 72,188,56\nRa 172,3,11264\ns_add_u32 s5, s94, 131072\nMvav 72,184,48\n"
"Sv 148,1,16,5,1024\nCV 144,144,145\nZvav 160,128,96\nRa 176,3,12288\nCV 145,146,147\nMvav 160,132,104\nCV 146,152,153\nCV 147,154,155\nMvav 160,136,112\nRa 180,3,13312\n"
"Sv 144,1,16,5,0\nMvav 160,140,120\nWL 6\nCV 68,68,69\nZvav 164,140,124\nRa 184,3,14336\nCV 69,70,71\nMvav 164,136,116\n.W565:\ns_cbranch_execz .WB151\nCV 70,76,77\nCV 71,78,79\n"
"Mvav 164,132,108\nRa 188,3,15360\ns_add_u32 s5, s94, 262144\nMvav 164,128,100\nSv 68,1,16,5,1024\nCV 64,64,65\nMvav 164,144,36\nRa 128,3,16384\nCV 65,66,67\nMvav 164,148,44\n"
"CV 66,72,73\nCV 67,74,75\nMvav 164,152,52\nRa 132,3,17408\nSv 64,1,16,5,0\nMvav 164,156,60\nWL 6\nMvav 160,156,56\nRa 136,3,18432\nMvav 160,152,48\nMvav 160,148,40\n"
"Ra 140,3,19456\nMvav 160,144,32\nZvav 168,160,96\nRa 144,3,20480\nMvav 168,164,104\nMvav 168,168,112\nRa 148,3,21504\nMvav 168,172,120\nWL 6\nZvav 172,172,124\nRa 152,3,22528\n"
"Mvav 172,168,116\nMvav 172,164,108\nRa 156,3,23552\nMvav 172,160,100\nMvav 172,176,36\nRa 160,3,24576\nMvav 172,180,44\nMvav 172,184,52\nRa 164,3,25600\nMvav 172,188,60\nWL 6\n"
"Mvav 168,188,56\nRa 168,3,26624\nMvav 168,184,48\nMvav 168,180,40\nRa 172,3,27648\nMvav 168,176,32\nZvav 176,128,96\nRa 176,3,28672\nCV 164,164,165\nMvav 176,132,104\n"
"CV 165,166,167\nCV 166,172,173\nMvav 176,136,112\nRa 180,3,29696\nCV 167,174,175\nMvav 176,140,120\ns_waitcnt vmcnt(11) lgkmcnt(6)\ns_add_u32 s5, s94, 393216\nZvav 180,140,124\n"
"Ra 184,3,30720\nSv 164,1,16,5,1024\nMvav 180,136,116\nCV 160,160,161\nCV 161,162,163\nMvav 180,132,108\nRa 188,3,31744\ns_barrier\nMvav 180,128,100\nCV 162,168,169\n"
"CV 163,170,171\nMvav 180,144,36\nRa 128,3,32768\nSv 160,1,16,5,0\nMvav 180,148,44\nMvav 180,152,52\nRa 132,3,33792\nMvav 180,156,60\nWL 6\ns_add_u32 m0, s89, 16384\n"
"s_add_u32 s46, s96, 1277952\nMvav 176,156,56\nRa 136,3,34816\nMvav 176,152,48\nD1 1,16,46,0\nMvav 176,148,40\nRa 140,3,35840\nMvav 176,144,32\nZvav 184,160,96\nRa 144,3,36864\n"
"Mvav 184,164,104\nMvav 184,168,112\nRa 148,3,37888\nMvav 184,172,120\nWL 6\nZvav 188,172,124\nRa 152,3,38912\nMvav 188,168,116\nD1 1,16,46,1024\nMvav 188,164,108\nRa 156,3,39936\n"
"Mvav 188,160,100\nMvav 188,176,36\nRa 160,3,40960\nWV 9\nv_cmp_ne_u32 vcc, s15, v27\ns_nop 0\ns_cbranch_vccnz .S246\n.S247:\nMvav 188,180,44\ns_add_u32 s4, s92, 0\n"
"Mvav 188,184,52\nRa 164,3,41984\nMvav 188,188,60\nWL 6\nbuffer_load_dwordx4 v[208:211], v1, s[16:19], s4 offen offset:0 sc0 sc1\nMvav 184,188,56\nRa 168,3,43008\nMvav 184,184,48\n"
"D1 1,16,46,2048\nMvav 184,180,40\nRa 172,3,44032\nMvav 184,176,32\nbuffer_load_dwordx4 v[212:215], v1, s[16:19], s4 offen offset:1024 sc0 sc1\nZvav 192,128,96\nRa 176,3,45056\n"
"v_lshrrev_b32 v88, 8, v1\nMvav 192,132,104\nv_lshlrev_b32 v89, 5, v88\nCV 180,180,181\nMvav 192,136,112\nRa 180,3,46080\nCV 181,182,183\nMvav 192,140,120\nWL 6\nCV 182,188,189\n"
"Zvav 196,140,124\nRa 184,3,47104\nv_bfe_u32 v90, v1, 4, 4\nMvav 196,136,116\nD1 1,16,46,3072\nCV 183,190,191\nMvav 196,132,108\nRa 188,3,48128\ns_add_u32 s5, s94, 524288\n"
"Mvav 196,128,100\nv_mul_u32_u24 v91, 896, v90\nSv 180,1,16,5,1024\nMvav 196,144,36\nRa 128,3,49152\nv_lshl_add_u32 v91, v88, 4, v91\nMvav 196,148,44\nv_add_u32 v90, s21, v90\n"
"CV 176,176,177\nMvav 196,152,52\nRa 132,3,50176\nCV 177,178,179\nMvav 196,156,60\nWL 6\ns_add_u32 m0, s89, 0\ns_add_u32 s46, s96, 1310720\nMvav 192,156,56\nRa 136,3,51200\n"
"v_lshlrev_b32 v88, 3, v90\nMvav 192,152,48\nD1 1,16,46,0\nCV 178,184,185\nMvav 192,148,40\nRa 140,3,52224\nCV 179,186,187\nMvav 192,144,32\nv_add_u32 v94, 64, v90\n"
"Sv 176,1,16,5,0\nZvav 200,160,96\nRa 144,3,53248\ns_mov_b32 s63, 0xc040000\nMvav 200,164,104\nv_lshrrev_b32 v92, 8, v1\nMvav 200,168,112\nRa 148,3,54272\nMvav 200,172,120\nWL 6\n"
"Zvav 204,172,124\nRa 152,3,55296\nv_bfe_u32 v93, v1, 6, 2\nMvav 204,168,116\nD1 1,16,46,1024\nv_cmp_eq_u32 vcc, v92, v93\nv_and_b32 v92, 16, v1\nv_mov_b32 v93, 0x3f80\n"
"v_cndmask_b32 v93, 0, v93, vcc\nMvav 204,164,108\nRa 156,3,56320\nMvav 204,160,100\nv_lshlrev_b32 v93, v92, v93\nMvav 204,176,36\nRa 160,3,57344\nv_and_b32 v92, 32, v1\n"
"Mvav 204,180,44\ns_add_u32 s4, s92, 131072\nv_cmp_eq_u32 vcc, 0, v92\nv_mov_b32 v4, 0\nv_mov_b32 v5, 0\nv_cndmask_b32 v6, 0, v93, vcc\nMvav 204,184,52\nRa 164,3,58368\n"
"Mvav 204,188,60\nWL 6\nbuffer_load_dwordx4 v[216:219], v1, s[16:19], s4 offen offset:0 sc0 sc1\nMvav 200,188,56\nRa 168,3,59392\nv_sub_u32 v7, v93, v6\nMvav 200,184,48\n"
"D1 1,16,46,2048\nMvav 200,180,40\nRa 172,3,60416\nMvav 200,176,32\nbuffer_load_dwordx4 v[220:223], v1, s[16:19], s4 offen offset:1024 sc0 sc1\nZvav 160,128,96\nRa 176,3,61440\n"
"v_mov_b32 v8, 0\nMvav 160,132,104\nv_mov_b32 v9, 0\nCV 196,196,197\nMvav 160,136,112\nRa 180,3,62464\nCV 197,198,199\nMvav 160,140,120\nWL 6\nCV 198,204,205\nZvav 164,140,124\n"
"Ra 184,3,63488\nCV 199,206,207\nMvav 164,136,116\nD1 1,16,46,3072\ns_add_u32 s5, s94, 655360\nMvav 164,132,108\nRa 188,3,64512\ns_barrier\nMvav 164,128,100\nSv 196,1,16,5,1024\n"
"CV 192,192,193\nMvav 164,144,36\nRa 128,0,0\nCV 193,194,195\nMvav 164,148,44\nCV 194,200,201\nCV 195,202,203\nMvav 164,152,52\nRa 132,0,1024\nSv 192,1,16,5,0\nMvav 164,156,60\n"
"WL 6\ns_add_u32 m0, s89, 32768\ns_add_u32 s46, s96, 1343488\nMvav 160,156,56\nRa 136,0,2048\nMvav 160,152,48\nD1 1,16,46,0\nMvav 160,148,40\nRa 140,0,3072\nMvav 160,144,32\n"
"Zvav 168,160,96\nRa 144,0,4096\nWV 12\nv_max_u32 v21, v211, v215\nv_cmp_eq_u32 vcc, -1, v21\ns_nop 0\ns_cbranch_vccnz .S222\n.S223:\nMvav 168,164,104\n"
"v_lshlrev_b32 v176, 16, v208\nMvav 168,168,112\nRa 148,0,5120\nMvav 168,172,120\nWL 6\nv_and_b32 v177, 0xffff0000, v208\nZvav 172,172,124\nRa 152,0,6144\nMvav 172,168,116\n"
"D1 1,16,46,1024\nMvav 172,164,108\nRa 156,0,7168\nv_lshlrev_b32 v178, 16, v209\nMvav 172,160,100\nv_and_b32 v179, 0xffff0000, v209\nMvav 172,176,36\nRa 160,0,8192\n"
"v_lshlrev_b32 v180, 16, v212\nMvav 172,180,44\nv_and_b32 v181, 0xffff0000, v212\ns_add_u32 s4, s92, 262144\nMvav 172,184,52\nRa 164,0,9216\nMvav 172,188,60\nWL 6\n"
"v_lshlrev_b32 v182, 16, v213\nbuffer_load_dwordx4 v[224:227], v1, s[16:19], s4 offen offset:0 sc0 sc1\nMvav 168,188,56\nRa 168,0,10240\nMvav 168,184,48\nD1 1,16,46,2048\n"
"Mvav 168,180,40\nRa 172,0,11264\nv_and_b32 v183, 0xffff0000, v213\nMvav 168,176,32\nv_lshlrev_b32 v184, 16, v210\n"
"buffer_load_dwordx4 v[228:231], v1, s[16:19], s4 offen offset:1024 sc0 sc1\nMvav 176,128,96\nRa 176,0,12288\nv_and_b32 v185, 0xffff0000, v210\nMvav 176,132,104\n"
"v_lshlrev_b32 v186, 16, v211\nCV 164,164,165\nMvav 176,136,112\nRa 180,0,13312\nCV 165,166,167\nMvav 176,140,120\nWL 6\nCV 166,172,173\nMvav 180,140,124\nRa 184,0,14336\n"
"v_and_b32 v187, 0xffff0000, v211\nMvav 180,136,116\nD1 1,16,46,3072\nCV 167,174,175\nMvav 180,132,108\nRa 188,0,15360\ns_add_u32 s5, s94, 786432\nMvav 180,128,100\n"
"v_lshlrev_b32 v188, 16, v214\nSv 164,1,16,5,1024\nMvav 180,144,36\nRa 128,0,16384\nv_and_b32 v189, 0xffff0000, v214\nMvav 180,148,44\nv_lshlrev_b32 v190, 16, v215\nCV 160,160,161\n"
"Mvav 180,152,52\nRa 132,0,17408\nCV 161,162,163\nMvav 180,156,60\nWL 6\ns_add_u32 m0, s89, 49152\ns_add_u32 s46, s96, 1376256\nMvav 176,156,56\nRa 136,0,18432\n"
"v_and_b32 v191, 0xffff0000, v215\nMvav 176,152,48\nD1 1,16,46,0\nCV 162,168,169\nMvav 176,148,40\nRa 140,0,19456\nCV 163,170,171\nMvav 176,144,32\nSv 160,1,16,5,0\n"
"Mvav 184,160,96\nRa 144,0,20480\nWV 12\nv_max_u32 v21, v219, v223\nv_cmp_eq_u32 vcc, -1, v21\ns_nop 0\ns_cbranch_vccnz .S225\n.S226:\nMvav 184,164,104\n"
"v_lshlrev_b32 v192, 16, v216\nMvav 184,168,112\nRa 148,0,21504\nMvav 184,172,120\nWL 6\nv_and_b32 v193, 0xffff0000, v216\nMvav 188,172,124\nRa 152,0,22528\nMvav 188,168,116\n"
"D1 1,16,46,1024\nMvav 188,164,108\nRa 156,0,23552\nv_lshlrev_b32 v194, 16, v217\nMvav 188,160,100\nv_and_b32 v195, 0xffff0000, v217\nMvav 188,176,36\nRa 160,0,24576\n"
"v_lshlrev_b32 v196, 16, v220\nMvav 188,180,44\nv_and_b32 v197, 0xffff0000, v220\ns_add_u32 s4, s92, 393216\nMvav 188,184,52\nRa 164,0,25600\nMvav 188,188,60\nWL 6\n"
"v_lshlrev_b32 v198, 16, v221\nbuffer_load_dwordx4 v[208:211], v1, s[16:19], s4 offen offset:0 sc0 sc1\nMvav 184,188,56\nRa 168,0,26624\nMvav 184,184,48\nD1 1,16,46,2048\n"
"Mvav 184,180,40\nRa 172,0,27648\nv_and_b32 v199, 0xffff0000, v221\nMvav 184,176,32\nv_lshlrev_b32 v200, 16, v218\n"
"buffer_load_dwordx4 v[212:215], v1, s[16:19], s4 offen offset:1024 sc0 sc1\nMvav 192,128,96\nRa 176,0,28672\nv_and_b32 v201, 0xffff0000, v218\nMvav 192,132,104\n"
"v_lshlrev_b32 v202, 16, v219\nCV 180,180,181\nMvav 192,136,112\nRa 180,0,29696\nCV 181,182,183\nMvav 192,140,120\ns_waitcnt vmcnt(15) lgkmcnt(6)\nCV 182,188,189\nMvav 196,140,124\n"
"Ra 184,0,30720\nv_and_b32 v203, 0xffff0000, v219\nMvav 196,136,116\nD1 1,16,46,3072\nCV 183,190,191\nMvav 196,132,108\nRa 188,0,31744\ns_barrier\nMvav 196,128,100\n"
"v_lshlrev_b32 v204, 16, v222\ns_add_u32 s5, s94, 917504\nMvav 196,144,36\nRa 128,2,16384\nv_and_b32 v205, 0xffff0000, v222\nMvav 196,148,44\nv_lshlrev_b32 v206, 16, v223\n"
"Sv 180,1,16,5,1024\nMvav 196,152,52\nRa 132,2,17408\nCV 176,176,177\nMvav 196,156,60\nWL 6\ns_add_u32 m0, s89, 65536\ns_add_u32 s46, s96, 1409024\nMvav 192,156,56\nRa 136,2,18432\n"
"v_and_b32 v207, 0xffff0000, v223\nMvav 192,152,48\nD1 1,16,46,0\nCV 177,178,179\nMvav 192,148,40\nRa 140,2,19456\nCV 178,184,185\nMvav 192,144,32\nCV 179,186,187\nSv 176,1,16,5,0\n"
"Mvav 200,160,96\nRa 144,2,20480\nWV 12\nv_max_u32 v21, v227, v231\nv_cmp_eq_u32 vcc, -1, v21\ns_nop 0\ns_cbranch_vccnz .S228\n.S229:\nMvav 200,164,104\n"
"v_lshlrev_b32 v160, 16, v224\nMvav 200,168,112\nRa 148,2,21504\nMvav 200,172,120\nWL 6\nv_and_b32 v161, 0xffff0000, v224\nMvav 204,172,124\nRa 152,2,22528\nMvav 204,168,116\n"
"D1 1,16,46,1024\nMvav 204,164,108\nRa 156,2,23552\nv_lshlrev_b32 v162, 16, v225\nMvav 204,160,100\nv_and_b32 v163, 0xffff0000, v225\nMvav 204,176,36\nRa 160,2,24576\n"
"v_lshlrev_b32 v164, 16, v228\nMvav 204,180,44\nv_and_b32 v165, 0xffff0000, v228\ns_add_u32 s4, s92, 524288\nMvav 204,184,52\nRa 164,2,25600\nMvav 204,188,60\nWL 6\n"
"v_lshlrev_b32 v166, 16, v229\nbuffer_load_dwordx4 v[216:219], v1, s[16:19], s4 offen offset:0 sc0 sc1\nMvav 200,188,56\nRa 168,2,26624\nMvav 200,184,48\nD1 1,16,46,2048\n"
"Mvav 200,180,40\nRa 172,2,27648\nv_and_b32 v167, 0xffff0000, v229\nMvav 200,176,32\nv_lshlrev_b32 v168, 16, v226\n"
"buffer_load_dwordx4 v[220:223], v1, s[16:19], s4 offen offset:1024 sc0 sc1\nMvav 160,128,96\nRa 176,2,28672\nv_and_b32 v169, 0xffff0000, v226\nMvav 160,132,104\n"
"v_lshlrev_b32 v170, 16, v227\nCV 196,196,197\nMvav 160,136,112\nRa 180,2,29696\nCV 197,198,199\nMvav 160,140,120\nWL 6\nCV 198,204,205\nMvav 164,140,124\nRa 184,2,30720\n"
"v_and_b32 v24, 0x70, v1\nMvav 164,136,116\nD1 1,16,46,3072\nCV 199,206,207\nMvav 164,132,108\nRa 188,2,31744\nv_and_b32 v171, 0xffff0000, v227\nMvav 164,128,100\n"
"v_lshlrev_b32 v172, 16, v230\ns_add_u32 s5, s94, 1048576\nMvav 164,144,36\nRa 128,2,0\nv_lshlrev_b32 v24, 5, v24\nMvav 164,148,44\nv_and_b32 v173, 0xffff0000, v230\n"
"Sv 196,1,16,5,1024\nMvav 164,152,52\nRa 132,2,1024\nCV 192,192,193\nMvav 164,156,60\nWL 6\nv_lshlrev_b32 v174, 16, v231\nMvav 160,156,56\nRa 136,2,2048\n"
"buffer_load_dword v27, v24, s[16:19], s41 offen sc0 sc1\nMvav 160,152,48\nv_and_b32 v175, 0xffff0000, v231\nCV 193,194,195\nMvav 160,148,40\nRa 140,2,3072\nCV 194,200,201\n"
"Mvav 160,144,32\nCV 195,202,203\nSv 192,1,16,5,0\nMvav 168,160,96\nRa 144,2,4096\nWV 12\nv_max_u32 v21, v211, v215\nv_cmp_eq_u32 vcc, -1, v21\ns_nop 0\ns_cbranch_vccnz .S231\n"
".S232:\nMvav 168,164,104\nv_lshlrev_b32 v176, 16, v208\nMvav 168,168,112\nRa 148,2,5120\nMvav 168,172,120\nWL 6\nMvav 172,172,124\nRa 152,2,6144\nv_and_b32 v177, 0xffff0000, v208\n"
".W566:\ns_cbranch_execz .WB151\nMvav 172,168,116\nv_lshlrev_b32 v178, 16, v209\nMvav 172,164,108\nRa 156,2,7168\nMvav 172,160,100\nv_and_b32 v179, 0xffff0000, v209\n"
"Mvav 172,176,36\nRa 160,2,8192\nv_lshlrev_b32 v180, 16, v212\nMvav 172,180,44\nv_and_b32 v181, 0xffff0000, v212\ns_add_u32 s4, s92, 655360\nMvav 172,184,52\nRa 164,2,9216\n"
"Mvav 172,188,60\nWL 6\nbuffer_load_dwordx4 v[224:227], v1, s[16:19], s4 offen offset:0 sc0 sc1\nMvav 168,188,56\nRa 168,2,10240\nv_lshlrev_b32 v182, 16, v213\nMvav 168,184,48\n"
"v_and_b32 v183, 0xffff0000, v213\nMvav 168,180,40\nRa 172,2,11264\nMvav 168,176,32\nv_lshlrev_b32 v184, 16, v210\n"
"buffer_load_dwordx4 v[228:231], v1, s[16:19], s4 offen offset:1024 sc0 sc1\nMvav 176,128,96\nRa 176,2,12288\nv_and_b32 v185, 0xffff0000, v210\nMvav 176,132,104\n"
"v_lshlrev_b32 v186, 16, v211\nCV 164,164,165\nMvav 176,136,112\nRa 180,2,13312\nCV 165,166,167\nMvav 176,140,120\ns_waitcnt vmcnt(13) lgkmcnt(6)\nCV 166,172,173\nMvav 180,140,124\n"
"Ra 184,2,14336\nCV 167,174,175\nMvav 180,136,116\nv_and_b32 v187, 0xffff0000, v211\ns_add_u32 s5, s94, 1179648\nMvav 180,132,108\nRa 188,2,15360\ns_barrier\nMvav 180,128,100\n"
"v_lshlrev_b32 v188, 16, v214\nSv 164,1,16,5,1024\nMvav 180,144,36\nRa 128,2,32768\nv_and_b32 v189, 0xffff0000, v214\nMvav 180,148,44\nv_lshlrev_b32 v190, 16, v215\nCV 160,160,161\n"
"Mvav 180,152,52\nRa 132,2,33792\nCV 161,162,163\nMvav 180,156,60\nWL 6\nCV 162,168,169\nMvav 176,156,56\nRa 136,2,34816\nCV 163,170,171\nMvav 176,152,48\n"
"v_and_b32 v191, 0xffff0000, v215\nSv 160,1,16,5,0\nMvav 176,148,40\nRa 140,2,35840\nMvav 176,144,32\nMvav 184,160,96\nRa 144,2,36864\nWV 8\nv_max_u32 v21, v219, v223\n"
"v_cmp_eq_u32 vcc, -1, v21\ns_nop 0\ns_cbranch_vccnz .S234\n.S235:\nMvav 184,164,104\nv_lshlrev_b32 v192, 16, v216\nMvav 184,168,112\nRa 148,2,37888\nMvav 184,172,120\nWL 6\n"
"Mvav 188,172,124\nRa 152,2,38912\nv_and_b32 v193, 0xffff0000, v216\nMvav 188,168,116\nv_lshlrev_b32 v194, 16, v217\nMvav 188,164,108\nRa 156,2,39936\nMvav 188,160,100\n"
"v_and_b32 v195, 0xffff0000, v217\nMvav 188,176,36\nRa 160,2,40960\nv_lshlrev_b32 v196, 16, v220\nMvav 188,180,44\nv_and_b32 v197, 0xffff0000, v220\ns_add_u32 s4, s92, 786432\n"
"Mvav 188,184,52\nRa 164,2,41984\nMvav 188,188,60\nWL 6\nbuffer_load_dwordx4 v[208:211], v1, s[16:19], s4 offen offset:0 sc0 sc1\nMvav 184,188,56\nRa 168,2,43008\n"
"v_lshlrev_b32 v198, 16, v221\nMvav 184,184,48\nv_and_b32 v199, 0xffff0000, v221\nMvav 184,180,40\nRa 172,2,44032\nMvav 184,176,32\nv_lshlrev_b32 v200, 16, v218\n"
"buffer_load_dwordx4 v[212:215], v1, s[16:19], s4 offen offset:1024 sc0 sc1\nMvav 192,128,96\nRa 176,2,45056\nv_and_b32 v201, 0xffff0000, v218\nMvav 192,132,104\n"
"v_lshlrev_b32 v202, 16, v219\nCV 180,180,181\nMvav 192,136,112\nRa 180,2,46080\nCV 181,182,183\nMvav 192,140,120\nWL 6\nCV 182,188,189\nMvav 196,140,124\nRa 184,2,47104\n"
"CV 183,190,191\nMvav 196,136,116\nv_and_b32 v203, 0xffff0000, v219\ns_add_u32 s5, s94, 1310720\nMvav 196,132,108\nRa 188,2,48128\nSv 180,1,16,5,1024\nMvav 196,128,100\n"
"v_lshlrev_b32 v204, 16, v222\nCV 176,176,177\nMvav 196,144,36\nRa 128,2,49152\nv_and_b32 v205, 0xffff0000, v222\nMvav 196,148,44\nv_lshlrev_b32 v206, 16, v223\nCV 177,178,179\n"
"Mvav 196,152,52\nRa 132,2,50176\nCV 178,184,185\nMvav 196,156,60\nWL 6\nCV 179,186,187\nMvav 192,156,56\nRa 136,2,51200\nSv 176,1,16,5,0\nMvav 192,152,48\n"
"v_and_b32 v207, 0xffff0000, v223\nMvav 192,148,40\nRa 140,2,52224\nMvav 192,144,32\nMvav 200,160,96\nRa 144,2,53248\nWV 6\nv_max_u32 v21, v227, v231\nv_cmp_eq_u32 vcc, -1, v21\n"
"s_nop 0\ns_cbranch_vccnz .S237\n.S238:\nMvav 200,164,104\nv_lshlrev_b32 v160, 16, v224\nv_xor_b32 v27, s15, v27\nv_and_b32 v26, 0xffffdfff, v27\nv_cmp_ne_u32 s[64:65], 0, v27\n"
"v_cmp_ne_u32 vcc, 0, v26\ns_nop 0\ns_cbranch_vccnz .S249\n.S250:\nMvav 200,168,112\nRa 148,2,54272\ns_bitcmp1_b32 s64, 0\ns_cselect_b32 s2, 0x2000, 0\nMvav 200,172,120\nWL 6\n"
"Mvav 204,172,124\nRa 152,2,55296\nv_and_b32 v161, 0xffff0000, v224\nMvav 204,168,116\nv_lshlrev_b32 v162, 16, v225\nMvav 204,164,108\nRa 156,2,56320\ns_xor_b32 s2, s2, s20\n"
"Mvav 204,160,100\nv_and_b32 v163, 0xffff0000, v225\nMvav 204,176,36\nRa 160,2,57344\nv_lshlrev_b32 v164, 16, v228\nMvav 204,180,44\nv_and_b32 v165, 0xffff0000, v228\n"
"Mvav 204,184,52\nRa 164,2,58368\nMvav 204,188,60\nWL 6\ns_add_u32 s24, s2, 0x0\nMvav 200,188,56\nRa 168,2,59392\nv_lshlrev_b32 v166, 16, v229\nMvav 200,184,48\n"
"v_and_b32 v167, 0xffff0000, v229\ns_bitcmp1_b32 s64, 1\ns_cselect_b32 s2, 0x2000, 0\nMvav 200,180,40\nRa 172,2,60416\nMvav 200,176,32\nv_lshlrev_b32 v168, 16, v226\n"
"Mvav 160,128,96\nRa 176,2,61440\nv_and_b32 v169, 0xffff0000, v226\nMvav 160,132,104\nv_lshlrev_b32 v170, 16, v227\nCV 196,196,197\nMvav 160,136,112\nRa 180,2,62464\n"
"CV 197,198,199\nMvav 160,140,120\nWL 6\nCV 198,204,205\nMvav 164,140,124\nRa 184,2,63488\nCV 199,206,207\nMvav 164,136,116\nv_and_b32 v171, 0xffff0000, v227\n"
"s_add_u32 s5, s94, 1441792\nMvav 164,132,108\nRa 188,2,64512\ns_barrier\nMvav 164,128,100\nv_lshlrev_b32 v172, 16, v230\nSv 196,1,16,5,1024\nMvav 164,144,36\nRa 128,3,0\n"
"v_and_b32 v173, 0xffff0000, v230\nMvav 164,148,44\nv_lshlrev_b32 v174, 16, v231\nCV 192,192,193\nMvav 164,152,52\nRa 132,3,1024\nCV 193,194,195\nMvav 164,156,60\nWL 6\n"
"s_xor_b32 s2, s2, s20\nMvav 160,156,56\nRa 136,3,2048\nCV 194,200,201\nMvav 160,152,48\nv_and_b32 v175, 0xffff0000, v231\nCV 195,202,203\nMvav 160,148,40\nRa 140,3,3072\n"
"Sv 192,1,16,5,0\nMvav 160,144,32\ns_add_u32 s25, s2, 0x1800000\ns_bitcmp1_b32 s64, 2\ns_cselect_b32 s2, 0x2000, 0\nMvav 168,160,96\nRa 144,3,4096\nWV 4\nv_max_u32 v21, v211, v215\n"
"v_cmp_eq_u32 vcc, -1, v21\ns_nop 0\ns_cbranch_vccnz .S240\n.S241:\nMvav 168,164,104\nv_lshlrev_b32 v176, 16, v208\nMvav 168,168,112\nRa 148,3,5120\nMvav 168,172,120\nWL 6\n"
"Mvav 172,172,124\nRa 152,3,6144\nv_and_b32 v177, 0xffff0000, v208\nMvav 172,168,116\nv_lshlrev_b32 v178, 16, v209\nMvav 172,164,108\nRa 156,3,7168\ns_xor_b32 s2, s2, s20\n"
"Mvav 172,160,100\nv_and_b32 v179, 0xffff0000, v209\nMvav 172,176,36\nRa 160,3,8192\nv_lshlrev_b32 v180, 16, v212\nMvav 172,180,44\nv_and_b32 v181, 0xffff0000, v212\n"
"Mvav 172,184,52\nRa 164,3,9216\nMvav 172,188,60\nWL 6\ns_add_u32 s26, s2, 0x3000000\nMvav 168,188,56\nRa 168,3,10240\nv_lshlrev_b32 v182, 16, v213\nMvav 168,184,48\n"
"v_and_b32 v183, 0xffff0000, v213\ns_bitcmp1_b32 s64, 3\ns_cselect_b32 s2, 0x2000, 0\nMvav 168,180,40\nRa 172,3,11264\nMvav 168,176,32\nv_lshlrev_b32 v184, 16, v210\n"
"Mvav 176,128,96\nRa 176,3,12288\ns_xor_b32 s2, s2, s20\nMvav 176,132,104\nv_and_b32 v185, 0xffff0000, v210\nCV 164,164,165\nMvav 176,136,112\nRa 180,3,13312\nCV 165,166,167\n"
"Mvav 176,140,120\nWL 6\nCV 166,172,173\nMvav 180,140,124\nRa 184,3,14336\nCV 167,174,175\nMvav 180,136,116\nv_lshlrev_b32 v186, 16, v211\ns_add_u32 s5, s94, 1572864\n"
"Mvav 180,132,108\nRa 188,3,15360\ns_add_u32 s27, s2, 0x4800000\nMvav 180,128,100\nv_and_b32 v187, 0xffff0000, v211\ns_bitcmp1_b32 s64, 4\ns_cselect_b32 s2, 0x2000, 0\n"
"Mvav 180,144,36\nv_lshlrev_b32 v188, 16, v214\nSv 164,1,16,5,1024\nMvav 180,148,44\nv_and_b32 v189, 0xffff0000, v214\nCV 160,160,161\nMvav 180,152,52\n"
"v_lshlrev_b32 v190, 16, v215\nCV 161,162,163\nMvav 180,156,60\nWL 4\ns_xor_b32 s2, s2, s20\nMvav 176,156,56\nv_and_b32 v191, 0xffff0000, v215\nCV 162,168,169\nMvav 176,152,48\n"
"s_add_u32 s32, s2, 0x6000000\ns_bitcmp1_b32 s64, 5\ns_cselect_b32 s2, 0x2000, 0\nMvav 176,148,40\ns_xor_b32 s2, s2, s20\nCV 163,170,171\nMvav 176,144,32\n"
"s_add_u32 s33, s2, 0x7800000\ns_bitcmp1_b32 s64, 6\ns_cselect_b32 s2, 0x2000, 0\nMvav 184,160,96\ns_xor_b32 s2, s2, s20\ns_add_u32 s34, s2, 0x9000000\nMvav 184,164,104\n"
"s_bitcmp1_b32 s64, 7\ns_cselect_b32 s2, 0x2000, 0\nMvav 184,168,112\ns_xor_b32 s2, s2, s20\nSv 160,1,16,5,0\nMvav 184,172,120\nWL 0\nMvav 188,172,124\n"
"s_add_u32 s35, s2, 0xa800000\nMvav 188,168,116\nMvav 188,164,108\nMvav 188,160,100\nMvav 188,176,36\nMvav 188,180,44\nMvav 188,184,52\nMvav 188,188,60\nMvav 184,188,56\n"
"Mvav 184,184,48\nMvav 184,180,40\nMvav 184,176,32\nCV 180,180,181\nCV 181,182,183\ns_nop 1\nCV 182,188,189\nCV 183,190,191\ns_add_u32 s5, s94, 1703936\nSv 180,1,16,5,1024\n"
"CV 176,176,177\nCV 177,178,179\nCV 178,184,185\nCV 179,186,187\nSv 176,1,16,5,0\ns_cmp_eq_u32 s13, 0\ns_cbranch_scc1 .Z252\n"
"buffer_load_dwordx4 v[12:15], v91, s[28:31], s22 offen offset:0\nbuffer_load_dwordx4 v[16:19], v91, s[28:31], s23 offen offset:0\n"
"buffer_load_dwordx4 v[64:67], v89, s[16:19], s40 offen offset:0 sc0 sc1\nbuffer_load_dwordx4 v[68:71], v89, s[16:19], s40 offen offset:16 sc0 sc1\n"
"buffer_load_dwordx4 v[72:75], v89, s[16:19], s40 offen offset:1792 sc0 sc1\nbuffer_load_dwordx4 v[76:79], v89, s[16:19], s40 offen offset:1808 sc0 sc1\n"
"buffer_load_dwordx2 v[80:81], v88, s[16:19], s63 offen offset:0 sc0 sc1\nbuffer_load_dwordx2 v[82:83], v88, s[16:19], s63 offen offset:512 sc0 sc1\n"
"buffer_load_dwordx4 v[96:99], v1, s[16:19], s24 offen offset:0 sc0 sc1\nbuffer_load_dwordx4 v[100:103], v1, s[16:19], s24 offen offset:1024 sc0 sc1\n"
"buffer_load_dwordx4 v[104:107], v1, s[16:19], s25 offen offset:0 sc0 sc1\nbuffer_load_dwordx4 v[108:111], v1, s[16:19], s25 offen offset:1024 sc0 sc1\n"
"buffer_load_dwordx4 v[112:115], v1, s[16:19], s26 offen offset:0 sc0 sc1\nbuffer_load_dwordx4 v[116:119], v1, s[16:19], s26 offen offset:1024 sc0 sc1\n"
"buffer_load_dwordx4 v[120:123], v1, s[16:19], s27 offen offset:0 sc0 sc1\nbuffer_load_dwordx4 v[124:127], v1, s[16:19], s27 offen offset:1024 sc0 sc1\n"
"buffer_load_dwordx4 v[32:35], v1, s[16:19], s32 offen offset:0 sc0 sc1\nbuffer_load_dwordx4 v[36:39], v1, s[16:19], s32 offen offset:1024 sc0 sc1\n"
"buffer_load_dwordx4 v[40:43], v1, s[16:19], s33 offen offset:0 sc0 sc1\nbuffer_load_dwordx4 v[44:47], v1, s[16:19], s33 offen offset:1024 sc0 sc1\n"
"buffer_load_dwordx4 v[48:51], v1, s[16:19], s34 offen offset:0 sc0 sc1\nbuffer_load_dwordx4 v[52:55], v1, s[16:19], s34 offen offset:1024 sc0 sc1\n"
"buffer_load_dwordx4 v[56:59], v1, s[16:19], s35 offen offset:0 sc0 sc1\nbuffer_load_dwordx4 v[60:63], v1, s[16:19], s35 offen offset:1024 sc0 sc1\nWV 16\nv_xor_b32 v66, s15, v66\n"
"v_xor_b32 v70, s15, v70\nv_xor_b32 v74, s15, v74\nv_xor_b32 v78, s15, v78\nv_xor_b32 v81, s15, v81\nv_xor_b32 v83, s15, v83\nv_or3_b32 v92, v66, v70, v74\n"
"v_or3_b32 v92, v92, v78, v81\nv_or_b32 v92, v92, v83\nv_cmp_ne_u32 vcc, 0, v92\ns_nop 0\ns_cbranch_vccnz .S243\n.S244:\nv_and_b32 v92, 0x7ff, v67\nv_lshrrev_b32 v93, 16, v67\n"
"v_cmp_eq_u32 s[44:45], v92, v90\nv_cmp_eq_u32 s[46:47], v93, v90\nv_cmp_eq_u32 s[48:49], v92, v94\nv_cmp_eq_u32 s[50:51], v93, v94\nv_cndmask_b32 v12, v12, v64, s[44:45]\n"
"v_cndmask_b32 v13, v13, v65, s[44:45]\nv_cndmask_b32 v14, v14, v68, s[44:45]\nv_cndmask_b32 v15, v15, v69, s[44:45]\nv_cndmask_b32 v12, v12, v72, s[46:47]\n"
"v_cndmask_b32 v13, v13, v73, s[46:47]\nv_cndmask_b32 v14, v14, v76, s[46:47]\nv_cndmask_b32 v15, v15, v77, s[46:47]\nv_cndmask_b32 v16, v16, v64, s[48:49]\n"
"v_cndmask_b32 v17, v17, v65, s[48:49]\nv_cndmask_b32 v18, v18, v68, s[48:49]\nv_cndmask_b32 v19, v19, v69, s[48:49]\nv_cndmask_b32 v16, v16, v72, s[50:51]\n"
"v_cndmask_b32 v17, v17, v73, s[50:51]\nv_cndmask_b32 v18, v18, v76, s[50:51]\nv_cndmask_b32 v19, v19, v77, s[50:51]\nv_sub_f32 v92, 1.0, v80\nv_sub_f32 v93, 1.0, v82\nCV 92,92,93\n"
"v_and_b32 v93, 0xffff0000, v92\nv_lshlrev_b32 v92, 16, v92\nv_lshlrev_b32 v64, 16, v12\nv_and_b32 v65, 0xffff0000, v12\nMU 64,64,92\nMU 65,65,92\nv_lshlrev_b32 v66, 16, v13\n"
"v_and_b32 v67, 0xffff0000, v13\nMU 66,66,92\nMU 67,67,92\nv_lshlrev_b32 v68, 16, v14\nv_and_b32 v69, 0xffff0000, v14\nMU 68,68,92\nMU 69,69,92\nv_lshlrev_b32 v70, 16, v15\n"
"v_and_b32 v71, 0xffff0000, v15\nMU 70,70,92\nMU 71,71,92\nv_lshlrev_b32 v72, 16, v16\nv_and_b32 v73, 0xffff0000, v16\nMU 72,72,93\nMU 73,73,93\nv_lshlrev_b32 v74, 16, v17\n"
"v_and_b32 v75, 0xffff0000, v17\nMU 74,74,93\nMU 75,75,93\nv_lshlrev_b32 v76, 16, v18\nv_and_b32 v77, 0xffff0000, v18\nMU 76,76,93\nMU 77,77,93\nv_lshlrev_b32 v78, 16, v19\n"
"v_and_b32 v79, 0xffff0000, v19\nMU 78,78,93\nMU 79,79,93\ns_mov_b32 s7, 262144\nWV 0\n.Z255:\nv_max_u32 v84, v99, v103\nv_cmp_eq_u32 s[44:45], -1, v84\nv_max_u32 v84, v107, v111\n"
"v_cmp_eq_u32 s[46:47], -1, v84\nv_max_u32 v84, v115, v119\nv_cmp_eq_u32 s[48:49], -1, v84\nv_max_u32 v84, v123, v127\nv_cmp_eq_u32 s[50:51], -1, v84\nv_max_u32 v84, v35, v39\n"
"v_cmp_eq_u32 s[52:53], -1, v84\nv_max_u32 v84, v43, v47\nv_cmp_eq_u32 s[54:55], -1, v84\nv_max_u32 v84, v51, v55\nv_cmp_eq_u32 s[56:57], -1, v84\nv_max_u32 v84, v59, v63\n"
"v_cmp_eq_u32 s[58:59], -1, v84\ns_or_b64 s[0:1], s[44:45], s[46:47]\ns_or_b64 s[2:3], s[48:49], s[50:51]\ns_or_b64 s[0:1], s[0:1], s[52:53]\ns_or_b64 s[2:3], s[2:3], s[54:55]\n"
"s_or_b64 s[0:1], s[0:1], s[56:57]\ns_or_b64 s[2:3], s[2:3], s[58:59]\ns_or_b64 s[0:1], s[0:1], s[2:3]\ns_cbranch_scc1 .Z253\n.Z254:\nZvvv 136,6,96\nMvvv 136,6,104\nMvvv 136,6,112\n"
"Mvvv 136,6,120\nMvvv 136,6,32\nMvvv 136,6,40\nMvvv 136,6,48\nMvvv 136,6,56\nZvvv 140,4,96\nMvvv 140,4,104\nMvvv 140,4,112\nMvvv 140,4,120\nMvvv 140,4,32\nMvvv 140,4,40\n"
"Mvvv 140,4,48\nMvvv 140,4,56\nZvvv 144,6,100\nMvvv 144,6,108\nMvvv 144,6,116\nMvvv 144,6,124\nMvvv 144,6,36\nMvvv 144,6,44\nMvvv 144,6,52\nMvvv 144,6,60\nZvvv 148,4,100\n"
"Mvvv 148,4,108\nMvvv 148,4,116\nMvvv 148,4,124\nMvvv 148,4,36\nMvvv 148,4,44\nMvvv 148,4,52\nMvvv 148,4,60\nFM 64,80,136\nFM 65,80,137\nFM 66,80,138\nFM 67,80,139\nFM 68,80,140\n"
"FM 69,80,141\nFM 70,80,142\nFM 71,80,143\nCV 12,64,65\nCV 13,66,67\nCV 14,68,69\nCV 15,70,71\nSv 12,91,36,22,0\nFM 72,82,144\nFM 73,82,145\nFM 74,82,146\nFM 75,82,147\n"
"FM 76,82,148\nFM 77,82,149\nFM 78,82,150\nFM 79,82,151\nCV 16,72,73\nCV 17,74,75\nCV 18,76,77\nCV 19,78,79\nSv 16,91,36,23,0\n.Z252:\ns_endpgm\n.EP264:\n.S144:\n"
"s_mov_b32 s7, 262144\n.S146:\nbuffer_load_dwordx4 v[32:35], v1, s[16:19], s6 offen offset:0 sc0 sc1\nbuffer_load_dwordx4 v[36:39], v1, s[16:19], s6 offen offset:1024 sc0 sc1\n"
"buffer_load_dwordx4 v[40:43], v1, s[16:19], s6 offen offset:2048 sc0 sc1\nbuffer_load_dwordx4 v[44:47], v1, s[16:19], s6 offen offset:3072 sc0 sc1\nWV 0\nv_xor_b32 v48, s15, v33\n"
"v_xor_b32 v49, s15, v35\nv_xor_b32 v50, s15, v37\nv_xor_b32 v51, s15, v39\nv_xor_b32 v52, s15, v41\nv_xor_b32 v53, s15, v43\nv_xor_b32 v54, s15, v45\nv_xor_b32 v55, s15, v47\n"
"v_or3_b32 v48, v48, v49, v50\nv_or3_b32 v51, v51, v52, v53\nv_or3_b32 v48, v48, v54, v55\nv_or_b32 v48, v48, v51\nv_cmp_ne_u32 vcc, 0, v48\ns_nop 0\ns_cbranch_vccz .S145\n"
"s_sleep 6\ns_sub_u32 s7, s7, 1\ns_cbranch_scc0 .S146\ns_branch .S145\n.S147:\ns_mov_b32 s7, 262144\n.S149:\ns_lshl_b32 s5, s73, 9\ns_add_u32 s5, s5, 0xc049400\n"
"v_lshrrev_b32 v27, 1, v1\nbuffer_load_dwordx2 v[22:23], v27, s[16:19], s5 offen sc0 sc1\nWV 0\nv_cmp_ne_u32 vcc, s15, v23\ns_nop 0\ns_cbranch_vccz .S148\ns_sleep 6\n"
"s_sub_u32 s7, s7, 1\ns_cbranch_scc0 .S149\ns_branch .S148\n.T153:\ns_mov_b32 s62, 1\ns_call_b64 s[80:81], .RA10\ns_branch .T154\n.T155:\ns_mov_b32 s62, 1\n"
"s_call_b64 s[80:81], .RA10\ns_branch .T156\n.T158:\ns_mov_b32 s62, 0\ns_call_b64 s[80:81], .RB11\ns_branch .T159\n.T160:\ns_mov_b32 s62, 0\ns_call_b64 s[80:81], .RA10\n"
"s_branch .T161\n.T163:\ns_mov_b32 s62, 0\ns_call_b64 s[80:81], .RB11\ns_branch .T164\n.T165:\ns_mov_b32 s62, 0\ns_call_b64 s[80:81], .RA10\ns_branch .T166\n.S168:\n"
"s_mov_b32 s7, 262144\n.S170:\nv_and_b32 v27, 0x70, v1\nbuffer_load_dword v27, v27, s[16:19], s61 offen offset:128 sc0 sc1\nWV 0\nv_cmp_ne_u32 vcc, s15, v27\ns_nop 0\n"
"s_cbranch_vccz .S169\ns_sleep 6\ns_sub_u32 s7, s7, 1\ns_cbranch_scc0 .S170\ns_branch .S169\n.S171:\ns_mov_b32 s7, 262144\n.S173:\nv_and_b32 v27, 0x70, v1\n"
"buffer_load_dword v27, v27, s[16:19], s61 offen offset:136 sc0 sc1\nWV 0\nv_cmp_ne_u32 vcc, s15, v27\ns_nop 0\ns_cbranch_vccz .S172\ns_sleep 6\ns_sub_u32 s7, s7, 1\n"
"s_cbranch_scc0 .S173\ns_branch .S172\n.S174:\ns_mov_b32 s7, 262144\n.S176:\nv_and_b32 v27, 0x70, v1\nbuffer_load_dword v27, v27, s[16:19], s61 offen offset:140 sc0 sc1\nWV 0\n"
"v_cmp_ne_u32 vcc, s15, v27\ns_nop 0\ns_cbranch_vccz .S175\ns_sleep 6\ns_sub_u32 s7, s7, 1\ns_cbranch_scc0 .S176\ns_branch .S175\n.S177:\ns_mov_b32 s7, 262144\n.S179:\n"
"v_and_b32 v27, 0x70, v1\nbuffer_load_dword v27, v27, s[16:19], s61 offen offset:12 sc0 sc1\nWV 0\nv_cmp_ne_u32 vcc, s15, v27\ns_nop 0\ns_cbranch_vccz .S178\ns_sleep 6\n"
"s_sub_u32 s7, s7, 1\ns_cbranch_scc0 .S179\ns_branch .S178\n.S180:\ns_mov_b32 s7, 262144\n.S182:\nv_and_b32 v27, 0x70, v1\n"
"buffer_load_dword v27, v27, s[16:19], s61 offen offset:8 sc0 sc1\nWV 0\nv_cmp_ne_u32 vcc, s15, v27\ns_nop 0\ns_cbranch_vccz .S181\ns_sleep 6\ns_sub_u32 s7, s7, 1\n"
"s_cbranch_scc0 .S182\ns_branch .S181\n.S183:\ns_mov_b32 s7, 262144\n.S185:\nv_and_b32 v27, 0xf0, v1\nbuffer_load_dword v27, v27, s[16:19], s61 offen offset:4 sc0 sc1\nWV 0\n"
"v_cmp_ne_u32 vcc, s15, v27\ns_nop 0\ns_cbranch_vccz .S184\ns_sleep 6\ns_sub_u32 s7, s7, 1\ns_cbranch_scc0 .S185\ns_branch .S184\n.S187:\ns_mov_b32 s7, 262144\n.S189:\n"
"v_lshrrev_b32 v104, 9, v1\nv_mul_u32_u24 v104, 0x18000, v104\nv_lshlrev_b32 v104, 8, v104\nv_and_b32 v105, 0x1f0, v1\nv_add_u32 v104, v104, v105\ns_mov_b32 s6, 0x15c0000\n"
"buffer_load_dword v96, v104, s[16:19], s6 offen offset:4 sc0 sc1\ns_mov_b32 s6, 0x15c2000\nbuffer_load_dword v97, v104, s[16:19], s6 offen offset:4 sc0 sc1\n"
"s_mov_b32 s6, 0x45c0000\nbuffer_load_dword v98, v104, s[16:19], s6 offen offset:4 sc0 sc1\ns_mov_b32 s6, 0x45c2000\n"
"buffer_load_dword v99, v104, s[16:19], s6 offen offset:4 sc0 sc1\ns_mov_b32 s6, 0x75c0000\nbuffer_load_dword v100, v104, s[16:19], s6 offen offset:4 sc0 sc1\n"
"s_mov_b32 s6, 0x75c2000\nbuffer_load_dword v101, v104, s[16:19], s6 offen offset:4 sc0 sc1\ns_mov_b32 s6, 0xa5c0000\n"
"buffer_load_dword v102, v104, s[16:19], s6 offen offset:4 sc0 sc1\ns_mov_b32 s6, 0xa5c2000\nbuffer_load_dword v103, v104, s[16:19], s6 offen offset:4 sc0 sc1\nWV 0\n"
"v_xor_b32 v96, s15, v96\nv_xor_b32 v97, s15, v97\nv_xor_b32 v98, s15, v98\nv_xor_b32 v99, s15, v99\nv_xor_b32 v100, s15, v100\nv_xor_b32 v101, s15, v101\n"
"v_xor_b32 v102, s15, v102\nv_xor_b32 v103, s15, v103\nv_min_u32 v96, v96, v97\nv_min_u32 v98, v98, v99\nv_min_u32 v100, v100, v101\nv_min_u32 v102, v102, v103\n"
"v_or3_b32 v96, v96, v98, v100\nv_or_b32 v96, v96, v102\nv_cmp_ne_u32 vcc, 0, v96\ns_nop 0\ns_cbranch_vccz .S188\ns_sleep 6\ns_sub_u32 s7, s7, 1\ns_cbranch_scc0 .S189\n"
"s_branch .S188\n.FF186:\nv_lshrrev_b32 v104, 9, v1\nv_mul_u32_u24 v104, 0x18000, v104\nv_lshlrev_b32 v104, 8, v104\nv_and_b32 v105, 0x1f0, v1\nv_add_u32 v104, v104, v105\n"
"s_mov_b32 s6, 0x15c0000\nbuffer_load_dword v96, v104, s[16:19], s6 offen offset:4 sc0 sc1\ns_mov_b32 s6, 0x15c2000\n"
"buffer_load_dword v97, v104, s[16:19], s6 offen offset:4 sc0 sc1\ns_mov_b32 s6, 0x45c0000\nbuffer_load_dword v98, v104, s[16:19], s6 offen offset:4 sc0 sc1\n"
"s_mov_b32 s6, 0x45c2000\nbuffer_load_dword v99, v104, s[16:19], s6 offen offset:4 sc0 sc1\ns_mov_b32 s6, 0x75c0000\n"
"buffer_load_dword v100, v104, s[16:19], s6 offen offset:4 sc0 sc1\ns_mov_b32 s6, 0x75c2000\nbuffer_load_dword v101, v104, s[16:19], s6 offen offset:4 sc0 sc1\n"
"s_mov_b32 s6, 0xa5c0000\nbuffer_load_dword v102, v104, s[16:19], s6 offen offset:4 sc0 sc1\ns_mov_b32 s6, 0xa5c2000\n"
"buffer_load_dword v103, v104, s[16:19], s6 offen offset:4 sc0 sc1\ns_mov_b32 s90, 0\ns_swappc_b64 s[8:9], s[80:81]\nv_xor_b32 v96, s15, v96\nv_xor_b32 v97, s15, v97\n"
"v_xor_b32 v98, s15, v98\nv_xor_b32 v99, s15, v99\nv_xor_b32 v100, s15, v100\nv_xor_b32 v101, s15, v101\nv_xor_b32 v102, s15, v102\nv_xor_b32 v103, s15, v103\n"
"v_min_u32 v96, v96, v97\nv_min_u32 v98, v98, v99\nv_min_u32 v100, v100, v101\nv_min_u32 v102, v102, v103\nv_or3_b32 v96, v96, v98, v100\nv_or_b32 v96, v96, v102\n"
"v_cmp_ne_u32 vcc, 0, v96\ns_nop 0\ns_cbranch_vccnz .S187\n.S188:\ns_and_b32 s4, s13, 1\ns_lshl1_add_u32 s4, s4, s11\ns_lshl2_add_u32 s4, s12, s4\ns_lshr_b32 s5, s71, 16\n"
"s_cmp_lt_u32 s4, 28\ns_cselect_b32 s5, s71, s5\ns_cselect_b32 s3, 0, 28\ns_and_b32 s5, s5, 0x7ff\ns_sub_u32 s3, s4, s3\ns_mul_i32 s2, s4, 7168\ns_add_u32 s2, s2, 0xc04c800\n"
"s_add_u32 s6, s2, 4096\nbuffer_load_dwordx4 v[96:99], v1, s[16:19], s2 offen offset:0 sc0 sc1\nbuffer_load_dwordx4 v[100:103], v1, s[16:19], s2 offen offset:1024 sc0 sc1\n"
"buffer_load_dwordx4 v[104:107], v1, s[16:19], s2 offen offset:2048 sc0 sc1\nbuffer_load_dwordx4 v[108:111], v1, s[16:19], s2 offen offset:3072 sc0 sc1\n"
"buffer_load_dwordx4 v[112:115], v1, s[16:19], s6 offen offset:0 sc0 sc1\nbuffer_load_dwordx4 v[116:119], v1, s[16:19], s6 offen offset:1024 sc0 sc1\n"
"buffer_load_dwordx4 v[120:123], v1, s[16:19], s6 offen offset:2048 sc0 sc1\nv_lshrrev_b32 v127, 1, v1\nv_and_b32 v127, 24, v127\ns_mul_i32 s2, s5, 896\ns_lshl_b32 s3, s3, 5\n"
"s_add_u32 s2, s2, s3\ns_lshl2_add_u32 s5, s5, 0xc03e000\nbuffer_load_dwordx2 v[124:125], v127, s[28:31], s2 offen\nbuffer_load_dword v126, off, s[16:19], s5 sc0 sc1\n"
"s_mov_b32 s90, 0\ns_swappc_b64 s[8:9], s[80:81]\nAD 96,96,100\nAD 97,97,101\nAD 98,98,102\nAD 99,99,103\nAD 104,104,108\nAD 105,105,109\nAD 106,106,110\nAD 107,107,111\n"
"AD 112,112,116\nAD 113,113,117\nAD 114,114,118\nAD 115,115,119\nAD 96,96,104\nAD 97,97,105\nAD 98,98,106\nAD 99,99,107\nAD 112,112,120\nAD 113,113,121\nAD 114,114,122\n"
"AD 115,115,123\nAD 96,96,112\nAD 97,97,113\nAD 98,98,114\nAD 99,99,115\nv_mov_b32 v100, v96\nv_mov_b32 v101, v97\nv_mov_b32 v102, v98\nv_mov_b32 v103, v99\n"
"v_permlane32_swap_b32 v96, v100\nv_permlane32_swap_b32 v97, v101\nv_permlane32_swap_b32 v98, v102\nv_permlane32_swap_b32 v99, v103\nAD 96,96,100\nAD 97,97,101\nAD 98,98,102\n"
"AD 99,99,103\nv_mov_b32 v100, v96\nv_mov_b32 v101, v97\nv_mov_b32 v102, v98\nv_mov_b32 v103, v99\nv_permlane16_swap_b32 v96, v100\nv_permlane16_swap_b32 v97, v101\n"
"v_permlane16_swap_b32 v98, v102\nv_permlane16_swap_b32 v99, v103\nAD 96,96,100\nAD 97,97,101\nAD 98,98,102\nAD 99,99,103\n"
"v_add_f32_dpp v96, v96, v96 row_ror:8 row_mask:0xf bank_mask:0xf\nv_add_f32_dpp v97, v97, v97 row_ror:8 row_mask:0xf bank_mask:0xf\n"
"v_add_f32_dpp v98, v98, v98 row_ror:8 row_mask:0xf bank_mask:0xf\nv_add_f32_dpp v99, v99, v99 row_ror:8 row_mask:0xf bank_mask:0xf\n"
"v_add_f32_dpp v96, v96, v96 row_ror:4 row_mask:0xf bank_mask:0xf\nv_add_f32_dpp v97, v97, v97 row_ror:4 row_mask:0xf bank_mask:0xf\n"
"v_add_f32_dpp v98, v98, v98 row_ror:4 row_mask:0xf bank_mask:0xf\nv_add_f32_dpp v99, v99, v99 row_ror:4 row_mask:0xf bank_mask:0xf\nv_mul_f32 v126, 0xbfb8aa3b, v126\nEX 126,126\n"
"CV 96,96,97\nv_add_f32 v126, 1.0, v126\nv_rcp_f32 v126, v126\nCV 98,98,99\nCV 126,126,126\nv_lshlrev_b32 v126, 16, v126\nv_and_b32 v97, 0xffff0000, v96\n"
"v_lshlrev_b32 v96, 16, v96\nv_and_b32 v99, 0xffff0000, v98\nv_lshlrev_b32 v98, 16, v98\nMU 96,96,126\nMU 97,97,126\nMU 98,98,126\nMU 99,99,126\nCV 96,96,97\nCV 98,98,99\n"
"v_and_b32 v97, 0xffff0000, v96\nv_lshlrev_b32 v96, 16, v96\nv_and_b32 v99, 0xffff0000, v98\nv_lshlrev_b32 v98, 16, v98\nv_lshlrev_b32 v100, 16, v124\n"
"v_and_b32 v101, 0xffff0000, v124\nv_lshlrev_b32 v102, 16, v125\nv_and_b32 v103, 0xffff0000, v125\nAD 96,96,100\nAD 97,97,101\nAD 98,98,102\nAD 99,99,103\nCV 96,96,97\nCV 97,98,99\n"
"v_mov_b32 v98, s15\nv_mov_b32 v99, s71\ns_and_b32 s4, s13, 1\ns_lshl1_add_u32 s4, s4, s11\ns_lshl2_add_u32 s4, s12, s4\ns_lshr_b32 s5, s71, 16\ns_cmp_lt_u32 s4, 28\n"
"s_cselect_b32 s5, s71, s5\ns_cselect_b32 s3, 0, 28\ns_and_b32 s5, s5, 0x7ff\ns_sub_u32 s3, s4, s3\nv_mov_b32 v100, 0x40000000\nv_cmp_gt_u32 vcc, 64, v1\ns_lshl_b32 s4, s4, 6\n"
"s_add_u32 s4, s4, 0xc04b800\nv_cndmask_b32 v100, v100, v1, vcc\nSv 96,100,16,4,0\ns_mov_b32 s90, 100\ns_swappc_b64 s[8:9], s[80:81]\n.T194:\ns_mov_b32 s62, 0\n"
"s_call_b64 s[80:81], .RB11\ns_branch .T195\n.T196:\ns_mov_b32 s62, 0\ns_call_b64 s[80:81], .RA10\ns_branch .T197\n.T201:\ns_mov_b32 s62, 0\ns_call_b64 s[80:81], .RB11\n"
"s_branch .T202\n.T203:\ns_mov_b32 s62, 0\ns_call_b64 s[80:81], .RA10\ns_branch .T204\n.T207:\ns_mov_b32 s62, 0\ns_call_b64 s[80:81], .RB11\ns_branch .T208\n.T209:\n"
"s_mov_b32 s62, 0\ns_call_b64 s[80:81], .RA10\ns_branch .T210\n.S215:\ns_mov_b32 s7, 262144\n.S217:\nbuffer_load_dwordx4 v[100:103], v108, s[16:19], s65 offen offset:0 sc0 sc1\n"
"buffer_load_dwordx4 v[104:107], v108, s[16:19], s65 offen offset:256 sc0 sc1\nWV 0\nv_xor_b32 v109, s15, v102\nv_xor_b32 v110, s15, v106\nv_or_b32 v109, v109, v110\n"
"v_cmp_ne_u32 vcc, 0, v109\ns_nop 0\ns_cbranch_vccz .S216\ns_sleep 6\ns_sub_u32 s7, s7, 1\ns_cbranch_scc0 .S217\ns_branch .S216\n.D213:\nv_mov_b32 v114, 0\nv_mov_b32 v115, 0\n"
"s_mov_b32 s63, 0xff800000\ns_mov_b32 s67, 1\ns_branch .D214\n.T219:\ns_lshl3_add_u32 s62, s67, 2\ns_call_b64 s[80:81], .RB11\ns_cmp_eq_u32 s67, 0\ns_cbranch_scc1 .T220\n"
"SU 20,100,88\nv_min_f32 v20, 0, v20\nEX 20,20\ns_nop 0\nFM 86,101,20\nSU 21,104,89\nv_min_f32 v21, 0, v21\nEX 21,21\ns_nop 0\nFM 87,105,21\nAR 22,0\n"
"v_and_b32 v23, 0xffff0000, v22\nv_lshlrev_b32 v22, 16, v22\nMU 22,22,20\nMU 23,23,20\nCV 22,22,23\nAW 0,22\nAR 22,1\nv_and_b32 v23, 0xffff0000, v22\nv_lshlrev_b32 v22, 16, v22\n"
"MU 22,22,20\nMU 23,23,20\nCV 22,22,23\nAW 1,22\nAR 22,2\nv_and_b32 v23, 0xffff0000, v22\nv_lshlrev_b32 v22, 16, v22\nMU 22,22,20\nMU 23,23,20\nCV 22,22,23\nAW 2,22\nAR 22,3\n"
"v_and_b32 v23, 0xffff0000, v22\nv_lshlrev_b32 v22, 16, v22\nMU 22,22,20\nMU 23,23,20\nCV 22,22,23\nAW 3,22\nAR 22,4\nv_and_b32 v23, 0xffff0000, v22\nv_lshlrev_b32 v22, 16, v22\n"
"MU 22,22,21\nMU 23,23,21\nCV 22,22,23\nAW 4,22\nAR 22,5\nv_and_b32 v23, 0xffff0000, v22\nv_lshlrev_b32 v22, 16, v22\nMU 22,22,21\nMU 23,23,21\nCV 22,22,23\nAW 5,22\nAR 22,6\n"
"v_and_b32 v23, 0xffff0000, v22\nv_lshlrev_b32 v22, 16, v22\nMU 22,22,21\nMU 23,23,21\nCV 22,22,23\nAW 6,22\nAR 22,7\nv_and_b32 v23, 0xffff0000, v22\nv_lshlrev_b32 v22, 16, v22\n"
"MU 22,22,21\nMU 23,23,21\nCV 22,22,23\nAW 7,22\nAR 22,8\nv_and_b32 v23, 0xffff0000, v22\nv_lshlrev_b32 v22, 16, v22\nMU 22,22,20\nMU 23,23,20\nCV 22,22,23\nAW 8,22\nAR 22,9\n"
"v_and_b32 v23, 0xffff0000, v22\nv_lshlrev_b32 v22, 16, v22\nMU 22,22,20\nMU 23,23,20\nCV 22,22,23\nAW 9,22\nAR 22,10\nv_and_b32 v23, 0xffff0000, v22\nv_lshlrev_b32 v22, 16, v22\n"
"MU 22,22,20\nMU 23,23,20\nCV 22,22,23\nAW 10,22\nAR 22,11\nv_and_b32 v23, 0xffff0000, v22\nv_lshlrev_b32 v22, 16, v22\nMU 22,22,20\nMU 23,23,20\nCV 22,22,23\nAW 11,22\nAR 22,12\n"
"v_and_b32 v23, 0xffff0000, v22\nv_lshlrev_b32 v22, 16, v22\nMU 22,22,21\nMU 23,23,21\nCV 22,22,23\nAW 12,22\nAR 22,13\nv_and_b32 v23, 0xffff0000, v22\nv_lshlrev_b32 v22, 16, v22\n"
"MU 22,22,21\nMU 23,23,21\nCV 22,22,23\nAW 13,22\nAR 22,14\nv_and_b32 v23, 0xffff0000, v22\nv_lshlrev_b32 v22, 16, v22\nMU 22,22,21\nMU 23,23,21\nCV 22,22,23\nAW 14,22\nAR 22,15\n"
"v_and_b32 v23, 0xffff0000, v22\nv_lshlrev_b32 v22, 16, v22\nMU 22,22,21\nMU 23,23,21\nCV 22,22,23\nAW 15,22\nAR 22,16\nv_and_b32 v23, 0xffff0000, v22\nv_lshlrev_b32 v22, 16, v22\n"
"MU 22,22,20\nMU 23,23,20\nCV 22,22,23\nAW 16,22\nAR 22,17\nv_and_b32 v23, 0xffff0000, v22\nv_lshlrev_b32 v22, 16, v22\nMU 22,22,20\nMU 23,23,20\nCV 22,22,23\nAW 17,22\nAR 22,18\n"
"v_and_b32 v23, 0xffff0000, v22\nv_lshlrev_b32 v22, 16, v22\nMU 22,22,20\nMU 23,23,20\nCV 22,22,23\nAW 18,22\nAR 22,19\nv_and_b32 v23, 0xffff0000, v22\nv_lshlrev_b32 v22, 16, v22\n"
"MU 22,22,20\nMU 23,23,20\nCV 22,22,23\nAW 19,22\nAR 22,20\nv_and_b32 v23, 0xffff0000, v22\nv_lshlrev_b32 v22, 16, v22\nMU 22,22,21\nMU 23,23,21\nCV 22,22,23\nAW 20,22\nAR 22,21\n"
"v_and_b32 v23, 0xffff0000, v22\nv_lshlrev_b32 v22, 16, v22\nMU 22,22,21\nMU 23,23,21\nCV 22,22,23\nAW 21,22\nAR 22,22\nv_and_b32 v23, 0xffff0000, v22\nv_lshlrev_b32 v22, 16, v22\n"
"MU 22,22,21\nMU 23,23,21\nCV 22,22,23\nAW 22,22\nAR 22,23\nv_and_b32 v23, 0xffff0000, v22\nv_lshlrev_b32 v22, 16, v22\nMU 22,22,21\nMU 23,23,21\nCV 22,22,23\nAW 23,22\nAR 22,24\n"
"v_and_b32 v23, 0xffff0000, v22\nv_lshlrev_b32 v22, 16, v22\nMU 22,22,20\nMU 23,23,20\nCV 22,22,23\nAW 24,22\nAR 22,25\nv_and_b32 v23, 0xffff0000, v22\nv_lshlrev_b32 v22, 16, v22\n"
"MU 22,22,20\nMU 23,23,20\nCV 22,22,23\nAW 25,22\nAR 22,26\nv_and_b32 v23, 0xffff0000, v22\nv_lshlrev_b32 v22, 16, v22\nMU 22,22,20\nMU 23,23,20\nCV 22,22,23\nAW 26,22\nAR 22,27\n"
"v_and_b32 v23, 0xffff0000, v22\nv_lshlrev_b32 v22, 16, v22\nMU 22,22,20\nMU 23,23,20\nCV 22,22,23\nAW 27,22\nAR 22,28\nv_and_b32 v23, 0xffff0000, v22\nv_lshlrev_b32 v22, 16, v22\n"
"MU 22,22,21\nMU 23,23,21\nCV 22,22,23\nAW 28,22\nAR 22,29\nv_and_b32 v23, 0xffff0000, v22\nv_lshlrev_b32 v22, 16, v22\nMU 22,22,21\nMU 23,23,21\nCV 22,22,23\nAW 29,22\nAR 22,30\n"
"v_and_b32 v23, 0xffff0000, v22\nv_lshlrev_b32 v22, 16, v22\nMU 22,22,21\nMU 23,23,21\nCV 22,22,23\nAW 30,22\nAR 22,31\nv_and_b32 v23, 0xffff0000, v22\nv_lshlrev_b32 v22, 16, v22\n"
"MU 22,22,21\nMU 23,23,21\nCV 22,22,23\nAW 31,22\nAR 22,32\nv_and_b32 v23, 0xffff0000, v22\nv_lshlrev_b32 v22, 16, v22\nMU 22,22,20\nMU 23,23,20\nCV 22,22,23\nAW 32,22\nAR 22,33\n"
"v_and_b32 v23, 0xffff0000, v22\nv_lshlrev_b32 v22, 16, v22\nMU 22,22,20\nMU 23,23,20\nCV 22,22,23\nAW 33,22\nAR 22,34\nv_and_b32 v23, 0xffff0000, v22\nv_lshlrev_b32 v22, 16, v22\n"
"MU 22,22,20\nMU 23,23,20\nCV 22,22,23\nAW 34,22\nAR 22,35\nv_and_b32 v23, 0xffff0000, v22\nv_lshlrev_b32 v22, 16, v22\nMU 22,22,20\nMU 23,23,20\nCV 22,22,23\nAW 35,22\nAR 22,36\n"
"v_and_b32 v23, 0xffff0000, v22\nv_lshlrev_b32 v22, 16, v22\nMU 22,22,21\nMU 23,23,21\nCV 22,22,23\nAW 36,22\nAR 22,37\nv_and_b32 v23, 0xffff0000, v22\nv_lshlrev_b32 v22, 16, v22\n"
"MU 22,22,21\nMU 23,23,21\nCV 22,22,23\nAW 37,22\nAR 22,38\nv_and_b32 v23, 0xffff0000, v22\nv_lshlrev_b32 v22, 16, v22\nMU 22,22,21\nMU 23,23,21\nCV 22,22,23\nAW 38,22\nAR 22,39\n"
"v_and_b32 v23, 0xffff0000, v22\nv_lshlrev_b32 v22, 16, v22\nMU 22,22,21\nMU 23,23,21\nCV 22,22,23\nAW 39,22\nAR 22,40\nv_and_b32 v23, 0xffff0000, v22\nv_lshlrev_b32 v22, 16, v22\n"
"MU 22,22,20\nMU 23,23,20\nCV 22,22,23\nAW 40,22\nAR 22,41\nv_and_b32 v23, 0xffff0000, v22\nv_lshlrev_b32 v22, 16, v22\nMU 22,22,20\nMU 23,23,20\nCV 22,22,23\nAW 41,22\nAR 22,42\n"
"v_and_b32 v23, 0xffff0000, v22\nv_lshlrev_b32 v22, 16, v22\nMU 22,22,20\nMU 23,23,20\nCV 22,22,23\nAW 42,22\nAR 22,43\nv_and_b32 v23, 0xffff0000, v22\nv_lshlrev_b32 v22, 16, v22\n"
"MU 22,22,20\nMU 23,23,20\nCV 22,22,23\nAW 43,22\nAR 22,44\nv_and_b32 v23, 0xffff0000, v22\nv_lshlrev_b32 v22, 16, v22\nMU 22,22,21\nMU 23,23,21\nCV 22,22,23\nAW 44,22\nAR 22,45\n"
"v_and_b32 v23, 0xffff0000, v22\nv_lshlrev_b32 v22, 16, v22\nMU 22,22,21\nMU 23,23,21\nCV 22,22,23\nAW 45,22\nAR 22,46\nv_and_b32 v23, 0xffff0000, v22\nv_lshlrev_b32 v22, 16, v22\n"
"MU 22,22,21\nMU 23,23,21\nCV 22,22,23\nAW 46,22\nAR 22,47\nv_and_b32 v23, 0xffff0000, v22\nv_lshlrev_b32 v22, 16, v22\nMU 22,22,21\nMU 23,23,21\nCV 22,22,23\nAW 47,22\nAR 22,48\n"
"v_and_b32 v23, 0xffff0000, v22\nv_lshlrev_b32 v22, 16, v22\nMU 22,22,20\nMU 23,23,20\nCV 22,22,23\nAW 48,22\nAR 22,49\nv_and_b32 v23, 0xffff0000, v22\nv_lshlrev_b32 v22, 16, v22\n"
"MU 22,22,20\nMU 23,23,20\nCV 22,22,23\nAW 49,22\nAR 22,50\nv_and_b32 v23, 0xffff0000, v22\nv_lshlrev_b32 v22, 16, v22\nMU 22,22,20\nMU 23,23,20\nCV 22,22,23\nAW 50,22\nAR 22,51\n"
"v_and_b32 v23, 0xffff0000, v22\nv_lshlrev_b32 v22, 16, v22\nMU 22,22,20\nMU 23,23,20\nCV 22,22,23\nAW 51,22\nAR 22,52\nv_and_b32 v23, 0xffff0000, v22\nv_lshlrev_b32 v22, 16, v22\n"
"MU 22,22,21\nMU 23,23,21\nCV 22,22,23\nAW 52,22\nAR 22,53\nv_and_b32 v23, 0xffff0000, v22\nv_lshlrev_b32 v22, 16, v22\nMU 22,22,21\nMU 23,23,21\nCV 22,22,23\nAW 53,22\nAR 22,54\n"
"v_and_b32 v23, 0xffff0000, v22\nv_lshlrev_b32 v22, 16, v22\nMU 22,22,21\nMU 23,23,21\nCV 22,22,23\nAW 54,22\nAR 22,55\nv_and_b32 v23, 0xffff0000, v22\nv_lshlrev_b32 v22, 16, v22\n"
"MU 22,22,21\nMU 23,23,21\nCV 22,22,23\nAW 55,22\nAR 22,56\nv_and_b32 v23, 0xffff0000, v22\nv_lshlrev_b32 v22, 16, v22\nMU 22,22,20\nMU 23,23,20\nCV 22,22,23\nAW 56,22\nAR 22,57\n"
"v_and_b32 v23, 0xffff0000, v22\nv_lshlrev_b32 v22, 16, v22\nMU 22,22,20\nMU 23,23,20\nCV 22,22,23\nAW 57,22\nAR 22,58\nv_and_b32 v23, 0xffff0000, v22\nv_lshlrev_b32 v22, 16, v22\n"
"MU 22,22,20\nMU 23,23,20\nCV 22,22,23\nAW 58,22\nAR 22,59\nv_and_b32 v23, 0xffff0000, v22\nv_lshlrev_b32 v22, 16, v22\nMU 22,22,20\nMU 23,23,20\nCV 22,22,23\nAW 59,22\nAR 22,60\n"
"v_and_b32 v23, 0xffff0000, v22\nv_lshlrev_b32 v22, 16, v22\nMU 22,22,21\nMU 23,23,21\nCV 22,22,23\nAW 60,22\nAR 22,61\nv_and_b32 v23, 0xffff0000, v22\nv_lshlrev_b32 v22, 16, v22\n"
"MU 22,22,21\nMU 23,23,21\nCV 22,22,23\nAW 61,22\nAR 22,62\nv_and_b32 v23, 0xffff0000, v22\nv_lshlrev_b32 v22, 16, v22\nMU 22,22,21\nMU 23,23,21\nCV 22,22,23\nAW 62,22\nAR 22,63\n"
"v_and_b32 v23, 0xffff0000, v22\nv_lshlrev_b32 v22, 16, v22\nMU 22,22,21\nMU 23,23,21\nCV 22,22,23\nAW 63,22\nv_lshrrev_b32 v22, 8, v1\nv_bfe_u32 v23, v1, 6, 2\n"
"v_cmp_eq_u32 vcc, v22, v23\nv_and_b32 v22, 16, v1\nv_mov_b32 v23, 0x3f80\nv_cndmask_b32 v23, 0, v23, vcc\nv_lshlrev_b32 v23, v22, v23\nv_and_b32 v22, 32, v1\n"
"v_cmp_eq_u32 vcc, 0, v22\nv_mov_b32 v112, 0\nv_mov_b32 v113, 0\nv_cndmask_b32 v114, 0, v23, vcc\nv_sub_u32 v115, v23, v114\nv_mov_b32 v116, 0\nv_mov_b32 v117, 0\ns_nop 1\n"
"Mvva 128,114,0\nMvva 136,112,0\nMvva 140,112,4\nMvva 132,114,4\nMvva 144,114,8\nMvva 152,112,8\nMvva 156,112,12\nMvva 148,114,12\nMvva 160,114,16\nMvva 168,112,16\n"
"Mvva 172,112,20\nMvva 164,114,20\nMvva 176,114,24\nMvva 184,112,24\nMvva 188,112,28\nMvva 180,114,28\nMvva 192,114,32\nMvva 200,112,32\nMvva 204,112,36\nMvva 196,114,36\n"
"Mvva 208,114,40\nMvva 216,112,40\nMvva 220,112,44\nMvva 212,114,44\nMvva 224,114,48\nMvva 232,112,48\nMvva 236,112,52\nMvva 228,114,52\nMvva 240,114,56\nMvva 248,112,56\n"
"Mvva 252,112,60\nMvva 244,114,60\ns_nop 7\ns_branch .T220\n.S222:\ns_mov_b32 s7, 262144\n.S224:\ns_add_u32 s0, s92, 0\n"
"buffer_load_dwordx4 v[208:211], v1, s[16:19], s0 offen offset:0 sc0 sc1\nbuffer_load_dwordx4 v[212:215], v1, s[16:19], s0 offen offset:1024 sc0 sc1\nWV 0\n"
"v_max_u32 v21, v211, v215\nv_cmp_eq_u32 vcc, -1, v21\ns_nop 0\ns_cbranch_vccz .S223\ns_sleep 6\ns_sub_u32 s7, s7, 1\ns_cbranch_scc0 .S224\ns_branch .S223\n.S225:\n"
"s_mov_b32 s7, 262144\n.S227:\ns_add_u32 s0, s92, 131072\nbuffer_load_dwordx4 v[216:219], v1, s[16:19], s0 offen offset:0 sc0 sc1\n"
"buffer_load_dwordx4 v[220:223], v1, s[16:19], s0 offen offset:1024 sc0 sc1\nWV 0\nv_max_u32 v21, v219, v223\nv_cmp_eq_u32 vcc, -1, v21\ns_nop 0\ns_cbranch_vccz .S226\ns_sleep 6\n"
"s_sub_u32 s7, s7, 1\ns_cbranch_scc0 .S227\ns_branch .S226\n.S228:\ns_mov_b32 s7, 262144\n.S230:\ns_add_u32 s0, s92, 262144\n"
"buffer_load_dwordx4 v[224:227], v1, s[16:19], s0 offen offset:0 sc0 sc1\nbuffer_load_dwordx4 v[228:231], v1, s[16:19], s0 offen offset:1024 sc0 sc1\nWV 0\n"
"v_max_u32 v21, v227, v231\nv_cmp_eq_u32 vcc, -1, v21\ns_nop 0\ns_cbranch_vccz .S229\ns_sleep 6\ns_sub_u32 s7, s7, 1\ns_cbranch_scc0 .S230\ns_branch .S229\n.S231:\n"
"s_mov_b32 s7, 262144\n.S233:\ns_add_u32 s0, s92, 393216\nbuffer_load_dwordx4 v[208:211], v1, s[16:19], s0 offen offset:0 sc0 sc1\n"
"buffer_load_dwordx4 v[212:215], v1, s[16:19], s0 offen offset:1024 sc0 sc1\nWV 0\nv_max_u32 v21, v211, v215\nv_cmp_eq_u32 vcc, -1, v21\ns_nop 0\ns_cbranch_vccz .S232\ns_sleep 6\n"
"s_sub_u32 s7, s7, 1\ns_cbranch_scc0 .S233\ns_branch .S232\n.S234:\ns_mov_b32 s7, 262144\n.S236:\ns_add_u32 s0, s92, 524288\n"
"buffer_load_dwordx4 v[216:219], v1, s[16:19], s0 offen offset:0 sc0 sc1\nbuffer_load_dwordx4 v[220:223], v1, s[16:19], s0 offen offset:1024 sc0 sc1\nWV 0\n"
"v_max_u32 v21, v219, v223\nv_cmp_eq_u32 vcc, -1, v21\ns_nop 0\ns_cbranch_vccz .S235\ns_sleep 6\ns_sub_u32 s7, s7, 1\ns_cbranch_scc0 .S236\ns_branch .S235\n.S237:\n"
"s_mov_b32 s7, 262144\n.S239:\ns_add_u32 s0, s92, 655360\nbuffer_load_dwordx4 v[224:227], v1, s[16:19], s0 offen offset:0 sc0 sc1\n"
"buffer_load_dwordx4 v[228:231], v1, s[16:19], s0 offen offset:1024 sc0 sc1\nWV 0\nv_max_u32 v21, v227, v231\nv_cmp_eq_u32 vcc, -1, v21\ns_nop 0\ns_cbranch_vccz .S238\ns_sleep 6\n"
"s_sub_u32 s7, s7, 1\ns_cbranch_scc0 .S239\ns_branch .S238\n.S240:\ns_mov_b32 s7, 262144\n.S242:\ns_add_u32 s0, s92, 786432\n"
"buffer_load_dwordx4 v[208:211], v1, s[16:19], s0 offen offset:0 sc0 sc1\nbuffer_load_dwordx4 v[212:215], v1, s[16:19], s0 offen offset:1024 sc0 sc1\nWV 0\n"
"v_max_u32 v21, v211, v215\nv_cmp_eq_u32 vcc, -1, v21\ns_nop 0\ns_cbranch_vccz .S241\ns_sleep 6\ns_sub_u32 s7, s7, 1\ns_cbranch_scc0 .S242\ns_branch .S241\n.S243:\n"
"s_mov_b32 s7, 262144\n.S245:\nbuffer_load_dwordx4 v[64:67], v89, s[16:19], s40 offen offset:0 sc0 sc1\nbuffer_load_dwordx4 v[68:71], v89, s[16:19], s40 offen offset:16 sc0 sc1\n"
"buffer_load_dwordx4 v[72:75], v89, s[16:19], s40 offen offset:1792 sc0 sc1\nbuffer_load_dwordx4 v[76:79], v89, s[16:19], s40 offen offset:1808 sc0 sc1\n"
"buffer_load_dwordx2 v[80:81], v88, s[16:19], s63 offen offset:0 sc0 sc1\nbuffer_load_dwordx2 v[82:83], v88, s[16:19], s63 offen offset:512 sc0 sc1\nWV 0\nv_xor_b32 v66, s15, v66\n"
"v_xor_b32 v70, s15, v70\nv_xor_b32 v74, s15, v74\nv_xor_b32 v78, s15, v78\nv_xor_b32 v81, s15, v81\nv_xor_b32 v83, s15, v83\nv_or3_b32 v92, v66, v70, v74\n"
"v_or3_b32 v92, v92, v78, v81\nv_or_b32 v92, v92, v83\nv_cmp_ne_u32 vcc, 0, v92\ns_nop 0\ns_cbranch_vccz .S244\ns_sleep 6\ns_sub_u32 s7, s7, 1\ns_cbranch_scc0 .S245\n"
"s_branch .S244\n.S246:\ns_mov_b32 s7, 262144\n.S248:\nbuffer_load_dword v27, off, s[16:19], s60 offset:512 sc0 sc1\nWV 0\nv_cmp_ne_u32 vcc, s15, v27\ns_nop 0\n"
"s_cbranch_vccz .S247\ns_sleep 6\ns_sub_u32 s7, s7, 1\ns_cbranch_scc0 .S248\ns_branch .S247\n.S249:\ns_mov_b32 s7, 262144\n.S251:\n"
"buffer_load_dword v27, v24, s[16:19], s41 offen sc0 sc1\nWV 0\nv_xor_b32 v27, s15, v27\nv_and_b32 v26, 0xffffdfff, v27\nv_cmp_ne_u32 s[64:65], 0, v27\nv_cmp_ne_u32 vcc, 0, v26\n"
"s_nop 0\ns_cbranch_vccz .S250\ns_sleep 6\ns_sub_u32 s7, s7, 1\ns_cbranch_scc0 .S251\ns_branch .S250\n.Z253:\ns_sub_u32 s7, s7, 1\ns_cbranch_scc1 .Z254\ns_cmp_lg_u64 s[44:45], 0\n"
"s_cbranch_scc0 .Z256\nbuffer_load_dwordx4 v[96:99], v1, s[16:19], s24 offen offset:0 sc0 sc1\nbuffer_load_dwordx4 v[100:103], v1, s[16:19], s24 offen offset:1024 sc0 sc1\n.Z256:\n"
"s_cmp_lg_u64 s[46:47], 0\ns_cbranch_scc0 .Z257\nbuffer_load_dwordx4 v[104:107], v1, s[16:19], s25 offen offset:0 sc0 sc1\n"
"buffer_load_dwordx4 v[108:111], v1, s[16:19], s25 offen offset:1024 sc0 sc1\n.Z257:\ns_cmp_lg_u64 s[48:49], 0\ns_cbranch_scc0 .Z258\n"
"buffer_load_dwordx4 v[112:115], v1, s[16:19], s26 offen offset:0 sc0 sc1\nbuffer_load_dwordx4 v[116:119], v1, s[16:19], s26 offen offset:1024 sc0 sc1\n.Z258:\n"
"s_cmp_lg_u64 s[50:51], 0\ns_cbranch_scc0 .Z259\nbuffer_load_dwordx4 v[120:123], v1, s[16:19], s27 offen offset:0 sc0 sc1\n"
"buffer_load_dwordx4 v[124:127], v1, s[16:19], s27 offen offset:1024 sc0 sc1\n.Z259:\ns_cmp_lg_u64 s[52:53], 0\ns_cbranch_scc0 .Z260\n"
"buffer_load_dwordx4 v[32:35], v1, s[16:19], s32 offen offset:0 sc0 sc1\nbuffer_load_dwordx4 v[36:39], v1, s[16:19], s32 offen offset:1024 sc0 sc1\n.Z260:\n"
"s_cmp_lg_u64 s[54:55], 0\ns_cbranch_scc0 .Z261\nbuffer_load_dwordx4 v[40:43], v1, s[16:19], s33 offen offset:0 sc0 sc1\n"
"buffer_load_dwordx4 v[44:47], v1, s[16:19], s33 offen offset:1024 sc0 sc1\n.Z261:\ns_cmp_lg_u64 s[56:57], 0\ns_cbranch_scc0 .Z262\n"
"buffer_load_dwordx4 v[48:51], v1, s[16:19], s34 offen offset:0 sc0 sc1\nbuffer_load_dwordx4 v[52:55], v1, s[16:19], s34 offen offset:1024 sc0 sc1\n.Z262:\n"
"s_cmp_lg_u64 s[58:59], 0\ns_cbranch_scc0 .Z263\nbuffer_load_dwordx4 v[56:59], v1, s[16:19], s35 offen offset:0 sc0 sc1\n"
"buffer_load_dwordx4 v[60:63], v1, s[16:19], s35 offen offset:1024 sc0 sc1\n.Z263:\ns_sleep 8\nWV 0\ns_branch .Z255\n.p2align 12\n.PA4:\ns_mov_b64 exec, 0\ns_cmp_eq_u32 s11, 1\n"
"s_cbranch_scc1 .W568\ns_cmp_eq_u32 s11, 2\ns_cbranch_scc1 .W569\ns_cmp_eq_u32 s11, 3\ns_cbranch_scc1 .W570\ns_branch .W567\n.WB273:\ns_mov_b64 exec, -1\ns_add_u32 m0, s47, -49152\n"
"s_add_u32 s48, s48, 128\nD0 2,20,48,0\nD0 3,20,48,1024\nD0 4,20,48,2048\nD0 5,20,48,3072\ns_and_b32 s86, s74, 15\ns_lshl2_add_u32 s86, s86, s11\ns_mov_b32 s88, 0\n"
"s_lshl_b32 s93, s73, 9\ns_lshl3_add_u32 s93, s86, s93\ns_add_u32 s93, s93, 0xc049400\ns_lshl_b32 s86, s86, 14\ns_mul_i32 s91, s73, 25165824\ns_add_u32 s86, s86, s91\n"
"s_and_b32 s92, s13, 1\ns_lshl_b32 s92, s92, 13\ns_add_u32 s86, s86, s92\ns_add_u32 s94, s86, 0x1480000\ns_xor_b32 s95, s94, 0x2000\ns_add_u32 s86, s94, 512\n"
"s_load_dword s87, s[54:55], s86\ns_add_u32 m0, s47, -32768\ns_add_u32 s45, s45, 128\nD0 2,20,45,0\nD0 3,20,45,1024\nD0 4,20,45,2048\nD0 5,20,45,3072\ns_add_u32 m0, s47, 32768\n"
"s_add_u32 s44, s44, 0x40000\nD0 6,24,44,0\nD0 7,24,44,1024\nD0 8,24,44,2048\nD0 9,24,44,3072\ns_add_u32 m0, s47, 49152\ns_add_u32 s44, s44, 0x40000\nD0 6,24,44,0\nD0 7,24,44,1024\n"
"D0 8,24,44,2048\nD0 9,24,44,3072\ns_add_u32 m0, s47, -16384\ns_add_u32 s48, s48, 128\nD0 2,20,48,0\nD0 3,20,48,1024\nD0 4,20,48,2048\nD0 5,20,48,3072\nv_mov_b32 v52, 0x40000000\n"
"v_cmp_gt_u32 vcc, 56, v0\ns_lshl2_add_u32 s2, s10, s11\ns_mul_i32 s2, s2, 1792\nv_cndmask_b32 v53, v52, v1, vcc\ns_mov_b32 s36, s56\ns_and_b32 s37, s57, 0xffff\n"
"s_mov_b32 s38, 0x380\ns_mov_b32 s39, 0x20000\ns_mov_b32 s40, s58\ns_and_b32 s41, s59, 0xffff\ns_mov_b32 s42, 0x380\ns_mov_b32 s43, 0x20000\n"
"buffer_load_dwordx4 v[32:35], v53, s[28:31], s2 offen offset:0\nbuffer_load_dwordx4 v[36:39], v53, s[28:31], s2 offen offset:896\n"
"buffer_load_dwordx4 v[40:43], v53, s[36:39], 0 offen offset:0\nbuffer_load_dwordx4 v[44:47], v53, s[40:43], 0 offen offset:0\nWL 0\ns_mul_i32 s15, s15, 1664525\n"
"s_add_u32 s15, s15, 1013904223\nv_mov_b32 v25, s15\ns_mov_b32 s57, 0x3db8aa3b\ns_and_b32 s59, s11, 1\ns_lshl_b32 s59, s59, 13\ns_mul_i32 s2, s73, 25165824\ns_and_b32 s3, s12, 1\n"
"s_lshl_b32 s3, s3, 21\ns_add_u32 s2, s2, s3\ns_lshl2_add_u32 s3, s13, s11\ns_lshl_b32 s3, s3, 15\ns_add_u32 s2, s2, s3\ns_add_u32 s56, s2, 0x800000\ns_xor_b32 s53, s56, 0xc000\n"
"buffer_load_dword v31, v24, s[24:27], s96 offen\ns_add_u32 s96, s96, 0x80000\nbuffer_load_dword v31, v24, s[24:27], s96 offen\ns_add_u32 s96, s96, 0x80000\n"
"v_lshrrev_b32 v60, 3, v0\nv_and_b32 v61, 0x70, v1\nv_lshl_or_b32 v60, v60, 10, v61\nv_mov_b32 v20, 0\nv_mov_b32 v21, 0\nv_mov_b32 v22, 0\nv_mov_b32 v23, 0\nSv 20,60,16,94,0\n"
"Sv 20,60,16,95,0\nWV 28\ns_barrier\nRa 128,10,0\nRa 132,11,0\nRa 136,10,2048\nRa 140,11,2048\nRa 144,10,8192\nRa 148,11,8192\nRa 152,10,10240\nRa 156,11,10240\nTa 160,12,0\n"
"Ta 162,12,2048\nTa 164,12,16384\nTa 166,12,18432\nTa 168,13,0\nTa 170,13,2048\nTa 172,13,16384\nTa 174,13,18432\nTa 176,14,0\nTa 178,14,2048\nTa 180,14,16384\nTa 182,14,18432\n"
"Ta 184,15,0\nTa 186,15,2048\nTa 188,15,16384\nTa 190,15,18432\nWL 0\ns_barrier\nZvaa 64,160,128\nTa 224,16,0\nMvaa 64,164,132\nTa 226,16,2048\nZvaa 96,164,140\n"
"s_add_u32 m0, s47, -65536\ns_add_u32 s45, s45, 128\nMvaa 96,160,136\nTa 228,16,16384\nZvaa 128,160,144\nTa 230,16,18432\nMvaa 128,164,148\nD0 2,20,45,0\nZvaa 160,164,156\n"
"Ta 232,17,0\nMvaa 160,160,152\nTa 234,17,2048\nZvaa 164,168,152\ns_mov_b32 s36, s60\nMvaa 164,172,156\nTa 236,17,16384\nZvaa 132,172,148\nTa 238,17,18432\nMvaa 132,168,144\n"
"s_and_b32 s37, s61, 0xffff\nZvaa 100,168,136\nTa 240,18,0\nMvaa 100,172,140\nTa 242,18,2048\nZvaa 68,172,132\nD0 3,20,45,1024\nMvaa 68,168,128\nTa 244,18,16384\nZvaa 72,176,128\n"
"Ta 246,18,18432\nMvaa 72,180,132\ns_mov_b32 s38, 0x380000\nZvaa 104,180,140\nTa 248,19,0\nMvaa 104,176,136\nTa 250,19,2048\nZvaa 136,176,144\ns_mov_b32 s39, 0x20000\n"
"Mvaa 136,180,148\nTa 252,19,16384\nZvaa 168,180,156\nTa 254,19,18432\nMvaa 168,176,152\nD0 4,20,45,2048\nZvaa 172,184,152\ns_lshl3_add_u32 s75, s12, s13\nMvaa 172,188,156\n"
"s_lshl2_add_u32 s75, s75, s11\nZvaa 140,188,148\ns_mul_i32 s75, s75, 7168\nMvaa 140,184,144\ns_mul_i32 s76, s11, 7168\nZvaa 108,184,136\ns_add_u32 s76, s76, 131072\n"
"Mvaa 108,188,140\nD0 5,20,45,3072\nZvaa 76,188,132\ns_mov_b32 s42, 1605632\nMvaa 76,184,128\ns_waitcnt vmcnt(24) lgkmcnt(0)\ns_barrier\nZvaa 80,224,128\nRa 192,10,16384\n"
"Mvaa 80,228,132\ns_add_u32 m0, s47, 0\ns_add_u32 s44, s44, 0x40000\nZvaa 112,228,140\nRa 196,11,16384\nMvaa 112,224,136\nZvaa 144,224,144\nRa 200,10,18432\nMvaa 144,228,148\n"
"D0 6,24,44,0\nZvaa 176,228,156\nRa 204,11,18432\nMvaa 176,224,152\nZvaa 180,232,152\nRa 208,10,24576\nMvaa 180,236,156\ns_and_b32 s78, s74, 3\ns_lshl2_add_u32 s78, s73, s78\n"
"s_lshl_b32 s78, s78, 12\ns_bitcmp1_b32 s11, 0\ns_cselect_b32 s79, s48, s45\ns_add_u32 s79, s79, 512\ns_and_b32 s79, s79, 0xfffe0000\ns_add_u32 s78, s78, s79\n"
"buffer_load_dword v31, v26, s[20:23], s78 offen\nZvaa 148,236,148\nRa 212,11,24576\nMvaa 148,232,144\nZvaa 116,232,136\nRa 216,10,26624\nMvaa 116,236,140\nD0 7,24,44,1024\n"
"Zvaa 84,236,132\nRa 220,11,26624\nMvaa 84,232,128\nZvaa 88,240,128\nMvaa 88,244,132\nZvaa 120,244,140\nMvaa 120,240,136\nZvaa 152,240,144\nMvaa 152,244,148\nD0 8,24,44,2048\n"
"Zvaa 184,244,156\nMvaa 184,240,152\nZvaa 188,248,152\nMvaa 188,252,156\nZvaa 156,252,148\nMvaa 156,248,144\nZvaa 124,248,136\nMvaa 124,252,140\nD0 9,24,44,3072\nZvaa 92,252,132\n"
"Mvaa 92,248,128\nWL 0\nZaaa 16,224,192\nRa 128,10,32768\nMaaa 16,228,196\ns_add_u32 m0, s47, 16384\ns_add_u32 s44, s44, 0x40000\nZaaa 48,228,204\nRa 132,11,32768\nMaaa 48,224,200\n"
"Zaaa 80,224,208\nRa 136,10,34816\nMaaa 80,228,212\nD0 6,24,44,0\nZaaa 112,228,220\nRa 140,11,34816\nMaaa 112,224,216\nZaaa 116,232,216\nRa 144,10,40960\nMaaa 116,236,220\n"
"Zaaa 84,236,212\nRa 148,11,40960\nMaaa 84,232,208\nZaaa 52,232,200\nRa 152,10,43008\nMaaa 52,236,204\nD0 7,24,44,1024\nZaaa 20,236,196\nRa 156,11,43008\nMaaa 20,232,192\n"
"Zaaa 24,240,192\nMaaa 24,244,196\nZaaa 56,244,204\nMaaa 56,240,200\nZaaa 88,240,208\nMaaa 88,244,212\nD0 8,24,44,2048\nZaaa 120,244,220\nMaaa 120,240,216\nZaaa 124,248,216\n"
"Maaa 124,252,220\nZaaa 92,252,212\nMaaa 92,248,208\nZaaa 60,248,200\nMaaa 60,252,204\nD0 9,24,44,3072\nZaaa 28,252,196\nMaaa 28,248,192\ns_waitcnt vmcnt(25) lgkmcnt(0)\ns_barrier\n"
"Zaaa 0,160,192\nTa 224,16,32768\nWV 17\nMaaa 0,164,196\nTa 226,16,34816\nZaaa 32,164,204\ns_add_u32 m0, s47, -49152\ns_add_u32 s48, s48, 128\nMaaa 32,160,200\nTa 228,16,49152\n"
"Zaaa 64,160,208\nTa 230,16,51200\nbuffer_load_dword v31, v24, s[24:27], s96 offen\ns_add_u32 s96, s96, 0x80000\nMaaa 64,164,212\nD0 2,20,48,0\nv_lshlrev_b32 v48, 16, v32\n"
"Zaaa 96,164,220\nTa 232,17,32768\nMaaa 96,160,216\nTa 234,17,34816\nv_lshlrev_b32 v49, 16, v36\nZaaa 100,168,216\nv_lshlrev_b32 v50, 16, v40\nv_lshlrev_b32 v51, 16, v44\n"
"Maaa 100,172,220\nTa 236,17,49152\nZaaa 68,172,212\nTa 238,17,51200\nMU 52,48,50\nMaaa 68,168,208\nMU 53,49,50\nZaaa 36,168,200\nTa 240,18,32768\nCV 52,52,53\nMaaa 36,172,204\n"
"Ta 242,18,34816\nv_lshlrev_b32 v54, 16, v52\nZaaa 4,172,196\nD0 3,20,48,1024\nMaaa 4,168,192\nTa 244,18,49152\nv_and_b32 v55, 0xffff0000, v52\nZaaa 8,176,192\nTa 246,18,51200\n"
"MU 56,48,51\nMaaa 8,180,196\nMU 57,49,51\nZaaa 40,180,204\nTa 248,19,32768\nv_and_b32 v48, 0xffff0000, v32\nMaaa 40,176,200\nTa 250,19,34816\nZaaa 72,176,208\n"
"v_and_b32 v49, 0xffff0000, v36\nv_and_b32 v50, 0xffff0000, v40\nMaaa 72,180,212\nTa 252,19,49152\nv_and_b32 v51, 0xffff0000, v44\nZaaa 104,180,220\nTa 254,19,51200\n"
"Maaa 104,176,216\nD0 4,20,48,2048\nMU 52,48,50\nZaaa 108,184,216\nMU 53,49,50\nCV 52,52,53\nMaaa 108,188,220\nv_and_b32 v53, 0xffff0000, v52\nZaaa 76,188,212\n"
"v_lshlrev_b32 v52, 16, v52\nFM 56,48,51\nMaaa 76,184,208\nFM 57,49,51\nZaaa 44,184,200\nAD 54,54,52\nAD 55,55,53\nMaaa 44,188,204\nD0 5,20,48,3072\nv_lshlrev_b32 v48, 16, v33\n"
"Zaaa 12,188,196\nv_lshlrev_b32 v49, 16, v37\nMaaa 12,184,192\nWL 0\nMvaa 80,224,128\nTa 160,12,32768\nv_lshlrev_b32 v50, 16, v41\nMvaa 80,228,132\nTa 162,12,34816\n"
"Mvaa 112,228,140\ns_add_u32 m0, s47, -32768\ns_add_u32 s45, s45, 128\nMvaa 112,224,136\nTa 164,12,49152\nMvaa 144,224,144\nTa 166,12,51200\nv_lshlrev_b32 v51, 16, v45\n"
"Mvaa 144,228,148\nD0 2,20,45,0\nMU 52,48,50\nMvaa 176,228,156\nTa 168,13,32768\nMvaa 176,224,152\nTa 170,13,34816\nMU 53,49,50\nMvaa 180,232,152\nCV 52,52,53\n"
"v_and_b32 v53, 0xffff0000, v52\nMvaa 180,236,156\nTa 172,13,49152\nMvaa 148,236,148\nTa 174,13,51200\nv_lshlrev_b32 v52, 16, v52\nMvaa 148,232,144\nFM 56,48,51\nMvaa 116,232,136\n"
"Ta 176,14,32768\nFM 57,49,51\nMvaa 116,236,140\nTa 178,14,34816\nAD 54,54,52\nMvaa 84,236,132\nD0 3,20,45,1024\nMvaa 84,232,128\nTa 180,14,49152\nAD 55,55,53\nMvaa 88,240,128\n"
"Ta 182,14,51200\nv_and_b32 v48, 0xffff0000, v33\nMvaa 88,244,132\nv_and_b32 v49, 0xffff0000, v37\nMvaa 120,244,140\nTa 184,15,32768\nv_and_b32 v50, 0xffff0000, v41\n"
"Mvaa 120,240,136\nTa 186,15,34816\nMvaa 152,240,144\nv_and_b32 v51, 0xffff0000, v45\nMU 52,48,50\nMvaa 152,244,148\nTa 188,15,49152\nMU 53,49,50\nMvaa 184,244,156\n"
"Ta 190,15,51200\nMvaa 184,240,152\nD0 4,20,45,2048\nCV 52,52,53\nMvaa 188,248,152\nv_and_b32 v53, 0xffff0000, v52\nv_lshlrev_b32 v52, 16, v52\nMvaa 188,252,156\nFM 56,48,51\n"
"Mvaa 156,252,148\nFM 57,49,51\nAD 54,54,52\nMvaa 156,248,144\nAD 55,55,53\nMvaa 124,248,136\nv_lshlrev_b32 v48, 16, v34\nv_lshlrev_b32 v49, 16, v38\nMvaa 124,252,140\n"
"D0 5,20,45,3072\nv_lshlrev_b32 v50, 16, v42\nMvaa 92,252,132\nv_lshlrev_b32 v51, 16, v46\nMvaa 92,248,128\ns_waitcnt vmcnt(18) lgkmcnt(0)\ns_barrier\nMvaa 64,160,128\n"
"Ra 192,10,49152\nMU 52,48,50\nMvaa 64,164,132\ns_add_u32 m0, s47, 32768\ns_add_u32 s44, s44, 0x40000\nMvaa 96,164,140\nRa 196,11,49152\nMU 53,49,50\nMvaa 96,160,136\nCV 52,52,53\n"
"Mvaa 128,160,144\nRa 200,10,51200\nv_and_b32 v53, 0xffff0000, v52\nMvaa 128,164,148\nD0 6,24,44,0\nv_lshlrev_b32 v52, 16, v52\nMvaa 160,164,156\nRa 204,11,51200\nMvaa 160,160,152\n"
"FM 56,48,51\nFM 57,49,51\nMvaa 164,168,152\nRa 208,10,57344\nAD 54,54,52\nMvaa 164,172,156\nAD 55,55,53\nMvaa 132,172,148\nRa 212,11,57344\nv_and_b32 v48, 0xffff0000, v34\n"
"Mvaa 132,168,144\nv_and_b32 v49, 0xffff0000, v38\nMvaa 100,168,136\nRa 216,10,59392\nv_and_b32 v50, 0xffff0000, v42\nMvaa 100,172,140\nD0 7,24,44,1024\n"
"v_and_b32 v51, 0xffff0000, v46\nMvaa 68,172,132\nRa 220,11,59392\nMvaa 68,168,128\nMU 52,48,50\nMU 53,49,50\nMvaa 72,176,128\nCV 52,52,53\nv_and_b32 v53, 0xffff0000, v52\n"
"Mvaa 72,180,132\nv_lshlrev_b32 v52, 16, v52\nMvaa 104,180,140\nFM 56,48,51\nFM 57,49,51\nMvaa 104,176,136\nAD 54,54,52\nMvaa 136,176,144\nAD 55,55,53\nv_lshlrev_b32 v48, 16, v35\n"
"Mvaa 136,180,148\nD0 8,24,44,2048\nv_lshlrev_b32 v49, 16, v39\nMvaa 168,180,156\nv_lshlrev_b32 v50, 16, v43\nMvaa 168,176,152\nv_lshlrev_b32 v51, 16, v47\nMU 52,48,50\n"
"Mvaa 172,184,152\nMU 53,49,50\nCV 52,52,53\nMvaa 172,188,156\nv_and_b32 v53, 0xffff0000, v52\nMvaa 140,188,148\nv_lshlrev_b32 v52, 16, v52\nFM 56,48,51\nMvaa 140,184,144\n"
"FM 57,49,51\nMvaa 108,184,136\nAD 54,54,52\nAD 55,55,53\nMvaa 108,188,140\nD0 9,24,44,3072\nv_and_b32 v48, 0xffff0000, v35\nMvaa 76,188,132\nv_and_b32 v49, 0xffff0000, v39\n"
"Mvaa 76,184,128\nWL 0\nMaaa 0,160,192\nRa 128,10,0\nv_and_b32 v50, 0xffff0000, v43\nMaaa 0,164,196\ns_add_u32 m0, s47, 49152\ns_add_u32 s44, s44, 0x40000\nMaaa 32,164,204\n"
"Ra 132,11,0\nv_and_b32 v51, 0xffff0000, v47\nMaaa 32,160,200\nMU 52,48,50\nMaaa 64,160,208\nRa 136,10,2048\nMU 53,49,50\nMaaa 64,164,212\nD0 6,24,44,0\nCV 52,52,53\n"
"Maaa 96,164,220\nRa 140,11,2048\nMaaa 96,160,216\nv_and_b32 v53, 0xffff0000, v52\nv_lshlrev_b32 v52, 16, v52\nMaaa 100,168,216\nRa 144,10,8192\nFM 56,48,51\nMaaa 100,172,220\n"
"FM 57,49,51\n.W567:\ns_cbranch_execz .W571\nMaaa 68,172,212\nRa 148,11,8192\nAD 54,54,52\nMaaa 68,168,208\nAD 55,55,53\nMaaa 36,168,200\nRa 152,10,10240\n"
"v_permlane32_swap_b32 v54, v55\nMaaa 36,172,204\nD0 7,24,44,1024\nv_permlane32_swap_b32 v56, v57\nMaaa 4,172,196\nRa 156,11,10240\nMaaa 4,168,192\nAD 54,54,55\nAD 56,56,57\n"
"Maaa 8,176,192\nMaaa 8,180,196\nv_permlane16_swap_b32 v54, v56\nMaaa 40,180,204\nAD 54,54,56\nMaaa 40,176,200\nMaaa 72,176,208\n"
"v_add_f32_dpp v54, v54, v54 row_ror:8 row_mask:0xf bank_mask:0xf\nMaaa 72,180,212\nD0 8,24,44,2048\nMaaa 104,180,220\n"
"v_add_f32_dpp v54, v54, v54 row_ror:4 row_mask:0xf bank_mask:0xf\nMaaa 104,176,216\nMaaa 108,184,216\nv_add_f32_dpp v54, v54, v54 row_ror:2 row_mask:0xf bank_mask:0xf\n"
"Maaa 108,188,220\nMaaa 76,188,212\nv_add_f32_dpp v54, v54, v54 row_ror:1 row_mask:0xf bank_mask:0xf\nCV 54,54,54\nMaaa 76,184,208\nv_lshlrev_b32 v54, 16, v54\nMaaa 44,184,200\n"
"v_mul_f32 v55, 0xbfb8aa3b, v54\nMaaa 44,188,204\nD0 9,24,44,3072\nMaaa 12,188,196\nEX 55,55\nMaaa 12,184,192\ns_waitcnt vmcnt(12) lgkmcnt(0)\ns_barrier\nMaaa 16,224,192\n"
"Ta 160,12,0\nv_add_f32 v56, 0, v54\nMaaa 16,228,196\nTa 162,12,2048\nMaaa 48,228,204\ns_add_u32 m0, s47, -16384\ns_add_u32 s48, s48, 128\nMaaa 48,224,200\nTa 164,12,16384\n"
"Maaa 80,224,208\nTa 166,12,18432\nbuffer_load_dword v31, v24, s[24:27], s96 offen\ns_add_u32 s96, s96, 0x80000\nMaaa 80,228,212\nD0 2,20,48,0\nv_add_f32 v55, 1.0, v55\n"
"Maaa 112,228,220\nTa 168,13,0\nv_rcp_f32 v55, v55\nMaaa 112,224,216\nTa 170,13,2048\nv_ashrrev_i32 v57, 31, v56\nMaaa 116,232,216\nCV 55,55,55\nv_lshlrev_b32 v55, 16, v55\n"
"Maaa 116,236,220\nTa 172,13,16384\nMaaa 84,236,212\nTa 174,13,18432\nv_or_b32 v57, 0x80000000, v57\nMaaa 84,232,208\nv_xor_b32 v56, v56, v57\nMaaa 52,232,200\nTa 176,14,0\n"
"v_and_b32 v56, 0xffff0000, v56\nMaaa 52,236,204\nTa 178,14,2048\ns_lshl2_add_u32 s2, s10, s11\nMaaa 20,236,196\nD0 3,20,48,1024\nMaaa 20,232,192\nTa 180,14,16384\n"
"v_lshrrev_b32 v48, 5, v0\nMaaa 24,240,192\nTa 182,14,18432\nv_lshl_add_u32 v48, s2, 1, v48\nMaaa 24,244,196\nv_sub_u32 v49, 0xffff, v48\nMaaa 56,244,204\nTa 184,15,0\n"
"v_or_b32 v56, v56, v49\nMaaa 56,240,200\nTa 186,15,2048\nMaaa 88,240,208\nv_mov_b32 v57, s15\nv_and_b32 v49, 31, v0\nMaaa 88,244,212\nTa 188,15,16384\nv_mov_b32 v52, 0x40000000\n"
"Maaa 120,244,220\nTa 190,15,18432\nv_cmp_eq_u32 vcc, 16, v49\nv_lshlrev_b32 v50, 2, v48\nv_lshlrev_b32 v51, 10, v49\nv_cndmask_b32 v50, v52, v50, vcc\n"
"v_cndmask_b32 v56, v55, v56, vcc\nMaaa 120,240,216\nD0 4,20,48,2048\nv_lshl_add_u32 v51, v48, 3, v51\nMaaa 124,248,216\nv_and_b32 v49, 15, v0\nv_cmp_eq_u32 vcc, 0, v49\n"
"s_mov_b32 s2, 0xc040000\ns_mov_b32 s3, 0xc03e000\nv_cndmask_b32 v51, v52, v51, vcc\nbuffer_store_dwordx2 v[56:57], v51, s[16:19], s2 offen sc0 sc1\n"
"buffer_store_dword v54, v50, s[16:19], s3 offen sc0 sc1\nMaaa 124,252,220\nMaaa 92,252,212\nMaaa 92,248,208\nMaaa 60,248,200\nMaaa 60,252,204\nD0 5,20,48,3072\nMaaa 28,252,196\n"
"Maaa 28,248,192\nWL 0\ns_mov_b32 s50, 3\n.GL272:\nMvaa 64,160,128\nTa 224,16,0\nMvaa 64,164,132\nTa 226,16,2048\nMvaa 96,164,140\nTa 228,16,16384\ns_add_u32 m0, s47, -65536\n"
"s_add_u32 s45, s45, 128\nMvaa 96,160,136\nTa 230,16,18432\nMvaa 128,160,144\nTa 232,17,0\nMvaa 128,164,148\nTa 234,17,2048\nD0 2,20,45,0\nMvaa 160,164,156\nTa 236,17,16384\n"
"Mvaa 160,160,152\nTa 238,17,18432\nMvaa 164,168,152\nTa 240,18,0\nMvaa 164,172,156\nTa 242,18,2048\nMvaa 132,172,148\nTa 244,18,16384\nMvaa 132,168,144\nTa 246,18,18432\n"
"Mvaa 100,168,136\nTa 248,19,0\nMvaa 100,172,140\nTa 250,19,2048\nMvaa 68,172,132\nTa 252,19,16384\nD0 3,20,45,1024\nMvaa 68,168,128\nTa 254,19,18432\nMvaa 72,176,128\n"
"Ra 192,10,16384\nMvaa 72,180,132\nRa 196,11,16384\nMvaa 104,180,140\nRa 200,10,18432\nMvaa 104,176,136\nRa 204,11,18432\nMvaa 136,176,144\nRa 208,10,24576\nMvaa 136,180,148\n"
"Ra 212,11,24576\nMvaa 168,180,156\nRa 216,10,26624\nMvaa 168,176,152\nRa 220,11,26624\nD0 4,20,45,2048\nMvaa 172,184,152\nMvaa 172,188,156\nMvaa 140,188,148\nMvaa 140,184,144\n"
"Mvaa 108,184,136\nMvaa 108,188,140\nD0 5,20,45,3072\nMvaa 76,188,132\nMvaa 76,184,128\ns_waitcnt vmcnt(18) lgkmcnt(8)\ns_barrier\nMvaa 80,224,128\nMvaa 80,228,132\n"
"s_add_u32 m0, s47, 0\ns_add_u32 s44, s44, 0x40000\ns_mov_b32 s86, 1\ns_load_dword s86, s[54:55], s94\nMvaa 112,228,140\ns_add_u32 s94, s94, 1024\nMvaa 112,224,136\n"
"Mvaa 144,224,144\nMvaa 144,228,148\nD0 6,24,44,0\nMvaa 176,228,156\nMvaa 176,224,152\nMvaa 180,232,152\nMvaa 180,236,156\nMvaa 148,236,148\nMvaa 148,232,144\n"
"s_add_u32 s88, s88, s86\nMvaa 116,232,136\nMvaa 116,236,140\nD0 7,24,44,1024\ns_add_u32 s88, s88, s86\nMvaa 84,236,132\nMvaa 84,232,128\ns_add_u32 s88, s88, s86\nMvaa 88,240,128\n"
"Mvaa 88,244,132\ns_add_u32 s88, s88, s86\nMvaa 120,244,140\nMvaa 120,240,136\ns_add_u32 s88, s88, s86\nMvaa 152,240,144\nMvaa 152,244,148\nD0 8,24,44,2048\n"
"s_add_u32 s88, s88, s86\nMvaa 184,244,156\nMvaa 184,240,152\ns_add_u32 s88, s88, s86\nMvaa 188,248,152\nMvaa 188,252,156\ns_add_u32 s88, s88, s86\nMvaa 156,252,148\n"
"Mvaa 156,248,144\ns_add_u32 s88, s88, s86\nMvaa 124,248,136\nMvaa 124,252,140\nD0 9,24,44,3072\ns_add_u32 s88, s88, s86\nMvaa 92,252,132\nMvaa 92,248,128\nWL 0\n"
"s_add_u32 s88, s88, s86\nMaaa 16,224,192\nRa 128,10,32768\nMaaa 16,228,196\ns_add_u32 m0, s47, 16384\ns_add_u32 s44, s44, 0x40000\ns_add_u32 s88, s88, s86\nMaaa 48,228,204\n"
"Ra 132,11,32768\nMaaa 48,224,200\ns_add_u32 s88, s88, s86\nMaaa 80,224,208\nRa 136,10,34816\nMaaa 80,228,212\nD0 6,24,44,0\ns_add_u32 s88, s88, s86\nMaaa 112,228,220\n"
"Ra 140,11,34816\nMaaa 112,224,216\ns_add_u32 s88, s88, s86\nMaaa 116,232,216\nRa 144,10,40960\nMaaa 116,236,220\ns_add_u32 s88, s88, s86\nMaaa 84,236,212\nRa 148,11,40960\n"
"Maaa 84,232,208\nMaaa 52,232,200\nRa 152,10,43008\nMaaa 52,236,204\nD0 7,24,44,1024\nMaaa 20,236,196\nRa 156,11,43008\nMaaa 20,232,192\nMaaa 24,240,192\nMaaa 24,244,196\n"
"Maaa 56,244,204\nMaaa 56,240,200\nMaaa 88,240,208\nMaaa 88,244,212\nD0 8,24,44,2048\nMaaa 120,244,220\nMaaa 120,240,216\nMaaa 124,248,216\nMaaa 124,252,220\nMaaa 92,252,212\n"
"Maaa 92,248,208\nMaaa 60,248,200\nMaaa 60,252,204\nD0 9,24,44,3072\nMaaa 28,252,196\nMaaa 28,248,192\ns_waitcnt vmcnt(12) lgkmcnt(0)\ns_barrier\nMaaa 0,160,192\nTa 224,16,32768\n"
"s_mov_b32 m0, s76\ns_add_u32 s76, s76, 1024\nMaaa 0,164,196\nTa 226,16,34816\nD0 1,36,75,0\ns_add_u32 s75, s75, 1024\nMaaa 32,164,204\ns_add_u32 m0, s47, -49152\n"
"s_add_u32 s48, s48, 128\nMaaa 32,160,200\nTa 228,16,49152\nMaaa 64,160,208\nTa 230,16,51200\nbuffer_load_dword v31, v24, s[24:27], s96 offen\ns_add_u32 s96, s96, 0x80000\n"
"Maaa 64,164,212\nD0 2,20,48,0\nMaaa 96,164,220\nTa 232,17,32768\nMaaa 96,160,216\nTa 234,17,34816\nMaaa 100,168,216\nMaaa 100,172,220\nTa 236,17,49152\nMaaa 68,172,212\n"
"Ta 238,17,51200\nMaaa 68,168,208\nMaaa 36,168,200\nTa 240,18,32768\nMaaa 36,172,204\nTa 242,18,34816\nMaaa 4,172,196\nD0 3,20,48,1024\nMaaa 4,168,192\nTa 244,18,49152\n"
"Maaa 8,176,192\nTa 246,18,51200\nMaaa 8,180,196\nMaaa 40,180,204\nTa 248,19,32768\nMaaa 40,176,200\nTa 250,19,34816\nMaaa 72,176,208\nMaaa 72,180,212\nTa 252,19,49152\n"
"Maaa 104,180,220\nTa 254,19,51200\nMaaa 104,176,216\nD0 4,20,48,2048\nMaaa 108,184,216\nMaaa 108,188,220\nMaaa 76,188,212\nMaaa 76,184,208\nMaaa 44,184,200\nMaaa 44,188,204\n"
"D0 5,20,48,3072\nMaaa 12,188,196\nMaaa 12,184,192\nWL 0\nMvaa 80,224,128\nTa 160,12,32768\nMvaa 80,228,132\nTa 162,12,34816\nMvaa 112,228,140\nTa 164,12,49152\n"
"s_add_u32 m0, s47, -32768\ns_add_u32 s45, s45, 128\nMvaa 112,224,136\nTa 166,12,51200\nMvaa 144,224,144\nTa 168,13,32768\nMvaa 144,228,148\nTa 170,13,34816\nD0 2,20,45,0\n"
"Mvaa 176,228,156\nTa 172,13,49152\nMvaa 176,224,152\nTa 174,13,51200\nMvaa 180,232,152\nTa 176,14,32768\nMvaa 180,236,156\nTa 178,14,34816\nMvaa 148,236,148\nTa 180,14,49152\n"
"Mvaa 148,232,144\nTa 182,14,51200\nMvaa 116,232,136\nTa 184,15,32768\nMvaa 116,236,140\nTa 186,15,34816\nMvaa 84,236,132\nTa 188,15,49152\nD0 3,20,45,1024\nMvaa 84,232,128\n"
"Ta 190,15,51200\nMvaa 88,240,128\nRa 192,10,49152\nMvaa 88,244,132\nRa 196,11,49152\nMvaa 120,244,140\nRa 200,10,51200\nMvaa 120,240,136\nRa 204,11,51200\nMvaa 152,240,144\n"
"Ra 208,10,57344\nMvaa 152,244,148\nRa 212,11,57344\nMvaa 184,244,156\nRa 216,10,59392\nMvaa 184,240,152\nRa 220,11,59392\nD0 4,20,45,2048\nMvaa 188,248,152\nMvaa 188,252,156\n"
"Mvaa 156,252,148\nMvaa 156,248,144\nMvaa 124,248,136\nMvaa 124,252,140\nD0 5,20,45,3072\nMvaa 92,252,132\nMvaa 92,248,128\ns_waitcnt vmcnt(18) lgkmcnt(8)\ns_barrier\n"
"Mvaa 64,160,128\nMvaa 64,164,132\ns_add_u32 m0, s47, 32768\ns_add_u32 s44, s44, 0x40000\ns_mov_b32 s86, 1\ns_load_dword s86, s[54:55], s95\nMvaa 96,164,140\n"
"s_add_u32 s95, s95, 1024\nMvaa 96,160,136\nMvaa 128,160,144\nMvaa 128,164,148\nD0 6,24,44,0\nMvaa 160,164,156\nMvaa 160,160,152\nMvaa 164,168,152\nMvaa 164,172,156\n"
"Mvaa 132,172,148\nMvaa 132,168,144\ns_sub_u32 s88, s88, s86\nMvaa 100,168,136\nMvaa 100,172,140\nD0 7,24,44,1024\ns_sub_u32 s88, s88, s86\nMvaa 68,172,132\nMvaa 68,168,128\n"
"s_sub_u32 s88, s88, s86\nMvaa 72,176,128\nMvaa 72,180,132\ns_sub_u32 s88, s88, s86\nMvaa 104,180,140\nMvaa 104,176,136\ns_sub_u32 s88, s88, s86\nMvaa 136,176,144\n"
"Mvaa 136,180,148\nD0 8,24,44,2048\ns_sub_u32 s88, s88, s86\nMvaa 168,180,156\nMvaa 168,176,152\ns_sub_u32 s88, s88, s86\nMvaa 172,184,152\nMvaa 172,188,156\n"
"s_sub_u32 s88, s88, s86\nMvaa 140,188,148\nMvaa 140,184,144\ns_sub_u32 s88, s88, s86\nMvaa 108,184,136\nMvaa 108,188,140\nD0 9,24,44,3072\ns_sub_u32 s88, s88, s86\n"
"Mvaa 76,188,132\nMvaa 76,184,128\nWL 0\ns_sub_u32 s88, s88, s86\nMaaa 0,160,192\nRa 128,10,0\nMaaa 0,164,196\ns_add_u32 m0, s47, 49152\ns_add_u32 s44, s44, 0x40000\n"
"s_sub_u32 s88, s88, s86\nMaaa 32,164,204\nRa 132,11,0\nMaaa 32,160,200\ns_sub_u32 s88, s88, s86\nMaaa 64,160,208\nRa 136,10,2048\nMaaa 64,164,212\nD0 6,24,44,0\n"
"s_sub_u32 s88, s88, s86\nMaaa 96,164,220\nRa 140,11,2048\nMaaa 96,160,216\ns_sub_u32 s88, s88, s86\nMaaa 100,168,216\nRa 144,10,8192\nMaaa 100,172,220\ns_sub_u32 s88, s88, s86\n"
"Maaa 68,172,212\nRa 148,11,8192\nMaaa 68,168,208\nMaaa 36,168,200\nRa 152,10,10240\nMaaa 36,172,204\nD0 7,24,44,1024\nMaaa 4,172,196\nRa 156,11,10240\nMaaa 4,168,192\n"
"Maaa 8,176,192\nMaaa 8,180,196\nMaaa 40,180,204\nMaaa 40,176,200\nMaaa 72,176,208\nMaaa 72,180,212\nD0 8,24,44,2048\nMaaa 104,180,220\nMaaa 104,176,216\nMaaa 108,184,216\n"
"Maaa 108,188,220\nMaaa 76,188,212\nMaaa 76,184,208\nMaaa 44,184,200\nMaaa 44,188,204\nD0 9,24,44,3072\nMaaa 12,188,196\nMaaa 12,184,192\ns_waitcnt vmcnt(12) lgkmcnt(0)\ns_barrier\n"
"Maaa 16,224,192\nTa 160,12,0\ns_mov_b32 m0, s76\ns_add_u32 s76, s76, 1024\nMaaa 16,228,196\nTa 162,12,2048\nD0 1,36,75,0\ns_add_u32 s75, s75, 1024\nMaaa 48,228,204\n"
"s_add_u32 m0, s47, -16384\ns_add_u32 s48, s48, 128\nMaaa 48,224,200\nTa 164,12,16384\nMaaa 80,224,208\nTa 166,12,18432\nbuffer_load_dword v31, v24, s[24:27], s96 offen\n"
"s_add_u32 s96, s96, 0x80000\nMaaa 80,228,212\n.W568:\ns_cbranch_execz .W572\nD0 2,20,48,0\nMaaa 112,228,220\nTa 168,13,0\nMaaa 112,224,216\nTa 170,13,2048\nMaaa 116,232,216\n"
"Maaa 116,236,220\nTa 172,13,16384\nMaaa 84,236,212\nTa 174,13,18432\nMaaa 84,232,208\nMaaa 52,232,200\nTa 176,14,0\nMaaa 52,236,204\nTa 178,14,2048\nMaaa 20,236,196\n"
"D0 3,20,48,1024\nMaaa 20,232,192\nTa 180,14,16384\nMaaa 24,240,192\nTa 182,14,18432\nMaaa 24,244,196\nMaaa 56,244,204\nTa 184,15,0\nMaaa 56,240,200\nTa 186,15,2048\n"
"Maaa 88,240,208\nMaaa 88,244,212\nTa 188,15,16384\nMaaa 120,244,220\nTa 190,15,18432\nMaaa 120,240,216\nD0 4,20,48,2048\nMaaa 124,248,216\nMaaa 124,252,220\nMaaa 92,252,212\n"
"Maaa 92,248,208\nMaaa 60,248,200\ns_xor_b32 s94, s94, 0x2000\ns_xor_b32 s95, s95, 0x2000\ns_sub_u32 s88, 0, s88\nMaaa 60,252,204\nD0 5,20,48,3072\ns_sub_u32 s50, s50, 1\n"
"Maaa 28,252,196\nMaaa 28,248,192\nWL 0\ns_cbranch_scc0 .GL272\nMvaa 64,160,128\nTa 224,16,0\nMvaa 64,164,132\nTa 226,16,2048\nMvaa 96,164,140\ns_add_u32 m0, s47, -65536\n"
"s_add_u32 s45, s45, 128\nMvaa 96,160,136\nTa 228,16,16384\nMvaa 128,160,144\nTa 230,16,18432\nMvaa 128,164,148\nD0 2,20,45,0\nMvaa 160,164,156\nTa 232,17,0\nMvaa 160,160,152\n"
"Ta 234,17,2048\nMvaa 164,168,152\ns_sub_u32 s89, 0, s88\ns_cmp_eq_u32 s92, 0\ns_cselect_b32 s88, s88, s89\nMvaa 164,172,156\nTa 236,17,16384\nMvaa 132,172,148\nTa 238,17,18432\n"
"Mvaa 132,168,144\nv_mov_b32 v22, s88\nMvaa 100,168,136\nTa 240,18,0\nMvaa 100,172,140\nTa 242,18,2048\nMvaa 68,172,132\nD0 3,20,45,1024\nMvaa 68,168,128\nTa 244,18,16384\n"
"Mvaa 72,176,128\nTa 246,18,18432\nMvaa 72,180,132\nv_mov_b32 v23, s15\nMvaa 104,180,140\nTa 248,19,0\nMvaa 104,176,136\nTa 250,19,2048\nMvaa 136,176,144\n"
"buffer_store_dwordx2 v[22:23], off, s[16:19], s93 sc0 sc1\nMvaa 136,180,148\nTa 252,19,16384\nMvaa 168,180,156\nTa 254,19,18432\nMvaa 168,176,152\nD0 4,20,45,2048\n"
"Mvaa 172,184,152\nMvaa 172,188,156\nMvaa 140,188,148\nMvaa 140,184,144\nMvaa 108,184,136\nMvaa 108,188,140\nD0 5,20,45,3072\nMvaa 76,188,132\nMvaa 76,184,128\n"
"s_waitcnt vmcnt(19) lgkmcnt(0)\ns_barrier\nMvaa 80,224,128\nRa 192,10,16384\nMvaa 80,228,132\ns_add_u32 m0, s47, 0\ns_add_u32 s44, s44, 0x40000\nMvaa 112,228,140\nRa 196,11,16384\n"
"Mvaa 112,224,136\nMvaa 144,224,144\nRa 200,10,18432\nMvaa 144,228,148\nD0 6,24,44,0\nMvaa 176,228,156\nRa 204,11,18432\nMvaa 176,224,152\nMvaa 180,232,152\nRa 208,10,24576\n"
"Mvaa 180,236,156\nMvaa 148,236,148\nRa 212,11,24576\nMvaa 148,232,144\nMvaa 116,232,136\nRa 216,10,26624\nMvaa 116,236,140\nD0 7,24,44,1024\nMvaa 84,236,132\nRa 220,11,26624\n"
"Mvaa 84,232,128\nMvaa 88,240,128\nMvaa 88,244,132\nMvaa 120,244,140\nMvaa 120,240,136\nMvaa 152,240,144\nMvaa 152,244,148\nD0 8,24,44,2048\nMvaa 184,244,156\nMvaa 184,240,152\n"
"Mvaa 188,248,152\nMvaa 188,252,156\nMvaa 156,252,148\nMvaa 156,248,144\nMvaa 124,248,136\nMvaa 124,252,140\nD0 9,24,44,3072\nMvaa 92,252,132\nMvaa 92,248,128\nWL 0\n"
"Maaa 16,224,192\nRa 128,10,32768\nMaaa 16,228,196\ns_add_u32 m0, s47, 16384\ns_add_u32 s44, s44, 0x40000\nMaaa 48,228,204\nRa 132,11,32768\nMaaa 48,224,200\nMaaa 80,224,208\n"
"Ra 136,10,34816\nMaaa 80,228,212\nD0 6,24,44,0\nMaaa 112,228,220\nRa 140,11,34816\nMaaa 112,224,216\nMaaa 116,232,216\nRa 144,10,40960\nMaaa 116,236,220\nMaaa 84,236,212\n"
"Ra 148,11,40960\nMaaa 84,232,208\nMaaa 52,232,200\nRa 152,10,43008\nMaaa 52,236,204\nD0 7,24,44,1024\nMaaa 20,236,196\nRa 156,11,43008\nMaaa 20,232,192\nMaaa 24,240,192\n"
"Maaa 24,244,196\nMaaa 56,244,204\nMaaa 56,240,200\nMaaa 88,240,208\nMaaa 88,244,212\nD0 8,24,44,2048\nMaaa 120,244,220\nMaaa 120,240,216\nMaaa 124,248,216\nMaaa 124,252,220\n"
"Maaa 92,252,212\nMaaa 92,248,208\nMaaa 60,248,200\nMaaa 60,252,204\nD0 9,24,44,3072\nMaaa 28,252,196\nMaaa 28,248,192\ns_waitcnt vmcnt(19) lgkmcnt(0)\ns_barrier\nMaaa 0,160,192\n"
"Ta 224,16,32768\nMaaa 0,164,196\nTa 226,16,34816\nMaaa 32,164,204\ns_add_u32 m0, s47, -49152\ns_add_u32 s48, s48, 128\nMaaa 32,160,200\nTa 228,16,49152\nMaaa 64,160,208\n"
"Ta 230,16,51200\nbuffer_load_dword v31, v24, s[24:27], s96 offen\ns_add_u32 s96, s96, 0x80000\nMaaa 64,164,212\nD0 2,20,48,0\nMaaa 96,164,220\nTa 232,17,32768\nMaaa 96,160,216\n"
"Ta 234,17,34816\nMaaa 100,168,216\nMaaa 100,172,220\nTa 236,17,49152\nMaaa 68,172,212\nTa 238,17,51200\nMaaa 68,168,208\nMaaa 36,168,200\nTa 240,18,32768\nMaaa 36,172,204\n"
"Ta 242,18,34816\nMaaa 4,172,196\nD0 3,20,48,1024\nMaaa 4,168,192\nTa 244,18,49152\nMaaa 8,176,192\nTa 246,18,51200\nMaaa 8,180,196\nMaaa 40,180,204\nTa 248,19,32768\n"
"Maaa 40,176,200\nTa 250,19,34816\nMaaa 72,176,208\nMaaa 72,180,212\nTa 252,19,49152\nMaaa 104,180,220\nTa 254,19,51200\nMaaa 104,176,216\nD0 4,20,48,2048\nMaaa 108,184,216\n"
"Maaa 108,188,220\nMaaa 76,188,212\nMaaa 76,184,208\nMaaa 44,184,200\nMaaa 44,188,204\nD0 5,20,48,3072\nMaaa 12,188,196\nMaaa 12,184,192\nWL 0\nMvaa 80,224,128\nTa 160,12,32768\n"
"Mvaa 80,228,132\nTa 162,12,34816\nMvaa 112,228,140\ns_add_u32 m0, s47, -32768\ns_add_u32 s45, s45, 128\nMvaa 112,224,136\nTa 164,12,49152\nMvaa 144,224,144\nTa 166,12,51200\n"
"Mvaa 144,228,148\nD0 2,20,45,0\nMvaa 176,228,156\nTa 168,13,32768\nMvaa 176,224,152\nTa 170,13,34816\nMvaa 180,232,152\nMvaa 180,236,156\nTa 172,13,49152\nMvaa 148,236,148\n"
"Ta 174,13,51200\nMvaa 148,232,144\nMvaa 116,232,136\nTa 176,14,32768\nMvaa 116,236,140\nTa 178,14,34816\nMvaa 84,236,132\nD0 3,20,45,1024\nMvaa 84,232,128\nTa 180,14,49152\n"
"Mvaa 88,240,128\nTa 182,14,51200\nMvaa 88,244,132\nMvaa 120,244,140\nTa 184,15,32768\nMvaa 120,240,136\nTa 186,15,34816\nMvaa 152,240,144\nMvaa 152,244,148\nTa 188,15,49152\n"
"Mvaa 184,244,156\nTa 190,15,51200\nMvaa 184,240,152\nD0 4,20,45,2048\nMvaa 188,248,152\nMvaa 188,252,156\nMvaa 156,252,148\nMvaa 156,248,144\nMvaa 124,248,136\nMvaa 124,252,140\n"
"D0 5,20,45,3072\nMvaa 92,252,132\nMvaa 92,248,128\ns_waitcnt vmcnt(17) lgkmcnt(0)\ns_barrier\nMvaa 64,160,128\nRa 192,10,49152\nMvaa 64,164,132\ns_add_u32 m0, s47, 32768\n"
"s_add_u32 s44, s44, 0x40000\nMvaa 96,164,140\nRa 196,11,49152\nMvaa 96,160,136\nMvaa 128,160,144\nRa 200,10,51200\nMvaa 128,164,148\nD0 6,24,44,0\nMvaa 160,164,156\n"
"Ra 204,11,51200\nMvaa 160,160,152\nMvaa 164,168,152\nRa 208,10,57344\nMvaa 164,172,156\ns_lshl_b32 s5, s73, 9\ns_add_u32 s5, s5, 0xc049400\nv_lshrrev_b32 v27, 1, v1\n"
"buffer_load_dwordx2 v[22:23], v27, s[16:19], s5 offen sc0 sc1\nMvaa 132,172,148\nRa 212,11,57344\nMvaa 132,168,144\nMvaa 100,168,136\nRa 216,10,59392\nMvaa 100,172,140\n"
"D0 7,24,44,1024\nMvaa 68,172,132\nRa 220,11,59392\nMvaa 68,168,128\nMvaa 72,176,128\nMvaa 72,180,132\nMvaa 104,180,140\nMvaa 104,176,136\nMvaa 136,176,144\nMvaa 136,180,148\n"
"D0 8,24,44,2048\nMvaa 168,180,156\nMvaa 168,176,152\nMvaa 172,184,152\nMvaa 172,188,156\nMvaa 140,188,148\nMvaa 140,184,144\nMvaa 108,184,136\nMvaa 108,188,140\nD0 9,24,44,3072\n"
"Mvaa 76,188,132\nMvaa 76,184,128\nWL 0\nMaaa 0,160,192\nRa 128,10,0\nMaaa 0,164,196\ns_add_u32 m0, s47, 49152\ns_add_u32 s44, s44, 0x40000\nMaaa 32,164,204\nRa 132,11,0\n"
"Maaa 32,160,200\nMaaa 64,160,208\nRa 136,10,2048\nMaaa 64,164,212\nD0 6,24,44,0\nMaaa 96,164,220\nRa 140,11,2048\nMaaa 96,160,216\nMaaa 100,168,216\nRa 144,10,8192\n"
"Maaa 100,172,220\nMaaa 68,172,212\nRa 148,11,8192\nMaaa 68,168,208\nMaaa 36,168,200\nRa 152,10,10240\nMaaa 36,172,204\nD0 7,24,44,1024\nMaaa 4,172,196\nRa 156,11,10240\n"
"Maaa 4,168,192\nMaaa 8,176,192\nMaaa 8,180,196\nMaaa 40,180,204\nMaaa 40,176,200\nMaaa 72,176,208\nMaaa 72,180,212\nD0 8,24,44,2048\nMaaa 104,180,220\nMaaa 104,176,216\n"
"Maaa 108,184,216\nMaaa 108,188,220\nMaaa 76,188,212\nMaaa 76,184,208\nMaaa 44,184,200\nMaaa 44,188,204\nD0 9,24,44,3072\nMaaa 12,188,196\nMaaa 12,184,192\n"
"s_waitcnt vmcnt(18) lgkmcnt(0)\ns_barrier\nMaaa 16,224,192\nTa 160,12,0\nMaaa 16,228,196\nTa 162,12,2048\nMaaa 48,228,204\ns_add_u32 m0, s47, -16384\ns_add_u32 s48, s48, 128\n"
"Maaa 48,224,200\nTa 164,12,16384\nMaaa 80,224,208\nTa 166,12,18432\nbuffer_load_dword v31, v24, s[24:27], s96 offen\ns_add_u32 s96, s96, 0x80000\nMaaa 80,228,212\nD0 2,20,48,0\n"
"Maaa 112,228,220\nTa 168,13,0\nMaaa 112,224,216\nTa 170,13,2048\nMaaa 116,232,216\nMaaa 116,236,220\nTa 172,13,16384\nMaaa 84,236,212\nTa 174,13,18432\nMaaa 84,232,208\n"
"Maaa 52,232,200\nTa 176,14,0\nMaaa 52,236,204\nTa 178,14,2048\nMaaa 20,236,196\nD0 3,20,48,1024\nMaaa 20,232,192\nTa 180,14,16384\nMaaa 24,240,192\nTa 182,14,18432\n"
"Maaa 24,244,196\nMaaa 56,244,204\nTa 184,15,0\nMaaa 56,240,200\nTa 186,15,2048\nMaaa 88,240,208\nMaaa 88,244,212\nTa 188,15,16384\nMaaa 120,244,220\nTa 190,15,18432\n"
"Maaa 120,240,216\nD0 4,20,48,2048\nMaaa 124,248,216\nMaaa 124,252,220\nMaaa 92,252,212\nMaaa 92,248,208\nMaaa 60,248,200\nMaaa 60,252,204\nD0 5,20,48,3072\nMaaa 28,252,196\n"
"Maaa 28,248,192\nWL 0\nMvaa 64,160,128\nTa 224,16,0\nWV 12\nv_cmp_ne_u32 vcc, s15, v23\ns_nop 0\ns_cbranch_vccnz .S269\n.S270:\nMvaa 64,164,132\nTa 226,16,2048\nMvaa 96,164,140\n"
"s_add_u32 m0, s47, -65536\ns_add_u32 s45, s45, 128\nMvaa 96,160,136\nTa 228,16,16384\nMvaa 128,160,144\nTa 230,16,18432\nMvaa 128,164,148\nD0 2,20,45,0\nMvaa 160,164,156\n"
"Ta 232,17,0\nMvaa 160,160,152\nTa 234,17,2048\nMvaa 164,168,152\nv_add_u32_dpp v22, v22, v22 row_ror:8 row_mask:0xf bank_mask:0xf\nMvaa 164,172,156\nTa 236,17,16384\n"
"Mvaa 132,172,148\nTa 238,17,18432\nMvaa 132,168,144\nv_add_u32_dpp v22, v22, v22 row_ror:4 row_mask:0xf bank_mask:0xf\nMvaa 100,168,136\nTa 240,18,0\nMvaa 100,172,140\n"
"Ta 242,18,2048\nMvaa 68,172,132\nD0 3,20,45,1024\nMvaa 68,168,128\nTa 244,18,16384\nMvaa 72,176,128\nTa 246,18,18432\nMvaa 72,180,132\n"
"v_add_u32_dpp v22, v22, v22 row_ror:2 row_mask:0xf bank_mask:0xf\nMvaa 104,180,140\nTa 248,19,0\nMvaa 104,176,136\nTa 250,19,2048\nMvaa 136,176,144\n"
"v_add_u32_dpp v22, v22, v22 row_ror:1 row_mask:0xf bank_mask:0xf\nMvaa 136,180,148\nTa 252,19,16384\nMvaa 168,180,156\nTa 254,19,18432\n.W569:\ns_cbranch_execz .W573\n"
"Mvaa 168,176,152\nD0 4,20,45,2048\nMvaa 172,184,152\nv_mov_b32 v23, v22\nMvaa 172,188,156\nMvaa 140,188,148\nv_permlane16_swap_b32 v22, v23\nMvaa 140,184,144\n"
"v_add_u32 v22, v22, v23\nMvaa 108,184,136\nv_mov_b32 v23, v22\nMvaa 108,188,140\nD0 5,20,45,3072\nMvaa 76,188,132\nv_permlane32_swap_b32 v22, v23\nMvaa 76,184,128\nWL 0\n"
"s_barrier\nMvaa 80,224,128\nRa 192,10,16384\nMvaa 80,228,132\ns_add_u32 m0, s47, 0\ns_add_u32 s44, s44, 0x40000\nMvaa 112,228,140\nRa 196,11,16384\nMvaa 112,224,136\n"
"v_add_u32 v22, v22, v23\nMvaa 144,224,144\nRa 200,10,18432\nMvaa 144,228,148\nD0 6,24,44,0\nMvaa 176,228,156\nRa 204,11,18432\nMvaa 176,224,152\nv_readfirstlane_b32 s4, v22\n"
"Mvaa 180,232,152\nRa 208,10,24576\nMvaa 180,236,156\nMvaa 148,236,148\nRa 212,11,24576\ns_cmp_gt_i32 s4, 0\ns_cselect_b32 s4, 1, 0\nMvaa 148,232,144\ns_xor_b32 s4, s4, s12\n"
"Mvaa 116,232,136\nRa 216,10,26624\nMvaa 116,236,140\nD0 7,24,44,1024\nMvaa 84,236,132\nRa 220,11,26624\nMvaa 84,232,128\ns_and_b32 s4, s4, 1\nMvaa 88,240,128\n"
"s_lshl_b32 s4, s4, 13\nMvaa 88,244,132\ns_mul_i32 s72, s73, 25165824\nMvaa 120,244,140\ns_add_u32 s72, s72, s4\nMvaa 120,240,136\ns_and_b32 s4, s12, 1\nMvaa 152,240,144\n"
"s_lshl_b32 s4, s4, 21\nMvaa 152,244,148\nD0 8,24,44,2048\nMvaa 184,244,156\ns_add_u32 s72, s72, s4\nMvaa 184,240,152\ns_lshl3_add_u32 s2, s13, s11\nMvaa 188,248,152\n"
"s_lshl_b32 s2, s2, 11\nMvaa 188,252,156\ns_lshl_b32 s3, s14, 22\nMvaa 156,252,148\ns_add_u32 s2, s2, s3\nMvaa 156,248,144\ns_add_u32 s2, s2, s72\nMvaa 124,248,136\n"
"s_add_u32 s49, s2, 0xbe0000\nMvaa 124,252,140\nD0 9,24,44,3072\nMvaa 92,252,132\nMvaa 92,248,128\nWL 0\nMaaa 16,224,192\nRa 128,10,32768\ns_mov_b32 s4, s13\nMaaa 16,228,196\n"
"s_add_u32 m0, s47, 16384\ns_add_u32 s44, s44, 0x40000\nMaaa 48,228,204\nRa 132,11,32768\nv_lshrrev_b32 v32, 2, v0\nMaaa 48,224,200\nv_mul_u32_u24 v32, 896, v32\nMaaa 80,224,208\n"
"Ra 136,10,34816\nv_and_b32 v33, 3, v0\nMaaa 80,228,212\nD0 6,24,44,0\nMaaa 112,228,220\nRa 140,11,34816\nv_lshl_add_u32 v32, v33, 4, v32\nMaaa 112,224,216\ns_lshr_b32 s2, s11, 1\n"
"Maaa 116,232,216\nRa 144,10,40960\ns_mul_i32 s3, s2, 448\nMaaa 116,236,220\ns_and_b32 s5, s11, 1\nMaaa 84,236,212\nRa 148,11,40960\ns_lshl3_add_u32 s3, s5, s3\nMaaa 84,232,208\n"
"s_add_u32 s3, s3, 131072\nMaaa 52,232,200\nRa 152,10,43008\nv_add_u32 v32, s3, v32\nMaaa 52,236,204\nD0 7,24,44,1024\nMaaa 20,236,196\nRa 156,11,43008\nTv 36,32,0\n"
"Maaa 20,232,192\nTv 38,32,14336\nMaaa 24,240,192\nTv 40,32,64\nTv 42,32,14400\nMaaa 24,244,196\nTv 44,32,128\nMaaa 56,244,204\nTv 46,32,14464\nTv 48,32,192\nMaaa 56,240,200\n"
"Tv 50,32,14528\nMaaa 88,240,208\nTv 52,32,256\nTv 54,32,14592\nMaaa 88,244,212\nD0 8,24,44,2048\nMaaa 120,244,220\nTv 56,32,320\nTv 58,32,14656\nMaaa 120,240,216\nTv 60,32,384\n"
"Maaa 124,248,216\nTv 62,32,14720\ns_lshr_b32 s0, s4, 1\nMaaa 124,252,220\ns_mul_i32 s0, s0, 12\nMaaa 92,252,212\ns_lshl1_add_u32 s0, s5, s0\ns_and_b32 s6, s4, 1\nMaaa 92,248,208\n"
"s_add_u32 s0, s0, s6\nMaaa 60,248,200\ns_lshl3_add_u32 s1, s5, s4\ns_add_u32 s77, s1, 48\nMaaa 60,252,204\nD0 9,24,44,3072\nMaaa 28,252,196\ns_and_b32 s4, s12, 1\n"
"s_mov_b32 s7, 0x6543210\nMaaa 28,248,192\ns_waitcnt vmcnt(17) lgkmcnt(14)\ns_barrier\nMaaa 0,160,192\nTa 224,16,32768\ns_cmp_lg_u32 s4, s2\ns_cselect_b32 s6, s0, s1\n"
"s_cselect_b32 s5, 4, 16\ns_cselect_b32 s7, 0x6352410, s7\ns_cselect_b32 s3, 0, 229376\nMaaa 0,164,196\nTa 226,16,34816\nMaaa 32,164,204\ns_add_u32 m0, s47, -49152\n"
"s_add_u32 s48, s48, 128\nMaaa 32,160,200\nTa 228,16,49152\nMaaa 64,160,208\nTa 230,16,51200\ns_add_u32 s3, s3, s72\nMaaa 64,164,212\nD0 2,20,48,0\nMaaa 96,164,220\n"
"Ta 232,17,32768\ns_add_u32 s3, s3, 0x1400000\nMaaa 96,160,216\nTa 234,17,34816\nMaaa 100,168,216\nWL 14\ns_lshr_b32 s0, s6, 4\nMaaa 100,172,220\nTa 236,17,49152\nMaaa 68,172,212\n"
"Ta 238,17,51200\ns_lshl_b32 s0, s0, 2\nMaaa 68,168,208\ns_lshr_b32 s0, s7, s0\nMaaa 36,168,200\nTa 240,18,32768\ns_and_b32 s0, s0, 7\nMaaa 36,172,204\nTa 242,18,34816\n"
"Maaa 4,172,196\nD0 3,20,48,1024\ns_lshl_b32 s0, s0, 5\nMaaa 4,168,192\nTa 244,18,49152\nMaaa 8,176,192\nTa 246,18,51200\ns_and_b32 s1, s6, 15\nMaaa 8,180,196\n"
"s_add_u32 s0, s0, s1\nMaaa 40,180,204\nTa 248,19,32768\ns_and_b32 s1, s6, 8\nMaaa 40,176,200\nTa 250,19,34816\nMaaa 72,176,208\ns_add_u32 s0, s0, s1\ns_lshl_b32 s0, s0, 10\n"
"Maaa 72,180,212\nTa 252,19,49152\nMaaa 104,180,220\nTa 254,19,51200\ns_add_u32 s0, s0, s3\nMaaa 104,176,216\nD0 4,20,48,2048\nMaaa 108,184,216\nSv 36,1,16,0,0\n"
"s_add_u32 s6, s6, s5\nMaaa 108,188,220\nMaaa 76,188,212\nMaaa 76,184,208\nMaaa 44,184,200\nMaaa 44,188,204\nD0 5,20,48,3072\nMaaa 12,188,196\nMaaa 12,184,192\nWL 0\n"
"Mvaa 80,224,128\nTa 160,12,32768\ns_lshr_b32 s0, s6, 4\nMvaa 80,228,132\nTa 162,12,34816\nMvaa 112,228,140\ns_add_u32 m0, s47, -32768\ns_add_u32 s45, s45, 128\nMvaa 112,224,136\n"
"Ta 164,12,49152\nMvaa 144,224,144\nTa 166,12,51200\ns_lshl_b32 s0, s0, 2\nMvaa 144,228,148\nD0 2,20,45,0\nMvaa 176,228,156\nTa 168,13,32768\ns_lshr_b32 s0, s7, s0\n"
"Mvaa 176,224,152\nTa 170,13,34816\nMvaa 180,232,152\ns_and_b32 s0, s0, 7\ns_lshl_b32 s0, s0, 5\nMvaa 180,236,156\nTa 172,13,49152\nMvaa 148,236,148\nTa 174,13,51200\n"
"s_and_b32 s1, s6, 15\nMvaa 148,232,144\ns_add_u32 s0, s0, s1\nMvaa 116,232,136\nTa 176,14,32768\ns_and_b32 s1, s6, 8\nMvaa 116,236,140\nTa 178,14,34816\nMvaa 84,236,132\n"
"D0 3,20,45,1024\ns_add_u32 s0, s0, s1\nMvaa 84,232,128\nTa 180,14,49152\nMvaa 88,240,128\nTa 182,14,51200\ns_lshl_b32 s0, s0, 10\nMvaa 88,244,132\ns_add_u32 s0, s0, s3\n"
"Mvaa 120,244,140\nTa 184,15,32768\nSv 40,1,16,0,0\nMvaa 120,240,136\nTa 186,15,34816\nMvaa 152,240,144\ns_add_u32 s6, s6, s5\nMvaa 152,244,148\nTa 188,15,49152\nMvaa 184,244,156\n"
"Ta 190,15,51200\nMvaa 184,240,152\nD0 4,20,45,2048\nMvaa 188,248,152\nMvaa 188,252,156\nMvaa 156,252,148\nMvaa 156,248,144\nMvaa 124,248,136\ns_lshr_b32 s0, s6, 4\n"
"s_lshl_b32 s0, s0, 2\nMvaa 124,252,140\nD0 5,20,45,3072\nMvaa 92,252,132\ns_lshr_b32 s0, s7, s0\ns_and_b32 s0, s0, 7\nMvaa 92,248,128\ns_waitcnt vmcnt(18) lgkmcnt(0)\ns_barrier\n"
"Mvaa 64,160,128\nRa 192,10,49152\ns_lshl_b32 s0, s0, 5\nMvaa 64,164,132\ns_add_u32 m0, s47, 32768\ns_add_u32 s44, s44, 0x40000\nMvaa 96,164,140\nRa 196,11,49152\n"
"s_and_b32 s1, s6, 15\nMvaa 96,160,136\ns_add_u32 s0, s0, s1\nMvaa 128,160,144\nRa 200,10,51200\ns_and_b32 s1, s6, 8\nMvaa 128,164,148\nD0 6,24,44,0\nMvaa 160,164,156\n"
"Ra 204,11,51200\ns_add_u32 s0, s0, s1\nMvaa 160,160,152\ns_lshl_b32 s0, s0, 10\nMvaa 164,168,152\nRa 208,10,57344\ns_add_u32 s0, s0, s3\nMvaa 164,172,156\nSv 44,1,16,0,0\n"
"Mvaa 132,172,148\nRa 212,11,57344\ns_add_u32 s6, s6, s5\nMvaa 132,168,144\nMvaa 100,168,136\nRa 216,10,59392\nMvaa 100,172,140\nD0 7,24,44,1024\nMvaa 68,172,132\nRa 220,11,59392\n"
"Mvaa 68,168,128\nMvaa 72,176,128\nMvaa 72,180,132\nMvaa 104,180,140\ns_mov_b32 s6, s77\ns_mov_b32 s5, 16\nMvaa 104,176,136\ns_lshr_b32 s0, s6, 4\nMvaa 136,176,144\n"
"s_lshl_b32 s0, s0, 2\ns_lshr_b32 s0, s7, s0\nMvaa 136,180,148\nD0 8,24,44,2048\nMvaa 168,180,156\ns_and_b32 s0, s0, 7\ns_lshl_b32 s0, s0, 5\nMvaa 168,176,152\n"
"s_and_b32 s1, s6, 15\nMvaa 172,184,152\ns_add_u32 s0, s0, s1\ns_and_b32 s1, s6, 8\nMvaa 172,188,156\ns_add_u32 s0, s0, s1\nMvaa 140,188,148\ns_lshl_b32 s0, s0, 10\n"
"s_add_u32 s0, s0, s3\nMvaa 140,184,144\nSv 48,1,16,0,0\nMvaa 108,184,136\ns_add_u32 s6, s6, s5\nMvaa 108,188,140\nD0 9,24,44,3072\nMvaa 76,188,132\nMvaa 76,184,128\nWL 0\n"
"Maaa 0,160,192\nRa 128,10,0\nMaaa 0,164,196\ns_add_u32 m0, s47, 49152\ns_add_u32 s44, s44, 0x40000\nMaaa 32,164,204\nRa 132,11,0\nMaaa 32,160,200\nMaaa 64,160,208\nRa 136,10,2048\n"
"s_lshr_b32 s0, s6, 4\nMaaa 64,164,212\nD0 6,24,44,0\nMaaa 96,164,220\nRa 140,11,2048\ns_lshl_b32 s0, s0, 2\nMaaa 96,160,216\ns_lshr_b32 s0, s7, s0\nMaaa 100,168,216\n"
"Ra 144,10,8192\ns_and_b32 s0, s0, 7\nMaaa 100,172,220\ns_lshl_b32 s0, s0, 5\nMaaa 68,172,212\nRa 148,11,8192\ns_and_b32 s1, s6, 15\nMaaa 68,168,208\ns_add_u32 s0, s0, s1\n"
"Maaa 36,168,200\nRa 152,10,10240\ns_and_b32 s1, s6, 8\nMaaa 36,172,204\nD0 7,24,44,1024\nMaaa 4,172,196\nRa 156,11,10240\ns_add_u32 s0, s0, s1\nMaaa 4,168,192\n"
"s_lshl_b32 s0, s0, 10\nMaaa 8,176,192\ns_add_u32 s0, s0, s3\nSv 52,1,16,0,0\nMaaa 8,180,196\ns_add_u32 s6, s6, s5\nMaaa 40,180,204\nMaaa 40,176,200\nMaaa 72,176,208\n"
"Maaa 72,180,212\nD0 8,24,44,2048\nMaaa 104,180,220\nMaaa 104,176,216\nMaaa 108,184,216\nMaaa 108,188,220\ns_lshr_b32 s0, s6, 4\nMaaa 76,188,212\ns_lshl_b32 s0, s0, 2\n"
"s_lshr_b32 s0, s7, s0\nMaaa 76,184,208\ns_and_b32 s0, s0, 7\nMaaa 44,184,200\ns_lshl_b32 s0, s0, 5\ns_and_b32 s1, s6, 15\nMaaa 44,188,204\nD0 9,24,44,3072\nMaaa 12,188,196\n"
"s_add_u32 s0, s0, s1\ns_and_b32 s1, s6, 8\nMaaa 12,184,192\nWV 21\ns_barrier\nMaaa 16,224,192\nTa 160,12,0\ns_add_u32 s0, s0, s1\nMaaa 16,228,196\nTa 162,12,2048\nMaaa 48,228,204\n"
"s_add_u32 m0, s47, -16384\ns_add_u32 s48, s48, 128\nMaaa 48,224,200\nTa 164,12,16384\nMaaa 80,224,208\nTa 166,12,18432\ns_lshl_b32 s0, s0, 10\nMaaa 80,228,212\nD0 2,20,48,0\n"
"Maaa 112,228,220\nTa 168,13,0\ns_add_u32 s0, s0, s3\nMaaa 112,224,216\nTa 170,13,2048\nMaaa 116,232,216\nSv 56,1,16,0,0\ns_add_u32 s6, s6, s5\nMaaa 116,236,220\nTa 172,13,16384\n"
"Maaa 84,236,212\nTa 174,13,18432\nMaaa 84,232,208\nMaaa 52,232,200\nTa 176,14,0\nMaaa 52,236,204\nTa 178,14,2048\nMaaa 20,236,196\nD0 3,20,48,1024\nMaaa 20,232,192\n"
"Ta 180,14,16384\nMaaa 24,240,192\nTa 182,14,18432\ns_lshr_b32 s0, s6, 4\nMaaa 24,244,196\ns_lshl_b32 s0, s0, 2\nMaaa 56,244,204\nTa 184,15,0\ns_lshr_b32 s0, s7, s0\n"
"Maaa 56,240,200\nTa 186,15,2048\nMaaa 88,240,208\ns_and_b32 s0, s0, 7\ns_lshl_b32 s0, s0, 5\nMaaa 88,244,212\nTa 188,15,16384\nMaaa 120,244,220\nTa 190,15,18432\n"
"s_and_b32 s1, s6, 15\nMaaa 120,240,216\nD0 4,20,48,2048\nMaaa 124,248,216\ns_add_u32 s0, s0, s1\ns_and_b32 s1, s6, 8\nMaaa 124,252,220\ns_add_u32 s0, s0, s1\nMaaa 92,252,212\n"
"s_lshl_b32 s0, s0, 10\ns_add_u32 s0, s0, s3\nMaaa 92,248,208\nSv 60,1,16,0,0\nMaaa 60,248,200\ns_add_u32 s6, s6, s5\nMaaa 60,252,204\nD0 5,20,48,3072\nMaaa 28,252,196\n"
"Maaa 28,248,192\nWL 0\nMvaa 64,160,128\nTa 224,16,0\nMvaa 64,164,132\nTa 226,16,2048\ns_add_u32 m0, s47, 65536\ns_add_u32 s46, s51, 0\nMvaa 96,164,140\nv_lshrrev_b32 v48, 4, v0\n"
"Mvaa 96,160,136\nTa 228,16,16384\nD0 6,32,46,0\nMvaa 128,160,144\nTa 230,16,18432\nMvaa 128,164,148\nv_and_b32 v49, 7, v0\nMvaa 160,164,156\nTa 232,17,0\nMvaa 160,160,152\n"
"Ta 234,17,2048\nMvaa 164,168,152\nv_xor_b32 v49, v49, v48\nD0 7,32,46,1024\nMvaa 164,172,156\nTa 236,17,16384\nMvaa 132,172,148\nTa 238,17,18432\nMvaa 132,168,144\n"
"v_lshlrev_b32 v49, 4, v49\nMvaa 100,168,136\nTa 240,18,0\nMvaa 100,172,140\nTa 242,18,2048\nD0 8,32,46,2048\nMvaa 68,172,132\nv_lshrrev_b32 v48, 3, v0\nMvaa 68,168,128\n"
"Ta 244,18,16384\nMvaa 72,176,128\nTa 246,18,18432\nMvaa 72,180,132\nv_mul_u32_u24 v48, 896, v48\nMvaa 104,180,140\nTa 248,19,0\nD0 9,32,46,3072\nMvaa 104,176,136\nTa 250,19,2048\n"
"Mvaa 136,176,144\nv_add_u32 v40, v48, v49\nMvaa 136,180,148\nTa 252,19,16384\ns_add_u32 m0, s47, 81920\ns_add_u32 s46, s51, 262144\nMvaa 168,180,156\nTa 254,19,18432\n"
"Mvaa 168,176,152\nv_xor_b32 v49, 64, v40\nD0 6,32,46,0\nMvaa 172,184,152\nv_add_u32 v41, 6144, v49\n.W570:\ns_cbranch_execz .W574\nMvaa 172,188,156\nv_add_u32 v42, 55296, v40\n"
"Mvaa 140,188,148\nv_add_u32 v43, 61440, v49\nMvaa 140,184,144\ns_mov_b32 s2, s13\nMvaa 108,184,136\ns_lshl3_add_u32 s2, s2, s11\nD0 7,32,46,1024\nMvaa 108,188,140\n"
"s_mul_i32 s52, s2, 14336\nMvaa 76,188,132\ns_lshr_b32 s2, s13, 2\ns_bfe_u32 s3, s13, 0x10001\nMvaa 76,184,128\ns_waitcnt vmcnt(23) lgkmcnt(0)\ns_barrier\nMvaa 80,224,128\n"
"Ra 192,10,16384\ns_mul_i32 s3, s3, 17\nMvaa 80,228,132\ns_add_u32 s2, s2, s3\nD0 8,32,46,2048\nMvaa 112,228,140\nRa 196,11,16384\ns_lshl2_add_u32 s3, s14, 0\nMvaa 112,224,136\n"
"s_lshl_b32 s3, s3, 2\nMvaa 144,224,144\nRa 200,10,18432\ns_lshr_b32 s3, 0xe420ca86, s3\nMvaa 144,228,148\ns_and_b32 s3, s3, 15\nMvaa 176,228,156\nRa 204,11,18432\n"
"s_add_u32 s2, s2, s3\nD0 9,32,46,3072\nMvaa 176,224,152\ns_cmp_eq_u32 s2, 32\ns_cselect_b32 s4, 16, s2\nMvaa 180,232,152\nRa 208,10,24576\ns_mul_i32 s75, s4, 65536\n"
"Mvaa 180,236,156\ns_lshr_b32 s2, s13, 2\ns_add_u32 m0, s47, -65536\ns_add_u32 s46, s52, 0\nMvaa 148,236,148\nRa 212,11,24576\ns_bfe_u32 s3, s13, 0x10001\nMvaa 148,232,144\n"
"s_mul_i32 s3, s3, 17\nD0 40,28,46,0\nMvaa 116,232,136\nRa 216,10,26624\ns_add_u32 s2, s2, s3\nMvaa 116,236,140\ns_lshl2_add_u32 s3, s14, 1\nMvaa 84,236,132\nRa 220,11,26624\n"
"s_lshl_b32 s3, s3, 2\nMvaa 84,232,128\ns_lshr_b32 s3, 0xe420ca86, s3\nMvaa 88,240,128\ns_and_b32 s3, s3, 15\ns_add_u32 s2, s2, s3\nD0 41,28,46,1024\nMvaa 88,244,132\n"
"s_cmp_eq_u32 s2, 32\ns_cselect_b32 s4, 16, s2\nMvaa 120,244,140\ns_mul_i32 s76, s4, 65536\ns_lshr_b32 s2, s13, 2\nMvaa 120,240,136\ns_bfe_u32 s3, s13, 0x10001\nMvaa 152,240,144\n"
"s_mul_i32 s3, s3, 17\ns_add_u32 s2, s2, s3\nMvaa 152,244,148\ns_lshl2_add_u32 s3, s14, 2\nD0 42,28,46,2048\nMvaa 184,244,156\ns_lshl_b32 s3, s3, 2\ns_lshr_b32 s3, 0xe420ca86, s3\n"
"Mvaa 184,240,152\ns_and_b32 s3, s3, 15\nMvaa 188,248,152\ns_add_u32 s2, s2, s3\nMvaa 188,252,156\ns_cmp_eq_u32 s2, 32\ns_cselect_b32 s4, 16, s2\nMvaa 156,252,148\n"
"s_mul_i32 s77, s4, 65536\ns_lshr_b32 s2, s13, 2\nD0 43,28,46,3072\nMvaa 156,248,144\ns_bfe_u32 s3, s13, 0x10001\nMvaa 124,248,136\ns_mul_i32 s3, s3, 17\ns_add_u32 s2, s2, s3\n"
"Mvaa 124,252,140\ns_lshl2_add_u32 s3, s14, 3\nMvaa 92,252,132\ns_lshl_b32 s3, s3, 2\ns_lshr_b32 s3, 0xe420ca86, s3\nMvaa 92,248,128\nWL 0\nMaaa 16,224,192\nRa 128,10,32768\n"
"s_and_b32 s3, s3, 15\ns_add_u32 m0, s47, 0\ns_add_u32 s46, s51, 524288\nD0 6,32,46,0\nMaaa 16,228,196\ns_add_u32 s2, s2, s3\ns_cmp_eq_u32 s2, 32\ns_cselect_b32 s4, 16, s2\n"
"Maaa 48,228,204\nRa 132,11,32768\ns_mul_i32 s78, s4, 65536\nMaaa 48,224,200\ns_lshr_b32 s3, s11, 1\nMaaa 80,224,208\nRa 136,10,34816\ns_lshl_b32 s3, s3, 15\nMaaa 80,228,212\n"
"s_add_u32 s2, s72, s3\nD0 7,32,46,1024\nMaaa 112,228,220\nRa 140,11,34816\ns_and_b32 s3, s11, 1\nMaaa 112,224,216\ns_lshl_b32 s3, s3, 12\nMaaa 116,232,216\nRa 144,10,40960\n"
"s_add_u32 s2, s2, s3\nMaaa 116,236,220\ns_add_u32 s2, s2, 0x0\nMaaa 84,236,212\nRa 148,11,40960\ns_mov_b32 s55, 16384\nD0 8,32,46,2048\nMaaa 84,232,208\ns_add_u32 s75, s75, s2\n"
"Maaa 52,232,200\nRa 152,10,43008\ns_add_u32 s76, s76, s2\nMaaa 52,236,204\ns_add_u32 s77, s77, s2\nMaaa 20,236,196\nRa 156,11,43008\ns_add_u32 s78, s78, s2\nMaaa 20,232,192\n"
"v_add_u32 v32, 0x10000, v12\nD0 9,32,46,3072\nMaaa 24,240,192\nv_add_u32 v33, 0x10000, v13\nv_add_u32 v34, 0x10000, v14\nMaaa 24,244,196\nv_add_u32 v35, 0x10000, v15\n"
"Maaa 56,244,204\nv_add_u32 v36, 0x10000, v16\nv_add_u32 v37, 0x10000, v17\ns_add_u32 m0, s47, 16384\ns_add_u32 s46, s51, 786432\nMaaa 56,240,200\nv_add_u32 v38, 0x10000, v18\n"
"Maaa 88,240,208\nv_add_u32 v39, 0x10000, v19\ns_add_u32 s2, s11, 1\nD0 6,32,46,0\nMaaa 88,244,212\ns_lshr_b32 s2, s2, 1\nMaaa 120,244,220\ns_lshl_b32 s2, s2, 12\n"
"v_add_u32 v44, s2, v10\nMaaa 120,240,216\nv_add_u32 v45, s2, v11\nMaaa 124,248,216\nv_xor_b32 v46, 0x1000, v44\nv_xor_b32 v47, 0x1000, v45\nMaaa 124,252,220\n"
"s_lshr_b32 s3, s11, 1\nD0 7,32,46,1024\nMaaa 92,252,212\ns_lshl_b32 s3, s3, 13\ns_lshl_b32 s2, s11, 12\nMaaa 92,248,208\ns_add_u32 s2, s2, s3\nMaaa 60,248,200\n"
"s_add_u32 s2, s2, s72\ns_lshl_b32 s3, s11, 12\nMaaa 60,252,204\ns_add_u32 s89, s3, 0x10000\nMaaa 28,252,196\ns_mov_b32 s88, 0xffff8000\ns_lshl_b32 s4, s13, 1\nD0 8,32,46,2048\n"
"Maaa 28,248,192\ns_waitcnt vmcnt(25) lgkmcnt(0)\ns_barrier\nMaaa 0,160,192\nTa 224,16,32768\ns_sub_u32 s79, 15, s4\nMaaa 0,164,196\nTa 226,16,34816\nMaaa 32,164,204\n"
"s_add_u32 s96, s2, 0x1310000\ns_mov_b32 s63, 0x53800000\nMaaa 32,160,200\nTa 228,16,49152\nD0 9,32,46,3072\nMaaa 64,160,208\nTa 230,16,51200\ns_mov_b32 s64, 0x2b800000\n"
"Maaa 64,164,212\ns_sub_u32 s98, s4, 1\nMaaa 96,164,220\nTa 232,17,32768\ns_lshl_b32 s5, s13, 5\ns_add_u32 m0, s47, -32768\ns_add_u32 s46, s52, 128\nMaaa 96,160,216\n"
"Ta 234,17,34816\nMaaa 100,168,216\ns_add_u32 s5, s5, s11\ns_lshl_b32 s65, s5, 9\nD0 40,28,46,0\nMaaa 100,172,220\nTa 236,17,49152\nMaaa 68,172,212\nTa 238,17,51200\n"
"s_add_u32 s65, s65, s72\nMaaa 68,168,208\ns_add_u32 s65, s65, 0x1580000\nMaaa 36,168,200\nTa 240,18,32768\ns_and_b32 s66, s72, 0x2000\nMaaa 36,172,204\nTa 242,18,34816\n"
"D0 41,28,46,1024\nMaaa 4,172,196\ns_add_u32 s66, s66, s56\ns_add_u32 s66, s66, 0x100000\nMaaa 4,168,192\nTa 244,18,49152\nMaaa 8,176,192\nTa 246,18,51200\ns_and_b32 s5, s12, 1\n"
"Maaa 8,180,196\ns_mul_i32 s58, s5, 0x202000\nMaaa 40,180,204\nTa 248,19,32768\ns_xor_b32 s58, s58, s72\nD0 42,28,46,2048\nMaaa 40,176,200\nTa 250,19,34816\nMaaa 72,176,208\n"
"s_add_u32 s58, s58, 0x15c0000\ns_lshl_b32 s5, s5, 8\nMaaa 72,180,212\nTa 252,19,49152\nMaaa 104,180,220\nTa 254,19,51200\ns_add_u32 s61, s58, s5\nMaaa 104,176,216\n"
"s_lshl4_add_u32 s60, s13, s61\nD0 43,28,46,3072\nMaaa 108,184,216\ns_add_u32 s54, s60, 128\ns_xor_b32 s60, s54, 0x100\nMaaa 108,188,220\ns_cmp_eq_u32 s11, 0\n"
"s_cselect_b32 s54, s54, 0x40000000\nMaaa 76,188,212\ns_getpc_b64 s[8:9]\n.PC282:\ns_add_u32 s8, s8, .HD268-.PC282\ns_addc_u32 s9, s9, 0\ns_add_u32 m0, s47, -49152\n"
"s_add_u32 s46, s52, 256\nMaaa 76,184,208\ns_mov_b32 s97, 0xff800000\nMaaa 44,184,200\ns_mov_b32 s90, 0\nD0 40,28,46,0\nMaaa 44,188,204\ns_cmp_lt_u32 s11, 2\n"
"s_cselect_b32 s92, 2, 0\nMaaa 12,188,196\ns_mul_i32 s4, s79, 65536\ns_sub_u32 s4, s2, s4\nMaaa 12,184,192\nWL 0\nMvaa 80,224,128\nTa 160,12,32768\ns_add_u32 s94, s4, 0x20000\n"
"Mvaa 80,228,132\nTa 162,12,34816\nD0 41,28,46,1024\nMvaa 84,236,132\ns_add_u32 s95, s4, 0x410000\ns_add_u32 s86, s2, 0x130000\nMvaa 84,232,128\nTa 164,12,49152\nMvaa 88,240,128\n"
"Ta 166,12,51200\ns_add_u32 s87, s2, 0x520000\nMvaa 88,244,132\ns_sub_u32 s99, s79, 2\ns_cmp_eq_u32 s13, 7\ns_cselect_b32 s93, 1.0, 0\nMvaa 92,252,132\nTa 168,13,32768\n"
"s_mov_b32 s36, s68\nD0 42,28,46,2048\nMvaa 92,248,128\nTa 170,13,34816\nMvaa 124,248,136\ns_and_b32 s37, s69, 0xffff\ns_mov_b32 s38, 0x1c0000\nMvaa 124,252,140\nTa 172,13,49152\n"
"CV 200,80,81\nMvaa 120,244,140\nTa 174,13,51200\ns_mov_b32 s39, 0x20000\nMvaa 120,240,136\ns_and_b32 s2, s74, 15\nD0 43,28,46,3072\nMvaa 116,232,136\nTa 176,14,32768\n"
"s_min_u32 s2, s2, 13\nMvaa 116,236,140\nTa 178,14,34816\nMvaa 112,228,140\ns_sub_u32 s2, s2, 0\ns_mul_i32 s3, s73, 14\nMvaa 112,224,136\nTa 180,14,49152\nCV 201,82,83\n"
"Mvaa 144,224,144\nTa 182,14,51200\ns_add_u32 s2, s2, s3\nMvaa 144,228,148\ns_lshl2_add_u32 s2, s2, s11\nMvaa 148,236,148\nTa 184,15,32768\ns_mul_i32 s3, s2, 4682\n"
"Mvaa 148,232,144\nTa 186,15,34816\nCV 202,84,85\nMvaa 152,240,144\ns_lshr_b32 s3, s3, 16\ns_mul_i32 s22, s3, 14\nMvaa 152,244,148\nTa 188,15,49152\nCV 203,86,87\nMvaa 156,252,148\n"
"Ta 190,15,51200\ns_sub_u32 s22, s2, s22\nMvaa 156,248,144\ns_and_b32 s21, s3, 28\nMvaa 188,248,152\ns_add_u32 s21, s21, s3\ns_lshl_b32 s21, s21, 4\nMvaa 188,252,156\n"
"s_bfe_u32 s41, s3, 0x30002\ns_cmp_gt_u32 s22, 6\ns_cselect_b32 s2, 1179648, 0\ns_cselect_b32 s1, 16, 0\nMvaa 184,244,156\ns_add_u32 s41, s41, s1\n"
"s_lshl4_add_u32 s41, s41, 0xc048480\nMvaa 184,240,152\ns_and_b32 s1, s3, 28\nCV 204,88,89\nMvaa 180,232,152\ns_add_u32 s3, s3, s1\ns_lshl_b32 s3, s3, 11\nMvaa 180,236,156\n"
"s_add_u32 s2, s2, s3\nMvaa 176,228,156\ns_lshl_b32 s3, s22, 17\ns_add_u32 s2, s2, s3\nMvaa 176,224,152\ns_waitcnt vmcnt(16) lgkmcnt(0)\ns_barrier\nMvaa 64,160,128\n"
"Ra 192,10,49152\ns_add_u32 s20, s2, 0x10e0000\ns_add_u32 m0, s47, 32768\ns_add_u32 s46, s51, 1048576\nMvaa 64,164,132\ns_lshl_b32 s2, s22, 7\nMvaa 68,172,132\nRa 196,11,49152\n"
"s_add_u32 s40, s2, 0xc04b800\nD0 6,32,46,0\nMvaa 68,168,128\ns_mul_i32 s2, s21, 896\nCV 205,90,91\nMvaa 72,176,128\nRa 200,10,51200\ns_lshl_b32 s3, s22, 6\nMvaa 72,180,132\n"
"s_add_u32 s22, s2, s3\nCV 206,92,93\nMvaa 76,188,132\nRa 204,11,51200\ns_add_u32 s23, s22, 57344\nMvaa 76,184,128\nD0 7,32,46,1024\nMvaa 108,184,136\nRa 208,10,57344\n"
"CV 207,94,95\nMvaa 108,188,140\nCV 220,120,121\nCV 221,122,123\nMvaa 104,180,140\nRa 212,11,57344\nMvaa 104,176,136\nCV 222,124,125\nCV 223,126,127\nMvaa 100,168,136\n"
"Ra 216,10,59392\nD0 8,32,46,2048\ns_lshl3_add_u32 s5, s12, s13\ns_lshl2_add_u32 s5, s5, s11\ns_lshl2_add_u32 s5, s5, 0xc04a400\nbuffer_load_dword v27, off, s[16:19], s5 sc0 sc1\n"
"Mvaa 100,172,140\nCV 216,112,113\nMvaa 96,164,140\nRa 220,11,59392\nCV 217,114,115\nMvaa 96,160,136\nCV 218,116,117\nCV 219,118,119\nMvaa 128,160,144\nCV 192,64,65\nCV 193,66,67\n"
"Mvaa 128,164,148\nD0 9,32,46,3072\nMvaa 132,172,148\nCV 194,68,69\nCV 195,70,71\nMvaa 132,168,144\nCV 196,72,73\nCV 197,74,75\nMvaa 136,176,144\ns_add_u32 m0, s47, 49152\n"
"s_add_u32 s46, s51, 1310720\nMvaa 136,180,148\nCV 198,76,77\nCV 199,78,79\nMvaa 140,188,148\nD0 6,32,46,0\nCV 212,104,105\nMvaa 140,184,144\nCV 213,106,107\nMvaa 172,184,152\n"
"CV 214,108,109\nCV 215,110,111\nMvaa 172,188,156\nCV 208,96,97\nCV 209,98,99\nMvaa 168,180,156\nCV 210,100,101\nMvaa 168,176,152\nD0 7,32,46,1024\nCV 211,102,103\n"
"Mvaa 164,168,152\nCV 232,144,145\nMvaa 164,172,156\nCV 233,146,147\nSv 192,1,16,75,0\nMvaa 160,164,156\nCV 234,148,149\nMvaa 160,160,152\nWL 0\nMaaa 0,160,192\nD0 8,32,46,2048\n"
"Ra 128,44,0\nMaaa 0,164,196\nCV 235,150,151\nMaaa 4,172,196\nRa 132,45,0\nMaaa 4,168,192\nCV 236,152,153\nMaaa 8,176,192\nRa 136,44,2048\nMaaa 8,180,196\nD0 9,32,46,3072\n"
"Maaa 12,188,196\nRa 140,45,2048\nSv 196,1,16,75,1024\nMaaa 12,184,192\nCV 237,154,155\nMaaa 44,184,200\nRa 144,46,0\nMaaa 44,188,204\nCV 238,156,157\nMaaa 40,180,204\nRa 148,47,0\n"
"Maaa 40,176,200\nCV 239,158,159\nMaaa 36,168,200\nRa 152,46,2048\nMaaa 36,172,204\nCV 252,184,185\nMaaa 32,164,204\nRa 156,47,2048\nMaaa 32,160,200\nCV 253,186,187\n"
"Sv 200,1,16,75,2048\nMaaa 64,160,208\nCV 254,188,189\nCV 255,190,191\nMaaa 64,164,212\nCV 248,176,177\nMaaa 68,172,212\nCV 249,178,179\nMaaa 68,168,208\nCV 250,180,181\n"
"Maaa 72,176,208\nCV 251,182,183\nMaaa 72,180,212\nCV 224,128,129\nMaaa 76,188,212\nCV 225,130,131\nMaaa 76,184,208\nCV 226,132,133\nMaaa 108,184,216\nCV 227,134,135\n"
"Sv 204,1,16,75,3072\nMaaa 108,188,220\nCV 228,136,137\nMaaa 104,180,220\nCV 229,138,139\nMaaa 104,176,216\nCV 230,140,141\nMaaa 100,168,216\nCV 231,142,143\nMaaa 100,172,220\n"
"CV 244,168,169\nMaaa 96,164,220\nCV 245,170,171\nMaaa 96,160,216\ns_barrier\nMaaa 16,224,192\ns_add_u32 m0, s47, -16384\ns_add_u32 s46, s52, 384\nTa 160,32,0\nMaaa 16,228,196\n"
"Ta 162,32,2048\nMaaa 20,236,196\nD0 40,28,46,0\ns_add_u32 s75, s75, s55\nSv 208,1,16,75,0\nMaaa 20,232,192\nTa 164,32,16384\nMaaa 24,240,192\nTa 166,32,18432\nMaaa 24,244,196\n"
"CV 246,172,173\nMaaa 28,252,196\nTa 168,33,0\nMaaa 28,248,192\nD0 41,28,46,1024\nTa 170,33,2048\nMaaa 60,248,200\nCV 247,174,175\nMaaa 60,252,204\n.W571:\ns_cbranch_execz .W575\n"
"Ta 172,33,16384\nMaaa 56,244,204\nTa 174,33,18432\nMaaa 56,240,200\nCV 240,160,161\nSv 212,1,16,75,1024\nMaaa 52,232,200\nD0 42,28,46,2048\nTa 176,34,0\nMaaa 52,236,204\n"
"Ta 178,34,2048\nMaaa 48,228,204\nCV 241,162,163\nMaaa 48,224,200\nTa 180,34,16384\nMaaa 80,224,208\nTa 182,34,18432\nCV 242,164,165\nMaaa 80,228,212\nD0 43,28,46,3072\n"
"Maaa 84,236,212\nTa 184,35,0\nMaaa 84,232,208\nTa 186,35,2048\nMaaa 88,240,208\nCV 243,166,167\nSv 216,1,16,75,2048\nMaaa 88,244,212\nTa 188,35,16384\nMaaa 92,252,212\n"
"Ta 190,35,18432\nMaaa 92,248,208\nMaaa 124,248,216\nMaaa 124,252,220\nMaaa 120,244,220\nMaaa 120,240,216\nMaaa 116,232,216\nMaaa 116,236,220\nSv 220,1,16,75,3072\n"
"Maaa 112,228,220\nMaaa 112,224,216\nWL 0\nZvaa 64,160,128\nTa 224,36,0\nAR 60,0\nMvaa 64,164,132\nTa 226,36,2048\nZvaa 96,164,140\nAR 61,1\nMvaa 96,160,136\nTa 228,36,16384\n"
"CV 192,60,61\nZvaa 128,160,144\nTa 230,36,18432\nMvaa 128,164,148\nAR 60,2\nAR 61,3\nZvaa 160,164,156\nTa 232,37,0\nSv 224,1,16,76,0\nMvaa 160,160,152\nTa 234,37,2048\n"
"CV 193,60,61\nZvaa 164,168,152\nAR 60,4\nMvaa 164,172,156\nTa 236,37,16384\nZvaa 132,172,148\nTa 238,37,18432\nAR 61,5\nMvaa 132,168,144\nCV 194,60,61\nZvaa 100,168,136\n"
"Ta 240,38,0\nAR 60,6\nMvaa 100,172,140\nTa 242,38,2048\nZvaa 68,172,132\nSv 228,1,16,76,1024\nAR 61,7\nMvaa 68,168,128\nTa 244,38,16384\nZvaa 72,176,128\nTa 246,38,18432\n"
"CV 195,60,61\nMvaa 72,180,132\nAR 60,8\nZvaa 104,180,140\nTa 248,39,0\nMvaa 104,176,136\nTa 250,39,2048\nAR 61,9\nZvaa 136,176,144\nCV 196,60,61\nMvaa 136,180,148\n"
"Ta 252,39,16384\nAR 60,10\nZvaa 168,180,156\nTa 254,39,18432\nSv 232,1,16,76,2048\nMvaa 168,176,152\nAR 61,11\nCV 197,60,61\nZvaa 172,184,152\nAR 60,12\nMvaa 172,188,156\n"
"AR 61,13\nZvaa 140,188,148\nCV 198,60,61\nAR 60,14\nMvaa 140,184,144\nAR 61,15\nZvaa 108,184,136\nCV 199,60,61\nAR 60,16\nMvaa 108,188,140\nSv 236,1,16,76,3072\nZvaa 76,188,132\n"
"AR 61,17\nCV 200,60,61\nMvaa 76,184,128\ns_waitcnt vmcnt(29) lgkmcnt(0)\ns_barrier\nZvaa 80,224,128\nRa 192,44,32768\nAR 60,18\nMvaa 80,228,132\nRa 196,45,32768\n"
"s_add_u32 m0, s47, 65536\ns_add_u32 s46, s51, 1572864\nD0 6,32,46,0\nZvaa 112,228,140\nRa 200,44,34816\nMvaa 112,224,136\nRa 204,45,34816\nAR 61,19\nZvaa 144,224,144\n"
"Ra 208,46,32768\nMvaa 144,228,148\nRa 212,47,32768\nCV 201,60,61\nD0 7,32,46,1024\nZvaa 176,228,156\nRa 216,46,34816\nMvaa 176,224,152\nRa 220,47,34816\nAR 60,20\n"
"Zvaa 180,232,152\nTa 160,12,0\nMvaa 180,236,156\nTa 162,12,2048\nD0 8,32,46,2048\nZvaa 148,236,148\nTa 164,12,16384\nAR 61,21\nMvaa 148,232,144\nTa 166,12,18432\nZvaa 116,232,136\n"
"Ta 168,13,0\nCV 202,60,61\nMvaa 116,236,140\nTa 170,13,2048\nD0 9,32,46,3072\nZvaa 84,236,132\nTa 172,13,16384\nAR 60,22\nMvaa 84,232,128\nTa 174,13,18432\n"
"s_add_u32 m0, s47, 81920\ns_add_u32 s46, s51, 1835008\nZvaa 88,240,128\nTa 176,14,0\nAR 61,23\nMvaa 88,244,132\nTa 178,14,2048\nD0 6,32,46,0\nZvaa 120,244,140\nTa 180,14,16384\n"
"Mvaa 120,240,136\nTa 182,14,18432\nCV 203,60,61\nZvaa 152,240,144\nTa 184,15,0\nMvaa 152,244,148\nTa 186,15,2048\nAR 60,24\nD0 7,32,46,1024\nZvaa 184,244,156\nTa 188,15,16384\n"
"Mvaa 184,240,152\nTa 190,15,18432\nAR 61,25\nZvaa 188,248,152\nCV 204,60,61\nMvaa 188,252,156\nAR 60,26\nD0 8,32,46,2048\nZvaa 156,252,148\nAR 61,27\nCV 205,60,61\n"
"Mvaa 156,248,144\nAR 60,28\nZvaa 124,248,136\nAR 61,29\nCV 206,60,61\nMvaa 124,252,140\nAR 60,30\nD0 9,32,46,3072\nZvaa 92,252,132\nAR 61,31\nCV 207,60,61\nMvaa 92,248,128\nWL 0\n"
"Mvaa 64,160,192\nTa 224,16,0\nAR 60,32\nMvaa 64,164,196\nTa 226,16,2048\ns_add_u32 m0, s47, -65536\ns_add_u32 s46, s52, 512\nD0 40,28,46,0\nMvaa 96,164,204\nAR 61,33\n"
"Mvaa 96,160,200\nTa 228,16,16384\nCV 208,60,61\nMvaa 128,160,208\nTa 230,16,18432\nMvaa 128,164,212\nAR 60,34\nAR 61,35\nD0 41,28,46,1024\nMvaa 160,164,220\nTa 232,17,0\n"
"Mvaa 160,160,216\nTa 234,17,2048\nCV 209,60,61\nMvaa 164,168,216\nAR 60,36\nMvaa 164,172,220\nTa 236,17,16384\nD0 42,28,46,2048\nMvaa 132,172,212\nTa 238,17,18432\nAR 61,37\n"
"Mvaa 132,168,208\nCV 210,60,61\nMvaa 100,168,200\nTa 240,18,0\nAR 60,38\nMvaa 100,172,204\nTa 242,18,2048\nD0 43,28,46,3072\nMvaa 68,172,196\ns_add_u32 s76, s76, s55\n"
"Sv 240,1,16,76,0\nMvaa 68,168,192\nTa 244,18,16384\nMvaa 72,176,192\nTa 246,18,18432\nAR 61,39\nMvaa 72,180,196\nCV 211,60,61\nMvaa 104,180,204\nTa 248,19,0\nMvaa 104,176,200\n"
"Ta 250,19,2048\nSv 244,1,16,76,1024\nMvaa 136,176,208\nAR 60,40\nMvaa 136,180,212\nTa 252,19,16384\nAR 61,41\nMvaa 168,180,220\nTa 254,19,18432\nMvaa 168,176,216\nCV 212,60,61\n"
"AR 60,42\nMvaa 172,184,216\nSv 248,1,16,76,2048\nMvaa 172,188,220\nAR 61,43\nMvaa 140,188,212\nCV 213,60,61\nAR 60,44\nMvaa 140,184,208\nAR 61,45\nMvaa 108,184,200\nCV 214,60,61\n"
"AR 60,46\nMvaa 108,188,204\nSv 252,1,16,76,3072\nMvaa 76,188,196\nAR 61,47\nCV 215,60,61\nMvaa 76,184,192\ns_waitcnt vmcnt(20) lgkmcnt(0)\ns_barrier\nMvaa 80,224,192\n"
"Ra 128,44,16384\nAR 60,48\nv_cmp_ne_u32 vcc, s15, v27\ns_nop 0\ns_cbranch_vccnz .S283\n.S284:\nMvaa 80,228,196\nRa 132,45,16384\n"
"buffer_store_dword v25, off, s[16:19], s54 offset:0 sc0 sc1\ns_add_u32 m0, s47, 0\ns_add_u32 s46, s51, 2097152\nD0 6,32,46,0\nMvaa 112,228,204\nRa 136,44,18432\nMvaa 112,224,200\n"
"Ra 140,45,18432\nAR 61,49\nMvaa 144,224,208\nRa 144,46,16384\nMvaa 144,228,212\nRa 148,47,16384\nCV 216,60,61\nD0 7,32,46,1024\nMvaa 176,228,220\nRa 152,46,18432\n"
"Mvaa 176,224,216\nRa 156,47,18432\nAR 60,50\nMvaa 180,232,216\nTa 160,12,32768\nMvaa 180,236,220\nTa 162,12,34816\nD0 8,32,46,2048\nMvaa 148,236,212\nTa 164,12,49152\nAR 61,51\n"
"Mvaa 148,232,208\nTa 166,12,51200\nMvaa 116,232,200\nTa 168,13,32768\nCV 217,60,61\nMvaa 116,236,204\nTa 170,13,34816\nD0 9,32,46,3072\nMvaa 84,236,196\nTa 172,13,49152\nAR 60,52\n"
"Mvaa 84,232,192\nTa 174,13,51200\ns_add_u32 m0, s47, 16384\ns_add_u32 s46, s51, 2359296\nMvaa 88,240,192\nTa 176,14,32768\nAR 61,53\nMvaa 88,244,196\nTa 178,14,34816\n"
"D0 6,32,46,0\nMvaa 120,244,204\nTa 180,14,49152\nMvaa 120,240,200\nTa 182,14,51200\nCV 218,60,61\nMvaa 152,240,208\nTa 184,15,32768\nMvaa 152,244,212\nTa 186,15,34816\nAR 60,54\n"
"D0 7,32,46,1024\nMvaa 184,244,220\nTa 188,15,49152\nMvaa 184,240,216\nTa 190,15,51200\nAR 61,55\nMvaa 188,248,216\nCV 219,60,61\nMvaa 188,252,220\nAR 60,56\nD0 8,32,46,2048\n"
"Mvaa 156,252,212\nAR 61,57\nCV 220,60,61\nMvaa 156,248,208\nAR 60,58\nMvaa 124,248,200\nAR 61,59\nCV 221,60,61\nMvaa 124,252,204\nAR 60,60\nD0 9,32,46,3072\nMvaa 92,252,196\n"
"AR 61,61\nCV 222,60,61\nMvaa 92,248,192\nWL 0\nMvaa 64,160,128\nTa 224,16,32768\nAR 60,62\nMvaa 64,164,132\nTa 226,16,34816\ns_add_u32 m0, s47, -32768\ns_add_u32 s46, s52, 640\n"
"D0 40,28,46,0\nMvaa 96,164,140\nAR 61,63\nMvaa 96,160,136\nTa 228,16,49152\nCV 223,60,61\nMvaa 128,160,144\nTa 230,16,51200\nMvaa 128,164,148\nD0 41,28,46,1024\nMvaa 160,164,156\n"
"Ta 232,17,32768\nAR 60,126\nMvaa 160,160,152\nTa 234,17,34816\nMvaa 164,168,152\nAR 61,127\nCV 60,60,61\nMvaa 164,172,156\nTa 236,17,49152\nD0 42,28,46,2048\nMvaa 132,172,148\n"
"Ta 238,17,51200\nMvaa 132,168,144\nAW 127,60\nAR 60,124\nMvaa 100,168,136\nTa 240,18,32768\nMvaa 100,172,140\nTa 242,18,34816\nD0 43,28,46,3072\nMvaa 68,172,132\nSv 192,1,16,77,0\n"
"AR 61,125\nMvaa 68,168,128\nTa 244,18,49152\nMvaa 72,176,128\nTa 246,18,51200\nCV 60,60,61\nMvaa 72,180,132\nAW 126,60\nMvaa 104,180,140\nTa 248,19,32768\nMvaa 104,176,136\n"
"Ta 250,19,34816\nSv 196,1,16,77,1024\nMvaa 136,176,144\nAR 60,122\nMvaa 136,180,148\nTa 252,19,49152\nMvaa 168,180,156\nTa 254,19,51200\nAR 61,123\nMvaa 168,176,152\nCV 60,60,61\n"
"Mvaa 172,184,152\nSv 200,1,16,77,2048\nAW 125,60\nMvaa 172,188,156\nAR 60,120\nMvaa 140,188,148\nAR 61,121\nMvaa 140,184,144\nCV 60,60,61\nAW 124,60\nMvaa 108,184,136\nAR 60,118\n"
"Mvaa 108,188,140\nSv 204,1,16,77,3072\nMvaa 76,188,132\nAR 61,119\nCV 60,60,61\nMvaa 76,184,128\ns_waitcnt vmcnt(17) lgkmcnt(0)\ns_barrier\nMvaa 80,224,128\nRa 192,44,49152\n"
"AW 123,60\nMvaa 80,228,132\nRa 196,45,49152\nbuffer_store_dword v25, off, s[16:19], s54 offset:8 sc0 sc1\ns_add_u32 m0, s47, 32768\ns_add_u32 s46, s51, 2621440\nD0 6,32,46,0\n"
"Mvaa 112,228,140\nRa 200,44,51200\nMvaa 112,224,136\nRa 204,45,51200\nAR 60,116\nMvaa 144,224,144\nRa 208,46,49152\nMvaa 144,228,148\nRa 212,47,49152\nD0 7,32,46,1024\n"
"Mvaa 176,228,156\nRa 216,46,51200\nAR 61,117\nMvaa 176,224,152\nRa 220,47,51200\nMvaa 180,232,152\nTa 160,32,0\nCV 60,60,61\nMvaa 180,236,156\nTa 162,32,2048\nD0 8,32,46,2048\n"
"Mvaa 148,236,148\nTa 164,32,16384\nMvaa 148,232,144\nTa 166,32,18432\nAW 122,60\nMvaa 116,232,136\nTa 168,33,0\nMvaa 116,236,140\nTa 170,33,2048\nD0 9,32,46,3072\nMvaa 84,236,132\n"
"Ta 172,33,16384\nAR 60,114\nMvaa 84,232,128\nTa 174,33,18432\n.W572:\ns_cbranch_execz .W576\ns_add_u32 m0, s47, 49152\ns_add_u32 s46, s51, 2883584\nMvaa 88,240,128\nTa 176,34,0\n"
"AR 61,115\nMvaa 88,244,132\nTa 178,34,2048\nD0 6,32,46,0\nMvaa 120,244,140\nTa 180,34,16384\nMvaa 120,240,136\nTa 182,34,18432\nCV 60,60,61\nMvaa 152,240,144\nTa 184,35,0\n"
"Mvaa 152,244,148\nTa 186,35,2048\nD0 7,32,46,1024\nMvaa 184,244,156\nTa 188,35,16384\nAW 121,60\nMvaa 184,240,152\nTa 190,35,18432\nMvaa 188,248,152\nAR 60,112\nAR 61,113\n"
"Mvaa 188,252,156\nD0 8,32,46,2048\nMvaa 156,252,148\nCV 60,60,61\nMvaa 156,248,144\nAW 120,60\nAR 60,110\nMvaa 124,248,136\nAR 61,111\nMvaa 124,252,140\nD0 9,32,46,3072\n"
"Mvaa 92,252,132\nCV 60,60,61\nAW 119,60\nMvaa 92,248,128\nWL 0\nMvaa 64,160,192\nTa 224,36,0\nAR 60,108\nMvaa 64,164,196\nTa 226,36,2048\ns_add_u32 m0, s47, -49152\n"
"s_add_u32 s46, s52, 768\nD0 40,28,46,0\nMvaa 96,164,204\nAR 61,109\nMvaa 96,160,200\nTa 228,36,16384\nCV 60,60,61\nMvaa 128,160,208\nTa 230,36,18432\nMvaa 128,164,212\n"
"D0 41,28,46,1024\nMvaa 160,164,220\nTa 232,37,0\nAW 118,60\nMvaa 160,160,216\nTa 234,37,2048\nMvaa 164,168,216\nAR 60,106\nAR 61,107\nMvaa 164,172,220\nTa 236,37,16384\n"
"D0 42,28,46,2048\nMvaa 132,172,212\nTa 238,37,18432\nMvaa 132,168,208\nCV 60,60,61\nAW 117,60\nMvaa 100,168,200\nTa 240,38,0\nMvaa 100,172,204\nTa 242,38,2048\nD0 43,28,46,3072\n"
"Mvaa 68,172,196\ns_add_u32 s77, s77, s55\nSv 208,1,16,77,0\nMvaa 68,168,192\nTa 244,38,16384\nMvaa 72,176,192\nTa 246,38,18432\nAR 60,104\nMvaa 72,180,196\nAR 61,105\n"
"Mvaa 104,180,204\nTa 248,39,0\nMvaa 104,176,200\nTa 250,39,2048\nSv 212,1,16,77,1024\nMvaa 136,176,208\nCV 60,60,61\nMvaa 136,180,212\nTa 252,39,16384\nMvaa 168,180,220\n"
"Ta 254,39,18432\nAW 116,60\nMvaa 168,176,216\nAR 60,102\nMvaa 172,184,216\nSv 216,1,16,77,2048\nAR 61,103\nMvaa 172,188,220\nCV 60,60,61\nMvaa 140,188,212\nAW 115,60\n"
"Mvaa 140,184,208\nAR 60,100\nAR 61,101\nMvaa 108,184,200\nCV 60,60,61\nMvaa 108,188,204\nSv 220,1,16,77,3072\nMvaa 76,188,196\nAW 114,60\nAR 60,98\nMvaa 76,184,192\n"
"s_waitcnt vmcnt(25) lgkmcnt(0)\ns_barrier\nMvaa 80,224,192\nRa 128,44,0\nv_and_b32 v27, 0x70, v1\nbuffer_load_dword v27, v27, s[16:19], s61 offen offset:128 sc0 sc1\n"
"Mvaa 80,228,196\nRa 132,45,0\ns_add_u32 m0, s47, 65536\ns_add_u32 s46, s51, 3145728\nD0 6,32,46,0\nMvaa 112,228,204\nRa 136,44,2048\nMvaa 112,224,200\nRa 140,45,2048\nAR 61,99\n"
"Mvaa 144,224,208\nRa 144,46,0\nMvaa 144,228,212\nRa 148,47,0\nD0 7,32,46,1024\nMvaa 176,228,220\nRa 152,46,2048\nCV 60,60,61\nMvaa 176,224,216\nRa 156,47,2048\nMvaa 180,232,216\n"
"Ta 160,12,0\nAW 113,60\nMvaa 180,236,220\nTa 162,12,2048\nD0 8,32,46,2048\nMvaa 148,236,212\nTa 164,12,16384\nMvaa 148,232,208\nTa 166,12,18432\nAR 60,96\nMvaa 116,232,200\n"
"Ta 168,13,0\nMvaa 116,236,204\nTa 170,13,2048\nD0 9,32,46,3072\nMvaa 84,236,196\nTa 172,13,16384\nAR 61,97\nMvaa 84,232,192\nTa 174,13,18432\ns_add_u32 m0, s47, 81920\n"
"s_add_u32 s46, s51, 3407872\nMvaa 88,240,192\nTa 176,14,0\nCV 60,60,61\nMvaa 88,244,196\nTa 178,14,2048\nD0 6,32,46,0\nMvaa 120,244,204\nTa 180,14,16384\nMvaa 120,240,200\n"
"Ta 182,14,18432\nAW 112,60\nMvaa 152,240,208\nTa 184,15,0\nMvaa 152,244,212\nTa 186,15,2048\nD0 7,32,46,1024\nMvaa 184,244,220\nTa 188,15,16384\nAR 60,94\nMvaa 184,240,216\n"
"Ta 190,15,18432\nMvaa 188,248,216\nAR 61,95\nCV 60,60,61\nMvaa 188,252,220\nD0 8,32,46,2048\nMvaa 156,252,212\nAW 111,60\nMvaa 156,248,208\nAR 60,92\nAR 61,93\nMvaa 124,248,200\n"
"CV 60,60,61\nMvaa 124,252,204\nD0 9,32,46,3072\nMvaa 92,252,196\nAW 110,60\nAR 60,90\nMvaa 92,248,192\nWL 0\nMvaa 64,160,128\nTa 224,16,0\nAR 61,91\nMvaa 64,164,132\n"
"Ta 226,16,2048\ns_add_u32 s6, s56, 0\nbuffer_load_dwordx4 a[0:3], v1, s[16:19], s6 offen offset:0 sc1\nMvaa 96,164,140\nCV 60,60,61\nMvaa 96,160,136\nTa 228,16,16384\nAW 109,60\n"
"Mvaa 128,160,144\nTa 230,16,18432\nMvaa 128,164,148\nbuffer_load_dwordx4 a[4:7], v1, s[16:19], s6 offen offset:1024 sc1\nMvaa 160,164,156\nTa 232,17,0\nAR 60,88\nMvaa 160,160,152\n"
"Ta 234,17,2048\nMvaa 164,168,152\nAR 61,89\nCV 60,60,61\nMvaa 164,172,156\nTa 236,17,16384\nbuffer_load_dwordx4 a[8:11], v1, s[16:19], s6 offen offset:2048 sc1\nMvaa 132,172,148\n"
"Ta 238,17,18432\nMvaa 132,168,144\nAW 108,60\nAR 60,86\nMvaa 100,168,136\nTa 240,18,0\nMvaa 100,172,140\nTa 242,18,2048\n"
"buffer_load_dwordx4 a[12:15], v1, s[16:19], s6 offen offset:3072 sc1\nMvaa 68,172,132\nAR 61,87\nCV 60,60,61\nMvaa 68,168,128\nTa 244,18,16384\nMvaa 72,176,128\nTa 246,18,18432\n"
"AW 107,60\nMvaa 72,180,132\nAR 60,84\nMvaa 104,180,140\nTa 248,19,0\nMvaa 104,176,136\nTa 250,19,2048\nAR 61,85\nMvaa 136,176,144\nCV 60,60,61\nMvaa 136,180,148\nTa 252,19,16384\n"
"Mvaa 168,180,156\nTa 254,19,18432\nAW 106,60\nMvaa 168,176,152\nAR 60,82\nMvaa 172,184,152\nAR 61,83\nCV 60,60,61\nMvaa 172,188,156\nAW 105,60\nMvaa 140,188,148\nAR 60,80\n"
"Mvaa 140,184,144\nAR 61,81\nCV 60,60,61\nMvaa 108,184,136\nAW 104,60\nMvaa 108,188,140\nAR 60,78\nMvaa 76,188,132\nAR 61,79\nCV 60,60,61\nMvaa 76,184,128\n"
"s_waitcnt vmcnt(12) lgkmcnt(0)\ns_barrier\nMvaa 80,224,128\nRa 192,44,32768\nv_cmp_ne_u32 vcc, s15, v27\ns_nop 0\ns_cbranch_vccnz .S286\n.S287:\ns_sub_u32 s45, s86, 131072\n"
"s_cmp_eq_u32 s79, 1\ns_cselect_b32 s86, s94, s86\ns_add_u32 m0, s47, -65536\ns_add_u32 s46, s45, 0x0\nMvaa 80,228,132\nRa 196,45,32768\nMvaa 112,228,140\nRa 200,44,34816\n"
"D1 1,16,46,0\nMvaa 112,224,136\nRa 204,45,34816\nAW 103,60\nMvaa 144,224,144\nRa 208,46,32768\nMvaa 144,228,148\nRa 212,47,32768\nMvaa 176,228,156\nRa 216,46,34816\n"
"D1 1,16,46,1024\nMvaa 176,224,152\nRa 220,47,34816\nMvaa 180,232,152\nTa 160,12,32768\nAR 60,76\nMvaa 180,236,156\nTa 162,12,34816\nMvaa 148,236,148\nTa 164,12,49152\n"
"D1 1,16,46,2048\nMvaa 148,232,144\nTa 166,12,51200\nAR 61,77\nMvaa 116,232,136\nTa 168,13,32768\nMvaa 116,236,140\nTa 170,13,34816\nMvaa 84,236,132\nTa 172,13,49152\n"
"D1 1,16,46,3072\nMvaa 84,232,128\nTa 174,13,51200\nMvaa 88,240,128\nTa 176,14,32768\ns_add_u32 s6, s56, 4096\nMvaa 88,244,132\nTa 178,14,34816\nMvaa 120,244,140\nTa 180,14,49152\n"
"buffer_load_dwordx4 a[16:19], v1, s[16:19], s6 offen offset:0 sc1\nMvaa 120,240,136\nTa 182,14,51200\nCV 60,60,61\nMvaa 152,240,144\nTa 184,15,32768\nMvaa 152,244,148\n"
"Ta 186,15,34816\nMvaa 184,244,156\nTa 188,15,49152\nbuffer_load_dwordx4 a[20:23], v1, s[16:19], s6 offen offset:1024 sc1\nMvaa 184,240,152\nTa 190,15,51200\nMvaa 188,248,152\n"
"AW 102,60\nAR 60,74\nMvaa 188,252,156\nAR 61,75\nMvaa 156,252,148\nbuffer_load_dwordx4 a[24:27], v1, s[16:19], s6 offen offset:2048 sc1\nMvaa 156,248,144\nCV 60,60,61\nAW 101,60\n"
"Mvaa 124,248,136\nAR 60,72\nMvaa 124,252,140\nAR 61,73\nMvaa 92,252,132\nbuffer_load_dwordx4 a[28:31], v1, s[16:19], s6 offen offset:3072 sc1\nCV 60,60,61\nMvaa 92,248,128\nWL 0\n"
"Mvaa 64,160,192\nTa 224,16,32768\ns_add_u32 s6, s56, 16384\nMvaa 64,164,196\nTa 226,16,34816\nMvaa 96,164,204\nbuffer_load_dwordx4 a[32:35], v1, s[16:19], s6 offen offset:0 sc1\n"
"Mvaa 96,160,200\nTa 228,16,49152\nAW 100,60\nMvaa 128,160,208\nTa 230,16,51200\nMvaa 128,164,212\nAR 60,70\nMvaa 160,164,220\nTa 232,17,32768\n"
"buffer_load_dwordx4 a[36:39], v1, s[16:19], s6 offen offset:1024 sc1\nMvaa 160,160,216\nTa 234,17,34816\nMvaa 164,168,216\nAR 61,71\nCV 60,60,61\nMvaa 164,172,220\n"
"Ta 236,17,49152\nMvaa 132,172,212\nTa 238,17,51200\nbuffer_load_dwordx4 a[40:43], v1, s[16:19], s6 offen offset:2048 sc1\nMvaa 132,168,208\nAW 99,60\nAR 60,68\nMvaa 100,168,200\n"
"Ta 240,18,32768\nMvaa 100,172,204\nTa 242,18,34816\nMvaa 68,172,196\nbuffer_load_dwordx4 a[44:47], v1, s[16:19], s6 offen offset:3072 sc1\nAR 61,69\nMvaa 68,168,192\n"
"Ta 244,18,49152\nMvaa 72,176,192\nTa 246,18,51200\nCV 60,60,61\nMvaa 72,180,196\nAW 98,60\nMvaa 104,180,204\nTa 248,19,32768\nMvaa 104,176,200\nTa 250,19,34816\nAR 60,66\n"
"Mvaa 136,176,208\nAR 61,67\nMvaa 136,180,212\nTa 252,19,49152\nMvaa 168,180,220\nTa 254,19,51200\nCV 60,60,61\nMvaa 168,176,216\nAW 97,60\nMvaa 172,184,216\nAR 60,64\nAR 61,65\n"
"Mvaa 172,188,220\nCV 60,60,61\nMvaa 140,188,212\nAW 96,60\nv_add_u32 v2, 0x0, v1\nMvaa 140,184,208\ns_lshr_b32 s2, s59, 1\nMvaa 108,184,200\nv_add_u32 v24, s2, v2\n"
"v_xor_b32 v26, 0x1000, v24\nMvaa 108,188,204\nv_lshrrev_b32 v20, 4, v0\nMvaa 76,188,196\nv_lshlrev_b32 v20, 2, v20\nv_and_b32 v0, 15, v0\nMvaa 76,184,192\n"
"s_waitcnt vmcnt(16) lgkmcnt(0)\ns_barrier\nMvaa 80,224,192\nRa 128,44,16384\nv_sub_u32 v0, v0, v20\nMvaa 80,228,196\nRa 132,45,16384\n"
"buffer_store_dword v25, off, s[16:19], s54 offset:12 sc0 sc1\ns_add_u32 s6, s56, 20480\nbuffer_load_dwordx4 a[48:51], v1, s[16:19], s6 offen offset:0 sc1\nMvaa 112,228,204\n"
"Ra 136,44,18432\ns_lshl_b32 s2, s11, 4\nMvaa 112,224,200\nRa 140,45,18432\nMvaa 144,224,208\nRa 144,46,16384\nv_add_u32 v3, s2, v0\nMvaa 144,228,212\nRa 148,47,16384\n"
"buffer_load_dwordx4 a[52:55], v1, s[16:19], s6 offen offset:1024 sc1\nMvaa 176,228,220\nRa 152,46,18432\nv_subrev_u32 v0, 32, v3\nMvaa 176,224,216\nRa 156,47,18432\n"
"Mvaa 180,232,216\nTa 160,32,0\ns_lshl_b32 s2, s11, 13\nMvaa 180,236,220\nTa 162,32,2048\nbuffer_load_dwordx4 a[56:59], v1, s[16:19], s6 offen offset:2048 sc1\nMvaa 148,236,212\n"
"Ta 164,32,16384\ns_add_u32 s2, s2, 0x20000\nMvaa 148,232,208\nTa 166,32,18432\nMvaa 116,232,200\nTa 168,33,0\nv_add_u32 v22, s2, v1\nMvaa 116,236,204\nTa 170,33,2048\n"
"buffer_load_dwordx4 a[60:63], v1, s[16:19], s6 offen offset:3072 sc1\nMvaa 84,236,196\nTa 172,33,16384\nv_xor_b32 v23, 0x2000, v22\nMvaa 84,232,192\nTa 174,33,18432\n"
"s_add_u32 m0, s47, -32768\ns_add_u32 s46, s45, 0x8000\nMvaa 88,240,192\nTa 176,34,0\nv_mov_b32 v4, 0\nMvaa 88,244,196\nTa 178,34,2048\nD1 1,16,46,0\nMvaa 120,244,204\n"
"Ta 180,34,16384\nv_mov_b32 v5, 0\nMvaa 120,240,200\nTa 182,34,18432\nMvaa 152,240,208\nTa 184,35,0\nv_mov_b32 v6, 0\nMvaa 152,244,212\nTa 186,35,2048\nD1 1,16,46,1024\n"
"Mvaa 184,244,220\nTa 188,35,16384\nv_mov_b32 v7, 0\nMvaa 184,240,216\nTa 190,35,18432\nMvaa 188,248,216\n.W573:\ns_cbranch_execz .W577\nv_mov_b32 v8, 0\nv_mov_b32 v9, 0\n"
"Mvaa 188,252,220\nD1 1,16,46,2048\nMvaa 156,252,212\nv_mov_b32 v10, 0\nv_mov_b32 v11, 0\nMvaa 156,248,208\nv_mov_b32 v12, 0\nMvaa 124,248,200\nv_mov_b32 v13, 0\nv_mov_b32 v14, 0\n"
"Mvaa 124,252,204\nD1 1,16,46,3072\nMvaa 92,252,196\nv_mov_b32 v15, 0\nv_mov_b32 v16, 0\nMvaa 92,248,192\nWL 0\nMvaa 64,160,128\nTa 224,36,0\nv_mov_b32 v17, 0\nMvaa 64,164,132\n"
"Ta 226,36,2048\ns_add_u32 m0, s47, -16384\ns_add_u32 s46, s45, 0x400000\nD1 1,16,46,0\nMvaa 68,172,132\nv_mov_b32 v18, 0\nv_mov_b32 v19, 0\nMvaa 68,168,128\nTa 228,36,16384\n"
"Mvaa 72,176,128\nTa 230,36,18432\nMvaa 72,180,132\nD1 1,16,46,1024\nMvaa 76,188,132\nTa 232,37,0\nMvaa 76,184,128\nTa 234,37,2048\nMvaa 108,184,136\nv_mul_f32 v64, s57, v64\n"
"v_mul_f32 v65, s57, v65\nMvaa 108,188,140\nTa 236,37,16384\nD1 1,16,46,2048\nMvaa 104,180,140\nTa 238,37,18432\nCV 64,64,65\nMvaa 104,176,136\nv_mul_f32 v66, s57, v66\n"
"v_mul_f32 v67, s57, v67\nMvaa 100,168,136\nTa 240,38,0\nCV 65,66,67\nMvaa 100,172,140\nTa 242,38,2048\nD1 1,16,46,3072\nMvaa 96,164,140\nv_mul_f32 v68, s57, v68\n"
"v_mul_f32 v69, s57, v69\nMvaa 96,160,136\nTa 244,38,16384\nCV 66,68,69\nMvaa 128,160,144\nTa 246,38,18432\nv_mul_f32 v70, s57, v70\nMvaa 128,164,148\nv_mul_f32 v71, s57, v71\n"
"Mvaa 132,172,148\nTa 248,39,0\nCV 67,70,71\nMvaa 132,168,144\nTa 250,39,2048\nAW 192,64\nMvaa 136,176,144\nAW 193,65\nAW 194,66\nMvaa 136,180,148\nTa 252,39,16384\nAW 195,67\n"
"Mvaa 140,188,148\nTa 254,39,18432\nv_mul_f32 v72, s57, v72\nMvaa 140,184,144\nv_mul_f32 v73, s57, v73\nCV 72,72,73\nMvaa 172,184,152\nv_mul_f32 v74, s57, v74\n"
"v_mul_f32 v75, s57, v75\nMvaa 172,188,156\nCV 73,74,75\nv_mul_f32 v76, s57, v76\nMvaa 168,180,156\nv_mul_f32 v77, s57, v77\nCV 74,76,77\nMvaa 168,176,152\nv_mul_f32 v78, s57, v78\n"
"v_mul_f32 v79, s57, v79\nMvaa 164,168,152\nCV 75,78,79\nAW 200,72\nMvaa 164,172,156\nAW 201,73\nAW 202,74\nMvaa 160,164,156\nAW 203,75\nv_mul_f32 v96, s57, v96\nMvaa 160,160,152\n"
"s_waitcnt vmcnt(21) lgkmcnt(0)\ns_barrier\nMvaa 80,224,128\nv_mul_f32 v97, s57, v97\nCV 96,96,97\nMvaa 80,228,132\ns_add_u32 m0, s47, 0\ns_add_u32 s46, s86, 0xffff0000\n"
"D1 1,16,46,0\nMvaa 84,236,132\nv_mul_f32 v98, s57, v98\nv_mul_f32 v99, s57, v99\nMvaa 84,232,128\nCV 97,98,99\nv_mul_f32 v100, s57, v100\nMvaa 88,240,128\n"
"v_mul_f32 v101, s57, v101\nCV 98,100,101\nMvaa 88,244,132\nD1 1,16,46,1024\nv_mul_f32 v102, s57, v102\nMvaa 92,252,132\nv_mul_f32 v103, s57, v103\nCV 99,102,103\nMvaa 92,248,128\n"
"AW 196,96\nAW 197,97\nMvaa 124,248,136\nAW 198,98\nAW 199,99\nMvaa 124,252,140\nD1 1,16,46,2048\nv_mul_f32 v128, s57, v128\nMvaa 120,244,140\nv_mul_f32 v129, s57, v129\n"
"CV 128,128,129\nMvaa 120,240,136\nv_mul_f32 v130, s57, v130\nv_mul_f32 v131, s57, v131\nMvaa 116,232,136\nRa 160,24,0\nCV 129,130,131\nMvaa 116,236,140\nD1 1,16,46,3072\n"
"v_mul_f32 v132, s57, v132\nMvaa 112,228,140\nRa 164,24,1024\nv_mul_f32 v133, s57, v133\nMvaa 112,224,136\ns_add_u32 m0, s47, 16384\ns_add_u32 s46, s45, 0x408000\nMvaa 144,224,144\n"
"Ra 168,24,2048\nCV 130,132,133\nMvaa 144,228,148\nD1 1,16,46,0\nMvaa 148,236,148\nRa 172,24,3072\nv_mul_f32 v134, s57, v134\nMvaa 148,232,144\nv_mul_f32 v135, s57, v135\n"
"CV 131,134,135\nMvaa 152,240,144\nRa 176,26,0\nds_write_b128 v23, v[128:131] offset:0\nMvaa 152,244,148\nD1 1,16,46,1024\nv_mul_f32 v80, s57, v80\nMvaa 156,252,148\n"
"Ra 180,26,1024\nv_mul_f32 v81, s57, v81\nMvaa 156,248,144\nCV 80,80,81\nv_mul_f32 v82, s57, v82\nMvaa 188,248,152\nRa 184,26,2048\nv_mul_f32 v83, s57, v83\nMvaa 188,252,156\n"
"D1 1,16,46,2048\nCV 81,82,83\nMvaa 184,244,156\nRa 188,26,3072\nv_mul_f32 v84, s57, v84\nMvaa 184,240,152\nv_mul_f32 v85, s57, v85\nCV 82,84,85\nMvaa 180,232,152\n"
"v_mul_f32 v86, s57, v86\nv_mul_f32 v87, s57, v87\nMvaa 180,236,156\nD1 1,16,46,3072\nCV 83,86,87\nMvaa 176,228,156\nAW 208,80\nAW 209,81\nMvaa 176,224,152\n"
"s_waitcnt vmcnt(25) lgkmcnt(5)\nCvaa 32,160,0,4\nRa 128,24,8192\nAW 210,82\nMvaa 32,164,8\nAW 211,83\nMvaa 32,168,16\nRa 132,24,9216\nv_mul_f32 v88, s57, v88\nMvaa 32,172,24\n"
"v_mul_f32 v89, s57, v89\nCV 88,88,89\nCvaa 36,172,28,8\nRa 136,24,10240\nv_mul_f32 v90, s57, v90\nMvaa 36,168,20\ns_add_u32 m0, s89, 32768\nv_mul_f32 v91, s57, v91\n"
"Mvaa 36,164,12\nRa 140,24,11264\ns_waitcnt vmcnt(16) lgkmcnt(4)\ns_barrier\nMvaa 36,160,4\nCV 89,90,91\nv_mul_f32 v92, s57, v92\nMvaa 36,176,36\nRa 144,26,8192\nSa 96,1,16,78,0\n"
"Mvaa 36,180,44\ns_add_u32 s46, s86, s88\nv_mul_f32 v93, s57, v93\nMvaa 36,184,52\nRa 148,26,9216\nCV 90,92,93\nMvaa 36,188,60\nv_mul_f32 v94, s57, v94\nv_mul_f32 v95, s57, v95\n"
"Mvaa 32,188,56\nRa 152,26,10240\nCV 91,94,95\nMvaa 32,184,48\nD1 1,16,46,0\nAW 216,88\nMvaa 32,180,40\nRa 156,26,11264\nWL 4\nMvaa 32,176,32\nAW 217,89\nAW 218,90\n"
"Cvaa 40,128,0,12\nSa 100,1,16,78,1024\nAW 219,91\nMvaa 40,132,8\nv_mov_b32 v86, 0\nMvaa 40,136,16\nv_mov_b32 v87, 0\nv_mov_b32 v88, 0\nMvaa 40,140,24\nv_mov_b32 v89, 0\n"
"v_mul_f32 v136, s57, v136\nv_mul_f32 v137, s57, v137\nCV 136,136,137\nv_mul_f32 v138, s57, v138\nv_mul_f32 v139, s57, v139\nCV 137,138,139\nCvaa 44,140,28,16\n"
"v_and_b32 v27, 0x70, v1\nbuffer_load_dword v27, v27, s[16:19], s61 offen offset:136 sc0 sc1\nv_mul_f32 v140, s57, v140\nv_mul_f32 v141, s57, v141\nCV 138,140,141\n"
"v_mul_f32 v142, s57, v142\nv_mul_f32 v143, s57, v143\nMvaa 44,136,20\nD1 1,16,46,1024\nCV 139,142,143\nds_write_b128 v23, v[136:139] offset:2048\nv_mul_f32 v144, s57, v144\n"
"v_mul_f32 v145, s57, v145\nCV 144,144,145\nv_mul_f32 v146, s57, v146\nMvaa 44,132,12\nWL 1\nv_mul_f32 v147, s57, v147\nCV 145,146,147\nv_mul_f32 v148, s57, v148\n"
"v_mul_f32 v149, s57, v149\nCV 146,148,149\nMvaa 44,128,4\nv_mul_f32 v150, s57, v150\nv_mul_f32 v151, s57, v151\nCV 147,150,151\nds_write_b128 v23, v[144:147] offset:4096\n"
"v_mul_f32 v152, s57, v152\nv_mul_f32 v153, s57, v153\nCV 152,152,153\nMvaa 44,144,36\nSa 104,1,16,78,2048\nv_mul_f32 v154, s57, v154\nv_mul_f32 v155, s57, v155\nCV 153,154,155\n"
"v_mul_f32 v156, s57, v156\nv_mul_f32 v157, s57, v157\nCV 154,156,157\nMvaa 44,148,44\ns_add_u32 s88, s88, 32768\nEX 80,32\nv_mul_f32 v158, s57, v158\nv_mul_f32 v159, s57, v159\n"
"CV 155,158,159\nds_write_b128 v23, v[152:155] offset:6144\nMvaa 44,152,52\nv_mul_f32 v160, s57, v160\nv_mul_f32 v161, s57, v161\nCV 160,160,161\nv_mul_f32 v162, s57, v162\n"
"v_mul_f32 v163, s57, v163\nCV 161,162,163\nv_mul_f32 v164, s57, v164\nMvaa 44,156,60\nEX 81,33\nv_mul_f32 v165, s57, v165\nCV 162,164,165\nv_mul_f32 v166, s57, v166\n"
"Mvaa 40,156,56\nv_mul_f32 v167, s57, v167\nCV 163,166,167\nds_write_b128 v23, v[160:163] offset:1024\nv_mul_f32 v168, s57, v168\nv_mul_f32 v169, s57, v169\nCV 168,168,169\n"
"v_mul_f32 v170, s57, v170\nMvaa 40,152,48\nD1 1,16,46,2048\nv_mul_f32 v171, s57, v171\nCV 169,170,171\nv_mul_f32 v172, s57, v172\nv_mul_f32 v173, s57, v173\nCV 170,172,173\n"
"v_mul_f32 v174, s57, v174\nMvaa 40,148,40\nv_mul_f32 v175, s57, v175\nCV 171,174,175\nds_write_b128 v23, v[168:171] offset:3072\nv_mul_f32 v176, s57, v176\n"
"v_mul_f32 v177, s57, v177\nCV 176,176,177\nv_mul_f32 v178, s57, v178\nMvaa 40,144,32\nAD 84,80,81\nCV 64,80,81\nv_mul_f32 v179, s57, v179\nCV 177,178,179\n"
"v_mul_f32 v180, s57, v180\nv_mul_f32 v181, s57, v181\nCV 178,180,181\nSa 108,1,16,78,3072\nv_mul_f32 v182, s57, v182\nv_mul_f32 v183, s57, v183\nCV 179,182,183\n"
"ds_write_b128 v23, v[176:179] offset:5120\nv_mul_f32 v184, s57, v184\nv_mul_f32 v185, s57, v185\nEX 82,34\nCV 184,184,185\nv_mul_f32 v186, s57, v186\nv_mul_f32 v187, s57, v187\n"
"CV 185,186,187\nv_mul_f32 v188, s57, v188\nv_mul_f32 v189, s57, v189\nCV 186,188,189\nv_mul_f32 v190, s57, v190\nv_mul_f32 v191, s57, v191\nCV 187,190,191\n"
"ds_write_b128 v23, v[184:187] offset:7168\ns_waitcnt vmcnt(16) lgkmcnt(0)\nEX 83,35\nAD 84,84,82\nD1 1,16,46,3072\ns_add_u32 m0, s89, 49152\ns_barrier\nAD 84,84,83\nCV 65,82,83\n"
"Ra 128,24,32768\nRa 224,22,0\nRa 228,22,1024\nRa 232,22,2048\ns_add_u32 s46, s87, s88\nEX 80,36\nRa 132,24,33792\nRa 236,22,3072\nRa 240,22,4096\nEX 81,37\nRa 136,24,34816\n"
"Ra 244,22,5120\nRa 248,22,6144\nRa 252,22,7168\nD1 1,16,46,0\nv_mul_f32 v104, s57, v104\nv_mul_f32 v105, s57, v105\nRa 140,24,35840\nCV 104,104,105\nv_mul_f32 v106, s57, v106\n"
"AD 85,80,81\nCV 68,80,81\nv_mul_f32 v107, s57, v107\nRa 144,26,32768\nCV 105,106,107\nv_mul_f32 v108, s57, v108\nv_mul_f32 v109, s57, v109\nEX 82,38\nCV 106,108,109\n"
"Ra 148,26,33792\nv_mul_f32 v110, s57, v110\nv_mul_f32 v111, s57, v111\nEX 83,39\nRa 152,26,34816\nAD 85,85,82\nCV 107,110,111\nAW 204,104\nD1 1,16,46,1024\nAW 205,105\nAW 206,106\n"
"Ra 156,26,35840\nAW 207,107\nv_mul_f32 v112, s57, v112\nAD 85,85,83\nCV 69,82,83\nv_mul_f32 v113, s57, v113\nRa 160,24,40960\nCV 112,112,113\nv_mul_f32 v114, s57, v114\n"
"v_mul_f32 v115, s57, v115\ns_xor_b32 s89, s89, 0x10000\nEX 80,40\nRa 164,24,41984\nCV 113,114,115\nv_mul_f32 v116, s57, v116\nWL 6\nEX 81,41\nRa 168,24,43008\nAD 84,84,80\n"
"v_mul_f32 v117, s57, v117\nCV 114,116,117\nD1 1,16,46,2048\nv_mul_f32 v118, s57, v118\nv_mul_f32 v119, s57, v119\nRa 172,24,44032\nCV 115,118,119\nAW 212,112\nAD 84,84,81\n"
"CV 66,80,81\nAW 213,113\nCvaa 48,128,0,4\nRa 176,26,40960\nAW 214,114\nAW 215,115\nv_mul_f32 v120, s57, v120\nMvaa 48,132,8\nEX 82,42\nv_mul_f32 v121, s57, v121\nMvaa 48,136,16\n"
"Ra 180,26,41984\nCV 120,120,121\nv_mul_f32 v122, s57, v122\nMvaa 48,140,24\nWL 6\nEX 83,43\nWV 8\nv_cmp_ne_u32 vcc, s15, v27\ns_nop 0\ns_cbranch_vccnz .S289\n.S290:\n"
"Cvaa 52,140,28,8\nRa 184,26,43008\nAD 84,84,82\nv_mul_f32 v123, s57, v123\nCV 121,122,123\nMvaa 52,136,20\nD1 1,16,46,3072\ns_add_u32 m0, s89, 0\nv_mul_f32 v124, s57, v124\n"
"Mvaa 52,132,12\nRa 188,26,44032\nv_mul_f32 v125, s57, v125\nCV 122,124,125\nMvaa 52,128,4\nAD 84,84,83\nCV 67,82,83\nv_mul_f32 v126, s57, v126\nMvaa 52,144,36\nRa 128,2,49152\n"
"s_add_u32 s78, s78, s55\nSa 112,1,16,78,0\nv_mul_f32 v127, s57, v127\nMvaa 52,148,44\ns_add_u32 s46, s86, s88\nEX 80,44\nMvaa 52,152,52\nRa 132,2,50176\n"
"v_xor_b32 v24, 0x10000, v24\nCV 123,126,127\nMvaa 52,156,60\nWL 6\nEX 81,45\nMvaa 48,156,56\nRa 136,2,51200\nAD 85,85,80\nAW 220,120\nAW 221,121\nMvaa 48,152,48\nD1 1,16,46,0\n"
"AW 222,122\nAW 223,123\nMvaa 48,148,40\nRa 140,2,52224\nv_mov_b32 v115, -1\nMvaa 48,144,32\nAD 85,85,81\nCV 70,80,81\nCvaa 56,160,0,12\nRa 144,2,53248\nSa 116,1,16,78,1024\n"
"Mvaa 56,164,8\nEX 82,46\nMvaa 56,168,16\nRa 148,2,54272\nv_xor_b32 v26, 0x10000, v26\nMvaa 56,172,24\nWL 6\nEX 83,47\nCvaa 60,172,28,16\nRa 152,2,55296\nAD 85,85,82\n"
"Mvaa 60,168,20\nD1 1,16,46,1024\nMvaa 60,164,12\nRa 156,2,56320\nMvaa 60,160,4\nAD 85,85,83\nCV 71,82,83\nMvaa 60,176,36\nRa 160,2,57344\nv_max_f32 v90, v84, v85\nMvaa 60,180,44\n"
"s_add_u32 s88, s88, 32768\nEX 80,48\nMvaa 60,184,52\nRa 164,2,58368\nv_cmp_lt_f32 vcc, s63, v90\nMvaa 60,188,60\nWL 8\nEX 81,49\nMvaa 56,188,56\nRa 168,2,59392\n"
"s_cbranch_vccnz .T278\n.T279:\nv_add_f32 v20, s93, v84\nv_mov_b32 v21, v85\nv_mov_b32 v22, v20\nv_mov_b32 v23, v21\nMvaa 56,184,48\nD1 1,16,46,2048\n"
"v_permlane32_swap_b32 v20, v22\nv_permlane32_swap_b32 v21, v23\nAD 20,20,22\nAD 21,21,23\nv_mov_b32 v22, v20\nv_mov_b32 v23, v21\nMvaa 56,180,40\nRa 172,2,60416\n"
"v_permlane16_swap_b32 v20, v22\nv_permlane16_swap_b32 v21, v23\nAD 20,20,22\nAD 21,21,23\nv_min_f32 v90, v20, v21\nv_cmp_gt_f32 vcc, s64, v90\ns_nop 0\ns_cbranch_vccnz .T280\n"
".T281:\nAD 86,86,84\nMvaa 56,176,32\nAD 84,80,81\nCV 72,80,81\nZvav 128,128,64\nRa 176,2,61440\nSa 120,1,16,78,2048\nZvav 132,128,68\nEX 82,50\nZvav 140,132,68\nRa 180,2,62464\n"
"Zvav 136,132,64\nWL 8\nEX 83,51\nZvav 144,136,64\nRa 184,2,63488\nAD 84,84,82\nZvav 148,136,68\nD1 1,16,46,3072\ns_add_u32 m0, s89, 16384\nZvav 156,140,68\nRa 188,2,64512\n"
"s_barrier\nZvav 152,140,64\nAD 84,84,83\nCV 73,82,83\nZvav 160,144,64\nRa 128,24,0\nSa 124,1,16,78,3072\nZvav 164,144,68\ns_add_u32 s46, s87, s88\nEX 80,52\nZvav 172,148,68\n"
"Ra 132,24,1024\nv_xor_b32 v2, 0x10000, v2\nZvav 168,148,64\nWL 8\nEX 81,53\nZvav 176,152,64\nRa 136,24,2048\nAD 87,87,85\nZvav 180,152,68\nD1 1,16,46,0\nZvav 188,156,68\n"
"Ra 140,24,3072\nZvav 184,156,64\nAD 85,80,81\nCV 76,80,81\n.W574:\ns_cbranch_execz .W578\nZvav 192,160,64\nRa 144,26,0\nZvav 196,160,68\ns_cmp_lt_u32 s99, s92\nEX 82,54\n"
"Zvav 204,164,68\nRa 148,26,1024\ns_cbranch_scc0 .E276\nv_cmpx_gt_i32 vcc, 19, v3\nv_mov_b32 v15, s97\nv_cmpx_gt_i32 vcc, 18, v3\nv_mov_b32 v14, s97\nv_cmpx_gt_i32 vcc, 17, v3\n"
"v_mov_b32 v13, s97\nv_cmpx_gt_i32 vcc, 16, v3\nv_mov_b32 v12, s97\nv_cmpx_gt_i32 vcc, 3, v3\nv_mov_b32 v7, s97\nv_cmpx_gt_i32 vcc, 2, v3\nv_mov_b32 v6, s97\n"
"v_cmpx_gt_i32 vcc, 1, v3\nv_mov_b32 v5, s97\nv_cmpx_gt_i32 vcc, 0, v3\nv_mov_b32 v4, s97\ns_mov_b64 exec, -1\ns_nop 2\n.E276:\ns_nop 0\nZvav 200,164,64\nWL 8\nEX 83,55\n"
"Zvav 208,168,64\nRa 152,26,2048\nAD 85,85,82\nZvav 212,168,68\nD1 1,16,46,1024\nZvav 220,172,68\nRa 156,26,3072\nZvav 216,172,64\nAD 85,85,83\nCV 77,82,83\nZvav 224,176,64\n"
"Ra 160,24,8192\nZvav 228,176,68\nEX 80,56\nZvav 236,180,68\nRa 164,24,9216\nZvav 232,180,64\nWL 6\nEX 81,57\nZvav 240,184,64\nRa 168,24,10240\nAD 84,84,80\n"
"v_and_b32 v27, 0x70, v1\nbuffer_load_dword v27, v27, s[16:19], s61 offen offset:140 sc0 sc1\nZvav 244,184,68\nD1 1,16,46,2048\nZvav 252,188,68\nRa 172,24,11264\nZvav 248,188,64\n"
"AD 84,84,81\nCV 74,80,81\ns_sub_u32 s99, s99, 1\n.p2align 6\n.A304:\nCvaa 32,128,0,4\nRa 176,26,8192\nMvaa 32,132,8\ns_add_u32 s49, s49, 131072\nEX 82,58\nMvaa 32,136,16\n"
"Ra 180,26,9216\nMvaa 32,140,24\nWL 6\nEX 83,59\nCvaa 36,140,28,8\nRa 184,26,10240\nAD 84,84,82\nMvaa 36,136,20\nD1 1,16,46,3072\ns_add_u32 m0, s89, 32768\nMvaa 36,132,12\n"
"Ra 188,26,11264\nMvaa 36,128,4\nAD 84,84,83\nCV 75,82,83\nMvaa 36,144,36\nRa 128,2,16384\nMvaa 36,148,44\ns_add_u32 s46, s86, s88\nEX 80,60\nMvaa 36,152,52\nRa 132,2,17408\n"
"Mvaa 36,156,60\nWL 6\nEX 81,61\nMvaa 32,156,56\nRa 136,2,18432\nAD 85,85,80\nMvaa 32,152,48\nD1 1,16,46,0\nMvaa 32,148,40\nRa 140,2,19456\nMvaa 32,144,32\nAD 85,85,81\n"
"CV 78,80,81\nCvaa 40,160,0,12\nRa 144,2,20480\nMvaa 40,164,8\nSv 112,1,16,49,0\nEX 82,62\nMvaa 40,168,16\nRa 148,2,21504\nMvaa 40,172,24\nWL 6\nEX 83,63\nCvaa 44,172,28,16\n"
"Ra 152,2,22528\nAD 85,85,82\nMvaa 44,168,20\nD1 1,16,46,1024\nMvaa 44,164,12\nRa 156,2,23552\nMvaa 44,160,4\nAD 85,85,83\nCV 79,82,83\nMvaa 44,176,36\nRa 160,2,24576\n"
"v_max_f32 v90, v84, v85\nMvaa 44,180,44\ns_add_u32 s88, s88, 32768\nEX 80,32\nMvaa 44,184,52\nRa 164,2,25600\nv_cmp_lt_f32 vcc, s63, v90\nMvaa 44,188,60\nWL 8\nEX 81,33\n"
"Mvaa 40,188,56\nRa 168,2,26624\ns_cbranch_vccnz .T312\n.T313:\nMvaa 40,184,48\nD1 1,16,46,2048\nMvaa 40,180,40\nRa 172,2,27648\nAD 86,86,84\nMvaa 40,176,32\nAD 84,80,81\n"
"CV 64,80,81\nMvav 128,128,72\nRa 176,2,28672\nMvav 132,128,76\nEX 82,34\nMvav 140,132,76\nRa 180,2,29696\nMvav 136,132,72\ns_waitcnt vmcnt(13) lgkmcnt(8)\nEX 83,35\n"
"Mvav 144,136,72\nRa 184,2,30720\nAD 84,84,82\nMvav 148,136,76\nD1 1,16,46,3072\ns_add_u32 m0, s89, 49152\nMvav 156,140,76\nRa 188,2,31744\ns_barrier\nMvav 152,140,72\nAD 84,84,83\n"
"CV 65,82,83\nMvav 160,144,72\nRa 128,24,32768\nMvav 164,144,76\ns_add_u32 s46, s87, s88\nEX 80,36\nMvav 172,148,76\nRa 132,24,33792\nMvav 168,148,72\nWL 8\nEX 81,37\n"
"Mvav 176,152,72\nRa 136,24,34816\nAD 87,87,85\nMvav 180,152,76\nD1 1,16,46,0\nMvav 188,156,76\nRa 140,24,35840\nMvav 184,156,72\nAD 85,80,81\nCV 68,80,81\nMvav 192,160,72\n"
"Ra 144,26,32768\nMvav 196,160,76\ns_cmp_eq_u32 s99, 0\nEX 82,38\nMvav 204,164,76\nRa 148,26,33792\ns_cbranch_scc1 .E306\n.E307:\nMvav 200,164,72\nWL 8\nEX 83,39\nMvav 208,168,72\n"
"Ra 152,26,34816\nAD 85,85,82\nMvav 212,168,76\nD1 1,16,46,1024\nMvav 220,172,76\nRa 156,26,35840\nMvav 216,172,72\nAD 85,85,83\nCV 69,82,83\nMvav 224,176,72\nRa 160,24,40960\n"
"Mvav 228,176,76\ns_xor_b32 s89, s89, 0x10000\nEX 80,40\nMvav 236,180,76\nRa 164,24,41984\nMvav 232,180,72\nWL 6\nEX 81,41\nMvav 240,184,72\nRa 168,24,43008\nAD 84,84,80\n"
"Mvav 244,184,76\nD1 1,16,46,2048\nMvav 252,188,76\nRa 172,24,44032\nMvav 248,188,72\nAD 84,84,81\nCV 66,80,81\nCvaa 48,128,0,4\nRa 176,26,40960\nMvaa 48,132,8\n"
"s_cmp_eq_u32 s99, 0\nEX 82,42\nMvaa 48,136,16\nRa 180,26,41984\ns_cselect_b32 s86, s94, s86\nMvaa 48,140,24\nWL 6\nEX 83,43\nCvaa 52,140,28,8\nRa 184,26,43008\nAD 84,84,82\n"
"Mvaa 52,136,20\nD1 1,16,46,3072\ns_add_u32 m0, s89, 0\nMvaa 52,132,12\nRa 188,26,44032\nMvaa 52,128,4\nAD 84,84,83\nCV 67,82,83\nMvaa 52,144,36\nRa 128,2,49152\nMvaa 52,148,44\n"
"s_add_u32 s46, s86, s88\nEX 80,44\nMvaa 52,152,52\nRa 132,2,50176\nv_xor_b32 v24, 0x10000, v24\nMvaa 52,156,60\nWL 6\nEX 81,45\nMvaa 48,156,56\nRa 136,2,51200\nAD 85,85,80\n"
"Mvaa 48,152,48\nD1 1,16,46,0\nMvaa 48,148,40\nRa 140,2,52224\nMvaa 48,144,32\nAD 85,85,81\nCV 70,80,81\nCvaa 56,160,0,12\nRa 144,2,53248\nMvaa 56,164,8\nSv 112,1,16,49,1024\n"
"EX 82,46\nMvaa 56,168,16\nRa 148,2,54272\nv_xor_b32 v26, 0x10000, v26\nMvaa 56,172,24\nWL 6\nEX 83,47\nCvaa 60,172,28,16\nRa 152,2,55296\nAD 85,85,82\nMvaa 60,168,20\n"
"D1 1,16,46,1024\nMvaa 60,164,12\nRa 156,2,56320\nMvaa 60,160,4\nAD 85,85,83\nCV 71,82,83\nMvaa 60,176,36\nRa 160,2,57344\nv_max_f32 v90, v84, v85\nMvaa 60,180,44\n"
"s_add_u32 s88, s88, 32768\nEX 80,48\nMvaa 60,184,52\nRa 164,2,58368\nv_cmp_lt_f32 vcc, s63, v90\nMvaa 60,188,60\nWL 8\nEX 81,49\nMvaa 56,188,56\nRa 168,2,59392\n"
"s_cbranch_vccnz .T314\n.T315:\nMvaa 56,184,48\nD1 1,16,46,2048\nMvaa 56,180,40\nRa 172,2,60416\nAD 86,86,84\nMvaa 56,176,32\nAD 84,80,81\nCV 72,80,81\nMvav 128,128,64\n"
"Ra 176,2,61440\nMvav 132,128,68\nEX 82,50\nMvav 140,132,68\nRa 180,2,62464\nMvav 136,132,64\ns_waitcnt vmcnt(13) lgkmcnt(8)\nEX 83,51\nMvav 144,136,64\nRa 184,2,63488\n"
"AD 84,84,82\nMvav 148,136,68\nD1 1,16,46,3072\ns_add_u32 m0, s89, 16384\nMvav 156,140,68\nRa 188,2,64512\ns_barrier\nMvav 152,140,64\nAD 84,84,83\nCV 73,82,83\nMvav 160,144,64\n"
"Ra 128,24,0\nMvav 164,144,68\ns_add_u32 s46, s87, s88\nEX 80,52\nMvav 172,148,68\nRa 132,24,1024\nv_xor_b32 v2, 0x10000, v2\nMvav 168,148,64\nWL 8\nEX 81,53\nMvav 176,152,64\n"
"Ra 136,24,2048\nAD 87,87,85\nMvav 180,152,68\nD1 1,16,46,0\nMvav 188,156,68\nRa 140,24,3072\nMvav 184,156,64\nAD 85,80,81\nCV 76,80,81\nMvav 192,160,64\nRa 144,26,0\n"
"Mvav 196,160,68\ns_cmp_lt_u32 s99, s92\nEX 82,54\nMvav 204,164,68\nRa 148,26,1024\ns_cbranch_scc1 .E308\n.E309:\nMvav 200,164,64\nWL 8\nEX 83,55\nMvav 208,168,64\nRa 152,26,2048\n"
"AD 85,85,82\nMvav 212,168,68\nD1 1,16,46,1024\nMvav 220,172,68\nRa 156,26,3072\nMvav 216,172,64\nAD 85,85,83\nCV 77,82,83\nMvav 224,176,64\nRa 160,24,8192\nMvav 228,176,68\n"
"s_sub_u32 s90, s90, 1\nEX 80,56\nMvav 236,180,68\nRa 164,24,9216\ns_cbranch_scc1 .E310\n.E311:\nMvav 232,180,64\nWL 6\nEX 81,57\nMvav 240,184,64\nRa 168,24,10240\nAD 84,84,80\n"
"Mvav 244,184,68\nD1 1,16,46,2048\ns_sub_u32 s99, s99, 1\nMvav 252,188,68\nRa 172,24,11264\nMvav 248,188,64\nAD 84,84,81\nCV 74,80,81\ns_cbranch_scc0 .A304\ns_branch .A316\n.E306:\n"
"v_cmpx_gt_i32 vcc, 19, v0\nv_mov_b32 v15, s97\nv_cmpx_gt_i32 vcc, 18, v0\nv_mov_b32 v14, s97\nv_cmpx_gt_i32 vcc, 17, v0\nv_mov_b32 v13, s97\nv_cmpx_gt_i32 vcc, 16, v0\n"
"v_mov_b32 v12, s97\nv_cmpx_gt_i32 vcc, 3, v0\nv_mov_b32 v7, s97\nv_cmpx_gt_i32 vcc, 2, v0\nv_mov_b32 v6, s97\nv_cmpx_gt_i32 vcc, 1, v0\nv_mov_b32 v5, s97\n"
"v_cmpx_gt_i32 vcc, 0, v0\nv_mov_b32 v4, s97\ns_mov_b64 exec, -1\ns_nop 2\ns_branch .E307\n.E308:\ns_cmp_eq_u32 s99, 0\ns_cbranch_scc1 .K305\nv_cmpx_gt_i32 vcc, 19, v3\n"
"v_mov_b32 v15, s97\nv_cmpx_gt_i32 vcc, 18, v3\nv_mov_b32 v14, s97\nv_cmpx_gt_i32 vcc, 17, v3\nv_mov_b32 v13, s97\nv_cmpx_gt_i32 vcc, 16, v3\nv_mov_b32 v12, s97\n"
"v_cmpx_gt_i32 vcc, 3, v3\nv_mov_b32 v7, s97\nv_cmpx_gt_i32 vcc, 2, v3\nv_mov_b32 v6, s97\nv_cmpx_gt_i32 vcc, 1, v3\nv_mov_b32 v5, s97\nv_cmpx_gt_i32 vcc, 0, v3\nv_mov_b32 v4, s97\n"
"s_mov_b64 exec, -1\ns_nop 2\ns_branch .E309\n.K305:\nv_cmpx_gt_i32 vcc, 19, v3\nv_mov_b32 v19, s97\nv_cmpx_gt_i32 vcc, 18, v3\nv_mov_b32 v18, s97\nv_cmpx_gt_i32 vcc, 17, v3\n"
"v_mov_b32 v17, s97\nv_cmpx_gt_i32 vcc, 16, v3\nv_mov_b32 v16, s97\nv_cmpx_gt_i32 vcc, 3, v3\nv_mov_b32 v11, s97\nv_cmpx_gt_i32 vcc, 2, v3\nv_mov_b32 v10, s97\n"
"v_cmpx_gt_i32 vcc, 1, v3\nv_mov_b32 v9, s97\nv_cmpx_gt_i32 vcc, 0, v3\nv_mov_b32 v8, s97\ns_mov_b64 exec, -1\ns_nop 2\ns_branch .E309\n.E310:\ns_swappc_b64 s[80:81], s[8:9]\n"
"s_branch .E311\n.HD268:\nv_cmp_ne_u32 vcc, s15, v27\ns_nop 0\ns_cbranch_vccnz .S292\n.S293:\nbuffer_store_dword v25, off, s[16:19], s54 offset:4 sc0 sc1\nv_and_b32 v27, 0x70, v1\n"
"buffer_load_dword v27, v27, s[16:19], s61 offen offset:12 sc0 sc1\ns_mov_b32 s90, 1\ns_swappc_b64 s[8:9], s[80:81]\nv_cmp_ne_u32 vcc, s15, v27\ns_nop 0\ns_cbranch_vccnz .S295\n"
".S296:\nv_and_b32 v27, 0x70, v1\nbuffer_load_dword v27, v27, s[16:19], s61 offen offset:8 sc0 sc1\ns_mov_b32 s90, 1\ns_swappc_b64 s[8:9], s[80:81]\nv_cmp_ne_u32 vcc, s15, v27\n"
"s_nop 0\ns_cbranch_vccnz .S298\n.S299:\nv_and_b32 v27, 0xf0, v1\nbuffer_load_dword v27, v27, s[16:19], s61 offen offset:4 sc0 sc1\ns_mov_b32 s90, 1\ns_swappc_b64 s[8:9], s[80:81]\n"
"v_cmp_ne_u32 vcc, s15, v27\ns_nop 0\ns_cbranch_vccnz .S301\n.S302:\ns_mov_b32 s90, 100\ns_swappc_b64 s[8:9], s[80:81]\n.A316:\nRa 176,26,8192\ns_add_u32 s49, s49, 131072\n"
"EX 82,58\nRa 180,26,9216\nWL 6\nEX 83,59\nCvaa 36,140,28,8\nRa 184,26,10240\nAD 84,84,82\nMvaa 36,136,20\nD1 1,16,46,3072\ns_add_u32 m0, s89, 32768\nMvaa 36,132,12\n"
"Ra 188,26,11264\nMvaa 36,128,4\nAD 84,84,83\nCV 75,82,83\nMvaa 36,144,36\nRa 128,2,16384\nMvaa 36,148,44\ns_add_u32 s46, s86, s88\nEX 80,60\nMvaa 36,152,52\nRa 132,2,17408\n"
"Mvaa 36,156,60\nEX 81,61\nRa 136,2,18432\nAD 85,85,80\nD1 1,16,46,0\nRa 140,2,19456\nAD 85,85,81\nCV 78,80,81\nRa 144,2,20480\nSv 112,1,16,49,0\ns_mov_b32 s87, s95\nEX 82,62\n"
"Ra 148,2,21504\nWL 6\nEX 83,63\nCvaa 44,172,28,16\nRa 152,2,22528\nAD 85,85,82\nMvaa 44,168,20\nD1 1,16,46,1024\nMvaa 44,164,12\nRa 156,2,23552\nMvaa 44,160,4\nAD 85,85,83\n"
"CV 79,82,83\nMvaa 44,176,36\nRa 160,2,24576\nv_max_f32 v90, v84, v85\nMvaa 44,180,44\ns_add_u32 s88, s88, 32768\nMvaa 44,184,52\nRa 164,2,25600\nv_cmp_lt_f32 vcc, s63, v90\n"
"Mvaa 44,188,60\nWL 8\nRa 168,2,26624\ns_cbranch_vccnz .T318\n.T319:\nD1 1,16,46,2048\nRa 172,2,27648\nAD 86,86,84\nMvav 128,128,72\nRa 176,2,28672\nMvav 132,128,76\n"
"Mvav 140,132,76\nRa 180,2,29696\nMvav 136,132,72\ns_waitcnt vmcnt(13) lgkmcnt(8)\nMvav 144,136,72\nRa 184,2,30720\nMvav 148,136,76\nD1 1,16,46,3072\ns_add_u32 m0, s89, 49152\n"
"Mvav 156,140,76\nRa 188,2,31744\ns_barrier\nMvav 152,140,72\nMvav 160,144,72\nRa 128,24,32768\nMvav 164,144,76\ns_add_u32 s46, s87, s88\nEX 80,36\nMvav 172,148,76\n"
"Ra 132,24,33792\nMvav 168,148,72\nWL 8\nEX 81,37\nMvav 176,152,72\nRa 136,24,34816\nAD 87,87,85\nMvav 180,152,76\nD1 1,16,46,0\nMvav 188,156,76\nRa 140,24,35840\nMvav 184,156,72\n"
"AD 85,80,81\nCV 68,80,81\nMvav 192,160,72\nRa 144,26,32768\nMvav 196,160,76\ns_nop 0\nEX 82,38\nMvav 204,164,76\nRa 148,26,33792\nv_cmpx_gt_i32 vcc, 19, v0\nv_mov_b32 v19, s97\n"
"v_cmpx_gt_i32 vcc, 18, v0\nv_mov_b32 v18, s97\nv_cmpx_gt_i32 vcc, 17, v0\nv_mov_b32 v17, s97\nv_cmpx_gt_i32 vcc, 16, v0\nv_mov_b32 v16, s97\nv_cmpx_gt_i32 vcc, 3, v0\n"
"v_mov_b32 v11, s97\nv_cmpx_gt_i32 vcc, 2, v0\nv_mov_b32 v10, s97\nv_cmpx_gt_i32 vcc, 1, v0\nv_mov_b32 v9, s97\nv_cmpx_gt_i32 vcc, 0, v0\nv_mov_b32 v8, s97\ns_mov_b64 exec, -1\n"
"s_nop 2\ns_nop 0\nMvav 200,164,72\nWL 8\nEX 83,39\nMvav 208,168,72\nRa 152,26,34816\nAD 85,85,82\nMvav 212,168,76\nD1 1,16,46,1024\nMvav 220,172,76\nRa 156,26,35840\n"
"Mvav 216,172,72\nAD 85,85,83\nCV 69,82,83\nMvav 224,176,72\nRa 160,24,40960\nMvav 228,176,76\ns_xor_b32 s89, s89, 0x10000\nMvav 236,180,76\nRa 164,24,41984\nMvav 232,180,72\n"
"WL 10\nMvav 240,184,72\nRa 168,24,43008\nMvav 244,184,76\nD1 1,16,46,2048\nMvav 252,188,76\nRa 172,24,44032\nMvav 248,188,72\nRa 176,26,40960\nRa 180,26,41984\n.W575:\n"
"s_cbranch_execz .W579\nWL 6\nCvaa 52,140,28,8\nRa 184,26,43008\nMvaa 52,136,20\nD1 1,16,46,3072\ns_add_u32 m0, s89, 0\nMvaa 52,132,12\nRa 188,26,44032\nMvaa 52,128,4\n"
"Mvaa 52,144,36\nRa 128,2,49152\nMvaa 52,148,44\ns_add_u32 s46, s86, s88\nEX 80,44\nMvaa 52,152,52\nRa 132,2,50176\nv_xor_b32 v24, 0x10000, v24\nMvaa 52,156,60\nEX 81,45\n"
"Ra 136,2,51200\nAD 85,85,80\nD1 1,16,46,0\nRa 140,2,52224\nAD 85,85,81\nCV 70,80,81\nRa 144,2,53248\nSv 112,1,16,49,1024\nEX 82,46\nRa 148,2,54272\nv_xor_b32 v26, 0x10000, v26\n"
"WL 6\nEX 83,47\nCvaa 60,172,28,16\nRa 152,2,55296\nAD 85,85,82\nMvaa 60,168,20\nD1 1,16,46,1024\nMvaa 60,164,12\nRa 156,2,56320\nMvaa 60,160,4\nAD 85,85,83\nCV 71,82,83\n"
"Mvaa 60,176,36\nRa 160,2,57344\nMvaa 60,180,44\ns_add_u32 s88, s88, 32768\nMvaa 60,184,52\nRa 164,2,58368\nv_cmp_lt_f32 vcc, s63, v85\nMvaa 60,188,60\nWL 8\nRa 168,2,59392\n"
"s_cbranch_vccnz .T320\n.T321:\nD1 1,16,46,2048\nRa 172,2,60416\nRa 176,2,61440\nMvav 132,128,68\nMvav 140,132,68\nRa 180,2,62464\ns_waitcnt vmcnt(13) lgkmcnt(8)\nRa 184,2,63488\n"
"Mvav 148,136,68\nD1 1,16,46,3072\ns_add_u32 m0, s89, 16384\nMvav 156,140,68\nRa 188,2,64512\ns_barrier\nRa 128,24,0\nMvav 164,144,68\ns_add_u32 s46, s87, s88\nEX 80,52\n"
"Mvav 172,148,68\nRa 132,24,1024\nv_xor_b32 v2, 0x10000, v2\nWL 8\nEX 81,53\nRa 136,24,2048\nAD 87,87,85\nMvav 180,152,68\nD1 1,16,46,0\nMvav 188,156,68\nRa 140,24,3072\n"
"AD 85,80,81\nCV 76,80,81\nRa 144,26,0\nMvav 196,160,68\nEX 82,54\nMvav 204,164,68\nRa 148,26,1024\nWL 8\nEX 83,55\nRa 152,26,2048\nAD 85,85,82\nMvav 212,168,68\nD1 1,16,46,1024\n"
"Mvav 220,172,68\nRa 156,26,3072\nAD 85,85,83\nCV 77,82,83\nRa 160,24,8192\nMvav 228,176,68\ns_sub_u32 s90, s90, 1\nMvav 236,180,68\nRa 164,24,9216\nWL 6\nRa 168,24,10240\n"
"Mvav 244,184,68\nD1 1,16,46,2048\nMvav 252,188,68\nRa 172,24,11264\n.SW274:\nv_mov_b32 v4, 0\nv_mov_b32 v5, 0\nv_mov_b32 v6, 0\nv_mov_b32 v7, 0\nv_mov_b32 v8, 0\nv_mov_b32 v9, 0\n"
"v_mov_b32 v10, 0\nv_mov_b32 v11, 0\nv_mov_b32 v12, 0\nv_mov_b32 v13, 0\nv_mov_b32 v14, 0\nv_mov_b32 v15, 0\nv_mov_b32 v16, 0\nv_mov_b32 v17, 0\nv_mov_b32 v18, 0\nv_mov_b32 v19, 0\n"
"s_add_u32 s99, s98, 1\ns_mov_b32 s94, s96\ns_add_u32 s95, s96, 65536\ns_cmp_lt_u32 s99, s92\ns_cbranch_scc0 .E322\nv_cmpx_gt_i32 vcc, 19, v3\nv_mov_b32 v15, s97\n"
"v_cmpx_gt_i32 vcc, 18, v3\nv_mov_b32 v14, s97\nv_cmpx_gt_i32 vcc, 17, v3\nv_mov_b32 v13, s97\nv_cmpx_gt_i32 vcc, 16, v3\nv_mov_b32 v12, s97\nv_cmpx_gt_i32 vcc, 3, v3\n"
"v_mov_b32 v7, s97\nv_cmpx_gt_i32 vcc, 2, v3\nv_mov_b32 v6, s97\nv_cmpx_gt_i32 vcc, 1, v3\nv_mov_b32 v5, s97\nv_cmpx_gt_i32 vcc, 0, v3\nv_mov_b32 v4, s97\ns_mov_b64 exec, -1\n"
"s_nop 2\n.E322:\ns_nop 0\nCvaa 32,128,192,4\nRa 176,26,8192\nMvaa 32,132,200\nMvaa 32,136,208\nRa 180,26,9216\nMvaa 32,140,216\nWL 6\nCvaa 36,140,220,8\nRa 184,26,10240\n"
"Mvaa 36,136,212\nD1 1,16,46,3072\ns_add_u32 m0, s89, 32768\nMvaa 36,132,204\nRa 188,26,11264\nMvaa 36,128,196\nMvaa 36,144,228\nRa 128,2,16384\nMvaa 36,148,236\n"
"s_add_u32 s46, s86, s88\nEX 80,60\nMvaa 36,152,244\nRa 132,2,17408\nMvaa 36,156,252\nWL 6\nEX 81,61\nMvaa 32,156,248\nRa 136,2,18432\nAD 85,85,80\nMvaa 32,152,240\nD1 1,16,46,0\n"
"Mvaa 32,148,232\nRa 140,2,19456\nMvaa 32,144,224\nAD 85,85,81\nCV 78,80,81\nCvaa 40,160,192,12\nRa 144,2,20480\nMvaa 40,164,200\nEX 82,62\nMvaa 40,168,208\nRa 148,2,21504\n"
"Mvaa 40,172,216\nWL 6\nEX 83,63\nCvaa 44,172,220,16\nRa 152,2,22528\nAD 85,85,82\nMvaa 44,168,212\nD1 1,16,46,1024\nMvaa 44,164,204\nRa 156,2,23552\nMvaa 44,160,196\nAD 85,85,83\n"
"CV 79,82,83\nMvaa 44,176,228\nRa 160,2,24576\nMvaa 44,180,236\ns_add_u32 s88, s88, 32768\nEX 80,32\nMvaa 44,184,244\nRa 164,2,25600\nv_cmp_lt_f32 vcc, s63, v85\nMvaa 44,188,252\n"
"WL 8\nEX 81,33\nMvaa 40,188,248\nRa 168,2,26624\ns_cbranch_vccnz .T327\n.T328:\nMvaa 40,184,240\nD1 1,16,46,2048\nCV 128,128,129\nCV 129,130,131\nMvaa 40,180,232\nRa 172,2,27648\n"
"CV 130,136,137\nCV 131,138,139\nMvaa 40,176,224\nAD 84,80,81\nCV 64,80,81\ns_add_u32 s5, s66, 0\nSv 128,1,16,5,0\nRa 176,2,28672\nCV 144,144,145\nCV 145,146,147\nMvav 132,128,76\n"
"EX 82,34\nMvav 140,132,76\nRa 180,2,29696\nCV 146,152,153\nCV 147,154,155\ns_waitcnt vmcnt(13) lgkmcnt(8)\nEX 83,35\ns_add_u32 s5, s66, 0\nSv 144,1,16,5,2048\nRa 184,2,30720\n"
"AD 84,84,82\nCV 160,160,161\nMvav 148,136,76\nD1 1,16,46,3072\ns_add_u32 m0, s89, 49152\nCV 161,162,163\nMvav 156,140,76\nRa 188,2,31744\ns_barrier\nCV 162,168,169\n"
"CV 163,170,171\nAD 84,84,83\nCV 65,82,83\ns_add_u32 s5, s66, 4096\nSv 160,1,16,5,0\nRa 128,24,32768\nCV 176,176,177\nCV 177,178,179\nMvav 164,144,76\ns_add_u32 s46, s87, s88\n"
"EX 80,36\nMvav 172,148,76\nRa 132,24,33792\nCV 178,184,185\nCV 179,186,187\nCV 132,132,133\nWL 8\nEX 81,37\ns_add_u32 s5, s66, 4096\nSv 176,1,16,5,2048\nRa 136,24,34816\n"
"AD 87,87,85\nCV 133,134,135\nMvav 180,152,76\nD1 1,16,46,0\nv_mov_b32 v96, v86\nv_mov_b32 v97, v87\nCV 134,140,141\nMvav 188,156,76\nRa 140,24,35840\n"
"v_permlane32_swap_b32 v86, v96\nCV 135,142,143\nCV 148,148,149\nCV 149,150,151\nAD 85,80,81\nCV 68,80,81\ns_add_u32 s5, s66, 0\nSv 132,1,16,5,1024\nRa 144,26,32768\n"
"v_permlane32_swap_b32 v87, v97\nAD 86,86,96\nCV 150,156,157\nCV 151,158,159\nMvav 196,160,76\ns_cmp_eq_u32 s99, 0\nEX 82,38\nMvav 204,164,76\nRa 148,26,33792\n"
"s_cbranch_scc0 .E324\nv_cmpx_gt_i32 vcc, 19, v0\nv_mov_b32 v15, s97\nv_cmpx_gt_i32 vcc, 18, v0\nv_mov_b32 v14, s97\nv_cmpx_gt_i32 vcc, 17, v0\nv_mov_b32 v13, s97\n"
"v_cmpx_gt_i32 vcc, 16, v0\nv_mov_b32 v12, s97\nv_cmpx_gt_i32 vcc, 3, v0\nv_mov_b32 v7, s97\nv_cmpx_gt_i32 vcc, 2, v0\nv_mov_b32 v6, s97\nv_cmpx_gt_i32 vcc, 1, v0\n"
"v_mov_b32 v5, s97\nv_cmpx_gt_i32 vcc, 0, v0\nv_mov_b32 v4, s97\ns_mov_b64 exec, -1\ns_nop 2\n.E324:\nWL 8\nEX 83,39\ns_add_u32 s5, s66, 0\nSv 148,1,16,5,3072\nRa 152,26,34816\n"
"AD 85,85,82\nAD 87,87,97\nCV 192,192,193\nMvav 212,168,76\nD1 1,16,46,1024\nv_mov_b32 v96, v86\nCV 193,194,195\nMvav 220,172,76\nRa 156,26,35840\nv_mov_b32 v97, v87\n"
"CV 194,200,201\nCV 195,202,203\nAD 85,85,83\nCV 69,82,83\nv_permlane16_swap_b32 v86, v96\ns_add_u32 s5, s66, 16384\nSv 192,1,16,5,0\nRa 160,24,40960\n"
"v_permlane16_swap_b32 v87, v97\nCV 164,164,165\nCV 165,166,167\nMvav 228,176,76\ns_xor_b32 s89, s89, 0x10000\nEX 80,40\nCV 166,172,173\nMvav 236,180,76\nRa 164,24,41984\n"
"AD 86,86,96\nAD 87,87,97\nCV 167,174,175\nWL 6\nEX 81,41\ns_add_u32 s5, s66, 4096\nSv 164,1,16,5,1024\nRa 168,24,43008\nAD 84,84,80\nCV 208,208,209\nCV 209,210,211\n"
"Mvav 244,184,76\nD1 1,16,46,2048\nv_cmp_gt_u32 vcc, 0x100, v1\nv_mov_b32 v94, s15\nv_mov_b32 v95, s15\nv_cndmask_b32 v92, v89, v88, vcc\nv_cndmask_b32 v93, v87, v86, vcc\n"
"Mvav 252,188,76\nRa 172,24,44032\nv_mov_b32 v86, 0\nCV 210,216,217\nCV 211,218,219\nAD 84,84,81\nCV 66,80,81\ns_add_u32 s5, s66, 16384\nSv 208,1,16,5,2048\nCvaa 48,128,192,4\n"
"Ra 176,26,40960\nv_mov_b32 v87, 0\nv_mov_b32 v88, 0\nCV 180,180,181\nCV 181,182,183\nMvaa 48,132,200\ns_cmp_eq_u32 s99, 0\nEX 82,42\nMvaa 48,136,208\nRa 180,26,41984\n"
"s_cselect_b32 s86, s94, s86\nCV 182,188,189\nCV 183,190,191\nMvaa 48,140,216\nWL 6\nEX 83,43\ns_add_u32 s5, s66, 4096\nSv 180,1,16,5,3072\nCvaa 52,140,220,8\nRa 184,26,43008\n"
"AD 84,84,82\nv_mov_b32 v89, 0\nMvaa 52,136,212\nD1 1,16,46,3072\ns_add_u32 m0, s89, 0\nCV 224,224,225\nMvaa 52,132,204\nRa 188,26,44032\nCV 225,226,227\nCV 226,232,233\n"
"CV 227,234,235\nMvaa 52,128,196\nAD 84,84,83\nCV 67,82,83\ns_add_u32 s5, s66, 20480\nSv 224,1,16,5,0\nMvaa 52,144,228\nRa 128,2,49152\nCV 196,196,197\nCV 197,198,199\n"
"CV 198,204,205\nMvaa 52,148,236\ns_add_u32 s46, s86, s88\nEX 80,44\nMvaa 52,152,244\nRa 132,2,50176\nv_xor_b32 v24, 0x10000, v24\nCV 199,206,207\nMvaa 52,156,252\nWL 6\nEX 81,45\n"
"s_add_u32 s5, s66, 16384\nSv 196,1,16,5,1024\nMvaa 48,156,248\nRa 136,2,51200\nAD 85,85,80\nCV 240,240,241\nMvaa 48,152,240\nD1 1,16,46,0\nCV 241,242,243\nMvaa 48,148,232\n"
"Ra 140,2,52224\nCV 242,248,249\nCV 243,250,251\nMvaa 48,144,224\nAD 85,85,81\nCV 70,80,81\ns_add_u32 s5, s66, 20480\nSv 240,1,16,5,2048\nCvaa 56,160,192,12\nRa 144,2,53248\n"
"CV 212,212,213\nCV 213,214,215\nMvaa 56,164,200\nEX 82,46\nCV 214,220,221\nMvaa 56,168,208\nRa 148,2,54272\nv_xor_b32 v26, 0x10000, v26\nCV 215,222,223\nMvaa 56,172,216\nWL 6\n"
"EX 83,47\ns_add_u32 s5, s66, 16384\nSv 212,1,16,5,3072\nCvaa 60,172,220,16\nRa 152,2,55296\nAD 85,85,82\nCV 228,228,229\nMvaa 60,168,212\nD1 1,16,46,1024\nCV 229,230,231\n"
"Mvaa 60,164,204\nRa 156,2,56320\nCV 230,236,237\nCV 231,238,239\nCV 244,244,245\nMvaa 60,160,196\nAD 85,85,83\nCV 71,82,83\ns_add_u32 s5, s66, 20480\nSv 228,1,16,5,1024\n"
"Mvaa 60,176,228\nRa 160,2,57344\nv_max_f32 v90, v84, v85\nCV 245,246,247\nCV 246,252,253\nMvaa 60,180,236\ns_add_u32 s88, s88, 32768\nEX 80,48\nCV 247,254,255\nMvaa 60,184,244\n"
"Ra 164,2,58368\nv_cmp_lt_f32 vcc, s63, v90\nMvaa 60,188,252\nWL 8\nEX 81,49\nMvaa 56,188,248\nRa 168,2,59392\ns_cbranch_vccnz .T329\n.T330:\nv_mov_b32 v20, v84\n"
"v_mov_b32 v21, v85\nv_mov_b32 v22, v20\nv_mov_b32 v23, v21\ns_add_u32 s5, s66, 20480\nSv 244,1,16,5,3072\nMvaa 56,184,240\nD1 1,16,46,2048\nv_permlane32_swap_b32 v20, v22\n"
"v_permlane32_swap_b32 v21, v23\nAD 20,20,22\nAD 21,21,23\nv_mov_b32 v22, v20\nv_mov_b32 v23, v21\nMvaa 56,180,232\nRa 172,2,60416\nv_permlane16_swap_b32 v20, v22\n"
"v_permlane16_swap_b32 v21, v23\nAD 20,20,22\nAD 21,21,23\nv_min_f32 v90, v20, v21\nv_cmp_gt_f32 vcc, s64, v90\ns_nop 0\ns_cbranch_vccnz .T331\n.T332:\nAD 86,86,84\n"
"Mvaa 56,176,224\nAD 84,80,81\nCV 72,80,81\nZvav 128,128,64\nRa 176,2,61440\nZvav 132,128,68\nEX 82,50\nZvav 140,132,68\nRa 180,2,62464\nZvav 136,132,64\n"
"s_waitcnt vmcnt(27) lgkmcnt(8)\nEX 83,51\nZvav 144,136,64\nRa 184,2,63488\nAD 84,84,82\nZvav 148,136,68\nD1 1,16,46,3072\ns_add_u32 m0, s89, 16384\nZvav 156,140,68\n"
"Ra 188,2,64512\ns_barrier\nZvav 152,140,64\nAD 84,84,83\nCV 73,82,83\nZvav 160,144,64\nRa 128,24,0\nZvav 164,144,68\ns_add_u32 s46, s87, s88\nEX 80,52\nZvav 172,148,68\n"
"Ra 132,24,1024\nv_xor_b32 v2, 0x10000, v2\nZvav 168,148,64\nWL 8\nEX 81,53\nZvav 176,152,64\nRa 136,24,2048\nAD 87,87,85\nZvav 180,152,68\nD1 1,16,46,0\nZvav 188,156,68\n"
"Ra 140,24,3072\nZvav 184,156,64\nAD 85,80,81\nCV 76,80,81\nZvav 192,160,64\nRa 144,26,0\nZvav 196,160,68\ns_cmp_lt_u32 s99, s92\nEX 82,54\nZvav 204,164,68\nRa 148,26,1024\n"
"s_cbranch_scc0 .E325\nv_cmpx_gt_i32 vcc, 19, v3\nv_mov_b32 v19, s97\nv_cmpx_gt_i32 vcc, 18, v3\nv_mov_b32 v18, s97\nv_cmpx_gt_i32 vcc, 17, v3\nv_mov_b32 v17, s97\n"
"v_cmpx_gt_i32 vcc, 16, v3\nv_mov_b32 v16, s97\nv_cmpx_gt_i32 vcc, 3, v3\nv_mov_b32 v11, s97\nv_cmpx_gt_i32 vcc, 2, v3\nv_mov_b32 v10, s97\nv_cmpx_gt_i32 vcc, 1, v3\n"
"v_mov_b32 v9, s97\nv_cmpx_gt_i32 vcc, 0, v3\nv_mov_b32 v8, s97\ns_mov_b64 exec, -1\ns_nop 2\n.E325:\ns_nop 0\nZvav 200,164,64\nWL 8\nEX 83,55\nZvav 208,168,64\nRa 152,26,2048\n"
"AD 85,85,82\nZvav 212,168,68\nD1 1,16,46,1024\nZvav 220,172,68\nRa 156,26,3072\nZvav 216,172,64\nAD 85,85,83\nCV 77,82,83\nZvav 224,176,64\nRa 160,24,8192\nZvav 228,176,68\n"
"s_sub_u32 s90, s90, 1\nEX 80,56\nZvav 236,180,68\nRa 164,24,9216\ns_cbranch_scc0 .E323\ns_swappc_b64 s[80:81], s[8:9]\n.E323:\nZvav 232,180,64\nWL 6\nEX 81,57\nZvav 240,184,64\n"
"Ra 168,24,10240\nAD 84,84,80\nZvav 244,184,68\nD1 1,16,46,2048\nZvav 252,188,68\nRa 172,24,11264\nZvav 248,188,64\nAD 84,84,81\n.W576:\ns_cbranch_execz .WB273\nCV 74,80,81\n"
"s_cmp_eq_u32 s13, 0\ns_cbranch_scc0 .E333\nv_mov_b32 v91, 0x40000000\nv_cmp_gt_u32 vcc, 0x200, v1\nWV 5\ns_nop 0\nv_cndmask_b32 v91, v91, v1, vcc\nSv 92,91,16,65,0\n.E333:\n"
"s_sub_u32 s99, s99, 1\ns_cbranch_scc1 .LE275\n.p2align 6\n.A334:\nCvaa 32,128,192,4\nRa 176,26,8192\nMvaa 32,132,200\ns_add_u32 s49, s49, 131072\nEX 82,58\nMvaa 32,136,208\n"
"Ra 180,26,9216\nMvaa 32,140,216\nWL 6\nEX 83,59\nCvaa 36,140,220,8\nRa 184,26,10240\nAD 84,84,82\nMvaa 36,136,212\nD1 1,16,46,3072\ns_add_u32 m0, s89, 32768\nMvaa 36,132,204\n"
"Ra 188,26,11264\nMvaa 36,128,196\nAD 84,84,83\nCV 75,82,83\nMvaa 36,144,228\nRa 128,2,16384\nMvaa 36,148,236\ns_add_u32 s46, s86, s88\nEX 80,60\nMvaa 36,152,244\nRa 132,2,17408\n"
"Mvaa 36,156,252\nWL 6\nEX 81,61\nMvaa 32,156,248\nRa 136,2,18432\nAD 85,85,80\nMvaa 32,152,240\nD1 1,16,46,0\nMvaa 32,148,232\nRa 140,2,19456\nMvaa 32,144,224\nAD 85,85,81\n"
"CV 78,80,81\nCvaa 40,160,192,12\nRa 144,2,20480\nMvaa 40,164,200\nSv 112,1,16,49,0\nEX 82,62\nMvaa 40,168,208\nRa 148,2,21504\nMvaa 40,172,216\nWL 6\nEX 83,63\nCvaa 44,172,220,16\n"
"Ra 152,2,22528\nAD 85,85,82\nMvaa 44,168,212\nD1 1,16,46,1024\nMvaa 44,164,204\nRa 156,2,23552\nMvaa 44,160,196\nAD 85,85,83\nCV 79,82,83\nMvaa 44,176,228\nRa 160,2,24576\n"
"v_max_f32 v90, v84, v85\nMvaa 44,180,236\ns_add_u32 s88, s88, 32768\nEX 80,32\nMvaa 44,184,244\nRa 164,2,25600\nv_cmp_lt_f32 vcc, s63, v90\nMvaa 44,188,252\nWL 8\nEX 81,33\n"
"Mvaa 40,188,248\nRa 168,2,26624\ns_cbranch_vccnz .T344\n.T345:\nMvaa 40,184,240\nD1 1,16,46,2048\nMvaa 40,180,232\nRa 172,2,27648\nAD 86,86,84\nMvaa 40,176,224\nAD 84,80,81\n"
"CV 64,80,81\nMvav 128,128,72\nRa 176,2,28672\nMvav 132,128,76\nEX 82,34\nMvav 140,132,76\nRa 180,2,29696\nMvav 136,132,72\ns_waitcnt vmcnt(13) lgkmcnt(8)\nEX 83,35\n"
"Mvav 144,136,72\nRa 184,2,30720\nAD 84,84,82\nMvav 148,136,76\nD1 1,16,46,3072\ns_add_u32 m0, s89, 49152\nMvav 156,140,76\nRa 188,2,31744\ns_barrier\nMvav 152,140,72\nAD 84,84,83\n"
"CV 65,82,83\nMvav 160,144,72\nRa 128,24,32768\nMvav 164,144,76\ns_add_u32 s46, s87, s88\nEX 80,36\nMvav 172,148,76\nRa 132,24,33792\nMvav 168,148,72\nWL 8\nEX 81,37\n"
"Mvav 176,152,72\nRa 136,24,34816\nAD 87,87,85\nMvav 180,152,76\nD1 1,16,46,0\nMvav 188,156,76\nRa 140,24,35840\nMvav 184,156,72\nAD 85,80,81\nCV 68,80,81\nMvav 192,160,72\n"
"Ra 144,26,32768\nMvav 196,160,76\ns_cmp_eq_u32 s99, 0\nEX 82,38\nMvav 204,164,76\nRa 148,26,33792\ns_cbranch_scc1 .E336\n.E337:\nMvav 200,164,72\nWL 8\nEX 83,39\nMvav 208,168,72\n"
"Ra 152,26,34816\nAD 85,85,82\nMvav 212,168,76\nD1 1,16,46,1024\nMvav 220,172,76\nRa 156,26,35840\nMvav 216,172,72\nAD 85,85,83\nCV 69,82,83\nMvav 224,176,72\nRa 160,24,40960\n"
"Mvav 228,176,76\ns_xor_b32 s89, s89, 0x10000\nEX 80,40\nMvav 236,180,76\nRa 164,24,41984\nMvav 232,180,72\nWL 6\nEX 81,41\nMvav 240,184,72\nRa 168,24,43008\nAD 84,84,80\n"
"Mvav 244,184,76\nD1 1,16,46,2048\nMvav 252,188,76\nRa 172,24,44032\nMvav 248,188,72\nAD 84,84,81\nCV 66,80,81\nCvaa 48,128,192,4\nRa 176,26,40960\nMvaa 48,132,200\n"
"s_cmp_eq_u32 s99, 0\nEX 82,42\nMvaa 48,136,208\nRa 180,26,41984\ns_cselect_b32 s86, s94, s86\nMvaa 48,140,216\nWL 6\nEX 83,43\nCvaa 52,140,220,8\nRa 184,26,43008\nAD 84,84,82\n"
"Mvaa 52,136,212\nD1 1,16,46,3072\ns_add_u32 m0, s89, 0\nMvaa 52,132,204\nRa 188,26,44032\nMvaa 52,128,196\nAD 84,84,83\nCV 67,82,83\nMvaa 52,144,228\nRa 128,2,49152\n"
"Mvaa 52,148,236\ns_add_u32 s46, s86, s88\nEX 80,44\nMvaa 52,152,244\nRa 132,2,50176\nv_xor_b32 v24, 0x10000, v24\nMvaa 52,156,252\nWL 6\nEX 81,45\nMvaa 48,156,248\nRa 136,2,51200\n"
"AD 85,85,80\nMvaa 48,152,240\nD1 1,16,46,0\nMvaa 48,148,232\nRa 140,2,52224\nMvaa 48,144,224\nAD 85,85,81\nCV 70,80,81\nCvaa 56,160,192,12\nRa 144,2,53248\nMvaa 56,164,200\n"
"Sv 112,1,16,49,1024\nEX 82,46\nMvaa 56,168,208\nRa 148,2,54272\nv_xor_b32 v26, 0x10000, v26\nMvaa 56,172,216\nWL 6\nEX 83,47\nCvaa 60,172,220,16\nRa 152,2,55296\nAD 85,85,82\n"
"Mvaa 60,168,212\nD1 1,16,46,1024\nMvaa 60,164,204\nRa 156,2,56320\nMvaa 60,160,196\nAD 85,85,83\nCV 71,82,83\nMvaa 60,176,228\nRa 160,2,57344\nv_max_f32 v90, v84, v85\n"
"Mvaa 60,180,236\ns_add_u32 s88, s88, 32768\nEX 80,48\nMvaa 60,184,244\nRa 164,2,58368\nv_cmp_lt_f32 vcc, s63, v90\nMvaa 60,188,252\nWL 8\nEX 81,49\nMvaa 56,188,248\n"
"Ra 168,2,59392\ns_cbranch_vccnz .T346\n.T347:\nMvaa 56,184,240\nD1 1,16,46,2048\nMvaa 56,180,232\nRa 172,2,60416\nAD 86,86,84\nMvaa 56,176,224\nAD 84,80,81\nCV 72,80,81\n"
"Mvav 128,128,64\nRa 176,2,61440\nMvav 132,128,68\ns_cmp_eq_u32 s99, s98\nEX 82,50\nMvav 140,132,68\nRa 180,2,62464\ns_cbranch_scc1 .E342\n.E343:\nMvav 136,132,64\n"
"s_waitcnt vmcnt(13) lgkmcnt(8)\nEX 83,51\nMvav 144,136,64\nRa 184,2,63488\nAD 84,84,82\nMvav 148,136,68\nD1 1,16,46,3072\ns_add_u32 m0, s89, 16384\nMvav 156,140,68\n"
"Ra 188,2,64512\ns_barrier\nMvav 152,140,64\nAD 84,84,83\nCV 73,82,83\nMvav 160,144,64\nRa 128,24,0\nMvav 164,144,68\ns_add_u32 s46, s87, s88\nEX 80,52\nMvav 172,148,68\n"
"Ra 132,24,1024\nv_xor_b32 v2, 0x10000, v2\nMvav 168,148,64\nWL 8\nEX 81,53\nMvav 176,152,64\nRa 136,24,2048\nAD 87,87,85\nMvav 180,152,68\nD1 1,16,46,0\nMvav 188,156,68\n"
"Ra 140,24,3072\nMvav 184,156,64\nAD 85,80,81\nCV 76,80,81\nMvav 192,160,64\nRa 144,26,0\nMvav 196,160,68\ns_cmp_lt_u32 s99, s92\nEX 82,54\nMvav 204,164,68\nRa 148,26,1024\n"
"s_cbranch_scc1 .E338\n.E339:\nMvav 200,164,64\nWL 8\nEX 83,55\nMvav 208,168,64\nRa 152,26,2048\nAD 85,85,82\nMvav 212,168,68\nD1 1,16,46,1024\nMvav 220,172,68\nRa 156,26,3072\n"
"Mvav 216,172,64\nAD 85,85,83\nCV 77,82,83\nMvav 224,176,64\nRa 160,24,8192\nMvav 228,176,68\ns_sub_u32 s90, s90, 1\nEX 80,56\nMvav 236,180,68\nRa 164,24,9216\n"
"s_cbranch_scc1 .E340\n.E341:\nMvav 232,180,64\nWL 6\nEX 81,57\nMvav 240,184,64\nRa 168,24,10240\nAD 84,84,80\nMvav 244,184,68\nD1 1,16,46,2048\ns_sub_u32 s99, s99, 1\n"
"Mvav 252,188,68\nRa 172,24,11264\nMvav 248,188,64\nAD 84,84,81\nCV 74,80,81\ns_cbranch_scc0 .A334\ns_branch .A348\n.E336:\nv_cmpx_gt_i32 vcc, 19, v0\nv_mov_b32 v15, s97\n"
"v_cmpx_gt_i32 vcc, 18, v0\nv_mov_b32 v14, s97\nv_cmpx_gt_i32 vcc, 17, v0\nv_mov_b32 v13, s97\nv_cmpx_gt_i32 vcc, 16, v0\nv_mov_b32 v12, s97\nv_cmpx_gt_i32 vcc, 3, v0\n"
"v_mov_b32 v7, s97\nv_cmpx_gt_i32 vcc, 2, v0\nv_mov_b32 v6, s97\nv_cmpx_gt_i32 vcc, 1, v0\nv_mov_b32 v5, s97\nv_cmpx_gt_i32 vcc, 0, v0\nv_mov_b32 v4, s97\ns_mov_b64 exec, -1\n"
"s_nop 2\ns_branch .E337\n.E338:\ns_cmp_eq_u32 s99, 0\ns_cbranch_scc1 .K335\nv_cmpx_gt_i32 vcc, 19, v3\nv_mov_b32 v15, s97\nv_cmpx_gt_i32 vcc, 18, v3\nv_mov_b32 v14, s97\n"
"v_cmpx_gt_i32 vcc, 17, v3\nv_mov_b32 v13, s97\nv_cmpx_gt_i32 vcc, 16, v3\nv_mov_b32 v12, s97\nv_cmpx_gt_i32 vcc, 3, v3\nv_mov_b32 v7, s97\nv_cmpx_gt_i32 vcc, 2, v3\n"
"v_mov_b32 v6, s97\nv_cmpx_gt_i32 vcc, 1, v3\nv_mov_b32 v5, s97\nv_cmpx_gt_i32 vcc, 0, v3\nv_mov_b32 v4, s97\ns_mov_b64 exec, -1\ns_nop 2\ns_branch .E339\n.K335:\n"
"v_cmpx_gt_i32 vcc, 19, v3\nv_mov_b32 v19, s97\nv_cmpx_gt_i32 vcc, 18, v3\nv_mov_b32 v18, s97\nv_cmpx_gt_i32 vcc, 17, v3\nv_mov_b32 v17, s97\nv_cmpx_gt_i32 vcc, 16, v3\n"
"v_mov_b32 v16, s97\nv_cmpx_gt_i32 vcc, 3, v3\nv_mov_b32 v11, s97\nv_cmpx_gt_i32 vcc, 2, v3\nv_mov_b32 v10, s97\nv_cmpx_gt_i32 vcc, 1, v3\nv_mov_b32 v9, s97\n"
"v_cmpx_gt_i32 vcc, 0, v3\nv_mov_b32 v8, s97\ns_mov_b64 exec, -1\ns_nop 2\ns_branch .E339\n.E340:\ns_swappc_b64 s[80:81], s[8:9]\ns_branch .E341\n.E342:\nv_mov_b32 v91, 0x40000000\n"
"v_cmp_gt_u32 vcc, 0x200, v1\nWV 19\ns_nop 0\nv_cndmask_b32 v91, v91, v1, vcc\nSv 92,91,16,65,0\ns_branch .E343\n.A348:\n.LE275:\nRa 176,26,8192\nEX 82,58\nRa 180,26,9216\nWL 6\n"
"EX 83,59\nCvaa 36,140,220,8\nRa 184,26,10240\nAD 84,84,82\nMvaa 36,136,212\nD1 1,16,46,3072\ns_add_u32 m0, s89, 32768\nMvaa 36,132,204\nRa 188,26,11264\nMvaa 36,128,196\n"
"AD 84,84,83\nCV 75,82,83\nMvaa 36,144,228\nRa 128,2,16384\nMvaa 36,148,236\ns_add_u32 s46, s86, s88\nEX 80,60\nMvaa 36,152,244\nRa 132,2,17408\nMvaa 36,156,252\nEX 81,61\n"
"Ra 136,2,18432\nAD 85,85,80\nD1 1,16,46,0\nRa 140,2,19456\nAD 85,85,81\nCV 78,80,81\nRa 144,2,20480\ns_mov_b32 s87, s95\nEX 82,62\nRa 148,2,21504\nWL 6\nEX 83,63\n"
"Cvaa 44,172,220,16\nRa 152,2,22528\nAD 85,85,82\nMvaa 44,168,212\nD1 1,16,46,1024\nMvaa 44,164,204\nRa 156,2,23552\nMvaa 44,160,196\nAD 85,85,83\nCV 79,82,83\nMvaa 44,176,228\n"
"Ra 160,2,24576\nv_max_f32 v90, v84, v85\nMvaa 44,180,236\ns_add_u32 s88, s88, 32768\nMvaa 44,184,244\nRa 164,2,25600\nv_cmp_lt_f32 vcc, s63, v90\nMvaa 44,188,252\nWL 8\n"
"Ra 168,2,26624\ns_cbranch_vccnz .T350\n.T351:\nD1 1,16,46,2048\nRa 172,2,27648\nAD 86,86,84\nMvav 128,128,72\nRa 176,2,28672\nMvav 132,128,76\nMvav 140,132,76\nRa 180,2,29696\n"
"Mvav 136,132,72\ns_waitcnt vmcnt(12) lgkmcnt(8)\nMvav 144,136,72\nRa 184,2,30720\nMvav 148,136,76\nD1 1,16,46,3072\ns_add_u32 m0, s89, 49152\nMvav 156,140,76\nRa 188,2,31744\n"
"s_barrier\nMvav 152,140,72\nMvav 160,144,72\nRa 128,24,32768\nMvav 164,144,76\ns_add_u32 s46, s87, s88\nEX 80,36\nMvav 172,148,76\nRa 132,24,33792\nMvav 168,148,72\nWL 8\n"
"EX 81,37\nMvav 176,152,72\nRa 136,24,34816\nAD 87,87,85\nMvav 180,152,76\nD1 1,16,46,0\nMvav 188,156,76\nRa 140,24,35840\nMvav 184,156,72\nAD 85,80,81\nCV 68,80,81\n"
"Mvav 192,160,72\nRa 144,26,32768\nMvav 196,160,76\ns_nop 0\nEX 82,38\nMvav 204,164,76\nRa 148,26,33792\nv_cmpx_gt_i32 vcc, 19, v0\nv_mov_b32 v19, s97\nv_cmpx_gt_i32 vcc, 18, v0\n"
"v_mov_b32 v18, s97\nv_cmpx_gt_i32 vcc, 17, v0\nv_mov_b32 v17, s97\nv_cmpx_gt_i32 vcc, 16, v0\nv_mov_b32 v16, s97\nv_cmpx_gt_i32 vcc, 3, v0\nv_mov_b32 v11, s97\n"
"v_cmpx_gt_i32 vcc, 2, v0\nv_mov_b32 v10, s97\nv_cmpx_gt_i32 vcc, 1, v0\nv_mov_b32 v9, s97\nv_cmpx_gt_i32 vcc, 0, v0\nv_mov_b32 v8, s97\ns_mov_b64 exec, -1\ns_nop 2\ns_nop 0\n"
"Mvav 200,164,72\nWL 8\nEX 83,39\nMvav 208,168,72\nRa 152,26,34816\nAD 85,85,82\nMvav 212,168,76\nD1 1,16,46,1024\nMvav 220,172,76\nRa 156,26,35840\nMvav 216,172,72\nAD 85,85,83\n"
"CV 69,82,83\nMvav 224,176,72\nRa 160,24,40960\nMvav 228,176,76\ns_xor_b32 s89, s89, 0x10000\nMvav 236,180,76\nRa 164,24,41984\nMvav 232,180,72\nWL 10\nMvav 240,184,72\n"
"Ra 168,24,43008\nMvav 244,184,76\nD1 1,16,46,2048\nMvav 252,188,76\nRa 172,24,44032\nMvav 248,188,72\nRa 176,26,40960\nRa 180,26,41984\nWL 6\nCvaa 52,140,220,8\nRa 184,26,43008\n"
"Mvaa 52,136,212\nD1 1,16,46,3072\ns_add_u32 m0, s89, 0\nMvaa 52,132,204\nRa 188,26,44032\nMvaa 52,128,196\nMvaa 52,144,228\nRa 128,2,49152\nMvaa 52,148,236\n"
"s_add_u32 s46, s86, s88\nEX 80,44\nMvaa 52,152,244\nRa 132,2,50176\nv_xor_b32 v24, 0x10000, v24\nMvaa 52,156,252\nEX 81,45\nRa 136,2,51200\nAD 85,85,80\nD1 1,16,46,0\n"
"Ra 140,2,52224\nAD 85,85,81\nCV 70,80,81\nRa 144,2,53248\nEX 82,46\nRa 148,2,54272\nv_xor_b32 v26, 0x10000, v26\nWL 6\nEX 83,47\nCvaa 60,172,220,16\nRa 152,2,55296\nAD 85,85,82\n"
"Mvaa 60,168,212\nD1 1,16,46,1024\nMvaa 60,164,204\nRa 156,2,56320\nMvaa 60,160,196\nAD 85,85,83\nCV 71,82,83\nMvaa 60,176,228\nRa 160,2,57344\nMvaa 60,180,236\n"
"s_add_u32 s88, s88, 32768\nMvaa 60,184,244\nRa 164,2,58368\nv_cmp_lt_f32 vcc, s63, v85\nMvaa 60,188,252\nWL 8\nRa 168,2,59392\ns_cbranch_vccnz .T352\n.T353:\nD1 1,16,46,2048\n"
"Ra 172,2,60416\nRa 176,2,61440\nMvav 132,128,68\nMvav 140,132,68\nRa 180,2,62464\ns_waitcnt vmcnt(11) lgkmcnt(8)\nRa 184,2,63488\nMvav 148,136,68\nD1 1,16,46,3072\n.W577:\n"
"s_cbranch_execz .WB273\ns_add_u32 m0, s89, 16384\nMvav 156,140,68\nRa 188,2,64512\ns_barrier\nRa 128,24,0\nMvav 164,144,68\ns_add_u32 s46, s87, s88\nEX 80,52\nMvav 172,148,68\n"
"Ra 132,24,1024\nv_xor_b32 v2, 0x10000, v2\nWL 8\nEX 81,53\nRa 136,24,2048\nAD 87,87,85\nMvav 180,152,68\nD1 1,16,46,0\nMvav 188,156,68\nRa 140,24,3072\nAD 85,80,81\nCV 76,80,81\n"
"Ra 144,26,0\nMvav 196,160,68\nEX 82,54\nMvav 204,164,68\nRa 148,26,1024\nWL 8\nEX 83,55\nRa 152,26,2048\nAD 85,85,82\nMvav 212,168,68\nD1 1,16,46,1024\nMvav 220,172,68\n"
"Ra 156,26,3072\nAD 85,85,83\nCV 77,82,83\nRa 160,24,8192\nMvav 228,176,68\nMvav 236,180,68\nRa 164,24,9216\nWL 10\nRa 168,24,10240\nMvav 244,184,68\nD1 1,16,46,2048\n"
"Mvav 252,188,68\nRa 172,24,11264\nD1 1,16,46,3072\ns_add_u32 m0, s89, 32768\ns_add_u32 s46, s86, s88\nRa 128,2,16384\nv_xor_b32 v3, 0x10000, v2\nv_add_u32 v0, 0x20000, v1\n"
"EX 80,60\nRa 132,2,17408\ns_sub_u32 s94, s49, 1703936\nEX 81,61\nRa 136,2,18432\nAD 85,85,80\ns_xor_b32 s92, s94, 0x202000\nD1 1,16,46,0\ns_lshl_b32 s89, s11, 12\nRa 140,2,19456\n"
"s_lshl1_add_u32 s2, s12, 1\nAD 85,85,81\nCV 78,80,81\nRa 144,2,20480\ns_lshl3_add_u32 s2, s2, s13\nEX 82,62\nRa 148,2,21504\ns_lshl4_add_u32 s2, s2, 0xc048200\nEX 83,63\n"
"Ra 152,2,22528\nAD 85,85,82\nD1 1,16,46,1024\nRa 156,2,23552\ns_and_b32 s3, s72, 0x2000\nAD 85,85,83\nCV 79,82,83\nRa 160,2,24576\ns_cmp_eq_u32 s11, 1\ns_cselect_b32 s54, s2, s54\n"
"s_cselect_b32 s3, s3, 0\nv_cmp_lt_f32 vcc, s63, v85\nRa 164,2,25600\ns_cbranch_vccnz .T354\n.T355:\nAD 87,87,85\nWL 8\nv_mov_b32 v84, v86\nv_mov_b32 v85, v87\nRa 168,2,26624\n"
"v_permlane32_swap_b32 v86, v84\nv_permlane32_swap_b32 v87, v85\nD1 1,16,46,2048\nAD 86,86,84\nRa 172,2,27648\nAD 87,87,85\nv_mov_b32 v84, v86\nv_mov_b32 v85, v87\nRa 176,2,28672\n"
"v_permlane16_swap_b32 v86, v84\nv_permlane16_swap_b32 v87, v85\nMvav 132,128,76\nAD 86,86,84\nAD 87,87,85\nMvav 140,132,76\nRa 180,2,29696\nv_rcp_f32 v86, v86\nWL 8\n"
"v_rcp_f32 v87, v87\nRa 184,2,30720\nv_xor_b32 v25, s3, v25\nMvav 148,136,76\nD1 1,16,46,3072\nMvav 156,140,76\nRa 188,2,31744\nMU 128,128,86\nMU 129,129,86\nCV 96,128,129\n"
"MU 130,130,86\nMU 131,131,86\nRa 128,2,0\nCV 97,130,131\nMU 132,132,87\nMvav 164,144,76\nMU 133,133,87\nCV 100,132,133\nMvav 172,148,76\nRa 132,2,1024\nMU 134,134,87\n"
"MU 135,135,87\nWL 8\ns_add_u32 m0, s89, 131072\ns_add_u32 s46, s96, 1212416\nRa 136,2,2048\nCV 101,134,135\nMU 136,136,86\nMvav 180,152,76\nD1 1,16,46,0\nMU 137,137,86\n"
"Mvav 188,156,76\nRa 140,2,3072\nCV 98,136,137\nMU 138,138,86\nMU 139,139,86\nCV 99,138,139\nMU 140,140,87\nRa 144,2,4096\nMU 141,141,87\nCV 102,140,141\nMvav 196,160,76\n"
"MU 142,142,87\nMU 143,143,87\nMvav 204,164,76\nRa 148,2,5120\nCV 103,142,143\nMU 144,144,86\nWL 8\nMU 145,145,86\nCV 104,144,145\nRa 152,2,6144\nMU 146,146,86\nMU 147,147,86\n"
"Mvav 212,168,76\nD1 1,16,46,1024\nCV 105,146,147\nMvav 220,172,76\nRa 156,2,7168\nMU 148,148,87\nMU 149,149,87\nCV 108,148,149\nMU 150,150,87\nMU 151,151,87\nRa 160,2,8192\n"
"CV 109,150,151\nMU 152,152,86\nMvav 228,176,76\nMU 153,153,86\nCV 106,152,153\nMvav 236,180,76\nRa 164,2,9216\nMU 154,154,86\nMU 155,155,86\nWL 8\nCV 107,154,155\nMU 156,156,87\n"
"Ra 168,2,10240\nMU 157,157,87\nCV 110,156,157\nMvav 244,184,76\nD1 1,16,46,2048\nMU 158,158,87\nMvav 252,188,76\nRa 172,2,11264\nMU 159,159,87\nCV 111,158,159\nMU 160,160,86\n"
"MU 161,161,86\nCV 112,160,161\nZvav 128,128,96\nRa 176,2,12288\nMU 162,162,86\nMU 163,163,86\nMvav 128,132,104\nCV 113,162,163\nMU 164,164,87\nZvav 132,132,108\nRa 180,2,13312\n"
"MU 165,165,87\nCV 116,164,165\nMvav 132,128,100\ns_waitcnt vmcnt(15) lgkmcnt(8)\nMU 166,166,87\nMU 167,167,87\nZvav 140,136,100\nRa 184,2,14336\nCV 117,166,167\nMU 168,168,86\n"
"Mvav 140,140,108\nD1 1,16,46,3072\nMU 169,169,86\nCV 114,168,169\nZvav 136,140,104\nRa 188,2,15360\ns_barrier\nMU 170,170,86\nMvav 136,136,96\nMU 171,171,86\nCV 115,170,171\n"
"MU 172,172,87\nZvav 144,144,96\nRa 128,2,32768\nMU 173,173,87\nCV 118,172,173\nMvav 144,148,104\nMU 174,174,87\nMU 175,175,87\n"
"buffer_store_dword v25, off, s[16:19], s54 offset:512 sc0 sc1\nZvav 148,148,108\nRa 132,2,33792\nCV 119,174,175\nMU 176,176,86\nMvav 148,144,100\nWL 8\ns_add_u32 m0, s89, 49152\n"
"s_add_u32 s46, s96, 1179648\nZvav 156,152,100\nRa 136,2,34816\nMU 177,177,86\nCV 120,176,177\nMvav 156,156,108\nD1 1,16,46,0\nMU 178,178,86\nMU 179,179,86\nZvav 152,156,104\n"
"Ra 140,2,35840\nCV 121,178,179\nMU 180,180,87\nMvav 152,152,96\nMU 181,181,87\nCV 124,180,181\nMU 182,182,87\nZvav 64,160,96\nRa 144,2,36864\nMU 183,183,87\nCV 125,182,183\n"
"Mvav 64,164,104\nMU 184,184,86\nMU 185,185,86\nZvav 68,164,108\nRa 148,2,37888\nCV 122,184,185\nMU 186,186,86\nMvav 68,160,100\nWL 8\nMU 187,187,86\nCV 123,186,187\n"
"Zvav 76,168,100\nRa 152,2,38912\nMU 188,188,87\nMU 189,189,87\nMvav 76,172,108\nD1 1,16,46,1024\nCV 126,188,189\nMU 190,190,87\nZvav 72,172,104\nRa 156,2,39936\nMU 191,191,87\n"
"CV 127,190,191\nMvav 72,168,96\nMU 192,192,86\nMU 193,193,86\nCV 32,192,193\nMvav 128,176,112\nRa 160,2,40960\nMU 194,194,86\nMU 195,195,86\nMvav 128,180,120\nCV 33,194,195\n"
"MU 196,196,87\nMU 197,197,87\nMvav 132,180,124\nRa 164,2,41984\nCV 36,196,197\nMU 198,198,87\nMvav 132,176,116\nWL 8\nMU 199,199,87\nMvav 140,184,116\nRa 168,2,43008\n"
"CV 37,198,199\nMU 200,200,86\nMvav 140,188,124\nD1 1,16,46,2048\nMU 201,201,86\nCV 34,200,201\nMvav 136,188,120\nRa 172,2,44032\nMU 202,202,86\nMU 203,203,86\nMvav 136,184,112\n"
"CV 35,202,203\nMU 204,204,87\nMU 205,205,87\nMvav 144,128,112\nRa 176,2,45056\nCV 38,204,205\nMU 206,206,87\nMvav 144,132,120\nMU 207,207,87\nCV 39,206,207\nMvav 148,132,124\n"
"Ra 180,2,46080\nMU 208,208,86\nMU 209,209,86\nMvav 148,128,116\nWL 8\nCV 40,208,209\nMU 210,210,86\nMvav 156,136,116\nRa 184,2,47104\nMU 211,211,86\nCV 41,210,211\n"
"Mvav 156,140,124\nD1 1,16,46,3072\nMU 212,212,87\nMU 213,213,87\nMvav 152,140,120\nRa 188,2,48128\nCV 44,212,213\nMvav 152,136,112\nMU 214,214,87\nMU 215,215,87\nCV 45,214,215\n"
"Mvav 64,144,112\nRa 128,2,49152\nMU 216,216,86\nMU 217,217,86\nMvav 64,148,120\nCV 42,216,217\nMU 218,218,86\nMU 219,219,86\nMvav 68,148,124\nRa 132,2,50176\nCV 43,218,219\n"
"MU 220,220,87\nMvav 68,144,116\nWL 8\ns_add_u32 m0, s89, 147456\ns_add_u32 s46, s96, 1245184\nMvav 76,152,116\nRa 136,2,51200\nMU 221,221,87\nCV 46,220,221\nMvav 76,156,124\n"
"D1 1,16,46,0\nMU 222,222,87\nMU 223,223,87\nMvav 72,156,120\nRa 140,2,52224\nCV 47,222,223\nMvav 72,152,112\nMU 224,224,86\nMU 225,225,86\nCV 48,224,225\nMvav 128,160,32\n"
"Ra 144,2,53248\nMU 226,226,86\nMU 227,227,86\nMvav 128,164,40\nCV 49,226,227\nMU 228,228,87\nMvav 132,164,44\nRa 148,2,54272\nMU 229,229,87\nCV 52,228,229\nMvav 132,160,36\nWL 8\n"
"MU 230,230,87\nMU 231,231,87\nMvav 140,168,36\nRa 152,2,55296\nCV 53,230,231\nMU 232,232,86\nMvav 140,172,44\nD1 1,16,46,1024\nMU 233,233,86\nCV 50,232,233\nMvav 136,172,40\n"
"Ra 156,2,56320\nMU 234,234,86\nMvav 136,168,32\nMU 235,235,86\nCV 51,234,235\nMU 236,236,87\nMvav 144,176,32\nRa 160,2,57344\nMU 237,237,87\nCV 54,236,237\nMvav 144,180,40\n"
"MU 238,238,87\nMU 239,239,87\nCV 55,238,239\nMvav 148,180,44\nRa 164,2,58368\nMU 240,240,86\nMU 241,241,86\nMvav 148,176,36\nWL 8\nCV 56,240,241\nMvav 156,184,36\nRa 168,2,59392\n"
"MU 242,242,86\nMU 243,243,86\nMvav 156,188,44\nD1 1,16,46,2048\nCV 57,242,243\nMU 244,244,87\nMvav 152,188,40\nRa 172,2,60416\nMU 245,245,87\nCV 60,244,245\nMvav 152,184,32\n"
"MU 246,246,87\nMU 247,247,87\nCV 61,246,247\nMvav 64,128,32\nRa 176,2,61440\nMU 248,248,86\nMU 249,249,86\nMvav 64,132,40\nCV 58,248,249\nMU 250,250,86\nMvav 68,132,44\n"
"Ra 180,2,62464\nMU 251,251,86\nCV 59,250,251\nMvav 68,128,36\ns_waitcnt vmcnt(16) lgkmcnt(8)\nMU 252,252,87\nMU 253,253,87\nMvav 76,136,36\nRa 184,2,63488\nCV 62,252,253\n"
"MU 254,254,87\nMvav 76,140,44\nD1 1,16,46,3072\nMU 255,255,87\nCV 63,254,255\nMvav 72,140,40\nRa 188,2,64512\ns_barrier\nMvav 72,136,32\nMvav 128,144,48\nRa 128,3,0\n"
"Mvav 128,148,56\nMvav 132,148,60\nRa 132,3,1024\nMvav 132,144,52\nWL 8\nMvav 140,152,52\nRa 136,3,2048\nMvav 140,156,60\nMvav 136,156,56\nRa 140,3,3072\nMvav 136,152,48\n"
"Mvav 144,160,48\nRa 144,3,4096\nMvav 144,164,56\nMvav 148,164,60\nRa 148,3,5120\nCV 132,132,133\nMvav 148,160,52\nWL 8\nCV 133,134,135\nMvav 156,168,52\nRa 152,3,6144\n"
"CV 134,140,141\nMvav 156,172,60\nCV 135,142,143\ns_add_u32 s5, s94, 0\nMvav 152,172,56\nRa 156,3,7168\nSv 132,1,16,5,1024\nMvav 152,168,48\nCV 128,128,129\nCV 129,130,131\n"
"Mvav 64,176,48\nRa 160,3,8192\nCV 130,136,137\nMvav 64,180,56\nbuffer_load_dword v27, off, s[16:19], s60 offset:512 sc0 sc1\nCV 131,138,139\nMvav 68,180,60\nRa 164,3,9216\n"
"Sv 128,1,16,5,0\nMvav 68,176,52\nWL 6\nCV 148,148,149\nMvav 76,184,52\nRa 168,3,10240\nCV 149,150,151\nMvav 76,188,60\nCV 150,156,157\nCV 151,158,159\nMvav 72,188,56\n"
"Ra 172,3,11264\ns_add_u32 s5, s94, 131072\nMvav 72,184,48\nSv 148,1,16,5,1024\nCV 144,144,145\nZvav 160,128,96\nRa 176,3,12288\nCV 145,146,147\nMvav 160,132,104\nCV 146,152,153\n"
"CV 147,154,155\nMvav 160,136,112\nRa 180,3,13312\nSv 144,1,16,5,0\nMvav 160,140,120\nWL 6\nCV 68,68,69\nZvav 164,140,124\nRa 184,3,14336\nCV 69,70,71\nMvav 164,136,116\n"
"CV 70,76,77\nCV 71,78,79\nMvav 164,132,108\nRa 188,3,15360\ns_add_u32 s5, s94, 262144\nMvav 164,128,100\nSv 68,1,16,5,1024\nCV 64,64,65\nMvav 164,144,36\nRa 128,3,16384\n"
"CV 65,66,67\nMvav 164,148,44\nCV 66,72,73\nCV 67,74,75\nMvav 164,152,52\nRa 132,3,17408\nSv 64,1,16,5,0\nMvav 164,156,60\nWL 6\nMvav 160,156,56\nRa 136,3,18432\nMvav 160,152,48\n"
"Mvav 160,148,40\nRa 140,3,19456\nMvav 160,144,32\nZvav 168,160,96\nRa 144,3,20480\nMvav 168,164,104\nMvav 168,168,112\nRa 148,3,21504\nMvav 168,172,120\nWL 6\nZvav 172,172,124\n"
"Ra 152,3,22528\nMvav 172,168,116\nMvav 172,164,108\nRa 156,3,23552\nMvav 172,160,100\nMvav 172,176,36\nRa 160,3,24576\nMvav 172,180,44\nMvav 172,184,52\nRa 164,3,25600\n"
"Mvav 172,188,60\nWL 6\nMvav 168,188,56\nRa 168,3,26624\nMvav 168,184,48\nMvav 168,180,40\nRa 172,3,27648\nMvav 168,176,32\nZvav 176,128,96\nRa 176,3,28672\nCV 164,164,165\n"
"Mvav 176,132,104\nCV 165,166,167\nCV 166,172,173\nMvav 176,136,112\nRa 180,3,29696\nCV 167,174,175\nMvav 176,140,120\ns_waitcnt vmcnt(11) lgkmcnt(6)\ns_add_u32 s5, s94, 393216\n"
"Zvav 180,140,124\nRa 184,3,30720\n.W578:\ns_cbranch_execz .WB273\nSv 164,1,16,5,1024\nMvav 180,136,116\nCV 160,160,161\nCV 161,162,163\nMvav 180,132,108\nRa 188,3,31744\n"
"s_barrier\nMvav 180,128,100\nCV 162,168,169\nCV 163,170,171\nMvav 180,144,36\nRa 128,3,32768\nSv 160,1,16,5,0\nMvav 180,148,44\nMvav 180,152,52\nRa 132,3,33792\nMvav 180,156,60\n"
"WL 6\ns_add_u32 m0, s89, 81920\ns_add_u32 s46, s96, 1277952\nMvav 176,156,56\nRa 136,3,34816\nMvav 176,152,48\nD1 1,16,46,0\nMvav 176,148,40\nRa 140,3,35840\nMvav 176,144,32\n"
"Zvav 184,160,96\nRa 144,3,36864\nMvav 184,164,104\nMvav 184,168,112\nRa 148,3,37888\nMvav 184,172,120\nWL 6\nZvav 188,172,124\nRa 152,3,38912\nMvav 188,168,116\nD1 1,16,46,1024\n"
"Mvav 188,164,108\nRa 156,3,39936\nMvav 188,160,100\nMvav 188,176,36\nRa 160,3,40960\nWV 9\nv_cmp_ne_u32 vcc, s15, v27\ns_nop 0\ns_cbranch_vccnz .S380\n.S381:\nMvav 188,180,44\n"
"s_add_u32 s4, s92, 0\nMvav 188,184,52\nRa 164,3,41984\nMvav 188,188,60\nWL 6\nbuffer_load_dwordx4 v[208:211], v1, s[16:19], s4 offen offset:0 sc0 sc1\nMvav 184,188,56\n"
"Ra 168,3,43008\nMvav 184,184,48\nD1 1,16,46,2048\nMvav 184,180,40\nRa 172,3,44032\nMvav 184,176,32\nbuffer_load_dwordx4 v[212:215], v1, s[16:19], s4 offen offset:1024 sc0 sc1\n"
"Zvav 192,128,96\nRa 176,3,45056\nv_lshrrev_b32 v88, 8, v1\nMvav 192,132,104\nv_lshlrev_b32 v89, 5, v88\nCV 180,180,181\nMvav 192,136,112\nRa 180,3,46080\nCV 181,182,183\n"
"Mvav 192,140,120\nWL 6\nCV 182,188,189\nZvav 196,140,124\nRa 184,3,47104\nv_bfe_u32 v90, v1, 4, 4\nMvav 196,136,116\nD1 1,16,46,3072\nCV 183,190,191\nMvav 196,132,108\n"
"Ra 188,3,48128\ns_add_u32 s5, s94, 524288\nMvav 196,128,100\nv_mul_u32_u24 v91, 896, v90\nSv 180,1,16,5,1024\nMvav 196,144,36\nRa 128,3,49152\nv_lshl_add_u32 v91, v88, 4, v91\n"
"Mvav 196,148,44\nv_add_u32 v90, s21, v90\nCV 176,176,177\nMvav 196,152,52\nRa 132,3,50176\nCV 177,178,179\nMvav 196,156,60\nWL 6\ns_add_u32 m0, s89, 65536\n"
"s_add_u32 s46, s96, 1310720\nMvav 192,156,56\nRa 136,3,51200\nv_lshlrev_b32 v88, 3, v90\nMvav 192,152,48\nD1 1,16,46,0\nCV 178,184,185\nMvav 192,148,40\nRa 140,3,52224\n"
"CV 179,186,187\nMvav 192,144,32\nv_add_u32 v94, 64, v90\nSv 176,1,16,5,0\nZvav 200,160,96\nRa 144,3,53248\ns_mov_b32 s63, 0xc040000\nMvav 200,164,104\nv_lshrrev_b32 v92, 8, v1\n"
"Mvav 200,168,112\nRa 148,3,54272\nMvav 200,172,120\nWL 6\nZvav 204,172,124\nRa 152,3,55296\nv_bfe_u32 v93, v1, 6, 2\nMvav 204,168,116\nD1 1,16,46,1024\nv_cmp_eq_u32 vcc, v92, v93\n"
"v_and_b32 v92, 16, v1\nv_mov_b32 v93, 0x3f80\nv_cndmask_b32 v93, 0, v93, vcc\nMvav 204,164,108\nRa 156,3,56320\nMvav 204,160,100\nv_lshlrev_b32 v93, v92, v93\nMvav 204,176,36\n"
"Ra 160,3,57344\nv_and_b32 v92, 32, v1\nMvav 204,180,44\ns_add_u32 s4, s92, 131072\nv_cmp_eq_u32 vcc, 0, v92\nv_mov_b32 v4, 0\nv_mov_b32 v5, 0\nv_cndmask_b32 v6, 0, v93, vcc\n"
"Mvav 204,184,52\nRa 164,3,58368\nMvav 204,188,60\nWL 6\nbuffer_load_dwordx4 v[216:219], v1, s[16:19], s4 offen offset:0 sc0 sc1\nMvav 200,188,56\nRa 168,3,59392\n"
"v_sub_u32 v7, v93, v6\nMvav 200,184,48\nD1 1,16,46,2048\nMvav 200,180,40\nRa 172,3,60416\nMvav 200,176,32\n"
"buffer_load_dwordx4 v[220:223], v1, s[16:19], s4 offen offset:1024 sc0 sc1\nZvav 160,128,96\nRa 176,3,61440\nv_mov_b32 v8, 0\nMvav 160,132,104\nv_mov_b32 v9, 0\nCV 196,196,197\n"
"Mvav 160,136,112\nRa 180,3,62464\nCV 197,198,199\nMvav 160,140,120\nWL 6\nCV 198,204,205\nZvav 164,140,124\nRa 184,3,63488\nCV 199,206,207\nMvav 164,136,116\nD1 1,16,46,3072\n"
"s_add_u32 s5, s94, 655360\nMvav 164,132,108\nRa 188,3,64512\ns_barrier\nMvav 164,128,100\nSv 196,1,16,5,1024\nCV 192,192,193\nMvav 164,144,36\nRa 128,0,0\nCV 193,194,195\n"
"Mvav 164,148,44\nCV 194,200,201\nCV 195,202,203\nMvav 164,152,52\nRa 132,0,1024\nSv 192,1,16,5,0\nMvav 164,156,60\nWL 6\ns_add_u32 m0, s89, 98304\ns_add_u32 s46, s96, 1343488\n"
"Mvav 160,156,56\nRa 136,0,2048\nMvav 160,152,48\nD1 1,16,46,0\nMvav 160,148,40\nRa 140,0,3072\nMvav 160,144,32\nZvav 168,160,96\nRa 144,0,4096\nWV 12\nv_max_u32 v21, v211, v215\n"
"v_cmp_eq_u32 vcc, -1, v21\ns_nop 0\ns_cbranch_vccnz .S356\n.S357:\nMvav 168,164,104\nv_lshlrev_b32 v176, 16, v208\nMvav 168,168,112\nRa 148,0,5120\nMvav 168,172,120\nWL 6\n"
"v_and_b32 v177, 0xffff0000, v208\nZvav 172,172,124\nRa 152,0,6144\nMvav 172,168,116\nD1 1,16,46,1024\nMvav 172,164,108\nRa 156,0,7168\nv_lshlrev_b32 v178, 16, v209\n"
"Mvav 172,160,100\nv_and_b32 v179, 0xffff0000, v209\nMvav 172,176,36\nRa 160,0,8192\nv_lshlrev_b32 v180, 16, v212\nMvav 172,180,44\nv_and_b32 v181, 0xffff0000, v212\n"
"s_add_u32 s4, s92, 262144\nMvav 172,184,52\nRa 164,0,9216\nMvav 172,188,60\nWL 6\nv_lshlrev_b32 v182, 16, v213\n"
"buffer_load_dwordx4 v[224:227], v1, s[16:19], s4 offen offset:0 sc0 sc1\nMvav 168,188,56\nRa 168,0,10240\nMvav 168,184,48\nD1 1,16,46,2048\nMvav 168,180,40\nRa 172,0,11264\n"
"v_and_b32 v183, 0xffff0000, v213\nMvav 168,176,32\nv_lshlrev_b32 v184, 16, v210\nbuffer_load_dwordx4 v[228:231], v1, s[16:19], s4 offen offset:1024 sc0 sc1\nMvav 176,128,96\n"
"Ra 176,0,12288\nv_and_b32 v185, 0xffff0000, v210\nMvav 176,132,104\nv_lshlrev_b32 v186, 16, v211\nCV 164,164,165\nMvav 176,136,112\nRa 180,0,13312\nCV 165,166,167\n"
"Mvav 176,140,120\nWL 6\nCV 166,172,173\nMvav 180,140,124\nRa 184,0,14336\nv_and_b32 v187, 0xffff0000, v211\nMvav 180,136,116\nD1 1,16,46,3072\nCV 167,174,175\nMvav 180,132,108\n"
"Ra 188,0,15360\ns_add_u32 s5, s94, 786432\nMvav 180,128,100\nv_lshlrev_b32 v188, 16, v214\nSv 164,1,16,5,1024\nMvav 180,144,36\nRa 128,0,16384\nv_and_b32 v189, 0xffff0000, v214\n"
"Mvav 180,148,44\nv_lshlrev_b32 v190, 16, v215\nCV 160,160,161\nMvav 180,152,52\nRa 132,0,17408\nCV 161,162,163\nMvav 180,156,60\nWL 6\ns_add_u32 m0, s89, 114688\n"
"s_add_u32 s46, s96, 1376256\nMvav 176,156,56\nRa 136,0,18432\nv_and_b32 v191, 0xffff0000, v215\nMvav 176,152,48\nD1 1,16,46,0\nCV 162,168,169\nMvav 176,148,40\nRa 140,0,19456\n"
"CV 163,170,171\nMvav 176,144,32\nSv 160,1,16,5,0\nMvav 184,160,96\nRa 144,0,20480\nWV 12\nv_max_u32 v21, v219, v223\nv_cmp_eq_u32 vcc, -1, v21\ns_nop 0\ns_cbranch_vccnz .S359\n"
".S360:\nMvav 184,164,104\nv_lshlrev_b32 v192, 16, v216\nMvav 184,168,112\nRa 148,0,21504\nMvav 184,172,120\nWL 6\nv_and_b32 v193, 0xffff0000, v216\nMvav 188,172,124\n"
"Ra 152,0,22528\nMvav 188,168,116\nD1 1,16,46,1024\nMvav 188,164,108\nRa 156,0,23552\nv_lshlrev_b32 v194, 16, v217\nMvav 188,160,100\nv_and_b32 v195, 0xffff0000, v217\n"
"Mvav 188,176,36\nRa 160,0,24576\nv_lshlrev_b32 v196, 16, v220\nMvav 188,180,44\nv_and_b32 v197, 0xffff0000, v220\ns_add_u32 s4, s92, 393216\nMvav 188,184,52\nRa 164,0,25600\n"
"Mvav 188,188,60\nWL 6\nv_lshlrev_b32 v198, 16, v221\nbuffer_load_dwordx4 v[208:211], v1, s[16:19], s4 offen offset:0 sc0 sc1\nMvav 184,188,56\nRa 168,0,26624\nMvav 184,184,48\n"
"D1 1,16,46,2048\nMvav 184,180,40\nRa 172,0,27648\nv_and_b32 v199, 0xffff0000, v221\nMvav 184,176,32\nv_lshlrev_b32 v200, 16, v218\n"
"buffer_load_dwordx4 v[212:215], v1, s[16:19], s4 offen offset:1024 sc0 sc1\nMvav 192,128,96\nRa 176,0,28672\nv_and_b32 v201, 0xffff0000, v218\nMvav 192,132,104\n"
"v_lshlrev_b32 v202, 16, v219\nCV 180,180,181\nMvav 192,136,112\nRa 180,0,29696\nCV 181,182,183\nMvav 192,140,120\ns_waitcnt vmcnt(15) lgkmcnt(6)\nCV 182,188,189\nMvav 196,140,124\n"
"Ra 184,0,30720\nv_and_b32 v203, 0xffff0000, v219\nMvav 196,136,116\nD1 1,16,46,3072\nCV 183,190,191\nMvav 196,132,108\nRa 188,0,31744\ns_barrier\nMvav 196,128,100\n"
"v_lshlrev_b32 v204, 16, v222\ns_add_u32 s5, s94, 917504\nMvav 196,144,36\nRa 128,2,16384\nv_and_b32 v205, 0xffff0000, v222\nMvav 196,148,44\nv_lshlrev_b32 v206, 16, v223\n"
"Sv 180,1,16,5,1024\nMvav 196,152,52\nRa 132,2,17408\nCV 176,176,177\nMvav 196,156,60\nWL 6\ns_add_u32 m0, s89, 0\ns_add_u32 s46, s96, 1409024\nMvav 192,156,56\nRa 136,2,18432\n"
"v_and_b32 v207, 0xffff0000, v223\nMvav 192,152,48\nD1 1,16,46,0\nCV 177,178,179\nMvav 192,148,40\nRa 140,2,19456\nCV 178,184,185\nMvav 192,144,32\nCV 179,186,187\nSv 176,1,16,5,0\n"
"Mvav 200,160,96\nRa 144,2,20480\nWV 12\nv_max_u32 v21, v227, v231\nv_cmp_eq_u32 vcc, -1, v21\ns_nop 0\ns_cbranch_vccnz .S362\n.S363:\nMvav 200,164,104\n"
"v_lshlrev_b32 v160, 16, v224\nMvav 200,168,112\nRa 148,2,21504\nMvav 200,172,120\nWL 6\nv_and_b32 v161, 0xffff0000, v224\nMvav 204,172,124\nRa 152,2,22528\nMvav 204,168,116\n"
"D1 1,16,46,1024\nMvav 204,164,108\nRa 156,2,23552\nv_lshlrev_b32 v162, 16, v225\nMvav 204,160,100\nv_and_b32 v163, 0xffff0000, v225\nMvav 204,176,36\nRa 160,2,24576\n"
"v_lshlrev_b32 v164, 16, v228\nMvav 204,180,44\nv_and_b32 v165, 0xffff0000, v228\ns_add_u32 s4, s92, 524288\nMvav 204,184,52\nRa 164,2,25600\nMvav 204,188,60\nWL 6\n"
"v_lshlrev_b32 v166, 16, v229\nbuffer_load_dwordx4 v[216:219], v1, s[16:19], s4 offen offset:0 sc0 sc1\nMvav 200,188,56\nRa 168,2,26624\nMvav 200,184,48\nD1 1,16,46,2048\n"
"Mvav 200,180,40\nRa 172,2,27648\nv_and_b32 v167, 0xffff0000, v229\nMvav 200,176,32\nv_lshlrev_b32 v168, 16, v226\n"
"buffer_load_dwordx4 v[220:223], v1, s[16:19], s4 offen offset:1024 sc0 sc1\nMvav 160,128,96\nRa 176,2,28672\nv_and_b32 v169, 0xffff0000, v226\nMvav 160,132,104\n"
"v_lshlrev_b32 v170, 16, v227\nCV 196,196,197\nMvav 160,136,112\nRa 180,2,29696\nCV 197,198,199\nMvav 160,140,120\nWL 6\nCV 198,204,205\nMvav 164,140,124\nRa 184,2,30720\n"
"v_and_b32 v24, 0x70, v1\nMvav 164,136,116\nD1 1,16,46,3072\nCV 199,206,207\nMvav 164,132,108\nRa 188,2,31744\nv_and_b32 v171, 0xffff0000, v227\nMvav 164,128,100\n"
"v_lshlrev_b32 v172, 16, v230\ns_add_u32 s5, s94, 1048576\nMvav 164,144,36\nRa 128,2,0\nv_lshlrev_b32 v24, 5, v24\nMvav 164,148,44\nv_and_b32 v173, 0xffff0000, v230\n"
"Sv 196,1,16,5,1024\nMvav 164,152,52\nRa 132,2,1024\nCV 192,192,193\nMvav 164,156,60\nWL 6\nv_lshlrev_b32 v174, 16, v231\nMvav 160,156,56\nRa 136,2,2048\n"
"buffer_load_dword v27, v24, s[16:19], s41 offen sc0 sc1\nMvav 160,152,48\nv_and_b32 v175, 0xffff0000, v231\nCV 193,194,195\nMvav 160,148,40\nRa 140,2,3072\nCV 194,200,201\n"
"Mvav 160,144,32\nCV 195,202,203\nSv 192,1,16,5,0\nMvav 168,160,96\nRa 144,2,4096\nWV 12\nv_max_u32 v21, v211, v215\nv_cmp_eq_u32 vcc, -1, v21\ns_nop 0\ns_cbranch_vccnz .S365\n"
".S366:\nMvav 168,164,104\nv_lshlrev_b32 v176, 16, v208\nMvav 168,168,112\nRa 148,2,5120\nMvav 168,172,120\nWL 6\nMvav 172,172,124\nRa 152,2,6144\nv_and_b32 v177, 0xffff0000, v208\n"
"Mvav 172,168,116\nv_lshlrev_b32 v178, 16, v209\nMvav 172,164,108\nRa 156,2,7168\nMvav 172,160,100\nv_and_b32 v179, 0xffff0000, v209\nMvav 172,176,36\nRa 160,2,8192\n"
"v_lshlrev_b32 v180, 16, v212\nMvav 172,180,44\nv_and_b32 v181, 0xffff0000, v212\ns_add_u32 s4, s92, 655360\nMvav 172,184,52\nRa 164,2,9216\nMvav 172,188,60\nWL 6\n"
"buffer_load_dwordx4 v[224:227], v1, s[16:19], s4 offen offset:0 sc0 sc1\nMvav 168,188,56\nRa 168,2,10240\nv_lshlrev_b32 v182, 16, v213\nMvav 168,184,48\n"
"v_and_b32 v183, 0xffff0000, v213\nMvav 168,180,40\nRa 172,2,11264\nMvav 168,176,32\nv_lshlrev_b32 v184, 16, v210\n"
"buffer_load_dwordx4 v[228:231], v1, s[16:19], s4 offen offset:1024 sc0 sc1\nMvav 176,128,96\nRa 176,2,12288\nv_and_b32 v185, 0xffff0000, v210\nMvav 176,132,104\n"
"v_lshlrev_b32 v186, 16, v211\nCV 164,164,165\nMvav 176,136,112\nRa 180,2,13312\nCV 165,166,167\nMvav 176,140,120\ns_waitcnt vmcnt(13) lgkmcnt(6)\nCV 166,172,173\nMvav 180,140,124\n"
"Ra 184,2,14336\nCV 167,174,175\nMvav 180,136,116\nv_and_b32 v187, 0xffff0000, v211\ns_add_u32 s5, s94, 1179648\nMvav 180,132,108\nRa 188,2,15360\ns_barrier\nMvav 180,128,100\n"
"v_lshlrev_b32 v188, 16, v214\nSv 164,1,16,5,1024\nMvav 180,144,36\nRa 128,2,32768\nv_and_b32 v189, 0xffff0000, v214\nMvav 180,148,44\nv_lshlrev_b32 v190, 16, v215\nCV 160,160,161\n"
"Mvav 180,152,52\nRa 132,2,33792\nCV 161,162,163\nMvav 180,156,60\nWL 6\nCV 162,168,169\nMvav 176,156,56\nRa 136,2,34816\nCV 163,170,171\nMvav 176,152,48\n"
"v_and_b32 v191, 0xffff0000, v215\n.W579:\ns_cbranch_execz .WB273\nSv 160,1,16,5,0\nMvav 176,148,40\nRa 140,2,35840\nMvav 176,144,32\nMvav 184,160,96\nRa 144,2,36864\nWV 8\n"
"v_max_u32 v21, v219, v223\nv_cmp_eq_u32 vcc, -1, v21\ns_nop 0\ns_cbranch_vccnz .S368\n.S369:\nMvav 184,164,104\nv_lshlrev_b32 v192, 16, v216\nMvav 184,168,112\nRa 148,2,37888\n"
"Mvav 184,172,120\nWL 6\nMvav 188,172,124\nRa 152,2,38912\nv_and_b32 v193, 0xffff0000, v216\nMvav 188,168,116\nv_lshlrev_b32 v194, 16, v217\nMvav 188,164,108\nRa 156,2,39936\n"
"Mvav 188,160,100\nv_and_b32 v195, 0xffff0000, v217\nMvav 188,176,36\nRa 160,2,40960\nv_lshlrev_b32 v196, 16, v220\nMvav 188,180,44\nv_and_b32 v197, 0xffff0000, v220\n"
"s_add_u32 s4, s92, 786432\nMvav 188,184,52\nRa 164,2,41984\nMvav 188,188,60\nWL 6\nbuffer_load_dwordx4 v[208:211], v1, s[16:19], s4 offen offset:0 sc0 sc1\nMvav 184,188,56\n"
"Ra 168,2,43008\nv_lshlrev_b32 v198, 16, v221\nMvav 184,184,48\nv_and_b32 v199, 0xffff0000, v221\nMvav 184,180,40\nRa 172,2,44032\nMvav 184,176,32\nv_lshlrev_b32 v200, 16, v218\n"
"buffer_load_dwordx4 v[212:215], v1, s[16:19], s4 offen offset:1024 sc0 sc1\nMvav 192,128,96\nRa 176,2,45056\nv_and_b32 v201, 0xffff0000, v218\nMvav 192,132,104\n"
"v_lshlrev_b32 v202, 16, v219\nCV 180,180,181\nMvav 192,136,112\nRa 180,2,46080\nCV 181,182,183\nMvav 192,140,120\nWL 6\nCV 182,188,189\nMvav 196,140,124\nRa 184,2,47104\n"
"CV 183,190,191\nMvav 196,136,116\nv_and_b32 v203, 0xffff0000, v219\ns_add_u32 s5, s94, 1310720\nMvav 196,132,108\nRa 188,2,48128\nSv 180,1,16,5,1024\nMvav 196,128,100\n"
"v_lshlrev_b32 v204, 16, v222\nCV 176,176,177\nMvav 196,144,36\nRa 128,2,49152\nv_and_b32 v205, 0xffff0000, v222\nMvav 196,148,44\nv_lshlrev_b32 v206, 16, v223\nCV 177,178,179\n"
"Mvav 196,152,52\nRa 132,2,50176\nCV 178,184,185\nMvav 196,156,60\nWL 6\nCV 179,186,187\nMvav 192,156,56\nRa 136,2,51200\nSv 176,1,16,5,0\nMvav 192,152,48\n"
"v_and_b32 v207, 0xffff0000, v223\nMvav 192,148,40\nRa 140,2,52224\nMvav 192,144,32\nMvav 200,160,96\nRa 144,2,53248\nWV 6\nv_max_u32 v21, v227, v231\nv_cmp_eq_u32 vcc, -1, v21\n"
"s_nop 0\ns_cbranch_vccnz .S371\n.S372:\nMvav 200,164,104\nv_lshlrev_b32 v160, 16, v224\nv_xor_b32 v27, s15, v27\nv_and_b32 v26, 0xffffdfff, v27\nv_cmp_ne_u32 s[64:65], 0, v27\n"
"v_cmp_ne_u32 vcc, 0, v26\ns_nop 0\ns_cbranch_vccnz .S383\n.S384:\nMvav 200,168,112\nRa 148,2,54272\ns_bitcmp1_b32 s64, 0\ns_cselect_b32 s2, 0x2000, 0\nMvav 200,172,120\nWL 6\n"
"Mvav 204,172,124\nRa 152,2,55296\nv_and_b32 v161, 0xffff0000, v224\nMvav 204,168,116\nv_lshlrev_b32 v162, 16, v225\nMvav 204,164,108\nRa 156,2,56320\ns_xor_b32 s2, s2, s20\n"
"Mvav 204,160,100\nv_and_b32 v163, 0xffff0000, v225\nMvav 204,176,36\nRa 160,2,57344\nv_lshlrev_b32 v164, 16, v228\nMvav 204,180,44\nv_and_b32 v165, 0xffff0000, v228\n"
"Mvav 204,184,52\nRa 164,2,58368\nMvav 204,188,60\nWL 6\ns_add_u32 s24, s2, 0x0\nMvav 200,188,56\nRa 168,2,59392\nv_lshlrev_b32 v166, 16, v229\nMvav 200,184,48\n"
"v_and_b32 v167, 0xffff0000, v229\ns_bitcmp1_b32 s64, 1\ns_cselect_b32 s2, 0x2000, 0\nMvav 200,180,40\nRa 172,2,60416\nMvav 200,176,32\nv_lshlrev_b32 v168, 16, v226\n"
"Mvav 160,128,96\nRa 176,2,61440\nv_and_b32 v169, 0xffff0000, v226\nMvav 160,132,104\nv_lshlrev_b32 v170, 16, v227\nCV 196,196,197\nMvav 160,136,112\nRa 180,2,62464\n"
"CV 197,198,199\nMvav 160,140,120\nWL 6\nCV 198,204,205\nMvav 164,140,124\nRa 184,2,63488\nCV 199,206,207\nMvav 164,136,116\nv_and_b32 v171, 0xffff0000, v227\n"
"s_add_u32 s5, s94, 1441792\nMvav 164,132,108\nRa 188,2,64512\ns_barrier\nMvav 164,128,100\nv_lshlrev_b32 v172, 16, v230\nSv 196,1,16,5,1024\nMvav 164,144,36\nRa 128,3,0\n"
"v_and_b32 v173, 0xffff0000, v230\nMvav 164,148,44\nv_lshlrev_b32 v174, 16, v231\nCV 192,192,193\nMvav 164,152,52\nRa 132,3,1024\nCV 193,194,195\nMvav 164,156,60\nWL 6\n"
"s_xor_b32 s2, s2, s20\nMvav 160,156,56\nRa 136,3,2048\nCV 194,200,201\nMvav 160,152,48\nv_and_b32 v175, 0xffff0000, v231\nCV 195,202,203\nMvav 160,148,40\nRa 140,3,3072\n"
"Sv 192,1,16,5,0\nMvav 160,144,32\ns_add_u32 s25, s2, 0x1800000\ns_bitcmp1_b32 s64, 2\ns_cselect_b32 s2, 0x2000, 0\nMvav 168,160,96\nRa 144,3,4096\nWV 4\nv_max_u32 v21, v211, v215\n"
"v_cmp_eq_u32 vcc, -1, v21\ns_nop 0\ns_cbranch_vccnz .S374\n.S375:\nMvav 168,164,104\nv_lshlrev_b32 v176, 16, v208\nMvav 168,168,112\nRa 148,3,5120\nMvav 168,172,120\nWL 6\n"
"Mvav 172,172,124\nRa 152,3,6144\nv_and_b32 v177, 0xffff0000, v208\nMvav 172,168,116\nv_lshlrev_b32 v178, 16, v209\nMvav 172,164,108\nRa 156,3,7168\ns_xor_b32 s2, s2, s20\n"
"Mvav 172,160,100\nv_and_b32 v179, 0xffff0000, v209\nMvav 172,176,36\nRa 160,3,8192\nv_lshlrev_b32 v180, 16, v212\nMvav 172,180,44\nv_and_b32 v181, 0xffff0000, v212\n"
"Mvav 172,184,52\nRa 164,3,9216\nMvav 172,188,60\nWL 6\ns_add_u32 s26, s2, 0x3000000\nMvav 168,188,56\nRa 168,3,10240\nv_lshlrev_b32 v182, 16, v213\nMvav 168,184,48\n"
"v_and_b32 v183, 0xffff0000, v213\ns_bitcmp1_b32 s64, 3\ns_cselect_b32 s2, 0x2000, 0\nMvav 168,180,40\nRa 172,3,11264\nMvav 168,176,32\nv_lshlrev_b32 v184, 16, v210\n"
"Mvav 176,128,96\nRa 176,3,12288\ns_xor_b32 s2, s2, s20\nMvav 176,132,104\nv_and_b32 v185, 0xffff0000, v210\nCV 164,164,165\nMvav 176,136,112\nRa 180,3,13312\nCV 165,166,167\n"
"Mvav 176,140,120\nWL 6\nCV 166,172,173\nMvav 180,140,124\nRa 184,3,14336\nCV 167,174,175\nMvav 180,136,116\nv_lshlrev_b32 v186, 16, v211\ns_add_u32 s5, s94, 1572864\n"
"Mvav 180,132,108\nRa 188,3,15360\ns_add_u32 s27, s2, 0x4800000\nMvav 180,128,100\nv_and_b32 v187, 0xffff0000, v211\ns_bitcmp1_b32 s64, 4\ns_cselect_b32 s2, 0x2000, 0\n"
"Mvav 180,144,36\nv_lshlrev_b32 v188, 16, v214\nSv 164,1,16,5,1024\nMvav 180,148,44\nv_and_b32 v189, 0xffff0000, v214\nCV 160,160,161\nMvav 180,152,52\n"
"v_lshlrev_b32 v190, 16, v215\nCV 161,162,163\nMvav 180,156,60\nWL 4\ns_xor_b32 s2, s2, s20\nMvav 176,156,56\nv_and_b32 v191, 0xffff0000, v215\nCV 162,168,169\nMvav 176,152,48\n"
"s_add_u32 s32, s2, 0x6000000\ns_bitcmp1_b32 s64, 5\ns_cselect_b32 s2, 0x2000, 0\nMvav 176,148,40\ns_xor_b32 s2, s2, s20\nCV 163,170,171\nMvav 176,144,32\n"
"s_add_u32 s33, s2, 0x7800000\ns_bitcmp1_b32 s64, 6\ns_cselect_b32 s2, 0x2000, 0\nMvav 184,160,96\ns_xor_b32 s2, s2, s20\ns_add_u32 s34, s2, 0x9000000\nMvav 184,164,104\n"
"s_bitcmp1_b32 s64, 7\ns_cselect_b32 s2, 0x2000, 0\nMvav 184,168,112\ns_xor_b32 s2, s2, s20\nSv 160,1,16,5,0\nMvav 184,172,120\nWL 0\nMvav 188,172,124\n"
"s_add_u32 s35, s2, 0xa800000\nMvav 188,168,116\nMvav 188,164,108\nMvav 188,160,100\nMvav 188,176,36\nMvav 188,180,44\nMvav 188,184,52\nMvav 188,188,60\nMvav 184,188,56\n"
"Mvav 184,184,48\nMvav 184,180,40\nMvav 184,176,32\nCV 180,180,181\nCV 181,182,183\ns_nop 1\nCV 182,188,189\nCV 183,190,191\ns_add_u32 s5, s94, 1703936\nSv 180,1,16,5,1024\n"
"CV 176,176,177\nCV 177,178,179\nCV 178,184,185\nCV 179,186,187\nSv 176,1,16,5,0\ns_cmp_eq_u32 s13, 7\ns_cbranch_scc1 .Z386\n"
"buffer_load_dwordx4 v[12:15], v91, s[28:31], s22 offen offset:0\nbuffer_load_dwordx4 v[16:19], v91, s[28:31], s23 offen offset:0\n"
"buffer_load_dwordx4 v[64:67], v89, s[16:19], s40 offen offset:0 sc0 sc1\nbuffer_load_dwordx4 v[68:71], v89, s[16:19], s40 offen offset:16 sc0 sc1\n"
"buffer_load_dwordx4 v[72:75], v89, s[16:19], s40 offen offset:1792 sc0 sc1\nbuffer_load_dwordx4 v[76:79], v89, s[16:19], s40 offen offset:1808 sc0 sc1\n"
"buffer_load_dwordx2 v[80:81], v88, s[16:19], s63 offen offset:0 sc0 sc1\nbuffer_load_dwordx2 v[82:83], v88, s[16:19], s63 offen offset:512 sc0 sc1\n"
"buffer_load_dwordx4 v[96:99], v1, s[16:19], s24 offen offset:0 sc0 sc1\nbuffer_load_dwordx4 v[100:103], v1, s[16:19], s24 offen offset:1024 sc0 sc1\n"
"buffer_load_dwordx4 v[104:107], v1, s[16:19], s25 offen offset:0 sc0 sc1\nbuffer_load_dwordx4 v[108:111], v1, s[16:19], s25 offen offset:1024 sc0 sc1\n"
"buffer_load_dwordx4 v[112:115], v1, s[16:19], s26 offen offset:0 sc0 sc1\nbuffer_load_dwordx4 v[116:119], v1, s[16:19], s26 offen offset:1024 sc0 sc1\n"
"buffer_load_dwordx4 v[120:123], v1, s[16:19], s27 offen offset:0 sc0 sc1\nbuffer_load_dwordx4 v[124:127], v1, s[16:19], s27 offen offset:1024 sc0 sc1\n"
"buffer_load_dwordx4 v[32:35], v1, s[16:19], s32 offen offset:0 sc0 sc1\nbuffer_load_dwordx4 v[36:39], v1, s[16:19], s32 offen offset:1024 sc0 sc1\n"
"buffer_load_dwordx4 v[40:43], v1, s[16:19], s33 offen offset:0 sc0 sc1\nbuffer_load_dwordx4 v[44:47], v1, s[16:19], s33 offen offset:1024 sc0 sc1\n"
"buffer_load_dwordx4 v[48:51], v1, s[16:19], s34 offen offset:0 sc0 sc1\nbuffer_load_dwordx4 v[52:55], v1, s[16:19], s34 offen offset:1024 sc0 sc1\n"
"buffer_load_dwordx4 v[56:59], v1, s[16:19], s35 offen offset:0 sc0 sc1\nbuffer_load_dwordx4 v[60:63], v1, s[16:19], s35 offen offset:1024 sc0 sc1\nWV 16\nv_xor_b32 v66, s15, v66\n"
"v_xor_b32 v70, s15, v70\nv_xor_b32 v74, s15, v74\nv_xor_b32 v78, s15, v78\nv_xor_b32 v81, s15, v81\nv_xor_b32 v83, s15, v83\nv_or3_b32 v92, v66, v70, v74\n"
"v_or3_b32 v92, v92, v78, v81\nv_or_b32 v92, v92, v83\nv_cmp_ne_u32 vcc, 0, v92\ns_nop 0\ns_cbranch_vccnz .S377\n.S378:\nv_and_b32 v92, 0x7ff, v67\nv_lshrrev_b32 v93, 16, v67\n"
"v_cmp_eq_u32 s[44:45], v92, v90\nv_cmp_eq_u32 s[46:47], v93, v90\nv_cmp_eq_u32 s[48:49], v92, v94\nv_cmp_eq_u32 s[50:51], v93, v94\nv_cndmask_b32 v12, v12, v64, s[44:45]\n"
"v_cndmask_b32 v13, v13, v65, s[44:45]\nv_cndmask_b32 v14, v14, v68, s[44:45]\nv_cndmask_b32 v15, v15, v69, s[44:45]\nv_cndmask_b32 v12, v12, v72, s[46:47]\n"
"v_cndmask_b32 v13, v13, v73, s[46:47]\nv_cndmask_b32 v14, v14, v76, s[46:47]\nv_cndmask_b32 v15, v15, v77, s[46:47]\nv_cndmask_b32 v16, v16, v64, s[48:49]\n"
"v_cndmask_b32 v17, v17, v65, s[48:49]\nv_cndmask_b32 v18, v18, v68, s[48:49]\nv_cndmask_b32 v19, v19, v69, s[48:49]\nv_cndmask_b32 v16, v16, v72, s[50:51]\n"
"v_cndmask_b32 v17, v17, v73, s[50:51]\nv_cndmask_b32 v18, v18, v76, s[50:51]\nv_cndmask_b32 v19, v19, v77, s[50:51]\nv_sub_f32 v92, 1.0, v80\nv_sub_f32 v93, 1.0, v82\nCV 92,92,93\n"
"v_and_b32 v93, 0xffff0000, v92\nv_lshlrev_b32 v92, 16, v92\nv_lshlrev_b32 v64, 16, v12\nv_and_b32 v65, 0xffff0000, v12\nMU 64,64,92\nMU 65,65,92\nv_lshlrev_b32 v66, 16, v13\n"
"v_and_b32 v67, 0xffff0000, v13\nMU 66,66,92\nMU 67,67,92\nv_lshlrev_b32 v68, 16, v14\nv_and_b32 v69, 0xffff0000, v14\nMU 68,68,92\nMU 69,69,92\nv_lshlrev_b32 v70, 16, v15\n"
"v_and_b32 v71, 0xffff0000, v15\nMU 70,70,92\nMU 71,71,92\nv_lshlrev_b32 v72, 16, v16\nv_and_b32 v73, 0xffff0000, v16\nMU 72,72,93\nMU 73,73,93\nv_lshlrev_b32 v74, 16, v17\n"
"v_and_b32 v75, 0xffff0000, v17\nMU 74,74,93\nMU 75,75,93\nv_lshlrev_b32 v76, 16, v18\nv_and_b32 v77, 0xffff0000, v18\nMU 76,76,93\nMU 77,77,93\nv_lshlrev_b32 v78, 16, v19\n"
"v_and_b32 v79, 0xffff0000, v19\nMU 78,78,93\nMU 79,79,93\ns_mov_b32 s7, 262144\nWV 0\n.Z389:\nv_max_u32 v84, v99, v103\nv_cmp_eq_u32 s[44:45], -1, v84\nv_max_u32 v84, v107, v111\n"
"v_cmp_eq_u32 s[46:47], -1, v84\nv_max_u32 v84, v115, v119\nv_cmp_eq_u32 s[48:49], -1, v84\nv_max_u32 v84, v123, v127\nv_cmp_eq_u32 s[50:51], -1, v84\nv_max_u32 v84, v35, v39\n"
"v_cmp_eq_u32 s[52:53], -1, v84\nv_max_u32 v84, v43, v47\nv_cmp_eq_u32 s[54:55], -1, v84\nv_max_u32 v84, v51, v55\nv_cmp_eq_u32 s[56:57], -1, v84\nv_max_u32 v84, v59, v63\n"
"v_cmp_eq_u32 s[58:59], -1, v84\ns_or_b64 s[0:1], s[44:45], s[46:47]\ns_or_b64 s[2:3], s[48:49], s[50:51]\ns_or_b64 s[0:1], s[0:1], s[52:53]\ns_or_b64 s[2:3], s[2:3], s[54:55]\n"
"s_or_b64 s[0:1], s[0:1], s[56:57]\ns_or_b64 s[2:3], s[2:3], s[58:59]\ns_or_b64 s[0:1], s[0:1], s[2:3]\ns_cbranch_scc1 .Z387\n.Z388:\nZvvv 136,6,96\nMvvv 136,6,104\nMvvv 136,6,112\n"
"Mvvv 136,6,120\nMvvv 136,6,32\nMvvv 136,6,40\nMvvv 136,6,48\nMvvv 136,6,56\nZvvv 140,4,96\nMvvv 140,4,104\nMvvv 140,4,112\nMvvv 140,4,120\nMvvv 140,4,32\nMvvv 140,4,40\n"
"Mvvv 140,4,48\nMvvv 140,4,56\nZvvv 144,6,100\nMvvv 144,6,108\nMvvv 144,6,116\nMvvv 144,6,124\nMvvv 144,6,36\nMvvv 144,6,44\nMvvv 144,6,52\nMvvv 144,6,60\nZvvv 148,4,100\n"
"Mvvv 148,4,108\nMvvv 148,4,116\nMvvv 148,4,124\nMvvv 148,4,36\nMvvv 148,4,44\nMvvv 148,4,52\nMvvv 148,4,60\nFM 64,80,136\nFM 65,80,137\nFM 66,80,138\nFM 67,80,139\nFM 68,80,140\n"
"FM 69,80,141\nFM 70,80,142\nFM 71,80,143\nCV 12,64,65\nCV 13,66,67\nCV 14,68,69\nCV 15,70,71\nSv 12,91,36,22,0\nFM 72,82,144\nFM 73,82,145\nFM 74,82,146\nFM 75,82,147\n"
"FM 76,82,148\nFM 77,82,149\nFM 78,82,150\nFM 79,82,151\nCV 16,72,73\nCV 17,74,75\nCV 18,76,77\nCV 19,78,79\nSv 16,91,36,23,0\n.Z386:\ns_endpgm\n.EP398:\n.S269:\n"
"s_mov_b32 s7, 262144\n.S271:\ns_lshl_b32 s5, s73, 9\ns_add_u32 s5, s5, 0xc049400\nv_lshrrev_b32 v27, 1, v1\nbuffer_load_dwordx2 v[22:23], v27, s[16:19], s5 offen sc0 sc1\nWV 0\n"
"v_cmp_ne_u32 vcc, s15, v23\ns_nop 0\ns_cbranch_vccz .S270\ns_sleep 6\ns_sub_u32 s7, s7, 1\ns_cbranch_scc0 .S271\ns_branch .S270\n.T278:\ns_mov_b32 s62, 1\n"
"s_call_b64 s[80:81], .RA265\ns_branch .T279\n.T280:\ns_mov_b32 s62, 1\ns_call_b64 s[80:81], .RA265\ns_branch .T281\n.S283:\ns_mov_b32 s7, 262144\n.S285:\n"
"buffer_load_dword v27, off, s[16:19], s5 sc0 sc1\nWV 0\nv_cmp_ne_u32 vcc, s15, v27\ns_nop 0\ns_cbranch_vccz .S284\ns_sleep 6\ns_sub_u32 s7, s7, 1\ns_cbranch_scc0 .S285\n"
"s_branch .S284\n.S286:\ns_mov_b32 s7, 262144\n.S288:\nv_and_b32 v27, 0x70, v1\nbuffer_load_dword v27, v27, s[16:19], s61 offen offset:128 sc0 sc1\nWV 0\n"
"v_cmp_ne_u32 vcc, s15, v27\ns_nop 0\ns_cbranch_vccz .S287\ns_sleep 6\ns_sub_u32 s7, s7, 1\ns_cbranch_scc0 .S288\ns_branch .S287\n.S289:\ns_mov_b32 s7, 262144\n.S291:\n"
"v_and_b32 v27, 0x70, v1\nbuffer_load_dword v27, v27, s[16:19], s61 offen offset:136 sc0 sc1\nWV 0\nv_cmp_ne_u32 vcc, s15, v27\ns_nop 0\ns_cbranch_vccz .S290\ns_sleep 6\n"
"s_sub_u32 s7, s7, 1\ns_cbranch_scc0 .S291\ns_branch .S290\n.S292:\ns_mov_b32 s7, 262144\n.S294:\nv_and_b32 v27, 0x70, v1\n"
"buffer_load_dword v27, v27, s[16:19], s61 offen offset:140 sc0 sc1\nWV 0\nv_cmp_ne_u32 vcc, s15, v27\ns_nop 0\ns_cbranch_vccz .S293\ns_sleep 6\ns_sub_u32 s7, s7, 1\n"
"s_cbranch_scc0 .S294\ns_branch .S293\n.S295:\ns_mov_b32 s7, 262144\n.S297:\nv_and_b32 v27, 0x70, v1\nbuffer_load_dword v27, v27, s[16:19], s61 offen offset:12 sc0 sc1\nWV 0\n"
"v_cmp_ne_u32 vcc, s15, v27\ns_nop 0\ns_cbranch_vccz .S296\ns_sleep 6\ns_sub_u32 s7, s7, 1\ns_cbranch_scc0 .S297\ns_branch .S296\n.S298:\ns_mov_b32 s7, 262144\n.S300:\n"
"v_and_b32 v27, 0x70, v1\nbuffer_load_dword v27, v27, s[16:19], s61 offen offset:8 sc0 sc1\nWV 0\nv_cmp_ne_u32 vcc, s15, v27\ns_nop 0\ns_cbranch_vccz .S299\ns_sleep 6\n"
"s_sub_u32 s7, s7, 1\ns_cbranch_scc0 .S300\ns_branch .S299\n.S301:\ns_mov_b32 s7, 262144\n.S303:\nv_and_b32 v27, 0xf0, v1\n"
"buffer_load_dword v27, v27, s[16:19], s61 offen offset:4 sc0 sc1\nWV 0\nv_cmp_ne_u32 vcc, s15, v27\ns_nop 0\ns_cbranch_vccz .S302\ns_sleep 6\ns_sub_u32 s7, s7, 1\n"
"s_cbranch_scc0 .S303\ns_branch .S302\n.T312:\ns_mov_b32 s62, 0\ns_call_b64 s[80:81], .RB266\ns_branch .T313\n.T314:\ns_mov_b32 s62, 0\ns_call_b64 s[80:81], .RA265\ns_branch .T315\n"
".T318:\ns_mov_b32 s62, 0\ns_call_b64 s[80:81], .RB266\ns_branch .T319\n.T320:\ns_mov_b32 s62, 0\ns_call_b64 s[80:81], .RA265\ns_branch .T321\n.T327:\ns_mov_b32 s62, 2\n"
"s_call_b64 s[80:81], .RB266\ns_branch .T328\n.T329:\ns_mov_b32 s62, 1\ns_call_b64 s[80:81], .RA265\ns_branch .T330\n.T331:\ns_mov_b32 s62, 1\ns_call_b64 s[80:81], .RA265\n"
"s_branch .T332\n.T344:\ns_mov_b32 s62, 0\ns_call_b64 s[80:81], .RB266\ns_branch .T345\n.T346:\ns_mov_b32 s62, 0\ns_call_b64 s[80:81], .RA265\ns_branch .T347\n.T350:\n"
"s_mov_b32 s62, 0\ns_call_b64 s[80:81], .RB266\ns_branch .T351\n.T352:\ns_mov_b32 s62, 0\ns_call_b64 s[80:81], .RA265\ns_branch .T353\n.T354:\ns_mov_b32 s62, 2\n"
"s_call_b64 s[80:81], .RB266\ns_branch .T355\n.S356:\ns_mov_b32 s7, 262144\n.S358:\ns_add_u32 s0, s92, 0\nbuffer_load_dwordx4 v[208:211], v1, s[16:19], s0 offen offset:0 sc0 sc1\n"
"buffer_load_dwordx4 v[212:215], v1, s[16:19], s0 offen offset:1024 sc0 sc1\nWV 0\nv_max_u32 v21, v211, v215\nv_cmp_eq_u32 vcc, -1, v21\ns_nop 0\ns_cbranch_vccz .S357\ns_sleep 6\n"
"s_sub_u32 s7, s7, 1\ns_cbranch_scc0 .S358\ns_branch .S357\n.S359:\ns_mov_b32 s7, 262144\n.S361:\ns_add_u32 s0, s92, 131072\n"
"buffer_load_dwordx4 v[216:219], v1, s[16:19], s0 offen offset:0 sc0 sc1\nbuffer_load_dwordx4 v[220:223], v1, s[16:19], s0 offen offset:1024 sc0 sc1\nWV 0\n"
"v_max_u32 v21, v219, v223\nv_cmp_eq_u32 vcc, -1, v21\ns_nop 0\ns_cbranch_vccz .S360\ns_sleep 6\ns_sub_u32 s7, s7, 1\ns_cbranch_scc0 .S361\ns_branch .S360\n.S362:\n"
"s_mov_b32 s7, 262144\n.S364:\ns_add_u32 s0, s92, 262144\nbuffer_load_dwordx4 v[224:227], v1, s[16:19], s0 offen offset:0 sc0 sc1\n"
"buffer_load_dwordx4 v[228:231], v1, s[16:19], s0 offen offset:1024 sc0 sc1\nWV 0\nv_max_u32 v21, v227, v231\nv_cmp_eq_u32 vcc, -1, v21\ns_nop 0\ns_cbranch_vccz .S363\ns_sleep 6\n"
"s_sub_u32 s7, s7, 1\ns_cbranch_scc0 .S364\ns_branch .S363\n.S365:\ns_mov_b32 s7, 262144\n.S367:\ns_add_u32 s0, s92, 393216\n"
"buffer_load_dwordx4 v[208:211], v1, s[16:19], s0 offen offset:0 sc0 sc1\nbuffer_load_dwordx4 v[212:215], v1, s[16:19], s0 offen offset:1024 sc0 sc1\nWV 0\n"
"v_max_u32 v21, v211, v215\nv_cmp_eq_u32 vcc, -1, v21\ns_nop 0\ns_cbranch_vccz .S366\ns_sleep 6\ns_sub_u32 s7, s7, 1\ns_cbranch_scc0 .S367\ns_branch .S366\n.S368:\n"
"s_mov_b32 s7, 262144\n.S370:\ns_add_u32 s0, s92, 524288\nbuffer_load_dwordx4 v[216:219], v1, s[16:19], s0 offen offset:0 sc0 sc1\n"
"buffer_load_dwordx4 v[220:223], v1, s[16:19], s0 offen offset:1024 sc0 sc1\nWV 0\nv_max_u32 v21, v219, v223\nv_cmp_eq_u32 vcc, -1, v21\ns_nop 0\ns_cbranch_vccz .S369\ns_sleep 6\n"
"s_sub_u32 s7, s7, 1\ns_cbranch_scc0 .S370\ns_branch .S369\n.S371:\ns_mov_b32 s7, 262144\n.S373:\ns_add_u32 s0, s92, 655360\n"
"buffer_load_dwordx4 v[224:227], v1, s[16:19], s0 offen offset:0 sc0 sc1\nbuffer_load_dwordx4 v[228:231], v1, s[16:19], s0 offen offset:1024 sc0 sc1\nWV 0\n"
"v_max_u32 v21, v227, v231\nv_cmp_eq_u32 vcc, -1, v21\ns_nop 0\ns_cbranch_vccz .S372\ns_sleep 6\ns_sub_u32 s7, s7, 1\ns_cbranch_scc0 .S373\ns_branch .S372\n.S374:\n"
"s_mov_b32 s7, 262144\n.S376:\ns_add_u32 s0, s92, 786432\nbuffer_load_dwordx4 v[208:211], v1, s[16:19], s0 offen offset:0 sc0 sc1\n"
"buffer_load_dwordx4 v[212:215], v1, s[16:19], s0 offen offset:1024 sc0 sc1\nWV 0\nv_max_u32 v21, v211, v215\nv_cmp_eq_u32 vcc, -1, v21\ns_nop 0\ns_cbranch_vccz .S375\ns_sleep 6\n"
"s_sub_u32 s7, s7, 1\ns_cbranch_scc0 .S376\ns_branch .S375\n.S377:\ns_mov_b32 s7, 262144\n.S379:\nbuffer_load_dwordx4 v[64:67], v89, s[16:19], s40 offen offset:0 sc0 sc1\n"
"buffer_load_dwordx4 v[68:71], v89, s[16:19], s40 offen offset:16 sc0 sc1\nbuffer_load_dwordx4 v[72:75], v89, s[16:19], s40 offen offset:1792 sc0 sc1\n"
"buffer_load_dwordx4 v[76:79], v89, s[16:19], s40 offen offset:1808 sc0 sc1\nbuffer_load_dwordx2 v[80:81], v88, s[16:19], s63 offen offset:0 sc0 sc1\n"
"buffer_load_dwordx2 v[82:83], v88, s[16:19], s63 offen offset:512 sc0 sc1\nWV 0\nv_xor_b32 v66, s15, v66\nv_xor_b32 v70, s15, v70\nv_xor_b32 v74, s15, v74\n"
"v_xor_b32 v78, s15, v78\nv_xor_b32 v81, s15, v81\nv_xor_b32 v83, s15, v83\nv_or3_b32 v92, v66, v70, v74\nv_or3_b32 v92, v92, v78, v81\nv_or_b32 v92, v92, v83\n"
"v_cmp_ne_u32 vcc, 0, v92\ns_nop 0\ns_cbranch_vccz .S378\ns_sleep 6\ns_sub_u32 s7, s7, 1\ns_cbranch_scc0 .S379\ns_branch .S378\n.S380:\ns_mov_b32 s7, 262144\n.S382:\n"
"buffer_load_dword v27, off, s[16:19], s60 offset:512 sc0 sc1\nWV 0\nv_cmp_ne_u32 vcc, s15, v27\ns_nop 0\ns_cbranch_vccz .S381\ns_sleep 6\ns_sub_u32 s7, s7, 1\n"
"s_cbranch_scc0 .S382\ns_branch .S381\n.S383:\ns_mov_b32 s7, 262144\n.S385:\nbuffer_load_dword v27, v24, s[16:19], s41 offen sc0 sc1\nWV 0\nv_xor_b32 v27, s15, v27\n"
"v_and_b32 v26, 0xffffdfff, v27\nv_cmp_ne_u32 s[64:65], 0, v27\nv_cmp_ne_u32 vcc, 0, v26\ns_nop 0\ns_cbranch_vccz .S384\ns_sleep 6\ns_sub_u32 s7, s7, 1\ns_cbranch_scc0 .S385\n"
"s_branch .S384\n.Z387:\ns_sub_u32 s7, s7, 1\ns_cbranch_scc1 .Z388\ns_cmp_lg_u64 s[44:45], 0\ns_cbranch_scc0 .Z390\n"
"buffer_load_dwordx4 v[96:99], v1, s[16:19], s24 offen offset:0 sc0 sc1\nbuffer_load_dwordx4 v[100:103], v1, s[16:19], s24 offen offset:1024 sc0 sc1\n.Z390:\n"
"s_cmp_lg_u64 s[46:47], 0\ns_cbranch_scc0 .Z391\nbuffer_load_dwordx4 v[104:107], v1, s[16:19], s25 offen offset:0 sc0 sc1\n"
"buffer_load_dwordx4 v[108:111], v1, s[16:19], s25 offen offset:1024 sc0 sc1\n.Z391:\ns_cmp_lg_u64 s[48:49], 0\ns_cbranch_scc0 .Z392\n"
"buffer_load_dwordx4 v[112:115], v1, s[16:19], s26 offen offset:0 sc0 sc1\nbuffer_load_dwordx4 v[116:119], v1, s[16:19], s26 offen offset:1024 sc0 sc1\n.Z392:\n"
"s_cmp_lg_u64 s[50:51], 0\ns_cbranch_scc0 .Z393\nbuffer_load_dwordx4 v[120:123], v1, s[16:19], s27 offen offset:0 sc0 sc1\n"
"buffer_load_dwordx4 v[124:127], v1, s[16:19], s27 offen offset:1024 sc0 sc1\n.Z393:\ns_cmp_lg_u64 s[52:53], 0\ns_cbranch_scc0 .Z394\n"
"buffer_load_dwordx4 v[32:35], v1, s[16:19], s32 offen offset:0 sc0 sc1\nbuffer_load_dwordx4 v[36:39], v1, s[16:19], s32 offen offset:1024 sc0 sc1\n.Z394:\n"
"s_cmp_lg_u64 s[54:55], 0\ns_cbranch_scc0 .Z395\nbuffer_load_dwordx4 v[40:43], v1, s[16:19], s33 offen offset:0 sc0 sc1\n"
"buffer_load_dwordx4 v[44:47], v1, s[16:19], s33 offen offset:1024 sc0 sc1\n.Z395:\ns_cmp_lg_u64 s[56:57], 0\ns_cbranch_scc0 .Z396\n"
"buffer_load_dwordx4 v[48:51], v1, s[16:19], s34 offen offset:0 sc0 sc1\nbuffer_load_dwordx4 v[52:55], v1, s[16:19], s34 offen offset:1024 sc0 sc1\n.Z396:\n"
"s_cmp_lg_u64 s[58:59], 0\ns_cbranch_scc0 .Z397\nbuffer_load_dwordx4 v[56:59], v1, s[16:19], s35 offen offset:0 sc0 sc1\n"
"buffer_load_dwordx4 v[60:63], v1, s[16:19], s35 offen offset:1024 sc0 sc1\n.Z397:\ns_sleep 8\nWV 0\ns_branch .Z389\n.RA265:\ns_nop 7\ns_nop 3\nv_max3_f32 v22, v32, v33, v34\n"
"v_max3_f32 v22, v22, v35, v40\nv_max3_f32 v22, v22, v41, v42\nv_max_f32 v22, v22, v43\nv_mov_b32 v91, v22\ns_nop 1\nv_permlane32_swap_b32 v22, v91\nv_max_f32 v22, v22, v91\n"
"v_mov_b32 v91, v22\ns_nop 1\nv_permlane16_swap_b32 v22, v91\nv_max_f32 v22, v22, v91\nv_ceil_f32 v22, v22\nv_max3_f32 v23, v36, v37, v38\nv_max3_f32 v23, v23, v39, v44\n"
"v_max3_f32 v23, v23, v45, v46\nv_max_f32 v23, v23, v47\nv_mov_b32 v91, v23\ns_nop 1\nv_permlane32_swap_b32 v23, v91\nv_max_f32 v23, v23, v91\nv_mov_b32 v91, v23\ns_nop 1\n"
"v_permlane16_swap_b32 v23, v91\nv_max_f32 v23, v23, v91\nv_ceil_f32 v23, v23\ns_bitcmp1_b32 s62, 0\ns_cbranch_scc0 .R399\nv_cmp_lt_f32 vcc, 0xff000000, v22\ns_nop 1\n"
"v_cndmask_b32 v22, 0, v22, vcc\nv_cmp_lt_f32 vcc, 0xff000000, v23\ns_nop 1\nv_cndmask_b32 v23, 0, v23, vcc\ns_branch .R400\n.R399:\nv_max_f32 v22, 0, v22\nv_max_f32 v23, 0, v23\n"
"s_bitcmp1_b32 s62, 3\ns_cbranch_scc0 .R400\nv_max_f32 v22, v20, v22\nv_max_f32 v23, v21, v23\n.R400:\nAD 88,88,22\nAD 89,89,23\nSU 32,32,22\nSU 33,33,22\nSU 34,34,22\nSU 35,35,22\n"
"SU 36,36,23\nSU 37,37,23\nSU 38,38,23\nSU 39,39,23\nSU 40,40,22\nSU 41,41,22\nSU 42,42,22\nSU 43,43,22\nSU 44,44,23\nSU 45,45,23\nSU 46,46,23\nSU 47,47,23\nv_mov_b32 v90, v80\n"
"v_mov_b32 v91, v81\nEX 80,32\nEX 81,33\ns_nop 0\nAD 84,80,81\nCV 64,80,81\nEX 82,34\nEX 83,35\nAD 84,84,82\nAD 84,84,83\nCV 65,82,83\nEX 80,36\nEX 81,37\ns_nop 0\nAD 85,80,81\n"
"CV 68,80,81\nEX 82,38\nEX 83,39\nAD 85,85,82\nAD 85,85,83\nCV 69,82,83\nEX 80,40\nEX 81,41\nAD 84,84,80\nAD 84,84,81\nCV 66,80,81\nEX 82,42\nEX 83,43\nAD 84,84,82\nAD 84,84,83\n"
"CV 67,82,83\nEX 80,44\nEX 81,45\nAD 85,85,80\nAD 85,85,81\nCV 70,80,81\nEX 82,46\nEX 83,47\nAD 85,85,82\nAD 85,85,83\nCV 71,82,83\nv_mov_b32 v80, v90\nv_mov_b32 v81, v91\n"
"s_bitcmp1_b32 s62, 1\ns_cbranch_scc1 .R401\nSU 48,48,22\nSU 49,49,22\nSU 50,50,22\nSU 51,51,22\nSU 52,52,23\nSU 53,53,23\nSU 54,54,23\nSU 55,55,23\nSU 56,56,22\nSU 57,57,22\n"
"SU 58,58,22\nSU 59,59,22\nSU 60,60,23\nSU 61,61,23\nSU 62,62,23\nSU 63,63,23\nSU 4,4,22\nSU 5,5,22\nSU 6,6,22\nSU 7,7,22\nSU 8,8,23\nSU 9,9,23\nSU 10,10,23\nSU 11,11,23\n"
"SU 12,12,22\nSU 13,13,22\nSU 14,14,22\nSU 15,15,22\nSU 16,16,23\nSU 17,17,23\nSU 18,18,23\nSU 19,19,23\nEX 80,48\nEX 81,49\n.R401:\ns_bitcmp1_b32 s62, 0\ns_cbranch_scc0 .RS267\n"
"s_nop 1\ns_setpc_b64 s[80:81]\n.RB266:\ns_nop 7\ns_nop 3\nv_max3_f32 v22, v48, v49, v50\nv_max3_f32 v22, v22, v51, v56\nv_max3_f32 v22, v22, v57, v58\nv_max_f32 v22, v22, v59\n"
"v_mov_b32 v91, v22\ns_nop 1\nv_permlane32_swap_b32 v22, v91\nv_max_f32 v22, v22, v91\nv_mov_b32 v91, v22\ns_nop 1\nv_permlane16_swap_b32 v22, v91\nv_max_f32 v22, v22, v91\n"
"v_ceil_f32 v22, v22\nv_max3_f32 v23, v52, v53, v54\nv_max3_f32 v23, v23, v55, v60\nv_max3_f32 v23, v23, v61, v62\nv_max_f32 v23, v23, v63\nv_mov_b32 v91, v23\ns_nop 1\n"
"v_permlane32_swap_b32 v23, v91\nv_max_f32 v23, v23, v91\nv_mov_b32 v91, v23\ns_nop 1\nv_permlane16_swap_b32 v23, v91\nv_max_f32 v23, v23, v91\nv_ceil_f32 v23, v23\n"
"s_bitcmp1_b32 s62, 0\ns_cbranch_scc0 .R404\nv_cmp_lt_f32 vcc, 0xff000000, v22\nv_cndmask_b32 v22, 0, v22, vcc\nv_cmp_lt_f32 vcc, 0xff000000, v23\nv_cndmask_b32 v23, 0, v23, vcc\n"
"s_branch .R405\n.R404:\nv_max_f32 v22, 0, v22\nv_max_f32 v23, 0, v23\ns_bitcmp1_b32 s62, 3\ns_cbranch_scc0 .R405\nv_max_f32 v22, v20, v22\nv_max_f32 v23, v21, v23\n.R405:\n"
"AD 88,88,22\nAD 89,89,23\nSU 48,48,22\nSU 49,49,22\nSU 50,50,22\nSU 51,51,22\nSU 52,52,23\nSU 53,53,23\nSU 54,54,23\nSU 55,55,23\nSU 56,56,22\nSU 57,57,22\nSU 58,58,22\n"
"SU 59,59,22\nSU 60,60,23\nSU 61,61,23\nSU 62,62,23\nSU 63,63,23\nv_mov_b32 v90, v80\nv_mov_b32 v91, v81\nEX 80,48\nEX 81,49\ns_nop 0\nAD 84,80,81\nCV 72,80,81\nEX 82,50\nEX 83,51\n"
"AD 84,84,82\nAD 84,84,83\nCV 73,82,83\nEX 80,52\nEX 81,53\ns_nop 0\nAD 85,80,81\nCV 76,80,81\nEX 82,54\nEX 83,55\nAD 85,85,82\nAD 85,85,83\nCV 77,82,83\nEX 80,56\nEX 81,57\n"
"AD 84,84,80\nAD 84,84,81\nCV 74,80,81\nEX 82,58\nEX 83,59\nAD 84,84,82\nAD 84,84,83\nCV 75,82,83\nEX 80,60\nEX 81,61\nAD 85,85,80\nAD 85,85,81\nCV 78,80,81\nEX 82,62\nEX 83,63\n"
"AD 85,85,82\nAD 85,85,83\nCV 79,82,83\nv_mov_b32 v80, v90\nv_mov_b32 v81, v91\ns_bitcmp1_b32 s62, 1\ns_cbranch_scc1 .R406\nSU 32,32,22\nSU 33,33,22\nSU 34,34,22\nSU 35,35,22\n"
"SU 36,36,23\nSU 37,37,23\nSU 38,38,23\nSU 39,39,23\nSU 40,40,22\nSU 41,41,22\nSU 42,42,22\nSU 43,43,22\nSU 44,44,23\nSU 45,45,23\nSU 46,46,23\nSU 47,47,23\nSU 4,4,22\nSU 5,5,22\n"
"SU 6,6,22\nSU 7,7,22\nSU 8,8,23\nSU 9,9,23\nSU 10,10,23\nSU 11,11,23\nSU 12,12,22\nSU 13,13,22\nSU 14,14,22\nSU 15,15,22\nSU 16,16,23\nSU 17,17,23\nSU 18,18,23\nSU 19,19,23\n"
"EX 80,32\nEX 81,33\n.R406:\ns_bitcmp1_b32 s62, 0\ns_cbranch_scc0 .RS267\ns_nop 1\ns_setpc_b64 s[80:81]\n.RS267:\nv_exp_f32 v22, -v22\nv_exp_f32 v23, -v23\nMU 86,86,22\n"
"MU 87,87,23\nMU 128,128,22\nMU 129,129,22\nMU 130,130,22\nMU 131,131,22\nMU 132,132,23\nMU 133,133,23\nMU 134,134,23\nMU 135,135,23\nMU 136,136,22\nMU 137,137,22\nMU 138,138,22\n"
"MU 139,139,22\nMU 140,140,23\nMU 141,141,23\nMU 142,142,23\nMU 143,143,23\nMU 144,144,22\nMU 145,145,22\nMU 146,146,22\nMU 147,147,22\nMU 148,148,23\nMU 149,149,23\nMU 150,150,23\n"
"MU 151,151,23\nMU 152,152,22\nMU 153,153,22\nMU 154,154,22\nMU 155,155,22\nMU 156,156,23\nMU 157,157,23\nMU 158,158,23\nMU 159,159,23\nMU 160,160,22\nMU 161,161,22\nMU 162,162,22\n"
"MU 163,163,22\nMU 164,164,23\nMU 165,165,23\nMU 166,166,23\nMU 167,167,23\nMU 168,168,22\nMU 169,169,22\nMU 170,170,22\nMU 171,171,22\nMU 172,172,23\nMU 173,173,23\nMU 174,174,23\n"
"MU 175,175,23\nMU 176,176,22\nMU 177,177,22\nMU 178,178,22\nMU 179,179,22\nMU 180,180,23\nMU 181,181,23\nMU 182,182,23\nMU 183,183,23\nMU 184,184,22\nMU 185,185,22\nMU 186,186,22\n"
"MU 187,187,22\nMU 188,188,23\nMU 189,189,23\nMU 190,190,23\nMU 191,191,23\nMU 192,192,22\nMU 193,193,22\nMU 194,194,22\nMU 195,195,22\nMU 196,196,23\nMU 197,197,23\nMU 198,198,23\n"
"MU 199,199,23\nMU 200,200,22\nMU 201,201,22\nMU 202,202,22\nMU 203,203,22\nMU 204,204,23\nMU 205,205,23\nMU 206,206,23\nMU 207,207,23\nMU 208,208,22\nMU 209,209,22\nMU 210,210,22\n"
"MU 211,211,22\nMU 212,212,23\nMU 213,213,23\nMU 214,214,23\nMU 215,215,23\nMU 216,216,22\nMU 217,217,22\nMU 218,218,22\nMU 219,219,22\nMU 220,220,23\nMU 221,221,23\nMU 222,222,23\n"
"MU 223,223,23\nMU 224,224,22\nMU 225,225,22\nMU 226,226,22\nMU 227,227,22\nMU 228,228,23\nMU 229,229,23\nMU 230,230,23\nMU 231,231,23\nMU 232,232,22\nMU 233,233,22\nMU 234,234,22\n"
"MU 235,235,22\nMU 236,236,23\nMU 237,237,23\nMU 238,238,23\nMU 239,239,23\nMU 240,240,22\nMU 241,241,22\nMU 242,242,22\nMU 243,243,22\nMU 244,244,23\nMU 245,245,23\nMU 246,246,23\n"
"MU 247,247,23\nMU 248,248,22\nMU 249,249,22\nMU 250,250,22\nMU 251,251,22\nMU 252,252,23\nMU 253,253,23\nMU 254,254,23\nMU 255,255,23\ns_nop 1\ns_setpc_b64 s[80:81]\n.p2align 12\n"
".PA5:\ns_mov_b64 exec, 0\ns_cmp_eq_u32 s11, 1\ns_cbranch_scc1 .W581\ns_cmp_eq_u32 s11, 2\ns_cbranch_scc1 .W582\ns_cmp_eq_u32 s11, 3\ns_cbranch_scc1 .W583\ns_branch .W580\n.WB414:\n"
"s_mov_b64 exec, -1\ns_add_u32 m0, s47, -49152\ns_add_u32 s48, s48, 128\nD0 2,20,48,0\nD0 3,20,48,1024\nD0 4,20,48,2048\nD0 5,20,48,3072\ns_and_b32 s86, s74, 15\n"
"s_lshl2_add_u32 s86, s86, s11\ns_mov_b32 s88, 0\ns_lshl_b32 s93, s73, 9\ns_lshl3_add_u32 s93, s86, s93\ns_add_u32 s93, s93, 0xc049400\ns_lshl_b32 s86, s86, 14\n"
"s_mul_i32 s91, s73, 25165824\ns_add_u32 s86, s86, s91\ns_and_b32 s92, s13, 1\ns_lshl_b32 s92, s92, 13\ns_add_u32 s86, s86, s92\ns_add_u32 s94, s86, 0x1480000\n"
"s_xor_b32 s95, s94, 0x2000\ns_add_u32 s86, s94, 512\ns_load_dword s87, s[54:55], s86\ns_add_u32 m0, s47, -32768\ns_add_u32 s45, s45, 128\nD0 2,20,45,0\nD0 3,20,45,1024\n"
"D0 4,20,45,2048\nD0 5,20,45,3072\ns_add_u32 m0, s47, 32768\ns_add_u32 s44, s44, 0x40000\nD0 6,24,44,0\nD0 7,24,44,1024\nD0 8,24,44,2048\nD0 9,24,44,3072\ns_add_u32 m0, s47, 49152\n"
"s_add_u32 s44, s44, 0x40000\nD0 6,24,44,0\nD0 7,24,44,1024\nD0 8,24,44,2048\nD0 9,24,44,3072\ns_add_u32 m0, s47, -16384\ns_add_u32 s48, s48, 128\nD0 2,20,48,0\nD0 3,20,48,1024\n"
"D0 4,20,48,2048\nD0 5,20,48,3072\nv_mov_b32 v52, 0x40000000\nv_cmp_gt_u32 vcc, 56, v0\ns_lshl2_add_u32 s2, s10, s11\ns_mul_i32 s2, s2, 1792\nv_cndmask_b32 v53, v52, v1, vcc\n"
"s_mov_b32 s36, s56\ns_and_b32 s37, s57, 0xffff\ns_mov_b32 s38, 0x380\ns_mov_b32 s39, 0x20000\ns_mov_b32 s40, s58\ns_and_b32 s41, s59, 0xffff\ns_mov_b32 s42, 0x380\n"
"s_mov_b32 s43, 0x20000\nbuffer_load_dwordx4 v[32:35], v53, s[28:31], s2 offen offset:0\nbuffer_load_dwordx4 v[36:39], v53, s[28:31], s2 offen offset:896\n"
"buffer_load_dwordx4 v[40:43], v53, s[36:39], 0 offen offset:0\nbuffer_load_dwordx4 v[44:47], v53, s[40:43], 0 offen offset:0\nWL 0\ns_mul_i32 s15, s15, 1664525\n"
"s_add_u32 s15, s15, 1013904223\nv_mov_b32 v25, s15\ns_mov_b32 s57, 0x3db8aa3b\ns_and_b32 s59, s11, 1\ns_lshl_b32 s59, s59, 13\ns_mul_i32 s2, s73, 25165824\ns_and_b32 s3, s12, 1\n"
"s_lshl_b32 s3, s3, 21\ns_add_u32 s2, s2, s3\ns_lshl2_add_u32 s3, s13, s11\ns_lshl_b32 s3, s3, 15\ns_add_u32 s2, s2, s3\ns_add_u32 s56, s2, 0x800000\ns_xor_b32 s53, s56, 0xc000\n"
"buffer_load_dword v31, v24, s[24:27], s96 offen\ns_add_u32 s96, s96, 0x80000\nbuffer_load_dword v31, v24, s[24:27], s96 offen\ns_add_u32 s96, s96, 0x80000\n"
"v_lshrrev_b32 v60, 3, v0\nv_and_b32 v61, 0x70, v1\nv_lshl_or_b32 v60, v60, 10, v61\nv_mov_b32 v20, 0\nv_mov_b32 v21, 0\nv_mov_b32 v22, 0\nv_mov_b32 v23, 0\nSv 20,60,16,94,0\n"
"Sv 20,60,16,95,0\nWV 28\ns_barrier\nRa 128,10,0\nRa 132,11,0\nRa 136,10,2048\nRa 140,11,2048\nRa 144,10,8192\nRa 148,11,8192\nRa 152,10,10240\nRa 156,11,10240\nTa 160,12,0\n"
"Ta 162,12,2048\nTa 164,12,16384\nTa 166,12,18432\nTa 168,13,0\nTa 170,13,2048\nTa 172,13,16384\nTa 174,13,18432\nTa 176,14,0\nTa 178,14,2048\nTa 180,14,16384\nTa 182,14,18432\n"
"Ta 184,15,0\nTa 186,15,2048\nTa 188,15,16384\nTa 190,15,18432\nWL 0\ns_barrier\nZvaa 64,128,160\nTa 224,16,0\nMvaa 64,132,164\nTa 226,16,2048\nZvaa 68,140,164\n"
"s_add_u32 m0, s47, -65536\ns_add_u32 s45, s45, 128\nMvaa 68,136,160\nTa 228,16,16384\nZvaa 128,144,160\nTa 230,16,18432\nMvaa 128,148,164\nD0 2,20,45,0\nZvaa 132,156,164\n"
"Ta 232,17,0\nMvaa 132,152,160\nTa 234,17,2048\nZvaa 140,152,168\ns_mov_b32 s36, s60\nMvaa 140,156,172\nTa 236,17,16384\nZvaa 136,148,172\nTa 238,17,18432\nMvaa 136,144,168\n"
"s_and_b32 s37, s61, 0xffff\nZvaa 76,136,168\nTa 240,18,0\nMvaa 76,140,172\nTa 242,18,2048\nZvaa 72,132,172\nD0 3,20,45,1024\nMvaa 72,128,168\nTa 244,18,16384\nZvaa 80,128,176\n"
"Ta 246,18,18432\nMvaa 80,132,180\ns_mov_b32 s38, 0x380000\nZvaa 84,140,180\nTa 248,19,0\nMvaa 84,136,176\nTa 250,19,2048\nZvaa 144,144,176\ns_mov_b32 s39, 0x20000\n"
"Mvaa 144,148,180\nTa 252,19,16384\nZvaa 148,156,180\nTa 254,19,18432\nMvaa 148,152,176\nD0 4,20,45,2048\nZvaa 156,152,184\ns_lshl3_add_u32 s75, s12, s13\nMvaa 156,156,188\n"
"s_lshl2_add_u32 s75, s75, s11\nZvaa 152,148,188\ns_mul_i32 s75, s75, 7168\nMvaa 152,144,184\ns_mul_i32 s76, s11, 7168\nZvaa 92,136,184\ns_add_u32 s76, s76, 131072\n"
"Mvaa 92,140,188\nD0 5,20,45,3072\nZvaa 88,132,188\ns_mov_b32 s42, 1605632\nMvaa 88,128,184\ns_waitcnt vmcnt(24) lgkmcnt(0)\ns_barrier\nZvaa 96,128,224\nRa 192,10,16384\n"
"Mvaa 96,132,228\ns_add_u32 m0, s47, 0\ns_add_u32 s44, s44, 0x40000\nZvaa 100,140,228\nRa 196,11,16384\nMvaa 100,136,224\nZvaa 160,144,224\nRa 200,10,18432\nMvaa 160,148,228\n"
"D0 6,24,44,0\nZvaa 164,156,228\nRa 204,11,18432\nMvaa 164,152,224\nZvaa 172,152,232\nRa 208,10,24576\nMvaa 172,156,236\ns_and_b32 s78, s74, 3\ns_lshl2_add_u32 s78, s73, s78\n"
"s_lshl_b32 s78, s78, 12\ns_bitcmp1_b32 s11, 0\ns_cselect_b32 s79, s48, s45\ns_add_u32 s79, s79, 512\ns_and_b32 s79, s79, 0xfffe0000\ns_add_u32 s78, s78, s79\n"
"buffer_load_dword v31, v26, s[20:23], s78 offen\nZvaa 168,148,236\nRa 212,11,24576\nMvaa 168,144,232\nZvaa 108,136,232\nRa 216,10,26624\nMvaa 108,140,236\nD0 7,24,44,1024\n"
"Zvaa 104,132,236\nRa 220,11,26624\nMvaa 104,128,232\nZvaa 112,128,240\nMvaa 112,132,244\nZvaa 116,140,244\nMvaa 116,136,240\nZvaa 176,144,240\nMvaa 176,148,244\nD0 8,24,44,2048\n"
"Zvaa 180,156,244\nMvaa 180,152,240\nZvaa 188,152,248\nMvaa 188,156,252\nZvaa 184,148,252\nMvaa 184,144,248\nZvaa 124,136,248\nMvaa 124,140,252\nD0 9,24,44,3072\nZvaa 120,132,252\n"
"Mvaa 120,128,248\nWL 0\nZaaa 32,192,224\nRa 128,10,32768\nMaaa 32,196,228\ns_add_u32 m0, s47, 16384\ns_add_u32 s44, s44, 0x40000\nZaaa 36,204,228\nRa 132,11,32768\n"
"Maaa 36,200,224\nZaaa 96,208,224\nRa 136,10,34816\nMaaa 96,212,228\nD0 6,24,44,0\nZaaa 100,220,228\nRa 140,11,34816\nMaaa 100,216,224\nZaaa 108,216,232\nRa 144,10,40960\n"
"Maaa 108,220,236\nZaaa 104,212,236\nRa 148,11,40960\nMaaa 104,208,232\nZaaa 44,200,232\nRa 152,10,43008\nMaaa 44,204,236\nD0 7,24,44,1024\nZaaa 40,196,236\nRa 156,11,43008\n"
"Maaa 40,192,232\nZaaa 48,192,240\nMaaa 48,196,244\nZaaa 52,204,244\nMaaa 52,200,240\nZaaa 112,208,240\nMaaa 112,212,244\nD0 8,24,44,2048\nZaaa 116,220,244\nMaaa 116,216,240\n"
"Zaaa 124,216,248\nMaaa 124,220,252\nZaaa 120,212,252\nMaaa 120,208,248\nZaaa 60,200,248\nMaaa 60,204,252\nD0 9,24,44,3072\nZaaa 56,196,252\nMaaa 56,192,248\n"
"s_waitcnt vmcnt(25) lgkmcnt(0)\ns_barrier\nZaaa 0,192,160\nTa 224,16,32768\nWV 17\nMaaa 0,196,164\nTa 226,16,34816\nZaaa 4,204,164\ns_add_u32 m0, s47, -49152\n"
"s_add_u32 s48, s48, 128\nMaaa 4,200,160\nTa 228,16,49152\nZaaa 64,208,160\nTa 230,16,51200\nbuffer_load_dword v31, v24, s[24:27], s96 offen\ns_add_u32 s96, s96, 0x80000\n"
"Maaa 64,212,164\nD0 2,20,48,0\nv_lshlrev_b32 v48, 16, v32\nZaaa 68,220,164\nTa 232,17,32768\nMaaa 68,216,160\nTa 234,17,34816\nv_lshlrev_b32 v49, 16, v36\nZaaa 76,216,168\n"
"v_lshlrev_b32 v50, 16, v40\nv_lshlrev_b32 v51, 16, v44\nMaaa 76,220,172\nTa 236,17,49152\nZaaa 72,212,172\nTa 238,17,51200\nMU 52,48,50\nMaaa 72,208,168\nMU 53,49,50\n"
"Zaaa 12,200,168\nTa 240,18,32768\nCV 52,52,53\nMaaa 12,204,172\nTa 242,18,34816\nv_lshlrev_b32 v54, 16, v52\nZaaa 8,196,172\nD0 3,20,48,1024\nMaaa 8,192,168\nTa 244,18,49152\n"
"v_and_b32 v55, 0xffff0000, v52\nZaaa 16,192,176\nTa 246,18,51200\nMU 56,48,51\nMaaa 16,196,180\nMU 57,49,51\nZaaa 20,204,180\nTa 248,19,32768\nv_and_b32 v48, 0xffff0000, v32\n"
"Maaa 20,200,176\nTa 250,19,34816\nZaaa 80,208,176\nv_and_b32 v49, 0xffff0000, v36\nv_and_b32 v50, 0xffff0000, v40\nMaaa 80,212,180\nTa 252,19,49152\n"
"v_and_b32 v51, 0xffff0000, v44\nZaaa 84,220,180\nTa 254,19,51200\nMaaa 84,216,176\nD0 4,20,48,2048\nMU 52,48,50\nZaaa 92,216,184\nMU 53,49,50\nCV 52,52,53\nMaaa 92,220,188\n"
"v_and_b32 v53, 0xffff0000, v52\nZaaa 88,212,188\nv_lshlrev_b32 v52, 16, v52\nFM 56,48,51\nMaaa 88,208,184\nFM 57,49,51\nZaaa 28,200,184\nAD 54,54,52\nAD 55,55,53\nMaaa 28,204,188\n"
"D0 5,20,48,3072\nv_lshlrev_b32 v48, 16, v33\nZaaa 24,196,188\nv_lshlrev_b32 v49, 16, v37\nMaaa 24,192,184\nWL 0\nMvaa 96,128,224\nTa 160,12,32768\nv_lshlrev_b32 v50, 16, v41\n"
"Mvaa 96,132,228\nTa 162,12,34816\nMvaa 100,140,228\ns_add_u32 m0, s47, -32768\ns_add_u32 s45, s45, 128\nMvaa 100,136,224\nTa 164,12,49152\nMvaa 160,144,224\nTa 166,12,51200\n"
"v_lshlrev_b32 v51, 16, v45\nMvaa 160,148,228\nD0 2,20,45,0\nMU 52,48,50\nMvaa 164,156,228\nTa 168,13,32768\nMvaa 164,152,224\nTa 170,13,34816\nMU 53,49,50\nMvaa 172,152,232\n"
"CV 52,52,53\nv_and_b32 v53, 0xffff0000, v52\nMvaa 172,156,236\nTa 172,13,49152\nMvaa 168,148,236\nTa 174,13,51200\nv_lshlrev_b32 v52, 16, v52\nMvaa 168,144,232\nFM 56,48,51\n"
"Mvaa 108,136,232\nTa 176,14,32768\nFM 57,49,51\nMvaa 108,140,236\nTa 178,14,34816\nAD 54,54,52\nMvaa 104,132,236\nD0 3,20,45,1024\nMvaa 104,128,232\nTa 180,14,49152\nAD 55,55,53\n"
"Mvaa 112,128,240\nTa 182,14,51200\nv_and_b32 v48, 0xffff0000, v33\nMvaa 112,132,244\nv_and_b32 v49, 0xffff0000, v37\nMvaa 116,140,244\nTa 184,15,32768\n"
"v_and_b32 v50, 0xffff0000, v41\nMvaa 116,136,240\nTa 186,15,34816\nMvaa 176,144,240\nv_and_b32 v51, 0xffff0000, v45\nMU 52,48,50\nMvaa 176,148,244\nTa 188,15,49152\nMU 53,49,50\n"
"Mvaa 180,156,244\nTa 190,15,51200\nMvaa 180,152,240\nD0 4,20,45,2048\nCV 52,52,53\nMvaa 188,152,248\nv_and_b32 v53, 0xffff0000, v52\nv_lshlrev_b32 v52, 16, v52\nMvaa 188,156,252\n"
"FM 56,48,51\nMvaa 184,148,252\nFM 57,49,51\nAD 54,54,52\nMvaa 184,144,248\nAD 55,55,53\nMvaa 124,136,248\nv_lshlrev_b32 v48, 16, v34\nv_lshlrev_b32 v49, 16, v38\nMvaa 124,140,252\n"
"D0 5,20,45,3072\nv_lshlrev_b32 v50, 16, v42\nMvaa 120,132,252\nv_lshlrev_b32 v51, 16, v46\nMvaa 120,128,248\ns_waitcnt vmcnt(18) lgkmcnt(0)\ns_barrier\nMvaa 64,128,160\n"
"Ra 192,10,49152\nMU 52,48,50\nMvaa 64,132,164\ns_add_u32 m0, s47, 32768\ns_add_u32 s44, s44, 0x40000\nMvaa 68,140,164\nRa 196,11,49152\nMU 53,49,50\nMvaa 68,136,160\nCV 52,52,53\n"
"Mvaa 128,144,160\nRa 200,10,51200\nv_and_b32 v53, 0xffff0000, v52\nMvaa 128,148,164\nD0 6,24,44,0\nv_lshlrev_b32 v52, 16, v52\nMvaa 132,156,164\nRa 204,11,51200\nMvaa 132,152,160\n"
"FM 56,48,51\nFM 57,49,51\nMvaa 140,152,168\nRa 208,10,57344\nAD 54,54,52\nMvaa 140,156,172\nAD 55,55,53\nMvaa 136,148,172\nRa 212,11,57344\nv_and_b32 v48, 0xffff0000, v34\n"
"Mvaa 136,144,168\nv_and_b32 v49, 0xffff0000, v38\nMvaa 76,136,168\nRa 216,10,59392\nv_and_b32 v50, 0xffff0000, v42\nMvaa 76,140,172\nD0 7,24,44,1024\n"
"v_and_b32 v51, 0xffff0000, v46\nMvaa 72,132,172\nRa 220,11,59392\nMvaa 72,128,168\nMU 52,48,50\nMU 53,49,50\nMvaa 80,128,176\nCV 52,52,53\nv_and_b32 v53, 0xffff0000, v52\n"
"Mvaa 80,132,180\nv_lshlrev_b32 v52, 16, v52\nMvaa 84,140,180\nFM 56,48,51\nFM 57,49,51\nMvaa 84,136,176\nAD 54,54,52\nMvaa 144,144,176\nAD 55,55,53\nv_lshlrev_b32 v48, 16, v35\n"
"Mvaa 144,148,180\nD0 8,24,44,2048\nv_lshlrev_b32 v49, 16, v39\nMvaa 148,156,180\nv_lshlrev_b32 v50, 16, v43\nMvaa 148,152,176\nv_lshlrev_b32 v51, 16, v47\nMU 52,48,50\n"
"Mvaa 156,152,184\nMU 53,49,50\nCV 52,52,53\nMvaa 156,156,188\nv_and_b32 v53, 0xffff0000, v52\nMvaa 152,148,188\nv_lshlrev_b32 v52, 16, v52\nFM 56,48,51\nMvaa 152,144,184\n"
"FM 57,49,51\nMvaa 92,136,184\nAD 54,54,52\nAD 55,55,53\nMvaa 92,140,188\nD0 9,24,44,3072\nv_and_b32 v48, 0xffff0000, v35\nMvaa 88,132,188\nv_and_b32 v49, 0xffff0000, v39\n"
"Mvaa 88,128,184\nWL 0\nMaaa 0,192,160\nRa 128,10,0\nv_and_b32 v50, 0xffff0000, v43\nMaaa 0,196,164\ns_add_u32 m0, s47, 49152\ns_add_u32 s44, s44, 0x40000\nMaaa 4,204,164\n"
"Ra 132,11,0\nv_and_b32 v51, 0xffff0000, v47\nMaaa 4,200,160\nMU 52,48,50\nMaaa 64,208,160\nRa 136,10,2048\nMU 53,49,50\nMaaa 64,212,164\nD0 6,24,44,0\nCV 52,52,53\n"
"Maaa 68,220,164\nRa 140,11,2048\nMaaa 68,216,160\nv_and_b32 v53, 0xffff0000, v52\nv_lshlrev_b32 v52, 16, v52\nMaaa 76,216,168\nRa 144,10,8192\nFM 56,48,51\nMaaa 76,220,172\n"
"FM 57,49,51\n.W580:\ns_cbranch_execz .W584\nMaaa 72,212,172\nRa 148,11,8192\nAD 54,54,52\nMaaa 72,208,168\nAD 55,55,53\nMaaa 12,200,168\nRa 152,10,10240\n"
"v_permlane32_swap_b32 v54, v55\nMaaa 12,204,172\nD0 7,24,44,1024\nv_permlane32_swap_b32 v56, v57\nMaaa 8,196,172\nRa 156,11,10240\nMaaa 8,192,168\nAD 54,54,55\nAD 56,56,57\n"
"Maaa 16,192,176\nMaaa 16,196,180\nv_permlane16_swap_b32 v54, v56\nMaaa 20,204,180\nAD 54,54,56\nMaaa 20,200,176\nMaaa 80,208,176\n"
"v_add_f32_dpp v54, v54, v54 row_ror:8 row_mask:0xf bank_mask:0xf\nMaaa 80,212,180\nD0 8,24,44,2048\nMaaa 84,220,180\n"
"v_add_f32_dpp v54, v54, v54 row_ror:4 row_mask:0xf bank_mask:0xf\nMaaa 84,216,176\nMaaa 92,216,184\nv_add_f32_dpp v54, v54, v54 row_ror:2 row_mask:0xf bank_mask:0xf\n"
"Maaa 92,220,188\nMaaa 88,212,188\nv_add_f32_dpp v54, v54, v54 row_ror:1 row_mask:0xf bank_mask:0xf\nCV 54,54,54\nMaaa 88,208,184\nv_lshlrev_b32 v54, 16, v54\nMaaa 28,200,184\n"
"v_mul_f32 v55, 0xbfb8aa3b, v54\nMaaa 28,204,188\nD0 9,24,44,3072\nMaaa 24,196,188\nEX 55,55\nMaaa 24,192,184\ns_waitcnt vmcnt(12) lgkmcnt(0)\ns_barrier\nMaaa 32,192,224\n"
"Ta 160,12,0\nv_add_f32 v56, 0, v54\nMaaa 32,196,228\nTa 162,12,2048\nMaaa 36,204,228\ns_add_u32 m0, s47, -16384\ns_add_u32 s48, s48, 128\nMaaa 36,200,224\nTa 164,12,16384\n"
"Maaa 96,208,224\nTa 166,12,18432\nbuffer_load_dword v31, v24, s[24:27], s96 offen\ns_add_u32 s96, s96, 0x80000\nMaaa 96,212,228\nD0 2,20,48,0\nv_add_f32 v55, 1.0, v55\n"
"Maaa 100,220,228\nTa 168,13,0\nv_rcp_f32 v55, v55\nMaaa 100,216,224\nTa 170,13,2048\nv_ashrrev_i32 v57, 31, v56\nMaaa 108,216,232\nCV 55,55,55\nv_lshlrev_b32 v55, 16, v55\n"
"Maaa 108,220,236\nTa 172,13,16384\nMaaa 104,212,236\nTa 174,13,18432\nv_or_b32 v57, 0x80000000, v57\nMaaa 104,208,232\nv_xor_b32 v56, v56, v57\nMaaa 44,200,232\nTa 176,14,0\n"
"v_and_b32 v56, 0xffff0000, v56\nMaaa 44,204,236\nTa 178,14,2048\ns_lshl2_add_u32 s2, s10, s11\nMaaa 40,196,236\nD0 3,20,48,1024\nMaaa 40,192,232\nTa 180,14,16384\n"
"v_lshrrev_b32 v48, 5, v0\nMaaa 48,192,240\nTa 182,14,18432\nv_lshl_add_u32 v48, s2, 1, v48\nMaaa 48,196,244\nv_sub_u32 v49, 0xffff, v48\nMaaa 52,204,244\nTa 184,15,0\n"
"v_or_b32 v56, v56, v49\nMaaa 52,200,240\nTa 186,15,2048\nMaaa 112,208,240\nv_mov_b32 v57, s15\nv_and_b32 v49, 31, v0\nMaaa 112,212,244\nTa 188,15,16384\nv_mov_b32 v52, 0x40000000\n"
"Maaa 116,220,244\nTa 190,15,18432\nv_cmp_eq_u32 vcc, 16, v49\nv_lshlrev_b32 v50, 2, v48\nv_lshlrev_b32 v51, 10, v49\nv_cndmask_b32 v50, v52, v50, vcc\n"
"v_cndmask_b32 v56, v55, v56, vcc\nMaaa 116,216,240\nD0 4,20,48,2048\nv_lshl_add_u32 v51, v48, 3, v51\nMaaa 124,216,248\nv_and_b32 v49, 15, v0\nv_cmp_eq_u32 vcc, 0, v49\n"
"s_mov_b32 s2, 0xc040000\ns_mov_b32 s3, 0xc03e000\nv_cndmask_b32 v51, v52, v51, vcc\nbuffer_store_dwordx2 v[56:57], v51, s[16:19], s2 offen sc0 sc1\n"
"buffer_store_dword v54, v50, s[16:19], s3 offen sc0 sc1\nMaaa 124,220,252\nMaaa 120,212,252\nMaaa 120,208,248\nMaaa 60,200,248\nMaaa 60,204,252\nD0 5,20,48,3072\nMaaa 56,196,252\n"
"Maaa 56,192,248\nWL 0\ns_mov_b32 s50, 3\n.GL413:\nMvaa 64,128,160\nTa 224,16,0\nMvaa 64,132,164\nTa 226,16,2048\nMvaa 68,140,164\nTa 228,16,16384\ns_add_u32 m0, s47, -65536\n"
"s_add_u32 s45, s45, 128\nMvaa 68,136,160\nTa 230,16,18432\nMvaa 128,144,160\nTa 232,17,0\nMvaa 128,148,164\nTa 234,17,2048\nD0 2,20,45,0\nMvaa 132,156,164\nTa 236,17,16384\n"
"Mvaa 132,152,160\nTa 238,17,18432\nMvaa 140,152,168\nTa 240,18,0\nMvaa 140,156,172\nTa 242,18,2048\nMvaa 136,148,172\nTa 244,18,16384\nMvaa 136,144,168\nTa 246,18,18432\n"
"Mvaa 76,136,168\nTa 248,19,0\nMvaa 76,140,172\nTa 250,19,2048\nMvaa 72,132,172\nTa 252,19,16384\nD0 3,20,45,1024\nMvaa 72,128,168\nTa 254,19,18432\nMvaa 80,128,176\n"
"Ra 192,10,16384\nMvaa 80,132,180\nRa 196,11,16384\nMvaa 84,140,180\nRa 200,10,18432\nMvaa 84,136,176\nRa 204,11,18432\nMvaa 144,144,176\nRa 208,10,24576\nMvaa 144,148,180\n"
"Ra 212,11,24576\nMvaa 148,156,180\nRa 216,10,26624\nMvaa 148,152,176\nRa 220,11,26624\nD0 4,20,45,2048\nMvaa 156,152,184\nMvaa 156,156,188\nMvaa 152,148,188\nMvaa 152,144,184\n"
"Mvaa 92,136,184\nMvaa 92,140,188\nD0 5,20,45,3072\nMvaa 88,132,188\nMvaa 88,128,184\ns_waitcnt vmcnt(18) lgkmcnt(8)\ns_barrier\nMvaa 96,128,224\nMvaa 96,132,228\n"
"s_add_u32 m0, s47, 0\ns_add_u32 s44, s44, 0x40000\ns_mov_b32 s86, 1\ns_load_dword s86, s[54:55], s94\nMvaa 100,140,228\ns_add_u32 s94, s94, 1024\nMvaa 100,136,224\n"
"Mvaa 160,144,224\nMvaa 160,148,228\nD0 6,24,44,0\nMvaa 164,156,228\nMvaa 164,152,224\nMvaa 172,152,232\nMvaa 172,156,236\nMvaa 168,148,236\nMvaa 168,144,232\n"
"s_add_u32 s88, s88, s86\nMvaa 108,136,232\nMvaa 108,140,236\nD0 7,24,44,1024\ns_add_u32 s88, s88, s86\nMvaa 104,132,236\nMvaa 104,128,232\ns_add_u32 s88, s88, s86\n"
"Mvaa 112,128,240\nMvaa 112,132,244\ns_add_u32 s88, s88, s86\nMvaa 116,140,244\nMvaa 116,136,240\ns_add_u32 s88, s88, s86\nMvaa 176,144,240\nMvaa 176,148,244\nD0 8,24,44,2048\n"
"s_add_u32 s88, s88, s86\nMvaa 180,156,244\nMvaa 180,152,240\ns_add_u32 s88, s88, s86\nMvaa 188,152,248\nMvaa 188,156,252\ns_add_u32 s88, s88, s86\nMvaa 184,148,252\n"
"Mvaa 184,144,248\ns_add_u32 s88, s88, s86\nMvaa 124,136,248\nMvaa 124,140,252\nD0 9,24,44,3072\ns_add_u32 s88, s88, s86\nMvaa 120,132,252\nMvaa 120,128,248\nWL 0\n"
"s_add_u32 s88, s88, s86\nMaaa 32,192,224\nRa 128,10,32768\nMaaa 32,196,228\ns_add_u32 m0, s47, 16384\ns_add_u32 s44, s44, 0x40000\ns_add_u32 s88, s88, s86\nMaaa 36,204,228\n"
"Ra 132,11,32768\nMaaa 36,200,224\ns_add_u32 s88, s88, s86\nMaaa 96,208,224\nRa 136,10,34816\nMaaa 96,212,228\nD0 6,24,44,0\ns_add_u32 s88, s88, s86\nMaaa 100,220,228\n"
"Ra 140,11,34816\nMaaa 100,216,224\ns_add_u32 s88, s88, s86\nMaaa 108,216,232\nRa 144,10,40960\nMaaa 108,220,236\ns_add_u32 s88, s88, s86\nMaaa 104,212,236\nRa 148,11,40960\n"
"Maaa 104,208,232\nMaaa 44,200,232\nRa 152,10,43008\nMaaa 44,204,236\nD0 7,24,44,1024\nMaaa 40,196,236\nRa 156,11,43008\nMaaa 40,192,232\nMaaa 48,192,240\nMaaa 48,196,244\n"
"Maaa 52,204,244\nMaaa 52,200,240\nMaaa 112,208,240\nMaaa 112,212,244\nD0 8,24,44,2048\nMaaa 116,220,244\nMaaa 116,216,240\nMaaa 124,216,248\nMaaa 124,220,252\nMaaa 120,212,252\n"
"Maaa 120,208,248\nMaaa 60,200,248\nMaaa 60,204,252\nD0 9,24,44,3072\nMaaa 56,196,252\nMaaa 56,192,248\ns_waitcnt vmcnt(12) lgkmcnt(0)\ns_barrier\nMaaa 0,192,160\nTa 224,16,32768\n"
"s_mov_b32 m0, s76\ns_add_u32 s76, s76, 1024\nMaaa 0,196,164\nTa 226,16,34816\nD0 1,36,75,0\ns_add_u32 s75, s75, 1024\nMaaa 4,204,164\ns_add_u32 m0, s47, -49152\n"
"s_add_u32 s48, s48, 128\nMaaa 4,200,160\nTa 228,16,49152\nMaaa 64,208,160\nTa 230,16,51200\nbuffer_load_dword v31, v24, s[24:27], s96 offen\ns_add_u32 s96, s96, 0x80000\n"
"Maaa 64,212,164\nD0 2,20,48,0\nMaaa 68,220,164\nTa 232,17,32768\nMaaa 68,216,160\nTa 234,17,34816\nMaaa 76,216,168\nMaaa 76,220,172\nTa 236,17,49152\nMaaa 72,212,172\n"
"Ta 238,17,51200\nMaaa 72,208,168\nMaaa 12,200,168\nTa 240,18,32768\nMaaa 12,204,172\nTa 242,18,34816\nMaaa 8,196,172\nD0 3,20,48,1024\nMaaa 8,192,168\nTa 244,18,49152\n"
"Maaa 16,192,176\nTa 246,18,51200\nMaaa 16,196,180\nMaaa 20,204,180\nTa 248,19,32768\nMaaa 20,200,176\nTa 250,19,34816\nMaaa 80,208,176\nMaaa 80,212,180\nTa 252,19,49152\n"
"Maaa 84,220,180\nTa 254,19,51200\nMaaa 84,216,176\nD0 4,20,48,2048\nMaaa 92,216,184\nMaaa 92,220,188\nMaaa 88,212,188\nMaaa 88,208,184\nMaaa 28,200,184\nMaaa 28,204,188\n"
"D0 5,20,48,3072\nMaaa 24,196,188\nMaaa 24,192,184\nWL 0\nMvaa 96,128,224\nTa 160,12,32768\nMvaa 96,132,228\nTa 162,12,34816\nMvaa 100,140,228\nTa 164,12,49152\n"
"s_add_u32 m0, s47, -32768\ns_add_u32 s45, s45, 128\nMvaa 100,136,224\nTa 166,12,51200\nMvaa 160,144,224\nTa 168,13,32768\nMvaa 160,148,228\nTa 170,13,34816\nD0 2,20,45,0\n"
"Mvaa 164,156,228\nTa 172,13,49152\nMvaa 164,152,224\nTa 174,13,51200\nMvaa 172,152,232\nTa 176,14,32768\nMvaa 172,156,236\nTa 178,14,34816\nMvaa 168,148,236\nTa 180,14,49152\n"
"Mvaa 168,144,232\nTa 182,14,51200\nMvaa 108,136,232\nTa 184,15,32768\nMvaa 108,140,236\nTa 186,15,34816\nMvaa 104,132,236\nTa 188,15,49152\nD0 3,20,45,1024\nMvaa 104,128,232\n"
"Ta 190,15,51200\nMvaa 112,128,240\nRa 192,10,49152\nMvaa 112,132,244\nRa 196,11,49152\nMvaa 116,140,244\nRa 200,10,51200\nMvaa 116,136,240\nRa 204,11,51200\nMvaa 176,144,240\n"
"Ra 208,10,57344\nMvaa 176,148,244\nRa 212,11,57344\nMvaa 180,156,244\nRa 216,10,59392\nMvaa 180,152,240\nRa 220,11,59392\nD0 4,20,45,2048\nMvaa 188,152,248\nMvaa 188,156,252\n"
"Mvaa 184,148,252\nMvaa 184,144,248\nMvaa 124,136,248\nMvaa 124,140,252\nD0 5,20,45,3072\nMvaa 120,132,252\nMvaa 120,128,248\ns_waitcnt vmcnt(18) lgkmcnt(8)\ns_barrier\n"
"Mvaa 64,128,160\nMvaa 64,132,164\ns_add_u32 m0, s47, 32768\ns_add_u32 s44, s44, 0x40000\ns_mov_b32 s86, 1\ns_load_dword s86, s[54:55], s95\nMvaa 68,140,164\n"
"s_add_u32 s95, s95, 1024\nMvaa 68,136,160\nMvaa 128,144,160\nMvaa 128,148,164\nD0 6,24,44,0\nMvaa 132,156,164\nMvaa 132,152,160\nMvaa 140,152,168\nMvaa 140,156,172\n"
"Mvaa 136,148,172\nMvaa 136,144,168\ns_sub_u32 s88, s88, s86\nMvaa 76,136,168\nMvaa 76,140,172\nD0 7,24,44,1024\ns_sub_u32 s88, s88, s86\nMvaa 72,132,172\nMvaa 72,128,168\n"
"s_sub_u32 s88, s88, s86\nMvaa 80,128,176\nMvaa 80,132,180\ns_sub_u32 s88, s88, s86\nMvaa 84,140,180\nMvaa 84,136,176\ns_sub_u32 s88, s88, s86\nMvaa 144,144,176\nMvaa 144,148,180\n"
"D0 8,24,44,2048\ns_sub_u32 s88, s88, s86\nMvaa 148,156,180\nMvaa 148,152,176\ns_sub_u32 s88, s88, s86\nMvaa 156,152,184\nMvaa 156,156,188\ns_sub_u32 s88, s88, s86\n"
"Mvaa 152,148,188\nMvaa 152,144,184\ns_sub_u32 s88, s88, s86\nMvaa 92,136,184\nMvaa 92,140,188\nD0 9,24,44,3072\ns_sub_u32 s88, s88, s86\nMvaa 88,132,188\nMvaa 88,128,184\nWL 0\n"
"s_sub_u32 s88, s88, s86\nMaaa 0,192,160\nRa 128,10,0\nMaaa 0,196,164\ns_add_u32 m0, s47, 49152\ns_add_u32 s44, s44, 0x40000\ns_sub_u32 s88, s88, s86\nMaaa 4,204,164\nRa 132,11,0\n"
"Maaa 4,200,160\ns_sub_u32 s88, s88, s86\nMaaa 64,208,160\nRa 136,10,2048\nMaaa 64,212,164\nD0 6,24,44,0\ns_sub_u32 s88, s88, s86\nMaaa 68,220,164\nRa 140,11,2048\nMaaa 68,216,160\n"
"s_sub_u32 s88, s88, s86\nMaaa 76,216,168\nRa 144,10,8192\nMaaa 76,220,172\ns_sub_u32 s88, s88, s86\nMaaa 72,212,172\nRa 148,11,8192\nMaaa 72,208,168\nMaaa 12,200,168\n"
"Ra 152,10,10240\nMaaa 12,204,172\nD0 7,24,44,1024\nMaaa 8,196,172\nRa 156,11,10240\nMaaa 8,192,168\nMaaa 16,192,176\nMaaa 16,196,180\nMaaa 20,204,180\nMaaa 20,200,176\n"
"Maaa 80,208,176\nMaaa 80,212,180\nD0 8,24,44,2048\nMaaa 84,220,180\nMaaa 84,216,176\nMaaa 92,216,184\nMaaa 92,220,188\nMaaa 88,212,188\nMaaa 88,208,184\nMaaa 28,200,184\n"
"Maaa 28,204,188\nD0 9,24,44,3072\nMaaa 24,196,188\nMaaa 24,192,184\ns_waitcnt vmcnt(12) lgkmcnt(0)\ns_barrier\nMaaa 32,192,224\nTa 160,12,0\ns_mov_b32 m0, s76\n"
"s_add_u32 s76, s76, 1024\nMaaa 32,196,228\nTa 162,12,2048\nD0 1,36,75,0\ns_add_u32 s75, s75, 1024\nMaaa 36,204,228\ns_add_u32 m0, s47, -16384\ns_add_u32 s48, s48, 128\n"
"Maaa 36,200,224\nTa 164,12,16384\nMaaa 96,208,224\nTa 166,12,18432\nbuffer_load_dword v31, v24, s[24:27], s96 offen\ns_add_u32 s96, s96, 0x80000\nMaaa 96,212,228\n.W581:\n"
"s_cbranch_execz .W585\nD0 2,20,48,0\nMaaa 100,220,228\nTa 168,13,0\nMaaa 100,216,224\nTa 170,13,2048\nMaaa 108,216,232\nMaaa 108,220,236\nTa 172,13,16384\nMaaa 104,212,236\n"
"Ta 174,13,18432\nMaaa 104,208,232\nMaaa 44,200,232\nTa 176,14,0\nMaaa 44,204,236\nTa 178,14,2048\nMaaa 40,196,236\nD0 3,20,48,1024\nMaaa 40,192,232\nTa 180,14,16384\n"
"Maaa 48,192,240\nTa 182,14,18432\nMaaa 48,196,244\nMaaa 52,204,244\nTa 184,15,0\nMaaa 52,200,240\nTa 186,15,2048\nMaaa 112,208,240\nMaaa 112,212,244\nTa 188,15,16384\n"
"Maaa 116,220,244\nTa 190,15,18432\nMaaa 116,216,240\nD0 4,20,48,2048\nMaaa 124,216,248\nMaaa 124,220,252\nMaaa 120,212,252\nMaaa 120,208,248\nMaaa 60,200,248\n"
"s_xor_b32 s94, s94, 0x2000\ns_xor_b32 s95, s95, 0x2000\ns_sub_u32 s88, 0, s88\nMaaa 60,204,252\nD0 5,20,48,3072\ns_sub_u32 s50, s50, 1\nMaaa 56,196,252\nMaaa 56,192,248\nWL 0\n"
"s_cbranch_scc0 .GL413\nMvaa 64,128,160\nTa 224,16,0\nMvaa 64,132,164\nTa 226,16,2048\nMvaa 68,140,164\ns_add_u32 m0, s47, -65536\ns_add_u32 s45, s45, 128\nMvaa 68,136,160\n"
"Ta 228,16,16384\nMvaa 128,144,160\nTa 230,16,18432\nMvaa 128,148,164\nD0 2,20,45,0\nMvaa 132,156,164\nTa 232,17,0\nMvaa 132,152,160\nTa 234,17,2048\nMvaa 140,152,168\n"
"s_sub_u32 s89, 0, s88\ns_cmp_eq_u32 s92, 0\ns_cselect_b32 s88, s88, s89\nMvaa 140,156,172\nTa 236,17,16384\nMvaa 136,148,172\nTa 238,17,18432\nMvaa 136,144,168\n"
"v_mov_b32 v22, s88\nMvaa 76,136,168\nTa 240,18,0\nMvaa 76,140,172\nTa 242,18,2048\nMvaa 72,132,172\nD0 3,20,45,1024\nMvaa 72,128,168\nTa 244,18,16384\nMvaa 80,128,176\n"
"Ta 246,18,18432\nMvaa 80,132,180\nv_mov_b32 v23, s15\nMvaa 84,140,180\nTa 248,19,0\nMvaa 84,136,176\nTa 250,19,2048\nMvaa 144,144,176\n"
"buffer_store_dwordx2 v[22:23], off, s[16:19], s93 sc0 sc1\nMvaa 144,148,180\nTa 252,19,16384\nMvaa 148,156,180\nTa 254,19,18432\nMvaa 148,152,176\nD0 4,20,45,2048\n"
"Mvaa 156,152,184\nMvaa 156,156,188\nMvaa 152,148,188\nMvaa 152,144,184\nMvaa 92,136,184\nMvaa 92,140,188\nD0 5,20,45,3072\nMvaa 88,132,188\nMvaa 88,128,184\n"
"s_waitcnt vmcnt(19) lgkmcnt(0)\ns_barrier\nMvaa 96,128,224\nRa 192,10,16384\nMvaa 96,132,228\ns_add_u32 m0, s47, 0\ns_add_u32 s44, s44, 0x40000\nMvaa 100,140,228\nRa 196,11,16384\n"
"Mvaa 100,136,224\nMvaa 160,144,224\nRa 200,10,18432\nMvaa 160,148,228\nD0 6,24,44,0\nMvaa 164,156,228\nRa 204,11,18432\nMvaa 164,152,224\nMvaa 172,152,232\nRa 208,10,24576\n"
"Mvaa 172,156,236\nMvaa 168,148,236\nRa 212,11,24576\nMvaa 168,144,232\nMvaa 108,136,232\nRa 216,10,26624\nMvaa 108,140,236\nD0 7,24,44,1024\nMvaa 104,132,236\nRa 220,11,26624\n"
"Mvaa 104,128,232\nMvaa 112,128,240\nMvaa 112,132,244\nMvaa 116,140,244\nMvaa 116,136,240\nMvaa 176,144,240\nMvaa 176,148,244\nD0 8,24,44,2048\nMvaa 180,156,244\nMvaa 180,152,240\n"
"Mvaa 188,152,248\nMvaa 188,156,252\nMvaa 184,148,252\nMvaa 184,144,248\nMvaa 124,136,248\nMvaa 124,140,252\nD0 9,24,44,3072\nMvaa 120,132,252\nMvaa 120,128,248\nWL 0\n"
"Maaa 32,192,224\nRa 128,10,32768\nMaaa 32,196,228\ns_add_u32 m0, s47, 16384\ns_add_u32 s44, s44, 0x40000\nMaaa 36,204,228\nRa 132,11,32768\nMaaa 36,200,224\nMaaa 96,208,224\n"
"Ra 136,10,34816\nMaaa 96,212,228\nD0 6,24,44,0\nMaaa 100,220,228\nRa 140,11,34816\nMaaa 100,216,224\nMaaa 108,216,232\nRa 144,10,40960\nMaaa 108,220,236\nMaaa 104,212,236\n"
"Ra 148,11,40960\nMaaa 104,208,232\nMaaa 44,200,232\nRa 152,10,43008\nMaaa 44,204,236\nD0 7,24,44,1024\nMaaa 40,196,236\nRa 156,11,43008\nMaaa 40,192,232\nMaaa 48,192,240\n"
"Maaa 48,196,244\nMaaa 52,204,244\nMaaa 52,200,240\nMaaa 112,208,240\nMaaa 112,212,244\nD0 8,24,44,2048\nMaaa 116,220,244\nMaaa 116,216,240\nMaaa 124,216,248\nMaaa 124,220,252\n"
"Maaa 120,212,252\nMaaa 120,208,248\nMaaa 60,200,248\nMaaa 60,204,252\nD0 9,24,44,3072\nMaaa 56,196,252\nMaaa 56,192,248\ns_waitcnt vmcnt(19) lgkmcnt(0)\ns_barrier\nMaaa 0,192,160\n"
"Ta 224,16,32768\nMaaa 0,196,164\nTa 226,16,34816\nMaaa 4,204,164\ns_add_u32 m0, s47, -49152\ns_add_u32 s48, s48, 128\nMaaa 4,200,160\nTa 228,16,49152\nMaaa 64,208,160\n"
"Ta 230,16,51200\nbuffer_load_dword v31, v24, s[24:27], s96 offen\ns_add_u32 s96, s96, 0x80000\nMaaa 64,212,164\nD0 2,20,48,0\nMaaa 68,220,164\nTa 232,17,32768\nMaaa 68,216,160\n"
"Ta 234,17,34816\nMaaa 76,216,168\nMaaa 76,220,172\nTa 236,17,49152\nMaaa 72,212,172\nTa 238,17,51200\nMaaa 72,208,168\nMaaa 12,200,168\nTa 240,18,32768\nMaaa 12,204,172\n"
"Ta 242,18,34816\nMaaa 8,196,172\nD0 3,20,48,1024\nMaaa 8,192,168\nTa 244,18,49152\nMaaa 16,192,176\nTa 246,18,51200\nMaaa 16,196,180\nMaaa 20,204,180\nTa 248,19,32768\n"
"Maaa 20,200,176\nTa 250,19,34816\nMaaa 80,208,176\nMaaa 80,212,180\nTa 252,19,49152\nMaaa 84,220,180\nTa 254,19,51200\nMaaa 84,216,176\nD0 4,20,48,2048\nMaaa 92,216,184\n"
"Maaa 92,220,188\nMaaa 88,212,188\nMaaa 88,208,184\nMaaa 28,200,184\nMaaa 28,204,188\nD0 5,20,48,3072\nMaaa 24,196,188\nMaaa 24,192,184\nWL 0\nMvaa 96,128,224\nTa 160,12,32768\n"
"Mvaa 96,132,228\nTa 162,12,34816\nMvaa 100,140,228\ns_add_u32 m0, s47, -32768\ns_add_u32 s45, s45, 128\nMvaa 100,136,224\nTa 164,12,49152\nMvaa 160,144,224\nTa 166,12,51200\n"
"Mvaa 160,148,228\nD0 2,20,45,0\nMvaa 164,156,228\nTa 168,13,32768\nMvaa 164,152,224\nTa 170,13,34816\nMvaa 172,152,232\nMvaa 172,156,236\nTa 172,13,49152\nMvaa 168,148,236\n"
"Ta 174,13,51200\nMvaa 168,144,232\nMvaa 108,136,232\nTa 176,14,32768\nMvaa 108,140,236\nTa 178,14,34816\nMvaa 104,132,236\nD0 3,20,45,1024\nMvaa 104,128,232\nTa 180,14,49152\n"
"Mvaa 112,128,240\nTa 182,14,51200\nMvaa 112,132,244\nMvaa 116,140,244\nTa 184,15,32768\nMvaa 116,136,240\nTa 186,15,34816\nMvaa 176,144,240\nMvaa 176,148,244\nTa 188,15,49152\n"
"Mvaa 180,156,244\nTa 190,15,51200\nMvaa 180,152,240\nD0 4,20,45,2048\nMvaa 188,152,248\nMvaa 188,156,252\nMvaa 184,148,252\nMvaa 184,144,248\nMvaa 124,136,248\nMvaa 124,140,252\n"
"D0 5,20,45,3072\nMvaa 120,132,252\nMvaa 120,128,248\ns_waitcnt vmcnt(17) lgkmcnt(0)\ns_barrier\nMvaa 64,128,160\nRa 192,10,49152\nMvaa 64,132,164\ns_add_u32 m0, s47, 32768\n"
"s_add_u32 s44, s44, 0x40000\nMvaa 68,140,164\nRa 196,11,49152\nMvaa 68,136,160\nMvaa 128,144,160\nRa 200,10,51200\nMvaa 128,148,164\nD0 6,24,44,0\nMvaa 132,156,164\n"
"Ra 204,11,51200\nMvaa 132,152,160\nMvaa 140,152,168\nRa 208,10,57344\nMvaa 140,156,172\ns_lshl_b32 s5, s73, 9\ns_add_u32 s5, s5, 0xc049400\nv_lshrrev_b32 v27, 1, v1\n"
"buffer_load_dwordx2 v[22:23], v27, s[16:19], s5 offen sc0 sc1\nMvaa 136,148,172\nRa 212,11,57344\nMvaa 136,144,168\nMvaa 76,136,168\nRa 216,10,59392\nMvaa 76,140,172\n"
"D0 7,24,44,1024\nMvaa 72,132,172\nRa 220,11,59392\nMvaa 72,128,168\nMvaa 80,128,176\nMvaa 80,132,180\nMvaa 84,140,180\nMvaa 84,136,176\nMvaa 144,144,176\nMvaa 144,148,180\n"
"D0 8,24,44,2048\nMvaa 148,156,180\nMvaa 148,152,176\nMvaa 156,152,184\nMvaa 156,156,188\nMvaa 152,148,188\nMvaa 152,144,184\nMvaa 92,136,184\nMvaa 92,140,188\nD0 9,24,44,3072\n"
"Mvaa 88,132,188\nMvaa 88,128,184\nWL 0\nMaaa 0,192,160\nRa 128,10,0\nMaaa 0,196,164\ns_add_u32 m0, s47, 49152\ns_add_u32 s44, s44, 0x40000\nMaaa 4,204,164\nRa 132,11,0\n"
"Maaa 4,200,160\nMaaa 64,208,160\nRa 136,10,2048\nMaaa 64,212,164\nD0 6,24,44,0\nMaaa 68,220,164\nRa 140,11,2048\nMaaa 68,216,160\nMaaa 76,216,168\nRa 144,10,8192\nMaaa 76,220,172\n"
"Maaa 72,212,172\nRa 148,11,8192\nMaaa 72,208,168\nMaaa 12,200,168\nRa 152,10,10240\nMaaa 12,204,172\nD0 7,24,44,1024\nMaaa 8,196,172\nRa 156,11,10240\nMaaa 8,192,168\n"
"Maaa 16,192,176\nMaaa 16,196,180\nMaaa 20,204,180\nMaaa 20,200,176\nMaaa 80,208,176\nMaaa 80,212,180\nD0 8,24,44,2048\nMaaa 84,220,180\nMaaa 84,216,176\nMaaa 92,216,184\n"
"Maaa 92,220,188\nMaaa 88,212,188\nMaaa 88,208,184\nMaaa 28,200,184\nMaaa 28,204,188\nD0 9,24,44,3072\nMaaa 24,196,188\nMaaa 24,192,184\ns_waitcnt vmcnt(18) lgkmcnt(0)\ns_barrier\n"
"Maaa 32,192,224\nTa 160,12,0\nMaaa 32,196,228\nTa 162,12,2048\nMaaa 36,204,228\ns_add_u32 m0, s47, -16384\ns_add_u32 s48, s48, 128\nMaaa 36,200,224\nTa 164,12,16384\n"
"Maaa 96,208,224\nTa 166,12,18432\nbuffer_load_dword v31, v24, s[24:27], s96 offen\ns_add_u32 s96, s96, 0x80000\nMaaa 96,212,228\nD0 2,20,48,0\nMaaa 100,220,228\nTa 168,13,0\n"
"Maaa 100,216,224\nTa 170,13,2048\nMaaa 108,216,232\nMaaa 108,220,236\nTa 172,13,16384\nMaaa 104,212,236\nTa 174,13,18432\nMaaa 104,208,232\nMaaa 44,200,232\nTa 176,14,0\n"
"Maaa 44,204,236\nTa 178,14,2048\nMaaa 40,196,236\nD0 3,20,48,1024\nMaaa 40,192,232\nTa 180,14,16384\nMaaa 48,192,240\nTa 182,14,18432\nMaaa 48,196,244\nMaaa 52,204,244\n"
"Ta 184,15,0\nMaaa 52,200,240\nTa 186,15,2048\nMaaa 112,208,240\nMaaa 112,212,244\nTa 188,15,16384\nMaaa 116,220,244\nTa 190,15,18432\nMaaa 116,216,240\nD0 4,20,48,2048\n"
"Maaa 124,216,248\nMaaa 124,220,252\nMaaa 120,212,252\nMaaa 120,208,248\nMaaa 60,200,248\nMaaa 60,204,252\nD0 5,20,48,3072\nMaaa 56,196,252\nMaaa 56,192,248\nWL 0\nMvaa 64,128,160\n"
"Ta 224,16,0\nWV 12\nv_cmp_ne_u32 vcc, s15, v23\ns_nop 0\ns_cbranch_vccnz .S410\n.S411:\nMvaa 64,132,164\nTa 226,16,2048\nMvaa 68,140,164\ns_add_u32 m0, s47, -65536\n"
"s_add_u32 s45, s45, 128\nMvaa 68,136,160\nTa 228,16,16384\nMvaa 128,144,160\nTa 230,16,18432\nMvaa 128,148,164\nD0 2,20,45,0\nMvaa 132,156,164\nTa 232,17,0\nMvaa 132,152,160\n"
"Ta 234,17,2048\nMvaa 140,152,168\nv_add_u32_dpp v22, v22, v22 row_ror:8 row_mask:0xf bank_mask:0xf\nMvaa 140,156,172\nTa 236,17,16384\nMvaa 136,148,172\nTa 238,17,18432\n"
"Mvaa 136,144,168\nv_add_u32_dpp v22, v22, v22 row_ror:4 row_mask:0xf bank_mask:0xf\nMvaa 76,136,168\nTa 240,18,0\nMvaa 76,140,172\nTa 242,18,2048\nMvaa 72,132,172\n"
"D0 3,20,45,1024\nMvaa 72,128,168\nTa 244,18,16384\nMvaa 80,128,176\nTa 246,18,18432\nMvaa 80,132,180\nv_add_u32_dpp v22, v22, v22 row_ror:2 row_mask:0xf bank_mask:0xf\n"
"Mvaa 84,140,180\nTa 248,19,0\nMvaa 84,136,176\nTa 250,19,2048\nMvaa 144,144,176\nv_add_u32_dpp v22, v22, v22 row_ror:1 row_mask:0xf bank_mask:0xf\nMvaa 144,148,180\n"
"Ta 252,19,16384\nMvaa 148,156,180\nTa 254,19,18432\n.W582:\ns_cbranch_execz .W586\nMvaa 148,152,176\nD0 4,20,45,2048\nMvaa 156,152,184\nv_mov_b32 v23, v22\nMvaa 156,156,188\n"
"Mvaa 152,148,188\nv_permlane16_swap_b32 v22, v23\nMvaa 152,144,184\nv_add_u32 v22, v22, v23\nMvaa 92,136,184\nv_mov_b32 v23, v22\nMvaa 92,140,188\nD0 5,20,45,3072\n"
"Mvaa 88,132,188\nv_permlane32_swap_b32 v22, v23\nMvaa 88,128,184\nWL 0\ns_barrier\nMvaa 96,128,224\nRa 192,10,16384\nMvaa 96,132,228\ns_add_u32 m0, s47, 0\n"
"s_add_u32 s44, s44, 0x40000\nMvaa 100,140,228\nRa 196,11,16384\nMvaa 100,136,224\nv_add_u32 v22, v22, v23\nMvaa 160,144,224\nRa 200,10,18432\nMvaa 160,148,228\nD0 6,24,44,0\n"
"Mvaa 164,156,228\nRa 204,11,18432\nMvaa 164,152,224\nv_readfirstlane_b32 s4, v22\nMvaa 172,152,232\nRa 208,10,24576\nMvaa 172,156,236\nMvaa 168,148,236\nRa 212,11,24576\n"
"s_cmp_gt_i32 s4, 0\ns_cselect_b32 s4, 1, 0\nMvaa 168,144,232\ns_xor_b32 s4, s4, s12\nMvaa 108,136,232\nRa 216,10,26624\nMvaa 108,140,236\nD0 7,24,44,1024\nMvaa 104,132,236\n"
"Ra 220,11,26624\nMvaa 104,128,232\ns_and_b32 s4, s4, 1\nMvaa 112,128,240\ns_lshl_b32 s4, s4, 13\nMvaa 112,132,244\ns_mul_i32 s72, s73, 25165824\nMvaa 116,140,244\n"
"s_add_u32 s72, s72, s4\nMvaa 116,136,240\ns_and_b32 s4, s12, 1\nMvaa 176,144,240\ns_lshl_b32 s4, s4, 21\nMvaa 176,148,244\nD0 8,24,44,2048\nMvaa 180,156,244\n"
"s_add_u32 s72, s72, s4\nMvaa 180,152,240\ns_lshl3_add_u32 s2, s13, s11\nMvaa 188,152,248\ns_lshl_b32 s2, s2, 11\nMvaa 188,156,252\ns_lshl_b32 s3, s14, 22\nMvaa 184,148,252\n"
"s_add_u32 s2, s2, s3\nMvaa 184,144,248\ns_add_u32 s2, s2, s72\nMvaa 124,136,248\ns_add_u32 s49, s2, 0xbe0000\nMvaa 124,140,252\nD0 9,24,44,3072\nMvaa 120,132,252\n"
"Mvaa 120,128,248\nWL 0\nMaaa 32,192,224\nRa 128,10,32768\ns_mov_b32 s4, s13\nMaaa 32,196,228\ns_add_u32 m0, s47, 16384\ns_add_u32 s44, s44, 0x40000\nMaaa 36,204,228\n"
"Ra 132,11,32768\nv_lshrrev_b32 v32, 2, v0\nMaaa 36,200,224\nv_mul_u32_u24 v32, 896, v32\nMaaa 96,208,224\nRa 136,10,34816\nv_and_b32 v33, 3, v0\nMaaa 96,212,228\nD0 6,24,44,0\n"
"Maaa 100,220,228\nRa 140,11,34816\nv_lshl_add_u32 v32, v33, 4, v32\nMaaa 100,216,224\ns_lshr_b32 s2, s11, 1\nMaaa 108,216,232\nRa 144,10,40960\ns_mul_i32 s3, s2, 448\n"
"Maaa 108,220,236\ns_and_b32 s5, s11, 1\nMaaa 104,212,236\nRa 148,11,40960\ns_lshl3_add_u32 s3, s5, s3\nMaaa 104,208,232\ns_add_u32 s3, s3, 131072\nMaaa 44,200,232\n"
"Ra 152,10,43008\nv_add_u32 v32, s3, v32\nMaaa 44,204,236\nD0 7,24,44,1024\nMaaa 40,196,236\nRa 156,11,43008\nTv 36,32,0\nMaaa 40,192,232\nTv 38,32,14336\nMaaa 48,192,240\n"
"Tv 40,32,64\nTv 42,32,14400\nMaaa 48,196,244\nTv 44,32,128\nMaaa 52,204,244\nTv 46,32,14464\nTv 48,32,192\nMaaa 52,200,240\nTv 50,32,14528\nMaaa 112,208,240\nTv 52,32,256\n"
"Tv 54,32,14592\nMaaa 112,212,244\nD0 8,24,44,2048\nMaaa 116,220,244\nTv 56,32,320\nTv 58,32,14656\nMaaa 116,216,240\nTv 60,32,384\nMaaa 124,216,248\nTv 62,32,14720\n"
"s_lshr_b32 s0, s4, 1\nMaaa 124,220,252\ns_mul_i32 s0, s0, 12\nMaaa 120,212,252\ns_lshl1_add_u32 s0, s5, s0\ns_and_b32 s6, s4, 1\nMaaa 120,208,248\ns_add_u32 s0, s0, s6\n"
"Maaa 60,200,248\ns_lshl3_add_u32 s1, s5, s4\ns_add_u32 s77, s1, 48\nMaaa 60,204,252\nD0 9,24,44,3072\nMaaa 56,196,252\ns_and_b32 s4, s12, 1\ns_mov_b32 s7, 0x6543210\n"
"Maaa 56,192,248\ns_waitcnt vmcnt(17) lgkmcnt(14)\ns_barrier\nMaaa 0,192,160\nTa 224,16,32768\ns_cmp_lg_u32 s4, s2\ns_cselect_b32 s6, s0, s1\ns_cselect_b32 s5, 4, 16\n"
"s_cselect_b32 s7, 0x6352410, s7\ns_cselect_b32 s3, 0, 229376\nMaaa 0,196,164\nTa 226,16,34816\nMaaa 4,204,164\ns_add_u32 m0, s47, -49152\ns_add_u32 s48, s48, 128\nMaaa 4,200,160\n"
"Ta 228,16,49152\nMaaa 64,208,160\nTa 230,16,51200\ns_add_u32 s3, s3, s72\nMaaa 64,212,164\nD0 2,20,48,0\nMaaa 68,220,164\nTa 232,17,32768\ns_add_u32 s3, s3, 0x1400000\n"
"Maaa 68,216,160\nTa 234,17,34816\nMaaa 76,216,168\nWL 14\ns_lshr_b32 s0, s6, 4\nMaaa 76,220,172\nTa 236,17,49152\nMaaa 72,212,172\nTa 238,17,51200\ns_lshl_b32 s0, s0, 2\n"
"Maaa 72,208,168\ns_lshr_b32 s0, s7, s0\nMaaa 12,200,168\nTa 240,18,32768\ns_and_b32 s0, s0, 7\nMaaa 12,204,172\nTa 242,18,34816\nMaaa 8,196,172\nD0 3,20,48,1024\n"
"s_lshl_b32 s0, s0, 5\nMaaa 8,192,168\nTa 244,18,49152\nMaaa 16,192,176\nTa 246,18,51200\ns_and_b32 s1, s6, 15\nMaaa 16,196,180\ns_add_u32 s0, s0, s1\nMaaa 20,204,180\n"
"Ta 248,19,32768\ns_and_b32 s1, s6, 8\nMaaa 20,200,176\nTa 250,19,34816\nMaaa 80,208,176\ns_add_u32 s0, s0, s1\ns_lshl_b32 s0, s0, 10\nMaaa 80,212,180\nTa 252,19,49152\n"
"Maaa 84,220,180\nTa 254,19,51200\ns_add_u32 s0, s0, s3\nMaaa 84,216,176\nD0 4,20,48,2048\nMaaa 92,216,184\nSv 36,1,16,0,0\ns_add_u32 s6, s6, s5\nMaaa 92,220,188\nMaaa 88,212,188\n"
"Maaa 88,208,184\nMaaa 28,200,184\nMaaa 28,204,188\nD0 5,20,48,3072\nMaaa 24,196,188\nMaaa 24,192,184\nWL 0\nMvaa 96,128,224\nTa 160,12,32768\ns_lshr_b32 s0, s6, 4\n"
"Mvaa 96,132,228\nTa 162,12,34816\nMvaa 100,140,228\ns_add_u32 m0, s47, -32768\ns_add_u32 s45, s45, 128\nMvaa 100,136,224\nTa 164,12,49152\nMvaa 160,144,224\nTa 166,12,51200\n"
"s_lshl_b32 s0, s0, 2\nMvaa 160,148,228\nD0 2,20,45,0\nMvaa 164,156,228\nTa 168,13,32768\ns_lshr_b32 s0, s7, s0\nMvaa 164,152,224\nTa 170,13,34816\nMvaa 172,152,232\n"
"s_and_b32 s0, s0, 7\ns_lshl_b32 s0, s0, 5\nMvaa 172,156,236\nTa 172,13,49152\nMvaa 168,148,236\nTa 174,13,51200\ns_and_b32 s1, s6, 15\nMvaa 168,144,232\ns_add_u32 s0, s0, s1\n"
"Mvaa 108,136,232\nTa 176,14,32768\ns_and_b32 s1, s6, 8\nMvaa 108,140,236\nTa 178,14,34816\nMvaa 104,132,236\nD0 3,20,45,1024\ns_add_u32 s0, s0, s1\nMvaa 104,128,232\n"
"Ta 180,14,49152\nMvaa 112,128,240\nTa 182,14,51200\ns_lshl_b32 s0, s0, 10\nMvaa 112,132,244\ns_add_u32 s0, s0, s3\nMvaa 116,140,244\nTa 184,15,32768\nSv 40,1,16,0,0\n"
"Mvaa 116,136,240\nTa 186,15,34816\nMvaa 176,144,240\ns_add_u32 s6, s6, s5\nMvaa 176,148,244\nTa 188,15,49152\nMvaa 180,156,244\nTa 190,15,51200\nMvaa 180,152,240\nD0 4,20,45,2048\n"
"Mvaa 188,152,248\nMvaa 188,156,252\nMvaa 184,148,252\nMvaa 184,144,248\nMvaa 124,136,248\ns_lshr_b32 s0, s6, 4\ns_lshl_b32 s0, s0, 2\nMvaa 124,140,252\nD0 5,20,45,3072\n"
"Mvaa 120,132,252\ns_lshr_b32 s0, s7, s0\ns_and_b32 s0, s0, 7\nMvaa 120,128,248\ns_waitcnt vmcnt(18) lgkmcnt(0)\ns_barrier\nMvaa 64,128,160\nRa 192,10,49152\ns_lshl_b32 s0, s0, 5\n"
"Mvaa 64,132,164\ns_add_u32 m0, s47, 32768\ns_add_u32 s44, s44, 0x40000\nMvaa 68,140,164\nRa 196,11,49152\ns_and_b32 s1, s6, 15\nMvaa 68,136,160\ns_add_u32 s0, s0, s1\n"
"Mvaa 128,144,160\nRa 200,10,51200\ns_and_b32 s1, s6, 8\nMvaa 128,148,164\nD0 6,24,44,0\nMvaa 132,156,164\nRa 204,11,51200\ns_add_u32 s0, s0, s1\nMvaa 132,152,160\n"
"s_lshl_b32 s0, s0, 10\nMvaa 140,152,168\nRa 208,10,57344\ns_add_u32 s0, s0, s3\nMvaa 140,156,172\nSv 44,1,16,0,0\nMvaa 136,148,172\nRa 212,11,57344\ns_add_u32 s6, s6, s5\n"
"Mvaa 136,144,168\nMvaa 76,136,168\nRa 216,10,59392\nMvaa 76,140,172\nD0 7,24,44,1024\nMvaa 72,132,172\nRa 220,11,59392\nMvaa 72,128,168\nMvaa 80,128,176\nMvaa 80,132,180\n"
"Mvaa 84,140,180\ns_mov_b32 s6, s77\ns_mov_b32 s5, 16\nMvaa 84,136,176\ns_lshr_b32 s0, s6, 4\nMvaa 144,144,176\ns_lshl_b32 s0, s0, 2\ns_lshr_b32 s0, s7, s0\nMvaa 144,148,180\n"
"D0 8,24,44,2048\nMvaa 148,156,180\ns_and_b32 s0, s0, 7\ns_lshl_b32 s0, s0, 5\nMvaa 148,152,176\ns_and_b32 s1, s6, 15\nMvaa 156,152,184\ns_add_u32 s0, s0, s1\ns_and_b32 s1, s6, 8\n"
"Mvaa 156,156,188\ns_add_u32 s0, s0, s1\nMvaa 152,148,188\ns_lshl_b32 s0, s0, 10\ns_add_u32 s0, s0, s3\nMvaa 152,144,184\nSv 48,1,16,0,0\nMvaa 92,136,184\ns_add_u32 s6, s6, s5\n"
"Mvaa 92,140,188\nD0 9,24,44,3072\nMvaa 88,132,188\nMvaa 88,128,184\nWL 0\nMaaa 0,192,160\nRa 128,10,0\nMaaa 0,196,164\ns_add_u32 m0, s47, 49152\ns_add_u32 s44, s44, 0x40000\n"
"Maaa 4,204,164\nRa 132,11,0\nMaaa 4,200,160\nMaaa 64,208,160\nRa 136,10,2048\ns_lshr_b32 s0, s6, 4\nMaaa 64,212,164\nD0 6,24,44,0\nMaaa 68,220,164\nRa 140,11,2048\n"
"s_lshl_b32 s0, s0, 2\nMaaa 68,216,160\ns_lshr_b32 s0, s7, s0\nMaaa 76,216,168\nRa 144,10,8192\ns_and_b32 s0, s0, 7\nMaaa 76,220,172\ns_lshl_b32 s0, s0, 5\nMaaa 72,212,172\n"
"Ra 148,11,8192\ns_and_b32 s1, s6, 15\nMaaa 72,208,168\ns_add_u32 s0, s0, s1\nMaaa 12,200,168\nRa 152,10,10240\ns_and_b32 s1, s6, 8\nMaaa 12,204,172\nD0 7,24,44,1024\n"
"Maaa 8,196,172\nRa 156,11,10240\ns_add_u32 s0, s0, s1\nMaaa 8,192,168\ns_lshl_b32 s0, s0, 10\nMaaa 16,192,176\ns_add_u32 s0, s0, s3\nSv 52,1,16,0,0\nMaaa 16,196,180\n"
"s_add_u32 s6, s6, s5\nMaaa 20,204,180\nMaaa 20,200,176\nMaaa 80,208,176\nMaaa 80,212,180\nD0 8,24,44,2048\nMaaa 84,220,180\nMaaa 84,216,176\nMaaa 92,216,184\nMaaa 92,220,188\n"
"s_lshr_b32 s0, s6, 4\nMaaa 88,212,188\ns_lshl_b32 s0, s0, 2\ns_lshr_b32 s0, s7, s0\nMaaa 88,208,184\ns_and_b32 s0, s0, 7\nMaaa 28,200,184\ns_lshl_b32 s0, s0, 5\n"
"s_and_b32 s1, s6, 15\nMaaa 28,204,188\nD0 9,24,44,3072\nMaaa 24,196,188\ns_add_u32 s0, s0, s1\ns_and_b32 s1, s6, 8\nMaaa 24,192,184\nWV 21\ns_barrier\nMaaa 32,192,224\n"
"Ta 160,12,0\ns_add_u32 s0, s0, s1\nMaaa 32,196,228\nTa 162,12,2048\nMaaa 36,204,228\ns_add_u32 m0, s47, -16384\ns_add_u32 s48, s48, 128\nMaaa 36,200,224\nTa 164,12,16384\n"
"Maaa 96,208,224\nTa 166,12,18432\ns_lshl_b32 s0, s0, 10\nMaaa 96,212,228\nD0 2,20,48,0\nMaaa 100,220,228\nTa 168,13,0\ns_add_u32 s0, s0, s3\nMaaa 100,216,224\nTa 170,13,2048\n"
"Maaa 108,216,232\nSv 56,1,16,0,0\ns_add_u32 s6, s6, s5\nMaaa 108,220,236\nTa 172,13,16384\nMaaa 104,212,236\nTa 174,13,18432\nMaaa 104,208,232\nMaaa 44,200,232\nTa 176,14,0\n"
"Maaa 44,204,236\nTa 178,14,2048\nMaaa 40,196,236\nD0 3,20,48,1024\nMaaa 40,192,232\nTa 180,14,16384\nMaaa 48,192,240\nTa 182,14,18432\ns_lshr_b32 s0, s6, 4\nMaaa 48,196,244\n"
"s_lshl_b32 s0, s0, 2\nMaaa 52,204,244\nTa 184,15,0\ns_lshr_b32 s0, s7, s0\nMaaa 52,200,240\nTa 186,15,2048\nMaaa 112,208,240\ns_and_b32 s0, s0, 7\ns_lshl_b32 s0, s0, 5\n"
"Maaa 112,212,244\nTa 188,15,16384\nMaaa 116,220,244\nTa 190,15,18432\ns_and_b32 s1, s6, 15\nMaaa 116,216,240\nD0 4,20,48,2048\nMaaa 124,216,248\ns_add_u32 s0, s0, s1\n"
"s_and_b32 s1, s6, 8\nMaaa 124,220,252\ns_add_u32 s0, s0, s1\nMaaa 120,212,252\ns_lshl_b32 s0, s0, 10\ns_add_u32 s0, s0, s3\nMaaa 120,208,248\nSv 60,1,16,0,0\nMaaa 60,200,248\n"
"s_add_u32 s6, s6, s5\nMaaa 60,204,252\nD0 5,20,48,3072\nMaaa 56,196,252\nMaaa 56,192,248\nWL 0\nMvaa 64,128,160\nTa 224,16,0\nMvaa 64,132,164\nTa 226,16,2048\n"
"s_add_u32 m0, s47, 65536\ns_add_u32 s46, s51, 0\nMvaa 68,140,164\nv_lshrrev_b32 v48, 4, v0\nMvaa 68,136,160\nTa 228,16,16384\nD0 6,32,46,0\nMvaa 128,144,160\nTa 230,16,18432\n"
"Mvaa 128,148,164\nv_and_b32 v49, 7, v0\nMvaa 132,156,164\nTa 232,17,0\nMvaa 132,152,160\nTa 234,17,2048\nMvaa 140,152,168\nv_xor_b32 v49, v49, v48\nD0 7,32,46,1024\n"
"Mvaa 140,156,172\nTa 236,17,16384\nMvaa 136,148,172\nTa 238,17,18432\nMvaa 136,144,168\nv_lshlrev_b32 v49, 4, v49\nMvaa 76,136,168\nTa 240,18,0\nMvaa 76,140,172\nTa 242,18,2048\n"
"D0 8,32,46,2048\nMvaa 72,132,172\nv_lshrrev_b32 v48, 3, v0\nMvaa 72,128,168\nTa 244,18,16384\nMvaa 80,128,176\nTa 246,18,18432\nMvaa 80,132,180\nv_mul_u32_u24 v48, 896, v48\n"
"Mvaa 84,140,180\nTa 248,19,0\nD0 9,32,46,3072\nMvaa 84,136,176\nTa 250,19,2048\nMvaa 144,144,176\nv_add_u32 v40, v48, v49\nMvaa 144,148,180\nTa 252,19,16384\n"
"s_add_u32 m0, s47, 81920\ns_add_u32 s46, s51, 262144\nMvaa 148,156,180\nTa 254,19,18432\nMvaa 148,152,176\nv_xor_b32 v49, 64, v40\nD0 6,32,46,0\nMvaa 156,152,184\n"
"v_add_u32 v41, 6144, v49\n.W583:\ns_cbranch_execz .W587\nMvaa 156,156,188\nv_add_u32 v42, 55296, v40\nMvaa 152,148,188\nv_add_u32 v43, 61440, v49\nMvaa 152,144,184\n"
"s_mov_b32 s2, s13\nMvaa 92,136,184\ns_lshl3_add_u32 s2, s2, s11\nD0 7,32,46,1024\nMvaa 92,140,188\ns_mul_i32 s52, s2, 14336\nMvaa 88,132,188\ns_lshr_b32 s2, s13, 2\n"
"s_bfe_u32 s3, s13, 0x10001\nMvaa 88,128,184\ns_waitcnt vmcnt(23) lgkmcnt(0)\ns_barrier\nMvaa 96,128,224\nRa 192,10,16384\ns_mul_i32 s3, s3, 17\nMvaa 96,132,228\n"
"s_add_u32 s2, s2, s3\nD0 8,32,46,2048\nMvaa 100,140,228\nRa 196,11,16384\ns_lshl2_add_u32 s3, s14, 0\nMvaa 100,136,224\ns_lshl_b32 s3, s3, 2\nMvaa 160,144,224\nRa 200,10,18432\n"
"s_lshr_b32 s3, 0xe420ca86, s3\nMvaa 160,148,228\ns_and_b32 s3, s3, 15\nMvaa 164,156,228\nRa 204,11,18432\ns_add_u32 s2, s2, s3\nD0 9,32,46,3072\nMvaa 164,152,224\n"
"s_cmp_eq_u32 s2, 32\ns_cselect_b32 s4, 16, s2\nMvaa 172,152,232\nRa 208,10,24576\ns_mul_i32 s75, s4, 65536\nMvaa 172,156,236\ns_lshr_b32 s2, s13, 2\ns_add_u32 m0, s47, -65536\n"
"s_add_u32 s46, s52, 0\nMvaa 168,148,236\nRa 212,11,24576\ns_bfe_u32 s3, s13, 0x10001\nMvaa 168,144,232\ns_mul_i32 s3, s3, 17\nD0 40,28,46,0\nMvaa 108,136,232\nRa 216,10,26624\n"
"s_add_u32 s2, s2, s3\nMvaa 108,140,236\ns_lshl2_add_u32 s3, s14, 1\nMvaa 104,132,236\nRa 220,11,26624\ns_lshl_b32 s3, s3, 2\nMvaa 104,128,232\ns_lshr_b32 s3, 0xe420ca86, s3\n"
"Mvaa 112,128,240\ns_and_b32 s3, s3, 15\ns_add_u32 s2, s2, s3\nD0 41,28,46,1024\nMvaa 112,132,244\ns_cmp_eq_u32 s2, 32\ns_cselect_b32 s4, 16, s2\nMvaa 116,140,244\n"
"s_mul_i32 s76, s4, 65536\ns_lshr_b32 s2, s13, 2\nMvaa 116,136,240\ns_bfe_u32 s3, s13, 0x10001\nMvaa 176,144,240\ns_mul_i32 s3, s3, 17\ns_add_u32 s2, s2, s3\nMvaa 176,148,244\n"
"s_lshl2_add_u32 s3, s14, 2\nD0 42,28,46,2048\nMvaa 180,156,244\ns_lshl_b32 s3, s3, 2\ns_lshr_b32 s3, 0xe420ca86, s3\nMvaa 180,152,240\ns_and_b32 s3, s3, 15\nMvaa 188,152,248\n"
"s_add_u32 s2, s2, s3\nMvaa 188,156,252\ns_cmp_eq_u32 s2, 32\ns_cselect_b32 s4, 16, s2\nMvaa 184,148,252\ns_mul_i32 s77, s4, 65536\ns_lshr_b32 s2, s13, 2\nD0 43,28,46,3072\n"
"Mvaa 184,144,248\ns_bfe_u32 s3, s13, 0x10001\nMvaa 124,136,248\ns_mul_i32 s3, s3, 17\ns_add_u32 s2, s2, s3\nMvaa 124,140,252\ns_lshl2_add_u32 s3, s14, 3\nMvaa 120,132,252\n"
"s_lshl_b32 s3, s3, 2\ns_lshr_b32 s3, 0xe420ca86, s3\nMvaa 120,128,248\nWL 0\nMaaa 32,192,224\nRa 128,10,32768\ns_and_b32 s3, s3, 15\ns_add_u32 m0, s47, 0\n"
"s_add_u32 s46, s51, 524288\nD0 6,32,46,0\nMaaa 32,196,228\ns_add_u32 s2, s2, s3\ns_cmp_eq_u32 s2, 32\ns_cselect_b32 s4, 16, s2\nMaaa 36,204,228\nRa 132,11,32768\n"
"s_mul_i32 s78, s4, 65536\nMaaa 36,200,224\ns_lshr_b32 s3, s11, 1\nMaaa 96,208,224\nRa 136,10,34816\ns_lshl_b32 s3, s3, 15\nMaaa 96,212,228\ns_add_u32 s2, s72, s3\nD0 7,32,46,1024\n"
"Maaa 100,220,228\nRa 140,11,34816\ns_and_b32 s3, s11, 1\nMaaa 100,216,224\ns_lshl_b32 s3, s3, 14\nMaaa 108,216,232\nRa 144,10,40960\ns_add_u32 s2, s2, s3\nMaaa 108,220,236\n"
"s_add_u32 s2, s2, 0x400000\nMaaa 104,212,236\nRa 148,11,40960\ns_mov_b32 s55, 4096\nD0 8,32,46,2048\nMaaa 104,208,232\ns_add_u32 s75, s75, s2\nMaaa 44,200,232\nRa 152,10,43008\n"
"s_add_u32 s76, s76, s2\nMaaa 44,204,236\ns_add_u32 s77, s77, s2\nMaaa 40,196,236\nRa 156,11,43008\ns_add_u32 s78, s78, s2\nMaaa 40,192,232\nv_add_u32 v32, 0x10000, v12\n"
"D0 9,32,46,3072\nMaaa 48,192,240\nv_add_u32 v33, 0x10000, v13\nv_add_u32 v34, 0x10000, v14\nMaaa 48,196,244\nv_add_u32 v35, 0x10000, v15\nMaaa 52,204,244\n"
"v_add_u32 v36, 0x10000, v16\nv_add_u32 v37, 0x10000, v17\ns_add_u32 m0, s47, 16384\ns_add_u32 s46, s51, 786432\nMaaa 52,200,240\nv_add_u32 v38, 0x10000, v18\nMaaa 112,208,240\n"
"v_add_u32 v39, 0x10000, v19\ns_add_u32 s2, s11, 1\nD0 6,32,46,0\nMaaa 112,212,244\ns_lshr_b32 s2, s2, 1\nMaaa 116,220,244\ns_lshl_b32 s2, s2, 12\nv_add_u32 v44, s2, v10\n"
"Maaa 116,216,240\nv_add_u32 v45, s2, v11\nMaaa 124,216,248\nv_xor_b32 v46, 0x1000, v44\nv_xor_b32 v47, 0x1000, v45\nMaaa 124,220,252\ns_lshr_b32 s3, s11, 1\nD0 7,32,46,1024\n"
"Maaa 120,212,252\ns_lshl_b32 s3, s3, 13\ns_lshl_b32 s2, s11, 12\nMaaa 120,208,248\ns_add_u32 s2, s2, s3\nMaaa 60,200,248\ns_add_u32 s2, s2, s72\ns_lshl_b32 s3, s11, 12\n"
"Maaa 60,204,252\ns_add_u32 s89, s3, 0x10000\nMaaa 56,196,252\ns_mov_b32 s88, 0xffff8000\ns_lshl_b32 s4, s13, 1\nD0 8,32,46,2048\nMaaa 56,192,248\ns_waitcnt vmcnt(25) lgkmcnt(0)\n"
"s_barrier\nMaaa 0,192,160\nTa 224,16,32768\ns_sub_u32 s79, 15, s4\nMaaa 0,196,164\nTa 226,16,34816\nMaaa 4,204,164\ns_add_u32 s96, s2, 0x1310000\ns_mov_b32 s63, 0x53800000\n"
"Maaa 4,200,160\nTa 228,16,49152\nD0 9,32,46,3072\nMaaa 64,208,160\nTa 230,16,51200\ns_mov_b32 s64, 0x2b800000\nMaaa 64,212,164\ns_sub_u32 s98, s4, 1\nMaaa 68,220,164\n"
"Ta 232,17,32768\ns_lshl_b32 s5, s13, 5\ns_add_u32 m0, s47, -32768\ns_add_u32 s46, s52, 128\nMaaa 68,216,160\nTa 234,17,34816\nMaaa 76,216,168\ns_add_u32 s5, s5, s11\n"
"s_lshl_b32 s65, s5, 9\nD0 40,28,46,0\nMaaa 76,220,172\nTa 236,17,49152\nMaaa 72,212,172\nTa 238,17,51200\ns_add_u32 s65, s65, s72\nMaaa 72,208,168\ns_add_u32 s65, s65, 0x1580000\n"
"Maaa 12,200,168\nTa 240,18,32768\ns_and_b32 s66, s72, 0x2000\nMaaa 12,204,172\nTa 242,18,34816\nD0 41,28,46,1024\nMaaa 8,196,172\ns_add_u32 s66, s66, s56\n"
"s_add_u32 s66, s66, 0x100000\nMaaa 8,192,168\nTa 244,18,49152\nMaaa 16,192,176\nTa 246,18,51200\ns_and_b32 s5, s12, 1\nMaaa 16,196,180\ns_mul_i32 s58, s5, 0x202000\n"
"Maaa 20,204,180\nTa 248,19,32768\ns_xor_b32 s58, s58, s72\nD0 42,28,46,2048\nMaaa 20,200,176\nTa 250,19,34816\nMaaa 80,208,176\ns_add_u32 s58, s58, 0x15c0000\n"
"s_lshl_b32 s5, s5, 8\nMaaa 80,212,180\nTa 252,19,49152\nMaaa 84,220,180\nTa 254,19,51200\ns_add_u32 s61, s58, s5\nMaaa 84,216,176\ns_lshl4_add_u32 s60, s13, s61\nD0 43,28,46,3072\n"
"Maaa 92,216,184\ns_add_u32 s54, s60, 128\ns_xor_b32 s60, s54, 0x100\nMaaa 92,220,188\ns_cmp_eq_u32 s11, 0\ns_cselect_b32 s54, s54, 0x40000000\nMaaa 88,212,188\ns_getpc_b64 s[8:9]\n"
".PC423:\ns_add_u32 s8, s8, .HD409-.PC423\ns_addc_u32 s9, s9, 0\ns_add_u32 m0, s47, -49152\ns_add_u32 s46, s52, 256\nMaaa 88,208,184\ns_mov_b32 s97, 0xff800000\nMaaa 28,200,184\n"
"s_mov_b32 s90, 0\nD0 40,28,46,0\nMaaa 28,204,188\ns_cmp_lt_u32 s11, 2\ns_cselect_b32 s92, 2, 0\nMaaa 24,196,188\ns_mul_i32 s4, s79, 65536\ns_sub_u32 s4, s2, s4\nMaaa 24,192,184\n"
"WL 0\nMvaa 96,128,224\nTa 160,12,32768\ns_add_u32 s94, s4, 0x20000\nMvaa 96,132,228\nTa 162,12,34816\nD0 41,28,46,1024\nMvaa 100,140,228\ns_add_u32 s95, s4, 0x410000\n"
"s_add_u32 s86, s2, 0x130000\nMvaa 100,136,224\nTa 164,12,49152\nMvaa 108,136,232\nTa 166,12,51200\ns_add_u32 s87, s2, 0x520000\nMvaa 108,140,236\ns_sub_u32 s99, s79, 2\n"
"s_cmp_eq_u32 s13, 7\ns_cselect_b32 s93, 1.0, 0\nMvaa 104,132,236\nTa 168,13,32768\ns_mov_b32 s36, s68\nD0 42,28,46,2048\nMvaa 104,128,232\nTa 170,13,34816\nMvaa 112,128,240\n"
"s_and_b32 s37, s69, 0xffff\ns_mov_b32 s38, 0x1c0000\nMvaa 112,132,244\nTa 172,13,49152\nCV 208,96,97\nMvaa 116,140,244\nTa 174,13,51200\ns_mov_b32 s39, 0x20000\nMvaa 116,136,240\n"
"s_and_b32 s2, s74, 15\nD0 43,28,46,3072\nMvaa 124,136,248\nTa 176,14,32768\ns_min_u32 s2, s2, 13\nMvaa 124,140,252\nTa 178,14,34816\nMvaa 120,132,252\ns_sub_u32 s2, s2, 0\n"
"s_mul_i32 s3, s73, 14\nMvaa 120,128,248\nTa 180,14,49152\nCV 209,98,99\nMvaa 160,144,224\nTa 182,14,51200\ns_add_u32 s2, s2, s3\nMvaa 160,148,228\ns_lshl2_add_u32 s2, s2, s11\n"
"Mvaa 164,156,228\nTa 184,15,32768\ns_mul_i32 s3, s2, 4682\nMvaa 164,152,224\nTa 186,15,34816\nCV 210,100,101\nMvaa 172,152,232\ns_lshr_b32 s3, s3, 16\ns_mul_i32 s22, s3, 14\n"
"Mvaa 172,156,236\nTa 188,15,49152\nCV 211,102,103\nMvaa 168,148,236\nTa 190,15,51200\ns_sub_u32 s22, s2, s22\nMvaa 168,144,232\ns_and_b32 s21, s3, 28\nMvaa 176,144,240\n"
"s_add_u32 s21, s21, s3\ns_lshl_b32 s21, s21, 4\nMvaa 176,148,244\ns_bfe_u32 s41, s3, 0x30002\ns_cmp_gt_u32 s22, 6\ns_cselect_b32 s2, 1179648, 0\ns_cselect_b32 s1, 16, 0\n"
"Mvaa 180,156,244\ns_add_u32 s41, s41, s1\ns_lshl4_add_u32 s41, s41, 0xc048480\nMvaa 180,152,240\ns_and_b32 s1, s3, 28\nCV 212,104,105\nMvaa 188,152,248\ns_add_u32 s3, s3, s1\n"
"s_lshl_b32 s3, s3, 11\nMvaa 188,156,252\ns_add_u32 s2, s2, s3\nMvaa 184,148,252\ns_lshl_b32 s3, s22, 17\ns_add_u32 s2, s2, s3\nMvaa 184,144,248\ns_waitcnt vmcnt(16) lgkmcnt(0)\n"
"s_barrier\nMvaa 64,128,160\nRa 192,10,49152\ns_add_u32 s20, s2, 0x10e0000\ns_add_u32 m0, s47, 32768\ns_add_u32 s46, s51, 1048576\nMvaa 64,132,164\ns_lshl_b32 s2, s22, 7\n"
"Mvaa 68,140,164\nRa 196,11,49152\ns_add_u32 s40, s2, 0xc04b800\nD0 6,32,46,0\nMvaa 68,136,160\ns_mul_i32 s2, s21, 896\nCV 213,106,107\nMvaa 76,136,168\nRa 200,10,51200\n"
"s_lshl_b32 s3, s22, 6\nMvaa 76,140,172\ns_add_u32 s22, s2, s3\nCV 214,108,109\nMvaa 72,132,172\nRa 204,11,51200\ns_add_u32 s23, s22, 57344\nMvaa 72,128,168\nD0 7,32,46,1024\n"
"Mvaa 80,128,176\nRa 208,10,57344\nCV 215,110,111\nMvaa 80,132,180\nCV 216,112,113\nCV 217,114,115\nMvaa 84,140,180\nRa 212,11,57344\nMvaa 84,136,176\nCV 218,116,117\n"
"CV 219,118,119\nMvaa 92,136,184\nRa 216,10,59392\nD0 8,32,46,2048\ns_lshl3_add_u32 s5, s12, s13\ns_lshl2_add_u32 s5, s5, s11\ns_lshl2_add_u32 s5, s5, 0xc04a400\n"
"buffer_load_dword v27, off, s[16:19], s5 sc0 sc1\nMvaa 92,140,188\nCV 220,120,121\nMvaa 88,132,188\nRa 220,11,59392\nCV 221,122,123\nMvaa 88,128,184\nCV 222,124,125\n"
"CV 223,126,127\nMvaa 128,144,160\nCV 192,64,65\nCV 193,66,67\nMvaa 128,148,164\nD0 9,32,46,3072\nMvaa 132,156,164\nCV 194,68,69\nCV 195,70,71\nMvaa 132,152,160\nCV 196,72,73\n"
"CV 197,74,75\nMvaa 140,152,168\ns_add_u32 m0, s47, 49152\ns_add_u32 s46, s51, 1310720\nMvaa 140,156,172\nCV 198,76,77\nCV 199,78,79\nMvaa 136,148,172\nD0 6,32,46,0\nCV 200,80,81\n"
"Mvaa 136,144,168\nCV 201,82,83\nMvaa 144,144,176\nCV 202,84,85\nCV 203,86,87\nMvaa 144,148,180\nCV 204,88,89\nCV 205,90,91\nMvaa 148,156,180\nCV 206,92,93\nMvaa 148,152,176\n"
"D0 7,32,46,1024\nCV 207,94,95\nMvaa 156,152,184\nCV 240,160,161\nMvaa 156,156,188\nCV 241,162,163\nSv 192,1,16,75,0\nMvaa 152,148,188\nCV 242,164,165\nMvaa 152,144,184\nWL 0\n"
"Maaa 0,192,160\nD0 8,32,46,2048\nRa 128,44,0\nMaaa 0,196,164\nCV 243,166,167\nMaaa 4,204,164\nRa 132,45,0\nMaaa 4,200,160\nCV 244,168,169\nMaaa 12,200,168\nRa 136,44,2048\n"
"Maaa 12,204,172\nD0 9,32,46,3072\nMaaa 8,196,172\nRa 140,45,2048\nSv 196,1,16,75,1024\nMaaa 8,192,168\nCV 245,170,171\nMaaa 16,192,176\nRa 144,46,0\nMaaa 16,196,180\n"
"CV 246,172,173\nMaaa 20,204,180\nRa 148,47,0\nMaaa 20,200,176\nCV 247,174,175\nMaaa 28,200,184\nRa 152,46,2048\nMaaa 28,204,188\nCV 248,176,177\nMaaa 24,196,188\nRa 156,47,2048\n"
"Maaa 24,192,184\nCV 249,178,179\nSv 200,1,16,75,2048\nMaaa 64,208,160\nCV 250,180,181\nCV 251,182,183\nMaaa 64,212,164\nCV 252,184,185\nMaaa 68,220,164\nCV 253,186,187\n"
"Maaa 68,216,160\nCV 254,188,189\nMaaa 76,216,168\nCV 255,190,191\nMaaa 76,220,172\nCV 224,128,129\nMaaa 72,212,172\nCV 225,130,131\nMaaa 72,208,168\nCV 226,132,133\n"
"Maaa 80,208,176\nCV 227,134,135\nSv 204,1,16,75,3072\nMaaa 80,212,180\nCV 228,136,137\nMaaa 84,220,180\nCV 229,138,139\nMaaa 84,216,176\nCV 230,140,141\nMaaa 92,216,184\n"
"CV 231,142,143\nMaaa 92,220,188\nCV 232,144,145\nMaaa 88,212,188\nCV 233,146,147\nMaaa 88,208,184\ns_barrier\nMaaa 32,192,224\ns_add_u32 m0, s47, -16384\ns_add_u32 s46, s52, 384\n"
"Ta 160,32,0\nMaaa 32,196,228\nTa 162,32,2048\nMaaa 36,204,228\nD0 40,28,46,0\ns_add_u32 s75, s75, s55\nSv 208,1,16,75,0\nMaaa 36,200,224\nTa 164,32,16384\nMaaa 44,200,232\n"
"Ta 166,32,18432\nMaaa 44,204,236\nCV 234,148,149\nMaaa 40,196,236\nTa 168,33,0\nMaaa 40,192,232\nD0 41,28,46,1024\nTa 170,33,2048\nMaaa 48,192,240\nCV 235,150,151\n"
"Maaa 48,196,244\n.W584:\ns_cbranch_execz .W588\nTa 172,33,16384\nMaaa 52,204,244\nTa 174,33,18432\nMaaa 52,200,240\nCV 236,152,153\nSv 212,1,16,75,1024\nMaaa 60,200,248\n"
"D0 42,28,46,2048\nTa 176,34,0\nMaaa 60,204,252\nTa 178,34,2048\nMaaa 56,196,252\nCV 237,154,155\nMaaa 56,192,248\nTa 180,34,16384\nMaaa 96,208,224\nTa 182,34,18432\n"
"CV 238,156,157\nMaaa 96,212,228\nD0 43,28,46,3072\nMaaa 100,220,228\nTa 184,35,0\nMaaa 100,216,224\nTa 186,35,2048\nMaaa 108,216,232\nCV 239,158,159\nSv 216,1,16,75,2048\n"
"Maaa 108,220,236\nTa 188,35,16384\nMaaa 104,212,236\nTa 190,35,18432\nMaaa 104,208,232\nMaaa 112,208,240\nMaaa 112,212,244\nMaaa 116,220,244\nMaaa 116,216,240\nMaaa 124,216,248\n"
"Maaa 124,220,252\nSv 220,1,16,75,3072\nMaaa 120,212,252\nMaaa 120,208,248\nWL 0\nZvaa 64,160,128\nTa 224,36,0\nAR 60,0\nMvaa 64,164,132\nTa 226,36,2048\nZvaa 96,164,140\nAR 61,1\n"
"Mvaa 96,160,136\nTa 228,36,16384\nCV 192,60,61\nZvaa 128,160,144\nTa 230,36,18432\nMvaa 128,164,148\nAR 60,2\nAR 61,3\nZvaa 160,164,156\nTa 232,37,0\nSv 224,1,16,76,0\n"
"Mvaa 160,160,152\nTa 234,37,2048\nCV 193,60,61\nZvaa 164,168,152\nAR 60,4\nMvaa 164,172,156\nTa 236,37,16384\nZvaa 132,172,148\nTa 238,37,18432\nAR 61,5\nMvaa 132,168,144\n"
"CV 194,60,61\nZvaa 100,168,136\nTa 240,38,0\nAR 60,6\nMvaa 100,172,140\nTa 242,38,2048\nZvaa 68,172,132\nSv 228,1,16,76,1024\nAR 61,7\nMvaa 68,168,128\nTa 244,38,16384\n"
"Zvaa 72,176,128\nTa 246,38,18432\nCV 195,60,61\nMvaa 72,180,132\nAR 60,8\nZvaa 104,180,140\nTa 248,39,0\nMvaa 104,176,136\nTa 250,39,2048\nAR 61,9\nZvaa 136,176,144\nCV 196,60,61\n"
"Mvaa 136,180,148\nTa 252,39,16384\nAR 60,10\nZvaa 168,180,156\nTa 254,39,18432\nSv 232,1,16,76,2048\nMvaa 168,176,152\nAR 61,11\nCV 197,60,61\nZvaa 172,184,152\nAR 60,12\n"
"Mvaa 172,188,156\nAR 61,13\nZvaa 140,188,148\nCV 198,60,61\nAR 60,14\nMvaa 140,184,144\nAR 61,15\nZvaa 108,184,136\nCV 199,60,61\nAR 60,16\nMvaa 108,188,140\nSv 236,1,16,76,3072\n"
"Zvaa 76,188,132\nAR 61,17\nCV 200,60,61\nMvaa 76,184,128\ns_waitcnt vmcnt(29) lgkmcnt(0)\ns_barrier\nZvaa 80,224,128\nRa 192,44,32768\nAR 60,18\nMvaa 80,228,132\nRa 196,45,32768\n"
"s_add_u32 m0, s47, 65536\ns_add_u32 s46, s51, 1572864\nD0 6,32,46,0\nZvaa 112,228,140\nRa 200,44,34816\nMvaa 112,224,136\nRa 204,45,34816\nAR 61,19\nZvaa 144,224,144\n"
"Ra 208,46,32768\nMvaa 144,228,148\nRa 212,47,32768\nCV 201,60,61\nD0 7,32,46,1024\nZvaa 176,228,156\nRa 216,46,34816\nMvaa 176,224,152\nRa 220,47,34816\nAR 60,20\n"
"Zvaa 180,232,152\nTa 160,12,0\nMvaa 180,236,156\nTa 162,12,2048\nD0 8,32,46,2048\nZvaa 148,236,148\nTa 164,12,16384\nAR 61,21\nMvaa 148,232,144\nTa 166,12,18432\nZvaa 116,232,136\n"
"Ta 168,13,0\nCV 202,60,61\nMvaa 116,236,140\nTa 170,13,2048\nD0 9,32,46,3072\nZvaa 84,236,132\nTa 172,13,16384\nAR 60,22\nMvaa 84,232,128\nTa 174,13,18432\n"
"s_add_u32 m0, s47, 81920\ns_add_u32 s46, s51, 1835008\nZvaa 88,240,128\nTa 176,14,0\nAR 61,23\nMvaa 88,244,132\nTa 178,14,2048\nD0 6,32,46,0\nZvaa 120,244,140\nTa 180,14,16384\n"
"Mvaa 120,240,136\nTa 182,14,18432\nCV 203,60,61\nZvaa 152,240,144\nTa 184,15,0\nMvaa 152,244,148\nTa 186,15,2048\nAR 60,24\nD0 7,32,46,1024\nZvaa 184,244,156\nTa 188,15,16384\n"
"Mvaa 184,240,152\nTa 190,15,18432\nAR 61,25\nZvaa 188,248,152\nCV 204,60,61\nMvaa 188,252,156\nAR 60,26\nD0 8,32,46,2048\nZvaa 156,252,148\nAR 61,27\nCV 205,60,61\n"
"Mvaa 156,248,144\nAR 60,28\nZvaa 124,248,136\nAR 61,29\nCV 206,60,61\nMvaa 124,252,140\nAR 60,30\nD0 9,32,46,3072\nZvaa 92,252,132\nAR 61,31\nCV 207,60,61\nMvaa 92,248,128\nWL 0\n"
"Mvaa 64,160,192\nTa 224,16,0\nAR 60,32\nMvaa 64,164,196\nTa 226,16,2048\ns_add_u32 m0, s47, -65536\ns_add_u32 s46, s52, 512\nD0 40,28,46,0\nMvaa 96,164,204\nAR 61,33\n"
"Mvaa 96,160,200\nTa 228,16,16384\nCV 208,60,61\nMvaa 128,160,208\nTa 230,16,18432\nMvaa 128,164,212\nAR 60,34\nAR 61,35\nD0 41,28,46,1024\nMvaa 160,164,220\nTa 232,17,0\n"
"Mvaa 160,160,216\nTa 234,17,2048\nCV 209,60,61\nMvaa 164,168,216\nAR 60,36\nMvaa 164,172,220\nTa 236,17,16384\nD0 42,28,46,2048\nMvaa 132,172,212\nTa 238,17,18432\nAR 61,37\n"
"Mvaa 132,168,208\nCV 210,60,61\nMvaa 100,168,200\nTa 240,18,0\nAR 60,38\nMvaa 100,172,204\nTa 242,18,2048\nD0 43,28,46,3072\nMvaa 68,172,196\ns_add_u32 s76, s76, s55\n"
"Sv 240,1,16,76,0\nMvaa 68,168,192\nTa 244,18,16384\nMvaa 72,176,192\nTa 246,18,18432\nAR 61,39\nMvaa 72,180,196\nCV 211,60,61\nMvaa 104,180,204\nTa 248,19,0\nMvaa 104,176,200\n"
"Ta 250,19,2048\nSv 244,1,16,76,1024\nMvaa 136,176,208\nAR 60,40\nMvaa 136,180,212\nTa 252,19,16384\nAR 61,41\nMvaa 168,180,220\nTa 254,19,18432\nMvaa 168,176,216\nCV 212,60,61\n"
"AR 60,42\nMvaa 172,184,216\nSv 248,1,16,76,2048\nMvaa 172,188,220\nAR 61,43\nMvaa 140,188,212\nCV 213,60,61\nAR 60,44\nMvaa 140,184,208\nAR 61,45\nMvaa 108,184,200\nCV 214,60,61\n"
"AR 60,46\nMvaa 108,188,204\nSv 252,1,16,76,3072\nMvaa 76,188,196\nAR 61,47\nCV 215,60,61\nMvaa 76,184,192\ns_waitcnt vmcnt(20) lgkmcnt(0)\ns_barrier\nMvaa 80,224,192\n"
"Ra 128,44,16384\nAR 60,48\nv_cmp_ne_u32 vcc, s15, v27\ns_nop 0\ns_cbranch_vccnz .S424\n.S425:\nMvaa 80,228,196\nRa 132,45,16384\n"
"buffer_store_dword v25, off, s[16:19], s54 offset:0 sc0 sc1\ns_add_u32 m0, s47, 0\ns_add_u32 s46, s51, 2097152\nD0 6,32,46,0\nMvaa 112,228,204\nRa 136,44,18432\nMvaa 112,224,200\n"
"Ra 140,45,18432\nAR 61,49\nMvaa 144,224,208\nRa 144,46,16384\nMvaa 144,228,212\nRa 148,47,16384\nCV 216,60,61\nD0 7,32,46,1024\nMvaa 176,228,220\nRa 152,46,18432\n"
"Mvaa 176,224,216\nRa 156,47,18432\nAR 60,50\nMvaa 180,232,216\nTa 160,12,32768\nMvaa 180,236,220\nTa 162,12,34816\nD0 8,32,46,2048\nMvaa 148,236,212\nTa 164,12,49152\nAR 61,51\n"
"Mvaa 148,232,208\nTa 166,12,51200\nMvaa 116,232,200\nTa 168,13,32768\nCV 217,60,61\nMvaa 116,236,204\nTa 170,13,34816\nD0 9,32,46,3072\nMvaa 84,236,196\nTa 172,13,49152\nAR 60,52\n"
"Mvaa 84,232,192\nTa 174,13,51200\ns_add_u32 m0, s47, 16384\ns_add_u32 s46, s51, 2359296\nMvaa 88,240,192\nTa 176,14,32768\nAR 61,53\nMvaa 88,244,196\nTa 178,14,34816\n"
"D0 6,32,46,0\nMvaa 120,244,204\nTa 180,14,49152\nMvaa 120,240,200\nTa 182,14,51200\nCV 218,60,61\nMvaa 152,240,208\nTa 184,15,32768\nMvaa 152,244,212\nTa 186,15,34816\nAR 60,54\n"
"D0 7,32,46,1024\nMvaa 184,244,220\nTa 188,15,49152\nMvaa 184,240,216\nTa 190,15,51200\nAR 61,55\nMvaa 188,248,216\nCV 219,60,61\nMvaa 188,252,220\nAR 60,56\nD0 8,32,46,2048\n"
"Mvaa 156,252,212\nAR 61,57\nCV 220,60,61\nMvaa 156,248,208\nAR 60,58\nMvaa 124,248,200\nAR 61,59\nCV 221,60,61\nMvaa 124,252,204\nAR 60,60\nD0 9,32,46,3072\nMvaa 92,252,196\n"
"AR 61,61\nCV 222,60,61\nMvaa 92,248,192\nWL 0\nMvaa 64,160,128\nTa 224,16,32768\nAR 60,62\nMvaa 64,164,132\nTa 226,16,34816\ns_add_u32 m0, s47, -32768\ns_add_u32 s46, s52, 640\n"
"D0 40,28,46,0\nMvaa 96,164,140\nAR 61,63\nMvaa 96,160,136\nTa 228,16,49152\nCV 223,60,61\nMvaa 128,160,144\nTa 230,16,51200\nMvaa 128,164,148\nD0 41,28,46,1024\nMvaa 160,164,156\n"
"Ta 232,17,32768\nAR 60,126\nMvaa 160,160,152\nTa 234,17,34816\nMvaa 164,168,152\nAR 61,127\nCV 60,60,61\nMvaa 164,172,156\nTa 236,17,49152\nD0 42,28,46,2048\nMvaa 132,172,148\n"
"Ta 238,17,51200\nMvaa 132,168,144\nAW 127,60\nAR 60,124\nMvaa 100,168,136\nTa 240,18,32768\nMvaa 100,172,140\nTa 242,18,34816\nD0 43,28,46,3072\nMvaa 68,172,132\nSv 192,1,16,77,0\n"
"AR 61,125\nMvaa 68,168,128\nTa 244,18,49152\nMvaa 72,176,128\nTa 246,18,51200\nCV 60,60,61\nMvaa 72,180,132\nAW 126,60\nMvaa 104,180,140\nTa 248,19,32768\nMvaa 104,176,136\n"
"Ta 250,19,34816\nSv 196,1,16,77,1024\nMvaa 136,176,144\nAR 60,122\nMvaa 136,180,148\nTa 252,19,49152\nMvaa 168,180,156\nTa 254,19,51200\nAR 61,123\nMvaa 168,176,152\nCV 60,60,61\n"
"Mvaa 172,184,152\nSv 200,1,16,77,2048\nAW 125,60\nMvaa 172,188,156\nAR 60,120\nMvaa 140,188,148\nAR 61,121\nMvaa 140,184,144\nCV 60,60,61\nAW 124,60\nMvaa 108,184,136\nAR 60,118\n"
"Mvaa 108,188,140\nSv 204,1,16,77,3072\nMvaa 76,188,132\nAR 61,119\nCV 60,60,61\nMvaa 76,184,128\ns_waitcnt vmcnt(17) lgkmcnt(0)\ns_barrier\nMvaa 80,224,128\nRa 192,44,49152\n"
"AW 123,60\nMvaa 80,228,132\nRa 196,45,49152\nbuffer_store_dword v25, off, s[16:19], s54 offset:8 sc0 sc1\ns_add_u32 m0, s47, 32768\ns_add_u32 s46, s51, 2621440\nD0 6,32,46,0\n"
"Mvaa 112,228,140\nRa 200,44,51200\nMvaa 112,224,136\nRa 204,45,51200\nAR 60,116\nMvaa 144,224,144\nRa 208,46,49152\nMvaa 144,228,148\nRa 212,47,49152\nD0 7,32,46,1024\n"
"Mvaa 176,228,156\nRa 216,46,51200\nAR 61,117\nMvaa 176,224,152\nRa 220,47,51200\nMvaa 180,232,152\nTa 160,32,0\nCV 60,60,61\nMvaa 180,236,156\nTa 162,32,2048\nD0 8,32,46,2048\n"
"Mvaa 148,236,148\nTa 164,32,16384\nMvaa 148,232,144\nTa 166,32,18432\nAW 122,60\nMvaa 116,232,136\nTa 168,33,0\nMvaa 116,236,140\nTa 170,33,2048\nD0 9,32,46,3072\nMvaa 84,236,132\n"
"Ta 172,33,16384\nAR 60,114\nMvaa 84,232,128\n.W585:\ns_cbranch_execz .W589\nTa 174,33,18432\ns_add_u32 m0, s47, 49152\ns_add_u32 s46, s51, 2883584\nMvaa 88,240,128\nTa 176,34,0\n"
"AR 61,115\nMvaa 88,244,132\nTa 178,34,2048\nD0 6,32,46,0\nMvaa 120,244,140\nTa 180,34,16384\nMvaa 120,240,136\nTa 182,34,18432\nCV 60,60,61\nMvaa 152,240,144\nTa 184,35,0\n"
"Mvaa 152,244,148\nTa 186,35,2048\nD0 7,32,46,1024\nMvaa 184,244,156\nTa 188,35,16384\nAW 121,60\nMvaa 184,240,152\nTa 190,35,18432\nMvaa 188,248,152\nAR 60,112\nAR 61,113\n"
"Mvaa 188,252,156\nD0 8,32,46,2048\nMvaa 156,252,148\nCV 60,60,61\nMvaa 156,248,144\nAW 120,60\nAR 60,110\nMvaa 124,248,136\nAR 61,111\nMvaa 124,252,140\nD0 9,32,46,3072\n"
"Mvaa 92,252,132\nCV 60,60,61\nAW 119,60\nMvaa 92,248,128\nWL 0\nMvaa 64,160,192\nTa 224,36,0\nAR 60,108\nMvaa 64,164,196\nTa 226,36,2048\ns_add_u32 m0, s47, -49152\n"
"s_add_u32 s46, s52, 768\nD0 40,28,46,0\nMvaa 96,164,204\nAR 61,109\nMvaa 96,160,200\nTa 228,36,16384\nCV 60,60,61\nMvaa 128,160,208\nTa 230,36,18432\nMvaa 128,164,212\n"
"D0 41,28,46,1024\nMvaa 160,164,220\nTa 232,37,0\nAW 118,60\nMvaa 160,160,216\nTa 234,37,2048\nMvaa 164,168,216\nAR 60,106\nAR 61,107\nMvaa 164,172,220\nTa 236,37,16384\n"
"D0 42,28,46,2048\nMvaa 132,172,212\nTa 238,37,18432\nMvaa 132,168,208\nCV 60,60,61\nAW 117,60\nMvaa 100,168,200\nTa 240,38,0\nMvaa 100,172,204\nTa 242,38,2048\nD0 43,28,46,3072\n"
"Mvaa 68,172,196\ns_add_u32 s77, s77, s55\nSv 208,1,16,77,0\nMvaa 68,168,192\nTa 244,38,16384\nMvaa 72,176,192\nTa 246,38,18432\nAR 60,104\nMvaa 72,180,196\nAR 61,105\n"
"Mvaa 104,180,204\nTa 248,39,0\nMvaa 104,176,200\nTa 250,39,2048\nSv 212,1,16,77,1024\nMvaa 136,176,208\nCV 60,60,61\nMvaa 136,180,212\nTa 252,39,16384\nMvaa 168,180,220\n"
"Ta 254,39,18432\nAW 116,60\nMvaa 168,176,216\nAR 60,102\nMvaa 172,184,216\nSv 216,1,16,77,2048\nAR 61,103\nMvaa 172,188,220\nCV 60,60,61\nMvaa 140,188,212\nAW 115,60\n"
"Mvaa 140,184,208\nAR 60,100\nAR 61,101\nMvaa 108,184,200\nCV 60,60,61\nMvaa 108,188,204\nSv 220,1,16,77,3072\nMvaa 76,188,196\nAW 114,60\nAR 60,98\nMvaa 76,184,192\n"
"s_waitcnt vmcnt(25) lgkmcnt(0)\ns_barrier\nMvaa 80,224,192\nRa 128,44,0\nv_and_b32 v27, 0x70, v1\nbuffer_load_dword v27, v27, s[16:19], s61 offen offset:128 sc0 sc1\n"
"Mvaa 80,228,196\nRa 132,45,0\ns_add_u32 m0, s47, 65536\ns_add_u32 s46, s51, 3145728\nD0 6,32,46,0\nMvaa 112,228,204\nRa 136,44,2048\nMvaa 112,224,200\nRa 140,45,2048\nAR 61,99\n"
"Mvaa 144,224,208\nRa 144,46,0\nMvaa 144,228,212\nRa 148,47,0\nD0 7,32,46,1024\nMvaa 176,228,220\nRa 152,46,2048\nCV 60,60,61\nMvaa 176,224,216\nRa 156,47,2048\nMvaa 180,232,216\n"
"Ta 160,12,0\nAW 113,60\nMvaa 180,236,220\nTa 162,12,2048\nD0 8,32,46,2048\nMvaa 148,236,212\nTa 164,12,16384\nMvaa 148,232,208\nTa 166,12,18432\nAR 60,96\nMvaa 116,232,200\n"
"Ta 168,13,0\nMvaa 116,236,204\nTa 170,13,2048\nD0 9,32,46,3072\nMvaa 84,236,196\nTa 172,13,16384\nAR 61,97\nMvaa 84,232,192\nTa 174,13,18432\ns_add_u32 m0, s47, 81920\n"
"s_add_u32 s46, s51, 3407872\nMvaa 88,240,192\nTa 176,14,0\nCV 60,60,61\nMvaa 88,244,196\nTa 178,14,2048\nD0 6,32,46,0\nMvaa 120,244,204\nTa 180,14,16384\nMvaa 120,240,200\n"
"Ta 182,14,18432\nAW 112,60\nMvaa 152,240,208\nTa 184,15,0\nMvaa 152,244,212\nTa 186,15,2048\nD0 7,32,46,1024\nMvaa 184,244,220\nTa 188,15,16384\nAR 60,94\nMvaa 184,240,216\n"
"Ta 190,15,18432\nMvaa 188,248,216\nAR 61,95\nCV 60,60,61\nMvaa 188,252,220\nD0 8,32,46,2048\nMvaa 156,252,212\nAW 111,60\nMvaa 156,248,208\nAR 60,92\nAR 61,93\nMvaa 124,248,200\n"
"CV 60,60,61\nMvaa 124,252,204\nD0 9,32,46,3072\nMvaa 92,252,196\nAW 110,60\nAR 60,90\nMvaa 92,248,192\nWL 0\nMvaa 64,160,128\nTa 224,16,0\nAR 61,91\nMvaa 64,164,132\n"
"Ta 226,16,2048\ns_add_u32 s6, s56, 0\nbuffer_load_dwordx4 a[0:3], v1, s[16:19], s6 offen offset:0 sc1\nMvaa 96,164,140\nCV 60,60,61\nMvaa 96,160,136\nTa 228,16,16384\nAW 109,60\n"
"Mvaa 128,160,144\nTa 230,16,18432\nMvaa 128,164,148\nbuffer_load_dwordx4 a[4:7], v1, s[16:19], s6 offen offset:1024 sc1\nMvaa 160,164,156\nTa 232,17,0\nAR 60,88\nMvaa 160,160,152\n"
"Ta 234,17,2048\nMvaa 164,168,152\nAR 61,89\nCV 60,60,61\nMvaa 164,172,156\nTa 236,17,16384\nbuffer_load_dwordx4 a[8:11], v1, s[16:19], s6 offen offset:2048 sc1\nMvaa 132,172,148\n"
"Ta 238,17,18432\nMvaa 132,168,144\nAW 108,60\nAR 60,86\nMvaa 100,168,136\nTa 240,18,0\nMvaa 100,172,140\nTa 242,18,2048\n"
"buffer_load_dwordx4 a[12:15], v1, s[16:19], s6 offen offset:3072 sc1\nMvaa 68,172,132\nAR 61,87\nCV 60,60,61\nMvaa 68,168,128\nTa 244,18,16384\nMvaa 72,176,128\nTa 246,18,18432\n"
"AW 107,60\nMvaa 72,180,132\nAR 60,84\nMvaa 104,180,140\nTa 248,19,0\nMvaa 104,176,136\nTa 250,19,2048\nAR 61,85\nMvaa 136,176,144\nCV 60,60,61\nMvaa 136,180,148\nTa 252,19,16384\n"
"Mvaa 168,180,156\nTa 254,19,18432\nAW 106,60\nMvaa 168,176,152\nAR 60,82\nMvaa 172,184,152\nAR 61,83\nCV 60,60,61\nMvaa 172,188,156\nAW 105,60\nMvaa 140,188,148\nAR 60,80\n"
"Mvaa 140,184,144\nAR 61,81\nCV 60,60,61\nMvaa 108,184,136\nAW 104,60\nMvaa 108,188,140\nAR 60,78\nMvaa 76,188,132\nAR 61,79\nCV 60,60,61\nMvaa 76,184,128\n"
"s_waitcnt vmcnt(12) lgkmcnt(0)\ns_barrier\nMvaa 80,224,128\nRa 192,44,32768\nv_cmp_ne_u32 vcc, s15, v27\ns_nop 0\ns_cbranch_vccnz .S427\n.S428:\ns_sub_u32 s45, s86, 131072\n"
"s_cmp_eq_u32 s79, 1\ns_cselect_b32 s86, s94, s86\ns_add_u32 m0, s47, -65536\ns_add_u32 s46, s45, 0x0\nMvaa 80,228,132\nRa 196,45,32768\nMvaa 112,228,140\nRa 200,44,34816\n"
"D1 1,16,46,0\nMvaa 112,224,136\nRa 204,45,34816\nAW 103,60\nMvaa 144,224,144\nRa 208,46,32768\nMvaa 144,228,148\nRa 212,47,32768\nMvaa 176,228,156\nRa 216,46,34816\n"
"D1 1,16,46,1024\nMvaa 176,224,152\nRa 220,47,34816\nMvaa 180,232,152\nTa 160,12,32768\nAR 60,76\nMvaa 180,236,156\nTa 162,12,34816\nMvaa 148,236,148\nTa 164,12,49152\n"
"D1 1,16,46,2048\nMvaa 148,232,144\nTa 166,12,51200\nAR 61,77\nMvaa 116,232,136\nTa 168,13,32768\nMvaa 116,236,140\nTa 170,13,34816\nMvaa 84,236,132\nTa 172,13,49152\n"
"D1 1,16,46,3072\nMvaa 84,232,128\nTa 174,13,51200\nMvaa 88,240,128\nTa 176,14,32768\ns_add_u32 s6, s56, 4096\nMvaa 88,244,132\nTa 178,14,34816\nMvaa 120,244,140\nTa 180,14,49152\n"
"buffer_load_dwordx4 a[16:19], v1, s[16:19], s6 offen offset:0 sc1\nMvaa 120,240,136\nTa 182,14,51200\nCV 60,60,61\nMvaa 152,240,144\nTa 184,15,32768\nMvaa 152,244,148\n"
"Ta 186,15,34816\nMvaa 184,244,156\nTa 188,15,49152\nbuffer_load_dwordx4 a[20:23], v1, s[16:19], s6 offen offset:1024 sc1\nMvaa 184,240,152\nTa 190,15,51200\nMvaa 188,248,152\n"
"AW 102,60\nAR 60,74\nMvaa 188,252,156\nAR 61,75\nMvaa 156,252,148\nbuffer_load_dwordx4 a[24:27], v1, s[16:19], s6 offen offset:2048 sc1\nMvaa 156,248,144\nCV 60,60,61\nAW 101,60\n"
"Mvaa 124,248,136\nAR 60,72\nMvaa 124,252,140\nAR 61,73\nMvaa 92,252,132\nbuffer_load_dwordx4 a[28:31], v1, s[16:19], s6 offen offset:3072 sc1\nCV 60,60,61\nMvaa 92,248,128\nWL 0\n"
"Mvaa 64,160,192\nTa 224,16,32768\ns_add_u32 s6, s56, 16384\nMvaa 64,164,196\nTa 226,16,34816\nMvaa 96,164,204\nbuffer_load_dwordx4 a[32:35], v1, s[16:19], s6 offen offset:0 sc1\n"
"Mvaa 96,160,200\nTa 228,16,49152\nAW 100,60\nMvaa 128,160,208\nTa 230,16,51200\nMvaa 128,164,212\nAR 60,70\nMvaa 160,164,220\nTa 232,17,32768\n"
"buffer_load_dwordx4 a[36:39], v1, s[16:19], s6 offen offset:1024 sc1\nMvaa 160,160,216\nTa 234,17,34816\nMvaa 164,168,216\nAR 61,71\nCV 60,60,61\nMvaa 164,172,220\n"
"Ta 236,17,49152\nMvaa 132,172,212\nTa 238,17,51200\nbuffer_load_dwordx4 a[40:43], v1, s[16:19], s6 offen offset:2048 sc1\nMvaa 132,168,208\nAW 99,60\nAR 60,68\nMvaa 100,168,200\n"
"Ta 240,18,32768\nMvaa 100,172,204\nTa 242,18,34816\nMvaa 68,172,196\nbuffer_load_dwordx4 a[44:47], v1, s[16:19], s6 offen offset:3072 sc1\nAR 61,69\nMvaa 68,168,192\n"
"Ta 244,18,49152\nMvaa 72,176,192\nTa 246,18,51200\nCV 60,60,61\nMvaa 72,180,196\nAW 98,60\nMvaa 104,180,204\nTa 248,19,32768\nMvaa 104,176,200\nTa 250,19,34816\nAR 60,66\n"
"Mvaa 136,176,208\nAR 61,67\nMvaa 136,180,212\nTa 252,19,49152\nMvaa 168,180,220\nTa 254,19,51200\nCV 60,60,61\nMvaa 168,176,216\nAW 97,60\nMvaa 172,184,216\nAR 60,64\nAR 61,65\n"
"Mvaa 172,188,220\nCV 60,60,61\nMvaa 140,188,212\nAW 96,60\nv_add_u32 v2, 0x0, v1\nMvaa 140,184,208\ns_lshr_b32 s2, s59, 1\nMvaa 108,184,200\nv_add_u32 v24, s2, v2\n"
"v_xor_b32 v26, 0x1000, v24\nMvaa 108,188,204\nv_lshrrev_b32 v20, 4, v0\nMvaa 76,188,196\nv_lshlrev_b32 v20, 2, v20\nv_and_b32 v0, 15, v0\nMvaa 76,184,192\n"
"s_waitcnt vmcnt(16) lgkmcnt(0)\ns_barrier\nMvaa 80,224,192\nRa 128,44,16384\nv_sub_u32 v0, v0, v20\nMvaa 80,228,196\nRa 132,45,16384\n"
"buffer_store_dword v25, off, s[16:19], s54 offset:12 sc0 sc1\ns_add_u32 s6, s56, 20480\nbuffer_load_dwordx4 a[48:51], v1, s[16:19], s6 offen offset:0 sc1\nMvaa 112,228,204\n"
"Ra 136,44,18432\ns_lshl_b32 s2, s11, 4\nMvaa 112,224,200\nRa 140,45,18432\nMvaa 144,224,208\nRa 144,46,16384\nv_add_u32 v3, s2, v0\nMvaa 144,228,212\nRa 148,47,16384\n"
"buffer_load_dwordx4 a[52:55], v1, s[16:19], s6 offen offset:1024 sc1\nMvaa 176,228,220\nRa 152,46,18432\nv_subrev_u32 v0, 32, v3\nMvaa 176,224,216\nRa 156,47,18432\n"
"Mvaa 180,232,216\nTa 160,32,0\ns_lshl_b32 s2, s11, 13\nMvaa 180,236,220\nTa 162,32,2048\nbuffer_load_dwordx4 a[56:59], v1, s[16:19], s6 offen offset:2048 sc1\nMvaa 148,236,212\n"
"Ta 164,32,16384\ns_add_u32 s2, s2, 0x20000\nMvaa 148,232,208\nTa 166,32,18432\nMvaa 116,232,200\nTa 168,33,0\nv_add_u32 v22, s2, v1\nMvaa 116,236,204\nTa 170,33,2048\n"
"buffer_load_dwordx4 a[60:63], v1, s[16:19], s6 offen offset:3072 sc1\nMvaa 84,236,196\nTa 172,33,16384\nv_xor_b32 v23, 0x2000, v22\nMvaa 84,232,192\nTa 174,33,18432\n"
"s_add_u32 m0, s47, -32768\ns_add_u32 s46, s45, 0x8000\nMvaa 88,240,192\nTa 176,34,0\nv_mov_b32 v4, 0\nMvaa 88,244,196\nTa 178,34,2048\nD1 1,16,46,0\nMvaa 120,244,204\n"
"Ta 180,34,16384\nv_mov_b32 v5, 0\nMvaa 120,240,200\nTa 182,34,18432\nMvaa 152,240,208\nTa 184,35,0\nv_mov_b32 v6, 0\nMvaa 152,244,212\nTa 186,35,2048\nD1 1,16,46,1024\n"
"Mvaa 184,244,220\nTa 188,35,16384\nv_mov_b32 v7, 0\nMvaa 184,240,216\nTa 190,35,18432\n.W586:\ns_cbranch_execz .W590\nMvaa 188,248,216\nv_mov_b32 v8, 0\nv_mov_b32 v9, 0\n"
"Mvaa 188,252,220\nD1 1,16,46,2048\nMvaa 156,252,212\nv_mov_b32 v10, 0\nv_mov_b32 v11, 0\nMvaa 156,248,208\nv_mov_b32 v12, 0\nMvaa 124,248,200\nv_mov_b32 v13, 0\nv_mov_b32 v14, 0\n"
"Mvaa 124,252,204\nD1 1,16,46,3072\nMvaa 92,252,196\nv_mov_b32 v15, 0\nv_mov_b32 v16, 0\nMvaa 92,248,192\nWL 0\nMvaa 64,160,128\nTa 224,36,0\nv_mov_b32 v17, 0\nMvaa 64,164,132\n"
"Ta 226,36,2048\ns_add_u32 m0, s47, -16384\ns_add_u32 s46, s45, 0x400000\nD1 1,16,46,0\nMvaa 68,172,132\nv_mov_b32 v18, 0\nv_mov_b32 v19, 0\nMvaa 68,168,128\nTa 228,36,16384\n"
"Mvaa 72,176,128\nTa 230,36,18432\nMvaa 72,180,132\nD1 1,16,46,1024\nMvaa 76,188,132\nTa 232,37,0\nMvaa 76,184,128\nTa 234,37,2048\nMvaa 108,184,136\nv_mul_f32 v64, s57, v64\n"
"v_mul_f32 v65, s57, v65\nMvaa 108,188,140\nTa 236,37,16384\nD1 1,16,46,2048\nMvaa 104,180,140\nTa 238,37,18432\nCV 64,64,65\nMvaa 104,176,136\nv_mul_f32 v66, s57, v66\n"
"v_mul_f32 v67, s57, v67\nMvaa 100,168,136\nTa 240,38,0\nCV 65,66,67\nMvaa 100,172,140\nTa 242,38,2048\nD1 1,16,46,3072\nMvaa 96,164,140\nv_mul_f32 v68, s57, v68\n"
"v_mul_f32 v69, s57, v69\nMvaa 96,160,136\nTa 244,38,16384\nCV 66,68,69\nMvaa 128,160,144\nTa 246,38,18432\nv_mul_f32 v70, s57, v70\nMvaa 128,164,148\nv_mul_f32 v71, s57, v71\n"
"CV 67,70,71\nMvaa 132,172,148\nTa 248,39,0\nAW 192,64\nMvaa 132,168,144\nTa 250,39,2048\nAW 193,65\nMvaa 136,176,144\nAW 194,66\nAW 195,67\nMvaa 136,180,148\nTa 252,39,16384\n"
"v_mul_f32 v72, s57, v72\nMvaa 140,188,148\nTa 254,39,18432\nv_mul_f32 v73, s57, v73\nMvaa 140,184,144\nCV 72,72,73\nv_mul_f32 v74, s57, v74\nMvaa 172,184,152\n"
"v_mul_f32 v75, s57, v75\nCV 73,74,75\nMvaa 172,188,156\nv_mul_f32 v76, s57, v76\nv_mul_f32 v77, s57, v77\nMvaa 168,180,156\nCV 74,76,77\nv_mul_f32 v78, s57, v78\nMvaa 168,176,152\n"
"v_mul_f32 v79, s57, v79\nCV 75,78,79\nMvaa 164,168,152\nAW 200,72\nAW 201,73\nMvaa 164,172,156\nAW 202,74\nAW 203,75\nMvaa 160,164,156\nv_mul_f32 v96, s57, v96\n"
"v_mul_f32 v97, s57, v97\nMvaa 160,160,152\ns_waitcnt vmcnt(21) lgkmcnt(0)\ns_barrier\nMvaa 80,224,128\nCV 96,96,97\nv_mul_f32 v98, s57, v98\nMvaa 80,228,132\ns_add_u32 m0, s47, 0\n"
"s_add_u32 s46, s86, 0xffff0000\nD1 1,16,46,0\nMvaa 84,236,132\nv_mul_f32 v99, s57, v99\nCV 97,98,99\nMvaa 84,232,128\nv_mul_f32 v100, s57, v100\nv_mul_f32 v101, s57, v101\n"
"Mvaa 88,240,128\nCV 98,100,101\nv_mul_f32 v102, s57, v102\nMvaa 88,244,132\nD1 1,16,46,1024\nv_mul_f32 v103, s57, v103\nMvaa 92,252,132\nCV 99,102,103\nAW 196,96\nMvaa 92,248,128\n"
"AW 197,97\nAW 198,98\nMvaa 124,248,136\nAW 199,99\nv_mul_f32 v128, s57, v128\nMvaa 124,252,140\nD1 1,16,46,2048\nv_mul_f32 v129, s57, v129\nMvaa 120,244,140\nCV 128,128,129\n"
"v_mul_f32 v130, s57, v130\nMvaa 120,240,136\nv_mul_f32 v131, s57, v131\nCV 129,130,131\nMvaa 116,232,136\nRa 160,24,0\nv_mul_f32 v132, s57, v132\nMvaa 116,236,140\n"
"D1 1,16,46,3072\nv_mul_f32 v133, s57, v133\nMvaa 112,228,140\nRa 164,24,1024\nCV 130,132,133\nMvaa 112,224,136\ns_add_u32 m0, s47, 16384\ns_add_u32 s46, s45, 0x408000\n"
"Mvaa 144,224,144\nRa 168,24,2048\nv_mul_f32 v134, s57, v134\nMvaa 144,228,148\nD1 1,16,46,0\nv_mul_f32 v135, s57, v135\nMvaa 148,236,148\nRa 172,24,3072\nCV 131,134,135\n"
"Mvaa 148,232,144\nds_write_b128 v23, v[128:131] offset:0\nv_mul_f32 v80, s57, v80\nMvaa 152,240,144\nRa 176,26,0\nv_mul_f32 v81, s57, v81\nMvaa 152,244,148\nD1 1,16,46,1024\n"
"CV 80,80,81\nMvaa 156,252,148\nRa 180,26,1024\nv_mul_f32 v82, s57, v82\nMvaa 156,248,144\nv_mul_f32 v83, s57, v83\nCV 81,82,83\nMvaa 188,248,152\nRa 184,26,2048\n"
"v_mul_f32 v84, s57, v84\nMvaa 188,252,156\nD1 1,16,46,2048\nv_mul_f32 v85, s57, v85\nMvaa 184,244,156\nRa 188,26,3072\nCV 82,84,85\nMvaa 184,240,152\nv_mul_f32 v86, s57, v86\n"
"v_mul_f32 v87, s57, v87\nMvaa 180,232,152\nCV 83,86,87\nAW 208,80\nMvaa 180,236,156\nD1 1,16,46,3072\nAW 209,81\nMvaa 176,228,156\nAW 210,82\nAW 211,83\nMvaa 176,224,152\n"
"s_waitcnt vmcnt(25) lgkmcnt(5)\ns_cmp_eq_u32 s79, 1\ns_cbranch_scc0 .E430\nv_mov_b32 v4, s97\nv_mov_b32 v5, s97\nv_mov_b32 v6, s97\nv_mov_b32 v7, s97\nv_mov_b32 v12, s97\n"
"v_mov_b32 v13, s97\nv_mov_b32 v14, s97\nv_mov_b32 v15, s97\nv_cmpx_gt_i32 vcc, 19, v3\nv_mov_b32 v19, s97\nv_cmpx_gt_i32 vcc, 18, v3\nv_mov_b32 v18, s97\n"
"v_cmpx_gt_i32 vcc, 17, v3\nv_mov_b32 v17, s97\nv_cmpx_gt_i32 vcc, 16, v3\nv_mov_b32 v16, s97\nv_cmpx_gt_i32 vcc, 3, v3\nv_mov_b32 v11, s97\nv_cmpx_gt_i32 vcc, 2, v3\n"
"v_mov_b32 v10, s97\nv_cmpx_gt_i32 vcc, 1, v3\nv_mov_b32 v9, s97\nv_cmpx_gt_i32 vcc, 0, v3\nv_mov_b32 v8, s97\ns_mov_b64 exec, -1\ns_nop 2\n.E430:\ns_nop 0\nCvaa 32,160,0,4\n"
"Ra 128,24,8192\nv_mul_f32 v88, s57, v88\nMvaa 32,164,8\nv_mul_f32 v89, s57, v89\nCV 88,88,89\nMvaa 32,168,16\nRa 132,24,9216\nv_mul_f32 v90, s57, v90\nMvaa 32,172,24\n"
"v_mul_f32 v91, s57, v91\nCV 89,90,91\nCvaa 36,172,28,8\nRa 136,24,10240\nv_mul_f32 v92, s57, v92\nMvaa 36,168,20\ns_add_u32 m0, s89, 32768\nv_mul_f32 v93, s57, v93\n"
"Mvaa 36,164,12\nRa 140,24,11264\ns_waitcnt vmcnt(16) lgkmcnt(4)\ns_barrier\nMvaa 36,160,4\nCV 90,92,93\nv_mul_f32 v94, s57, v94\nMvaa 36,176,36\nRa 144,26,8192\nSa 96,1,16,78,0\n"
"Mvaa 36,180,44\ns_add_u32 s46, s86, s88\nv_mul_f32 v95, s57, v95\nMvaa 36,184,52\nRa 148,26,9216\nCV 91,94,95\nMvaa 36,188,60\nAW 216,88\nAW 217,89\nMvaa 32,188,56\n"
"Ra 152,26,10240\nAW 218,90\nMvaa 32,184,48\nD1 1,16,46,0\nAW 219,91\nMvaa 32,180,40\nRa 156,26,11264\nWL 4\nMvaa 32,176,32\nv_mov_b32 v86, 0\nv_mov_b32 v87, 0\nCvaa 40,128,0,12\n"
"Sa 100,1,16,78,1024\nv_mov_b32 v88, 0\nMvaa 40,132,8\ns_cmp_eq_u32 s99, -1\nMvaa 40,136,16\ns_cselect_b32 s87, s95, s87\nv_mov_b32 v89, 0\nv_mul_f32 v136, s57, v136\n"
"v_mul_f32 v137, s57, v137\nCV 136,136,137\nv_mul_f32 v138, s57, v138\nv_mul_f32 v139, s57, v139\nMvaa 40,140,24\nCV 137,138,139\nv_mul_f32 v140, s57, v140\n"
"v_mul_f32 v141, s57, v141\nCV 138,140,141\nv_mul_f32 v142, s57, v142\nv_mul_f32 v143, s57, v143\nCvaa 44,140,28,16\nv_and_b32 v27, 0x70, v1\n"
"buffer_load_dword v27, v27, s[16:19], s61 offen offset:136 sc0 sc1\nCV 139,142,143\nds_write_b128 v23, v[136:139] offset:2048\nv_mul_f32 v144, s57, v144\n"
"v_mul_f32 v145, s57, v145\nCV 144,144,145\nMvaa 44,136,20\nD1 1,16,46,1024\nv_mul_f32 v146, s57, v146\nv_mul_f32 v147, s57, v147\nCV 145,146,147\nv_mul_f32 v148, s57, v148\n"
"v_mul_f32 v149, s57, v149\nMvaa 44,132,12\nWL 1\nCV 146,148,149\nv_mul_f32 v150, s57, v150\nv_mul_f32 v151, s57, v151\nCV 147,150,151\nds_write_b128 v23, v[144:147] offset:4096\n"
"v_mul_f32 v152, s57, v152\nMvaa 44,128,4\nv_mul_f32 v153, s57, v153\nCV 152,152,153\nv_mul_f32 v154, s57, v154\nv_mul_f32 v155, s57, v155\nCV 153,154,155\n"
"v_mul_f32 v156, s57, v156\nMvaa 44,144,36\nSa 104,1,16,78,2048\nv_mul_f32 v157, s57, v157\nCV 154,156,157\nv_mul_f32 v158, s57, v158\nv_mul_f32 v159, s57, v159\nCV 155,158,159\n"
"ds_write_b128 v23, v[152:155] offset:6144\nMvaa 44,148,44\ns_add_u32 s88, s88, 32768\nEX 80,32\nv_mul_f32 v160, s57, v160\nv_mul_f32 v161, s57, v161\nCV 160,160,161\n"
"Mvaa 44,152,52\nv_mul_f32 v162, s57, v162\nv_mul_f32 v163, s57, v163\nCV 161,162,163\nv_mul_f32 v164, s57, v164\nv_mul_f32 v165, s57, v165\nCV 162,164,165\n"
"v_mul_f32 v166, s57, v166\nMvaa 44,156,60\nEX 81,33\nv_mul_f32 v167, s57, v167\nCV 163,166,167\nds_write_b128 v23, v[160:163] offset:1024\nMvaa 40,156,56\n"
"v_mul_f32 v168, s57, v168\nv_mul_f32 v169, s57, v169\nCV 168,168,169\nv_mul_f32 v170, s57, v170\nv_mul_f32 v171, s57, v171\nCV 169,170,171\nv_mul_f32 v172, s57, v172\n"
"Mvaa 40,152,48\nD1 1,16,46,2048\nv_mul_f32 v173, s57, v173\nCV 170,172,173\nv_mul_f32 v174, s57, v174\nv_mul_f32 v175, s57, v175\nCV 171,174,175\nMvaa 40,148,40\n"
"ds_write_b128 v23, v[168:171] offset:3072\nv_mul_f32 v176, s57, v176\nv_mul_f32 v177, s57, v177\nCV 176,176,177\nv_mul_f32 v178, s57, v178\nv_mul_f32 v179, s57, v179\n"
"CV 177,178,179\nMvaa 40,144,32\nAD 84,80,81\nCV 64,80,81\nv_mul_f32 v180, s57, v180\nv_mul_f32 v181, s57, v181\nCV 178,180,181\nv_mul_f32 v182, s57, v182\nSa 108,1,16,78,3072\n"
"v_mul_f32 v183, s57, v183\nCV 179,182,183\nds_write_b128 v23, v[176:179] offset:5120\nv_mul_f32 v184, s57, v184\nv_mul_f32 v185, s57, v185\nCV 184,184,185\nEX 82,34\n"
"v_mul_f32 v186, s57, v186\nv_mul_f32 v187, s57, v187\nCV 185,186,187\nv_mul_f32 v188, s57, v188\nv_mul_f32 v189, s57, v189\nCV 186,188,189\nv_mul_f32 v190, s57, v190\n"
"v_mul_f32 v191, s57, v191\nCV 187,190,191\nds_write_b128 v23, v[184:187] offset:7168\ns_waitcnt vmcnt(16) lgkmcnt(0)\nEX 83,35\nAD 84,84,82\nD1 1,16,46,3072\n"
"s_add_u32 m0, s89, 49152\ns_barrier\nAD 84,84,83\nCV 65,82,83\nRa 128,24,32768\nRa 224,22,0\nRa 228,22,1024\ns_add_u32 s46, s87, s88\nEX 80,36\nRa 132,24,33792\nRa 232,22,2048\n"
"Ra 236,22,3072\nRa 240,22,4096\nEX 81,37\nRa 136,24,34816\nRa 244,22,5120\nRa 248,22,6144\nD1 1,16,46,0\nRa 252,22,7168\nv_mul_f32 v104, s57, v104\nv_mul_f32 v105, s57, v105\n"
"Ra 140,24,35840\nCV 104,104,105\nv_mul_f32 v106, s57, v106\nAD 85,80,81\nCV 68,80,81\nv_mul_f32 v107, s57, v107\nRa 144,26,32768\nCV 105,106,107\nv_mul_f32 v108, s57, v108\n"
"v_mul_f32 v109, s57, v109\ns_cmp_eq_u32 s13, 7\nEX 82,38\nRa 148,26,33792\ns_cbranch_scc0 .E417\nv_cmpx_gt_i32 vcc, 19, v0\nv_mov_b32 v19, s97\nv_cmpx_gt_i32 vcc, 18, v0\n"
"v_mov_b32 v18, s97\nv_cmpx_gt_i32 vcc, 17, v0\nv_mov_b32 v17, s97\nv_cmpx_gt_i32 vcc, 16, v0\nv_mov_b32 v16, s97\nv_cmpx_gt_i32 vcc, 3, v0\nv_mov_b32 v11, s97\n"
"v_cmpx_gt_i32 vcc, 2, v0\nv_mov_b32 v10, s97\nv_cmpx_gt_i32 vcc, 1, v0\nv_mov_b32 v9, s97\nv_cmpx_gt_i32 vcc, 0, v0\nv_mov_b32 v8, s97\ns_mov_b64 exec, -1\ns_nop 2\n.E417:\n"
"EX 83,39\nRa 152,26,34816\nAD 85,85,82\nCV 106,108,109\nv_mul_f32 v110, s57, v110\nD1 1,16,46,1024\nv_mul_f32 v111, s57, v111\nCV 107,110,111\nRa 156,26,35840\nAW 204,104\n"
"AW 205,105\nAD 85,85,83\nCV 69,82,83\nAW 206,106\nAW 207,107\nRa 160,24,40960\nv_mul_f32 v112, s57, v112\nv_mul_f32 v113, s57, v113\ns_xor_b32 s89, s89, 0x10000\nEX 80,40\n"
"Ra 164,24,41984\nCV 112,112,113\nv_mul_f32 v114, s57, v114\nv_mul_f32 v115, s57, v115\nWL 6\nEX 81,41\nRa 168,24,43008\nAD 84,84,80\nCV 113,114,115\nD1 1,16,46,2048\n"
"v_mul_f32 v116, s57, v116\nv_mul_f32 v117, s57, v117\nCV 114,116,117\nRa 172,24,44032\nv_mul_f32 v118, s57, v118\nv_mul_f32 v119, s57, v119\nAD 84,84,81\nCV 66,80,81\n"
"CV 115,118,119\nCvaa 48,128,0,4\nRa 176,26,40960\nAW 212,112\nAW 213,113\nAW 214,114\nMvaa 48,132,8\nEX 82,42\nAW 215,115\nMvaa 48,136,16\nRa 180,26,41984\n"
"v_mul_f32 v120, s57, v120\nv_mul_f32 v121, s57, v121\nMvaa 48,140,24\nWL 6\nEX 83,43\nWV 8\nv_cmp_ne_u32 vcc, s15, v27\ns_nop 0\ns_cbranch_vccnz .S431\n.S432:\nCvaa 52,140,28,8\n"
"Ra 184,26,43008\nAD 84,84,82\nCV 120,120,121\nv_mul_f32 v122, s57, v122\nMvaa 52,136,20\nD1 1,16,46,3072\ns_add_u32 m0, s89, 0\nv_mul_f32 v123, s57, v123\nMvaa 52,132,12\n"
"Ra 188,26,44032\nCV 121,122,123\nv_mul_f32 v124, s57, v124\nMvaa 52,128,4\nAD 84,84,83\nCV 67,82,83\nv_mul_f32 v125, s57, v125\nCV 122,124,125\nMvaa 52,144,36\nRa 128,2,49152\n"
"s_add_u32 s78, s78, s55\nSa 112,1,16,78,0\nMvaa 52,148,44\ns_add_u32 s46, s86, s88\nEX 80,44\nMvaa 52,152,52\nRa 132,2,50176\nv_xor_b32 v24, 0x10000, v24\n"
"v_mul_f32 v126, s57, v126\nv_mul_f32 v127, s57, v127\nMvaa 52,156,60\nWL 6\nEX 81,45\nMvaa 48,156,56\nRa 136,2,51200\nAD 85,85,80\nCV 123,126,127\nMvaa 48,152,48\nD1 1,16,46,0\n"
"AW 220,120\nAW 221,121\nAW 222,122\nMvaa 48,148,40\nRa 140,2,52224\nAW 223,123\nv_mov_b32 v115, -1\nMvaa 48,144,32\nAD 85,85,81\nCV 70,80,81\nCvaa 56,160,0,12\nRa 144,2,53248\n"
"Sa 116,1,16,78,1024\nMvaa 56,164,8\nEX 82,46\nMvaa 56,168,16\nRa 148,2,54272\nv_xor_b32 v26, 0x10000, v26\nMvaa 56,172,24\nWL 6\nEX 83,47\nCvaa 60,172,28,16\nRa 152,2,55296\n"
"AD 85,85,82\nMvaa 60,168,20\nD1 1,16,46,1024\nMvaa 60,164,12\nRa 156,2,56320\nMvaa 60,160,4\nAD 85,85,83\nCV 71,82,83\nMvaa 60,176,36\nRa 160,2,57344\nv_max_f32 v90, v84, v85\n"
"Mvaa 60,180,44\ns_add_u32 s88, s88, 32768\nEX 80,48\nMvaa 60,184,52\nRa 164,2,58368\nv_cmp_lt_f32 vcc, s63, v90\nMvaa 60,188,60\nWL 8\nEX 81,49\nMvaa 56,188,56\nRa 168,2,59392\n"
"s_cbranch_vccnz .T419\n.T420:\nv_add_f32 v20, s93, v84\nv_mov_b32 v21, v85\nv_mov_b32 v22, v20\nv_mov_b32 v23, v21\nMvaa 56,184,48\nD1 1,16,46,2048\n"
"v_permlane32_swap_b32 v20, v22\nv_permlane32_swap_b32 v21, v23\nAD 20,20,22\nAD 21,21,23\nv_mov_b32 v22, v20\nv_mov_b32 v23, v21\nMvaa 56,180,40\nRa 172,2,60416\n"
"v_permlane16_swap_b32 v20, v22\nv_permlane16_swap_b32 v21, v23\nAD 20,20,22\nAD 21,21,23\nv_min_f32 v90, v20, v21\nv_cmp_gt_f32 vcc, s64, v90\ns_nop 0\ns_cbranch_vccnz .T421\n"
".T422:\nAD 86,86,84\nMvaa 56,176,32\nAD 84,80,81\nCV 72,80,81\nZvav 128,128,64\nRa 176,2,61440\nSa 120,1,16,78,2048\nZvav 132,128,68\nEX 82,50\nZvav 140,132,68\nRa 180,2,62464\n"
"Zvav 136,132,64\nWL 8\nEX 83,51\nZvav 144,136,64\nRa 184,2,63488\nAD 84,84,82\nZvav 148,136,68\n.W587:\ns_cbranch_execz .W591\nD1 1,16,46,3072\ns_add_u32 m0, s89, 16384\n"
"Zvav 156,140,68\nRa 188,2,64512\ns_barrier\nZvav 152,140,64\nAD 84,84,83\nCV 73,82,83\nZvav 160,144,64\nRa 128,24,0\nSa 124,1,16,78,3072\nZvav 164,144,68\ns_add_u32 s46, s87, s88\n"
"EX 80,52\nZvav 172,148,68\nRa 132,24,1024\nv_xor_b32 v2, 0x10000, v2\nZvav 168,148,64\nWL 8\nEX 81,53\nZvav 176,152,64\nRa 136,24,2048\nAD 87,87,85\nZvav 180,152,68\nD1 1,16,46,0\n"
"Zvav 188,156,68\nRa 140,24,3072\nZvav 184,156,64\nAD 85,80,81\nCV 76,80,81\nZvav 192,160,64\nRa 144,26,0\nZvav 196,160,68\nEX 82,54\nZvav 204,164,68\nRa 148,26,1024\n"
"Zvav 200,164,64\nWL 8\nEX 83,55\nZvav 208,168,64\nRa 152,26,2048\nAD 85,85,82\nZvav 212,168,68\nD1 1,16,46,1024\nZvav 220,172,68\nRa 156,26,3072\nZvav 216,172,64\nAD 85,85,83\n"
"CV 77,82,83\nZvav 224,176,64\nRa 160,24,8192\nZvav 228,176,68\nEX 80,56\nZvav 236,180,68\nRa 164,24,9216\nZvav 232,180,64\nWL 6\nEX 81,57\nZvav 240,184,64\nRa 168,24,10240\n"
"AD 84,84,80\nv_and_b32 v27, 0x70, v1\nbuffer_load_dword v27, v27, s[16:19], s61 offen offset:140 sc0 sc1\nZvav 244,184,68\nD1 1,16,46,2048\nZvav 252,188,68\nRa 172,24,11264\n"
"Zvav 248,188,64\nAD 84,84,81\nCV 74,80,81\ns_sub_u32 s99, s99, 1\ns_cmp_eq_u32 s13, 7\ns_cbranch_scc1 .SW415\n.p2align 6\n.A446:\nCvaa 32,128,0,4\nRa 176,26,8192\nMvaa 32,132,8\n"
"s_add_u32 s49, s49, 131072\nEX 82,58\nMvaa 32,136,16\nRa 180,26,9216\nMvaa 32,140,24\nWL 6\nEX 83,59\nCvaa 36,140,28,8\nRa 184,26,10240\nAD 84,84,82\nMvaa 36,136,20\n"
"D1 1,16,46,3072\ns_add_u32 m0, s89, 32768\nMvaa 36,132,12\nRa 188,26,11264\nMvaa 36,128,4\nAD 84,84,83\nCV 75,82,83\nMvaa 36,144,36\nRa 128,2,16384\nMvaa 36,148,44\n"
"s_add_u32 s46, s86, s88\nEX 80,60\nMvaa 36,152,52\nRa 132,2,17408\nMvaa 36,156,60\nWL 6\nEX 81,61\nMvaa 32,156,56\nRa 136,2,18432\nAD 85,85,80\nMvaa 32,152,48\nD1 1,16,46,0\n"
"Mvaa 32,148,40\nRa 140,2,19456\nMvaa 32,144,32\nAD 85,85,81\nCV 78,80,81\nCvaa 40,160,0,12\nRa 144,2,20480\nMvaa 40,164,8\nSv 112,1,16,49,0\nEX 82,62\nMvaa 40,168,16\n"
"Ra 148,2,21504\nMvaa 40,172,24\nWL 6\nEX 83,63\nCvaa 44,172,28,16\nRa 152,2,22528\nAD 85,85,82\nMvaa 44,168,20\nD1 1,16,46,1024\nMvaa 44,164,12\nRa 156,2,23552\nMvaa 44,160,4\n"
"AD 85,85,83\nCV 79,82,83\nMvaa 44,176,36\nRa 160,2,24576\nv_max_f32 v90, v84, v85\nMvaa 44,180,44\ns_add_u32 s88, s88, 32768\nEX 80,32\nMvaa 44,184,52\nRa 164,2,25600\n"
"v_cmp_lt_f32 vcc, s63, v90\nMvaa 44,188,60\nWL 8\nEX 81,33\nMvaa 40,188,56\nRa 168,2,26624\ns_cbranch_vccnz .T454\n.T455:\nMvaa 40,184,48\nD1 1,16,46,2048\nMvaa 40,180,40\n"
"Ra 172,2,27648\nAD 86,86,84\nMvaa 40,176,32\nAD 84,80,81\nCV 64,80,81\nMvav 128,128,72\nRa 176,2,28672\nMvav 132,128,76\nEX 82,34\nMvav 140,132,76\nRa 180,2,29696\n"
"Mvav 136,132,72\ns_waitcnt vmcnt(13) lgkmcnt(8)\nEX 83,35\nMvav 144,136,72\nRa 184,2,30720\nAD 84,84,82\nMvav 148,136,76\nD1 1,16,46,3072\ns_add_u32 m0, s89, 49152\n"
"Mvav 156,140,76\nRa 188,2,31744\ns_barrier\nMvav 152,140,72\nAD 84,84,83\nCV 65,82,83\nMvav 160,144,72\nRa 128,24,32768\nMvav 164,144,76\ns_add_u32 s46, s87, s88\nEX 80,36\n"
"Mvav 172,148,76\nRa 132,24,33792\nMvav 168,148,72\nWL 8\nEX 81,37\nMvav 176,152,72\nRa 136,24,34816\nAD 87,87,85\nMvav 180,152,76\nD1 1,16,46,0\nMvav 188,156,76\nRa 140,24,35840\n"
"Mvav 184,156,72\nAD 85,80,81\nCV 68,80,81\nMvav 192,160,72\nRa 144,26,32768\nMvav 196,160,76\ns_cmp_eq_u32 s99, 0\nEX 82,38\nMvav 204,164,76\nRa 148,26,33792\n"
"s_cbranch_scc1 .E448\n.E449:\nMvav 200,164,72\nWL 8\nEX 83,39\nMvav 208,168,72\nRa 152,26,34816\nAD 85,85,82\nMvav 212,168,76\nD1 1,16,46,1024\nMvav 220,172,76\nRa 156,26,35840\n"
"Mvav 216,172,72\nAD 85,85,83\nCV 69,82,83\nMvav 224,176,72\nRa 160,24,40960\nMvav 228,176,76\ns_xor_b32 s89, s89, 0x10000\nEX 80,40\nMvav 236,180,76\nRa 164,24,41984\n"
"Mvav 232,180,72\nWL 6\nEX 81,41\nMvav 240,184,72\nRa 168,24,43008\nAD 84,84,80\nMvav 244,184,76\nD1 1,16,46,2048\nMvav 252,188,76\nRa 172,24,44032\nMvav 248,188,72\nAD 84,84,81\n"
"CV 66,80,81\nCvaa 48,128,0,4\nRa 176,26,40960\nMvaa 48,132,8\ns_cmp_eq_u32 s99, 0\nEX 82,42\nMvaa 48,136,16\nRa 180,26,41984\ns_cselect_b32 s86, s94, s86\nMvaa 48,140,24\nWL 6\n"
"EX 83,43\nCvaa 52,140,28,8\nRa 184,26,43008\nAD 84,84,82\nMvaa 52,136,20\nD1 1,16,46,3072\ns_add_u32 m0, s89, 0\nMvaa 52,132,12\nRa 188,26,44032\nMvaa 52,128,4\nAD 84,84,83\n"
"CV 67,82,83\nMvaa 52,144,36\nRa 128,2,49152\nMvaa 52,148,44\ns_add_u32 s46, s86, s88\nEX 80,44\nMvaa 52,152,52\nRa 132,2,50176\nv_xor_b32 v24, 0x10000, v24\nMvaa 52,156,60\nWL 6\n"
"EX 81,45\nMvaa 48,156,56\nRa 136,2,51200\nAD 85,85,80\nMvaa 48,152,48\nD1 1,16,46,0\nMvaa 48,148,40\nRa 140,2,52224\nMvaa 48,144,32\nAD 85,85,81\nCV 70,80,81\nCvaa 56,160,0,12\n"
"Ra 144,2,53248\nMvaa 56,164,8\nSv 112,1,16,49,1024\nEX 82,46\nMvaa 56,168,16\nRa 148,2,54272\nv_xor_b32 v26, 0x10000, v26\nMvaa 56,172,24\nWL 6\nEX 83,47\nCvaa 60,172,28,16\n"
"Ra 152,2,55296\nAD 85,85,82\nMvaa 60,168,20\nD1 1,16,46,1024\nMvaa 60,164,12\nRa 156,2,56320\nMvaa 60,160,4\nAD 85,85,83\nCV 71,82,83\nMvaa 60,176,36\nRa 160,2,57344\n"
"v_max_f32 v90, v84, v85\nMvaa 60,180,44\ns_add_u32 s88, s88, 32768\nEX 80,48\nMvaa 60,184,52\nRa 164,2,58368\nv_cmp_lt_f32 vcc, s63, v90\nMvaa 60,188,60\nWL 8\nEX 81,49\n"
"Mvaa 56,188,56\nRa 168,2,59392\ns_cbranch_vccnz .T456\n.T457:\nMvaa 56,184,48\nD1 1,16,46,2048\nMvaa 56,180,40\nRa 172,2,60416\nAD 86,86,84\nMvaa 56,176,32\nAD 84,80,81\n"
"CV 72,80,81\nMvav 128,128,64\nRa 176,2,61440\nMvav 132,128,68\nEX 82,50\nMvav 140,132,68\nRa 180,2,62464\nMvav 136,132,64\ns_waitcnt vmcnt(13) lgkmcnt(8)\nEX 83,51\n"
"Mvav 144,136,64\nRa 184,2,63488\nAD 84,84,82\nMvav 148,136,68\nD1 1,16,46,3072\ns_add_u32 m0, s89, 16384\nMvav 156,140,68\nRa 188,2,64512\ns_barrier\nMvav 152,140,64\nAD 84,84,83\n"
"CV 73,82,83\nMvav 160,144,64\nRa 128,24,0\nMvav 164,144,68\ns_add_u32 s46, s87, s88\nEX 80,52\nMvav 172,148,68\nRa 132,24,1024\nv_xor_b32 v2, 0x10000, v2\nMvav 168,148,64\nWL 8\n"
"EX 81,53\nMvav 176,152,64\nRa 136,24,2048\nAD 87,87,85\nMvav 180,152,68\nD1 1,16,46,0\nMvav 188,156,68\nRa 140,24,3072\nMvav 184,156,64\nAD 85,80,81\nCV 76,80,81\nMvav 192,160,64\n"
"Ra 144,26,0\nMvav 196,160,68\ns_cmp_lt_u32 s99, s92\nEX 82,54\nMvav 204,164,68\nRa 148,26,1024\ns_cbranch_scc1 .E450\n.E451:\nMvav 200,164,64\nWL 8\nEX 83,55\nMvav 208,168,64\n"
"Ra 152,26,2048\nAD 85,85,82\nMvav 212,168,68\nD1 1,16,46,1024\nMvav 220,172,68\nRa 156,26,3072\nMvav 216,172,64\nAD 85,85,83\nCV 77,82,83\nMvav 224,176,64\nRa 160,24,8192\n"
"Mvav 228,176,68\ns_sub_u32 s90, s90, 1\nEX 80,56\nMvav 236,180,68\nRa 164,24,9216\ns_cbranch_scc1 .E452\n.E453:\nMvav 232,180,64\nWL 6\nEX 81,57\nMvav 240,184,64\nRa 168,24,10240\n"
"AD 84,84,80\nMvav 244,184,68\nD1 1,16,46,2048\ns_sub_u32 s99, s99, 1\nMvav 252,188,68\nRa 172,24,11264\nMvav 248,188,64\nAD 84,84,81\nCV 74,80,81\ns_cbranch_scc0 .A446\n"
"s_branch .A458\n.E448:\nv_cmpx_gt_i32 vcc, 19, v0\nv_mov_b32 v15, s97\nv_cmpx_gt_i32 vcc, 18, v0\nv_mov_b32 v14, s97\nv_cmpx_gt_i32 vcc, 17, v0\nv_mov_b32 v13, s97\n"
"v_cmpx_gt_i32 vcc, 16, v0\nv_mov_b32 v12, s97\nv_cmpx_gt_i32 vcc, 3, v0\nv_mov_b32 v7, s97\nv_cmpx_gt_i32 vcc, 2, v0\nv_mov_b32 v6, s97\nv_cmpx_gt_i32 vcc, 1, v0\n"
"v_mov_b32 v5, s97\nv_cmpx_gt_i32 vcc, 0, v0\nv_mov_b32 v4, s97\ns_mov_b64 exec, -1\ns_nop 2\ns_branch .E449\n.E450:\ns_cmp_eq_u32 s99, 0\ns_cbranch_scc1 .K447\n"
"v_cmpx_gt_i32 vcc, 19, v3\nv_mov_b32 v15, s97\nv_cmpx_gt_i32 vcc, 18, v3\nv_mov_b32 v14, s97\nv_cmpx_gt_i32 vcc, 17, v3\nv_mov_b32 v13, s97\nv_cmpx_gt_i32 vcc, 16, v3\n"
"v_mov_b32 v12, s97\nv_cmpx_gt_i32 vcc, 3, v3\nv_mov_b32 v7, s97\nv_cmpx_gt_i32 vcc, 2, v3\nv_mov_b32 v6, s97\nv_cmpx_gt_i32 vcc, 1, v3\nv_mov_b32 v5, s97\n"
"v_cmpx_gt_i32 vcc, 0, v3\nv_mov_b32 v4, s97\ns_mov_b64 exec, -1\ns_nop 2\ns_branch .E451\n.K447:\nv_cmpx_gt_i32 vcc, 19, v3\nv_mov_b32 v19, s97\nv_cmpx_gt_i32 vcc, 18, v3\n"
"v_mov_b32 v18, s97\nv_cmpx_gt_i32 vcc, 17, v3\nv_mov_b32 v17, s97\nv_cmpx_gt_i32 vcc, 16, v3\nv_mov_b32 v16, s97\nv_cmpx_gt_i32 vcc, 3, v3\nv_mov_b32 v11, s97\n"
"v_cmpx_gt_i32 vcc, 2, v3\nv_mov_b32 v10, s97\nv_cmpx_gt_i32 vcc, 1, v3\nv_mov_b32 v9, s97\nv_cmpx_gt_i32 vcc, 0, v3\nv_mov_b32 v8, s97\ns_mov_b64 exec, -1\ns_nop 2\n"
"s_branch .E451\n.E452:\ns_swappc_b64 s[80:81], s[8:9]\ns_branch .E453\n.HD409:\nv_cmp_ne_u32 vcc, s15, v27\ns_nop 0\ns_cbranch_vccnz .S434\n.S435:\n"
"buffer_store_dword v25, off, s[16:19], s54 offset:4 sc0 sc1\nv_and_b32 v27, 0x70, v1\nbuffer_load_dword v27, v27, s[16:19], s61 offen offset:12 sc0 sc1\ns_mov_b32 s90, 1\n"
"s_swappc_b64 s[8:9], s[80:81]\nv_cmp_ne_u32 vcc, s15, v27\ns_nop 0\ns_cbranch_vccnz .S437\n.S438:\nv_and_b32 v27, 0x70, v1\n"
"buffer_load_dword v27, v27, s[16:19], s61 offen offset:8 sc0 sc1\ns_mov_b32 s90, 1\ns_swappc_b64 s[8:9], s[80:81]\nv_cmp_ne_u32 vcc, s15, v27\ns_nop 0\ns_cbranch_vccnz .S440\n"
".S441:\nv_and_b32 v27, 0xf0, v1\nbuffer_load_dword v27, v27, s[16:19], s61 offen offset:4 sc0 sc1\ns_mov_b32 s90, 1\ns_swappc_b64 s[8:9], s[80:81]\nv_cmp_ne_u32 vcc, s15, v27\n"
"s_nop 0\ns_cbranch_vccnz .S443\n.S444:\ns_mov_b32 s90, 100\ns_swappc_b64 s[8:9], s[80:81]\n.A458:\nRa 176,26,8192\ns_add_u32 s49, s49, 131072\nEX 82,58\nRa 180,26,9216\nWL 6\n"
"EX 83,59\nCvaa 36,140,28,8\nRa 184,26,10240\nAD 84,84,82\nMvaa 36,136,20\nD1 1,16,46,3072\ns_add_u32 m0, s89, 32768\nMvaa 36,132,12\nRa 188,26,11264\nMvaa 36,128,4\nAD 84,84,83\n"
"CV 75,82,83\nMvaa 36,144,36\nRa 128,2,16384\nMvaa 36,148,44\ns_add_u32 s46, s86, s88\nEX 80,60\nMvaa 36,152,52\nRa 132,2,17408\nMvaa 36,156,60\nEX 81,61\nRa 136,2,18432\n"
"AD 85,85,80\nD1 1,16,46,0\nRa 140,2,19456\nAD 85,85,81\nCV 78,80,81\nRa 144,2,20480\nSv 112,1,16,49,0\ns_mov_b32 s87, s95\nEX 82,62\nRa 148,2,21504\nWL 6\nEX 83,63\n"
"Cvaa 44,172,28,16\nRa 152,2,22528\nAD 85,85,82\nMvaa 44,168,20\nD1 1,16,46,1024\nMvaa 44,164,12\nRa 156,2,23552\nMvaa 44,160,4\nAD 85,85,83\nCV 79,82,83\nMvaa 44,176,36\n"
"Ra 160,2,24576\nv_max_f32 v90, v84, v85\nMvaa 44,180,44\ns_add_u32 s88, s88, 32768\nMvaa 44,184,52\nRa 164,2,25600\nv_cmp_lt_f32 vcc, s63, v90\nMvaa 44,188,60\nWL 8\n"
"Ra 168,2,26624\ns_cbranch_vccnz .T460\n.T461:\nD1 1,16,46,2048\nRa 172,2,27648\nAD 86,86,84\nMvav 128,128,72\nRa 176,2,28672\nMvav 132,128,76\nMvav 140,132,76\nRa 180,2,29696\n"
"Mvav 136,132,72\ns_waitcnt vmcnt(13) lgkmcnt(8)\nMvav 144,136,72\nRa 184,2,30720\nMvav 148,136,76\nD1 1,16,46,3072\ns_add_u32 m0, s89, 49152\nMvav 156,140,76\nRa 188,2,31744\n"
"s_barrier\nMvav 152,140,72\nMvav 160,144,72\nRa 128,24,32768\nMvav 164,144,76\ns_add_u32 s46, s87, s88\nEX 80,36\nMvav 172,148,76\nRa 132,24,33792\nMvav 168,148,72\nWL 8\n"
"EX 81,37\nMvav 176,152,72\nRa 136,24,34816\nAD 87,87,85\nMvav 180,152,76\nD1 1,16,46,0\nMvav 188,156,76\nRa 140,24,35840\nMvav 184,156,72\nAD 85,80,81\nCV 68,80,81\n"
"Mvav 192,160,72\nRa 144,26,32768\nMvav 196,160,76\ns_nop 0\nEX 82,38\nMvav 204,164,76\nRa 148,26,33792\nv_cmpx_gt_i32 vcc, 19, v0\nv_mov_b32 v19, s97\nv_cmpx_gt_i32 vcc, 18, v0\n"
"v_mov_b32 v18, s97\nv_cmpx_gt_i32 vcc, 17, v0\nv_mov_b32 v17, s97\nv_cmpx_gt_i32 vcc, 16, v0\nv_mov_b32 v16, s97\nv_cmpx_gt_i32 vcc, 3, v0\nv_mov_b32 v11, s97\n"
"v_cmpx_gt_i32 vcc, 2, v0\nv_mov_b32 v10, s97\nv_cmpx_gt_i32 vcc, 1, v0\nv_mov_b32 v9, s97\nv_cmpx_gt_i32 vcc, 0, v0\nv_mov_b32 v8, s97\ns_mov_b64 exec, -1\ns_nop 2\ns_nop 0\n"
"Mvav 200,164,72\nWL 8\nEX 83,39\nMvav 208,168,72\nRa 152,26,34816\nAD 85,85,82\nMvav 212,168,76\nD1 1,16,46,1024\nMvav 220,172,76\nRa 156,26,35840\nMvav 216,172,72\nAD 85,85,83\n"
"CV 69,82,83\n.W588:\ns_cbranch_execz .W592\nMvav 224,176,72\nRa 160,24,40960\nMvav 228,176,76\ns_xor_b32 s89, s89, 0x10000\nMvav 236,180,76\nRa 164,24,41984\nMvav 232,180,72\n"
"WL 10\nMvav 240,184,72\nRa 168,24,43008\nMvav 244,184,76\nD1 1,16,46,2048\nMvav 252,188,76\nRa 172,24,44032\nMvav 248,188,72\nRa 176,26,40960\nRa 180,26,41984\nWL 6\n"
"Cvaa 52,140,28,8\nRa 184,26,43008\nMvaa 52,136,20\nD1 1,16,46,3072\ns_add_u32 m0, s89, 0\nMvaa 52,132,12\nRa 188,26,44032\nMvaa 52,128,4\nMvaa 52,144,36\nRa 128,2,49152\n"
"Mvaa 52,148,44\ns_add_u32 s46, s86, s88\nEX 80,44\nMvaa 52,152,52\nRa 132,2,50176\nv_xor_b32 v24, 0x10000, v24\nMvaa 52,156,60\nEX 81,45\nRa 136,2,51200\nAD 85,85,80\n"
"D1 1,16,46,0\nRa 140,2,52224\nAD 85,85,81\nCV 70,80,81\nRa 144,2,53248\nSv 112,1,16,49,1024\nEX 82,46\nRa 148,2,54272\nv_xor_b32 v26, 0x10000, v26\nWL 6\nEX 83,47\n"
"Cvaa 60,172,28,16\nRa 152,2,55296\nAD 85,85,82\nMvaa 60,168,20\nD1 1,16,46,1024\nMvaa 60,164,12\nRa 156,2,56320\nMvaa 60,160,4\nAD 85,85,83\nCV 71,82,83\nMvaa 60,176,36\n"
"Ra 160,2,57344\nMvaa 60,180,44\ns_add_u32 s88, s88, 32768\nMvaa 60,184,52\nRa 164,2,58368\nv_cmp_lt_f32 vcc, s63, v85\nMvaa 60,188,60\nWL 8\nRa 168,2,59392\ns_cbranch_vccnz .T462\n"
".T463:\nD1 1,16,46,2048\nRa 172,2,60416\nRa 176,2,61440\nMvav 132,128,68\nMvav 140,132,68\nRa 180,2,62464\ns_waitcnt vmcnt(13) lgkmcnt(8)\nRa 184,2,63488\nMvav 148,136,68\n"
"D1 1,16,46,3072\ns_add_u32 m0, s89, 16384\nMvav 156,140,68\nRa 188,2,64512\ns_barrier\nRa 128,24,0\nMvav 164,144,68\ns_add_u32 s46, s87, s88\nEX 80,52\nMvav 172,148,68\n"
"Ra 132,24,1024\nv_xor_b32 v2, 0x10000, v2\nWL 8\nEX 81,53\nRa 136,24,2048\nAD 87,87,85\nMvav 180,152,68\nD1 1,16,46,0\nMvav 188,156,68\nRa 140,24,3072\nAD 85,80,81\nCV 76,80,81\n"
"Ra 144,26,0\nMvav 196,160,68\nEX 82,54\nMvav 204,164,68\nRa 148,26,1024\nWL 8\nEX 83,55\nRa 152,26,2048\nAD 85,85,82\nMvav 212,168,68\nD1 1,16,46,1024\nMvav 220,172,68\n"
"Ra 156,26,3072\nAD 85,85,83\nCV 77,82,83\nRa 160,24,8192\nMvav 228,176,68\ns_sub_u32 s90, s90, 1\nMvav 236,180,68\nRa 164,24,9216\nWL 6\nRa 168,24,10240\nMvav 244,184,68\n"
"D1 1,16,46,2048\nMvav 252,188,68\nRa 172,24,11264\n.SW415:\nv_mov_b32 v4, 0\nv_mov_b32 v5, 0\nv_mov_b32 v6, 0\nv_mov_b32 v7, 0\nv_mov_b32 v8, 0\nv_mov_b32 v9, 0\nv_mov_b32 v10, 0\n"
"v_mov_b32 v11, 0\nv_mov_b32 v12, 0\nv_mov_b32 v13, 0\nv_mov_b32 v14, 0\nv_mov_b32 v15, 0\nv_mov_b32 v16, 0\nv_mov_b32 v17, 0\nv_mov_b32 v18, 0\nv_mov_b32 v19, 0\n"
"s_add_u32 s99, s98, 1\ns_mov_b32 s94, s96\ns_add_u32 s95, s96, 65536\nCvaa 32,128,192,4\nRa 176,26,8192\nMvaa 32,132,200\nMvaa 32,136,208\nRa 180,26,9216\nMvaa 32,140,216\nWL 6\n"
"Cvaa 36,140,220,8\nRa 184,26,10240\nMvaa 36,136,212\nD1 1,16,46,3072\ns_add_u32 m0, s89, 32768\nMvaa 36,132,204\nRa 188,26,11264\nMvaa 36,128,196\nMvaa 36,144,228\nRa 128,2,16384\n"
"Mvaa 36,148,236\ns_add_u32 s46, s86, s88\nEX 80,60\nMvaa 36,152,244\nRa 132,2,17408\nMvaa 36,156,252\nWL 6\nEX 81,61\nMvaa 32,156,248\nRa 136,2,18432\nAD 85,85,80\n"
"Mvaa 32,152,240\nD1 1,16,46,0\nMvaa 32,148,232\nRa 140,2,19456\nMvaa 32,144,224\nAD 85,85,81\nCV 78,80,81\nCvaa 40,160,192,12\nRa 144,2,20480\nMvaa 40,164,200\nEX 82,62\n"
"Mvaa 40,168,208\nRa 148,2,21504\nMvaa 40,172,216\nWL 6\nEX 83,63\nCvaa 44,172,220,16\nRa 152,2,22528\nAD 85,85,82\nMvaa 44,168,212\nD1 1,16,46,1024\nMvaa 44,164,204\n"
"Ra 156,2,23552\nMvaa 44,160,196\nAD 85,85,83\nCV 79,82,83\nMvaa 44,176,228\nRa 160,2,24576\nMvaa 44,180,236\ns_add_u32 s88, s88, 32768\nEX 80,32\nMvaa 44,184,244\nRa 164,2,25600\n"
"v_cmp_lt_f32 vcc, s63, v85\nMvaa 44,188,252\nWL 8\nEX 81,33\nMvaa 40,188,248\nRa 168,2,26624\ns_cbranch_vccnz .T466\n.T467:\nMvaa 40,184,240\nD1 1,16,46,2048\nCV 128,128,129\n"
"CV 129,130,131\nMvaa 40,180,232\nRa 172,2,27648\nCV 130,136,137\nMvaa 40,176,224\nAD 84,80,81\nCV 64,80,81\nCV 131,138,139\nRa 176,2,28672\ns_add_u32 s5, s66, 0\nSv 128,1,16,5,0\n"
"Mvav 132,128,76\nEX 82,34\nMvav 140,132,76\nRa 180,2,29696\ns_waitcnt vmcnt(13) lgkmcnt(8)\nEX 83,35\nRa 184,2,30720\nAD 84,84,82\nMvav 148,136,76\nD1 1,16,46,3072\n"
"s_add_u32 m0, s89, 49152\nMvav 156,140,76\nRa 188,2,31744\ns_barrier\nAD 84,84,83\nCV 65,82,83\nRa 128,24,32768\nMvav 164,144,76\ns_add_u32 s46, s87, s88\nEX 80,36\n"
"CV 132,132,133\nMvav 172,148,76\nRa 132,24,33792\nCV 133,134,135\nCV 134,140,141\nWL 8\nEX 81,37\nCV 135,142,143\nCV 144,144,145\nCV 145,146,147\nCV 146,152,153\nCV 147,154,155\n"
"Ra 136,24,34816\nAD 87,87,85\ns_add_u32 s5, s66, 0\nSv 132,1,16,5,1024\ns_add_u32 s5, s66, 0\nSv 144,1,16,5,2048\nMvav 180,152,76\nD1 1,16,46,0\nv_mov_b32 v96, v86\n"
"CV 148,148,149\nMvav 188,156,76\nRa 140,24,35840\nv_mov_b32 v97, v87\nCV 149,150,151\nAD 85,80,81\nCV 68,80,81\nCV 150,156,157\nRa 144,26,32768\nv_permlane32_swap_b32 v86, v96\n"
"v_permlane32_swap_b32 v87, v97\nMvav 196,160,76\nEX 82,38\nCV 151,158,159\nMvav 204,164,76\nRa 148,26,33792\nAD 86,86,96\ns_add_u32 s5, s66, 0\nSv 148,1,16,5,3072\nWL 8\nEX 83,39\n"
"Ra 152,26,34816\nAD 85,85,82\nAD 87,87,97\nMvav 212,168,76\nD1 1,16,46,1024\nv_mov_b32 v96, v86\nCV 160,160,161\nMvav 220,172,76\nRa 156,26,35840\nv_mov_b32 v97, v87\n"
"CV 161,162,163\nCV 162,168,169\nAD 85,85,83\nCV 69,82,83\nCV 163,170,171\nCV 164,164,165\nRa 160,24,40960\nv_permlane16_swap_b32 v86, v96\nv_permlane16_swap_b32 v87, v97\n"
"s_add_u32 s5, s66, 4096\nSv 160,1,16,5,0\nMvav 228,176,76\ns_xor_b32 s89, s89, 0x10000\nEX 80,40\nMvav 236,180,76\nRa 164,24,41984\nAD 86,86,96\nCV 165,166,167\nCV 166,172,173\n"
"WL 6\nEX 81,41\nRa 168,24,43008\nAD 84,84,80\nAD 87,87,97\nCV 167,174,175\nMvav 244,184,76\nD1 1,16,46,2048\nv_cmp_gt_u32 vcc, 0x100, v1\nv_mov_b32 v94, s15\nv_mov_b32 v95, s15\n"
"v_cndmask_b32 v92, v89, v88, vcc\nv_cndmask_b32 v93, v87, v86, vcc\nMvav 252,188,76\nRa 172,24,44032\nv_mov_b32 v86, 0\ns_add_u32 s5, s66, 4096\nSv 164,1,16,5,1024\nAD 84,84,81\n"
"CV 66,80,81\nCV 176,176,177\nCvaa 48,128,192,4\nRa 176,26,40960\nv_mov_b32 v87, 0\nv_mov_b32 v88, 0\nCV 177,178,179\nMvaa 48,132,200\nEX 82,42\nCV 178,184,185\nMvaa 48,136,208\n"
"Ra 180,26,41984\nv_mov_b32 v89, 0\nCV 179,186,187\nMvaa 48,140,216\nWL 6\nEX 83,43\nCvaa 52,140,220,8\nRa 184,26,43008\nAD 84,84,82\ns_add_u32 s5, s66, 4096\nSv 176,1,16,5,2048\n"
"Mvaa 52,136,212\nD1 1,16,46,3072\ns_add_u32 m0, s89, 0\nMvaa 52,132,204\nRa 188,26,44032\nCV 180,180,181\nCV 181,182,183\nMvaa 52,128,196\nAD 84,84,83\nCV 67,82,83\n"
"Mvaa 52,144,228\nRa 128,2,49152\nCV 182,188,189\nCV 183,190,191\nMvaa 52,148,236\ns_add_u32 s46, s86, s88\nEX 80,44\nMvaa 52,152,244\nRa 132,2,50176\nv_xor_b32 v24, 0x10000, v24\n"
"s_add_u32 s5, s66, 4096\nSv 180,1,16,5,3072\nMvaa 52,156,252\nWL 6\nEX 81,45\nMvaa 48,156,248\nRa 136,2,51200\nAD 85,85,80\nCV 192,192,193\nMvaa 48,152,240\nD1 1,16,46,0\n"
"CV 193,194,195\nMvaa 48,148,232\nRa 140,2,52224\nCV 194,200,201\nCV 195,202,203\nMvaa 48,144,224\nAD 85,85,81\nCV 70,80,81\nCV 196,196,197\nCvaa 56,160,192,12\nRa 144,2,53248\n"
"s_add_u32 s5, s66, 16384\nSv 192,1,16,5,0\nMvaa 56,164,200\nEX 82,46\nMvaa 56,168,208\nRa 148,2,54272\nv_xor_b32 v26, 0x10000, v26\nCV 197,198,199\nMvaa 56,172,216\nWL 6\n"
"EX 83,47\nCvaa 60,172,220,16\nRa 152,2,55296\nAD 85,85,82\nCV 198,204,205\nMvaa 60,168,212\nD1 1,16,46,1024\nCV 199,206,207\nCV 208,208,209\nMvaa 60,164,204\nRa 156,2,56320\n"
"s_add_u32 s5, s66, 16384\nSv 196,1,16,5,1024\nMvaa 60,160,196\nAD 85,85,83\nCV 71,82,83\nCV 209,210,211\nMvaa 60,176,228\nRa 160,2,57344\nv_max_f32 v90, v84, v85\nCV 210,216,217\n"
"CV 211,218,219\nMvaa 60,180,236\ns_add_u32 s88, s88, 32768\nEX 80,48\nMvaa 60,184,244\nRa 164,2,58368\nv_cmp_lt_f32 vcc, s63, v90\nMvaa 60,188,252\nWL 8\nEX 81,49\n"
"Mvaa 56,188,248\nRa 168,2,59392\ns_cbranch_vccnz .T468\n.T469:\nv_mov_b32 v20, v84\nv_mov_b32 v21, v85\nv_mov_b32 v22, v20\nv_mov_b32 v23, v21\ns_add_u32 s5, s66, 16384\n"
"Sv 208,1,16,5,2048\nMvaa 56,184,240\nD1 1,16,46,2048\nv_permlane32_swap_b32 v20, v22\nv_permlane32_swap_b32 v21, v23\nAD 20,20,22\nAD 21,21,23\nv_mov_b32 v22, v20\n"
"v_mov_b32 v23, v21\nMvaa 56,180,232\nRa 172,2,60416\nv_permlane16_swap_b32 v20, v22\nv_permlane16_swap_b32 v21, v23\nAD 20,20,22\nAD 21,21,23\nv_min_f32 v90, v20, v21\n"
"v_cmp_gt_f32 vcc, s64, v90\ns_nop 0\ns_cbranch_vccnz .T470\n.T471:\nAD 86,86,84\nMvaa 56,176,224\nAD 84,80,81\nCV 72,80,81\nCV 212,212,213\nZvav 128,128,64\nRa 176,2,61440\n"
"CV 213,214,215\nCV 214,220,221\nZvav 132,128,68\nEX 82,50\nCV 215,222,223\nZvav 140,132,68\nRa 180,2,62464\ns_add_u32 s5, s66, 16384\nSv 212,1,16,5,3072\nZvav 136,132,64\n"
"s_waitcnt vmcnt(23) lgkmcnt(8)\nEX 83,51\nZvav 144,136,64\nRa 184,2,63488\nAD 84,84,82\nCV 224,224,225\nZvav 148,136,68\nD1 1,16,46,3072\ns_add_u32 m0, s89, 16384\nCV 225,226,227\n"
"Zvav 156,140,68\nRa 188,2,64512\ns_barrier\nCV 226,232,233\nZvav 152,140,64\nAD 84,84,83\nCV 73,82,83\nCV 227,234,235\nZvav 160,144,64\nRa 128,24,0\ns_add_u32 s5, s66, 20480\n"
"Sv 224,1,16,5,0\nZvav 164,144,68\ns_add_u32 s46, s87, s88\nEX 80,52\nZvav 172,148,68\nRa 132,24,1024\nv_xor_b32 v2, 0x10000, v2\nCV 228,228,229\nZvav 168,148,64\nWL 8\nEX 81,53\n"
"Zvav 176,152,64\nRa 136,24,2048\nAD 87,87,85\nCV 229,230,231\nZvav 180,152,68\nD1 1,16,46,0\nCV 230,236,237\nCV 231,238,239\nZvav 188,156,68\nRa 140,24,3072\n"
"s_add_u32 s5, s66, 20480\nSv 228,1,16,5,1024\nZvav 184,156,64\nAD 85,80,81\nCV 76,80,81\nZvav 192,160,64\nRa 144,26,0\nCV 240,240,241\nCV 241,242,243\nZvav 196,160,68\nEX 82,54\n"
"Zvav 204,164,68\nRa 148,26,1024\nCV 242,248,249\nCV 243,250,251\nZvav 200,164,64\nWL 8\nEX 83,55\nZvav 208,168,64\nRa 152,26,2048\nAD 85,85,82\ns_add_u32 s5, s66, 20480\n"
"Sv 240,1,16,5,2048\nZvav 212,168,68\nD1 1,16,46,1024\nCV 244,244,245\nZvav 220,172,68\nRa 156,26,3072\nCV 245,246,247\nZvav 216,172,64\nAD 85,85,83\nCV 77,82,83\nZvav 224,176,64\n"
"Ra 160,24,8192\nCV 246,252,253\nCV 247,254,255\nZvav 228,176,68\ns_sub_u32 s90, s90, 1\nEX 80,56\nZvav 236,180,68\nRa 164,24,9216\ns_cbranch_scc0 .E464\n"
"s_swappc_b64 s[80:81], s[8:9]\n.E464:\ns_add_u32 s5, s66, 20480\nSv 244,1,16,5,3072\nZvav 232,180,64\nWL 6\nEX 81,57\nZvav 240,184,64\nRa 168,24,10240\nAD 84,84,80\n"
"Zvav 244,184,68\nD1 1,16,46,2048\nZvav 252,188,68\nRa 172,24,11264\nZvav 248,188,64\nAD 84,84,81\nCV 74,80,81\ns_sub_u32 s99, s99, 1\n.p2align 6\n.A472:\nCvaa 32,128,192,4\n"
"Ra 176,26,8192\nMvaa 32,132,200\ns_add_u32 s49, s49, 131072\nEX 82,58\nMvaa 32,136,208\nRa 180,26,9216\nMvaa 32,140,216\nWL 6\n.W589:\ns_cbranch_execz .WB414\nEX 83,59\n"
"Cvaa 36,140,220,8\nRa 184,26,10240\nAD 84,84,82\nMvaa 36,136,212\nD1 1,16,46,3072\ns_add_u32 m0, s89, 32768\nMvaa 36,132,204\nRa 188,26,11264\nMvaa 36,128,196\nAD 84,84,83\n"
"CV 75,82,83\nMvaa 36,144,228\nRa 128,2,16384\nMvaa 36,148,236\ns_add_u32 s46, s86, s88\nEX 80,60\nMvaa 36,152,244\nRa 132,2,17408\nMvaa 36,156,252\nWL 6\nEX 81,61\n"
"Mvaa 32,156,248\nRa 136,2,18432\nAD 85,85,80\nMvaa 32,152,240\nD1 1,16,46,0\nMvaa 32,148,232\nRa 140,2,19456\nMvaa 32,144,224\nAD 85,85,81\nCV 78,80,81\nCvaa 40,160,192,12\n"
"Ra 144,2,20480\nMvaa 40,164,200\nSv 112,1,16,49,0\nEX 82,62\nMvaa 40,168,208\nRa 148,2,21504\nMvaa 40,172,216\nWL 6\nEX 83,63\nCvaa 44,172,220,16\nRa 152,2,22528\nAD 85,85,82\n"
"Mvaa 44,168,212\nD1 1,16,46,1024\nMvaa 44,164,204\nRa 156,2,23552\nMvaa 44,160,196\nAD 85,85,83\nCV 79,82,83\nMvaa 44,176,228\nRa 160,2,24576\nv_max_f32 v90, v84, v85\n"
"Mvaa 44,180,236\ns_add_u32 s88, s88, 32768\nEX 80,32\nMvaa 44,184,244\nRa 164,2,25600\nv_cmp_lt_f32 vcc, s63, v90\nMvaa 44,188,252\nWL 8\nEX 81,33\nMvaa 40,188,248\n"
"Ra 168,2,26624\ns_cbranch_vccnz .T482\n.T483:\nMvaa 40,184,240\nD1 1,16,46,2048\nMvaa 40,180,232\nRa 172,2,27648\nAD 86,86,84\nMvaa 40,176,224\nAD 84,80,81\nCV 64,80,81\n"
"Mvav 128,128,72\nRa 176,2,28672\nMvav 132,128,76\nEX 82,34\nMvav 140,132,76\nRa 180,2,29696\nMvav 136,132,72\ns_waitcnt vmcnt(13) lgkmcnt(8)\nEX 83,35\nMvav 144,136,72\n"
"Ra 184,2,30720\nAD 84,84,82\nMvav 148,136,76\nD1 1,16,46,3072\ns_add_u32 m0, s89, 49152\nMvav 156,140,76\nRa 188,2,31744\ns_barrier\nMvav 152,140,72\nAD 84,84,83\nCV 65,82,83\n"
"Mvav 160,144,72\nRa 128,24,32768\nMvav 164,144,76\ns_add_u32 s46, s87, s88\nEX 80,36\nMvav 172,148,76\nRa 132,24,33792\nMvav 168,148,72\nWL 8\nEX 81,37\nMvav 176,152,72\n"
"Ra 136,24,34816\nAD 87,87,85\nMvav 180,152,76\nD1 1,16,46,0\nMvav 188,156,76\nRa 140,24,35840\nMvav 184,156,72\nAD 85,80,81\nCV 68,80,81\nMvav 192,160,72\nRa 144,26,32768\n"
"Mvav 196,160,76\ns_cmp_eq_u32 s99, 0\nEX 82,38\nMvav 204,164,76\nRa 148,26,33792\ns_cbranch_scc1 .E474\n.E475:\nMvav 200,164,72\nWL 8\nEX 83,39\nMvav 208,168,72\nRa 152,26,34816\n"
"AD 85,85,82\nMvav 212,168,76\nD1 1,16,46,1024\nMvav 220,172,76\nRa 156,26,35840\nMvav 216,172,72\nAD 85,85,83\nCV 69,82,83\nMvav 224,176,72\nRa 160,24,40960\nMvav 228,176,76\n"
"s_xor_b32 s89, s89, 0x10000\nEX 80,40\nMvav 236,180,76\nRa 164,24,41984\nMvav 232,180,72\nWL 6\nEX 81,41\nMvav 240,184,72\nRa 168,24,43008\nAD 84,84,80\nMvav 244,184,76\n"
"D1 1,16,46,2048\nMvav 252,188,76\nRa 172,24,44032\nMvav 248,188,72\nAD 84,84,81\nCV 66,80,81\nCvaa 48,128,192,4\nRa 176,26,40960\nMvaa 48,132,200\ns_cmp_eq_u32 s99, 0\nEX 82,42\n"
"Mvaa 48,136,208\nRa 180,26,41984\ns_cselect_b32 s86, s94, s86\nMvaa 48,140,216\nWL 6\nEX 83,43\nCvaa 52,140,220,8\nRa 184,26,43008\nAD 84,84,82\nMvaa 52,136,212\nD1 1,16,46,3072\n"
"s_add_u32 m0, s89, 0\nMvaa 52,132,204\nRa 188,26,44032\nMvaa 52,128,196\nAD 84,84,83\nCV 67,82,83\nMvaa 52,144,228\nRa 128,2,49152\nMvaa 52,148,236\ns_add_u32 s46, s86, s88\n"
"EX 80,44\nMvaa 52,152,244\nRa 132,2,50176\nv_xor_b32 v24, 0x10000, v24\nMvaa 52,156,252\nWL 6\nEX 81,45\nMvaa 48,156,248\nRa 136,2,51200\nAD 85,85,80\nMvaa 48,152,240\n"
"D1 1,16,46,0\nMvaa 48,148,232\nRa 140,2,52224\nMvaa 48,144,224\nAD 85,85,81\nCV 70,80,81\nCvaa 56,160,192,12\nRa 144,2,53248\nMvaa 56,164,200\nSv 112,1,16,49,1024\nEX 82,46\n"
"Mvaa 56,168,208\nRa 148,2,54272\nv_xor_b32 v26, 0x10000, v26\nMvaa 56,172,216\nWL 6\nEX 83,47\nCvaa 60,172,220,16\nRa 152,2,55296\nAD 85,85,82\nMvaa 60,168,212\nD1 1,16,46,1024\n"
"Mvaa 60,164,204\nRa 156,2,56320\nMvaa 60,160,196\nAD 85,85,83\nCV 71,82,83\nMvaa 60,176,228\nRa 160,2,57344\nv_max_f32 v90, v84, v85\nMvaa 60,180,236\ns_add_u32 s88, s88, 32768\n"
"EX 80,48\nMvaa 60,184,244\nRa 164,2,58368\nv_cmp_lt_f32 vcc, s63, v90\nMvaa 60,188,252\nWL 8\nEX 81,49\nMvaa 56,188,248\nRa 168,2,59392\ns_cbranch_vccnz .T484\n.T485:\n"
"Mvaa 56,184,240\nD1 1,16,46,2048\nMvaa 56,180,232\nRa 172,2,60416\nAD 86,86,84\nMvaa 56,176,224\nAD 84,80,81\nCV 72,80,81\nMvav 128,128,64\nRa 176,2,61440\nMvav 132,128,68\n"
"s_cmp_eq_u32 s99, s98\nEX 82,50\nMvav 140,132,68\nRa 180,2,62464\ns_cbranch_scc1 .E480\n.E481:\nMvav 136,132,64\ns_waitcnt vmcnt(13) lgkmcnt(8)\nEX 83,51\nMvav 144,136,64\n"
"Ra 184,2,63488\nAD 84,84,82\nMvav 148,136,68\nD1 1,16,46,3072\ns_add_u32 m0, s89, 16384\nMvav 156,140,68\nRa 188,2,64512\ns_barrier\nMvav 152,140,64\nAD 84,84,83\nCV 73,82,83\n"
"Mvav 160,144,64\nRa 128,24,0\nMvav 164,144,68\ns_add_u32 s46, s87, s88\nEX 80,52\nMvav 172,148,68\nRa 132,24,1024\nv_xor_b32 v2, 0x10000, v2\nMvav 168,148,64\nWL 8\nEX 81,53\n"
"Mvav 176,152,64\nRa 136,24,2048\nAD 87,87,85\nMvav 180,152,68\nD1 1,16,46,0\nMvav 188,156,68\nRa 140,24,3072\nMvav 184,156,64\nAD 85,80,81\nCV 76,80,81\nMvav 192,160,64\n"
"Ra 144,26,0\nMvav 196,160,68\ns_cmp_lt_u32 s99, s92\nEX 82,54\nMvav 204,164,68\nRa 148,26,1024\ns_cbranch_scc1 .E476\n.E477:\nMvav 200,164,64\nWL 8\nEX 83,55\nMvav 208,168,64\n"
"Ra 152,26,2048\nAD 85,85,82\nMvav 212,168,68\nD1 1,16,46,1024\nMvav 220,172,68\nRa 156,26,3072\nMvav 216,172,64\nAD 85,85,83\nCV 77,82,83\nMvav 224,176,64\nRa 160,24,8192\n"
"Mvav 228,176,68\ns_sub_u32 s90, s90, 1\nEX 80,56\nMvav 236,180,68\nRa 164,24,9216\ns_cbranch_scc1 .E478\n.E479:\nMvav 232,180,64\nWL 6\nEX 81,57\nMvav 240,184,64\nRa 168,24,10240\n"
"AD 84,84,80\nMvav 244,184,68\nD1 1,16,46,2048\ns_sub_u32 s99, s99, 1\nMvav 252,188,68\nRa 172,24,11264\nMvav 248,188,64\nAD 84,84,81\nCV 74,80,81\ns_cbranch_scc0 .A472\n"
"s_branch .A486\n.E474:\nv_cmpx_gt_i32 vcc, 19, v0\nv_mov_b32 v15, s97\nv_cmpx_gt_i32 vcc, 18, v0\nv_mov_b32 v14, s97\nv_cmpx_gt_i32 vcc, 17, v0\nv_mov_b32 v13, s97\n"
"v_cmpx_gt_i32 vcc, 16, v0\nv_mov_b32 v12, s97\nv_cmpx_gt_i32 vcc, 3, v0\nv_mov_b32 v7, s97\nv_cmpx_gt_i32 vcc, 2, v0\nv_mov_b32 v6, s97\nv_cmpx_gt_i32 vcc, 1, v0\n"
"v_mov_b32 v5, s97\nv_cmpx_gt_i32 vcc, 0, v0\nv_mov_b32 v4, s97\ns_mov_b64 exec, -1\ns_nop 2\ns_branch .E475\n.E476:\ns_cmp_eq_u32 s99, 0\ns_cbranch_scc1 .K473\n"
"v_cmpx_gt_i32 vcc, 19, v3\nv_mov_b32 v15, s97\nv_cmpx_gt_i32 vcc, 18, v3\nv_mov_b32 v14, s97\nv_cmpx_gt_i32 vcc, 17, v3\nv_mov_b32 v13, s97\nv_cmpx_gt_i32 vcc, 16, v3\n"
"v_mov_b32 v12, s97\nv_cmpx_gt_i32 vcc, 3, v3\nv_mov_b32 v7, s97\nv_cmpx_gt_i32 vcc, 2, v3\nv_mov_b32 v6, s97\nv_cmpx_gt_i32 vcc, 1, v3\nv_mov_b32 v5, s97\n"
"v_cmpx_gt_i32 vcc, 0, v3\nv_mov_b32 v4, s97\ns_mov_b64 exec, -1\ns_nop 2\ns_branch .E477\n.K473:\nv_cmpx_gt_i32 vcc, 19, v3\nv_mov_b32 v19, s97\nv_cmpx_gt_i32 vcc, 18, v3\n"
"v_mov_b32 v18, s97\nv_cmpx_gt_i32 vcc, 17, v3\nv_mov_b32 v17, s97\nv_cmpx_gt_i32 vcc, 16, v3\nv_mov_b32 v16, s97\nv_cmpx_gt_i32 vcc, 3, v3\nv_mov_b32 v11, s97\n"
"v_cmpx_gt_i32 vcc, 2, v3\nv_mov_b32 v10, s97\nv_cmpx_gt_i32 vcc, 1, v3\nv_mov_b32 v9, s97\nv_cmpx_gt_i32 vcc, 0, v3\nv_mov_b32 v8, s97\ns_mov_b64 exec, -1\ns_nop 2\n"
"s_branch .E477\n.E478:\ns_swappc_b64 s[80:81], s[8:9]\ns_branch .E479\n.E480:\nv_mov_b32 v91, 0x40000000\nv_cmp_gt_u32 vcc, 0x200, v1\nWV 15\ns_nop 0\n"
"v_cndmask_b32 v91, v91, v1, vcc\nSv 92,91,16,65,0\ns_branch .E481\n.A486:\n.LE416:\nRa 176,26,8192\nEX 82,58\nRa 180,26,9216\nWL 6\nEX 83,59\nCvaa 36,140,220,8\nRa 184,26,10240\n"
"AD 84,84,82\nMvaa 36,136,212\nD1 1,16,46,3072\ns_add_u32 m0, s89, 32768\nMvaa 36,132,204\nRa 188,26,11264\nMvaa 36,128,196\nAD 84,84,83\nCV 75,82,83\nMvaa 36,144,228\n"
"Ra 128,2,16384\nMvaa 36,148,236\ns_add_u32 s46, s86, s88\nEX 80,60\nMvaa 36,152,244\nRa 132,2,17408\nMvaa 36,156,252\nEX 81,61\nRa 136,2,18432\nAD 85,85,80\nD1 1,16,46,0\n"
"Ra 140,2,19456\nAD 85,85,81\nCV 78,80,81\nRa 144,2,20480\ns_mov_b32 s87, s95\nEX 82,62\nRa 148,2,21504\nWL 6\nEX 83,63\nCvaa 44,172,220,16\nRa 152,2,22528\nAD 85,85,82\n"
"Mvaa 44,168,212\nD1 1,16,46,1024\nMvaa 44,164,204\nRa 156,2,23552\nMvaa 44,160,196\nAD 85,85,83\nCV 79,82,83\nMvaa 44,176,228\nRa 160,2,24576\nv_max_f32 v90, v84, v85\n"
"Mvaa 44,180,236\ns_add_u32 s88, s88, 32768\nMvaa 44,184,244\nRa 164,2,25600\nv_cmp_lt_f32 vcc, s63, v90\nMvaa 44,188,252\nWL 8\nRa 168,2,26624\ns_cbranch_vccnz .T488\n.T489:\n"
"D1 1,16,46,2048\nRa 172,2,27648\nAD 86,86,84\nMvav 128,128,72\nRa 176,2,28672\nMvav 132,128,76\nMvav 140,132,76\nRa 180,2,29696\nMvav 136,132,72\ns_waitcnt vmcnt(12) lgkmcnt(8)\n"
"Mvav 144,136,72\nRa 184,2,30720\nMvav 148,136,76\nD1 1,16,46,3072\ns_add_u32 m0, s89, 49152\nMvav 156,140,76\nRa 188,2,31744\ns_barrier\nMvav 152,140,72\nMvav 160,144,72\n"
"Ra 128,24,32768\nMvav 164,144,76\ns_add_u32 s46, s87, s88\nEX 80,36\nMvav 172,148,76\nRa 132,24,33792\nMvav 168,148,72\nWL 8\nEX 81,37\nMvav 176,152,72\nRa 136,24,34816\n"
"AD 87,87,85\nMvav 180,152,76\nD1 1,16,46,0\nMvav 188,156,76\nRa 140,24,35840\nMvav 184,156,72\nAD 85,80,81\nCV 68,80,81\nMvav 192,160,72\nRa 144,26,32768\nMvav 196,160,76\n"
"s_nop 0\nEX 82,38\nMvav 204,164,76\nRa 148,26,33792\nv_cmpx_gt_i32 vcc, 19, v0\nv_mov_b32 v19, s97\nv_cmpx_gt_i32 vcc, 18, v0\nv_mov_b32 v18, s97\nv_cmpx_gt_i32 vcc, 17, v0\n"
"v_mov_b32 v17, s97\nv_cmpx_gt_i32 vcc, 16, v0\nv_mov_b32 v16, s97\nv_cmpx_gt_i32 vcc, 3, v0\nv_mov_b32 v11, s97\nv_cmpx_gt_i32 vcc, 2, v0\nv_mov_b32 v10, s97\n"
"v_cmpx_gt_i32 vcc, 1, v0\nv_mov_b32 v9, s97\nv_cmpx_gt_i32 vcc, 0, v0\nv_mov_b32 v8, s97\ns_mov_b64 exec, -1\ns_nop 2\ns_nop 0\nMvav 200,164,72\nWL 8\nEX 83,39\nMvav 208,168,72\n"
"Ra 152,26,34816\nAD 85,85,82\nMvav 212,168,76\nD1 1,16,46,1024\nMvav 220,172,76\nRa 156,26,35840\nMvav 216,172,72\nAD 85,85,83\nCV 69,82,83\nMvav 224,176,72\nRa 160,24,40960\n"
"Mvav 228,176,76\ns_xor_b32 s89, s89, 0x10000\nMvav 236,180,76\nRa 164,24,41984\nMvav 232,180,72\nWL 10\nMvav 240,184,72\nRa 168,24,43008\nMvav 244,184,76\nD1 1,16,46,2048\n"
"Mvav 252,188,76\nRa 172,24,44032\nMvav 248,188,72\nRa 176,26,40960\nRa 180,26,41984\nWL 6\nCvaa 52,140,220,8\nRa 184,26,43008\nMvaa 52,136,212\nD1 1,16,46,3072\n"
"s_add_u32 m0, s89, 0\nMvaa 52,132,204\nRa 188,26,44032\nMvaa 52,128,196\nMvaa 52,144,228\nRa 128,2,49152\nMvaa 52,148,236\ns_add_u32 s46, s86, s88\nEX 80,44\nMvaa 52,152,244\n"
"Ra 132,2,50176\nv_xor_b32 v24, 0x10000, v24\nMvaa 52,156,252\nEX 81,45\nRa 136,2,51200\nAD 85,85,80\nD1 1,16,46,0\nRa 140,2,52224\nAD 85,85,81\nCV 70,80,81\nRa 144,2,53248\n"
"EX 82,46\nRa 148,2,54272\nv_xor_b32 v26, 0x10000, v26\nWL 6\nEX 83,47\nCvaa 60,172,220,16\nRa 152,2,55296\nAD 85,85,82\nMvaa 60,168,212\nD1 1,16,46,1024\nMvaa 60,164,204\n"
"Ra 156,2,56320\nMvaa 60,160,196\nAD 85,85,83\nCV 71,82,83\nMvaa 60,176,228\nRa 160,2,57344\nMvaa 60,180,236\ns_add_u32 s88, s88, 32768\nMvaa 60,184,244\nRa 164,2,58368\n"
"v_cmp_lt_f32 vcc, s63, v85\nMvaa 60,188,252\nWL 8\nRa 168,2,59392\ns_cbranch_vccnz .T490\n.T491:\nD1 1,16,46,2048\nRa 172,2,60416\nRa 176,2,61440\nMvav 132,128,68\n"
"Mvav 140,132,68\nRa 180,2,62464\ns_waitcnt vmcnt(11) lgkmcnt(8)\nRa 184,2,63488\nMvav 148,136,68\nD1 1,16,46,3072\ns_add_u32 m0, s89, 16384\nMvav 156,140,68\nRa 188,2,64512\n"
"s_barrier\nRa 128,24,0\nMvav 164,144,68\ns_add_u32 s46, s87, s88\nEX 80,52\nMvav 172,148,68\nRa 132,24,1024\nv_xor_b32 v2, 0x10000, v2\nWL 8\nEX 81,53\nRa 136,24,2048\n"
"AD 87,87,85\nMvav 180,152,68\nD1 1,16,46,0\nMvav 188,156,68\n.W590:\ns_cbranch_execz .WB414\nRa 140,24,3072\nAD 85,80,81\nCV 76,80,81\nRa 144,26,0\nMvav 196,160,68\nEX 82,54\n"
"Mvav 204,164,68\nRa 148,26,1024\nWL 8\nEX 83,55\nRa 152,26,2048\nAD 85,85,82\nMvav 212,168,68\nD1 1,16,46,1024\nMvav 220,172,68\nRa 156,26,3072\nAD 85,85,83\nCV 77,82,83\n"
"Ra 160,24,8192\nMvav 228,176,68\nMvav 236,180,68\nRa 164,24,9216\nWL 10\nRa 168,24,10240\nMvav 244,184,68\nD1 1,16,46,2048\nMvav 252,188,68\nRa 172,24,11264\nD1 1,16,46,3072\n"
"s_add_u32 m0, s89, 32768\ns_add_u32 s46, s86, s88\nRa 128,2,16384\nv_xor_b32 v3, 0x10000, v2\nv_add_u32 v0, 0x20000, v1\nEX 80,60\nRa 132,2,17408\ns_sub_u32 s94, s49, 1703936\n"
"EX 81,61\nRa 136,2,18432\nAD 85,85,80\ns_xor_b32 s92, s94, 0x202000\nD1 1,16,46,0\ns_lshl_b32 s89, s11, 12\nRa 140,2,19456\ns_lshl1_add_u32 s2, s12, 1\nAD 85,85,81\nCV 78,80,81\n"
"Ra 144,2,20480\ns_lshl3_add_u32 s2, s2, s13\nEX 82,62\nRa 148,2,21504\ns_lshl4_add_u32 s2, s2, 0xc048200\nEX 83,63\nRa 152,2,22528\nAD 85,85,82\nD1 1,16,46,1024\nRa 156,2,23552\n"
"s_and_b32 s3, s72, 0x2000\nAD 85,85,83\nCV 79,82,83\nRa 160,2,24576\ns_cmp_eq_u32 s11, 1\ns_cselect_b32 s54, s2, s54\ns_cselect_b32 s3, s3, 0\nv_cmp_lt_f32 vcc, s63, v85\n"
"Ra 164,2,25600\ns_cbranch_vccnz .T492\n.T493:\nAD 87,87,85\nWL 8\nv_mov_b32 v84, v86\nv_mov_b32 v85, v87\nRa 168,2,26624\nv_permlane32_swap_b32 v86, v84\n"
"v_permlane32_swap_b32 v87, v85\nD1 1,16,46,2048\nAD 86,86,84\nRa 172,2,27648\nAD 87,87,85\nv_mov_b32 v84, v86\nv_mov_b32 v85, v87\nRa 176,2,28672\nv_permlane16_swap_b32 v86, v84\n"
"v_permlane16_swap_b32 v87, v85\nMvav 132,128,76\nAD 86,86,84\nAD 87,87,85\nMvav 140,132,76\nRa 180,2,29696\nv_rcp_f32 v86, v86\nWL 8\nv_rcp_f32 v87, v87\nRa 184,2,30720\n"
"v_xor_b32 v25, s3, v25\nMvav 148,136,76\nD1 1,16,46,3072\nMvav 156,140,76\nRa 188,2,31744\nMU 128,128,86\nMU 129,129,86\nCV 96,128,129\nMU 130,130,86\nMU 131,131,86\nRa 128,2,0\n"
"CV 97,130,131\nMU 132,132,87\nMvav 164,144,76\nMU 133,133,87\nCV 100,132,133\nMvav 172,148,76\nRa 132,2,1024\nMU 134,134,87\nMU 135,135,87\nWL 8\ns_add_u32 m0, s89, 131072\n"
"s_add_u32 s46, s96, 1212416\nRa 136,2,2048\nCV 101,134,135\nMU 136,136,86\nMvav 180,152,76\nD1 1,16,46,0\nMU 137,137,86\nMvav 188,156,76\nRa 140,2,3072\nCV 98,136,137\n"
"MU 138,138,86\nMU 139,139,86\nCV 99,138,139\nMU 140,140,87\nRa 144,2,4096\nMU 141,141,87\nCV 102,140,141\nMvav 196,160,76\nMU 142,142,87\nMU 143,143,87\nMvav 204,164,76\n"
"Ra 148,2,5120\nCV 103,142,143\nMU 144,144,86\nWL 8\nMU 145,145,86\nCV 104,144,145\nRa 152,2,6144\nMU 146,146,86\nMU 147,147,86\nMvav 212,168,76\nD1 1,16,46,1024\nCV 105,146,147\n"
"Mvav 220,172,76\nRa 156,2,7168\nMU 148,148,87\nMU 149,149,87\nCV 108,148,149\nMU 150,150,87\nMU 151,151,87\nRa 160,2,8192\nCV 109,150,151\nMU 152,152,86\nMvav 228,176,76\n"
"MU 153,153,86\nCV 106,152,153\nMvav 236,180,76\nRa 164,2,9216\nMU 154,154,86\nMU 155,155,86\nWL 8\nCV 107,154,155\nMU 156,156,87\nRa 168,2,10240\nMU 157,157,87\nCV 110,156,157\n"
"Mvav 244,184,76\nD1 1,16,46,2048\nMU 158,158,87\nMvav 252,188,76\nRa 172,2,11264\nMU 159,159,87\nCV 111,158,159\nMU 160,160,86\nMU 161,161,86\nCV 112,160,161\nZvav 128,128,96\n"
"Ra 176,2,12288\nMU 162,162,86\nMU 163,163,86\nMvav 128,132,104\nCV 113,162,163\nMU 164,164,87\nZvav 132,132,108\nRa 180,2,13312\nMU 165,165,87\nCV 116,164,165\nMvav 132,128,100\n"
"s_waitcnt vmcnt(15) lgkmcnt(8)\nMU 166,166,87\nMU 167,167,87\nZvav 140,136,100\nRa 184,2,14336\nCV 117,166,167\nMU 168,168,86\nMvav 140,140,108\nD1 1,16,46,3072\nMU 169,169,86\n"
"CV 114,168,169\nZvav 136,140,104\nRa 188,2,15360\ns_barrier\nMU 170,170,86\nMvav 136,136,96\nMU 171,171,86\nCV 115,170,171\nMU 172,172,87\nZvav 144,144,96\nRa 128,2,32768\n"
"MU 173,173,87\nCV 118,172,173\nMvav 144,148,104\nMU 174,174,87\nMU 175,175,87\nbuffer_store_dword v25, off, s[16:19], s54 offset:512 sc0 sc1\nZvav 148,148,108\nRa 132,2,33792\n"
"CV 119,174,175\nMU 176,176,86\nMvav 148,144,100\nWL 8\ns_add_u32 m0, s89, 49152\ns_add_u32 s46, s96, 1179648\nZvav 156,152,100\nRa 136,2,34816\nMU 177,177,86\nCV 120,176,177\n"
"Mvav 156,156,108\nD1 1,16,46,0\nMU 178,178,86\nMU 179,179,86\nZvav 152,156,104\nRa 140,2,35840\nCV 121,178,179\nMU 180,180,87\nMvav 152,152,96\nMU 181,181,87\nCV 124,180,181\n"
"MU 182,182,87\nZvav 64,160,96\nRa 144,2,36864\nMU 183,183,87\nCV 125,182,183\nMvav 64,164,104\nMU 184,184,86\nMU 185,185,86\nZvav 68,164,108\nRa 148,2,37888\nCV 122,184,185\n"
"MU 186,186,86\nMvav 68,160,100\nWL 8\nMU 187,187,86\nCV 123,186,187\nZvav 76,168,100\nRa 152,2,38912\nMU 188,188,87\nMU 189,189,87\nMvav 76,172,108\nD1 1,16,46,1024\n"
"CV 126,188,189\nMU 190,190,87\nZvav 72,172,104\nRa 156,2,39936\nMU 191,191,87\nCV 127,190,191\nMvav 72,168,96\nMU 192,192,86\nMU 193,193,86\nCV 32,192,193\nMvav 128,176,112\n"
"Ra 160,2,40960\nMU 194,194,86\nMU 195,195,86\nMvav 128,180,120\nCV 33,194,195\nMU 196,196,87\nMU 197,197,87\nMvav 132,180,124\nRa 164,2,41984\nCV 36,196,197\nMU 198,198,87\n"
"Mvav 132,176,116\nWL 8\nMU 199,199,87\nMvav 140,184,116\nRa 168,2,43008\nCV 37,198,199\nMU 200,200,86\nMvav 140,188,124\nD1 1,16,46,2048\nMU 201,201,86\nCV 34,200,201\n"
"Mvav 136,188,120\nRa 172,2,44032\nMU 202,202,86\nMU 203,203,86\nMvav 136,184,112\nCV 35,202,203\nMU 204,204,87\nMU 205,205,87\nMvav 144,128,112\nRa 176,2,45056\nCV 38,204,205\n"
"MU 206,206,87\nMvav 144,132,120\nMU 207,207,87\nCV 39,206,207\nMvav 148,132,124\nRa 180,2,46080\nMU 208,208,86\nMU 209,209,86\nMvav 148,128,116\nWL 8\nCV 40,208,209\n"
"MU 210,210,86\nMvav 156,136,116\nRa 184,2,47104\nMU 211,211,86\nCV 41,210,211\nMvav 156,140,124\nD1 1,16,46,3072\nMU 212,212,87\nMU 213,213,87\nMvav 152,140,120\nRa 188,2,48128\n"
"CV 44,212,213\nMvav 152,136,112\nMU 214,214,87\nMU 215,215,87\nCV 45,214,215\nMvav 64,144,112\nRa 128,2,49152\nMU 216,216,86\nMU 217,217,86\nMvav 64,148,120\nCV 42,216,217\n"
"MU 218,218,86\nMU 219,219,86\nMvav 68,148,124\nRa 132,2,50176\nCV 43,218,219\nMU 220,220,87\nMvav 68,144,116\nWL 8\ns_add_u32 m0, s89, 147456\ns_add_u32 s46, s96, 1245184\n"
"Mvav 76,152,116\nRa 136,2,51200\nMU 221,221,87\nCV 46,220,221\nMvav 76,156,124\nD1 1,16,46,0\nMU 222,222,87\nMU 223,223,87\nMvav 72,156,120\nRa 140,2,52224\nCV 47,222,223\n"
"Mvav 72,152,112\nMU 224,224,86\nMU 225,225,86\nCV 48,224,225\nMvav 128,160,32\nRa 144,2,53248\nMU 226,226,86\nMU 227,227,86\nMvav 128,164,40\nCV 49,226,227\nMU 228,228,87\n"
"Mvav 132,164,44\nRa 148,2,54272\nMU 229,229,87\nCV 52,228,229\nMvav 132,160,36\nWL 8\nMU 230,230,87\nMU 231,231,87\nMvav 140,168,36\nRa 152,2,55296\nCV 53,230,231\nMU 232,232,86\n"
"Mvav 140,172,44\nD1 1,16,46,1024\nMU 233,233,86\nCV 50,232,233\nMvav 136,172,40\nRa 156,2,56320\nMU 234,234,86\nMvav 136,168,32\nMU 235,235,86\nCV 51,234,235\nMU 236,236,87\n"
"Mvav 144,176,32\nRa 160,2,57344\nMU 237,237,87\nCV 54,236,237\nMvav 144,180,40\nMU 238,238,87\nMU 239,239,87\nCV 55,238,239\nMvav 148,180,44\nRa 164,2,58368\nMU 240,240,86\n"
"MU 241,241,86\nMvav 148,176,36\nWL 8\nCV 56,240,241\nMvav 156,184,36\nRa 168,2,59392\nMU 242,242,86\nMU 243,243,86\nMvav 156,188,44\nD1 1,16,46,2048\nCV 57,242,243\nMU 244,244,87\n"
"Mvav 152,188,40\nRa 172,2,60416\nMU 245,245,87\nCV 60,244,245\nMvav 152,184,32\nMU 246,246,87\nMU 247,247,87\nCV 61,246,247\nMvav 64,128,32\nRa 176,2,61440\nMU 248,248,86\n"
"MU 249,249,86\nMvav 64,132,40\nCV 58,248,249\nMU 250,250,86\nMvav 68,132,44\nRa 180,2,62464\nMU 251,251,86\nCV 59,250,251\nMvav 68,128,36\ns_waitcnt vmcnt(16) lgkmcnt(8)\n"
"MU 252,252,87\nMU 253,253,87\nMvav 76,136,36\nRa 184,2,63488\nCV 62,252,253\nMU 254,254,87\nMvav 76,140,44\nD1 1,16,46,3072\nMU 255,255,87\nCV 63,254,255\nMvav 72,140,40\n"
"Ra 188,2,64512\ns_barrier\nMvav 72,136,32\nMvav 128,144,48\nRa 128,3,0\nMvav 128,148,56\nMvav 132,148,60\nRa 132,3,1024\nMvav 132,144,52\nWL 8\nMvav 140,152,52\nRa 136,3,2048\n"
"Mvav 140,156,60\nMvav 136,156,56\nRa 140,3,3072\nMvav 136,152,48\nMvav 144,160,48\nRa 144,3,4096\nMvav 144,164,56\nMvav 148,164,60\nRa 148,3,5120\nCV 132,132,133\nMvav 148,160,52\n"
"WL 8\nCV 133,134,135\nMvav 156,168,52\nRa 152,3,6144\nCV 134,140,141\nMvav 156,172,60\nCV 135,142,143\ns_add_u32 s5, s94, 0\nMvav 152,172,56\nRa 156,3,7168\nSv 132,1,16,5,1024\n"
"Mvav 152,168,48\nCV 128,128,129\nCV 129,130,131\nMvav 64,176,48\nRa 160,3,8192\nCV 130,136,137\nMvav 64,180,56\nbuffer_load_dword v27, off, s[16:19], s60 offset:512 sc0 sc1\n"
"CV 131,138,139\nMvav 68,180,60\nRa 164,3,9216\nSv 128,1,16,5,0\nMvav 68,176,52\nWL 6\nCV 148,148,149\nMvav 76,184,52\nRa 168,3,10240\nCV 149,150,151\nMvav 76,188,60\n"
"CV 150,156,157\nCV 151,158,159\nMvav 72,188,56\nRa 172,3,11264\ns_add_u32 s5, s94, 131072\nMvav 72,184,48\nSv 148,1,16,5,1024\nCV 144,144,145\nZvav 160,128,96\nRa 176,3,12288\n"
"CV 145,146,147\nMvav 160,132,104\nCV 146,152,153\nCV 147,154,155\nMvav 160,136,112\nRa 180,3,13312\nSv 144,1,16,5,0\nMvav 160,140,120\nWL 6\nCV 68,68,69\nZvav 164,140,124\n"
"Ra 184,3,14336\nCV 69,70,71\nMvav 164,136,116\nCV 70,76,77\nCV 71,78,79\nMvav 164,132,108\nRa 188,3,15360\ns_add_u32 s5, s94, 262144\nMvav 164,128,100\nSv 68,1,16,5,1024\n"
"CV 64,64,65\nMvav 164,144,36\nRa 128,3,16384\nCV 65,66,67\nMvav 164,148,44\nCV 66,72,73\nCV 67,74,75\nMvav 164,152,52\nRa 132,3,17408\nSv 64,1,16,5,0\nMvav 164,156,60\nWL 6\n"
"Mvav 160,156,56\nRa 136,3,18432\nMvav 160,152,48\nMvav 160,148,40\nRa 140,3,19456\nMvav 160,144,32\nZvav 168,160,96\nRa 144,3,20480\nMvav 168,164,104\nMvav 168,168,112\n"
"Ra 148,3,21504\nMvav 168,172,120\nWL 6\nZvav 172,172,124\nRa 152,3,22528\nMvav 172,168,116\nMvav 172,164,108\nRa 156,3,23552\nMvav 172,160,100\nMvav 172,176,36\nRa 160,3,24576\n"
"Mvav 172,180,44\nMvav 172,184,52\nRa 164,3,25600\nMvav 172,188,60\nWL 6\nMvav 168,188,56\nRa 168,3,26624\nMvav 168,184,48\nMvav 168,180,40\nRa 172,3,27648\nMvav 168,176,32\n"
"Zvav 176,128,96\nRa 176,3,28672\nCV 164,164,165\nMvav 176,132,104\nCV 165,166,167\nCV 166,172,173\nMvav 176,136,112\nRa 180,3,29696\nCV 167,174,175\nMvav 176,140,120\n"
"s_waitcnt vmcnt(11) lgkmcnt(6)\ns_add_u32 s5, s94, 393216\nZvav 180,140,124\nRa 184,3,30720\nSv 164,1,16,5,1024\nMvav 180,136,116\nCV 160,160,161\nCV 161,162,163\n"
"Mvav 180,132,108\nRa 188,3,31744\ns_barrier\nMvav 180,128,100\nCV 162,168,169\nCV 163,170,171\nMvav 180,144,36\nRa 128,3,32768\nSv 160,1,16,5,0\nMvav 180,148,44\nMvav 180,152,52\n"
"Ra 132,3,33792\n.W591:\ns_cbranch_execz .WB414\nMvav 180,156,60\nWL 6\ns_add_u32 m0, s89, 81920\ns_add_u32 s46, s96, 1277952\nMvav 176,156,56\nRa 136,3,34816\nMvav 176,152,48\n"
"D1 1,16,46,0\nMvav 176,148,40\nRa 140,3,35840\nMvav 176,144,32\nZvav 184,160,96\nRa 144,3,36864\nMvav 184,164,104\nMvav 184,168,112\nRa 148,3,37888\nMvav 184,172,120\nWL 6\n"
"Zvav 188,172,124\nRa 152,3,38912\nMvav 188,168,116\nD1 1,16,46,1024\nMvav 188,164,108\nRa 156,3,39936\nMvav 188,160,100\nMvav 188,176,36\nRa 160,3,40960\nWV 9\n"
"v_cmp_ne_u32 vcc, s15, v27\ns_nop 0\ns_cbranch_vccnz .S518\n.S519:\nMvav 188,180,44\ns_add_u32 s4, s92, 0\nMvav 188,184,52\nRa 164,3,41984\nMvav 188,188,60\nWL 6\n"
"buffer_load_dwordx4 v[208:211], v1, s[16:19], s4 offen offset:0 sc0 sc1\nMvav 184,188,56\nRa 168,3,43008\nMvav 184,184,48\nD1 1,16,46,2048\nMvav 184,180,40\nRa 172,3,44032\n"
"Mvav 184,176,32\nbuffer_load_dwordx4 v[212:215], v1, s[16:19], s4 offen offset:1024 sc0 sc1\nZvav 192,128,96\nRa 176,3,45056\nv_lshrrev_b32 v88, 8, v1\nMvav 192,132,104\n"
"v_lshlrev_b32 v89, 5, v88\nCV 180,180,181\nMvav 192,136,112\nRa 180,3,46080\nCV 181,182,183\nMvav 192,140,120\nWL 6\nCV 182,188,189\nZvav 196,140,124\nRa 184,3,47104\n"
"v_bfe_u32 v90, v1, 4, 4\nMvav 196,136,116\nD1 1,16,46,3072\nCV 183,190,191\nMvav 196,132,108\nRa 188,3,48128\ns_add_u32 s5, s94, 524288\nMvav 196,128,100\n"
"v_mul_u32_u24 v91, 896, v90\nSv 180,1,16,5,1024\nMvav 196,144,36\nRa 128,3,49152\nv_lshl_add_u32 v91, v88, 4, v91\nMvav 196,148,44\nv_add_u32 v90, s21, v90\nCV 176,176,177\n"
"Mvav 196,152,52\nRa 132,3,50176\nCV 177,178,179\nMvav 196,156,60\nWL 6\ns_add_u32 m0, s89, 65536\ns_add_u32 s46, s96, 1310720\nMvav 192,156,56\nRa 136,3,51200\n"
"v_lshlrev_b32 v88, 3, v90\nMvav 192,152,48\nD1 1,16,46,0\nCV 178,184,185\nMvav 192,148,40\nRa 140,3,52224\nCV 179,186,187\nMvav 192,144,32\nv_add_u32 v94, 64, v90\n"
"Sv 176,1,16,5,0\nZvav 200,160,96\nRa 144,3,53248\ns_mov_b32 s63, 0xc040000\nMvav 200,164,104\nv_lshrrev_b32 v92, 8, v1\nMvav 200,168,112\nRa 148,3,54272\nMvav 200,172,120\nWL 6\n"
"Zvav 204,172,124\nRa 152,3,55296\nv_bfe_u32 v93, v1, 6, 2\nMvav 204,168,116\nD1 1,16,46,1024\nv_cmp_eq_u32 vcc, v92, v93\nv_and_b32 v92, 16, v1\nv_mov_b32 v93, 0x3f80\n"
"v_cndmask_b32 v93, 0, v93, vcc\nMvav 204,164,108\nRa 156,3,56320\nMvav 204,160,100\nv_lshlrev_b32 v93, v92, v93\nMvav 204,176,36\nRa 160,3,57344\nv_and_b32 v92, 32, v1\n"
"Mvav 204,180,44\ns_add_u32 s4, s92, 131072\nv_cmp_eq_u32 vcc, 0, v92\nv_mov_b32 v4, 0\nv_mov_b32 v5, 0\nv_cndmask_b32 v6, 0, v93, vcc\nMvav 204,184,52\nRa 164,3,58368\n"
"Mvav 204,188,60\nWL 6\nbuffer_load_dwordx4 v[216:219], v1, s[16:19], s4 offen offset:0 sc0 sc1\nMvav 200,188,56\nRa 168,3,59392\nv_sub_u32 v7, v93, v6\nMvav 200,184,48\n"
"D1 1,16,46,2048\nMvav 200,180,40\nRa 172,3,60416\nMvav 200,176,32\nbuffer_load_dwordx4 v[220:223], v1, s[16:19], s4 offen offset:1024 sc0 sc1\nZvav 160,128,96\nRa 176,3,61440\n"
"v_mov_b32 v8, 0\nMvav 160,132,104\nv_mov_b32 v9, 0\nCV 196,196,197\nMvav 160,136,112\nRa 180,3,62464\nCV 197,198,199\nMvav 160,140,120\nWL 6\nCV 198,204,205\nZvav 164,140,124\n"
"Ra 184,3,63488\nCV 199,206,207\nMvav 164,136,116\nD1 1,16,46,3072\ns_add_u32 s5, s94, 655360\nMvav 164,132,108\nRa 188,3,64512\ns_barrier\nMvav 164,128,100\nSv 196,1,16,5,1024\n"
"CV 192,192,193\nMvav 164,144,36\nRa 128,0,0\nCV 193,194,195\nMvav 164,148,44\nCV 194,200,201\nCV 195,202,203\nMvav 164,152,52\nRa 132,0,1024\nSv 192,1,16,5,0\nMvav 164,156,60\n"
"WL 6\ns_add_u32 m0, s89, 98304\ns_add_u32 s46, s96, 1343488\nMvav 160,156,56\nRa 136,0,2048\nMvav 160,152,48\nD1 1,16,46,0\nMvav 160,148,40\nRa 140,0,3072\nMvav 160,144,32\n"
"Zvav 168,160,96\nRa 144,0,4096\nWV 12\nv_max_u32 v21, v211, v215\nv_cmp_eq_u32 vcc, -1, v21\ns_nop 0\ns_cbranch_vccnz .S494\n.S495:\nMvav 168,164,104\n"
"v_lshlrev_b32 v176, 16, v208\nMvav 168,168,112\nRa 148,0,5120\nMvav 168,172,120\nWL 6\nv_and_b32 v177, 0xffff0000, v208\nZvav 172,172,124\nRa 152,0,6144\nMvav 172,168,116\n"
"D1 1,16,46,1024\nMvav 172,164,108\nRa 156,0,7168\nv_lshlrev_b32 v178, 16, v209\nMvav 172,160,100\nv_and_b32 v179, 0xffff0000, v209\nMvav 172,176,36\nRa 160,0,8192\n"
"v_lshlrev_b32 v180, 16, v212\nMvav 172,180,44\nv_and_b32 v181, 0xffff0000, v212\ns_add_u32 s4, s92, 262144\nMvav 172,184,52\nRa 164,0,9216\nMvav 172,188,60\nWL 6\n"
"v_lshlrev_b32 v182, 16, v213\nbuffer_load_dwordx4 v[224:227], v1, s[16:19], s4 offen offset:0 sc0 sc1\nMvav 168,188,56\nRa 168,0,10240\nMvav 168,184,48\nD1 1,16,46,2048\n"
"Mvav 168,180,40\nRa 172,0,11264\nv_and_b32 v183, 0xffff0000, v213\nMvav 168,176,32\nv_lshlrev_b32 v184, 16, v210\n"
"buffer_load_dwordx4 v[228:231], v1, s[16:19], s4 offen offset:1024 sc0 sc1\nMvav 176,128,96\nRa 176,0,12288\nv_and_b32 v185, 0xffff0000, v210\nMvav 176,132,104\n"
"v_lshlrev_b32 v186, 16, v211\nCV 164,164,165\nMvav 176,136,112\nRa 180,0,13312\nCV 165,166,167\nMvav 176,140,120\nWL 6\nCV 166,172,173\nMvav 180,140,124\nRa 184,0,14336\n"
"v_and_b32 v187, 0xffff0000, v211\nMvav 180,136,116\nD1 1,16,46,3072\nCV 167,174,175\nMvav 180,132,108\nRa 188,0,15360\ns_add_u32 s5, s94, 786432\nMvav 180,128,100\n"
"v_lshlrev_b32 v188, 16, v214\nSv 164,1,16,5,1024\nMvav 180,144,36\nRa 128,0,16384\nv_and_b32 v189, 0xffff0000, v214\nMvav 180,148,44\nv_lshlrev_b32 v190, 16, v215\nCV 160,160,161\n"
"Mvav 180,152,52\nRa 132,0,17408\nCV 161,162,163\nMvav 180,156,60\nWL 6\ns_add_u32 m0, s89, 114688\ns_add_u32 s46, s96, 1376256\nMvav 176,156,56\nRa 136,0,18432\n"
"v_and_b32 v191, 0xffff0000, v215\nMvav 176,152,48\nD1 1,16,46,0\nCV 162,168,169\nMvav 176,148,40\nRa 140,0,19456\nCV 163,170,171\nMvav 176,144,32\nSv 160,1,16,5,0\n"
"Mvav 184,160,96\nRa 144,0,20480\nWV 12\nv_max_u32 v21, v219, v223\nv_cmp_eq_u32 vcc, -1, v21\ns_nop 0\ns_cbranch_vccnz .S497\n.S498:\nMvav 184,164,104\n"
"v_lshlrev_b32 v192, 16, v216\nMvav 184,168,112\nRa 148,0,21504\nMvav 184,172,120\nWL 6\nv_and_b32 v193, 0xffff0000, v216\nMvav 188,172,124\nRa 152,0,22528\nMvav 188,168,116\n"
"D1 1,16,46,1024\nMvav 188,164,108\nRa 156,0,23552\nv_lshlrev_b32 v194, 16, v217\nMvav 188,160,100\nv_and_b32 v195, 0xffff0000, v217\nMvav 188,176,36\nRa 160,0,24576\n"
"v_lshlrev_b32 v196, 16, v220\nMvav 188,180,44\nv_and_b32 v197, 0xffff0000, v220\ns_add_u32 s4, s92, 393216\nMvav 188,184,52\nRa 164,0,25600\nMvav 188,188,60\nWL 6\n"
"v_lshlrev_b32 v198, 16, v221\nbuffer_load_dwordx4 v[208:211], v1, s[16:19], s4 offen offset:0 sc0 sc1\nMvav 184,188,56\nRa 168,0,26624\nMvav 184,184,48\nD1 1,16,46,2048\n"
"Mvav 184,180,40\nRa 172,0,27648\nv_and_b32 v199, 0xffff0000, v221\nMvav 184,176,32\nv_lshlrev_b32 v200, 16, v218\n"
"buffer_load_dwordx4 v[212:215], v1, s[16:19], s4 offen offset:1024 sc0 sc1\nMvav 192,128,96\nRa 176,0,28672\nv_and_b32 v201, 0xffff0000, v218\nMvav 192,132,104\n"
"v_lshlrev_b32 v202, 16, v219\nCV 180,180,181\nMvav 192,136,112\nRa 180,0,29696\nCV 181,182,183\nMvav 192,140,120\ns_waitcnt vmcnt(15) lgkmcnt(6)\nCV 182,188,189\nMvav 196,140,124\n"
"Ra 184,0,30720\nv_and_b32 v203, 0xffff0000, v219\nMvav 196,136,116\nD1 1,16,46,3072\nCV 183,190,191\nMvav 196,132,108\nRa 188,0,31744\ns_barrier\nMvav 196,128,100\n"
"v_lshlrev_b32 v204, 16, v222\ns_add_u32 s5, s94, 917504\nMvav 196,144,36\nRa 128,2,16384\nv_and_b32 v205, 0xffff0000, v222\nMvav 196,148,44\nv_lshlrev_b32 v206, 16, v223\n"
"Sv 180,1,16,5,1024\nMvav 196,152,52\nRa 132,2,17408\nCV 176,176,177\nMvav 196,156,60\nWL 6\ns_add_u32 m0, s89, 0\ns_add_u32 s46, s96, 1409024\nMvav 192,156,56\nRa 136,2,18432\n"
"v_and_b32 v207, 0xffff0000, v223\nMvav 192,152,48\nD1 1,16,46,0\nCV 177,178,179\nMvav 192,148,40\nRa 140,2,19456\nCV 178,184,185\nMvav 192,144,32\nCV 179,186,187\nSv 176,1,16,5,0\n"
"Mvav 200,160,96\nRa 144,2,20480\nWV 12\nv_max_u32 v21, v227, v231\nv_cmp_eq_u32 vcc, -1, v21\ns_nop 0\ns_cbranch_vccnz .S500\n.S501:\nMvav 200,164,104\n"
"v_lshlrev_b32 v160, 16, v224\nMvav 200,168,112\nRa 148,2,21504\nMvav 200,172,120\nWL 6\nv_and_b32 v161, 0xffff0000, v224\nMvav 204,172,124\nRa 152,2,22528\nMvav 204,168,116\n"
"D1 1,16,46,1024\nMvav 204,164,108\nRa 156,2,23552\nv_lshlrev_b32 v162, 16, v225\nMvav 204,160,100\nv_and_b32 v163, 0xffff0000, v225\nMvav 204,176,36\nRa 160,2,24576\n"
"v_lshlrev_b32 v164, 16, v228\nMvav 204,180,44\nv_and_b32 v165, 0xffff0000, v228\ns_add_u32 s4, s92, 524288\nMvav 204,184,52\nRa 164,2,25600\nMvav 204,188,60\nWL 6\n"
"v_lshlrev_b32 v166, 16, v229\nbuffer_load_dwordx4 v[216:219], v1, s[16:19], s4 offen offset:0 sc0 sc1\nMvav 200,188,56\nRa 168,2,26624\nMvav 200,184,48\nD1 1,16,46,2048\n"
"Mvav 200,180,40\nRa 172,2,27648\nv_and_b32 v167, 0xffff0000, v229\nMvav 200,176,32\nv_lshlrev_b32 v168, 16, v226\n"
"buffer_load_dwordx4 v[220:223], v1, s[16:19], s4 offen offset:1024 sc0 sc1\nMvav 160,128,96\nRa 176,2,28672\nv_and_b32 v169, 0xffff0000, v226\nMvav 160,132,104\n"
"v_lshlrev_b32 v170, 16, v227\nCV 196,196,197\nMvav 160,136,112\nRa 180,2,29696\nCV 197,198,199\nMvav 160,140,120\nWL 6\nCV 198,204,205\nMvav 164,140,124\nRa 184,2,30720\n"
"v_and_b32 v24, 0x70, v1\nMvav 164,136,116\nD1 1,16,46,3072\nCV 199,206,207\nMvav 164,132,108\nRa 188,2,31744\nv_and_b32 v171, 0xffff0000, v227\nMvav 164,128,100\n"
"v_lshlrev_b32 v172, 16, v230\ns_add_u32 s5, s94, 1048576\nMvav 164,144,36\nRa 128,2,0\nv_lshlrev_b32 v24, 5, v24\nMvav 164,148,44\nv_and_b32 v173, 0xffff0000, v230\n"
"Sv 196,1,16,5,1024\nMvav 164,152,52\nRa 132,2,1024\nCV 192,192,193\nMvav 164,156,60\nWL 6\nv_lshlrev_b32 v174, 16, v231\nMvav 160,156,56\nRa 136,2,2048\n"
"buffer_load_dword v27, v24, s[16:19], s41 offen sc0 sc1\nMvav 160,152,48\nv_and_b32 v175, 0xffff0000, v231\nCV 193,194,195\nMvav 160,148,40\nRa 140,2,3072\nCV 194,200,201\n"
"Mvav 160,144,32\nCV 195,202,203\nSv 192,1,16,5,0\nMvav 168,160,96\nRa 144,2,4096\nWV 12\nv_max_u32 v21, v211, v215\nv_cmp_eq_u32 vcc, -1, v21\ns_nop 0\ns_cbranch_vccnz .S503\n"
".S504:\nMvav 168,164,104\nv_lshlrev_b32 v176, 16, v208\nMvav 168,168,112\nRa 148,2,5120\nMvav 168,172,120\nWL 6\nMvav 172,172,124\nRa 152,2,6144\nv_and_b32 v177, 0xffff0000, v208\n"
"Mvav 172,168,116\nv_lshlrev_b32 v178, 16, v209\nMvav 172,164,108\nRa 156,2,7168\nMvav 172,160,100\nv_and_b32 v179, 0xffff0000, v209\nMvav 172,176,36\nRa 160,2,8192\n"
"v_lshlrev_b32 v180, 16, v212\nMvav 172,180,44\nv_and_b32 v181, 0xffff0000, v212\ns_add_u32 s4, s92, 655360\nMvav 172,184,52\nRa 164,2,9216\nMvav 172,188,60\nWL 6\n"
"buffer_load_dwordx4 v[224:227], v1, s[16:19], s4 offen offset:0 sc0 sc1\nMvav 168,188,56\nRa 168,2,10240\nv_lshlrev_b32 v182, 16, v213\nMvav 168,184,48\n"
"v_and_b32 v183, 0xffff0000, v213\nMvav 168,180,40\nRa 172,2,11264\nMvav 168,176,32\nv_lshlrev_b32 v184, 16, v210\n"
"buffer_load_dwordx4 v[228:231], v1, s[16:19], s4 offen offset:1024 sc0 sc1\nMvav 176,128,96\nRa 176,2,12288\nv_and_b32 v185, 0xffff0000, v210\nMvav 176,132,104\n"
"v_lshlrev_b32 v186, 16, v211\nCV 164,164,165\nMvav 176,136,112\nRa 180,2,13312\nCV 165,166,167\nMvav 176,140,120\ns_waitcnt vmcnt(13) lgkmcnt(6)\nCV 166,172,173\nMvav 180,140,124\n"
"Ra 184,2,14336\nCV 167,174,175\nMvav 180,136,116\nv_and_b32 v187, 0xffff0000, v211\ns_add_u32 s5, s94, 1179648\nMvav 180,132,108\nRa 188,2,15360\ns_barrier\nMvav 180,128,100\n"
"v_lshlrev_b32 v188, 16, v214\nSv 164,1,16,5,1024\nMvav 180,144,36\nRa 128,2,32768\nv_and_b32 v189, 0xffff0000, v214\nMvav 180,148,44\nv_lshlrev_b32 v190, 16, v215\nCV 160,160,161\n"
"Mvav 180,152,52\nRa 132,2,33792\nCV 161,162,163\nMvav 180,156,60\nWL 6\nCV 162,168,169\nMvav 176,156,56\nRa 136,2,34816\nCV 163,170,171\nMvav 176,152,48\n"
"v_and_b32 v191, 0xffff0000, v215\nSv 160,1,16,5,0\nMvav 176,148,40\nRa 140,2,35840\nMvav 176,144,32\nMvav 184,160,96\nRa 144,2,36864\nWV 8\nv_max_u32 v21, v219, v223\n"
"v_cmp_eq_u32 vcc, -1, v21\ns_nop 0\ns_cbranch_vccnz .S506\n.S507:\nMvav 184,164,104\nv_lshlrev_b32 v192, 16, v216\nMvav 184,168,112\nRa 148,2,37888\nMvav 184,172,120\nWL 6\n"
"Mvav 188,172,124\nRa 152,2,38912\n.W592:\ns_cbranch_execz .WB414\nv_and_b32 v193, 0xffff0000, v216\nMvav 188,168,116\nv_lshlrev_b32 v194, 16, v217\nMvav 188,164,108\n"
"Ra 156,2,39936\nMvav 188,160,100\nv_and_b32 v195, 0xffff0000, v217\nMvav 188,176,36\nRa 160,2,40960\nv_lshlrev_b32 v196, 16, v220\nMvav 188,180,44\n"
"v_and_b32 v197, 0xffff0000, v220\ns_add_u32 s4, s92, 786432\nMvav 188,184,52\nRa 164,2,41984\nMvav 188,188,60\nWL 6\n"
"buffer_load_dwordx4 v[208:211], v1, s[16:19], s4 offen offset:0 sc0 sc1\nMvav 184,188,56\nRa 168,2,43008\nv_lshlrev_b32 v198, 16, v221\nMvav 184,184,48\n"
"v_and_b32 v199, 0xffff0000, v221\nMvav 184,180,40\nRa 172,2,44032\nMvav 184,176,32\nv_lshlrev_b32 v200, 16, v218\n"
"buffer_load_dwordx4 v[212:215], v1, s[16:19], s4 offen offset:1024 sc0 sc1\nMvav 192,128,96\nRa 176,2,45056\nv_and_b32 v201, 0xffff0000, v218\nMvav 192,132,104\n"
"v_lshlrev_b32 v202, 16, v219\nCV 180,180,181\nMvav 192,136,112\nRa 180,2,46080\nCV 181,182,183\nMvav 192,140,120\nWL 6\nCV 182,188,189\nMvav 196,140,124\nRa 184,2,47104\n"
"CV 183,190,191\nMvav 196,136,116\nv_and_b32 v203, 0xffff0000, v219\ns_add_u32 s5, s94, 1310720\nMvav 196,132,108\nRa 188,2,48128\nSv 180,1,16,5,1024\nMvav 196,128,100\n"
"v_lshlrev_b32 v204, 16, v222\nCV 176,176,177\nMvav 196,144,36\nRa 128,2,49152\nv_and_b32 v205, 0xffff0000, v222\nMvav 196,148,44\nv_lshlrev_b32 v206, 16, v223\nCV 177,178,179\n"
"Mvav 196,152,52\nRa 132,2,50176\nCV 178,184,185\nMvav 196,156,60\nWL 6\nCV 179,186,187\nMvav 192,156,56\nRa 136,2,51200\nSv 176,1,16,5,0\nMvav 192,152,48\n"
"v_and_b32 v207, 0xffff0000, v223\nMvav 192,148,40\nRa 140,2,52224\nMvav 192,144,32\nMvav 200,160,96\nRa 144,2,53248\nWV 6\nv_max_u32 v21, v227, v231\nv_cmp_eq_u32 vcc, -1, v21\n"
"s_nop 0\ns_cbranch_vccnz .S509\n.S510:\nMvav 200,164,104\nv_lshlrev_b32 v160, 16, v224\nv_xor_b32 v27, s15, v27\nv_and_b32 v26, 0xffffdfff, v27\nv_cmp_ne_u32 s[64:65], 0, v27\n"
"v_cmp_ne_u32 vcc, 0, v26\ns_nop 0\ns_cbranch_vccnz .S521\n.S522:\nMvav 200,168,112\nRa 148,2,54272\ns_bitcmp1_b32 s64, 0\ns_cselect_b32 s2, 0x2000, 0\nMvav 200,172,120\nWL 6\n"
"Mvav 204,172,124\nRa 152,2,55296\nv_and_b32 v161, 0xffff0000, v224\nMvav 204,168,116\nv_lshlrev_b32 v162, 16, v225\nMvav 204,164,108\nRa 156,2,56320\ns_xor_b32 s2, s2, s20\n"
"Mvav 204,160,100\nv_and_b32 v163, 0xffff0000, v225\nMvav 204,176,36\nRa 160,2,57344\nv_lshlrev_b32 v164, 16, v228\nMvav 204,180,44\nv_and_b32 v165, 0xffff0000, v228\n"
"Mvav 204,184,52\nRa 164,2,58368\nMvav 204,188,60\nWL 6\ns_add_u32 s24, s2, 0x0\nMvav 200,188,56\nRa 168,2,59392\nv_lshlrev_b32 v166, 16, v229\nMvav 200,184,48\n"
"v_and_b32 v167, 0xffff0000, v229\ns_bitcmp1_b32 s64, 1\ns_cselect_b32 s2, 0x2000, 0\nMvav 200,180,40\nRa 172,2,60416\nMvav 200,176,32\nv_lshlrev_b32 v168, 16, v226\n"
"Mvav 160,128,96\nRa 176,2,61440\nv_and_b32 v169, 0xffff0000, v226\nMvav 160,132,104\nv_lshlrev_b32 v170, 16, v227\nCV 196,196,197\nMvav 160,136,112\nRa 180,2,62464\n"
"CV 197,198,199\nMvav 160,140,120\nWL 6\nCV 198,204,205\nMvav 164,140,124\nRa 184,2,63488\nCV 199,206,207\nMvav 164,136,116\nv_and_b32 v171, 0xffff0000, v227\n"
"s_add_u32 s5, s94, 1441792\nMvav 164,132,108\nRa 188,2,64512\ns_barrier\nMvav 164,128,100\nv_lshlrev_b32 v172, 16, v230\nSv 196,1,16,5,1024\nMvav 164,144,36\nRa 128,3,0\n"
"v_and_b32 v173, 0xffff0000, v230\nMvav 164,148,44\nv_lshlrev_b32 v174, 16, v231\nCV 192,192,193\nMvav 164,152,52\nRa 132,3,1024\nCV 193,194,195\nMvav 164,156,60\nWL 6\n"
"s_xor_b32 s2, s2, s20\nMvav 160,156,56\nRa 136,3,2048\nCV 194,200,201\nMvav 160,152,48\nv_and_b32 v175, 0xffff0000, v231\nCV 195,202,203\nMvav 160,148,40\nRa 140,3,3072\n"
"Sv 192,1,16,5,0\nMvav 160,144,32\ns_add_u32 s25, s2, 0x1800000\ns_bitcmp1_b32 s64, 2\ns_cselect_b32 s2, 0x2000, 0\nMvav 168,160,96\nRa 144,3,4096\nWV 4\nv_max_u32 v21, v211, v215\n"
"v_cmp_eq_u32 vcc, -1, v21\ns_nop 0\ns_cbranch_vccnz .S512\n.S513:\nMvav 168,164,104\nv_lshlrev_b32 v176, 16, v208\nMvav 168,168,112\nRa 148,3,5120\nMvav 168,172,120\nWL 6\n"
"Mvav 172,172,124\nRa 152,3,6144\nv_and_b32 v177, 0xffff0000, v208\nMvav 172,168,116\nv_lshlrev_b32 v178, 16, v209\nMvav 172,164,108\nRa 156,3,7168\ns_xor_b32 s2, s2, s20\n"
"Mvav 172,160,100\nv_and_b32 v179, 0xffff0000, v209\nMvav 172,176,36\nRa 160,3,8192\nv_lshlrev_b32 v180, 16, v212\nMvav 172,180,44\nv_and_b32 v181, 0xffff0000, v212\n"
"Mvav 172,184,52\nRa 164,3,9216\nMvav 172,188,60\nWL 6\ns_add_u32 s26, s2, 0x3000000\nMvav 168,188,56\nRa 168,3,10240\nv_lshlrev_b32 v182, 16, v213\nMvav 168,184,48\n"
"v_and_b32 v183, 0xffff0000, v213\ns_bitcmp1_b32 s64, 3\ns_cselect_b32 s2, 0x2000, 0\nMvav 168,180,40\nRa 172,3,11264\nMvav 168,176,32\nv_lshlrev_b32 v184, 16, v210\n"
"Mvav 176,128,96\nRa 176,3,12288\ns_xor_b32 s2, s2, s20\nMvav 176,132,104\nv_and_b32 v185, 0xffff0000, v210\nCV 164,164,165\nMvav 176,136,112\nRa 180,3,13312\nCV 165,166,167\n"
"Mvav 176,140,120\nWL 6\nCV 166,172,173\nMvav 180,140,124\nRa 184,3,14336\nCV 167,174,175\nMvav 180,136,116\nv_lshlrev_b32 v186, 16, v211\ns_add_u32 s5, s94, 1572864\n"
"Mvav 180,132,108\nRa 188,3,15360\ns_add_u32 s27, s2, 0x4800000\nMvav 180,128,100\nv_and_b32 v187, 0xffff0000, v211\ns_bitcmp1_b32 s64, 4\ns_cselect_b32 s2, 0x2000, 0\n"
"Mvav 180,144,36\nv_lshlrev_b32 v188, 16, v214\nSv 164,1,16,5,1024\nMvav 180,148,44\nv_and_b32 v189, 0xffff0000, v214\nCV 160,160,161\nMvav 180,152,52\n"
"v_lshlrev_b32 v190, 16, v215\nCV 161,162,163\nMvav 180,156,60\nWL 4\ns_xor_b32 s2, s2, s20\nMvav 176,156,56\nv_and_b32 v191, 0xffff0000, v215\nCV 162,168,169\nMvav 176,152,48\n"
"s_add_u32 s32, s2, 0x6000000\ns_bitcmp1_b32 s64, 5\ns_cselect_b32 s2, 0x2000, 0\nMvav 176,148,40\ns_xor_b32 s2, s2, s20\nCV 163,170,171\nMvav 176,144,32\n"
"s_add_u32 s33, s2, 0x7800000\ns_bitcmp1_b32 s64, 6\ns_cselect_b32 s2, 0x2000, 0\nMvav 184,160,96\ns_xor_b32 s2, s2, s20\ns_add_u32 s34, s2, 0x9000000\nMvav 184,164,104\n"
"s_bitcmp1_b32 s64, 7\ns_cselect_b32 s2, 0x2000, 0\nMvav 184,168,112\ns_xor_b32 s2, s2, s20\nSv 160,1,16,5,0\nMvav 184,172,120\nWL 0\nMvav 188,172,124\n"
"s_add_u32 s35, s2, 0xa800000\nMvav 188,168,116\nMvav 188,164,108\nMvav 188,160,100\nMvav 188,176,36\nMvav 188,180,44\nMvav 188,184,52\nMvav 188,188,60\nMvav 184,188,56\n"
"Mvav 184,184,48\nMvav 184,180,40\nMvav 184,176,32\nCV 180,180,181\nCV 181,182,183\ns_nop 1\nCV 182,188,189\nCV 183,190,191\ns_add_u32 s5, s94, 1703936\nSv 180,1,16,5,1024\n"
"CV 176,176,177\nCV 177,178,179\nCV 178,184,185\nCV 179,186,187\nSv 176,1,16,5,0\ns_cmp_eq_u32 s13, 7\ns_cbranch_scc1 .Z524\n"
"buffer_load_dwordx4 v[12:15], v91, s[28:31], s22 offen offset:0\nbuffer_load_dwordx4 v[16:19], v91, s[28:31], s23 offen offset:0\n"
"buffer_load_dwordx4 v[64:67], v89, s[16:19], s40 offen offset:0 sc0 sc1\nbuffer_load_dwordx4 v[68:71], v89, s[16:19], s40 offen offset:16 sc0 sc1\n"
"buffer_load_dwordx4 v[72:75], v89, s[16:19], s40 offen offset:1792 sc0 sc1\nbuffer_load_dwordx4 v[76:79], v89, s[16:19], s40 offen offset:1808 sc0 sc1\n"
"buffer_load_dwordx2 v[80:81], v88, s[16:19], s63 offen offset:0 sc0 sc1\nbuffer_load_dwordx2 v[82:83], v88, s[16:19], s63 offen offset:512 sc0 sc1\n"
"buffer_load_dwordx4 v[96:99], v1, s[16:19], s24 offen offset:0 sc0 sc1\nbuffer_load_dwordx4 v[100:103], v1, s[16:19], s24 offen offset:1024 sc0 sc1\n"
"buffer_load_dwordx4 v[104:107], v1, s[16:19], s25 offen offset:0 sc0 sc1\nbuffer_load_dwordx4 v[108:111], v1, s[16:19], s25 offen offset:1024 sc0 sc1\n"
"buffer_load_dwordx4 v[112:115], v1, s[16:19], s26 offen offset:0 sc0 sc1\nbuffer_load_dwordx4 v[116:119], v1, s[16:19], s26 offen offset:1024 sc0 sc1\n"
"buffer_load_dwordx4 v[120:123], v1, s[16:19], s27 offen offset:0 sc0 sc1\nbuffer_load_dwordx4 v[124:127], v1, s[16:19], s27 offen offset:1024 sc0 sc1\n"
"buffer_load_dwordx4 v[32:35], v1, s[16:19], s32 offen offset:0 sc0 sc1\nbuffer_load_dwordx4 v[36:39], v1, s[16:19], s32 offen offset:1024 sc0 sc1\n"
"buffer_load_dwordx4 v[40:43], v1, s[16:19], s33 offen offset:0 sc0 sc1\nbuffer_load_dwordx4 v[44:47], v1, s[16:19], s33 offen offset:1024 sc0 sc1\n"
"buffer_load_dwordx4 v[48:51], v1, s[16:19], s34 offen offset:0 sc0 sc1\nbuffer_load_dwordx4 v[52:55], v1, s[16:19], s34 offen offset:1024 sc0 sc1\n"
"buffer_load_dwordx4 v[56:59], v1, s[16:19], s35 offen offset:0 sc0 sc1\nbuffer_load_dwordx4 v[60:63], v1, s[16:19], s35 offen offset:1024 sc0 sc1\nWV 16\nv_xor_b32 v66, s15, v66\n"
"v_xor_b32 v70, s15, v70\nv_xor_b32 v74, s15, v74\nv_xor_b32 v78, s15, v78\nv_xor_b32 v81, s15, v81\nv_xor_b32 v83, s15, v83\nv_or3_b32 v92, v66, v70, v74\n"
"v_or3_b32 v92, v92, v78, v81\nv_or_b32 v92, v92, v83\nv_cmp_ne_u32 vcc, 0, v92\ns_nop 0\ns_cbranch_vccnz .S515\n.S516:\nv_and_b32 v92, 0x7ff, v67\nv_lshrrev_b32 v93, 16, v67\n"
"v_cmp_eq_u32 s[44:45], v92, v90\nv_cmp_eq_u32 s[46:47], v93, v90\nv_cmp_eq_u32 s[48:49], v92, v94\nv_cmp_eq_u32 s[50:51], v93, v94\nv_cndmask_b32 v12, v12, v64, s[44:45]\n"
"v_cndmask_b32 v13, v13, v65, s[44:45]\nv_cndmask_b32 v14, v14, v68, s[44:45]\nv_cndmask_b32 v15, v15, v69, s[44:45]\nv_cndmask_b32 v12, v12, v72, s[46:47]\n"
"v_cndmask_b32 v13, v13, v73, s[46:47]\nv_cndmask_b32 v14, v14, v76, s[46:47]\nv_cndmask_b32 v15, v15, v77, s[46:47]\nv_cndmask_b32 v16, v16, v64, s[48:49]\n"
"v_cndmask_b32 v17, v17, v65, s[48:49]\nv_cndmask_b32 v18, v18, v68, s[48:49]\nv_cndmask_b32 v19, v19, v69, s[48:49]\nv_cndmask_b32 v16, v16, v72, s[50:51]\n"
"v_cndmask_b32 v17, v17, v73, s[50:51]\nv_cndmask_b32 v18, v18, v76, s[50:51]\nv_cndmask_b32 v19, v19, v77, s[50:51]\nv_sub_f32 v92, 1.0, v80\nv_sub_f32 v93, 1.0, v82\nCV 92,92,93\n"
"v_and_b32 v93, 0xffff0000, v92\nv_lshlrev_b32 v92, 16, v92\nv_lshlrev_b32 v64, 16, v12\nv_and_b32 v65, 0xffff0000, v12\nMU 64,64,92\nMU 65,65,92\nv_lshlrev_b32 v66, 16, v13\n"
"v_and_b32 v67, 0xffff0000, v13\nMU 66,66,92\nMU 67,67,92\nv_lshlrev_b32 v68, 16, v14\nv_and_b32 v69, 0xffff0000, v14\nMU 68,68,92\nMU 69,69,92\nv_lshlrev_b32 v70, 16, v15\n"
"v_and_b32 v71, 0xffff0000, v15\nMU 70,70,92\nMU 71,71,92\nv_lshlrev_b32 v72, 16, v16\nv_and_b32 v73, 0xffff0000, v16\nMU 72,72,93\nMU 73,73,93\nv_lshlrev_b32 v74, 16, v17\n"
"v_and_b32 v75, 0xffff0000, v17\nMU 74,74,93\nMU 75,75,93\nv_lshlrev_b32 v76, 16, v18\nv_and_b32 v77, 0xffff0000, v18\nMU 76,76,93\nMU 77,77,93\nv_lshlrev_b32 v78, 16, v19\n"
"v_and_b32 v79, 0xffff0000, v19\nMU 78,78,93\nMU 79,79,93\ns_mov_b32 s7, 262144\nWV 0\n.Z527:\nv_max_u32 v84, v99, v103\nv_cmp_eq_u32 s[44:45], -1, v84\nv_max_u32 v84, v107, v111\n"
"v_cmp_eq_u32 s[46:47], -1, v84\nv_max_u32 v84, v115, v119\nv_cmp_eq_u32 s[48:49], -1, v84\nv_max_u32 v84, v123, v127\nv_cmp_eq_u32 s[50:51], -1, v84\nv_max_u32 v84, v35, v39\n"
"v_cmp_eq_u32 s[52:53], -1, v84\nv_max_u32 v84, v43, v47\nv_cmp_eq_u32 s[54:55], -1, v84\nv_max_u32 v84, v51, v55\nv_cmp_eq_u32 s[56:57], -1, v84\nv_max_u32 v84, v59, v63\n"
"v_cmp_eq_u32 s[58:59], -1, v84\ns_or_b64 s[0:1], s[44:45], s[46:47]\ns_or_b64 s[2:3], s[48:49], s[50:51]\ns_or_b64 s[0:1], s[0:1], s[52:53]\ns_or_b64 s[2:3], s[2:3], s[54:55]\n"
"s_or_b64 s[0:1], s[0:1], s[56:57]\ns_or_b64 s[2:3], s[2:3], s[58:59]\ns_or_b64 s[0:1], s[0:1], s[2:3]\ns_cbranch_scc1 .Z525\n.Z526:\nZvvv 136,6,96\nMvvv 136,6,104\nMvvv 136,6,112\n"
"Mvvv 136,6,120\nMvvv 136,6,32\nMvvv 136,6,40\nMvvv 136,6,48\nMvvv 136,6,56\nZvvv 140,4,96\nMvvv 140,4,104\nMvvv 140,4,112\nMvvv 140,4,120\nMvvv 140,4,32\nMvvv 140,4,40\n"
"Mvvv 140,4,48\nMvvv 140,4,56\nZvvv 144,6,100\nMvvv 144,6,108\nMvvv 144,6,116\nMvvv 144,6,124\nMvvv 144,6,36\nMvvv 144,6,44\nMvvv 144,6,52\nMvvv 144,6,60\nZvvv 148,4,100\n"
"Mvvv 148,4,108\nMvvv 148,4,116\nMvvv 148,4,124\nMvvv 148,4,36\nMvvv 148,4,44\nMvvv 148,4,52\nMvvv 148,4,60\nFM 64,80,136\nFM 65,80,137\nFM 66,80,138\nFM 67,80,139\nFM 68,80,140\n"
"FM 69,80,141\nFM 70,80,142\nFM 71,80,143\nCV 12,64,65\nCV 13,66,67\nCV 14,68,69\nCV 15,70,71\nSv 12,91,36,22,0\nFM 72,82,144\nFM 73,82,145\nFM 74,82,146\nFM 75,82,147\n"
"FM 76,82,148\nFM 77,82,149\nFM 78,82,150\nFM 79,82,151\nCV 16,72,73\nCV 17,74,75\nCV 18,76,77\nCV 19,78,79\nSv 16,91,36,23,0\n.Z524:\ns_endpgm\n.EP536:\n.S410:\n"
"s_mov_b32 s7, 262144\n.S412:\ns_lshl_b32 s5, s73, 9\ns_add_u32 s5, s5, 0xc049400\nv_lshrrev_b32 v27, 1, v1\nbuffer_load_dwordx2 v[22:23], v27, s[16:19], s5 offen sc0 sc1\nWV 0\n"
"v_cmp_ne_u32 vcc, s15, v23\ns_nop 0\ns_cbranch_vccz .S411\ns_sleep 6\ns_sub_u32 s7, s7, 1\ns_cbranch_scc0 .S412\ns_branch .S411\n.T419:\ns_mov_b32 s62, 1\n"
"s_call_b64 s[80:81], .RA265\ns_branch .T420\n.T421:\ns_mov_b32 s62, 1\ns_call_b64 s[80:81], .RA265\ns_branch .T422\n.S424:\ns_mov_b32 s7, 262144\n.S426:\n"
"buffer_load_dword v27, off, s[16:19], s5 sc0 sc1\nWV 0\nv_cmp_ne_u32 vcc, s15, v27\ns_nop 0\ns_cbranch_vccz .S425\ns_sleep 6\ns_sub_u32 s7, s7, 1\ns_cbranch_scc0 .S426\n"
"s_branch .S425\n.S427:\ns_mov_b32 s7, 262144\n.S429:\nv_and_b32 v27, 0x70, v1\nbuffer_load_dword v27, v27, s[16:19], s61 offen offset:128 sc0 sc1\nWV 0\n"
"v_cmp_ne_u32 vcc, s15, v27\ns_nop 0\ns_cbranch_vccz .S428\ns_sleep 6\ns_sub_u32 s7, s7, 1\ns_cbranch_scc0 .S429\ns_branch .S428\n.S431:\ns_mov_b32 s7, 262144\n.S433:\n"
"v_and_b32 v27, 0x70, v1\nbuffer_load_dword v27, v27, s[16:19], s61 offen offset:136 sc0 sc1\nWV 0\nv_cmp_ne_u32 vcc, s15, v27\ns_nop 0\ns_cbranch_vccz .S432\ns_sleep 6\n"
"s_sub_u32 s7, s7, 1\ns_cbranch_scc0 .S433\ns_branch .S432\n.S434:\ns_mov_b32 s7, 262144\n.S436:\nv_and_b32 v27, 0x70, v1\n"
"buffer_load_dword v27, v27, s[16:19], s61 offen offset:140 sc0 sc1\nWV 0\nv_cmp_ne_u32 vcc, s15, v27\ns_nop 0\ns_cbranch_vccz .S435\ns_sleep 6\ns_sub_u32 s7, s7, 1\n"
"s_cbranch_scc0 .S436\ns_branch .S435\n.S437:\ns_mov_b32 s7, 262144\n.S439:\nv_and_b32 v27, 0x70, v1\nbuffer_load_dword v27, v27, s[16:19], s61 offen offset:12 sc0 sc1\nWV 0\n"
"v_cmp_ne_u32 vcc, s15, v27\ns_nop 0\ns_cbranch_vccz .S438\ns_sleep 6\ns_sub_u32 s7, s7, 1\ns_cbranch_scc0 .S439\ns_branch .S438\n.S440:\ns_mov_b32 s7, 262144\n.S442:\n"
"v_and_b32 v27, 0x70, v1\nbuffer_load_dword v27, v27, s[16:19], s61 offen offset:8 sc0 sc1\nWV 0\nv_cmp_ne_u32 vcc, s15, v27\ns_nop 0\ns_cbranch_vccz .S441\ns_sleep 6\n"
"s_sub_u32 s7, s7, 1\ns_cbranch_scc0 .S442\ns_branch .S441\n.S443:\ns_mov_b32 s7, 262144\n.S445:\nv_and_b32 v27, 0xf0, v1\n"
"buffer_load_dword v27, v27, s[16:19], s61 offen offset:4 sc0 sc1\nWV 0\nv_cmp_ne_u32 vcc, s15, v27\ns_nop 0\ns_cbranch_vccz .S444\ns_sleep 6\ns_sub_u32 s7, s7, 1\n"
"s_cbranch_scc0 .S445\ns_branch .S444\n.T454:\ns_mov_b32 s62, 0\ns_call_b64 s[80:81], .RB266\ns_branch .T455\n.T456:\ns_mov_b32 s62, 0\ns_call_b64 s[80:81], .RA265\ns_branch .T457\n"
".T460:\ns_mov_b32 s62, 0\ns_call_b64 s[80:81], .RB266\ns_branch .T461\n.T462:\ns_mov_b32 s62, 0\ns_call_b64 s[80:81], .RA265\ns_branch .T463\n.T466:\ns_mov_b32 s62, 2\n"
"s_call_b64 s[80:81], .RB266\ns_branch .T467\n.T468:\ns_mov_b32 s62, 1\ns_call_b64 s[80:81], .RA265\ns_branch .T469\n.T470:\ns_mov_b32 s62, 1\ns_call_b64 s[80:81], .RA265\n"
"s_branch .T471\n.T482:\ns_mov_b32 s62, 0\ns_call_b64 s[80:81], .RB266\ns_branch .T483\n.T484:\ns_mov_b32 s62, 0\ns_call_b64 s[80:81], .RA265\ns_branch .T485\n.T488:\n"
"s_mov_b32 s62, 0\ns_call_b64 s[80:81], .RB266\ns_branch .T489\n.T490:\ns_mov_b32 s62, 0\ns_call_b64 s[80:81], .RA265\ns_branch .T491\n.T492:\ns_mov_b32 s62, 2\n"
"s_call_b64 s[80:81], .RB266\ns_branch .T493\n.S494:\ns_mov_b32 s7, 262144\n.S496:\ns_add_u32 s0, s92, 0\nbuffer_load_dwordx4 v[208:211], v1, s[16:19], s0 offen offset:0 sc0 sc1\n"
"buffer_load_dwordx4 v[212:215], v1, s[16:19], s0 offen offset:1024 sc0 sc1\nWV 0\nv_max_u32 v21, v211, v215\nv_cmp_eq_u32 vcc, -1, v21\ns_nop 0\ns_cbranch_vccz .S495\ns_sleep 6\n"
"s_sub_u32 s7, s7, 1\ns_cbranch_scc0 .S496\ns_branch .S495\n.S497:\ns_mov_b32 s7, 262144\n.S499:\ns_add_u32 s0, s92, 131072\n"
"buffer_load_dwordx4 v[216:219], v1, s[16:19], s0 offen offset:0 sc0 sc1\nbuffer_load_dwordx4 v[220:223], v1, s[16:19], s0 offen offset:1024 sc0 sc1\nWV 0\n"
"v_max_u32 v21, v219, v223\nv_cmp_eq_u32 vcc, -1, v21\ns_nop 0\ns_cbranch_vccz .S498\ns_sleep 6\ns_sub_u32 s7, s7, 1\ns_cbranch_scc0 .S499\ns_branch .S498\n.S500:\n"
"s_mov_b32 s7, 262144\n.S502:\ns_add_u32 s0, s92, 262144\nbuffer_load_dwordx4 v[224:227], v1, s[16:19], s0 offen offset:0 sc0 sc1\n"
"buffer_load_dwordx4 v[228:231], v1, s[16:19], s0 offen offset:1024 sc0 sc1\nWV 0\nv_max_u32 v21, v227, v231\nv_cmp_eq_u32 vcc, -1, v21\ns_nop 0\ns_cbranch_vccz .S501\ns_sleep 6\n"
"s_sub_u32 s7, s7, 1\ns_cbranch_scc0 .S502\ns_branch .S501\n.S503:\ns_mov_b32 s7, 262144\n.S505:\ns_add_u32 s0, s92, 393216\n"
"buffer_load_dwordx4 v[208:211], v1, s[16:19], s0 offen offset:0 sc0 sc1\nbuffer_load_dwordx4 v[212:215], v1, s[16:19], s0 offen offset:1024 sc0 sc1\nWV 0\n"
"v_max_u32 v21, v211, v215\nv_cmp_eq_u32 vcc, -1, v21\ns_nop 0\ns_cbranch_vccz .S504\ns_sleep 6\ns_sub_u32 s7, s7, 1\ns_cbranch_scc0 .S505\ns_branch .S504\n.S506:\n"
"s_mov_b32 s7, 262144\n.S508:\ns_add_u32 s0, s92, 524288\nbuffer_load_dwordx4 v[216:219], v1, s[16:19], s0 offen offset:0 sc0 sc1\n"
"buffer_load_dwordx4 v[220:223], v1, s[16:19], s0 offen offset:1024 sc0 sc1\nWV 0\nv_max_u32 v21, v219, v223\nv_cmp_eq_u32 vcc, -1, v21\ns_nop 0\ns_cbranch_vccz .S507\ns_sleep 6\n"
"s_sub_u32 s7, s7, 1\ns_cbranch_scc0 .S508\ns_branch .S507\n.S509:\ns_mov_b32 s7, 262144\n.S511:\ns_add_u32 s0, s92, 655360\n"
"buffer_load_dwordx4 v[224:227], v1, s[16:19], s0 offen offset:0 sc0 sc1\nbuffer_load_dwordx4 v[228:231], v1, s[16:19], s0 offen offset:1024 sc0 sc1\nWV 0\n"
"v_max_u32 v21, v227, v231\nv_cmp_eq_u32 vcc, -1, v21\ns_nop 0\ns_cbranch_vccz .S510\ns_sleep 6\ns_sub_u32 s7, s7, 1\ns_cbranch_scc0 .S511\ns_branch .S510\n.S512:\n"
"s_mov_b32 s7, 262144\n.S514:\ns_add_u32 s0, s92, 786432\nbuffer_load_dwordx4 v[208:211], v1, s[16:19], s0 offen offset:0 sc0 sc1\n"
"buffer_load_dwordx4 v[212:215], v1, s[16:19], s0 offen offset:1024 sc0 sc1\nWV 0\nv_max_u32 v21, v211, v215\nv_cmp_eq_u32 vcc, -1, v21\ns_nop 0\ns_cbranch_vccz .S513\ns_sleep 6\n"
"s_sub_u32 s7, s7, 1\ns_cbranch_scc0 .S514\ns_branch .S513\n.S515:\ns_mov_b32 s7, 262144\n.S517:\nbuffer_load_dwordx4 v[64:67], v89, s[16:19], s40 offen offset:0 sc0 sc1\n"
"buffer_load_dwordx4 v[68:71], v89, s[16:19], s40 offen offset:16 sc0 sc1\nbuffer_load_dwordx4 v[72:75], v89, s[16:19], s40 offen offset:1792 sc0 sc1\n"
"buffer_load_dwordx4 v[76:79], v89, s[16:19], s40 offen offset:1808 sc0 sc1\nbuffer_load_dwordx2 v[80:81], v88, s[16:19], s63 offen offset:0 sc0 sc1\n"
"buffer_load_dwordx2 v[82:83], v88, s[16:19], s63 offen offset:512 sc0 sc1\nWV 0\nv_xor_b32 v66, s15, v66\nv_xor_b32 v70, s15, v70\nv_xor_b32 v74, s15, v74\n"
"v_xor_b32 v78, s15, v78\nv_xor_b32 v81, s15, v81\nv_xor_b32 v83, s15, v83\nv_or3_b32 v92, v66, v70, v74\nv_or3_b32 v92, v92, v78, v81\nv_or_b32 v92, v92, v83\n"
"v_cmp_ne_u32 vcc, 0, v92\ns_nop 0\ns_cbranch_vccz .S516\ns_sleep 6\ns_sub_u32 s7, s7, 1\ns_cbranch_scc0 .S517\ns_branch .S516\n.S518:\ns_mov_b32 s7, 262144\n.S520:\n"
"buffer_load_dword v27, off, s[16:19], s60 offset:512 sc0 sc1\nWV 0\nv_cmp_ne_u32 vcc, s15, v27\ns_nop 0\ns_cbranch_vccz .S519\ns_sleep 6\ns_sub_u32 s7, s7, 1\n"
"s_cbranch_scc0 .S520\ns_branch .S519\n.S521:\ns_mov_b32 s7, 262144\n.S523:\nbuffer_load_dword v27, v24, s[16:19], s41 offen sc0 sc1\nWV 0\nv_xor_b32 v27, s15, v27\n"
"v_and_b32 v26, 0xffffdfff, v27\nv_cmp_ne_u32 s[64:65], 0, v27\nv_cmp_ne_u32 vcc, 0, v26\ns_nop 0\ns_cbranch_vccz .S522\ns_sleep 6\ns_sub_u32 s7, s7, 1\ns_cbranch_scc0 .S523\n"
"s_branch .S522\n.Z525:\ns_sub_u32 s7, s7, 1\ns_cbranch_scc1 .Z526\ns_cmp_lg_u64 s[44:45], 0\ns_cbranch_scc0 .Z528\n"
"buffer_load_dwordx4 v[96:99], v1, s[16:19], s24 offen offset:0 sc0 sc1\nbuffer_load_dwordx4 v[100:103], v1, s[16:19], s24 offen offset:1024 sc0 sc1\n.Z528:\n"
"s_cmp_lg_u64 s[46:47], 0\ns_cbranch_scc0 .Z529\nbuffer_load_dwordx4 v[104:107], v1, s[16:19], s25 offen offset:0 sc0 sc1\n"
"buffer_load_dwordx4 v[108:111], v1, s[16:19], s25 offen offset:1024 sc0 sc1\n.Z529:\ns_cmp_lg_u64 s[48:49], 0\ns_cbranch_scc0 .Z530\n"
"buffer_load_dwordx4 v[112:115], v1, s[16:19], s26 offen offset:0 sc0 sc1\nbuffer_load_dwordx4 v[116:119], v1, s[16:19], s26 offen offset:1024 sc0 sc1\n.Z530:\n"
"s_cmp_lg_u64 s[50:51], 0\ns_cbranch_scc0 .Z531\nbuffer_load_dwordx4 v[120:123], v1, s[16:19], s27 offen offset:0 sc0 sc1\n"
"buffer_load_dwordx4 v[124:127], v1, s[16:19], s27 offen offset:1024 sc0 sc1\n.Z531:\ns_cmp_lg_u64 s[52:53], 0\ns_cbranch_scc0 .Z532\n"
"buffer_load_dwordx4 v[32:35], v1, s[16:19], s32 offen offset:0 sc0 sc1\nbuffer_load_dwordx4 v[36:39], v1, s[16:19], s32 offen offset:1024 sc0 sc1\n.Z532:\n"
"s_cmp_lg_u64 s[54:55], 0\ns_cbranch_scc0 .Z533\nbuffer_load_dwordx4 v[40:43], v1, s[16:19], s33 offen offset:0 sc0 sc1\n"
"buffer_load_dwordx4 v[44:47], v1, s[16:19], s33 offen offset:1024 sc0 sc1\n.Z533:\ns_cmp_lg_u64 s[56:57], 0\ns_cbranch_scc0 .Z534\n"
"buffer_load_dwordx4 v[48:51], v1, s[16:19], s34 offen offset:0 sc0 sc1\nbuffer_load_dwordx4 v[52:55], v1, s[16:19], s34 offen offset:1024 sc0 sc1\n.Z534:\n"
"s_cmp_lg_u64 s[58:59], 0\ns_cbranch_scc0 .Z535\nbuffer_load_dwordx4 v[56:59], v1, s[16:19], s35 offen offset:0 sc0 sc1\n"
"buffer_load_dwordx4 v[60:63], v1, s[16:19], s35 offen offset:1024 sc0 sc1\n.Z535:\ns_sleep 8\nWV 0\ns_branch .Z527\n.Lcode1:\n"
  :: "s"(__builtin_amdgcn_kernarg_segment_ptr()), "s"(blockIdx.x), "v"(threadIdx.x)
  : "memory", "scc", "vcc", "v1", "v2", "v3", "v4", "v5", "v6", "v7", "v8", "v9", "v10", "v11", "v12", "v13", "v14", "v15", "v16", "v17", "v18", "v19", "v20", "v21", "v22", "v23", "v24", "v25", "v26", "v27", "v28", "v29", "v30", "v31", "v32", "v33", "v34", "v35", "v36", "v37", "v38", "v39", "v40", "v41", "v42", "v43", "v44", "v45", "v46", "v47", "v48", "v49", "v50", "v51", "v52", "v53", "v54", "v55", "v56", "v57", "v58", "v59", "v60", "v61", "v62", "v63", "v64", "v65", "v66", "v67", "v68", "v69", "v70", "v71", "v72", "v73", "v74", "v75", "v76", "v77", "v78", "v79", "v80", "v81", "v82", "v83", "v84", "v85", "v86", "v87", "v88", "v89", "v90", "v91", "v92", "v93", "v94", "v95", "v96", "v97", "v98", "v99", "v100", "v101", "v102", "v103", "v104", "v105", "v106", "v107", "v108", "v109", "v110", "v111", "v112", "v113", "v114", "v115", "v116", "v117", "v118", "v119", "v120", "v121", "v122", "v123", "v124", "v125", "v126", "v127", "v128", "v129", "v130", "v131", "v132", "v133", "v134", "v135", "v136", "v137", "v138", "v139", "v140", "v141", "v142", "v143", "v144", "v145", "v146", "v147", "v148", "v149", "v150", "v151", "v152", "v153", "v154", "v155", "v156", "v157", "v158", "v159", "v160", "v161", "v162", "v163", "v164", "v165", "v166", "v167", "v168", "v169", "v170", "v171", "v172", "v173", "v174", "v175", "v176", "v177", "v178", "v179", "v180", "v181", "v182", "v183", "v184", "v185", "v186", "v187", "v188", "v189", "v190", "v191", "v192", "v193", "v194", "v195", "v196", "v197", "v198", "v199", "v200", "v201", "v202", "v203", "v204", "v205", "v206", "v207", "v208", "v209", "v210", "v211", "v212", "v213", "v214", "v215", "v216", "v217", "v218", "v219", "v220", "v221", "v222", "v223", "v224", "v225", "v226", "v227", "v228", "v229", "v230", "v231", "v232", "v233", "v234", "v235", "v236", "v237", "v238", "v239", "v240", "v241", "v242", "v243", "v244", "v245", "v246", "v247", "v248", "v249", "v250", "v251", "v252", "v253", "v254", "v255", "a0", "a1", "a2", "a3", "a4", "a5", "a6", "a7", "a8", "a9", "a10", "a11", "a12", "a13", "a14", "a15", "a16", "a17", "a18", "a19", "a20", "a21", "a22", "a23", "a24", "a25", "a26", "a27", "a28", "a29", "a30", "a31", "a32", "a33", "a34", "a35", "a36", "a37", "a38", "a39", "a40", "a41", "a42", "a43", "a44", "a45", "a46", "a47", "a48", "a49", "a50", "a51", "a52", "a53", "a54", "a55", "a56", "a57", "a58", "a59", "a60", "a61", "a62", "a63", "a64", "a65", "a66", "a67", "a68", "a69", "a70", "a71", "a72", "a73", "a74", "a75", "a76", "a77", "a78", "a79", "a80", "a81", "a82", "a83", "a84", "a85", "a86", "a87", "a88", "a89", "a90", "a91", "a92", "a93", "a94", "a95", "a96", "a97", "a98", "a99", "a100", "a101", "a102", "a103", "a104", "a105", "a106", "a107", "a108", "a109", "a110", "a111", "a112", "a113", "a114", "a115", "a116", "a117", "a118", "a119", "a120", "a121", "a122", "a123", "a124", "a125", "a126", "a127", "a128", "a129", "a130", "a131", "a132", "a133", "a134", "a135", "a136", "a137", "a138", "a139", "a140", "a141", "a142", "a143", "a144", "a145", "a146", "a147", "a148", "a149", "a150", "a151", "a152", "a153", "a154", "a155", "a156", "a157", "a158", "a159", "a160", "a161", "a162", "a163", "a164", "a165", "a166", "a167", "a168", "a169", "a170", "a171", "a172", "a173", "a174", "a175", "a176", "a177", "a178", "a179", "a180", "a181", "a182", "a183", "a184", "a185", "a186", "a187", "a188", "a189", "a190", "a191", "a192", "a193", "a194", "a195", "a196", "a197", "a198", "a199", "a200", "a201", "a202", "a203", "a204", "a205", "a206", "a207", "a208", "a209", "a210", "a211", "a212", "a213", "a214", "a215", "a216", "a217", "a218", "a219", "a220", "a221", "a222", "a223", "a224", "a225", "a226", "a227", "a228", "a229", "a230", "a231", "a232", "a233", "a234", "a235", "a236", "a237", "a238", "a239", "a240", "a241", "a242", "a243", "a244", "a245", "a246", "a247", "a248", "a249", "a250", "a251", "a252", "a253", "a254", "a255", "s8", "s9", "s10", "s11", "s12", "s13", "s14", "s15", "s16", "s17", "s18", "s19", "s20", "s21", "s22", "s23", "s24", "s25", "s26", "s27", "s28", "s29", "s30", "s31", "s33", "s34", "s35", "s36", "s37", "s38", "s39", "s40", "s41", "s42", "s43", "s44", "s45", "s46", "s47", "s48", "s49", "s50", "s51", "s52", "s53", "s54", "s55", "s56", "s57", "s58", "s59", "s60", "s61", "s62", "s63", "s64", "s65", "s66", "s67", "s68", "s69", "s70", "s71", "s72", "s73", "s74", "s75", "s76", "s77", "s78", "s79", "s80", "s81", "s82", "s83", "s84", "s85", "s86", "s87", "s88", "s89", "s90", "s91", "s92", "s93", "s94", "s95", "s96", "s97", "s98", "s99");
#endif
}

extern "C" void kernel_launch(void* const* d_in, const int* in_sizes, int n_in,
                              void* d_out, int out_size, void* d_ws, size_t ws_size,
                              hipStream_t stream) {
  (void)hipFuncSetAttribute((const void*)k_main, hipFuncAttributeMaxDynamicSharedMemorySize, 163840);
  k_main<<<256, 256, 163840, stream>>>(d_in[0], d_in[1], d_in[2], d_in[3], d_in[4], d_ws, d_in[11], d_in[7], d_in[5], d_in[8], d_in[9], d_in[10], d_out);
}
```

```cpp
#include <hip/hip_runtime.h>
#include <stdint.h>

extern "C" __global__ __launch_bounds__(256, 1)
void k_main(const void* x, const void* ckv, const void* wq, const void* wuk, const void* wuv, void* ws,
            const void* gate_w, const void* w_r, const void* wo, const void* wg, const void* wu, const void* wd, void* out) {
#if defined(__HIP_DEVICE_COMPILE__)
  asm volatile(
"s_mov_b64 s[8:9], %0\ns_mov_b32 s10, %1\nv_mov_b32 v0, %2\n.macro WL n\ns_waitcnt lgkmcnt(\\n)\n.endm\n.macro D0 x,r,s,o\n"
"buffer_load_dwordx4 v\\x, s[\\r:\\r+3], s\\s offen offset:\\o lds\n.endm\n.macro WV n\ns_waitcnt vmcnt(\\n)\n.endm\n.macro Ra d,x,o\nds_read_b128 a[\\d:\\d+3], v\\x offset:\\o\n"
".endm\n.macro Ta d,x,o\nds_read_b64_tr_b16 a[\\d:\\d+1], v\\x offset:\\o\n.endm\n.macro Zvaa d,x,y\nv_mfma_f32_16x16x32_bf16 v[\\d:\\d+3], a[\\x:\\x+3], a[\\y:\\y+3], 0\n.endm\n"
".macro Mvaa d,x,y\nv_mfma_f32_16x16x32_bf16 v[\\d:\\d+3], a[\\x:\\x+3], a[\\y:\\y+3], v[\\d:\\d+3]\n.endm\n.macro Zaaa d,x,y\n"
"v_mfma_f32_16x16x32_bf16 a[\\d:\\d+3], a[\\x:\\x+3], a[\\y:\\y+3], 0\n.endm\n.macro Maaa d,x,y\nv_mfma_f32_16x16x32_bf16 a[\\d:\\d+3], a[\\x:\\x+3], a[\\y:\\y+3], a[\\d:\\d+3]\n"
".endm\n.macro MU d,x,y\nv_mul_f32 v\\d, v\\x, v\\y\n.endm\n.macro CV d,x,y\nv_cvt_pk_bf16_f32 v\\d, v\\x, v\\y\n.endm\n.macro FM d,x,y\nv_fmac_f32 v\\d, v\\x, v\\y\n.endm\n"
".macro AD d,x,y\nv_add_f32 v\\d, v\\x, v\\y\n.endm\n.macro EX d,x\nv_exp_f32 v\\d, v\\x\n.endm\n.macro AR d,x\nv_accvgpr_read_b32 v\\d, a\\x\n.endm\n.macro Sv d,x,r,s,o\n"
"buffer_store_dwordx4 v[\\d:\\d+3], v\\x, s[\\r:\\r+3], s\\s offen offset:\\o sc0 sc1\n.endm\n.macro Rv d,x,o\nds_read_b128 v[\\d:\\d+3], v\\x offset:\\o\n.endm\n.macro D1 x,r,s,o\n"
"buffer_load_dwordx4 v\\x, s[\\r:\\r+3], s\\s offen offset:\\o sc1 lds\n.endm\n.macro Cvaa d,x,y,c\nv_mfma_f32_16x16x32_bf16 v[\\d:\\d+3], a[\\x:\\x+3], a[\\y:\\y+3], v[\\c:\\c+3]\n"
".endm\n.macro Zvav d,x,y\nv_mfma_f32_16x16x32_bf16 v[\\d:\\d+3], a[\\x:\\x+3], v[\\y:\\y+3], 0\n.endm\n.macro Mvav d,x,y\n"
"v_mfma_f32_16x16x32_bf16 v[\\d:\\d+3], a[\\x:\\x+3], v[\\y:\\y+3], v[\\d:\\d+3]\n.endm\n.macro SU d,x,y\nv_sub_f32 v\\d, v\\x, v\\y\n.endm\n.macro Mvva d,x,y\n"
"v_mfma_f32_16x16x32_bf16 v[\\d:\\d+3], v[\\x:\\x+3], a[\\y:\\y+3], v[\\d:\\d+3]\n.endm\n.macro Zvvv d,x,y\nv_mfma_f32_16x16x32_bf16 v[\\d:\\d+3], v[\\x:\\x+3], v[\\y:\\y+3], 0\n"
".endm\n.macro Mvvv d,x,y\nv_mfma_f32_16x16x32_bf16 v[\\d:\\d+3], v[\\x:\\x+3], v[\\y:\\y+3], v[\\d:\\d+3]\n.endm\n.macro AW d,x\nv_accvgpr_write_b32 a\\d, v\\x\n.endm\n"
".macro Tv d,x,o\nds_read_b64_tr_b16 v[\\d:\\d+1], v\\x offset:\\o\n.endm\n.macro Sa d,x,r,s,o\n"
"buffer_store_dwordx4 a[\\d:\\d+3], v\\x, s[\\r:\\r+3], s\\s offen offset:\\o sc0 sc1\n.endm\n.Lcode0:\ns_mov_b32 s70, 0\ns_load_dwordx16 s[44:59], s[8:9], 0x0\n"
"s_load_dwordx8 s[60:67], s[8:9], 0x40\ns_load_dwordx2 s[68:69], s[8:9], 0x60\nv_lshrrev_b32 v2, 6, v0\nv_and_b32 v0, 63, v0\nv_readfirstlane_b32 s11, v2\nv_lshlrev_b32 v1, 4, v0\n"
"s_and_b32 s2, s10, 0x17\ns_bfe_u32 s3, s10, 0x10003\ns_lshl_b32 s3, s3, 7\ns_or_b32 s2, s2, s3\ns_bfe_u32 s3, s10, 0x10005\ns_lshl3_add_u32 s2, s3, s2\ns_lshr_b32 s3, s10, 6\n"
"s_lshl_b32 s3, s3, 5\ns_or_b32 s10, s2, s3\ns_and_b32 s73, s10, 7\ns_lshr_b32 s74, s10, 3\ns_and_b32 s2, s74, 1\ns_lshl1_add_u32 s12, s73, s2\ns_bfe_u32 s13, s74, 0x30001\n"
"s_lshr_b32 s14, s74, 4\ns_xor_b32 s14, s14, 1\nv_lshrrev_b32 v32, 5, v0\ns_and_b32 s3, s11, 1\ns_lshl_b32 s2, s3, 3\nv_lshl_or_b32 v33, v32, 1, s2\nv_and_b32 v34, 31, v0\n"
"v_xor_b32 v33, v33, v34\nv_lshlrev_b32 v33, 4, v33\nv_lshl_or_b32 v6, v32, 13, v33\nv_xor_b32 v33, 64, v6\nv_add_u32 v7, 15360, v33\nv_add_u32 v8, 30720, v6\n"
"v_add_u32 v9, 46080, v33\ns_lshl_b32 s2, s11, 16\ns_lshl_b32 s4, s12, 9\ns_add_u32 s5, s2, s4\ns_getpc_b64 s[40:41]\n.PC8:\ns_sub_u32 s40, s40, .PC8-.Lcode0\n"
"s_subb_u32 s41, s41, 0\ns_mov_b32 s42, .Lcode1-.Lcode0\ns_mov_b32 s43, 0x20000\ns_lshl2_add_u32 s6, s74, s11\ns_lshl_b32 s6, s6, 7\nv_lshlrev_b32 v54, 10, v1\n"
"buffer_load_dword v31, v54, s[40:43], s6 offen\ns_lshl_b32 s90, s11, 12\ns_lshr_b32 s91, 0x20000, s14\ns_add_u32 m0, s90, s91\ns_mov_b32 s39, 0x20000\ns_add_u32 s92, s5, 0x40000\n"
"s_cmp_eq_u32 s14, 0\ns_mov_b32 s26, 0x800000\ns_cselect_b32 s38, 0x380000, s26\ns_bitcmp1_b32 s13, 0\nWL 0\ns_cselect_b64 s[50:51], s[52:53], s[50:51]\ns_cmp_eq_u32 s14, 0\n"
"s_cselect_b64 s[36:37], s[48:49], s[50:51]\ns_and_b32 s37, s37, 0xffff\nD0 6,36,5,0\nD0 7,36,5,1024\nD0 8,36,5,2048\nD0 9,36,5,3072\ns_add_u32 m0, m0, 0x4000\ns_nop 0\n"
"D0 6,36,92,0\nD0 7,36,92,1024\nD0 8,36,92,2048\nD0 9,36,92,3072\nv_lshrrev_b32 v32, 4, v0\nv_and_b32 v33, 7, v0\nv_xor_b32 v33, v33, v32\nv_lshlrev_b32 v33, 4, v33\n"
"v_lshrrev_b32 v32, 3, v0\nv_lshl_or_b32 v2, v32, 11, v33\nv_xor_b32 v33, 64, v2\nv_add_u32 v3, 15360, v33\nv_add_u32 v4, 30720, v2\nv_add_u32 v5, 46080, v33\n"
"v_lshrrev_b32 v144, 4, v0\nv_and_b32 v145, 7, v0\nv_xor_b32 v145, v145, v144\nv_lshlrev_b32 v145, 4, v145\nv_lshrrev_b32 v144, 3, v0\nv_mul_u32_u24 v144, 896, v144\n"
"v_add_u32 v136, v144, v145\nv_xor_b32 v145, 64, v136\nv_add_u32 v137, 6144, v145\nv_add_u32 v138, 55296, v136\nv_add_u32 v139, 61440, v145\ns_lshr_b32 s88, s11, 1\n"
"s_lshr_b32 s2, s13, 2\ns_bfe_u32 s3, s13, 0x10001\ns_mul_i32 s3, s3, 17\ns_add_u32 s2, s2, s3\ns_lshl2_add_u32 s3, s14, s88\ns_lshl_b32 s3, s3, 2\ns_lshr_b32 s3, 0xe420ca86, s3\n"
"s_and_b32 s3, s3, 15\ns_add_u32 s2, s2, s3\ns_cmp_eq_u32 s2, 32\ns_cselect_b32 s86, 16, s2\ns_and_b32 s89, s11, 1\ns_lshl1_add_u32 s86, s86, s89\ns_mov_b32 s28, s44\n"
"s_and_b32 s29, s45, 0xffff\ns_mov_b32 s30, 0x1c0000\ns_mov_b32 s31, 0x20000\ns_mov_b32 s20, s46\ns_and_b32 s21, s47, 0xffff\ns_mov_b32 s22, 0x400000\ns_mov_b32 s23, 0x20000\n"
"s_add_u32 s47, s90, 0x10000\ns_lshl_b32 s45, s86, 16\ns_sub_u32 s45, s45, 128\ns_sub_u32 s2, 15, s13\ns_lshl3_add_u32 s2, s2, s11\ns_mul_i32 s52, s2, 14336\ns_cmp_eq_u32 s14, 1\n"
"s_cbranch_scc1 .B6\ns_add_u32 m0, s47, -65536\ns_add_u32 s46, s52, 0\nD0 136,28,46,0\nD0 137,28,46,1024\nD0 138,28,46,2048\nD0 139,28,46,3072\ns_sub_u32 s92, s5, 0x40000\n"
"s_branch .B7\n.B6:\ns_add_u32 m0, s47, -65536\ns_add_u32 s45, s45, 128\nD0 2,20,45,0\nD0 3,20,45,1024\nD0 4,20,45,2048\nD0 5,20,45,3072\n.B7:\ns_mov_b32 s32, s48\n"
"s_and_b32 s33, s49, 0xffff\ns_mov_b32 s34, 0x380000\ns_mov_b32 s35, 0x20000\ns_mov_b32 s24, s50\ns_and_b32 s25, s51, 0xffff\ns_mov_b32 s26, 0x800000\ns_mov_b32 s27, 0x20000\n"
"s_mov_b32 s44, s92\ns_mov_b32 s51, s5\ns_mov_b32 s18, 0xc0ae800\ns_mov_b32 s19, 0x20000\ns_add_u32 s88, s88, 2\ns_lshr_b32 s2, s13, 2\ns_bfe_u32 s3, s13, 0x10001\n"
"s_mul_i32 s3, s3, 17\ns_add_u32 s2, s2, s3\ns_lshl2_add_u32 s3, s14, s88\ns_lshl_b32 s3, s3, 2\ns_lshr_b32 s3, 0xe420ca86, s3\ns_and_b32 s3, s3, 15\ns_add_u32 s2, s2, s3\n"
"s_cmp_eq_u32 s2, 32\ns_cselect_b32 s88, 16, s2\ns_lshl1_add_u32 s88, s88, s89\ns_lshl_b32 s48, s88, 16\ns_sub_u32 s48, s48, 128\ns_add_u32 s54, s54, 0x1fffff\n"
"s_addc_u32 s55, s55, 0\ns_and_b32 s54, s54, 0xffe00000\ns_mov_b32 s16, s54\ns_and_b32 s17, s55, 0xffff\ns_mov_b32 s2, 0xc048000\ns_load_dword s15, s[54:55], s2\n"
"v_lshrrev_b32 v24, 3, v0\nv_lshlrev_b32 v24, 13, v24\nv_and_b32 v26, 7, v0\nv_lshl_or_b32 v24, v26, 7, v24\nv_lshrrev_b32 v26, 4, v0\nv_lshl_or_b32 v24, v26, 28, v24\n"
"v_lshrrev_b32 v26, 5, v0\nv_lshlrev_b32 v26, 30, v26\nv_lshl_or_b32 v26, v0, 7, v26\ns_and_b32 s2, s13, 6\ns_and_b32 s3, s12, 1\ns_add_u32 s2, s2, s3\ns_lshl2_add_u32 s2, s2, s11\n"
"s_lshl_b32 s2, s2, 14\ns_lshl_b32 s3, s73, 10\ns_add_u32 s2, s2, s3\ns_add_u32 s96, s2, 1048576\ns_and_b32 s2, s10, 127\ns_lshl2_add_u32 s2, s2, s11\ns_lshl_b32 s97, s2, 12\n"
"v_and_b32 v48, 15, v0\nv_lshrrev_b32 v49, 4, v0\nv_lshrrev_b32 v50, 1, v48\nv_xor_b32 v50, v50, v49\nv_lshlrev_b32 v50, 4, v50\nv_lshl_or_b32 v50, v48, 7, v50\n"
"s_lshr_b32 s2, s11, 1\ns_lshl_b32 s2, s2, 12\nv_add_u32 v10, s2, v50\nv_xor_b32 v11, 64, v10\nv_lshrrev_b32 v50, 2, v48\nv_lshl_add_u32 v51, v49, 3, v50\nv_and_b32 v52, 1, v49\n"
"v_lshl_or_b32 v50, v52, 2, v50\nv_lshlrev_b32 v50, 5, v50\nv_lshl_or_b32 v50, v51, 9, v50\nv_and_b32 v52, 3, v0\nv_lshl_or_b32 v50, v52, 3, v50\ns_and_b32 s2, s11, 1\n"
"s_lshl_b32 s2, s2, 8\ns_add_u32 s2, s2, 0x10000\nv_add_u32 v12, s2, v50\nv_xor_b32 v13, 32, v12\nv_xor_b32 v14, 64, v12\nv_xor_b32 v15, 96, v12\nv_xor_b32 v16, 128, v12\n"
"v_xor_b32 v17, 160, v12\nv_xor_b32 v18, 192, v12\nv_xor_b32 v19, 224, v12\ns_getpc_b64 s[0:1]\n.PC9:\ns_mov_b32 s2, .PA2-.PC9\ns_mov_b32 s3, .PA4-.PC9\ns_bitcmp1_b32 s13, 0\n"
"s_cselect_b32 s2, .PA3-.PC9, s2\ns_cselect_b32 s3, .PA5-.PC9, s3\ns_cmp_eq_u32 s14, 0\ns_cselect_b32 s2, s2, s3\ns_add_u32 s0, s0, s2\ns_addc_u32 s1, s1, 0\ns_setpc_b64 s[0:1]\n"
".p2align 12\n.PA2:\nv_add_u32 v128, 0x10000, v12\nv_add_u32 v129, 0x10000, v13\nv_add_u32 v130, 0x10000, v14\nv_add_u32 v131, 0x10000, v15\nv_add_u32 v132, 0x10000, v16\n"
"v_add_u32 v133, 0x10000, v17\nv_add_u32 v134, 0x10000, v18\nv_add_u32 v135, 0x10000, v19\ns_add_u32 s2, s11, 1\ns_lshr_b32 s2, s2, 1\ns_lshl_b32 s2, s2, 12\n"
"v_add_u32 v140, s2, v10\nv_add_u32 v141, s2, v11\nv_xor_b32 v142, 0x1000, v140\nv_xor_b32 v143, 0x1000, v141\ns_add_u32 m0, s47, 0\ns_add_u32 s46, s51, 524288\nD0 6,32,46,0\n"
"D0 7,32,46,1024\nD0 8,32,46,2048\nD0 9,32,46,3072\ns_add_u32 m0, s47, -49152\ns_add_u32 s46, s52, 128\nD0 136,28,46,0\nD0 137,28,46,1024\nD0 138,28,46,2048\nD0 139,28,46,3072\n"
"s_add_u32 m0, s47, 16384\ns_add_u32 s46, s51, 786432\nD0 6,32,46,0\nD0 7,32,46,1024\nD0 8,32,46,2048\nD0 9,32,46,3072\nv_mov_b32 v116, 0x40000000\nv_cmp_gt_u32 vcc, 56, v0\n"
"s_lshl2_add_u32 s2, s10, s11\ns_mul_i32 s2, s2, 1792\nv_cndmask_b32 v117, v116, v1, vcc\ns_mov_b32 s36, s56\ns_and_b32 s37, s57, 0xffff\ns_mov_b32 s38, 0x380\n"
"s_mov_b32 s39, 0x20000\ns_mov_b32 s40, s58\ns_and_b32 s41, s59, 0xffff\ns_mov_b32 s42, 0x380\ns_mov_b32 s43, 0x20000\n"
"buffer_load_dwordx4 v[96:99], v117, s[28:31], s2 offen offset:0\nbuffer_load_dwordx4 v[100:103], v117, s[28:31], s2 offen offset:896\n"
"buffer_load_dwordx4 v[104:107], v117, s[36:39], 0 offen offset:0\nbuffer_load_dwordx4 v[108:111], v117, s[40:43], 0 offen offset:0\ns_add_u32 m0, s47, 32768\n"
"s_add_u32 s46, s51, 1048576\nD0 6,32,46,0\nD0 7,32,46,1024\nD0 8,32,46,2048\nD0 9,32,46,3072\nWL 0\ns_mul_i32 s15, s15, 1664525\ns_add_u32 s15, s15, 1013904223\n"
"v_mov_b32 v25, s15\ns_mov_b32 s57, 0x3db8aa3b\ns_and_b32 s59, s11, 1\ns_lshl_b32 s59, s59, 13\ns_mul_i32 s2, s73, 25165824\ns_and_b32 s3, s12, 1\ns_lshl_b32 s3, s3, 21\n"
"s_add_u32 s2, s2, s3\ns_lshl2_add_u32 s3, s13, s11\ns_lshl_b32 s3, s3, 15\ns_add_u32 s2, s2, s3\ns_add_u32 s56, s2, 0x800000\ns_xor_b32 s53, s56, 0xc000\ns_mov_b64 exec, 0\n"
"s_cmp_eq_u32 s11, 1\ns_cbranch_scc1 .W538\ns_cmp_eq_u32 s11, 2\ns_cbranch_scc1 .W539\ns_cmp_eq_u32 s11, 3\ns_cbranch_scc1 .W540\ns_branch .W537\n.WB21:\ns_mov_b64 exec, -1\nWV 20\n"
"s_barrier\nRa 192,140,0\nRa 196,141,0\nRa 200,140,2048\nRa 204,141,2048\nRa 208,142,0\nRa 212,143,0\nRa 216,142,2048\nRa 220,143,2048\nTa 160,128,0\nTa 162,128,2048\n"
"Ta 164,128,16384\nTa 166,128,18432\nTa 168,129,0\nTa 170,129,2048\nTa 172,129,16384\nTa 174,129,18432\nTa 176,130,0\nTa 178,130,2048\nTa 180,130,16384\nTa 182,130,18432\n"
"Ta 184,131,0\nTa 186,131,2048\nTa 188,131,16384\nTa 190,131,18432\nWL 0\nZvaa 32,160,192\nTa 224,132,0\ns_add_u32 m0, s47, -32768\ns_add_u32 s46, s52, 256\nMvaa 32,164,196\n"
"Ta 226,132,2048\nZaaa 96,164,204\nD0 136,28,46,0\nMaaa 96,160,200\nTa 228,132,16384\nZvaa 192,160,208\nTa 230,132,18432\nMvaa 192,164,212\nZvaa 224,164,220\nTa 232,133,0\n"
"Mvaa 224,160,216\nTa 234,133,2048\nD0 137,28,46,1024\nZvaa 228,168,216\nMvaa 228,172,220\nTa 236,133,16384\nZvaa 196,172,212\nTa 238,133,18432\nMvaa 196,168,208\nZaaa 100,168,200\n"
"Ta 240,134,0\nD0 138,28,46,2048\nMaaa 100,172,204\nTa 242,134,2048\nZvaa 36,172,196\nMvaa 36,168,192\nTa 244,134,16384\nZvaa 40,176,192\nTa 246,134,18432\nMvaa 40,180,196\n"
"D0 139,28,46,3072\nZaaa 104,180,204\nTa 248,135,0\nMaaa 104,176,200\nTa 250,135,2048\nZvaa 200,176,208\ns_add_u32 m0, s47, 49152\ns_add_u32 s46, s51, 1310720\nMvaa 200,180,212\n"
"Ta 252,135,16384\nZvaa 232,180,220\nTa 254,135,18432\nD0 6,32,46,0\nMvaa 232,176,216\nZvaa 236,184,216\nMvaa 236,188,220\nZvaa 204,188,212\nMvaa 204,184,208\nD0 7,32,46,1024\n"
"Zaaa 108,184,200\nMaaa 108,188,204\nZvaa 44,188,196\nMvaa 44,184,192\ns_waitcnt vmcnt(14) lgkmcnt(0)\ns_barrier\nZvaa 48,224,192\nRa 128,140,16384\nD0 8,32,46,2048\n"
"Mvaa 48,228,196\nRa 132,141,16384\nZaaa 112,228,204\nRa 136,140,18432\nMaaa 112,224,200\nRa 140,141,18432\nZvaa 208,224,208\nRa 144,142,16384\nWV 11\nMvaa 208,228,212\n"
"Ra 148,143,16384\nD0 9,32,46,3072\nZvaa 240,228,220\nRa 152,142,18432\nMvaa 240,224,216\nRa 156,143,18432\nZvaa 244,232,216\nTa 160,12,0\ns_add_u32 m0, s47, -16384\n"
"s_add_u32 s46, s52, 384\nMvaa 244,236,220\nTa 162,12,2048\nZvaa 212,236,212\nTa 164,12,16384\nD0 136,28,46,0\nMvaa 212,232,208\nTa 166,12,18432\nZaaa 116,232,200\nTa 168,13,0\n"
"v_lshlrev_b32 v112, 16, v96\nMaaa 116,236,204\nTa 170,13,2048\nZvaa 52,236,196\nTa 172,13,16384\nMvaa 52,232,192\nTa 174,13,18432\nD0 137,28,46,1024\nZvaa 56,240,192\nTa 176,14,0\n"
"v_lshlrev_b32 v113, 16, v100\nMvaa 56,244,196\nTa 178,14,2048\nZaaa 120,244,204\nTa 180,14,16384\nMaaa 120,240,200\nTa 182,14,18432\nZvaa 216,240,208\nTa 184,15,0\n"
"D0 138,28,46,2048\nMvaa 216,244,212\nTa 186,15,2048\nZvaa 248,244,220\nTa 188,15,16384\nMvaa 248,240,216\nTa 190,15,18432\nZvaa 252,248,216\nv_lshlrev_b32 v114, 16, v104\n"
"v_lshlrev_b32 v115, 16, v108\nMvaa 252,252,220\nD0 139,28,46,3072\nZvaa 220,252,212\nMU 116,112,114\nMvaa 220,248,208\nMU 117,113,114\nZaaa 124,248,200\ns_add_u32 m0, s47, 65536\n"
"s_add_u32 s46, s51, 1572864\nMaaa 124,252,204\nCV 116,116,117\nZvaa 60,252,196\nD0 6,32,46,0\nMvaa 60,248,192\nWL 0\nMvaa 32,160,128\nTa 224,16,0\nv_lshlrev_b32 v118, 16, v116\n"
"Mvaa 32,164,132\nTa 226,16,2048\nMaaa 96,164,140\nv_and_b32 v119, 0xffff0000, v116\nMaaa 96,160,136\nTa 228,16,16384\nD0 7,32,46,1024\nMvaa 192,160,144\nTa 230,16,18432\n"
"MU 120,112,115\nMvaa 192,164,148\nMU 121,113,115\nMvaa 224,164,156\nTa 232,17,0\nMvaa 224,160,152\nTa 234,17,2048\nMvaa 228,168,152\nD0 8,32,46,2048\n"
"v_and_b32 v112, 0xffff0000, v96\nMvaa 228,172,156\nTa 236,17,16384\nMvaa 196,172,148\nTa 238,17,18432\nMvaa 196,168,144\nv_and_b32 v113, 0xffff0000, v100\nMaaa 100,168,136\n"
"Ta 240,18,0\nv_and_b32 v114, 0xffff0000, v104\nMaaa 100,172,140\nTa 242,18,2048\nD0 9,32,46,3072\nMvaa 36,172,132\nv_and_b32 v115, 0xffff0000, v108\nMvaa 36,168,128\n"
"Ta 244,18,16384\nMvaa 40,176,128\nTa 246,18,18432\ns_add_u32 m0, s47, 81920\ns_add_u32 s46, s51, 1835008\nMvaa 40,180,132\nMU 116,112,114\nMaaa 104,180,140\nTa 248,19,0\n"
"D0 6,32,46,0\nMaaa 104,176,136\nTa 250,19,2048\nMvaa 200,176,144\nMU 117,113,114\nCV 116,116,117\nMvaa 200,180,148\nTa 252,19,16384\nMvaa 232,180,156\nTa 254,19,18432\n"
"Mvaa 232,176,152\nD0 7,32,46,1024\nMvaa 236,184,152\nv_and_b32 v117, 0xffff0000, v116\nv_lshlrev_b32 v116, 16, v116\nMvaa 236,188,156\nFM 120,112,115\nMvaa 204,188,148\n"
"FM 121,113,115\nMvaa 204,184,144\nAD 118,118,116\nMaaa 108,184,136\nD0 8,32,46,2048\nAD 119,119,117\nMaaa 108,188,140\nv_lshlrev_b32 v112, 16, v97\nMvaa 44,188,132\n"
"v_lshlrev_b32 v113, 16, v101\nMvaa 44,184,128\ns_waitcnt vmcnt(11) lgkmcnt(0)\ns_barrier\nMvaa 48,224,128\nRa 192,140,32768\nv_lshlrev_b32 v114, 16, v105\nMvaa 48,228,132\n"
"Ra 196,141,32768\nD0 9,32,46,3072\nMaaa 112,228,140\nRa 200,140,34816\nMaaa 112,224,136\nRa 204,141,34816\nMvaa 208,224,144\nRa 208,142,32768\ns_add_u32 m0, s47, -49152\n"
"s_add_u32 s46, s52, 512\nMvaa 208,228,148\nRa 212,143,32768\nMvaa 240,228,156\nRa 216,142,34816\nD0 136,28,46,0\nMvaa 240,224,152\nRa 220,143,34816\nMvaa 244,232,152\n"
"Ta 160,12,32768\nv_lshlrev_b32 v115, 16, v109\nMvaa 244,236,156\nTa 162,12,34816\nMvaa 212,236,148\nTa 164,12,49152\nMvaa 212,232,144\nTa 166,12,51200\nD0 137,28,46,1024\n"
"Maaa 116,232,136\nTa 168,13,32768\nMU 116,112,114\nMaaa 116,236,140\nTa 170,13,34816\nMvaa 52,236,132\nTa 172,13,49152\nMvaa 52,232,128\nTa 174,13,51200\nMvaa 56,240,128\n"
"Ta 176,14,32768\nD0 138,28,46,2048\nMvaa 56,244,132\nTa 178,14,34816\nMaaa 120,244,140\nTa 180,14,49152\nMaaa 120,240,136\nTa 182,14,51200\nMvaa 216,240,144\nTa 184,15,32768\n"
"MU 117,113,114\nMvaa 216,244,148\nTa 186,15,34816\nD0 139,28,46,3072\nMvaa 248,244,156\nTa 188,15,49152\nMvaa 248,240,152\nTa 190,15,51200\nMvaa 252,248,152\ns_add_u32 m0, s47, 0\n"
"s_add_u32 s46, s51, 2097152\nMvaa 252,252,156\nCV 116,116,117\nMvaa 220,252,148\nD0 6,32,46,0\nMvaa 220,248,144\nv_and_b32 v117, 0xffff0000, v116\nMaaa 124,248,136\n"
"v_lshlrev_b32 v116, 16, v116\nFM 120,112,115\nMaaa 124,252,140\nFM 121,113,115\nMvaa 60,252,132\nAD 118,118,116\nMvaa 60,248,128\nWL 0\nMvaa 32,160,192\nTa 224,16,32768\n"
"D0 7,32,46,1024\nMvaa 32,164,196\nTa 226,16,34816\nMaaa 96,164,204\nAD 119,119,117\nMaaa 96,160,200\nTa 228,16,49152\nMvaa 192,160,208\nTa 230,16,51200\n"
"v_and_b32 v112, 0xffff0000, v97\nMvaa 192,164,212\nD0 8,32,46,2048\nMvaa 224,164,220\nTa 232,17,32768\nMvaa 224,160,216\nTa 234,17,34816\nMvaa 228,168,216\n"
"v_and_b32 v113, 0xffff0000, v101\nv_and_b32 v114, 0xffff0000, v105\nMvaa 228,172,220\nTa 236,17,49152\nMvaa 196,172,212\nTa 238,17,51200\nD0 9,32,46,3072\nMvaa 196,168,208\n"
"v_and_b32 v115, 0xffff0000, v109\nMaaa 100,168,200\nTa 240,18,32768\nMU 116,112,114\nMaaa 100,172,204\nTa 242,18,34816\ns_add_u32 m0, s47, 16384\ns_add_u32 s46, s51, 2359296\n"
"Mvaa 36,172,196\nMU 117,113,114\nMvaa 36,168,192\nTa 244,18,49152\nD0 6,32,46,0\nMvaa 40,176,192\nTa 246,18,51200\nCV 116,116,117\nMvaa 40,180,196\n"
"v_and_b32 v117, 0xffff0000, v116\nMaaa 104,180,204\nTa 248,19,32768\nMaaa 104,176,200\nTa 250,19,34816\nMvaa 200,176,208\nD0 7,32,46,1024\nv_lshlrev_b32 v116, 16, v116\n"
"Mvaa 200,180,212\nTa 252,19,49152\nMvaa 232,180,220\nTa 254,19,51200\nMvaa 232,176,216\nFM 120,112,115\nMvaa 236,184,216\nFM 121,113,115\nAD 118,118,116\nMvaa 236,188,220\n"
"D0 8,32,46,2048\nMvaa 204,188,212\nAD 119,119,117\nMvaa 204,184,208\nv_lshlrev_b32 v112, 16, v98\nMaaa 108,184,200\nv_lshlrev_b32 v113, 16, v102\nv_lshlrev_b32 v114, 16, v106\n"
"Maaa 108,188,204\nv_lshlrev_b32 v115, 16, v110\nMvaa 44,188,196\nD0 9,32,46,3072\nMvaa 44,184,192\ns_waitcnt vmcnt(12) lgkmcnt(0)\ns_barrier\nMvaa 48,224,192\nRa 128,140,49152\n"
"MU 116,112,114\nMvaa 48,228,196\nRa 132,141,49152\ns_add_u32 m0, s47, -32768\ns_add_u32 s46, s52, 640\nMaaa 112,228,204\nRa 136,140,51200\nMaaa 112,224,200\nRa 140,141,51200\n"
"D0 136,28,46,0\nMvaa 208,224,208\nRa 144,142,49152\nMU 117,113,114\nMvaa 208,228,212\nRa 148,143,49152\nMvaa 240,228,220\nRa 152,142,51200\nMvaa 240,224,216\nRa 156,143,51200\n"
"Mvaa 244,232,216\nTa 160,128,0\nD0 137,28,46,1024\nMvaa 244,236,220\nTa 162,128,2048\nMvaa 212,236,212\nTa 164,128,16384\nMvaa 212,232,208\nTa 166,128,18432\nMaaa 116,232,200\n"
"Ta 168,129,0\nCV 116,116,117\nMaaa 116,236,204\nTa 170,129,2048\nD0 138,28,46,2048\nMvaa 52,236,196\nTa 172,129,16384\nMvaa 52,232,192\nTa 174,129,18432\nMvaa 56,240,192\n"
"Ta 176,130,0\nv_and_b32 v117, 0xffff0000, v116\nMvaa 56,244,196\nTa 178,130,2048\nMaaa 120,244,204\nTa 180,130,16384\nD0 139,28,46,3072\nMaaa 120,240,200\nTa 182,130,18432\n"
"Mvaa 216,240,208\nTa 184,131,0\nv_lshlrev_b32 v116, 16, v116\nMvaa 216,244,212\nTa 186,131,2048\ns_add_u32 m0, s47, 32768\ns_add_u32 s46, s51, 2621440\nMvaa 248,244,220\n"
"Ta 188,131,16384\nMvaa 248,240,216\nTa 190,131,18432\nD0 6,32,46,0\nMvaa 252,248,216\nFM 120,112,115\nFM 121,113,115\nMvaa 252,252,220\nAD 118,118,116\nMvaa 220,252,212\n"
"AD 119,119,117\nMvaa 220,248,208\nv_and_b32 v112, 0xffff0000, v98\nMaaa 124,248,200\nD0 7,32,46,1024\nv_and_b32 v113, 0xffff0000, v102\nMaaa 124,252,204\n"
"v_and_b32 v114, 0xffff0000, v106\nMvaa 60,252,196\nv_and_b32 v115, 0xffff0000, v110\nMvaa 60,248,192\nWL 0\nMvaa 32,160,128\nTa 224,132,0\n.W537:\ns_cbranch_execz .W541\n"
"MU 116,112,114\nMvaa 32,164,132\nTa 226,132,2048\nD0 8,32,46,2048\nMaaa 96,164,140\nMU 117,113,114\nMaaa 96,160,136\nTa 228,132,16384\nMvaa 192,160,144\nTa 230,132,18432\n"
"CV 116,116,117\nMvaa 192,164,148\nv_and_b32 v117, 0xffff0000, v116\nMvaa 224,164,156\nTa 232,133,0\nD0 9,32,46,3072\nMvaa 224,160,152\nTa 234,133,2048\nMvaa 228,168,152\n"
"v_lshlrev_b32 v116, 16, v116\nFM 120,112,115\nMvaa 228,172,156\nTa 236,133,16384\ns_add_u32 m0, s47, 49152\ns_add_u32 s46, s51, 2883584\nMvaa 196,172,148\nTa 238,133,18432\n"
"Mvaa 196,168,144\nD0 6,32,46,0\nMaaa 100,168,136\nTa 240,134,0\nFM 121,113,115\nMaaa 100,172,140\nTa 242,134,2048\nMvaa 36,172,132\nAD 118,118,116\nMvaa 36,168,128\n"
"Ta 244,134,16384\nMvaa 40,176,128\nTa 246,134,18432\nD0 7,32,46,1024\nMvaa 40,180,132\nAD 119,119,117\nMaaa 104,180,140\nTa 248,135,0\nMaaa 104,176,136\nTa 250,135,2048\n"
"Mvaa 200,176,144\nv_lshlrev_b32 v112, 16, v99\nv_lshlrev_b32 v113, 16, v103\nMvaa 200,180,148\nTa 252,135,16384\nD0 8,32,46,2048\nMvaa 232,180,156\nTa 254,135,18432\n"
"Mvaa 232,176,152\nv_lshlrev_b32 v114, 16, v107\nMvaa 236,184,152\nv_lshlrev_b32 v115, 16, v111\nMU 116,112,114\nMvaa 236,188,156\nMU 117,113,114\nMvaa 204,188,148\n"
"D0 9,32,46,3072\nMvaa 204,184,144\nCV 116,116,117\nMaaa 108,184,136\nv_and_b32 v117, 0xffff0000, v116\nv_lshlrev_b32 v116, 16, v116\ns_and_b32 s78, s74, 3\n"
"s_lshl2_add_u32 s78, s73, s78\ns_lshl_b32 s78, s78, 12\ns_bitcmp1_b32 s11, 0\ns_cselect_b32 s79, s48, s45\ns_add_u32 s79, s79, 512\ns_and_b32 s79, s79, 0xfffe0000\n"
"s_add_u32 s78, s78, s79\nbuffer_load_dword v31, v26, s[20:23], s78 offen\nMaaa 108,188,140\nFM 120,112,115\nMvaa 44,188,132\nFM 121,113,115\nMvaa 44,184,128\n"
"s_waitcnt vmcnt(13) lgkmcnt(0)\ns_barrier\nMvaa 48,224,128\nRa 192,140,16384\ns_add_u32 m0, s47, -16384\ns_add_u32 s46, s52, 768\nMvaa 48,228,132\nRa 196,141,16384\n"
"Maaa 112,228,140\nRa 200,140,18432\nD0 136,28,46,0\nMaaa 112,224,136\nRa 204,141,18432\nMvaa 208,224,144\nRa 208,142,16384\nAD 118,118,116\nMvaa 208,228,148\nRa 212,143,16384\n"
"Mvaa 240,228,156\nRa 216,142,18432\nMvaa 240,224,152\nRa 220,143,18432\nD0 137,28,46,1024\nMvaa 244,232,152\nTa 160,12,0\nAD 119,119,117\nMvaa 244,236,156\nTa 162,12,2048\n"
"Mvaa 212,236,148\nTa 164,12,16384\nMvaa 212,232,144\nTa 166,12,18432\nMaaa 116,232,136\nTa 168,13,0\nD0 138,28,46,2048\nMaaa 116,236,140\nTa 170,13,2048\nMvaa 52,236,132\n"
"Ta 172,13,16384\nMvaa 52,232,128\nTa 174,13,18432\nMvaa 56,240,128\nTa 176,14,0\nv_and_b32 v112, 0xffff0000, v99\nMvaa 56,244,132\nTa 178,14,2048\nD0 139,28,46,3072\n"
"Maaa 120,244,140\nTa 180,14,16384\nMaaa 120,240,136\nTa 182,14,18432\nMvaa 216,240,144\nTa 184,15,0\ns_add_u32 m0, s47, 65536\ns_add_u32 s46, s51, 3145728\nMvaa 216,244,148\n"
"Ta 186,15,2048\nMvaa 248,244,156\nTa 188,15,16384\nD0 6,32,46,0\nMvaa 248,240,152\nTa 190,15,18432\nMvaa 252,248,152\nv_and_b32 v113, 0xffff0000, v103\n"
"v_and_b32 v114, 0xffff0000, v107\nMvaa 252,252,156\nv_and_b32 v115, 0xffff0000, v111\nMvaa 220,252,148\nMU 116,112,114\nMvaa 220,248,144\nD0 7,32,46,1024\nMaaa 124,248,136\n"
"MU 117,113,114\nCV 116,116,117\nMaaa 124,252,140\nv_and_b32 v117, 0xffff0000, v116\nMvaa 60,252,132\nv_lshlrev_b32 v116, 16, v116\nMvaa 60,248,128\nWL 0\nMvaa 32,160,192\n"
"Ta 224,16,0\nD0 8,32,46,2048\nMvaa 32,164,196\nTa 226,16,2048\nMaaa 96,164,204\nFM 120,112,115\nMaaa 96,160,200\nTa 228,16,16384\nMvaa 192,160,208\nTa 230,16,18432\n"
"FM 121,113,115\nMvaa 192,164,212\nD0 9,32,46,3072\nMvaa 224,164,220\nTa 232,17,0\nMvaa 224,160,216\nTa 234,17,2048\nMvaa 228,168,216\ns_add_u32 m0, s47, 81920\n"
"s_add_u32 s46, s51, 3407872\nMvaa 228,172,220\nTa 236,17,16384\nMvaa 196,172,212\nTa 238,17,18432\nD0 6,32,46,0\nMvaa 196,168,208\nAD 118,118,116\nMaaa 100,168,200\nTa 240,18,0\n"
"AD 119,119,117\nMaaa 100,172,204\nTa 242,18,2048\nMvaa 36,172,196\nv_permlane32_swap_b32 v118, v119\nMvaa 36,168,192\nTa 244,18,16384\nD0 7,32,46,1024\nMvaa 40,176,192\n"
"Ta 246,18,18432\nv_permlane32_swap_b32 v120, v121\nMvaa 40,180,196\nAD 118,118,119\nMaaa 104,180,204\nTa 248,19,0\nMaaa 104,176,200\nTa 250,19,2048\nMvaa 200,176,208\n"
"D0 8,32,46,2048\nAD 120,120,121\nMvaa 200,180,212\nTa 252,19,16384\nMvaa 232,180,220\nTa 254,19,18432\nMvaa 232,176,216\nv_permlane16_swap_b32 v118, v120\nMvaa 236,184,216\n"
"AD 118,118,120\nMvaa 236,188,220\nD0 9,32,46,3072\nMvaa 204,188,212\nv_add_f32_dpp v118, v118, v118 row_ror:8 row_mask:0xf bank_mask:0xf\nMvaa 204,184,208\nMaaa 108,184,200\n"
"v_add_f32_dpp v118, v118, v118 row_ror:4 row_mask:0xf bank_mask:0xf\nMaaa 108,188,204\nMvaa 44,188,196\nv_add_f32_dpp v118, v118, v118 row_ror:2 row_mask:0xf bank_mask:0xf\n"
"Mvaa 44,184,192\ns_waitcnt vmcnt(13) lgkmcnt(0)\ns_barrier\nMvaa 48,224,192\nRa 128,140,32768\ns_add_u32 m0, s47, -65536\ns_add_u32 s45, s45, 128\nMvaa 48,228,196\n"
"Ra 132,141,32768\nMaaa 112,228,204\nRa 136,140,34816\nD0 2,20,45,0\nMaaa 112,224,200\nRa 140,141,34816\nMvaa 208,224,208\nRa 144,142,32768\n"
"v_add_f32_dpp v118, v118, v118 row_ror:1 row_mask:0xf bank_mask:0xf\nMvaa 208,228,212\nRa 148,143,32768\nMvaa 240,228,220\nRa 152,142,34816\nMvaa 240,224,216\nRa 156,143,34816\n"
"D0 3,20,45,1024\nMvaa 244,232,216\nTa 160,12,32768\nCV 118,118,118\nMvaa 244,236,220\nTa 162,12,34816\nMvaa 212,236,212\nTa 164,12,49152\nMvaa 212,232,208\nTa 166,12,51200\n"
"Maaa 116,232,200\nTa 168,13,32768\nD0 4,20,45,2048\nMaaa 116,236,204\nTa 170,13,34816\nMvaa 52,236,196\nTa 172,13,49152\nMvaa 52,232,192\nTa 174,13,51200\nMvaa 56,240,192\n"
"Ta 176,14,32768\nv_lshlrev_b32 v118, 16, v118\nMvaa 56,244,196\nTa 178,14,34816\nD0 5,20,45,3072\nMaaa 120,244,204\nTa 180,14,49152\nMaaa 120,240,200\nTa 182,14,51200\n"
"Mvaa 216,240,208\nTa 184,15,32768\ns_add_u32 m0, s47, 0\ns_add_u32 s44, s44, 0x40000\nMvaa 216,244,212\nTa 186,15,34816\nMvaa 248,244,220\nTa 188,15,49152\nD0 6,24,44,0\n"
"Mvaa 248,240,216\nTa 190,15,51200\nMvaa 252,248,216\nv_mul_f32 v119, 0xbfb8aa3b, v118\nMvaa 252,252,220\nEX 119,119\nMvaa 220,252,212\nv_add_f32 v120, 0, v118\nMvaa 220,248,208\n"
"D0 7,24,44,1024\nMaaa 124,248,200\nv_add_f32 v119, 1.0, v119\nMaaa 124,252,204\nv_rcp_f32 v119, v119\nMvaa 60,252,196\nv_ashrrev_i32 v121, 31, v120\nMvaa 60,248,192\nWL 0\n"
"Mvaa 32,160,128\nTa 224,16,32768\nD0 8,24,44,2048\nMvaa 32,164,132\nTa 226,16,34816\nMaaa 96,164,140\nCV 119,119,119\nMaaa 96,160,136\nTa 228,16,49152\nMvaa 192,160,144\n"
"Ta 230,16,51200\nv_lshlrev_b32 v119, 16, v119\nMvaa 192,164,148\nD0 9,24,44,3072\nMvaa 224,164,156\nTa 232,17,32768\nMvaa 224,160,152\nTa 234,17,34816\nMvaa 228,168,152\n"
"s_add_u32 m0, s47, 16384\ns_add_u32 s44, s44, 0x40000\nMvaa 228,172,156\nTa 236,17,49152\nMvaa 196,172,148\nTa 238,17,51200\nD0 6,24,44,0\nMvaa 196,168,144\n"
"v_or_b32 v121, 0x80000000, v121\nMaaa 100,168,136\nTa 240,18,32768\nv_xor_b32 v120, v120, v121\nMaaa 100,172,140\nTa 242,18,34816\nMvaa 36,172,132\n"
"v_and_b32 v120, 0xffff0000, v120\nMvaa 36,168,128\nTa 244,18,49152\nD0 7,24,44,1024\nMvaa 40,176,128\nTa 246,18,51200\ns_lshl2_add_u32 s2, s10, s11\nMvaa 40,180,132\n"
"v_lshrrev_b32 v112, 5, v0\nMaaa 104,180,140\nTa 248,19,32768\nMaaa 104,176,136\nTa 250,19,34816\nMvaa 200,176,144\nD0 8,24,44,2048\nv_lshl_add_u32 v112, s2, 1, v112\n"
"Mvaa 200,180,148\nTa 252,19,49152\nMvaa 232,180,156\nTa 254,19,51200\nMvaa 232,176,152\nv_sub_u32 v113, 0xffff, v112\nMvaa 236,184,152\nv_or_b32 v120, v120, v113\n"
"v_mov_b32 v121, s15\nMvaa 236,188,156\nD0 9,24,44,3072\nMvaa 204,188,148\nv_and_b32 v113, 31, v0\nMvaa 204,184,144\nv_mov_b32 v116, 0x40000000\nv_cmp_eq_u32 vcc, 16, v113\n"
"v_lshlrev_b32 v114, 2, v112\nv_lshlrev_b32 v115, 10, v113\nv_cndmask_b32 v114, v116, v114, vcc\nv_cndmask_b32 v120, v119, v120, vcc\nMaaa 108,184,136\n"
"v_lshl_add_u32 v115, v112, 3, v115\nv_and_b32 v113, 15, v0\nMaaa 108,188,140\nv_cmp_eq_u32 vcc, 0, v113\ns_mov_b32 s2, 0xc040000\ns_mov_b32 s3, 0xc03e000\n"
"v_cndmask_b32 v115, v116, v115, vcc\nbuffer_store_dwordx2 v[120:121], v115, s[16:19], s2 offen sc0 sc1\nbuffer_store_dword v118, v114, s[16:19], s3 offen sc0 sc1\nMvaa 44,188,132\n"
"Mvaa 44,184,128\ns_waitcnt vmcnt(14) lgkmcnt(0)\ns_barrier\nMvaa 48,224,128\nRa 192,140,49152\ns_add_u32 m0, s47, -49152\ns_add_u32 s48, s48, 128\nMvaa 48,228,132\n"
"Ra 196,141,49152\nMaaa 112,228,140\nRa 200,140,51200\nD0 2,20,48,0\nMaaa 112,224,136\nRa 204,141,51200\nMvaa 208,224,144\nRa 208,142,49152\nMvaa 208,228,148\nRa 212,143,49152\n"
"Mvaa 240,228,156\nRa 216,142,51200\nMvaa 240,224,152\nRa 220,143,51200\nD0 3,20,48,1024\nMvaa 244,232,152\nTa 160,128,0\nMvaa 244,236,156\nTa 162,128,2048\nMvaa 212,236,148\n"
"Ta 164,128,16384\nMvaa 212,232,144\nTa 166,128,18432\nMaaa 116,232,136\nTa 168,129,0\nD0 4,20,48,2048\nMaaa 116,236,140\nTa 170,129,2048\nMvaa 52,236,132\nTa 172,129,16384\n"
"Mvaa 52,232,128\nTa 174,129,18432\nMvaa 56,240,128\nTa 176,130,0\nMvaa 56,244,132\nTa 178,130,2048\nD0 5,20,48,3072\nMaaa 120,244,140\nTa 180,130,16384\nMaaa 120,240,136\n"
"Ta 182,130,18432\nMvaa 216,240,144\nTa 184,131,0\ns_add_u32 m0, s47, -32768\ns_add_u32 s45, s45, 128\nMvaa 216,244,148\nTa 186,131,2048\nMvaa 248,244,156\nTa 188,131,16384\n"
"D0 2,20,45,0\nMvaa 248,240,152\nTa 190,131,18432\nMvaa 252,248,152\nMvaa 252,252,156\nMvaa 220,252,148\nMvaa 220,248,144\nD0 3,20,45,1024\nMaaa 124,248,136\nMaaa 124,252,140\n"
"Mvaa 60,252,132\nMvaa 60,248,128\nWL 0\nMvaa 32,160,192\nTa 224,132,0\nD0 4,20,45,2048\nMvaa 32,164,196\nTa 226,132,2048\nMvaa 36,172,196\nMvaa 36,168,192\nTa 228,132,16384\n"
"Mvaa 40,176,192\nTa 230,132,18432\nMvaa 40,180,196\nD0 5,20,45,3072\nMvaa 44,188,196\nTa 232,133,0\nMvaa 44,184,192\nTa 234,133,2048\nMaaa 108,184,200\ns_add_u32 m0, s47, 32768\n"
"s_add_u32 s44, s44, 0x40000\nMaaa 108,188,204\nTa 236,133,16384\nMaaa 104,180,204\nTa 238,133,18432\nD0 6,24,44,0\nMaaa 104,176,200\nv_mul_f32 v32, s57, v32\n"
"v_mul_f32 v33, s57, v33\nMaaa 100,168,200\nTa 240,134,0\nCV 32,32,33\nMaaa 100,172,204\nTa 242,134,2048\nv_mul_f32 v34, s57, v34\nMaaa 96,164,204\nv_mul_f32 v35, s57, v35\n"
"CV 33,34,35\nMaaa 96,160,200\nTa 244,134,16384\nD0 7,24,44,1024\nMvaa 192,160,208\nTa 246,134,18432\nv_mul_f32 v36, s57, v36\nMvaa 192,164,212\nv_mul_f32 v37, s57, v37\n"
"Mvaa 196,172,212\nTa 248,135,0\nCV 34,36,37\nMvaa 196,168,208\nTa 250,135,2048\nv_mul_f32 v38, s57, v38\nMvaa 200,176,208\nD0 8,24,44,2048\nv_mul_f32 v39, s57, v39\n"
"Mvaa 200,180,212\nTa 252,135,16384\nCV 35,38,39\nMvaa 204,188,212\nTa 254,135,18432\nv_mul_f32 v40, s57, v40\nMvaa 204,184,208\nv_mul_f32 v41, s57, v41\nCV 40,40,41\n"
"Mvaa 236,184,216\nv_mul_f32 v42, s57, v42\nv_mul_f32 v43, s57, v43\nMvaa 236,188,220\nD0 9,24,44,3072\nMvaa 232,180,220\nCV 41,42,43\nv_mul_f32 v44, s57, v44\nMvaa 232,176,216\n"
"v_mul_f32 v45, s57, v45\nCV 42,44,45\nMvaa 228,168,216\nv_mul_f32 v46, s57, v46\nv_mul_f32 v47, s57, v47\nMvaa 228,172,220\nCV 43,46,47\nAR 20,96\nMvaa 224,164,220\nAR 21,97\n"
"v_mul_f32 v20, s57, v20\nMvaa 224,160,216\ns_waitcnt vmcnt(14) lgkmcnt(0)\ns_barrier\nMvaa 48,224,192\ns_add_u32 m0, s47, 49152\ns_add_u32 s44, s44, 0x40000\nRa 128,10,0\n"
"Mvaa 48,228,196\nRa 132,11,0\nMvaa 52,236,196\nD0 6,24,44,0\nRa 136,10,2048\nMvaa 52,232,192\n.W538:\ns_cbranch_execz .W542\nRa 140,11,2048\nv_mul_f32 v21, s57, v21\n"
"Mvaa 56,240,192\nRa 144,10,8192\nCV 36,20,21\nMvaa 56,244,196\nRa 148,11,8192\nAR 20,98\nMvaa 60,252,196\nD0 7,24,44,1024\nRa 152,10,10240\nMvaa 60,248,192\nRa 156,11,10240\n"
"AR 21,99\nMaaa 124,248,200\nTa 160,12,0\nv_mul_f32 v20, s57, v20\nMaaa 124,252,204\nTa 162,12,2048\nMaaa 120,244,204\nD0 8,24,44,2048\nTa 164,12,16384\nMaaa 120,240,200\n"
"Ta 166,12,18432\nv_mul_f32 v21, s57, v21\nMaaa 116,232,200\nTa 168,13,0\nCV 37,20,21\nMaaa 116,236,204\nTa 170,13,2048\nAR 20,100\nMaaa 112,228,204\nD0 9,24,44,3072\n"
"Ta 172,13,16384\nMaaa 112,224,200\nTa 174,13,18432\nAR 21,101\nMvaa 208,224,208\ns_add_u32 m0, s47, -16384\ns_add_u32 s48, s48, 128\nTa 176,14,0\nMvaa 208,228,212\nTa 178,14,2048\n"
"Mvaa 212,236,212\nD0 2,20,48,0\nTa 180,14,16384\nMvaa 212,232,208\nTa 182,14,18432\nv_mul_f32 v20, s57, v20\nMvaa 216,240,208\nTa 184,15,0\nv_mul_f32 v21, s57, v21\n"
"Mvaa 216,244,212\nTa 186,15,2048\nCV 38,20,21\nMvaa 220,252,212\nD0 3,20,48,1024\nTa 188,15,16384\nMvaa 220,248,208\nTa 190,15,18432\nAR 20,102\nMvaa 252,248,216\nAR 21,103\n"
"v_mul_f32 v20, s57, v20\nMvaa 252,252,220\nv_mul_f32 v21, s57, v21\nMvaa 248,244,220\nD0 4,20,48,2048\nCV 39,20,21\nMvaa 248,240,216\nAR 20,104\nAR 21,105\nMvaa 244,232,216\n"
"v_mul_f32 v20, s57, v20\nv_mul_f32 v21, s57, v21\nMvaa 244,236,220\nCV 44,20,21\nAR 20,106\nMvaa 240,228,220\nD0 5,20,48,3072\nAR 21,107\nMvaa 240,224,216\nWL 0\ns_barrier\n"
"Zvaa 64,160,128\nTa 224,16,0\nv_mul_f32 v20, s57, v20\nMvaa 64,164,132\nTa 226,16,2048\nZvaa 96,164,140\ns_add_u32 m0, s47, -65536\ns_add_u32 s45, s45, 128\nMvaa 96,160,136\n"
"Ta 228,16,16384\nv_mul_f32 v21, s57, v21\nZvaa 128,160,144\nTa 230,16,18432\nCV 45,20,21\nMvaa 128,164,148\nD0 2,20,45,0\nAR 20,108\nZvaa 160,164,156\nTa 232,17,0\nAR 21,109\n"
"Mvaa 160,160,152\nTa 234,17,2048\nv_mul_f32 v20, s57, v20\nZvaa 164,168,152\nv_mul_f32 v21, s57, v21\nCV 46,20,21\nMvaa 164,172,156\nTa 236,17,16384\ns_add_u32 s6, s56, 0\n"
"Sv 32,1,16,6,0\nZvaa 132,172,148\nTa 238,17,18432\nAR 20,110\nMvaa 132,168,144\nAR 21,111\nv_mul_f32 v20, s57, v20\nZvaa 100,168,136\nTa 240,18,0\nv_mul_f32 v21, s57, v21\n"
"Mvaa 100,172,140\nTa 242,18,2048\nCV 47,20,21\nZvaa 68,172,132\nD0 3,20,45,1024\nv_mul_f32 v48, s57, v48\nMvaa 68,168,128\nTa 244,18,16384\nv_mul_f32 v49, s57, v49\n"
"Zvaa 72,176,128\nTa 246,18,18432\nCV 48,48,49\nMvaa 72,180,132\nv_mul_f32 v50, s57, v50\nZvaa 104,180,140\nTa 248,19,0\nv_mul_f32 v51, s57, v51\nMvaa 104,176,136\nTa 250,19,2048\n"
"CV 49,50,51\nZvaa 136,176,144\nv_mul_f32 v52, s57, v52\nv_mul_f32 v53, s57, v53\nMvaa 136,180,148\nTa 252,19,16384\nCV 50,52,53\nZvaa 168,180,156\nTa 254,19,18432\n"
"v_mul_f32 v54, s57, v54\nMvaa 168,176,152\nD0 4,20,45,2048\nv_mul_f32 v55, s57, v55\nZvaa 172,184,152\nCV 51,54,55\nv_mul_f32 v56, s57, v56\nMvaa 172,188,156\n"
"v_mul_f32 v57, s57, v57\ns_add_u32 s6, s56, 0\nSv 40,1,16,6,2048\nZvaa 140,188,148\nCV 56,56,57\nv_mul_f32 v58, s57, v58\nMvaa 140,184,144\nv_mul_f32 v59, s57, v59\nCV 57,58,59\n"
"Zvaa 108,184,136\nv_mul_f32 v60, s57, v60\nv_mul_f32 v61, s57, v61\nMvaa 108,188,140\nD0 5,20,45,3072\nCV 58,60,61\nZvaa 76,188,132\nv_mul_f32 v62, s57, v62\n"
"v_mul_f32 v63, s57, v63\nMvaa 76,184,128\ns_waitcnt vmcnt(18) lgkmcnt(0)\ns_barrier\nZvaa 80,224,128\nRa 192,10,16384\nCV 59,62,63\nMvaa 80,228,132\ns_add_u32 m0, s47, 0\n"
"s_add_u32 s44, s44, 0x40000\nZvaa 112,228,140\nRa 196,11,16384\nAR 20,112\nMvaa 112,224,136\nAR 21,113\nv_mul_f32 v20, s57, v20\nZvaa 144,224,144\nRa 200,10,18432\n"
"v_mul_f32 v21, s57, v21\nMvaa 144,228,148\nD0 6,24,44,0\nCV 52,20,21\nZvaa 176,228,156\nRa 204,11,18432\nAR 20,114\nMvaa 176,224,152\nAR 21,115\nv_mul_f32 v20, s57, v20\n"
"Zvaa 180,232,152\nRa 208,10,24576\nv_mul_f32 v21, s57, v21\nMvaa 180,236,156\nCV 53,20,21\ns_add_u32 s6, s56, 0\nSv 36,1,16,6,1024\nZvaa 148,236,148\nRa 212,11,24576\nAR 20,116\n"
"Mvaa 148,232,144\nAR 21,117\nv_mul_f32 v20, s57, v20\nZvaa 116,232,136\nRa 216,10,26624\nv_mul_f32 v21, s57, v21\nMvaa 116,236,140\nD0 7,24,44,1024\nCV 54,20,21\nZvaa 84,236,132\n"
"Ra 220,11,26624\nAR 20,118\nMvaa 84,232,128\nAR 21,119\nv_mul_f32 v20, s57, v20\nZvaa 88,240,128\nv_mul_f32 v21, s57, v21\nCV 55,20,21\nMvaa 88,244,132\nAR 20,120\n"
"Zvaa 120,244,140\nAR 21,121\nv_mul_f32 v20, s57, v20\nMvaa 120,240,136\nv_mul_f32 v21, s57, v21\nCV 60,20,21\nZvaa 152,240,144\nAR 20,122\nAR 21,123\nMvaa 152,244,148\n"
"D0 8,24,44,2048\nv_mul_f32 v20, s57, v20\nZvaa 184,244,156\nv_mul_f32 v21, s57, v21\nCV 61,20,21\nMvaa 184,240,152\nAR 20,124\nAR 21,125\nZvaa 188,248,152\n"
"v_mul_f32 v20, s57, v20\nv_mul_f32 v21, s57, v21\nMvaa 188,252,156\nCV 62,20,21\ns_add_u32 s6, s56, 0\nSv 44,1,16,6,3072\nZvaa 156,252,148\nAR 20,126\nAR 21,127\nMvaa 156,248,144\n"
"v_mul_f32 v20, s57, v20\nv_mul_f32 v21, s57, v21\nZvaa 124,248,136\nCV 63,20,21\nv_mul_f32 v192, s57, v192\nMvaa 124,252,140\nD0 9,24,44,3072\nv_mul_f32 v193, s57, v193\n"
"Zvaa 92,252,132\nCV 192,192,193\nv_mul_f32 v194, s57, v194\nMvaa 92,248,128\nWL 0\nZaaa 16,224,192\nRa 128,10,32768\nv_mul_f32 v195, s57, v195\nMaaa 16,228,196\n"
"s_add_u32 m0, s47, 16384\ns_add_u32 s44, s44, 0x40000\nZaaa 48,228,204\nRa 132,11,32768\nCV 193,194,195\nMaaa 48,224,200\nv_mul_f32 v196, s57, v196\nv_mul_f32 v197, s57, v197\n"
"Zaaa 80,224,208\nRa 136,10,34816\nCV 194,196,197\nMaaa 80,228,212\nD0 6,24,44,0\nv_mul_f32 v198, s57, v198\nZaaa 112,228,220\nRa 140,11,34816\nv_mul_f32 v199, s57, v199\n"
"Maaa 112,224,216\nCV 195,198,199\nv_mul_f32 v200, s57, v200\nZaaa 116,232,216\nRa 144,10,40960\nv_mul_f32 v201, s57, v201\nMaaa 116,236,220\nCV 200,200,201\n"
"v_mul_f32 v202, s57, v202\ns_add_u32 s6, s56, 4096\nSv 48,1,16,6,0\nZaaa 84,236,212\nRa 148,11,40960\nv_mul_f32 v203, s57, v203\nMaaa 84,232,208\nCV 201,202,203\n"
"v_mul_f32 v204, s57, v204\nZaaa 52,232,200\nRa 152,10,43008\nv_mul_f32 v205, s57, v205\nMaaa 52,236,204\nD0 7,24,44,1024\nCV 202,204,205\nZaaa 20,236,196\nRa 156,11,43008\n"
"v_mul_f32 v206, s57, v206\nMaaa 20,232,192\nv_mul_f32 v207, s57, v207\nCV 203,206,207\nZaaa 24,240,192\nv_mul_f32 v208, s57, v208\nv_mul_f32 v209, s57, v209\nMaaa 24,244,196\n"
"CV 208,208,209\nv_mul_f32 v210, s57, v210\nZaaa 56,244,204\nv_mul_f32 v211, s57, v211\nCV 209,210,211\nMaaa 56,240,200\nv_mul_f32 v212, s57, v212\nv_mul_f32 v213, s57, v213\n"
"Zaaa 88,240,208\nCV 210,212,213\nv_mul_f32 v214, s57, v214\nMaaa 88,244,212\nD0 8,24,44,2048\nv_mul_f32 v215, s57, v215\nZaaa 120,244,220\nCV 211,214,215\n"
"v_mul_f32 v216, s57, v216\nMaaa 120,240,216\nv_mul_f32 v217, s57, v217\nCV 216,216,217\nZaaa 124,248,216\nv_mul_f32 v218, s57, v218\nv_mul_f32 v219, s57, v219\nMaaa 124,252,220\n"
"CV 217,218,219\nv_mul_f32 v220, s57, v220\ns_add_u32 s6, s56, 4096\nSv 56,1,16,6,2048\nZaaa 92,252,212\nv_mul_f32 v221, s57, v221\nCV 218,220,221\nMaaa 92,248,208\n"
"v_mul_f32 v222, s57, v222\nv_mul_f32 v223, s57, v223\nZaaa 60,248,200\nCV 219,222,223\nv_mul_f32 v224, s57, v224\nMaaa 60,252,204\nD0 9,24,44,3072\nv_mul_f32 v225, s57, v225\n"
"Zaaa 28,252,196\nCV 224,224,225\nv_mul_f32 v226, s57, v226\nMaaa 28,248,192\ns_waitcnt vmcnt(22) lgkmcnt(0)\ns_barrier\nZaaa 0,160,192\nTa 224,16,32768\nv_mul_f32 v227, s57, v227\n"
"Maaa 0,164,196\nTa 226,16,34816\nCV 225,226,227\nZaaa 32,164,204\ns_add_u32 m0, s47, -49152\ns_add_u32 s48, s48, 128\nMaaa 32,160,200\nTa 228,16,49152\nv_mul_f32 v228, s57, v228\n"
"Zaaa 64,160,208\nTa 230,16,51200\nv_mul_f32 v229, s57, v229\nMaaa 64,164,212\nD0 2,20,48,0\nCV 226,228,229\nZaaa 96,164,220\nTa 232,17,32768\nv_mul_f32 v230, s57, v230\n"
"Maaa 96,160,216\nTa 234,17,34816\nv_mul_f32 v231, s57, v231\nZaaa 100,168,216\nCV 227,230,231\nv_mul_f32 v232, s57, v232\nMaaa 100,172,220\nTa 236,17,49152\n"
"v_mul_f32 v233, s57, v233\ns_add_u32 s6, s56, 4096\nSv 52,1,16,6,1024\nZaaa 68,172,212\nTa 238,17,51200\nCV 232,232,233\nMaaa 68,168,208\nv_mul_f32 v234, s57, v234\n"
"v_mul_f32 v235, s57, v235\nZaaa 36,168,200\nTa 240,18,32768\nCV 233,234,235\nMaaa 36,172,204\nTa 242,18,34816\nv_mul_f32 v236, s57, v236\nZaaa 4,172,196\nD0 3,20,48,1024\n"
"v_mul_f32 v237, s57, v237\nMaaa 4,168,192\nTa 244,18,49152\nCV 234,236,237\nZaaa 8,176,192\nTa 246,18,51200\nv_mul_f32 v238, s57, v238\nMaaa 8,180,196\nv_mul_f32 v239, s57, v239\n"
"CV 235,238,239\nZaaa 40,180,204\nTa 248,19,32768\nv_mul_f32 v240, s57, v240\nMaaa 40,176,200\nTa 250,19,34816\nv_mul_f32 v241, s57, v241\nZaaa 72,176,208\nCV 240,240,241\n"
"v_mul_f32 v242, s57, v242\nMaaa 72,180,212\nTa 252,19,49152\nv_mul_f32 v243, s57, v243\nZaaa 104,180,220\nTa 254,19,51200\nCV 241,242,243\nMaaa 104,176,216\nD0 4,20,48,2048\n"
"v_mul_f32 v244, s57, v244\nZaaa 108,184,216\nv_mul_f32 v245, s57, v245\nCV 242,244,245\nMaaa 108,188,220\nv_mul_f32 v246, s57, v246\nv_mul_f32 v247, s57, v247\n"
"s_add_u32 s6, s56, 4096\nSv 60,1,16,6,3072\nZaaa 76,188,212\nCV 243,246,247\nv_mul_f32 v248, s57, v248\nMaaa 76,184,208\nv_mul_f32 v249, s57, v249\nCV 248,248,249\n"
"Zaaa 44,184,200\nv_mul_f32 v250, s57, v250\nv_mul_f32 v251, s57, v251\nMaaa 44,188,204\nD0 5,20,48,3072\nCV 249,250,251\nZaaa 12,188,196\nv_mul_f32 v252, s57, v252\n"
"v_mul_f32 v253, s57, v253\nMaaa 12,184,192\nWL 0\nMvaa 80,224,128\nTa 160,12,32768\nCV 250,252,253\nMvaa 80,228,132\nTa 162,12,34816\nv_mul_f32 v254, s57, v254\nMvaa 112,228,140\n"
"s_add_u32 m0, s47, -32768\ns_add_u32 s45, s45, 128\nMvaa 112,224,136\nTa 164,12,49152\nv_mul_f32 v255, s57, v255\nMvaa 144,224,144\nTa 166,12,51200\nCV 251,254,255\n"
"Mvaa 144,228,148\nD0 2,20,45,0\nMvaa 176,228,156\nTa 168,13,32768\nMvaa 176,224,152\nTa 170,13,34816\nMvaa 180,232,152\nMvaa 180,236,156\nTa 172,13,49152\ns_add_u32 s6, s53, 0\n"
"Sv 192,1,16,6,0\nMvaa 148,236,148\nTa 174,13,51200\nMvaa 148,232,144\nMvaa 116,232,136\nTa 176,14,32768\nMvaa 116,236,140\nTa 178,14,34816\nMvaa 84,236,132\nD0 3,20,45,1024\n"
"Mvaa 84,232,128\nTa 180,14,49152\nMvaa 88,240,128\nTa 182,14,51200\nMvaa 88,244,132\nMvaa 120,244,140\nTa 184,15,32768\nMvaa 120,240,136\nTa 186,15,34816\nMvaa 152,240,144\n"
"Mvaa 152,244,148\nTa 188,15,49152\nMvaa 184,244,156\nTa 190,15,51200\nMvaa 184,240,152\nD0 4,20,45,2048\nMvaa 188,248,152\nMvaa 188,252,156\ns_add_u32 s6, s53, 0\n"
"Sv 200,1,16,6,2048\nMvaa 156,252,148\nMvaa 156,248,144\nMvaa 124,248,136\nMvaa 124,252,140\nD0 5,20,45,3072\nMvaa 92,252,132\nMvaa 92,248,128\ns_waitcnt vmcnt(24) lgkmcnt(0)\n"
"s_barrier\nMvaa 64,160,128\nRa 192,10,49152\nMvaa 64,164,132\ns_add_u32 m0, s47, 32768\ns_add_u32 s44, s44, 0x40000\nMvaa 96,164,140\nRa 196,11,49152\nMvaa 96,160,136\n"
"Mvaa 128,160,144\nRa 200,10,51200\nMvaa 128,164,148\nD0 6,24,44,0\nMvaa 160,164,156\nRa 204,11,51200\nMvaa 160,160,152\nMvaa 164,168,152\nRa 208,10,57344\nMvaa 164,172,156\n"
"s_add_u32 s6, s53, 4096\nSv 208,1,16,6,0\nMvaa 132,172,148\nRa 212,11,57344\nMvaa 132,168,144\nMvaa 100,168,136\nRa 216,10,59392\nMvaa 100,172,140\nD0 7,24,44,1024\n"
"Mvaa 68,172,132\nRa 220,11,59392\nMvaa 68,168,128\nMvaa 72,176,128\nMvaa 72,180,132\nMvaa 104,180,140\nMvaa 104,176,136\nMvaa 136,176,144\nMvaa 136,180,148\n.W539:\n"
"s_cbranch_execz .W543\nD0 8,24,44,2048\nMvaa 168,180,156\nMvaa 168,176,152\nMvaa 172,184,152\nMvaa 172,188,156\ns_add_u32 s6, s53, 4096\nSv 216,1,16,6,2048\nMvaa 140,188,148\n"
"Mvaa 140,184,144\nMvaa 108,184,136\nMvaa 108,188,140\nD0 9,24,44,3072\nMvaa 76,188,132\nMvaa 76,184,128\nWL 0\nMaaa 0,160,192\nRa 128,10,0\nMaaa 0,164,196\n"
"s_add_u32 m0, s47, 49152\ns_add_u32 s44, s44, 0x40000\nMaaa 32,164,204\nRa 132,11,0\nMaaa 32,160,200\nMaaa 64,160,208\nRa 136,10,2048\nMaaa 64,164,212\nD0 6,24,44,0\n"
"Maaa 96,164,220\nRa 140,11,2048\nMaaa 96,160,216\nMaaa 100,168,216\nRa 144,10,8192\nMaaa 100,172,220\ns_add_u32 s6, s53, 0\nSv 224,1,16,6,1024\nMaaa 68,172,212\nRa 148,11,8192\n"
"Maaa 68,168,208\nMaaa 36,168,200\nRa 152,10,10240\nMaaa 36,172,204\nD0 7,24,44,1024\nMaaa 4,172,196\nRa 156,11,10240\nMaaa 4,168,192\nMaaa 8,176,192\nMaaa 8,180,196\n"
"Maaa 40,180,204\nMaaa 40,176,200\nMaaa 72,176,208\nMaaa 72,180,212\nD0 8,24,44,2048\nMaaa 104,180,220\nMaaa 104,176,216\nMaaa 108,184,216\nMaaa 108,188,220\ns_add_u32 s6, s53, 0\n"
"Sv 232,1,16,6,3072\nMaaa 76,188,212\nMaaa 76,184,208\nMaaa 44,184,200\nMaaa 44,188,204\nD0 9,24,44,3072\nMaaa 12,188,196\nMaaa 12,184,192\ns_waitcnt vmcnt(24) lgkmcnt(0)\n"
"s_barrier\nMaaa 16,224,192\nTa 160,12,0\nMaaa 16,228,196\nTa 162,12,2048\nMaaa 48,228,204\ns_add_u32 m0, s47, -16384\ns_add_u32 s48, s48, 128\nMaaa 48,224,200\nTa 164,12,16384\n"
"Maaa 80,224,208\nTa 166,12,18432\nMaaa 80,228,212\nD0 2,20,48,0\nMaaa 112,228,220\nTa 168,13,0\nMaaa 112,224,216\nTa 170,13,2048\nMaaa 116,232,216\nMaaa 116,236,220\n"
"Ta 172,13,16384\ns_add_u32 s6, s53, 4096\nSv 240,1,16,6,1024\nMaaa 84,236,212\nTa 174,13,18432\nMaaa 84,232,208\nMaaa 52,232,200\nTa 176,14,0\nMaaa 52,236,204\nTa 178,14,2048\n"
"Maaa 20,236,196\nD0 3,20,48,1024\nMaaa 20,232,192\nTa 180,14,16384\nMaaa 24,240,192\nTa 182,14,18432\nMaaa 24,244,196\ns_add_u32 s6, s53, 4096\nSv 248,1,16,6,3072\n"
"Maaa 56,244,204\nTa 184,15,0\nMaaa 56,240,200\nTa 186,15,2048\nMaaa 88,240,208\nMaaa 88,244,212\nTa 188,15,16384\nMaaa 120,244,220\nTa 190,15,18432\nMaaa 120,240,216\n"
"D0 4,20,48,2048\nMaaa 124,248,216\nMaaa 124,252,220\nMaaa 92,252,212\nMaaa 92,248,208\ns_lshl3_add_u32 s90, s12, s13\ns_lshl2_add_u32 s90, s90, s11\n"
"s_lshl2_add_u32 s90, s90, 0xc04a400\nMaaa 60,248,200\nMaaa 60,252,204\nD0 5,20,48,3072\nMaaa 28,252,196\nMaaa 28,248,192\nWL 0\ns_mov_b64 s[76:77], s[62:63]\n"
"s_mov_b64 s[78:79], s[64:65]\ns_mov_b32 s42, 1605632\ns_mov_b32 s50, 1\n.GL20:\nMvaa 64,160,128\nTa 224,16,0\nMvaa 64,164,132\nTa 226,16,2048\nMvaa 96,164,140\n"
"s_add_u32 m0, s47, -65536\ns_add_u32 s45, s45, 128\nMvaa 96,160,136\nTa 228,16,16384\nMvaa 128,160,144\nTa 230,16,18432\nMvaa 128,164,148\nD0 2,20,45,0\nMvaa 160,164,156\n"
"Ta 232,17,0\nMvaa 160,160,152\nTa 234,17,2048\nMvaa 164,168,152\nMvaa 164,172,156\nTa 236,17,16384\nMvaa 132,172,148\nTa 238,17,18432\nMvaa 132,168,144\nMvaa 100,168,136\n"
"Ta 240,18,0\nMvaa 100,172,140\nTa 242,18,2048\nMvaa 68,172,132\nD0 3,20,45,1024\nMvaa 68,168,128\nTa 244,18,16384\nMvaa 72,176,128\nTa 246,18,18432\nMvaa 72,180,132\n"
"Mvaa 104,180,140\nTa 248,19,0\nMvaa 104,176,136\nTa 250,19,2048\nMvaa 136,176,144\nMvaa 136,180,148\nTa 252,19,16384\nMvaa 168,180,156\nTa 254,19,18432\nMvaa 168,176,152\n"
"D0 4,20,45,2048\nMvaa 172,184,152\nMvaa 172,188,156\nMvaa 140,188,148\nMvaa 140,184,144\ns_mov_b32 s40, s76\ns_and_b32 s41, s77, 0xffff\nMvaa 108,184,136\nMvaa 108,188,140\n"
"D0 5,20,45,3072\nMvaa 76,188,132\nMvaa 76,184,128\ns_waitcnt vmcnt(17) lgkmcnt(0)\ns_barrier\nMvaa 80,224,128\nRa 192,10,16384\nMvaa 80,228,132\ns_add_u32 m0, s47, 0\n"
"s_add_u32 s44, s44, 0x40000\nbuffer_load_dword v31, v26, s[40:43], s97 offen\ns_mov_b64 s[76:77], s[66:67]\nMvaa 112,228,140\nRa 196,11,16384\nMvaa 112,224,136\nMvaa 144,224,144\n"
"Ra 200,10,18432\nMvaa 144,228,148\nD0 6,24,44,0\nMvaa 176,228,156\nRa 204,11,18432\nMvaa 176,224,152\nMvaa 180,232,152\nRa 208,10,24576\nMvaa 180,236,156\nMvaa 148,236,148\n"
"Ra 212,11,24576\nMvaa 148,232,144\nMvaa 116,232,136\nRa 216,10,26624\nMvaa 116,236,140\nD0 7,24,44,1024\nMvaa 84,236,132\nRa 220,11,26624\nMvaa 84,232,128\nMvaa 88,240,128\n"
"Mvaa 88,244,132\nMvaa 120,244,140\nMvaa 120,240,136\nMvaa 152,240,144\nMvaa 152,244,148\nD0 8,24,44,2048\nMvaa 184,244,156\nMvaa 184,240,152\nMvaa 188,248,152\nMvaa 188,252,156\n"
"Mvaa 156,252,148\nMvaa 156,248,144\nMvaa 124,248,136\nMvaa 124,252,140\nD0 9,24,44,3072\nMvaa 92,252,132\nMvaa 92,248,128\nWL 0\nMaaa 16,224,192\nRa 128,10,32768\nMaaa 16,228,196\n"
"s_add_u32 m0, s47, 16384\ns_add_u32 s44, s44, 0x40000\nMaaa 48,228,204\nRa 132,11,32768\nMaaa 48,224,200\nMaaa 80,224,208\nRa 136,10,34816\nMaaa 80,228,212\nD0 6,24,44,0\n"
"Maaa 112,228,220\nRa 140,11,34816\nMaaa 112,224,216\nMaaa 116,232,216\nRa 144,10,40960\nMaaa 116,236,220\nMaaa 84,236,212\nRa 148,11,40960\nMaaa 84,232,208\nMaaa 52,232,200\n"
"Ra 152,10,43008\nMaaa 52,236,204\nD0 7,24,44,1024\nMaaa 20,236,196\nRa 156,11,43008\nMaaa 20,232,192\nMaaa 24,240,192\nMaaa 24,244,196\nMaaa 56,244,204\nMaaa 56,240,200\n"
"Maaa 88,240,208\nMaaa 88,244,212\nD0 8,24,44,2048\nMaaa 120,244,220\nMaaa 120,240,216\nMaaa 124,248,216\nMaaa 124,252,220\nMaaa 92,252,212\nMaaa 92,248,208\nMaaa 60,248,200\n"
"Maaa 60,252,204\nD0 9,24,44,3072\nMaaa 28,252,196\nMaaa 28,248,192\ns_waitcnt vmcnt(17) lgkmcnt(0)\ns_barrier\nMaaa 0,160,192\nTa 224,16,32768\nMaaa 0,164,196\nTa 226,16,34816\n"
"Maaa 32,164,204\ns_add_u32 m0, s47, -49152\ns_add_u32 s48, s48, 128\nMaaa 32,160,200\nTa 228,16,49152\nMaaa 64,160,208\nTa 230,16,51200\nMaaa 64,164,212\nD0 2,20,48,0\n"
"Maaa 96,164,220\nTa 232,17,32768\nMaaa 96,160,216\nTa 234,17,34816\nMaaa 100,168,216\nMaaa 100,172,220\nTa 236,17,49152\nMaaa 68,172,212\nTa 238,17,51200\nMaaa 68,168,208\n"
"Maaa 36,168,200\nTa 240,18,32768\nMaaa 36,172,204\nTa 242,18,34816\nMaaa 4,172,196\nD0 3,20,48,1024\nMaaa 4,168,192\nTa 244,18,49152\nMaaa 8,176,192\nTa 246,18,51200\n"
"Maaa 8,180,196\nMaaa 40,180,204\nTa 248,19,32768\nMaaa 40,176,200\nTa 250,19,34816\nMaaa 72,176,208\nMaaa 72,180,212\nTa 252,19,49152\nMaaa 104,180,220\nTa 254,19,51200\n"
"Maaa 104,176,216\nD0 4,20,48,2048\nMaaa 108,184,216\nMaaa 108,188,220\nMaaa 76,188,212\nMaaa 76,184,208\nMaaa 44,184,200\nMaaa 44,188,204\nD0 5,20,48,3072\nMaaa 12,188,196\n"
"Maaa 12,184,192\nWL 0\nMvaa 80,224,128\nTa 160,12,32768\nMvaa 80,228,132\nTa 162,12,34816\nMvaa 112,228,140\ns_add_u32 m0, s47, -32768\ns_add_u32 s45, s45, 128\nMvaa 112,224,136\n"
"Ta 164,12,49152\nMvaa 144,224,144\nTa 166,12,51200\nMvaa 144,228,148\nD0 2,20,45,0\nMvaa 176,228,156\nTa 168,13,32768\nMvaa 176,224,152\nTa 170,13,34816\nMvaa 180,232,152\n"
"Mvaa 180,236,156\nTa 172,13,49152\nMvaa 148,236,148\nTa 174,13,51200\nMvaa 148,232,144\nMvaa 116,232,136\nTa 176,14,32768\nMvaa 116,236,140\nTa 178,14,34816\nMvaa 84,236,132\n"
"D0 3,20,45,1024\nMvaa 84,232,128\nTa 180,14,49152\nMvaa 88,240,128\nTa 182,14,51200\nMvaa 88,244,132\nMvaa 120,244,140\nTa 184,15,32768\nMvaa 120,240,136\nTa 186,15,34816\n"
"Mvaa 152,240,144\nMvaa 152,244,148\nTa 188,15,49152\nMvaa 184,244,156\nTa 190,15,51200\nMvaa 184,240,152\nD0 4,20,45,2048\nMvaa 188,248,152\nMvaa 188,252,156\nMvaa 156,252,148\n"
"Mvaa 156,248,144\ns_mov_b32 s40, s78\ns_and_b32 s41, s79, 0xffff\nMvaa 124,248,136\nMvaa 124,252,140\nD0 5,20,45,3072\nMvaa 92,252,132\nMvaa 92,248,128\n"
"s_waitcnt vmcnt(17) lgkmcnt(0)\ns_barrier\nMvaa 64,160,128\nRa 192,10,49152\nMvaa 64,164,132\ns_add_u32 m0, s47, 32768\ns_add_u32 s44, s44, 0x40000\n"
"buffer_load_dword v31, v26, s[40:43], s97 offen\ns_mov_b64 s[78:79], s[66:67]\nMvaa 96,164,140\nRa 196,11,49152\nMvaa 96,160,136\nMvaa 128,160,144\nRa 200,10,51200\n"
"Mvaa 128,164,148\nD0 6,24,44,0\nMvaa 160,164,156\nRa 204,11,51200\nMvaa 160,160,152\nMvaa 164,168,152\nRa 208,10,57344\nMvaa 164,172,156\nMvaa 132,172,148\nRa 212,11,57344\n"
"Mvaa 132,168,144\nMvaa 100,168,136\nRa 216,10,59392\nMvaa 100,172,140\nD0 7,24,44,1024\nMvaa 68,172,132\nRa 220,11,59392\nMvaa 68,168,128\nMvaa 72,176,128\nMvaa 72,180,132\n"
"Mvaa 104,180,140\nMvaa 104,176,136\nMvaa 136,176,144\nMvaa 136,180,148\nD0 8,24,44,2048\nMvaa 168,180,156\nMvaa 168,176,152\nMvaa 172,184,152\nMvaa 172,188,156\nMvaa 140,188,148\n"
"Mvaa 140,184,144\nMvaa 108,184,136\nMvaa 108,188,140\nD0 9,24,44,3072\nMvaa 76,188,132\nMvaa 76,184,128\nWL 0\nMaaa 0,160,192\nRa 128,10,0\nMaaa 0,164,196\n"
"s_add_u32 m0, s47, 49152\ns_add_u32 s44, s44, 0x40000\nMaaa 32,164,204\nRa 132,11,0\nMaaa 32,160,200\nMaaa 64,160,208\nRa 136,10,2048\nMaaa 64,164,212\nD0 6,24,44,0\n"
"Maaa 96,164,220\nRa 140,11,2048\nMaaa 96,160,216\nMaaa 100,168,216\nRa 144,10,8192\nMaaa 100,172,220\nMaaa 68,172,212\nRa 148,11,8192\nMaaa 68,168,208\nMaaa 36,168,200\n"
"Ra 152,10,10240\nMaaa 36,172,204\nD0 7,24,44,1024\nMaaa 4,172,196\nRa 156,11,10240\nMaaa 4,168,192\nMaaa 8,176,192\nMaaa 8,180,196\nMaaa 40,180,204\nMaaa 40,176,200\n"
"Maaa 72,176,208\nMaaa 72,180,212\nD0 8,24,44,2048\nMaaa 104,180,220\nMaaa 104,176,216\nMaaa 108,184,216\nMaaa 108,188,220\nMaaa 76,188,212\nMaaa 76,184,208\nMaaa 44,184,200\n"
"Maaa 44,188,204\nD0 9,24,44,3072\nMaaa 12,188,196\nMaaa 12,184,192\ns_waitcnt vmcnt(17) lgkmcnt(0)\ns_barrier\nMaaa 16,224,192\nTa 160,12,0\nMaaa 16,228,196\nTa 162,12,2048\n"
"Maaa 48,228,204\ns_add_u32 m0, s47, -16384\ns_add_u32 s48, s48, 128\nMaaa 48,224,200\nTa 164,12,16384\nMaaa 80,224,208\nTa 166,12,18432\nMaaa 80,228,212\nD0 2,20,48,0\n"
"Maaa 112,228,220\nTa 168,13,0\nMaaa 112,224,216\nTa 170,13,2048\nMaaa 116,232,216\nMaaa 116,236,220\nTa 172,13,16384\nMaaa 84,236,212\nTa 174,13,18432\nMaaa 84,232,208\n"
"Maaa 52,232,200\nTa 176,14,0\nMaaa 52,236,204\nTa 178,14,2048\n.W540:\ns_cbranch_execz .W544\nMaaa 20,236,196\nD0 3,20,48,1024\nMaaa 20,232,192\nTa 180,14,16384\nMaaa 24,240,192\n"
"Ta 182,14,18432\nMaaa 24,244,196\nMaaa 56,244,204\nTa 184,15,0\nMaaa 56,240,200\nTa 186,15,2048\nMaaa 88,240,208\nMaaa 88,244,212\nTa 188,15,16384\nMaaa 120,244,220\n"
"Ta 190,15,18432\nMaaa 120,240,216\nD0 4,20,48,2048\nMaaa 124,248,216\nMaaa 124,252,220\nMaaa 92,252,212\nMaaa 92,248,208\nMaaa 60,248,200\nMaaa 60,252,204\nD0 5,20,48,3072\n"
"s_sub_u32 s50, s50, 1\nMaaa 28,252,196\nMaaa 28,248,192\nWL 0\ns_cbranch_scc0 .GL20\nMvaa 64,160,128\nTa 224,16,0\nMvaa 64,164,132\nTa 226,16,2048\nMvaa 96,164,140\n"
"s_add_u32 m0, s47, -65536\ns_add_u32 s45, s45, 128\nMvaa 96,160,136\nTa 228,16,16384\nMvaa 128,160,144\nTa 230,16,18432\nMvaa 128,164,148\nD0 2,20,45,0\nMvaa 160,164,156\n"
"Ta 232,17,0\nMvaa 160,160,152\nTa 234,17,2048\nMvaa 164,168,152\ns_and_b32 s2, s74, 15\nMvaa 164,172,156\nTa 236,17,16384\n"
"buffer_store_dword v25, off, s[16:19], s90 offset:0 sc0 sc1\nMvaa 132,172,148\nTa 238,17,18432\nMvaa 132,168,144\ns_sub_u32 s2, s2, 2\ns_cmp_lt_u32 s2, 14\n"
"s_cselect_b32 s5, 0, 0x40000000\nMvaa 100,168,136\nTa 240,18,0\nMvaa 100,172,140\nTa 242,18,2048\nMvaa 68,172,132\nD0 3,20,45,1024\nMvaa 68,168,128\nTa 244,18,16384\n"
"Mvaa 72,176,128\nTa 246,18,18432\nMvaa 72,180,132\ns_and_b32 s2, s2, 15\nMvaa 104,180,140\nTa 248,19,0\nMvaa 104,176,136\nTa 250,19,2048\nMvaa 136,176,144\ns_mul_i32 s4, s73, 14\n"
"Mvaa 136,180,148\nTa 252,19,16384\nMvaa 168,180,156\nTa 254,19,18432\nMvaa 168,176,152\nD0 4,20,45,2048\nMvaa 172,184,152\ns_add_u32 s4, s4, s2\nMvaa 172,188,156\n"
"s_mov_b32 s36, s62\nMvaa 140,188,148\ns_and_b32 s37, s63, 0xffff\nMvaa 140,184,144\ns_mov_b32 s38, 0x188000\nMvaa 108,184,136\ns_mov_b32 s39, 0x20000\nMvaa 108,188,140\n"
"D0 5,20,45,3072\nMvaa 76,188,132\ns_mov_b32 s40, s64\nMvaa 76,184,128\ns_waitcnt vmcnt(18) lgkmcnt(0)\ns_barrier\nMvaa 80,224,128\nRa 192,10,16384\nMvaa 80,228,132\n"
"s_add_u32 m0, s47, 0\ns_add_u32 s44, s44, 0x40000\nMvaa 112,228,140\nRa 196,11,16384\nMvaa 112,224,136\ns_and_b32 s41, s65, 0xffff\nMvaa 144,224,144\nRa 200,10,18432\n"
"Mvaa 144,228,148\nD0 6,24,44,0\nMvaa 176,228,156\nRa 204,11,18432\nMvaa 176,224,152\ns_mov_b32 s42, 0x188000\nMvaa 180,232,152\nRa 208,10,24576\nMvaa 180,236,156\n"
"s_mov_b32 s43, 0x20000\nMvaa 148,236,148\nRa 212,11,24576\nMvaa 148,232,144\ns_mov_b32 s92, s66\nMvaa 116,232,136\nRa 216,10,26624\nMvaa 116,236,140\nD0 7,24,44,1024\n"
"Mvaa 84,236,132\nRa 220,11,26624\nMvaa 84,232,128\ns_and_b32 s93, s67, 0xffff\nMvaa 88,240,128\ns_mov_b32 s94, 0x188000\nMvaa 88,244,132\ns_mov_b32 s95, 0x20000\nMvaa 120,244,140\n"
"v_lshrrev_b32 v252, 1, v0\nMvaa 120,240,136\nv_mul_u32_u24 v253, 14336, v252\nMvaa 152,240,144\nv_and_b32 v254, 1, v0\nMvaa 152,244,148\nD0 8,24,44,2048\nMvaa 184,244,156\n"
"v_lshl_add_u32 v253, v254, 4, v253\nMvaa 184,240,152\nv_mov_b32 v254, 0x40000000\nv_cmp_gt_u32 vcc, 56, v0\nv_lshlrev_b32 v252, 3, v252\ns_mul_i32 s2, s11, 401408\n"
"v_cndmask_b32 v21, v254, v253, vcc\nv_cndmask_b32 v255, v254, v252, vcc\nMvaa 188,248,152\ns_lshl_b32 s3, s4, 5\nMvaa 188,252,156\ns_add_u32 s2, s2, s3\nMvaa 156,252,148\n"
"s_or_b32 s86, s2, s5\nMvaa 156,248,144\ns_add_u32 s87, s86, 7168\nMvaa 124,248,136\nv_lshrrev_b32 v251, 4, v0\nMvaa 124,252,140\nD0 9,24,44,3072\nMvaa 92,252,132\n"
"v_mul_u32_u24 v252, 3584, v251\nMvaa 92,248,128\nWL 0\nMaaa 16,224,192\nRa 128,10,32768\nMaaa 16,228,196\ns_add_u32 m0, s47, 16384\ns_add_u32 s44, s44, 0x40000\nMaaa 48,228,204\n"
"Ra 132,11,32768\nMaaa 48,224,200\nv_and_b32 v251, 15, v0\nMaaa 80,224,208\nRa 136,10,34816\nMaaa 80,228,212\nD0 6,24,44,0\nMaaa 112,228,220\nRa 140,11,34816\nMaaa 112,224,216\n"
"v_lshl_add_u32 v252, v251, 4, v252\nv_cmp_gt_u32 vcc, 14, v251\ns_mul_i32 s2, s4, 14336\ns_mul_i32 s3, s11, 224\nv_cndmask_b32 v20, v254, v252, vcc\nMaaa 116,232,216\n"
"Ra 144,10,40960\nMaaa 116,236,220\ns_add_u32 s2, s2, s3\nMaaa 84,236,212\nRa 148,11,40960\nMaaa 84,232,208\ns_or_b32 s88, s2, s5\nMaaa 52,232,200\nRa 152,10,43008\n"
"Maaa 52,236,204\nD0 7,24,44,1024\nMaaa 20,236,196\nRa 156,11,43008\nMaaa 20,232,192\nMaaa 24,240,192\nMaaa 24,244,196\nMaaa 56,244,204\nMaaa 56,240,200\nMaaa 88,240,208\n"
"Maaa 88,244,212\nD0 8,24,44,2048\nMaaa 120,244,220\nMaaa 120,240,216\nMaaa 124,248,216\nMaaa 124,252,220\nMaaa 92,252,212\nMaaa 92,248,208\nMaaa 60,248,200\nMaaa 60,252,204\n"
"D0 9,24,44,3072\nMaaa 28,252,196\nMaaa 28,248,192\ns_waitcnt vmcnt(17) lgkmcnt(0)\ns_barrier\nMaaa 0,160,192\nTa 224,16,32768\nMaaa 0,164,196\nTa 226,16,34816\n"
"buffer_load_dwordx4 v[192:195], v21, s[36:39], s86 offen offset:0\nMaaa 32,164,204\ns_add_u32 m0, s47, -49152\ns_add_u32 s48, s48, 128\nMaaa 32,160,200\nTa 228,16,49152\n"
"s_lshl_b32 s6, s11, 12\ns_add_u32 s6, s6, 0xc044000\nMaaa 64,160,208\nTa 230,16,51200\nMaaa 64,164,212\nD0 2,20,48,0\nMaaa 96,164,220\nTa 232,17,32768\nMaaa 96,160,216\n"
"Ta 234,17,34816\nMaaa 100,168,216\nbuffer_load_dwordx4 v[32:35], v1, s[16:19], s6 offen offset:0 sc0 sc1\nMaaa 100,172,220\nTa 236,17,49152\nMaaa 68,172,212\nTa 238,17,51200\n"
"Maaa 68,168,208\nMaaa 36,168,200\nTa 240,18,32768\nMaaa 36,172,204\nTa 242,18,34816\nMaaa 4,172,196\nD0 3,20,48,1024\nMaaa 4,168,192\nTa 244,18,49152\nMaaa 8,176,192\n"
"Ta 246,18,51200\nMaaa 8,180,196\nbuffer_load_dwordx4 v[36:39], v1, s[16:19], s6 offen offset:1024 sc0 sc1\nMaaa 40,180,204\nTa 248,19,32768\nMaaa 40,176,200\nTa 250,19,34816\n"
"Maaa 72,176,208\nMaaa 72,180,212\nTa 252,19,49152\nMaaa 104,180,220\nTa 254,19,51200\nMaaa 104,176,216\nD0 4,20,48,2048\nMaaa 108,184,216\n"
"buffer_load_dwordx4 v[40:43], v1, s[16:19], s6 offen offset:2048 sc0 sc1\nMaaa 108,188,220\nMaaa 76,188,212\nMaaa 76,184,208\nMaaa 44,184,200\n"
"buffer_load_dwordx4 v[44:47], v1, s[16:19], s6 offen offset:3072 sc0 sc1\nMaaa 44,188,204\nD0 5,20,48,3072\nMaaa 12,188,196\nMaaa 12,184,192\nWL 0\nMvaa 80,224,128\n"
"Ta 160,12,32768\nMvaa 80,228,132\nTa 162,12,34816\nMvaa 112,228,140\ns_add_u32 m0, s47, -32768\ns_add_u32 s45, s45, 128\nMvaa 112,224,136\nTa 164,12,49152\nMvaa 144,224,144\n"
"Ta 166,12,51200\nMvaa 144,228,148\nD0 2,20,45,0\nMvaa 176,228,156\nTa 168,13,32768\nMvaa 176,224,152\nTa 170,13,34816\nMvaa 180,232,152\nMvaa 180,236,156\nTa 172,13,49152\n"
"Mvaa 148,236,148\nTa 174,13,51200\nMvaa 148,232,144\nMvaa 116,232,136\nTa 176,14,32768\nMvaa 116,236,140\nTa 178,14,34816\nMvaa 84,236,132\nD0 3,20,45,1024\nMvaa 84,232,128\n"
"Ta 180,14,49152\nMvaa 88,240,128\nTa 182,14,51200\nMvaa 88,244,132\nMvaa 120,244,140\nTa 184,15,32768\nMvaa 120,240,136\nTa 186,15,34816\nMvaa 152,240,144\nMvaa 152,244,148\n"
"Ta 188,15,49152\nMvaa 184,244,156\nTa 190,15,51200\nMvaa 184,240,152\nD0 4,20,45,2048\nMvaa 188,248,152\nMvaa 188,252,156\nMvaa 156,252,148\nMvaa 156,248,144\nMvaa 124,248,136\n"
"Mvaa 124,252,140\nD0 5,20,45,3072\nMvaa 92,252,132\nMvaa 92,248,128\ns_waitcnt vmcnt(21) lgkmcnt(0)\ns_barrier\nMvaa 64,160,128\nRa 192,10,49152\nMvaa 64,164,132\n"
"s_add_u32 m0, s47, 32768\ns_add_u32 s44, s44, 0x40000\nbuffer_load_dwordx4 v[208:211], v21, s[40:43], s86 offen offset:0\nMvaa 96,164,140\nRa 196,11,49152\nMvaa 96,160,136\n"
"Mvaa 128,160,144\nRa 200,10,51200\nMvaa 128,164,148\nD0 6,24,44,0\nMvaa 160,164,156\nRa 204,11,51200\nMvaa 160,160,152\nMvaa 164,168,152\nRa 208,10,57344\nMvaa 164,172,156\n"
"Mvaa 132,172,148\nRa 212,11,57344\nMvaa 132,168,144\nMvaa 100,168,136\nRa 216,10,59392\nMvaa 100,172,140\nD0 7,24,44,1024\nMvaa 68,172,132\nRa 220,11,59392\nMvaa 68,168,128\n"
"Mvaa 72,176,128\nMvaa 72,180,132\nMvaa 104,180,140\nMvaa 104,176,136\nMvaa 136,176,144\nMvaa 136,180,148\nD0 8,24,44,2048\nMvaa 168,180,156\nMvaa 168,176,152\nMvaa 172,184,152\n"
"Mvaa 172,188,156\nMvaa 140,188,148\nMvaa 140,184,144\nMvaa 108,184,136\nMvaa 108,188,140\nD0 9,24,44,3072\nMvaa 76,188,132\nMvaa 76,184,128\nWL 0\nMaaa 0,160,192\nRa 128,10,0\n"
"Maaa 0,164,196\ns_add_u32 m0, s47, 49152\ns_add_u32 s44, s44, 0x40000\nMaaa 32,164,204\nRa 132,11,0\nMaaa 32,160,200\nMaaa 64,160,208\nRa 136,10,2048\nMaaa 64,164,212\n"
"D0 6,24,44,0\nMaaa 96,164,220\nRa 140,11,2048\nMaaa 96,160,216\nMaaa 100,168,216\nRa 144,10,8192\nMaaa 100,172,220\nMaaa 68,172,212\nRa 148,11,8192\nMaaa 68,168,208\n"
"Maaa 36,168,200\nRa 152,10,10240\nMaaa 36,172,204\nD0 7,24,44,1024\nMaaa 4,172,196\nRa 156,11,10240\nMaaa 4,168,192\nMaaa 8,176,192\nMaaa 8,180,196\nMaaa 40,180,204\n"
"Maaa 40,176,200\nMaaa 72,176,208\nMaaa 72,180,212\nD0 8,24,44,2048\nMaaa 104,180,220\nMaaa 104,176,216\nMaaa 108,184,216\nMaaa 108,188,220\nMaaa 76,188,212\nMaaa 76,184,208\n"
"Maaa 44,184,200\nMaaa 44,188,204\nD0 9,24,44,3072\nMaaa 12,188,196\nMaaa 12,184,192\ns_waitcnt vmcnt(14) lgkmcnt(0)\ns_barrier\nMaaa 16,224,192\nTa 160,12,0\n"
"v_xor_b32 v48, s15, v33\nMaaa 16,228,196\nTa 162,12,2048\nbuffer_load_dwordx4 v[196:199], v21, s[36:39], s86 offen offset:3584\nMaaa 48,228,204\ns_add_u32 m0, s47, -16384\n"
"s_add_u32 s48, s48, 128\nMaaa 48,224,200\nTa 164,12,16384\nMaaa 80,224,208\nTa 166,12,18432\nMaaa 80,228,212\nD0 2,20,48,0\nMaaa 112,228,220\nTa 168,13,0\nMaaa 112,224,216\n"
"Ta 170,13,2048\nMaaa 116,232,216\nv_xor_b32 v49, s15, v35\nMaaa 116,236,220\nTa 172,13,16384\nMaaa 84,236,212\nTa 174,13,18432\nMaaa 84,232,208\nv_xor_b32 v50, s15, v37\n"
"Maaa 52,232,200\nTa 176,14,0\nMaaa 52,236,204\nTa 178,14,2048\nMaaa 20,236,196\nD0 3,20,48,1024\nMaaa 20,232,192\nTa 180,14,16384\nMaaa 24,240,192\nTa 182,14,18432\n"
"v_xor_b32 v51, s15, v39\nMaaa 24,244,196\nv_xor_b32 v52, s15, v41\nMaaa 56,244,204\nTa 184,15,0\nMaaa 56,240,200\nTa 186,15,2048\nMaaa 88,240,208\nv_xor_b32 v53, s15, v43\n"
"Maaa 88,244,212\nTa 188,15,16384\nMaaa 120,244,220\nTa 190,15,18432\nMaaa 120,240,216\nD0 4,20,48,2048\nMaaa 124,248,216\nv_xor_b32 v54, s15, v45\nMaaa 124,252,220\n"
"v_xor_b32 v55, s15, v47\nMaaa 92,252,212\nv_or3_b32 v48, v48, v49, v50\nMaaa 92,248,208\nv_or3_b32 v51, v51, v52, v53\nMaaa 60,248,200\nv_or3_b32 v48, v48, v54, v55\n"
"Maaa 60,252,204\nD0 5,20,48,3072\nMaaa 28,252,196\nv_or_b32 v48, v48, v51\nMaaa 28,248,192\nWL 0\nMvaa 64,160,128\nTa 224,16,0\nv_cmp_ne_u32 vcc, 0, v48\nMvaa 64,164,132\n"
"Ta 226,16,2048\nMvaa 96,164,140\ns_add_u32 m0, s47, -65536\ns_add_u32 s45, s45, 128\nMvaa 96,160,136\nTa 228,16,16384\nMvaa 128,160,144\nTa 230,16,18432\nMvaa 128,164,148\n"
"D0 2,20,45,0\nMvaa 160,164,156\nTa 232,17,0\nMvaa 160,160,152\nTa 234,17,2048\nMvaa 164,168,152\ns_cbranch_vccnz .S14\n.S15:\nMvaa 164,172,156\nTa 236,17,16384\nMvaa 132,172,148\n"
"Ta 238,17,18432\nMvaa 132,168,144\nv_max_u32 v48, v32, v34\nMvaa 100,168,136\nTa 240,18,0\nMvaa 100,172,140\nTa 242,18,2048\nMvaa 68,172,132\nD0 3,20,45,1024\n.W541:\n"
"s_cbranch_execz .W545\nMvaa 68,168,128\nTa 244,18,16384\nMvaa 72,176,128\nTa 246,18,18432\nv_min_u32 v49, v32, v34\nMvaa 72,180,132\nv_med3_u32 v49, v48, v49, v36\n"
"Mvaa 104,180,140\nTa 248,19,0\nMvaa 104,176,136\nTa 250,19,2048\nMvaa 136,176,144\nv_max_u32 v48, v48, v36\nMvaa 136,180,148\nTa 252,19,16384\nMvaa 168,180,156\nTa 254,19,18432\n"
"Mvaa 168,176,152\nD0 4,20,45,2048\nMvaa 172,184,152\nv_med3_u32 v49, v48, v49, v38\nMvaa 172,188,156\nv_max_u32 v48, v48, v38\nMvaa 140,188,148\nv_med3_u32 v49, v48, v49, v40\n"
"Mvaa 140,184,144\nv_max_u32 v48, v48, v40\nMvaa 108,184,136\nv_med3_u32 v49, v48, v49, v42\nMvaa 108,188,140\nD0 5,20,45,3072\nMvaa 76,188,132\nv_max_u32 v48, v48, v42\n"
"Mvaa 76,184,128\ns_waitcnt vmcnt(18) lgkmcnt(0)\ns_barrier\nMvaa 80,224,128\nRa 192,10,16384\nv_med3_u32 v49, v48, v49, v44\nMvaa 80,228,132\ns_add_u32 m0, s47, 0\n"
"s_add_u32 s44, s44, 0x40000\nbuffer_load_dwordx4 v[212:215], v21, s[40:43], s86 offen offset:3584\nMvaa 112,228,140\nRa 196,11,16384\nMvaa 112,224,136\nv_max_u32 v48, v48, v44\n"
"Mvaa 144,224,144\nRa 200,10,18432\nMvaa 144,228,148\nD0 6,24,44,0\nMvaa 176,228,156\nRa 204,11,18432\nMvaa 176,224,152\nv_med3_u32 v49, v48, v49, v46\nMvaa 180,232,152\n"
"Ra 208,10,24576\nMvaa 180,236,156\nv_max_u32 v48, v48, v46\nMvaa 148,236,148\nRa 212,11,24576\nMvaa 148,232,144\nv_mov_b32 v50, v48\nMvaa 116,232,136\nRa 216,10,26624\n"
"Mvaa 116,236,140\nD0 7,24,44,1024\nMvaa 84,236,132\nRa 220,11,26624\nMvaa 84,232,128\nv_max_u32_dpp v50, v50, v50 row_ror:8 row_mask:0xf bank_mask:0xf\nMvaa 88,240,128\n"
"Mvaa 88,244,132\nv_max_u32_dpp v50, v50, v50 row_ror:4 row_mask:0xf bank_mask:0xf\nMvaa 120,244,140\nMvaa 120,240,136\n"
"v_max_u32_dpp v50, v50, v50 row_ror:2 row_mask:0xf bank_mask:0xf\nMvaa 152,240,144\nMvaa 152,244,148\nD0 8,24,44,2048\nMvaa 184,244,156\n"
"v_max_u32_dpp v50, v50, v50 row_ror:1 row_mask:0xf bank_mask:0xf\nMvaa 184,240,152\nv_mov_b32 v52, v50\nMvaa 188,248,152\nMvaa 188,252,156\nv_permlane16_swap_b32 v50, v52\n"
"Mvaa 156,252,148\nv_max_u32 v50, v50, v52\nMvaa 156,248,144\nv_mov_b32 v52, v50\nMvaa 124,248,136\nMvaa 124,252,140\nD0 9,24,44,3072\nMvaa 92,252,132\n"
"v_permlane32_swap_b32 v50, v52\nMvaa 92,248,128\nWL 0\nMaaa 16,224,192\nRa 128,10,32768\nv_max_u32 v50, v50, v52\nMaaa 16,228,196\ns_add_u32 m0, s47, 16384\n"
"s_add_u32 s44, s44, 0x40000\nMaaa 48,228,204\nRa 132,11,32768\nv_cmp_eq_u32 vcc, v48, v50\ns_lshl_b32 s2, s11, 3\ns_add_u32 s2, s2, 131072\nv_cndmask_b32 v51, v48, v49, vcc\n"
"Maaa 48,224,200\nMaaa 80,224,208\nRa 136,10,34816\nMaaa 80,228,212\nD0 6,24,44,0\nMaaa 112,228,220\nRa 140,11,34816\nMaaa 112,224,216\n"
"v_max_u32_dpp v51, v51, v51 row_ror:8 row_mask:0xf bank_mask:0xf\nMaaa 116,232,216\nRa 144,10,40960\nMaaa 116,236,220\n"
"v_max_u32_dpp v51, v51, v51 row_ror:4 row_mask:0xf bank_mask:0xf\nMaaa 84,236,212\nRa 148,11,40960\nMaaa 84,232,208\n"
"v_max_u32_dpp v51, v51, v51 row_ror:2 row_mask:0xf bank_mask:0xf\nMaaa 52,232,200\nRa 152,10,43008\nMaaa 52,236,204\nD0 7,24,44,1024\nMaaa 20,236,196\nRa 156,11,43008\n"
"Maaa 20,232,192\nv_max_u32_dpp v51, v51, v51 row_ror:1 row_mask:0xf bank_mask:0xf\nMaaa 24,240,192\nv_mov_b32 v52, v51\nMaaa 24,244,196\nMaaa 56,244,204\n"
"v_permlane16_swap_b32 v51, v52\nMaaa 56,240,200\nv_max_u32 v51, v51, v52\nMaaa 88,240,208\nv_mov_b32 v52, v51\nMaaa 88,244,212\nD0 8,24,44,2048\nMaaa 120,244,220\n"
"v_permlane32_swap_b32 v51, v52\nMaaa 120,240,216\nv_max_u32 v51, v51, v52\nMaaa 124,248,216\nv_mov_b32 v52, s2\nMaaa 124,252,220\nds_write_b64 v52, v[50:51]\nMaaa 92,252,212\n"
"v_mov_b32 v52, 131072\nMaaa 92,248,208\nMaaa 60,248,200\nMaaa 60,252,204\nD0 9,24,44,3072\nMaaa 28,252,196\nMaaa 28,248,192\ns_waitcnt vmcnt(18) lgkmcnt(0)\ns_barrier\n"
"Maaa 0,160,192\nTa 224,16,32768\nMaaa 0,164,196\nTa 226,16,34816\nbuffer_load_dwordx4 v[200:203], v21, s[36:39], s87 offen offset:0\nMaaa 32,164,204\ns_add_u32 m0, s47, -49152\n"
"s_add_u32 s48, s48, 128\nMaaa 32,160,200\nTa 228,16,49152\nMaaa 64,160,208\nTa 230,16,51200\nMaaa 64,164,212\nD0 2,20,48,0\nMaaa 96,164,220\nTa 232,17,32768\nMaaa 96,160,216\n"
"Ta 234,17,34816\nMaaa 100,168,216\nRv 32,52,0\nMaaa 100,172,220\nTa 236,17,49152\nMaaa 68,172,212\nTa 238,17,51200\nMaaa 68,168,208\nRv 36,52,16\nMaaa 36,168,200\nTa 240,18,32768\n"
"Maaa 36,172,204\nTa 242,18,34816\nMaaa 4,172,196\nD0 3,20,48,1024\nMaaa 4,168,192\nTa 244,18,49152\nMaaa 8,176,192\nTa 246,18,51200\nMaaa 8,180,196\nMaaa 40,180,204\n"
"Ta 248,19,32768\nMaaa 40,176,200\nTa 250,19,34816\nMaaa 72,176,208\nMaaa 72,180,212\nTa 252,19,49152\nMaaa 104,180,220\nTa 254,19,51200\nMaaa 104,176,216\nD0 4,20,48,2048\n"
"Maaa 108,184,216\nWL 11\nMaaa 108,188,220\nv_max_u32 v48, v32, v33\nMaaa 76,188,212\nv_min_u32 v49, v32, v33\nMaaa 76,184,208\nv_med3_u32 v49, v48, v49, v34\nMaaa 44,184,200\n"
"v_max_u32 v48, v48, v34\nMaaa 44,188,204\nD0 5,20,48,3072\nMaaa 12,188,196\nv_med3_u32 v49, v48, v49, v35\nMaaa 12,184,192\nWL 0\nMvaa 80,224,128\nTa 160,12,32768\n"
"Mvaa 80,228,132\nTa 162,12,34816\nMvaa 112,228,140\ns_add_u32 m0, s47, -32768\ns_add_u32 s45, s45, 128\nMvaa 112,224,136\nTa 164,12,49152\nMvaa 144,224,144\nTa 166,12,51200\n"
"Mvaa 144,228,148\nD0 2,20,45,0\nMvaa 176,228,156\nTa 168,13,32768\nMvaa 176,224,152\nTa 170,13,34816\nMvaa 180,232,152\nv_max_u32 v48, v48, v35\nMvaa 180,236,156\nTa 172,13,49152\n"
"Mvaa 148,236,148\nTa 174,13,51200\nMvaa 148,232,144\nv_med3_u32 v49, v48, v49, v36\nMvaa 116,232,136\nTa 176,14,32768\nMvaa 116,236,140\nTa 178,14,34816\nMvaa 84,236,132\n"
"D0 3,20,45,1024\nMvaa 84,232,128\nTa 180,14,49152\nMvaa 88,240,128\nTa 182,14,51200\nMvaa 88,244,132\nv_max_u32 v48, v48, v36\nMvaa 120,244,140\nTa 184,15,32768\nMvaa 120,240,136\n"
"Ta 186,15,34816\nMvaa 152,240,144\nv_med3_u32 v49, v48, v49, v37\nMvaa 152,244,148\nTa 188,15,49152\nMvaa 184,244,156\nTa 190,15,51200\nMvaa 184,240,152\nD0 4,20,45,2048\n"
"Mvaa 188,248,152\nv_max_u32 v48, v48, v37\nMvaa 188,252,156\nv_med3_u32 v49, v48, v49, v38\nMvaa 156,252,148\nv_max_u32 v48, v48, v38\nMvaa 156,248,144\n"
"v_med3_u32 v49, v48, v49, v39\nMvaa 124,248,136\nv_max_u32 v48, v48, v39\nMvaa 124,252,140\nD0 5,20,45,3072\nMvaa 92,252,132\nv_readfirstlane_b32 s2, v48\nMvaa 92,248,128\n"
"s_waitcnt vmcnt(18) lgkmcnt(0)\ns_barrier\nMvaa 64,160,128\nRa 192,10,49152\nMvaa 64,164,132\ns_add_u32 m0, s47, 32768\ns_add_u32 s44, s44, 0x40000\n"
"buffer_load_dwordx4 v[216:219], v21, s[40:43], s87 offen offset:0\nMvaa 96,164,140\nRa 196,11,49152\nMvaa 96,160,136\nv_readfirstlane_b32 s3, v49\nMvaa 128,160,144\n"
"Ra 200,10,51200\nMvaa 128,164,148\nD0 6,24,44,0\nMvaa 160,164,156\nRa 204,11,51200\nMvaa 160,160,152\ns_andn2_b32 s2, 0x7ff, s2\nMvaa 164,168,152\nRa 208,10,57344\n"
"Mvaa 164,172,156\ns_andn2_b32 s3, 0x7ff, s3\nMvaa 132,172,148\nRa 212,11,57344\nMvaa 132,168,144\ns_lshl_b32 s71, s3, 16\nMvaa 100,168,136\nRa 216,10,59392\nMvaa 100,172,140\n"
"D0 7,24,44,1024\nMvaa 68,172,132\nRa 220,11,59392\nMvaa 68,168,128\ns_or_b32 s71, s71, s2\nMvaa 72,176,128\ns_mul_i32 s2, s2, 896\nMvaa 72,180,132\ns_mul_i32 s3, s3, 896\n"
"Mvaa 104,180,140\ns_mul_i32 s6, s11, 224\nMvaa 104,176,136\ns_add_u32 s2, s2, s6\nMvaa 136,176,144\ns_add_u32 s3, s3, s6\nMvaa 136,180,148\nD0 8,24,44,2048\nMvaa 168,180,156\n"
"buffer_load_dwordx2 v[240:241], v255, s[28:31], s2 offen\nMvaa 168,176,152\nMvaa 172,184,152\nMvaa 172,188,156\nMvaa 140,188,148\n"
"buffer_load_dwordx2 v[242:243], v255, s[28:31], s3 offen\ns_lshl2_add_u32 s2, s10, s11\ns_cmp_eq_u32 s2, 512\ns_cselect_b32 s2, 0, 0x40000000\ns_add_u32 s2, s2, 0xc048000\n"
"Mvaa 140,184,144\nMvaa 108,184,136\nMvaa 108,188,140\nD0 9,24,44,3072\nMvaa 76,188,132\nbuffer_store_dword v25, off, s[16:19], s2 offset:0 sc0 sc1\nMvaa 76,184,128\nWL 0\n"
"Maaa 0,160,192\nRa 128,10,0\nMaaa 0,164,196\ns_add_u32 m0, s47, 49152\ns_add_u32 s44, s44, 0x40000\nMaaa 32,164,204\nRa 132,11,0\nMaaa 32,160,200\nMaaa 64,160,208\nRa 136,10,2048\n"
"Maaa 64,164,212\nD0 6,24,44,0\nMaaa 96,164,220\nRa 140,11,2048\nMaaa 96,160,216\nMaaa 100,168,216\nRa 144,10,8192\nMaaa 100,172,220\nMaaa 68,172,212\nRa 148,11,8192\n"
"Maaa 68,168,208\nMaaa 36,168,200\nRa 152,10,10240\nMaaa 36,172,204\nD0 7,24,44,1024\nMaaa 4,172,196\nRa 156,11,10240\nMaaa 4,168,192\nMaaa 8,176,192\nMaaa 8,180,196\n"
"Maaa 40,180,204\nMaaa 40,176,200\nMaaa 72,176,208\nMaaa 72,180,212\nD0 8,24,44,2048\nMaaa 104,180,220\nMaaa 104,176,216\nMaaa 108,184,216\nMaaa 108,188,220\nMaaa 76,188,212\n"
"Maaa 76,184,208\nMaaa 44,184,200\nMaaa 44,188,204\nD0 9,24,44,3072\nMaaa 12,188,196\nMaaa 12,184,192\ns_waitcnt vmcnt(21) lgkmcnt(0)\ns_barrier\nMaaa 16,224,192\nTa 160,12,0\n"
"Maaa 16,228,196\nTa 162,12,2048\nbuffer_load_dwordx4 v[204:207], v21, s[36:39], s87 offen offset:3584\nMaaa 48,228,204\ns_add_u32 m0, s47, -16384\ns_add_u32 s48, s48, 128\n"
"s_lshl_b32 s5, s73, 9\ns_add_u32 s5, s5, 0xc049400\nv_lshrrev_b32 v27, 1, v1\nbuffer_load_dwordx2 v[22:23], v27, s[16:19], s5 offen sc0 sc1\nMaaa 48,224,200\nTa 164,12,16384\n"
"Maaa 80,224,208\nTa 166,12,18432\nMaaa 80,228,212\nD0 2,20,48,0\nMaaa 112,228,220\nTa 168,13,0\nMaaa 112,224,216\nTa 170,13,2048\nMaaa 116,232,216\nMaaa 116,236,220\n"
"Ta 172,13,16384\nMaaa 84,236,212\nTa 174,13,18432\nMaaa 84,232,208\nMaaa 52,232,200\nTa 176,14,0\nMaaa 52,236,204\nTa 178,14,2048\nMaaa 20,236,196\nD0 3,20,48,1024\n"
"Maaa 20,232,192\nTa 180,14,16384\nMaaa 24,240,192\nTa 182,14,18432\nMaaa 24,244,196\nMaaa 56,244,204\nTa 184,15,0\nMaaa 56,240,200\nTa 186,15,2048\nMaaa 88,240,208\n"
"Maaa 88,244,212\nTa 188,15,16384\nMaaa 120,244,220\nTa 190,15,18432\nMaaa 120,240,216\nD0 4,20,48,2048\nMaaa 124,248,216\nMaaa 124,252,220\nMaaa 92,252,212\nMaaa 92,248,208\n"
"Maaa 60,248,200\nMaaa 60,252,204\nD0 5,20,48,3072\nMaaa 28,252,196\nMaaa 28,248,192\nWL 0\nMvaa 64,160,128\nTa 224,16,0\nMvaa 64,164,132\nTa 226,16,2048\nMvaa 96,164,140\n"
"s_add_u32 m0, s47, -65536\ns_add_u32 s45, s45, 128\nMvaa 96,160,136\nTa 228,16,16384\nMvaa 128,160,144\nTa 230,16,18432\nMvaa 128,164,148\nD0 2,20,45,0\nMvaa 160,164,156\n"
"Ta 232,17,0\nMvaa 160,160,152\nTa 234,17,2048\nMvaa 164,168,152\nMvaa 164,172,156\nTa 236,17,16384\nMvaa 132,172,148\nTa 238,17,18432\nMvaa 132,168,144\nMvaa 100,168,136\n"
"Ta 240,18,0\nMvaa 100,172,140\nTa 242,18,2048\nMvaa 68,172,132\nD0 3,20,45,1024\nMvaa 68,168,128\nTa 244,18,16384\nMvaa 72,176,128\nTa 246,18,18432\nMvaa 72,180,132\n"
"Mvaa 104,180,140\nTa 248,19,0\nMvaa 104,176,136\nTa 250,19,2048\nMvaa 136,176,144\nMvaa 136,180,148\nTa 252,19,16384\nMvaa 168,180,156\nTa 254,19,18432\nMvaa 168,176,152\n"
"D0 4,20,45,2048\nMvaa 172,184,152\nMvaa 172,188,156\nMvaa 140,188,148\nMvaa 140,184,144\nMvaa 108,184,136\nMvaa 108,188,140\nD0 5,20,45,3072\nMvaa 76,188,132\nMvaa 76,184,128\n"
"s_waitcnt vmcnt(22) lgkmcnt(0)\ns_barrier\nMvaa 80,224,128\nRa 192,10,16384\nWV 16\nMvaa 80,228,132\ns_add_u32 m0, s47, 0\ns_add_u32 s44, s44, 0x40000\n"
"buffer_load_dwordx4 v[220:223], v21, s[40:43], s87 offen offset:3584\nMvaa 112,228,140\nRa 196,11,16384\nv_lshlrev_b32 v244, 16, v240\nMvaa 112,224,136\n"
"v_and_b32 v245, 0xffff0000, v240\nv_lshlrev_b32 v246, 16, v241\nMvaa 144,224,144\nRa 200,10,18432\nv_and_b32 v247, 0xffff0000, v241\nMvaa 144,228,148\nD0 6,24,44,0\n"
"v_lshlrev_b32 v248, 16, v242\nMvaa 176,228,156\n.W542:\ns_cbranch_execz .W546\nRa 204,11,18432\nv_and_b32 v249, 0xffff0000, v242\nMvaa 176,224,152\nv_lshlrev_b32 v250, 16, v243\n"
"v_and_b32 v251, 0xffff0000, v243\nMvaa 180,232,152\nRa 208,10,24576\nv_lshlrev_b32 v252, 16, v192\nMvaa 180,236,156\nv_and_b32 v253, 0xffff0000, v192\nMU 32,244,252\n"
"Mvaa 148,236,148\nRa 212,11,24576\nMU 33,244,253\nMvaa 148,232,144\nMU 40,248,252\nMU 41,248,253\nMvaa 116,232,136\nRa 216,10,26624\nv_lshlrev_b32 v252, 16, v193\n"
"Mvaa 116,236,140\nD0 7,24,44,1024\nv_and_b32 v253, 0xffff0000, v193\nMvaa 84,236,132\nRa 220,11,26624\nMU 34,244,252\nMvaa 84,232,128\nMU 35,244,253\nMU 42,248,252\n"
"Mvaa 88,240,128\nMU 43,248,253\nv_lshlrev_b32 v252, 16, v194\nMvaa 88,244,132\nv_and_b32 v253, 0xffff0000, v194\nMvaa 120,244,140\nMU 36,244,252\nMU 37,244,253\nMvaa 120,240,136\n"
"MU 44,248,252\nMU 45,248,253\nMvaa 152,240,144\nv_lshlrev_b32 v252, 16, v195\nv_and_b32 v253, 0xffff0000, v195\nMvaa 152,244,148\nD0 8,24,44,2048\nMU 38,244,252\nMvaa 184,244,156\n"
"MU 39,244,253\nMU 46,248,252\nMvaa 184,240,152\nMU 47,248,253\nv_lshlrev_b32 v252, 16, v208\nMvaa 188,248,152\nv_and_b32 v253, 0xffff0000, v208\nMU 48,244,252\nMvaa 188,252,156\n"
"MU 49,244,253\nMU 56,248,252\nMvaa 156,252,148\nMU 57,248,253\nv_lshlrev_b32 v252, 16, v209\nMvaa 156,248,144\nv_and_b32 v253, 0xffff0000, v209\nMU 50,244,252\nMvaa 124,248,136\n"
"MU 51,244,253\nMU 58,248,252\nMvaa 124,252,140\nD0 9,24,44,3072\nMU 59,248,253\nMvaa 92,252,132\nv_lshlrev_b32 v252, 16, v210\nv_and_b32 v253, 0xffff0000, v210\nMvaa 92,248,128\n"
"WL 0\nMaaa 16,224,192\nRa 128,10,32768\nMU 52,244,252\nMaaa 16,228,196\ns_add_u32 m0, s47, 16384\ns_add_u32 s44, s44, 0x40000\nMaaa 48,228,204\nRa 132,11,32768\nMU 53,244,253\n"
"Maaa 48,224,200\nMU 60,248,252\nMU 61,248,253\nMaaa 80,224,208\nRa 136,10,34816\nv_lshlrev_b32 v252, 16, v211\nMaaa 80,228,212\nD0 6,24,44,0\nv_and_b32 v253, 0xffff0000, v211\n"
"Maaa 112,228,220\nRa 140,11,34816\nMU 54,244,252\nMaaa 112,224,216\nMU 55,244,253\nMU 62,248,252\nMaaa 116,232,216\nRa 144,10,40960\nMU 63,248,253\nMaaa 116,236,220\n"
"v_lshlrev_b32 v252, 16, v196\nv_and_b32 v253, 0xffff0000, v196\nMaaa 84,236,212\nRa 148,11,40960\nFM 32,245,252\nMaaa 84,232,208\nFM 33,245,253\nFM 40,249,252\nMaaa 52,232,200\n"
"Ra 152,10,43008\nFM 41,249,253\nMaaa 52,236,204\nD0 7,24,44,1024\nv_lshlrev_b32 v252, 16, v197\nMaaa 20,236,196\nRa 156,11,43008\nv_and_b32 v253, 0xffff0000, v197\n"
"Maaa 20,232,192\nFM 34,245,252\nFM 35,245,253\nMaaa 24,240,192\nFM 42,249,252\nFM 43,249,253\nMaaa 24,244,196\nv_lshlrev_b32 v252, 16, v198\nMaaa 56,244,204\n"
"v_and_b32 v253, 0xffff0000, v198\nFM 36,245,252\nMaaa 56,240,200\nFM 37,245,253\nFM 44,249,252\nMaaa 88,240,208\nFM 45,249,253\nv_lshlrev_b32 v252, 16, v199\nMaaa 88,244,212\n"
"D0 8,24,44,2048\nv_and_b32 v253, 0xffff0000, v199\nMaaa 120,244,220\nFM 38,245,252\nFM 39,245,253\nMaaa 120,240,216\nFM 46,249,252\nFM 47,249,253\nMaaa 124,248,216\n"
"v_lshlrev_b32 v252, 16, v212\nv_and_b32 v253, 0xffff0000, v212\nMaaa 124,252,220\nFM 48,245,252\nFM 49,245,253\nMaaa 92,252,212\nFM 56,249,252\nFM 57,249,253\nMaaa 92,248,208\n"
"v_lshlrev_b32 v252, 16, v213\nv_and_b32 v253, 0xffff0000, v213\nMaaa 60,248,200\nFM 50,245,252\nFM 51,245,253\nMaaa 60,252,204\nD0 9,24,44,3072\nFM 58,249,252\nMaaa 28,252,196\n"
"FM 59,249,253\nv_lshlrev_b32 v252, 16, v214\nMaaa 28,248,192\ns_waitcnt vmcnt(19) lgkmcnt(0)\ns_barrier\nMaaa 0,160,192\nTa 224,16,32768\nv_and_b32 v253, 0xffff0000, v214\n"
"Maaa 0,164,196\nTa 226,16,34816\nbuffer_load_dwordx4 v[224:227], v20, s[92:95], s88 offen offset:0\nMaaa 32,164,204\ns_add_u32 m0, s47, -49152\ns_add_u32 s48, s48, 128\n"
"Maaa 32,160,200\nTa 228,16,49152\nFM 52,245,252\nMaaa 64,160,208\nTa 230,16,51200\nFM 53,245,253\nMaaa 64,164,212\nD0 2,20,48,0\nFM 60,249,252\nMaaa 96,164,220\nTa 232,17,32768\n"
"FM 61,249,253\nMaaa 96,160,216\nTa 234,17,34816\nv_lshlrev_b32 v252, 16, v215\nMaaa 100,168,216\nv_and_b32 v253, 0xffff0000, v215\nFM 54,245,252\nMaaa 100,172,220\n"
"Ta 236,17,49152\nFM 55,245,253\nMaaa 68,172,212\nTa 238,17,51200\nFM 62,249,252\nMaaa 68,168,208\nFM 63,249,253\nv_lshlrev_b32 v252, 16, v200\nMaaa 36,168,200\nTa 240,18,32768\n"
"v_and_b32 v253, 0xffff0000, v200\nMaaa 36,172,204\nTa 242,18,34816\nFM 32,246,252\nMaaa 4,172,196\nD0 3,20,48,1024\nFM 33,246,253\nMaaa 4,168,192\nTa 244,18,49152\nFM 40,250,252\n"
"Maaa 8,176,192\nTa 246,18,51200\nFM 41,250,253\nMaaa 8,180,196\nv_lshlrev_b32 v252, 16, v201\nMaaa 40,180,204\nTa 248,19,32768\nv_and_b32 v253, 0xffff0000, v201\nMaaa 40,176,200\n"
"Ta 250,19,34816\nFM 34,246,252\nMaaa 72,176,208\nFM 35,246,253\nFM 42,250,252\nMaaa 72,180,212\nTa 252,19,49152\nFM 43,250,253\nMaaa 104,180,220\nTa 254,19,51200\n"
"v_lshlrev_b32 v252, 16, v202\nMaaa 104,176,216\nD0 4,20,48,2048\nv_and_b32 v253, 0xffff0000, v202\nMaaa 108,184,216\nFM 36,246,252\nFM 37,246,253\nMaaa 108,188,220\nFM 44,250,252\n"
"FM 45,250,253\nMaaa 76,188,212\nv_lshlrev_b32 v252, 16, v203\nv_and_b32 v253, 0xffff0000, v203\nMaaa 76,184,208\nFM 38,246,252\nFM 39,246,253\nMaaa 44,184,200\nFM 46,250,252\n"
"FM 47,250,253\nMaaa 44,188,204\nD0 5,20,48,3072\nv_lshlrev_b32 v252, 16, v216\nMaaa 12,188,196\nv_and_b32 v253, 0xffff0000, v216\nFM 48,246,252\nMaaa 12,184,192\nWL 0\n"
"Mvaa 80,224,128\nTa 160,12,32768\nFM 49,246,253\nMvaa 80,228,132\nTa 162,12,34816\nbuffer_load_dwordx4 v[228:231], v20, s[92:95], s88 offen offset:896\nMvaa 112,228,140\n"
"s_add_u32 m0, s47, -32768\ns_add_u32 s45, s45, 128\nMvaa 112,224,136\nTa 164,12,49152\nFM 56,250,252\nMvaa 144,224,144\nTa 166,12,51200\nFM 57,250,253\nMvaa 144,228,148\n"
"D0 2,20,45,0\nv_lshlrev_b32 v252, 16, v217\nMvaa 176,228,156\nTa 168,13,32768\nv_and_b32 v253, 0xffff0000, v217\nMvaa 176,224,152\nTa 170,13,34816\nFM 50,246,252\n"
"Mvaa 180,232,152\nFM 51,246,253\nFM 58,250,252\nMvaa 180,236,156\nTa 172,13,49152\nFM 59,250,253\nMvaa 148,236,148\nTa 174,13,51200\nv_lshlrev_b32 v252, 16, v218\n"
"Mvaa 148,232,144\nv_and_b32 v253, 0xffff0000, v218\nFM 52,246,252\nMvaa 116,232,136\nTa 176,14,32768\nFM 53,246,253\nMvaa 116,236,140\nTa 178,14,34816\nFM 60,250,252\n"
"Mvaa 84,236,132\nD0 3,20,45,1024\nFM 61,250,253\nMvaa 84,232,128\nTa 180,14,49152\nv_lshlrev_b32 v252, 16, v219\nMvaa 88,240,128\nTa 182,14,51200\n"
"v_and_b32 v253, 0xffff0000, v219\nMvaa 88,244,132\nFM 54,246,252\nWV 25\nv_cmp_ne_u32 vcc, s15, v23\ns_nop 0\ns_cbranch_vccnz .S17\n.S18:\nMvaa 120,244,140\nTa 184,15,32768\n"
"v_add_u32_dpp v22, v22, v22 row_ror:8 row_mask:0xf bank_mask:0xf\nMvaa 120,240,136\nTa 186,15,34816\nMvaa 152,240,144\n"
"v_add_u32_dpp v22, v22, v22 row_ror:4 row_mask:0xf bank_mask:0xf\nMvaa 152,244,148\nTa 188,15,49152\nMvaa 184,244,156\nTa 190,15,51200\n"
"v_add_u32_dpp v22, v22, v22 row_ror:2 row_mask:0xf bank_mask:0xf\nMvaa 184,240,152\nD0 4,20,45,2048\nMvaa 188,248,152\n"
"v_add_u32_dpp v22, v22, v22 row_ror:1 row_mask:0xf bank_mask:0xf\nMvaa 188,252,156\nMvaa 156,252,148\nv_mov_b32 v23, v22\nMvaa 156,248,144\nMvaa 124,248,136\n"
"v_permlane16_swap_b32 v22, v23\nv_add_u32 v22, v22, v23\nMvaa 124,252,140\nD0 5,20,45,3072\nv_mov_b32 v23, v22\nMvaa 92,252,132\nMvaa 92,248,128\ns_waitcnt vmcnt(19) lgkmcnt(0)\n"
"s_barrier\nMvaa 64,160,128\nRa 192,10,49152\nv_permlane32_swap_b32 v22, v23\nMvaa 64,164,132\ns_add_u32 m0, s47, 32768\ns_add_u32 s44, s44, 0x40000\n"
"buffer_load_dwordx4 v[232:235], v20, s[92:95], s88 offen offset:1792\nMvaa 96,164,140\nRa 196,11,49152\nv_add_u32 v22, v22, v23\nMvaa 96,160,136\nMvaa 128,160,144\n"
"Ra 200,10,51200\nv_readfirstlane_b32 s4, v22\nMvaa 128,164,148\nD0 6,24,44,0\nMvaa 160,164,156\nRa 204,11,51200\nMvaa 160,160,152\ns_cmp_gt_i32 s4, 0\ns_cselect_b32 s4, 1, 0\n"
"Mvaa 164,168,152\nRa 208,10,57344\ns_xor_b32 s4, s4, s12\nMvaa 164,172,156\ns_and_b32 s4, s4, 1\ns_lshl_b32 s4, s4, 13\nMvaa 132,172,148\nRa 212,11,57344\n"
"s_mul_i32 s72, s73, 25165824\nMvaa 132,168,144\ns_add_u32 s72, s72, s4\ns_and_b32 s4, s12, 1\nMvaa 100,168,136\nRa 216,10,59392\ns_lshl_b32 s4, s4, 21\nMvaa 100,172,140\n"
"D0 7,24,44,1024\ns_add_u32 s72, s72, s4\nMvaa 68,172,132\nRa 220,11,59392\ns_lshl3_add_u32 s2, s13, s11\nMvaa 68,168,128\ns_lshl_b32 s2, s2, 11\ns_lshl_b32 s3, s14, 22\n"
"Mvaa 72,176,128\ns_add_u32 s2, s2, s3\ns_add_u32 s2, s2, s72\nMvaa 72,180,132\ns_add_u32 s49, s2, 0xbe0000\nMvaa 104,180,140\nFM 55,246,253\nFM 62,250,252\nMvaa 104,176,136\n"
"FM 63,250,253\nv_lshlrev_b32 v252, 16, v204\nMvaa 136,176,144\nv_and_b32 v253, 0xffff0000, v204\nFM 32,247,252\nMvaa 136,180,148\nD0 8,24,44,2048\nFM 33,247,253\nMvaa 168,180,156\n"
"FM 40,251,252\nFM 41,251,253\nMvaa 168,176,152\nv_lshlrev_b32 v252, 16, v205\nv_and_b32 v253, 0xffff0000, v205\nMvaa 172,184,152\nFM 34,247,252\nFM 35,247,253\nMvaa 172,188,156\n"
"FM 42,251,252\nFM 43,251,253\nMvaa 140,188,148\nv_lshlrev_b32 v252, 16, v206\nv_and_b32 v253, 0xffff0000, v206\nMvaa 140,184,144\nFM 36,247,252\nFM 37,247,253\nMvaa 108,184,136\n"
"FM 44,251,252\nFM 45,251,253\nMvaa 108,188,140\nD0 9,24,44,3072\nv_lshlrev_b32 v252, 16, v207\nMvaa 76,188,132\nv_and_b32 v253, 0xffff0000, v207\nFM 38,247,252\nMvaa 76,184,128\n"
"s_waitcnt vmcnt(23) lgkmcnt(0)\nMaaa 0,160,192\nRa 128,10,0\nFM 39,247,253\nMaaa 0,164,196\ns_add_u32 m0, s47, 49152\ns_add_u32 s44, s44, 0x40000\n"
"buffer_load_dwordx4 v[236:239], v20, s[92:95], s88 offen offset:2688\nMaaa 32,164,204\nRa 132,11,0\nFM 46,251,252\nMaaa 32,160,200\nFM 47,251,253\nv_lshlrev_b32 v252, 16, v220\n"
"Maaa 64,160,208\nRa 136,10,2048\nv_and_b32 v253, 0xffff0000, v220\nMaaa 64,164,212\nD0 6,24,44,0\nFM 48,247,252\nMaaa 96,164,220\nRa 140,11,2048\nFM 49,247,253\nMaaa 96,160,216\n"
"FM 56,251,252\nFM 57,251,253\nMaaa 100,168,216\nRa 144,10,8192\nv_lshlrev_b32 v252, 16, v221\nMaaa 100,172,220\nv_and_b32 v253, 0xffff0000, v221\nFM 50,247,252\nMaaa 68,172,212\n"
"Ra 148,11,8192\nFM 51,247,253\nMaaa 68,168,208\nFM 58,251,252\nFM 59,251,253\nMaaa 36,168,200\nRa 152,10,10240\nv_lshlrev_b32 v252, 16, v222\nMaaa 36,172,204\nD0 7,24,44,1024\n"
"v_and_b32 v253, 0xffff0000, v222\nMaaa 4,172,196\nRa 156,11,10240\nFM 52,247,252\nMaaa 4,168,192\nFM 53,247,253\nFM 60,251,252\nMaaa 8,176,192\nFM 61,251,253\n"
"v_lshlrev_b32 v252, 16, v223\nMaaa 8,180,196\nv_and_b32 v253, 0xffff0000, v223\nMaaa 40,180,204\nFM 54,247,252\nFM 55,247,253\nMaaa 40,176,200\nFM 62,251,252\nFM 63,251,253\n"
"Maaa 72,176,208\nv_permlane32_swap_b32 v32, v48\nv_permlane32_swap_b32 v33, v49\nMaaa 72,180,212\nD0 8,24,44,2048\nv_permlane32_swap_b32 v34, v50\nMaaa 104,180,220\n"
"v_permlane32_swap_b32 v35, v51\nv_permlane32_swap_b32 v36, v52\nMaaa 104,176,216\nv_permlane32_swap_b32 v37, v53\nv_permlane32_swap_b32 v38, v54\nMaaa 108,184,216\n"
"v_permlane32_swap_b32 v39, v55\nv_permlane32_swap_b32 v40, v56\nMaaa 108,188,220\nv_permlane32_swap_b32 v41, v57\nv_permlane32_swap_b32 v42, v58\nMaaa 76,188,212\n"
"v_permlane32_swap_b32 v43, v59\nv_permlane32_swap_b32 v44, v60\nMaaa 76,184,208\nv_permlane32_swap_b32 v45, v61\nv_permlane32_swap_b32 v46, v62\nMaaa 44,184,200\n"
"v_permlane32_swap_b32 v47, v63\nAD 32,32,48\nMaaa 44,188,204\nD0 9,24,44,3072\nAD 33,33,49\nMaaa 12,188,196\nAD 34,34,50\nAD 35,35,51\nMaaa 12,184,192\n"
"s_waitcnt vmcnt(20) lgkmcnt(0)\ns_barrier\nMaaa 16,224,192\nTa 160,12,0\nAD 36,36,52\nMaaa 16,228,196\nTa 162,12,2048\nMaaa 48,228,204\ns_add_u32 m0, s47, -16384\n"
"s_add_u32 s48, s48, 128\nMaaa 48,224,200\nTa 164,12,16384\nAD 37,37,53\nMaaa 80,224,208\nTa 166,12,18432\nAD 38,38,54\nMaaa 80,228,212\nD0 2,20,48,0\nAD 39,39,55\n"
"Maaa 112,228,220\nTa 168,13,0\nAD 40,40,56\nMaaa 112,224,216\nTa 170,13,2048\nAD 41,41,57\nMaaa 116,232,216\nAD 42,42,58\nAD 43,43,59\nMaaa 116,236,220\nTa 172,13,16384\n"
"AD 44,44,60\nMaaa 84,236,212\nTa 174,13,18432\nAD 45,45,61\nMaaa 84,232,208\nAD 46,46,62\nAD 47,47,63\nMaaa 52,232,200\nTa 176,14,0\nv_permlane16_swap_b32 v32, v40\n"
"Maaa 52,236,204\nTa 178,14,2048\nv_permlane16_swap_b32 v33, v41\nMaaa 20,236,196\nD0 3,20,48,1024\nv_permlane16_swap_b32 v34, v42\nMaaa 20,232,192\nTa 180,14,16384\n"
"v_permlane16_swap_b32 v35, v43\nMaaa 24,240,192\nTa 182,14,18432\nv_permlane16_swap_b32 v36, v44\nMaaa 24,244,196\nv_permlane16_swap_b32 v37, v45\nMaaa 56,244,204\nTa 184,15,0\n"
"v_permlane16_swap_b32 v38, v46\nMaaa 56,240,200\nTa 186,15,2048\nv_permlane16_swap_b32 v39, v47\nMaaa 88,240,208\nAD 32,32,40\nAD 33,33,41\nMaaa 88,244,212\nTa 188,15,16384\n"
"AD 34,34,42\nMaaa 120,244,220\nTa 190,15,18432\nAD 35,35,43\nMaaa 120,240,216\nD0 4,20,48,2048\nAD 36,36,44\nMaaa 124,248,216\nAD 37,37,45\nAD 38,38,46\nMaaa 124,252,220\n"
"AD 39,39,47\nv_add_f32_dpp v32, v32, v32 row_ror:8 row_mask:0xf bank_mask:0xf\nMaaa 92,252,212\nv_add_f32_dpp v33, v33, v33 row_ror:8 row_mask:0xf bank_mask:0xf\n"
"v_add_f32_dpp v34, v34, v34 row_ror:8 row_mask:0xf bank_mask:0xf\nMaaa 92,248,208\nv_add_f32_dpp v35, v35, v35 row_ror:8 row_mask:0xf bank_mask:0xf\n"
"v_add_f32_dpp v36, v36, v36 row_ror:8 row_mask:0xf bank_mask:0xf\nMaaa 60,248,200\nv_add_f32_dpp v37, v37, v37 row_ror:8 row_mask:0xf bank_mask:0xf\n"
"v_add_f32_dpp v38, v38, v38 row_ror:8 row_mask:0xf bank_mask:0xf\nMaaa 60,252,204\nD0 5,20,48,3072\nv_add_f32_dpp v39, v39, v39 row_ror:8 row_mask:0xf bank_mask:0xf\n"
"Maaa 28,252,196\nv_add_f32_dpp v32, v32, v32 row_ror:4 row_mask:0xf bank_mask:0xf\nv_add_f32_dpp v33, v33, v33 row_ror:4 row_mask:0xf bank_mask:0xf\nMaaa 28,248,192\nWL 0\n"
"Mvaa 64,160,128\nTa 224,16,0\nv_add_f32_dpp v34, v34, v34 row_ror:4 row_mask:0xf bank_mask:0xf\n.W543:\ns_cbranch_execz .W547\nMvaa 64,164,132\nTa 226,16,2048\nMvaa 96,164,140\n"
"s_add_u32 m0, s47, -65536\ns_add_u32 s45, s45, 128\nMvaa 96,160,136\nTa 228,16,16384\nv_add_f32_dpp v35, v35, v35 row_ror:4 row_mask:0xf bank_mask:0xf\nMvaa 128,160,144\n"
"Ta 230,16,18432\nv_add_f32_dpp v36, v36, v36 row_ror:4 row_mask:0xf bank_mask:0xf\nMvaa 128,164,148\nD0 2,20,45,0\n"
"v_add_f32_dpp v37, v37, v37 row_ror:4 row_mask:0xf bank_mask:0xf\nMvaa 160,164,156\nTa 232,17,0\nv_add_f32_dpp v38, v38, v38 row_ror:4 row_mask:0xf bank_mask:0xf\n"
"Mvaa 160,160,152\nTa 234,17,2048\nv_add_f32_dpp v39, v39, v39 row_ror:4 row_mask:0xf bank_mask:0xf\nMvaa 164,168,152\n"
"v_add_f32_dpp v32, v32, v32 row_ror:2 row_mask:0xf bank_mask:0xf\nv_add_f32_dpp v33, v33, v33 row_ror:2 row_mask:0xf bank_mask:0xf\nMvaa 164,172,156\nTa 236,17,16384\n"
"v_add_f32_dpp v34, v34, v34 row_ror:2 row_mask:0xf bank_mask:0xf\nMvaa 132,172,148\nTa 238,17,18432\nv_add_f32_dpp v35, v35, v35 row_ror:2 row_mask:0xf bank_mask:0xf\n"
"Mvaa 132,168,144\nv_add_f32_dpp v36, v36, v36 row_ror:2 row_mask:0xf bank_mask:0xf\nv_add_f32_dpp v37, v37, v37 row_ror:2 row_mask:0xf bank_mask:0xf\nMvaa 100,168,136\n"
"Ta 240,18,0\nv_add_f32_dpp v38, v38, v38 row_ror:2 row_mask:0xf bank_mask:0xf\nMvaa 100,172,140\nTa 242,18,2048\nv_add_f32_dpp v39, v39, v39 row_ror:2 row_mask:0xf bank_mask:0xf\n"
"Mvaa 68,172,132\nD0 3,20,45,1024\nv_lshrrev_b32 v48, 4, v0\nMvaa 68,168,128\nTa 244,18,16384\nv_and_b32 v49, 1, v0\nMvaa 72,176,128\nTa 246,18,18432\nv_lshlrev_b32 v49, 5, v49\n"
"Mvaa 72,180,132\nv_lshl_add_u32 v49, v48, 6, v49\nMvaa 104,180,140\nTa 248,19,0\ns_lshl_b32 s2, s11, 8\nMvaa 104,176,136\nTa 250,19,2048\ns_add_u32 s2, s2, 132096\n"
"Mvaa 136,176,144\nv_add_u32 v49, s2, v49\nds_write_b128 v49, v[32:35]\nMvaa 136,180,148\nTa 252,19,16384\nds_write_b128 v49, v[36:39] offset:16\nMvaa 168,180,156\nTa 254,19,18432\n"
"v_and_b32 v50, 31, v0\nMvaa 168,176,152\nD0 4,20,45,2048\nv_lshlrev_b32 v50, 2, v50\nMvaa 172,184,152\ns_lshl_b32 s2, s11, 10\ns_add_u32 s2, s2, 133120\nMvaa 172,188,156\n"
"v_add_u32 v51, s2, v50\nv_add_u32 v50, 132096, v50\nMvaa 140,188,148\nv_lshl_add_u32 v52, v48, 4, s2\nMvaa 140,184,144\nMvaa 108,184,136\nMvaa 108,188,140\nD0 5,20,45,3072\n"
"Mvaa 76,188,132\nMvaa 76,184,128\ns_waitcnt vmcnt(18) lgkmcnt(0)\ns_barrier\nMvaa 80,224,128\nRa 192,10,16384\nds_read_b32 v32, v50 offset:0\nMvaa 80,228,132\n"
"s_add_u32 m0, s47, 0\ns_add_u32 s44, s44, 0x40000\nMvaa 112,228,140\nRa 196,11,16384\nMvaa 112,224,136\nds_read_b32 v33, v50 offset:256\nds_read_b32 v34, v50 offset:512\n"
"Mvaa 144,224,144\nRa 200,10,18432\nMvaa 144,228,148\nD0 6,24,44,0\nMvaa 176,228,156\nRa 204,11,18432\nds_read_b32 v35, v50 offset:768\nMvaa 176,224,152\n"
"ds_read_b32 v36, v50 offset:128\nMvaa 180,232,152\nRa 208,10,24576\nds_read_b32 v37, v50 offset:384\nMvaa 180,236,156\nds_read_b32 v38, v50 offset:640\nMvaa 148,236,148\n"
"Ra 212,11,24576\nMvaa 148,232,144\nds_read_b32 v39, v50 offset:896\nMvaa 116,232,136\nRa 216,10,26624\nMvaa 116,236,140\nD0 7,24,44,1024\nMvaa 84,236,132\nRa 220,11,26624\n"
"Mvaa 84,232,128\nMvaa 88,240,128\nMvaa 88,244,132\nMvaa 120,244,140\nMvaa 120,240,136\nMvaa 152,240,144\nMvaa 152,244,148\nD0 8,24,44,2048\nMvaa 184,244,156\nWL 2\nAD 32,32,33\n"
"Mvaa 184,240,152\nAD 34,34,35\nMvaa 188,248,152\nAD 36,36,37\nAD 38,38,39\nMvaa 188,252,156\nAD 32,32,34\nMvaa 156,252,148\nAD 36,36,38\nMvaa 156,248,144\nCV 32,32,36\n"
"v_and_b32 v36, 0xffff0000, v32\nMvaa 124,248,136\nv_lshlrev_b32 v32, 16, v32\nMvaa 124,252,140\nD0 9,24,44,3072\nMvaa 92,252,132\nv_mul_f32 v33, 0xbfb8aa3b, v32\nMvaa 92,248,128\n"
"WL 0\nMaaa 16,224,192\nRa 128,10,32768\nMaaa 16,228,196\ns_add_u32 m0, s47, 16384\ns_add_u32 s44, s44, 0x40000\nMaaa 48,228,204\nRa 132,11,32768\nEX 33,33\nMaaa 48,224,200\n"
"v_add_f32 v33, 1.0, v33\nMaaa 80,224,208\nRa 136,10,34816\nv_rcp_f32 v33, v33\nMaaa 80,228,212\nD0 6,24,44,0\nMaaa 112,228,220\nRa 140,11,34816\nMU 33,32,33\nMaaa 112,224,216\n"
"CV 33,33,33\nMaaa 116,232,216\nRa 144,10,40960\nv_lshlrev_b32 v33, 16, v33\nMaaa 116,236,220\nMU 33,33,36\nMaaa 84,236,212\nRa 148,11,40960\nMaaa 84,232,208\nCV 33,33,33\n"
"v_lshlrev_b32 v33, 16, v33\nMaaa 52,232,200\nRa 152,10,43008\nMaaa 52,236,204\nD0 7,24,44,1024\nMaaa 20,236,196\nRa 156,11,43008\nds_write_b32 v51, v33\nMaaa 20,232,192\n"
"Rv 244,52,0\nMaaa 24,240,192\nRv 248,52,64\ns_and_b32 s2, s74, 15\nMaaa 24,244,196\ns_sub_u32 s2, s2, 2\ns_cmp_lt_u32 s2, 14\ns_cselect_b32 s5, 0, 0x40000000\nMaaa 56,244,204\n"
"s_and_b32 s2, s2, 15\nMaaa 56,240,200\ns_mul_i32 s4, s73, 14\ns_add_u32 s4, s4, s2\nMaaa 88,240,208\nv_and_b32 v49, 15, v0\nMaaa 88,244,212\nD0 8,24,44,2048\nMaaa 120,244,220\n"
"v_lshlrev_b32 v50, 3, v49\nv_and_b32 v51, 1, v48\nMaaa 120,240,216\nv_lshl_add_u32 v50, v51, 2, v50\nMaaa 124,248,216\ns_mul_i32 s2, s11, 112\nv_add_u32 v50, s2, v50\n"
"Maaa 124,252,220\nv_lshrrev_b32 v51, 4, v50\nMaaa 92,252,212\nv_lshrrev_b32 v48, 1, v48\nMaaa 92,248,208\nv_mad_u32_u24 v51, v48, 28, v51\nv_mul_u32_u24 v51, 7168, v51\n"
"Maaa 60,248,200\nv_and_b32 v50, 15, v50\nMaaa 60,252,204\nD0 9,24,44,3072\nMaaa 28,252,196\nv_lshl_add_u32 v51, v50, 2, v51\nv_cmp_gt_u32 vcc, 14, v49\ns_lshl_b32 s4, s4, 6\n"
"s_add_u32 s4, s4, 0xc04c800\nv_cndmask_b32 v51, v254, v51, vcc\nMaaa 28,248,192\ns_waitcnt vmcnt(16) lgkmcnt(3)\ns_barrier\nMaaa 0,160,192\nTa 224,16,32768\ns_or_b32 s4, s4, s5\n"
"Maaa 0,164,196\nTa 226,16,34816\nMaaa 32,164,204\ns_add_u32 m0, s47, -49152\ns_add_u32 s48, s48, 128\nMaaa 32,160,200\nTa 228,16,49152\nWL 3\nMaaa 64,160,208\nTa 230,16,51200\n"
"Maaa 64,164,212\nD0 2,20,48,0\nMaaa 96,164,220\nTa 232,17,32768\nv_lshlrev_b32 v252, 16, v224\nMaaa 96,160,216\nTa 234,17,34816\nMaaa 100,168,216\n"
"v_and_b32 v253, 0xffff0000, v224\nMU 32,244,252\nMaaa 100,172,220\nTa 236,17,49152\nMaaa 68,172,212\nTa 238,17,51200\nMaaa 68,168,208\nMU 33,244,253\nMU 40,248,252\n"
"Maaa 36,168,200\nTa 240,18,32768\nMaaa 36,172,204\nTa 242,18,34816\nMaaa 4,172,196\nD0 3,20,48,1024\nMU 41,248,253\nMaaa 4,168,192\nTa 244,18,49152\nMaaa 8,176,192\n"
"Ta 246,18,51200\nv_lshlrev_b32 v252, 16, v225\nMaaa 8,180,196\nv_and_b32 v253, 0xffff0000, v225\nMaaa 40,180,204\nTa 248,19,32768\nMaaa 40,176,200\nTa 250,19,34816\nMU 34,244,252\n"
"Maaa 72,176,208\nMU 35,244,253\nMaaa 72,180,212\nTa 252,19,49152\nMaaa 104,180,220\nTa 254,19,51200\nMU 42,248,252\nMaaa 104,176,216\nD0 4,20,48,2048\nMaaa 108,184,216\n"
"MU 43,248,253\nv_lshlrev_b32 v252, 16, v226\nMaaa 108,188,220\nv_and_b32 v253, 0xffff0000, v226\nMaaa 76,188,212\nMU 36,244,252\nMaaa 76,184,208\nMU 37,244,253\nMU 44,248,252\n"
"Maaa 44,184,200\nMU 45,248,253\nMaaa 44,188,204\nD0 5,20,48,3072\nMaaa 12,188,196\nv_lshlrev_b32 v252, 16, v227\nv_and_b32 v253, 0xffff0000, v227\nMaaa 12,184,192\nWL 0\n"
"Mvaa 80,224,128\nTa 160,12,32768\nMU 38,244,252\nMvaa 80,228,132\nTa 162,12,34816\nMvaa 112,228,140\ns_add_u32 m0, s47, -32768\ns_add_u32 s45, s45, 128\nMvaa 112,224,136\n"
"Ta 164,12,49152\nMU 39,244,253\nMvaa 144,224,144\nTa 166,12,51200\nMvaa 144,228,148\nD0 2,20,45,0\nMvaa 176,228,156\nTa 168,13,32768\nMU 46,248,252\nMvaa 176,224,152\n"
"Ta 170,13,34816\nMvaa 180,232,152\nMU 47,248,253\nv_lshlrev_b32 v252, 16, v228\nMvaa 180,236,156\nTa 172,13,49152\nMvaa 148,236,148\nTa 174,13,51200\nMvaa 148,232,144\n"
"v_and_b32 v253, 0xffff0000, v228\nFM 32,245,252\nMvaa 116,232,136\nTa 176,14,32768\nMvaa 116,236,140\nTa 178,14,34816\nMvaa 84,236,132\nD0 3,20,45,1024\nFM 33,245,253\n"
"Mvaa 84,232,128\nTa 180,14,49152\nMvaa 88,240,128\nTa 182,14,51200\nFM 40,249,252\nMvaa 88,244,132\nFM 41,249,253\nMvaa 120,244,140\nTa 184,15,32768\nMvaa 120,240,136\n"
"Ta 186,15,34816\nv_lshlrev_b32 v252, 16, v229\nMvaa 152,240,144\nv_and_b32 v253, 0xffff0000, v229\nMvaa 152,244,148\nTa 188,15,49152\nMvaa 184,244,156\nTa 190,15,51200\n"
"FM 34,245,252\nMvaa 184,240,152\nD0 4,20,45,2048\nMvaa 188,248,152\nFM 35,245,253\nFM 42,249,252\nMvaa 188,252,156\nFM 43,249,253\nMvaa 156,252,148\nv_lshlrev_b32 v252, 16, v230\n"
"Mvaa 156,248,144\nv_and_b32 v253, 0xffff0000, v230\nFM 36,245,252\nMvaa 124,248,136\nFM 37,245,253\nMvaa 124,252,140\nD0 5,20,45,3072\nMvaa 92,252,132\nFM 44,249,252\n"
"FM 45,249,253\nMvaa 92,248,128\ns_waitcnt vmcnt(16) lgkmcnt(0)\ns_barrier\nMvaa 64,160,128\nRa 192,10,49152\nv_lshlrev_b32 v252, 16, v231\nMvaa 64,164,132\n"
"s_add_u32 m0, s47, 32768\ns_add_u32 s44, s44, 0x40000\nMvaa 96,164,140\nRa 196,11,49152\nMvaa 96,160,136\nv_and_b32 v253, 0xffff0000, v231\nFM 38,245,252\nMvaa 128,160,144\n"
"Ra 200,10,51200\nMvaa 128,164,148\nD0 6,24,44,0\nMvaa 160,164,156\nRa 204,11,51200\nFM 39,245,253\nMvaa 160,160,152\nFM 46,249,252\nMvaa 164,168,152\nRa 208,10,57344\n"
"FM 47,249,253\nMvaa 164,172,156\nv_lshlrev_b32 v252, 16, v232\nMvaa 132,172,148\nRa 212,11,57344\nMvaa 132,168,144\nv_and_b32 v253, 0xffff0000, v232\nFM 32,246,252\n"
"Mvaa 100,168,136\nRa 216,10,59392\nMvaa 100,172,140\nD0 7,24,44,1024\nMvaa 68,172,132\nRa 220,11,59392\nFM 33,246,253\nMvaa 68,168,128\nFM 40,250,252\nMvaa 72,176,128\n"
"FM 41,250,253\nv_lshlrev_b32 v252, 16, v233\nMvaa 72,180,132\nv_and_b32 v253, 0xffff0000, v233\nMvaa 104,180,140\nFM 34,246,252\nMvaa 104,176,136\nFM 35,246,253\nFM 42,250,252\n"
"Mvaa 136,176,144\nFM 43,250,253\nMvaa 136,180,148\nD0 8,24,44,2048\nMvaa 168,180,156\nv_lshlrev_b32 v252, 16, v234\nv_and_b32 v253, 0xffff0000, v234\nMvaa 168,176,152\n"
"FM 36,246,252\nMvaa 172,184,152\nFM 37,246,253\nFM 44,250,252\nMvaa 172,188,156\nFM 45,250,253\nMvaa 140,188,148\nv_lshlrev_b32 v252, 16, v235\nMvaa 140,184,144\n"
"v_and_b32 v253, 0xffff0000, v235\nFM 38,246,252\nMvaa 108,184,136\nFM 39,246,253\nMvaa 108,188,140\nD0 9,24,44,3072\nMvaa 76,188,132\nFM 46,250,252\nFM 47,250,253\n"
"Mvaa 76,184,128\nWL 0\nMaaa 0,160,192\nRa 128,10,0\nv_lshlrev_b32 v252, 16, v236\nMaaa 0,164,196\ns_add_u32 m0, s47, 49152\ns_add_u32 s44, s44, 0x40000\nMaaa 32,164,204\n"
"Ra 132,11,0\nMaaa 32,160,200\nv_and_b32 v253, 0xffff0000, v236\nFM 32,247,252\nMaaa 64,160,208\nRa 136,10,2048\nMaaa 64,164,212\nD0 6,24,44,0\nMaaa 96,164,220\nRa 140,11,2048\n"
"FM 33,247,253\nMaaa 96,160,216\nFM 40,251,252\nMaaa 100,168,216\nRa 144,10,8192\nFM 41,251,253\nMaaa 100,172,220\nv_lshlrev_b32 v252, 16, v237\nMaaa 68,172,212\nRa 148,11,8192\n"
"Maaa 68,168,208\nv_and_b32 v253, 0xffff0000, v237\nFM 34,247,252\nMaaa 36,168,200\nRa 152,10,10240\nMaaa 36,172,204\nD0 7,24,44,1024\nMaaa 4,172,196\nRa 156,11,10240\n"
"FM 35,247,253\nMaaa 4,168,192\nFM 42,251,252\nMaaa 8,176,192\nFM 43,251,253\nv_lshlrev_b32 v252, 16, v238\nMaaa 8,180,196\nv_and_b32 v253, 0xffff0000, v238\nMaaa 40,180,204\n"
"FM 36,247,252\nMaaa 40,176,200\nFM 37,247,253\nFM 44,251,252\nMaaa 72,176,208\nFM 45,251,253\nMaaa 72,180,212\nD0 8,24,44,2048\nMaaa 104,180,220\nv_lshlrev_b32 v252, 16, v239\n"
"v_and_b32 v253, 0xffff0000, v239\nMaaa 104,176,216\nFM 38,247,252\nMaaa 108,184,216\nFM 39,247,253\nFM 46,251,252\nMaaa 108,188,220\nFM 47,251,253\nMaaa 76,188,212\n"
"v_permlane32_swap_b32 v32, v40\nMaaa 76,184,208\nv_permlane32_swap_b32 v33, v41\nv_permlane32_swap_b32 v34, v42\nMaaa 44,184,200\nv_permlane32_swap_b32 v35, v43\nMaaa 44,188,204\n"
"D0 9,24,44,3072\nMaaa 12,188,196\nv_permlane32_swap_b32 v36, v44\nv_permlane32_swap_b32 v37, v45\nMaaa 12,184,192\nWV 16\ns_barrier\nMaaa 16,224,192\nTa 160,12,0\n"
"v_permlane32_swap_b32 v38, v46\nMaaa 16,228,196\nTa 162,12,2048\nMaaa 48,228,204\ns_add_u32 m0, s47, -16384\ns_add_u32 s48, s48, 128\nMaaa 48,224,200\nTa 164,12,16384\n"
"v_permlane32_swap_b32 v39, v47\nMaaa 80,224,208\nTa 166,12,18432\nMaaa 80,228,212\nD0 2,20,48,0\nMaaa 112,228,220\nTa 168,13,0\nAD 32,32,40\nMaaa 112,224,216\nTa 170,13,2048\n"
"Maaa 116,232,216\nAD 33,33,41\nAD 34,34,42\nMaaa 116,236,220\nTa 172,13,16384\nMaaa 84,236,212\nTa 174,13,18432\nMaaa 84,232,208\nAD 35,35,43\nAD 36,36,44\nMaaa 52,232,200\n"
"Ta 176,14,0\nMaaa 52,236,204\nTa 178,14,2048\nMaaa 20,236,196\nD0 3,20,48,1024\nAD 37,37,45\nMaaa 20,232,192\nTa 180,14,16384\nMaaa 24,240,192\nTa 182,14,18432\nAD 38,38,46\n"
"Maaa 24,244,196\nAD 39,39,47\nMaaa 56,244,204\n.W544:\ns_cbranch_execz .W548\nTa 184,15,0\nMaaa 56,240,200\nTa 186,15,2048\nv_permlane16_swap_b32 v32, v36\nMaaa 88,240,208\n"
"v_permlane16_swap_b32 v33, v37\nMaaa 88,244,212\nTa 188,15,16384\nMaaa 120,244,220\nTa 190,15,18432\nv_permlane16_swap_b32 v34, v38\nMaaa 120,240,216\nD0 4,20,48,2048\n"
"Maaa 124,248,216\nv_permlane16_swap_b32 v35, v39\nAD 32,32,36\nMaaa 124,252,220\nAD 33,33,37\nMaaa 92,252,212\nAD 34,34,38\nMaaa 92,248,208\nAD 35,35,39\nSv 32,51,16,4,0\n"
"Maaa 60,248,200\nMaaa 60,252,204\nD0 5,20,48,3072\nMaaa 28,252,196\nMaaa 28,248,192\nWL 0\nMvaa 64,160,128\nTa 224,16,0\ns_lshr_b32 s3, s11, 1\nMvaa 64,164,132\nTa 226,16,2048\n"
"Mvaa 96,164,140\ns_lshl_b32 s3, s3, 13\nMvaa 96,160,136\nTa 228,16,16384\nMvaa 128,160,144\nTa 230,16,18432\nMvaa 128,164,148\ns_lshl_b32 s2, s11, 12\nMvaa 160,164,156\n"
"Ta 232,17,0\nMvaa 160,160,152\nTa 234,17,2048\nMvaa 164,168,152\ns_add_u32 s2, s2, s3\ns_add_u32 s2, s2, s72\nMvaa 164,172,156\nTa 236,17,16384\nMvaa 132,172,148\nTa 238,17,18432\n"
"Mvaa 132,168,144\ns_lshl_b32 s3, s11, 12\nMvaa 100,168,136\nTa 240,18,0\nMvaa 100,172,140\nTa 242,18,2048\nMvaa 68,172,132\ns_add_u32 s89, s3, 0x0\nMvaa 68,168,128\n"
"Ta 244,18,16384\nMvaa 72,176,128\nTa 246,18,18432\ns_mov_b32 s88, 0xffff8000\nMvaa 72,180,132\ns_lshl_b32 s4, s13, 1\nMvaa 104,180,140\nTa 248,19,0\nMvaa 104,176,136\n"
"Ta 250,19,2048\nMvaa 136,176,144\ns_sub_u32 s79, 15, s4\nMvaa 136,180,148\nTa 252,19,16384\nMvaa 168,180,156\nTa 254,19,18432\nMvaa 168,176,152\ns_add_u32 s96, s2, 0x1310000\n"
"Mvaa 172,184,152\ns_mov_b32 s63, 0x53800000\ns_mov_b32 s64, 0x2b800000\nMvaa 172,188,156\ns_sub_u32 s98, s4, 1\nMvaa 140,188,148\ns_lshl_b32 s5, s13, 5\nMvaa 140,184,144\n"
"s_add_u32 s5, s5, s11\nMvaa 108,184,136\ns_lshl_b32 s65, s5, 9\nMvaa 108,188,140\ns_add_u32 s65, s65, s72\nMvaa 76,188,132\ns_add_u32 s65, s65, 0x1580000\nMvaa 76,184,128\n"
"s_waitcnt vmcnt(13) lgkmcnt(0)\ns_barrier\nMvaa 80,224,128\nRa 192,10,16384\ns_and_b32 s66, s72, 0x2000\ns_add_u32 m0, s47, 65536\ns_add_u32 s46, s56, 0x0\nMvaa 80,228,132\n"
"s_add_u32 s66, s66, s56\nMvaa 112,228,140\nRa 196,11,16384\nD1 1,16,46,0\nMvaa 112,224,136\ns_add_u32 s66, s66, 0x100000\nMvaa 144,224,144\nRa 200,10,18432\nMvaa 144,228,148\n"
"s_and_b32 s5, s12, 1\nMvaa 176,228,156\nRa 204,11,18432\nMvaa 176,224,152\ns_mul_i32 s58, s5, 0x202000\nD1 1,16,46,1024\nMvaa 180,232,152\nRa 208,10,24576\n"
"s_xor_b32 s58, s58, s72\nMvaa 180,236,156\ns_add_u32 s58, s58, 0x15c0000\nMvaa 148,236,148\nRa 212,11,24576\nMvaa 148,232,144\ns_lshl_b32 s5, s5, 8\nMvaa 116,232,136\n"
"Ra 216,10,26624\nD1 1,16,46,2048\nMvaa 116,236,140\ns_add_u32 s61, s58, s5\nMvaa 84,236,132\nRa 220,11,26624\nMvaa 84,232,128\ns_lshl4_add_u32 s60, s13, s61\nMvaa 88,240,128\n"
"s_add_u32 s54, s60, 0\ns_xor_b32 s60, s54, 0x100\nMvaa 88,244,132\ns_cmp_eq_u32 s11, 0\ns_cselect_b32 s54, s54, 0x40000000\nD1 1,16,46,3072\nMvaa 120,244,140\ns_getpc_b64 s[8:9]\n"
".PC37:\ns_add_u32 s8, s8, .HD13-.PC37\ns_addc_u32 s9, s9, 0\nMvaa 120,240,136\ns_mov_b32 s97, 0xff800000\nMvaa 152,240,144\ns_mov_b32 s90, 3\ns_cmp_lt_u32 s11, 2\n"
"s_cselect_b32 s92, 2, 0\ns_add_u32 m0, s47, 81920\ns_add_u32 s46, s56, 0x1000\nMvaa 152,244,148\ns_add_u32 s86, s2, 0x20000\nMvaa 184,244,156\ns_add_u32 s87, s2, 0x410000\n"
"s_cmp_eq_u32 s13, 7\ns_cselect_b32 s92, s92, 0\nD1 1,16,46,0\nMvaa 184,240,152\ns_cmp_gt_u32 s13, 5\ns_cselect_b32 s4, s12, 15\nMvaa 188,248,152\ns_cmp_lt_u32 s4, 14\n"
"s_cselect_b32 s5, s11, 3\nMvaa 188,252,156\ns_cmp_lt_u32 s5, 2\ns_cselect_b32 s91, 1, 100\nMvaa 156,252,148\nv_and_b32 v27, 0x70, v1\n"
"buffer_load_dword v27, v27, s[16:19], s61 offen offset:128 sc0 sc1\nMvaa 156,248,144\ns_lshr_b32 s2, s13, 2\ns_bfe_u32 s3, s13, 0x10001\nD1 1,16,46,1024\nMvaa 124,248,136\n"
"s_mul_i32 s3, s3, 17\ns_add_u32 s2, s2, s3\nMvaa 124,252,140\ns_lshl2_add_u32 s3, s14, 0\nMvaa 92,252,132\ns_lshl_b32 s3, s3, 2\ns_lshr_b32 s3, 0xe420ca86, s3\nMvaa 92,248,128\n"
"WL 0\nMaaa 16,224,192\nRa 128,10,32768\ns_and_b32 s3, s3, 15\nD1 1,16,46,2048\nMaaa 16,228,196\ns_add_u32 s2, s2, s3\ns_cmp_eq_u32 s2, 32\ns_cselect_b32 s4, 16, s2\n"
"Maaa 48,228,204\nRa 132,11,32768\ns_mul_i32 s75, s4, 65536\nMaaa 48,224,200\ns_lshr_b32 s2, s13, 2\ns_bfe_u32 s3, s13, 0x10001\nMaaa 80,224,208\nRa 136,10,34816\nMaaa 80,228,212\n"
"s_mul_i32 s3, s3, 17\ns_add_u32 s2, s2, s3\nD1 1,16,46,3072\nMaaa 112,228,220\nRa 140,11,34816\ns_lshl2_add_u32 s3, s14, 1\nMaaa 112,224,216\ns_lshl_b32 s3, s3, 2\n"
"Maaa 116,232,216\nRa 144,10,40960\ns_lshr_b32 s3, 0xe420ca86, s3\ns_add_u32 m0, s47, 16384\ns_add_u32 s46, s56, 0x4000\nMaaa 116,236,220\ns_and_b32 s3, s3, 15\n"
"s_add_u32 s2, s2, s3\nMaaa 84,236,212\nRa 148,11,40960\ns_cmp_eq_u32 s2, 32\ns_cselect_b32 s4, 16, s2\nD1 1,16,46,0\nMaaa 84,232,208\ns_mul_i32 s76, s4, 65536\n"
"s_lshr_b32 s2, s13, 2\nMaaa 52,232,200\nRa 152,10,43008\ns_bfe_u32 s3, s13, 0x10001\nMaaa 52,236,204\ns_mul_i32 s3, s3, 17\nMaaa 20,236,196\nRa 156,11,43008\ns_add_u32 s2, s2, s3\n"
"Maaa 20,232,192\ns_lshl2_add_u32 s3, s14, 2\ns_lshl_b32 s3, s3, 2\nD1 1,16,46,1024\nMaaa 24,240,192\ns_lshr_b32 s3, 0xe420ca86, s3\ns_and_b32 s3, s3, 15\nMaaa 24,244,196\n"
"s_add_u32 s2, s2, s3\ns_cmp_eq_u32 s2, 32\ns_cselect_b32 s4, 16, s2\nMaaa 56,244,204\ns_mul_i32 s77, s4, 65536\ns_lshr_b32 s2, s13, 2\nMaaa 56,240,200\ns_bfe_u32 s3, s13, 0x10001\n"
"s_mul_i32 s3, s3, 17\nMaaa 88,240,208\ns_add_u32 s2, s2, s3\nD1 1,16,46,2048\nMaaa 88,244,212\ns_lshl2_add_u32 s3, s14, 3\ns_lshl_b32 s3, s3, 2\nMaaa 120,244,220\n"
"s_lshr_b32 s3, 0xe420ca86, s3\ns_and_b32 s3, s3, 15\nMaaa 120,240,216\ns_add_u32 s2, s2, s3\ns_cmp_eq_u32 s2, 32\ns_cselect_b32 s4, 16, s2\nMaaa 124,248,216\n"
"s_mul_i32 s78, s4, 65536\ns_lshr_b32 s3, s11, 1\nMaaa 124,252,220\ns_lshl_b32 s3, s3, 15\ns_add_u32 s2, s72, s3\nD1 1,16,46,3072\nMaaa 92,252,212\ns_and_b32 s3, s11, 1\n"
"Maaa 92,248,208\ns_lshl_b32 s3, s3, 12\ns_add_u32 s2, s2, s3\nMaaa 60,248,200\ns_add_u32 s2, s2, 0x0\ns_mov_b32 s55, 16384\nMaaa 60,252,204\ns_add_u32 s75, s75, s2\n"
"Maaa 28,252,196\ns_add_u32 s76, s76, s2\ns_add_u32 s77, s77, s2\nMaaa 28,248,192\ns_waitcnt vmcnt(18) lgkmcnt(0)\ns_barrier\nMaaa 0,160,192\nTa 224,16,32768\n"
"s_add_u32 s78, s78, s2\nMaaa 0,164,196\nTa 226,16,34816\nMaaa 32,164,204\nMaaa 32,160,200\nTa 228,16,49152\nMaaa 64,160,208\nTa 230,16,51200\nMaaa 64,164,212\nMaaa 96,164,220\n"
"Ta 232,17,32768\nMaaa 96,160,216\nTa 234,17,34816\nMaaa 100,168,216\nWV 7\nv_cmp_ne_u32 vcc, s15, v27\ns_nop 0\ns_cbranch_vccnz .S38\n.S39:\ns_sub_u32 s45, s86, 131072\n"
"Maaa 100,172,220\nTa 236,17,49152\ns_add_u32 m0, s47, 0\ns_add_u32 s46, s45, 0x0\nMaaa 68,172,212\nTa 238,17,51200\nMaaa 68,168,208\nD1 1,16,46,0\nMaaa 36,168,200\n"
"Ta 240,18,32768\ns_mov_b32 s36, s68\nMaaa 36,172,204\nTa 242,18,34816\nMaaa 4,172,196\ns_and_b32 s37, s69, 0xffff\nMaaa 4,168,192\nTa 244,18,49152\nMaaa 8,176,192\n"
"Ta 246,18,51200\nD1 1,16,46,1024\nMaaa 8,180,196\ns_mov_b32 s38, 0x1c0000\nMaaa 40,180,204\nTa 248,19,32768\nMaaa 40,176,200\nTa 250,19,34816\nMaaa 72,176,208\n"
"s_mov_b32 s39, 0x20000\ns_and_b32 s2, s74, 15\nMaaa 72,180,212\nTa 252,19,49152\nD1 1,16,46,2048\nMaaa 104,180,220\nTa 254,19,51200\nMaaa 104,176,216\ns_max_u32 s2, s2, 2\n"
"Maaa 108,184,216\ns_sub_u32 s2, s2, 2\ns_mul_i32 s3, s73, 14\nMaaa 108,188,220\ns_add_u32 s2, s2, s3\nMaaa 76,188,212\nD1 1,16,46,3072\nMaaa 76,184,208\n"
"s_lshl2_add_u32 s2, s2, s11\nMaaa 44,184,200\ns_mul_i32 s3, s2, 4682\ns_lshr_b32 s3, s3, 16\nMaaa 44,188,204\ns_mul_i32 s22, s3, 14\nMaaa 12,188,196\ns_sub_u32 s22, s2, s22\n"
"Maaa 12,184,192\nWL 0\nMvaa 80,224,128\nTa 160,12,32768\ns_add_u32 m0, s47, -32768\ns_add_u32 s46, s56, 0x5000\nD1 1,16,46,0\nMvaa 80,228,132\nTa 162,12,34816\nMvaa 84,236,132\n"
"s_and_b32 s21, s3, 28\nMvaa 84,232,128\nTa 164,12,49152\nMvaa 88,240,128\nTa 166,12,51200\ns_add_u32 s21, s21, s3\nMvaa 88,244,132\nD1 1,16,46,1024\nMvaa 92,252,132\n"
"Ta 168,13,32768\nMvaa 92,248,128\nTa 170,13,34816\nMvaa 124,248,136\ns_lshl_b32 s21, s21, 4\ns_add_u32 s21, s21, 1024\nMvaa 124,252,140\nTa 172,13,49152\nMvaa 120,244,140\n"
"Ta 174,13,51200\nD1 1,16,46,2048\nMvaa 120,240,136\ns_xor_b32 s3, s3, 28\nMvaa 116,232,136\nTa 176,14,32768\ns_bfe_u32 s41, s3, 0x30002\nMvaa 116,236,140\nTa 178,14,34816\n"
"s_cmp_gt_u32 s22, 6\ns_cselect_b32 s2, 1179648, 0\ns_cselect_b32 s1, 16, 0\nMvaa 112,228,140\ns_add_u32 s41, s41, s1\nCV 40,80,81\nMvaa 112,224,136\nTa 180,14,49152\n"
"D1 1,16,46,3072\nMvaa 144,224,144\nTa 182,14,51200\ns_lshl4_add_u32 s41, s41, 0xc048400\nMvaa 144,228,148\ns_and_b32 s1, s3, 28\nMvaa 148,236,148\nTa 184,15,32768\n"
"s_add_u32 m0, s47, -65536\ns_add_u32 s46, s45, 0x10000\nMvaa 148,232,144\nTa 186,15,34816\nCV 41,82,83\nMvaa 152,240,144\nD1 1,16,46,0\ns_add_u32 s3, s3, s1\nMvaa 152,244,148\n"
"Ta 188,15,49152\nCV 42,84,85\nMvaa 156,252,148\nTa 190,15,51200\nMvaa 156,248,144\ns_lshl_b32 s3, s3, 11\nCV 43,86,87\nMvaa 188,248,152\ns_add_u32 s2, s2, s3\n"
"s_lshl_b32 s3, s22, 17\nMvaa 188,252,156\nD1 1,16,46,1024\nMvaa 184,244,156\ns_add_u32 s2, s2, s3\nCV 44,88,89\nMvaa 184,240,152\ns_add_u32 s20, s2, 0xce0000\nMvaa 180,232,152\n"
"s_lshl_b32 s2, s22, 7\ns_add_u32 s40, s2, 0xc04b800\nMvaa 180,236,156\ns_mul_i32 s2, s21, 896\nMvaa 176,228,156\nD1 1,16,46,2048\nCV 45,90,91\nMvaa 176,224,152\nWL 0\ns_barrier\n"
"Mvaa 64,160,128\nRa 192,10,49152\ns_lshl_b32 s3, s22, 6\nMvaa 64,164,132\ns_add_u32 s22, s2, s3\nMvaa 68,172,132\nRa 196,11,49152\nMvaa 68,168,128\nD1 1,16,46,3072\nCV 46,92,93\n"
"Mvaa 72,176,128\nRa 200,10,51200\ns_add_u32 s23, s22, 57344\nMvaa 72,180,132\nCV 47,94,95\nCV 60,120,121\nMvaa 76,188,132\nRa 204,11,51200\ns_add_u32 m0, s47, 32768\n"
"s_add_u32 s46, s45, 0x8000\nMvaa 76,184,128\nCV 61,122,123\nCV 62,124,125\nMvaa 108,184,136\nRa 208,10,57344\nD1 1,16,46,0\nMvaa 108,188,140\nCV 63,126,127\nMvaa 104,180,140\n"
"Ra 212,11,57344\nCV 56,112,113\nMvaa 104,176,136\nCV 57,114,115\nMvaa 100,168,136\nRa 216,10,59392\nCV 58,116,117\nMvaa 100,172,140\nD1 1,16,46,1024\nMvaa 96,164,140\n"
"Ra 220,11,59392\nCV 59,118,119\nMvaa 96,160,136\nCV 32,64,65\nMvaa 128,160,144\nCV 33,66,67\nCV 34,68,69\nMvaa 128,164,148\nCV 35,70,71\nMvaa 132,172,148\nD1 1,16,46,2048\n"
"Mvaa 132,168,144\nCV 36,72,73\nCV 37,74,75\nMvaa 136,176,144\nCV 38,76,77\nMvaa 136,180,148\nCV 39,78,79\nCV 52,104,105\nMvaa 140,188,148\nCV 53,106,107\nMvaa 140,184,144\n"
"D1 1,16,46,3072\nCV 54,108,109\nMvaa 172,184,152\nCV 55,110,111\nMvaa 172,188,156\nCV 48,96,97\nMvaa 168,180,156\ns_add_u32 m0, s47, 49152\ns_add_u32 s46, s45, 0x400000\n"
"Mvaa 168,176,152\nCV 49,98,99\nMvaa 164,168,152\nD1 1,16,46,0\nCV 50,100,101\nMvaa 164,172,156\nCV 51,102,103\nCV 104,144,145\nCV 105,146,147\nMvaa 160,164,156\nCV 106,148,149\n"
"CV 107,150,151\nMvaa 160,160,152\nWL 0\nMaaa 0,160,192\nSv 32,1,16,75,0\nCV 108,152,153\nCV 109,154,155\nMaaa 0,164,196\nD1 1,16,46,1024\nCV 110,156,157\nMaaa 4,172,196\n"
"CV 111,158,159\nCV 124,184,185\nMaaa 4,168,192\nCV 125,186,187\nCV 126,188,189\nMaaa 8,176,192\nCV 127,190,191\nCV 120,176,177\nCV 121,178,179\nMaaa 8,180,196\nCV 122,180,181\n"
"CV 123,182,183\nMaaa 12,188,196\nD1 1,16,46,2048\nCV 96,128,129\nMaaa 12,184,192\nCV 97,130,131\nCV 98,132,133\nCV 99,134,135\nMaaa 44,184,200\nSv 36,1,16,75,1024\nCV 100,136,137\n"
"Maaa 44,188,204\nCV 101,138,139\nCV 102,140,141\nMaaa 40,180,204\nCV 103,142,143\nCV 116,168,169\nCV 117,170,171\nMaaa 40,176,200\nD1 1,16,46,3072\nCV 118,172,173\n.W545:\n"
"s_cbranch_execz .W549\nMaaa 36,168,200\nCV 119,174,175\nCV 112,160,161\nMaaa 36,172,204\nCV 113,162,163\nCV 114,164,165\nCV 115,166,167\nMaaa 32,164,204\n"
"s_add_u32 m0, s47, -49152\ns_add_u32 s46, s45, 0x408000\nMaaa 32,160,200\nv_add_u32 v2, 0x10000, v1\ns_lshr_b32 s2, s59, 1\nMaaa 64,160,208\nD1 1,16,46,0\nSv 40,1,16,75,2048\n"
"v_add_u32 v24, s2, v2\nMaaa 64,164,212\nv_xor_b32 v26, 0x1000, v24\nv_lshrrev_b32 v20, 4, v0\nMaaa 68,172,212\nv_lshlrev_b32 v20, 2, v20\nv_and_b32 v0, 15, v0\nMaaa 68,168,208\n"
"v_sub_u32 v0, v0, v20\ns_lshl_b32 s2, s11, 4\nMaaa 72,176,208\nv_add_u32 v3, s2, v0\nv_subrev_u32 v0, 32, v3\ns_lshl_b32 s2, s11, 12\nMaaa 72,180,212\nD1 1,16,46,1024\n"
"v_add_u32 v22, s2, v1\nMaaa 76,188,212\nv_add_u32 v23, 0x20000, v22\nv_add_u32 v22, 0x8000, v22\nMaaa 76,184,208\nv_mov_b32 v4, 0\nv_mov_b32 v5, 0\nv_mov_b32 v6, 0\n"
"Maaa 108,184,216\nSv 44,1,16,75,3072\nv_mov_b32 v7, 0\nMaaa 108,188,220\nv_mov_b32 v8, 0\nv_mov_b32 v9, 0\nMaaa 104,180,220\nD1 1,16,46,2048\nv_mov_b32 v10, 0\nv_mov_b32 v11, 0\n"
"Maaa 104,176,216\nv_mov_b32 v12, 0\nv_mov_b32 v13, 0\nMaaa 100,168,216\nv_mov_b32 v14, 0\nv_mov_b32 v15, 0\nMaaa 100,172,220\nv_mov_b32 v16, 0\nv_mov_b32 v17, 0\nv_mov_b32 v18, 0\n"
"Maaa 96,164,220\nv_mov_b32 v19, 0\nv_mov_b32 v86, 0\nMaaa 96,160,216\nWV 19\ns_barrier\nMaaa 16,224,192\nD1 1,16,46,3072\ns_add_u32 s75, s75, s55\nSv 48,1,16,75,0\n"
"Maaa 16,228,196\nv_mov_b32 v87, 0\nv_mov_b32 v88, 0\nMaaa 20,236,196\nv_mov_b32 v89, 0\nv_mov_b32 v95, -1\nMaaa 20,232,192\nMaaa 24,240,192\nRa 128,24,0\nMaaa 24,244,196\n"
"Maaa 28,252,196\nRa 196,23,1024\nRa 132,24,1024\nMaaa 28,248,192\nRa 192,23,0\nMaaa 60,248,200\nSv 52,1,16,75,1024\nRa 136,24,2048\nMaaa 60,252,204\nMaaa 56,244,204\n"
"Ra 140,24,3072\nMaaa 56,240,200\nMaaa 52,232,200\nRa 144,26,0\nMaaa 52,236,204\nMaaa 48,228,204\nRa 204,23,3072\nRa 148,26,1024\nMaaa 48,224,200\nRa 200,23,2048\nMaaa 80,224,208\n"
"Sv 56,1,16,75,2048\nRa 152,26,2048\nMaaa 80,228,212\nMaaa 84,236,212\nRa 156,26,3072\nMaaa 84,232,208\nMaaa 88,240,208\nMaaa 88,244,212\nMaaa 92,252,212\nRa 212,23,17408\n"
"Maaa 92,248,208\nRa 208,23,16384\nMaaa 124,248,216\nRa 248,22,2048\nSv 60,1,16,75,3072\nMaaa 124,252,220\nRa 252,22,3072\nMaaa 120,244,220\nRa 244,22,1024\nMaaa 120,240,216\n"
"Ra 240,22,0\nMaaa 116,232,216\nRa 232,22,51200\nMaaa 116,236,220\nRa 236,22,52224\nMaaa 112,228,220\nRa 228,22,50176\nRa 220,23,19456\nMaaa 112,224,216\nRa 224,22,49152\n"
"Ra 216,23,18432\nWL 0\nCvaa 32,128,192,4\nRa 160,24,8192\nMvaa 32,132,200\nMvaa 32,136,208\nRa 164,24,9216\nMvaa 32,140,216\nCvaa 36,140,220,8\nRa 168,24,10240\nMvaa 36,136,212\n"
"s_add_u32 m0, s89, 32768\nMvaa 36,132,204\nRa 172,24,11264\ns_barrier\nMvaa 36,128,196\nMvaa 36,144,228\nRa 176,26,8192\nSv 96,1,16,76,0\nMvaa 36,148,236\ns_add_u32 s46, s86, s88\n"
"Mvaa 36,152,244\nRa 180,26,9216\nMvaa 36,156,252\nAR 92,0\nMvaa 32,156,248\nRa 184,26,10240\nAR 93,1\nMvaa 32,152,240\nD1 1,16,46,0\nMvaa 32,148,232\nRa 188,26,11264\nWL 4\n"
"Mvaa 32,144,224\nCV 96,92,93\nCvaa 40,160,192,12\nAR 92,2\nAR 93,3\nMvaa 40,164,200\nCV 97,92,93\nMvaa 40,168,208\nv_and_b32 v27, 0x70, v1\n"
"buffer_load_dword v27, v27, s[16:19], s61 offen offset:136 sc0 sc1\nMvaa 40,172,216\nAR 92,4\nCvaa 44,172,220,16\nAR 93,5\nCV 98,92,93\nMvaa 44,168,212\nD1 1,16,46,1024\n"
"Mvaa 44,164,204\nWL 0\nMvaa 44,160,196\nAR 92,6\nMvaa 44,176,228\nSv 100,1,16,76,1024\nAR 93,7\nMvaa 44,180,236\ns_add_u32 s88, s88, 32768\nEX 80,32\nMvaa 44,184,244\nCV 99,92,93\n"
"AR 92,8\nMvaa 44,188,252\nEX 81,33\nMvaa 40,188,248\nAR 93,9\nCV 100,92,93\nMvaa 40,184,240\nD1 1,16,46,2048\nMvaa 40,180,232\nAR 92,10\nAR 93,11\nMvaa 40,176,224\nAD 84,80,81\n"
"CV 64,80,81\nCV 101,92,93\nAR 92,12\nEX 82,34\nAR 93,13\nCV 102,92,93\nWV 16\nEX 83,35\nAD 84,84,82\nAR 92,14\nD1 1,16,46,3072\ns_add_u32 m0, s89, 49152\ns_barrier\nAR 93,15\n"
"AD 84,84,83\nCV 65,82,83\nRa 128,24,32768\nSv 104,1,16,76,2048\ns_add_u32 s46, s87, s88\nEX 80,36\nRa 132,24,33792\nCV 103,92,93\nEX 81,37\nRa 136,24,34816\nAR 92,16\nAR 93,17\n"
"D1 1,16,46,0\nCV 104,92,93\nRa 140,24,35840\nAR 92,18\nAD 85,80,81\nCV 68,80,81\nRa 144,26,32768\nAR 93,19\nCV 105,92,93\nEX 82,38\nRa 148,26,33792\nAR 92,20\nEX 83,39\n"
"Ra 152,26,34816\nAD 85,85,82\nAR 93,21\nD1 1,16,46,1024\nCV 106,92,93\nRa 156,26,35840\nAR 92,22\nAD 85,85,83\nCV 69,82,83\nRa 160,24,40960\nSv 108,1,16,76,3072\nAR 93,23\n"
"s_xor_b32 s89, s89, 0x10000\nEX 80,40\nRa 164,24,41984\nCV 107,92,93\nAR 92,24\nWL 6\nEX 81,41\nRa 168,24,43008\nAD 84,84,80\nAR 93,25\nD1 1,16,46,2048\nCV 108,92,93\n"
"Ra 172,24,44032\nAR 92,26\nAR 93,27\nAD 84,84,81\nCV 66,80,81\nCvaa 48,128,192,4\nRa 176,26,40960\nCV 109,92,93\nAR 92,28\nMvaa 48,132,200\nEX 82,42\nMvaa 48,136,208\n"
"Ra 180,26,41984\nAR 93,29\nMvaa 48,140,216\nWL 6\nEX 83,43\nWV 9\nv_cmp_ne_u32 vcc, s15, v27\ns_nop 0\ns_cbranch_vccnz .S41\n.S42:\nCvaa 52,140,220,8\nRa 184,26,43008\n"
"AD 84,84,82\nMvaa 52,136,212\nD1 1,16,46,3072\ns_add_u32 m0, s89, 0\nCV 110,92,93\nMvaa 52,132,204\nRa 188,26,44032\nAR 92,30\nMvaa 52,128,196\nAD 84,84,83\nCV 67,82,83\nAR 93,31\n"
"Mvaa 52,144,228\nRa 128,2,49152\ns_add_u32 s76, s76, s55\nSv 112,1,16,76,0\nMvaa 52,148,236\ns_add_u32 s46, s86, s88\nEX 80,44\nMvaa 52,152,244\nRa 132,2,50176\n"
"v_xor_b32 v24, 0x10000, v24\nCV 111,92,93\nMvaa 52,156,252\nWL 6\nEX 81,45\nMvaa 48,156,248\nRa 136,2,51200\nAD 85,85,80\nAR 92,32\nMvaa 48,152,240\nD1 1,16,46,0\nAR 93,33\n"
"CV 112,92,93\nMvaa 48,148,232\nRa 140,2,52224\nAR 92,34\nMvaa 48,144,224\nAD 85,85,81\nCV 70,80,81\nAR 93,35\nCvaa 56,160,192,12\nRa 144,2,53248\nCV 113,92,93\nAR 92,36\n"
"Mvaa 56,164,200\nEX 82,46\nMvaa 56,168,208\nRa 148,2,54272\nv_xor_b32 v26, 0x10000, v26\nAR 93,37\nMvaa 56,172,216\nWL 6\nEX 83,47\nCvaa 60,172,220,16\nRa 152,2,55296\n"
"AD 85,85,82\nCV 114,92,93\nMvaa 60,168,212\nD1 1,16,46,1024\nAR 92,38\nAR 93,39\nMvaa 60,164,204\nRa 156,2,56320\nCV 115,92,93\nMvaa 60,160,196\nAD 85,85,83\nCV 71,82,83\n"
"Mvaa 60,176,228\nRa 160,2,57344\nv_max_f32 v90, v84, v85\nSv 116,1,16,76,1024\nMvaa 60,180,236\ns_add_u32 s88, s88, 32768\nEX 80,48\nMvaa 60,184,244\nRa 164,2,58368\n"
"v_cmp_lt_f32 vcc, s63, v90\nMvaa 60,188,252\nWL 8\nEX 81,49\nMvaa 56,188,248\nRa 168,2,59392\ns_cbranch_vccnz .T23\n.T24:\nv_mov_b32 v20, v84\nv_mov_b32 v21, v85\n"
"v_mov_b32 v22, v20\nv_mov_b32 v23, v21\nMvaa 56,184,240\nD1 1,16,46,2048\nv_permlane32_swap_b32 v20, v22\nv_permlane32_swap_b32 v21, v23\nAD 20,20,22\nAD 21,21,23\n"
"v_mov_b32 v22, v20\nv_mov_b32 v23, v21\nMvaa 56,180,232\nRa 172,2,60416\nv_permlane16_swap_b32 v20, v22\nv_permlane16_swap_b32 v21, v23\nAD 20,20,22\nAD 21,21,23\n"
"v_min_f32 v90, v20, v21\nv_cmp_gt_f32 vcc, s64, v90\ns_nop 0\ns_cbranch_vccnz .T25\n.T26:\nAD 86,86,84\nMvaa 56,176,224\nAD 84,80,81\nCV 72,80,81\nAR 92,40\nZvav 128,128,64\n"
"Ra 176,2,61440\nAR 93,41\nCV 116,92,93\nZvav 132,128,68\nEX 82,50\nAR 92,42\nZvav 140,132,68\nRa 180,2,62464\nAR 93,43\nCV 117,92,93\nZvav 136,132,64\nWL 8\nEX 83,51\n"
"Zvav 144,136,64\nRa 184,2,63488\nAD 84,84,82\nAR 92,44\nZvav 148,136,68\nD1 1,16,46,3072\ns_add_u32 m0, s89, 16384\nAR 93,45\nZvav 156,140,68\nRa 188,2,64512\ns_barrier\n"
"CV 118,92,93\nZvav 152,140,64\nAD 84,84,83\nCV 73,82,83\nAR 92,46\nZvav 160,144,64\nRa 128,24,0\nSv 120,1,16,76,2048\nAR 93,47\nZvav 164,144,68\ns_add_u32 s46, s87, s88\nEX 80,52\n"
"buffer_store_dword v25, off, s[16:19], s54 offset:12 sc0 sc1\nZvav 172,148,68\nRa 132,24,1024\nv_xor_b32 v2, 0x10000, v2\nCV 119,92,93\nZvav 168,148,64\nWL 8\nEX 81,53\n"
"Zvav 176,152,64\nRa 136,24,2048\nAD 87,87,85\nZvav 180,152,68\nD1 1,16,46,0\nAR 92,48\nAR 93,49\nZvav 188,156,68\nRa 140,24,3072\nCV 120,92,93\nZvav 184,156,64\nAD 85,80,81\n"
"CV 76,80,81\nZvav 192,160,64\nRa 144,26,0\nAR 92,50\nAR 93,51\nZvav 196,160,68\nEX 82,54\nZvav 204,164,68\nRa 148,26,1024\nCV 121,92,93\nZvav 200,164,64\nWL 8\nEX 83,55\n"
"Zvav 208,168,64\nRa 152,26,2048\nAD 85,85,82\nAR 92,52\nZvav 212,168,68\nD1 1,16,46,1024\nAR 93,53\nZvav 220,172,68\nRa 156,26,3072\nCV 122,92,93\nZvav 216,172,64\nAD 85,85,83\n"
"CV 77,82,83\nAR 92,54\nZvav 224,176,64\nRa 160,24,8192\nSv 124,1,16,76,3072\nZvav 228,176,68\nEX 80,56\nZvav 236,180,68\nRa 164,24,9216\nAR 93,55\nCV 123,92,93\nZvav 232,180,64\n"
"WL 6\nEX 81,57\nZvav 240,184,64\nRa 168,24,10240\nAD 84,84,80\nAR 92,56\nZvav 244,184,68\nD1 1,16,46,2048\nAR 93,57\nZvav 252,188,68\nRa 172,24,11264\nCV 124,92,93\nAR 92,58\n"
"Zvav 248,188,64\nAD 84,84,81\nCV 74,80,81\nCvaa 32,128,192,4\nRa 176,26,8192\nAR 93,59\nCV 125,92,93\nMvaa 32,132,200\nEX 82,58\nMvaa 32,136,208\nRa 180,26,9216\nAR 92,60\n"
"Mvaa 32,140,216\nWL 6\nEX 83,59\nCvaa 36,140,220,8\nRa 184,26,10240\nAD 84,84,82\nMvaa 36,136,212\nD1 1,16,46,3072\ns_add_u32 m0, s89, 32768\nAR 93,61\nMvaa 36,132,204\n"
"Ra 188,26,11264\nCV 126,92,93\nMvaa 36,128,196\nAD 84,84,83\nCV 75,82,83\nAR 92,62\nMvaa 36,144,228\nRa 128,2,16384\nSv 96,1,16,77,0\nMvaa 36,148,236\ns_add_u32 s46, s86, s88\n"
"EX 80,60\nMvaa 36,152,244\nRa 132,2,17408\nAR 93,63\nCV 127,92,93\nMvaa 36,156,252\nWL 6\nEX 81,61\nMvaa 32,156,248\nRa 136,2,18432\nAD 85,85,80\nAR 92,64\nMvaa 32,152,240\n"
"D1 1,16,46,0\nAR 93,65\nMvaa 32,148,232\nRa 140,2,19456\nCV 96,92,93\nAR 92,66\nMvaa 32,144,224\nAD 85,85,81\nCV 78,80,81\nCvaa 40,160,192,12\nRa 144,2,20480\nAR 93,67\n"
"CV 97,92,93\nMvaa 40,164,200\nEX 82,62\nMvaa 40,168,208\nRa 148,2,21504\nAR 92,68\nAR 93,69\nMvaa 40,172,216\nWL 6\nEX 83,63\nCvaa 44,172,220,16\nRa 152,2,22528\nAD 85,85,82\n"
"CV 98,92,93\nMvaa 44,168,212\nD1 1,16,46,1024\n.W546:\ns_cbranch_execz .W550\nAR 92,70\nMvaa 44,164,204\nRa 156,2,23552\nAR 93,71\nCV 99,92,93\nMvaa 44,160,196\nAD 85,85,83\n"
"CV 79,82,83\nMvaa 44,176,228\nRa 160,2,24576\nv_max_f32 v90, v84, v85\nSv 100,1,16,77,1024\nMvaa 44,180,236\ns_add_u32 s88, s88, 32768\nEX 80,32\nMvaa 44,184,244\nRa 164,2,25600\n"
"v_cmp_lt_f32 vcc, s63, v90\nMvaa 44,188,252\nWL 8\nEX 81,33\nMvaa 40,188,248\nRa 168,2,26624\ns_cbranch_vccnz .T28\n.T29:\nAR 92,72\nMvaa 40,184,240\nD1 1,16,46,2048\nAR 93,73\n"
"Mvaa 40,180,232\nRa 172,2,27648\nAD 86,86,84\nCV 100,92,93\nMvaa 40,176,224\nAD 84,80,81\nCV 64,80,81\nMvav 128,128,72\nRa 176,2,28672\nAR 92,74\nAR 93,75\nMvav 132,128,76\n"
"EX 82,34\nMvav 140,132,76\nRa 180,2,29696\nCV 101,92,93\nAR 92,76\nMvav 136,132,72\ns_waitcnt vmcnt(18) lgkmcnt(8)\nEX 83,35\nMvav 144,136,72\nRa 184,2,30720\nAD 84,84,82\n"
"AR 93,77\nMvav 148,136,76\nD1 1,16,46,3072\ns_add_u32 m0, s89, 49152\nMvav 156,140,76\nRa 188,2,31744\ns_barrier\nCV 102,92,93\nMvav 152,140,72\nAD 84,84,83\nCV 65,82,83\n"
"Mvav 160,144,72\nRa 128,24,32768\nSv 104,1,16,77,2048\nAR 92,78\nMvav 164,144,76\ns_add_u32 s46, s87, s88\nEX 80,36\nMvav 172,148,76\nRa 132,24,33792\nAR 93,79\nCV 103,92,93\n"
"Mvav 168,148,72\nWL 8\nEX 81,37\nMvav 176,152,72\nRa 136,24,34816\nAD 87,87,85\nMvav 180,152,76\nD1 1,16,46,0\nAR 92,80\nAR 93,81\nMvav 188,156,76\nRa 140,24,35840\nCV 104,92,93\n"
"Mvav 184,156,72\nAD 85,80,81\nCV 68,80,81\nMvav 192,160,72\nRa 144,26,32768\nAR 92,82\nAR 93,83\nMvav 196,160,76\nEX 82,38\nMvav 204,164,76\nRa 148,26,33792\nCV 105,92,93\n"
"Mvav 200,164,72\nWL 8\nEX 83,39\nMvav 208,168,72\nRa 152,26,34816\nAD 85,85,82\nAR 92,84\nMvav 212,168,76\nD1 1,16,46,1024\nAR 93,85\nMvav 220,172,76\nRa 156,26,35840\n"
"CV 106,92,93\nMvav 216,172,72\nAD 85,85,83\nCV 69,82,83\nAR 92,86\nMvav 224,176,72\nRa 160,24,40960\nSv 108,1,16,77,3072\nMvav 228,176,76\ns_xor_b32 s89, s89, 0x10000\nEX 80,40\n"
"Mvav 236,180,76\nRa 164,24,41984\nAR 93,87\nCV 107,92,93\nMvav 232,180,72\nWL 6\nEX 81,41\nMvav 240,184,72\nRa 168,24,43008\nAD 84,84,80\nAR 92,88\nMvav 244,184,76\n"
"D1 1,16,46,2048\nAR 93,89\nCV 108,92,93\nMvav 252,188,76\nRa 172,24,44032\nAR 92,90\nMvav 248,188,72\nAD 84,84,81\nCV 66,80,81\nAR 93,91\nCvaa 48,128,192,4\nRa 176,26,40960\n"
"CV 109,92,93\nAR 92,92\nMvaa 48,132,200\nEX 82,42\nMvaa 48,136,208\nRa 180,26,41984\nAR 93,93\nCV 110,92,93\nMvaa 48,140,216\nWL 6\nEX 83,43\nCvaa 52,140,220,8\nRa 184,26,43008\n"
"AD 84,84,82\nv_and_b32 v27, 0x70, v1\nbuffer_load_dword v27, v27, s[16:19], s61 offen offset:140 sc0 sc1\nMvaa 52,136,212\nD1 1,16,46,3072\ns_add_u32 m0, s89, 0\nAR 92,94\n"
"Mvaa 52,132,204\nRa 188,26,44032\nAR 93,95\nMvaa 52,128,196\nAD 84,84,83\nCV 67,82,83\nCV 111,92,93\nMvaa 52,144,228\nRa 128,2,49152\ns_add_u32 s77, s77, s55\nSv 112,1,16,77,0\n"
"Mvaa 52,148,236\ns_add_u32 s46, s86, s88\nEX 80,44\nMvaa 52,152,244\nRa 132,2,50176\nv_xor_b32 v24, 0x10000, v24\nAR 92,96\nMvaa 52,156,252\nWL 6\nEX 81,45\nMvaa 48,156,248\n"
"Ra 136,2,51200\nAD 85,85,80\nAR 93,97\nMvaa 48,152,240\nD1 1,16,46,0\nCV 112,92,93\nMvaa 48,148,232\nRa 140,2,52224\nAR 92,98\nAR 93,99\nMvaa 48,144,224\nAD 85,85,81\nCV 70,80,81\n"
"Cvaa 56,160,192,12\nRa 144,2,53248\nCV 113,92,93\nAR 92,100\nMvaa 56,164,200\nEX 82,46\nMvaa 56,168,208\nRa 148,2,54272\nv_xor_b32 v26, 0x10000, v26\nAR 93,101\nMvaa 56,172,216\n"
"WL 6\nEX 83,47\nCvaa 60,172,220,16\nRa 152,2,55296\nAD 85,85,82\nCV 114,92,93\nMvaa 60,168,212\nD1 1,16,46,1024\nAR 92,102\nMvaa 60,164,204\nRa 156,2,56320\nAR 93,103\n"
"CV 115,92,93\nMvaa 60,160,196\nAD 85,85,83\nCV 71,82,83\nMvaa 60,176,228\nRa 160,2,57344\nv_max_f32 v90, v84, v85\nSv 116,1,16,77,1024\nMvaa 60,180,236\ns_add_u32 s88, s88, 32768\n"
"EX 80,48\nMvaa 60,184,244\nRa 164,2,58368\nv_cmp_lt_f32 vcc, s63, v90\nMvaa 60,188,252\nWL 8\nEX 81,49\nMvaa 56,188,248\nRa 168,2,59392\ns_cbranch_vccnz .T30\n.T31:\nAR 92,104\n"
"Mvaa 56,184,240\nD1 1,16,46,2048\nAR 93,105\nCV 116,92,93\nMvaa 56,180,232\nRa 172,2,60416\nAD 86,86,84\nMvaa 56,176,224\nAD 84,80,81\nCV 72,80,81\nAR 92,106\nMvav 128,128,64\n"
"Ra 176,2,61440\nAR 93,107\nCV 117,92,93\nMvav 132,128,68\nEX 82,50\nMvav 140,132,68\nRa 180,2,62464\nAR 92,108\nAR 93,109\nMvav 136,132,64\ns_waitcnt vmcnt(18) lgkmcnt(8)\n"
"EX 83,51\nMvav 144,136,64\nRa 184,2,63488\nAD 84,84,82\nCV 118,92,93\nMvav 148,136,68\nD1 1,16,46,3072\ns_add_u32 m0, s89, 16384\nAR 92,110\nMvav 156,140,68\nRa 188,2,64512\n"
"s_barrier\nMvav 152,140,64\nAD 84,84,83\nCV 73,82,83\nAR 93,111\nMvav 160,144,64\nRa 128,24,0\nSv 120,1,16,77,2048\nCV 119,92,93\nMvav 164,144,68\ns_add_u32 s46, s87, s88\n"
"EX 80,52\nMvav 172,148,68\nRa 132,24,1024\nv_xor_b32 v2, 0x10000, v2\nAR 92,112\nMvav 168,148,64\nWL 8\nEX 81,53\nMvav 176,152,64\nRa 136,24,2048\nAD 87,87,85\n"
"buffer_store_dword v25, off, s[16:19], s54 offset:8 sc0 sc1\nMvav 180,152,68\nD1 1,16,46,0\nAR 93,113\nCV 120,92,93\nMvav 188,156,68\nRa 140,24,3072\nAR 92,114\nMvav 184,156,64\n"
"AD 85,80,81\nCV 76,80,81\nMvav 192,160,64\nRa 144,26,0\nAR 93,115\nCV 121,92,93\nMvav 196,160,68\nEX 82,54\nMvav 204,164,68\nRa 148,26,1024\nAR 92,116\nMvav 200,164,64\nWL 8\n"
"EX 83,55\nMvav 208,168,64\nRa 152,26,2048\nAD 85,85,82\nAR 93,117\nMvav 212,168,68\nD1 1,16,46,1024\nCV 122,92,93\nMvav 220,172,68\nRa 156,26,3072\nAR 92,118\nMvav 216,172,64\n"
"AD 85,85,83\nCV 77,82,83\nAR 93,119\nMvav 224,176,64\nRa 160,24,8192\nSv 124,1,16,77,3072\nMvav 228,176,68\nEX 80,56\nMvav 236,180,68\nRa 164,24,9216\nCV 123,92,93\nAR 92,120\n"
"Mvav 232,180,64\nWL 6\nEX 81,57\nMvav 240,184,64\nRa 168,24,10240\nAD 84,84,80\nMvav 244,184,68\nD1 1,16,46,2048\nAR 93,121\nCV 124,92,93\nMvav 252,188,68\nRa 172,24,11264\n"
"AR 92,122\nMvav 248,188,64\nAD 84,84,81\nCV 74,80,81\nCvaa 32,128,192,4\nRa 176,26,8192\nAR 93,123\nCV 125,92,93\nMvaa 32,132,200\ns_add_u32 s49, s49, 131072\nEX 82,58\n"
"Mvaa 32,136,208\nRa 180,26,9216\nAR 92,124\nMvaa 32,140,216\nWL 6\nEX 83,59\nCvaa 36,140,220,8\nRa 184,26,10240\nAD 84,84,82\nAR 93,125\nMvaa 36,136,212\nD1 1,16,46,3072\n"
"s_add_u32 m0, s89, 32768\nMvaa 36,132,204\nRa 188,26,11264\nCV 126,92,93\nMvaa 36,128,196\nAD 84,84,83\nCV 75,82,83\nAR 92,126\nMvaa 36,144,228\nRa 128,2,16384\nSv 96,1,16,78,0\n"
"Mvaa 36,148,236\ns_add_u32 s46, s86, s88\nEX 80,60\nMvaa 36,152,244\nRa 132,2,17408\nAR 93,127\nCV 127,92,93\nMvaa 36,156,252\nWL 6\nEX 81,61\nMvaa 32,156,248\nRa 136,2,18432\n"
"AD 85,85,80\nMvaa 32,152,240\nD1 1,16,46,0\nMvaa 32,148,232\nRa 140,2,19456\nMvaa 32,144,224\nAD 85,85,81\nCV 78,80,81\nCvaa 40,160,192,12\nRa 144,2,20480\nMvaa 40,164,200\n"
"Sv 92,1,16,49,0\nEX 82,62\nMvaa 40,168,208\nRa 148,2,21504\nMvaa 40,172,216\nWL 6\nEX 83,63\nCvaa 44,172,220,16\nRa 152,2,22528\nAD 85,85,82\nMvaa 44,168,212\nD1 1,16,46,1024\n"
"Mvaa 44,164,204\nRa 156,2,23552\nMvaa 44,160,196\nAD 85,85,83\nCV 79,82,83\nMvaa 44,176,228\nRa 160,2,24576\nv_max_f32 v90, v84, v85\nSv 100,1,16,78,1024\nMvaa 44,180,236\n"
"s_add_u32 s88, s88, 32768\nEX 80,32\nMvaa 44,184,244\nRa 164,2,25600\nv_cmp_lt_f32 vcc, s63, v90\nMvaa 44,188,252\nWL 8\nEX 81,33\nMvaa 40,188,248\nRa 168,2,26624\n"
"s_cbranch_vccnz .T33\n.T34:\nMvaa 40,184,240\nD1 1,16,46,2048\nMvaa 40,180,232\nRa 172,2,27648\nAD 86,86,84\nMvaa 40,176,224\nAD 84,80,81\nCV 64,80,81\nMvav 128,128,72\n"
"Ra 176,2,28672\nMvav 132,128,76\nEX 82,34\nMvav 140,132,76\nRa 180,2,29696\nMvav 136,132,72\ns_waitcnt vmcnt(19) lgkmcnt(8)\nEX 83,35\nMvav 144,136,72\nRa 184,2,30720\n"
"AD 84,84,82\nMvav 148,136,76\nD1 1,16,46,3072\ns_add_u32 m0, s89, 49152\nMvav 156,140,76\nRa 188,2,31744\ns_barrier\nMvav 152,140,72\nAD 84,84,83\nCV 65,82,83\nMvav 160,144,72\n"
"Ra 128,24,32768\nSv 104,1,16,78,2048\nMvav 164,144,76\ns_add_u32 s46, s87, s88\nEX 80,36\nMvav 172,148,76\nRa 132,24,33792\nMvav 168,148,72\nWL 8\nEX 81,37\nMvav 176,152,72\n"
"Ra 136,24,34816\nAD 87,87,85\nMvav 180,152,76\nD1 1,16,46,0\nMvav 188,156,76\nRa 140,24,35840\nMvav 184,156,72\nAD 85,80,81\nCV 68,80,81\nMvav 192,160,72\nRa 144,26,32768\n"
"Mvav 196,160,76\nEX 82,38\nMvav 204,164,76\nRa 148,26,33792\nMvav 200,164,72\nWL 8\nEX 83,39\nMvav 208,168,72\nRa 152,26,34816\nAD 85,85,82\nMvav 212,168,76\nD1 1,16,46,1024\n"
"Mvav 220,172,76\nRa 156,26,35840\nMvav 216,172,72\nAD 85,85,83\nCV 69,82,83\nMvav 224,176,72\nRa 160,24,40960\nSv 108,1,16,78,3072\nMvav 228,176,76\ns_xor_b32 s89, s89, 0x10000\n"
"EX 80,40\nMvav 236,180,76\nRa 164,24,41984\nMvav 232,180,72\nWL 6\nEX 81,41\nMvav 240,184,72\nRa 168,24,43008\nAD 84,84,80\nMvav 244,184,76\nD1 1,16,46,2048\nMvav 252,188,76\n"
"Ra 172,24,44032\nMvav 248,188,72\nAD 84,84,81\nCV 66,80,81\nCvaa 48,128,192,4\nRa 176,26,40960\nMvaa 48,132,200\nEX 82,42\nMvaa 48,136,208\nRa 180,26,41984\nMvaa 48,140,216\nWL 6\n"
"EX 83,43\nv_cmp_ne_u32 vcc, s15, v27\ns_nop 0\ns_cbranch_vccnz .S44\n.S45:\nv_and_b32 v27, 0x70, v1\nbuffer_load_dword v27, v27, s[16:19], s61 offen offset:12 sc0 sc1\n"
"Cvaa 52,140,220,8\nRa 184,26,43008\nAD 84,84,82\nMvaa 52,136,212\nD1 1,16,46,3072\ns_add_u32 m0, s89, 0\nMvaa 52,132,204\nRa 188,26,44032\nMvaa 52,128,196\nAD 84,84,83\n"
"CV 67,82,83\nMvaa 52,144,228\nRa 128,2,49152\ns_add_u32 s78, s78, s55\nSv 112,1,16,78,0\nMvaa 52,148,236\ns_add_u32 s46, s86, s88\nEX 80,44\nMvaa 52,152,244\nRa 132,2,50176\n"
"v_xor_b32 v24, 0x10000, v24\nMvaa 52,156,252\nWL 6\nEX 81,45\nMvaa 48,156,248\nRa 136,2,51200\nAD 85,85,80\nMvaa 48,152,240\nD1 1,16,46,0\n.W547:\ns_cbranch_execz .W551\n"
"Mvaa 48,148,232\nRa 140,2,52224\nMvaa 48,144,224\nAD 85,85,81\nCV 70,80,81\nCvaa 56,160,192,12\nRa 144,2,53248\nMvaa 56,164,200\nSv 92,1,16,49,1024\nEX 82,46\nMvaa 56,168,208\n"
"Ra 148,2,54272\nv_xor_b32 v26, 0x10000, v26\nMvaa 56,172,216\nWL 6\nEX 83,47\nCvaa 60,172,220,16\nRa 152,2,55296\nAD 85,85,82\nMvaa 60,168,212\nD1 1,16,46,1024\nMvaa 60,164,204\n"
"Ra 156,2,56320\nMvaa 60,160,196\nAD 85,85,83\nCV 71,82,83\nMvaa 60,176,228\nRa 160,2,57344\nv_max_f32 v90, v84, v85\nSv 116,1,16,78,1024\nMvaa 60,180,236\n"
"s_add_u32 s88, s88, 32768\nEX 80,48\nMvaa 60,184,244\nRa 164,2,58368\nv_cmp_lt_f32 vcc, s63, v90\nMvaa 60,188,252\nWL 8\nEX 81,49\nMvaa 56,188,248\nRa 168,2,59392\n"
"s_cbranch_vccnz .T35\n.T36:\nMvaa 56,184,240\nD1 1,16,46,2048\nMvaa 56,180,232\nRa 172,2,60416\nAD 86,86,84\nMvaa 56,176,224\nAD 84,80,81\nCV 72,80,81\nMvav 128,128,64\n"
"Ra 176,2,61440\nMvav 132,128,68\nEX 82,50\nMvav 140,132,68\nRa 180,2,62464\nMvav 136,132,64\ns_waitcnt vmcnt(20) lgkmcnt(8)\nEX 83,51\nMvav 144,136,64\nRa 184,2,63488\n"
"AD 84,84,82\nMvav 148,136,68\nD1 1,16,46,3072\ns_add_u32 m0, s89, 16384\nMvav 156,140,68\nRa 188,2,64512\ns_barrier\nMvav 152,140,64\nAD 84,84,83\nCV 73,82,83\nMvav 160,144,64\n"
"Ra 128,24,0\nSv 120,1,16,78,2048\nMvav 164,144,68\ns_add_u32 s46, s87, s88\nEX 80,52\nMvav 172,148,68\nRa 132,24,1024\nv_xor_b32 v2, 0x10000, v2\nMvav 168,148,64\nWL 8\nEX 81,53\n"
"Mvav 176,152,64\nRa 136,24,2048\nAD 87,87,85\nMvav 180,152,68\nD1 1,16,46,0\nMvav 188,156,68\nRa 140,24,3072\nMvav 184,156,64\nAD 85,80,81\nCV 76,80,81\nMvav 192,160,64\n"
"Ra 144,26,0\nMvav 196,160,68\nEX 82,54\nMvav 204,164,68\nRa 148,26,1024\nMvav 200,164,64\nWL 8\nEX 83,55\nMvav 208,168,64\nRa 152,26,2048\nAD 85,85,82\nMvav 212,168,68\n"
"D1 1,16,46,1024\nMvav 220,172,68\nRa 156,26,3072\nMvav 216,172,64\nAD 85,85,83\nCV 77,82,83\nMvav 224,176,64\nRa 160,24,8192\nSv 124,1,16,78,3072\nMvav 228,176,68\nEX 80,56\n"
"Mvav 236,180,68\nRa 164,24,9216\nMvav 232,180,64\nWL 6\nEX 81,57\nMvav 240,184,64\nRa 168,24,10240\nAD 84,84,80\nMvav 244,184,68\nD1 1,16,46,2048\nMvav 252,188,68\n"
"Ra 172,24,11264\nMvav 248,188,64\nAD 84,84,81\nCV 74,80,81\ns_mov_b32 s99, 11\ns_mov_b32 s90, 0\n.p2align 6\n.A60:\nCvaa 32,128,192,4\nRa 176,26,8192\nMvaa 32,132,200\n"
"s_add_u32 s49, s49, 131072\nEX 82,58\nMvaa 32,136,208\nRa 180,26,9216\nMvaa 32,140,216\nWL 6\nEX 83,59\nCvaa 36,140,220,8\nRa 184,26,10240\nAD 84,84,82\nMvaa 36,136,212\n"
"D1 1,16,46,3072\ns_add_u32 m0, s89, 32768\nMvaa 36,132,204\nRa 188,26,11264\nMvaa 36,128,196\nAD 84,84,83\nCV 75,82,83\nMvaa 36,144,228\nRa 128,2,16384\nMvaa 36,148,236\n"
"s_add_u32 s46, s86, s88\nEX 80,60\nMvaa 36,152,244\nRa 132,2,17408\nMvaa 36,156,252\nWL 6\nEX 81,61\nMvaa 32,156,248\nRa 136,2,18432\nAD 85,85,80\nMvaa 32,152,240\nD1 1,16,46,0\n"
"Mvaa 32,148,232\nRa 140,2,19456\nMvaa 32,144,224\nAD 85,85,81\nCV 78,80,81\nCvaa 40,160,192,12\nRa 144,2,20480\nMvaa 40,164,200\nSv 92,1,16,49,0\nEX 82,62\nMvaa 40,168,208\n"
"Ra 148,2,21504\nMvaa 40,172,216\nWL 6\nEX 83,63\nCvaa 44,172,220,16\nRa 152,2,22528\nAD 85,85,82\nMvaa 44,168,212\nD1 1,16,46,1024\nMvaa 44,164,204\nRa 156,2,23552\n"
"Mvaa 44,160,196\nAD 85,85,83\nCV 79,82,83\nMvaa 44,176,228\nRa 160,2,24576\nv_max_f32 v90, v84, v85\nMvaa 44,180,236\ns_add_u32 s88, s88, 32768\nEX 80,32\nMvaa 44,184,244\n"
"Ra 164,2,25600\nv_cmp_lt_f32 vcc, s63, v90\nMvaa 44,188,252\nWL 8\nEX 81,33\nMvaa 40,188,248\nRa 168,2,26624\ns_cbranch_vccnz .T64\n.T65:\nMvaa 40,184,240\nD1 1,16,46,2048\n"
"Mvaa 40,180,232\nRa 172,2,27648\nAD 86,86,84\nMvaa 40,176,224\nAD 84,80,81\nCV 64,80,81\nMvav 128,128,72\nRa 176,2,28672\nMvav 132,128,76\nEX 82,34\nMvav 140,132,76\n"
"Ra 180,2,29696\nMvav 136,132,72\ns_waitcnt vmcnt(13) lgkmcnt(8)\nEX 83,35\nMvav 144,136,72\nRa 184,2,30720\nAD 84,84,82\nMvav 148,136,76\nD1 1,16,46,3072\n"
"s_add_u32 m0, s89, 49152\nMvav 156,140,76\nRa 188,2,31744\ns_barrier\nMvav 152,140,72\nAD 84,84,83\nCV 65,82,83\nMvav 160,144,72\nRa 128,24,32768\nMvav 164,144,76\n"
"s_add_u32 s46, s87, s88\nEX 80,36\nMvav 172,148,76\nRa 132,24,33792\nMvav 168,148,72\nWL 8\nEX 81,37\nMvav 176,152,72\nRa 136,24,34816\nAD 87,87,85\nMvav 180,152,76\nD1 1,16,46,0\n"
"Mvav 188,156,76\nRa 140,24,35840\nMvav 184,156,72\nAD 85,80,81\nCV 68,80,81\nMvav 192,160,72\nRa 144,26,32768\nMvav 196,160,76\nEX 82,38\nMvav 204,164,76\nRa 148,26,33792\n"
"Mvav 200,164,72\nWL 8\nEX 83,39\nMvav 208,168,72\nRa 152,26,34816\nAD 85,85,82\nMvav 212,168,76\nD1 1,16,46,1024\nMvav 220,172,76\nRa 156,26,35840\nMvav 216,172,72\nAD 85,85,83\n"
"CV 69,82,83\nMvav 224,176,72\nRa 160,24,40960\nMvav 228,176,76\ns_xor_b32 s89, s89, 0x10000\nEX 80,40\nMvav 236,180,76\nRa 164,24,41984\nMvav 232,180,72\nWL 6\nEX 81,41\n"
"Mvav 240,184,72\nRa 168,24,43008\nAD 84,84,80\nMvav 244,184,76\nD1 1,16,46,2048\nMvav 252,188,76\nRa 172,24,44032\nMvav 248,188,72\nAD 84,84,81\nCV 66,80,81\nCvaa 48,128,192,4\n"
"Ra 176,26,40960\nMvaa 48,132,200\nEX 82,42\nMvaa 48,136,208\nRa 180,26,41984\nMvaa 48,140,216\nWL 6\nEX 83,43\nCvaa 52,140,220,8\nRa 184,26,43008\nAD 84,84,82\nMvaa 52,136,212\n"
"D1 1,16,46,3072\ns_add_u32 m0, s89, 0\nMvaa 52,132,204\nRa 188,26,44032\nMvaa 52,128,196\nAD 84,84,83\nCV 67,82,83\nMvaa 52,144,228\nRa 128,2,49152\nMvaa 52,148,236\n"
"s_add_u32 s46, s86, s88\nEX 80,44\nMvaa 52,152,244\nRa 132,2,50176\nv_xor_b32 v24, 0x10000, v24\nMvaa 52,156,252\nWL 6\nEX 81,45\nMvaa 48,156,248\nRa 136,2,51200\nAD 85,85,80\n"
"Mvaa 48,152,240\nD1 1,16,46,0\nMvaa 48,148,232\nRa 140,2,52224\nMvaa 48,144,224\nAD 85,85,81\nCV 70,80,81\nCvaa 56,160,192,12\nRa 144,2,53248\nMvaa 56,164,200\nSv 92,1,16,49,1024\n"
"EX 82,46\nMvaa 56,168,208\nRa 148,2,54272\nv_xor_b32 v26, 0x10000, v26\nMvaa 56,172,216\nWL 6\nEX 83,47\nCvaa 60,172,220,16\nRa 152,2,55296\nAD 85,85,82\nMvaa 60,168,212\n"
"D1 1,16,46,1024\nMvaa 60,164,204\nRa 156,2,56320\nMvaa 60,160,196\nAD 85,85,83\nCV 71,82,83\nMvaa 60,176,228\nRa 160,2,57344\nv_max_f32 v90, v84, v85\nMvaa 60,180,236\n"
"s_add_u32 s88, s88, 32768\nEX 80,48\nMvaa 60,184,244\nRa 164,2,58368\nv_cmp_lt_f32 vcc, s63, v90\nMvaa 60,188,252\nWL 8\nEX 81,49\nMvaa 56,188,248\nRa 168,2,59392\n"
"s_cbranch_vccnz .T66\n.T67:\nMvaa 56,184,240\nD1 1,16,46,2048\nMvaa 56,180,232\nRa 172,2,60416\nAD 86,86,84\nMvaa 56,176,224\nAD 84,80,81\nCV 72,80,81\nMvav 128,128,64\n"
"Ra 176,2,61440\nMvav 132,128,68\nEX 82,50\nMvav 140,132,68\nRa 180,2,62464\nMvav 136,132,64\ns_waitcnt vmcnt(13) lgkmcnt(8)\nEX 83,51\nMvav 144,136,64\nRa 184,2,63488\n"
"AD 84,84,82\nMvav 148,136,68\nD1 1,16,46,3072\ns_add_u32 m0, s89, 16384\nMvav 156,140,68\nRa 188,2,64512\ns_barrier\nMvav 152,140,64\nAD 84,84,83\nCV 73,82,83\nMvav 160,144,64\n"
"Ra 128,24,0\nMvav 164,144,68\ns_add_u32 s46, s87, s88\nEX 80,52\nMvav 172,148,68\nRa 132,24,1024\nv_xor_b32 v2, 0x10000, v2\nMvav 168,148,64\nWL 8\nEX 81,53\nMvav 176,152,64\n"
"Ra 136,24,2048\nAD 87,87,85\nMvav 180,152,68\nD1 1,16,46,0\nMvav 188,156,68\nRa 140,24,3072\nMvav 184,156,64\nAD 85,80,81\nCV 76,80,81\nMvav 192,160,64\nRa 144,26,0\n"
"Mvav 196,160,68\nEX 82,54\nMvav 204,164,68\nRa 148,26,1024\nMvav 200,164,64\nWL 8\nEX 83,55\nMvav 208,168,64\nRa 152,26,2048\nAD 85,85,82\nMvav 212,168,68\nD1 1,16,46,1024\n"
"Mvav 220,172,68\nRa 156,26,3072\nMvav 216,172,64\nAD 85,85,83\nCV 77,82,83\nMvav 224,176,64\nRa 160,24,8192\nMvav 228,176,68\ns_sub_u32 s90, s90, 1\nEX 80,56\nMvav 236,180,68\n"
"Ra 164,24,9216\ns_cbranch_scc1 .E62\n.E63:\nMvav 232,180,64\nWL 6\nEX 81,57\nMvav 240,184,64\nRa 168,24,10240\nAD 84,84,80\nMvav 244,184,68\nD1 1,16,46,2048\n"
"s_sub_u32 s99, s99, 1\nMvav 252,188,68\nRa 172,24,11264\nMvav 248,188,64\nAD 84,84,81\nCV 74,80,81\ns_cbranch_scc0 .A60\ns_branch .A68\n.E62:\ns_swappc_b64 s[80:81], s[8:9]\n"
"s_branch .E63\n.HD13:\nbuffer_store_dword v25, off, s[16:19], s54 offset:4 sc0 sc1\nv_cmp_ne_u32 vcc, s15, v27\ns_nop 0\ns_cbranch_vccnz .S47\n.S48:\nv_and_b32 v27, 0x70, v1\n"
"buffer_load_dword v27, v27, s[16:19], s61 offen offset:8 sc0 sc1\ns_mov_b32 s90, 1\ns_swappc_b64 s[8:9], s[80:81]\nv_cmp_ne_u32 vcc, s15, v27\ns_nop 0\ns_cbranch_vccnz .S50\n"
".S51:\nv_and_b32 v27, 0xf0, v1\nbuffer_load_dword v27, v27, s[16:19], s61 offen offset:4 sc0 sc1\ns_mov_b32 s90, 1\ns_swappc_b64 s[8:9], s[80:81]\nv_cmp_ne_u32 vcc, s15, v27\n"
"s_nop 0\ns_cbranch_vccnz .S53\n.S54:\ns_mov_b32 s90, s91\ns_swappc_b64 s[8:9], s[80:81]\ns_branch .FF56\n.A68:\nCvaa 32,128,192,4\nRa 176,26,8192\nMvaa 32,132,200\n"
"s_add_u32 s49, s49, 131072\nEX 82,58\nMvaa 32,136,208\nRa 180,26,9216\nMvaa 32,140,216\nWL 6\nEX 83,59\nCvaa 36,140,220,8\nRa 184,26,10240\nAD 84,84,82\nMvaa 36,136,212\n"
"D1 1,16,46,3072\ns_add_u32 m0, s89, 32768\nMvaa 36,132,204\nRa 188,26,11264\nMvaa 36,128,196\nAD 84,84,83\nCV 75,82,83\ns_mov_b32 s94, s96\ns_add_u32 s95, s96, 65536\n"
"v_and_b32 v108, 0xf0, v1\nMvaa 36,144,228\nRa 128,2,16384\nMvaa 36,148,236\ns_add_u32 s46, s86, s88\nEX 80,60\nMvaa 36,152,244\nRa 132,2,17408\nMvaa 36,156,252\nWL 6\nEX 81,61\n"
"Mvaa 32,156,248\nRa 136,2,18432\nAD 85,85,80\nMvaa 32,152,240\nD1 1,16,46,0\nMvaa 32,148,232\nRa 140,2,19456\nMvaa 32,144,224\nAD 85,85,81\nCV 78,80,81\nCvaa 40,160,192,12\n"
"Ra 144,2,20480\nMvaa 40,164,200\nSv 92,1,16,49,0\nEX 82,62\nMvaa 40,168,208\nRa 148,2,21504\nMvaa 40,172,216\nWL 6\nEX 83,63\nCvaa 44,172,220,16\nRa 152,2,22528\nAD 85,85,82\n"
"Mvaa 44,168,212\nD1 1,16,46,1024\nMvaa 44,164,204\nRa 156,2,23552\nMvaa 44,160,196\nAD 85,85,83\nCV 79,82,83\nMvaa 44,176,228\nRa 160,2,24576\nv_max_f32 v90, v84, v85\n"
"Mvaa 44,180,236\ns_add_u32 s88, s88, 32768\nEX 80,32\nMvaa 44,184,244\nRa 164,2,25600\nv_cmp_lt_f32 vcc, s63, v90\nMvaa 44,188,252\nWL 8\nEX 81,33\nMvaa 40,188,248\n"
"Ra 168,2,26624\ns_cbranch_vccnz .T70\n.T71:\nMvaa 40,184,240\nD1 1,16,46,2048\nMvaa 40,180,232\nRa 172,2,27648\nAD 86,86,84\nMvaa 40,176,224\nAD 84,80,81\nCV 64,80,81\n"
"Mvav 128,128,72\nRa 176,2,28672\nv_lshrrev_b32 v92, 8, v1\nMvav 132,128,76\nEX 82,34\nMvav 140,132,76\nRa 180,2,29696\nMvav 136,132,72\ns_waitcnt vmcnt(13) lgkmcnt(8)\nEX 83,35\n"
"Mvav 144,136,72\nRa 184,2,30720\nAD 84,84,82\nMvav 148,136,76\nD1 1,16,46,3072\ns_add_u32 m0, s89, 49152\nMvav 156,140,76\nRa 188,2,31744\ns_barrier\nMvav 152,140,72\nAD 84,84,83\n"
"CV 65,82,83\n.W548:\ns_cbranch_execz .WB21\nMvav 160,144,72\nRa 128,24,32768\nv_bfe_u32 v93, v1, 6, 2\nMvav 164,144,76\ns_add_u32 s46, s87, s88\nEX 80,36\nMvav 172,148,76\n"
"Ra 132,24,33792\nv_cmp_eq_u32 vcc, v92, v93\nv_and_b32 v92, 16, v1\nv_mov_b32 v93, 0x3f80\nv_cndmask_b32 v93, 0, v93, vcc\nMvav 168,148,72\nWL 8\nEX 81,37\nMvav 176,152,72\n"
"Ra 136,24,34816\nAD 87,87,85\nMvav 180,152,76\nD1 1,16,46,0\nMvav 188,156,76\nRa 140,24,35840\nv_lshlrev_b32 v93, v92, v93\nMvav 184,156,72\nAD 85,80,81\nCV 68,80,81\n"
"Mvav 192,160,72\nRa 144,26,32768\nv_and_b32 v92, 32, v1\nMvav 196,160,76\nEX 82,38\nMvav 204,164,76\nRa 148,26,33792\nv_cmp_eq_u32 vcc, 0, v92\nv_mov_b32 v112, 0\n"
"v_mov_b32 v113, 0\nv_cndmask_b32 v114, 0, v93, vcc\nMvav 200,164,72\nWL 8\nEX 83,39\nMvav 208,168,72\nRa 152,26,34816\nAD 85,85,82\nMvav 212,168,76\nD1 1,16,46,1024\n"
"Mvav 220,172,76\nRa 156,26,35840\nv_sub_u32 v115, v93, v114\nMvav 216,172,72\nAD 85,85,83\nCV 69,82,83\nMvav 224,176,72\nRa 160,24,40960\nv_mov_b32 v116, 0\nMvav 228,176,76\n"
"s_xor_b32 s89, s89, 0x10000\nEX 80,40\nMvav 236,180,76\nRa 164,24,41984\nMvav 232,180,72\nWL 6\nEX 81,41\nMvav 240,184,72\nRa 168,24,43008\nAD 84,84,80\nMvav 244,184,76\n"
"D1 1,16,46,2048\nMvav 252,188,76\nRa 172,24,44032\nv_mov_b32 v117, 0\nMvav 248,188,72\nAD 84,84,81\nCV 66,80,81\nCvaa 48,128,192,4\nRa 176,26,40960\nMvaa 48,132,200\n"
"s_mov_b32 s86, s94\nEX 82,42\nMvaa 48,136,208\nRa 180,26,41984\nMvaa 48,140,216\nWL 6\nEX 83,43\nCvaa 52,140,220,8\nRa 184,26,43008\nAD 84,84,82\nMvaa 52,136,212\nD1 1,16,46,3072\n"
"s_add_u32 m0, s89, 0\nMvaa 52,132,204\nRa 188,26,44032\nMvaa 52,128,196\nAD 84,84,83\nCV 67,82,83\nMvaa 52,144,228\nRa 128,2,49152\nMvaa 52,148,236\ns_add_u32 s46, s86, s88\n"
"EX 80,44\nMvaa 52,152,244\nRa 132,2,50176\nv_xor_b32 v24, 0x10000, v24\nMvaa 52,156,252\nWL 6\nEX 81,45\nMvaa 48,156,248\nRa 136,2,51200\nAD 85,85,80\nMvaa 48,152,240\n"
"D1 1,16,46,0\nMvaa 48,148,232\nRa 140,2,52224\nMvaa 48,144,224\nAD 85,85,81\nCV 70,80,81\nCvaa 56,160,192,12\nRa 144,2,53248\nMvaa 56,164,200\nSv 92,1,16,49,1024\nEX 82,46\n"
"Mvaa 56,168,208\nRa 148,2,54272\nv_xor_b32 v26, 0x10000, v26\nMvaa 56,172,216\nWL 6\nEX 83,47\nCvaa 60,172,220,16\nRa 152,2,55296\nAD 85,85,82\nMvaa 60,168,212\nD1 1,16,46,1024\n"
"Mvaa 60,164,204\nRa 156,2,56320\nMvaa 60,160,196\nAD 85,85,83\nCV 71,82,83\nMvaa 60,176,228\nRa 160,2,57344\nv_max_f32 v90, v84, v85\nMvaa 60,180,236\ns_add_u32 s88, s88, 32768\n"
"EX 80,48\nMvaa 60,184,244\nRa 164,2,58368\nv_cmp_lt_f32 vcc, s63, v90\nMvaa 60,188,252\nWL 8\nEX 81,49\nMvaa 56,188,248\nRa 168,2,59392\ns_cbranch_vccnz .T72\n.T73:\n"
"Mvaa 56,184,240\nD1 1,16,46,2048\nMvaa 56,180,232\nRa 172,2,60416\nAD 86,86,84\nMvaa 56,176,224\nAD 84,80,81\nCV 72,80,81\nMvav 128,128,64\nRa 176,2,61440\nMvav 132,128,68\n"
"EX 82,50\nMvav 140,132,68\nRa 180,2,62464\nMvav 136,132,64\ns_waitcnt vmcnt(13) lgkmcnt(8)\nEX 83,51\nMvav 144,136,64\nRa 184,2,63488\nAD 84,84,82\nMvav 148,136,68\n"
"D1 1,16,46,3072\ns_add_u32 m0, s89, 16384\nMvav 156,140,68\nRa 188,2,64512\ns_barrier\nMvav 152,140,64\nAD 84,84,83\nCV 73,82,83\nMvav 160,144,64\nRa 128,24,0\nMvav 164,144,68\n"
"s_add_u32 s46, s87, s88\nEX 80,52\nMvav 172,148,68\nRa 132,24,1024\nv_xor_b32 v2, 0x10000, v2\nMvav 168,148,64\nWL 8\nEX 81,53\nMvav 176,152,64\nRa 136,24,2048\nAD 87,87,85\n"
"Mvav 180,152,68\nD1 1,16,46,0\nMvav 188,156,68\nRa 140,24,3072\nMvav 184,156,64\nAD 85,80,81\nCV 76,80,81\nMvav 192,160,64\nRa 144,26,0\nMvav 196,160,68\nEX 82,54\n"
"Mvav 204,164,68\nRa 148,26,1024\nMvav 200,164,64\nWL 8\nEX 83,55\nMvav 208,168,64\nRa 152,26,2048\nAD 85,85,82\nMvav 212,168,68\nD1 1,16,46,1024\nMvav 220,172,68\nRa 156,26,3072\n"
"Mvav 216,172,64\nAD 85,85,83\nCV 77,82,83\nMvav 224,176,64\nRa 160,24,8192\nMvav 228,176,68\nEX 80,56\nMvav 236,180,68\nRa 164,24,9216\nMvav 232,180,64\nWL 6\nEX 81,57\n"
"Mvav 240,184,64\nRa 168,24,10240\nAD 84,84,80\nMvav 244,184,68\nD1 1,16,46,2048\nMvav 252,188,68\nRa 172,24,11264\nMvav 248,188,64\nAD 84,84,81\nCV 74,80,81\nCvaa 32,128,192,4\n"
"Ra 176,26,8192\nMvaa 32,132,200\nEX 82,58\nMvaa 32,136,208\nRa 180,26,9216\nMvaa 32,140,216\nWL 6\nEX 83,59\nCvaa 36,140,220,8\nRa 184,26,10240\nAD 84,84,82\nMvaa 36,136,212\n"
"D1 1,16,46,3072\ns_add_u32 m0, s89, 32768\nMvaa 36,132,204\nRa 188,26,11264\nMvaa 36,128,196\nAD 84,84,83\nCV 75,82,83\nMvaa 36,144,228\nRa 128,2,16384\nMvaa 36,148,236\n"
"s_add_u32 s46, s86, s88\nEX 80,60\nMvaa 36,152,244\nRa 132,2,17408\nMvaa 36,156,252\nWL 6\nEX 81,61\nMvaa 32,156,248\nRa 136,2,18432\nAD 85,85,80\nMvaa 32,152,240\nD1 1,16,46,0\n"
"Mvaa 32,148,232\nRa 140,2,19456\nMvaa 32,144,224\nAD 85,85,81\nCV 78,80,81\nCvaa 40,160,192,12\nRa 144,2,20480\nMvaa 40,164,200\ns_mov_b32 s87, s95\nEX 82,62\nMvaa 40,168,208\n"
"Ra 148,2,21504\nMvaa 40,172,216\nWL 6\nEX 83,63\nCvaa 44,172,220,16\nRa 152,2,22528\nAD 85,85,82\nMvaa 44,168,212\nD1 1,16,46,1024\nMvaa 44,164,204\nRa 156,2,23552\n"
"Mvaa 44,160,196\nAD 85,85,83\nCV 79,82,83\nMvaa 44,176,228\nRa 160,2,24576\nv_max_f32 v90, v84, v85\nMvaa 44,180,236\ns_add_u32 s88, s88, 32768\nEX 80,32\nMvaa 44,184,244\n"
"Ra 164,2,25600\nv_cmp_lt_f32 vcc, s63, v90\nMvaa 44,188,252\nWL 8\nEX 81,33\nMvaa 40,188,248\nRa 168,2,26624\ns_cbranch_vccnz .T75\n.T76:\nMvaa 40,184,240\nD1 1,16,46,2048\n"
"Mvaa 40,180,232\nRa 172,2,27648\nAD 86,86,84\nMvaa 40,176,224\nAD 84,80,81\nCV 64,80,81\nMvav 128,128,72\nRa 176,2,28672\nMvav 132,128,76\nEX 82,34\nMvav 140,132,76\n"
"Ra 180,2,29696\nMvav 136,132,72\ns_waitcnt vmcnt(12) lgkmcnt(8)\nEX 83,35\nMvav 144,136,72\nRa 184,2,30720\nAD 84,84,82\nMvav 148,136,76\nD1 1,16,46,3072\n"
"s_add_u32 m0, s89, 49152\nMvav 156,140,76\nRa 188,2,31744\ns_barrier\nMvav 152,140,72\nAD 84,84,83\nCV 65,82,83\nMvav 160,144,72\nRa 128,24,32768\nMvav 164,144,76\n"
"s_add_u32 s46, s87, s88\nEX 80,36\nMvav 172,148,76\nRa 132,24,33792\nMvav 168,148,72\nWL 8\nEX 81,37\nMvav 176,152,72\nRa 136,24,34816\nAD 87,87,85\nMvav 180,152,76\nD1 1,16,46,0\n"
"Mvav 188,156,76\nRa 140,24,35840\nMvav 184,156,72\nAD 85,80,81\nCV 68,80,81\nMvav 192,160,72\nRa 144,26,32768\n"
"buffer_load_dwordx4 v[100:103], v108, s[16:19], s65 offen offset:0 sc0 sc1\nbuffer_load_dwordx4 v[104:107], v108, s[16:19], s65 offen offset:256 sc0 sc1\nMvav 196,160,76\n"
"EX 82,38\nMvav 204,164,76\nRa 148,26,33792\nMvav 200,164,72\nWL 8\nEX 83,39\nMvav 208,168,72\nRa 152,26,34816\nAD 85,85,82\nMvav 212,168,76\nD1 1,16,46,1024\nMvav 220,172,76\n"
"Ra 156,26,35840\nMvav 216,172,72\nAD 85,85,83\nCV 69,82,83\nMvav 224,176,72\nRa 160,24,40960\nMvav 228,176,76\ns_xor_b32 s89, s89, 0x10000\nEX 80,40\nMvav 236,180,76\n"
"Ra 164,24,41984\nMvav 232,180,72\nWL 6\nEX 81,41\nMvav 240,184,72\nRa 168,24,43008\nAD 84,84,80\nMvav 244,184,76\nD1 1,16,46,2048\nMvav 252,188,76\nRa 172,24,44032\n"
"Mvav 248,188,72\nAD 84,84,81\nCV 66,80,81\nCvaa 48,128,192,4\nRa 176,26,40960\nMvaa 48,132,200\nEX 82,42\nMvaa 48,136,208\nRa 180,26,41984\nMvaa 48,140,216\nWL 6\nEX 83,43\n"
"Cvaa 52,140,220,8\nRa 184,26,43008\nAD 84,84,82\nMvaa 52,136,212\nD1 1,16,46,3072\ns_add_u32 m0, s89, 0\nMvaa 52,132,204\nRa 188,26,44032\nMvaa 52,128,196\nAD 84,84,83\n"
"CV 67,82,83\nMvaa 52,144,228\nRa 128,2,49152\nWV 3\nv_xor_b32 v109, s15, v102\nv_xor_b32 v110, s15, v106\nv_or_b32 v109, v109, v110\nv_cmp_ne_u32 vcc, 0, v109\ns_nop 3\n"
"s_cmp_lg_u64 vcc, 0\ns_cselect_b32 s67, 1, 0\ns_cselect_b32 s6, 0x40000000, s66\nMvaa 52,148,236\ns_add_u32 s46, s86, s88\nEX 80,44\ns_add_u32 s5, s6, 0\nMvaa 52,152,244\n"
"Ra 132,2,50176\nv_xor_b32 v24, 0x10000, v24\nMvaa 52,156,252\nWL 6\nEX 81,45\nbuffer_load_dwordx4 a[0:3], v1, s[16:19], s5 offen offset:0 sc1\nMvaa 48,156,248\nRa 136,2,51200\n"
"AD 85,85,80\nMvaa 48,152,240\nD1 1,16,46,0\nMvaa 48,148,232\nRa 140,2,52224\nbuffer_load_dwordx4 a[4:7], v1, s[16:19], s5 offen offset:1024 sc1\nMvaa 48,144,224\nAD 85,85,81\n"
"CV 70,80,81\nCvaa 56,160,192,12\nRa 144,2,53248\nMvaa 56,164,200\nEX 82,46\nbuffer_load_dwordx4 a[8:11], v1, s[16:19], s5 offen offset:2048 sc1\nMvaa 56,168,208\nRa 148,2,54272\n"
"v_xor_b32 v26, 0x10000, v26\nMvaa 56,172,216\nWL 6\nEX 83,47\nCvaa 60,172,220,16\nRa 152,2,55296\nAD 85,85,82\n"
"buffer_load_dwordx4 a[12:15], v1, s[16:19], s5 offen offset:3072 sc1\nMvaa 60,168,212\nD1 1,16,46,1024\ns_add_u32 s5, s6, 4096\nMvaa 60,164,204\nRa 156,2,56320\nMvaa 60,160,196\n"
"AD 85,85,83\nCV 71,82,83\nbuffer_load_dwordx4 a[16:19], v1, s[16:19], s5 offen offset:0 sc1\nMvaa 60,176,228\nRa 160,2,57344\nv_max_f32 v90, v84, v85\nMvaa 60,180,236\n"
"s_add_u32 s88, s88, 32768\nEX 80,48\nMvaa 60,184,244\nRa 164,2,58368\nv_cmp_lt_f32 vcc, s63, v90\nMvaa 60,188,252\nWL 8\nEX 81,49\nMvaa 56,188,248\nRa 168,2,59392\n"
"s_cbranch_vccnz .T77\n.T78:\nbuffer_load_dwordx4 a[20:23], v1, s[16:19], s5 offen offset:1024 sc1\nMvaa 56,184,240\nD1 1,16,46,2048\nMvaa 56,180,232\nRa 172,2,60416\nAD 86,86,84\n"
"Mvaa 56,176,224\nAD 84,80,81\nCV 72,80,81\nbuffer_load_dwordx4 a[24:27], v1, s[16:19], s5 offen offset:2048 sc1\nMvav 128,128,64\nRa 176,2,61440\nMvav 132,128,68\nEX 82,50\n"
"Mvav 140,132,68\nRa 180,2,62464\nbuffer_load_dwordx4 a[28:31], v1, s[16:19], s5 offen offset:3072 sc1\nMvav 136,132,64\nWL 8\nEX 83,51\ns_add_u32 s5, s6, 16384\nMvav 144,136,64\n"
"Ra 184,2,63488\nAD 84,84,82\nMvav 148,136,68\nD1 1,16,46,3072\ns_add_u32 m0, s89, 16384\nbuffer_load_dwordx4 a[32:35], v1, s[16:19], s5 offen offset:0 sc1\nMvav 156,140,68\n"
"Ra 188,2,64512\ns_barrier\nMvav 152,140,64\nAD 84,84,83\nCV 73,82,83\nMvav 160,144,64\nRa 128,24,0\nbuffer_load_dwordx4 a[36:39], v1, s[16:19], s5 offen offset:1024 sc1\n"
"Mvav 164,144,68\ns_add_u32 s46, s87, s88\nEX 80,52\nMvav 172,148,68\nRa 132,24,1024\nv_xor_b32 v2, 0x10000, v2\nMvav 168,148,64\nWL 8\nEX 81,53\n"
"buffer_load_dwordx4 a[40:43], v1, s[16:19], s5 offen offset:2048 sc1\nMvav 176,152,64\nRa 136,24,2048\nAD 87,87,85\nMvav 180,152,68\nD1 1,16,46,0\nMvav 188,156,68\nRa 140,24,3072\n"
"buffer_load_dwordx4 a[44:47], v1, s[16:19], s5 offen offset:3072 sc1\nMvav 184,156,64\nAD 85,80,81\nCV 76,80,81\ns_add_u32 s5, s6, 20480\nMvav 192,160,64\nRa 144,26,0\n"
"Mvav 196,160,68\nEX 82,54\nbuffer_load_dwordx4 a[48:51], v1, s[16:19], s5 offen offset:0 sc1\nMvav 204,164,68\nRa 148,26,1024\nMvav 200,164,64\nWL 8\nEX 83,55\nMvav 208,168,64\n"
"Ra 152,26,2048\nAD 85,85,82\nbuffer_load_dwordx4 a[52:55], v1, s[16:19], s5 offen offset:1024 sc1\nMvav 212,168,68\nD1 1,16,46,1024\nMvav 220,172,68\nRa 156,26,3072\n"
"Mvav 216,172,64\nAD 85,85,83\nCV 77,82,83\nbuffer_load_dwordx4 a[56:59], v1, s[16:19], s5 offen offset:2048 sc1\nMvav 224,176,64\nRa 160,24,8192\nMvav 228,176,68\nEX 80,56\n"
"Mvav 236,180,68\nRa 164,24,9216\nbuffer_load_dwordx4 a[60:63], v1, s[16:19], s5 offen offset:3072 sc1\nMvav 232,180,64\nWL 10\nEX 81,57\nMvav 240,184,64\nRa 168,24,10240\n"
"AD 84,84,80\nMvav 244,184,68\nD1 1,16,46,2048\nMvav 252,188,68\nRa 172,24,11264\nMvav 248,188,64\nAD 84,84,81\nCV 74,80,81\nWV 20\ns_cmp_lg_u32 s67, 0\ns_cbranch_scc0 .E86\n"
"v_xor_b32 v109, s15, v102\nv_xor_b32 v110, s15, v106\nv_or_b32 v109, v109, v110\nv_cmp_ne_u32 vcc, 0, v109\ns_nop 0\ns_cbranch_vccnz .S83\n.S84:\n.W549:\ns_cbranch_execz .WB21\n"
"s_add_u32 s5, s66, 0\nbuffer_load_dwordx4 a[0:3], v1, s[16:19], s5 offen offset:0 sc1\nbuffer_load_dwordx4 a[4:7], v1, s[16:19], s5 offen offset:1024 sc1\n"
"buffer_load_dwordx4 a[8:11], v1, s[16:19], s5 offen offset:2048 sc1\nbuffer_load_dwordx4 a[12:15], v1, s[16:19], s5 offen offset:3072 sc1\ns_add_u32 s5, s66, 4096\n"
"buffer_load_dwordx4 a[16:19], v1, s[16:19], s5 offen offset:0 sc1\nbuffer_load_dwordx4 a[20:23], v1, s[16:19], s5 offen offset:1024 sc1\n"
"buffer_load_dwordx4 a[24:27], v1, s[16:19], s5 offen offset:2048 sc1\nbuffer_load_dwordx4 a[28:31], v1, s[16:19], s5 offen offset:3072 sc1\ns_add_u32 s5, s66, 16384\n"
"buffer_load_dwordx4 a[32:35], v1, s[16:19], s5 offen offset:0 sc1\nbuffer_load_dwordx4 a[36:39], v1, s[16:19], s5 offen offset:1024 sc1\n"
"buffer_load_dwordx4 a[40:43], v1, s[16:19], s5 offen offset:2048 sc1\nbuffer_load_dwordx4 a[44:47], v1, s[16:19], s5 offen offset:3072 sc1\ns_add_u32 s5, s66, 20480\n"
"buffer_load_dwordx4 a[48:51], v1, s[16:19], s5 offen offset:0 sc1\nbuffer_load_dwordx4 a[52:55], v1, s[16:19], s5 offen offset:1024 sc1\n"
"buffer_load_dwordx4 a[56:59], v1, s[16:19], s5 offen offset:2048 sc1\nbuffer_load_dwordx4 a[60:63], v1, s[16:19], s5 offen offset:3072 sc1\nWV 0\ns_mov_b32 s67, 0\n.E86:\n"
"v_cmp_lt_f32 vcc, 0, v101\nSU 20,100,88\nv_mul_f32 v101, 0x3e800000, v101\nv_cndmask_b32 v20, 0, v20, vcc\nv_cmp_lt_f32 vcc, 0, v105\nSU 21,104,89\n"
"v_mul_f32 v105, 0x3e800000, v105\nv_cndmask_b32 v21, 0, v21, vcc\nv_or_b32 v91, v20, v21\nv_cmp_ne_u32 vcc, 0, v91\ns_nop 0\ns_cbranch_vccnz .D81\nAD 86,86,101\nAD 87,87,105\n"
".D82:\nMvva 128,114,0\nMvva 136,112,0\nEX 82,58\nMvva 140,112,4\nMvva 132,114,4\nWV 15\nEX 83,59\nMvva 144,114,8\nAD 84,84,82\nMvva 152,112,8\nD1 1,16,46,3072\nMvva 156,112,12\n"
"s_add_u32 m0, s89, 32768\ns_add_u32 s46, s86, s88\nMvva 148,114,12\nAD 84,84,83\nCV 75,82,83\nMvva 160,114,16\nRa 128,2,16384\nv_xor_b32 v3, 0x10000, v2\n"
"v_add_u32 v0, 0x20000, v1\nMvva 168,112,16\nEX 80,60\nMvva 172,112,20\nRa 132,2,17408\ns_sub_u32 s94, s49, 1703936\nMvva 164,114,20\nWV 10\nEX 81,61\nMvva 176,114,24\n"
"Ra 136,2,18432\nAD 85,85,80\ns_xor_b32 s92, s94, 0x202000\nMvva 184,112,24\nD1 1,16,46,0\ns_lshl_b32 s89, s11, 12\nMvva 188,112,28\nRa 140,2,19456\ns_lshl1_add_u32 s2, s12, 0\n"
"Mvva 180,114,28\nAD 85,85,81\nCV 78,80,81\nMvva 192,114,32\nRa 144,2,20480\ns_lshl3_add_u32 s2, s2, s13\ns_lshl4_add_u32 s2, s2, 0xc048200\nMvva 200,112,32\nEX 82,62\n"
"Mvva 204,112,36\nRa 148,2,21504\ns_and_b32 s3, s72, 0x2000\nMvva 196,114,36\nWV 6\nEX 83,63\nMvva 208,114,40\nRa 152,2,22528\nAD 85,85,82\nMvva 216,112,40\nD1 1,16,46,1024\n"
"s_cmp_eq_u32 s11, 1\ns_cselect_b32 s54, s2, s54\ns_cselect_b32 s3, s3, 0\nMvva 220,112,44\nRa 156,2,23552\nv_xor_b32 v25, s3, v25\nMvva 212,114,44\nAD 85,85,83\nCV 79,82,83\n"
"Mvva 224,114,48\nRa 160,2,24576\nv_max_f32 v90, v84, v85\nMvva 232,112,48\nv_cmp_lt_f32 vcc, s63, v90\nMvva 236,112,52\nRa 164,2,25600\nMvva 228,114,52\n"
"s_waitcnt vmcnt(4) lgkmcnt(8)\nMvva 240,114,56\nRa 168,2,26624\nMvva 248,112,56\nD1 1,16,46,2048\nMvva 252,112,60\nRa 172,2,27648\nMvva 244,114,60\ns_cbranch_vccnz .T87\n.T88:\n"
"AD 86,86,84\nAD 87,87,85\nMvav 128,128,72\nRa 176,2,28672\nv_mov_b32 v84, v86\nv_mov_b32 v85, v87\nMvav 132,128,76\nv_permlane32_swap_b32 v86, v84\nv_permlane32_swap_b32 v87, v85\n"
"Mvav 140,132,76\nRa 180,2,29696\nAD 86,86,84\nMvav 136,132,72\nWL 8\nAD 87,87,85\nMvav 144,136,72\nRa 184,2,30720\nv_mov_b32 v84, v86\nv_mov_b32 v85, v87\nMvav 148,136,76\n"
"D1 1,16,46,3072\nv_permlane16_swap_b32 v86, v84\nMvav 156,140,76\nRa 188,2,31744\nv_permlane16_swap_b32 v87, v85\nMvav 152,140,72\nAD 86,86,84\nAD 87,87,85\nMvav 160,144,72\n"
"Ra 128,2,0\nv_rcp_f32 v86, v86\nMvav 164,144,76\nv_rcp_f32 v87, v87\nMvav 172,148,76\nRa 132,2,1024\nMU 128,128,86\nMU 129,129,86\nMvav 168,148,72\nWL 8\n"
"s_add_u32 m0, s89, 131072\ns_add_u32 s46, s96, 1212416\nCV 96,128,129\nMvav 176,152,72\nRa 136,2,2048\nMU 130,130,86\nMU 131,131,86\nMvav 180,152,76\nD1 1,16,46,0\nCV 97,130,131\n"
"MU 132,132,87\nMvav 188,156,76\nRa 140,2,3072\nMU 133,133,87\nCV 100,132,133\nMvav 184,156,72\nMU 134,134,87\nMU 135,135,87\nCV 101,134,135\nMvav 192,160,72\nRa 144,2,4096\n"
"MU 136,136,86\nMU 137,137,86\nCV 98,136,137\nMvav 196,160,76\nMU 138,138,86\nMU 139,139,86\nCV 99,138,139\nMvav 204,164,76\nRa 148,2,5120\nMU 140,140,87\nMU 141,141,87\n"
"Mvav 200,164,72\nWL 8\nCV 102,140,141\nMU 142,142,87\nMvav 208,168,72\nRa 152,2,6144\nMU 143,143,87\nCV 103,142,143\nMvav 212,168,76\nD1 1,16,46,1024\nMU 144,144,86\n"
"MU 145,145,86\nMvav 220,172,76\nRa 156,2,7168\nCV 104,144,145\nMU 146,146,86\nMU 147,147,86\nMvav 216,172,72\nCV 105,146,147\nMU 148,148,87\nMU 149,149,87\nMvav 224,176,72\n"
"Ra 160,2,8192\nCV 108,148,149\nMU 150,150,87\nMvav 228,176,76\nMU 151,151,87\nCV 109,150,151\nMU 152,152,86\nMvav 236,180,76\nRa 164,2,9216\nMU 153,153,86\nCV 106,152,153\n"
"Mvav 232,180,72\nWL 8\nMU 154,154,86\nMU 155,155,86\nCV 107,154,155\nMvav 240,184,72\nRa 168,2,10240\nMU 156,156,87\nMU 157,157,87\nMvav 244,184,76\nD1 1,16,46,2048\n"
"CV 110,156,157\nMU 158,158,87\nMvav 252,188,76\nRa 172,2,11264\nMU 159,159,87\nCV 111,158,159\nMvav 248,188,72\nMU 160,160,86\nMU 161,161,86\nCV 112,160,161\nZvav 128,128,96\n"
"Ra 176,2,12288\nMU 162,162,86\nMU 163,163,86\nMvav 128,132,104\nCV 113,162,163\nMU 164,164,87\nZvav 132,132,108\nRa 180,2,13312\nMU 165,165,87\nCV 116,164,165\nMvav 132,128,100\n"
"WL 8\nMU 166,166,87\nMU 167,167,87\nZvav 140,136,100\nRa 184,2,14336\nCV 117,166,167\nMU 168,168,86\nMvav 140,140,108\nD1 1,16,46,3072\nMU 169,169,86\nCV 114,168,169\n"
"Zvav 136,140,104\nRa 188,2,15360\ns_barrier\nMU 170,170,86\nMvav 136,136,96\nMU 171,171,86\nCV 115,170,171\nMU 172,172,87\nZvav 144,144,96\nRa 128,2,32768\nMU 173,173,87\n"
"CV 118,172,173\nMvav 144,148,104\nMU 174,174,87\nMU 175,175,87\nbuffer_store_dword v25, off, s[16:19], s54 offset:512 sc0 sc1\nZvav 148,148,108\nRa 132,2,33792\nCV 119,174,175\n"
"MU 176,176,86\nMvav 148,144,100\nWL 8\ns_add_u32 m0, s89, 114688\ns_add_u32 s46, s96, 1179648\nZvav 156,152,100\nRa 136,2,34816\nMU 177,177,86\nCV 120,176,177\nMvav 156,156,108\n"
"D1 1,16,46,0\nMU 178,178,86\nMU 179,179,86\nZvav 152,156,104\nRa 140,2,35840\nCV 121,178,179\nMU 180,180,87\nMvav 152,152,96\nMU 181,181,87\nCV 124,180,181\nMU 182,182,87\n"
"Zvav 64,160,96\nRa 144,2,36864\nMU 183,183,87\nCV 125,182,183\nMvav 64,164,104\nMU 184,184,86\nMU 185,185,86\nZvav 68,164,108\nRa 148,2,37888\nCV 122,184,185\nMU 186,186,86\n"
"Mvav 68,160,100\nWL 8\nMU 187,187,86\nCV 123,186,187\nZvav 76,168,100\nRa 152,2,38912\nMU 188,188,87\nMU 189,189,87\nMvav 76,172,108\nD1 1,16,46,1024\nCV 126,188,189\n"
"MU 190,190,87\nZvav 72,172,104\nRa 156,2,39936\nMU 191,191,87\nCV 127,190,191\nMvav 72,168,96\nMU 192,192,86\nMU 193,193,86\nCV 32,192,193\nMvav 128,176,112\nRa 160,2,40960\n"
"MU 194,194,86\nMU 195,195,86\nMvav 128,180,120\nCV 33,194,195\nMU 196,196,87\nMU 197,197,87\nMvav 132,180,124\nRa 164,2,41984\nCV 36,196,197\nMU 198,198,87\nMvav 132,176,116\n"
"WL 8\nMU 199,199,87\nMvav 140,184,116\nRa 168,2,43008\nCV 37,198,199\nMU 200,200,86\nMvav 140,188,124\nD1 1,16,46,2048\nMU 201,201,86\nCV 34,200,201\nMvav 136,188,120\n"
"Ra 172,2,44032\nMU 202,202,86\nMU 203,203,86\nMvav 136,184,112\nCV 35,202,203\nMU 204,204,87\nMU 205,205,87\nMvav 144,128,112\nRa 176,2,45056\nCV 38,204,205\nMU 206,206,87\n"
"Mvav 144,132,120\nMU 207,207,87\nCV 39,206,207\nMvav 148,132,124\nRa 180,2,46080\nMU 208,208,86\nMU 209,209,86\nMvav 148,128,116\nWL 8\nCV 40,208,209\nMU 210,210,86\n"
"Mvav 156,136,116\nRa 184,2,47104\nMU 211,211,86\nCV 41,210,211\nMvav 156,140,124\nD1 1,16,46,3072\nMU 212,212,87\nMU 213,213,87\nMvav 152,140,120\nRa 188,2,48128\nCV 44,212,213\n"
"Mvav 152,136,112\nMU 214,214,87\nMU 215,215,87\nCV 45,214,215\nMvav 64,144,112\nRa 128,2,49152\nMU 216,216,86\nMU 217,217,86\nMvav 64,148,120\nCV 42,216,217\nMU 218,218,86\n"
"MU 219,219,86\nMvav 68,148,124\nRa 132,2,50176\nCV 43,218,219\nMU 220,220,87\nMvav 68,144,116\nWL 8\ns_add_u32 m0, s89, 147456\ns_add_u32 s46, s96, 1245184\nMvav 76,152,116\n"
"Ra 136,2,51200\nMU 221,221,87\nCV 46,220,221\nMvav 76,156,124\nD1 1,16,46,0\nMU 222,222,87\nMU 223,223,87\nMvav 72,156,120\nRa 140,2,52224\nCV 47,222,223\nMvav 72,152,112\n"
"MU 224,224,86\nMU 225,225,86\nCV 48,224,225\nMvav 128,160,32\nRa 144,2,53248\nMU 226,226,86\nMU 227,227,86\nMvav 128,164,40\nCV 49,226,227\nMU 228,228,87\nMvav 132,164,44\n"
"Ra 148,2,54272\nMU 229,229,87\nCV 52,228,229\nMvav 132,160,36\nWL 8\nMU 230,230,87\nMU 231,231,87\nMvav 140,168,36\nRa 152,2,55296\nCV 53,230,231\nMU 232,232,86\nMvav 140,172,44\n"
"D1 1,16,46,1024\nMU 233,233,86\nCV 50,232,233\nMvav 136,172,40\nRa 156,2,56320\nMU 234,234,86\nMvav 136,168,32\nMU 235,235,86\nCV 51,234,235\nMU 236,236,87\nMvav 144,176,32\n"
"Ra 160,2,57344\nMU 237,237,87\nCV 54,236,237\nMvav 144,180,40\nMU 238,238,87\nMU 239,239,87\nCV 55,238,239\nMvav 148,180,44\nRa 164,2,58368\nMU 240,240,86\nMU 241,241,86\n"
"Mvav 148,176,36\nWL 8\nCV 56,240,241\nMvav 156,184,36\nRa 168,2,59392\nMU 242,242,86\nMU 243,243,86\nMvav 156,188,44\nD1 1,16,46,2048\nCV 57,242,243\nMU 244,244,87\n"
"Mvav 152,188,40\nRa 172,2,60416\nMU 245,245,87\nCV 60,244,245\nMvav 152,184,32\nMU 246,246,87\nMU 247,247,87\nCV 61,246,247\nMvav 64,128,32\nRa 176,2,61440\nMU 248,248,86\n"
"MU 249,249,86\nMvav 64,132,40\nCV 58,248,249\nMU 250,250,86\nMvav 68,132,44\nRa 180,2,62464\nMU 251,251,86\nCV 59,250,251\nMvav 68,128,36\ns_waitcnt vmcnt(16) lgkmcnt(8)\n"
"MU 252,252,87\nMU 253,253,87\nMvav 76,136,36\nRa 184,2,63488\nCV 62,252,253\nMU 254,254,87\nMvav 76,140,44\nD1 1,16,46,3072\nMU 255,255,87\nCV 63,254,255\nMvav 72,140,40\n"
"Ra 188,2,64512\ns_barrier\nMvav 72,136,32\nMvav 128,144,48\nRa 128,3,0\nMvav 128,148,56\nMvav 132,148,60\nRa 132,3,1024\nMvav 132,144,52\nWL 8\nMvav 140,152,52\nRa 136,3,2048\n"
"Mvav 140,156,60\nMvav 136,156,56\nRa 140,3,3072\nMvav 136,152,48\nMvav 144,160,48\nRa 144,3,4096\nMvav 144,164,56\nMvav 148,164,60\nRa 148,3,5120\nCV 132,132,133\nMvav 148,160,52\n"
"WL 8\nCV 133,134,135\nMvav 156,168,52\nRa 152,3,6144\nCV 134,140,141\nMvav 156,172,60\nCV 135,142,143\ns_add_u32 s5, s94, 0\nMvav 152,172,56\nRa 156,3,7168\nSv 132,1,16,5,1024\n"
"Mvav 152,168,48\nCV 128,128,129\nCV 129,130,131\nMvav 64,176,48\nRa 160,3,8192\nCV 130,136,137\nMvav 64,180,56\nbuffer_load_dword v27, off, s[16:19], s60 offset:512 sc0 sc1\n"
"CV 131,138,139\nMvav 68,180,60\nRa 164,3,9216\nSv 128,1,16,5,0\nMvav 68,176,52\nWL 6\nCV 148,148,149\nMvav 76,184,52\nRa 168,3,10240\nCV 149,150,151\nMvav 76,188,60\n"
"CV 150,156,157\nCV 151,158,159\nMvav 72,188,56\nRa 172,3,11264\ns_add_u32 s5, s94, 131072\nMvav 72,184,48\nSv 148,1,16,5,1024\nCV 144,144,145\nZvav 160,128,96\nRa 176,3,12288\n"
"CV 145,146,147\nMvav 160,132,104\nCV 146,152,153\nCV 147,154,155\nMvav 160,136,112\nRa 180,3,13312\nSv 144,1,16,5,0\nMvav 160,140,120\nWL 6\nCV 68,68,69\nZvav 164,140,124\n"
"Ra 184,3,14336\nCV 69,70,71\nMvav 164,136,116\nCV 70,76,77\nCV 71,78,79\nMvav 164,132,108\nRa 188,3,15360\ns_add_u32 s5, s94, 262144\nMvav 164,128,100\nSv 68,1,16,5,1024\n"
"CV 64,64,65\nMvav 164,144,36\nRa 128,3,16384\nCV 65,66,67\nMvav 164,148,44\nCV 66,72,73\nCV 67,74,75\nMvav 164,152,52\nRa 132,3,17408\nSv 64,1,16,5,0\nMvav 164,156,60\nWL 6\n"
"Mvav 160,156,56\nRa 136,3,18432\n.W550:\ns_cbranch_execz .WB21\nMvav 160,152,48\nMvav 160,148,40\nRa 140,3,19456\nMvav 160,144,32\nZvav 168,160,96\nRa 144,3,20480\n"
"Mvav 168,164,104\nMvav 168,168,112\nRa 148,3,21504\nMvav 168,172,120\nWL 6\nZvav 172,172,124\nRa 152,3,22528\nMvav 172,168,116\nMvav 172,164,108\nRa 156,3,23552\nMvav 172,160,100\n"
"Mvav 172,176,36\nRa 160,3,24576\nMvav 172,180,44\nMvav 172,184,52\nRa 164,3,25600\nMvav 172,188,60\nWL 6\nMvav 168,188,56\nRa 168,3,26624\nMvav 168,184,48\nMvav 168,180,40\n"
"Ra 172,3,27648\nMvav 168,176,32\nZvav 176,128,96\nRa 176,3,28672\nCV 164,164,165\nMvav 176,132,104\nCV 165,166,167\nCV 166,172,173\nMvav 176,136,112\nRa 180,3,29696\n"
"CV 167,174,175\nMvav 176,140,120\ns_waitcnt vmcnt(11) lgkmcnt(6)\ns_add_u32 s5, s94, 393216\nZvav 180,140,124\nRa 184,3,30720\nSv 164,1,16,5,1024\nMvav 180,136,116\n"
"CV 160,160,161\nCV 161,162,163\nMvav 180,132,108\nRa 188,3,31744\ns_barrier\nMvav 180,128,100\nCV 162,168,169\nCV 163,170,171\nMvav 180,144,36\nRa 128,3,32768\nSv 160,1,16,5,0\n"
"Mvav 180,148,44\nMvav 180,152,52\nRa 132,3,33792\nMvav 180,156,60\nWL 6\ns_add_u32 m0, s89, 16384\ns_add_u32 s46, s96, 1277952\nMvav 176,156,56\nRa 136,3,34816\nMvav 176,152,48\n"
"D1 1,16,46,0\nMvav 176,148,40\nRa 140,3,35840\nMvav 176,144,32\nZvav 184,160,96\nRa 144,3,36864\nMvav 184,164,104\nMvav 184,168,112\nRa 148,3,37888\nMvav 184,172,120\nWL 6\n"
"Zvav 188,172,124\nRa 152,3,38912\nMvav 188,168,116\nD1 1,16,46,1024\nMvav 188,164,108\nRa 156,3,39936\nMvav 188,160,100\nMvav 188,176,36\nRa 160,3,40960\nWV 9\n"
"v_cmp_ne_u32 vcc, s15, v27\ns_nop 0\ns_cbranch_vccnz .S114\n.S115:\nMvav 188,180,44\ns_add_u32 s4, s92, 0\nMvav 188,184,52\nRa 164,3,41984\nMvav 188,188,60\nWL 6\n"
"buffer_load_dwordx4 v[208:211], v1, s[16:19], s4 offen offset:0 sc0 sc1\nMvav 184,188,56\nRa 168,3,43008\nMvav 184,184,48\nD1 1,16,46,2048\nMvav 184,180,40\nRa 172,3,44032\n"
"Mvav 184,176,32\nbuffer_load_dwordx4 v[212:215], v1, s[16:19], s4 offen offset:1024 sc0 sc1\nZvav 192,128,96\nRa 176,3,45056\nv_lshrrev_b32 v88, 8, v1\nMvav 192,132,104\n"
"v_lshlrev_b32 v89, 5, v88\nCV 180,180,181\nMvav 192,136,112\nRa 180,3,46080\nCV 181,182,183\nMvav 192,140,120\nWL 6\nCV 182,188,189\nZvav 196,140,124\nRa 184,3,47104\n"
"v_bfe_u32 v90, v1, 4, 4\nMvav 196,136,116\nD1 1,16,46,3072\nCV 183,190,191\nMvav 196,132,108\nRa 188,3,48128\ns_add_u32 s5, s94, 524288\nMvav 196,128,100\n"
"v_mul_u32_u24 v91, 896, v90\nSv 180,1,16,5,1024\nMvav 196,144,36\nRa 128,3,49152\nv_lshl_add_u32 v91, v88, 4, v91\nMvav 196,148,44\nv_add_u32 v90, s21, v90\nCV 176,176,177\n"
"Mvav 196,152,52\nRa 132,3,50176\nCV 177,178,179\nMvav 196,156,60\nWL 6\ns_add_u32 m0, s89, 0\ns_add_u32 s46, s96, 1310720\nMvav 192,156,56\nRa 136,3,51200\n"
"v_lshlrev_b32 v88, 3, v90\nMvav 192,152,48\nD1 1,16,46,0\nCV 178,184,185\nMvav 192,148,40\nRa 140,3,52224\nCV 179,186,187\nMvav 192,144,32\nv_add_u32 v94, 64, v90\n"
"Sv 176,1,16,5,0\nZvav 200,160,96\nRa 144,3,53248\ns_mov_b32 s63, 0xc040000\nMvav 200,164,104\nv_lshrrev_b32 v92, 8, v1\nMvav 200,168,112\nRa 148,3,54272\nMvav 200,172,120\nWL 6\n"
"Zvav 204,172,124\nRa 152,3,55296\nv_bfe_u32 v93, v1, 6, 2\nMvav 204,168,116\nD1 1,16,46,1024\nv_cmp_eq_u32 vcc, v92, v93\nv_and_b32 v92, 16, v1\nv_mov_b32 v93, 0x3f80\n"
"v_cndmask_b32 v93, 0, v93, vcc\nMvav 204,164,108\nRa 156,3,56320\nMvav 204,160,100\nv_lshlrev_b32 v93, v92, v93\nMvav 204,176,36\nRa 160,3,57344\nv_and_b32 v92, 32, v1\n"
"Mvav 204,180,44\ns_add_u32 s4, s92, 131072\nv_cmp_eq_u32 vcc, 0, v92\nv_mov_b32 v4, 0\nv_mov_b32 v5, 0\nv_cndmask_b32 v6, 0, v93, vcc\nMvav 204,184,52\nRa 164,3,58368\n"
"Mvav 204,188,60\nWL 6\nbuffer_load_dwordx4 v[216:219], v1, s[16:19], s4 offen offset:0 sc0 sc1\nMvav 200,188,56\nRa 168,3,59392\nv_sub_u32 v7, v93, v6\nMvav 200,184,48\n"
"D1 1,16,46,2048\nMvav 200,180,40\nRa 172,3,60416\nMvav 200,176,32\nbuffer_load_dwordx4 v[220:223], v1, s[16:19], s4 offen offset:1024 sc0 sc1\nZvav 160,128,96\nRa 176,3,61440\n"
"v_mov_b32 v8, 0\nMvav 160,132,104\nv_mov_b32 v9, 0\nCV 196,196,197\nMvav 160,136,112\nRa 180,3,62464\nCV 197,198,199\nMvav 160,140,120\nWL 6\nCV 198,204,205\nZvav 164,140,124\n"
"Ra 184,3,63488\nCV 199,206,207\nMvav 164,136,116\nD1 1,16,46,3072\ns_add_u32 s5, s94, 655360\nMvav 164,132,108\nRa 188,3,64512\ns_barrier\nMvav 164,128,100\nSv 196,1,16,5,1024\n"
"CV 192,192,193\nMvav 164,144,36\nRa 128,0,0\nCV 193,194,195\nMvav 164,148,44\nCV 194,200,201\nCV 195,202,203\nMvav 164,152,52\nRa 132,0,1024\nSv 192,1,16,5,0\nMvav 164,156,60\n"
"WL 6\ns_add_u32 m0, s89, 32768\ns_add_u32 s46, s96, 1343488\nMvav 160,156,56\nRa 136,0,2048\nMvav 160,152,48\nD1 1,16,46,0\nMvav 160,148,40\nRa 140,0,3072\nMvav 160,144,32\n"
"Zvav 168,160,96\nRa 144,0,4096\nWV 12\nv_max_u32 v21, v211, v215\nv_cmp_eq_u32 vcc, -1, v21\ns_nop 0\ns_cbranch_vccnz .S90\n.S91:\nMvav 168,164,104\nv_lshlrev_b32 v176, 16, v208\n"
"Mvav 168,168,112\nRa 148,0,5120\nMvav 168,172,120\nWL 6\nv_and_b32 v177, 0xffff0000, v208\nZvav 172,172,124\nRa 152,0,6144\nMvav 172,168,116\nD1 1,16,46,1024\nMvav 172,164,108\n"
"Ra 156,0,7168\nv_lshlrev_b32 v178, 16, v209\nMvav 172,160,100\nv_and_b32 v179, 0xffff0000, v209\nMvav 172,176,36\nRa 160,0,8192\nv_lshlrev_b32 v180, 16, v212\nMvav 172,180,44\n"
"v_and_b32 v181, 0xffff0000, v212\ns_add_u32 s4, s92, 262144\nMvav 172,184,52\nRa 164,0,9216\nMvav 172,188,60\nWL 6\nv_lshlrev_b32 v182, 16, v213\n"
"buffer_load_dwordx4 v[224:227], v1, s[16:19], s4 offen offset:0 sc0 sc1\nMvav 168,188,56\nRa 168,0,10240\nMvav 168,184,48\nD1 1,16,46,2048\nMvav 168,180,40\nRa 172,0,11264\n"
"v_and_b32 v183, 0xffff0000, v213\nMvav 168,176,32\nv_lshlrev_b32 v184, 16, v210\nbuffer_load_dwordx4 v[228:231], v1, s[16:19], s4 offen offset:1024 sc0 sc1\nMvav 176,128,96\n"
"Ra 176,0,12288\nv_and_b32 v185, 0xffff0000, v210\nMvav 176,132,104\nv_lshlrev_b32 v186, 16, v211\nCV 164,164,165\nMvav 176,136,112\nRa 180,0,13312\nCV 165,166,167\n"
"Mvav 176,140,120\nWL 6\nCV 166,172,173\nMvav 180,140,124\nRa 184,0,14336\nv_and_b32 v187, 0xffff0000, v211\nMvav 180,136,116\nD1 1,16,46,3072\nCV 167,174,175\nMvav 180,132,108\n"
"Ra 188,0,15360\ns_add_u32 s5, s94, 786432\nMvav 180,128,100\nv_lshlrev_b32 v188, 16, v214\nSv 164,1,16,5,1024\nMvav 180,144,36\nRa 128,0,16384\nv_and_b32 v189, 0xffff0000, v214\n"
"Mvav 180,148,44\nv_lshlrev_b32 v190, 16, v215\nCV 160,160,161\nMvav 180,152,52\nRa 132,0,17408\nCV 161,162,163\nMvav 180,156,60\nWL 6\ns_add_u32 m0, s89, 49152\n"
"s_add_u32 s46, s96, 1376256\nMvav 176,156,56\nRa 136,0,18432\nv_and_b32 v191, 0xffff0000, v215\nMvav 176,152,48\nD1 1,16,46,0\nCV 162,168,169\nMvav 176,148,40\nRa 140,0,19456\n"
"CV 163,170,171\nMvav 176,144,32\nSv 160,1,16,5,0\nMvav 184,160,96\nRa 144,0,20480\nWV 12\nv_max_u32 v21, v219, v223\nv_cmp_eq_u32 vcc, -1, v21\ns_nop 0\ns_cbranch_vccnz .S93\n"
".S94:\nMvav 184,164,104\nv_lshlrev_b32 v192, 16, v216\nMvav 184,168,112\nRa 148,0,21504\nMvav 184,172,120\nWL 6\nv_and_b32 v193, 0xffff0000, v216\nMvav 188,172,124\n"
"Ra 152,0,22528\nMvav 188,168,116\nD1 1,16,46,1024\nMvav 188,164,108\nRa 156,0,23552\nv_lshlrev_b32 v194, 16, v217\nMvav 188,160,100\nv_and_b32 v195, 0xffff0000, v217\n"
"Mvav 188,176,36\nRa 160,0,24576\nv_lshlrev_b32 v196, 16, v220\nMvav 188,180,44\nv_and_b32 v197, 0xffff0000, v220\ns_add_u32 s4, s92, 393216\nMvav 188,184,52\nRa 164,0,25600\n"
"Mvav 188,188,60\nWL 6\nv_lshlrev_b32 v198, 16, v221\nbuffer_load_dwordx4 v[208:211], v1, s[16:19], s4 offen offset:0 sc0 sc1\nMvav 184,188,56\nRa 168,0,26624\nMvav 184,184,48\n"
"D1 1,16,46,2048\nMvav 184,180,40\nRa 172,0,27648\nv_and_b32 v199, 0xffff0000, v221\nMvav 184,176,32\nv_lshlrev_b32 v200, 16, v218\n"
"buffer_load_dwordx4 v[212:215], v1, s[16:19], s4 offen offset:1024 sc0 sc1\nMvav 192,128,96\nRa 176,0,28672\nv_and_b32 v201, 0xffff0000, v218\nMvav 192,132,104\n"
"v_lshlrev_b32 v202, 16, v219\nCV 180,180,181\nMvav 192,136,112\nRa 180,0,29696\nCV 181,182,183\nMvav 192,140,120\ns_waitcnt vmcnt(15) lgkmcnt(6)\nCV 182,188,189\nMvav 196,140,124\n"
"Ra 184,0,30720\nv_and_b32 v203, 0xffff0000, v219\nMvav 196,136,116\nD1 1,16,46,3072\nCV 183,190,191\nMvav 196,132,108\nRa 188,0,31744\ns_barrier\nMvav 196,128,100\n"
"v_lshlrev_b32 v204, 16, v222\ns_add_u32 s5, s94, 917504\nMvav 196,144,36\nRa 128,2,16384\nv_and_b32 v205, 0xffff0000, v222\nMvav 196,148,44\nv_lshlrev_b32 v206, 16, v223\n"
"Sv 180,1,16,5,1024\nMvav 196,152,52\nRa 132,2,17408\nCV 176,176,177\nMvav 196,156,60\nWL 6\ns_add_u32 m0, s89, 65536\ns_add_u32 s46, s96, 1409024\nMvav 192,156,56\nRa 136,2,18432\n"
"v_and_b32 v207, 0xffff0000, v223\nMvav 192,152,48\nD1 1,16,46,0\nCV 177,178,179\nMvav 192,148,40\nRa 140,2,19456\nCV 178,184,185\nMvav 192,144,32\nCV 179,186,187\nSv 176,1,16,5,0\n"
"Mvav 200,160,96\nRa 144,2,20480\nWV 12\nv_max_u32 v21, v227, v231\nv_cmp_eq_u32 vcc, -1, v21\ns_nop 0\ns_cbranch_vccnz .S96\n.S97:\nMvav 200,164,104\nv_lshlrev_b32 v160, 16, v224\n"
"Mvav 200,168,112\nRa 148,2,21504\nMvav 200,172,120\nWL 6\nv_and_b32 v161, 0xffff0000, v224\nMvav 204,172,124\nRa 152,2,22528\nMvav 204,168,116\nD1 1,16,46,1024\nMvav 204,164,108\n"
"Ra 156,2,23552\nv_lshlrev_b32 v162, 16, v225\nMvav 204,160,100\nv_and_b32 v163, 0xffff0000, v225\nMvav 204,176,36\nRa 160,2,24576\nv_lshlrev_b32 v164, 16, v228\nMvav 204,180,44\n"
"v_and_b32 v165, 0xffff0000, v228\ns_add_u32 s4, s92, 524288\nMvav 204,184,52\nRa 164,2,25600\nMvav 204,188,60\nWL 6\nv_lshlrev_b32 v166, 16, v229\n"
"buffer_load_dwordx4 v[216:219], v1, s[16:19], s4 offen offset:0 sc0 sc1\nMvav 200,188,56\nRa 168,2,26624\nMvav 200,184,48\nD1 1,16,46,2048\nMvav 200,180,40\nRa 172,2,27648\n"
"v_and_b32 v167, 0xffff0000, v229\nMvav 200,176,32\nv_lshlrev_b32 v168, 16, v226\nbuffer_load_dwordx4 v[220:223], v1, s[16:19], s4 offen offset:1024 sc0 sc1\nMvav 160,128,96\n"
"Ra 176,2,28672\nv_and_b32 v169, 0xffff0000, v226\nMvav 160,132,104\nv_lshlrev_b32 v170, 16, v227\nCV 196,196,197\nMvav 160,136,112\nRa 180,2,29696\nCV 197,198,199\n"
"Mvav 160,140,120\nWL 6\nCV 198,204,205\nMvav 164,140,124\nRa 184,2,30720\nv_and_b32 v24, 0x70, v1\nMvav 164,136,116\nD1 1,16,46,3072\nCV 199,206,207\nMvav 164,132,108\n"
"Ra 188,2,31744\nv_and_b32 v171, 0xffff0000, v227\nMvav 164,128,100\nv_lshlrev_b32 v172, 16, v230\ns_add_u32 s5, s94, 1048576\nMvav 164,144,36\nRa 128,2,0\n"
"v_lshlrev_b32 v24, 5, v24\nMvav 164,148,44\nv_and_b32 v173, 0xffff0000, v230\nSv 196,1,16,5,1024\nMvav 164,152,52\nRa 132,2,1024\nCV 192,192,193\nMvav 164,156,60\nWL 6\n"
"v_lshlrev_b32 v174, 16, v231\nMvav 160,156,56\nRa 136,2,2048\nbuffer_load_dword v27, v24, s[16:19], s41 offen sc0 sc1\nMvav 160,152,48\nv_and_b32 v175, 0xffff0000, v231\n"
"CV 193,194,195\nMvav 160,148,40\nRa 140,2,3072\nCV 194,200,201\nMvav 160,144,32\nCV 195,202,203\nSv 192,1,16,5,0\nMvav 168,160,96\nRa 144,2,4096\nWV 12\nv_max_u32 v21, v211, v215\n"
"v_cmp_eq_u32 vcc, -1, v21\ns_nop 0\ns_cbranch_vccnz .S99\n.S100:\nMvav 168,164,104\nv_lshlrev_b32 v176, 16, v208\nMvav 168,168,112\nRa 148,2,5120\nMvav 168,172,120\nWL 6\n"
"Mvav 172,172,124\nRa 152,2,6144\nv_and_b32 v177, 0xffff0000, v208\nMvav 172,168,116\nv_lshlrev_b32 v178, 16, v209\nMvav 172,164,108\nRa 156,2,7168\nMvav 172,160,100\n"
"v_and_b32 v179, 0xffff0000, v209\nMvav 172,176,36\nRa 160,2,8192\nv_lshlrev_b32 v180, 16, v212\nMvav 172,180,44\nv_and_b32 v181, 0xffff0000, v212\ns_add_u32 s4, s92, 655360\n"
"Mvav 172,184,52\nRa 164,2,9216\nMvav 172,188,60\nWL 6\nbuffer_load_dwordx4 v[224:227], v1, s[16:19], s4 offen offset:0 sc0 sc1\nMvav 168,188,56\nRa 168,2,10240\n"
"v_lshlrev_b32 v182, 16, v213\nMvav 168,184,48\nv_and_b32 v183, 0xffff0000, v213\nMvav 168,180,40\n.W551:\ns_cbranch_execz .WB21\nRa 172,2,11264\nMvav 168,176,32\n"
"v_lshlrev_b32 v184, 16, v210\nbuffer_load_dwordx4 v[228:231], v1, s[16:19], s4 offen offset:1024 sc0 sc1\nMvav 176,128,96\nRa 176,2,12288\nv_and_b32 v185, 0xffff0000, v210\n"
"Mvav 176,132,104\nv_lshlrev_b32 v186, 16, v211\nCV 164,164,165\nMvav 176,136,112\nRa 180,2,13312\nCV 165,166,167\nMvav 176,140,120\ns_waitcnt vmcnt(13) lgkmcnt(6)\nCV 166,172,173\n"
"Mvav 180,140,124\nRa 184,2,14336\nCV 167,174,175\nMvav 180,136,116\nv_and_b32 v187, 0xffff0000, v211\ns_add_u32 s5, s94, 1179648\nMvav 180,132,108\nRa 188,2,15360\ns_barrier\n"
"Mvav 180,128,100\nv_lshlrev_b32 v188, 16, v214\nSv 164,1,16,5,1024\nMvav 180,144,36\nRa 128,2,32768\nv_and_b32 v189, 0xffff0000, v214\nMvav 180,148,44\n"
"v_lshlrev_b32 v190, 16, v215\nCV 160,160,161\nMvav 180,152,52\nRa 132,2,33792\nCV 161,162,163\nMvav 180,156,60\nWL 6\nCV 162,168,169\nMvav 176,156,56\nRa 136,2,34816\n"
"CV 163,170,171\nMvav 176,152,48\nv_and_b32 v191, 0xffff0000, v215\nSv 160,1,16,5,0\nMvav 176,148,40\nRa 140,2,35840\nMvav 176,144,32\nMvav 184,160,96\nRa 144,2,36864\nWV 8\n"
"v_max_u32 v21, v219, v223\nv_cmp_eq_u32 vcc, -1, v21\ns_nop 0\ns_cbranch_vccnz .S102\n.S103:\nMvav 184,164,104\nv_lshlrev_b32 v192, 16, v216\nMvav 184,168,112\nRa 148,2,37888\n"
"Mvav 184,172,120\nWL 6\nMvav 188,172,124\nRa 152,2,38912\nv_and_b32 v193, 0xffff0000, v216\nMvav 188,168,116\nv_lshlrev_b32 v194, 16, v217\nMvav 188,164,108\nRa 156,2,39936\n"
"Mvav 188,160,100\nv_and_b32 v195, 0xffff0000, v217\nMvav 188,176,36\nRa 160,2,40960\nv_lshlrev_b32 v196, 16, v220\nMvav 188,180,44\nv_and_b32 v197, 0xffff0000, v220\n"
"s_add_u32 s4, s92, 786432\nMvav 188,184,52\nRa 164,2,41984\nMvav 188,188,60\nWL 6\nbuffer_load_dwordx4 v[208:211], v1, s[16:19], s4 offen offset:0 sc0 sc1\nMvav 184,188,56\n"
"Ra 168,2,43008\nv_lshlrev_b32 v198, 16, v221\nMvav 184,184,48\nv_and_b32 v199, 0xffff0000, v221\nMvav 184,180,40\nRa 172,2,44032\nMvav 184,176,32\nv_lshlrev_b32 v200, 16, v218\n"
"buffer_load_dwordx4 v[212:215], v1, s[16:19], s4 offen offset:1024 sc0 sc1\nMvav 192,128,96\nRa 176,2,45056\nv_and_b32 v201, 0xffff0000, v218\nMvav 192,132,104\n"
"v_lshlrev_b32 v202, 16, v219\nCV 180,180,181\nMvav 192,136,112\nRa 180,2,46080\nCV 181,182,183\nMvav 192,140,120\nWL 6\nCV 182,188,189\nMvav 196,140,124\nRa 184,2,47104\n"
"CV 183,190,191\nMvav 196,136,116\nv_and_b32 v203, 0xffff0000, v219\ns_add_u32 s5, s94, 1310720\nMvav 196,132,108\nRa 188,2,48128\nSv 180,1,16,5,1024\nMvav 196,128,100\n"
"v_lshlrev_b32 v204, 16, v222\nCV 176,176,177\nMvav 196,144,36\nRa 128,2,49152\nv_and_b32 v205, 0xffff0000, v222\nMvav 196,148,44\nv_lshlrev_b32 v206, 16, v223\nCV 177,178,179\n"
"Mvav 196,152,52\nRa 132,2,50176\nCV 178,184,185\nMvav 196,156,60\nWL 6\nCV 179,186,187\nMvav 192,156,56\nRa 136,2,51200\nSv 176,1,16,5,0\nMvav 192,152,48\n"
"v_and_b32 v207, 0xffff0000, v223\nMvav 192,148,40\nRa 140,2,52224\nMvav 192,144,32\nMvav 200,160,96\nRa 144,2,53248\nWV 6\nv_max_u32 v21, v227, v231\nv_cmp_eq_u32 vcc, -1, v21\n"
"s_nop 0\ns_cbranch_vccnz .S105\n.S106:\nMvav 200,164,104\nv_lshlrev_b32 v160, 16, v224\nv_xor_b32 v27, s15, v27\nv_and_b32 v26, 0xffffdfff, v27\nv_cmp_ne_u32 s[64:65], 0, v27\n"
"v_cmp_ne_u32 vcc, 0, v26\ns_nop 0\ns_cbranch_vccnz .S117\n.S118:\nMvav 200,168,112\nRa 148,2,54272\ns_bitcmp1_b32 s64, 0\ns_cselect_b32 s2, 0x2000, 0\nMvav 200,172,120\nWL 6\n"
"Mvav 204,172,124\nRa 152,2,55296\nv_and_b32 v161, 0xffff0000, v224\nMvav 204,168,116\nv_lshlrev_b32 v162, 16, v225\nMvav 204,164,108\nRa 156,2,56320\ns_xor_b32 s2, s2, s20\n"
"Mvav 204,160,100\nv_and_b32 v163, 0xffff0000, v225\nMvav 204,176,36\nRa 160,2,57344\nv_lshlrev_b32 v164, 16, v228\nMvav 204,180,44\nv_and_b32 v165, 0xffff0000, v228\n"
"Mvav 204,184,52\nRa 164,2,58368\nMvav 204,188,60\nWL 6\ns_add_u32 s24, s2, 0x0\nMvav 200,188,56\nRa 168,2,59392\nv_lshlrev_b32 v166, 16, v229\nMvav 200,184,48\n"
"v_and_b32 v167, 0xffff0000, v229\ns_bitcmp1_b32 s64, 1\ns_cselect_b32 s2, 0x2000, 0\nMvav 200,180,40\nRa 172,2,60416\nMvav 200,176,32\nv_lshlrev_b32 v168, 16, v226\n"
"Mvav 160,128,96\nRa 176,2,61440\nv_and_b32 v169, 0xffff0000, v226\nMvav 160,132,104\nv_lshlrev_b32 v170, 16, v227\nCV 196,196,197\nMvav 160,136,112\nRa 180,2,62464\n"
"CV 197,198,199\nMvav 160,140,120\nWL 6\nCV 198,204,205\nMvav 164,140,124\nRa 184,2,63488\nCV 199,206,207\nMvav 164,136,116\nv_and_b32 v171, 0xffff0000, v227\n"
"s_add_u32 s5, s94, 1441792\nMvav 164,132,108\nRa 188,2,64512\ns_barrier\nMvav 164,128,100\nv_lshlrev_b32 v172, 16, v230\nSv 196,1,16,5,1024\nMvav 164,144,36\nRa 128,3,0\n"
"v_and_b32 v173, 0xffff0000, v230\nMvav 164,148,44\nv_lshlrev_b32 v174, 16, v231\nCV 192,192,193\nMvav 164,152,52\nRa 132,3,1024\nCV 193,194,195\nMvav 164,156,60\nWL 6\n"
"s_xor_b32 s2, s2, s20\nMvav 160,156,56\nRa 136,3,2048\nCV 194,200,201\nMvav 160,152,48\nv_and_b32 v175, 0xffff0000, v231\nCV 195,202,203\nMvav 160,148,40\nRa 140,3,3072\n"
"Sv 192,1,16,5,0\nMvav 160,144,32\ns_add_u32 s25, s2, 0x1800000\ns_bitcmp1_b32 s64, 2\ns_cselect_b32 s2, 0x2000, 0\nMvav 168,160,96\nRa 144,3,4096\nWV 4\nv_max_u32 v21, v211, v215\n"
"v_cmp_eq_u32 vcc, -1, v21\ns_nop 0\ns_cbranch_vccnz .S108\n.S109:\nMvav 168,164,104\nv_lshlrev_b32 v176, 16, v208\nMvav 168,168,112\nRa 148,3,5120\nMvav 168,172,120\nWL 6\n"
"Mvav 172,172,124\nRa 152,3,6144\nv_and_b32 v177, 0xffff0000, v208\nMvav 172,168,116\nv_lshlrev_b32 v178, 16, v209\nMvav 172,164,108\nRa 156,3,7168\ns_xor_b32 s2, s2, s20\n"
"Mvav 172,160,100\nv_and_b32 v179, 0xffff0000, v209\nMvav 172,176,36\nRa 160,3,8192\nv_lshlrev_b32 v180, 16, v212\nMvav 172,180,44\nv_and_b32 v181, 0xffff0000, v212\n"
"Mvav 172,184,52\nRa 164,3,9216\nMvav 172,188,60\nWL 6\ns_add_u32 s26, s2, 0x3000000\nMvav 168,188,56\nRa 168,3,10240\nv_lshlrev_b32 v182, 16, v213\nMvav 168,184,48\n"
"v_and_b32 v183, 0xffff0000, v213\ns_bitcmp1_b32 s64, 3\ns_cselect_b32 s2, 0x2000, 0\nMvav 168,180,40\nRa 172,3,11264\nMvav 168,176,32\nv_lshlrev_b32 v184, 16, v210\n"
"Mvav 176,128,96\nRa 176,3,12288\ns_xor_b32 s2, s2, s20\nMvav 176,132,104\nv_and_b32 v185, 0xffff0000, v210\nCV 164,164,165\nMvav 176,136,112\nRa 180,3,13312\nCV 165,166,167\n"
"Mvav 176,140,120\nWL 6\nCV 166,172,173\nMvav 180,140,124\nRa 184,3,14336\nCV 167,174,175\nMvav 180,136,116\nv_lshlrev_b32 v186, 16, v211\ns_add_u32 s5, s94, 1572864\n"
"Mvav 180,132,108\nRa 188,3,15360\ns_add_u32 s27, s2, 0x4800000\nMvav 180,128,100\nv_and_b32 v187, 0xffff0000, v211\ns_bitcmp1_b32 s64, 4\ns_cselect_b32 s2, 0x2000, 0\n"
"Mvav 180,144,36\nv_lshlrev_b32 v188, 16, v214\nSv 164,1,16,5,1024\nMvav 180,148,44\nv_and_b32 v189, 0xffff0000, v214\nCV 160,160,161\nMvav 180,152,52\n"
"v_lshlrev_b32 v190, 16, v215\nCV 161,162,163\nMvav 180,156,60\nWL 4\ns_xor_b32 s2, s2, s20\nMvav 176,156,56\nv_and_b32 v191, 0xffff0000, v215\nCV 162,168,169\nMvav 176,152,48\n"
"s_add_u32 s32, s2, 0x6000000\ns_bitcmp1_b32 s64, 5\ns_cselect_b32 s2, 0x2000, 0\nMvav 176,148,40\ns_xor_b32 s2, s2, s20\nCV 163,170,171\nMvav 176,144,32\n"
"s_add_u32 s33, s2, 0x7800000\ns_bitcmp1_b32 s64, 6\ns_cselect_b32 s2, 0x2000, 0\nMvav 184,160,96\ns_xor_b32 s2, s2, s20\ns_add_u32 s34, s2, 0x9000000\nMvav 184,164,104\n"
"s_bitcmp1_b32 s64, 7\ns_cselect_b32 s2, 0x2000, 0\nMvav 184,168,112\ns_xor_b32 s2, s2, s20\nSv 160,1,16,5,0\nMvav 184,172,120\nWL 0\nMvav 188,172,124\n"
"s_add_u32 s35, s2, 0xa800000\nMvav 188,168,116\nMvav 188,164,108\nMvav 188,160,100\nMvav 188,176,36\nMvav 188,180,44\nMvav 188,184,52\nMvav 188,188,60\nMvav 184,188,56\n"
"Mvav 184,184,48\nMvav 184,180,40\nMvav 184,176,32\nCV 180,180,181\nCV 181,182,183\ns_nop 1\nCV 182,188,189\nCV 183,190,191\ns_add_u32 s5, s94, 1703936\nSv 180,1,16,5,1024\n"
"CV 176,176,177\nCV 177,178,179\nCV 178,184,185\nCV 179,186,187\nSv 176,1,16,5,0\ns_cmp_eq_u32 s13, 0\ns_cbranch_scc1 .Z120\n"
"buffer_load_dwordx4 v[12:15], v91, s[28:31], s22 offen offset:0\nbuffer_load_dwordx4 v[16:19], v91, s[28:31], s23 offen offset:0\n"
"buffer_load_dwordx4 v[64:67], v89, s[16:19], s40 offen offset:0 sc0 sc1\nbuffer_load_dwordx4 v[68:71], v89, s[16:19], s40 offen offset:16 sc0 sc1\n"
"buffer_load_dwordx4 v[72:75], v89, s[16:19], s40 offen offset:1792 sc0 sc1\nbuffer_load_dwordx4 v[76:79], v89, s[16:19], s40 offen offset:1808 sc0 sc1\n"
"buffer_load_dwordx2 v[80:81], v88, s[16:19], s63 offen offset:0 sc0 sc1\nbuffer_load_dwordx2 v[82:83], v88, s[16:19], s63 offen offset:512 sc0 sc1\n"
"buffer_load_dwordx4 v[96:99], v1, s[16:19], s24 offen offset:0 sc0 sc1\nbuffer_load_dwordx4 v[100:103], v1, s[16:19], s24 offen offset:1024 sc0 sc1\n"
"buffer_load_dwordx4 v[104:107], v1, s[16:19], s25 offen offset:0 sc0 sc1\nbuffer_load_dwordx4 v[108:111], v1, s[16:19], s25 offen offset:1024 sc0 sc1\n"
"buffer_load_dwordx4 v[112:115], v1, s[16:19], s26 offen offset:0 sc0 sc1\nbuffer_load_dwordx4 v[116:119], v1, s[16:19], s26 offen offset:1024 sc0 sc1\n"
"buffer_load_dwordx4 v[120:123], v1, s[16:19], s27 offen offset:0 sc0 sc1\nbuffer_load_dwordx4 v[124:127], v1, s[16:19], s27 offen offset:1024 sc0 sc1\n"
"buffer_load_dwordx4 v[32:35], v1, s[16:19], s32 offen offset:0 sc0 sc1\nbuffer_load_dwordx4 v[36:39], v1, s[16:19], s32 offen offset:1024 sc0 sc1\n"
"buffer_load_dwordx4 v[40:43], v1, s[16:19], s33 offen offset:0 sc0 sc1\nbuffer_load_dwordx4 v[44:47], v1, s[16:19], s33 offen offset:1024 sc0 sc1\n"
"buffer_load_dwordx4 v[48:51], v1, s[16:19], s34 offen offset:0 sc0 sc1\nbuffer_load_dwordx4 v[52:55], v1, s[16:19], s34 offen offset:1024 sc0 sc1\n"
"buffer_load_dwordx4 v[56:59], v1, s[16:19], s35 offen offset:0 sc0 sc1\nbuffer_load_dwordx4 v[60:63], v1, s[16:19], s35 offen offset:1024 sc0 sc1\nWV 16\nv_xor_b32 v66, s15, v66\n"
"v_xor_b32 v70, s15, v70\nv_xor_b32 v74, s15, v74\nv_xor_b32 v78, s15, v78\nv_xor_b32 v81, s15, v81\nv_xor_b32 v83, s15, v83\nv_or3_b32 v92, v66, v70, v74\n"
"v_or3_b32 v92, v92, v78, v81\nv_or_b32 v92, v92, v83\nv_cmp_ne_u32 vcc, 0, v92\ns_nop 0\ns_cbranch_vccnz .S111\n.S112:\nv_and_b32 v92, 0x7ff, v67\nv_lshrrev_b32 v93, 16, v67\n"
"v_cmp_eq_u32 s[44:45], v92, v90\nv_cmp_eq_u32 s[46:47], v93, v90\nv_cmp_eq_u32 s[48:49], v92, v94\nv_cmp_eq_u32 s[50:51], v93, v94\nv_cndmask_b32 v12, v12, v64, s[44:45]\n"
"v_cndmask_b32 v13, v13, v65, s[44:45]\nv_cndmask_b32 v14, v14, v68, s[44:45]\nv_cndmask_b32 v15, v15, v69, s[44:45]\nv_cndmask_b32 v12, v12, v72, s[46:47]\n"
"v_cndmask_b32 v13, v13, v73, s[46:47]\nv_cndmask_b32 v14, v14, v76, s[46:47]\nv_cndmask_b32 v15, v15, v77, s[46:47]\nv_cndmask_b32 v16, v16, v64, s[48:49]\n"
"v_cndmask_b32 v17, v17, v65, s[48:49]\nv_cndmask_b32 v18, v18, v68, s[48:49]\nv_cndmask_b32 v19, v19, v69, s[48:49]\nv_cndmask_b32 v16, v16, v72, s[50:51]\n"
"v_cndmask_b32 v17, v17, v73, s[50:51]\nv_cndmask_b32 v18, v18, v76, s[50:51]\nv_cndmask_b32 v19, v19, v77, s[50:51]\nv_sub_f32 v92, 1.0, v80\nv_sub_f32 v93, 1.0, v82\nCV 92,92,93\n"
"v_and_b32 v93, 0xffff0000, v92\nv_lshlrev_b32 v92, 16, v92\nv_lshlrev_b32 v64, 16, v12\nv_and_b32 v65, 0xffff0000, v12\nMU 64,64,92\nMU 65,65,92\nv_lshlrev_b32 v66, 16, v13\n"
"v_and_b32 v67, 0xffff0000, v13\nMU 66,66,92\nMU 67,67,92\nv_lshlrev_b32 v68, 16, v14\nv_and_b32 v69, 0xffff0000, v14\nMU 68,68,92\nMU 69,69,92\nv_lshlrev_b32 v70, 16, v15\n"
"v_and_b32 v71, 0xffff0000, v15\nMU 70,70,92\nMU 71,71,92\nv_lshlrev_b32 v72, 16, v16\nv_and_b32 v73, 0xffff0000, v16\nMU 72,72,93\nMU 73,73,93\nv_lshlrev_b32 v74, 16, v17\n"
"v_and_b32 v75, 0xffff0000, v17\nMU 74,74,93\nMU 75,75,93\nv_lshlrev_b32 v76, 16, v18\nv_and_b32 v77, 0xffff0000, v18\nMU 76,76,93\nMU 77,77,93\nv_lshlrev_b32 v78, 16, v19\n"
"v_and_b32 v79, 0xffff0000, v19\nMU 78,78,93\nMU 79,79,93\ns_mov_b32 s7, 262144\nWV 0\n.Z123:\nv_max_u32 v84, v99, v103\nv_cmp_eq_u32 s[44:45], -1, v84\nv_max_u32 v84, v107, v111\n"
"v_cmp_eq_u32 s[46:47], -1, v84\nv_max_u32 v84, v115, v119\nv_cmp_eq_u32 s[48:49], -1, v84\nv_max_u32 v84, v123, v127\nv_cmp_eq_u32 s[50:51], -1, v84\nv_max_u32 v84, v35, v39\n"
"v_cmp_eq_u32 s[52:53], -1, v84\nv_max_u32 v84, v43, v47\nv_cmp_eq_u32 s[54:55], -1, v84\nv_max_u32 v84, v51, v55\nv_cmp_eq_u32 s[56:57], -1, v84\nv_max_u32 v84, v59, v63\n"
"v_cmp_eq_u32 s[58:59], -1, v84\ns_or_b64 s[0:1], s[44:45], s[46:47]\ns_or_b64 s[2:3], s[48:49], s[50:51]\ns_or_b64 s[0:1], s[0:1], s[52:53]\ns_or_b64 s[2:3], s[2:3], s[54:55]\n"
"s_or_b64 s[0:1], s[0:1], s[56:57]\ns_or_b64 s[2:3], s[2:3], s[58:59]\ns_or_b64 s[0:1], s[0:1], s[2:3]\ns_cbranch_scc1 .Z121\n.Z122:\nZvvv 136,6,96\nMvvv 136,6,104\nMvvv 136,6,112\n"
"Mvvv 136,6,120\nMvvv 136,6,32\nMvvv 136,6,40\nMvvv 136,6,48\nMvvv 136,6,56\nZvvv 140,4,96\nMvvv 140,4,104\nMvvv 140,4,112\nMvvv 140,4,120\nMvvv 140,4,32\nMvvv 140,4,40\n"
"Mvvv 140,4,48\nMvvv 140,4,56\nZvvv 144,6,100\nMvvv 144,6,108\nMvvv 144,6,116\nMvvv 144,6,124\nMvvv 144,6,36\nMvvv 144,6,44\nMvvv 144,6,52\nMvvv 144,6,60\nZvvv 148,4,100\n"
"Mvvv 148,4,108\nMvvv 148,4,116\nMvvv 148,4,124\nMvvv 148,4,36\nMvvv 148,4,44\nMvvv 148,4,52\nMvvv 148,4,60\nFM 64,80,136\nFM 65,80,137\nFM 66,80,138\nFM 67,80,139\nFM 68,80,140\n"
"FM 69,80,141\nFM 70,80,142\nFM 71,80,143\nCV 12,64,65\nCV 13,66,67\nCV 14,68,69\nCV 15,70,71\nSv 12,91,36,22,0\nFM 72,82,144\nFM 73,82,145\nFM 74,82,146\nFM 75,82,147\n"
"FM 76,82,148\nFM 77,82,149\nFM 78,82,150\nFM 79,82,151\nCV 16,72,73\nCV 17,74,75\nCV 18,76,77\nCV 19,78,79\nSv 16,91,36,23,0\n.Z120:\ns_endpgm\n.EP132:\n.S14:\n"
"s_mov_b32 s7, 262144\n.S16:\nbuffer_load_dwordx4 v[32:35], v1, s[16:19], s6 offen offset:0 sc0 sc1\nbuffer_load_dwordx4 v[36:39], v1, s[16:19], s6 offen offset:1024 sc0 sc1\n"
"buffer_load_dwordx4 v[40:43], v1, s[16:19], s6 offen offset:2048 sc0 sc1\nbuffer_load_dwordx4 v[44:47], v1, s[16:19], s6 offen offset:3072 sc0 sc1\nWV 0\nv_xor_b32 v48, s15, v33\n"
"v_xor_b32 v49, s15, v35\nv_xor_b32 v50, s15, v37\nv_xor_b32 v51, s15, v39\nv_xor_b32 v52, s15, v41\nv_xor_b32 v53, s15, v43\nv_xor_b32 v54, s15, v45\nv_xor_b32 v55, s15, v47\n"
"v_or3_b32 v48, v48, v49, v50\nv_or3_b32 v51, v51, v52, v53\nv_or3_b32 v48, v48, v54, v55\nv_or_b32 v48, v48, v51\nv_cmp_ne_u32 vcc, 0, v48\ns_nop 0\ns_cbranch_vccz .S15\n"
"s_sleep 6\ns_sub_u32 s7, s7, 1\ns_cbranch_scc0 .S16\ns_branch .S15\n.S17:\ns_mov_b32 s7, 262144\n.S19:\ns_lshl_b32 s5, s73, 9\ns_add_u32 s5, s5, 0xc049400\n"
"v_lshrrev_b32 v27, 1, v1\nbuffer_load_dwordx2 v[22:23], v27, s[16:19], s5 offen sc0 sc1\nWV 0\nv_cmp_ne_u32 vcc, s15, v23\ns_nop 0\ns_cbranch_vccz .S18\ns_sleep 6\n"
"s_sub_u32 s7, s7, 1\ns_cbranch_scc0 .S19\ns_branch .S18\n.T23:\ns_mov_b32 s62, 1\ns_call_b64 s[80:81], .RA10\ns_branch .T24\n.T25:\ns_mov_b32 s62, 1\ns_call_b64 s[80:81], .RA10\n"
"s_branch .T26\n.T28:\ns_mov_b32 s62, 0\ns_call_b64 s[80:81], .RB11\ns_branch .T29\n.T30:\ns_mov_b32 s62, 0\ns_call_b64 s[80:81], .RA10\ns_branch .T31\n.T33:\ns_mov_b32 s62, 0\n"
"s_call_b64 s[80:81], .RB11\ns_branch .T34\n.T35:\ns_mov_b32 s62, 0\ns_call_b64 s[80:81], .RA10\ns_branch .T36\n.S38:\ns_mov_b32 s7, 262144\n.S40:\nv_and_b32 v27, 0x70, v1\n"
"buffer_load_dword v27, v27, s[16:19], s61 offen offset:128 sc0 sc1\nWV 0\nv_cmp_ne_u32 vcc, s15, v27\ns_nop 0\ns_cbranch_vccz .S39\ns_sleep 6\ns_sub_u32 s7, s7, 1\n"
"s_cbranch_scc0 .S40\ns_branch .S39\n.S41:\ns_mov_b32 s7, 262144\n.S43:\nv_and_b32 v27, 0x70, v1\nbuffer_load_dword v27, v27, s[16:19], s61 offen offset:136 sc0 sc1\nWV 0\n"
"v_cmp_ne_u32 vcc, s15, v27\ns_nop 0\ns_cbranch_vccz .S42\ns_sleep 6\ns_sub_u32 s7, s7, 1\ns_cbranch_scc0 .S43\ns_branch .S42\n.S44:\ns_mov_b32 s7, 262144\n.S46:\n"
"v_and_b32 v27, 0x70, v1\nbuffer_load_dword v27, v27, s[16:19], s61 offen offset:140 sc0 sc1\nWV 0\nv_cmp_ne_u32 vcc, s15, v27\ns_nop 0\ns_cbranch_vccz .S45\ns_sleep 6\n"
"s_sub_u32 s7, s7, 1\ns_cbranch_scc0 .S46\ns_branch .S45\n.S47:\ns_mov_b32 s7, 262144\n.S49:\nv_and_b32 v27, 0x70, v1\n"
"buffer_load_dword v27, v27, s[16:19], s61 offen offset:12 sc0 sc1\nWV 0\nv_cmp_ne_u32 vcc, s15, v27\ns_nop 0\ns_cbranch_vccz .S48\ns_sleep 6\ns_sub_u32 s7, s7, 1\n"
"s_cbranch_scc0 .S49\ns_branch .S48\n.S50:\ns_mov_b32 s7, 262144\n.S52:\nv_and_b32 v27, 0x70, v1\nbuffer_load_dword v27, v27, s[16:19], s61 offen offset:8 sc0 sc1\nWV 0\n"
"v_cmp_ne_u32 vcc, s15, v27\ns_nop 0\ns_cbranch_vccz .S51\ns_sleep 6\ns_sub_u32 s7, s7, 1\ns_cbranch_scc0 .S52\ns_branch .S51\n.S53:\ns_mov_b32 s7, 262144\n.S55:\n"
"v_and_b32 v27, 0xf0, v1\nbuffer_load_dword v27, v27, s[16:19], s61 offen offset:4 sc0 sc1\nWV 0\nv_cmp_ne_u32 vcc, s15, v27\ns_nop 0\ns_cbranch_vccz .S54\ns_sleep 6\n"
"s_sub_u32 s7, s7, 1\ns_cbranch_scc0 .S55\ns_branch .S54\n.S57:\ns_mov_b32 s7, 262144\n.S59:\nv_lshrrev_b32 v104, 9, v1\nv_mul_u32_u24 v104, 0x18000, v104\n"
"v_lshlrev_b32 v104, 8, v104\nv_and_b32 v105, 0x1f0, v1\nv_add_u32 v104, v104, v105\ns_mov_b32 s6, 0x15c0000\nbuffer_load_dword v96, v104, s[16:19], s6 offen offset:4 sc0 sc1\n"
"s_mov_b32 s6, 0x15c2000\nbuffer_load_dword v97, v104, s[16:19], s6 offen offset:4 sc0 sc1\ns_mov_b32 s6, 0x45c0000\n"
"buffer_load_dword v98, v104, s[16:19], s6 offen offset:4 sc0 sc1\ns_mov_b32 s6, 0x45c2000\nbuffer_load_dword v99, v104, s[16:19], s6 offen offset:4 sc0 sc1\n"
"s_mov_b32 s6, 0x75c0000\nbuffer_load_dword v100, v104, s[16:19], s6 offen offset:4 sc0 sc1\ns_mov_b32 s6, 0x75c2000\n"
"buffer_load_dword v101, v104, s[16:19], s6 offen offset:4 sc0 sc1\ns_mov_b32 s6, 0xa5c0000\nbuffer_load_dword v102, v104, s[16:19], s6 offen offset:4 sc0 sc1\n"
"s_mov_b32 s6, 0xa5c2000\nbuffer_load_dword v103, v104, s[16:19], s6 offen offset:4 sc0 sc1\nWV 0\nv_xor_b32 v96, s15, v96\nv_xor_b32 v97, s15, v97\nv_xor_b32 v98, s15, v98\n"
"v_xor_b32 v99, s15, v99\nv_xor_b32 v100, s15, v100\nv_xor_b32 v101, s15, v101\nv_xor_b32 v102, s15, v102\nv_xor_b32 v103, s15, v103\nv_min_u32 v96, v96, v97\n"
"v_min_u32 v98, v98, v99\nv_min_u32 v100, v100, v101\nv_min_u32 v102, v102, v103\nv_or3_b32 v96, v96, v98, v100\nv_or_b32 v96, v96, v102\nv_cmp_ne_u32 vcc, 0, v96\ns_nop 0\n"
"s_cbranch_vccz .S58\ns_sleep 6\ns_sub_u32 s7, s7, 1\ns_cbranch_scc0 .S59\ns_branch .S58\n.FF56:\nv_lshrrev_b32 v104, 9, v1\nv_mul_u32_u24 v104, 0x18000, v104\n"
"v_lshlrev_b32 v104, 8, v104\nv_and_b32 v105, 0x1f0, v1\nv_add_u32 v104, v104, v105\ns_mov_b32 s6, 0x15c0000\nbuffer_load_dword v96, v104, s[16:19], s6 offen offset:4 sc0 sc1\n"
"s_mov_b32 s6, 0x15c2000\nbuffer_load_dword v97, v104, s[16:19], s6 offen offset:4 sc0 sc1\ns_mov_b32 s6, 0x45c0000\n"
"buffer_load_dword v98, v104, s[16:19], s6 offen offset:4 sc0 sc1\ns_mov_b32 s6, 0x45c2000\nbuffer_load_dword v99, v104, s[16:19], s6 offen offset:4 sc0 sc1\n"
"s_mov_b32 s6, 0x75c0000\nbuffer_load_dword v100, v104, s[16:19], s6 offen offset:4 sc0 sc1\ns_mov_b32 s6, 0x75c2000\n"
"buffer_load_dword v101, v104, s[16:19], s6 offen offset:4 sc0 sc1\ns_mov_b32 s6, 0xa5c0000\nbuffer_load_dword v102, v104, s[16:19], s6 offen offset:4 sc0 sc1\n"
"s_mov_b32 s6, 0xa5c2000\nbuffer_load_dword v103, v104, s[16:19], s6 offen offset:4 sc0 sc1\ns_mov_b32 s90, 0\ns_swappc_b64 s[8:9], s[80:81]\nv_xor_b32 v96, s15, v96\n"
"v_xor_b32 v97, s15, v97\nv_xor_b32 v98, s15, v98\nv_xor_b32 v99, s15, v99\nv_xor_b32 v100, s15, v100\nv_xor_b32 v101, s15, v101\nv_xor_b32 v102, s15, v102\n"
"v_xor_b32 v103, s15, v103\nv_min_u32 v96, v96, v97\nv_min_u32 v98, v98, v99\nv_min_u32 v100, v100, v101\nv_min_u32 v102, v102, v103\nv_or3_b32 v96, v96, v98, v100\n"
"v_or_b32 v96, v96, v102\nv_cmp_ne_u32 vcc, 0, v96\ns_nop 0\ns_cbranch_vccnz .S57\n.S58:\ns_and_b32 s4, s13, 1\ns_lshl1_add_u32 s4, s4, s11\ns_lshl2_add_u32 s4, s12, s4\n"
"s_lshr_b32 s5, s71, 16\ns_cmp_lt_u32 s4, 28\ns_cselect_b32 s5, s71, s5\ns_cselect_b32 s3, 0, 28\ns_and_b32 s5, s5, 0x7ff\ns_sub_u32 s3, s4, s3\ns_mul_i32 s2, s4, 7168\n"
"s_add_u32 s2, s2, 0xc04c800\ns_add_u32 s6, s2, 4096\nbuffer_load_dwordx4 v[96:99], v1, s[16:19], s2 offen offset:0 sc0 sc1\n"
"buffer_load_dwordx4 v[100:103], v1, s[16:19], s2 offen offset:1024 sc0 sc1\nbuffer_load_dwordx4 v[104:107], v1, s[16:19], s2 offen offset:2048 sc0 sc1\n"
"buffer_load_dwordx4 v[108:111], v1, s[16:19], s2 offen offset:3072 sc0 sc1\nbuffer_load_dwordx4 v[112:115], v1, s[16:19], s6 offen offset:0 sc0 sc1\n"
"buffer_load_dwordx4 v[116:119], v1, s[16:19], s6 offen offset:1024 sc0 sc1\nbuffer_load_dwordx4 v[120:123], v1, s[16:19], s6 offen offset:2048 sc0 sc1\nv_lshrrev_b32 v127, 1, v1\n"
"v_and_b32 v127, 24, v127\ns_mul_i32 s2, s5, 896\ns_lshl_b32 s3, s3, 5\ns_add_u32 s2, s2, s3\ns_lshl2_add_u32 s5, s5, 0xc03e000\n"
"buffer_load_dwordx2 v[124:125], v127, s[28:31], s2 offen\nbuffer_load_dword v126, off, s[16:19], s5 sc0 sc1\ns_mov_b32 s90, 0\ns_swappc_b64 s[8:9], s[80:81]\nAD 96,96,100\n"
"AD 97,97,101\nAD 98,98,102\nAD 99,99,103\nAD 104,104,108\nAD 105,105,109\nAD 106,106,110\nAD 107,107,111\nAD 112,112,116\nAD 113,113,117\nAD 114,114,118\nAD 115,115,119\n"
"AD 96,96,104\nAD 97,97,105\nAD 98,98,106\nAD 99,99,107\nAD 112,112,120\nAD 113,113,121\nAD 114,114,122\nAD 115,115,123\nAD 96,96,112\nAD 97,97,113\nAD 98,98,114\nAD 99,99,115\n"
"v_mov_b32 v100, v96\nv_mov_b32 v101, v97\nv_mov_b32 v102, v98\nv_mov_b32 v103, v99\nv_permlane32_swap_b32 v96, v100\nv_permlane32_swap_b32 v97, v101\n"
"v_permlane32_swap_b32 v98, v102\nv_permlane32_swap_b32 v99, v103\nAD 96,96,100\nAD 97,97,101\nAD 98,98,102\nAD 99,99,103\nv_mov_b32 v100, v96\nv_mov_b32 v101, v97\n"
"v_mov_b32 v102, v98\nv_mov_b32 v103, v99\nv_permlane16_swap_b32 v96, v100\nv_permlane16_swap_b32 v97, v101\nv_permlane16_swap_b32 v98, v102\nv_permlane16_swap_b32 v99, v103\n"
"AD 96,96,100\nAD 97,97,101\nAD 98,98,102\nAD 99,99,103\nv_add_f32_dpp v96, v96, v96 row_ror:8 row_mask:0xf bank_mask:0xf\n"
"v_add_f32_dpp v97, v97, v97 row_ror:8 row_mask:0xf bank_mask:0xf\nv_add_f32_dpp v98, v98, v98 row_ror:8 row_mask:0xf bank_mask:0xf\n"
"v_add_f32_dpp v99, v99, v99 row_ror:8 row_mask:0xf bank_mask:0xf\nv_add_f32_dpp v96, v96, v96 row_ror:4 row_mask:0xf bank_mask:0xf\n"
"v_add_f32_dpp v97, v97, v97 row_ror:4 row_mask:0xf bank_mask:0xf\nv_add_f32_dpp v98, v98, v98 row_ror:4 row_mask:0xf bank_mask:0xf\n"
"v_add_f32_dpp v99, v99, v99 row_ror:4 row_mask:0xf bank_mask:0xf\nv_mul_f32 v126, 0xbfb8aa3b, v126\nEX 126,126\nCV 96,96,97\nv_add_f32 v126, 1.0, v126\nv_rcp_f32 v126, v126\n"
"CV 98,98,99\nCV 126,126,126\nv_lshlrev_b32 v126, 16, v126\nv_and_b32 v97, 0xffff0000, v96\nv_lshlrev_b32 v96, 16, v96\nv_and_b32 v99, 0xffff0000, v98\nv_lshlrev_b32 v98, 16, v98\n"
"MU 96,96,126\nMU 97,97,126\nMU 98,98,126\nMU 99,99,126\nCV 96,96,97\nCV 98,98,99\nv_and_b32 v97, 0xffff0000, v96\nv_lshlrev_b32 v96, 16, v96\nv_and_b32 v99, 0xffff0000, v98\n"
"v_lshlrev_b32 v98, 16, v98\nv_lshlrev_b32 v100, 16, v124\nv_and_b32 v101, 0xffff0000, v124\nv_lshlrev_b32 v102, 16, v125\nv_and_b32 v103, 0xffff0000, v125\nAD 96,96,100\n"
"AD 97,97,101\nAD 98,98,102\nAD 99,99,103\nCV 96,96,97\nCV 97,98,99\nv_mov_b32 v98, s15\nv_mov_b32 v99, s71\ns_and_b32 s4, s13, 1\ns_lshl1_add_u32 s4, s4, s11\n"
"s_lshl2_add_u32 s4, s12, s4\ns_lshr_b32 s5, s71, 16\ns_cmp_lt_u32 s4, 28\ns_cselect_b32 s5, s71, s5\ns_cselect_b32 s3, 0, 28\ns_and_b32 s5, s5, 0x7ff\ns_sub_u32 s3, s4, s3\n"
"v_mov_b32 v100, 0x40000000\nv_cmp_gt_u32 vcc, 64, v1\ns_lshl_b32 s4, s4, 6\ns_add_u32 s4, s4, 0xc04b800\nv_cndmask_b32 v100, v100, v1, vcc\nSv 96,100,16,4,0\ns_mov_b32 s90, 100\n"
"s_swappc_b64 s[8:9], s[80:81]\n.T64:\ns_mov_b32 s62, 0\ns_call_b64 s[80:81], .RB11\ns_branch .T65\n.T66:\ns_mov_b32 s62, 0\ns_call_b64 s[80:81], .RA10\ns_branch .T67\n.T70:\n"
"s_mov_b32 s62, 0\ns_call_b64 s[80:81], .RB11\ns_branch .T71\n.T72:\ns_mov_b32 s62, 0\ns_call_b64 s[80:81], .RA10\ns_branch .T73\n.T75:\ns_mov_b32 s62, 0\n"
"s_call_b64 s[80:81], .RB11\ns_branch .T76\n.T77:\ns_mov_b32 s62, 0\ns_call_b64 s[80:81], .RA10\ns_branch .T78\n.S83:\ns_mov_b32 s7, 262144\n.S85:\n"
"buffer_load_dwordx4 v[100:103], v108, s[16:19], s65 offen offset:0 sc0 sc1\nbuffer_load_dwordx4 v[104:107], v108, s[16:19], s65 offen offset:256 sc0 sc1\nWV 0\n"
"v_xor_b32 v109, s15, v102\nv_xor_b32 v110, s15, v106\nv_or_b32 v109, v109, v110\nv_cmp_ne_u32 vcc, 0, v109\ns_nop 0\ns_cbranch_vccz .S84\ns_sleep 6\ns_sub_u32 s7, s7, 1\n"
"s_cbranch_scc0 .S85\ns_branch .S84\n.D81:\nv_mov_b32 v114, 0\nv_mov_b32 v115, 0\ns_mov_b32 s63, 0xff800000\ns_mov_b32 s67, 1\ns_branch .D82\n.T87:\ns_lshl3_add_u32 s62, s67, 2\n"
"s_call_b64 s[80:81], .RB11\ns_cmp_eq_u32 s67, 0\ns_cbranch_scc1 .T88\nSU 20,100,88\nv_min_f32 v20, 0, v20\nEX 20,20\ns_nop 0\nFM 86,101,20\nSU 21,104,89\nv_min_f32 v21, 0, v21\n"
"EX 21,21\ns_nop 0\nFM 87,105,21\nAR 22,0\nv_and_b32 v23, 0xffff0000, v22\nv_lshlrev_b32 v22, 16, v22\nMU 22,22,20\nMU 23,23,20\nCV 22,22,23\nAW 0,22\nAR 22,1\n"
"v_and_b32 v23, 0xffff0000, v22\nv_lshlrev_b32 v22, 16, v22\nMU 22,22,20\nMU 23,23,20\nCV 22,22,23\nAW 1,22\nAR 22,2\nv_and_b32 v23, 0xffff0000, v22\nv_lshlrev_b32 v22, 16, v22\n"
"MU 22,22,20\nMU 23,23,20\nCV 22,22,23\nAW 2,22\nAR 22,3\nv_and_b32 v23, 0xffff0000, v22\nv_lshlrev_b32 v22, 16, v22\nMU 22,22,20\nMU 23,23,20\nCV 22,22,23\nAW 3,22\nAR 22,4\n"
"v_and_b32 v23, 0xffff0000, v22\nv_lshlrev_b32 v22, 16, v22\nMU 22,22,21\nMU 23,23,21\nCV 22,22,23\nAW 4,22\nAR 22,5\nv_and_b32 v23, 0xffff0000, v22\nv_lshlrev_b32 v22, 16, v22\n"
"MU 22,22,21\nMU 23,23,21\nCV 22,22,23\nAW 5,22\nAR 22,6\nv_and_b32 v23, 0xffff0000, v22\nv_lshlrev_b32 v22, 16, v22\nMU 22,22,21\nMU 23,23,21\nCV 22,22,23\nAW 6,22\nAR 22,7\n"
"v_and_b32 v23, 0xffff0000, v22\nv_lshlrev_b32 v22, 16, v22\nMU 22,22,21\nMU 23,23,21\nCV 22,22,23\nAW 7,22\nAR 22,8\nv_and_b32 v23, 0xffff0000, v22\nv_lshlrev_b32 v22, 16, v22\n"
"MU 22,22,20\nMU 23,23,20\nCV 22,22,23\nAW 8,22\nAR 22,9\nv_and_b32 v23, 0xffff0000, v22\nv_lshlrev_b32 v22, 16, v22\nMU 22,22,20\nMU 23,23,20\nCV 22,22,23\nAW 9,22\nAR 22,10\n"
"v_and_b32 v23, 0xffff0000, v22\nv_lshlrev_b32 v22, 16, v22\nMU 22,22,20\nMU 23,23,20\nCV 22,22,23\nAW 10,22\nAR 22,11\nv_and_b32 v23, 0xffff0000, v22\nv_lshlrev_b32 v22, 16, v22\n"
"MU 22,22,20\nMU 23,23,20\nCV 22,22,23\nAW 11,22\nAR 22,12\nv_and_b32 v23, 0xffff0000, v22\nv_lshlrev_b32 v22, 16, v22\nMU 22,22,21\nMU 23,23,21\nCV 22,22,23\nAW 12,22\nAR 22,13\n"
"v_and_b32 v23, 0xffff0000, v22\nv_lshlrev_b32 v22, 16, v22\nMU 22,22,21\nMU 23,23,21\nCV 22,22,23\nAW 13,22\nAR 22,14\nv_and_b32 v23, 0xffff0000, v22\nv_lshlrev_b32 v22, 16, v22\n"
"MU 22,22,21\nMU 23,23,21\nCV 22,22,23\nAW 14,22\nAR 22,15\nv_and_b32 v23, 0xffff0000, v22\nv_lshlrev_b32 v22, 16, v22\nMU 22,22,21\nMU 23,23,21\nCV 22,22,23\nAW 15,22\nAR 22,16\n"
"v_and_b32 v23, 0xffff0000, v22\nv_lshlrev_b32 v22, 16, v22\nMU 22,22,20\nMU 23,23,20\nCV 22,22,23\nAW 16,22\nAR 22,17\nv_and_b32 v23, 0xffff0000, v22\nv_lshlrev_b32 v22, 16, v22\n"
"MU 22,22,20\nMU 23,23,20\nCV 22,22,23\nAW 17,22\nAR 22,18\nv_and_b32 v23, 0xffff0000, v22\nv_lshlrev_b32 v22, 16, v22\nMU 22,22,20\nMU 23,23,20\nCV 22,22,23\nAW 18,22\nAR 22,19\n"
"v_and_b32 v23, 0xffff0000, v22\nv_lshlrev_b32 v22, 16, v22\nMU 22,22,20\nMU 23,23,20\nCV 22,22,23\nAW 19,22\nAR 22,20\nv_and_b32 v23, 0xffff0000, v22\nv_lshlrev_b32 v22, 16, v22\n"
"MU 22,22,21\nMU 23,23,21\nCV 22,22,23\nAW 20,22\nAR 22,21\nv_and_b32 v23, 0xffff0000, v22\nv_lshlrev_b32 v22, 16, v22\nMU 22,22,21\nMU 23,23,21\nCV 22,22,23\nAW 21,22\nAR 22,22\n"
"v_and_b32 v23, 0xffff0000, v22\nv_lshlrev_b32 v22, 16, v22\nMU 22,22,21\nMU 23,23,21\nCV 22,22,23\nAW 22,22\nAR 22,23\nv_and_b32 v23, 0xffff0000, v22\nv_lshlrev_b32 v22, 16, v22\n"
"MU 22,22,21\nMU 23,23,21\nCV 22,22,23\nAW 23,22\nAR 22,24\nv_and_b32 v23, 0xffff0000, v22\nv_lshlrev_b32 v22, 16, v22\nMU 22,22,20\nMU 23,23,20\nCV 22,22,23\nAW 24,22\nAR 22,25\n"
"v_and_b32 v23, 0xffff0000, v22\nv_lshlrev_b32 v22, 16, v22\nMU 22,22,20\nMU 23,23,20\nCV 22,22,23\nAW 25,22\nAR 22,26\nv_and_b32 v23, 0xffff0000, v22\nv_lshlrev_b32 v22, 16, v22\n"
"MU 22,22,20\nMU 23,23,20\nCV 22,22,23\nAW 26,22\nAR 22,27\nv_and_b32 v23, 0xffff0000, v22\nv_lshlrev_b32 v22, 16, v22\nMU 22,22,20\nMU 23,23,20\nCV 22,22,23\nAW 27,22\nAR 22,28\n"
"v_and_b32 v23, 0xffff0000, v22\nv_lshlrev_b32 v22, 16, v22\nMU 22,22,21\nMU 23,23,21\nCV 22,22,23\nAW 28,22\nAR 22,29\nv_and_b32 v23, 0xffff0000, v22\nv_lshlrev_b32 v22, 16, v22\n"
"MU 22,22,21\nMU 23,23,21\nCV 22,22,23\nAW 29,22\nAR 22,30\nv_and_b32 v23, 0xffff0000, v22\nv_lshlrev_b32 v22, 16, v22\nMU 22,22,21\nMU 23,23,21\nCV 22,22,23\nAW 30,22\nAR 22,31\n"
"v_and_b32 v23, 0xffff0000, v22\nv_lshlrev_b32 v22, 16, v22\nMU 22,22,21\nMU 23,23,21\nCV 22,22,23\nAW 31,22\nAR 22,32\nv_and_b32 v23, 0xffff0000, v22\nv_lshlrev_b32 v22, 16, v22\n"
"MU 22,22,20\nMU 23,23,20\nCV 22,22,23\nAW 32,22\nAR 22,33\nv_and_b32 v23, 0xffff0000, v22\nv_lshlrev_b32 v22, 16, v22\nMU 22,22,20\nMU 23,23,20\nCV 22,22,23\nAW 33,22\nAR 22,34\n"
"v_and_b32 v23, 0xffff0000, v22\nv_lshlrev_b32 v22, 16, v22\nMU 22,22,20\nMU 23,23,20\nCV 22,22,23\nAW 34,22\nAR 22,35\nv_and_b32 v23, 0xffff0000, v22\nv_lshlrev_b32 v22, 16, v22\n"
"MU 22,22,20\nMU 23,23,20\nCV 22,22,23\nAW 35,22\nAR 22,36\nv_and_b32 v23, 0xffff0000, v22\nv_lshlrev_b32 v22, 16, v22\nMU 22,22,21\nMU 23,23,21\nCV 22,22,23\nAW 36,22\nAR 22,37\n"
"v_and_b32 v23, 0xffff0000, v22\nv_lshlrev_b32 v22, 16, v22\nMU 22,22,21\nMU 23,23,21\nCV 22,22,23\nAW 37,22\nAR 22,38\nv_and_b32 v23, 0xffff0000, v22\nv_lshlrev_b32 v22, 16, v22\n"
"MU 22,22,21\nMU 23,23,21\nCV 22,22,23\nAW 38,22\nAR 22,39\nv_and_b32 v23, 0xffff0000, v22\nv_lshlrev_b32 v22, 16, v22\nMU 22,22,21\nMU 23,23,21\nCV 22,22,23\nAW 39,22\nAR 22,40\n"
"v_and_b32 v23, 0xffff0000, v22\nv_lshlrev_b32 v22, 16, v22\nMU 22,22,20\nMU 23,23,20\nCV 22,22,23\nAW 40,22\nAR 22,41\nv_and_b32 v23, 0xffff0000, v22\nv_lshlrev_b32 v22, 16, v22\n"
"MU 22,22,20\nMU 23,23,20\nCV 22,22,23\nAW 41,22\nAR 22,42\nv_and_b32 v23, 0xffff0000, v22\nv_lshlrev_b32 v22, 16, v22\nMU 22,22,20\nMU 23,23,20\nCV 22,22,23\nAW 42,22\nAR 22,43\n"
"v_and_b32 v23, 0xffff0000, v22\nv_lshlrev_b32 v22, 16, v22\nMU 22,22,20\nMU 23,23,20\nCV 22,22,23\nAW 43,22\nAR 22,44\nv_and_b32 v23, 0xffff0000, v22\nv_lshlrev_b32 v22, 16, v22\n"
"MU 22,22,21\nMU 23,23,21\nCV 22,22,23\nAW 44,22\nAR 22,45\nv_and_b32 v23, 0xffff0000, v22\nv_lshlrev_b32 v22, 16, v22\nMU 22,22,21\nMU 23,23,21\nCV 22,22,23\nAW 45,22\nAR 22,46\n"
"v_and_b32 v23, 0xffff0000, v22\nv_lshlrev_b32 v22, 16, v22\nMU 22,22,21\nMU 23,23,21\nCV 22,22,23\nAW 46,22\nAR 22,47\nv_and_b32 v23, 0xffff0000, v22\nv_lshlrev_b32 v22, 16, v22\n"
"MU 22,22,21\nMU 23,23,21\nCV 22,22,23\nAW 47,22\nAR 22,48\nv_and_b32 v23, 0xffff0000, v22\nv_lshlrev_b32 v22, 16, v22\nMU 22,22,20\nMU 23,23,20\nCV 22,22,23\nAW 48,22\nAR 22,49\n"
"v_and_b32 v23, 0xffff0000, v22\nv_lshlrev_b32 v22, 16, v22\nMU 22,22,20\nMU 23,23,20\nCV 22,22,23\nAW 49,22\nAR 22,50\nv_and_b32 v23, 0xffff0000, v22\nv_lshlrev_b32 v22, 16, v22\n"
"MU 22,22,20\nMU 23,23,20\nCV 22,22,23\nAW 50,22\nAR 22,51\nv_and_b32 v23, 0xffff0000, v22\nv_lshlrev_b32 v22, 16, v22\nMU 22,22,20\nMU 23,23,20\nCV 22,22,23\nAW 51,22\nAR 22,52\n"
"v_and_b32 v23, 0xffff0000, v22\nv_lshlrev_b32 v22, 16, v22\nMU 22,22,21\nMU 23,23,21\nCV 22,22,23\nAW 52,22\nAR 22,53\nv_and_b32 v23, 0xffff0000, v22\nv_lshlrev_b32 v22, 16, v22\n"
"MU 22,22,21\nMU 23,23,21\nCV 22,22,23\nAW 53,22\nAR 22,54\nv_and_b32 v23, 0xffff0000, v22\nv_lshlrev_b32 v22, 16, v22\nMU 22,22,21\nMU 23,23,21\nCV 22,22,23\nAW 54,22\nAR 22,55\n"
"v_and_b32 v23, 0xffff0000, v22\nv_lshlrev_b32 v22, 16, v22\nMU 22,22,21\nMU 23,23,21\nCV 22,22,23\nAW 55,22\nAR 22,56\nv_and_b32 v23, 0xffff0000, v22\nv_lshlrev_b32 v22, 16, v22\n"
"MU 22,22,20\nMU 23,23,20\nCV 22,22,23\nAW 56,22\nAR 22,57\nv_and_b32 v23, 0xffff0000, v22\nv_lshlrev_b32 v22, 16, v22\nMU 22,22,20\nMU 23,23,20\nCV 22,22,23\nAW 57,22\nAR 22,58\n"
"v_and_b32 v23, 0xffff0000, v22\nv_lshlrev_b32 v22, 16, v22\nMU 22,22,20\nMU 23,23,20\nCV 22,22,23\nAW 58,22\nAR 22,59\nv_and_b32 v23, 0xffff0000, v22\nv_lshlrev_b32 v22, 16, v22\n"
"MU 22,22,20\nMU 23,23,20\nCV 22,22,23\nAW 59,22\nAR 22,60\nv_and_b32 v23, 0xffff0000, v22\nv_lshlrev_b32 v22, 16, v22\nMU 22,22,21\nMU 23,23,21\nCV 22,22,23\nAW 60,22\nAR 22,61\n"
"v_and_b32 v23, 0xffff0000, v22\nv_lshlrev_b32 v22, 16, v22\nMU 22,22,21\nMU 23,23,21\nCV 22,22,23\nAW 61,22\nAR 22,62\nv_and_b32 v23, 0xffff0000, v22\nv_lshlrev_b32 v22, 16, v22\n"
"MU 22,22,21\nMU 23,23,21\nCV 22,22,23\nAW 62,22\nAR 22,63\nv_and_b32 v23, 0xffff0000, v22\nv_lshlrev_b32 v22, 16, v22\nMU 22,22,21\nMU 23,23,21\nCV 22,22,23\nAW 63,22\n"
"v_lshrrev_b32 v22, 8, v1\nv_bfe_u32 v23, v1, 6, 2\nv_cmp_eq_u32 vcc, v22, v23\nv_and_b32 v22, 16, v1\nv_mov_b32 v23, 0x3f80\nv_cndmask_b32 v23, 0, v23, vcc\n"
"v_lshlrev_b32 v23, v22, v23\nv_and_b32 v22, 32, v1\nv_cmp_eq_u32 vcc, 0, v22\nv_mov_b32 v112, 0\nv_mov_b32 v113, 0\nv_cndmask_b32 v114, 0, v23, vcc\nv_sub_u32 v115, v23, v114\n"
"v_mov_b32 v116, 0\nv_mov_b32 v117, 0\ns_nop 1\nMvva 128,114,0\nMvva 136,112,0\nMvva 140,112,4\nMvva 132,114,4\nMvva 144,114,8\nMvva 152,112,8\nMvva 156,112,12\nMvva 148,114,12\n"
"Mvva 160,114,16\nMvva 168,112,16\nMvva 172,112,20\nMvva 164,114,20\nMvva 176,114,24\nMvva 184,112,24\nMvva 188,112,28\nMvva 180,114,28\nMvva 192,114,32\nMvva 200,112,32\n"
"Mvva 204,112,36\nMvva 196,114,36\nMvva 208,114,40\nMvva 216,112,40\nMvva 220,112,44\nMvva 212,114,44\nMvva 224,114,48\nMvva 232,112,48\nMvva 236,112,52\nMvva 228,114,52\n"
"Mvva 240,114,56\nMvva 248,112,56\nMvva 252,112,60\nMvva 244,114,60\ns_nop 7\ns_branch .T88\n.S90:\ns_mov_b32 s7, 262144\n.S92:\ns_add_u32 s0, s92, 0\n"
"buffer_load_dwordx4 v[208:211], v1, s[16:19], s0 offen offset:0 sc0 sc1\nbuffer_load_dwordx4 v[212:215], v1, s[16:19], s0 offen offset:1024 sc0 sc1\nWV 0\n"
"v_max_u32 v21, v211, v215\nv_cmp_eq_u32 vcc, -1, v21\ns_nop 0\ns_cbranch_vccz .S91\ns_sleep 6\ns_sub_u32 s7, s7, 1\ns_cbranch_scc0 .S92\ns_branch .S91\n.S93:\n"
"s_mov_b32 s7, 262144\n.S95:\ns_add_u32 s0, s92, 131072\nbuffer_load_dwordx4 v[216:219], v1, s[16:19], s0 offen offset:0 sc0 sc1\n"
"buffer_load_dwordx4 v[220:223], v1, s[16:19], s0 offen offset:1024 sc0 sc1\nWV 0\nv_max_u32 v21, v219, v223\nv_cmp_eq_u32 vcc, -1, v21\ns_nop 0\ns_cbranch_vccz .S94\ns_sleep 6\n"
"s_sub_u32 s7, s7, 1\ns_cbranch_scc0 .S95\ns_branch .S94\n.S96:\ns_mov_b32 s7, 262144\n.S98:\ns_add_u32 s0, s92, 262144\n"
"buffer_load_dwordx4 v[224:227], v1, s[16:19], s0 offen offset:0 sc0 sc1\nbuffer_load_dwordx4 v[228:231], v1, s[16:19], s0 offen offset:1024 sc0 sc1\nWV 0\n"
"v_max_u32 v21, v227, v231\nv_cmp_eq_u32 vcc, -1, v21\ns_nop 0\ns_cbranch_vccz .S97\ns_sleep 6\ns_sub_u32 s7, s7, 1\ns_cbranch_scc0 .S98\ns_branch .S97\n.S99:\n"
"s_mov_b32 s7, 262144\n.S101:\ns_add_u32 s0, s92, 393216\nbuffer_load_dwordx4 v[208:211], v1, s[16:19], s0 offen offset:0 sc0 sc1\n"
"buffer_load_dwordx4 v[212:215], v1, s[16:19], s0 offen offset:1024 sc0 sc1\nWV 0\nv_max_u32 v21, v211, v215\nv_cmp_eq_u32 vcc, -1, v21\ns_nop 0\ns_cbranch_vccz .S100\ns_sleep 6\n"
"s_sub_u32 s7, s7, 1\ns_cbranch_scc0 .S101\ns_branch .S100\n.S102:\ns_mov_b32 s7, 262144\n.S104:\ns_add_u32 s0, s92, 524288\n"
"buffer_load_dwordx4 v[216:219], v1, s[16:19], s0 offen offset:0 sc0 sc1\nbuffer_load_dwordx4 v[220:223], v1, s[16:19], s0 offen offset:1024 sc0 sc1\nWV 0\n"
"v_max_u32 v21, v219, v223\nv_cmp_eq_u32 vcc, -1, v21\ns_nop 0\ns_cbranch_vccz .S103\ns_sleep 6\ns_sub_u32 s7, s7, 1\ns_cbranch_scc0 .S104\ns_branch .S103\n.S105:\n"
"s_mov_b32 s7, 262144\n.S107:\ns_add_u32 s0, s92, 655360\nbuffer_load_dwordx4 v[224:227], v1, s[16:19], s0 offen offset:0 sc0 sc1\n"
"buffer_load_dwordx4 v[228:231], v1, s[16:19], s0 offen offset:1024 sc0 sc1\nWV 0\nv_max_u32 v21, v227, v231\nv_cmp_eq_u32 vcc, -1, v21\ns_nop 0\ns_cbranch_vccz .S106\ns_sleep 6\n"
"s_sub_u32 s7, s7, 1\ns_cbranch_scc0 .S107\ns_branch .S106\n.S108:\ns_mov_b32 s7, 262144\n.S110:\ns_add_u32 s0, s92, 786432\n"
"buffer_load_dwordx4 v[208:211], v1, s[16:19], s0 offen offset:0 sc0 sc1\nbuffer_load_dwordx4 v[212:215], v1, s[16:19], s0 offen offset:1024 sc0 sc1\nWV 0\n"
"v_max_u32 v21, v211, v215\nv_cmp_eq_u32 vcc, -1, v21\ns_nop 0\ns_cbranch_vccz .S109\ns_sleep 6\ns_sub_u32 s7, s7, 1\ns_cbranch_scc0 .S110\ns_branch .S109\n.S111:\n"
"s_mov_b32 s7, 262144\n.S113:\nbuffer_load_dwordx4 v[64:67], v89, s[16:19], s40 offen offset:0 sc0 sc1\nbuffer_load_dwordx4 v[68:71], v89, s[16:19], s40 offen offset:16 sc0 sc1\n"
"buffer_load_dwordx4 v[72:75], v89, s[16:19], s40 offen offset:1792 sc0 sc1\nbuffer_load_dwordx4 v[76:79], v89, s[16:19], s40 offen offset:1808 sc0 sc1\n"
"buffer_load_dwordx2 v[80:81], v88, s[16:19], s63 offen offset:0 sc0 sc1\nbuffer_load_dwordx2 v[82:83], v88, s[16:19], s63 offen offset:512 sc0 sc1\nWV 0\nv_xor_b32 v66, s15, v66\n"
"v_xor_b32 v70, s15, v70\nv_xor_b32 v74, s15, v74\nv_xor_b32 v78, s15, v78\nv_xor_b32 v81, s15, v81\nv_xor_b32 v83, s15, v83\nv_or3_b32 v92, v66, v70, v74\n"
"v_or3_b32 v92, v92, v78, v81\nv_or_b32 v92, v92, v83\nv_cmp_ne_u32 vcc, 0, v92\ns_nop 0\ns_cbranch_vccz .S112\ns_sleep 6\ns_sub_u32 s7, s7, 1\ns_cbranch_scc0 .S113\n"
"s_branch .S112\n.S114:\ns_mov_b32 s7, 262144\n.S116:\nbuffer_load_dword v27, off, s[16:19], s60 offset:512 sc0 sc1\nWV 0\nv_cmp_ne_u32 vcc, s15, v27\ns_nop 0\n"
"s_cbranch_vccz .S115\ns_sleep 6\ns_sub_u32 s7, s7, 1\ns_cbranch_scc0 .S116\ns_branch .S115\n.S117:\ns_mov_b32 s7, 262144\n.S119:\n"
"buffer_load_dword v27, v24, s[16:19], s41 offen sc0 sc1\nWV 0\nv_xor_b32 v27, s15, v27\nv_and_b32 v26, 0xffffdfff, v27\nv_cmp_ne_u32 s[64:65], 0, v27\nv_cmp_ne_u32 vcc, 0, v26\n"
"s_nop 0\ns_cbranch_vccz .S118\ns_sleep 6\ns_sub_u32 s7, s7, 1\ns_cbranch_scc0 .S119\ns_branch .S118\n.Z121:\ns_sub_u32 s7, s7, 1\ns_cbranch_scc1 .Z122\ns_cmp_lg_u64 s[44:45], 0\n"
"s_cbranch_scc0 .Z124\nbuffer_load_dwordx4 v[96:99], v1, s[16:19], s24 offen offset:0 sc0 sc1\nbuffer_load_dwordx4 v[100:103], v1, s[16:19], s24 offen offset:1024 sc0 sc1\n.Z124:\n"
"s_cmp_lg_u64 s[46:47], 0\ns_cbranch_scc0 .Z125\nbuffer_load_dwordx4 v[104:107], v1, s[16:19], s25 offen offset:0 sc0 sc1\n"
"buffer_load_dwordx4 v[108:111], v1, s[16:19], s25 offen offset:1024 sc0 sc1\n.Z125:\ns_cmp_lg_u64 s[48:49], 0\ns_cbranch_scc0 .Z126\n"
"buffer_load_dwordx4 v[112:115], v1, s[16:19], s26 offen offset:0 sc0 sc1\nbuffer_load_dwordx4 v[116:119], v1, s[16:19], s26 offen offset:1024 sc0 sc1\n.Z126:\n"
"s_cmp_lg_u64 s[50:51], 0\ns_cbranch_scc0 .Z127\nbuffer_load_dwordx4 v[120:123], v1, s[16:19], s27 offen offset:0 sc0 sc1\n"
"buffer_load_dwordx4 v[124:127], v1, s[16:19], s27 offen offset:1024 sc0 sc1\n.Z127:\ns_cmp_lg_u64 s[52:53], 0\ns_cbranch_scc0 .Z128\n"
"buffer_load_dwordx4 v[32:35], v1, s[16:19], s32 offen offset:0 sc0 sc1\nbuffer_load_dwordx4 v[36:39], v1, s[16:19], s32 offen offset:1024 sc0 sc1\n.Z128:\n"
"s_cmp_lg_u64 s[54:55], 0\ns_cbranch_scc0 .Z129\nbuffer_load_dwordx4 v[40:43], v1, s[16:19], s33 offen offset:0 sc0 sc1\n"
"buffer_load_dwordx4 v[44:47], v1, s[16:19], s33 offen offset:1024 sc0 sc1\n.Z129:\ns_cmp_lg_u64 s[56:57], 0\ns_cbranch_scc0 .Z130\n"
"buffer_load_dwordx4 v[48:51], v1, s[16:19], s34 offen offset:0 sc0 sc1\nbuffer_load_dwordx4 v[52:55], v1, s[16:19], s34 offen offset:1024 sc0 sc1\n.Z130:\n"
"s_cmp_lg_u64 s[58:59], 0\ns_cbranch_scc0 .Z131\nbuffer_load_dwordx4 v[56:59], v1, s[16:19], s35 offen offset:0 sc0 sc1\n"
"buffer_load_dwordx4 v[60:63], v1, s[16:19], s35 offen offset:1024 sc0 sc1\n.Z131:\ns_sleep 8\nWV 0\ns_branch .Z123\n.RA10:\ns_nop 7\ns_nop 3\nv_max3_f32 v22, v32, v33, v34\n"
"v_max3_f32 v22, v22, v35, v40\nv_max3_f32 v22, v22, v41, v42\nv_max_f32 v22, v22, v43\nv_mov_b32 v91, v22\ns_nop 1\nv_permlane32_swap_b32 v22, v91\nv_max_f32 v22, v22, v91\n"
"v_mov_b32 v91, v22\ns_nop 1\nv_permlane16_swap_b32 v22, v91\nv_max_f32 v22, v22, v91\nv_ceil_f32 v22, v22\nv_max3_f32 v23, v36, v37, v38\nv_max3_f32 v23, v23, v39, v44\n"
"v_max3_f32 v23, v23, v45, v46\nv_max_f32 v23, v23, v47\nv_mov_b32 v91, v23\ns_nop 1\nv_permlane32_swap_b32 v23, v91\nv_max_f32 v23, v23, v91\nv_mov_b32 v91, v23\ns_nop 1\n"
"v_permlane16_swap_b32 v23, v91\nv_max_f32 v23, v23, v91\nv_ceil_f32 v23, v23\ns_bitcmp1_b32 s62, 0\ns_cbranch_scc0 .R133\nv_cmp_lt_f32 vcc, 0xff000000, v22\ns_nop 1\n"
"v_cndmask_b32 v22, 0, v22, vcc\nv_cmp_lt_f32 vcc, 0xff000000, v23\ns_nop 1\nv_cndmask_b32 v23, 0, v23, vcc\ns_branch .R134\n.R133:\nv_max_f32 v22, 0, v22\nv_max_f32 v23, 0, v23\n"
"s_bitcmp1_b32 s62, 3\ns_cbranch_scc0 .R134\nv_max_f32 v22, v20, v22\nv_max_f32 v23, v21, v23\n.R134:\nAD 88,88,22\nAD 89,89,23\nSU 32,32,22\nSU 33,33,22\nSU 34,34,22\nSU 35,35,22\n"
"SU 36,36,23\nSU 37,37,23\nSU 38,38,23\nSU 39,39,23\nSU 40,40,22\nSU 41,41,22\nSU 42,42,22\nSU 43,43,22\nSU 44,44,23\nSU 45,45,23\nSU 46,46,23\nSU 47,47,23\nv_mov_b32 v90, v80\n"
"v_mov_b32 v91, v81\nEX 80,32\nEX 81,33\ns_nop 0\nAD 84,80,81\nCV 64,80,81\nEX 82,34\nEX 83,35\nAD 84,84,82\nAD 84,84,83\nCV 65,82,83\nEX 80,36\nEX 81,37\ns_nop 0\nAD 85,80,81\n"
"CV 68,80,81\nEX 82,38\nEX 83,39\nAD 85,85,82\nAD 85,85,83\nCV 69,82,83\nEX 80,40\nEX 81,41\nAD 84,84,80\nAD 84,84,81\nCV 66,80,81\nEX 82,42\nEX 83,43\nAD 84,84,82\nAD 84,84,83\n"
"CV 67,82,83\nEX 80,44\nEX 81,45\nAD 85,85,80\nAD 85,85,81\nCV 70,80,81\nEX 82,46\nEX 83,47\nAD 85,85,82\nAD 85,85,83\nCV 71,82,83\nv_mov_b32 v80, v90\nv_mov_b32 v81, v91\n"
"s_bitcmp1_b32 s62, 1\ns_cbranch_scc1 .R135\nSU 48,48,22\nSU 49,49,22\nSU 50,50,22\nSU 51,51,22\nSU 52,52,23\nSU 53,53,23\nSU 54,54,23\nSU 55,55,23\nSU 56,56,22\nSU 57,57,22\n"
"SU 58,58,22\nSU 59,59,22\nSU 60,60,23\nSU 61,61,23\nSU 62,62,23\nSU 63,63,23\nSU 4,4,22\nSU 5,5,22\nSU 6,6,22\nSU 7,7,22\nSU 8,8,23\nSU 9,9,23\nSU 10,10,23\nSU 11,11,23\n"
"SU 12,12,22\nSU 13,13,22\nSU 14,14,22\nSU 15,15,22\nSU 16,16,23\nSU 17,17,23\nSU 18,18,23\nSU 19,19,23\nEX 80,48\nEX 81,49\n.R135:\ns_bitcmp1_b32 s62, 0\ns_cbranch_scc0 .RS12\n"
"s_nop 1\ns_setpc_b64 s[80:81]\n.RB11:\ns_nop 7\ns_nop 3\nv_max3_f32 v22, v48, v49, v50\nv_max3_f32 v22, v22, v51, v56\nv_max3_f32 v22, v22, v57, v58\nv_max_f32 v22, v22, v59\n"
"v_mov_b32 v91, v22\ns_nop 1\nv_permlane32_swap_b32 v22, v91\nv_max_f32 v22, v22, v91\nv_mov_b32 v91, v22\ns_nop 1\nv_permlane16_swap_b32 v22, v91\nv_max_f32 v22, v22, v91\n"
"v_ceil_f32 v22, v22\nv_max3_f32 v23, v52, v53, v54\nv_max3_f32 v23, v23, v55, v60\nv_max3_f32 v23, v23, v61, v62\nv_max_f32 v23, v23, v63\nv_mov_b32 v91, v23\ns_nop 1\n"
"v_permlane32_swap_b32 v23, v91\nv_max_f32 v23, v23, v91\nv_mov_b32 v91, v23\ns_nop 1\nv_permlane16_swap_b32 v23, v91\nv_max_f32 v23, v23, v91\nv_ceil_f32 v23, v23\n"
"s_bitcmp1_b32 s62, 0\ns_cbranch_scc0 .R138\nv_cmp_lt_f32 vcc, 0xff000000, v22\nv_cndmask_b32 v22, 0, v22, vcc\nv_cmp_lt_f32 vcc, 0xff000000, v23\nv_cndmask_b32 v23, 0, v23, vcc\n"
"s_branch .R139\n.R138:\nv_max_f32 v22, 0, v22\nv_max_f32 v23, 0, v23\ns_bitcmp1_b32 s62, 3\ns_cbranch_scc0 .R139\nv_max_f32 v22, v20, v22\nv_max_f32 v23, v21, v23\n.R139:\n"
"AD 88,88,22\nAD 89,89,23\nSU 48,48,22\nSU 49,49,22\nSU 50,50,22\nSU 51,51,22\nSU 52,52,23\nSU 53,53,23\nSU 54,54,23\nSU 55,55,23\nSU 56,56,22\nSU 57,57,22\nSU 58,58,22\n"
"SU 59,59,22\nSU 60,60,23\nSU 61,61,23\nSU 62,62,23\nSU 63,63,23\nv_mov_b32 v90, v80\nv_mov_b32 v91, v81\nEX 80,48\nEX 81,49\ns_nop 0\nAD 84,80,81\nCV 72,80,81\nEX 82,50\nEX 83,51\n"
"AD 84,84,82\nAD 84,84,83\nCV 73,82,83\nEX 80,52\nEX 81,53\ns_nop 0\nAD 85,80,81\nCV 76,80,81\nEX 82,54\nEX 83,55\nAD 85,85,82\nAD 85,85,83\nCV 77,82,83\nEX 80,56\nEX 81,57\n"
"AD 84,84,80\nAD 84,84,81\nCV 74,80,81\nEX 82,58\nEX 83,59\nAD 84,84,82\nAD 84,84,83\nCV 75,82,83\nEX 80,60\nEX 81,61\nAD 85,85,80\nAD 85,85,81\nCV 78,80,81\nEX 82,62\nEX 83,63\n"
"AD 85,85,82\nAD 85,85,83\nCV 79,82,83\nv_mov_b32 v80, v90\nv_mov_b32 v81, v91\ns_bitcmp1_b32 s62, 1\ns_cbranch_scc1 .R140\nSU 32,32,22\nSU 33,33,22\nSU 34,34,22\nSU 35,35,22\n"
"SU 36,36,23\nSU 37,37,23\nSU 38,38,23\nSU 39,39,23\nSU 40,40,22\nSU 41,41,22\nSU 42,42,22\nSU 43,43,22\nSU 44,44,23\nSU 45,45,23\nSU 46,46,23\nSU 47,47,23\nSU 4,4,22\nSU 5,5,22\n"
"SU 6,6,22\nSU 7,7,22\nSU 8,8,23\nSU 9,9,23\nSU 10,10,23\nSU 11,11,23\nSU 12,12,22\nSU 13,13,22\nSU 14,14,22\nSU 15,15,22\nSU 16,16,23\nSU 17,17,23\nSU 18,18,23\nSU 19,19,23\n"
"EX 80,32\nEX 81,33\n.R140:\ns_bitcmp1_b32 s62, 0\ns_cbranch_scc0 .RS12\ns_nop 1\ns_setpc_b64 s[80:81]\n.RS12:\nv_exp_f32 v22, -v22\nv_exp_f32 v23, -v23\nMU 86,86,22\nMU 87,87,23\n"
"MU 128,128,22\nMU 129,129,22\nMU 130,130,22\nMU 131,131,22\nMU 132,132,23\nMU 133,133,23\nMU 134,134,23\nMU 135,135,23\nMU 136,136,22\nMU 137,137,22\nMU 138,138,22\nMU 139,139,22\n"
"MU 140,140,23\nMU 141,141,23\nMU 142,142,23\nMU 143,143,23\nMU 144,144,22\nMU 145,145,22\nMU 146,146,22\nMU 147,147,22\nMU 148,148,23\nMU 149,149,23\nMU 150,150,23\nMU 151,151,23\n"
"MU 152,152,22\nMU 153,153,22\nMU 154,154,22\nMU 155,155,22\nMU 156,156,23\nMU 157,157,23\nMU 158,158,23\nMU 159,159,23\nMU 160,160,22\nMU 161,161,22\nMU 162,162,22\nMU 163,163,22\n"
"MU 164,164,23\nMU 165,165,23\nMU 166,166,23\nMU 167,167,23\nMU 168,168,22\nMU 169,169,22\nMU 170,170,22\nMU 171,171,22\nMU 172,172,23\nMU 173,173,23\nMU 174,174,23\nMU 175,175,23\n"
"MU 176,176,22\nMU 177,177,22\nMU 178,178,22\nMU 179,179,22\nMU 180,180,23\nMU 181,181,23\nMU 182,182,23\nMU 183,183,23\nMU 184,184,22\nMU 185,185,22\nMU 186,186,22\nMU 187,187,22\n"
"MU 188,188,23\nMU 189,189,23\nMU 190,190,23\nMU 191,191,23\nMU 192,192,22\nMU 193,193,22\nMU 194,194,22\nMU 195,195,22\nMU 196,196,23\nMU 197,197,23\nMU 198,198,23\nMU 199,199,23\n"
"MU 200,200,22\nMU 201,201,22\nMU 202,202,22\nMU 203,203,22\nMU 204,204,23\nMU 205,205,23\nMU 206,206,23\nMU 207,207,23\nMU 208,208,22\nMU 209,209,22\nMU 210,210,22\nMU 211,211,22\n"
"MU 212,212,23\nMU 213,213,23\nMU 214,214,23\nMU 215,215,23\nMU 216,216,22\nMU 217,217,22\nMU 218,218,22\nMU 219,219,22\nMU 220,220,23\nMU 221,221,23\nMU 222,222,23\nMU 223,223,23\n"
"MU 224,224,22\nMU 225,225,22\nMU 226,226,22\nMU 227,227,22\nMU 228,228,23\nMU 229,229,23\nMU 230,230,23\nMU 231,231,23\nMU 232,232,22\nMU 233,233,22\nMU 234,234,22\nMU 235,235,22\n"
"MU 236,236,23\nMU 237,237,23\nMU 238,238,23\nMU 239,239,23\nMU 240,240,22\nMU 241,241,22\nMU 242,242,22\nMU 243,243,22\nMU 244,244,23\nMU 245,245,23\nMU 246,246,23\nMU 247,247,23\n"
"MU 248,248,22\nMU 249,249,22\nMU 250,250,22\nMU 251,251,22\nMU 252,252,23\nMU 253,253,23\nMU 254,254,23\nMU 255,255,23\ns_nop 1\ns_setpc_b64 s[80:81]\n.p2align 12\n.PA3:\n"
"v_add_u32 v128, 0x10000, v12\nv_add_u32 v129, 0x10000, v13\nv_add_u32 v130, 0x10000, v14\nv_add_u32 v131, 0x10000, v15\nv_add_u32 v132, 0x10000, v16\nv_add_u32 v133, 0x10000, v17\n"
"v_add_u32 v134, 0x10000, v18\nv_add_u32 v135, 0x10000, v19\ns_add_u32 s2, s11, 1\ns_lshr_b32 s2, s2, 1\ns_lshl_b32 s2, s2, 12\nv_add_u32 v140, s2, v10\nv_add_u32 v141, s2, v11\n"
"v_xor_b32 v142, 0x1000, v140\nv_xor_b32 v143, 0x1000, v141\ns_add_u32 m0, s47, 0\ns_add_u32 s46, s51, 524288\nD0 6,32,46,0\nD0 7,32,46,1024\nD0 8,32,46,2048\nD0 9,32,46,3072\n"
"s_add_u32 m0, s47, -49152\ns_add_u32 s46, s52, 128\nD0 136,28,46,0\nD0 137,28,46,1024\nD0 138,28,46,2048\nD0 139,28,46,3072\ns_add_u32 m0, s47, 16384\ns_add_u32 s46, s51, 786432\n"
"D0 6,32,46,0\nD0 7,32,46,1024\nD0 8,32,46,2048\nD0 9,32,46,3072\nv_mov_b32 v116, 0x40000000\nv_cmp_gt_u32 vcc, 56, v0\ns_lshl2_add_u32 s2, s10, s11\ns_mul_i32 s2, s2, 1792\n"
"v_cndmask_b32 v117, v116, v1, vcc\ns_mov_b32 s36, s56\ns_and_b32 s37, s57, 0xffff\ns_mov_b32 s38, 0x380\ns_mov_b32 s39, 0x20000\ns_mov_b32 s40, s58\ns_and_b32 s41, s59, 0xffff\n"
"s_mov_b32 s42, 0x380\ns_mov_b32 s43, 0x20000\nbuffer_load_dwordx4 v[96:99], v117, s[28:31], s2 offen offset:0\nbuffer_load_dwordx4 v[100:103], v117, s[28:31], s2 offen offset:896\n"
"buffer_load_dwordx4 v[104:107], v117, s[36:39], 0 offen offset:0\nbuffer_load_dwordx4 v[108:111], v117, s[40:43], 0 offen offset:0\ns_add_u32 m0, s47, 32768\n"
"s_add_u32 s46, s51, 1048576\nD0 6,32,46,0\nD0 7,32,46,1024\nD0 8,32,46,2048\nD0 9,32,46,3072\nWL 0\ns_mul_i32 s15, s15, 1664525\ns_add_u32 s15, s15, 1013904223\n"
"v_mov_b32 v25, s15\ns_mov_b32 s57, 0x3db8aa3b\ns_and_b32 s59, s11, 1\ns_lshl_b32 s59, s59, 13\ns_mul_i32 s2, s73, 25165824\ns_and_b32 s3, s12, 1\ns_lshl_b32 s3, s3, 21\n"
"s_add_u32 s2, s2, s3\ns_lshl2_add_u32 s3, s13, s11\ns_lshl_b32 s3, s3, 15\ns_add_u32 s2, s2, s3\ns_add_u32 s56, s2, 0x800000\ns_xor_b32 s53, s56, 0xc000\ns_mov_b64 exec, 0\n"
"s_cmp_eq_u32 s11, 1\ns_cbranch_scc1 .W553\ns_cmp_eq_u32 s11, 2\ns_cbranch_scc1 .W554\ns_cmp_eq_u32 s11, 3\ns_cbranch_scc1 .W555\ns_branch .W552\n.WB151:\ns_mov_b64 exec, -1\n"
"WV 20\ns_barrier\nRa 192,140,0\nRa 196,141,0\nRa 200,140,2048\nRa 204,141,2048\nRa 208,142,0\nRa 212,143,0\nRa 216,142,2048\nRa 220,143,2048\nTa 160,128,0\nTa 162,128,2048\n"
"Ta 164,128,16384\nTa 166,128,18432\nTa 168,129,0\nTa 170,129,2048\nTa 172,129,16384\nTa 174,129,18432\nTa 176,130,0\nTa 178,130,2048\nTa 180,130,16384\nTa 182,130,18432\n"
"Ta 184,131,0\nTa 186,131,2048\nTa 188,131,16384\nTa 190,131,18432\nWL 0\nZvaa 32,160,192\nTa 224,132,0\ns_add_u32 m0, s47, -32768\ns_add_u32 s46, s52, 256\nMvaa 32,164,196\n"
"Ta 226,132,2048\nZaaa 96,164,204\nD0 136,28,46,0\nMaaa 96,160,200\nTa 228,132,16384\nZvaa 192,160,208\nTa 230,132,18432\nMvaa 192,164,212\nZvaa 224,164,220\nTa 232,133,0\n"
"Mvaa 224,160,216\nTa 234,133,2048\nD0 137,28,46,1024\nZvaa 228,168,216\nMvaa 228,172,220\nTa 236,133,16384\nZvaa 196,172,212\nTa 238,133,18432\nMvaa 196,168,208\nZaaa 100,168,200\n"
"Ta 240,134,0\nD0 138,28,46,2048\nMaaa 100,172,204\nTa 242,134,2048\nZvaa 36,172,196\nMvaa 36,168,192\nTa 244,134,16384\nZvaa 40,176,192\nTa 246,134,18432\nMvaa 40,180,196\n"
"D0 139,28,46,3072\nZaaa 104,180,204\nTa 248,135,0\nMaaa 104,176,200\nTa 250,135,2048\nZvaa 200,176,208\ns_add_u32 m0, s47, 49152\ns_add_u32 s46, s51, 1310720\nMvaa 200,180,212\n"
"Ta 252,135,16384\nZvaa 232,180,220\nTa 254,135,18432\nD0 6,32,46,0\nMvaa 232,176,216\nZvaa 236,184,216\nMvaa 236,188,220\nZvaa 204,188,212\nMvaa 204,184,208\nD0 7,32,46,1024\n"
"Zaaa 108,184,200\nMaaa 108,188,204\nZvaa 44,188,196\nMvaa 44,184,192\ns_waitcnt vmcnt(14) lgkmcnt(0)\ns_barrier\nZvaa 48,224,192\nRa 128,140,16384\nD0 8,32,46,2048\n"
"Mvaa 48,228,196\nRa 132,141,16384\nZaaa 112,228,204\nRa 136,140,18432\nMaaa 112,224,200\nRa 140,141,18432\nZvaa 208,224,208\nRa 144,142,16384\nWV 11\nMvaa 208,228,212\n"
"Ra 148,143,16384\nD0 9,32,46,3072\nZvaa 240,228,220\nRa 152,142,18432\nMvaa 240,224,216\nRa 156,143,18432\nZvaa 244,232,216\nTa 160,12,0\ns_add_u32 m0, s47, -16384\n"
"s_add_u32 s46, s52, 384\nMvaa 244,236,220\nTa 162,12,2048\nZvaa 212,236,212\nTa 164,12,16384\nD0 136,28,46,0\nMvaa 212,232,208\nTa 166,12,18432\nZaaa 116,232,200\nTa 168,13,0\n"
"v_lshlrev_b32 v112, 16, v96\nMaaa 116,236,204\nTa 170,13,2048\nZvaa 52,236,196\nTa 172,13,16384\nMvaa 52,232,192\nTa 174,13,18432\nD0 137,28,46,1024\nZvaa 56,240,192\nTa 176,14,0\n"
"v_lshlrev_b32 v113, 16, v100\nMvaa 56,244,196\nTa 178,14,2048\nZaaa 120,244,204\nTa 180,14,16384\nMaaa 120,240,200\nTa 182,14,18432\nZvaa 216,240,208\nTa 184,15,0\n"
"D0 138,28,46,2048\nMvaa 216,244,212\nTa 186,15,2048\nZvaa 248,244,220\nTa 188,15,16384\nMvaa 248,240,216\nTa 190,15,18432\nZvaa 252,248,216\nv_lshlrev_b32 v114, 16, v104\n"
"v_lshlrev_b32 v115, 16, v108\nMvaa 252,252,220\nD0 139,28,46,3072\nZvaa 220,252,212\nMU 116,112,114\nMvaa 220,248,208\nMU 117,113,114\nZaaa 124,248,200\ns_add_u32 m0, s47, 65536\n"
"s_add_u32 s46, s51, 1572864\nMaaa 124,252,204\nCV 116,116,117\nZvaa 60,252,196\nD0 6,32,46,0\nMvaa 60,248,192\nWL 0\nMvaa 32,160,128\nTa 224,16,0\nv_lshlrev_b32 v118, 16, v116\n"
"Mvaa 32,164,132\nTa 226,16,2048\nMaaa 96,164,140\nv_and_b32 v119, 0xffff0000, v116\nMaaa 96,160,136\nTa 228,16,16384\nD0 7,32,46,1024\nMvaa 192,160,144\nTa 230,16,18432\n"
"MU 120,112,115\nMvaa 192,164,148\nMU 121,113,115\nMvaa 224,164,156\nTa 232,17,0\nMvaa 224,160,152\nTa 234,17,2048\nMvaa 228,168,152\nD0 8,32,46,2048\n"
"v_and_b32 v112, 0xffff0000, v96\nMvaa 228,172,156\nTa 236,17,16384\nMvaa 196,172,148\nTa 238,17,18432\nMvaa 196,168,144\nv_and_b32 v113, 0xffff0000, v100\nMaaa 100,168,136\n"
"Ta 240,18,0\nv_and_b32 v114, 0xffff0000, v104\nMaaa 100,172,140\nTa 242,18,2048\nD0 9,32,46,3072\nMvaa 36,172,132\nv_and_b32 v115, 0xffff0000, v108\nMvaa 36,168,128\n"
"Ta 244,18,16384\nMvaa 40,176,128\nTa 246,18,18432\ns_add_u32 m0, s47, 81920\ns_add_u32 s46, s51, 1835008\nMvaa 40,180,132\nMU 116,112,114\nMaaa 104,180,140\nTa 248,19,0\n"
"D0 6,32,46,0\nMaaa 104,176,136\nTa 250,19,2048\nMvaa 200,176,144\nMU 117,113,114\nCV 116,116,117\nMvaa 200,180,148\nTa 252,19,16384\nMvaa 232,180,156\nTa 254,19,18432\n"
"Mvaa 232,176,152\nD0 7,32,46,1024\nMvaa 236,184,152\nv_and_b32 v117, 0xffff0000, v116\nv_lshlrev_b32 v116, 16, v116\nMvaa 236,188,156\nFM 120,112,115\nMvaa 204,188,148\n"
"FM 121,113,115\nMvaa 204,184,144\nAD 118,118,116\nMaaa 108,184,136\nD0 8,32,46,2048\nAD 119,119,117\nMaaa 108,188,140\nv_lshlrev_b32 v112, 16, v97\nMvaa 44,188,132\n"
"v_lshlrev_b32 v113, 16, v101\nMvaa 44,184,128\ns_waitcnt vmcnt(11) lgkmcnt(0)\ns_barrier\nMvaa 48,224,128\nRa 192,140,32768\nv_lshlrev_b32 v114, 16, v105\nMvaa 48,228,132\n"
"Ra 196,141,32768\nD0 9,32,46,3072\nMaaa 112,228,140\nRa 200,140,34816\nMaaa 112,224,136\nRa 204,141,34816\nMvaa 208,224,144\nRa 208,142,32768\ns_add_u32 m0, s47, -49152\n"
"s_add_u32 s46, s52, 512\nMvaa 208,228,148\nRa 212,143,32768\nMvaa 240,228,156\nRa 216,142,34816\nD0 136,28,46,0\nMvaa 240,224,152\nRa 220,143,34816\nMvaa 244,232,152\n"
"Ta 160,12,32768\nv_lshlrev_b32 v115, 16, v109\nMvaa 244,236,156\nTa 162,12,34816\nMvaa 212,236,148\nTa 164,12,49152\nMvaa 212,232,144\nTa 166,12,51200\nD0 137,28,46,1024\n"
"Maaa 116,232,136\nTa 168,13,32768\nMU 116,112,114\nMaaa 116,236,140\nTa 170,13,34816\nMvaa 52,236,132\nTa 172,13,49152\nMvaa 52,232,128\nTa 174,13,51200\nMvaa 56,240,128\n"
"Ta 176,14,32768\nD0 138,28,46,2048\nMvaa 56,244,132\nTa 178,14,34816\nMaaa 120,244,140\nTa 180,14,49152\nMaaa 120,240,136\nTa 182,14,51200\nMvaa 216,240,144\nTa 184,15,32768\n"
"MU 117,113,114\nMvaa 216,244,148\nTa 186,15,34816\nD0 139,28,46,3072\nMvaa 248,244,156\nTa 188,15,49152\nMvaa 248,240,152\nTa 190,15,51200\nMvaa 252,248,152\ns_add_u32 m0, s47, 0\n"
"s_add_u32 s46, s51, 2097152\nMvaa 252,252,156\nCV 116,116,117\nMvaa 220,252,148\nD0 6,32,46,0\nMvaa 220,248,144\nv_and_b32 v117, 0xffff0000, v116\nMaaa 124,248,136\n"
"v_lshlrev_b32 v116, 16, v116\nFM 120,112,115\nMaaa 124,252,140\nFM 121,113,115\nMvaa 60,252,132\nAD 118,118,116\nMvaa 60,248,128\nWL 0\nMvaa 32,160,192\nTa 224,16,32768\n"
"D0 7,32,46,1024\nMvaa 32,164,196\nTa 226,16,34816\nMaaa 96,164,204\nAD 119,119,117\nMaaa 96,160,200\nTa 228,16,49152\nMvaa 192,160,208\nTa 230,16,51200\n"
"v_and_b32 v112, 0xffff0000, v97\nMvaa 192,164,212\nD0 8,32,46,2048\nMvaa 224,164,220\nTa 232,17,32768\nMvaa 224,160,216\nTa 234,17,34816\nMvaa 228,168,216\n"
"v_and_b32 v113, 0xffff0000, v101\nv_and_b32 v114, 0xffff0000, v105\nMvaa 228,172,220\nTa 236,17,49152\nMvaa 196,172,212\nTa 238,17,51200\nD0 9,32,46,3072\nMvaa 196,168,208\n"
"v_and_b32 v115, 0xffff0000, v109\nMaaa 100,168,200\nTa 240,18,32768\nMU 116,112,114\nMaaa 100,172,204\nTa 242,18,34816\ns_add_u32 m0, s47, 16384\ns_add_u32 s46, s51, 2359296\n"
"Mvaa 36,172,196\nMU 117,113,114\nMvaa 36,168,192\nTa 244,18,49152\nD0 6,32,46,0\nMvaa 40,176,192\nTa 246,18,51200\nCV 116,116,117\nMvaa 40,180,196\n"
"v_and_b32 v117, 0xffff0000, v116\nMaaa 104,180,204\nTa 248,19,32768\nMaaa 104,176,200\nTa 250,19,34816\nMvaa 200,176,208\nD0 7,32,46,1024\nv_lshlrev_b32 v116, 16, v116\n"
"Mvaa 200,180,212\nTa 252,19,49152\nMvaa 232,180,220\nTa 254,19,51200\nMvaa 232,176,216\nFM 120,112,115\nMvaa 236,184,216\nFM 121,113,115\nAD 118,118,116\nMvaa 236,188,220\n"
"D0 8,32,46,2048\nMvaa 204,188,212\nAD 119,119,117\nMvaa 204,184,208\nv_lshlrev_b32 v112, 16, v98\nMaaa 108,184,200\nv_lshlrev_b32 v113, 16, v102\nv_lshlrev_b32 v114, 16, v106\n"
"Maaa 108,188,204\nv_lshlrev_b32 v115, 16, v110\nMvaa 44,188,196\nD0 9,32,46,3072\nMvaa 44,184,192\ns_waitcnt vmcnt(12) lgkmcnt(0)\ns_barrier\nMvaa 48,224,192\nRa 128,140,49152\n"
"MU 116,112,114\nMvaa 48,228,196\nRa 132,141,49152\ns_add_u32 m0, s47, -32768\ns_add_u32 s46, s52, 640\nMaaa 112,228,204\nRa 136,140,51200\nMaaa 112,224,200\nRa 140,141,51200\n"
"D0 136,28,46,0\nMvaa 208,224,208\nRa 144,142,49152\nMU 117,113,114\nMvaa 208,228,212\nRa 148,143,49152\nMvaa 240,228,220\nRa 152,142,51200\nMvaa 240,224,216\nRa 156,143,51200\n"
"Mvaa 244,232,216\nTa 160,128,0\nD0 137,28,46,1024\nMvaa 244,236,220\nTa 162,128,2048\nMvaa 212,236,212\nTa 164,128,16384\nMvaa 212,232,208\nTa 166,128,18432\nMaaa 116,232,200\n"
"Ta 168,129,0\nCV 116,116,117\nMaaa 116,236,204\nTa 170,129,2048\nD0 138,28,46,2048\nMvaa 52,236,196\nTa 172,129,16384\nMvaa 52,232,192\nTa 174,129,18432\nMvaa 56,240,192\n"
"Ta 176,130,0\nv_and_b32 v117, 0xffff0000, v116\nMvaa 56,244,196\nTa 178,130,2048\nMaaa 120,244,204\nTa 180,130,16384\nD0 139,28,46,3072\nMaaa 120,240,200\nTa 182,130,18432\n"
"Mvaa 216,240,208\nTa 184,131,0\nv_lshlrev_b32 v116, 16, v116\nMvaa 216,244,212\nTa 186,131,2048\ns_add_u32 m0, s47, 32768\ns_add_u32 s46, s51, 2621440\nMvaa 248,244,220\n"
"Ta 188,131,16384\nMvaa 248,240,216\nTa 190,131,18432\nD0 6,32,46,0\nMvaa 252,248,216\nFM 120,112,115\nFM 121,113,115\nMvaa 252,252,220\nAD 118,118,116\nMvaa 220,252,212\n"
"AD 119,119,117\nMvaa 220,248,208\nv_and_b32 v112, 0xffff0000, v98\nMaaa 124,248,200\nD0 7,32,46,1024\nv_and_b32 v113, 0xffff0000, v102\nMaaa 124,252,204\n"
"v_and_b32 v114, 0xffff0000, v106\nMvaa 60,252,196\nv_and_b32 v115, 0xffff0000, v110\nMvaa 60,248,192\nWL 0\nMvaa 32,160,128\nTa 224,132,0\n.W552:\ns_cbranch_execz .W556\n"
"MU 116,112,114\nMvaa 32,164,132\nTa 226,132,2048\nD0 8,32,46,2048\nMaaa 96,164,140\nMU 117,113,114\nMaaa 96,160,136\nTa 228,132,16384\nMvaa 192,160,144\nTa 230,132,18432\n"
"CV 116,116,117\nMvaa 192,164,148\nv_and_b32 v117, 0xffff0000, v116\nMvaa 224,164,156\nTa 232,133,0\nD0 9,32,46,3072\nMvaa 224,160,152\nTa 234,133,2048\nMvaa 228,168,152\n"
"v_lshlrev_b32 v116, 16, v116\nFM 120,112,115\nMvaa 228,172,156\nTa 236,133,16384\ns_add_u32 m0, s47, 49152\ns_add_u32 s46, s51, 2883584\nMvaa 196,172,148\nTa 238,133,18432\n"
"Mvaa 196,168,144\nD0 6,32,46,0\nMaaa 100,168,136\nTa 240,134,0\nFM 121,113,115\nMaaa 100,172,140\nTa 242,134,2048\nMvaa 36,172,132\nAD 118,118,116\nMvaa 36,168,128\n"
"Ta 244,134,16384\nMvaa 40,176,128\nTa 246,134,18432\nD0 7,32,46,1024\nMvaa 40,180,132\nAD 119,119,117\nMaaa 104,180,140\nTa 248,135,0\nMaaa 104,176,136\nTa 250,135,2048\n"
"Mvaa 200,176,144\nv_lshlrev_b32 v112, 16, v99\nv_lshlrev_b32 v113, 16, v103\nMvaa 200,180,148\nTa 252,135,16384\nD0 8,32,46,2048\nMvaa 232,180,156\nTa 254,135,18432\n"
"Mvaa 232,176,152\nv_lshlrev_b32 v114, 16, v107\nMvaa 236,184,152\nv_lshlrev_b32 v115, 16, v111\nMU 116,112,114\nMvaa 236,188,156\nMU 117,113,114\nMvaa 204,188,148\n"
"D0 9,32,46,3072\nMvaa 204,184,144\nCV 116,116,117\nMaaa 108,184,136\nv_and_b32 v117, 0xffff0000, v116\nv_lshlrev_b32 v116, 16, v116\ns_and_b32 s78, s74, 3\n"
"s_lshl2_add_u32 s78, s73, s78\ns_lshl_b32 s78, s78, 12\ns_bitcmp1_b32 s11, 0\ns_cselect_b32 s79, s48, s45\ns_add_u32 s79, s79, 512\ns_and_b32 s79, s79, 0xfffe0000\n"
"s_add_u32 s78, s78, s79\nbuffer_load_dword v31, v26, s[20:23], s78 offen\nMaaa 108,188,140\nFM 120,112,115\nMvaa 44,188,132\nFM 121,113,115\nMvaa 44,184,128\n"
"s_waitcnt vmcnt(13) lgkmcnt(0)\ns_barrier\nMvaa 48,224,128\nRa 192,140,16384\ns_add_u32 m0, s47, -16384\ns_add_u32 s46, s52, 768\nMvaa 48,228,132\nRa 196,141,16384\n"
"Maaa 112,228,140\nRa 200,140,18432\nD0 136,28,46,0\nMaaa 112,224,136\nRa 204,141,18432\nMvaa 208,224,144\nRa 208,142,16384\nAD 118,118,116\nMvaa 208,228,148\nRa 212,143,16384\n"
"Mvaa 240,228,156\nRa 216,142,18432\nMvaa 240,224,152\nRa 220,143,18432\nD0 137,28,46,1024\nMvaa 244,232,152\nTa 160,12,0\nAD 119,119,117\nMvaa 244,236,156\nTa 162,12,2048\n"
"Mvaa 212,236,148\nTa 164,12,16384\nMvaa 212,232,144\nTa 166,12,18432\nMaaa 116,232,136\nTa 168,13,0\nD0 138,28,46,2048\nMaaa 116,236,140\nTa 170,13,2048\nMvaa 52,236,132\n"
"Ta 172,13,16384\nMvaa 52,232,128\nTa 174,13,18432\nMvaa 56,240,128\nTa 176,14,0\nv_and_b32 v112, 0xffff0000, v99\nMvaa 56,244,132\nTa 178,14,2048\nD0 139,28,46,3072\n"
"Maaa 120,244,140\nTa 180,14,16384\nMaaa 120,240,136\nTa 182,14,18432\nMvaa 216,240,144\nTa 184,15,0\ns_add_u32 m0, s47, 65536\ns_add_u32 s46, s51, 3145728\nMvaa 216,244,148\n"
"Ta 186,15,2048\nMvaa 248,244,156\nTa 188,15,16384\nD0 6,32,46,0\nMvaa 248,240,152\nTa 190,15,18432\nMvaa 252,248,152\nv_and_b32 v113, 0xffff0000, v103\n"
"v_and_b32 v114, 0xffff0000, v107\nMvaa 252,252,156\nv_and_b32 v115, 0xffff0000, v111\nMvaa 220,252,148\nMU 116,112,114\nMvaa 220,248,144\nD0 7,32,46,1024\nMaaa 124,248,136\n"
"MU 117,113,114\nCV 116,116,117\nMaaa 124,252,140\nv_and_b32 v117, 0xffff0000, v116\nMvaa 60,252,132\nv_lshlrev_b32 v116, 16, v116\nMvaa 60,248,128\nWL 0\nMvaa 32,160,192\n"
"Ta 224,16,0\nD0 8,32,46,2048\nMvaa 32,164,196\nTa 226,16,2048\nMaaa 96,164,204\nFM 120,112,115\nMaaa 96,160,200\nTa 228,16,16384\nMvaa 192,160,208\nTa 230,16,18432\n"
"FM 121,113,115\nMvaa 192,164,212\nD0 9,32,46,3072\nMvaa 224,164,220\nTa 232,17,0\nMvaa 224,160,216\nTa 234,17,2048\nMvaa 228,168,216\ns_add_u32 m0, s47, 81920\n"
"s_add_u32 s46, s51, 3407872\nMvaa 228,172,220\nTa 236,17,16384\nMvaa 196,172,212\nTa 238,17,18432\nD0 6,32,46,0\nMvaa 196,168,208\nAD 118,118,116\nMaaa 100,168,200\nTa 240,18,0\n"
"AD 119,119,117\nMaaa 100,172,204\nTa 242,18,2048\nMvaa 36,172,196\nv_permlane32_swap_b32 v118, v119\nMvaa 36,168,192\nTa 244,18,16384\nD0 7,32,46,1024\nMvaa 40,176,192\n"
"Ta 246,18,18432\nv_permlane32_swap_b32 v120, v121\nMvaa 40,180,196\nAD 118,118,119\nMaaa 104,180,204\nTa 248,19,0\nMaaa 104,176,200\nTa 250,19,2048\nMvaa 200,176,208\n"
"D0 8,32,46,2048\nAD 120,120,121\nMvaa 200,180,212\nTa 252,19,16384\nMvaa 232,180,220\nTa 254,19,18432\nMvaa 232,176,216\nv_permlane16_swap_b32 v118, v120\nMvaa 236,184,216\n"
"AD 118,118,120\nMvaa 236,188,220\nD0 9,32,46,3072\nMvaa 204,188,212\nv_add_f32_dpp v118, v118, v118 row_ror:8 row_mask:0xf bank_mask:0xf\nMvaa 204,184,208\nMaaa 108,184,200\n"
"v_add_f32_dpp v118, v118, v118 row_ror:4 row_mask:0xf bank_mask:0xf\nMaaa 108,188,204\nMvaa 44,188,196\nv_add_f32_dpp v118, v118, v118 row_ror:2 row_mask:0xf bank_mask:0xf\n"
"Mvaa 44,184,192\ns_waitcnt vmcnt(13) lgkmcnt(0)\ns_barrier\nMvaa 48,224,192\nRa 128,140,32768\ns_add_u32 m0, s47, -65536\ns_add_u32 s45, s45, 128\nMvaa 48,228,196\n"
"Ra 132,141,32768\nMaaa 112,228,204\nRa 136,140,34816\nD0 2,20,45,0\nMaaa 112,224,200\nRa 140,141,34816\nMvaa 208,224,208\nRa 144,142,32768\n"
"v_add_f32_dpp v118, v118, v118 row_ror:1 row_mask:0xf bank_mask:0xf\nMvaa 208,228,212\nRa 148,143,32768\nMvaa 240,228,220\nRa 152,142,34816\nMvaa 240,224,216\nRa 156,143,34816\n"
"D0 3,20,45,1024\nMvaa 244,232,216\nTa 160,12,32768\nCV 118,118,118\nMvaa 244,236,220\nTa 162,12,34816\nMvaa 212,236,212\nTa 164,12,49152\nMvaa 212,232,208\nTa 166,12,51200\n"
"Maaa 116,232,200\nTa 168,13,32768\nD0 4,20,45,2048\nMaaa 116,236,204\nTa 170,13,34816\nMvaa 52,236,196\nTa 172,13,49152\nMvaa 52,232,192\nTa 174,13,51200\nMvaa 56,240,192\n"
"Ta 176,14,32768\nv_lshlrev_b32 v118, 16, v118\nMvaa 56,244,196\nTa 178,14,34816\nD0 5,20,45,3072\nMaaa 120,244,204\nTa 180,14,49152\nMaaa 120,240,200\nTa 182,14,51200\n"
"Mvaa 216,240,208\nTa 184,15,32768\ns_add_u32 m0, s47, 0\ns_add_u32 s44, s44, 0x40000\nMvaa 216,244,212\nTa 186,15,34816\nMvaa 248,244,220\nTa 188,15,49152\nD0 6,24,44,0\n"
"Mvaa 248,240,216\nTa 190,15,51200\nMvaa 252,248,216\nv_mul_f32 v119, 0xbfb8aa3b, v118\nMvaa 252,252,220\nEX 119,119\nMvaa 220,252,212\nv_add_f32 v120, 0, v118\nMvaa 220,248,208\n"
"D0 7,24,44,1024\nMaaa 124,248,200\nv_add_f32 v119, 1.0, v119\nMaaa 124,252,204\nv_rcp_f32 v119, v119\nMvaa 60,252,196\nv_ashrrev_i32 v121, 31, v120\nMvaa 60,248,192\nWL 0\n"
"Mvaa 32,160,128\nTa 224,16,32768\nD0 8,24,44,2048\nMvaa 32,164,132\nTa 226,16,34816\nMaaa 96,164,140\nCV 119,119,119\nMaaa 96,160,136\nTa 228,16,49152\nMvaa 192,160,144\n"
"Ta 230,16,51200\nv_lshlrev_b32 v119, 16, v119\nMvaa 192,164,148\nD0 9,24,44,3072\nMvaa 224,164,156\nTa 232,17,32768\nMvaa 224,160,152\nTa 234,17,34816\nMvaa 228,168,152\n"
"s_add_u32 m0, s47, 16384\ns_add_u32 s44, s44, 0x40000\nMvaa 228,172,156\nTa 236,17,49152\nMvaa 196,172,148\nTa 238,17,51200\nD0 6,24,44,0\nMvaa 196,168,144\n"
"v_or_b32 v121, 0x80000000, v121\nMaaa 100,168,136\nTa 240,18,32768\nv_xor_b32 v120, v120, v121\nMaaa 100,172,140\nTa 242,18,34816\nMvaa 36,172,132\n"
"v_and_b32 v120, 0xffff0000, v120\nMvaa 36,168,128\nTa 244,18,49152\nD0 7,24,44,1024\nMvaa 40,176,128\nTa 246,18,51200\ns_lshl2_add_u32 s2, s10, s11\nMvaa 40,180,132\n"
"v_lshrrev_b32 v112, 5, v0\nMaaa 104,180,140\nTa 248,19,32768\nMaaa 104,176,136\nTa 250,19,34816\nMvaa 200,176,144\nD0 8,24,44,2048\nv_lshl_add_u32 v112, s2, 1, v112\n"
"Mvaa 200,180,148\nTa 252,19,49152\nMvaa 232,180,156\nTa 254,19,51200\nMvaa 232,176,152\nv_sub_u32 v113, 0xffff, v112\nMvaa 236,184,152\nv_or_b32 v120, v120, v113\n"
"v_mov_b32 v121, s15\nMvaa 236,188,156\nD0 9,24,44,3072\nMvaa 204,188,148\nv_and_b32 v113, 31, v0\nMvaa 204,184,144\nv_mov_b32 v116, 0x40000000\nv_cmp_eq_u32 vcc, 16, v113\n"
"v_lshlrev_b32 v114, 2, v112\nv_lshlrev_b32 v115, 10, v113\nv_cndmask_b32 v114, v116, v114, vcc\nv_cndmask_b32 v120, v119, v120, vcc\nMaaa 108,184,136\n"
"v_lshl_add_u32 v115, v112, 3, v115\nv_and_b32 v113, 15, v0\nMaaa 108,188,140\nv_cmp_eq_u32 vcc, 0, v113\ns_mov_b32 s2, 0xc040000\ns_mov_b32 s3, 0xc03e000\n"
"v_cndmask_b32 v115, v116, v115, vcc\nbuffer_store_dwordx2 v[120:121], v115, s[16:19], s2 offen sc0 sc1\nbuffer_store_dword v118, v114, s[16:19], s3 offen sc0 sc1\nMvaa 44,188,132\n"
"Mvaa 44,184,128\ns_waitcnt vmcnt(14) lgkmcnt(0)\ns_barrier\nMvaa 48,224,128\nRa 192,140,49152\ns_add_u32 m0, s47, -49152\ns_add_u32 s48, s48, 128\nMvaa 48,228,132\n"
"Ra 196,141,49152\nMaaa 112,228,140\nRa 200,140,51200\nD0 2,20,48,0\nMaaa 112,224,136\nRa 204,141,51200\nMvaa 208,224,144\nRa 208,142,49152\nMvaa 208,228,148\nRa 212,143,49152\n"
"Mvaa 240,228,156\nRa 216,142,51200\nMvaa 240,224,152\nRa 220,143,51200\nD0 3,20,48,1024\nMvaa 244,232,152\nTa 160,128,0\nMvaa 244,236,156\nTa 162,128,2048\nMvaa 212,236,148\n"
"Ta 164,128,16384\nMvaa 212,232,144\nTa 166,128,18432\nMaaa 116,232,136\nTa 168,129,0\nD0 4,20,48,2048\nMaaa 116,236,140\nTa 170,129,2048\nMvaa 52,236,132\nTa 172,129,16384\n"
"Mvaa 52,232,128\nTa 174,129,18432\nMvaa 56,240,128\nTa 176,130,0\nMvaa 56,244,132\nTa 178,130,2048\nD0 5,20,48,3072\nMaaa 120,244,140\nTa 180,130,16384\nMaaa 120,240,136\n"
"Ta 182,130,18432\nMvaa 216,240,144\nTa 184,131,0\ns_add_u32 m0, s47, -32768\ns_add_u32 s45, s45, 128\nMvaa 216,244,148\nTa 186,131,2048\nMvaa 248,244,156\nTa 188,131,16384\n"
"D0 2,20,45,0\nMvaa 248,240,152\nTa 190,131,18432\nMvaa 252,248,152\nMvaa 252,252,156\nMvaa 220,252,148\nMvaa 220,248,144\nD0 3,20,45,1024\nMaaa 124,248,136\nMaaa 124,252,140\n"
"Mvaa 60,252,132\nMvaa 60,248,128\nWL 0\nMvaa 32,160,192\nTa 224,132,0\nD0 4,20,45,2048\nMvaa 32,164,196\nTa 226,132,2048\nMvaa 36,172,196\nMvaa 36,168,192\nTa 228,132,16384\n"
"Mvaa 40,176,192\nTa 230,132,18432\nMvaa 40,180,196\nD0 5,20,45,3072\nMvaa 44,188,196\nTa 232,133,0\nMvaa 44,184,192\nTa 234,133,2048\nMaaa 108,184,200\ns_add_u32 m0, s47, 32768\n"
"s_add_u32 s44, s44, 0x40000\nMaaa 108,188,204\nTa 236,133,16384\nMaaa 104,180,204\nTa 238,133,18432\nD0 6,24,44,0\nMaaa 104,176,200\nv_mul_f32 v32, s57, v32\n"
"v_mul_f32 v33, s57, v33\nMaaa 100,168,200\nTa 240,134,0\nCV 32,32,33\nMaaa 100,172,204\nTa 242,134,2048\nv_mul_f32 v34, s57, v34\nMaaa 96,164,204\nv_mul_f32 v35, s57, v35\n"
"CV 33,34,35\nMaaa 96,160,200\nTa 244,134,16384\nD0 7,24,44,1024\nMvaa 192,160,208\nTa 246,134,18432\nv_mul_f32 v36, s57, v36\nMvaa 192,164,212\nv_mul_f32 v37, s57, v37\n"
"Mvaa 196,172,212\nTa 248,135,0\nCV 34,36,37\nMvaa 196,168,208\nTa 250,135,2048\nv_mul_f32 v38, s57, v38\nMvaa 200,176,208\nD0 8,24,44,2048\nv_mul_f32 v39, s57, v39\n"
"Mvaa 200,180,212\nTa 252,135,16384\nCV 35,38,39\nMvaa 204,188,212\nTa 254,135,18432\nv_mul_f32 v40, s57, v40\nMvaa 204,184,208\nv_mul_f32 v41, s57, v41\nCV 40,40,41\n"
"Mvaa 236,184,216\nv_mul_f32 v42, s57, v42\nv_mul_f32 v43, s57, v43\nMvaa 236,188,220\nD0 9,24,44,3072\nMvaa 232,180,220\nCV 41,42,43\nv_mul_f32 v44, s57, v44\nMvaa 232,176,216\n"
"v_mul_f32 v45, s57, v45\nCV 42,44,45\nMvaa 228,168,216\nv_mul_f32 v46, s57, v46\nv_mul_f32 v47, s57, v47\nMvaa 228,172,220\nCV 43,46,47\nAR 20,96\nMvaa 224,164,220\nAR 21,97\n"
"v_mul_f32 v20, s57, v20\nMvaa 224,160,216\ns_waitcnt vmcnt(14) lgkmcnt(0)\ns_barrier\nMvaa 48,224,192\ns_add_u32 m0, s47, 49152\ns_add_u32 s44, s44, 0x40000\nRa 128,10,0\n"
"Mvaa 48,228,196\nRa 132,11,0\nMvaa 52,236,196\nD0 6,24,44,0\nRa 136,10,2048\nMvaa 52,232,192\n.W553:\ns_cbranch_execz .W557\nRa 140,11,2048\nv_mul_f32 v21, s57, v21\n"
"Mvaa 56,240,192\nRa 144,10,8192\nCV 36,20,21\nMvaa 56,244,196\nRa 148,11,8192\nAR 20,98\nMvaa 60,252,196\nD0 7,24,44,1024\nRa 152,10,10240\nMvaa 60,248,192\nRa 156,11,10240\n"
"AR 21,99\nMaaa 124,248,200\nTa 160,12,0\nv_mul_f32 v20, s57, v20\nMaaa 124,252,204\nTa 162,12,2048\nMaaa 120,244,204\nD0 8,24,44,2048\nTa 164,12,16384\nMaaa 120,240,200\n"
"Ta 166,12,18432\nv_mul_f32 v21, s57, v21\nMaaa 116,232,200\nTa 168,13,0\nCV 37,20,21\nMaaa 116,236,204\nTa 170,13,2048\nAR 20,100\nMaaa 112,228,204\nD0 9,24,44,3072\n"
"Ta 172,13,16384\nMaaa 112,224,200\nTa 174,13,18432\nAR 21,101\nMvaa 208,224,208\ns_add_u32 m0, s47, -16384\ns_add_u32 s48, s48, 128\nTa 176,14,0\nMvaa 208,228,212\nTa 178,14,2048\n"
"Mvaa 212,236,212\nD0 2,20,48,0\nTa 180,14,16384\nMvaa 212,232,208\nTa 182,14,18432\nv_mul_f32 v20, s57, v20\nMvaa 216,240,208\nTa 184,15,0\nv_mul_f32 v21, s57, v21\n"
"Mvaa 216,244,212\nTa 186,15,2048\nCV 38,20,21\nMvaa 220,252,212\nD0 3,20,48,1024\nTa 188,15,16384\nMvaa 220,248,208\nTa 190,15,18432\nAR 20,102\nMvaa 252,248,216\nAR 21,103\n"
"v_mul_f32 v20, s57, v20\nMvaa 252,252,220\nv_mul_f32 v21, s57, v21\nMvaa 248,244,220\nD0 4,20,48,2048\nCV 39,20,21\nMvaa 248,240,216\nAR 20,104\nAR 21,105\nMvaa 244,232,216\n"
"v_mul_f32 v20, s57, v20\nv_mul_f32 v21, s57, v21\nMvaa 244,236,220\nCV 44,20,21\nAR 20,106\nMvaa 240,228,220\nD0 5,20,48,3072\nAR 21,107\nMvaa 240,224,216\nWL 0\ns_barrier\n"
"Zvaa 64,128,160\nTa 224,16,0\nv_mul_f32 v20, s57, v20\nMvaa 64,132,164\nTa 226,16,2048\nZvaa 68,140,164\ns_add_u32 m0, s47, -65536\ns_add_u32 s45, s45, 128\nMvaa 68,136,160\n"
"Ta 228,16,16384\nv_mul_f32 v21, s57, v21\nZvaa 128,144,160\nTa 230,16,18432\nCV 45,20,21\nMvaa 128,148,164\nD0 2,20,45,0\nAR 20,108\nZvaa 132,156,164\nTa 232,17,0\nAR 21,109\n"
"Mvaa 132,152,160\nTa 234,17,2048\nv_mul_f32 v20, s57, v20\nZvaa 140,152,168\nv_mul_f32 v21, s57, v21\nCV 46,20,21\nMvaa 140,156,172\nTa 236,17,16384\ns_add_u32 s6, s56, 0\n"
"Sv 32,1,16,6,0\nZvaa 136,148,172\nTa 238,17,18432\nAR 20,110\nMvaa 136,144,168\nAR 21,111\nv_mul_f32 v20, s57, v20\nZvaa 76,136,168\nTa 240,18,0\nv_mul_f32 v21, s57, v21\n"
"Mvaa 76,140,172\nTa 242,18,2048\nCV 47,20,21\nZvaa 72,132,172\nD0 3,20,45,1024\nv_mul_f32 v48, s57, v48\nMvaa 72,128,168\nTa 244,18,16384\nv_mul_f32 v49, s57, v49\n"
"Zvaa 80,128,176\nTa 246,18,18432\nCV 48,48,49\nMvaa 80,132,180\nv_mul_f32 v50, s57, v50\nZvaa 84,140,180\nTa 248,19,0\nv_mul_f32 v51, s57, v51\nMvaa 84,136,176\nTa 250,19,2048\n"
"CV 49,50,51\nZvaa 144,144,176\nv_mul_f32 v52, s57, v52\nv_mul_f32 v53, s57, v53\nMvaa 144,148,180\nTa 252,19,16384\nCV 50,52,53\nZvaa 148,156,180\nTa 254,19,18432\n"
"v_mul_f32 v54, s57, v54\nMvaa 148,152,176\nD0 4,20,45,2048\nv_mul_f32 v55, s57, v55\nZvaa 156,152,184\nCV 51,54,55\nv_mul_f32 v56, s57, v56\nMvaa 156,156,188\n"
"v_mul_f32 v57, s57, v57\ns_add_u32 s6, s56, 0\nSv 40,1,16,6,2048\nZvaa 152,148,188\nCV 56,56,57\nv_mul_f32 v58, s57, v58\nMvaa 152,144,184\nv_mul_f32 v59, s57, v59\nCV 57,58,59\n"
"Zvaa 92,136,184\nv_mul_f32 v60, s57, v60\nv_mul_f32 v61, s57, v61\nMvaa 92,140,188\nD0 5,20,45,3072\nCV 58,60,61\nZvaa 88,132,188\nv_mul_f32 v62, s57, v62\n"
"v_mul_f32 v63, s57, v63\nMvaa 88,128,184\ns_waitcnt vmcnt(18) lgkmcnt(0)\ns_barrier\nZvaa 96,128,224\nRa 192,10,16384\nCV 59,62,63\nMvaa 96,132,228\ns_add_u32 m0, s47, 0\n"
"s_add_u32 s44, s44, 0x40000\nZvaa 100,140,228\nRa 196,11,16384\nAR 20,112\nMvaa 100,136,224\nAR 21,113\nv_mul_f32 v20, s57, v20\nZvaa 160,144,224\nRa 200,10,18432\n"
"v_mul_f32 v21, s57, v21\nMvaa 160,148,228\nD0 6,24,44,0\nCV 52,20,21\nZvaa 164,156,228\nRa 204,11,18432\nAR 20,114\nMvaa 164,152,224\nAR 21,115\nv_mul_f32 v20, s57, v20\n"
"Zvaa 172,152,232\nRa 208,10,24576\nv_mul_f32 v21, s57, v21\nMvaa 172,156,236\nCV 53,20,21\ns_add_u32 s6, s56, 0\nSv 36,1,16,6,1024\nZvaa 168,148,236\nRa 212,11,24576\nAR 20,116\n"
"Mvaa 168,144,232\nAR 21,117\nv_mul_f32 v20, s57, v20\nZvaa 108,136,232\nRa 216,10,26624\nv_mul_f32 v21, s57, v21\nMvaa 108,140,236\nD0 7,24,44,1024\nCV 54,20,21\nZvaa 104,132,236\n"
"Ra 220,11,26624\nAR 20,118\nMvaa 104,128,232\nAR 21,119\nv_mul_f32 v20, s57, v20\nZvaa 112,128,240\nv_mul_f32 v21, s57, v21\nCV 55,20,21\nMvaa 112,132,244\nAR 20,120\n"
"Zvaa 116,140,244\nAR 21,121\nv_mul_f32 v20, s57, v20\nMvaa 116,136,240\nv_mul_f32 v21, s57, v21\nCV 60,20,21\nZvaa 176,144,240\nAR 20,122\nAR 21,123\nMvaa 176,148,244\n"
"D0 8,24,44,2048\nv_mul_f32 v20, s57, v20\nZvaa 180,156,244\nv_mul_f32 v21, s57, v21\nCV 61,20,21\nMvaa 180,152,240\nAR 20,124\nAR 21,125\nZvaa 188,152,248\n"
"v_mul_f32 v20, s57, v20\nv_mul_f32 v21, s57, v21\nMvaa 188,156,252\nCV 62,20,21\ns_add_u32 s6, s56, 0\nSv 44,1,16,6,3072\nZvaa 184,148,252\nAR 20,126\nAR 21,127\nMvaa 184,144,248\n"
"v_mul_f32 v20, s57, v20\nv_mul_f32 v21, s57, v21\nZvaa 124,136,248\nCV 63,20,21\nv_mul_f32 v192, s57, v192\nMvaa 124,140,252\nD0 9,24,44,3072\nv_mul_f32 v193, s57, v193\n"
"Zvaa 120,132,252\nCV 192,192,193\nv_mul_f32 v194, s57, v194\nMvaa 120,128,248\nWL 0\nZaaa 32,192,224\nRa 128,10,32768\nv_mul_f32 v195, s57, v195\nMaaa 32,196,228\n"
"s_add_u32 m0, s47, 16384\ns_add_u32 s44, s44, 0x40000\nZaaa 36,204,228\nRa 132,11,32768\nCV 193,194,195\nMaaa 36,200,224\nv_mul_f32 v196, s57, v196\nv_mul_f32 v197, s57, v197\n"
"Zaaa 96,208,224\nRa 136,10,34816\nCV 194,196,197\nMaaa 96,212,228\nD0 6,24,44,0\nv_mul_f32 v198, s57, v198\nZaaa 100,220,228\nRa 140,11,34816\nv_mul_f32 v199, s57, v199\n"
"Maaa 100,216,224\nCV 195,198,199\nv_mul_f32 v200, s57, v200\nZaaa 108,216,232\nRa 144,10,40960\nv_mul_f32 v201, s57, v201\nMaaa 108,220,236\nCV 200,200,201\n"
"v_mul_f32 v202, s57, v202\ns_add_u32 s6, s56, 4096\nSv 48,1,16,6,0\nZaaa 104,212,236\nRa 148,11,40960\nv_mul_f32 v203, s57, v203\nMaaa 104,208,232\nCV 201,202,203\n"
"v_mul_f32 v204, s57, v204\nZaaa 44,200,232\nRa 152,10,43008\nv_mul_f32 v205, s57, v205\nMaaa 44,204,236\nD0 7,24,44,1024\nCV 202,204,205\nZaaa 40,196,236\nRa 156,11,43008\n"
"v_mul_f32 v206, s57, v206\nMaaa 40,192,232\nv_mul_f32 v207, s57, v207\nCV 203,206,207\nZaaa 48,192,240\nv_mul_f32 v208, s57, v208\nv_mul_f32 v209, s57, v209\nMaaa 48,196,244\n"
"CV 208,208,209\nv_mul_f32 v210, s57, v210\nZaaa 52,204,244\nv_mul_f32 v211, s57, v211\nCV 209,210,211\nMaaa 52,200,240\nv_mul_f32 v212, s57, v212\nv_mul_f32 v213, s57, v213\n"
"Zaaa 112,208,240\nCV 210,212,213\nv_mul_f32 v214, s57, v214\nMaaa 112,212,244\nD0 8,24,44,2048\nv_mul_f32 v215, s57, v215\nZaaa 116,220,244\nCV 211,214,215\n"
"v_mul_f32 v216, s57, v216\nMaaa 116,216,240\nv_mul_f32 v217, s57, v217\nCV 216,216,217\nZaaa 124,216,248\nv_mul_f32 v218, s57, v218\nv_mul_f32 v219, s57, v219\nMaaa 124,220,252\n"
"CV 217,218,219\nv_mul_f32 v220, s57, v220\ns_add_u32 s6, s56, 4096\nSv 56,1,16,6,2048\nZaaa 120,212,252\nv_mul_f32 v221, s57, v221\nCV 218,220,221\nMaaa 120,208,248\n"
"v_mul_f32 v222, s57, v222\nv_mul_f32 v223, s57, v223\nZaaa 60,200,248\nCV 219,222,223\nv_mul_f32 v224, s57, v224\nMaaa 60,204,252\nD0 9,24,44,3072\nv_mul_f32 v225, s57, v225\n"
"Zaaa 56,196,252\nCV 224,224,225\nv_mul_f32 v226, s57, v226\nMaaa 56,192,248\ns_waitcnt vmcnt(22) lgkmcnt(0)\ns_barrier\nZaaa 0,192,160\nTa 224,16,32768\nv_mul_f32 v227, s57, v227\n"
"Maaa 0,196,164\nTa 226,16,34816\nCV 225,226,227\nZaaa 4,204,164\ns_add_u32 m0, s47, -49152\ns_add_u32 s48, s48, 128\nMaaa 4,200,160\nTa 228,16,49152\nv_mul_f32 v228, s57, v228\n"
"Zaaa 64,208,160\nTa 230,16,51200\nv_mul_f32 v229, s57, v229\nMaaa 64,212,164\nD0 2,20,48,0\nCV 226,228,229\nZaaa 68,220,164\nTa 232,17,32768\nv_mul_f32 v230, s57, v230\n"
"Maaa 68,216,160\nTa 234,17,34816\nv_mul_f32 v231, s57, v231\nZaaa 76,216,168\nCV 227,230,231\nv_mul_f32 v232, s57, v232\nMaaa 76,220,172\nTa 236,17,49152\n"
"v_mul_f32 v233, s57, v233\ns_add_u32 s6, s56, 4096\nSv 52,1,16,6,1024\nZaaa 72,212,172\nTa 238,17,51200\nCV 232,232,233\nMaaa 72,208,168\nv_mul_f32 v234, s57, v234\n"
"v_mul_f32 v235, s57, v235\nZaaa 12,200,168\nTa 240,18,32768\nCV 233,234,235\nMaaa 12,204,172\nTa 242,18,34816\nv_mul_f32 v236, s57, v236\nZaaa 8,196,172\nD0 3,20,48,1024\n"
"v_mul_f32 v237, s57, v237\nMaaa 8,192,168\nTa 244,18,49152\nCV 234,236,237\nZaaa 16,192,176\nTa 246,18,51200\nv_mul_f32 v238, s57, v238\nMaaa 16,196,180\n"
"v_mul_f32 v239, s57, v239\nCV 235,238,239\nZaaa 20,204,180\nTa 248,19,32768\nv_mul_f32 v240, s57, v240\nMaaa 20,200,176\nTa 250,19,34816\nv_mul_f32 v241, s57, v241\n"
"Zaaa 80,208,176\nCV 240,240,241\nv_mul_f32 v242, s57, v242\nMaaa 80,212,180\nTa 252,19,49152\nv_mul_f32 v243, s57, v243\nZaaa 84,220,180\nTa 254,19,51200\nCV 241,242,243\n"
"Maaa 84,216,176\nD0 4,20,48,2048\nv_mul_f32 v244, s57, v244\nZaaa 92,216,184\nv_mul_f32 v245, s57, v245\nCV 242,244,245\nMaaa 92,220,188\nv_mul_f32 v246, s57, v246\n"
"v_mul_f32 v247, s57, v247\ns_add_u32 s6, s56, 4096\nSv 60,1,16,6,3072\nZaaa 88,212,188\nCV 243,246,247\nv_mul_f32 v248, s57, v248\nMaaa 88,208,184\nv_mul_f32 v249, s57, v249\n"
"CV 248,248,249\nZaaa 28,200,184\nv_mul_f32 v250, s57, v250\nv_mul_f32 v251, s57, v251\nMaaa 28,204,188\nD0 5,20,48,3072\nCV 249,250,251\nZaaa 24,196,188\n"
"v_mul_f32 v252, s57, v252\nv_mul_f32 v253, s57, v253\nMaaa 24,192,184\nWL 0\nMvaa 96,128,224\nTa 160,12,32768\nCV 250,252,253\nMvaa 96,132,228\nTa 162,12,34816\n"
"v_mul_f32 v254, s57, v254\nMvaa 100,140,228\ns_add_u32 m0, s47, -32768\ns_add_u32 s45, s45, 128\nMvaa 100,136,224\nTa 164,12,49152\nv_mul_f32 v255, s57, v255\nMvaa 160,144,224\n"
"Ta 166,12,51200\nCV 251,254,255\nMvaa 160,148,228\nD0 2,20,45,0\nMvaa 164,156,228\nTa 168,13,32768\nMvaa 164,152,224\nTa 170,13,34816\nMvaa 172,152,232\nMvaa 172,156,236\n"
"Ta 172,13,49152\ns_add_u32 s6, s53, 0\nSv 192,1,16,6,0\nMvaa 168,148,236\nTa 174,13,51200\nMvaa 168,144,232\nMvaa 108,136,232\nTa 176,14,32768\nMvaa 108,140,236\nTa 178,14,34816\n"
"Mvaa 104,132,236\nD0 3,20,45,1024\nMvaa 104,128,232\nTa 180,14,49152\nMvaa 112,128,240\nTa 182,14,51200\nMvaa 112,132,244\nMvaa 116,140,244\nTa 184,15,32768\nMvaa 116,136,240\n"
"Ta 186,15,34816\nMvaa 176,144,240\nMvaa 176,148,244\nTa 188,15,49152\nMvaa 180,156,244\nTa 190,15,51200\nMvaa 180,152,240\nD0 4,20,45,2048\nMvaa 188,152,248\nMvaa 188,156,252\n"
"s_add_u32 s6, s53, 0\nSv 200,1,16,6,2048\nMvaa 184,148,252\nMvaa 184,144,248\nMvaa 124,136,248\nMvaa 124,140,252\nD0 5,20,45,3072\nMvaa 120,132,252\nMvaa 120,128,248\n"
"s_waitcnt vmcnt(24) lgkmcnt(0)\ns_barrier\nMvaa 64,128,160\nRa 192,10,49152\nMvaa 64,132,164\ns_add_u32 m0, s47, 32768\ns_add_u32 s44, s44, 0x40000\nMvaa 68,140,164\n"
"Ra 196,11,49152\nMvaa 68,136,160\nMvaa 128,144,160\nRa 200,10,51200\nMvaa 128,148,164\nD0 6,24,44,0\nMvaa 132,156,164\nRa 204,11,51200\nMvaa 132,152,160\nMvaa 140,152,168\n"
"Ra 208,10,57344\nMvaa 140,156,172\ns_add_u32 s6, s53, 4096\nSv 208,1,16,6,0\nMvaa 136,148,172\nRa 212,11,57344\nMvaa 136,144,168\nMvaa 76,136,168\nRa 216,10,59392\n"
"Mvaa 76,140,172\nD0 7,24,44,1024\nMvaa 72,132,172\nRa 220,11,59392\nMvaa 72,128,168\nMvaa 80,128,176\nMvaa 80,132,180\nMvaa 84,140,180\nMvaa 84,136,176\nMvaa 144,144,176\n"
"Mvaa 144,148,180\n.W554:\ns_cbranch_execz .W558\nD0 8,24,44,2048\nMvaa 148,156,180\nMvaa 148,152,176\nMvaa 156,152,184\nMvaa 156,156,188\ns_add_u32 s6, s53, 4096\n"
"Sv 216,1,16,6,2048\nMvaa 152,148,188\nMvaa 152,144,184\nMvaa 92,136,184\nMvaa 92,140,188\nD0 9,24,44,3072\nMvaa 88,132,188\nMvaa 88,128,184\nWL 0\nMaaa 0,192,160\nRa 128,10,0\n"
"Maaa 0,196,164\ns_add_u32 m0, s47, 49152\ns_add_u32 s44, s44, 0x40000\nMaaa 4,204,164\nRa 132,11,0\nMaaa 4,200,160\nMaaa 64,208,160\nRa 136,10,2048\nMaaa 64,212,164\nD0 6,24,44,0\n"
"Maaa 68,220,164\nRa 140,11,2048\nMaaa 68,216,160\nMaaa 76,216,168\nRa 144,10,8192\nMaaa 76,220,172\ns_add_u32 s6, s53, 0\nSv 224,1,16,6,1024\nMaaa 72,212,172\nRa 148,11,8192\n"
"Maaa 72,208,168\nMaaa 12,200,168\nRa 152,10,10240\nMaaa 12,204,172\nD0 7,24,44,1024\nMaaa 8,196,172\nRa 156,11,10240\nMaaa 8,192,168\nMaaa 16,192,176\nMaaa 16,196,180\n"
"Maaa 20,204,180\nMaaa 20,200,176\nMaaa 80,208,176\nMaaa 80,212,180\nD0 8,24,44,2048\nMaaa 84,220,180\nMaaa 84,216,176\nMaaa 92,216,184\nMaaa 92,220,188\ns_add_u32 s6, s53, 0\n"
"Sv 232,1,16,6,3072\nMaaa 88,212,188\nMaaa 88,208,184\nMaaa 28,200,184\nMaaa 28,204,188\nD0 9,24,44,3072\nMaaa 24,196,188\nMaaa 24,192,184\ns_waitcnt vmcnt(24) lgkmcnt(0)\n"
"s_barrier\nMaaa 32,192,224\nTa 160,12,0\nMaaa 32,196,228\nTa 162,12,2048\nMaaa 36,204,228\ns_add_u32 m0, s47, -16384\ns_add_u32 s48, s48, 128\nMaaa 36,200,224\nTa 164,12,16384\n"
"Maaa 96,208,224\nTa 166,12,18432\nMaaa 96,212,228\nD0 2,20,48,0\nMaaa 100,220,228\nTa 168,13,0\nMaaa 100,216,224\nTa 170,13,2048\nMaaa 108,216,232\nMaaa 108,220,236\n"
"Ta 172,13,16384\ns_add_u32 s6, s53, 4096\nSv 240,1,16,6,1024\nMaaa 104,212,236\nTa 174,13,18432\nMaaa 104,208,232\nMaaa 44,200,232\nTa 176,14,0\nMaaa 44,204,236\nTa 178,14,2048\n"
"Maaa 40,196,236\nD0 3,20,48,1024\nMaaa 40,192,232\nTa 180,14,16384\nMaaa 48,192,240\nTa 182,14,18432\nMaaa 48,196,244\ns_add_u32 s6, s53, 4096\nSv 248,1,16,6,3072\n"
"Maaa 52,204,244\nTa 184,15,0\nMaaa 52,200,240\nTa 186,15,2048\nMaaa 112,208,240\nMaaa 112,212,244\nTa 188,15,16384\nMaaa 116,220,244\nTa 190,15,18432\nMaaa 116,216,240\n"
"D0 4,20,48,2048\nMaaa 124,216,248\nMaaa 124,220,252\nMaaa 120,212,252\nMaaa 120,208,248\ns_lshl3_add_u32 s90, s12, s13\ns_lshl2_add_u32 s90, s90, s11\n"
"s_lshl2_add_u32 s90, s90, 0xc04a400\nMaaa 60,200,248\nMaaa 60,204,252\nD0 5,20,48,3072\nMaaa 56,196,252\nMaaa 56,192,248\nWL 0\ns_mov_b64 s[76:77], s[62:63]\n"
"s_mov_b64 s[78:79], s[64:65]\ns_mov_b32 s42, 1605632\ns_mov_b32 s50, 1\n.GL150:\nMvaa 64,128,160\nTa 224,16,0\nMvaa 64,132,164\nTa 226,16,2048\nMvaa 68,140,164\n"
"s_add_u32 m0, s47, -65536\ns_add_u32 s45, s45, 128\nMvaa 68,136,160\nTa 228,16,16384\nMvaa 128,144,160\nTa 230,16,18432\nMvaa 128,148,164\nD0 2,20,45,0\nMvaa 132,156,164\n"
"Ta 232,17,0\nMvaa 132,152,160\nTa 234,17,2048\nMvaa 140,152,168\nMvaa 140,156,172\nTa 236,17,16384\nMvaa 136,148,172\nTa 238,17,18432\nMvaa 136,144,168\nMvaa 76,136,168\n"
"Ta 240,18,0\nMvaa 76,140,172\nTa 242,18,2048\nMvaa 72,132,172\nD0 3,20,45,1024\nMvaa 72,128,168\nTa 244,18,16384\nMvaa 80,128,176\nTa 246,18,18432\nMvaa 80,132,180\n"
"Mvaa 84,140,180\nTa 248,19,0\nMvaa 84,136,176\nTa 250,19,2048\nMvaa 144,144,176\nMvaa 144,148,180\nTa 252,19,16384\nMvaa 148,156,180\nTa 254,19,18432\nMvaa 148,152,176\n"
"D0 4,20,45,2048\nMvaa 156,152,184\nMvaa 156,156,188\nMvaa 152,148,188\nMvaa 152,144,184\ns_mov_b32 s40, s76\ns_and_b32 s41, s77, 0xffff\nMvaa 92,136,184\nMvaa 92,140,188\n"
"D0 5,20,45,3072\nMvaa 88,132,188\nMvaa 88,128,184\ns_waitcnt vmcnt(17) lgkmcnt(0)\ns_barrier\nMvaa 96,128,224\nRa 192,10,16384\nMvaa 96,132,228\ns_add_u32 m0, s47, 0\n"
"s_add_u32 s44, s44, 0x40000\nbuffer_load_dword v31, v26, s[40:43], s97 offen\ns_mov_b64 s[76:77], s[66:67]\nMvaa 100,140,228\nRa 196,11,16384\nMvaa 100,136,224\nMvaa 160,144,224\n"
"Ra 200,10,18432\nMvaa 160,148,228\nD0 6,24,44,0\nMvaa 164,156,228\nRa 204,11,18432\nMvaa 164,152,224\nMvaa 172,152,232\nRa 208,10,24576\nMvaa 172,156,236\nMvaa 168,148,236\n"
"Ra 212,11,24576\nMvaa 168,144,232\nMvaa 108,136,232\nRa 216,10,26624\nMvaa 108,140,236\nD0 7,24,44,1024\nMvaa 104,132,236\nRa 220,11,26624\nMvaa 104,128,232\nMvaa 112,128,240\n"
"Mvaa 112,132,244\nMvaa 116,140,244\nMvaa 116,136,240\nMvaa 176,144,240\nMvaa 176,148,244\nD0 8,24,44,2048\nMvaa 180,156,244\nMvaa 180,152,240\nMvaa 188,152,248\nMvaa 188,156,252\n"
"Mvaa 184,148,252\nMvaa 184,144,248\nMvaa 124,136,248\nMvaa 124,140,252\nD0 9,24,44,3072\nMvaa 120,132,252\nMvaa 120,128,248\nWL 0\nMaaa 32,192,224\nRa 128,10,32768\n"
"Maaa 32,196,228\ns_add_u32 m0, s47, 16384\ns_add_u32 s44, s44, 0x40000\nMaaa 36,204,228\nRa 132,11,32768\nMaaa 36,200,224\nMaaa 96,208,224\nRa 136,10,34816\nMaaa 96,212,228\n"
"D0 6,24,44,0\nMaaa 100,220,228\nRa 140,11,34816\nMaaa 100,216,224\nMaaa 108,216,232\nRa 144,10,40960\nMaaa 108,220,236\nMaaa 104,212,236\nRa 148,11,40960\nMaaa 104,208,232\n"
"Maaa 44,200,232\nRa 152,10,43008\nMaaa 44,204,236\nD0 7,24,44,1024\nMaaa 40,196,236\nRa 156,11,43008\nMaaa 40,192,232\nMaaa 48,192,240\nMaaa 48,196,244\nMaaa 52,204,244\n"
"Maaa 52,200,240\nMaaa 112,208,240\nMaaa 112,212,244\nD0 8,24,44,2048\nMaaa 116,220,244\nMaaa 116,216,240\nMaaa 124,216,248\nMaaa 124,220,252\nMaaa 120,212,252\nMaaa 120,208,248\n"
"Maaa 60,200,248\nMaaa 60,204,252\nD0 9,24,44,3072\nMaaa 56,196,252\nMaaa 56,192,248\ns_waitcnt vmcnt(17) lgkmcnt(0)\ns_barrier\nMaaa 0,192,160\nTa 224,16,32768\nMaaa 0,196,164\n"
"Ta 226,16,34816\nMaaa 4,204,164\ns_add_u32 m0, s47, -49152\ns_add_u32 s48, s48, 128\nMaaa 4,200,160\nTa 228,16,49152\nMaaa 64,208,160\nTa 230,16,51200\nMaaa 64,212,164\n"
"D0 2,20,48,0\nMaaa 68,220,164\nTa 232,17,32768\nMaaa 68,216,160\nTa 234,17,34816\nMaaa 76,216,168\nMaaa 76,220,172\nTa 236,17,49152\nMaaa 72,212,172\nTa 238,17,51200\n"
"Maaa 72,208,168\nMaaa 12,200,168\nTa 240,18,32768\nMaaa 12,204,172\nTa 242,18,34816\nMaaa 8,196,172\nD0 3,20,48,1024\nMaaa 8,192,168\nTa 244,18,49152\nMaaa 16,192,176\n"
"Ta 246,18,51200\nMaaa 16,196,180\nMaaa 20,204,180\nTa 248,19,32768\nMaaa 20,200,176\nTa 250,19,34816\nMaaa 80,208,176\nMaaa 80,212,180\nTa 252,19,49152\nMaaa 84,220,180\n"
"Ta 254,19,51200\nMaaa 84,216,176\nD0 4,20,48,2048\nMaaa 92,216,184\nMaaa 92,220,188\nMaaa 88,212,188\nMaaa 88,208,184\nMaaa 28,200,184\nMaaa 28,204,188\nD0 5,20,48,3072\n"
"Maaa 24,196,188\nMaaa 24,192,184\nWL 0\nMvaa 96,128,224\nTa 160,12,32768\nMvaa 96,132,228\nTa 162,12,34816\nMvaa 100,140,228\ns_add_u32 m0, s47, -32768\ns_add_u32 s45, s45, 128\n"
"Mvaa 100,136,224\nTa 164,12,49152\nMvaa 160,144,224\nTa 166,12,51200\nMvaa 160,148,228\nD0 2,20,45,0\nMvaa 164,156,228\nTa 168,13,32768\nMvaa 164,152,224\nTa 170,13,34816\n"
"Mvaa 172,152,232\nMvaa 172,156,236\nTa 172,13,49152\nMvaa 168,148,236\nTa 174,13,51200\nMvaa 168,144,232\nMvaa 108,136,232\nTa 176,14,32768\nMvaa 108,140,236\nTa 178,14,34816\n"
"Mvaa 104,132,236\nD0 3,20,45,1024\nMvaa 104,128,232\nTa 180,14,49152\nMvaa 112,128,240\nTa 182,14,51200\nMvaa 112,132,244\nMvaa 116,140,244\nTa 184,15,32768\nMvaa 116,136,240\n"
"Ta 186,15,34816\nMvaa 176,144,240\nMvaa 176,148,244\nTa 188,15,49152\nMvaa 180,156,244\nTa 190,15,51200\nMvaa 180,152,240\nD0 4,20,45,2048\nMvaa 188,152,248\nMvaa 188,156,252\n"
"Mvaa 184,148,252\nMvaa 184,144,248\ns_mov_b32 s40, s78\ns_and_b32 s41, s79, 0xffff\nMvaa 124,136,248\nMvaa 124,140,252\nD0 5,20,45,3072\nMvaa 120,132,252\nMvaa 120,128,248\n"
"s_waitcnt vmcnt(17) lgkmcnt(0)\ns_barrier\nMvaa 64,128,160\nRa 192,10,49152\nMvaa 64,132,164\ns_add_u32 m0, s47, 32768\ns_add_u32 s44, s44, 0x40000\n"
"buffer_load_dword v31, v26, s[40:43], s97 offen\ns_mov_b64 s[78:79], s[66:67]\nMvaa 68,140,164\nRa 196,11,49152\nMvaa 68,136,160\nMvaa 128,144,160\nRa 200,10,51200\n"
"Mvaa 128,148,164\nD0 6,24,44,0\nMvaa 132,156,164\nRa 204,11,51200\nMvaa 132,152,160\nMvaa 140,152,168\nRa 208,10,57344\nMvaa 140,156,172\nMvaa 136,148,172\nRa 212,11,57344\n"
"Mvaa 136,144,168\nMvaa 76,136,168\nRa 216,10,59392\nMvaa 76,140,172\nD0 7,24,44,1024\nMvaa 72,132,172\nRa 220,11,59392\nMvaa 72,128,168\nMvaa 80,128,176\nMvaa 80,132,180\n"
"Mvaa 84,140,180\nMvaa 84,136,176\nMvaa 144,144,176\nMvaa 144,148,180\nD0 8,24,44,2048\nMvaa 148,156,180\nMvaa 148,152,176\nMvaa 156,152,184\nMvaa 156,156,188\nMvaa 152,148,188\n"
"Mvaa 152,144,184\nMvaa 92,136,184\nMvaa 92,140,188\nD0 9,24,44,3072\nMvaa 88,132,188\nMvaa 88,128,184\nWL 0\nMaaa 0,192,160\nRa 128,10,0\nMaaa 0,196,164\ns_add_u32 m0, s47, 49152\n"
"s_add_u32 s44, s44, 0x40000\nMaaa 4,204,164\nRa 132,11,0\nMaaa 4,200,160\nMaaa 64,208,160\nRa 136,10,2048\nMaaa 64,212,164\nD0 6,24,44,0\nMaaa 68,220,164\nRa 140,11,2048\n"
"Maaa 68,216,160\nMaaa 76,216,168\nRa 144,10,8192\nMaaa 76,220,172\nMaaa 72,212,172\nRa 148,11,8192\nMaaa 72,208,168\nMaaa 12,200,168\nRa 152,10,10240\nMaaa 12,204,172\n"
"D0 7,24,44,1024\nMaaa 8,196,172\nRa 156,11,10240\nMaaa 8,192,168\nMaaa 16,192,176\nMaaa 16,196,180\nMaaa 20,204,180\nMaaa 20,200,176\nMaaa 80,208,176\nMaaa 80,212,180\n"
"D0 8,24,44,2048\nMaaa 84,220,180\nMaaa 84,216,176\nMaaa 92,216,184\nMaaa 92,220,188\nMaaa 88,212,188\nMaaa 88,208,184\nMaaa 28,200,184\nMaaa 28,204,188\nD0 9,24,44,3072\n"
"Maaa 24,196,188\nMaaa 24,192,184\ns_waitcnt vmcnt(17) lgkmcnt(0)\ns_barrier\nMaaa 32,192,224\nTa 160,12,0\nMaaa 32,196,228\nTa 162,12,2048\nMaaa 36,204,228\n"
"s_add_u32 m0, s47, -16384\ns_add_u32 s48, s48, 128\nMaaa 36,200,224\nTa 164,12,16384\nMaaa 96,208,224\nTa 166,12,18432\nMaaa 96,212,228\nD0 2,20,48,0\nMaaa 100,220,228\n"
"Ta 168,13,0\nMaaa 100,216,224\nTa 170,13,2048\nMaaa 108,216,232\nMaaa 108,220,236\nTa 172,13,16384\nMaaa 104,212,236\nTa 174,13,18432\nMaaa 104,208,232\nMaaa 44,200,232\n"
"Ta 176,14,0\nMaaa 44,204,236\nTa 178,14,2048\n.W555:\ns_cbranch_execz .W559\nMaaa 40,196,236\nD0 3,20,48,1024\nMaaa 40,192,232\nTa 180,14,16384\nMaaa 48,192,240\nTa 182,14,18432\n"
"Maaa 48,196,244\nMaaa 52,204,244\nTa 184,15,0\nMaaa 52,200,240\nTa 186,15,2048\nMaaa 112,208,240\nMaaa 112,212,244\nTa 188,15,16384\nMaaa 116,220,244\nTa 190,15,18432\n"
"Maaa 116,216,240\nD0 4,20,48,2048\nMaaa 124,216,248\nMaaa 124,220,252\nMaaa 120,212,252\nMaaa 120,208,248\nMaaa 60,200,248\nMaaa 60,204,252\nD0 5,20,48,3072\n"
"s_sub_u32 s50, s50, 1\nMaaa 56,196,252\nMaaa 56,192,248\nWL 0\ns_cbranch_scc0 .GL150\nMvaa 64,128,160\nTa 224,16,0\nMvaa 64,132,164\nTa 226,16,2048\nMvaa 68,140,164\n"
"s_add_u32 m0, s47, -65536\ns_add_u32 s45, s45, 128\nMvaa 68,136,160\nTa 228,16,16384\nMvaa 128,144,160\nTa 230,16,18432\nMvaa 128,148,164\nD0 2,20,45,0\nMvaa 132,156,164\n"
"Ta 232,17,0\nMvaa 132,152,160\nTa 234,17,2048\nMvaa 140,152,168\ns_and_b32 s2, s74, 15\nMvaa 140,156,172\nTa 236,17,16384\n"
"buffer_store_dword v25, off, s[16:19], s90 offset:0 sc0 sc1\nMvaa 136,148,172\nTa 238,17,18432\nMvaa 136,144,168\ns_sub_u32 s2, s2, 2\ns_cmp_lt_u32 s2, 14\n"
"s_cselect_b32 s5, 0, 0x40000000\nMvaa 76,136,168\nTa 240,18,0\nMvaa 76,140,172\nTa 242,18,2048\nMvaa 72,132,172\nD0 3,20,45,1024\nMvaa 72,128,168\nTa 244,18,16384\n"
"Mvaa 80,128,176\nTa 246,18,18432\nMvaa 80,132,180\ns_and_b32 s2, s2, 15\nMvaa 84,140,180\nTa 248,19,0\nMvaa 84,136,176\nTa 250,19,2048\nMvaa 144,144,176\ns_mul_i32 s4, s73, 14\n"
"Mvaa 144,148,180\nTa 252,19,16384\nMvaa 148,156,180\nTa 254,19,18432\nMvaa 148,152,176\nD0 4,20,45,2048\nMvaa 156,152,184\ns_add_u32 s4, s4, s2\nMvaa 156,156,188\n"
"s_mov_b32 s36, s62\nMvaa 152,148,188\ns_and_b32 s37, s63, 0xffff\nMvaa 152,144,184\ns_mov_b32 s38, 0x188000\nMvaa 92,136,184\ns_mov_b32 s39, 0x20000\nMvaa 92,140,188\n"
"D0 5,20,45,3072\nMvaa 88,132,188\ns_mov_b32 s40, s64\nMvaa 88,128,184\ns_waitcnt vmcnt(18) lgkmcnt(0)\ns_barrier\nMvaa 96,128,224\nRa 192,10,16384\nMvaa 96,132,228\n"
"s_add_u32 m0, s47, 0\ns_add_u32 s44, s44, 0x40000\nMvaa 100,140,228\nRa 196,11,16384\nMvaa 100,136,224\ns_and_b32 s41, s65, 0xffff\nMvaa 160,144,224\nRa 200,10,18432\n"
"Mvaa 160,148,228\nD0 6,24,44,0\nMvaa 164,156,228\nRa 204,11,18432\nMvaa 164,152,224\ns_mov_b32 s42, 0x188000\nMvaa 172,152,232\nRa 208,10,24576\nMvaa 172,156,236\n"
"s_mov_b32 s43, 0x20000\nMvaa 168,148,236\nRa 212,11,24576\nMvaa 168,144,232\ns_mov_b32 s92, s66\nMvaa 108,136,232\nRa 216,10,26624\nMvaa 108,140,236\nD0 7,24,44,1024\n"
"Mvaa 104,132,236\nRa 220,11,26624\nMvaa 104,128,232\ns_and_b32 s93, s67, 0xffff\nMvaa 112,128,240\ns_mov_b32 s94, 0x188000\nMvaa 112,132,244\ns_mov_b32 s95, 0x20000\n"
"Mvaa 116,140,244\nv_lshrrev_b32 v252, 1, v0\nMvaa 116,136,240\nv_mul_u32_u24 v253, 14336, v252\nMvaa 176,144,240\nv_and_b32 v254, 1, v0\nMvaa 176,148,244\nD0 8,24,44,2048\n"
"Mvaa 180,156,244\nv_lshl_add_u32 v253, v254, 4, v253\nMvaa 180,152,240\nv_mov_b32 v254, 0x40000000\nv_cmp_gt_u32 vcc, 56, v0\nv_lshlrev_b32 v252, 3, v252\n"
"s_mul_i32 s2, s11, 401408\nv_cndmask_b32 v21, v254, v253, vcc\nv_cndmask_b32 v255, v254, v252, vcc\nMvaa 188,152,248\ns_lshl_b32 s3, s4, 5\nMvaa 188,156,252\ns_add_u32 s2, s2, s3\n"
"Mvaa 184,148,252\ns_or_b32 s86, s2, s5\nMvaa 184,144,248\ns_add_u32 s87, s86, 7168\nMvaa 124,136,248\nv_lshrrev_b32 v251, 4, v0\nMvaa 124,140,252\nD0 9,24,44,3072\n"
"Mvaa 120,132,252\nv_mul_u32_u24 v252, 3584, v251\nMvaa 120,128,248\nWL 0\nMaaa 32,192,224\nRa 128,10,32768\nMaaa 32,196,228\ns_add_u32 m0, s47, 16384\ns_add_u32 s44, s44, 0x40000\n"
"Maaa 36,204,228\nRa 132,11,32768\nMaaa 36,200,224\nv_and_b32 v251, 15, v0\nMaaa 96,208,224\nRa 136,10,34816\nMaaa 96,212,228\nD0 6,24,44,0\nMaaa 100,220,228\nRa 140,11,34816\n"
"Maaa 100,216,224\nv_lshl_add_u32 v252, v251, 4, v252\nv_cmp_gt_u32 vcc, 14, v251\ns_mul_i32 s2, s4, 14336\ns_mul_i32 s3, s11, 224\nv_cndmask_b32 v20, v254, v252, vcc\n"
"Maaa 108,216,232\nRa 144,10,40960\nMaaa 108,220,236\ns_add_u32 s2, s2, s3\nMaaa 104,212,236\nRa 148,11,40960\nMaaa 104,208,232\ns_or_b32 s88, s2, s5\nMaaa 44,200,232\n"
"Ra 152,10,43008\nMaaa 44,204,236\nD0 7,24,44,1024\nMaaa 40,196,236\nRa 156,11,43008\nMaaa 40,192,232\nMaaa 48,192,240\nMaaa 48,196,244\nMaaa 52,204,244\nMaaa 52,200,240\n"
"Maaa 112,208,240\nMaaa 112,212,244\nD0 8,24,44,2048\nMaaa 116,220,244\nMaaa 116,216,240\nMaaa 124,216,248\nMaaa 124,220,252\nMaaa 120,212,252\nMaaa 120,208,248\nMaaa 60,200,248\n"
"Maaa 60,204,252\nD0 9,24,44,3072\nMaaa 56,196,252\nMaaa 56,192,248\ns_waitcnt vmcnt(17) lgkmcnt(0)\ns_barrier\nMaaa 0,192,160\nTa 224,16,32768\nMaaa 0,196,164\nTa 226,16,34816\n"
"buffer_load_dwordx4 v[192:195], v21, s[36:39], s86 offen offset:0\nMaaa 4,204,164\ns_add_u32 m0, s47, -49152\ns_add_u32 s48, s48, 128\nMaaa 4,200,160\nTa 228,16,49152\n"
"s_lshl_b32 s6, s11, 12\ns_add_u32 s6, s6, 0xc044000\nMaaa 64,208,160\nTa 230,16,51200\nMaaa 64,212,164\nD0 2,20,48,0\nMaaa 68,220,164\nTa 232,17,32768\nMaaa 68,216,160\n"
"Ta 234,17,34816\nMaaa 76,216,168\nbuffer_load_dwordx4 v[32:35], v1, s[16:19], s6 offen offset:0 sc0 sc1\nMaaa 76,220,172\nTa 236,17,49152\nMaaa 72,212,172\nTa 238,17,51200\n"
"Maaa 72,208,168\nMaaa 12,200,168\nTa 240,18,32768\nMaaa 12,204,172\nTa 242,18,34816\nMaaa 8,196,172\nD0 3,20,48,1024\nMaaa 8,192,168\nTa 244,18,49152\nMaaa 16,192,176\n"
"Ta 246,18,51200\nMaaa 16,196,180\nbuffer_load_dwordx4 v[36:39], v1, s[16:19], s6 offen offset:1024 sc0 sc1\nMaaa 20,204,180\nTa 248,19,32768\nMaaa 20,200,176\nTa 250,19,34816\n"
"Maaa 80,208,176\nMaaa 80,212,180\nTa 252,19,49152\nMaaa 84,220,180\nTa 254,19,51200\nMaaa 84,216,176\nD0 4,20,48,2048\nMaaa 92,216,184\n"
"buffer_load_dwordx4 v[40:43], v1, s[16:19], s6 offen offset:2048 sc0 sc1\nMaaa 92,220,188\nMaaa 88,212,188\nMaaa 88,208,184\nMaaa 28,200,184\n"
"buffer_load_dwordx4 v[44:47], v1, s[16:19], s6 offen offset:3072 sc0 sc1\nMaaa 28,204,188\nD0 5,20,48,3072\nMaaa 24,196,188\nMaaa 24,192,184\nWL 0\nMvaa 96,128,224\n"
"Ta 160,12,32768\nMvaa 96,132,228\nTa 162,12,34816\nMvaa 100,140,228\ns_add_u32 m0, s47, -32768\ns_add_u32 s45, s45, 128\nMvaa 100,136,224\nTa 164,12,49152\nMvaa 160,144,224\n"
"Ta 166,12,51200\nMvaa 160,148,228\nD0 2,20,45,0\nMvaa 164,156,228\nTa 168,13,32768\nMvaa 164,152,224\nTa 170,13,34816\nMvaa 172,152,232\nMvaa 172,156,236\nTa 172,13,49152\n"
"Mvaa 168,148,236\nTa 174,13,51200\nMvaa 168,144,232\nMvaa 108,136,232\nTa 176,14,32768\nMvaa 108,140,236\nTa 178,14,34816\nMvaa 104,132,236\nD0 3,20,45,1024\nMvaa 104,128,232\n"
"Ta 180,14,49152\nMvaa 112,128,240\nTa 182,14,51200\nMvaa 112,132,244\nMvaa 116,140,244\nTa 184,15,32768\nMvaa 116,136,240\nTa 186,15,34816\nMvaa 176,144,240\nMvaa 176,148,244\n"
"Ta 188,15,49152\nMvaa 180,156,244\nTa 190,15,51200\nMvaa 180,152,240\nD0 4,20,45,2048\nMvaa 188,152,248\nMvaa 188,156,252\nMvaa 184,148,252\nMvaa 184,144,248\nMvaa 124,136,248\n"
"Mvaa 124,140,252\nD0 5,20,45,3072\nMvaa 120,132,252\nMvaa 120,128,248\ns_waitcnt vmcnt(21) lgkmcnt(0)\ns_barrier\nMvaa 64,128,160\nRa 192,10,49152\nMvaa 64,132,164\n"
"s_add_u32 m0, s47, 32768\ns_add_u32 s44, s44, 0x40000\nbuffer_load_dwordx4 v[208:211], v21, s[40:43], s86 offen offset:0\nMvaa 68,140,164\nRa 196,11,49152\nMvaa 68,136,160\n"
"Mvaa 128,144,160\nRa 200,10,51200\nMvaa 128,148,164\nD0 6,24,44,0\nMvaa 132,156,164\nRa 204,11,51200\nMvaa 132,152,160\nMvaa 140,152,168\nRa 208,10,57344\nMvaa 140,156,172\n"
"Mvaa 136,148,172\nRa 212,11,57344\nMvaa 136,144,168\nMvaa 76,136,168\nRa 216,10,59392\nMvaa 76,140,172\nD0 7,24,44,1024\nMvaa 72,132,172\nRa 220,11,59392\nMvaa 72,128,168\n"
"Mvaa 80,128,176\nMvaa 80,132,180\nMvaa 84,140,180\nMvaa 84,136,176\nMvaa 144,144,176\nMvaa 144,148,180\nD0 8,24,44,2048\nMvaa 148,156,180\nMvaa 148,152,176\nMvaa 156,152,184\n"
"Mvaa 156,156,188\nMvaa 152,148,188\nMvaa 152,144,184\nMvaa 92,136,184\nMvaa 92,140,188\nD0 9,24,44,3072\nMvaa 88,132,188\nMvaa 88,128,184\nWL 0\nMaaa 0,192,160\nRa 128,10,0\n"
"Maaa 0,196,164\ns_add_u32 m0, s47, 49152\ns_add_u32 s44, s44, 0x40000\nMaaa 4,204,164\nRa 132,11,0\nMaaa 4,200,160\nMaaa 64,208,160\nRa 136,10,2048\nMaaa 64,212,164\nD0 6,24,44,0\n"
"Maaa 68,220,164\nRa 140,11,2048\nMaaa 68,216,160\nMaaa 76,216,168\nRa 144,10,8192\nMaaa 76,220,172\nMaaa 72,212,172\nRa 148,11,8192\nMaaa 72,208,168\nMaaa 12,200,168\n"
"Ra 152,10,10240\nMaaa 12,204,172\nD0 7,24,44,1024\nMaaa 8,196,172\nRa 156,11,10240\nMaaa 8,192,168\nMaaa 16,192,176\nMaaa 16,196,180\nMaaa 20,204,180\nMaaa 20,200,176\n"
"Maaa 80,208,176\nMaaa 80,212,180\nD0 8,24,44,2048\nMaaa 84,220,180\nMaaa 84,216,176\nMaaa 92,216,184\nMaaa 92,220,188\nMaaa 88,212,188\nMaaa 88,208,184\nMaaa 28,200,184\n"
"Maaa 28,204,188\nD0 9,24,44,3072\nMaaa 24,196,188\nMaaa 24,192,184\ns_waitcnt vmcnt(14) lgkmcnt(0)\ns_barrier\nMaaa 32,192,224\nTa 160,12,0\nv_xor_b32 v48, s15, v33\n"
"Maaa 32,196,228\nTa 162,12,2048\nbuffer_load_dwordx4 v[196:199], v21, s[36:39], s86 offen offset:3584\nMaaa 36,204,228\ns_add_u32 m0, s47, -16384\ns_add_u32 s48, s48, 128\n"
"Maaa 36,200,224\nTa 164,12,16384\nMaaa 96,208,224\nTa 166,12,18432\nMaaa 96,212,228\nD0 2,20,48,0\nMaaa 100,220,228\nTa 168,13,0\nMaaa 100,216,224\nTa 170,13,2048\n"
"Maaa 108,216,232\nv_xor_b32 v49, s15, v35\nMaaa 108,220,236\nTa 172,13,16384\nMaaa 104,212,236\nTa 174,13,18432\nMaaa 104,208,232\nv_xor_b32 v50, s15, v37\nMaaa 44,200,232\n"
"Ta 176,14,0\nMaaa 44,204,236\nTa 178,14,2048\nMaaa 40,196,236\nD0 3,20,48,1024\nMaaa 40,192,232\nTa 180,14,16384\nMaaa 48,192,240\nTa 182,14,18432\nv_xor_b32 v51, s15, v39\n"
"Maaa 48,196,244\nv_xor_b32 v52, s15, v41\nMaaa 52,204,244\nTa 184,15,0\nMaaa 52,200,240\nTa 186,15,2048\nMaaa 112,208,240\nv_xor_b32 v53, s15, v43\nMaaa 112,212,244\n"
"Ta 188,15,16384\nMaaa 116,220,244\nTa 190,15,18432\nMaaa 116,216,240\nD0 4,20,48,2048\nMaaa 124,216,248\nv_xor_b32 v54, s15, v45\nMaaa 124,220,252\nv_xor_b32 v55, s15, v47\n"
"Maaa 120,212,252\nv_or3_b32 v48, v48, v49, v50\nMaaa 120,208,248\nv_or3_b32 v51, v51, v52, v53\nMaaa 60,200,248\nv_or3_b32 v48, v48, v54, v55\nMaaa 60,204,252\nD0 5,20,48,3072\n"
"Maaa 56,196,252\nv_or_b32 v48, v48, v51\nMaaa 56,192,248\nWL 0\nMvaa 64,128,160\nTa 224,16,0\nv_cmp_ne_u32 vcc, 0, v48\nMvaa 64,132,164\nTa 226,16,2048\nMvaa 68,140,164\n"
"s_add_u32 m0, s47, -65536\ns_add_u32 s45, s45, 128\nMvaa 68,136,160\nTa 228,16,16384\nMvaa 128,144,160\nTa 230,16,18432\nMvaa 128,148,164\nD0 2,20,45,0\nMvaa 132,156,164\n"
"Ta 232,17,0\nMvaa 132,152,160\nTa 234,17,2048\nMvaa 140,152,168\ns_cbranch_vccnz .S144\n.S145:\nMvaa 140,156,172\nTa 236,17,16384\nMvaa 136,148,172\nTa 238,17,18432\n"
"Mvaa 136,144,168\nv_max_u32 v48, v32, v34\nMvaa 76,136,168\nTa 240,18,0\nMvaa 76,140,172\nTa 242,18,2048\nMvaa 72,132,172\nD0 3,20,45,1024\n.W556:\ns_cbranch_execz .W560\n"
"Mvaa 72,128,168\nTa 244,18,16384\nMvaa 80,128,176\nTa 246,18,18432\nv_min_u32 v49, v32, v34\nMvaa 80,132,180\nv_med3_u32 v49, v48, v49, v36\nMvaa 84,140,180\nTa 248,19,0\n"
"Mvaa 84,136,176\nTa 250,19,2048\nMvaa 144,144,176\nv_max_u32 v48, v48, v36\nMvaa 144,148,180\nTa 252,19,16384\nMvaa 148,156,180\nTa 254,19,18432\nMvaa 148,152,176\n"
"D0 4,20,45,2048\nMvaa 156,152,184\nv_med3_u32 v49, v48, v49, v38\nMvaa 156,156,188\nv_max_u32 v48, v48, v38\nMvaa 152,148,188\nv_med3_u32 v49, v48, v49, v40\nMvaa 152,144,184\n"
"v_max_u32 v48, v48, v40\nMvaa 92,136,184\nv_med3_u32 v49, v48, v49, v42\nMvaa 92,140,188\nD0 5,20,45,3072\nMvaa 88,132,188\nv_max_u32 v48, v48, v42\nMvaa 88,128,184\n"
"s_waitcnt vmcnt(18) lgkmcnt(0)\ns_barrier\nMvaa 96,128,224\nRa 192,10,16384\nv_med3_u32 v49, v48, v49, v44\nMvaa 96,132,228\ns_add_u32 m0, s47, 0\ns_add_u32 s44, s44, 0x40000\n"
"buffer_load_dwordx4 v[212:215], v21, s[40:43], s86 offen offset:3584\nMvaa 100,140,228\nRa 196,11,16384\nMvaa 100,136,224\nv_max_u32 v48, v48, v44\nMvaa 160,144,224\n"
"Ra 200,10,18432\nMvaa 160,148,228\nD0 6,24,44,0\nMvaa 164,156,228\nRa 204,11,18432\nMvaa 164,152,224\nv_med3_u32 v49, v48, v49, v46\nMvaa 172,152,232\nRa 208,10,24576\n"
"Mvaa 172,156,236\nv_max_u32 v48, v48, v46\nMvaa 168,148,236\nRa 212,11,24576\nMvaa 168,144,232\nv_mov_b32 v50, v48\nMvaa 108,136,232\nRa 216,10,26624\nMvaa 108,140,236\n"
"D0 7,24,44,1024\nMvaa 104,132,236\nRa 220,11,26624\nMvaa 104,128,232\nv_max_u32_dpp v50, v50, v50 row_ror:8 row_mask:0xf bank_mask:0xf\nMvaa 112,128,240\nMvaa 112,132,244\n"
"v_max_u32_dpp v50, v50, v50 row_ror:4 row_mask:0xf bank_mask:0xf\nMvaa 116,140,244\nMvaa 116,136,240\nv_max_u32_dpp v50, v50, v50 row_ror:2 row_mask:0xf bank_mask:0xf\n"
"Mvaa 176,144,240\nMvaa 176,148,244\nD0 8,24,44,2048\nMvaa 180,156,244\nv_max_u32_dpp v50, v50, v50 row_ror:1 row_mask:0xf bank_mask:0xf\nMvaa 180,152,240\nv_mov_b32 v52, v50\n"
"Mvaa 188,152,248\nMvaa 188,156,252\nv_permlane16_swap_b32 v50, v52\nMvaa 184,148,252\nv_max_u32 v50, v50, v52\nMvaa 184,144,248\nv_mov_b32 v52, v50\nMvaa 124,136,248\n"
"Mvaa 124,140,252\nD0 9,24,44,3072\nMvaa 120,132,252\nv_permlane32_swap_b32 v50, v52\nMvaa 120,128,248\nWL 0\nMaaa 32,192,224\nRa 128,10,32768\nv_max_u32 v50, v50, v52\n"
"Maaa 32,196,228\ns_add_u32 m0, s47, 16384\ns_add_u32 s44, s44, 0x40000\nMaaa 36,204,228\nRa 132,11,32768\nv_cmp_eq_u32 vcc, v48, v50\ns_lshl_b32 s2, s11, 3\n"
"s_add_u32 s2, s2, 131072\nv_cndmask_b32 v51, v48, v49, vcc\nMaaa 36,200,224\nMaaa 96,208,224\nRa 136,10,34816\nMaaa 96,212,228\nD0 6,24,44,0\nMaaa 100,220,228\nRa 140,11,34816\n"
"Maaa 100,216,224\nv_max_u32_dpp v51, v51, v51 row_ror:8 row_mask:0xf bank_mask:0xf\nMaaa 108,216,232\nRa 144,10,40960\nMaaa 108,220,236\n"
"v_max_u32_dpp v51, v51, v51 row_ror:4 row_mask:0xf bank_mask:0xf\nMaaa 104,212,236\nRa 148,11,40960\nMaaa 104,208,232\n"
"v_max_u32_dpp v51, v51, v51 row_ror:2 row_mask:0xf bank_mask:0xf\nMaaa 44,200,232\nRa 152,10,43008\nMaaa 44,204,236\nD0 7,24,44,1024\nMaaa 40,196,236\nRa 156,11,43008\n"
"Maaa 40,192,232\nv_max_u32_dpp v51, v51, v51 row_ror:1 row_mask:0xf bank_mask:0xf\nMaaa 48,192,240\nv_mov_b32 v52, v51\nMaaa 48,196,244\nMaaa 52,204,244\n"
"v_permlane16_swap_b32 v51, v52\nMaaa 52,200,240\nv_max_u32 v51, v51, v52\nMaaa 112,208,240\nv_mov_b32 v52, v51\nMaaa 112,212,244\nD0 8,24,44,2048\nMaaa 116,220,244\n"
"v_permlane32_swap_b32 v51, v52\nMaaa 116,216,240\nv_max_u32 v51, v51, v52\nMaaa 124,216,248\nv_mov_b32 v52, s2\nMaaa 124,220,252\nds_write_b64 v52, v[50:51]\nMaaa 120,212,252\n"
"v_mov_b32 v52, 131072\nMaaa 120,208,248\nMaaa 60,200,248\nMaaa 60,204,252\nD0 9,24,44,3072\nMaaa 56,196,252\nMaaa 56,192,248\ns_waitcnt vmcnt(18) lgkmcnt(0)\ns_barrier\n"
"Maaa 0,192,160\nTa 224,16,32768\nMaaa 0,196,164\nTa 226,16,34816\nbuffer_load_dwordx4 v[200:203], v21, s[36:39], s87 offen offset:0\nMaaa 4,204,164\ns_add_u32 m0, s47, -49152\n"
"s_add_u32 s48, s48, 128\nMaaa 4,200,160\nTa 228,16,49152\nMaaa 64,208,160\nTa 230,16,51200\nMaaa 64,212,164\nD0 2,20,48,0\nMaaa 68,220,164\nTa 232,17,32768\nMaaa 68,216,160\n"
"Ta 234,17,34816\nMaaa 76,216,168\nRv 32,52,0\nMaaa 76,220,172\nTa 236,17,49152\nMaaa 72,212,172\nTa 238,17,51200\nMaaa 72,208,168\nRv 36,52,16\nMaaa 12,200,168\nTa 240,18,32768\n"
"Maaa 12,204,172\nTa 242,18,34816\nMaaa 8,196,172\nD0 3,20,48,1024\nMaaa 8,192,168\nTa 244,18,49152\nMaaa 16,192,176\nTa 246,18,51200\nMaaa 16,196,180\nMaaa 20,204,180\n"
"Ta 248,19,32768\nMaaa 20,200,176\nTa 250,19,34816\nMaaa 80,208,176\nMaaa 80,212,180\nTa 252,19,49152\nMaaa 84,220,180\nTa 254,19,51200\nMaaa 84,216,176\nD0 4,20,48,2048\n"
"Maaa 92,216,184\nWL 11\nMaaa 92,220,188\nv_max_u32 v48, v32, v33\nMaaa 88,212,188\nv_min_u32 v49, v32, v33\nMaaa 88,208,184\nv_med3_u32 v49, v48, v49, v34\nMaaa 28,200,184\n"
"v_max_u32 v48, v48, v34\nMaaa 28,204,188\nD0 5,20,48,3072\nMaaa 24,196,188\nv_med3_u32 v49, v48, v49, v35\nMaaa 24,192,184\nWL 0\nMvaa 96,128,224\nTa 160,12,32768\n"
"Mvaa 96,132,228\nTa 162,12,34816\nMvaa 100,140,228\ns_add_u32 m0, s47, -32768\ns_add_u32 s45, s45, 128\nMvaa 100,136,224\nTa 164,12,49152\nMvaa 160,144,224\nTa 166,12,51200\n"
"Mvaa 160,148,228\nD0 2,20,45,0\nMvaa 164,156,228\nTa 168,13,32768\nMvaa 164,152,224\nTa 170,13,34816\nMvaa 172,152,232\nv_max_u32 v48, v48, v35\nMvaa 172,156,236\nTa 172,13,49152\n"
"Mvaa 168,148,236\nTa 174,13,51200\nMvaa 168,144,232\nv_med3_u32 v49, v48, v49, v36\nMvaa 108,136,232\nTa 176,14,32768\nMvaa 108,140,236\nTa 178,14,34816\nMvaa 104,132,236\n"
"D0 3,20,45,1024\nMvaa 104,128,232\nTa 180,14,49152\nMvaa 112,128,240\nTa 182,14,51200\nMvaa 112,132,244\nv_max_u32 v48, v48, v36\nMvaa 116,140,244\nTa 184,15,32768\n"
"Mvaa 116,136,240\nTa 186,15,34816\nMvaa 176,144,240\nv_med3_u32 v49, v48, v49, v37\nMvaa 176,148,244\nTa 188,15,49152\nMvaa 180,156,244\nTa 190,15,51200\nMvaa 180,152,240\n"
"D0 4,20,45,2048\nMvaa 188,152,248\nv_max_u32 v48, v48, v37\nMvaa 188,156,252\nv_med3_u32 v49, v48, v49, v38\nMvaa 184,148,252\nv_max_u32 v48, v48, v38\nMvaa 184,144,248\n"
"v_med3_u32 v49, v48, v49, v39\nMvaa 124,136,248\nv_max_u32 v48, v48, v39\nMvaa 124,140,252\nD0 5,20,45,3072\nMvaa 120,132,252\nv_readfirstlane_b32 s2, v48\nMvaa 120,128,248\n"
"s_waitcnt vmcnt(18) lgkmcnt(0)\ns_barrier\nMvaa 64,128,160\nRa 192,10,49152\nMvaa 64,132,164\ns_add_u32 m0, s47, 32768\ns_add_u32 s44, s44, 0x40000\n"
"buffer_load_dwordx4 v[216:219], v21, s[40:43], s87 offen offset:0\nMvaa 68,140,164\nRa 196,11,49152\nMvaa 68,136,160\nv_readfirstlane_b32 s3, v49\nMvaa 128,144,160\n"
"Ra 200,10,51200\nMvaa 128,148,164\nD0 6,24,44,0\nMvaa 132,156,164\nRa 204,11,51200\nMvaa 132,152,160\ns_andn2_b32 s2, 0x7ff, s2\nMvaa 140,152,168\nRa 208,10,57344\n"
"Mvaa 140,156,172\ns_andn2_b32 s3, 0x7ff, s3\nMvaa 136,148,172\nRa 212,11,57344\nMvaa 136,144,168\ns_lshl_b32 s71, s3, 16\nMvaa 76,136,168\nRa 216,10,59392\nMvaa 76,140,172\n"
"D0 7,24,44,1024\nMvaa 72,132,172\nRa 220,11,59392\nMvaa 72,128,168\ns_or_b32 s71, s71, s2\nMvaa 80,128,176\ns_mul_i32 s2, s2, 896\nMvaa 80,132,180\ns_mul_i32 s3, s3, 896\n"
"Mvaa 84,140,180\ns_mul_i32 s6, s11, 224\nMvaa 84,136,176\ns_add_u32 s2, s2, s6\nMvaa 144,144,176\ns_add_u32 s3, s3, s6\nMvaa 144,148,180\nD0 8,24,44,2048\nMvaa 148,156,180\n"
"buffer_load_dwordx2 v[240:241], v255, s[28:31], s2 offen\nMvaa 148,152,176\nMvaa 156,152,184\nMvaa 156,156,188\nMvaa 152,148,188\n"
"buffer_load_dwordx2 v[242:243], v255, s[28:31], s3 offen\ns_lshl2_add_u32 s2, s10, s11\ns_cmp_eq_u32 s2, 512\ns_cselect_b32 s2, 0, 0x40000000\ns_add_u32 s2, s2, 0xc048000\n"
"Mvaa 152,144,184\nMvaa 92,136,184\nMvaa 92,140,188\nD0 9,24,44,3072\nMvaa 88,132,188\nbuffer_store_dword v25, off, s[16:19], s2 offset:0 sc0 sc1\nMvaa 88,128,184\nWL 0\n"
"Maaa 0,192,160\nRa 128,10,0\nMaaa 0,196,164\ns_add_u32 m0, s47, 49152\ns_add_u32 s44, s44, 0x40000\nMaaa 4,204,164\nRa 132,11,0\nMaaa 4,200,160\nMaaa 64,208,160\nRa 136,10,2048\n"
"Maaa 64,212,164\nD0 6,24,44,0\nMaaa 68,220,164\nRa 140,11,2048\nMaaa 68,216,160\nMaaa 76,216,168\nRa 144,10,8192\nMaaa 76,220,172\nMaaa 72,212,172\nRa 148,11,8192\n"
"Maaa 72,208,168\nMaaa 12,200,168\nRa 152,10,10240\nMaaa 12,204,172\nD0 7,24,44,1024\nMaaa 8,196,172\nRa 156,11,10240\nMaaa 8,192,168\nMaaa 16,192,176\nMaaa 16,196,180\n"
"Maaa 20,204,180\nMaaa 20,200,176\nMaaa 80,208,176\nMaaa 80,212,180\nD0 8,24,44,2048\nMaaa 84,220,180\nMaaa 84,216,176\nMaaa 92,216,184\nMaaa 92,220,188\nMaaa 88,212,188\n"
"Maaa 88,208,184\nMaaa 28,200,184\nMaaa 28,204,188\nD0 9,24,44,3072\nMaaa 24,196,188\nMaaa 24,192,184\ns_waitcnt vmcnt(21) lgkmcnt(0)\ns_barrier\nMaaa 32,192,224\nTa 160,12,0\n"
"Maaa 32,196,228\nTa 162,12,2048\nbuffer_load_dwordx4 v[204:207], v21, s[36:39], s87 offen offset:3584\nMaaa 36,204,228\ns_add_u32 m0, s47, -16384\ns_add_u32 s48, s48, 128\n"
"s_lshl_b32 s5, s73, 9\ns_add_u32 s5, s5, 0xc049400\nv_lshrrev_b32 v27, 1, v1\nbuffer_load_dwordx2 v[22:23], v27, s[16:19], s5 offen sc0 sc1\nMaaa 36,200,224\nTa 164,12,16384\n"
"Maaa 96,208,224\nTa 166,12,18432\nMaaa 96,212,228\nD0 2,20,48,0\nMaaa 100,220,228\nTa 168,13,0\nMaaa 100,216,224\nTa 170,13,2048\nMaaa 108,216,232\nMaaa 108,220,236\n"
"Ta 172,13,16384\nMaaa 104,212,236\nTa 174,13,18432\nMaaa 104,208,232\nMaaa 44,200,232\nTa 176,14,0\nMaaa 44,204,236\nTa 178,14,2048\nMaaa 40,196,236\nD0 3,20,48,1024\n"
"Maaa 40,192,232\nTa 180,14,16384\nMaaa 48,192,240\nTa 182,14,18432\nMaaa 48,196,244\nMaaa 52,204,244\nTa 184,15,0\nMaaa 52,200,240\nTa 186,15,2048\nMaaa 112,208,240\n"
"Maaa 112,212,244\nTa 188,15,16384\nMaaa 116,220,244\nTa 190,15,18432\nMaaa 116,216,240\nD0 4,20,48,2048\nMaaa 124,216,248\nMaaa 124,220,252\nMaaa 120,212,252\nMaaa 120,208,248\n"
"Maaa 60,200,248\nMaaa 60,204,252\nD0 5,20,48,3072\nMaaa 56,196,252\nMaaa 56,192,248\nWL 0\nMvaa 64,128,160\nTa 224,16,0\nMvaa 64,132,164\nTa 226,16,2048\nMvaa 68,140,164\n"
"s_add_u32 m0, s47, -65536\ns_add_u32 s45, s45, 128\nMvaa 68,136,160\nTa 228,16,16384\nMvaa 128,144,160\nTa 230,16,18432\nMvaa 128,148,164\nD0 2,20,45,0\nMvaa 132,156,164\n"
"Ta 232,17,0\nMvaa 132,152,160\nTa 234,17,2048\nMvaa 140,152,168\nMvaa 140,156,172\nTa 236,17,16384\nMvaa 136,148,172\nTa 238,17,18432\nMvaa 136,144,168\nMvaa 76,136,168\n"
"Ta 240,18,0\nMvaa 76,140,172\nTa 242,18,2048\nMvaa 72,132,172\nD0 3,20,45,1024\nMvaa 72,128,168\nTa 244,18,16384\nMvaa 80,128,176\nTa 246,18,18432\nMvaa 80,132,180\n"
"Mvaa 84,140,180\nTa 248,19,0\nMvaa 84,136,176\nTa 250,19,2048\nMvaa 144,144,176\nMvaa 144,148,180\nTa 252,19,16384\nMvaa 148,156,180\nTa 254,19,18432\nMvaa 148,152,176\n"
"D0 4,20,45,2048\nMvaa 156,152,184\nMvaa 156,156,188\nMvaa 152,148,188\nMvaa 152,144,184\nMvaa 92,136,184\nMvaa 92,140,188\nD0 5,20,45,3072\nMvaa 88,132,188\nMvaa 88,128,184\n"
"s_waitcnt vmcnt(22) lgkmcnt(0)\ns_barrier\nMvaa 96,128,224\nRa 192,10,16384\nWV 16\nMvaa 96,132,228\ns_add_u32 m0, s47, 0\ns_add_u32 s44, s44, 0x40000\n"
"buffer_load_dwordx4 v[220:223], v21, s[40:43], s87 offen offset:3584\nMvaa 100,140,228\nRa 196,11,16384\nv_lshlrev_b32 v244, 16, v240\nMvaa 100,136,224\n"
"v_and_b32 v245, 0xffff0000, v240\nv_lshlrev_b32 v246, 16, v241\nMvaa 160,144,224\nRa 200,10,18432\nv_and_b32 v247, 0xffff0000, v241\nMvaa 160,148,228\nD0 6,24,44,0\n"
"v_lshlrev_b32 v248, 16, v242\nMvaa 164,156,228\n.W557:\ns_cbranch_execz .W561\nRa 204,11,18432\nv_and_b32 v249, 0xffff0000, v242\nMvaa 164,152,224\nv_lshlrev_b32 v250, 16, v243\n"
"v_and_b32 v251, 0xffff0000, v243\nMvaa 172,152,232\nRa 208,10,24576\nv_lshlrev_b32 v252, 16, v192\nMvaa 172,156,236\nv_and_b32 v253, 0xffff0000, v192\nMU 32,244,252\n"
"Mvaa 168,148,236\nRa 212,11,24576\nMU 33,244,253\nMvaa 168,144,232\nMU 40,248,252\nMU 41,248,253\nMvaa 108,136,232\nRa 216,10,26624\nv_lshlrev_b32 v252, 16, v193\n"
"Mvaa 108,140,236\nD0 7,24,44,1024\nv_and_b32 v253, 0xffff0000, v193\nMvaa 104,132,236\nRa 220,11,26624\nMU 34,244,252\nMvaa 104,128,232\nMU 35,244,253\nMU 42,248,252\n"
"Mvaa 112,128,240\nMU 43,248,253\nv_lshlrev_b32 v252, 16, v194\nMvaa 112,132,244\nv_and_b32 v253, 0xffff0000, v194\nMvaa 116,140,244\nMU 36,244,252\nMU 37,244,253\n"
"Mvaa 116,136,240\nMU 44,248,252\nMU 45,248,253\nMvaa 176,144,240\nv_lshlrev_b32 v252, 16, v195\nv_and_b32 v253, 0xffff0000, v195\nMvaa 176,148,244\nD0 8,24,44,2048\nMU 38,244,252\n"
"Mvaa 180,156,244\nMU 39,244,253\nMU 46,248,252\nMvaa 180,152,240\nMU 47,248,253\nv_lshlrev_b32 v252, 16, v208\nMvaa 188,152,248\nv_and_b32 v253, 0xffff0000, v208\nMU 48,244,252\n"
"Mvaa 188,156,252\nMU 49,244,253\nMU 56,248,252\nMvaa 184,148,252\nMU 57,248,253\nv_lshlrev_b32 v252, 16, v209\nMvaa 184,144,248\nv_and_b32 v253, 0xffff0000, v209\nMU 50,244,252\n"
"Mvaa 124,136,248\nMU 51,244,253\nMU 58,248,252\nMvaa 124,140,252\nD0 9,24,44,3072\nMU 59,248,253\nMvaa 120,132,252\nv_lshlrev_b32 v252, 16, v210\nv_and_b32 v253, 0xffff0000, v210\n"
"Mvaa 120,128,248\nWL 0\nMaaa 32,192,224\nRa 128,10,32768\nMU 52,244,252\nMaaa 32,196,228\ns_add_u32 m0, s47, 16384\ns_add_u32 s44, s44, 0x40000\nMaaa 36,204,228\nRa 132,11,32768\n"
"MU 53,244,253\nMaaa 36,200,224\nMU 60,248,252\nMU 61,248,253\nMaaa 96,208,224\nRa 136,10,34816\nv_lshlrev_b32 v252, 16, v211\nMaaa 96,212,228\nD0 6,24,44,0\n"
"v_and_b32 v253, 0xffff0000, v211\nMaaa 100,220,228\nRa 140,11,34816\nMU 54,244,252\nMaaa 100,216,224\nMU 55,244,253\nMU 62,248,252\nMaaa 108,216,232\nRa 144,10,40960\n"
"MU 63,248,253\nMaaa 108,220,236\nv_lshlrev_b32 v252, 16, v196\nv_and_b32 v253, 0xffff0000, v196\nMaaa 104,212,236\nRa 148,11,40960\nFM 32,245,252\nMaaa 104,208,232\nFM 33,245,253\n"
"FM 40,249,252\nMaaa 44,200,232\nRa 152,10,43008\nFM 41,249,253\nMaaa 44,204,236\nD0 7,24,44,1024\nv_lshlrev_b32 v252, 16, v197\nMaaa 40,196,236\nRa 156,11,43008\n"
"v_and_b32 v253, 0xffff0000, v197\nMaaa 40,192,232\nFM 34,245,252\nFM 35,245,253\nMaaa 48,192,240\nFM 42,249,252\nFM 43,249,253\nMaaa 48,196,244\nv_lshlrev_b32 v252, 16, v198\n"
"Maaa 52,204,244\nv_and_b32 v253, 0xffff0000, v198\nFM 36,245,252\nMaaa 52,200,240\nFM 37,245,253\nFM 44,249,252\nMaaa 112,208,240\nFM 45,249,253\nv_lshlrev_b32 v252, 16, v199\n"
"Maaa 112,212,244\nD0 8,24,44,2048\nv_and_b32 v253, 0xffff0000, v199\nMaaa 116,220,244\nFM 38,245,252\nFM 39,245,253\nMaaa 116,216,240\nFM 46,249,252\nFM 47,249,253\n"
"Maaa 124,216,248\nv_lshlrev_b32 v252, 16, v212\nv_and_b32 v253, 0xffff0000, v212\nMaaa 124,220,252\nFM 48,245,252\nFM 49,245,253\nMaaa 120,212,252\nFM 56,249,252\nFM 57,249,253\n"
"Maaa 120,208,248\nv_lshlrev_b32 v252, 16, v213\nv_and_b32 v253, 0xffff0000, v213\nMaaa 60,200,248\nFM 50,245,252\nFM 51,245,253\nMaaa 60,204,252\nD0 9,24,44,3072\nFM 58,249,252\n"
"Maaa 56,196,252\nFM 59,249,253\nv_lshlrev_b32 v252, 16, v214\nMaaa 56,192,248\ns_waitcnt vmcnt(19) lgkmcnt(0)\ns_barrier\nMaaa 0,192,160\nTa 224,16,32768\n"
"v_and_b32 v253, 0xffff0000, v214\nMaaa 0,196,164\nTa 226,16,34816\nbuffer_load_dwordx4 v[224:227], v20, s[92:95], s88 offen offset:0\nMaaa 4,204,164\ns_add_u32 m0, s47, -49152\n"
"s_add_u32 s48, s48, 128\nMaaa 4,200,160\nTa 228,16,49152\nFM 52,245,252\nMaaa 64,208,160\nTa 230,16,51200\nFM 53,245,253\nMaaa 64,212,164\nD0 2,20,48,0\nFM 60,249,252\n"
"Maaa 68,220,164\nTa 232,17,32768\nFM 61,249,253\nMaaa 68,216,160\nTa 234,17,34816\nv_lshlrev_b32 v252, 16, v215\nMaaa 76,216,168\nv_and_b32 v253, 0xffff0000, v215\nFM 54,245,252\n"
"Maaa 76,220,172\nTa 236,17,49152\nFM 55,245,253\nMaaa 72,212,172\nTa 238,17,51200\nFM 62,249,252\nMaaa 72,208,168\nFM 63,249,253\nv_lshlrev_b32 v252, 16, v200\nMaaa 12,200,168\n"
"Ta 240,18,32768\nv_and_b32 v253, 0xffff0000, v200\nMaaa 12,204,172\nTa 242,18,34816\nFM 32,246,252\nMaaa 8,196,172\nD0 3,20,48,1024\nFM 33,246,253\nMaaa 8,192,168\n"
"Ta 244,18,49152\nFM 40,250,252\nMaaa 16,192,176\nTa 246,18,51200\nFM 41,250,253\nMaaa 16,196,180\nv_lshlrev_b32 v252, 16, v201\nMaaa 20,204,180\nTa 248,19,32768\n"
"v_and_b32 v253, 0xffff0000, v201\nMaaa 20,200,176\nTa 250,19,34816\nFM 34,246,252\nMaaa 80,208,176\nFM 35,246,253\nFM 42,250,252\nMaaa 80,212,180\nTa 252,19,49152\nFM 43,250,253\n"
"Maaa 84,220,180\nTa 254,19,51200\nv_lshlrev_b32 v252, 16, v202\nMaaa 84,216,176\nD0 4,20,48,2048\nv_and_b32 v253, 0xffff0000, v202\nMaaa 92,216,184\nFM 36,246,252\nFM 37,246,253\n"
"Maaa 92,220,188\nFM 44,250,252\nFM 45,250,253\nMaaa 88,212,188\nv_lshlrev_b32 v252, 16, v203\nv_and_b32 v253, 0xffff0000, v203\nMaaa 88,208,184\nFM 38,246,252\nFM 39,246,253\n"
"Maaa 28,200,184\nFM 46,250,252\nFM 47,250,253\nMaaa 28,204,188\nD0 5,20,48,3072\nv_lshlrev_b32 v252, 16, v216\nMaaa 24,196,188\nv_and_b32 v253, 0xffff0000, v216\nFM 48,246,252\n"
"Maaa 24,192,184\nWL 0\nMvaa 96,128,224\nTa 160,12,32768\nFM 49,246,253\nMvaa 96,132,228\nTa 162,12,34816\nbuffer_load_dwordx4 v[228:231], v20, s[92:95], s88 offen offset:896\n"
"Mvaa 100,140,228\ns_add_u32 m0, s47, -32768\ns_add_u32 s45, s45, 128\nMvaa 100,136,224\nTa 164,12,49152\nFM 56,250,252\nMvaa 160,144,224\nTa 166,12,51200\nFM 57,250,253\n"
"Mvaa 160,148,228\nD0 2,20,45,0\nv_lshlrev_b32 v252, 16, v217\nMvaa 164,156,228\nTa 168,13,32768\nv_and_b32 v253, 0xffff0000, v217\nMvaa 164,152,224\nTa 170,13,34816\n"
"FM 50,246,252\nMvaa 172,152,232\nFM 51,246,253\nFM 58,250,252\nMvaa 172,156,236\nTa 172,13,49152\nFM 59,250,253\nMvaa 168,148,236\nTa 174,13,51200\nv_lshlrev_b32 v252, 16, v218\n"
"Mvaa 168,144,232\nv_and_b32 v253, 0xffff0000, v218\nFM 52,246,252\nMvaa 108,136,232\nTa 176,14,32768\nFM 53,246,253\nMvaa 108,140,236\nTa 178,14,34816\nFM 60,250,252\n"
"Mvaa 104,132,236\nD0 3,20,45,1024\nFM 61,250,253\nMvaa 104,128,232\nTa 180,14,49152\nv_lshlrev_b32 v252, 16, v219\nMvaa 112,128,240\nTa 182,14,51200\n"
"v_and_b32 v253, 0xffff0000, v219\nMvaa 112,132,244\nFM 54,246,252\nWV 25\nv_cmp_ne_u32 vcc, s15, v23\ns_nop 0\ns_cbranch_vccnz .S147\n.S148:\nMvaa 116,140,244\nTa 184,15,32768\n"
"v_add_u32_dpp v22, v22, v22 row_ror:8 row_mask:0xf bank_mask:0xf\nMvaa 116,136,240\nTa 186,15,34816\nMvaa 176,144,240\n"
"v_add_u32_dpp v22, v22, v22 row_ror:4 row_mask:0xf bank_mask:0xf\nMvaa 176,148,244\nTa 188,15,49152\nMvaa 180,156,244\nTa 190,15,51200\n"
"v_add_u32_dpp v22, v22, v22 row_ror:2 row_mask:0xf bank_mask:0xf\nMvaa 180,152,240\nD0 4,20,45,2048\nMvaa 188,152,248\n"
"v_add_u32_dpp v22, v22, v22 row_ror:1 row_mask:0xf bank_mask:0xf\nMvaa 188,156,252\nMvaa 184,148,252\nv_mov_b32 v23, v22\nMvaa 184,144,248\nMvaa 124,136,248\n"
"v_permlane16_swap_b32 v22, v23\nv_add_u32 v22, v22, v23\nMvaa 124,140,252\nD0 5,20,45,3072\nv_mov_b32 v23, v22\nMvaa 120,132,252\nMvaa 120,128,248\ns_waitcnt vmcnt(19) lgkmcnt(0)\n"
"s_barrier\nMvaa 64,128,160\nRa 192,10,49152\nv_permlane32_swap_b32 v22, v23\nMvaa 64,132,164\ns_add_u32 m0, s47, 32768\ns_add_u32 s44, s44, 0x40000\n"
"buffer_load_dwordx4 v[232:235], v20, s[92:95], s88 offen offset:1792\nMvaa 68,140,164\nRa 196,11,49152\nv_add_u32 v22, v22, v23\nMvaa 68,136,160\nMvaa 128,144,160\n"
"Ra 200,10,51200\nv_readfirstlane_b32 s4, v22\nMvaa 128,148,164\nD0 6,24,44,0\nMvaa 132,156,164\nRa 204,11,51200\nMvaa 132,152,160\ns_cmp_gt_i32 s4, 0\ns_cselect_b32 s4, 1, 0\n"
"Mvaa 140,152,168\nRa 208,10,57344\ns_xor_b32 s4, s4, s12\nMvaa 140,156,172\ns_and_b32 s4, s4, 1\ns_lshl_b32 s4, s4, 13\nMvaa 136,148,172\nRa 212,11,57344\n"
"s_mul_i32 s72, s73, 25165824\nMvaa 136,144,168\ns_add_u32 s72, s72, s4\ns_and_b32 s4, s12, 1\nMvaa 76,136,168\nRa 216,10,59392\ns_lshl_b32 s4, s4, 21\nMvaa 76,140,172\n"
"D0 7,24,44,1024\ns_add_u32 s72, s72, s4\nMvaa 72,132,172\nRa 220,11,59392\ns_lshl3_add_u32 s2, s13, s11\nMvaa 72,128,168\ns_lshl_b32 s2, s2, 11\ns_lshl_b32 s3, s14, 22\n"
"Mvaa 80,128,176\ns_add_u32 s2, s2, s3\ns_add_u32 s2, s2, s72\nMvaa 80,132,180\ns_add_u32 s49, s2, 0xbe0000\nMvaa 84,140,180\nFM 55,246,253\nFM 62,250,252\nMvaa 84,136,176\n"
"FM 63,250,253\nv_lshlrev_b32 v252, 16, v204\nMvaa 144,144,176\nv_and_b32 v253, 0xffff0000, v204\nFM 32,247,252\nMvaa 144,148,180\nD0 8,24,44,2048\nFM 33,247,253\nMvaa 148,156,180\n"
"FM 40,251,252\nFM 41,251,253\nMvaa 148,152,176\nv_lshlrev_b32 v252, 16, v205\nv_and_b32 v253, 0xffff0000, v205\nMvaa 156,152,184\nFM 34,247,252\nFM 35,247,253\nMvaa 156,156,188\n"
"FM 42,251,252\nFM 43,251,253\nMvaa 152,148,188\nv_lshlrev_b32 v252, 16, v206\nv_and_b32 v253, 0xffff0000, v206\nMvaa 152,144,184\nFM 36,247,252\nFM 37,247,253\nMvaa 92,136,184\n"
"FM 44,251,252\nFM 45,251,253\nMvaa 92,140,188\nD0 9,24,44,3072\nv_lshlrev_b32 v252, 16, v207\nMvaa 88,132,188\nv_and_b32 v253, 0xffff0000, v207\nFM 38,247,252\nMvaa 88,128,184\n"
"s_waitcnt vmcnt(23) lgkmcnt(0)\nMaaa 0,192,160\nRa 128,10,0\nFM 39,247,253\nMaaa 0,196,164\ns_add_u32 m0, s47, 49152\ns_add_u32 s44, s44, 0x40000\n"
"buffer_load_dwordx4 v[236:239], v20, s[92:95], s88 offen offset:2688\nMaaa 4,204,164\nRa 132,11,0\nFM 46,251,252\nMaaa 4,200,160\nFM 47,251,253\nv_lshlrev_b32 v252, 16, v220\n"
"Maaa 64,208,160\nRa 136,10,2048\nv_and_b32 v253, 0xffff0000, v220\nMaaa 64,212,164\nD0 6,24,44,0\nFM 48,247,252\nMaaa 68,220,164\nRa 140,11,2048\nFM 49,247,253\nMaaa 68,216,160\n"
"FM 56,251,252\nFM 57,251,253\nMaaa 76,216,168\nRa 144,10,8192\nv_lshlrev_b32 v252, 16, v221\nMaaa 76,220,172\nv_and_b32 v253, 0xffff0000, v221\nFM 50,247,252\nMaaa 72,212,172\n"
"Ra 148,11,8192\nFM 51,247,253\nMaaa 72,208,168\nFM 58,251,252\nFM 59,251,253\nMaaa 12,200,168\nRa 152,10,10240\nv_lshlrev_b32 v252, 16, v222\nMaaa 12,204,172\nD0 7,24,44,1024\n"
"v_and_b32 v253, 0xffff0000, v222\nMaaa 8,196,172\nRa 156,11,10240\nFM 52,247,252\nMaaa 8,192,168\nFM 53,247,253\nFM 60,251,252\nMaaa 16,192,176\nFM 61,251,253\n"
"v_lshlrev_b32 v252, 16, v223\nMaaa 16,196,180\nv_and_b32 v253, 0xffff0000, v223\nMaaa 20,204,180\nFM 54,247,252\nFM 55,247,253\nMaaa 20,200,176\nFM 62,251,252\nFM 63,251,253\n"
"Maaa 80,208,176\nv_permlane32_swap_b32 v32, v48\nv_permlane32_swap_b32 v33, v49\nMaaa 80,212,180\nD0 8,24,44,2048\nv_permlane32_swap_b32 v34, v50\nMaaa 84,220,180\n"
"v_permlane32_swap_b32 v35, v51\nv_permlane32_swap_b32 v36, v52\nMaaa 84,216,176\nv_permlane32_swap_b32 v37, v53\nv_permlane32_swap_b32 v38, v54\nMaaa 92,216,184\n"
"v_permlane32_swap_b32 v39, v55\nv_permlane32_swap_b32 v40, v56\nMaaa 92,220,188\nv_permlane32_swap_b32 v41, v57\nv_permlane32_swap_b32 v42, v58\nMaaa 88,212,188\n"
"v_permlane32_swap_b32 v43, v59\nv_permlane32_swap_b32 v44, v60\nMaaa 88,208,184\nv_permlane32_swap_b32 v45, v61\nv_permlane32_swap_b32 v46, v62\nMaaa 28,200,184\n"
"v_permlane32_swap_b32 v47, v63\nAD 32,32,48\nMaaa 28,204,188\nD0 9,24,44,3072\nAD 33,33,49\nMaaa 24,196,188\nAD 34,34,50\nAD 35,35,51\nMaaa 24,192,184\n"
"s_waitcnt vmcnt(20) lgkmcnt(0)\ns_barrier\nMaaa 32,192,224\nTa 160,12,0\nAD 36,36,52\nMaaa 32,196,228\nTa 162,12,2048\nMaaa 36,204,228\ns_add_u32 m0, s47, -16384\n"
"s_add_u32 s48, s48, 128\nMaaa 36,200,224\nTa 164,12,16384\nAD 37,37,53\nMaaa 96,208,224\nTa 166,12,18432\nAD 38,38,54\nMaaa 96,212,228\nD0 2,20,48,0\nAD 39,39,55\n"
"Maaa 100,220,228\nTa 168,13,0\nAD 40,40,56\nMaaa 100,216,224\nTa 170,13,2048\nAD 41,41,57\nMaaa 108,216,232\nAD 42,42,58\nAD 43,43,59\nMaaa 108,220,236\nTa 172,13,16384\n"
"AD 44,44,60\nMaaa 104,212,236\nTa 174,13,18432\nAD 45,45,61\nMaaa 104,208,232\nAD 46,46,62\nAD 47,47,63\nMaaa 44,200,232\nTa 176,14,0\nv_permlane16_swap_b32 v32, v40\n"
"Maaa 44,204,236\nTa 178,14,2048\nv_permlane16_swap_b32 v33, v41\nMaaa 40,196,236\nD0 3,20,48,1024\nv_permlane16_swap_b32 v34, v42\nMaaa 40,192,232\nTa 180,14,16384\n"
"v_permlane16_swap_b32 v35, v43\nMaaa 48,192,240\nTa 182,14,18432\nv_permlane16_swap_b32 v36, v44\nMaaa 48,196,244\nv_permlane16_swap_b32 v37, v45\nMaaa 52,204,244\nTa 184,15,0\n"
"v_permlane16_swap_b32 v38, v46\nMaaa 52,200,240\nTa 186,15,2048\nv_permlane16_swap_b32 v39, v47\nMaaa 112,208,240\nAD 32,32,40\nAD 33,33,41\nMaaa 112,212,244\nTa 188,15,16384\n"
"AD 34,34,42\nMaaa 116,220,244\nTa 190,15,18432\nAD 35,35,43\nMaaa 116,216,240\nD0 4,20,48,2048\nAD 36,36,44\nMaaa 124,216,248\nAD 37,37,45\nAD 38,38,46\nMaaa 124,220,252\n"
"AD 39,39,47\nv_add_f32_dpp v32, v32, v32 row_ror:8 row_mask:0xf bank_mask:0xf\nMaaa 120,212,252\nv_add_f32_dpp v33, v33, v33 row_ror:8 row_mask:0xf bank_mask:0xf\n"
"v_add_f32_dpp v34, v34, v34 row_ror:8 row_mask:0xf bank_mask:0xf\nMaaa 120,208,248\nv_add_f32_dpp v35, v35, v35 row_ror:8 row_mask:0xf bank_mask:0xf\n"
"v_add_f32_dpp v36, v36, v36 row_ror:8 row_mask:0xf bank_mask:0xf\nMaaa 60,200,248\nv_add_f32_dpp v37, v37, v37 row_ror:8 row_mask:0xf bank_mask:0xf\n"
"v_add_f32_dpp v38, v38, v38 row_ror:8 row_mask:0xf bank_mask:0xf\nMaaa 60,204,252\nD0 5,20,48,3072\nv_add_f32_dpp v39, v39, v39 row_ror:8 row_mask:0xf bank_mask:0xf\n"
"Maaa 56,196,252\nv_add_f32_dpp v32, v32, v32 row_ror:4 row_mask:0xf bank_mask:0xf\nv_add_f32_dpp v33, v33, v33 row_ror:4 row_mask:0xf bank_mask:0xf\nMaaa 56,192,248\nWL 0\n"
"Mvaa 64,128,160\nTa 224,16,0\nv_add_f32_dpp v34, v34, v34 row_ror:4 row_mask:0xf bank_mask:0xf\n.W558:\ns_cbranch_execz .W562\nMvaa 64,132,164\nTa 226,16,2048\nMvaa 68,140,164\n"
"s_add_u32 m0, s47, -65536\ns_add_u32 s45, s45, 128\nMvaa 68,136,160\nTa 228,16,16384\nv_add_f32_dpp v35, v35, v35 row_ror:4 row_mask:0xf bank_mask:0xf\nMvaa 128,144,160\n"
"Ta 230,16,18432\nv_add_f32_dpp v36, v36, v36 row_ror:4 row_mask:0xf bank_mask:0xf\nMvaa 128,148,164\nD0 2,20,45,0\n"
"v_add_f32_dpp v37, v37, v37 row_ror:4 row_mask:0xf bank_mask:0xf\nMvaa 132,156,164\nTa 232,17,0\nv_add_f32_dpp v38, v38, v38 row_ror:4 row_mask:0xf bank_mask:0xf\n"
"Mvaa 132,152,160\nTa 234,17,2048\nv_add_f32_dpp v39, v39, v39 row_ror:4 row_mask:0xf bank_mask:0xf\nMvaa 140,152,168\n"
"v_add_f32_dpp v32, v32, v32 row_ror:2 row_mask:0xf bank_mask:0xf\nv_add_f32_dpp v33, v33, v33 row_ror:2 row_mask:0xf bank_mask:0xf\nMvaa 140,156,172\nTa 236,17,16384\n"
"v_add_f32_dpp v34, v34, v34 row_ror:2 row_mask:0xf bank_mask:0xf\nMvaa 136,148,172\nTa 238,17,18432\nv_add_f32_dpp v35, v35, v35 row_ror:2 row_mask:0xf bank_mask:0xf\n"
"Mvaa 136,144,168\nv_add_f32_dpp v36, v36, v36 row_ror:2 row_mask:0xf bank_mask:0xf\nv_add_f32_dpp v37, v37, v37 row_ror:2 row_mask:0xf bank_mask:0xf\nMvaa 76,136,168\nTa 240,18,0\n"
"v_add_f32_dpp v38, v38, v38 row_ror:2 row_mask:0xf bank_mask:0xf\nMvaa 76,140,172\nTa 242,18,2048\nv_add_f32_dpp v39, v39, v39 row_ror:2 row_mask:0xf bank_mask:0xf\n"
"Mvaa 72,132,172\nD0 3,20,45,1024\nv_lshrrev_b32 v48, 4, v0\nMvaa 72,128,168\nTa 244,18,16384\nv_and_b32 v49, 1, v0\nMvaa 80,128,176\nTa 246,18,18432\nv_lshlrev_b32 v49, 5, v49\n"
"Mvaa 80,132,180\nv_lshl_add_u32 v49, v48, 6, v49\nMvaa 84,140,180\nTa 248,19,0\ns_lshl_b32 s2, s11, 8\nMvaa 84,136,176\nTa 250,19,2048\ns_add_u32 s2, s2, 132096\nMvaa 144,144,176\n"
"v_add_u32 v49, s2, v49\nds_write_b128 v49, v[32:35]\nMvaa 144,148,180\nTa 252,19,16384\nds_write_b128 v49, v[36:39] offset:16\nMvaa 148,156,180\nTa 254,19,18432\n"
"v_and_b32 v50, 31, v0\nMvaa 148,152,176\nD0 4,20,45,2048\nv_lshlrev_b32 v50, 2, v50\nMvaa 156,152,184\ns_lshl_b32 s2, s11, 10\ns_add_u32 s2, s2, 133120\nMvaa 156,156,188\n"
"v_add_u32 v51, s2, v50\nv_add_u32 v50, 132096, v50\nMvaa 152,148,188\nv_lshl_add_u32 v52, v48, 4, s2\nMvaa 152,144,184\nMvaa 92,136,184\nMvaa 92,140,188\nD0 5,20,45,3072\n"
"Mvaa 88,132,188\nMvaa 88,128,184\ns_waitcnt vmcnt(18) lgkmcnt(0)\ns_barrier\nMvaa 96,128,224\nRa 192,10,16384\nds_read_b32 v32, v50 offset:0\nMvaa 96,132,228\n"
"s_add_u32 m0, s47, 0\ns_add_u32 s44, s44, 0x40000\nMvaa 100,140,228\nRa 196,11,16384\nMvaa 100,136,224\nds_read_b32 v33, v50 offset:256\nds_read_b32 v34, v50 offset:512\n"
"Mvaa 160,144,224\nRa 200,10,18432\nMvaa 160,148,228\nD0 6,24,44,0\nMvaa 164,156,228\nRa 204,11,18432\nds_read_b32 v35, v50 offset:768\nMvaa 164,152,224\n"
"ds_read_b32 v36, v50 offset:128\nMvaa 172,152,232\nRa 208,10,24576\nds_read_b32 v37, v50 offset:384\nMvaa 172,156,236\nds_read_b32 v38, v50 offset:640\nMvaa 168,148,236\n"
"Ra 212,11,24576\nMvaa 168,144,232\nds_read_b32 v39, v50 offset:896\nMvaa 108,136,232\nRa 216,10,26624\nMvaa 108,140,236\nD0 7,24,44,1024\nMvaa 104,132,236\nRa 220,11,26624\n"
"Mvaa 104,128,232\nMvaa 112,128,240\nMvaa 112,132,244\nMvaa 116,140,244\nMvaa 116,136,240\nMvaa 176,144,240\nMvaa 176,148,244\nD0 8,24,44,2048\nMvaa 180,156,244\nWL 2\nAD 32,32,33\n"
"Mvaa 180,152,240\nAD 34,34,35\nMvaa 188,152,248\nAD 36,36,37\nAD 38,38,39\nMvaa 188,156,252\nAD 32,32,34\nMvaa 184,148,252\nAD 36,36,38\nMvaa 184,144,248\nCV 32,32,36\n"
"v_and_b32 v36, 0xffff0000, v32\nMvaa 124,136,248\nv_lshlrev_b32 v32, 16, v32\nMvaa 124,140,252\nD0 9,24,44,3072\nMvaa 120,132,252\nv_mul_f32 v33, 0xbfb8aa3b, v32\n"
"Mvaa 120,128,248\nWL 0\nMaaa 32,192,224\nRa 128,10,32768\nMaaa 32,196,228\ns_add_u32 m0, s47, 16384\ns_add_u32 s44, s44, 0x40000\nMaaa 36,204,228\nRa 132,11,32768\nEX 33,33\n"
"Maaa 36,200,224\nv_add_f32 v33, 1.0, v33\nMaaa 96,208,224\nRa 136,10,34816\nv_rcp_f32 v33, v33\nMaaa 96,212,228\nD0 6,24,44,0\nMaaa 100,220,228\nRa 140,11,34816\nMU 33,32,33\n"
"Maaa 100,216,224\nCV 33,33,33\nMaaa 108,216,232\nRa 144,10,40960\nv_lshlrev_b32 v33, 16, v33\nMaaa 108,220,236\nMU 33,33,36\nMaaa 104,212,236\nRa 148,11,40960\nMaaa 104,208,232\n"
"CV 33,33,33\nv_lshlrev_b32 v33, 16, v33\nMaaa 44,200,232\nRa 152,10,43008\nMaaa 44,204,236\nD0 7,24,44,1024\nMaaa 40,196,236\nRa 156,11,43008\nds_write_b32 v51, v33\n"
"Maaa 40,192,232\nRv 244,52,0\nMaaa 48,192,240\nRv 248,52,64\ns_and_b32 s2, s74, 15\nMaaa 48,196,244\ns_sub_u32 s2, s2, 2\ns_cmp_lt_u32 s2, 14\ns_cselect_b32 s5, 0, 0x40000000\n"
"Maaa 52,204,244\ns_and_b32 s2, s2, 15\nMaaa 52,200,240\ns_mul_i32 s4, s73, 14\ns_add_u32 s4, s4, s2\nMaaa 112,208,240\nv_and_b32 v49, 15, v0\nMaaa 112,212,244\nD0 8,24,44,2048\n"
"Maaa 116,220,244\nv_lshlrev_b32 v50, 3, v49\nv_and_b32 v51, 1, v48\nMaaa 116,216,240\nv_lshl_add_u32 v50, v51, 2, v50\nMaaa 124,216,248\ns_mul_i32 s2, s11, 112\n"
"v_add_u32 v50, s2, v50\nMaaa 124,220,252\nv_lshrrev_b32 v51, 4, v50\nMaaa 120,212,252\nv_lshrrev_b32 v48, 1, v48\nMaaa 120,208,248\nv_mad_u32_u24 v51, v48, 28, v51\n"
"v_mul_u32_u24 v51, 7168, v51\nMaaa 60,200,248\nv_and_b32 v50, 15, v50\nMaaa 60,204,252\nD0 9,24,44,3072\nMaaa 56,196,252\nv_lshl_add_u32 v51, v50, 2, v51\n"
"v_cmp_gt_u32 vcc, 14, v49\ns_lshl_b32 s4, s4, 6\ns_add_u32 s4, s4, 0xc04c800\nv_cndmask_b32 v51, v254, v51, vcc\nMaaa 56,192,248\ns_waitcnt vmcnt(16) lgkmcnt(3)\ns_barrier\n"
"Maaa 0,192,160\nTa 224,16,32768\ns_or_b32 s4, s4, s5\nMaaa 0,196,164\nTa 226,16,34816\nMaaa 4,204,164\ns_add_u32 m0, s47, -49152\ns_add_u32 s48, s48, 128\nMaaa 4,200,160\n"
"Ta 228,16,49152\nWL 3\nMaaa 64,208,160\nTa 230,16,51200\nMaaa 64,212,164\nD0 2,20,48,0\nMaaa 68,220,164\nTa 232,17,32768\nv_lshlrev_b32 v252, 16, v224\nMaaa 68,216,160\n"
"Ta 234,17,34816\nMaaa 76,216,168\nv_and_b32 v253, 0xffff0000, v224\nMU 32,244,252\nMaaa 76,220,172\nTa 236,17,49152\nMaaa 72,212,172\nTa 238,17,51200\nMaaa 72,208,168\n"
"MU 33,244,253\nMU 40,248,252\nMaaa 12,200,168\nTa 240,18,32768\nMaaa 12,204,172\nTa 242,18,34816\nMaaa 8,196,172\nD0 3,20,48,1024\nMU 41,248,253\nMaaa 8,192,168\nTa 244,18,49152\n"
"Maaa 16,192,176\nTa 246,18,51200\nv_lshlrev_b32 v252, 16, v225\nMaaa 16,196,180\nv_and_b32 v253, 0xffff0000, v225\nMaaa 20,204,180\nTa 248,19,32768\nMaaa 20,200,176\n"
"Ta 250,19,34816\nMU 34,244,252\nMaaa 80,208,176\nMU 35,244,253\nMaaa 80,212,180\nTa 252,19,49152\nMaaa 84,220,180\nTa 254,19,51200\nMU 42,248,252\nMaaa 84,216,176\n"
"D0 4,20,48,2048\nMaaa 92,216,184\nMU 43,248,253\nv_lshlrev_b32 v252, 16, v226\nMaaa 92,220,188\nv_and_b32 v253, 0xffff0000, v226\nMaaa 88,212,188\nMU 36,244,252\nMaaa 88,208,184\n"
"MU 37,244,253\nMU 44,248,252\nMaaa 28,200,184\nMU 45,248,253\nMaaa 28,204,188\nD0 5,20,48,3072\nMaaa 24,196,188\nv_lshlrev_b32 v252, 16, v227\nv_and_b32 v253, 0xffff0000, v227\n"
"Maaa 24,192,184\nWL 0\nMvaa 96,128,224\nTa 160,12,32768\nMU 38,244,252\nMvaa 96,132,228\nTa 162,12,34816\nMvaa 100,140,228\ns_add_u32 m0, s47, -32768\ns_add_u32 s45, s45, 128\n"
"Mvaa 100,136,224\nTa 164,12,49152\nMU 39,244,253\nMvaa 160,144,224\nTa 166,12,51200\nMvaa 160,148,228\nD0 2,20,45,0\nMvaa 164,156,228\nTa 168,13,32768\nMU 46,248,252\n"
"Mvaa 164,152,224\nTa 170,13,34816\nMvaa 172,152,232\nMU 47,248,253\nv_lshlrev_b32 v252, 16, v228\nMvaa 172,156,236\nTa 172,13,49152\nMvaa 168,148,236\nTa 174,13,51200\n"
"Mvaa 168,144,232\nv_and_b32 v253, 0xffff0000, v228\nFM 32,245,252\nMvaa 108,136,232\nTa 176,14,32768\nMvaa 108,140,236\nTa 178,14,34816\nMvaa 104,132,236\nD0 3,20,45,1024\n"
"FM 33,245,253\nMvaa 104,128,232\nTa 180,14,49152\nMvaa 112,128,240\nTa 182,14,51200\nFM 40,249,252\nMvaa 112,132,244\nFM 41,249,253\nMvaa 116,140,244\nTa 184,15,32768\n"
"Mvaa 116,136,240\nTa 186,15,34816\nv_lshlrev_b32 v252, 16, v229\nMvaa 176,144,240\nv_and_b32 v253, 0xffff0000, v229\nMvaa 176,148,244\nTa 188,15,49152\nMvaa 180,156,244\n"
"Ta 190,15,51200\nFM 34,245,252\nMvaa 180,152,240\nD0 4,20,45,2048\nMvaa 188,152,248\nFM 35,245,253\nFM 42,249,252\nMvaa 188,156,252\nFM 43,249,253\nMvaa 184,148,252\n"
"v_lshlrev_b32 v252, 16, v230\nMvaa 184,144,248\nv_and_b32 v253, 0xffff0000, v230\nFM 36,245,252\nMvaa 124,136,248\nFM 37,245,253\nMvaa 124,140,252\nD0 5,20,45,3072\n"
"Mvaa 120,132,252\nFM 44,249,252\nFM 45,249,253\nMvaa 120,128,248\ns_waitcnt vmcnt(16) lgkmcnt(0)\ns_barrier\nMvaa 64,128,160\nRa 192,10,49152\nv_lshlrev_b32 v252, 16, v231\n"
"Mvaa 64,132,164\ns_add_u32 m0, s47, 32768\ns_add_u32 s44, s44, 0x40000\nMvaa 68,140,164\nRa 196,11,49152\nMvaa 68,136,160\nv_and_b32 v253, 0xffff0000, v231\nFM 38,245,252\n"
"Mvaa 128,144,160\nRa 200,10,51200\nMvaa 128,148,164\nD0 6,24,44,0\nMvaa 132,156,164\nRa 204,11,51200\nFM 39,245,253\nMvaa 132,152,160\nFM 46,249,252\nMvaa 140,152,168\n"
"Ra 208,10,57344\nFM 47,249,253\nMvaa 140,156,172\nv_lshlrev_b32 v252, 16, v232\nMvaa 136,148,172\nRa 212,11,57344\nMvaa 136,144,168\nv_and_b32 v253, 0xffff0000, v232\n"
"FM 32,246,252\nMvaa 76,136,168\nRa 216,10,59392\nMvaa 76,140,172\nD0 7,24,44,1024\nMvaa 72,132,172\nRa 220,11,59392\nFM 33,246,253\nMvaa 72,128,168\nFM 40,250,252\n"
"Mvaa 80,128,176\nFM 41,250,253\nv_lshlrev_b32 v252, 16, v233\nMvaa 80,132,180\nv_and_b32 v253, 0xffff0000, v233\nMvaa 84,140,180\nFM 34,246,252\nMvaa 84,136,176\nFM 35,246,253\n"
"FM 42,250,252\nMvaa 144,144,176\nFM 43,250,253\nMvaa 144,148,180\nD0 8,24,44,2048\nMvaa 148,156,180\nv_lshlrev_b32 v252, 16, v234\nv_and_b32 v253, 0xffff0000, v234\n"
"Mvaa 148,152,176\nFM 36,246,252\nMvaa 156,152,184\nFM 37,246,253\nFM 44,250,252\nMvaa 156,156,188\nFM 45,250,253\nMvaa 152,148,188\nv_lshlrev_b32 v252, 16, v235\nMvaa 152,144,184\n"
"v_and_b32 v253, 0xffff0000, v235\nFM 38,246,252\nMvaa 92,136,184\nFM 39,246,253\nMvaa 92,140,188\nD0 9,24,44,3072\nMvaa 88,132,188\nFM 46,250,252\nFM 47,250,253\nMvaa 88,128,184\n"
"WL 0\nMaaa 0,192,160\nRa 128,10,0\nv_lshlrev_b32 v252, 16, v236\nMaaa 0,196,164\ns_add_u32 m0, s47, 49152\ns_add_u32 s44, s44, 0x40000\nMaaa 4,204,164\nRa 132,11,0\n"
"Maaa 4,200,160\nv_and_b32 v253, 0xffff0000, v236\nFM 32,247,252\nMaaa 64,208,160\nRa 136,10,2048\nMaaa 64,212,164\nD0 6,24,44,0\nMaaa 68,220,164\nRa 140,11,2048\nFM 33,247,253\n"
"Maaa 68,216,160\nFM 40,251,252\nMaaa 76,216,168\nRa 144,10,8192\nFM 41,251,253\nMaaa 76,220,172\nv_lshlrev_b32 v252, 16, v237\nMaaa 72,212,172\nRa 148,11,8192\nMaaa 72,208,168\n"
"v_and_b32 v253, 0xffff0000, v237\nFM 34,247,252\nMaaa 12,200,168\nRa 152,10,10240\nMaaa 12,204,172\nD0 7,24,44,1024\nMaaa 8,196,172\nRa 156,11,10240\nFM 35,247,253\n"
"Maaa 8,192,168\nFM 42,251,252\nMaaa 16,192,176\nFM 43,251,253\nv_lshlrev_b32 v252, 16, v238\nMaaa 16,196,180\nv_and_b32 v253, 0xffff0000, v238\nMaaa 20,204,180\nFM 36,247,252\n"
"Maaa 20,200,176\nFM 37,247,253\nFM 44,251,252\nMaaa 80,208,176\nFM 45,251,253\nMaaa 80,212,180\nD0 8,24,44,2048\nMaaa 84,220,180\nv_lshlrev_b32 v252, 16, v239\n"
"v_and_b32 v253, 0xffff0000, v239\nMaaa 84,216,176\nFM 38,247,252\nMaaa 92,216,184\nFM 39,247,253\nFM 46,251,252\nMaaa 92,220,188\nFM 47,251,253\nMaaa 88,212,188\n"
"v_permlane32_swap_b32 v32, v40\nMaaa 88,208,184\nv_permlane32_swap_b32 v33, v41\nv_permlane32_swap_b32 v34, v42\nMaaa 28,200,184\nv_permlane32_swap_b32 v35, v43\nMaaa 28,204,188\n"
"D0 9,24,44,3072\nMaaa 24,196,188\nv_permlane32_swap_b32 v36, v44\nv_permlane32_swap_b32 v37, v45\nMaaa 24,192,184\nWV 16\ns_barrier\nMaaa 32,192,224\nTa 160,12,0\n"
"v_permlane32_swap_b32 v38, v46\nMaaa 32,196,228\nTa 162,12,2048\nMaaa 36,204,228\ns_add_u32 m0, s47, -16384\ns_add_u32 s48, s48, 128\nMaaa 36,200,224\nTa 164,12,16384\n"
"v_permlane32_swap_b32 v39, v47\nMaaa 96,208,224\nTa 166,12,18432\nMaaa 96,212,228\nD0 2,20,48,0\nMaaa 100,220,228\nTa 168,13,0\nAD 32,32,40\nMaaa 100,216,224\nTa 170,13,2048\n"
"Maaa 108,216,232\nAD 33,33,41\nAD 34,34,42\nMaaa 108,220,236\nTa 172,13,16384\nMaaa 104,212,236\nTa 174,13,18432\nMaaa 104,208,232\nAD 35,35,43\nAD 36,36,44\nMaaa 44,200,232\n"
"Ta 176,14,0\nMaaa 44,204,236\nTa 178,14,2048\nMaaa 40,196,236\nD0 3,20,48,1024\nAD 37,37,45\nMaaa 40,192,232\nTa 180,14,16384\nMaaa 48,192,240\nTa 182,14,18432\nAD 38,38,46\n"
"Maaa 48,196,244\nAD 39,39,47\nMaaa 52,204,244\n.W559:\ns_cbranch_execz .W563\nTa 184,15,0\nMaaa 52,200,240\nTa 186,15,2048\nv_permlane16_swap_b32 v32, v36\nMaaa 112,208,240\n"
"v_permlane16_swap_b32 v33, v37\nMaaa 112,212,244\nTa 188,15,16384\nMaaa 116,220,244\nTa 190,15,18432\nv_permlane16_swap_b32 v34, v38\nMaaa 116,216,240\nD0 4,20,48,2048\n"
"Maaa 124,216,248\nv_permlane16_swap_b32 v35, v39\nAD 32,32,36\nMaaa 124,220,252\nAD 33,33,37\nMaaa 120,212,252\nAD 34,34,38\nMaaa 120,208,248\nAD 35,35,39\nSv 32,51,16,4,0\n"
"Maaa 60,200,248\nMaaa 60,204,252\nD0 5,20,48,3072\nMaaa 56,196,252\nMaaa 56,192,248\nWL 0\nMvaa 64,128,160\nTa 224,16,0\ns_lshr_b32 s3, s11, 1\nMvaa 64,132,164\nTa 226,16,2048\n"
"Mvaa 68,140,164\ns_lshl_b32 s3, s3, 13\nMvaa 68,136,160\nTa 228,16,16384\nMvaa 128,144,160\nTa 230,16,18432\nMvaa 128,148,164\ns_lshl_b32 s2, s11, 12\nMvaa 132,156,164\n"
"Ta 232,17,0\nMvaa 132,152,160\nTa 234,17,2048\nMvaa 140,152,168\ns_add_u32 s2, s2, s3\ns_add_u32 s2, s2, s72\nMvaa 140,156,172\nTa 236,17,16384\nMvaa 136,148,172\nTa 238,17,18432\n"
"Mvaa 136,144,168\ns_lshl_b32 s3, s11, 12\nMvaa 76,136,168\nTa 240,18,0\nMvaa 76,140,172\nTa 242,18,2048\nMvaa 72,132,172\ns_add_u32 s89, s3, 0x0\nMvaa 72,128,168\nTa 244,18,16384\n"
"Mvaa 80,128,176\nTa 246,18,18432\ns_mov_b32 s88, 0xffff8000\nMvaa 80,132,180\ns_lshl_b32 s4, s13, 1\nMvaa 84,140,180\nTa 248,19,0\nMvaa 84,136,176\nTa 250,19,2048\n"
"Mvaa 144,144,176\ns_sub_u32 s79, 15, s4\nMvaa 144,148,180\nTa 252,19,16384\nMvaa 148,156,180\nTa 254,19,18432\nMvaa 148,152,176\ns_add_u32 s96, s2, 0x1310000\nMvaa 156,152,184\n"
"s_mov_b32 s63, 0x53800000\ns_mov_b32 s64, 0x2b800000\nMvaa 156,156,188\ns_sub_u32 s98, s4, 1\nMvaa 152,148,188\ns_lshl_b32 s5, s13, 5\nMvaa 152,144,184\ns_add_u32 s5, s5, s11\n"
"Mvaa 92,136,184\ns_lshl_b32 s65, s5, 9\nMvaa 92,140,188\ns_add_u32 s65, s65, s72\nMvaa 88,132,188\ns_add_u32 s65, s65, 0x1580000\nMvaa 88,128,184\ns_waitcnt vmcnt(13) lgkmcnt(0)\n"
"s_barrier\nMvaa 96,128,224\nRa 192,10,16384\ns_and_b32 s66, s72, 0x2000\ns_add_u32 m0, s47, 65536\ns_add_u32 s46, s56, 0x0\nMvaa 96,132,228\ns_add_u32 s66, s66, s56\n"
"Mvaa 100,140,228\nRa 196,11,16384\nD1 1,16,46,0\nMvaa 100,136,224\ns_add_u32 s66, s66, 0x100000\nMvaa 160,144,224\nRa 200,10,18432\nMvaa 160,148,228\ns_and_b32 s5, s12, 1\n"
"Mvaa 164,156,228\nRa 204,11,18432\nMvaa 164,152,224\ns_mul_i32 s58, s5, 0x202000\nD1 1,16,46,1024\nMvaa 172,152,232\nRa 208,10,24576\ns_xor_b32 s58, s58, s72\nMvaa 172,156,236\n"
"s_add_u32 s58, s58, 0x15c0000\nMvaa 168,148,236\nRa 212,11,24576\nMvaa 168,144,232\ns_lshl_b32 s5, s5, 8\nMvaa 108,136,232\nRa 216,10,26624\nD1 1,16,46,2048\nMvaa 108,140,236\n"
"s_add_u32 s61, s58, s5\nMvaa 104,132,236\nRa 220,11,26624\nMvaa 104,128,232\ns_lshl4_add_u32 s60, s13, s61\nMvaa 112,128,240\ns_add_u32 s54, s60, 0\ns_xor_b32 s60, s54, 0x100\n"
"Mvaa 112,132,244\ns_cmp_eq_u32 s11, 0\ns_cselect_b32 s54, s54, 0x40000000\nD1 1,16,46,3072\nMvaa 116,140,244\ns_getpc_b64 s[8:9]\n.PC167:\ns_add_u32 s8, s8, .HD143-.PC167\n"
"s_addc_u32 s9, s9, 0\nMvaa 116,136,240\ns_mov_b32 s97, 0xff800000\nMvaa 176,144,240\ns_mov_b32 s90, 3\ns_cmp_lt_u32 s11, 2\ns_cselect_b32 s92, 2, 0\ns_add_u32 m0, s47, 81920\n"
"s_add_u32 s46, s56, 0x1000\nMvaa 176,148,244\ns_add_u32 s86, s2, 0x20000\nMvaa 180,156,244\ns_add_u32 s87, s2, 0x410000\ns_cmp_eq_u32 s13, 7\ns_cselect_b32 s92, s92, 0\n"
"D1 1,16,46,0\nMvaa 180,152,240\ns_cmp_gt_u32 s13, 5\ns_cselect_b32 s4, s12, 15\nMvaa 188,152,248\ns_cmp_lt_u32 s4, 14\ns_cselect_b32 s5, s11, 3\nMvaa 188,156,252\n"
"s_cmp_lt_u32 s5, 2\ns_cselect_b32 s91, 1, 100\nMvaa 184,148,252\nv_and_b32 v27, 0x70, v1\nbuffer_load_dword v27, v27, s[16:19], s61 offen offset:128 sc0 sc1\nMvaa 184,144,248\n"
"s_lshr_b32 s2, s13, 2\ns_bfe_u32 s3, s13, 0x10001\nD1 1,16,46,1024\nMvaa 124,136,248\ns_mul_i32 s3, s3, 17\ns_add_u32 s2, s2, s3\nMvaa 124,140,252\ns_lshl2_add_u32 s3, s14, 0\n"
"Mvaa 120,132,252\ns_lshl_b32 s3, s3, 2\ns_lshr_b32 s3, 0xe420ca86, s3\nMvaa 120,128,248\nWL 0\nMaaa 32,192,224\nRa 128,10,32768\ns_and_b32 s3, s3, 15\nD1 1,16,46,2048\n"
"Maaa 32,196,228\ns_add_u32 s2, s2, s3\ns_cmp_eq_u32 s2, 32\ns_cselect_b32 s4, 16, s2\nMaaa 36,204,228\nRa 132,11,32768\ns_mul_i32 s75, s4, 65536\nMaaa 36,200,224\n"
"s_lshr_b32 s2, s13, 2\ns_bfe_u32 s3, s13, 0x10001\nMaaa 96,208,224\nRa 136,10,34816\nMaaa 96,212,228\ns_mul_i32 s3, s3, 17\ns_add_u32 s2, s2, s3\nD1 1,16,46,3072\n"
"Maaa 100,220,228\nRa 140,11,34816\ns_lshl2_add_u32 s3, s14, 1\nMaaa 100,216,224\ns_lshl_b32 s3, s3, 2\nMaaa 108,216,232\nRa 144,10,40960\ns_lshr_b32 s3, 0xe420ca86, s3\n"
"s_add_u32 m0, s47, 16384\ns_add_u32 s46, s56, 0x4000\nMaaa 108,220,236\ns_and_b32 s3, s3, 15\ns_add_u32 s2, s2, s3\nMaaa 104,212,236\nRa 148,11,40960\ns_cmp_eq_u32 s2, 32\n"
"s_cselect_b32 s4, 16, s2\nD1 1,16,46,0\nMaaa 104,208,232\ns_mul_i32 s76, s4, 65536\ns_lshr_b32 s2, s13, 2\nMaaa 44,200,232\nRa 152,10,43008\ns_bfe_u32 s3, s13, 0x10001\n"
"Maaa 44,204,236\ns_mul_i32 s3, s3, 17\nMaaa 40,196,236\nRa 156,11,43008\ns_add_u32 s2, s2, s3\nMaaa 40,192,232\ns_lshl2_add_u32 s3, s14, 2\ns_lshl_b32 s3, s3, 2\nD1 1,16,46,1024\n"
"Maaa 48,192,240\ns_lshr_b32 s3, 0xe420ca86, s3\ns_and_b32 s3, s3, 15\nMaaa 48,196,244\ns_add_u32 s2, s2, s3\ns_cmp_eq_u32 s2, 32\ns_cselect_b32 s4, 16, s2\nMaaa 52,204,244\n"
"s_mul_i32 s77, s4, 65536\ns_lshr_b32 s2, s13, 2\nMaaa 52,200,240\ns_bfe_u32 s3, s13, 0x10001\ns_mul_i32 s3, s3, 17\nMaaa 112,208,240\ns_add_u32 s2, s2, s3\nD1 1,16,46,2048\n"
"Maaa 112,212,244\ns_lshl2_add_u32 s3, s14, 3\ns_lshl_b32 s3, s3, 2\nMaaa 116,220,244\ns_lshr_b32 s3, 0xe420ca86, s3\ns_and_b32 s3, s3, 15\nMaaa 116,216,240\ns_add_u32 s2, s2, s3\n"
"s_cmp_eq_u32 s2, 32\ns_cselect_b32 s4, 16, s2\nMaaa 124,216,248\ns_mul_i32 s78, s4, 65536\ns_lshr_b32 s3, s11, 1\nMaaa 124,220,252\ns_lshl_b32 s3, s3, 15\ns_add_u32 s2, s72, s3\n"
"D1 1,16,46,3072\nMaaa 120,212,252\ns_and_b32 s3, s11, 1\nMaaa 120,208,248\ns_lshl_b32 s3, s3, 14\ns_add_u32 s2, s2, s3\nMaaa 60,200,248\ns_add_u32 s2, s2, 0x400000\n"
"s_mov_b32 s55, 4096\nMaaa 60,204,252\ns_add_u32 s75, s75, s2\nMaaa 56,196,252\ns_add_u32 s76, s76, s2\ns_add_u32 s77, s77, s2\nMaaa 56,192,248\ns_waitcnt vmcnt(18) lgkmcnt(0)\n"
"s_barrier\nMaaa 0,192,160\nTa 224,16,32768\ns_add_u32 s78, s78, s2\nMaaa 0,196,164\nTa 226,16,34816\nMaaa 4,204,164\nMaaa 4,200,160\nTa 228,16,49152\nMaaa 64,208,160\n"
"Ta 230,16,51200\nMaaa 64,212,164\nMaaa 68,220,164\nTa 232,17,32768\nMaaa 68,216,160\nTa 234,17,34816\nMaaa 76,216,168\nWV 7\nv_cmp_ne_u32 vcc, s15, v27\ns_nop 0\n"
"s_cbranch_vccnz .S168\n.S169:\ns_sub_u32 s45, s86, 131072\nMaaa 76,220,172\nTa 236,17,49152\ns_add_u32 m0, s47, 0\ns_add_u32 s46, s45, 0x0\nMaaa 72,212,172\nTa 238,17,51200\n"
"Maaa 72,208,168\nD1 1,16,46,0\nMaaa 12,200,168\nTa 240,18,32768\ns_mov_b32 s36, s68\nMaaa 12,204,172\nTa 242,18,34816\nMaaa 8,196,172\ns_and_b32 s37, s69, 0xffff\nMaaa 8,192,168\n"
"Ta 244,18,49152\nMaaa 16,192,176\nTa 246,18,51200\nD1 1,16,46,1024\nMaaa 16,196,180\ns_mov_b32 s38, 0x1c0000\nMaaa 20,204,180\nTa 248,19,32768\nMaaa 20,200,176\nTa 250,19,34816\n"
"Maaa 80,208,176\ns_mov_b32 s39, 0x20000\ns_and_b32 s2, s74, 15\nMaaa 80,212,180\nTa 252,19,49152\nD1 1,16,46,2048\nMaaa 84,220,180\nTa 254,19,51200\nMaaa 84,216,176\n"
"s_max_u32 s2, s2, 2\nMaaa 92,216,184\ns_sub_u32 s2, s2, 2\ns_mul_i32 s3, s73, 14\nMaaa 92,220,188\ns_add_u32 s2, s2, s3\nMaaa 88,212,188\nD1 1,16,46,3072\nMaaa 88,208,184\n"
"s_lshl2_add_u32 s2, s2, s11\nMaaa 28,200,184\ns_mul_i32 s3, s2, 4682\ns_lshr_b32 s3, s3, 16\nMaaa 28,204,188\ns_mul_i32 s22, s3, 14\nMaaa 24,196,188\ns_sub_u32 s22, s2, s22\n"
"Maaa 24,192,184\nWL 0\nMvaa 96,128,224\nTa 160,12,32768\ns_add_u32 m0, s47, -32768\ns_add_u32 s46, s56, 0x5000\nD1 1,16,46,0\nMvaa 96,132,228\nTa 162,12,34816\nMvaa 100,140,228\n"
"s_and_b32 s21, s3, 28\nMvaa 100,136,224\nTa 164,12,49152\nMvaa 108,136,232\nTa 166,12,51200\ns_add_u32 s21, s21, s3\nMvaa 108,140,236\nD1 1,16,46,1024\nMvaa 104,132,236\n"
"Ta 168,13,32768\nMvaa 104,128,232\nTa 170,13,34816\nMvaa 112,128,240\ns_lshl_b32 s21, s21, 4\ns_add_u32 s21, s21, 1024\nMvaa 112,132,244\nTa 172,13,49152\nMvaa 116,140,244\n"
"Ta 174,13,51200\nD1 1,16,46,2048\nMvaa 116,136,240\ns_xor_b32 s3, s3, 28\nMvaa 124,136,248\nTa 176,14,32768\ns_bfe_u32 s41, s3, 0x30002\nMvaa 124,140,252\nTa 178,14,34816\n"
"s_cmp_gt_u32 s22, 6\ns_cselect_b32 s2, 1179648, 0\ns_cselect_b32 s1, 16, 0\nMvaa 120,132,252\ns_add_u32 s41, s41, s1\nCV 48,96,97\nMvaa 120,128,248\nTa 180,14,49152\n"
"D1 1,16,46,3072\nMvaa 160,144,224\nTa 182,14,51200\ns_lshl4_add_u32 s41, s41, 0xc048400\nMvaa 160,148,228\ns_and_b32 s1, s3, 28\nMvaa 164,156,228\nTa 184,15,32768\n"
"s_add_u32 m0, s47, -65536\ns_add_u32 s46, s45, 0x10000\nMvaa 164,152,224\nTa 186,15,34816\nCV 49,98,99\nMvaa 172,152,232\nD1 1,16,46,0\ns_add_u32 s3, s3, s1\nMvaa 172,156,236\n"
"Ta 188,15,49152\nCV 50,100,101\nMvaa 168,148,236\nTa 190,15,51200\nMvaa 168,144,232\ns_lshl_b32 s3, s3, 11\nCV 51,102,103\nMvaa 176,144,240\ns_add_u32 s2, s2, s3\n"
"s_lshl_b32 s3, s22, 17\nMvaa 176,148,244\nD1 1,16,46,1024\nMvaa 180,156,244\ns_add_u32 s2, s2, s3\nCV 52,104,105\nMvaa 180,152,240\ns_add_u32 s20, s2, 0xce0000\nMvaa 188,152,248\n"
"s_lshl_b32 s2, s22, 7\ns_add_u32 s40, s2, 0xc04b800\nMvaa 188,156,252\ns_mul_i32 s2, s21, 896\nMvaa 184,148,252\nD1 1,16,46,2048\nCV 53,106,107\nMvaa 184,144,248\nWL 0\ns_barrier\n"
"Mvaa 64,128,160\nRa 192,10,49152\ns_lshl_b32 s3, s22, 6\nMvaa 64,132,164\ns_add_u32 s22, s2, s3\nMvaa 68,140,164\nRa 196,11,49152\nMvaa 68,136,160\nD1 1,16,46,3072\nCV 54,108,109\n"
"Mvaa 76,136,168\nRa 200,10,51200\ns_add_u32 s23, s22, 57344\nMvaa 76,140,172\nCV 55,110,111\nCV 56,112,113\nMvaa 72,132,172\nRa 204,11,51200\ns_add_u32 m0, s47, 32768\n"
"s_add_u32 s46, s45, 0x8000\nMvaa 72,128,168\nCV 57,114,115\nCV 58,116,117\nMvaa 80,128,176\nRa 208,10,57344\nD1 1,16,46,0\nMvaa 80,132,180\nCV 59,118,119\nMvaa 84,140,180\n"
"Ra 212,11,57344\nCV 60,120,121\nMvaa 84,136,176\nCV 61,122,123\nMvaa 92,136,184\nRa 216,10,59392\nCV 62,124,125\nMvaa 92,140,188\nD1 1,16,46,1024\nMvaa 88,132,188\n"
"Ra 220,11,59392\nCV 63,126,127\nMvaa 88,128,184\nCV 32,64,65\nMvaa 128,144,160\nCV 33,66,67\nCV 34,68,69\nMvaa 128,148,164\nCV 35,70,71\nMvaa 132,156,164\nD1 1,16,46,2048\n"
"Mvaa 132,152,160\nCV 36,72,73\nCV 37,74,75\nMvaa 140,152,168\nCV 38,76,77\nMvaa 140,156,172\nCV 39,78,79\nCV 40,80,81\nMvaa 136,148,172\nCV 41,82,83\nMvaa 136,144,168\n"
"D1 1,16,46,3072\nCV 42,84,85\nMvaa 144,144,176\nCV 43,86,87\nMvaa 144,148,180\nCV 44,88,89\nMvaa 148,156,180\ns_add_u32 m0, s47, 49152\ns_add_u32 s46, s45, 0x400000\n"
"Mvaa 148,152,176\nCV 45,90,91\nMvaa 156,152,184\nD1 1,16,46,0\nCV 46,92,93\nMvaa 156,156,188\nCV 47,94,95\nCV 112,160,161\nCV 113,162,163\nMvaa 152,148,188\nCV 114,164,165\n"
"CV 115,166,167\nMvaa 152,144,184\nWL 0\nMaaa 0,192,160\nSv 32,1,16,75,0\nCV 116,168,169\nCV 117,170,171\nMaaa 0,196,164\nD1 1,16,46,1024\nCV 118,172,173\nMaaa 4,204,164\n"
"CV 119,174,175\nCV 120,176,177\nMaaa 4,200,160\nCV 121,178,179\nCV 122,180,181\nMaaa 12,200,168\nCV 123,182,183\nCV 124,184,185\nCV 125,186,187\nMaaa 12,204,172\nCV 126,188,189\n"
"CV 127,190,191\nMaaa 8,196,172\nD1 1,16,46,2048\nCV 96,128,129\nMaaa 8,192,168\nCV 97,130,131\nCV 98,132,133\nCV 99,134,135\nMaaa 16,192,176\nSv 36,1,16,75,1024\nCV 100,136,137\n"
"Maaa 16,196,180\nCV 101,138,139\nCV 102,140,141\nMaaa 20,204,180\nCV 103,142,143\nCV 104,144,145\nCV 105,146,147\nMaaa 20,200,176\nD1 1,16,46,3072\n.W560:\ns_cbranch_execz .W564\n"
"CV 106,148,149\nMaaa 28,200,184\nCV 107,150,151\nCV 108,152,153\nMaaa 28,204,188\nCV 109,154,155\nCV 110,156,157\nCV 111,158,159\nMaaa 24,196,188\ns_add_u32 m0, s47, -49152\n"
"s_add_u32 s46, s45, 0x408000\nMaaa 24,192,184\nv_add_u32 v2, 0x10000, v1\ns_lshr_b32 s2, s59, 1\nMaaa 64,208,160\nD1 1,16,46,0\nSv 40,1,16,75,2048\nv_add_u32 v24, s2, v2\n"
"Maaa 64,212,164\nv_xor_b32 v26, 0x1000, v24\nv_lshrrev_b32 v20, 4, v0\nMaaa 68,220,164\nv_lshlrev_b32 v20, 2, v20\nv_and_b32 v0, 15, v0\nMaaa 68,216,160\nv_sub_u32 v0, v0, v20\n"
"s_lshl_b32 s2, s11, 4\nMaaa 76,216,168\nv_add_u32 v3, s2, v0\nv_subrev_u32 v0, 32, v3\ns_lshl_b32 s2, s11, 12\nMaaa 76,220,172\nD1 1,16,46,1024\nv_add_u32 v22, s2, v1\n"
"Maaa 72,212,172\nv_add_u32 v23, 0x20000, v22\nv_add_u32 v22, 0x8000, v22\nMaaa 72,208,168\nv_mov_b32 v4, 0\nv_mov_b32 v5, 0\nv_mov_b32 v6, 0\nMaaa 80,208,176\nSv 44,1,16,75,3072\n"
"v_mov_b32 v7, 0\nMaaa 80,212,180\nv_mov_b32 v8, 0\nv_mov_b32 v9, 0\nMaaa 84,220,180\nD1 1,16,46,2048\nv_mov_b32 v10, 0\nv_mov_b32 v11, 0\nMaaa 84,216,176\nv_mov_b32 v12, 0\n"
"v_mov_b32 v13, 0\nMaaa 92,216,184\nv_mov_b32 v14, 0\nv_mov_b32 v15, 0\nMaaa 92,220,188\nv_mov_b32 v16, 0\nv_mov_b32 v17, 0\nv_mov_b32 v18, 0\nMaaa 88,212,188\nv_mov_b32 v19, 0\n"
"v_mov_b32 v86, 0\nMaaa 88,208,184\nWV 19\ns_barrier\nMaaa 32,192,224\nD1 1,16,46,3072\ns_add_u32 s75, s75, s55\nSv 48,1,16,75,0\nMaaa 32,196,228\nv_mov_b32 v87, 0\n"
"v_mov_b32 v88, 0\nMaaa 36,204,228\nv_mov_b32 v89, 0\nv_mov_b32 v95, -1\nMaaa 36,200,224\nMaaa 44,200,232\nRa 128,24,0\nMaaa 44,204,236\nMaaa 40,196,236\nRa 132,24,1024\n"
"Maaa 40,192,232\nMaaa 48,192,240\nSv 52,1,16,75,1024\nRa 136,24,2048\nMaaa 48,196,244\nMaaa 52,204,244\nRa 140,24,3072\nMaaa 52,200,240\nMaaa 60,200,248\nRa 200,23,2048\n"
"Ra 144,26,0\nMaaa 60,204,252\nRa 204,23,3072\nMaaa 56,196,252\nRa 196,23,1024\nRa 148,26,1024\nMaaa 56,192,248\nRa 192,23,0\nMaaa 96,208,224\nSv 56,1,16,75,2048\nRa 152,26,2048\n"
"Maaa 96,212,228\nMaaa 100,220,228\nRa 228,22,50176\nRa 156,26,3072\nMaaa 100,216,224\nRa 224,22,49152\nMaaa 108,216,232\nMaaa 108,220,236\nMaaa 104,212,236\nRa 236,22,52224\n"
"Maaa 104,208,232\nRa 232,22,51200\nMaaa 112,208,240\nSv 60,1,16,75,3072\nMaaa 112,212,244\nMaaa 116,220,244\nRa 244,22,1024\nMaaa 116,216,240\nRa 240,22,0\nMaaa 124,216,248\n"
"Ra 216,23,18432\nMaaa 124,220,252\nRa 220,23,19456\nMaaa 120,212,252\nRa 252,22,3072\nRa 212,23,17408\nMaaa 120,208,248\nRa 248,22,2048\nRa 208,23,16384\nWL 0\nCvaa 32,128,192,4\n"
"Ra 160,24,8192\nMvaa 32,132,200\nMvaa 32,136,208\nRa 164,24,9216\nMvaa 32,140,216\nCvaa 36,140,220,8\nRa 168,24,10240\nMvaa 36,136,212\ns_add_u32 m0, s89, 32768\nMvaa 36,132,204\n"
"Ra 172,24,11264\ns_barrier\nMvaa 36,128,196\nMvaa 36,144,228\nRa 176,26,8192\nSv 96,1,16,76,0\nMvaa 36,148,236\ns_add_u32 s46, s86, s88\nMvaa 36,152,244\nRa 180,26,9216\n"
"Mvaa 36,156,252\nAR 92,0\nMvaa 32,156,248\nRa 184,26,10240\nAR 93,1\nMvaa 32,152,240\nD1 1,16,46,0\nMvaa 32,148,232\nRa 188,26,11264\nWL 4\nMvaa 32,144,224\nCV 96,92,93\n"
"Cvaa 40,160,192,12\nAR 92,2\nAR 93,3\nMvaa 40,164,200\nCV 97,92,93\nMvaa 40,168,208\nv_and_b32 v27, 0x70, v1\nbuffer_load_dword v27, v27, s[16:19], s61 offen offset:136 sc0 sc1\n"
"Mvaa 40,172,216\nAR 92,4\nCvaa 44,172,220,16\nAR 93,5\nCV 98,92,93\nMvaa 44,168,212\nD1 1,16,46,1024\nMvaa 44,164,204\nWL 0\nMvaa 44,160,196\nAR 92,6\nMvaa 44,176,228\n"
"Sv 100,1,16,76,1024\nAR 93,7\nMvaa 44,180,236\ns_add_u32 s88, s88, 32768\nEX 80,32\nMvaa 44,184,244\nCV 99,92,93\nAR 92,8\nMvaa 44,188,252\nEX 81,33\nMvaa 40,188,248\nAR 93,9\n"
"CV 100,92,93\nMvaa 40,184,240\nD1 1,16,46,2048\nMvaa 40,180,232\nAR 92,10\nAR 93,11\nMvaa 40,176,224\nAD 84,80,81\nCV 64,80,81\nCV 101,92,93\nAR 92,12\nEX 82,34\nAR 93,13\n"
"CV 102,92,93\nWV 16\nEX 83,35\nAD 84,84,82\nAR 92,14\nD1 1,16,46,3072\ns_add_u32 m0, s89, 49152\ns_barrier\nAR 93,15\nAD 84,84,83\nCV 65,82,83\nRa 128,24,32768\n"
"Sv 104,1,16,76,2048\ns_add_u32 s46, s87, s88\nEX 80,36\nRa 132,24,33792\nCV 103,92,93\nEX 81,37\nRa 136,24,34816\nAR 92,16\nAR 93,17\nD1 1,16,46,0\nCV 104,92,93\nRa 140,24,35840\n"
"AR 92,18\nAD 85,80,81\nCV 68,80,81\nRa 144,26,32768\nAR 93,19\nCV 105,92,93\nEX 82,38\nRa 148,26,33792\nAR 92,20\nEX 83,39\nRa 152,26,34816\nAD 85,85,82\nAR 93,21\n"
"D1 1,16,46,1024\nCV 106,92,93\nRa 156,26,35840\nAR 92,22\nAD 85,85,83\nCV 69,82,83\nRa 160,24,40960\nSv 108,1,16,76,3072\nAR 93,23\ns_xor_b32 s89, s89, 0x10000\nEX 80,40\n"
"Ra 164,24,41984\nCV 107,92,93\nAR 92,24\nWL 6\nEX 81,41\nRa 168,24,43008\nAD 84,84,80\nAR 93,25\nD1 1,16,46,2048\nCV 108,92,93\nRa 172,24,44032\nAR 92,26\nAR 93,27\nAD 84,84,81\n"
"CV 66,80,81\nCvaa 48,128,192,4\nRa 176,26,40960\nCV 109,92,93\nAR 92,28\nMvaa 48,132,200\nEX 82,42\nMvaa 48,136,208\nRa 180,26,41984\nAR 93,29\nMvaa 48,140,216\nWL 6\nEX 83,43\n"
"WV 9\nv_cmp_ne_u32 vcc, s15, v27\ns_nop 0\ns_cbranch_vccnz .S171\n.S172:\nCvaa 52,140,220,8\nRa 184,26,43008\nAD 84,84,82\nMvaa 52,136,212\nD1 1,16,46,3072\ns_add_u32 m0, s89, 0\n"
"CV 110,92,93\nMvaa 52,132,204\nRa 188,26,44032\nAR 92,30\nMvaa 52,128,196\nAD 84,84,83\nCV 67,82,83\nAR 93,31\nMvaa 52,144,228\nRa 128,2,49152\ns_add_u32 s76, s76, s55\n"
"Sv 112,1,16,76,0\nMvaa 52,148,236\ns_add_u32 s46, s86, s88\nEX 80,44\nMvaa 52,152,244\nRa 132,2,50176\nv_xor_b32 v24, 0x10000, v24\nCV 111,92,93\nMvaa 52,156,252\nWL 6\nEX 81,45\n"
"Mvaa 48,156,248\nRa 136,2,51200\nAD 85,85,80\nAR 92,32\nMvaa 48,152,240\nD1 1,16,46,0\nAR 93,33\nCV 112,92,93\nMvaa 48,148,232\nRa 140,2,52224\nAR 92,34\nMvaa 48,144,224\n"
"AD 85,85,81\nCV 70,80,81\nAR 93,35\nCvaa 56,160,192,12\nRa 144,2,53248\nCV 113,92,93\nAR 92,36\nMvaa 56,164,200\nEX 82,46\nMvaa 56,168,208\nRa 148,2,54272\n"
"v_xor_b32 v26, 0x10000, v26\nAR 93,37\nMvaa 56,172,216\nWL 6\nEX 83,47\nCvaa 60,172,220,16\nRa 152,2,55296\nAD 85,85,82\nCV 114,92,93\nMvaa 60,168,212\nD1 1,16,46,1024\nAR 92,38\n"
"AR 93,39\nMvaa 60,164,204\nRa 156,2,56320\nCV 115,92,93\nMvaa 60,160,196\nAD 85,85,83\nCV 71,82,83\nMvaa 60,176,228\nRa 160,2,57344\nv_max_f32 v90, v84, v85\nSv 116,1,16,76,1024\n"
"Mvaa 60,180,236\ns_add_u32 s88, s88, 32768\nEX 80,48\nMvaa 60,184,244\nRa 164,2,58368\nv_cmp_lt_f32 vcc, s63, v90\nMvaa 60,188,252\nWL 8\nEX 81,49\nMvaa 56,188,248\n"
"Ra 168,2,59392\ns_cbranch_vccnz .T153\n.T154:\nv_mov_b32 v20, v84\nv_mov_b32 v21, v85\nv_mov_b32 v22, v20\nv_mov_b32 v23, v21\nMvaa 56,184,240\nD1 1,16,46,2048\n"
"v_permlane32_swap_b32 v20, v22\nv_permlane32_swap_b32 v21, v23\nAD 20,20,22\nAD 21,21,23\nv_mov_b32 v22, v20\nv_mov_b32 v23, v21\nMvaa 56,180,232\nRa 172,2,60416\n"
"v_permlane16_swap_b32 v20, v22\nv_permlane16_swap_b32 v21, v23\nAD 20,20,22\nAD 21,21,23\nv_min_f32 v90, v20, v21\nv_cmp_gt_f32 vcc, s64, v90\ns_nop 0\ns_cbranch_vccnz .T155\n"
".T156:\nAD 86,86,84\nMvaa 56,176,224\nAD 84,80,81\nCV 72,80,81\nAR 92,40\nZvav 128,128,64\nRa 176,2,61440\nAR 93,41\nCV 116,92,93\nZvav 132,128,68\nEX 82,50\nAR 92,42\n"
"Zvav 140,132,68\nRa 180,2,62464\nAR 93,43\nCV 117,92,93\nZvav 136,132,64\nWL 8\nEX 83,51\nZvav 144,136,64\nRa 184,2,63488\nAD 84,84,82\nAR 92,44\nZvav 148,136,68\nD1 1,16,46,3072\n"
"s_add_u32 m0, s89, 16384\nAR 93,45\nZvav 156,140,68\nRa 188,2,64512\ns_barrier\nCV 118,92,93\nZvav 152,140,64\nAD 84,84,83\nCV 73,82,83\nAR 92,46\nZvav 160,144,64\nRa 128,24,0\n"
"Sv 120,1,16,76,2048\nAR 93,47\nZvav 164,144,68\ns_add_u32 s46, s87, s88\nEX 80,52\nbuffer_store_dword v25, off, s[16:19], s54 offset:12 sc0 sc1\nZvav 172,148,68\nRa 132,24,1024\n"
"v_xor_b32 v2, 0x10000, v2\nCV 119,92,93\nZvav 168,148,64\nWL 8\nEX 81,53\nZvav 176,152,64\nRa 136,24,2048\nAD 87,87,85\nZvav 180,152,68\nD1 1,16,46,0\nAR 92,48\nAR 93,49\n"
"Zvav 188,156,68\nRa 140,24,3072\nCV 120,92,93\nZvav 184,156,64\nAD 85,80,81\nCV 76,80,81\nZvav 192,160,64\nRa 144,26,0\nAR 92,50\nAR 93,51\nZvav 196,160,68\nEX 82,54\n"
"Zvav 204,164,68\nRa 148,26,1024\nCV 121,92,93\nZvav 200,164,64\nWL 8\nEX 83,55\nZvav 208,168,64\nRa 152,26,2048\nAD 85,85,82\nAR 92,52\nZvav 212,168,68\nD1 1,16,46,1024\nAR 93,53\n"
"Zvav 220,172,68\nRa 156,26,3072\nCV 122,92,93\nZvav 216,172,64\nAD 85,85,83\nCV 77,82,83\nAR 92,54\nZvav 224,176,64\nRa 160,24,8192\nSv 124,1,16,76,3072\nZvav 228,176,68\n"
"EX 80,56\nZvav 236,180,68\nRa 164,24,9216\nAR 93,55\nCV 123,92,93\nZvav 232,180,64\nWL 6\nEX 81,57\nZvav 240,184,64\nRa 168,24,10240\nAD 84,84,80\nAR 92,56\nZvav 244,184,68\n"
"D1 1,16,46,2048\nAR 93,57\nZvav 252,188,68\nRa 172,24,11264\nCV 124,92,93\nAR 92,58\nZvav 248,188,64\nAD 84,84,81\nCV 74,80,81\nCvaa 32,128,192,4\nRa 176,26,8192\nAR 93,59\n"
"CV 125,92,93\nMvaa 32,132,200\nEX 82,58\nMvaa 32,136,208\nRa 180,26,9216\nAR 92,60\nMvaa 32,140,216\nWL 6\nEX 83,59\nCvaa 36,140,220,8\nRa 184,26,10240\nAD 84,84,82\n"
"Mvaa 36,136,212\nD1 1,16,46,3072\ns_add_u32 m0, s89, 32768\nAR 93,61\nMvaa 36,132,204\nRa 188,26,11264\nCV 126,92,93\nMvaa 36,128,196\nAD 84,84,83\nCV 75,82,83\nAR 92,62\n"
"Mvaa 36,144,228\nRa 128,2,16384\nSv 96,1,16,77,0\nMvaa 36,148,236\ns_add_u32 s46, s86, s88\nEX 80,60\nMvaa 36,152,244\nRa 132,2,17408\nAR 93,63\nCV 127,92,93\nMvaa 36,156,252\n"
"WL 6\nEX 81,61\nMvaa 32,156,248\nRa 136,2,18432\nAD 85,85,80\nAR 92,64\nMvaa 32,152,240\nD1 1,16,46,0\nAR 93,65\nMvaa 32,148,232\nRa 140,2,19456\nCV 96,92,93\nAR 92,66\n"
"Mvaa 32,144,224\nAD 85,85,81\nCV 78,80,81\nCvaa 40,160,192,12\nRa 144,2,20480\nAR 93,67\nCV 97,92,93\nMvaa 40,164,200\nEX 82,62\nMvaa 40,168,208\nRa 148,2,21504\nAR 92,68\n"
"AR 93,69\nMvaa 40,172,216\nWL 6\nEX 83,63\nCvaa 44,172,220,16\nRa 152,2,22528\nAD 85,85,82\nCV 98,92,93\nMvaa 44,168,212\n.W561:\ns_cbranch_execz .W565\nD1 1,16,46,1024\nAR 92,70\n"
"Mvaa 44,164,204\nRa 156,2,23552\nAR 93,71\nCV 99,92,93\nMvaa 44,160,196\nAD 85,85,83\nCV 79,82,83\nMvaa 44,176,228\nRa 160,2,24576\nv_max_f32 v90, v84, v85\nSv 100,1,16,77,1024\n"
"Mvaa 44,180,236\ns_add_u32 s88, s88, 32768\nEX 80,32\nMvaa 44,184,244\nRa 164,2,25600\nv_cmp_lt_f32 vcc, s63, v90\nMvaa 44,188,252\nWL 8\nEX 81,33\nMvaa 40,188,248\n"
"Ra 168,2,26624\ns_cbranch_vccnz .T158\n.T159:\nAR 92,72\nMvaa 40,184,240\nD1 1,16,46,2048\nAR 93,73\nMvaa 40,180,232\nRa 172,2,27648\nAD 86,86,84\nCV 100,92,93\nMvaa 40,176,224\n"
"AD 84,80,81\nCV 64,80,81\nMvav 128,128,72\nRa 176,2,28672\nAR 92,74\nAR 93,75\nMvav 132,128,76\nEX 82,34\nMvav 140,132,76\nRa 180,2,29696\nCV 101,92,93\nAR 92,76\nMvav 136,132,72\n"
"s_waitcnt vmcnt(18) lgkmcnt(8)\nEX 83,35\nMvav 144,136,72\nRa 184,2,30720\nAD 84,84,82\nAR 93,77\nMvav 148,136,76\nD1 1,16,46,3072\ns_add_u32 m0, s89, 49152\nMvav 156,140,76\n"
"Ra 188,2,31744\ns_barrier\nCV 102,92,93\nMvav 152,140,72\nAD 84,84,83\nCV 65,82,83\nMvav 160,144,72\nRa 128,24,32768\nSv 104,1,16,77,2048\nAR 92,78\nMvav 164,144,76\n"
"s_add_u32 s46, s87, s88\nEX 80,36\nMvav 172,148,76\nRa 132,24,33792\nAR 93,79\nCV 103,92,93\nMvav 168,148,72\nWL 8\nEX 81,37\nMvav 176,152,72\nRa 136,24,34816\nAD 87,87,85\n"
"Mvav 180,152,76\nD1 1,16,46,0\nAR 92,80\nAR 93,81\nMvav 188,156,76\nRa 140,24,35840\nCV 104,92,93\nMvav 184,156,72\nAD 85,80,81\nCV 68,80,81\nMvav 192,160,72\nRa 144,26,32768\n"
"AR 92,82\nAR 93,83\nMvav 196,160,76\nEX 82,38\nMvav 204,164,76\nRa 148,26,33792\nCV 105,92,93\nMvav 200,164,72\nWL 8\nEX 83,39\nMvav 208,168,72\nRa 152,26,34816\nAD 85,85,82\n"
"AR 92,84\nMvav 212,168,76\nD1 1,16,46,1024\nAR 93,85\nMvav 220,172,76\nRa 156,26,35840\nCV 106,92,93\nMvav 216,172,72\nAD 85,85,83\nCV 69,82,83\nAR 92,86\nMvav 224,176,72\n"
"Ra 160,24,40960\nSv 108,1,16,77,3072\nMvav 228,176,76\ns_xor_b32 s89, s89, 0x10000\nEX 80,40\nMvav 236,180,76\nRa 164,24,41984\nAR 93,87\nCV 107,92,93\nMvav 232,180,72\nWL 6\n"
"EX 81,41\nMvav 240,184,72\nRa 168,24,43008\nAD 84,84,80\nAR 92,88\nMvav 244,184,76\nD1 1,16,46,2048\nAR 93,89\nCV 108,92,93\nMvav 252,188,76\nRa 172,24,44032\nAR 92,90\n"
"Mvav 248,188,72\nAD 84,84,81\nCV 66,80,81\nAR 93,91\nCvaa 48,128,192,4\nRa 176,26,40960\nCV 109,92,93\nAR 92,92\nMvaa 48,132,200\nEX 82,42\nMvaa 48,136,208\nRa 180,26,41984\n"
"AR 93,93\nCV 110,92,93\nMvaa 48,140,216\nWL 6\nEX 83,43\nCvaa 52,140,220,8\nRa 184,26,43008\nAD 84,84,82\nv_and_b32 v27, 0x70, v1\n"
"buffer_load_dword v27, v27, s[16:19], s61 offen offset:140 sc0 sc1\nMvaa 52,136,212\nD1 1,16,46,3072\ns_add_u32 m0, s89, 0\nAR 92,94\nMvaa 52,132,204\nRa 188,26,44032\nAR 93,95\n"
"Mvaa 52,128,196\nAD 84,84,83\nCV 67,82,83\nCV 111,92,93\nMvaa 52,144,228\nRa 128,2,49152\ns_add_u32 s77, s77, s55\nSv 112,1,16,77,0\nMvaa 52,148,236\ns_add_u32 s46, s86, s88\n"
"EX 80,44\nMvaa 52,152,244\nRa 132,2,50176\nv_xor_b32 v24, 0x10000, v24\nAR 92,96\nMvaa 52,156,252\nWL 6\nEX 81,45\nMvaa 48,156,248\nRa 136,2,51200\nAD 85,85,80\nAR 93,97\n"
"Mvaa 48,152,240\nD1 1,16,46,0\nCV 112,92,93\nMvaa 48,148,232\nRa 140,2,52224\nAR 92,98\nAR 93,99\nMvaa 48,144,224\nAD 85,85,81\nCV 70,80,81\nCvaa 56,160,192,12\nRa 144,2,53248\n"
"CV 113,92,93\nAR 92,100\nMvaa 56,164,200\nEX 82,46\nMvaa 56,168,208\nRa 148,2,54272\nv_xor_b32 v26, 0x10000, v26\nAR 93,101\nMvaa 56,172,216\nWL 6\nEX 83,47\nCvaa 60,172,220,16\n"
"Ra 152,2,55296\nAD 85,85,82\nCV 114,92,93\nMvaa 60,168,212\nD1 1,16,46,1024\nAR 92,102\nMvaa 60,164,204\nRa 156,2,56320\nAR 93,103\nCV 115,92,93\nMvaa 60,160,196\nAD 85,85,83\n"
"CV 71,82,83\nMvaa 60,176,228\nRa 160,2,57344\nv_max_f32 v90, v84, v85\nSv 116,1,16,77,1024\nMvaa 60,180,236\ns_add_u32 s88, s88, 32768\nEX 80,48\nMvaa 60,184,244\nRa 164,2,58368\n"
"v_cmp_lt_f32 vcc, s63, v90\nMvaa 60,188,252\nWL 8\nEX 81,49\nMvaa 56,188,248\nRa 168,2,59392\ns_cbranch_vccnz .T160\n.T161:\nAR 92,104\nMvaa 56,184,240\nD1 1,16,46,2048\n"
"AR 93,105\nCV 116,92,93\nMvaa 56,180,232\nRa 172,2,60416\nAD 86,86,84\nMvaa 56,176,224\nAD 84,80,81\nCV 72,80,81\nAR 92,106\nMvav 128,128,64\nRa 176,2,61440\nAR 93,107\n"
"CV 117,92,93\nMvav 132,128,68\nEX 82,50\nMvav 140,132,68\nRa 180,2,62464\nAR 92,108\nAR 93,109\nMvav 136,132,64\ns_waitcnt vmcnt(18) lgkmcnt(8)\nEX 83,51\nMvav 144,136,64\n"
"Ra 184,2,63488\nAD 84,84,82\nCV 118,92,93\nMvav 148,136,68\nD1 1,16,46,3072\ns_add_u32 m0, s89, 16384\nAR 92,110\nMvav 156,140,68\nRa 188,2,64512\ns_barrier\nMvav 152,140,64\n"
"AD 84,84,83\nCV 73,82,83\nAR 93,111\nMvav 160,144,64\nRa 128,24,0\nSv 120,1,16,77,2048\nCV 119,92,93\nMvav 164,144,68\ns_add_u32 s46, s87, s88\nEX 80,52\nMvav 172,148,68\n"
"Ra 132,24,1024\nv_xor_b32 v2, 0x10000, v2\nAR 92,112\nMvav 168,148,64\nWL 8\nEX 81,53\nMvav 176,152,64\nRa 136,24,2048\nAD 87,87,85\n"
"buffer_store_dword v25, off, s[16:19], s54 offset:8 sc0 sc1\nMvav 180,152,68\nD1 1,16,46,0\nAR 93,113\nCV 120,92,93\nMvav 188,156,68\nRa 140,24,3072\nAR 92,114\nMvav 184,156,64\n"
"AD 85,80,81\nCV 76,80,81\nMvav 192,160,64\nRa 144,26,0\nAR 93,115\nCV 121,92,93\nMvav 196,160,68\nEX 82,54\nMvav 204,164,68\nRa 148,26,1024\nAR 92,116\nMvav 200,164,64\nWL 8\n"
"EX 83,55\nMvav 208,168,64\nRa 152,26,2048\nAD 85,85,82\nAR 93,117\nMvav 212,168,68\nD1 1,16,46,1024\nCV 122,92,93\nMvav 220,172,68\nRa 156,26,3072\nAR 92,118\nMvav 216,172,64\n"
"AD 85,85,83\nCV 77,82,83\nAR 93,119\nMvav 224,176,64\nRa 160,24,8192\nSv 124,1,16,77,3072\nMvav 228,176,68\nEX 80,56\nMvav 236,180,68\nRa 164,24,9216\nCV 123,92,93\nAR 92,120\n"
"Mvav 232,180,64\nWL 6\nEX 81,57\nMvav 240,184,64\nRa 168,24,10240\nAD 84,84,80\nMvav 244,184,68\nD1 1,16,46,2048\nAR 93,121\nCV 124,92,93\nMvav 252,188,68\nRa 172,24,11264\n"
"AR 92,122\nMvav 248,188,64\nAD 84,84,81\nCV 74,80,81\nCvaa 32,128,192,4\nRa 176,26,8192\nAR 93,123\nCV 125,92,93\nMvaa 32,132,200\ns_add_u32 s49, s49, 131072\nEX 82,58\n"
"Mvaa 32,136,208\nRa 180,26,9216\nAR 92,124\nMvaa 32,140,216\nWL 6\nEX 83,59\nCvaa 36,140,220,8\nRa 184,26,10240\nAD 84,84,82\nAR 93,125\nMvaa 36,136,212\nD1 1,16,46,3072\n"
"s_add_u32 m0, s89, 32768\nMvaa 36,132,204\nRa 188,26,11264\nCV 126,92,93\nMvaa 36,128,196\nAD 84,84,83\nCV 75,82,83\nAR 92,126\nMvaa 36,144,228\nRa 128,2,16384\nSv 96,1,16,78,0\n"
"Mvaa 36,148,236\ns_add_u32 s46, s86, s88\nEX 80,60\nMvaa 36,152,244\nRa 132,2,17408\nAR 93,127\nCV 127,92,93\nMvaa 36,156,252\nWL 6\nEX 81,61\nMvaa 32,156,248\nRa 136,2,18432\n"
"AD 85,85,80\nMvaa 32,152,240\nD1 1,16,46,0\nMvaa 32,148,232\nRa 140,2,19456\nMvaa 32,144,224\nAD 85,85,81\nCV 78,80,81\nCvaa 40,160,192,12\nRa 144,2,20480\nMvaa 40,164,200\n"
"Sv 92,1,16,49,0\nEX 82,62\nMvaa 40,168,208\nRa 148,2,21504\nMvaa 40,172,216\nWL 6\nEX 83,63\nCvaa 44,172,220,16\nRa 152,2,22528\nAD 85,85,82\nMvaa 44,168,212\nD1 1,16,46,1024\n"
"Mvaa 44,164,204\nRa 156,2,23552\nMvaa 44,160,196\nAD 85,85,83\nCV 79,82,83\nMvaa 44,176,228\nRa 160,2,24576\nv_max_f32 v90, v84, v85\nSv 100,1,16,78,1024\nMvaa 44,180,236\n"
"s_add_u32 s88, s88, 32768\nEX 80,32\nMvaa 44,184,244\nRa 164,2,25600\nv_cmp_lt_f32 vcc, s63, v90\nMvaa 44,188,252\nWL 8\nEX 81,33\nMvaa 40,188,248\nRa 168,2,26624\n"
"s_cbranch_vccnz .T163\n.T164:\nMvaa 40,184,240\nD1 1,16,46,2048\nMvaa 40,180,232\nRa 172,2,27648\nAD 86,86,84\nMvaa 40,176,224\nAD 84,80,81\nCV 64,80,81\nMvav 128,128,72\n"
"Ra 176,2,28672\nMvav 132,128,76\nEX 82,34\nMvav 140,132,76\nRa 180,2,29696\nMvav 136,132,72\ns_waitcnt vmcnt(19) lgkmcnt(8)\nEX 83,35\nMvav 144,136,72\nRa 184,2,30720\n"
"AD 84,84,82\nMvav 148,136,76\nD1 1,16,46,3072\ns_add_u32 m0, s89, 49152\nMvav 156,140,76\nRa 188,2,31744\ns_barrier\nMvav 152,140,72\nAD 84,84,83\nCV 65,82,83\nMvav 160,144,72\n"
"Ra 128,24,32768\nSv 104,1,16,78,2048\nMvav 164,144,76\ns_add_u32 s46, s87, s88\nEX 80,36\nMvav 172,148,76\nRa 132,24,33792\nMvav 168,148,72\nWL 8\nEX 81,37\nMvav 176,152,72\n"
"Ra 136,24,34816\nAD 87,87,85\nMvav 180,152,76\nD1 1,16,46,0\nMvav 188,156,76\nRa 140,24,35840\nMvav 184,156,72\nAD 85,80,81\nCV 68,80,81\nMvav 192,160,72\nRa 144,26,32768\n"
"Mvav 196,160,76\nEX 82,38\nMvav 204,164,76\nRa 148,26,33792\nMvav 200,164,72\nWL 8\nEX 83,39\nMvav 208,168,72\nRa 152,26,34816\nAD 85,85,82\nMvav 212,168,76\nD1 1,16,46,1024\n"
"Mvav 220,172,76\nRa 156,26,35840\nMvav 216,172,72\nAD 85,85,83\nCV 69,82,83\nMvav 224,176,72\nRa 160,24,40960\nSv 108,1,16,78,3072\nMvav 228,176,76\ns_xor_b32 s89, s89, 0x10000\n"
"EX 80,40\nMvav 236,180,76\nRa 164,24,41984\nMvav 232,180,72\nWL 6\nEX 81,41\nMvav 240,184,72\nRa 168,24,43008\nAD 84,84,80\nMvav 244,184,76\nD1 1,16,46,2048\nMvav 252,188,76\n"
"Ra 172,24,44032\nMvav 248,188,72\nAD 84,84,81\nCV 66,80,81\nCvaa 48,128,192,4\nRa 176,26,40960\nMvaa 48,132,200\nEX 82,42\nMvaa 48,136,208\nRa 180,26,41984\nMvaa 48,140,216\nWL 6\n"
"EX 83,43\nv_cmp_ne_u32 vcc, s15, v27\ns_nop 0\ns_cbranch_vccnz .S174\n.S175:\nv_and_b32 v27, 0x70, v1\nbuffer_load_dword v27, v27, s[16:19], s61 offen offset:12 sc0 sc1\n"
"Cvaa 52,140,220,8\nRa 184,26,43008\nAD 84,84,82\nMvaa 52,136,212\nD1 1,16,46,3072\ns_add_u32 m0, s89, 0\nMvaa 52,132,204\nRa 188,26,44032\nMvaa 52,128,196\nAD 84,84,83\n"
"CV 67,82,83\nMvaa 52,144,228\nRa 128,2,49152\ns_add_u32 s78, s78, s55\nSv 112,1,16,78,0\nMvaa 52,148,236\ns_add_u32 s46, s86, s88\nEX 80,44\nMvaa 52,152,244\nRa 132,2,50176\n"
"v_xor_b32 v24, 0x10000, v24\nMvaa 52,156,252\nWL 6\nEX 81,45\nMvaa 48,156,248\nRa 136,2,51200\nAD 85,85,80\nMvaa 48,152,240\nD1 1,16,46,0\n.W562:\ns_cbranch_execz .W566\n"
"Mvaa 48,148,232\nRa 140,2,52224\nMvaa 48,144,224\nAD 85,85,81\nCV 70,80,81\nCvaa 56,160,192,12\nRa 144,2,53248\nMvaa 56,164,200\nSv 92,1,16,49,1024\nEX 82,46\nMvaa 56,168,208\n"
"Ra 148,2,54272\nv_xor_b32 v26, 0x10000, v26\nMvaa 56,172,216\nWL 6\nEX 83,47\nCvaa 60,172,220,16\nRa 152,2,55296\nAD 85,85,82\nMvaa 60,168,212\nD1 1,16,46,1024\nMvaa 60,164,204\n"
"Ra 156,2,56320\nMvaa 60,160,196\nAD 85,85,83\nCV 71,82,83\nMvaa 60,176,228\nRa 160,2,57344\nv_max_f32 v90, v84, v85\nSv 116,1,16,78,1024\nMvaa 60,180,236\n"
"s_add_u32 s88, s88, 32768\nEX 80,48\nMvaa 60,184,244\nRa 164,2,58368\nv_cmp_lt_f32 vcc, s63, v90\nMvaa 60,188,252\nWL 8\nEX 81,49\nMvaa 56,188,248\nRa 168,2,59392\n"
"s_cbranch_vccnz .T165\n.T166:\nMvaa 56,184,240\nD1 1,16,46,2048\nMvaa 56,180,232\nRa 172,2,60416\nAD 86,86,84\nMvaa 56,176,224\nAD 84,80,81\nCV 72,80,81\nMvav 128,128,64\n"
"Ra 176,2,61440\nMvav 132,128,68\nEX 82,50\nMvav 140,132,68\nRa 180,2,62464\nMvav 136,132,64\ns_waitcnt vmcnt(20) lgkmcnt(8)\nEX 83,51\nMvav 144,136,64\nRa 184,2,63488\n"
"AD 84,84,82\nMvav 148,136,68\nD1 1,16,46,3072\ns_add_u32 m0, s89, 16384\nMvav 156,140,68\nRa 188,2,64512\ns_barrier\nMvav 152,140,64\nAD 84,84,83\nCV 73,82,83\nMvav 160,144,64\n"
"Ra 128,24,0\nSv 120,1,16,78,2048\nMvav 164,144,68\ns_add_u32 s46, s87, s88\nEX 80,52\nMvav 172,148,68\nRa 132,24,1024\nv_xor_b32 v2, 0x10000, v2\nMvav 168,148,64\nWL 8\nEX 81,53\n"
"Mvav 176,152,64\nRa 136,24,2048\nAD 87,87,85\nMvav 180,152,68\nD1 1,16,46,0\nMvav 188,156,68\nRa 140,24,3072\nMvav 184,156,64\nAD 85,80,81\nCV 76,80,81\nMvav 192,160,64\n"
"Ra 144,26,0\nMvav 196,160,68\nEX 82,54\nMvav 204,164,68\nRa 148,26,1024\nMvav 200,164,64\nWL 8\nEX 83,55\nMvav 208,168,64\nRa 152,26,2048\nAD 85,85,82\nMvav 212,168,68\n"
"D1 1,16,46,1024\nMvav 220,172,68\nRa 156,26,3072\nMvav 216,172,64\nAD 85,85,83\nCV 77,82,83\nMvav 224,176,64\nRa 160,24,8192\nSv 124,1,16,78,3072\nMvav 228,176,68\nEX 80,56\n"
"Mvav 236,180,68\nRa 164,24,9216\nMvav 232,180,64\nWL 6\nEX 81,57\nMvav 240,184,64\nRa 168,24,10240\nAD 84,84,80\nMvav 244,184,68\nD1 1,16,46,2048\nMvav 252,188,68\n"
"Ra 172,24,11264\nMvav 248,188,64\nAD 84,84,81\nCV 74,80,81\ns_mov_b32 s99, 11\ns_mov_b32 s90, 0\n.p2align 6\n.A190:\nCvaa 32,128,192,4\nRa 176,26,8192\nMvaa 32,132,200\n"
"s_add_u32 s49, s49, 131072\nEX 82,58\nMvaa 32,136,208\nRa 180,26,9216\nMvaa 32,140,216\nWL 6\nEX 83,59\nCvaa 36,140,220,8\nRa 184,26,10240\nAD 84,84,82\nMvaa 36,136,212\n"
"D1 1,16,46,3072\ns_add_u32 m0, s89, 32768\nMvaa 36,132,204\nRa 188,26,11264\nMvaa 36,128,196\nAD 84,84,83\nCV 75,82,83\nMvaa 36,144,228\nRa 128,2,16384\nMvaa 36,148,236\n"
"s_add_u32 s46, s86, s88\nEX 80,60\nMvaa 36,152,244\nRa 132,2,17408\nMvaa 36,156,252\nWL 6\nEX 81,61\nMvaa 32,156,248\nRa 136,2,18432\nAD 85,85,80\nMvaa 32,152,240\nD1 1,16,46,0\n"
"Mvaa 32,148,232\nRa 140,2,19456\nMvaa 32,144,224\nAD 85,85,81\nCV 78,80,81\nCvaa 40,160,192,12\nRa 144,2,20480\nMvaa 40,164,200\nSv 92,1,16,49,0\nEX 82,62\nMvaa 40,168,208\n"
"Ra 148,2,21504\nMvaa 40,172,216\nWL 6\nEX 83,63\nCvaa 44,172,220,16\nRa 152,2,22528\nAD 85,85,82\nMvaa 44,168,212\nD1 1,16,46,1024\nMvaa 44,164,204\nRa 156,2,23552\n"
"Mvaa 44,160,196\nAD 85,85,83\nCV 79,82,83\nMvaa 44,176,228\nRa 160,2,24576\nv_max_f32 v90, v84, v85\nMvaa 44,180,236\ns_add_u32 s88, s88, 32768\nEX 80,32\nMvaa 44,184,244\n"
"Ra 164,2,25600\nv_cmp_lt_f32 vcc, s63, v90\nMvaa 44,188,252\nWL 8\nEX 81,33\nMvaa 40,188,248\nRa 168,2,26624\ns_cbranch_vccnz .T194\n.T195:\nMvaa 40,184,240\nD1 1,16,46,2048\n"
"Mvaa 40,180,232\nRa 172,2,27648\nAD 86,86,84\nMvaa 40,176,224\nAD 84,80,81\nCV 64,80,81\nMvav 128,128,72\nRa 176,2,28672\nMvav 132,128,76\nEX 82,34\nMvav 140,132,76\n"
"Ra 180,2,29696\nMvav 136,132,72\ns_waitcnt vmcnt(13) lgkmcnt(8)\nEX 83,35\nMvav 144,136,72\nRa 184,2,30720\nAD 84,84,82\nMvav 148,136,76\nD1 1,16,46,3072\n"
"s_add_u32 m0, s89, 49152\nMvav 156,140,76\nRa 188,2,31744\ns_barrier\nMvav 152,140,72\nAD 84,84,83\nCV 65,82,83\nMvav 160,144,72\nRa 128,24,32768\nMvav 164,144,76\n"
"s_add_u32 s46, s87, s88\nEX 80,36\nMvav 172,148,76\nRa 132,24,33792\nMvav 168,148,72\nWL 8\nEX 81,37\nMvav 176,152,72\nRa 136,24,34816\nAD 87,87,85\nMvav 180,152,76\nD1 1,16,46,0\n"
"Mvav 188,156,76\nRa 140,24,35840\nMvav 184,156,72\nAD 85,80,81\nCV 68,80,81\nMvav 192,160,72\nRa 144,26,32768\nMvav 196,160,76\nEX 82,38\nMvav 204,164,76\nRa 148,26,33792\n"
"Mvav 200,164,72\nWL 8\nEX 83,39\nMvav 208,168,72\nRa 152,26,34816\nAD 85,85,82\nMvav 212,168,76\nD1 1,16,46,1024\nMvav 220,172,76\nRa 156,26,35840\nMvav 216,172,72\nAD 85,85,83\n"
"CV 69,82,83\nMvav 224,176,72\nRa 160,24,40960\nMvav 228,176,76\ns_xor_b32 s89, s89, 0x10000\nEX 80,40\nMvav 236,180,76\nRa 164,24,41984\nMvav 232,180,72\nWL 6\nEX 81,41\n"
"Mvav 240,184,72\nRa 168,24,43008\nAD 84,84,80\nMvav 244,184,76\nD1 1,16,46,2048\nMvav 252,188,76\nRa 172,24,44032\nMvav 248,188,72\nAD 84,84,81\nCV 66,80,81\nCvaa 48,128,192,4\n"
"Ra 176,26,40960\nMvaa 48,132,200\nEX 82,42\nMvaa 48,136,208\nRa 180,26,41984\nMvaa 48,140,216\nWL 6\nEX 83,43\nCvaa 52,140,220,8\nRa 184,26,43008\nAD 84,84,82\nMvaa 52,136,212\n"
"D1 1,16,46,3072\ns_add_u32 m0, s89, 0\nMvaa 52,132,204\nRa 188,26,44032\nMvaa 52,128,196\nAD 84,84,83\nCV 67,82,83\nMvaa 52,144,228\nRa 128,2,49152\nMvaa 52,148,236\n"
"s_add_u32 s46, s86, s88\nEX 80,44\nMvaa 52,152,244\nRa 132,2,50176\nv_xor_b32 v24, 0x10000, v24\nMvaa 52,156,252\nWL 6\nEX 81,45\nMvaa 48,156,248\nRa 136,2,51200\nAD 85,85,80\n"
"Mvaa 48,152,240\nD1 1,16,46,0\nMvaa 48,148,232\nRa 140,2,52224\nMvaa 48,144,224\nAD 85,85,81\nCV 70,80,81\nCvaa 56,160,192,12\nRa 144,2,53248\nMvaa 56,164,200\nSv 92,1,16,49,1024\n"
"EX 82,46\nMvaa 56,168,208\nRa 148,2,54272\nv_xor_b32 v26, 0x10000, v26\nMvaa 56,172,216\nWL 6\nEX 83,47\nCvaa 60,172,220,16\nRa 152,2,55296\nAD 85,85,82\nMvaa 60,168,212\n"
"D1 1,16,46,1024\nMvaa 60,164,204\nRa 156,2,56320\nMvaa 60,160,196\nAD 85,85,83\nCV 71,82,83\nMvaa 60,176,228\nRa 160,2,57344\nv_max_f32 v90, v84, v85\nMvaa 60,180,236\n"
"s_add_u32 s88, s88, 32768\nEX 80,48\nMvaa 60,184,244\nRa 164,2,58368\nv_cmp_lt_f32 vcc, s63, v90\nMvaa 60,188,252\nWL 8\nEX 81,49\nMvaa 56,188,248\nRa 168,2,59392\n"
"s_cbranch_vccnz .T196\n.T197:\nMvaa 56,184,240\nD1 1,16,46,2048\nMvaa 56,180,232\nRa 172,2,60416\nAD 86,86,84\nMvaa 56,176,224\nAD 84,80,81\nCV 72,80,81\nMvav 128,128,64\n"
"Ra 176,2,61440\nMvav 132,128,68\nEX 82,50\nMvav 140,132,68\nRa 180,2,62464\nMvav 136,132,64\ns_waitcnt vmcnt(13) lgkmcnt(8)\nEX 83,51\nMvav 144,136,64\nRa 184,2,63488\n"
"AD 84,84,82\nMvav 148,136,68\nD1 1,16,46,3072\ns_add_u32 m0, s89, 16384\nMvav 156,140,68\nRa 188,2,64512\ns_barrier\nMvav 152,140,64\nAD 84,84,83\nCV 73,82,83\nMvav 160,144,64\n"
"Ra 128,24,0\nMvav 164,144,68\ns_add_u32 s46, s87, s88\nEX 80,52\nMvav 172,148,68\nRa 132,24,1024\nv_xor_b32 v2, 0x10000, v2\nMvav 168,148,64\nWL 8\nEX 81,53\nMvav 176,152,64\n"
"Ra 136,24,2048\nAD 87,87,85\nMvav 180,152,68\nD1 1,16,46,0\nMvav 188,156,68\nRa 140,24,3072\nMvav 184,156,64\nAD 85,80,81\nCV 76,80,81\nMvav 192,160,64\nRa 144,26,0\n"
"Mvav 196,160,68\nEX 82,54\nMvav 204,164,68\nRa 148,26,1024\nMvav 200,164,64\nWL 8\nEX 83,55\nMvav 208,168,64\nRa 152,26,2048\nAD 85,85,82\nMvav 212,168,68\nD1 1,16,46,1024\n"
"Mvav 220,172,68\nRa 156,26,3072\nMvav 216,172,64\nAD 85,85,83\nCV 77,82,83\nMvav 224,176,64\nRa 160,24,8192\nMvav 228,176,68\ns_sub_u32 s90, s90, 1\nEX 80,56\nMvav 236,180,68\n"
"Ra 164,24,9216\ns_cbranch_scc1 .E192\n.E193:\nMvav 232,180,64\nWL 6\nEX 81,57\nMvav 240,184,64\nRa 168,24,10240\nAD 84,84,80\nMvav 244,184,68\nD1 1,16,46,2048\n"
"s_sub_u32 s99, s99, 1\nMvav 252,188,68\nRa 172,24,11264\nMvav 248,188,64\nAD 84,84,81\nCV 74,80,81\ns_cbranch_scc0 .A190\ns_branch .A198\n.E192:\ns_swappc_b64 s[80:81], s[8:9]\n"
"s_branch .E193\n.HD143:\nbuffer_store_dword v25, off, s[16:19], s54 offset:4 sc0 sc1\nv_cmp_ne_u32 vcc, s15, v27\ns_nop 0\ns_cbranch_vccnz .S177\n.S178:\nv_and_b32 v27, 0x70, v1\n"
"buffer_load_dword v27, v27, s[16:19], s61 offen offset:8 sc0 sc1\ns_mov_b32 s90, 1\ns_swappc_b64 s[8:9], s[80:81]\nv_cmp_ne_u32 vcc, s15, v27\ns_nop 0\ns_cbranch_vccnz .S180\n"
".S181:\nv_and_b32 v27, 0xf0, v1\nbuffer_load_dword v27, v27, s[16:19], s61 offen offset:4 sc0 sc1\ns_mov_b32 s90, 1\ns_swappc_b64 s[8:9], s[80:81]\nv_cmp_ne_u32 vcc, s15, v27\n"
"s_nop 0\ns_cbranch_vccnz .S183\n.S184:\ns_mov_b32 s90, s91\ns_swappc_b64 s[8:9], s[80:81]\ns_branch .FF186\n.A198:\nCvaa 32,128,192,4\nRa 176,26,8192\nMvaa 32,132,200\n"
"s_add_u32 s49, s49, 131072\nEX 82,58\nMvaa 32,136,208\nRa 180,26,9216\nMvaa 32,140,216\nWL 6\nEX 83,59\nCvaa 36,140,220,8\nRa 184,26,10240\nAD 84,84,82\nMvaa 36,136,212\n"
"D1 1,16,46,3072\ns_add_u32 m0, s89, 32768\nMvaa 36,132,204\nRa 188,26,11264\nMvaa 36,128,196\nAD 84,84,83\nCV 75,82,83\ns_mov_b32 s94, s96\ns_add_u32 s95, s96, 65536\n"
"v_and_b32 v108, 0xf0, v1\nMvaa 36,144,228\nRa 128,2,16384\nMvaa 36,148,236\ns_add_u32 s46, s86, s88\nEX 80,60\nMvaa 36,152,244\nRa 132,2,17408\nMvaa 36,156,252\nWL 6\nEX 81,61\n"
"Mvaa 32,156,248\nRa 136,2,18432\nAD 85,85,80\nMvaa 32,152,240\nD1 1,16,46,0\nMvaa 32,148,232\nRa 140,2,19456\nMvaa 32,144,224\nAD 85,85,81\nCV 78,80,81\nCvaa 40,160,192,12\n"
"Ra 144,2,20480\nMvaa 40,164,200\nSv 92,1,16,49,0\nEX 82,62\nMvaa 40,168,208\nRa 148,2,21504\nMvaa 40,172,216\nWL 6\nEX 83,63\nCvaa 44,172,220,16\nRa 152,2,22528\nAD 85,85,82\n"
"Mvaa 44,168,212\nD1 1,16,46,1024\nMvaa 44,164,204\nRa 156,2,23552\nMvaa 44,160,196\nAD 85,85,83\nCV 79,82,83\nMvaa 44,176,228\nRa 160,2,24576\nv_max_f32 v90, v84, v85\n"
"Mvaa 44,180,236\ns_add_u32 s88, s88, 32768\nEX 80,32\nMvaa 44,184,244\nRa 164,2,25600\nv_cmp_lt_f32 vcc, s63, v90\nMvaa 44,188,252\nWL 8\nEX 81,33\nMvaa 40,188,248\n"
"Ra 168,2,26624\ns_cbranch_vccnz .T201\n.T202:\nMvaa 40,184,240\nD1 1,16,46,2048\nMvaa 40,180,232\nRa 172,2,27648\nAD 86,86,84\nMvaa 40,176,224\nAD 84,80,81\nCV 64,80,81\n"
"Mvav 128,128,72\nRa 176,2,28672\nv_lshrrev_b32 v92, 8, v1\nMvav 132,128,76\nEX 82,34\nMvav 140,132,76\nRa 180,2,29696\nMvav 136,132,72\ns_waitcnt vmcnt(13) lgkmcnt(8)\nEX 83,35\n"
"Mvav 144,136,72\nRa 184,2,30720\nAD 84,84,82\nMvav 148,136,76\nD1 1,16,46,3072\ns_add_u32 m0, s89, 49152\nMvav 156,140,76\nRa 188,2,31744\ns_barrier\nMvav 152,140,72\nAD 84,84,83\n"
"CV 65,82,83\n.W563:\ns_cbranch_execz .WB151\nMvav 160,144,72\nRa 128,24,32768\nv_bfe_u32 v93, v1, 6, 2\nMvav 164,144,76\ns_add_u32 s46, s87, s88\nEX 80,36\nMvav 172,148,76\n"
"Ra 132,24,33792\nv_cmp_eq_u32 vcc, v92, v93\nv_and_b32 v92, 16, v1\nv_mov_b32 v93, 0x3f80\nv_cndmask_b32 v93, 0, v93, vcc\nMvav 168,148,72\nWL 8\nEX 81,37\nMvav 176,152,72\n"
"Ra 136,24,34816\nAD 87,87,85\nMvav 180,152,76\nD1 1,16,46,0\nMvav 188,156,76\nRa 140,24,35840\nv_lshlrev_b32 v93, v92, v93\nMvav 184,156,72\nAD 85,80,81\nCV 68,80,81\n"
"Mvav 192,160,72\nRa 144,26,32768\nv_and_b32 v92, 32, v1\nMvav 196,160,76\nEX 82,38\nMvav 204,164,76\nRa 148,26,33792\nv_cmp_eq_u32 vcc, 0, v92\nv_mov_b32 v112, 0\n"
"v_mov_b32 v113, 0\nv_cndmask_b32 v114, 0, v93, vcc\nMvav 200,164,72\nWL 8\nEX 83,39\nMvav 208,168,72\nRa 152,26,34816\nAD 85,85,82\nMvav 212,168,76\nD1 1,16,46,1024\n"
"Mvav 220,172,76\nRa 156,26,35840\nv_sub_u32 v115, v93, v114\nMvav 216,172,72\nAD 85,85,83\nCV 69,82,83\nMvav 224,176,72\nRa 160,24,40960\nv_mov_b32 v116, 0\nMvav 228,176,76\n"
"s_xor_b32 s89, s89, 0x10000\nEX 80,40\nMvav 236,180,76\nRa 164,24,41984\nMvav 232,180,72\nWL 6\nEX 81,41\nMvav 240,184,72\nRa 168,24,43008\nAD 84,84,80\nMvav 244,184,76\n"
"D1 1,16,46,2048\nMvav 252,188,76\nRa 172,24,44032\nv_mov_b32 v117, 0\nMvav 248,188,72\nAD 84,84,81\nCV 66,80,81\nCvaa 48,128,192,4\nRa 176,26,40960\nMvaa 48,132,200\n"
"s_mov_b32 s86, s94\nEX 82,42\nMvaa 48,136,208\nRa 180,26,41984\nMvaa 48,140,216\nWL 6\nEX 83,43\nCvaa 52,140,220,8\nRa 184,26,43008\nAD 84,84,82\nMvaa 52,136,212\nD1 1,16,46,3072\n"
"s_add_u32 m0, s89, 0\nMvaa 52,132,204\nRa 188,26,44032\nMvaa 52,128,196\nAD 84,84,83\nCV 67,82,83\nMvaa 52,144,228\nRa 128,2,49152\nMvaa 52,148,236\ns_add_u32 s46, s86, s88\n"
"EX 80,44\nMvaa 52,152,244\nRa 132,2,50176\nv_xor_b32 v24, 0x10000, v24\nMvaa 52,156,252\nWL 6\nEX 81,45\nMvaa 48,156,248\nRa 136,2,51200\nAD 85,85,80\nMvaa 48,152,240\n"
"D1 1,16,46,0\nMvaa 48,148,232\nRa 140,2,52224\nMvaa 48,144,224\nAD 85,85,81\nCV 70,80,81\nCvaa 56,160,192,12\nRa 144,2,53248\nMvaa 56,164,200\nSv 92,1,16,49,1024\nEX 82,46\n"
"Mvaa 56,168,208\nRa 148,2,54272\nv_xor_b32 v26, 0x10000, v26\nMvaa 56,172,216\nWL 6\nEX 83,47\nCvaa 60,172,220,16\nRa 152,2,55296\nAD 85,85,82\nMvaa 60,168,212\nD1 1,16,46,1024\n"
"Mvaa 60,164,204\nRa 156,2,56320\nMvaa 60,160,196\nAD 85,85,83\nCV 71,82,83\nMvaa 60,176,228\nRa 160,2,57344\nv_max_f32 v90, v84, v85\nMvaa 60,180,236\ns_add_u32 s88, s88, 32768\n"
"EX 80,48\nMvaa 60,184,244\nRa 164,2,58368\nv_cmp_lt_f32 vcc, s63, v90\nMvaa 60,188,252\nWL 8\nEX 81,49\nMvaa 56,188,248\nRa 168,2,59392\ns_cbranch_vccnz .T203\n.T204:\n"
"Mvaa 56,184,240\nD1 1,16,46,2048\nMvaa 56,180,232\nRa 172,2,60416\nAD 86,86,84\nMvaa 56,176,224\nAD 84,80,81\nCV 72,80,81\nMvav 128,128,64\nRa 176,2,61440\nMvav 132,128,68\n"
"EX 82,50\nMvav 140,132,68\nRa 180,2,62464\nMvav 136,132,64\ns_waitcnt vmcnt(13) lgkmcnt(8)\nEX 83,51\nMvav 144,136,64\nRa 184,2,63488\nAD 84,84,82\nMvav 148,136,68\n"
"D1 1,16,46,3072\ns_add_u32 m0, s89, 16384\nMvav 156,140,68\nRa 188,2,64512\ns_barrier\nMvav 152,140,64\nAD 84,84,83\nCV 73,82,83\nMvav 160,144,64\nRa 128,24,0\nMvav 164,144,68\n"
"s_add_u32 s46, s87, s88\nEX 80,52\nMvav 172,148,68\nRa 132,24,1024\nv_xor_b32 v2, 0x10000, v2\nMvav 168,148,64\nWL 8\nEX 81,53\nMvav 176,152,64\nRa 136,24,2048\nAD 87,87,85\n"
"Mvav 180,152,68\nD1 1,16,46,0\nMvav 188,156,68\nRa 140,24,3072\nMvav 184,156,64\nAD 85,80,81\nCV 76,80,81\nMvav 192,160,64\nRa 144,26,0\nMvav 196,160,68\ns_cmp_lg_u32 s92, 0\n"
"EX 82,54\nMvav 204,164,68\nRa 148,26,1024\ns_cbranch_scc0 .E199\nv_cmpx_gt_i32 vcc, 19, v3\nv_mov_b32 v15, s97\nv_cmpx_gt_i32 vcc, 18, v3\nv_mov_b32 v14, s97\n"
"v_cmpx_gt_i32 vcc, 17, v3\nv_mov_b32 v13, s97\nv_cmpx_gt_i32 vcc, 16, v3\nv_mov_b32 v12, s97\nv_cmpx_gt_i32 vcc, 3, v3\nv_mov_b32 v7, s97\nv_cmpx_gt_i32 vcc, 2, v3\n"
"v_mov_b32 v6, s97\nv_cmpx_gt_i32 vcc, 1, v3\nv_mov_b32 v5, s97\nv_cmpx_gt_i32 vcc, 0, v3\nv_mov_b32 v4, s97\ns_mov_b64 exec, -1\ns_nop 2\n.E199:\ns_nop 0\nMvav 200,164,64\nWL 8\n"
"EX 83,55\nMvav 208,168,64\nRa 152,26,2048\nAD 85,85,82\nMvav 212,168,68\nD1 1,16,46,1024\nMvav 220,172,68\nRa 156,26,3072\nMvav 216,172,64\nAD 85,85,83\nCV 77,82,83\n"
"Mvav 224,176,64\nRa 160,24,8192\nMvav 228,176,68\nEX 80,56\nMvav 236,180,68\nRa 164,24,9216\nMvav 232,180,64\nWL 6\nEX 81,57\nMvav 240,184,64\nRa 168,24,10240\nAD 84,84,80\n"
"Mvav 244,184,68\nD1 1,16,46,2048\nMvav 252,188,68\nRa 172,24,11264\nMvav 248,188,64\nAD 84,84,81\nCV 74,80,81\nCvaa 32,128,192,4\nRa 176,26,8192\nMvaa 32,132,200\nEX 82,58\n"
"Mvaa 32,136,208\nRa 180,26,9216\nMvaa 32,140,216\nWL 6\nEX 83,59\nCvaa 36,140,220,8\nRa 184,26,10240\nAD 84,84,82\nMvaa 36,136,212\nD1 1,16,46,3072\ns_add_u32 m0, s89, 32768\n"
"Mvaa 36,132,204\nRa 188,26,11264\nMvaa 36,128,196\nAD 84,84,83\nCV 75,82,83\nMvaa 36,144,228\nRa 128,2,16384\nMvaa 36,148,236\ns_add_u32 s46, s86, s88\nEX 80,60\nMvaa 36,152,244\n"
"Ra 132,2,17408\nMvaa 36,156,252\nWL 6\nEX 81,61\nMvaa 32,156,248\nRa 136,2,18432\nAD 85,85,80\nMvaa 32,152,240\nD1 1,16,46,0\nMvaa 32,148,232\nRa 140,2,19456\nMvaa 32,144,224\n"
"AD 85,85,81\nCV 78,80,81\nCvaa 40,160,192,12\nRa 144,2,20480\nMvaa 40,164,200\ns_mov_b32 s87, s95\nEX 82,62\nMvaa 40,168,208\nRa 148,2,21504\nMvaa 40,172,216\nWL 6\nEX 83,63\n"
"Cvaa 44,172,220,16\nRa 152,2,22528\nAD 85,85,82\nMvaa 44,168,212\nD1 1,16,46,1024\nMvaa 44,164,204\nRa 156,2,23552\nMvaa 44,160,196\nAD 85,85,83\nCV 79,82,83\nMvaa 44,176,228\n"
"Ra 160,2,24576\nv_max_f32 v90, v84, v85\nMvaa 44,180,236\ns_add_u32 s88, s88, 32768\nEX 80,32\nMvaa 44,184,244\nRa 164,2,25600\nv_cmp_lt_f32 vcc, s63, v90\nMvaa 44,188,252\nWL 8\n"
"EX 81,33\nMvaa 40,188,248\nRa 168,2,26624\ns_cbranch_vccnz .T207\n.T208:\nMvaa 40,184,240\nD1 1,16,46,2048\nMvaa 40,180,232\nRa 172,2,27648\nAD 86,86,84\nMvaa 40,176,224\n"
"AD 84,80,81\nCV 64,80,81\nMvav 128,128,72\nRa 176,2,28672\nMvav 132,128,76\nEX 82,34\nMvav 140,132,76\nRa 180,2,29696\nMvav 136,132,72\ns_waitcnt vmcnt(12) lgkmcnt(8)\nEX 83,35\n"
"Mvav 144,136,72\nRa 184,2,30720\nAD 84,84,82\nMvav 148,136,76\nD1 1,16,46,3072\ns_add_u32 m0, s89, 49152\nMvav 156,140,76\nRa 188,2,31744\ns_barrier\nMvav 152,140,72\nAD 84,84,83\n"
"CV 65,82,83\nMvav 160,144,72\nRa 128,24,32768\nMvav 164,144,76\ns_add_u32 s46, s87, s88\nEX 80,36\nMvav 172,148,76\nRa 132,24,33792\nMvav 168,148,72\nWL 8\nEX 81,37\n"
"Mvav 176,152,72\nRa 136,24,34816\nAD 87,87,85\nMvav 180,152,76\nD1 1,16,46,0\nMvav 188,156,76\nRa 140,24,35840\nMvav 184,156,72\nAD 85,80,81\nCV 68,80,81\nMvav 192,160,72\n"
"Ra 144,26,32768\nbuffer_load_dwordx4 v[100:103], v108, s[16:19], s65 offen offset:0 sc0 sc1\nbuffer_load_dwordx4 v[104:107], v108, s[16:19], s65 offen offset:256 sc0 sc1\n"
"Mvav 196,160,76\ns_cmp_eq_u32 s13, 7\nEX 82,38\nMvav 204,164,76\nRa 148,26,33792\ns_cbranch_scc0 .E205\nv_cmpx_gt_i32 vcc, 19, v0\nv_mov_b32 v15, s97\nv_cmpx_gt_i32 vcc, 18, v0\n"
"v_mov_b32 v14, s97\nv_cmpx_gt_i32 vcc, 17, v0\nv_mov_b32 v13, s97\nv_cmpx_gt_i32 vcc, 16, v0\nv_mov_b32 v12, s97\nv_cmpx_gt_i32 vcc, 3, v0\nv_mov_b32 v7, s97\n"
"v_cmpx_gt_i32 vcc, 2, v0\nv_mov_b32 v6, s97\nv_cmpx_gt_i32 vcc, 1, v0\nv_mov_b32 v5, s97\nv_cmpx_gt_i32 vcc, 0, v0\nv_mov_b32 v4, s97\ns_mov_b64 exec, -1\ns_nop 2\n.E205:\n"
"s_nop 0\nMvav 200,164,72\nWL 8\nEX 83,39\nMvav 208,168,72\nRa 152,26,34816\nAD 85,85,82\nMvav 212,168,76\nD1 1,16,46,1024\nMvav 220,172,76\nRa 156,26,35840\nMvav 216,172,72\n"
"AD 85,85,83\nCV 69,82,83\nMvav 224,176,72\nRa 160,24,40960\nMvav 228,176,76\ns_xor_b32 s89, s89, 0x10000\nEX 80,40\nMvav 236,180,76\nRa 164,24,41984\nMvav 232,180,72\nWL 6\n"
"EX 81,41\nMvav 240,184,72\nRa 168,24,43008\nAD 84,84,80\nMvav 244,184,76\nD1 1,16,46,2048\nMvav 252,188,76\nRa 172,24,44032\nMvav 248,188,72\nAD 84,84,81\nCV 66,80,81\n"
"Cvaa 48,128,192,4\nRa 176,26,40960\nMvaa 48,132,200\nEX 82,42\nMvaa 48,136,208\nRa 180,26,41984\nMvaa 48,140,216\nWL 6\nEX 83,43\nCvaa 52,140,220,8\nRa 184,26,43008\nAD 84,84,82\n"
"Mvaa 52,136,212\nD1 1,16,46,3072\ns_add_u32 m0, s89, 0\nMvaa 52,132,204\nRa 188,26,44032\nMvaa 52,128,196\nAD 84,84,83\nCV 67,82,83\nMvaa 52,144,228\nRa 128,2,49152\nWV 3\n"
"v_xor_b32 v109, s15, v102\nv_xor_b32 v110, s15, v106\nv_or_b32 v109, v109, v110\nv_cmp_ne_u32 vcc, 0, v109\ns_nop 3\ns_cmp_lg_u64 vcc, 0\ns_cselect_b32 s67, 1, 0\n"
"s_cselect_b32 s6, 0x40000000, s66\nMvaa 52,148,236\ns_add_u32 s46, s86, s88\nEX 80,44\ns_add_u32 s5, s6, 0\nMvaa 52,152,244\nRa 132,2,50176\nv_xor_b32 v24, 0x10000, v24\n"
"Mvaa 52,156,252\nWL 6\nEX 81,45\nbuffer_load_dwordx4 a[0:3], v1, s[16:19], s5 offen offset:0 sc1\nMvaa 48,156,248\nRa 136,2,51200\nAD 85,85,80\nMvaa 48,152,240\nD1 1,16,46,0\n"
"Mvaa 48,148,232\nRa 140,2,52224\nbuffer_load_dwordx4 a[4:7], v1, s[16:19], s5 offen offset:1024 sc1\nMvaa 48,144,224\nAD 85,85,81\nCV 70,80,81\nCvaa 56,160,192,12\nRa 144,2,53248\n"
"Mvaa 56,164,200\nEX 82,46\nbuffer_load_dwordx4 a[8:11], v1, s[16:19], s5 offen offset:2048 sc1\nMvaa 56,168,208\nRa 148,2,54272\nv_xor_b32 v26, 0x10000, v26\nMvaa 56,172,216\n"
"WL 6\nEX 83,47\nCvaa 60,172,220,16\nRa 152,2,55296\nAD 85,85,82\nbuffer_load_dwordx4 a[12:15], v1, s[16:19], s5 offen offset:3072 sc1\nMvaa 60,168,212\nD1 1,16,46,1024\n"
"s_add_u32 s5, s6, 4096\nMvaa 60,164,204\nRa 156,2,56320\nMvaa 60,160,196\nAD 85,85,83\nCV 71,82,83\nbuffer_load_dwordx4 a[16:19], v1, s[16:19], s5 offen offset:0 sc1\n"
"Mvaa 60,176,228\nRa 160,2,57344\nv_max_f32 v90, v84, v85\nMvaa 60,180,236\ns_add_u32 s88, s88, 32768\nEX 80,48\nMvaa 60,184,244\nRa 164,2,58368\nv_cmp_lt_f32 vcc, s63, v90\n"
"Mvaa 60,188,252\nWL 8\nEX 81,49\nMvaa 56,188,248\nRa 168,2,59392\ns_cbranch_vccnz .T209\n.T210:\nbuffer_load_dwordx4 a[20:23], v1, s[16:19], s5 offen offset:1024 sc1\n"
"Mvaa 56,184,240\nD1 1,16,46,2048\nMvaa 56,180,232\nRa 172,2,60416\nAD 86,86,84\nMvaa 56,176,224\nAD 84,80,81\nCV 72,80,81\n"
"buffer_load_dwordx4 a[24:27], v1, s[16:19], s5 offen offset:2048 sc1\nMvav 128,128,64\nRa 176,2,61440\nMvav 132,128,68\nEX 82,50\nMvav 140,132,68\nRa 180,2,62464\n"
"buffer_load_dwordx4 a[28:31], v1, s[16:19], s5 offen offset:3072 sc1\nMvav 136,132,64\nWL 8\nEX 83,51\ns_add_u32 s5, s6, 16384\nMvav 144,136,64\nRa 184,2,63488\nAD 84,84,82\n"
"Mvav 148,136,68\nD1 1,16,46,3072\ns_add_u32 m0, s89, 16384\nbuffer_load_dwordx4 a[32:35], v1, s[16:19], s5 offen offset:0 sc1\nMvav 156,140,68\nRa 188,2,64512\ns_barrier\n"
"Mvav 152,140,64\nAD 84,84,83\nCV 73,82,83\nMvav 160,144,64\nRa 128,24,0\nbuffer_load_dwordx4 a[36:39], v1, s[16:19], s5 offen offset:1024 sc1\nMvav 164,144,68\n"
"s_add_u32 s46, s87, s88\nEX 80,52\nMvav 172,148,68\nRa 132,24,1024\nv_xor_b32 v2, 0x10000, v2\nMvav 168,148,64\nWL 8\nEX 81,53\n"
"buffer_load_dwordx4 a[40:43], v1, s[16:19], s5 offen offset:2048 sc1\nMvav 176,152,64\nRa 136,24,2048\nAD 87,87,85\nMvav 180,152,68\nD1 1,16,46,0\nMvav 188,156,68\nRa 140,24,3072\n"
"buffer_load_dwordx4 a[44:47], v1, s[16:19], s5 offen offset:3072 sc1\nMvav 184,156,64\nAD 85,80,81\nCV 76,80,81\ns_add_u32 s5, s6, 20480\nMvav 192,160,64\nRa 144,26,0\n"
"Mvav 196,160,68\nEX 82,54\nbuffer_load_dwordx4 a[48:51], v1, s[16:19], s5 offen offset:0 sc1\nMvav 204,164,68\nRa 148,26,1024\nMvav 200,164,64\nWL 8\nEX 83,55\nMvav 208,168,64\n"
"Ra 152,26,2048\nAD 85,85,82\nbuffer_load_dwordx4 a[52:55], v1, s[16:19], s5 offen offset:1024 sc1\nMvav 212,168,68\nD1 1,16,46,1024\nMvav 220,172,68\nRa 156,26,3072\n"
"Mvav 216,172,64\nAD 85,85,83\nCV 77,82,83\nbuffer_load_dwordx4 a[56:59], v1, s[16:19], s5 offen offset:2048 sc1\nMvav 224,176,64\n.W564:\ns_cbranch_execz .WB151\nRa 160,24,8192\n"
"Mvav 228,176,68\nEX 80,56\nMvav 236,180,68\nRa 164,24,9216\nbuffer_load_dwordx4 a[60:63], v1, s[16:19], s5 offen offset:3072 sc1\nMvav 232,180,64\nWL 10\nEX 81,57\n"
"Mvav 240,184,64\nRa 168,24,10240\nAD 84,84,80\nMvav 244,184,68\nD1 1,16,46,2048\nMvav 252,188,68\nRa 172,24,11264\nMvav 248,188,64\nAD 84,84,81\nCV 74,80,81\nWV 20\n"
"s_cmp_lg_u32 s67, 0\ns_cbranch_scc0 .E218\nv_xor_b32 v109, s15, v102\nv_xor_b32 v110, s15, v106\nv_or_b32 v109, v109, v110\nv_cmp_ne_u32 vcc, 0, v109\ns_nop 0\n"
"s_cbranch_vccnz .S215\n.S216:\ns_add_u32 s5, s66, 0\nbuffer_load_dwordx4 a[0:3], v1, s[16:19], s5 offen offset:0 sc1\n"
"buffer_load_dwordx4 a[4:7], v1, s[16:19], s5 offen offset:1024 sc1\nbuffer_load_dwordx4 a[8:11], v1, s[16:19], s5 offen offset:2048 sc1\n"
"buffer_load_dwordx4 a[12:15], v1, s[16:19], s5 offen offset:3072 sc1\ns_add_u32 s5, s66, 4096\nbuffer_load_dwordx4 a[16:19], v1, s[16:19], s5 offen offset:0 sc1\n"
"buffer_load_dwordx4 a[20:23], v1, s[16:19], s5 offen offset:1024 sc1\nbuffer_load_dwordx4 a[24:27], v1, s[16:19], s5 offen offset:2048 sc1\n"
"buffer_load_dwordx4 a[28:31], v1, s[16:19], s5 offen offset:3072 sc1\ns_add_u32 s5, s66, 16384\nbuffer_load_dwordx4 a[32:35], v1, s[16:19], s5 offen offset:0 sc1\n"
"buffer_load_dwordx4 a[36:39], v1, s[16:19], s5 offen offset:1024 sc1\nbuffer_load_dwordx4 a[40:43], v1, s[16:19], s5 offen offset:2048 sc1\n"
"buffer_load_dwordx4 a[44:47], v1, s[16:19], s5 offen offset:3072 sc1\ns_add_u32 s5, s66, 20480\nbuffer_load_dwordx4 a[48:51], v1, s[16:19], s5 offen offset:0 sc1\n"
"buffer_load_dwordx4 a[52:55], v1, s[16:19], s5 offen offset:1024 sc1\nbuffer_load_dwordx4 a[56:59], v1, s[16:19], s5 offen offset:2048 sc1\n"
"buffer_load_dwordx4 a[60:63], v1, s[16:19], s5 offen offset:3072 sc1\nWV 0\ns_mov_b32 s67, 0\n.E218:\nv_cmp_lt_f32 vcc, 0, v101\nSU 20,100,88\nv_mul_f32 v101, 0x3e800000, v101\n"
"v_cndmask_b32 v20, 0, v20, vcc\nv_cmp_lt_f32 vcc, 0, v105\nSU 21,104,89\nv_mul_f32 v105, 0x3e800000, v105\nv_cndmask_b32 v21, 0, v21, vcc\nv_or_b32 v91, v20, v21\n"
"v_cmp_ne_u32 vcc, 0, v91\ns_nop 0\ns_cbranch_vccnz .D213\nAD 86,86,101\nAD 87,87,105\n.D214:\nMvva 128,114,0\nMvva 136,112,0\nEX 82,58\nMvva 140,112,4\nMvva 132,114,4\nWV 15\n"
"EX 83,59\nMvva 144,114,8\nAD 84,84,82\nMvva 152,112,8\nD1 1,16,46,3072\nMvva 156,112,12\ns_add_u32 m0, s89, 32768\ns_add_u32 s46, s86, s88\nMvva 148,114,12\nAD 84,84,83\n"
"CV 75,82,83\nMvva 160,114,16\nRa 128,2,16384\nv_xor_b32 v3, 0x10000, v2\nv_add_u32 v0, 0x20000, v1\nMvva 168,112,16\nEX 80,60\nMvva 172,112,20\nRa 132,2,17408\n"
"s_sub_u32 s94, s49, 1703936\nMvva 164,114,20\nWV 10\nEX 81,61\nMvva 176,114,24\nRa 136,2,18432\nAD 85,85,80\ns_xor_b32 s92, s94, 0x202000\nMvva 184,112,24\nD1 1,16,46,0\n"
"s_lshl_b32 s89, s11, 12\nMvva 188,112,28\nRa 140,2,19456\ns_lshl1_add_u32 s2, s12, 0\nMvva 180,114,28\nAD 85,85,81\nCV 78,80,81\nMvva 192,114,32\nRa 144,2,20480\n"
"s_lshl3_add_u32 s2, s2, s13\ns_lshl4_add_u32 s2, s2, 0xc048200\nMvva 200,112,32\nEX 82,62\nMvva 204,112,36\nRa 148,2,21504\ns_and_b32 s3, s72, 0x2000\nMvva 196,114,36\nWV 6\n"
"EX 83,63\nMvva 208,114,40\nRa 152,2,22528\nAD 85,85,82\nMvva 216,112,40\nD1 1,16,46,1024\ns_cmp_eq_u32 s11, 1\ns_cselect_b32 s54, s2, s54\ns_cselect_b32 s3, s3, 0\n"
"Mvva 220,112,44\nRa 156,2,23552\nv_xor_b32 v25, s3, v25\nMvva 212,114,44\nAD 85,85,83\nCV 79,82,83\nMvva 224,114,48\nRa 160,2,24576\nv_max_f32 v90, v84, v85\nMvva 232,112,48\n"
"v_cmp_lt_f32 vcc, s63, v90\nMvva 236,112,52\nRa 164,2,25600\nMvva 228,114,52\ns_waitcnt vmcnt(4) lgkmcnt(8)\nMvva 240,114,56\nRa 168,2,26624\nMvva 248,112,56\nD1 1,16,46,2048\n"
"Mvva 252,112,60\nRa 172,2,27648\nMvva 244,114,60\ns_cbranch_vccnz .T219\n.T220:\nAD 86,86,84\nAD 87,87,85\nMvav 128,128,72\nRa 176,2,28672\nv_mov_b32 v84, v86\nv_mov_b32 v85, v87\n"
"Mvav 132,128,76\nv_permlane32_swap_b32 v86, v84\nv_permlane32_swap_b32 v87, v85\nMvav 140,132,76\nRa 180,2,29696\nAD 86,86,84\nMvav 136,132,72\nWL 8\nAD 87,87,85\nMvav 144,136,72\n"
"Ra 184,2,30720\nv_mov_b32 v84, v86\nv_mov_b32 v85, v87\nMvav 148,136,76\nD1 1,16,46,3072\nv_permlane16_swap_b32 v86, v84\nMvav 156,140,76\nRa 188,2,31744\n"
"v_permlane16_swap_b32 v87, v85\nMvav 152,140,72\nAD 86,86,84\nAD 87,87,85\nMvav 160,144,72\nRa 128,2,0\nv_rcp_f32 v86, v86\nMvav 164,144,76\nv_rcp_f32 v87, v87\nMvav 172,148,76\n"
"Ra 132,2,1024\nMU 128,128,86\nMU 129,129,86\nMvav 168,148,72\nWL 8\ns_add_u32 m0, s89, 131072\ns_add_u32 s46, s96, 1212416\nCV 96,128,129\nMvav 176,152,72\nRa 136,2,2048\n"
"MU 130,130,86\nMU 131,131,86\nMvav 180,152,76\nD1 1,16,46,0\nCV 97,130,131\nMU 132,132,87\nMvav 188,156,76\nRa 140,2,3072\nMU 133,133,87\nCV 100,132,133\nMvav 184,156,72\n"
"MU 134,134,87\nMU 135,135,87\nCV 101,134,135\nMvav 192,160,72\nRa 144,2,4096\nMU 136,136,86\nMU 137,137,86\nCV 98,136,137\nMvav 196,160,76\nMU 138,138,86\nMU 139,139,86\n"
"CV 99,138,139\nMvav 204,164,76\nRa 148,2,5120\nMU 140,140,87\nMU 141,141,87\nMvav 200,164,72\nWL 8\nCV 102,140,141\nMU 142,142,87\nMvav 208,168,72\nRa 152,2,6144\nMU 143,143,87\n"
"CV 103,142,143\nMvav 212,168,76\nD1 1,16,46,1024\nMU 144,144,86\nMU 145,145,86\nMvav 220,172,76\nRa 156,2,7168\nCV 104,144,145\nMU 146,146,86\nMU 147,147,86\nMvav 216,172,72\n"
"CV 105,146,147\nMU 148,148,87\nMU 149,149,87\nMvav 224,176,72\nRa 160,2,8192\nCV 108,148,149\nMU 150,150,87\nMvav 228,176,76\nMU 151,151,87\nCV 109,150,151\nMU 152,152,86\n"
"Mvav 236,180,76\nRa 164,2,9216\nMU 153,153,86\nCV 106,152,153\nMvav 232,180,72\nWL 8\nMU 154,154,86\nMU 155,155,86\nCV 107,154,155\nMvav 240,184,72\nRa 168,2,10240\nMU 156,156,87\n"
"MU 157,157,87\nMvav 244,184,76\nD1 1,16,46,2048\nCV 110,156,157\nMU 158,158,87\nMvav 252,188,76\nRa 172,2,11264\nMU 159,159,87\nCV 111,158,159\nMvav 248,188,72\nMU 160,160,86\n"
"MU 161,161,86\nCV 112,160,161\nZvav 128,128,96\nRa 176,2,12288\nMU 162,162,86\nMU 163,163,86\nMvav 128,132,104\nCV 113,162,163\nMU 164,164,87\nZvav 132,132,108\nRa 180,2,13312\n"
"MU 165,165,87\nCV 116,164,165\nMvav 132,128,100\nWL 8\nMU 166,166,87\nMU 167,167,87\nZvav 140,136,100\nRa 184,2,14336\nCV 117,166,167\nMU 168,168,86\nMvav 140,140,108\n"
"D1 1,16,46,3072\nMU 169,169,86\nCV 114,168,169\nZvav 136,140,104\nRa 188,2,15360\ns_barrier\nMU 170,170,86\nMvav 136,136,96\nMU 171,171,86\nCV 115,170,171\nMU 172,172,87\n"
"Zvav 144,144,96\nRa 128,2,32768\nMU 173,173,87\nCV 118,172,173\nMvav 144,148,104\nMU 174,174,87\nMU 175,175,87\nbuffer_store_dword v25, off, s[16:19], s54 offset:512 sc0 sc1\n"
"Zvav 148,148,108\nRa 132,2,33792\nCV 119,174,175\nMU 176,176,86\nMvav 148,144,100\nWL 8\ns_add_u32 m0, s89, 114688\ns_add_u32 s46, s96, 1179648\nZvav 156,152,100\nRa 136,2,34816\n"
"MU 177,177,86\nCV 120,176,177\nMvav 156,156,108\nD1 1,16,46,0\nMU 178,178,86\nMU 179,179,86\nZvav 152,156,104\nRa 140,2,35840\nCV 121,178,179\nMU 180,180,87\nMvav 152,152,96\n"
"MU 181,181,87\nCV 124,180,181\nMU 182,182,87\nZvav 64,160,96\nRa 144,2,36864\nMU 183,183,87\nCV 125,182,183\nMvav 64,164,104\nMU 184,184,86\nMU 185,185,86\nZvav 68,164,108\n"
"Ra 148,2,37888\nCV 122,184,185\nMU 186,186,86\nMvav 68,160,100\nWL 8\nMU 187,187,86\nCV 123,186,187\nZvav 76,168,100\nRa 152,2,38912\nMU 188,188,87\nMU 189,189,87\n"
"Mvav 76,172,108\nD1 1,16,46,1024\nCV 126,188,189\nMU 190,190,87\nZvav 72,172,104\nRa 156,2,39936\nMU 191,191,87\nCV 127,190,191\nMvav 72,168,96\nMU 192,192,86\nMU 193,193,86\n"
"CV 32,192,193\nMvav 128,176,112\nRa 160,2,40960\nMU 194,194,86\nMU 195,195,86\nMvav 128,180,120\nCV 33,194,195\nMU 196,196,87\nMU 197,197,87\nMvav 132,180,124\nRa 164,2,41984\n"
"CV 36,196,197\nMU 198,198,87\nMvav 132,176,116\nWL 8\nMU 199,199,87\nMvav 140,184,116\nRa 168,2,43008\nCV 37,198,199\nMU 200,200,86\nMvav 140,188,124\nD1 1,16,46,2048\n"
"MU 201,201,86\nCV 34,200,201\nMvav 136,188,120\nRa 172,2,44032\nMU 202,202,86\nMU 203,203,86\nMvav 136,184,112\nCV 35,202,203\nMU 204,204,87\nMU 205,205,87\nMvav 144,128,112\n"
"Ra 176,2,45056\nCV 38,204,205\nMU 206,206,87\nMvav 144,132,120\nMU 207,207,87\nCV 39,206,207\nMvav 148,132,124\nRa 180,2,46080\nMU 208,208,86\nMU 209,209,86\nMvav 148,128,116\n"
"WL 8\nCV 40,208,209\nMU 210,210,86\nMvav 156,136,116\nRa 184,2,47104\nMU 211,211,86\nCV 41,210,211\nMvav 156,140,124\nD1 1,16,46,3072\nMU 212,212,87\nMU 213,213,87\n"
"Mvav 152,140,120\nRa 188,2,48128\nCV 44,212,213\nMvav 152,136,112\nMU 214,214,87\nMU 215,215,87\nCV 45,214,215\nMvav 64,144,112\nRa 128,2,49152\nMU 216,216,86\nMU 217,217,86\n"
"Mvav 64,148,120\nCV 42,216,217\nMU 218,218,86\nMU 219,219,86\nMvav 68,148,124\nRa 132,2,50176\nCV 43,218,219\nMU 220,220,87\nMvav 68,144,116\nWL 8\ns_add_u32 m0, s89, 147456\n"
"s_add_u32 s46, s96, 1245184\nMvav 76,152,116\nRa 136,2,51200\nMU 221,221,87\nCV 46,220,221\nMvav 76,156,124\nD1 1,16,46,0\nMU 222,222,87\nMU 223,223,87\nMvav 72,156,120\n"
"Ra 140,2,52224\nCV 47,222,223\nMvav 72,152,112\nMU 224,224,86\nMU 225,225,86\nCV 48,224,225\nMvav 128,160,32\nRa 144,2,53248\nMU 226,226,86\nMU 227,227,86\nMvav 128,164,40\n"
"CV 49,226,227\nMU 228,228,87\nMvav 132,164,44\nRa 148,2,54272\nMU 229,229,87\nCV 52,228,229\nMvav 132,160,36\nWL 8\nMU 230,230,87\nMU 231,231,87\nMvav 140,168,36\nRa 152,2,55296\n"
"CV 53,230,231\nMU 232,232,86\nMvav 140,172,44\nD1 1,16,46,1024\nMU 233,233,86\nCV 50,232,233\nMvav 136,172,40\nRa 156,2,56320\nMU 234,234,86\nMvav 136,168,32\nMU 235,235,86\n"
"CV 51,234,235\nMU 236,236,87\nMvav 144,176,32\nRa 160,2,57344\nMU 237,237,87\nCV 54,236,237\nMvav 144,180,40\nMU 238,238,87\nMU 239,239,87\nCV 55,238,239\nMvav 148,180,44\n"
"Ra 164,2,58368\nMU 240,240,86\nMU 241,241,86\nMvav 148,176,36\nWL 8\nCV 56,240,241\nMvav 156,184,36\nRa 168,2,59392\nMU 242,242,86\nMU 243,243,86\nMvav 156,188,44\n"
"D1 1,16,46,2048\nCV 57,242,243\nMU 244,244,87\nMvav 152,188,40\nRa 172,2,60416\nMU 245,245,87\nCV 60,244,245\nMvav 152,184,32\nMU 246,246,87\nMU 247,247,87\nCV 61,246,247\n"
"Mvav 64,128,32\nRa 176,2,61440\nMU 248,248,86\nMU 249,249,86\nMvav 64,132,40\nCV 58,248,249\nMU 250,250,86\nMvav 68,132,44\nRa 180,2,62464\nMU 251,251,86\nCV 59,250,251\n"
"Mvav 68,128,36\ns_waitcnt vmcnt(16) lgkmcnt(8)\nMU 252,252,87\nMU 253,253,87\nMvav 76,136,36\nRa 184,2,63488\nCV 62,252,253\nMU 254,254,87\nMvav 76,140,44\nD1 1,16,46,3072\n"
"MU 255,255,87\nCV 63,254,255\nMvav 72,140,40\nRa 188,2,64512\ns_barrier\nMvav 72,136,32\nMvav 128,144,48\nRa 128,3,0\nMvav 128,148,56\nMvav 132,148,60\nRa 132,3,1024\n"
"Mvav 132,144,52\nWL 8\nMvav 140,152,52\nRa 136,3,2048\nMvav 140,156,60\nMvav 136,156,56\nRa 140,3,3072\nMvav 136,152,48\nMvav 144,160,48\nRa 144,3,4096\nMvav 144,164,56\n"
"Mvav 148,164,60\nRa 148,3,5120\nCV 132,132,133\nMvav 148,160,52\nWL 8\nCV 133,134,135\nMvav 156,168,52\nRa 152,3,6144\nCV 134,140,141\nMvav 156,172,60\nCV 135,142,143\n"
"s_add_u32 s5, s94, 0\nMvav 152,172,56\nRa 156,3,7168\nSv 132,1,16,5,1024\nMvav 152,168,48\nCV 128,128,129\nCV 129,130,131\nMvav 64,176,48\nRa 160,3,8192\nCV 130,136,137\n"
"Mvav 64,180,56\nbuffer_load_dword v27, off, s[16:19], s60 offset:512 sc0 sc1\nCV 131,138,139\nMvav 68,180,60\nRa 164,3,9216\nSv 128,1,16,5,0\nMvav 68,176,52\nWL 6\nCV 148,148,149\n"
"Mvav 76,184,52\nRa 168,3,10240\nCV 149,150,151\nMvav 76,188,60\nCV 150,156,157\nCV 151,158,159\nMvav 72,188,56\nRa 172,3,11264\ns_add_u32 s5, s94, 131072\nMvav 72,184,48\n"
"Sv 148,1,16,5,1024\nCV 144,144,145\nZvav 160,128,96\nRa 176,3,12288\nCV 145,146,147\nMvav 160,132,104\nCV 146,152,153\nCV 147,154,155\nMvav 160,136,112\nRa 180,3,13312\n"
"Sv 144,1,16,5,0\nMvav 160,140,120\nWL 6\nCV 68,68,69\nZvav 164,140,124\nRa 184,3,14336\nCV 69,70,71\nMvav 164,136,116\n.W565:\ns_cbranch_execz .WB151\nCV 70,76,77\nCV 71,78,79\n"
"Mvav 164,132,108\nRa 188,3,15360\ns_add_u32 s5, s94, 262144\nMvav 164,128,100\nSv 68,1,16,5,1024\nCV 64,64,65\nMvav 164,144,36\nRa 128,3,16384\nCV 65,66,67\nMvav 164,148,44\n"
"CV 66,72,73\nCV 67,74,75\nMvav 164,152,52\nRa 132,3,17408\nSv 64,1,16,5,0\nMvav 164,156,60\nWL 6\nMvav 160,156,56\nRa 136,3,18432\nMvav 160,152,48\nMvav 160,148,40\n"
"Ra 140,3,19456\nMvav 160,144,32\nZvav 168,160,96\nRa 144,3,20480\nMvav 168,164,104\nMvav 168,168,112\nRa 148,3,21504\nMvav 168,172,120\nWL 6\nZvav 172,172,124\nRa 152,3,22528\n"
"Mvav 172,168,116\nMvav 172,164,108\nRa 156,3,23552\nMvav 172,160,100\nMvav 172,176,36\nRa 160,3,24576\nMvav 172,180,44\nMvav 172,184,52\nRa 164,3,25600\nMvav 172,188,60\nWL 6\n"
"Mvav 168,188,56\nRa 168,3,26624\nMvav 168,184,48\nMvav 168,180,40\nRa 172,3,27648\nMvav 168,176,32\nZvav 176,128,96\nRa 176,3,28672\nCV 164,164,165\nMvav 176,132,104\n"
"CV 165,166,167\nCV 166,172,173\nMvav 176,136,112\nRa 180,3,29696\nCV 167,174,175\nMvav 176,140,120\ns_waitcnt vmcnt(11) lgkmcnt(6)\ns_add_u32 s5, s94, 393216\nZvav 180,140,124\n"
"Ra 184,3,30720\nSv 164,1,16,5,1024\nMvav 180,136,116\nCV 160,160,161\nCV 161,162,163\nMvav 180,132,108\nRa 188,3,31744\ns_barrier\nMvav 180,128,100\nCV 162,168,169\n"
"CV 163,170,171\nMvav 180,144,36\nRa 128,3,32768\nSv 160,1,16,5,0\nMvav 180,148,44\nMvav 180,152,52\nRa 132,3,33792\nMvav 180,156,60\nWL 6\ns_add_u32 m0, s89, 16384\n"
"s_add_u32 s46, s96, 1277952\nMvav 176,156,56\nRa 136,3,34816\nMvav 176,152,48\nD1 1,16,46,0\nMvav 176,148,40\nRa 140,3,35840\nMvav 176,144,32\nZvav 184,160,96\nRa 144,3,36864\n"
"Mvav 184,164,104\nMvav 184,168,112\nRa 148,3,37888\nMvav 184,172,120\nWL 6\nZvav 188,172,124\nRa 152,3,38912\nMvav 188,168,116\nD1 1,16,46,1024\nMvav 188,164,108\nRa 156,3,39936\n"
"Mvav 188,160,100\nMvav 188,176,36\nRa 160,3,40960\nWV 9\nv_cmp_ne_u32 vcc, s15, v27\ns_nop 0\ns_cbranch_vccnz .S246\n.S247:\nMvav 188,180,44\ns_add_u32 s4, s92, 0\n"
"Mvav 188,184,52\nRa 164,3,41984\nMvav 188,188,60\nWL 6\nbuffer_load_dwordx4 v[208:211], v1, s[16:19], s4 offen offset:0 sc0 sc1\nMvav 184,188,56\nRa 168,3,43008\nMvav 184,184,48\n"
"D1 1,16,46,2048\nMvav 184,180,40\nRa 172,3,44032\nMvav 184,176,32\nbuffer_load_dwordx4 v[212:215], v1, s[16:19], s4 offen offset:1024 sc0 sc1\nZvav 192,128,96\nRa 176,3,45056\n"
"v_lshrrev_b32 v88, 8, v1\nMvav 192,132,104\nv_lshlrev_b32 v89, 5, v88\nCV 180,180,181\nMvav 192,136,112\nRa 180,3,46080\nCV 181,182,183\nMvav 192,140,120\nWL 6\nCV 182,188,189\n"
"Zvav 196,140,124\nRa 184,3,47104\nv_bfe_u32 v90, v1, 4, 4\nMvav 196,136,116\nD1 1,16,46,3072\nCV 183,190,191\nMvav 196,132,108\nRa 188,3,48128\ns_add_u32 s5, s94, 524288\n"
"Mvav 196,128,100\nv_mul_u32_u24 v91, 896, v90\nSv 180,1,16,5,1024\nMvav 196,144,36\nRa 128,3,49152\nv_lshl_add_u32 v91, v88, 4, v91\nMvav 196,148,44\nv_add_u32 v90, s21, v90\n"
"CV 176,176,177\nMvav 196,152,52\nRa 132,3,50176\nCV 177,178,179\nMvav 196,156,60\nWL 6\ns_add_u32 m0, s89, 0\ns_add_u32 s46, s96, 1310720\nMvav 192,156,56\nRa 136,3,51200\n"
"v_lshlrev_b32 v88, 3, v90\nMvav 192,152,48\nD1 1,16,46,0\nCV 178,184,185\nMvav 192,148,40\nRa 140,3,52224\nCV 179,186,187\nMvav 192,144,32\nv_add_u32 v94, 64, v90\n"
"Sv 176,1,16,5,0\nZvav 200,160,96\nRa 144,3,53248\ns_mov_b32 s63, 0xc040000\nMvav 200,164,104\nv_lshrrev_b32 v92, 8, v1\nMvav 200,168,112\nRa 148,3,54272\nMvav 200,172,120\nWL 6\n"
"Zvav 204,172,124\nRa 152,3,55296\nv_bfe_u32 v93, v1, 6, 2\nMvav 204,168,116\nD1 1,16,46,1024\nv_cmp_eq_u32 vcc, v92, v93\nv_and_b32 v92, 16, v1\nv_mov_b32 v93, 0x3f80\n"
"v_cndmask_b32 v93, 0, v93, vcc\nMvav 204,164,108\nRa 156,3,56320\nMvav 204,160,100\nv_lshlrev_b32 v93, v92, v93\nMvav 204,176,36\nRa 160,3,57344\nv_and_b32 v92, 32, v1\n"
"Mvav 204,180,44\ns_add_u32 s4, s92, 131072\nv_cmp_eq_u32 vcc, 0, v92\nv_mov_b32 v4, 0\nv_mov_b32 v5, 0\nv_cndmask_b32 v6, 0, v93, vcc\nMvav 204,184,52\nRa 164,3,58368\n"
"Mvav 204,188,60\nWL 6\nbuffer_load_dwordx4 v[216:219], v1, s[16:19], s4 offen offset:0 sc0 sc1\nMvav 200,188,56\nRa 168,3,59392\nv_sub_u32 v7, v93, v6\nMvav 200,184,48\n"
"D1 1,16,46,2048\nMvav 200,180,40\nRa 172,3,60416\nMvav 200,176,32\nbuffer_load_dwordx4 v[220:223], v1, s[16:19], s4 offen offset:1024 sc0 sc1\nZvav 160,128,96\nRa 176,3,61440\n"
"v_mov_b32 v8, 0\nMvav 160,132,104\nv_mov_b32 v9, 0\nCV 196,196,197\nMvav 160,136,112\nRa 180,3,62464\nCV 197,198,199\nMvav 160,140,120\nWL 6\nCV 198,204,205\nZvav 164,140,124\n"
"Ra 184,3,63488\nCV 199,206,207\nMvav 164,136,116\nD1 1,16,46,3072\ns_add_u32 s5, s94, 655360\nMvav 164,132,108\nRa 188,3,64512\ns_barrier\nMvav 164,128,100\nSv 196,1,16,5,1024\n"
"CV 192,192,193\nMvav 164,144,36\nRa 128,0,0\nCV 193,194,195\nMvav 164,148,44\nCV 194,200,201\nCV 195,202,203\nMvav 164,152,52\nRa 132,0,1024\nSv 192,1,16,5,0\nMvav 164,156,60\n"
"WL 6\ns_add_u32 m0, s89, 32768\ns_add_u32 s46, s96, 1343488\nMvav 160,156,56\nRa 136,0,2048\nMvav 160,152,48\nD1 1,16,46,0\nMvav 160,148,40\nRa 140,0,3072\nMvav 160,144,32\n"
"Zvav 168,160,96\nRa 144,0,4096\nWV 12\nv_max_u32 v21, v211, v215\nv_cmp_eq_u32 vcc, -1, v21\ns_nop 0\ns_cbranch_vccnz .S222\n.S223:\nMvav 168,164,104\n"
"v_lshlrev_b32 v176, 16, v208\nMvav 168,168,112\nRa 148,0,5120\nMvav 168,172,120\nWL 6\nv_and_b32 v177, 0xffff0000, v208\nZvav 172,172,124\nRa 152,0,6144\nMvav 172,168,116\n"
"D1 1,16,46,1024\nMvav 172,164,108\nRa 156,0,7168\nv_lshlrev_b32 v178, 16, v209\nMvav 172,160,100\nv_and_b32 v179, 0xffff0000, v209\nMvav 172,176,36\nRa 160,0,8192\n"
"v_lshlrev_b32 v180, 16, v212\nMvav 172,180,44\nv_and_b32 v181, 0xffff0000, v212\ns_add_u32 s4, s92, 262144\nMvav 172,184,52\nRa 164,0,9216\nMvav 172,188,60\nWL 6\n"
"v_lshlrev_b32 v182, 16, v213\nbuffer_load_dwordx4 v[224:227], v1, s[16:19], s4 offen offset:0 sc0 sc1\nMvav 168,188,56\nRa 168,0,10240\nMvav 168,184,48\nD1 1,16,46,2048\n"
"Mvav 168,180,40\nRa 172,0,11264\nv_and_b32 v183, 0xffff0000, v213\nMvav 168,176,32\nv_lshlrev_b32 v184, 16, v210\n"
"buffer_load_dwordx4 v[228:231], v1, s[16:19], s4 offen offset:1024 sc0 sc1\nMvav 176,128,96\nRa 176,0,12288\nv_and_b32 v185, 0xffff0000, v210\nMvav 176,132,104\n"
"v_lshlrev_b32 v186, 16, v211\nCV 164,164,165\nMvav 176,136,112\nRa 180,0,13312\nCV 165,166,167\nMvav 176,140,120\nWL 6\nCV 166,172,173\nMvav 180,140,124\nRa 184,0,14336\n"
"v_and_b32 v187, 0xffff0000, v211\nMvav 180,136,116\nD1 1,16,46,3072\nCV 167,174,175\nMvav 180,132,108\nRa 188,0,15360\ns_add_u32 s5, s94, 786432\nMvav 180,128,100\n"
"v_lshlrev_b32 v188, 16, v214\nSv 164,1,16,5,1024\nMvav 180,144,36\nRa 128,0,16384\nv_and_b32 v189, 0xffff0000, v214\nMvav 180,148,44\nv_lshlrev_b32 v190, 16, v215\nCV 160,160,161\n"
"Mvav 180,152,52\nRa 132,0,17408\nCV 161,162,163\nMvav 180,156,60\nWL 6\ns_add_u32 m0, s89, 49152\ns_add_u32 s46, s96, 1376256\nMvav 176,156,56\nRa 136,0,18432\n"
"v_and_b32 v191, 0xffff0000, v215\nMvav 176,152,48\nD1 1,16,46,0\nCV 162,168,169\nMvav 176,148,40\nRa 140,0,19456\nCV 163,170,171\nMvav 176,144,32\nSv 160,1,16,5,0\n"
"Mvav 184,160,96\nRa 144,0,20480\nWV 12\nv_max_u32 v21, v219, v223\nv_cmp_eq_u32 vcc, -1, v21\ns_nop 0\ns_cbranch_vccnz .S225\n.S226:\nMvav 184,164,104\n"
"v_lshlrev_b32 v192, 16, v216\nMvav 184,168,112\nRa 148,0,21504\nMvav 184,172,120\nWL 6\nv_and_b32 v193, 0xffff0000, v216\nMvav 188,172,124\nRa 152,0,22528\nMvav 188,168,116\n"
"D1 1,16,46,1024\nMvav 188,164,108\nRa 156,0,23552\nv_lshlrev_b32 v194, 16, v217\nMvav 188,160,100\nv_and_b32 v195, 0xffff0000, v217\nMvav 188,176,36\nRa 160,0,24576\n"
"v_lshlrev_b32 v196, 16, v220\nMvav 188,180,44\nv_and_b32 v197, 0xffff0000, v220\ns_add_u32 s4, s92, 393216\nMvav 188,184,52\nRa 164,0,25600\nMvav 188,188,60\nWL 6\n"
"v_lshlrev_b32 v198, 16, v221\nbuffer_load_dwordx4 v[208:211], v1, s[16:19], s4 offen offset:0 sc0 sc1\nMvav 184,188,56\nRa 168,0,26624\nMvav 184,184,48\nD1 1,16,46,2048\n"
"Mvav 184,180,40\nRa 172,0,27648\nv_and_b32 v199, 0xffff0000, v221\nMvav 184,176,32\nv_lshlrev_b32 v200, 16, v218\n"
"buffer_load_dwordx4 v[212:215], v1, s[16:19], s4 offen offset:1024 sc0 sc1\nMvav 192,128,96\nRa 176,0,28672\nv_and_b32 v201, 0xffff0000, v218\nMvav 192,132,104\n"
"v_lshlrev_b32 v202, 16, v219\nCV 180,180,181\nMvav 192,136,112\nRa 180,0,29696\nCV 181,182,183\nMvav 192,140,120\ns_waitcnt vmcnt(15) lgkmcnt(6)\nCV 182,188,189\nMvav 196,140,124\n"
"Ra 184,0,30720\nv_and_b32 v203, 0xffff0000, v219\nMvav 196,136,116\nD1 1,16,46,3072\nCV 183,190,191\nMvav 196,132,108\nRa 188,0,31744\ns_barrier\nMvav 196,128,100\n"
"v_lshlrev_b32 v204, 16, v222\ns_add_u32 s5, s94, 917504\nMvav 196,144,36\nRa 128,2,16384\nv_and_b32 v205, 0xffff0000, v222\nMvav 196,148,44\nv_lshlrev_b32 v206, 16, v223\n"
"Sv 180,1,16,5,1024\nMvav 196,152,52\nRa 132,2,17408\nCV 176,176,177\nMvav 196,156,60\nWL 6\ns_add_u32 m0, s89, 65536\ns_add_u32 s46, s96, 1409024\nMvav 192,156,56\nRa 136,2,18432\n"
"v_and_b32 v207, 0xffff0000, v223\nMvav 192,152,48\nD1 1,16,46,0\nCV 177,178,179\nMvav 192,148,40\nRa 140,2,19456\nCV 178,184,185\nMvav 192,144,32\nCV 179,186,187\nSv 176,1,16,5,0\n"
"Mvav 200,160,96\nRa 144,2,20480\nWV 12\nv_max_u32 v21, v227, v231\nv_cmp_eq_u32 vcc, -1, v21\ns_nop 0\ns_cbranch_vccnz .S228\n.S229:\nMvav 200,164,104\n"
"v_lshlrev_b32 v160, 16, v224\nMvav 200,168,112\nRa 148,2,21504\nMvav 200,172,120\nWL 6\nv_and_b32 v161, 0xffff0000, v224\nMvav 204,172,124\nRa 152,2,22528\nMvav 204,168,116\n"
"D1 1,16,46,1024\nMvav 204,164,108\nRa 156,2,23552\nv_lshlrev_b32 v162, 16, v225\nMvav 204,160,100\nv_and_b32 v163, 0xffff0000, v225\nMvav 204,176,36\nRa 160,2,24576\n"
"v_lshlrev_b32 v164, 16, v228\nMvav 204,180,44\nv_and_b32 v165, 0xffff0000, v228\ns_add_u32 s4, s92, 524288\nMvav 204,184,52\nRa 164,2,25600\nMvav 204,188,60\nWL 6\n"
"v_lshlrev_b32 v166, 16, v229\nbuffer_load_dwordx4 v[216:219], v1, s[16:19], s4 offen offset:0 sc0 sc1\nMvav 200,188,56\nRa 168,2,26624\nMvav 200,184,48\nD1 1,16,46,2048\n"
"Mvav 200,180,40\nRa 172,2,27648\nv_and_b32 v167, 0xffff0000, v229\nMvav 200,176,32\nv_lshlrev_b32 v168, 16, v226\n"
"buffer_load_dwordx4 v[220:223], v1, s[16:19], s4 offen offset:1024 sc0 sc1\nMvav 160,128,96\nRa 176,2,28672\nv_and_b32 v169, 0xffff0000, v226\nMvav 160,132,104\n"
"v_lshlrev_b32 v170, 16, v227\nCV 196,196,197\nMvav 160,136,112\nRa 180,2,29696\nCV 197,198,199\nMvav 160,140,120\nWL 6\nCV 198,204,205\nMvav 164,140,124\nRa 184,2,30720\n"
"v_and_b32 v24, 0x70, v1\nMvav 164,136,116\nD1 1,16,46,3072\nCV 199,206,207\nMvav 164,132,108\nRa 188,2,31744\nv_and_b32 v171, 0xffff0000, v227\nMvav 164,128,100\n"
"v_lshlrev_b32 v172, 16, v230\ns_add_u32 s5, s94, 1048576\nMvav 164,144,36\nRa 128,2,0\nv_lshlrev_b32 v24, 5, v24\nMvav 164,148,44\nv_and_b32 v173, 0xffff0000, v230\n"
"Sv 196,1,16,5,1024\nMvav 164,152,52\nRa 132,2,1024\nCV 192,192,193\nMvav 164,156,60\nWL 6\nv_lshlrev_b32 v174, 16, v231\nMvav 160,156,56\nRa 136,2,2048\n"
"buffer_load_dword v27, v24, s[16:19], s41 offen sc0 sc1\nMvav 160,152,48\nv_and_b32 v175, 0xffff0000, v231\nCV 193,194,195\nMvav 160,148,40\nRa 140,2,3072\nCV 194,200,201\n"
"Mvav 160,144,32\nCV 195,202,203\nSv 192,1,16,5,0\nMvav 168,160,96\nRa 144,2,4096\nWV 12\nv_max_u32 v21, v211, v215\nv_cmp_eq_u32 vcc, -1, v21\ns_nop 0\ns_cbranch_vccnz .S231\n"
".S232:\nMvav 168,164,104\nv_lshlrev_b32 v176, 16, v208\nMvav 168,168,112\nRa 148,2,5120\nMvav 168,172,120\nWL 6\nMvav 172,172,124\nRa 152,2,6144\nv_and_b32 v177, 0xffff0000, v208\n"
".W566:\ns_cbranch_execz .WB151\nMvav 172,168,116\nv_lshlrev_b32 v178, 16, v209\nMvav 172,164,108\nRa 156,2,7168\nMvav 172,160,100\nv_and_b32 v179, 0xffff0000, v209\n"
"Mvav 172,176,36\nRa 160,2,8192\nv_lshlrev_b32 v180, 16, v212\nMvav 172,180,44\nv_and_b32 v181, 0xffff0000, v212\ns_add_u32 s4, s92, 655360\nMvav 172,184,52\nRa 164,2,9216\n"
"Mvav 172,188,60\nWL 6\nbuffer_load_dwordx4 v[224:227], v1, s[16:19], s4 offen offset:0 sc0 sc1\nMvav 168,188,56\nRa 168,2,10240\nv_lshlrev_b32 v182, 16, v213\nMvav 168,184,48\n"
"v_and_b32 v183, 0xffff0000, v213\nMvav 168,180,40\nRa 172,2,11264\nMvav 168,176,32\nv_lshlrev_b32 v184, 16, v210\n"
"buffer_load_dwordx4 v[228:231], v1, s[16:19], s4 offen offset:1024 sc0 sc1\nMvav 176,128,96\nRa 176,2,12288\nv_and_b32 v185, 0xffff0000, v210\nMvav 176,132,104\n"
"v_lshlrev_b32 v186, 16, v211\nCV 164,164,165\nMvav 176,136,112\nRa 180,2,13312\nCV 165,166,167\nMvav 176,140,120\ns_waitcnt vmcnt(13) lgkmcnt(6)\nCV 166,172,173\nMvav 180,140,124\n"
"Ra 184,2,14336\nCV 167,174,175\nMvav 180,136,116\nv_and_b32 v187, 0xffff0000, v211\ns_add_u32 s5, s94, 1179648\nMvav 180,132,108\nRa 188,2,15360\ns_barrier\nMvav 180,128,100\n"
"v_lshlrev_b32 v188, 16, v214\nSv 164,1,16,5,1024\nMvav 180,144,36\nRa 128,2,32768\nv_and_b32 v189, 0xffff0000, v214\nMvav 180,148,44\nv_lshlrev_b32 v190, 16, v215\nCV 160,160,161\n"
"Mvav 180,152,52\nRa 132,2,33792\nCV 161,162,163\nMvav 180,156,60\nWL 6\nCV 162,168,169\nMvav 176,156,56\nRa 136,2,34816\nCV 163,170,171\nMvav 176,152,48\n"
"v_and_b32 v191, 0xffff0000, v215\nSv 160,1,16,5,0\nMvav 176,148,40\nRa 140,2,35840\nMvav 176,144,32\nMvav 184,160,96\nRa 144,2,36864\nWV 8\nv_max_u32 v21, v219, v223\n"
"v_cmp_eq_u32 vcc, -1, v21\ns_nop 0\ns_cbranch_vccnz .S234\n.S235:\nMvav 184,164,104\nv_lshlrev_b32 v192, 16, v216\nMvav 184,168,112\nRa 148,2,37888\nMvav 184,172,120\nWL 6\n"
"Mvav 188,172,124\nRa 152,2,38912\nv_and_b32 v193, 0xffff0000, v216\nMvav 188,168,116\nv_lshlrev_b32 v194, 16, v217\nMvav 188,164,108\nRa 156,2,39936\nMvav 188,160,100\n"
"v_and_b32 v195, 0xffff0000, v217\nMvav 188,176,36\nRa 160,2,40960\nv_lshlrev_b32 v196, 16, v220\nMvav 188,180,44\nv_and_b32 v197, 0xffff0000, v220\ns_add_u32 s4, s92, 786432\n"
"Mvav 188,184,52\nRa 164,2,41984\nMvav 188,188,60\nWL 6\nbuffer_load_dwordx4 v[208:211], v1, s[16:19], s4 offen offset:0 sc0 sc1\nMvav 184,188,56\nRa 168,2,43008\n"
"v_lshlrev_b32 v198, 16, v221\nMvav 184,184,48\nv_and_b32 v199, 0xffff0000, v221\nMvav 184,180,40\nRa 172,2,44032\nMvav 184,176,32\nv_lshlrev_b32 v200, 16, v218\n"
"buffer_load_dwordx4 v[212:215], v1, s[16:19], s4 offen offset:1024 sc0 sc1\nMvav 192,128,96\nRa 176,2,45056\nv_and_b32 v201, 0xffff0000, v218\nMvav 192,132,104\n"
"v_lshlrev_b32 v202, 16, v219\nCV 180,180,181\nMvav 192,136,112\nRa 180,2,46080\nCV 181,182,183\nMvav 192,140,120\nWL 6\nCV 182,188,189\nMvav 196,140,124\nRa 184,2,47104\n"
"CV 183,190,191\nMvav 196,136,116\nv_and_b32 v203, 0xffff0000, v219\ns_add_u32 s5, s94, 1310720\nMvav 196,132,108\nRa 188,2,48128\nSv 180,1,16,5,1024\nMvav 196,128,100\n"
"v_lshlrev_b32 v204, 16, v222\nCV 176,176,177\nMvav 196,144,36\nRa 128,2,49152\nv_and_b32 v205, 0xffff0000, v222\nMvav 196,148,44\nv_lshlrev_b32 v206, 16, v223\nCV 177,178,179\n"
"Mvav 196,152,52\nRa 132,2,50176\nCV 178,184,185\nMvav 196,156,60\nWL 6\nCV 179,186,187\nMvav 192,156,56\nRa 136,2,51200\nSv 176,1,16,5,0\nMvav 192,152,48\n"
"v_and_b32 v207, 0xffff0000, v223\nMvav 192,148,40\nRa 140,2,52224\nMvav 192,144,32\nMvav 200,160,96\nRa 144,2,53248\nWV 6\nv_max_u32 v21, v227, v231\nv_cmp_eq_u32 vcc, -1, v21\n"
"s_nop 0\ns_cbranch_vccnz .S237\n.S238:\nMvav 200,164,104\nv_lshlrev_b32 v160, 16, v224\nv_xor_b32 v27, s15, v27\nv_and_b32 v26, 0xffffdfff, v27\nv_cmp_ne_u32 s[64:65], 0, v27\n"
"v_cmp_ne_u32 vcc, 0, v26\ns_nop 0\ns_cbranch_vccnz .S249\n.S250:\nMvav 200,168,112\nRa 148,2,54272\ns_bitcmp1_b32 s64, 0\ns_cselect_b32 s2, 0x2000, 0\nMvav 200,172,120\nWL 6\n"
"Mvav 204,172,124\nRa 152,2,55296\nv_and_b32 v161, 0xffff0000, v224\nMvav 204,168,116\nv_lshlrev_b32 v162, 16, v225\nMvav 204,164,108\nRa 156,2,56320\ns_xor_b32 s2, s2, s20\n"
"Mvav 204,160,100\nv_and_b32 v163, 0xffff0000, v225\nMvav 204,176,36\nRa 160,2,57344\nv_lshlrev_b32 v164, 16, v228\nMvav 204,180,44\nv_and_b32 v165, 0xffff0000, v228\n"
"Mvav 204,184,52\nRa 164,2,58368\nMvav 204,188,60\nWL 6\ns_add_u32 s24, s2, 0x0\nMvav 200,188,56\nRa 168,2,59392\nv_lshlrev_b32 v166, 16, v229\nMvav 200,184,48\n"
"v_and_b32 v167, 0xffff0000, v229\ns_bitcmp1_b32 s64, 1\ns_cselect_b32 s2, 0x2000, 0\nMvav 200,180,40\nRa 172,2,60416\nMvav 200,176,32\nv_lshlrev_b32 v168, 16, v226\n"
"Mvav 160,128,96\nRa 176,2,61440\nv_and_b32 v169, 0xffff0000, v226\nMvav 160,132,104\nv_lshlrev_b32 v170, 16, v227\nCV 196,196,197\nMvav 160,136,112\nRa 180,2,62464\n"
"CV 197,198,199\nMvav 160,140,120\nWL 6\nCV 198,204,205\nMvav 164,140,124\nRa 184,2,63488\nCV 199,206,207\nMvav 164,136,116\nv_and_b32 v171, 0xffff0000, v227\n"
"s_add_u32 s5, s94, 1441792\nMvav 164,132,108\nRa 188,2,64512\ns_barrier\nMvav 164,128,100\nv_lshlrev_b32 v172, 16, v230\nSv 196,1,16,5,1024\nMvav 164,144,36\nRa 128,3,0\n"
"v_and_b32 v173, 0xffff0000, v230\nMvav 164,148,44\nv_lshlrev_b32 v174, 16, v231\nCV 192,192,193\nMvav 164,152,52\nRa 132,3,1024\nCV 193,194,195\nMvav 164,156,60\nWL 6\n"
"s_xor_b32 s2, s2, s20\nMvav 160,156,56\nRa 136,3,2048\nCV 194,200,201\nMvav 160,152,48\nv_and_b32 v175, 0xffff0000, v231\nCV 195,202,203\nMvav 160,148,40\nRa 140,3,3072\n"
"Sv 192,1,16,5,0\nMvav 160,144,32\ns_add_u32 s25, s2, 0x1800000\ns_bitcmp1_b32 s64, 2\ns_cselect_b32 s2, 0x2000, 0\nMvav 168,160,96\nRa 144,3,4096\nWV 4\nv_max_u32 v21, v211, v215\n"
"v_cmp_eq_u32 vcc, -1, v21\ns_nop 0\ns_cbranch_vccnz .S240\n.S241:\nMvav 168,164,104\nv_lshlrev_b32 v176, 16, v208\nMvav 168,168,112\nRa 148,3,5120\nMvav 168,172,120\nWL 6\n"
"Mvav 172,172,124\nRa 152,3,6144\nv_and_b32 v177, 0xffff0000, v208\nMvav 172,168,116\nv_lshlrev_b32 v178, 16, v209\nMvav 172,164,108\nRa 156,3,7168\ns_xor_b32 s2, s2, s20\n"
"Mvav 172,160,100\nv_and_b32 v179, 0xffff0000, v209\nMvav 172,176,36\nRa 160,3,8192\nv_lshlrev_b32 v180, 16, v212\nMvav 172,180,44\nv_and_b32 v181, 0xffff0000, v212\n"
"Mvav 172,184,52\nRa 164,3,9216\nMvav 172,188,60\nWL 6\ns_add_u32 s26, s2, 0x3000000\nMvav 168,188,56\nRa 168,3,10240\nv_lshlrev_b32 v182, 16, v213\nMvav 168,184,48\n"
"v_and_b32 v183, 0xffff0000, v213\ns_bitcmp1_b32 s64, 3\ns_cselect_b32 s2, 0x2000, 0\nMvav 168,180,40\nRa 172,3,11264\nMvav 168,176,32\nv_lshlrev_b32 v184, 16, v210\n"
"Mvav 176,128,96\nRa 176,3,12288\ns_xor_b32 s2, s2, s20\nMvav 176,132,104\nv_and_b32 v185, 0xffff0000, v210\nCV 164,164,165\nMvav 176,136,112\nRa 180,3,13312\nCV 165,166,167\n"
"Mvav 176,140,120\nWL 6\nCV 166,172,173\nMvav 180,140,124\nRa 184,3,14336\nCV 167,174,175\nMvav 180,136,116\nv_lshlrev_b32 v186, 16, v211\ns_add_u32 s5, s94, 1572864\n"
"Mvav 180,132,108\nRa 188,3,15360\ns_add_u32 s27, s2, 0x4800000\nMvav 180,128,100\nv_and_b32 v187, 0xffff0000, v211\ns_bitcmp1_b32 s64, 4\ns_cselect_b32 s2, 0x2000, 0\n"
"Mvav 180,144,36\nv_lshlrev_b32 v188, 16, v214\nSv 164,1,16,5,1024\nMvav 180,148,44\nv_and_b32 v189, 0xffff0000, v214\nCV 160,160,161\nMvav 180,152,52\n"
"v_lshlrev_b32 v190, 16, v215\nCV 161,162,163\nMvav 180,156,60\nWL 4\ns_xor_b32 s2, s2, s20\nMvav 176,156,56\nv_and_b32 v191, 0xffff0000, v215\nCV 162,168,169\nMvav 176,152,48\n"
"s_add_u32 s32, s2, 0x6000000\ns_bitcmp1_b32 s64, 5\ns_cselect_b32 s2, 0x2000, 0\nMvav 176,148,40\ns_xor_b32 s2, s2, s20\nCV 163,170,171\nMvav 176,144,32\n"
"s_add_u32 s33, s2, 0x7800000\ns_bitcmp1_b32 s64, 6\ns_cselect_b32 s2, 0x2000, 0\nMvav 184,160,96\ns_xor_b32 s2, s2, s20\ns_add_u32 s34, s2, 0x9000000\nMvav 184,164,104\n"
"s_bitcmp1_b32 s64, 7\ns_cselect_b32 s2, 0x2000, 0\nMvav 184,168,112\ns_xor_b32 s2, s2, s20\nSv 160,1,16,5,0\nMvav 184,172,120\nWL 0\nMvav 188,172,124\n"
"s_add_u32 s35, s2, 0xa800000\nMvav 188,168,116\nMvav 188,164,108\nMvav 188,160,100\nMvav 188,176,36\nMvav 188,180,44\nMvav 188,184,52\nMvav 188,188,60\nMvav 184,188,56\n"
"Mvav 184,184,48\nMvav 184,180,40\nMvav 184,176,32\nCV 180,180,181\nCV 181,182,183\ns_nop 1\nCV 182,188,189\nCV 183,190,191\ns_add_u32 s5, s94, 1703936\nSv 180,1,16,5,1024\n"
"CV 176,176,177\nCV 177,178,179\nCV 178,184,185\nCV 179,186,187\nSv 176,1,16,5,0\ns_cmp_eq_u32 s13, 0\ns_cbranch_scc1 .Z252\n"
"buffer_load_dwordx4 v[12:15], v91, s[28:31], s22 offen offset:0\nbuffer_load_dwordx4 v[16:19], v91, s[28:31], s23 offen offset:0\n"
"buffer_load_dwordx4 v[64:67], v89, s[16:19], s40 offen offset:0 sc0 sc1\nbuffer_load_dwordx4 v[68:71], v89, s[16:19], s40 offen offset:16 sc0 sc1\n"
"buffer_load_dwordx4 v[72:75], v89, s[16:19], s40 offen offset:1792 sc0 sc1\nbuffer_load_dwordx4 v[76:79], v89, s[16:19], s40 offen offset:1808 sc0 sc1\n"
"buffer_load_dwordx2 v[80:81], v88, s[16:19], s63 offen offset:0 sc0 sc1\nbuffer_load_dwordx2 v[82:83], v88, s[16:19], s63 offen offset:512 sc0 sc1\n"
"buffer_load_dwordx4 v[96:99], v1, s[16:19], s24 offen offset:0 sc0 sc1\nbuffer_load_dwordx4 v[100:103], v1, s[16:19], s24 offen offset:1024 sc0 sc1\n"
"buffer_load_dwordx4 v[104:107], v1, s[16:19], s25 offen offset:0 sc0 sc1\nbuffer_load_dwordx4 v[108:111], v1, s[16:19], s25 offen offset:1024 sc0 sc1\n"
"buffer_load_dwordx4 v[112:115], v1, s[16:19], s26 offen offset:0 sc0 sc1\nbuffer_load_dwordx4 v[116:119], v1, s[16:19], s26 offen offset:1024 sc0 sc1\n"
"buffer_load_dwordx4 v[120:123], v1, s[16:19], s27 offen offset:0 sc0 sc1\nbuffer_load_dwordx4 v[124:127], v1, s[16:19], s27 offen offset:1024 sc0 sc1\n"
"buffer_load_dwordx4 v[32:35], v1, s[16:19], s32 offen offset:0 sc0 sc1\nbuffer_load_dwordx4 v[36:39], v1, s[16:19], s32 offen offset:1024 sc0 sc1\n"
"buffer_load_dwordx4 v[40:43], v1, s[16:19], s33 offen offset:0 sc0 sc1\nbuffer_load_dwordx4 v[44:47], v1, s[16:19], s33 offen offset:1024 sc0 sc1\n"
"buffer_load_dwordx4 v[48:51], v1, s[16:19], s34 offen offset:0 sc0 sc1\nbuffer_load_dwordx4 v[52:55], v1, s[16:19], s34 offen offset:1024 sc0 sc1\n"
"buffer_load_dwordx4 v[56:59], v1, s[16:19], s35 offen offset:0 sc0 sc1\nbuffer_load_dwordx4 v[60:63], v1, s[16:19], s35 offen offset:1024 sc0 sc1\nWV 16\nv_xor_b32 v66, s15, v66\n"
"v_xor_b32 v70, s15, v70\nv_xor_b32 v74, s15, v74\nv_xor_b32 v78, s15, v78\nv_xor_b32 v81, s15, v81\nv_xor_b32 v83, s15, v83\nv_or3_b32 v92, v66, v70, v74\n"
"v_or3_b32 v92, v92, v78, v81\nv_or_b32 v92, v92, v83\nv_cmp_ne_u32 vcc, 0, v92\ns_nop 0\ns_cbranch_vccnz .S243\n.S244:\nv_and_b32 v92, 0x7ff, v67\nv_lshrrev_b32 v93, 16, v67\n"
"v_cmp_eq_u32 s[44:45], v92, v90\nv_cmp_eq_u32 s[46:47], v93, v90\nv_cmp_eq_u32 s[48:49], v92, v94\nv_cmp_eq_u32 s[50:51], v93, v94\nv_cndmask_b32 v12, v12, v64, s[44:45]\n"
"v_cndmask_b32 v13, v13, v65, s[44:45]\nv_cndmask_b32 v14, v14, v68, s[44:45]\nv_cndmask_b32 v15, v15, v69, s[44:45]\nv_cndmask_b32 v12, v12, v72, s[46:47]\n"
"v_cndmask_b32 v13, v13, v73, s[46:47]\nv_cndmask_b32 v14, v14, v76, s[46:47]\nv_cndmask_b32 v15, v15, v77, s[46:47]\nv_cndmask_b32 v16, v16, v64, s[48:49]\n"
"v_cndmask_b32 v17, v17, v65, s[48:49]\nv_cndmask_b32 v18, v18, v68, s[48:49]\nv_cndmask_b32 v19, v19, v69, s[48:49]\nv_cndmask_b32 v16, v16, v72, s[50:51]\n"
"v_cndmask_b32 v17, v17, v73, s[50:51]\nv_cndmask_b32 v18, v18, v76, s[50:51]\nv_cndmask_b32 v19, v19, v77, s[50:51]\nv_sub_f32 v92, 1.0, v80\nv_sub_f32 v93, 1.0, v82\nCV 92,92,93\n"
"v_and_b32 v93, 0xffff0000, v92\nv_lshlrev_b32 v92, 16, v92\nv_lshlrev_b32 v64, 16, v12\nv_and_b32 v65, 0xffff0000, v12\nMU 64,64,92\nMU 65,65,92\nv_lshlrev_b32 v66, 16, v13\n"
"v_and_b32 v67, 0xffff0000, v13\nMU 66,66,92\nMU 67,67,92\nv_lshlrev_b32 v68, 16, v14\nv_and_b32 v69, 0xffff0000, v14\nMU 68,68,92\nMU 69,69,92\nv_lshlrev_b32 v70, 16, v15\n"
"v_and_b32 v71, 0xffff0000, v15\nMU 70,70,92\nMU 71,71,92\nv_lshlrev_b32 v72, 16, v16\nv_and_b32 v73, 0xffff0000, v16\nMU 72,72,93\nMU 73,73,93\nv_lshlrev_b32 v74, 16, v17\n"
"v_and_b32 v75, 0xffff0000, v17\nMU 74,74,93\nMU 75,75,93\nv_lshlrev_b32 v76, 16, v18\nv_and_b32 v77, 0xffff0000, v18\nMU 76,76,93\nMU 77,77,93\nv_lshlrev_b32 v78, 16, v19\n"
"v_and_b32 v79, 0xffff0000, v19\nMU 78,78,93\nMU 79,79,93\ns_mov_b32 s7, 262144\nWV 0\n.Z255:\nv_max_u32 v84, v99, v103\nv_cmp_eq_u32 s[44:45], -1, v84\nv_max_u32 v84, v107, v111\n"
"v_cmp_eq_u32 s[46:47], -1, v84\nv_max_u32 v84, v115, v119\nv_cmp_eq_u32 s[48:49], -1, v84\nv_max_u32 v84, v123, v127\nv_cmp_eq_u32 s[50:51], -1, v84\nv_max_u32 v84, v35, v39\n"
"v_cmp_eq_u32 s[52:53], -1, v84\nv_max_u32 v84, v43, v47\nv_cmp_eq_u32 s[54:55], -1, v84\nv_max_u32 v84, v51, v55\nv_cmp_eq_u32 s[56:57], -1, v84\nv_max_u32 v84, v59, v63\n"
"v_cmp_eq_u32 s[58:59], -1, v84\ns_or_b64 s[0:1], s[44:45], s[46:47]\ns_or_b64 s[2:3], s[48:49], s[50:51]\ns_or_b64 s[0:1], s[0:1], s[52:53]\ns_or_b64 s[2:3], s[2:3], s[54:55]\n"
"s_or_b64 s[0:1], s[0:1], s[56:57]\ns_or_b64 s[2:3], s[2:3], s[58:59]\ns_or_b64 s[0:1], s[0:1], s[2:3]\ns_cbranch_scc1 .Z253\n.Z254:\nZvvv 136,6,96\nMvvv 136,6,104\nMvvv 136,6,112\n"
"Mvvv 136,6,120\nMvvv 136,6,32\nMvvv 136,6,40\nMvvv 136,6,48\nMvvv 136,6,56\nZvvv 140,4,96\nMvvv 140,4,104\nMvvv 140,4,112\nMvvv 140,4,120\nMvvv 140,4,32\nMvvv 140,4,40\n"
"Mvvv 140,4,48\nMvvv 140,4,56\nZvvv 144,6,100\nMvvv 144,6,108\nMvvv 144,6,116\nMvvv 144,6,124\nMvvv 144,6,36\nMvvv 144,6,44\nMvvv 144,6,52\nMvvv 144,6,60\nZvvv 148,4,100\n"
"Mvvv 148,4,108\nMvvv 148,4,116\nMvvv 148,4,124\nMvvv 148,4,36\nMvvv 148,4,44\nMvvv 148,4,52\nMvvv 148,4,60\nFM 64,80,136\nFM 65,80,137\nFM 66,80,138\nFM 67,80,139\nFM 68,80,140\n"
"FM 69,80,141\nFM 70,80,142\nFM 71,80,143\nCV 12,64,65\nCV 13,66,67\nCV 14,68,69\nCV 15,70,71\nSv 12,91,36,22,0\nFM 72,82,144\nFM 73,82,145\nFM 74,82,146\nFM 75,82,147\n"
"FM 76,82,148\nFM 77,82,149\nFM 78,82,150\nFM 79,82,151\nCV 16,72,73\nCV 17,74,75\nCV 18,76,77\nCV 19,78,79\nSv 16,91,36,23,0\n.Z252:\ns_endpgm\n.EP264:\n.S144:\n"
"s_mov_b32 s7, 262144\n.S146:\nbuffer_load_dwordx4 v[32:35], v1, s[16:19], s6 offen offset:0 sc0 sc1\nbuffer_load_dwordx4 v[36:39], v1, s[16:19], s6 offen offset:1024 sc0 sc1\n"
"buffer_load_dwordx4 v[40:43], v1, s[16:19], s6 offen offset:2048 sc0 sc1\nbuffer_load_dwordx4 v[44:47], v1, s[16:19], s6 offen offset:3072 sc0 sc1\nWV 0\nv_xor_b32 v48, s15, v33\n"
"v_xor_b32 v49, s15, v35\nv_xor_b32 v50, s15, v37\nv_xor_b32 v51, s15, v39\nv_xor_b32 v52, s15, v41\nv_xor_b32 v53, s15, v43\nv_xor_b32 v54, s15, v45\nv_xor_b32 v55, s15, v47\n"
"v_or3_b32 v48, v48, v49, v50\nv_or3_b32 v51, v51, v52, v53\nv_or3_b32 v48, v48, v54, v55\nv_or_b32 v48, v48, v51\nv_cmp_ne_u32 vcc, 0, v48\ns_nop 0\ns_cbranch_vccz .S145\n"
"s_sleep 6\ns_sub_u32 s7, s7, 1\ns_cbranch_scc0 .S146\ns_branch .S145\n.S147:\ns_mov_b32 s7, 262144\n.S149:\ns_lshl_b32 s5, s73, 9\ns_add_u32 s5, s5, 0xc049400\n"
"v_lshrrev_b32 v27, 1, v1\nbuffer_load_dwordx2 v[22:23], v27, s[16:19], s5 offen sc0 sc1\nWV 0\nv_cmp_ne_u32 vcc, s15, v23\ns_nop 0\ns_cbranch_vccz .S148\ns_sleep 6\n"
"s_sub_u32 s7, s7, 1\ns_cbranch_scc0 .S149\ns_branch .S148\n.T153:\ns_mov_b32 s62, 1\ns_call_b64 s[80:81], .RA10\ns_branch .T154\n.T155:\ns_mov_b32 s62, 1\n"
"s_call_b64 s[80:81], .RA10\ns_branch .T156\n.T158:\ns_mov_b32 s62, 0\ns_call_b64 s[80:81], .RB11\ns_branch .T159\n.T160:\ns_mov_b32 s62, 0\ns_call_b64 s[80:81], .RA10\n"
"s_branch .T161\n.T163:\ns_mov_b32 s62, 0\ns_call_b64 s[80:81], .RB11\ns_branch .T164\n.T165:\ns_mov_b32 s62, 0\ns_call_b64 s[80:81], .RA10\ns_branch .T166\n.S168:\n"
"s_mov_b32 s7, 262144\n.S170:\nv_and_b32 v27, 0x70, v1\nbuffer_load_dword v27, v27, s[16:19], s61 offen offset:128 sc0 sc1\nWV 0\nv_cmp_ne_u32 vcc, s15, v27\ns_nop 0\n"
"s_cbranch_vccz .S169\ns_sleep 6\ns_sub_u32 s7, s7, 1\ns_cbranch_scc0 .S170\ns_branch .S169\n.S171:\ns_mov_b32 s7, 262144\n.S173:\nv_and_b32 v27, 0x70, v1\n"
"buffer_load_dword v27, v27, s[16:19], s61 offen offset:136 sc0 sc1\nWV 0\nv_cmp_ne_u32 vcc, s15, v27\ns_nop 0\ns_cbranch_vccz .S172\ns_sleep 6\ns_sub_u32 s7, s7, 1\n"
"s_cbranch_scc0 .S173\ns_branch .S172\n.S174:\ns_mov_b32 s7, 262144\n.S176:\nv_and_b32 v27, 0x70, v1\nbuffer_load_dword v27, v27, s[16:19], s61 offen offset:140 sc0 sc1\nWV 0\n"
"v_cmp_ne_u32 vcc, s15, v27\ns_nop 0\ns_cbranch_vccz .S175\ns_sleep 6\ns_sub_u32 s7, s7, 1\ns_cbranch_scc0 .S176\ns_branch .S175\n.S177:\ns_mov_b32 s7, 262144\n.S179:\n"
"v_and_b32 v27, 0x70, v1\nbuffer_load_dword v27, v27, s[16:19], s61 offen offset:12 sc0 sc1\nWV 0\nv_cmp_ne_u32 vcc, s15, v27\ns_nop 0\ns_cbranch_vccz .S178\ns_sleep 6\n"
"s_sub_u32 s7, s7, 1\ns_cbranch_scc0 .S179\ns_branch .S178\n.S180:\ns_mov_b32 s7, 262144\n.S182:\nv_and_b32 v27, 0x70, v1\n"
"buffer_load_dword v27, v27, s[16:19], s61 offen offset:8 sc0 sc1\nWV 0\nv_cmp_ne_u32 vcc, s15, v27\ns_nop 0\ns_cbranch_vccz .S181\ns_sleep 6\ns_sub_u32 s7, s7, 1\n"
"s_cbranch_scc0 .S182\ns_branch .S181\n.S183:\ns_mov_b32 s7, 262144\n.S185:\nv_and_b32 v27, 0xf0, v1\nbuffer_load_dword v27, v27, s[16:19], s61 offen offset:4 sc0 sc1\nWV 0\n"
"v_cmp_ne_u32 vcc, s15, v27\ns_nop 0\ns_cbranch_vccz .S184\ns_sleep 6\ns_sub_u32 s7, s7, 1\ns_cbranch_scc0 .S185\ns_branch .S184\n.S187:\ns_mov_b32 s7, 262144\n.S189:\n"
"v_lshrrev_b32 v104, 9, v1\nv_mul_u32_u24 v104, 0x18000, v104\nv_lshlrev_b32 v104, 8, v104\nv_and_b32 v105, 0x1f0, v1\nv_add_u32 v104, v104, v105\ns_mov_b32 s6, 0x15c0000\n"
"buffer_load_dword v96, v104, s[16:19], s6 offen offset:4 sc0 sc1\ns_mov_b32 s6, 0x15c2000\nbuffer_load_dword v97, v104, s[16:19], s6 offen offset:4 sc0 sc1\n"
"s_mov_b32 s6, 0x45c0000\nbuffer_load_dword v98, v104, s[16:19], s6 offen offset:4 sc0 sc1\ns_mov_b32 s6, 0x45c2000\n"
"buffer_load_dword v99, v104, s[16:19], s6 offen offset:4 sc0 sc1\ns_mov_b32 s6, 0x75c0000\nbuffer_load_dword v100, v104, s[16:19], s6 offen offset:4 sc0 sc1\n"
"s_mov_b32 s6, 0x75c2000\nbuffer_load_dword v101, v104, s[16:19], s6 offen offset:4 sc0 sc1\ns_mov_b32 s6, 0xa5c0000\n"
"buffer_load_dword v102, v104, s[16:19], s6 offen offset:4 sc0 sc1\ns_mov_b32 s6, 0xa5c2000\nbuffer_load_dword v103, v104, s[16:19], s6 offen offset:4 sc0 sc1\nWV 0\n"
"v_xor_b32 v96, s15, v96\nv_xor_b32 v97, s15, v97\nv_xor_b32 v98, s15, v98\nv_xor_b32 v99, s15, v99\nv_xor_b32 v100, s15, v100\nv_xor_b32 v101, s15, v101\n"
"v_xor_b32 v102, s15, v102\nv_xor_b32 v103, s15, v103\nv_min_u32 v96, v96, v97\nv_min_u32 v98, v98, v99\nv_min_u32 v100, v100, v101\nv_min_u32 v102, v102, v103\n"
"v_or3_b32 v96, v96, v98, v100\nv_or_b32 v96, v96, v102\nv_cmp_ne_u32 vcc, 0, v96\ns_nop 0\ns_cbranch_vccz .S188\ns_sleep 6\ns_sub_u32 s7, s7, 1\ns_cbranch_scc0 .S189\n"
"s_branch .S188\n.FF186:\nv_lshrrev_b32 v104, 9, v1\nv_mul_u32_u24 v104, 0x18000, v104\nv_lshlrev_b32 v104, 8, v104\nv_and_b32 v105, 0x1f0, v1\nv_add_u32 v104, v104, v105\n"
"s_mov_b32 s6, 0x15c0000\nbuffer_load_dword v96, v104, s[16:19], s6 offen offset:4 sc0 sc1\ns_mov_b32 s6, 0x15c2000\n"
"buffer_load_dword v97, v104, s[16:19], s6 offen offset:4 sc0 sc1\ns_mov_b32 s6, 0x45c0000\nbuffer_load_dword v98, v104, s[16:19], s6 offen offset:4 sc0 sc1\n"
"s_mov_b32 s6, 0x45c2000\nbuffer_load_dword v99, v104, s[16:19], s6 offen offset:4 sc0 sc1\ns_mov_b32 s6, 0x75c0000\n"
"buffer_load_dword v100, v104, s[16:19], s6 offen offset:4 sc0 sc1\ns_mov_b32 s6, 0x75c2000\nbuffer_load_dword v101, v104, s[16:19], s6 offen offset:4 sc0 sc1\n"
"s_mov_b32 s6, 0xa5c0000\nbuffer_load_dword v102, v104, s[16:19], s6 offen offset:4 sc0 sc1\ns_mov_b32 s6, 0xa5c2000\n"
"buffer_load_dword v103, v104, s[16:19], s6 offen offset:4 sc0 sc1\ns_mov_b32 s90, 0\ns_swappc_b64 s[8:9], s[80:81]\nv_xor_b32 v96, s15, v96\nv_xor_b32 v97, s15, v97\n"
"v_xor_b32 v98, s15, v98\nv_xor_b32 v99, s15, v99\nv_xor_b32 v100, s15, v100\nv_xor_b32 v101, s15, v101\nv_xor_b32 v102, s15, v102\nv_xor_b32 v103, s15, v103\n"
"v_min_u32 v96, v96, v97\nv_min_u32 v98, v98, v99\nv_min_u32 v100, v100, v101\nv_min_u32 v102, v102, v103\nv_or3_b32 v96, v96, v98, v100\nv_or_b32 v96, v96, v102\n"
"v_cmp_ne_u32 vcc, 0, v96\ns_nop 0\ns_cbranch_vccnz .S187\n.S188:\ns_and_b32 s4, s13, 1\ns_lshl1_add_u32 s4, s4, s11\ns_lshl2_add_u32 s4, s12, s4\ns_lshr_b32 s5, s71, 16\n"
"s_cmp_lt_u32 s4, 28\ns_cselect_b32 s5, s71, s5\ns_cselect_b32 s3, 0, 28\ns_and_b32 s5, s5, 0x7ff\ns_sub_u32 s3, s4, s3\ns_mul_i32 s2, s4, 7168\ns_add_u32 s2, s2, 0xc04c800\n"
"s_add_u32 s6, s2, 4096\nbuffer_load_dwordx4 v[96:99], v1, s[16:19], s2 offen offset:0 sc0 sc1\nbuffer_load_dwordx4 v[100:103], v1, s[16:19], s2 offen offset:1024 sc0 sc1\n"
"buffer_load_dwordx4 v[104:107], v1, s[16:19], s2 offen offset:2048 sc0 sc1\nbuffer_load_dwordx4 v[108:111], v1, s[16:19], s2 offen offset:3072 sc0 sc1\n"
"buffer_load_dwordx4 v[112:115], v1, s[16:19], s6 offen offset:0 sc0 sc1\nbuffer_load_dwordx4 v[116:119], v1, s[16:19], s6 offen offset:1024 sc0 sc1\n"
"buffer_load_dwordx4 v[120:123], v1, s[16:19], s6 offen offset:2048 sc0 sc1\nv_lshrrev_b32 v127, 1, v1\nv_and_b32 v127, 24, v127\ns_mul_i32 s2, s5, 896\ns_lshl_b32 s3, s3, 5\n"
"s_add_u32 s2, s2, s3\ns_lshl2_add_u32 s5, s5, 0xc03e000\nbuffer_load_dwordx2 v[124:125], v127, s[28:31], s2 offen\nbuffer_load_dword v126, off, s[16:19], s5 sc0 sc1\n"
"s_mov_b32 s90, 0\ns_swappc_b64 s[8:9], s[80:81]\nAD 96,96,100\nAD 97,97,101\nAD 98,98,102\nAD 99,99,103\nAD 104,104,108\nAD 105,105,109\nAD 106,106,110\nAD 107,107,111\n"
"AD 112,112,116\nAD 113,113,117\nAD 114,114,118\nAD 115,115,119\nAD 96,96,104\nAD 97,97,105\nAD 98,98,106\nAD 99,99,107\nAD 112,112,120\nAD 113,113,121\nAD 114,114,122\n"
"AD 115,115,123\nAD 96,96,112\nAD 97,97,113\nAD 98,98,114\nAD 99,99,115\nv_mov_b32 v100, v96\nv_mov_b32 v101, v97\nv_mov_b32 v102, v98\nv_mov_b32 v103, v99\n"
"v_permlane32_swap_b32 v96, v100\nv_permlane32_swap_b32 v97, v101\nv_permlane32_swap_b32 v98, v102\nv_permlane32_swap_b32 v99, v103\nAD 96,96,100\nAD 97,97,101\nAD 98,98,102\n"
"AD 99,99,103\nv_mov_b32 v100, v96\nv_mov_b32 v101, v97\nv_mov_b32 v102, v98\nv_mov_b32 v103, v99\nv_permlane16_swap_b32 v96, v100\nv_permlane16_swap_b32 v97, v101\n"
"v_permlane16_swap_b32 v98, v102\nv_permlane16_swap_b32 v99, v103\nAD 96,96,100\nAD 97,97,101\nAD 98,98,102\nAD 99,99,103\n"
"v_add_f32_dpp v96, v96, v96 row_ror:8 row_mask:0xf bank_mask:0xf\nv_add_f32_dpp v97, v97, v97 row_ror:8 row_mask:0xf bank_mask:0xf\n"
"v_add_f32_dpp v98, v98, v98 row_ror:8 row_mask:0xf bank_mask:0xf\nv_add_f32_dpp v99, v99, v99 row_ror:8 row_mask:0xf bank_mask:0xf\n"
"v_add_f32_dpp v96, v96, v96 row_ror:4 row_mask:0xf bank_mask:0xf\nv_add_f32_dpp v97, v97, v97 row_ror:4 row_mask:0xf bank_mask:0xf\n"
"v_add_f32_dpp v98, v98, v98 row_ror:4 row_mask:0xf bank_mask:0xf\nv_add_f32_dpp v99, v99, v99 row_ror:4 row_mask:0xf bank_mask:0xf\nv_mul_f32 v126, 0xbfb8aa3b, v126\nEX 126,126\n"
"CV 96,96,97\nv_add_f32 v126, 1.0, v126\nv_rcp_f32 v126, v126\nCV 98,98,99\nCV 126,126,126\nv_lshlrev_b32 v126, 16, v126\nv_and_b32 v97, 0xffff0000, v96\n"
"v_lshlrev_b32 v96, 16, v96\nv_and_b32 v99, 0xffff0000, v98\nv_lshlrev_b32 v98, 16, v98\nMU 96,96,126\nMU 97,97,126\nMU 98,98,126\nMU 99,99,126\nCV 96,96,97\nCV 98,98,99\n"
"v_and_b32 v97, 0xffff0000, v96\nv_lshlrev_b32 v96, 16, v96\nv_and_b32 v99, 0xffff0000, v98\nv_lshlrev_b32 v98, 16, v98\nv_lshlrev_b32 v100, 16, v124\n"
"v_and_b32 v101, 0xffff0000, v124\nv_lshlrev_b32 v102, 16, v125\nv_and_b32 v103, 0xffff0000, v125\nAD 96,96,100\nAD 97,97,101\nAD 98,98,102\nAD 99,99,103\nCV 96,96,97\nCV 97,98,99\n"
"v_mov_b32 v98, s15\nv_mov_b32 v99, s71\ns_and_b32 s4, s13, 1\ns_lshl1_add_u32 s4, s4, s11\ns_lshl2_add_u32 s4, s12, s4\ns_lshr_b32 s5, s71, 16\ns_cmp_lt_u32 s4, 28\n"
"s_cselect_b32 s5, s71, s5\ns_cselect_b32 s3, 0, 28\ns_and_b32 s5, s5, 0x7ff\ns_sub_u32 s3, s4, s3\nv_mov_b32 v100, 0x40000000\nv_cmp_gt_u32 vcc, 64, v1\ns_lshl_b32 s4, s4, 6\n"
"s_add_u32 s4, s4, 0xc04b800\nv_cndmask_b32 v100, v100, v1, vcc\nSv 96,100,16,4,0\ns_mov_b32 s90, 100\ns_swappc_b64 s[8:9], s[80:81]\n.T194:\ns_mov_b32 s62, 0\n"
"s_call_b64 s[80:81], .RB11\ns_branch .T195\n.T196:\ns_mov_b32 s62, 0\ns_call_b64 s[80:81], .RA10\ns_branch .T197\n.T201:\ns_mov_b32 s62, 0\ns_call_b64 s[80:81], .RB11\n"
"s_branch .T202\n.T203:\ns_mov_b32 s62, 0\ns_call_b64 s[80:81], .RA10\ns_branch .T204\n.T207:\ns_mov_b32 s62, 0\ns_call_b64 s[80:81], .RB11\ns_branch .T208\n.T209:\n"
"s_mov_b32 s62, 0\ns_call_b64 s[80:81], .RA10\ns_branch .T210\n.S215:\ns_mov_b32 s7, 262144\n.S217:\nbuffer_load_dwordx4 v[100:103], v108, s[16:19], s65 offen offset:0 sc0 sc1\n"
"buffer_load_dwordx4 v[104:107], v108, s[16:19], s65 offen offset:256 sc0 sc1\nWV 0\nv_xor_b32 v109, s15, v102\nv_xor_b32 v110, s15, v106\nv_or_b32 v109, v109, v110\n"
"v_cmp_ne_u32 vcc, 0, v109\ns_nop 0\ns_cbranch_vccz .S216\ns_sleep 6\ns_sub_u32 s7, s7, 1\ns_cbranch_scc0 .S217\ns_branch .S216\n.D213:\nv_mov_b32 v114, 0\nv_mov_b32 v115, 0\n"
"s_mov_b32 s63, 0xff800000\ns_mov_b32 s67, 1\ns_branch .D214\n.T219:\ns_lshl3_add_u32 s62, s67, 2\ns_call_b64 s[80:81], .RB11\ns_cmp_eq_u32 s67, 0\ns_cbranch_scc1 .T220\n"
"SU 20,100,88\nv_min_f32 v20, 0, v20\nEX 20,20\ns_nop 0\nFM 86,101,20\nSU 21,104,89\nv_min_f32 v21, 0, v21\nEX 21,21\ns_nop 0\nFM 87,105,21\nAR 22,0\n"
"v_and_b32 v23, 0xffff0000, v22\nv_lshlrev_b32 v22, 16, v22\nMU 22,22,20\nMU 23,23,20\nCV 22,22,23\nAW 0,22\nAR 22,1\nv_and_b32 v23, 0xffff0000, v22\nv_lshlrev_b32 v22, 16, v22\n"
"MU 22,22,20\nMU 23,23,20\nCV 22,22,23\nAW 1,22\nAR 22,2\nv_and_b32 v23, 0xffff0000, v22\nv_lshlrev_b32 v22, 16, v22\nMU 22,22,20\nMU 23,23,20\nCV 22,22,23\nAW 2,22\nAR 22,3\n"
"v_and_b32 v23, 0xffff0000, v22\nv_lshlrev_b32 v22, 16, v22\nMU 22,22,20\nMU 23,23,20\nCV 22,22,23\nAW 3,22\nAR 22,4\nv_and_b32 v23, 0xffff0000, v22\nv_lshlrev_b32 v22, 16, v22\n"
"MU 22,22,21\nMU 23,23,21\nCV 22,22,23\nAW 4,22\nAR 22,5\nv_and_b32 v23, 0xffff0000, v22\nv_lshlrev_b32 v22, 16, v22\nMU 22,22,21\nMU 23,23,21\nCV 22,22,23\nAW 5,22\nAR 22,6\n"
"v_and_b32 v23, 0xffff0000, v22\nv_lshlrev_b32 v22, 16, v22\nMU 22,22,21\nMU 23,23,21\nCV 22,22,23\nAW 6,22\nAR 22,7\nv_and_b32 v23, 0xffff0000, v22\nv_lshlrev_b32 v22, 16, v22\n"
"MU 22,22,21\nMU 23,23,21\nCV 22,22,23\nAW 7,22\nAR 22,8\nv_and_b32 v23, 0xffff0000, v22\nv_lshlrev_b32 v22, 16, v22\nMU 22,22,20\nMU 23,23,20\nCV 22,22,23\nAW 8,22\nAR 22,9\n"
"v_and_b32 v23, 0xffff0000, v22\nv_lshlrev_b32 v22, 16, v22\nMU 22,22,20\nMU 23,23,20\nCV 22,22,23\nAW 9,22\nAR 22,10\nv_and_b32 v23, 0xffff0000, v22\nv_lshlrev_b32 v22, 16, v22\n"
"MU 22,22,20\nMU 23,23,20\nCV 22,22,23\nAW 10,22\nAR 22,11\nv_and_b32 v23, 0xffff0000, v22\nv_lshlrev_b32 v22, 16, v22\nMU 22,22,20\nMU 23,23,20\nCV 22,22,23\nAW 11,22\nAR 22,12\n"
"v_and_b32 v23, 0xffff0000, v22\nv_lshlrev_b32 v22, 16, v22\nMU 22,22,21\nMU 23,23,21\nCV 22,22,23\nAW 12,22\nAR 22,13\nv_and_b32 v23, 0xffff0000, v22\nv_lshlrev_b32 v22, 16, v22\n"
"MU 22,22,21\nMU 23,23,21\nCV 22,22,23\nAW 13,22\nAR 22,14\nv_and_b32 v23, 0xffff0000, v22\nv_lshlrev_b32 v22, 16, v22\nMU 22,22,21\nMU 23,23,21\nCV 22,22,23\nAW 14,22\nAR 22,15\n"
"v_and_b32 v23, 0xffff0000, v22\nv_lshlrev_b32 v22, 16, v22\nMU 22,22,21\nMU 23,23,21\nCV 22,22,23\nAW 15,22\nAR 22,16\nv_and_b32 v23, 0xffff0000, v22\nv_lshlrev_b32 v22, 16, v22\n"
"MU 22,22,20\nMU 23,23,20\nCV 22,22,23\nAW 16,22\nAR 22,17\nv_and_b32 v23, 0xffff0000, v22\nv_lshlrev_b32 v22, 16, v22\nMU 22,22,20\nMU 23,23,20\nCV 22,22,23\nAW 17,22\nAR 22,18\n"
"v_and_b32 v23, 0xffff0000, v22\nv_lshlrev_b32 v22, 16, v22\nMU 22,22,20\nMU 23,23,20\nCV 22,22,23\nAW 18,22\nAR 22,19\nv_and_b32 v23, 0xffff0000, v22\nv_lshlrev_b32 v22, 16, v22\n"
"MU 22,22,20\nMU 23,23,20\nCV 22,22,23\nAW 19,22\nAR 22,20\nv_and_b32 v23, 0xffff0000, v22\nv_lshlrev_b32 v22, 16, v22\nMU 22,22,21\nMU 23,23,21\nCV 22,22,23\nAW 20,22\nAR 22,21\n"
"v_and_b32 v23, 0xffff0000, v22\nv_lshlrev_b32 v22, 16, v22\nMU 22,22,21\nMU 23,23,21\nCV 22,22,23\nAW 21,22\nAR 22,22\nv_and_b32 v23, 0xffff0000, v22\nv_lshlrev_b32 v22, 16, v22\n"
"MU 22,22,21\nMU 23,23,21\nCV 22,22,23\nAW 22,22\nAR 22,23\nv_and_b32 v23, 0xffff0000, v22\nv_lshlrev_b32 v22, 16, v22\nMU 22,22,21\nMU 23,23,21\nCV 22,22,23\nAW 23,22\nAR 22,24\n"
"v_and_b32 v23, 0xffff0000, v22\nv_lshlrev_b32 v22, 16, v22\nMU 22,22,20\nMU 23,23,20\nCV 22,22,23\nAW 24,22\nAR 22,25\nv_and_b32 v23, 0xffff0000, v22\nv_lshlrev_b32 v22, 16, v22\n"
"MU 22,22,20\nMU 23,23,20\nCV 22,22,23\nAW 25,22\nAR 22,26\nv_and_b32 v23, 0xffff0000, v22\nv_lshlrev_b32 v22, 16, v22\nMU 22,22,20\nMU 23,23,20\nCV 22,22,23\nAW 26,22\nAR 22,27\n"
"v_and_b32 v23, 0xffff0000, v22\nv_lshlrev_b32 v22, 16, v22\nMU 22,22,20\nMU 23,23,20\nCV 22,22,23\nAW 27,22\nAR 22,28\nv_and_b32 v23, 0xffff0000, v22\nv_lshlrev_b32 v22, 16, v22\n"
"MU 22,22,21\nMU 23,23,21\nCV 22,22,23\nAW 28,22\nAR 22,29\nv_and_b32 v23, 0xffff0000, v22\nv_lshlrev_b32 v22, 16, v22\nMU 22,22,21\nMU 23,23,21\nCV 22,22,23\nAW 29,22\nAR 22,30\n"
"v_and_b32 v23, 0xffff0000, v22\nv_lshlrev_b32 v22, 16, v22\nMU 22,22,21\nMU 23,23,21\nCV 22,22,23\nAW 30,22\nAR 22,31\nv_and_b32 v23, 0xffff0000, v22\nv_lshlrev_b32 v22, 16, v22\n"
"MU 22,22,21\nMU 23,23,21\nCV 22,22,23\nAW 31,22\nAR 22,32\nv_and_b32 v23, 0xffff0000, v22\nv_lshlrev_b32 v22, 16, v22\nMU 22,22,20\nMU 23,23,20\nCV 22,22,23\nAW 32,22\nAR 22,33\n"
"v_and_b32 v23, 0xffff0000, v22\nv_lshlrev_b32 v22, 16, v22\nMU 22,22,20\nMU 23,23,20\nCV 22,22,23\nAW 33,22\nAR 22,34\nv_and_b32 v23, 0xffff0000, v22\nv_lshlrev_b32 v22, 16, v22\n"
"MU 22,22,20\nMU 23,23,20\nCV 22,22,23\nAW 34,22\nAR 22,35\nv_and_b32 v23, 0xffff0000, v22\nv_lshlrev_b32 v22, 16, v22\nMU 22,22,20\nMU 23,23,20\nCV 22,22,23\nAW 35,22\nAR 22,36\n"
"v_and_b32 v23, 0xffff0000, v22\nv_lshlrev_b32 v22, 16, v22\nMU 22,22,21\nMU 23,23,21\nCV 22,22,23\nAW 36,22\nAR 22,37\nv_and_b32 v23, 0xffff0000, v22\nv_lshlrev_b32 v22, 16, v22\n"
"MU 22,22,21\nMU 23,23,21\nCV 22,22,23\nAW 37,22\nAR 22,38\nv_and_b32 v23, 0xffff0000, v22\nv_lshlrev_b32 v22, 16, v22\nMU 22,22,21\nMU 23,23,21\nCV 22,22,23\nAW 38,22\nAR 22,39\n"
"v_and_b32 v23, 0xffff0000, v22\nv_lshlrev_b32 v22, 16, v22\nMU 22,22,21\nMU 23,23,21\nCV 22,22,23\nAW 39,22\nAR 22,40\nv_and_b32 v23, 0xffff0000, v22\nv_lshlrev_b32 v22, 16, v22\n"
"MU 22,22,20\nMU 23,23,20\nCV 22,22,23\nAW 40,22\nAR 22,41\nv_and_b32 v23, 0xffff0000, v22\nv_lshlrev_b32 v22, 16, v22\nMU 22,22,20\nMU 23,23,20\nCV 22,22,23\nAW 41,22\nAR 22,42\n"
"v_and_b32 v23, 0xffff0000, v22\nv_lshlrev_b32 v22, 16, v22\nMU 22,22,20\nMU 23,23,20\nCV 22,22,23\nAW 42,22\nAR 22,43\nv_and_b32 v23, 0xffff0000, v22\nv_lshlrev_b32 v22, 16, v22\n"
"MU 22,22,20\nMU 23,23,20\nCV 22,22,23\nAW 43,22\nAR 22,44\nv_and_b32 v23, 0xffff0000, v22\nv_lshlrev_b32 v22, 16, v22\nMU 22,22,21\nMU 23,23,21\nCV 22,22,23\nAW 44,22\nAR 22,45\n"
"v_and_b32 v23, 0xffff0000, v22\nv_lshlrev_b32 v22, 16, v22\nMU 22,22,21\nMU 23,23,21\nCV 22,22,23\nAW 45,22\nAR 22,46\nv_and_b32 v23, 0xffff0000, v22\nv_lshlrev_b32 v22, 16, v22\n"
"MU 22,22,21\nMU 23,23,21\nCV 22,22,23\nAW 46,22\nAR 22,47\nv_and_b32 v23, 0xffff0000, v22\nv_lshlrev_b32 v22, 16, v22\nMU 22,22,21\nMU 23,23,21\nCV 22,22,23\nAW 47,22\nAR 22,48\n"
"v_and_b32 v23, 0xffff0000, v22\nv_lshlrev_b32 v22, 16, v22\nMU 22,22,20\nMU 23,23,20\nCV 22,22,23\nAW 48,22\nAR 22,49\nv_and_b32 v23, 0xffff0000, v22\nv_lshlrev_b32 v22, 16, v22\n"
"MU 22,22,20\nMU 23,23,20\nCV 22,22,23\nAW 49,22\nAR 22,50\nv_and_b32 v23, 0xffff0000, v22\nv_lshlrev_b32 v22, 16, v22\nMU 22,22,20\nMU 23,23,20\nCV 22,22,23\nAW 50,22\nAR 22,51\n"
"v_and_b32 v23, 0xffff0000, v22\nv_lshlrev_b32 v22, 16, v22\nMU 22,22,20\nMU 23,23,20\nCV 22,22,23\nAW 51,22\nAR 22,52\nv_and_b32 v23, 0xffff0000, v22\nv_lshlrev_b32 v22, 16, v22\n"
"MU 22,22,21\nMU 23,23,21\nCV 22,22,23\nAW 52,22\nAR 22,53\nv_and_b32 v23, 0xffff0000, v22\nv_lshlrev_b32 v22, 16, v22\nMU 22,22,21\nMU 23,23,21\nCV 22,22,23\nAW 53,22\nAR 22,54\n"
"v_and_b32 v23, 0xffff0000, v22\nv_lshlrev_b32 v22, 16, v22\nMU 22,22,21\nMU 23,23,21\nCV 22,22,23\nAW 54,22\nAR 22,55\nv_and_b32 v23, 0xffff0000, v22\nv_lshlrev_b32 v22, 16, v22\n"
"MU 22,22,21\nMU 23,23,21\nCV 22,22,23\nAW 55,22\nAR 22,56\nv_and_b32 v23, 0xffff0000, v22\nv_lshlrev_b32 v22, 16, v22\nMU 22,22,20\nMU 23,23,20\nCV 22,22,23\nAW 56,22\nAR 22,57\n"
"v_and_b32 v23, 0xffff0000, v22\nv_lshlrev_b32 v22, 16, v22\nMU 22,22,20\nMU 23,23,20\nCV 22,22,23\nAW 57,22\nAR 22,58\nv_and_b32 v23, 0xffff0000, v22\nv_lshlrev_b32 v22, 16, v22\n"
"MU 22,22,20\nMU 23,23,20\nCV 22,22,23\nAW 58,22\nAR 22,59\nv_and_b32 v23, 0xffff0000, v22\nv_lshlrev_b32 v22, 16, v22\nMU 22,22,20\nMU 23,23,20\nCV 22,22,23\nAW 59,22\nAR 22,60\n"
"v_and_b32 v23, 0xffff0000, v22\nv_lshlrev_b32 v22, 16, v22\nMU 22,22,21\nMU 23,23,21\nCV 22,22,23\nAW 60,22\nAR 22,61\nv_and_b32 v23, 0xffff0000, v22\nv_lshlrev_b32 v22, 16, v22\n"
"MU 22,22,21\nMU 23,23,21\nCV 22,22,23\nAW 61,22\nAR 22,62\nv_and_b32 v23, 0xffff0000, v22\nv_lshlrev_b32 v22, 16, v22\nMU 22,22,21\nMU 23,23,21\nCV 22,22,23\nAW 62,22\nAR 22,63\n"
"v_and_b32 v23, 0xffff0000, v22\nv_lshlrev_b32 v22, 16, v22\nMU 22,22,21\nMU 23,23,21\nCV 22,22,23\nAW 63,22\nv_lshrrev_b32 v22, 8, v1\nv_bfe_u32 v23, v1, 6, 2\n"
"v_cmp_eq_u32 vcc, v22, v23\nv_and_b32 v22, 16, v1\nv_mov_b32 v23, 0x3f80\nv_cndmask_b32 v23, 0, v23, vcc\nv_lshlrev_b32 v23, v22, v23\nv_and_b32 v22, 32, v1\n"
"v_cmp_eq_u32 vcc, 0, v22\nv_mov_b32 v112, 0\nv_mov_b32 v113, 0\nv_cndmask_b32 v114, 0, v23, vcc\nv_sub_u32 v115, v23, v114\nv_mov_b32 v116, 0\nv_mov_b32 v117, 0\ns_nop 1\n"
"Mvva 128,114,0\nMvva 136,112,0\nMvva 140,112,4\nMvva 132,114,4\nMvva 144,114,8\nMvva 152,112,8\nMvva 156,112,12\nMvva 148,114,12\nMvva 160,114,16\nMvva 168,112,16\n"
"Mvva 172,112,20\nMvva 164,114,20\nMvva 176,114,24\nMvva 184,112,24\nMvva 188,112,28\nMvva 180,114,28\nMvva 192,114,32\nMvva 200,112,32\nMvva 204,112,36\nMvva 196,114,36\n"
"Mvva 208,114,40\nMvva 216,112,40\nMvva 220,112,44\nMvva 212,114,44\nMvva 224,114,48\nMvva 232,112,48\nMvva 236,112,52\nMvva 228,114,52\nMvva 240,114,56\nMvva 248,112,56\n"
"Mvva 252,112,60\nMvva 244,114,60\ns_nop 7\ns_branch .T220\n.S222:\ns_mov_b32 s7, 262144\n.S224:\ns_add_u32 s0, s92, 0\n"
"buffer_load_dwordx4 v[208:211], v1, s[16:19], s0 offen offset:0 sc0 sc1\nbuffer_load_dwordx4 v[212:215], v1, s[16:19], s0 offen offset:1024 sc0 sc1\nWV 0\n"
"v_max_u32 v21, v211, v215\nv_cmp_eq_u32 vcc, -1, v21\ns_nop 0\ns_cbranch_vccz .S223\ns_sleep 6\ns_sub_u32 s7, s7, 1\ns_cbranch_scc0 .S224\ns_branch .S223\n.S225:\n"
"s_mov_b32 s7, 262144\n.S227:\ns_add_u32 s0, s92, 131072\nbuffer_load_dwordx4 v[216:219], v1, s[16:19], s0 offen offset:0 sc0 sc1\n"
"buffer_load_dwordx4 v[220:223], v1, s[16:19], s0 offen offset:1024 sc0 sc1\nWV 0\nv_max_u32 v21, v219, v223\nv_cmp_eq_u32 vcc, -1, v21\ns_nop 0\ns_cbranch_vccz .S226\ns_sleep 6\n"
"s_sub_u32 s7, s7, 1\ns_cbranch_scc0 .S227\ns_branch .S226\n.S228:\ns_mov_b32 s7, 262144\n.S230:\ns_add_u32 s0, s92, 262144\n"
"buffer_load_dwordx4 v[224:227], v1, s[16:19], s0 offen offset:0 sc0 sc1\nbuffer_load_dwordx4 v[228:231], v1, s[16:19], s0 offen offset:1024 sc0 sc1\nWV 0\n"
"v_max_u32 v21, v227, v231\nv_cmp_eq_u32 vcc, -1, v21\ns_nop 0\ns_cbranch_vccz .S229\ns_sleep 6\ns_sub_u32 s7, s7, 1\ns_cbranch_scc0 .S230\ns_branch .S229\n.S231:\n"
"s_mov_b32 s7, 262144\n.S233:\ns_add_u32 s0, s92, 393216\nbuffer_load_dwordx4 v[208:211], v1, s[16:19], s0 offen offset:0 sc0 sc1\n"
"buffer_load_dwordx4 v[212:215], v1, s[16:19], s0 offen offset:1024 sc0 sc1\nWV 0\nv_max_u32 v21, v211, v215\nv_cmp_eq_u32 vcc, -1, v21\ns_nop 0\ns_cbranch_vccz .S232\ns_sleep 6\n"
"s_sub_u32 s7, s7, 1\ns_cbranch_scc0 .S233\ns_branch .S232\n.S234:\ns_mov_b32 s7, 262144\n.S236:\ns_add_u32 s0, s92, 524288\n"
"buffer_load_dwordx4 v[216:219], v1, s[16:19], s0 offen offset:0 sc0 sc1\nbuffer_load_dwordx4 v[220:223], v1, s[16:19], s0 offen offset:1024 sc0 sc1\nWV 0\n"
"v_max_u32 v21, v219, v223\nv_cmp_eq_u32 vcc, -1, v21\ns_nop 0\ns_cbranch_vccz .S235\ns_sleep 6\ns_sub_u32 s7, s7, 1\ns_cbranch_scc0 .S236\ns_branch .S235\n.S237:\n"
"s_mov_b32 s7, 262144\n.S239:\ns_add_u32 s0, s92, 655360\nbuffer_load_dwordx4 v[224:227], v1, s[16:19], s0 offen offset:0 sc0 sc1\n"
"buffer_load_dwordx4 v[228:231], v1, s[16:19], s0 offen offset:1024 sc0 sc1\nWV 0\nv_max_u32 v21, v227, v231\nv_cmp_eq_u32 vcc, -1, v21\ns_nop 0\ns_cbranch_vccz .S238\ns_sleep 6\n"
"s_sub_u32 s7, s7, 1\ns_cbranch_scc0 .S239\ns_branch .S238\n.S240:\ns_mov_b32 s7, 262144\n.S242:\ns_add_u32 s0, s92, 786432\n"
"buffer_load_dwordx4 v[208:211], v1, s[16:19], s0 offen offset:0 sc0 sc1\nbuffer_load_dwordx4 v[212:215], v1, s[16:19], s0 offen offset:1024 sc0 sc1\nWV 0\n"
"v_max_u32 v21, v211, v215\nv_cmp_eq_u32 vcc, -1, v21\ns_nop 0\ns_cbranch_vccz .S241\ns_sleep 6\ns_sub_u32 s7, s7, 1\ns_cbranch_scc0 .S242\ns_branch .S241\n.S243:\n"
"s_mov_b32 s7, 262144\n.S245:\nbuffer_load_dwordx4 v[64:67], v89, s[16:19], s40 offen offset:0 sc0 sc1\nbuffer_load_dwordx4 v[68:71], v89, s[16:19], s40 offen offset:16 sc0 sc1\n"
"buffer_load_dwordx4 v[72:75], v89, s[16:19], s40 offen offset:1792 sc0 sc1\nbuffer_load_dwordx4 v[76:79], v89, s[16:19], s40 offen offset:1808 sc0 sc1\n"
"buffer_load_dwordx2 v[80:81], v88, s[16:19], s63 offen offset:0 sc0 sc1\nbuffer_load_dwordx2 v[82:83], v88, s[16:19], s63 offen offset:512 sc0 sc1\nWV 0\nv_xor_b32 v66, s15, v66\n"
"v_xor_b32 v70, s15, v70\nv_xor_b32 v74, s15, v74\nv_xor_b32 v78, s15, v78\nv_xor_b32 v81, s15, v81\nv_xor_b32 v83, s15, v83\nv_or3_b32 v92, v66, v70, v74\n"
"v_or3_b32 v92, v92, v78, v81\nv_or_b32 v92, v92, v83\nv_cmp_ne_u32 vcc, 0, v92\ns_nop 0\ns_cbranch_vccz .S244\ns_sleep 6\ns_sub_u32 s7, s7, 1\ns_cbranch_scc0 .S245\n"
"s_branch .S244\n.S246:\ns_mov_b32 s7, 262144\n.S248:\nbuffer_load_dword v27, off, s[16:19], s60 offset:512 sc0 sc1\nWV 0\nv_cmp_ne_u32 vcc, s15, v27\ns_nop 0\n"
"s_cbranch_vccz .S247\ns_sleep 6\ns_sub_u32 s7, s7, 1\ns_cbranch_scc0 .S248\ns_branch .S247\n.S249:\ns_mov_b32 s7, 262144\n.S251:\n"
"buffer_load_dword v27, v24, s[16:19], s41 offen sc0 sc1\nWV 0\nv_xor_b32 v27, s15, v27\nv_and_b32 v26, 0xffffdfff, v27\nv_cmp_ne_u32 s[64:65], 0, v27\nv_cmp_ne_u32 vcc, 0, v26\n"
"s_nop 0\ns_cbranch_vccz .S250\ns_sleep 6\ns_sub_u32 s7, s7, 1\ns_cbranch_scc0 .S251\ns_branch .S250\n.Z253:\ns_sub_u32 s7, s7, 1\ns_cbranch_scc1 .Z254\ns_cmp_lg_u64 s[44:45], 0\n"
"s_cbranch_scc0 .Z256\nbuffer_load_dwordx4 v[96:99], v1, s[16:19], s24 offen offset:0 sc0 sc1\nbuffer_load_dwordx4 v[100:103], v1, s[16:19], s24 offen offset:1024 sc0 sc1\n.Z256:\n"
"s_cmp_lg_u64 s[46:47], 0\ns_cbranch_scc0 .Z257\nbuffer_load_dwordx4 v[104:107], v1, s[16:19], s25 offen offset:0 sc0 sc1\n"
"buffer_load_dwordx4 v[108:111], v1, s[16:19], s25 offen offset:1024 sc0 sc1\n.Z257:\ns_cmp_lg_u64 s[48:49], 0\ns_cbranch_scc0 .Z258\n"
"buffer_load_dwordx4 v[112:115], v1, s[16:19], s26 offen offset:0 sc0 sc1\nbuffer_load_dwordx4 v[116:119], v1, s[16:19], s26 offen offset:1024 sc0 sc1\n.Z258:\n"
"s_cmp_lg_u64 s[50:51], 0\ns_cbranch_scc0 .Z259\nbuffer_load_dwordx4 v[120:123], v1, s[16:19], s27 offen offset:0 sc0 sc1\n"
"buffer_load_dwordx4 v[124:127], v1, s[16:19], s27 offen offset:1024 sc0 sc1\n.Z259:\ns_cmp_lg_u64 s[52:53], 0\ns_cbranch_scc0 .Z260\n"
"buffer_load_dwordx4 v[32:35], v1, s[16:19], s32 offen offset:0 sc0 sc1\nbuffer_load_dwordx4 v[36:39], v1, s[16:19], s32 offen offset:1024 sc0 sc1\n.Z260:\n"
"s_cmp_lg_u64 s[54:55], 0\ns_cbranch_scc0 .Z261\nbuffer_load_dwordx4 v[40:43], v1, s[16:19], s33 offen offset:0 sc0 sc1\n"
"buffer_load_dwordx4 v[44:47], v1, s[16:19], s33 offen offset:1024 sc0 sc1\n.Z261:\ns_cmp_lg_u64 s[56:57], 0\ns_cbranch_scc0 .Z262\n"
"buffer_load_dwordx4 v[48:51], v1, s[16:19], s34 offen offset:0 sc0 sc1\nbuffer_load_dwordx4 v[52:55], v1, s[16:19], s34 offen offset:1024 sc0 sc1\n.Z262:\n"
"s_cmp_lg_u64 s[58:59], 0\ns_cbranch_scc0 .Z263\nbuffer_load_dwordx4 v[56:59], v1, s[16:19], s35 offen offset:0 sc0 sc1\n"
"buffer_load_dwordx4 v[60:63], v1, s[16:19], s35 offen offset:1024 sc0 sc1\n.Z263:\ns_sleep 8\nWV 0\ns_branch .Z255\n.p2align 12\n.PA4:\ns_add_u32 m0, s47, -49152\n"
"s_add_u32 s48, s48, 128\nD0 2,20,48,0\nD0 3,20,48,1024\nD0 4,20,48,2048\nD0 5,20,48,3072\ns_and_b32 s86, s74, 15\ns_lshl2_add_u32 s86, s86, s11\ns_mov_b32 s88, 0\n"
"s_lshl_b32 s93, s73, 9\ns_lshl3_add_u32 s93, s86, s93\ns_add_u32 s93, s93, 0xc049400\ns_lshl_b32 s86, s86, 14\ns_mul_i32 s91, s73, 25165824\ns_add_u32 s86, s86, s91\n"
"s_and_b32 s92, s13, 1\ns_lshl_b32 s92, s92, 13\ns_add_u32 s86, s86, s92\ns_add_u32 s94, s86, 0x1480000\ns_xor_b32 s95, s94, 0x2000\ns_add_u32 s86, s94, 512\n"
"s_load_dword s87, s[54:55], s86\ns_add_u32 m0, s47, -32768\ns_add_u32 s45, s45, 128\nD0 2,20,45,0\nD0 3,20,45,1024\nD0 4,20,45,2048\nD0 5,20,45,3072\nv_mov_b32 v52, 0x40000000\n"
"v_cmp_gt_u32 vcc, 56, v0\ns_lshl2_add_u32 s2, s10, s11\ns_mul_i32 s2, s2, 1792\nv_cndmask_b32 v53, v52, v1, vcc\ns_mov_b32 s36, s56\ns_and_b32 s37, s57, 0xffff\n"
"s_mov_b32 s38, 0x380\ns_mov_b32 s39, 0x20000\ns_mov_b32 s40, s58\ns_and_b32 s41, s59, 0xffff\ns_mov_b32 s42, 0x380\ns_mov_b32 s43, 0x20000\n"
"buffer_load_dwordx4 v[32:35], v53, s[28:31], s2 offen offset:0\nbuffer_load_dwordx4 v[36:39], v53, s[28:31], s2 offen offset:896\n"
"buffer_load_dwordx4 v[40:43], v53, s[36:39], 0 offen offset:0\nbuffer_load_dwordx4 v[44:47], v53, s[40:43], 0 offen offset:0\nWL 0\ns_mul_i32 s15, s15, 1664525\n"
"s_add_u32 s15, s15, 1013904223\nv_mov_b32 v25, s15\ns_mov_b32 s57, 0x3db8aa3b\ns_and_b32 s59, s11, 1\ns_lshl_b32 s59, s59, 13\ns_mul_i32 s2, s73, 25165824\ns_and_b32 s3, s12, 1\n"
"s_lshl_b32 s3, s3, 21\ns_add_u32 s2, s2, s3\ns_lshl2_add_u32 s3, s13, s11\ns_lshl_b32 s3, s3, 15\ns_add_u32 s2, s2, s3\ns_add_u32 s56, s2, 0x800000\ns_xor_b32 s53, s56, 0xc000\n"
"buffer_load_dword v31, v24, s[24:27], s96 offen\ns_add_u32 s96, s96, 0x80000\nbuffer_load_dword v31, v24, s[24:27], s96 offen\ns_add_u32 s96, s96, 0x80000\ns_mov_b64 exec, 0\n"
"s_cmp_eq_u32 s11, 1\ns_cbranch_scc1 .W568\ns_cmp_eq_u32 s11, 2\ns_cbranch_scc1 .W569\ns_cmp_eq_u32 s11, 3\ns_cbranch_scc1 .W570\ns_branch .W567\n.WB273:\ns_mov_b64 exec, -1\n"
"WV 14\ns_barrier\nRa 128,10,0\nRa 132,11,0\nRa 136,10,2048\nRa 140,11,2048\nRa 144,10,8192\nRa 148,11,8192\nRa 152,10,10240\nRa 156,11,10240\nTa 160,12,0\nTa 162,12,2048\n"
"Ta 164,12,16384\nTa 166,12,18432\nTa 168,13,0\nTa 170,13,2048\nTa 172,13,16384\nTa 174,13,18432\nTa 176,14,0\nTa 178,14,2048\nTa 180,14,16384\nTa 182,14,18432\nTa 184,15,0\n"
"Ta 186,15,2048\nTa 188,15,16384\nTa 190,15,18432\nWL 0\ns_barrier\nZvaa 64,160,128\nTa 224,16,0\ns_add_u32 m0, s47, 32768\ns_add_u32 s44, s44, 0x40000\nMvaa 64,164,132\n"
"Ta 226,16,2048\nZvaa 96,164,140\nD0 6,24,44,0\nMvaa 96,160,136\nTa 228,16,16384\nZvaa 128,160,144\nTa 230,16,18432\nMvaa 128,164,148\ns_mov_b32 s36, s60\nZvaa 160,164,156\n"
"Ta 232,17,0\nD0 7,24,44,1024\nMvaa 160,160,152\nTa 234,17,2048\nZvaa 164,168,152\ns_and_b32 s37, s61, 0xffff\nMvaa 164,172,156\nTa 236,17,16384\nZvaa 132,172,148\nTa 238,17,18432\n"
"D0 8,24,44,2048\nMvaa 132,168,144\ns_mov_b32 s38, 0x380000\nZvaa 100,168,136\nTa 240,18,0\nMvaa 100,172,140\nTa 242,18,2048\nZvaa 68,172,132\nD0 9,24,44,3072\nMvaa 68,168,128\n"
"Ta 244,18,16384\nZvaa 72,176,128\nTa 246,18,18432\ns_add_u32 m0, s47, 49152\ns_add_u32 s44, s44, 0x40000\nMvaa 72,180,132\ns_mov_b32 s39, 0x20000\nZvaa 104,180,140\nTa 248,19,0\n"
"D0 6,24,44,0\nMvaa 104,176,136\nTa 250,19,2048\nZvaa 136,176,144\ns_lshl3_add_u32 s75, s12, s13\nMvaa 136,180,148\nTa 252,19,16384\nZvaa 168,180,156\nTa 254,19,18432\n"
"D0 7,24,44,1024\nMvaa 168,176,152\ns_lshl2_add_u32 s75, s75, s11\nZvaa 172,184,152\ns_mul_i32 s75, s75, 7168\nMvaa 172,188,156\ns_mul_i32 s76, s11, 7168\nZvaa 140,188,148\n"
"D0 8,24,44,2048\nMvaa 140,184,144\ns_add_u32 s76, s76, 131072\nZvaa 108,184,136\ns_mov_b32 s42, 1605632\nMvaa 108,188,140\nZvaa 76,188,132\nD0 9,24,44,3072\nMvaa 76,184,128\n"
"s_waitcnt vmcnt(14) lgkmcnt(0)\ns_barrier\nZvaa 80,224,128\nRa 192,10,16384\ns_add_u32 m0, s47, -16384\ns_add_u32 s48, s48, 128\nMvaa 80,228,132\nZvaa 112,228,140\n"
"Ra 196,11,16384\nD0 2,20,48,0\nMvaa 112,224,136\nZvaa 144,224,144\nRa 200,10,18432\nMvaa 144,228,148\nZvaa 176,228,156\nRa 204,11,18432\nD0 3,20,48,1024\nMvaa 176,224,152\n"
"Zvaa 180,232,152\nRa 208,10,24576\nMvaa 180,236,156\ns_and_b32 s78, s74, 3\ns_lshl2_add_u32 s78, s73, s78\ns_lshl_b32 s78, s78, 12\ns_bitcmp1_b32 s11, 0\n"
"s_cselect_b32 s79, s48, s45\ns_add_u32 s79, s79, 512\ns_and_b32 s79, s79, 0xfffe0000\ns_add_u32 s78, s78, s79\nbuffer_load_dword v31, v26, s[20:23], s78 offen\nZvaa 148,236,148\n"
"Ra 212,11,24576\nD0 4,20,48,2048\nMvaa 148,232,144\nZvaa 116,232,136\nRa 216,10,26624\nMvaa 116,236,140\nZvaa 84,236,132\nRa 220,11,26624\nD0 5,20,48,3072\nMvaa 84,232,128\n"
"Zvaa 88,240,128\ns_add_u32 m0, s47, -65536\ns_add_u32 s45, s45, 128\nMvaa 88,244,132\nZvaa 120,244,140\nD0 2,20,45,0\nMvaa 120,240,136\nZvaa 152,240,144\nMvaa 152,244,148\n"
"Zvaa 184,244,156\nD0 3,20,45,1024\nMvaa 184,240,152\nZvaa 188,248,152\nMvaa 188,252,156\nZvaa 156,252,148\nD0 4,20,45,2048\nMvaa 156,248,144\nZvaa 124,248,136\nMvaa 124,252,140\n"
"Zvaa 92,252,132\nD0 5,20,45,3072\nMvaa 92,248,128\nWL 0\nZaaa 16,224,192\nRa 128,10,32768\ns_add_u32 m0, s47, 0\ns_add_u32 s44, s44, 0x40000\nMaaa 16,228,196\nZaaa 48,228,204\n"
"Ra 132,11,32768\nD0 6,24,44,0\nMaaa 48,224,200\nZaaa 80,224,208\nRa 136,10,34816\nMaaa 80,228,212\nZaaa 112,228,220\nRa 140,11,34816\nD0 7,24,44,1024\nMaaa 112,224,216\n"
"Zaaa 116,232,216\nRa 144,10,40960\nMaaa 116,236,220\nZaaa 84,236,212\nRa 148,11,40960\nD0 8,24,44,2048\nMaaa 84,232,208\nZaaa 52,232,200\nRa 152,10,43008\nMaaa 52,236,204\n"
"Zaaa 20,236,196\nRa 156,11,43008\nD0 9,24,44,3072\nMaaa 20,232,192\nZaaa 24,240,192\ns_add_u32 m0, s47, 16384\ns_add_u32 s44, s44, 0x40000\nMaaa 24,244,196\nZaaa 56,244,204\n"
"D0 6,24,44,0\nMaaa 56,240,200\nZaaa 88,240,208\nMaaa 88,244,212\nZaaa 120,244,220\nD0 7,24,44,1024\nMaaa 120,240,216\nZaaa 124,248,216\nMaaa 124,252,220\nZaaa 92,252,212\n"
"D0 8,24,44,2048\nMaaa 92,248,208\nZaaa 60,248,200\nMaaa 60,252,204\nZaaa 28,252,196\nD0 9,24,44,3072\nMaaa 28,248,192\ns_waitcnt vmcnt(17) lgkmcnt(0)\ns_barrier\nZaaa 0,160,192\n"
"Ta 224,16,32768\ns_add_u32 m0, s47, -49152\ns_add_u32 s48, s48, 128\nMaaa 0,164,196\nTa 226,16,34816\nZaaa 32,164,204\nD0 2,20,48,0\nMaaa 32,160,200\nTa 228,16,49152\n"
"Zaaa 64,160,208\nTa 230,16,51200\nbuffer_load_dword v31, v24, s[24:27], s96 offen\ns_add_u32 s96, s96, 0x80000\nMaaa 64,164,212\nv_lshlrev_b32 v48, 16, v32\n"
"v_lshlrev_b32 v49, 16, v36\nZaaa 96,164,220\nTa 232,17,32768\nD0 3,20,48,1024\nMaaa 96,160,216\nTa 234,17,34816\nv_lshlrev_b32 v50, 16, v40\nZaaa 100,168,216\n"
"v_lshlrev_b32 v51, 16, v44\nMU 52,48,50\nMaaa 100,172,220\nTa 236,17,49152\nZaaa 68,172,212\nTa 238,17,51200\nD0 4,20,48,2048\nMaaa 68,168,208\nv_lshrrev_b32 v60, 3, v0\n"
"v_and_b32 v61, 0x70, v1\nv_lshl_or_b32 v60, v60, 10, v61\nv_mov_b32 v20, 0\nv_mov_b32 v21, 0\nv_mov_b32 v22, 0\nv_mov_b32 v23, 0\nSv 20,60,16,94,0\nSv 20,60,16,95,0\n"
"Zaaa 36,168,200\nTa 240,18,32768\nMU 53,49,50\nMaaa 36,172,204\nTa 242,18,34816\nCV 52,52,53\nZaaa 4,172,196\nD0 5,20,48,3072\nMaaa 4,168,192\nTa 244,18,49152\n"
"v_lshlrev_b32 v54, 16, v52\nZaaa 8,176,192\nTa 246,18,51200\nv_and_b32 v55, 0xffff0000, v52\nMaaa 8,180,196\nMU 56,48,51\nZaaa 40,180,204\nTa 248,19,32768\nMU 57,49,51\n"
"Maaa 40,176,200\nTa 250,19,34816\nZaaa 72,176,208\nv_and_b32 v48, 0xffff0000, v32\nv_and_b32 v49, 0xffff0000, v36\nMaaa 72,180,212\nTa 252,19,49152\n"
"v_and_b32 v50, 0xffff0000, v40\nZaaa 104,180,220\nTa 254,19,51200\nMaaa 104,176,216\nv_and_b32 v51, 0xffff0000, v44\nMU 52,48,50\nZaaa 108,184,216\nMU 53,49,50\nCV 52,52,53\n"
"Maaa 108,188,220\nv_and_b32 v53, 0xffff0000, v52\nZaaa 76,188,212\nv_lshlrev_b32 v52, 16, v52\nFM 56,48,51\nMaaa 76,184,208\nFM 57,49,51\nZaaa 44,184,200\nAD 54,54,52\n"
"AD 55,55,53\nMaaa 44,188,204\nv_lshlrev_b32 v48, 16, v33\nv_lshlrev_b32 v49, 16, v37\nZaaa 12,188,196\nv_lshlrev_b32 v50, 16, v41\nMaaa 12,184,192\nWL 0\nMvaa 80,224,128\n"
"Ta 160,12,32768\nv_lshlrev_b32 v51, 16, v45\nMvaa 80,228,132\nTa 162,12,34816\nMvaa 112,228,140\ns_add_u32 m0, s47, -32768\ns_add_u32 s45, s45, 128\nMvaa 112,224,136\n"
"Ta 164,12,49152\nMvaa 144,224,144\nTa 166,12,51200\nMU 52,48,50\nMvaa 144,228,148\nD0 2,20,45,0\nMU 53,49,50\nMvaa 176,228,156\nTa 168,13,32768\nMvaa 176,224,152\nTa 170,13,34816\n"
"CV 52,52,53\nMvaa 180,232,152\nv_and_b32 v53, 0xffff0000, v52\nv_lshlrev_b32 v52, 16, v52\nMvaa 180,236,156\nTa 172,13,49152\nMvaa 148,236,148\nTa 174,13,51200\nFM 56,48,51\n"
"Mvaa 148,232,144\nFM 57,49,51\nMvaa 116,232,136\nTa 176,14,32768\nAD 54,54,52\nMvaa 116,236,140\nTa 178,14,34816\nAD 55,55,53\nMvaa 84,236,132\nD0 3,20,45,1024\nMvaa 84,232,128\n"
"Ta 180,14,49152\nv_and_b32 v48, 0xffff0000, v33\nMvaa 88,240,128\nTa 182,14,51200\nv_and_b32 v49, 0xffff0000, v37\nMvaa 88,244,132\nv_and_b32 v50, 0xffff0000, v41\n"
"Mvaa 120,244,140\nTa 184,15,32768\nv_and_b32 v51, 0xffff0000, v45\nMvaa 120,240,136\nTa 186,15,34816\nMvaa 152,240,144\nMU 52,48,50\nMU 53,49,50\nMvaa 152,244,148\n"
"Ta 188,15,49152\nCV 52,52,53\nMvaa 184,244,156\nTa 190,15,51200\nMvaa 184,240,152\nD0 4,20,45,2048\nv_and_b32 v53, 0xffff0000, v52\nMvaa 188,248,152\nv_lshlrev_b32 v52, 16, v52\n"
"FM 56,48,51\nMvaa 188,252,156\nFM 57,49,51\nMvaa 156,252,148\nAD 54,54,52\nAD 55,55,53\nMvaa 156,248,144\nv_lshlrev_b32 v48, 16, v34\nMvaa 124,248,136\nv_lshlrev_b32 v49, 16, v38\n"
"v_lshlrev_b32 v50, 16, v42\nMvaa 124,252,140\nD0 5,20,45,3072\nv_lshlrev_b32 v51, 16, v46\nMvaa 92,252,132\nMU 52,48,50\nMvaa 92,248,128\ns_waitcnt vmcnt(19) lgkmcnt(0)\n"
"s_barrier\nMvaa 64,160,128\nRa 192,10,49152\nMU 53,49,50\nMvaa 64,164,132\ns_add_u32 m0, s47, 32768\ns_add_u32 s44, s44, 0x40000\nMvaa 96,164,140\nRa 196,11,49152\nCV 52,52,53\n"
"Mvaa 96,160,136\nv_and_b32 v53, 0xffff0000, v52\nMvaa 128,160,144\nRa 200,10,51200\nv_lshlrev_b32 v52, 16, v52\nMvaa 128,164,148\nD0 6,24,44,0\nFM 56,48,51\nMvaa 160,164,156\n"
"Ra 204,11,51200\nMvaa 160,160,152\nFM 57,49,51\nAD 54,54,52\nMvaa 164,168,152\nRa 208,10,57344\nAD 55,55,53\nMvaa 164,172,156\nv_and_b32 v48, 0xffff0000, v34\nMvaa 132,172,148\n"
"Ra 212,11,57344\nv_and_b32 v49, 0xffff0000, v38\nMvaa 132,168,144\nv_and_b32 v50, 0xffff0000, v42\nMvaa 100,168,136\nRa 216,10,59392\nv_and_b32 v51, 0xffff0000, v46\n"
"Mvaa 100,172,140\nD0 7,24,44,1024\nMU 52,48,50\nMvaa 68,172,132\nRa 220,11,59392\nMvaa 68,168,128\nMU 53,49,50\nCV 52,52,53\nMvaa 72,176,128\nv_and_b32 v53, 0xffff0000, v52\n"
"v_lshlrev_b32 v52, 16, v52\nMvaa 72,180,132\nFM 56,48,51\nMvaa 104,180,140\nFM 57,49,51\nAD 54,54,52\nMvaa 104,176,136\nAD 55,55,53\nMvaa 136,176,144\nv_lshlrev_b32 v48, 16, v35\n"
"v_lshlrev_b32 v49, 16, v39\nMvaa 136,180,148\nD0 8,24,44,2048\nv_lshlrev_b32 v50, 16, v43\nMvaa 168,180,156\nv_lshlrev_b32 v51, 16, v47\nMvaa 168,176,152\nMU 52,48,50\n"
"MU 53,49,50\nMvaa 172,184,152\nCV 52,52,53\nv_and_b32 v53, 0xffff0000, v52\nMvaa 172,188,156\nv_lshlrev_b32 v52, 16, v52\nMvaa 140,188,148\nFM 56,48,51\nFM 57,49,51\n"
"Mvaa 140,184,144\nAD 54,54,52\nMvaa 108,184,136\nAD 55,55,53\nv_and_b32 v48, 0xffff0000, v35\nMvaa 108,188,140\nD0 9,24,44,3072\nv_and_b32 v49, 0xffff0000, v39\nMvaa 76,188,132\n"
"v_and_b32 v50, 0xffff0000, v43\nMvaa 76,184,128\nWL 0\nMaaa 0,160,192\nRa 128,10,0\nv_and_b32 v51, 0xffff0000, v47\nMaaa 0,164,196\ns_add_u32 m0, s47, 49152\n"
"s_add_u32 s44, s44, 0x40000\nMaaa 32,164,204\nRa 132,11,0\nMU 52,48,50\nMaaa 32,160,200\nMU 53,49,50\nMaaa 64,160,208\nRa 136,10,2048\nCV 52,52,53\nMaaa 64,164,212\nD0 6,24,44,0\n"
"v_and_b32 v53, 0xffff0000, v52\nMaaa 96,164,220\nRa 140,11,2048\nMaaa 96,160,216\nv_lshlrev_b32 v52, 16, v52\nFM 56,48,51\nMaaa 100,168,216\nRa 144,10,8192\nFM 57,49,51\n"
"Maaa 100,172,220\nAD 54,54,52\n.W567:\ns_cbranch_execz .W571\nMaaa 68,172,212\nRa 148,11,8192\nAD 55,55,53\nMaaa 68,168,208\ns_nop 0\nv_permlane32_swap_b32 v54, v55\n"
"Maaa 36,168,200\nRa 152,10,10240\nv_permlane32_swap_b32 v56, v57\nMaaa 36,172,204\nD0 7,24,44,1024\nAD 54,54,55\nMaaa 4,172,196\nRa 156,11,10240\nMaaa 4,168,192\nAD 56,56,57\n"
"Maaa 8,176,192\nMaaa 8,180,196\nv_permlane16_swap_b32 v54, v56\nMaaa 40,180,204\nAD 54,54,56\nMaaa 40,176,200\nMaaa 72,176,208\n"
"v_add_f32_dpp v54, v54, v54 row_ror:8 row_mask:0xf bank_mask:0xf\nMaaa 72,180,212\nD0 8,24,44,2048\nMaaa 104,180,220\n"
"v_add_f32_dpp v54, v54, v54 row_ror:4 row_mask:0xf bank_mask:0xf\nMaaa 104,176,216\nMaaa 108,184,216\nv_add_f32_dpp v54, v54, v54 row_ror:2 row_mask:0xf bank_mask:0xf\n"
"Maaa 108,188,220\nMaaa 76,188,212\nv_add_f32_dpp v54, v54, v54 row_ror:1 row_mask:0xf bank_mask:0xf\nCV 54,54,54\nMaaa 76,184,208\nv_lshlrev_b32 v54, 16, v54\nMaaa 44,184,200\n"
"v_mul_f32 v55, 0xbfb8aa3b, v54\nMaaa 44,188,204\nD0 9,24,44,3072\nMaaa 12,188,196\nEX 55,55\nMaaa 12,184,192\ns_waitcnt vmcnt(12) lgkmcnt(0)\ns_barrier\nMaaa 16,224,192\n"
"Ta 160,12,0\nv_add_f32 v56, 0, v54\nMaaa 16,228,196\nTa 162,12,2048\nMaaa 48,228,204\ns_add_u32 m0, s47, -16384\ns_add_u32 s48, s48, 128\nMaaa 48,224,200\nTa 164,12,16384\n"
"Maaa 80,224,208\nTa 166,12,18432\nbuffer_load_dword v31, v24, s[24:27], s96 offen\ns_add_u32 s96, s96, 0x80000\nMaaa 80,228,212\nD0 2,20,48,0\nv_add_f32 v55, 1.0, v55\n"
"Maaa 112,228,220\nTa 168,13,0\nv_rcp_f32 v55, v55\nMaaa 112,224,216\nTa 170,13,2048\nv_ashrrev_i32 v57, 31, v56\nMaaa 116,232,216\nCV 55,55,55\nv_lshlrev_b32 v55, 16, v55\n"
"Maaa 116,236,220\nTa 172,13,16384\nMaaa 84,236,212\nTa 174,13,18432\nv_or_b32 v57, 0x80000000, v57\nMaaa 84,232,208\nv_xor_b32 v56, v56, v57\nMaaa 52,232,200\nTa 176,14,0\n"
"v_and_b32 v56, 0xffff0000, v56\nMaaa 52,236,204\nTa 178,14,2048\ns_lshl2_add_u32 s2, s10, s11\nMaaa 20,236,196\nD0 3,20,48,1024\nMaaa 20,232,192\nTa 180,14,16384\n"
"v_lshrrev_b32 v48, 5, v0\nMaaa 24,240,192\nTa 182,14,18432\nv_lshl_add_u32 v48, s2, 1, v48\nMaaa 24,244,196\nv_sub_u32 v49, 0xffff, v48\nMaaa 56,244,204\nTa 184,15,0\n"
"v_or_b32 v56, v56, v49\nMaaa 56,240,200\nTa 186,15,2048\nMaaa 88,240,208\nv_mov_b32 v57, s15\nv_and_b32 v49, 31, v0\nMaaa 88,244,212\nTa 188,15,16384\nv_mov_b32 v52, 0x40000000\n"
"Maaa 120,244,220\nTa 190,15,18432\nv_cmp_eq_u32 vcc, 16, v49\nv_lshlrev_b32 v50, 2, v48\nv_lshlrev_b32 v51, 10, v49\nv_cndmask_b32 v50, v52, v50, vcc\n"
"v_cndmask_b32 v56, v55, v56, vcc\nMaaa 120,240,216\nD0 4,20,48,2048\nv_lshl_add_u32 v51, v48, 3, v51\nMaaa 124,248,216\nv_and_b32 v49, 15, v0\nv_cmp_eq_u32 vcc, 0, v49\n"
"s_mov_b32 s2, 0xc040000\ns_mov_b32 s3, 0xc03e000\nv_cndmask_b32 v51, v52, v51, vcc\nbuffer_store_dwordx2 v[56:57], v51, s[16:19], s2 offen sc0 sc1\n"
"buffer_store_dword v54, v50, s[16:19], s3 offen sc0 sc1\nMaaa 124,252,220\nMaaa 92,252,212\nMaaa 92,248,208\nMaaa 60,248,200\nMaaa 60,252,204\nD0 5,20,48,3072\nMaaa 28,252,196\n"
"Maaa 28,248,192\nWL 0\ns_mov_b32 s50, 3\n.GL272:\nMvaa 64,160,128\nTa 224,16,0\nMvaa 64,164,132\nTa 226,16,2048\nMvaa 96,164,140\nTa 228,16,16384\ns_add_u32 m0, s47, -65536\n"
"s_add_u32 s45, s45, 128\nMvaa 96,160,136\nTa 230,16,18432\nMvaa 128,160,144\nTa 232,17,0\nMvaa 128,164,148\nTa 234,17,2048\nD0 2,20,45,0\nMvaa 160,164,156\nTa 236,17,16384\n"
"Mvaa 160,160,152\nTa 238,17,18432\nMvaa 164,168,152\nTa 240,18,0\nMvaa 164,172,156\nTa 242,18,2048\nMvaa 132,172,148\nTa 244,18,16384\nMvaa 132,168,144\nTa 246,18,18432\n"
"Mvaa 100,168,136\nTa 248,19,0\nMvaa 100,172,140\nTa 250,19,2048\nMvaa 68,172,132\nTa 252,19,16384\nD0 3,20,45,1024\nMvaa 68,168,128\nTa 254,19,18432\nMvaa 72,176,128\n"
"Ra 192,10,16384\nMvaa 72,180,132\nRa 196,11,16384\nMvaa 104,180,140\nRa 200,10,18432\nMvaa 104,176,136\nRa 204,11,18432\nMvaa 136,176,144\nRa 208,10,24576\nMvaa 136,180,148\n"
"Ra 212,11,24576\nMvaa 168,180,156\nRa 216,10,26624\nMvaa 168,176,152\nRa 220,11,26624\nD0 4,20,45,2048\nMvaa 172,184,152\nMvaa 172,188,156\nMvaa 140,188,148\nMvaa 140,184,144\n"
"Mvaa 108,184,136\nMvaa 108,188,140\nD0 5,20,45,3072\nMvaa 76,188,132\nMvaa 76,184,128\ns_waitcnt vmcnt(18) lgkmcnt(8)\ns_barrier\nMvaa 80,224,128\nMvaa 80,228,132\n"
"s_add_u32 m0, s47, 0\ns_add_u32 s44, s44, 0x40000\ns_mov_b32 s86, 1\ns_load_dword s86, s[54:55], s94\nMvaa 112,228,140\ns_add_u32 s94, s94, 1024\nMvaa 112,224,136\n"
"Mvaa 144,224,144\nMvaa 144,228,148\nD0 6,24,44,0\nMvaa 176,228,156\nMvaa 176,224,152\nMvaa 180,232,152\nMvaa 180,236,156\nMvaa 148,236,148\nMvaa 148,232,144\n"
"s_add_u32 s88, s88, s86\nMvaa 116,232,136\nMvaa 116,236,140\nD0 7,24,44,1024\ns_add_u32 s88, s88, s86\nMvaa 84,236,132\nMvaa 84,232,128\ns_add_u32 s88, s88, s86\nMvaa 88,240,128\n"
"Mvaa 88,244,132\ns_add_u32 s88, s88, s86\nMvaa 120,244,140\nMvaa 120,240,136\ns_add_u32 s88, s88, s86\nMvaa 152,240,144\nMvaa 152,244,148\nD0 8,24,44,2048\n"
"s_add_u32 s88, s88, s86\nMvaa 184,244,156\nMvaa 184,240,152\ns_add_u32 s88, s88, s86\nMvaa 188,248,152\nMvaa 188,252,156\ns_add_u32 s88, s88, s86\nMvaa 156,252,148\n"
"Mvaa 156,248,144\ns_add_u32 s88, s88, s86\nMvaa 124,248,136\nMvaa 124,252,140\nD0 9,24,44,3072\ns_add_u32 s88, s88, s86\nMvaa 92,252,132\nMvaa 92,248,128\nWL 0\n"
"s_add_u32 s88, s88, s86\nMaaa 16,224,192\nRa 128,10,32768\nMaaa 16,228,196\ns_add_u32 m0, s47, 16384\ns_add_u32 s44, s44, 0x40000\ns_add_u32 s88, s88, s86\nMaaa 48,228,204\n"
"Ra 132,11,32768\nMaaa 48,224,200\ns_add_u32 s88, s88, s86\nMaaa 80,224,208\nRa 136,10,34816\nMaaa 80,228,212\nD0 6,24,44,0\ns_add_u32 s88, s88, s86\nMaaa 112,228,220\n"
"Ra 140,11,34816\nMaaa 112,224,216\ns_add_u32 s88, s88, s86\nMaaa 116,232,216\nRa 144,10,40960\nMaaa 116,236,220\ns_add_u32 s88, s88, s86\nMaaa 84,236,212\nRa 148,11,40960\n"
"Maaa 84,232,208\nMaaa 52,232,200\nRa 152,10,43008\nMaaa 52,236,204\nD0 7,24,44,1024\nMaaa 20,236,196\nRa 156,11,43008\nMaaa 20,232,192\nMaaa 24,240,192\nMaaa 24,244,196\n"
"Maaa 56,244,204\nMaaa 56,240,200\nMaaa 88,240,208\nMaaa 88,244,212\nD0 8,24,44,2048\nMaaa 120,244,220\nMaaa 120,240,216\nMaaa 124,248,216\nMaaa 124,252,220\nMaaa 92,252,212\n"
"Maaa 92,248,208\nMaaa 60,248,200\nMaaa 60,252,204\nD0 9,24,44,3072\nMaaa 28,252,196\nMaaa 28,248,192\ns_waitcnt vmcnt(12) lgkmcnt(0)\ns_barrier\nMaaa 0,160,192\nTa 224,16,32768\n"
"s_mov_b32 m0, s76\ns_add_u32 s76, s76, 1024\nMaaa 0,164,196\nTa 226,16,34816\nD0 1,36,75,0\ns_add_u32 s75, s75, 1024\nMaaa 32,164,204\ns_add_u32 m0, s47, -49152\n"
"s_add_u32 s48, s48, 128\nMaaa 32,160,200\nTa 228,16,49152\nMaaa 64,160,208\nTa 230,16,51200\nbuffer_load_dword v31, v24, s[24:27], s96 offen\ns_add_u32 s96, s96, 0x80000\n"
"Maaa 64,164,212\nD0 2,20,48,0\nMaaa 96,164,220\nTa 232,17,32768\nMaaa 96,160,216\nTa 234,17,34816\nMaaa 100,168,216\nMaaa 100,172,220\nTa 236,17,49152\nMaaa 68,172,212\n"
"Ta 238,17,51200\nMaaa 68,168,208\nMaaa 36,168,200\nTa 240,18,32768\nMaaa 36,172,204\nTa 242,18,34816\nMaaa 4,172,196\nD0 3,20,48,1024\nMaaa 4,168,192\nTa 244,18,49152\n"
"Maaa 8,176,192\nTa 246,18,51200\nMaaa 8,180,196\nMaaa 40,180,204\nTa 248,19,32768\nMaaa 40,176,200\nTa 250,19,34816\nMaaa 72,176,208\nMaaa 72,180,212\nTa 252,19,49152\n"
"Maaa 104,180,220\nTa 254,19,51200\nMaaa 104,176,216\nD0 4,20,48,2048\nMaaa 108,184,216\nMaaa 108,188,220\nMaaa 76,188,212\nMaaa 76,184,208\nMaaa 44,184,200\nMaaa 44,188,204\n"
"D0 5,20,48,3072\nMaaa 12,188,196\nMaaa 12,184,192\nWL 0\nMvaa 80,224,128\nTa 160,12,32768\nMvaa 80,228,132\nTa 162,12,34816\nMvaa 112,228,140\nTa 164,12,49152\n"
"s_add_u32 m0, s47, -32768\ns_add_u32 s45, s45, 128\nMvaa 112,224,136\nTa 166,12,51200\nMvaa 144,224,144\nTa 168,13,32768\nMvaa 144,228,148\nTa 170,13,34816\nD0 2,20,45,0\n"
"Mvaa 176,228,156\nTa 172,13,49152\nMvaa 176,224,152\nTa 174,13,51200\nMvaa 180,232,152\nTa 176,14,32768\nMvaa 180,236,156\nTa 178,14,34816\nMvaa 148,236,148\nTa 180,14,49152\n"
"Mvaa 148,232,144\nTa 182,14,51200\nMvaa 116,232,136\nTa 184,15,32768\nMvaa 116,236,140\nTa 186,15,34816\nMvaa 84,236,132\nTa 188,15,49152\nD0 3,20,45,1024\nMvaa 84,232,128\n"
"Ta 190,15,51200\nMvaa 88,240,128\nRa 192,10,49152\nMvaa 88,244,132\nRa 196,11,49152\nMvaa 120,244,140\nRa 200,10,51200\nMvaa 120,240,136\nRa 204,11,51200\nMvaa 152,240,144\n"
"Ra 208,10,57344\nMvaa 152,244,148\nRa 212,11,57344\nMvaa 184,244,156\nRa 216,10,59392\nMvaa 184,240,152\nRa 220,11,59392\nD0 4,20,45,2048\nMvaa 188,248,152\nMvaa 188,252,156\n"
"Mvaa 156,252,148\nMvaa 156,248,144\nMvaa 124,248,136\nMvaa 124,252,140\nD0 5,20,45,3072\nMvaa 92,252,132\nMvaa 92,248,128\ns_waitcnt vmcnt(18) lgkmcnt(8)\ns_barrier\n"
"Mvaa 64,160,128\nMvaa 64,164,132\ns_add_u32 m0, s47, 32768\ns_add_u32 s44, s44, 0x40000\ns_mov_b32 s86, 1\ns_load_dword s86, s[54:55], s95\nMvaa 96,164,140\n"
"s_add_u32 s95, s95, 1024\nMvaa 96,160,136\nMvaa 128,160,144\nMvaa 128,164,148\nD0 6,24,44,0\nMvaa 160,164,156\nMvaa 160,160,152\nMvaa 164,168,152\nMvaa 164,172,156\n"
"Mvaa 132,172,148\nMvaa 132,168,144\ns_sub_u32 s88, s88, s86\nMvaa 100,168,136\nMvaa 100,172,140\nD0 7,24,44,1024\ns_sub_u32 s88, s88, s86\nMvaa 68,172,132\nMvaa 68,168,128\n"
"s_sub_u32 s88, s88, s86\nMvaa 72,176,128\nMvaa 72,180,132\ns_sub_u32 s88, s88, s86\nMvaa 104,180,140\nMvaa 104,176,136\ns_sub_u32 s88, s88, s86\nMvaa 136,176,144\n"
"Mvaa 136,180,148\nD0 8,24,44,2048\ns_sub_u32 s88, s88, s86\nMvaa 168,180,156\nMvaa 168,176,152\ns_sub_u32 s88, s88, s86\nMvaa 172,184,152\nMvaa 172,188,156\n"
"s_sub_u32 s88, s88, s86\nMvaa 140,188,148\nMvaa 140,184,144\ns_sub_u32 s88, s88, s86\nMvaa 108,184,136\nMvaa 108,188,140\nD0 9,24,44,3072\ns_sub_u32 s88, s88, s86\n"
"Mvaa 76,188,132\nMvaa 76,184,128\nWL 0\ns_sub_u32 s88, s88, s86\nMaaa 0,160,192\nRa 128,10,0\nMaaa 0,164,196\ns_add_u32 m0, s47, 49152\ns_add_u32 s44, s44, 0x40000\n"
"s_sub_u32 s88, s88, s86\nMaaa 32,164,204\nRa 132,11,0\nMaaa 32,160,200\ns_sub_u32 s88, s88, s86\nMaaa 64,160,208\nRa 136,10,2048\nMaaa 64,164,212\nD0 6,24,44,0\n"
"s_sub_u32 s88, s88, s86\nMaaa 96,164,220\nRa 140,11,2048\nMaaa 96,160,216\ns_sub_u32 s88, s88, s86\nMaaa 100,168,216\nRa 144,10,8192\nMaaa 100,172,220\ns_sub_u32 s88, s88, s86\n"
"Maaa 68,172,212\nRa 148,11,8192\nMaaa 68,168,208\nMaaa 36,168,200\nRa 152,10,10240\nMaaa 36,172,204\nD0 7,24,44,1024\nMaaa 4,172,196\nRa 156,11,10240\nMaaa 4,168,192\n"
"Maaa 8,176,192\nMaaa 8,180,196\nMaaa 40,180,204\nMaaa 40,176,200\nMaaa 72,176,208\nMaaa 72,180,212\nD0 8,24,44,2048\nMaaa 104,180,220\nMaaa 104,176,216\nMaaa 108,184,216\n"
"Maaa 108,188,220\nMaaa 76,188,212\nMaaa 76,184,208\nMaaa 44,184,200\nMaaa 44,188,204\nD0 9,24,44,3072\nMaaa 12,188,196\nMaaa 12,184,192\ns_waitcnt vmcnt(12) lgkmcnt(0)\ns_barrier\n"
"Maaa 16,224,192\nTa 160,12,0\ns_mov_b32 m0, s76\ns_add_u32 s76, s76, 1024\nMaaa 16,228,196\nTa 162,12,2048\nD0 1,36,75,0\ns_add_u32 s75, s75, 1024\nMaaa 48,228,204\n"
"s_add_u32 m0, s47, -16384\ns_add_u32 s48, s48, 128\nMaaa 48,224,200\nTa 164,12,16384\nMaaa 80,224,208\nTa 166,12,18432\nbuffer_load_dword v31, v24, s[24:27], s96 offen\n"
"s_add_u32 s96, s96, 0x80000\nMaaa 80,228,212\n.W568:\ns_cbranch_execz .W572\nD0 2,20,48,0\nMaaa 112,228,220\nTa 168,13,0\nMaaa 112,224,216\nTa 170,13,2048\nMaaa 116,232,216\n"
"Maaa 116,236,220\nTa 172,13,16384\nMaaa 84,236,212\nTa 174,13,18432\nMaaa 84,232,208\nMaaa 52,232,200\nTa 176,14,0\nMaaa 52,236,204\nTa 178,14,2048\nMaaa 20,236,196\n"
"D0 3,20,48,1024\nMaaa 20,232,192\nTa 180,14,16384\nMaaa 24,240,192\nTa 182,14,18432\nMaaa 24,244,196\nMaaa 56,244,204\nTa 184,15,0\nMaaa 56,240,200\nTa 186,15,2048\n"
"Maaa 88,240,208\nMaaa 88,244,212\nTa 188,15,16384\nMaaa 120,244,220\nTa 190,15,18432\nMaaa 120,240,216\nD0 4,20,48,2048\nMaaa 124,248,216\nMaaa 124,252,220\nMaaa 92,252,212\n"
"Maaa 92,248,208\nMaaa 60,248,200\ns_xor_b32 s94, s94, 0x2000\ns_xor_b32 s95, s95, 0x2000\ns_sub_u32 s88, 0, s88\nMaaa 60,252,204\nD0 5,20,48,3072\ns_sub_u32 s50, s50, 1\n"
"Maaa 28,252,196\nMaaa 28,248,192\nWL 0\ns_cbranch_scc0 .GL272\nMvaa 64,160,128\nTa 224,16,0\nMvaa 64,164,132\nTa 226,16,2048\nMvaa 96,164,140\ns_add_u32 m0, s47, -65536\n"
"s_add_u32 s45, s45, 128\nMvaa 96,160,136\nTa 228,16,16384\nMvaa 128,160,144\nTa 230,16,18432\nMvaa 128,164,148\nD0 2,20,45,0\nMvaa 160,164,156\nTa 232,17,0\nMvaa 160,160,152\n"
"Ta 234,17,2048\nMvaa 164,168,152\ns_sub_u32 s89, 0, s88\ns_cmp_eq_u32 s92, 0\ns_cselect_b32 s88, s88, s89\nMvaa 164,172,156\nTa 236,17,16384\nMvaa 132,172,148\nTa 238,17,18432\n"
"Mvaa 132,168,144\nv_mov_b32 v22, s88\nMvaa 100,168,136\nTa 240,18,0\nMvaa 100,172,140\nTa 242,18,2048\nMvaa 68,172,132\nD0 3,20,45,1024\nMvaa 68,168,128\nTa 244,18,16384\n"
"Mvaa 72,176,128\nTa 246,18,18432\nMvaa 72,180,132\nv_mov_b32 v23, s15\nMvaa 104,180,140\nTa 248,19,0\nMvaa 104,176,136\nTa 250,19,2048\nMvaa 136,176,144\n"
"buffer_store_dwordx2 v[22:23], off, s[16:19], s93 sc0 sc1\nMvaa 136,180,148\nTa 252,19,16384\nMvaa 168,180,156\nTa 254,19,18432\nMvaa 168,176,152\nD0 4,20,45,2048\n"
"Mvaa 172,184,152\nMvaa 172,188,156\nMvaa 140,188,148\nMvaa 140,184,144\nMvaa 108,184,136\nMvaa 108,188,140\nD0 5,20,45,3072\nMvaa 76,188,132\nMvaa 76,184,128\n"
"s_waitcnt vmcnt(19) lgkmcnt(0)\ns_barrier\nMvaa 80,224,128\nRa 192,10,16384\nMvaa 80,228,132\ns_add_u32 m0, s47, 0\ns_add_u32 s44, s44, 0x40000\nMvaa 112,228,140\nRa 196,11,16384\n"
"Mvaa 112,224,136\nMvaa 144,224,144\nRa 200,10,18432\nMvaa 144,228,148\nD0 6,24,44,0\nMvaa 176,228,156\nRa 204,11,18432\nMvaa 176,224,152\nMvaa 180,232,152\nRa 208,10,24576\n"
"Mvaa 180,236,156\nMvaa 148,236,148\nRa 212,11,24576\nMvaa 148,232,144\nMvaa 116,232,136\nRa 216,10,26624\nMvaa 116,236,140\nD0 7,24,44,1024\nMvaa 84,236,132\nRa 220,11,26624\n"
"Mvaa 84,232,128\nMvaa 88,240,128\nMvaa 88,244,132\nMvaa 120,244,140\nMvaa 120,240,136\nMvaa 152,240,144\nMvaa 152,244,148\nD0 8,24,44,2048\nMvaa 184,244,156\nMvaa 184,240,152\n"
"Mvaa 188,248,152\nMvaa 188,252,156\nMvaa 156,252,148\nMvaa 156,248,144\nMvaa 124,248,136\nMvaa 124,252,140\nD0 9,24,44,3072\nMvaa 92,252,132\nMvaa 92,248,128\nWL 0\n"
"Maaa 16,224,192\nRa 128,10,32768\nMaaa 16,228,196\ns_add_u32 m0, s47, 16384\ns_add_u32 s44, s44, 0x40000\nMaaa 48,228,204\nRa 132,11,32768\nMaaa 48,224,200\nMaaa 80,224,208\n"
"Ra 136,10,34816\nMaaa 80,228,212\nD0 6,24,44,0\nMaaa 112,228,220\nRa 140,11,34816\nMaaa 112,224,216\nMaaa 116,232,216\nRa 144,10,40960\nMaaa 116,236,220\nMaaa 84,236,212\n"
"Ra 148,11,40960\nMaaa 84,232,208\nMaaa 52,232,200\nRa 152,10,43008\nMaaa 52,236,204\nD0 7,24,44,1024\nMaaa 20,236,196\nRa 156,11,43008\nMaaa 20,232,192\nMaaa 24,240,192\n"
"Maaa 24,244,196\nMaaa 56,244,204\nMaaa 56,240,200\nMaaa 88,240,208\nMaaa 88,244,212\nD0 8,24,44,2048\nMaaa 120,244,220\nMaaa 120,240,216\nMaaa 124,248,216\nMaaa 124,252,220\n"
"Maaa 92,252,212\nMaaa 92,248,208\nMaaa 60,248,200\nMaaa 60,252,204\nD0 9,24,44,3072\nMaaa 28,252,196\nMaaa 28,248,192\ns_waitcnt vmcnt(19) lgkmcnt(0)\ns_barrier\nMaaa 0,160,192\n"
"Ta 224,16,32768\nMaaa 0,164,196\nTa 226,16,34816\nMaaa 32,164,204\ns_add_u32 m0, s47, -49152\ns_add_u32 s48, s48, 128\nMaaa 32,160,200\nTa 228,16,49152\nMaaa 64,160,208\n"
"Ta 230,16,51200\nbuffer_load_dword v31, v24, s[24:27], s96 offen\ns_add_u32 s96, s96, 0x80000\nMaaa 64,164,212\nD0 2,20,48,0\nMaaa 96,164,220\nTa 232,17,32768\nMaaa 96,160,216\n"
"Ta 234,17,34816\nMaaa 100,168,216\nMaaa 100,172,220\nTa 236,17,49152\nMaaa 68,172,212\nTa 238,17,51200\nMaaa 68,168,208\nMaaa 36,168,200\nTa 240,18,32768\nMaaa 36,172,204\n"
"Ta 242,18,34816\nMaaa 4,172,196\nD0 3,20,48,1024\nMaaa 4,168,192\nTa 244,18,49152\nMaaa 8,176,192\nTa 246,18,51200\nMaaa 8,180,196\nMaaa 40,180,204\nTa 248,19,32768\n"
"Maaa 40,176,200\nTa 250,19,34816\nMaaa 72,176,208\nMaaa 72,180,212\nTa 252,19,49152\nMaaa 104,180,220\nTa 254,19,51200\nMaaa 104,176,216\nD0 4,20,48,2048\nMaaa 108,184,216\n"
"Maaa 108,188,220\nMaaa 76,188,212\nMaaa 76,184,208\nMaaa 44,184,200\nMaaa 44,188,204\nD0 5,20,48,3072\nMaaa 12,188,196\nMaaa 12,184,192\nWL 0\nMvaa 80,224,128\nTa 160,12,32768\n"
"Mvaa 80,228,132\nTa 162,12,34816\nMvaa 112,228,140\ns_add_u32 m0, s47, -32768\ns_add_u32 s45, s45, 128\nMvaa 112,224,136\nTa 164,12,49152\nMvaa 144,224,144\nTa 166,12,51200\n"
"Mvaa 144,228,148\nD0 2,20,45,0\nMvaa 176,228,156\nTa 168,13,32768\nMvaa 176,224,152\nTa 170,13,34816\nMvaa 180,232,152\nMvaa 180,236,156\nTa 172,13,49152\nMvaa 148,236,148\n"
"Ta 174,13,51200\nMvaa 148,232,144\nMvaa 116,232,136\nTa 176,14,32768\nMvaa 116,236,140\nTa 178,14,34816\nMvaa 84,236,132\nD0 3,20,45,1024\nMvaa 84,232,128\nTa 180,14,49152\n"
"Mvaa 88,240,128\nTa 182,14,51200\nMvaa 88,244,132\nMvaa 120,244,140\nTa 184,15,32768\nMvaa 120,240,136\nTa 186,15,34816\nMvaa 152,240,144\nMvaa 152,244,148\nTa 188,15,49152\n"
"Mvaa 184,244,156\nTa 190,15,51200\nMvaa 184,240,152\nD0 4,20,45,2048\nMvaa 188,248,152\nMvaa 188,252,156\nMvaa 156,252,148\nMvaa 156,248,144\nMvaa 124,248,136\nMvaa 124,252,140\n"
"D0 5,20,45,3072\nMvaa 92,252,132\nMvaa 92,248,128\ns_waitcnt vmcnt(17) lgkmcnt(0)\ns_barrier\nMvaa 64,160,128\nRa 192,10,49152\nMvaa 64,164,132\ns_add_u32 m0, s47, 32768\n"
"s_add_u32 s44, s44, 0x40000\nMvaa 96,164,140\nRa 196,11,49152\nMvaa 96,160,136\nMvaa 128,160,144\nRa 200,10,51200\nMvaa 128,164,148\nD0 6,24,44,0\nMvaa 160,164,156\n"
"Ra 204,11,51200\nMvaa 160,160,152\nMvaa 164,168,152\nRa 208,10,57344\nMvaa 164,172,156\ns_lshl_b32 s5, s73, 9\ns_add_u32 s5, s5, 0xc049400\nv_lshrrev_b32 v27, 1, v1\n"
"buffer_load_dwordx2 v[22:23], v27, s[16:19], s5 offen sc0 sc1\nMvaa 132,172,148\nRa 212,11,57344\nMvaa 132,168,144\nMvaa 100,168,136\nRa 216,10,59392\nMvaa 100,172,140\n"
"D0 7,24,44,1024\nMvaa 68,172,132\nRa 220,11,59392\nMvaa 68,168,128\nMvaa 72,176,128\nMvaa 72,180,132\nMvaa 104,180,140\nMvaa 104,176,136\nMvaa 136,176,144\nMvaa 136,180,148\n"
"D0 8,24,44,2048\nMvaa 168,180,156\nMvaa 168,176,152\nMvaa 172,184,152\nMvaa 172,188,156\nMvaa 140,188,148\nMvaa 140,184,144\nMvaa 108,184,136\nMvaa 108,188,140\nD0 9,24,44,3072\n"
"Mvaa 76,188,132\nMvaa 76,184,128\nWL 0\nMaaa 0,160,192\nRa 128,10,0\nMaaa 0,164,196\ns_add_u32 m0, s47, 49152\ns_add_u32 s44, s44, 0x40000\nMaaa 32,164,204\nRa 132,11,0\n"
"Maaa 32,160,200\nMaaa 64,160,208\nRa 136,10,2048\nMaaa 64,164,212\nD0 6,24,44,0\nMaaa 96,164,220\nRa 140,11,2048\nMaaa 96,160,216\nMaaa 100,168,216\nRa 144,10,8192\n"
"Maaa 100,172,220\nMaaa 68,172,212\nRa 148,11,8192\nMaaa 68,168,208\nMaaa 36,168,200\nRa 152,10,10240\nMaaa 36,172,204\nD0 7,24,44,1024\nMaaa 4,172,196\nRa 156,11,10240\n"
"Maaa 4,168,192\nMaaa 8,176,192\nMaaa 8,180,196\nMaaa 40,180,204\nMaaa 40,176,200\nMaaa 72,176,208\nMaaa 72,180,212\nD0 8,24,44,2048\nMaaa 104,180,220\nMaaa 104,176,216\n"
"Maaa 108,184,216\nMaaa 108,188,220\nMaaa 76,188,212\nMaaa 76,184,208\nMaaa 44,184,200\nMaaa 44,188,204\nD0 9,24,44,3072\nMaaa 12,188,196\nMaaa 12,184,192\n"
"s_waitcnt vmcnt(18) lgkmcnt(0)\ns_barrier\nMaaa 16,224,192\nTa 160,12,0\nMaaa 16,228,196\nTa 162,12,2048\nMaaa 48,228,204\ns_add_u32 m0, s47, -16384\ns_add_u32 s48, s48, 128\n"
"Maaa 48,224,200\nTa 164,12,16384\nMaaa 80,224,208\nTa 166,12,18432\nbuffer_load_dword v31, v24, s[24:27], s96 offen\ns_add_u32 s96, s96, 0x80000\nMaaa 80,228,212\nD0 2,20,48,0\n"
"Maaa 112,228,220\nTa 168,13,0\nMaaa 112,224,216\nTa 170,13,2048\nMaaa 116,232,216\nMaaa 116,236,220\nTa 172,13,16384\nMaaa 84,236,212\nTa 174,13,18432\nMaaa 84,232,208\n"
"Maaa 52,232,200\nTa 176,14,0\nMaaa 52,236,204\nTa 178,14,2048\nMaaa 20,236,196\nD0 3,20,48,1024\nMaaa 20,232,192\nTa 180,14,16384\nMaaa 24,240,192\nTa 182,14,18432\n"
"Maaa 24,244,196\nMaaa 56,244,204\nTa 184,15,0\nMaaa 56,240,200\nTa 186,15,2048\nMaaa 88,240,208\nMaaa 88,244,212\nTa 188,15,16384\nMaaa 120,244,220\nTa 190,15,18432\n"
"Maaa 120,240,216\nD0 4,20,48,2048\nMaaa 124,248,216\nMaaa 124,252,220\nMaaa 92,252,212\nMaaa 92,248,208\nMaaa 60,248,200\nMaaa 60,252,204\nD0 5,20,48,3072\nMaaa 28,252,196\n"
"Maaa 28,248,192\nWL 0\nMvaa 64,160,128\nTa 224,16,0\nWV 12\nv_cmp_ne_u32 vcc, s15, v23\ns_nop 0\ns_cbranch_vccnz .S269\n.S270:\nMvaa 64,164,132\nTa 226,16,2048\nMvaa 96,164,140\n"
"s_add_u32 m0, s47, -65536\ns_add_u32 s45, s45, 128\nMvaa 96,160,136\nTa 228,16,16384\nMvaa 128,160,144\nTa 230,16,18432\nMvaa 128,164,148\nD0 2,20,45,0\nMvaa 160,164,156\n"
"Ta 232,17,0\nMvaa 160,160,152\nTa 234,17,2048\nMvaa 164,168,152\nv_add_u32_dpp v22, v22, v22 row_ror:8 row_mask:0xf bank_mask:0xf\nMvaa 164,172,156\nTa 236,17,16384\n"
"Mvaa 132,172,148\nTa 238,17,18432\nMvaa 132,168,144\nv_add_u32_dpp v22, v22, v22 row_ror:4 row_mask:0xf bank_mask:0xf\nMvaa 100,168,136\nTa 240,18,0\nMvaa 100,172,140\n"
"Ta 242,18,2048\nMvaa 68,172,132\nD0 3,20,45,1024\nMvaa 68,168,128\nTa 244,18,16384\nMvaa 72,176,128\nTa 246,18,18432\nMvaa 72,180,132\n"
"v_add_u32_dpp v22, v22, v22 row_ror:2 row_mask:0xf bank_mask:0xf\nMvaa 104,180,140\nTa 248,19,0\nMvaa 104,176,136\nTa 250,19,2048\nMvaa 136,176,144\n"
"v_add_u32_dpp v22, v22, v22 row_ror:1 row_mask:0xf bank_mask:0xf\nMvaa 136,180,148\nTa 252,19,16384\nMvaa 168,180,156\nTa 254,19,18432\n.W569:\ns_cbranch_execz .W573\n"
"Mvaa 168,176,152\nD0 4,20,45,2048\nMvaa 172,184,152\nv_mov_b32 v23, v22\nMvaa 172,188,156\nMvaa 140,188,148\nv_permlane16_swap_b32 v22, v23\nMvaa 140,184,144\n"
"v_add_u32 v22, v22, v23\nMvaa 108,184,136\nv_mov_b32 v23, v22\nMvaa 108,188,140\nD0 5,20,45,3072\nMvaa 76,188,132\nv_permlane32_swap_b32 v22, v23\nMvaa 76,184,128\nWL 0\n"
"s_barrier\nMvaa 80,224,128\nRa 192,10,16384\nMvaa 80,228,132\ns_add_u32 m0, s47, 0\ns_add_u32 s44, s44, 0x40000\nMvaa 112,228,140\nRa 196,11,16384\nMvaa 112,224,136\n"
"v_add_u32 v22, v22, v23\nMvaa 144,224,144\nRa 200,10,18432\nMvaa 144,228,148\nD0 6,24,44,0\nMvaa 176,228,156\nRa 204,11,18432\nMvaa 176,224,152\nv_readfirstlane_b32 s4, v22\n"
"Mvaa 180,232,152\nRa 208,10,24576\nMvaa 180,236,156\nMvaa 148,236,148\nRa 212,11,24576\ns_cmp_gt_i32 s4, 0\ns_cselect_b32 s4, 1, 0\nMvaa 148,232,144\ns_xor_b32 s4, s4, s12\n"
"Mvaa 116,232,136\nRa 216,10,26624\nMvaa 116,236,140\nD0 7,24,44,1024\nMvaa 84,236,132\nRa 220,11,26624\nMvaa 84,232,128\ns_and_b32 s4, s4, 1\nMvaa 88,240,128\n"
"s_lshl_b32 s4, s4, 13\nMvaa 88,244,132\ns_mul_i32 s72, s73, 25165824\nMvaa 120,244,140\ns_add_u32 s72, s72, s4\nMvaa 120,240,136\ns_and_b32 s4, s12, 1\nMvaa 152,240,144\n"
"s_lshl_b32 s4, s4, 21\nMvaa 152,244,148\nD0 8,24,44,2048\nMvaa 184,244,156\ns_add_u32 s72, s72, s4\nMvaa 184,240,152\ns_lshl3_add_u32 s2, s13, s11\nMvaa 188,248,152\n"
"s_lshl_b32 s2, s2, 11\nMvaa 188,252,156\ns_lshl_b32 s3, s14, 22\nMvaa 156,252,148\ns_add_u32 s2, s2, s3\nMvaa 156,248,144\ns_add_u32 s2, s2, s72\nMvaa 124,248,136\n"
"s_add_u32 s49, s2, 0xbe0000\nMvaa 124,252,140\nD0 9,24,44,3072\nMvaa 92,252,132\nMvaa 92,248,128\nWL 0\nMaaa 16,224,192\nRa 128,10,32768\ns_mov_b32 s4, s13\nMaaa 16,228,196\n"
"s_add_u32 m0, s47, 16384\ns_add_u32 s44, s44, 0x40000\nMaaa 48,228,204\nRa 132,11,32768\nv_lshrrev_b32 v32, 2, v0\nMaaa 48,224,200\nv_mul_u32_u24 v32, 896, v32\nMaaa 80,224,208\n"
"Ra 136,10,34816\nv_and_b32 v33, 3, v0\nMaaa 80,228,212\nD0 6,24,44,0\nMaaa 112,228,220\nRa 140,11,34816\nv_lshl_add_u32 v32, v33, 4, v32\nMaaa 112,224,216\ns_lshr_b32 s2, s11, 1\n"
"Maaa 116,232,216\nRa 144,10,40960\ns_mul_i32 s3, s2, 448\nMaaa 116,236,220\ns_and_b32 s5, s11, 1\nMaaa 84,236,212\nRa 148,11,40960\ns_lshl3_add_u32 s3, s5, s3\nMaaa 84,232,208\n"
"s_add_u32 s3, s3, 131072\nMaaa 52,232,200\nRa 152,10,43008\nv_add_u32 v32, s3, v32\nMaaa 52,236,204\nD0 7,24,44,1024\nMaaa 20,236,196\nRa 156,11,43008\nTv 36,32,0\n"
"Maaa 20,232,192\nTv 38,32,14336\nMaaa 24,240,192\nTv 40,32,64\nTv 42,32,14400\nMaaa 24,244,196\nTv 44,32,128\nMaaa 56,244,204\nTv 46,32,14464\nTv 48,32,192\nMaaa 56,240,200\n"
"Tv 50,32,14528\nMaaa 88,240,208\nTv 52,32,256\nTv 54,32,14592\nMaaa 88,244,212\nD0 8,24,44,2048\nMaaa 120,244,220\nTv 56,32,320\nTv 58,32,14656\nMaaa 120,240,216\nTv 60,32,384\n"
"Maaa 124,248,216\nTv 62,32,14720\ns_lshr_b32 s0, s4, 1\nMaaa 124,252,220\ns_mul_i32 s0, s0, 12\nMaaa 92,252,212\ns_lshl1_add_u32 s0, s5, s0\ns_and_b32 s6, s4, 1\nMaaa 92,248,208\n"
"s_add_u32 s0, s0, s6\nMaaa 60,248,200\ns_lshl3_add_u32 s1, s5, s4\ns_add_u32 s77, s1, 48\nMaaa 60,252,204\nD0 9,24,44,3072\nMaaa 28,252,196\ns_and_b32 s4, s12, 1\n"
"s_mov_b32 s7, 0x6543210\nMaaa 28,248,192\ns_waitcnt vmcnt(17) lgkmcnt(14)\ns_barrier\nMaaa 0,160,192\nTa 224,16,32768\ns_cmp_lg_u32 s4, s2\ns_cselect_b32 s6, s0, s1\n"
"s_cselect_b32 s5, 4, 16\ns_cselect_b32 s7, 0x6352410, s7\ns_cselect_b32 s3, 0, 229376\nMaaa 0,164,196\nTa 226,16,34816\nMaaa 32,164,204\ns_add_u32 m0, s47, -49152\n"
"s_add_u32 s48, s48, 128\nMaaa 32,160,200\nTa 228,16,49152\nMaaa 64,160,208\nTa 230,16,51200\ns_add_u32 s3, s3, s72\nMaaa 64,164,212\nD0 2,20,48,0\nMaaa 96,164,220\n"
"Ta 232,17,32768\ns_add_u32 s3, s3, 0x1400000\nMaaa 96,160,216\nTa 234,17,34816\nMaaa 100,168,216\nWL 14\ns_lshr_b32 s0, s6, 4\nMaaa 100,172,220\nTa 236,17,49152\nMaaa 68,172,212\n"
"Ta 238,17,51200\ns_lshl_b32 s0, s0, 2\nMaaa 68,168,208\ns_lshr_b32 s0, s7, s0\nMaaa 36,168,200\nTa 240,18,32768\ns_and_b32 s0, s0, 7\nMaaa 36,172,204\nTa 242,18,34816\n"
"Maaa 4,172,196\nD0 3,20,48,1024\ns_lshl_b32 s0, s0, 5\nMaaa 4,168,192\nTa 244,18,49152\nMaaa 8,176,192\nTa 246,18,51200\ns_and_b32 s1, s6, 15\nMaaa 8,180,196\n"
"s_add_u32 s0, s0, s1\nMaaa 40,180,204\nTa 248,19,32768\ns_and_b32 s1, s6, 8\nMaaa 40,176,200\nTa 250,19,34816\nMaaa 72,176,208\ns_add_u32 s0, s0, s1\ns_lshl_b32 s0, s0, 10\n"
"Maaa 72,180,212\nTa 252,19,49152\nMaaa 104,180,220\nTa 254,19,51200\ns_add_u32 s0, s0, s3\nMaaa 104,176,216\nD0 4,20,48,2048\nMaaa 108,184,216\nSv 36,1,16,0,0\n"
"s_add_u32 s6, s6, s5\nMaaa 108,188,220\nMaaa 76,188,212\nMaaa 76,184,208\nMaaa 44,184,200\nMaaa 44,188,204\nD0 5,20,48,3072\nMaaa 12,188,196\nMaaa 12,184,192\nWL 0\n"
"Mvaa 80,224,128\nTa 160,12,32768\ns_lshr_b32 s0, s6, 4\nMvaa 80,228,132\nTa 162,12,34816\nMvaa 112,228,140\ns_add_u32 m0, s47, -32768\ns_add_u32 s45, s45, 128\nMvaa 112,224,136\n"
"Ta 164,12,49152\nMvaa 144,224,144\nTa 166,12,51200\ns_lshl_b32 s0, s0, 2\nMvaa 144,228,148\nD0 2,20,45,0\nMvaa 176,228,156\nTa 168,13,32768\ns_lshr_b32 s0, s7, s0\n"
"Mvaa 176,224,152\nTa 170,13,34816\nMvaa 180,232,152\ns_and_b32 s0, s0, 7\ns_lshl_b32 s0, s0, 5\nMvaa 180,236,156\nTa 172,13,49152\nMvaa 148,236,148\nTa 174,13,51200\n"
"s_and_b32 s1, s6, 15\nMvaa 148,232,144\ns_add_u32 s0, s0, s1\nMvaa 116,232,136\nTa 176,14,32768\ns_and_b32 s1, s6, 8\nMvaa 116,236,140\nTa 178,14,34816\nMvaa 84,236,132\n"
"D0 3,20,45,1024\ns_add_u32 s0, s0, s1\nMvaa 84,232,128\nTa 180,14,49152\nMvaa 88,240,128\nTa 182,14,51200\ns_lshl_b32 s0, s0, 10\nMvaa 88,244,132\ns_add_u32 s0, s0, s3\n"
"Mvaa 120,244,140\nTa 184,15,32768\nSv 40,1,16,0,0\nMvaa 120,240,136\nTa 186,15,34816\nMvaa 152,240,144\ns_add_u32 s6, s6, s5\nMvaa 152,244,148\nTa 188,15,49152\nMvaa 184,244,156\n"
"Ta 190,15,51200\nMvaa 184,240,152\nD0 4,20,45,2048\nMvaa 188,248,152\nMvaa 188,252,156\nMvaa 156,252,148\nMvaa 156,248,144\nMvaa 124,248,136\ns_lshr_b32 s0, s6, 4\n"
"s_lshl_b32 s0, s0, 2\nMvaa 124,252,140\nD0 5,20,45,3072\nMvaa 92,252,132\ns_lshr_b32 s0, s7, s0\ns_and_b32 s0, s0, 7\nMvaa 92,248,128\ns_waitcnt vmcnt(18) lgkmcnt(0)\ns_barrier\n"
"Mvaa 64,160,128\nRa 192,10,49152\ns_lshl_b32 s0, s0, 5\nMvaa 64,164,132\ns_add_u32 m0, s47, 32768\ns_add_u32 s44, s44, 0x40000\nMvaa 96,164,140\nRa 196,11,49152\n"
"s_and_b32 s1, s6, 15\nMvaa 96,160,136\ns_add_u32 s0, s0, s1\nMvaa 128,160,144\nRa 200,10,51200\ns_and_b32 s1, s6, 8\nMvaa 128,164,148\nD0 6,24,44,0\nMvaa 160,164,156\n"
"Ra 204,11,51200\ns_add_u32 s0, s0, s1\nMvaa 160,160,152\ns_lshl_b32 s0, s0, 10\nMvaa 164,168,152\nRa 208,10,57344\ns_add_u32 s0, s0, s3\nMvaa 164,172,156\nSv 44,1,16,0,0\n"
"Mvaa 132,172,148\nRa 212,11,57344\ns_add_u32 s6, s6, s5\nMvaa 132,168,144\nMvaa 100,168,136\nRa 216,10,59392\nMvaa 100,172,140\nD0 7,24,44,1024\nMvaa 68,172,132\nRa 220,11,59392\n"
"Mvaa 68,168,128\nMvaa 72,176,128\nMvaa 72,180,132\nMvaa 104,180,140\ns_mov_b32 s6, s77\ns_mov_b32 s5, 16\nMvaa 104,176,136\ns_lshr_b32 s0, s6, 4\nMvaa 136,176,144\n"
"s_lshl_b32 s0, s0, 2\ns_lshr_b32 s0, s7, s0\nMvaa 136,180,148\nD0 8,24,44,2048\nMvaa 168,180,156\ns_and_b32 s0, s0, 7\ns_lshl_b32 s0, s0, 5\nMvaa 168,176,152\n"
"s_and_b32 s1, s6, 15\nMvaa 172,184,152\ns_add_u32 s0, s0, s1\ns_and_b32 s1, s6, 8\nMvaa 172,188,156\ns_add_u32 s0, s0, s1\nMvaa 140,188,148\ns_lshl_b32 s0, s0, 10\n"
"s_add_u32 s0, s0, s3\nMvaa 140,184,144\nSv 48,1,16,0,0\nMvaa 108,184,136\ns_add_u32 s6, s6, s5\nMvaa 108,188,140\nD0 9,24,44,3072\nMvaa 76,188,132\nMvaa 76,184,128\nWL 0\n"
"Maaa 0,160,192\nRa 128,10,0\nMaaa 0,164,196\ns_add_u32 m0, s47, 49152\ns_add_u32 s44, s44, 0x40000\nMaaa 32,164,204\nRa 132,11,0\nMaaa 32,160,200\nMaaa 64,160,208\nRa 136,10,2048\n"
"s_lshr_b32 s0, s6, 4\nMaaa 64,164,212\nD0 6,24,44,0\nMaaa 96,164,220\nRa 140,11,2048\ns_lshl_b32 s0, s0, 2\nMaaa 96,160,216\ns_lshr_b32 s0, s7, s0\nMaaa 100,168,216\n"
"Ra 144,10,8192\ns_and_b32 s0, s0, 7\nMaaa 100,172,220\ns_lshl_b32 s0, s0, 5\nMaaa 68,172,212\nRa 148,11,8192\ns_and_b32 s1, s6, 15\nMaaa 68,168,208\ns_add_u32 s0, s0, s1\n"
"Maaa 36,168,200\nRa 152,10,10240\ns_and_b32 s1, s6, 8\nMaaa 36,172,204\nD0 7,24,44,1024\nMaaa 4,172,196\nRa 156,11,10240\ns_add_u32 s0, s0, s1\nMaaa 4,168,192\n"
"s_lshl_b32 s0, s0, 10\nMaaa 8,176,192\ns_add_u32 s0, s0, s3\nSv 52,1,16,0,0\nMaaa 8,180,196\ns_add_u32 s6, s6, s5\nMaaa 40,180,204\nMaaa 40,176,200\nMaaa 72,176,208\n"
"Maaa 72,180,212\nD0 8,24,44,2048\nMaaa 104,180,220\nMaaa 104,176,216\nMaaa 108,184,216\nMaaa 108,188,220\ns_lshr_b32 s0, s6, 4\nMaaa 76,188,212\ns_lshl_b32 s0, s0, 2\n"
"s_lshr_b32 s0, s7, s0\nMaaa 76,184,208\ns_and_b32 s0, s0, 7\nMaaa 44,184,200\ns_lshl_b32 s0, s0, 5\ns_and_b32 s1, s6, 15\nMaaa 44,188,204\nD0 9,24,44,3072\nMaaa 12,188,196\n"
"s_add_u32 s0, s0, s1\ns_and_b32 s1, s6, 8\nMaaa 12,184,192\nWV 21\ns_barrier\nMaaa 16,224,192\nTa 160,12,0\ns_add_u32 s0, s0, s1\nMaaa 16,228,196\nTa 162,12,2048\nMaaa 48,228,204\n"
"s_add_u32 m0, s47, -16384\ns_add_u32 s48, s48, 128\nMaaa 48,224,200\nTa 164,12,16384\nMaaa 80,224,208\nTa 166,12,18432\ns_lshl_b32 s0, s0, 10\nMaaa 80,228,212\nD0 2,20,48,0\n"
"Maaa 112,228,220\nTa 168,13,0\ns_add_u32 s0, s0, s3\nMaaa 112,224,216\nTa 170,13,2048\nMaaa 116,232,216\nSv 56,1,16,0,0\ns_add_u32 s6, s6, s5\nMaaa 116,236,220\nTa 172,13,16384\n"
"Maaa 84,236,212\nTa 174,13,18432\nMaaa 84,232,208\nMaaa 52,232,200\nTa 176,14,0\nMaaa 52,236,204\nTa 178,14,2048\nMaaa 20,236,196\nD0 3,20,48,1024\nMaaa 20,232,192\n"
"Ta 180,14,16384\nMaaa 24,240,192\nTa 182,14,18432\ns_lshr_b32 s0, s6, 4\nMaaa 24,244,196\ns_lshl_b32 s0, s0, 2\nMaaa 56,244,204\nTa 184,15,0\ns_lshr_b32 s0, s7, s0\n"
"Maaa 56,240,200\nTa 186,15,2048\nMaaa 88,240,208\ns_and_b32 s0, s0, 7\ns_lshl_b32 s0, s0, 5\nMaaa 88,244,212\nTa 188,15,16384\nMaaa 120,244,220\nTa 190,15,18432\n"
"s_and_b32 s1, s6, 15\nMaaa 120,240,216\nD0 4,20,48,2048\nMaaa 124,248,216\ns_add_u32 s0, s0, s1\ns_and_b32 s1, s6, 8\nMaaa 124,252,220\ns_add_u32 s0, s0, s1\nMaaa 92,252,212\n"
"s_lshl_b32 s0, s0, 10\ns_add_u32 s0, s0, s3\nMaaa 92,248,208\nSv 60,1,16,0,0\nMaaa 60,248,200\ns_add_u32 s6, s6, s5\nMaaa 60,252,204\nD0 5,20,48,3072\nMaaa 28,252,196\n"
"Maaa 28,248,192\nWL 0\nMvaa 64,160,128\nTa 224,16,0\nMvaa 64,164,132\nTa 226,16,2048\ns_add_u32 m0, s47, 65536\ns_add_u32 s46, s51, 0\nMvaa 96,164,140\nv_lshrrev_b32 v48, 4, v0\n"
"Mvaa 96,160,136\nTa 228,16,16384\nD0 6,32,46,0\nMvaa 128,160,144\nTa 230,16,18432\nMvaa 128,164,148\nv_and_b32 v49, 7, v0\nMvaa 160,164,156\nTa 232,17,0\nMvaa 160,160,152\n"
"Ta 234,17,2048\nMvaa 164,168,152\nv_xor_b32 v49, v49, v48\nD0 7,32,46,1024\nMvaa 164,172,156\nTa 236,17,16384\nMvaa 132,172,148\nTa 238,17,18432\nMvaa 132,168,144\n"
"v_lshlrev_b32 v49, 4, v49\nMvaa 100,168,136\nTa 240,18,0\nMvaa 100,172,140\nTa 242,18,2048\nD0 8,32,46,2048\nMvaa 68,172,132\nv_lshrrev_b32 v48, 3, v0\nMvaa 68,168,128\n"
"Ta 244,18,16384\nMvaa 72,176,128\nTa 246,18,18432\nMvaa 72,180,132\nv_mul_u32_u24 v48, 896, v48\nMvaa 104,180,140\nTa 248,19,0\nD0 9,32,46,3072\nMvaa 104,176,136\nTa 250,19,2048\n"
"Mvaa 136,176,144\nv_add_u32 v40, v48, v49\nMvaa 136,180,148\nTa 252,19,16384\ns_add_u32 m0, s47, 81920\ns_add_u32 s46, s51, 262144\nMvaa 168,180,156\nTa 254,19,18432\n"
"Mvaa 168,176,152\nv_xor_b32 v49, 64, v40\nD0 6,32,46,0\nMvaa 172,184,152\nv_add_u32 v41, 6144, v49\n.W570:\ns_cbranch_execz .W574\nMvaa 172,188,156\nv_add_u32 v42, 55296, v40\n"
"Mvaa 140,188,148\nv_add_u32 v43, 61440, v49\nMvaa 140,184,144\ns_mov_b32 s2, s13\nMvaa 108,184,136\ns_lshl3_add_u32 s2, s2, s11\nD0 7,32,46,1024\nMvaa 108,188,140\n"
"s_mul_i32 s52, s2, 14336\nMvaa 76,188,132\ns_lshr_b32 s2, s13, 2\ns_bfe_u32 s3, s13, 0x10001\nMvaa 76,184,128\ns_waitcnt vmcnt(23) lgkmcnt(0)\ns_barrier\nMvaa 80,224,128\n"
"Ra 192,10,16384\ns_mul_i32 s3, s3, 17\nMvaa 80,228,132\ns_add_u32 s2, s2, s3\nD0 8,32,46,2048\nMvaa 112,228,140\nRa 196,11,16384\ns_lshl2_add_u32 s3, s14, 0\nMvaa 112,224,136\n"
"s_lshl_b32 s3, s3, 2\nMvaa 144,224,144\nRa 200,10,18432\ns_lshr_b32 s3, 0xe420ca86, s3\nMvaa 144,228,148\ns_and_b32 s3, s3, 15\nMvaa 176,228,156\nRa 204,11,18432\n"
"s_add_u32 s2, s2, s3\nD0 9,32,46,3072\nMvaa 176,224,152\ns_cmp_eq_u32 s2, 32\ns_cselect_b32 s4, 16, s2\nMvaa 180,232,152\nRa 208,10,24576\ns_mul_i32 s75, s4, 65536\n"
"Mvaa 180,236,156\ns_lshr_b32 s2, s13, 2\ns_add_u32 m0, s47, -65536\ns_add_u32 s46, s52, 0\nMvaa 148,236,148\nRa 212,11,24576\ns_bfe_u32 s3, s13, 0x10001\nMvaa 148,232,144\n"
"s_mul_i32 s3, s3, 17\nD0 40,28,46,0\nMvaa 116,232,136\nRa 216,10,26624\ns_add_u32 s2, s2, s3\nMvaa 116,236,140\ns_lshl2_add_u32 s3, s14, 1\nMvaa 84,236,132\nRa 220,11,26624\n"
"s_lshl_b32 s3, s3, 2\nMvaa 84,232,128\ns_lshr_b32 s3, 0xe420ca86, s3\nMvaa 88,240,128\ns_and_b32 s3, s3, 15\ns_add_u32 s2, s2, s3\nD0 41,28,46,1024\nMvaa 88,244,132\n"
"s_cmp_eq_u32 s2, 32\ns_cselect_b32 s4, 16, s2\nMvaa 120,244,140\ns_mul_i32 s76, s4, 65536\ns_lshr_b32 s2, s13, 2\nMvaa 120,240,136\ns_bfe_u32 s3, s13, 0x10001\nMvaa 152,240,144\n"
"s_mul_i32 s3, s3, 17\ns_add_u32 s2, s2, s3\nMvaa 152,244,148\ns_lshl2_add_u32 s3, s14, 2\nD0 42,28,46,2048\nMvaa 184,244,156\ns_lshl_b32 s3, s3, 2\ns_lshr_b32 s3, 0xe420ca86, s3\n"
"Mvaa 184,240,152\ns_and_b32 s3, s3, 15\nMvaa 188,248,152\ns_add_u32 s2, s2, s3\nMvaa 188,252,156\ns_cmp_eq_u32 s2, 32\ns_cselect_b32 s4, 16, s2\nMvaa 156,252,148\n"
"s_mul_i32 s77, s4, 65536\ns_lshr_b32 s2, s13, 2\nD0 43,28,46,3072\nMvaa 156,248,144\ns_bfe_u32 s3, s13, 0x10001\nMvaa 124,248,136\ns_mul_i32 s3, s3, 17\ns_add_u32 s2, s2, s3\n"
"Mvaa 124,252,140\ns_lshl2_add_u32 s3, s14, 3\nMvaa 92,252,132\ns_lshl_b32 s3, s3, 2\ns_lshr_b32 s3, 0xe420ca86, s3\nMvaa 92,248,128\nWL 0\nMaaa 16,224,192\nRa 128,10,32768\n"
"s_and_b32 s3, s3, 15\ns_add_u32 m0, s47, 0\ns_add_u32 s46, s51, 524288\nD0 6,32,46,0\nMaaa 16,228,196\ns_add_u32 s2, s2, s3\ns_cmp_eq_u32 s2, 32\ns_cselect_b32 s4, 16, s2\n"
"Maaa 48,228,204\nRa 132,11,32768\ns_mul_i32 s78, s4, 65536\nMaaa 48,224,200\ns_lshr_b32 s3, s11, 1\nMaaa 80,224,208\nRa 136,10,34816\ns_lshl_b32 s3, s3, 15\nMaaa 80,228,212\n"
"s_add_u32 s2, s72, s3\nD0 7,32,46,1024\nMaaa 112,228,220\nRa 140,11,34816\ns_and_b32 s3, s11, 1\nMaaa 112,224,216\ns_lshl_b32 s3, s3, 12\nMaaa 116,232,216\nRa 144,10,40960\n"
"s_add_u32 s2, s2, s3\nMaaa 116,236,220\ns_add_u32 s2, s2, 0x0\nMaaa 84,236,212\nRa 148,11,40960\ns_mov_b32 s55, 16384\nD0 8,32,46,2048\nMaaa 84,232,208\ns_add_u32 s75, s75, s2\n"
"Maaa 52,232,200\nRa 152,10,43008\ns_add_u32 s76, s76, s2\nMaaa 52,236,204\ns_add_u32 s77, s77, s2\nMaaa 20,236,196\nRa 156,11,43008\ns_add_u32 s78, s78, s2\nMaaa 20,232,192\n"
"v_add_u32 v32, 0x10000, v12\nD0 9,32,46,3072\nMaaa 24,240,192\nv_add_u32 v33, 0x10000, v13\nv_add_u32 v34, 0x10000, v14\nMaaa 24,244,196\nv_add_u32 v35, 0x10000, v15\n"
"Maaa 56,244,204\nv_add_u32 v36, 0x10000, v16\nv_add_u32 v37, 0x10000, v17\ns_add_u32 m0, s47, 16384\ns_add_u32 s46, s51, 786432\nMaaa 56,240,200\nv_add_u32 v38, 0x10000, v18\n"
"Maaa 88,240,208\nv_add_u32 v39, 0x10000, v19\ns_add_u32 s2, s11, 1\nD0 6,32,46,0\nMaaa 88,244,212\ns_lshr_b32 s2, s2, 1\nMaaa 120,244,220\ns_lshl_b32 s2, s2, 12\n"
"v_add_u32 v44, s2, v10\nMaaa 120,240,216\nv_add_u32 v45, s2, v11\nMaaa 124,248,216\nv_xor_b32 v46, 0x1000, v44\nv_xor_b32 v47, 0x1000, v45\nMaaa 124,252,220\n"
"s_lshr_b32 s3, s11, 1\nD0 7,32,46,1024\nMaaa 92,252,212\ns_lshl_b32 s3, s3, 13\ns_lshl_b32 s2, s11, 12\nMaaa 92,248,208\ns_add_u32 s2, s2, s3\nMaaa 60,248,200\n"
"s_add_u32 s2, s2, s72\ns_lshl_b32 s3, s11, 12\nMaaa 60,252,204\ns_add_u32 s89, s3, 0x10000\nMaaa 28,252,196\ns_mov_b32 s88, 0xffff8000\ns_lshl_b32 s4, s13, 1\nD0 8,32,46,2048\n"
"Maaa 28,248,192\ns_waitcnt vmcnt(25) lgkmcnt(0)\ns_barrier\nMaaa 0,160,192\nTa 224,16,32768\ns_sub_u32 s79, 15, s4\nMaaa 0,164,196\nTa 226,16,34816\nMaaa 32,164,204\n"
"s_add_u32 s96, s2, 0x1310000\ns_mov_b32 s63, 0x53800000\nMaaa 32,160,200\nTa 228,16,49152\nD0 9,32,46,3072\nMaaa 64,160,208\nTa 230,16,51200\ns_mov_b32 s64, 0x2b800000\n"
"Maaa 64,164,212\ns_sub_u32 s98, s4, 1\nMaaa 96,164,220\nTa 232,17,32768\ns_lshl_b32 s5, s13, 5\ns_add_u32 m0, s47, -32768\ns_add_u32 s46, s52, 128\nMaaa 96,160,216\n"
"Ta 234,17,34816\nMaaa 100,168,216\ns_add_u32 s5, s5, s11\ns_lshl_b32 s65, s5, 9\nD0 40,28,46,0\nMaaa 100,172,220\nTa 236,17,49152\nMaaa 68,172,212\nTa 238,17,51200\n"
"s_add_u32 s65, s65, s72\nMaaa 68,168,208\ns_add_u32 s65, s65, 0x1580000\nMaaa 36,168,200\nTa 240,18,32768\ns_and_b32 s66, s72, 0x2000\nMaaa 36,172,204\nTa 242,18,34816\n"
"D0 41,28,46,1024\nMaaa 4,172,196\ns_add_u32 s66, s66, s56\ns_add_u32 s66, s66, 0x100000\nMaaa 4,168,192\nTa 244,18,49152\nMaaa 8,176,192\nTa 246,18,51200\ns_and_b32 s5, s12, 1\n"
"Maaa 8,180,196\ns_mul_i32 s58, s5, 0x202000\nMaaa 40,180,204\nTa 248,19,32768\ns_xor_b32 s58, s58, s72\nD0 42,28,46,2048\nMaaa 40,176,200\nTa 250,19,34816\nMaaa 72,176,208\n"
"s_add_u32 s58, s58, 0x15c0000\ns_lshl_b32 s5, s5, 8\nMaaa 72,180,212\nTa 252,19,49152\nMaaa 104,180,220\nTa 254,19,51200\ns_add_u32 s61, s58, s5\nMaaa 104,176,216\n"
"s_lshl4_add_u32 s60, s13, s61\nD0 43,28,46,3072\nMaaa 108,184,216\ns_add_u32 s54, s60, 128\ns_xor_b32 s60, s54, 0x100\nMaaa 108,188,220\ns_cmp_eq_u32 s11, 0\n"
"s_cselect_b32 s54, s54, 0x40000000\nMaaa 76,188,212\ns_getpc_b64 s[8:9]\n.PC282:\ns_add_u32 s8, s8, .HD268-.PC282\ns_addc_u32 s9, s9, 0\ns_add_u32 m0, s47, -49152\n"
"s_add_u32 s46, s52, 256\nMaaa 76,184,208\ns_mov_b32 s97, 0xff800000\nMaaa 44,184,200\ns_mov_b32 s90, 0\nD0 40,28,46,0\nMaaa 44,188,204\ns_cmp_lt_u32 s11, 2\n"
"s_cselect_b32 s92, 2, 0\nMaaa 12,188,196\ns_mul_i32 s4, s79, 65536\ns_sub_u32 s4, s2, s4\nMaaa 12,184,192\nWL 0\nMvaa 80,224,128\nTa 160,12,32768\ns_add_u32 s94, s4, 0x20000\n"
"Mvaa 80,228,132\nTa 162,12,34816\nD0 41,28,46,1024\nMvaa 84,236,132\ns_add_u32 s95, s4, 0x410000\ns_add_u32 s86, s2, 0x130000\nMvaa 84,232,128\nTa 164,12,49152\nMvaa 88,240,128\n"
"Ta 166,12,51200\ns_add_u32 s87, s2, 0x520000\nMvaa 88,244,132\ns_sub_u32 s99, s79, 2\ns_cmp_eq_u32 s13, 7\ns_cselect_b32 s93, 1.0, 0\nMvaa 92,252,132\nTa 168,13,32768\n"
"s_mov_b32 s36, s68\nD0 42,28,46,2048\nMvaa 92,248,128\nTa 170,13,34816\nMvaa 124,248,136\ns_and_b32 s37, s69, 0xffff\ns_mov_b32 s38, 0x1c0000\nMvaa 124,252,140\nTa 172,13,49152\n"
"CV 200,80,81\nMvaa 120,244,140\nTa 174,13,51200\ns_mov_b32 s39, 0x20000\nMvaa 120,240,136\ns_and_b32 s2, s74, 15\nD0 43,28,46,3072\nMvaa 116,232,136\nTa 176,14,32768\n"
"s_min_u32 s2, s2, 13\nMvaa 116,236,140\nTa 178,14,34816\nMvaa 112,228,140\ns_sub_u32 s2, s2, 0\ns_mul_i32 s3, s73, 14\nMvaa 112,224,136\nTa 180,14,49152\nCV 201,82,83\n"
"Mvaa 144,224,144\nTa 182,14,51200\ns_add_u32 s2, s2, s3\nMvaa 144,228,148\ns_lshl2_add_u32 s2, s2, s11\nMvaa 148,236,148\nTa 184,15,32768\ns_mul_i32 s3, s2, 4682\n"
"Mvaa 148,232,144\nTa 186,15,34816\nCV 202,84,85\nMvaa 152,240,144\ns_lshr_b32 s3, s3, 16\ns_mul_i32 s22, s3, 14\nMvaa 152,244,148\nTa 188,15,49152\nCV 203,86,87\nMvaa 156,252,148\n"
"Ta 190,15,51200\ns_sub_u32 s22, s2, s22\nMvaa 156,248,144\ns_and_b32 s21, s3, 28\nMvaa 188,248,152\ns_add_u32 s21, s21, s3\ns_lshl_b32 s21, s21, 4\nMvaa 188,252,156\n"
"s_bfe_u32 s41, s3, 0x30002\ns_cmp_gt_u32 s22, 6\ns_cselect_b32 s2, 1179648, 0\ns_cselect_b32 s1, 16, 0\nMvaa 184,244,156\ns_add_u32 s41, s41, s1\n"
"s_lshl4_add_u32 s41, s41, 0xc048480\nMvaa 184,240,152\ns_and_b32 s1, s3, 28\nCV 204,88,89\nMvaa 180,232,152\ns_add_u32 s3, s3, s1\ns_lshl_b32 s3, s3, 11\nMvaa 180,236,156\n"
"s_add_u32 s2, s2, s3\nMvaa 176,228,156\ns_lshl_b32 s3, s22, 17\ns_add_u32 s2, s2, s3\nMvaa 176,224,152\ns_waitcnt vmcnt(16) lgkmcnt(0)\ns_barrier\nMvaa 64,160,128\n"
"Ra 192,10,49152\ns_add_u32 s20, s2, 0x10e0000\ns_add_u32 m0, s47, 32768\ns_add_u32 s46, s51, 1048576\nMvaa 64,164,132\ns_lshl_b32 s2, s22, 7\nMvaa 68,172,132\nRa 196,11,49152\n"
"s_add_u32 s40, s2, 0xc04b800\nD0 6,32,46,0\nMvaa 68,168,128\ns_mul_i32 s2, s21, 896\nCV 205,90,91\nMvaa 72,176,128\nRa 200,10,51200\ns_lshl_b32 s3, s22, 6\nMvaa 72,180,132\n"
"s_add_u32 s22, s2, s3\nCV 206,92,93\nMvaa 76,188,132\nRa 204,11,51200\ns_add_u32 s23, s22, 57344\nMvaa 76,184,128\nD0 7,32,46,1024\nMvaa 108,184,136\nRa 208,10,57344\n"
"CV 207,94,95\nMvaa 108,188,140\nCV 220,120,121\nCV 221,122,123\nMvaa 104,180,140\nRa 212,11,57344\nMvaa 104,176,136\nCV 222,124,125\nCV 223,126,127\nMvaa 100,168,136\n"
"Ra 216,10,59392\nD0 8,32,46,2048\ns_lshl3_add_u32 s5, s12, s13\ns_lshl2_add_u32 s5, s5, s11\ns_lshl2_add_u32 s5, s5, 0xc04a400\nbuffer_load_dword v27, off, s[16:19], s5 sc0 sc1\n"
"Mvaa 100,172,140\nCV 216,112,113\nMvaa 96,164,140\nRa 220,11,59392\nCV 217,114,115\nMvaa 96,160,136\nCV 218,116,117\nCV 219,118,119\nMvaa 128,160,144\nCV 192,64,65\nCV 193,66,67\n"
"Mvaa 128,164,148\nD0 9,32,46,3072\nMvaa 132,172,148\nCV 194,68,69\nCV 195,70,71\nMvaa 132,168,144\nCV 196,72,73\nCV 197,74,75\nMvaa 136,176,144\ns_add_u32 m0, s47, 49152\n"
"s_add_u32 s46, s51, 1310720\nMvaa 136,180,148\nCV 198,76,77\nCV 199,78,79\nMvaa 140,188,148\nD0 6,32,46,0\nCV 212,104,105\nMvaa 140,184,144\nCV 213,106,107\nMvaa 172,184,152\n"
"CV 214,108,109\nCV 215,110,111\nMvaa 172,188,156\nCV 208,96,97\nCV 209,98,99\nMvaa 168,180,156\nCV 210,100,101\nMvaa 168,176,152\nD0 7,32,46,1024\nCV 211,102,103\n"
"Mvaa 164,168,152\nCV 232,144,145\nMvaa 164,172,156\nCV 233,146,147\nSv 192,1,16,75,0\nMvaa 160,164,156\nCV 234,148,149\nMvaa 160,160,152\nWL 0\nMaaa 0,160,192\nD0 8,32,46,2048\n"
"Ra 128,44,0\nMaaa 0,164,196\nCV 235,150,151\nMaaa 4,172,196\nRa 132,45,0\nMaaa 4,168,192\nCV 236,152,153\nMaaa 8,176,192\nRa 136,44,2048\nMaaa 8,180,196\nD0 9,32,46,3072\n"
"Maaa 12,188,196\nRa 140,45,2048\nSv 196,1,16,75,1024\nMaaa 12,184,192\nCV 237,154,155\nMaaa 44,184,200\nRa 144,46,0\nMaaa 44,188,204\nCV 238,156,157\nMaaa 40,180,204\nRa 148,47,0\n"
"Maaa 40,176,200\nCV 239,158,159\nMaaa 36,168,200\nRa 152,46,2048\nMaaa 36,172,204\nCV 252,184,185\nMaaa 32,164,204\nRa 156,47,2048\nMaaa 32,160,200\nCV 253,186,187\n"
"Sv 200,1,16,75,2048\nMaaa 64,160,208\nCV 254,188,189\nCV 255,190,191\nMaaa 64,164,212\nCV 248,176,177\nMaaa 68,172,212\nCV 249,178,179\nMaaa 68,168,208\nCV 250,180,181\n"
"Maaa 72,176,208\nCV 251,182,183\nMaaa 72,180,212\nCV 224,128,129\nMaaa 76,188,212\nCV 225,130,131\nMaaa 76,184,208\nCV 226,132,133\nMaaa 108,184,216\nCV 227,134,135\n"
"Sv 204,1,16,75,3072\nMaaa 108,188,220\nCV 228,136,137\nMaaa 104,180,220\nCV 229,138,139\nMaaa 104,176,216\nCV 230,140,141\nMaaa 100,168,216\nCV 231,142,143\nMaaa 100,172,220\n"
"CV 244,168,169\nMaaa 96,164,220\nCV 245,170,171\nMaaa 96,160,216\ns_barrier\nMaaa 16,224,192\ns_add_u32 m0, s47, -16384\ns_add_u32 s46, s52, 384\nTa 160,32,0\nMaaa 16,228,196\n"
"Ta 162,32,2048\nMaaa 20,236,196\nD0 40,28,46,0\ns_add_u32 s75, s75, s55\nSv 208,1,16,75,0\nMaaa 20,232,192\nTa 164,32,16384\nMaaa 24,240,192\nTa 166,32,18432\nMaaa 24,244,196\n"
"CV 246,172,173\nMaaa 28,252,196\nTa 168,33,0\nMaaa 28,248,192\nD0 41,28,46,1024\nTa 170,33,2048\nMaaa 60,248,200\nCV 247,174,175\nMaaa 60,252,204\n.W571:\ns_cbranch_execz .W575\n"
"Ta 172,33,16384\nMaaa 56,244,204\nTa 174,33,18432\nMaaa 56,240,200\nCV 240,160,161\nSv 212,1,16,75,1024\nMaaa 52,232,200\nD0 42,28,46,2048\nTa 176,34,0\nMaaa 52,236,204\n"
"Ta 178,34,2048\nMaaa 48,228,204\nCV 241,162,163\nMaaa 48,224,200\nTa 180,34,16384\nMaaa 80,224,208\nTa 182,34,18432\nCV 242,164,165\nMaaa 80,228,212\nD0 43,28,46,3072\n"
"Maaa 84,236,212\nTa 184,35,0\nMaaa 84,232,208\nTa 186,35,2048\nMaaa 88,240,208\nCV 243,166,167\nSv 216,1,16,75,2048\nMaaa 88,244,212\nTa 188,35,16384\nMaaa 92,252,212\n"
"Ta 190,35,18432\nMaaa 92,248,208\nMaaa 124,248,216\nMaaa 124,252,220\nMaaa 120,244,220\nMaaa 120,240,216\nMaaa 116,232,216\nMaaa 116,236,220\nSv 220,1,16,75,3072\n"
"Maaa 112,228,220\nMaaa 112,224,216\nWL 0\nZvaa 64,160,128\nTa 224,36,0\nAR 60,0\nMvaa 64,164,132\nTa 226,36,2048\nZvaa 96,164,140\nAR 61,1\nMvaa 96,160,136\nTa 228,36,16384\n"
"CV 192,60,61\nZvaa 128,160,144\nTa 230,36,18432\nMvaa 128,164,148\nAR 60,2\nAR 61,3\nZvaa 160,164,156\nTa 232,37,0\nSv 224,1,16,76,0\nMvaa 160,160,152\nTa 234,37,2048\n"
"CV 193,60,61\nZvaa 164,168,152\nAR 60,4\nMvaa 164,172,156\nTa 236,37,16384\nZvaa 132,172,148\nTa 238,37,18432\nAR 61,5\nMvaa 132,168,144\nCV 194,60,61\nZvaa 100,168,136\n"
"Ta 240,38,0\nAR 60,6\nMvaa 100,172,140\nTa 242,38,2048\nZvaa 68,172,132\nSv 228,1,16,76,1024\nAR 61,7\nMvaa 68,168,128\nTa 244,38,16384\nZvaa 72,176,128\nTa 246,38,18432\n"
"CV 195,60,61\nMvaa 72,180,132\nAR 60,8\nZvaa 104,180,140\nTa 248,39,0\nMvaa 104,176,136\nTa 250,39,2048\nAR 61,9\nZvaa 136,176,144\nCV 196,60,61\nMvaa 136,180,148\n"
"Ta 252,39,16384\nAR 60,10\nZvaa 168,180,156\nTa 254,39,18432\nSv 232,1,16,76,2048\nMvaa 168,176,152\nAR 61,11\nCV 197,60,61\nZvaa 172,184,152\nAR 60,12\nMvaa 172,188,156\n"
"AR 61,13\nZvaa 140,188,148\nCV 198,60,61\nAR 60,14\nMvaa 140,184,144\nAR 61,15\nZvaa 108,184,136\nCV 199,60,61\nAR 60,16\nMvaa 108,188,140\nSv 236,1,16,76,3072\nZvaa 76,188,132\n"
"AR 61,17\nCV 200,60,61\nMvaa 76,184,128\ns_waitcnt vmcnt(29) lgkmcnt(0)\ns_barrier\nZvaa 80,224,128\nRa 192,44,32768\nAR 60,18\nMvaa 80,228,132\nRa 196,45,32768\n"
"s_add_u32 m0, s47, 65536\ns_add_u32 s46, s51, 1572864\nD0 6,32,46,0\nZvaa 112,228,140\nRa 200,44,34816\nMvaa 112,224,136\nRa 204,45,34816\nAR 61,19\nZvaa 144,224,144\n"
"Ra 208,46,32768\nMvaa 144,228,148\nRa 212,47,32768\nCV 201,60,61\nD0 7,32,46,1024\nZvaa 176,228,156\nRa 216,46,34816\nMvaa 176,224,152\nRa 220,47,34816\nAR 60,20\n"
"Zvaa 180,232,152\nTa 160,12,0\nMvaa 180,236,156\nTa 162,12,2048\nD0 8,32,46,2048\nZvaa 148,236,148\nTa 164,12,16384\nAR 61,21\nMvaa 148,232,144\nTa 166,12,18432\nZvaa 116,232,136\n"
"Ta 168,13,0\nCV 202,60,61\nMvaa 116,236,140\nTa 170,13,2048\nD0 9,32,46,3072\nZvaa 84,236,132\nTa 172,13,16384\nAR 60,22\nMvaa 84,232,128\nTa 174,13,18432\n"
"s_add_u32 m0, s47, 81920\ns_add_u32 s46, s51, 1835008\nZvaa 88,240,128\nTa 176,14,0\nAR 61,23\nMvaa 88,244,132\nTa 178,14,2048\nD0 6,32,46,0\nZvaa 120,244,140\nTa 180,14,16384\n"
"Mvaa 120,240,136\nTa 182,14,18432\nCV 203,60,61\nZvaa 152,240,144\nTa 184,15,0\nMvaa 152,244,148\nTa 186,15,2048\nAR 60,24\nD0 7,32,46,1024\nZvaa 184,244,156\nTa 188,15,16384\n"
"Mvaa 184,240,152\nTa 190,15,18432\nAR 61,25\nZvaa 188,248,152\nCV 204,60,61\nMvaa 188,252,156\nAR 60,26\nD0 8,32,46,2048\nZvaa 156,252,148\nAR 61,27\nCV 205,60,61\n"
"Mvaa 156,248,144\nAR 60,28\nZvaa 124,248,136\nAR 61,29\nCV 206,60,61\nMvaa 124,252,140\nAR 60,30\nD0 9,32,46,3072\nZvaa 92,252,132\nAR 61,31\nCV 207,60,61\nMvaa 92,248,128\nWL 0\n"
"Mvaa 64,160,192\nTa 224,16,0\nAR 60,32\nMvaa 64,164,196\nTa 226,16,2048\ns_add_u32 m0, s47, -65536\ns_add_u32 s46, s52, 512\nD0 40,28,46,0\nMvaa 96,164,204\nAR 61,33\n"
"Mvaa 96,160,200\nTa 228,16,16384\nCV 208,60,61\nMvaa 128,160,208\nTa 230,16,18432\nMvaa 128,164,212\nAR 60,34\nAR 61,35\nD0 41,28,46,1024\nMvaa 160,164,220\nTa 232,17,0\n"
"Mvaa 160,160,216\nTa 234,17,2048\nCV 209,60,61\nMvaa 164,168,216\nAR 60,36\nMvaa 164,172,220\nTa 236,17,16384\nD0 42,28,46,2048\nMvaa 132,172,212\nTa 238,17,18432\nAR 61,37\n"
"Mvaa 132,168,208\nCV 210,60,61\nMvaa 100,168,200\nTa 240,18,0\nAR 60,38\nMvaa 100,172,204\nTa 242,18,2048\nD0 43,28,46,3072\nMvaa 68,172,196\ns_add_u32 s76, s76, s55\n"
"Sv 240,1,16,76,0\nMvaa 68,168,192\nTa 244,18,16384\nMvaa 72,176,192\nTa 246,18,18432\nAR 61,39\nMvaa 72,180,196\nCV 211,60,61\nMvaa 104,180,204\nTa 248,19,0\nMvaa 104,176,200\n"
"Ta 250,19,2048\nSv 244,1,16,76,1024\nMvaa 136,176,208\nAR 60,40\nMvaa 136,180,212\nTa 252,19,16384\nAR 61,41\nMvaa 168,180,220\nTa 254,19,18432\nMvaa 168,176,216\nCV 212,60,61\n"
"AR 60,42\nMvaa 172,184,216\nSv 248,1,16,76,2048\nMvaa 172,188,220\nAR 61,43\nMvaa 140,188,212\nCV 213,60,61\nAR 60,44\nMvaa 140,184,208\nAR 61,45\nMvaa 108,184,200\nCV 214,60,61\n"
"AR 60,46\nMvaa 108,188,204\nSv 252,1,16,76,3072\nMvaa 76,188,196\nAR 61,47\nCV 215,60,61\nMvaa 76,184,192\ns_waitcnt vmcnt(20) lgkmcnt(0)\ns_barrier\nMvaa 80,224,192\n"
"Ra 128,44,16384\nAR 60,48\nv_cmp_ne_u32 vcc, s15, v27\ns_nop 0\ns_cbranch_vccnz .S283\n.S284:\nMvaa 80,228,196\nRa 132,45,16384\n"
"buffer_store_dword v25, off, s[16:19], s54 offset:0 sc0 sc1\ns_add_u32 m0, s47, 0\ns_add_u32 s46, s51, 2097152\nD0 6,32,46,0\nMvaa 112,228,204\nRa 136,44,18432\nMvaa 112,224,200\n"
"Ra 140,45,18432\nAR 61,49\nMvaa 144,224,208\nRa 144,46,16384\nMvaa 144,228,212\nRa 148,47,16384\nCV 216,60,61\nD0 7,32,46,1024\nMvaa 176,228,220\nRa 152,46,18432\n"
"Mvaa 176,224,216\nRa 156,47,18432\nAR 60,50\nMvaa 180,232,216\nTa 160,12,32768\nMvaa 180,236,220\nTa 162,12,34816\nD0 8,32,46,2048\nMvaa 148,236,212\nTa 164,12,49152\nAR 61,51\n"
"Mvaa 148,232,208\nTa 166,12,51200\nMvaa 116,232,200\nTa 168,13,32768\nCV 217,60,61\nMvaa 116,236,204\nTa 170,13,34816\nD0 9,32,46,3072\nMvaa 84,236,196\nTa 172,13,49152\nAR 60,52\n"
"Mvaa 84,232,192\nTa 174,13,51200\ns_add_u32 m0, s47, 16384\ns_add_u32 s46, s51, 2359296\nMvaa 88,240,192\nTa 176,14,32768\nAR 61,53\nMvaa 88,244,196\nTa 178,14,34816\n"
"D0 6,32,46,0\nMvaa 120,244,204\nTa 180,14,49152\nMvaa 120,240,200\nTa 182,14,51200\nCV 218,60,61\nMvaa 152,240,208\nTa 184,15,32768\nMvaa 152,244,212\nTa 186,15,34816\nAR 60,54\n"
"D0 7,32,46,1024\nMvaa 184,244,220\nTa 188,15,49152\nMvaa 184,240,216\nTa 190,15,51200\nAR 61,55\nMvaa 188,248,216\nCV 219,60,61\nMvaa 188,252,220\nAR 60,56\nD0 8,32,46,2048\n"
"Mvaa 156,252,212\nAR 61,57\nCV 220,60,61\nMvaa 156,248,208\nAR 60,58\nMvaa 124,248,200\nAR 61,59\nCV 221,60,61\nMvaa 124,252,204\nAR 60,60\nD0 9,32,46,3072\nMvaa 92,252,196\n"
"AR 61,61\nCV 222,60,61\nMvaa 92,248,192\nWL 0\nMvaa 64,160,128\nTa 224,16,32768\nAR 60,62\nMvaa 64,164,132\nTa 226,16,34816\ns_add_u32 m0, s47, -32768\ns_add_u32 s46, s52, 640\n"
"D0 40,28,46,0\nMvaa 96,164,140\nAR 61,63\nMvaa 96,160,136\nTa 228,16,49152\nCV 223,60,61\nMvaa 128,160,144\nTa 230,16,51200\nMvaa 128,164,148\nD0 41,28,46,1024\nMvaa 160,164,156\n"
"Ta 232,17,32768\nAR 60,126\nMvaa 160,160,152\nTa 234,17,34816\nMvaa 164,168,152\nAR 61,127\nCV 60,60,61\nMvaa 164,172,156\nTa 236,17,49152\nD0 42,28,46,2048\nMvaa 132,172,148\n"
"Ta 238,17,51200\nMvaa 132,168,144\nAW 127,60\nAR 60,124\nMvaa 100,168,136\nTa 240,18,32768\nMvaa 100,172,140\nTa 242,18,34816\nD0 43,28,46,3072\nMvaa 68,172,132\nSv 192,1,16,77,0\n"
"AR 61,125\nMvaa 68,168,128\nTa 244,18,49152\nMvaa 72,176,128\nTa 246,18,51200\nCV 60,60,61\nMvaa 72,180,132\nAW 126,60\nMvaa 104,180,140\nTa 248,19,32768\nMvaa 104,176,136\n"
"Ta 250,19,34816\nSv 196,1,16,77,1024\nMvaa 136,176,144\nAR 60,122\nMvaa 136,180,148\nTa 252,19,49152\nMvaa 168,180,156\nTa 254,19,51200\nAR 61,123\nMvaa 168,176,152\nCV 60,60,61\n"
"Mvaa 172,184,152\nSv 200,1,16,77,2048\nAW 125,60\nMvaa 172,188,156\nAR 60,120\nMvaa 140,188,148\nAR 61,121\nMvaa 140,184,144\nCV 60,60,61\nAW 124,60\nMvaa 108,184,136\nAR 60,118\n"
"Mvaa 108,188,140\nSv 204,1,16,77,3072\nMvaa 76,188,132\nAR 61,119\nCV 60,60,61\nMvaa 76,184,128\ns_waitcnt vmcnt(17) lgkmcnt(0)\ns_barrier\nMvaa 80,224,128\nRa 192,44,49152\n"
"AW 123,60\nMvaa 80,228,132\nRa 196,45,49152\nbuffer_store_dword v25, off, s[16:19], s54 offset:8 sc0 sc1\ns_add_u32 m0, s47, 32768\ns_add_u32 s46, s51, 2621440\nD0 6,32,46,0\n"
"Mvaa 112,228,140\nRa 200,44,51200\nMvaa 112,224,136\nRa 204,45,51200\nAR 60,116\nMvaa 144,224,144\nRa 208,46,49152\nMvaa 144,228,148\nRa 212,47,49152\nD0 7,32,46,1024\n"
"Mvaa 176,228,156\nRa 216,46,51200\nAR 61,117\nMvaa 176,224,152\nRa 220,47,51200\nMvaa 180,232,152\nTa 160,32,0\nCV 60,60,61\nMvaa 180,236,156\nTa 162,32,2048\nD0 8,32,46,2048\n"
"Mvaa 148,236,148\nTa 164,32,16384\nMvaa 148,232,144\nTa 166,32,18432\nAW 122,60\nMvaa 116,232,136\nTa 168,33,0\nMvaa 116,236,140\nTa 170,33,2048\nD0 9,32,46,3072\nMvaa 84,236,132\n"
"Ta 172,33,16384\nAR 60,114\nMvaa 84,232,128\nTa 174,33,18432\n.W572:\ns_cbranch_execz .W576\ns_add_u32 m0, s47, 49152\ns_add_u32 s46, s51, 2883584\nMvaa 88,240,128\nTa 176,34,0\n"
"AR 61,115\nMvaa 88,244,132\nTa 178,34,2048\nD0 6,32,46,0\nMvaa 120,244,140\nTa 180,34,16384\nMvaa 120,240,136\nTa 182,34,18432\nCV 60,60,61\nMvaa 152,240,144\nTa 184,35,0\n"
"Mvaa 152,244,148\nTa 186,35,2048\nD0 7,32,46,1024\nMvaa 184,244,156\nTa 188,35,16384\nAW 121,60\nMvaa 184,240,152\nTa 190,35,18432\nMvaa 188,248,152\nAR 60,112\nAR 61,113\n"
"Mvaa 188,252,156\nD0 8,32,46,2048\nMvaa 156,252,148\nCV 60,60,61\nMvaa 156,248,144\nAW 120,60\nAR 60,110\nMvaa 124,248,136\nAR 61,111\nMvaa 124,252,140\nD0 9,32,46,3072\n"
"Mvaa 92,252,132\nCV 60,60,61\nAW 119,60\nMvaa 92,248,128\nWL 0\nMvaa 64,160,192\nTa 224,36,0\nAR 60,108\nMvaa 64,164,196\nTa 226,36,2048\ns_add_u32 m0, s47, -49152\n"
"s_add_u32 s46, s52, 768\nD0 40,28,46,0\nMvaa 96,164,204\nAR 61,109\nMvaa 96,160,200\nTa 228,36,16384\nCV 60,60,61\nMvaa 128,160,208\nTa 230,36,18432\nMvaa 128,164,212\n"
"D0 41,28,46,1024\nMvaa 160,164,220\nTa 232,37,0\nAW 118,60\nMvaa 160,160,216\nTa 234,37,2048\nMvaa 164,168,216\nAR 60,106\nAR 61,107\nMvaa 164,172,220\nTa 236,37,16384\n"
"D0 42,28,46,2048\nMvaa 132,172,212\nTa 238,37,18432\nMvaa 132,168,208\nCV 60,60,61\nAW 117,60\nMvaa 100,168,200\nTa 240,38,0\nMvaa 100,172,204\nTa 242,38,2048\nD0 43,28,46,3072\n"
"Mvaa 68,172,196\ns_add_u32 s77, s77, s55\nSv 208,1,16,77,0\nMvaa 68,168,192\nTa 244,38,16384\nMvaa 72,176,192\nTa 246,38,18432\nAR 60,104\nMvaa 72,180,196\nAR 61,105\n"
"Mvaa 104,180,204\nTa 248,39,0\nMvaa 104,176,200\nTa 250,39,2048\nSv 212,1,16,77,1024\nMvaa 136,176,208\nCV 60,60,61\nMvaa 136,180,212\nTa 252,39,16384\nMvaa 168,180,220\n"
"Ta 254,39,18432\nAW 116,60\nMvaa 168,176,216\nAR 60,102\nMvaa 172,184,216\nSv 216,1,16,77,2048\nAR 61,103\nMvaa 172,188,220\nCV 60,60,61\nMvaa 140,188,212\nAW 115,60\n"
"Mvaa 140,184,208\nAR 60,100\nAR 61,101\nMvaa 108,184,200\nCV 60,60,61\nMvaa 108,188,204\nSv 220,1,16,77,3072\nMvaa 76,188,196\nAW 114,60\nAR 60,98\nMvaa 76,184,192\n"
"s_waitcnt vmcnt(25) lgkmcnt(0)\ns_barrier\nMvaa 80,224,192\nRa 128,44,0\nv_and_b32 v27, 0x70, v1\nbuffer_load_dword v27, v27, s[16:19], s61 offen offset:128 sc0 sc1\n"
"Mvaa 80,228,196\nRa 132,45,0\ns_add_u32 m0, s47, 65536\ns_add_u32 s46, s51, 3145728\nD0 6,32,46,0\nMvaa 112,228,204\nRa 136,44,2048\nMvaa 112,224,200\nRa 140,45,2048\nAR 61,99\n"
"Mvaa 144,224,208\nRa 144,46,0\nMvaa 144,228,212\nRa 148,47,0\nD0 7,32,46,1024\nMvaa 176,228,220\nRa 152,46,2048\nCV 60,60,61\nMvaa 176,224,216\nRa 156,47,2048\nMvaa 180,232,216\n"
"Ta 160,12,0\nAW 113,60\nMvaa 180,236,220\nTa 162,12,2048\nD0 8,32,46,2048\nMvaa 148,236,212\nTa 164,12,16384\nMvaa 148,232,208\nTa 166,12,18432\nAR 60,96\nMvaa 116,232,200\n"
"Ta 168,13,0\nMvaa 116,236,204\nTa 170,13,2048\nD0 9,32,46,3072\nMvaa 84,236,196\nTa 172,13,16384\nAR 61,97\nMvaa 84,232,192\nTa 174,13,18432\ns_add_u32 m0, s47, 81920\n"
"s_add_u32 s46, s51, 3407872\nMvaa 88,240,192\nTa 176,14,0\nCV 60,60,61\nMvaa 88,244,196\nTa 178,14,2048\nD0 6,32,46,0\nMvaa 120,244,204\nTa 180,14,16384\nMvaa 120,240,200\n"
"Ta 182,14,18432\nAW 112,60\nMvaa 152,240,208\nTa 184,15,0\nMvaa 152,244,212\nTa 186,15,2048\nD0 7,32,46,1024\nMvaa 184,244,220\nTa 188,15,16384\nAR 60,94\nMvaa 184,240,216\n"
"Ta 190,15,18432\nMvaa 188,248,216\nAR 61,95\nCV 60,60,61\nMvaa 188,252,220\nD0 8,32,46,2048\nMvaa 156,252,212\nAW 111,60\nMvaa 156,248,208\nAR 60,92\nAR 61,93\nMvaa 124,248,200\n"
"CV 60,60,61\nMvaa 124,252,204\nD0 9,32,46,3072\nMvaa 92,252,196\nAW 110,60\nAR 60,90\nMvaa 92,248,192\nWL 0\nMvaa 64,160,128\nTa 224,16,0\nAR 61,91\nMvaa 64,164,132\n"
"Ta 226,16,2048\ns_add_u32 s6, s56, 0\nbuffer_load_dwordx4 a[0:3], v1, s[16:19], s6 offen offset:0 sc1\nMvaa 96,164,140\nCV 60,60,61\nMvaa 96,160,136\nTa 228,16,16384\nAW 109,60\n"
"Mvaa 128,160,144\nTa 230,16,18432\nMvaa 128,164,148\nbuffer_load_dwordx4 a[4:7], v1, s[16:19], s6 offen offset:1024 sc1\nMvaa 160,164,156\nTa 232,17,0\nAR 60,88\nMvaa 160,160,152\n"
"Ta 234,17,2048\nMvaa 164,168,152\nAR 61,89\nCV 60,60,61\nMvaa 164,172,156\nTa 236,17,16384\nbuffer_load_dwordx4 a[8:11], v1, s[16:19], s6 offen offset:2048 sc1\nMvaa 132,172,148\n"
"Ta 238,17,18432\nMvaa 132,168,144\nAW 108,60\nAR 60,86\nMvaa 100,168,136\nTa 240,18,0\nMvaa 100,172,140\nTa 242,18,2048\n"
"buffer_load_dwordx4 a[12:15], v1, s[16:19], s6 offen offset:3072 sc1\nMvaa 68,172,132\nAR 61,87\nCV 60,60,61\nMvaa 68,168,128\nTa 244,18,16384\nMvaa 72,176,128\nTa 246,18,18432\n"
"AW 107,60\nMvaa 72,180,132\nAR 60,84\nMvaa 104,180,140\nTa 248,19,0\nMvaa 104,176,136\nTa 250,19,2048\nAR 61,85\nMvaa 136,176,144\nCV 60,60,61\nMvaa 136,180,148\nTa 252,19,16384\n"
"Mvaa 168,180,156\nTa 254,19,18432\nAW 106,60\nMvaa 168,176,152\nAR 60,82\nMvaa 172,184,152\nAR 61,83\nCV 60,60,61\nMvaa 172,188,156\nAW 105,60\nMvaa 140,188,148\nAR 60,80\n"
"Mvaa 140,184,144\nAR 61,81\nCV 60,60,61\nMvaa 108,184,136\nAW 104,60\nMvaa 108,188,140\nAR 60,78\nMvaa 76,188,132\nAR 61,79\nCV 60,60,61\nMvaa 76,184,128\n"
"s_waitcnt vmcnt(12) lgkmcnt(0)\ns_barrier\nMvaa 80,224,128\nRa 192,44,32768\nv_cmp_ne_u32 vcc, s15, v27\ns_nop 0\ns_cbranch_vccnz .S286\n.S287:\ns_sub_u32 s45, s86, 131072\n"
"s_cmp_eq_u32 s79, 1\ns_cselect_b32 s86, s94, s86\ns_add_u32 m0, s47, -65536\ns_add_u32 s46, s45, 0x0\nMvaa 80,228,132\nRa 196,45,32768\nMvaa 112,228,140\nRa 200,44,34816\n"
"D1 1,16,46,0\nMvaa 112,224,136\nRa 204,45,34816\nAW 103,60\nMvaa 144,224,144\nRa 208,46,32768\nMvaa 144,228,148\nRa 212,47,32768\nMvaa 176,228,156\nRa 216,46,34816\n"
"D1 1,16,46,1024\nMvaa 176,224,152\nRa 220,47,34816\nMvaa 180,232,152\nTa 160,12,32768\nAR 60,76\nMvaa 180,236,156\nTa 162,12,34816\nMvaa 148,236,148\nTa 164,12,49152\n"
"D1 1,16,46,2048\nMvaa 148,232,144\nTa 166,12,51200\nAR 61,77\nMvaa 116,232,136\nTa 168,13,32768\nMvaa 116,236,140\nTa 170,13,34816\nMvaa 84,236,132\nTa 172,13,49152\n"
"D1 1,16,46,3072\nMvaa 84,232,128\nTa 174,13,51200\nMvaa 88,240,128\nTa 176,14,32768\ns_add_u32 s6, s56, 4096\nMvaa 88,244,132\nTa 178,14,34816\nMvaa 120,244,140\nTa 180,14,49152\n"
"buffer_load_dwordx4 a[16:19], v1, s[16:19], s6 offen offset:0 sc1\nMvaa 120,240,136\nTa 182,14,51200\nCV 60,60,61\nMvaa 152,240,144\nTa 184,15,32768\nMvaa 152,244,148\n"
"Ta 186,15,34816\nMvaa 184,244,156\nTa 188,15,49152\nbuffer_load_dwordx4 a[20:23], v1, s[16:19], s6 offen offset:1024 sc1\nMvaa 184,240,152\nTa 190,15,51200\nMvaa 188,248,152\n"
"AW 102,60\nAR 60,74\nMvaa 188,252,156\nAR 61,75\nMvaa 156,252,148\nbuffer_load_dwordx4 a[24:27], v1, s[16:19], s6 offen offset:2048 sc1\nMvaa 156,248,144\nCV 60,60,61\nAW 101,60\n"
"Mvaa 124,248,136\nAR 60,72\nMvaa 124,252,140\nAR 61,73\nMvaa 92,252,132\nbuffer_load_dwordx4 a[28:31], v1, s[16:19], s6 offen offset:3072 sc1\nCV 60,60,61\nMvaa 92,248,128\nWL 0\n"
"Mvaa 64,160,192\nTa 224,16,32768\ns_add_u32 s6, s56, 16384\nMvaa 64,164,196\nTa 226,16,34816\nMvaa 96,164,204\nbuffer_load_dwordx4 a[32:35], v1, s[16:19], s6 offen offset:0 sc1\n"
"Mvaa 96,160,200\nTa 228,16,49152\nAW 100,60\nMvaa 128,160,208\nTa 230,16,51200\nMvaa 128,164,212\nAR 60,70\nMvaa 160,164,220\nTa 232,17,32768\n"
"buffer_load_dwordx4 a[36:39], v1, s[16:19], s6 offen offset:1024 sc1\nMvaa 160,160,216\nTa 234,17,34816\nMvaa 164,168,216\nAR 61,71\nCV 60,60,61\nMvaa 164,172,220\n"
"Ta 236,17,49152\nMvaa 132,172,212\nTa 238,17,51200\nbuffer_load_dwordx4 a[40:43], v1, s[16:19], s6 offen offset:2048 sc1\nMvaa 132,168,208\nAW 99,60\nAR 60,68\nMvaa 100,168,200\n"
"Ta 240,18,32768\nMvaa 100,172,204\nTa 242,18,34816\nMvaa 68,172,196\nbuffer_load_dwordx4 a[44:47], v1, s[16:19], s6 offen offset:3072 sc1\nAR 61,69\nMvaa 68,168,192\n"
"Ta 244,18,49152\nMvaa 72,176,192\nTa 246,18,51200\nCV 60,60,61\nMvaa 72,180,196\nAW 98,60\nMvaa 104,180,204\nTa 248,19,32768\nMvaa 104,176,200\nTa 250,19,34816\nAR 60,66\n"
"Mvaa 136,176,208\nAR 61,67\nMvaa 136,180,212\nTa 252,19,49152\nMvaa 168,180,220\nTa 254,19,51200\nCV 60,60,61\nMvaa 168,176,216\nAW 97,60\nMvaa 172,184,216\nAR 60,64\nAR 61,65\n"
"Mvaa 172,188,220\nCV 60,60,61\nMvaa 140,188,212\nAW 96,60\nv_add_u32 v2, 0x0, v1\nMvaa 140,184,208\ns_lshr_b32 s2, s59, 1\nMvaa 108,184,200\nv_add_u32 v24, s2, v2\n"
"v_xor_b32 v26, 0x1000, v24\nMvaa 108,188,204\nv_lshrrev_b32 v20, 4, v0\nMvaa 76,188,196\nv_lshlrev_b32 v20, 2, v20\nv_and_b32 v0, 15, v0\nMvaa 76,184,192\n"
"s_waitcnt vmcnt(16) lgkmcnt(0)\ns_barrier\nMvaa 80,224,192\nRa 128,44,16384\nv_sub_u32 v0, v0, v20\nMvaa 80,228,196\nRa 132,45,16384\n"
"buffer_store_dword v25, off, s[16:19], s54 offset:12 sc0 sc1\ns_add_u32 s6, s56, 20480\nbuffer_load_dwordx4 a[48:51], v1, s[16:19], s6 offen offset:0 sc1\nMvaa 112,228,204\n"
"Ra 136,44,18432\ns_lshl_b32 s2, s11, 4\nMvaa 112,224,200\nRa 140,45,18432\nMvaa 144,224,208\nRa 144,46,16384\nv_add_u32 v3, s2, v0\nMvaa 144,228,212\nRa 148,47,16384\n"
"buffer_load_dwordx4 a[52:55], v1, s[16:19], s6 offen offset:1024 sc1\nMvaa 176,228,220\nRa 152,46,18432\nv_subrev_u32 v0, 32, v3\nMvaa 176,224,216\nRa 156,47,18432\n"
"Mvaa 180,232,216\nTa 160,32,0\ns_lshl_b32 s2, s11, 13\nMvaa 180,236,220\nTa 162,32,2048\nbuffer_load_dwordx4 a[56:59], v1, s[16:19], s6 offen offset:2048 sc1\nMvaa 148,236,212\n"
"Ta 164,32,16384\ns_add_u32 s2, s2, 0x20000\nMvaa 148,232,208\nTa 166,32,18432\nMvaa 116,232,200\nTa 168,33,0\nv_add_u32 v22, s2, v1\nMvaa 116,236,204\nTa 170,33,2048\n"
"buffer_load_dwordx4 a[60:63], v1, s[16:19], s6 offen offset:3072 sc1\nMvaa 84,236,196\nTa 172,33,16384\nv_xor_b32 v23, 0x2000, v22\nMvaa 84,232,192\nTa 174,33,18432\n"
"s_add_u32 m0, s47, -32768\ns_add_u32 s46, s45, 0x8000\nMvaa 88,240,192\nTa 176,34,0\nv_mov_b32 v4, 0\nMvaa 88,244,196\nTa 178,34,2048\nD1 1,16,46,0\nMvaa 120,244,204\n"
"Ta 180,34,16384\nv_mov_b32 v5, 0\nMvaa 120,240,200\nTa 182,34,18432\nMvaa 152,240,208\nTa 184,35,0\nv_mov_b32 v6, 0\nMvaa 152,244,212\nTa 186,35,2048\nD1 1,16,46,1024\n"
"Mvaa 184,244,220\nTa 188,35,16384\nv_mov_b32 v7, 0\nMvaa 184,240,216\nTa 190,35,18432\nMvaa 188,248,216\n.W573:\ns_cbranch_execz .W577\nv_mov_b32 v8, 0\nv_mov_b32 v9, 0\n"
"Mvaa 188,252,220\nD1 1,16,46,2048\nMvaa 156,252,212\nv_mov_b32 v10, 0\nv_mov_b32 v11, 0\nMvaa 156,248,208\nv_mov_b32 v12, 0\nMvaa 124,248,200\nv_mov_b32 v13, 0\nv_mov_b32 v14, 0\n"
"Mvaa 124,252,204\nD1 1,16,46,3072\nMvaa 92,252,196\nv_mov_b32 v15, 0\nv_mov_b32 v16, 0\nMvaa 92,248,192\nWL 0\nMvaa 64,160,128\nTa 224,36,0\nv_mov_b32 v17, 0\nMvaa 64,164,132\n"
"Ta 226,36,2048\ns_add_u32 m0, s47, -16384\ns_add_u32 s46, s45, 0x400000\nD1 1,16,46,0\nMvaa 68,172,132\nv_mov_b32 v18, 0\nv_mov_b32 v19, 0\nMvaa 68,168,128\nTa 228,36,16384\n"
"Mvaa 72,176,128\nTa 230,36,18432\nMvaa 72,180,132\nD1 1,16,46,1024\nMvaa 76,188,132\nTa 232,37,0\nMvaa 76,184,128\nTa 234,37,2048\nMvaa 108,184,136\nv_mul_f32 v64, s57, v64\n"
"v_mul_f32 v65, s57, v65\nMvaa 108,188,140\nTa 236,37,16384\nD1 1,16,46,2048\nMvaa 104,180,140\nTa 238,37,18432\nCV 64,64,65\nMvaa 104,176,136\nv_mul_f32 v66, s57, v66\n"
"v_mul_f32 v67, s57, v67\nMvaa 100,168,136\nTa 240,38,0\nCV 65,66,67\nMvaa 100,172,140\nTa 242,38,2048\nD1 1,16,46,3072\nMvaa 96,164,140\nv_mul_f32 v68, s57, v68\n"
"v_mul_f32 v69, s57, v69\nMvaa 96,160,136\nTa 244,38,16384\nCV 66,68,69\nMvaa 128,160,144\nTa 246,38,18432\nv_mul_f32 v70, s57, v70\nMvaa 128,164,148\nv_mul_f32 v71, s57, v71\n"
"Mvaa 132,172,148\nTa 248,39,0\nCV 67,70,71\nMvaa 132,168,144\nTa 250,39,2048\nAW 192,64\nMvaa 136,176,144\nAW 193,65\nAW 194,66\nMvaa 136,180,148\nTa 252,39,16384\nAW 195,67\n"
"Mvaa 140,188,148\nTa 254,39,18432\nv_mul_f32 v72, s57, v72\nMvaa 140,184,144\nv_mul_f32 v73, s57, v73\nCV 72,72,73\nMvaa 172,184,152\nv_mul_f32 v74, s57, v74\n"
"v_mul_f32 v75, s57, v75\nMvaa 172,188,156\nCV 73,74,75\nv_mul_f32 v76, s57, v76\nMvaa 168,180,156\nv_mul_f32 v77, s57, v77\nCV 74,76,77\nMvaa 168,176,152\nv_mul_f32 v78, s57, v78\n"
"v_mul_f32 v79, s57, v79\nMvaa 164,168,152\nCV 75,78,79\nAW 200,72\nMvaa 164,172,156\nAW 201,73\nAW 202,74\nMvaa 160,164,156\nAW 203,75\nv_mul_f32 v96, s57, v96\nMvaa 160,160,152\n"
"s_waitcnt vmcnt(21) lgkmcnt(0)\ns_barrier\nMvaa 80,224,128\nv_mul_f32 v97, s57, v97\nCV 96,96,97\nMvaa 80,228,132\ns_add_u32 m0, s47, 0\ns_add_u32 s46, s86, 0xffff0000\n"
"D1 1,16,46,0\nMvaa 84,236,132\nv_mul_f32 v98, s57, v98\nv_mul_f32 v99, s57, v99\nMvaa 84,232,128\nCV 97,98,99\nv_mul_f32 v100, s57, v100\nMvaa 88,240,128\n"
"v_mul_f32 v101, s57, v101\nCV 98,100,101\nMvaa 88,244,132\nD1 1,16,46,1024\nv_mul_f32 v102, s57, v102\nMvaa 92,252,132\nv_mul_f32 v103, s57, v103\nCV 99,102,103\nMvaa 92,248,128\n"
"AW 196,96\nAW 197,97\nMvaa 124,248,136\nAW 198,98\nAW 199,99\nMvaa 124,252,140\nD1 1,16,46,2048\nv_mul_f32 v128, s57, v128\nMvaa 120,244,140\nv_mul_f32 v129, s57, v129\n"
"CV 128,128,129\nMvaa 120,240,136\nv_mul_f32 v130, s57, v130\nv_mul_f32 v131, s57, v131\nMvaa 116,232,136\nRa 160,24,0\nCV 129,130,131\nMvaa 116,236,140\nD1 1,16,46,3072\n"
"v_mul_f32 v132, s57, v132\nMvaa 112,228,140\nRa 164,24,1024\nv_mul_f32 v133, s57, v133\nMvaa 112,224,136\ns_add_u32 m0, s47, 16384\ns_add_u32 s46, s45, 0x408000\nMvaa 144,224,144\n"
"Ra 168,24,2048\nCV 130,132,133\nMvaa 144,228,148\nD1 1,16,46,0\nMvaa 148,236,148\nRa 172,24,3072\nv_mul_f32 v134, s57, v134\nMvaa 148,232,144\nv_mul_f32 v135, s57, v135\n"
"CV 131,134,135\nMvaa 152,240,144\nRa 176,26,0\nds_write_b128 v23, v[128:131] offset:0\nMvaa 152,244,148\nD1 1,16,46,1024\nv_mul_f32 v80, s57, v80\nMvaa 156,252,148\n"
"Ra 180,26,1024\nv_mul_f32 v81, s57, v81\nMvaa 156,248,144\nCV 80,80,81\nv_mul_f32 v82, s57, v82\nMvaa 188,248,152\nRa 184,26,2048\nv_mul_f32 v83, s57, v83\nMvaa 188,252,156\n"
"D1 1,16,46,2048\nCV 81,82,83\nMvaa 184,244,156\nRa 188,26,3072\nv_mul_f32 v84, s57, v84\nMvaa 184,240,152\nv_mul_f32 v85, s57, v85\nCV 82,84,85\nMvaa 180,232,152\n"
"v_mul_f32 v86, s57, v86\nv_mul_f32 v87, s57, v87\nMvaa 180,236,156\nD1 1,16,46,3072\nCV 83,86,87\nMvaa 176,228,156\nAW 208,80\nAW 209,81\nMvaa 176,224,152\n"
"s_waitcnt vmcnt(25) lgkmcnt(5)\nCvaa 32,160,0,4\nRa 128,24,8192\nAW 210,82\nMvaa 32,164,8\nAW 211,83\nMvaa 32,168,16\nRa 132,24,9216\nv_mul_f32 v88, s57, v88\nMvaa 32,172,24\n"
"v_mul_f32 v89, s57, v89\nCV 88,88,89\nCvaa 36,172,28,8\nRa 136,24,10240\nv_mul_f32 v90, s57, v90\nMvaa 36,168,20\ns_add_u32 m0, s89, 32768\nv_mul_f32 v91, s57, v91\n"
"Mvaa 36,164,12\nRa 140,24,11264\ns_waitcnt vmcnt(16) lgkmcnt(4)\ns_barrier\nMvaa 36,160,4\nCV 89,90,91\nv_mul_f32 v92, s57, v92\nMvaa 36,176,36\nRa 144,26,8192\nSa 96,1,16,78,0\n"
"Mvaa 36,180,44\ns_add_u32 s46, s86, s88\nv_mul_f32 v93, s57, v93\nMvaa 36,184,52\nRa 148,26,9216\nCV 90,92,93\nMvaa 36,188,60\nv_mul_f32 v94, s57, v94\nv_mul_f32 v95, s57, v95\n"
"Mvaa 32,188,56\nRa 152,26,10240\nCV 91,94,95\nMvaa 32,184,48\nD1 1,16,46,0\nAW 216,88\nMvaa 32,180,40\nRa 156,26,11264\nWL 4\nMvaa 32,176,32\nAW 217,89\nAW 218,90\n"
"Cvaa 40,128,0,12\nSa 100,1,16,78,1024\nAW 219,91\nMvaa 40,132,8\nv_mov_b32 v86, 0\nMvaa 40,136,16\nv_mov_b32 v87, 0\nv_mov_b32 v88, 0\nMvaa 40,140,24\nv_mov_b32 v89, 0\n"
"v_mul_f32 v136, s57, v136\nv_mul_f32 v137, s57, v137\nCV 136,136,137\nv_mul_f32 v138, s57, v138\nv_mul_f32 v139, s57, v139\nCV 137,138,139\nCvaa 44,140,28,16\n"
"v_and_b32 v27, 0x70, v1\nbuffer_load_dword v27, v27, s[16:19], s61 offen offset:136 sc0 sc1\nv_mul_f32 v140, s57, v140\nv_mul_f32 v141, s57, v141\nCV 138,140,141\n"
"v_mul_f32 v142, s57, v142\nv_mul_f32 v143, s57, v143\nMvaa 44,136,20\nD1 1,16,46,1024\nCV 139,142,143\nds_write_b128 v23, v[136:139] offset:2048\nv_mul_f32 v144, s57, v144\n"
"v_mul_f32 v145, s57, v145\nCV 144,144,145\nv_mul_f32 v146, s57, v146\nMvaa 44,132,12\nWL 1\nv_mul_f32 v147, s57, v147\nCV 145,146,147\nv_mul_f32 v148, s57, v148\n"
"v_mul_f32 v149, s57, v149\nCV 146,148,149\nMvaa 44,128,4\nv_mul_f32 v150, s57, v150\nv_mul_f32 v151, s57, v151\nCV 147,150,151\nds_write_b128 v23, v[144:147] offset:4096\n"
"v_mul_f32 v152, s57, v152\nv_mul_f32 v153, s57, v153\nCV 152,152,153\nMvaa 44,144,36\nSa 104,1,16,78,2048\nv_mul_f32 v154, s57, v154\nv_mul_f32 v155, s57, v155\nCV 153,154,155\n"
"v_mul_f32 v156, s57, v156\nv_mul_f32 v157, s57, v157\nCV 154,156,157\nMvaa 44,148,44\ns_add_u32 s88, s88, 32768\nEX 80,32\nv_mul_f32 v158, s57, v158\nv_mul_f32 v159, s57, v159\n"
"CV 155,158,159\nds_write_b128 v23, v[152:155] offset:6144\nMvaa 44,152,52\nv_mul_f32 v160, s57, v160\nv_mul_f32 v161, s57, v161\nCV 160,160,161\nv_mul_f32 v162, s57, v162\n"
"v_mul_f32 v163, s57, v163\nCV 161,162,163\nv_mul_f32 v164, s57, v164\nMvaa 44,156,60\nEX 81,33\nv_mul_f32 v165, s57, v165\nCV 162,164,165\nv_mul_f32 v166, s57, v166\n"
"Mvaa 40,156,56\nv_mul_f32 v167, s57, v167\nCV 163,166,167\nds_write_b128 v23, v[160:163] offset:1024\nv_mul_f32 v168, s57, v168\nv_mul_f32 v169, s57, v169\nCV 168,168,169\n"
"v_mul_f32 v170, s57, v170\nMvaa 40,152,48\nD1 1,16,46,2048\nv_mul_f32 v171, s57, v171\nCV 169,170,171\nv_mul_f32 v172, s57, v172\nv_mul_f32 v173, s57, v173\nCV 170,172,173\n"
"v_mul_f32 v174, s57, v174\nMvaa 40,148,40\nv_mul_f32 v175, s57, v175\nCV 171,174,175\nds_write_b128 v23, v[168:171] offset:3072\nv_mul_f32 v176, s57, v176\n"
"v_mul_f32 v177, s57, v177\nCV 176,176,177\nv_mul_f32 v178, s57, v178\nMvaa 40,144,32\nAD 84,80,81\nCV 64,80,81\nv_mul_f32 v179, s57, v179\nCV 177,178,179\n"
"v_mul_f32 v180, s57, v180\nv_mul_f32 v181, s57, v181\nCV 178,180,181\nSa 108,1,16,78,3072\nv_mul_f32 v182, s57, v182\nv_mul_f32 v183, s57, v183\nCV 179,182,183\n"
"ds_write_b128 v23, v[176:179] offset:5120\nv_mul_f32 v184, s57, v184\nv_mul_f32 v185, s57, v185\nEX 82,34\nCV 184,184,185\nv_mul_f32 v186, s57, v186\nv_mul_f32 v187, s57, v187\n"
"CV 185,186,187\nv_mul_f32 v188, s57, v188\nv_mul_f32 v189, s57, v189\nCV 186,188,189\nv_mul_f32 v190, s57, v190\nv_mul_f32 v191, s57, v191\nCV 187,190,191\n"
"ds_write_b128 v23, v[184:187] offset:7168\ns_waitcnt vmcnt(16) lgkmcnt(0)\nEX 83,35\nAD 84,84,82\nD1 1,16,46,3072\ns_add_u32 m0, s89, 49152\ns_barrier\nAD 84,84,83\nCV 65,82,83\n"
"Ra 128,24,32768\nRa 224,22,0\nRa 228,22,1024\nRa 232,22,2048\ns_add_u32 s46, s87, s88\nEX 80,36\nRa 132,24,33792\nRa 236,22,3072\nRa 240,22,4096\nEX 81,37\nRa 136,24,34816\n"
"Ra 244,22,5120\nRa 248,22,6144\nRa 252,22,7168\nD1 1,16,46,0\nv_mul_f32 v104, s57, v104\nv_mul_f32 v105, s57, v105\nRa 140,24,35840\nCV 104,104,105\nv_mul_f32 v106, s57, v106\n"
"AD 85,80,81\nCV 68,80,81\nv_mul_f32 v107, s57, v107\nRa 144,26,32768\nCV 105,106,107\nv_mul_f32 v108, s57, v108\nv_mul_f32 v109, s57, v109\nEX 82,38\nCV 106,108,109\n"
"Ra 148,26,33792\nv_mul_f32 v110, s57, v110\nv_mul_f32 v111, s57, v111\nEX 83,39\nRa 152,26,34816\nAD 85,85,82\nCV 107,110,111\nAW 204,104\nD1 1,16,46,1024\nAW 205,105\nAW 206,106\n"
"Ra 156,26,35840\nAW 207,107\nv_mul_f32 v112, s57, v112\nAD 85,85,83\nCV 69,82,83\nv_mul_f32 v113, s57, v113\nRa 160,24,40960\nCV 112,112,113\nv_mul_f32 v114, s57, v114\n"
"v_mul_f32 v115, s57, v115\ns_xor_b32 s89, s89, 0x10000\nEX 80,40\nRa 164,24,41984\nCV 113,114,115\nv_mul_f32 v116, s57, v116\nWL 6\nEX 81,41\nRa 168,24,43008\nAD 84,84,80\n"
"v_mul_f32 v117, s57, v117\nCV 114,116,117\nD1 1,16,46,2048\nv_mul_f32 v118, s57, v118\nv_mul_f32 v119, s57, v119\nRa 172,24,44032\nCV 115,118,119\nAW 212,112\nAD 84,84,81\n"
"CV 66,80,81\nAW 213,113\nCvaa 48,128,0,4\nRa 176,26,40960\nAW 214,114\nAW 215,115\nv_mul_f32 v120, s57, v120\nMvaa 48,132,8\nEX 82,42\nv_mul_f32 v121, s57, v121\nMvaa 48,136,16\n"
"Ra 180,26,41984\nCV 120,120,121\nv_mul_f32 v122, s57, v122\nMvaa 48,140,24\nWL 6\nEX 83,43\nWV 8\nv_cmp_ne_u32 vcc, s15, v27\ns_nop 0\ns_cbranch_vccnz .S289\n.S290:\n"
"Cvaa 52,140,28,8\nRa 184,26,43008\nAD 84,84,82\nv_mul_f32 v123, s57, v123\nCV 121,122,123\nMvaa 52,136,20\nD1 1,16,46,3072\ns_add_u32 m0, s89, 0\nv_mul_f32 v124, s57, v124\n"
"Mvaa 52,132,12\nRa 188,26,44032\nv_mul_f32 v125, s57, v125\nCV 122,124,125\nMvaa 52,128,4\nAD 84,84,83\nCV 67,82,83\nv_mul_f32 v126, s57, v126\nMvaa 52,144,36\nRa 128,2,49152\n"
"s_add_u32 s78, s78, s55\nSa 112,1,16,78,0\nv_mul_f32 v127, s57, v127\nMvaa 52,148,44\ns_add_u32 s46, s86, s88\nEX 80,44\nMvaa 52,152,52\nRa 132,2,50176\n"
"v_xor_b32 v24, 0x10000, v24\nCV 123,126,127\nMvaa 52,156,60\nWL 6\nEX 81,45\nMvaa 48,156,56\nRa 136,2,51200\nAD 85,85,80\nAW 220,120\nAW 221,121\nMvaa 48,152,48\nD1 1,16,46,0\n"
"AW 222,122\nAW 223,123\nMvaa 48,148,40\nRa 140,2,52224\nv_mov_b32 v115, -1\nMvaa 48,144,32\nAD 85,85,81\nCV 70,80,81\nCvaa 56,160,0,12\nRa 144,2,53248\nSa 116,1,16,78,1024\n"
"Mvaa 56,164,8\nEX 82,46\nMvaa 56,168,16\nRa 148,2,54272\nv_xor_b32 v26, 0x10000, v26\nMvaa 56,172,24\nWL 6\nEX 83,47\nCvaa 60,172,28,16\nRa 152,2,55296\nAD 85,85,82\n"
"Mvaa 60,168,20\nD1 1,16,46,1024\nMvaa 60,164,12\nRa 156,2,56320\nMvaa 60,160,4\nAD 85,85,83\nCV 71,82,83\nMvaa 60,176,36\nRa 160,2,57344\nv_max_f32 v90, v84, v85\nMvaa 60,180,44\n"
"s_add_u32 s88, s88, 32768\nEX 80,48\nMvaa 60,184,52\nRa 164,2,58368\nv_cmp_lt_f32 vcc, s63, v90\nMvaa 60,188,60\nWL 8\nEX 81,49\nMvaa 56,188,56\nRa 168,2,59392\n"
"s_cbranch_vccnz .T278\n.T279:\nv_add_f32 v20, s93, v84\nv_mov_b32 v21, v85\nv_mov_b32 v22, v20\nv_mov_b32 v23, v21\nMvaa 56,184,48\nD1 1,16,46,2048\n"
"v_permlane32_swap_b32 v20, v22\nv_permlane32_swap_b32 v21, v23\nAD 20,20,22\nAD 21,21,23\nv_mov_b32 v22, v20\nv_mov_b32 v23, v21\nMvaa 56,180,40\nRa 172,2,60416\n"
"v_permlane16_swap_b32 v20, v22\nv_permlane16_swap_b32 v21, v23\nAD 20,20,22\nAD 21,21,23\nv_min_f32 v90, v20, v21\nv_cmp_gt_f32 vcc, s64, v90\ns_nop 0\ns_cbranch_vccnz .T280\n"
".T281:\nAD 86,86,84\nMvaa 56,176,32\nAD 84,80,81\nCV 72,80,81\nZvav 128,128,64\nRa 176,2,61440\nSa 120,1,16,78,2048\nZvav 132,128,68\nEX 82,50\nZvav 140,132,68\nRa 180,2,62464\n"
"Zvav 136,132,64\nWL 8\nEX 83,51\nZvav 144,136,64\nRa 184,2,63488\nAD 84,84,82\nZvav 148,136,68\nD1 1,16,46,3072\ns_add_u32 m0, s89, 16384\nZvav 156,140,68\nRa 188,2,64512\n"
"s_barrier\nZvav 152,140,64\nAD 84,84,83\nCV 73,82,83\nZvav 160,144,64\nRa 128,24,0\nSa 124,1,16,78,3072\nZvav 164,144,68\ns_add_u32 s46, s87, s88\nEX 80,52\nZvav 172,148,68\n"
"Ra 132,24,1024\nv_xor_b32 v2, 0x10000, v2\nZvav 168,148,64\nWL 8\nEX 81,53\nZvav 176,152,64\nRa 136,24,2048\nAD 87,87,85\nZvav 180,152,68\nD1 1,16,46,0\nZvav 188,156,68\n"
"Ra 140,24,3072\nZvav 184,156,64\nAD 85,80,81\nCV 76,80,81\n.W574:\ns_cbranch_execz .W578\nZvav 192,160,64\nRa 144,26,0\nZvav 196,160,68\ns_cmp_lt_u32 s99, s92\nEX 82,54\n"
"Zvav 204,164,68\nRa 148,26,1024\ns_cbranch_scc0 .E276\nv_cmpx_gt_i32 vcc, 19, v3\nv_mov_b32 v15, s97\nv_cmpx_gt_i32 vcc, 18, v3\nv_mov_b32 v14, s97\nv_cmpx_gt_i32 vcc, 17, v3\n"
"v_mov_b32 v13, s97\nv_cmpx_gt_i32 vcc, 16, v3\nv_mov_b32 v12, s97\nv_cmpx_gt_i32 vcc, 3, v3\nv_mov_b32 v7, s97\nv_cmpx_gt_i32 vcc, 2, v3\nv_mov_b32 v6, s97\n"
"v_cmpx_gt_i32 vcc, 1, v3\nv_mov_b32 v5, s97\nv_cmpx_gt_i32 vcc, 0, v3\nv_mov_b32 v4, s97\ns_mov_b64 exec, -1\ns_nop 2\n.E276:\ns_nop 0\nZvav 200,164,64\nWL 8\nEX 83,55\n"
"Zvav 208,168,64\nRa 152,26,2048\nAD 85,85,82\nZvav 212,168,68\nD1 1,16,46,1024\nZvav 220,172,68\nRa 156,26,3072\nZvav 216,172,64\nAD 85,85,83\nCV 77,82,83\nZvav 224,176,64\n"
"Ra 160,24,8192\nZvav 228,176,68\nEX 80,56\nZvav 236,180,68\nRa 164,24,9216\nZvav 232,180,64\nWL 6\nEX 81,57\nZvav 240,184,64\nRa 168,24,10240\nAD 84,84,80\n"
"v_and_b32 v27, 0x70, v1\nbuffer_load_dword v27, v27, s[16:19], s61 offen offset:140 sc0 sc1\nZvav 244,184,68\nD1 1,16,46,2048\nZvav 252,188,68\nRa 172,24,11264\nZvav 248,188,64\n"
"AD 84,84,81\nCV 74,80,81\ns_sub_u32 s99, s99, 1\n.p2align 6\n.A304:\nCvaa 32,128,0,4\nRa 176,26,8192\nMvaa 32,132,8\ns_add_u32 s49, s49, 131072\nEX 82,58\nMvaa 32,136,16\n"
"Ra 180,26,9216\nMvaa 32,140,24\nWL 6\nEX 83,59\nCvaa 36,140,28,8\nRa 184,26,10240\nAD 84,84,82\nMvaa 36,136,20\nD1 1,16,46,3072\ns_add_u32 m0, s89, 32768\nMvaa 36,132,12\n"
"Ra 188,26,11264\nMvaa 36,128,4\nAD 84,84,83\nCV 75,82,83\nMvaa 36,144,36\nRa 128,2,16384\nMvaa 36,148,44\ns_add_u32 s46, s86, s88\nEX 80,60\nMvaa 36,152,52\nRa 132,2,17408\n"
"Mvaa 36,156,60\nWL 6\nEX 81,61\nMvaa 32,156,56\nRa 136,2,18432\nAD 85,85,80\nMvaa 32,152,48\nD1 1,16,46,0\nMvaa 32,148,40\nRa 140,2,19456\nMvaa 32,144,32\nAD 85,85,81\n"
"CV 78,80,81\nCvaa 40,160,0,12\nRa 144,2,20480\nMvaa 40,164,8\nSv 112,1,16,49,0\nEX 82,62\nMvaa 40,168,16\nRa 148,2,21504\nMvaa 40,172,24\nWL 6\nEX 83,63\nCvaa 44,172,28,16\n"
"Ra 152,2,22528\nAD 85,85,82\nMvaa 44,168,20\nD1 1,16,46,1024\nMvaa 44,164,12\nRa 156,2,23552\nMvaa 44,160,4\nAD 85,85,83\nCV 79,82,83\nMvaa 44,176,36\nRa 160,2,24576\n"
"v_max_f32 v90, v84, v85\nMvaa 44,180,44\ns_add_u32 s88, s88, 32768\nEX 80,32\nMvaa 44,184,52\nRa 164,2,25600\nv_cmp_lt_f32 vcc, s63, v90\nMvaa 44,188,60\nWL 8\nEX 81,33\n"
"Mvaa 40,188,56\nRa 168,2,26624\ns_cbranch_vccnz .T312\n.T313:\nMvaa 40,184,48\nD1 1,16,46,2048\nMvaa 40,180,40\nRa 172,2,27648\nAD 86,86,84\nMvaa 40,176,32\nAD 84,80,81\n"
"CV 64,80,81\nMvav 128,128,72\nRa 176,2,28672\nMvav 132,128,76\nEX 82,34\nMvav 140,132,76\nRa 180,2,29696\nMvav 136,132,72\ns_waitcnt vmcnt(13) lgkmcnt(8)\nEX 83,35\n"
"Mvav 144,136,72\nRa 184,2,30720\nAD 84,84,82\nMvav 148,136,76\nD1 1,16,46,3072\ns_add_u32 m0, s89, 49152\nMvav 156,140,76\nRa 188,2,31744\ns_barrier\nMvav 152,140,72\nAD 84,84,83\n"
"CV 65,82,83\nMvav 160,144,72\nRa 128,24,32768\nMvav 164,144,76\ns_add_u32 s46, s87, s88\nEX 80,36\nMvav 172,148,76\nRa 132,24,33792\nMvav 168,148,72\nWL 8\nEX 81,37\n"
"Mvav 176,152,72\nRa 136,24,34816\nAD 87,87,85\nMvav 180,152,76\nD1 1,16,46,0\nMvav 188,156,76\nRa 140,24,35840\nMvav 184,156,72\nAD 85,80,81\nCV 68,80,81\nMvav 192,160,72\n"
"Ra 144,26,32768\nMvav 196,160,76\ns_cmp_eq_u32 s99, 0\nEX 82,38\nMvav 204,164,76\nRa 148,26,33792\ns_cbranch_scc1 .E306\n.E307:\nMvav 200,164,72\nWL 8\nEX 83,39\nMvav 208,168,72\n"
"Ra 152,26,34816\nAD 85,85,82\nMvav 212,168,76\nD1 1,16,46,1024\nMvav 220,172,76\nRa 156,26,35840\nMvav 216,172,72\nAD 85,85,83\nCV 69,82,83\nMvav 224,176,72\nRa 160,24,40960\n"
"Mvav 228,176,76\ns_xor_b32 s89, s89, 0x10000\nEX 80,40\nMvav 236,180,76\nRa 164,24,41984\nMvav 232,180,72\nWL 6\nEX 81,41\nMvav 240,184,72\nRa 168,24,43008\nAD 84,84,80\n"
"Mvav 244,184,76\nD1 1,16,46,2048\nMvav 252,188,76\nRa 172,24,44032\nMvav 248,188,72\nAD 84,84,81\nCV 66,80,81\nCvaa 48,128,0,4\nRa 176,26,40960\nMvaa 48,132,8\n"
"s_cmp_eq_u32 s99, 0\nEX 82,42\nMvaa 48,136,16\nRa 180,26,41984\ns_cselect_b32 s86, s94, s86\nMvaa 48,140,24\nWL 6\nEX 83,43\nCvaa 52,140,28,8\nRa 184,26,43008\nAD 84,84,82\n"
"Mvaa 52,136,20\nD1 1,16,46,3072\ns_add_u32 m0, s89, 0\nMvaa 52,132,12\nRa 188,26,44032\nMvaa 52,128,4\nAD 84,84,83\nCV 67,82,83\nMvaa 52,144,36\nRa 128,2,49152\nMvaa 52,148,44\n"
"s_add_u32 s46, s86, s88\nEX 80,44\nMvaa 52,152,52\nRa 132,2,50176\nv_xor_b32 v24, 0x10000, v24\nMvaa 52,156,60\nWL 6\nEX 81,45\nMvaa 48,156,56\nRa 136,2,51200\nAD 85,85,80\n"
"Mvaa 48,152,48\nD1 1,16,46,0\nMvaa 48,148,40\nRa 140,2,52224\nMvaa 48,144,32\nAD 85,85,81\nCV 70,80,81\nCvaa 56,160,0,12\nRa 144,2,53248\nMvaa 56,164,8\nSv 112,1,16,49,1024\n"
"EX 82,46\nMvaa 56,168,16\nRa 148,2,54272\nv_xor_b32 v26, 0x10000, v26\nMvaa 56,172,24\nWL 6\nEX 83,47\nCvaa 60,172,28,16\nRa 152,2,55296\nAD 85,85,82\nMvaa 60,168,20\n"
"D1 1,16,46,1024\nMvaa 60,164,12\nRa 156,2,56320\nMvaa 60,160,4\nAD 85,85,83\nCV 71,82,83\nMvaa 60,176,36\nRa 160,2,57344\nv_max_f32 v90, v84, v85\nMvaa 60,180,44\n"
"s_add_u32 s88, s88, 32768\nEX 80,48\nMvaa 60,184,52\nRa 164,2,58368\nv_cmp_lt_f32 vcc, s63, v90\nMvaa 60,188,60\nWL 8\nEX 81,49\nMvaa 56,188,56\nRa 168,2,59392\n"
"s_cbranch_vccnz .T314\n.T315:\nMvaa 56,184,48\nD1 1,16,46,2048\nMvaa 56,180,40\nRa 172,2,60416\nAD 86,86,84\nMvaa 56,176,32\nAD 84,80,81\nCV 72,80,81\nMvav 128,128,64\n"
"Ra 176,2,61440\nMvav 132,128,68\nEX 82,50\nMvav 140,132,68\nRa 180,2,62464\nMvav 136,132,64\ns_waitcnt vmcnt(13) lgkmcnt(8)\nEX 83,51\nMvav 144,136,64\nRa 184,2,63488\n"
"AD 84,84,82\nMvav 148,136,68\nD1 1,16,46,3072\ns_add_u32 m0, s89, 16384\nMvav 156,140,68\nRa 188,2,64512\ns_barrier\nMvav 152,140,64\nAD 84,84,83\nCV 73,82,83\nMvav 160,144,64\n"
"Ra 128,24,0\nMvav 164,144,68\ns_add_u32 s46, s87, s88\nEX 80,52\nMvav 172,148,68\nRa 132,24,1024\nv_xor_b32 v2, 0x10000, v2\nMvav 168,148,64\nWL 8\nEX 81,53\nMvav 176,152,64\n"
"Ra 136,24,2048\nAD 87,87,85\nMvav 180,152,68\nD1 1,16,46,0\nMvav 188,156,68\nRa 140,24,3072\nMvav 184,156,64\nAD 85,80,81\nCV 76,80,81\nMvav 192,160,64\nRa 144,26,0\n"
"Mvav 196,160,68\ns_cmp_lt_u32 s99, s92\nEX 82,54\nMvav 204,164,68\nRa 148,26,1024\ns_cbranch_scc1 .E308\n.E309:\nMvav 200,164,64\nWL 8\nEX 83,55\nMvav 208,168,64\nRa 152,26,2048\n"
"AD 85,85,82\nMvav 212,168,68\nD1 1,16,46,1024\nMvav 220,172,68\nRa 156,26,3072\nMvav 216,172,64\nAD 85,85,83\nCV 77,82,83\nMvav 224,176,64\nRa 160,24,8192\nMvav 228,176,68\n"
"s_sub_u32 s90, s90, 1\nEX 80,56\nMvav 236,180,68\nRa 164,24,9216\ns_cbranch_scc1 .E310\n.E311:\nMvav 232,180,64\nWL 6\nEX 81,57\nMvav 240,184,64\nRa 168,24,10240\nAD 84,84,80\n"
"Mvav 244,184,68\nD1 1,16,46,2048\ns_sub_u32 s99, s99, 1\nMvav 252,188,68\nRa 172,24,11264\nMvav 248,188,64\nAD 84,84,81\nCV 74,80,81\ns_cbranch_scc0 .A304\ns_branch .A316\n.E306:\n"
"v_cmpx_gt_i32 vcc, 19, v0\nv_mov_b32 v15, s97\nv_cmpx_gt_i32 vcc, 18, v0\nv_mov_b32 v14, s97\nv_cmpx_gt_i32 vcc, 17, v0\nv_mov_b32 v13, s97\nv_cmpx_gt_i32 vcc, 16, v0\n"
"v_mov_b32 v12, s97\nv_cmpx_gt_i32 vcc, 3, v0\nv_mov_b32 v7, s97\nv_cmpx_gt_i32 vcc, 2, v0\nv_mov_b32 v6, s97\nv_cmpx_gt_i32 vcc, 1, v0\nv_mov_b32 v5, s97\n"
"v_cmpx_gt_i32 vcc, 0, v0\nv_mov_b32 v4, s97\ns_mov_b64 exec, -1\ns_nop 2\ns_branch .E307\n.E308:\ns_cmp_eq_u32 s99, 0\ns_cbranch_scc1 .K305\nv_cmpx_gt_i32 vcc, 19, v3\n"
"v_mov_b32 v15, s97\nv_cmpx_gt_i32 vcc, 18, v3\nv_mov_b32 v14, s97\nv_cmpx_gt_i32 vcc, 17, v3\nv_mov_b32 v13, s97\nv_cmpx_gt_i32 vcc, 16, v3\nv_mov_b32 v12, s97\n"
"v_cmpx_gt_i32 vcc, 3, v3\nv_mov_b32 v7, s97\nv_cmpx_gt_i32 vcc, 2, v3\nv_mov_b32 v6, s97\nv_cmpx_gt_i32 vcc, 1, v3\nv_mov_b32 v5, s97\nv_cmpx_gt_i32 vcc, 0, v3\nv_mov_b32 v4, s97\n"
"s_mov_b64 exec, -1\ns_nop 2\ns_branch .E309\n.K305:\nv_cmpx_gt_i32 vcc, 19, v3\nv_mov_b32 v19, s97\nv_cmpx_gt_i32 vcc, 18, v3\nv_mov_b32 v18, s97\nv_cmpx_gt_i32 vcc, 17, v3\n"
"v_mov_b32 v17, s97\nv_cmpx_gt_i32 vcc, 16, v3\nv_mov_b32 v16, s97\nv_cmpx_gt_i32 vcc, 3, v3\nv_mov_b32 v11, s97\nv_cmpx_gt_i32 vcc, 2, v3\nv_mov_b32 v10, s97\n"
"v_cmpx_gt_i32 vcc, 1, v3\nv_mov_b32 v9, s97\nv_cmpx_gt_i32 vcc, 0, v3\nv_mov_b32 v8, s97\ns_mov_b64 exec, -1\ns_nop 2\ns_branch .E309\n.E310:\ns_swappc_b64 s[80:81], s[8:9]\n"
"s_branch .E311\n.HD268:\nv_cmp_ne_u32 vcc, s15, v27\ns_nop 0\ns_cbranch_vccnz .S292\n.S293:\nbuffer_store_dword v25, off, s[16:19], s54 offset:4 sc0 sc1\nv_and_b32 v27, 0x70, v1\n"
"buffer_load_dword v27, v27, s[16:19], s61 offen offset:12 sc0 sc1\ns_mov_b32 s90, 1\ns_swappc_b64 s[8:9], s[80:81]\nv_cmp_ne_u32 vcc, s15, v27\ns_nop 0\ns_cbranch_vccnz .S295\n"
".S296:\nv_and_b32 v27, 0x70, v1\nbuffer_load_dword v27, v27, s[16:19], s61 offen offset:8 sc0 sc1\ns_mov_b32 s90, 1\ns_swappc_b64 s[8:9], s[80:81]\nv_cmp_ne_u32 vcc, s15, v27\n"
"s_nop 0\ns_cbranch_vccnz .S298\n.S299:\nv_and_b32 v27, 0xf0, v1\nbuffer_load_dword v27, v27, s[16:19], s61 offen offset:4 sc0 sc1\ns_mov_b32 s90, 1\ns_swappc_b64 s[8:9], s[80:81]\n"
"v_cmp_ne_u32 vcc, s15, v27\ns_nop 0\ns_cbranch_vccnz .S301\n.S302:\ns_mov_b32 s90, 100\ns_swappc_b64 s[8:9], s[80:81]\n.A316:\nRa 176,26,8192\ns_add_u32 s49, s49, 131072\n"
"EX 82,58\nRa 180,26,9216\nWL 6\nEX 83,59\nCvaa 36,140,28,8\nRa 184,26,10240\nAD 84,84,82\nMvaa 36,136,20\nD1 1,16,46,3072\ns_add_u32 m0, s89, 32768\nMvaa 36,132,12\n"
"Ra 188,26,11264\nMvaa 36,128,4\nAD 84,84,83\nCV 75,82,83\nMvaa 36,144,36\nRa 128,2,16384\nMvaa 36,148,44\ns_add_u32 s46, s86, s88\nEX 80,60\nMvaa 36,152,52\nRa 132,2,17408\n"
"Mvaa 36,156,60\nEX 81,61\nRa 136,2,18432\nAD 85,85,80\nD1 1,16,46,0\nRa 140,2,19456\nAD 85,85,81\nCV 78,80,81\nRa 144,2,20480\nSv 112,1,16,49,0\ns_mov_b32 s87, s95\nEX 82,62\n"
"Ra 148,2,21504\nWL 6\nEX 83,63\nCvaa 44,172,28,16\nRa 152,2,22528\nAD 85,85,82\nMvaa 44,168,20\nD1 1,16,46,1024\nMvaa 44,164,12\nRa 156,2,23552\nMvaa 44,160,4\nAD 85,85,83\n"
"CV 79,82,83\nMvaa 44,176,36\nRa 160,2,24576\nv_max_f32 v90, v84, v85\nMvaa 44,180,44\ns_add_u32 s88, s88, 32768\nMvaa 44,184,52\nRa 164,2,25600\nv_cmp_lt_f32 vcc, s63, v90\n"
"Mvaa 44,188,60\nWL 8\nRa 168,2,26624\ns_cbranch_vccnz .T318\n.T319:\nD1 1,16,46,2048\nRa 172,2,27648\nAD 86,86,84\nMvav 128,128,72\nRa 176,2,28672\nMvav 132,128,76\n"
"Mvav 140,132,76\nRa 180,2,29696\nMvav 136,132,72\ns_waitcnt vmcnt(13) lgkmcnt(8)\nMvav 144,136,72\nRa 184,2,30720\nMvav 148,136,76\nD1 1,16,46,3072\ns_add_u32 m0, s89, 49152\n"
"Mvav 156,140,76\nRa 188,2,31744\ns_barrier\nMvav 152,140,72\nMvav 160,144,72\nRa 128,24,32768\nMvav 164,144,76\ns_add_u32 s46, s87, s88\nEX 80,36\nMvav 172,148,76\n"
"Ra 132,24,33792\nMvav 168,148,72\nWL 8\nEX 81,37\nMvav 176,152,72\nRa 136,24,34816\nAD 87,87,85\nMvav 180,152,76\nD1 1,16,46,0\nMvav 188,156,76\nRa 140,24,35840\nMvav 184,156,72\n"
"AD 85,80,81\nCV 68,80,81\nMvav 192,160,72\nRa 144,26,32768\nMvav 196,160,76\ns_nop 0\nEX 82,38\nMvav 204,164,76\nRa 148,26,33792\nv_cmpx_gt_i32 vcc, 19, v0\nv_mov_b32 v19, s97\n"
"v_cmpx_gt_i32 vcc, 18, v0\nv_mov_b32 v18, s97\nv_cmpx_gt_i32 vcc, 17, v0\nv_mov_b32 v17, s97\nv_cmpx_gt_i32 vcc, 16, v0\nv_mov_b32 v16, s97\nv_cmpx_gt_i32 vcc, 3, v0\n"
"v_mov_b32 v11, s97\nv_cmpx_gt_i32 vcc, 2, v0\nv_mov_b32 v10, s97\nv_cmpx_gt_i32 vcc, 1, v0\nv_mov_b32 v9, s97\nv_cmpx_gt_i32 vcc, 0, v0\nv_mov_b32 v8, s97\ns_mov_b64 exec, -1\n"
"s_nop 2\ns_nop 0\nMvav 200,164,72\nWL 8\nEX 83,39\nMvav 208,168,72\nRa 152,26,34816\nAD 85,85,82\nMvav 212,168,76\nD1 1,16,46,1024\nMvav 220,172,76\nRa 156,26,35840\n"
"Mvav 216,172,72\nAD 85,85,83\nCV 69,82,83\nMvav 224,176,72\nRa 160,24,40960\nMvav 228,176,76\ns_xor_b32 s89, s89, 0x10000\nMvav 236,180,76\nRa 164,24,41984\nMvav 232,180,72\n"
"WL 10\nMvav 240,184,72\nRa 168,24,43008\nMvav 244,184,76\nD1 1,16,46,2048\nMvav 252,188,76\nRa 172,24,44032\nMvav 248,188,72\nRa 176,26,40960\nRa 180,26,41984\n.W575:\n"
"s_cbranch_execz .W579\nWL 6\nCvaa 52,140,28,8\nRa 184,26,43008\nMvaa 52,136,20\nD1 1,16,46,3072\ns_add_u32 m0, s89, 0\nMvaa 52,132,12\nRa 188,26,44032\nMvaa 52,128,4\n"
"Mvaa 52,144,36\nRa 128,2,49152\nMvaa 52,148,44\ns_add_u32 s46, s86, s88\nEX 80,44\nMvaa 52,152,52\nRa 132,2,50176\nv_xor_b32 v24, 0x10000, v24\nMvaa 52,156,60\nEX 81,45\n"
"Ra 136,2,51200\nAD 85,85,80\nD1 1,16,46,0\nRa 140,2,52224\nAD 85,85,81\nCV 70,80,81\nRa 144,2,53248\nSv 112,1,16,49,1024\nEX 82,46\nRa 148,2,54272\nv_xor_b32 v26, 0x10000, v26\n"
"WL 6\nEX 83,47\nCvaa 60,172,28,16\nRa 152,2,55296\nAD 85,85,82\nMvaa 60,168,20\nD1 1,16,46,1024\nMvaa 60,164,12\nRa 156,2,56320\nMvaa 60,160,4\nAD 85,85,83\nCV 71,82,83\n"
"Mvaa 60,176,36\nRa 160,2,57344\nMvaa 60,180,44\ns_add_u32 s88, s88, 32768\nMvaa 60,184,52\nRa 164,2,58368\nv_cmp_lt_f32 vcc, s63, v85\nMvaa 60,188,60\nWL 8\nRa 168,2,59392\n"
"s_cbranch_vccnz .T320\n.T321:\nD1 1,16,46,2048\nRa 172,2,60416\nRa 176,2,61440\nMvav 132,128,68\nMvav 140,132,68\nRa 180,2,62464\ns_waitcnt vmcnt(13) lgkmcnt(8)\nRa 184,2,63488\n"
"Mvav 148,136,68\nD1 1,16,46,3072\ns_add_u32 m0, s89, 16384\nMvav 156,140,68\nRa 188,2,64512\ns_barrier\nRa 128,24,0\nMvav 164,144,68\ns_add_u32 s46, s87, s88\nEX 80,52\n"
"Mvav 172,148,68\nRa 132,24,1024\nv_xor_b32 v2, 0x10000, v2\nWL 8\nEX 81,53\nRa 136,24,2048\nAD 87,87,85\nMvav 180,152,68\nD1 1,16,46,0\nMvav 188,156,68\nRa 140,24,3072\n"
"AD 85,80,81\nCV 76,80,81\nRa 144,26,0\nMvav 196,160,68\nEX 82,54\nMvav 204,164,68\nRa 148,26,1024\nWL 8\nEX 83,55\nRa 152,26,2048\nAD 85,85,82\nMvav 212,168,68\nD1 1,16,46,1024\n"
"Mvav 220,172,68\nRa 156,26,3072\nAD 85,85,83\nCV 77,82,83\nRa 160,24,8192\nMvav 228,176,68\ns_sub_u32 s90, s90, 1\nMvav 236,180,68\nRa 164,24,9216\nWL 6\nRa 168,24,10240\n"
"Mvav 244,184,68\nD1 1,16,46,2048\nMvav 252,188,68\nRa 172,24,11264\n.SW274:\nv_mov_b32 v4, 0\nv_mov_b32 v5, 0\nv_mov_b32 v6, 0\nv_mov_b32 v7, 0\nv_mov_b32 v8, 0\nv_mov_b32 v9, 0\n"
"v_mov_b32 v10, 0\nv_mov_b32 v11, 0\nv_mov_b32 v12, 0\nv_mov_b32 v13, 0\nv_mov_b32 v14, 0\nv_mov_b32 v15, 0\nv_mov_b32 v16, 0\nv_mov_b32 v17, 0\nv_mov_b32 v18, 0\nv_mov_b32 v19, 0\n"
"s_add_u32 s99, s98, 1\ns_mov_b32 s94, s96\ns_add_u32 s95, s96, 65536\ns_cmp_lt_u32 s99, s92\ns_cbranch_scc0 .E322\nv_cmpx_gt_i32 vcc, 19, v3\nv_mov_b32 v15, s97\n"
"v_cmpx_gt_i32 vcc, 18, v3\nv_mov_b32 v14, s97\nv_cmpx_gt_i32 vcc, 17, v3\nv_mov_b32 v13, s97\nv_cmpx_gt_i32 vcc, 16, v3\nv_mov_b32 v12, s97\nv_cmpx_gt_i32 vcc, 3, v3\n"
"v_mov_b32 v7, s97\nv_cmpx_gt_i32 vcc, 2, v3\nv_mov_b32 v6, s97\nv_cmpx_gt_i32 vcc, 1, v3\nv_mov_b32 v5, s97\nv_cmpx_gt_i32 vcc, 0, v3\nv_mov_b32 v4, s97\ns_mov_b64 exec, -1\n"
"s_nop 2\n.E322:\ns_nop 0\nCvaa 32,128,192,4\nRa 176,26,8192\nMvaa 32,132,200\nMvaa 32,136,208\nRa 180,26,9216\nMvaa 32,140,216\nWL 6\nCvaa 36,140,220,8\nRa 184,26,10240\n"
"Mvaa 36,136,212\nD1 1,16,46,3072\ns_add_u32 m0, s89, 32768\nMvaa 36,132,204\nRa 188,26,11264\nMvaa 36,128,196\nMvaa 36,144,228\nRa 128,2,16384\nMvaa 36,148,236\n"
"s_add_u32 s46, s86, s88\nEX 80,60\nMvaa 36,152,244\nRa 132,2,17408\nMvaa 36,156,252\nWL 6\nEX 81,61\nMvaa 32,156,248\nRa 136,2,18432\nAD 85,85,80\nMvaa 32,152,240\nD1 1,16,46,0\n"
"Mvaa 32,148,232\nRa 140,2,19456\nMvaa 32,144,224\nAD 85,85,81\nCV 78,80,81\nCvaa 40,160,192,12\nRa 144,2,20480\nMvaa 40,164,200\nEX 82,62\nMvaa 40,168,208\nRa 148,2,21504\n"
"Mvaa 40,172,216\nWL 6\nEX 83,63\nCvaa 44,172,220,16\nRa 152,2,22528\nAD 85,85,82\nMvaa 44,168,212\nD1 1,16,46,1024\nMvaa 44,164,204\nRa 156,2,23552\nMvaa 44,160,196\nAD 85,85,83\n"
"CV 79,82,83\nMvaa 44,176,228\nRa 160,2,24576\nMvaa 44,180,236\ns_add_u32 s88, s88, 32768\nEX 80,32\nMvaa 44,184,244\nRa 164,2,25600\nv_cmp_lt_f32 vcc, s63, v85\nMvaa 44,188,252\n"
"WL 8\nEX 81,33\nMvaa 40,188,248\nRa 168,2,26624\ns_cbranch_vccnz .T327\n.T328:\nMvaa 40,184,240\nD1 1,16,46,2048\nCV 128,128,129\nCV 129,130,131\nMvaa 40,180,232\nRa 172,2,27648\n"
"CV 130,136,137\nCV 131,138,139\nMvaa 40,176,224\nAD 84,80,81\nCV 64,80,81\ns_add_u32 s5, s66, 0\nSv 128,1,16,5,0\nRa 176,2,28672\nCV 144,144,145\nCV 145,146,147\nMvav 132,128,76\n"
"EX 82,34\nMvav 140,132,76\nRa 180,2,29696\nCV 146,152,153\nCV 147,154,155\ns_waitcnt vmcnt(13) lgkmcnt(8)\nEX 83,35\ns_add_u32 s5, s66, 0\nSv 144,1,16,5,2048\nRa 184,2,30720\n"
"AD 84,84,82\nCV 160,160,161\nMvav 148,136,76\nD1 1,16,46,3072\ns_add_u32 m0, s89, 49152\nCV 161,162,163\nMvav 156,140,76\nRa 188,2,31744\ns_barrier\nCV 162,168,169\n"
"CV 163,170,171\nAD 84,84,83\nCV 65,82,83\ns_add_u32 s5, s66, 4096\nSv 160,1,16,5,0\nRa 128,24,32768\nCV 176,176,177\nCV 177,178,179\nMvav 164,144,76\ns_add_u32 s46, s87, s88\n"
"EX 80,36\nMvav 172,148,76\nRa 132,24,33792\nCV 178,184,185\nCV 179,186,187\nCV 132,132,133\nWL 8\nEX 81,37\ns_add_u32 s5, s66, 4096\nSv 176,1,16,5,2048\nRa 136,24,34816\n"
"AD 87,87,85\nCV 133,134,135\nMvav 180,152,76\nD1 1,16,46,0\nv_mov_b32 v96, v86\nv_mov_b32 v97, v87\nCV 134,140,141\nMvav 188,156,76\nRa 140,24,35840\n"
"v_permlane32_swap_b32 v86, v96\nCV 135,142,143\nCV 148,148,149\nCV 149,150,151\nAD 85,80,81\nCV 68,80,81\ns_add_u32 s5, s66, 0\nSv 132,1,16,5,1024\nRa 144,26,32768\n"
"v_permlane32_swap_b32 v87, v97\nAD 86,86,96\nCV 150,156,157\nCV 151,158,159\nMvav 196,160,76\ns_cmp_eq_u32 s99, 0\nEX 82,38\nMvav 204,164,76\nRa 148,26,33792\n"
"s_cbranch_scc0 .E324\nv_cmpx_gt_i32 vcc, 19, v0\nv_mov_b32 v15, s97\nv_cmpx_gt_i32 vcc, 18, v0\nv_mov_b32 v14, s97\nv_cmpx_gt_i32 vcc, 17, v0\nv_mov_b32 v13, s97\n"
"v_cmpx_gt_i32 vcc, 16, v0\nv_mov_b32 v12, s97\nv_cmpx_gt_i32 vcc, 3, v0\nv_mov_b32 v7, s97\nv_cmpx_gt_i32 vcc, 2, v0\nv_mov_b32 v6, s97\nv_cmpx_gt_i32 vcc, 1, v0\n"
"v_mov_b32 v5, s97\nv_cmpx_gt_i32 vcc, 0, v0\nv_mov_b32 v4, s97\ns_mov_b64 exec, -1\ns_nop 2\n.E324:\nWL 8\nEX 83,39\ns_add_u32 s5, s66, 0\nSv 148,1,16,5,3072\nRa 152,26,34816\n"
"AD 85,85,82\nAD 87,87,97\nCV 192,192,193\nMvav 212,168,76\nD1 1,16,46,1024\nv_mov_b32 v96, v86\nCV 193,194,195\nMvav 220,172,76\nRa 156,26,35840\nv_mov_b32 v97, v87\n"
"CV 194,200,201\nCV 195,202,203\nAD 85,85,83\nCV 69,82,83\nv_permlane16_swap_b32 v86, v96\ns_add_u32 s5, s66, 16384\nSv 192,1,16,5,0\nRa 160,24,40960\n"
"v_permlane16_swap_b32 v87, v97\nCV 164,164,165\nCV 165,166,167\nMvav 228,176,76\ns_xor_b32 s89, s89, 0x10000\nEX 80,40\nCV 166,172,173\nMvav 236,180,76\nRa 164,24,41984\n"
"AD 86,86,96\nAD 87,87,97\nCV 167,174,175\nWL 6\nEX 81,41\ns_add_u32 s5, s66, 4096\nSv 164,1,16,5,1024\nRa 168,24,43008\nAD 84,84,80\nCV 208,208,209\nCV 209,210,211\n"
"Mvav 244,184,76\nD1 1,16,46,2048\nv_cmp_gt_u32 vcc, 0x100, v1\nv_mov_b32 v94, s15\nv_mov_b32 v95, s15\nv_cndmask_b32 v92, v89, v88, vcc\nv_cndmask_b32 v93, v87, v86, vcc\n"
"Mvav 252,188,76\nRa 172,24,44032\nv_mov_b32 v86, 0\nCV 210,216,217\nCV 211,218,219\nAD 84,84,81\nCV 66,80,81\ns_add_u32 s5, s66, 16384\nSv 208,1,16,5,2048\nCvaa 48,128,192,4\n"
"Ra 176,26,40960\nv_mov_b32 v87, 0\nv_mov_b32 v88, 0\nCV 180,180,181\nCV 181,182,183\nMvaa 48,132,200\ns_cmp_eq_u32 s99, 0\nEX 82,42\nMvaa 48,136,208\nRa 180,26,41984\n"
"s_cselect_b32 s86, s94, s86\nCV 182,188,189\nCV 183,190,191\nMvaa 48,140,216\nWL 6\nEX 83,43\ns_add_u32 s5, s66, 4096\nSv 180,1,16,5,3072\nCvaa 52,140,220,8\nRa 184,26,43008\n"
"AD 84,84,82\nv_mov_b32 v89, 0\nMvaa 52,136,212\nD1 1,16,46,3072\ns_add_u32 m0, s89, 0\nCV 224,224,225\nMvaa 52,132,204\nRa 188,26,44032\nCV 225,226,227\nCV 226,232,233\n"
"CV 227,234,235\nMvaa 52,128,196\nAD 84,84,83\nCV 67,82,83\ns_add_u32 s5, s66, 20480\nSv 224,1,16,5,0\nMvaa 52,144,228\nRa 128,2,49152\nCV 196,196,197\nCV 197,198,199\n"
"CV 198,204,205\nMvaa 52,148,236\ns_add_u32 s46, s86, s88\nEX 80,44\nMvaa 52,152,244\nRa 132,2,50176\nv_xor_b32 v24, 0x10000, v24\nCV 199,206,207\nMvaa 52,156,252\nWL 6\nEX 81,45\n"
"s_add_u32 s5, s66, 16384\nSv 196,1,16,5,1024\nMvaa 48,156,248\nRa 136,2,51200\nAD 85,85,80\nCV 240,240,241\nMvaa 48,152,240\nD1 1,16,46,0\nCV 241,242,243\nMvaa 48,148,232\n"
"Ra 140,2,52224\nCV 242,248,249\nCV 243,250,251\nMvaa 48,144,224\nAD 85,85,81\nCV 70,80,81\ns_add_u32 s5, s66, 20480\nSv 240,1,16,5,2048\nCvaa 56,160,192,12\nRa 144,2,53248\n"
"CV 212,212,213\nCV 213,214,215\nMvaa 56,164,200\nEX 82,46\nCV 214,220,221\nMvaa 56,168,208\nRa 148,2,54272\nv_xor_b32 v26, 0x10000, v26\nCV 215,222,223\nMvaa 56,172,216\nWL 6\n"
"EX 83,47\ns_add_u32 s5, s66, 16384\nSv 212,1,16,5,3072\nCvaa 60,172,220,16\nRa 152,2,55296\nAD 85,85,82\nCV 228,228,229\nMvaa 60,168,212\nD1 1,16,46,1024\nCV 229,230,231\n"
"Mvaa 60,164,204\nRa 156,2,56320\nCV 230,236,237\nCV 231,238,239\nCV 244,244,245\nMvaa 60,160,196\nAD 85,85,83\nCV 71,82,83\ns_add_u32 s5, s66, 20480\nSv 228,1,16,5,1024\n"
"Mvaa 60,176,228\nRa 160,2,57344\nv_max_f32 v90, v84, v85\nCV 245,246,247\nCV 246,252,253\nMvaa 60,180,236\ns_add_u32 s88, s88, 32768\nEX 80,48\nCV 247,254,255\nMvaa 60,184,244\n"
"Ra 164,2,58368\nv_cmp_lt_f32 vcc, s63, v90\nMvaa 60,188,252\nWL 8\nEX 81,49\nMvaa 56,188,248\nRa 168,2,59392\ns_cbranch_vccnz .T329\n.T330:\nv_mov_b32 v20, v84\n"
"v_mov_b32 v21, v85\nv_mov_b32 v22, v20\nv_mov_b32 v23, v21\ns_add_u32 s5, s66, 20480\nSv 244,1,16,5,3072\nMvaa 56,184,240\nD1 1,16,46,2048\nv_permlane32_swap_b32 v20, v22\n"
"v_permlane32_swap_b32 v21, v23\nAD 20,20,22\nAD 21,21,23\nv_mov_b32 v22, v20\nv_mov_b32 v23, v21\nMvaa 56,180,232\nRa 172,2,60416\nv_permlane16_swap_b32 v20, v22\n"
"v_permlane16_swap_b32 v21, v23\nAD 20,20,22\nAD 21,21,23\nv_min_f32 v90, v20, v21\nv_cmp_gt_f32 vcc, s64, v90\ns_nop 0\ns_cbranch_vccnz .T331\n.T332:\nAD 86,86,84\n"
"Mvaa 56,176,224\nAD 84,80,81\nCV 72,80,81\nZvav 128,128,64\nRa 176,2,61440\nZvav 132,128,68\nEX 82,50\nZvav 140,132,68\nRa 180,2,62464\nZvav 136,132,64\n"
"s_waitcnt vmcnt(27) lgkmcnt(8)\nEX 83,51\nZvav 144,136,64\nRa 184,2,63488\nAD 84,84,82\nZvav 148,136,68\nD1 1,16,46,3072\ns_add_u32 m0, s89, 16384\nZvav 156,140,68\n"
"Ra 188,2,64512\ns_barrier\nZvav 152,140,64\nAD 84,84,83\nCV 73,82,83\nZvav 160,144,64\nRa 128,24,0\nZvav 164,144,68\ns_add_u32 s46, s87, s88\nEX 80,52\nZvav 172,148,68\n"
"Ra 132,24,1024\nv_xor_b32 v2, 0x10000, v2\nZvav 168,148,64\nWL 8\nEX 81,53\nZvav 176,152,64\nRa 136,24,2048\nAD 87,87,85\nZvav 180,152,68\nD1 1,16,46,0\nZvav 188,156,68\n"
"Ra 140,24,3072\nZvav 184,156,64\nAD 85,80,81\nCV 76,80,81\nZvav 192,160,64\nRa 144,26,0\nZvav 196,160,68\ns_cmp_lt_u32 s99, s92\nEX 82,54\nZvav 204,164,68\nRa 148,26,1024\n"
"s_cbranch_scc0 .E325\nv_cmpx_gt_i32 vcc, 19, v3\nv_mov_b32 v19, s97\nv_cmpx_gt_i32 vcc, 18, v3\nv_mov_b32 v18, s97\nv_cmpx_gt_i32 vcc, 17, v3\nv_mov_b32 v17, s97\n"
"v_cmpx_gt_i32 vcc, 16, v3\nv_mov_b32 v16, s97\nv_cmpx_gt_i32 vcc, 3, v3\nv_mov_b32 v11, s97\nv_cmpx_gt_i32 vcc, 2, v3\nv_mov_b32 v10, s97\nv_cmpx_gt_i32 vcc, 1, v3\n"
"v_mov_b32 v9, s97\nv_cmpx_gt_i32 vcc, 0, v3\nv_mov_b32 v8, s97\ns_mov_b64 exec, -1\ns_nop 2\n.E325:\ns_nop 0\nZvav 200,164,64\nWL 8\nEX 83,55\nZvav 208,168,64\nRa 152,26,2048\n"
"AD 85,85,82\nZvav 212,168,68\nD1 1,16,46,1024\nZvav 220,172,68\nRa 156,26,3072\nZvav 216,172,64\nAD 85,85,83\nCV 77,82,83\nZvav 224,176,64\nRa 160,24,8192\nZvav 228,176,68\n"
"s_sub_u32 s90, s90, 1\nEX 80,56\nZvav 236,180,68\nRa 164,24,9216\ns_cbranch_scc0 .E323\ns_swappc_b64 s[80:81], s[8:9]\n.E323:\nZvav 232,180,64\nWL 6\nEX 81,57\nZvav 240,184,64\n"
"Ra 168,24,10240\nAD 84,84,80\nZvav 244,184,68\nD1 1,16,46,2048\nZvav 252,188,68\nRa 172,24,11264\nZvav 248,188,64\nAD 84,84,81\n.W576:\ns_cbranch_execz .WB273\nCV 74,80,81\n"
"s_cmp_eq_u32 s13, 0\ns_cbranch_scc0 .E333\nv_mov_b32 v91, 0x40000000\nv_cmp_gt_u32 vcc, 0x200, v1\nWV 5\ns_nop 0\nv_cndmask_b32 v91, v91, v1, vcc\nSv 92,91,16,65,0\n.E333:\n"
"s_sub_u32 s99, s99, 1\ns_cbranch_scc1 .LE275\n.p2align 6\n.A334:\nCvaa 32,128,192,4\nRa 176,26,8192\nMvaa 32,132,200\ns_add_u32 s49, s49, 131072\nEX 82,58\nMvaa 32,136,208\n"
"Ra 180,26,9216\nMvaa 32,140,216\nWL 6\nEX 83,59\nCvaa 36,140,220,8\nRa 184,26,10240\nAD 84,84,82\nMvaa 36,136,212\nD1 1,16,46,3072\ns_add_u32 m0, s89, 32768\nMvaa 36,132,204\n"
"Ra 188,26,11264\nMvaa 36,128,196\nAD 84,84,83\nCV 75,82,83\nMvaa 36,144,228\nRa 128,2,16384\nMvaa 36,148,236\ns_add_u32 s46, s86, s88\nEX 80,60\nMvaa 36,152,244\nRa 132,2,17408\n"
"Mvaa 36,156,252\nWL 6\nEX 81,61\nMvaa 32,156,248\nRa 136,2,18432\nAD 85,85,80\nMvaa 32,152,240\nD1 1,16,46,0\nMvaa 32,148,232\nRa 140,2,19456\nMvaa 32,144,224\nAD 85,85,81\n"
"CV 78,80,81\nCvaa 40,160,192,12\nRa 144,2,20480\nMvaa 40,164,200\nSv 112,1,16,49,0\nEX 82,62\nMvaa 40,168,208\nRa 148,2,21504\nMvaa 40,172,216\nWL 6\nEX 83,63\nCvaa 44,172,220,16\n"
"Ra 152,2,22528\nAD 85,85,82\nMvaa 44,168,212\nD1 1,16,46,1024\nMvaa 44,164,204\nRa 156,2,23552\nMvaa 44,160,196\nAD 85,85,83\nCV 79,82,83\nMvaa 44,176,228\nRa 160,2,24576\n"
"v_max_f32 v90, v84, v85\nMvaa 44,180,236\ns_add_u32 s88, s88, 32768\nEX 80,32\nMvaa 44,184,244\nRa 164,2,25600\nv_cmp_lt_f32 vcc, s63, v90\nMvaa 44,188,252\nWL 8\nEX 81,33\n"
"Mvaa 40,188,248\nRa 168,2,26624\ns_cbranch_vccnz .T344\n.T345:\nMvaa 40,184,240\nD1 1,16,46,2048\nMvaa 40,180,232\nRa 172,2,27648\nAD 86,86,84\nMvaa 40,176,224\nAD 84,80,81\n"
"CV 64,80,81\nMvav 128,128,72\nRa 176,2,28672\nMvav 132,128,76\nEX 82,34\nMvav 140,132,76\nRa 180,2,29696\nMvav 136,132,72\ns_waitcnt vmcnt(13) lgkmcnt(8)\nEX 83,35\n"
"Mvav 144,136,72\nRa 184,2,30720\nAD 84,84,82\nMvav 148,136,76\nD1 1,16,46,3072\ns_add_u32 m0, s89, 49152\nMvav 156,140,76\nRa 188,2,31744\ns_barrier\nMvav 152,140,72\nAD 84,84,83\n"
"CV 65,82,83\nMvav 160,144,72\nRa 128,24,32768\nMvav 164,144,76\ns_add_u32 s46, s87, s88\nEX 80,36\nMvav 172,148,76\nRa 132,24,33792\nMvav 168,148,72\nWL 8\nEX 81,37\n"
"Mvav 176,152,72\nRa 136,24,34816\nAD 87,87,85\nMvav 180,152,76\nD1 1,16,46,0\nMvav 188,156,76\nRa 140,24,35840\nMvav 184,156,72\nAD 85,80,81\nCV 68,80,81\nMvav 192,160,72\n"
"Ra 144,26,32768\nMvav 196,160,76\ns_cmp_eq_u32 s99, 0\nEX 82,38\nMvav 204,164,76\nRa 148,26,33792\ns_cbranch_scc1 .E336\n.E337:\nMvav 200,164,72\nWL 8\nEX 83,39\nMvav 208,168,72\n"
"Ra 152,26,34816\nAD 85,85,82\nMvav 212,168,76\nD1 1,16,46,1024\nMvav 220,172,76\nRa 156,26,35840\nMvav 216,172,72\nAD 85,85,83\nCV 69,82,83\nMvav 224,176,72\nRa 160,24,40960\n"
"Mvav 228,176,76\ns_xor_b32 s89, s89, 0x10000\nEX 80,40\nMvav 236,180,76\nRa 164,24,41984\nMvav 232,180,72\nWL 6\nEX 81,41\nMvav 240,184,72\nRa 168,24,43008\nAD 84,84,80\n"
"Mvav 244,184,76\nD1 1,16,46,2048\nMvav 252,188,76\nRa 172,24,44032\nMvav 248,188,72\nAD 84,84,81\nCV 66,80,81\nCvaa 48,128,192,4\nRa 176,26,40960\nMvaa 48,132,200\n"
"s_cmp_eq_u32 s99, 0\nEX 82,42\nMvaa 48,136,208\nRa 180,26,41984\ns_cselect_b32 s86, s94, s86\nMvaa 48,140,216\nWL 6\nEX 83,43\nCvaa 52,140,220,8\nRa 184,26,43008\nAD 84,84,82\n"
"Mvaa 52,136,212\nD1 1,16,46,3072\ns_add_u32 m0, s89, 0\nMvaa 52,132,204\nRa 188,26,44032\nMvaa 52,128,196\nAD 84,84,83\nCV 67,82,83\nMvaa 52,144,228\nRa 128,2,49152\n"
"Mvaa 52,148,236\ns_add_u32 s46, s86, s88\nEX 80,44\nMvaa 52,152,244\nRa 132,2,50176\nv_xor_b32 v24, 0x10000, v24\nMvaa 52,156,252\nWL 6\nEX 81,45\nMvaa 48,156,248\nRa 136,2,51200\n"
"AD 85,85,80\nMvaa 48,152,240\nD1 1,16,46,0\nMvaa 48,148,232\nRa 140,2,52224\nMvaa 48,144,224\nAD 85,85,81\nCV 70,80,81\nCvaa 56,160,192,12\nRa 144,2,53248\nMvaa 56,164,200\n"
"Sv 112,1,16,49,1024\nEX 82,46\nMvaa 56,168,208\nRa 148,2,54272\nv_xor_b32 v26, 0x10000, v26\nMvaa 56,172,216\nWL 6\nEX 83,47\nCvaa 60,172,220,16\nRa 152,2,55296\nAD 85,85,82\n"
"Mvaa 60,168,212\nD1 1,16,46,1024\nMvaa 60,164,204\nRa 156,2,56320\nMvaa 60,160,196\nAD 85,85,83\nCV 71,82,83\nMvaa 60,176,228\nRa 160,2,57344\nv_max_f32 v90, v84, v85\n"
"Mvaa 60,180,236\ns_add_u32 s88, s88, 32768\nEX 80,48\nMvaa 60,184,244\nRa 164,2,58368\nv_cmp_lt_f32 vcc, s63, v90\nMvaa 60,188,252\nWL 8\nEX 81,49\nMvaa 56,188,248\n"
"Ra 168,2,59392\ns_cbranch_vccnz .T346\n.T347:\nMvaa 56,184,240\nD1 1,16,46,2048\nMvaa 56,180,232\nRa 172,2,60416\nAD 86,86,84\nMvaa 56,176,224\nAD 84,80,81\nCV 72,80,81\n"
"Mvav 128,128,64\nRa 176,2,61440\nMvav 132,128,68\ns_cmp_eq_u32 s99, s98\nEX 82,50\nMvav 140,132,68\nRa 180,2,62464\ns_cbranch_scc1 .E342\n.E343:\nMvav 136,132,64\n"
"s_waitcnt vmcnt(13) lgkmcnt(8)\nEX 83,51\nMvav 144,136,64\nRa 184,2,63488\nAD 84,84,82\nMvav 148,136,68\nD1 1,16,46,3072\ns_add_u32 m0, s89, 16384\nMvav 156,140,68\n"
"Ra 188,2,64512\ns_barrier\nMvav 152,140,64\nAD 84,84,83\nCV 73,82,83\nMvav 160,144,64\nRa 128,24,0\nMvav 164,144,68\ns_add_u32 s46, s87, s88\nEX 80,52\nMvav 172,148,68\n"
"Ra 132,24,1024\nv_xor_b32 v2, 0x10000, v2\nMvav 168,148,64\nWL 8\nEX 81,53\nMvav 176,152,64\nRa 136,24,2048\nAD 87,87,85\nMvav 180,152,68\nD1 1,16,46,0\nMvav 188,156,68\n"
"Ra 140,24,3072\nMvav 184,156,64\nAD 85,80,81\nCV 76,80,81\nMvav 192,160,64\nRa 144,26,0\nMvav 196,160,68\ns_cmp_lt_u32 s99, s92\nEX 82,54\nMvav 204,164,68\nRa 148,26,1024\n"
"s_cbranch_scc1 .E338\n.E339:\nMvav 200,164,64\nWL 8\nEX 83,55\nMvav 208,168,64\nRa 152,26,2048\nAD 85,85,82\nMvav 212,168,68\nD1 1,16,46,1024\nMvav 220,172,68\nRa 156,26,3072\n"
"Mvav 216,172,64\nAD 85,85,83\nCV 77,82,83\nMvav 224,176,64\nRa 160,24,8192\nMvav 228,176,68\ns_sub_u32 s90, s90, 1\nEX 80,56\nMvav 236,180,68\nRa 164,24,9216\n"
"s_cbranch_scc1 .E340\n.E341:\nMvav 232,180,64\nWL 6\nEX 81,57\nMvav 240,184,64\nRa 168,24,10240\nAD 84,84,80\nMvav 244,184,68\nD1 1,16,46,2048\ns_sub_u32 s99, s99, 1\n"
"Mvav 252,188,68\nRa 172,24,11264\nMvav 248,188,64\nAD 84,84,81\nCV 74,80,81\ns_cbranch_scc0 .A334\ns_branch .A348\n.E336:\nv_cmpx_gt_i32 vcc, 19, v0\nv_mov_b32 v15, s97\n"
"v_cmpx_gt_i32 vcc, 18, v0\nv_mov_b32 v14, s97\nv_cmpx_gt_i32 vcc, 17, v0\nv_mov_b32 v13, s97\nv_cmpx_gt_i32 vcc, 16, v0\nv_mov_b32 v12, s97\nv_cmpx_gt_i32 vcc, 3, v0\n"
"v_mov_b32 v7, s97\nv_cmpx_gt_i32 vcc, 2, v0\nv_mov_b32 v6, s97\nv_cmpx_gt_i32 vcc, 1, v0\nv_mov_b32 v5, s97\nv_cmpx_gt_i32 vcc, 0, v0\nv_mov_b32 v4, s97\ns_mov_b64 exec, -1\n"
"s_nop 2\ns_branch .E337\n.E338:\ns_cmp_eq_u32 s99, 0\ns_cbranch_scc1 .K335\nv_cmpx_gt_i32 vcc, 19, v3\nv_mov_b32 v15, s97\nv_cmpx_gt_i32 vcc, 18, v3\nv_mov_b32 v14, s97\n"
"v_cmpx_gt_i32 vcc, 17, v3\nv_mov_b32 v13, s97\nv_cmpx_gt_i32 vcc, 16, v3\nv_mov_b32 v12, s97\nv_cmpx_gt_i32 vcc, 3, v3\nv_mov_b32 v7, s97\nv_cmpx_gt_i32 vcc, 2, v3\n"
"v_mov_b32 v6, s97\nv_cmpx_gt_i32 vcc, 1, v3\nv_mov_b32 v5, s97\nv_cmpx_gt_i32 vcc, 0, v3\nv_mov_b32 v4, s97\ns_mov_b64 exec, -1\ns_nop 2\ns_branch .E339\n.K335:\n"
"v_cmpx_gt_i32 vcc, 19, v3\nv_mov_b32 v19, s97\nv_cmpx_gt_i32 vcc, 18, v3\nv_mov_b32 v18, s97\nv_cmpx_gt_i32 vcc, 17, v3\nv_mov_b32 v17, s97\nv_cmpx_gt_i32 vcc, 16, v3\n"
"v_mov_b32 v16, s97\nv_cmpx_gt_i32 vcc, 3, v3\nv_mov_b32 v11, s97\nv_cmpx_gt_i32 vcc, 2, v3\nv_mov_b32 v10, s97\nv_cmpx_gt_i32 vcc, 1, v3\nv_mov_b32 v9, s97\n"
"v_cmpx_gt_i32 vcc, 0, v3\nv_mov_b32 v8, s97\ns_mov_b64 exec, -1\ns_nop 2\ns_branch .E339\n.E340:\ns_swappc_b64 s[80:81], s[8:9]\ns_branch .E341\n.E342:\nv_mov_b32 v91, 0x40000000\n"
"v_cmp_gt_u32 vcc, 0x200, v1\nWV 19\ns_nop 0\nv_cndmask_b32 v91, v91, v1, vcc\nSv 92,91,16,65,0\ns_branch .E343\n.A348:\n.LE275:\nRa 176,26,8192\nEX 82,58\nRa 180,26,9216\nWL 6\n"
"EX 83,59\nCvaa 36,140,220,8\nRa 184,26,10240\nAD 84,84,82\nMvaa 36,136,212\nD1 1,16,46,3072\ns_add_u32 m0, s89, 32768\nMvaa 36,132,204\nRa 188,26,11264\nMvaa 36,128,196\n"
"AD 84,84,83\nCV 75,82,83\nMvaa 36,144,228\nRa 128,2,16384\nMvaa 36,148,236\ns_add_u32 s46, s86, s88\nEX 80,60\nMvaa 36,152,244\nRa 132,2,17408\nMvaa 36,156,252\nEX 81,61\n"
"Ra 136,2,18432\nAD 85,85,80\nD1 1,16,46,0\nRa 140,2,19456\nAD 85,85,81\nCV 78,80,81\nRa 144,2,20480\ns_mov_b32 s87, s95\nEX 82,62\nRa 148,2,21504\nWL 6\nEX 83,63\n"
"Cvaa 44,172,220,16\nRa 152,2,22528\nAD 85,85,82\nMvaa 44,168,212\nD1 1,16,46,1024\nMvaa 44,164,204\nRa 156,2,23552\nMvaa 44,160,196\nAD 85,85,83\nCV 79,82,83\nMvaa 44,176,228\n"
"Ra 160,2,24576\nv_max_f32 v90, v84, v85\nMvaa 44,180,236\ns_add_u32 s88, s88, 32768\nMvaa 44,184,244\nRa 164,2,25600\nv_cmp_lt_f32 vcc, s63, v90\nMvaa 44,188,252\nWL 8\n"
"Ra 168,2,26624\ns_cbranch_vccnz .T350\n.T351:\nD1 1,16,46,2048\nRa 172,2,27648\nAD 86,86,84\nMvav 128,128,72\nRa 176,2,28672\nMvav 132,128,76\nMvav 140,132,76\nRa 180,2,29696\n"
"Mvav 136,132,72\ns_waitcnt vmcnt(12) lgkmcnt(8)\nMvav 144,136,72\nRa 184,2,30720\nMvav 148,136,76\nD1 1,16,46,3072\ns_add_u32 m0, s89, 49152\nMvav 156,140,76\nRa 188,2,31744\n"
"s_barrier\nMvav 152,140,72\nMvav 160,144,72\nRa 128,24,32768\nMvav 164,144,76\ns_add_u32 s46, s87, s88\nEX 80,36\nMvav 172,148,76\nRa 132,24,33792\nMvav 168,148,72\nWL 8\n"
"EX 81,37\nMvav 176,152,72\nRa 136,24,34816\nAD 87,87,85\nMvav 180,152,76\nD1 1,16,46,0\nMvav 188,156,76\nRa 140,24,35840\nMvav 184,156,72\nAD 85,80,81\nCV 68,80,81\n"
"Mvav 192,160,72\nRa 144,26,32768\nMvav 196,160,76\ns_nop 0\nEX 82,38\nMvav 204,164,76\nRa 148,26,33792\nv_cmpx_gt_i32 vcc, 19, v0\nv_mov_b32 v19, s97\nv_cmpx_gt_i32 vcc, 18, v0\n"
"v_mov_b32 v18, s97\nv_cmpx_gt_i32 vcc, 17, v0\nv_mov_b32 v17, s97\nv_cmpx_gt_i32 vcc, 16, v0\nv_mov_b32 v16, s97\nv_cmpx_gt_i32 vcc, 3, v0\nv_mov_b32 v11, s97\n"
"v_cmpx_gt_i32 vcc, 2, v0\nv_mov_b32 v10, s97\nv_cmpx_gt_i32 vcc, 1, v0\nv_mov_b32 v9, s97\nv_cmpx_gt_i32 vcc, 0, v0\nv_mov_b32 v8, s97\ns_mov_b64 exec, -1\ns_nop 2\ns_nop 0\n"
"Mvav 200,164,72\nWL 8\nEX 83,39\nMvav 208,168,72\nRa 152,26,34816\nAD 85,85,82\nMvav 212,168,76\nD1 1,16,46,1024\nMvav 220,172,76\nRa 156,26,35840\nMvav 216,172,72\nAD 85,85,83\n"
"CV 69,82,83\nMvav 224,176,72\nRa 160,24,40960\nMvav 228,176,76\ns_xor_b32 s89, s89, 0x10000\nMvav 236,180,76\nRa 164,24,41984\nMvav 232,180,72\nWL 10\nMvav 240,184,72\n"
"Ra 168,24,43008\nMvav 244,184,76\nD1 1,16,46,2048\nMvav 252,188,76\nRa 172,24,44032\nMvav 248,188,72\nRa 176,26,40960\nRa 180,26,41984\nWL 6\nCvaa 52,140,220,8\nRa 184,26,43008\n"
"Mvaa 52,136,212\nD1 1,16,46,3072\ns_add_u32 m0, s89, 0\nMvaa 52,132,204\nRa 188,26,44032\nMvaa 52,128,196\nMvaa 52,144,228\nRa 128,2,49152\nMvaa 52,148,236\n"
"s_add_u32 s46, s86, s88\nEX 80,44\nMvaa 52,152,244\nRa 132,2,50176\nv_xor_b32 v24, 0x10000, v24\nMvaa 52,156,252\nEX 81,45\nRa 136,2,51200\nAD 85,85,80\nD1 1,16,46,0\n"
"Ra 140,2,52224\nAD 85,85,81\nCV 70,80,81\nRa 144,2,53248\nEX 82,46\nRa 148,2,54272\nv_xor_b32 v26, 0x10000, v26\nWL 6\nEX 83,47\nCvaa 60,172,220,16\nRa 152,2,55296\nAD 85,85,82\n"
"Mvaa 60,168,212\nD1 1,16,46,1024\nMvaa 60,164,204\nRa 156,2,56320\nMvaa 60,160,196\nAD 85,85,83\nCV 71,82,83\nMvaa 60,176,228\nRa 160,2,57344\nMvaa 60,180,236\n"
"s_add_u32 s88, s88, 32768\nMvaa 60,184,244\nRa 164,2,58368\nv_cmp_lt_f32 vcc, s63, v85\nMvaa 60,188,252\nWL 8\nRa 168,2,59392\ns_cbranch_vccnz .T352\n.T353:\nD1 1,16,46,2048\n"
"Ra 172,2,60416\nRa 176,2,61440\nMvav 132,128,68\nMvav 140,132,68\nRa 180,2,62464\ns_waitcnt vmcnt(11) lgkmcnt(8)\nRa 184,2,63488\nMvav 148,136,68\nD1 1,16,46,3072\n.W577:\n"
"s_cbranch_execz .WB273\ns_add_u32 m0, s89, 16384\nMvav 156,140,68\nRa 188,2,64512\ns_barrier\nRa 128,24,0\nMvav 164,144,68\ns_add_u32 s46, s87, s88\nEX 80,52\nMvav 172,148,68\n"
"Ra 132,24,1024\nv_xor_b32 v2, 0x10000, v2\nWL 8\nEX 81,53\nRa 136,24,2048\nAD 87,87,85\nMvav 180,152,68\nD1 1,16,46,0\nMvav 188,156,68\nRa 140,24,3072\nAD 85,80,81\nCV 76,80,81\n"
"Ra 144,26,0\nMvav 196,160,68\nEX 82,54\nMvav 204,164,68\nRa 148,26,1024\nWL 8\nEX 83,55\nRa 152,26,2048\nAD 85,85,82\nMvav 212,168,68\nD1 1,16,46,1024\nMvav 220,172,68\n"
"Ra 156,26,3072\nAD 85,85,83\nCV 77,82,83\nRa 160,24,8192\nMvav 228,176,68\nMvav 236,180,68\nRa 164,24,9216\nWL 10\nRa 168,24,10240\nMvav 244,184,68\nD1 1,16,46,2048\n"
"Mvav 252,188,68\nRa 172,24,11264\nD1 1,16,46,3072\ns_add_u32 m0, s89, 32768\ns_add_u32 s46, s86, s88\nRa 128,2,16384\nv_xor_b32 v3, 0x10000, v2\nv_add_u32 v0, 0x20000, v1\n"
"EX 80,60\nRa 132,2,17408\ns_sub_u32 s94, s49, 1703936\nEX 81,61\nRa 136,2,18432\nAD 85,85,80\ns_xor_b32 s92, s94, 0x202000\nD1 1,16,46,0\ns_lshl_b32 s89, s11, 12\nRa 140,2,19456\n"
"s_lshl1_add_u32 s2, s12, 1\nAD 85,85,81\nCV 78,80,81\nRa 144,2,20480\ns_lshl3_add_u32 s2, s2, s13\nEX 82,62\nRa 148,2,21504\ns_lshl4_add_u32 s2, s2, 0xc048200\nEX 83,63\n"
"Ra 152,2,22528\nAD 85,85,82\nD1 1,16,46,1024\nRa 156,2,23552\ns_and_b32 s3, s72, 0x2000\nAD 85,85,83\nCV 79,82,83\nRa 160,2,24576\ns_cmp_eq_u32 s11, 1\ns_cselect_b32 s54, s2, s54\n"
"s_cselect_b32 s3, s3, 0\nv_cmp_lt_f32 vcc, s63, v85\nRa 164,2,25600\ns_cbranch_vccnz .T354\n.T355:\nAD 87,87,85\nWL 8\nv_mov_b32 v84, v86\nv_mov_b32 v85, v87\nRa 168,2,26624\n"
"v_permlane32_swap_b32 v86, v84\nv_permlane32_swap_b32 v87, v85\nD1 1,16,46,2048\nAD 86,86,84\nRa 172,2,27648\nAD 87,87,85\nv_mov_b32 v84, v86\nv_mov_b32 v85, v87\nRa 176,2,28672\n"
"v_permlane16_swap_b32 v86, v84\nv_permlane16_swap_b32 v87, v85\nMvav 132,128,76\nAD 86,86,84\nAD 87,87,85\nMvav 140,132,76\nRa 180,2,29696\nv_rcp_f32 v86, v86\nWL 8\n"
"v_rcp_f32 v87, v87\nRa 184,2,30720\nv_xor_b32 v25, s3, v25\nMvav 148,136,76\nD1 1,16,46,3072\nMvav 156,140,76\nRa 188,2,31744\nMU 128,128,86\nMU 129,129,86\nCV 96,128,129\n"
"MU 130,130,86\nMU 131,131,86\nRa 128,2,0\nCV 97,130,131\nMU 132,132,87\nMvav 164,144,76\nMU 133,133,87\nCV 100,132,133\nMvav 172,148,76\nRa 132,2,1024\nMU 134,134,87\n"
"MU 135,135,87\nWL 8\ns_add_u32 m0, s89, 131072\ns_add_u32 s46, s96, 1212416\nRa 136,2,2048\nCV 101,134,135\nMU 136,136,86\nMvav 180,152,76\nD1 1,16,46,0\nMU 137,137,86\n"
"Mvav 188,156,76\nRa 140,2,3072\nCV 98,136,137\nMU 138,138,86\nMU 139,139,86\nCV 99,138,139\nMU 140,140,87\nRa 144,2,4096\nMU 141,141,87\nCV 102,140,141\nMvav 196,160,76\n"
"MU 142,142,87\nMU 143,143,87\nMvav 204,164,76\nRa 148,2,5120\nCV 103,142,143\nMU 144,144,86\nWL 8\nMU 145,145,86\nCV 104,144,145\nRa 152,2,6144\nMU 146,146,86\nMU 147,147,86\n"
"Mvav 212,168,76\nD1 1,16,46,1024\nCV 105,146,147\nMvav 220,172,76\nRa 156,2,7168\nMU 148,148,87\nMU 149,149,87\nCV 108,148,149\nMU 150,150,87\nMU 151,151,87\nRa 160,2,8192\n"
"CV 109,150,151\nMU 152,152,86\nMvav 228,176,76\nMU 153,153,86\nCV 106,152,153\nMvav 236,180,76\nRa 164,2,9216\nMU 154,154,86\nMU 155,155,86\nWL 8\nCV 107,154,155\nMU 156,156,87\n"
"Ra 168,2,10240\nMU 157,157,87\nCV 110,156,157\nMvav 244,184,76\nD1 1,16,46,2048\nMU 158,158,87\nMvav 252,188,76\nRa 172,2,11264\nMU 159,159,87\nCV 111,158,159\nMU 160,160,86\n"
"MU 161,161,86\nCV 112,160,161\nZvav 128,128,96\nRa 176,2,12288\nMU 162,162,86\nMU 163,163,86\nMvav 128,132,104\nCV 113,162,163\nMU 164,164,87\nZvav 132,132,108\nRa 180,2,13312\n"
"MU 165,165,87\nCV 116,164,165\nMvav 132,128,100\ns_waitcnt vmcnt(15) lgkmcnt(8)\nMU 166,166,87\nMU 167,167,87\nZvav 140,136,100\nRa 184,2,14336\nCV 117,166,167\nMU 168,168,86\n"
"Mvav 140,140,108\nD1 1,16,46,3072\nMU 169,169,86\nCV 114,168,169\nZvav 136,140,104\nRa 188,2,15360\ns_barrier\nMU 170,170,86\nMvav 136,136,96\nMU 171,171,86\nCV 115,170,171\n"
"MU 172,172,87\nZvav 144,144,96\nRa 128,2,32768\nMU 173,173,87\nCV 118,172,173\nMvav 144,148,104\nMU 174,174,87\nMU 175,175,87\n"
"buffer_store_dword v25, off, s[16:19], s54 offset:512 sc0 sc1\nZvav 148,148,108\nRa 132,2,33792\nCV 119,174,175\nMU 176,176,86\nMvav 148,144,100\nWL 8\ns_add_u32 m0, s89, 49152\n"
"s_add_u32 s46, s96, 1179648\nZvav 156,152,100\nRa 136,2,34816\nMU 177,177,86\nCV 120,176,177\nMvav 156,156,108\nD1 1,16,46,0\nMU 178,178,86\nMU 179,179,86\nZvav 152,156,104\n"
"Ra 140,2,35840\nCV 121,178,179\nMU 180,180,87\nMvav 152,152,96\nMU 181,181,87\nCV 124,180,181\nMU 182,182,87\nZvav 64,160,96\nRa 144,2,36864\nMU 183,183,87\nCV 125,182,183\n"
"Mvav 64,164,104\nMU 184,184,86\nMU 185,185,86\nZvav 68,164,108\nRa 148,2,37888\nCV 122,184,185\nMU 186,186,86\nMvav 68,160,100\nWL 8\nMU 187,187,86\nCV 123,186,187\n"
"Zvav 76,168,100\nRa 152,2,38912\nMU 188,188,87\nMU 189,189,87\nMvav 76,172,108\nD1 1,16,46,1024\nCV 126,188,189\nMU 190,190,87\nZvav 72,172,104\nRa 156,2,39936\nMU 191,191,87\n"
"CV 127,190,191\nMvav 72,168,96\nMU 192,192,86\nMU 193,193,86\nCV 32,192,193\nMvav 128,176,112\nRa 160,2,40960\nMU 194,194,86\nMU 195,195,86\nMvav 128,180,120\nCV 33,194,195\n"
"MU 196,196,87\nMU 197,197,87\nMvav 132,180,124\nRa 164,2,41984\nCV 36,196,197\nMU 198,198,87\nMvav 132,176,116\nWL 8\nMU 199,199,87\nMvav 140,184,116\nRa 168,2,43008\n"
"CV 37,198,199\nMU 200,200,86\nMvav 140,188,124\nD1 1,16,46,2048\nMU 201,201,86\nCV 34,200,201\nMvav 136,188,120\nRa 172,2,44032\nMU 202,202,86\nMU 203,203,86\nMvav 136,184,112\n"
"CV 35,202,203\nMU 204,204,87\nMU 205,205,87\nMvav 144,128,112\nRa 176,2,45056\nCV 38,204,205\nMU 206,206,87\nMvav 144,132,120\nMU 207,207,87\nCV 39,206,207\nMvav 148,132,124\n"
"Ra 180,2,46080\nMU 208,208,86\nMU 209,209,86\nMvav 148,128,116\nWL 8\nCV 40,208,209\nMU 210,210,86\nMvav 156,136,116\nRa 184,2,47104\nMU 211,211,86\nCV 41,210,211\n"
"Mvav 156,140,124\nD1 1,16,46,3072\nMU 212,212,87\nMU 213,213,87\nMvav 152,140,120\nRa 188,2,48128\nCV 44,212,213\nMvav 152,136,112\nMU 214,214,87\nMU 215,215,87\nCV 45,214,215\n"
"Mvav 64,144,112\nRa 128,2,49152\nMU 216,216,86\nMU 217,217,86\nMvav 64,148,120\nCV 42,216,217\nMU 218,218,86\nMU 219,219,86\nMvav 68,148,124\nRa 132,2,50176\nCV 43,218,219\n"
"MU 220,220,87\nMvav 68,144,116\nWL 8\ns_add_u32 m0, s89, 147456\ns_add_u32 s46, s96, 1245184\nMvav 76,152,116\nRa 136,2,51200\nMU 221,221,87\nCV 46,220,221\nMvav 76,156,124\n"
"D1 1,16,46,0\nMU 222,222,87\nMU 223,223,87\nMvav 72,156,120\nRa 140,2,52224\nCV 47,222,223\nMvav 72,152,112\nMU 224,224,86\nMU 225,225,86\nCV 48,224,225\nMvav 128,160,32\n"
"Ra 144,2,53248\nMU 226,226,86\nMU 227,227,86\nMvav 128,164,40\nCV 49,226,227\nMU 228,228,87\nMvav 132,164,44\nRa 148,2,54272\nMU 229,229,87\nCV 52,228,229\nMvav 132,160,36\nWL 8\n"
"MU 230,230,87\nMU 231,231,87\nMvav 140,168,36\nRa 152,2,55296\nCV 53,230,231\nMU 232,232,86\nMvav 140,172,44\nD1 1,16,46,1024\nMU 233,233,86\nCV 50,232,233\nMvav 136,172,40\n"
"Ra 156,2,56320\nMU 234,234,86\nMvav 136,168,32\nMU 235,235,86\nCV 51,234,235\nMU 236,236,87\nMvav 144,176,32\nRa 160,2,57344\nMU 237,237,87\nCV 54,236,237\nMvav 144,180,40\n"
"MU 238,238,87\nMU 239,239,87\nCV 55,238,239\nMvav 148,180,44\nRa 164,2,58368\nMU 240,240,86\nMU 241,241,86\nMvav 148,176,36\nWL 8\nCV 56,240,241\nMvav 156,184,36\nRa 168,2,59392\n"
"MU 242,242,86\nMU 243,243,86\nMvav 156,188,44\nD1 1,16,46,2048\nCV 57,242,243\nMU 244,244,87\nMvav 152,188,40\nRa 172,2,60416\nMU 245,245,87\nCV 60,244,245\nMvav 152,184,32\n"
"MU 246,246,87\nMU 247,247,87\nCV 61,246,247\nMvav 64,128,32\nRa 176,2,61440\nMU 248,248,86\nMU 249,249,86\nMvav 64,132,40\nCV 58,248,249\nMU 250,250,86\nMvav 68,132,44\n"
"Ra 180,2,62464\nMU 251,251,86\nCV 59,250,251\nMvav 68,128,36\ns_waitcnt vmcnt(16) lgkmcnt(8)\nMU 252,252,87\nMU 253,253,87\nMvav 76,136,36\nRa 184,2,63488\nCV 62,252,253\n"
"MU 254,254,87\nMvav 76,140,44\nD1 1,16,46,3072\nMU 255,255,87\nCV 63,254,255\nMvav 72,140,40\nRa 188,2,64512\ns_barrier\nMvav 72,136,32\nMvav 128,144,48\nRa 128,3,0\n"
"Mvav 128,148,56\nMvav 132,148,60\nRa 132,3,1024\nMvav 132,144,52\nWL 8\nMvav 140,152,52\nRa 136,3,2048\nMvav 140,156,60\nMvav 136,156,56\nRa 140,3,3072\nMvav 136,152,48\n"
"Mvav 144,160,48\nRa 144,3,4096\nMvav 144,164,56\nMvav 148,164,60\nRa 148,3,5120\nCV 132,132,133\nMvav 148,160,52\nWL 8\nCV 133,134,135\nMvav 156,168,52\nRa 152,3,6144\n"
"CV 134,140,141\nMvav 156,172,60\nCV 135,142,143\ns_add_u32 s5, s94, 0\nMvav 152,172,56\nRa 156,3,7168\nSv 132,1,16,5,1024\nMvav 152,168,48\nCV 128,128,129\nCV 129,130,131\n"
"Mvav 64,176,48\nRa 160,3,8192\nCV 130,136,137\nMvav 64,180,56\nbuffer_load_dword v27, off, s[16:19], s60 offset:512 sc0 sc1\nCV 131,138,139\nMvav 68,180,60\nRa 164,3,9216\n"
"Sv 128,1,16,5,0\nMvav 68,176,52\nWL 6\nCV 148,148,149\nMvav 76,184,52\nRa 168,3,10240\nCV 149,150,151\nMvav 76,188,60\nCV 150,156,157\nCV 151,158,159\nMvav 72,188,56\n"
"Ra 172,3,11264\ns_add_u32 s5, s94, 131072\nMvav 72,184,48\nSv 148,1,16,5,1024\nCV 144,144,145\nZvav 160,128,96\nRa 176,3,12288\nCV 145,146,147\nMvav 160,132,104\nCV 146,152,153\n"
"CV 147,154,155\nMvav 160,136,112\nRa 180,3,13312\nSv 144,1,16,5,0\nMvav 160,140,120\nWL 6\nCV 68,68,69\nZvav 164,140,124\nRa 184,3,14336\nCV 69,70,71\nMvav 164,136,116\n"
"CV 70,76,77\nCV 71,78,79\nMvav 164,132,108\nRa 188,3,15360\ns_add_u32 s5, s94, 262144\nMvav 164,128,100\nSv 68,1,16,5,1024\nCV 64,64,65\nMvav 164,144,36\nRa 128,3,16384\n"
"CV 65,66,67\nMvav 164,148,44\nCV 66,72,73\nCV 67,74,75\nMvav 164,152,52\nRa 132,3,17408\nSv 64,1,16,5,0\nMvav 164,156,60\nWL 6\nMvav 160,156,56\nRa 136,3,18432\nMvav 160,152,48\n"
"Mvav 160,148,40\nRa 140,3,19456\nMvav 160,144,32\nZvav 168,160,96\nRa 144,3,20480\nMvav 168,164,104\nMvav 168,168,112\nRa 148,3,21504\nMvav 168,172,120\nWL 6\nZvav 172,172,124\n"
"Ra 152,3,22528\nMvav 172,168,116\nMvav 172,164,108\nRa 156,3,23552\nMvav 172,160,100\nMvav 172,176,36\nRa 160,3,24576\nMvav 172,180,44\nMvav 172,184,52\nRa 164,3,25600\n"
"Mvav 172,188,60\nWL 6\nMvav 168,188,56\nRa 168,3,26624\nMvav 168,184,48\nMvav 168,180,40\nRa 172,3,27648\nMvav 168,176,32\nZvav 176,128,96\nRa 176,3,28672\nCV 164,164,165\n"
"Mvav 176,132,104\nCV 165,166,167\nCV 166,172,173\nMvav 176,136,112\nRa 180,3,29696\nCV 167,174,175\nMvav 176,140,120\ns_waitcnt vmcnt(11) lgkmcnt(6)\ns_add_u32 s5, s94, 393216\n"
"Zvav 180,140,124\nRa 184,3,30720\n.W578:\ns_cbranch_execz .WB273\nSv 164,1,16,5,1024\nMvav 180,136,116\nCV 160,160,161\nCV 161,162,163\nMvav 180,132,108\nRa 188,3,31744\n"
"s_barrier\nMvav 180,128,100\nCV 162,168,169\nCV 163,170,171\nMvav 180,144,36\nRa 128,3,32768\nSv 160,1,16,5,0\nMvav 180,148,44\nMvav 180,152,52\nRa 132,3,33792\nMvav 180,156,60\n"
"WL 6\ns_add_u32 m0, s89, 81920\ns_add_u32 s46, s96, 1277952\nMvav 176,156,56\nRa 136,3,34816\nMvav 176,152,48\nD1 1,16,46,0\nMvav 176,148,40\nRa 140,3,35840\nMvav 176,144,32\n"
"Zvav 184,160,96\nRa 144,3,36864\nMvav 184,164,104\nMvav 184,168,112\nRa 148,3,37888\nMvav 184,172,120\nWL 6\nZvav 188,172,124\nRa 152,3,38912\nMvav 188,168,116\nD1 1,16,46,1024\n"
"Mvav 188,164,108\nRa 156,3,39936\nMvav 188,160,100\nMvav 188,176,36\nRa 160,3,40960\nWV 9\nv_cmp_ne_u32 vcc, s15, v27\ns_nop 0\ns_cbranch_vccnz .S380\n.S381:\nMvav 188,180,44\n"
"s_add_u32 s4, s92, 0\nMvav 188,184,52\nRa 164,3,41984\nMvav 188,188,60\nWL 6\nbuffer_load_dwordx4 v[208:211], v1, s[16:19], s4 offen offset:0 sc0 sc1\nMvav 184,188,56\n"
"Ra 168,3,43008\nMvav 184,184,48\nD1 1,16,46,2048\nMvav 184,180,40\nRa 172,3,44032\nMvav 184,176,32\nbuffer_load_dwordx4 v[212:215], v1, s[16:19], s4 offen offset:1024 sc0 sc1\n"
"Zvav 192,128,96\nRa 176,3,45056\nv_lshrrev_b32 v88, 8, v1\nMvav 192,132,104\nv_lshlrev_b32 v89, 5, v88\nCV 180,180,181\nMvav 192,136,112\nRa 180,3,46080\nCV 181,182,183\n"
"Mvav 192,140,120\nWL 6\nCV 182,188,189\nZvav 196,140,124\nRa 184,3,47104\nv_bfe_u32 v90, v1, 4, 4\nMvav 196,136,116\nD1 1,16,46,3072\nCV 183,190,191\nMvav 196,132,108\n"
"Ra 188,3,48128\ns_add_u32 s5, s94, 524288\nMvav 196,128,100\nv_mul_u32_u24 v91, 896, v90\nSv 180,1,16,5,1024\nMvav 196,144,36\nRa 128,3,49152\nv_lshl_add_u32 v91, v88, 4, v91\n"
"Mvav 196,148,44\nv_add_u32 v90, s21, v90\nCV 176,176,177\nMvav 196,152,52\nRa 132,3,50176\nCV 177,178,179\nMvav 196,156,60\nWL 6\ns_add_u32 m0, s89, 65536\n"
"s_add_u32 s46, s96, 1310720\nMvav 192,156,56\nRa 136,3,51200\nv_lshlrev_b32 v88, 3, v90\nMvav 192,152,48\nD1 1,16,46,0\nCV 178,184,185\nMvav 192,148,40\nRa 140,3,52224\n"
"CV 179,186,187\nMvav 192,144,32\nv_add_u32 v94, 64, v90\nSv 176,1,16,5,0\nZvav 200,160,96\nRa 144,3,53248\ns_mov_b32 s63, 0xc040000\nMvav 200,164,104\nv_lshrrev_b32 v92, 8, v1\n"
"Mvav 200,168,112\nRa 148,3,54272\nMvav 200,172,120\nWL 6\nZvav 204,172,124\nRa 152,3,55296\nv_bfe_u32 v93, v1, 6, 2\nMvav 204,168,116\nD1 1,16,46,1024\nv_cmp_eq_u32 vcc, v92, v93\n"
"v_and_b32 v92, 16, v1\nv_mov_b32 v93, 0x3f80\nv_cndmask_b32 v93, 0, v93, vcc\nMvav 204,164,108\nRa 156,3,56320\nMvav 204,160,100\nv_lshlrev_b32 v93, v92, v93\nMvav 204,176,36\n"
"Ra 160,3,57344\nv_and_b32 v92, 32, v1\nMvav 204,180,44\ns_add_u32 s4, s92, 131072\nv_cmp_eq_u32 vcc, 0, v92\nv_mov_b32 v4, 0\nv_mov_b32 v5, 0\nv_cndmask_b32 v6, 0, v93, vcc\n"
"Mvav 204,184,52\nRa 164,3,58368\nMvav 204,188,60\nWL 6\nbuffer_load_dwordx4 v[216:219], v1, s[16:19], s4 offen offset:0 sc0 sc1\nMvav 200,188,56\nRa 168,3,59392\n"
"v_sub_u32 v7, v93, v6\nMvav 200,184,48\nD1 1,16,46,2048\nMvav 200,180,40\nRa 172,3,60416\nMvav 200,176,32\n"
"buffer_load_dwordx4 v[220:223], v1, s[16:19], s4 offen offset:1024 sc0 sc1\nZvav 160,128,96\nRa 176,3,61440\nv_mov_b32 v8, 0\nMvav 160,132,104\nv_mov_b32 v9, 0\nCV 196,196,197\n"
"Mvav 160,136,112\nRa 180,3,62464\nCV 197,198,199\nMvav 160,140,120\nWL 6\nCV 198,204,205\nZvav 164,140,124\nRa 184,3,63488\nCV 199,206,207\nMvav 164,136,116\nD1 1,16,46,3072\n"
"s_add_u32 s5, s94, 655360\nMvav 164,132,108\nRa 188,3,64512\ns_barrier\nMvav 164,128,100\nSv 196,1,16,5,1024\nCV 192,192,193\nMvav 164,144,36\nRa 128,0,0\nCV 193,194,195\n"
"Mvav 164,148,44\nCV 194,200,201\nCV 195,202,203\nMvav 164,152,52\nRa 132,0,1024\nSv 192,1,16,5,0\nMvav 164,156,60\nWL 6\ns_add_u32 m0, s89, 98304\ns_add_u32 s46, s96, 1343488\n"
"Mvav 160,156,56\nRa 136,0,2048\nMvav 160,152,48\nD1 1,16,46,0\nMvav 160,148,40\nRa 140,0,3072\nMvav 160,144,32\nZvav 168,160,96\nRa 144,0,4096\nWV 12\nv_max_u32 v21, v211, v215\n"
"v_cmp_eq_u32 vcc, -1, v21\ns_nop 0\ns_cbranch_vccnz .S356\n.S357:\nMvav 168,164,104\nv_lshlrev_b32 v176, 16, v208\nMvav 168,168,112\nRa 148,0,5120\nMvav 168,172,120\nWL 6\n"
"v_and_b32 v177, 0xffff0000, v208\nZvav 172,172,124\nRa 152,0,6144\nMvav 172,168,116\nD1 1,16,46,1024\nMvav 172,164,108\nRa 156,0,7168\nv_lshlrev_b32 v178, 16, v209\n"
"Mvav 172,160,100\nv_and_b32 v179, 0xffff0000, v209\nMvav 172,176,36\nRa 160,0,8192\nv_lshlrev_b32 v180, 16, v212\nMvav 172,180,44\nv_and_b32 v181, 0xffff0000, v212\n"
"s_add_u32 s4, s92, 262144\nMvav 172,184,52\nRa 164,0,9216\nMvav 172,188,60\nWL 6\nv_lshlrev_b32 v182, 16, v213\n"
"buffer_load_dwordx4 v[224:227], v1, s[16:19], s4 offen offset:0 sc0 sc1\nMvav 168,188,56\nRa 168,0,10240\nMvav 168,184,48\nD1 1,16,46,2048\nMvav 168,180,40\nRa 172,0,11264\n"
"v_and_b32 v183, 0xffff0000, v213\nMvav 168,176,32\nv_lshlrev_b32 v184, 16, v210\nbuffer_load_dwordx4 v[228:231], v1, s[16:19], s4 offen offset:1024 sc0 sc1\nMvav 176,128,96\n"
"Ra 176,0,12288\nv_and_b32 v185, 0xffff0000, v210\nMvav 176,132,104\nv_lshlrev_b32 v186, 16, v211\nCV 164,164,165\nMvav 176,136,112\nRa 180,0,13312\nCV 165,166,167\n"
"Mvav 176,140,120\nWL 6\nCV 166,172,173\nMvav 180,140,124\nRa 184,0,14336\nv_and_b32 v187, 0xffff0000, v211\nMvav 180,136,116\nD1 1,16,46,3072\nCV 167,174,175\nMvav 180,132,108\n"
"Ra 188,0,15360\ns_add_u32 s5, s94, 786432\nMvav 180,128,100\nv_lshlrev_b32 v188, 16, v214\nSv 164,1,16,5,1024\nMvav 180,144,36\nRa 128,0,16384\nv_and_b32 v189, 0xffff0000, v214\n"
"Mvav 180,148,44\nv_lshlrev_b32 v190, 16, v215\nCV 160,160,161\nMvav 180,152,52\nRa 132,0,17408\nCV 161,162,163\nMvav 180,156,60\nWL 6\ns_add_u32 m0, s89, 114688\n"
"s_add_u32 s46, s96, 1376256\nMvav 176,156,56\nRa 136,0,18432\nv_and_b32 v191, 0xffff0000, v215\nMvav 176,152,48\nD1 1,16,46,0\nCV 162,168,169\nMvav 176,148,40\nRa 140,0,19456\n"
"CV 163,170,171\nMvav 176,144,32\nSv 160,1,16,5,0\nMvav 184,160,96\nRa 144,0,20480\nWV 12\nv_max_u32 v21, v219, v223\nv_cmp_eq_u32 vcc, -1, v21\ns_nop 0\ns_cbranch_vccnz .S359\n"
".S360:\nMvav 184,164,104\nv_lshlrev_b32 v192, 16, v216\nMvav 184,168,112\nRa 148,0,21504\nMvav 184,172,120\nWL 6\nv_and_b32 v193, 0xffff0000, v216\nMvav 188,172,124\n"
"Ra 152,0,22528\nMvav 188,168,116\nD1 1,16,46,1024\nMvav 188,164,108\nRa 156,0,23552\nv_lshlrev_b32 v194, 16, v217\nMvav 188,160,100\nv_and_b32 v195, 0xffff0000, v217\n"
"Mvav 188,176,36\nRa 160,0,24576\nv_lshlrev_b32 v196, 16, v220\nMvav 188,180,44\nv_and_b32 v197, 0xffff0000, v220\ns_add_u32 s4, s92, 393216\nMvav 188,184,52\nRa 164,0,25600\n"
"Mvav 188,188,60\nWL 6\nv_lshlrev_b32 v198, 16, v221\nbuffer_load_dwordx4 v[208:211], v1, s[16:19], s4 offen offset:0 sc0 sc1\nMvav 184,188,56\nRa 168,0,26624\nMvav 184,184,48\n"
"D1 1,16,46,2048\nMvav 184,180,40\nRa 172,0,27648\nv_and_b32 v199, 0xffff0000, v221\nMvav 184,176,32\nv_lshlrev_b32 v200, 16, v218\n"
"buffer_load_dwordx4 v[212:215], v1, s[16:19], s4 offen offset:1024 sc0 sc1\nMvav 192,128,96\nRa 176,0,28672\nv_and_b32 v201, 0xffff0000, v218\nMvav 192,132,104\n"
"v_lshlrev_b32 v202, 16, v219\nCV 180,180,181\nMvav 192,136,112\nRa 180,0,29696\nCV 181,182,183\nMvav 192,140,120\ns_waitcnt vmcnt(15) lgkmcnt(6)\nCV 182,188,189\nMvav 196,140,124\n"
"Ra 184,0,30720\nv_and_b32 v203, 0xffff0000, v219\nMvav 196,136,116\nD1 1,16,46,3072\nCV 183,190,191\nMvav 196,132,108\nRa 188,0,31744\ns_barrier\nMvav 196,128,100\n"
"v_lshlrev_b32 v204, 16, v222\ns_add_u32 s5, s94, 917504\nMvav 196,144,36\nRa 128,2,16384\nv_and_b32 v205, 0xffff0000, v222\nMvav 196,148,44\nv_lshlrev_b32 v206, 16, v223\n"
"Sv 180,1,16,5,1024\nMvav 196,152,52\nRa 132,2,17408\nCV 176,176,177\nMvav 196,156,60\nWL 6\ns_add_u32 m0, s89, 0\ns_add_u32 s46, s96, 1409024\nMvav 192,156,56\nRa 136,2,18432\n"
"v_and_b32 v207, 0xffff0000, v223\nMvav 192,152,48\nD1 1,16,46,0\nCV 177,178,179\nMvav 192,148,40\nRa 140,2,19456\nCV 178,184,185\nMvav 192,144,32\nCV 179,186,187\nSv 176,1,16,5,0\n"
"Mvav 200,160,96\nRa 144,2,20480\nWV 12\nv_max_u32 v21, v227, v231\nv_cmp_eq_u32 vcc, -1, v21\ns_nop 0\ns_cbranch_vccnz .S362\n.S363:\nMvav 200,164,104\n"
"v_lshlrev_b32 v160, 16, v224\nMvav 200,168,112\nRa 148,2,21504\nMvav 200,172,120\nWL 6\nv_and_b32 v161, 0xffff0000, v224\nMvav 204,172,124\nRa 152,2,22528\nMvav 204,168,116\n"
"D1 1,16,46,1024\nMvav 204,164,108\nRa 156,2,23552\nv_lshlrev_b32 v162, 16, v225\nMvav 204,160,100\nv_and_b32 v163, 0xffff0000, v225\nMvav 204,176,36\nRa 160,2,24576\n"
"v_lshlrev_b32 v164, 16, v228\nMvav 204,180,44\nv_and_b32 v165, 0xffff0000, v228\ns_add_u32 s4, s92, 524288\nMvav 204,184,52\nRa 164,2,25600\nMvav 204,188,60\nWL 6\n"
"v_lshlrev_b32 v166, 16, v229\nbuffer_load_dwordx4 v[216:219], v1, s[16:19], s4 offen offset:0 sc0 sc1\nMvav 200,188,56\nRa 168,2,26624\nMvav 200,184,48\nD1 1,16,46,2048\n"
"Mvav 200,180,40\nRa 172,2,27648\nv_and_b32 v167, 0xffff0000, v229\nMvav 200,176,32\nv_lshlrev_b32 v168, 16, v226\n"
"buffer_load_dwordx4 v[220:223], v1, s[16:19], s4 offen offset:1024 sc0 sc1\nMvav 160,128,96\nRa 176,2,28672\nv_and_b32 v169, 0xffff0000, v226\nMvav 160,132,104\n"
"v_lshlrev_b32 v170, 16, v227\nCV 196,196,197\nMvav 160,136,112\nRa 180,2,29696\nCV 197,198,199\nMvav 160,140,120\nWL 6\nCV 198,204,205\nMvav 164,140,124\nRa 184,2,30720\n"
"v_and_b32 v24, 0x70, v1\nMvav 164,136,116\nD1 1,16,46,3072\nCV 199,206,207\nMvav 164,132,108\nRa 188,2,31744\nv_and_b32 v171, 0xffff0000, v227\nMvav 164,128,100\n"
"v_lshlrev_b32 v172, 16, v230\ns_add_u32 s5, s94, 1048576\nMvav 164,144,36\nRa 128,2,0\nv_lshlrev_b32 v24, 5, v24\nMvav 164,148,44\nv_and_b32 v173, 0xffff0000, v230\n"
"Sv 196,1,16,5,1024\nMvav 164,152,52\nRa 132,2,1024\nCV 192,192,193\nMvav 164,156,60\nWL 6\nv_lshlrev_b32 v174, 16, v231\nMvav 160,156,56\nRa 136,2,2048\n"
"buffer_load_dword v27, v24, s[16:19], s41 offen sc0 sc1\nMvav 160,152,48\nv_and_b32 v175, 0xffff0000, v231\nCV 193,194,195\nMvav 160,148,40\nRa 140,2,3072\nCV 194,200,201\n"
"Mvav 160,144,32\nCV 195,202,203\nSv 192,1,16,5,0\nMvav 168,160,96\nRa 144,2,4096\nWV 12\nv_max_u32 v21, v211, v215\nv_cmp_eq_u32 vcc, -1, v21\ns_nop 0\ns_cbranch_vccnz .S365\n"
".S366:\nMvav 168,164,104\nv_lshlrev_b32 v176, 16, v208\nMvav 168,168,112\nRa 148,2,5120\nMvav 168,172,120\nWL 6\nMvav 172,172,124\nRa 152,2,6144\nv_and_b32 v177, 0xffff0000, v208\n"
"Mvav 172,168,116\nv_lshlrev_b32 v178, 16, v209\nMvav 172,164,108\nRa 156,2,7168\nMvav 172,160,100\nv_and_b32 v179, 0xffff0000, v209\nMvav 172,176,36\nRa 160,2,8192\n"
"v_lshlrev_b32 v180, 16, v212\nMvav 172,180,44\nv_and_b32 v181, 0xffff0000, v212\ns_add_u32 s4, s92, 655360\nMvav 172,184,52\nRa 164,2,9216\nMvav 172,188,60\nWL 6\n"
"buffer_load_dwordx4 v[224:227], v1, s[16:19], s4 offen offset:0 sc0 sc1\nMvav 168,188,56\nRa 168,2,10240\nv_lshlrev_b32 v182, 16, v213\nMvav 168,184,48\n"
"v_and_b32 v183, 0xffff0000, v213\nMvav 168,180,40\nRa 172,2,11264\nMvav 168,176,32\nv_lshlrev_b32 v184, 16, v210\n"
"buffer_load_dwordx4 v[228:231], v1, s[16:19], s4 offen offset:1024 sc0 sc1\nMvav 176,128,96\nRa 176,2,12288\nv_and_b32 v185, 0xffff0000, v210\nMvav 176,132,104\n"
"v_lshlrev_b32 v186, 16, v211\nCV 164,164,165\nMvav 176,136,112\nRa 180,2,13312\nCV 165,166,167\nMvav 176,140,120\ns_waitcnt vmcnt(13) lgkmcnt(6)\nCV 166,172,173\nMvav 180,140,124\n"
"Ra 184,2,14336\nCV 167,174,175\nMvav 180,136,116\nv_and_b32 v187, 0xffff0000, v211\ns_add_u32 s5, s94, 1179648\nMvav 180,132,108\nRa 188,2,15360\ns_barrier\nMvav 180,128,100\n"
"v_lshlrev_b32 v188, 16, v214\nSv 164,1,16,5,1024\nMvav 180,144,36\nRa 128,2,32768\nv_and_b32 v189, 0xffff0000, v214\nMvav 180,148,44\nv_lshlrev_b32 v190, 16, v215\nCV 160,160,161\n"
"Mvav 180,152,52\nRa 132,2,33792\nCV 161,162,163\nMvav 180,156,60\nWL 6\nCV 162,168,169\nMvav 176,156,56\nRa 136,2,34816\nCV 163,170,171\nMvav 176,152,48\n"
"v_and_b32 v191, 0xffff0000, v215\n.W579:\ns_cbranch_execz .WB273\nSv 160,1,16,5,0\nMvav 176,148,40\nRa 140,2,35840\nMvav 176,144,32\nMvav 184,160,96\nRa 144,2,36864\nWV 8\n"
"v_max_u32 v21, v219, v223\nv_cmp_eq_u32 vcc, -1, v21\ns_nop 0\ns_cbranch_vccnz .S368\n.S369:\nMvav 184,164,104\nv_lshlrev_b32 v192, 16, v216\nMvav 184,168,112\nRa 148,2,37888\n"
"Mvav 184,172,120\nWL 6\nMvav 188,172,124\nRa 152,2,38912\nv_and_b32 v193, 0xffff0000, v216\nMvav 188,168,116\nv_lshlrev_b32 v194, 16, v217\nMvav 188,164,108\nRa 156,2,39936\n"
"Mvav 188,160,100\nv_and_b32 v195, 0xffff0000, v217\nMvav 188,176,36\nRa 160,2,40960\nv_lshlrev_b32 v196, 16, v220\nMvav 188,180,44\nv_and_b32 v197, 0xffff0000, v220\n"
"s_add_u32 s4, s92, 786432\nMvav 188,184,52\nRa 164,2,41984\nMvav 188,188,60\nWL 6\nbuffer_load_dwordx4 v[208:211], v1, s[16:19], s4 offen offset:0 sc0 sc1\nMvav 184,188,56\n"
"Ra 168,2,43008\nv_lshlrev_b32 v198, 16, v221\nMvav 184,184,48\nv_and_b32 v199, 0xffff0000, v221\nMvav 184,180,40\nRa 172,2,44032\nMvav 184,176,32\nv_lshlrev_b32 v200, 16, v218\n"
"buffer_load_dwordx4 v[212:215], v1, s[16:19], s4 offen offset:1024 sc0 sc1\nMvav 192,128,96\nRa 176,2,45056\nv_and_b32 v201, 0xffff0000, v218\nMvav 192,132,104\n"
"v_lshlrev_b32 v202, 16, v219\nCV 180,180,181\nMvav 192,136,112\nRa 180,2,46080\nCV 181,182,183\nMvav 192,140,120\nWL 6\nCV 182,188,189\nMvav 196,140,124\nRa 184,2,47104\n"
"CV 183,190,191\nMvav 196,136,116\nv_and_b32 v203, 0xffff0000, v219\ns_add_u32 s5, s94, 1310720\nMvav 196,132,108\nRa 188,2,48128\nSv 180,1,16,5,1024\nMvav 196,128,100\n"
"v_lshlrev_b32 v204, 16, v222\nCV 176,176,177\nMvav 196,144,36\nRa 128,2,49152\nv_and_b32 v205, 0xffff0000, v222\nMvav 196,148,44\nv_lshlrev_b32 v206, 16, v223\nCV 177,178,179\n"
"Mvav 196,152,52\nRa 132,2,50176\nCV 178,184,185\nMvav 196,156,60\nWL 6\nCV 179,186,187\nMvav 192,156,56\nRa 136,2,51200\nSv 176,1,16,5,0\nMvav 192,152,48\n"
"v_and_b32 v207, 0xffff0000, v223\nMvav 192,148,40\nRa 140,2,52224\nMvav 192,144,32\nMvav 200,160,96\nRa 144,2,53248\nWV 6\nv_max_u32 v21, v227, v231\nv_cmp_eq_u32 vcc, -1, v21\n"
"s_nop 0\ns_cbranch_vccnz .S371\n.S372:\nMvav 200,164,104\nv_lshlrev_b32 v160, 16, v224\nv_xor_b32 v27, s15, v27\nv_and_b32 v26, 0xffffdfff, v27\nv_cmp_ne_u32 s[64:65], 0, v27\n"
"v_cmp_ne_u32 vcc, 0, v26\ns_nop 0\ns_cbranch_vccnz .S383\n.S384:\nMvav 200,168,112\nRa 148,2,54272\ns_bitcmp1_b32 s64, 0\ns_cselect_b32 s2, 0x2000, 0\nMvav 200,172,120\nWL 6\n"
"Mvav 204,172,124\nRa 152,2,55296\nv_and_b32 v161, 0xffff0000, v224\nMvav 204,168,116\nv_lshlrev_b32 v162, 16, v225\nMvav 204,164,108\nRa 156,2,56320\ns_xor_b32 s2, s2, s20\n"
"Mvav 204,160,100\nv_and_b32 v163, 0xffff0000, v225\nMvav 204,176,36\nRa 160,2,57344\nv_lshlrev_b32 v164, 16, v228\nMvav 204,180,44\nv_and_b32 v165, 0xffff0000, v228\n"
"Mvav 204,184,52\nRa 164,2,58368\nMvav 204,188,60\nWL 6\ns_add_u32 s24, s2, 0x0\nMvav 200,188,56\nRa 168,2,59392\nv_lshlrev_b32 v166, 16, v229\nMvav 200,184,48\n"
"v_and_b32 v167, 0xffff0000, v229\ns_bitcmp1_b32 s64, 1\ns_cselect_b32 s2, 0x2000, 0\nMvav 200,180,40\nRa 172,2,60416\nMvav 200,176,32\nv_lshlrev_b32 v168, 16, v226\n"
"Mvav 160,128,96\nRa 176,2,61440\nv_and_b32 v169, 0xffff0000, v226\nMvav 160,132,104\nv_lshlrev_b32 v170, 16, v227\nCV 196,196,197\nMvav 160,136,112\nRa 180,2,62464\n"
"CV 197,198,199\nMvav 160,140,120\nWL 6\nCV 198,204,205\nMvav 164,140,124\nRa 184,2,63488\nCV 199,206,207\nMvav 164,136,116\nv_and_b32 v171, 0xffff0000, v227\n"
"s_add_u32 s5, s94, 1441792\nMvav 164,132,108\nRa 188,2,64512\ns_barrier\nMvav 164,128,100\nv_lshlrev_b32 v172, 16, v230\nSv 196,1,16,5,1024\nMvav 164,144,36\nRa 128,3,0\n"
"v_and_b32 v173, 0xffff0000, v230\nMvav 164,148,44\nv_lshlrev_b32 v174, 16, v231\nCV 192,192,193\nMvav 164,152,52\nRa 132,3,1024\nCV 193,194,195\nMvav 164,156,60\nWL 6\n"
"s_xor_b32 s2, s2, s20\nMvav 160,156,56\nRa 136,3,2048\nCV 194,200,201\nMvav 160,152,48\nv_and_b32 v175, 0xffff0000, v231\nCV 195,202,203\nMvav 160,148,40\nRa 140,3,3072\n"
"Sv 192,1,16,5,0\nMvav 160,144,32\ns_add_u32 s25, s2, 0x1800000\ns_bitcmp1_b32 s64, 2\ns_cselect_b32 s2, 0x2000, 0\nMvav 168,160,96\nRa 144,3,4096\nWV 4\nv_max_u32 v21, v211, v215\n"
"v_cmp_eq_u32 vcc, -1, v21\ns_nop 0\ns_cbranch_vccnz .S374\n.S375:\nMvav 168,164,104\nv_lshlrev_b32 v176, 16, v208\nMvav 168,168,112\nRa 148,3,5120\nMvav 168,172,120\nWL 6\n"
"Mvav 172,172,124\nRa 152,3,6144\nv_and_b32 v177, 0xffff0000, v208\nMvav 172,168,116\nv_lshlrev_b32 v178, 16, v209\nMvav 172,164,108\nRa 156,3,7168\ns_xor_b32 s2, s2, s20\n"
"Mvav 172,160,100\nv_and_b32 v179, 0xffff0000, v209\nMvav 172,176,36\nRa 160,3,8192\nv_lshlrev_b32 v180, 16, v212\nMvav 172,180,44\nv_and_b32 v181, 0xffff0000, v212\n"
"Mvav 172,184,52\nRa 164,3,9216\nMvav 172,188,60\nWL 6\ns_add_u32 s26, s2, 0x3000000\nMvav 168,188,56\nRa 168,3,10240\nv_lshlrev_b32 v182, 16, v213\nMvav 168,184,48\n"
"v_and_b32 v183, 0xffff0000, v213\ns_bitcmp1_b32 s64, 3\ns_cselect_b32 s2, 0x2000, 0\nMvav 168,180,40\nRa 172,3,11264\nMvav 168,176,32\nv_lshlrev_b32 v184, 16, v210\n"
"Mvav 176,128,96\nRa 176,3,12288\ns_xor_b32 s2, s2, s20\nMvav 176,132,104\nv_and_b32 v185, 0xffff0000, v210\nCV 164,164,165\nMvav 176,136,112\nRa 180,3,13312\nCV 165,166,167\n"
"Mvav 176,140,120\nWL 6\nCV 166,172,173\nMvav 180,140,124\nRa 184,3,14336\nCV 167,174,175\nMvav 180,136,116\nv_lshlrev_b32 v186, 16, v211\ns_add_u32 s5, s94, 1572864\n"
"Mvav 180,132,108\nRa 188,3,15360\ns_add_u32 s27, s2, 0x4800000\nMvav 180,128,100\nv_and_b32 v187, 0xffff0000, v211\ns_bitcmp1_b32 s64, 4\ns_cselect_b32 s2, 0x2000, 0\n"
"Mvav 180,144,36\nv_lshlrev_b32 v188, 16, v214\nSv 164,1,16,5,1024\nMvav 180,148,44\nv_and_b32 v189, 0xffff0000, v214\nCV 160,160,161\nMvav 180,152,52\n"
"v_lshlrev_b32 v190, 16, v215\nCV 161,162,163\nMvav 180,156,60\nWL 4\ns_xor_b32 s2, s2, s20\nMvav 176,156,56\nv_and_b32 v191, 0xffff0000, v215\nCV 162,168,169\nMvav 176,152,48\n"
"s_add_u32 s32, s2, 0x6000000\ns_bitcmp1_b32 s64, 5\ns_cselect_b32 s2, 0x2000, 0\nMvav 176,148,40\ns_xor_b32 s2, s2, s20\nCV 163,170,171\nMvav 176,144,32\n"
"s_add_u32 s33, s2, 0x7800000\ns_bitcmp1_b32 s64, 6\ns_cselect_b32 s2, 0x2000, 0\nMvav 184,160,96\ns_xor_b32 s2, s2, s20\ns_add_u32 s34, s2, 0x9000000\nMvav 184,164,104\n"
"s_bitcmp1_b32 s64, 7\ns_cselect_b32 s2, 0x2000, 0\nMvav 184,168,112\ns_xor_b32 s2, s2, s20\nSv 160,1,16,5,0\nMvav 184,172,120\nWL 0\nMvav 188,172,124\n"
"s_add_u32 s35, s2, 0xa800000\nMvav 188,168,116\nMvav 188,164,108\nMvav 188,160,100\nMvav 188,176,36\nMvav 188,180,44\nMvav 188,184,52\nMvav 188,188,60\nMvav 184,188,56\n"
"Mvav 184,184,48\nMvav 184,180,40\nMvav 184,176,32\nCV 180,180,181\nCV 181,182,183\ns_nop 1\nCV 182,188,189\nCV 183,190,191\ns_add_u32 s5, s94, 1703936\nSv 180,1,16,5,1024\n"
"CV 176,176,177\nCV 177,178,179\nCV 178,184,185\nCV 179,186,187\nSv 176,1,16,5,0\ns_cmp_eq_u32 s13, 7\ns_cbranch_scc1 .Z386\n"
"buffer_load_dwordx4 v[12:15], v91, s[28:31], s22 offen offset:0\nbuffer_load_dwordx4 v[16:19], v91, s[28:31], s23 offen offset:0\n"
"buffer_load_dwordx4 v[64:67], v89, s[16:19], s40 offen offset:0 sc0 sc1\nbuffer_load_dwordx4 v[68:71], v89, s[16:19], s40 offen offset:16 sc0 sc1\n"
"buffer_load_dwordx4 v[72:75], v89, s[16:19], s40 offen offset:1792 sc0 sc1\nbuffer_load_dwordx4 v[76:79], v89, s[16:19], s40 offen offset:1808 sc0 sc1\n"
"buffer_load_dwordx2 v[80:81], v88, s[16:19], s63 offen offset:0 sc0 sc1\nbuffer_load_dwordx2 v[82:83], v88, s[16:19], s63 offen offset:512 sc0 sc1\n"
"buffer_load_dwordx4 v[96:99], v1, s[16:19], s24 offen offset:0 sc0 sc1\nbuffer_load_dwordx4 v[100:103], v1, s[16:19], s24 offen offset:1024 sc0 sc1\n"
"buffer_load_dwordx4 v[104:107], v1, s[16:19], s25 offen offset:0 sc0 sc1\nbuffer_load_dwordx4 v[108:111], v1, s[16:19], s25 offen offset:1024 sc0 sc1\n"
"buffer_load_dwordx4 v[112:115], v1, s[16:19], s26 offen offset:0 sc0 sc1\nbuffer_load_dwordx4 v[116:119], v1, s[16:19], s26 offen offset:1024 sc0 sc1\n"
"buffer_load_dwordx4 v[120:123], v1, s[16:19], s27 offen offset:0 sc0 sc1\nbuffer_load_dwordx4 v[124:127], v1, s[16:19], s27 offen offset:1024 sc0 sc1\n"
"buffer_load_dwordx4 v[32:35], v1, s[16:19], s32 offen offset:0 sc0 sc1\nbuffer_load_dwordx4 v[36:39], v1, s[16:19], s32 offen offset:1024 sc0 sc1\n"
"buffer_load_dwordx4 v[40:43], v1, s[16:19], s33 offen offset:0 sc0 sc1\nbuffer_load_dwordx4 v[44:47], v1, s[16:19], s33 offen offset:1024 sc0 sc1\n"
"buffer_load_dwordx4 v[48:51], v1, s[16:19], s34 offen offset:0 sc0 sc1\nbuffer_load_dwordx4 v[52:55], v1, s[16:19], s34 offen offset:1024 sc0 sc1\n"
"buffer_load_dwordx4 v[56:59], v1, s[16:19], s35 offen offset:0 sc0 sc1\nbuffer_load_dwordx4 v[60:63], v1, s[16:19], s35 offen offset:1024 sc0 sc1\nWV 16\nv_xor_b32 v66, s15, v66\n"
"v_xor_b32 v70, s15, v70\nv_xor_b32 v74, s15, v74\nv_xor_b32 v78, s15, v78\nv_xor_b32 v81, s15, v81\nv_xor_b32 v83, s15, v83\nv_or3_b32 v92, v66, v70, v74\n"
"v_or3_b32 v92, v92, v78, v81\nv_or_b32 v92, v92, v83\nv_cmp_ne_u32 vcc, 0, v92\ns_nop 0\ns_cbranch_vccnz .S377\n.S378:\nv_and_b32 v92, 0x7ff, v67\nv_lshrrev_b32 v93, 16, v67\n"
"v_cmp_eq_u32 s[44:45], v92, v90\nv_cmp_eq_u32 s[46:47], v93, v90\nv_cmp_eq_u32 s[48:49], v92, v94\nv_cmp_eq_u32 s[50:51], v93, v94\nv_cndmask_b32 v12, v12, v64, s[44:45]\n"
"v_cndmask_b32 v13, v13, v65, s[44:45]\nv_cndmask_b32 v14, v14, v68, s[44:45]\nv_cndmask_b32 v15, v15, v69, s[44:45]\nv_cndmask_b32 v12, v12, v72, s[46:47]\n"
"v_cndmask_b32 v13, v13, v73, s[46:47]\nv_cndmask_b32 v14, v14, v76, s[46:47]\nv_cndmask_b32 v15, v15, v77, s[46:47]\nv_cndmask_b32 v16, v16, v64, s[48:49]\n"
"v_cndmask_b32 v17, v17, v65, s[48:49]\nv_cndmask_b32 v18, v18, v68, s[48:49]\nv_cndmask_b32 v19, v19, v69, s[48:49]\nv_cndmask_b32 v16, v16, v72, s[50:51]\n"
"v_cndmask_b32 v17, v17, v73, s[50:51]\nv_cndmask_b32 v18, v18, v76, s[50:51]\nv_cndmask_b32 v19, v19, v77, s[50:51]\nv_sub_f32 v92, 1.0, v80\nv_sub_f32 v93, 1.0, v82\nCV 92,92,93\n"
"v_and_b32 v93, 0xffff0000, v92\nv_lshlrev_b32 v92, 16, v92\nv_lshlrev_b32 v64, 16, v12\nv_and_b32 v65, 0xffff0000, v12\nMU 64,64,92\nMU 65,65,92\nv_lshlrev_b32 v66, 16, v13\n"
"v_and_b32 v67, 0xffff0000, v13\nMU 66,66,92\nMU 67,67,92\nv_lshlrev_b32 v68, 16, v14\nv_and_b32 v69, 0xffff0000, v14\nMU 68,68,92\nMU 69,69,92\nv_lshlrev_b32 v70, 16, v15\n"
"v_and_b32 v71, 0xffff0000, v15\nMU 70,70,92\nMU 71,71,92\nv_lshlrev_b32 v72, 16, v16\nv_and_b32 v73, 0xffff0000, v16\nMU 72,72,93\nMU 73,73,93\nv_lshlrev_b32 v74, 16, v17\n"
"v_and_b32 v75, 0xffff0000, v17\nMU 74,74,93\nMU 75,75,93\nv_lshlrev_b32 v76, 16, v18\nv_and_b32 v77, 0xffff0000, v18\nMU 76,76,93\nMU 77,77,93\nv_lshlrev_b32 v78, 16, v19\n"
"v_and_b32 v79, 0xffff0000, v19\nMU 78,78,93\nMU 79,79,93\ns_mov_b32 s7, 262144\nWV 0\n.Z389:\nv_max_u32 v84, v99, v103\nv_cmp_eq_u32 s[44:45], -1, v84\nv_max_u32 v84, v107, v111\n"
"v_cmp_eq_u32 s[46:47], -1, v84\nv_max_u32 v84, v115, v119\nv_cmp_eq_u32 s[48:49], -1, v84\nv_max_u32 v84, v123, v127\nv_cmp_eq_u32 s[50:51], -1, v84\nv_max_u32 v84, v35, v39\n"
"v_cmp_eq_u32 s[52:53], -1, v84\nv_max_u32 v84, v43, v47\nv_cmp_eq_u32 s[54:55], -1, v84\nv_max_u32 v84, v51, v55\nv_cmp_eq_u32 s[56:57], -1, v84\nv_max_u32 v84, v59, v63\n"
"v_cmp_eq_u32 s[58:59], -1, v84\ns_or_b64 s[0:1], s[44:45], s[46:47]\ns_or_b64 s[2:3], s[48:49], s[50:51]\ns_or_b64 s[0:1], s[0:1], s[52:53]\ns_or_b64 s[2:3], s[2:3], s[54:55]\n"
"s_or_b64 s[0:1], s[0:1], s[56:57]\ns_or_b64 s[2:3], s[2:3], s[58:59]\ns_or_b64 s[0:1], s[0:1], s[2:3]\ns_cbranch_scc1 .Z387\n.Z388:\nZvvv 136,6,96\nMvvv 136,6,104\nMvvv 136,6,112\n"
"Mvvv 136,6,120\nMvvv 136,6,32\nMvvv 136,6,40\nMvvv 136,6,48\nMvvv 136,6,56\nZvvv 140,4,96\nMvvv 140,4,104\nMvvv 140,4,112\nMvvv 140,4,120\nMvvv 140,4,32\nMvvv 140,4,40\n"
"Mvvv 140,4,48\nMvvv 140,4,56\nZvvv 144,6,100\nMvvv 144,6,108\nMvvv 144,6,116\nMvvv 144,6,124\nMvvv 144,6,36\nMvvv 144,6,44\nMvvv 144,6,52\nMvvv 144,6,60\nZvvv 148,4,100\n"
"Mvvv 148,4,108\nMvvv 148,4,116\nMvvv 148,4,124\nMvvv 148,4,36\nMvvv 148,4,44\nMvvv 148,4,52\nMvvv 148,4,60\nFM 64,80,136\nFM 65,80,137\nFM 66,80,138\nFM 67,80,139\nFM 68,80,140\n"
"FM 69,80,141\nFM 70,80,142\nFM 71,80,143\nCV 12,64,65\nCV 13,66,67\nCV 14,68,69\nCV 15,70,71\nSv 12,91,36,22,0\nFM 72,82,144\nFM 73,82,145\nFM 74,82,146\nFM 75,82,147\n"
"FM 76,82,148\nFM 77,82,149\nFM 78,82,150\nFM 79,82,151\nCV 16,72,73\nCV 17,74,75\nCV 18,76,77\nCV 19,78,79\nSv 16,91,36,23,0\n.Z386:\ns_endpgm\n.EP398:\n.S269:\n"
"s_mov_b32 s7, 262144\n.S271:\ns_lshl_b32 s5, s73, 9\ns_add_u32 s5, s5, 0xc049400\nv_lshrrev_b32 v27, 1, v1\nbuffer_load_dwordx2 v[22:23], v27, s[16:19], s5 offen sc0 sc1\nWV 0\n"
"v_cmp_ne_u32 vcc, s15, v23\ns_nop 0\ns_cbranch_vccz .S270\ns_sleep 6\ns_sub_u32 s7, s7, 1\ns_cbranch_scc0 .S271\ns_branch .S270\n.T278:\ns_mov_b32 s62, 1\n"
"s_call_b64 s[80:81], .RA265\ns_branch .T279\n.T280:\ns_mov_b32 s62, 1\ns_call_b64 s[80:81], .RA265\ns_branch .T281\n.S283:\ns_mov_b32 s7, 262144\n.S285:\n"
"buffer_load_dword v27, off, s[16:19], s5 sc0 sc1\nWV 0\nv_cmp_ne_u32 vcc, s15, v27\ns_nop 0\ns_cbranch_vccz .S284\ns_sleep 6\ns_sub_u32 s7, s7, 1\ns_cbranch_scc0 .S285\n"
"s_branch .S284\n.S286:\ns_mov_b32 s7, 262144\n.S288:\nv_and_b32 v27, 0x70, v1\nbuffer_load_dword v27, v27, s[16:19], s61 offen offset:128 sc0 sc1\nWV 0\n"
"v_cmp_ne_u32 vcc, s15, v27\ns_nop 0\ns_cbranch_vccz .S287\ns_sleep 6\ns_sub_u32 s7, s7, 1\ns_cbranch_scc0 .S288\ns_branch .S287\n.S289:\ns_mov_b32 s7, 262144\n.S291:\n"
"v_and_b32 v27, 0x70, v1\nbuffer_load_dword v27, v27, s[16:19], s61 offen offset:136 sc0 sc1\nWV 0\nv_cmp_ne_u32 vcc, s15, v27\ns_nop 0\ns_cbranch_vccz .S290\ns_sleep 6\n"
"s_sub_u32 s7, s7, 1\ns_cbranch_scc0 .S291\ns_branch .S290\n.S292:\ns_mov_b32 s7, 262144\n.S294:\nv_and_b32 v27, 0x70, v1\n"
"buffer_load_dword v27, v27, s[16:19], s61 offen offset:140 sc0 sc1\nWV 0\nv_cmp_ne_u32 vcc, s15, v27\ns_nop 0\ns_cbranch_vccz .S293\ns_sleep 6\ns_sub_u32 s7, s7, 1\n"
"s_cbranch_scc0 .S294\ns_branch .S293\n.S295:\ns_mov_b32 s7, 262144\n.S297:\nv_and_b32 v27, 0x70, v1\nbuffer_load_dword v27, v27, s[16:19], s61 offen offset:12 sc0 sc1\nWV 0\n"
"v_cmp_ne_u32 vcc, s15, v27\ns_nop 0\ns_cbranch_vccz .S296\ns_sleep 6\ns_sub_u32 s7, s7, 1\ns_cbranch_scc0 .S297\ns_branch .S296\n.S298:\ns_mov_b32 s7, 262144\n.S300:\n"
"v_and_b32 v27, 0x70, v1\nbuffer_load_dword v27, v27, s[16:19], s61 offen offset:8 sc0 sc1\nWV 0\nv_cmp_ne_u32 vcc, s15, v27\ns_nop 0\ns_cbranch_vccz .S299\ns_sleep 6\n"
"s_sub_u32 s7, s7, 1\ns_cbranch_scc0 .S300\ns_branch .S299\n.S301:\ns_mov_b32 s7, 262144\n.S303:\nv_and_b32 v27, 0xf0, v1\n"
"buffer_load_dword v27, v27, s[16:19], s61 offen offset:4 sc0 sc1\nWV 0\nv_cmp_ne_u32 vcc, s15, v27\ns_nop 0\ns_cbranch_vccz .S302\ns_sleep 6\ns_sub_u32 s7, s7, 1\n"
"s_cbranch_scc0 .S303\ns_branch .S302\n.T312:\ns_mov_b32 s62, 0\ns_call_b64 s[80:81], .RB266\ns_branch .T313\n.T314:\ns_mov_b32 s62, 0\ns_call_b64 s[80:81], .RA265\ns_branch .T315\n"
".T318:\ns_mov_b32 s62, 0\ns_call_b64 s[80:81], .RB266\ns_branch .T319\n.T320:\ns_mov_b32 s62, 0\ns_call_b64 s[80:81], .RA265\ns_branch .T321\n.T327:\ns_mov_b32 s62, 2\n"
"s_call_b64 s[80:81], .RB266\ns_branch .T328\n.T329:\ns_mov_b32 s62, 1\ns_call_b64 s[80:81], .RA265\ns_branch .T330\n.T331:\ns_mov_b32 s62, 1\ns_call_b64 s[80:81], .RA265\n"
"s_branch .T332\n.T344:\ns_mov_b32 s62, 0\ns_call_b64 s[80:81], .RB266\ns_branch .T345\n.T346:\ns_mov_b32 s62, 0\ns_call_b64 s[80:81], .RA265\ns_branch .T347\n.T350:\n"
"s_mov_b32 s62, 0\ns_call_b64 s[80:81], .RB266\ns_branch .T351\n.T352:\ns_mov_b32 s62, 0\ns_call_b64 s[80:81], .RA265\ns_branch .T353\n.T354:\ns_mov_b32 s62, 2\n"
"s_call_b64 s[80:81], .RB266\ns_branch .T355\n.S356:\ns_mov_b32 s7, 262144\n.S358:\ns_add_u32 s0, s92, 0\nbuffer_load_dwordx4 v[208:211], v1, s[16:19], s0 offen offset:0 sc0 sc1\n"
"buffer_load_dwordx4 v[212:215], v1, s[16:19], s0 offen offset:1024 sc0 sc1\nWV 0\nv_max_u32 v21, v211, v215\nv_cmp_eq_u32 vcc, -1, v21\ns_nop 0\ns_cbranch_vccz .S357\ns_sleep 6\n"
"s_sub_u32 s7, s7, 1\ns_cbranch_scc0 .S358\ns_branch .S357\n.S359:\ns_mov_b32 s7, 262144\n.S361:\ns_add_u32 s0, s92, 131072\n"
"buffer_load_dwordx4 v[216:219], v1, s[16:19], s0 offen offset:0 sc0 sc1\nbuffer_load_dwordx4 v[220:223], v1, s[16:19], s0 offen offset:1024 sc0 sc1\nWV 0\n"
"v_max_u32 v21, v219, v223\nv_cmp_eq_u32 vcc, -1, v21\ns_nop 0\ns_cbranch_vccz .S360\ns_sleep 6\ns_sub_u32 s7, s7, 1\ns_cbranch_scc0 .S361\ns_branch .S360\n.S362:\n"
"s_mov_b32 s7, 262144\n.S364:\ns_add_u32 s0, s92, 262144\nbuffer_load_dwordx4 v[224:227], v1, s[16:19], s0 offen offset:0 sc0 sc1\n"
"buffer_load_dwordx4 v[228:231], v1, s[16:19], s0 offen offset:1024 sc0 sc1\nWV 0\nv_max_u32 v21, v227, v231\nv_cmp_eq_u32 vcc, -1, v21\ns_nop 0\ns_cbranch_vccz .S363\ns_sleep 6\n"
"s_sub_u32 s7, s7, 1\ns_cbranch_scc0 .S364\ns_branch .S363\n.S365:\ns_mov_b32 s7, 262144\n.S367:\ns_add_u32 s0, s92, 393216\n"
"buffer_load_dwordx4 v[208:211], v1, s[16:19], s0 offen offset:0 sc0 sc1\nbuffer_load_dwordx4 v[212:215], v1, s[16:19], s0 offen offset:1024 sc0 sc1\nWV 0\n"
"v_max_u32 v21, v211, v215\nv_cmp_eq_u32 vcc, -1, v21\ns_nop 0\ns_cbranch_vccz .S366\ns_sleep 6\ns_sub_u32 s7, s7, 1\ns_cbranch_scc0 .S367\ns_branch .S366\n.S368:\n"
"s_mov_b32 s7, 262144\n.S370:\ns_add_u32 s0, s92, 524288\nbuffer_load_dwordx4 v[216:219], v1, s[16:19], s0 offen offset:0 sc0 sc1\n"
"buffer_load_dwordx4 v[220:223], v1, s[16:19], s0 offen offset:1024 sc0 sc1\nWV 0\nv_max_u32 v21, v219, v223\nv_cmp_eq_u32 vcc, -1, v21\ns_nop 0\ns_cbranch_vccz .S369\ns_sleep 6\n"
"s_sub_u32 s7, s7, 1\ns_cbranch_scc0 .S370\ns_branch .S369\n.S371:\ns_mov_b32 s7, 262144\n.S373:\ns_add_u32 s0, s92, 655360\n"
"buffer_load_dwordx4 v[224:227], v1, s[16:19], s0 offen offset:0 sc0 sc1\nbuffer_load_dwordx4 v[228:231], v1, s[16:19], s0 offen offset:1024 sc0 sc1\nWV 0\n"
"v_max_u32 v21, v227, v231\nv_cmp_eq_u32 vcc, -1, v21\ns_nop 0\ns_cbranch_vccz .S372\ns_sleep 6\ns_sub_u32 s7, s7, 1\ns_cbranch_scc0 .S373\ns_branch .S372\n.S374:\n"
"s_mov_b32 s7, 262144\n.S376:\ns_add_u32 s0, s92, 786432\nbuffer_load_dwordx4 v[208:211], v1, s[16:19], s0 offen offset:0 sc0 sc1\n"
"buffer_load_dwordx4 v[212:215], v1, s[16:19], s0 offen offset:1024 sc0 sc1\nWV 0\nv_max_u32 v21, v211, v215\nv_cmp_eq_u32 vcc, -1, v21\ns_nop 0\ns_cbranch_vccz .S375\ns_sleep 6\n"
"s_sub_u32 s7, s7, 1\ns_cbranch_scc0 .S376\ns_branch .S375\n.S377:\ns_mov_b32 s7, 262144\n.S379:\nbuffer_load_dwordx4 v[64:67], v89, s[16:19], s40 offen offset:0 sc0 sc1\n"
"buffer_load_dwordx4 v[68:71], v89, s[16:19], s40 offen offset:16 sc0 sc1\nbuffer_load_dwordx4 v[72:75], v89, s[16:19], s40 offen offset:1792 sc0 sc1\n"
"buffer_load_dwordx4 v[76:79], v89, s[16:19], s40 offen offset:1808 sc0 sc1\nbuffer_load_dwordx2 v[80:81], v88, s[16:19], s63 offen offset:0 sc0 sc1\n"
"buffer_load_dwordx2 v[82:83], v88, s[16:19], s63 offen offset:512 sc0 sc1\nWV 0\nv_xor_b32 v66, s15, v66\nv_xor_b32 v70, s15, v70\nv_xor_b32 v74, s15, v74\n"
"v_xor_b32 v78, s15, v78\nv_xor_b32 v81, s15, v81\nv_xor_b32 v83, s15, v83\nv_or3_b32 v92, v66, v70, v74\nv_or3_b32 v92, v92, v78, v81\nv_or_b32 v92, v92, v83\n"
"v_cmp_ne_u32 vcc, 0, v92\ns_nop 0\ns_cbranch_vccz .S378\ns_sleep 6\ns_sub_u32 s7, s7, 1\ns_cbranch_scc0 .S379\ns_branch .S378\n.S380:\ns_mov_b32 s7, 262144\n.S382:\n"
"buffer_load_dword v27, off, s[16:19], s60 offset:512 sc0 sc1\nWV 0\nv_cmp_ne_u32 vcc, s15, v27\ns_nop 0\ns_cbranch_vccz .S381\ns_sleep 6\ns_sub_u32 s7, s7, 1\n"
"s_cbranch_scc0 .S382\ns_branch .S381\n.S383:\ns_mov_b32 s7, 262144\n.S385:\nbuffer_load_dword v27, v24, s[16:19], s41 offen sc0 sc1\nWV 0\nv_xor_b32 v27, s15, v27\n"
"v_and_b32 v26, 0xffffdfff, v27\nv_cmp_ne_u32 s[64:65], 0, v27\nv_cmp_ne_u32 vcc, 0, v26\ns_nop 0\ns_cbranch_vccz .S384\ns_sleep 6\ns_sub_u32 s7, s7, 1\ns_cbranch_scc0 .S385\n"
"s_branch .S384\n.Z387:\ns_sub_u32 s7, s7, 1\ns_cbranch_scc1 .Z388\ns_cmp_lg_u64 s[44:45], 0\ns_cbranch_scc0 .Z390\n"
"buffer_load_dwordx4 v[96:99], v1, s[16:19], s24 offen offset:0 sc0 sc1\nbuffer_load_dwordx4 v[100:103], v1, s[16:19], s24 offen offset:1024 sc0 sc1\n.Z390:\n"
"s_cmp_lg_u64 s[46:47], 0\ns_cbranch_scc0 .Z391\nbuffer_load_dwordx4 v[104:107], v1, s[16:19], s25 offen offset:0 sc0 sc1\n"
"buffer_load_dwordx4 v[108:111], v1, s[16:19], s25 offen offset:1024 sc0 sc1\n.Z391:\ns_cmp_lg_u64 s[48:49], 0\ns_cbranch_scc0 .Z392\n"
"buffer_load_dwordx4 v[112:115], v1, s[16:19], s26 offen offset:0 sc0 sc1\nbuffer_load_dwordx4 v[116:119], v1, s[16:19], s26 offen offset:1024 sc0 sc1\n.Z392:\n"
"s_cmp_lg_u64 s[50:51], 0\ns_cbranch_scc0 .Z393\nbuffer_load_dwordx4 v[120:123], v1, s[16:19], s27 offen offset:0 sc0 sc1\n"
"buffer_load_dwordx4 v[124:127], v1, s[16:19], s27 offen offset:1024 sc0 sc1\n.Z393:\ns_cmp_lg_u64 s[52:53], 0\ns_cbranch_scc0 .Z394\n"
"buffer_load_dwordx4 v[32:35], v1, s[16:19], s32 offen offset:0 sc0 sc1\nbuffer_load_dwordx4 v[36:39], v1, s[16:19], s32 offen offset:1024 sc0 sc1\n.Z394:\n"
"s_cmp_lg_u64 s[54:55], 0\ns_cbranch_scc0 .Z395\nbuffer_load_dwordx4 v[40:43], v1, s[16:19], s33 offen offset:0 sc0 sc1\n"
"buffer_load_dwordx4 v[44:47], v1, s[16:19], s33 offen offset:1024 sc0 sc1\n.Z395:\ns_cmp_lg_u64 s[56:57], 0\ns_cbranch_scc0 .Z396\n"
"buffer_load_dwordx4 v[48:51], v1, s[16:19], s34 offen offset:0 sc0 sc1\nbuffer_load_dwordx4 v[52:55], v1, s[16:19], s34 offen offset:1024 sc0 sc1\n.Z396:\n"
"s_cmp_lg_u64 s[58:59], 0\ns_cbranch_scc0 .Z397\nbuffer_load_dwordx4 v[56:59], v1, s[16:19], s35 offen offset:0 sc0 sc1\n"
"buffer_load_dwordx4 v[60:63], v1, s[16:19], s35 offen offset:1024 sc0 sc1\n.Z397:\ns_sleep 8\nWV 0\ns_branch .Z389\n.RA265:\ns_nop 7\ns_nop 3\nv_max3_f32 v22, v32, v33, v34\n"
"v_max3_f32 v22, v22, v35, v40\nv_max3_f32 v22, v22, v41, v42\nv_max_f32 v22, v22, v43\nv_mov_b32 v91, v22\ns_nop 1\nv_permlane32_swap_b32 v22, v91\nv_max_f32 v22, v22, v91\n"
"v_mov_b32 v91, v22\ns_nop 1\nv_permlane16_swap_b32 v22, v91\nv_max_f32 v22, v22, v91\nv_ceil_f32 v22, v22\nv_max3_f32 v23, v36, v37, v38\nv_max3_f32 v23, v23, v39, v44\n"
"v_max3_f32 v23, v23, v45, v46\nv_max_f32 v23, v23, v47\nv_mov_b32 v91, v23\ns_nop 1\nv_permlane32_swap_b32 v23, v91\nv_max_f32 v23, v23, v91\nv_mov_b32 v91, v23\ns_nop 1\n"
"v_permlane16_swap_b32 v23, v91\nv_max_f32 v23, v23, v91\nv_ceil_f32 v23, v23\ns_bitcmp1_b32 s62, 0\ns_cbranch_scc0 .R399\nv_cmp_lt_f32 vcc, 0xff000000, v22\ns_nop 1\n"
"v_cndmask_b32 v22, 0, v22, vcc\nv_cmp_lt_f32 vcc, 0xff000000, v23\ns_nop 1\nv_cndmask_b32 v23, 0, v23, vcc\ns_branch .R400\n.R399:\nv_max_f32 v22, 0, v22\nv_max_f32 v23, 0, v23\n"
"s_bitcmp1_b32 s62, 3\ns_cbranch_scc0 .R400\nv_max_f32 v22, v20, v22\nv_max_f32 v23, v21, v23\n.R400:\nAD 88,88,22\nAD 89,89,23\nSU 32,32,22\nSU 33,33,22\nSU 34,34,22\nSU 35,35,22\n"
"SU 36,36,23\nSU 37,37,23\nSU 38,38,23\nSU 39,39,23\nSU 40,40,22\nSU 41,41,22\nSU 42,42,22\nSU 43,43,22\nSU 44,44,23\nSU 45,45,23\nSU 46,46,23\nSU 47,47,23\nv_mov_b32 v90, v80\n"
"v_mov_b32 v91, v81\nEX 80,32\nEX 81,33\ns_nop 0\nAD 84,80,81\nCV 64,80,81\nEX 82,34\nEX 83,35\nAD 84,84,82\nAD 84,84,83\nCV 65,82,83\nEX 80,36\nEX 81,37\ns_nop 0\nAD 85,80,81\n"
"CV 68,80,81\nEX 82,38\nEX 83,39\nAD 85,85,82\nAD 85,85,83\nCV 69,82,83\nEX 80,40\nEX 81,41\nAD 84,84,80\nAD 84,84,81\nCV 66,80,81\nEX 82,42\nEX 83,43\nAD 84,84,82\nAD 84,84,83\n"
"CV 67,82,83\nEX 80,44\nEX 81,45\nAD 85,85,80\nAD 85,85,81\nCV 70,80,81\nEX 82,46\nEX 83,47\nAD 85,85,82\nAD 85,85,83\nCV 71,82,83\nv_mov_b32 v80, v90\nv_mov_b32 v81, v91\n"
"s_bitcmp1_b32 s62, 1\ns_cbranch_scc1 .R401\nSU 48,48,22\nSU 49,49,22\nSU 50,50,22\nSU 51,51,22\nSU 52,52,23\nSU 53,53,23\nSU 54,54,23\nSU 55,55,23\nSU 56,56,22\nSU 57,57,22\n"
"SU 58,58,22\nSU 59,59,22\nSU 60,60,23\nSU 61,61,23\nSU 62,62,23\nSU 63,63,23\nSU 4,4,22\nSU 5,5,22\nSU 6,6,22\nSU 7,7,22\nSU 8,8,23\nSU 9,9,23\nSU 10,10,23\nSU 11,11,23\n"
"SU 12,12,22\nSU 13,13,22\nSU 14,14,22\nSU 15,15,22\nSU 16,16,23\nSU 17,17,23\nSU 18,18,23\nSU 19,19,23\nEX 80,48\nEX 81,49\n.R401:\ns_bitcmp1_b32 s62, 0\ns_cbranch_scc0 .RS267\n"
"s_nop 1\ns_setpc_b64 s[80:81]\n.RB266:\ns_nop 7\ns_nop 3\nv_max3_f32 v22, v48, v49, v50\nv_max3_f32 v22, v22, v51, v56\nv_max3_f32 v22, v22, v57, v58\nv_max_f32 v22, v22, v59\n"
"v_mov_b32 v91, v22\ns_nop 1\nv_permlane32_swap_b32 v22, v91\nv_max_f32 v22, v22, v91\nv_mov_b32 v91, v22\ns_nop 1\nv_permlane16_swap_b32 v22, v91\nv_max_f32 v22, v22, v91\n"
"v_ceil_f32 v22, v22\nv_max3_f32 v23, v52, v53, v54\nv_max3_f32 v23, v23, v55, v60\nv_max3_f32 v23, v23, v61, v62\nv_max_f32 v23, v23, v63\nv_mov_b32 v91, v23\ns_nop 1\n"
"v_permlane32_swap_b32 v23, v91\nv_max_f32 v23, v23, v91\nv_mov_b32 v91, v23\ns_nop 1\nv_permlane16_swap_b32 v23, v91\nv_max_f32 v23, v23, v91\nv_ceil_f32 v23, v23\n"
"s_bitcmp1_b32 s62, 0\ns_cbranch_scc0 .R404\nv_cmp_lt_f32 vcc, 0xff000000, v22\nv_cndmask_b32 v22, 0, v22, vcc\nv_cmp_lt_f32 vcc, 0xff000000, v23\nv_cndmask_b32 v23, 0, v23, vcc\n"
"s_branch .R405\n.R404:\nv_max_f32 v22, 0, v22\nv_max_f32 v23, 0, v23\ns_bitcmp1_b32 s62, 3\ns_cbranch_scc0 .R405\nv_max_f32 v22, v20, v22\nv_max_f32 v23, v21, v23\n.R405:\n"
"AD 88,88,22\nAD 89,89,23\nSU 48,48,22\nSU 49,49,22\nSU 50,50,22\nSU 51,51,22\nSU 52,52,23\nSU 53,53,23\nSU 54,54,23\nSU 55,55,23\nSU 56,56,22\nSU 57,57,22\nSU 58,58,22\n"
"SU 59,59,22\nSU 60,60,23\nSU 61,61,23\nSU 62,62,23\nSU 63,63,23\nv_mov_b32 v90, v80\nv_mov_b32 v91, v81\nEX 80,48\nEX 81,49\ns_nop 0\nAD 84,80,81\nCV 72,80,81\nEX 82,50\nEX 83,51\n"
"AD 84,84,82\nAD 84,84,83\nCV 73,82,83\nEX 80,52\nEX 81,53\ns_nop 0\nAD 85,80,81\nCV 76,80,81\nEX 82,54\nEX 83,55\nAD 85,85,82\nAD 85,85,83\nCV 77,82,83\nEX 80,56\nEX 81,57\n"
"AD 84,84,80\nAD 84,84,81\nCV 74,80,81\nEX 82,58\nEX 83,59\nAD 84,84,82\nAD 84,84,83\nCV 75,82,83\nEX 80,60\nEX 81,61\nAD 85,85,80\nAD 85,85,81\nCV 78,80,81\nEX 82,62\nEX 83,63\n"
"AD 85,85,82\nAD 85,85,83\nCV 79,82,83\nv_mov_b32 v80, v90\nv_mov_b32 v81, v91\ns_bitcmp1_b32 s62, 1\ns_cbranch_scc1 .R406\nSU 32,32,22\nSU 33,33,22\nSU 34,34,22\nSU 35,35,22\n"
"SU 36,36,23\nSU 37,37,23\nSU 38,38,23\nSU 39,39,23\nSU 40,40,22\nSU 41,41,22\nSU 42,42,22\nSU 43,43,22\nSU 44,44,23\nSU 45,45,23\nSU 46,46,23\nSU 47,47,23\nSU 4,4,22\nSU 5,5,22\n"
"SU 6,6,22\nSU 7,7,22\nSU 8,8,23\nSU 9,9,23\nSU 10,10,23\nSU 11,11,23\nSU 12,12,22\nSU 13,13,22\nSU 14,14,22\nSU 15,15,22\nSU 16,16,23\nSU 17,17,23\nSU 18,18,23\nSU 19,19,23\n"
"EX 80,32\nEX 81,33\n.R406:\ns_bitcmp1_b32 s62, 0\ns_cbranch_scc0 .RS267\ns_nop 1\ns_setpc_b64 s[80:81]\n.RS267:\nv_exp_f32 v22, -v22\nv_exp_f32 v23, -v23\nMU 86,86,22\n"
"MU 87,87,23\nMU 128,128,22\nMU 129,129,22\nMU 130,130,22\nMU 131,131,22\nMU 132,132,23\nMU 133,133,23\nMU 134,134,23\nMU 135,135,23\nMU 136,136,22\nMU 137,137,22\nMU 138,138,22\n"
"MU 139,139,22\nMU 140,140,23\nMU 141,141,23\nMU 142,142,23\nMU 143,143,23\nMU 144,144,22\nMU 145,145,22\nMU 146,146,22\nMU 147,147,22\nMU 148,148,23\nMU 149,149,23\nMU 150,150,23\n"
"MU 151,151,23\nMU 152,152,22\nMU 153,153,22\nMU 154,154,22\nMU 155,155,22\nMU 156,156,23\nMU 157,157,23\nMU 158,158,23\nMU 159,159,23\nMU 160,160,22\nMU 161,161,22\nMU 162,162,22\n"
"MU 163,163,22\nMU 164,164,23\nMU 165,165,23\nMU 166,166,23\nMU 167,167,23\nMU 168,168,22\nMU 169,169,22\nMU 170,170,22\nMU 171,171,22\nMU 172,172,23\nMU 173,173,23\nMU 174,174,23\n"
"MU 175,175,23\nMU 176,176,22\nMU 177,177,22\nMU 178,178,22\nMU 179,179,22\nMU 180,180,23\nMU 181,181,23\nMU 182,182,23\nMU 183,183,23\nMU 184,184,22\nMU 185,185,22\nMU 186,186,22\n"
"MU 187,187,22\nMU 188,188,23\nMU 189,189,23\nMU 190,190,23\nMU 191,191,23\nMU 192,192,22\nMU 193,193,22\nMU 194,194,22\nMU 195,195,22\nMU 196,196,23\nMU 197,197,23\nMU 198,198,23\n"
"MU 199,199,23\nMU 200,200,22\nMU 201,201,22\nMU 202,202,22\nMU 203,203,22\nMU 204,204,23\nMU 205,205,23\nMU 206,206,23\nMU 207,207,23\nMU 208,208,22\nMU 209,209,22\nMU 210,210,22\n"
"MU 211,211,22\nMU 212,212,23\nMU 213,213,23\nMU 214,214,23\nMU 215,215,23\nMU 216,216,22\nMU 217,217,22\nMU 218,218,22\nMU 219,219,22\nMU 220,220,23\nMU 221,221,23\nMU 222,222,23\n"
"MU 223,223,23\nMU 224,224,22\nMU 225,225,22\nMU 226,226,22\nMU 227,227,22\nMU 228,228,23\nMU 229,229,23\nMU 230,230,23\nMU 231,231,23\nMU 232,232,22\nMU 233,233,22\nMU 234,234,22\n"
"MU 235,235,22\nMU 236,236,23\nMU 237,237,23\nMU 238,238,23\nMU 239,239,23\nMU 240,240,22\nMU 241,241,22\nMU 242,242,22\nMU 243,243,22\nMU 244,244,23\nMU 245,245,23\nMU 246,246,23\n"
"MU 247,247,23\nMU 248,248,22\nMU 249,249,22\nMU 250,250,22\nMU 251,251,22\nMU 252,252,23\nMU 253,253,23\nMU 254,254,23\nMU 255,255,23\ns_nop 1\ns_setpc_b64 s[80:81]\n.p2align 12\n"
".PA5:\ns_add_u32 m0, s47, -49152\ns_add_u32 s48, s48, 128\nD0 2,20,48,0\nD0 3,20,48,1024\nD0 4,20,48,2048\nD0 5,20,48,3072\ns_and_b32 s86, s74, 15\ns_lshl2_add_u32 s86, s86, s11\n"
"s_mov_b32 s88, 0\ns_lshl_b32 s93, s73, 9\ns_lshl3_add_u32 s93, s86, s93\ns_add_u32 s93, s93, 0xc049400\ns_lshl_b32 s86, s86, 14\ns_mul_i32 s91, s73, 25165824\n"
"s_add_u32 s86, s86, s91\ns_and_b32 s92, s13, 1\ns_lshl_b32 s92, s92, 13\ns_add_u32 s86, s86, s92\ns_add_u32 s94, s86, 0x1480000\ns_xor_b32 s95, s94, 0x2000\n"
"s_add_u32 s86, s94, 512\ns_load_dword s87, s[54:55], s86\ns_add_u32 m0, s47, -32768\ns_add_u32 s45, s45, 128\nD0 2,20,45,0\nD0 3,20,45,1024\nD0 4,20,45,2048\nD0 5,20,45,3072\n"
"v_mov_b32 v52, 0x40000000\nv_cmp_gt_u32 vcc, 56, v0\ns_lshl2_add_u32 s2, s10, s11\ns_mul_i32 s2, s2, 1792\nv_cndmask_b32 v53, v52, v1, vcc\ns_mov_b32 s36, s56\n"
"s_and_b32 s37, s57, 0xffff\ns_mov_b32 s38, 0x380\ns_mov_b32 s39, 0x20000\ns_mov_b32 s40, s58\ns_and_b32 s41, s59, 0xffff\ns_mov_b32 s42, 0x380\ns_mov_b32 s43, 0x20000\n"
"buffer_load_dwordx4 v[32:35], v53, s[28:31], s2 offen offset:0\nbuffer_load_dwordx4 v[36:39], v53, s[28:31], s2 offen offset:896\n"
"buffer_load_dwordx4 v[40:43], v53, s[36:39], 0 offen offset:0\nbuffer_load_dwordx4 v[44:47], v53, s[40:43], 0 offen offset:0\nWL 0\ns_mul_i32 s15, s15, 1664525\n"
"s_add_u32 s15, s15, 1013904223\nv_mov_b32 v25, s15\ns_mov_b32 s57, 0x3db8aa3b\ns_and_b32 s59, s11, 1\ns_lshl_b32 s59, s59, 13\ns_mul_i32 s2, s73, 25165824\ns_and_b32 s3, s12, 1\n"
"s_lshl_b32 s3, s3, 21\ns_add_u32 s2, s2, s3\ns_lshl2_add_u32 s3, s13, s11\ns_lshl_b32 s3, s3, 15\ns_add_u32 s2, s2, s3\ns_add_u32 s56, s2, 0x800000\ns_xor_b32 s53, s56, 0xc000\n"
"buffer_load_dword v31, v24, s[24:27], s96 offen\ns_add_u32 s96, s96, 0x80000\nbuffer_load_dword v31, v24, s[24:27], s96 offen\ns_add_u32 s96, s96, 0x80000\ns_mov_b64 exec, 0\n"
"s_cmp_eq_u32 s11, 1\ns_cbranch_scc1 .W581\ns_cmp_eq_u32 s11, 2\ns_cbranch_scc1 .W582\ns_cmp_eq_u32 s11, 3\ns_cbranch_scc1 .W583\ns_branch .W580\n.WB414:\ns_mov_b64 exec, -1\n"
"WV 14\ns_barrier\nRa 128,10,0\nRa 132,11,0\nRa 136,10,2048\nRa 140,11,2048\nRa 144,10,8192\nRa 148,11,8192\nRa 152,10,10240\nRa 156,11,10240\nTa 160,12,0\nTa 162,12,2048\n"
"Ta 164,12,16384\nTa 166,12,18432\nTa 168,13,0\nTa 170,13,2048\nTa 172,13,16384\nTa 174,13,18432\nTa 176,14,0\nTa 178,14,2048\nTa 180,14,16384\nTa 182,14,18432\nTa 184,15,0\n"
"Ta 186,15,2048\nTa 188,15,16384\nTa 190,15,18432\nWL 0\ns_barrier\nZvaa 64,128,160\nTa 224,16,0\ns_add_u32 m0, s47, 32768\ns_add_u32 s44, s44, 0x40000\nMvaa 64,132,164\n"
"Ta 226,16,2048\nZvaa 68,140,164\nD0 6,24,44,0\nMvaa 68,136,160\nTa 228,16,16384\nZvaa 128,144,160\nTa 230,16,18432\nMvaa 128,148,164\ns_mov_b32 s36, s60\nZvaa 132,156,164\n"
"Ta 232,17,0\nD0 7,24,44,1024\nMvaa 132,152,160\nTa 234,17,2048\nZvaa 140,152,168\ns_and_b32 s37, s61, 0xffff\nMvaa 140,156,172\nTa 236,17,16384\nZvaa 136,148,172\nTa 238,17,18432\n"
"D0 8,24,44,2048\nMvaa 136,144,168\ns_mov_b32 s38, 0x380000\nZvaa 76,136,168\nTa 240,18,0\nMvaa 76,140,172\nTa 242,18,2048\nZvaa 72,132,172\nD0 9,24,44,3072\nMvaa 72,128,168\n"
"Ta 244,18,16384\nZvaa 80,128,176\nTa 246,18,18432\ns_add_u32 m0, s47, 49152\ns_add_u32 s44, s44, 0x40000\nMvaa 80,132,180\ns_mov_b32 s39, 0x20000\nZvaa 84,140,180\nTa 248,19,0\n"
"D0 6,24,44,0\nMvaa 84,136,176\nTa 250,19,2048\nZvaa 144,144,176\ns_lshl3_add_u32 s75, s12, s13\nMvaa 144,148,180\nTa 252,19,16384\nZvaa 148,156,180\nTa 254,19,18432\n"
"D0 7,24,44,1024\nMvaa 148,152,176\ns_lshl2_add_u32 s75, s75, s11\nZvaa 156,152,184\ns_mul_i32 s75, s75, 7168\nMvaa 156,156,188\ns_mul_i32 s76, s11, 7168\nZvaa 152,148,188\n"
"D0 8,24,44,2048\nMvaa 152,144,184\ns_add_u32 s76, s76, 131072\nZvaa 92,136,184\ns_mov_b32 s42, 1605632\nMvaa 92,140,188\nZvaa 88,132,188\nD0 9,24,44,3072\nMvaa 88,128,184\n"
"s_waitcnt vmcnt(14) lgkmcnt(0)\ns_barrier\nZvaa 96,128,224\nRa 192,10,16384\ns_add_u32 m0, s47, -16384\ns_add_u32 s48, s48, 128\nMvaa 96,132,228\nZvaa 100,140,228\n"
"Ra 196,11,16384\nD0 2,20,48,0\nMvaa 100,136,224\nZvaa 160,144,224\nRa 200,10,18432\nMvaa 160,148,228\nZvaa 164,156,228\nRa 204,11,18432\nD0 3,20,48,1024\nMvaa 164,152,224\n"
"Zvaa 172,152,232\nRa 208,10,24576\nMvaa 172,156,236\ns_and_b32 s78, s74, 3\ns_lshl2_add_u32 s78, s73, s78\ns_lshl_b32 s78, s78, 12\ns_bitcmp1_b32 s11, 0\n"
"s_cselect_b32 s79, s48, s45\ns_add_u32 s79, s79, 512\ns_and_b32 s79, s79, 0xfffe0000\ns_add_u32 s78, s78, s79\nbuffer_load_dword v31, v26, s[20:23], s78 offen\nZvaa 168,148,236\n"
"Ra 212,11,24576\nD0 4,20,48,2048\nMvaa 168,144,232\nZvaa 108,136,232\nRa 216,10,26624\nMvaa 108,140,236\nZvaa 104,132,236\nRa 220,11,26624\nD0 5,20,48,3072\nMvaa 104,128,232\n"
"Zvaa 112,128,240\ns_add_u32 m0, s47, -65536\ns_add_u32 s45, s45, 128\nMvaa 112,132,244\nZvaa 116,140,244\nD0 2,20,45,0\nMvaa 116,136,240\nZvaa 176,144,240\nMvaa 176,148,244\n"
"Zvaa 180,156,244\nD0 3,20,45,1024\nMvaa 180,152,240\nZvaa 188,152,248\nMvaa 188,156,252\nZvaa 184,148,252\nD0 4,20,45,2048\nMvaa 184,144,248\nZvaa 124,136,248\nMvaa 124,140,252\n"
"Zvaa 120,132,252\nD0 5,20,45,3072\nMvaa 120,128,248\nWL 0\nZaaa 32,192,224\nRa 128,10,32768\ns_add_u32 m0, s47, 0\ns_add_u32 s44, s44, 0x40000\nMaaa 32,196,228\nZaaa 36,204,228\n"
"Ra 132,11,32768\nD0 6,24,44,0\nMaaa 36,200,224\nZaaa 96,208,224\nRa 136,10,34816\nMaaa 96,212,228\nZaaa 100,220,228\nRa 140,11,34816\nD0 7,24,44,1024\nMaaa 100,216,224\n"
"Zaaa 108,216,232\nRa 144,10,40960\nMaaa 108,220,236\nZaaa 104,212,236\nRa 148,11,40960\nD0 8,24,44,2048\nMaaa 104,208,232\nZaaa 44,200,232\nRa 152,10,43008\nMaaa 44,204,236\n"
"Zaaa 40,196,236\nRa 156,11,43008\nD0 9,24,44,3072\nMaaa 40,192,232\nZaaa 48,192,240\ns_add_u32 m0, s47, 16384\ns_add_u32 s44, s44, 0x40000\nMaaa 48,196,244\nZaaa 52,204,244\n"
"D0 6,24,44,0\nMaaa 52,200,240\nZaaa 112,208,240\nMaaa 112,212,244\nZaaa 116,220,244\nD0 7,24,44,1024\nMaaa 116,216,240\nZaaa 124,216,248\nMaaa 124,220,252\nZaaa 120,212,252\n"
"D0 8,24,44,2048\nMaaa 120,208,248\nZaaa 60,200,248\nMaaa 60,204,252\nZaaa 56,196,252\nD0 9,24,44,3072\nMaaa 56,192,248\ns_waitcnt vmcnt(17) lgkmcnt(0)\ns_barrier\nZaaa 0,192,160\n"
"Ta 224,16,32768\ns_add_u32 m0, s47, -49152\ns_add_u32 s48, s48, 128\nMaaa 0,196,164\nTa 226,16,34816\nZaaa 4,204,164\nD0 2,20,48,0\nMaaa 4,200,160\nTa 228,16,49152\n"
"Zaaa 64,208,160\nTa 230,16,51200\nbuffer_load_dword v31, v24, s[24:27], s96 offen\ns_add_u32 s96, s96, 0x80000\nMaaa 64,212,164\nv_lshlrev_b32 v48, 16, v32\n"
"v_lshlrev_b32 v49, 16, v36\nZaaa 68,220,164\nTa 232,17,32768\nD0 3,20,48,1024\nMaaa 68,216,160\nTa 234,17,34816\nv_lshlrev_b32 v50, 16, v40\nZaaa 76,216,168\n"
"v_lshlrev_b32 v51, 16, v44\nMU 52,48,50\nMaaa 76,220,172\nTa 236,17,49152\nZaaa 72,212,172\nTa 238,17,51200\nD0 4,20,48,2048\nMaaa 72,208,168\nv_lshrrev_b32 v60, 3, v0\n"
"v_and_b32 v61, 0x70, v1\nv_lshl_or_b32 v60, v60, 10, v61\nv_mov_b32 v20, 0\nv_mov_b32 v21, 0\nv_mov_b32 v22, 0\nv_mov_b32 v23, 0\nSv 20,60,16,94,0\nSv 20,60,16,95,0\n"
"Zaaa 12,200,168\nTa 240,18,32768\nMU 53,49,50\nMaaa 12,204,172\nTa 242,18,34816\nCV 52,52,53\nZaaa 8,196,172\nD0 5,20,48,3072\nMaaa 8,192,168\nTa 244,18,49152\n"
"v_lshlrev_b32 v54, 16, v52\nZaaa 16,192,176\nTa 246,18,51200\nv_and_b32 v55, 0xffff0000, v52\nMaaa 16,196,180\nMU 56,48,51\nZaaa 20,204,180\nTa 248,19,32768\nMU 57,49,51\n"
"Maaa 20,200,176\nTa 250,19,34816\nZaaa 80,208,176\nv_and_b32 v48, 0xffff0000, v32\nv_and_b32 v49, 0xffff0000, v36\nMaaa 80,212,180\nTa 252,19,49152\n"
"v_and_b32 v50, 0xffff0000, v40\nZaaa 84,220,180\nTa 254,19,51200\nMaaa 84,216,176\nv_and_b32 v51, 0xffff0000, v44\nMU 52,48,50\nZaaa 92,216,184\nMU 53,49,50\nCV 52,52,53\n"
"Maaa 92,220,188\nv_and_b32 v53, 0xffff0000, v52\nZaaa 88,212,188\nv_lshlrev_b32 v52, 16, v52\nFM 56,48,51\nMaaa 88,208,184\nFM 57,49,51\nZaaa 28,200,184\nAD 54,54,52\nAD 55,55,53\n"
"Maaa 28,204,188\nv_lshlrev_b32 v48, 16, v33\nv_lshlrev_b32 v49, 16, v37\nZaaa 24,196,188\nv_lshlrev_b32 v50, 16, v41\nMaaa 24,192,184\nWL 0\nMvaa 96,128,224\nTa 160,12,32768\n"
"v_lshlrev_b32 v51, 16, v45\nMvaa 96,132,228\nTa 162,12,34816\nMvaa 100,140,228\ns_add_u32 m0, s47, -32768\ns_add_u32 s45, s45, 128\nMvaa 100,136,224\nTa 164,12,49152\n"
"Mvaa 160,144,224\nTa 166,12,51200\nMU 52,48,50\nMvaa 160,148,228\nD0 2,20,45,0\nMU 53,49,50\nMvaa 164,156,228\nTa 168,13,32768\nMvaa 164,152,224\nTa 170,13,34816\nCV 52,52,53\n"
"Mvaa 172,152,232\nv_and_b32 v53, 0xffff0000, v52\nv_lshlrev_b32 v52, 16, v52\nMvaa 172,156,236\nTa 172,13,49152\nMvaa 168,148,236\nTa 174,13,51200\nFM 56,48,51\nMvaa 168,144,232\n"
"FM 57,49,51\nMvaa 108,136,232\nTa 176,14,32768\nAD 54,54,52\nMvaa 108,140,236\nTa 178,14,34816\nAD 55,55,53\nMvaa 104,132,236\nD0 3,20,45,1024\nMvaa 104,128,232\nTa 180,14,49152\n"
"v_and_b32 v48, 0xffff0000, v33\nMvaa 112,128,240\nTa 182,14,51200\nv_and_b32 v49, 0xffff0000, v37\nMvaa 112,132,244\nv_and_b32 v50, 0xffff0000, v41\nMvaa 116,140,244\n"
"Ta 184,15,32768\nv_and_b32 v51, 0xffff0000, v45\nMvaa 116,136,240\nTa 186,15,34816\nMvaa 176,144,240\nMU 52,48,50\nMU 53,49,50\nMvaa 176,148,244\nTa 188,15,49152\nCV 52,52,53\n"
"Mvaa 180,156,244\nTa 190,15,51200\nMvaa 180,152,240\nD0 4,20,45,2048\nv_and_b32 v53, 0xffff0000, v52\nMvaa 188,152,248\nv_lshlrev_b32 v52, 16, v52\nFM 56,48,51\nMvaa 188,156,252\n"
"FM 57,49,51\nMvaa 184,148,252\nAD 54,54,52\nAD 55,55,53\nMvaa 184,144,248\nv_lshlrev_b32 v48, 16, v34\nMvaa 124,136,248\nv_lshlrev_b32 v49, 16, v38\nv_lshlrev_b32 v50, 16, v42\n"
"Mvaa 124,140,252\nD0 5,20,45,3072\nv_lshlrev_b32 v51, 16, v46\nMvaa 120,132,252\nMU 52,48,50\nMvaa 120,128,248\ns_waitcnt vmcnt(19) lgkmcnt(0)\ns_barrier\nMvaa 64,128,160\n"
"Ra 192,10,49152\nMU 53,49,50\nMvaa 64,132,164\ns_add_u32 m0, s47, 32768\ns_add_u32 s44, s44, 0x40000\nMvaa 68,140,164\nRa 196,11,49152\nCV 52,52,53\nMvaa 68,136,160\n"
"v_and_b32 v53, 0xffff0000, v52\nMvaa 128,144,160\nRa 200,10,51200\nv_lshlrev_b32 v52, 16, v52\nMvaa 128,148,164\nD0 6,24,44,0\nFM 56,48,51\nMvaa 132,156,164\nRa 204,11,51200\n"
"Mvaa 132,152,160\nFM 57,49,51\nAD 54,54,52\nMvaa 140,152,168\nRa 208,10,57344\nAD 55,55,53\nMvaa 140,156,172\nv_and_b32 v48, 0xffff0000, v34\nMvaa 136,148,172\nRa 212,11,57344\n"
"v_and_b32 v49, 0xffff0000, v38\nMvaa 136,144,168\nv_and_b32 v50, 0xffff0000, v42\nMvaa 76,136,168\nRa 216,10,59392\nv_and_b32 v51, 0xffff0000, v46\nMvaa 76,140,172\n"
"D0 7,24,44,1024\nMU 52,48,50\nMvaa 72,132,172\nRa 220,11,59392\nMvaa 72,128,168\nMU 53,49,50\nCV 52,52,53\nMvaa 80,128,176\nv_and_b32 v53, 0xffff0000, v52\n"
"v_lshlrev_b32 v52, 16, v52\nMvaa 80,132,180\nFM 56,48,51\nMvaa 84,140,180\nFM 57,49,51\nAD 54,54,52\nMvaa 84,136,176\nAD 55,55,53\nMvaa 144,144,176\nv_lshlrev_b32 v48, 16, v35\n"
"v_lshlrev_b32 v49, 16, v39\nMvaa 144,148,180\nD0 8,24,44,2048\nv_lshlrev_b32 v50, 16, v43\nMvaa 148,156,180\nv_lshlrev_b32 v51, 16, v47\nMvaa 148,152,176\nMU 52,48,50\n"
"MU 53,49,50\nMvaa 156,152,184\nCV 52,52,53\nv_and_b32 v53, 0xffff0000, v52\nMvaa 156,156,188\nv_lshlrev_b32 v52, 16, v52\nMvaa 152,148,188\nFM 56,48,51\nFM 57,49,51\n"
"Mvaa 152,144,184\nAD 54,54,52\nMvaa 92,136,184\nAD 55,55,53\nv_and_b32 v48, 0xffff0000, v35\nMvaa 92,140,188\nD0 9,24,44,3072\nv_and_b32 v49, 0xffff0000, v39\nMvaa 88,132,188\n"
"v_and_b32 v50, 0xffff0000, v43\nMvaa 88,128,184\nWL 0\nMaaa 0,192,160\nRa 128,10,0\nv_and_b32 v51, 0xffff0000, v47\nMaaa 0,196,164\ns_add_u32 m0, s47, 49152\n"
"s_add_u32 s44, s44, 0x40000\nMaaa 4,204,164\nRa 132,11,0\nMU 52,48,50\nMaaa 4,200,160\nMU 53,49,50\nMaaa 64,208,160\nRa 136,10,2048\nCV 52,52,53\nMaaa 64,212,164\nD0 6,24,44,0\n"
"v_and_b32 v53, 0xffff0000, v52\nMaaa 68,220,164\nRa 140,11,2048\nMaaa 68,216,160\nv_lshlrev_b32 v52, 16, v52\nFM 56,48,51\nMaaa 76,216,168\nRa 144,10,8192\nFM 57,49,51\n"
"Maaa 76,220,172\nAD 54,54,52\n.W580:\ns_cbranch_execz .W584\nMaaa 72,212,172\nRa 148,11,8192\nAD 55,55,53\nMaaa 72,208,168\ns_nop 0\nv_permlane32_swap_b32 v54, v55\n"
"Maaa 12,200,168\nRa 152,10,10240\nv_permlane32_swap_b32 v56, v57\nMaaa 12,204,172\nD0 7,24,44,1024\nAD 54,54,55\nMaaa 8,196,172\nRa 156,11,10240\nMaaa 8,192,168\nAD 56,56,57\n"
"Maaa 16,192,176\nMaaa 16,196,180\nv_permlane16_swap_b32 v54, v56\nMaaa 20,204,180\nAD 54,54,56\nMaaa 20,200,176\nMaaa 80,208,176\n"
"v_add_f32_dpp v54, v54, v54 row_ror:8 row_mask:0xf bank_mask:0xf\nMaaa 80,212,180\nD0 8,24,44,2048\nMaaa 84,220,180\n"
"v_add_f32_dpp v54, v54, v54 row_ror:4 row_mask:0xf bank_mask:0xf\nMaaa 84,216,176\nMaaa 92,216,184\nv_add_f32_dpp v54, v54, v54 row_ror:2 row_mask:0xf bank_mask:0xf\n"
"Maaa 92,220,188\nMaaa 88,212,188\nv_add_f32_dpp v54, v54, v54 row_ror:1 row_mask:0xf bank_mask:0xf\nCV 54,54,54\nMaaa 88,208,184\nv_lshlrev_b32 v54, 16, v54\nMaaa 28,200,184\n"
"v_mul_f32 v55, 0xbfb8aa3b, v54\nMaaa 28,204,188\nD0 9,24,44,3072\nMaaa 24,196,188\nEX 55,55\nMaaa 24,192,184\ns_waitcnt vmcnt(12) lgkmcnt(0)\ns_barrier\nMaaa 32,192,224\n"
"Ta 160,12,0\nv_add_f32 v56, 0, v54\nMaaa 32,196,228\nTa 162,12,2048\nMaaa 36,204,228\ns_add_u32 m0, s47, -16384\ns_add_u32 s48, s48, 128\nMaaa 36,200,224\nTa 164,12,16384\n"
"Maaa 96,208,224\nTa 166,12,18432\nbuffer_load_dword v31, v24, s[24:27], s96 offen\ns_add_u32 s96, s96, 0x80000\nMaaa 96,212,228\nD0 2,20,48,0\nv_add_f32 v55, 1.0, v55\n"
"Maaa 100,220,228\nTa 168,13,0\nv_rcp_f32 v55, v55\nMaaa 100,216,224\nTa 170,13,2048\nv_ashrrev_i32 v57, 31, v56\nMaaa 108,216,232\nCV 55,55,55\nv_lshlrev_b32 v55, 16, v55\n"
"Maaa 108,220,236\nTa 172,13,16384\nMaaa 104,212,236\nTa 174,13,18432\nv_or_b32 v57, 0x80000000, v57\nMaaa 104,208,232\nv_xor_b32 v56, v56, v57\nMaaa 44,200,232\nTa 176,14,0\n"
"v_and_b32 v56, 0xffff0000, v56\nMaaa 44,204,236\nTa 178,14,2048\ns_lshl2_add_u32 s2, s10, s11\nMaaa 40,196,236\nD0 3,20,48,1024\nMaaa 40,192,232\nTa 180,14,16384\n"
"v_lshrrev_b32 v48, 5, v0\nMaaa 48,192,240\nTa 182,14,18432\nv_lshl_add_u32 v48, s2, 1, v48\nMaaa 48,196,244\nv_sub_u32 v49, 0xffff, v48\nMaaa 52,204,244\nTa 184,15,0\n"
"v_or_b32 v56, v56, v49\nMaaa 52,200,240\nTa 186,15,2048\nMaaa 112,208,240\nv_mov_b32 v57, s15\nv_and_b32 v49, 31, v0\nMaaa 112,212,244\nTa 188,15,16384\nv_mov_b32 v52, 0x40000000\n"
"Maaa 116,220,244\nTa 190,15,18432\nv_cmp_eq_u32 vcc, 16, v49\nv_lshlrev_b32 v50, 2, v48\nv_lshlrev_b32 v51, 10, v49\nv_cndmask_b32 v50, v52, v50, vcc\n"
"v_cndmask_b32 v56, v55, v56, vcc\nMaaa 116,216,240\nD0 4,20,48,2048\nv_lshl_add_u32 v51, v48, 3, v51\nMaaa 124,216,248\nv_and_b32 v49, 15, v0\nv_cmp_eq_u32 vcc, 0, v49\n"
"s_mov_b32 s2, 0xc040000\ns_mov_b32 s3, 0xc03e000\nv_cndmask_b32 v51, v52, v51, vcc\nbuffer_store_dwordx2 v[56:57], v51, s[16:19], s2 offen sc0 sc1\n"
"buffer_store_dword v54, v50, s[16:19], s3 offen sc0 sc1\nMaaa 124,220,252\nMaaa 120,212,252\nMaaa 120,208,248\nMaaa 60,200,248\nMaaa 60,204,252\nD0 5,20,48,3072\nMaaa 56,196,252\n"
"Maaa 56,192,248\nWL 0\ns_mov_b32 s50, 3\n.GL413:\nMvaa 64,128,160\nTa 224,16,0\nMvaa 64,132,164\nTa 226,16,2048\nMvaa 68,140,164\nTa 228,16,16384\ns_add_u32 m0, s47, -65536\n"
"s_add_u32 s45, s45, 128\nMvaa 68,136,160\nTa 230,16,18432\nMvaa 128,144,160\nTa 232,17,0\nMvaa 128,148,164\nTa 234,17,2048\nD0 2,20,45,0\nMvaa 132,156,164\nTa 236,17,16384\n"
"Mvaa 132,152,160\nTa 238,17,18432\nMvaa 140,152,168\nTa 240,18,0\nMvaa 140,156,172\nTa 242,18,2048\nMvaa 136,148,172\nTa 244,18,16384\nMvaa 136,144,168\nTa 246,18,18432\n"
"Mvaa 76,136,168\nTa 248,19,0\nMvaa 76,140,172\nTa 250,19,2048\nMvaa 72,132,172\nTa 252,19,16384\nD0 3,20,45,1024\nMvaa 72,128,168\nTa 254,19,18432\nMvaa 80,128,176\n"
"Ra 192,10,16384\nMvaa 80,132,180\nRa 196,11,16384\nMvaa 84,140,180\nRa 200,10,18432\nMvaa 84,136,176\nRa 204,11,18432\nMvaa 144,144,176\nRa 208,10,24576\nMvaa 144,148,180\n"
"Ra 212,11,24576\nMvaa 148,156,180\nRa 216,10,26624\nMvaa 148,152,176\nRa 220,11,26624\nD0 4,20,45,2048\nMvaa 156,152,184\nMvaa 156,156,188\nMvaa 152,148,188\nMvaa 152,144,184\n"
"Mvaa 92,136,184\nMvaa 92,140,188\nD0 5,20,45,3072\nMvaa 88,132,188\nMvaa 88,128,184\ns_waitcnt vmcnt(18) lgkmcnt(8)\ns_barrier\nMvaa 96,128,224\nMvaa 96,132,228\n"
"s_add_u32 m0, s47, 0\ns_add_u32 s44, s44, 0x40000\ns_mov_b32 s86, 1\ns_load_dword s86, s[54:55], s94\nMvaa 100,140,228\ns_add_u32 s94, s94, 1024\nMvaa 100,136,224\n"
"Mvaa 160,144,224\nMvaa 160,148,228\nD0 6,24,44,0\nMvaa 164,156,228\nMvaa 164,152,224\nMvaa 172,152,232\nMvaa 172,156,236\nMvaa 168,148,236\nMvaa 168,144,232\n"
"s_add_u32 s88, s88, s86\nMvaa 108,136,232\nMvaa 108,140,236\nD0 7,24,44,1024\ns_add_u32 s88, s88, s86\nMvaa 104,132,236\nMvaa 104,128,232\ns_add_u32 s88, s88, s86\n"
"Mvaa 112,128,240\nMvaa 112,132,244\ns_add_u32 s88, s88, s86\nMvaa 116,140,244\nMvaa 116,136,240\ns_add_u32 s88, s88, s86\nMvaa 176,144,240\nMvaa 176,148,244\nD0 8,24,44,2048\n"
"s_add_u32 s88, s88, s86\nMvaa 180,156,244\nMvaa 180,152,240\ns_add_u32 s88, s88, s86\nMvaa 188,152,248\nMvaa 188,156,252\ns_add_u32 s88, s88, s86\nMvaa 184,148,252\n"
"Mvaa 184,144,248\ns_add_u32 s88, s88, s86\nMvaa 124,136,248\nMvaa 124,140,252\nD0 9,24,44,3072\ns_add_u32 s88, s88, s86\nMvaa 120,132,252\nMvaa 120,128,248\nWL 0\n"
"s_add_u32 s88, s88, s86\nMaaa 32,192,224\nRa 128,10,32768\nMaaa 32,196,228\ns_add_u32 m0, s47, 16384\ns_add_u32 s44, s44, 0x40000\ns_add_u32 s88, s88, s86\nMaaa 36,204,228\n"
"Ra 132,11,32768\nMaaa 36,200,224\ns_add_u32 s88, s88, s86\nMaaa 96,208,224\nRa 136,10,34816\nMaaa 96,212,228\nD0 6,24,44,0\ns_add_u32 s88, s88, s86\nMaaa 100,220,228\n"
"Ra 140,11,34816\nMaaa 100,216,224\ns_add_u32 s88, s88, s86\nMaaa 108,216,232\nRa 144,10,40960\nMaaa 108,220,236\ns_add_u32 s88, s88, s86\nMaaa 104,212,236\nRa 148,11,40960\n"
"Maaa 104,208,232\nMaaa 44,200,232\nRa 152,10,43008\nMaaa 44,204,236\nD0 7,24,44,1024\nMaaa 40,196,236\nRa 156,11,43008\nMaaa 40,192,232\nMaaa 48,192,240\nMaaa 48,196,244\n"
"Maaa 52,204,244\nMaaa 52,200,240\nMaaa 112,208,240\nMaaa 112,212,244\nD0 8,24,44,2048\nMaaa 116,220,244\nMaaa 116,216,240\nMaaa 124,216,248\nMaaa 124,220,252\nMaaa 120,212,252\n"
"Maaa 120,208,248\nMaaa 60,200,248\nMaaa 60,204,252\nD0 9,24,44,3072\nMaaa 56,196,252\nMaaa 56,192,248\ns_waitcnt vmcnt(12) lgkmcnt(0)\ns_barrier\nMaaa 0,192,160\nTa 224,16,32768\n"
"s_mov_b32 m0, s76\ns_add_u32 s76, s76, 1024\nMaaa 0,196,164\nTa 226,16,34816\nD0 1,36,75,0\ns_add_u32 s75, s75, 1024\nMaaa 4,204,164\ns_add_u32 m0, s47, -49152\n"
"s_add_u32 s48, s48, 128\nMaaa 4,200,160\nTa 228,16,49152\nMaaa 64,208,160\nTa 230,16,51200\nbuffer_load_dword v31, v24, s[24:27], s96 offen\ns_add_u32 s96, s96, 0x80000\n"
"Maaa 64,212,164\nD0 2,20,48,0\nMaaa 68,220,164\nTa 232,17,32768\nMaaa 68,216,160\nTa 234,17,34816\nMaaa 76,216,168\nMaaa 76,220,172\nTa 236,17,49152\nMaaa 72,212,172\n"
"Ta 238,17,51200\nMaaa 72,208,168\nMaaa 12,200,168\nTa 240,18,32768\nMaaa 12,204,172\nTa 242,18,34816\nMaaa 8,196,172\nD0 3,20,48,1024\nMaaa 8,192,168\nTa 244,18,49152\n"
"Maaa 16,192,176\nTa 246,18,51200\nMaaa 16,196,180\nMaaa 20,204,180\nTa 248,19,32768\nMaaa 20,200,176\nTa 250,19,34816\nMaaa 80,208,176\nMaaa 80,212,180\nTa 252,19,49152\n"
"Maaa 84,220,180\nTa 254,19,51200\nMaaa 84,216,176\nD0 4,20,48,2048\nMaaa 92,216,184\nMaaa 92,220,188\nMaaa 88,212,188\nMaaa 88,208,184\nMaaa 28,200,184\nMaaa 28,204,188\n"
"D0 5,20,48,3072\nMaaa 24,196,188\nMaaa 24,192,184\nWL 0\nMvaa 96,128,224\nTa 160,12,32768\nMvaa 96,132,228\nTa 162,12,34816\nMvaa 100,140,228\nTa 164,12,49152\n"
"s_add_u32 m0, s47, -32768\ns_add_u32 s45, s45, 128\nMvaa 100,136,224\nTa 166,12,51200\nMvaa 160,144,224\nTa 168,13,32768\nMvaa 160,148,228\nTa 170,13,34816\nD0 2,20,45,0\n"
"Mvaa 164,156,228\nTa 172,13,49152\nMvaa 164,152,224\nTa 174,13,51200\nMvaa 172,152,232\nTa 176,14,32768\nMvaa 172,156,236\nTa 178,14,34816\nMvaa 168,148,236\nTa 180,14,49152\n"
"Mvaa 168,144,232\nTa 182,14,51200\nMvaa 108,136,232\nTa 184,15,32768\nMvaa 108,140,236\nTa 186,15,34816\nMvaa 104,132,236\nTa 188,15,49152\nD0 3,20,45,1024\nMvaa 104,128,232\n"
"Ta 190,15,51200\nMvaa 112,128,240\nRa 192,10,49152\nMvaa 112,132,244\nRa 196,11,49152\nMvaa 116,140,244\nRa 200,10,51200\nMvaa 116,136,240\nRa 204,11,51200\nMvaa 176,144,240\n"
"Ra 208,10,57344\nMvaa 176,148,244\nRa 212,11,57344\nMvaa 180,156,244\nRa 216,10,59392\nMvaa 180,152,240\nRa 220,11,59392\nD0 4,20,45,2048\nMvaa 188,152,248\nMvaa 188,156,252\n"
"Mvaa 184,148,252\nMvaa 184,144,248\nMvaa 124,136,248\nMvaa 124,140,252\nD0 5,20,45,3072\nMvaa 120,132,252\nMvaa 120,128,248\ns_waitcnt vmcnt(18) lgkmcnt(8)\ns_barrier\n"
"Mvaa 64,128,160\nMvaa 64,132,164\ns_add_u32 m0, s47, 32768\ns_add_u32 s44, s44, 0x40000\ns_mov_b32 s86, 1\ns_load_dword s86, s[54:55], s95\nMvaa 68,140,164\n"
"s_add_u32 s95, s95, 1024\nMvaa 68,136,160\nMvaa 128,144,160\nMvaa 128,148,164\nD0 6,24,44,0\nMvaa 132,156,164\nMvaa 132,152,160\nMvaa 140,152,168\nMvaa 140,156,172\n"
"Mvaa 136,148,172\nMvaa 136,144,168\ns_sub_u32 s88, s88, s86\nMvaa 76,136,168\nMvaa 76,140,172\nD0 7,24,44,1024\ns_sub_u32 s88, s88, s86\nMvaa 72,132,172\nMvaa 72,128,168\n"
"s_sub_u32 s88, s88, s86\nMvaa 80,128,176\nMvaa 80,132,180\ns_sub_u32 s88, s88, s86\nMvaa 84,140,180\nMvaa 84,136,176\ns_sub_u32 s88, s88, s86\nMvaa 144,144,176\nMvaa 144,148,180\n"
"D0 8,24,44,2048\ns_sub_u32 s88, s88, s86\nMvaa 148,156,180\nMvaa 148,152,176\ns_sub_u32 s88, s88, s86\nMvaa 156,152,184\nMvaa 156,156,188\ns_sub_u32 s88, s88, s86\n"
"Mvaa 152,148,188\nMvaa 152,144,184\ns_sub_u32 s88, s88, s86\nMvaa 92,136,184\nMvaa 92,140,188\nD0 9,24,44,3072\ns_sub_u32 s88, s88, s86\nMvaa 88,132,188\nMvaa 88,128,184\nWL 0\n"
"s_sub_u32 s88, s88, s86\nMaaa 0,192,160\nRa 128,10,0\nMaaa 0,196,164\ns_add_u32 m0, s47, 49152\ns_add_u32 s44, s44, 0x40000\ns_sub_u32 s88, s88, s86\nMaaa 4,204,164\nRa 132,11,0\n"
"Maaa 4,200,160\ns_sub_u32 s88, s88, s86\nMaaa 64,208,160\nRa 136,10,2048\nMaaa 64,212,164\nD0 6,24,44,0\ns_sub_u32 s88, s88, s86\nMaaa 68,220,164\nRa 140,11,2048\nMaaa 68,216,160\n"
"s_sub_u32 s88, s88, s86\nMaaa 76,216,168\nRa 144,10,8192\nMaaa 76,220,172\ns_sub_u32 s88, s88, s86\nMaaa 72,212,172\nRa 148,11,8192\nMaaa 72,208,168\nMaaa 12,200,168\n"
"Ra 152,10,10240\nMaaa 12,204,172\nD0 7,24,44,1024\nMaaa 8,196,172\nRa 156,11,10240\nMaaa 8,192,168\nMaaa 16,192,176\nMaaa 16,196,180\nMaaa 20,204,180\nMaaa 20,200,176\n"
"Maaa 80,208,176\nMaaa 80,212,180\nD0 8,24,44,2048\nMaaa 84,220,180\nMaaa 84,216,176\nMaaa 92,216,184\nMaaa 92,220,188\nMaaa 88,212,188\nMaaa 88,208,184\nMaaa 28,200,184\n"
"Maaa 28,204,188\nD0 9,24,44,3072\nMaaa 24,196,188\nMaaa 24,192,184\ns_waitcnt vmcnt(12) lgkmcnt(0)\ns_barrier\nMaaa 32,192,224\nTa 160,12,0\ns_mov_b32 m0, s76\n"
"s_add_u32 s76, s76, 1024\nMaaa 32,196,228\nTa 162,12,2048\nD0 1,36,75,0\ns_add_u32 s75, s75, 1024\nMaaa 36,204,228\ns_add_u32 m0, s47, -16384\ns_add_u32 s48, s48, 128\n"
"Maaa 36,200,224\nTa 164,12,16384\nMaaa 96,208,224\nTa 166,12,18432\nbuffer_load_dword v31, v24, s[24:27], s96 offen\ns_add_u32 s96, s96, 0x80000\nMaaa 96,212,228\n.W581:\n"
"s_cbranch_execz .W585\nD0 2,20,48,0\nMaaa 100,220,228\nTa 168,13,0\nMaaa 100,216,224\nTa 170,13,2048\nMaaa 108,216,232\nMaaa 108,220,236\nTa 172,13,16384\nMaaa 104,212,236\n"
"Ta 174,13,18432\nMaaa 104,208,232\nMaaa 44,200,232\nTa 176,14,0\nMaaa 44,204,236\nTa 178,14,2048\nMaaa 40,196,236\nD0 3,20,48,1024\nMaaa 40,192,232\nTa 180,14,16384\n"
"Maaa 48,192,240\nTa 182,14,18432\nMaaa 48,196,244\nMaaa 52,204,244\nTa 184,15,0\nMaaa 52,200,240\nTa 186,15,2048\nMaaa 112,208,240\nMaaa 112,212,244\nTa 188,15,16384\n"
"Maaa 116,220,244\nTa 190,15,18432\nMaaa 116,216,240\nD0 4,20,48,2048\nMaaa 124,216,248\nMaaa 124,220,252\nMaaa 120,212,252\nMaaa 120,208,248\nMaaa 60,200,248\n"
"s_xor_b32 s94, s94, 0x2000\ns_xor_b32 s95, s95, 0x2000\ns_sub_u32 s88, 0, s88\nMaaa 60,204,252\nD0 5,20,48,3072\ns_sub_u32 s50, s50, 1\nMaaa 56,196,252\nMaaa 56,192,248\nWL 0\n"
"s_cbranch_scc0 .GL413\nMvaa 64,128,160\nTa 224,16,0\nMvaa 64,132,164\nTa 226,16,2048\nMvaa 68,140,164\ns_add_u32 m0, s47, -65536\ns_add_u32 s45, s45, 128\nMvaa 68,136,160\n"
"Ta 228,16,16384\nMvaa 128,144,160\nTa 230,16,18432\nMvaa 128,148,164\nD0 2,20,45,0\nMvaa 132,156,164\nTa 232,17,0\nMvaa 132,152,160\nTa 234,17,2048\nMvaa 140,152,168\n"
"s_sub_u32 s89, 0, s88\ns_cmp_eq_u32 s92, 0\ns_cselect_b32 s88, s88, s89\nMvaa 140,156,172\nTa 236,17,16384\nMvaa 136,148,172\nTa 238,17,18432\nMvaa 136,144,168\n"
"v_mov_b32 v22, s88\nMvaa 76,136,168\nTa 240,18,0\nMvaa 76,140,172\nTa 242,18,2048\nMvaa 72,132,172\nD0 3,20,45,1024\nMvaa 72,128,168\nTa 244,18,16384\nMvaa 80,128,176\n"
"Ta 246,18,18432\nMvaa 80,132,180\nv_mov_b32 v23, s15\nMvaa 84,140,180\nTa 248,19,0\nMvaa 84,136,176\nTa 250,19,2048\nMvaa 144,144,176\n"
"buffer_store_dwordx2 v[22:23], off, s[16:19], s93 sc0 sc1\nMvaa 144,148,180\nTa 252,19,16384\nMvaa 148,156,180\nTa 254,19,18432\nMvaa 148,152,176\nD0 4,20,45,2048\n"
"Mvaa 156,152,184\nMvaa 156,156,188\nMvaa 152,148,188\nMvaa 152,144,184\nMvaa 92,136,184\nMvaa 92,140,188\nD0 5,20,45,3072\nMvaa 88,132,188\nMvaa 88,128,184\n"
"s_waitcnt vmcnt(19) lgkmcnt(0)\ns_barrier\nMvaa 96,128,224\nRa 192,10,16384\nMvaa 96,132,228\ns_add_u32 m0, s47, 0\ns_add_u32 s44, s44, 0x40000\nMvaa 100,140,228\nRa 196,11,16384\n"
"Mvaa 100,136,224\nMvaa 160,144,224\nRa 200,10,18432\nMvaa 160,148,228\nD0 6,24,44,0\nMvaa 164,156,228\nRa 204,11,18432\nMvaa 164,152,224\nMvaa 172,152,232\nRa 208,10,24576\n"
"Mvaa 172,156,236\nMvaa 168,148,236\nRa 212,11,24576\nMvaa 168,144,232\nMvaa 108,136,232\nRa 216,10,26624\nMvaa 108,140,236\nD0 7,24,44,1024\nMvaa 104,132,236\nRa 220,11,26624\n"
"Mvaa 104,128,232\nMvaa 112,128,240\nMvaa 112,132,244\nMvaa 116,140,244\nMvaa 116,136,240\nMvaa 176,144,240\nMvaa 176,148,244\nD0 8,24,44,2048\nMvaa 180,156,244\nMvaa 180,152,240\n"
"Mvaa 188,152,248\nMvaa 188,156,252\nMvaa 184,148,252\nMvaa 184,144,248\nMvaa 124,136,248\nMvaa 124,140,252\nD0 9,24,44,3072\nMvaa 120,132,252\nMvaa 120,128,248\nWL 0\n"
"Maaa 32,192,224\nRa 128,10,32768\nMaaa 32,196,228\ns_add_u32 m0, s47, 16384\ns_add_u32 s44, s44, 0x40000\nMaaa 36,204,228\nRa 132,11,32768\nMaaa 36,200,224\nMaaa 96,208,224\n"
"Ra 136,10,34816\nMaaa 96,212,228\nD0 6,24,44,0\nMaaa 100,220,228\nRa 140,11,34816\nMaaa 100,216,224\nMaaa 108,216,232\nRa 144,10,40960\nMaaa 108,220,236\nMaaa 104,212,236\n"
"Ra 148,11,40960\nMaaa 104,208,232\nMaaa 44,200,232\nRa 152,10,43008\nMaaa 44,204,236\nD0 7,24,44,1024\nMaaa 40,196,236\nRa 156,11,43008\nMaaa 40,192,232\nMaaa 48,192,240\n"
"Maaa 48,196,244\nMaaa 52,204,244\nMaaa 52,200,240\nMaaa 112,208,240\nMaaa 112,212,244\nD0 8,24,44,2048\nMaaa 116,220,244\nMaaa 116,216,240\nMaaa 124,216,248\nMaaa 124,220,252\n"
"Maaa 120,212,252\nMaaa 120,208,248\nMaaa 60,200,248\nMaaa 60,204,252\nD0 9,24,44,3072\nMaaa 56,196,252\nMaaa 56,192,248\ns_waitcnt vmcnt(19) lgkmcnt(0)\ns_barrier\nMaaa 0,192,160\n"
"Ta 224,16,32768\nMaaa 0,196,164\nTa 226,16,34816\nMaaa 4,204,164\ns_add_u32 m0, s47, -49152\ns_add_u32 s48, s48, 128\nMaaa 4,200,160\nTa 228,16,49152\nMaaa 64,208,160\n"
"Ta 230,16,51200\nbuffer_load_dword v31, v24, s[24:27], s96 offen\ns_add_u32 s96, s96, 0x80000\nMaaa 64,212,164\nD0 2,20,48,0\nMaaa 68,220,164\nTa 232,17,32768\nMaaa 68,216,160\n"
"Ta 234,17,34816\nMaaa 76,216,168\nMaaa 76,220,172\nTa 236,17,49152\nMaaa 72,212,172\nTa 238,17,51200\nMaaa 72,208,168\nMaaa 12,200,168\nTa 240,18,32768\nMaaa 12,204,172\n"
"Ta 242,18,34816\nMaaa 8,196,172\nD0 3,20,48,1024\nMaaa 8,192,168\nTa 244,18,49152\nMaaa 16,192,176\nTa 246,18,51200\nMaaa 16,196,180\nMaaa 20,204,180\nTa 248,19,32768\n"
"Maaa 20,200,176\nTa 250,19,34816\nMaaa 80,208,176\nMaaa 80,212,180\nTa 252,19,49152\nMaaa 84,220,180\nTa 254,19,51200\nMaaa 84,216,176\nD0 4,20,48,2048\nMaaa 92,216,184\n"
"Maaa 92,220,188\nMaaa 88,212,188\nMaaa 88,208,184\nMaaa 28,200,184\nMaaa 28,204,188\nD0 5,20,48,3072\nMaaa 24,196,188\nMaaa 24,192,184\nWL 0\nMvaa 96,128,224\nTa 160,12,32768\n"
"Mvaa 96,132,228\nTa 162,12,34816\nMvaa 100,140,228\ns_add_u32 m0, s47, -32768\ns_add_u32 s45, s45, 128\nMvaa 100,136,224\nTa 164,12,49152\nMvaa 160,144,224\nTa 166,12,51200\n"
"Mvaa 160,148,228\nD0 2,20,45,0\nMvaa 164,156,228\nTa 168,13,32768\nMvaa 164,152,224\nTa 170,13,34816\nMvaa 172,152,232\nMvaa 172,156,236\nTa 172,13,49152\nMvaa 168,148,236\n"
"Ta 174,13,51200\nMvaa 168,144,232\nMvaa 108,136,232\nTa 176,14,32768\nMvaa 108,140,236\nTa 178,14,34816\nMvaa 104,132,236\nD0 3,20,45,1024\nMvaa 104,128,232\nTa 180,14,49152\n"
"Mvaa 112,128,240\nTa 182,14,51200\nMvaa 112,132,244\nMvaa 116,140,244\nTa 184,15,32768\nMvaa 116,136,240\nTa 186,15,34816\nMvaa 176,144,240\nMvaa 176,148,244\nTa 188,15,49152\n"
"Mvaa 180,156,244\nTa 190,15,51200\nMvaa 180,152,240\nD0 4,20,45,2048\nMvaa 188,152,248\nMvaa 188,156,252\nMvaa 184,148,252\nMvaa 184,144,248\nMvaa 124,136,248\nMvaa 124,140,252\n"
"D0 5,20,45,3072\nMvaa 120,132,252\nMvaa 120,128,248\ns_waitcnt vmcnt(17) lgkmcnt(0)\ns_barrier\nMvaa 64,128,160\nRa 192,10,49152\nMvaa 64,132,164\ns_add_u32 m0, s47, 32768\n"
"s_add_u32 s44, s44, 0x40000\nMvaa 68,140,164\nRa 196,11,49152\nMvaa 68,136,160\nMvaa 128,144,160\nRa 200,10,51200\nMvaa 128,148,164\nD0 6,24,44,0\nMvaa 132,156,164\n"
"Ra 204,11,51200\nMvaa 132,152,160\nMvaa 140,152,168\nRa 208,10,57344\nMvaa 140,156,172\ns_lshl_b32 s5, s73, 9\ns_add_u32 s5, s5, 0xc049400\nv_lshrrev_b32 v27, 1, v1\n"
"buffer_load_dwordx2 v[22:23], v27, s[16:19], s5 offen sc0 sc1\nMvaa 136,148,172\nRa 212,11,57344\nMvaa 136,144,168\nMvaa 76,136,168\nRa 216,10,59392\nMvaa 76,140,172\n"
"D0 7,24,44,1024\nMvaa 72,132,172\nRa 220,11,59392\nMvaa 72,128,168\nMvaa 80,128,176\nMvaa 80,132,180\nMvaa 84,140,180\nMvaa 84,136,176\nMvaa 144,144,176\nMvaa 144,148,180\n"
"D0 8,24,44,2048\nMvaa 148,156,180\nMvaa 148,152,176\nMvaa 156,152,184\nMvaa 156,156,188\nMvaa 152,148,188\nMvaa 152,144,184\nMvaa 92,136,184\nMvaa 92,140,188\nD0 9,24,44,3072\n"
"Mvaa 88,132,188\nMvaa 88,128,184\nWL 0\nMaaa 0,192,160\nRa 128,10,0\nMaaa 0,196,164\ns_add_u32 m0, s47, 49152\ns_add_u32 s44, s44, 0x40000\nMaaa 4,204,164\nRa 132,11,0\n"
"Maaa 4,200,160\nMaaa 64,208,160\nRa 136,10,2048\nMaaa 64,212,164\nD0 6,24,44,0\nMaaa 68,220,164\nRa 140,11,2048\nMaaa 68,216,160\nMaaa 76,216,168\nRa 144,10,8192\nMaaa 76,220,172\n"
"Maaa 72,212,172\nRa 148,11,8192\nMaaa 72,208,168\nMaaa 12,200,168\nRa 152,10,10240\nMaaa 12,204,172\nD0 7,24,44,1024\nMaaa 8,196,172\nRa 156,11,10240\nMaaa 8,192,168\n"
"Maaa 16,192,176\nMaaa 16,196,180\nMaaa 20,204,180\nMaaa 20,200,176\nMaaa 80,208,176\nMaaa 80,212,180\nD0 8,24,44,2048\nMaaa 84,220,180\nMaaa 84,216,176\nMaaa 92,216,184\n"
"Maaa 92,220,188\nMaaa 88,212,188\nMaaa 88,208,184\nMaaa 28,200,184\nMaaa 28,204,188\nD0 9,24,44,3072\nMaaa 24,196,188\nMaaa 24,192,184\ns_waitcnt vmcnt(18) lgkmcnt(0)\ns_barrier\n"
"Maaa 32,192,224\nTa 160,12,0\nMaaa 32,196,228\nTa 162,12,2048\nMaaa 36,204,228\ns_add_u32 m0, s47, -16384\ns_add_u32 s48, s48, 128\nMaaa 36,200,224\nTa 164,12,16384\n"
"Maaa 96,208,224\nTa 166,12,18432\nbuffer_load_dword v31, v24, s[24:27], s96 offen\ns_add_u32 s96, s96, 0x80000\nMaaa 96,212,228\nD0 2,20,48,0\nMaaa 100,220,228\nTa 168,13,0\n"
"Maaa 100,216,224\nTa 170,13,2048\nMaaa 108,216,232\nMaaa 108,220,236\nTa 172,13,16384\nMaaa 104,212,236\nTa 174,13,18432\nMaaa 104,208,232\nMaaa 44,200,232\nTa 176,14,0\n"
"Maaa 44,204,236\nTa 178,14,2048\nMaaa 40,196,236\nD0 3,20,48,1024\nMaaa 40,192,232\nTa 180,14,16384\nMaaa 48,192,240\nTa 182,14,18432\nMaaa 48,196,244\nMaaa 52,204,244\n"
"Ta 184,15,0\nMaaa 52,200,240\nTa 186,15,2048\nMaaa 112,208,240\nMaaa 112,212,244\nTa 188,15,16384\nMaaa 116,220,244\nTa 190,15,18432\nMaaa 116,216,240\nD0 4,20,48,2048\n"
"Maaa 124,216,248\nMaaa 124,220,252\nMaaa 120,212,252\nMaaa 120,208,248\nMaaa 60,200,248\nMaaa 60,204,252\nD0 5,20,48,3072\nMaaa 56,196,252\nMaaa 56,192,248\nWL 0\nMvaa 64,128,160\n"
"Ta 224,16,0\nWV 12\nv_cmp_ne_u32 vcc, s15, v23\ns_nop 0\ns_cbranch_vccnz .S410\n.S411:\nMvaa 64,132,164\nTa 226,16,2048\nMvaa 68,140,164\ns_add_u32 m0, s47, -65536\n"
"s_add_u32 s45, s45, 128\nMvaa 68,136,160\nTa 228,16,16384\nMvaa 128,144,160\nTa 230,16,18432\nMvaa 128,148,164\nD0 2,20,45,0\nMvaa 132,156,164\nTa 232,17,0\nMvaa 132,152,160\n"
"Ta 234,17,2048\nMvaa 140,152,168\nv_add_u32_dpp v22, v22, v22 row_ror:8 row_mask:0xf bank_mask:0xf\nMvaa 140,156,172\nTa 236,17,16384\nMvaa 136,148,172\nTa 238,17,18432\n"
"Mvaa 136,144,168\nv_add_u32_dpp v22, v22, v22 row_ror:4 row_mask:0xf bank_mask:0xf\nMvaa 76,136,168\nTa 240,18,0\nMvaa 76,140,172\nTa 242,18,2048\nMvaa 72,132,172\n"
"D0 3,20,45,1024\nMvaa 72,128,168\nTa 244,18,16384\nMvaa 80,128,176\nTa 246,18,18432\nMvaa 80,132,180\nv_add_u32_dpp v22, v22, v22 row_ror:2 row_mask:0xf bank_mask:0xf\n"
"Mvaa 84,140,180\nTa 248,19,0\nMvaa 84,136,176\nTa 250,19,2048\nMvaa 144,144,176\nv_add_u32_dpp v22, v22, v22 row_ror:1 row_mask:0xf bank_mask:0xf\nMvaa 144,148,180\n"
"Ta 252,19,16384\nMvaa 148,156,180\nTa 254,19,18432\n.W582:\ns_cbranch_execz .W586\nMvaa 148,152,176\nD0 4,20,45,2048\nMvaa 156,152,184\nv_mov_b32 v23, v22\nMvaa 156,156,188\n"
"Mvaa 152,148,188\nv_permlane16_swap_b32 v22, v23\nMvaa 152,144,184\nv_add_u32 v22, v22, v23\nMvaa 92,136,184\nv_mov_b32 v23, v22\nMvaa 92,140,188\nD0 5,20,45,3072\n"
"Mvaa 88,132,188\nv_permlane32_swap_b32 v22, v23\nMvaa 88,128,184\nWL 0\ns_barrier\nMvaa 96,128,224\nRa 192,10,16384\nMvaa 96,132,228\ns_add_u32 m0, s47, 0\n"
"s_add_u32 s44, s44, 0x40000\nMvaa 100,140,228\nRa 196,11,16384\nMvaa 100,136,224\nv_add_u32 v22, v22, v23\nMvaa 160,144,224\nRa 200,10,18432\nMvaa 160,148,228\nD0 6,24,44,0\n"
"Mvaa 164,156,228\nRa 204,11,18432\nMvaa 164,152,224\nv_readfirstlane_b32 s4, v22\nMvaa 172,152,232\nRa 208,10,24576\nMvaa 172,156,236\nMvaa 168,148,236\nRa 212,11,24576\n"
"s_cmp_gt_i32 s4, 0\ns_cselect_b32 s4, 1, 0\nMvaa 168,144,232\ns_xor_b32 s4, s4, s12\nMvaa 108,136,232\nRa 216,10,26624\nMvaa 108,140,236\nD0 7,24,44,1024\nMvaa 104,132,236\n"
"Ra 220,11,26624\nMvaa 104,128,232\ns_and_b32 s4, s4, 1\nMvaa 112,128,240\ns_lshl_b32 s4, s4, 13\nMvaa 112,132,244\ns_mul_i32 s72, s73, 25165824\nMvaa 116,140,244\n"
"s_add_u32 s72, s72, s4\nMvaa 116,136,240\ns_and_b32 s4, s12, 1\nMvaa 176,144,240\ns_lshl_b32 s4, s4, 21\nMvaa 176,148,244\nD0 8,24,44,2048\nMvaa 180,156,244\n"
"s_add_u32 s72, s72, s4\nMvaa 180,152,240\ns_lshl3_add_u32 s2, s13, s11\nMvaa 188,152,248\ns_lshl_b32 s2, s2, 11\nMvaa 188,156,252\ns_lshl_b32 s3, s14, 22\nMvaa 184,148,252\n"
"s_add_u32 s2, s2, s3\nMvaa 184,144,248\ns_add_u32 s2, s2, s72\nMvaa 124,136,248\ns_add_u32 s49, s2, 0xbe0000\nMvaa 124,140,252\nD0 9,24,44,3072\nMvaa 120,132,252\n"
"Mvaa 120,128,248\nWL 0\nMaaa 32,192,224\nRa 128,10,32768\ns_mov_b32 s4, s13\nMaaa 32,196,228\ns_add_u32 m0, s47, 16384\ns_add_u32 s44, s44, 0x40000\nMaaa 36,204,228\n"
"Ra 132,11,32768\nv_lshrrev_b32 v32, 2, v0\nMaaa 36,200,224\nv_mul_u32_u24 v32, 896, v32\nMaaa 96,208,224\nRa 136,10,34816\nv_and_b32 v33, 3, v0\nMaaa 96,212,228\nD0 6,24,44,0\n"
"Maaa 100,220,228\nRa 140,11,34816\nv_lshl_add_u32 v32, v33, 4, v32\nMaaa 100,216,224\ns_lshr_b32 s2, s11, 1\nMaaa 108,216,232\nRa 144,10,40960\ns_mul_i32 s3, s2, 448\n"
"Maaa 108,220,236\ns_and_b32 s5, s11, 1\nMaaa 104,212,236\nRa 148,11,40960\ns_lshl3_add_u32 s3, s5, s3\nMaaa 104,208,232\ns_add_u32 s3, s3, 131072\nMaaa 44,200,232\n"
"Ra 152,10,43008\nv_add_u32 v32, s3, v32\nMaaa 44,204,236\nD0 7,24,44,1024\nMaaa 40,196,236\nRa 156,11,43008\nTv 36,32,0\nMaaa 40,192,232\nTv 38,32,14336\nMaaa 48,192,240\n"
"Tv 40,32,64\nTv 42,32,14400\nMaaa 48,196,244\nTv 44,32,128\nMaaa 52,204,244\nTv 46,32,14464\nTv 48,32,192\nMaaa 52,200,240\nTv 50,32,14528\nMaaa 112,208,240\nTv 52,32,256\n"
"Tv 54,32,14592\nMaaa 112,212,244\nD0 8,24,44,2048\nMaaa 116,220,244\nTv 56,32,320\nTv 58,32,14656\nMaaa 116,216,240\nTv 60,32,384\nMaaa 124,216,248\nTv 62,32,14720\n"
"s_lshr_b32 s0, s4, 1\nMaaa 124,220,252\ns_mul_i32 s0, s0, 12\nMaaa 120,212,252\ns_lshl1_add_u32 s0, s5, s0\ns_and_b32 s6, s4, 1\nMaaa 120,208,248\ns_add_u32 s0, s0, s6\n"
"Maaa 60,200,248\ns_lshl3_add_u32 s1, s5, s4\ns_add_u32 s77, s1, 48\nMaaa 60,204,252\nD0 9,24,44,3072\nMaaa 56,196,252\ns_and_b32 s4, s12, 1\ns_mov_b32 s7, 0x6543210\n"
"Maaa 56,192,248\ns_waitcnt vmcnt(17) lgkmcnt(14)\ns_barrier\nMaaa 0,192,160\nTa 224,16,32768\ns_cmp_lg_u32 s4, s2\ns_cselect_b32 s6, s0, s1\ns_cselect_b32 s5, 4, 16\n"
"s_cselect_b32 s7, 0x6352410, s7\ns_cselect_b32 s3, 0, 229376\nMaaa 0,196,164\nTa 226,16,34816\nMaaa 4,204,164\ns_add_u32 m0, s47, -49152\ns_add_u32 s48, s48, 128\nMaaa 4,200,160\n"
"Ta 228,16,49152\nMaaa 64,208,160\nTa 230,16,51200\ns_add_u32 s3, s3, s72\nMaaa 64,212,164\nD0 2,20,48,0\nMaaa 68,220,164\nTa 232,17,32768\ns_add_u32 s3, s3, 0x1400000\n"
"Maaa 68,216,160\nTa 234,17,34816\nMaaa 76,216,168\nWL 14\ns_lshr_b32 s0, s6, 4\nMaaa 76,220,172\nTa 236,17,49152\nMaaa 72,212,172\nTa 238,17,51200\ns_lshl_b32 s0, s0, 2\n"
"Maaa 72,208,168\ns_lshr_b32 s0, s7, s0\nMaaa 12,200,168\nTa 240,18,32768\ns_and_b32 s0, s0, 7\nMaaa 12,204,172\nTa 242,18,34816\nMaaa 8,196,172\nD0 3,20,48,1024\n"
"s_lshl_b32 s0, s0, 5\nMaaa 8,192,168\nTa 244,18,49152\nMaaa 16,192,176\nTa 246,18,51200\ns_and_b32 s1, s6, 15\nMaaa 16,196,180\ns_add_u32 s0, s0, s1\nMaaa 20,204,180\n"
"Ta 248,19,32768\ns_and_b32 s1, s6, 8\nMaaa 20,200,176\nTa 250,19,34816\nMaaa 80,208,176\ns_add_u32 s0, s0, s1\ns_lshl_b32 s0, s0, 10\nMaaa 80,212,180\nTa 252,19,49152\n"
"Maaa 84,220,180\nTa 254,19,51200\ns_add_u32 s0, s0, s3\nMaaa 84,216,176\nD0 4,20,48,2048\nMaaa 92,216,184\nSv 36,1,16,0,0\ns_add_u32 s6, s6, s5\nMaaa 92,220,188\nMaaa 88,212,188\n"
"Maaa 88,208,184\nMaaa 28,200,184\nMaaa 28,204,188\nD0 5,20,48,3072\nMaaa 24,196,188\nMaaa 24,192,184\nWL 0\nMvaa 96,128,224\nTa 160,12,32768\ns_lshr_b32 s0, s6, 4\n"
"Mvaa 96,132,228\nTa 162,12,34816\nMvaa 100,140,228\ns_add_u32 m0, s47, -32768\ns_add_u32 s45, s45, 128\nMvaa 100,136,224\nTa 164,12,49152\nMvaa 160,144,224\nTa 166,12,51200\n"
"s_lshl_b32 s0, s0, 2\nMvaa 160,148,228\nD0 2,20,45,0\nMvaa 164,156,228\nTa 168,13,32768\ns_lshr_b32 s0, s7, s0\nMvaa 164,152,224\nTa 170,13,34816\nMvaa 172,152,232\n"
"s_and_b32 s0, s0, 7\ns_lshl_b32 s0, s0, 5\nMvaa 172,156,236\nTa 172,13,49152\nMvaa 168,148,236\nTa 174,13,51200\ns_and_b32 s1, s6, 15\nMvaa 168,144,232\ns_add_u32 s0, s0, s1\n"
"Mvaa 108,136,232\nTa 176,14,32768\ns_and_b32 s1, s6, 8\nMvaa 108,140,236\nTa 178,14,34816\nMvaa 104,132,236\nD0 3,20,45,1024\ns_add_u32 s0, s0, s1\nMvaa 104,128,232\n"
"Ta 180,14,49152\nMvaa 112,128,240\nTa 182,14,51200\ns_lshl_b32 s0, s0, 10\nMvaa 112,132,244\ns_add_u32 s0, s0, s3\nMvaa 116,140,244\nTa 184,15,32768\nSv 40,1,16,0,0\n"
"Mvaa 116,136,240\nTa 186,15,34816\nMvaa 176,144,240\ns_add_u32 s6, s6, s5\nMvaa 176,148,244\nTa 188,15,49152\nMvaa 180,156,244\nTa 190,15,51200\nMvaa 180,152,240\nD0 4,20,45,2048\n"
"Mvaa 188,152,248\nMvaa 188,156,252\nMvaa 184,148,252\nMvaa 184,144,248\nMvaa 124,136,248\ns_lshr_b32 s0, s6, 4\ns_lshl_b32 s0, s0, 2\nMvaa 124,140,252\nD0 5,20,45,3072\n"
"Mvaa 120,132,252\ns_lshr_b32 s0, s7, s0\ns_and_b32 s0, s0, 7\nMvaa 120,128,248\ns_waitcnt vmcnt(18) lgkmcnt(0)\ns_barrier\nMvaa 64,128,160\nRa 192,10,49152\ns_lshl_b32 s0, s0, 5\n"
"Mvaa 64,132,164\ns_add_u32 m0, s47, 32768\ns_add_u32 s44, s44, 0x40000\nMvaa 68,140,164\nRa 196,11,49152\ns_and_b32 s1, s6, 15\nMvaa 68,136,160\ns_add_u32 s0, s0, s1\n"
"Mvaa 128,144,160\nRa 200,10,51200\ns_and_b32 s1, s6, 8\nMvaa 128,148,164\nD0 6,24,44,0\nMvaa 132,156,164\nRa 204,11,51200\ns_add_u32 s0, s0, s1\nMvaa 132,152,160\n"
"s_lshl_b32 s0, s0, 10\nMvaa 140,152,168\nRa 208,10,57344\ns_add_u32 s0, s0, s3\nMvaa 140,156,172\nSv 44,1,16,0,0\nMvaa 136,148,172\nRa 212,11,57344\ns_add_u32 s6, s6, s5\n"
"Mvaa 136,144,168\nMvaa 76,136,168\nRa 216,10,59392\nMvaa 76,140,172\nD0 7,24,44,1024\nMvaa 72,132,172\nRa 220,11,59392\nMvaa 72,128,168\nMvaa 80,128,176\nMvaa 80,132,180\n"
"Mvaa 84,140,180\ns_mov_b32 s6, s77\ns_mov_b32 s5, 16\nMvaa 84,136,176\ns_lshr_b32 s0, s6, 4\nMvaa 144,144,176\ns_lshl_b32 s0, s0, 2\ns_lshr_b32 s0, s7, s0\nMvaa 144,148,180\n"
"D0 8,24,44,2048\nMvaa 148,156,180\ns_and_b32 s0, s0, 7\ns_lshl_b32 s0, s0, 5\nMvaa 148,152,176\ns_and_b32 s1, s6, 15\nMvaa 156,152,184\ns_add_u32 s0, s0, s1\ns_and_b32 s1, s6, 8\n"
"Mvaa 156,156,188\ns_add_u32 s0, s0, s1\nMvaa 152,148,188\ns_lshl_b32 s0, s0, 10\ns_add_u32 s0, s0, s3\nMvaa 152,144,184\nSv 48,1,16,0,0\nMvaa 92,136,184\ns_add_u32 s6, s6, s5\n"
"Mvaa 92,140,188\nD0 9,24,44,3072\nMvaa 88,132,188\nMvaa 88,128,184\nWL 0\nMaaa 0,192,160\nRa 128,10,0\nMaaa 0,196,164\ns_add_u32 m0, s47, 49152\ns_add_u32 s44, s44, 0x40000\n"
"Maaa 4,204,164\nRa 132,11,0\nMaaa 4,200,160\nMaaa 64,208,160\nRa 136,10,2048\ns_lshr_b32 s0, s6, 4\nMaaa 64,212,164\nD0 6,24,44,0\nMaaa 68,220,164\nRa 140,11,2048\n"
"s_lshl_b32 s0, s0, 2\nMaaa 68,216,160\ns_lshr_b32 s0, s7, s0\nMaaa 76,216,168\nRa 144,10,8192\ns_and_b32 s0, s0, 7\nMaaa 76,220,172\ns_lshl_b32 s0, s0, 5\nMaaa 72,212,172\n"
"Ra 148,11,8192\ns_and_b32 s1, s6, 15\nMaaa 72,208,168\ns_add_u32 s0, s0, s1\nMaaa 12,200,168\nRa 152,10,10240\ns_and_b32 s1, s6, 8\nMaaa 12,204,172\nD0 7,24,44,1024\n"
"Maaa 8,196,172\nRa 156,11,10240\ns_add_u32 s0, s0, s1\nMaaa 8,192,168\ns_lshl_b32 s0, s0, 10\nMaaa 16,192,176\ns_add_u32 s0, s0, s3\nSv 52,1,16,0,0\nMaaa 16,196,180\n"
"s_add_u32 s6, s6, s5\nMaaa 20,204,180\nMaaa 20,200,176\nMaaa 80,208,176\nMaaa 80,212,180\nD0 8,24,44,2048\nMaaa 84,220,180\nMaaa 84,216,176\nMaaa 92,216,184\nMaaa 92,220,188\n"
"s_lshr_b32 s0, s6, 4\nMaaa 88,212,188\ns_lshl_b32 s0, s0, 2\ns_lshr_b32 s0, s7, s0\nMaaa 88,208,184\ns_and_b32 s0, s0, 7\nMaaa 28,200,184\ns_lshl_b32 s0, s0, 5\n"
"s_and_b32 s1, s6, 15\nMaaa 28,204,188\nD0 9,24,44,3072\nMaaa 24,196,188\ns_add_u32 s0, s0, s1\ns_and_b32 s1, s6, 8\nMaaa 24,192,184\nWV 21\ns_barrier\nMaaa 32,192,224\n"
"Ta 160,12,0\ns_add_u32 s0, s0, s1\nMaaa 32,196,228\nTa 162,12,2048\nMaaa 36,204,228\ns_add_u32 m0, s47, -16384\ns_add_u32 s48, s48, 128\nMaaa 36,200,224\nTa 164,12,16384\n"
"Maaa 96,208,224\nTa 166,12,18432\ns_lshl_b32 s0, s0, 10\nMaaa 96,212,228\nD0 2,20,48,0\nMaaa 100,220,228\nTa 168,13,0\ns_add_u32 s0, s0, s3\nMaaa 100,216,224\nTa 170,13,2048\n"
"Maaa 108,216,232\nSv 56,1,16,0,0\ns_add_u32 s6, s6, s5\nMaaa 108,220,236\nTa 172,13,16384\nMaaa 104,212,236\nTa 174,13,18432\nMaaa 104,208,232\nMaaa 44,200,232\nTa 176,14,0\n"
"Maaa 44,204,236\nTa 178,14,2048\nMaaa 40,196,236\nD0 3,20,48,1024\nMaaa 40,192,232\nTa 180,14,16384\nMaaa 48,192,240\nTa 182,14,18432\ns_lshr_b32 s0, s6, 4\nMaaa 48,196,244\n"
"s_lshl_b32 s0, s0, 2\nMaaa 52,204,244\nTa 184,15,0\ns_lshr_b32 s0, s7, s0\nMaaa 52,200,240\nTa 186,15,2048\nMaaa 112,208,240\ns_and_b32 s0, s0, 7\ns_lshl_b32 s0, s0, 5\n"
"Maaa 112,212,244\nTa 188,15,16384\nMaaa 116,220,244\nTa 190,15,18432\ns_and_b32 s1, s6, 15\nMaaa 116,216,240\nD0 4,20,48,2048\nMaaa 124,216,248\ns_add_u32 s0, s0, s1\n"
"s_and_b32 s1, s6, 8\nMaaa 124,220,252\ns_add_u32 s0, s0, s1\nMaaa 120,212,252\ns_lshl_b32 s0, s0, 10\ns_add_u32 s0, s0, s3\nMaaa 120,208,248\nSv 60,1,16,0,0\nMaaa 60,200,248\n"
"s_add_u32 s6, s6, s5\nMaaa 60,204,252\nD0 5,20,48,3072\nMaaa 56,196,252\nMaaa 56,192,248\nWL 0\nMvaa 64,128,160\nTa 224,16,0\nMvaa 64,132,164\nTa 226,16,2048\n"
"s_add_u32 m0, s47, 65536\ns_add_u32 s46, s51, 0\nMvaa 68,140,164\nv_lshrrev_b32 v48, 4, v0\nMvaa 68,136,160\nTa 228,16,16384\nD0 6,32,46,0\nMvaa 128,144,160\nTa 230,16,18432\n"
"Mvaa 128,148,164\nv_and_b32 v49, 7, v0\nMvaa 132,156,164\nTa 232,17,0\nMvaa 132,152,160\nTa 234,17,2048\nMvaa 140,152,168\nv_xor_b32 v49, v49, v48\nD0 7,32,46,1024\n"
"Mvaa 140,156,172\nTa 236,17,16384\nMvaa 136,148,172\nTa 238,17,18432\nMvaa 136,144,168\nv_lshlrev_b32 v49, 4, v49\nMvaa 76,136,168\nTa 240,18,0\nMvaa 76,140,172\nTa 242,18,2048\n"
"D0 8,32,46,2048\nMvaa 72,132,172\nv_lshrrev_b32 v48, 3, v0\nMvaa 72,128,168\nTa 244,18,16384\nMvaa 80,128,176\nTa 246,18,18432\nMvaa 80,132,180\nv_mul_u32_u24 v48, 896, v48\n"
"Mvaa 84,140,180\nTa 248,19,0\nD0 9,32,46,3072\nMvaa 84,136,176\nTa 250,19,2048\nMvaa 144,144,176\nv_add_u32 v40, v48, v49\nMvaa 144,148,180\nTa 252,19,16384\n"
"s_add_u32 m0, s47, 81920\ns_add_u32 s46, s51, 262144\nMvaa 148,156,180\nTa 254,19,18432\nMvaa 148,152,176\nv_xor_b32 v49, 64, v40\nD0 6,32,46,0\nMvaa 156,152,184\n"
"v_add_u32 v41, 6144, v49\n.W583:\ns_cbranch_execz .W587\nMvaa 156,156,188\nv_add_u32 v42, 55296, v40\nMvaa 152,148,188\nv_add_u32 v43, 61440, v49\nMvaa 152,144,184\n"
"s_mov_b32 s2, s13\nMvaa 92,136,184\ns_lshl3_add_u32 s2, s2, s11\nD0 7,32,46,1024\nMvaa 92,140,188\ns_mul_i32 s52, s2, 14336\nMvaa 88,132,188\ns_lshr_b32 s2, s13, 2\n"
"s_bfe_u32 s3, s13, 0x10001\nMvaa 88,128,184\ns_waitcnt vmcnt(23) lgkmcnt(0)\ns_barrier\nMvaa 96,128,224\nRa 192,10,16384\ns_mul_i32 s3, s3, 17\nMvaa 96,132,228\n"
"s_add_u32 s2, s2, s3\nD0 8,32,46,2048\nMvaa 100,140,228\nRa 196,11,16384\ns_lshl2_add_u32 s3, s14, 0\nMvaa 100,136,224\ns_lshl_b32 s3, s3, 2\nMvaa 160,144,224\nRa 200,10,18432\n"
"s_lshr_b32 s3, 0xe420ca86, s3\nMvaa 160,148,228\ns_and_b32 s3, s3, 15\nMvaa 164,156,228\nRa 204,11,18432\ns_add_u32 s2, s2, s3\nD0 9,32,46,3072\nMvaa 164,152,224\n"
"s_cmp_eq_u32 s2, 32\ns_cselect_b32 s4, 16, s2\nMvaa 172,152,232\nRa 208,10,24576\ns_mul_i32 s75, s4, 65536\nMvaa 172,156,236\ns_lshr_b32 s2, s13, 2\ns_add_u32 m0, s47, -65536\n"
"s_add_u32 s46, s52, 0\nMvaa 168,148,236\nRa 212,11,24576\ns_bfe_u32 s3, s13, 0x10001\nMvaa 168,144,232\ns_mul_i32 s3, s3, 17\nD0 40,28,46,0\nMvaa 108,136,232\nRa 216,10,26624\n"
"s_add_u32 s2, s2, s3\nMvaa 108,140,236\ns_lshl2_add_u32 s3, s14, 1\nMvaa 104,132,236\nRa 220,11,26624\ns_lshl_b32 s3, s3, 2\nMvaa 104,128,232\ns_lshr_b32 s3, 0xe420ca86, s3\n"
"Mvaa 112,128,240\ns_and_b32 s3, s3, 15\ns_add_u32 s2, s2, s3\nD0 41,28,46,1024\nMvaa 112,132,244\ns_cmp_eq_u32 s2, 32\ns_cselect_b32 s4, 16, s2\nMvaa 116,140,244\n"
"s_mul_i32 s76, s4, 65536\ns_lshr_b32 s2, s13, 2\nMvaa 116,136,240\ns_bfe_u32 s3, s13, 0x10001\nMvaa 176,144,240\ns_mul_i32 s3, s3, 17\ns_add_u32 s2, s2, s3\nMvaa 176,148,244\n"
"s_lshl2_add_u32 s3, s14, 2\nD0 42,28,46,2048\nMvaa 180,156,244\ns_lshl_b32 s3, s3, 2\ns_lshr_b32 s3, 0xe420ca86, s3\nMvaa 180,152,240\ns_and_b32 s3, s3, 15\nMvaa 188,152,248\n"
"s_add_u32 s2, s2, s3\nMvaa 188,156,252\ns_cmp_eq_u32 s2, 32\ns_cselect_b32 s4, 16, s2\nMvaa 184,148,252\ns_mul_i32 s77, s4, 65536\ns_lshr_b32 s2, s13, 2\nD0 43,28,46,3072\n"
"Mvaa 184,144,248\ns_bfe_u32 s3, s13, 0x10001\nMvaa 124,136,248\ns_mul_i32 s3, s3, 17\ns_add_u32 s2, s2, s3\nMvaa 124,140,252\ns_lshl2_add_u32 s3, s14, 3\nMvaa 120,132,252\n"
"s_lshl_b32 s3, s3, 2\ns_lshr_b32 s3, 0xe420ca86, s3\nMvaa 120,128,248\nWL 0\nMaaa 32,192,224\nRa 128,10,32768\ns_and_b32 s3, s3, 15\ns_add_u32 m0, s47, 0\n"
"s_add_u32 s46, s51, 524288\nD0 6,32,46,0\nMaaa 32,196,228\ns_add_u32 s2, s2, s3\ns_cmp_eq_u32 s2, 32\ns_cselect_b32 s4, 16, s2\nMaaa 36,204,228\nRa 132,11,32768\n"
"s_mul_i32 s78, s4, 65536\nMaaa 36,200,224\ns_lshr_b32 s3, s11, 1\nMaaa 96,208,224\nRa 136,10,34816\ns_lshl_b32 s3, s3, 15\nMaaa 96,212,228\ns_add_u32 s2, s72, s3\nD0 7,32,46,1024\n"
"Maaa 100,220,228\nRa 140,11,34816\ns_and_b32 s3, s11, 1\nMaaa 100,216,224\ns_lshl_b32 s3, s3, 14\nMaaa 108,216,232\nRa 144,10,40960\ns_add_u32 s2, s2, s3\nMaaa 108,220,236\n"
"s_add_u32 s2, s2, 0x400000\nMaaa 104,212,236\nRa 148,11,40960\ns_mov_b32 s55, 4096\nD0 8,32,46,2048\nMaaa 104,208,232\ns_add_u32 s75, s75, s2\nMaaa 44,200,232\nRa 152,10,43008\n"
"s_add_u32 s76, s76, s2\nMaaa 44,204,236\ns_add_u32 s77, s77, s2\nMaaa 40,196,236\nRa 156,11,43008\ns_add_u32 s78, s78, s2\nMaaa 40,192,232\nv_add_u32 v32, 0x10000, v12\n"
"D0 9,32,46,3072\nMaaa 48,192,240\nv_add_u32 v33, 0x10000, v13\nv_add_u32 v34, 0x10000, v14\nMaaa 48,196,244\nv_add_u32 v35, 0x10000, v15\nMaaa 52,204,244\n"
"v_add_u32 v36, 0x10000, v16\nv_add_u32 v37, 0x10000, v17\ns_add_u32 m0, s47, 16384\ns_add_u32 s46, s51, 786432\nMaaa 52,200,240\nv_add_u32 v38, 0x10000, v18\nMaaa 112,208,240\n"
"v_add_u32 v39, 0x10000, v19\ns_add_u32 s2, s11, 1\nD0 6,32,46,0\nMaaa 112,212,244\ns_lshr_b32 s2, s2, 1\nMaaa 116,220,244\ns_lshl_b32 s2, s2, 12\nv_add_u32 v44, s2, v10\n"
"Maaa 116,216,240\nv_add_u32 v45, s2, v11\nMaaa 124,216,248\nv_xor_b32 v46, 0x1000, v44\nv_xor_b32 v47, 0x1000, v45\nMaaa 124,220,252\ns_lshr_b32 s3, s11, 1\nD0 7,32,46,1024\n"
"Maaa 120,212,252\ns_lshl_b32 s3, s3, 13\ns_lshl_b32 s2, s11, 12\nMaaa 120,208,248\ns_add_u32 s2, s2, s3\nMaaa 60,200,248\ns_add_u32 s2, s2, s72\ns_lshl_b32 s3, s11, 12\n"
"Maaa 60,204,252\ns_add_u32 s89, s3, 0x10000\nMaaa 56,196,252\ns_mov_b32 s88, 0xffff8000\ns_lshl_b32 s4, s13, 1\nD0 8,32,46,2048\nMaaa 56,192,248\ns_waitcnt vmcnt(25) lgkmcnt(0)\n"
"s_barrier\nMaaa 0,192,160\nTa 224,16,32768\ns_sub_u32 s79, 15, s4\nMaaa 0,196,164\nTa 226,16,34816\nMaaa 4,204,164\ns_add_u32 s96, s2, 0x1310000\ns_mov_b32 s63, 0x53800000\n"
"Maaa 4,200,160\nTa 228,16,49152\nD0 9,32,46,3072\nMaaa 64,208,160\nTa 230,16,51200\ns_mov_b32 s64, 0x2b800000\nMaaa 64,212,164\ns_sub_u32 s98, s4, 1\nMaaa 68,220,164\n"
"Ta 232,17,32768\ns_lshl_b32 s5, s13, 5\ns_add_u32 m0, s47, -32768\ns_add_u32 s46, s52, 128\nMaaa 68,216,160\nTa 234,17,34816\nMaaa 76,216,168\ns_add_u32 s5, s5, s11\n"
"s_lshl_b32 s65, s5, 9\nD0 40,28,46,0\nMaaa 76,220,172\nTa 236,17,49152\nMaaa 72,212,172\nTa 238,17,51200\ns_add_u32 s65, s65, s72\nMaaa 72,208,168\ns_add_u32 s65, s65, 0x1580000\n"
"Maaa 12,200,168\nTa 240,18,32768\ns_and_b32 s66, s72, 0x2000\nMaaa 12,204,172\nTa 242,18,34816\nD0 41,28,46,1024\nMaaa 8,196,172\ns_add_u32 s66, s66, s56\n"
"s_add_u32 s66, s66, 0x100000\nMaaa 8,192,168\nTa 244,18,49152\nMaaa 16,192,176\nTa 246,18,51200\ns_and_b32 s5, s12, 1\nMaaa 16,196,180\ns_mul_i32 s58, s5, 0x202000\n"
"Maaa 20,204,180\nTa 248,19,32768\ns_xor_b32 s58, s58, s72\nD0 42,28,46,2048\nMaaa 20,200,176\nTa 250,19,34816\nMaaa 80,208,176\ns_add_u32 s58, s58, 0x15c0000\n"
"s_lshl_b32 s5, s5, 8\nMaaa 80,212,180\nTa 252,19,49152\nMaaa 84,220,180\nTa 254,19,51200\ns_add_u32 s61, s58, s5\nMaaa 84,216,176\ns_lshl4_add_u32 s60, s13, s61\nD0 43,28,46,3072\n"
"Maaa 92,216,184\ns_add_u32 s54, s60, 128\ns_xor_b32 s60, s54, 0x100\nMaaa 92,220,188\ns_cmp_eq_u32 s11, 0\ns_cselect_b32 s54, s54, 0x40000000\nMaaa 88,212,188\ns_getpc_b64 s[8:9]\n"
".PC423:\ns_add_u32 s8, s8, .HD409-.PC423\ns_addc_u32 s9, s9, 0\ns_add_u32 m0, s47, -49152\ns_add_u32 s46, s52, 256\nMaaa 88,208,184\ns_mov_b32 s97, 0xff800000\nMaaa 28,200,184\n"
"s_mov_b32 s90, 0\nD0 40,28,46,0\nMaaa 28,204,188\ns_cmp_lt_u32 s11, 2\ns_cselect_b32 s92, 2, 0\nMaaa 24,196,188\ns_mul_i32 s4, s79, 65536\ns_sub_u32 s4, s2, s4\nMaaa 24,192,184\n"
"WL 0\nMvaa 96,128,224\nTa 160,12,32768\ns_add_u32 s94, s4, 0x20000\nMvaa 96,132,228\nTa 162,12,34816\nD0 41,28,46,1024\nMvaa 100,140,228\ns_add_u32 s95, s4, 0x410000\n"
"s_add_u32 s86, s2, 0x130000\nMvaa 100,136,224\nTa 164,12,49152\nMvaa 108,136,232\nTa 166,12,51200\ns_add_u32 s87, s2, 0x520000\nMvaa 108,140,236\ns_sub_u32 s99, s79, 2\n"
"s_cmp_eq_u32 s13, 7\ns_cselect_b32 s93, 1.0, 0\nMvaa 104,132,236\nTa 168,13,32768\ns_mov_b32 s36, s68\nD0 42,28,46,2048\nMvaa 104,128,232\nTa 170,13,34816\nMvaa 112,128,240\n"
"s_and_b32 s37, s69, 0xffff\ns_mov_b32 s38, 0x1c0000\nMvaa 112,132,244\nTa 172,13,49152\nCV 208,96,97\nMvaa 116,140,244\nTa 174,13,51200\ns_mov_b32 s39, 0x20000\nMvaa 116,136,240\n"
"s_and_b32 s2, s74, 15\nD0 43,28,46,3072\nMvaa 124,136,248\nTa 176,14,32768\ns_min_u32 s2, s2, 13\nMvaa 124,140,252\nTa 178,14,34816\nMvaa 120,132,252\ns_sub_u32 s2, s2, 0\n"
"s_mul_i32 s3, s73, 14\nMvaa 120,128,248\nTa 180,14,49152\nCV 209,98,99\nMvaa 160,144,224\nTa 182,14,51200\ns_add_u32 s2, s2, s3\nMvaa 160,148,228\ns_lshl2_add_u32 s2, s2, s11\n"
"Mvaa 164,156,228\nTa 184,15,32768\ns_mul_i32 s3, s2, 4682\nMvaa 164,152,224\nTa 186,15,34816\nCV 210,100,101\nMvaa 172,152,232\ns_lshr_b32 s3, s3, 16\ns_mul_i32 s22, s3, 14\n"
"Mvaa 172,156,236\nTa 188,15,49152\nCV 211,102,103\nMvaa 168,148,236\nTa 190,15,51200\ns_sub_u32 s22, s2, s22\nMvaa 168,144,232\ns_and_b32 s21, s3, 28\nMvaa 176,144,240\n"
"s_add_u32 s21, s21, s3\ns_lshl_b32 s21, s21, 4\nMvaa 176,148,244\ns_bfe_u32 s41, s3, 0x30002\ns_cmp_gt_u32 s22, 6\ns_cselect_b32 s2, 1179648, 0\ns_cselect_b32 s1, 16, 0\n"
"Mvaa 180,156,244\ns_add_u32 s41, s41, s1\ns_lshl4_add_u32 s41, s41, 0xc048480\nMvaa 180,152,240\ns_and_b32 s1, s3, 28\nCV 212,104,105\nMvaa 188,152,248\ns_add_u32 s3, s3, s1\n"
"s_lshl_b32 s3, s3, 11\nMvaa 188,156,252\ns_add_u32 s2, s2, s3\nMvaa 184,148,252\ns_lshl_b32 s3, s22, 17\ns_add_u32 s2, s2, s3\nMvaa 184,144,248\ns_waitcnt vmcnt(16) lgkmcnt(0)\n"
"s_barrier\nMvaa 64,128,160\nRa 192,10,49152\ns_add_u32 s20, s2, 0x10e0000\ns_add_u32 m0, s47, 32768\ns_add_u32 s46, s51, 1048576\nMvaa 64,132,164\ns_lshl_b32 s2, s22, 7\n"
"Mvaa 68,140,164\nRa 196,11,49152\ns_add_u32 s40, s2, 0xc04b800\nD0 6,32,46,0\nMvaa 68,136,160\ns_mul_i32 s2, s21, 896\nCV 213,106,107\nMvaa 76,136,168\nRa 200,10,51200\n"
"s_lshl_b32 s3, s22, 6\nMvaa 76,140,172\ns_add_u32 s22, s2, s3\nCV 214,108,109\nMvaa 72,132,172\nRa 204,11,51200\ns_add_u32 s23, s22, 57344\nMvaa 72,128,168\nD0 7,32,46,1024\n"
"Mvaa 80,128,176\nRa 208,10,57344\nCV 215,110,111\nMvaa 80,132,180\nCV 216,112,113\nCV 217,114,115\nMvaa 84,140,180\nRa 212,11,57344\nMvaa 84,136,176\nCV 218,116,117\n"
"CV 219,118,119\nMvaa 92,136,184\nRa 216,10,59392\nD0 8,32,46,2048\ns_lshl3_add_u32 s5, s12, s13\ns_lshl2_add_u32 s5, s5, s11\ns_lshl2_add_u32 s5, s5, 0xc04a400\n"
"buffer_load_dword v27, off, s[16:19], s5 sc0 sc1\nMvaa 92,140,188\nCV 220,120,121\nMvaa 88,132,188\nRa 220,11,59392\nCV 221,122,123\nMvaa 88,128,184\nCV 222,124,125\n"
"CV 223,126,127\nMvaa 128,144,160\nCV 192,64,65\nCV 193,66,67\nMvaa 128,148,164\nD0 9,32,46,3072\nMvaa 132,156,164\nCV 194,68,69\nCV 195,70,71\nMvaa 132,152,160\nCV 196,72,73\n"
"CV 197,74,75\nMvaa 140,152,168\ns_add_u32 m0, s47, 49152\ns_add_u32 s46, s51, 1310720\nMvaa 140,156,172\nCV 198,76,77\nCV 199,78,79\nMvaa 136,148,172\nD0 6,32,46,0\nCV 200,80,81\n"
"Mvaa 136,144,168\nCV 201,82,83\nMvaa 144,144,176\nCV 202,84,85\nCV 203,86,87\nMvaa 144,148,180\nCV 204,88,89\nCV 205,90,91\nMvaa 148,156,180\nCV 206,92,93\nMvaa 148,152,176\n"
"D0 7,32,46,1024\nCV 207,94,95\nMvaa 156,152,184\nCV 240,160,161\nMvaa 156,156,188\nCV 241,162,163\nSv 192,1,16,75,0\nMvaa 152,148,188\nCV 242,164,165\nMvaa 152,144,184\nWL 0\n"
"Maaa 0,192,160\nD0 8,32,46,2048\nRa 128,44,0\nMaaa 0,196,164\nCV 243,166,167\nMaaa 4,204,164\nRa 132,45,0\nMaaa 4,200,160\nCV 244,168,169\nMaaa 12,200,168\nRa 136,44,2048\n"
"Maaa 12,204,172\nD0 9,32,46,3072\nMaaa 8,196,172\nRa 140,45,2048\nSv 196,1,16,75,1024\nMaaa 8,192,168\nCV 245,170,171\nMaaa 16,192,176\nRa 144,46,0\nMaaa 16,196,180\n"
"CV 246,172,173\nMaaa 20,204,180\nRa 148,47,0\nMaaa 20,200,176\nCV 247,174,175\nMaaa 28,200,184\nRa 152,46,2048\nMaaa 28,204,188\nCV 248,176,177\nMaaa 24,196,188\nRa 156,47,2048\n"
"Maaa 24,192,184\nCV 249,178,179\nSv 200,1,16,75,2048\nMaaa 64,208,160\nCV 250,180,181\nCV 251,182,183\nMaaa 64,212,164\nCV 252,184,185\nMaaa 68,220,164\nCV 253,186,187\n"
"Maaa 68,216,160\nCV 254,188,189\nMaaa 76,216,168\nCV 255,190,191\nMaaa 76,220,172\nCV 224,128,129\nMaaa 72,212,172\nCV 225,130,131\nMaaa 72,208,168\nCV 226,132,133\n"
"Maaa 80,208,176\nCV 227,134,135\nSv 204,1,16,75,3072\nMaaa 80,212,180\nCV 228,136,137\nMaaa 84,220,180\nCV 229,138,139\nMaaa 84,216,176\nCV 230,140,141\nMaaa 92,216,184\n"
"CV 231,142,143\nMaaa 92,220,188\nCV 232,144,145\nMaaa 88,212,188\nCV 233,146,147\nMaaa 88,208,184\ns_barrier\nMaaa 32,192,224\ns_add_u32 m0, s47, -16384\ns_add_u32 s46, s52, 384\n"
"Ta 160,32,0\nMaaa 32,196,228\nTa 162,32,2048\nMaaa 36,204,228\nD0 40,28,46,0\ns_add_u32 s75, s75, s55\nSv 208,1,16,75,0\nMaaa 36,200,224\nTa 164,32,16384\nMaaa 44,200,232\n"
"Ta 166,32,18432\nMaaa 44,204,236\nCV 234,148,149\nMaaa 40,196,236\nTa 168,33,0\nMaaa 40,192,232\nD0 41,28,46,1024\nTa 170,33,2048\nMaaa 48,192,240\nCV 235,150,151\n"
"Maaa 48,196,244\n.W584:\ns_cbranch_execz .W588\nTa 172,33,16384\nMaaa 52,204,244\nTa 174,33,18432\nMaaa 52,200,240\nCV 236,152,153\nSv 212,1,16,75,1024\nMaaa 60,200,248\n"
"D0 42,28,46,2048\nTa 176,34,0\nMaaa 60,204,252\nTa 178,34,2048\nMaaa 56,196,252\nCV 237,154,155\nMaaa 56,192,248\nTa 180,34,16384\nMaaa 96,208,224\nTa 182,34,18432\n"
"CV 238,156,157\nMaaa 96,212,228\nD0 43,28,46,3072\nMaaa 100,220,228\nTa 184,35,0\nMaaa 100,216,224\nTa 186,35,2048\nMaaa 108,216,232\nCV 239,158,159\nSv 216,1,16,75,2048\n"
"Maaa 108,220,236\nTa 188,35,16384\nMaaa 104,212,236\nTa 190,35,18432\nMaaa 104,208,232\nMaaa 112,208,240\nMaaa 112,212,244\nMaaa 116,220,244\nMaaa 116,216,240\nMaaa 124,216,248\n"
"Maaa 124,220,252\nSv 220,1,16,75,3072\nMaaa 120,212,252\nMaaa 120,208,248\nWL 0\nZvaa 64,160,128\nTa 224,36,0\nAR 60,0\nMvaa 64,164,132\nTa 226,36,2048\nZvaa 96,164,140\nAR 61,1\n"
"Mvaa 96,160,136\nTa 228,36,16384\nCV 192,60,61\nZvaa 128,160,144\nTa 230,36,18432\nMvaa 128,164,148\nAR 60,2\nAR 61,3\nZvaa 160,164,156\nTa 232,37,0\nSv 224,1,16,76,0\n"
"Mvaa 160,160,152\nTa 234,37,2048\nCV 193,60,61\nZvaa 164,168,152\nAR 60,4\nMvaa 164,172,156\nTa 236,37,16384\nZvaa 132,172,148\nTa 238,37,18432\nAR 61,5\nMvaa 132,168,144\n"
"CV 194,60,61\nZvaa 100,168,136\nTa 240,38,0\nAR 60,6\nMvaa 100,172,140\nTa 242,38,2048\nZvaa 68,172,132\nSv 228,1,16,76,1024\nAR 61,7\nMvaa 68,168,128\nTa 244,38,16384\n"
"Zvaa 72,176,128\nTa 246,38,18432\nCV 195,60,61\nMvaa 72,180,132\nAR 60,8\nZvaa 104,180,140\nTa 248,39,0\nMvaa 104,176,136\nTa 250,39,2048\nAR 61,9\nZvaa 136,176,144\nCV 196,60,61\n"
"Mvaa 136,180,148\nTa 252,39,16384\nAR 60,10\nZvaa 168,180,156\nTa 254,39,18432\nSv 232,1,16,76,2048\nMvaa 168,176,152\nAR 61,11\nCV 197,60,61\nZvaa 172,184,152\nAR 60,12\n"
"Mvaa 172,188,156\nAR 61,13\nZvaa 140,188,148\nCV 198,60,61\nAR 60,14\nMvaa 140,184,144\nAR 61,15\nZvaa 108,184,136\nCV 199,60,61\nAR 60,16\nMvaa 108,188,140\nSv 236,1,16,76,3072\n"
"Zvaa 76,188,132\nAR 61,17\nCV 200,60,61\nMvaa 76,184,128\ns_waitcnt vmcnt(29) lgkmcnt(0)\ns_barrier\nZvaa 80,224,128\nRa 192,44,32768\nAR 60,18\nMvaa 80,228,132\nRa 196,45,32768\n"
"s_add_u32 m0, s47, 65536\ns_add_u32 s46, s51, 1572864\nD0 6,32,46,0\nZvaa 112,228,140\nRa 200,44,34816\nMvaa 112,224,136\nRa 204,45,34816\nAR 61,19\nZvaa 144,224,144\n"
"Ra 208,46,32768\nMvaa 144,228,148\nRa 212,47,32768\nCV 201,60,61\nD0 7,32,46,1024\nZvaa 176,228,156\nRa 216,46,34816\nMvaa 176,224,152\nRa 220,47,34816\nAR 60,20\n"
"Zvaa 180,232,152\nTa 160,12,0\nMvaa 180,236,156\nTa 162,12,2048\nD0 8,32,46,2048\nZvaa 148,236,148\nTa 164,12,16384\nAR 61,21\nMvaa 148,232,144\nTa 166,12,18432\nZvaa 116,232,136\n"
"Ta 168,13,0\nCV 202,60,61\nMvaa 116,236,140\nTa 170,13,2048\nD0 9,32,46,3072\nZvaa 84,236,132\nTa 172,13,16384\nAR 60,22\nMvaa 84,232,128\nTa 174,13,18432\n"
"s_add_u32 m0, s47, 81920\ns_add_u32 s46, s51, 1835008\nZvaa 88,240,128\nTa 176,14,0\nAR 61,23\nMvaa 88,244,132\nTa 178,14,2048\nD0 6,32,46,0\nZvaa 120,244,140\nTa 180,14,16384\n"
"Mvaa 120,240,136\nTa 182,14,18432\nCV 203,60,61\nZvaa 152,240,144\nTa 184,15,0\nMvaa 152,244,148\nTa 186,15,2048\nAR 60,24\nD0 7,32,46,1024\nZvaa 184,244,156\nTa 188,15,16384\n"
"Mvaa 184,240,152\nTa 190,15,18432\nAR 61,25\nZvaa 188,248,152\nCV 204,60,61\nMvaa 188,252,156\nAR 60,26\nD0 8,32,46,2048\nZvaa 156,252,148\nAR 61,27\nCV 205,60,61\n"
"Mvaa 156,248,144\nAR 60,28\nZvaa 124,248,136\nAR 61,29\nCV 206,60,61\nMvaa 124,252,140\nAR 60,30\nD0 9,32,46,3072\nZvaa 92,252,132\nAR 61,31\nCV 207,60,61\nMvaa 92,248,128\nWL 0\n"
"Mvaa 64,160,192\nTa 224,16,0\nAR 60,32\nMvaa 64,164,196\nTa 226,16,2048\ns_add_u32 m0, s47, -65536\ns_add_u32 s46, s52, 512\nD0 40,28,46,0\nMvaa 96,164,204\nAR 61,33\n"
"Mvaa 96,160,200\nTa 228,16,16384\nCV 208,60,61\nMvaa 128,160,208\nTa 230,16,18432\nMvaa 128,164,212\nAR 60,34\nAR 61,35\nD0 41,28,46,1024\nMvaa 160,164,220\nTa 232,17,0\n"
"Mvaa 160,160,216\nTa 234,17,2048\nCV 209,60,61\nMvaa 164,168,216\nAR 60,36\nMvaa 164,172,220\nTa 236,17,16384\nD0 42,28,46,2048\nMvaa 132,172,212\nTa 238,17,18432\nAR 61,37\n"
"Mvaa 132,168,208\nCV 210,60,61\nMvaa 100,168,200\nTa 240,18,0\nAR 60,38\nMvaa 100,172,204\nTa 242,18,2048\nD0 43,28,46,3072\nMvaa 68,172,196\ns_add_u32 s76, s76, s55\n"
"Sv 240,1,16,76,0\nMvaa 68,168,192\nTa 244,18,16384\nMvaa 72,176,192\nTa 246,18,18432\nAR 61,39\nMvaa 72,180,196\nCV 211,60,61\nMvaa 104,180,204\nTa 248,19,0\nMvaa 104,176,200\n"
"Ta 250,19,2048\nSv 244,1,16,76,1024\nMvaa 136,176,208\nAR 60,40\nMvaa 136,180,212\nTa 252,19,16384\nAR 61,41\nMvaa 168,180,220\nTa 254,19,18432\nMvaa 168,176,216\nCV 212,60,61\n"
"AR 60,42\nMvaa 172,184,216\nSv 248,1,16,76,2048\nMvaa 172,188,220\nAR 61,43\nMvaa 140,188,212\nCV 213,60,61\nAR 60,44\nMvaa 140,184,208\nAR 61,45\nMvaa 108,184,200\nCV 214,60,61\n"
"AR 60,46\nMvaa 108,188,204\nSv 252,1,16,76,3072\nMvaa 76,188,196\nAR 61,47\nCV 215,60,61\nMvaa 76,184,192\ns_waitcnt vmcnt(20) lgkmcnt(0)\ns_barrier\nMvaa 80,224,192\n"
"Ra 128,44,16384\nAR 60,48\nv_cmp_ne_u32 vcc, s15, v27\ns_nop 0\ns_cbranch_vccnz .S424\n.S425:\nMvaa 80,228,196\nRa 132,45,16384\n"
"buffer_store_dword v25, off, s[16:19], s54 offset:0 sc0 sc1\ns_add_u32 m0, s47, 0\ns_add_u32 s46, s51, 2097152\nD0 6,32,46,0\nMvaa 112,228,204\nRa 136,44,18432\nMvaa 112,224,200\n"
"Ra 140,45,18432\nAR 61,49\nMvaa 144,224,208\nRa 144,46,16384\nMvaa 144,228,212\nRa 148,47,16384\nCV 216,60,61\nD0 7,32,46,1024\nMvaa 176,228,220\nRa 152,46,18432\n"
"Mvaa 176,224,216\nRa 156,47,18432\nAR 60,50\nMvaa 180,232,216\nTa 160,12,32768\nMvaa 180,236,220\nTa 162,12,34816\nD0 8,32,46,2048\nMvaa 148,236,212\nTa 164,12,49152\nAR 61,51\n"
"Mvaa 148,232,208\nTa 166,12,51200\nMvaa 116,232,200\nTa 168,13,32768\nCV 217,60,61\nMvaa 116,236,204\nTa 170,13,34816\nD0 9,32,46,3072\nMvaa 84,236,196\nTa 172,13,49152\nAR 60,52\n"
"Mvaa 84,232,192\nTa 174,13,51200\ns_add_u32 m0, s47, 16384\ns_add_u32 s46, s51, 2359296\nMvaa 88,240,192\nTa 176,14,32768\nAR 61,53\nMvaa 88,244,196\nTa 178,14,34816\n"
"D0 6,32,46,0\nMvaa 120,244,204\nTa 180,14,49152\nMvaa 120,240,200\nTa 182,14,51200\nCV 218,60,61\nMvaa 152,240,208\nTa 184,15,32768\nMvaa 152,244,212\nTa 186,15,34816\nAR 60,54\n"
"D0 7,32,46,1024\nMvaa 184,244,220\nTa 188,15,49152\nMvaa 184,240,216\nTa 190,15,51200\nAR 61,55\nMvaa 188,248,216\nCV 219,60,61\nMvaa 188,252,220\nAR 60,56\nD0 8,32,46,2048\n"
"Mvaa 156,252,212\nAR 61,57\nCV 220,60,61\nMvaa 156,248,208\nAR 60,58\nMvaa 124,248,200\nAR 61,59\nCV 221,60,61\nMvaa 124,252,204\nAR 60,60\nD0 9,32,46,3072\nMvaa 92,252,196\n"
"AR 61,61\nCV 222,60,61\nMvaa 92,248,192\nWL 0\nMvaa 64,160,128\nTa 224,16,32768\nAR 60,62\nMvaa 64,164,132\nTa 226,16,34816\ns_add_u32 m0, s47, -32768\ns_add_u32 s46, s52, 640\n"
"D0 40,28,46,0\nMvaa 96,164,140\nAR 61,63\nMvaa 96,160,136\nTa 228,16,49152\nCV 223,60,61\nMvaa 128,160,144\nTa 230,16,51200\nMvaa 128,164,148\nD0 41,28,46,1024\nMvaa 160,164,156\n"
"Ta 232,17,32768\nAR 60,126\nMvaa 160,160,152\nTa 234,17,34816\nMvaa 164,168,152\nAR 61,127\nCV 60,60,61\nMvaa 164,172,156\nTa 236,17,49152\nD0 42,28,46,2048\nMvaa 132,172,148\n"
"Ta 238,17,51200\nMvaa 132,168,144\nAW 127,60\nAR 60,124\nMvaa 100,168,136\nTa 240,18,32768\nMvaa 100,172,140\nTa 242,18,34816\nD0 43,28,46,3072\nMvaa 68,172,132\nSv 192,1,16,77,0\n"
"AR 61,125\nMvaa 68,168,128\nTa 244,18,49152\nMvaa 72,176,128\nTa 246,18,51200\nCV 60,60,61\nMvaa 72,180,132\nAW 126,60\nMvaa 104,180,140\nTa 248,19,32768\nMvaa 104,176,136\n"
"Ta 250,19,34816\nSv 196,1,16,77,1024\nMvaa 136,176,144\nAR 60,122\nMvaa 136,180,148\nTa 252,19,49152\nMvaa 168,180,156\nTa 254,19,51200\nAR 61,123\nMvaa 168,176,152\nCV 60,60,61\n"
"Mvaa 172,184,152\nSv 200,1,16,77,2048\nAW 125,60\nMvaa 172,188,156\nAR 60,120\nMvaa 140,188,148\nAR 61,121\nMvaa 140,184,144\nCV 60,60,61\nAW 124,60\nMvaa 108,184,136\nAR 60,118\n"
"Mvaa 108,188,140\nSv 204,1,16,77,3072\nMvaa 76,188,132\nAR 61,119\nCV 60,60,61\nMvaa 76,184,128\ns_waitcnt vmcnt(17) lgkmcnt(0)\ns_barrier\nMvaa 80,224,128\nRa 192,44,49152\n"
"AW 123,60\nMvaa 80,228,132\nRa 196,45,49152\nbuffer_store_dword v25, off, s[16:19], s54 offset:8 sc0 sc1\ns_add_u32 m0, s47, 32768\ns_add_u32 s46, s51, 2621440\nD0 6,32,46,0\n"
"Mvaa 112,228,140\nRa 200,44,51200\nMvaa 112,224,136\nRa 204,45,51200\nAR 60,116\nMvaa 144,224,144\nRa 208,46,49152\nMvaa 144,228,148\nRa 212,47,49152\nD0 7,32,46,1024\n"
"Mvaa 176,228,156\nRa 216,46,51200\nAR 61,117\nMvaa 176,224,152\nRa 220,47,51200\nMvaa 180,232,152\nTa 160,32,0\nCV 60,60,61\nMvaa 180,236,156\nTa 162,32,2048\nD0 8,32,46,2048\n"
"Mvaa 148,236,148\nTa 164,32,16384\nMvaa 148,232,144\nTa 166,32,18432\nAW 122,60\nMvaa 116,232,136\nTa 168,33,0\nMvaa 116,236,140\nTa 170,33,2048\nD0 9,32,46,3072\nMvaa 84,236,132\n"
"Ta 172,33,16384\nAR 60,114\nMvaa 84,232,128\n.W585:\ns_cbranch_execz .W589\nTa 174,33,18432\ns_add_u32 m0, s47, 49152\ns_add_u32 s46, s51, 2883584\nMvaa 88,240,128\nTa 176,34,0\n"
"AR 61,115\nMvaa 88,244,132\nTa 178,34,2048\nD0 6,32,46,0\nMvaa 120,244,140\nTa 180,34,16384\nMvaa 120,240,136\nTa 182,34,18432\nCV 60,60,61\nMvaa 152,240,144\nTa 184,35,0\n"
"Mvaa 152,244,148\nTa 186,35,2048\nD0 7,32,46,1024\nMvaa 184,244,156\nTa 188,35,16384\nAW 121,60\nMvaa 184,240,152\nTa 190,35,18432\nMvaa 188,248,152\nAR 60,112\nAR 61,113\n"
"Mvaa 188,252,156\nD0 8,32,46,2048\nMvaa 156,252,148\nCV 60,60,61\nMvaa 156,248,144\nAW 120,60\nAR 60,110\nMvaa 124,248,136\nAR 61,111\nMvaa 124,252,140\nD0 9,32,46,3072\n"
"Mvaa 92,252,132\nCV 60,60,61\nAW 119,60\nMvaa 92,248,128\nWL 0\nMvaa 64,160,192\nTa 224,36,0\nAR 60,108\nMvaa 64,164,196\nTa 226,36,2048\ns_add_u32 m0, s47, -49152\n"
"s_add_u32 s46, s52, 768\nD0 40,28,46,0\nMvaa 96,164,204\nAR 61,109\nMvaa 96,160,200\nTa 228,36,16384\nCV 60,60,61\nMvaa 128,160,208\nTa 230,36,18432\nMvaa 128,164,212\n"
"D0 41,28,46,1024\nMvaa 160,164,220\nTa 232,37,0\nAW 118,60\nMvaa 160,160,216\nTa 234,37,2048\nMvaa 164,168,216\nAR 60,106\nAR 61,107\nMvaa 164,172,220\nTa 236,37,16384\n"
"D0 42,28,46,2048\nMvaa 132,172,212\nTa 238,37,18432\nMvaa 132,168,208\nCV 60,60,61\nAW 117,60\nMvaa 100,168,200\nTa 240,38,0\nMvaa 100,172,204\nTa 242,38,2048\nD0 43,28,46,3072\n"
"Mvaa 68,172,196\ns_add_u32 s77, s77, s55\nSv 208,1,16,77,0\nMvaa 68,168,192\nTa 244,38,16384\nMvaa 72,176,192\nTa 246,38,18432\nAR 60,104\nMvaa 72,180,196\nAR 61,105\n"
"Mvaa 104,180,204\nTa 248,39,0\nMvaa 104,176,200\nTa 250,39,2048\nSv 212,1,16,77,1024\nMvaa 136,176,208\nCV 60,60,61\nMvaa 136,180,212\nTa 252,39,16384\nMvaa 168,180,220\n"
"Ta 254,39,18432\nAW 116,60\nMvaa 168,176,216\nAR 60,102\nMvaa 172,184,216\nSv 216,1,16,77,2048\nAR 61,103\nMvaa 172,188,220\nCV 60,60,61\nMvaa 140,188,212\nAW 115,60\n"
"Mvaa 140,184,208\nAR 60,100\nAR 61,101\nMvaa 108,184,200\nCV 60,60,61\nMvaa 108,188,204\nSv 220,1,16,77,3072\nMvaa 76,188,196\nAW 114,60\nAR 60,98\nMvaa 76,184,192\n"
"s_waitcnt vmcnt(25) lgkmcnt(0)\ns_barrier\nMvaa 80,224,192\nRa 128,44,0\nv_and_b32 v27, 0x70, v1\nbuffer_load_dword v27, v27, s[16:19], s61 offen offset:128 sc0 sc1\n"
"Mvaa 80,228,196\nRa 132,45,0\ns_add_u32 m0, s47, 65536\ns_add_u32 s46, s51, 3145728\nD0 6,32,46,0\nMvaa 112,228,204\nRa 136,44,2048\nMvaa 112,224,200\nRa 140,45,2048\nAR 61,99\n"
"Mvaa 144,224,208\nRa 144,46,0\nMvaa 144,228,212\nRa 148,47,0\nD0 7,32,46,1024\nMvaa 176,228,220\nRa 152,46,2048\nCV 60,60,61\nMvaa 176,224,216\nRa 156,47,2048\nMvaa 180,232,216\n"
"Ta 160,12,0\nAW 113,60\nMvaa 180,236,220\nTa 162,12,2048\nD0 8,32,46,2048\nMvaa 148,236,212\nTa 164,12,16384\nMvaa 148,232,208\nTa 166,12,18432\nAR 60,96\nMvaa 116,232,200\n"
"Ta 168,13,0\nMvaa 116,236,204\nTa 170,13,2048\nD0 9,32,46,3072\nMvaa 84,236,196\nTa 172,13,16384\nAR 61,97\nMvaa 84,232,192\nTa 174,13,18432\ns_add_u32 m0, s47, 81920\n"
"s_add_u32 s46, s51, 3407872\nMvaa 88,240,192\nTa 176,14,0\nCV 60,60,61\nMvaa 88,244,196\nTa 178,14,2048\nD0 6,32,46,0\nMvaa 120,244,204\nTa 180,14,16384\nMvaa 120,240,200\n"
"Ta 182,14,18432\nAW 112,60\nMvaa 152,240,208\nTa 184,15,0\nMvaa 152,244,212\nTa 186,15,2048\nD0 7,32,46,1024\nMvaa 184,244,220\nTa 188,15,16384\nAR 60,94\nMvaa 184,240,216\n"
"Ta 190,15,18432\nMvaa 188,248,216\nAR 61,95\nCV 60,60,61\nMvaa 188,252,220\nD0 8,32,46,2048\nMvaa 156,252,212\nAW 111,60\nMvaa 156,248,208\nAR 60,92\nAR 61,93\nMvaa 124,248,200\n"
"CV 60,60,61\nMvaa 124,252,204\nD0 9,32,46,3072\nMvaa 92,252,196\nAW 110,60\nAR 60,90\nMvaa 92,248,192\nWL 0\nMvaa 64,160,128\nTa 224,16,0\nAR 61,91\nMvaa 64,164,132\n"
"Ta 226,16,2048\ns_add_u32 s6, s56, 0\nbuffer_load_dwordx4 a[0:3], v1, s[16:19], s6 offen offset:0 sc1\nMvaa 96,164,140\nCV 60,60,61\nMvaa 96,160,136\nTa 228,16,16384\nAW 109,60\n"
"Mvaa 128,160,144\nTa 230,16,18432\nMvaa 128,164,148\nbuffer_load_dwordx4 a[4:7], v1, s[16:19], s6 offen offset:1024 sc1\nMvaa 160,164,156\nTa 232,17,0\nAR 60,88\nMvaa 160,160,152\n"
"Ta 234,17,2048\nMvaa 164,168,152\nAR 61,89\nCV 60,60,61\nMvaa 164,172,156\nTa 236,17,16384\nbuffer_load_dwordx4 a[8:11], v1, s[16:19], s6 offen offset:2048 sc1\nMvaa 132,172,148\n"
"Ta 238,17,18432\nMvaa 132,168,144\nAW 108,60\nAR 60,86\nMvaa 100,168,136\nTa 240,18,0\nMvaa 100,172,140\nTa 242,18,2048\n"
"buffer_load_dwordx4 a[12:15], v1, s[16:19], s6 offen offset:3072 sc1\nMvaa 68,172,132\nAR 61,87\nCV 60,60,61\nMvaa 68,168,128\nTa 244,18,16384\nMvaa 72,176,128\nTa 246,18,18432\n"
"AW 107,60\nMvaa 72,180,132\nAR 60,84\nMvaa 104,180,140\nTa 248,19,0\nMvaa 104,176,136\nTa 250,19,2048\nAR 61,85\nMvaa 136,176,144\nCV 60,60,61\nMvaa 136,180,148\nTa 252,19,16384\n"
"Mvaa 168,180,156\nTa 254,19,18432\nAW 106,60\nMvaa 168,176,152\nAR 60,82\nMvaa 172,184,152\nAR 61,83\nCV 60,60,61\nMvaa 172,188,156\nAW 105,60\nMvaa 140,188,148\nAR 60,80\n"
"Mvaa 140,184,144\nAR 61,81\nCV 60,60,61\nMvaa 108,184,136\nAW 104,60\nMvaa 108,188,140\nAR 60,78\nMvaa 76,188,132\nAR 61,79\nCV 60,60,61\nMvaa 76,184,128\n"
"s_waitcnt vmcnt(12) lgkmcnt(0)\ns_barrier\nMvaa 80,224,128\nRa 192,44,32768\nv_cmp_ne_u32 vcc, s15, v27\ns_nop 0\ns_cbranch_vccnz .S427\n.S428:\ns_sub_u32 s45, s86, 131072\n"
"s_cmp_eq_u32 s79, 1\ns_cselect_b32 s86, s94, s86\ns_add_u32 m0, s47, -65536\ns_add_u32 s46, s45, 0x0\nMvaa 80,228,132\nRa 196,45,32768\nMvaa 112,228,140\nRa 200,44,34816\n"
"D1 1,16,46,0\nMvaa 112,224,136\nRa 204,45,34816\nAW 103,60\nMvaa 144,224,144\nRa 208,46,32768\nMvaa 144,228,148\nRa 212,47,32768\nMvaa 176,228,156\nRa 216,46,34816\n"
"D1 1,16,46,1024\nMvaa 176,224,152\nRa 220,47,34816\nMvaa 180,232,152\nTa 160,12,32768\nAR 60,76\nMvaa 180,236,156\nTa 162,12,34816\nMvaa 148,236,148\nTa 164,12,49152\n"
"D1 1,16,46,2048\nMvaa 148,232,144\nTa 166,12,51200\nAR 61,77\nMvaa 116,232,136\nTa 168,13,32768\nMvaa 116,236,140\nTa 170,13,34816\nMvaa 84,236,132\nTa 172,13,49152\n"
"D1 1,16,46,3072\nMvaa 84,232,128\nTa 174,13,51200\nMvaa 88,240,128\nTa 176,14,32768\ns_add_u32 s6, s56, 4096\nMvaa 88,244,132\nTa 178,14,34816\nMvaa 120,244,140\nTa 180,14,49152\n"
"buffer_load_dwordx4 a[16:19], v1, s[16:19], s6 offen offset:0 sc1\nMvaa 120,240,136\nTa 182,14,51200\nCV 60,60,61\nMvaa 152,240,144\nTa 184,15,32768\nMvaa 152,244,148\n"
"Ta 186,15,34816\nMvaa 184,244,156\nTa 188,15,49152\nbuffer_load_dwordx4 a[20:23], v1, s[16:19], s6 offen offset:1024 sc1\nMvaa 184,240,152\nTa 190,15,51200\nMvaa 188,248,152\n"
"AW 102,60\nAR 60,74\nMvaa 188,252,156\nAR 61,75\nMvaa 156,252,148\nbuffer_load_dwordx4 a[24:27], v1, s[16:19], s6 offen offset:2048 sc1\nMvaa 156,248,144\nCV 60,60,61\nAW 101,60\n"
"Mvaa 124,248,136\nAR 60,72\nMvaa 124,252,140\nAR 61,73\nMvaa 92,252,132\nbuffer_load_dwordx4 a[28:31], v1, s[16:19], s6 offen offset:3072 sc1\nCV 60,60,61\nMvaa 92,248,128\nWL 0\n"
"Mvaa 64,160,192\nTa 224,16,32768\ns_add_u32 s6, s56, 16384\nMvaa 64,164,196\nTa 226,16,34816\nMvaa 96,164,204\nbuffer_load_dwordx4 a[32:35], v1, s[16:19], s6 offen offset:0 sc1\n"
"Mvaa 96,160,200\nTa 228,16,49152\nAW 100,60\nMvaa 128,160,208\nTa 230,16,51200\nMvaa 128,164,212\nAR 60,70\nMvaa 160,164,220\nTa 232,17,32768\n"
"buffer_load_dwordx4 a[36:39], v1, s[16:19], s6 offen offset:1024 sc1\nMvaa 160,160,216\nTa 234,17,34816\nMvaa 164,168,216\nAR 61,71\nCV 60,60,61\nMvaa 164,172,220\n"
"Ta 236,17,49152\nMvaa 132,172,212\nTa 238,17,51200\nbuffer_load_dwordx4 a[40:43], v1, s[16:19], s6 offen offset:2048 sc1\nMvaa 132,168,208\nAW 99,60\nAR 60,68\nMvaa 100,168,200\n"
"Ta 240,18,32768\nMvaa 100,172,204\nTa 242,18,34816\nMvaa 68,172,196\nbuffer_load_dwordx4 a[44:47], v1, s[16:19], s6 offen offset:3072 sc1\nAR 61,69\nMvaa 68,168,192\n"
"Ta 244,18,49152\nMvaa 72,176,192\nTa 246,18,51200\nCV 60,60,61\nMvaa 72,180,196\nAW 98,60\nMvaa 104,180,204\nTa 248,19,32768\nMvaa 104,176,200\nTa 250,19,34816\nAR 60,66\n"
"Mvaa 136,176,208\nAR 61,67\nMvaa 136,180,212\nTa 252,19,49152\nMvaa 168,180,220\nTa 254,19,51200\nCV 60,60,61\nMvaa 168,176,216\nAW 97,60\nMvaa 172,184,216\nAR 60,64\nAR 61,65\n"
"Mvaa 172,188,220\nCV 60,60,61\nMvaa 140,188,212\nAW 96,60\nv_add_u32 v2, 0x0, v1\nMvaa 140,184,208\ns_lshr_b32 s2, s59, 1\nMvaa 108,184,200\nv_add_u32 v24, s2, v2\n"
"v_xor_b32 v26, 0x1000, v24\nMvaa 108,188,204\nv_lshrrev_b32 v20, 4, v0\nMvaa 76,188,196\nv_lshlrev_b32 v20, 2, v20\nv_and_b32 v0, 15, v0\nMvaa 76,184,192\n"
"s_waitcnt vmcnt(16) lgkmcnt(0)\ns_barrier\nMvaa 80,224,192\nRa 128,44,16384\nv_sub_u32 v0, v0, v20\nMvaa 80,228,196\nRa 132,45,16384\n"
"buffer_store_dword v25, off, s[16:19], s54 offset:12 sc0 sc1\ns_add_u32 s6, s56, 20480\nbuffer_load_dwordx4 a[48:51], v1, s[16:19], s6 offen offset:0 sc1\nMvaa 112,228,204\n"
"Ra 136,44,18432\ns_lshl_b32 s2, s11, 4\nMvaa 112,224,200\nRa 140,45,18432\nMvaa 144,224,208\nRa 144,46,16384\nv_add_u32 v3, s2, v0\nMvaa 144,228,212\nRa 148,47,16384\n"
"buffer_load_dwordx4 a[52:55], v1, s[16:19], s6 offen offset:1024 sc1\nMvaa 176,228,220\nRa 152,46,18432\nv_subrev_u32 v0, 32, v3\nMvaa 176,224,216\nRa 156,47,18432\n"
"Mvaa 180,232,216\nTa 160,32,0\ns_lshl_b32 s2, s11, 13\nMvaa 180,236,220\nTa 162,32,2048\nbuffer_load_dwordx4 a[56:59], v1, s[16:19], s6 offen offset:2048 sc1\nMvaa 148,236,212\n"
"Ta 164,32,16384\ns_add_u32 s2, s2, 0x20000\nMvaa 148,232,208\nTa 166,32,18432\nMvaa 116,232,200\nTa 168,33,0\nv_add_u32 v22, s2, v1\nMvaa 116,236,204\nTa 170,33,2048\n"
"buffer_load_dwordx4 a[60:63], v1, s[16:19], s6 offen offset:3072 sc1\nMvaa 84,236,196\nTa 172,33,16384\nv_xor_b32 v23, 0x2000, v22\nMvaa 84,232,192\nTa 174,33,18432\n"
"s_add_u32 m0, s47, -32768\ns_add_u32 s46, s45, 0x8000\nMvaa 88,240,192\nTa 176,34,0\nv_mov_b32 v4, 0\nMvaa 88,244,196\nTa 178,34,2048\nD1 1,16,46,0\nMvaa 120,244,204\n"
"Ta 180,34,16384\nv_mov_b32 v5, 0\nMvaa 120,240,200\nTa 182,34,18432\nMvaa 152,240,208\nTa 184,35,0\nv_mov_b32 v6, 0\nMvaa 152,244,212\nTa 186,35,2048\nD1 1,16,46,1024\n"
"Mvaa 184,244,220\nTa 188,35,16384\nv_mov_b32 v7, 0\nMvaa 184,240,216\nTa 190,35,18432\n.W586:\ns_cbranch_execz .W590\nMvaa 188,248,216\nv_mov_b32 v8, 0\nv_mov_b32 v9, 0\n"
"Mvaa 188,252,220\nD1 1,16,46,2048\nMvaa 156,252,212\nv_mov_b32 v10, 0\nv_mov_b32 v11, 0\nMvaa 156,248,208\nv_mov_b32 v12, 0\nMvaa 124,248,200\nv_mov_b32 v13, 0\nv_mov_b32 v14, 0\n"
"Mvaa 124,252,204\nD1 1,16,46,3072\nMvaa 92,252,196\nv_mov_b32 v15, 0\nv_mov_b32 v16, 0\nMvaa 92,248,192\nWL 0\nMvaa 64,160,128\nTa 224,36,0\nv_mov_b32 v17, 0\nMvaa 64,164,132\n"
"Ta 226,36,2048\ns_add_u32 m0, s47, -16384\ns_add_u32 s46, s45, 0x400000\nD1 1,16,46,0\nMvaa 68,172,132\nv_mov_b32 v18, 0\nv_mov_b32 v19, 0\nMvaa 68,168,128\nTa 228,36,16384\n"
"Mvaa 72,176,128\nTa 230,36,18432\nMvaa 72,180,132\nD1 1,16,46,1024\nMvaa 76,188,132\nTa 232,37,0\nMvaa 76,184,128\nTa 234,37,2048\nMvaa 108,184,136\nv_mul_f32 v64, s57, v64\n"
"v_mul_f32 v65, s57, v65\nMvaa 108,188,140\nTa 236,37,16384\nD1 1,16,46,2048\nMvaa 104,180,140\nTa 238,37,18432\nCV 64,64,65\nMvaa 104,176,136\nv_mul_f32 v66, s57, v66\n"
"v_mul_f32 v67, s57, v67\nMvaa 100,168,136\nTa 240,38,0\nCV 65,66,67\nMvaa 100,172,140\nTa 242,38,2048\nD1 1,16,46,3072\nMvaa 96,164,140\nv_mul_f32 v68, s57, v68\n"
"v_mul_f32 v69, s57, v69\nMvaa 96,160,136\nTa 244,38,16384\nCV 66,68,69\nMvaa 128,160,144\nTa 246,38,18432\nv_mul_f32 v70, s57, v70\nMvaa 128,164,148\nv_mul_f32 v71, s57, v71\n"
"CV 67,70,71\nMvaa 132,172,148\nTa 248,39,0\nAW 192,64\nMvaa 132,168,144\nTa 250,39,2048\nAW 193,65\nMvaa 136,176,144\nAW 194,66\nAW 195,67\nMvaa 136,180,148\nTa 252,39,16384\n"
"v_mul_f32 v72, s57, v72\nMvaa 140,188,148\nTa 254,39,18432\nv_mul_f32 v73, s57, v73\nMvaa 140,184,144\nCV 72,72,73\nv_mul_f32 v74, s57, v74\nMvaa 172,184,152\n"
"v_mul_f32 v75, s57, v75\nCV 73,74,75\nMvaa 172,188,156\nv_mul_f32 v76, s57, v76\nv_mul_f32 v77, s57, v77\nMvaa 168,180,156\nCV 74,76,77\nv_mul_f32 v78, s57, v78\nMvaa 168,176,152\n"
"v_mul_f32 v79, s57, v79\nCV 75,78,79\nMvaa 164,168,152\nAW 200,72\nAW 201,73\nMvaa 164,172,156\nAW 202,74\nAW 203,75\nMvaa 160,164,156\nv_mul_f32 v96, s57, v96\n"
"v_mul_f32 v97, s57, v97\nMvaa 160,160,152\ns_waitcnt vmcnt(21) lgkmcnt(0)\ns_barrier\nMvaa 80,224,128\nCV 96,96,97\nv_mul_f32 v98, s57, v98\nMvaa 80,228,132\ns_add_u32 m0, s47, 0\n"
"s_add_u32 s46, s86, 0xffff0000\nD1 1,16,46,0\nMvaa 84,236,132\nv_mul_f32 v99, s57, v99\nCV 97,98,99\nMvaa 84,232,128\nv_mul_f32 v100, s57, v100\nv_mul_f32 v101, s57, v101\n"
"Mvaa 88,240,128\nCV 98,100,101\nv_mul_f32 v102, s57, v102\nMvaa 88,244,132\nD1 1,16,46,1024\nv_mul_f32 v103, s57, v103\nMvaa 92,252,132\nCV 99,102,103\nAW 196,96\nMvaa 92,248,128\n"
"AW 197,97\nAW 198,98\nMvaa 124,248,136\nAW 199,99\nv_mul_f32 v128, s57, v128\nMvaa 124,252,140\nD1 1,16,46,2048\nv_mul_f32 v129, s57, v129\nMvaa 120,244,140\nCV 128,128,129\n"
"v_mul_f32 v130, s57, v130\nMvaa 120,240,136\nv_mul_f32 v131, s57, v131\nCV 129,130,131\nMvaa 116,232,136\nRa 160,24,0\nv_mul_f32 v132, s57, v132\nMvaa 116,236,140\n"
"D1 1,16,46,3072\nv_mul_f32 v133, s57, v133\nMvaa 112,228,140\nRa 164,24,1024\nCV 130,132,133\nMvaa 112,224,136\ns_add_u32 m0, s47, 16384\ns_add_u32 s46, s45, 0x408000\n"
"Mvaa 144,224,144\nRa 168,24,2048\nv_mul_f32 v134, s57, v134\nMvaa 144,228,148\nD1 1,16,46,0\nv_mul_f32 v135, s57, v135\nMvaa 148,236,148\nRa 172,24,3072\nCV 131,134,135\n"
"Mvaa 148,232,144\nds_write_b128 v23, v[128:131] offset:0\nv_mul_f32 v80, s57, v80\nMvaa 152,240,144\nRa 176,26,0\nv_mul_f32 v81, s57, v81\nMvaa 152,244,148\nD1 1,16,46,1024\n"
"CV 80,80,81\nMvaa 156,252,148\nRa 180,26,1024\nv_mul_f32 v82, s57, v82\nMvaa 156,248,144\nv_mul_f32 v83, s57, v83\nCV 81,82,83\nMvaa 188,248,152\nRa 184,26,2048\n"
"v_mul_f32 v84, s57, v84\nMvaa 188,252,156\nD1 1,16,46,2048\nv_mul_f32 v85, s57, v85\nMvaa 184,244,156\nRa 188,26,3072\nCV 82,84,85\nMvaa 184,240,152\nv_mul_f32 v86, s57, v86\n"
"v_mul_f32 v87, s57, v87\nMvaa 180,232,152\nCV 83,86,87\nAW 208,80\nMvaa 180,236,156\nD1 1,16,46,3072\nAW 209,81\nMvaa 176,228,156\nAW 210,82\nAW 211,83\nMvaa 176,224,152\n"
"s_waitcnt vmcnt(25) lgkmcnt(5)\ns_cmp_eq_u32 s79, 1\ns_cbranch_scc0 .E430\nv_mov_b32 v4, s97\nv_mov_b32 v5, s97\nv_mov_b32 v6, s97\nv_mov_b32 v7, s97\nv_mov_b32 v12, s97\n"
"v_mov_b32 v13, s97\nv_mov_b32 v14, s97\nv_mov_b32 v15, s97\nv_cmpx_gt_i32 vcc, 19, v3\nv_mov_b32 v19, s97\nv_cmpx_gt_i32 vcc, 18, v3\nv_mov_b32 v18, s97\n"
"v_cmpx_gt_i32 vcc, 17, v3\nv_mov_b32 v17, s97\nv_cmpx_gt_i32 vcc, 16, v3\nv_mov_b32 v16, s97\nv_cmpx_gt_i32 vcc, 3, v3\nv_mov_b32 v11, s97\nv_cmpx_gt_i32 vcc, 2, v3\n"
"v_mov_b32 v10, s97\nv_cmpx_gt_i32 vcc, 1, v3\nv_mov_b32 v9, s97\nv_cmpx_gt_i32 vcc, 0, v3\nv_mov_b32 v8, s97\ns_mov_b64 exec, -1\ns_nop 2\n.E430:\ns_nop 0\nCvaa 32,160,0,4\n"
"Ra 128,24,8192\nv_mul_f32 v88, s57, v88\nMvaa 32,164,8\nv_mul_f32 v89, s57, v89\nCV 88,88,89\nMvaa 32,168,16\nRa 132,24,9216\nv_mul_f32 v90, s57, v90\nMvaa 32,172,24\n"
"v_mul_f32 v91, s57, v91\nCV 89,90,91\nCvaa 36,172,28,8\nRa 136,24,10240\nv_mul_f32 v92, s57, v92\nMvaa 36,168,20\ns_add_u32 m0, s89, 32768\nv_mul_f32 v93, s57, v93\n"
"Mvaa 36,164,12\nRa 140,24,11264\ns_waitcnt vmcnt(16) lgkmcnt(4)\ns_barrier\nMvaa 36,160,4\nCV 90,92,93\nv_mul_f32 v94, s57, v94\nMvaa 36,176,36\nRa 144,26,8192\nSa 96,1,16,78,0\n"
"Mvaa 36,180,44\ns_add_u32 s46, s86, s88\nv_mul_f32 v95, s57, v95\nMvaa 36,184,52\nRa 148,26,9216\nCV 91,94,95\nMvaa 36,188,60\nAW 216,88\nAW 217,89\nMvaa 32,188,56\n"
"Ra 152,26,10240\nAW 218,90\nMvaa 32,184,48\nD1 1,16,46,0\nAW 219,91\nMvaa 32,180,40\nRa 156,26,11264\nWL 4\nMvaa 32,176,32\nv_mov_b32 v86, 0\nv_mov_b32 v87, 0\nCvaa 40,128,0,12\n"
"Sa 100,1,16,78,1024\nv_mov_b32 v88, 0\nMvaa 40,132,8\ns_cmp_eq_u32 s99, -1\nMvaa 40,136,16\ns_cselect_b32 s87, s95, s87\nv_mov_b32 v89, 0\nv_mul_f32 v136, s57, v136\n"
"v_mul_f32 v137, s57, v137\nCV 136,136,137\nv_mul_f32 v138, s57, v138\nv_mul_f32 v139, s57, v139\nMvaa 40,140,24\nCV 137,138,139\nv_mul_f32 v140, s57, v140\n"
"v_mul_f32 v141, s57, v141\nCV 138,140,141\nv_mul_f32 v142, s57, v142\nv_mul_f32 v143, s57, v143\nCvaa 44,140,28,16\nv_and_b32 v27, 0x70, v1\n"
"buffer_load_dword v27, v27, s[16:19], s61 offen offset:136 sc0 sc1\nCV 139,142,143\nds_write_b128 v23, v[136:139] offset:2048\nv_mul_f32 v144, s57, v144\n"
"v_mul_f32 v145, s57, v145\nCV 144,144,145\nMvaa 44,136,20\nD1 1,16,46,1024\nv_mul_f32 v146, s57, v146\nv_mul_f32 v147, s57, v147\nCV 145,146,147\nv_mul_f32 v148, s57, v148\n"
"v_mul_f32 v149, s57, v149\nMvaa 44,132,12\nWL 1\nCV 146,148,149\nv_mul_f32 v150, s57, v150\nv_mul_f32 v151, s57, v151\nCV 147,150,151\nds_write_b128 v23, v[144:147] offset:4096\n"
"v_mul_f32 v152, s57, v152\nMvaa 44,128,4\nv_mul_f32 v153, s57, v153\nCV 152,152,153\nv_mul_f32 v154, s57, v154\nv_mul_f32 v155, s57, v155\nCV 153,154,155\n"
"v_mul_f32 v156, s57, v156\nMvaa 44,144,36\nSa 104,1,16,78,2048\nv_mul_f32 v157, s57, v157\nCV 154,156,157\nv_mul_f32 v158, s57, v158\nv_mul_f32 v159, s57, v159\nCV 155,158,159\n"
"ds_write_b128 v23, v[152:155] offset:6144\nMvaa 44,148,44\ns_add_u32 s88, s88, 32768\nEX 80,32\nv_mul_f32 v160, s57, v160\nv_mul_f32 v161, s57, v161\nCV 160,160,161\n"
"Mvaa 44,152,52\nv_mul_f32 v162, s57, v162\nv_mul_f32 v163, s57, v163\nCV 161,162,163\nv_mul_f32 v164, s57, v164\nv_mul_f32 v165, s57, v165\nCV 162,164,165\n"
"v_mul_f32 v166, s57, v166\nMvaa 44,156,60\nEX 81,33\nv_mul_f32 v167, s57, v167\nCV 163,166,167\nds_write_b128 v23, v[160:163] offset:1024\nMvaa 40,156,56\n"
"v_mul_f32 v168, s57, v168\nv_mul_f32 v169, s57, v169\nCV 168,168,169\nv_mul_f32 v170, s57, v170\nv_mul_f32 v171, s57, v171\nCV 169,170,171\nv_mul_f32 v172, s57, v172\n"
"Mvaa 40,152,48\nD1 1,16,46,2048\nv_mul_f32 v173, s57, v173\nCV 170,172,173\nv_mul_f32 v174, s57, v174\nv_mul_f32 v175, s57, v175\nCV 171,174,175\nMvaa 40,148,40\n"
"ds_write_b128 v23, v[168:171] offset:3072\nv_mul_f32 v176, s57, v176\nv_mul_f32 v177, s57, v177\nCV 176,176,177\nv_mul_f32 v178, s57, v178\nv_mul_f32 v179, s57, v179\n"
"CV 177,178,179\nMvaa 40,144,32\nAD 84,80,81\nCV 64,80,81\nv_mul_f32 v180, s57, v180\nv_mul_f32 v181, s57, v181\nCV 178,180,181\nv_mul_f32 v182, s57, v182\nSa 108,1,16,78,3072\n"
"v_mul_f32 v183, s57, v183\nCV 179,182,183\nds_write_b128 v23, v[176:179] offset:5120\nv_mul_f32 v184, s57, v184\nv_mul_f32 v185, s57, v185\nCV 184,184,185\nEX 82,34\n"
"v_mul_f32 v186, s57, v186\nv_mul_f32 v187, s57, v187\nCV 185,186,187\nv_mul_f32 v188, s57, v188\nv_mul_f32 v189, s57, v189\nCV 186,188,189\nv_mul_f32 v190, s57, v190\n"
"v_mul_f32 v191, s57, v191\nCV 187,190,191\nds_write_b128 v23, v[184:187] offset:7168\ns_waitcnt vmcnt(16) lgkmcnt(0)\nEX 83,35\nAD 84,84,82\nD1 1,16,46,3072\n"
"s_add_u32 m0, s89, 49152\ns_barrier\nAD 84,84,83\nCV 65,82,83\nRa 128,24,32768\nRa 224,22,0\nRa 228,22,1024\ns_add_u32 s46, s87, s88\nEX 80,36\nRa 132,24,33792\nRa 232,22,2048\n"
"Ra 236,22,3072\nRa 240,22,4096\nEX 81,37\nRa 136,24,34816\nRa 244,22,5120\nRa 248,22,6144\nD1 1,16,46,0\nRa 252,22,7168\nv_mul_f32 v104, s57, v104\nv_mul_f32 v105, s57, v105\n"
"Ra 140,24,35840\nCV 104,104,105\nv_mul_f32 v106, s57, v106\nAD 85,80,81\nCV 68,80,81\nv_mul_f32 v107, s57, v107\nRa 144,26,32768\nCV 105,106,107\nv_mul_f32 v108, s57, v108\n"
"v_mul_f32 v109, s57, v109\ns_cmp_eq_u32 s13, 7\nEX 82,38\nRa 148,26,33792\ns_cbranch_scc0 .E417\nv_cmpx_gt_i32 vcc, 19, v0\nv_mov_b32 v19, s97\nv_cmpx_gt_i32 vcc, 18, v0\n"
"v_mov_b32 v18, s97\nv_cmpx_gt_i32 vcc, 17, v0\nv_mov_b32 v17, s97\nv_cmpx_gt_i32 vcc, 16, v0\nv_mov_b32 v16, s97\nv_cmpx_gt_i32 vcc, 3, v0\nv_mov_b32 v11, s97\n"
"v_cmpx_gt_i32 vcc, 2, v0\nv_mov_b32 v10, s97\nv_cmpx_gt_i32 vcc, 1, v0\nv_mov_b32 v9, s97\nv_cmpx_gt_i32 vcc, 0, v0\nv_mov_b32 v8, s97\ns_mov_b64 exec, -1\ns_nop 2\n.E417:\n"
"EX 83,39\nRa 152,26,34816\nAD 85,85,82\nCV 106,108,109\nv_mul_f32 v110, s57, v110\nD1 1,16,46,1024\nv_mul_f32 v111, s57, v111\nCV 107,110,111\nRa 156,26,35840\nAW 204,104\n"
"AW 205,105\nAD 85,85,83\nCV 69,82,83\nAW 206,106\nAW 207,107\nRa 160,24,40960\nv_mul_f32 v112, s57, v112\nv_mul_f32 v113, s57, v113\ns_xor_b32 s89, s89, 0x10000\nEX 80,40\n"
"Ra 164,24,41984\nCV 112,112,113\nv_mul_f32 v114, s57, v114\nv_mul_f32 v115, s57, v115\nWL 6\nEX 81,41\nRa 168,24,43008\nAD 84,84,80\nCV 113,114,115\nD1 1,16,46,2048\n"
"v_mul_f32 v116, s57, v116\nv_mul_f32 v117, s57, v117\nCV 114,116,117\nRa 172,24,44032\nv_mul_f32 v118, s57, v118\nv_mul_f32 v119, s57, v119\nAD 84,84,81\nCV 66,80,81\n"
"CV 115,118,119\nCvaa 48,128,0,4\nRa 176,26,40960\nAW 212,112\nAW 213,113\nAW 214,114\nMvaa 48,132,8\nEX 82,42\nAW 215,115\nMvaa 48,136,16\nRa 180,26,41984\n"
"v_mul_f32 v120, s57, v120\nv_mul_f32 v121, s57, v121\nMvaa 48,140,24\nWL 6\nEX 83,43\nWV 8\nv_cmp_ne_u32 vcc, s15, v27\ns_nop 0\ns_cbranch_vccnz .S431\n.S432:\nCvaa 52,140,28,8\n"
"Ra 184,26,43008\nAD 84,84,82\nCV 120,120,121\nv_mul_f32 v122, s57, v122\nMvaa 52,136,20\nD1 1,16,46,3072\ns_add_u32 m0, s89, 0\nv_mul_f32 v123, s57, v123\nMvaa 52,132,12\n"
"Ra 188,26,44032\nCV 121,122,123\nv_mul_f32 v124, s57, v124\nMvaa 52,128,4\nAD 84,84,83\nCV 67,82,83\nv_mul_f32 v125, s57, v125\nCV 122,124,125\nMvaa 52,144,36\nRa 128,2,49152\n"
"s_add_u32 s78, s78, s55\nSa 112,1,16,78,0\nMvaa 52,148,44\ns_add_u32 s46, s86, s88\nEX 80,44\nMvaa 52,152,52\nRa 132,2,50176\nv_xor_b32 v24, 0x10000, v24\n"
"v_mul_f32 v126, s57, v126\nv_mul_f32 v127, s57, v127\nMvaa 52,156,60\nWL 6\nEX 81,45\nMvaa 48,156,56\nRa 136,2,51200\nAD 85,85,80\nCV 123,126,127\nMvaa 48,152,48\nD1 1,16,46,0\n"
"AW 220,120\nAW 221,121\nAW 222,122\nMvaa 48,148,40\nRa 140,2,52224\nAW 223,123\nv_mov_b32 v115, -1\nMvaa 48,144,32\nAD 85,85,81\nCV 70,80,81\nCvaa 56,160,0,12\nRa 144,2,53248\n"
"Sa 116,1,16,78,1024\nMvaa 56,164,8\nEX 82,46\nMvaa 56,168,16\nRa 148,2,54272\nv_xor_b32 v26, 0x10000, v26\nMvaa 56,172,24\nWL 6\nEX 83,47\nCvaa 60,172,28,16\nRa 152,2,55296\n"
"AD 85,85,82\nMvaa 60,168,20\nD1 1,16,46,1024\nMvaa 60,164,12\nRa 156,2,56320\nMvaa 60,160,4\nAD 85,85,83\nCV 71,82,83\nMvaa 60,176,36\nRa 160,2,57344\nv_max_f32 v90, v84, v85\n"
"Mvaa 60,180,44\ns_add_u32 s88, s88, 32768\nEX 80,48\nMvaa 60,184,52\nRa 164,2,58368\nv_cmp_lt_f32 vcc, s63, v90\nMvaa 60,188,60\nWL 8\nEX 81,49\nMvaa 56,188,56\nRa 168,2,59392\n"
"s_cbranch_vccnz .T419\n.T420:\nv_add_f32 v20, s93, v84\nv_mov_b32 v21, v85\nv_mov_b32 v22, v20\nv_mov_b32 v23, v21\nMvaa 56,184,48\nD1 1,16,46,2048\n"
"v_permlane32_swap_b32 v20, v22\nv_permlane32_swap_b32 v21, v23\nAD 20,20,22\nAD 21,21,23\nv_mov_b32 v22, v20\nv_mov_b32 v23, v21\nMvaa 56,180,40\nRa 172,2,60416\n"
"v_permlane16_swap_b32 v20, v22\nv_permlane16_swap_b32 v21, v23\nAD 20,20,22\nAD 21,21,23\nv_min_f32 v90, v20, v21\nv_cmp_gt_f32 vcc, s64, v90\ns_nop 0\ns_cbranch_vccnz .T421\n"
".T422:\nAD 86,86,84\nMvaa 56,176,32\nAD 84,80,81\nCV 72,80,81\nZvav 128,128,64\nRa 176,2,61440\nSa 120,1,16,78,2048\nZvav 132,128,68\nEX 82,50\nZvav 140,132,68\nRa 180,2,62464\n"
"Zvav 136,132,64\nWL 8\nEX 83,51\nZvav 144,136,64\nRa 184,2,63488\nAD 84,84,82\nZvav 148,136,68\n.W587:\ns_cbranch_execz .W591\nD1 1,16,46,3072\ns_add_u32 m0, s89, 16384\n"
"Zvav 156,140,68\nRa 188,2,64512\ns_barrier\nZvav 152,140,64\nAD 84,84,83\nCV 73,82,83\nZvav 160,144,64\nRa 128,24,0\nSa 124,1,16,78,3072\nZvav 164,144,68\ns_add_u32 s46, s87, s88\n"
"EX 80,52\nZvav 172,148,68\nRa 132,24,1024\nv_xor_b32 v2, 0x10000, v2\nZvav 168,148,64\nWL 8\nEX 81,53\nZvav 176,152,64\nRa 136,24,2048\nAD 87,87,85\nZvav 180,152,68\nD1 1,16,46,0\n"
"Zvav 188,156,68\nRa 140,24,3072\nZvav 184,156,64\nAD 85,80,81\nCV 76,80,81\nZvav 192,160,64\nRa 144,26,0\nZvav 196,160,68\nEX 82,54\nZvav 204,164,68\nRa 148,26,1024\n"
"Zvav 200,164,64\nWL 8\nEX 83,55\nZvav 208,168,64\nRa 152,26,2048\nAD 85,85,82\nZvav 212,168,68\nD1 1,16,46,1024\nZvav 220,172,68\nRa 156,26,3072\nZvav 216,172,64\nAD 85,85,83\n"
"CV 77,82,83\nZvav 224,176,64\nRa 160,24,8192\nZvav 228,176,68\nEX 80,56\nZvav 236,180,68\nRa 164,24,9216\nZvav 232,180,64\nWL 6\nEX 81,57\nZvav 240,184,64\nRa 168,24,10240\n"
"AD 84,84,80\nv_and_b32 v27, 0x70, v1\nbuffer_load_dword v27, v27, s[16:19], s61 offen offset:140 sc0 sc1\nZvav 244,184,68\nD1 1,16,46,2048\nZvav 252,188,68\nRa 172,24,11264\n"
"Zvav 248,188,64\nAD 84,84,81\nCV 74,80,81\ns_sub_u32 s99, s99, 1\ns_cmp_eq_u32 s13, 7\ns_cbranch_scc1 .SW415\n.p2align 6\n.A446:\nCvaa 32,128,0,4\nRa 176,26,8192\nMvaa 32,132,8\n"
"s_add_u32 s49, s49, 131072\nEX 82,58\nMvaa 32,136,16\nRa 180,26,9216\nMvaa 32,140,24\nWL 6\nEX 83,59\nCvaa 36,140,28,8\nRa 184,26,10240\nAD 84,84,82\nMvaa 36,136,20\n"
"D1 1,16,46,3072\ns_add_u32 m0, s89, 32768\nMvaa 36,132,12\nRa 188,26,11264\nMvaa 36,128,4\nAD 84,84,83\nCV 75,82,83\nMvaa 36,144,36\nRa 128,2,16384\nMvaa 36,148,44\n"
"s_add_u32 s46, s86, s88\nEX 80,60\nMvaa 36,152,52\nRa 132,2,17408\nMvaa 36,156,60\nWL 6\nEX 81,61\nMvaa 32,156,56\nRa 136,2,18432\nAD 85,85,80\nMvaa 32,152,48\nD1 1,16,46,0\n"
"Mvaa 32,148,40\nRa 140,2,19456\nMvaa 32,144,32\nAD 85,85,81\nCV 78,80,81\nCvaa 40,160,0,12\nRa 144,2,20480\nMvaa 40,164,8\nSv 112,1,16,49,0\nEX 82,62\nMvaa 40,168,16\n"
"Ra 148,2,21504\nMvaa 40,172,24\nWL 6\nEX 83,63\nCvaa 44,172,28,16\nRa 152,2,22528\nAD 85,85,82\nMvaa 44,168,20\nD1 1,16,46,1024\nMvaa 44,164,12\nRa 156,2,23552\nMvaa 44,160,4\n"
"AD 85,85,83\nCV 79,82,83\nMvaa 44,176,36\nRa 160,2,24576\nv_max_f32 v90, v84, v85\nMvaa 44,180,44\ns_add_u32 s88, s88, 32768\nEX 80,32\nMvaa 44,184,52\nRa 164,2,25600\n"
"v_cmp_lt_f32 vcc, s63, v90\nMvaa 44,188,60\nWL 8\nEX 81,33\nMvaa 40,188,56\nRa 168,2,26624\ns_cbranch_vccnz .T454\n.T455:\nMvaa 40,184,48\nD1 1,16,46,2048\nMvaa 40,180,40\n"
"Ra 172,2,27648\nAD 86,86,84\nMvaa 40,176,32\nAD 84,80,81\nCV 64,80,81\nMvav 128,128,72\nRa 176,2,28672\nMvav 132,128,76\nEX 82,34\nMvav 140,132,76\nRa 180,2,29696\n"
"Mvav 136,132,72\ns_waitcnt vmcnt(13) lgkmcnt(8)\nEX 83,35\nMvav 144,136,72\nRa 184,2,30720\nAD 84,84,82\nMvav 148,136,76\nD1 1,16,46,3072\ns_add_u32 m0, s89, 49152\n"
"Mvav 156,140,76\nRa 188,2,31744\ns_barrier\nMvav 152,140,72\nAD 84,84,83\nCV 65,82,83\nMvav 160,144,72\nRa 128,24,32768\nMvav 164,144,76\ns_add_u32 s46, s87, s88\nEX 80,36\n"
"Mvav 172,148,76\nRa 132,24,33792\nMvav 168,148,72\nWL 8\nEX 81,37\nMvav 176,152,72\nRa 136,24,34816\nAD 87,87,85\nMvav 180,152,76\nD1 1,16,46,0\nMvav 188,156,76\nRa 140,24,35840\n"
"Mvav 184,156,72\nAD 85,80,81\nCV 68,80,81\nMvav 192,160,72\nRa 144,26,32768\nMvav 196,160,76\ns_cmp_eq_u32 s99, 0\nEX 82,38\nMvav 204,164,76\nRa 148,26,33792\n"
"s_cbranch_scc1 .E448\n.E449:\nMvav 200,164,72\nWL 8\nEX 83,39\nMvav 208,168,72\nRa 152,26,34816\nAD 85,85,82\nMvav 212,168,76\nD1 1,16,46,1024\nMvav 220,172,76\nRa 156,26,35840\n"
"Mvav 216,172,72\nAD 85,85,83\nCV 69,82,83\nMvav 224,176,72\nRa 160,24,40960\nMvav 228,176,76\ns_xor_b32 s89, s89, 0x10000\nEX 80,40\nMvav 236,180,76\nRa 164,24,41984\n"
"Mvav 232,180,72\nWL 6\nEX 81,41\nMvav 240,184,72\nRa 168,24,43008\nAD 84,84,80\nMvav 244,184,76\nD1 1,16,46,2048\nMvav 252,188,76\nRa 172,24,44032\nMvav 248,188,72\nAD 84,84,81\n"
"CV 66,80,81\nCvaa 48,128,0,4\nRa 176,26,40960\nMvaa 48,132,8\ns_cmp_eq_u32 s99, 0\nEX 82,42\nMvaa 48,136,16\nRa 180,26,41984\ns_cselect_b32 s86, s94, s86\nMvaa 48,140,24\nWL 6\n"
"EX 83,43\nCvaa 52,140,28,8\nRa 184,26,43008\nAD 84,84,82\nMvaa 52,136,20\nD1 1,16,46,3072\ns_add_u32 m0, s89, 0\nMvaa 52,132,12\nRa 188,26,44032\nMvaa 52,128,4\nAD 84,84,83\n"
"CV 67,82,83\nMvaa 52,144,36\nRa 128,2,49152\nMvaa 52,148,44\ns_add_u32 s46, s86, s88\nEX 80,44\nMvaa 52,152,52\nRa 132,2,50176\nv_xor_b32 v24, 0x10000, v24\nMvaa 52,156,60\nWL 6\n"
"EX 81,45\nMvaa 48,156,56\nRa 136,2,51200\nAD 85,85,80\nMvaa 48,152,48\nD1 1,16,46,0\nMvaa 48,148,40\nRa 140,2,52224\nMvaa 48,144,32\nAD 85,85,81\nCV 70,80,81\nCvaa 56,160,0,12\n"
"Ra 144,2,53248\nMvaa 56,164,8\nSv 112,1,16,49,1024\nEX 82,46\nMvaa 56,168,16\nRa 148,2,54272\nv_xor_b32 v26, 0x10000, v26\nMvaa 56,172,24\nWL 6\nEX 83,47\nCvaa 60,172,28,16\n"
"Ra 152,2,55296\nAD 85,85,82\nMvaa 60,168,20\nD1 1,16,46,1024\nMvaa 60,164,12\nRa 156,2,56320\nMvaa 60,160,4\nAD 85,85,83\nCV 71,82,83\nMvaa 60,176,36\nRa 160,2,57344\n"
"v_max_f32 v90, v84, v85\nMvaa 60,180,44\ns_add_u32 s88, s88, 32768\nEX 80,48\nMvaa 60,184,52\nRa 164,2,58368\nv_cmp_lt_f32 vcc, s63, v90\nMvaa 60,188,60\nWL 8\nEX 81,49\n"
"Mvaa 56,188,56\nRa 168,2,59392\ns_cbranch_vccnz .T456\n.T457:\nMvaa 56,184,48\nD1 1,16,46,2048\nMvaa 56,180,40\nRa 172,2,60416\nAD 86,86,84\nMvaa 56,176,32\nAD 84,80,81\n"
"CV 72,80,81\nMvav 128,128,64\nRa 176,2,61440\nMvav 132,128,68\nEX 82,50\nMvav 140,132,68\nRa 180,2,62464\nMvav 136,132,64\ns_waitcnt vmcnt(13) lgkmcnt(8)\nEX 83,51\n"
"Mvav 144,136,64\nRa 184,2,63488\nAD 84,84,82\nMvav 148,136,68\nD1 1,16,46,3072\ns_add_u32 m0, s89, 16384\nMvav 156,140,68\nRa 188,2,64512\ns_barrier\nMvav 152,140,64\nAD 84,84,83\n"
"CV 73,82,83\nMvav 160,144,64\nRa 128,24,0\nMvav 164,144,68\ns_add_u32 s46, s87, s88\nEX 80,52\nMvav 172,148,68\nRa 132,24,1024\nv_xor_b32 v2, 0x10000, v2\nMvav 168,148,64\nWL 8\n"
"EX 81,53\nMvav 176,152,64\nRa 136,24,2048\nAD 87,87,85\nMvav 180,152,68\nD1 1,16,46,0\nMvav 188,156,68\nRa 140,24,3072\nMvav 184,156,64\nAD 85,80,81\nCV 76,80,81\nMvav 192,160,64\n"
"Ra 144,26,0\nMvav 196,160,68\ns_cmp_lt_u32 s99, s92\nEX 82,54\nMvav 204,164,68\nRa 148,26,1024\ns_cbranch_scc1 .E450\n.E451:\nMvav 200,164,64\nWL 8\nEX 83,55\nMvav 208,168,64\n"
"Ra 152,26,2048\nAD 85,85,82\nMvav 212,168,68\nD1 1,16,46,1024\nMvav 220,172,68\nRa 156,26,3072\nMvav 216,172,64\nAD 85,85,83\nCV 77,82,83\nMvav 224,176,64\nRa 160,24,8192\n"
"Mvav 228,176,68\ns_sub_u32 s90, s90, 1\nEX 80,56\nMvav 236,180,68\nRa 164,24,9216\ns_cbranch_scc1 .E452\n.E453:\nMvav 232,180,64\nWL 6\nEX 81,57\nMvav 240,184,64\nRa 168,24,10240\n"
"AD 84,84,80\nMvav 244,184,68\nD1 1,16,46,2048\ns_sub_u32 s99, s99, 1\nMvav 252,188,68\nRa 172,24,11264\nMvav 248,188,64\nAD 84,84,81\nCV 74,80,81\ns_cbranch_scc0 .A446\n"
"s_branch .A458\n.E448:\nv_cmpx_gt_i32 vcc, 19, v0\nv_mov_b32 v15, s97\nv_cmpx_gt_i32 vcc, 18, v0\nv_mov_b32 v14, s97\nv_cmpx_gt_i32 vcc, 17, v0\nv_mov_b32 v13, s97\n"
"v_cmpx_gt_i32 vcc, 16, v0\nv_mov_b32 v12, s97\nv_cmpx_gt_i32 vcc, 3, v0\nv_mov_b32 v7, s97\nv_cmpx_gt_i32 vcc, 2, v0\nv_mov_b32 v6, s97\nv_cmpx_gt_i32 vcc, 1, v0\n"
"v_mov_b32 v5, s97\nv_cmpx_gt_i32 vcc, 0, v0\nv_mov_b32 v4, s97\ns_mov_b64 exec, -1\ns_nop 2\ns_branch .E449\n.E450:\ns_cmp_eq_u32 s99, 0\ns_cbranch_scc1 .K447\n"
"v_cmpx_gt_i32 vcc, 19, v3\nv_mov_b32 v15, s97\nv_cmpx_gt_i32 vcc, 18, v3\nv_mov_b32 v14, s97\nv_cmpx_gt_i32 vcc, 17, v3\nv_mov_b32 v13, s97\nv_cmpx_gt_i32 vcc, 16, v3\n"
"v_mov_b32 v12, s97\nv_cmpx_gt_i32 vcc, 3, v3\nv_mov_b32 v7, s97\nv_cmpx_gt_i32 vcc, 2, v3\nv_mov_b32 v6, s97\nv_cmpx_gt_i32 vcc, 1, v3\nv_mov_b32 v5, s97\n"
"v_cmpx_gt_i32 vcc, 0, v3\nv_mov_b32 v4, s97\ns_mov_b64 exec, -1\ns_nop 2\ns_branch .E451\n.K447:\nv_cmpx_gt_i32 vcc, 19, v3\nv_mov_b32 v19, s97\nv_cmpx_gt_i32 vcc, 18, v3\n"
"v_mov_b32 v18, s97\nv_cmpx_gt_i32 vcc, 17, v3\nv_mov_b32 v17, s97\nv_cmpx_gt_i32 vcc, 16, v3\nv_mov_b32 v16, s97\nv_cmpx_gt_i32 vcc, 3, v3\nv_mov_b32 v11, s97\n"
"v_cmpx_gt_i32 vcc, 2, v3\nv_mov_b32 v10, s97\nv_cmpx_gt_i32 vcc, 1, v3\nv_mov_b32 v9, s97\nv_cmpx_gt_i32 vcc, 0, v3\nv_mov_b32 v8, s97\ns_mov_b64 exec, -1\ns_nop 2\n"
"s_branch .E451\n.E452:\ns_swappc_b64 s[80:81], s[8:9]\ns_branch .E453\n.HD409:\nv_cmp_ne_u32 vcc, s15, v27\ns_nop 0\ns_cbranch_vccnz .S434\n.S435:\n"
"buffer_store_dword v25, off, s[16:19], s54 offset:4 sc0 sc1\nv_and_b32 v27, 0x70, v1\nbuffer_load_dword v27, v27, s[16:19], s61 offen offset:12 sc0 sc1\ns_mov_b32 s90, 1\n"
"s_swappc_b64 s[8:9], s[80:81]\nv_cmp_ne_u32 vcc, s15, v27\ns_nop 0\ns_cbranch_vccnz .S437\n.S438:\nv_and_b32 v27, 0x70, v1\n"
"buffer_load_dword v27, v27, s[16:19], s61 offen offset:8 sc0 sc1\ns_mov_b32 s90, 1\ns_swappc_b64 s[8:9], s[80:81]\nv_cmp_ne_u32 vcc, s15, v27\ns_nop 0\ns_cbranch_vccnz .S440\n"
".S441:\nv_and_b32 v27, 0xf0, v1\nbuffer_load_dword v27, v27, s[16:19], s61 offen offset:4 sc0 sc1\ns_mov_b32 s90, 1\ns_swappc_b64 s[8:9], s[80:81]\nv_cmp_ne_u32 vcc, s15, v27\n"
"s_nop 0\ns_cbranch_vccnz .S443\n.S444:\ns_mov_b32 s90, 100\ns_swappc_b64 s[8:9], s[80:81]\n.A458:\nRa 176,26,8192\ns_add_u32 s49, s49, 131072\nEX 82,58\nRa 180,26,9216\nWL 6\n"
"EX 83,59\nCvaa 36,140,28,8\nRa 184,26,10240\nAD 84,84,82\nMvaa 36,136,20\nD1 1,16,46,3072\ns_add_u32 m0, s89, 32768\nMvaa 36,132,12\nRa 188,26,11264\nMvaa 36,128,4\nAD 84,84,83\n"
"CV 75,82,83\nMvaa 36,144,36\nRa 128,2,16384\nMvaa 36,148,44\ns_add_u32 s46, s86, s88\nEX 80,60\nMvaa 36,152,52\nRa 132,2,17408\nMvaa 36,156,60\nEX 81,61\nRa 136,2,18432\n"
"AD 85,85,80\nD1 1,16,46,0\nRa 140,2,19456\nAD 85,85,81\nCV 78,80,81\nRa 144,2,20480\nSv 112,1,16,49,0\ns_mov_b32 s87, s95\nEX 82,62\nRa 148,2,21504\nWL 6\nEX 83,63\n"
"Cvaa 44,172,28,16\nRa 152,2,22528\nAD 85,85,82\nMvaa 44,168,20\nD1 1,16,46,1024\nMvaa 44,164,12\nRa 156,2,23552\nMvaa 44,160,4\nAD 85,85,83\nCV 79,82,83\nMvaa 44,176,36\n"
"Ra 160,2,24576\nv_max_f32 v90, v84, v85\nMvaa 44,180,44\ns_add_u32 s88, s88, 32768\nMvaa 44,184,52\nRa 164,2,25600\nv_cmp_lt_f32 vcc, s63, v90\nMvaa 44,188,60\nWL 8\n"
"Ra 168,2,26624\ns_cbranch_vccnz .T460\n.T461:\nD1 1,16,46,2048\nRa 172,2,27648\nAD 86,86,84\nMvav 128,128,72\nRa 176,2,28672\nMvav 132,128,76\nMvav 140,132,76\nRa 180,2,29696\n"
"Mvav 136,132,72\ns_waitcnt vmcnt(13) lgkmcnt(8)\nMvav 144,136,72\nRa 184,2,30720\nMvav 148,136,76\nD1 1,16,46,3072\ns_add_u32 m0, s89, 49152\nMvav 156,140,76\nRa 188,2,31744\n"
"s_barrier\nMvav 152,140,72\nMvav 160,144,72\nRa 128,24,32768\nMvav 164,144,76\ns_add_u32 s46, s87, s88\nEX 80,36\nMvav 172,148,76\nRa 132,24,33792\nMvav 168,148,72\nWL 8\n"
"EX 81,37\nMvav 176,152,72\nRa 136,24,34816\nAD 87,87,85\nMvav 180,152,76\nD1 1,16,46,0\nMvav 188,156,76\nRa 140,24,35840\nMvav 184,156,72\nAD 85,80,81\nCV 68,80,81\n"
"Mvav 192,160,72\nRa 144,26,32768\nMvav 196,160,76\ns_nop 0\nEX 82,38\nMvav 204,164,76\nRa 148,26,33792\nv_cmpx_gt_i32 vcc, 19, v0\nv_mov_b32 v19, s97\nv_cmpx_gt_i32 vcc, 18, v0\n"
"v_mov_b32 v18, s97\nv_cmpx_gt_i32 vcc, 17, v0\nv_mov_b32 v17, s97\nv_cmpx_gt_i32 vcc, 16, v0\nv_mov_b32 v16, s97\nv_cmpx_gt_i32 vcc, 3, v0\nv_mov_b32 v11, s97\n"
"v_cmpx_gt_i32 vcc, 2, v0\nv_mov_b32 v10, s97\nv_cmpx_gt_i32 vcc, 1, v0\nv_mov_b32 v9, s97\nv_cmpx_gt_i32 vcc, 0, v0\nv_mov_b32 v8, s97\ns_mov_b64 exec, -1\ns_nop 2\ns_nop 0\n"
"Mvav 200,164,72\nWL 8\nEX 83,39\nMvav 208,168,72\nRa 152,26,34816\nAD 85,85,82\nMvav 212,168,76\nD1 1,16,46,1024\nMvav 220,172,76\nRa 156,26,35840\nMvav 216,172,72\nAD 85,85,83\n"
"CV 69,82,83\n.W588:\ns_cbranch_execz .W592\nMvav 224,176,72\nRa 160,24,40960\nMvav 228,176,76\ns_xor_b32 s89, s89, 0x10000\nMvav 236,180,76\nRa 164,24,41984\nMvav 232,180,72\n"
"WL 10\nMvav 240,184,72\nRa 168,24,43008\nMvav 244,184,76\nD1 1,16,46,2048\nMvav 252,188,76\nRa 172,24,44032\nMvav 248,188,72\nRa 176,26,40960\nRa 180,26,41984\nWL 6\n"
"Cvaa 52,140,28,8\nRa 184,26,43008\nMvaa 52,136,20\nD1 1,16,46,3072\ns_add_u32 m0, s89, 0\nMvaa 52,132,12\nRa 188,26,44032\nMvaa 52,128,4\nMvaa 52,144,36\nRa 128,2,49152\n"
"Mvaa 52,148,44\ns_add_u32 s46, s86, s88\nEX 80,44\nMvaa 52,152,52\nRa 132,2,50176\nv_xor_b32 v24, 0x10000, v24\nMvaa 52,156,60\nEX 81,45\nRa 136,2,51200\nAD 85,85,80\n"
"D1 1,16,46,0\nRa 140,2,52224\nAD 85,85,81\nCV 70,80,81\nRa 144,2,53248\nSv 112,1,16,49,1024\nEX 82,46\nRa 148,2,54272\nv_xor_b32 v26, 0x10000, v26\nWL 6\nEX 83,47\n"
"Cvaa 60,172,28,16\nRa 152,2,55296\nAD 85,85,82\nMvaa 60,168,20\nD1 1,16,46,1024\nMvaa 60,164,12\nRa 156,2,56320\nMvaa 60,160,4\nAD 85,85,83\nCV 71,82,83\nMvaa 60,176,36\n"
"Ra 160,2,57344\nMvaa 60,180,44\ns_add_u32 s88, s88, 32768\nMvaa 60,184,52\nRa 164,2,58368\nv_cmp_lt_f32 vcc, s63, v85\nMvaa 60,188,60\nWL 8\nRa 168,2,59392\ns_cbranch_vccnz .T462\n"
".T463:\nD1 1,16,46,2048\nRa 172,2,60416\nRa 176,2,61440\nMvav 132,128,68\nMvav 140,132,68\nRa 180,2,62464\ns_waitcnt vmcnt(13) lgkmcnt(8)\nRa 184,2,63488\nMvav 148,136,68\n"
"D1 1,16,46,3072\ns_add_u32 m0, s89, 16384\nMvav 156,140,68\nRa 188,2,64512\ns_barrier\nRa 128,24,0\nMvav 164,144,68\ns_add_u32 s46, s87, s88\nEX 80,52\nMvav 172,148,68\n"
"Ra 132,24,1024\nv_xor_b32 v2, 0x10000, v2\nWL 8\nEX 81,53\nRa 136,24,2048\nAD 87,87,85\nMvav 180,152,68\nD1 1,16,46,0\nMvav 188,156,68\nRa 140,24,3072\nAD 85,80,81\nCV 76,80,81\n"
"Ra 144,26,0\nMvav 196,160,68\nEX 82,54\nMvav 204,164,68\nRa 148,26,1024\nWL 8\nEX 83,55\nRa 152,26,2048\nAD 85,85,82\nMvav 212,168,68\nD1 1,16,46,1024\nMvav 220,172,68\n"
"Ra 156,26,3072\nAD 85,85,83\nCV 77,82,83\nRa 160,24,8192\nMvav 228,176,68\ns_sub_u32 s90, s90, 1\nMvav 236,180,68\nRa 164,24,9216\nWL 6\nRa 168,24,10240\nMvav 244,184,68\n"
"D1 1,16,46,2048\nMvav 252,188,68\nRa 172,24,11264\n.SW415:\nv_mov_b32 v4, 0\nv_mov_b32 v5, 0\nv_mov_b32 v6, 0\nv_mov_b32 v7, 0\nv_mov_b32 v8, 0\nv_mov_b32 v9, 0\nv_mov_b32 v10, 0\n"
"v_mov_b32 v11, 0\nv_mov_b32 v12, 0\nv_mov_b32 v13, 0\nv_mov_b32 v14, 0\nv_mov_b32 v15, 0\nv_mov_b32 v16, 0\nv_mov_b32 v17, 0\nv_mov_b32 v18, 0\nv_mov_b32 v19, 0\n"
"s_add_u32 s99, s98, 1\ns_mov_b32 s94, s96\ns_add_u32 s95, s96, 65536\nCvaa 32,128,192,4\nRa 176,26,8192\nMvaa 32,132,200\nMvaa 32,136,208\nRa 180,26,9216\nMvaa 32,140,216\nWL 6\n"
"Cvaa 36,140,220,8\nRa 184,26,10240\nMvaa 36,136,212\nD1 1,16,46,3072\ns_add_u32 m0, s89, 32768\nMvaa 36,132,204\nRa 188,26,11264\nMvaa 36,128,196\nMvaa 36,144,228\nRa 128,2,16384\n"
"Mvaa 36,148,236\ns_add_u32 s46, s86, s88\nEX 80,60\nMvaa 36,152,244\nRa 132,2,17408\nMvaa 36,156,252\nWL 6\nEX 81,61\nMvaa 32,156,248\nRa 136,2,18432\nAD 85,85,80\n"
"Mvaa 32,152,240\nD1 1,16,46,0\nMvaa 32,148,232\nRa 140,2,19456\nMvaa 32,144,224\nAD 85,85,81\nCV 78,80,81\nCvaa 40,160,192,12\nRa 144,2,20480\nMvaa 40,164,200\nEX 82,62\n"
"Mvaa 40,168,208\nRa 148,2,21504\nMvaa 40,172,216\nWL 6\nEX 83,63\nCvaa 44,172,220,16\nRa 152,2,22528\nAD 85,85,82\nMvaa 44,168,212\nD1 1,16,46,1024\nMvaa 44,164,204\n"
"Ra 156,2,23552\nMvaa 44,160,196\nAD 85,85,83\nCV 79,82,83\nMvaa 44,176,228\nRa 160,2,24576\nMvaa 44,180,236\ns_add_u32 s88, s88, 32768\nEX 80,32\nMvaa 44,184,244\nRa 164,2,25600\n"
"v_cmp_lt_f32 vcc, s63, v85\nMvaa 44,188,252\nWL 8\nEX 81,33\nMvaa 40,188,248\nRa 168,2,26624\ns_cbranch_vccnz .T466\n.T467:\nMvaa 40,184,240\nD1 1,16,46,2048\nCV 128,128,129\n"
"CV 129,130,131\nMvaa 40,180,232\nRa 172,2,27648\nCV 130,136,137\nMvaa 40,176,224\nAD 84,80,81\nCV 64,80,81\nCV 131,138,139\nRa 176,2,28672\ns_add_u32 s5, s66, 0\nSv 128,1,16,5,0\n"
"Mvav 132,128,76\nEX 82,34\nMvav 140,132,76\nRa 180,2,29696\ns_waitcnt vmcnt(13) lgkmcnt(8)\nEX 83,35\nRa 184,2,30720\nAD 84,84,82\nMvav 148,136,76\nD1 1,16,46,3072\n"
"s_add_u32 m0, s89, 49152\nMvav 156,140,76\nRa 188,2,31744\ns_barrier\nAD 84,84,83\nCV 65,82,83\nRa 128,24,32768\nMvav 164,144,76\ns_add_u32 s46, s87, s88\nEX 80,36\n"
"CV 132,132,133\nMvav 172,148,76\nRa 132,24,33792\nCV 133,134,135\nCV 134,140,141\nWL 8\nEX 81,37\nCV 135,142,143\nCV 144,144,145\nCV 145,146,147\nCV 146,152,153\nCV 147,154,155\n"
"Ra 136,24,34816\nAD 87,87,85\ns_add_u32 s5, s66, 0\nSv 132,1,16,5,1024\ns_add_u32 s5, s66, 0\nSv 144,1,16,5,2048\nMvav 180,152,76\nD1 1,16,46,0\nv_mov_b32 v96, v86\n"
"CV 148,148,149\nMvav 188,156,76\nRa 140,24,35840\nv_mov_b32 v97, v87\nCV 149,150,151\nAD 85,80,81\nCV 68,80,81\nCV 150,156,157\nRa 144,26,32768\nv_permlane32_swap_b32 v86, v96\n"
"v_permlane32_swap_b32 v87, v97\nMvav 196,160,76\nEX 82,38\nCV 151,158,159\nMvav 204,164,76\nRa 148,26,33792\nAD 86,86,96\ns_add_u32 s5, s66, 0\nSv 148,1,16,5,3072\nWL 8\nEX 83,39\n"
"Ra 152,26,34816\nAD 85,85,82\nAD 87,87,97\nMvav 212,168,76\nD1 1,16,46,1024\nv_mov_b32 v96, v86\nCV 160,160,161\nMvav 220,172,76\nRa 156,26,35840\nv_mov_b32 v97, v87\n"
"CV 161,162,163\nCV 162,168,169\nAD 85,85,83\nCV 69,82,83\nCV 163,170,171\nCV 164,164,165\nRa 160,24,40960\nv_permlane16_swap_b32 v86, v96\nv_permlane16_swap_b32 v87, v97\n"
"s_add_u32 s5, s66, 4096\nSv 160,1,16,5,0\nMvav 228,176,76\ns_xor_b32 s89, s89, 0x10000\nEX 80,40\nMvav 236,180,76\nRa 164,24,41984\nAD 86,86,96\nCV 165,166,167\nCV 166,172,173\n"
"WL 6\nEX 81,41\nRa 168,24,43008\nAD 84,84,80\nAD 87,87,97\nCV 167,174,175\nMvav 244,184,76\nD1 1,16,46,2048\nv_cmp_gt_u32 vcc, 0x100, v1\nv_mov_b32 v94, s15\nv_mov_b32 v95, s15\n"
"v_cndmask_b32 v92, v89, v88, vcc\nv_cndmask_b32 v93, v87, v86, vcc\nMvav 252,188,76\nRa 172,24,44032\nv_mov_b32 v86, 0\ns_add_u32 s5, s66, 4096\nSv 164,1,16,5,1024\nAD 84,84,81\n"
"CV 66,80,81\nCV 176,176,177\nCvaa 48,128,192,4\nRa 176,26,40960\nv_mov_b32 v87, 0\nv_mov_b32 v88, 0\nCV 177,178,179\nMvaa 48,132,200\nEX 82,42\nCV 178,184,185\nMvaa 48,136,208\n"
"Ra 180,26,41984\nv_mov_b32 v89, 0\nCV 179,186,187\nMvaa 48,140,216\nWL 6\nEX 83,43\nCvaa 52,140,220,8\nRa 184,26,43008\nAD 84,84,82\ns_add_u32 s5, s66, 4096\nSv 176,1,16,5,2048\n"
"Mvaa 52,136,212\nD1 1,16,46,3072\ns_add_u32 m0, s89, 0\nMvaa 52,132,204\nRa 188,26,44032\nCV 180,180,181\nCV 181,182,183\nMvaa 52,128,196\nAD 84,84,83\nCV 67,82,83\n"
"Mvaa 52,144,228\nRa 128,2,49152\nCV 182,188,189\nCV 183,190,191\nMvaa 52,148,236\ns_add_u32 s46, s86, s88\nEX 80,44\nMvaa 52,152,244\nRa 132,2,50176\nv_xor_b32 v24, 0x10000, v24\n"
"s_add_u32 s5, s66, 4096\nSv 180,1,16,5,3072\nMvaa 52,156,252\nWL 6\nEX 81,45\nMvaa 48,156,248\nRa 136,2,51200\nAD 85,85,80\nCV 192,192,193\nMvaa 48,152,240\nD1 1,16,46,0\n"
"CV 193,194,195\nMvaa 48,148,232\nRa 140,2,52224\nCV 194,200,201\nCV 195,202,203\nMvaa 48,144,224\nAD 85,85,81\nCV 70,80,81\nCV 196,196,197\nCvaa 56,160,192,12\nRa 144,2,53248\n"
"s_add_u32 s5, s66, 16384\nSv 192,1,16,5,0\nMvaa 56,164,200\nEX 82,46\nMvaa 56,168,208\nRa 148,2,54272\nv_xor_b32 v26, 0x10000, v26\nCV 197,198,199\nMvaa 56,172,216\nWL 6\n"
"EX 83,47\nCvaa 60,172,220,16\nRa 152,2,55296\nAD 85,85,82\nCV 198,204,205\nMvaa 60,168,212\nD1 1,16,46,1024\nCV 199,206,207\nCV 208,208,209\nMvaa 60,164,204\nRa 156,2,56320\n"
"s_add_u32 s5, s66, 16384\nSv 196,1,16,5,1024\nMvaa 60,160,196\nAD 85,85,83\nCV 71,82,83\nCV 209,210,211\nMvaa 60,176,228\nRa 160,2,57344\nv_max_f32 v90, v84, v85\nCV 210,216,217\n"
"CV 211,218,219\nMvaa 60,180,236\ns_add_u32 s88, s88, 32768\nEX 80,48\nMvaa 60,184,244\nRa 164,2,58368\nv_cmp_lt_f32 vcc, s63, v90\nMvaa 60,188,252\nWL 8\nEX 81,49\n"
"Mvaa 56,188,248\nRa 168,2,59392\ns_cbranch_vccnz .T468\n.T469:\nv_mov_b32 v20, v84\nv_mov_b32 v21, v85\nv_mov_b32 v22, v20\nv_mov_b32 v23, v21\ns_add_u32 s5, s66, 16384\n"
"Sv 208,1,16,5,2048\nMvaa 56,184,240\nD1 1,16,46,2048\nv_permlane32_swap_b32 v20, v22\nv_permlane32_swap_b32 v21, v23\nAD 20,20,22\nAD 21,21,23\nv_mov_b32 v22, v20\n"
"v_mov_b32 v23, v21\nMvaa 56,180,232\nRa 172,2,60416\nv_permlane16_swap_b32 v20, v22\nv_permlane16_swap_b32 v21, v23\nAD 20,20,22\nAD 21,21,23\nv_min_f32 v90, v20, v21\n"
"v_cmp_gt_f32 vcc, s64, v90\ns_nop 0\ns_cbranch_vccnz .T470\n.T471:\nAD 86,86,84\nMvaa 56,176,224\nAD 84,80,81\nCV 72,80,81\nCV 212,212,213\nZvav 128,128,64\nRa 176,2,61440\n"
"CV 213,214,215\nCV 214,220,221\nZvav 132,128,68\nEX 82,50\nCV 215,222,223\nZvav 140,132,68\nRa 180,2,62464\ns_add_u32 s5, s66, 16384\nSv 212,1,16,5,3072\nZvav 136,132,64\n"
"s_waitcnt vmcnt(23) lgkmcnt(8)\nEX 83,51\nZvav 144,136,64\nRa 184,2,63488\nAD 84,84,82\nCV 224,224,225\nZvav 148,136,68\nD1 1,16,46,3072\ns_add_u32 m0, s89, 16384\nCV 225,226,227\n"
"Zvav 156,140,68\nRa 188,2,64512\ns_barrier\nCV 226,232,233\nZvav 152,140,64\nAD 84,84,83\nCV 73,82,83\nCV 227,234,235\nZvav 160,144,64\nRa 128,24,0\ns_add_u32 s5, s66, 20480\n"
"Sv 224,1,16,5,0\nZvav 164,144,68\ns_add_u32 s46, s87, s88\nEX 80,52\nZvav 172,148,68\nRa 132,24,1024\nv_xor_b32 v2, 0x10000, v2\nCV 228,228,229\nZvav 168,148,64\nWL 8\nEX 81,53\n"
"Zvav 176,152,64\nRa 136,24,2048\nAD 87,87,85\nCV 229,230,231\nZvav 180,152,68\nD1 1,16,46,0\nCV 230,236,237\nCV 231,238,239\nZvav 188,156,68\nRa 140,24,3072\n"
"s_add_u32 s5, s66, 20480\nSv 228,1,16,5,1024\nZvav 184,156,64\nAD 85,80,81\nCV 76,80,81\nZvav 192,160,64\nRa 144,26,0\nCV 240,240,241\nCV 241,242,243\nZvav 196,160,68\nEX 82,54\n"
"Zvav 204,164,68\nRa 148,26,1024\nCV 242,248,249\nCV 243,250,251\nZvav 200,164,64\nWL 8\nEX 83,55\nZvav 208,168,64\nRa 152,26,2048\nAD 85,85,82\ns_add_u32 s5, s66, 20480\n"
"Sv 240,1,16,5,2048\nZvav 212,168,68\nD1 1,16,46,1024\nCV 244,244,245\nZvav 220,172,68\nRa 156,26,3072\nCV 245,246,247\nZvav 216,172,64\nAD 85,85,83\nCV 77,82,83\nZvav 224,176,64\n"
"Ra 160,24,8192\nCV 246,252,253\nCV 247,254,255\nZvav 228,176,68\ns_sub_u32 s90, s90, 1\nEX 80,56\nZvav 236,180,68\nRa 164,24,9216\ns_cbranch_scc0 .E464\n"
"s_swappc_b64 s[80:81], s[8:9]\n.E464:\ns_add_u32 s5, s66, 20480\nSv 244,1,16,5,3072\nZvav 232,180,64\nWL 6\nEX 81,57\nZvav 240,184,64\nRa 168,24,10240\nAD 84,84,80\n"
"Zvav 244,184,68\nD1 1,16,46,2048\nZvav 252,188,68\nRa 172,24,11264\nZvav 248,188,64\nAD 84,84,81\nCV 74,80,81\ns_sub_u32 s99, s99, 1\n.p2align 6\n.A472:\nCvaa 32,128,192,4\n"
"Ra 176,26,8192\nMvaa 32,132,200\ns_add_u32 s49, s49, 131072\nEX 82,58\nMvaa 32,136,208\nRa 180,26,9216\nMvaa 32,140,216\nWL 6\n.W589:\ns_cbranch_execz .WB414\nEX 83,59\n"
"Cvaa 36,140,220,8\nRa 184,26,10240\nAD 84,84,82\nMvaa 36,136,212\nD1 1,16,46,3072\ns_add_u32 m0, s89, 32768\nMvaa 36,132,204\nRa 188,26,11264\nMvaa 36,128,196\nAD 84,84,83\n"
"CV 75,82,83\nMvaa 36,144,228\nRa 128,2,16384\nMvaa 36,148,236\ns_add_u32 s46, s86, s88\nEX 80,60\nMvaa 36,152,244\nRa 132,2,17408\nMvaa 36,156,252\nWL 6\nEX 81,61\n"
"Mvaa 32,156,248\nRa 136,2,18432\nAD 85,85,80\nMvaa 32,152,240\nD1 1,16,46,0\nMvaa 32,148,232\nRa 140,2,19456\nMvaa 32,144,224\nAD 85,85,81\nCV 78,80,81\nCvaa 40,160,192,12\n"
"Ra 144,2,20480\nMvaa 40,164,200\nSv 112,1,16,49,0\nEX 82,62\nMvaa 40,168,208\nRa 148,2,21504\nMvaa 40,172,216\nWL 6\nEX 83,63\nCvaa 44,172,220,16\nRa 152,2,22528\nAD 85,85,82\n"
"Mvaa 44,168,212\nD1 1,16,46,1024\nMvaa 44,164,204\nRa 156,2,23552\nMvaa 44,160,196\nAD 85,85,83\nCV 79,82,83\nMvaa 44,176,228\nRa 160,2,24576\nv_max_f32 v90, v84, v85\n"
"Mvaa 44,180,236\ns_add_u32 s88, s88, 32768\nEX 80,32\nMvaa 44,184,244\nRa 164,2,25600\nv_cmp_lt_f32 vcc, s63, v90\nMvaa 44,188,252\nWL 8\nEX 81,33\nMvaa 40,188,248\n"
"Ra 168,2,26624\ns_cbranch_vccnz .T482\n.T483:\nMvaa 40,184,240\nD1 1,16,46,2048\nMvaa 40,180,232\nRa 172,2,27648\nAD 86,86,84\nMvaa 40,176,224\nAD 84,80,81\nCV 64,80,81\n"
"Mvav 128,128,72\nRa 176,2,28672\nMvav 132,128,76\nEX 82,34\nMvav 140,132,76\nRa 180,2,29696\nMvav 136,132,72\ns_waitcnt vmcnt(13) lgkmcnt(8)\nEX 83,35\nMvav 144,136,72\n"
"Ra 184,2,30720\nAD 84,84,82\nMvav 148,136,76\nD1 1,16,46,3072\ns_add_u32 m0, s89, 49152\nMvav 156,140,76\nRa 188,2,31744\ns_barrier\nMvav 152,140,72\nAD 84,84,83\nCV 65,82,83\n"
"Mvav 160,144,72\nRa 128,24,32768\nMvav 164,144,76\ns_add_u32 s46, s87, s88\nEX 80,36\nMvav 172,148,76\nRa 132,24,33792\nMvav 168,148,72\nWL 8\nEX 81,37\nMvav 176,152,72\n"
"Ra 136,24,34816\nAD 87,87,85\nMvav 180,152,76\nD1 1,16,46,0\nMvav 188,156,76\nRa 140,24,35840\nMvav 184,156,72\nAD 85,80,81\nCV 68,80,81\nMvav 192,160,72\nRa 144,26,32768\n"
"Mvav 196,160,76\ns_cmp_eq_u32 s99, 0\nEX 82,38\nMvav 204,164,76\nRa 148,26,33792\ns_cbranch_scc1 .E474\n.E475:\nMvav 200,164,72\nWL 8\nEX 83,39\nMvav 208,168,72\nRa 152,26,34816\n"
"AD 85,85,82\nMvav 212,168,76\nD1 1,16,46,1024\nMvav 220,172,76\nRa 156,26,35840\nMvav 216,172,72\nAD 85,85,83\nCV 69,82,83\nMvav 224,176,72\nRa 160,24,40960\nMvav 228,176,76\n"
"s_xor_b32 s89, s89, 0x10000\nEX 80,40\nMvav 236,180,76\nRa 164,24,41984\nMvav 232,180,72\nWL 6\nEX 81,41\nMvav 240,184,72\nRa 168,24,43008\nAD 84,84,80\nMvav 244,184,76\n"
"D1 1,16,46,2048\nMvav 252,188,76\nRa 172,24,44032\nMvav 248,188,72\nAD 84,84,81\nCV 66,80,81\nCvaa 48,128,192,4\nRa 176,26,40960\nMvaa 48,132,200\ns_cmp_eq_u32 s99, 0\nEX 82,42\n"
"Mvaa 48,136,208\nRa 180,26,41984\ns_cselect_b32 s86, s94, s86\nMvaa 48,140,216\nWL 6\nEX 83,43\nCvaa 52,140,220,8\nRa 184,26,43008\nAD 84,84,82\nMvaa 52,136,212\nD1 1,16,46,3072\n"
"s_add_u32 m0, s89, 0\nMvaa 52,132,204\nRa 188,26,44032\nMvaa 52,128,196\nAD 84,84,83\nCV 67,82,83\nMvaa 52,144,228\nRa 128,2,49152\nMvaa 52,148,236\ns_add_u32 s46, s86, s88\n"
"EX 80,44\nMvaa 52,152,244\nRa 132,2,50176\nv_xor_b32 v24, 0x10000, v24\nMvaa 52,156,252\nWL 6\nEX 81,45\nMvaa 48,156,248\nRa 136,2,51200\nAD 85,85,80\nMvaa 48,152,240\n"
"D1 1,16,46,0\nMvaa 48,148,232\nRa 140,2,52224\nMvaa 48,144,224\nAD 85,85,81\nCV 70,80,81\nCvaa 56,160,192,12\nRa 144,2,53248\nMvaa 56,164,200\nSv 112,1,16,49,1024\nEX 82,46\n"
"Mvaa 56,168,208\nRa 148,2,54272\nv_xor_b32 v26, 0x10000, v26\nMvaa 56,172,216\nWL 6\nEX 83,47\nCvaa 60,172,220,16\nRa 152,2,55296\nAD 85,85,82\nMvaa 60,168,212\nD1 1,16,46,1024\n"
"Mvaa 60,164,204\nRa 156,2,56320\nMvaa 60,160,196\nAD 85,85,83\nCV 71,82,83\nMvaa 60,176,228\nRa 160,2,57344\nv_max_f32 v90, v84, v85\nMvaa 60,180,236\ns_add_u32 s88, s88, 32768\n"
"EX 80,48\nMvaa 60,184,244\nRa 164,2,58368\nv_cmp_lt_f32 vcc, s63, v90\nMvaa 60,188,252\nWL 8\nEX 81,49\nMvaa 56,188,248\nRa 168,2,59392\ns_cbranch_vccnz .T484\n.T485:\n"
"Mvaa 56,184,240\nD1 1,16,46,2048\nMvaa 56,180,232\nRa 172,2,60416\nAD 86,86,84\nMvaa 56,176,224\nAD 84,80,81\nCV 72,80,81\nMvav 128,128,64\nRa 176,2,61440\nMvav 132,128,68\n"
"s_cmp_eq_u32 s99, s98\nEX 82,50\nMvav 140,132,68\nRa 180,2,62464\ns_cbranch_scc1 .E480\n.E481:\nMvav 136,132,64\ns_waitcnt vmcnt(13) lgkmcnt(8)\nEX 83,51\nMvav 144,136,64\n"
"Ra 184,2,63488\nAD 84,84,82\nMvav 148,136,68\nD1 1,16,46,3072\ns_add_u32 m0, s89, 16384\nMvav 156,140,68\nRa 188,2,64512\ns_barrier\nMvav 152,140,64\nAD 84,84,83\nCV 73,82,83\n"
"Mvav 160,144,64\nRa 128,24,0\nMvav 164,144,68\ns_add_u32 s46, s87, s88\nEX 80,52\nMvav 172,148,68\nRa 132,24,1024\nv_xor_b32 v2, 0x10000, v2\nMvav 168,148,64\nWL 8\nEX 81,53\n"
"Mvav 176,152,64\nRa 136,24,2048\nAD 87,87,85\nMvav 180,152,68\nD1 1,16,46,0\nMvav 188,156,68\nRa 140,24,3072\nMvav 184,156,64\nAD 85,80,81\nCV 76,80,81\nMvav 192,160,64\n"
"Ra 144,26,0\nMvav 196,160,68\ns_cmp_lt_u32 s99, s92\nEX 82,54\nMvav 204,164,68\nRa 148,26,1024\ns_cbranch_scc1 .E476\n.E477:\nMvav 200,164,64\nWL 8\nEX 83,55\nMvav 208,168,64\n"
"Ra 152,26,2048\nAD 85,85,82\nMvav 212,168,68\nD1 1,16,46,1024\nMvav 220,172,68\nRa 156,26,3072\nMvav 216,172,64\nAD 85,85,83\nCV 77,82,83\nMvav 224,176,64\nRa 160,24,8192\n"
"Mvav 228,176,68\ns_sub_u32 s90, s90, 1\nEX 80,56\nMvav 236,180,68\nRa 164,24,9216\ns_cbranch_scc1 .E478\n.E479:\nMvav 232,180,64\nWL 6\nEX 81,57\nMvav 240,184,64\nRa 168,24,10240\n"
"AD 84,84,80\nMvav 244,184,68\nD1 1,16,46,2048\ns_sub_u32 s99, s99, 1\nMvav 252,188,68\nRa 172,24,11264\nMvav 248,188,64\nAD 84,84,81\nCV 74,80,81\ns_cbranch_scc0 .A472\n"
"s_branch .A486\n.E474:\nv_cmpx_gt_i32 vcc, 19, v0\nv_mov_b32 v15, s97\nv_cmpx_gt_i32 vcc, 18, v0\nv_mov_b32 v14, s97\nv_cmpx_gt_i32 vcc, 17, v0\nv_mov_b32 v13, s97\n"
"v_cmpx_gt_i32 vcc, 16, v0\nv_mov_b32 v12, s97\nv_cmpx_gt_i32 vcc, 3, v0\nv_mov_b32 v7, s97\nv_cmpx_gt_i32 vcc, 2, v0\nv_mov_b32 v6, s97\nv_cmpx_gt_i32 vcc, 1, v0\n"
"v_mov_b32 v5, s97\nv_cmpx_gt_i32 vcc, 0, v0\nv_mov_b32 v4, s97\ns_mov_b64 exec, -1\ns_nop 2\ns_branch .E475\n.E476:\ns_cmp_eq_u32 s99, 0\ns_cbranch_scc1 .K473\n"
"v_cmpx_gt_i32 vcc, 19, v3\nv_mov_b32 v15, s97\nv_cmpx_gt_i32 vcc, 18, v3\nv_mov_b32 v14, s97\nv_cmpx_gt_i32 vcc, 17, v3\nv_mov_b32 v13, s97\nv_cmpx_gt_i32 vcc, 16, v3\n"
"v_mov_b32 v12, s97\nv_cmpx_gt_i32 vcc, 3, v3\nv_mov_b32 v7, s97\nv_cmpx_gt_i32 vcc, 2, v3\nv_mov_b32 v6, s97\nv_cmpx_gt_i32 vcc, 1, v3\nv_mov_b32 v5, s97\n"
"v_cmpx_gt_i32 vcc, 0, v3\nv_mov_b32 v4, s97\ns_mov_b64 exec, -1\ns_nop 2\ns_branch .E477\n.K473:\nv_cmpx_gt_i32 vcc, 19, v3\nv_mov_b32 v19, s97\nv_cmpx_gt_i32 vcc, 18, v3\n"
"v_mov_b32 v18, s97\nv_cmpx_gt_i32 vcc, 17, v3\nv_mov_b32 v17, s97\nv_cmpx_gt_i32 vcc, 16, v3\nv_mov_b32 v16, s97\nv_cmpx_gt_i32 vcc, 3, v3\nv_mov_b32 v11, s97\n"
"v_cmpx_gt_i32 vcc, 2, v3\nv_mov_b32 v10, s97\nv_cmpx_gt_i32 vcc, 1, v3\nv_mov_b32 v9, s97\nv_cmpx_gt_i32 vcc, 0, v3\nv_mov_b32 v8, s97\ns_mov_b64 exec, -1\ns_nop 2\n"
"s_branch .E477\n.E478:\ns_swappc_b64 s[80:81], s[8:9]\ns_branch .E479\n.E480:\nv_mov_b32 v91, 0x40000000\nv_cmp_gt_u32 vcc, 0x200, v1\nWV 15\ns_nop 0\n"
"v_cndmask_b32 v91, v91, v1, vcc\nSv 92,91,16,65,0\ns_branch .E481\n.A486:\n.LE416:\nRa 176,26,8192\nEX 82,58\nRa 180,26,9216\nWL 6\nEX 83,59\nCvaa 36,140,220,8\nRa 184,26,10240\n"
"AD 84,84,82\nMvaa 36,136,212\nD1 1,16,46,3072\ns_add_u32 m0, s89, 32768\nMvaa 36,132,204\nRa 188,26,11264\nMvaa 36,128,196\nAD 84,84,83\nCV 75,82,83\nMvaa 36,144,228\n"
"Ra 128,2,16384\nMvaa 36,148,236\ns_add_u32 s46, s86, s88\nEX 80,60\nMvaa 36,152,244\nRa 132,2,17408\nMvaa 36,156,252\nEX 81,61\nRa 136,2,18432\nAD 85,85,80\nD1 1,16,46,0\n"
"Ra 140,2,19456\nAD 85,85,81\nCV 78,80,81\nRa 144,2,20480\ns_mov_b32 s87, s95\nEX 82,62\nRa 148,2,21504\nWL 6\nEX 83,63\nCvaa 44,172,220,16\nRa 152,2,22528\nAD 85,85,82\n"
"Mvaa 44,168,212\nD1 1,16,46,1024\nMvaa 44,164,204\nRa 156,2,23552\nMvaa 44,160,196\nAD 85,85,83\nCV 79,82,83\nMvaa 44,176,228\nRa 160,2,24576\nv_max_f32 v90, v84, v85\n"
"Mvaa 44,180,236\ns_add_u32 s88, s88, 32768\nMvaa 44,184,244\nRa 164,2,25600\nv_cmp_lt_f32 vcc, s63, v90\nMvaa 44,188,252\nWL 8\nRa 168,2,26624\ns_cbranch_vccnz .T488\n.T489:\n"
"D1 1,16,46,2048\nRa 172,2,27648\nAD 86,86,84\nMvav 128,128,72\nRa 176,2,28672\nMvav 132,128,76\nMvav 140,132,76\nRa 180,2,29696\nMvav 136,132,72\ns_waitcnt vmcnt(12) lgkmcnt(8)\n"
"Mvav 144,136,72\nRa 184,2,30720\nMvav 148,136,76\nD1 1,16,46,3072\ns_add_u32 m0, s89, 49152\nMvav 156,140,76\nRa 188,2,31744\ns_barrier\nMvav 152,140,72\nMvav 160,144,72\n"
"Ra 128,24,32768\nMvav 164,144,76\ns_add_u32 s46, s87, s88\nEX 80,36\nMvav 172,148,76\nRa 132,24,33792\nMvav 168,148,72\nWL 8\nEX 81,37\nMvav 176,152,72\nRa 136,24,34816\n"
"AD 87,87,85\nMvav 180,152,76\nD1 1,16,46,0\nMvav 188,156,76\nRa 140,24,35840\nMvav 184,156,72\nAD 85,80,81\nCV 68,80,81\nMvav 192,160,72\nRa 144,26,32768\nMvav 196,160,76\n"
"s_nop 0\nEX 82,38\nMvav 204,164,76\nRa 148,26,33792\nv_cmpx_gt_i32 vcc, 19, v0\nv_mov_b32 v19, s97\nv_cmpx_gt_i32 vcc, 18, v0\nv_mov_b32 v18, s97\nv_cmpx_gt_i32 vcc, 17, v0\n"
"v_mov_b32 v17, s97\nv_cmpx_gt_i32 vcc, 16, v0\nv_mov_b32 v16, s97\nv_cmpx_gt_i32 vcc, 3, v0\nv_mov_b32 v11, s97\nv_cmpx_gt_i32 vcc, 2, v0\nv_mov_b32 v10, s97\n"
"v_cmpx_gt_i32 vcc, 1, v0\nv_mov_b32 v9, s97\nv_cmpx_gt_i32 vcc, 0, v0\nv_mov_b32 v8, s97\ns_mov_b64 exec, -1\ns_nop 2\ns_nop 0\nMvav 200,164,72\nWL 8\nEX 83,39\nMvav 208,168,72\n"
"Ra 152,26,34816\nAD 85,85,82\nMvav 212,168,76\nD1 1,16,46,1024\nMvav 220,172,76\nRa 156,26,35840\nMvav 216,172,72\nAD 85,85,83\nCV 69,82,83\nMvav 224,176,72\nRa 160,24,40960\n"
"Mvav 228,176,76\ns_xor_b32 s89, s89, 0x10000\nMvav 236,180,76\nRa 164,24,41984\nMvav 232,180,72\nWL 10\nMvav 240,184,72\nRa 168,24,43008\nMvav 244,184,76\nD1 1,16,46,2048\n"
"Mvav 252,188,76\nRa 172,24,44032\nMvav 248,188,72\nRa 176,26,40960\nRa 180,26,41984\nWL 6\nCvaa 52,140,220,8\nRa 184,26,43008\nMvaa 52,136,212\nD1 1,16,46,3072\n"
"s_add_u32 m0, s89, 0\nMvaa 52,132,204\nRa 188,26,44032\nMvaa 52,128,196\nMvaa 52,144,228\nRa 128,2,49152\nMvaa 52,148,236\ns_add_u32 s46, s86, s88\nEX 80,44\nMvaa 52,152,244\n"
"Ra 132,2,50176\nv_xor_b32 v24, 0x10000, v24\nMvaa 52,156,252\nEX 81,45\nRa 136,2,51200\nAD 85,85,80\nD1 1,16,46,0\nRa 140,2,52224\nAD 85,85,81\nCV 70,80,81\nRa 144,2,53248\n"
"EX 82,46\nRa 148,2,54272\nv_xor_b32 v26, 0x10000, v26\nWL 6\nEX 83,47\nCvaa 60,172,220,16\nRa 152,2,55296\nAD 85,85,82\nMvaa 60,168,212\nD1 1,16,46,1024\nMvaa 60,164,204\n"
"Ra 156,2,56320\nMvaa 60,160,196\nAD 85,85,83\nCV 71,82,83\nMvaa 60,176,228\nRa 160,2,57344\nMvaa 60,180,236\ns_add_u32 s88, s88, 32768\nMvaa 60,184,244\nRa 164,2,58368\n"
"v_cmp_lt_f32 vcc, s63, v85\nMvaa 60,188,252\nWL 8\nRa 168,2,59392\ns_cbranch_vccnz .T490\n.T491:\nD1 1,16,46,2048\nRa 172,2,60416\nRa 176,2,61440\nMvav 132,128,68\n"
"Mvav 140,132,68\nRa 180,2,62464\ns_waitcnt vmcnt(11) lgkmcnt(8)\nRa 184,2,63488\nMvav 148,136,68\nD1 1,16,46,3072\ns_add_u32 m0, s89, 16384\nMvav 156,140,68\nRa 188,2,64512\n"
"s_barrier\nRa 128,24,0\nMvav 164,144,68\ns_add_u32 s46, s87, s88\nEX 80,52\nMvav 172,148,68\nRa 132,24,1024\nv_xor_b32 v2, 0x10000, v2\nWL 8\nEX 81,53\nRa 136,24,2048\n"
"AD 87,87,85\nMvav 180,152,68\nD1 1,16,46,0\nMvav 188,156,68\n.W590:\ns_cbranch_execz .WB414\nRa 140,24,3072\nAD 85,80,81\nCV 76,80,81\nRa 144,26,0\nMvav 196,160,68\nEX 82,54\n"
"Mvav 204,164,68\nRa 148,26,1024\nWL 8\nEX 83,55\nRa 152,26,2048\nAD 85,85,82\nMvav 212,168,68\nD1 1,16,46,1024\nMvav 220,172,68\nRa 156,26,3072\nAD 85,85,83\nCV 77,82,83\n"
"Ra 160,24,8192\nMvav 228,176,68\nMvav 236,180,68\nRa 164,24,9216\nWL 10\nRa 168,24,10240\nMvav 244,184,68\nD1 1,16,46,2048\nMvav 252,188,68\nRa 172,24,11264\nD1 1,16,46,3072\n"
"s_add_u32 m0, s89, 32768\ns_add_u32 s46, s86, s88\nRa 128,2,16384\nv_xor_b32 v3, 0x10000, v2\nv_add_u32 v0, 0x20000, v1\nEX 80,60\nRa 132,2,17408\ns_sub_u32 s94, s49, 1703936\n"
"EX 81,61\nRa 136,2,18432\nAD 85,85,80\ns_xor_b32 s92, s94, 0x202000\nD1 1,16,46,0\ns_lshl_b32 s89, s11, 12\nRa 140,2,19456\ns_lshl1_add_u32 s2, s12, 1\nAD 85,85,81\nCV 78,80,81\n"
"Ra 144,2,20480\ns_lshl3_add_u32 s2, s2, s13\nEX 82,62\nRa 148,2,21504\ns_lshl4_add_u32 s2, s2, 0xc048200\nEX 83,63\nRa 152,2,22528\nAD 85,85,82\nD1 1,16,46,1024\nRa 156,2,23552\n"
"s_and_b32 s3, s72, 0x2000\nAD 85,85,83\nCV 79,82,83\nRa 160,2,24576\ns_cmp_eq_u32 s11, 1\ns_cselect_b32 s54, s2, s54\ns_cselect_b32 s3, s3, 0\nv_cmp_lt_f32 vcc, s63, v85\n"
"Ra 164,2,25600\ns_cbranch_vccnz .T492\n.T493:\nAD 87,87,85\nWL 8\nv_mov_b32 v84, v86\nv_mov_b32 v85, v87\nRa 168,2,26624\nv_permlane32_swap_b32 v86, v84\n"
"v_permlane32_swap_b32 v87, v85\nD1 1,16,46,2048\nAD 86,86,84\nRa 172,2,27648\nAD 87,87,85\nv_mov_b32 v84, v86\nv_mov_b32 v85, v87\nRa 176,2,28672\nv_permlane16_swap_b32 v86, v84\n"
"v_permlane16_swap_b32 v87, v85\nMvav 132,128,76\nAD 86,86,84\nAD 87,87,85\nMvav 140,132,76\nRa 180,2,29696\nv_rcp_f32 v86, v86\nWL 8\nv_rcp_f32 v87, v87\nRa 184,2,30720\n"
"v_xor_b32 v25, s3, v25\nMvav 148,136,76\nD1 1,16,46,3072\nMvav 156,140,76\nRa 188,2,31744\nMU 128,128,86\nMU 129,129,86\nCV 96,128,129\nMU 130,130,86\nMU 131,131,86\nRa 128,2,0\n"
"CV 97,130,131\nMU 132,132,87\nMvav 164,144,76\nMU 133,133,87\nCV 100,132,133\nMvav 172,148,76\nRa 132,2,1024\nMU 134,134,87\nMU 135,135,87\nWL 8\ns_add_u32 m0, s89, 131072\n"
"s_add_u32 s46, s96, 1212416\nRa 136,2,2048\nCV 101,134,135\nMU 136,136,86\nMvav 180,152,76\nD1 1,16,46,0\nMU 137,137,86\nMvav 188,156,76\nRa 140,2,3072\nCV 98,136,137\n"
"MU 138,138,86\nMU 139,139,86\nCV 99,138,139\nMU 140,140,87\nRa 144,2,4096\nMU 141,141,87\nCV 102,140,141\nMvav 196,160,76\nMU 142,142,87\nMU 143,143,87\nMvav 204,164,76\n"
"Ra 148,2,5120\nCV 103,142,143\nMU 144,144,86\nWL 8\nMU 145,145,86\nCV 104,144,145\nRa 152,2,6144\nMU 146,146,86\nMU 147,147,86\nMvav 212,168,76\nD1 1,16,46,1024\nCV 105,146,147\n"
"Mvav 220,172,76\nRa 156,2,7168\nMU 148,148,87\nMU 149,149,87\nCV 108,148,149\nMU 150,150,87\nMU 151,151,87\nRa 160,2,8192\nCV 109,150,151\nMU 152,152,86\nMvav 228,176,76\n"
"MU 153,153,86\nCV 106,152,153\nMvav 236,180,76\nRa 164,2,9216\nMU 154,154,86\nMU 155,155,86\nWL 8\nCV 107,154,155\nMU 156,156,87\nRa 168,2,10240\nMU 157,157,87\nCV 110,156,157\n"
"Mvav 244,184,76\nD1 1,16,46,2048\nMU 158,158,87\nMvav 252,188,76\nRa 172,2,11264\nMU 159,159,87\nCV 111,158,159\nMU 160,160,86\nMU 161,161,86\nCV 112,160,161\nZvav 128,128,96\n"
"Ra 176,2,12288\nMU 162,162,86\nMU 163,163,86\nMvav 128,132,104\nCV 113,162,163\nMU 164,164,87\nZvav 132,132,108\nRa 180,2,13312\nMU 165,165,87\nCV 116,164,165\nMvav 132,128,100\n"
"s_waitcnt vmcnt(15) lgkmcnt(8)\nMU 166,166,87\nMU 167,167,87\nZvav 140,136,100\nRa 184,2,14336\nCV 117,166,167\nMU 168,168,86\nMvav 140,140,108\nD1 1,16,46,3072\nMU 169,169,86\n"
"CV 114,168,169\nZvav 136,140,104\nRa 188,2,15360\ns_barrier\nMU 170,170,86\nMvav 136,136,96\nMU 171,171,86\nCV 115,170,171\nMU 172,172,87\nZvav 144,144,96\nRa 128,2,32768\n"
"MU 173,173,87\nCV 118,172,173\nMvav 144,148,104\nMU 174,174,87\nMU 175,175,87\nbuffer_store_dword v25, off, s[16:19], s54 offset:512 sc0 sc1\nZvav 148,148,108\nRa 132,2,33792\n"
"CV 119,174,175\nMU 176,176,86\nMvav 148,144,100\nWL 8\ns_add_u32 m0, s89, 49152\ns_add_u32 s46, s96, 1179648\nZvav 156,152,100\nRa 136,2,34816\nMU 177,177,86\nCV 120,176,177\n"
"Mvav 156,156,108\nD1 1,16,46,0\nMU 178,178,86\nMU 179,179,86\nZvav 152,156,104\nRa 140,2,35840\nCV 121,178,179\nMU 180,180,87\nMvav 152,152,96\nMU 181,181,87\nCV 124,180,181\n"
"MU 182,182,87\nZvav 64,160,96\nRa 144,2,36864\nMU 183,183,87\nCV 125,182,183\nMvav 64,164,104\nMU 184,184,86\nMU 185,185,86\nZvav 68,164,108\nRa 148,2,37888\nCV 122,184,185\n"
"MU 186,186,86\nMvav 68,160,100\nWL 8\nMU 187,187,86\nCV 123,186,187\nZvav 76,168,100\nRa 152,2,38912\nMU 188,188,87\nMU 189,189,87\nMvav 76,172,108\nD1 1,16,46,1024\n"
"CV 126,188,189\nMU 190,190,87\nZvav 72,172,104\nRa 156,2,39936\nMU 191,191,87\nCV 127,190,191\nMvav 72,168,96\nMU 192,192,86\nMU 193,193,86\nCV 32,192,193\nMvav 128,176,112\n"
"Ra 160,2,40960\nMU 194,194,86\nMU 195,195,86\nMvav 128,180,120\nCV 33,194,195\nMU 196,196,87\nMU 197,197,87\nMvav 132,180,124\nRa 164,2,41984\nCV 36,196,197\nMU 198,198,87\n"
"Mvav 132,176,116\nWL 8\nMU 199,199,87\nMvav 140,184,116\nRa 168,2,43008\nCV 37,198,199\nMU 200,200,86\nMvav 140,188,124\nD1 1,16,46,2048\nMU 201,201,86\nCV 34,200,201\n"
"Mvav 136,188,120\nRa 172,2,44032\nMU 202,202,86\nMU 203,203,86\nMvav 136,184,112\nCV 35,202,203\nMU 204,204,87\nMU 205,205,87\nMvav 144,128,112\nRa 176,2,45056\nCV 38,204,205\n"
"MU 206,206,87\nMvav 144,132,120\nMU 207,207,87\nCV 39,206,207\nMvav 148,132,124\nRa 180,2,46080\nMU 208,208,86\nMU 209,209,86\nMvav 148,128,116\nWL 8\nCV 40,208,209\n"
"MU 210,210,86\nMvav 156,136,116\nRa 184,2,47104\nMU 211,211,86\nCV 41,210,211\nMvav 156,140,124\nD1 1,16,46,3072\nMU 212,212,87\nMU 213,213,87\nMvav 152,140,120\nRa 188,2,48128\n"
"CV 44,212,213\nMvav 152,136,112\nMU 214,214,87\nMU 215,215,87\nCV 45,214,215\nMvav 64,144,112\nRa 128,2,49152\nMU 216,216,86\nMU 217,217,86\nMvav 64,148,120\nCV 42,216,217\n"
"MU 218,218,86\nMU 219,219,86\nMvav 68,148,124\nRa 132,2,50176\nCV 43,218,219\nMU 220,220,87\nMvav 68,144,116\nWL 8\ns_add_u32 m0, s89, 147456\ns_add_u32 s46, s96, 1245184\n"
"Mvav 76,152,116\nRa 136,2,51200\nMU 221,221,87\nCV 46,220,221\nMvav 76,156,124\nD1 1,16,46,0\nMU 222,222,87\nMU 223,223,87\nMvav 72,156,120\nRa 140,2,52224\nCV 47,222,223\n"
"Mvav 72,152,112\nMU 224,224,86\nMU 225,225,86\nCV 48,224,225\nMvav 128,160,32\nRa 144,2,53248\nMU 226,226,86\nMU 227,227,86\nMvav 128,164,40\nCV 49,226,227\nMU 228,228,87\n"
"Mvav 132,164,44\nRa 148,2,54272\nMU 229,229,87\nCV 52,228,229\nMvav 132,160,36\nWL 8\nMU 230,230,87\nMU 231,231,87\nMvav 140,168,36\nRa 152,2,55296\nCV 53,230,231\nMU 232,232,86\n"
"Mvav 140,172,44\nD1 1,16,46,1024\nMU 233,233,86\nCV 50,232,233\nMvav 136,172,40\nRa 156,2,56320\nMU 234,234,86\nMvav 136,168,32\nMU 235,235,86\nCV 51,234,235\nMU 236,236,87\n"
"Mvav 144,176,32\nRa 160,2,57344\nMU 237,237,87\nCV 54,236,237\nMvav 144,180,40\nMU 238,238,87\nMU 239,239,87\nCV 55,238,239\nMvav 148,180,44\nRa 164,2,58368\nMU 240,240,86\n"
"MU 241,241,86\nMvav 148,176,36\nWL 8\nCV 56,240,241\nMvav 156,184,36\nRa 168,2,59392\nMU 242,242,86\nMU 243,243,86\nMvav 156,188,44\nD1 1,16,46,2048\nCV 57,242,243\nMU 244,244,87\n"
"Mvav 152,188,40\nRa 172,2,60416\nMU 245,245,87\nCV 60,244,245\nMvav 152,184,32\nMU 246,246,87\nMU 247,247,87\nCV 61,246,247\nMvav 64,128,32\nRa 176,2,61440\nMU 248,248,86\n"
"MU 249,249,86\nMvav 64,132,40\nCV 58,248,249\nMU 250,250,86\nMvav 68,132,44\nRa 180,2,62464\nMU 251,251,86\nCV 59,250,251\nMvav 68,128,36\ns_waitcnt vmcnt(16) lgkmcnt(8)\n"
"MU 252,252,87\nMU 253,253,87\nMvav 76,136,36\nRa 184,2,63488\nCV 62,252,253\nMU 254,254,87\nMvav 76,140,44\nD1 1,16,46,3072\nMU 255,255,87\nCV 63,254,255\nMvav 72,140,40\n"
"Ra 188,2,64512\ns_barrier\nMvav 72,136,32\nMvav 128,144,48\nRa 128,3,0\nMvav 128,148,56\nMvav 132,148,60\nRa 132,3,1024\nMvav 132,144,52\nWL 8\nMvav 140,152,52\nRa 136,3,2048\n"
"Mvav 140,156,60\nMvav 136,156,56\nRa 140,3,3072\nMvav 136,152,48\nMvav 144,160,48\nRa 144,3,4096\nMvav 144,164,56\nMvav 148,164,60\nRa 148,3,5120\nCV 132,132,133\nMvav 148,160,52\n"
"WL 8\nCV 133,134,135\nMvav 156,168,52\nRa 152,3,6144\nCV 134,140,141\nMvav 156,172,60\nCV 135,142,143\ns_add_u32 s5, s94, 0\nMvav 152,172,56\nRa 156,3,7168\nSv 132,1,16,5,1024\n"
"Mvav 152,168,48\nCV 128,128,129\nCV 129,130,131\nMvav 64,176,48\nRa 160,3,8192\nCV 130,136,137\nMvav 64,180,56\nbuffer_load_dword v27, off, s[16:19], s60 offset:512 sc0 sc1\n"
"CV 131,138,139\nMvav 68,180,60\nRa 164,3,9216\nSv 128,1,16,5,0\nMvav 68,176,52\nWL 6\nCV 148,148,149\nMvav 76,184,52\nRa 168,3,10240\nCV 149,150,151\nMvav 76,188,60\n"
"CV 150,156,157\nCV 151,158,159\nMvav 72,188,56\nRa 172,3,11264\ns_add_u32 s5, s94, 131072\nMvav 72,184,48\nSv 148,1,16,5,1024\nCV 144,144,145\nZvav 160,128,96\nRa 176,3,12288\n"
"CV 145,146,147\nMvav 160,132,104\nCV 146,152,153\nCV 147,154,155\nMvav 160,136,112\nRa 180,3,13312\nSv 144,1,16,5,0\nMvav 160,140,120\nWL 6\nCV 68,68,69\nZvav 164,140,124\n"
"Ra 184,3,14336\nCV 69,70,71\nMvav 164,136,116\nCV 70,76,77\nCV 71,78,79\nMvav 164,132,108\nRa 188,3,15360\ns_add_u32 s5, s94, 262144\nMvav 164,128,100\nSv 68,1,16,5,1024\n"
"CV 64,64,65\nMvav 164,144,36\nRa 128,3,16384\nCV 65,66,67\nMvav 164,148,44\nCV 66,72,73\nCV 67,74,75\nMvav 164,152,52\nRa 132,3,17408\nSv 64,1,16,5,0\nMvav 164,156,60\nWL 6\n"
"Mvav 160,156,56\nRa 136,3,18432\nMvav 160,152,48\nMvav 160,148,40\nRa 140,3,19456\nMvav 160,144,32\nZvav 168,160,96\nRa 144,3,20480\nMvav 168,164,104\nMvav 168,168,112\n"
"Ra 148,3,21504\nMvav 168,172,120\nWL 6\nZvav 172,172,124\nRa 152,3,22528\nMvav 172,168,116\nMvav 172,164,108\nRa 156,3,23552\nMvav 172,160,100\nMvav 172,176,36\nRa 160,3,24576\n"
"Mvav 172,180,44\nMvav 172,184,52\nRa 164,3,25600\nMvav 172,188,60\nWL 6\nMvav 168,188,56\nRa 168,3,26624\nMvav 168,184,48\nMvav 168,180,40\nRa 172,3,27648\nMvav 168,176,32\n"
"Zvav 176,128,96\nRa 176,3,28672\nCV 164,164,165\nMvav 176,132,104\nCV 165,166,167\nCV 166,172,173\nMvav 176,136,112\nRa 180,3,29696\nCV 167,174,175\nMvav 176,140,120\n"
"s_waitcnt vmcnt(11) lgkmcnt(6)\ns_add_u32 s5, s94, 393216\nZvav 180,140,124\nRa 184,3,30720\nSv 164,1,16,5,1024\nMvav 180,136,116\nCV 160,160,161\nCV 161,162,163\n"
"Mvav 180,132,108\nRa 188,3,31744\ns_barrier\nMvav 180,128,100\nCV 162,168,169\nCV 163,170,171\nMvav 180,144,36\nRa 128,3,32768\nSv 160,1,16,5,0\nMvav 180,148,44\nMvav 180,152,52\n"
"Ra 132,3,33792\n.W591:\ns_cbranch_execz .WB414\nMvav 180,156,60\nWL 6\ns_add_u32 m0, s89, 81920\ns_add_u32 s46, s96, 1277952\nMvav 176,156,56\nRa 136,3,34816\nMvav 176,152,48\n"
"D1 1,16,46,0\nMvav 176,148,40\nRa 140,3,35840\nMvav 176,144,32\nZvav 184,160,96\nRa 144,3,36864\nMvav 184,164,104\nMvav 184,168,112\nRa 148,3,37888\nMvav 184,172,120\nWL 6\n"
"Zvav 188,172,124\nRa 152,3,38912\nMvav 188,168,116\nD1 1,16,46,1024\nMvav 188,164,108\nRa 156,3,39936\nMvav 188,160,100\nMvav 188,176,36\nRa 160,3,40960\nWV 9\n"
"v_cmp_ne_u32 vcc, s15, v27\ns_nop 0\ns_cbranch_vccnz .S518\n.S519:\nMvav 188,180,44\ns_add_u32 s4, s92, 0\nMvav 188,184,52\nRa 164,3,41984\nMvav 188,188,60\nWL 6\n"
"buffer_load_dwordx4 v[208:211], v1, s[16:19], s4 offen offset:0 sc0 sc1\nMvav 184,188,56\nRa 168,3,43008\nMvav 184,184,48\nD1 1,16,46,2048\nMvav 184,180,40\nRa 172,3,44032\n"
"Mvav 184,176,32\nbuffer_load_dwordx4 v[212:215], v1, s[16:19], s4 offen offset:1024 sc0 sc1\nZvav 192,128,96\nRa 176,3,45056\nv_lshrrev_b32 v88, 8, v1\nMvav 192,132,104\n"
"v_lshlrev_b32 v89, 5, v88\nCV 180,180,181\nMvav 192,136,112\nRa 180,3,46080\nCV 181,182,183\nMvav 192,140,120\nWL 6\nCV 182,188,189\nZvav 196,140,124\nRa 184,3,47104\n"
"v_bfe_u32 v90, v1, 4, 4\nMvav 196,136,116\nD1 1,16,46,3072\nCV 183,190,191\nMvav 196,132,108\nRa 188,3,48128\ns_add_u32 s5, s94, 524288\nMvav 196,128,100\n"
"v_mul_u32_u24 v91, 896, v90\nSv 180,1,16,5,1024\nMvav 196,144,36\nRa 128,3,49152\nv_lshl_add_u32 v91, v88, 4, v91\nMvav 196,148,44\nv_add_u32 v90, s21, v90\nCV 176,176,177\n"
"Mvav 196,152,52\nRa 132,3,50176\nCV 177,178,179\nMvav 196,156,60\nWL 6\ns_add_u32 m0, s89, 65536\ns_add_u32 s46, s96, 1310720\nMvav 192,156,56\nRa 136,3,51200\n"
"v_lshlrev_b32 v88, 3, v90\nMvav 192,152,48\nD1 1,16,46,0\nCV 178,184,185\nMvav 192,148,40\nRa 140,3,52224\nCV 179,186,187\nMvav 192,144,32\nv_add_u32 v94, 64, v90\n"
"Sv 176,1,16,5,0\nZvav 200,160,96\nRa 144,3,53248\ns_mov_b32 s63, 0xc040000\nMvav 200,164,104\nv_lshrrev_b32 v92, 8, v1\nMvav 200,168,112\nRa 148,3,54272\nMvav 200,172,120\nWL 6\n"
"Zvav 204,172,124\nRa 152,3,55296\nv_bfe_u32 v93, v1, 6, 2\nMvav 204,168,116\nD1 1,16,46,1024\nv_cmp_eq_u32 vcc, v92, v93\nv_and_b32 v92, 16, v1\nv_mov_b32 v93, 0x3f80\n"
"v_cndmask_b32 v93, 0, v93, vcc\nMvav 204,164,108\nRa 156,3,56320\nMvav 204,160,100\nv_lshlrev_b32 v93, v92, v93\nMvav 204,176,36\nRa 160,3,57344\nv_and_b32 v92, 32, v1\n"
"Mvav 204,180,44\ns_add_u32 s4, s92, 131072\nv_cmp_eq_u32 vcc, 0, v92\nv_mov_b32 v4, 0\nv_mov_b32 v5, 0\nv_cndmask_b32 v6, 0, v93, vcc\nMvav 204,184,52\nRa 164,3,58368\n"
"Mvav 204,188,60\nWL 6\nbuffer_load_dwordx4 v[216:219], v1, s[16:19], s4 offen offset:0 sc0 sc1\nMvav 200,188,56\nRa 168,3,59392\nv_sub_u32 v7, v93, v6\nMvav 200,184,48\n"
"D1 1,16,46,2048\nMvav 200,180,40\nRa 172,3,60416\nMvav 200,176,32\nbuffer_load_dwordx4 v[220:223], v1, s[16:19], s4 offen offset:1024 sc0 sc1\nZvav 160,128,96\nRa 176,3,61440\n"
"v_mov_b32 v8, 0\nMvav 160,132,104\nv_mov_b32 v9, 0\nCV 196,196,197\nMvav 160,136,112\nRa 180,3,62464\nCV 197,198,199\nMvav 160,140,120\nWL 6\nCV 198,204,205\nZvav 164,140,124\n"
"Ra 184,3,63488\nCV 199,206,207\nMvav 164,136,116\nD1 1,16,46,3072\ns_add_u32 s5, s94, 655360\nMvav 164,132,108\nRa 188,3,64512\ns_barrier\nMvav 164,128,100\nSv 196,1,16,5,1024\n"
"CV 192,192,193\nMvav 164,144,36\nRa 128,0,0\nCV 193,194,195\nMvav 164,148,44\nCV 194,200,201\nCV 195,202,203\nMvav 164,152,52\nRa 132,0,1024\nSv 192,1,16,5,0\nMvav 164,156,60\n"
"WL 6\ns_add_u32 m0, s89, 98304\ns_add_u32 s46, s96, 1343488\nMvav 160,156,56\nRa 136,0,2048\nMvav 160,152,48\nD1 1,16,46,0\nMvav 160,148,40\nRa 140,0,3072\nMvav 160,144,32\n"
"Zvav 168,160,96\nRa 144,0,4096\nWV 12\nv_max_u32 v21, v211, v215\nv_cmp_eq_u32 vcc, -1, v21\ns_nop 0\ns_cbranch_vccnz .S494\n.S495:\nMvav 168,164,104\n"
"v_lshlrev_b32 v176, 16, v208\nMvav 168,168,112\nRa 148,0,5120\nMvav 168,172,120\nWL 6\nv_and_b32 v177, 0xffff0000, v208\nZvav 172,172,124\nRa 152,0,6144\nMvav 172,168,116\n"
"D1 1,16,46,1024\nMvav 172,164,108\nRa 156,0,7168\nv_lshlrev_b32 v178, 16, v209\nMvav 172,160,100\nv_and_b32 v179, 0xffff0000, v209\nMvav 172,176,36\nRa 160,0,8192\n"
"v_lshlrev_b32 v180, 16, v212\nMvav 172,180,44\nv_and_b32 v181, 0xffff0000, v212\ns_add_u32 s4, s92, 262144\nMvav 172,184,52\nRa 164,0,9216\nMvav 172,188,60\nWL 6\n"
"v_lshlrev_b32 v182, 16, v213\nbuffer_load_dwordx4 v[224:227], v1, s[16:19], s4 offen offset:0 sc0 sc1\nMvav 168,188,56\nRa 168,0,10240\nMvav 168,184,48\nD1 1,16,46,2048\n"
"Mvav 168,180,40\nRa 172,0,11264\nv_and_b32 v183, 0xffff0000, v213\nMvav 168,176,32\nv_lshlrev_b32 v184, 16, v210\n"
"buffer_load_dwordx4 v[228:231], v1, s[16:19], s4 offen offset:1024 sc0 sc1\nMvav 176,128,96\nRa 176,0,12288\nv_and_b32 v185, 0xffff0000, v210\nMvav 176,132,104\n"
"v_lshlrev_b32 v186, 16, v211\nCV 164,164,165\nMvav 176,136,112\nRa 180,0,13312\nCV 165,166,167\nMvav 176,140,120\nWL 6\nCV 166,172,173\nMvav 180,140,124\nRa 184,0,14336\n"
"v_and_b32 v187, 0xffff0000, v211\nMvav 180,136,116\nD1 1,16,46,3072\nCV 167,174,175\nMvav 180,132,108\nRa 188,0,15360\ns_add_u32 s5, s94, 786432\nMvav 180,128,100\n"
"v_lshlrev_b32 v188, 16, v214\nSv 164,1,16,5,1024\nMvav 180,144,36\nRa 128,0,16384\nv_and_b32 v189, 0xffff0000, v214\nMvav 180,148,44\nv_lshlrev_b32 v190, 16, v215\nCV 160,160,161\n"
"Mvav 180,152,52\nRa 132,0,17408\nCV 161,162,163\nMvav 180,156,60\nWL 6\ns_add_u32 m0, s89, 114688\ns_add_u32 s46, s96, 1376256\nMvav 176,156,56\nRa 136,0,18432\n"
"v_and_b32 v191, 0xffff0000, v215\nMvav 176,152,48\nD1 1,16,46,0\nCV 162,168,169\nMvav 176,148,40\nRa 140,0,19456\nCV 163,170,171\nMvav 176,144,32\nSv 160,1,16,5,0\n"
"Mvav 184,160,96\nRa 144,0,20480\nWV 12\nv_max_u32 v21, v219, v223\nv_cmp_eq_u32 vcc, -1, v21\ns_nop 0\ns_cbranch_vccnz .S497\n.S498:\nMvav 184,164,104\n"
"v_lshlrev_b32 v192, 16, v216\nMvav 184,168,112\nRa 148,0,21504\nMvav 184,172,120\nWL 6\nv_and_b32 v193, 0xffff0000, v216\nMvav 188,172,124\nRa 152,0,22528\nMvav 188,168,116\n"
"D1 1,16,46,1024\nMvav 188,164,108\nRa 156,0,23552\nv_lshlrev_b32 v194, 16, v217\nMvav 188,160,100\nv_and_b32 v195, 0xffff0000, v217\nMvav 188,176,36\nRa 160,0,24576\n"
"v_lshlrev_b32 v196, 16, v220\nMvav 188,180,44\nv_and_b32 v197, 0xffff0000, v220\ns_add_u32 s4, s92, 393216\nMvav 188,184,52\nRa 164,0,25600\nMvav 188,188,60\nWL 6\n"
"v_lshlrev_b32 v198, 16, v221\nbuffer_load_dwordx4 v[208:211], v1, s[16:19], s4 offen offset:0 sc0 sc1\nMvav 184,188,56\nRa 168,0,26624\nMvav 184,184,48\nD1 1,16,46,2048\n"
"Mvav 184,180,40\nRa 172,0,27648\nv_and_b32 v199, 0xffff0000, v221\nMvav 184,176,32\nv_lshlrev_b32 v200, 16, v218\n"
"buffer_load_dwordx4 v[212:215], v1, s[16:19], s4 offen offset:1024 sc0 sc1\nMvav 192,128,96\nRa 176,0,28672\nv_and_b32 v201, 0xffff0000, v218\nMvav 192,132,104\n"
"v_lshlrev_b32 v202, 16, v219\nCV 180,180,181\nMvav 192,136,112\nRa 180,0,29696\nCV 181,182,183\nMvav 192,140,120\ns_waitcnt vmcnt(15) lgkmcnt(6)\nCV 182,188,189\nMvav 196,140,124\n"
"Ra 184,0,30720\nv_and_b32 v203, 0xffff0000, v219\nMvav 196,136,116\nD1 1,16,46,3072\nCV 183,190,191\nMvav 196,132,108\nRa 188,0,31744\ns_barrier\nMvav 196,128,100\n"
"v_lshlrev_b32 v204, 16, v222\ns_add_u32 s5, s94, 917504\nMvav 196,144,36\nRa 128,2,16384\nv_and_b32 v205, 0xffff0000, v222\nMvav 196,148,44\nv_lshlrev_b32 v206, 16, v223\n"
"Sv 180,1,16,5,1024\nMvav 196,152,52\nRa 132,2,17408\nCV 176,176,177\nMvav 196,156,60\nWL 6\ns_add_u32 m0, s89, 0\ns_add_u32 s46, s96, 1409024\nMvav 192,156,56\nRa 136,2,18432\n"
"v_and_b32 v207, 0xffff0000, v223\nMvav 192,152,48\nD1 1,16,46,0\nCV 177,178,179\nMvav 192,148,40\nRa 140,2,19456\nCV 178,184,185\nMvav 192,144,32\nCV 179,186,187\nSv 176,1,16,5,0\n"
"Mvav 200,160,96\nRa 144,2,20480\nWV 12\nv_max_u32 v21, v227, v231\nv_cmp_eq_u32 vcc, -1, v21\ns_nop 0\ns_cbranch_vccnz .S500\n.S501:\nMvav 200,164,104\n"
"v_lshlrev_b32 v160, 16, v224\nMvav 200,168,112\nRa 148,2,21504\nMvav 200,172,120\nWL 6\nv_and_b32 v161, 0xffff0000, v224\nMvav 204,172,124\nRa 152,2,22528\nMvav 204,168,116\n"
"D1 1,16,46,1024\nMvav 204,164,108\nRa 156,2,23552\nv_lshlrev_b32 v162, 16, v225\nMvav 204,160,100\nv_and_b32 v163, 0xffff0000, v225\nMvav 204,176,36\nRa 160,2,24576\n"
"v_lshlrev_b32 v164, 16, v228\nMvav 204,180,44\nv_and_b32 v165, 0xffff0000, v228\ns_add_u32 s4, s92, 524288\nMvav 204,184,52\nRa 164,2,25600\nMvav 204,188,60\nWL 6\n"
"v_lshlrev_b32 v166, 16, v229\nbuffer_load_dwordx4 v[216:219], v1, s[16:19], s4 offen offset:0 sc0 sc1\nMvav 200,188,56\nRa 168,2,26624\nMvav 200,184,48\nD1 1,16,46,2048\n"
"Mvav 200,180,40\nRa 172,2,27648\nv_and_b32 v167, 0xffff0000, v229\nMvav 200,176,32\nv_lshlrev_b32 v168, 16, v226\n"
"buffer_load_dwordx4 v[220:223], v1, s[16:19], s4 offen offset:1024 sc0 sc1\nMvav 160,128,96\nRa 176,2,28672\nv_and_b32 v169, 0xffff0000, v226\nMvav 160,132,104\n"
"v_lshlrev_b32 v170, 16, v227\nCV 196,196,197\nMvav 160,136,112\nRa 180,2,29696\nCV 197,198,199\nMvav 160,140,120\nWL 6\nCV 198,204,205\nMvav 164,140,124\nRa 184,2,30720\n"
"v_and_b32 v24, 0x70, v1\nMvav 164,136,116\nD1 1,16,46,3072\nCV 199,206,207\nMvav 164,132,108\nRa 188,2,31744\nv_and_b32 v171, 0xffff0000, v227\nMvav 164,128,100\n"
"v_lshlrev_b32 v172, 16, v230\ns_add_u32 s5, s94, 1048576\nMvav 164,144,36\nRa 128,2,0\nv_lshlrev_b32 v24, 5, v24\nMvav 164,148,44\nv_and_b32 v173, 0xffff0000, v230\n"
"Sv 196,1,16,5,1024\nMvav 164,152,52\nRa 132,2,1024\nCV 192,192,193\nMvav 164,156,60\nWL 6\nv_lshlrev_b32 v174, 16, v231\nMvav 160,156,56\nRa 136,2,2048\n"
"buffer_load_dword v27, v24, s[16:19], s41 offen sc0 sc1\nMvav 160,152,48\nv_and_b32 v175, 0xffff0000, v231\nCV 193,194,195\nMvav 160,148,40\nRa 140,2,3072\nCV 194,200,201\n"
"Mvav 160,144,32\nCV 195,202,203\nSv 192,1,16,5,0\nMvav 168,160,96\nRa 144,2,4096\nWV 12\nv_max_u32 v21, v211, v215\nv_cmp_eq_u32 vcc, -1, v21\ns_nop 0\ns_cbranch_vccnz .S503\n"
".S504:\nMvav 168,164,104\nv_lshlrev_b32 v176, 16, v208\nMvav 168,168,112\nRa 148,2,5120\nMvav 168,172,120\nWL 6\nMvav 172,172,124\nRa 152,2,6144\nv_and_b32 v177, 0xffff0000, v208\n"
"Mvav 172,168,116\nv_lshlrev_b32 v178, 16, v209\nMvav 172,164,108\nRa 156,2,7168\nMvav 172,160,100\nv_and_b32 v179, 0xffff0000, v209\nMvav 172,176,36\nRa 160,2,8192\n"
"v_lshlrev_b32 v180, 16, v212\nMvav 172,180,44\nv_and_b32 v181, 0xffff0000, v212\ns_add_u32 s4, s92, 655360\nMvav 172,184,52\nRa 164,2,9216\nMvav 172,188,60\nWL 6\n"
"buffer_load_dwordx4 v[224:227], v1, s[16:19], s4 offen offset:0 sc0 sc1\nMvav 168,188,56\nRa 168,2,10240\nv_lshlrev_b32 v182, 16, v213\nMvav 168,184,48\n"
"v_and_b32 v183, 0xffff0000, v213\nMvav 168,180,40\nRa 172,2,11264\nMvav 168,176,32\nv_lshlrev_b32 v184, 16, v210\n"
"buffer_load_dwordx4 v[228:231], v1, s[16:19], s4 offen offset:1024 sc0 sc1\nMvav 176,128,96\nRa 176,2,12288\nv_and_b32 v185, 0xffff0000, v210\nMvav 176,132,104\n"
"v_lshlrev_b32 v186, 16, v211\nCV 164,164,165\nMvav 176,136,112\nRa 180,2,13312\nCV 165,166,167\nMvav 176,140,120\ns_waitcnt vmcnt(13) lgkmcnt(6)\nCV 166,172,173\nMvav 180,140,124\n"
"Ra 184,2,14336\nCV 167,174,175\nMvav 180,136,116\nv_and_b32 v187, 0xffff0000, v211\ns_add_u32 s5, s94, 1179648\nMvav 180,132,108\nRa 188,2,15360\ns_barrier\nMvav 180,128,100\n"
"v_lshlrev_b32 v188, 16, v214\nSv 164,1,16,5,1024\nMvav 180,144,36\nRa 128,2,32768\nv_and_b32 v189, 0xffff0000, v214\nMvav 180,148,44\nv_lshlrev_b32 v190, 16, v215\nCV 160,160,161\n"
"Mvav 180,152,52\nRa 132,2,33792\nCV 161,162,163\nMvav 180,156,60\nWL 6\nCV 162,168,169\nMvav 176,156,56\nRa 136,2,34816\nCV 163,170,171\nMvav 176,152,48\n"
"v_and_b32 v191, 0xffff0000, v215\nSv 160,1,16,5,0\nMvav 176,148,40\nRa 140,2,35840\nMvav 176,144,32\nMvav 184,160,96\nRa 144,2,36864\nWV 8\nv_max_u32 v21, v219, v223\n"
"v_cmp_eq_u32 vcc, -1, v21\ns_nop 0\ns_cbranch_vccnz .S506\n.S507:\nMvav 184,164,104\nv_lshlrev_b32 v192, 16, v216\nMvav 184,168,112\nRa 148,2,37888\nMvav 184,172,120\nWL 6\n"
"Mvav 188,172,124\nRa 152,2,38912\n.W592:\ns_cbranch_execz .WB414\nv_and_b32 v193, 0xffff0000, v216\nMvav 188,168,116\nv_lshlrev_b32 v194, 16, v217\nMvav 188,164,108\n"
"Ra 156,2,39936\nMvav 188,160,100\nv_and_b32 v195, 0xffff0000, v217\nMvav 188,176,36\nRa 160,2,40960\nv_lshlrev_b32 v196, 16, v220\nMvav 188,180,44\n"
"v_and_b32 v197, 0xffff0000, v220\ns_add_u32 s4, s92, 786432\nMvav 188,184,52\nRa 164,2,41984\nMvav 188,188,60\nWL 6\n"
"buffer_load_dwordx4 v[208:211], v1, s[16:19], s4 offen offset:0 sc0 sc1\nMvav 184,188,56\nRa 168,2,43008\nv_lshlrev_b32 v198, 16, v221\nMvav 184,184,48\n"
"v_and_b32 v199, 0xffff0000, v221\nMvav 184,180,40\nRa 172,2,44032\nMvav 184,176,32\nv_lshlrev_b32 v200, 16, v218\n"
"buffer_load_dwordx4 v[212:215], v1, s[16:19], s4 offen offset:1024 sc0 sc1\nMvav 192,128,96\nRa 176,2,45056\nv_and_b32 v201, 0xffff0000, v218\nMvav 192,132,104\n"
"v_lshlrev_b32 v202, 16, v219\nCV 180,180,181\nMvav 192,136,112\nRa 180,2,46080\nCV 181,182,183\nMvav 192,140,120\nWL 6\nCV 182,188,189\nMvav 196,140,124\nRa 184,2,47104\n"
"CV 183,190,191\nMvav 196,136,116\nv_and_b32 v203, 0xffff0000, v219\ns_add_u32 s5, s94, 1310720\nMvav 196,132,108\nRa 188,2,48128\nSv 180,1,16,5,1024\nMvav 196,128,100\n"
"v_lshlrev_b32 v204, 16, v222\nCV 176,176,177\nMvav 196,144,36\nRa 128,2,49152\nv_and_b32 v205, 0xffff0000, v222\nMvav 196,148,44\nv_lshlrev_b32 v206, 16, v223\nCV 177,178,179\n"
"Mvav 196,152,52\nRa 132,2,50176\nCV 178,184,185\nMvav 196,156,60\nWL 6\nCV 179,186,187\nMvav 192,156,56\nRa 136,2,51200\nSv 176,1,16,5,0\nMvav 192,152,48\n"
"v_and_b32 v207, 0xffff0000, v223\nMvav 192,148,40\nRa 140,2,52224\nMvav 192,144,32\nMvav 200,160,96\nRa 144,2,53248\nWV 6\nv_max_u32 v21, v227, v231\nv_cmp_eq_u32 vcc, -1, v21\n"
"s_nop 0\ns_cbranch_vccnz .S509\n.S510:\nMvav 200,164,104\nv_lshlrev_b32 v160, 16, v224\nv_xor_b32 v27, s15, v27\nv_and_b32 v26, 0xffffdfff, v27\nv_cmp_ne_u32 s[64:65], 0, v27\n"
"v_cmp_ne_u32 vcc, 0, v26\ns_nop 0\ns_cbranch_vccnz .S521\n.S522:\nMvav 200,168,112\nRa 148,2,54272\ns_bitcmp1_b32 s64, 0\ns_cselect_b32 s2, 0x2000, 0\nMvav 200,172,120\nWL 6\n"
"Mvav 204,172,124\nRa 152,2,55296\nv_and_b32 v161, 0xffff0000, v224\nMvav 204,168,116\nv_lshlrev_b32 v162, 16, v225\nMvav 204,164,108\nRa 156,2,56320\ns_xor_b32 s2, s2, s20\n"
"Mvav 204,160,100\nv_and_b32 v163, 0xffff0000, v225\nMvav 204,176,36\nRa 160,2,57344\nv_lshlrev_b32 v164, 16, v228\nMvav 204,180,44\nv_and_b32 v165, 0xffff0000, v228\n"
"Mvav 204,184,52\nRa 164,2,58368\nMvav 204,188,60\nWL 6\ns_add_u32 s24, s2, 0x0\nMvav 200,188,56\nRa 168,2,59392\nv_lshlrev_b32 v166, 16, v229\nMvav 200,184,48\n"
"v_and_b32 v167, 0xffff0000, v229\ns_bitcmp1_b32 s64, 1\ns_cselect_b32 s2, 0x2000, 0\nMvav 200,180,40\nRa 172,2,60416\nMvav 200,176,32\nv_lshlrev_b32 v168, 16, v226\n"
"Mvav 160,128,96\nRa 176,2,61440\nv_and_b32 v169, 0xffff0000, v226\nMvav 160,132,104\nv_lshlrev_b32 v170, 16, v227\nCV 196,196,197\nMvav 160,136,112\nRa 180,2,62464\n"
"CV 197,198,199\nMvav 160,140,120\nWL 6\nCV 198,204,205\nMvav 164,140,124\nRa 184,2,63488\nCV 199,206,207\nMvav 164,136,116\nv_and_b32 v171, 0xffff0000, v227\n"
"s_add_u32 s5, s94, 1441792\nMvav 164,132,108\nRa 188,2,64512\ns_barrier\nMvav 164,128,100\nv_lshlrev_b32 v172, 16, v230\nSv 196,1,16,5,1024\nMvav 164,144,36\nRa 128,3,0\n"
"v_and_b32 v173, 0xffff0000, v230\nMvav 164,148,44\nv_lshlrev_b32 v174, 16, v231\nCV 192,192,193\nMvav 164,152,52\nRa 132,3,1024\nCV 193,194,195\nMvav 164,156,60\nWL 6\n"
"s_xor_b32 s2, s2, s20\nMvav 160,156,56\nRa 136,3,2048\nCV 194,200,201\nMvav 160,152,48\nv_and_b32 v175, 0xffff0000, v231\nCV 195,202,203\nMvav 160,148,40\nRa 140,3,3072\n"
"Sv 192,1,16,5,0\nMvav 160,144,32\ns_add_u32 s25, s2, 0x1800000\ns_bitcmp1_b32 s64, 2\ns_cselect_b32 s2, 0x2000, 0\nMvav 168,160,96\nRa 144,3,4096\nWV 4\nv_max_u32 v21, v211, v215\n"
"v_cmp_eq_u32 vcc, -1, v21\ns_nop 0\ns_cbranch_vccnz .S512\n.S513:\nMvav 168,164,104\nv_lshlrev_b32 v176, 16, v208\nMvav 168,168,112\nRa 148,3,5120\nMvav 168,172,120\nWL 6\n"
"Mvav 172,172,124\nRa 152,3,6144\nv_and_b32 v177, 0xffff0000, v208\nMvav 172,168,116\nv_lshlrev_b32 v178, 16, v209\nMvav 172,164,108\nRa 156,3,7168\ns_xor_b32 s2, s2, s20\n"
"Mvav 172,160,100\nv_and_b32 v179, 0xffff0000, v209\nMvav 172,176,36\nRa 160,3,8192\nv_lshlrev_b32 v180, 16, v212\nMvav 172,180,44\nv_and_b32 v181, 0xffff0000, v212\n"
"Mvav 172,184,52\nRa 164,3,9216\nMvav 172,188,60\nWL 6\ns_add_u32 s26, s2, 0x3000000\nMvav 168,188,56\nRa 168,3,10240\nv_lshlrev_b32 v182, 16, v213\nMvav 168,184,48\n"
"v_and_b32 v183, 0xffff0000, v213\ns_bitcmp1_b32 s64, 3\ns_cselect_b32 s2, 0x2000, 0\nMvav 168,180,40\nRa 172,3,11264\nMvav 168,176,32\nv_lshlrev_b32 v184, 16, v210\n"
"Mvav 176,128,96\nRa 176,3,12288\ns_xor_b32 s2, s2, s20\nMvav 176,132,104\nv_and_b32 v185, 0xffff0000, v210\nCV 164,164,165\nMvav 176,136,112\nRa 180,3,13312\nCV 165,166,167\n"
"Mvav 176,140,120\nWL 6\nCV 166,172,173\nMvav 180,140,124\nRa 184,3,14336\nCV 167,174,175\nMvav 180,136,116\nv_lshlrev_b32 v186, 16, v211\ns_add_u32 s5, s94, 1572864\n"
"Mvav 180,132,108\nRa 188,3,15360\ns_add_u32 s27, s2, 0x4800000\nMvav 180,128,100\nv_and_b32 v187, 0xffff0000, v211\ns_bitcmp1_b32 s64, 4\ns_cselect_b32 s2, 0x2000, 0\n"
"Mvav 180,144,36\nv_lshlrev_b32 v188, 16, v214\nSv 164,1,16,5,1024\nMvav 180,148,44\nv_and_b32 v189, 0xffff0000, v214\nCV 160,160,161\nMvav 180,152,52\n"
"v_lshlrev_b32 v190, 16, v215\nCV 161,162,163\nMvav 180,156,60\nWL 4\ns_xor_b32 s2, s2, s20\nMvav 176,156,56\nv_and_b32 v191, 0xffff0000, v215\nCV 162,168,169\nMvav 176,152,48\n"
"s_add_u32 s32, s2, 0x6000000\ns_bitcmp1_b32 s64, 5\ns_cselect_b32 s2, 0x2000, 0\nMvav 176,148,40\ns_xor_b32 s2, s2, s20\nCV 163,170,171\nMvav 176,144,32\n"
"s_add_u32 s33, s2, 0x7800000\ns_bitcmp1_b32 s64, 6\ns_cselect_b32 s2, 0x2000, 0\nMvav 184,160,96\ns_xor_b32 s2, s2, s20\ns_add_u32 s34, s2, 0x9000000\nMvav 184,164,104\n"
"s_bitcmp1_b32 s64, 7\ns_cselect_b32 s2, 0x2000, 0\nMvav 184,168,112\ns_xor_b32 s2, s2, s20\nSv 160,1,16,5,0\nMvav 184,172,120\nWL 0\nMvav 188,172,124\n"
"s_add_u32 s35, s2, 0xa800000\nMvav 188,168,116\nMvav 188,164,108\nMvav 188,160,100\nMvav 188,176,36\nMvav 188,180,44\nMvav 188,184,52\nMvav 188,188,60\nMvav 184,188,56\n"
"Mvav 184,184,48\nMvav 184,180,40\nMvav 184,176,32\nCV 180,180,181\nCV 181,182,183\ns_nop 1\nCV 182,188,189\nCV 183,190,191\ns_add_u32 s5, s94, 1703936\nSv 180,1,16,5,1024\n"
"CV 176,176,177\nCV 177,178,179\nCV 178,184,185\nCV 179,186,187\nSv 176,1,16,5,0\ns_cmp_eq_u32 s13, 7\ns_cbranch_scc1 .Z524\n"
"buffer_load_dwordx4 v[12:15], v91, s[28:31], s22 offen offset:0\nbuffer_load_dwordx4 v[16:19], v91, s[28:31], s23 offen offset:0\n"
"buffer_load_dwordx4 v[64:67], v89, s[16:19], s40 offen offset:0 sc0 sc1\nbuffer_load_dwordx4 v[68:71], v89, s[16:19], s40 offen offset:16 sc0 sc1\n"
"buffer_load_dwordx4 v[72:75], v89, s[16:19], s40 offen offset:1792 sc0 sc1\nbuffer_load_dwordx4 v[76:79], v89, s[16:19], s40 offen offset:1808 sc0 sc1\n"
"buffer_load_dwordx2 v[80:81], v88, s[16:19], s63 offen offset:0 sc0 sc1\nbuffer_load_dwordx2 v[82:83], v88, s[16:19], s63 offen offset:512 sc0 sc1\n"
"buffer_load_dwordx4 v[96:99], v1, s[16:19], s24 offen offset:0 sc0 sc1\nbuffer_load_dwordx4 v[100:103], v1, s[16:19], s24 offen offset:1024 sc0 sc1\n"
"buffer_load_dwordx4 v[104:107], v1, s[16:19], s25 offen offset:0 sc0 sc1\nbuffer_load_dwordx4 v[108:111], v1, s[16:19], s25 offen offset:1024 sc0 sc1\n"
"buffer_load_dwordx4 v[112:115], v1, s[16:19], s26 offen offset:0 sc0 sc1\nbuffer_load_dwordx4 v[116:119], v1, s[16:19], s26 offen offset:1024 sc0 sc1\n"
"buffer_load_dwordx4 v[120:123], v1, s[16:19], s27 offen offset:0 sc0 sc1\nbuffer_load_dwordx4 v[124:127], v1, s[16:19], s27 offen offset:1024 sc0 sc1\n"
"buffer_load_dwordx4 v[32:35], v1, s[16:19], s32 offen offset:0 sc0 sc1\nbuffer_load_dwordx4 v[36:39], v1, s[16:19], s32 offen offset:1024 sc0 sc1\n"
"buffer_load_dwordx4 v[40:43], v1, s[16:19], s33 offen offset:0 sc0 sc1\nbuffer_load_dwordx4 v[44:47], v1, s[16:19], s33 offen offset:1024 sc0 sc1\n"
"buffer_load_dwordx4 v[48:51], v1, s[16:19], s34 offen offset:0 sc0 sc1\nbuffer_load_dwordx4 v[52:55], v1, s[16:19], s34 offen offset:1024 sc0 sc1\n"
"buffer_load_dwordx4 v[56:59], v1, s[16:19], s35 offen offset:0 sc0 sc1\nbuffer_load_dwordx4 v[60:63], v1, s[16:19], s35 offen offset:1024 sc0 sc1\nWV 16\nv_xor_b32 v66, s15, v66\n"
"v_xor_b32 v70, s15, v70\nv_xor_b32 v74, s15, v74\nv_xor_b32 v78, s15, v78\nv_xor_b32 v81, s15, v81\nv_xor_b32 v83, s15, v83\nv_or3_b32 v92, v66, v70, v74\n"
"v_or3_b32 v92, v92, v78, v81\nv_or_b32 v92, v92, v83\nv_cmp_ne_u32 vcc, 0, v92\ns_nop 0\ns_cbranch_vccnz .S515\n.S516:\nv_and_b32 v92, 0x7ff, v67\nv_lshrrev_b32 v93, 16, v67\n"
"v_cmp_eq_u32 s[44:45], v92, v90\nv_cmp_eq_u32 s[46:47], v93, v90\nv_cmp_eq_u32 s[48:49], v92, v94\nv_cmp_eq_u32 s[50:51], v93, v94\nv_cndmask_b32 v12, v12, v64, s[44:45]\n"
"v_cndmask_b32 v13, v13, v65, s[44:45]\nv_cndmask_b32 v14, v14, v68, s[44:45]\nv_cndmask_b32 v15, v15, v69, s[44:45]\nv_cndmask_b32 v12, v12, v72, s[46:47]\n"
"v_cndmask_b32 v13, v13, v73, s[46:47]\nv_cndmask_b32 v14, v14, v76, s[46:47]\nv_cndmask_b32 v15, v15, v77, s[46:47]\nv_cndmask_b32 v16, v16, v64, s[48:49]\n"
"v_cndmask_b32 v17, v17, v65, s[48:49]\nv_cndmask_b32 v18, v18, v68, s[48:49]\nv_cndmask_b32 v19, v19, v69, s[48:49]\nv_cndmask_b32 v16, v16, v72, s[50:51]\n"
"v_cndmask_b32 v17, v17, v73, s[50:51]\nv_cndmask_b32 v18, v18, v76, s[50:51]\nv_cndmask_b32 v19, v19, v77, s[50:51]\nv_sub_f32 v92, 1.0, v80\nv_sub_f32 v93, 1.0, v82\nCV 92,92,93\n"
"v_and_b32 v93, 0xffff0000, v92\nv_lshlrev_b32 v92, 16, v92\nv_lshlrev_b32 v64, 16, v12\nv_and_b32 v65, 0xffff0000, v12\nMU 64,64,92\nMU 65,65,92\nv_lshlrev_b32 v66, 16, v13\n"
"v_and_b32 v67, 0xffff0000, v13\nMU 66,66,92\nMU 67,67,92\nv_lshlrev_b32 v68, 16, v14\nv_and_b32 v69, 0xffff0000, v14\nMU 68,68,92\nMU 69,69,92\nv_lshlrev_b32 v70, 16, v15\n"
"v_and_b32 v71, 0xffff0000, v15\nMU 70,70,92\nMU 71,71,92\nv_lshlrev_b32 v72, 16, v16\nv_and_b32 v73, 0xffff0000, v16\nMU 72,72,93\nMU 73,73,93\nv_lshlrev_b32 v74, 16, v17\n"
"v_and_b32 v75, 0xffff0000, v17\nMU 74,74,93\nMU 75,75,93\nv_lshlrev_b32 v76, 16, v18\nv_and_b32 v77, 0xffff0000, v18\nMU 76,76,93\nMU 77,77,93\nv_lshlrev_b32 v78, 16, v19\n"
"v_and_b32 v79, 0xffff0000, v19\nMU 78,78,93\nMU 79,79,93\ns_mov_b32 s7, 262144\nWV 0\n.Z527:\nv_max_u32 v84, v99, v103\nv_cmp_eq_u32 s[44:45], -1, v84\nv_max_u32 v84, v107, v111\n"
"v_cmp_eq_u32 s[46:47], -1, v84\nv_max_u32 v84, v115, v119\nv_cmp_eq_u32 s[48:49], -1, v84\nv_max_u32 v84, v123, v127\nv_cmp_eq_u32 s[50:51], -1, v84\nv_max_u32 v84, v35, v39\n"
"v_cmp_eq_u32 s[52:53], -1, v84\nv_max_u32 v84, v43, v47\nv_cmp_eq_u32 s[54:55], -1, v84\nv_max_u32 v84, v51, v55\nv_cmp_eq_u32 s[56:57], -1, v84\nv_max_u32 v84, v59, v63\n"
"v_cmp_eq_u32 s[58:59], -1, v84\ns_or_b64 s[0:1], s[44:45], s[46:47]\ns_or_b64 s[2:3], s[48:49], s[50:51]\ns_or_b64 s[0:1], s[0:1], s[52:53]\ns_or_b64 s[2:3], s[2:3], s[54:55]\n"
"s_or_b64 s[0:1], s[0:1], s[56:57]\ns_or_b64 s[2:3], s[2:3], s[58:59]\ns_or_b64 s[0:1], s[0:1], s[2:3]\ns_cbranch_scc1 .Z525\n.Z526:\nZvvv 136,6,96\nMvvv 136,6,104\nMvvv 136,6,112\n"
"Mvvv 136,6,120\nMvvv 136,6,32\nMvvv 136,6,40\nMvvv 136,6,48\nMvvv 136,6,56\nZvvv 140,4,96\nMvvv 140,4,104\nMvvv 140,4,112\nMvvv 140,4,120\nMvvv 140,4,32\nMvvv 140,4,40\n"
"Mvvv 140,4,48\nMvvv 140,4,56\nZvvv 144,6,100\nMvvv 144,6,108\nMvvv 144,6,116\nMvvv 144,6,124\nMvvv 144,6,36\nMvvv 144,6,44\nMvvv 144,6,52\nMvvv 144,6,60\nZvvv 148,4,100\n"
"Mvvv 148,4,108\nMvvv 148,4,116\nMvvv 148,4,124\nMvvv 148,4,36\nMvvv 148,4,44\nMvvv 148,4,52\nMvvv 148,4,60\nFM 64,80,136\nFM 65,80,137\nFM 66,80,138\nFM 67,80,139\nFM 68,80,140\n"
"FM 69,80,141\nFM 70,80,142\nFM 71,80,143\nCV 12,64,65\nCV 13,66,67\nCV 14,68,69\nCV 15,70,71\nSv 12,91,36,22,0\nFM 72,82,144\nFM 73,82,145\nFM 74,82,146\nFM 75,82,147\n"
"FM 76,82,148\nFM 77,82,149\nFM 78,82,150\nFM 79,82,151\nCV 16,72,73\nCV 17,74,75\nCV 18,76,77\nCV 19,78,79\nSv 16,91,36,23,0\n.Z524:\ns_endpgm\n.EP536:\n.S410:\n"
"s_mov_b32 s7, 262144\n.S412:\ns_lshl_b32 s5, s73, 9\ns_add_u32 s5, s5, 0xc049400\nv_lshrrev_b32 v27, 1, v1\nbuffer_load_dwordx2 v[22:23], v27, s[16:19], s5 offen sc0 sc1\nWV 0\n"
"v_cmp_ne_u32 vcc, s15, v23\ns_nop 0\ns_cbranch_vccz .S411\ns_sleep 6\ns_sub_u32 s7, s7, 1\ns_cbranch_scc0 .S412\ns_branch .S411\n.T419:\ns_mov_b32 s62, 1\n"
"s_call_b64 s[80:81], .RA265\ns_branch .T420\n.T421:\ns_mov_b32 s62, 1\ns_call_b64 s[80:81], .RA265\ns_branch .T422\n.S424:\ns_mov_b32 s7, 262144\n.S426:\n"
"buffer_load_dword v27, off, s[16:19], s5 sc0 sc1\nWV 0\nv_cmp_ne_u32 vcc, s15, v27\ns_nop 0\ns_cbranch_vccz .S425\ns_sleep 6\ns_sub_u32 s7, s7, 1\ns_cbranch_scc0 .S426\n"
"s_branch .S425\n.S427:\ns_mov_b32 s7, 262144\n.S429:\nv_and_b32 v27, 0x70, v1\nbuffer_load_dword v27, v27, s[16:19], s61 offen offset:128 sc0 sc1\nWV 0\n"
"v_cmp_ne_u32 vcc, s15, v27\ns_nop 0\ns_cbranch_vccz .S428\ns_sleep 6\ns_sub_u32 s7, s7, 1\ns_cbranch_scc0 .S429\ns_branch .S428\n.S431:\ns_mov_b32 s7, 262144\n.S433:\n"
"v_and_b32 v27, 0x70, v1\nbuffer_load_dword v27, v27, s[16:19], s61 offen offset:136 sc0 sc1\nWV 0\nv_cmp_ne_u32 vcc, s15, v27\ns_nop 0\ns_cbranch_vccz .S432\ns_sleep 6\n"
"s_sub_u32 s7, s7, 1\ns_cbranch_scc0 .S433\ns_branch .S432\n.S434:\ns_mov_b32 s7, 262144\n.S436:\nv_and_b32 v27, 0x70, v1\n"
"buffer_load_dword v27, v27, s[16:19], s61 offen offset:140 sc0 sc1\nWV 0\nv_cmp_ne_u32 vcc, s15, v27\ns_nop 0\ns_cbranch_vccz .S435\ns_sleep 6\ns_sub_u32 s7, s7, 1\n"
"s_cbranch_scc0 .S436\ns_branch .S435\n.S437:\ns_mov_b32 s7, 262144\n.S439:\nv_and_b32 v27, 0x70, v1\nbuffer_load_dword v27, v27, s[16:19], s61 offen offset:12 sc0 sc1\nWV 0\n"
"v_cmp_ne_u32 vcc, s15, v27\ns_nop 0\ns_cbranch_vccz .S438\ns_sleep 6\ns_sub_u32 s7, s7, 1\ns_cbranch_scc0 .S439\ns_branch .S438\n.S440:\ns_mov_b32 s7, 262144\n.S442:\n"
"v_and_b32 v27, 0x70, v1\nbuffer_load_dword v27, v27, s[16:19], s61 offen offset:8 sc0 sc1\nWV 0\nv_cmp_ne_u32 vcc, s15, v27\ns_nop 0\ns_cbranch_vccz .S441\ns_sleep 6\n"
"s_sub_u32 s7, s7, 1\ns_cbranch_scc0 .S442\ns_branch .S441\n.S443:\ns_mov_b32 s7, 262144\n.S445:\nv_and_b32 v27, 0xf0, v1\n"
"buffer_load_dword v27, v27, s[16:19], s61 offen offset:4 sc0 sc1\nWV 0\nv_cmp_ne_u32 vcc, s15, v27\ns_nop 0\ns_cbranch_vccz .S444\ns_sleep 6\ns_sub_u32 s7, s7, 1\n"
"s_cbranch_scc0 .S445\ns_branch .S444\n.T454:\ns_mov_b32 s62, 0\ns_call_b64 s[80:81], .RB266\ns_branch .T455\n.T456:\ns_mov_b32 s62, 0\ns_call_b64 s[80:81], .RA265\ns_branch .T457\n"
".T460:\ns_mov_b32 s62, 0\ns_call_b64 s[80:81], .RB266\ns_branch .T461\n.T462:\ns_mov_b32 s62, 0\ns_call_b64 s[80:81], .RA265\ns_branch .T463\n.T466:\ns_mov_b32 s62, 2\n"
"s_call_b64 s[80:81], .RB266\ns_branch .T467\n.T468:\ns_mov_b32 s62, 1\ns_call_b64 s[80:81], .RA265\ns_branch .T469\n.T470:\ns_mov_b32 s62, 1\ns_call_b64 s[80:81], .RA265\n"
"s_branch .T471\n.T482:\ns_mov_b32 s62, 0\ns_call_b64 s[80:81], .RB266\ns_branch .T483\n.T484:\ns_mov_b32 s62, 0\ns_call_b64 s[80:81], .RA265\ns_branch .T485\n.T488:\n"
"s_mov_b32 s62, 0\ns_call_b64 s[80:81], .RB266\ns_branch .T489\n.T490:\ns_mov_b32 s62, 0\ns_call_b64 s[80:81], .RA265\ns_branch .T491\n.T492:\ns_mov_b32 s62, 2\n"
"s_call_b64 s[80:81], .RB266\ns_branch .T493\n.S494:\ns_mov_b32 s7, 262144\n.S496:\ns_add_u32 s0, s92, 0\nbuffer_load_dwordx4 v[208:211], v1, s[16:19], s0 offen offset:0 sc0 sc1\n"
"buffer_load_dwordx4 v[212:215], v1, s[16:19], s0 offen offset:1024 sc0 sc1\nWV 0\nv_max_u32 v21, v211, v215\nv_cmp_eq_u32 vcc, -1, v21\ns_nop 0\ns_cbranch_vccz .S495\ns_sleep 6\n"
"s_sub_u32 s7, s7, 1\ns_cbranch_scc0 .S496\ns_branch .S495\n.S497:\ns_mov_b32 s7, 262144\n.S499:\ns_add_u32 s0, s92, 131072\n"
"buffer_load_dwordx4 v[216:219], v1, s[16:19], s0 offen offset:0 sc0 sc1\nbuffer_load_dwordx4 v[220:223], v1, s[16:19], s0 offen offset:1024 sc0 sc1\nWV 0\n"
"v_max_u32 v21, v219, v223\nv_cmp_eq_u32 vcc, -1, v21\ns_nop 0\ns_cbranch_vccz .S498\ns_sleep 6\ns_sub_u32 s7, s7, 1\ns_cbranch_scc0 .S499\ns_branch .S498\n.S500:\n"
"s_mov_b32 s7, 262144\n.S502:\ns_add_u32 s0, s92, 262144\nbuffer_load_dwordx4 v[224:227], v1, s[16:19], s0 offen offset:0 sc0 sc1\n"
"buffer_load_dwordx4 v[228:231], v1, s[16:19], s0 offen offset:1024 sc0 sc1\nWV 0\nv_max_u32 v21, v227, v231\nv_cmp_eq_u32 vcc, -1, v21\ns_nop 0\ns_cbranch_vccz .S501\ns_sleep 6\n"
"s_sub_u32 s7, s7, 1\ns_cbranch_scc0 .S502\ns_branch .S501\n.S503:\ns_mov_b32 s7, 262144\n.S505:\ns_add_u32 s0, s92, 393216\n"
"buffer_load_dwordx4 v[208:211], v1, s[16:19], s0 offen offset:0 sc0 sc1\nbuffer_load_dwordx4 v[212:215], v1, s[16:19], s0 offen offset:1024 sc0 sc1\nWV 0\n"
"v_max_u32 v21, v211, v215\nv_cmp_eq_u32 vcc, -1, v21\ns_nop 0\ns_cbranch_vccz .S504\ns_sleep 6\ns_sub_u32 s7, s7, 1\ns_cbranch_scc0 .S505\ns_branch .S504\n.S506:\n"
"s_mov_b32 s7, 262144\n.S508:\ns_add_u32 s0, s92, 524288\nbuffer_load_dwordx4 v[216:219], v1, s[16:19], s0 offen offset:0 sc0 sc1\n"
"buffer_load_dwordx4 v[220:223], v1, s[16:19], s0 offen offset:1024 sc0 sc1\nWV 0\nv_max_u32 v21, v219, v223\nv_cmp_eq_u32 vcc, -1, v21\ns_nop 0\ns_cbranch_vccz .S507\ns_sleep 6\n"
"s_sub_u32 s7, s7, 1\ns_cbranch_scc0 .S508\ns_branch .S507\n.S509:\ns_mov_b32 s7, 262144\n.S511:\ns_add_u32 s0, s92, 655360\n"
"buffer_load_dwordx4 v[224:227], v1, s[16:19], s0 offen offset:0 sc0 sc1\nbuffer_load_dwordx4 v[228:231], v1, s[16:19], s0 offen offset:1024 sc0 sc1\nWV 0\n"
"v_max_u32 v21, v227, v231\nv_cmp_eq_u32 vcc, -1, v21\ns_nop 0\ns_cbranch_vccz .S510\ns_sleep 6\ns_sub_u32 s7, s7, 1\ns_cbranch_scc0 .S511\ns_branch .S510\n.S512:\n"
"s_mov_b32 s7, 262144\n.S514:\ns_add_u32 s0, s92, 786432\nbuffer_load_dwordx4 v[208:211], v1, s[16:19], s0 offen offset:0 sc0 sc1\n"
"buffer_load_dwordx4 v[212:215], v1, s[16:19], s0 offen offset:1024 sc0 sc1\nWV 0\nv_max_u32 v21, v211, v215\nv_cmp_eq_u32 vcc, -1, v21\ns_nop 0\ns_cbranch_vccz .S513\ns_sleep 6\n"
"s_sub_u32 s7, s7, 1\ns_cbranch_scc0 .S514\ns_branch .S513\n.S515:\ns_mov_b32 s7, 262144\n.S517:\nbuffer_load_dwordx4 v[64:67], v89, s[16:19], s40 offen offset:0 sc0 sc1\n"
"buffer_load_dwordx4 v[68:71], v89, s[16:19], s40 offen offset:16 sc0 sc1\nbuffer_load_dwordx4 v[72:75], v89, s[16:19], s40 offen offset:1792 sc0 sc1\n"
"buffer_load_dwordx4 v[76:79], v89, s[16:19], s40 offen offset:1808 sc0 sc1\nbuffer_load_dwordx2 v[80:81], v88, s[16:19], s63 offen offset:0 sc0 sc1\n"
"buffer_load_dwordx2 v[82:83], v88, s[16:19], s63 offen offset:512 sc0 sc1\nWV 0\nv_xor_b32 v66, s15, v66\nv_xor_b32 v70, s15, v70\nv_xor_b32 v74, s15, v74\n"
"v_xor_b32 v78, s15, v78\nv_xor_b32 v81, s15, v81\nv_xor_b32 v83, s15, v83\nv_or3_b32 v92, v66, v70, v74\nv_or3_b32 v92, v92, v78, v81\nv_or_b32 v92, v92, v83\n"
"v_cmp_ne_u32 vcc, 0, v92\ns_nop 0\ns_cbranch_vccz .S516\ns_sleep 6\ns_sub_u32 s7, s7, 1\ns_cbranch_scc0 .S517\ns_branch .S516\n.S518:\ns_mov_b32 s7, 262144\n.S520:\n"
"buffer_load_dword v27, off, s[16:19], s60 offset:512 sc0 sc1\nWV 0\nv_cmp_ne_u32 vcc, s15, v27\ns_nop 0\ns_cbranch_vccz .S519\ns_sleep 6\ns_sub_u32 s7, s7, 1\n"
"s_cbranch_scc0 .S520\ns_branch .S519\n.S521:\ns_mov_b32 s7, 262144\n.S523:\nbuffer_load_dword v27, v24, s[16:19], s41 offen sc0 sc1\nWV 0\nv_xor_b32 v27, s15, v27\n"
"v_and_b32 v26, 0xffffdfff, v27\nv_cmp_ne_u32 s[64:65], 0, v27\nv_cmp_ne_u32 vcc, 0, v26\ns_nop 0\ns_cbranch_vccz .S522\ns_sleep 6\ns_sub_u32 s7, s7, 1\ns_cbranch_scc0 .S523\n"
"s_branch .S522\n.Z525:\ns_sub_u32 s7, s7, 1\ns_cbranch_scc1 .Z526\ns_cmp_lg_u64 s[44:45], 0\ns_cbranch_scc0 .Z528\n"
"buffer_load_dwordx4 v[96:99], v1, s[16:19], s24 offen offset:0 sc0 sc1\nbuffer_load_dwordx4 v[100:103], v1, s[16:19], s24 offen offset:1024 sc0 sc1\n.Z528:\n"
"s_cmp_lg_u64 s[46:47], 0\ns_cbranch_scc0 .Z529\nbuffer_load_dwordx4 v[104:107], v1, s[16:19], s25 offen offset:0 sc0 sc1\n"
"buffer_load_dwordx4 v[108:111], v1, s[16:19], s25 offen offset:1024 sc0 sc1\n.Z529:\ns_cmp_lg_u64 s[48:49], 0\ns_cbranch_scc0 .Z530\n"
"buffer_load_dwordx4 v[112:115], v1, s[16:19], s26 offen offset:0 sc0 sc1\nbuffer_load_dwordx4 v[116:119], v1, s[16:19], s26 offen offset:1024 sc0 sc1\n.Z530:\n"
"s_cmp_lg_u64 s[50:51], 0\ns_cbranch_scc0 .Z531\nbuffer_load_dwordx4 v[120:123], v1, s[16:19], s27 offen offset:0 sc0 sc1\n"
"buffer_load_dwordx4 v[124:127], v1, s[16:19], s27 offen offset:1024 sc0 sc1\n.Z531:\ns_cmp_lg_u64 s[52:53], 0\ns_cbranch_scc0 .Z532\n"
"buffer_load_dwordx4 v[32:35], v1, s[16:19], s32 offen offset:0 sc0 sc1\nbuffer_load_dwordx4 v[36:39], v1, s[16:19], s32 offen offset:1024 sc0 sc1\n.Z532:\n"
"s_cmp_lg_u64 s[54:55], 0\ns_cbranch_scc0 .Z533\nbuffer_load_dwordx4 v[40:43], v1, s[16:19], s33 offen offset:0 sc0 sc1\n"
"buffer_load_dwordx4 v[44:47], v1, s[16:19], s33 offen offset:1024 sc0 sc1\n.Z533:\ns_cmp_lg_u64 s[56:57], 0\ns_cbranch_scc0 .Z534\n"
"buffer_load_dwordx4 v[48:51], v1, s[16:19], s34 offen offset:0 sc0 sc1\nbuffer_load_dwordx4 v[52:55], v1, s[16:19], s34 offen offset:1024 sc0 sc1\n.Z534:\n"
"s_cmp_lg_u64 s[58:59], 0\ns_cbranch_scc0 .Z535\nbuffer_load_dwordx4 v[56:59], v1, s[16:19], s35 offen offset:0 sc0 sc1\n"
"buffer_load_dwordx4 v[60:63], v1, s[16:19], s35 offen offset:1024 sc0 sc1\n.Z535:\ns_sleep 8\nWV 0\ns_branch .Z527\n.Lcode1:\n"
  :: "s"(__builtin_amdgcn_kernarg_segment_ptr()), "s"(blockIdx.x), "v"(threadIdx.x)
  : "memory", "scc", "vcc", "v1", "v2", "v3", "v4", "v5", "v6", "v7", "v8", "v9", "v10", "v11", "v12", "v13", "v14", "v15", "v16", "v17", "v18", "v19", "v20", "v21", "v22", "v23", "v24", "v25", "v26", "v27", "v28", "v29", "v30", "v31", "v32", "v33", "v34", "v35", "v36", "v37", "v38", "v39", "v40", "v41", "v42", "v43", "v44", "v45", "v46", "v47", "v48", "v49", "v50", "v51", "v52", "v53", "v54", "v55", "v56", "v57", "v58", "v59", "v60", "v61", "v62", "v63", "v64", "v65", "v66", "v67", "v68", "v69", "v70", "v71", "v72", "v73", "v74", "v75", "v76", "v77", "v78", "v79", "v80", "v81", "v82", "v83", "v84", "v85", "v86", "v87", "v88", "v89", "v90", "v91", "v92", "v93", "v94", "v95", "v96", "v97", "v98", "v99", "v100", "v101", "v102", "v103", "v104", "v105", "v106", "v107", "v108", "v109", "v110", "v111", "v112", "v113", "v114", "v115", "v116", "v117", "v118", "v119", "v120", "v121", "v122", "v123", "v124", "v125", "v126", "v127", "v128", "v129", "v130", "v131", "v132", "v133", "v134", "v135", "v136", "v137", "v138", "v139", "v140", "v141", "v142", "v143", "v144", "v145", "v146", "v147", "v148", "v149", "v150", "v151", "v152", "v153", "v154", "v155", "v156", "v157", "v158", "v159", "v160", "v161", "v162", "v163", "v164", "v165", "v166", "v167", "v168", "v169", "v170", "v171", "v172", "v173", "v174", "v175", "v176", "v177", "v178", "v179", "v180", "v181", "v182", "v183", "v184", "v185", "v186", "v187", "v188", "v189", "v190", "v191", "v192", "v193", "v194", "v195", "v196", "v197", "v198", "v199", "v200", "v201", "v202", "v203", "v204", "v205", "v206", "v207", "v208", "v209", "v210", "v211", "v212", "v213", "v214", "v215", "v216", "v217", "v218", "v219", "v220", "v221", "v222", "v223", "v224", "v225", "v226", "v227", "v228", "v229", "v230", "v231", "v232", "v233", "v234", "v235", "v236", "v237", "v238", "v239", "v240", "v241", "v242", "v243", "v244", "v245", "v246", "v247", "v248", "v249", "v250", "v251", "v252", "v253", "v254", "v255", "a0", "a1", "a2", "a3", "a4", "a5", "a6", "a7", "a8", "a9", "a10", "a11", "a12", "a13", "a14", "a15", "a16", "a17", "a18", "a19", "a20", "a21", "a22", "a23", "a24", "a25", "a26", "a27", "a28", "a29", "a30", "a31", "a32", "a33", "a34", "a35", "a36", "a37", "a38", "a39", "a40", "a41", "a42", "a43", "a44", "a45", "a46", "a47", "a48", "a49", "a50", "a51", "a52", "a53", "a54", "a55", "a56", "a57", "a58", "a59", "a60", "a61", "a62", "a63", "a64", "a65", "a66", "a67", "a68", "a69", "a70", "a71", "a72", "a73", "a74", "a75", "a76", "a77", "a78", "a79", "a80", "a81", "a82", "a83", "a84", "a85", "a86", "a87", "a88", "a89", "a90", "a91", "a92", "a93", "a94", "a95", "a96", "a97", "a98", "a99", "a100", "a101", "a102", "a103", "a104", "a105", "a106", "a107", "a108", "a109", "a110", "a111", "a112", "a113", "a114", "a115", "a116", "a117", "a118", "a119", "a120", "a121", "a122", "a123", "a124", "a125", "a126", "a127", "a128", "a129", "a130", "a131", "a132", "a133", "a134", "a135", "a136", "a137", "a138", "a139", "a140", "a141", "a142", "a143", "a144", "a145", "a146", "a147", "a148", "a149", "a150", "a151", "a152", "a153", "a154", "a155", "a156", "a157", "a158", "a159", "a160", "a161", "a162", "a163", "a164", "a165", "a166", "a167", "a168", "a169", "a170", "a171", "a172", "a173", "a174", "a175", "a176", "a177", "a178", "a179", "a180", "a181", "a182", "a183", "a184", "a185", "a186", "a187", "a188", "a189", "a190", "a191", "a192", "a193", "a194", "a195", "a196", "a197", "a198", "a199", "a200", "a201", "a202", "a203", "a204", "a205", "a206", "a207", "a208", "a209", "a210", "a211", "a212", "a213", "a214", "a215", "a216", "a217", "a218", "a219", "a220", "a221", "a222", "a223", "a224", "a225", "a226", "a227", "a228", "a229", "a230", "a231", "a232", "a233", "a234", "a235", "a236", "a237", "a238", "a239", "a240", "a241", "a242", "a243", "a244", "a245", "a246", "a247", "a248", "a249", "a250", "a251", "a252", "a253", "a254", "a255", "s8", "s9", "s10", "s11", "s12", "s13", "s14", "s15", "s16", "s17", "s18", "s19", "s20", "s21", "s22", "s23", "s24", "s25", "s26", "s27", "s28", "s29", "s30", "s31", "s33", "s34", "s35", "s36", "s37", "s38", "s39", "s40", "s41", "s42", "s43", "s44", "s45", "s46", "s47", "s48", "s49", "s50", "s51", "s52", "s53", "s54", "s55", "s56", "s57", "s58", "s59", "s60", "s61", "s62", "s63", "s64", "s65", "s66", "s67", "s68", "s69", "s70", "s71", "s72", "s73", "s74", "s75", "s76", "s77", "s78", "s79", "s80", "s81", "s82", "s83", "s84", "s85", "s86", "s87", "s88", "s89", "s90", "s91", "s92", "s93", "s94", "s95", "s96", "s97", "s98", "s99");
#endif
}

extern "C" void kernel_launch(void* const* d_in, const int* in_sizes, int n_in,
                              void* d_out, int out_size, void* d_ws, size_t ws_size,
                              hipStream_t stream) {
  (void)hipFuncSetAttribute((const void*)k_main, hipFuncAttributeMaxDynamicSharedMemorySize, 163840);
  k_main<<<256, 256, 163840, stream>>>(d_in[0], d_in[1], d_in[2], d_in[3], d_in[4], d_ws, d_in[11], d_in[7], d_in[5], d_in[8], d_in[9], d_in[10], d_out);
}
```
